```python
import torch

def _softmax(x, axis=-1):
    return torch.softmax(x, dim=axis)
def _gelu(x):
    return torch.nn.functional.gelu(x, approximate="tanh")
def _silu(x):
    return torch.nn.functional.silu(x)
def _sigmoid(x):
    return torch.sigmoid(x)
def _layernorm(x, axis=-1, eps=1e-5):
    m = x.mean(dim=axis, keepdim=True)
    v = x.var(dim=axis, keepdim=True, unbiased=False)
    return (x - m) / torch.sqrt(v + eps)
def _scatter_add(tgt, idx, src, axis=0):
    return tgt.clone().index_add_(axis, idx, src)
def _segment_sum(src, seg, n):
    out = torch.zeros((n,) + src.shape[1:], dtype=src.dtype)
    return out.index_add_(0, seg, src)
def _pad(x, pw):
    flat = tuple(v for pair in reversed(pw) for v in pair)
    return torch.nn.functional.pad(x, flat)

kernel_name = 'interleave_layers__ssm_conv_h3__mixer_s_mlp__prompt'

def setup_inputs():

    _w = (lambda a: (lambda t: t.to(torch.bfloat16) if t.is_floating_point() else t)(torch.as_tensor(a)))
    def _A_setup():
        B, S, D = 1, 2048, 1024
        ks = 4
        return {
            "q": _w((lambda *s: torch.randn(*s, dtype=torch.bfloat16) * 0.05)(B, S, D)),
            "k": _w((lambda *s: torch.randn(*s, dtype=torch.bfloat16) * 0.4)(B, S, D)),
            "v": _w((lambda *s: torch.randn(*s, dtype=torch.bfloat16) * 0.4)(B, S, D)),
            "shift_k": _w((lambda *s: torch.randn(*s, dtype=torch.bfloat16) * 0.4)(D, ks) * 0.5),
            "ssm_k": _w((lambda *s: torch.randn(*s, dtype=torch.bfloat16) * 0.4)(D, S) * (1.0 / S ** 0.5)),
            "ssm_D": _w((lambda *s: torch.randn(*s, dtype=torch.bfloat16) * 0.4)(D)),
        }
    _A_in = _A_setup()

    def _B_setup():
        B, S, D, F = 1, 2048, 1024, 3584
        return {
            "x":  _w((lambda *s: torch.randn(*s, dtype=torch.bfloat16) * 0.05)(B, S, D)),
            "w1": _w((lambda *s: torch.randn(*s, dtype=torch.bfloat16) * 1.6)(D, D) * (D ** -0.5)),
            "w2": _w((lambda *s: torch.randn(*s, dtype=torch.bfloat16) * 1.6)(D, D) * (D ** -0.5)),
            "w3": _w((lambda *s: torch.randn(*s, dtype=torch.bfloat16) * 1.6)(D, F) * (D ** -0.5)),
            "w4": _w((lambda *s: torch.randn(*s, dtype=torch.bfloat16) * 1.6)(F, D) * (F ** -0.5)),
        }
    _B_in = _B_setup()

    return {
        "A_q": _A_in["q"],
        "A_k": _A_in["k"],
        "A_v": _A_in["v"],
        "A_shift_k": _A_in["shift_k"],
        "A_ssm_k": _A_in["ssm_k"],
        "A_ssm_D": _A_in["ssm_D"],
        "B_x": _B_in["x"],
        "B_w1": _B_in["w1"],
        "B_w2": _B_in["w2"],
        "B_w3": _B_in["w3"],
        "B_w4": _B_in["w4"],
    }

def reference(A_q, A_k, A_v, A_shift_k, A_ssm_k, A_ssm_D, B_x, B_w1, B_w2, B_w3, B_w4):
    def _A_ref(q, k, v, shift_k, ssm_k, ssm_D):
        B, S, D = q.shape
        ks = shift_k.shape[-1]
        k_pad = _pad(k, ((0, 0), (ks - 1, 0), (0, 0)))
        k_shift = torch.zeros_like(k)
        for t in range(ks):
            k_shift = k_shift + k_pad[:, t:t + S, :] * shift_k[:, ks - 1 - t][None, None, :]
        kv = k_shift * v
        L = 2 * S
        kv_pad = _pad(kv, ((0, 0), (0, S), (0, 0)))
        kf_pad = _pad(ssm_k, ((0, 0), (0, S)))
        kv_f = (lambda x, axes=(-2,-1): torch.fft.rfft2(x.float(), dim=axes))(kv_pad[:, None, :, :], axes=(1, 2))
        kf_f = (lambda x, axes=(-2,-1): torch.fft.rfft2(x.float(), dim=axes))(kf_pad[:, None, :], axes=(1, 2))
        kf_f = (lambda x, *d: x.permute(*d))(kf_f, 1, 2, 0)
        y_f = kv_f * kf_f[None, :, :, :]
        y = (lambda x, s, axes=(-2,-1): torch.fft.irfft2(x, s=s, dim=axes).to(torch.bfloat16))(y_f, (1, L), axes=(1, 2))[:, 0, :S, :]
        y = y + kv * ssm_D[None, None, :]
        return q * y

    def _B_ref(x, w1, w2, w3, w4):
        B, S, D = x.shape
        h = _gelu(_layernorm(x, axis=-1) @ w1)
        c = D // 4
        g0 = _pad(h[:, :, 0*c:1*c], ((0, 0), (1, 0), (0, 0)))[:, :S, :]
        g1 = _pad(h[:, :, 1*c:2*c], ((0, 0), (0, 1), (0, 0)))[:, 1:, :]
        g2 = _pad(h[:, :, 2*c:3*c], ((0, 0), (2, 0), (0, 0)))[:, :S, :]
        g3 = _pad(h[:, :, 3*c:   ], ((0, 0), (0, 2), (0, 0)))[:, 2:, :]
        hs = torch.concatenate([g0, g1, g2, g3], axis=-1) @ w2
        x1 = x + hs
        h2 = _gelu(_layernorm(x1, axis=-1) @ w3) @ w4
        return x1 + h2

    x = A_q
    x = x + _A_ref(_layernorm(x, axis=-1), A_k, A_v, A_shift_k, A_ssm_k, A_ssm_D)
    x = x + _B_ref(_layernorm(x, axis=-1), B_w1, B_w2, B_w3, B_w4)
    x = x + _A_ref(_layernorm(x, axis=-1), A_k, A_v, A_shift_k, A_ssm_k, A_ssm_D)
    x = x + _B_ref(_layernorm(x, axis=-1), B_w1, B_w2, B_w3, B_w4)
    return x
```

```cpp
#include <hip/hip_runtime.h>
#include <hip/hip_bf16.h>
#include <stdint.h>
#include <type_traits>

typedef short s16x8 __attribute__((ext_vector_type(8)));
typedef short s16x4 __attribute__((ext_vector_type(4)));
typedef float f32x4 __attribute__((ext_vector_type(4)));
typedef unsigned short u16;
typedef unsigned short u16x8 __attribute__((ext_vector_type(8)));
typedef unsigned short u16x4 __attribute__((ext_vector_type(4)));
typedef float f32x2 __attribute__((ext_vector_type(2)));
typedef __bf16 bf16x2 __attribute__((ext_vector_type(2)));
typedef unsigned u32x4v __attribute__((ext_vector_type(4)));
typedef __attribute__((address_space(1))) unsigned gu32;

constexpr int S_ = 2048, D_ = 1024, F_ = 3584;
#define PF 1

constexpr int RT1 = 0, LB = 66, FL_BYTES = 2141312, WS_USED = 111193216;
constexpr int OFF_KVR = 4194304, OFF_YF = 8388608, OFF_XP = 12582912, OFF_HS = 22085632, OFF_X1 = 31457280, OFF_G = 40894464, OFF_P0 = 71303168, OFF_DBG = 80740352, OFF_P1 = 89128960, OFF_Y = 98566144, OFF_S1 = 106954752, OFF_ST = 107479040, OFF_FL = 109051904, OFF_FXP = 109052928, OFF_FH = 109325312, OFF_FX1 = 109597696, OFF_FP = 109871104, OFF_FG = 110416384, OFF_FKV = 111094784, OFF_FY = 111127552, OFF_EP = 111193088;

#define WS_DIAG 0

__device__ __forceinline__ float bf2f(u16 v) { return __uint_as_float(((unsigned)v) << 16); }
__device__ __forceinline__ u16 f2bf(float f) { return __builtin_bit_cast(u16, (__bf16)f); }
__device__ __forceinline__ float rbf(float f) { return bf2f(f2bf(f)); }
__device__ __forceinline__ void st16(void* p, u16x8 v) {
  asm volatile("global_store_dwordx4 %0, %1, off sc0 sc1\n\ts_nop 1" :: "v"(p), "v"(v) : "memory");
}
#define WAIT_VM(n) asm volatile("s_waitcnt vmcnt(%0)" ::"n"(n) : "memory")
#define WAIT_LGKM0() asm volatile("s_waitcnt lgkmcnt(0)" ::: "memory")

#define CODE_RANGE(B, E, p, n)                                                                                   \
  const char* p; unsigned n;                                                                                     \
  { unsigned lo_, hi_;                                                                                           \
    asm volatile("s_getpc_b64 s[98:99]\nLpc%=:\n\ts_add_u32 %0, s98, (" B "-Lpc%=)&4294967295\n\ts_addc_u32 %1, s99, (" B "-Lpc%=)>>32\n\t" \
                 "s_mov_b32 %2, " E "-" B : "=s"(lo_), "=s"(hi_), "=s"(n) :: "s98", "s99", "scc");               \
    p = (const char*)(((unsigned long long)hi_ << 32) | lo_); }
__device__ __forceinline__ unsigned touch(const char* p, unsigned n, unsigned off) {
  unsigned t;
  asm volatile("global_load_dword %0, %1, off" : "=v"(t) : "v"(p + (off < n ? off : 0u)) : "memory");
  return t;
}

__device__ __forceinline__ void touch_lds(const char* p, unsigned a) {
  asm volatile("s_mov_b32 m0, %1\n\ts_nop 0\n\tglobal_load_lds_dword %0, off" :: "v"(p), "s"(a) : "memory");
}
#define PFPOL ""
__device__ __forceinline__ void touch_pol(const char* p, unsigned a) {
  asm volatile("s_mov_b32 m0, %1\n\ts_nop 0\n\tglobal_load_lds_dword %0, off " PFPOL :: "v"(p), "s"(a) : "memory");
}
#define YSLEEP 4
#define KSLEEP 2
#define CTS(i)
__device__ __forceinline__ f32x2 up2(unsigned p) { return (f32x2){__uint_as_float(p << 16), __uint_as_float(p & 0xffff0000u)}; }
__device__ __forceinline__ unsigned pk2(f32x2 v) { return __builtin_bit_cast(unsigned, __builtin_convertvector(v, bf16x2)); }
__device__ __forceinline__ f32x2 rbf2(f32x2 v) { return up2(pk2(v)); }
template <int N, class F> __device__ __forceinline__ void sfor(F&& f) {
  if constexpr (N > 0) { sfor<N - 1>(f); f(std::integral_constant<int, N - 1>{}); }
}
template <int NL>
__device__ __forceinline__ void poll_flags(const char* p, int n, int lane, unsigned ep) {
  for (int it = 0; it < 20000; ++it) {
    bool ok = true;
#pragma unroll
    for (int i = 0; i < NL; ++i) {
      const int f = lane + 64 * i;
      ok &= __hip_atomic_load((gu32*)(p + (f < n ? f : 0) * 128), __ATOMIC_RELAXED, __HIP_MEMORY_SCOPE_AGENT) == ep;
    }
    if (__all(ok)) break;
    __builtin_amdgcn_s_sleep(YSLEEP);
  }
  asm volatile("" ::: "memory");
}

constexpr int CV_HP = (2048 + 32) * 2 + 0;
constexpr int CV_UR = (2048 + 256) * 2;
constexpr int CV_CH = 4 * CV_HP + CV_UR;
#define PFW_R 2
constexpr int NCV = 171;
constexpr int CV_STG = 6 * CV_CH;

struct KvRegs { u32x4v k0, k1, k2, k3, k4, k5, k6, v0, v1, v2, v3, s0, s1, s2, s3; };
#define LD16(d, p, pol) asm volatile("global_load_dwordx4 %0, %1, off " pol : "=v"(d) : "v"(p) : "memory")
__device__ __forceinline__ void kv_load(KvRegs& R, const u16* __restrict__ kin, const u16* __restrict__ vin, const u16* __restrict__ shift_k, int b, int tid) {
  const int tr = (b >> 4) * 128 + 4 * (tid >> 3), cc = (b & 15) * 64 + 8 * (tid & 7);
  const u16* kp = kin + (size_t)tr * D_ + cc;
  const u16* vp = vin + (size_t)tr * D_ + cc;
  const u16* sp = shift_k + (size_t)cc * 4;
  LD16(R.k3, kp, "nt"); LD16(R.k4, kp + D_, "nt"); LD16(R.k5, kp + 2 * D_, "nt"); LD16(R.k6, kp + 3 * D_, "nt");
  LD16(R.v0, vp, "nt"); LD16(R.v1, vp + D_, "nt"); LD16(R.v2, vp + 2 * D_, "nt"); LD16(R.v3, vp + 3 * D_, "nt");
  const u16* kq = tr ? kp - 3 * D_ : kp;
  LD16(R.k0, kq, "nt"); LD16(R.k1, kq + D_, "nt"); LD16(R.k2, kq + 2 * D_, "nt");
  LD16(R.s0, sp, ""); LD16(R.s1, sp + 8, ""); LD16(R.s2, sp + 16, ""); LD16(R.s3, sp + 24, "");
}
template <int N>
__device__ __forceinline__ void kv_wait(KvRegs& R) {
  asm volatile("s_waitcnt vmcnt(%15)" : "+v"(R.k0), "+v"(R.k1), "+v"(R.k2), "+v"(R.k3), "+v"(R.k4), "+v"(R.k5), "+v"(R.k6), "+v"(R.v0), "+v"(R.v1), "+v"(R.v2),
               "+v"(R.v3), "+v"(R.s0), "+v"(R.s1), "+v"(R.s2), "+v"(R.s3) : "n"(N) : "memory");
}
__device__ __forceinline__ void kv_store(KvRegs& R, char* __restrict__ ws, char* T, int b, int tid) {
  const int t0 = (b >> 4) * 128, c0 = (b & 15) * 64, ch8 = tid & 7, rg = tid >> 3;
  if (t0 + 4 * rg < 3) R.k0 = R.k1 = R.k2 = (u32x4v){0u, 0u, 0u, 0u};
  typedef unsigned u32x2 __attribute__((ext_vector_type(2)));
  const u32x4v kr[7] = {R.k0, R.k1, R.k2, R.k3, R.k4, R.k5, R.k6}, vr[4] = {R.v0, R.v1, R.v2, R.v3}, sk[4] = {R.s0, R.s1, R.s2, R.s3};
#pragma unroll
  for (int j2 = 0; j2 < 4; ++j2) {
    const f32x2 sa = up2(sk[j2][0]), sb = up2(sk[j2][1]), sc = up2(sk[j2][2]), sd = up2(sk[j2][3]);
    const f32x2 s0 = {sa[0], sc[0]}, s1 = {sa[1], sc[1]}, s2 = {sb[0], sd[0]}, s3 = {sb[1], sd[1]};
    f32x2 kf[7], o[4];
#pragma unroll
    for (int r = 0; r < 7; ++r) kf[r] = up2(kr[r][j2]);
#pragma unroll
    for (int r = 0; r < 4; ++r)
      o[r] = __builtin_elementwise_fma(kf[r + 3], s0, __builtin_elementwise_fma(kf[r + 2], s1, __builtin_elementwise_fma(kf[r + 1], s2, kf[r] * s3))) * up2(vr[r][j2]);
#pragma unroll
    for (int e = 0; e < 2; ++e)
      *(u32x2*)(T + (8 * ch8 + 2 * j2 + e) * 264 + (124 - 4 * rg) * 2) = (u32x2){pk2((f32x2){o[3][e], o[2][e]}), pk2((f32x2){o[1][e], o[0][e]})};
  }
  __syncthreads();
  u16* kvr = (u16*)(ws + OFF_KVR);
#pragma unroll
  for (int i = 0; i < 4; ++i) {
    const int idx = tid + 256 * i, ch = idx >> 4, cb = idx & 15;
    const u16x4 lo = *(const u16x4*)(T + ch * 264 + cb * 16), hi = *(const u16x4*)(T + ch * 264 + cb * 16 + 8);
    st16(kvr + (size_t)(c0 + ch) * S_ + (1920 - t0) + cb * 8, __builtin_shufflevector(lo, hi, 0, 1, 2, 3, 4, 5, 6, 7));
  }
}

typedef unsigned u32x2v __attribute__((ext_vector_type(2)));
struct HRegs { u32x4v hv0, hv1, hv2, hv3, hv4, hv5; u32x2v hn0, hn1, hn2, hn3, hn4, hn5; unsigned d0; };
#define H_EACH(F) F(0) F(1) F(2) F(3) F(4) F(5)
__device__ __forceinline__ void h_load(HRegs& H, const u16* __restrict__ ssm_k, const u16* __restrict__ ssm_D, int ci, int tid) {
  const int c0 = 6 * ci;
#define F(i) { const u16* hp = ssm_k + (size_t)min(c0 + i, D_ - 1) * S_ + tid * 8; \
    asm volatile("global_load_dwordx4 %0, %1, off nt" : "=v"(H.hv##i) : "v"(hp) : "memory"); \
    asm volatile("global_load_dwordx2 %0, %1, off nt" : "=v"(H.hn##i) : "v"(hp + (tid < 255 ? 8 : 0)) : "memory"); }
  H_EACH(F)
#undef F
  const u16* dp = ssm_D + min(c0 + ((tid & 32) ? 4 + (tid >> 7) : (tid >> 6)), D_ - 1);
  asm volatile("global_load_ushort %0, %1, off" : "=v"(H.d0) : "v"(dp) : "memory");
}
__device__ __forceinline__ void h_spread(HRegs& H, char* lds, int tid) {
  if (tid < 48) *(u32x4v*)(lds + (tid >> 3) * CV_CH + ((tid >> 1) & 3) * CV_HP + (tid & 1) * 16) = (u32x4v){0u, 0u, 0u, 0u};
  if (tid >= 64) *(u32x4v*)(lds + ((tid - 64) >> 5) * CV_CH + 4 * CV_HP + 4096 + (tid & 31) * 16) = (u32x4v){0u, 0u, 0u, 0u};
  auto one = [&](int i, u32x4v hv, u32x2v hn) {
    char* base = lds + i * CV_CH + 32 + tid * 16;
    const unsigned w0 = hv[0], w1 = hv[1], w2 = hv[2], w3 = hv[3], w4 = hn[0], w5 = hn[1];
#define AB(a, b) __builtin_amdgcn_alignbyte(a, b, 2)
    *(u32x4v*)(base) = hv;
    *(u32x4v*)(base + CV_HP) = (u32x4v){AB(w1, w0), AB(w2, w1), AB(w3, w2), AB(w4, w3)};
    *(u32x4v*)(base + 2 * CV_HP) = (u32x4v){w1, w2, w3, w4};
    *(u32x4v*)(base + 3 * CV_HP) = (u32x4v){AB(w2, w1), AB(w3, w2), AB(w4, w3), AB(w5, w4)};
#undef AB
  };
#define F(i) one(i, H.hv##i, H.hn##i);
  H_EACH(F)
#undef F
}
__device__ __forceinline__ void h_patch(HRegs& H, char* lds, int tid) {
  if (tid == 0) {
    auto one = [&](int i, u32x4v hv) {
      u16* base = (u16*)(lds + i * CV_CH);
      const u16 h0 = (u16)(hv[0] & 0xffff), h1 = (u16)(hv[0] >> 16), h2 = (u16)(hv[1] & 0xffff);
      base[CV_HP / 2 + 15] = h0;
      base[2 * (CV_HP / 2) + 14] = h0; base[2 * (CV_HP / 2) + 15] = h1;
      base[3 * (CV_HP / 2) + 13] = h0; base[3 * (CV_HP / 2) + 14] = h1; base[3 * (CV_HP / 2) + 15] = h2;
    };
#define F(i) one(i, H.hv##i);
    H_EACH(F)
#undef F
  }
}

template <int PH, int NW>
__device__ __forceinline__ void prewrite(char* __restrict__ ws, int w, int lane, unsigned ep) {
  constexpr int NH = 66 * 2 * 8, NS_ = 0, N1 = 66, NF = PH ? 0 : (OFF_FKV - OFF_FL) / 1024;
  for (int qc = w; qc < NH + NS_ + N1 + NF; qc += NW) {
    const int f = qc - (NH + NS_ + N1), fh = f - (OFF_FH - OFF_FL) / 1024;
    const unsigned off = qc < NH ? OFF_HS - 65536 + (PH * NH + qc) * 4096 + (lane >> 2) * 256 + ((qc & 2) ? 192 : 0) + (lane & 3) * 16 :
                         (qc < NH + NS_ ? OFF_ST + (PH * NS_ + qc - NH) * 1024 :
                          qc < NH + NS_ + N1 ? OFF_S1 + (PH * N1 + qc - NH - NS_) * 1024 : OFF_FL + f * 1024) + lane * 16;
    const bool set = f >= 0 && (fh == -1 || fh == 128 || fh == 131 || fh == 260) && (lane & 7) == 0;
    st16(ws + off, __builtin_bit_cast(u16x8, (u32x4v){set ? ep : 0u, 0u, 0u, 0u}));
  }
}

__device__ __forceinline__ void conv_part(char* __restrict__ ws, HRegs& H, char* lds, int ci, int tid, int lane, int wave, unsigned ep,
                                          const u16* __restrict__ w3, const u16* __restrict__ w4) {
  const int c0 = 6 * ci;
  const u16* kvr = (const u16*)(ws + OFF_KVR);
  auto kv_fetch = [&](bool late) {
    if (wave == 0) {
      const int cg = lane < 16 ? c0 >> 6 : min(c0 + 5, D_ - 1) >> 6, ts = late ? 14 + (lane & 1) : min(lane & 15, 13);
      gu32* f = (gu32*)(ws + OFF_FKV + (ts * 16 + cg) * 128);
      for (int it = 0; it < 20000; ++it) {
        if (__all(__hip_atomic_load(f, __ATOMIC_RELAXED, __HIP_MEMORY_SCOPE_AGENT) == ep)) break;
        __builtin_amdgcn_s_sleep(KSLEEP);
      }
    }
    if (!late) __syncthreads();
    if (late ? (wave == 0 && lane < 32) : (wave > 0 || lane >= 32)) {
#pragma unroll
      for (int i = 0; i < 6; ++i)
        __builtin_amdgcn_global_load_lds((const unsigned*)(kvr + (size_t)min(c0 + i, D_ - 1) * S_ + wave * 512 + lane * 8),
                                         (unsigned*)(lds + i * CV_CH + 4 * CV_HP + wave * 1024), 16, 0, 16);
    }
  };
  kv_fetch(false);
  CTS(3);
  WAIT_VM(0);
  __syncthreads();
  CTS(4);
  const int l15 = lane & 15, lg = lane >> 4, wsh = (l15 + 1) & 3, sh = 4 + (wave >> 1);
  const unsigned lb = (unsigned)(size_t)lds;
  const unsigned ao = wsh * CV_HP + 2 * (8 * lg + (l15 + 1 - wsh)), bo = 4 * CV_HP + 2 * (2032 - 16 * l15 + 8 * lg) - 2 * 256 * 7;
  const unsigned own = lb + wave * CV_CH, shd = lb + sh * CV_CH + 256 * (wave & 1);
  char* stg = lds + CV_STG;
  gu32* fy = (gu32*)(ws + OFF_FY + ci * 128);
  auto rd1 = [&](unsigned base, int i, s16x4 (&ah)[8][2], s16x8 (&bfr)[8]) {
    const int m = i / 3, e = i % 3;
    if (e < 2) asm volatile("ds_read_b64 %0, %1 offset:%2" : "=v"(ah[m][e & 1]) : "v"(base + ao), "n"(512 * m + 8 * e));
    else asm volatile("ds_read_b128 %0, %1 offset:%2" : "=v"(bfr[m]) : "v"(base + bo), "n"(512 * (7 - m)));
  };
  const float dOwn = bf2f((u16)__builtin_amdgcn_readlane(H.d0, 0)), dShd = (wave & 1) ? 0.f : bf2f((u16)__builtin_amdgcn_readlane(H.d0, 32));
  auto finish = [&](auto s0_c, auto ns_c, f32x4 (&acc)[2][7]) {
    constexpr int S0 = decltype(s0_c)::value, NS = decltype(ns_c)::value;
    u32x2v ku[2][NS];
    const unsigned uo = 4 * CV_HP + 2 * (2044 - 16 * l15 - 4 * lg) - 512 * 7;
#pragma unroll
    for (int s = 0; s < NS; ++s) {
      asm volatile("ds_read_b64 %0, %1 offset:%2" : "=v"(ku[0][s]) : "v"(lb + wave * CV_CH + uo), "n"(512 * (7 - S0 - s)));
      asm volatile("ds_read_b64 %0, %1 offset:%2" : "=v"(ku[1][s]) : "v"(lb + sh * CV_CH + uo), "n"(512 * (7 - S0 - s)));
    }
    WAIT_LGKM0();
    __builtin_amdgcn_sched_barrier(0);
#pragma unroll
    for (int u = 0; u < 2; ++u)
#pragma unroll
      for (int s = 0; s < NS; ++s) {
        const f32x2 k0 = up2(ku[u][s][0]), k1 = up2(ku[u][s][1]);
        const float d = u ? dShd : dOwn;
        *(u32x2v*)(stg + u * 14336 + wave * 3584 + 2 * (256 * s + 16 * l15 + 4 * lg)) =
            (u32x2v){pk2((f32x2){fmaf(k1[1], d, acc[u][s][0]), fmaf(k1[0], d, acc[u][s][1])}), pk2((f32x2){fmaf(k0[1], d, acc[u][s][2]), fmaf(k0[0], d, acc[u][s][3])})};
      }
    __syncthreads();
    const int ch = (tid & 7) < 6 ? (tid & 7) : 0, tb0 = tid >> 3;
    const char* p0 = stg + (ch < 4 ? ch * 3584 : 14336 + (ch - 4) * 7168) + tb0 * 16;
    char* yo = ws + OFF_Y + ((size_t)(32 * S0 + tb0) * NCV + ci) * 128 + (tid & 7) * 16;
#pragma unroll
    for (int i = 0; i < NS; ++i) {
      const u32x4v ya = *(const u32x4v*)(p0 + 512 * i), yb = *(const u32x4v*)(p0 + 512 * i + 3584);
      u32x4v o;
#pragma unroll
      for (int r = 0; r < 4; ++r) o[r] = ch < 4 ? ya[r] : pk2(up2(ya[r]) + up2(yb[r]));
      st16(yo + (size_t)i * 32 * NCV * 128, __builtin_bit_cast(u16x8, o));
    }
  };
  s16x4 a0[8][2], a1[8][2];
  s16x8 b0[8], b1[8];
  f32x4 acc[2][7];
#pragma unroll
  for (int i = 0; i < 4; ++i) acc[i >> 1][i & 1] = (f32x4){0.f, 0.f, 0.f, 0.f};
  {
    s16x4 ax[12][2][2];
    s16x8 bx[12][2];
#pragma unroll
    for (int q = 0; q < 12; ++q) {
      const unsigned base = q < 8 ? own + 64 * q : shd + 64 * (q - 8);
#pragma unroll
      for (int m = 0; m < 2; ++m) {
        asm volatile("ds_read_b64 %0, %1 offset:%2" : "=v"(ax[q][m][0]) : "v"(base + ao), "n"(512 * m));
        asm volatile("ds_read_b64 %0, %1 offset:%2" : "=v"(ax[q][m][1]) : "v"(base + ao), "n"(512 * m + 8));
        asm volatile("ds_read_b128 %0, %1 offset:%2" : "=v"(bx[q][m]) : "v"(base + bo), "n"(512 * (7 - m)));
      }
    }
    WAIT_LGKM0();
    __builtin_amdgcn_sched_barrier(0);
#pragma unroll
    for (int q = 0; q < 12; ++q) {
      const s16x8 a0f = __builtin_shufflevector(ax[q][0][0], ax[q][0][1], 0, 1, 2, 3, 4, 5, 6, 7), a1f = __builtin_shufflevector(ax[q][1][0], ax[q][1][1], 0, 1, 2, 3, 4, 5, 6, 7);
      acc[q >= 8][0] = __builtin_amdgcn_mfma_f32_16x16x32_bf16(a0f, bx[q][0], acc[q >= 8][0], 0, 0, 0);
      acc[q >= 8][1] = __builtin_amdgcn_mfma_f32_16x16x32_bf16(a0f, bx[q][1], acc[q >= 8][1], 0, 0, 0);
      acc[q >= 8][1] = __builtin_amdgcn_mfma_f32_16x16x32_bf16(a1f, bx[q][0], acc[q >= 8][1], 0, 0, 0);
    }
  }
  finish(std::integral_constant<int, 0>{}, std::integral_constant<int, 2>{}, acc);
  CTS(5);
  WAIT_VM(0);
  __syncthreads();
  if (tid == 0) __hip_atomic_store(fy, ep, __ATOMIC_RELAXED, __HIP_MEMORY_SCOPE_AGENT);
  CTS(6);
  kv_fetch(true);
  WAIT_VM(0);
  __syncthreads();
  prewrite<1, 4 * NCV>(ws, ci * 4 + wave, lane, ep);
  auto round = [&](auto u_c, s16x4 (&ah)[8][2], s16x8 (&bfr)[8], s16x4 (&ahn)[8][2], s16x8 (&bfn)[8], unsigned nb) {
    constexpr int u = decltype(u_c)::value;
    int k = 0;
#pragma unroll
    for (int s = 2; s <= 7; ++s)
#pragma unroll
      for (int m = 0; m <= s; ++m) {
        const s16x8 af = __builtin_shufflevector(ah[m][0], ah[m][1], 0, 1, 2, 3, 4, 5, 6, 7);
        acc[u][s - 2] = __builtin_amdgcn_mfma_f32_16x16x32_bf16(af, bfr[s - m], acc[u][s - 2], 0, 0, 0);
        if (k < 24) rd1(nb, k, ahn, bfn);
        ++k;
        __builtin_amdgcn_sched_barrier(0);
      }
    WAIT_LGKM0();
    __builtin_amdgcn_sched_barrier(0);
  };
#pragma unroll
  for (int i = 0; i < 24; ++i) rd1(own, i, a0, b0);
  WAIT_LGKM0();
  __builtin_amdgcn_sched_barrier(0);
#pragma unroll
  for (int i = 0; i < 14; ++i) acc[i / 7][i % 7] = (f32x4){0.f, 0.f, 0.f, 0.f};
#pragma unroll 1
  for (int r = 0; r < 8; r += 2) {
    round(std::integral_constant<int, 0>{}, a0, b0, a1, b1, own + 64 * r + 64);
    round(std::integral_constant<int, 0>{}, a1, b1, a0, b0, r < 6 ? own + 64 * r + 128 : shd);
    if (r == PFW_R) {
#pragma unroll
      for (int i = 0; i < 3; ++i) {
        const unsigned g = ci * 256 + tid + NCV * 256 * i;
        if (g < 114688) touch_pol((const char*)(g < 57344 ? w3 : w4) + (size_t)(g < 57344 ? g : g - 57344) * 128, lb + 163584);
      }
    }
  }
#pragma unroll 1
  for (int r = 0; r < 4; r += 2) {
    round(std::integral_constant<int, 1>{}, a0, b0, a1, b1, shd + 64 * r + 64);
    round(std::integral_constant<int, 1>{}, a1, b1, a0, b0, shd + 64 * r + 128);
  }
  CTS(11);
  finish(std::integral_constant<int, 2>{}, std::integral_constant<int, 6>{}, acc);
  CTS(7);
  WAIT_VM(0);
  __syncthreads();
  if (tid == 0) __hip_atomic_store(fy + 256 * 32, ep, __ATOMIC_RELAXED, __HIP_MEMORY_SCOPE_AGENT);
}
__device__ __forceinline__ float sum2_seg(float s, float q) {
  s += __builtin_bit_cast(float, __builtin_amdgcn_update_dpp(0, __builtin_bit_cast(int, s), 0x128, 0xf, 0xf, true));
  q += __builtin_bit_cast(float, __builtin_amdgcn_update_dpp(0, __builtin_bit_cast(int, q), 0x128, 0xf, 0xf, true));
  asm volatile("s_nop 1\n\tv_permlane16_swap_b32 %0, %1" : "+v"(s), "+v"(q));
  s += q;
  q = s;
  asm volatile("s_nop 1\n\tv_permlane32_swap_b32 %0, %1" : "+v"(s), "+v"(q));
  return s + q;
}
#define LASTSLEEP 8
__device__ __forceinline__ void wait_flags(const char* p, int lane, int n, unsigned ep) {
  gu32* f = (gu32*)(p + (lane < n ? lane : 0) * 128);
  for (int it = 0; it < 10000; ++it) {
    if (__all(__hip_atomic_load(f, __ATOMIC_RELAXED, __HIP_MEMORY_SCOPE_AGENT) == ep)) break;
    __builtin_amdgcn_s_sleep(LASTSLEEP);
  }
  asm volatile("" ::: "memory");
}
__device__ __forceinline__ void row0_q(const u16* __restrict__ q, int t, int lane, int wave, u32x4v (&xq)[4]) {
#pragma unroll
  for (int i = 0; i < 4; ++i) xq[i] = __builtin_nontemporal_load((const u32x4v*)(q + (size_t)(t * 8 + (lane & 7)) * D_ + wave * 256 + i * 64 + (lane >> 3) * 8));
}
__device__ __forceinline__ void row0_y(char* __restrict__ ws, int t, int tid, u32x4v (&yc)[6]) {
  const __amdgpu_buffer_rsrc_t rs = __builtin_amdgcn_make_buffer_rsrc(ws, 0, WS_USED, 0x00020000);
#pragma unroll
  for (int i = 0; i < 6; ++i) {
    const int x = tid + 256 * i;
    yc[i] = __builtin_bit_cast(u32x4v, __builtin_amdgcn_raw_buffer_load_b128(rs, t * (NCV * 128) + (x < NCV * 8 ? x : 0) * 16, OFF_Y, 16));
  }
}
__device__ __forceinline__ void row_stats(const u32x4v (&xq)[4], char* lds, int k, int lane, int wave, float& mean, float& var) {
  f32x2* red = (f32x2*)(lds + 16640);
  const int r8 = lane & 7, seg = lane >> 3;
  f32x2 s = {0.f, 0.f}, sq = {0.f, 0.f};
#pragma unroll
  for (int i = 0; i < 16; ++i) { const f32x2 x = up2(xq[i >> 2][i & 3]); s += x; sq = __builtin_elementwise_fma(x, x, sq); }
  const float a = sum2_seg(s[0] + s[1], sq[0] + sq[1]);
  if ((seg & 5) == 0) ((float*)red)[((k * 4 + wave) * 8 + r8) * 2 + (seg >> 1)] = a;
  __syncthreads();
  const f32x2 t = (red[(k * 4) * 8 + r8] + red[(k * 4 + 1) * 8 + r8]) + (red[(k * 4 + 2) * 8 + r8] + red[(k * 4 + 3) * 8 + r8]);
  mean = t[0] * (1.0f / 1024.0f);
  var = fmaxf(t[1] * (1.0f / 1024.0f) - mean * mean, 0.f);
}
__device__ __forceinline__ void row0_body(char* __restrict__ ws, char* lds, int t, int k, int lane, int wave, u32x4v (&xq)[4], const u32x4v (&yc)[6],
                                          float mean, float var) {
  const __amdgpu_buffer_rsrc_t rs = __builtin_amdgcn_make_buffer_rsrc(ws, 0, WS_USED, 0x00020000);
  const int r8 = lane & 7, seg = lane >> 3, tid = wave * 64 + lane, blk = t >> 2, qt = t & 3;
  const unsigned fo = blk * 65536 + (qt >> 1) * 32768 + (wave * 8 + (seg >> 2)) * 1024 + ((seg & 3) * 16 + (qt & 1) * 8 + r8) * 16;
  const bool odd = lane & 1;
#pragma unroll
  for (int i = 0; i < 6; ++i) {
    const int x = tid + 256 * i, c = (x >> 3) * 6 + (x & 6);
    const unsigned s0 = odd ? yc[i][0] : yc[i][2], s1 = odd ? yc[i][1] : yc[i][3];
    const unsigned r0 = __builtin_amdgcn_update_dpp(0, (int)s0, 0xb1, 0xf, 0xf, true), r1 = __builtin_amdgcn_update_dpp(0, (int)s1, 0xb1, 0xf, 0xf, true);
    const unsigned lo0 = odd ? r0 : yc[i][0], lo1 = odd ? r1 : yc[i][1], hi0 = odd ? yc[i][2] : r0, hi1 = odd ? yc[i][3] : r1;
    if (x < NCV * 8 && (x & 7) < 6 && c < D_) {
      char* p = lds + (odd ? 4 : 0) * 2064 + c * 2;
      *(unsigned*)(p) = __builtin_amdgcn_perm(hi0, lo0, 0x05040100u);
      *(unsigned*)(p + 2064) = __builtin_amdgcn_perm(hi0, lo0, 0x07060302u);
      *(unsigned*)(p + 2 * 2064) = __builtin_amdgcn_perm(hi1, lo1, 0x05040100u);
      *(unsigned*)(p + 3 * 2064) = __builtin_amdgcn_perm(hi1, lo1, 0x07060302u);
    }
  }
  __syncthreads();
  const float rd = __builtin_amdgcn_rsqf(var + 1e-5f);
  const f32x2 rd2 = {rd, rd}, nm2 = {-mean * rd, -mean * rd};
#pragma unroll
  for (int i = 0; i < 4; ++i) {
    const u32x4v y4 = *(const u32x4v*)(lds + r8 * 2064 + wave * 512 + i * 128 + seg * 16);
    __builtin_amdgcn_raw_buffer_store_b128(y4, rs, fo + i * 2048, OFF_YF, 17);
#pragma unroll
    for (int e = 0; e < 4; ++e) {
      const f32x2 x = up2(xq[i][e]);
      xq[i][e] = pk2(__builtin_elementwise_fma(__builtin_elementwise_fma(x, rd2, nm2), up2(y4[e]), x));
    }
    __builtin_amdgcn_raw_buffer_store_b128(xq[i], rs, fo + i * 2048, OFF_XP, 17);
  }
  row_stats(xq, lds, k, lane, wave, mean, var);
  if (wave == 0 && seg < 2) {
    const float a = var + 1e-5f, b = fmaf(1e-5f, a, var), rd1 = __builtin_amdgcn_rsqf(a), rdd = __builtin_amdgcn_rsqf(b);
    const float k1 = -2.0f * 0.7978845608028654f * 1.4426950408889634f;
    const f32x4 o = seg == 0 ? (f32x4){mean, k1 * rdd, k1 * 0.044715f * rdd * rdd * rdd, __builtin_amdgcn_sqrtf(b)} : (f32x4){-mean * rd1, rd1, 0.f, 0.f};
    __builtin_amdgcn_raw_buffer_store_b128(__builtin_bit_cast(u32x4v, o), rs, blk * 1024 + (qt * 8 + r8) * 32 + seg * 16, OFF_S1, 17);
  }
}
__device__ __forceinline__ void row0_flag(char* __restrict__ ws, int t, int lane, int wave, unsigned ep) {
  if (lane == 0) __hip_atomic_store((gu32*)(ws + OFF_FXP + (t >> 2) * 2048 + ((t & 3) * 4 + wave) * 128), ep, __ATOMIC_RELAXED, __HIP_MEMORY_SCOPE_AGENT);
}
__device__ __forceinline__ void row_last(char* __restrict__ ws, u16* __restrict__ out, int t, int lane, int wave, unsigned ep) {
  const __amdgpu_buffer_rsrc_t rs = __builtin_amdgcn_make_buffer_rsrc(ws, 0, WS_USED, 0x00020000);
  const int r8 = lane & 7, seg = lane >> 3, qt = t & 3, vbp = LB + (t >> 2);
  const unsigned fo = vbp * 65536 + (qt >> 1) * 32768 + (wave * 8 + (seg >> 2)) * 1024 + ((seg & 3) * 16 + (qt & 1) * 8 + r8) * 16;
  wait_flags(ws + OFF_FP + (2 * LB + (t >> 1)) * 2048, lane, 16, ep);
  u32x4v p0[4], p1[4];
#pragma unroll
  for (int i = 0; i < 4; ++i) {
    p0[i] = __builtin_bit_cast(u32x4v, __builtin_amdgcn_raw_buffer_load_b128(rs, fo + i * 2048, OFF_P0, 16));
    p1[i] = __builtin_bit_cast(u32x4v, __builtin_amdgcn_raw_buffer_load_b128(rs, fo + i * 2048, OFF_P1, 16));
  }
#pragma unroll
  for (int i = 0; i < 4; ++i) {
    u32x4v o;
#pragma unroll
    for (int e = 0; e < 4; ++e) o[e] = pk2(up2(p0[i][e]) + up2(p1[i][e]));
    st16(out + (size_t)(t * 8 + r8) * D_ + wave * 256 + i * 64 + seg * 8, __builtin_bit_cast(u16x8, o));
  }
}

__device__ __forceinline__ int swzB(int r) { return ((((r >> 1) & 1) | (((r >> 3) & 1) << 1)) << 1); }
#define ASM_G12(W) asm volatile( \
  "Lcode_g12_b:\n" \
  "s_cmp_eq_u32 %8, 2\n" \
  "s_cbranch_scc1 Lg2_begin_%=\n" \
  "s_mov_b64 s[36:37], %1\n" \
  "s_mov_b32 s38, 111193216\n" \
  "s_mov_b32 s39, 0x00020000\n" \
  "s_mov_b32 s54, %2\n" \
  "s_mov_b32 s55, %3\n" \
  "s_mov_b32 s56, %4\n" \
  "s_mov_b32 s58, %5\n" \
  "s_mov_b32 s83, %6\n" \
  "s_mov_b32 s82, %7\n" \
  "s_mov_b32 s84, %8\n" \
  "v_mov_b32 v0, %9\n" \
  "v_mov_b32 v2, %10\n" \
  "v_mov_b32 v3, %11\n" \
  "v_mov_b32 v4, %12\n" \
  "v_mov_b32 v6, %13\n" \
  "v_mov_b32 v7, %14\n" \
  "v_mov_b32 v8, %15\n" \
  "v_mov_b32 v9, %16\n" \
  "v_mov_b32 v10, %17\n" \
  "v_mov_b32 v11, %18\n" \
  "v_mov_b32 v12, %19\n" \
  "v_mov_b32 v13, %20\n" \
  "v_mov_b32 v14, %21\n" \
  "v_mov_b32 v24, %22\n" \
  "v_mov_b32 v25, %23\n" \
  "v_mov_b32 v26, %24\n" \
  "v_mov_b32 v27, %25\n" \
  "v_mov_b32 v28, %26\n" \
  "s_mov_b64 s[40:41], %27\n" \
  "s_mov_b32 s42, 2097152\n" \
  "s_mov_b32 s43, 0x00020000\n" \
  "s_lshl_b32 s88, s54, 11\n" \
  "s_mov_b32 s69, 111193088\n" \
  "buffer_load_dword v5, off, s[36:39], s69 sc1\n" \
  "s_add_u32 s61, s82, 0\n" \
  "s_add_u32 m0, s88, 0\n" \
  "s_nop 0\n" \
  "buffer_load_dwordx4 v24, s[40:43], s61 offen offset:0  lds\n" \
  "s_add_u32 m0, s88, 1024\n" \
  "s_nop 0\n" \
  "buffer_load_dwordx4 v25, s[40:43], s61 offen offset:0  lds\n" \
  "s_add_u32 m0, s88, 8064\n" \
  "s_nop 0\n" \
  "buffer_load_dwordx4 v24, s[40:43], s61 offen offset:128  lds\n" \
  "s_add_u32 m0, s88, 9088\n" \
  "s_nop 0\n" \
  "buffer_load_dwordx4 v25, s[40:43], s61 offen offset:128  lds\n" \
  "s_add_u32 s61, s82, 131072\n" \
  "s_add_u32 m0, s88, 16384\n" \
  "s_nop 0\n" \
  "buffer_load_dwordx4 v24, s[40:43], s61 offen offset:0  lds\n" \
  "s_add_u32 m0, s88, 17408\n" \
  "s_nop 0\n" \
  "buffer_load_dwordx4 v25, s[40:43], s61 offen offset:0  lds\n" \
  "s_add_u32 m0, s88, 24448\n" \
  "s_nop 0\n" \
  "buffer_load_dwordx4 v24, s[40:43], s61 offen offset:128  lds\n" \
  "s_add_u32 m0, s88, 25472\n" \
  "s_nop 0\n" \
  "buffer_load_dwordx4 v25, s[40:43], s61 offen offset:128  lds\n" \
  "s_add_u32 s61, s82, 262144\n" \
  "s_add_u32 m0, s88, 32768\n" \
  "s_nop 0\n" \
  "buffer_load_dwordx4 v24, s[40:43], s61 offen offset:0  lds\n" \
  "s_add_u32 m0, s88, 33792\n" \
  "s_nop 0\n" \
  "buffer_load_dwordx4 v25, s[40:43], s61 offen offset:0  lds\n" \
  "s_add_u32 m0, s88, 40832\n" \
  "s_nop 0\n" \
  "buffer_load_dwordx4 v24, s[40:43], s61 offen offset:128  lds\n" \
  "s_add_u32 m0, s88, 41856\n" \
  "s_nop 0\n" \
  "buffer_load_dwordx4 v25, s[40:43], s61 offen offset:128  lds\n" \
  "s_add_u32 s61, s82, 393216\n" \
  "s_add_u32 m0, s88, 49152\n" \
  "s_nop 0\n" \
  "buffer_load_dwordx4 v24, s[40:43], s61 offen offset:0  lds\n" \
  "s_add_u32 m0, s88, 50176\n" \
  "s_nop 0\n" \
  "buffer_load_dwordx4 v25, s[40:43], s61 offen offset:0  lds\n" \
  "s_add_u32 m0, s88, 57216\n" \
  "s_nop 0\n" \
  "buffer_load_dwordx4 v24, s[40:43], s61 offen offset:128  lds\n" \
  "s_add_u32 m0, s88, 58240\n" \
  "s_nop 0\n" \
  "buffer_load_dwordx4 v25, s[40:43], s61 offen offset:128  lds\n" \
  "s_add_u32 s61, s82, 524288\n" \
  "s_add_u32 m0, s88, 65536\n" \
  "s_nop 0\n" \
  "buffer_load_dwordx4 v24, s[40:43], s61 offen offset:0  lds\n" \
  "s_add_u32 m0, s88, 66560\n" \
  "s_nop 0\n" \
  "buffer_load_dwordx4 v25, s[40:43], s61 offen offset:0  lds\n" \
  "s_add_u32 m0, s88, 73600\n" \
  "s_nop 0\n" \
  "buffer_load_dwordx4 v24, s[40:43], s61 offen offset:128  lds\n" \
  "s_add_u32 m0, s88, 74624\n" \
  "s_nop 0\n" \
  "buffer_load_dwordx4 v25, s[40:43], s61 offen offset:128  lds\n" \
  "s_add_u32 s61, s82, 655360\n" \
  "s_add_u32 m0, s88, 81920\n" \
  "s_nop 0\n" \
  "buffer_load_dwordx4 v24, s[40:43], s61 offen offset:0  lds\n" \
  "s_add_u32 m0, s88, 82944\n" \
  "s_nop 0\n" \
  "buffer_load_dwordx4 v25, s[40:43], s61 offen offset:0  lds\n" \
  "s_add_u32 m0, s88, 89984\n" \
  "s_nop 0\n" \
  "buffer_load_dwordx4 v24, s[40:43], s61 offen offset:128  lds\n" \
  "s_add_u32 m0, s88, 91008\n" \
  "s_nop 0\n" \
  "buffer_load_dwordx4 v25, s[40:43], s61 offen offset:128  lds\n" \
  "s_waitcnt vmcnt(20)\n" \
  "s_waitcnt lgkmcnt(0)\n" \
  "s_barrier\n" \
  "s_add_u32 s61, s82, 786432\n" \
  "s_add_u32 m0, s88, 98304\n" \
  "s_nop 0\n" \
  "buffer_load_dwordx4 v24, s[40:43], s61 offen offset:0  lds\n" \
  "s_add_u32 m0, s88, 99328\n" \
  "s_nop 0\n" \
  "buffer_load_dwordx4 v25, s[40:43], s61 offen offset:0  lds\n" \
  "s_add_u32 m0, s88, 106368\n" \
  "s_nop 0\n" \
  "buffer_load_dwordx4 v24, s[40:43], s61 offen offset:128  lds\n" \
  "s_add_u32 m0, s88, 107392\n" \
  "s_nop 0\n" \
  "buffer_load_dwordx4 v25, s[40:43], s61 offen offset:128  lds\n" \
  "v_add_u32 v72, 0, v26\n" \
  "v_add_u32 v73, 0, v27\n" \
  "ds_read_b64_tr_b16 a[0:1], v72 offset:0\n" \
  "ds_read_b64_tr_b16 a[2:3], v72 offset:512\n" \
  "ds_read_b64_tr_b16 a[4:5], v73 offset:0\n" \
  "ds_read_b64_tr_b16 a[6:7], v73 offset:512\n" \
  "ds_read_b64_tr_b16 a[8:9], v72 offset:4096\n" \
  "ds_read_b64_tr_b16 a[10:11], v72 offset:4608\n" \
  "ds_read_b64_tr_b16 a[12:13], v73 offset:4096\n" \
  "ds_read_b64_tr_b16 a[14:15], v73 offset:4608\n" \
  "s_waitcnt vmcnt(20)\n" \
  "s_waitcnt lgkmcnt(0)\n" \
  "s_barrier\n" \
  "s_add_u32 s61, s82, 917504\n" \
  "s_add_u32 m0, s88, 0\n" \
  "s_nop 0\n" \
  "buffer_load_dwordx4 v24, s[40:43], s61 offen offset:0  lds\n" \
  "s_add_u32 m0, s88, 1024\n" \
  "s_nop 0\n" \
  "buffer_load_dwordx4 v25, s[40:43], s61 offen offset:0  lds\n" \
  "s_add_u32 m0, s88, 8064\n" \
  "s_nop 0\n" \
  "buffer_load_dwordx4 v24, s[40:43], s61 offen offset:128  lds\n" \
  "s_add_u32 m0, s88, 9088\n" \
  "s_nop 0\n" \
  "buffer_load_dwordx4 v25, s[40:43], s61 offen offset:128  lds\n" \
  "v_add_u32 v72, 16384, v26\n" \
  "v_add_u32 v73, 16384, v27\n" \
  "ds_read_b64_tr_b16 a[16:17], v72 offset:0\n" \
  "ds_read_b64_tr_b16 a[18:19], v72 offset:512\n" \
  "ds_read_b64_tr_b16 a[20:21], v73 offset:0\n" \
  "ds_read_b64_tr_b16 a[22:23], v73 offset:512\n" \
  "ds_read_b64_tr_b16 a[24:25], v72 offset:4096\n" \
  "ds_read_b64_tr_b16 a[26:27], v72 offset:4608\n" \
  "ds_read_b64_tr_b16 a[28:29], v73 offset:4096\n" \
  "ds_read_b64_tr_b16 a[30:31], v73 offset:4608\n" \
  "s_waitcnt vmcnt(20)\n" \
  "s_waitcnt lgkmcnt(0)\n" \
  "s_barrier\n" \
  "s_add_u32 s61, s82, 1048576\n" \
  "s_add_u32 m0, s88, 16384\n" \
  "s_nop 0\n" \
  "buffer_load_dwordx4 v24, s[40:43], s61 offen offset:0  lds\n" \
  "s_add_u32 m0, s88, 17408\n" \
  "s_nop 0\n" \
  "buffer_load_dwordx4 v25, s[40:43], s61 offen offset:0  lds\n" \
  "s_add_u32 m0, s88, 24448\n" \
  "s_nop 0\n" \
  "buffer_load_dwordx4 v24, s[40:43], s61 offen offset:128  lds\n" \
  "s_add_u32 m0, s88, 25472\n" \
  "s_nop 0\n" \
  "buffer_load_dwordx4 v25, s[40:43], s61 offen offset:128  lds\n" \
  "v_add_u32 v72, 32768, v26\n" \
  "v_add_u32 v73, 32768, v27\n" \
  "ds_read_b64_tr_b16 a[32:33], v72 offset:0\n" \
  "ds_read_b64_tr_b16 a[34:35], v72 offset:512\n" \
  "ds_read_b64_tr_b16 a[36:37], v73 offset:0\n" \
  "ds_read_b64_tr_b16 a[38:39], v73 offset:512\n" \
  "ds_read_b64_tr_b16 a[40:41], v72 offset:4096\n" \
  "ds_read_b64_tr_b16 a[42:43], v72 offset:4608\n" \
  "ds_read_b64_tr_b16 a[44:45], v73 offset:4096\n" \
  "ds_read_b64_tr_b16 a[46:47], v73 offset:4608\n" \
  "s_waitcnt vmcnt(20)\n" \
  "s_waitcnt lgkmcnt(0)\n" \
  "s_barrier\n" \
  "s_add_u32 s61, s82, 1179648\n" \
  "s_add_u32 m0, s88, 32768\n" \
  "s_nop 0\n" \
  "buffer_load_dwordx4 v24, s[40:43], s61 offen offset:0  lds\n" \
  "s_add_u32 m0, s88, 33792\n" \
  "s_nop 0\n" \
  "buffer_load_dwordx4 v25, s[40:43], s61 offen offset:0  lds\n" \
  "s_add_u32 m0, s88, 40832\n" \
  "s_nop 0\n" \
  "buffer_load_dwordx4 v24, s[40:43], s61 offen offset:128  lds\n" \
  "s_add_u32 m0, s88, 41856\n" \
  "s_nop 0\n" \
  "buffer_load_dwordx4 v25, s[40:43], s61 offen offset:128  lds\n" \
  "v_add_u32 v72, 49152, v26\n" \
  "v_add_u32 v73, 49152, v27\n" \
  "ds_read_b64_tr_b16 a[48:49], v72 offset:0\n" \
  "ds_read_b64_tr_b16 a[50:51], v72 offset:512\n" \
  "ds_read_b64_tr_b16 a[52:53], v73 offset:0\n" \
  "ds_read_b64_tr_b16 a[54:55], v73 offset:512\n" \
  "ds_read_b64_tr_b16 a[56:57], v72 offset:4096\n" \
  "ds_read_b64_tr_b16 a[58:59], v72 offset:4608\n" \
  "ds_read_b64_tr_b16 a[60:61], v73 offset:4096\n" \
  "ds_read_b64_tr_b16 a[62:63], v73 offset:4608\n" \
  "s_waitcnt vmcnt(20)\n" \
  "s_waitcnt lgkmcnt(0)\n" \
  "s_barrier\n" \
  "s_add_u32 s61, s82, 1310720\n" \
  "s_add_u32 m0, s88, 49152\n" \
  "s_nop 0\n" \
  "buffer_load_dwordx4 v24, s[40:43], s61 offen offset:0  lds\n" \
  "s_add_u32 m0, s88, 50176\n" \
  "s_nop 0\n" \
  "buffer_load_dwordx4 v25, s[40:43], s61 offen offset:0  lds\n" \
  "s_add_u32 m0, s88, 57216\n" \
  "s_nop 0\n" \
  "buffer_load_dwordx4 v24, s[40:43], s61 offen offset:128  lds\n" \
  "s_add_u32 m0, s88, 58240\n" \
  "s_nop 0\n" \
  "buffer_load_dwordx4 v25, s[40:43], s61 offen offset:128  lds\n" \
  "v_add_u32 v72, 65536, v26\n" \
  "v_add_u32 v73, 65536, v27\n" \
  "ds_read_b64_tr_b16 a[64:65], v72 offset:0\n" \
  "ds_read_b64_tr_b16 a[66:67], v72 offset:512\n" \
  "ds_read_b64_tr_b16 a[68:69], v73 offset:0\n" \
  "ds_read_b64_tr_b16 a[70:71], v73 offset:512\n" \
  "ds_read_b64_tr_b16 a[72:73], v72 offset:4096\n" \
  "ds_read_b64_tr_b16 a[74:75], v72 offset:4608\n" \
  "ds_read_b64_tr_b16 a[76:77], v73 offset:4096\n" \
  "ds_read_b64_tr_b16 a[78:79], v73 offset:4608\n" \
  "s_waitcnt vmcnt(20)\n" \
  "s_waitcnt lgkmcnt(0)\n" \
  "s_barrier\n" \
  "s_add_u32 s61, s82, 1441792\n" \
  "s_add_u32 m0, s88, 65536\n" \
  "s_nop 0\n" \
  "buffer_load_dwordx4 v24, s[40:43], s61 offen offset:0  lds\n" \
  "s_add_u32 m0, s88, 66560\n" \
  "s_nop 0\n" \
  "buffer_load_dwordx4 v25, s[40:43], s61 offen offset:0  lds\n" \
  "s_add_u32 m0, s88, 73600\n" \
  "s_nop 0\n" \
  "buffer_load_dwordx4 v24, s[40:43], s61 offen offset:128  lds\n" \
  "s_add_u32 m0, s88, 74624\n" \
  "s_nop 0\n" \
  "buffer_load_dwordx4 v25, s[40:43], s61 offen offset:128  lds\n" \
  "v_add_u32 v72, 81920, v26\n" \
  "v_add_u32 v73, 81920, v27\n" \
  "ds_read_b64_tr_b16 a[80:81], v72 offset:0\n" \
  "ds_read_b64_tr_b16 a[82:83], v72 offset:512\n" \
  "ds_read_b64_tr_b16 a[84:85], v73 offset:0\n" \
  "ds_read_b64_tr_b16 a[86:87], v73 offset:512\n" \
  "ds_read_b64_tr_b16 a[88:89], v72 offset:4096\n" \
  "ds_read_b64_tr_b16 a[90:91], v72 offset:4608\n" \
  "ds_read_b64_tr_b16 a[92:93], v73 offset:4096\n" \
  "ds_read_b64_tr_b16 a[94:95], v73 offset:4608\n" \
  "s_waitcnt vmcnt(20)\n" \
  "s_waitcnt lgkmcnt(0)\n" \
  "s_barrier\n" \
  "s_add_u32 s61, s82, 1572864\n" \
  "s_add_u32 m0, s88, 81920\n" \
  "s_nop 0\n" \
  "buffer_load_dwordx4 v24, s[40:43], s61 offen offset:0  lds\n" \
  "s_add_u32 m0, s88, 82944\n" \
  "s_nop 0\n" \
  "buffer_load_dwordx4 v25, s[40:43], s61 offen offset:0  lds\n" \
  "s_add_u32 m0, s88, 89984\n" \
  "s_nop 0\n" \
  "buffer_load_dwordx4 v24, s[40:43], s61 offen offset:128  lds\n" \
  "s_add_u32 m0, s88, 91008\n" \
  "s_nop 0\n" \
  "buffer_load_dwordx4 v25, s[40:43], s61 offen offset:128  lds\n" \
  "v_add_u32 v72, 98304, v26\n" \
  "v_add_u32 v73, 98304, v27\n" \
  "ds_read_b64_tr_b16 a[96:97], v72 offset:0\n" \
  "ds_read_b64_tr_b16 a[98:99], v72 offset:512\n" \
  "ds_read_b64_tr_b16 a[100:101], v73 offset:0\n" \
  "ds_read_b64_tr_b16 a[102:103], v73 offset:512\n" \
  "ds_read_b64_tr_b16 a[104:105], v72 offset:4096\n" \
  "ds_read_b64_tr_b16 a[106:107], v72 offset:4608\n" \
  "ds_read_b64_tr_b16 a[108:109], v73 offset:4096\n" \
  "ds_read_b64_tr_b16 a[110:111], v73 offset:4608\n" \
  "s_waitcnt vmcnt(20)\n" \
  "s_waitcnt lgkmcnt(0)\n" \
  "s_barrier\n" \
  "s_add_u32 s61, s82, 1703936\n" \
  "s_add_u32 m0, s88, 98304\n" \
  "s_nop 0\n" \
  "buffer_load_dwordx4 v24, s[40:43], s61 offen offset:0  lds\n" \
  "s_add_u32 m0, s88, 99328\n" \
  "s_nop 0\n" \
  "buffer_load_dwordx4 v25, s[40:43], s61 offen offset:0  lds\n" \
  "s_add_u32 m0, s88, 106368\n" \
  "s_nop 0\n" \
  "buffer_load_dwordx4 v24, s[40:43], s61 offen offset:128  lds\n" \
  "s_add_u32 m0, s88, 107392\n" \
  "s_nop 0\n" \
  "buffer_load_dwordx4 v25, s[40:43], s61 offen offset:128  lds\n" \
  "v_add_u32 v72, 0, v26\n" \
  "v_add_u32 v73, 0, v27\n" \
  "ds_read_b64_tr_b16 a[112:113], v72 offset:0\n" \
  "ds_read_b64_tr_b16 a[114:115], v72 offset:512\n" \
  "ds_read_b64_tr_b16 a[116:117], v73 offset:0\n" \
  "ds_read_b64_tr_b16 a[118:119], v73 offset:512\n" \
  "ds_read_b64_tr_b16 a[120:121], v72 offset:4096\n" \
  "ds_read_b64_tr_b16 a[122:123], v72 offset:4608\n" \
  "ds_read_b64_tr_b16 a[124:125], v73 offset:4096\n" \
  "ds_read_b64_tr_b16 a[126:127], v73 offset:4608\n" \
  "s_waitcnt vmcnt(20)\n" \
  "s_waitcnt lgkmcnt(0)\n" \
  "s_barrier\n" \
  "s_add_u32 s61, s82, 1835008\n" \
  "s_add_u32 m0, s88, 0\n" \
  "s_nop 0\n" \
  "buffer_load_dwordx4 v24, s[40:43], s61 offen offset:0  lds\n" \
  "s_add_u32 m0, s88, 1024\n" \
  "s_nop 0\n" \
  "buffer_load_dwordx4 v25, s[40:43], s61 offen offset:0  lds\n" \
  "s_add_u32 m0, s88, 8064\n" \
  "s_nop 0\n" \
  "buffer_load_dwordx4 v24, s[40:43], s61 offen offset:128  lds\n" \
  "s_add_u32 m0, s88, 9088\n" \
  "s_nop 0\n" \
  "buffer_load_dwordx4 v25, s[40:43], s61 offen offset:128  lds\n" \
  "v_add_u32 v72, 16384, v26\n" \
  "v_add_u32 v73, 16384, v27\n" \
  "ds_read_b64_tr_b16 a[128:129], v72 offset:0\n" \
  "ds_read_b64_tr_b16 a[130:131], v72 offset:512\n" \
  "ds_read_b64_tr_b16 a[132:133], v73 offset:0\n" \
  "ds_read_b64_tr_b16 a[134:135], v73 offset:512\n" \
  "ds_read_b64_tr_b16 a[136:137], v72 offset:4096\n" \
  "ds_read_b64_tr_b16 a[138:139], v72 offset:4608\n" \
  "ds_read_b64_tr_b16 a[140:141], v73 offset:4096\n" \
  "ds_read_b64_tr_b16 a[142:143], v73 offset:4608\n" \
  "s_waitcnt vmcnt(20)\n" \
  "s_waitcnt lgkmcnt(0)\n" \
  "s_barrier\n" \
  "s_add_u32 s61, s82, 1966080\n" \
  "s_add_u32 m0, s88, 16384\n" \
  "s_nop 0\n" \
  "buffer_load_dwordx4 v24, s[40:43], s61 offen offset:0  lds\n" \
  "s_add_u32 m0, s88, 17408\n" \
  "s_nop 0\n" \
  "buffer_load_dwordx4 v25, s[40:43], s61 offen offset:0  lds\n" \
  "s_add_u32 m0, s88, 24448\n" \
  "s_nop 0\n" \
  "buffer_load_dwordx4 v24, s[40:43], s61 offen offset:128  lds\n" \
  "s_add_u32 m0, s88, 25472\n" \
  "s_nop 0\n" \
  "buffer_load_dwordx4 v25, s[40:43], s61 offen offset:128  lds\n" \
  "v_add_u32 v72, 32768, v26\n" \
  "v_add_u32 v73, 32768, v27\n" \
  "ds_read_b64_tr_b16 a[144:145], v72 offset:0\n" \
  "ds_read_b64_tr_b16 a[146:147], v72 offset:512\n" \
  "ds_read_b64_tr_b16 a[148:149], v73 offset:0\n" \
  "ds_read_b64_tr_b16 a[150:151], v73 offset:512\n" \
  "ds_read_b64_tr_b16 a[152:153], v72 offset:4096\n" \
  "ds_read_b64_tr_b16 a[154:155], v72 offset:4608\n" \
  "ds_read_b64_tr_b16 a[156:157], v73 offset:4096\n" \
  "ds_read_b64_tr_b16 a[158:159], v73 offset:4608\n" \
  "s_waitcnt vmcnt(20)\n" \
  "s_waitcnt lgkmcnt(0)\n" \
  "s_barrier\n" \
  "v_add_u32 v72, 49152, v26\n" \
  "v_add_u32 v73, 49152, v27\n" \
  "ds_read_b64_tr_b16 a[160:161], v72 offset:0\n" \
  "ds_read_b64_tr_b16 a[162:163], v72 offset:512\n" \
  "ds_read_b64_tr_b16 a[164:165], v73 offset:0\n" \
  "ds_read_b64_tr_b16 a[166:167], v73 offset:512\n" \
  "ds_read_b64_tr_b16 a[168:169], v72 offset:4096\n" \
  "ds_read_b64_tr_b16 a[170:171], v72 offset:4608\n" \
  "ds_read_b64_tr_b16 a[172:173], v73 offset:4096\n" \
  "ds_read_b64_tr_b16 a[174:175], v73 offset:4608\n" \
  "s_waitcnt vmcnt(16)\n" \
  "s_waitcnt lgkmcnt(0)\n" \
  "s_barrier\n" \
  "v_add_u32 v72, 65536, v26\n" \
  "v_add_u32 v73, 65536, v27\n" \
  "ds_read_b64_tr_b16 a[176:177], v72 offset:0\n" \
  "ds_read_b64_tr_b16 a[178:179], v72 offset:512\n" \
  "ds_read_b64_tr_b16 a[180:181], v73 offset:0\n" \
  "ds_read_b64_tr_b16 a[182:183], v73 offset:512\n" \
  "ds_read_b64_tr_b16 a[184:185], v72 offset:4096\n" \
  "ds_read_b64_tr_b16 a[186:187], v72 offset:4608\n" \
  "ds_read_b64_tr_b16 a[188:189], v73 offset:4096\n" \
  "ds_read_b64_tr_b16 a[190:191], v73 offset:4608\n" \
  "s_waitcnt vmcnt(12)\n" \
  "s_waitcnt lgkmcnt(0)\n" \
  "s_barrier\n" \
  "v_add_u32 v72, 81920, v26\n" \
  "v_add_u32 v73, 81920, v27\n" \
  "ds_read_b64_tr_b16 a[192:193], v72 offset:0\n" \
  "ds_read_b64_tr_b16 a[194:195], v72 offset:512\n" \
  "ds_read_b64_tr_b16 a[196:197], v73 offset:0\n" \
  "ds_read_b64_tr_b16 a[198:199], v73 offset:512\n" \
  "ds_read_b64_tr_b16 a[200:201], v72 offset:4096\n" \
  "ds_read_b64_tr_b16 a[202:203], v72 offset:4608\n" \
  "ds_read_b64_tr_b16 a[204:205], v73 offset:4096\n" \
  "ds_read_b64_tr_b16 a[206:207], v73 offset:4608\n" \
  "s_waitcnt vmcnt(8)\n" \
  "s_waitcnt lgkmcnt(0)\n" \
  "s_barrier\n" \
  "v_add_u32 v72, 98304, v26\n" \
  "v_add_u32 v73, 98304, v27\n" \
  "ds_read_b64_tr_b16 a[208:209], v72 offset:0\n" \
  "ds_read_b64_tr_b16 a[210:211], v72 offset:512\n" \
  "ds_read_b64_tr_b16 a[212:213], v73 offset:0\n" \
  "ds_read_b64_tr_b16 a[214:215], v73 offset:512\n" \
  "ds_read_b64_tr_b16 a[216:217], v72 offset:4096\n" \
  "ds_read_b64_tr_b16 a[218:219], v72 offset:4608\n" \
  "ds_read_b64_tr_b16 a[220:221], v73 offset:4096\n" \
  "ds_read_b64_tr_b16 a[222:223], v73 offset:4608\n" \
  "s_waitcnt vmcnt(4)\n" \
  "s_waitcnt lgkmcnt(0)\n" \
  "s_barrier\n" \
  "v_add_u32 v72, 0, v26\n" \
  "v_add_u32 v73, 0, v27\n" \
  "ds_read_b64_tr_b16 a[224:225], v72 offset:0\n" \
  "ds_read_b64_tr_b16 a[226:227], v72 offset:512\n" \
  "ds_read_b64_tr_b16 a[228:229], v73 offset:0\n" \
  "ds_read_b64_tr_b16 a[230:231], v73 offset:512\n" \
  "ds_read_b64_tr_b16 a[232:233], v72 offset:4096\n" \
  "ds_read_b64_tr_b16 a[234:235], v72 offset:4608\n" \
  "ds_read_b64_tr_b16 a[236:237], v73 offset:4096\n" \
  "ds_read_b64_tr_b16 a[238:239], v73 offset:4608\n" \
  "s_waitcnt vmcnt(0)\n" \
  "s_waitcnt lgkmcnt(0)\n" \
  "s_barrier\n" \
  "v_add_u32 v72, 16384, v26\n" \
  "v_add_u32 v73, 16384, v27\n" \
  "ds_read_b64_tr_b16 a[240:241], v72 offset:0\n" \
  "ds_read_b64_tr_b16 a[242:243], v72 offset:512\n" \
  "ds_read_b64_tr_b16 a[244:245], v73 offset:0\n" \
  "ds_read_b64_tr_b16 a[246:247], v73 offset:512\n" \
  "ds_read_b64_tr_b16 a[248:249], v72 offset:4096\n" \
  "ds_read_b64_tr_b16 a[250:251], v72 offset:4608\n" \
  "ds_read_b64_tr_b16 a[252:253], v73 offset:4096\n" \
  "ds_read_b64_tr_b16 a[254:255], v73 offset:4608\n" \
  "s_waitcnt lgkmcnt(0)\n" \
  "s_barrier\n" \
  "v_readfirstlane_b32 s60, v5\n" \
  "s_nop 3\n" \
  "s_add_u32 s60, s60, 1\n" \
  "s_cmp_eq_u32 s60, 0\n" \
  "s_cselect_b32 s60, 1, s60\n" \
  "v_mov_b32 v40, 0x3f803f80\n" \
  "v_mov_b32 v41, 0x3f803f80\n" \
  "v_mov_b32 v42, 0x3f803f80\n" \
  "v_mov_b32 v43, 0x3f803f80\n" \
  "s_nop 1\n" \
  "v_mfma_f32_16x16x32_bf16 v[16:19], a[0:3], v[40:43], 0\n" \
  "v_mfma_f32_16x16x32_bf16 v[20:23], a[4:7], v[40:43], 0\n" \
  "v_mfma_f32_16x16x32_bf16 v[16:19], a[8:11], v[40:43], v[16:19]\n" \
  "v_mfma_f32_16x16x32_bf16 v[20:23], a[12:15], v[40:43], v[20:23]\n" \
  "v_mfma_f32_16x16x32_bf16 v[16:19], a[16:19], v[40:43], v[16:19]\n" \
  "v_mfma_f32_16x16x32_bf16 v[20:23], a[20:23], v[40:43], v[20:23]\n" \
  "v_mfma_f32_16x16x32_bf16 v[16:19], a[24:27], v[40:43], v[16:19]\n" \
  "v_mfma_f32_16x16x32_bf16 v[20:23], a[28:31], v[40:43], v[20:23]\n" \
  "v_mfma_f32_16x16x32_bf16 v[16:19], a[32:35], v[40:43], v[16:19]\n" \
  "v_mfma_f32_16x16x32_bf16 v[20:23], a[36:39], v[40:43], v[20:23]\n" \
  "v_mfma_f32_16x16x32_bf16 v[16:19], a[40:43], v[40:43], v[16:19]\n" \
  "v_mfma_f32_16x16x32_bf16 v[20:23], a[44:47], v[40:43], v[20:23]\n" \
  "v_mfma_f32_16x16x32_bf16 v[16:19], a[48:51], v[40:43], v[16:19]\n" \
  "v_mfma_f32_16x16x32_bf16 v[20:23], a[52:55], v[40:43], v[20:23]\n" \
  "v_mfma_f32_16x16x32_bf16 v[16:19], a[56:59], v[40:43], v[16:19]\n" \
  "v_mfma_f32_16x16x32_bf16 v[20:23], a[60:63], v[40:43], v[20:23]\n" \
  "v_mfma_f32_16x16x32_bf16 v[16:19], a[64:67], v[40:43], v[16:19]\n" \
  "v_mfma_f32_16x16x32_bf16 v[20:23], a[68:71], v[40:43], v[20:23]\n" \
  "v_mfma_f32_16x16x32_bf16 v[16:19], a[72:75], v[40:43], v[16:19]\n" \
  "v_mfma_f32_16x16x32_bf16 v[20:23], a[76:79], v[40:43], v[20:23]\n" \
  "v_mfma_f32_16x16x32_bf16 v[16:19], a[80:83], v[40:43], v[16:19]\n" \
  "v_mfma_f32_16x16x32_bf16 v[20:23], a[84:87], v[40:43], v[20:23]\n" \
  "v_mfma_f32_16x16x32_bf16 v[16:19], a[88:91], v[40:43], v[16:19]\n" \
  "v_mfma_f32_16x16x32_bf16 v[20:23], a[92:95], v[40:43], v[20:23]\n" \
  "v_mfma_f32_16x16x32_bf16 v[16:19], a[96:99], v[40:43], v[16:19]\n" \
  "v_mfma_f32_16x16x32_bf16 v[20:23], a[100:103], v[40:43], v[20:23]\n" \
  "v_mfma_f32_16x16x32_bf16 v[16:19], a[104:107], v[40:43], v[16:19]\n" \
  "v_mfma_f32_16x16x32_bf16 v[20:23], a[108:111], v[40:43], v[20:23]\n" \
  "v_mfma_f32_16x16x32_bf16 v[16:19], a[112:115], v[40:43], v[16:19]\n" \
  "v_mfma_f32_16x16x32_bf16 v[20:23], a[116:119], v[40:43], v[20:23]\n" \
  "v_mfma_f32_16x16x32_bf16 v[16:19], a[120:123], v[40:43], v[16:19]\n" \
  "v_mfma_f32_16x16x32_bf16 v[20:23], a[124:127], v[40:43], v[20:23]\n" \
  "v_mfma_f32_16x16x32_bf16 v[16:19], a[128:131], v[40:43], v[16:19]\n" \
  "v_mfma_f32_16x16x32_bf16 v[20:23], a[132:135], v[40:43], v[20:23]\n" \
  "v_mfma_f32_16x16x32_bf16 v[16:19], a[136:139], v[40:43], v[16:19]\n" \
  "v_mfma_f32_16x16x32_bf16 v[20:23], a[140:143], v[40:43], v[20:23]\n" \
  "v_mfma_f32_16x16x32_bf16 v[16:19], a[144:147], v[40:43], v[16:19]\n" \
  "v_mfma_f32_16x16x32_bf16 v[20:23], a[148:151], v[40:43], v[20:23]\n" \
  "v_mfma_f32_16x16x32_bf16 v[16:19], a[152:155], v[40:43], v[16:19]\n" \
  "v_mfma_f32_16x16x32_bf16 v[20:23], a[156:159], v[40:43], v[20:23]\n" \
  "v_mfma_f32_16x16x32_bf16 v[16:19], a[160:163], v[40:43], v[16:19]\n" \
  "v_mfma_f32_16x16x32_bf16 v[20:23], a[164:167], v[40:43], v[20:23]\n" \
  "v_mfma_f32_16x16x32_bf16 v[16:19], a[168:171], v[40:43], v[16:19]\n" \
  "v_mfma_f32_16x16x32_bf16 v[20:23], a[172:175], v[40:43], v[20:23]\n" \
  "v_mfma_f32_16x16x32_bf16 v[16:19], a[176:179], v[40:43], v[16:19]\n" \
  "v_mfma_f32_16x16x32_bf16 v[20:23], a[180:183], v[40:43], v[20:23]\n" \
  "v_mfma_f32_16x16x32_bf16 v[16:19], a[184:187], v[40:43], v[16:19]\n" \
  "v_mfma_f32_16x16x32_bf16 v[20:23], a[188:191], v[40:43], v[20:23]\n" \
  "v_mfma_f32_16x16x32_bf16 v[16:19], a[192:195], v[40:43], v[16:19]\n" \
  "v_mfma_f32_16x16x32_bf16 v[20:23], a[196:199], v[40:43], v[20:23]\n" \
  "v_mfma_f32_16x16x32_bf16 v[16:19], a[200:203], v[40:43], v[16:19]\n" \
  "v_mfma_f32_16x16x32_bf16 v[20:23], a[204:207], v[40:43], v[20:23]\n" \
  "v_mfma_f32_16x16x32_bf16 v[16:19], a[208:211], v[40:43], v[16:19]\n" \
  "v_mfma_f32_16x16x32_bf16 v[20:23], a[212:215], v[40:43], v[20:23]\n" \
  "v_mfma_f32_16x16x32_bf16 v[16:19], a[216:219], v[40:43], v[16:19]\n" \
  "v_mfma_f32_16x16x32_bf16 v[20:23], a[220:223], v[40:43], v[20:23]\n" \
  "v_mfma_f32_16x16x32_bf16 v[16:19], a[224:227], v[40:43], v[16:19]\n" \
  "v_mfma_f32_16x16x32_bf16 v[20:23], a[228:231], v[40:43], v[20:23]\n" \
  "v_mfma_f32_16x16x32_bf16 v[16:19], a[232:235], v[40:43], v[16:19]\n" \
  "v_mfma_f32_16x16x32_bf16 v[20:23], a[236:239], v[40:43], v[20:23]\n" \
  "v_mfma_f32_16x16x32_bf16 v[16:19], a[240:243], v[40:43], v[16:19]\n" \
  "v_mfma_f32_16x16x32_bf16 v[20:23], a[244:247], v[40:43], v[20:23]\n" \
  "v_mfma_f32_16x16x32_bf16 v[16:19], a[248:251], v[40:43], v[16:19]\n" \
  "v_mfma_f32_16x16x32_bf16 v[20:23], a[252:255], v[40:43], v[20:23]\n" \
  "v_mov_b32 v5, s60\n" \
  "s_mov_b32 s59, 536870912\n" \
  "s_add_u32 s57, s56, 131072\n" \
  "s_mov_b32 s79, 0\n" \
  "s_mov_b32 s75, 0x3a800000\n" \
  "s_mov_b32 s76, 0xc0135761\n" \
  "s_mov_b32 s77, 0x3d372713\n" \
  "s_mov_b32 s44, s59\n" \
  "s_mov_b32 s45, s59\n" \
  "s_mov_b32 s46, s59\n" \
  "s_mov_b32 s47, s59\n" \
  "s_mov_b32 s48, s83\n" \
  "s_add_u32 s49, s48, s55\n" \
  "s_cmp_eq_u32 s49, s56\n" \
  "s_cselect_b32 s49, s57, s49\n" \
  "s_cmp_eq_u32 s49, s58\n" \
  "s_cselect_b32 s49, s59, s49\n" \
  "s_add_u32 s50, s49, s55\n" \
  "s_cmp_eq_u32 s50, s56\n" \
  "s_cselect_b32 s50, s57, s50\n" \
  "s_cmp_eq_u32 s50, s58\n" \
  "s_cselect_b32 s50, s59, s50\n" \
  "s_add_u32 s51, s50, s55\n" \
  "s_cmp_eq_u32 s51, s56\n" \
  "s_cselect_b32 s51, s57, s51\n" \
  "s_cmp_eq_u32 s51, s58\n" \
  "s_cselect_b32 s51, s59, s51\n" \
  "s_add_u32 s52, s51, s55\n" \
  "s_cmp_eq_u32 s52, s56\n" \
  "s_cselect_b32 s52, s57, s52\n" \
  "s_cmp_eq_u32 s52, s58\n" \
  "s_cselect_b32 s52, s59, s52\n" \
  "s_lshl_b32 s63, s54, 12\n" \
  "s_add_u32 s63, s63, 65536\n" \
  "s_add_u32 s64, s63, 32768\n" \
  "v_add_u32 v1, 16384, v0\n" \
  "s_lshl_b32 s88, s54, 10\n" \
  "s_add_u32 s88, s88, 131072\n" \
  "s_lshl_b32 s78, s54, 11\n" \
  "s_add_u32 s78, s78, 132608\n" \
  "s_mov_b32 s66, s59\n" \
  "s_branch Lg1_slow0_%=\n" \
  "Lg1_d0_%=:\n" \
  "v_mfma_f32_16x16x32_bf16 v[24:27], a[0:3], v[40:43], 0\n" \
  "s_lshr_b32 s73, s47, 0\n" \
  "ds_read_b128 v[68:71], v2 offset:7168\n" \
  "s_cmp_eq_u32 s54, 1\n" \
  "v_mfma_f32_16x16x32_bf16 v[28:31], a[4:7], v[40:43], 0\n" \
  "s_cselect_b32 s68, s51, s50\n" \
  "s_lshr_b32 s68, s68, 5\n" \
  "s_lshr_b32 s70, s45, 5\n" \
  "v_mfma_f32_16x16x32_bf16 v[24:27], a[8:11], v[44:47], v[24:27]\n" \
  "s_add_u32 m0, s78, 0\n" \
  "ds_read_b128 v[40:43], v2 offset:8192\n" \
  "s_xor_b32 s69, s64, 65536\n" \
  "v_mfma_f32_16x16x32_bf16 v[28:31], a[12:15], v[44:47], v[28:31]\n" \
  "s_cmp_ge_u32 s66, s59\n" \
  "v_fma_f32 v32, -v92, v16, v32\n" \
  "v_fma_f32 v33, -v92, v17, v33\n" \
  "v_fma_f32 v34, -v92, v18, v34\n" \
  "v_mfma_f32_16x16x32_bf16 v[24:27], a[16:19], v[48:51], v[24:27]\n" \
  "s_cselect_b32 s62, s69, s63\n" \
  "ds_read_b128 v[44:47], v2 offset:9216\n" \
  "v_fma_f32 v35, -v92, v19, v35\n" \
  "v_mfma_f32_16x16x32_bf16 v[28:31], a[20:23], v[48:51], v[28:31]\n" \
  "s_cselect_b32 s86, s49, s50\n" \
  "v_mul_f32 v72, v32, v32\n" \
  "v_mul_f32 v73, v33, v33\n" \
  "v_mfma_f32_16x16x32_bf16 v[24:27], a[24:27], v[52:55], v[24:27]\n" \
  "ds_read_b128 v[48:51], v2 offset:10240\n" \
  "v_mul_f32 v74, v34, v34\n" \
  "v_mfma_f32_16x16x32_bf16 v[28:31], a[28:31], v[52:55], v[28:31]\n" \
  "s_waitcnt lgkmcnt(3)\n" \
  "v_mul_f32 v75, v35, v35\n" \
  "v_fma_f32 v72, v94, v72, v93\n" \
  "v_mul_f32 v72, v32, v72\n" \
  "v_mfma_f32_16x16x32_bf16 v[24:27], a[32:35], v[56:59], v[24:27]\n" \
  "ds_read_b128 v[52:55], v2 offset:11264\n" \
  "v_fma_f32 v73, v94, v73, v93\n" \
  "v_mfma_f32_16x16x32_bf16 v[28:31], a[36:39], v[56:59], v[28:31]\n" \
  "v_exp_f32 v72, v72\n" \
  "v_mfma_f32_16x16x32_bf16 v[24:27], a[40:43], v[60:63], v[24:27]\n" \
  "ds_read_b128 v[56:59], v2 offset:12288\n" \
  "v_mul_f32 v73, v33, v73\n" \
  "v_mfma_f32_16x16x32_bf16 v[28:31], a[44:47], v[60:63], v[28:31]\n" \
  "v_exp_f32 v73, v73\n" \
  "v_fma_f32 v72, v72, v95, v95\n" \
  "v_mfma_f32_16x16x32_bf16 v[24:27], a[48:51], v[64:67], v[24:27]\n" \
  "ds_read_b128 v[60:63], v2 offset:13312\n" \
  "v_fma_f32 v73, v73, v95, v95\n" \
  "v_mfma_f32_16x16x32_bf16 v[28:31], a[52:55], v[64:67], v[28:31]\n" \
  "v_rcp_f32 v72, v72\n" \
  "v_mfma_f32_16x16x32_bf16 v[24:27], a[56:59], v[68:71], v[24:27]\n" \
  "ds_read_b128 v[64:67], v2 offset:14336\n" \
  "v_fma_f32 v74, v94, v74, v93\n" \
  "v_mfma_f32_16x16x32_bf16 v[28:31], a[60:63], v[68:71], v[28:31]\n" \
  "s_waitcnt lgkmcnt(3)\n" \
  "v_rcp_f32 v73, v73\n" \
  "v_mul_f32 v74, v34, v74\n" \
  "v_mfma_f32_16x16x32_bf16 v[24:27], a[64:67], v[40:43], v[24:27]\n" \
  "s_waitcnt vmcnt(10)\n" \
  "ds_read_b128 v[68:71], v2 offset:15360\n" \
  "v_fma_f32 v75, v94, v75, v93\n" \
  "v_mfma_f32_16x16x32_bf16 v[28:31], a[68:71], v[40:43], v[28:31]\n" \
  "s_barrier\n" \
  "v_mfma_f32_16x16x32_bf16 v[24:27], a[72:75], v[44:47], v[24:27]\n" \
  "ds_read_b128 v[40:43], v2 offset:16384\n" \
  "ds_read_b32 v15, v7 offset:256\n" \
  "v_mfma_f32_16x16x32_bf16 v[28:31], a[76:79], v[44:47], v[28:31]\n" \
  "buffer_store_dword v5, v4, s[36:39], s70 offen offset:0 sc0 sc1\n" \
  "v_exp_f32 v74, v74\n" \
  "v_mul_f32 v75, v35, v75\n" \
  "v_mfma_f32_16x16x32_bf16 v[24:27], a[80:83], v[48:51], v[24:27]\n" \
  "ds_read_b128 v[44:47], v2 offset:17408\n" \
  "v_fma_f32 v74, v74, v95, v95\n" \
  "v_mfma_f32_16x16x32_bf16 v[28:31], a[84:87], v[48:51], v[28:31]\n" \
  "buffer_load_dword v6, s[36:39], s68 offen offset:0 sc1 lds\n" \
  "v_mfma_f32_16x16x32_bf16 v[24:27], a[88:91], v[52:55], v[24:27]\n" \
  "s_waitcnt lgkmcnt(1)\n" \
  "ds_read_b128 v[48:51], v2 offset:18432\n" \
  "v_mul_f32 v32, v32, v72\n" \
  "v_mfma_f32_16x16x32_bf16 v[28:31], a[92:95], v[52:55], v[28:31]\n" \
  "v_cmp_eq_u32 vcc, s60, v15\n" \
  "v_exp_f32 v75, v75\n" \
  "v_mul_f32 v33, v33, v73\n" \
  "v_mfma_f32_16x16x32_bf16 v[24:27], a[96:99], v[56:59], v[24:27]\n" \
  "s_mov_b32 m0, s62\n" \
  "ds_read_b128 v[52:55], v2 offset:19456\n" \
  "v_fma_f32 v75, v75, v95, v95\n" \
  "v_mfma_f32_16x16x32_bf16 v[28:31], a[100:103], v[56:59], v[28:31]\n" \
  "s_cselect_b32 s87, vcc_lo, vcc_hi\n" \
  "v_rcp_f32 v74, v74\n" \
  "v_mfma_f32_16x16x32_bf16 v[24:27], a[104:107], v[60:63], v[24:27]\n" \
  "s_cmp_eq_u32 s87, -1\n" \
  "ds_read_b128 v[56:59], v2 offset:20480\n" \
  "v_rcp_f32 v75, v75\n" \
  "v_mfma_f32_16x16x32_bf16 v[28:31], a[108:111], v[60:63], v[28:31]\n" \
  "s_cselect_b32 s67, s86, s59\n" \
  "v_mul_f32 v34, v34, v74\n" \
  "v_mul_f32 v35, v35, v75\n" \
  "v_mfma_f32_16x16x32_bf16 v[24:27], a[112:115], v[64:67], v[24:27]\n" \
  "s_xor_b32 s63, s63, 65536\n" \
  "ds_read_b128 v[60:63], v2 offset:21504\n" \
  "ds_read_b128 v[88:91], v8 offset:0\n" \
  "v_mfma_f32_16x16x32_bf16 v[28:31], a[116:119], v[64:67], v[28:31]\n" \
  "buffer_load_dwordx4 v0, s[36:39], s67 offen offset:0 sc1 lds\n" \
  "v_mfma_f32_16x16x32_bf16 v[24:27], a[120:123], v[68:71], v[24:27]\n" \
  "s_add_u32 s53, s52, s55\n" \
  "ds_read_b128 v[64:67], v2 offset:22528\n" \
  "v_cvt_pk_bf16_f32 v80, v32, v33\n" \
  "v_cvt_pk_bf16_f32 v81, v34, v35\n" \
  "v_mfma_f32_16x16x32_bf16 v[28:31], a[124:127], v[68:71], v[28:31]\n" \
  "s_waitcnt lgkmcnt(4)\n" \
  "v_fma_f32 v36, -v92, v20, v36\n" \
  "v_fma_f32 v37, -v92, v21, v37\n" \
  "v_mfma_f32_16x16x32_bf16 v[24:27], a[128:131], v[40:43], v[24:27]\n" \
  "s_cmp_eq_u32 s53, s56\n" \
  "ds_read_b128 v[68:71], v2 offset:23552\n" \
  "v_fma_f32 v38, -v92, v22, v38\n" \
  "v_mfma_f32_16x16x32_bf16 v[28:31], a[132:135], v[40:43], v[28:31]\n" \
  "buffer_load_dwordx4 v0, s[36:39], s67 offen offset:1024 sc1 lds\n" \
  "v_mfma_f32_16x16x32_bf16 v[24:27], a[136:139], v[44:47], v[24:27]\n" \
  "s_waitcnt lgkmcnt(2)\n" \
  "ds_read_b128 v[40:43], v2 offset:24576\n" \
  "v_fma_f32 v39, -v92, v23, v39\n" \
  "v_mul_f32 v76, v36, v36\n" \
  "v_mfma_f32_16x16x32_bf16 v[28:31], a[140:143], v[44:47], v[28:31]\n" \
  "s_cselect_b32 s53, s57, s53\n" \
  "v_mul_f32 v77, v37, v37\n" \
  "v_mul_f32 v78, v38, v38\n" \
  "v_mfma_f32_16x16x32_bf16 v[24:27], a[144:147], v[48:51], v[24:27]\n" \
  "s_cmp_eq_u32 s53, s58\n" \
  "ds_read_b128 v[44:47], v2 offset:25600\n" \
  "v_mul_f32 v79, v39, v39\n" \
  "v_mfma_f32_16x16x32_bf16 v[28:31], a[148:151], v[48:51], v[28:31]\n" \
  "buffer_load_dwordx4 v0, s[36:39], s67 offen offset:2048 sc1 lds\n" \
  "v_mfma_f32_16x16x32_bf16 v[24:27], a[152:155], v[52:55], v[24:27]\n" \
  "s_cselect_b32 s53, s59, s53\n" \
  "ds_read_b128 v[48:51], v2 offset:26624\n" \
  "v_fma_f32 v76, v94, v76, v93\n" \
  "v_mul_f32 v76, v36, v76\n" \
  "v_mfma_f32_16x16x32_bf16 v[28:31], a[156:159], v[52:55], v[28:31]\n" \
  "s_waitcnt lgkmcnt(3)\n" \
  "v_exp_f32 v76, v76\n" \
  "v_mfma_f32_16x16x32_bf16 v[24:27], a[160:163], v[56:59], v[24:27]\n" \
  "ds_read_b128 v[52:55], v2 offset:27648\n" \
  "v_fma_f32 v76, v76, v95, v95\n" \
  "v_mfma_f32_16x16x32_bf16 v[28:31], a[164:167], v[56:59], v[28:31]\n" \
  "buffer_load_dwordx4 v0, s[36:39], s67 offen offset:3072 sc1 lds\n" \
  "v_mfma_f32_16x16x32_bf16 v[24:27], a[168:171], v[60:63], v[24:27]\n" \
  "s_add_u32 m0, s62, 16384\n" \
  "ds_read_b128 v[56:59], v2 offset:28672\n" \
  "v_rcp_f32 v76, v76\n" \
  "v_mfma_f32_16x16x32_bf16 v[28:31], a[172:175], v[60:63], v[28:31]\n" \
  "s_lshr_b32 s72, s67, 6\n" \
  "v_fma_f32 v77, v94, v77, v93\n" \
  "v_mul_f32 v77, v37, v77\n" \
  "v_mfma_f32_16x16x32_bf16 v[24:27], a[176:179], v[64:67], v[24:27]\n" \
  "s_lshr_b32 s71, s62, 2\n" \
  "ds_read_b128 v[60:63], v2 offset:29696\n" \
  "v_fma_f32 v78, v94, v78, v93\n" \
  "v_mfma_f32_16x16x32_bf16 v[28:31], a[180:183], v[64:67], v[28:31]\n" \
  "buffer_load_dwordx4 v1, s[36:39], s67 offen offset:0 sc1 lds\n" \
  "v_mfma_f32_16x16x32_bf16 v[24:27], a[184:187], v[68:71], v[24:27]\n" \
  "ds_read_b128 v[64:67], v2 offset:30720\n" \
  "v_exp_f32 v77, v77\n" \
  "v_mfma_f32_16x16x32_bf16 v[28:31], a[188:191], v[68:71], v[28:31]\n" \
  "s_waitcnt lgkmcnt(3)\n" \
  "v_mul_f32 v78, v38, v78\n" \
  "v_fma_f32 v77, v77, v95, v95\n" \
  "v_mfma_f32_16x16x32_bf16 v[24:27], a[192:195], v[40:43], v[24:27]\n" \
  "s_waitcnt vmcnt(13)\n" \
  "ds_read_b128 v[68:71], v2 offset:31744\n" \
  "v_fma_f32 v79, v94, v79, v93\n" \
  "v_mfma_f32_16x16x32_bf16 v[28:31], a[196:199], v[40:43], v[28:31]\n" \
  "s_barrier\n" \
  "v_mfma_f32_16x16x32_bf16 v[24:27], a[200:203], v[44:47], v[24:27]\n" \
  "ds_read_b128 v[40:43], v2 offset:32768\n" \
  "buffer_load_dwordx4 v1, s[36:39], s67 offen offset:1024 sc1 lds\n" \
  "v_mfma_f32_16x16x32_bf16 v[28:31], a[204:207], v[44:47], v[28:31]\n" \
  "v_exp_f32 v78, v78\n" \
  "v_mfma_f32_16x16x32_bf16 v[24:27], a[208:211], v[48:51], v[24:27]\n" \
  "ds_read_b128 v[44:47], v2 offset:33792\n" \
  "v_mul_f32 v79, v39, v79\n" \
  "v_mfma_f32_16x16x32_bf16 v[28:31], a[212:215], v[48:51], v[28:31]\n" \
  "v_exp_f32 v79, v79\n" \
  "v_mfma_f32_16x16x32_bf16 v[24:27], a[216:219], v[52:55], v[24:27]\n" \
  "ds_read_b128 v[48:51], v2 offset:34816\n" \
  "buffer_load_dwordx4 v1, s[36:39], s67 offen offset:2048 sc1 lds\n" \
  "v_mfma_f32_16x16x32_bf16 v[28:31], a[220:223], v[52:55], v[28:31]\n" \
  "s_waitcnt lgkmcnt(3)\n" \
  "v_rcp_f32 v77, v77\n" \
  "v_mfma_f32_16x16x32_bf16 v[24:27], a[224:227], v[56:59], v[24:27]\n" \
  "ds_read_b128 v[52:55], v2 offset:35840\n" \
  "v_fma_f32 v78, v78, v95, v95\n" \
  "v_mfma_f32_16x16x32_bf16 v[28:31], a[228:231], v[56:59], v[28:31]\n" \
  "v_rcp_f32 v78, v78\n" \
  "v_mfma_f32_16x16x32_bf16 v[24:27], a[232:235], v[60:63], v[24:27]\n" \
  "ds_read_b128 v[56:59], v2 offset:36864\n" \
  "buffer_load_dwordx4 v1, s[36:39], s67 offen offset:3072 sc1 lds\n" \
  "v_mfma_f32_16x16x32_bf16 v[28:31], a[236:239], v[60:63], v[28:31]\n" \
  "s_add_u32 m0, s71, s88\n" \
  "v_fma_f32 v79, v79, v95, v95\n" \
  "v_rcp_f32 v79, v79\n" \
  "v_mfma_f32_16x16x32_bf16 v[24:27], a[240:243], v[64:67], v[24:27]\n" \
  "s_cmp_ge_u32 s66, s59\n" \
  "ds_read_b128 v[60:63], v2 offset:37888\n" \
  "v_mul_f32 v36, v36, v76\n" \
  "v_mul_f32 v37, v37, v77\n" \
  "v_mul_f32 v38, v38, v78\n" \
  "v_mul_f32 v39, v39, v79\n" \
  "v_cvt_pk_bf16_f32 v82, v36, v37\n" \
  "v_cvt_pk_bf16_f32 v83, v38, v39\n" \
  "v_mfma_f32_16x16x32_bf16 v[28:31], a[244:247], v[64:67], v[28:31]\n" \
  "buffer_load_dword v10, s[36:39], s72 offen offset:0 sc1 lds\n" \
  "buffer_store_dwordx4 v[80:83], v3, s[36:39], s73 offen offset:0 sc0 sc1\n" \
  "v_mfma_f32_16x16x32_bf16 v[24:27], a[248:251], v[68:71], v[24:27]\n" \
  "s_waitcnt lgkmcnt(2)\n" \
  "ds_read_b128 v[64:67], v2 offset:38912\n" \
  "v_mfma_f32_16x16x32_bf16 v[28:31], a[252:255], v[68:71], v[28:31]\n" \
  "s_cbranch_scc1 Lg1_slow1_%=\n" \
  "Lg1_d1_%=:\n" \
  "v_mfma_f32_16x16x32_bf16 v[32:35], a[0:3], v[40:43], 0\n" \
  "s_lshr_b32 s73, s48, 0\n" \
  "ds_read_b128 v[68:71], v2 offset:39936\n" \
  "s_cmp_eq_u32 s54, 1\n" \
  "v_mfma_f32_16x16x32_bf16 v[36:39], a[4:7], v[40:43], 0\n" \
  "s_cselect_b32 s68, s52, s51\n" \
  "s_lshr_b32 s68, s68, 5\n" \
  "s_lshr_b32 s70, s46, 5\n" \
  "v_mfma_f32_16x16x32_bf16 v[32:35], a[8:11], v[44:47], v[32:35]\n" \
  "s_add_u32 m0, s78, 256\n" \
  "ds_read_b128 v[40:43], v2 offset:40960\n" \
  "s_xor_b32 s69, s63, 65536\n" \
  "v_mfma_f32_16x16x32_bf16 v[36:39], a[12:15], v[44:47], v[36:39]\n" \
  "s_cmp_ge_u32 s67, s59\n" \
  "v_fma_f32 v24, -v88, v16, v24\n" \
  "v_fma_f32 v25, -v88, v17, v25\n" \
  "v_fma_f32 v26, -v88, v18, v26\n" \
  "v_mfma_f32_16x16x32_bf16 v[32:35], a[16:19], v[48:51], v[32:35]\n" \
  "s_cselect_b32 s62, s69, s64\n" \
  "ds_read_b128 v[44:47], v2 offset:41984\n" \
  "v_fma_f32 v27, -v88, v19, v27\n" \
  "v_mfma_f32_16x16x32_bf16 v[36:39], a[20:23], v[48:51], v[36:39]\n" \
  "s_cselect_b32 s86, s50, s51\n" \
  "v_mul_f32 v72, v24, v24\n" \
  "v_mul_f32 v73, v25, v25\n" \
  "v_mfma_f32_16x16x32_bf16 v[32:35], a[24:27], v[52:55], v[32:35]\n" \
  "s_mov_b64 s[44:45], s[46:47]\n" \
  "ds_read_b128 v[48:51], v2 offset:43008\n" \
  "v_mul_f32 v74, v26, v26\n" \
  "v_mul_f32 v75, v27, v27\n" \
  "v_mfma_f32_16x16x32_bf16 v[36:39], a[28:31], v[52:55], v[36:39]\n" \
  "s_waitcnt lgkmcnt(3)\n" \
  "v_fma_f32 v72, v90, v72, v89\n" \
  "v_mul_f32 v72, v24, v72\n" \
  "v_mfma_f32_16x16x32_bf16 v[32:35], a[32:35], v[56:59], v[32:35]\n" \
  "s_mov_b64 s[46:47], s[48:49]\n" \
  "ds_read_b128 v[52:55], v2 offset:44032\n" \
  "v_fma_f32 v73, v90, v73, v89\n" \
  "v_mfma_f32_16x16x32_bf16 v[36:39], a[36:39], v[56:59], v[36:39]\n" \
  "s_mov_b64 s[48:49], s[50:51]\n" \
  "v_exp_f32 v72, v72\n" \
  "v_mfma_f32_16x16x32_bf16 v[32:35], a[40:43], v[60:63], v[32:35]\n" \
  "s_mov_b64 s[50:51], s[52:53]\n" \
  "ds_read_b128 v[56:59], v2 offset:45056\n" \
  "v_mul_f32 v73, v25, v73\n" \
  "v_fma_f32 v72, v72, v91, v91\n" \
  "v_mfma_f32_16x16x32_bf16 v[36:39], a[44:47], v[60:63], v[36:39]\n" \
  "v_exp_f32 v73, v73\n" \
  "v_mfma_f32_16x16x32_bf16 v[32:35], a[48:51], v[64:67], v[32:35]\n" \
  "ds_read_b128 v[60:63], v2 offset:46080\n" \
  "v_fma_f32 v73, v73, v91, v91\n" \
  "v_mfma_f32_16x16x32_bf16 v[36:39], a[52:55], v[64:67], v[36:39]\n" \
  "v_rcp_f32 v72, v72\n" \
  "v_mfma_f32_16x16x32_bf16 v[32:35], a[56:59], v[68:71], v[32:35]\n" \
  "ds_read_b128 v[64:67], v2 offset:47104\n" \
  "v_rcp_f32 v73, v73\n" \
  "v_mfma_f32_16x16x32_bf16 v[36:39], a[60:63], v[68:71], v[36:39]\n" \
  "s_waitcnt lgkmcnt(3)\n" \
  "v_fma_f32 v74, v90, v74, v89\n" \
  "v_mul_f32 v74, v26, v74\n" \
  "v_mfma_f32_16x16x32_bf16 v[32:35], a[64:67], v[40:43], v[32:35]\n" \
  "s_waitcnt vmcnt(10)\n" \
  "ds_read_b128 v[68:71], v2 offset:48128\n" \
  "v_fma_f32 v75, v90, v75, v89\n" \
  "v_mfma_f32_16x16x32_bf16 v[36:39], a[68:71], v[40:43], v[36:39]\n" \
  "s_barrier\n" \
  "v_mfma_f32_16x16x32_bf16 v[32:35], a[72:75], v[44:47], v[32:35]\n" \
  "ds_read_b128 v[40:43], v2 offset:49152\n" \
  "ds_read_b32 v15, v7 offset:0\n" \
  "v_mul_f32 v75, v27, v75\n" \
  "v_mfma_f32_16x16x32_bf16 v[36:39], a[76:79], v[44:47], v[36:39]\n" \
  "buffer_store_dword v5, v4, s[36:39], s70 offen offset:0 sc0 sc1\n" \
  "v_exp_f32 v74, v74\n" \
  "v_mul_f32 v24, v24, v72\n" \
  "v_mfma_f32_16x16x32_bf16 v[32:35], a[80:83], v[48:51], v[32:35]\n" \
  "ds_read_b128 v[44:47], v2 offset:50176\n" \
  "v_fma_f32 v74, v74, v91, v91\n" \
  "v_mfma_f32_16x16x32_bf16 v[36:39], a[84:87], v[48:51], v[36:39]\n" \
  "buffer_load_dword v6, s[36:39], s68 offen offset:0 sc1 lds\n" \
  "v_mul_f32 v25, v25, v73\n" \
  "v_mfma_f32_16x16x32_bf16 v[32:35], a[88:91], v[52:55], v[32:35]\n" \
  "s_waitcnt lgkmcnt(1)\n" \
  "ds_read_b128 v[48:51], v2 offset:51200\n" \
  "v_cvt_pk_bf16_f32 v80, v24, v25\n" \
  "v_mfma_f32_16x16x32_bf16 v[36:39], a[92:95], v[52:55], v[36:39]\n" \
  "v_cmp_eq_u32 vcc, s60, v15\n" \
  "v_exp_f32 v75, v75\n" \
  "v_fma_f32 v28, -v88, v20, v28\n" \
  "v_mfma_f32_16x16x32_bf16 v[32:35], a[96:99], v[56:59], v[32:35]\n" \
  "s_mov_b32 m0, s62\n" \
  "ds_read_b128 v[52:55], v2 offset:52224\n" \
  "v_fma_f32 v75, v75, v91, v91\n" \
  "v_mfma_f32_16x16x32_bf16 v[36:39], a[100:103], v[56:59], v[36:39]\n" \
  "s_cselect_b32 s87, vcc_lo, vcc_hi\n" \
  "v_rcp_f32 v74, v74\n" \
  "v_fma_f32 v29, -v88, v21, v29\n" \
  "v_mfma_f32_16x16x32_bf16 v[32:35], a[104:107], v[60:63], v[32:35]\n" \
  "s_cmp_eq_u32 s87, -1\n" \
  "ds_read_b128 v[56:59], v2 offset:53248\n" \
  "v_mul_f32 v26, v26, v74\n" \
  "v_mfma_f32_16x16x32_bf16 v[36:39], a[108:111], v[60:63], v[36:39]\n" \
  "s_cselect_b32 s66, s86, s59\n" \
  "v_rcp_f32 v75, v75\n" \
  "v_mfma_f32_16x16x32_bf16 v[32:35], a[112:115], v[64:67], v[32:35]\n" \
  "s_xor_b32 s64, s64, 65536\n" \
  "ds_read_b128 v[60:63], v2 offset:54272\n" \
  "ds_read_b128 v[92:95], v8 offset:8192\n" \
  "v_mfma_f32_16x16x32_bf16 v[36:39], a[116:119], v[64:67], v[36:39]\n" \
  "buffer_load_dwordx4 v0, s[36:39], s66 offen offset:0 sc1 lds\n" \
  "v_mul_f32 v27, v27, v75\n" \
  "v_mfma_f32_16x16x32_bf16 v[32:35], a[120:123], v[68:71], v[32:35]\n" \
  "s_add_u32 s74, s53, s55\n" \
  "ds_read_b128 v[64:67], v2 offset:55296\n" \
  "v_cvt_pk_bf16_f32 v81, v26, v27\n" \
  "v_mfma_f32_16x16x32_bf16 v[36:39], a[124:127], v[68:71], v[36:39]\n" \
  "s_waitcnt lgkmcnt(4)\n" \
  "v_fma_f32 v30, -v88, v22, v30\n" \
  "v_fma_f32 v31, -v88, v23, v31\n" \
  "v_mfma_f32_16x16x32_bf16 v[32:35], a[128:131], v[40:43], v[32:35]\n" \
  "s_cmp_eq_u32 s74, s56\n" \
  "ds_read_b128 v[68:71], v2 offset:56320\n" \
  "v_mul_f32 v76, v28, v28\n" \
  "v_mul_f32 v77, v29, v29\n" \
  "v_mfma_f32_16x16x32_bf16 v[36:39], a[132:135], v[40:43], v[36:39]\n" \
  "buffer_load_dwordx4 v0, s[36:39], s66 offen offset:1024 sc1 lds\n" \
  "v_mfma_f32_16x16x32_bf16 v[32:35], a[136:139], v[44:47], v[32:35]\n" \
  "s_waitcnt lgkmcnt(2)\n" \
  "ds_read_b128 v[40:43], v2 offset:57344\n" \
  "v_mul_f32 v78, v30, v30\n" \
  "v_mfma_f32_16x16x32_bf16 v[36:39], a[140:143], v[44:47], v[36:39]\n" \
  "s_cselect_b32 s74, s57, s74\n" \
  "v_mul_f32 v79, v31, v31\n" \
  "v_fma_f32 v76, v90, v76, v89\n" \
  "v_mfma_f32_16x16x32_bf16 v[32:35], a[144:147], v[48:51], v[32:35]\n" \
  "s_cmp_eq_u32 s74, s58\n" \
  "ds_read_b128 v[44:47], v2 offset:58368\n" \
  "v_mul_f32 v76, v28, v76\n" \
  "v_fma_f32 v77, v90, v77, v89\n" \
  "v_mfma_f32_16x16x32_bf16 v[36:39], a[148:151], v[48:51], v[36:39]\n" \
  "buffer_load_dwordx4 v0, s[36:39], s66 offen offset:2048 sc1 lds\n" \
  "v_mfma_f32_16x16x32_bf16 v[32:35], a[152:155], v[52:55], v[32:35]\n" \
  "s_cselect_b32 s74, s59, s74\n" \
  "ds_read_b128 v[48:51], v2 offset:59392\n" \
  "v_mul_f32 v77, v29, v77\n" \
  "v_mfma_f32_16x16x32_bf16 v[36:39], a[156:159], v[52:55], v[36:39]\n" \
  "s_waitcnt lgkmcnt(3)\n" \
  "v_exp_f32 v76, v76\n" \
  "v_mfma_f32_16x16x32_bf16 v[32:35], a[160:163], v[56:59], v[32:35]\n" \
  "s_mov_b32 s52, s74\n" \
  "ds_read_b128 v[52:55], v2 offset:60416\n" \
  "v_exp_f32 v77, v77\n" \
  "v_mfma_f32_16x16x32_bf16 v[36:39], a[164:167], v[56:59], v[36:39]\n" \
  "buffer_load_dwordx4 v0, s[36:39], s66 offen offset:3072 sc1 lds\n" \
  "v_mfma_f32_16x16x32_bf16 v[32:35], a[168:171], v[60:63], v[32:35]\n" \
  "s_add_u32 m0, s62, 16384\n" \
  "ds_read_b128 v[56:59], v2 offset:61440\n" \
  "v_fma_f32 v76, v76, v91, v91\n" \
  "v_mfma_f32_16x16x32_bf16 v[36:39], a[172:175], v[60:63], v[36:39]\n" \
  "s_lshr_b32 s72, s66, 6\n" \
  "v_rcp_f32 v76, v76\n" \
  "v_fma_f32 v77, v77, v91, v91\n" \
  "v_mfma_f32_16x16x32_bf16 v[32:35], a[176:179], v[64:67], v[32:35]\n" \
  "s_lshr_b32 s71, s62, 2\n" \
  "ds_read_b128 v[60:63], v2 offset:62464\n" \
  "v_fma_f32 v78, v90, v78, v89\n" \
  "v_mfma_f32_16x16x32_bf16 v[36:39], a[180:183], v[64:67], v[36:39]\n" \
  "buffer_load_dwordx4 v1, s[36:39], s66 offen offset:0 sc1 lds\n" \
  "v_mfma_f32_16x16x32_bf16 v[32:35], a[184:187], v[68:71], v[32:35]\n" \
  "ds_read_b128 v[64:67], v2 offset:63488\n" \
  "v_mul_f32 v78, v30, v78\n" \
  "v_mfma_f32_16x16x32_bf16 v[36:39], a[188:191], v[68:71], v[36:39]\n" \
  "s_waitcnt lgkmcnt(3)\n" \
  "v_exp_f32 v78, v78\n" \
  "v_fma_f32 v79, v90, v79, v89\n" \
  "v_mfma_f32_16x16x32_bf16 v[32:35], a[192:195], v[40:43], v[32:35]\n" \
  "s_waitcnt vmcnt(13)\n" \
  "ds_read_b128 v[68:71], v2 offset:64512\n" \
  "v_xor_b32 v2, 65536, v2\n" \
  "v_mfma_f32_16x16x32_bf16 v[36:39], a[196:199], v[40:43], v[36:39]\n" \
  "s_barrier\n" \
  "v_mfma_f32_16x16x32_bf16 v[32:35], a[200:203], v[44:47], v[32:35]\n" \
  "ds_read_b128 v[40:43], v2 offset:0\n" \
  "v_mul_f32 v79, v31, v79\n" \
  "v_mfma_f32_16x16x32_bf16 v[36:39], a[204:207], v[44:47], v[36:39]\n" \
  "buffer_load_dwordx4 v1, s[36:39], s66 offen offset:1024 sc1 lds\n" \
  "v_fma_f32 v78, v78, v91, v91\n" \
  "v_mfma_f32_16x16x32_bf16 v[32:35], a[208:211], v[48:51], v[32:35]\n" \
  "ds_read_b128 v[44:47], v2 offset:1024\n" \
  "v_mul_f32 v28, v28, v76\n" \
  "v_mfma_f32_16x16x32_bf16 v[36:39], a[212:215], v[48:51], v[36:39]\n" \
  "v_exp_f32 v79, v79\n" \
  "v_mfma_f32_16x16x32_bf16 v[32:35], a[216:219], v[52:55], v[32:35]\n" \
  "ds_read_b128 v[48:51], v2 offset:2048\n" \
  "v_rcp_f32 v77, v77\n" \
  "v_mfma_f32_16x16x32_bf16 v[36:39], a[220:223], v[52:55], v[36:39]\n" \
  "s_waitcnt lgkmcnt(3)\n" \
  "buffer_load_dwordx4 v1, s[36:39], s66 offen offset:2048 sc1 lds\n" \
  "v_mfma_f32_16x16x32_bf16 v[32:35], a[224:227], v[56:59], v[32:35]\n" \
  "ds_read_b128 v[52:55], v2 offset:3072\n" \
  "v_fma_f32 v79, v79, v91, v91\n" \
  "v_mfma_f32_16x16x32_bf16 v[36:39], a[228:231], v[56:59], v[36:39]\n" \
  "v_rcp_f32 v78, v78\n" \
  "v_mfma_f32_16x16x32_bf16 v[32:35], a[232:235], v[60:63], v[32:35]\n" \
  "ds_read_b128 v[56:59], v2 offset:4096\n" \
  "v_rcp_f32 v79, v79\n" \
  "v_mfma_f32_16x16x32_bf16 v[36:39], a[236:239], v[60:63], v[36:39]\n" \
  "buffer_load_dwordx4 v1, s[36:39], s66 offen offset:3072 sc1 lds\n" \
  "v_mfma_f32_16x16x32_bf16 v[32:35], a[240:243], v[64:67], v[32:35]\n" \
  "s_add_u32 m0, s71, s88\n" \
  "ds_read_b128 v[60:63], v2 offset:5120\n" \
  "v_mul_f32 v29, v29, v77\n" \
  "v_mul_f32 v30, v30, v78\n" \
  "v_mul_f32 v31, v31, v79\n" \
  "v_cvt_pk_bf16_f32 v82, v28, v29\n" \
  "v_cvt_pk_bf16_f32 v83, v30, v31\n" \
  "v_mfma_f32_16x16x32_bf16 v[36:39], a[244:247], v[64:67], v[36:39]\n" \
  "buffer_load_dword v10, s[36:39], s72 offen offset:0 sc1 lds\n" \
  "buffer_store_dwordx4 v[80:83], v3, s[36:39], s73 offen offset:0 sc0 sc1\n" \
  "v_xor_b32 v8, 16384, v8\n" \
  "v_mfma_f32_16x16x32_bf16 v[32:35], a[248:251], v[68:71], v[32:35]\n" \
  "s_waitcnt lgkmcnt(2)\n" \
  "ds_read_b128 v[64:67], v2 offset:6144\n" \
  "s_cmp_lt_u32 s67, s59\n" \
  "v_mfma_f32_16x16x32_bf16 v[36:39], a[252:255], v[68:71], v[36:39]\n" \
  "s_cbranch_scc1 Lg1_d0_%=\n" \
  "Lg1_slow0_%=:\n" \
  "s_cmp_eq_u32 s54, 1\n" \
  "s_cselect_b32 s68, s49, s48\n" \
  "s_lshr_b32 s68, s68, 5\n" \
  "s_add_u32 m0, s78, 8192\n" \
  "s_nop 0\n" \
  "buffer_load_dword v6, s[36:39], s68 offen offset:0 sc1 lds\n" \
  "s_lshr_b32 s73, s47, 0\n" \
  "v_fma_f32 v32, -v92, v16, v32\n" \
  "v_fma_f32 v33, -v92, v17, v33\n" \
  "v_fma_f32 v34, -v92, v18, v34\n" \
  "v_fma_f32 v35, -v92, v19, v35\n" \
  "v_mul_f32 v72, v32, v32\n" \
  "v_mul_f32 v73, v33, v33\n" \
  "v_mul_f32 v74, v34, v34\n" \
  "v_mul_f32 v75, v35, v35\n" \
  "v_fma_f32 v72, v94, v72, v93\n" \
  "v_fma_f32 v73, v94, v73, v93\n" \
  "v_fma_f32 v74, v94, v74, v93\n" \
  "v_fma_f32 v75, v94, v75, v93\n" \
  "v_mul_f32 v72, v32, v72\n" \
  "v_mul_f32 v73, v33, v73\n" \
  "v_mul_f32 v74, v34, v74\n" \
  "v_mul_f32 v75, v35, v75\n" \
  "v_exp_f32 v72, v72\n" \
  "v_exp_f32 v73, v73\n" \
  "v_exp_f32 v74, v74\n" \
  "v_exp_f32 v75, v75\n" \
  "v_fma_f32 v72, v72, v95, v95\n" \
  "v_fma_f32 v73, v73, v95, v95\n" \
  "v_fma_f32 v74, v74, v95, v95\n" \
  "v_fma_f32 v75, v75, v95, v95\n" \
  "v_rcp_f32 v72, v72\n" \
  "v_rcp_f32 v73, v73\n" \
  "v_rcp_f32 v74, v74\n" \
  "v_rcp_f32 v75, v75\n" \
  "v_mul_f32 v32, v32, v72\n" \
  "v_mul_f32 v33, v33, v73\n" \
  "v_mul_f32 v34, v34, v74\n" \
  "v_mul_f32 v35, v35, v75\n" \
  "v_cvt_pk_bf16_f32 v80, v32, v33\n" \
  "v_cvt_pk_bf16_f32 v81, v34, v35\n" \
  "s_add_u32 m0, s78, 8448\n" \
  "s_nop 0\n" \
  "buffer_load_dword v6, s[36:39], s68 offen offset:0 sc1 lds\n" \
  "v_fma_f32 v36, -v92, v20, v36\n" \
  "v_fma_f32 v37, -v92, v21, v37\n" \
  "v_fma_f32 v38, -v92, v22, v38\n" \
  "v_fma_f32 v39, -v92, v23, v39\n" \
  "v_mul_f32 v76, v36, v36\n" \
  "v_mul_f32 v77, v37, v37\n" \
  "v_mul_f32 v78, v38, v38\n" \
  "v_mul_f32 v79, v39, v39\n" \
  "v_fma_f32 v76, v94, v76, v93\n" \
  "v_fma_f32 v77, v94, v77, v93\n" \
  "v_fma_f32 v78, v94, v78, v93\n" \
  "v_fma_f32 v79, v94, v79, v93\n" \
  "v_mul_f32 v76, v36, v76\n" \
  "v_mul_f32 v77, v37, v77\n" \
  "v_mul_f32 v78, v38, v78\n" \
  "v_mul_f32 v79, v39, v79\n" \
  "v_exp_f32 v76, v76\n" \
  "v_exp_f32 v77, v77\n" \
  "v_exp_f32 v78, v78\n" \
  "v_exp_f32 v79, v79\n" \
  "v_fma_f32 v76, v76, v95, v95\n" \
  "v_fma_f32 v77, v77, v95, v95\n" \
  "v_fma_f32 v78, v78, v95, v95\n" \
  "v_fma_f32 v79, v79, v95, v95\n" \
  "v_rcp_f32 v76, v76\n" \
  "v_rcp_f32 v77, v77\n" \
  "v_rcp_f32 v78, v78\n" \
  "v_rcp_f32 v79, v79\n" \
  "v_mul_f32 v36, v36, v76\n" \
  "v_mul_f32 v37, v37, v77\n" \
  "v_mul_f32 v38, v38, v78\n" \
  "v_mul_f32 v39, v39, v79\n" \
  "v_cvt_pk_bf16_f32 v82, v36, v37\n" \
  "v_cvt_pk_bf16_f32 v83, v38, v39\n" \
  "buffer_store_dwordx4 v[80:83], v3, s[36:39], s73 offen offset:0 sc0 sc1\n" \
  "s_cmp_ge_u32 s48, s59\n" \
  "s_cbranch_scc1 Lg1_done0_%=\n" \
  "s_add_u32 m0, s78, 16384\n" \
  "s_nop 0\n" \
  "buffer_load_dword v6, s[36:39], s68 offen offset:0 sc1 lds\n" \
  "s_waitcnt vmcnt(3)\n" \
  "s_barrier\n" \
  "ds_read_b32 v15, v7 offset:8192\n" \
  "s_lshr_b32 s70, s45, 5\n" \
  "buffer_store_dword v5, v4, s[36:39], s70 offen offset:0 sc0 sc1\n" \
  "s_mov_b32 s45, s59\n" \
  "s_lshr_b32 s70, s46, 5\n" \
  "buffer_store_dword v5, v4, s[36:39], s70 offen offset:0 sc0 sc1\n" \
  "s_mov_b32 s46, s59\n" \
  "s_mov_b32 s69, 18432\n" \
  "s_waitcnt lgkmcnt(0)\n" \
  "v_cmp_eq_u32 vcc, s60, v15\n" \
  "s_nop 0\n" \
  "s_cmp_eq_u32 vcc_lo, -1\n" \
  "s_cbranch_scc1 Lg1_go0_%=\n" \
  "s_add_u32 m0, s78, 16640\n" \
  "s_nop 0\n" \
  "buffer_load_dword v6, s[36:39], s68 offen offset:0 sc1 lds\n" \
  "s_waitcnt vmcnt(5)\n" \
  "s_barrier\n" \
  "ds_read_b32 v15, v7 offset:8448\n" \
  "s_mov_b32 s69, 18688\n" \
  "s_waitcnt lgkmcnt(0)\n" \
  "v_cmp_eq_u32 vcc, s60, v15\n" \
  "s_nop 0\n" \
  "s_cmp_eq_u32 vcc_lo, -1\n" \
  "s_cbranch_scc1 Lg1_go0_%=\n" \
  "s_add_u32 m0, s78, 8192\n" \
  "s_nop 0\n" \
  "buffer_load_dword v6, s[36:39], s68 offen offset:0 sc1 lds\n" \
  "s_waitcnt vmcnt(4)\n" \
  "s_barrier\n" \
  "ds_read_b32 v15, v7 offset:16384\n" \
  "s_lshr_b32 s70, s47, 5\n" \
  "buffer_store_dword v5, v4, s[36:39], s70 offen offset:0 sc0 sc1\n" \
  "s_mov_b32 s47, s59\n" \
  "s_mov_b32 s69, 10240\n" \
  "s_waitcnt lgkmcnt(0)\n" \
  "v_cmp_eq_u32 vcc, s60, v15\n" \
  "s_nop 0\n" \
  "s_cmp_eq_u32 vcc_lo, -1\n" \
  "s_cbranch_scc1 Lg1_go0_%=\n" \
  "s_add_u32 m0, s78, 8448\n" \
  "s_nop 0\n" \
  "buffer_load_dword v6, s[36:39], s68 offen offset:0 sc1 lds\n" \
  "s_waitcnt vmcnt(3)\n" \
  "s_barrier\n" \
  "ds_read_b32 v15, v7 offset:16640\n" \
  "s_mov_b32 s69, 10496\n" \
  "s_waitcnt lgkmcnt(0)\n" \
  "v_cmp_eq_u32 vcc, s60, v15\n" \
  "s_nop 0\n" \
  "s_cmp_eq_u32 vcc_lo, -1\n" \
  "s_cbranch_scc1 Lg1_go0_%=\n" \
  "Lg1_poll0_%=:\n" \
  "s_add_u32 m0, s78, 16384\n" \
  "s_nop 0\n" \
  "buffer_load_dword v6, s[36:39], s68 offen offset:0 sc1 lds\n" \
  "s_waitcnt vmcnt(2)\n" \
  "s_barrier\n" \
  "ds_read_b32 v15, v7 offset:8192\n" \
  "s_mov_b32 s69, 18432\n" \
  "s_waitcnt lgkmcnt(0)\n" \
  "v_cmp_eq_u32 vcc, s60, v15\n" \
  "s_nop 0\n" \
  "s_cmp_eq_u32 vcc_lo, -1\n" \
  "s_cbranch_scc1 Lg1_go0_%=\n" \
  "s_add_u32 m0, s78, 16640\n" \
  "s_nop 0\n" \
  "buffer_load_dword v6, s[36:39], s68 offen offset:0 sc1 lds\n" \
  "s_waitcnt vmcnt(2)\n" \
  "s_barrier\n" \
  "ds_read_b32 v15, v7 offset:8448\n" \
  "s_mov_b32 s69, 18688\n" \
  "s_waitcnt lgkmcnt(0)\n" \
  "v_cmp_eq_u32 vcc, s60, v15\n" \
  "s_nop 0\n" \
  "s_cmp_eq_u32 vcc_lo, -1\n" \
  "s_cbranch_scc1 Lg1_go0_%=\n" \
  "s_add_u32 m0, s78, 8192\n" \
  "s_nop 0\n" \
  "buffer_load_dword v6, s[36:39], s68 offen offset:0 sc1 lds\n" \
  "s_waitcnt vmcnt(2)\n" \
  "s_barrier\n" \
  "ds_read_b32 v15, v7 offset:16384\n" \
  "s_mov_b32 s69, 10240\n" \
  "s_waitcnt lgkmcnt(0)\n" \
  "v_cmp_eq_u32 vcc, s60, v15\n" \
  "s_nop 0\n" \
  "s_cmp_eq_u32 vcc_lo, -1\n" \
  "s_cbranch_scc1 Lg1_go0_%=\n" \
  "s_add_u32 m0, s78, 8448\n" \
  "s_nop 0\n" \
  "buffer_load_dword v6, s[36:39], s68 offen offset:0 sc1 lds\n" \
  "s_waitcnt vmcnt(2)\n" \
  "s_barrier\n" \
  "ds_read_b32 v15, v7 offset:16640\n" \
  "s_mov_b32 s69, 10496\n" \
  "s_waitcnt lgkmcnt(0)\n" \
  "v_cmp_eq_u32 vcc, s60, v15\n" \
  "s_nop 0\n" \
  "s_cmp_eq_u32 vcc_lo, -1\n" \
  "s_cbranch_scc1 Lg1_go0_%=\n" \
  "s_add_u32 s79, s79, 1\n" \
  "s_cmp_lt_u32 s79, 20000\n" \
  "s_cbranch_scc1 Lg1_poll0_%=\n" \
  "Lg1_go0_%=:\n" \
  "s_xor_b32 s62, s63, 65536\n" \
  "s_cmp_lt_u32 s66, s59\n" \
  "s_cbranch_scc1 Lg1_gs0_%=\n" \
  "s_mov_b32 m0, s62\n" \
  "s_nop 0\n" \
  "buffer_load_dwordx4 v0, s[36:39], s48 offen offset:0 sc1 lds\n" \
  "buffer_load_dwordx4 v0, s[36:39], s48 offen offset:1024 sc1 lds\n" \
  "buffer_load_dwordx4 v0, s[36:39], s48 offen offset:2048 sc1 lds\n" \
  "buffer_load_dwordx4 v0, s[36:39], s48 offen offset:3072 sc1 lds\n" \
  "s_add_u32 m0, s62, 16384\n" \
  "s_lshr_b32 s72, s48, 6\n" \
  "s_lshr_b32 s71, s62, 2\n" \
  "buffer_load_dwordx4 v1, s[36:39], s48 offen offset:0 sc1 lds\n" \
  "buffer_load_dwordx4 v1, s[36:39], s48 offen offset:1024 sc1 lds\n" \
  "buffer_load_dwordx4 v1, s[36:39], s48 offen offset:2048 sc1 lds\n" \
  "buffer_load_dwordx4 v1, s[36:39], s48 offen offset:3072 sc1 lds\n" \
  "s_add_u32 m0, s71, s88\n" \
  "s_nop 0\n" \
  "buffer_load_dword v10, s[36:39], s72 offen offset:0 sc1 lds\n" \
  "Lg1_gs0_%=:\n" \
  "s_cmp_eq_u32 s54, 1\n" \
  "s_cselect_b32 s68, s50, s49\n" \
  "s_lshr_b32 s68, s68, 5\n" \
  "s_add_u32 m0, s78, 256\n" \
  "s_nop 0\n" \
  "buffer_load_dword v6, s[36:39], s68 offen offset:0 sc1 lds\n" \
  "s_cmp_eq_u32 vcc_hi, -1\n" \
  "s_cselect_b32 s66, s49, s59\n" \
  "s_xor_b32 s62, s64, 65536\n" \
  "s_mov_b32 m0, s62\n" \
  "s_nop 0\n" \
  "buffer_load_dwordx4 v0, s[36:39], s66 offen offset:0 sc1 lds\n" \
  "buffer_load_dwordx4 v0, s[36:39], s66 offen offset:1024 sc1 lds\n" \
  "buffer_load_dwordx4 v0, s[36:39], s66 offen offset:2048 sc1 lds\n" \
  "buffer_load_dwordx4 v0, s[36:39], s66 offen offset:3072 sc1 lds\n" \
  "s_add_u32 m0, s62, 16384\n" \
  "s_lshr_b32 s72, s66, 6\n" \
  "s_lshr_b32 s71, s62, 2\n" \
  "buffer_load_dwordx4 v1, s[36:39], s66 offen offset:0 sc1 lds\n" \
  "buffer_load_dwordx4 v1, s[36:39], s66 offen offset:1024 sc1 lds\n" \
  "buffer_load_dwordx4 v1, s[36:39], s66 offen offset:2048 sc1 lds\n" \
  "buffer_load_dwordx4 v1, s[36:39], s66 offen offset:3072 sc1 lds\n" \
  "s_add_u32 m0, s71, s88\n" \
  "s_nop 0\n" \
  "buffer_load_dword v10, s[36:39], s72 offen offset:0 sc1 lds\n" \
  "s_waitcnt vmcnt(15)\n" \
  "s_barrier\n" \
  "ds_read_b128 v[40:43], v2 offset:0\n" \
  "ds_read_b128 v[44:47], v2 offset:1024\n" \
  "ds_read_b128 v[48:51], v2 offset:2048\n" \
  "ds_read_b128 v[52:55], v2 offset:3072\n" \
  "ds_read_b128 v[56:59], v2 offset:4096\n" \
  "ds_read_b128 v[60:63], v2 offset:5120\n" \
  "ds_read_b128 v[64:67], v2 offset:6144\n" \
  "s_lshr_b32 s70, s45, 5\n" \
  "buffer_store_dword v5, v4, s[36:39], s70 offen offset:0 sc0 sc1\n" \
  "s_mov_b32 s45, s59\n" \
  "s_lshr_b32 s70, s46, 5\n" \
  "buffer_store_dword v5, v4, s[36:39], s70 offen offset:0 sc0 sc1\n" \
  "s_mov_b32 s46, s59\n" \
  "s_lshr_b32 s70, s47, 5\n" \
  "buffer_store_dword v5, v4, s[36:39], s70 offen offset:0 sc0 sc1\n" \
  "s_mov_b32 s47, s59\n" \
  "s_waitcnt lgkmcnt(3)\n" \
  "s_branch Lg1_d0_%=\n" \
  "Lg1_done0_%=:\n" \
  "s_waitcnt vmcnt(0)\n" \
  "s_barrier\n" \
  "s_lshr_b32 s70, s45, 5\n" \
  "buffer_store_dword v5, v4, s[36:39], s70 offen offset:0 sc0 sc1\n" \
  "s_lshr_b32 s70, s46, 5\n" \
  "buffer_store_dword v5, v4, s[36:39], s70 offen offset:0 sc0 sc1\n" \
  "s_lshr_b32 s70, s47, 5\n" \
  "buffer_store_dword v5, v4, s[36:39], s70 offen offset:0 sc0 sc1\n" \
  "s_branch Lg1_done_%=\n" \
  "Lg1_slow1_%=:\n" \
  "s_cmp_eq_u32 s54, 1\n" \
  "s_cselect_b32 s68, s50, s49\n" \
  "s_lshr_b32 s68, s68, 5\n" \
  "s_add_u32 m0, s78, 8192\n" \
  "s_nop 0\n" \
  "buffer_load_dword v6, s[36:39], s68 offen offset:0 sc1 lds\n" \
  "s_lshr_b32 s73, s48, 0\n" \
  "v_fma_f32 v24, -v88, v16, v24\n" \
  "v_fma_f32 v25, -v88, v17, v25\n" \
  "v_fma_f32 v26, -v88, v18, v26\n" \
  "v_fma_f32 v27, -v88, v19, v27\n" \
  "v_mul_f32 v72, v24, v24\n" \
  "v_mul_f32 v73, v25, v25\n" \
  "v_mul_f32 v74, v26, v26\n" \
  "v_mul_f32 v75, v27, v27\n" \
  "v_fma_f32 v72, v90, v72, v89\n" \
  "v_fma_f32 v73, v90, v73, v89\n" \
  "v_fma_f32 v74, v90, v74, v89\n" \
  "v_fma_f32 v75, v90, v75, v89\n" \
  "v_mul_f32 v72, v24, v72\n" \
  "v_mul_f32 v73, v25, v73\n" \
  "v_mul_f32 v74, v26, v74\n" \
  "v_mul_f32 v75, v27, v75\n" \
  "v_exp_f32 v72, v72\n" \
  "v_exp_f32 v73, v73\n" \
  "v_exp_f32 v74, v74\n" \
  "v_exp_f32 v75, v75\n" \
  "v_fma_f32 v72, v72, v91, v91\n" \
  "v_fma_f32 v73, v73, v91, v91\n" \
  "v_fma_f32 v74, v74, v91, v91\n" \
  "v_fma_f32 v75, v75, v91, v91\n" \
  "v_rcp_f32 v72, v72\n" \
  "v_rcp_f32 v73, v73\n" \
  "v_rcp_f32 v74, v74\n" \
  "v_rcp_f32 v75, v75\n" \
  "v_mul_f32 v24, v24, v72\n" \
  "v_mul_f32 v25, v25, v73\n" \
  "v_mul_f32 v26, v26, v74\n" \
  "v_mul_f32 v27, v27, v75\n" \
  "v_cvt_pk_bf16_f32 v80, v24, v25\n" \
  "v_cvt_pk_bf16_f32 v81, v26, v27\n" \
  "s_add_u32 m0, s78, 8448\n" \
  "s_nop 0\n" \
  "buffer_load_dword v6, s[36:39], s68 offen offset:0 sc1 lds\n" \
  "v_fma_f32 v28, -v88, v20, v28\n" \
  "v_fma_f32 v29, -v88, v21, v29\n" \
  "v_fma_f32 v30, -v88, v22, v30\n" \
  "v_fma_f32 v31, -v88, v23, v31\n" \
  "v_mul_f32 v76, v28, v28\n" \
  "v_mul_f32 v77, v29, v29\n" \
  "v_mul_f32 v78, v30, v30\n" \
  "v_mul_f32 v79, v31, v31\n" \
  "v_fma_f32 v76, v90, v76, v89\n" \
  "v_fma_f32 v77, v90, v77, v89\n" \
  "v_fma_f32 v78, v90, v78, v89\n" \
  "v_fma_f32 v79, v90, v79, v89\n" \
  "v_mul_f32 v76, v28, v76\n" \
  "v_mul_f32 v77, v29, v77\n" \
  "v_mul_f32 v78, v30, v78\n" \
  "v_mul_f32 v79, v31, v79\n" \
  "v_exp_f32 v76, v76\n" \
  "v_exp_f32 v77, v77\n" \
  "v_exp_f32 v78, v78\n" \
  "v_exp_f32 v79, v79\n" \
  "v_fma_f32 v76, v76, v91, v91\n" \
  "v_fma_f32 v77, v77, v91, v91\n" \
  "v_fma_f32 v78, v78, v91, v91\n" \
  "v_fma_f32 v79, v79, v91, v91\n" \
  "v_rcp_f32 v76, v76\n" \
  "v_rcp_f32 v77, v77\n" \
  "v_rcp_f32 v78, v78\n" \
  "v_rcp_f32 v79, v79\n" \
  "v_mul_f32 v28, v28, v76\n" \
  "v_mul_f32 v29, v29, v77\n" \
  "v_mul_f32 v30, v30, v78\n" \
  "v_mul_f32 v31, v31, v79\n" \
  "v_cvt_pk_bf16_f32 v82, v28, v29\n" \
  "v_cvt_pk_bf16_f32 v83, v30, v31\n" \
  "buffer_store_dwordx4 v[80:83], v3, s[36:39], s73 offen offset:0 sc0 sc1\n" \
  "s_cmp_ge_u32 s49, s59\n" \
  "s_cbranch_scc1 Lg1_done1_%=\n" \
  "s_add_u32 m0, s78, 16384\n" \
  "s_nop 0\n" \
  "buffer_load_dword v6, s[36:39], s68 offen offset:0 sc1 lds\n" \
  "s_waitcnt vmcnt(3)\n" \
  "s_barrier\n" \
  "ds_read_b32 v15, v7 offset:8192\n" \
  "s_lshr_b32 s70, s46, 5\n" \
  "buffer_store_dword v5, v4, s[36:39], s70 offen offset:0 sc0 sc1\n" \
  "s_mov_b32 s46, s59\n" \
  "s_lshr_b32 s70, s47, 5\n" \
  "buffer_store_dword v5, v4, s[36:39], s70 offen offset:0 sc0 sc1\n" \
  "s_mov_b32 s47, s59\n" \
  "s_mov_b32 s69, 18432\n" \
  "s_waitcnt lgkmcnt(0)\n" \
  "v_cmp_eq_u32 vcc, s60, v15\n" \
  "s_nop 0\n" \
  "s_cmp_eq_u32 vcc_lo, -1\n" \
  "s_cbranch_scc1 Lg1_go1_%=\n" \
  "s_add_u32 m0, s78, 16640\n" \
  "s_nop 0\n" \
  "buffer_load_dword v6, s[36:39], s68 offen offset:0 sc1 lds\n" \
  "s_waitcnt vmcnt(5)\n" \
  "s_barrier\n" \
  "ds_read_b32 v15, v7 offset:8448\n" \
  "s_mov_b32 s69, 18688\n" \
  "s_waitcnt lgkmcnt(0)\n" \
  "v_cmp_eq_u32 vcc, s60, v15\n" \
  "s_nop 0\n" \
  "s_cmp_eq_u32 vcc_lo, -1\n" \
  "s_cbranch_scc1 Lg1_go1_%=\n" \
  "s_add_u32 m0, s78, 8192\n" \
  "s_nop 0\n" \
  "buffer_load_dword v6, s[36:39], s68 offen offset:0 sc1 lds\n" \
  "s_waitcnt vmcnt(4)\n" \
  "s_barrier\n" \
  "ds_read_b32 v15, v7 offset:16384\n" \
  "s_lshr_b32 s70, s48, 5\n" \
  "buffer_store_dword v5, v4, s[36:39], s70 offen offset:0 sc0 sc1\n" \
  "s_mov_b32 s48, s59\n" \
  "s_mov_b32 s69, 10240\n" \
  "s_waitcnt lgkmcnt(0)\n" \
  "v_cmp_eq_u32 vcc, s60, v15\n" \
  "s_nop 0\n" \
  "s_cmp_eq_u32 vcc_lo, -1\n" \
  "s_cbranch_scc1 Lg1_go1_%=\n" \
  "s_add_u32 m0, s78, 8448\n" \
  "s_nop 0\n" \
  "buffer_load_dword v6, s[36:39], s68 offen offset:0 sc1 lds\n" \
  "s_waitcnt vmcnt(3)\n" \
  "s_barrier\n" \
  "ds_read_b32 v15, v7 offset:16640\n" \
  "s_mov_b32 s69, 10496\n" \
  "s_waitcnt lgkmcnt(0)\n" \
  "v_cmp_eq_u32 vcc, s60, v15\n" \
  "s_nop 0\n" \
  "s_cmp_eq_u32 vcc_lo, -1\n" \
  "s_cbranch_scc1 Lg1_go1_%=\n" \
  "Lg1_poll1_%=:\n" \
  "s_add_u32 m0, s78, 16384\n" \
  "s_nop 0\n" \
  "buffer_load_dword v6, s[36:39], s68 offen offset:0 sc1 lds\n" \
  "s_waitcnt vmcnt(2)\n" \
  "s_barrier\n" \
  "ds_read_b32 v15, v7 offset:8192\n" \
  "s_mov_b32 s69, 18432\n" \
  "s_waitcnt lgkmcnt(0)\n" \
  "v_cmp_eq_u32 vcc, s60, v15\n" \
  "s_nop 0\n" \
  "s_cmp_eq_u32 vcc_lo, -1\n" \
  "s_cbranch_scc1 Lg1_go1_%=\n" \
  "s_add_u32 m0, s78, 16640\n" \
  "s_nop 0\n" \
  "buffer_load_dword v6, s[36:39], s68 offen offset:0 sc1 lds\n" \
  "s_waitcnt vmcnt(2)\n" \
  "s_barrier\n" \
  "ds_read_b32 v15, v7 offset:8448\n" \
  "s_mov_b32 s69, 18688\n" \
  "s_waitcnt lgkmcnt(0)\n" \
  "v_cmp_eq_u32 vcc, s60, v15\n" \
  "s_nop 0\n" \
  "s_cmp_eq_u32 vcc_lo, -1\n" \
  "s_cbranch_scc1 Lg1_go1_%=\n" \
  "s_add_u32 m0, s78, 8192\n" \
  "s_nop 0\n" \
  "buffer_load_dword v6, s[36:39], s68 offen offset:0 sc1 lds\n" \
  "s_waitcnt vmcnt(2)\n" \
  "s_barrier\n" \
  "ds_read_b32 v15, v7 offset:16384\n" \
  "s_mov_b32 s69, 10240\n" \
  "s_waitcnt lgkmcnt(0)\n" \
  "v_cmp_eq_u32 vcc, s60, v15\n" \
  "s_nop 0\n" \
  "s_cmp_eq_u32 vcc_lo, -1\n" \
  "s_cbranch_scc1 Lg1_go1_%=\n" \
  "s_add_u32 m0, s78, 8448\n" \
  "s_nop 0\n" \
  "buffer_load_dword v6, s[36:39], s68 offen offset:0 sc1 lds\n" \
  "s_waitcnt vmcnt(2)\n" \
  "s_barrier\n" \
  "ds_read_b32 v15, v7 offset:16640\n" \
  "s_mov_b32 s69, 10496\n" \
  "s_waitcnt lgkmcnt(0)\n" \
  "v_cmp_eq_u32 vcc, s60, v15\n" \
  "s_nop 0\n" \
  "s_cmp_eq_u32 vcc_lo, -1\n" \
  "s_cbranch_scc1 Lg1_go1_%=\n" \
  "s_add_u32 s79, s79, 1\n" \
  "s_cmp_lt_u32 s79, 20000\n" \
  "s_cbranch_scc1 Lg1_poll1_%=\n" \
  "Lg1_go1_%=:\n" \
  "s_xor_b32 s62, s64, 65536\n" \
  "s_cmp_lt_u32 s67, s59\n" \
  "s_cbranch_scc1 Lg1_gs1_%=\n" \
  "s_mov_b32 m0, s62\n" \
  "s_nop 0\n" \
  "buffer_load_dwordx4 v0, s[36:39], s49 offen offset:0 sc1 lds\n" \
  "buffer_load_dwordx4 v0, s[36:39], s49 offen offset:1024 sc1 lds\n" \
  "buffer_load_dwordx4 v0, s[36:39], s49 offen offset:2048 sc1 lds\n" \
  "buffer_load_dwordx4 v0, s[36:39], s49 offen offset:3072 sc1 lds\n" \
  "s_add_u32 m0, s62, 16384\n" \
  "s_lshr_b32 s72, s49, 6\n" \
  "s_lshr_b32 s71, s62, 2\n" \
  "buffer_load_dwordx4 v1, s[36:39], s49 offen offset:0 sc1 lds\n" \
  "buffer_load_dwordx4 v1, s[36:39], s49 offen offset:1024 sc1 lds\n" \
  "buffer_load_dwordx4 v1, s[36:39], s49 offen offset:2048 sc1 lds\n" \
  "buffer_load_dwordx4 v1, s[36:39], s49 offen offset:3072 sc1 lds\n" \
  "s_add_u32 m0, s71, s88\n" \
  "s_nop 0\n" \
  "buffer_load_dword v10, s[36:39], s72 offen offset:0 sc1 lds\n" \
  "Lg1_gs1_%=:\n" \
  "s_cmp_eq_u32 s54, 1\n" \
  "s_cselect_b32 s68, s51, s50\n" \
  "s_lshr_b32 s68, s68, 5\n" \
  "s_add_u32 m0, s78, 0\n" \
  "s_nop 0\n" \
  "buffer_load_dword v6, s[36:39], s68 offen offset:0 sc1 lds\n" \
  "s_cmp_eq_u32 vcc_hi, -1\n" \
  "s_cselect_b32 s67, s50, s59\n" \
  "s_xor_b32 s62, s63, 65536\n" \
  "s_mov_b32 m0, s62\n" \
  "s_nop 0\n" \
  "buffer_load_dwordx4 v0, s[36:39], s67 offen offset:0 sc1 lds\n" \
  "buffer_load_dwordx4 v0, s[36:39], s67 offen offset:1024 sc1 lds\n" \
  "buffer_load_dwordx4 v0, s[36:39], s67 offen offset:2048 sc1 lds\n" \
  "buffer_load_dwordx4 v0, s[36:39], s67 offen offset:3072 sc1 lds\n" \
  "s_add_u32 m0, s62, 16384\n" \
  "s_lshr_b32 s72, s67, 6\n" \
  "s_lshr_b32 s71, s62, 2\n" \
  "buffer_load_dwordx4 v1, s[36:39], s67 offen offset:0 sc1 lds\n" \
  "buffer_load_dwordx4 v1, s[36:39], s67 offen offset:1024 sc1 lds\n" \
  "buffer_load_dwordx4 v1, s[36:39], s67 offen offset:2048 sc1 lds\n" \
  "buffer_load_dwordx4 v1, s[36:39], s67 offen offset:3072 sc1 lds\n" \
  "s_add_u32 m0, s71, s88\n" \
  "s_nop 0\n" \
  "buffer_load_dword v10, s[36:39], s72 offen offset:0 sc1 lds\n" \
  "s_waitcnt vmcnt(15)\n" \
  "s_barrier\n" \
  "ds_read_b128 v[40:43], v2 offset:32768\n" \
  "ds_read_b128 v[44:47], v2 offset:33792\n" \
  "ds_read_b128 v[48:51], v2 offset:34816\n" \
  "ds_read_b128 v[52:55], v2 offset:35840\n" \
  "ds_read_b128 v[56:59], v2 offset:36864\n" \
  "ds_read_b128 v[60:63], v2 offset:37888\n" \
  "ds_read_b128 v[64:67], v2 offset:38912\n" \
  "s_lshr_b32 s70, s46, 5\n" \
  "buffer_store_dword v5, v4, s[36:39], s70 offen offset:0 sc0 sc1\n" \
  "s_mov_b32 s46, s59\n" \
  "s_lshr_b32 s70, s47, 5\n" \
  "buffer_store_dword v5, v4, s[36:39], s70 offen offset:0 sc0 sc1\n" \
  "s_mov_b32 s47, s59\n" \
  "s_lshr_b32 s70, s48, 5\n" \
  "buffer_store_dword v5, v4, s[36:39], s70 offen offset:0 sc0 sc1\n" \
  "s_mov_b32 s48, s59\n" \
  "s_waitcnt lgkmcnt(3)\n" \
  "s_branch Lg1_d1_%=\n" \
  "Lg1_done1_%=:\n" \
  "s_waitcnt vmcnt(0)\n" \
  "s_barrier\n" \
  "s_lshr_b32 s70, s46, 5\n" \
  "buffer_store_dword v5, v4, s[36:39], s70 offen offset:0 sc0 sc1\n" \
  "s_lshr_b32 s70, s47, 5\n" \
  "buffer_store_dword v5, v4, s[36:39], s70 offen offset:0 sc0 sc1\n" \
  "s_lshr_b32 s70, s48, 5\n" \
  "buffer_store_dword v5, v4, s[36:39], s70 offen offset:0 sc0 sc1\n" \
  "s_branch Lg1_done_%=\n" \
  "Lg1_done_%=:\n" \
  "s_waitcnt vmcnt(0) lgkmcnt(0)\n" \
  "s_barrier\n" \
  "s_branch Lg2_end_%=\n" \
  "Lg2_begin_%=:\n" \
  "s_mov_b64 s[36:37], %1\n" \
  "s_mov_b32 s38, 111193216\n" \
  "s_mov_b32 s39, 0x00020000\n" \
  "s_mov_b32 s54, %2\n" \
  "s_mov_b32 s55, %3\n" \
  "s_mov_b32 s56, %4\n" \
  "s_mov_b32 s58, %5\n" \
  "s_mov_b32 s83, %6\n" \
  "s_mov_b32 s82, %7\n" \
  "s_mov_b32 s84, %8\n" \
  "v_mov_b32 v0, %9\n" \
  "v_mov_b32 v2, %10\n" \
  "v_mov_b32 v3, %11\n" \
  "v_mov_b32 v4, %12\n" \
  "v_mov_b32 v6, %13\n" \
  "v_mov_b32 v7, %14\n" \
  "v_mov_b32 v8, %15\n" \
  "v_mov_b32 v9, %16\n" \
  "v_mov_b32 v10, %17\n" \
  "v_mov_b32 v11, %18\n" \
  "v_mov_b32 v12, %19\n" \
  "v_mov_b32 v13, %20\n" \
  "v_mov_b32 v14, %21\n" \
  "v_mov_b32 v16, %22\n" \
  "v_mov_b32 v17, %23\n" \
  "v_mov_b32 v18, %24\n" \
  "v_mov_b32 v19, %25\n" \
  "v_mov_b32 v20, %26\n" \
  "s_mov_b64 s[40:41], %27\n" \
  "s_mov_b32 s42, 2097152\n" \
  "s_mov_b32 s43, 0x00020000\n" \
  "s_lshl_b32 s88, s54, 11\n" \
  "s_mov_b32 s69, 111193088\n" \
  "buffer_load_dword v5, off, s[36:39], s69 sc1\n" \
  "s_add_u32 s61, s82, 0\n" \
  "s_add_u32 m0, s88, 0\n" \
  "s_nop 0\n" \
  "buffer_load_dwordx4 v16, s[40:43], s61 offen offset:0  lds\n" \
  "s_add_u32 m0, s88, 1024\n" \
  "s_nop 0\n" \
  "buffer_load_dwordx4 v17, s[40:43], s61 offen offset:0  lds\n" \
  "s_add_u32 m0, s88, 8064\n" \
  "s_nop 0\n" \
  "buffer_load_dwordx4 v16, s[40:43], s61 offen offset:128  lds\n" \
  "s_add_u32 m0, s88, 9088\n" \
  "s_nop 0\n" \
  "buffer_load_dwordx4 v17, s[40:43], s61 offen offset:128  lds\n" \
  "s_add_u32 s61, s82, 131072\n" \
  "s_add_u32 m0, s88, 16384\n" \
  "s_nop 0\n" \
  "buffer_load_dwordx4 v16, s[40:43], s61 offen offset:0  lds\n" \
  "s_add_u32 m0, s88, 17408\n" \
  "s_nop 0\n" \
  "buffer_load_dwordx4 v17, s[40:43], s61 offen offset:0  lds\n" \
  "s_add_u32 m0, s88, 24448\n" \
  "s_nop 0\n" \
  "buffer_load_dwordx4 v16, s[40:43], s61 offen offset:128  lds\n" \
  "s_add_u32 m0, s88, 25472\n" \
  "s_nop 0\n" \
  "buffer_load_dwordx4 v17, s[40:43], s61 offen offset:128  lds\n" \
  "s_add_u32 s61, s82, 262144\n" \
  "s_add_u32 m0, s88, 32768\n" \
  "s_nop 0\n" \
  "buffer_load_dwordx4 v16, s[40:43], s61 offen offset:0  lds\n" \
  "s_add_u32 m0, s88, 33792\n" \
  "s_nop 0\n" \
  "buffer_load_dwordx4 v17, s[40:43], s61 offen offset:0  lds\n" \
  "s_add_u32 m0, s88, 40832\n" \
  "s_nop 0\n" \
  "buffer_load_dwordx4 v16, s[40:43], s61 offen offset:128  lds\n" \
  "s_add_u32 m0, s88, 41856\n" \
  "s_nop 0\n" \
  "buffer_load_dwordx4 v17, s[40:43], s61 offen offset:128  lds\n" \
  "s_add_u32 s61, s82, 393216\n" \
  "s_add_u32 m0, s88, 49152\n" \
  "s_nop 0\n" \
  "buffer_load_dwordx4 v16, s[40:43], s61 offen offset:0  lds\n" \
  "s_add_u32 m0, s88, 50176\n" \
  "s_nop 0\n" \
  "buffer_load_dwordx4 v17, s[40:43], s61 offen offset:0  lds\n" \
  "s_add_u32 m0, s88, 57216\n" \
  "s_nop 0\n" \
  "buffer_load_dwordx4 v16, s[40:43], s61 offen offset:128  lds\n" \
  "s_add_u32 m0, s88, 58240\n" \
  "s_nop 0\n" \
  "buffer_load_dwordx4 v17, s[40:43], s61 offen offset:128  lds\n" \
  "s_add_u32 s61, s82, 524288\n" \
  "s_add_u32 m0, s88, 65536\n" \
  "s_nop 0\n" \
  "buffer_load_dwordx4 v16, s[40:43], s61 offen offset:0  lds\n" \
  "s_add_u32 m0, s88, 66560\n" \
  "s_nop 0\n" \
  "buffer_load_dwordx4 v17, s[40:43], s61 offen offset:0  lds\n" \
  "s_add_u32 m0, s88, 73600\n" \
  "s_nop 0\n" \
  "buffer_load_dwordx4 v16, s[40:43], s61 offen offset:128  lds\n" \
  "s_add_u32 m0, s88, 74624\n" \
  "s_nop 0\n" \
  "buffer_load_dwordx4 v17, s[40:43], s61 offen offset:128  lds\n" \
  "s_add_u32 s61, s82, 655360\n" \
  "s_add_u32 m0, s88, 81920\n" \
  "s_nop 0\n" \
  "buffer_load_dwordx4 v16, s[40:43], s61 offen offset:0  lds\n" \
  "s_add_u32 m0, s88, 82944\n" \
  "s_nop 0\n" \
  "buffer_load_dwordx4 v17, s[40:43], s61 offen offset:0  lds\n" \
  "s_add_u32 m0, s88, 89984\n" \
  "s_nop 0\n" \
  "buffer_load_dwordx4 v16, s[40:43], s61 offen offset:128  lds\n" \
  "s_add_u32 m0, s88, 91008\n" \
  "s_nop 0\n" \
  "buffer_load_dwordx4 v17, s[40:43], s61 offen offset:128  lds\n" \
  "s_waitcnt vmcnt(20)\n" \
  "s_waitcnt lgkmcnt(0)\n" \
  "s_barrier\n" \
  "s_add_u32 s61, s82, 786432\n" \
  "s_add_u32 m0, s88, 98304\n" \
  "s_nop 0\n" \
  "buffer_load_dwordx4 v16, s[40:43], s61 offen offset:0  lds\n" \
  "s_add_u32 m0, s88, 99328\n" \
  "s_nop 0\n" \
  "buffer_load_dwordx4 v17, s[40:43], s61 offen offset:0  lds\n" \
  "s_add_u32 m0, s88, 106368\n" \
  "s_nop 0\n" \
  "buffer_load_dwordx4 v16, s[40:43], s61 offen offset:128  lds\n" \
  "s_add_u32 m0, s88, 107392\n" \
  "s_nop 0\n" \
  "buffer_load_dwordx4 v17, s[40:43], s61 offen offset:128  lds\n" \
  "v_add_u32 v64, 0, v18\n" \
  "v_add_u32 v65, 0, v19\n" \
  "ds_read_b64_tr_b16 a[0:1], v64 offset:0\n" \
  "ds_read_b64_tr_b16 a[2:3], v64 offset:512\n" \
  "ds_read_b64_tr_b16 a[4:5], v65 offset:0\n" \
  "ds_read_b64_tr_b16 a[6:7], v65 offset:512\n" \
  "ds_read_b64_tr_b16 a[8:9], v64 offset:4096\n" \
  "ds_read_b64_tr_b16 a[10:11], v64 offset:4608\n" \
  "ds_read_b64_tr_b16 a[12:13], v65 offset:4096\n" \
  "ds_read_b64_tr_b16 a[14:15], v65 offset:4608\n" \
  "s_waitcnt vmcnt(20)\n" \
  "s_waitcnt lgkmcnt(0)\n" \
  "s_barrier\n" \
  "s_add_u32 s61, s82, 917504\n" \
  "s_add_u32 m0, s88, 0\n" \
  "s_nop 0\n" \
  "buffer_load_dwordx4 v16, s[40:43], s61 offen offset:0  lds\n" \
  "s_add_u32 m0, s88, 1024\n" \
  "s_nop 0\n" \
  "buffer_load_dwordx4 v17, s[40:43], s61 offen offset:0  lds\n" \
  "s_add_u32 m0, s88, 8064\n" \
  "s_nop 0\n" \
  "buffer_load_dwordx4 v16, s[40:43], s61 offen offset:128  lds\n" \
  "s_add_u32 m0, s88, 9088\n" \
  "s_nop 0\n" \
  "buffer_load_dwordx4 v17, s[40:43], s61 offen offset:128  lds\n" \
  "v_add_u32 v64, 16384, v18\n" \
  "v_add_u32 v65, 16384, v19\n" \
  "ds_read_b64_tr_b16 a[16:17], v64 offset:0\n" \
  "ds_read_b64_tr_b16 a[18:19], v64 offset:512\n" \
  "ds_read_b64_tr_b16 a[20:21], v65 offset:0\n" \
  "ds_read_b64_tr_b16 a[22:23], v65 offset:512\n" \
  "ds_read_b64_tr_b16 a[24:25], v64 offset:4096\n" \
  "ds_read_b64_tr_b16 a[26:27], v64 offset:4608\n" \
  "ds_read_b64_tr_b16 a[28:29], v65 offset:4096\n" \
  "ds_read_b64_tr_b16 a[30:31], v65 offset:4608\n" \
  "s_waitcnt vmcnt(20)\n" \
  "s_waitcnt lgkmcnt(0)\n" \
  "s_barrier\n" \
  "s_add_u32 s61, s82, 1048576\n" \
  "s_add_u32 m0, s88, 16384\n" \
  "s_nop 0\n" \
  "buffer_load_dwordx4 v16, s[40:43], s61 offen offset:0  lds\n" \
  "s_add_u32 m0, s88, 17408\n" \
  "s_nop 0\n" \
  "buffer_load_dwordx4 v17, s[40:43], s61 offen offset:0  lds\n" \
  "s_add_u32 m0, s88, 24448\n" \
  "s_nop 0\n" \
  "buffer_load_dwordx4 v16, s[40:43], s61 offen offset:128  lds\n" \
  "s_add_u32 m0, s88, 25472\n" \
  "s_nop 0\n" \
  "buffer_load_dwordx4 v17, s[40:43], s61 offen offset:128  lds\n" \
  "v_add_u32 v64, 32768, v18\n" \
  "v_add_u32 v65, 32768, v19\n" \
  "ds_read_b64_tr_b16 a[32:33], v64 offset:0\n" \
  "ds_read_b64_tr_b16 a[34:35], v64 offset:512\n" \
  "ds_read_b64_tr_b16 a[36:37], v65 offset:0\n" \
  "ds_read_b64_tr_b16 a[38:39], v65 offset:512\n" \
  "ds_read_b64_tr_b16 a[40:41], v64 offset:4096\n" \
  "ds_read_b64_tr_b16 a[42:43], v64 offset:4608\n" \
  "ds_read_b64_tr_b16 a[44:45], v65 offset:4096\n" \
  "ds_read_b64_tr_b16 a[46:47], v65 offset:4608\n" \
  "s_waitcnt vmcnt(20)\n" \
  "s_waitcnt lgkmcnt(0)\n" \
  "s_barrier\n" \
  "s_add_u32 s61, s82, 1179648\n" \
  "s_add_u32 m0, s88, 32768\n" \
  "s_nop 0\n" \
  "buffer_load_dwordx4 v16, s[40:43], s61 offen offset:0  lds\n" \
  "s_add_u32 m0, s88, 33792\n" \
  "s_nop 0\n" \
  "buffer_load_dwordx4 v17, s[40:43], s61 offen offset:0  lds\n" \
  "s_add_u32 m0, s88, 40832\n" \
  "s_nop 0\n" \
  "buffer_load_dwordx4 v16, s[40:43], s61 offen offset:128  lds\n" \
  "s_add_u32 m0, s88, 41856\n" \
  "s_nop 0\n" \
  "buffer_load_dwordx4 v17, s[40:43], s61 offen offset:128  lds\n" \
  "v_add_u32 v64, 49152, v18\n" \
  "v_add_u32 v65, 49152, v19\n" \
  "ds_read_b64_tr_b16 a[48:49], v64 offset:0\n" \
  "ds_read_b64_tr_b16 a[50:51], v64 offset:512\n" \
  "ds_read_b64_tr_b16 a[52:53], v65 offset:0\n" \
  "ds_read_b64_tr_b16 a[54:55], v65 offset:512\n" \
  "ds_read_b64_tr_b16 a[56:57], v64 offset:4096\n" \
  "ds_read_b64_tr_b16 a[58:59], v64 offset:4608\n" \
  "ds_read_b64_tr_b16 a[60:61], v65 offset:4096\n" \
  "ds_read_b64_tr_b16 a[62:63], v65 offset:4608\n" \
  "s_waitcnt vmcnt(20)\n" \
  "s_waitcnt lgkmcnt(0)\n" \
  "s_barrier\n" \
  "s_add_u32 s61, s82, 1310720\n" \
  "s_add_u32 m0, s88, 49152\n" \
  "s_nop 0\n" \
  "buffer_load_dwordx4 v16, s[40:43], s61 offen offset:0  lds\n" \
  "s_add_u32 m0, s88, 50176\n" \
  "s_nop 0\n" \
  "buffer_load_dwordx4 v17, s[40:43], s61 offen offset:0  lds\n" \
  "s_add_u32 m0, s88, 57216\n" \
  "s_nop 0\n" \
  "buffer_load_dwordx4 v16, s[40:43], s61 offen offset:128  lds\n" \
  "s_add_u32 m0, s88, 58240\n" \
  "s_nop 0\n" \
  "buffer_load_dwordx4 v17, s[40:43], s61 offen offset:128  lds\n" \
  "v_add_u32 v64, 65536, v18\n" \
  "v_add_u32 v65, 65536, v19\n" \
  "ds_read_b64_tr_b16 a[64:65], v64 offset:0\n" \
  "ds_read_b64_tr_b16 a[66:67], v64 offset:512\n" \
  "ds_read_b64_tr_b16 a[68:69], v65 offset:0\n" \
  "ds_read_b64_tr_b16 a[70:71], v65 offset:512\n" \
  "ds_read_b64_tr_b16 a[72:73], v64 offset:4096\n" \
  "ds_read_b64_tr_b16 a[74:75], v64 offset:4608\n" \
  "ds_read_b64_tr_b16 a[76:77], v65 offset:4096\n" \
  "ds_read_b64_tr_b16 a[78:79], v65 offset:4608\n" \
  "s_waitcnt vmcnt(20)\n" \
  "s_waitcnt lgkmcnt(0)\n" \
  "s_barrier\n" \
  "s_add_u32 s61, s82, 1441792\n" \
  "s_add_u32 m0, s88, 65536\n" \
  "s_nop 0\n" \
  "buffer_load_dwordx4 v16, s[40:43], s61 offen offset:0  lds\n" \
  "s_add_u32 m0, s88, 66560\n" \
  "s_nop 0\n" \
  "buffer_load_dwordx4 v17, s[40:43], s61 offen offset:0  lds\n" \
  "s_add_u32 m0, s88, 73600\n" \
  "s_nop 0\n" \
  "buffer_load_dwordx4 v16, s[40:43], s61 offen offset:128  lds\n" \
  "s_add_u32 m0, s88, 74624\n" \
  "s_nop 0\n" \
  "buffer_load_dwordx4 v17, s[40:43], s61 offen offset:128  lds\n" \
  "v_add_u32 v64, 81920, v18\n" \
  "v_add_u32 v65, 81920, v19\n" \
  "ds_read_b64_tr_b16 a[80:81], v64 offset:0\n" \
  "ds_read_b64_tr_b16 a[82:83], v64 offset:512\n" \
  "ds_read_b64_tr_b16 a[84:85], v65 offset:0\n" \
  "ds_read_b64_tr_b16 a[86:87], v65 offset:512\n" \
  "ds_read_b64_tr_b16 a[88:89], v64 offset:4096\n" \
  "ds_read_b64_tr_b16 a[90:91], v64 offset:4608\n" \
  "ds_read_b64_tr_b16 a[92:93], v65 offset:4096\n" \
  "ds_read_b64_tr_b16 a[94:95], v65 offset:4608\n" \
  "s_waitcnt vmcnt(20)\n" \
  "s_waitcnt lgkmcnt(0)\n" \
  "s_barrier\n" \
  "s_add_u32 s61, s82, 1572864\n" \
  "s_add_u32 m0, s88, 81920\n" \
  "s_nop 0\n" \
  "buffer_load_dwordx4 v16, s[40:43], s61 offen offset:0  lds\n" \
  "s_add_u32 m0, s88, 82944\n" \
  "s_nop 0\n" \
  "buffer_load_dwordx4 v17, s[40:43], s61 offen offset:0  lds\n" \
  "s_add_u32 m0, s88, 89984\n" \
  "s_nop 0\n" \
  "buffer_load_dwordx4 v16, s[40:43], s61 offen offset:128  lds\n" \
  "s_add_u32 m0, s88, 91008\n" \
  "s_nop 0\n" \
  "buffer_load_dwordx4 v17, s[40:43], s61 offen offset:128  lds\n" \
  "v_add_u32 v64, 98304, v18\n" \
  "v_add_u32 v65, 98304, v19\n" \
  "ds_read_b64_tr_b16 a[96:97], v64 offset:0\n" \
  "ds_read_b64_tr_b16 a[98:99], v64 offset:512\n" \
  "ds_read_b64_tr_b16 a[100:101], v65 offset:0\n" \
  "ds_read_b64_tr_b16 a[102:103], v65 offset:512\n" \
  "ds_read_b64_tr_b16 a[104:105], v64 offset:4096\n" \
  "ds_read_b64_tr_b16 a[106:107], v64 offset:4608\n" \
  "ds_read_b64_tr_b16 a[108:109], v65 offset:4096\n" \
  "ds_read_b64_tr_b16 a[110:111], v65 offset:4608\n" \
  "s_waitcnt vmcnt(20)\n" \
  "s_waitcnt lgkmcnt(0)\n" \
  "s_barrier\n" \
  "s_add_u32 s61, s82, 1703936\n" \
  "s_add_u32 m0, s88, 98304\n" \
  "s_nop 0\n" \
  "buffer_load_dwordx4 v16, s[40:43], s61 offen offset:0  lds\n" \
  "s_add_u32 m0, s88, 99328\n" \
  "s_nop 0\n" \
  "buffer_load_dwordx4 v17, s[40:43], s61 offen offset:0  lds\n" \
  "s_add_u32 m0, s88, 106368\n" \
  "s_nop 0\n" \
  "buffer_load_dwordx4 v16, s[40:43], s61 offen offset:128  lds\n" \
  "s_add_u32 m0, s88, 107392\n" \
  "s_nop 0\n" \
  "buffer_load_dwordx4 v17, s[40:43], s61 offen offset:128  lds\n" \
  "v_add_u32 v64, 0, v18\n" \
  "v_add_u32 v65, 0, v19\n" \
  "ds_read_b64_tr_b16 a[112:113], v64 offset:0\n" \
  "ds_read_b64_tr_b16 a[114:115], v64 offset:512\n" \
  "ds_read_b64_tr_b16 a[116:117], v65 offset:0\n" \
  "ds_read_b64_tr_b16 a[118:119], v65 offset:512\n" \
  "ds_read_b64_tr_b16 a[120:121], v64 offset:4096\n" \
  "ds_read_b64_tr_b16 a[122:123], v64 offset:4608\n" \
  "ds_read_b64_tr_b16 a[124:125], v65 offset:4096\n" \
  "ds_read_b64_tr_b16 a[126:127], v65 offset:4608\n" \
  "s_waitcnt vmcnt(20)\n" \
  "s_waitcnt lgkmcnt(0)\n" \
  "s_barrier\n" \
  "s_add_u32 s61, s82, 1835008\n" \
  "s_add_u32 m0, s88, 0\n" \
  "s_nop 0\n" \
  "buffer_load_dwordx4 v16, s[40:43], s61 offen offset:0  lds\n" \
  "s_add_u32 m0, s88, 1024\n" \
  "s_nop 0\n" \
  "buffer_load_dwordx4 v17, s[40:43], s61 offen offset:0  lds\n" \
  "s_add_u32 m0, s88, 8064\n" \
  "s_nop 0\n" \
  "buffer_load_dwordx4 v16, s[40:43], s61 offen offset:128  lds\n" \
  "s_add_u32 m0, s88, 9088\n" \
  "s_nop 0\n" \
  "buffer_load_dwordx4 v17, s[40:43], s61 offen offset:128  lds\n" \
  "v_add_u32 v64, 16384, v18\n" \
  "v_add_u32 v65, 16384, v19\n" \
  "ds_read_b64_tr_b16 a[128:129], v64 offset:0\n" \
  "ds_read_b64_tr_b16 a[130:131], v64 offset:512\n" \
  "ds_read_b64_tr_b16 a[132:133], v65 offset:0\n" \
  "ds_read_b64_tr_b16 a[134:135], v65 offset:512\n" \
  "ds_read_b64_tr_b16 a[136:137], v64 offset:4096\n" \
  "ds_read_b64_tr_b16 a[138:139], v64 offset:4608\n" \
  "ds_read_b64_tr_b16 a[140:141], v65 offset:4096\n" \
  "ds_read_b64_tr_b16 a[142:143], v65 offset:4608\n" \
  "s_waitcnt vmcnt(20)\n" \
  "s_waitcnt lgkmcnt(0)\n" \
  "s_barrier\n" \
  "s_add_u32 s61, s82, 1966080\n" \
  "s_add_u32 m0, s88, 16384\n" \
  "s_nop 0\n" \
  "buffer_load_dwordx4 v16, s[40:43], s61 offen offset:0  lds\n" \
  "s_add_u32 m0, s88, 17408\n" \
  "s_nop 0\n" \
  "buffer_load_dwordx4 v17, s[40:43], s61 offen offset:0  lds\n" \
  "s_add_u32 m0, s88, 24448\n" \
  "s_nop 0\n" \
  "buffer_load_dwordx4 v16, s[40:43], s61 offen offset:128  lds\n" \
  "s_add_u32 m0, s88, 25472\n" \
  "s_nop 0\n" \
  "buffer_load_dwordx4 v17, s[40:43], s61 offen offset:128  lds\n" \
  "v_add_u32 v64, 32768, v18\n" \
  "v_add_u32 v65, 32768, v19\n" \
  "ds_read_b64_tr_b16 a[144:145], v64 offset:0\n" \
  "ds_read_b64_tr_b16 a[146:147], v64 offset:512\n" \
  "ds_read_b64_tr_b16 a[148:149], v65 offset:0\n" \
  "ds_read_b64_tr_b16 a[150:151], v65 offset:512\n" \
  "ds_read_b64_tr_b16 a[152:153], v64 offset:4096\n" \
  "ds_read_b64_tr_b16 a[154:155], v64 offset:4608\n" \
  "ds_read_b64_tr_b16 a[156:157], v65 offset:4096\n" \
  "ds_read_b64_tr_b16 a[158:159], v65 offset:4608\n" \
  "s_waitcnt vmcnt(20)\n" \
  "s_waitcnt lgkmcnt(0)\n" \
  "s_barrier\n" \
  "v_add_u32 v64, 49152, v18\n" \
  "v_add_u32 v65, 49152, v19\n" \
  "ds_read_b64_tr_b16 a[160:161], v64 offset:0\n" \
  "ds_read_b64_tr_b16 a[162:163], v64 offset:512\n" \
  "ds_read_b64_tr_b16 a[164:165], v65 offset:0\n" \
  "ds_read_b64_tr_b16 a[166:167], v65 offset:512\n" \
  "ds_read_b64_tr_b16 a[168:169], v64 offset:4096\n" \
  "ds_read_b64_tr_b16 a[170:171], v64 offset:4608\n" \
  "ds_read_b64_tr_b16 a[172:173], v65 offset:4096\n" \
  "ds_read_b64_tr_b16 a[174:175], v65 offset:4608\n" \
  "s_waitcnt vmcnt(16)\n" \
  "s_waitcnt lgkmcnt(0)\n" \
  "s_barrier\n" \
  "v_add_u32 v64, 65536, v18\n" \
  "v_add_u32 v65, 65536, v19\n" \
  "ds_read_b64_tr_b16 a[176:177], v64 offset:0\n" \
  "ds_read_b64_tr_b16 a[178:179], v64 offset:512\n" \
  "ds_read_b64_tr_b16 a[180:181], v65 offset:0\n" \
  "ds_read_b64_tr_b16 a[182:183], v65 offset:512\n" \
  "ds_read_b64_tr_b16 a[184:185], v64 offset:4096\n" \
  "ds_read_b64_tr_b16 a[186:187], v64 offset:4608\n" \
  "ds_read_b64_tr_b16 a[188:189], v65 offset:4096\n" \
  "ds_read_b64_tr_b16 a[190:191], v65 offset:4608\n" \
  "s_waitcnt vmcnt(12)\n" \
  "s_waitcnt lgkmcnt(0)\n" \
  "s_barrier\n" \
  "v_add_u32 v64, 81920, v18\n" \
  "v_add_u32 v65, 81920, v19\n" \
  "ds_read_b64_tr_b16 a[192:193], v64 offset:0\n" \
  "ds_read_b64_tr_b16 a[194:195], v64 offset:512\n" \
  "ds_read_b64_tr_b16 a[196:197], v65 offset:0\n" \
  "ds_read_b64_tr_b16 a[198:199], v65 offset:512\n" \
  "ds_read_b64_tr_b16 a[200:201], v64 offset:4096\n" \
  "ds_read_b64_tr_b16 a[202:203], v64 offset:4608\n" \
  "ds_read_b64_tr_b16 a[204:205], v65 offset:4096\n" \
  "ds_read_b64_tr_b16 a[206:207], v65 offset:4608\n" \
  "s_waitcnt vmcnt(8)\n" \
  "s_waitcnt lgkmcnt(0)\n" \
  "s_barrier\n" \
  "v_add_u32 v64, 98304, v18\n" \
  "v_add_u32 v65, 98304, v19\n" \
  "ds_read_b64_tr_b16 a[208:209], v64 offset:0\n" \
  "ds_read_b64_tr_b16 a[210:211], v64 offset:512\n" \
  "ds_read_b64_tr_b16 a[212:213], v65 offset:0\n" \
  "ds_read_b64_tr_b16 a[214:215], v65 offset:512\n" \
  "ds_read_b64_tr_b16 a[216:217], v64 offset:4096\n" \
  "ds_read_b64_tr_b16 a[218:219], v64 offset:4608\n" \
  "ds_read_b64_tr_b16 a[220:221], v65 offset:4096\n" \
  "ds_read_b64_tr_b16 a[222:223], v65 offset:4608\n" \
  "s_waitcnt vmcnt(4)\n" \
  "s_waitcnt lgkmcnt(0)\n" \
  "s_barrier\n" \
  "v_add_u32 v64, 0, v18\n" \
  "v_add_u32 v65, 0, v19\n" \
  "ds_read_b64_tr_b16 a[224:225], v64 offset:0\n" \
  "ds_read_b64_tr_b16 a[226:227], v64 offset:512\n" \
  "ds_read_b64_tr_b16 a[228:229], v65 offset:0\n" \
  "ds_read_b64_tr_b16 a[230:231], v65 offset:512\n" \
  "ds_read_b64_tr_b16 a[232:233], v64 offset:4096\n" \
  "ds_read_b64_tr_b16 a[234:235], v64 offset:4608\n" \
  "ds_read_b64_tr_b16 a[236:237], v65 offset:4096\n" \
  "ds_read_b64_tr_b16 a[238:239], v65 offset:4608\n" \
  "s_waitcnt vmcnt(0)\n" \
  "s_waitcnt lgkmcnt(0)\n" \
  "s_barrier\n" \
  "v_add_u32 v64, 16384, v18\n" \
  "v_add_u32 v65, 16384, v19\n" \
  "ds_read_b64_tr_b16 a[240:241], v64 offset:0\n" \
  "ds_read_b64_tr_b16 a[242:243], v64 offset:512\n" \
  "ds_read_b64_tr_b16 a[244:245], v65 offset:0\n" \
  "ds_read_b64_tr_b16 a[246:247], v65 offset:512\n" \
  "ds_read_b64_tr_b16 a[248:249], v64 offset:4096\n" \
  "ds_read_b64_tr_b16 a[250:251], v64 offset:4608\n" \
  "ds_read_b64_tr_b16 a[252:253], v65 offset:4096\n" \
  "ds_read_b64_tr_b16 a[254:255], v65 offset:4608\n" \
  "s_waitcnt lgkmcnt(0)\n" \
  "s_barrier\n" \
  "v_readfirstlane_b32 s60, v5\n" \
  "s_nop 3\n" \
  "s_add_u32 s60, s60, 1\n" \
  "s_cmp_eq_u32 s60, 0\n" \
  "s_cselect_b32 s60, 1, s60\n" \
  "v_mov_b32 v5, s60\n" \
  "s_mov_b32 s59, 536870912\n" \
  "s_add_u32 s57, s56, 131072\n" \
  "s_mov_b32 s79, 0\n" \
  "s_mov_b32 s75, 0x3a800000\n" \
  "s_mov_b32 s76, 0xc0135761\n" \
  "s_mov_b32 s77, 0x3d372713\n" \
  "s_mov_b32 s44, s59\n" \
  "s_mov_b32 s45, s59\n" \
  "s_mov_b32 s46, s59\n" \
  "s_mov_b32 s47, s59\n" \
  "s_mov_b32 s48, s83\n" \
  "s_add_u32 s49, s48, s55\n" \
  "s_cmp_eq_u32 s49, s56\n" \
  "s_cselect_b32 s49, s57, s49\n" \
  "s_cmp_eq_u32 s49, s58\n" \
  "s_cselect_b32 s49, s59, s49\n" \
  "s_add_u32 s50, s49, s55\n" \
  "s_cmp_eq_u32 s50, s56\n" \
  "s_cselect_b32 s50, s57, s50\n" \
  "s_cmp_eq_u32 s50, s58\n" \
  "s_cselect_b32 s50, s59, s50\n" \
  "s_add_u32 s51, s50, s55\n" \
  "s_cmp_eq_u32 s51, s56\n" \
  "s_cselect_b32 s51, s57, s51\n" \
  "s_cmp_eq_u32 s51, s58\n" \
  "s_cselect_b32 s51, s59, s51\n" \
  "s_add_u32 s52, s51, s55\n" \
  "s_cmp_eq_u32 s52, s56\n" \
  "s_cselect_b32 s52, s57, s52\n" \
  "s_cmp_eq_u32 s52, s58\n" \
  "s_cselect_b32 s52, s59, s52\n" \
  "s_lshl_b32 s63, s54, 12\n" \
  "s_add_u32 s63, s63, 65536\n" \
  "s_add_u32 s64, s63, 32768\n" \
  "v_add_u32 v1, 16384, v0\n" \
  "s_lshl_b32 s88, s54, 10\n" \
  "s_add_u32 s88, s88, 131072\n" \
  "s_lshl_b32 s78, s54, 11\n" \
  "s_add_u32 s78, s78, 132608\n" \
  "s_mov_b32 s66, s59\n" \
  "s_branch Lg2_slow0_%=\n" \
  "Lg2_d0_%=:\n" \
  "v_mfma_f32_16x16x32_bf16 v[16:19], a[0:3], v[32:35], 0\n" \
  "s_lshr_b32 s73, s47, 0\n" \
  "ds_read_b128 v[60:63], v2 offset:7168\n" \
  "v_lshlrev_b32 v64, 16, v84\n" \
  "v_mfma_f32_16x16x32_bf16 v[20:23], a[4:7], v[32:35], 0\n" \
  "s_lshr_b32 s85, s47, 5\n" \
  "v_and_b32 v65, 0xffff0000, v84\n" \
  "v_lshlrev_b32 v66, 16, v85\n" \
  "v_mfma_f32_16x16x32_bf16 v[16:19], a[8:11], v[36:39], v[16:19]\n" \
  "s_cmp_eq_u32 s54, 1\n" \
  "ds_read_b128 v[32:35], v2 offset:8192\n" \
  "v_and_b32 v67, 0xffff0000, v85\n" \
  "v_mfma_f32_16x16x32_bf16 v[20:23], a[12:15], v[36:39], v[20:23]\n" \
  "s_cselect_b32 s68, s51, s50\n" \
  "v_lshlrev_b32 v68, 16, v86\n" \
  "v_and_b32 v69, 0xffff0000, v86\n" \
  "v_mfma_f32_16x16x32_bf16 v[16:19], a[16:19], v[40:43], v[16:19]\n" \
  "s_lshr_b32 s68, s68, 5\n" \
  "ds_read_b128 v[36:39], v2 offset:9216\n" \
  "v_lshlrev_b32 v70, 16, v87\n" \
  "v_mfma_f32_16x16x32_bf16 v[20:23], a[20:23], v[40:43], v[20:23]\n" \
  "s_lshr_b32 s70, s45, 5\n" \
  "v_and_b32 v71, 0xffff0000, v87\n" \
  "v_fma_f32 v64, v64, v91, v90\n" \
  "v_mfma_f32_16x16x32_bf16 v[16:19], a[24:27], v[44:47], v[16:19]\n" \
  "s_add_u32 m0, s78, 0\n" \
  "ds_read_b128 v[40:43], v2 offset:10240\n" \
  "v_fma_f32 v65, v65, v91, v90\n" \
  "v_mfma_f32_16x16x32_bf16 v[20:23], a[28:31], v[44:47], v[20:23]\n" \
  "s_waitcnt lgkmcnt(3)\n" \
  "v_fma_f32 v66, v66, v91, v90\n" \
  "v_fma_f32 v67, v67, v91, v90\n" \
  "v_mfma_f32_16x16x32_bf16 v[16:19], a[32:35], v[48:51], v[16:19]\n" \
  "s_xor_b32 s69, s64, 65536\n" \
  "ds_read_b128 v[44:47], v2 offset:11264\n" \
  "v_fma_f32 v68, v68, v91, v90\n" \
  "v_mfma_f32_16x16x32_bf16 v[20:23], a[36:39], v[48:51], v[20:23]\n" \
  "s_cmp_ge_u32 s66, s59\n" \
  "v_fma_f32 v69, v69, v91, v90\n" \
  "v_fma_f32 v70, v70, v91, v90\n" \
  "v_mfma_f32_16x16x32_bf16 v[16:19], a[40:43], v[52:55], v[16:19]\n" \
  "s_cselect_b32 s62, s69, s63\n" \
  "ds_read_b128 v[48:51], v2 offset:12288\n" \
  "v_fma_f32 v71, v71, v91, v90\n" \
  "v_mfma_f32_16x16x32_bf16 v[20:23], a[44:47], v[52:55], v[20:23]\n" \
  "s_cselect_b32 s86, s49, s50\n" \
  "v_add_f32 v24, v24, v64\n" \
  "v_add_f32 v25, v25, v65\n" \
  "v_mfma_f32_16x16x32_bf16 v[16:19], a[48:51], v[56:59], v[16:19]\n" \
  "ds_read_b128 v[52:55], v2 offset:13312\n" \
  "v_add_f32 v26, v26, v66\n" \
  "v_mfma_f32_16x16x32_bf16 v[20:23], a[52:55], v[56:59], v[20:23]\n" \
  "v_add_f32 v27, v27, v67\n" \
  "v_add_f32 v28, v28, v68\n" \
  "v_mfma_f32_16x16x32_bf16 v[16:19], a[56:59], v[60:63], v[16:19]\n" \
  "ds_read_b128 v[56:59], v2 offset:14336\n" \
  "v_add_f32 v29, v29, v69\n" \
  "v_mfma_f32_16x16x32_bf16 v[20:23], a[60:63], v[60:63], v[20:23]\n" \
  "s_waitcnt lgkmcnt(3)\n" \
  "v_add_f32 v30, v30, v70\n" \
  "v_add_f32 v31, v31, v71\n" \
  "v_mfma_f32_16x16x32_bf16 v[16:19], a[64:67], v[32:35], v[16:19]\n" \
  "s_waitcnt vmcnt(12)\n" \
  "ds_read_b128 v[60:63], v2 offset:15360\n" \
  "v_cvt_pk_bf16_f32 v72, v24, v25\n" \
  "v_mfma_f32_16x16x32_bf16 v[20:23], a[68:71], v[32:35], v[20:23]\n" \
  "s_barrier\n" \
  "v_mfma_f32_16x16x32_bf16 v[16:19], a[72:75], v[36:39], v[16:19]\n" \
  "ds_read_b128 v[32:35], v2 offset:16384\n" \
  "ds_read_b32 v15, v7 offset:256\n" \
  "v_mfma_f32_16x16x32_bf16 v[20:23], a[76:79], v[36:39], v[20:23]\n" \
  "buffer_store_dword v5, v4, s[36:39], s70 offen offset:0 sc0 sc1\n" \
  "v_cvt_pk_bf16_f32 v73, v26, v27\n" \
  "v_cvt_pk_bf16_f32 v74, v28, v29\n" \
  "v_cvt_pk_bf16_f32 v75, v30, v31\n" \
  "v_mfma_f32_16x16x32_bf16 v[16:19], a[80:83], v[40:43], v[16:19]\n" \
  "ds_read_b128 v[36:39], v2 offset:17408\n" \
  "v_add_f32 v64, v24, v25\n" \
  "v_mfma_f32_16x16x32_bf16 v[20:23], a[84:87], v[40:43], v[20:23]\n" \
  "buffer_load_dword v6, s[36:39], s68 offen offset:0 sc1 lds\n" \
  "v_mfma_f32_16x16x32_bf16 v[16:19], a[88:91], v[44:47], v[16:19]\n" \
  "s_waitcnt lgkmcnt(1)\n" \
  "ds_read_b128 v[40:43], v2 offset:18432\n" \
  "v_mul_f32 v65, v24, v24\n" \
  "v_mfma_f32_16x16x32_bf16 v[20:23], a[92:95], v[44:47], v[20:23]\n" \
  "v_cmp_eq_u32 vcc, s60, v15\n" \
  "v_fma_f32 v65, v25, v25, v65\n" \
  "v_add_f32 v64, v64, v26\n" \
  "v_fma_f32 v65, v26, v26, v65\n" \
  "v_mfma_f32_16x16x32_bf16 v[16:19], a[96:99], v[48:51], v[16:19]\n" \
  "s_mov_b32 m0, s62\n" \
  "ds_read_b128 v[44:47], v2 offset:19456\n" \
  "v_add_f32 v64, v64, v27\n" \
  "v_mfma_f32_16x16x32_bf16 v[20:23], a[100:103], v[48:51], v[20:23]\n" \
  "s_cselect_b32 s87, vcc_lo, vcc_hi\n" \
  "v_fma_f32 v65, v27, v27, v65\n" \
  "v_add_f32 v64, v64, v28\n" \
  "v_mfma_f32_16x16x32_bf16 v[16:19], a[104:107], v[52:55], v[16:19]\n" \
  "s_cmp_eq_u32 s87, -1\n" \
  "ds_read_b128 v[48:51], v2 offset:20480\n" \
  "v_fma_f32 v65, v28, v28, v65\n" \
  "v_mfma_f32_16x16x32_bf16 v[20:23], a[108:111], v[52:55], v[20:23]\n" \
  "s_cselect_b32 s67, s86, s59\n" \
  "v_add_f32 v64, v64, v29\n" \
  "v_fma_f32 v65, v29, v29, v65\n" \
  "v_mfma_f32_16x16x32_bf16 v[16:19], a[112:115], v[56:59], v[16:19]\n" \
  "s_xor_b32 s63, s63, 65536\n" \
  "ds_read_b128 v[52:55], v2 offset:21504\n" \
  "ds_read_b128 v[80:83], v8 offset:0\n" \
  "v_mfma_f32_16x16x32_bf16 v[20:23], a[116:119], v[56:59], v[20:23]\n" \
  "buffer_load_dwordx4 v0, s[36:39], s67 offen offset:0 sc1 lds\n" \
  "v_mfma_f32_16x16x32_bf16 v[16:19], a[120:123], v[60:63], v[16:19]\n" \
  "s_add_u32 s53, s52, s55\n" \
  "ds_read_b128 v[56:59], v2 offset:22528\n" \
  "ds_read_b64 v[88:89], v9 offset:1024\n" \
  "v_mfma_f32_16x16x32_bf16 v[20:23], a[124:127], v[60:63], v[20:23]\n" \
  "s_waitcnt lgkmcnt(5)\n" \
  "v_add_f32 v64, v64, v30\n" \
  "v_add_f32 v64, v64, v31\n" \
  "v_mfma_f32_16x16x32_bf16 v[16:19], a[128:131], v[32:35], v[16:19]\n" \
  "s_cmp_eq_u32 s53, s56\n" \
  "ds_read_b128 v[60:63], v2 offset:23552\n" \
  "v_fma_f32 v65, v30, v30, v65\n" \
  "v_mfma_f32_16x16x32_bf16 v[20:23], a[132:135], v[32:35], v[20:23]\n" \
  "buffer_load_dwordx4 v0, s[36:39], s67 offen offset:1024 sc1 lds\n" \
  "v_mfma_f32_16x16x32_bf16 v[16:19], a[136:139], v[36:39], v[16:19]\n" \
  "s_cselect_b32 s53, s57, s53\n" \
  "ds_read_b128 v[32:35], v2 offset:24576\n" \
  "v_fma_f32 v65, v31, v31, v65\n" \
  "v_mfma_f32_16x16x32_bf16 v[20:23], a[140:143], v[36:39], v[20:23]\n" \
  "s_cmp_eq_u32 s53, s58\n" \
  "v_permlane16_swap_b32 v64, v65\n" \
  "v_mfma_f32_16x16x32_bf16 v[16:19], a[144:147], v[40:43], v[16:19]\n" \
  "s_waitcnt lgkmcnt(2)\n" \
  "ds_read_b128 v[36:39], v2 offset:25600\n" \
  "v_add_f32 v64, v64, v65\n" \
  "v_mfma_f32_16x16x32_bf16 v[20:23], a[148:151], v[40:43], v[20:23]\n" \
  "buffer_load_dwordx4 v0, s[36:39], s67 offen offset:2048 sc1 lds\n" \
  "v_mfma_f32_16x16x32_bf16 v[16:19], a[152:155], v[44:47], v[16:19]\n" \
  "s_cselect_b32 s53, s59, s53\n" \
  "ds_read_b128 v[40:43], v2 offset:26624\n" \
  "v_mov_b32 v65, v64\n" \
  "v_mfma_f32_16x16x32_bf16 v[20:23], a[156:159], v[44:47], v[20:23]\n" \
  "s_waitcnt lgkmcnt(3)\n" \
  "v_permlane32_swap_b32 v64, v65\n" \
  "v_mfma_f32_16x16x32_bf16 v[16:19], a[160:163], v[48:51], v[16:19]\n" \
  "ds_read_b128 v[44:47], v2 offset:27648\n" \
  "v_add_f32 v64, v64, v65\n" \
  "v_mfma_f32_16x16x32_bf16 v[20:23], a[164:167], v[48:51], v[20:23]\n" \
  "buffer_load_dwordx4 v0, s[36:39], s67 offen offset:3072 sc1 lds\n" \
  "v_mfma_f32_16x16x32_bf16 v[16:19], a[168:171], v[52:55], v[16:19]\n" \
  "s_add_u32 m0, s62, 16384\n" \
  "ds_read_b128 v[48:51], v2 offset:28672\n" \
  "s_lshr_b32 s72, s67, 6\n" \
  "v_mfma_f32_16x16x32_bf16 v[20:23], a[172:175], v[52:55], v[20:23]\n" \
  "s_lshr_b32 s71, s62, 2\n" \
  "ds_write_b32 v13, v64 offset:0\n" \
  "v_mfma_f32_16x16x32_bf16 v[16:19], a[176:179], v[56:59], v[16:19]\n" \
  "ds_read_b128 v[52:55], v2 offset:29696\n" \
  "v_mfma_f32_16x16x32_bf16 v[20:23], a[180:183], v[56:59], v[20:23]\n" \
  "buffer_store_dwordx4 v[72:75], v3, s[36:39], s73 offen offset:0 sc0 sc1\n" \
  "buffer_load_dwordx4 v1, s[36:39], s67 offen offset:0 sc1 lds\n" \
  "v_mfma_f32_16x16x32_bf16 v[16:19], a[184:187], v[60:63], v[16:19]\n" \
  "ds_read_b128 v[56:59], v2 offset:30720\n" \
  "v_mfma_f32_16x16x32_bf16 v[20:23], a[188:191], v[60:63], v[20:23]\n" \
  "s_waitcnt lgkmcnt(4)\n" \
  "s_waitcnt lgkmcnt(2)\n" \
  "v_mfma_f32_16x16x32_bf16 v[16:19], a[192:195], v[32:35], v[16:19]\n" \
  "s_waitcnt vmcnt(16)\n" \
  "ds_read_b128 v[60:63], v2 offset:31744\n" \
  "v_mfma_f32_16x16x32_bf16 v[20:23], a[196:199], v[32:35], v[20:23]\n" \
  "s_barrier\n" \
  "buffer_load_dwordx4 v1, s[36:39], s67 offen offset:1024 sc1 lds\n" \
  "v_mfma_f32_16x16x32_bf16 v[16:19], a[200:203], v[36:39], v[16:19]\n" \
  "ds_read_b128 v[32:35], v2 offset:32768\n" \
  "ds_read_b128 v[76:79], v14 offset:0\n" \
  "v_mfma_f32_16x16x32_bf16 v[20:23], a[204:207], v[36:39], v[20:23]\n" \
  "v_mfma_f32_16x16x32_bf16 v[16:19], a[208:211], v[40:43], v[16:19]\n" \
  "ds_read_b128 v[36:39], v2 offset:33792\n" \
  "v_mfma_f32_16x16x32_bf16 v[20:23], a[212:215], v[40:43], v[20:23]\n" \
  "buffer_load_dwordx4 v1, s[36:39], s67 offen offset:2048 sc1 lds\n" \
  "v_mfma_f32_16x16x32_bf16 v[16:19], a[216:219], v[44:47], v[16:19]\n" \
  "s_waitcnt lgkmcnt(1)\n" \
  "ds_read_b128 v[40:43], v2 offset:34816\n" \
  "v_add_f32 v76, v76, v77\n" \
  "v_mfma_f32_16x16x32_bf16 v[20:23], a[220:223], v[44:47], v[20:23]\n" \
  "v_add_f32 v78, v78, v79\n" \
  "v_add_f32 v76, v76, v78\n" \
  "v_mfma_f32_16x16x32_bf16 v[16:19], a[224:227], v[48:51], v[16:19]\n" \
  "ds_read_b128 v[44:47], v2 offset:35840\n" \
  "v_mfma_f32_16x16x32_bf16 v[20:23], a[228:231], v[48:51], v[20:23]\n" \
  "buffer_store_dword v76, v12, s[36:39], s85 offen offset:0 sc0 sc1\n" \
  "buffer_load_dwordx4 v1, s[36:39], s67 offen offset:3072 sc1 lds\n" \
  "s_add_u32 m0, s71, s88\n" \
  "v_mfma_f32_16x16x32_bf16 v[16:19], a[232:235], v[52:55], v[16:19]\n" \
  "s_cmp_ge_u32 s66, s59\n" \
  "ds_read_b128 v[48:51], v2 offset:36864\n" \
  "v_mfma_f32_16x16x32_bf16 v[20:23], a[236:239], v[52:55], v[20:23]\n" \
  "v_mfma_f32_16x16x32_bf16 v[16:19], a[240:243], v[56:59], v[16:19]\n" \
  "ds_read_b128 v[52:55], v2 offset:37888\n" \
  "v_mfma_f32_16x16x32_bf16 v[20:23], a[244:247], v[56:59], v[20:23]\n" \
  "buffer_load_dwordx4 v10, s[36:39], s67 offen offset:0 sc1 lds\n" \
  "v_mfma_f32_16x16x32_bf16 v[16:19], a[248:251], v[60:63], v[16:19]\n" \
  "s_waitcnt lgkmcnt(2)\n" \
  "ds_read_b128 v[56:59], v2 offset:38912\n" \
  "buffer_load_dword v11, s[36:39], s72 offen offset:1024 sc1 lds\n" \
  "v_mfma_f32_16x16x32_bf16 v[20:23], a[252:255], v[60:63], v[20:23]\n" \
  "s_cbranch_scc1 Lg2_slow1_%=\n" \
  "Lg2_d1_%=:\n" \
  "v_mfma_f32_16x16x32_bf16 v[24:27], a[0:3], v[32:35], 0\n" \
  "s_lshr_b32 s73, s48, 0\n" \
  "ds_read_b128 v[60:63], v2 offset:39936\n" \
  "v_lshlrev_b32 v64, 16, v80\n" \
  "v_mfma_f32_16x16x32_bf16 v[28:31], a[4:7], v[32:35], 0\n" \
  "s_lshr_b32 s85, s48, 5\n" \
  "v_and_b32 v65, 0xffff0000, v80\n" \
  "v_lshlrev_b32 v66, 16, v81\n" \
  "v_mfma_f32_16x16x32_bf16 v[24:27], a[8:11], v[36:39], v[24:27]\n" \
  "s_cmp_eq_u32 s54, 1\n" \
  "ds_read_b128 v[32:35], v2 offset:40960\n" \
  "v_and_b32 v67, 0xffff0000, v81\n" \
  "v_mfma_f32_16x16x32_bf16 v[28:31], a[12:15], v[36:39], v[28:31]\n" \
  "s_cselect_b32 s68, s52, s51\n" \
  "v_lshlrev_b32 v68, 16, v82\n" \
  "v_and_b32 v69, 0xffff0000, v82\n" \
  "v_mfma_f32_16x16x32_bf16 v[24:27], a[16:19], v[40:43], v[24:27]\n" \
  "s_lshr_b32 s68, s68, 5\n" \
  "ds_read_b128 v[36:39], v2 offset:41984\n" \
  "v_lshlrev_b32 v70, 16, v83\n" \
  "v_mfma_f32_16x16x32_bf16 v[28:31], a[20:23], v[40:43], v[28:31]\n" \
  "s_lshr_b32 s70, s46, 5\n" \
  "v_and_b32 v71, 0xffff0000, v83\n" \
  "v_fma_f32 v64, v64, v89, v88\n" \
  "v_mfma_f32_16x16x32_bf16 v[24:27], a[24:27], v[44:47], v[24:27]\n" \
  "s_add_u32 m0, s78, 256\n" \
  "ds_read_b128 v[40:43], v2 offset:43008\n" \
  "v_fma_f32 v65, v65, v89, v88\n" \
  "v_mfma_f32_16x16x32_bf16 v[28:31], a[28:31], v[44:47], v[28:31]\n" \
  "s_waitcnt lgkmcnt(3)\n" \
  "v_fma_f32 v66, v66, v89, v88\n" \
  "v_fma_f32 v67, v67, v89, v88\n" \
  "v_mfma_f32_16x16x32_bf16 v[24:27], a[32:35], v[48:51], v[24:27]\n" \
  "s_xor_b32 s69, s63, 65536\n" \
  "ds_read_b128 v[44:47], v2 offset:44032\n" \
  "v_fma_f32 v68, v68, v89, v88\n" \
  "v_mfma_f32_16x16x32_bf16 v[28:31], a[36:39], v[48:51], v[28:31]\n" \
  "s_cmp_ge_u32 s67, s59\n" \
  "v_fma_f32 v69, v69, v89, v88\n" \
  "v_fma_f32 v70, v70, v89, v88\n" \
  "v_mfma_f32_16x16x32_bf16 v[24:27], a[40:43], v[52:55], v[24:27]\n" \
  "s_cselect_b32 s62, s69, s64\n" \
  "ds_read_b128 v[48:51], v2 offset:45056\n" \
  "v_fma_f32 v71, v71, v89, v88\n" \
  "v_mfma_f32_16x16x32_bf16 v[28:31], a[44:47], v[52:55], v[28:31]\n" \
  "s_cselect_b32 s86, s50, s51\n" \
  "v_add_f32 v16, v16, v64\n" \
  "v_add_f32 v17, v17, v65\n" \
  "v_mfma_f32_16x16x32_bf16 v[24:27], a[48:51], v[56:59], v[24:27]\n" \
  "s_mov_b64 s[44:45], s[46:47]\n" \
  "ds_read_b128 v[52:55], v2 offset:46080\n" \
  "v_add_f32 v18, v18, v66\n" \
  "v_mfma_f32_16x16x32_bf16 v[28:31], a[52:55], v[56:59], v[28:31]\n" \
  "s_mov_b64 s[46:47], s[48:49]\n" \
  "v_add_f32 v19, v19, v67\n" \
  "v_add_f32 v20, v20, v68\n" \
  "v_mfma_f32_16x16x32_bf16 v[24:27], a[56:59], v[60:63], v[24:27]\n" \
  "s_mov_b64 s[48:49], s[50:51]\n" \
  "ds_read_b128 v[56:59], v2 offset:47104\n" \
  "v_add_f32 v21, v21, v69\n" \
  "v_mfma_f32_16x16x32_bf16 v[28:31], a[60:63], v[60:63], v[28:31]\n" \
  "s_waitcnt lgkmcnt(3)\n" \
  "v_add_f32 v22, v22, v70\n" \
  "v_add_f32 v23, v23, v71\n" \
  "v_mfma_f32_16x16x32_bf16 v[24:27], a[64:67], v[32:35], v[24:27]\n" \
  "s_waitcnt vmcnt(12)\n" \
  "ds_read_b128 v[60:63], v2 offset:48128\n" \
  "v_cvt_pk_bf16_f32 v72, v16, v17\n" \
  "v_mfma_f32_16x16x32_bf16 v[28:31], a[68:71], v[32:35], v[28:31]\n" \
  "s_barrier\n" \
  "v_mfma_f32_16x16x32_bf16 v[24:27], a[72:75], v[36:39], v[24:27]\n" \
  "s_mov_b64 s[50:51], s[52:53]\n" \
  "ds_read_b128 v[32:35], v2 offset:49152\n" \
  "ds_read_b32 v15, v7 offset:0\n" \
  "v_mfma_f32_16x16x32_bf16 v[28:31], a[76:79], v[36:39], v[28:31]\n" \
  "buffer_store_dword v5, v4, s[36:39], s70 offen offset:0 sc0 sc1\n" \
  "v_cvt_pk_bf16_f32 v73, v18, v19\n" \
  "v_cvt_pk_bf16_f32 v74, v20, v21\n" \
  "v_cvt_pk_bf16_f32 v75, v22, v23\n" \
  "v_mfma_f32_16x16x32_bf16 v[24:27], a[80:83], v[40:43], v[24:27]\n" \
  "ds_read_b128 v[36:39], v2 offset:50176\n" \
  "v_add_f32 v64, v16, v17\n" \
  "v_mfma_f32_16x16x32_bf16 v[28:31], a[84:87], v[40:43], v[28:31]\n" \
  "buffer_load_dword v6, s[36:39], s68 offen offset:0 sc1 lds\n" \
  "v_mfma_f32_16x16x32_bf16 v[24:27], a[88:91], v[44:47], v[24:27]\n" \
  "s_waitcnt lgkmcnt(1)\n" \
  "ds_read_b128 v[40:43], v2 offset:51200\n" \
  "v_mul_f32 v65, v16, v16\n" \
  "v_mfma_f32_16x16x32_bf16 v[28:31], a[92:95], v[44:47], v[28:31]\n" \
  "v_cmp_eq_u32 vcc, s60, v15\n" \
  "v_fma_f32 v65, v17, v17, v65\n" \
  "v_add_f32 v64, v64, v18\n" \
  "v_fma_f32 v65, v18, v18, v65\n" \
  "v_mfma_f32_16x16x32_bf16 v[24:27], a[96:99], v[48:51], v[24:27]\n" \
  "s_mov_b32 m0, s62\n" \
  "ds_read_b128 v[44:47], v2 offset:52224\n" \
  "v_add_f32 v64, v64, v19\n" \
  "v_mfma_f32_16x16x32_bf16 v[28:31], a[100:103], v[48:51], v[28:31]\n" \
  "s_cselect_b32 s87, vcc_lo, vcc_hi\n" \
  "v_fma_f32 v65, v19, v19, v65\n" \
  "v_add_f32 v64, v64, v20\n" \
  "v_mfma_f32_16x16x32_bf16 v[24:27], a[104:107], v[52:55], v[24:27]\n" \
  "s_cmp_eq_u32 s87, -1\n" \
  "ds_read_b128 v[48:51], v2 offset:53248\n" \
  "v_fma_f32 v65, v20, v20, v65\n" \
  "v_mfma_f32_16x16x32_bf16 v[28:31], a[108:111], v[52:55], v[28:31]\n" \
  "s_cselect_b32 s66, s86, s59\n" \
  "v_add_f32 v64, v64, v21\n" \
  "v_fma_f32 v65, v21, v21, v65\n" \
  "v_mfma_f32_16x16x32_bf16 v[24:27], a[112:115], v[56:59], v[24:27]\n" \
  "s_xor_b32 s64, s64, 65536\n" \
  "ds_read_b128 v[52:55], v2 offset:54272\n" \
  "ds_read_b128 v[84:87], v8 offset:8192\n" \
  "v_mfma_f32_16x16x32_bf16 v[28:31], a[116:119], v[56:59], v[28:31]\n" \
  "buffer_load_dwordx4 v0, s[36:39], s66 offen offset:0 sc1 lds\n" \
  "v_mfma_f32_16x16x32_bf16 v[24:27], a[120:123], v[60:63], v[24:27]\n" \
  "s_add_u32 s74, s53, s55\n" \
  "ds_read_b128 v[56:59], v2 offset:55296\n" \
  "ds_read_b64 v[90:91], v9 offset:9216\n" \
  "v_mfma_f32_16x16x32_bf16 v[28:31], a[124:127], v[60:63], v[28:31]\n" \
  "s_waitcnt lgkmcnt(5)\n" \
  "v_add_f32 v64, v64, v22\n" \
  "v_add_f32 v64, v64, v23\n" \
  "v_mfma_f32_16x16x32_bf16 v[24:27], a[128:131], v[32:35], v[24:27]\n" \
  "s_cmp_eq_u32 s74, s56\n" \
  "ds_read_b128 v[60:63], v2 offset:56320\n" \
  "v_fma_f32 v65, v22, v22, v65\n" \
  "v_mfma_f32_16x16x32_bf16 v[28:31], a[132:135], v[32:35], v[28:31]\n" \
  "buffer_load_dwordx4 v0, s[36:39], s66 offen offset:1024 sc1 lds\n" \
  "v_mfma_f32_16x16x32_bf16 v[24:27], a[136:139], v[36:39], v[24:27]\n" \
  "s_cselect_b32 s74, s57, s74\n" \
  "ds_read_b128 v[32:35], v2 offset:57344\n" \
  "v_fma_f32 v65, v23, v23, v65\n" \
  "v_mfma_f32_16x16x32_bf16 v[28:31], a[140:143], v[36:39], v[28:31]\n" \
  "s_cmp_eq_u32 s74, s58\n" \
  "v_permlane16_swap_b32 v64, v65\n" \
  "v_mfma_f32_16x16x32_bf16 v[24:27], a[144:147], v[40:43], v[24:27]\n" \
  "s_waitcnt lgkmcnt(2)\n" \
  "ds_read_b128 v[36:39], v2 offset:58368\n" \
  "v_add_f32 v64, v64, v65\n" \
  "v_mfma_f32_16x16x32_bf16 v[28:31], a[148:151], v[40:43], v[28:31]\n" \
  "buffer_load_dwordx4 v0, s[36:39], s66 offen offset:2048 sc1 lds\n" \
  "v_mfma_f32_16x16x32_bf16 v[24:27], a[152:155], v[44:47], v[24:27]\n" \
  "s_cselect_b32 s74, s59, s74\n" \
  "ds_read_b128 v[40:43], v2 offset:59392\n" \
  "v_mov_b32 v65, v64\n" \
  "v_mfma_f32_16x16x32_bf16 v[28:31], a[156:159], v[44:47], v[28:31]\n" \
  "s_waitcnt lgkmcnt(3)\n" \
  "v_permlane32_swap_b32 v64, v65\n" \
  "v_mfma_f32_16x16x32_bf16 v[24:27], a[160:163], v[48:51], v[24:27]\n" \
  "s_mov_b32 s52, s74\n" \
  "ds_read_b128 v[44:47], v2 offset:60416\n" \
  "v_add_f32 v64, v64, v65\n" \
  "v_mfma_f32_16x16x32_bf16 v[28:31], a[164:167], v[48:51], v[28:31]\n" \
  "buffer_load_dwordx4 v0, s[36:39], s66 offen offset:3072 sc1 lds\n" \
  "v_mfma_f32_16x16x32_bf16 v[24:27], a[168:171], v[52:55], v[24:27]\n" \
  "s_add_u32 m0, s62, 16384\n" \
  "ds_read_b128 v[48:51], v2 offset:61440\n" \
  "v_xor_b32 v8, 16384, v8\n" \
  "v_mfma_f32_16x16x32_bf16 v[28:31], a[172:175], v[52:55], v[28:31]\n" \
  "s_lshr_b32 s72, s66, 6\n" \
  "ds_write_b32 v13, v64 offset:2048\n" \
  "v_mfma_f32_16x16x32_bf16 v[24:27], a[176:179], v[56:59], v[24:27]\n" \
  "s_lshr_b32 s71, s62, 2\n" \
  "ds_read_b128 v[52:55], v2 offset:62464\n" \
  "v_xor_b32 v9, 16384, v9\n" \
  "v_mfma_f32_16x16x32_bf16 v[28:31], a[180:183], v[56:59], v[28:31]\n" \
  "buffer_store_dwordx4 v[72:75], v3, s[36:39], s73 offen offset:0 sc0 sc1\n" \
  "buffer_load_dwordx4 v1, s[36:39], s66 offen offset:0 sc1 lds\n" \
  "v_mfma_f32_16x16x32_bf16 v[24:27], a[184:187], v[60:63], v[24:27]\n" \
  "ds_read_b128 v[56:59], v2 offset:63488\n" \
  "v_mfma_f32_16x16x32_bf16 v[28:31], a[188:191], v[60:63], v[28:31]\n" \
  "s_waitcnt lgkmcnt(4)\n" \
  "s_waitcnt lgkmcnt(2)\n" \
  "v_mfma_f32_16x16x32_bf16 v[24:27], a[192:195], v[32:35], v[24:27]\n" \
  "s_waitcnt vmcnt(16)\n" \
  "ds_read_b128 v[60:63], v2 offset:64512\n" \
  "v_xor_b32 v2, 65536, v2\n" \
  "v_mfma_f32_16x16x32_bf16 v[28:31], a[196:199], v[32:35], v[28:31]\n" \
  "s_barrier\n" \
  "buffer_load_dwordx4 v1, s[36:39], s66 offen offset:1024 sc1 lds\n" \
  "v_mfma_f32_16x16x32_bf16 v[24:27], a[200:203], v[36:39], v[24:27]\n" \
  "ds_read_b128 v[32:35], v2 offset:0\n" \
  "ds_read_b128 v[76:79], v14 offset:2048\n" \
  "v_mfma_f32_16x16x32_bf16 v[28:31], a[204:207], v[36:39], v[28:31]\n" \
  "v_mfma_f32_16x16x32_bf16 v[24:27], a[208:211], v[40:43], v[24:27]\n" \
  "ds_read_b128 v[36:39], v2 offset:1024\n" \
  "v_mfma_f32_16x16x32_bf16 v[28:31], a[212:215], v[40:43], v[28:31]\n" \
  "buffer_load_dwordx4 v1, s[36:39], s66 offen offset:2048 sc1 lds\n" \
  "v_mfma_f32_16x16x32_bf16 v[24:27], a[216:219], v[44:47], v[24:27]\n" \
  "s_waitcnt lgkmcnt(1)\n" \
  "ds_read_b128 v[40:43], v2 offset:2048\n" \
  "v_add_f32 v76, v76, v77\n" \
  "v_mfma_f32_16x16x32_bf16 v[28:31], a[220:223], v[44:47], v[28:31]\n" \
  "v_add_f32 v78, v78, v79\n" \
  "v_add_f32 v76, v76, v78\n" \
  "v_mfma_f32_16x16x32_bf16 v[24:27], a[224:227], v[48:51], v[24:27]\n" \
  "ds_read_b128 v[44:47], v2 offset:3072\n" \
  "v_mfma_f32_16x16x32_bf16 v[28:31], a[228:231], v[48:51], v[28:31]\n" \
  "buffer_store_dword v76, v12, s[36:39], s85 offen offset:0 sc0 sc1\n" \
  "buffer_load_dwordx4 v1, s[36:39], s66 offen offset:3072 sc1 lds\n" \
  "s_add_u32 m0, s71, s88\n" \
  "v_mfma_f32_16x16x32_bf16 v[24:27], a[232:235], v[52:55], v[24:27]\n" \
  "s_cmp_lt_u32 s67, s59\n" \
  "ds_read_b128 v[48:51], v2 offset:4096\n" \
  "v_mfma_f32_16x16x32_bf16 v[28:31], a[236:239], v[52:55], v[28:31]\n" \
  "v_mfma_f32_16x16x32_bf16 v[24:27], a[240:243], v[56:59], v[24:27]\n" \
  "ds_read_b128 v[52:55], v2 offset:5120\n" \
  "v_mfma_f32_16x16x32_bf16 v[28:31], a[244:247], v[56:59], v[28:31]\n" \
  "buffer_load_dwordx4 v10, s[36:39], s66 offen offset:0 sc1 lds\n" \
  "v_mfma_f32_16x16x32_bf16 v[24:27], a[248:251], v[60:63], v[24:27]\n" \
  "s_waitcnt lgkmcnt(2)\n" \
  "ds_read_b128 v[56:59], v2 offset:6144\n" \
  "buffer_load_dword v11, s[36:39], s72 offen offset:1024 sc1 lds\n" \
  "v_mfma_f32_16x16x32_bf16 v[28:31], a[252:255], v[60:63], v[28:31]\n" \
  "s_cbranch_scc1 Lg2_d0_%=\n" \
  "Lg2_slow0_%=:\n" \
  "s_cmp_eq_u32 s54, 1\n" \
  "s_cselect_b32 s68, s49, s48\n" \
  "s_lshr_b32 s68, s68, 5\n" \
  "s_add_u32 m0, s78, 8192\n" \
  "s_nop 0\n" \
  "buffer_load_dword v6, s[36:39], s68 offen offset:0 sc1 lds\n" \
  "s_lshr_b32 s73, s47, 0\n" \
  "s_lshr_b32 s85, s47, 5\n" \
  "v_lshlrev_b32 v64, 16, v84\n" \
  "v_and_b32 v65, 0xffff0000, v84\n" \
  "v_lshlrev_b32 v66, 16, v85\n" \
  "v_and_b32 v67, 0xffff0000, v85\n" \
  "v_lshlrev_b32 v68, 16, v86\n" \
  "v_and_b32 v69, 0xffff0000, v86\n" \
  "v_lshlrev_b32 v70, 16, v87\n" \
  "v_and_b32 v71, 0xffff0000, v87\n" \
  "v_fma_f32 v64, v64, v91, v90\n" \
  "v_fma_f32 v65, v65, v91, v90\n" \
  "v_fma_f32 v66, v66, v91, v90\n" \
  "v_fma_f32 v67, v67, v91, v90\n" \
  "v_fma_f32 v68, v68, v91, v90\n" \
  "v_fma_f32 v69, v69, v91, v90\n" \
  "v_fma_f32 v70, v70, v91, v90\n" \
  "v_fma_f32 v71, v71, v91, v90\n" \
  "v_add_f32 v24, v24, v64\n" \
  "v_add_f32 v25, v25, v65\n" \
  "v_add_f32 v26, v26, v66\n" \
  "v_add_f32 v27, v27, v67\n" \
  "v_add_f32 v28, v28, v68\n" \
  "v_add_f32 v29, v29, v69\n" \
  "v_add_f32 v30, v30, v70\n" \
  "v_add_f32 v31, v31, v71\n" \
  "v_cvt_pk_bf16_f32 v72, v24, v25\n" \
  "v_cvt_pk_bf16_f32 v73, v26, v27\n" \
  "v_cvt_pk_bf16_f32 v74, v28, v29\n" \
  "v_cvt_pk_bf16_f32 v75, v30, v31\n" \
  "s_add_u32 m0, s78, 8448\n" \
  "s_nop 0\n" \
  "buffer_load_dword v6, s[36:39], s68 offen offset:0 sc1 lds\n" \
  "buffer_store_dwordx4 v[72:75], v3, s[36:39], s73 offen offset:0 sc0 sc1\n" \
  "v_add_f32 v64, v24, v25\n" \
  "v_mul_f32 v65, v24, v24\n" \
  "v_fma_f32 v65, v25, v25, v65\n" \
  "v_add_f32 v64, v64, v26\n" \
  "v_fma_f32 v65, v26, v26, v65\n" \
  "v_add_f32 v64, v64, v27\n" \
  "v_fma_f32 v65, v27, v27, v65\n" \
  "v_add_f32 v64, v64, v28\n" \
  "v_fma_f32 v65, v28, v28, v65\n" \
  "v_add_f32 v64, v64, v29\n" \
  "v_fma_f32 v65, v29, v29, v65\n" \
  "v_add_f32 v64, v64, v30\n" \
  "v_fma_f32 v65, v30, v30, v65\n" \
  "v_add_f32 v64, v64, v31\n" \
  "v_fma_f32 v65, v31, v31, v65\n" \
  "s_nop 1\n" \
  "v_permlane16_swap_b32 v64, v65\n" \
  "v_add_f32 v64, v64, v65\n" \
  "v_mov_b32 v65, v64\n" \
  "s_nop 1\n" \
  "v_permlane32_swap_b32 v64, v65\n" \
  "v_add_f32 v64, v64, v65\n" \
  "ds_write_b32 v13, v64 offset:0\n" \
  "s_waitcnt lgkmcnt(0)\n" \
  "s_barrier\n" \
  "ds_read_b128 v[76:79], v14 offset:0\n" \
  "s_waitcnt lgkmcnt(0)\n" \
  "v_add_f32 v76, v76, v77\n" \
  "v_add_f32 v78, v78, v79\n" \
  "v_add_f32 v76, v76, v78\n" \
  "buffer_store_dword v76, v12, s[36:39], s85 offen offset:0 sc0 sc1\n" \
  "s_cmp_ge_u32 s48, s59\n" \
  "s_cbranch_scc1 Lg2_done0_%=\n" \
  "s_add_u32 m0, s78, 16384\n" \
  "s_nop 0\n" \
  "buffer_load_dword v6, s[36:39], s68 offen offset:0 sc1 lds\n" \
  "s_waitcnt vmcnt(4)\n" \
  "s_barrier\n" \
  "ds_read_b32 v15, v7 offset:8192\n" \
  "s_lshr_b32 s70, s45, 5\n" \
  "buffer_store_dword v5, v4, s[36:39], s70 offen offset:0 sc0 sc1\n" \
  "s_mov_b32 s45, s59\n" \
  "s_lshr_b32 s70, s46, 5\n" \
  "buffer_store_dword v5, v4, s[36:39], s70 offen offset:0 sc0 sc1\n" \
  "s_mov_b32 s46, s59\n" \
  "s_mov_b32 s69, 18432\n" \
  "s_waitcnt lgkmcnt(0)\n" \
  "v_cmp_eq_u32 vcc, s60, v15\n" \
  "s_nop 0\n" \
  "s_cmp_eq_u32 vcc_lo, -1\n" \
  "s_cbranch_scc1 Lg2_go0_%=\n" \
  "s_add_u32 m0, s78, 16640\n" \
  "s_nop 0\n" \
  "buffer_load_dword v6, s[36:39], s68 offen offset:0 sc1 lds\n" \
  "s_waitcnt vmcnt(6)\n" \
  "s_barrier\n" \
  "ds_read_b32 v15, v7 offset:8448\n" \
  "s_mov_b32 s69, 18688\n" \
  "s_waitcnt lgkmcnt(0)\n" \
  "v_cmp_eq_u32 vcc, s60, v15\n" \
  "s_nop 0\n" \
  "s_cmp_eq_u32 vcc_lo, -1\n" \
  "s_cbranch_scc1 Lg2_go0_%=\n" \
  "s_add_u32 m0, s78, 8192\n" \
  "s_nop 0\n" \
  "buffer_load_dword v6, s[36:39], s68 offen offset:0 sc1 lds\n" \
  "s_waitcnt vmcnt(4)\n" \
  "s_barrier\n" \
  "ds_read_b32 v15, v7 offset:16384\n" \
  "s_lshr_b32 s70, s47, 5\n" \
  "buffer_store_dword v5, v4, s[36:39], s70 offen offset:0 sc0 sc1\n" \
  "s_mov_b32 s47, s59\n" \
  "s_mov_b32 s69, 10240\n" \
  "s_waitcnt lgkmcnt(0)\n" \
  "v_cmp_eq_u32 vcc, s60, v15\n" \
  "s_nop 0\n" \
  "s_cmp_eq_u32 vcc_lo, -1\n" \
  "s_cbranch_scc1 Lg2_go0_%=\n" \
  "s_add_u32 m0, s78, 8448\n" \
  "s_nop 0\n" \
  "buffer_load_dword v6, s[36:39], s68 offen offset:0 sc1 lds\n" \
  "s_waitcnt vmcnt(3)\n" \
  "s_barrier\n" \
  "ds_read_b32 v15, v7 offset:16640\n" \
  "s_mov_b32 s69, 10496\n" \
  "s_waitcnt lgkmcnt(0)\n" \
  "v_cmp_eq_u32 vcc, s60, v15\n" \
  "s_nop 0\n" \
  "s_cmp_eq_u32 vcc_lo, -1\n" \
  "s_cbranch_scc1 Lg2_go0_%=\n" \
  "Lg2_poll0_%=:\n" \
  "s_add_u32 m0, s78, 16384\n" \
  "s_nop 0\n" \
  "buffer_load_dword v6, s[36:39], s68 offen offset:0 sc1 lds\n" \
  "s_waitcnt vmcnt(2)\n" \
  "s_barrier\n" \
  "ds_read_b32 v15, v7 offset:8192\n" \
  "s_mov_b32 s69, 18432\n" \
  "s_waitcnt lgkmcnt(0)\n" \
  "v_cmp_eq_u32 vcc, s60, v15\n" \
  "s_nop 0\n" \
  "s_cmp_eq_u32 vcc_lo, -1\n" \
  "s_cbranch_scc1 Lg2_go0_%=\n" \
  "s_add_u32 m0, s78, 16640\n" \
  "s_nop 0\n" \
  "buffer_load_dword v6, s[36:39], s68 offen offset:0 sc1 lds\n" \
  "s_waitcnt vmcnt(2)\n" \
  "s_barrier\n" \
  "ds_read_b32 v15, v7 offset:8448\n" \
  "s_mov_b32 s69, 18688\n" \
  "s_waitcnt lgkmcnt(0)\n" \
  "v_cmp_eq_u32 vcc, s60, v15\n" \
  "s_nop 0\n" \
  "s_cmp_eq_u32 vcc_lo, -1\n" \
  "s_cbranch_scc1 Lg2_go0_%=\n" \
  "s_add_u32 m0, s78, 8192\n" \
  "s_nop 0\n" \
  "buffer_load_dword v6, s[36:39], s68 offen offset:0 sc1 lds\n" \
  "s_waitcnt vmcnt(2)\n" \
  "s_barrier\n" \
  "ds_read_b32 v15, v7 offset:16384\n" \
  "s_mov_b32 s69, 10240\n" \
  "s_waitcnt lgkmcnt(0)\n" \
  "v_cmp_eq_u32 vcc, s60, v15\n" \
  "s_nop 0\n" \
  "s_cmp_eq_u32 vcc_lo, -1\n" \
  "s_cbranch_scc1 Lg2_go0_%=\n" \
  "s_add_u32 m0, s78, 8448\n" \
  "s_nop 0\n" \
  "buffer_load_dword v6, s[36:39], s68 offen offset:0 sc1 lds\n" \
  "s_waitcnt vmcnt(2)\n" \
  "s_barrier\n" \
  "ds_read_b32 v15, v7 offset:16640\n" \
  "s_mov_b32 s69, 10496\n" \
  "s_waitcnt lgkmcnt(0)\n" \
  "v_cmp_eq_u32 vcc, s60, v15\n" \
  "s_nop 0\n" \
  "s_cmp_eq_u32 vcc_lo, -1\n" \
  "s_cbranch_scc1 Lg2_go0_%=\n" \
  "s_add_u32 s79, s79, 1\n" \
  "s_cmp_lt_u32 s79, 20000\n" \
  "s_cbranch_scc1 Lg2_poll0_%=\n" \
  "Lg2_go0_%=:\n" \
  "s_xor_b32 s62, s63, 65536\n" \
  "s_cmp_lt_u32 s66, s59\n" \
  "s_cbranch_scc1 Lg2_gs0_%=\n" \
  "s_mov_b32 m0, s62\n" \
  "s_nop 0\n" \
  "buffer_load_dwordx4 v0, s[36:39], s48 offen offset:0 sc1 lds\n" \
  "buffer_load_dwordx4 v0, s[36:39], s48 offen offset:1024 sc1 lds\n" \
  "buffer_load_dwordx4 v0, s[36:39], s48 offen offset:2048 sc1 lds\n" \
  "buffer_load_dwordx4 v0, s[36:39], s48 offen offset:3072 sc1 lds\n" \
  "s_add_u32 m0, s62, 16384\n" \
  "s_lshr_b32 s72, s48, 6\n" \
  "s_lshr_b32 s71, s62, 2\n" \
  "buffer_load_dwordx4 v1, s[36:39], s48 offen offset:0 sc1 lds\n" \
  "buffer_load_dwordx4 v1, s[36:39], s48 offen offset:1024 sc1 lds\n" \
  "buffer_load_dwordx4 v1, s[36:39], s48 offen offset:2048 sc1 lds\n" \
  "buffer_load_dwordx4 v1, s[36:39], s48 offen offset:3072 sc1 lds\n" \
  "s_add_u32 m0, s71, s88\n" \
  "s_nop 0\n" \
  "buffer_load_dwordx4 v10, s[36:39], s48 offen offset:0 sc1 lds\n" \
  "buffer_load_dword v11, s[36:39], s72 offen offset:1024 sc1 lds\n" \
  "Lg2_gs0_%=:\n" \
  "s_cmp_eq_u32 s54, 1\n" \
  "s_cselect_b32 s68, s50, s49\n" \
  "s_lshr_b32 s68, s68, 5\n" \
  "s_add_u32 m0, s78, 256\n" \
  "s_nop 0\n" \
  "buffer_load_dword v6, s[36:39], s68 offen offset:0 sc1 lds\n" \
  "s_cmp_eq_u32 vcc_hi, -1\n" \
  "s_cselect_b32 s66, s49, s59\n" \
  "s_xor_b32 s62, s64, 65536\n" \
  "s_mov_b32 m0, s62\n" \
  "s_nop 0\n" \
  "buffer_load_dwordx4 v0, s[36:39], s66 offen offset:0 sc1 lds\n" \
  "buffer_load_dwordx4 v0, s[36:39], s66 offen offset:1024 sc1 lds\n" \
  "buffer_load_dwordx4 v0, s[36:39], s66 offen offset:2048 sc1 lds\n" \
  "buffer_load_dwordx4 v0, s[36:39], s66 offen offset:3072 sc1 lds\n" \
  "s_add_u32 m0, s62, 16384\n" \
  "s_lshr_b32 s72, s66, 6\n" \
  "s_lshr_b32 s71, s62, 2\n" \
  "buffer_load_dwordx4 v1, s[36:39], s66 offen offset:0 sc1 lds\n" \
  "buffer_load_dwordx4 v1, s[36:39], s66 offen offset:1024 sc1 lds\n" \
  "buffer_load_dwordx4 v1, s[36:39], s66 offen offset:2048 sc1 lds\n" \
  "buffer_load_dwordx4 v1, s[36:39], s66 offen offset:3072 sc1 lds\n" \
  "s_add_u32 m0, s71, s88\n" \
  "s_nop 0\n" \
  "buffer_load_dwordx4 v10, s[36:39], s66 offen offset:0 sc1 lds\n" \
  "buffer_load_dword v11, s[36:39], s72 offen offset:1024 sc1 lds\n" \
  "s_waitcnt vmcnt(17)\n" \
  "s_barrier\n" \
  "ds_read_b128 v[32:35], v2 offset:0\n" \
  "ds_read_b128 v[36:39], v2 offset:1024\n" \
  "ds_read_b128 v[40:43], v2 offset:2048\n" \
  "ds_read_b128 v[44:47], v2 offset:3072\n" \
  "ds_read_b128 v[48:51], v2 offset:4096\n" \
  "ds_read_b128 v[52:55], v2 offset:5120\n" \
  "ds_read_b128 v[56:59], v2 offset:6144\n" \
  "s_lshr_b32 s70, s45, 5\n" \
  "buffer_store_dword v5, v4, s[36:39], s70 offen offset:0 sc0 sc1\n" \
  "s_mov_b32 s45, s59\n" \
  "s_lshr_b32 s70, s46, 5\n" \
  "buffer_store_dword v5, v4, s[36:39], s70 offen offset:0 sc0 sc1\n" \
  "s_mov_b32 s46, s59\n" \
  "s_lshr_b32 s70, s47, 5\n" \
  "buffer_store_dword v5, v4, s[36:39], s70 offen offset:0 sc0 sc1\n" \
  "s_mov_b32 s47, s59\n" \
  "s_waitcnt lgkmcnt(3)\n" \
  "s_branch Lg2_d0_%=\n" \
  "Lg2_done0_%=:\n" \
  "s_waitcnt vmcnt(0)\n" \
  "s_barrier\n" \
  "s_lshr_b32 s70, s45, 5\n" \
  "buffer_store_dword v5, v4, s[36:39], s70 offen offset:0 sc0 sc1\n" \
  "s_lshr_b32 s70, s46, 5\n" \
  "buffer_store_dword v5, v4, s[36:39], s70 offen offset:0 sc0 sc1\n" \
  "s_lshr_b32 s70, s47, 5\n" \
  "buffer_store_dword v5, v4, s[36:39], s70 offen offset:0 sc0 sc1\n" \
  "s_branch Lg2_done_%=\n" \
  "Lg2_slow1_%=:\n" \
  "s_cmp_eq_u32 s54, 1\n" \
  "s_cselect_b32 s68, s50, s49\n" \
  "s_lshr_b32 s68, s68, 5\n" \
  "s_add_u32 m0, s78, 8192\n" \
  "s_nop 0\n" \
  "buffer_load_dword v6, s[36:39], s68 offen offset:0 sc1 lds\n" \
  "s_lshr_b32 s73, s48, 0\n" \
  "s_lshr_b32 s85, s48, 5\n" \
  "v_lshlrev_b32 v64, 16, v80\n" \
  "v_and_b32 v65, 0xffff0000, v80\n" \
  "v_lshlrev_b32 v66, 16, v81\n" \
  "v_and_b32 v67, 0xffff0000, v81\n" \
  "v_lshlrev_b32 v68, 16, v82\n" \
  "v_and_b32 v69, 0xffff0000, v82\n" \
  "v_lshlrev_b32 v70, 16, v83\n" \
  "v_and_b32 v71, 0xffff0000, v83\n" \
  "v_fma_f32 v64, v64, v89, v88\n" \
  "v_fma_f32 v65, v65, v89, v88\n" \
  "v_fma_f32 v66, v66, v89, v88\n" \
  "v_fma_f32 v67, v67, v89, v88\n" \
  "v_fma_f32 v68, v68, v89, v88\n" \
  "v_fma_f32 v69, v69, v89, v88\n" \
  "v_fma_f32 v70, v70, v89, v88\n" \
  "v_fma_f32 v71, v71, v89, v88\n" \
  "v_add_f32 v16, v16, v64\n" \
  "v_add_f32 v17, v17, v65\n" \
  "v_add_f32 v18, v18, v66\n" \
  "v_add_f32 v19, v19, v67\n" \
  "v_add_f32 v20, v20, v68\n" \
  "v_add_f32 v21, v21, v69\n" \
  "v_add_f32 v22, v22, v70\n" \
  "v_add_f32 v23, v23, v71\n" \
  "v_cvt_pk_bf16_f32 v72, v16, v17\n" \
  "v_cvt_pk_bf16_f32 v73, v18, v19\n" \
  "v_cvt_pk_bf16_f32 v74, v20, v21\n" \
  "v_cvt_pk_bf16_f32 v75, v22, v23\n" \
  "s_add_u32 m0, s78, 8448\n" \
  "s_nop 0\n" \
  "buffer_load_dword v6, s[36:39], s68 offen offset:0 sc1 lds\n" \
  "buffer_store_dwordx4 v[72:75], v3, s[36:39], s73 offen offset:0 sc0 sc1\n" \
  "v_add_f32 v64, v16, v17\n" \
  "v_mul_f32 v65, v16, v16\n" \
  "v_fma_f32 v65, v17, v17, v65\n" \
  "v_add_f32 v64, v64, v18\n" \
  "v_fma_f32 v65, v18, v18, v65\n" \
  "v_add_f32 v64, v64, v19\n" \
  "v_fma_f32 v65, v19, v19, v65\n" \
  "v_add_f32 v64, v64, v20\n" \
  "v_fma_f32 v65, v20, v20, v65\n" \
  "v_add_f32 v64, v64, v21\n" \
  "v_fma_f32 v65, v21, v21, v65\n" \
  "v_add_f32 v64, v64, v22\n" \
  "v_fma_f32 v65, v22, v22, v65\n" \
  "v_add_f32 v64, v64, v23\n" \
  "v_fma_f32 v65, v23, v23, v65\n" \
  "s_nop 1\n" \
  "v_permlane16_swap_b32 v64, v65\n" \
  "v_add_f32 v64, v64, v65\n" \
  "v_mov_b32 v65, v64\n" \
  "s_nop 1\n" \
  "v_permlane32_swap_b32 v64, v65\n" \
  "v_add_f32 v64, v64, v65\n" \
  "ds_write_b32 v13, v64 offset:2048\n" \
  "s_waitcnt lgkmcnt(0)\n" \
  "s_barrier\n" \
  "ds_read_b128 v[76:79], v14 offset:2048\n" \
  "s_waitcnt lgkmcnt(0)\n" \
  "v_add_f32 v76, v76, v77\n" \
  "v_add_f32 v78, v78, v79\n" \
  "v_add_f32 v76, v76, v78\n" \
  "buffer_store_dword v76, v12, s[36:39], s85 offen offset:0 sc0 sc1\n" \
  "s_cmp_ge_u32 s49, s59\n" \
  "s_cbranch_scc1 Lg2_done1_%=\n" \
  "s_add_u32 m0, s78, 16384\n" \
  "s_nop 0\n" \
  "buffer_load_dword v6, s[36:39], s68 offen offset:0 sc1 lds\n" \
  "s_waitcnt vmcnt(4)\n" \
  "s_barrier\n" \
  "ds_read_b32 v15, v7 offset:8192\n" \
  "s_lshr_b32 s70, s46, 5\n" \
  "buffer_store_dword v5, v4, s[36:39], s70 offen offset:0 sc0 sc1\n" \
  "s_mov_b32 s46, s59\n" \
  "s_lshr_b32 s70, s47, 5\n" \
  "buffer_store_dword v5, v4, s[36:39], s70 offen offset:0 sc0 sc1\n" \
  "s_mov_b32 s47, s59\n" \
  "s_mov_b32 s69, 18432\n" \
  "s_waitcnt lgkmcnt(0)\n" \
  "v_cmp_eq_u32 vcc, s60, v15\n" \
  "s_nop 0\n" \
  "s_cmp_eq_u32 vcc_lo, -1\n" \
  "s_cbranch_scc1 Lg2_go1_%=\n" \
  "s_add_u32 m0, s78, 16640\n" \
  "s_nop 0\n" \
  "buffer_load_dword v6, s[36:39], s68 offen offset:0 sc1 lds\n" \
  "s_waitcnt vmcnt(6)\n" \
  "s_barrier\n" \
  "ds_read_b32 v15, v7 offset:8448\n" \
  "s_mov_b32 s69, 18688\n" \
  "s_waitcnt lgkmcnt(0)\n" \
  "v_cmp_eq_u32 vcc, s60, v15\n" \
  "s_nop 0\n" \
  "s_cmp_eq_u32 vcc_lo, -1\n" \
  "s_cbranch_scc1 Lg2_go1_%=\n" \
  "s_add_u32 m0, s78, 8192\n" \
  "s_nop 0\n" \
  "buffer_load_dword v6, s[36:39], s68 offen offset:0 sc1 lds\n" \
  "s_waitcnt vmcnt(4)\n" \
  "s_barrier\n" \
  "ds_read_b32 v15, v7 offset:16384\n" \
  "s_lshr_b32 s70, s48, 5\n" \
  "buffer_store_dword v5, v4, s[36:39], s70 offen offset:0 sc0 sc1\n" \
  "s_mov_b32 s48, s59\n" \
  "s_mov_b32 s69, 10240\n" \
  "s_waitcnt lgkmcnt(0)\n" \
  "v_cmp_eq_u32 vcc, s60, v15\n" \
  "s_nop 0\n" \
  "s_cmp_eq_u32 vcc_lo, -1\n" \
  "s_cbranch_scc1 Lg2_go1_%=\n" \
  "s_add_u32 m0, s78, 8448\n" \
  "s_nop 0\n" \
  "buffer_load_dword v6, s[36:39], s68 offen offset:0 sc1 lds\n" \
  "s_waitcnt vmcnt(3)\n" \
  "s_barrier\n" \
  "ds_read_b32 v15, v7 offset:16640\n" \
  "s_mov_b32 s69, 10496\n" \
  "s_waitcnt lgkmcnt(0)\n" \
  "v_cmp_eq_u32 vcc, s60, v15\n" \
  "s_nop 0\n" \
  "s_cmp_eq_u32 vcc_lo, -1\n" \
  "s_cbranch_scc1 Lg2_go1_%=\n" \
  "Lg2_poll1_%=:\n" \
  "s_add_u32 m0, s78, 16384\n" \
  "s_nop 0\n" \
  "buffer_load_dword v6, s[36:39], s68 offen offset:0 sc1 lds\n" \
  "s_waitcnt vmcnt(2)\n" \
  "s_barrier\n" \
  "ds_read_b32 v15, v7 offset:8192\n" \
  "s_mov_b32 s69, 18432\n" \
  "s_waitcnt lgkmcnt(0)\n" \
  "v_cmp_eq_u32 vcc, s60, v15\n" \
  "s_nop 0\n" \
  "s_cmp_eq_u32 vcc_lo, -1\n" \
  "s_cbranch_scc1 Lg2_go1_%=\n" \
  "s_add_u32 m0, s78, 16640\n" \
  "s_nop 0\n" \
  "buffer_load_dword v6, s[36:39], s68 offen offset:0 sc1 lds\n" \
  "s_waitcnt vmcnt(2)\n" \
  "s_barrier\n" \
  "ds_read_b32 v15, v7 offset:8448\n" \
  "s_mov_b32 s69, 18688\n" \
  "s_waitcnt lgkmcnt(0)\n" \
  "v_cmp_eq_u32 vcc, s60, v15\n" \
  "s_nop 0\n" \
  "s_cmp_eq_u32 vcc_lo, -1\n" \
  "s_cbranch_scc1 Lg2_go1_%=\n" \
  "s_add_u32 m0, s78, 8192\n" \
  "s_nop 0\n" \
  "buffer_load_dword v6, s[36:39], s68 offen offset:0 sc1 lds\n" \
  "s_waitcnt vmcnt(2)\n" \
  "s_barrier\n" \
  "ds_read_b32 v15, v7 offset:16384\n" \
  "s_mov_b32 s69, 10240\n" \
  "s_waitcnt lgkmcnt(0)\n" \
  "v_cmp_eq_u32 vcc, s60, v15\n" \
  "s_nop 0\n" \
  "s_cmp_eq_u32 vcc_lo, -1\n" \
  "s_cbranch_scc1 Lg2_go1_%=\n" \
  "s_add_u32 m0, s78, 8448\n" \
  "s_nop 0\n" \
  "buffer_load_dword v6, s[36:39], s68 offen offset:0 sc1 lds\n" \
  "s_waitcnt vmcnt(2)\n" \
  "s_barrier\n" \
  "ds_read_b32 v15, v7 offset:16640\n" \
  "s_mov_b32 s69, 10496\n" \
  "s_waitcnt lgkmcnt(0)\n" \
  "v_cmp_eq_u32 vcc, s60, v15\n" \
  "s_nop 0\n" \
  "s_cmp_eq_u32 vcc_lo, -1\n" \
  "s_cbranch_scc1 Lg2_go1_%=\n" \
  "s_add_u32 s79, s79, 1\n" \
  "s_cmp_lt_u32 s79, 20000\n" \
  "s_cbranch_scc1 Lg2_poll1_%=\n" \
  "Lg2_go1_%=:\n" \
  "s_xor_b32 s62, s64, 65536\n" \
  "s_cmp_lt_u32 s67, s59\n" \
  "s_cbranch_scc1 Lg2_gs1_%=\n" \
  "s_mov_b32 m0, s62\n" \
  "s_nop 0\n" \
  "buffer_load_dwordx4 v0, s[36:39], s49 offen offset:0 sc1 lds\n" \
  "buffer_load_dwordx4 v0, s[36:39], s49 offen offset:1024 sc1 lds\n" \
  "buffer_load_dwordx4 v0, s[36:39], s49 offen offset:2048 sc1 lds\n" \
  "buffer_load_dwordx4 v0, s[36:39], s49 offen offset:3072 sc1 lds\n" \
  "s_add_u32 m0, s62, 16384\n" \
  "s_lshr_b32 s72, s49, 6\n" \
  "s_lshr_b32 s71, s62, 2\n" \
  "buffer_load_dwordx4 v1, s[36:39], s49 offen offset:0 sc1 lds\n" \
  "buffer_load_dwordx4 v1, s[36:39], s49 offen offset:1024 sc1 lds\n" \
  "buffer_load_dwordx4 v1, s[36:39], s49 offen offset:2048 sc1 lds\n" \
  "buffer_load_dwordx4 v1, s[36:39], s49 offen offset:3072 sc1 lds\n" \
  "s_add_u32 m0, s71, s88\n" \
  "s_nop 0\n" \
  "buffer_load_dwordx4 v10, s[36:39], s49 offen offset:0 sc1 lds\n" \
  "buffer_load_dword v11, s[36:39], s72 offen offset:1024 sc1 lds\n" \
  "Lg2_gs1_%=:\n" \
  "s_cmp_eq_u32 s54, 1\n" \
  "s_cselect_b32 s68, s51, s50\n" \
  "s_lshr_b32 s68, s68, 5\n" \
  "s_add_u32 m0, s78, 0\n" \
  "s_nop 0\n" \
  "buffer_load_dword v6, s[36:39], s68 offen offset:0 sc1 lds\n" \
  "s_cmp_eq_u32 vcc_hi, -1\n" \
  "s_cselect_b32 s67, s50, s59\n" \
  "s_xor_b32 s62, s63, 65536\n" \
  "s_mov_b32 m0, s62\n" \
  "s_nop 0\n" \
  "buffer_load_dwordx4 v0, s[36:39], s67 offen offset:0 sc1 lds\n" \
  "buffer_load_dwordx4 v0, s[36:39], s67 offen offset:1024 sc1 lds\n" \
  "buffer_load_dwordx4 v0, s[36:39], s67 offen offset:2048 sc1 lds\n" \
  "buffer_load_dwordx4 v0, s[36:39], s67 offen offset:3072 sc1 lds\n" \
  "s_add_u32 m0, s62, 16384\n" \
  "s_lshr_b32 s72, s67, 6\n" \
  "s_lshr_b32 s71, s62, 2\n" \
  "buffer_load_dwordx4 v1, s[36:39], s67 offen offset:0 sc1 lds\n" \
  "buffer_load_dwordx4 v1, s[36:39], s67 offen offset:1024 sc1 lds\n" \
  "buffer_load_dwordx4 v1, s[36:39], s67 offen offset:2048 sc1 lds\n" \
  "buffer_load_dwordx4 v1, s[36:39], s67 offen offset:3072 sc1 lds\n" \
  "s_add_u32 m0, s71, s88\n" \
  "s_nop 0\n" \
  "buffer_load_dwordx4 v10, s[36:39], s67 offen offset:0 sc1 lds\n" \
  "buffer_load_dword v11, s[36:39], s72 offen offset:1024 sc1 lds\n" \
  "s_waitcnt vmcnt(17)\n" \
  "s_barrier\n" \
  "ds_read_b128 v[32:35], v2 offset:32768\n" \
  "ds_read_b128 v[36:39], v2 offset:33792\n" \
  "ds_read_b128 v[40:43], v2 offset:34816\n" \
  "ds_read_b128 v[44:47], v2 offset:35840\n" \
  "ds_read_b128 v[48:51], v2 offset:36864\n" \
  "ds_read_b128 v[52:55], v2 offset:37888\n" \
  "ds_read_b128 v[56:59], v2 offset:38912\n" \
  "s_lshr_b32 s70, s46, 5\n" \
  "buffer_store_dword v5, v4, s[36:39], s70 offen offset:0 sc0 sc1\n" \
  "s_mov_b32 s46, s59\n" \
  "s_lshr_b32 s70, s47, 5\n" \
  "buffer_store_dword v5, v4, s[36:39], s70 offen offset:0 sc0 sc1\n" \
  "s_mov_b32 s47, s59\n" \
  "s_lshr_b32 s70, s48, 5\n" \
  "buffer_store_dword v5, v4, s[36:39], s70 offen offset:0 sc0 sc1\n" \
  "s_mov_b32 s48, s59\n" \
  "s_waitcnt lgkmcnt(3)\n" \
  "s_branch Lg2_d1_%=\n" \
  "Lg2_done1_%=:\n" \
  "s_waitcnt vmcnt(0)\n" \
  "s_barrier\n" \
  "s_lshr_b32 s70, s46, 5\n" \
  "buffer_store_dword v5, v4, s[36:39], s70 offen offset:0 sc0 sc1\n" \
  "s_lshr_b32 s70, s47, 5\n" \
  "buffer_store_dword v5, v4, s[36:39], s70 offen offset:0 sc0 sc1\n" \
  "s_lshr_b32 s70, s48, 5\n" \
  "buffer_store_dword v5, v4, s[36:39], s70 offen offset:0 sc0 sc1\n" \
  "s_branch Lg2_done_%=\n" \
  "Lg2_done_%=:\n" \
  "s_waitcnt vmcnt(0) lgkmcnt(0)\n" \
  "s_barrier\n" \
  "Lg2_end_%=:\n" \
  "s_mov_b32 %0, s60\n" \
  "Lcode_g12_e:\n" \
  : "=&s"(epOut) : "s"(ws), "s"(wave), "s"(sStep), "s"(sX1), "s"(sX2), "s"(sU0), "s"(sWw), "s"(sBuf), "v"(vSrc), "v"(vA), "v"(vOut), "v"(vFlag), "v"(vPollOff), "v"(vSnapRd), "v"(vX), "v"(vX2), "v"(vAuxS), "v"(vAuxS2), "v"(vOut2), "v"(vStW), "v"(vStR), "v"(vWd0), "v"(vWd1), "v"(vWr0), "v"(vWr1), "v"(vWr2), "s"(W) : "v0", "v1", "v2", "v3", "v4", "v5", "v6", "v7", "v8", "v9", "v10", "v11", "v12", "v13", "v14", "v15", "v16", "v17", "v18", "v19", "v20", "v21", "v22", "v23", "v24", "v25", "v26", "v27", "v28", "v29", "v30", "v31", "v32", "v33", "v34", "v35", "v36", "v37", "v38", "v39", "v40", "v41", "v42", "v43", "v44", "v45", "v46", "v47", "v48", "v49", "v50", "v51", "v52", "v53", "v54", "v55", "v56", "v57", "v58", "v59", "v60", "v61", "v62", "v63", "v64", "v65", "v66", "v67", "v68", "v69", "v70", "v71", "v72", "v73", "v74", "v75", "v76", "v77", "v78", "v79", "v80", "v81", "v82", "v83", "v84", "v85", "v86", "v87", "v88", "v89", "v90", "v91", "v92", "v93", "v94", "v95", "v96", "v97", "v98", "v99", "v100", "v101", "v102", "v103", "v104", "v105", "v106", "v107", "v108", "v109", "v110", "v111", "v112", "v113", "v114", "v115", "v116", "v117", "v118", "v119", "v120", "v121", "v122", "v123", "v124", "v125", "v126", "v127", "v128", "v129", "v130", "v131", "v132", "v133", "v134", "v135", "v136", "v137", "v138", "v139", "v140", "v141", "v142", "v143", "v144", "v145", "v146", "v147", "v148", "v149", "v150", "v151", "v152", "v153", "v154", "v155", "v156", "v157", "v158", "v159", "v160", "v161", "v162", "v163", "v164", "v165", "v166", "v167", "v168", "v169", "v170", "v171", "v172", "v173", "v174", "v175", "v176", "v177", "v178", "v179", "v180", "v181", "v182", "v183", "v184", "v185", "v186", "v187", "v188", "v189", "v190", "v191", "v192", "v193", "v194", "v195", "v196", "v197", "v198", "v199", "v200", "v201", "v202", "v203", "v204", "v205", "v206", "v207", "v208", "v209", "v210", "v211", "v212", "v213", "v214", "v215", "v216", "v217", "v218", "v219", "v220", "v221", "v222", "v223", "v224", "v225", "v226", "v227", "v228", "v229", "v230", "v231", "v255", "a0", "a1", "a2", "a3", "a4", "a5", "a6", "a7", "a8", "a9", "a10", "a11", "a12", "a13", "a14", "a15", "a16", "a17", "a18", "a19", "a20", "a21", "a22", "a23", "a24", "a25", "a26", "a27", "a28", "a29", "a30", "a31", "a32", "a33", "a34", "a35", "a36", "a37", "a38", "a39", "a40", "a41", "a42", "a43", "a44", "a45", "a46", "a47", "a48", "a49", "a50", "a51", "a52", "a53", "a54", "a55", "a56", "a57", "a58", "a59", "a60", "a61", "a62", "a63", "a64", "a65", "a66", "a67", "a68", "a69", "a70", "a71", "a72", "a73", "a74", "a75", "a76", "a77", "a78", "a79", "a80", "a81", "a82", "a83", "a84", "a85", "a86", "a87", "a88", "a89", "a90", "a91", "a92", "a93", "a94", "a95", "a96", "a97", "a98", "a99", "a100", "a101", "a102", "a103", "a104", "a105", "a106", "a107", "a108", "a109", "a110", "a111", "a112", "a113", "a114", "a115", "a116", "a117", "a118", "a119", "a120", "a121", "a122", "a123", "a124", "a125", "a126", "a127", "a128", "a129", "a130", "a131", "a132", "a133", "a134", "a135", "a136", "a137", "a138", "a139", "a140", "a141", "a142", "a143", "a144", "a145", "a146", "a147", "a148", "a149", "a150", "a151", "a152", "a153", "a154", "a155", "a156", "a157", "a158", "a159", "a160", "a161", "a162", "a163", "a164", "a165", "a166", "a167", "a168", "a169", "a170", "a171", "a172", "a173", "a174", "a175", "a176", "a177", "a178", "a179", "a180", "a181", "a182", "a183", "a184", "a185", "a186", "a187", "a188", "a189", "a190", "a191", "a192", "a193", "a194", "a195", "a196", "a197", "a198", "a199", "a200", "a201", "a202", "a203", "a204", "a205", "a206", "a207", "a208", "a209", "a210", "a211", "a212", "a213", "a214", "a215", "a216", "a217", "a218", "a219", "a220", "a221", "a222", "a223", "a224", "a225", "a226", "a227", "a228", "a229", "a230", "a231", "a232", "a233", "a234", "a235", "a236", "a237", "a238", "a239", "a240", "a241", "a242", "a243", "a244", "a245", "a246", "a247", "a248", "a249", "a250", "a251", "a252", "a253", "a254", "a255", "s36", "s37", "s38", "s39", "s40", "s41", "s42", "s43", "s44", "s45", "s46", "s47", "s48", "s49", "s50", "s51", "s52", "s53", "s54", "s55", "s56", "s57", "s58", "s59", "s60", "s61", "s62", "s63", "s64", "s65", "s66", "s67", "s68", "s69", "s70", "s71", "s72", "s73", "s74", "s75", "s76", "s77", "s78", "s79", "s80", "s81", "s82", "s83", "s84", "s85", "s86", "s87", "s88", "s89", "s90", "s91", "s92", "s93", "s94", "s95", "s96", "s97", "vcc", "scc", "memory")
#define ASM_G3(W) asm volatile( \
  "Lcode_g3_b:\n" \
  "s_mov_b64 s[36:37], %0\n" \
  "s_mov_b32 s38, 111193216\n" \
  "s_mov_b32 s39, 0x00020000\n" \
  "s_mov_b32 s54, %1\n" \
  "s_mov_b32 s55, %2\n" \
  "s_mov_b32 s56, %3\n" \
  "s_mov_b32 s58, %4\n" \
  "s_mov_b32 s83, %5\n" \
  "s_mov_b32 s82, %6\n" \
  "s_mov_b32 s84, %7\n" \
  "v_mov_b32 v0, %8\n" \
  "v_mov_b32 v2, %9\n" \
  "v_mov_b32 v3, %10\n" \
  "v_mov_b32 v4, %11\n" \
  "v_mov_b32 v6, %12\n" \
  "v_mov_b32 v7, %13\n" \
  "v_mov_b32 v8, %14\n" \
  "v_mov_b32 v9, %15\n" \
  "v_mov_b32 v10, %16\n" \
  "v_mov_b32 v11, %17\n" \
  "v_mov_b32 v12, %18\n" \
  "v_mov_b32 v28, %19\n" \
  "v_mov_b32 v29, %20\n" \
  "v_mov_b32 v30, %21\n" \
  "v_mov_b32 v31, %22\n" \
  "v_mov_b32 v32, %23\n" \
  "s_mov_b64 s[40:41], %24\n" \
  "s_mov_b32 s42, 7340032\n" \
  "s_mov_b32 s43, 0x00020000\n" \
  "s_lshl_b32 s88, s54, 11\n" \
  "s_mov_b32 s69, 111193088\n" \
  "buffer_load_dword v5, off, s[36:39], s69 sc1\n" \
  "s_add_u32 s61, s82, 0\n" \
  "s_add_u32 m0, s88, 0\n" \
  "s_nop 0\n" \
  "buffer_load_dwordx4 v28, s[40:43], s61 offen offset:0  lds\n" \
  "s_add_u32 m0, s88, 1024\n" \
  "s_nop 0\n" \
  "buffer_load_dwordx4 v29, s[40:43], s61 offen offset:0  lds\n" \
  "s_add_u32 m0, s88, 8064\n" \
  "s_nop 0\n" \
  "buffer_load_dwordx4 v28, s[40:43], s61 offen offset:128  lds\n" \
  "s_add_u32 m0, s88, 9088\n" \
  "s_nop 0\n" \
  "buffer_load_dwordx4 v29, s[40:43], s61 offen offset:128  lds\n" \
  "s_add_u32 m0, s88, 16128\n" \
  "s_nop 0\n" \
  "buffer_load_dwordx4 v28, s[40:43], s61 offen offset:256  lds\n" \
  "s_add_u32 m0, s88, 17152\n" \
  "s_nop 0\n" \
  "buffer_load_dwordx4 v29, s[40:43], s61 offen offset:256  lds\n" \
  "s_add_u32 s61, s82, 458752\n" \
  "s_add_u32 m0, s88, 24576\n" \
  "s_nop 0\n" \
  "buffer_load_dwordx4 v28, s[40:43], s61 offen offset:0  lds\n" \
  "s_add_u32 m0, s88, 25600\n" \
  "s_nop 0\n" \
  "buffer_load_dwordx4 v29, s[40:43], s61 offen offset:0  lds\n" \
  "s_add_u32 m0, s88, 32640\n" \
  "s_nop 0\n" \
  "buffer_load_dwordx4 v28, s[40:43], s61 offen offset:128  lds\n" \
  "s_add_u32 m0, s88, 33664\n" \
  "s_nop 0\n" \
  "buffer_load_dwordx4 v29, s[40:43], s61 offen offset:128  lds\n" \
  "s_add_u32 m0, s88, 40704\n" \
  "s_nop 0\n" \
  "buffer_load_dwordx4 v28, s[40:43], s61 offen offset:256  lds\n" \
  "s_add_u32 m0, s88, 41728\n" \
  "s_nop 0\n" \
  "buffer_load_dwordx4 v29, s[40:43], s61 offen offset:256  lds\n" \
  "s_add_u32 s61, s82, 917504\n" \
  "s_add_u32 m0, s88, 49152\n" \
  "s_nop 0\n" \
  "buffer_load_dwordx4 v28, s[40:43], s61 offen offset:0  lds\n" \
  "s_add_u32 m0, s88, 50176\n" \
  "s_nop 0\n" \
  "buffer_load_dwordx4 v29, s[40:43], s61 offen offset:0  lds\n" \
  "s_add_u32 m0, s88, 57216\n" \
  "s_nop 0\n" \
  "buffer_load_dwordx4 v28, s[40:43], s61 offen offset:128  lds\n" \
  "s_add_u32 m0, s88, 58240\n" \
  "s_nop 0\n" \
  "buffer_load_dwordx4 v29, s[40:43], s61 offen offset:128  lds\n" \
  "s_add_u32 m0, s88, 65280\n" \
  "s_nop 0\n" \
  "buffer_load_dwordx4 v28, s[40:43], s61 offen offset:256  lds\n" \
  "s_add_u32 m0, s88, 66304\n" \
  "s_nop 0\n" \
  "buffer_load_dwordx4 v29, s[40:43], s61 offen offset:256  lds\n" \
  "s_add_u32 s61, s82, 1376256\n" \
  "s_add_u32 m0, s88, 73728\n" \
  "s_nop 0\n" \
  "buffer_load_dwordx4 v28, s[40:43], s61 offen offset:0  lds\n" \
  "s_add_u32 m0, s88, 74752\n" \
  "s_nop 0\n" \
  "buffer_load_dwordx4 v29, s[40:43], s61 offen offset:0  lds\n" \
  "s_add_u32 m0, s88, 81792\n" \
  "s_nop 0\n" \
  "buffer_load_dwordx4 v28, s[40:43], s61 offen offset:128  lds\n" \
  "s_add_u32 m0, s88, 82816\n" \
  "s_nop 0\n" \
  "buffer_load_dwordx4 v29, s[40:43], s61 offen offset:128  lds\n" \
  "s_add_u32 m0, s88, 89856\n" \
  "s_nop 0\n" \
  "buffer_load_dwordx4 v28, s[40:43], s61 offen offset:256  lds\n" \
  "s_add_u32 m0, s88, 90880\n" \
  "s_nop 0\n" \
  "buffer_load_dwordx4 v29, s[40:43], s61 offen offset:256  lds\n" \
  "s_add_u32 s61, s82, 1835008\n" \
  "s_add_u32 m0, s88, 98304\n" \
  "s_nop 0\n" \
  "buffer_load_dwordx4 v28, s[40:43], s61 offen offset:0  lds\n" \
  "s_add_u32 m0, s88, 99328\n" \
  "s_nop 0\n" \
  "buffer_load_dwordx4 v29, s[40:43], s61 offen offset:0  lds\n" \
  "s_add_u32 m0, s88, 106368\n" \
  "s_nop 0\n" \
  "buffer_load_dwordx4 v28, s[40:43], s61 offen offset:128  lds\n" \
  "s_add_u32 m0, s88, 107392\n" \
  "s_nop 0\n" \
  "buffer_load_dwordx4 v29, s[40:43], s61 offen offset:128  lds\n" \
  "s_add_u32 m0, s88, 114432\n" \
  "s_nop 0\n" \
  "buffer_load_dwordx4 v28, s[40:43], s61 offen offset:256  lds\n" \
  "s_add_u32 m0, s88, 115456\n" \
  "s_nop 0\n" \
  "buffer_load_dwordx4 v29, s[40:43], s61 offen offset:256  lds\n" \
  "s_waitcnt vmcnt(24)\n" \
  "s_waitcnt lgkmcnt(0)\n" \
  "s_barrier\n" \
  "s_add_u32 s61, s82, 2293760\n" \
  "s_add_u32 m0, s88, 122880\n" \
  "s_nop 0\n" \
  "buffer_load_dwordx4 v28, s[40:43], s61 offen offset:0  lds\n" \
  "s_add_u32 m0, s88, 123904\n" \
  "s_nop 0\n" \
  "buffer_load_dwordx4 v29, s[40:43], s61 offen offset:0  lds\n" \
  "s_add_u32 m0, s88, 130944\n" \
  "s_nop 0\n" \
  "buffer_load_dwordx4 v28, s[40:43], s61 offen offset:128  lds\n" \
  "s_add_u32 m0, s88, 131968\n" \
  "s_nop 0\n" \
  "buffer_load_dwordx4 v29, s[40:43], s61 offen offset:128  lds\n" \
  "s_add_u32 m0, s88, 139008\n" \
  "s_nop 0\n" \
  "buffer_load_dwordx4 v28, s[40:43], s61 offen offset:256  lds\n" \
  "s_add_u32 m0, s88, 140032\n" \
  "s_nop 0\n" \
  "buffer_load_dwordx4 v29, s[40:43], s61 offen offset:256  lds\n" \
  "v_add_u32 v84, 0, v30\n" \
  "v_add_u32 v85, 0, v31\n" \
  "v_add_u32 v86, 0, v32\n" \
  "ds_read_b64_tr_b16 a[0:1], v84 offset:0\n" \
  "ds_read_b64_tr_b16 a[2:3], v84 offset:512\n" \
  "ds_read_b64_tr_b16 a[4:5], v85 offset:0\n" \
  "ds_read_b64_tr_b16 a[6:7], v85 offset:512\n" \
  "ds_read_b64_tr_b16 a[8:9], v86 offset:0\n" \
  "ds_read_b64_tr_b16 a[10:11], v86 offset:512\n" \
  "ds_read_b64_tr_b16 a[12:13], v84 offset:4096\n" \
  "ds_read_b64_tr_b16 a[14:15], v84 offset:4608\n" \
  "ds_read_b64_tr_b16 a[16:17], v85 offset:4096\n" \
  "ds_read_b64_tr_b16 a[18:19], v85 offset:4608\n" \
  "ds_read_b64_tr_b16 a[20:21], v86 offset:4096\n" \
  "ds_read_b64_tr_b16 a[22:23], v86 offset:4608\n" \
  "s_waitcnt vmcnt(24)\n" \
  "s_waitcnt lgkmcnt(0)\n" \
  "s_barrier\n" \
  "s_add_u32 s61, s82, 2752512\n" \
  "s_add_u32 m0, s88, 0\n" \
  "s_nop 0\n" \
  "buffer_load_dwordx4 v28, s[40:43], s61 offen offset:0  lds\n" \
  "s_add_u32 m0, s88, 1024\n" \
  "s_nop 0\n" \
  "buffer_load_dwordx4 v29, s[40:43], s61 offen offset:0  lds\n" \
  "s_add_u32 m0, s88, 8064\n" \
  "s_nop 0\n" \
  "buffer_load_dwordx4 v28, s[40:43], s61 offen offset:128  lds\n" \
  "s_add_u32 m0, s88, 9088\n" \
  "s_nop 0\n" \
  "buffer_load_dwordx4 v29, s[40:43], s61 offen offset:128  lds\n" \
  "s_add_u32 m0, s88, 16128\n" \
  "s_nop 0\n" \
  "buffer_load_dwordx4 v28, s[40:43], s61 offen offset:256  lds\n" \
  "s_add_u32 m0, s88, 17152\n" \
  "s_nop 0\n" \
  "buffer_load_dwordx4 v29, s[40:43], s61 offen offset:256  lds\n" \
  "v_add_u32 v84, 24576, v30\n" \
  "v_add_u32 v85, 24576, v31\n" \
  "v_add_u32 v86, 24576, v32\n" \
  "ds_read_b64_tr_b16 a[24:25], v84 offset:0\n" \
  "ds_read_b64_tr_b16 a[26:27], v84 offset:512\n" \
  "ds_read_b64_tr_b16 a[28:29], v85 offset:0\n" \
  "ds_read_b64_tr_b16 a[30:31], v85 offset:512\n" \
  "ds_read_b64_tr_b16 a[32:33], v86 offset:0\n" \
  "ds_read_b64_tr_b16 a[34:35], v86 offset:512\n" \
  "ds_read_b64_tr_b16 a[36:37], v84 offset:4096\n" \
  "ds_read_b64_tr_b16 a[38:39], v84 offset:4608\n" \
  "ds_read_b64_tr_b16 a[40:41], v85 offset:4096\n" \
  "ds_read_b64_tr_b16 a[42:43], v85 offset:4608\n" \
  "ds_read_b64_tr_b16 a[44:45], v86 offset:4096\n" \
  "ds_read_b64_tr_b16 a[46:47], v86 offset:4608\n" \
  "s_waitcnt vmcnt(24)\n" \
  "s_waitcnt lgkmcnt(0)\n" \
  "s_barrier\n" \
  "s_add_u32 s61, s82, 3211264\n" \
  "s_add_u32 m0, s88, 24576\n" \
  "s_nop 0\n" \
  "buffer_load_dwordx4 v28, s[40:43], s61 offen offset:0  lds\n" \
  "s_add_u32 m0, s88, 25600\n" \
  "s_nop 0\n" \
  "buffer_load_dwordx4 v29, s[40:43], s61 offen offset:0  lds\n" \
  "s_add_u32 m0, s88, 32640\n" \
  "s_nop 0\n" \
  "buffer_load_dwordx4 v28, s[40:43], s61 offen offset:128  lds\n" \
  "s_add_u32 m0, s88, 33664\n" \
  "s_nop 0\n" \
  "buffer_load_dwordx4 v29, s[40:43], s61 offen offset:128  lds\n" \
  "s_add_u32 m0, s88, 40704\n" \
  "s_nop 0\n" \
  "buffer_load_dwordx4 v28, s[40:43], s61 offen offset:256  lds\n" \
  "s_add_u32 m0, s88, 41728\n" \
  "s_nop 0\n" \
  "buffer_load_dwordx4 v29, s[40:43], s61 offen offset:256  lds\n" \
  "v_add_u32 v84, 49152, v30\n" \
  "v_add_u32 v85, 49152, v31\n" \
  "v_add_u32 v86, 49152, v32\n" \
  "ds_read_b64_tr_b16 a[48:49], v84 offset:0\n" \
  "ds_read_b64_tr_b16 a[50:51], v84 offset:512\n" \
  "ds_read_b64_tr_b16 a[52:53], v85 offset:0\n" \
  "ds_read_b64_tr_b16 a[54:55], v85 offset:512\n" \
  "ds_read_b64_tr_b16 a[56:57], v86 offset:0\n" \
  "ds_read_b64_tr_b16 a[58:59], v86 offset:512\n" \
  "ds_read_b64_tr_b16 a[60:61], v84 offset:4096\n" \
  "ds_read_b64_tr_b16 a[62:63], v84 offset:4608\n" \
  "ds_read_b64_tr_b16 a[64:65], v85 offset:4096\n" \
  "ds_read_b64_tr_b16 a[66:67], v85 offset:4608\n" \
  "ds_read_b64_tr_b16 a[68:69], v86 offset:4096\n" \
  "ds_read_b64_tr_b16 a[70:71], v86 offset:4608\n" \
  "s_waitcnt vmcnt(24)\n" \
  "s_waitcnt lgkmcnt(0)\n" \
  "s_barrier\n" \
  "s_add_u32 s61, s82, 3670016\n" \
  "s_add_u32 m0, s88, 49152\n" \
  "s_nop 0\n" \
  "buffer_load_dwordx4 v28, s[40:43], s61 offen offset:0  lds\n" \
  "s_add_u32 m0, s88, 50176\n" \
  "s_nop 0\n" \
  "buffer_load_dwordx4 v29, s[40:43], s61 offen offset:0  lds\n" \
  "s_add_u32 m0, s88, 57216\n" \
  "s_nop 0\n" \
  "buffer_load_dwordx4 v28, s[40:43], s61 offen offset:128  lds\n" \
  "s_add_u32 m0, s88, 58240\n" \
  "s_nop 0\n" \
  "buffer_load_dwordx4 v29, s[40:43], s61 offen offset:128  lds\n" \
  "s_add_u32 m0, s88, 65280\n" \
  "s_nop 0\n" \
  "buffer_load_dwordx4 v28, s[40:43], s61 offen offset:256  lds\n" \
  "s_add_u32 m0, s88, 66304\n" \
  "s_nop 0\n" \
  "buffer_load_dwordx4 v29, s[40:43], s61 offen offset:256  lds\n" \
  "v_add_u32 v84, 73728, v30\n" \
  "v_add_u32 v85, 73728, v31\n" \
  "v_add_u32 v86, 73728, v32\n" \
  "ds_read_b64_tr_b16 a[72:73], v84 offset:0\n" \
  "ds_read_b64_tr_b16 a[74:75], v84 offset:512\n" \
  "ds_read_b64_tr_b16 a[76:77], v85 offset:0\n" \
  "ds_read_b64_tr_b16 a[78:79], v85 offset:512\n" \
  "ds_read_b64_tr_b16 a[80:81], v86 offset:0\n" \
  "ds_read_b64_tr_b16 a[82:83], v86 offset:512\n" \
  "ds_read_b64_tr_b16 a[84:85], v84 offset:4096\n" \
  "ds_read_b64_tr_b16 a[86:87], v84 offset:4608\n" \
  "ds_read_b64_tr_b16 a[88:89], v85 offset:4096\n" \
  "ds_read_b64_tr_b16 a[90:91], v85 offset:4608\n" \
  "ds_read_b64_tr_b16 a[92:93], v86 offset:4096\n" \
  "ds_read_b64_tr_b16 a[94:95], v86 offset:4608\n" \
  "s_waitcnt vmcnt(24)\n" \
  "s_waitcnt lgkmcnt(0)\n" \
  "s_barrier\n" \
  "s_add_u32 s61, s82, 4128768\n" \
  "s_add_u32 m0, s88, 73728\n" \
  "s_nop 0\n" \
  "buffer_load_dwordx4 v28, s[40:43], s61 offen offset:0  lds\n" \
  "s_add_u32 m0, s88, 74752\n" \
  "s_nop 0\n" \
  "buffer_load_dwordx4 v29, s[40:43], s61 offen offset:0  lds\n" \
  "s_add_u32 m0, s88, 81792\n" \
  "s_nop 0\n" \
  "buffer_load_dwordx4 v28, s[40:43], s61 offen offset:128  lds\n" \
  "s_add_u32 m0, s88, 82816\n" \
  "s_nop 0\n" \
  "buffer_load_dwordx4 v29, s[40:43], s61 offen offset:128  lds\n" \
  "s_add_u32 m0, s88, 89856\n" \
  "s_nop 0\n" \
  "buffer_load_dwordx4 v28, s[40:43], s61 offen offset:256  lds\n" \
  "s_add_u32 m0, s88, 90880\n" \
  "s_nop 0\n" \
  "buffer_load_dwordx4 v29, s[40:43], s61 offen offset:256  lds\n" \
  "v_add_u32 v84, 98304, v30\n" \
  "v_add_u32 v85, 98304, v31\n" \
  "v_add_u32 v86, 98304, v32\n" \
  "ds_read_b64_tr_b16 a[96:97], v84 offset:0\n" \
  "ds_read_b64_tr_b16 a[98:99], v84 offset:512\n" \
  "ds_read_b64_tr_b16 a[100:101], v85 offset:0\n" \
  "ds_read_b64_tr_b16 a[102:103], v85 offset:512\n" \
  "ds_read_b64_tr_b16 a[104:105], v86 offset:0\n" \
  "ds_read_b64_tr_b16 a[106:107], v86 offset:512\n" \
  "ds_read_b64_tr_b16 a[108:109], v84 offset:4096\n" \
  "ds_read_b64_tr_b16 a[110:111], v84 offset:4608\n" \
  "ds_read_b64_tr_b16 a[112:113], v85 offset:4096\n" \
  "ds_read_b64_tr_b16 a[114:115], v85 offset:4608\n" \
  "ds_read_b64_tr_b16 a[116:117], v86 offset:4096\n" \
  "ds_read_b64_tr_b16 a[118:119], v86 offset:4608\n" \
  "s_waitcnt vmcnt(24)\n" \
  "s_waitcnt lgkmcnt(0)\n" \
  "s_barrier\n" \
  "s_add_u32 s61, s82, 4587520\n" \
  "s_add_u32 m0, s88, 98304\n" \
  "s_nop 0\n" \
  "buffer_load_dwordx4 v28, s[40:43], s61 offen offset:0  lds\n" \
  "s_add_u32 m0, s88, 99328\n" \
  "s_nop 0\n" \
  "buffer_load_dwordx4 v29, s[40:43], s61 offen offset:0  lds\n" \
  "s_add_u32 m0, s88, 106368\n" \
  "s_nop 0\n" \
  "buffer_load_dwordx4 v28, s[40:43], s61 offen offset:128  lds\n" \
  "s_add_u32 m0, s88, 107392\n" \
  "s_nop 0\n" \
  "buffer_load_dwordx4 v29, s[40:43], s61 offen offset:128  lds\n" \
  "s_add_u32 m0, s88, 114432\n" \
  "s_nop 0\n" \
  "buffer_load_dwordx4 v28, s[40:43], s61 offen offset:256  lds\n" \
  "s_add_u32 m0, s88, 115456\n" \
  "s_nop 0\n" \
  "buffer_load_dwordx4 v29, s[40:43], s61 offen offset:256  lds\n" \
  "v_add_u32 v84, 122880, v30\n" \
  "v_add_u32 v85, 122880, v31\n" \
  "v_add_u32 v86, 122880, v32\n" \
  "ds_read_b64_tr_b16 a[120:121], v84 offset:0\n" \
  "ds_read_b64_tr_b16 a[122:123], v84 offset:512\n" \
  "ds_read_b64_tr_b16 a[124:125], v85 offset:0\n" \
  "ds_read_b64_tr_b16 a[126:127], v85 offset:512\n" \
  "ds_read_b64_tr_b16 a[128:129], v86 offset:0\n" \
  "ds_read_b64_tr_b16 a[130:131], v86 offset:512\n" \
  "ds_read_b64_tr_b16 a[132:133], v84 offset:4096\n" \
  "ds_read_b64_tr_b16 a[134:135], v84 offset:4608\n" \
  "ds_read_b64_tr_b16 a[136:137], v85 offset:4096\n" \
  "ds_read_b64_tr_b16 a[138:139], v85 offset:4608\n" \
  "ds_read_b64_tr_b16 a[140:141], v86 offset:4096\n" \
  "ds_read_b64_tr_b16 a[142:143], v86 offset:4608\n" \
  "s_waitcnt vmcnt(24)\n" \
  "s_waitcnt lgkmcnt(0)\n" \
  "s_barrier\n" \
  "s_add_u32 s61, s82, 5046272\n" \
  "s_add_u32 m0, s88, 122880\n" \
  "s_nop 0\n" \
  "buffer_load_dwordx4 v28, s[40:43], s61 offen offset:0  lds\n" \
  "s_add_u32 m0, s88, 123904\n" \
  "s_nop 0\n" \
  "buffer_load_dwordx4 v29, s[40:43], s61 offen offset:0  lds\n" \
  "s_add_u32 m0, s88, 130944\n" \
  "s_nop 0\n" \
  "buffer_load_dwordx4 v28, s[40:43], s61 offen offset:128  lds\n" \
  "s_add_u32 m0, s88, 131968\n" \
  "s_nop 0\n" \
  "buffer_load_dwordx4 v29, s[40:43], s61 offen offset:128  lds\n" \
  "s_add_u32 m0, s88, 139008\n" \
  "s_nop 0\n" \
  "buffer_load_dwordx4 v28, s[40:43], s61 offen offset:256  lds\n" \
  "s_add_u32 m0, s88, 140032\n" \
  "s_nop 0\n" \
  "buffer_load_dwordx4 v29, s[40:43], s61 offen offset:256  lds\n" \
  "v_add_u32 v84, 0, v30\n" \
  "v_add_u32 v85, 0, v31\n" \
  "v_add_u32 v86, 0, v32\n" \
  "ds_read_b64_tr_b16 a[144:145], v84 offset:0\n" \
  "ds_read_b64_tr_b16 a[146:147], v84 offset:512\n" \
  "ds_read_b64_tr_b16 a[148:149], v85 offset:0\n" \
  "ds_read_b64_tr_b16 a[150:151], v85 offset:512\n" \
  "ds_read_b64_tr_b16 a[152:153], v86 offset:0\n" \
  "ds_read_b64_tr_b16 a[154:155], v86 offset:512\n" \
  "ds_read_b64_tr_b16 a[156:157], v84 offset:4096\n" \
  "ds_read_b64_tr_b16 a[158:159], v84 offset:4608\n" \
  "ds_read_b64_tr_b16 a[160:161], v85 offset:4096\n" \
  "ds_read_b64_tr_b16 a[162:163], v85 offset:4608\n" \
  "ds_read_b64_tr_b16 a[164:165], v86 offset:4096\n" \
  "ds_read_b64_tr_b16 a[166:167], v86 offset:4608\n" \
  "s_waitcnt vmcnt(24)\n" \
  "s_waitcnt lgkmcnt(0)\n" \
  "s_barrier\n" \
  "s_add_u32 s61, s82, 5505024\n" \
  "s_add_u32 m0, s88, 0\n" \
  "s_nop 0\n" \
  "buffer_load_dwordx4 v28, s[40:43], s61 offen offset:0  lds\n" \
  "s_add_u32 m0, s88, 1024\n" \
  "s_nop 0\n" \
  "buffer_load_dwordx4 v29, s[40:43], s61 offen offset:0  lds\n" \
  "s_add_u32 m0, s88, 8064\n" \
  "s_nop 0\n" \
  "buffer_load_dwordx4 v28, s[40:43], s61 offen offset:128  lds\n" \
  "s_add_u32 m0, s88, 9088\n" \
  "s_nop 0\n" \
  "buffer_load_dwordx4 v29, s[40:43], s61 offen offset:128  lds\n" \
  "s_add_u32 m0, s88, 16128\n" \
  "s_nop 0\n" \
  "buffer_load_dwordx4 v28, s[40:43], s61 offen offset:256  lds\n" \
  "s_add_u32 m0, s88, 17152\n" \
  "s_nop 0\n" \
  "buffer_load_dwordx4 v29, s[40:43], s61 offen offset:256  lds\n" \
  "v_add_u32 v84, 24576, v30\n" \
  "v_add_u32 v85, 24576, v31\n" \
  "v_add_u32 v86, 24576, v32\n" \
  "ds_read_b64_tr_b16 a[168:169], v84 offset:0\n" \
  "ds_read_b64_tr_b16 a[170:171], v84 offset:512\n" \
  "ds_read_b64_tr_b16 a[172:173], v85 offset:0\n" \
  "ds_read_b64_tr_b16 a[174:175], v85 offset:512\n" \
  "ds_read_b64_tr_b16 a[176:177], v86 offset:0\n" \
  "ds_read_b64_tr_b16 a[178:179], v86 offset:512\n" \
  "ds_read_b64_tr_b16 a[180:181], v84 offset:4096\n" \
  "ds_read_b64_tr_b16 a[182:183], v84 offset:4608\n" \
  "ds_read_b64_tr_b16 a[184:185], v85 offset:4096\n" \
  "ds_read_b64_tr_b16 a[186:187], v85 offset:4608\n" \
  "ds_read_b64_tr_b16 a[188:189], v86 offset:4096\n" \
  "ds_read_b64_tr_b16 a[190:191], v86 offset:4608\n" \
  "s_waitcnt vmcnt(24)\n" \
  "s_waitcnt lgkmcnt(0)\n" \
  "s_barrier\n" \
  "s_add_u32 s61, s82, 5963776\n" \
  "s_add_u32 m0, s88, 24576\n" \
  "s_nop 0\n" \
  "buffer_load_dwordx4 v28, s[40:43], s61 offen offset:0  lds\n" \
  "s_add_u32 m0, s88, 25600\n" \
  "s_nop 0\n" \
  "buffer_load_dwordx4 v29, s[40:43], s61 offen offset:0  lds\n" \
  "s_add_u32 m0, s88, 32640\n" \
  "s_nop 0\n" \
  "buffer_load_dwordx4 v28, s[40:43], s61 offen offset:128  lds\n" \
  "s_add_u32 m0, s88, 33664\n" \
  "s_nop 0\n" \
  "buffer_load_dwordx4 v29, s[40:43], s61 offen offset:128  lds\n" \
  "s_add_u32 m0, s88, 40704\n" \
  "s_nop 0\n" \
  "buffer_load_dwordx4 v28, s[40:43], s61 offen offset:256  lds\n" \
  "s_add_u32 m0, s88, 41728\n" \
  "s_nop 0\n" \
  "buffer_load_dwordx4 v29, s[40:43], s61 offen offset:256  lds\n" \
  "v_add_u32 v84, 49152, v30\n" \
  "v_add_u32 v85, 49152, v31\n" \
  "v_add_u32 v86, 49152, v32\n" \
  "ds_read_b64_tr_b16 a[192:193], v84 offset:0\n" \
  "ds_read_b64_tr_b16 a[194:195], v84 offset:512\n" \
  "ds_read_b64_tr_b16 a[196:197], v85 offset:0\n" \
  "ds_read_b64_tr_b16 a[198:199], v85 offset:512\n" \
  "ds_read_b64_tr_b16 a[200:201], v86 offset:0\n" \
  "ds_read_b64_tr_b16 a[202:203], v86 offset:512\n" \
  "ds_read_b64_tr_b16 a[204:205], v84 offset:4096\n" \
  "ds_read_b64_tr_b16 a[206:207], v84 offset:4608\n" \
  "ds_read_b64_tr_b16 a[208:209], v85 offset:4096\n" \
  "ds_read_b64_tr_b16 a[210:211], v85 offset:4608\n" \
  "ds_read_b64_tr_b16 a[212:213], v86 offset:4096\n" \
  "ds_read_b64_tr_b16 a[214:215], v86 offset:4608\n" \
  "s_waitcnt vmcnt(24)\n" \
  "s_waitcnt lgkmcnt(0)\n" \
  "s_barrier\n" \
  "s_add_u32 s61, s82, 6422528\n" \
  "s_add_u32 m0, s88, 49152\n" \
  "s_nop 0\n" \
  "buffer_load_dwordx4 v28, s[40:43], s61 offen offset:0  lds\n" \
  "s_add_u32 m0, s88, 50176\n" \
  "s_nop 0\n" \
  "buffer_load_dwordx4 v29, s[40:43], s61 offen offset:0  lds\n" \
  "s_add_u32 m0, s88, 57216\n" \
  "s_nop 0\n" \
  "buffer_load_dwordx4 v28, s[40:43], s61 offen offset:128  lds\n" \
  "s_add_u32 m0, s88, 58240\n" \
  "s_nop 0\n" \
  "buffer_load_dwordx4 v29, s[40:43], s61 offen offset:128  lds\n" \
  "s_add_u32 m0, s88, 65280\n" \
  "s_nop 0\n" \
  "buffer_load_dwordx4 v28, s[40:43], s61 offen offset:256  lds\n" \
  "s_add_u32 m0, s88, 66304\n" \
  "s_nop 0\n" \
  "buffer_load_dwordx4 v29, s[40:43], s61 offen offset:256  lds\n" \
  "v_add_u32 v84, 73728, v30\n" \
  "v_add_u32 v85, 73728, v31\n" \
  "v_add_u32 v86, 73728, v32\n" \
  "ds_read_b64_tr_b16 a[216:217], v84 offset:0\n" \
  "ds_read_b64_tr_b16 a[218:219], v84 offset:512\n" \
  "ds_read_b64_tr_b16 a[220:221], v85 offset:0\n" \
  "ds_read_b64_tr_b16 a[222:223], v85 offset:512\n" \
  "ds_read_b64_tr_b16 a[224:225], v86 offset:0\n" \
  "ds_read_b64_tr_b16 a[226:227], v86 offset:512\n" \
  "ds_read_b64_tr_b16 a[228:229], v84 offset:4096\n" \
  "ds_read_b64_tr_b16 a[230:231], v84 offset:4608\n" \
  "ds_read_b64_tr_b16 a[232:233], v85 offset:4096\n" \
  "ds_read_b64_tr_b16 a[234:235], v85 offset:4608\n" \
  "ds_read_b64_tr_b16 a[236:237], v86 offset:4096\n" \
  "ds_read_b64_tr_b16 a[238:239], v86 offset:4608\n" \
  "s_waitcnt vmcnt(24)\n" \
  "s_waitcnt lgkmcnt(0)\n" \
  "s_barrier\n" \
  "s_add_u32 s61, s82, 6881280\n" \
  "s_add_u32 m0, s88, 73728\n" \
  "s_nop 0\n" \
  "buffer_load_dwordx4 v28, s[40:43], s61 offen offset:0  lds\n" \
  "s_add_u32 m0, s88, 74752\n" \
  "s_nop 0\n" \
  "buffer_load_dwordx4 v29, s[40:43], s61 offen offset:0  lds\n" \
  "s_add_u32 m0, s88, 81792\n" \
  "s_nop 0\n" \
  "buffer_load_dwordx4 v28, s[40:43], s61 offen offset:128  lds\n" \
  "s_add_u32 m0, s88, 82816\n" \
  "s_nop 0\n" \
  "buffer_load_dwordx4 v29, s[40:43], s61 offen offset:128  lds\n" \
  "s_add_u32 m0, s88, 89856\n" \
  "s_nop 0\n" \
  "buffer_load_dwordx4 v28, s[40:43], s61 offen offset:256  lds\n" \
  "s_add_u32 m0, s88, 90880\n" \
  "s_nop 0\n" \
  "buffer_load_dwordx4 v29, s[40:43], s61 offen offset:256  lds\n" \
  "v_add_u32 v84, 98304, v30\n" \
  "v_add_u32 v85, 98304, v31\n" \
  "v_add_u32 v86, 98304, v32\n" \
  "ds_read_b64_tr_b16 a[240:241], v84 offset:0\n" \
  "ds_read_b64_tr_b16 a[242:243], v84 offset:512\n" \
  "ds_read_b64_tr_b16 a[244:245], v85 offset:0\n" \
  "ds_read_b64_tr_b16 a[246:247], v85 offset:512\n" \
  "ds_read_b64_tr_b16 a[248:249], v86 offset:0\n" \
  "ds_read_b64_tr_b16 a[250:251], v86 offset:512\n" \
  "ds_read_b64_tr_b16 a[252:253], v84 offset:4096\n" \
  "ds_read_b64_tr_b16 a[254:255], v84 offset:4608\n" \
  "ds_read_b64_tr_b16 v[128:129], v85 offset:4096\n" \
  "ds_read_b64_tr_b16 v[130:131], v85 offset:4608\n" \
  "ds_read_b64_tr_b16 v[132:133], v86 offset:4096\n" \
  "ds_read_b64_tr_b16 v[134:135], v86 offset:4608\n" \
  "s_waitcnt vmcnt(24)\n" \
  "s_waitcnt lgkmcnt(0)\n" \
  "s_barrier\n" \
  "v_add_u32 v84, 122880, v30\n" \
  "v_add_u32 v85, 122880, v31\n" \
  "v_add_u32 v86, 122880, v32\n" \
  "ds_read_b64_tr_b16 v[136:137], v84 offset:0\n" \
  "ds_read_b64_tr_b16 v[138:139], v84 offset:512\n" \
  "ds_read_b64_tr_b16 v[140:141], v85 offset:0\n" \
  "ds_read_b64_tr_b16 v[142:143], v85 offset:512\n" \
  "ds_read_b64_tr_b16 v[144:145], v86 offset:0\n" \
  "ds_read_b64_tr_b16 v[146:147], v86 offset:512\n" \
  "ds_read_b64_tr_b16 v[148:149], v84 offset:4096\n" \
  "ds_read_b64_tr_b16 v[150:151], v84 offset:4608\n" \
  "ds_read_b64_tr_b16 v[152:153], v85 offset:4096\n" \
  "ds_read_b64_tr_b16 v[154:155], v85 offset:4608\n" \
  "ds_read_b64_tr_b16 v[156:157], v86 offset:4096\n" \
  "ds_read_b64_tr_b16 v[158:159], v86 offset:4608\n" \
  "s_waitcnt vmcnt(18)\n" \
  "s_waitcnt lgkmcnt(0)\n" \
  "s_barrier\n" \
  "v_add_u32 v84, 0, v30\n" \
  "v_add_u32 v85, 0, v31\n" \
  "v_add_u32 v86, 0, v32\n" \
  "ds_read_b64_tr_b16 v[160:161], v84 offset:0\n" \
  "ds_read_b64_tr_b16 v[162:163], v84 offset:512\n" \
  "ds_read_b64_tr_b16 v[164:165], v85 offset:0\n" \
  "ds_read_b64_tr_b16 v[166:167], v85 offset:512\n" \
  "ds_read_b64_tr_b16 v[168:169], v86 offset:0\n" \
  "ds_read_b64_tr_b16 v[170:171], v86 offset:512\n" \
  "ds_read_b64_tr_b16 v[172:173], v84 offset:4096\n" \
  "ds_read_b64_tr_b16 v[174:175], v84 offset:4608\n" \
  "ds_read_b64_tr_b16 v[176:177], v85 offset:4096\n" \
  "ds_read_b64_tr_b16 v[178:179], v85 offset:4608\n" \
  "ds_read_b64_tr_b16 v[180:181], v86 offset:4096\n" \
  "ds_read_b64_tr_b16 v[182:183], v86 offset:4608\n" \
  "s_waitcnt vmcnt(12)\n" \
  "s_waitcnt lgkmcnt(0)\n" \
  "s_barrier\n" \
  "v_add_u32 v84, 24576, v30\n" \
  "v_add_u32 v85, 24576, v31\n" \
  "v_add_u32 v86, 24576, v32\n" \
  "ds_read_b64_tr_b16 v[184:185], v84 offset:0\n" \
  "ds_read_b64_tr_b16 v[186:187], v84 offset:512\n" \
  "ds_read_b64_tr_b16 v[188:189], v85 offset:0\n" \
  "ds_read_b64_tr_b16 v[190:191], v85 offset:512\n" \
  "ds_read_b64_tr_b16 v[192:193], v86 offset:0\n" \
  "ds_read_b64_tr_b16 v[194:195], v86 offset:512\n" \
  "ds_read_b64_tr_b16 v[196:197], v84 offset:4096\n" \
  "ds_read_b64_tr_b16 v[198:199], v84 offset:4608\n" \
  "ds_read_b64_tr_b16 v[200:201], v85 offset:4096\n" \
  "ds_read_b64_tr_b16 v[202:203], v85 offset:4608\n" \
  "ds_read_b64_tr_b16 v[204:205], v86 offset:4096\n" \
  "ds_read_b64_tr_b16 v[206:207], v86 offset:4608\n" \
  "s_waitcnt vmcnt(6)\n" \
  "s_waitcnt lgkmcnt(0)\n" \
  "s_barrier\n" \
  "v_add_u32 v84, 49152, v30\n" \
  "v_add_u32 v85, 49152, v31\n" \
  "v_add_u32 v86, 49152, v32\n" \
  "ds_read_b64_tr_b16 v[208:209], v84 offset:0\n" \
  "ds_read_b64_tr_b16 v[210:211], v84 offset:512\n" \
  "ds_read_b64_tr_b16 v[212:213], v85 offset:0\n" \
  "ds_read_b64_tr_b16 v[214:215], v85 offset:512\n" \
  "ds_read_b64_tr_b16 v[216:217], v86 offset:0\n" \
  "ds_read_b64_tr_b16 v[218:219], v86 offset:512\n" \
  "ds_read_b64_tr_b16 v[220:221], v84 offset:4096\n" \
  "ds_read_b64_tr_b16 v[222:223], v84 offset:4608\n" \
  "ds_read_b64_tr_b16 v[224:225], v85 offset:4096\n" \
  "ds_read_b64_tr_b16 v[226:227], v85 offset:4608\n" \
  "ds_read_b64_tr_b16 v[228:229], v86 offset:4096\n" \
  "ds_read_b64_tr_b16 v[230:231], v86 offset:4608\n" \
  "s_waitcnt vmcnt(0)\n" \
  "s_waitcnt lgkmcnt(0)\n" \
  "s_barrier\n" \
  "v_add_u32 v84, 73728, v30\n" \
  "v_add_u32 v85, 73728, v31\n" \
  "v_add_u32 v86, 73728, v32\n" \
  "ds_read_b64_tr_b16 v[232:233], v84 offset:0\n" \
  "ds_read_b64_tr_b16 v[234:235], v84 offset:512\n" \
  "ds_read_b64_tr_b16 v[236:237], v85 offset:0\n" \
  "ds_read_b64_tr_b16 v[238:239], v85 offset:512\n" \
  "ds_read_b64_tr_b16 v[240:241], v86 offset:0\n" \
  "ds_read_b64_tr_b16 v[242:243], v86 offset:512\n" \
  "ds_read_b64_tr_b16 v[244:245], v84 offset:4096\n" \
  "ds_read_b64_tr_b16 v[246:247], v84 offset:4608\n" \
  "ds_read_b64_tr_b16 v[248:249], v85 offset:4096\n" \
  "ds_read_b64_tr_b16 v[250:251], v85 offset:4608\n" \
  "ds_read_b64_tr_b16 v[252:253], v86 offset:4096\n" \
  "ds_read_b64_tr_b16 v[254:255], v86 offset:4608\n" \
  "s_waitcnt lgkmcnt(0)\n" \
  "s_barrier\n" \
  "v_readfirstlane_b32 s60, v5\n" \
  "s_nop 3\n" \
  "s_add_u32 s60, s60, 1\n" \
  "s_cmp_eq_u32 s60, 0\n" \
  "s_cselect_b32 s60, 1, s60\n" \
  "v_mov_b32 v52, 0x3f803f80\n" \
  "v_mov_b32 v53, 0x3f803f80\n" \
  "v_mov_b32 v54, 0x3f803f80\n" \
  "v_mov_b32 v55, 0x3f803f80\n" \
  "s_nop 1\n" \
  "v_mfma_f32_16x16x32_bf16 v[16:19], a[0:3], v[52:55], 0\n" \
  "v_mfma_f32_16x16x32_bf16 v[20:23], a[4:7], v[52:55], 0\n" \
  "v_mfma_f32_16x16x32_bf16 v[24:27], a[8:11], v[52:55], 0\n" \
  "v_mfma_f32_16x16x32_bf16 v[16:19], a[12:15], v[52:55], v[16:19]\n" \
  "v_mfma_f32_16x16x32_bf16 v[20:23], a[16:19], v[52:55], v[20:23]\n" \
  "v_mfma_f32_16x16x32_bf16 v[24:27], a[20:23], v[52:55], v[24:27]\n" \
  "v_mfma_f32_16x16x32_bf16 v[16:19], a[24:27], v[52:55], v[16:19]\n" \
  "v_mfma_f32_16x16x32_bf16 v[20:23], a[28:31], v[52:55], v[20:23]\n" \
  "v_mfma_f32_16x16x32_bf16 v[24:27], a[32:35], v[52:55], v[24:27]\n" \
  "v_mfma_f32_16x16x32_bf16 v[16:19], a[36:39], v[52:55], v[16:19]\n" \
  "v_mfma_f32_16x16x32_bf16 v[20:23], a[40:43], v[52:55], v[20:23]\n" \
  "v_mfma_f32_16x16x32_bf16 v[24:27], a[44:47], v[52:55], v[24:27]\n" \
  "v_mfma_f32_16x16x32_bf16 v[16:19], a[48:51], v[52:55], v[16:19]\n" \
  "v_mfma_f32_16x16x32_bf16 v[20:23], a[52:55], v[52:55], v[20:23]\n" \
  "v_mfma_f32_16x16x32_bf16 v[24:27], a[56:59], v[52:55], v[24:27]\n" \
  "v_mfma_f32_16x16x32_bf16 v[16:19], a[60:63], v[52:55], v[16:19]\n" \
  "v_mfma_f32_16x16x32_bf16 v[20:23], a[64:67], v[52:55], v[20:23]\n" \
  "v_mfma_f32_16x16x32_bf16 v[24:27], a[68:71], v[52:55], v[24:27]\n" \
  "v_mfma_f32_16x16x32_bf16 v[16:19], a[72:75], v[52:55], v[16:19]\n" \
  "v_mfma_f32_16x16x32_bf16 v[20:23], a[76:79], v[52:55], v[20:23]\n" \
  "v_mfma_f32_16x16x32_bf16 v[24:27], a[80:83], v[52:55], v[24:27]\n" \
  "v_mfma_f32_16x16x32_bf16 v[16:19], a[84:87], v[52:55], v[16:19]\n" \
  "v_mfma_f32_16x16x32_bf16 v[20:23], a[88:91], v[52:55], v[20:23]\n" \
  "v_mfma_f32_16x16x32_bf16 v[24:27], a[92:95], v[52:55], v[24:27]\n" \
  "v_mfma_f32_16x16x32_bf16 v[16:19], a[96:99], v[52:55], v[16:19]\n" \
  "v_mfma_f32_16x16x32_bf16 v[20:23], a[100:103], v[52:55], v[20:23]\n" \
  "v_mfma_f32_16x16x32_bf16 v[24:27], a[104:107], v[52:55], v[24:27]\n" \
  "v_mfma_f32_16x16x32_bf16 v[16:19], a[108:111], v[52:55], v[16:19]\n" \
  "v_mfma_f32_16x16x32_bf16 v[20:23], a[112:115], v[52:55], v[20:23]\n" \
  "v_mfma_f32_16x16x32_bf16 v[24:27], a[116:119], v[52:55], v[24:27]\n" \
  "v_mfma_f32_16x16x32_bf16 v[16:19], a[120:123], v[52:55], v[16:19]\n" \
  "v_mfma_f32_16x16x32_bf16 v[20:23], a[124:127], v[52:55], v[20:23]\n" \
  "v_mfma_f32_16x16x32_bf16 v[24:27], a[128:131], v[52:55], v[24:27]\n" \
  "v_mfma_f32_16x16x32_bf16 v[16:19], a[132:135], v[52:55], v[16:19]\n" \
  "v_mfma_f32_16x16x32_bf16 v[20:23], a[136:139], v[52:55], v[20:23]\n" \
  "v_mfma_f32_16x16x32_bf16 v[24:27], a[140:143], v[52:55], v[24:27]\n" \
  "v_mfma_f32_16x16x32_bf16 v[16:19], a[144:147], v[52:55], v[16:19]\n" \
  "v_mfma_f32_16x16x32_bf16 v[20:23], a[148:151], v[52:55], v[20:23]\n" \
  "v_mfma_f32_16x16x32_bf16 v[24:27], a[152:155], v[52:55], v[24:27]\n" \
  "v_mfma_f32_16x16x32_bf16 v[16:19], a[156:159], v[52:55], v[16:19]\n" \
  "v_mfma_f32_16x16x32_bf16 v[20:23], a[160:163], v[52:55], v[20:23]\n" \
  "v_mfma_f32_16x16x32_bf16 v[24:27], a[164:167], v[52:55], v[24:27]\n" \
  "v_mfma_f32_16x16x32_bf16 v[16:19], a[168:171], v[52:55], v[16:19]\n" \
  "v_mfma_f32_16x16x32_bf16 v[20:23], a[172:175], v[52:55], v[20:23]\n" \
  "v_mfma_f32_16x16x32_bf16 v[24:27], a[176:179], v[52:55], v[24:27]\n" \
  "v_mfma_f32_16x16x32_bf16 v[16:19], a[180:183], v[52:55], v[16:19]\n" \
  "v_mfma_f32_16x16x32_bf16 v[20:23], a[184:187], v[52:55], v[20:23]\n" \
  "v_mfma_f32_16x16x32_bf16 v[24:27], a[188:191], v[52:55], v[24:27]\n" \
  "v_mfma_f32_16x16x32_bf16 v[16:19], a[192:195], v[52:55], v[16:19]\n" \
  "v_mfma_f32_16x16x32_bf16 v[20:23], a[196:199], v[52:55], v[20:23]\n" \
  "v_mfma_f32_16x16x32_bf16 v[24:27], a[200:203], v[52:55], v[24:27]\n" \
  "v_mfma_f32_16x16x32_bf16 v[16:19], a[204:207], v[52:55], v[16:19]\n" \
  "v_mfma_f32_16x16x32_bf16 v[20:23], a[208:211], v[52:55], v[20:23]\n" \
  "v_mfma_f32_16x16x32_bf16 v[24:27], a[212:215], v[52:55], v[24:27]\n" \
  "v_mfma_f32_16x16x32_bf16 v[16:19], a[216:219], v[52:55], v[16:19]\n" \
  "v_mfma_f32_16x16x32_bf16 v[20:23], a[220:223], v[52:55], v[20:23]\n" \
  "v_mfma_f32_16x16x32_bf16 v[24:27], a[224:227], v[52:55], v[24:27]\n" \
  "v_mfma_f32_16x16x32_bf16 v[16:19], a[228:231], v[52:55], v[16:19]\n" \
  "v_mfma_f32_16x16x32_bf16 v[20:23], a[232:235], v[52:55], v[20:23]\n" \
  "v_mfma_f32_16x16x32_bf16 v[24:27], a[236:239], v[52:55], v[24:27]\n" \
  "v_mfma_f32_16x16x32_bf16 v[16:19], a[240:243], v[52:55], v[16:19]\n" \
  "v_mfma_f32_16x16x32_bf16 v[20:23], a[244:247], v[52:55], v[20:23]\n" \
  "v_mfma_f32_16x16x32_bf16 v[24:27], a[248:251], v[52:55], v[24:27]\n" \
  "v_mfma_f32_16x16x32_bf16 v[16:19], a[252:255], v[52:55], v[16:19]\n" \
  "v_mfma_f32_16x16x32_bf16 v[20:23], v[128:131], v[52:55], v[20:23]\n" \
  "v_mfma_f32_16x16x32_bf16 v[24:27], v[132:135], v[52:55], v[24:27]\n" \
  "v_mfma_f32_16x16x32_bf16 v[16:19], v[136:139], v[52:55], v[16:19]\n" \
  "v_mfma_f32_16x16x32_bf16 v[20:23], v[140:143], v[52:55], v[20:23]\n" \
  "v_mfma_f32_16x16x32_bf16 v[24:27], v[144:147], v[52:55], v[24:27]\n" \
  "v_mfma_f32_16x16x32_bf16 v[16:19], v[148:151], v[52:55], v[16:19]\n" \
  "v_mfma_f32_16x16x32_bf16 v[20:23], v[152:155], v[52:55], v[20:23]\n" \
  "v_mfma_f32_16x16x32_bf16 v[24:27], v[156:159], v[52:55], v[24:27]\n" \
  "v_mfma_f32_16x16x32_bf16 v[16:19], v[160:163], v[52:55], v[16:19]\n" \
  "v_mfma_f32_16x16x32_bf16 v[20:23], v[164:167], v[52:55], v[20:23]\n" \
  "v_mfma_f32_16x16x32_bf16 v[24:27], v[168:171], v[52:55], v[24:27]\n" \
  "v_mfma_f32_16x16x32_bf16 v[16:19], v[172:175], v[52:55], v[16:19]\n" \
  "v_mfma_f32_16x16x32_bf16 v[20:23], v[176:179], v[52:55], v[20:23]\n" \
  "v_mfma_f32_16x16x32_bf16 v[24:27], v[180:183], v[52:55], v[24:27]\n" \
  "v_mfma_f32_16x16x32_bf16 v[16:19], v[184:187], v[52:55], v[16:19]\n" \
  "v_mfma_f32_16x16x32_bf16 v[20:23], v[188:191], v[52:55], v[20:23]\n" \
  "v_mfma_f32_16x16x32_bf16 v[24:27], v[192:195], v[52:55], v[24:27]\n" \
  "v_mfma_f32_16x16x32_bf16 v[16:19], v[196:199], v[52:55], v[16:19]\n" \
  "v_mfma_f32_16x16x32_bf16 v[20:23], v[200:203], v[52:55], v[20:23]\n" \
  "v_mfma_f32_16x16x32_bf16 v[24:27], v[204:207], v[52:55], v[24:27]\n" \
  "v_mfma_f32_16x16x32_bf16 v[16:19], v[208:211], v[52:55], v[16:19]\n" \
  "v_mfma_f32_16x16x32_bf16 v[20:23], v[212:215], v[52:55], v[20:23]\n" \
  "v_mfma_f32_16x16x32_bf16 v[24:27], v[216:219], v[52:55], v[24:27]\n" \
  "v_mfma_f32_16x16x32_bf16 v[16:19], v[220:223], v[52:55], v[16:19]\n" \
  "v_mfma_f32_16x16x32_bf16 v[20:23], v[224:227], v[52:55], v[20:23]\n" \
  "v_mfma_f32_16x16x32_bf16 v[24:27], v[228:231], v[52:55], v[24:27]\n" \
  "v_mfma_f32_16x16x32_bf16 v[16:19], v[232:235], v[52:55], v[16:19]\n" \
  "v_mfma_f32_16x16x32_bf16 v[20:23], v[236:239], v[52:55], v[20:23]\n" \
  "v_mfma_f32_16x16x32_bf16 v[24:27], v[240:243], v[52:55], v[24:27]\n" \
  "v_mfma_f32_16x16x32_bf16 v[16:19], v[244:247], v[52:55], v[16:19]\n" \
  "v_mfma_f32_16x16x32_bf16 v[20:23], v[248:251], v[52:55], v[20:23]\n" \
  "v_mfma_f32_16x16x32_bf16 v[24:27], v[252:255], v[52:55], v[24:27]\n" \
  "v_mov_b32 v5, s60\n" \
  "s_mov_b32 s59, 536870912\n" \
  "s_add_u32 s57, s56, 131072\n" \
  "s_mov_b32 s79, 0\n" \
  "s_mov_b32 s75, 0x3a800000\n" \
  "s_mov_b32 s76, 0xc0135761\n" \
  "s_mov_b32 s77, 0x3d372713\n" \
  "s_mov_b32 s44, s59\n" \
  "s_mov_b32 s45, s59\n" \
  "s_mov_b32 s46, s59\n" \
  "s_mov_b32 s47, s59\n" \
  "s_mov_b32 s48, s83\n" \
  "s_add_u32 s49, s48, s55\n" \
  "s_cmp_eq_u32 s49, s56\n" \
  "s_cselect_b32 s49, s57, s49\n" \
  "s_cmp_eq_u32 s49, s58\n" \
  "s_cselect_b32 s49, s59, s49\n" \
  "s_add_u32 s50, s49, s55\n" \
  "s_cmp_eq_u32 s50, s56\n" \
  "s_cselect_b32 s50, s57, s50\n" \
  "s_cmp_eq_u32 s50, s58\n" \
  "s_cselect_b32 s50, s59, s50\n" \
  "s_add_u32 s51, s50, s55\n" \
  "s_cmp_eq_u32 s51, s56\n" \
  "s_cselect_b32 s51, s57, s51\n" \
  "s_cmp_eq_u32 s51, s58\n" \
  "s_cselect_b32 s51, s59, s51\n" \
  "s_add_u32 s52, s51, s55\n" \
  "s_cmp_eq_u32 s52, s56\n" \
  "s_cselect_b32 s52, s57, s52\n" \
  "s_cmp_eq_u32 s52, s58\n" \
  "s_cselect_b32 s52, s59, s52\n" \
  "s_lshl_b32 s63, s54, 12\n" \
  "s_add_u32 s63, s63, 65536\n" \
  "s_add_u32 s64, s63, 32768\n" \
  "v_add_u32 v1, 16384, v0\n" \
  "s_lshl_b32 s88, s54, 10\n" \
  "s_add_u32 s88, s88, 131072\n" \
  "s_lshl_b32 s78, s54, 11\n" \
  "s_add_u32 s78, s78, 132608\n" \
  "v_mov_b32 v13, 0x3727c5ac\n" \
  "s_mov_b32 s66, s59\n" \
  "s_branch Lg3_slow0_%=\n" \
  "Lg3_d0_%=:\n" \
  "v_mfma_f32_16x16x32_bf16 v[28:31], a[0:3], v[52:55], 0\n" \
  "s_mov_b32 m0, s62\n" \
  "ds_read_b128 v[72:75], v2 offset:5120\n" \
  "v_mul_f32 v116, s75, v112\n" \
  "v_mfma_f32_16x16x32_bf16 v[32:35], a[4:7], v[52:55], 0\n" \
  "buffer_load_dwordx4 v0, s[36:39], s61 offen offset:0 sc1 lds\n" \
  "v_mfma_f32_16x16x32_bf16 v[36:39], a[8:11], v[52:55], 0\n" \
  "s_mul_i32 s73, s47, 7\n" \
  "v_fma_f32 v113, v113, s75, v13\n" \
  "v_fma_f32 v113, -v116, v116, v113\n" \
  "v_mfma_f32_16x16x32_bf16 v[28:31], a[12:15], v[56:59], v[28:31]\n" \
  "s_lshr_b32 s73, s73, 1\n" \
  "ds_read_b128 v[76:79], v2 offset:6144\n" \
  "v_max_f32 v113, v113, v13\n" \
  "v_mfma_f32_16x16x32_bf16 v[32:35], a[16:19], v[56:59], v[32:35]\n" \
  "s_add_u32 s53, s52, s55\n" \
  "v_rsq_f32 v112, v113\n" \
  "v_mfma_f32_16x16x32_bf16 v[36:39], a[20:23], v[56:59], v[36:39]\n" \
  "s_waitcnt lgkmcnt(3)\n" \
  "buffer_load_dwordx4 v0, s[36:39], s61 offen offset:1024 sc1 lds\n" \
  "v_mfma_f32_16x16x32_bf16 v[28:31], a[24:27], v[60:63], v[28:31]\n" \
  "s_cmp_eq_u32 s53, s56\n" \
  "ds_read_b128 v[80:83], v2 offset:7168\n" \
  "v_mul_f32 v117, s76, v112\n" \
  "v_mfma_f32_16x16x32_bf16 v[32:35], a[28:31], v[60:63], v[32:35]\n" \
  "s_cselect_b32 s53, s57, s53\n" \
  "v_mul_f32 v118, v112, v112\n" \
  "v_mul_f32 v119, v113, v112\n" \
  "v_mfma_f32_16x16x32_bf16 v[36:39], a[32:35], v[60:63], v[36:39]\n" \
  "s_cmp_eq_u32 s53, s58\n" \
  "v_mul_f32 v118, s77, v118\n" \
  "v_mul_f32 v118, v118, v117\n" \
  "v_mfma_f32_16x16x32_bf16 v[28:31], a[36:39], v[64:67], v[28:31]\n" \
  "s_cselect_b32 s53, s59, s53\n" \
  "ds_read_b128 v[52:55], v2 offset:8192\n" \
  "v_fma_f32 v40, -v116, v16, v40\n" \
  "v_mfma_f32_16x16x32_bf16 v[32:35], a[40:43], v[64:67], v[32:35]\n" \
  "buffer_load_dwordx4 v0, s[36:39], s61 offen offset:2048 sc1 lds\n" \
  "v_mfma_f32_16x16x32_bf16 v[36:39], a[44:47], v[64:67], v[36:39]\n" \
  "s_waitcnt lgkmcnt(3)\n" \
  "v_fma_f32 v41, -v116, v17, v41\n" \
  "v_fma_f32 v42, -v116, v18, v42\n" \
  "v_mfma_f32_16x16x32_bf16 v[28:31], a[48:51], v[68:71], v[28:31]\n" \
  "s_lshr_b32 s68, s50, 5\n" \
  "ds_read_b128 v[56:59], v2 offset:9216\n" \
  "v_fma_f32 v43, -v116, v19, v43\n" \
  "v_mfma_f32_16x16x32_bf16 v[32:35], a[52:55], v[68:71], v[32:35]\n" \
  "s_mul_i32 s70, s46, 5\n" \
  "v_mul_f32 v84, v40, v40\n" \
  "v_mul_f32 v85, v41, v41\n" \
  "v_mfma_f32_16x16x32_bf16 v[36:39], a[56:59], v[68:71], v[36:39]\n" \
  "buffer_load_dwordx4 v0, s[36:39], s61 offen offset:3072 sc1 lds\n" \
  "v_mfma_f32_16x16x32_bf16 v[28:31], a[60:63], v[72:75], v[28:31]\n" \
  "s_lshr_b32 s70, s70, 6\n" \
  "ds_read_b128 v[60:63], v2 offset:10240\n" \
  "v_mul_f32 v86, v42, v42\n" \
  "v_mfma_f32_16x16x32_bf16 v[32:35], a[64:67], v[72:75], v[32:35]\n" \
  "s_add_u32 m0, s78, 0\n" \
  "v_mul_f32 v87, v43, v43\n" \
  "v_fma_f32 v84, v118, v84, v117\n" \
  "v_mfma_f32_16x16x32_bf16 v[36:39], a[68:71], v[72:75], v[36:39]\n" \
  "s_waitcnt lgkmcnt(3)\n" \
  "v_mul_f32 v84, v40, v84\n" \
  "v_fma_f32 v85, v118, v85, v117\n" \
  "v_mfma_f32_16x16x32_bf16 v[28:31], a[72:75], v[76:79], v[28:31]\n" \
  "ds_read_b128 v[64:67], v2 offset:11264\n" \
  "v_mul_f32 v85, v41, v85\n" \
  "v_mfma_f32_16x16x32_bf16 v[32:35], a[76:79], v[76:79], v[32:35]\n" \
  "v_exp_f32 v84, v84\n" \
  "v_mfma_f32_16x16x32_bf16 v[36:39], a[80:83], v[76:79], v[36:39]\n" \
  "v_exp_f32 v85, v85\n" \
  "v_mfma_f32_16x16x32_bf16 v[28:31], a[84:87], v[80:83], v[28:31]\n" \
  "ds_read_b128 v[68:71], v2 offset:12288\n" \
  "v_fma_f32 v84, v84, v119, v119\n" \
  "v_mfma_f32_16x16x32_bf16 v[32:35], a[88:91], v[80:83], v[32:35]\n" \
  "v_rcp_f32 v84, v84\n" \
  "v_mfma_f32_16x16x32_bf16 v[36:39], a[92:95], v[80:83], v[36:39]\n" \
  "s_waitcnt lgkmcnt(3)\n" \
  "v_fma_f32 v85, v85, v119, v119\n" \
  "v_fma_f32 v86, v118, v86, v117\n" \
  "v_mfma_f32_16x16x32_bf16 v[28:31], a[96:99], v[52:55], v[28:31]\n" \
  "ds_read_b128 v[72:75], v2 offset:13312\n" \
  "v_mul_f32 v86, v42, v86\n" \
  "v_mfma_f32_16x16x32_bf16 v[32:35], a[100:103], v[52:55], v[32:35]\n" \
  "v_exp_f32 v86, v86\n" \
  "v_mfma_f32_16x16x32_bf16 v[36:39], a[104:107], v[52:55], v[36:39]\n" \
  "v_rcp_f32 v85, v85\n" \
  "v_mfma_f32_16x16x32_bf16 v[28:31], a[108:111], v[56:59], v[28:31]\n" \
  "ds_read_b128 v[76:79], v2 offset:14336\n" \
  "v_fma_f32 v86, v86, v119, v119\n" \
  "v_mfma_f32_16x16x32_bf16 v[32:35], a[112:115], v[56:59], v[32:35]\n" \
  "v_rcp_f32 v86, v86\n" \
  "v_mfma_f32_16x16x32_bf16 v[36:39], a[116:119], v[56:59], v[36:39]\n" \
  "s_waitcnt lgkmcnt(3)\n" \
  "v_fma_f32 v87, v118, v87, v117\n" \
  "v_mul_f32 v87, v43, v87\n" \
  "v_mfma_f32_16x16x32_bf16 v[28:31], a[120:123], v[60:63], v[28:31]\n" \
  "ds_read_b128 v[80:83], v2 offset:15360\n" \
  "v_mul_f32 v40, v40, v84\n" \
  "v_mfma_f32_16x16x32_bf16 v[32:35], a[124:127], v[60:63], v[32:35]\n" \
  "s_waitcnt vmcnt(7)\n" \
  "v_exp_f32 v87, v87\n" \
  "v_mfma_f32_16x16x32_bf16 v[36:39], a[128:131], v[60:63], v[36:39]\n" \
  "s_barrier\n" \
  "v_mfma_f32_16x16x32_bf16 v[28:31], a[132:135], v[64:67], v[28:31]\n" \
  "ds_read_b128 v[52:55], v2 offset:16384\n" \
  "v_fma_f32 v87, v87, v119, v119\n" \
  "v_mfma_f32_16x16x32_bf16 v[32:35], a[136:139], v[64:67], v[32:35]\n" \
  "v_rcp_f32 v87, v87\n" \
  "v_mfma_f32_16x16x32_bf16 v[36:39], a[140:143], v[64:67], v[36:39]\n" \
  "s_waitcnt lgkmcnt(3)\n" \
  "v_add_f32_dpp v104, v104, v104 quad_perm:[1,0,3,2] row_mask:0xf bank_mask:0xf\n" \
  "v_mul_f32 v41, v41, v85\n" \
  "v_mfma_f32_16x16x32_bf16 v[28:31], a[144:147], v[68:71], v[28:31]\n" \
  "ds_read_b128 v[56:59], v2 offset:17408\n" \
  "v_add_f32_dpp v104, v104, v104 quad_perm:[2,3,0,1] row_mask:0xf bank_mask:0xf\n" \
  "v_mfma_f32_16x16x32_bf16 v[32:35], a[148:151], v[68:71], v[32:35]\n" \
  "s_nop 0\n" \
  "v_add_f32_dpp v104, v104, v104 row_half_mirror row_mask:0xf bank_mask:0xf\n" \
  "v_mul_f32 v42, v42, v86\n" \
  "v_mfma_f32_16x16x32_bf16 v[36:39], a[152:155], v[68:71], v[36:39]\n" \
  "ds_write_b32 v11, v104 offset:0\n" \
  "v_mfma_f32_16x16x32_bf16 v[28:31], a[156:159], v[72:75], v[28:31]\n" \
  "ds_read_b128 v[60:63], v2 offset:18432\n" \
  "v_mul_f32 v43, v43, v87\n" \
  "v_mfma_f32_16x16x32_bf16 v[32:35], a[160:163], v[72:75], v[32:35]\n" \
  "buffer_load_dword v6, s[36:39], s68 offen offset:0 sc1 lds\n" \
  "v_mfma_f32_16x16x32_bf16 v[36:39], a[164:167], v[72:75], v[36:39]\n" \
  "s_waitcnt lgkmcnt(4)\n" \
  "v_cvt_pk_bf16_f32 v96, v40, v41\n" \
  "v_cvt_pk_bf16_f32 v97, v42, v43\n" \
  "v_mfma_f32_16x16x32_bf16 v[28:31], a[168:171], v[76:79], v[28:31]\n" \
  "s_add_u32 m0, s62, 16384\n" \
  "ds_read_b128 v[64:67], v2 offset:19456\n" \
  "v_fma_f32 v44, -v116, v20, v44\n" \
  "v_mfma_f32_16x16x32_bf16 v[32:35], a[172:175], v[76:79], v[32:35]\n" \
  "buffer_load_dwordx4 v1, s[36:39], s61 offen offset:0 sc1 lds\n" \
  "v_mfma_f32_16x16x32_bf16 v[36:39], a[176:179], v[76:79], v[36:39]\n" \
  "s_lshr_b32 s72, s61, 5\n" \
  "v_fma_f32 v45, -v116, v21, v45\n" \
  "v_fma_f32 v46, -v116, v22, v46\n" \
  "v_mfma_f32_16x16x32_bf16 v[28:31], a[180:183], v[80:83], v[28:31]\n" \
  "s_mov_b32 s62, s63\n" \
  "ds_read_b128 v[68:71], v2 offset:20480\n" \
  "v_fma_f32 v47, -v116, v23, v47\n" \
  "v_mfma_f32_16x16x32_bf16 v[32:35], a[184:187], v[80:83], v[32:35]\n" \
  "v_mul_f32 v88, v44, v44\n" \
  "v_mul_f32 v89, v45, v45\n" \
  "v_mfma_f32_16x16x32_bf16 v[36:39], a[188:191], v[80:83], v[36:39]\n" \
  "s_waitcnt lgkmcnt(4)\n" \
  "buffer_load_dwordx4 v1, s[36:39], s61 offen offset:1024 sc1 lds\n" \
  "v_mfma_f32_16x16x32_bf16 v[28:31], a[192:195], v[52:55], v[28:31]\n" \
  "ds_read_b128 v[72:75], v2 offset:21504\n" \
  "v_mul_f32 v90, v46, v46\n" \
  "v_mfma_f32_16x16x32_bf16 v[32:35], a[196:199], v[52:55], v[32:35]\n" \
  "v_mul_f32 v91, v47, v47\n" \
  "v_fma_f32 v88, v118, v88, v117\n" \
  "v_mfma_f32_16x16x32_bf16 v[36:39], a[200:203], v[52:55], v[36:39]\n" \
  "v_mul_f32 v88, v44, v88\n" \
  "v_fma_f32 v89, v118, v89, v117\n" \
  "v_mfma_f32_16x16x32_bf16 v[28:31], a[204:207], v[56:59], v[28:31]\n" \
  "ds_read_b128 v[76:79], v2 offset:22528\n" \
  "v_mul_f32 v89, v45, v89\n" \
  "v_mfma_f32_16x16x32_bf16 v[32:35], a[208:211], v[56:59], v[32:35]\n" \
  "buffer_load_dwordx4 v1, s[36:39], s61 offen offset:2048 sc1 lds\n" \
  "v_mfma_f32_16x16x32_bf16 v[36:39], a[212:215], v[56:59], v[36:39]\n" \
  "s_waitcnt lgkmcnt(3)\n" \
  "v_exp_f32 v88, v88\n" \
  "v_mfma_f32_16x16x32_bf16 v[28:31], a[216:219], v[60:63], v[28:31]\n" \
  "ds_read_b128 v[80:83], v2 offset:23552\n" \
  "v_fma_f32 v88, v88, v119, v119\n" \
  "v_mfma_f32_16x16x32_bf16 v[32:35], a[220:223], v[60:63], v[32:35]\n" \
  "v_exp_f32 v89, v89\n" \
  "v_mfma_f32_16x16x32_bf16 v[36:39], a[224:227], v[60:63], v[36:39]\n" \
  "buffer_load_dwordx4 v1, s[36:39], s61 offen offset:3072 sc1 lds\n" \
  "v_mfma_f32_16x16x32_bf16 v[28:31], a[228:231], v[64:67], v[28:31]\n" \
  "ds_read_b128 v[52:55], v2 offset:24576\n" \
  "v_fma_f32 v89, v89, v119, v119\n" \
  "v_mfma_f32_16x16x32_bf16 v[32:35], a[232:235], v[64:67], v[32:35]\n" \
  "buffer_load_dword v108, v9, s[36:39], s72 offen offset:0 sc1\n" \
  "v_mfma_f32_16x16x32_bf16 v[36:39], a[236:239], v[64:67], v[36:39]\n" \
  "s_waitcnt lgkmcnt(3)\n" \
  "v_rcp_f32 v88, v88\n" \
  "v_mfma_f32_16x16x32_bf16 v[28:31], a[240:243], v[68:71], v[28:31]\n" \
  "ds_read_b128 v[56:59], v2 offset:25600\n" \
  "v_fma_f32 v90, v118, v90, v117\n" \
  "v_mfma_f32_16x16x32_bf16 v[32:35], a[244:247], v[68:71], v[32:35]\n" \
  "v_rcp_f32 v89, v89\n" \
  "v_mfma_f32_16x16x32_bf16 v[36:39], a[248:251], v[68:71], v[36:39]\n" \
  "v_mul_f32 v90, v46, v90\n" \
  "v_fma_f32 v91, v118, v91, v117\n" \
  "v_mfma_f32_16x16x32_bf16 v[28:31], a[252:255], v[72:75], v[28:31]\n" \
  "ds_read_b128 v[60:63], v2 offset:26624\n" \
  "v_mul_f32 v91, v47, v91\n" \
  "v_mfma_f32_16x16x32_bf16 v[32:35], v[128:131], v[72:75], v[32:35]\n" \
  "v_exp_f32 v90, v90\n" \
  "v_mfma_f32_16x16x32_bf16 v[36:39], v[132:135], v[72:75], v[36:39]\n" \
  "s_waitcnt lgkmcnt(3)\n" \
  "v_exp_f32 v91, v91\n" \
  "v_mfma_f32_16x16x32_bf16 v[28:31], v[136:139], v[76:79], v[28:31]\n" \
  "ds_read_b128 v[64:67], v2 offset:27648\n" \
  "v_fma_f32 v90, v90, v119, v119\n" \
  "v_mfma_f32_16x16x32_bf16 v[32:35], v[140:143], v[76:79], v[32:35]\n" \
  "v_rcp_f32 v90, v90\n" \
  "v_mfma_f32_16x16x32_bf16 v[36:39], v[144:147], v[76:79], v[36:39]\n" \
  "v_fma_f32 v91, v91, v119, v119\n" \
  "v_mul_f32 v44, v44, v88\n" \
  "v_mfma_f32_16x16x32_bf16 v[28:31], v[148:151], v[80:83], v[28:31]\n" \
  "ds_read_b128 v[68:71], v2 offset:28672\n" \
  "v_mul_f32 v45, v45, v89\n" \
  "v_mfma_f32_16x16x32_bf16 v[32:35], v[152:155], v[80:83], v[32:35]\n" \
  "v_rcp_f32 v91, v91\n" \
  "v_mfma_f32_16x16x32_bf16 v[36:39], v[156:159], v[80:83], v[36:39]\n" \
  "s_waitcnt lgkmcnt(3)\n" \
  "v_mul_f32 v46, v46, v90\n" \
  "v_mul_f32 v47, v47, v91\n" \
  "v_mfma_f32_16x16x32_bf16 v[28:31], v[160:163], v[52:55], v[28:31]\n" \
  "ds_read_b128 v[72:75], v2 offset:29696\n" \
  "v_cvt_pk_bf16_f32 v98, v44, v45\n" \
  "v_mfma_f32_16x16x32_bf16 v[32:35], v[164:167], v[52:55], v[32:35]\n" \
  "v_cvt_pk_bf16_f32 v99, v46, v47\n" \
  "v_fma_f32 v48, -v116, v24, v48\n" \
  "v_mfma_f32_16x16x32_bf16 v[36:39], v[168:171], v[52:55], v[36:39]\n" \
  "buffer_store_dwordx4 v[96:99], v3, s[36:39], s73 offen offset:0 sc0 sc1\n" \
  "v_fma_f32 v49, -v116, v25, v49\n" \
  "v_fma_f32 v50, -v116, v26, v50\n" \
  "v_fma_f32 v51, -v116, v27, v51\n" \
  "v_mfma_f32_16x16x32_bf16 v[28:31], v[172:175], v[56:59], v[28:31]\n" \
  "ds_read_b128 v[76:79], v2 offset:30720\n" \
  "v_mul_f32 v92, v48, v48\n" \
  "v_mfma_f32_16x16x32_bf16 v[32:35], v[176:179], v[56:59], v[32:35]\n" \
  "v_mul_f32 v93, v49, v49\n" \
  "v_mul_f32 v94, v50, v50\n" \
  "v_mfma_f32_16x16x32_bf16 v[36:39], v[180:183], v[56:59], v[36:39]\n" \
  "s_waitcnt lgkmcnt(3)\n" \
  "v_mul_f32 v95, v51, v51\n" \
  "v_fma_f32 v92, v118, v92, v117\n" \
  "v_mfma_f32_16x16x32_bf16 v[28:31], v[184:187], v[60:63], v[28:31]\n" \
  "ds_read_b128 v[80:83], v2 offset:31744\n" \
  "v_mul_f32 v92, v48, v92\n" \
  "v_mfma_f32_16x16x32_bf16 v[32:35], v[188:191], v[60:63], v[32:35]\n" \
  "s_waitcnt vmcnt(6)\n" \
  "v_exp_f32 v92, v92\n" \
  "v_mfma_f32_16x16x32_bf16 v[36:39], v[192:195], v[60:63], v[36:39]\n" \
  "s_barrier\n" \
  "v_mfma_f32_16x16x32_bf16 v[28:31], v[196:199], v[64:67], v[28:31]\n" \
  "ds_read_b128 v[52:55], v2 offset:32768\n" \
  "ds_read_b32 v14, v8 offset:0\n" \
  "v_mfma_f32_16x16x32_bf16 v[32:35], v[200:203], v[64:67], v[32:35]\n" \
  "buffer_store_dword v5, v4, s[36:39], s70 offen offset:0 sc0 sc1\n" \
  "ds_read_b64 v[112:113], v12 offset:0\n" \
  "v_fma_f32 v92, v92, v119, v119\n" \
  "v_fma_f32 v93, v118, v93, v117\n" \
  "v_mfma_f32_16x16x32_bf16 v[36:39], v[204:207], v[64:67], v[36:39]\n" \
  "s_waitcnt lgkmcnt(5)\n" \
  "v_rcp_f32 v92, v92\n" \
  "v_mfma_f32_16x16x32_bf16 v[28:31], v[208:211], v[68:71], v[28:31]\n" \
  "ds_read_b128 v[56:59], v2 offset:33792\n" \
  "v_mul_f32 v93, v49, v93\n" \
  "v_mfma_f32_16x16x32_bf16 v[32:35], v[212:215], v[68:71], v[32:35]\n" \
  "s_waitcnt lgkmcnt(2)\n" \
  "v_exp_f32 v93, v93\n" \
  "v_mfma_f32_16x16x32_bf16 v[36:39], v[216:219], v[68:71], v[36:39]\n" \
  "s_waitcnt lgkmcnt(1)\n" \
  "v_cmp_eq_u32 vcc, s60, v14\n" \
  "v_fma_f32 v93, v93, v119, v119\n" \
  "v_rcp_f32 v93, v93\n" \
  "v_mfma_f32_16x16x32_bf16 v[28:31], v[220:223], v[72:75], v[28:31]\n" \
  "s_cmp_eq_u32 vcc_lo, -1\n" \
  "ds_read_b128 v[60:63], v2 offset:34816\n" \
  "v_fma_f32 v94, v118, v94, v117\n" \
  "v_mfma_f32_16x16x32_bf16 v[32:35], v[224:227], v[72:75], v[32:35]\n" \
  "s_cselect_b32 s67, s50, s59\n" \
  "v_mul_f32 v94, v50, v94\n" \
  "v_fma_f32 v95, v118, v95, v117\n" \
  "v_mfma_f32_16x16x32_bf16 v[36:39], v[228:231], v[72:75], v[36:39]\n" \
  "v_exp_f32 v94, v94\n" \
  "v_mfma_f32_16x16x32_bf16 v[28:31], v[232:235], v[76:79], v[28:31]\n" \
  "s_max_u32 s67, s67, s66\n" \
  "ds_read_b128 v[64:67], v2 offset:35840\n" \
  "v_mul_f32 v95, v51, v95\n" \
  "v_exp_f32 v95, v95\n" \
  "v_mfma_f32_16x16x32_bf16 v[32:35], v[236:239], v[76:79], v[32:35]\n" \
  "s_lshr_b32 s61, s67, 0\n" \
  "v_fma_f32 v94, v94, v119, v119\n" \
  "v_fma_f32 v95, v95, v119, v119\n" \
  "v_rcp_f32 v94, v94\n" \
  "v_rcp_f32 v95, v95\n" \
  "v_mfma_f32_16x16x32_bf16 v[36:39], v[240:243], v[76:79], v[36:39]\n" \
  "s_xor_b32 s63, s63, 65536\n" \
  "v_mul_f32 v48, v48, v92\n" \
  "v_mul_f32 v49, v49, v93\n" \
  "v_mul_f32 v50, v50, v94\n" \
  "v_mul_f32 v51, v51, v95\n" \
  "v_cvt_pk_bf16_f32 v100, v48, v49\n" \
  "v_cvt_pk_bf16_f32 v101, v50, v51\n" \
  "v_mfma_f32_16x16x32_bf16 v[28:31], v[244:247], v[80:83], v[28:31]\n" \
  "s_cmp_ge_u32 s66, s59\n" \
  "ds_read_b128 v[68:71], v2 offset:36864\n" \
  "buffer_store_dwordx2 v[100:101], v10, s[36:39], s73 offen offset:0 sc0 sc1\n" \
  "v_mfma_f32_16x16x32_bf16 v[32:35], v[248:251], v[80:83], v[32:35]\n" \
  "s_waitcnt lgkmcnt(3)\n" \
  "v_mfma_f32_16x16x32_bf16 v[36:39], v[252:255], v[80:83], v[36:39]\n" \
  "s_cbranch_scc1 Lg3_slow1_%=\n" \
  "Lg3_d1_%=:\n" \
  "v_mfma_f32_16x16x32_bf16 v[40:43], a[0:3], v[52:55], 0\n" \
  "s_mov_b32 m0, s62\n" \
  "ds_read_b128 v[72:75], v2 offset:37888\n" \
  "v_mul_f32 v116, s75, v112\n" \
  "v_mfma_f32_16x16x32_bf16 v[44:47], a[4:7], v[52:55], 0\n" \
  "buffer_load_dwordx4 v0, s[36:39], s61 offen offset:0 sc1 lds\n" \
  "v_mfma_f32_16x16x32_bf16 v[48:51], a[8:11], v[52:55], 0\n" \
  "s_mul_i32 s73, s48, 7\n" \
  "v_fma_f32 v113, v113, s75, v13\n" \
  "v_fma_f32 v113, -v116, v116, v113\n" \
  "v_mfma_f32_16x16x32_bf16 v[40:43], a[12:15], v[56:59], v[40:43]\n" \
  "s_lshr_b32 s73, s73, 1\n" \
  "ds_read_b128 v[76:79], v2 offset:38912\n" \
  "v_max_f32 v113, v113, v13\n" \
  "v_mfma_f32_16x16x32_bf16 v[44:47], a[16:19], v[56:59], v[44:47]\n" \
  "s_add_u32 s74, s53, s55\n" \
  "v_rsq_f32 v112, v113\n" \
  "v_mfma_f32_16x16x32_bf16 v[48:51], a[20:23], v[56:59], v[48:51]\n" \
  "s_waitcnt lgkmcnt(3)\n" \
  "buffer_load_dwordx4 v0, s[36:39], s61 offen offset:1024 sc1 lds\n" \
  "v_mfma_f32_16x16x32_bf16 v[40:43], a[24:27], v[60:63], v[40:43]\n" \
  "s_cmp_eq_u32 s74, s56\n" \
  "ds_read_b128 v[80:83], v2 offset:39936\n" \
  "v_mul_f32 v117, s76, v112\n" \
  "v_mfma_f32_16x16x32_bf16 v[44:47], a[28:31], v[60:63], v[44:47]\n" \
  "s_cselect_b32 s74, s57, s74\n" \
  "v_mul_f32 v118, v112, v112\n" \
  "v_mul_f32 v119, v113, v112\n" \
  "v_mfma_f32_16x16x32_bf16 v[48:51], a[32:35], v[60:63], v[48:51]\n" \
  "s_cmp_eq_u32 s74, s58\n" \
  "v_mul_f32 v118, s77, v118\n" \
  "v_mul_f32 v118, v118, v117\n" \
  "v_mfma_f32_16x16x32_bf16 v[40:43], a[36:39], v[64:67], v[40:43]\n" \
  "s_cselect_b32 s74, s59, s74\n" \
  "ds_read_b128 v[52:55], v2 offset:40960\n" \
  "v_fma_f32 v28, -v116, v16, v28\n" \
  "v_mfma_f32_16x16x32_bf16 v[44:47], a[40:43], v[64:67], v[44:47]\n" \
  "buffer_load_dwordx4 v0, s[36:39], s61 offen offset:2048 sc1 lds\n" \
  "v_mfma_f32_16x16x32_bf16 v[48:51], a[44:47], v[64:67], v[48:51]\n" \
  "s_waitcnt lgkmcnt(3)\n" \
  "v_fma_f32 v29, -v116, v17, v29\n" \
  "v_fma_f32 v30, -v116, v18, v30\n" \
  "v_mfma_f32_16x16x32_bf16 v[40:43], a[48:51], v[68:71], v[40:43]\n" \
  "s_lshr_b32 s68, s51, 5\n" \
  "ds_read_b128 v[56:59], v2 offset:41984\n" \
  "v_fma_f32 v31, -v116, v19, v31\n" \
  "v_mfma_f32_16x16x32_bf16 v[44:47], a[52:55], v[68:71], v[44:47]\n" \
  "s_mul_i32 s70, s47, 5\n" \
  "v_mul_f32 v84, v28, v28\n" \
  "v_mul_f32 v85, v29, v29\n" \
  "v_mfma_f32_16x16x32_bf16 v[48:51], a[56:59], v[68:71], v[48:51]\n" \
  "buffer_load_dwordx4 v0, s[36:39], s61 offen offset:3072 sc1 lds\n" \
  "v_mfma_f32_16x16x32_bf16 v[40:43], a[60:63], v[72:75], v[40:43]\n" \
  "s_lshr_b32 s70, s70, 6\n" \
  "ds_read_b128 v[60:63], v2 offset:43008\n" \
  "v_mul_f32 v86, v30, v30\n" \
  "v_mfma_f32_16x16x32_bf16 v[44:47], a[64:67], v[72:75], v[44:47]\n" \
  "s_add_u32 m0, s78, 256\n" \
  "v_mul_f32 v87, v31, v31\n" \
  "v_fma_f32 v84, v118, v84, v117\n" \
  "v_mfma_f32_16x16x32_bf16 v[48:51], a[68:71], v[72:75], v[48:51]\n" \
  "s_waitcnt lgkmcnt(3)\n" \
  "v_mul_f32 v84, v28, v84\n" \
  "v_fma_f32 v85, v118, v85, v117\n" \
  "v_mfma_f32_16x16x32_bf16 v[40:43], a[72:75], v[76:79], v[40:43]\n" \
  "s_mov_b64 s[44:45], s[46:47]\n" \
  "ds_read_b128 v[64:67], v2 offset:44032\n" \
  "v_mul_f32 v85, v29, v85\n" \
  "v_mfma_f32_16x16x32_bf16 v[44:47], a[76:79], v[76:79], v[44:47]\n" \
  "s_mov_b64 s[46:47], s[48:49]\n" \
  "v_exp_f32 v84, v84\n" \
  "v_mfma_f32_16x16x32_bf16 v[48:51], a[80:83], v[76:79], v[48:51]\n" \
  "s_mov_b64 s[48:49], s[50:51]\n" \
  "v_exp_f32 v85, v85\n" \
  "v_mfma_f32_16x16x32_bf16 v[40:43], a[84:87], v[80:83], v[40:43]\n" \
  "ds_read_b128 v[68:71], v2 offset:45056\n" \
  "v_fma_f32 v84, v84, v119, v119\n" \
  "v_mfma_f32_16x16x32_bf16 v[44:47], a[88:91], v[80:83], v[44:47]\n" \
  "v_rcp_f32 v84, v84\n" \
  "v_mfma_f32_16x16x32_bf16 v[48:51], a[92:95], v[80:83], v[48:51]\n" \
  "s_waitcnt lgkmcnt(3)\n" \
  "v_fma_f32 v85, v85, v119, v119\n" \
  "v_fma_f32 v86, v118, v86, v117\n" \
  "v_mfma_f32_16x16x32_bf16 v[40:43], a[96:99], v[52:55], v[40:43]\n" \
  "ds_read_b128 v[72:75], v2 offset:46080\n" \
  "v_mul_f32 v86, v30, v86\n" \
  "v_mfma_f32_16x16x32_bf16 v[44:47], a[100:103], v[52:55], v[44:47]\n" \
  "v_exp_f32 v86, v86\n" \
  "v_mfma_f32_16x16x32_bf16 v[48:51], a[104:107], v[52:55], v[48:51]\n" \
  "v_rcp_f32 v85, v85\n" \
  "v_mfma_f32_16x16x32_bf16 v[40:43], a[108:111], v[56:59], v[40:43]\n" \
  "ds_read_b128 v[76:79], v2 offset:47104\n" \
  "v_fma_f32 v86, v86, v119, v119\n" \
  "v_mfma_f32_16x16x32_bf16 v[44:47], a[112:115], v[56:59], v[44:47]\n" \
  "v_rcp_f32 v86, v86\n" \
  "v_mfma_f32_16x16x32_bf16 v[48:51], a[116:119], v[56:59], v[48:51]\n" \
  "s_waitcnt lgkmcnt(3)\n" \
  "v_fma_f32 v87, v118, v87, v117\n" \
  "v_mul_f32 v87, v31, v87\n" \
  "v_mfma_f32_16x16x32_bf16 v[40:43], a[120:123], v[60:63], v[40:43]\n" \
  "ds_read_b128 v[80:83], v2 offset:48128\n" \
  "v_mul_f32 v28, v28, v84\n" \
  "v_mfma_f32_16x16x32_bf16 v[44:47], a[124:127], v[60:63], v[44:47]\n" \
  "s_waitcnt vmcnt(7)\n" \
  "v_exp_f32 v87, v87\n" \
  "v_mfma_f32_16x16x32_bf16 v[48:51], a[128:131], v[60:63], v[48:51]\n" \
  "s_barrier\n" \
  "v_mfma_f32_16x16x32_bf16 v[40:43], a[132:135], v[64:67], v[40:43]\n" \
  "ds_read_b128 v[52:55], v2 offset:49152\n" \
  "v_fma_f32 v87, v87, v119, v119\n" \
  "v_mfma_f32_16x16x32_bf16 v[44:47], a[136:139], v[64:67], v[44:47]\n" \
  "v_rcp_f32 v87, v87\n" \
  "v_mfma_f32_16x16x32_bf16 v[48:51], a[140:143], v[64:67], v[48:51]\n" \
  "s_waitcnt lgkmcnt(3)\n" \
  "v_add_f32_dpp v108, v108, v108 quad_perm:[1,0,3,2] row_mask:0xf bank_mask:0xf\n" \
  "v_mul_f32 v29, v29, v85\n" \
  "v_mfma_f32_16x16x32_bf16 v[40:43], a[144:147], v[68:71], v[40:43]\n" \
  "ds_read_b128 v[56:59], v2 offset:50176\n" \
  "v_add_f32_dpp v108, v108, v108 quad_perm:[2,3,0,1] row_mask:0xf bank_mask:0xf\n" \
  "v_mfma_f32_16x16x32_bf16 v[44:47], a[148:151], v[68:71], v[44:47]\n" \
  "s_nop 0\n" \
  "v_add_f32_dpp v108, v108, v108 row_half_mirror row_mask:0xf bank_mask:0xf\n" \
  "v_mul_f32 v30, v30, v86\n" \
  "v_mfma_f32_16x16x32_bf16 v[48:51], a[152:155], v[68:71], v[48:51]\n" \
  "ds_write_b32 v11, v108 offset:128\n" \
  "v_mfma_f32_16x16x32_bf16 v[40:43], a[156:159], v[72:75], v[40:43]\n" \
  "ds_read_b128 v[60:63], v2 offset:51200\n" \
  "v_mul_f32 v31, v31, v87\n" \
  "v_mfma_f32_16x16x32_bf16 v[44:47], a[160:163], v[72:75], v[44:47]\n" \
  "buffer_load_dword v6, s[36:39], s68 offen offset:0 sc1 lds\n" \
  "v_mfma_f32_16x16x32_bf16 v[48:51], a[164:167], v[72:75], v[48:51]\n" \
  "s_waitcnt lgkmcnt(4)\n" \
  "v_cvt_pk_bf16_f32 v96, v28, v29\n" \
  "v_cvt_pk_bf16_f32 v97, v30, v31\n" \
  "v_mfma_f32_16x16x32_bf16 v[40:43], a[168:171], v[76:79], v[40:43]\n" \
  "s_add_u32 m0, s62, 16384\n" \
  "ds_read_b128 v[64:67], v2 offset:52224\n" \
  "v_fma_f32 v32, -v116, v20, v32\n" \
  "v_mfma_f32_16x16x32_bf16 v[44:47], a[172:175], v[76:79], v[44:47]\n" \
  "buffer_load_dwordx4 v1, s[36:39], s61 offen offset:0 sc1 lds\n" \
  "v_mfma_f32_16x16x32_bf16 v[48:51], a[176:179], v[76:79], v[48:51]\n" \
  "s_lshr_b32 s72, s61, 5\n" \
  "v_fma_f32 v33, -v116, v21, v33\n" \
  "v_fma_f32 v34, -v116, v22, v34\n" \
  "v_mfma_f32_16x16x32_bf16 v[40:43], a[180:183], v[80:83], v[40:43]\n" \
  "s_mov_b32 s62, s64\n" \
  "ds_read_b128 v[68:71], v2 offset:53248\n" \
  "v_fma_f32 v35, -v116, v23, v35\n" \
  "v_mfma_f32_16x16x32_bf16 v[44:47], a[184:187], v[80:83], v[44:47]\n" \
  "v_mul_f32 v88, v32, v32\n" \
  "v_mul_f32 v89, v33, v33\n" \
  "v_mfma_f32_16x16x32_bf16 v[48:51], a[188:191], v[80:83], v[48:51]\n" \
  "s_waitcnt lgkmcnt(4)\n" \
  "buffer_load_dwordx4 v1, s[36:39], s61 offen offset:1024 sc1 lds\n" \
  "v_mfma_f32_16x16x32_bf16 v[40:43], a[192:195], v[52:55], v[40:43]\n" \
  "ds_read_b128 v[72:75], v2 offset:54272\n" \
  "v_mul_f32 v90, v34, v34\n" \
  "v_mfma_f32_16x16x32_bf16 v[44:47], a[196:199], v[52:55], v[44:47]\n" \
  "v_mul_f32 v91, v35, v35\n" \
  "v_fma_f32 v88, v118, v88, v117\n" \
  "v_mfma_f32_16x16x32_bf16 v[48:51], a[200:203], v[52:55], v[48:51]\n" \
  "v_mul_f32 v88, v32, v88\n" \
  "v_fma_f32 v89, v118, v89, v117\n" \
  "v_mfma_f32_16x16x32_bf16 v[40:43], a[204:207], v[56:59], v[40:43]\n" \
  "ds_read_b128 v[76:79], v2 offset:55296\n" \
  "v_mul_f32 v89, v33, v89\n" \
  "v_mfma_f32_16x16x32_bf16 v[44:47], a[208:211], v[56:59], v[44:47]\n" \
  "buffer_load_dwordx4 v1, s[36:39], s61 offen offset:2048 sc1 lds\n" \
  "v_mfma_f32_16x16x32_bf16 v[48:51], a[212:215], v[56:59], v[48:51]\n" \
  "s_waitcnt lgkmcnt(3)\n" \
  "v_exp_f32 v88, v88\n" \
  "v_mfma_f32_16x16x32_bf16 v[40:43], a[216:219], v[60:63], v[40:43]\n" \
  "ds_read_b128 v[80:83], v2 offset:56320\n" \
  "v_fma_f32 v88, v88, v119, v119\n" \
  "v_mfma_f32_16x16x32_bf16 v[44:47], a[220:223], v[60:63], v[44:47]\n" \
  "v_exp_f32 v89, v89\n" \
  "v_mfma_f32_16x16x32_bf16 v[48:51], a[224:227], v[60:63], v[48:51]\n" \
  "buffer_load_dwordx4 v1, s[36:39], s61 offen offset:3072 sc1 lds\n" \
  "v_mfma_f32_16x16x32_bf16 v[40:43], a[228:231], v[64:67], v[40:43]\n" \
  "ds_read_b128 v[52:55], v2 offset:57344\n" \
  "v_fma_f32 v89, v89, v119, v119\n" \
  "v_mfma_f32_16x16x32_bf16 v[44:47], a[232:235], v[64:67], v[44:47]\n" \
  "buffer_load_dword v104, v9, s[36:39], s72 offen offset:0 sc1\n" \
  "v_mfma_f32_16x16x32_bf16 v[48:51], a[236:239], v[64:67], v[48:51]\n" \
  "s_waitcnt lgkmcnt(3)\n" \
  "v_rcp_f32 v88, v88\n" \
  "v_mfma_f32_16x16x32_bf16 v[40:43], a[240:243], v[68:71], v[40:43]\n" \
  "ds_read_b128 v[56:59], v2 offset:58368\n" \
  "v_fma_f32 v90, v118, v90, v117\n" \
  "v_mfma_f32_16x16x32_bf16 v[44:47], a[244:247], v[68:71], v[44:47]\n" \
  "v_rcp_f32 v89, v89\n" \
  "v_mfma_f32_16x16x32_bf16 v[48:51], a[248:251], v[68:71], v[48:51]\n" \
  "v_mul_f32 v90, v34, v90\n" \
  "v_fma_f32 v91, v118, v91, v117\n" \
  "v_mfma_f32_16x16x32_bf16 v[40:43], a[252:255], v[72:75], v[40:43]\n" \
  "ds_read_b128 v[60:63], v2 offset:59392\n" \
  "v_mul_f32 v91, v35, v91\n" \
  "v_mfma_f32_16x16x32_bf16 v[44:47], v[128:131], v[72:75], v[44:47]\n" \
  "v_exp_f32 v90, v90\n" \
  "v_mfma_f32_16x16x32_bf16 v[48:51], v[132:135], v[72:75], v[48:51]\n" \
  "s_waitcnt lgkmcnt(3)\n" \
  "v_exp_f32 v91, v91\n" \
  "v_mfma_f32_16x16x32_bf16 v[40:43], v[136:139], v[76:79], v[40:43]\n" \
  "ds_read_b128 v[64:67], v2 offset:60416\n" \
  "v_fma_f32 v90, v90, v119, v119\n" \
  "v_mfma_f32_16x16x32_bf16 v[44:47], v[140:143], v[76:79], v[44:47]\n" \
  "v_rcp_f32 v90, v90\n" \
  "v_mfma_f32_16x16x32_bf16 v[48:51], v[144:147], v[76:79], v[48:51]\n" \
  "v_fma_f32 v91, v91, v119, v119\n" \
  "v_mul_f32 v32, v32, v88\n" \
  "v_mfma_f32_16x16x32_bf16 v[40:43], v[148:151], v[80:83], v[40:43]\n" \
  "ds_read_b128 v[68:71], v2 offset:61440\n" \
  "v_mul_f32 v33, v33, v89\n" \
  "v_mfma_f32_16x16x32_bf16 v[44:47], v[152:155], v[80:83], v[44:47]\n" \
  "v_rcp_f32 v91, v91\n" \
  "v_mfma_f32_16x16x32_bf16 v[48:51], v[156:159], v[80:83], v[48:51]\n" \
  "s_waitcnt lgkmcnt(3)\n" \
  "v_mul_f32 v34, v34, v90\n" \
  "v_mul_f32 v35, v35, v91\n" \
  "v_mfma_f32_16x16x32_bf16 v[40:43], v[160:163], v[52:55], v[40:43]\n" \
  "ds_read_b128 v[72:75], v2 offset:62464\n" \
  "v_cvt_pk_bf16_f32 v98, v32, v33\n" \
  "v_mfma_f32_16x16x32_bf16 v[44:47], v[164:167], v[52:55], v[44:47]\n" \
  "v_cvt_pk_bf16_f32 v99, v34, v35\n" \
  "v_fma_f32 v36, -v116, v24, v36\n" \
  "v_mfma_f32_16x16x32_bf16 v[48:51], v[168:171], v[52:55], v[48:51]\n" \
  "buffer_store_dwordx4 v[96:99], v3, s[36:39], s73 offen offset:0 sc0 sc1\n" \
  "v_fma_f32 v37, -v116, v25, v37\n" \
  "v_fma_f32 v38, -v116, v26, v38\n" \
  "v_fma_f32 v39, -v116, v27, v39\n" \
  "v_mfma_f32_16x16x32_bf16 v[40:43], v[172:175], v[56:59], v[40:43]\n" \
  "ds_read_b128 v[76:79], v2 offset:63488\n" \
  "v_mul_f32 v92, v36, v36\n" \
  "v_mfma_f32_16x16x32_bf16 v[44:47], v[176:179], v[56:59], v[44:47]\n" \
  "v_mul_f32 v93, v37, v37\n" \
  "v_mul_f32 v94, v38, v38\n" \
  "v_mfma_f32_16x16x32_bf16 v[48:51], v[180:183], v[56:59], v[48:51]\n" \
  "s_waitcnt lgkmcnt(3)\n" \
  "v_mul_f32 v95, v39, v39\n" \
  "v_fma_f32 v92, v118, v92, v117\n" \
  "v_mfma_f32_16x16x32_bf16 v[40:43], v[184:187], v[60:63], v[40:43]\n" \
  "ds_read_b128 v[80:83], v2 offset:64512\n" \
  "v_xor_b32 v2, 65536, v2\n" \
  "v_mfma_f32_16x16x32_bf16 v[44:47], v[188:191], v[60:63], v[44:47]\n" \
  "s_waitcnt vmcnt(6)\n" \
  "v_mul_f32 v92, v36, v92\n" \
  "v_fma_f32 v93, v118, v93, v117\n" \
  "v_mfma_f32_16x16x32_bf16 v[48:51], v[192:195], v[60:63], v[48:51]\n" \
  "s_barrier\n" \
  "v_mfma_f32_16x16x32_bf16 v[40:43], v[196:199], v[64:67], v[40:43]\n" \
  "ds_read_b128 v[52:55], v2 offset:0\n" \
  "ds_read_b32 v14, v8 offset:256\n" \
  "v_mfma_f32_16x16x32_bf16 v[44:47], v[200:203], v[64:67], v[44:47]\n" \
  "buffer_store_dword v5, v4, s[36:39], s70 offen offset:0 sc0 sc1\n" \
  "ds_read_b64 v[112:113], v12 offset:128\n" \
  "v_exp_f32 v92, v92\n" \
  "v_mfma_f32_16x16x32_bf16 v[48:51], v[204:207], v[64:67], v[48:51]\n" \
  "s_waitcnt lgkmcnt(5)\n" \
  "v_mul_f32 v93, v37, v93\n" \
  "v_fma_f32 v92, v92, v119, v119\n" \
  "v_mfma_f32_16x16x32_bf16 v[40:43], v[208:211], v[68:71], v[40:43]\n" \
  "ds_read_b128 v[56:59], v2 offset:1024\n" \
  "v_fma_f32 v94, v118, v94, v117\n" \
  "v_mfma_f32_16x16x32_bf16 v[44:47], v[212:215], v[68:71], v[44:47]\n" \
  "s_waitcnt lgkmcnt(2)\n" \
  "v_exp_f32 v93, v93\n" \
  "v_mfma_f32_16x16x32_bf16 v[48:51], v[216:219], v[68:71], v[48:51]\n" \
  "s_waitcnt lgkmcnt(1)\n" \
  "v_cmp_eq_u32 vcc, s60, v14\n" \
  "v_rcp_f32 v92, v92\n" \
  "v_mul_f32 v94, v38, v94\n" \
  "v_mfma_f32_16x16x32_bf16 v[40:43], v[220:223], v[72:75], v[40:43]\n" \
  "s_cmp_eq_u32 vcc_lo, -1\n" \
  "ds_read_b128 v[60:63], v2 offset:2048\n" \
  "v_fma_f32 v93, v93, v119, v119\n" \
  "v_mfma_f32_16x16x32_bf16 v[44:47], v[224:227], v[72:75], v[44:47]\n" \
  "s_cselect_b32 s66, s51, s59\n" \
  "v_exp_f32 v94, v94\n" \
  "v_mfma_f32_16x16x32_bf16 v[48:51], v[228:231], v[72:75], v[48:51]\n" \
  "v_rcp_f32 v93, v93\n" \
  "v_mfma_f32_16x16x32_bf16 v[40:43], v[232:235], v[76:79], v[40:43]\n" \
  "s_max_u32 s66, s66, s67\n" \
  "ds_read_b128 v[64:67], v2 offset:3072\n" \
  "v_fma_f32 v95, v118, v95, v117\n" \
  "v_mul_f32 v95, v39, v95\n" \
  "v_exp_f32 v95, v95\n" \
  "v_mfma_f32_16x16x32_bf16 v[44:47], v[236:239], v[76:79], v[44:47]\n" \
  "s_lshr_b32 s61, s66, 0\n" \
  "v_fma_f32 v94, v94, v119, v119\n" \
  "v_fma_f32 v95, v95, v119, v119\n" \
  "v_rcp_f32 v94, v94\n" \
  "v_rcp_f32 v95, v95\n" \
  "v_mfma_f32_16x16x32_bf16 v[48:51], v[240:243], v[76:79], v[48:51]\n" \
  "s_xor_b32 s64, s64, 65536\n" \
  "v_mul_f32 v36, v36, v92\n" \
  "v_mul_f32 v37, v37, v93\n" \
  "v_mul_f32 v38, v38, v94\n" \
  "v_mul_f32 v39, v39, v95\n" \
  "v_cvt_pk_bf16_f32 v100, v36, v37\n" \
  "v_cvt_pk_bf16_f32 v101, v38, v39\n" \
  "v_mfma_f32_16x16x32_bf16 v[40:43], v[244:247], v[80:83], v[40:43]\n" \
  "s_mov_b64 s[50:51], s[52:53]\n" \
  "ds_read_b128 v[68:71], v2 offset:4096\n" \
  "buffer_store_dwordx2 v[100:101], v10, s[36:39], s73 offen offset:0 sc0 sc1\n" \
  "s_mov_b32 s52, s74\n" \
  "s_cmp_lt_u32 s67, s59\n" \
  "v_mfma_f32_16x16x32_bf16 v[44:47], v[248:251], v[80:83], v[44:47]\n" \
  "s_waitcnt lgkmcnt(3)\n" \
  "v_mfma_f32_16x16x32_bf16 v[48:51], v[252:255], v[80:83], v[48:51]\n" \
  "s_cbranch_scc1 Lg3_d0_%=\n" \
  "Lg3_slow0_%=:\n" \
  "s_cmp_eq_u32 s54, 1\n" \
  "s_cselect_b32 s68, s49, s48\n" \
  "s_lshr_b32 s68, s68, 5\n" \
  "s_add_u32 m0, s78, 8192\n" \
  "s_nop 0\n" \
  "buffer_load_dword v7, s[36:39], s68 offen offset:0 sc1 lds\n" \
  "s_mul_i32 s73, s47, 7\n" \
  "s_lshr_b32 s73, s73, 1\n" \
  "v_mul_f32 v116, s75, v112\n" \
  "v_fma_f32 v113, v113, s75, v13\n" \
  "v_fma_f32 v113, -v116, v116, v113\n" \
  "v_max_f32 v113, v113, v13\n" \
  "v_rsq_f32 v112, v113\n" \
  "s_nop 0\n" \
  "v_mul_f32 v117, s76, v112\n" \
  "v_mul_f32 v118, v112, v112\n" \
  "v_mul_f32 v119, v113, v112\n" \
  "v_mul_f32 v118, s77, v118\n" \
  "v_mul_f32 v118, v118, v117\n" \
  "v_fma_f32 v40, -v116, v16, v40\n" \
  "v_fma_f32 v41, -v116, v17, v41\n" \
  "v_fma_f32 v42, -v116, v18, v42\n" \
  "v_fma_f32 v43, -v116, v19, v43\n" \
  "v_mul_f32 v84, v40, v40\n" \
  "v_mul_f32 v85, v41, v41\n" \
  "v_mul_f32 v86, v42, v42\n" \
  "v_mul_f32 v87, v43, v43\n" \
  "v_fma_f32 v84, v118, v84, v117\n" \
  "v_fma_f32 v85, v118, v85, v117\n" \
  "v_fma_f32 v86, v118, v86, v117\n" \
  "v_fma_f32 v87, v118, v87, v117\n" \
  "v_mul_f32 v84, v40, v84\n" \
  "v_mul_f32 v85, v41, v85\n" \
  "v_mul_f32 v86, v42, v86\n" \
  "v_mul_f32 v87, v43, v87\n" \
  "v_exp_f32 v84, v84\n" \
  "v_exp_f32 v85, v85\n" \
  "v_exp_f32 v86, v86\n" \
  "v_exp_f32 v87, v87\n" \
  "v_fma_f32 v84, v84, v119, v119\n" \
  "v_fma_f32 v85, v85, v119, v119\n" \
  "v_fma_f32 v86, v86, v119, v119\n" \
  "v_fma_f32 v87, v87, v119, v119\n" \
  "v_rcp_f32 v84, v84\n" \
  "v_rcp_f32 v85, v85\n" \
  "v_rcp_f32 v86, v86\n" \
  "v_rcp_f32 v87, v87\n" \
  "v_mul_f32 v40, v40, v84\n" \
  "v_mul_f32 v41, v41, v85\n" \
  "v_mul_f32 v42, v42, v86\n" \
  "v_mul_f32 v43, v43, v87\n" \
  "v_cvt_pk_bf16_f32 v96, v40, v41\n" \
  "v_cvt_pk_bf16_f32 v97, v42, v43\n" \
  "v_fma_f32 v44, -v116, v20, v44\n" \
  "v_fma_f32 v45, -v116, v21, v45\n" \
  "v_fma_f32 v46, -v116, v22, v46\n" \
  "v_fma_f32 v47, -v116, v23, v47\n" \
  "v_mul_f32 v88, v44, v44\n" \
  "v_mul_f32 v89, v45, v45\n" \
  "v_mul_f32 v90, v46, v46\n" \
  "v_mul_f32 v91, v47, v47\n" \
  "v_fma_f32 v88, v118, v88, v117\n" \
  "v_fma_f32 v89, v118, v89, v117\n" \
  "v_fma_f32 v90, v118, v90, v117\n" \
  "v_fma_f32 v91, v118, v91, v117\n" \
  "v_mul_f32 v88, v44, v88\n" \
  "s_add_u32 m0, s78, 8448\n" \
  "s_nop 0\n" \
  "buffer_load_dword v7, s[36:39], s68 offen offset:0 sc1 lds\n" \
  "v_mul_f32 v89, v45, v89\n" \
  "v_mul_f32 v90, v46, v90\n" \
  "v_mul_f32 v91, v47, v91\n" \
  "v_exp_f32 v88, v88\n" \
  "v_exp_f32 v89, v89\n" \
  "v_exp_f32 v90, v90\n" \
  "v_exp_f32 v91, v91\n" \
  "v_fma_f32 v88, v88, v119, v119\n" \
  "v_fma_f32 v89, v89, v119, v119\n" \
  "v_fma_f32 v90, v90, v119, v119\n" \
  "v_fma_f32 v91, v91, v119, v119\n" \
  "v_rcp_f32 v88, v88\n" \
  "v_rcp_f32 v89, v89\n" \
  "v_rcp_f32 v90, v90\n" \
  "v_rcp_f32 v91, v91\n" \
  "v_mul_f32 v44, v44, v88\n" \
  "v_mul_f32 v45, v45, v89\n" \
  "v_mul_f32 v46, v46, v90\n" \
  "v_mul_f32 v47, v47, v91\n" \
  "v_cvt_pk_bf16_f32 v98, v44, v45\n" \
  "v_cvt_pk_bf16_f32 v99, v46, v47\n" \
  "buffer_store_dwordx4 v[96:99], v3, s[36:39], s73 offen offset:0 sc0 sc1\n" \
  "v_fma_f32 v48, -v116, v24, v48\n" \
  "v_fma_f32 v49, -v116, v25, v49\n" \
  "v_fma_f32 v50, -v116, v26, v50\n" \
  "v_fma_f32 v51, -v116, v27, v51\n" \
  "v_mul_f32 v92, v48, v48\n" \
  "v_mul_f32 v93, v49, v49\n" \
  "v_mul_f32 v94, v50, v50\n" \
  "v_mul_f32 v95, v51, v51\n" \
  "v_fma_f32 v92, v118, v92, v117\n" \
  "v_fma_f32 v93, v118, v93, v117\n" \
  "v_fma_f32 v94, v118, v94, v117\n" \
  "v_fma_f32 v95, v118, v95, v117\n" \
  "v_mul_f32 v92, v48, v92\n" \
  "v_mul_f32 v93, v49, v93\n" \
  "v_mul_f32 v94, v50, v94\n" \
  "v_mul_f32 v95, v51, v95\n" \
  "v_exp_f32 v92, v92\n" \
  "v_exp_f32 v93, v93\n" \
  "v_exp_f32 v94, v94\n" \
  "v_exp_f32 v95, v95\n" \
  "v_fma_f32 v92, v92, v119, v119\n" \
  "v_fma_f32 v93, v93, v119, v119\n" \
  "v_fma_f32 v94, v94, v119, v119\n" \
  "v_fma_f32 v95, v95, v119, v119\n" \
  "v_rcp_f32 v92, v92\n" \
  "v_rcp_f32 v93, v93\n" \
  "v_rcp_f32 v94, v94\n" \
  "v_rcp_f32 v95, v95\n" \
  "v_mul_f32 v48, v48, v92\n" \
  "v_mul_f32 v49, v49, v93\n" \
  "v_mul_f32 v50, v50, v94\n" \
  "v_mul_f32 v51, v51, v95\n" \
  "v_cvt_pk_bf16_f32 v100, v48, v49\n" \
  "v_cvt_pk_bf16_f32 v101, v50, v51\n" \
  "buffer_store_dwordx2 v[100:101], v10, s[36:39], s73 offen offset:0 sc0 sc1\n" \
  "s_cmp_ge_u32 s48, s59\n" \
  "s_cbranch_scc1 Lg3_done0_%=\n" \
  "s_add_u32 m0, s78, 16384\n" \
  "s_nop 0\n" \
  "buffer_load_dword v7, s[36:39], s68 offen offset:0 sc1 lds\n" \
  "s_waitcnt vmcnt(4)\n" \
  "s_barrier\n" \
  "ds_read_b32 v14, v8 offset:8192\n" \
  "s_mul_i32 s70, s46, 5\n" \
  "s_lshr_b32 s70, s70, 6\n" \
  "buffer_store_dword v5, v4, s[36:39], s70 offen offset:0 sc0 sc1\n" \
  "s_mov_b32 s46, s59\n" \
  "s_mov_b32 s69, 18432\n" \
  "s_waitcnt lgkmcnt(0)\n" \
  "v_cmp_eq_u32 vcc, s60, v14\n" \
  "s_nop 0\n" \
  "s_cmp_eq_u32 vcc_lo, -1\n" \
  "s_cbranch_scc1 Lg3_go0_%=\n" \
  "s_add_u32 m0, s78, 16640\n" \
  "s_nop 0\n" \
  "buffer_load_dword v7, s[36:39], s68 offen offset:0 sc1 lds\n" \
  "s_waitcnt vmcnt(5)\n" \
  "s_barrier\n" \
  "ds_read_b32 v14, v8 offset:8448\n" \
  "s_mov_b32 s69, 18688\n" \
  "s_waitcnt lgkmcnt(0)\n" \
  "v_cmp_eq_u32 vcc, s60, v14\n" \
  "s_nop 0\n" \
  "s_cmp_eq_u32 vcc_lo, -1\n" \
  "s_cbranch_scc1 Lg3_go0_%=\n" \
  "s_add_u32 m0, s78, 8192\n" \
  "s_nop 0\n" \
  "buffer_load_dword v7, s[36:39], s68 offen offset:0 sc1 lds\n" \
  "s_waitcnt vmcnt(3)\n" \
  "s_barrier\n" \
  "ds_read_b32 v14, v8 offset:16384\n" \
  "s_mul_i32 s70, s47, 5\n" \
  "s_lshr_b32 s70, s70, 6\n" \
  "buffer_store_dword v5, v4, s[36:39], s70 offen offset:0 sc0 sc1\n" \
  "s_mov_b32 s47, s59\n" \
  "s_mov_b32 s69, 10240\n" \
  "s_waitcnt lgkmcnt(0)\n" \
  "v_cmp_eq_u32 vcc, s60, v14\n" \
  "s_nop 0\n" \
  "s_cmp_eq_u32 vcc_lo, -1\n" \
  "s_cbranch_scc1 Lg3_go0_%=\n" \
  "s_add_u32 m0, s78, 8448\n" \
  "s_nop 0\n" \
  "buffer_load_dword v7, s[36:39], s68 offen offset:0 sc1 lds\n" \
  "s_waitcnt vmcnt(3)\n" \
  "s_barrier\n" \
  "ds_read_b32 v14, v8 offset:16640\n" \
  "s_mov_b32 s69, 10496\n" \
  "s_waitcnt lgkmcnt(0)\n" \
  "v_cmp_eq_u32 vcc, s60, v14\n" \
  "s_nop 0\n" \
  "s_cmp_eq_u32 vcc_lo, -1\n" \
  "s_cbranch_scc1 Lg3_go0_%=\n" \
  "Lg3_poll0_%=:\n" \
  "s_add_u32 m0, s78, 16384\n" \
  "s_nop 0\n" \
  "buffer_load_dword v7, s[36:39], s68 offen offset:0 sc1 lds\n" \
  "s_waitcnt vmcnt(2)\n" \
  "s_barrier\n" \
  "ds_read_b32 v14, v8 offset:8192\n" \
  "s_mov_b32 s69, 18432\n" \
  "s_waitcnt lgkmcnt(0)\n" \
  "v_cmp_eq_u32 vcc, s60, v14\n" \
  "s_nop 0\n" \
  "s_cmp_eq_u32 vcc_lo, -1\n" \
  "s_cbranch_scc1 Lg3_go0_%=\n" \
  "s_add_u32 m0, s78, 16640\n" \
  "s_nop 0\n" \
  "buffer_load_dword v7, s[36:39], s68 offen offset:0 sc1 lds\n" \
  "s_waitcnt vmcnt(2)\n" \
  "s_barrier\n" \
  "ds_read_b32 v14, v8 offset:8448\n" \
  "s_mov_b32 s69, 18688\n" \
  "s_waitcnt lgkmcnt(0)\n" \
  "v_cmp_eq_u32 vcc, s60, v14\n" \
  "s_nop 0\n" \
  "s_cmp_eq_u32 vcc_lo, -1\n" \
  "s_cbranch_scc1 Lg3_go0_%=\n" \
  "s_add_u32 m0, s78, 8192\n" \
  "s_nop 0\n" \
  "buffer_load_dword v7, s[36:39], s68 offen offset:0 sc1 lds\n" \
  "s_waitcnt vmcnt(2)\n" \
  "s_barrier\n" \
  "ds_read_b32 v14, v8 offset:16384\n" \
  "s_mov_b32 s69, 10240\n" \
  "s_waitcnt lgkmcnt(0)\n" \
  "v_cmp_eq_u32 vcc, s60, v14\n" \
  "s_nop 0\n" \
  "s_cmp_eq_u32 vcc_lo, -1\n" \
  "s_cbranch_scc1 Lg3_go0_%=\n" \
  "s_add_u32 m0, s78, 8448\n" \
  "s_nop 0\n" \
  "buffer_load_dword v7, s[36:39], s68 offen offset:0 sc1 lds\n" \
  "s_waitcnt vmcnt(2)\n" \
  "s_barrier\n" \
  "ds_read_b32 v14, v8 offset:16640\n" \
  "s_mov_b32 s69, 10496\n" \
  "s_waitcnt lgkmcnt(0)\n" \
  "v_cmp_eq_u32 vcc, s60, v14\n" \
  "s_nop 0\n" \
  "s_cmp_eq_u32 vcc_lo, -1\n" \
  "s_cbranch_scc1 Lg3_go0_%=\n" \
  "s_add_u32 s79, s79, 1\n" \
  "s_cmp_lt_u32 s79, 20000\n" \
  "s_cbranch_scc1 Lg3_poll0_%=\n" \
  "Lg3_go0_%=:\n" \
  "s_xor_b32 s62, s63, 65536\n" \
  "s_mov_b32 s61, s48\n" \
  "s_mov_b32 m0, s62\n" \
  "s_nop 0\n" \
  "buffer_load_dwordx4 v0, s[36:39], s61 offen offset:0 sc1 lds\n" \
  "buffer_load_dwordx4 v0, s[36:39], s61 offen offset:1024 sc1 lds\n" \
  "buffer_load_dwordx4 v0, s[36:39], s61 offen offset:2048 sc1 lds\n" \
  "buffer_load_dwordx4 v0, s[36:39], s61 offen offset:3072 sc1 lds\n" \
  "s_add_u32 m0, s62, 16384\n" \
  "s_lshr_b32 s72, s61, 5\n" \
  "buffer_load_dwordx4 v1, s[36:39], s61 offen offset:0 sc1 lds\n" \
  "buffer_load_dwordx4 v1, s[36:39], s61 offen offset:1024 sc1 lds\n" \
  "buffer_load_dwordx4 v1, s[36:39], s61 offen offset:2048 sc1 lds\n" \
  "buffer_load_dwordx4 v1, s[36:39], s61 offen offset:3072 sc1 lds\n" \
  "buffer_load_dword v104, v9, s[36:39], s72 offen offset:0 sc1\n" \
  "v_add_u32 v84, s69, v8\n" \
  "s_waitcnt vmcnt(5)\n" \
  "s_barrier\n" \
  "ds_read_b32 v14, v84 offset:0\n" \
  "ds_read_b128 v[52:55], v2 offset:0\n" \
  "ds_read_b128 v[56:59], v2 offset:1024\n" \
  "ds_read_b128 v[60:63], v2 offset:2048\n" \
  "ds_read_b128 v[64:67], v2 offset:3072\n" \
  "ds_read_b128 v[68:71], v2 offset:4096\n" \
  "s_mul_i32 s70, s46, 5\n" \
  "s_lshr_b32 s70, s70, 6\n" \
  "buffer_store_dword v5, v4, s[36:39], s70 offen offset:0 sc0 sc1\n" \
  "s_mov_b32 s46, s59\n" \
  "s_mul_i32 s70, s47, 5\n" \
  "s_lshr_b32 s70, s70, 6\n" \
  "buffer_store_dword v5, v4, s[36:39], s70 offen offset:0 sc0 sc1\n" \
  "s_mov_b32 s47, s59\n" \
  "s_mul_i32 s70, s59, 5\n" \
  "s_lshr_b32 s70, s70, 6\n" \
  "buffer_store_dword v5, v4, s[36:39], s70 offen offset:0 sc0 sc1\n" \
  "s_waitcnt lgkmcnt(5)\n" \
  "v_cmp_eq_u32 vcc, s60, v14\n" \
  "s_nop 0\n" \
  "s_cmp_eq_u32 vcc_lo, -1\n" \
  "s_cselect_b32 s66, s49, s59\n" \
  "s_lshr_b32 s61, s66, 0\n" \
  "s_xor_b32 s62, s64, 65536\n" \
  "s_waitcnt lgkmcnt(3)\n" \
  "s_branch Lg3_d0_%=\n" \
  "Lg3_done0_%=:\n" \
  "s_waitcnt vmcnt(0)\n" \
  "s_barrier\n" \
  "s_mul_i32 s70, s46, 5\n" \
  "s_lshr_b32 s70, s70, 6\n" \
  "buffer_store_dword v5, v4, s[36:39], s70 offen offset:0 sc0 sc1\n" \
  "s_mul_i32 s70, s47, 5\n" \
  "s_lshr_b32 s70, s70, 6\n" \
  "buffer_store_dword v5, v4, s[36:39], s70 offen offset:0 sc0 sc1\n" \
  "s_branch Lg3_done_%=\n" \
  "Lg3_slow1_%=:\n" \
  "s_cmp_eq_u32 s54, 1\n" \
  "s_cselect_b32 s68, s50, s49\n" \
  "s_lshr_b32 s68, s68, 5\n" \
  "s_add_u32 m0, s78, 8192\n" \
  "s_nop 0\n" \
  "buffer_load_dword v7, s[36:39], s68 offen offset:0 sc1 lds\n" \
  "s_mul_i32 s73, s48, 7\n" \
  "s_lshr_b32 s73, s73, 1\n" \
  "v_mul_f32 v116, s75, v112\n" \
  "v_fma_f32 v113, v113, s75, v13\n" \
  "v_fma_f32 v113, -v116, v116, v113\n" \
  "v_max_f32 v113, v113, v13\n" \
  "v_rsq_f32 v112, v113\n" \
  "s_nop 0\n" \
  "v_mul_f32 v117, s76, v112\n" \
  "v_mul_f32 v118, v112, v112\n" \
  "v_mul_f32 v119, v113, v112\n" \
  "v_mul_f32 v118, s77, v118\n" \
  "v_mul_f32 v118, v118, v117\n" \
  "v_fma_f32 v28, -v116, v16, v28\n" \
  "v_fma_f32 v29, -v116, v17, v29\n" \
  "v_fma_f32 v30, -v116, v18, v30\n" \
  "v_fma_f32 v31, -v116, v19, v31\n" \
  "v_mul_f32 v84, v28, v28\n" \
  "v_mul_f32 v85, v29, v29\n" \
  "v_mul_f32 v86, v30, v30\n" \
  "v_mul_f32 v87, v31, v31\n" \
  "v_fma_f32 v84, v118, v84, v117\n" \
  "v_fma_f32 v85, v118, v85, v117\n" \
  "v_fma_f32 v86, v118, v86, v117\n" \
  "v_fma_f32 v87, v118, v87, v117\n" \
  "v_mul_f32 v84, v28, v84\n" \
  "v_mul_f32 v85, v29, v85\n" \
  "v_mul_f32 v86, v30, v86\n" \
  "v_mul_f32 v87, v31, v87\n" \
  "v_exp_f32 v84, v84\n" \
  "v_exp_f32 v85, v85\n" \
  "v_exp_f32 v86, v86\n" \
  "v_exp_f32 v87, v87\n" \
  "v_fma_f32 v84, v84, v119, v119\n" \
  "v_fma_f32 v85, v85, v119, v119\n" \
  "v_fma_f32 v86, v86, v119, v119\n" \
  "v_fma_f32 v87, v87, v119, v119\n" \
  "v_rcp_f32 v84, v84\n" \
  "v_rcp_f32 v85, v85\n" \
  "v_rcp_f32 v86, v86\n" \
  "v_rcp_f32 v87, v87\n" \
  "v_mul_f32 v28, v28, v84\n" \
  "v_mul_f32 v29, v29, v85\n" \
  "v_mul_f32 v30, v30, v86\n" \
  "v_mul_f32 v31, v31, v87\n" \
  "v_cvt_pk_bf16_f32 v96, v28, v29\n" \
  "v_cvt_pk_bf16_f32 v97, v30, v31\n" \
  "v_fma_f32 v32, -v116, v20, v32\n" \
  "v_fma_f32 v33, -v116, v21, v33\n" \
  "v_fma_f32 v34, -v116, v22, v34\n" \
  "v_fma_f32 v35, -v116, v23, v35\n" \
  "v_mul_f32 v88, v32, v32\n" \
  "v_mul_f32 v89, v33, v33\n" \
  "v_mul_f32 v90, v34, v34\n" \
  "v_mul_f32 v91, v35, v35\n" \
  "v_fma_f32 v88, v118, v88, v117\n" \
  "v_fma_f32 v89, v118, v89, v117\n" \
  "v_fma_f32 v90, v118, v90, v117\n" \
  "v_fma_f32 v91, v118, v91, v117\n" \
  "v_mul_f32 v88, v32, v88\n" \
  "s_add_u32 m0, s78, 8448\n" \
  "s_nop 0\n" \
  "buffer_load_dword v7, s[36:39], s68 offen offset:0 sc1 lds\n" \
  "v_mul_f32 v89, v33, v89\n" \
  "v_mul_f32 v90, v34, v90\n" \
  "v_mul_f32 v91, v35, v91\n" \
  "v_exp_f32 v88, v88\n" \
  "v_exp_f32 v89, v89\n" \
  "v_exp_f32 v90, v90\n" \
  "v_exp_f32 v91, v91\n" \
  "v_fma_f32 v88, v88, v119, v119\n" \
  "v_fma_f32 v89, v89, v119, v119\n" \
  "v_fma_f32 v90, v90, v119, v119\n" \
  "v_fma_f32 v91, v91, v119, v119\n" \
  "v_rcp_f32 v88, v88\n" \
  "v_rcp_f32 v89, v89\n" \
  "v_rcp_f32 v90, v90\n" \
  "v_rcp_f32 v91, v91\n" \
  "v_mul_f32 v32, v32, v88\n" \
  "v_mul_f32 v33, v33, v89\n" \
  "v_mul_f32 v34, v34, v90\n" \
  "v_mul_f32 v35, v35, v91\n" \
  "v_cvt_pk_bf16_f32 v98, v32, v33\n" \
  "v_cvt_pk_bf16_f32 v99, v34, v35\n" \
  "buffer_store_dwordx4 v[96:99], v3, s[36:39], s73 offen offset:0 sc0 sc1\n" \
  "v_fma_f32 v36, -v116, v24, v36\n" \
  "v_fma_f32 v37, -v116, v25, v37\n" \
  "v_fma_f32 v38, -v116, v26, v38\n" \
  "v_fma_f32 v39, -v116, v27, v39\n" \
  "v_mul_f32 v92, v36, v36\n" \
  "v_mul_f32 v93, v37, v37\n" \
  "v_mul_f32 v94, v38, v38\n" \
  "v_mul_f32 v95, v39, v39\n" \
  "v_fma_f32 v92, v118, v92, v117\n" \
  "v_fma_f32 v93, v118, v93, v117\n" \
  "v_fma_f32 v94, v118, v94, v117\n" \
  "v_fma_f32 v95, v118, v95, v117\n" \
  "v_mul_f32 v92, v36, v92\n" \
  "v_mul_f32 v93, v37, v93\n" \
  "v_mul_f32 v94, v38, v94\n" \
  "v_mul_f32 v95, v39, v95\n" \
  "v_exp_f32 v92, v92\n" \
  "v_exp_f32 v93, v93\n" \
  "v_exp_f32 v94, v94\n" \
  "v_exp_f32 v95, v95\n" \
  "v_fma_f32 v92, v92, v119, v119\n" \
  "v_fma_f32 v93, v93, v119, v119\n" \
  "v_fma_f32 v94, v94, v119, v119\n" \
  "v_fma_f32 v95, v95, v119, v119\n" \
  "v_rcp_f32 v92, v92\n" \
  "v_rcp_f32 v93, v93\n" \
  "v_rcp_f32 v94, v94\n" \
  "v_rcp_f32 v95, v95\n" \
  "v_mul_f32 v36, v36, v92\n" \
  "v_mul_f32 v37, v37, v93\n" \
  "v_mul_f32 v38, v38, v94\n" \
  "v_mul_f32 v39, v39, v95\n" \
  "v_cvt_pk_bf16_f32 v100, v36, v37\n" \
  "v_cvt_pk_bf16_f32 v101, v38, v39\n" \
  "buffer_store_dwordx2 v[100:101], v10, s[36:39], s73 offen offset:0 sc0 sc1\n" \
  "s_cmp_ge_u32 s49, s59\n" \
  "s_cbranch_scc1 Lg3_done1_%=\n" \
  "s_add_u32 m0, s78, 16384\n" \
  "s_nop 0\n" \
  "buffer_load_dword v7, s[36:39], s68 offen offset:0 sc1 lds\n" \
  "s_waitcnt vmcnt(4)\n" \
  "s_barrier\n" \
  "ds_read_b32 v14, v8 offset:8192\n" \
  "s_mul_i32 s70, s47, 5\n" \
  "s_lshr_b32 s70, s70, 6\n" \
  "buffer_store_dword v5, v4, s[36:39], s70 offen offset:0 sc0 sc1\n" \
  "s_mov_b32 s47, s59\n" \
  "s_mov_b32 s69, 18432\n" \
  "s_waitcnt lgkmcnt(0)\n" \
  "v_cmp_eq_u32 vcc, s60, v14\n" \
  "s_nop 0\n" \
  "s_cmp_eq_u32 vcc_lo, -1\n" \
  "s_cbranch_scc1 Lg3_go1_%=\n" \
  "s_add_u32 m0, s78, 16640\n" \
  "s_nop 0\n" \
  "buffer_load_dword v7, s[36:39], s68 offen offset:0 sc1 lds\n" \
  "s_waitcnt vmcnt(5)\n" \
  "s_barrier\n" \
  "ds_read_b32 v14, v8 offset:8448\n" \
  "s_mov_b32 s69, 18688\n" \
  "s_waitcnt lgkmcnt(0)\n" \
  "v_cmp_eq_u32 vcc, s60, v14\n" \
  "s_nop 0\n" \
  "s_cmp_eq_u32 vcc_lo, -1\n" \
  "s_cbranch_scc1 Lg3_go1_%=\n" \
  "s_add_u32 m0, s78, 8192\n" \
  "s_nop 0\n" \
  "buffer_load_dword v7, s[36:39], s68 offen offset:0 sc1 lds\n" \
  "s_waitcnt vmcnt(3)\n" \
  "s_barrier\n" \
  "ds_read_b32 v14, v8 offset:16384\n" \
  "s_mul_i32 s70, s48, 5\n" \
  "s_lshr_b32 s70, s70, 6\n" \
  "buffer_store_dword v5, v4, s[36:39], s70 offen offset:0 sc0 sc1\n" \
  "s_mov_b32 s48, s59\n" \
  "s_mov_b32 s69, 10240\n" \
  "s_waitcnt lgkmcnt(0)\n" \
  "v_cmp_eq_u32 vcc, s60, v14\n" \
  "s_nop 0\n" \
  "s_cmp_eq_u32 vcc_lo, -1\n" \
  "s_cbranch_scc1 Lg3_go1_%=\n" \
  "s_add_u32 m0, s78, 8448\n" \
  "s_nop 0\n" \
  "buffer_load_dword v7, s[36:39], s68 offen offset:0 sc1 lds\n" \
  "s_waitcnt vmcnt(3)\n" \
  "s_barrier\n" \
  "ds_read_b32 v14, v8 offset:16640\n" \
  "s_mov_b32 s69, 10496\n" \
  "s_waitcnt lgkmcnt(0)\n" \
  "v_cmp_eq_u32 vcc, s60, v14\n" \
  "s_nop 0\n" \
  "s_cmp_eq_u32 vcc_lo, -1\n" \
  "s_cbranch_scc1 Lg3_go1_%=\n" \
  "Lg3_poll1_%=:\n" \
  "s_add_u32 m0, s78, 16384\n" \
  "s_nop 0\n" \
  "buffer_load_dword v7, s[36:39], s68 offen offset:0 sc1 lds\n" \
  "s_waitcnt vmcnt(2)\n" \
  "s_barrier\n" \
  "ds_read_b32 v14, v8 offset:8192\n" \
  "s_mov_b32 s69, 18432\n" \
  "s_waitcnt lgkmcnt(0)\n" \
  "v_cmp_eq_u32 vcc, s60, v14\n" \
  "s_nop 0\n" \
  "s_cmp_eq_u32 vcc_lo, -1\n" \
  "s_cbranch_scc1 Lg3_go1_%=\n" \
  "s_add_u32 m0, s78, 16640\n" \
  "s_nop 0\n" \
  "buffer_load_dword v7, s[36:39], s68 offen offset:0 sc1 lds\n" \
  "s_waitcnt vmcnt(2)\n" \
  "s_barrier\n" \
  "ds_read_b32 v14, v8 offset:8448\n" \
  "s_mov_b32 s69, 18688\n" \
  "s_waitcnt lgkmcnt(0)\n" \
  "v_cmp_eq_u32 vcc, s60, v14\n" \
  "s_nop 0\n" \
  "s_cmp_eq_u32 vcc_lo, -1\n" \
  "s_cbranch_scc1 Lg3_go1_%=\n" \
  "s_add_u32 m0, s78, 8192\n" \
  "s_nop 0\n" \
  "buffer_load_dword v7, s[36:39], s68 offen offset:0 sc1 lds\n" \
  "s_waitcnt vmcnt(2)\n" \
  "s_barrier\n" \
  "ds_read_b32 v14, v8 offset:16384\n" \
  "s_mov_b32 s69, 10240\n" \
  "s_waitcnt lgkmcnt(0)\n" \
  "v_cmp_eq_u32 vcc, s60, v14\n" \
  "s_nop 0\n" \
  "s_cmp_eq_u32 vcc_lo, -1\n" \
  "s_cbranch_scc1 Lg3_go1_%=\n" \
  "s_add_u32 m0, s78, 8448\n" \
  "s_nop 0\n" \
  "buffer_load_dword v7, s[36:39], s68 offen offset:0 sc1 lds\n" \
  "s_waitcnt vmcnt(2)\n" \
  "s_barrier\n" \
  "ds_read_b32 v14, v8 offset:16640\n" \
  "s_mov_b32 s69, 10496\n" \
  "s_waitcnt lgkmcnt(0)\n" \
  "v_cmp_eq_u32 vcc, s60, v14\n" \
  "s_nop 0\n" \
  "s_cmp_eq_u32 vcc_lo, -1\n" \
  "s_cbranch_scc1 Lg3_go1_%=\n" \
  "s_add_u32 s79, s79, 1\n" \
  "s_cmp_lt_u32 s79, 20000\n" \
  "s_cbranch_scc1 Lg3_poll1_%=\n" \
  "Lg3_go1_%=:\n" \
  "s_xor_b32 s62, s64, 65536\n" \
  "s_mov_b32 s61, s49\n" \
  "s_mov_b32 m0, s62\n" \
  "s_nop 0\n" \
  "buffer_load_dwordx4 v0, s[36:39], s61 offen offset:0 sc1 lds\n" \
  "buffer_load_dwordx4 v0, s[36:39], s61 offen offset:1024 sc1 lds\n" \
  "buffer_load_dwordx4 v0, s[36:39], s61 offen offset:2048 sc1 lds\n" \
  "buffer_load_dwordx4 v0, s[36:39], s61 offen offset:3072 sc1 lds\n" \
  "s_add_u32 m0, s62, 16384\n" \
  "s_lshr_b32 s72, s61, 5\n" \
  "buffer_load_dwordx4 v1, s[36:39], s61 offen offset:0 sc1 lds\n" \
  "buffer_load_dwordx4 v1, s[36:39], s61 offen offset:1024 sc1 lds\n" \
  "buffer_load_dwordx4 v1, s[36:39], s61 offen offset:2048 sc1 lds\n" \
  "buffer_load_dwordx4 v1, s[36:39], s61 offen offset:3072 sc1 lds\n" \
  "buffer_load_dword v108, v9, s[36:39], s72 offen offset:0 sc1\n" \
  "v_add_u32 v84, s69, v8\n" \
  "s_waitcnt vmcnt(5)\n" \
  "s_barrier\n" \
  "ds_read_b32 v14, v84 offset:0\n" \
  "ds_read_b128 v[52:55], v2 offset:32768\n" \
  "ds_read_b128 v[56:59], v2 offset:33792\n" \
  "ds_read_b128 v[60:63], v2 offset:34816\n" \
  "ds_read_b128 v[64:67], v2 offset:35840\n" \
  "ds_read_b128 v[68:71], v2 offset:36864\n" \
  "s_mul_i32 s70, s47, 5\n" \
  "s_lshr_b32 s70, s70, 6\n" \
  "buffer_store_dword v5, v4, s[36:39], s70 offen offset:0 sc0 sc1\n" \
  "s_mov_b32 s47, s59\n" \
  "s_mul_i32 s70, s48, 5\n" \
  "s_lshr_b32 s70, s70, 6\n" \
  "buffer_store_dword v5, v4, s[36:39], s70 offen offset:0 sc0 sc1\n" \
  "s_mov_b32 s48, s59\n" \
  "s_mul_i32 s70, s59, 5\n" \
  "s_lshr_b32 s70, s70, 6\n" \
  "buffer_store_dword v5, v4, s[36:39], s70 offen offset:0 sc0 sc1\n" \
  "s_waitcnt lgkmcnt(5)\n" \
  "v_cmp_eq_u32 vcc, s60, v14\n" \
  "s_nop 0\n" \
  "s_cmp_eq_u32 vcc_lo, -1\n" \
  "s_cselect_b32 s67, s50, s59\n" \
  "s_lshr_b32 s61, s67, 0\n" \
  "s_xor_b32 s62, s63, 65536\n" \
  "s_waitcnt lgkmcnt(3)\n" \
  "s_branch Lg3_d1_%=\n" \
  "Lg3_done1_%=:\n" \
  "s_waitcnt vmcnt(0)\n" \
  "s_barrier\n" \
  "s_mul_i32 s70, s47, 5\n" \
  "s_lshr_b32 s70, s70, 6\n" \
  "buffer_store_dword v5, v4, s[36:39], s70 offen offset:0 sc0 sc1\n" \
  "s_mul_i32 s70, s48, 5\n" \
  "s_lshr_b32 s70, s70, 6\n" \
  "buffer_store_dword v5, v4, s[36:39], s70 offen offset:0 sc0 sc1\n" \
  "s_branch Lg3_done_%=\n" \
  "Lg3_done_%=:\n" \
  "s_waitcnt vmcnt(0) lgkmcnt(0)\n" \
  "s_barrier\n" \
  "s_endpgm\n" \
  "Lcode_g3_e:\n" \
  :: "s"(ws), "s"(wave), "s"(sStep), "s"(sX1), "s"(sX2), "s"(sU0), "s"(sWw), "s"(sBuf), "v"(vSrc), "v"(vA), "v"(vOut), "v"(vFlag), "v"(vSnapOff), "v"(vPollOff), "v"(vSnapRd), "v"(vAuxS), "v"(vOut2), "v"(vStW), "v"(vStR), "v"(vWd0), "v"(vWd1), "v"(vWr0), "v"(vWr1), "v"(vWr2), "s"(W) : "v0", "v1", "v2", "v3", "v4", "v5", "v6", "v7", "v8", "v9", "v10", "v11", "v12", "v13", "v14", "v15", "v16", "v17", "v18", "v19", "v20", "v21", "v22", "v23", "v24", "v25", "v26", "v27", "v28", "v29", "v30", "v31", "v32", "v33", "v34", "v35", "v36", "v37", "v38", "v39", "v40", "v41", "v42", "v43", "v44", "v45", "v46", "v47", "v48", "v49", "v50", "v51", "v52", "v53", "v54", "v55", "v56", "v57", "v58", "v59", "v60", "v61", "v62", "v63", "v64", "v65", "v66", "v67", "v68", "v69", "v70", "v71", "v72", "v73", "v74", "v75", "v76", "v77", "v78", "v79", "v80", "v81", "v82", "v83", "v84", "v85", "v86", "v87", "v88", "v89", "v90", "v91", "v92", "v93", "v94", "v95", "v96", "v97", "v98", "v99", "v100", "v101", "v102", "v103", "v104", "v105", "v106", "v107", "v108", "v109", "v110", "v111", "v112", "v113", "v114", "v115", "v116", "v117", "v118", "v119", "v120", "v121", "v122", "v123", "v124", "v125", "v126", "v127", "v128", "v129", "v130", "v131", "v132", "v133", "v134", "v135", "v136", "v137", "v138", "v139", "v140", "v141", "v142", "v143", "v144", "v145", "v146", "v147", "v148", "v149", "v150", "v151", "v152", "v153", "v154", "v155", "v156", "v157", "v158", "v159", "v160", "v161", "v162", "v163", "v164", "v165", "v166", "v167", "v168", "v169", "v170", "v171", "v172", "v173", "v174", "v175", "v176", "v177", "v178", "v179", "v180", "v181", "v182", "v183", "v184", "v185", "v186", "v187", "v188", "v189", "v190", "v191", "v192", "v193", "v194", "v195", "v196", "v197", "v198", "v199", "v200", "v201", "v202", "v203", "v204", "v205", "v206", "v207", "v208", "v209", "v210", "v211", "v212", "v213", "v214", "v215", "v216", "v217", "v218", "v219", "v220", "v221", "v222", "v223", "v224", "v225", "v226", "v227", "v228", "v229", "v230", "v231", "v255", "a0", "a1", "a2", "a3", "a4", "a5", "a6", "a7", "a8", "a9", "a10", "a11", "a12", "a13", "a14", "a15", "a16", "a17", "a18", "a19", "a20", "a21", "a22", "a23", "a24", "a25", "a26", "a27", "a28", "a29", "a30", "a31", "a32", "a33", "a34", "a35", "a36", "a37", "a38", "a39", "a40", "a41", "a42", "a43", "a44", "a45", "a46", "a47", "a48", "a49", "a50", "a51", "a52", "a53", "a54", "a55", "a56", "a57", "a58", "a59", "a60", "a61", "a62", "a63", "a64", "a65", "a66", "a67", "a68", "a69", "a70", "a71", "a72", "a73", "a74", "a75", "a76", "a77", "a78", "a79", "a80", "a81", "a82", "a83", "a84", "a85", "a86", "a87", "a88", "a89", "a90", "a91", "a92", "a93", "a94", "a95", "a96", "a97", "a98", "a99", "a100", "a101", "a102", "a103", "a104", "a105", "a106", "a107", "a108", "a109", "a110", "a111", "a112", "a113", "a114", "a115", "a116", "a117", "a118", "a119", "a120", "a121", "a122", "a123", "a124", "a125", "a126", "a127", "a128", "a129", "a130", "a131", "a132", "a133", "a134", "a135", "a136", "a137", "a138", "a139", "a140", "a141", "a142", "a143", "a144", "a145", "a146", "a147", "a148", "a149", "a150", "a151", "a152", "a153", "a154", "a155", "a156", "a157", "a158", "a159", "a160", "a161", "a162", "a163", "a164", "a165", "a166", "a167", "a168", "a169", "a170", "a171", "a172", "a173", "a174", "a175", "a176", "a177", "a178", "a179", "a180", "a181", "a182", "a183", "a184", "a185", "a186", "a187", "a188", "a189", "a190", "a191", "a192", "a193", "a194", "a195", "a196", "a197", "a198", "a199", "a200", "a201", "a202", "a203", "a204", "a205", "a206", "a207", "a208", "a209", "a210", "a211", "a212", "a213", "a214", "a215", "a216", "a217", "a218", "a219", "a220", "a221", "a222", "a223", "a224", "a225", "a226", "a227", "a228", "a229", "a230", "a231", "a232", "a233", "a234", "a235", "a236", "a237", "a238", "a239", "a240", "a241", "a242", "a243", "a244", "a245", "a246", "a247", "a248", "a249", "a250", "a251", "a252", "a253", "a254", "a255", "s36", "s37", "s38", "s39", "s40", "s41", "s42", "s43", "s44", "s45", "s46", "s47", "s48", "s49", "s50", "s51", "s52", "s53", "s54", "s55", "s56", "s57", "s58", "s59", "s60", "s61", "s62", "s63", "s64", "s65", "s66", "s67", "s68", "s69", "s70", "s71", "s72", "s73", "s74", "s75", "s76", "s77", "s78", "s79", "s80", "s81", "s82", "s83", "s84", "s85", "s86", "s87", "s88", "s89", "s90", "s91", "s92", "s93", "s94", "s95", "s96", "s97", "vcc", "scc", "memory")
#define ASM_G3O(W) asm volatile( \
  "Lcode_g3o_b:\n" \
  "s_mov_b64 s[36:37], %0\n" \
  "s_mov_b32 s38, 111193216\n" \
  "s_mov_b32 s39, 0x00020000\n" \
  "s_mov_b32 s54, %1\n" \
  "s_mov_b32 s55, %2\n" \
  "s_mov_b32 s56, %3\n" \
  "s_mov_b32 s58, %4\n" \
  "s_mov_b32 s83, %5\n" \
  "s_mov_b32 s82, %6\n" \
  "s_mov_b32 s84, %7\n" \
  "v_mov_b32 v0, %8\n" \
  "v_mov_b32 v2, %9\n" \
  "v_mov_b32 v3, %10\n" \
  "v_mov_b32 v4, %11\n" \
  "v_mov_b32 v6, %12\n" \
  "v_mov_b32 v7, %13\n" \
  "v_mov_b32 v8, %14\n" \
  "v_mov_b32 v9, %15\n" \
  "v_mov_b32 v10, %16\n" \
  "v_mov_b32 v11, %17\n" \
  "v_mov_b32 v24, %18\n" \
  "v_mov_b32 v25, %19\n" \
  "v_mov_b32 v26, %20\n" \
  "v_mov_b32 v27, %21\n" \
  "v_mov_b32 v28, %22\n" \
  "s_mov_b64 s[40:41], %23\n" \
  "s_mov_b32 s42, 7340032\n" \
  "s_mov_b32 s43, 0x00020000\n" \
  "s_lshl_b32 s88, s54, 11\n" \
  "s_mov_b32 s69, 111193088\n" \
  "buffer_load_dword v5, off, s[36:39], s69 sc1\n" \
  "s_add_u32 s61, s82, 0\n" \
  "s_add_u32 m0, s88, 0\n" \
  "s_nop 0\n" \
  "buffer_load_dwordx4 v24, s[40:43], s61 offen offset:0  lds\n" \
  "s_add_u32 m0, s88, 1024\n" \
  "s_nop 0\n" \
  "buffer_load_dwordx4 v25, s[40:43], s61 offen offset:0  lds\n" \
  "s_add_u32 m0, s88, 8064\n" \
  "s_nop 0\n" \
  "buffer_load_dwordx4 v24, s[40:43], s61 offen offset:128  lds\n" \
  "s_add_u32 m0, s88, 9088\n" \
  "s_nop 0\n" \
  "buffer_load_dwordx4 v25, s[40:43], s61 offen offset:128  lds\n" \
  "s_add_u32 s61, s82, 458752\n" \
  "s_add_u32 m0, s88, 16384\n" \
  "s_nop 0\n" \
  "buffer_load_dwordx4 v24, s[40:43], s61 offen offset:0  lds\n" \
  "s_add_u32 m0, s88, 17408\n" \
  "s_nop 0\n" \
  "buffer_load_dwordx4 v25, s[40:43], s61 offen offset:0  lds\n" \
  "s_add_u32 m0, s88, 24448\n" \
  "s_nop 0\n" \
  "buffer_load_dwordx4 v24, s[40:43], s61 offen offset:128  lds\n" \
  "s_add_u32 m0, s88, 25472\n" \
  "s_nop 0\n" \
  "buffer_load_dwordx4 v25, s[40:43], s61 offen offset:128  lds\n" \
  "s_add_u32 s61, s82, 917504\n" \
  "s_add_u32 m0, s88, 32768\n" \
  "s_nop 0\n" \
  "buffer_load_dwordx4 v24, s[40:43], s61 offen offset:0  lds\n" \
  "s_add_u32 m0, s88, 33792\n" \
  "s_nop 0\n" \
  "buffer_load_dwordx4 v25, s[40:43], s61 offen offset:0  lds\n" \
  "s_add_u32 m0, s88, 40832\n" \
  "s_nop 0\n" \
  "buffer_load_dwordx4 v24, s[40:43], s61 offen offset:128  lds\n" \
  "s_add_u32 m0, s88, 41856\n" \
  "s_nop 0\n" \
  "buffer_load_dwordx4 v25, s[40:43], s61 offen offset:128  lds\n" \
  "s_add_u32 s61, s82, 1376256\n" \
  "s_add_u32 m0, s88, 49152\n" \
  "s_nop 0\n" \
  "buffer_load_dwordx4 v24, s[40:43], s61 offen offset:0  lds\n" \
  "s_add_u32 m0, s88, 50176\n" \
  "s_nop 0\n" \
  "buffer_load_dwordx4 v25, s[40:43], s61 offen offset:0  lds\n" \
  "s_add_u32 m0, s88, 57216\n" \
  "s_nop 0\n" \
  "buffer_load_dwordx4 v24, s[40:43], s61 offen offset:128  lds\n" \
  "s_add_u32 m0, s88, 58240\n" \
  "s_nop 0\n" \
  "buffer_load_dwordx4 v25, s[40:43], s61 offen offset:128  lds\n" \
  "s_add_u32 s61, s82, 1835008\n" \
  "s_add_u32 m0, s88, 65536\n" \
  "s_nop 0\n" \
  "buffer_load_dwordx4 v24, s[40:43], s61 offen offset:0  lds\n" \
  "s_add_u32 m0, s88, 66560\n" \
  "s_nop 0\n" \
  "buffer_load_dwordx4 v25, s[40:43], s61 offen offset:0  lds\n" \
  "s_add_u32 m0, s88, 73600\n" \
  "s_nop 0\n" \
  "buffer_load_dwordx4 v24, s[40:43], s61 offen offset:128  lds\n" \
  "s_add_u32 m0, s88, 74624\n" \
  "s_nop 0\n" \
  "buffer_load_dwordx4 v25, s[40:43], s61 offen offset:128  lds\n" \
  "s_add_u32 s61, s82, 2293760\n" \
  "s_add_u32 m0, s88, 81920\n" \
  "s_nop 0\n" \
  "buffer_load_dwordx4 v24, s[40:43], s61 offen offset:0  lds\n" \
  "s_add_u32 m0, s88, 82944\n" \
  "s_nop 0\n" \
  "buffer_load_dwordx4 v25, s[40:43], s61 offen offset:0  lds\n" \
  "s_add_u32 m0, s88, 89984\n" \
  "s_nop 0\n" \
  "buffer_load_dwordx4 v24, s[40:43], s61 offen offset:128  lds\n" \
  "s_add_u32 m0, s88, 91008\n" \
  "s_nop 0\n" \
  "buffer_load_dwordx4 v25, s[40:43], s61 offen offset:128  lds\n" \
  "s_waitcnt vmcnt(20)\n" \
  "s_waitcnt lgkmcnt(0)\n" \
  "s_barrier\n" \
  "s_add_u32 s61, s82, 2752512\n" \
  "s_add_u32 m0, s88, 98304\n" \
  "s_nop 0\n" \
  "buffer_load_dwordx4 v24, s[40:43], s61 offen offset:0  lds\n" \
  "s_add_u32 m0, s88, 99328\n" \
  "s_nop 0\n" \
  "buffer_load_dwordx4 v25, s[40:43], s61 offen offset:0  lds\n" \
  "s_add_u32 m0, s88, 106368\n" \
  "s_nop 0\n" \
  "buffer_load_dwordx4 v24, s[40:43], s61 offen offset:128  lds\n" \
  "s_add_u32 m0, s88, 107392\n" \
  "s_nop 0\n" \
  "buffer_load_dwordx4 v25, s[40:43], s61 offen offset:128  lds\n" \
  "v_add_u32 v72, 0, v26\n" \
  "v_add_u32 v73, 0, v27\n" \
  "ds_read_b64_tr_b16 a[0:1], v72 offset:0\n" \
  "ds_read_b64_tr_b16 a[2:3], v72 offset:512\n" \
  "ds_read_b64_tr_b16 a[4:5], v73 offset:0\n" \
  "ds_read_b64_tr_b16 a[6:7], v73 offset:512\n" \
  "ds_read_b64_tr_b16 a[8:9], v72 offset:4096\n" \
  "ds_read_b64_tr_b16 a[10:11], v72 offset:4608\n" \
  "ds_read_b64_tr_b16 a[12:13], v73 offset:4096\n" \
  "ds_read_b64_tr_b16 a[14:15], v73 offset:4608\n" \
  "s_waitcnt vmcnt(20)\n" \
  "s_waitcnt lgkmcnt(0)\n" \
  "s_barrier\n" \
  "s_add_u32 s61, s82, 3211264\n" \
  "s_add_u32 m0, s88, 0\n" \
  "s_nop 0\n" \
  "buffer_load_dwordx4 v24, s[40:43], s61 offen offset:0  lds\n" \
  "s_add_u32 m0, s88, 1024\n" \
  "s_nop 0\n" \
  "buffer_load_dwordx4 v25, s[40:43], s61 offen offset:0  lds\n" \
  "s_add_u32 m0, s88, 8064\n" \
  "s_nop 0\n" \
  "buffer_load_dwordx4 v24, s[40:43], s61 offen offset:128  lds\n" \
  "s_add_u32 m0, s88, 9088\n" \
  "s_nop 0\n" \
  "buffer_load_dwordx4 v25, s[40:43], s61 offen offset:128  lds\n" \
  "v_add_u32 v72, 16384, v26\n" \
  "v_add_u32 v73, 16384, v27\n" \
  "ds_read_b64_tr_b16 a[16:17], v72 offset:0\n" \
  "ds_read_b64_tr_b16 a[18:19], v72 offset:512\n" \
  "ds_read_b64_tr_b16 a[20:21], v73 offset:0\n" \
  "ds_read_b64_tr_b16 a[22:23], v73 offset:512\n" \
  "ds_read_b64_tr_b16 a[24:25], v72 offset:4096\n" \
  "ds_read_b64_tr_b16 a[26:27], v72 offset:4608\n" \
  "ds_read_b64_tr_b16 a[28:29], v73 offset:4096\n" \
  "ds_read_b64_tr_b16 a[30:31], v73 offset:4608\n" \
  "s_waitcnt vmcnt(20)\n" \
  "s_waitcnt lgkmcnt(0)\n" \
  "s_barrier\n" \
  "s_add_u32 s61, s82, 3670016\n" \
  "s_add_u32 m0, s88, 16384\n" \
  "s_nop 0\n" \
  "buffer_load_dwordx4 v24, s[40:43], s61 offen offset:0  lds\n" \
  "s_add_u32 m0, s88, 17408\n" \
  "s_nop 0\n" \
  "buffer_load_dwordx4 v25, s[40:43], s61 offen offset:0  lds\n" \
  "s_add_u32 m0, s88, 24448\n" \
  "s_nop 0\n" \
  "buffer_load_dwordx4 v24, s[40:43], s61 offen offset:128  lds\n" \
  "s_add_u32 m0, s88, 25472\n" \
  "s_nop 0\n" \
  "buffer_load_dwordx4 v25, s[40:43], s61 offen offset:128  lds\n" \
  "v_add_u32 v72, 32768, v26\n" \
  "v_add_u32 v73, 32768, v27\n" \
  "ds_read_b64_tr_b16 a[32:33], v72 offset:0\n" \
  "ds_read_b64_tr_b16 a[34:35], v72 offset:512\n" \
  "ds_read_b64_tr_b16 a[36:37], v73 offset:0\n" \
  "ds_read_b64_tr_b16 a[38:39], v73 offset:512\n" \
  "ds_read_b64_tr_b16 a[40:41], v72 offset:4096\n" \
  "ds_read_b64_tr_b16 a[42:43], v72 offset:4608\n" \
  "ds_read_b64_tr_b16 a[44:45], v73 offset:4096\n" \
  "ds_read_b64_tr_b16 a[46:47], v73 offset:4608\n" \
  "s_waitcnt vmcnt(20)\n" \
  "s_waitcnt lgkmcnt(0)\n" \
  "s_barrier\n" \
  "s_add_u32 s61, s82, 4128768\n" \
  "s_add_u32 m0, s88, 32768\n" \
  "s_nop 0\n" \
  "buffer_load_dwordx4 v24, s[40:43], s61 offen offset:0  lds\n" \
  "s_add_u32 m0, s88, 33792\n" \
  "s_nop 0\n" \
  "buffer_load_dwordx4 v25, s[40:43], s61 offen offset:0  lds\n" \
  "s_add_u32 m0, s88, 40832\n" \
  "s_nop 0\n" \
  "buffer_load_dwordx4 v24, s[40:43], s61 offen offset:128  lds\n" \
  "s_add_u32 m0, s88, 41856\n" \
  "s_nop 0\n" \
  "buffer_load_dwordx4 v25, s[40:43], s61 offen offset:128  lds\n" \
  "v_add_u32 v72, 49152, v26\n" \
  "v_add_u32 v73, 49152, v27\n" \
  "ds_read_b64_tr_b16 a[48:49], v72 offset:0\n" \
  "ds_read_b64_tr_b16 a[50:51], v72 offset:512\n" \
  "ds_read_b64_tr_b16 a[52:53], v73 offset:0\n" \
  "ds_read_b64_tr_b16 a[54:55], v73 offset:512\n" \
  "ds_read_b64_tr_b16 a[56:57], v72 offset:4096\n" \
  "ds_read_b64_tr_b16 a[58:59], v72 offset:4608\n" \
  "ds_read_b64_tr_b16 a[60:61], v73 offset:4096\n" \
  "ds_read_b64_tr_b16 a[62:63], v73 offset:4608\n" \
  "s_waitcnt vmcnt(20)\n" \
  "s_waitcnt lgkmcnt(0)\n" \
  "s_barrier\n" \
  "s_add_u32 s61, s82, 4587520\n" \
  "s_add_u32 m0, s88, 49152\n" \
  "s_nop 0\n" \
  "buffer_load_dwordx4 v24, s[40:43], s61 offen offset:0  lds\n" \
  "s_add_u32 m0, s88, 50176\n" \
  "s_nop 0\n" \
  "buffer_load_dwordx4 v25, s[40:43], s61 offen offset:0  lds\n" \
  "s_add_u32 m0, s88, 57216\n" \
  "s_nop 0\n" \
  "buffer_load_dwordx4 v24, s[40:43], s61 offen offset:128  lds\n" \
  "s_add_u32 m0, s88, 58240\n" \
  "s_nop 0\n" \
  "buffer_load_dwordx4 v25, s[40:43], s61 offen offset:128  lds\n" \
  "v_add_u32 v72, 65536, v26\n" \
  "v_add_u32 v73, 65536, v27\n" \
  "ds_read_b64_tr_b16 a[64:65], v72 offset:0\n" \
  "ds_read_b64_tr_b16 a[66:67], v72 offset:512\n" \
  "ds_read_b64_tr_b16 a[68:69], v73 offset:0\n" \
  "ds_read_b64_tr_b16 a[70:71], v73 offset:512\n" \
  "ds_read_b64_tr_b16 a[72:73], v72 offset:4096\n" \
  "ds_read_b64_tr_b16 a[74:75], v72 offset:4608\n" \
  "ds_read_b64_tr_b16 a[76:77], v73 offset:4096\n" \
  "ds_read_b64_tr_b16 a[78:79], v73 offset:4608\n" \
  "s_waitcnt vmcnt(20)\n" \
  "s_waitcnt lgkmcnt(0)\n" \
  "s_barrier\n" \
  "s_add_u32 s61, s82, 5046272\n" \
  "s_add_u32 m0, s88, 65536\n" \
  "s_nop 0\n" \
  "buffer_load_dwordx4 v24, s[40:43], s61 offen offset:0  lds\n" \
  "s_add_u32 m0, s88, 66560\n" \
  "s_nop 0\n" \
  "buffer_load_dwordx4 v25, s[40:43], s61 offen offset:0  lds\n" \
  "s_add_u32 m0, s88, 73600\n" \
  "s_nop 0\n" \
  "buffer_load_dwordx4 v24, s[40:43], s61 offen offset:128  lds\n" \
  "s_add_u32 m0, s88, 74624\n" \
  "s_nop 0\n" \
  "buffer_load_dwordx4 v25, s[40:43], s61 offen offset:128  lds\n" \
  "v_add_u32 v72, 81920, v26\n" \
  "v_add_u32 v73, 81920, v27\n" \
  "ds_read_b64_tr_b16 a[80:81], v72 offset:0\n" \
  "ds_read_b64_tr_b16 a[82:83], v72 offset:512\n" \
  "ds_read_b64_tr_b16 a[84:85], v73 offset:0\n" \
  "ds_read_b64_tr_b16 a[86:87], v73 offset:512\n" \
  "ds_read_b64_tr_b16 a[88:89], v72 offset:4096\n" \
  "ds_read_b64_tr_b16 a[90:91], v72 offset:4608\n" \
  "ds_read_b64_tr_b16 a[92:93], v73 offset:4096\n" \
  "ds_read_b64_tr_b16 a[94:95], v73 offset:4608\n" \
  "s_waitcnt vmcnt(20)\n" \
  "s_waitcnt lgkmcnt(0)\n" \
  "s_barrier\n" \
  "s_add_u32 s61, s82, 5505024\n" \
  "s_add_u32 m0, s88, 81920\n" \
  "s_nop 0\n" \
  "buffer_load_dwordx4 v24, s[40:43], s61 offen offset:0  lds\n" \
  "s_add_u32 m0, s88, 82944\n" \
  "s_nop 0\n" \
  "buffer_load_dwordx4 v25, s[40:43], s61 offen offset:0  lds\n" \
  "s_add_u32 m0, s88, 89984\n" \
  "s_nop 0\n" \
  "buffer_load_dwordx4 v24, s[40:43], s61 offen offset:128  lds\n" \
  "s_add_u32 m0, s88, 91008\n" \
  "s_nop 0\n" \
  "buffer_load_dwordx4 v25, s[40:43], s61 offen offset:128  lds\n" \
  "v_add_u32 v72, 98304, v26\n" \
  "v_add_u32 v73, 98304, v27\n" \
  "ds_read_b64_tr_b16 a[96:97], v72 offset:0\n" \
  "ds_read_b64_tr_b16 a[98:99], v72 offset:512\n" \
  "ds_read_b64_tr_b16 a[100:101], v73 offset:0\n" \
  "ds_read_b64_tr_b16 a[102:103], v73 offset:512\n" \
  "ds_read_b64_tr_b16 a[104:105], v72 offset:4096\n" \
  "ds_read_b64_tr_b16 a[106:107], v72 offset:4608\n" \
  "ds_read_b64_tr_b16 a[108:109], v73 offset:4096\n" \
  "ds_read_b64_tr_b16 a[110:111], v73 offset:4608\n" \
  "s_waitcnt vmcnt(20)\n" \
  "s_waitcnt lgkmcnt(0)\n" \
  "s_barrier\n" \
  "s_add_u32 s61, s82, 5963776\n" \
  "s_add_u32 m0, s88, 98304\n" \
  "s_nop 0\n" \
  "buffer_load_dwordx4 v24, s[40:43], s61 offen offset:0  lds\n" \
  "s_add_u32 m0, s88, 99328\n" \
  "s_nop 0\n" \
  "buffer_load_dwordx4 v25, s[40:43], s61 offen offset:0  lds\n" \
  "s_add_u32 m0, s88, 106368\n" \
  "s_nop 0\n" \
  "buffer_load_dwordx4 v24, s[40:43], s61 offen offset:128  lds\n" \
  "s_add_u32 m0, s88, 107392\n" \
  "s_nop 0\n" \
  "buffer_load_dwordx4 v25, s[40:43], s61 offen offset:128  lds\n" \
  "v_add_u32 v72, 0, v26\n" \
  "v_add_u32 v73, 0, v27\n" \
  "ds_read_b64_tr_b16 a[112:113], v72 offset:0\n" \
  "ds_read_b64_tr_b16 a[114:115], v72 offset:512\n" \
  "ds_read_b64_tr_b16 a[116:117], v73 offset:0\n" \
  "ds_read_b64_tr_b16 a[118:119], v73 offset:512\n" \
  "ds_read_b64_tr_b16 a[120:121], v72 offset:4096\n" \
  "ds_read_b64_tr_b16 a[122:123], v72 offset:4608\n" \
  "ds_read_b64_tr_b16 a[124:125], v73 offset:4096\n" \
  "ds_read_b64_tr_b16 a[126:127], v73 offset:4608\n" \
  "s_waitcnt vmcnt(20)\n" \
  "s_waitcnt lgkmcnt(0)\n" \
  "s_barrier\n" \
  "s_add_u32 s61, s82, 6422528\n" \
  "s_add_u32 m0, s88, 0\n" \
  "s_nop 0\n" \
  "buffer_load_dwordx4 v24, s[40:43], s61 offen offset:0  lds\n" \
  "s_add_u32 m0, s88, 1024\n" \
  "s_nop 0\n" \
  "buffer_load_dwordx4 v25, s[40:43], s61 offen offset:0  lds\n" \
  "s_add_u32 m0, s88, 8064\n" \
  "s_nop 0\n" \
  "buffer_load_dwordx4 v24, s[40:43], s61 offen offset:128  lds\n" \
  "s_add_u32 m0, s88, 9088\n" \
  "s_nop 0\n" \
  "buffer_load_dwordx4 v25, s[40:43], s61 offen offset:128  lds\n" \
  "v_add_u32 v72, 16384, v26\n" \
  "v_add_u32 v73, 16384, v27\n" \
  "ds_read_b64_tr_b16 a[128:129], v72 offset:0\n" \
  "ds_read_b64_tr_b16 a[130:131], v72 offset:512\n" \
  "ds_read_b64_tr_b16 a[132:133], v73 offset:0\n" \
  "ds_read_b64_tr_b16 a[134:135], v73 offset:512\n" \
  "ds_read_b64_tr_b16 a[136:137], v72 offset:4096\n" \
  "ds_read_b64_tr_b16 a[138:139], v72 offset:4608\n" \
  "ds_read_b64_tr_b16 a[140:141], v73 offset:4096\n" \
  "ds_read_b64_tr_b16 a[142:143], v73 offset:4608\n" \
  "s_waitcnt vmcnt(20)\n" \
  "s_waitcnt lgkmcnt(0)\n" \
  "s_barrier\n" \
  "s_add_u32 s61, s82, 6881280\n" \
  "s_add_u32 m0, s88, 16384\n" \
  "s_nop 0\n" \
  "buffer_load_dwordx4 v24, s[40:43], s61 offen offset:0  lds\n" \
  "s_add_u32 m0, s88, 17408\n" \
  "s_nop 0\n" \
  "buffer_load_dwordx4 v25, s[40:43], s61 offen offset:0  lds\n" \
  "s_add_u32 m0, s88, 24448\n" \
  "s_nop 0\n" \
  "buffer_load_dwordx4 v24, s[40:43], s61 offen offset:128  lds\n" \
  "s_add_u32 m0, s88, 25472\n" \
  "s_nop 0\n" \
  "buffer_load_dwordx4 v25, s[40:43], s61 offen offset:128  lds\n" \
  "v_add_u32 v72, 32768, v26\n" \
  "v_add_u32 v73, 32768, v27\n" \
  "ds_read_b64_tr_b16 a[144:145], v72 offset:0\n" \
  "ds_read_b64_tr_b16 a[146:147], v72 offset:512\n" \
  "ds_read_b64_tr_b16 a[148:149], v73 offset:0\n" \
  "ds_read_b64_tr_b16 a[150:151], v73 offset:512\n" \
  "ds_read_b64_tr_b16 a[152:153], v72 offset:4096\n" \
  "ds_read_b64_tr_b16 a[154:155], v72 offset:4608\n" \
  "ds_read_b64_tr_b16 a[156:157], v73 offset:4096\n" \
  "ds_read_b64_tr_b16 a[158:159], v73 offset:4608\n" \
  "s_waitcnt vmcnt(20)\n" \
  "s_waitcnt lgkmcnt(0)\n" \
  "s_barrier\n" \
  "v_add_u32 v72, 49152, v26\n" \
  "v_add_u32 v73, 49152, v27\n" \
  "ds_read_b64_tr_b16 a[160:161], v72 offset:0\n" \
  "ds_read_b64_tr_b16 a[162:163], v72 offset:512\n" \
  "ds_read_b64_tr_b16 a[164:165], v73 offset:0\n" \
  "ds_read_b64_tr_b16 a[166:167], v73 offset:512\n" \
  "ds_read_b64_tr_b16 a[168:169], v72 offset:4096\n" \
  "ds_read_b64_tr_b16 a[170:171], v72 offset:4608\n" \
  "ds_read_b64_tr_b16 a[172:173], v73 offset:4096\n" \
  "ds_read_b64_tr_b16 a[174:175], v73 offset:4608\n" \
  "s_waitcnt vmcnt(16)\n" \
  "s_waitcnt lgkmcnt(0)\n" \
  "s_barrier\n" \
  "v_add_u32 v72, 65536, v26\n" \
  "v_add_u32 v73, 65536, v27\n" \
  "ds_read_b64_tr_b16 a[176:177], v72 offset:0\n" \
  "ds_read_b64_tr_b16 a[178:179], v72 offset:512\n" \
  "ds_read_b64_tr_b16 a[180:181], v73 offset:0\n" \
  "ds_read_b64_tr_b16 a[182:183], v73 offset:512\n" \
  "ds_read_b64_tr_b16 a[184:185], v72 offset:4096\n" \
  "ds_read_b64_tr_b16 a[186:187], v72 offset:4608\n" \
  "ds_read_b64_tr_b16 a[188:189], v73 offset:4096\n" \
  "ds_read_b64_tr_b16 a[190:191], v73 offset:4608\n" \
  "s_waitcnt vmcnt(12)\n" \
  "s_waitcnt lgkmcnt(0)\n" \
  "s_barrier\n" \
  "v_add_u32 v72, 81920, v26\n" \
  "v_add_u32 v73, 81920, v27\n" \
  "ds_read_b64_tr_b16 a[192:193], v72 offset:0\n" \
  "ds_read_b64_tr_b16 a[194:195], v72 offset:512\n" \
  "ds_read_b64_tr_b16 a[196:197], v73 offset:0\n" \
  "ds_read_b64_tr_b16 a[198:199], v73 offset:512\n" \
  "ds_read_b64_tr_b16 a[200:201], v72 offset:4096\n" \
  "ds_read_b64_tr_b16 a[202:203], v72 offset:4608\n" \
  "ds_read_b64_tr_b16 a[204:205], v73 offset:4096\n" \
  "ds_read_b64_tr_b16 a[206:207], v73 offset:4608\n" \
  "s_waitcnt vmcnt(8)\n" \
  "s_waitcnt lgkmcnt(0)\n" \
  "s_barrier\n" \
  "v_add_u32 v72, 98304, v26\n" \
  "v_add_u32 v73, 98304, v27\n" \
  "ds_read_b64_tr_b16 a[208:209], v72 offset:0\n" \
  "ds_read_b64_tr_b16 a[210:211], v72 offset:512\n" \
  "ds_read_b64_tr_b16 a[212:213], v73 offset:0\n" \
  "ds_read_b64_tr_b16 a[214:215], v73 offset:512\n" \
  "ds_read_b64_tr_b16 a[216:217], v72 offset:4096\n" \
  "ds_read_b64_tr_b16 a[218:219], v72 offset:4608\n" \
  "ds_read_b64_tr_b16 a[220:221], v73 offset:4096\n" \
  "ds_read_b64_tr_b16 a[222:223], v73 offset:4608\n" \
  "s_waitcnt vmcnt(4)\n" \
  "s_waitcnt lgkmcnt(0)\n" \
  "s_barrier\n" \
  "v_add_u32 v72, 0, v26\n" \
  "v_add_u32 v73, 0, v27\n" \
  "ds_read_b64_tr_b16 a[224:225], v72 offset:0\n" \
  "ds_read_b64_tr_b16 a[226:227], v72 offset:512\n" \
  "ds_read_b64_tr_b16 a[228:229], v73 offset:0\n" \
  "ds_read_b64_tr_b16 a[230:231], v73 offset:512\n" \
  "ds_read_b64_tr_b16 a[232:233], v72 offset:4096\n" \
  "ds_read_b64_tr_b16 a[234:235], v72 offset:4608\n" \
  "ds_read_b64_tr_b16 a[236:237], v73 offset:4096\n" \
  "ds_read_b64_tr_b16 a[238:239], v73 offset:4608\n" \
  "s_waitcnt vmcnt(0)\n" \
  "s_waitcnt lgkmcnt(0)\n" \
  "s_barrier\n" \
  "v_add_u32 v72, 16384, v26\n" \
  "v_add_u32 v73, 16384, v27\n" \
  "ds_read_b64_tr_b16 a[240:241], v72 offset:0\n" \
  "ds_read_b64_tr_b16 a[242:243], v72 offset:512\n" \
  "ds_read_b64_tr_b16 a[244:245], v73 offset:0\n" \
  "ds_read_b64_tr_b16 a[246:247], v73 offset:512\n" \
  "ds_read_b64_tr_b16 a[248:249], v72 offset:4096\n" \
  "ds_read_b64_tr_b16 a[250:251], v72 offset:4608\n" \
  "ds_read_b64_tr_b16 a[252:253], v73 offset:4096\n" \
  "ds_read_b64_tr_b16 a[254:255], v73 offset:4608\n" \
  "s_waitcnt lgkmcnt(0)\n" \
  "s_barrier\n" \
  "v_readfirstlane_b32 s60, v5\n" \
  "s_nop 3\n" \
  "s_add_u32 s60, s60, 1\n" \
  "s_cmp_eq_u32 s60, 0\n" \
  "s_cselect_b32 s60, 1, s60\n" \
  "v_mov_b32 v40, 0x3f803f80\n" \
  "v_mov_b32 v41, 0x3f803f80\n" \
  "v_mov_b32 v42, 0x3f803f80\n" \
  "v_mov_b32 v43, 0x3f803f80\n" \
  "s_nop 1\n" \
  "v_mfma_f32_16x16x32_bf16 v[16:19], a[0:3], v[40:43], 0\n" \
  "v_mfma_f32_16x16x32_bf16 v[20:23], a[4:7], v[40:43], 0\n" \
  "v_mfma_f32_16x16x32_bf16 v[16:19], a[8:11], v[40:43], v[16:19]\n" \
  "v_mfma_f32_16x16x32_bf16 v[20:23], a[12:15], v[40:43], v[20:23]\n" \
  "v_mfma_f32_16x16x32_bf16 v[16:19], a[16:19], v[40:43], v[16:19]\n" \
  "v_mfma_f32_16x16x32_bf16 v[20:23], a[20:23], v[40:43], v[20:23]\n" \
  "v_mfma_f32_16x16x32_bf16 v[16:19], a[24:27], v[40:43], v[16:19]\n" \
  "v_mfma_f32_16x16x32_bf16 v[20:23], a[28:31], v[40:43], v[20:23]\n" \
  "v_mfma_f32_16x16x32_bf16 v[16:19], a[32:35], v[40:43], v[16:19]\n" \
  "v_mfma_f32_16x16x32_bf16 v[20:23], a[36:39], v[40:43], v[20:23]\n" \
  "v_mfma_f32_16x16x32_bf16 v[16:19], a[40:43], v[40:43], v[16:19]\n" \
  "v_mfma_f32_16x16x32_bf16 v[20:23], a[44:47], v[40:43], v[20:23]\n" \
  "v_mfma_f32_16x16x32_bf16 v[16:19], a[48:51], v[40:43], v[16:19]\n" \
  "v_mfma_f32_16x16x32_bf16 v[20:23], a[52:55], v[40:43], v[20:23]\n" \
  "v_mfma_f32_16x16x32_bf16 v[16:19], a[56:59], v[40:43], v[16:19]\n" \
  "v_mfma_f32_16x16x32_bf16 v[20:23], a[60:63], v[40:43], v[20:23]\n" \
  "v_mfma_f32_16x16x32_bf16 v[16:19], a[64:67], v[40:43], v[16:19]\n" \
  "v_mfma_f32_16x16x32_bf16 v[20:23], a[68:71], v[40:43], v[20:23]\n" \
  "v_mfma_f32_16x16x32_bf16 v[16:19], a[72:75], v[40:43], v[16:19]\n" \
  "v_mfma_f32_16x16x32_bf16 v[20:23], a[76:79], v[40:43], v[20:23]\n" \
  "v_mfma_f32_16x16x32_bf16 v[16:19], a[80:83], v[40:43], v[16:19]\n" \
  "v_mfma_f32_16x16x32_bf16 v[20:23], a[84:87], v[40:43], v[20:23]\n" \
  "v_mfma_f32_16x16x32_bf16 v[16:19], a[88:91], v[40:43], v[16:19]\n" \
  "v_mfma_f32_16x16x32_bf16 v[20:23], a[92:95], v[40:43], v[20:23]\n" \
  "v_mfma_f32_16x16x32_bf16 v[16:19], a[96:99], v[40:43], v[16:19]\n" \
  "v_mfma_f32_16x16x32_bf16 v[20:23], a[100:103], v[40:43], v[20:23]\n" \
  "v_mfma_f32_16x16x32_bf16 v[16:19], a[104:107], v[40:43], v[16:19]\n" \
  "v_mfma_f32_16x16x32_bf16 v[20:23], a[108:111], v[40:43], v[20:23]\n" \
  "v_mfma_f32_16x16x32_bf16 v[16:19], a[112:115], v[40:43], v[16:19]\n" \
  "v_mfma_f32_16x16x32_bf16 v[20:23], a[116:119], v[40:43], v[20:23]\n" \
  "v_mfma_f32_16x16x32_bf16 v[16:19], a[120:123], v[40:43], v[16:19]\n" \
  "v_mfma_f32_16x16x32_bf16 v[20:23], a[124:127], v[40:43], v[20:23]\n" \
  "v_mfma_f32_16x16x32_bf16 v[16:19], a[128:131], v[40:43], v[16:19]\n" \
  "v_mfma_f32_16x16x32_bf16 v[20:23], a[132:135], v[40:43], v[20:23]\n" \
  "v_mfma_f32_16x16x32_bf16 v[16:19], a[136:139], v[40:43], v[16:19]\n" \
  "v_mfma_f32_16x16x32_bf16 v[20:23], a[140:143], v[40:43], v[20:23]\n" \
  "v_mfma_f32_16x16x32_bf16 v[16:19], a[144:147], v[40:43], v[16:19]\n" \
  "v_mfma_f32_16x16x32_bf16 v[20:23], a[148:151], v[40:43], v[20:23]\n" \
  "v_mfma_f32_16x16x32_bf16 v[16:19], a[152:155], v[40:43], v[16:19]\n" \
  "v_mfma_f32_16x16x32_bf16 v[20:23], a[156:159], v[40:43], v[20:23]\n" \
  "v_mfma_f32_16x16x32_bf16 v[16:19], a[160:163], v[40:43], v[16:19]\n" \
  "v_mfma_f32_16x16x32_bf16 v[20:23], a[164:167], v[40:43], v[20:23]\n" \
  "v_mfma_f32_16x16x32_bf16 v[16:19], a[168:171], v[40:43], v[16:19]\n" \
  "v_mfma_f32_16x16x32_bf16 v[20:23], a[172:175], v[40:43], v[20:23]\n" \
  "v_mfma_f32_16x16x32_bf16 v[16:19], a[176:179], v[40:43], v[16:19]\n" \
  "v_mfma_f32_16x16x32_bf16 v[20:23], a[180:183], v[40:43], v[20:23]\n" \
  "v_mfma_f32_16x16x32_bf16 v[16:19], a[184:187], v[40:43], v[16:19]\n" \
  "v_mfma_f32_16x16x32_bf16 v[20:23], a[188:191], v[40:43], v[20:23]\n" \
  "v_mfma_f32_16x16x32_bf16 v[16:19], a[192:195], v[40:43], v[16:19]\n" \
  "v_mfma_f32_16x16x32_bf16 v[20:23], a[196:199], v[40:43], v[20:23]\n" \
  "v_mfma_f32_16x16x32_bf16 v[16:19], a[200:203], v[40:43], v[16:19]\n" \
  "v_mfma_f32_16x16x32_bf16 v[20:23], a[204:207], v[40:43], v[20:23]\n" \
  "v_mfma_f32_16x16x32_bf16 v[16:19], a[208:211], v[40:43], v[16:19]\n" \
  "v_mfma_f32_16x16x32_bf16 v[20:23], a[212:215], v[40:43], v[20:23]\n" \
  "v_mfma_f32_16x16x32_bf16 v[16:19], a[216:219], v[40:43], v[16:19]\n" \
  "v_mfma_f32_16x16x32_bf16 v[20:23], a[220:223], v[40:43], v[20:23]\n" \
  "v_mfma_f32_16x16x32_bf16 v[16:19], a[224:227], v[40:43], v[16:19]\n" \
  "v_mfma_f32_16x16x32_bf16 v[20:23], a[228:231], v[40:43], v[20:23]\n" \
  "v_mfma_f32_16x16x32_bf16 v[16:19], a[232:235], v[40:43], v[16:19]\n" \
  "v_mfma_f32_16x16x32_bf16 v[20:23], a[236:239], v[40:43], v[20:23]\n" \
  "v_mfma_f32_16x16x32_bf16 v[16:19], a[240:243], v[40:43], v[16:19]\n" \
  "v_mfma_f32_16x16x32_bf16 v[20:23], a[244:247], v[40:43], v[20:23]\n" \
  "v_mfma_f32_16x16x32_bf16 v[16:19], a[248:251], v[40:43], v[16:19]\n" \
  "v_mfma_f32_16x16x32_bf16 v[20:23], a[252:255], v[40:43], v[20:23]\n" \
  "v_mov_b32 v5, s60\n" \
  "s_mov_b32 s59, 536870912\n" \
  "s_add_u32 s57, s56, 131072\n" \
  "s_mov_b32 s79, 0\n" \
  "s_mov_b32 s75, 0x3a800000\n" \
  "s_mov_b32 s76, 0xc0135761\n" \
  "s_mov_b32 s77, 0x3d372713\n" \
  "s_mov_b32 s44, s59\n" \
  "s_mov_b32 s45, s59\n" \
  "s_mov_b32 s46, s59\n" \
  "s_mov_b32 s47, s59\n" \
  "s_mov_b32 s48, s83\n" \
  "s_add_u32 s49, s48, s55\n" \
  "s_cmp_eq_u32 s49, s56\n" \
  "s_cselect_b32 s49, s57, s49\n" \
  "s_cmp_eq_u32 s49, s58\n" \
  "s_cselect_b32 s49, s59, s49\n" \
  "s_add_u32 s50, s49, s55\n" \
  "s_cmp_eq_u32 s50, s56\n" \
  "s_cselect_b32 s50, s57, s50\n" \
  "s_cmp_eq_u32 s50, s58\n" \
  "s_cselect_b32 s50, s59, s50\n" \
  "s_add_u32 s51, s50, s55\n" \
  "s_cmp_eq_u32 s51, s56\n" \
  "s_cselect_b32 s51, s57, s51\n" \
  "s_cmp_eq_u32 s51, s58\n" \
  "s_cselect_b32 s51, s59, s51\n" \
  "s_add_u32 s52, s51, s55\n" \
  "s_cmp_eq_u32 s52, s56\n" \
  "s_cselect_b32 s52, s57, s52\n" \
  "s_cmp_eq_u32 s52, s58\n" \
  "s_cselect_b32 s52, s59, s52\n" \
  "s_lshl_b32 s63, s54, 12\n" \
  "s_add_u32 s63, s63, 65536\n" \
  "s_add_u32 s64, s63, 32768\n" \
  "v_add_u32 v1, 16384, v0\n" \
  "s_lshl_b32 s88, s54, 10\n" \
  "s_add_u32 s88, s88, 131072\n" \
  "s_lshl_b32 s78, s54, 11\n" \
  "s_add_u32 s78, s78, 132608\n" \
  "v_mov_b32 v12, 0x3727c5ac\n" \
  "s_mov_b32 s66, s59\n" \
  "s_branch Lg3o_slow0_%=\n" \
  "Lg3o_d0_%=:\n" \
  "v_mfma_f32_16x16x32_bf16 v[24:27], a[0:3], v[40:43], 0\n" \
  "s_mul_i32 s73, s47, 7\n" \
  "ds_read_b128 v[68:71], v2 offset:7168\n" \
  "v_mul_f32 v96, s75, v92\n" \
  "v_mfma_f32_16x16x32_bf16 v[28:31], a[4:7], v[40:43], 0\n" \
  "s_lshr_b32 s73, s73, 1\n" \
  "v_fma_f32 v93, v93, s75, v12\n" \
  "v_fma_f32 v93, -v96, v96, v93\n" \
  "v_mfma_f32_16x16x32_bf16 v[24:27], a[8:11], v[44:47], v[24:27]\n" \
  "s_cmp_eq_u32 s54, 1\n" \
  "ds_read_b128 v[40:43], v2 offset:8192\n" \
  "v_max_f32 v93, v93, v12\n" \
  "v_mfma_f32_16x16x32_bf16 v[28:31], a[12:15], v[44:47], v[28:31]\n" \
  "s_cselect_b32 s68, s51, s50\n" \
  "v_rsq_f32 v92, v93\n" \
  "v_fma_f32 v32, -v96, v16, v32\n" \
  "v_mfma_f32_16x16x32_bf16 v[24:27], a[16:19], v[48:51], v[24:27]\n" \
  "s_lshr_b32 s68, s68, 5\n" \
  "ds_read_b128 v[44:47], v2 offset:9216\n" \
  "v_mul_f32 v97, s76, v92\n" \
  "v_mfma_f32_16x16x32_bf16 v[28:31], a[20:23], v[48:51], v[28:31]\n" \
  "s_mul_i32 s70, s45, 5\n" \
  "v_mul_f32 v98, v92, v92\n" \
  "v_mul_f32 v99, v93, v92\n" \
  "v_mul_f32 v98, s77, v98\n" \
  "v_mfma_f32_16x16x32_bf16 v[24:27], a[24:27], v[52:55], v[24:27]\n" \
  "s_lshr_b32 s70, s70, 6\n" \
  "ds_read_b128 v[48:51], v2 offset:10240\n" \
  "v_mul_f32 v98, v98, v97\n" \
  "v_mfma_f32_16x16x32_bf16 v[28:31], a[28:31], v[52:55], v[28:31]\n" \
  "s_waitcnt lgkmcnt(3)\n" \
  "v_fma_f32 v33, -v96, v17, v33\n" \
  "v_fma_f32 v34, -v96, v18, v34\n" \
  "v_fma_f32 v35, -v96, v19, v35\n" \
  "v_mfma_f32_16x16x32_bf16 v[24:27], a[32:35], v[56:59], v[24:27]\n" \
  "s_add_u32 m0, s78, 0\n" \
  "ds_read_b128 v[52:55], v2 offset:11264\n" \
  "v_mul_f32 v72, v32, v32\n" \
  "v_mfma_f32_16x16x32_bf16 v[28:31], a[36:39], v[56:59], v[28:31]\n" \
  "s_xor_b32 s69, s64, 65536\n" \
  "v_mul_f32 v73, v33, v33\n" \
  "v_mul_f32 v74, v34, v34\n" \
  "v_mul_f32 v75, v35, v35\n" \
  "v_mfma_f32_16x16x32_bf16 v[24:27], a[40:43], v[60:63], v[24:27]\n" \
  "s_cmp_ge_u32 s66, s59\n" \
  "ds_read_b128 v[56:59], v2 offset:12288\n" \
  "v_fma_f32 v72, v98, v72, v97\n" \
  "v_mfma_f32_16x16x32_bf16 v[28:31], a[44:47], v[60:63], v[28:31]\n" \
  "s_cselect_b32 s62, s69, s63\n" \
  "v_mul_f32 v72, v32, v72\n" \
  "v_exp_f32 v72, v72\n" \
  "v_mfma_f32_16x16x32_bf16 v[24:27], a[48:51], v[64:67], v[24:27]\n" \
  "s_cselect_b32 s86, s49, s50\n" \
  "ds_read_b128 v[60:63], v2 offset:13312\n" \
  "v_fma_f32 v72, v72, v99, v99\n" \
  "v_mfma_f32_16x16x32_bf16 v[28:31], a[52:55], v[64:67], v[28:31]\n" \
  "v_rcp_f32 v72, v72\n" \
  "v_fma_f32 v73, v98, v73, v97\n" \
  "v_mfma_f32_16x16x32_bf16 v[24:27], a[56:59], v[68:71], v[24:27]\n" \
  "ds_read_b128 v[64:67], v2 offset:14336\n" \
  "v_mul_f32 v73, v33, v73\n" \
  "v_mfma_f32_16x16x32_bf16 v[28:31], a[60:63], v[68:71], v[28:31]\n" \
  "s_waitcnt lgkmcnt(3)\n" \
  "v_exp_f32 v73, v73\n" \
  "v_fma_f32 v74, v98, v74, v97\n" \
  "v_mfma_f32_16x16x32_bf16 v[24:27], a[64:67], v[40:43], v[24:27]\n" \
  "s_waitcnt vmcnt(10)\n" \
  "ds_read_b128 v[68:71], v2 offset:15360\n" \
  "v_mul_f32 v74, v34, v74\n" \
  "v_mfma_f32_16x16x32_bf16 v[28:31], a[68:71], v[40:43], v[28:31]\n" \
  "s_barrier\n" \
  "v_mfma_f32_16x16x32_bf16 v[24:27], a[72:75], v[44:47], v[24:27]\n" \
  "ds_read_b128 v[40:43], v2 offset:16384\n" \
  "ds_read_b32 v13, v7 offset:256\n" \
  "v_mfma_f32_16x16x32_bf16 v[28:31], a[76:79], v[44:47], v[28:31]\n" \
  "buffer_store_dword v5, v4, s[36:39], s70 offen offset:0 sc0 sc1\n" \
  "v_exp_f32 v74, v74\n" \
  "v_fma_f32 v73, v73, v99, v99\n" \
  "v_mfma_f32_16x16x32_bf16 v[24:27], a[80:83], v[48:51], v[24:27]\n" \
  "ds_read_b128 v[44:47], v2 offset:17408\n" \
  "v_fma_f32 v74, v74, v99, v99\n" \
  "v_mfma_f32_16x16x32_bf16 v[28:31], a[84:87], v[48:51], v[28:31]\n" \
  "buffer_load_dword v6, s[36:39], s68 offen offset:0 sc1 lds\n" \
  "v_fma_f32 v75, v98, v75, v97\n" \
  "v_mfma_f32_16x16x32_bf16 v[24:27], a[88:91], v[52:55], v[24:27]\n" \
  "s_waitcnt lgkmcnt(1)\n" \
  "ds_read_b128 v[48:51], v2 offset:18432\n" \
  "ds_read_b32 v88, v8 offset:0\n" \
  "v_mul_f32 v75, v35, v75\n" \
  "v_mfma_f32_16x16x32_bf16 v[28:31], a[92:95], v[52:55], v[28:31]\n" \
  "v_cmp_eq_u32 vcc, s60, v13\n" \
  "v_exp_f32 v75, v75\n" \
  "v_mul_f32 v32, v32, v72\n" \
  "v_mfma_f32_16x16x32_bf16 v[24:27], a[96:99], v[56:59], v[24:27]\n" \
  "s_mov_b32 m0, s62\n" \
  "ds_read_b128 v[52:55], v2 offset:19456\n" \
  "v_rcp_f32 v73, v73\n" \
  "v_mfma_f32_16x16x32_bf16 v[28:31], a[100:103], v[56:59], v[28:31]\n" \
  "s_cselect_b32 s87, vcc_lo, vcc_hi\n" \
  "v_rcp_f32 v74, v74\n" \
  "v_mfma_f32_16x16x32_bf16 v[24:27], a[104:107], v[60:63], v[24:27]\n" \
  "s_cmp_eq_u32 s87, -1\n" \
  "ds_read_b128 v[56:59], v2 offset:20480\n" \
  "v_fma_f32 v75, v75, v99, v99\n" \
  "v_mul_f32 v33, v33, v73\n" \
  "v_mfma_f32_16x16x32_bf16 v[28:31], a[108:111], v[60:63], v[28:31]\n" \
  "s_cselect_b32 s67, s86, s59\n" \
  "v_rcp_f32 v75, v75\n" \
  "v_mfma_f32_16x16x32_bf16 v[24:27], a[112:115], v[64:67], v[24:27]\n" \
  "s_waitcnt lgkmcnt(2)\n" \
  "ds_read_b128 v[60:63], v2 offset:21504\n" \
  "buffer_load_dwordx4 v0, s[36:39], s67 offen offset:0 sc1 lds\n" \
  "v_mfma_f32_16x16x32_bf16 v[28:31], a[116:119], v[64:67], v[28:31]\n" \
  "s_xor_b32 s63, s63, 65536\n" \
  "v_add_f32_dpp v88, v88, v88 quad_perm:[1,0,3,2] row_mask:0xf bank_mask:0xf\n" \
  "v_mul_f32 v34, v34, v74\n" \
  "v_mfma_f32_16x16x32_bf16 v[24:27], a[120:123], v[68:71], v[24:27]\n" \
  "s_add_u32 s53, s52, s55\n" \
  "ds_read_b128 v[64:67], v2 offset:22528\n" \
  "v_add_f32_dpp v88, v88, v88 quad_perm:[2,3,0,1] row_mask:0xf bank_mask:0xf\n" \
  "v_mul_f32 v35, v35, v75\n" \
  "v_mfma_f32_16x16x32_bf16 v[28:31], a[124:127], v[68:71], v[28:31]\n" \
  "s_waitcnt lgkmcnt(3)\n" \
  "v_add_f32_dpp v88, v88, v88 row_half_mirror row_mask:0xf bank_mask:0xf\n" \
  "v_cvt_pk_bf16_f32 v80, v32, v33\n" \
  "v_mfma_f32_16x16x32_bf16 v[24:27], a[128:131], v[40:43], v[24:27]\n" \
  "s_cmp_eq_u32 s53, s56\n" \
  "ds_read_b128 v[68:71], v2 offset:23552\n" \
  "ds_write_b32 v10, v88 offset:0\n" \
  "v_mfma_f32_16x16x32_bf16 v[28:31], a[132:135], v[40:43], v[28:31]\n" \
  "buffer_load_dwordx4 v0, s[36:39], s67 offen offset:1024 sc1 lds\n" \
  "v_mfma_f32_16x16x32_bf16 v[24:27], a[136:139], v[44:47], v[24:27]\n" \
  "s_cselect_b32 s53, s57, s53\n" \
  "ds_read_b128 v[40:43], v2 offset:24576\n" \
  "v_cvt_pk_bf16_f32 v81, v34, v35\n" \
  "v_fma_f32 v36, -v96, v20, v36\n" \
  "v_mfma_f32_16x16x32_bf16 v[28:31], a[140:143], v[44:47], v[28:31]\n" \
  "s_cmp_eq_u32 s53, s58\n" \
  "v_fma_f32 v37, -v96, v21, v37\n" \
  "v_fma_f32 v38, -v96, v22, v38\n" \
  "v_mfma_f32_16x16x32_bf16 v[24:27], a[144:147], v[48:51], v[24:27]\n" \
  "s_cselect_b32 s53, s59, s53\n" \
  "ds_read_b128 v[44:47], v2 offset:25600\n" \
  "v_fma_f32 v39, -v96, v23, v39\n" \
  "v_mul_f32 v76, v36, v36\n" \
  "v_mfma_f32_16x16x32_bf16 v[28:31], a[148:151], v[48:51], v[28:31]\n" \
  "buffer_load_dwordx4 v0, s[36:39], s67 offen offset:2048 sc1 lds\n" \
  "v_mfma_f32_16x16x32_bf16 v[24:27], a[152:155], v[52:55], v[24:27]\n" \
  "ds_read_b128 v[48:51], v2 offset:26624\n" \
  "v_mul_f32 v77, v37, v37\n" \
  "v_mul_f32 v78, v38, v38\n" \
  "v_mfma_f32_16x16x32_bf16 v[28:31], a[156:159], v[52:55], v[28:31]\n" \
  "s_waitcnt lgkmcnt(4)\n" \
  "v_mul_f32 v79, v39, v39\n" \
  "v_fma_f32 v76, v98, v76, v97\n" \
  "v_mfma_f32_16x16x32_bf16 v[24:27], a[160:163], v[56:59], v[24:27]\n" \
  "ds_read_b128 v[52:55], v2 offset:27648\n" \
  "v_mul_f32 v76, v36, v76\n" \
  "v_fma_f32 v77, v98, v77, v97\n" \
  "v_mfma_f32_16x16x32_bf16 v[28:31], a[164:167], v[56:59], v[28:31]\n" \
  "buffer_load_dwordx4 v0, s[36:39], s67 offen offset:3072 sc1 lds\n" \
  "v_mfma_f32_16x16x32_bf16 v[24:27], a[168:171], v[60:63], v[24:27]\n" \
  "s_add_u32 m0, s62, 16384\n" \
  "ds_read_b128 v[56:59], v2 offset:28672\n" \
  "v_exp_f32 v76, v76\n" \
  "v_mfma_f32_16x16x32_bf16 v[28:31], a[172:175], v[60:63], v[28:31]\n" \
  "s_lshr_b32 s72, s67, 5\n" \
  "v_mul_f32 v77, v37, v77\n" \
  "v_fma_f32 v76, v76, v99, v99\n" \
  "v_mfma_f32_16x16x32_bf16 v[24:27], a[176:179], v[64:67], v[24:27]\n" \
  "s_lshr_b32 s71, s62, 2\n" \
  "ds_read_b128 v[60:63], v2 offset:29696\n" \
  "v_exp_f32 v77, v77\n" \
  "v_mfma_f32_16x16x32_bf16 v[28:31], a[180:183], v[64:67], v[28:31]\n" \
  "buffer_load_dwordx4 v1, s[36:39], s67 offen offset:0 sc1 lds\n" \
  "v_mfma_f32_16x16x32_bf16 v[24:27], a[184:187], v[68:71], v[24:27]\n" \
  "ds_read_b128 v[64:67], v2 offset:30720\n" \
  "v_rcp_f32 v76, v76\n" \
  "v_mfma_f32_16x16x32_bf16 v[28:31], a[188:191], v[68:71], v[28:31]\n" \
  "s_waitcnt lgkmcnt(3)\n" \
  "v_fma_f32 v77, v77, v99, v99\n" \
  "v_mfma_f32_16x16x32_bf16 v[24:27], a[192:195], v[40:43], v[24:27]\n" \
  "s_waitcnt vmcnt(13)\n" \
  "ds_read_b128 v[68:71], v2 offset:31744\n" \
  "v_rcp_f32 v77, v77\n" \
  "v_mfma_f32_16x16x32_bf16 v[28:31], a[196:199], v[40:43], v[28:31]\n" \
  "s_barrier\n" \
  "v_mfma_f32_16x16x32_bf16 v[24:27], a[200:203], v[44:47], v[24:27]\n" \
  "ds_read_b128 v[40:43], v2 offset:32768\n" \
  "buffer_load_dwordx4 v1, s[36:39], s67 offen offset:1024 sc1 lds\n" \
  "v_mfma_f32_16x16x32_bf16 v[28:31], a[204:207], v[44:47], v[28:31]\n" \
  "ds_read_b64 v[92:93], v11 offset:0\n" \
  "v_fma_f32 v78, v98, v78, v97\n" \
  "v_mfma_f32_16x16x32_bf16 v[24:27], a[208:211], v[48:51], v[24:27]\n" \
  "ds_read_b128 v[44:47], v2 offset:33792\n" \
  "v_mul_f32 v78, v38, v78\n" \
  "v_fma_f32 v79, v98, v79, v97\n" \
  "v_mfma_f32_16x16x32_bf16 v[28:31], a[212:215], v[48:51], v[28:31]\n" \
  "v_exp_f32 v78, v78\n" \
  "v_mfma_f32_16x16x32_bf16 v[24:27], a[216:219], v[52:55], v[24:27]\n" \
  "ds_read_b128 v[48:51], v2 offset:34816\n" \
  "buffer_load_dwordx4 v1, s[36:39], s67 offen offset:2048 sc1 lds\n" \
  "v_mfma_f32_16x16x32_bf16 v[28:31], a[220:223], v[52:55], v[28:31]\n" \
  "s_waitcnt lgkmcnt(4)\n" \
  "v_mul_f32 v79, v39, v79\n" \
  "v_fma_f32 v78, v78, v99, v99\n" \
  "v_mfma_f32_16x16x32_bf16 v[24:27], a[224:227], v[56:59], v[24:27]\n" \
  "s_waitcnt lgkmcnt(2)\n" \
  "ds_read_b128 v[52:55], v2 offset:35840\n" \
  "v_exp_f32 v79, v79\n" \
  "v_mfma_f32_16x16x32_bf16 v[28:31], a[228:231], v[56:59], v[28:31]\n" \
  "v_rcp_f32 v78, v78\n" \
  "v_mfma_f32_16x16x32_bf16 v[24:27], a[232:235], v[60:63], v[24:27]\n" \
  "ds_read_b128 v[56:59], v2 offset:36864\n" \
  "buffer_load_dwordx4 v1, s[36:39], s67 offen offset:3072 sc1 lds\n" \
  "v_mfma_f32_16x16x32_bf16 v[28:31], a[236:239], v[60:63], v[28:31]\n" \
  "s_add_u32 m0, s71, s88\n" \
  "v_fma_f32 v79, v79, v99, v99\n" \
  "v_rcp_f32 v79, v79\n" \
  "v_mfma_f32_16x16x32_bf16 v[24:27], a[240:243], v[64:67], v[24:27]\n" \
  "s_cmp_ge_u32 s66, s59\n" \
  "ds_read_b128 v[60:63], v2 offset:37888\n" \
  "v_mul_f32 v36, v36, v76\n" \
  "v_mul_f32 v37, v37, v77\n" \
  "v_mul_f32 v38, v38, v78\n" \
  "v_mul_f32 v39, v39, v79\n" \
  "v_cvt_pk_bf16_f32 v82, v36, v37\n" \
  "v_cvt_pk_bf16_f32 v83, v38, v39\n" \
  "v_mfma_f32_16x16x32_bf16 v[28:31], a[244:247], v[64:67], v[28:31]\n" \
  "buffer_load_dword v9, s[36:39], s72 offen offset:0 sc1 lds\n" \
  "buffer_store_dwordx4 v[80:83], v3, s[36:39], s73 offen offset:0 sc0 sc1\n" \
  "v_mfma_f32_16x16x32_bf16 v[24:27], a[248:251], v[68:71], v[24:27]\n" \
  "s_waitcnt lgkmcnt(2)\n" \
  "ds_read_b128 v[64:67], v2 offset:38912\n" \
  "v_mfma_f32_16x16x32_bf16 v[28:31], a[252:255], v[68:71], v[28:31]\n" \
  "s_cbranch_scc1 Lg3o_slow1_%=\n" \
  "Lg3o_d1_%=:\n" \
  "v_mfma_f32_16x16x32_bf16 v[32:35], a[0:3], v[40:43], 0\n" \
  "s_mul_i32 s73, s48, 7\n" \
  "ds_read_b128 v[68:71], v2 offset:39936\n" \
  "v_mul_f32 v96, s75, v92\n" \
  "v_mfma_f32_16x16x32_bf16 v[36:39], a[4:7], v[40:43], 0\n" \
  "s_lshr_b32 s73, s73, 1\n" \
  "v_fma_f32 v93, v93, s75, v12\n" \
  "v_fma_f32 v93, -v96, v96, v93\n" \
  "v_mfma_f32_16x16x32_bf16 v[32:35], a[8:11], v[44:47], v[32:35]\n" \
  "s_cmp_eq_u32 s54, 1\n" \
  "ds_read_b128 v[40:43], v2 offset:40960\n" \
  "v_max_f32 v93, v93, v12\n" \
  "s_cselect_b32 s68, s52, s51\n" \
  "v_mfma_f32_16x16x32_bf16 v[36:39], a[12:15], v[44:47], v[36:39]\n" \
  "s_lshr_b32 s68, s68, 5\n" \
  "v_rsq_f32 v92, v93\n" \
  "v_mfma_f32_16x16x32_bf16 v[32:35], a[16:19], v[48:51], v[32:35]\n" \
  "s_mul_i32 s70, s46, 5\n" \
  "ds_read_b128 v[44:47], v2 offset:41984\n" \
  "v_mul_f32 v97, s76, v92\n" \
  "v_mul_f32 v98, v92, v92\n" \
  "v_mfma_f32_16x16x32_bf16 v[36:39], a[20:23], v[48:51], v[36:39]\n" \
  "s_lshr_b32 s70, s70, 6\n" \
  "v_mul_f32 v99, v93, v92\n" \
  "v_mul_f32 v98, s77, v98\n" \
  "v_mfma_f32_16x16x32_bf16 v[32:35], a[24:27], v[52:55], v[32:35]\n" \
  "s_add_u32 m0, s78, 256\n" \
  "ds_read_b128 v[48:51], v2 offset:43008\n" \
  "v_mul_f32 v98, v98, v97\n" \
  "v_fma_f32 v24, -v96, v16, v24\n" \
  "v_mfma_f32_16x16x32_bf16 v[36:39], a[28:31], v[52:55], v[36:39]\n" \
  "s_waitcnt lgkmcnt(3)\n" \
  "v_fma_f32 v25, -v96, v17, v25\n" \
  "v_fma_f32 v26, -v96, v18, v26\n" \
  "v_mfma_f32_16x16x32_bf16 v[32:35], a[32:35], v[56:59], v[32:35]\n" \
  "s_xor_b32 s69, s63, 65536\n" \
  "ds_read_b128 v[52:55], v2 offset:44032\n" \
  "v_fma_f32 v27, -v96, v19, v27\n" \
  "v_mul_f32 v72, v24, v24\n" \
  "v_mfma_f32_16x16x32_bf16 v[36:39], a[36:39], v[56:59], v[36:39]\n" \
  "s_cmp_ge_u32 s67, s59\n" \
  "v_mul_f32 v73, v25, v25\n" \
  "v_mul_f32 v74, v26, v26\n" \
  "v_mfma_f32_16x16x32_bf16 v[32:35], a[40:43], v[60:63], v[32:35]\n" \
  "s_cselect_b32 s62, s69, s64\n" \
  "ds_read_b128 v[56:59], v2 offset:45056\n" \
  "v_mul_f32 v75, v27, v27\n" \
  "v_fma_f32 v72, v98, v72, v97\n" \
  "v_mfma_f32_16x16x32_bf16 v[36:39], a[44:47], v[60:63], v[36:39]\n" \
  "s_cselect_b32 s86, s50, s51\n" \
  "v_mul_f32 v72, v24, v72\n" \
  "v_fma_f32 v73, v98, v73, v97\n" \
  "v_mfma_f32_16x16x32_bf16 v[32:35], a[48:51], v[64:67], v[32:35]\n" \
  "s_mov_b64 s[44:45], s[46:47]\n" \
  "ds_read_b128 v[60:63], v2 offset:46080\n" \
  "v_exp_f32 v72, v72\n" \
  "v_mfma_f32_16x16x32_bf16 v[36:39], a[52:55], v[64:67], v[36:39]\n" \
  "s_mov_b64 s[46:47], s[48:49]\n" \
  "v_mul_f32 v73, v25, v73\n" \
  "v_fma_f32 v72, v72, v99, v99\n" \
  "v_mfma_f32_16x16x32_bf16 v[32:35], a[56:59], v[68:71], v[32:35]\n" \
  "s_mov_b64 s[48:49], s[50:51]\n" \
  "ds_read_b128 v[64:67], v2 offset:47104\n" \
  "v_exp_f32 v73, v73\n" \
  "v_mfma_f32_16x16x32_bf16 v[36:39], a[60:63], v[68:71], v[36:39]\n" \
  "s_waitcnt lgkmcnt(3)\n" \
  "v_rcp_f32 v72, v72\n" \
  "v_mfma_f32_16x16x32_bf16 v[32:35], a[64:67], v[40:43], v[32:35]\n" \
  "s_waitcnt vmcnt(10)\n" \
  "ds_read_b128 v[68:71], v2 offset:48128\n" \
  "v_fma_f32 v73, v73, v99, v99\n" \
  "v_fma_f32 v74, v98, v74, v97\n" \
  "v_mfma_f32_16x16x32_bf16 v[36:39], a[68:71], v[40:43], v[36:39]\n" \
  "s_barrier\n" \
  "v_mfma_f32_16x16x32_bf16 v[32:35], a[72:75], v[44:47], v[32:35]\n" \
  "s_mov_b64 s[50:51], s[52:53]\n" \
  "ds_read_b128 v[40:43], v2 offset:49152\n" \
  "ds_read_b32 v13, v7 offset:0\n" \
  "v_mul_f32 v74, v26, v74\n" \
  "v_mfma_f32_16x16x32_bf16 v[36:39], a[76:79], v[44:47], v[36:39]\n" \
  "buffer_store_dword v5, v4, s[36:39], s70 offen offset:0 sc0 sc1\n" \
  "v_exp_f32 v74, v74\n" \
  "v_fma_f32 v75, v98, v75, v97\n" \
  "v_mfma_f32_16x16x32_bf16 v[32:35], a[80:83], v[48:51], v[32:35]\n" \
  "ds_read_b128 v[44:47], v2 offset:50176\n" \
  "v_rcp_f32 v73, v73\n" \
  "v_mfma_f32_16x16x32_bf16 v[36:39], a[84:87], v[48:51], v[36:39]\n" \
  "buffer_load_dword v6, s[36:39], s68 offen offset:0 sc1 lds\n" \
  "v_mfma_f32_16x16x32_bf16 v[32:35], a[88:91], v[52:55], v[32:35]\n" \
  "s_waitcnt lgkmcnt(1)\n" \
  "ds_read_b128 v[48:51], v2 offset:51200\n" \
  "ds_read_b32 v88, v8 offset:8192\n" \
  "v_mul_f32 v75, v27, v75\n" \
  "v_mfma_f32_16x16x32_bf16 v[36:39], a[92:95], v[52:55], v[36:39]\n" \
  "v_cmp_eq_u32 vcc, s60, v13\n" \
  "v_exp_f32 v75, v75\n" \
  "v_fma_f32 v74, v74, v99, v99\n" \
  "v_mfma_f32_16x16x32_bf16 v[32:35], a[96:99], v[56:59], v[32:35]\n" \
  "s_mov_b32 m0, s62\n" \
  "ds_read_b128 v[52:55], v2 offset:52224\n" \
  "v_rcp_f32 v74, v74\n" \
  "v_mfma_f32_16x16x32_bf16 v[36:39], a[100:103], v[56:59], v[36:39]\n" \
  "s_cselect_b32 s87, vcc_lo, vcc_hi\n" \
  "v_fma_f32 v75, v75, v99, v99\n" \
  "v_mul_f32 v24, v24, v72\n" \
  "v_mfma_f32_16x16x32_bf16 v[32:35], a[104:107], v[60:63], v[32:35]\n" \
  "s_cmp_eq_u32 s87, -1\n" \
  "ds_read_b128 v[56:59], v2 offset:53248\n" \
  "v_rcp_f32 v75, v75\n" \
  "v_mfma_f32_16x16x32_bf16 v[36:39], a[108:111], v[60:63], v[36:39]\n" \
  "s_cselect_b32 s66, s86, s59\n" \
  "v_mul_f32 v25, v25, v73\n" \
  "v_mul_f32 v26, v26, v74\n" \
  "v_mfma_f32_16x16x32_bf16 v[32:35], a[112:115], v[64:67], v[32:35]\n" \
  "s_waitcnt lgkmcnt(2)\n" \
  "ds_read_b128 v[60:63], v2 offset:54272\n" \
  "buffer_load_dwordx4 v0, s[36:39], s66 offen offset:0 sc1 lds\n" \
  "v_mfma_f32_16x16x32_bf16 v[36:39], a[116:119], v[64:67], v[36:39]\n" \
  "s_xor_b32 s64, s64, 65536\n" \
  "v_add_f32_dpp v88, v88, v88 quad_perm:[1,0,3,2] row_mask:0xf bank_mask:0xf\n" \
  "v_mul_f32 v27, v27, v75\n" \
  "v_mfma_f32_16x16x32_bf16 v[32:35], a[120:123], v[68:71], v[32:35]\n" \
  "s_add_u32 s74, s53, s55\n" \
  "ds_read_b128 v[64:67], v2 offset:55296\n" \
  "v_add_f32_dpp v88, v88, v88 quad_perm:[2,3,0,1] row_mask:0xf bank_mask:0xf\n" \
  "v_cvt_pk_bf16_f32 v80, v24, v25\n" \
  "v_mfma_f32_16x16x32_bf16 v[36:39], a[124:127], v[68:71], v[36:39]\n" \
  "s_waitcnt lgkmcnt(3)\n" \
  "v_add_f32_dpp v88, v88, v88 row_half_mirror row_mask:0xf bank_mask:0xf\n" \
  "v_cvt_pk_bf16_f32 v81, v26, v27\n" \
  "v_mfma_f32_16x16x32_bf16 v[32:35], a[128:131], v[40:43], v[32:35]\n" \
  "s_cmp_eq_u32 s74, s56\n" \
  "ds_read_b128 v[68:71], v2 offset:56320\n" \
  "ds_write_b32 v10, v88 offset:128\n" \
  "v_mfma_f32_16x16x32_bf16 v[36:39], a[132:135], v[40:43], v[36:39]\n" \
  "buffer_load_dwordx4 v0, s[36:39], s66 offen offset:1024 sc1 lds\n" \
  "v_mfma_f32_16x16x32_bf16 v[32:35], a[136:139], v[44:47], v[32:35]\n" \
  "s_cselect_b32 s74, s57, s74\n" \
  "ds_read_b128 v[40:43], v2 offset:57344\n" \
  "v_fma_f32 v28, -v96, v20, v28\n" \
  "v_fma_f32 v29, -v96, v21, v29\n" \
  "v_mfma_f32_16x16x32_bf16 v[36:39], a[140:143], v[44:47], v[36:39]\n" \
  "s_cmp_eq_u32 s74, s58\n" \
  "v_fma_f32 v30, -v96, v22, v30\n" \
  "v_fma_f32 v31, -v96, v23, v31\n" \
  "v_mfma_f32_16x16x32_bf16 v[32:35], a[144:147], v[48:51], v[32:35]\n" \
  "s_cselect_b32 s74, s59, s74\n" \
  "ds_read_b128 v[44:47], v2 offset:58368\n" \
  "v_mul_f32 v76, v28, v28\n" \
  "v_mul_f32 v77, v29, v29\n" \
  "v_mfma_f32_16x16x32_bf16 v[36:39], a[148:151], v[48:51], v[36:39]\n" \
  "buffer_load_dwordx4 v0, s[36:39], s66 offen offset:2048 sc1 lds\n" \
  "v_mfma_f32_16x16x32_bf16 v[32:35], a[152:155], v[52:55], v[32:35]\n" \
  "s_mov_b32 s52, s74\n" \
  "ds_read_b128 v[48:51], v2 offset:59392\n" \
  "v_mul_f32 v78, v30, v30\n" \
  "v_mul_f32 v79, v31, v31\n" \
  "v_mfma_f32_16x16x32_bf16 v[36:39], a[156:159], v[52:55], v[36:39]\n" \
  "s_waitcnt lgkmcnt(4)\n" \
  "v_fma_f32 v76, v98, v76, v97\n" \
  "v_mul_f32 v76, v28, v76\n" \
  "v_mfma_f32_16x16x32_bf16 v[32:35], a[160:163], v[56:59], v[32:35]\n" \
  "ds_read_b128 v[52:55], v2 offset:60416\n" \
  "v_exp_f32 v76, v76\n" \
  "v_mfma_f32_16x16x32_bf16 v[36:39], a[164:167], v[56:59], v[36:39]\n" \
  "buffer_load_dwordx4 v0, s[36:39], s66 offen offset:3072 sc1 lds\n" \
  "v_mfma_f32_16x16x32_bf16 v[32:35], a[168:171], v[60:63], v[32:35]\n" \
  "s_add_u32 m0, s62, 16384\n" \
  "ds_read_b128 v[56:59], v2 offset:61440\n" \
  "v_fma_f32 v76, v76, v99, v99\n" \
  "v_fma_f32 v77, v98, v77, v97\n" \
  "v_mfma_f32_16x16x32_bf16 v[36:39], a[172:175], v[60:63], v[36:39]\n" \
  "s_lshr_b32 s72, s66, 5\n" \
  "v_rcp_f32 v76, v76\n" \
  "v_mfma_f32_16x16x32_bf16 v[32:35], a[176:179], v[64:67], v[32:35]\n" \
  "s_lshr_b32 s71, s62, 2\n" \
  "ds_read_b128 v[60:63], v2 offset:62464\n" \
  "v_mul_f32 v77, v29, v77\n" \
  "v_fma_f32 v78, v98, v78, v97\n" \
  "v_mfma_f32_16x16x32_bf16 v[36:39], a[180:183], v[64:67], v[36:39]\n" \
  "buffer_load_dwordx4 v1, s[36:39], s66 offen offset:0 sc1 lds\n" \
  "v_mfma_f32_16x16x32_bf16 v[32:35], a[184:187], v[68:71], v[32:35]\n" \
  "ds_read_b128 v[64:67], v2 offset:63488\n" \
  "v_exp_f32 v77, v77\n" \
  "v_mfma_f32_16x16x32_bf16 v[36:39], a[188:191], v[68:71], v[36:39]\n" \
  "s_waitcnt lgkmcnt(3)\n" \
  "v_mul_f32 v78, v30, v78\n" \
  "v_mfma_f32_16x16x32_bf16 v[32:35], a[192:195], v[40:43], v[32:35]\n" \
  "s_waitcnt vmcnt(13)\n" \
  "ds_read_b128 v[68:71], v2 offset:64512\n" \
  "v_xor_b32 v2, 65536, v2\n" \
  "v_fma_f32 v77, v77, v99, v99\n" \
  "v_mfma_f32_16x16x32_bf16 v[36:39], a[196:199], v[40:43], v[36:39]\n" \
  "s_barrier\n" \
  "v_mfma_f32_16x16x32_bf16 v[32:35], a[200:203], v[44:47], v[32:35]\n" \
  "ds_read_b128 v[40:43], v2 offset:0\n" \
  "buffer_load_dwordx4 v1, s[36:39], s66 offen offset:1024 sc1 lds\n" \
  "v_mfma_f32_16x16x32_bf16 v[36:39], a[204:207], v[44:47], v[36:39]\n" \
  "ds_read_b64 v[92:93], v11 offset:128\n" \
  "v_fma_f32 v79, v98, v79, v97\n" \
  "v_mfma_f32_16x16x32_bf16 v[32:35], a[208:211], v[48:51], v[32:35]\n" \
  "ds_read_b128 v[44:47], v2 offset:1024\n" \
  "v_exp_f32 v78, v78\n" \
  "v_mfma_f32_16x16x32_bf16 v[36:39], a[212:215], v[48:51], v[36:39]\n" \
  "v_rcp_f32 v77, v77\n" \
  "v_mfma_f32_16x16x32_bf16 v[32:35], a[216:219], v[52:55], v[32:35]\n" \
  "ds_read_b128 v[48:51], v2 offset:2048\n" \
  "buffer_load_dwordx4 v1, s[36:39], s66 offen offset:2048 sc1 lds\n" \
  "v_mfma_f32_16x16x32_bf16 v[36:39], a[220:223], v[52:55], v[36:39]\n" \
  "s_waitcnt lgkmcnt(4)\n" \
  "v_mul_f32 v79, v31, v79\n" \
  "v_fma_f32 v78, v78, v99, v99\n" \
  "v_mfma_f32_16x16x32_bf16 v[32:35], a[224:227], v[56:59], v[32:35]\n" \
  "s_waitcnt lgkmcnt(2)\n" \
  "ds_read_b128 v[52:55], v2 offset:3072\n" \
  "v_exp_f32 v79, v79\n" \
  "v_mfma_f32_16x16x32_bf16 v[36:39], a[228:231], v[56:59], v[36:39]\n" \
  "v_rcp_f32 v78, v78\n" \
  "v_mfma_f32_16x16x32_bf16 v[32:35], a[232:235], v[60:63], v[32:35]\n" \
  "ds_read_b128 v[56:59], v2 offset:4096\n" \
  "buffer_load_dwordx4 v1, s[36:39], s66 offen offset:3072 sc1 lds\n" \
  "v_mfma_f32_16x16x32_bf16 v[36:39], a[236:239], v[60:63], v[36:39]\n" \
  "s_add_u32 m0, s71, s88\n" \
  "v_fma_f32 v79, v79, v99, v99\n" \
  "v_rcp_f32 v79, v79\n" \
  "v_mfma_f32_16x16x32_bf16 v[32:35], a[240:243], v[64:67], v[32:35]\n" \
  "s_cmp_lt_u32 s67, s59\n" \
  "ds_read_b128 v[60:63], v2 offset:5120\n" \
  "v_mul_f32 v28, v28, v76\n" \
  "v_mul_f32 v29, v29, v77\n" \
  "v_mul_f32 v30, v30, v78\n" \
  "v_mul_f32 v31, v31, v79\n" \
  "v_cvt_pk_bf16_f32 v82, v28, v29\n" \
  "v_cvt_pk_bf16_f32 v83, v30, v31\n" \
  "v_mfma_f32_16x16x32_bf16 v[36:39], a[244:247], v[64:67], v[36:39]\n" \
  "buffer_load_dword v9, s[36:39], s72 offen offset:0 sc1 lds\n" \
  "buffer_store_dwordx4 v[80:83], v3, s[36:39], s73 offen offset:0 sc0 sc1\n" \
  "v_xor_b32 v8, 16384, v8\n" \
  "v_mfma_f32_16x16x32_bf16 v[32:35], a[248:251], v[68:71], v[32:35]\n" \
  "s_waitcnt lgkmcnt(2)\n" \
  "ds_read_b128 v[64:67], v2 offset:6144\n" \
  "v_mfma_f32_16x16x32_bf16 v[36:39], a[252:255], v[68:71], v[36:39]\n" \
  "s_cbranch_scc1 Lg3o_d0_%=\n" \
  "Lg3o_slow0_%=:\n" \
  "s_cmp_eq_u32 s54, 1\n" \
  "s_cselect_b32 s68, s49, s48\n" \
  "s_lshr_b32 s68, s68, 5\n" \
  "s_add_u32 m0, s78, 8192\n" \
  "s_nop 0\n" \
  "buffer_load_dword v6, s[36:39], s68 offen offset:0 sc1 lds\n" \
  "s_mul_i32 s73, s47, 7\n" \
  "s_lshr_b32 s73, s73, 1\n" \
  "v_mul_f32 v96, s75, v92\n" \
  "v_fma_f32 v93, v93, s75, v12\n" \
  "v_fma_f32 v93, -v96, v96, v93\n" \
  "v_max_f32 v93, v93, v12\n" \
  "v_rsq_f32 v92, v93\n" \
  "s_nop 0\n" \
  "v_mul_f32 v97, s76, v92\n" \
  "v_mul_f32 v98, v92, v92\n" \
  "v_mul_f32 v99, v93, v92\n" \
  "v_mul_f32 v98, s77, v98\n" \
  "v_mul_f32 v98, v98, v97\n" \
  "v_fma_f32 v32, -v96, v16, v32\n" \
  "v_fma_f32 v33, -v96, v17, v33\n" \
  "v_fma_f32 v34, -v96, v18, v34\n" \
  "v_fma_f32 v35, -v96, v19, v35\n" \
  "v_mul_f32 v72, v32, v32\n" \
  "v_mul_f32 v73, v33, v33\n" \
  "v_mul_f32 v74, v34, v34\n" \
  "v_mul_f32 v75, v35, v35\n" \
  "v_fma_f32 v72, v98, v72, v97\n" \
  "v_fma_f32 v73, v98, v73, v97\n" \
  "v_fma_f32 v74, v98, v74, v97\n" \
  "v_fma_f32 v75, v98, v75, v97\n" \
  "v_mul_f32 v72, v32, v72\n" \
  "v_mul_f32 v73, v33, v73\n" \
  "v_mul_f32 v74, v34, v74\n" \
  "v_mul_f32 v75, v35, v75\n" \
  "v_exp_f32 v72, v72\n" \
  "v_exp_f32 v73, v73\n" \
  "v_exp_f32 v74, v74\n" \
  "v_exp_f32 v75, v75\n" \
  "v_fma_f32 v72, v72, v99, v99\n" \
  "v_fma_f32 v73, v73, v99, v99\n" \
  "v_fma_f32 v74, v74, v99, v99\n" \
  "v_fma_f32 v75, v75, v99, v99\n" \
  "v_rcp_f32 v72, v72\n" \
  "v_rcp_f32 v73, v73\n" \
  "v_rcp_f32 v74, v74\n" \
  "v_rcp_f32 v75, v75\n" \
  "v_mul_f32 v32, v32, v72\n" \
  "s_add_u32 m0, s78, 8448\n" \
  "s_nop 0\n" \
  "buffer_load_dword v6, s[36:39], s68 offen offset:0 sc1 lds\n" \
  "v_mul_f32 v33, v33, v73\n" \
  "v_mul_f32 v34, v34, v74\n" \
  "v_mul_f32 v35, v35, v75\n" \
  "v_cvt_pk_bf16_f32 v80, v32, v33\n" \
  "v_cvt_pk_bf16_f32 v81, v34, v35\n" \
  "v_fma_f32 v36, -v96, v20, v36\n" \
  "v_fma_f32 v37, -v96, v21, v37\n" \
  "v_fma_f32 v38, -v96, v22, v38\n" \
  "v_fma_f32 v39, -v96, v23, v39\n" \
  "v_mul_f32 v76, v36, v36\n" \
  "v_mul_f32 v77, v37, v37\n" \
  "v_mul_f32 v78, v38, v38\n" \
  "v_mul_f32 v79, v39, v39\n" \
  "v_fma_f32 v76, v98, v76, v97\n" \
  "v_fma_f32 v77, v98, v77, v97\n" \
  "v_fma_f32 v78, v98, v78, v97\n" \
  "v_fma_f32 v79, v98, v79, v97\n" \
  "v_mul_f32 v76, v36, v76\n" \
  "v_mul_f32 v77, v37, v77\n" \
  "v_mul_f32 v78, v38, v78\n" \
  "v_mul_f32 v79, v39, v79\n" \
  "v_exp_f32 v76, v76\n" \
  "v_exp_f32 v77, v77\n" \
  "v_exp_f32 v78, v78\n" \
  "v_exp_f32 v79, v79\n" \
  "v_fma_f32 v76, v76, v99, v99\n" \
  "v_fma_f32 v77, v77, v99, v99\n" \
  "v_fma_f32 v78, v78, v99, v99\n" \
  "v_fma_f32 v79, v79, v99, v99\n" \
  "v_rcp_f32 v76, v76\n" \
  "v_rcp_f32 v77, v77\n" \
  "v_rcp_f32 v78, v78\n" \
  "v_rcp_f32 v79, v79\n" \
  "v_mul_f32 v36, v36, v76\n" \
  "v_mul_f32 v37, v37, v77\n" \
  "v_mul_f32 v38, v38, v78\n" \
  "v_mul_f32 v39, v39, v79\n" \
  "v_cvt_pk_bf16_f32 v82, v36, v37\n" \
  "v_cvt_pk_bf16_f32 v83, v38, v39\n" \
  "buffer_store_dwordx4 v[80:83], v3, s[36:39], s73 offen offset:0 sc0 sc1\n" \
  "s_cmp_ge_u32 s48, s59\n" \
  "s_cbranch_scc1 Lg3o_done0_%=\n" \
  "s_add_u32 m0, s78, 16384\n" \
  "s_nop 0\n" \
  "buffer_load_dword v6, s[36:39], s68 offen offset:0 sc1 lds\n" \
  "s_waitcnt vmcnt(3)\n" \
  "s_barrier\n" \
  "ds_read_b32 v13, v7 offset:8192\n" \
  "s_mul_i32 s70, s45, 5\n" \
  "s_lshr_b32 s70, s70, 6\n" \
  "buffer_store_dword v5, v4, s[36:39], s70 offen offset:0 sc0 sc1\n" \
  "s_mov_b32 s45, s59\n" \
  "s_mul_i32 s70, s46, 5\n" \
  "s_lshr_b32 s70, s70, 6\n" \
  "buffer_store_dword v5, v4, s[36:39], s70 offen offset:0 sc0 sc1\n" \
  "s_mov_b32 s46, s59\n" \
  "s_mov_b32 s69, 18432\n" \
  "s_waitcnt lgkmcnt(0)\n" \
  "v_cmp_eq_u32 vcc, s60, v13\n" \
  "s_nop 0\n" \
  "s_cmp_eq_u32 vcc_lo, -1\n" \
  "s_cbranch_scc1 Lg3o_go0_%=\n" \
  "s_add_u32 m0, s78, 16640\n" \
  "s_nop 0\n" \
  "buffer_load_dword v6, s[36:39], s68 offen offset:0 sc1 lds\n" \
  "s_waitcnt vmcnt(5)\n" \
  "s_barrier\n" \
  "ds_read_b32 v13, v7 offset:8448\n" \
  "s_mov_b32 s69, 18688\n" \
  "s_waitcnt lgkmcnt(0)\n" \
  "v_cmp_eq_u32 vcc, s60, v13\n" \
  "s_nop 0\n" \
  "s_cmp_eq_u32 vcc_lo, -1\n" \
  "s_cbranch_scc1 Lg3o_go0_%=\n" \
  "s_add_u32 m0, s78, 8192\n" \
  "s_nop 0\n" \
  "buffer_load_dword v6, s[36:39], s68 offen offset:0 sc1 lds\n" \
  "s_waitcnt vmcnt(4)\n" \
  "s_barrier\n" \
  "ds_read_b32 v13, v7 offset:16384\n" \
  "s_mul_i32 s70, s47, 5\n" \
  "s_lshr_b32 s70, s70, 6\n" \
  "buffer_store_dword v5, v4, s[36:39], s70 offen offset:0 sc0 sc1\n" \
  "s_mov_b32 s47, s59\n" \
  "s_mov_b32 s69, 10240\n" \
  "s_waitcnt lgkmcnt(0)\n" \
  "v_cmp_eq_u32 vcc, s60, v13\n" \
  "s_nop 0\n" \
  "s_cmp_eq_u32 vcc_lo, -1\n" \
  "s_cbranch_scc1 Lg3o_go0_%=\n" \
  "s_add_u32 m0, s78, 8448\n" \
  "s_nop 0\n" \
  "buffer_load_dword v6, s[36:39], s68 offen offset:0 sc1 lds\n" \
  "s_waitcnt vmcnt(3)\n" \
  "s_barrier\n" \
  "ds_read_b32 v13, v7 offset:16640\n" \
  "s_mov_b32 s69, 10496\n" \
  "s_waitcnt lgkmcnt(0)\n" \
  "v_cmp_eq_u32 vcc, s60, v13\n" \
  "s_nop 0\n" \
  "s_cmp_eq_u32 vcc_lo, -1\n" \
  "s_cbranch_scc1 Lg3o_go0_%=\n" \
  "Lg3o_poll0_%=:\n" \
  "s_add_u32 m0, s78, 16384\n" \
  "s_nop 0\n" \
  "buffer_load_dword v6, s[36:39], s68 offen offset:0 sc1 lds\n" \
  "s_waitcnt vmcnt(2)\n" \
  "s_barrier\n" \
  "ds_read_b32 v13, v7 offset:8192\n" \
  "s_mov_b32 s69, 18432\n" \
  "s_waitcnt lgkmcnt(0)\n" \
  "v_cmp_eq_u32 vcc, s60, v13\n" \
  "s_nop 0\n" \
  "s_cmp_eq_u32 vcc_lo, -1\n" \
  "s_cbranch_scc1 Lg3o_go0_%=\n" \
  "s_add_u32 m0, s78, 16640\n" \
  "s_nop 0\n" \
  "buffer_load_dword v6, s[36:39], s68 offen offset:0 sc1 lds\n" \
  "s_waitcnt vmcnt(2)\n" \
  "s_barrier\n" \
  "ds_read_b32 v13, v7 offset:8448\n" \
  "s_mov_b32 s69, 18688\n" \
  "s_waitcnt lgkmcnt(0)\n" \
  "v_cmp_eq_u32 vcc, s60, v13\n" \
  "s_nop 0\n" \
  "s_cmp_eq_u32 vcc_lo, -1\n" \
  "s_cbranch_scc1 Lg3o_go0_%=\n" \
  "s_add_u32 m0, s78, 8192\n" \
  "s_nop 0\n" \
  "buffer_load_dword v6, s[36:39], s68 offen offset:0 sc1 lds\n" \
  "s_waitcnt vmcnt(2)\n" \
  "s_barrier\n" \
  "ds_read_b32 v13, v7 offset:16384\n" \
  "s_mov_b32 s69, 10240\n" \
  "s_waitcnt lgkmcnt(0)\n" \
  "v_cmp_eq_u32 vcc, s60, v13\n" \
  "s_nop 0\n" \
  "s_cmp_eq_u32 vcc_lo, -1\n" \
  "s_cbranch_scc1 Lg3o_go0_%=\n" \
  "s_add_u32 m0, s78, 8448\n" \
  "s_nop 0\n" \
  "buffer_load_dword v6, s[36:39], s68 offen offset:0 sc1 lds\n" \
  "s_waitcnt vmcnt(2)\n" \
  "s_barrier\n" \
  "ds_read_b32 v13, v7 offset:16640\n" \
  "s_mov_b32 s69, 10496\n" \
  "s_waitcnt lgkmcnt(0)\n" \
  "v_cmp_eq_u32 vcc, s60, v13\n" \
  "s_nop 0\n" \
  "s_cmp_eq_u32 vcc_lo, -1\n" \
  "s_cbranch_scc1 Lg3o_go0_%=\n" \
  "s_add_u32 s79, s79, 1\n" \
  "s_cmp_lt_u32 s79, 20000\n" \
  "s_cbranch_scc1 Lg3o_poll0_%=\n" \
  "Lg3o_go0_%=:\n" \
  "s_xor_b32 s62, s63, 65536\n" \
  "s_cmp_lt_u32 s66, s59\n" \
  "s_cbranch_scc1 Lg3o_gs0_%=\n" \
  "s_mov_b32 m0, s62\n" \
  "s_nop 0\n" \
  "buffer_load_dwordx4 v0, s[36:39], s48 offen offset:0 sc1 lds\n" \
  "buffer_load_dwordx4 v0, s[36:39], s48 offen offset:1024 sc1 lds\n" \
  "buffer_load_dwordx4 v0, s[36:39], s48 offen offset:2048 sc1 lds\n" \
  "buffer_load_dwordx4 v0, s[36:39], s48 offen offset:3072 sc1 lds\n" \
  "s_add_u32 m0, s62, 16384\n" \
  "s_lshr_b32 s72, s48, 5\n" \
  "s_lshr_b32 s71, s62, 2\n" \
  "buffer_load_dwordx4 v1, s[36:39], s48 offen offset:0 sc1 lds\n" \
  "buffer_load_dwordx4 v1, s[36:39], s48 offen offset:1024 sc1 lds\n" \
  "buffer_load_dwordx4 v1, s[36:39], s48 offen offset:2048 sc1 lds\n" \
  "buffer_load_dwordx4 v1, s[36:39], s48 offen offset:3072 sc1 lds\n" \
  "s_add_u32 m0, s71, s88\n" \
  "s_nop 0\n" \
  "buffer_load_dword v9, s[36:39], s72 offen offset:0 sc1 lds\n" \
  "Lg3o_gs0_%=:\n" \
  "s_cmp_eq_u32 s54, 1\n" \
  "s_cselect_b32 s68, s50, s49\n" \
  "s_lshr_b32 s68, s68, 5\n" \
  "s_add_u32 m0, s78, 256\n" \
  "s_nop 0\n" \
  "buffer_load_dword v6, s[36:39], s68 offen offset:0 sc1 lds\n" \
  "s_cmp_eq_u32 vcc_hi, -1\n" \
  "s_cselect_b32 s66, s49, s59\n" \
  "s_xor_b32 s62, s64, 65536\n" \
  "s_mov_b32 m0, s62\n" \
  "s_nop 0\n" \
  "buffer_load_dwordx4 v0, s[36:39], s66 offen offset:0 sc1 lds\n" \
  "buffer_load_dwordx4 v0, s[36:39], s66 offen offset:1024 sc1 lds\n" \
  "buffer_load_dwordx4 v0, s[36:39], s66 offen offset:2048 sc1 lds\n" \
  "buffer_load_dwordx4 v0, s[36:39], s66 offen offset:3072 sc1 lds\n" \
  "s_add_u32 m0, s62, 16384\n" \
  "s_lshr_b32 s72, s66, 5\n" \
  "s_lshr_b32 s71, s62, 2\n" \
  "buffer_load_dwordx4 v1, s[36:39], s66 offen offset:0 sc1 lds\n" \
  "buffer_load_dwordx4 v1, s[36:39], s66 offen offset:1024 sc1 lds\n" \
  "buffer_load_dwordx4 v1, s[36:39], s66 offen offset:2048 sc1 lds\n" \
  "buffer_load_dwordx4 v1, s[36:39], s66 offen offset:3072 sc1 lds\n" \
  "s_add_u32 m0, s71, s88\n" \
  "s_nop 0\n" \
  "buffer_load_dword v9, s[36:39], s72 offen offset:0 sc1 lds\n" \
  "s_waitcnt vmcnt(15)\n" \
  "s_barrier\n" \
  "ds_read_b128 v[40:43], v2 offset:0\n" \
  "ds_read_b128 v[44:47], v2 offset:1024\n" \
  "ds_read_b128 v[48:51], v2 offset:2048\n" \
  "ds_read_b128 v[52:55], v2 offset:3072\n" \
  "ds_read_b128 v[56:59], v2 offset:4096\n" \
  "ds_read_b128 v[60:63], v2 offset:5120\n" \
  "ds_read_b128 v[64:67], v2 offset:6144\n" \
  "s_mul_i32 s70, s45, 5\n" \
  "s_lshr_b32 s70, s70, 6\n" \
  "buffer_store_dword v5, v4, s[36:39], s70 offen offset:0 sc0 sc1\n" \
  "s_mov_b32 s45, s59\n" \
  "s_mul_i32 s70, s46, 5\n" \
  "s_lshr_b32 s70, s70, 6\n" \
  "buffer_store_dword v5, v4, s[36:39], s70 offen offset:0 sc0 sc1\n" \
  "s_mov_b32 s46, s59\n" \
  "s_mul_i32 s70, s47, 5\n" \
  "s_lshr_b32 s70, s70, 6\n" \
  "buffer_store_dword v5, v4, s[36:39], s70 offen offset:0 sc0 sc1\n" \
  "s_mov_b32 s47, s59\n" \
  "s_waitcnt lgkmcnt(3)\n" \
  "s_branch Lg3o_d0_%=\n" \
  "Lg3o_done0_%=:\n" \
  "s_waitcnt vmcnt(0)\n" \
  "s_barrier\n" \
  "s_mul_i32 s70, s45, 5\n" \
  "s_lshr_b32 s70, s70, 6\n" \
  "buffer_store_dword v5, v4, s[36:39], s70 offen offset:0 sc0 sc1\n" \
  "s_mul_i32 s70, s46, 5\n" \
  "s_lshr_b32 s70, s70, 6\n" \
  "buffer_store_dword v5, v4, s[36:39], s70 offen offset:0 sc0 sc1\n" \
  "s_mul_i32 s70, s47, 5\n" \
  "s_lshr_b32 s70, s70, 6\n" \
  "buffer_store_dword v5, v4, s[36:39], s70 offen offset:0 sc0 sc1\n" \
  "s_branch Lg3o_done_%=\n" \
  "Lg3o_slow1_%=:\n" \
  "s_cmp_eq_u32 s54, 1\n" \
  "s_cselect_b32 s68, s50, s49\n" \
  "s_lshr_b32 s68, s68, 5\n" \
  "s_add_u32 m0, s78, 8192\n" \
  "s_nop 0\n" \
  "buffer_load_dword v6, s[36:39], s68 offen offset:0 sc1 lds\n" \
  "s_mul_i32 s73, s48, 7\n" \
  "s_lshr_b32 s73, s73, 1\n" \
  "v_mul_f32 v96, s75, v92\n" \
  "v_fma_f32 v93, v93, s75, v12\n" \
  "v_fma_f32 v93, -v96, v96, v93\n" \
  "v_max_f32 v93, v93, v12\n" \
  "v_rsq_f32 v92, v93\n" \
  "s_nop 0\n" \
  "v_mul_f32 v97, s76, v92\n" \
  "v_mul_f32 v98, v92, v92\n" \
  "v_mul_f32 v99, v93, v92\n" \
  "v_mul_f32 v98, s77, v98\n" \
  "v_mul_f32 v98, v98, v97\n" \
  "v_fma_f32 v24, -v96, v16, v24\n" \
  "v_fma_f32 v25, -v96, v17, v25\n" \
  "v_fma_f32 v26, -v96, v18, v26\n" \
  "v_fma_f32 v27, -v96, v19, v27\n" \
  "v_mul_f32 v72, v24, v24\n" \
  "v_mul_f32 v73, v25, v25\n" \
  "v_mul_f32 v74, v26, v26\n" \
  "v_mul_f32 v75, v27, v27\n" \
  "v_fma_f32 v72, v98, v72, v97\n" \
  "v_fma_f32 v73, v98, v73, v97\n" \
  "v_fma_f32 v74, v98, v74, v97\n" \
  "v_fma_f32 v75, v98, v75, v97\n" \
  "v_mul_f32 v72, v24, v72\n" \
  "v_mul_f32 v73, v25, v73\n" \
  "v_mul_f32 v74, v26, v74\n" \
  "v_mul_f32 v75, v27, v75\n" \
  "v_exp_f32 v72, v72\n" \
  "v_exp_f32 v73, v73\n" \
  "v_exp_f32 v74, v74\n" \
  "v_exp_f32 v75, v75\n" \
  "v_fma_f32 v72, v72, v99, v99\n" \
  "v_fma_f32 v73, v73, v99, v99\n" \
  "v_fma_f32 v74, v74, v99, v99\n" \
  "v_fma_f32 v75, v75, v99, v99\n" \
  "v_rcp_f32 v72, v72\n" \
  "v_rcp_f32 v73, v73\n" \
  "v_rcp_f32 v74, v74\n" \
  "v_rcp_f32 v75, v75\n" \
  "v_mul_f32 v24, v24, v72\n" \
  "s_add_u32 m0, s78, 8448\n" \
  "s_nop 0\n" \
  "buffer_load_dword v6, s[36:39], s68 offen offset:0 sc1 lds\n" \
  "v_mul_f32 v25, v25, v73\n" \
  "v_mul_f32 v26, v26, v74\n" \
  "v_mul_f32 v27, v27, v75\n" \
  "v_cvt_pk_bf16_f32 v80, v24, v25\n" \
  "v_cvt_pk_bf16_f32 v81, v26, v27\n" \
  "v_fma_f32 v28, -v96, v20, v28\n" \
  "v_fma_f32 v29, -v96, v21, v29\n" \
  "v_fma_f32 v30, -v96, v22, v30\n" \
  "v_fma_f32 v31, -v96, v23, v31\n" \
  "v_mul_f32 v76, v28, v28\n" \
  "v_mul_f32 v77, v29, v29\n" \
  "v_mul_f32 v78, v30, v30\n" \
  "v_mul_f32 v79, v31, v31\n" \
  "v_fma_f32 v76, v98, v76, v97\n" \
  "v_fma_f32 v77, v98, v77, v97\n" \
  "v_fma_f32 v78, v98, v78, v97\n" \
  "v_fma_f32 v79, v98, v79, v97\n" \
  "v_mul_f32 v76, v28, v76\n" \
  "v_mul_f32 v77, v29, v77\n" \
  "v_mul_f32 v78, v30, v78\n" \
  "v_mul_f32 v79, v31, v79\n" \
  "v_exp_f32 v76, v76\n" \
  "v_exp_f32 v77, v77\n" \
  "v_exp_f32 v78, v78\n" \
  "v_exp_f32 v79, v79\n" \
  "v_fma_f32 v76, v76, v99, v99\n" \
  "v_fma_f32 v77, v77, v99, v99\n" \
  "v_fma_f32 v78, v78, v99, v99\n" \
  "v_fma_f32 v79, v79, v99, v99\n" \
  "v_rcp_f32 v76, v76\n" \
  "v_rcp_f32 v77, v77\n" \
  "v_rcp_f32 v78, v78\n" \
  "v_rcp_f32 v79, v79\n" \
  "v_mul_f32 v28, v28, v76\n" \
  "v_mul_f32 v29, v29, v77\n" \
  "v_mul_f32 v30, v30, v78\n" \
  "v_mul_f32 v31, v31, v79\n" \
  "v_cvt_pk_bf16_f32 v82, v28, v29\n" \
  "v_cvt_pk_bf16_f32 v83, v30, v31\n" \
  "buffer_store_dwordx4 v[80:83], v3, s[36:39], s73 offen offset:0 sc0 sc1\n" \
  "s_cmp_ge_u32 s49, s59\n" \
  "s_cbranch_scc1 Lg3o_done1_%=\n" \
  "s_add_u32 m0, s78, 16384\n" \
  "s_nop 0\n" \
  "buffer_load_dword v6, s[36:39], s68 offen offset:0 sc1 lds\n" \
  "s_waitcnt vmcnt(3)\n" \
  "s_barrier\n" \
  "ds_read_b32 v13, v7 offset:8192\n" \
  "s_mul_i32 s70, s46, 5\n" \
  "s_lshr_b32 s70, s70, 6\n" \
  "buffer_store_dword v5, v4, s[36:39], s70 offen offset:0 sc0 sc1\n" \
  "s_mov_b32 s46, s59\n" \
  "s_mul_i32 s70, s47, 5\n" \
  "s_lshr_b32 s70, s70, 6\n" \
  "buffer_store_dword v5, v4, s[36:39], s70 offen offset:0 sc0 sc1\n" \
  "s_mov_b32 s47, s59\n" \
  "s_mov_b32 s69, 18432\n" \
  "s_waitcnt lgkmcnt(0)\n" \
  "v_cmp_eq_u32 vcc, s60, v13\n" \
  "s_nop 0\n" \
  "s_cmp_eq_u32 vcc_lo, -1\n" \
  "s_cbranch_scc1 Lg3o_go1_%=\n" \
  "s_add_u32 m0, s78, 16640\n" \
  "s_nop 0\n" \
  "buffer_load_dword v6, s[36:39], s68 offen offset:0 sc1 lds\n" \
  "s_waitcnt vmcnt(5)\n" \
  "s_barrier\n" \
  "ds_read_b32 v13, v7 offset:8448\n" \
  "s_mov_b32 s69, 18688\n" \
  "s_waitcnt lgkmcnt(0)\n" \
  "v_cmp_eq_u32 vcc, s60, v13\n" \
  "s_nop 0\n" \
  "s_cmp_eq_u32 vcc_lo, -1\n" \
  "s_cbranch_scc1 Lg3o_go1_%=\n" \
  "s_add_u32 m0, s78, 8192\n" \
  "s_nop 0\n" \
  "buffer_load_dword v6, s[36:39], s68 offen offset:0 sc1 lds\n" \
  "s_waitcnt vmcnt(4)\n" \
  "s_barrier\n" \
  "ds_read_b32 v13, v7 offset:16384\n" \
  "s_mul_i32 s70, s48, 5\n" \
  "s_lshr_b32 s70, s70, 6\n" \
  "buffer_store_dword v5, v4, s[36:39], s70 offen offset:0 sc0 sc1\n" \
  "s_mov_b32 s48, s59\n" \
  "s_mov_b32 s69, 10240\n" \
  "s_waitcnt lgkmcnt(0)\n" \
  "v_cmp_eq_u32 vcc, s60, v13\n" \
  "s_nop 0\n" \
  "s_cmp_eq_u32 vcc_lo, -1\n" \
  "s_cbranch_scc1 Lg3o_go1_%=\n" \
  "s_add_u32 m0, s78, 8448\n" \
  "s_nop 0\n" \
  "buffer_load_dword v6, s[36:39], s68 offen offset:0 sc1 lds\n" \
  "s_waitcnt vmcnt(3)\n" \
  "s_barrier\n" \
  "ds_read_b32 v13, v7 offset:16640\n" \
  "s_mov_b32 s69, 10496\n" \
  "s_waitcnt lgkmcnt(0)\n" \
  "v_cmp_eq_u32 vcc, s60, v13\n" \
  "s_nop 0\n" \
  "s_cmp_eq_u32 vcc_lo, -1\n" \
  "s_cbranch_scc1 Lg3o_go1_%=\n" \
  "Lg3o_poll1_%=:\n" \
  "s_add_u32 m0, s78, 16384\n" \
  "s_nop 0\n" \
  "buffer_load_dword v6, s[36:39], s68 offen offset:0 sc1 lds\n" \
  "s_waitcnt vmcnt(2)\n" \
  "s_barrier\n" \
  "ds_read_b32 v13, v7 offset:8192\n" \
  "s_mov_b32 s69, 18432\n" \
  "s_waitcnt lgkmcnt(0)\n" \
  "v_cmp_eq_u32 vcc, s60, v13\n" \
  "s_nop 0\n" \
  "s_cmp_eq_u32 vcc_lo, -1\n" \
  "s_cbranch_scc1 Lg3o_go1_%=\n" \
  "s_add_u32 m0, s78, 16640\n" \
  "s_nop 0\n" \
  "buffer_load_dword v6, s[36:39], s68 offen offset:0 sc1 lds\n" \
  "s_waitcnt vmcnt(2)\n" \
  "s_barrier\n" \
  "ds_read_b32 v13, v7 offset:8448\n" \
  "s_mov_b32 s69, 18688\n" \
  "s_waitcnt lgkmcnt(0)\n" \
  "v_cmp_eq_u32 vcc, s60, v13\n" \
  "s_nop 0\n" \
  "s_cmp_eq_u32 vcc_lo, -1\n" \
  "s_cbranch_scc1 Lg3o_go1_%=\n" \
  "s_add_u32 m0, s78, 8192\n" \
  "s_nop 0\n" \
  "buffer_load_dword v6, s[36:39], s68 offen offset:0 sc1 lds\n" \
  "s_waitcnt vmcnt(2)\n" \
  "s_barrier\n" \
  "ds_read_b32 v13, v7 offset:16384\n" \
  "s_mov_b32 s69, 10240\n" \
  "s_waitcnt lgkmcnt(0)\n" \
  "v_cmp_eq_u32 vcc, s60, v13\n" \
  "s_nop 0\n" \
  "s_cmp_eq_u32 vcc_lo, -1\n" \
  "s_cbranch_scc1 Lg3o_go1_%=\n" \
  "s_add_u32 m0, s78, 8448\n" \
  "s_nop 0\n" \
  "buffer_load_dword v6, s[36:39], s68 offen offset:0 sc1 lds\n" \
  "s_waitcnt vmcnt(2)\n" \
  "s_barrier\n" \
  "ds_read_b32 v13, v7 offset:16640\n" \
  "s_mov_b32 s69, 10496\n" \
  "s_waitcnt lgkmcnt(0)\n" \
  "v_cmp_eq_u32 vcc, s60, v13\n" \
  "s_nop 0\n" \
  "s_cmp_eq_u32 vcc_lo, -1\n" \
  "s_cbranch_scc1 Lg3o_go1_%=\n" \
  "s_add_u32 s79, s79, 1\n" \
  "s_cmp_lt_u32 s79, 20000\n" \
  "s_cbranch_scc1 Lg3o_poll1_%=\n" \
  "Lg3o_go1_%=:\n" \
  "s_xor_b32 s62, s64, 65536\n" \
  "s_cmp_lt_u32 s67, s59\n" \
  "s_cbranch_scc1 Lg3o_gs1_%=\n" \
  "s_mov_b32 m0, s62\n" \
  "s_nop 0\n" \
  "buffer_load_dwordx4 v0, s[36:39], s49 offen offset:0 sc1 lds\n" \
  "buffer_load_dwordx4 v0, s[36:39], s49 offen offset:1024 sc1 lds\n" \
  "buffer_load_dwordx4 v0, s[36:39], s49 offen offset:2048 sc1 lds\n" \
  "buffer_load_dwordx4 v0, s[36:39], s49 offen offset:3072 sc1 lds\n" \
  "s_add_u32 m0, s62, 16384\n" \
  "s_lshr_b32 s72, s49, 5\n" \
  "s_lshr_b32 s71, s62, 2\n" \
  "buffer_load_dwordx4 v1, s[36:39], s49 offen offset:0 sc1 lds\n" \
  "buffer_load_dwordx4 v1, s[36:39], s49 offen offset:1024 sc1 lds\n" \
  "buffer_load_dwordx4 v1, s[36:39], s49 offen offset:2048 sc1 lds\n" \
  "buffer_load_dwordx4 v1, s[36:39], s49 offen offset:3072 sc1 lds\n" \
  "s_add_u32 m0, s71, s88\n" \
  "s_nop 0\n" \
  "buffer_load_dword v9, s[36:39], s72 offen offset:0 sc1 lds\n" \
  "Lg3o_gs1_%=:\n" \
  "s_cmp_eq_u32 s54, 1\n" \
  "s_cselect_b32 s68, s51, s50\n" \
  "s_lshr_b32 s68, s68, 5\n" \
  "s_add_u32 m0, s78, 0\n" \
  "s_nop 0\n" \
  "buffer_load_dword v6, s[36:39], s68 offen offset:0 sc1 lds\n" \
  "s_cmp_eq_u32 vcc_hi, -1\n" \
  "s_cselect_b32 s67, s50, s59\n" \
  "s_xor_b32 s62, s63, 65536\n" \
  "s_mov_b32 m0, s62\n" \
  "s_nop 0\n" \
  "buffer_load_dwordx4 v0, s[36:39], s67 offen offset:0 sc1 lds\n" \
  "buffer_load_dwordx4 v0, s[36:39], s67 offen offset:1024 sc1 lds\n" \
  "buffer_load_dwordx4 v0, s[36:39], s67 offen offset:2048 sc1 lds\n" \
  "buffer_load_dwordx4 v0, s[36:39], s67 offen offset:3072 sc1 lds\n" \
  "s_add_u32 m0, s62, 16384\n" \
  "s_lshr_b32 s72, s67, 5\n" \
  "s_lshr_b32 s71, s62, 2\n" \
  "buffer_load_dwordx4 v1, s[36:39], s67 offen offset:0 sc1 lds\n" \
  "buffer_load_dwordx4 v1, s[36:39], s67 offen offset:1024 sc1 lds\n" \
  "buffer_load_dwordx4 v1, s[36:39], s67 offen offset:2048 sc1 lds\n" \
  "buffer_load_dwordx4 v1, s[36:39], s67 offen offset:3072 sc1 lds\n" \
  "s_add_u32 m0, s71, s88\n" \
  "s_nop 0\n" \
  "buffer_load_dword v9, s[36:39], s72 offen offset:0 sc1 lds\n" \
  "s_waitcnt vmcnt(15)\n" \
  "s_barrier\n" \
  "ds_read_b128 v[40:43], v2 offset:32768\n" \
  "ds_read_b128 v[44:47], v2 offset:33792\n" \
  "ds_read_b128 v[48:51], v2 offset:34816\n" \
  "ds_read_b128 v[52:55], v2 offset:35840\n" \
  "ds_read_b128 v[56:59], v2 offset:36864\n" \
  "ds_read_b128 v[60:63], v2 offset:37888\n" \
  "ds_read_b128 v[64:67], v2 offset:38912\n" \
  "s_mul_i32 s70, s46, 5\n" \
  "s_lshr_b32 s70, s70, 6\n" \
  "buffer_store_dword v5, v4, s[36:39], s70 offen offset:0 sc0 sc1\n" \
  "s_mov_b32 s46, s59\n" \
  "s_mul_i32 s70, s47, 5\n" \
  "s_lshr_b32 s70, s70, 6\n" \
  "buffer_store_dword v5, v4, s[36:39], s70 offen offset:0 sc0 sc1\n" \
  "s_mov_b32 s47, s59\n" \
  "s_mul_i32 s70, s48, 5\n" \
  "s_lshr_b32 s70, s70, 6\n" \
  "buffer_store_dword v5, v4, s[36:39], s70 offen offset:0 sc0 sc1\n" \
  "s_mov_b32 s48, s59\n" \
  "s_waitcnt lgkmcnt(3)\n" \
  "s_branch Lg3o_d1_%=\n" \
  "Lg3o_done1_%=:\n" \
  "s_waitcnt vmcnt(0)\n" \
  "s_barrier\n" \
  "s_mul_i32 s70, s46, 5\n" \
  "s_lshr_b32 s70, s70, 6\n" \
  "buffer_store_dword v5, v4, s[36:39], s70 offen offset:0 sc0 sc1\n" \
  "s_mul_i32 s70, s47, 5\n" \
  "s_lshr_b32 s70, s70, 6\n" \
  "buffer_store_dword v5, v4, s[36:39], s70 offen offset:0 sc0 sc1\n" \
  "s_mul_i32 s70, s48, 5\n" \
  "s_lshr_b32 s70, s70, 6\n" \
  "buffer_store_dword v5, v4, s[36:39], s70 offen offset:0 sc0 sc1\n" \
  "s_branch Lg3o_done_%=\n" \
  "Lg3o_done_%=:\n" \
  "s_waitcnt vmcnt(0) lgkmcnt(0)\n" \
  "s_barrier\n" \
  "s_endpgm\n" \
  "Lcode_g3o_e:\n" \
  :: "s"(ws), "s"(wave), "s"(sStep), "s"(sX1), "s"(sX2), "s"(sU0), "s"(sWw), "s"(sBuf), "v"(vSrc), "v"(vA), "v"(vOut), "v"(vFlag), "v"(vPollOff), "v"(vSnapRd), "v"(vX), "v"(vAuxS), "v"(vStW), "v"(vStR), "v"(vWd0), "v"(vWd1), "v"(vWr0), "v"(vWr1), "v"(vWr2), "s"(W) : "v0", "v1", "v2", "v3", "v4", "v5", "v6", "v7", "v8", "v9", "v10", "v11", "v12", "v13", "v14", "v15", "v16", "v17", "v18", "v19", "v20", "v21", "v22", "v23", "v24", "v25", "v26", "v27", "v28", "v29", "v30", "v31", "v32", "v33", "v34", "v35", "v36", "v37", "v38", "v39", "v40", "v41", "v42", "v43", "v44", "v45", "v46", "v47", "v48", "v49", "v50", "v51", "v52", "v53", "v54", "v55", "v56", "v57", "v58", "v59", "v60", "v61", "v62", "v63", "v64", "v65", "v66", "v67", "v68", "v69", "v70", "v71", "v72", "v73", "v74", "v75", "v76", "v77", "v78", "v79", "v80", "v81", "v82", "v83", "v84", "v85", "v86", "v87", "v88", "v89", "v90", "v91", "v92", "v93", "v94", "v95", "v96", "v97", "v98", "v99", "v100", "v101", "v102", "v103", "v104", "v105", "v106", "v107", "v108", "v109", "v110", "v111", "v112", "v113", "v114", "v115", "v116", "v117", "v118", "v119", "v120", "v121", "v122", "v123", "v124", "v125", "v126", "v127", "v128", "v129", "v130", "v131", "v132", "v133", "v134", "v135", "v136", "v137", "v138", "v139", "v140", "v141", "v142", "v143", "v144", "v145", "v146", "v147", "v148", "v149", "v150", "v151", "v152", "v153", "v154", "v155", "v156", "v157", "v158", "v159", "v160", "v161", "v162", "v163", "v164", "v165", "v166", "v167", "v168", "v169", "v170", "v171", "v172", "v173", "v174", "v175", "v176", "v177", "v178", "v179", "v180", "v181", "v182", "v183", "v184", "v185", "v186", "v187", "v188", "v189", "v190", "v191", "v192", "v193", "v194", "v195", "v196", "v197", "v198", "v199", "v200", "v201", "v202", "v203", "v204", "v205", "v206", "v207", "v208", "v209", "v210", "v211", "v212", "v213", "v214", "v215", "v216", "v217", "v218", "v219", "v220", "v221", "v222", "v223", "v224", "v225", "v226", "v227", "v228", "v229", "v230", "v231", "v255", "a0", "a1", "a2", "a3", "a4", "a5", "a6", "a7", "a8", "a9", "a10", "a11", "a12", "a13", "a14", "a15", "a16", "a17", "a18", "a19", "a20", "a21", "a22", "a23", "a24", "a25", "a26", "a27", "a28", "a29", "a30", "a31", "a32", "a33", "a34", "a35", "a36", "a37", "a38", "a39", "a40", "a41", "a42", "a43", "a44", "a45", "a46", "a47", "a48", "a49", "a50", "a51", "a52", "a53", "a54", "a55", "a56", "a57", "a58", "a59", "a60", "a61", "a62", "a63", "a64", "a65", "a66", "a67", "a68", "a69", "a70", "a71", "a72", "a73", "a74", "a75", "a76", "a77", "a78", "a79", "a80", "a81", "a82", "a83", "a84", "a85", "a86", "a87", "a88", "a89", "a90", "a91", "a92", "a93", "a94", "a95", "a96", "a97", "a98", "a99", "a100", "a101", "a102", "a103", "a104", "a105", "a106", "a107", "a108", "a109", "a110", "a111", "a112", "a113", "a114", "a115", "a116", "a117", "a118", "a119", "a120", "a121", "a122", "a123", "a124", "a125", "a126", "a127", "a128", "a129", "a130", "a131", "a132", "a133", "a134", "a135", "a136", "a137", "a138", "a139", "a140", "a141", "a142", "a143", "a144", "a145", "a146", "a147", "a148", "a149", "a150", "a151", "a152", "a153", "a154", "a155", "a156", "a157", "a158", "a159", "a160", "a161", "a162", "a163", "a164", "a165", "a166", "a167", "a168", "a169", "a170", "a171", "a172", "a173", "a174", "a175", "a176", "a177", "a178", "a179", "a180", "a181", "a182", "a183", "a184", "a185", "a186", "a187", "a188", "a189", "a190", "a191", "a192", "a193", "a194", "a195", "a196", "a197", "a198", "a199", "a200", "a201", "a202", "a203", "a204", "a205", "a206", "a207", "a208", "a209", "a210", "a211", "a212", "a213", "a214", "a215", "a216", "a217", "a218", "a219", "a220", "a221", "a222", "a223", "a224", "a225", "a226", "a227", "a228", "a229", "a230", "a231", "a232", "a233", "a234", "a235", "a236", "a237", "a238", "a239", "a240", "a241", "a242", "a243", "a244", "a245", "a246", "a247", "a248", "a249", "a250", "a251", "a252", "a253", "a254", "a255", "s36", "s37", "s38", "s39", "s40", "s41", "s42", "s43", "s44", "s45", "s46", "s47", "s48", "s49", "s50", "s51", "s52", "s53", "s54", "s55", "s56", "s57", "s58", "s59", "s60", "s61", "s62", "s63", "s64", "s65", "s66", "s67", "s68", "s69", "s70", "s71", "s72", "s73", "s74", "s75", "s76", "s77", "s78", "s79", "s80", "s81", "s82", "s83", "s84", "s85", "s86", "s87", "s88", "s89", "s90", "s91", "s92", "s93", "s94", "s95", "s96", "s97", "vcc", "scc", "memory")
#define ASM_G4(W) asm volatile( \
  "Lcode_g4_b:\n" \
  "s_mov_b64 s[36:37], %0\n" \
  "s_mov_b32 s38, 111193216\n" \
  "s_mov_b32 s39, 0x00020000\n" \
  "s_mov_b32 s54, %1\n" \
  "s_mov_b32 s55, %2\n" \
  "s_mov_b32 s56, %3\n" \
  "s_mov_b32 s58, %4\n" \
  "s_mov_b32 s83, %5\n" \
  "s_mov_b32 s82, %6\n" \
  "s_mov_b32 s84, %7\n" \
  "v_mov_b32 v0, %8\n" \
  "v_mov_b32 v1, %9\n" \
  "v_mov_b32 v3, %10\n" \
  "v_mov_b32 v4, %11\n" \
  "v_mov_b32 v6, %12\n" \
  "v_mov_b32 v7, %13\n" \
  "v_mov_b32 v8, %14\n" \
  "v_mov_b32 v9, %15\n" \
  "v_mov_b32 v12, %16\n" \
  "v_mov_b32 v13, %17\n" \
  "v_mov_b32 v14, %18\n" \
  "v_mov_b32 v15, %19\n" \
  "v_mov_b32 v16, %20\n" \
  "s_mov_b64 s[40:41], %21\n" \
  "s_mov_b32 s42, 7340032\n" \
  "s_mov_b32 s43, 0x00020000\n" \
  "s_lshl_b32 s88, s54, 11\n" \
  "s_mul_i32 s68, s54, 11264\n" \
  "s_add_u32 s68, s68, 114688\n" \
  "v_add_u32 v10, s68, v1\n" \
  "s_mov_b32 s69, 111193088\n" \
  "buffer_load_dword v5, off, s[36:39], s69 sc1\n" \
  "s_add_u32 s61, s82, 0\n" \
  "s_add_u32 m0, s88, 0\n" \
  "s_nop 0\n" \
  "buffer_load_dwordx4 v12, s[40:43], s61 offen offset:0  lds\n" \
  "s_add_u32 m0, s88, 1024\n" \
  "s_nop 0\n" \
  "buffer_load_dwordx4 v13, s[40:43], s61 offen offset:0  lds\n" \
  "s_add_u32 m0, s88, 8064\n" \
  "s_nop 0\n" \
  "buffer_load_dwordx4 v12, s[40:43], s61 offen offset:128  lds\n" \
  "s_add_u32 m0, s88, 9088\n" \
  "s_nop 0\n" \
  "buffer_load_dwordx4 v13, s[40:43], s61 offen offset:128  lds\n" \
  "s_add_u32 s61, s82, 131072\n" \
  "s_add_u32 m0, s88, 16384\n" \
  "s_nop 0\n" \
  "buffer_load_dwordx4 v12, s[40:43], s61 offen offset:0  lds\n" \
  "s_add_u32 m0, s88, 17408\n" \
  "s_nop 0\n" \
  "buffer_load_dwordx4 v13, s[40:43], s61 offen offset:0  lds\n" \
  "s_add_u32 m0, s88, 24448\n" \
  "s_nop 0\n" \
  "buffer_load_dwordx4 v12, s[40:43], s61 offen offset:128  lds\n" \
  "s_add_u32 m0, s88, 25472\n" \
  "s_nop 0\n" \
  "buffer_load_dwordx4 v13, s[40:43], s61 offen offset:128  lds\n" \
  "s_add_u32 s61, s82, 262144\n" \
  "s_add_u32 m0, s88, 32768\n" \
  "s_nop 0\n" \
  "buffer_load_dwordx4 v12, s[40:43], s61 offen offset:0  lds\n" \
  "s_add_u32 m0, s88, 33792\n" \
  "s_nop 0\n" \
  "buffer_load_dwordx4 v13, s[40:43], s61 offen offset:0  lds\n" \
  "s_add_u32 m0, s88, 40832\n" \
  "s_nop 0\n" \
  "buffer_load_dwordx4 v12, s[40:43], s61 offen offset:128  lds\n" \
  "s_add_u32 m0, s88, 41856\n" \
  "s_nop 0\n" \
  "buffer_load_dwordx4 v13, s[40:43], s61 offen offset:128  lds\n" \
  "s_add_u32 s61, s82, 393216\n" \
  "s_add_u32 m0, s88, 49152\n" \
  "s_nop 0\n" \
  "buffer_load_dwordx4 v12, s[40:43], s61 offen offset:0  lds\n" \
  "s_add_u32 m0, s88, 50176\n" \
  "s_nop 0\n" \
  "buffer_load_dwordx4 v13, s[40:43], s61 offen offset:0  lds\n" \
  "s_add_u32 m0, s88, 57216\n" \
  "s_nop 0\n" \
  "buffer_load_dwordx4 v12, s[40:43], s61 offen offset:128  lds\n" \
  "s_add_u32 m0, s88, 58240\n" \
  "s_nop 0\n" \
  "buffer_load_dwordx4 v13, s[40:43], s61 offen offset:128  lds\n" \
  "s_add_u32 s61, s82, 524288\n" \
  "s_add_u32 m0, s88, 65536\n" \
  "s_nop 0\n" \
  "buffer_load_dwordx4 v12, s[40:43], s61 offen offset:0  lds\n" \
  "s_add_u32 m0, s88, 66560\n" \
  "s_nop 0\n" \
  "buffer_load_dwordx4 v13, s[40:43], s61 offen offset:0  lds\n" \
  "s_add_u32 m0, s88, 73600\n" \
  "s_nop 0\n" \
  "buffer_load_dwordx4 v12, s[40:43], s61 offen offset:128  lds\n" \
  "s_add_u32 m0, s88, 74624\n" \
  "s_nop 0\n" \
  "buffer_load_dwordx4 v13, s[40:43], s61 offen offset:128  lds\n" \
  "s_add_u32 s61, s82, 655360\n" \
  "s_add_u32 m0, s88, 81920\n" \
  "s_nop 0\n" \
  "buffer_load_dwordx4 v12, s[40:43], s61 offen offset:0  lds\n" \
  "s_add_u32 m0, s88, 82944\n" \
  "s_nop 0\n" \
  "buffer_load_dwordx4 v13, s[40:43], s61 offen offset:0  lds\n" \
  "s_add_u32 m0, s88, 89984\n" \
  "s_nop 0\n" \
  "buffer_load_dwordx4 v12, s[40:43], s61 offen offset:128  lds\n" \
  "s_add_u32 m0, s88, 91008\n" \
  "s_nop 0\n" \
  "buffer_load_dwordx4 v13, s[40:43], s61 offen offset:128  lds\n" \
  "s_waitcnt vmcnt(20)\n" \
  "s_waitcnt lgkmcnt(0)\n" \
  "s_barrier\n" \
  "s_add_u32 s61, s82, 786432\n" \
  "s_add_u32 m0, s88, 98304\n" \
  "s_nop 0\n" \
  "buffer_load_dwordx4 v12, s[40:43], s61 offen offset:0  lds\n" \
  "s_add_u32 m0, s88, 99328\n" \
  "s_nop 0\n" \
  "buffer_load_dwordx4 v13, s[40:43], s61 offen offset:0  lds\n" \
  "s_add_u32 m0, s88, 106368\n" \
  "s_nop 0\n" \
  "buffer_load_dwordx4 v12, s[40:43], s61 offen offset:128  lds\n" \
  "s_add_u32 m0, s88, 107392\n" \
  "s_nop 0\n" \
  "buffer_load_dwordx4 v13, s[40:43], s61 offen offset:128  lds\n" \
  "v_add_u32 v60, 0, v14\n" \
  "v_add_u32 v61, 0, v15\n" \
  "ds_read_b64_tr_b16 a[0:1], v60 offset:0\n" \
  "ds_read_b64_tr_b16 a[2:3], v60 offset:512\n" \
  "ds_read_b64_tr_b16 a[4:5], v61 offset:0\n" \
  "ds_read_b64_tr_b16 a[6:7], v61 offset:512\n" \
  "ds_read_b64_tr_b16 a[8:9], v60 offset:4096\n" \
  "ds_read_b64_tr_b16 a[10:11], v60 offset:4608\n" \
  "ds_read_b64_tr_b16 a[12:13], v61 offset:4096\n" \
  "ds_read_b64_tr_b16 a[14:15], v61 offset:4608\n" \
  "s_waitcnt vmcnt(20)\n" \
  "s_waitcnt lgkmcnt(0)\n" \
  "s_barrier\n" \
  "s_add_u32 s61, s82, 917504\n" \
  "s_add_u32 m0, s88, 0\n" \
  "s_nop 0\n" \
  "buffer_load_dwordx4 v12, s[40:43], s61 offen offset:0  lds\n" \
  "s_add_u32 m0, s88, 1024\n" \
  "s_nop 0\n" \
  "buffer_load_dwordx4 v13, s[40:43], s61 offen offset:0  lds\n" \
  "s_add_u32 m0, s88, 8064\n" \
  "s_nop 0\n" \
  "buffer_load_dwordx4 v12, s[40:43], s61 offen offset:128  lds\n" \
  "s_add_u32 m0, s88, 9088\n" \
  "s_nop 0\n" \
  "buffer_load_dwordx4 v13, s[40:43], s61 offen offset:128  lds\n" \
  "v_add_u32 v60, 16384, v14\n" \
  "v_add_u32 v61, 16384, v15\n" \
  "ds_read_b64_tr_b16 a[16:17], v60 offset:0\n" \
  "ds_read_b64_tr_b16 a[18:19], v60 offset:512\n" \
  "ds_read_b64_tr_b16 a[20:21], v61 offset:0\n" \
  "ds_read_b64_tr_b16 a[22:23], v61 offset:512\n" \
  "ds_read_b64_tr_b16 a[24:25], v60 offset:4096\n" \
  "ds_read_b64_tr_b16 a[26:27], v60 offset:4608\n" \
  "ds_read_b64_tr_b16 a[28:29], v61 offset:4096\n" \
  "ds_read_b64_tr_b16 a[30:31], v61 offset:4608\n" \
  "s_waitcnt vmcnt(20)\n" \
  "s_waitcnt lgkmcnt(0)\n" \
  "s_barrier\n" \
  "s_add_u32 s61, s82, 1048576\n" \
  "s_add_u32 m0, s88, 16384\n" \
  "s_nop 0\n" \
  "buffer_load_dwordx4 v12, s[40:43], s61 offen offset:0  lds\n" \
  "s_add_u32 m0, s88, 17408\n" \
  "s_nop 0\n" \
  "buffer_load_dwordx4 v13, s[40:43], s61 offen offset:0  lds\n" \
  "s_add_u32 m0, s88, 24448\n" \
  "s_nop 0\n" \
  "buffer_load_dwordx4 v12, s[40:43], s61 offen offset:128  lds\n" \
  "s_add_u32 m0, s88, 25472\n" \
  "s_nop 0\n" \
  "buffer_load_dwordx4 v13, s[40:43], s61 offen offset:128  lds\n" \
  "v_add_u32 v60, 32768, v14\n" \
  "v_add_u32 v61, 32768, v15\n" \
  "ds_read_b64_tr_b16 a[32:33], v60 offset:0\n" \
  "ds_read_b64_tr_b16 a[34:35], v60 offset:512\n" \
  "ds_read_b64_tr_b16 a[36:37], v61 offset:0\n" \
  "ds_read_b64_tr_b16 a[38:39], v61 offset:512\n" \
  "ds_read_b64_tr_b16 a[40:41], v60 offset:4096\n" \
  "ds_read_b64_tr_b16 a[42:43], v60 offset:4608\n" \
  "ds_read_b64_tr_b16 a[44:45], v61 offset:4096\n" \
  "ds_read_b64_tr_b16 a[46:47], v61 offset:4608\n" \
  "s_waitcnt vmcnt(20)\n" \
  "s_waitcnt lgkmcnt(0)\n" \
  "s_barrier\n" \
  "s_add_u32 s61, s82, 1179648\n" \
  "s_add_u32 m0, s88, 32768\n" \
  "s_nop 0\n" \
  "buffer_load_dwordx4 v12, s[40:43], s61 offen offset:0  lds\n" \
  "s_add_u32 m0, s88, 33792\n" \
  "s_nop 0\n" \
  "buffer_load_dwordx4 v13, s[40:43], s61 offen offset:0  lds\n" \
  "s_add_u32 m0, s88, 40832\n" \
  "s_nop 0\n" \
  "buffer_load_dwordx4 v12, s[40:43], s61 offen offset:128  lds\n" \
  "s_add_u32 m0, s88, 41856\n" \
  "s_nop 0\n" \
  "buffer_load_dwordx4 v13, s[40:43], s61 offen offset:128  lds\n" \
  "v_add_u32 v60, 49152, v14\n" \
  "v_add_u32 v61, 49152, v15\n" \
  "ds_read_b64_tr_b16 a[48:49], v60 offset:0\n" \
  "ds_read_b64_tr_b16 a[50:51], v60 offset:512\n" \
  "ds_read_b64_tr_b16 a[52:53], v61 offset:0\n" \
  "ds_read_b64_tr_b16 a[54:55], v61 offset:512\n" \
  "ds_read_b64_tr_b16 a[56:57], v60 offset:4096\n" \
  "ds_read_b64_tr_b16 a[58:59], v60 offset:4608\n" \
  "ds_read_b64_tr_b16 a[60:61], v61 offset:4096\n" \
  "ds_read_b64_tr_b16 a[62:63], v61 offset:4608\n" \
  "s_waitcnt vmcnt(20)\n" \
  "s_waitcnt lgkmcnt(0)\n" \
  "s_barrier\n" \
  "s_add_u32 s61, s82, 1310720\n" \
  "s_add_u32 m0, s88, 49152\n" \
  "s_nop 0\n" \
  "buffer_load_dwordx4 v12, s[40:43], s61 offen offset:0  lds\n" \
  "s_add_u32 m0, s88, 50176\n" \
  "s_nop 0\n" \
  "buffer_load_dwordx4 v13, s[40:43], s61 offen offset:0  lds\n" \
  "s_add_u32 m0, s88, 57216\n" \
  "s_nop 0\n" \
  "buffer_load_dwordx4 v12, s[40:43], s61 offen offset:128  lds\n" \
  "s_add_u32 m0, s88, 58240\n" \
  "s_nop 0\n" \
  "buffer_load_dwordx4 v13, s[40:43], s61 offen offset:128  lds\n" \
  "v_add_u32 v60, 65536, v14\n" \
  "v_add_u32 v61, 65536, v15\n" \
  "ds_read_b64_tr_b16 a[64:65], v60 offset:0\n" \
  "ds_read_b64_tr_b16 a[66:67], v60 offset:512\n" \
  "ds_read_b64_tr_b16 a[68:69], v61 offset:0\n" \
  "ds_read_b64_tr_b16 a[70:71], v61 offset:512\n" \
  "ds_read_b64_tr_b16 a[72:73], v60 offset:4096\n" \
  "ds_read_b64_tr_b16 a[74:75], v60 offset:4608\n" \
  "ds_read_b64_tr_b16 a[76:77], v61 offset:4096\n" \
  "ds_read_b64_tr_b16 a[78:79], v61 offset:4608\n" \
  "s_waitcnt vmcnt(20)\n" \
  "s_waitcnt lgkmcnt(0)\n" \
  "s_barrier\n" \
  "s_add_u32 s61, s82, 1441792\n" \
  "s_add_u32 m0, s88, 65536\n" \
  "s_nop 0\n" \
  "buffer_load_dwordx4 v12, s[40:43], s61 offen offset:0  lds\n" \
  "s_add_u32 m0, s88, 66560\n" \
  "s_nop 0\n" \
  "buffer_load_dwordx4 v13, s[40:43], s61 offen offset:0  lds\n" \
  "s_add_u32 m0, s88, 73600\n" \
  "s_nop 0\n" \
  "buffer_load_dwordx4 v12, s[40:43], s61 offen offset:128  lds\n" \
  "s_add_u32 m0, s88, 74624\n" \
  "s_nop 0\n" \
  "buffer_load_dwordx4 v13, s[40:43], s61 offen offset:128  lds\n" \
  "v_add_u32 v60, 81920, v14\n" \
  "v_add_u32 v61, 81920, v15\n" \
  "ds_read_b64_tr_b16 a[80:81], v60 offset:0\n" \
  "ds_read_b64_tr_b16 a[82:83], v60 offset:512\n" \
  "ds_read_b64_tr_b16 a[84:85], v61 offset:0\n" \
  "ds_read_b64_tr_b16 a[86:87], v61 offset:512\n" \
  "ds_read_b64_tr_b16 a[88:89], v60 offset:4096\n" \
  "ds_read_b64_tr_b16 a[90:91], v60 offset:4608\n" \
  "ds_read_b64_tr_b16 a[92:93], v61 offset:4096\n" \
  "ds_read_b64_tr_b16 a[94:95], v61 offset:4608\n" \
  "s_waitcnt vmcnt(20)\n" \
  "s_waitcnt lgkmcnt(0)\n" \
  "s_barrier\n" \
  "s_add_u32 s61, s82, 1572864\n" \
  "s_add_u32 m0, s88, 81920\n" \
  "s_nop 0\n" \
  "buffer_load_dwordx4 v12, s[40:43], s61 offen offset:0  lds\n" \
  "s_add_u32 m0, s88, 82944\n" \
  "s_nop 0\n" \
  "buffer_load_dwordx4 v13, s[40:43], s61 offen offset:0  lds\n" \
  "s_add_u32 m0, s88, 89984\n" \
  "s_nop 0\n" \
  "buffer_load_dwordx4 v12, s[40:43], s61 offen offset:128  lds\n" \
  "s_add_u32 m0, s88, 91008\n" \
  "s_nop 0\n" \
  "buffer_load_dwordx4 v13, s[40:43], s61 offen offset:128  lds\n" \
  "v_add_u32 v60, 98304, v14\n" \
  "v_add_u32 v61, 98304, v15\n" \
  "ds_read_b64_tr_b16 a[96:97], v60 offset:0\n" \
  "ds_read_b64_tr_b16 a[98:99], v60 offset:512\n" \
  "ds_read_b64_tr_b16 a[100:101], v61 offset:0\n" \
  "ds_read_b64_tr_b16 a[102:103], v61 offset:512\n" \
  "ds_read_b64_tr_b16 a[104:105], v60 offset:4096\n" \
  "ds_read_b64_tr_b16 a[106:107], v60 offset:4608\n" \
  "ds_read_b64_tr_b16 a[108:109], v61 offset:4096\n" \
  "ds_read_b64_tr_b16 a[110:111], v61 offset:4608\n" \
  "s_waitcnt vmcnt(20)\n" \
  "s_waitcnt lgkmcnt(0)\n" \
  "s_barrier\n" \
  "s_add_u32 s61, s82, 1703936\n" \
  "s_add_u32 m0, s88, 98304\n" \
  "s_nop 0\n" \
  "buffer_load_dwordx4 v12, s[40:43], s61 offen offset:0  lds\n" \
  "s_add_u32 m0, s88, 99328\n" \
  "s_nop 0\n" \
  "buffer_load_dwordx4 v13, s[40:43], s61 offen offset:0  lds\n" \
  "s_add_u32 m0, s88, 106368\n" \
  "s_nop 0\n" \
  "buffer_load_dwordx4 v12, s[40:43], s61 offen offset:128  lds\n" \
  "s_add_u32 m0, s88, 107392\n" \
  "s_nop 0\n" \
  "buffer_load_dwordx4 v13, s[40:43], s61 offen offset:128  lds\n" \
  "v_add_u32 v60, 0, v14\n" \
  "v_add_u32 v61, 0, v15\n" \
  "ds_read_b64_tr_b16 a[112:113], v60 offset:0\n" \
  "ds_read_b64_tr_b16 a[114:115], v60 offset:512\n" \
  "ds_read_b64_tr_b16 a[116:117], v61 offset:0\n" \
  "ds_read_b64_tr_b16 a[118:119], v61 offset:512\n" \
  "ds_read_b64_tr_b16 a[120:121], v60 offset:4096\n" \
  "ds_read_b64_tr_b16 a[122:123], v60 offset:4608\n" \
  "ds_read_b64_tr_b16 a[124:125], v61 offset:4096\n" \
  "ds_read_b64_tr_b16 a[126:127], v61 offset:4608\n" \
  "s_waitcnt vmcnt(20)\n" \
  "s_waitcnt lgkmcnt(0)\n" \
  "s_barrier\n" \
  "s_add_u32 s61, s82, 1835008\n" \
  "s_add_u32 m0, s88, 0\n" \
  "s_nop 0\n" \
  "buffer_load_dwordx4 v12, s[40:43], s61 offen offset:0  lds\n" \
  "s_add_u32 m0, s88, 1024\n" \
  "s_nop 0\n" \
  "buffer_load_dwordx4 v13, s[40:43], s61 offen offset:0  lds\n" \
  "s_add_u32 m0, s88, 8064\n" \
  "s_nop 0\n" \
  "buffer_load_dwordx4 v12, s[40:43], s61 offen offset:128  lds\n" \
  "s_add_u32 m0, s88, 9088\n" \
  "s_nop 0\n" \
  "buffer_load_dwordx4 v13, s[40:43], s61 offen offset:128  lds\n" \
  "v_add_u32 v60, 16384, v14\n" \
  "v_add_u32 v61, 16384, v15\n" \
  "ds_read_b64_tr_b16 a[128:129], v60 offset:0\n" \
  "ds_read_b64_tr_b16 a[130:131], v60 offset:512\n" \
  "ds_read_b64_tr_b16 a[132:133], v61 offset:0\n" \
  "ds_read_b64_tr_b16 a[134:135], v61 offset:512\n" \
  "ds_read_b64_tr_b16 a[136:137], v60 offset:4096\n" \
  "ds_read_b64_tr_b16 a[138:139], v60 offset:4608\n" \
  "ds_read_b64_tr_b16 a[140:141], v61 offset:4096\n" \
  "ds_read_b64_tr_b16 a[142:143], v61 offset:4608\n" \
  "s_waitcnt vmcnt(20)\n" \
  "s_waitcnt lgkmcnt(0)\n" \
  "s_barrier\n" \
  "s_add_u32 s61, s82, 1966080\n" \
  "s_add_u32 m0, s88, 16384\n" \
  "s_nop 0\n" \
  "buffer_load_dwordx4 v12, s[40:43], s61 offen offset:0  lds\n" \
  "s_add_u32 m0, s88, 17408\n" \
  "s_nop 0\n" \
  "buffer_load_dwordx4 v13, s[40:43], s61 offen offset:0  lds\n" \
  "s_add_u32 m0, s88, 24448\n" \
  "s_nop 0\n" \
  "buffer_load_dwordx4 v12, s[40:43], s61 offen offset:128  lds\n" \
  "s_add_u32 m0, s88, 25472\n" \
  "s_nop 0\n" \
  "buffer_load_dwordx4 v13, s[40:43], s61 offen offset:128  lds\n" \
  "v_add_u32 v60, 32768, v14\n" \
  "v_add_u32 v61, 32768, v15\n" \
  "ds_read_b64_tr_b16 a[144:145], v60 offset:0\n" \
  "ds_read_b64_tr_b16 a[146:147], v60 offset:512\n" \
  "ds_read_b64_tr_b16 a[148:149], v61 offset:0\n" \
  "ds_read_b64_tr_b16 a[150:151], v61 offset:512\n" \
  "ds_read_b64_tr_b16 a[152:153], v60 offset:4096\n" \
  "ds_read_b64_tr_b16 a[154:155], v60 offset:4608\n" \
  "ds_read_b64_tr_b16 a[156:157], v61 offset:4096\n" \
  "ds_read_b64_tr_b16 a[158:159], v61 offset:4608\n" \
  "s_waitcnt vmcnt(20)\n" \
  "s_waitcnt lgkmcnt(0)\n" \
  "s_barrier\n" \
  "s_add_u32 s61, s82, 2097152\n" \
  "s_add_u32 m0, s88, 32768\n" \
  "s_nop 0\n" \
  "buffer_load_dwordx4 v12, s[40:43], s61 offen offset:0  lds\n" \
  "s_add_u32 m0, s88, 33792\n" \
  "s_nop 0\n" \
  "buffer_load_dwordx4 v13, s[40:43], s61 offen offset:0  lds\n" \
  "s_add_u32 m0, s88, 40832\n" \
  "s_nop 0\n" \
  "buffer_load_dwordx4 v12, s[40:43], s61 offen offset:128  lds\n" \
  "s_add_u32 m0, s88, 41856\n" \
  "s_nop 0\n" \
  "buffer_load_dwordx4 v13, s[40:43], s61 offen offset:128  lds\n" \
  "v_add_u32 v60, 49152, v14\n" \
  "v_add_u32 v61, 49152, v15\n" \
  "ds_read_b64_tr_b16 a[160:161], v60 offset:0\n" \
  "ds_read_b64_tr_b16 a[162:163], v60 offset:512\n" \
  "ds_read_b64_tr_b16 a[164:165], v61 offset:0\n" \
  "ds_read_b64_tr_b16 a[166:167], v61 offset:512\n" \
  "ds_read_b64_tr_b16 a[168:169], v60 offset:4096\n" \
  "ds_read_b64_tr_b16 a[170:171], v60 offset:4608\n" \
  "ds_read_b64_tr_b16 a[172:173], v61 offset:4096\n" \
  "ds_read_b64_tr_b16 a[174:175], v61 offset:4608\n" \
  "s_waitcnt vmcnt(20)\n" \
  "s_waitcnt lgkmcnt(0)\n" \
  "s_barrier\n" \
  "s_add_u32 s61, s82, 2228224\n" \
  "s_add_u32 m0, s88, 49152\n" \
  "s_nop 0\n" \
  "buffer_load_dwordx4 v12, s[40:43], s61 offen offset:0  lds\n" \
  "s_add_u32 m0, s88, 50176\n" \
  "s_nop 0\n" \
  "buffer_load_dwordx4 v13, s[40:43], s61 offen offset:0  lds\n" \
  "s_add_u32 m0, s88, 57216\n" \
  "s_nop 0\n" \
  "buffer_load_dwordx4 v12, s[40:43], s61 offen offset:128  lds\n" \
  "s_add_u32 m0, s88, 58240\n" \
  "s_nop 0\n" \
  "buffer_load_dwordx4 v13, s[40:43], s61 offen offset:128  lds\n" \
  "v_add_u32 v60, 65536, v14\n" \
  "v_add_u32 v61, 65536, v15\n" \
  "ds_read_b64_tr_b16 a[176:177], v60 offset:0\n" \
  "ds_read_b64_tr_b16 a[178:179], v60 offset:512\n" \
  "ds_read_b64_tr_b16 a[180:181], v61 offset:0\n" \
  "ds_read_b64_tr_b16 a[182:183], v61 offset:512\n" \
  "ds_read_b64_tr_b16 a[184:185], v60 offset:4096\n" \
  "ds_read_b64_tr_b16 a[186:187], v60 offset:4608\n" \
  "ds_read_b64_tr_b16 a[188:189], v61 offset:4096\n" \
  "ds_read_b64_tr_b16 a[190:191], v61 offset:4608\n" \
  "s_waitcnt vmcnt(20)\n" \
  "s_waitcnt lgkmcnt(0)\n" \
  "s_barrier\n" \
  "s_add_u32 s61, s82, 2359296\n" \
  "s_add_u32 m0, s88, 65536\n" \
  "s_nop 0\n" \
  "buffer_load_dwordx4 v12, s[40:43], s61 offen offset:0  lds\n" \
  "s_add_u32 m0, s88, 66560\n" \
  "s_nop 0\n" \
  "buffer_load_dwordx4 v13, s[40:43], s61 offen offset:0  lds\n" \
  "s_add_u32 m0, s88, 73600\n" \
  "s_nop 0\n" \
  "buffer_load_dwordx4 v12, s[40:43], s61 offen offset:128  lds\n" \
  "s_add_u32 m0, s88, 74624\n" \
  "s_nop 0\n" \
  "buffer_load_dwordx4 v13, s[40:43], s61 offen offset:128  lds\n" \
  "v_add_u32 v60, 81920, v14\n" \
  "v_add_u32 v61, 81920, v15\n" \
  "ds_read_b64_tr_b16 a[192:193], v60 offset:0\n" \
  "ds_read_b64_tr_b16 a[194:195], v60 offset:512\n" \
  "ds_read_b64_tr_b16 a[196:197], v61 offset:0\n" \
  "ds_read_b64_tr_b16 a[198:199], v61 offset:512\n" \
  "ds_read_b64_tr_b16 a[200:201], v60 offset:4096\n" \
  "ds_read_b64_tr_b16 a[202:203], v60 offset:4608\n" \
  "ds_read_b64_tr_b16 a[204:205], v61 offset:4096\n" \
  "ds_read_b64_tr_b16 a[206:207], v61 offset:4608\n" \
  "s_waitcnt vmcnt(20)\n" \
  "s_waitcnt lgkmcnt(0)\n" \
  "s_barrier\n" \
  "s_add_u32 s61, s82, 2490368\n" \
  "s_add_u32 m0, s88, 81920\n" \
  "s_nop 0\n" \
  "buffer_load_dwordx4 v12, s[40:43], s61 offen offset:0  lds\n" \
  "s_add_u32 m0, s88, 82944\n" \
  "s_nop 0\n" \
  "buffer_load_dwordx4 v13, s[40:43], s61 offen offset:0  lds\n" \
  "s_add_u32 m0, s88, 89984\n" \
  "s_nop 0\n" \
  "buffer_load_dwordx4 v12, s[40:43], s61 offen offset:128  lds\n" \
  "s_add_u32 m0, s88, 91008\n" \
  "s_nop 0\n" \
  "buffer_load_dwordx4 v13, s[40:43], s61 offen offset:128  lds\n" \
  "v_add_u32 v60, 98304, v14\n" \
  "v_add_u32 v61, 98304, v15\n" \
  "ds_read_b64_tr_b16 a[208:209], v60 offset:0\n" \
  "ds_read_b64_tr_b16 a[210:211], v60 offset:512\n" \
  "ds_read_b64_tr_b16 a[212:213], v61 offset:0\n" \
  "ds_read_b64_tr_b16 a[214:215], v61 offset:512\n" \
  "ds_read_b64_tr_b16 a[216:217], v60 offset:4096\n" \
  "ds_read_b64_tr_b16 a[218:219], v60 offset:4608\n" \
  "ds_read_b64_tr_b16 a[220:221], v61 offset:4096\n" \
  "ds_read_b64_tr_b16 a[222:223], v61 offset:4608\n" \
  "s_waitcnt vmcnt(20)\n" \
  "s_waitcnt lgkmcnt(0)\n" \
  "s_barrier\n" \
  "s_add_u32 s61, s82, 2621440\n" \
  "s_add_u32 m0, s88, 98304\n" \
  "s_nop 0\n" \
  "buffer_load_dwordx4 v12, s[40:43], s61 offen offset:0  lds\n" \
  "s_add_u32 m0, s88, 99328\n" \
  "s_nop 0\n" \
  "buffer_load_dwordx4 v13, s[40:43], s61 offen offset:0  lds\n" \
  "s_add_u32 m0, s88, 106368\n" \
  "s_nop 0\n" \
  "buffer_load_dwordx4 v12, s[40:43], s61 offen offset:128  lds\n" \
  "s_add_u32 m0, s88, 107392\n" \
  "s_nop 0\n" \
  "buffer_load_dwordx4 v13, s[40:43], s61 offen offset:128  lds\n" \
  "v_add_u32 v60, 0, v14\n" \
  "v_add_u32 v61, 0, v15\n" \
  "ds_read_b64_tr_b16 a[224:225], v60 offset:0\n" \
  "ds_read_b64_tr_b16 a[226:227], v60 offset:512\n" \
  "ds_read_b64_tr_b16 a[228:229], v61 offset:0\n" \
  "ds_read_b64_tr_b16 a[230:231], v61 offset:512\n" \
  "ds_read_b64_tr_b16 a[232:233], v60 offset:4096\n" \
  "ds_read_b64_tr_b16 a[234:235], v60 offset:4608\n" \
  "ds_read_b64_tr_b16 a[236:237], v61 offset:4096\n" \
  "ds_read_b64_tr_b16 a[238:239], v61 offset:4608\n" \
  "s_waitcnt vmcnt(20)\n" \
  "s_waitcnt lgkmcnt(0)\n" \
  "s_barrier\n" \
  "s_add_u32 s61, s82, 2752512\n" \
  "s_add_u32 m0, s88, 0\n" \
  "s_nop 0\n" \
  "buffer_load_dwordx4 v12, s[40:43], s61 offen offset:0  lds\n" \
  "s_add_u32 m0, s88, 1024\n" \
  "s_nop 0\n" \
  "buffer_load_dwordx4 v13, s[40:43], s61 offen offset:0  lds\n" \
  "s_add_u32 m0, s88, 8064\n" \
  "s_nop 0\n" \
  "buffer_load_dwordx4 v12, s[40:43], s61 offen offset:128  lds\n" \
  "s_add_u32 m0, s88, 9088\n" \
  "s_nop 0\n" \
  "buffer_load_dwordx4 v13, s[40:43], s61 offen offset:128  lds\n" \
  "v_add_u32 v60, 16384, v14\n" \
  "v_add_u32 v61, 16384, v15\n" \
  "ds_read_b64_tr_b16 a[240:241], v60 offset:0\n" \
  "ds_read_b64_tr_b16 a[242:243], v60 offset:512\n" \
  "ds_read_b64_tr_b16 a[244:245], v61 offset:0\n" \
  "ds_read_b64_tr_b16 a[246:247], v61 offset:512\n" \
  "ds_read_b64_tr_b16 a[248:249], v60 offset:4096\n" \
  "ds_read_b64_tr_b16 a[250:251], v60 offset:4608\n" \
  "ds_read_b64_tr_b16 a[252:253], v61 offset:4096\n" \
  "ds_read_b64_tr_b16 a[254:255], v61 offset:4608\n" \
  "s_waitcnt vmcnt(20)\n" \
  "s_waitcnt lgkmcnt(0)\n" \
  "s_barrier\n" \
  "s_add_u32 s61, s82, 2883584\n" \
  "s_add_u32 m0, s88, 16384\n" \
  "s_nop 0\n" \
  "buffer_load_dwordx4 v12, s[40:43], s61 offen offset:0  lds\n" \
  "s_add_u32 m0, s88, 17408\n" \
  "s_nop 0\n" \
  "buffer_load_dwordx4 v13, s[40:43], s61 offen offset:0  lds\n" \
  "s_add_u32 m0, s88, 24448\n" \
  "s_nop 0\n" \
  "buffer_load_dwordx4 v12, s[40:43], s61 offen offset:128  lds\n" \
  "s_add_u32 m0, s88, 25472\n" \
  "s_nop 0\n" \
  "buffer_load_dwordx4 v13, s[40:43], s61 offen offset:128  lds\n" \
  "v_add_u32 v60, 32768, v14\n" \
  "v_add_u32 v61, 32768, v15\n" \
  "ds_read_b64_tr_b16 v[108:109], v60 offset:0\n" \
  "ds_read_b64_tr_b16 v[110:111], v60 offset:512\n" \
  "ds_read_b64_tr_b16 v[112:113], v61 offset:0\n" \
  "ds_read_b64_tr_b16 v[114:115], v61 offset:512\n" \
  "ds_read_b64_tr_b16 v[116:117], v60 offset:4096\n" \
  "ds_read_b64_tr_b16 v[118:119], v60 offset:4608\n" \
  "ds_read_b64_tr_b16 v[120:121], v61 offset:4096\n" \
  "ds_read_b64_tr_b16 v[122:123], v61 offset:4608\n" \
  "s_waitcnt vmcnt(20)\n" \
  "s_waitcnt lgkmcnt(0)\n" \
  "s_barrier\n" \
  "s_add_u32 s61, s82, 3014656\n" \
  "s_add_u32 m0, s88, 32768\n" \
  "s_nop 0\n" \
  "buffer_load_dwordx4 v12, s[40:43], s61 offen offset:0  lds\n" \
  "s_add_u32 m0, s88, 33792\n" \
  "s_nop 0\n" \
  "buffer_load_dwordx4 v13, s[40:43], s61 offen offset:0  lds\n" \
  "s_add_u32 m0, s88, 40832\n" \
  "s_nop 0\n" \
  "buffer_load_dwordx4 v12, s[40:43], s61 offen offset:128  lds\n" \
  "s_add_u32 m0, s88, 41856\n" \
  "s_nop 0\n" \
  "buffer_load_dwordx4 v13, s[40:43], s61 offen offset:128  lds\n" \
  "v_add_u32 v60, 49152, v14\n" \
  "v_add_u32 v61, 49152, v15\n" \
  "ds_read_b64_tr_b16 v[124:125], v60 offset:0\n" \
  "ds_read_b64_tr_b16 v[126:127], v60 offset:512\n" \
  "ds_read_b64_tr_b16 v[128:129], v61 offset:0\n" \
  "ds_read_b64_tr_b16 v[130:131], v61 offset:512\n" \
  "ds_read_b64_tr_b16 v[132:133], v60 offset:4096\n" \
  "ds_read_b64_tr_b16 v[134:135], v60 offset:4608\n" \
  "ds_read_b64_tr_b16 v[136:137], v61 offset:4096\n" \
  "ds_read_b64_tr_b16 v[138:139], v61 offset:4608\n" \
  "s_waitcnt vmcnt(20)\n" \
  "s_waitcnt lgkmcnt(0)\n" \
  "s_barrier\n" \
  "s_add_u32 s61, s82, 3145728\n" \
  "s_add_u32 m0, s88, 49152\n" \
  "s_nop 0\n" \
  "buffer_load_dwordx4 v12, s[40:43], s61 offen offset:0  lds\n" \
  "s_add_u32 m0, s88, 50176\n" \
  "s_nop 0\n" \
  "buffer_load_dwordx4 v13, s[40:43], s61 offen offset:0  lds\n" \
  "s_add_u32 m0, s88, 57216\n" \
  "s_nop 0\n" \
  "buffer_load_dwordx4 v12, s[40:43], s61 offen offset:128  lds\n" \
  "s_add_u32 m0, s88, 58240\n" \
  "s_nop 0\n" \
  "buffer_load_dwordx4 v13, s[40:43], s61 offen offset:128  lds\n" \
  "v_add_u32 v60, 65536, v14\n" \
  "v_add_u32 v61, 65536, v15\n" \
  "ds_read_b64_tr_b16 v[140:141], v60 offset:0\n" \
  "ds_read_b64_tr_b16 v[142:143], v60 offset:512\n" \
  "ds_read_b64_tr_b16 v[144:145], v61 offset:0\n" \
  "ds_read_b64_tr_b16 v[146:147], v61 offset:512\n" \
  "ds_read_b64_tr_b16 v[148:149], v60 offset:4096\n" \
  "ds_read_b64_tr_b16 v[150:151], v60 offset:4608\n" \
  "ds_read_b64_tr_b16 v[152:153], v61 offset:4096\n" \
  "ds_read_b64_tr_b16 v[154:155], v61 offset:4608\n" \
  "s_waitcnt vmcnt(20)\n" \
  "s_waitcnt lgkmcnt(0)\n" \
  "s_barrier\n" \
  "s_add_u32 s61, s82, 3276800\n" \
  "s_add_u32 m0, s88, 65536\n" \
  "s_nop 0\n" \
  "buffer_load_dwordx4 v12, s[40:43], s61 offen offset:0  lds\n" \
  "s_add_u32 m0, s88, 66560\n" \
  "s_nop 0\n" \
  "buffer_load_dwordx4 v13, s[40:43], s61 offen offset:0  lds\n" \
  "s_add_u32 m0, s88, 73600\n" \
  "s_nop 0\n" \
  "buffer_load_dwordx4 v12, s[40:43], s61 offen offset:128  lds\n" \
  "s_add_u32 m0, s88, 74624\n" \
  "s_nop 0\n" \
  "buffer_load_dwordx4 v13, s[40:43], s61 offen offset:128  lds\n" \
  "v_add_u32 v60, 81920, v14\n" \
  "v_add_u32 v61, 81920, v15\n" \
  "ds_read_b64_tr_b16 v[156:157], v60 offset:0\n" \
  "ds_read_b64_tr_b16 v[158:159], v60 offset:512\n" \
  "ds_read_b64_tr_b16 v[160:161], v61 offset:0\n" \
  "ds_read_b64_tr_b16 v[162:163], v61 offset:512\n" \
  "ds_read_b64_tr_b16 v[164:165], v60 offset:4096\n" \
  "ds_read_b64_tr_b16 v[166:167], v60 offset:4608\n" \
  "ds_read_b64_tr_b16 v[168:169], v61 offset:4096\n" \
  "ds_read_b64_tr_b16 v[170:171], v61 offset:4608\n" \
  "s_waitcnt vmcnt(20)\n" \
  "s_waitcnt lgkmcnt(0)\n" \
  "s_barrier\n" \
  "s_add_u32 s61, s82, 3407872\n" \
  "s_add_u32 m0, s88, 81920\n" \
  "s_nop 0\n" \
  "buffer_load_dwordx4 v12, s[40:43], s61 offen offset:0  lds\n" \
  "s_add_u32 m0, s88, 82944\n" \
  "s_nop 0\n" \
  "buffer_load_dwordx4 v13, s[40:43], s61 offen offset:0  lds\n" \
  "s_add_u32 m0, s88, 89984\n" \
  "s_nop 0\n" \
  "buffer_load_dwordx4 v12, s[40:43], s61 offen offset:128  lds\n" \
  "s_add_u32 m0, s88, 91008\n" \
  "s_nop 0\n" \
  "buffer_load_dwordx4 v13, s[40:43], s61 offen offset:128  lds\n" \
  "v_add_u32 v60, 98304, v14\n" \
  "v_add_u32 v61, 98304, v15\n" \
  "ds_read_b64_tr_b16 v[172:173], v60 offset:0\n" \
  "ds_read_b64_tr_b16 v[174:175], v60 offset:512\n" \
  "ds_read_b64_tr_b16 v[176:177], v61 offset:0\n" \
  "ds_read_b64_tr_b16 v[178:179], v61 offset:512\n" \
  "ds_read_b64_tr_b16 v[180:181], v60 offset:4096\n" \
  "ds_read_b64_tr_b16 v[182:183], v60 offset:4608\n" \
  "ds_read_b64_tr_b16 v[184:185], v61 offset:4096\n" \
  "ds_read_b64_tr_b16 v[186:187], v61 offset:4608\n" \
  "s_waitcnt vmcnt(20)\n" \
  "s_waitcnt lgkmcnt(0)\n" \
  "s_barrier\n" \
  "s_add_u32 s61, s82, 3538944\n" \
  "s_add_u32 m0, s88, 98304\n" \
  "s_nop 0\n" \
  "buffer_load_dwordx4 v12, s[40:43], s61 offen offset:0  lds\n" \
  "s_add_u32 m0, s88, 99328\n" \
  "s_nop 0\n" \
  "buffer_load_dwordx4 v13, s[40:43], s61 offen offset:0  lds\n" \
  "s_add_u32 m0, s88, 106368\n" \
  "s_nop 0\n" \
  "buffer_load_dwordx4 v12, s[40:43], s61 offen offset:128  lds\n" \
  "s_add_u32 m0, s88, 107392\n" \
  "s_nop 0\n" \
  "buffer_load_dwordx4 v13, s[40:43], s61 offen offset:128  lds\n" \
  "v_add_u32 v60, 0, v14\n" \
  "v_add_u32 v61, 0, v15\n" \
  "ds_read_b64_tr_b16 v[188:189], v60 offset:0\n" \
  "ds_read_b64_tr_b16 v[190:191], v60 offset:512\n" \
  "ds_read_b64_tr_b16 v[192:193], v61 offset:0\n" \
  "ds_read_b64_tr_b16 v[194:195], v61 offset:512\n" \
  "ds_read_b64_tr_b16 v[196:197], v60 offset:4096\n" \
  "ds_read_b64_tr_b16 v[198:199], v60 offset:4608\n" \
  "ds_read_b64_tr_b16 v[200:201], v61 offset:4096\n" \
  "ds_read_b64_tr_b16 v[202:203], v61 offset:4608\n" \
  "s_waitcnt vmcnt(20)\n" \
  "s_waitcnt lgkmcnt(0)\n" \
  "s_barrier\n" \
  "v_add_u32 v60, 16384, v14\n" \
  "v_add_u32 v61, 16384, v15\n" \
  "ds_read_b64_tr_b16 v[204:205], v60 offset:0\n" \
  "ds_read_b64_tr_b16 v[206:207], v60 offset:512\n" \
  "ds_read_b64_tr_b16 v[208:209], v61 offset:0\n" \
  "ds_read_b64_tr_b16 v[210:211], v61 offset:512\n" \
  "ds_read_b64_tr_b16 v[212:213], v60 offset:4096\n" \
  "ds_read_b64_tr_b16 v[214:215], v60 offset:4608\n" \
  "ds_read_b64_tr_b16 v[32:33], v61 offset:4096\n" \
  "ds_read_b64_tr_b16 v[34:35], v61 offset:4608\n" \
  "s_waitcnt lgkmcnt(0)\n" \
  "ds_write_b128 v10, v[32:35] offset:0\n" \
  "s_waitcnt vmcnt(16)\n" \
  "s_waitcnt lgkmcnt(0)\n" \
  "s_barrier\n" \
  "v_add_u32 v60, 32768, v14\n" \
  "v_add_u32 v61, 32768, v15\n" \
  "ds_read_b64_tr_b16 v[216:217], v60 offset:0\n" \
  "ds_read_b64_tr_b16 v[218:219], v60 offset:512\n" \
  "ds_read_b64_tr_b16 v[28:29], v61 offset:0\n" \
  "ds_read_b64_tr_b16 v[30:31], v61 offset:512\n" \
  "ds_read_b64_tr_b16 v[220:221], v60 offset:4096\n" \
  "ds_read_b64_tr_b16 v[222:223], v60 offset:4608\n" \
  "ds_read_b64_tr_b16 v[32:33], v61 offset:4096\n" \
  "ds_read_b64_tr_b16 v[34:35], v61 offset:4608\n" \
  "s_waitcnt lgkmcnt(0)\n" \
  "ds_write_b128 v10, v[28:31] offset:1024\n" \
  "ds_write_b128 v10, v[32:35] offset:2048\n" \
  "s_waitcnt vmcnt(12)\n" \
  "s_waitcnt lgkmcnt(0)\n" \
  "s_barrier\n" \
  "v_add_u32 v60, 49152, v14\n" \
  "v_add_u32 v61, 49152, v15\n" \
  "ds_read_b64_tr_b16 v[224:225], v60 offset:0\n" \
  "ds_read_b64_tr_b16 v[226:227], v60 offset:512\n" \
  "ds_read_b64_tr_b16 v[28:29], v61 offset:0\n" \
  "ds_read_b64_tr_b16 v[30:31], v61 offset:512\n" \
  "ds_read_b64_tr_b16 v[228:229], v60 offset:4096\n" \
  "ds_read_b64_tr_b16 v[230:231], v60 offset:4608\n" \
  "ds_read_b64_tr_b16 v[32:33], v61 offset:4096\n" \
  "ds_read_b64_tr_b16 v[34:35], v61 offset:4608\n" \
  "s_waitcnt lgkmcnt(0)\n" \
  "ds_write_b128 v10, v[28:31] offset:3072\n" \
  "ds_write_b128 v10, v[32:35] offset:4096\n" \
  "s_waitcnt vmcnt(8)\n" \
  "s_waitcnt lgkmcnt(0)\n" \
  "s_barrier\n" \
  "v_add_u32 v60, 65536, v14\n" \
  "v_add_u32 v61, 65536, v15\n" \
  "ds_read_b64_tr_b16 v[232:233], v60 offset:0\n" \
  "ds_read_b64_tr_b16 v[234:235], v60 offset:512\n" \
  "ds_read_b64_tr_b16 v[28:29], v61 offset:0\n" \
  "ds_read_b64_tr_b16 v[30:31], v61 offset:512\n" \
  "ds_read_b64_tr_b16 v[236:237], v60 offset:4096\n" \
  "ds_read_b64_tr_b16 v[238:239], v60 offset:4608\n" \
  "ds_read_b64_tr_b16 v[32:33], v61 offset:4096\n" \
  "ds_read_b64_tr_b16 v[34:35], v61 offset:4608\n" \
  "s_waitcnt lgkmcnt(0)\n" \
  "ds_write_b128 v10, v[28:31] offset:5120\n" \
  "ds_write_b128 v10, v[32:35] offset:6144\n" \
  "s_waitcnt vmcnt(4)\n" \
  "s_waitcnt lgkmcnt(0)\n" \
  "s_barrier\n" \
  "v_add_u32 v60, 81920, v14\n" \
  "v_add_u32 v61, 81920, v15\n" \
  "ds_read_b64_tr_b16 v[240:241], v60 offset:0\n" \
  "ds_read_b64_tr_b16 v[242:243], v60 offset:512\n" \
  "ds_read_b64_tr_b16 v[28:29], v61 offset:0\n" \
  "ds_read_b64_tr_b16 v[30:31], v61 offset:512\n" \
  "ds_read_b64_tr_b16 v[244:245], v60 offset:4096\n" \
  "ds_read_b64_tr_b16 v[246:247], v60 offset:4608\n" \
  "ds_read_b64_tr_b16 v[32:33], v61 offset:4096\n" \
  "ds_read_b64_tr_b16 v[34:35], v61 offset:4608\n" \
  "s_waitcnt lgkmcnt(0)\n" \
  "ds_write_b128 v10, v[28:31] offset:7168\n" \
  "ds_write_b128 v10, v[32:35] offset:8192\n" \
  "s_waitcnt vmcnt(0)\n" \
  "s_waitcnt lgkmcnt(0)\n" \
  "s_barrier\n" \
  "v_add_u32 v60, 98304, v14\n" \
  "v_add_u32 v61, 98304, v15\n" \
  "ds_read_b64_tr_b16 v[248:249], v60 offset:0\n" \
  "ds_read_b64_tr_b16 v[250:251], v60 offset:512\n" \
  "ds_read_b64_tr_b16 v[28:29], v61 offset:0\n" \
  "ds_read_b64_tr_b16 v[30:31], v61 offset:512\n" \
  "ds_read_b64_tr_b16 v[252:253], v60 offset:4096\n" \
  "ds_read_b64_tr_b16 v[254:255], v60 offset:4608\n" \
  "ds_read_b64_tr_b16 v[32:33], v61 offset:4096\n" \
  "ds_read_b64_tr_b16 v[34:35], v61 offset:4608\n" \
  "s_waitcnt lgkmcnt(0)\n" \
  "ds_write_b128 v10, v[28:31] offset:9216\n" \
  "ds_write_b128 v10, v[32:35] offset:10240\n" \
  "s_waitcnt lgkmcnt(0)\n" \
  "s_barrier\n" \
  "v_readfirstlane_b32 s60, v5\n" \
  "s_nop 3\n" \
  "s_add_u32 s60, s60, 1\n" \
  "s_cmp_eq_u32 s60, 0\n" \
  "s_cselect_b32 s60, 1, s60\n" \
  "v_mov_b32 v5, s60\n" \
  "s_mov_b32 s59, 536870912\n" \
  "s_add_u32 s57, s56, 131072\n" \
  "s_mov_b32 s79, 0\n" \
  "s_mov_b32 s75, 0x3a800000\n" \
  "s_mov_b32 s76, 0xc0135761\n" \
  "s_mov_b32 s77, 0x3d372713\n" \
  "s_mov_b32 s44, s59\n" \
  "s_mov_b32 s45, s59\n" \
  "s_mov_b32 s46, s59\n" \
  "s_mov_b32 s47, s59\n" \
  "s_mov_b32 s48, s83\n" \
  "s_add_u32 s49, s48, s55\n" \
  "s_cmp_eq_u32 s49, s56\n" \
  "s_cselect_b32 s49, s57, s49\n" \
  "s_cmp_eq_u32 s49, s58\n" \
  "s_cselect_b32 s49, s59, s49\n" \
  "s_add_u32 s50, s49, s55\n" \
  "s_cmp_eq_u32 s50, s56\n" \
  "s_cselect_b32 s50, s57, s50\n" \
  "s_cmp_eq_u32 s50, s58\n" \
  "s_cselect_b32 s50, s59, s50\n" \
  "s_add_u32 s51, s50, s55\n" \
  "s_cmp_eq_u32 s51, s56\n" \
  "s_cselect_b32 s51, s57, s51\n" \
  "s_cmp_eq_u32 s51, s58\n" \
  "s_cselect_b32 s51, s59, s51\n" \
  "s_add_u32 s52, s51, s55\n" \
  "s_cmp_eq_u32 s52, s56\n" \
  "s_cselect_b32 s52, s57, s52\n" \
  "s_cmp_eq_u32 s52, s58\n" \
  "s_cselect_b32 s52, s59, s52\n" \
  "s_mul_i32 s88, s54, 7168\n" \
  "v_add_u32 v2, 57344, v1\n" \
  "s_lshl_b32 s78, s54, 8\n" \
  "s_add_u32 s78, s78, 159744\n" \
  "s_mov_b32 s66, s59\n" \
  "s_branch Lg4_slow0_%=\n" \
  "Lg4_d0_%=:\n" \
  "v_mfma_f32_16x16x32_bf16 v[12:15], a[0:3], v[28:31], 0\n" \
  "s_add_u32 m0, s88, 57344\n" \
  "ds_read_b128 v[56:59], v1 offset:7168\n" \
  "v_lshlrev_b32 v60, 16, v72\n" \
  "v_mfma_f32_16x16x32_bf16 v[16:19], a[4:7], v[28:31], 0\n" \
  "buffer_load_dwordx4 v[68:71], v9, s[36:39], s48 offen offset:0 sc1\n" \
  "v_mfma_f32_16x16x32_bf16 v[12:15], a[8:11], v[32:35], v[12:15]\n" \
  "s_lshr_b32 s73, s47, 0\n" \
  "ds_read_b128 v[28:31], v1 offset:8192\n" \
  "v_and_b32 v61, 0xffff0000, v72\n" \
  "v_mfma_f32_16x16x32_bf16 v[16:19], a[12:15], v[32:35], v[16:19]\n" \
  "s_waitcnt lgkmcnt(5)\n" \
  "buffer_load_dwordx4 v0, s[36:39], s61 offen offset:0 sc1 lds\n" \
  "v_mfma_f32_16x16x32_bf16 v[12:15], a[16:19], v[36:39], v[12:15]\n" \
  "s_mul_i32 s68, s50, 5\n" \
  "ds_read_b128 v[32:35], v1 offset:9216\n" \
  "v_lshlrev_b32 v62, 16, v73\n" \
  "v_mfma_f32_16x16x32_bf16 v[16:19], a[20:23], v[36:39], v[16:19]\n" \
  "v_and_b32 v63, 0xffff0000, v73\n" \
  "v_lshlrev_b32 v64, 16, v74\n" \
  "v_mfma_f32_16x16x32_bf16 v[12:15], a[24:27], v[40:43], v[12:15]\n" \
  "ds_read_b128 v[36:39], v1 offset:10240\n" \
  "v_and_b32 v65, 0xffff0000, v74\n" \
  "v_mfma_f32_16x16x32_bf16 v[16:19], a[28:31], v[40:43], v[16:19]\n" \
  "s_waitcnt lgkmcnt(5)\n" \
  "buffer_load_dwordx4 v0, s[36:39], s61 offen offset:1024 sc1 lds\n" \
  "v_mfma_f32_16x16x32_bf16 v[12:15], a[32:35], v[44:47], v[12:15]\n" \
  "ds_read_b128 v[40:43], v1 offset:11264\n" \
  "v_lshlrev_b32 v66, 16, v75\n" \
  "v_mfma_f32_16x16x32_bf16 v[16:19], a[36:39], v[44:47], v[16:19]\n" \
  "v_and_b32 v67, 0xffff0000, v75\n" \
  "v_add_f32 v20, v20, v60\n" \
  "v_mfma_f32_16x16x32_bf16 v[12:15], a[40:43], v[48:51], v[12:15]\n" \
  "ds_read_b128 v[44:47], v1 offset:12288\n" \
  "v_add_f32 v21, v21, v61\n" \
  "v_mfma_f32_16x16x32_bf16 v[16:19], a[44:47], v[48:51], v[16:19]\n" \
  "s_waitcnt lgkmcnt(5)\n" \
  "buffer_load_dwordx4 v0, s[36:39], s61 offen offset:2048 sc1 lds\n" \
  "v_mfma_f32_16x16x32_bf16 v[12:15], a[48:51], v[52:55], v[12:15]\n" \
  "ds_read_b128 v[48:51], v1 offset:13312\n" \
  "v_add_f32 v22, v22, v62\n" \
  "v_mfma_f32_16x16x32_bf16 v[16:19], a[52:55], v[52:55], v[16:19]\n" \
  "v_add_f32 v23, v23, v63\n" \
  "v_add_f32 v24, v24, v64\n" \
  "v_mfma_f32_16x16x32_bf16 v[12:15], a[56:59], v[56:59], v[12:15]\n" \
  "ds_read_b128 v[52:55], v1 offset:14336\n" \
  "v_add_f32 v25, v25, v65\n" \
  "v_mfma_f32_16x16x32_bf16 v[16:19], a[60:63], v[56:59], v[16:19]\n" \
  "s_waitcnt lgkmcnt(5)\n" \
  "buffer_load_dwordx4 v0, s[36:39], s61 offen offset:3072 sc1 lds\n" \
  "v_mfma_f32_16x16x32_bf16 v[12:15], a[64:67], v[28:31], v[12:15]\n" \
  "s_add_u32 s61, s61, 4096\n" \
  "ds_read_b128 v[56:59], v1 offset:15360\n" \
  "v_add_f32 v26, v26, v66\n" \
  "v_mfma_f32_16x16x32_bf16 v[16:19], a[68:71], v[28:31], v[16:19]\n" \
  "s_add_u32 m0, s88, 61440\n" \
  "v_add_f32 v27, v27, v67\n" \
  "v_cvt_pk_bf16_f32 v60, v20, v21\n" \
  "v_mfma_f32_16x16x32_bf16 v[12:15], a[72:75], v[32:35], v[12:15]\n" \
  "s_add_u32 s53, s52, s55\n" \
  "ds_read_b128 v[28:31], v1 offset:16384\n" \
  "v_cvt_pk_bf16_f32 v61, v22, v23\n" \
  "v_mfma_f32_16x16x32_bf16 v[16:19], a[76:79], v[32:35], v[16:19]\n" \
  "s_waitcnt lgkmcnt(5)\n" \
  "v_cvt_pk_bf16_f32 v62, v24, v25\n" \
  "v_cvt_pk_bf16_f32 v63, v26, v27\n" \
  "v_mfma_f32_16x16x32_bf16 v[12:15], a[80:83], v[36:39], v[12:15]\n" \
  "s_cmp_eq_u32 s53, s56\n" \
  "ds_read_b128 v[32:35], v1 offset:17408\n" \
  "s_cselect_b32 s53, s57, s53\n" \
  "v_mfma_f32_16x16x32_bf16 v[16:19], a[84:87], v[36:39], v[16:19]\n" \
  "buffer_store_dwordx4 v[60:63], v3, s[36:39], s73 offen offset:0 sc0 sc1\n" \
  "buffer_load_dwordx4 v0, s[36:39], s61 offen offset:0 sc1 lds\n" \
  "v_mfma_f32_16x16x32_bf16 v[12:15], a[88:91], v[40:43], v[12:15]\n" \
  "s_cmp_eq_u32 s53, s58\n" \
  "ds_read_b128 v[36:39], v1 offset:18432\n" \
  "s_cselect_b32 s53, s59, s53\n" \
  "v_mfma_f32_16x16x32_bf16 v[16:19], a[92:95], v[40:43], v[16:19]\n" \
  "s_waitcnt lgkmcnt(5)\n" \
  "s_lshr_b32 s68, s68, 6\n" \
  "s_lshr_b32 s70, s47, 4\n" \
  "v_mfma_f32_16x16x32_bf16 v[12:15], a[96:99], v[44:47], v[12:15]\n" \
  "ds_read_b128 v[40:43], v1 offset:19456\n" \
  "v_mfma_f32_16x16x32_bf16 v[16:19], a[100:103], v[44:47], v[16:19]\n" \
  "buffer_load_dwordx4 v0, s[36:39], s61 offen offset:1024 sc1 lds\n" \
  "v_mfma_f32_16x16x32_bf16 v[12:15], a[104:107], v[48:51], v[12:15]\n" \
  "ds_read_b128 v[44:47], v1 offset:20480\n" \
  "v_mfma_f32_16x16x32_bf16 v[16:19], a[108:111], v[48:51], v[16:19]\n" \
  "s_waitcnt lgkmcnt(5)\n" \
  "v_mfma_f32_16x16x32_bf16 v[12:15], a[112:115], v[52:55], v[12:15]\n" \
  "ds_read_b128 v[48:51], v1 offset:21504\n" \
  "v_mfma_f32_16x16x32_bf16 v[16:19], a[116:119], v[52:55], v[16:19]\n" \
  "buffer_load_dwordx4 v0, s[36:39], s61 offen offset:2048 sc1 lds\n" \
  "v_mfma_f32_16x16x32_bf16 v[12:15], a[120:123], v[56:59], v[12:15]\n" \
  "s_add_u32 m0, s78, 3072\n" \
  "ds_read_b128 v[52:55], v1 offset:22528\n" \
  "s_add_u32 s61, s61, 24576\n" \
  "v_mfma_f32_16x16x32_bf16 v[16:19], a[124:127], v[56:59], v[16:19]\n" \
  "s_waitcnt lgkmcnt(5)\n" \
  "v_mfma_f32_16x16x32_bf16 v[12:15], a[128:131], v[28:31], v[12:15]\n" \
  "ds_read_b128 v[56:59], v1 offset:23552\n" \
  "v_mfma_f32_16x16x32_bf16 v[16:19], a[132:135], v[28:31], v[16:19]\n" \
  "v_mfma_f32_16x16x32_bf16 v[12:15], a[136:139], v[32:35], v[12:15]\n" \
  "ds_read_b128 v[28:31], v1 offset:24576\n" \
  "v_mfma_f32_16x16x32_bf16 v[16:19], a[140:143], v[32:35], v[16:19]\n" \
  "s_waitcnt lgkmcnt(5)\n" \
  "v_mfma_f32_16x16x32_bf16 v[12:15], a[144:147], v[36:39], v[12:15]\n" \
  "ds_read_b128 v[32:35], v1 offset:25600\n" \
  "v_mfma_f32_16x16x32_bf16 v[16:19], a[148:151], v[36:39], v[16:19]\n" \
  "v_mfma_f32_16x16x32_bf16 v[12:15], a[152:155], v[40:43], v[12:15]\n" \
  "ds_read_b128 v[36:39], v1 offset:26624\n" \
  "v_mfma_f32_16x16x32_bf16 v[16:19], a[156:159], v[40:43], v[16:19]\n" \
  "s_waitcnt lgkmcnt(5)\n" \
  "v_mfma_f32_16x16x32_bf16 v[12:15], a[160:163], v[44:47], v[12:15]\n" \
  "s_waitcnt vmcnt(10)\n" \
  "ds_read_b128 v[40:43], v1 offset:27648\n" \
  "v_mfma_f32_16x16x32_bf16 v[16:19], a[164:167], v[44:47], v[16:19]\n" \
  "s_barrier\n" \
  "v_mfma_f32_16x16x32_bf16 v[12:15], a[168:171], v[48:51], v[12:15]\n" \
  "ds_read_b128 v[44:47], v1 offset:28672\n" \
  "v_mfma_f32_16x16x32_bf16 v[16:19], a[172:175], v[48:51], v[16:19]\n" \
  "s_waitcnt lgkmcnt(5)\n" \
  "buffer_load_dword v6, s[36:39], s68 offen offset:0 sc1 lds\n" \
  "v_mfma_f32_16x16x32_bf16 v[12:15], a[176:179], v[52:55], v[12:15]\n" \
  "s_add_u32 m0, s88, 86016\n" \
  "ds_read_b128 v[48:51], v1 offset:29696\n" \
  "v_mfma_f32_16x16x32_bf16 v[16:19], a[180:183], v[52:55], v[16:19]\n" \
  "buffer_load_dwordx4 v0, s[36:39], s61 offen offset:0 sc1 lds\n" \
  "v_mfma_f32_16x16x32_bf16 v[12:15], a[184:187], v[56:59], v[12:15]\n" \
  "ds_read_b128 v[52:55], v1 offset:30720\n" \
  "v_mfma_f32_16x16x32_bf16 v[16:19], a[188:191], v[56:59], v[16:19]\n" \
  "s_waitcnt lgkmcnt(5)\n" \
  "v_mfma_f32_16x16x32_bf16 v[12:15], a[192:195], v[28:31], v[12:15]\n" \
  "ds_read_b128 v[56:59], v1 offset:31744\n" \
  "v_mfma_f32_16x16x32_bf16 v[16:19], a[196:199], v[28:31], v[16:19]\n" \
  "buffer_load_dwordx4 v0, s[36:39], s61 offen offset:1024 sc1 lds\n" \
  "v_mfma_f32_16x16x32_bf16 v[12:15], a[200:203], v[32:35], v[12:15]\n" \
  "ds_read_b128 v[28:31], v1 offset:32768\n" \
  "v_mfma_f32_16x16x32_bf16 v[16:19], a[204:207], v[32:35], v[16:19]\n" \
  "s_waitcnt lgkmcnt(5)\n" \
  "v_mfma_f32_16x16x32_bf16 v[12:15], a[208:211], v[36:39], v[12:15]\n" \
  "ds_read_b128 v[32:35], v1 offset:33792\n" \
  "v_mfma_f32_16x16x32_bf16 v[16:19], a[212:215], v[36:39], v[16:19]\n" \
  "buffer_load_dwordx4 v0, s[36:39], s61 offen offset:2048 sc1 lds\n" \
  "v_mfma_f32_16x16x32_bf16 v[12:15], a[216:219], v[40:43], v[12:15]\n" \
  "ds_read_b128 v[36:39], v1 offset:34816\n" \
  "v_mfma_f32_16x16x32_bf16 v[16:19], a[220:223], v[40:43], v[16:19]\n" \
  "s_waitcnt lgkmcnt(5)\n" \
  "v_mfma_f32_16x16x32_bf16 v[12:15], a[224:227], v[44:47], v[12:15]\n" \
  "ds_read_b128 v[40:43], v1 offset:35840\n" \
  "v_mfma_f32_16x16x32_bf16 v[16:19], a[228:231], v[44:47], v[16:19]\n" \
  "buffer_load_dwordx4 v0, s[36:39], s61 offen offset:3072 sc1 lds\n" \
  "v_mfma_f32_16x16x32_bf16 v[12:15], a[232:235], v[48:51], v[12:15]\n" \
  "s_add_u32 s61, s61, 4096\n" \
  "ds_read_b128 v[44:47], v1 offset:36864\n" \
  "s_add_u32 m0, s88, 90112\n" \
  "v_mfma_f32_16x16x32_bf16 v[16:19], a[236:239], v[48:51], v[16:19]\n" \
  "s_waitcnt lgkmcnt(5)\n" \
  "v_mfma_f32_16x16x32_bf16 v[12:15], a[240:243], v[52:55], v[12:15]\n" \
  "ds_read_b128 v[48:51], v1 offset:37888\n" \
  "v_mfma_f32_16x16x32_bf16 v[16:19], a[244:247], v[52:55], v[16:19]\n" \
  "buffer_load_dwordx4 v0, s[36:39], s61 offen offset:0 sc1 lds\n" \
  "v_mfma_f32_16x16x32_bf16 v[12:15], a[248:251], v[56:59], v[12:15]\n" \
  "ds_read_b128 v[52:55], v1 offset:38912\n" \
  "v_mfma_f32_16x16x32_bf16 v[16:19], a[252:255], v[56:59], v[16:19]\n" \
  "s_waitcnt lgkmcnt(5)\n" \
  "v_mfma_f32_16x16x32_bf16 v[12:15], v[108:111], v[28:31], v[12:15]\n" \
  "ds_read_b128 v[56:59], v1 offset:39936\n" \
  "v_mfma_f32_16x16x32_bf16 v[16:19], v[112:115], v[28:31], v[16:19]\n" \
  "buffer_load_dwordx4 v0, s[36:39], s61 offen offset:1024 sc1 lds\n" \
  "v_mfma_f32_16x16x32_bf16 v[12:15], v[116:119], v[32:35], v[12:15]\n" \
  "ds_read_b128 v[28:31], v1 offset:40960\n" \
  "v_mfma_f32_16x16x32_bf16 v[16:19], v[120:123], v[32:35], v[16:19]\n" \
  "s_waitcnt lgkmcnt(5)\n" \
  "v_mfma_f32_16x16x32_bf16 v[12:15], v[124:127], v[36:39], v[12:15]\n" \
  "ds_read_b128 v[32:35], v1 offset:41984\n" \
  "v_mfma_f32_16x16x32_bf16 v[16:19], v[128:131], v[36:39], v[16:19]\n" \
  "buffer_load_dwordx4 v0, s[36:39], s61 offen offset:2048 sc1 lds\n" \
  "v_mfma_f32_16x16x32_bf16 v[12:15], v[132:135], v[40:43], v[12:15]\n" \
  "ds_read_b128 v[36:39], v1 offset:43008\n" \
  "v_mfma_f32_16x16x32_bf16 v[16:19], v[136:139], v[40:43], v[16:19]\n" \
  "s_waitcnt lgkmcnt(5)\n" \
  "v_mfma_f32_16x16x32_bf16 v[12:15], v[140:143], v[44:47], v[12:15]\n" \
  "ds_read_b128 v[40:43], v1 offset:44032\n" \
  "v_mfma_f32_16x16x32_bf16 v[16:19], v[144:147], v[44:47], v[16:19]\n" \
  "v_mfma_f32_16x16x32_bf16 v[12:15], v[148:151], v[48:51], v[12:15]\n" \
  "ds_read_b128 v[44:47], v1 offset:45056\n" \
  "v_mfma_f32_16x16x32_bf16 v[16:19], v[152:155], v[48:51], v[16:19]\n" \
  "s_waitcnt lgkmcnt(5)\n" \
  "v_mfma_f32_16x16x32_bf16 v[12:15], v[156:159], v[52:55], v[12:15]\n" \
  "ds_read_b128 v[48:51], v1 offset:46080\n" \
  "v_mfma_f32_16x16x32_bf16 v[16:19], v[160:163], v[52:55], v[16:19]\n" \
  "v_mfma_f32_16x16x32_bf16 v[12:15], v[164:167], v[56:59], v[12:15]\n" \
  "ds_read_b128 v[52:55], v1 offset:47104\n" \
  "v_mfma_f32_16x16x32_bf16 v[16:19], v[168:171], v[56:59], v[16:19]\n" \
  "s_waitcnt lgkmcnt(5)\n" \
  "v_mfma_f32_16x16x32_bf16 v[12:15], v[172:175], v[28:31], v[12:15]\n" \
  "ds_read_b128 v[56:59], v1 offset:48128\n" \
  "v_mfma_f32_16x16x32_bf16 v[16:19], v[176:179], v[28:31], v[16:19]\n" \
  "ds_read_b128 v[88:91], v10 offset:0\n" \
  "v_mfma_f32_16x16x32_bf16 v[12:15], v[180:183], v[32:35], v[12:15]\n" \
  "ds_read_b128 v[28:31], v1 offset:49152\n" \
  "v_mfma_f32_16x16x32_bf16 v[16:19], v[184:187], v[32:35], v[16:19]\n" \
  "s_waitcnt lgkmcnt(6)\n" \
  "ds_read_b128 v[92:95], v10 offset:1024\n" \
  "v_mfma_f32_16x16x32_bf16 v[12:15], v[188:191], v[36:39], v[12:15]\n" \
  "ds_read_b128 v[32:35], v1 offset:50176\n" \
  "v_mfma_f32_16x16x32_bf16 v[16:19], v[192:195], v[36:39], v[16:19]\n" \
  "ds_read_b128 v[96:99], v10 offset:2048\n" \
  "v_mfma_f32_16x16x32_bf16 v[12:15], v[196:199], v[40:43], v[12:15]\n" \
  "ds_read_b128 v[36:39], v1 offset:51200\n" \
  "v_mfma_f32_16x16x32_bf16 v[16:19], v[200:203], v[40:43], v[16:19]\n" \
  "s_waitcnt lgkmcnt(5)\n" \
  "ds_read_b128 v[76:79], v10 offset:3072\n" \
  "v_mfma_f32_16x16x32_bf16 v[12:15], v[204:207], v[44:47], v[12:15]\n" \
  "ds_read_b128 v[40:43], v1 offset:52224\n" \
  "v_mfma_f32_16x16x32_bf16 v[16:19], v[208:211], v[44:47], v[16:19]\n" \
  "ds_read_b128 v[80:83], v10 offset:4096\n" \
  "v_mfma_f32_16x16x32_bf16 v[12:15], v[212:215], v[48:51], v[12:15]\n" \
  "ds_read_b128 v[44:47], v1 offset:53248\n" \
  "v_mfma_f32_16x16x32_bf16 v[16:19], v[88:91], v[48:51], v[16:19]\n" \
  "s_waitcnt lgkmcnt(5)\n" \
  "ds_read_b128 v[84:87], v10 offset:5120\n" \
  "v_mfma_f32_16x16x32_bf16 v[12:15], v[216:219], v[52:55], v[12:15]\n" \
  "ds_read_b128 v[48:51], v1 offset:54272\n" \
  "v_mfma_f32_16x16x32_bf16 v[16:19], v[92:95], v[52:55], v[16:19]\n" \
  "ds_read_b128 v[88:91], v10 offset:6144\n" \
  "v_mfma_f32_16x16x32_bf16 v[12:15], v[220:223], v[56:59], v[12:15]\n" \
  "ds_read_b128 v[52:55], v1 offset:55296\n" \
  "v_mfma_f32_16x16x32_bf16 v[16:19], v[96:99], v[56:59], v[16:19]\n" \
  "s_waitcnt lgkmcnt(5)\n" \
  "ds_read_b128 v[92:95], v10 offset:7168\n" \
  "v_mfma_f32_16x16x32_bf16 v[12:15], v[224:227], v[28:31], v[12:15]\n" \
  "s_waitcnt vmcnt(7)\n" \
  "ds_read_b128 v[56:59], v1 offset:56320\n" \
  "ds_read_b128 v[96:99], v10 offset:8192\n" \
  "v_mfma_f32_16x16x32_bf16 v[16:19], v[76:79], v[28:31], v[16:19]\n" \
  "s_barrier\n" \
  "v_mfma_f32_16x16x32_bf16 v[12:15], v[228:231], v[32:35], v[12:15]\n" \
  "ds_read_b128 v[28:31], v2 offset:0\n" \
  "ds_read_b32 v11, v8 offset:3072\n" \
  "v_mfma_f32_16x16x32_bf16 v[16:19], v[80:83], v[32:35], v[16:19]\n" \
  "s_waitcnt lgkmcnt(6)\n" \
  "ds_read_b128 v[76:79], v10 offset:9216\n" \
  "v_mfma_f32_16x16x32_bf16 v[12:15], v[232:235], v[36:39], v[12:15]\n" \
  "ds_read_b128 v[32:35], v2 offset:1024\n" \
  "v_mfma_f32_16x16x32_bf16 v[16:19], v[84:87], v[36:39], v[16:19]\n" \
  "ds_read_b128 v[80:83], v10 offset:10240\n" \
  "v_mfma_f32_16x16x32_bf16 v[12:15], v[236:239], v[40:43], v[12:15]\n" \
  "s_waitcnt lgkmcnt(3)\n" \
  "ds_read_b128 v[36:39], v2 offset:2048\n" \
  "v_mfma_f32_16x16x32_bf16 v[16:19], v[88:91], v[40:43], v[16:19]\n" \
  "v_cmp_eq_u32 vcc, s60, v11\n" \
  "v_mfma_f32_16x16x32_bf16 v[12:15], v[240:243], v[44:47], v[12:15]\n" \
  "s_cmp_eq_u32 vcc_lo, -1\n" \
  "ds_read_b128 v[40:43], v2 offset:3072\n" \
  "s_cselect_b32 s67, s50, s59\n" \
  "v_mfma_f32_16x16x32_bf16 v[16:19], v[92:95], v[44:47], v[16:19]\n" \
  "buffer_store_dword v5, v4, s[36:39], s70 offen offset:0 sc0 sc1\n" \
  "v_mfma_f32_16x16x32_bf16 v[12:15], v[244:247], v[48:51], v[12:15]\n" \
  "s_max_u32 s67, s67, s66\n" \
  "ds_read_b128 v[44:47], v2 offset:4096\n" \
  "s_mul_i32 s61, s67, 7\n" \
  "v_mfma_f32_16x16x32_bf16 v[16:19], v[96:99], v[48:51], v[16:19]\n" \
  "s_waitcnt lgkmcnt(3)\n" \
  "s_lshr_b32 s61, s61, 1\n" \
  "s_cmp_ge_u32 s66, s59\n" \
  "v_mfma_f32_16x16x32_bf16 v[12:15], v[248:251], v[52:55], v[12:15]\n" \
  "ds_read_b128 v[48:51], v2 offset:5120\n" \
  "v_mfma_f32_16x16x32_bf16 v[16:19], v[76:79], v[52:55], v[16:19]\n" \
  "v_mfma_f32_16x16x32_bf16 v[12:15], v[252:255], v[56:59], v[12:15]\n" \
  "ds_read_b128 v[52:55], v2 offset:6144\n" \
  "v_mfma_f32_16x16x32_bf16 v[16:19], v[80:83], v[56:59], v[16:19]\n" \
  "s_cbranch_scc1 Lg4_slow1_%=\n" \
  "Lg4_d1_%=:\n" \
  "v_mfma_f32_16x16x32_bf16 v[20:23], a[0:3], v[28:31], 0\n" \
  "s_add_u32 m0, s88, 0\n" \
  "ds_read_b128 v[56:59], v2 offset:7168\n" \
  "v_lshlrev_b32 v60, 16, v68\n" \
  "v_mfma_f32_16x16x32_bf16 v[24:27], a[4:7], v[28:31], 0\n" \
  "buffer_load_dwordx4 v[72:75], v9, s[36:39], s49 offen offset:0 sc1\n" \
  "v_mfma_f32_16x16x32_bf16 v[20:23], a[8:11], v[32:35], v[20:23]\n" \
  "s_lshr_b32 s73, s48, 0\n" \
  "ds_read_b128 v[28:31], v2 offset:8192\n" \
  "v_and_b32 v61, 0xffff0000, v68\n" \
  "v_mfma_f32_16x16x32_bf16 v[24:27], a[12:15], v[32:35], v[24:27]\n" \
  "s_waitcnt lgkmcnt(5)\n" \
  "buffer_load_dwordx4 v0, s[36:39], s61 offen offset:0 sc1 lds\n" \
  "v_mfma_f32_16x16x32_bf16 v[20:23], a[16:19], v[36:39], v[20:23]\n" \
  "s_mul_i32 s68, s51, 5\n" \
  "ds_read_b128 v[32:35], v2 offset:9216\n" \
  "v_lshlrev_b32 v62, 16, v69\n" \
  "v_mfma_f32_16x16x32_bf16 v[24:27], a[20:23], v[36:39], v[24:27]\n" \
  "s_mov_b64 s[44:45], s[46:47]\n" \
  "v_and_b32 v63, 0xffff0000, v69\n" \
  "v_lshlrev_b32 v64, 16, v70\n" \
  "v_mfma_f32_16x16x32_bf16 v[20:23], a[24:27], v[40:43], v[20:23]\n" \
  "s_mov_b64 s[46:47], s[48:49]\n" \
  "ds_read_b128 v[36:39], v2 offset:10240\n" \
  "v_and_b32 v65, 0xffff0000, v70\n" \
  "v_mfma_f32_16x16x32_bf16 v[24:27], a[28:31], v[40:43], v[24:27]\n" \
  "s_waitcnt lgkmcnt(5)\n" \
  "buffer_load_dwordx4 v0, s[36:39], s61 offen offset:1024 sc1 lds\n" \
  "v_mfma_f32_16x16x32_bf16 v[20:23], a[32:35], v[44:47], v[20:23]\n" \
  "ds_read_b128 v[40:43], v2 offset:11264\n" \
  "v_lshlrev_b32 v66, 16, v71\n" \
  "v_mfma_f32_16x16x32_bf16 v[24:27], a[36:39], v[44:47], v[24:27]\n" \
  "v_and_b32 v67, 0xffff0000, v71\n" \
  "v_add_f32 v12, v12, v60\n" \
  "v_mfma_f32_16x16x32_bf16 v[20:23], a[40:43], v[48:51], v[20:23]\n" \
  "ds_read_b128 v[44:47], v2 offset:12288\n" \
  "v_add_f32 v13, v13, v61\n" \
  "v_mfma_f32_16x16x32_bf16 v[24:27], a[44:47], v[48:51], v[24:27]\n" \
  "s_waitcnt lgkmcnt(5)\n" \
  "buffer_load_dwordx4 v0, s[36:39], s61 offen offset:2048 sc1 lds\n" \
  "v_mfma_f32_16x16x32_bf16 v[20:23], a[48:51], v[52:55], v[20:23]\n" \
  "ds_read_b128 v[48:51], v2 offset:13312\n" \
  "v_add_f32 v14, v14, v62\n" \
  "v_mfma_f32_16x16x32_bf16 v[24:27], a[52:55], v[52:55], v[24:27]\n" \
  "v_add_f32 v15, v15, v63\n" \
  "v_add_f32 v16, v16, v64\n" \
  "v_mfma_f32_16x16x32_bf16 v[20:23], a[56:59], v[56:59], v[20:23]\n" \
  "ds_read_b128 v[52:55], v2 offset:14336\n" \
  "v_add_f32 v17, v17, v65\n" \
  "v_mfma_f32_16x16x32_bf16 v[24:27], a[60:63], v[56:59], v[24:27]\n" \
  "s_waitcnt lgkmcnt(5)\n" \
  "buffer_load_dwordx4 v0, s[36:39], s61 offen offset:3072 sc1 lds\n" \
  "v_mfma_f32_16x16x32_bf16 v[20:23], a[64:67], v[28:31], v[20:23]\n" \
  "s_add_u32 s61, s61, 4096\n" \
  "ds_read_b128 v[56:59], v2 offset:15360\n" \
  "v_add_f32 v18, v18, v66\n" \
  "v_mfma_f32_16x16x32_bf16 v[24:27], a[68:71], v[28:31], v[24:27]\n" \
  "s_add_u32 m0, s88, 4096\n" \
  "v_add_f32 v19, v19, v67\n" \
  "v_cvt_pk_bf16_f32 v60, v12, v13\n" \
  "v_mfma_f32_16x16x32_bf16 v[20:23], a[72:75], v[32:35], v[20:23]\n" \
  "s_add_u32 s74, s53, s55\n" \
  "ds_read_b128 v[28:31], v2 offset:16384\n" \
  "v_cvt_pk_bf16_f32 v61, v14, v15\n" \
  "v_mfma_f32_16x16x32_bf16 v[24:27], a[76:79], v[32:35], v[24:27]\n" \
  "s_waitcnt lgkmcnt(5)\n" \
  "v_cvt_pk_bf16_f32 v62, v16, v17\n" \
  "v_cvt_pk_bf16_f32 v63, v18, v19\n" \
  "v_mfma_f32_16x16x32_bf16 v[20:23], a[80:83], v[36:39], v[20:23]\n" \
  "s_cmp_eq_u32 s74, s56\n" \
  "ds_read_b128 v[32:35], v2 offset:17408\n" \
  "s_cselect_b32 s74, s57, s74\n" \
  "v_mfma_f32_16x16x32_bf16 v[24:27], a[84:87], v[36:39], v[24:27]\n" \
  "buffer_store_dwordx4 v[60:63], v3, s[36:39], s73 offen offset:0 sc0 sc1\n" \
  "buffer_load_dwordx4 v0, s[36:39], s61 offen offset:0 sc1 lds\n" \
  "v_mfma_f32_16x16x32_bf16 v[20:23], a[88:91], v[40:43], v[20:23]\n" \
  "s_cmp_eq_u32 s74, s58\n" \
  "ds_read_b128 v[36:39], v2 offset:18432\n" \
  "s_cselect_b32 s74, s59, s74\n" \
  "v_mfma_f32_16x16x32_bf16 v[24:27], a[92:95], v[40:43], v[24:27]\n" \
  "s_waitcnt lgkmcnt(5)\n" \
  "s_lshr_b32 s68, s68, 6\n" \
  "s_lshr_b32 s70, s48, 4\n" \
  "v_mfma_f32_16x16x32_bf16 v[20:23], a[96:99], v[44:47], v[20:23]\n" \
  "s_mov_b64 s[48:49], s[50:51]\n" \
  "ds_read_b128 v[40:43], v2 offset:19456\n" \
  "v_mfma_f32_16x16x32_bf16 v[24:27], a[100:103], v[44:47], v[24:27]\n" \
  "buffer_load_dwordx4 v0, s[36:39], s61 offen offset:1024 sc1 lds\n" \
  "v_mfma_f32_16x16x32_bf16 v[20:23], a[104:107], v[48:51], v[20:23]\n" \
  "ds_read_b128 v[44:47], v2 offset:20480\n" \
  "v_mfma_f32_16x16x32_bf16 v[24:27], a[108:111], v[48:51], v[24:27]\n" \
  "s_waitcnt lgkmcnt(5)\n" \
  "v_mfma_f32_16x16x32_bf16 v[20:23], a[112:115], v[52:55], v[20:23]\n" \
  "ds_read_b128 v[48:51], v2 offset:21504\n" \
  "v_mfma_f32_16x16x32_bf16 v[24:27], a[116:119], v[52:55], v[24:27]\n" \
  "buffer_load_dwordx4 v0, s[36:39], s61 offen offset:2048 sc1 lds\n" \
  "v_mfma_f32_16x16x32_bf16 v[20:23], a[120:123], v[56:59], v[20:23]\n" \
  "s_add_u32 m0, s78, 3072\n" \
  "ds_read_b128 v[52:55], v2 offset:22528\n" \
  "s_add_u32 s61, s61, 24576\n" \
  "v_mfma_f32_16x16x32_bf16 v[24:27], a[124:127], v[56:59], v[24:27]\n" \
  "s_waitcnt lgkmcnt(5)\n" \
  "v_mfma_f32_16x16x32_bf16 v[20:23], a[128:131], v[28:31], v[20:23]\n" \
  "ds_read_b128 v[56:59], v2 offset:23552\n" \
  "v_mfma_f32_16x16x32_bf16 v[24:27], a[132:135], v[28:31], v[24:27]\n" \
  "v_mfma_f32_16x16x32_bf16 v[20:23], a[136:139], v[32:35], v[20:23]\n" \
  "ds_read_b128 v[28:31], v2 offset:24576\n" \
  "v_mfma_f32_16x16x32_bf16 v[24:27], a[140:143], v[32:35], v[24:27]\n" \
  "s_waitcnt lgkmcnt(5)\n" \
  "v_mfma_f32_16x16x32_bf16 v[20:23], a[144:147], v[36:39], v[20:23]\n" \
  "ds_read_b128 v[32:35], v2 offset:25600\n" \
  "v_mfma_f32_16x16x32_bf16 v[24:27], a[148:151], v[36:39], v[24:27]\n" \
  "v_mfma_f32_16x16x32_bf16 v[20:23], a[152:155], v[40:43], v[20:23]\n" \
  "ds_read_b128 v[36:39], v2 offset:26624\n" \
  "v_mfma_f32_16x16x32_bf16 v[24:27], a[156:159], v[40:43], v[24:27]\n" \
  "s_waitcnt lgkmcnt(5)\n" \
  "v_mfma_f32_16x16x32_bf16 v[20:23], a[160:163], v[44:47], v[20:23]\n" \
  "s_waitcnt vmcnt(10)\n" \
  "ds_read_b128 v[40:43], v2 offset:27648\n" \
  "v_mfma_f32_16x16x32_bf16 v[24:27], a[164:167], v[44:47], v[24:27]\n" \
  "s_barrier\n" \
  "v_mfma_f32_16x16x32_bf16 v[20:23], a[168:171], v[48:51], v[20:23]\n" \
  "ds_read_b128 v[44:47], v2 offset:28672\n" \
  "v_mfma_f32_16x16x32_bf16 v[24:27], a[172:175], v[48:51], v[24:27]\n" \
  "s_waitcnt lgkmcnt(5)\n" \
  "buffer_load_dword v6, s[36:39], s68 offen offset:0 sc1 lds\n" \
  "v_mfma_f32_16x16x32_bf16 v[20:23], a[176:179], v[52:55], v[20:23]\n" \
  "s_add_u32 m0, s88, 28672\n" \
  "ds_read_b128 v[48:51], v2 offset:29696\n" \
  "v_mfma_f32_16x16x32_bf16 v[24:27], a[180:183], v[52:55], v[24:27]\n" \
  "buffer_load_dwordx4 v0, s[36:39], s61 offen offset:0 sc1 lds\n" \
  "v_mfma_f32_16x16x32_bf16 v[20:23], a[184:187], v[56:59], v[20:23]\n" \
  "ds_read_b128 v[52:55], v2 offset:30720\n" \
  "v_mfma_f32_16x16x32_bf16 v[24:27], a[188:191], v[56:59], v[24:27]\n" \
  "s_waitcnt lgkmcnt(5)\n" \
  "v_mfma_f32_16x16x32_bf16 v[20:23], a[192:195], v[28:31], v[20:23]\n" \
  "ds_read_b128 v[56:59], v2 offset:31744\n" \
  "v_mfma_f32_16x16x32_bf16 v[24:27], a[196:199], v[28:31], v[24:27]\n" \
  "buffer_load_dwordx4 v0, s[36:39], s61 offen offset:1024 sc1 lds\n" \
  "v_mfma_f32_16x16x32_bf16 v[20:23], a[200:203], v[32:35], v[20:23]\n" \
  "ds_read_b128 v[28:31], v2 offset:32768\n" \
  "v_mfma_f32_16x16x32_bf16 v[24:27], a[204:207], v[32:35], v[24:27]\n" \
  "s_waitcnt lgkmcnt(5)\n" \
  "v_mfma_f32_16x16x32_bf16 v[20:23], a[208:211], v[36:39], v[20:23]\n" \
  "ds_read_b128 v[32:35], v2 offset:33792\n" \
  "v_mfma_f32_16x16x32_bf16 v[24:27], a[212:215], v[36:39], v[24:27]\n" \
  "buffer_load_dwordx4 v0, s[36:39], s61 offen offset:2048 sc1 lds\n" \
  "v_mfma_f32_16x16x32_bf16 v[20:23], a[216:219], v[40:43], v[20:23]\n" \
  "ds_read_b128 v[36:39], v2 offset:34816\n" \
  "v_mfma_f32_16x16x32_bf16 v[24:27], a[220:223], v[40:43], v[24:27]\n" \
  "s_waitcnt lgkmcnt(5)\n" \
  "v_mfma_f32_16x16x32_bf16 v[20:23], a[224:227], v[44:47], v[20:23]\n" \
  "ds_read_b128 v[40:43], v2 offset:35840\n" \
  "v_mfma_f32_16x16x32_bf16 v[24:27], a[228:231], v[44:47], v[24:27]\n" \
  "buffer_load_dwordx4 v0, s[36:39], s61 offen offset:3072 sc1 lds\n" \
  "v_mfma_f32_16x16x32_bf16 v[20:23], a[232:235], v[48:51], v[20:23]\n" \
  "s_add_u32 s61, s61, 4096\n" \
  "ds_read_b128 v[44:47], v2 offset:36864\n" \
  "s_add_u32 m0, s88, 32768\n" \
  "v_mfma_f32_16x16x32_bf16 v[24:27], a[236:239], v[48:51], v[24:27]\n" \
  "s_waitcnt lgkmcnt(5)\n" \
  "v_mfma_f32_16x16x32_bf16 v[20:23], a[240:243], v[52:55], v[20:23]\n" \
  "ds_read_b128 v[48:51], v2 offset:37888\n" \
  "v_mfma_f32_16x16x32_bf16 v[24:27], a[244:247], v[52:55], v[24:27]\n" \
  "buffer_load_dwordx4 v0, s[36:39], s61 offen offset:0 sc1 lds\n" \
  "v_mfma_f32_16x16x32_bf16 v[20:23], a[248:251], v[56:59], v[20:23]\n" \
  "ds_read_b128 v[52:55], v2 offset:38912\n" \
  "v_mfma_f32_16x16x32_bf16 v[24:27], a[252:255], v[56:59], v[24:27]\n" \
  "s_waitcnt lgkmcnt(5)\n" \
  "v_mfma_f32_16x16x32_bf16 v[20:23], v[108:111], v[28:31], v[20:23]\n" \
  "ds_read_b128 v[56:59], v2 offset:39936\n" \
  "v_mfma_f32_16x16x32_bf16 v[24:27], v[112:115], v[28:31], v[24:27]\n" \
  "buffer_load_dwordx4 v0, s[36:39], s61 offen offset:1024 sc1 lds\n" \
  "v_mfma_f32_16x16x32_bf16 v[20:23], v[116:119], v[32:35], v[20:23]\n" \
  "ds_read_b128 v[28:31], v2 offset:40960\n" \
  "v_mfma_f32_16x16x32_bf16 v[24:27], v[120:123], v[32:35], v[24:27]\n" \
  "s_waitcnt lgkmcnt(5)\n" \
  "v_mfma_f32_16x16x32_bf16 v[20:23], v[124:127], v[36:39], v[20:23]\n" \
  "ds_read_b128 v[32:35], v2 offset:41984\n" \
  "v_mfma_f32_16x16x32_bf16 v[24:27], v[128:131], v[36:39], v[24:27]\n" \
  "buffer_load_dwordx4 v0, s[36:39], s61 offen offset:2048 sc1 lds\n" \
  "v_mfma_f32_16x16x32_bf16 v[20:23], v[132:135], v[40:43], v[20:23]\n" \
  "ds_read_b128 v[36:39], v2 offset:43008\n" \
  "v_mfma_f32_16x16x32_bf16 v[24:27], v[136:139], v[40:43], v[24:27]\n" \
  "s_waitcnt lgkmcnt(5)\n" \
  "v_mfma_f32_16x16x32_bf16 v[20:23], v[140:143], v[44:47], v[20:23]\n" \
  "ds_read_b128 v[40:43], v2 offset:44032\n" \
  "v_mfma_f32_16x16x32_bf16 v[24:27], v[144:147], v[44:47], v[24:27]\n" \
  "v_mfma_f32_16x16x32_bf16 v[20:23], v[148:151], v[48:51], v[20:23]\n" \
  "ds_read_b128 v[44:47], v2 offset:45056\n" \
  "v_mfma_f32_16x16x32_bf16 v[24:27], v[152:155], v[48:51], v[24:27]\n" \
  "s_waitcnt lgkmcnt(5)\n" \
  "v_mfma_f32_16x16x32_bf16 v[20:23], v[156:159], v[52:55], v[20:23]\n" \
  "ds_read_b128 v[48:51], v2 offset:46080\n" \
  "v_mfma_f32_16x16x32_bf16 v[24:27], v[160:163], v[52:55], v[24:27]\n" \
  "v_mfma_f32_16x16x32_bf16 v[20:23], v[164:167], v[56:59], v[20:23]\n" \
  "ds_read_b128 v[52:55], v2 offset:47104\n" \
  "v_mfma_f32_16x16x32_bf16 v[24:27], v[168:171], v[56:59], v[24:27]\n" \
  "s_waitcnt lgkmcnt(5)\n" \
  "v_mfma_f32_16x16x32_bf16 v[20:23], v[172:175], v[28:31], v[20:23]\n" \
  "ds_read_b128 v[56:59], v2 offset:48128\n" \
  "v_mfma_f32_16x16x32_bf16 v[24:27], v[176:179], v[28:31], v[24:27]\n" \
  "ds_read_b128 v[88:91], v10 offset:0\n" \
  "v_mfma_f32_16x16x32_bf16 v[20:23], v[180:183], v[32:35], v[20:23]\n" \
  "ds_read_b128 v[28:31], v2 offset:49152\n" \
  "v_mfma_f32_16x16x32_bf16 v[24:27], v[184:187], v[32:35], v[24:27]\n" \
  "s_waitcnt lgkmcnt(6)\n" \
  "ds_read_b128 v[92:95], v10 offset:1024\n" \
  "v_mfma_f32_16x16x32_bf16 v[20:23], v[188:191], v[36:39], v[20:23]\n" \
  "ds_read_b128 v[32:35], v2 offset:50176\n" \
  "v_mfma_f32_16x16x32_bf16 v[24:27], v[192:195], v[36:39], v[24:27]\n" \
  "ds_read_b128 v[96:99], v10 offset:2048\n" \
  "v_mfma_f32_16x16x32_bf16 v[20:23], v[196:199], v[40:43], v[20:23]\n" \
  "ds_read_b128 v[36:39], v2 offset:51200\n" \
  "v_mfma_f32_16x16x32_bf16 v[24:27], v[200:203], v[40:43], v[24:27]\n" \
  "s_waitcnt lgkmcnt(5)\n" \
  "ds_read_b128 v[76:79], v10 offset:3072\n" \
  "v_mfma_f32_16x16x32_bf16 v[20:23], v[204:207], v[44:47], v[20:23]\n" \
  "ds_read_b128 v[40:43], v2 offset:52224\n" \
  "v_mfma_f32_16x16x32_bf16 v[24:27], v[208:211], v[44:47], v[24:27]\n" \
  "ds_read_b128 v[80:83], v10 offset:4096\n" \
  "v_mfma_f32_16x16x32_bf16 v[20:23], v[212:215], v[48:51], v[20:23]\n" \
  "ds_read_b128 v[44:47], v2 offset:53248\n" \
  "v_mfma_f32_16x16x32_bf16 v[24:27], v[88:91], v[48:51], v[24:27]\n" \
  "s_waitcnt lgkmcnt(5)\n" \
  "ds_read_b128 v[84:87], v10 offset:5120\n" \
  "v_mfma_f32_16x16x32_bf16 v[20:23], v[216:219], v[52:55], v[20:23]\n" \
  "ds_read_b128 v[48:51], v2 offset:54272\n" \
  "v_mfma_f32_16x16x32_bf16 v[24:27], v[92:95], v[52:55], v[24:27]\n" \
  "ds_read_b128 v[88:91], v10 offset:6144\n" \
  "v_mfma_f32_16x16x32_bf16 v[20:23], v[220:223], v[56:59], v[20:23]\n" \
  "ds_read_b128 v[52:55], v2 offset:55296\n" \
  "v_mfma_f32_16x16x32_bf16 v[24:27], v[96:99], v[56:59], v[24:27]\n" \
  "s_waitcnt lgkmcnt(5)\n" \
  "ds_read_b128 v[92:95], v10 offset:7168\n" \
  "v_mfma_f32_16x16x32_bf16 v[20:23], v[224:227], v[28:31], v[20:23]\n" \
  "s_waitcnt vmcnt(7)\n" \
  "ds_read_b128 v[56:59], v2 offset:56320\n" \
  "ds_read_b128 v[96:99], v10 offset:8192\n" \
  "v_mfma_f32_16x16x32_bf16 v[24:27], v[76:79], v[28:31], v[24:27]\n" \
  "s_barrier\n" \
  "v_mfma_f32_16x16x32_bf16 v[20:23], v[228:231], v[32:35], v[20:23]\n" \
  "ds_read_b128 v[28:31], v1 offset:0\n" \
  "ds_read_b32 v11, v8 offset:3072\n" \
  "v_mfma_f32_16x16x32_bf16 v[24:27], v[80:83], v[32:35], v[24:27]\n" \
  "s_waitcnt lgkmcnt(6)\n" \
  "ds_read_b128 v[76:79], v10 offset:9216\n" \
  "v_mfma_f32_16x16x32_bf16 v[20:23], v[232:235], v[36:39], v[20:23]\n" \
  "ds_read_b128 v[32:35], v1 offset:1024\n" \
  "v_mfma_f32_16x16x32_bf16 v[24:27], v[84:87], v[36:39], v[24:27]\n" \
  "ds_read_b128 v[80:83], v10 offset:10240\n" \
  "v_mfma_f32_16x16x32_bf16 v[20:23], v[236:239], v[40:43], v[20:23]\n" \
  "s_waitcnt lgkmcnt(3)\n" \
  "ds_read_b128 v[36:39], v1 offset:2048\n" \
  "v_mfma_f32_16x16x32_bf16 v[24:27], v[88:91], v[40:43], v[24:27]\n" \
  "v_cmp_eq_u32 vcc, s60, v11\n" \
  "v_mfma_f32_16x16x32_bf16 v[20:23], v[240:243], v[44:47], v[20:23]\n" \
  "s_cmp_eq_u32 vcc_lo, -1\n" \
  "ds_read_b128 v[40:43], v1 offset:3072\n" \
  "s_cselect_b32 s66, s51, s59\n" \
  "v_mfma_f32_16x16x32_bf16 v[24:27], v[92:95], v[44:47], v[24:27]\n" \
  "buffer_store_dword v5, v4, s[36:39], s70 offen offset:0 sc0 sc1\n" \
  "v_mfma_f32_16x16x32_bf16 v[20:23], v[244:247], v[48:51], v[20:23]\n" \
  "s_max_u32 s66, s66, s67\n" \
  "ds_read_b128 v[44:47], v1 offset:4096\n" \
  "s_mul_i32 s61, s66, 7\n" \
  "v_mfma_f32_16x16x32_bf16 v[24:27], v[96:99], v[48:51], v[24:27]\n" \
  "s_waitcnt lgkmcnt(3)\n" \
  "s_lshr_b32 s61, s61, 1\n" \
  "s_mov_b64 s[50:51], s[52:53]\n" \
  "v_mfma_f32_16x16x32_bf16 v[20:23], v[248:251], v[52:55], v[20:23]\n" \
  "s_mov_b32 s52, s74\n" \
  "ds_read_b128 v[48:51], v1 offset:5120\n" \
  "s_cmp_lt_u32 s67, s59\n" \
  "v_mfma_f32_16x16x32_bf16 v[24:27], v[76:79], v[52:55], v[24:27]\n" \
  "v_mfma_f32_16x16x32_bf16 v[20:23], v[252:255], v[56:59], v[20:23]\n" \
  "ds_read_b128 v[52:55], v1 offset:6144\n" \
  "v_mfma_f32_16x16x32_bf16 v[24:27], v[80:83], v[56:59], v[24:27]\n" \
  "s_cbranch_scc1 Lg4_d0_%=\n" \
  "Lg4_slow0_%=:\n" \
  "s_cmp_eq_u32 s54, 1\n" \
  "s_cselect_b32 s68, s49, s48\n" \
  "s_mul_i32 s68, s68, 5\n" \
  "s_lshr_b32 s68, s68, 6\n" \
  "s_add_u32 m0, s78, 0\n" \
  "s_nop 0\n" \
  "buffer_load_dword v7, s[36:39], s68 offen offset:0 sc1 lds\n" \
  "s_lshr_b32 s73, s47, 0\n" \
  "v_lshlrev_b32 v60, 16, v72\n" \
  "v_and_b32 v61, 0xffff0000, v72\n" \
  "v_lshlrev_b32 v62, 16, v73\n" \
  "v_and_b32 v63, 0xffff0000, v73\n" \
  "v_lshlrev_b32 v64, 16, v74\n" \
  "v_and_b32 v65, 0xffff0000, v74\n" \
  "v_lshlrev_b32 v66, 16, v75\n" \
  "v_and_b32 v67, 0xffff0000, v75\n" \
  "v_add_f32 v20, v20, v60\n" \
  "v_add_f32 v21, v21, v61\n" \
  "s_add_u32 m0, s78, 1024\n" \
  "s_nop 0\n" \
  "buffer_load_dword v7, s[36:39], s68 offen offset:0 sc1 lds\n" \
  "v_add_f32 v22, v22, v62\n" \
  "v_add_f32 v23, v23, v63\n" \
  "v_add_f32 v24, v24, v64\n" \
  "v_add_f32 v25, v25, v65\n" \
  "v_add_f32 v26, v26, v66\n" \
  "v_add_f32 v27, v27, v67\n" \
  "v_cvt_pk_bf16_f32 v60, v20, v21\n" \
  "v_cvt_pk_bf16_f32 v61, v22, v23\n" \
  "v_cvt_pk_bf16_f32 v62, v24, v25\n" \
  "v_cvt_pk_bf16_f32 v63, v26, v27\n" \
  "buffer_store_dwordx4 v[60:63], v3, s[36:39], s73 offen offset:0 sc0 sc1\n" \
  "s_cmp_ge_u32 s48, s59\n" \
  "s_cbranch_scc1 Lg4_done0_%=\n" \
  "s_add_u32 m0, s78, 2048\n" \
  "s_nop 0\n" \
  "buffer_load_dword v7, s[36:39], s68 offen offset:0 sc1 lds\n" \
  "s_waitcnt vmcnt(3)\n" \
  "s_barrier\n" \
  "ds_read_b32 v11, v8 offset:0\n" \
  "s_mov_b32 s69, 2304\n" \
  "s_waitcnt lgkmcnt(0)\n" \
  "v_cmp_eq_u32 vcc, s60, v11\n" \
  "s_nop 0\n" \
  "s_cmp_eq_u32 vcc_lo, -1\n" \
  "s_cbranch_scc1 Lg4_go0_%=\n" \
  "s_add_u32 m0, s78, 3072\n" \
  "s_nop 0\n" \
  "buffer_load_dword v7, s[36:39], s68 offen offset:0 sc1 lds\n" \
  "s_waitcnt vmcnt(3)\n" \
  "s_barrier\n" \
  "ds_read_b32 v11, v8 offset:1024\n" \
  "s_mov_b32 s69, 3328\n" \
  "s_waitcnt lgkmcnt(0)\n" \
  "v_cmp_eq_u32 vcc, s60, v11\n" \
  "s_nop 0\n" \
  "s_cmp_eq_u32 vcc_lo, -1\n" \
  "s_cbranch_scc1 Lg4_go0_%=\n" \
  "s_add_u32 m0, s78, 0\n" \
  "s_nop 0\n" \
  "buffer_load_dword v7, s[36:39], s68 offen offset:0 sc1 lds\n" \
  "s_waitcnt vmcnt(2)\n" \
  "s_barrier\n" \
  "ds_read_b32 v11, v8 offset:2048\n" \
  "s_lshr_b32 s70, s47, 4\n" \
  "buffer_store_dword v5, v4, s[36:39], s70 offen offset:0 sc0 sc1\n" \
  "s_mov_b32 s47, s59\n" \
  "s_mov_b32 s69, 256\n" \
  "s_waitcnt lgkmcnt(0)\n" \
  "v_cmp_eq_u32 vcc, s60, v11\n" \
  "s_nop 0\n" \
  "s_cmp_eq_u32 vcc_lo, -1\n" \
  "s_cbranch_scc1 Lg4_go0_%=\n" \
  "s_add_u32 m0, s78, 1024\n" \
  "s_nop 0\n" \
  "buffer_load_dword v7, s[36:39], s68 offen offset:0 sc1 lds\n" \
  "s_waitcnt vmcnt(3)\n" \
  "s_barrier\n" \
  "ds_read_b32 v11, v8 offset:3072\n" \
  "s_mov_b32 s69, 1280\n" \
  "s_waitcnt lgkmcnt(0)\n" \
  "v_cmp_eq_u32 vcc, s60, v11\n" \
  "s_nop 0\n" \
  "s_cmp_eq_u32 vcc_lo, -1\n" \
  "s_cbranch_scc1 Lg4_go0_%=\n" \
  "Lg4_poll0_%=:\n" \
  "s_add_u32 m0, s78, 2048\n" \
  "s_nop 0\n" \
  "buffer_load_dword v7, s[36:39], s68 offen offset:0 sc1 lds\n" \
  "s_waitcnt vmcnt(2)\n" \
  "s_barrier\n" \
  "ds_read_b32 v11, v8 offset:0\n" \
  "s_mov_b32 s69, 2304\n" \
  "s_waitcnt lgkmcnt(0)\n" \
  "v_cmp_eq_u32 vcc, s60, v11\n" \
  "s_nop 0\n" \
  "s_cmp_eq_u32 vcc_lo, -1\n" \
  "s_cbranch_scc1 Lg4_go0_%=\n" \
  "s_add_u32 m0, s78, 3072\n" \
  "s_nop 0\n" \
  "buffer_load_dword v7, s[36:39], s68 offen offset:0 sc1 lds\n" \
  "s_waitcnt vmcnt(2)\n" \
  "s_barrier\n" \
  "ds_read_b32 v11, v8 offset:1024\n" \
  "s_mov_b32 s69, 3328\n" \
  "s_waitcnt lgkmcnt(0)\n" \
  "v_cmp_eq_u32 vcc, s60, v11\n" \
  "s_nop 0\n" \
  "s_cmp_eq_u32 vcc_lo, -1\n" \
  "s_cbranch_scc1 Lg4_go0_%=\n" \
  "s_add_u32 m0, s78, 0\n" \
  "s_nop 0\n" \
  "buffer_load_dword v7, s[36:39], s68 offen offset:0 sc1 lds\n" \
  "s_waitcnt vmcnt(2)\n" \
  "s_barrier\n" \
  "ds_read_b32 v11, v8 offset:2048\n" \
  "s_mov_b32 s69, 256\n" \
  "s_waitcnt lgkmcnt(0)\n" \
  "v_cmp_eq_u32 vcc, s60, v11\n" \
  "s_nop 0\n" \
  "s_cmp_eq_u32 vcc_lo, -1\n" \
  "s_cbranch_scc1 Lg4_go0_%=\n" \
  "s_add_u32 m0, s78, 1024\n" \
  "s_nop 0\n" \
  "buffer_load_dword v7, s[36:39], s68 offen offset:0 sc1 lds\n" \
  "s_waitcnt vmcnt(2)\n" \
  "s_barrier\n" \
  "ds_read_b32 v11, v8 offset:3072\n" \
  "s_mov_b32 s69, 1280\n" \
  "s_waitcnt lgkmcnt(0)\n" \
  "v_cmp_eq_u32 vcc, s60, v11\n" \
  "s_nop 0\n" \
  "s_cmp_eq_u32 vcc_lo, -1\n" \
  "s_cbranch_scc1 Lg4_go0_%=\n" \
  "s_add_u32 s79, s79, 1\n" \
  "s_cmp_lt_u32 s79, 20000\n" \
  "s_cbranch_scc1 Lg4_poll0_%=\n" \
  "Lg4_go0_%=:\n" \
  "s_mul_i32 s61, s48, 7\n" \
  "s_lshr_b32 s61, s61, 1\n" \
  "s_add_u32 m0, s88, 0\n" \
  "s_nop 0\n" \
  "buffer_load_dwordx4 v0, s[36:39], s61 offen offset:0 sc1 lds\n" \
  "buffer_load_dwordx4 v0, s[36:39], s61 offen offset:1024 sc1 lds\n" \
  "buffer_load_dwordx4 v0, s[36:39], s61 offen offset:2048 sc1 lds\n" \
  "buffer_load_dwordx4 v0, s[36:39], s61 offen offset:3072 sc1 lds\n" \
  "s_add_u32 s61, s61, 4096\n" \
  "s_add_u32 m0, s88, 4096\n" \
  "s_nop 0\n" \
  "buffer_load_dwordx4 v0, s[36:39], s61 offen offset:0 sc1 lds\n" \
  "buffer_load_dwordx4 v0, s[36:39], s61 offen offset:1024 sc1 lds\n" \
  "buffer_load_dwordx4 v0, s[36:39], s61 offen offset:2048 sc1 lds\n" \
  "s_add_u32 s61, s61, 24576\n" \
  "s_add_u32 m0, s88, 28672\n" \
  "s_nop 0\n" \
  "buffer_load_dwordx4 v0, s[36:39], s61 offen offset:0 sc1 lds\n" \
  "buffer_load_dwordx4 v0, s[36:39], s61 offen offset:1024 sc1 lds\n" \
  "buffer_load_dwordx4 v0, s[36:39], s61 offen offset:2048 sc1 lds\n" \
  "buffer_load_dwordx4 v0, s[36:39], s61 offen offset:3072 sc1 lds\n" \
  "s_add_u32 s61, s61, 4096\n" \
  "s_add_u32 m0, s88, 32768\n" \
  "s_nop 0\n" \
  "buffer_load_dwordx4 v0, s[36:39], s61 offen offset:0 sc1 lds\n" \
  "buffer_load_dwordx4 v0, s[36:39], s61 offen offset:1024 sc1 lds\n" \
  "buffer_load_dwordx4 v0, s[36:39], s61 offen offset:2048 sc1 lds\n" \
  "v_add_u32 v60, s69, v8\n" \
  "s_waitcnt vmcnt(7)\n" \
  "s_barrier\n" \
  "ds_read_b32 v11, v60 offset:0\n" \
  "ds_read_b128 v[28:31], v1 offset:0\n" \
  "ds_read_b128 v[32:35], v1 offset:1024\n" \
  "ds_read_b128 v[36:39], v1 offset:2048\n" \
  "ds_read_b128 v[40:43], v1 offset:3072\n" \
  "ds_read_b128 v[44:47], v1 offset:4096\n" \
  "ds_read_b128 v[48:51], v1 offset:5120\n" \
  "ds_read_b128 v[52:55], v1 offset:6144\n" \
  "s_lshr_b32 s70, s47, 4\n" \
  "buffer_store_dword v5, v4, s[36:39], s70 offen offset:0 sc0 sc1\n" \
  "s_mov_b32 s47, s59\n" \
  "s_waitcnt lgkmcnt(7)\n" \
  "v_cmp_eq_u32 vcc, s60, v11\n" \
  "s_nop 0\n" \
  "s_cmp_eq_u32 vcc_lo, -1\n" \
  "s_cselect_b32 s66, s49, s59\n" \
  "s_mul_i32 s61, s66, 7\n" \
  "s_lshr_b32 s61, s61, 1\n" \
  "s_waitcnt lgkmcnt(5)\n" \
  "s_branch Lg4_d0_%=\n" \
  "Lg4_done0_%=:\n" \
  "s_waitcnt vmcnt(0)\n" \
  "s_barrier\n" \
  "s_lshr_b32 s70, s47, 4\n" \
  "buffer_store_dword v5, v4, s[36:39], s70 offen offset:0 sc0 sc1\n" \
  "s_branch Lg4_done_%=\n" \
  "Lg4_slow1_%=:\n" \
  "s_cmp_eq_u32 s54, 1\n" \
  "s_cselect_b32 s68, s50, s49\n" \
  "s_mul_i32 s68, s68, 5\n" \
  "s_lshr_b32 s68, s68, 6\n" \
  "s_add_u32 m0, s78, 0\n" \
  "s_nop 0\n" \
  "buffer_load_dword v7, s[36:39], s68 offen offset:0 sc1 lds\n" \
  "s_lshr_b32 s73, s48, 0\n" \
  "v_lshlrev_b32 v60, 16, v68\n" \
  "v_and_b32 v61, 0xffff0000, v68\n" \
  "v_lshlrev_b32 v62, 16, v69\n" \
  "v_and_b32 v63, 0xffff0000, v69\n" \
  "v_lshlrev_b32 v64, 16, v70\n" \
  "v_and_b32 v65, 0xffff0000, v70\n" \
  "v_lshlrev_b32 v66, 16, v71\n" \
  "v_and_b32 v67, 0xffff0000, v71\n" \
  "v_add_f32 v12, v12, v60\n" \
  "v_add_f32 v13, v13, v61\n" \
  "s_add_u32 m0, s78, 1024\n" \
  "s_nop 0\n" \
  "buffer_load_dword v7, s[36:39], s68 offen offset:0 sc1 lds\n" \
  "v_add_f32 v14, v14, v62\n" \
  "v_add_f32 v15, v15, v63\n" \
  "v_add_f32 v16, v16, v64\n" \
  "v_add_f32 v17, v17, v65\n" \
  "v_add_f32 v18, v18, v66\n" \
  "v_add_f32 v19, v19, v67\n" \
  "v_cvt_pk_bf16_f32 v60, v12, v13\n" \
  "v_cvt_pk_bf16_f32 v61, v14, v15\n" \
  "v_cvt_pk_bf16_f32 v62, v16, v17\n" \
  "v_cvt_pk_bf16_f32 v63, v18, v19\n" \
  "buffer_store_dwordx4 v[60:63], v3, s[36:39], s73 offen offset:0 sc0 sc1\n" \
  "s_cmp_ge_u32 s49, s59\n" \
  "s_cbranch_scc1 Lg4_done1_%=\n" \
  "s_add_u32 m0, s78, 2048\n" \
  "s_nop 0\n" \
  "buffer_load_dword v7, s[36:39], s68 offen offset:0 sc1 lds\n" \
  "s_waitcnt vmcnt(3)\n" \
  "s_barrier\n" \
  "ds_read_b32 v11, v8 offset:0\n" \
  "s_mov_b32 s69, 2304\n" \
  "s_waitcnt lgkmcnt(0)\n" \
  "v_cmp_eq_u32 vcc, s60, v11\n" \
  "s_nop 0\n" \
  "s_cmp_eq_u32 vcc_lo, -1\n" \
  "s_cbranch_scc1 Lg4_go1_%=\n" \
  "s_add_u32 m0, s78, 3072\n" \
  "s_nop 0\n" \
  "buffer_load_dword v7, s[36:39], s68 offen offset:0 sc1 lds\n" \
  "s_waitcnt vmcnt(3)\n" \
  "s_barrier\n" \
  "ds_read_b32 v11, v8 offset:1024\n" \
  "s_mov_b32 s69, 3328\n" \
  "s_waitcnt lgkmcnt(0)\n" \
  "v_cmp_eq_u32 vcc, s60, v11\n" \
  "s_nop 0\n" \
  "s_cmp_eq_u32 vcc_lo, -1\n" \
  "s_cbranch_scc1 Lg4_go1_%=\n" \
  "s_add_u32 m0, s78, 0\n" \
  "s_nop 0\n" \
  "buffer_load_dword v7, s[36:39], s68 offen offset:0 sc1 lds\n" \
  "s_waitcnt vmcnt(2)\n" \
  "s_barrier\n" \
  "ds_read_b32 v11, v8 offset:2048\n" \
  "s_lshr_b32 s70, s48, 4\n" \
  "buffer_store_dword v5, v4, s[36:39], s70 offen offset:0 sc0 sc1\n" \
  "s_mov_b32 s48, s59\n" \
  "s_mov_b32 s69, 256\n" \
  "s_waitcnt lgkmcnt(0)\n" \
  "v_cmp_eq_u32 vcc, s60, v11\n" \
  "s_nop 0\n" \
  "s_cmp_eq_u32 vcc_lo, -1\n" \
  "s_cbranch_scc1 Lg4_go1_%=\n" \
  "s_add_u32 m0, s78, 1024\n" \
  "s_nop 0\n" \
  "buffer_load_dword v7, s[36:39], s68 offen offset:0 sc1 lds\n" \
  "s_waitcnt vmcnt(3)\n" \
  "s_barrier\n" \
  "ds_read_b32 v11, v8 offset:3072\n" \
  "s_mov_b32 s69, 1280\n" \
  "s_waitcnt lgkmcnt(0)\n" \
  "v_cmp_eq_u32 vcc, s60, v11\n" \
  "s_nop 0\n" \
  "s_cmp_eq_u32 vcc_lo, -1\n" \
  "s_cbranch_scc1 Lg4_go1_%=\n" \
  "Lg4_poll1_%=:\n" \
  "s_add_u32 m0, s78, 2048\n" \
  "s_nop 0\n" \
  "buffer_load_dword v7, s[36:39], s68 offen offset:0 sc1 lds\n" \
  "s_waitcnt vmcnt(2)\n" \
  "s_barrier\n" \
  "ds_read_b32 v11, v8 offset:0\n" \
  "s_mov_b32 s69, 2304\n" \
  "s_waitcnt lgkmcnt(0)\n" \
  "v_cmp_eq_u32 vcc, s60, v11\n" \
  "s_nop 0\n" \
  "s_cmp_eq_u32 vcc_lo, -1\n" \
  "s_cbranch_scc1 Lg4_go1_%=\n" \
  "s_add_u32 m0, s78, 3072\n" \
  "s_nop 0\n" \
  "buffer_load_dword v7, s[36:39], s68 offen offset:0 sc1 lds\n" \
  "s_waitcnt vmcnt(2)\n" \
  "s_barrier\n" \
  "ds_read_b32 v11, v8 offset:1024\n" \
  "s_mov_b32 s69, 3328\n" \
  "s_waitcnt lgkmcnt(0)\n" \
  "v_cmp_eq_u32 vcc, s60, v11\n" \
  "s_nop 0\n" \
  "s_cmp_eq_u32 vcc_lo, -1\n" \
  "s_cbranch_scc1 Lg4_go1_%=\n" \
  "s_add_u32 m0, s78, 0\n" \
  "s_nop 0\n" \
  "buffer_load_dword v7, s[36:39], s68 offen offset:0 sc1 lds\n" \
  "s_waitcnt vmcnt(2)\n" \
  "s_barrier\n" \
  "ds_read_b32 v11, v8 offset:2048\n" \
  "s_mov_b32 s69, 256\n" \
  "s_waitcnt lgkmcnt(0)\n" \
  "v_cmp_eq_u32 vcc, s60, v11\n" \
  "s_nop 0\n" \
  "s_cmp_eq_u32 vcc_lo, -1\n" \
  "s_cbranch_scc1 Lg4_go1_%=\n" \
  "s_add_u32 m0, s78, 1024\n" \
  "s_nop 0\n" \
  "buffer_load_dword v7, s[36:39], s68 offen offset:0 sc1 lds\n" \
  "s_waitcnt vmcnt(2)\n" \
  "s_barrier\n" \
  "ds_read_b32 v11, v8 offset:3072\n" \
  "s_mov_b32 s69, 1280\n" \
  "s_waitcnt lgkmcnt(0)\n" \
  "v_cmp_eq_u32 vcc, s60, v11\n" \
  "s_nop 0\n" \
  "s_cmp_eq_u32 vcc_lo, -1\n" \
  "s_cbranch_scc1 Lg4_go1_%=\n" \
  "s_add_u32 s79, s79, 1\n" \
  "s_cmp_lt_u32 s79, 20000\n" \
  "s_cbranch_scc1 Lg4_poll1_%=\n" \
  "Lg4_go1_%=:\n" \
  "s_mul_i32 s61, s49, 7\n" \
  "s_lshr_b32 s61, s61, 1\n" \
  "s_add_u32 m0, s88, 57344\n" \
  "s_nop 0\n" \
  "buffer_load_dwordx4 v0, s[36:39], s61 offen offset:0 sc1 lds\n" \
  "buffer_load_dwordx4 v0, s[36:39], s61 offen offset:1024 sc1 lds\n" \
  "buffer_load_dwordx4 v0, s[36:39], s61 offen offset:2048 sc1 lds\n" \
  "buffer_load_dwordx4 v0, s[36:39], s61 offen offset:3072 sc1 lds\n" \
  "s_add_u32 s61, s61, 4096\n" \
  "s_add_u32 m0, s88, 61440\n" \
  "s_nop 0\n" \
  "buffer_load_dwordx4 v0, s[36:39], s61 offen offset:0 sc1 lds\n" \
  "buffer_load_dwordx4 v0, s[36:39], s61 offen offset:1024 sc1 lds\n" \
  "buffer_load_dwordx4 v0, s[36:39], s61 offen offset:2048 sc1 lds\n" \
  "s_add_u32 s61, s61, 24576\n" \
  "s_add_u32 m0, s88, 86016\n" \
  "s_nop 0\n" \
  "buffer_load_dwordx4 v0, s[36:39], s61 offen offset:0 sc1 lds\n" \
  "buffer_load_dwordx4 v0, s[36:39], s61 offen offset:1024 sc1 lds\n" \
  "buffer_load_dwordx4 v0, s[36:39], s61 offen offset:2048 sc1 lds\n" \
  "buffer_load_dwordx4 v0, s[36:39], s61 offen offset:3072 sc1 lds\n" \
  "s_add_u32 s61, s61, 4096\n" \
  "s_add_u32 m0, s88, 90112\n" \
  "s_nop 0\n" \
  "buffer_load_dwordx4 v0, s[36:39], s61 offen offset:0 sc1 lds\n" \
  "buffer_load_dwordx4 v0, s[36:39], s61 offen offset:1024 sc1 lds\n" \
  "buffer_load_dwordx4 v0, s[36:39], s61 offen offset:2048 sc1 lds\n" \
  "v_add_u32 v60, s69, v8\n" \
  "s_waitcnt vmcnt(7)\n" \
  "s_barrier\n" \
  "ds_read_b32 v11, v60 offset:0\n" \
  "ds_read_b128 v[28:31], v2 offset:0\n" \
  "ds_read_b128 v[32:35], v2 offset:1024\n" \
  "ds_read_b128 v[36:39], v2 offset:2048\n" \
  "ds_read_b128 v[40:43], v2 offset:3072\n" \
  "ds_read_b128 v[44:47], v2 offset:4096\n" \
  "ds_read_b128 v[48:51], v2 offset:5120\n" \
  "ds_read_b128 v[52:55], v2 offset:6144\n" \
  "s_lshr_b32 s70, s48, 4\n" \
  "buffer_store_dword v5, v4, s[36:39], s70 offen offset:0 sc0 sc1\n" \
  "s_mov_b32 s48, s59\n" \
  "s_waitcnt lgkmcnt(7)\n" \
  "v_cmp_eq_u32 vcc, s60, v11\n" \
  "s_nop 0\n" \
  "s_cmp_eq_u32 vcc_lo, -1\n" \
  "s_cselect_b32 s67, s50, s59\n" \
  "s_mul_i32 s61, s67, 7\n" \
  "s_lshr_b32 s61, s61, 1\n" \
  "s_waitcnt lgkmcnt(5)\n" \
  "s_branch Lg4_d1_%=\n" \
  "Lg4_done1_%=:\n" \
  "s_waitcnt vmcnt(0)\n" \
  "s_barrier\n" \
  "s_lshr_b32 s70, s48, 4\n" \
  "buffer_store_dword v5, v4, s[36:39], s70 offen offset:0 sc0 sc1\n" \
  "s_branch Lg4_done_%=\n" \
  "Lg4_done_%=:\n" \
  "s_waitcnt vmcnt(0) lgkmcnt(0)\n" \
  "s_barrier\n" \
  "s_endpgm\n" \
  "Lcode_g4_e:\n" \
  :: "s"(ws), "s"(wave), "s"(sStep), "s"(sX1), "s"(sX2), "s"(sU0), "s"(sWw), "s"(sBuf), "v"(vSrc), "v"(vA), "v"(vOut), "v"(vFlag), "v"(vSnapOff), "v"(vPollOff), "v"(vSnapRd), "v"(vAuxS), "v"(vWd0), "v"(vWd1), "v"(vWr0), "v"(vWr1), "v"(vWr2), "s"(W) : "v0", "v1", "v2", "v3", "v4", "v5", "v6", "v7", "v8", "v9", "v10", "v11", "v12", "v13", "v14", "v15", "v16", "v17", "v18", "v19", "v20", "v21", "v22", "v23", "v24", "v25", "v26", "v27", "v28", "v29", "v30", "v31", "v32", "v33", "v34", "v35", "v36", "v37", "v38", "v39", "v40", "v41", "v42", "v43", "v44", "v45", "v46", "v47", "v48", "v49", "v50", "v51", "v52", "v53", "v54", "v55", "v56", "v57", "v58", "v59", "v60", "v61", "v62", "v63", "v64", "v65", "v66", "v67", "v68", "v69", "v70", "v71", "v72", "v73", "v74", "v75", "v76", "v77", "v78", "v79", "v80", "v81", "v82", "v83", "v84", "v85", "v86", "v87", "v88", "v89", "v90", "v91", "v92", "v93", "v94", "v95", "v96", "v97", "v98", "v99", "v100", "v101", "v102", "v103", "v104", "v105", "v106", "v107", "v108", "v109", "v110", "v111", "v112", "v113", "v114", "v115", "v116", "v117", "v118", "v119", "v120", "v121", "v122", "v123", "v124", "v125", "v126", "v127", "v128", "v129", "v130", "v131", "v132", "v133", "v134", "v135", "v136", "v137", "v138", "v139", "v140", "v141", "v142", "v143", "v144", "v145", "v146", "v147", "v148", "v149", "v150", "v151", "v152", "v153", "v154", "v155", "v156", "v157", "v158", "v159", "v160", "v161", "v162", "v163", "v164", "v165", "v166", "v167", "v168", "v169", "v170", "v171", "v172", "v173", "v174", "v175", "v176", "v177", "v178", "v179", "v180", "v181", "v182", "v183", "v184", "v185", "v186", "v187", "v188", "v189", "v190", "v191", "v192", "v193", "v194", "v195", "v196", "v197", "v198", "v199", "v200", "v201", "v202", "v203", "v204", "v205", "v206", "v207", "v208", "v209", "v210", "v211", "v212", "v213", "v214", "v215", "v216", "v217", "v218", "v219", "v220", "v221", "v222", "v223", "v224", "v225", "v226", "v227", "v228", "v229", "v230", "v231", "v255", "a0", "a1", "a2", "a3", "a4", "a5", "a6", "a7", "a8", "a9", "a10", "a11", "a12", "a13", "a14", "a15", "a16", "a17", "a18", "a19", "a20", "a21", "a22", "a23", "a24", "a25", "a26", "a27", "a28", "a29", "a30", "a31", "a32", "a33", "a34", "a35", "a36", "a37", "a38", "a39", "a40", "a41", "a42", "a43", "a44", "a45", "a46", "a47", "a48", "a49", "a50", "a51", "a52", "a53", "a54", "a55", "a56", "a57", "a58", "a59", "a60", "a61", "a62", "a63", "a64", "a65", "a66", "a67", "a68", "a69", "a70", "a71", "a72", "a73", "a74", "a75", "a76", "a77", "a78", "a79", "a80", "a81", "a82", "a83", "a84", "a85", "a86", "a87", "a88", "a89", "a90", "a91", "a92", "a93", "a94", "a95", "a96", "a97", "a98", "a99", "a100", "a101", "a102", "a103", "a104", "a105", "a106", "a107", "a108", "a109", "a110", "a111", "a112", "a113", "a114", "a115", "a116", "a117", "a118", "a119", "a120", "a121", "a122", "a123", "a124", "a125", "a126", "a127", "a128", "a129", "a130", "a131", "a132", "a133", "a134", "a135", "a136", "a137", "a138", "a139", "a140", "a141", "a142", "a143", "a144", "a145", "a146", "a147", "a148", "a149", "a150", "a151", "a152", "a153", "a154", "a155", "a156", "a157", "a158", "a159", "a160", "a161", "a162", "a163", "a164", "a165", "a166", "a167", "a168", "a169", "a170", "a171", "a172", "a173", "a174", "a175", "a176", "a177", "a178", "a179", "a180", "a181", "a182", "a183", "a184", "a185", "a186", "a187", "a188", "a189", "a190", "a191", "a192", "a193", "a194", "a195", "a196", "a197", "a198", "a199", "a200", "a201", "a202", "a203", "a204", "a205", "a206", "a207", "a208", "a209", "a210", "a211", "a212", "a213", "a214", "a215", "a216", "a217", "a218", "a219", "a220", "a221", "a222", "a223", "a224", "a225", "a226", "a227", "a228", "a229", "a230", "a231", "a232", "a233", "a234", "a235", "a236", "a237", "a238", "a239", "a240", "a241", "a242", "a243", "a244", "a245", "a246", "a247", "a248", "a249", "a250", "a251", "a252", "a253", "a254", "a255", "s36", "s37", "s38", "s39", "s40", "s41", "s42", "s43", "s44", "s45", "s46", "s47", "s48", "s49", "s50", "s51", "s52", "s53", "s54", "s55", "s56", "s57", "s58", "s59", "s60", "s61", "s62", "s63", "s64", "s65", "s66", "s67", "s68", "s69", "s70", "s71", "s72", "s73", "s74", "s75", "s76", "s77", "s78", "s79", "s80", "s81", "s82", "s83", "s84", "s85", "s86", "s87", "s88", "s89", "s90", "s91", "s92", "s93", "s94", "s95", "s96", "s97", "vcc", "scc", "memory")
#define ASM_R() asm volatile( \
  "Lcode_r_b:\n" \
  "s_mov_b64 s[36:37], %0\n" \
  "s_mov_b32 s38, 111193216\n" \
  "s_mov_b32 s39, 0x00020000\n" \
  "s_mov_b32 s54, %1\n" \
  "s_mov_b32 s50, %2\n" \
  "s_mov_b32 s92, %3\n" \
  "s_mov_b32 s65, %4\n" \
  "s_mov_b32 s48, %5\n" \
  "v_mov_b32 v0, %6\n" \
  "v_mov_b32 v1, %7\n" \
  "v_mov_b32 v2, %8\n" \
  "v_mov_b32 v3, %9\n" \
  "v_mov_b32 v4, %10\n" \
  "v_mov_b32 v5, %11\n" \
  "v_mov_b32 v6, %12\n" \
  "v_mov_b32 v7, s48\n" \
  "v_mbcnt_lo_u32_b32 v9, -1, 0\n" \
  "v_mbcnt_hi_u32_b32 v9, -1, v9\n" \
  "v_lshlrev_b32 v9, 4, v9\n" \
  "s_mov_b32 s74, 0\n" \
  "s_mov_b32 s96, 0\n" \
  "s_mov_b32 s95, 0\n" \
  "s_mov_b32 s51, 0x00003f80\n" \
  "s_mov_b32 s52, 0x3f800000\n" \
  "s_mov_b32 s46, 0xaaaaaaaa\n" \
  "s_mov_b32 s47, 0xaaaaaaaa\n" \
  "s_mov_b32 s56, 0xcccccccc\n" \
  "s_mov_b32 s57, 0xcccccccc\n" \
  "s_mov_b32 s76, 0x3a800000\n" \
  "s_mov_b32 s77, 0x3727c5ac\n" \
  "s_mov_b32 s78, 0xc0135761\n" \
  "s_mov_b32 s79, 0x3d372713\n" \
  "v_mov_b32 v176, 0x3f803f80\n" \
  "v_mov_b32 v177, 0x3f803f80\n" \
  "v_mov_b32 v178, 0x3f803f80\n" \
  "v_mov_b32 v179, 0x3f803f80\n" \
  "s_lshr_b32 s63, s54, 2\n" \
  "s_and_b32 s64, s54, 1\n" \
  "s_lshl_b32 s64, s64, 7\n" \
  "s_lshl_b32 s87, s63, 16\n" \
  "s_add_u32 s87, s87, s64\n" \
  "s_and_b32 s64, s54, 2\n" \
  "s_lshl_b32 s62, s64, 14\n" \
  "s_add_u32 s87, s87, s62\n" \
  "s_add_u32 s58, s87, 16908288\n" \
  "s_lshl_b32 s59, s54, 8\n" \
  "s_add_u32 s59, s59, 107022336\n" \
  "s_lshl_b32 s49, s54, 9\n" \
  "s_add_u32 s49, s49, s92\n" \
  "s_lshr_b32 s63, s54, 1\n" \
  "s_lshl_b32 s91, s63, 11\n" \
  "s_add_u32 s91, s91, 109871104\n" \
  "s_add_u32 s70, s54, 18\n" \
  "s_add_u32 s63, s54, s50\n" \
  "s_cmp_lt_u32 s54, 0\n" \
  "s_cselect_b32 s70, s65, s70\n" \
  "s_cmp_lt_u32 s63, 0\n" \
  "s_cselect_b32 s70, s63, s70\n" \
  "s_lshr_b32 s63, s70, 1\n" \
  "s_lshl_b32 s69, s63, 11\n" \
  "s_add_u32 s69, s69, 109871104\n" \
  "Lr_bp9_%=:\n" \
  "buffer_load_dword v8, v6, s[36:39], s91 offen offset:0 sc1\n" \
  "s_waitcnt vmcnt(0)\n" \
  "v_cmp_eq_u32 vcc, s48, v8\n" \
  "s_nop 0\n" \
  "s_cmp_eq_u64 vcc, exec\n" \
  "s_cselect_b32 s63, 1, s96\n" \
  "s_cmp_lg_u32 s63, 0\n" \
  "s_cbranch_scc1 Lr_bq9_%=\n" \
  "s_add_u32 s74, s74, 1\n" \
  "s_cmp_ge_u32 s74, 20000\n" \
  "s_cselect_b32 s96, 1, 0\n" \
  "s_sleep 4\n" \
  "s_branch Lr_bp9_%=\n" \
  "Lr_bq9_%=:\n" \
  "s_add_u32 s63, s87, 71303168\n" \
  "buffer_load_dwordx4 v[32:35], v1, s[36:39], s63 offen offset:0 sc1\n" \
  "buffer_load_dwordx4 v[36:39], v1, s[36:39], s63 offen offset:2048 sc1\n" \
  "s_add_u32 s63, s87, 71307264\n" \
  "buffer_load_dwordx4 v[40:43], v1, s[36:39], s63 offen offset:0 sc1\n" \
  "buffer_load_dwordx4 v[44:47], v1, s[36:39], s63 offen offset:2048 sc1\n" \
  "s_add_u32 s63, s87, 89128960\n" \
  "buffer_load_dwordx4 v[48:51], v1, s[36:39], s63 offen offset:0 sc1\n" \
  "buffer_load_dwordx4 v[52:55], v1, s[36:39], s63 offen offset:2048 sc1\n" \
  "s_add_u32 s63, s87, 89133056\n" \
  "buffer_load_dwordx4 v[56:59], v1, s[36:39], s63 offen offset:0 sc1\n" \
  "buffer_load_dwordx4 v[60:63], v1, s[36:39], s63 offen offset:2048 sc1\n" \
  "s_add_u32 s63, s87, 8388608\n" \
  "buffer_load_dwordx4 v[64:67], v1, s[36:39], s63 offen offset:0 sc1\n" \
  "buffer_load_dwordx4 v[68:71], v1, s[36:39], s63 offen offset:2048 sc1\n" \
  "s_add_u32 s63, s87, 8392704\n" \
  "buffer_load_dwordx4 v[72:75], v1, s[36:39], s63 offen offset:0 sc1\n" \
  "buffer_load_dwordx4 v[76:79], v1, s[36:39], s63 offen offset:2048 sc1\n" \
  "buffer_load_dword v8, v6, s[36:39], s69 offen offset:0 sc1\n" \
  "s_waitcnt vmcnt(0)\n" \
  "Lr_it0_%=:\n" \
  "s_mov_b32 s88, s58\n" \
  "s_mov_b32 s89, s59\n" \
  "s_mov_b32 s90, s49\n" \
  "s_mov_b32 s54, s70\n" \
  "s_lshr_b32 s63, s54, 2\n" \
  "s_and_b32 s64, s54, 1\n" \
  "s_lshl_b32 s64, s64, 7\n" \
  "s_lshl_b32 s87, s63, 16\n" \
  "s_add_u32 s87, s87, s64\n" \
  "s_and_b32 s64, s54, 2\n" \
  "s_lshl_b32 s62, s64, 14\n" \
  "s_add_u32 s87, s87, s62\n" \
  "s_add_u32 s58, s87, 16908288\n" \
  "s_lshl_b32 s59, s54, 8\n" \
  "s_add_u32 s59, s59, 107022336\n" \
  "s_lshl_b32 s49, s54, 9\n" \
  "s_add_u32 s49, s49, s92\n" \
  "s_lshr_b32 s63, s54, 1\n" \
  "s_lshl_b32 s91, s63, 11\n" \
  "s_add_u32 s91, s91, 109871104\n" \
  "s_add_u32 s70, s54, 18\n" \
  "s_add_u32 s63, s54, s50\n" \
  "s_cmp_lt_u32 s54, 0\n" \
  "s_cselect_b32 s70, s65, s70\n" \
  "s_cmp_lt_u32 s63, 0\n" \
  "s_cselect_b32 s70, s63, s70\n" \
  "s_lshr_b32 s63, s70, 1\n" \
  "s_lshl_b32 s69, s63, 11\n" \
  "s_add_u32 s69, s69, 109871104\n" \
  "v_cmp_eq_u32 vcc, s48, v8\n" \
  "s_nop 0\n" \
  "s_cmp_eq_u64 vcc, exec\n" \
  "s_cselect_b32 s63, s54, 256\n" \
  "s_cmp_lt_u32 s63, 256\n" \
  "s_cbranch_scc1 Lr_ta0_%=\n" \
  "buffer_load_dword v8, v6, s[36:39], s91 offen offset:0 sc1\n" \
  "v_dot2_f32_bf16 v128, v32, s51, 0\n" \
  "v_dot2_f32_bf16 v129, v32, s52, 0\n" \
  "v_dot2_f32_bf16 v130, v33, s51, 0\n" \
  "v_dot2_f32_bf16 v131, v33, s52, 0\n" \
  "v_dot2_f32_bf16 v132, v34, s51, 0\n" \
  "v_dot2_f32_bf16 v133, v34, s52, 0\n" \
  "v_dot2_f32_bf16 v134, v35, s51, 0\n" \
  "v_dot2_f32_bf16 v135, v35, s52, 0\n" \
  "v_dot2_f32_bf16 v128, v48, s51, v128\n" \
  "v_dot2_f32_bf16 v129, v48, s52, v129\n" \
  "v_dot2_f32_bf16 v130, v49, s51, v130\n" \
  "v_dot2_f32_bf16 v131, v49, s52, v131\n" \
  "v_dot2_f32_bf16 v132, v50, s51, v132\n" \
  "v_dot2_f32_bf16 v133, v50, s52, v133\n" \
  "v_dot2_f32_bf16 v134, v51, s51, v134\n" \
  "v_dot2_f32_bf16 v135, v51, s52, v135\n" \
  "v_dot2_f32_bf16 v136, v36, s51, 0\n" \
  "v_dot2_f32_bf16 v137, v36, s52, 0\n" \
  "v_dot2_f32_bf16 v138, v37, s51, 0\n" \
  "v_dot2_f32_bf16 v139, v37, s52, 0\n" \
  "v_dot2_f32_bf16 v140, v38, s51, 0\n" \
  "v_dot2_f32_bf16 v141, v38, s52, 0\n" \
  "v_dot2_f32_bf16 v142, v39, s51, 0\n" \
  "v_dot2_f32_bf16 v143, v39, s52, 0\n" \
  "v_dot2_f32_bf16 v136, v52, s51, v136\n" \
  "v_dot2_f32_bf16 v137, v52, s52, v137\n" \
  "v_dot2_f32_bf16 v138, v53, s51, v138\n" \
  "v_dot2_f32_bf16 v139, v53, s52, v139\n" \
  "v_dot2_f32_bf16 v140, v54, s51, v140\n" \
  "v_dot2_f32_bf16 v141, v54, s52, v141\n" \
  "v_dot2_f32_bf16 v142, v55, s51, v142\n" \
  "v_dot2_f32_bf16 v143, v55, s52, v143\n" \
  "v_cvt_pk_bf16_f32 v208, v128, v129\n" \
  "v_cvt_pk_bf16_f32 v209, v130, v131\n" \
  "v_cvt_pk_bf16_f32 v210, v132, v133\n" \
  "v_cvt_pk_bf16_f32 v211, v134, v135\n" \
  "s_nop 1\n" \
  "v_mfma_f32_16x16x32_bf16 v[180:183], v[176:179], v[208:211], 0\n" \
  "v_mfma_f32_16x16x32_bf16 v[184:187], v[208:211], v[208:211], 0\n" \
  "v_dot2_f32_bf16 v144, v40, s51, 0\n" \
  "v_dot2_f32_bf16 v145, v40, s52, 0\n" \
  "v_dot2_f32_bf16 v146, v41, s51, 0\n" \
  "v_dot2_f32_bf16 v147, v41, s52, 0\n" \
  "v_dot2_f32_bf16 v148, v42, s51, 0\n" \
  "v_dot2_f32_bf16 v149, v42, s52, 0\n" \
  "v_dot2_f32_bf16 v150, v43, s51, 0\n" \
  "v_dot2_f32_bf16 v151, v43, s52, 0\n" \
  "v_dot2_f32_bf16 v144, v56, s51, v144\n" \
  "v_dot2_f32_bf16 v145, v56, s52, v145\n" \
  "v_dot2_f32_bf16 v146, v57, s51, v146\n" \
  "v_dot2_f32_bf16 v147, v57, s52, v147\n" \
  "v_dot2_f32_bf16 v148, v58, s51, v148\n" \
  "v_dot2_f32_bf16 v149, v58, s52, v149\n" \
  "v_dot2_f32_bf16 v150, v59, s51, v150\n" \
  "v_dot2_f32_bf16 v151, v59, s52, v151\n" \
  "v_cvt_pk_bf16_f32 v212, v136, v137\n" \
  "v_cvt_pk_bf16_f32 v213, v138, v139\n" \
  "v_cvt_pk_bf16_f32 v214, v140, v141\n" \
  "v_cvt_pk_bf16_f32 v215, v142, v143\n" \
  "s_nop 1\n" \
  "v_mfma_f32_16x16x32_bf16 v[180:183], v[176:179], v[212:215], v[180:183]\n" \
  "v_mfma_f32_16x16x32_bf16 v[184:187], v[212:215], v[212:215], v[184:187]\n" \
  "v_dot2_f32_bf16 v152, v44, s51, 0\n" \
  "v_dot2_f32_bf16 v153, v44, s52, 0\n" \
  "v_dot2_f32_bf16 v154, v45, s51, 0\n" \
  "v_dot2_f32_bf16 v155, v45, s52, 0\n" \
  "v_dot2_f32_bf16 v156, v46, s51, 0\n" \
  "v_dot2_f32_bf16 v157, v46, s52, 0\n" \
  "v_dot2_f32_bf16 v158, v47, s51, 0\n" \
  "v_dot2_f32_bf16 v159, v47, s52, 0\n" \
  "v_dot2_f32_bf16 v152, v60, s51, v152\n" \
  "v_dot2_f32_bf16 v153, v60, s52, v153\n" \
  "v_dot2_f32_bf16 v154, v61, s51, v154\n" \
  "v_dot2_f32_bf16 v155, v61, s52, v155\n" \
  "v_dot2_f32_bf16 v156, v62, s51, v156\n" \
  "v_dot2_f32_bf16 v157, v62, s52, v157\n" \
  "v_dot2_f32_bf16 v158, v63, s51, v158\n" \
  "v_dot2_f32_bf16 v159, v63, s52, v159\n" \
  "v_cvt_pk_bf16_f32 v216, v144, v145\n" \
  "v_cvt_pk_bf16_f32 v217, v146, v147\n" \
  "v_cvt_pk_bf16_f32 v218, v148, v149\n" \
  "v_cvt_pk_bf16_f32 v219, v150, v151\n" \
  "s_nop 1\n" \
  "v_mfma_f32_16x16x32_bf16 v[180:183], v[176:179], v[216:219], v[180:183]\n" \
  "v_mfma_f32_16x16x32_bf16 v[184:187], v[216:219], v[216:219], v[184:187]\n" \
  "v_cvt_pk_bf16_f32 v220, v152, v153\n" \
  "v_cvt_pk_bf16_f32 v221, v154, v155\n" \
  "v_cvt_pk_bf16_f32 v222, v156, v157\n" \
  "v_cvt_pk_bf16_f32 v223, v158, v159\n" \
  "s_nop 1\n" \
  "v_mfma_f32_16x16x32_bf16 v[180:183], v[176:179], v[220:223], v[180:183]\n" \
  "v_mfma_f32_16x16x32_bf16 v[184:187], v[220:223], v[220:223], v[184:187]\n" \
  "s_nop 6\n" \
  "v_add_f32_dpp v224, v180, v180 row_ror:8 row_mask:0xf bank_mask:0xf\n" \
  "v_cndmask_b32 v225, v184, v185, s[46:47]\n" \
  "v_cndmask_b32 v226, v186, v187, s[46:47]\n" \
  "ds_write_b32 v2, v224 offset:0\n" \
  "v_cndmask_b32 v225, v225, v226, s[56:57]\n" \
  "ds_write_b32 v0, v225 offset:0\n" \
  "s_waitcnt lgkmcnt(0)\n" \
  "s_barrier\n" \
  "ds_read_b128 v[160:163], v3 offset:0\n" \
  "ds_read_b128 v[164:167], v3 offset:16\n" \
  "ds_read_b128 v[168:171], v3 offset:32\n" \
  "s_waitcnt lgkmcnt(0)\n" \
  "v_pk_add_f32 v[160:161], v[160:161], v[162:163]\n" \
  "v_pk_add_f32 v[164:165], v[164:165], v[166:167]\n" \
  "v_pk_add_f32 v[168:169], v[168:169], v[170:171]\n" \
  "v_add_f32 v160, v160, v161\n" \
  "v_pk_add_f32 v[164:165], v[164:165], v[168:169]\n" \
  "v_mul_f32 v160, s76, v160\n" \
  "v_add_f32 v161, v164, v165\n" \
  "v_mul_f32 v161, s76, v161\n" \
  "v_fma_f32 v161, -v160, v160, v161\n" \
  "v_max_f32 v161, 0, v161\n" \
  "s_waitcnt vmcnt(0)\n" \
  "s_cmp_eq_u32 s95, 0\n" \
  "s_cbranch_scc1 Lr_pb1_%=\n" \
  "buffer_store_dword v7, v5, s[36:39], s60 offen offset:0 sc0 sc1\n" \
  "s_mov_b32 s95, 0\n" \
  "Lr_pb1_%=:\n" \
  "v_cmp_eq_u32 vcc, s48, v8\n" \
  "s_nop 0\n" \
  "s_cmp_eq_u64 vcc, exec\n" \
  "s_cselect_b32 s63, s54, 256\n" \
  "s_cmp_lt_u32 s63, 256\n" \
  "s_cbranch_scc1 Lr_tb0_%=\n" \
  "buffer_load_dword v8, v6, s[36:39], s91 offen offset:0 sc1\n" \
  "v_add_f32 v161, s77, v161\n" \
  "v_rsq_f32 v228, v161\n" \
  "s_nop 0\n" \
  "v_mul_f32_e64 v230, -v160, v228\n" \
  "v_mov_b32 v229, v228\n" \
  "v_mov_b32 v231, v230\n" \
  "v_lshlrev_b32 v160, 16, v64\n" \
  "v_and_b32 v161, 0xffff0000, v64\n" \
  "v_pk_fma_f32 v[162:163], v[128:129], v[228:229], v[230:231]\n" \
  "v_pk_fma_f32 v[128:129], v[162:163], v[160:161], v[128:129]\n" \
  "v_lshlrev_b32 v164, 16, v65\n" \
  "v_and_b32 v165, 0xffff0000, v65\n" \
  "v_pk_fma_f32 v[166:167], v[130:131], v[228:229], v[230:231]\n" \
  "v_pk_fma_f32 v[130:131], v[166:167], v[164:165], v[130:131]\n" \
  "v_lshlrev_b32 v168, 16, v66\n" \
  "v_and_b32 v169, 0xffff0000, v66\n" \
  "v_pk_fma_f32 v[170:171], v[132:133], v[228:229], v[230:231]\n" \
  "v_pk_fma_f32 v[132:133], v[170:171], v[168:169], v[132:133]\n" \
  "v_lshlrev_b32 v172, 16, v67\n" \
  "v_and_b32 v173, 0xffff0000, v67\n" \
  "v_pk_fma_f32 v[174:175], v[134:135], v[228:229], v[230:231]\n" \
  "v_pk_fma_f32 v[134:135], v[174:175], v[172:173], v[134:135]\n" \
  "v_lshlrev_b32 v160, 16, v68\n" \
  "v_and_b32 v161, 0xffff0000, v68\n" \
  "v_pk_fma_f32 v[162:163], v[136:137], v[228:229], v[230:231]\n" \
  "v_pk_fma_f32 v[136:137], v[162:163], v[160:161], v[136:137]\n" \
  "v_lshlrev_b32 v164, 16, v69\n" \
  "v_and_b32 v165, 0xffff0000, v69\n" \
  "v_pk_fma_f32 v[166:167], v[138:139], v[228:229], v[230:231]\n" \
  "v_pk_fma_f32 v[138:139], v[166:167], v[164:165], v[138:139]\n" \
  "v_lshlrev_b32 v168, 16, v70\n" \
  "v_and_b32 v169, 0xffff0000, v70\n" \
  "v_pk_fma_f32 v[170:171], v[140:141], v[228:229], v[230:231]\n" \
  "v_pk_fma_f32 v[140:141], v[170:171], v[168:169], v[140:141]\n" \
  "v_lshlrev_b32 v172, 16, v71\n" \
  "v_and_b32 v173, 0xffff0000, v71\n" \
  "v_pk_fma_f32 v[174:175], v[142:143], v[228:229], v[230:231]\n" \
  "v_pk_fma_f32 v[142:143], v[174:175], v[172:173], v[142:143]\n" \
  "v_lshlrev_b32 v160, 16, v72\n" \
  "v_and_b32 v161, 0xffff0000, v72\n" \
  "v_pk_fma_f32 v[162:163], v[144:145], v[228:229], v[230:231]\n" \
  "v_pk_fma_f32 v[144:145], v[162:163], v[160:161], v[144:145]\n" \
  "v_lshlrev_b32 v164, 16, v73\n" \
  "v_and_b32 v165, 0xffff0000, v73\n" \
  "v_pk_fma_f32 v[166:167], v[146:147], v[228:229], v[230:231]\n" \
  "v_pk_fma_f32 v[146:147], v[166:167], v[164:165], v[146:147]\n" \
  "v_lshlrev_b32 v168, 16, v74\n" \
  "v_and_b32 v169, 0xffff0000, v74\n" \
  "v_pk_fma_f32 v[170:171], v[148:149], v[228:229], v[230:231]\n" \
  "v_pk_fma_f32 v[148:149], v[170:171], v[168:169], v[148:149]\n" \
  "v_lshlrev_b32 v172, 16, v75\n" \
  "v_and_b32 v173, 0xffff0000, v75\n" \
  "v_pk_fma_f32 v[174:175], v[150:151], v[228:229], v[230:231]\n" \
  "v_pk_fma_f32 v[150:151], v[174:175], v[172:173], v[150:151]\n" \
  "v_lshlrev_b32 v160, 16, v76\n" \
  "v_and_b32 v161, 0xffff0000, v76\n" \
  "v_pk_fma_f32 v[162:163], v[152:153], v[228:229], v[230:231]\n" \
  "v_pk_fma_f32 v[152:153], v[162:163], v[160:161], v[152:153]\n" \
  "v_lshlrev_b32 v164, 16, v77\n" \
  "v_and_b32 v165, 0xffff0000, v77\n" \
  "v_pk_fma_f32 v[166:167], v[154:155], v[228:229], v[230:231]\n" \
  "v_pk_fma_f32 v[154:155], v[166:167], v[164:165], v[154:155]\n" \
  "v_lshlrev_b32 v168, 16, v78\n" \
  "v_and_b32 v169, 0xffff0000, v78\n" \
  "v_pk_fma_f32 v[170:171], v[156:157], v[228:229], v[230:231]\n" \
  "v_pk_fma_f32 v[156:157], v[170:171], v[168:169], v[156:157]\n" \
  "v_lshlrev_b32 v172, 16, v79\n" \
  "v_and_b32 v173, 0xffff0000, v79\n" \
  "v_pk_fma_f32 v[174:175], v[158:159], v[228:229], v[230:231]\n" \
  "v_pk_fma_f32 v[158:159], v[174:175], v[172:173], v[158:159]\n" \
  "v_cvt_pk_bf16_f32 v208, v128, v129\n" \
  "v_cvt_pk_bf16_f32 v209, v130, v131\n" \
  "v_cvt_pk_bf16_f32 v210, v132, v133\n" \
  "v_cvt_pk_bf16_f32 v211, v134, v135\n" \
  "s_nop 1\n" \
  "v_mfma_f32_16x16x32_bf16 v[180:183], v[176:179], v[208:211], 0\n" \
  "v_mfma_f32_16x16x32_bf16 v[184:187], v[208:211], v[208:211], 0\n" \
  "buffer_store_dwordx4 v[208:211], v1, s[36:39], s88 offen offset:0 sc0 sc1\n" \
  "v_cvt_pk_bf16_f32 v212, v136, v137\n" \
  "v_cvt_pk_bf16_f32 v213, v138, v139\n" \
  "v_cvt_pk_bf16_f32 v214, v140, v141\n" \
  "v_cvt_pk_bf16_f32 v215, v142, v143\n" \
  "s_nop 1\n" \
  "v_mfma_f32_16x16x32_bf16 v[180:183], v[176:179], v[212:215], v[180:183]\n" \
  "v_mfma_f32_16x16x32_bf16 v[184:187], v[212:215], v[212:215], v[184:187]\n" \
  "buffer_store_dwordx4 v[212:215], v1, s[36:39], s88 offen offset:2048 sc0 sc1\n" \
  "v_cvt_pk_bf16_f32 v216, v144, v145\n" \
  "v_cvt_pk_bf16_f32 v217, v146, v147\n" \
  "v_cvt_pk_bf16_f32 v218, v148, v149\n" \
  "v_cvt_pk_bf16_f32 v219, v150, v151\n" \
  "s_nop 1\n" \
  "v_mfma_f32_16x16x32_bf16 v[180:183], v[176:179], v[216:219], v[180:183]\n" \
  "v_mfma_f32_16x16x32_bf16 v[184:187], v[216:219], v[216:219], v[184:187]\n" \
  "s_add_u32 s88, s88, 4096\n" \
  "buffer_store_dwordx4 v[216:219], v1, s[36:39], s88 offen offset:0 sc0 sc1\n" \
  "v_cvt_pk_bf16_f32 v220, v152, v153\n" \
  "v_cvt_pk_bf16_f32 v221, v154, v155\n" \
  "v_cvt_pk_bf16_f32 v222, v156, v157\n" \
  "v_cvt_pk_bf16_f32 v223, v158, v159\n" \
  "s_nop 1\n" \
  "v_mfma_f32_16x16x32_bf16 v[180:183], v[176:179], v[220:223], v[180:183]\n" \
  "v_mfma_f32_16x16x32_bf16 v[184:187], v[220:223], v[220:223], v[184:187]\n" \
  "buffer_store_dwordx4 v[220:223], v1, s[36:39], s88 offen offset:2048 sc0 sc1\n" \
  "s_nop 5\n" \
  "v_add_f32_dpp v224, v180, v180 row_ror:8 row_mask:0xf bank_mask:0xf\n" \
  "v_cndmask_b32 v225, v184, v185, s[46:47]\n" \
  "v_cndmask_b32 v226, v186, v187, s[46:47]\n" \
  "ds_write_b32 v2, v224 offset:1024\n" \
  "v_cndmask_b32 v225, v225, v226, s[56:57]\n" \
  "ds_write_b32 v0, v225 offset:1024\n" \
  "s_waitcnt lgkmcnt(0)\n" \
  "s_barrier\n" \
  "ds_read_b128 v[160:163], v3 offset:1024\n" \
  "ds_read_b128 v[164:167], v3 offset:1040\n" \
  "ds_read_b128 v[168:171], v3 offset:1056\n" \
  "s_waitcnt lgkmcnt(0)\n" \
  "v_pk_add_f32 v[160:161], v[160:161], v[162:163]\n" \
  "v_pk_add_f32 v[164:165], v[164:165], v[166:167]\n" \
  "v_pk_add_f32 v[168:169], v[168:169], v[170:171]\n" \
  "v_add_f32 v160, v160, v161\n" \
  "v_pk_add_f32 v[164:165], v[164:165], v[168:169]\n" \
  "v_mul_f32 v160, s76, v160\n" \
  "v_add_f32 v161, v164, v165\n" \
  "v_mul_f32 v161, s76, v161\n" \
  "v_fma_f32 v161, -v160, v160, v161\n" \
  "v_max_f32 v161, 0, v161\n" \
  "v_add_f32 v162, s77, v161\n" \
  "v_fma_f32 v163, s77, v162, v161\n" \
  "v_rsq_f32 v173, v162\n" \
  "v_sqrt_f32 v171, v163\n" \
  "v_rsq_f32 v162, v163\n" \
  "v_mov_b32 v168, v160\n" \
  "v_mul_f32_e64 v172, -v160, v173\n" \
  "v_mul_f32 v169, s78, v162\n" \
  "v_mul_f32 v163, v162, v162\n" \
  "v_mul_f32 v170, s79, v163\n" \
  "v_mul_f32 v170, v170, v169\n" \
  "buffer_store_dwordx4 v[168:171], v4, s[36:39], s89 offen offset:0 sc0 sc1\n" \
  "buffer_store_dwordx2 v[172:173], v4, s[36:39], s89 offen offset:16 sc0 sc1\n" \
  "s_mov_b32 s60, s90\n" \
  "s_mov_b32 s95, 1\n" \
  "s_waitcnt vmcnt(6)\n" \
  "v_cmp_eq_u32 vcc, s48, v8\n" \
  "s_nop 0\n" \
  "s_cmp_eq_u64 vcc, exec\n" \
  "s_cselect_b32 s63, s54, 256\n" \
  "s_cmp_lt_u32 s63, 256\n" \
  "s_cbranch_scc1 Lr_tc0_%=\n" \
  "s_waitcnt vmcnt(0)\n" \
  "s_cmp_eq_u32 s95, 0\n" \
  "s_cbranch_scc1 Lr_pb2_%=\n" \
  "buffer_store_dword v7, v5, s[36:39], s60 offen offset:0 sc0 sc1\n" \
  "s_mov_b32 s95, 0\n" \
  "Lr_pb2_%=:\n" \
  "s_cmp_ge_u32 s54, 256\n" \
  "s_cbranch_scc1 Lr_done_%=\n" \
  "Lr_bp0_%=:\n" \
  "buffer_load_dword v8, v6, s[36:39], s91 offen offset:0 sc1\n" \
  "s_waitcnt vmcnt(0)\n" \
  "v_cmp_eq_u32 vcc, s48, v8\n" \
  "s_nop 0\n" \
  "s_cmp_eq_u64 vcc, exec\n" \
  "s_cselect_b32 s63, 1, s96\n" \
  "s_cmp_lg_u32 s63, 0\n" \
  "s_cbranch_scc1 Lr_bq0_%=\n" \
  "s_add_u32 s74, s74, 1\n" \
  "s_cmp_ge_u32 s74, 20000\n" \
  "s_cselect_b32 s96, 1, 0\n" \
  "s_sleep 4\n" \
  "s_branch Lr_bp0_%=\n" \
  "Lr_bq0_%=:\n" \
  "s_add_u32 s63, s87, 71303168\n" \
  "buffer_load_dwordx4 v[80:83], v1, s[36:39], s63 offen offset:0 sc1\n" \
  "buffer_load_dwordx4 v[84:87], v1, s[36:39], s63 offen offset:2048 sc1\n" \
  "s_add_u32 s63, s87, 71307264\n" \
  "buffer_load_dwordx4 v[88:91], v1, s[36:39], s63 offen offset:0 sc1\n" \
  "buffer_load_dwordx4 v[92:95], v1, s[36:39], s63 offen offset:2048 sc1\n" \
  "s_add_u32 s63, s87, 89128960\n" \
  "buffer_load_dwordx4 v[96:99], v1, s[36:39], s63 offen offset:0 sc1\n" \
  "buffer_load_dwordx4 v[100:103], v1, s[36:39], s63 offen offset:2048 sc1\n" \
  "s_add_u32 s63, s87, 89133056\n" \
  "buffer_load_dwordx4 v[104:107], v1, s[36:39], s63 offen offset:0 sc1\n" \
  "buffer_load_dwordx4 v[108:111], v1, s[36:39], s63 offen offset:2048 sc1\n" \
  "s_add_u32 s63, s87, 8388608\n" \
  "buffer_load_dwordx4 v[112:115], v1, s[36:39], s63 offen offset:0 sc1\n" \
  "buffer_load_dwordx4 v[116:119], v1, s[36:39], s63 offen offset:2048 sc1\n" \
  "s_add_u32 s63, s87, 8392704\n" \
  "buffer_load_dwordx4 v[120:123], v1, s[36:39], s63 offen offset:0 sc1\n" \
  "buffer_load_dwordx4 v[124:127], v1, s[36:39], s63 offen offset:2048 sc1\n" \
  "buffer_load_dword v8, v6, s[36:39], s69 offen offset:0 sc1\n" \
  "s_waitcnt vmcnt(0)\n" \
  "s_branch Lr_it1_%=\n" \
  "Lr_ta0_%=:\n" \
  "v_dot2_f32_bf16 v128, v32, s51, 0\n" \
  "v_dot2_f32_bf16 v129, v32, s52, 0\n" \
  "v_dot2_f32_bf16 v130, v33, s51, 0\n" \
  "v_dot2_f32_bf16 v131, v33, s52, 0\n" \
  "v_dot2_f32_bf16 v132, v34, s51, 0\n" \
  "v_dot2_f32_bf16 v133, v34, s52, 0\n" \
  "s_add_u32 s63, s87, 71303168\n" \
  "buffer_load_dwordx4 v[80:83], v1, s[36:39], s63 offen offset:0 sc1\n" \
  "v_dot2_f32_bf16 v134, v35, s51, 0\n" \
  "v_dot2_f32_bf16 v135, v35, s52, 0\n" \
  "v_dot2_f32_bf16 v128, v48, s51, v128\n" \
  "v_dot2_f32_bf16 v129, v48, s52, v129\n" \
  "v_dot2_f32_bf16 v130, v49, s51, v130\n" \
  "v_dot2_f32_bf16 v131, v49, s52, v131\n" \
  "buffer_load_dwordx4 v[84:87], v1, s[36:39], s63 offen offset:2048 sc1\n" \
  "v_dot2_f32_bf16 v132, v50, s51, v132\n" \
  "v_dot2_f32_bf16 v133, v50, s52, v133\n" \
  "v_dot2_f32_bf16 v134, v51, s51, v134\n" \
  "v_dot2_f32_bf16 v135, v51, s52, v135\n" \
  "v_dot2_f32_bf16 v136, v36, s51, 0\n" \
  "v_dot2_f32_bf16 v137, v36, s52, 0\n" \
  "s_add_u32 s63, s87, 71307264\n" \
  "buffer_load_dwordx4 v[88:91], v1, s[36:39], s63 offen offset:0 sc1\n" \
  "v_dot2_f32_bf16 v138, v37, s51, 0\n" \
  "v_dot2_f32_bf16 v139, v37, s52, 0\n" \
  "v_dot2_f32_bf16 v140, v38, s51, 0\n" \
  "v_dot2_f32_bf16 v141, v38, s52, 0\n" \
  "v_dot2_f32_bf16 v142, v39, s51, 0\n" \
  "v_dot2_f32_bf16 v143, v39, s52, 0\n" \
  "buffer_load_dwordx4 v[92:95], v1, s[36:39], s63 offen offset:2048 sc1\n" \
  "v_dot2_f32_bf16 v136, v52, s51, v136\n" \
  "v_dot2_f32_bf16 v137, v52, s52, v137\n" \
  "v_dot2_f32_bf16 v138, v53, s51, v138\n" \
  "v_dot2_f32_bf16 v139, v53, s52, v139\n" \
  "v_dot2_f32_bf16 v140, v54, s51, v140\n" \
  "v_dot2_f32_bf16 v141, v54, s52, v141\n" \
  "s_add_u32 s63, s87, 89128960\n" \
  "buffer_load_dwordx4 v[96:99], v1, s[36:39], s63 offen offset:0 sc1\n" \
  "v_dot2_f32_bf16 v142, v55, s51, v142\n" \
  "v_dot2_f32_bf16 v143, v55, s52, v143\n" \
  "v_cvt_pk_bf16_f32 v208, v128, v129\n" \
  "v_cvt_pk_bf16_f32 v209, v130, v131\n" \
  "v_cvt_pk_bf16_f32 v210, v132, v133\n" \
  "v_cvt_pk_bf16_f32 v211, v134, v135\n" \
  "buffer_load_dwordx4 v[100:103], v1, s[36:39], s63 offen offset:2048 sc1\n" \
  "s_nop 0\n" \
  "v_mfma_f32_16x16x32_bf16 v[180:183], v[176:179], v[208:211], 0\n" \
  "v_mfma_f32_16x16x32_bf16 v[184:187], v[208:211], v[208:211], 0\n" \
  "v_dot2_f32_bf16 v144, v40, s51, 0\n" \
  "v_dot2_f32_bf16 v145, v40, s52, 0\n" \
  "v_dot2_f32_bf16 v146, v41, s51, 0\n" \
  "v_dot2_f32_bf16 v147, v41, s52, 0\n" \
  "s_add_u32 s63, s87, 89133056\n" \
  "buffer_load_dwordx4 v[104:107], v1, s[36:39], s63 offen offset:0 sc1\n" \
  "v_dot2_f32_bf16 v148, v42, s51, 0\n" \
  "v_dot2_f32_bf16 v149, v42, s52, 0\n" \
  "v_dot2_f32_bf16 v150, v43, s51, 0\n" \
  "v_dot2_f32_bf16 v151, v43, s52, 0\n" \
  "v_dot2_f32_bf16 v144, v56, s51, v144\n" \
  "v_dot2_f32_bf16 v145, v56, s52, v145\n" \
  "buffer_load_dwordx4 v[108:111], v1, s[36:39], s63 offen offset:2048 sc1\n" \
  "v_dot2_f32_bf16 v146, v57, s51, v146\n" \
  "v_dot2_f32_bf16 v147, v57, s52, v147\n" \
  "v_dot2_f32_bf16 v148, v58, s51, v148\n" \
  "v_dot2_f32_bf16 v149, v58, s52, v149\n" \
  "v_dot2_f32_bf16 v150, v59, s51, v150\n" \
  "v_dot2_f32_bf16 v151, v59, s52, v151\n" \
  "s_add_u32 s63, s87, 8388608\n" \
  "buffer_load_dwordx4 v[112:115], v1, s[36:39], s63 offen offset:0 sc1\n" \
  "v_cvt_pk_bf16_f32 v212, v136, v137\n" \
  "v_cvt_pk_bf16_f32 v213, v138, v139\n" \
  "v_cvt_pk_bf16_f32 v214, v140, v141\n" \
  "v_cvt_pk_bf16_f32 v215, v142, v143\n" \
  "s_nop 1\n" \
  "v_mfma_f32_16x16x32_bf16 v[180:183], v[176:179], v[212:215], v[180:183]\n" \
  "v_mfma_f32_16x16x32_bf16 v[184:187], v[212:215], v[212:215], v[184:187]\n" \
  "buffer_load_dwordx4 v[116:119], v1, s[36:39], s63 offen offset:2048 sc1\n" \
  "v_dot2_f32_bf16 v152, v44, s51, 0\n" \
  "v_dot2_f32_bf16 v153, v44, s52, 0\n" \
  "v_dot2_f32_bf16 v154, v45, s51, 0\n" \
  "v_dot2_f32_bf16 v155, v45, s52, 0\n" \
  "v_dot2_f32_bf16 v156, v46, s51, 0\n" \
  "v_dot2_f32_bf16 v157, v46, s52, 0\n" \
  "s_add_u32 s63, s87, 8392704\n" \
  "buffer_load_dwordx4 v[120:123], v1, s[36:39], s63 offen offset:0 sc1\n" \
  "v_dot2_f32_bf16 v158, v47, s51, 0\n" \
  "v_dot2_f32_bf16 v159, v47, s52, 0\n" \
  "v_dot2_f32_bf16 v152, v60, s51, v152\n" \
  "v_dot2_f32_bf16 v153, v60, s52, v153\n" \
  "v_dot2_f32_bf16 v154, v61, s51, v154\n" \
  "v_dot2_f32_bf16 v155, v61, s52, v155\n" \
  "buffer_load_dwordx4 v[124:127], v1, s[36:39], s63 offen offset:2048 sc1\n" \
  "v_dot2_f32_bf16 v156, v62, s51, v156\n" \
  "v_dot2_f32_bf16 v157, v62, s52, v157\n" \
  "v_dot2_f32_bf16 v158, v63, s51, v158\n" \
  "v_dot2_f32_bf16 v159, v63, s52, v159\n" \
  "v_cvt_pk_bf16_f32 v216, v144, v145\n" \
  "v_cvt_pk_bf16_f32 v217, v146, v147\n" \
  "v_cvt_pk_bf16_f32 v218, v148, v149\n" \
  "v_cvt_pk_bf16_f32 v219, v150, v151\n" \
  "s_nop 1\n" \
  "v_mfma_f32_16x16x32_bf16 v[180:183], v[176:179], v[216:219], v[180:183]\n" \
  "v_mfma_f32_16x16x32_bf16 v[184:187], v[216:219], v[216:219], v[184:187]\n" \
  "v_cvt_pk_bf16_f32 v220, v152, v153\n" \
  "v_cvt_pk_bf16_f32 v221, v154, v155\n" \
  "v_cvt_pk_bf16_f32 v222, v156, v157\n" \
  "v_cvt_pk_bf16_f32 v223, v158, v159\n" \
  "s_nop 1\n" \
  "v_mfma_f32_16x16x32_bf16 v[180:183], v[176:179], v[220:223], v[180:183]\n" \
  "v_mfma_f32_16x16x32_bf16 v[184:187], v[220:223], v[220:223], v[184:187]\n" \
  "s_nop 6\n" \
  "v_add_f32_dpp v224, v180, v180 row_ror:8 row_mask:0xf bank_mask:0xf\n" \
  "v_cndmask_b32 v225, v184, v185, s[46:47]\n" \
  "v_cndmask_b32 v226, v186, v187, s[46:47]\n" \
  "ds_write_b32 v2, v224 offset:0\n" \
  "v_cndmask_b32 v225, v225, v226, s[56:57]\n" \
  "ds_write_b32 v0, v225 offset:0\n" \
  "s_waitcnt lgkmcnt(0)\n" \
  "s_barrier\n" \
  "ds_read_b128 v[160:163], v3 offset:0\n" \
  "ds_read_b128 v[164:167], v3 offset:16\n" \
  "ds_read_b128 v[168:171], v3 offset:32\n" \
  "s_waitcnt lgkmcnt(0)\n" \
  "v_pk_add_f32 v[160:161], v[160:161], v[162:163]\n" \
  "v_pk_add_f32 v[164:165], v[164:165], v[166:167]\n" \
  "v_pk_add_f32 v[168:169], v[168:169], v[170:171]\n" \
  "v_add_f32 v160, v160, v161\n" \
  "v_pk_add_f32 v[164:165], v[164:165], v[168:169]\n" \
  "v_mul_f32 v160, s76, v160\n" \
  "v_add_f32 v161, v164, v165\n" \
  "v_mul_f32 v161, s76, v161\n" \
  "v_fma_f32 v161, -v160, v160, v161\n" \
  "v_max_f32 v161, 0, v161\n" \
  "s_waitcnt vmcnt(12)\n" \
  "s_cmp_eq_u32 s95, 0\n" \
  "s_cbranch_scc1 Lr_pb3_%=\n" \
  "buffer_store_dword v7, v5, s[36:39], s60 offen offset:0 sc0 sc1\n" \
  "s_mov_b32 s95, 0\n" \
  "Lr_pb3_%=:\n" \
  "v_add_f32 v161, s77, v161\n" \
  "v_rsq_f32 v228, v161\n" \
  "s_nop 0\n" \
  "v_mul_f32_e64 v230, -v160, v228\n" \
  "v_mov_b32 v229, v228\n" \
  "v_mov_b32 v231, v230\n" \
  "v_lshlrev_b32 v160, 16, v64\n" \
  "v_and_b32 v161, 0xffff0000, v64\n" \
  "v_pk_fma_f32 v[162:163], v[128:129], v[228:229], v[230:231]\n" \
  "v_pk_fma_f32 v[128:129], v[162:163], v[160:161], v[128:129]\n" \
  "v_lshlrev_b32 v164, 16, v65\n" \
  "v_and_b32 v165, 0xffff0000, v65\n" \
  "v_pk_fma_f32 v[166:167], v[130:131], v[228:229], v[230:231]\n" \
  "v_pk_fma_f32 v[130:131], v[166:167], v[164:165], v[130:131]\n" \
  "v_lshlrev_b32 v168, 16, v66\n" \
  "v_and_b32 v169, 0xffff0000, v66\n" \
  "v_pk_fma_f32 v[170:171], v[132:133], v[228:229], v[230:231]\n" \
  "v_pk_fma_f32 v[132:133], v[170:171], v[168:169], v[132:133]\n" \
  "v_lshlrev_b32 v172, 16, v67\n" \
  "v_and_b32 v173, 0xffff0000, v67\n" \
  "v_pk_fma_f32 v[174:175], v[134:135], v[228:229], v[230:231]\n" \
  "v_pk_fma_f32 v[134:135], v[174:175], v[172:173], v[134:135]\n" \
  "v_lshlrev_b32 v160, 16, v68\n" \
  "v_and_b32 v161, 0xffff0000, v68\n" \
  "v_pk_fma_f32 v[162:163], v[136:137], v[228:229], v[230:231]\n" \
  "v_pk_fma_f32 v[136:137], v[162:163], v[160:161], v[136:137]\n" \
  "v_lshlrev_b32 v164, 16, v69\n" \
  "v_and_b32 v165, 0xffff0000, v69\n" \
  "v_pk_fma_f32 v[166:167], v[138:139], v[228:229], v[230:231]\n" \
  "v_pk_fma_f32 v[138:139], v[166:167], v[164:165], v[138:139]\n" \
  "v_lshlrev_b32 v168, 16, v70\n" \
  "v_and_b32 v169, 0xffff0000, v70\n" \
  "v_pk_fma_f32 v[170:171], v[140:141], v[228:229], v[230:231]\n" \
  "v_pk_fma_f32 v[140:141], v[170:171], v[168:169], v[140:141]\n" \
  "v_lshlrev_b32 v172, 16, v71\n" \
  "v_and_b32 v173, 0xffff0000, v71\n" \
  "v_pk_fma_f32 v[174:175], v[142:143], v[228:229], v[230:231]\n" \
  "v_pk_fma_f32 v[142:143], v[174:175], v[172:173], v[142:143]\n" \
  "v_lshlrev_b32 v160, 16, v72\n" \
  "v_and_b32 v161, 0xffff0000, v72\n" \
  "v_pk_fma_f32 v[162:163], v[144:145], v[228:229], v[230:231]\n" \
  "v_pk_fma_f32 v[144:145], v[162:163], v[160:161], v[144:145]\n" \
  "v_lshlrev_b32 v164, 16, v73\n" \
  "v_and_b32 v165, 0xffff0000, v73\n" \
  "v_pk_fma_f32 v[166:167], v[146:147], v[228:229], v[230:231]\n" \
  "v_pk_fma_f32 v[146:147], v[166:167], v[164:165], v[146:147]\n" \
  "v_lshlrev_b32 v168, 16, v74\n" \
  "v_and_b32 v169, 0xffff0000, v74\n" \
  "v_pk_fma_f32 v[170:171], v[148:149], v[228:229], v[230:231]\n" \
  "v_pk_fma_f32 v[148:149], v[170:171], v[168:169], v[148:149]\n" \
  "v_lshlrev_b32 v172, 16, v75\n" \
  "v_and_b32 v173, 0xffff0000, v75\n" \
  "v_pk_fma_f32 v[174:175], v[150:151], v[228:229], v[230:231]\n" \
  "v_pk_fma_f32 v[150:151], v[174:175], v[172:173], v[150:151]\n" \
  "v_lshlrev_b32 v160, 16, v76\n" \
  "v_and_b32 v161, 0xffff0000, v76\n" \
  "v_pk_fma_f32 v[162:163], v[152:153], v[228:229], v[230:231]\n" \
  "v_pk_fma_f32 v[152:153], v[162:163], v[160:161], v[152:153]\n" \
  "v_lshlrev_b32 v164, 16, v77\n" \
  "v_and_b32 v165, 0xffff0000, v77\n" \
  "v_pk_fma_f32 v[166:167], v[154:155], v[228:229], v[230:231]\n" \
  "v_pk_fma_f32 v[154:155], v[166:167], v[164:165], v[154:155]\n" \
  "v_lshlrev_b32 v168, 16, v78\n" \
  "v_and_b32 v169, 0xffff0000, v78\n" \
  "v_pk_fma_f32 v[170:171], v[156:157], v[228:229], v[230:231]\n" \
  "v_pk_fma_f32 v[156:157], v[170:171], v[168:169], v[156:157]\n" \
  "v_lshlrev_b32 v172, 16, v79\n" \
  "v_and_b32 v173, 0xffff0000, v79\n" \
  "v_pk_fma_f32 v[174:175], v[158:159], v[228:229], v[230:231]\n" \
  "v_pk_fma_f32 v[158:159], v[174:175], v[172:173], v[158:159]\n" \
  "buffer_load_dword v8, v6, s[36:39], s69 offen offset:0 sc1\n" \
  "v_cvt_pk_bf16_f32 v208, v128, v129\n" \
  "v_cvt_pk_bf16_f32 v209, v130, v131\n" \
  "v_cvt_pk_bf16_f32 v210, v132, v133\n" \
  "v_cvt_pk_bf16_f32 v211, v134, v135\n" \
  "s_nop 1\n" \
  "v_mfma_f32_16x16x32_bf16 v[180:183], v[176:179], v[208:211], 0\n" \
  "v_mfma_f32_16x16x32_bf16 v[184:187], v[208:211], v[208:211], 0\n" \
  "buffer_store_dwordx4 v[208:211], v1, s[36:39], s88 offen offset:0 sc0 sc1\n" \
  "v_cvt_pk_bf16_f32 v212, v136, v137\n" \
  "v_cvt_pk_bf16_f32 v213, v138, v139\n" \
  "v_cvt_pk_bf16_f32 v214, v140, v141\n" \
  "v_cvt_pk_bf16_f32 v215, v142, v143\n" \
  "s_nop 1\n" \
  "v_mfma_f32_16x16x32_bf16 v[180:183], v[176:179], v[212:215], v[180:183]\n" \
  "v_mfma_f32_16x16x32_bf16 v[184:187], v[212:215], v[212:215], v[184:187]\n" \
  "buffer_store_dwordx4 v[212:215], v1, s[36:39], s88 offen offset:2048 sc0 sc1\n" \
  "v_cvt_pk_bf16_f32 v216, v144, v145\n" \
  "v_cvt_pk_bf16_f32 v217, v146, v147\n" \
  "v_cvt_pk_bf16_f32 v218, v148, v149\n" \
  "v_cvt_pk_bf16_f32 v219, v150, v151\n" \
  "s_nop 1\n" \
  "v_mfma_f32_16x16x32_bf16 v[180:183], v[176:179], v[216:219], v[180:183]\n" \
  "v_mfma_f32_16x16x32_bf16 v[184:187], v[216:219], v[216:219], v[184:187]\n" \
  "s_add_u32 s88, s88, 4096\n" \
  "buffer_store_dwordx4 v[216:219], v1, s[36:39], s88 offen offset:0 sc0 sc1\n" \
  "v_cvt_pk_bf16_f32 v220, v152, v153\n" \
  "v_cvt_pk_bf16_f32 v221, v154, v155\n" \
  "v_cvt_pk_bf16_f32 v222, v156, v157\n" \
  "v_cvt_pk_bf16_f32 v223, v158, v159\n" \
  "s_nop 1\n" \
  "v_mfma_f32_16x16x32_bf16 v[180:183], v[176:179], v[220:223], v[180:183]\n" \
  "v_mfma_f32_16x16x32_bf16 v[184:187], v[220:223], v[220:223], v[184:187]\n" \
  "buffer_store_dwordx4 v[220:223], v1, s[36:39], s88 offen offset:2048 sc0 sc1\n" \
  "s_nop 5\n" \
  "v_add_f32_dpp v224, v180, v180 row_ror:8 row_mask:0xf bank_mask:0xf\n" \
  "v_cndmask_b32 v225, v184, v185, s[46:47]\n" \
  "v_cndmask_b32 v226, v186, v187, s[46:47]\n" \
  "ds_write_b32 v2, v224 offset:1024\n" \
  "v_cndmask_b32 v225, v225, v226, s[56:57]\n" \
  "ds_write_b32 v0, v225 offset:1024\n" \
  "s_waitcnt lgkmcnt(0)\n" \
  "s_barrier\n" \
  "ds_read_b128 v[160:163], v3 offset:1024\n" \
  "ds_read_b128 v[164:167], v3 offset:1040\n" \
  "ds_read_b128 v[168:171], v3 offset:1056\n" \
  "s_waitcnt lgkmcnt(0)\n" \
  "v_pk_add_f32 v[160:161], v[160:161], v[162:163]\n" \
  "v_pk_add_f32 v[164:165], v[164:165], v[166:167]\n" \
  "v_pk_add_f32 v[168:169], v[168:169], v[170:171]\n" \
  "v_add_f32 v160, v160, v161\n" \
  "v_pk_add_f32 v[164:165], v[164:165], v[168:169]\n" \
  "v_mul_f32 v160, s76, v160\n" \
  "v_add_f32 v161, v164, v165\n" \
  "v_mul_f32 v161, s76, v161\n" \
  "v_fma_f32 v161, -v160, v160, v161\n" \
  "v_max_f32 v161, 0, v161\n" \
  "v_add_f32 v162, s77, v161\n" \
  "v_fma_f32 v163, s77, v162, v161\n" \
  "v_rsq_f32 v173, v162\n" \
  "v_sqrt_f32 v171, v163\n" \
  "v_rsq_f32 v162, v163\n" \
  "v_mov_b32 v168, v160\n" \
  "v_mul_f32_e64 v172, -v160, v173\n" \
  "v_mul_f32 v169, s78, v162\n" \
  "v_mul_f32 v163, v162, v162\n" \
  "v_mul_f32 v170, s79, v163\n" \
  "v_mul_f32 v170, v170, v169\n" \
  "buffer_store_dwordx4 v[168:171], v4, s[36:39], s89 offen offset:0 sc0 sc1\n" \
  "buffer_store_dwordx2 v[172:173], v4, s[36:39], s89 offen offset:16 sc0 sc1\n" \
  "s_mov_b32 s60, s90\n" \
  "s_mov_b32 s95, 1\n" \
  "s_waitcnt vmcnt(6)\n" \
  "s_branch Lr_it1_%=\n" \
  "Lr_tb0_%=:\n" \
  "v_add_f32 v161, s77, v161\n" \
  "v_rsq_f32 v228, v161\n" \
  "s_nop 0\n" \
  "v_mul_f32_e64 v230, -v160, v228\n" \
  "v_mov_b32 v229, v228\n" \
  "v_mov_b32 v231, v230\n" \
  "s_add_u32 s63, s87, 71303168\n" \
  "buffer_load_dwordx4 v[80:83], v1, s[36:39], s63 offen offset:0 sc1\n" \
  "v_lshlrev_b32 v160, 16, v64\n" \
  "v_and_b32 v161, 0xffff0000, v64\n" \
  "v_pk_fma_f32 v[162:163], v[128:129], v[228:229], v[230:231]\n" \
  "v_pk_fma_f32 v[128:129], v[162:163], v[160:161], v[128:129]\n" \
  "v_lshlrev_b32 v164, 16, v65\n" \
  "buffer_load_dwordx4 v[84:87], v1, s[36:39], s63 offen offset:2048 sc1\n" \
  "v_and_b32 v165, 0xffff0000, v65\n" \
  "v_pk_fma_f32 v[166:167], v[130:131], v[228:229], v[230:231]\n" \
  "v_pk_fma_f32 v[130:131], v[166:167], v[164:165], v[130:131]\n" \
  "v_lshlrev_b32 v168, 16, v66\n" \
  "v_and_b32 v169, 0xffff0000, v66\n" \
  "s_add_u32 s63, s87, 71307264\n" \
  "buffer_load_dwordx4 v[88:91], v1, s[36:39], s63 offen offset:0 sc1\n" \
  "v_pk_fma_f32 v[170:171], v[132:133], v[228:229], v[230:231]\n" \
  "v_pk_fma_f32 v[132:133], v[170:171], v[168:169], v[132:133]\n" \
  "v_lshlrev_b32 v172, 16, v67\n" \
  "v_and_b32 v173, 0xffff0000, v67\n" \
  "v_pk_fma_f32 v[174:175], v[134:135], v[228:229], v[230:231]\n" \
  "buffer_load_dwordx4 v[92:95], v1, s[36:39], s63 offen offset:2048 sc1\n" \
  "v_pk_fma_f32 v[134:135], v[174:175], v[172:173], v[134:135]\n" \
  "v_lshlrev_b32 v160, 16, v68\n" \
  "v_and_b32 v161, 0xffff0000, v68\n" \
  "v_pk_fma_f32 v[162:163], v[136:137], v[228:229], v[230:231]\n" \
  "v_pk_fma_f32 v[136:137], v[162:163], v[160:161], v[136:137]\n" \
  "s_add_u32 s63, s87, 89128960\n" \
  "buffer_load_dwordx4 v[96:99], v1, s[36:39], s63 offen offset:0 sc1\n" \
  "v_lshlrev_b32 v164, 16, v69\n" \
  "v_and_b32 v165, 0xffff0000, v69\n" \
  "v_pk_fma_f32 v[166:167], v[138:139], v[228:229], v[230:231]\n" \
  "v_pk_fma_f32 v[138:139], v[166:167], v[164:165], v[138:139]\n" \
  "v_lshlrev_b32 v168, 16, v70\n" \
  "buffer_load_dwordx4 v[100:103], v1, s[36:39], s63 offen offset:2048 sc1\n" \
  "v_and_b32 v169, 0xffff0000, v70\n" \
  "v_pk_fma_f32 v[170:171], v[140:141], v[228:229], v[230:231]\n" \
  "v_pk_fma_f32 v[140:141], v[170:171], v[168:169], v[140:141]\n" \
  "v_lshlrev_b32 v172, 16, v71\n" \
  "v_and_b32 v173, 0xffff0000, v71\n" \
  "s_add_u32 s63, s87, 89133056\n" \
  "buffer_load_dwordx4 v[104:107], v1, s[36:39], s63 offen offset:0 sc1\n" \
  "v_pk_fma_f32 v[174:175], v[142:143], v[228:229], v[230:231]\n" \
  "v_pk_fma_f32 v[142:143], v[174:175], v[172:173], v[142:143]\n" \
  "v_lshlrev_b32 v160, 16, v72\n" \
  "v_and_b32 v161, 0xffff0000, v72\n" \
  "v_pk_fma_f32 v[162:163], v[144:145], v[228:229], v[230:231]\n" \
  "buffer_load_dwordx4 v[108:111], v1, s[36:39], s63 offen offset:2048 sc1\n" \
  "v_pk_fma_f32 v[144:145], v[162:163], v[160:161], v[144:145]\n" \
  "v_lshlrev_b32 v164, 16, v73\n" \
  "v_and_b32 v165, 0xffff0000, v73\n" \
  "v_pk_fma_f32 v[166:167], v[146:147], v[228:229], v[230:231]\n" \
  "v_pk_fma_f32 v[146:147], v[166:167], v[164:165], v[146:147]\n" \
  "s_add_u32 s63, s87, 8388608\n" \
  "buffer_load_dwordx4 v[112:115], v1, s[36:39], s63 offen offset:0 sc1\n" \
  "v_lshlrev_b32 v168, 16, v74\n" \
  "v_and_b32 v169, 0xffff0000, v74\n" \
  "v_pk_fma_f32 v[170:171], v[148:149], v[228:229], v[230:231]\n" \
  "v_pk_fma_f32 v[148:149], v[170:171], v[168:169], v[148:149]\n" \
  "v_lshlrev_b32 v172, 16, v75\n" \
  "buffer_load_dwordx4 v[116:119], v1, s[36:39], s63 offen offset:2048 sc1\n" \
  "v_and_b32 v173, 0xffff0000, v75\n" \
  "v_pk_fma_f32 v[174:175], v[150:151], v[228:229], v[230:231]\n" \
  "v_pk_fma_f32 v[150:151], v[174:175], v[172:173], v[150:151]\n" \
  "v_lshlrev_b32 v160, 16, v76\n" \
  "v_and_b32 v161, 0xffff0000, v76\n" \
  "s_add_u32 s63, s87, 8392704\n" \
  "buffer_load_dwordx4 v[120:123], v1, s[36:39], s63 offen offset:0 sc1\n" \
  "v_pk_fma_f32 v[162:163], v[152:153], v[228:229], v[230:231]\n" \
  "v_pk_fma_f32 v[152:153], v[162:163], v[160:161], v[152:153]\n" \
  "v_lshlrev_b32 v164, 16, v77\n" \
  "v_and_b32 v165, 0xffff0000, v77\n" \
  "v_pk_fma_f32 v[166:167], v[154:155], v[228:229], v[230:231]\n" \
  "buffer_load_dwordx4 v[124:127], v1, s[36:39], s63 offen offset:2048 sc1\n" \
  "v_pk_fma_f32 v[154:155], v[166:167], v[164:165], v[154:155]\n" \
  "v_lshlrev_b32 v168, 16, v78\n" \
  "v_and_b32 v169, 0xffff0000, v78\n" \
  "v_pk_fma_f32 v[170:171], v[156:157], v[228:229], v[230:231]\n" \
  "v_pk_fma_f32 v[156:157], v[170:171], v[168:169], v[156:157]\n" \
  "v_lshlrev_b32 v172, 16, v79\n" \
  "v_and_b32 v173, 0xffff0000, v79\n" \
  "v_pk_fma_f32 v[174:175], v[158:159], v[228:229], v[230:231]\n" \
  "v_pk_fma_f32 v[158:159], v[174:175], v[172:173], v[158:159]\n" \
  "buffer_load_dword v8, v6, s[36:39], s69 offen offset:0 sc1\n" \
  "v_cvt_pk_bf16_f32 v208, v128, v129\n" \
  "v_cvt_pk_bf16_f32 v209, v130, v131\n" \
  "v_cvt_pk_bf16_f32 v210, v132, v133\n" \
  "v_cvt_pk_bf16_f32 v211, v134, v135\n" \
  "s_nop 1\n" \
  "v_mfma_f32_16x16x32_bf16 v[180:183], v[176:179], v[208:211], 0\n" \
  "v_mfma_f32_16x16x32_bf16 v[184:187], v[208:211], v[208:211], 0\n" \
  "buffer_store_dwordx4 v[208:211], v1, s[36:39], s88 offen offset:0 sc0 sc1\n" \
  "v_cvt_pk_bf16_f32 v212, v136, v137\n" \
  "v_cvt_pk_bf16_f32 v213, v138, v139\n" \
  "v_cvt_pk_bf16_f32 v214, v140, v141\n" \
  "v_cvt_pk_bf16_f32 v215, v142, v143\n" \
  "s_nop 1\n" \
  "v_mfma_f32_16x16x32_bf16 v[180:183], v[176:179], v[212:215], v[180:183]\n" \
  "v_mfma_f32_16x16x32_bf16 v[184:187], v[212:215], v[212:215], v[184:187]\n" \
  "buffer_store_dwordx4 v[212:215], v1, s[36:39], s88 offen offset:2048 sc0 sc1\n" \
  "v_cvt_pk_bf16_f32 v216, v144, v145\n" \
  "v_cvt_pk_bf16_f32 v217, v146, v147\n" \
  "v_cvt_pk_bf16_f32 v218, v148, v149\n" \
  "v_cvt_pk_bf16_f32 v219, v150, v151\n" \
  "s_nop 1\n" \
  "v_mfma_f32_16x16x32_bf16 v[180:183], v[176:179], v[216:219], v[180:183]\n" \
  "v_mfma_f32_16x16x32_bf16 v[184:187], v[216:219], v[216:219], v[184:187]\n" \
  "s_add_u32 s88, s88, 4096\n" \
  "buffer_store_dwordx4 v[216:219], v1, s[36:39], s88 offen offset:0 sc0 sc1\n" \
  "v_cvt_pk_bf16_f32 v220, v152, v153\n" \
  "v_cvt_pk_bf16_f32 v221, v154, v155\n" \
  "v_cvt_pk_bf16_f32 v222, v156, v157\n" \
  "v_cvt_pk_bf16_f32 v223, v158, v159\n" \
  "s_nop 1\n" \
  "v_mfma_f32_16x16x32_bf16 v[180:183], v[176:179], v[220:223], v[180:183]\n" \
  "v_mfma_f32_16x16x32_bf16 v[184:187], v[220:223], v[220:223], v[184:187]\n" \
  "buffer_store_dwordx4 v[220:223], v1, s[36:39], s88 offen offset:2048 sc0 sc1\n" \
  "s_nop 5\n" \
  "v_add_f32_dpp v224, v180, v180 row_ror:8 row_mask:0xf bank_mask:0xf\n" \
  "v_cndmask_b32 v225, v184, v185, s[46:47]\n" \
  "v_cndmask_b32 v226, v186, v187, s[46:47]\n" \
  "ds_write_b32 v2, v224 offset:1024\n" \
  "v_cndmask_b32 v225, v225, v226, s[56:57]\n" \
  "ds_write_b32 v0, v225 offset:1024\n" \
  "s_waitcnt lgkmcnt(0)\n" \
  "s_barrier\n" \
  "ds_read_b128 v[160:163], v3 offset:1024\n" \
  "ds_read_b128 v[164:167], v3 offset:1040\n" \
  "ds_read_b128 v[168:171], v3 offset:1056\n" \
  "s_waitcnt lgkmcnt(0)\n" \
  "v_pk_add_f32 v[160:161], v[160:161], v[162:163]\n" \
  "v_pk_add_f32 v[164:165], v[164:165], v[166:167]\n" \
  "v_pk_add_f32 v[168:169], v[168:169], v[170:171]\n" \
  "v_add_f32 v160, v160, v161\n" \
  "v_pk_add_f32 v[164:165], v[164:165], v[168:169]\n" \
  "v_mul_f32 v160, s76, v160\n" \
  "v_add_f32 v161, v164, v165\n" \
  "v_mul_f32 v161, s76, v161\n" \
  "v_fma_f32 v161, -v160, v160, v161\n" \
  "v_max_f32 v161, 0, v161\n" \
  "v_add_f32 v162, s77, v161\n" \
  "v_fma_f32 v163, s77, v162, v161\n" \
  "v_rsq_f32 v173, v162\n" \
  "v_sqrt_f32 v171, v163\n" \
  "v_rsq_f32 v162, v163\n" \
  "v_mov_b32 v168, v160\n" \
  "v_mul_f32_e64 v172, -v160, v173\n" \
  "v_mul_f32 v169, s78, v162\n" \
  "v_mul_f32 v163, v162, v162\n" \
  "v_mul_f32 v170, s79, v163\n" \
  "v_mul_f32 v170, v170, v169\n" \
  "buffer_store_dwordx4 v[168:171], v4, s[36:39], s89 offen offset:0 sc0 sc1\n" \
  "buffer_store_dwordx2 v[172:173], v4, s[36:39], s89 offen offset:16 sc0 sc1\n" \
  "s_mov_b32 s60, s90\n" \
  "s_mov_b32 s95, 1\n" \
  "s_waitcnt vmcnt(6)\n" \
  "s_branch Lr_it1_%=\n" \
  "Lr_tc0_%=:\n" \
  "s_add_u32 s63, s87, 71303168\n" \
  "buffer_load_dwordx4 v[80:83], v1, s[36:39], s63 offen offset:0 sc1\n" \
  "buffer_load_dwordx4 v[84:87], v1, s[36:39], s63 offen offset:2048 sc1\n" \
  "s_add_u32 s63, s87, 71307264\n" \
  "buffer_load_dwordx4 v[88:91], v1, s[36:39], s63 offen offset:0 sc1\n" \
  "buffer_load_dwordx4 v[92:95], v1, s[36:39], s63 offen offset:2048 sc1\n" \
  "s_add_u32 s63, s87, 89128960\n" \
  "buffer_load_dwordx4 v[96:99], v1, s[36:39], s63 offen offset:0 sc1\n" \
  "buffer_load_dwordx4 v[100:103], v1, s[36:39], s63 offen offset:2048 sc1\n" \
  "s_add_u32 s63, s87, 89133056\n" \
  "buffer_load_dwordx4 v[104:107], v1, s[36:39], s63 offen offset:0 sc1\n" \
  "buffer_load_dwordx4 v[108:111], v1, s[36:39], s63 offen offset:2048 sc1\n" \
  "s_add_u32 s63, s87, 8388608\n" \
  "buffer_load_dwordx4 v[112:115], v1, s[36:39], s63 offen offset:0 sc1\n" \
  "buffer_load_dwordx4 v[116:119], v1, s[36:39], s63 offen offset:2048 sc1\n" \
  "s_add_u32 s63, s87, 8392704\n" \
  "buffer_load_dwordx4 v[120:123], v1, s[36:39], s63 offen offset:0 sc1\n" \
  "buffer_load_dwordx4 v[124:127], v1, s[36:39], s63 offen offset:2048 sc1\n" \
  "buffer_load_dword v8, v6, s[36:39], s69 offen offset:0 sc1\n" \
  "s_waitcnt vmcnt(13)\n" \
  "s_cmp_eq_u32 s95, 0\n" \
  "s_cbranch_scc1 Lr_pb4_%=\n" \
  "buffer_store_dword v7, v5, s[36:39], s60 offen offset:0 sc0 sc1\n" \
  "s_mov_b32 s95, 0\n" \
  "Lr_pb4_%=:\n" \
  "s_waitcnt vmcnt(0)\n" \
  "s_branch Lr_it1_%=\n" \
  "Lr_it1_%=:\n" \
  "s_mov_b32 s88, s58\n" \
  "s_mov_b32 s89, s59\n" \
  "s_mov_b32 s90, s49\n" \
  "s_mov_b32 s54, s70\n" \
  "s_lshr_b32 s63, s54, 2\n" \
  "s_and_b32 s64, s54, 1\n" \
  "s_lshl_b32 s64, s64, 7\n" \
  "s_lshl_b32 s87, s63, 16\n" \
  "s_add_u32 s87, s87, s64\n" \
  "s_and_b32 s64, s54, 2\n" \
  "s_lshl_b32 s62, s64, 14\n" \
  "s_add_u32 s87, s87, s62\n" \
  "s_add_u32 s58, s87, 16908288\n" \
  "s_lshl_b32 s59, s54, 8\n" \
  "s_add_u32 s59, s59, 107022336\n" \
  "s_lshl_b32 s49, s54, 9\n" \
  "s_add_u32 s49, s49, s92\n" \
  "s_lshr_b32 s63, s54, 1\n" \
  "s_lshl_b32 s91, s63, 11\n" \
  "s_add_u32 s91, s91, 109871104\n" \
  "s_add_u32 s70, s54, 18\n" \
  "s_add_u32 s63, s54, s50\n" \
  "s_cmp_lt_u32 s54, 0\n" \
  "s_cselect_b32 s70, s65, s70\n" \
  "s_cmp_lt_u32 s63, 0\n" \
  "s_cselect_b32 s70, s63, s70\n" \
  "s_lshr_b32 s63, s70, 1\n" \
  "s_lshl_b32 s69, s63, 11\n" \
  "s_add_u32 s69, s69, 109871104\n" \
  "v_cmp_eq_u32 vcc, s48, v8\n" \
  "s_nop 0\n" \
  "s_cmp_eq_u64 vcc, exec\n" \
  "s_cselect_b32 s63, s54, 256\n" \
  "s_cmp_lt_u32 s63, 256\n" \
  "s_cbranch_scc1 Lr_ta1_%=\n" \
  "buffer_load_dword v8, v6, s[36:39], s91 offen offset:0 sc1\n" \
  "v_dot2_f32_bf16 v128, v80, s51, 0\n" \
  "v_dot2_f32_bf16 v129, v80, s52, 0\n" \
  "v_dot2_f32_bf16 v130, v81, s51, 0\n" \
  "v_dot2_f32_bf16 v131, v81, s52, 0\n" \
  "v_dot2_f32_bf16 v132, v82, s51, 0\n" \
  "v_dot2_f32_bf16 v133, v82, s52, 0\n" \
  "v_dot2_f32_bf16 v134, v83, s51, 0\n" \
  "v_dot2_f32_bf16 v135, v83, s52, 0\n" \
  "v_dot2_f32_bf16 v128, v96, s51, v128\n" \
  "v_dot2_f32_bf16 v129, v96, s52, v129\n" \
  "v_dot2_f32_bf16 v130, v97, s51, v130\n" \
  "v_dot2_f32_bf16 v131, v97, s52, v131\n" \
  "v_dot2_f32_bf16 v132, v98, s51, v132\n" \
  "v_dot2_f32_bf16 v133, v98, s52, v133\n" \
  "v_dot2_f32_bf16 v134, v99, s51, v134\n" \
  "v_dot2_f32_bf16 v135, v99, s52, v135\n" \
  "v_dot2_f32_bf16 v136, v84, s51, 0\n" \
  "v_dot2_f32_bf16 v137, v84, s52, 0\n" \
  "v_dot2_f32_bf16 v138, v85, s51, 0\n" \
  "v_dot2_f32_bf16 v139, v85, s52, 0\n" \
  "v_dot2_f32_bf16 v140, v86, s51, 0\n" \
  "v_dot2_f32_bf16 v141, v86, s52, 0\n" \
  "v_dot2_f32_bf16 v142, v87, s51, 0\n" \
  "v_dot2_f32_bf16 v143, v87, s52, 0\n" \
  "v_dot2_f32_bf16 v136, v100, s51, v136\n" \
  "v_dot2_f32_bf16 v137, v100, s52, v137\n" \
  "v_dot2_f32_bf16 v138, v101, s51, v138\n" \
  "v_dot2_f32_bf16 v139, v101, s52, v139\n" \
  "v_dot2_f32_bf16 v140, v102, s51, v140\n" \
  "v_dot2_f32_bf16 v141, v102, s52, v141\n" \
  "v_dot2_f32_bf16 v142, v103, s51, v142\n" \
  "v_dot2_f32_bf16 v143, v103, s52, v143\n" \
  "v_cvt_pk_bf16_f32 v208, v128, v129\n" \
  "v_cvt_pk_bf16_f32 v209, v130, v131\n" \
  "v_cvt_pk_bf16_f32 v210, v132, v133\n" \
  "v_cvt_pk_bf16_f32 v211, v134, v135\n" \
  "s_nop 1\n" \
  "v_mfma_f32_16x16x32_bf16 v[180:183], v[176:179], v[208:211], 0\n" \
  "v_mfma_f32_16x16x32_bf16 v[184:187], v[208:211], v[208:211], 0\n" \
  "v_dot2_f32_bf16 v144, v88, s51, 0\n" \
  "v_dot2_f32_bf16 v145, v88, s52, 0\n" \
  "v_dot2_f32_bf16 v146, v89, s51, 0\n" \
  "v_dot2_f32_bf16 v147, v89, s52, 0\n" \
  "v_dot2_f32_bf16 v148, v90, s51, 0\n" \
  "v_dot2_f32_bf16 v149, v90, s52, 0\n" \
  "v_dot2_f32_bf16 v150, v91, s51, 0\n" \
  "v_dot2_f32_bf16 v151, v91, s52, 0\n" \
  "v_dot2_f32_bf16 v144, v104, s51, v144\n" \
  "v_dot2_f32_bf16 v145, v104, s52, v145\n" \
  "v_dot2_f32_bf16 v146, v105, s51, v146\n" \
  "v_dot2_f32_bf16 v147, v105, s52, v147\n" \
  "v_dot2_f32_bf16 v148, v106, s51, v148\n" \
  "v_dot2_f32_bf16 v149, v106, s52, v149\n" \
  "v_dot2_f32_bf16 v150, v107, s51, v150\n" \
  "v_dot2_f32_bf16 v151, v107, s52, v151\n" \
  "v_cvt_pk_bf16_f32 v212, v136, v137\n" \
  "v_cvt_pk_bf16_f32 v213, v138, v139\n" \
  "v_cvt_pk_bf16_f32 v214, v140, v141\n" \
  "v_cvt_pk_bf16_f32 v215, v142, v143\n" \
  "s_nop 1\n" \
  "v_mfma_f32_16x16x32_bf16 v[180:183], v[176:179], v[212:215], v[180:183]\n" \
  "v_mfma_f32_16x16x32_bf16 v[184:187], v[212:215], v[212:215], v[184:187]\n" \
  "v_dot2_f32_bf16 v152, v92, s51, 0\n" \
  "v_dot2_f32_bf16 v153, v92, s52, 0\n" \
  "v_dot2_f32_bf16 v154, v93, s51, 0\n" \
  "v_dot2_f32_bf16 v155, v93, s52, 0\n" \
  "v_dot2_f32_bf16 v156, v94, s51, 0\n" \
  "v_dot2_f32_bf16 v157, v94, s52, 0\n" \
  "v_dot2_f32_bf16 v158, v95, s51, 0\n" \
  "v_dot2_f32_bf16 v159, v95, s52, 0\n" \
  "v_dot2_f32_bf16 v152, v108, s51, v152\n" \
  "v_dot2_f32_bf16 v153, v108, s52, v153\n" \
  "v_dot2_f32_bf16 v154, v109, s51, v154\n" \
  "v_dot2_f32_bf16 v155, v109, s52, v155\n" \
  "v_dot2_f32_bf16 v156, v110, s51, v156\n" \
  "v_dot2_f32_bf16 v157, v110, s52, v157\n" \
  "v_dot2_f32_bf16 v158, v111, s51, v158\n" \
  "v_dot2_f32_bf16 v159, v111, s52, v159\n" \
  "v_cvt_pk_bf16_f32 v216, v144, v145\n" \
  "v_cvt_pk_bf16_f32 v217, v146, v147\n" \
  "v_cvt_pk_bf16_f32 v218, v148, v149\n" \
  "v_cvt_pk_bf16_f32 v219, v150, v151\n" \
  "s_nop 1\n" \
  "v_mfma_f32_16x16x32_bf16 v[180:183], v[176:179], v[216:219], v[180:183]\n" \
  "v_mfma_f32_16x16x32_bf16 v[184:187], v[216:219], v[216:219], v[184:187]\n" \
  "v_cvt_pk_bf16_f32 v220, v152, v153\n" \
  "v_cvt_pk_bf16_f32 v221, v154, v155\n" \
  "v_cvt_pk_bf16_f32 v222, v156, v157\n" \
  "v_cvt_pk_bf16_f32 v223, v158, v159\n" \
  "s_nop 1\n" \
  "v_mfma_f32_16x16x32_bf16 v[180:183], v[176:179], v[220:223], v[180:183]\n" \
  "v_mfma_f32_16x16x32_bf16 v[184:187], v[220:223], v[220:223], v[184:187]\n" \
  "s_nop 6\n" \
  "v_add_f32_dpp v224, v180, v180 row_ror:8 row_mask:0xf bank_mask:0xf\n" \
  "v_cndmask_b32 v225, v184, v185, s[46:47]\n" \
  "v_cndmask_b32 v226, v186, v187, s[46:47]\n" \
  "ds_write_b32 v2, v224 offset:0\n" \
  "v_cndmask_b32 v225, v225, v226, s[56:57]\n" \
  "ds_write_b32 v0, v225 offset:0\n" \
  "s_waitcnt lgkmcnt(0)\n" \
  "s_barrier\n" \
  "ds_read_b128 v[160:163], v3 offset:0\n" \
  "ds_read_b128 v[164:167], v3 offset:16\n" \
  "ds_read_b128 v[168:171], v3 offset:32\n" \
  "s_waitcnt lgkmcnt(0)\n" \
  "v_pk_add_f32 v[160:161], v[160:161], v[162:163]\n" \
  "v_pk_add_f32 v[164:165], v[164:165], v[166:167]\n" \
  "v_pk_add_f32 v[168:169], v[168:169], v[170:171]\n" \
  "v_add_f32 v160, v160, v161\n" \
  "v_pk_add_f32 v[164:165], v[164:165], v[168:169]\n" \
  "v_mul_f32 v160, s76, v160\n" \
  "v_add_f32 v161, v164, v165\n" \
  "v_mul_f32 v161, s76, v161\n" \
  "v_fma_f32 v161, -v160, v160, v161\n" \
  "v_max_f32 v161, 0, v161\n" \
  "s_waitcnt vmcnt(0)\n" \
  "s_cmp_eq_u32 s95, 0\n" \
  "s_cbranch_scc1 Lr_pb5_%=\n" \
  "buffer_store_dword v7, v5, s[36:39], s60 offen offset:0 sc0 sc1\n" \
  "s_mov_b32 s95, 0\n" \
  "Lr_pb5_%=:\n" \
  "v_cmp_eq_u32 vcc, s48, v8\n" \
  "s_nop 0\n" \
  "s_cmp_eq_u64 vcc, exec\n" \
  "s_cselect_b32 s63, s54, 256\n" \
  "s_cmp_lt_u32 s63, 256\n" \
  "s_cbranch_scc1 Lr_tb1_%=\n" \
  "buffer_load_dword v8, v6, s[36:39], s91 offen offset:0 sc1\n" \
  "v_add_f32 v161, s77, v161\n" \
  "v_rsq_f32 v228, v161\n" \
  "s_nop 0\n" \
  "v_mul_f32_e64 v230, -v160, v228\n" \
  "v_mov_b32 v229, v228\n" \
  "v_mov_b32 v231, v230\n" \
  "v_lshlrev_b32 v160, 16, v112\n" \
  "v_and_b32 v161, 0xffff0000, v112\n" \
  "v_pk_fma_f32 v[162:163], v[128:129], v[228:229], v[230:231]\n" \
  "v_pk_fma_f32 v[128:129], v[162:163], v[160:161], v[128:129]\n" \
  "v_lshlrev_b32 v164, 16, v113\n" \
  "v_and_b32 v165, 0xffff0000, v113\n" \
  "v_pk_fma_f32 v[166:167], v[130:131], v[228:229], v[230:231]\n" \
  "v_pk_fma_f32 v[130:131], v[166:167], v[164:165], v[130:131]\n" \
  "v_lshlrev_b32 v168, 16, v114\n" \
  "v_and_b32 v169, 0xffff0000, v114\n" \
  "v_pk_fma_f32 v[170:171], v[132:133], v[228:229], v[230:231]\n" \
  "v_pk_fma_f32 v[132:133], v[170:171], v[168:169], v[132:133]\n" \
  "v_lshlrev_b32 v172, 16, v115\n" \
  "v_and_b32 v173, 0xffff0000, v115\n" \
  "v_pk_fma_f32 v[174:175], v[134:135], v[228:229], v[230:231]\n" \
  "v_pk_fma_f32 v[134:135], v[174:175], v[172:173], v[134:135]\n" \
  "v_lshlrev_b32 v160, 16, v116\n" \
  "v_and_b32 v161, 0xffff0000, v116\n" \
  "v_pk_fma_f32 v[162:163], v[136:137], v[228:229], v[230:231]\n" \
  "v_pk_fma_f32 v[136:137], v[162:163], v[160:161], v[136:137]\n" \
  "v_lshlrev_b32 v164, 16, v117\n" \
  "v_and_b32 v165, 0xffff0000, v117\n" \
  "v_pk_fma_f32 v[166:167], v[138:139], v[228:229], v[230:231]\n" \
  "v_pk_fma_f32 v[138:139], v[166:167], v[164:165], v[138:139]\n" \
  "v_lshlrev_b32 v168, 16, v118\n" \
  "v_and_b32 v169, 0xffff0000, v118\n" \
  "v_pk_fma_f32 v[170:171], v[140:141], v[228:229], v[230:231]\n" \
  "v_pk_fma_f32 v[140:141], v[170:171], v[168:169], v[140:141]\n" \
  "v_lshlrev_b32 v172, 16, v119\n" \
  "v_and_b32 v173, 0xffff0000, v119\n" \
  "v_pk_fma_f32 v[174:175], v[142:143], v[228:229], v[230:231]\n" \
  "v_pk_fma_f32 v[142:143], v[174:175], v[172:173], v[142:143]\n" \
  "v_lshlrev_b32 v160, 16, v120\n" \
  "v_and_b32 v161, 0xffff0000, v120\n" \
  "v_pk_fma_f32 v[162:163], v[144:145], v[228:229], v[230:231]\n" \
  "v_pk_fma_f32 v[144:145], v[162:163], v[160:161], v[144:145]\n" \
  "v_lshlrev_b32 v164, 16, v121\n" \
  "v_and_b32 v165, 0xffff0000, v121\n" \
  "v_pk_fma_f32 v[166:167], v[146:147], v[228:229], v[230:231]\n" \
  "v_pk_fma_f32 v[146:147], v[166:167], v[164:165], v[146:147]\n" \
  "v_lshlrev_b32 v168, 16, v122\n" \
  "v_and_b32 v169, 0xffff0000, v122\n" \
  "v_pk_fma_f32 v[170:171], v[148:149], v[228:229], v[230:231]\n" \
  "v_pk_fma_f32 v[148:149], v[170:171], v[168:169], v[148:149]\n" \
  "v_lshlrev_b32 v172, 16, v123\n" \
  "v_and_b32 v173, 0xffff0000, v123\n" \
  "v_pk_fma_f32 v[174:175], v[150:151], v[228:229], v[230:231]\n" \
  "v_pk_fma_f32 v[150:151], v[174:175], v[172:173], v[150:151]\n" \
  "v_lshlrev_b32 v160, 16, v124\n" \
  "v_and_b32 v161, 0xffff0000, v124\n" \
  "v_pk_fma_f32 v[162:163], v[152:153], v[228:229], v[230:231]\n" \
  "v_pk_fma_f32 v[152:153], v[162:163], v[160:161], v[152:153]\n" \
  "v_lshlrev_b32 v164, 16, v125\n" \
  "v_and_b32 v165, 0xffff0000, v125\n" \
  "v_pk_fma_f32 v[166:167], v[154:155], v[228:229], v[230:231]\n" \
  "v_pk_fma_f32 v[154:155], v[166:167], v[164:165], v[154:155]\n" \
  "v_lshlrev_b32 v168, 16, v126\n" \
  "v_and_b32 v169, 0xffff0000, v126\n" \
  "v_pk_fma_f32 v[170:171], v[156:157], v[228:229], v[230:231]\n" \
  "v_pk_fma_f32 v[156:157], v[170:171], v[168:169], v[156:157]\n" \
  "v_lshlrev_b32 v172, 16, v127\n" \
  "v_and_b32 v173, 0xffff0000, v127\n" \
  "v_pk_fma_f32 v[174:175], v[158:159], v[228:229], v[230:231]\n" \
  "v_pk_fma_f32 v[158:159], v[174:175], v[172:173], v[158:159]\n" \
  "v_cvt_pk_bf16_f32 v208, v128, v129\n" \
  "v_cvt_pk_bf16_f32 v209, v130, v131\n" \
  "v_cvt_pk_bf16_f32 v210, v132, v133\n" \
  "v_cvt_pk_bf16_f32 v211, v134, v135\n" \
  "s_nop 1\n" \
  "v_mfma_f32_16x16x32_bf16 v[180:183], v[176:179], v[208:211], 0\n" \
  "v_mfma_f32_16x16x32_bf16 v[184:187], v[208:211], v[208:211], 0\n" \
  "buffer_store_dwordx4 v[208:211], v1, s[36:39], s88 offen offset:0 sc0 sc1\n" \
  "v_cvt_pk_bf16_f32 v212, v136, v137\n" \
  "v_cvt_pk_bf16_f32 v213, v138, v139\n" \
  "v_cvt_pk_bf16_f32 v214, v140, v141\n" \
  "v_cvt_pk_bf16_f32 v215, v142, v143\n" \
  "s_nop 1\n" \
  "v_mfma_f32_16x16x32_bf16 v[180:183], v[176:179], v[212:215], v[180:183]\n" \
  "v_mfma_f32_16x16x32_bf16 v[184:187], v[212:215], v[212:215], v[184:187]\n" \
  "buffer_store_dwordx4 v[212:215], v1, s[36:39], s88 offen offset:2048 sc0 sc1\n" \
  "v_cvt_pk_bf16_f32 v216, v144, v145\n" \
  "v_cvt_pk_bf16_f32 v217, v146, v147\n" \
  "v_cvt_pk_bf16_f32 v218, v148, v149\n" \
  "v_cvt_pk_bf16_f32 v219, v150, v151\n" \
  "s_nop 1\n" \
  "v_mfma_f32_16x16x32_bf16 v[180:183], v[176:179], v[216:219], v[180:183]\n" \
  "v_mfma_f32_16x16x32_bf16 v[184:187], v[216:219], v[216:219], v[184:187]\n" \
  "s_add_u32 s88, s88, 4096\n" \
  "buffer_store_dwordx4 v[216:219], v1, s[36:39], s88 offen offset:0 sc0 sc1\n" \
  "v_cvt_pk_bf16_f32 v220, v152, v153\n" \
  "v_cvt_pk_bf16_f32 v221, v154, v155\n" \
  "v_cvt_pk_bf16_f32 v222, v156, v157\n" \
  "v_cvt_pk_bf16_f32 v223, v158, v159\n" \
  "s_nop 1\n" \
  "v_mfma_f32_16x16x32_bf16 v[180:183], v[176:179], v[220:223], v[180:183]\n" \
  "v_mfma_f32_16x16x32_bf16 v[184:187], v[220:223], v[220:223], v[184:187]\n" \
  "buffer_store_dwordx4 v[220:223], v1, s[36:39], s88 offen offset:2048 sc0 sc1\n" \
  "s_nop 5\n" \
  "v_add_f32_dpp v224, v180, v180 row_ror:8 row_mask:0xf bank_mask:0xf\n" \
  "v_cndmask_b32 v225, v184, v185, s[46:47]\n" \
  "v_cndmask_b32 v226, v186, v187, s[46:47]\n" \
  "ds_write_b32 v2, v224 offset:1024\n" \
  "v_cndmask_b32 v225, v225, v226, s[56:57]\n" \
  "ds_write_b32 v0, v225 offset:1024\n" \
  "s_waitcnt lgkmcnt(0)\n" \
  "s_barrier\n" \
  "ds_read_b128 v[160:163], v3 offset:1024\n" \
  "ds_read_b128 v[164:167], v3 offset:1040\n" \
  "ds_read_b128 v[168:171], v3 offset:1056\n" \
  "s_waitcnt lgkmcnt(0)\n" \
  "v_pk_add_f32 v[160:161], v[160:161], v[162:163]\n" \
  "v_pk_add_f32 v[164:165], v[164:165], v[166:167]\n" \
  "v_pk_add_f32 v[168:169], v[168:169], v[170:171]\n" \
  "v_add_f32 v160, v160, v161\n" \
  "v_pk_add_f32 v[164:165], v[164:165], v[168:169]\n" \
  "v_mul_f32 v160, s76, v160\n" \
  "v_add_f32 v161, v164, v165\n" \
  "v_mul_f32 v161, s76, v161\n" \
  "v_fma_f32 v161, -v160, v160, v161\n" \
  "v_max_f32 v161, 0, v161\n" \
  "v_add_f32 v162, s77, v161\n" \
  "v_fma_f32 v163, s77, v162, v161\n" \
  "v_rsq_f32 v173, v162\n" \
  "v_sqrt_f32 v171, v163\n" \
  "v_rsq_f32 v162, v163\n" \
  "v_mov_b32 v168, v160\n" \
  "v_mul_f32_e64 v172, -v160, v173\n" \
  "v_mul_f32 v169, s78, v162\n" \
  "v_mul_f32 v163, v162, v162\n" \
  "v_mul_f32 v170, s79, v163\n" \
  "v_mul_f32 v170, v170, v169\n" \
  "buffer_store_dwordx4 v[168:171], v4, s[36:39], s89 offen offset:0 sc0 sc1\n" \
  "buffer_store_dwordx2 v[172:173], v4, s[36:39], s89 offen offset:16 sc0 sc1\n" \
  "s_mov_b32 s60, s90\n" \
  "s_mov_b32 s95, 1\n" \
  "s_waitcnt vmcnt(6)\n" \
  "v_cmp_eq_u32 vcc, s48, v8\n" \
  "s_nop 0\n" \
  "s_cmp_eq_u64 vcc, exec\n" \
  "s_cselect_b32 s63, s54, 256\n" \
  "s_cmp_lt_u32 s63, 256\n" \
  "s_cbranch_scc1 Lr_tc1_%=\n" \
  "s_waitcnt vmcnt(0)\n" \
  "s_cmp_eq_u32 s95, 0\n" \
  "s_cbranch_scc1 Lr_pb6_%=\n" \
  "buffer_store_dword v7, v5, s[36:39], s60 offen offset:0 sc0 sc1\n" \
  "s_mov_b32 s95, 0\n" \
  "Lr_pb6_%=:\n" \
  "s_cmp_ge_u32 s54, 256\n" \
  "s_cbranch_scc1 Lr_done_%=\n" \
  "Lr_bp1_%=:\n" \
  "buffer_load_dword v8, v6, s[36:39], s91 offen offset:0 sc1\n" \
  "s_waitcnt vmcnt(0)\n" \
  "v_cmp_eq_u32 vcc, s48, v8\n" \
  "s_nop 0\n" \
  "s_cmp_eq_u64 vcc, exec\n" \
  "s_cselect_b32 s63, 1, s96\n" \
  "s_cmp_lg_u32 s63, 0\n" \
  "s_cbranch_scc1 Lr_bq1_%=\n" \
  "s_add_u32 s74, s74, 1\n" \
  "s_cmp_ge_u32 s74, 20000\n" \
  "s_cselect_b32 s96, 1, 0\n" \
  "s_sleep 4\n" \
  "s_branch Lr_bp1_%=\n" \
  "Lr_bq1_%=:\n" \
  "s_add_u32 s63, s87, 71303168\n" \
  "buffer_load_dwordx4 v[32:35], v1, s[36:39], s63 offen offset:0 sc1\n" \
  "buffer_load_dwordx4 v[36:39], v1, s[36:39], s63 offen offset:2048 sc1\n" \
  "s_add_u32 s63, s87, 71307264\n" \
  "buffer_load_dwordx4 v[40:43], v1, s[36:39], s63 offen offset:0 sc1\n" \
  "buffer_load_dwordx4 v[44:47], v1, s[36:39], s63 offen offset:2048 sc1\n" \
  "s_add_u32 s63, s87, 89128960\n" \
  "buffer_load_dwordx4 v[48:51], v1, s[36:39], s63 offen offset:0 sc1\n" \
  "buffer_load_dwordx4 v[52:55], v1, s[36:39], s63 offen offset:2048 sc1\n" \
  "s_add_u32 s63, s87, 89133056\n" \
  "buffer_load_dwordx4 v[56:59], v1, s[36:39], s63 offen offset:0 sc1\n" \
  "buffer_load_dwordx4 v[60:63], v1, s[36:39], s63 offen offset:2048 sc1\n" \
  "s_add_u32 s63, s87, 8388608\n" \
  "buffer_load_dwordx4 v[64:67], v1, s[36:39], s63 offen offset:0 sc1\n" \
  "buffer_load_dwordx4 v[68:71], v1, s[36:39], s63 offen offset:2048 sc1\n" \
  "s_add_u32 s63, s87, 8392704\n" \
  "buffer_load_dwordx4 v[72:75], v1, s[36:39], s63 offen offset:0 sc1\n" \
  "buffer_load_dwordx4 v[76:79], v1, s[36:39], s63 offen offset:2048 sc1\n" \
  "buffer_load_dword v8, v6, s[36:39], s69 offen offset:0 sc1\n" \
  "s_waitcnt vmcnt(0)\n" \
  "s_branch Lr_it0_%=\n" \
  "Lr_ta1_%=:\n" \
  "v_dot2_f32_bf16 v128, v80, s51, 0\n" \
  "v_dot2_f32_bf16 v129, v80, s52, 0\n" \
  "v_dot2_f32_bf16 v130, v81, s51, 0\n" \
  "v_dot2_f32_bf16 v131, v81, s52, 0\n" \
  "v_dot2_f32_bf16 v132, v82, s51, 0\n" \
  "v_dot2_f32_bf16 v133, v82, s52, 0\n" \
  "s_add_u32 s63, s87, 71303168\n" \
  "buffer_load_dwordx4 v[32:35], v1, s[36:39], s63 offen offset:0 sc1\n" \
  "v_dot2_f32_bf16 v134, v83, s51, 0\n" \
  "v_dot2_f32_bf16 v135, v83, s52, 0\n" \
  "v_dot2_f32_bf16 v128, v96, s51, v128\n" \
  "v_dot2_f32_bf16 v129, v96, s52, v129\n" \
  "v_dot2_f32_bf16 v130, v97, s51, v130\n" \
  "v_dot2_f32_bf16 v131, v97, s52, v131\n" \
  "buffer_load_dwordx4 v[36:39], v1, s[36:39], s63 offen offset:2048 sc1\n" \
  "v_dot2_f32_bf16 v132, v98, s51, v132\n" \
  "v_dot2_f32_bf16 v133, v98, s52, v133\n" \
  "v_dot2_f32_bf16 v134, v99, s51, v134\n" \
  "v_dot2_f32_bf16 v135, v99, s52, v135\n" \
  "v_dot2_f32_bf16 v136, v84, s51, 0\n" \
  "v_dot2_f32_bf16 v137, v84, s52, 0\n" \
  "s_add_u32 s63, s87, 71307264\n" \
  "buffer_load_dwordx4 v[40:43], v1, s[36:39], s63 offen offset:0 sc1\n" \
  "v_dot2_f32_bf16 v138, v85, s51, 0\n" \
  "v_dot2_f32_bf16 v139, v85, s52, 0\n" \
  "v_dot2_f32_bf16 v140, v86, s51, 0\n" \
  "v_dot2_f32_bf16 v141, v86, s52, 0\n" \
  "v_dot2_f32_bf16 v142, v87, s51, 0\n" \
  "v_dot2_f32_bf16 v143, v87, s52, 0\n" \
  "buffer_load_dwordx4 v[44:47], v1, s[36:39], s63 offen offset:2048 sc1\n" \
  "v_dot2_f32_bf16 v136, v100, s51, v136\n" \
  "v_dot2_f32_bf16 v137, v100, s52, v137\n" \
  "v_dot2_f32_bf16 v138, v101, s51, v138\n" \
  "v_dot2_f32_bf16 v139, v101, s52, v139\n" \
  "v_dot2_f32_bf16 v140, v102, s51, v140\n" \
  "v_dot2_f32_bf16 v141, v102, s52, v141\n" \
  "s_add_u32 s63, s87, 89128960\n" \
  "buffer_load_dwordx4 v[48:51], v1, s[36:39], s63 offen offset:0 sc1\n" \
  "v_dot2_f32_bf16 v142, v103, s51, v142\n" \
  "v_dot2_f32_bf16 v143, v103, s52, v143\n" \
  "v_cvt_pk_bf16_f32 v208, v128, v129\n" \
  "v_cvt_pk_bf16_f32 v209, v130, v131\n" \
  "v_cvt_pk_bf16_f32 v210, v132, v133\n" \
  "v_cvt_pk_bf16_f32 v211, v134, v135\n" \
  "buffer_load_dwordx4 v[52:55], v1, s[36:39], s63 offen offset:2048 sc1\n" \
  "s_nop 0\n" \
  "v_mfma_f32_16x16x32_bf16 v[180:183], v[176:179], v[208:211], 0\n" \
  "v_mfma_f32_16x16x32_bf16 v[184:187], v[208:211], v[208:211], 0\n" \
  "v_dot2_f32_bf16 v144, v88, s51, 0\n" \
  "v_dot2_f32_bf16 v145, v88, s52, 0\n" \
  "v_dot2_f32_bf16 v146, v89, s51, 0\n" \
  "v_dot2_f32_bf16 v147, v89, s52, 0\n" \
  "s_add_u32 s63, s87, 89133056\n" \
  "buffer_load_dwordx4 v[56:59], v1, s[36:39], s63 offen offset:0 sc1\n" \
  "v_dot2_f32_bf16 v148, v90, s51, 0\n" \
  "v_dot2_f32_bf16 v149, v90, s52, 0\n" \
  "v_dot2_f32_bf16 v150, v91, s51, 0\n" \
  "v_dot2_f32_bf16 v151, v91, s52, 0\n" \
  "v_dot2_f32_bf16 v144, v104, s51, v144\n" \
  "v_dot2_f32_bf16 v145, v104, s52, v145\n" \
  "buffer_load_dwordx4 v[60:63], v1, s[36:39], s63 offen offset:2048 sc1\n" \
  "v_dot2_f32_bf16 v146, v105, s51, v146\n" \
  "v_dot2_f32_bf16 v147, v105, s52, v147\n" \
  "v_dot2_f32_bf16 v148, v106, s51, v148\n" \
  "v_dot2_f32_bf16 v149, v106, s52, v149\n" \
  "v_dot2_f32_bf16 v150, v107, s51, v150\n" \
  "v_dot2_f32_bf16 v151, v107, s52, v151\n" \
  "s_add_u32 s63, s87, 8388608\n" \
  "buffer_load_dwordx4 v[64:67], v1, s[36:39], s63 offen offset:0 sc1\n" \
  "v_cvt_pk_bf16_f32 v212, v136, v137\n" \
  "v_cvt_pk_bf16_f32 v213, v138, v139\n" \
  "v_cvt_pk_bf16_f32 v214, v140, v141\n" \
  "v_cvt_pk_bf16_f32 v215, v142, v143\n" \
  "s_nop 1\n" \
  "v_mfma_f32_16x16x32_bf16 v[180:183], v[176:179], v[212:215], v[180:183]\n" \
  "v_mfma_f32_16x16x32_bf16 v[184:187], v[212:215], v[212:215], v[184:187]\n" \
  "buffer_load_dwordx4 v[68:71], v1, s[36:39], s63 offen offset:2048 sc1\n" \
  "v_dot2_f32_bf16 v152, v92, s51, 0\n" \
  "v_dot2_f32_bf16 v153, v92, s52, 0\n" \
  "v_dot2_f32_bf16 v154, v93, s51, 0\n" \
  "v_dot2_f32_bf16 v155, v93, s52, 0\n" \
  "v_dot2_f32_bf16 v156, v94, s51, 0\n" \
  "v_dot2_f32_bf16 v157, v94, s52, 0\n" \
  "s_add_u32 s63, s87, 8392704\n" \
  "buffer_load_dwordx4 v[72:75], v1, s[36:39], s63 offen offset:0 sc1\n" \
  "v_dot2_f32_bf16 v158, v95, s51, 0\n" \
  "v_dot2_f32_bf16 v159, v95, s52, 0\n" \
  "v_dot2_f32_bf16 v152, v108, s51, v152\n" \
  "v_dot2_f32_bf16 v153, v108, s52, v153\n" \
  "v_dot2_f32_bf16 v154, v109, s51, v154\n" \
  "v_dot2_f32_bf16 v155, v109, s52, v155\n" \
  "buffer_load_dwordx4 v[76:79], v1, s[36:39], s63 offen offset:2048 sc1\n" \
  "v_dot2_f32_bf16 v156, v110, s51, v156\n" \
  "v_dot2_f32_bf16 v157, v110, s52, v157\n" \
  "v_dot2_f32_bf16 v158, v111, s51, v158\n" \
  "v_dot2_f32_bf16 v159, v111, s52, v159\n" \
  "v_cvt_pk_bf16_f32 v216, v144, v145\n" \
  "v_cvt_pk_bf16_f32 v217, v146, v147\n" \
  "v_cvt_pk_bf16_f32 v218, v148, v149\n" \
  "v_cvt_pk_bf16_f32 v219, v150, v151\n" \
  "s_nop 1\n" \
  "v_mfma_f32_16x16x32_bf16 v[180:183], v[176:179], v[216:219], v[180:183]\n" \
  "v_mfma_f32_16x16x32_bf16 v[184:187], v[216:219], v[216:219], v[184:187]\n" \
  "v_cvt_pk_bf16_f32 v220, v152, v153\n" \
  "v_cvt_pk_bf16_f32 v221, v154, v155\n" \
  "v_cvt_pk_bf16_f32 v222, v156, v157\n" \
  "v_cvt_pk_bf16_f32 v223, v158, v159\n" \
  "s_nop 1\n" \
  "v_mfma_f32_16x16x32_bf16 v[180:183], v[176:179], v[220:223], v[180:183]\n" \
  "v_mfma_f32_16x16x32_bf16 v[184:187], v[220:223], v[220:223], v[184:187]\n" \
  "s_nop 6\n" \
  "v_add_f32_dpp v224, v180, v180 row_ror:8 row_mask:0xf bank_mask:0xf\n" \
  "v_cndmask_b32 v225, v184, v185, s[46:47]\n" \
  "v_cndmask_b32 v226, v186, v187, s[46:47]\n" \
  "ds_write_b32 v2, v224 offset:0\n" \
  "v_cndmask_b32 v225, v225, v226, s[56:57]\n" \
  "ds_write_b32 v0, v225 offset:0\n" \
  "s_waitcnt lgkmcnt(0)\n" \
  "s_barrier\n" \
  "ds_read_b128 v[160:163], v3 offset:0\n" \
  "ds_read_b128 v[164:167], v3 offset:16\n" \
  "ds_read_b128 v[168:171], v3 offset:32\n" \
  "s_waitcnt lgkmcnt(0)\n" \
  "v_pk_add_f32 v[160:161], v[160:161], v[162:163]\n" \
  "v_pk_add_f32 v[164:165], v[164:165], v[166:167]\n" \
  "v_pk_add_f32 v[168:169], v[168:169], v[170:171]\n" \
  "v_add_f32 v160, v160, v161\n" \
  "v_pk_add_f32 v[164:165], v[164:165], v[168:169]\n" \
  "v_mul_f32 v160, s76, v160\n" \
  "v_add_f32 v161, v164, v165\n" \
  "v_mul_f32 v161, s76, v161\n" \
  "v_fma_f32 v161, -v160, v160, v161\n" \
  "v_max_f32 v161, 0, v161\n" \
  "s_waitcnt vmcnt(12)\n" \
  "s_cmp_eq_u32 s95, 0\n" \
  "s_cbranch_scc1 Lr_pb7_%=\n" \
  "buffer_store_dword v7, v5, s[36:39], s60 offen offset:0 sc0 sc1\n" \
  "s_mov_b32 s95, 0\n" \
  "Lr_pb7_%=:\n" \
  "v_add_f32 v161, s77, v161\n" \
  "v_rsq_f32 v228, v161\n" \
  "s_nop 0\n" \
  "v_mul_f32_e64 v230, -v160, v228\n" \
  "v_mov_b32 v229, v228\n" \
  "v_mov_b32 v231, v230\n" \
  "v_lshlrev_b32 v160, 16, v112\n" \
  "v_and_b32 v161, 0xffff0000, v112\n" \
  "v_pk_fma_f32 v[162:163], v[128:129], v[228:229], v[230:231]\n" \
  "v_pk_fma_f32 v[128:129], v[162:163], v[160:161], v[128:129]\n" \
  "v_lshlrev_b32 v164, 16, v113\n" \
  "v_and_b32 v165, 0xffff0000, v113\n" \
  "v_pk_fma_f32 v[166:167], v[130:131], v[228:229], v[230:231]\n" \
  "v_pk_fma_f32 v[130:131], v[166:167], v[164:165], v[130:131]\n" \
  "v_lshlrev_b32 v168, 16, v114\n" \
  "v_and_b32 v169, 0xffff0000, v114\n" \
  "v_pk_fma_f32 v[170:171], v[132:133], v[228:229], v[230:231]\n" \
  "v_pk_fma_f32 v[132:133], v[170:171], v[168:169], v[132:133]\n" \
  "v_lshlrev_b32 v172, 16, v115\n" \
  "v_and_b32 v173, 0xffff0000, v115\n" \
  "v_pk_fma_f32 v[174:175], v[134:135], v[228:229], v[230:231]\n" \
  "v_pk_fma_f32 v[134:135], v[174:175], v[172:173], v[134:135]\n" \
  "v_lshlrev_b32 v160, 16, v116\n" \
  "v_and_b32 v161, 0xffff0000, v116\n" \
  "v_pk_fma_f32 v[162:163], v[136:137], v[228:229], v[230:231]\n" \
  "v_pk_fma_f32 v[136:137], v[162:163], v[160:161], v[136:137]\n" \
  "v_lshlrev_b32 v164, 16, v117\n" \
  "v_and_b32 v165, 0xffff0000, v117\n" \
  "v_pk_fma_f32 v[166:167], v[138:139], v[228:229], v[230:231]\n" \
  "v_pk_fma_f32 v[138:139], v[166:167], v[164:165], v[138:139]\n" \
  "v_lshlrev_b32 v168, 16, v118\n" \
  "v_and_b32 v169, 0xffff0000, v118\n" \
  "v_pk_fma_f32 v[170:171], v[140:141], v[228:229], v[230:231]\n" \
  "v_pk_fma_f32 v[140:141], v[170:171], v[168:169], v[140:141]\n" \
  "v_lshlrev_b32 v172, 16, v119\n" \
  "v_and_b32 v173, 0xffff0000, v119\n" \
  "v_pk_fma_f32 v[174:175], v[142:143], v[228:229], v[230:231]\n" \
  "v_pk_fma_f32 v[142:143], v[174:175], v[172:173], v[142:143]\n" \
  "v_lshlrev_b32 v160, 16, v120\n" \
  "v_and_b32 v161, 0xffff0000, v120\n" \
  "v_pk_fma_f32 v[162:163], v[144:145], v[228:229], v[230:231]\n" \
  "v_pk_fma_f32 v[144:145], v[162:163], v[160:161], v[144:145]\n" \
  "v_lshlrev_b32 v164, 16, v121\n" \
  "v_and_b32 v165, 0xffff0000, v121\n" \
  "v_pk_fma_f32 v[166:167], v[146:147], v[228:229], v[230:231]\n" \
  "v_pk_fma_f32 v[146:147], v[166:167], v[164:165], v[146:147]\n" \
  "v_lshlrev_b32 v168, 16, v122\n" \
  "v_and_b32 v169, 0xffff0000, v122\n" \
  "v_pk_fma_f32 v[170:171], v[148:149], v[228:229], v[230:231]\n" \
  "v_pk_fma_f32 v[148:149], v[170:171], v[168:169], v[148:149]\n" \
  "v_lshlrev_b32 v172, 16, v123\n" \
  "v_and_b32 v173, 0xffff0000, v123\n" \
  "v_pk_fma_f32 v[174:175], v[150:151], v[228:229], v[230:231]\n" \
  "v_pk_fma_f32 v[150:151], v[174:175], v[172:173], v[150:151]\n" \
  "v_lshlrev_b32 v160, 16, v124\n" \
  "v_and_b32 v161, 0xffff0000, v124\n" \
  "v_pk_fma_f32 v[162:163], v[152:153], v[228:229], v[230:231]\n" \
  "v_pk_fma_f32 v[152:153], v[162:163], v[160:161], v[152:153]\n" \
  "v_lshlrev_b32 v164, 16, v125\n" \
  "v_and_b32 v165, 0xffff0000, v125\n" \
  "v_pk_fma_f32 v[166:167], v[154:155], v[228:229], v[230:231]\n" \
  "v_pk_fma_f32 v[154:155], v[166:167], v[164:165], v[154:155]\n" \
  "v_lshlrev_b32 v168, 16, v126\n" \
  "v_and_b32 v169, 0xffff0000, v126\n" \
  "v_pk_fma_f32 v[170:171], v[156:157], v[228:229], v[230:231]\n" \
  "v_pk_fma_f32 v[156:157], v[170:171], v[168:169], v[156:157]\n" \
  "v_lshlrev_b32 v172, 16, v127\n" \
  "v_and_b32 v173, 0xffff0000, v127\n" \
  "v_pk_fma_f32 v[174:175], v[158:159], v[228:229], v[230:231]\n" \
  "v_pk_fma_f32 v[158:159], v[174:175], v[172:173], v[158:159]\n" \
  "buffer_load_dword v8, v6, s[36:39], s69 offen offset:0 sc1\n" \
  "v_cvt_pk_bf16_f32 v208, v128, v129\n" \
  "v_cvt_pk_bf16_f32 v209, v130, v131\n" \
  "v_cvt_pk_bf16_f32 v210, v132, v133\n" \
  "v_cvt_pk_bf16_f32 v211, v134, v135\n" \
  "s_nop 1\n" \
  "v_mfma_f32_16x16x32_bf16 v[180:183], v[176:179], v[208:211], 0\n" \
  "v_mfma_f32_16x16x32_bf16 v[184:187], v[208:211], v[208:211], 0\n" \
  "buffer_store_dwordx4 v[208:211], v1, s[36:39], s88 offen offset:0 sc0 sc1\n" \
  "v_cvt_pk_bf16_f32 v212, v136, v137\n" \
  "v_cvt_pk_bf16_f32 v213, v138, v139\n" \
  "v_cvt_pk_bf16_f32 v214, v140, v141\n" \
  "v_cvt_pk_bf16_f32 v215, v142, v143\n" \
  "s_nop 1\n" \
  "v_mfma_f32_16x16x32_bf16 v[180:183], v[176:179], v[212:215], v[180:183]\n" \
  "v_mfma_f32_16x16x32_bf16 v[184:187], v[212:215], v[212:215], v[184:187]\n" \
  "buffer_store_dwordx4 v[212:215], v1, s[36:39], s88 offen offset:2048 sc0 sc1\n" \
  "v_cvt_pk_bf16_f32 v216, v144, v145\n" \
  "v_cvt_pk_bf16_f32 v217, v146, v147\n" \
  "v_cvt_pk_bf16_f32 v218, v148, v149\n" \
  "v_cvt_pk_bf16_f32 v219, v150, v151\n" \
  "s_nop 1\n" \
  "v_mfma_f32_16x16x32_bf16 v[180:183], v[176:179], v[216:219], v[180:183]\n" \
  "v_mfma_f32_16x16x32_bf16 v[184:187], v[216:219], v[216:219], v[184:187]\n" \
  "s_add_u32 s88, s88, 4096\n" \
  "buffer_store_dwordx4 v[216:219], v1, s[36:39], s88 offen offset:0 sc0 sc1\n" \
  "v_cvt_pk_bf16_f32 v220, v152, v153\n" \
  "v_cvt_pk_bf16_f32 v221, v154, v155\n" \
  "v_cvt_pk_bf16_f32 v222, v156, v157\n" \
  "v_cvt_pk_bf16_f32 v223, v158, v159\n" \
  "s_nop 1\n" \
  "v_mfma_f32_16x16x32_bf16 v[180:183], v[176:179], v[220:223], v[180:183]\n" \
  "v_mfma_f32_16x16x32_bf16 v[184:187], v[220:223], v[220:223], v[184:187]\n" \
  "buffer_store_dwordx4 v[220:223], v1, s[36:39], s88 offen offset:2048 sc0 sc1\n" \
  "s_nop 5\n" \
  "v_add_f32_dpp v224, v180, v180 row_ror:8 row_mask:0xf bank_mask:0xf\n" \
  "v_cndmask_b32 v225, v184, v185, s[46:47]\n" \
  "v_cndmask_b32 v226, v186, v187, s[46:47]\n" \
  "ds_write_b32 v2, v224 offset:1024\n" \
  "v_cndmask_b32 v225, v225, v226, s[56:57]\n" \
  "ds_write_b32 v0, v225 offset:1024\n" \
  "s_waitcnt lgkmcnt(0)\n" \
  "s_barrier\n" \
  "ds_read_b128 v[160:163], v3 offset:1024\n" \
  "ds_read_b128 v[164:167], v3 offset:1040\n" \
  "ds_read_b128 v[168:171], v3 offset:1056\n" \
  "s_waitcnt lgkmcnt(0)\n" \
  "v_pk_add_f32 v[160:161], v[160:161], v[162:163]\n" \
  "v_pk_add_f32 v[164:165], v[164:165], v[166:167]\n" \
  "v_pk_add_f32 v[168:169], v[168:169], v[170:171]\n" \
  "v_add_f32 v160, v160, v161\n" \
  "v_pk_add_f32 v[164:165], v[164:165], v[168:169]\n" \
  "v_mul_f32 v160, s76, v160\n" \
  "v_add_f32 v161, v164, v165\n" \
  "v_mul_f32 v161, s76, v161\n" \
  "v_fma_f32 v161, -v160, v160, v161\n" \
  "v_max_f32 v161, 0, v161\n" \
  "v_add_f32 v162, s77, v161\n" \
  "v_fma_f32 v163, s77, v162, v161\n" \
  "v_rsq_f32 v173, v162\n" \
  "v_sqrt_f32 v171, v163\n" \
  "v_rsq_f32 v162, v163\n" \
  "v_mov_b32 v168, v160\n" \
  "v_mul_f32_e64 v172, -v160, v173\n" \
  "v_mul_f32 v169, s78, v162\n" \
  "v_mul_f32 v163, v162, v162\n" \
  "v_mul_f32 v170, s79, v163\n" \
  "v_mul_f32 v170, v170, v169\n" \
  "buffer_store_dwordx4 v[168:171], v4, s[36:39], s89 offen offset:0 sc0 sc1\n" \
  "buffer_store_dwordx2 v[172:173], v4, s[36:39], s89 offen offset:16 sc0 sc1\n" \
  "s_mov_b32 s60, s90\n" \
  "s_mov_b32 s95, 1\n" \
  "s_waitcnt vmcnt(6)\n" \
  "s_branch Lr_it0_%=\n" \
  "Lr_tb1_%=:\n" \
  "v_add_f32 v161, s77, v161\n" \
  "v_rsq_f32 v228, v161\n" \
  "s_nop 0\n" \
  "v_mul_f32_e64 v230, -v160, v228\n" \
  "v_mov_b32 v229, v228\n" \
  "v_mov_b32 v231, v230\n" \
  "s_add_u32 s63, s87, 71303168\n" \
  "buffer_load_dwordx4 v[32:35], v1, s[36:39], s63 offen offset:0 sc1\n" \
  "v_lshlrev_b32 v160, 16, v112\n" \
  "v_and_b32 v161, 0xffff0000, v112\n" \
  "v_pk_fma_f32 v[162:163], v[128:129], v[228:229], v[230:231]\n" \
  "v_pk_fma_f32 v[128:129], v[162:163], v[160:161], v[128:129]\n" \
  "v_lshlrev_b32 v164, 16, v113\n" \
  "buffer_load_dwordx4 v[36:39], v1, s[36:39], s63 offen offset:2048 sc1\n" \
  "v_and_b32 v165, 0xffff0000, v113\n" \
  "v_pk_fma_f32 v[166:167], v[130:131], v[228:229], v[230:231]\n" \
  "v_pk_fma_f32 v[130:131], v[166:167], v[164:165], v[130:131]\n" \
  "v_lshlrev_b32 v168, 16, v114\n" \
  "v_and_b32 v169, 0xffff0000, v114\n" \
  "s_add_u32 s63, s87, 71307264\n" \
  "buffer_load_dwordx4 v[40:43], v1, s[36:39], s63 offen offset:0 sc1\n" \
  "v_pk_fma_f32 v[170:171], v[132:133], v[228:229], v[230:231]\n" \
  "v_pk_fma_f32 v[132:133], v[170:171], v[168:169], v[132:133]\n" \
  "v_lshlrev_b32 v172, 16, v115\n" \
  "v_and_b32 v173, 0xffff0000, v115\n" \
  "v_pk_fma_f32 v[174:175], v[134:135], v[228:229], v[230:231]\n" \
  "buffer_load_dwordx4 v[44:47], v1, s[36:39], s63 offen offset:2048 sc1\n" \
  "v_pk_fma_f32 v[134:135], v[174:175], v[172:173], v[134:135]\n" \
  "v_lshlrev_b32 v160, 16, v116\n" \
  "v_and_b32 v161, 0xffff0000, v116\n" \
  "v_pk_fma_f32 v[162:163], v[136:137], v[228:229], v[230:231]\n" \
  "v_pk_fma_f32 v[136:137], v[162:163], v[160:161], v[136:137]\n" \
  "s_add_u32 s63, s87, 89128960\n" \
  "buffer_load_dwordx4 v[48:51], v1, s[36:39], s63 offen offset:0 sc1\n" \
  "v_lshlrev_b32 v164, 16, v117\n" \
  "v_and_b32 v165, 0xffff0000, v117\n" \
  "v_pk_fma_f32 v[166:167], v[138:139], v[228:229], v[230:231]\n" \
  "v_pk_fma_f32 v[138:139], v[166:167], v[164:165], v[138:139]\n" \
  "v_lshlrev_b32 v168, 16, v118\n" \
  "buffer_load_dwordx4 v[52:55], v1, s[36:39], s63 offen offset:2048 sc1\n" \
  "v_and_b32 v169, 0xffff0000, v118\n" \
  "v_pk_fma_f32 v[170:171], v[140:141], v[228:229], v[230:231]\n" \
  "v_pk_fma_f32 v[140:141], v[170:171], v[168:169], v[140:141]\n" \
  "v_lshlrev_b32 v172, 16, v119\n" \
  "v_and_b32 v173, 0xffff0000, v119\n" \
  "s_add_u32 s63, s87, 89133056\n" \
  "buffer_load_dwordx4 v[56:59], v1, s[36:39], s63 offen offset:0 sc1\n" \
  "v_pk_fma_f32 v[174:175], v[142:143], v[228:229], v[230:231]\n" \
  "v_pk_fma_f32 v[142:143], v[174:175], v[172:173], v[142:143]\n" \
  "v_lshlrev_b32 v160, 16, v120\n" \
  "v_and_b32 v161, 0xffff0000, v120\n" \
  "v_pk_fma_f32 v[162:163], v[144:145], v[228:229], v[230:231]\n" \
  "buffer_load_dwordx4 v[60:63], v1, s[36:39], s63 offen offset:2048 sc1\n" \
  "v_pk_fma_f32 v[144:145], v[162:163], v[160:161], v[144:145]\n" \
  "v_lshlrev_b32 v164, 16, v121\n" \
  "v_and_b32 v165, 0xffff0000, v121\n" \
  "v_pk_fma_f32 v[166:167], v[146:147], v[228:229], v[230:231]\n" \
  "v_pk_fma_f32 v[146:147], v[166:167], v[164:165], v[146:147]\n" \
  "s_add_u32 s63, s87, 8388608\n" \
  "buffer_load_dwordx4 v[64:67], v1, s[36:39], s63 offen offset:0 sc1\n" \
  "v_lshlrev_b32 v168, 16, v122\n" \
  "v_and_b32 v169, 0xffff0000, v122\n" \
  "v_pk_fma_f32 v[170:171], v[148:149], v[228:229], v[230:231]\n" \
  "v_pk_fma_f32 v[148:149], v[170:171], v[168:169], v[148:149]\n" \
  "v_lshlrev_b32 v172, 16, v123\n" \
  "buffer_load_dwordx4 v[68:71], v1, s[36:39], s63 offen offset:2048 sc1\n" \
  "v_and_b32 v173, 0xffff0000, v123\n" \
  "v_pk_fma_f32 v[174:175], v[150:151], v[228:229], v[230:231]\n" \
  "v_pk_fma_f32 v[150:151], v[174:175], v[172:173], v[150:151]\n" \
  "v_lshlrev_b32 v160, 16, v124\n" \
  "v_and_b32 v161, 0xffff0000, v124\n" \
  "s_add_u32 s63, s87, 8392704\n" \
  "buffer_load_dwordx4 v[72:75], v1, s[36:39], s63 offen offset:0 sc1\n" \
  "v_pk_fma_f32 v[162:163], v[152:153], v[228:229], v[230:231]\n" \
  "v_pk_fma_f32 v[152:153], v[162:163], v[160:161], v[152:153]\n" \
  "v_lshlrev_b32 v164, 16, v125\n" \
  "v_and_b32 v165, 0xffff0000, v125\n" \
  "v_pk_fma_f32 v[166:167], v[154:155], v[228:229], v[230:231]\n" \
  "buffer_load_dwordx4 v[76:79], v1, s[36:39], s63 offen offset:2048 sc1\n" \
  "v_pk_fma_f32 v[154:155], v[166:167], v[164:165], v[154:155]\n" \
  "v_lshlrev_b32 v168, 16, v126\n" \
  "v_and_b32 v169, 0xffff0000, v126\n" \
  "v_pk_fma_f32 v[170:171], v[156:157], v[228:229], v[230:231]\n" \
  "v_pk_fma_f32 v[156:157], v[170:171], v[168:169], v[156:157]\n" \
  "v_lshlrev_b32 v172, 16, v127\n" \
  "v_and_b32 v173, 0xffff0000, v127\n" \
  "v_pk_fma_f32 v[174:175], v[158:159], v[228:229], v[230:231]\n" \
  "v_pk_fma_f32 v[158:159], v[174:175], v[172:173], v[158:159]\n" \
  "buffer_load_dword v8, v6, s[36:39], s69 offen offset:0 sc1\n" \
  "v_cvt_pk_bf16_f32 v208, v128, v129\n" \
  "v_cvt_pk_bf16_f32 v209, v130, v131\n" \
  "v_cvt_pk_bf16_f32 v210, v132, v133\n" \
  "v_cvt_pk_bf16_f32 v211, v134, v135\n" \
  "s_nop 1\n" \
  "v_mfma_f32_16x16x32_bf16 v[180:183], v[176:179], v[208:211], 0\n" \
  "v_mfma_f32_16x16x32_bf16 v[184:187], v[208:211], v[208:211], 0\n" \
  "buffer_store_dwordx4 v[208:211], v1, s[36:39], s88 offen offset:0 sc0 sc1\n" \
  "v_cvt_pk_bf16_f32 v212, v136, v137\n" \
  "v_cvt_pk_bf16_f32 v213, v138, v139\n" \
  "v_cvt_pk_bf16_f32 v214, v140, v141\n" \
  "v_cvt_pk_bf16_f32 v215, v142, v143\n" \
  "s_nop 1\n" \
  "v_mfma_f32_16x16x32_bf16 v[180:183], v[176:179], v[212:215], v[180:183]\n" \
  "v_mfma_f32_16x16x32_bf16 v[184:187], v[212:215], v[212:215], v[184:187]\n" \
  "buffer_store_dwordx4 v[212:215], v1, s[36:39], s88 offen offset:2048 sc0 sc1\n" \
  "v_cvt_pk_bf16_f32 v216, v144, v145\n" \
  "v_cvt_pk_bf16_f32 v217, v146, v147\n" \
  "v_cvt_pk_bf16_f32 v218, v148, v149\n" \
  "v_cvt_pk_bf16_f32 v219, v150, v151\n" \
  "s_nop 1\n" \
  "v_mfma_f32_16x16x32_bf16 v[180:183], v[176:179], v[216:219], v[180:183]\n" \
  "v_mfma_f32_16x16x32_bf16 v[184:187], v[216:219], v[216:219], v[184:187]\n" \
  "s_add_u32 s88, s88, 4096\n" \
  "buffer_store_dwordx4 v[216:219], v1, s[36:39], s88 offen offset:0 sc0 sc1\n" \
  "v_cvt_pk_bf16_f32 v220, v152, v153\n" \
  "v_cvt_pk_bf16_f32 v221, v154, v155\n" \
  "v_cvt_pk_bf16_f32 v222, v156, v157\n" \
  "v_cvt_pk_bf16_f32 v223, v158, v159\n" \
  "s_nop 1\n" \
  "v_mfma_f32_16x16x32_bf16 v[180:183], v[176:179], v[220:223], v[180:183]\n" \
  "v_mfma_f32_16x16x32_bf16 v[184:187], v[220:223], v[220:223], v[184:187]\n" \
  "buffer_store_dwordx4 v[220:223], v1, s[36:39], s88 offen offset:2048 sc0 sc1\n" \
  "s_nop 5\n" \
  "v_add_f32_dpp v224, v180, v180 row_ror:8 row_mask:0xf bank_mask:0xf\n" \
  "v_cndmask_b32 v225, v184, v185, s[46:47]\n" \
  "v_cndmask_b32 v226, v186, v187, s[46:47]\n" \
  "ds_write_b32 v2, v224 offset:1024\n" \
  "v_cndmask_b32 v225, v225, v226, s[56:57]\n" \
  "ds_write_b32 v0, v225 offset:1024\n" \
  "s_waitcnt lgkmcnt(0)\n" \
  "s_barrier\n" \
  "ds_read_b128 v[160:163], v3 offset:1024\n" \
  "ds_read_b128 v[164:167], v3 offset:1040\n" \
  "ds_read_b128 v[168:171], v3 offset:1056\n" \
  "s_waitcnt lgkmcnt(0)\n" \
  "v_pk_add_f32 v[160:161], v[160:161], v[162:163]\n" \
  "v_pk_add_f32 v[164:165], v[164:165], v[166:167]\n" \
  "v_pk_add_f32 v[168:169], v[168:169], v[170:171]\n" \
  "v_add_f32 v160, v160, v161\n" \
  "v_pk_add_f32 v[164:165], v[164:165], v[168:169]\n" \
  "v_mul_f32 v160, s76, v160\n" \
  "v_add_f32 v161, v164, v165\n" \
  "v_mul_f32 v161, s76, v161\n" \
  "v_fma_f32 v161, -v160, v160, v161\n" \
  "v_max_f32 v161, 0, v161\n" \
  "v_add_f32 v162, s77, v161\n" \
  "v_fma_f32 v163, s77, v162, v161\n" \
  "v_rsq_f32 v173, v162\n" \
  "v_sqrt_f32 v171, v163\n" \
  "v_rsq_f32 v162, v163\n" \
  "v_mov_b32 v168, v160\n" \
  "v_mul_f32_e64 v172, -v160, v173\n" \
  "v_mul_f32 v169, s78, v162\n" \
  "v_mul_f32 v163, v162, v162\n" \
  "v_mul_f32 v170, s79, v163\n" \
  "v_mul_f32 v170, v170, v169\n" \
  "buffer_store_dwordx4 v[168:171], v4, s[36:39], s89 offen offset:0 sc0 sc1\n" \
  "buffer_store_dwordx2 v[172:173], v4, s[36:39], s89 offen offset:16 sc0 sc1\n" \
  "s_mov_b32 s60, s90\n" \
  "s_mov_b32 s95, 1\n" \
  "s_waitcnt vmcnt(6)\n" \
  "s_branch Lr_it0_%=\n" \
  "Lr_tc1_%=:\n" \
  "s_add_u32 s63, s87, 71303168\n" \
  "buffer_load_dwordx4 v[32:35], v1, s[36:39], s63 offen offset:0 sc1\n" \
  "buffer_load_dwordx4 v[36:39], v1, s[36:39], s63 offen offset:2048 sc1\n" \
  "s_add_u32 s63, s87, 71307264\n" \
  "buffer_load_dwordx4 v[40:43], v1, s[36:39], s63 offen offset:0 sc1\n" \
  "buffer_load_dwordx4 v[44:47], v1, s[36:39], s63 offen offset:2048 sc1\n" \
  "s_add_u32 s63, s87, 89128960\n" \
  "buffer_load_dwordx4 v[48:51], v1, s[36:39], s63 offen offset:0 sc1\n" \
  "buffer_load_dwordx4 v[52:55], v1, s[36:39], s63 offen offset:2048 sc1\n" \
  "s_add_u32 s63, s87, 89133056\n" \
  "buffer_load_dwordx4 v[56:59], v1, s[36:39], s63 offen offset:0 sc1\n" \
  "buffer_load_dwordx4 v[60:63], v1, s[36:39], s63 offen offset:2048 sc1\n" \
  "s_add_u32 s63, s87, 8388608\n" \
  "buffer_load_dwordx4 v[64:67], v1, s[36:39], s63 offen offset:0 sc1\n" \
  "buffer_load_dwordx4 v[68:71], v1, s[36:39], s63 offen offset:2048 sc1\n" \
  "s_add_u32 s63, s87, 8392704\n" \
  "buffer_load_dwordx4 v[72:75], v1, s[36:39], s63 offen offset:0 sc1\n" \
  "buffer_load_dwordx4 v[76:79], v1, s[36:39], s63 offen offset:2048 sc1\n" \
  "buffer_load_dword v8, v6, s[36:39], s69 offen offset:0 sc1\n" \
  "s_waitcnt vmcnt(13)\n" \
  "s_cmp_eq_u32 s95, 0\n" \
  "s_cbranch_scc1 Lr_pb8_%=\n" \
  "buffer_store_dword v7, v5, s[36:39], s60 offen offset:0 sc0 sc1\n" \
  "s_mov_b32 s95, 0\n" \
  "Lr_pb8_%=:\n" \
  "s_waitcnt vmcnt(0)\n" \
  "s_branch Lr_it0_%=\n" \
  "Lr_done_%=:\n" \
  "s_waitcnt vmcnt(0) lgkmcnt(0)\n" \
  "s_barrier\n" \
  "Lcode_r_e:\n" \
  :: "s"(ws), "s"(sK), "s"(sStep), "s"(sRq), "s"(sK1), "s"(sEpR), "v"(vRP), "v"(vRF), "v"(vRwr), "v"(vRrd), "v"(vRS), "v"(vFlag), "v"(vL4) : "v0", "v1", "v2", "v3", "v4", "v5", "v6", "v7", "v8", "v9", "v10", "v11", "v12", "v13", "v14", "v15", "v16", "v17", "v18", "v19", "v20", "v21", "v22", "v23", "v24", "v25", "v26", "v27", "v28", "v29", "v30", "v31", "v32", "v33", "v34", "v35", "v36", "v37", "v38", "v39", "v40", "v41", "v42", "v43", "v44", "v45", "v46", "v47", "v48", "v49", "v50", "v51", "v52", "v53", "v54", "v55", "v56", "v57", "v58", "v59", "v60", "v61", "v62", "v63", "v64", "v65", "v66", "v67", "v68", "v69", "v70", "v71", "v72", "v73", "v74", "v75", "v76", "v77", "v78", "v79", "v80", "v81", "v82", "v83", "v84", "v85", "v86", "v87", "v88", "v89", "v90", "v91", "v92", "v93", "v94", "v95", "v96", "v97", "v98", "v99", "v100", "v101", "v102", "v103", "v104", "v105", "v106", "v107", "v108", "v109", "v110", "v111", "v112", "v113", "v114", "v115", "v116", "v117", "v118", "v119", "v120", "v121", "v122", "v123", "v124", "v125", "v126", "v127", "v128", "v129", "v130", "v131", "v132", "v133", "v134", "v135", "v136", "v137", "v138", "v139", "v140", "v141", "v142", "v143", "v144", "v145", "v146", "v147", "v148", "v149", "v150", "v151", "v152", "v153", "v154", "v155", "v156", "v157", "v158", "v159", "v160", "v161", "v162", "v163", "v164", "v165", "v166", "v167", "v168", "v169", "v170", "v171", "v172", "v173", "v174", "v175", "v176", "v177", "v178", "v179", "v180", "v181", "v182", "v183", "v184", "v185", "v186", "v187", "v188", "v189", "v190", "v191", "v192", "v193", "v194", "v195", "v196", "v197", "v198", "v199", "v200", "v201", "v202", "v203", "v204", "v205", "v206", "v207", "v208", "v209", "v210", "v211", "v212", "v213", "v214", "v215", "v216", "v217", "v218", "v219", "v220", "v221", "v222", "v223", "v224", "v225", "v226", "v227", "v228", "v229", "v230", "v231", "v255", "s36", "s37", "s38", "s39", "s40", "s41", "s42", "s43", "s44", "s45", "s46", "s47", "s48", "s49", "s50", "s51", "s52", "s53", "s54", "s55", "s56", "s57", "s58", "s59", "s60", "s61", "s62", "s63", "s64", "s65", "s66", "s67", "s68", "s69", "s70", "s71", "s72", "s73", "s74", "s75", "s76", "s77", "s78", "s79", "s80", "s81", "s82", "s83", "s84", "s85", "s86", "s87", "s88", "s89", "s90", "s91", "s92", "s93", "s94", "s95", "s96", "s97", "vcc", "scc", "memory")

#define NRW 18
__global__ __launch_bounds__(256, 1) void flow_kernel(const u16* __restrict__ q, const u16* __restrict__ kin, const u16* __restrict__ vin,
                                                      const u16* __restrict__ shift_k, const u16* __restrict__ ssm_k, const u16* __restrict__ ssm_D,
                                                      const u16* __restrict__ w1, const u16* __restrict__ w2,
                                                      const u16* __restrict__ w3, const u16* __restrict__ w4, char* __restrict__ ws,
                                                      u16* __restrict__ out) {
  __shared__ __attribute__((aligned(16))) char lds[163840];
  const int tid = threadIdx.x, lane = tid & 63, wave = __builtin_amdgcn_readfirstlane(tid >> 6);
  const int b = blockIdx.x, xcd = b & 7, idx = b >> 3;
  const unsigned sLds = (unsigned)(size_t)lds;
  const int oct = idx >> 3, o8 = idx & 7;
  int kind, rep, slice, kh = 0;
  if (idx < 8) { kind = 1 + (xcd & 1); rep = xcd >> 1; slice = o8; }
  else if (idx < 20) {
    const int j = xcd * 12 + idx - 8;
    rep = (j * 57) >> 10;
    kind = j < 90 ? 3 : j < 94 ? 5 : 6; slice = j < 90 ? j - rep * 18 : j < 94 ? 18 : j - 96 + NRW; rep = j < 90 ? rep : j < 94 ? j - 90 : 0;
    if (j < 90) {
      const int i = idx - 8, hi = i >= 6;
      rep = xcd < 6 ? (xcd & 1) * 2 + hi : 4;
      slice = xcd < 6 ? (xcd >> 1) * 6 + i - 6 * hi : xcd == 6 ? i : 12 + i;
    }
  } else if (idx < 30) {
    const int h = idx < 28 ? xcd : 8 + (xcd >> 2);
    kind = 4; rep = h >> 1; kh = h & 1; slice = idx < 28 ? idx - 20 : (xcd & 3) * 2 + idx - 28;
  } else { kind = 6; rep = 0; slice = xcd * 2 + idx - 30; }
  CTS(0);
  {
    CODE_RANGE("Lpc%=", "Lcode_g12_b", c0, n0) CODE_RANGE("Lcode_r_e", "Lcode_g4_b", c7, n7)
    touch_lds(tid < 224 ? c0 + (tid * 128 < n0 ? tid * 128 : 0) : c7 + ((tid - 224) * 128 < n7 ? (tid - 224) * 128 : 0), sLds + 163584);
  }
  const int j3 = xcd * 12 + idx - 8, j4 = xcd * 10 + idx - 20;
  const unsigned sScr = sLds + 163584;
  auto code_touch = [&]() {
    CODE_RANGE("Lcode_g12_b", "Lcode_g12_e", c1, n1) CODE_RANGE("Lcode_g3_b", "Lcode_g3_e", c3, n3) CODE_RANGE("Lcode_g3o_b", "Lcode_g3o_e", c5, n5)
    CODE_RANGE("Lcode_g4_b", "Lcode_g4_e", c4, n4) CODE_RANGE("Lcode_r_b", "Lcode_r_e", c6, n6)
    const char* cp = kind < 3 ? c1 + (kind - 1) * ((n1 >> 1) & ~127u) : kind == 3 ? c3 : kind == 4 ? c4 : kind == 5 ? c5 : c6;
    const unsigned cn = kind < 3 ? n1 >> 1 : kind == 3 ? n3 : kind == 4 ? n4 : kind == 5 ? n5 : n6;
    touch_lds(cp + (tid * 128 < cn ? tid * 128 : 0), sScr);
    touch_lds(cp + (32768 + tid * 128 < cn ? 32768 + tid * 128 : 0), sScr);
  };
  unsigned ep = 0;
  if (kind == 1) code_touch();
  else {
  asm volatile("global_load_dword %0, %1, %2 sc1" : "=v"(ep) : "v"(0u), "s"(ws + OFF_EP) : "memory");
  const int ci = kind == 4 ? j4 : 80 + j3;
  const bool isconv = kind >= 3 && kind <= 5 && ci < NCV;
  const int tile = idx >= 8 ? (idx - 8) * 8 + xcd : 192 + (xcd >> 1) * 8 + idx;
  KvRegs KR;
  HRegs HR;
  kv_load(KR, kin, vin, shift_k, tile, tid);
  const bool helper = kind == 6 || (kind == 5 && !isconv);
  const int rw = kind == 6 ? slice : j3 - 73;
  const int tq0 = kind == 2 ? rep * 8 + slice : kind == 6 ? 35 + slice : 14 + rw;
  h_load(HR, ssm_k, ssm_D, isconv ? ci : 0, tid);
  code_touch();
  kv_wait<15>(KR);
  CTS(12);
  kv_store(KR, ws, lds + CV_STG, tile, tid);
  CTS(1);
  asm volatile("s_waitcnt vmcnt(4)" : "+v"(HR.hv0), "+v"(HR.hv1), "+v"(HR.hv2), "+v"(HR.hv3), "+v"(HR.hv4), "+v"(HR.hv5), "+v"(HR.hn0), "+v"(HR.hn1),
               "+v"(HR.hn2), "+v"(HR.hn3), "+v"(HR.hn4), "+v"(HR.hn5), "+v"(HR.d0), "+v"(ep) :: "memory");
  if (isconv) h_spread(HR, lds, tid);
  WAIT_VM(0);
  ep = __builtin_amdgcn_readfirstlane(ep) + 1u;
  if (ep == 0u) ep = 1u;
  __syncthreads();
  if (tid == 0) __hip_atomic_store((gu32*)(ws + OFF_FKV + tile * 128), ep, __ATOMIC_RELAXED, __HIP_MEMORY_SCOPE_AGENT);
  CTS(2);
  u32x4v qa[4], qb[4], ya[6], yb[6];
  if (isconv) prewrite<0, 4 * NCV>(ws, ci * 4 + wave, lane, ep);
  else row0_q(q, tq0, lane, wave, qa);
  if (kind == 2 && rep < 4) {
    kv_load(KR, kin, vin, shift_k, 224 + rep * 8 + slice, tid);
    kv_wait<0>(KR);
    kv_store(KR, ws, lds + CV_STG, 224 + rep * 8 + slice, tid);
    WAIT_VM(0);
    __syncthreads();
    if (tid == 0) __hip_atomic_store((gu32*)(ws + OFF_FKV + (224 + rep * 8 + slice) * 128), ep, __ATOMIC_RELAXED, __HIP_MEMORY_SCOPE_AGENT);
    CTS(3);
  }
  if (helper) {
    const int pw = rw * 256 + tid;
#pragma unroll
    for (int i = 0; i < 4; ++i) {
      const unsigned g = pw + 21 * 256 * i;
      if (g < 16384) touch_lds((const char*)w2 + (size_t)g * 128, sScr);
    }
  }
  if (isconv) {
    h_patch(HR, lds, tid);
    conv_part(ws, HR, lds, ci, tid, lane, wave, ep, w3, w4);
  }
  CTS(8);
  char* rl = lds + 144384;
  auto wait_y = [&](int g) {
    if (wave == 0) poll_flags<3>(ws + OFF_FY + g * 256 * 128, NCV, lane, ep);
    __syncthreads();
  };
  auto two = [&](int ta, int tb, bool have_qa, auto&& wait) {
    float ma, va, mb = 0.f, vb_ = 0.f;
    if (!have_qa) row0_q(q, ta, lane, wave, qa);
    if (tb >= 0) row0_q(q, tb, lane, wave, qb);
    row_stats(qa, rl, 0, lane, wave, ma, va);
    if (tb >= 0) row_stats(qb, rl, 1, lane, wave, mb, vb_);
    wait();
    row0_y(ws, ta, tid, ya);
    if (tb >= 0) row0_y(ws, tb, tid, yb);
    row0_body(ws, rl, ta, 0, lane, wave, qa, ya, ma, va);
    if (tb >= 0) row0_body(ws, rl, tb, 1, lane, wave, qb, yb, mb, vb_);
    WAIT_VM(0);
    row0_flag(ws, ta, lane, wave, ep);
    if (tb >= 0) row0_flag(ws, tb, lane, wave, ep);
    __syncthreads();
  };
#define RTOP 256
  if (kind == 4) two(64 + j4, j4 % 10 == 2 && RTOP + j4 / 10 < 256 ? RTOP + j4 / 10 : -1, false, [&]() { wait_y(1); CTS(13); });
  else if (kind == 6) {
    two(tq0, slice < 11 ? 53 + slice : -1, true, [&]() { wait_y(0); CTS(13); });
    bool first = true;
    for (int t = 144 + slice; t < RTOP; t += 36) {
      two(t, t + 18 < RTOP ? t + 18 : -1, false, [&]() { if (first) wait_y(1); });
      first = false;
    }
  } else if (!isconv) two(tq0, -1, true, [&]() { wait_y(0); CTS(13); });
  WAIT_VM(0);
  __syncthreads();
  CTS(10);
  }
  const unsigned sDbg0 = OFF_DBG + (b * 4 + wave) * 8192;
#define LANE_ID(x) int x; asm volatile("v_mbcnt_lo_u32_b32 %0, -1, 0\n\tv_mbcnt_hi_u32_b32 %0, -1, %0" : "=v"(x))
#define ROW_ROLE(K0, W) \
    { \
      LANE_ID(lr); \
      const int r8 = lr & 7, seg = lr >> 3; \
      const unsigned sEpR = ep, sK = (K0), sStep = NRW, sK1 = RT1 + (W), sRq = OFF_FXP + LB * 2048 + wave * 128, sDbg = sDbg0 + (kind == 6 ? 0 : 1024); \
      const unsigned vRF = (wave * 8 + (seg >> 2)) * 1024 + ((seg & 3) * 16 + r8) * 16; \
      const unsigned vRwr = sLds + (seg == 0 ? r8 * 64 + wave * 4 : 512 + lr * 4), vRrd = sLds + r8 * 64; \
      const unsigned vRP = sLds + ((lr >> 4) == ((lr & 15) >> 2) ? r8 * 64 + 16 + wave * 8 + (seg & 1) * 4 : 768 + lr * 4); \
      const unsigned vRS = (wave == 0 && seg == 0) ? r8 * 32 : 0x80000000u, vFlag = lr == 0 ? 0u : 0x80000000u, vL4 = (lr & 15) * 128; \
      ASM_R(); \
    }
#define OOB 0x80000000u
#define COMMON(NF, FIN, FOUT, SN0, WS, LDW, NREP)                                                                                    \
  LANE_ID(ln);                                                                                                                       \
  const int l15 = ln & 15, lg = ln >> 4, lq = l15 >> 2, lp = ln & 3, rr = 8 * lg + lq;                                               \
  const unsigned vA = ln * 16, vFlag = (ln == 0 && wave == 0) ? (FOUT) : OOB;                                                        \
  const unsigned vPollOff = wave < 2 && ln < (NF) ? (FIN) + ln * 128 : OOB, vSnapOff = wave == 0 ? vPollOff : OOB;                   \
  const unsigned vSnapRd = (SN0) + (ln >> 5) * (WS) + ((ln & 31) < (NF) ? (ln & 31) : 0) * 4;                                        \
  const unsigned vWd0 = (ln >> 3) * (LDW) + ((ln & 7) ^ swzB(ln >> 3)) * 16, vWd1 = ((ln >> 3) + 8) * (LDW) + ((ln & 7) ^ swzB(8 + (ln >> 3))) * 16; \
  const unsigned vWr0 = (wave >> 1) * 8192 + rr * 128 + (((wave & 1) * 4 + lp) ^ swzB(rr)) * 16, vWr1 = vWr0 + 8;                    \
  const unsigned vWr2 = 16384 + rr * 128 + ((2 * wave + (lp >> 1)) ^ swzB(rr)) * 16 + (lp & 1) * 8;                                  \
                                            \
  const unsigned sStep = (NREP) << 15, sU0 = rep << 15, sX1 = ((NREP) == 4 ? 128 + rep : 128 + (rep + 2) % 5) << 15;                 \
  const unsigned sX2 = ((NREP) == 4 ? 260 + rep : 260 + (rep + 4) % 5) << 15, sDbg = sDbg0, sBuf = kind
  constexpr unsigned AUX = 131072;
  if (kind == 3 || kind == 5) {
    COMMON(8, OFF_FX1, OFF_FG + slice * 128, AUX + 1536, 2048, 7168, kind == 3 ? 5 : 4);
    const unsigned vSrc = OFF_X1 + wave * 4096 + vA, vOut = OFF_G + (slice * 6 + wave) * 1024 + vA;
    const unsigned vOut2 = OFF_G + (slice * 6 + 4 + (wave >> 1)) * 1024 + (((wave & 1) * 2 + (lg >> 1)) * 16 + l15) * 16 + (lg & 1) * 8;
    const unsigned vAuxS = OFF_ST + (ln & 7) * 128 + (wave * 8 + (ln >> 3)) * 4, vX = AUX + wave * 2048 + ln * 4;
    const unsigned vStW = (ln & 7) == 0 ? AUX + 1280 + (wave * 8 + (ln >> 3)) * 4 : AUX + 24576 + wave * 2048 + 1536 + ln * 4, vStR = AUX + 1280 + l15 * 8;
    const unsigned sWw = 16 * wave * 7168 + slice * 384;
    if (kind == 3) { ASM_G3(w3); } else { ASM_G3O(w3); }
    __builtin_unreachable();
  } else if (kind == 4) {
    COMMON(10, OFF_FG + kh * 9 * 128, OFF_FP + (kh * 8 + slice) * 128, 159744, 256, 2048, 5);
    const unsigned vSrc = OFF_G + kh * 57344 + wave * 7168 + vA, vOut = (kh ? OFF_P1 : OFF_P0) + (slice * 4 + wave) * 1024 + vA;
    const unsigned vAuxS = (kh ? OFF_XP : OFF_X1) + (slice * 4 + wave) * 1024 + vA;
    const unsigned sWw = (kh * 1792 + 16 * wave) * 2048 + slice * 256;
    ASM_G4(w4);
    __builtin_unreachable();
  } else {
   if (kind != 6) {
    COMMON(kind == 1 ? 8 : 24, kind == 1 ? OFF_FXP : OFF_FH - 1024, (kind == 1 ? OFF_FH : OFF_FX1) + slice * 128, AUX + 1536, 2048, 2048, 4);
    const int grp = slice >> 1, r0 = 16 + l15 + (kind == 1 ? ((grp & 1) ? -1 : 1) * (1 + (grp >> 1)) : 0);
    const unsigned vSrc = (kind == 1 ? OFF_XP : OFF_HS) + wave * 4096 + vA;
    const unsigned vOut = (kind == 1 ? OFF_HS : OFF_X1) - 32768 + (slice * 4 + wave) * 1024 + (r0 >> 4) * 32768 + (lg * 16 + (r0 & 15)) * 16;
    const unsigned vX = AUX + wave * 2048 + (kind == 1 ? l15 * 16 : vA), vX2 = AUX + wave * 2048 + l15 * 8;
    const unsigned vAuxS = kind == 1 ? OFF_S1 + (ln >> 2) * 32 + (ln & 3) * 4 : OFF_XP + (slice * 4 + wave) * 1024 + vA;
    const unsigned vAuxS2 = ln < 32 ? OFF_S1 - 1024 + (ln >> 1) * 32 + 16 + (ln & 1) * 4 : OOB;
    const unsigned vOut2 = wave == 0 && ln < 32 ? OFF_ST + slice * 128 + ln * 4 : OOB;
    const unsigned vStW = AUX + 24576 + 1536 + ((l15 * 2 + (lg & 1)) * 4 + wave) * 4, vStR = AUX + 24576 + 1536 + (ln & 31) * 16;
    const unsigned sWw = 16 * wave * 2048 + slice * 256;
    const u16* w12 = kind == 1 ? w1 : w2;
    unsigned epOut;
    ASM_G12(w12);
    ep = epOut;
   }
    if (kind == 6) ROW_ROLE(slice, slice)
    LANE_ID(lane2);
    for (int t = kind == 6 ? 82 - NRW + slice : (kind - 1) * 32 + rep * 8 + slice; t < 256; t += 82)
      row_last(ws, out, t, lane2, wave, ep);
    if (b == 0 && lane2 == 0 && wave == 0) __hip_atomic_store((gu32*)(ws + OFF_EP), ep, __ATOMIC_RELAXED, __HIP_MEMORY_SCOPE_AGENT);
    asm volatile("s_endpgm" ::: "memory");
    __builtin_unreachable();
  }
}

extern "C" void kernel_launch(void* const* d_in, const int* in_sizes, int n_in, void* d_out, int out_size,
                              void* d_ws, size_t ws_size, hipStream_t stream) {
  const u16* q = (const u16*)d_in[0];
  const u16* k = (const u16*)d_in[1];
  const u16* v = (const u16*)d_in[2];
  const u16* shift_k = (const u16*)d_in[3];
  const u16* ssm_k = (const u16*)d_in[4];
  const u16* ssm_D = (const u16*)d_in[5];
  const u16* w1 = (const u16*)d_in[7];
  const u16* w2 = (const u16*)d_in[8];
  const u16* w3 = (const u16*)d_in[9];
  const u16* w4 = (const u16*)d_in[10];
  flow_kernel<<<256, 256, 0, stream>>>(q, k, v, shift_k, ssm_k, ssm_D, w1, w2, w3, w4, (char*)d_ws, (u16*)d_out);
}
```

```cpp
#include <hip/hip_runtime.h>
#include <hip/hip_bf16.h>
#include <stdint.h>
#include <type_traits>

typedef short s16x8 __attribute__((ext_vector_type(8)));
typedef short s16x4 __attribute__((ext_vector_type(4)));
typedef float f32x4 __attribute__((ext_vector_type(4)));
typedef unsigned short u16;
typedef unsigned short u16x8 __attribute__((ext_vector_type(8)));
typedef unsigned short u16x4 __attribute__((ext_vector_type(4)));
typedef float f32x2 __attribute__((ext_vector_type(2)));
typedef __bf16 bf16x2 __attribute__((ext_vector_type(2)));
typedef unsigned u32x4v __attribute__((ext_vector_type(4)));
typedef __attribute__((address_space(1))) unsigned gu32;

constexpr int S_ = 2048, D_ = 1024, F_ = 3584;
#define PF 1

constexpr int RT1 = 0, LB = 66, FL_BYTES = 2141312, WS_USED = 111193216;
constexpr int OFF_KVR = 4194304, OFF_YF = 8388608, OFF_XP = 12582912, OFF_HS = 22085632, OFF_X1 = 31457280, OFF_G = 40894464, OFF_P0 = 71303168, OFF_DBG = 80740352, OFF_P1 = 89128960, OFF_QS = 98566144, OFF_S1 = 106954752, OFF_ST = 107479040, OFF_FL = 109051904, OFF_FXP = 109052928, OFF_FH = 109325312, OFF_FX1 = 109597696, OFF_FP = 109871104, OFF_FG = 110416384, OFF_FKV = 111094784, OFF_FY = 111127552, OFF_EP = 111193088;

#define WS_DIAG 0

__device__ __forceinline__ float bf2f(u16 v) { return __uint_as_float(((unsigned)v) << 16); }
__device__ __forceinline__ u16 f2bf(float f) { return __builtin_bit_cast(u16, (__bf16)f); }
__device__ __forceinline__ float rbf(float f) { return bf2f(f2bf(f)); }
__device__ __forceinline__ void st16(void* p, u16x8 v) {
  asm volatile("global_store_dwordx4 %0, %1, off sc0 sc1\n\ts_nop 1" :: "v"(p), "v"(v) : "memory");
}
#define WAIT_VM(n) asm volatile("s_waitcnt vmcnt(%0)" ::"n"(n) : "memory")
#define WAIT_LGKM0() asm volatile("s_waitcnt lgkmcnt(0)" ::: "memory")

#define CODE_RANGE(B, E, p, n)                                                                                   \
  const char* p; unsigned n;                                                                                     \
  { unsigned lo_, hi_;                                                                                           \
    asm volatile("s_getpc_b64 s[98:99]\nLpc%=:\n\ts_add_u32 %0, s98, (" B "-Lpc%=)&4294967295\n\ts_addc_u32 %1, s99, (" B "-Lpc%=)>>32\n\t" \
                 "s_mov_b32 %2, " E "-" B : "=s"(lo_), "=s"(hi_), "=s"(n) :: "s98", "s99", "scc");               \
    p = (const char*)(((unsigned long long)hi_ << 32) | lo_); }
__device__ __forceinline__ unsigned touch(const char* p, unsigned n, unsigned off) {
  unsigned t;
  asm volatile("global_load_dword %0, %1, off" : "=v"(t) : "v"(p + (off < n ? off : 0u)) : "memory");
  return t;
}

__device__ __forceinline__ void touch_lds(const char* p, unsigned a) {
  asm volatile("s_mov_b32 m0, %1\n\ts_nop 0\n\tglobal_load_lds_dword %0, off" :: "v"(p), "s"(a) : "memory");
}
#define PFPOL ""
__device__ __forceinline__ void touch_pol(const char* p, unsigned a) {
  asm volatile("s_mov_b32 m0, %1\n\ts_nop 0\n\tglobal_load_lds_dword %0, off " PFPOL :: "v"(p), "s"(a) : "memory");
}
#define YSLEEP 4
#define KSLEEP 2
#define CTS(i)
#define CTS_INIT()
#define CTS_FLUSH()
__device__ __forceinline__ f32x2 up2(unsigned p) { return (f32x2){__uint_as_float(p << 16), __uint_as_float(p & 0xffff0000u)}; }
__device__ __forceinline__ unsigned pk2(f32x2 v) { return __builtin_bit_cast(unsigned, __builtin_convertvector(v, bf16x2)); }
__device__ __forceinline__ f32x2 rbf2(f32x2 v) { return up2(pk2(v)); }
#define BARRIER() asm volatile("s_waitcnt lgkmcnt(0)\n\ts_barrier" ::: "memory")
template <int N, class F> __device__ __forceinline__ void sfor(F&& f) {
  if constexpr (N > 0) { sfor<N - 1>(f); f(std::integral_constant<int, N - 1>{}); }
}
template <int NL>
__device__ __forceinline__ void poll_flags(const char* p, int n, int lane, unsigned ep) {
#pragma unroll 1
  for (int it = 0; it < 20000; ++it) {
    bool ok = true;
#pragma unroll
    for (int i = 0; i < NL; ++i) {
      const int f = lane + 64 * i;
      ok &= __hip_atomic_load((gu32*)(p + (f < n ? f : 0) * 128), __ATOMIC_RELAXED, __HIP_MEMORY_SCOPE_AGENT) == ep;
    }
    if (__all(ok)) break;
    __builtin_amdgcn_s_sleep(YSLEEP);
  }
  asm volatile("" ::: "memory");
}

constexpr int CV_HP = (2048 + 32) * 2;
constexpr int CV_UR = (2048 + 256) * 2;
constexpr int CV_CH = 2 * CV_HP + CV_UR;
#define MAP4 2
#define CNTL
#define PFL 0
constexpr int CV_STG = 8 * CV_CH;

struct KvRegs { u32x4v k0, k1, k2, k3, k4, k5, k6, v0, v1, v2, v3; };
struct SkRegs { u32x4v s0, s1, s2, s3; };
#define LD16(d, p, pol) asm volatile("global_load_dwordx4 %0, %1, off " pol : "=v"(d) : "v"(p) : "memory")
__device__ __forceinline__ void kv_load(KvRegs& R, const u16* __restrict__ kin, const u16* __restrict__ vin, int b, int tid) {
  const int tr = (b >> 2) * 32 + 4 * (tid >> 5), cc = (b & 3) * 256 + 8 * (tid & 31);
  const u16* kp = kin + (size_t)tr * D_ + cc;
  const u16* vp = vin + (size_t)tr * D_ + cc;
  LD16(R.k3, kp, "nt"); LD16(R.k4, kp + D_, "nt"); LD16(R.k5, kp + 2 * D_, "nt"); LD16(R.k6, kp + 3 * D_, "nt");
  LD16(R.v0, vp, "nt"); LD16(R.v1, vp + D_, "nt"); LD16(R.v2, vp + 2 * D_, "nt"); LD16(R.v3, vp + 3 * D_, "nt");
  const u16* kq = tr ? kp - 3 * D_ : kp;
  LD16(R.k0, kq, "nt"); LD16(R.k1, kq + D_, "nt"); LD16(R.k2, kq + 2 * D_, "nt");
}
__device__ __forceinline__ void sk_load(SkRegs& R, const u16* __restrict__ shift_k, int b, int tid) {
  const u16* sp = shift_k + (size_t)((b & 3) * 256 + 8 * (tid & 31)) * 4;
  LD16(R.s0, sp, ""); LD16(R.s1, sp + 8, ""); LD16(R.s2, sp + 16, ""); LD16(R.s3, sp + 24, "");
}
template <int N>
__device__ __forceinline__ void kv_wait(KvRegs& R, SkRegs& Q) {
  asm volatile("s_waitcnt vmcnt(%15)" : "+v"(R.k0), "+v"(R.k1), "+v"(R.k2), "+v"(R.k3), "+v"(R.k4), "+v"(R.k5), "+v"(R.k6), "+v"(R.v0), "+v"(R.v1), "+v"(R.v2),
               "+v"(R.v3), "+v"(Q.s0), "+v"(Q.s1), "+v"(Q.s2), "+v"(Q.s3) : "n"(N) : "memory");
}
__device__ __forceinline__ void kv_store(KvRegs& R, const SkRegs& Q, char* __restrict__ ws, char* T, int b, int tid) {
  const int t0 = (b >> 2) * 32, c0 = (b & 3) * 256, ch8 = tid & 31, rg = tid >> 5;
  if (t0 + 4 * rg < 3) R.k0 = R.k1 = R.k2 = (u32x4v){0u, 0u, 0u, 0u};
  typedef unsigned u32x2 __attribute__((ext_vector_type(2)));
  const u32x4v kr[7] = {R.k0, R.k1, R.k2, R.k3, R.k4, R.k5, R.k6}, vr[4] = {R.v0, R.v1, R.v2, R.v3}, sk[4] = {Q.s0, Q.s1, Q.s2, Q.s3};
#pragma unroll
  for (int j2 = 0; j2 < 4; ++j2) {
    const f32x2 sa = up2(sk[j2][0]), sb = up2(sk[j2][1]), sc = up2(sk[j2][2]), sd = up2(sk[j2][3]);
    const f32x2 s0 = {sa[0], sc[0]}, s1 = {sa[1], sc[1]}, s2 = {sb[0], sd[0]}, s3 = {sb[1], sd[1]};
    f32x2 kf[7], o[4];
#pragma unroll
    for (int r = 0; r < 7; ++r) kf[r] = up2(kr[r][j2]);
#pragma unroll
    for (int r = 0; r < 4; ++r)
      o[r] = __builtin_elementwise_fma(kf[r + 3], s0, __builtin_elementwise_fma(kf[r + 2], s1, __builtin_elementwise_fma(kf[r + 1], s2, kf[r] * s3))) * up2(vr[r][j2]);
#pragma unroll
    for (int e = 0; e < 2; ++e)
      *(u32x2*)(T + ch8 * 520 + (2 * j2 + e) * 64 + (28 - 4 * rg) * 2) = (u32x2){pk2((f32x2){o[3][e], o[2][e]}), pk2((f32x2){o[1][e], o[0][e]})};
  }
  BARRIER();
  u16* kvr = (u16*)(ws + OFF_KVR);
#pragma unroll
  for (int i = 0; i < 4; ++i) {
    const int ch = (tid >> 2) + 64 * i, cb = tid & 3;
    const char* p = T + ch * 64 + (ch >> 3) * 8 + cb * 16;
    const u16x4 lo = *(const u16x4*)p, hi = *(const u16x4*)(p + 8);
    st16(kvr + (size_t)(c0 + ch) * S_ + (2016 - t0) + cb * 8, __builtin_shufflevector(lo, hi, 0, 1, 2, 3, 4, 5, 6, 7));
  }
}

typedef unsigned u32x2v __attribute__((ext_vector_type(2)));
__device__ __forceinline__ void h_load(u32x4v (&hv)[8], unsigned& hx, unsigned& dD, const u16* __restrict__ ssm_k, const u16* __restrict__ ssm_D, int c0, int tid) {
  const int lane = tid & 63, wave = tid >> 6;
#pragma unroll
  for (int i = 0; i < 8; ++i) LD16(hv[i], ssm_k + (size_t)(c0 + i) * S_ + tid * 8, "nt");
  asm volatile("global_load_dword %0, %1, off" : "=v"(hx) : "v"(ssm_k + (size_t)(c0 + (lane & 7)) * S_ + (wave < 3 ? (wave + 1) * 512 : 0)) : "memory");
  asm volatile("global_load_dword %0, %1, off" : "=v"(dD) : "v"(ssm_D + c0 + 2 * wave) : "memory");
}
template <int N>
__device__ __forceinline__ void h_wait(u32x4v (&hv)[8], unsigned& hx, unsigned& dD) {
  asm volatile("s_waitcnt vmcnt(%10)" : "+v"(hv[0]), "+v"(hv[1]), "+v"(hv[2]), "+v"(hv[3]), "+v"(hv[4]), "+v"(hv[5]), "+v"(hv[6]), "+v"(hv[7]), "+v"(hx), "+v"(dD) : "n"(N) : "memory");
}
__device__ __forceinline__ void h_spread(const u32x4v (&hv)[8], unsigned hx, char* lds, int tid) {
  if (tid < 32 && (tid & 3) != 3) *(u32x4v*)(lds + (tid >> 2) * CV_CH + ((tid >> 1) & 1) * CV_HP + (tid & 1) * 16) = (u32x4v){0u, 0u, 0u, 0u};
  *(u32x4v*)(lds + (tid >> 5) * CV_CH + 2 * CV_HP + 4096 + (tid & 31) * 16) = (u32x4v){0u, 0u, 0u, 0u};
#pragma unroll
  for (int i = 0; i < 8; ++i) {
    char* base = lds + i * CV_CH + 32 + tid * 16;
    unsigned hn = __builtin_amdgcn_update_dpp(0, (int)hv[i][0], 0x130, 0xf, 0xf, true);
    const unsigned last = __builtin_amdgcn_readlane(hx, i);
    if ((tid & 63) == 63) hn = last;
#define AB(a, b) __builtin_amdgcn_alignbyte(a, b, 2)
    if (tid == 0) *(u32x4v*)(lds + i * CV_CH + CV_HP + 16) = (u32x4v){0u, 0u, 0u, hv[i][0] << 16};
    *(u32x4v*)(base) = hv[i];
    *(u32x4v*)(base + CV_HP) = (u32x4v){AB(hv[i][1], hv[i][0]), AB(hv[i][2], hv[i][1]), AB(hv[i][3], hv[i][2]), AB(hn, hv[i][3])};
#undef AB
  }
}

__device__ __forceinline__ void pads(char* __restrict__ ws, int w, int lane, unsigned ep) {
  if (w < 132) {
    const int k = w < 128 ? w >> 5 : w - 128, u = k == 0 ? -1 : k == 1 ? 128 : k == 2 ? 131 : 260;
    st16(ws + (w < 128 ? OFF_HS + u * 32768 + (w & 31) * 1024 : OFF_FH + u * 1024) + lane * 16, __builtin_bit_cast(u16x8, (u32x4v){w >= 128 && !(lane & 7) ? ep : 0u, 0u, 0u, 0u}));
  }
}
__device__ __forceinline__ void prewrite(char* __restrict__ ws, unsigned off, unsigned str, int rep, int tid) {
#pragma unroll
  for (int i = 0; i < 2; ++i) st16(ws + off + (rep + 4 * (tid >> 3) + 132 * i) * str + (tid & 7) * 16, __builtin_bit_cast(u16x8, (u32x4v){0u, 0u, 0u, 0u}));
}

__device__ __forceinline__ void conv_part(char* __restrict__ ws, unsigned dD, char* lds, int g, int tid, int lane, int wave, unsigned ep, const u16* __restrict__ w3, const u16* __restrict__ w4,
                                          const char* wp, unsigned wstep) {
  const __amdgpu_buffer_rsrc_t rs = __builtin_amdgcn_make_buffer_rsrc(ws, 0, WS_USED, 0x00020000);
  const u16* kvr = (const u16*)(ws + OFF_KVR) + (size_t)(8 * g + 2 * wave) * S_;
  auto wait1 = [&](unsigned off) {
#pragma unroll 1
    for (int it = 0; it < 20000; ++it) {
      if (__all(__hip_atomic_load((gu32*)(ws + off), __ATOMIC_RELAXED, __HIP_MEMORY_SCOPE_AGENT) == ep)) break;
      __builtin_amdgcn_s_sleep(KSLEEP);
    }
    asm volatile("" ::: "memory");
  };
  const unsigned lb = (unsigned)(size_t)lds;
  auto u_fetch = [&](int part) {
#pragma unroll
    for (int j = 0; j < 2; ++j)
      asm volatile("s_mov_b32 m0, %1\n\ts_nop 0\n\tglobal_load_lds_dwordx4 %0, off sc1" :: "v"(kvr + j * S_ + part * 512 + lane * 8),
                   "s"(lb + (2 * wave + j) * CV_CH + 2 * CV_HP + part * 1024) : "memory");
  };
  const unsigned xo = (tid >> 4) * 32768 + g * 256 + (tid & 15) * 16;
  u32x4v qv[6];
  u32x2v qs[6];
  auto q_fetch = [&](int s0, int ns) {
#pragma unroll
    for (int i = 0; i < 6; ++i)
      if (i < ns) {
        asm volatile("buffer_load_dwordx4 %0, %1, %2, %3 offen sc1" : "=v"(qv[i]) : "v"(xo), "s"(rs), "s"(OFF_XP + (s0 + i) * 524288) : "memory");
        asm volatile("buffer_load_dwordx2 %0, %1, %2, %3 offen sc1" : "=v"(qs[i]) : "v"(tid * 8), "s"(rs), "s"(OFF_QS + (s0 + i) * 2048) : "memory");
      }
  };
  const int cg = g >> 5;
  const int l15 = lane & 15, lg = lane >> 4, cp = (l15 + 1) & 1;
  const unsigned ao = cp * CV_HP + 2 * (8 * lg + (l15 + 1 - cp)), bo = 2 * CV_HP + 2 * (2032 - 16 * l15 + 8 * lg) - 2 * 256 * 7;
  const unsigned own = lb + 2 * wave * CV_CH;
  char* stg = lds + CV_STG;
  s16x8 ad[2];
#pragma unroll
  for (int j = 0; j < 2; ++j) {
    const int e = 15 - l15 - 8 * lg;
    const unsigned d = (j ? dD >> 16 : dD & 0xffffu) << ((e & 1) * 16);
    ad[j] = __builtin_bit_cast(s16x8, (u32x4v){e >> 1 == 0 ? d : 0u, e >> 1 == 1 ? d : 0u, e >> 1 == 2 ? d : 0u, e >> 1 == 3 ? d : 0u});
  }
  f32x4 acc[2][6];
  unsigned f0, f1 = 0;
  bool mid;
  auto finish = [&](auto s0_c, auto ns_c) {
    constexpr int S0 = decltype(s0_c)::value, NS = decltype(ns_c)::value, A0 = 6 - NS;
#pragma unroll
    for (int s = 0; s < NS; ++s)
#pragma unroll
      for (int r = 0; r < 4; ++r) *(unsigned*)(stg + s * 4352 + l15 * 272 + (4 * lg + r) * 16 + wave * 4) = pk2((f32x2){acc[0][A0 + s][r], acc[1][A0 + s][r]});
    if (NS == 2) {
      asm volatile("s_waitcnt vmcnt(0)" : "+v"(qv[0]), "+v"(qv[1]), "+v"(qs[0]), "+v"(qs[1]), "+v"(f0) :: "memory");
      mid = __all(f0 == ep);
      if (mid) { u_fetch(2); u_fetch(1); }
    } else asm volatile("" : "+v"(qv[0]), "+v"(qv[1]), "+v"(qv[2]), "+v"(qv[3]), "+v"(qv[4]), "+v"(qv[5]), "+v"(qs[0]), "+v"(qs[1]), "+v"(qs[2]), "+v"(qs[3]), "+v"(qs[4]), "+v"(qs[5]) :: "memory");
    BARRIER();
#pragma unroll
    for (int s = 0; s < NS; ++s) {
      const u32x4v y4 = *(const u32x4v*)(stg + s * 4352 + (tid >> 4) * 272 + (tid & 15) * 16);
      const f32x2 rd2 = {__uint_as_float(qs[s][0]), __uint_as_float(qs[s][0])}, nm2 = {__uint_as_float(qs[s][1]), __uint_as_float(qs[s][1])};
      u32x4v o;
#pragma unroll
      for (int e = 0; e < 4; ++e) {
        const f32x2 x = up2(qv[s][e]);
        o[e] = pk2(__builtin_elementwise_fma(__builtin_elementwise_fma(x, rd2, nm2), up2(y4[e]), x));
      }
      __builtin_amdgcn_raw_buffer_store_b128(o, rs, xo, OFF_XP + (S0 + s) * 524288, 17);
      __builtin_amdgcn_raw_buffer_store_b128(y4, rs, xo, OFF_YF + (S0 + s) * 524288, 17);
    }
    CTS(S0 ? 7 : 5);
  };
  auto count = [&](int s0, int ns) {
    WAIT_VM(0);
    BARRIER();
    if (tid < 4 * ns) __hip_atomic_fetch_add((gu32*)(ws + OFF_FXP + (s0 + (tid >> 2)) * 16384 + (tid & 3) * 1024 + (g >> 4) * 128), 1u, __ATOMIC_RELAXED, __HIP_MEMORY_SCOPE_AGENT);
  };
  auto pair = [&](unsigned bb) {
    u32x2v ax[2][2][2][2];
    s16x8 bx[2][2][2];
    auto rdA = [&](int q) {
#pragma unroll
      for (int j = 0; j < 2; ++j)
#pragma unroll
        for (int m = 0; m < 2; ++m) {
          asm volatile("ds_read2_b32 %0, %1 offset0:%2 offset1:%3" : "=v"(ax[q & 1][j][m][0]) : "v"(own + j * CV_CH + ao), "n"(128 * m + 16 * q), "n"(128 * m + 16 * q + 1));
          asm volatile("ds_read2_b32 %0, %1 offset0:%2 offset1:%3" : "=v"(ax[q & 1][j][m][1]) : "v"(own + j * CV_CH + ao), "n"(128 * m + 16 * q + 2), "n"(128 * m + 16 * q + 3));
          asm volatile("ds_read_b128 %0, %1 offset:%2" : "=v"(bx[q & 1][j][m]) : "v"(bb), "n"(j * CV_CH + 512 * (1 - m) + 64 * q));
        }
    };
    rdA(0);
    sfor<8>([&](auto q_c) {
      constexpr int q = decltype(q_c)::value, p = q & 1;
      if (q + 1 < 8) rdA(q + 1);
      asm volatile("s_waitcnt lgkmcnt(%0)" :: "n"(q + 1 < 8 ? 12 : 0) : "memory");
      __builtin_amdgcn_sched_barrier(0);
#pragma unroll
      for (int j = 0; j < 2; ++j) {
        const s16x8 a0f = __builtin_bit_cast(s16x8, __builtin_shufflevector(ax[p][j][0][0], ax[p][j][0][1], 0, 1, 2, 3)), a1f = __builtin_bit_cast(s16x8, __builtin_shufflevector(ax[p][j][1][0], ax[p][j][1][1], 0, 1, 2, 3));
        if (q == 0) {
          acc[j][4] = __builtin_amdgcn_mfma_f32_16x16x32_bf16(ad[j], bx[p][j][0], acc[j][4], 0, 0, 0);
          acc[j][5] = __builtin_amdgcn_mfma_f32_16x16x32_bf16(ad[j], bx[p][j][1], acc[j][5], 0, 0, 0);
        }
        acc[j][4] = __builtin_amdgcn_mfma_f32_16x16x32_bf16(a0f, bx[p][j][0], acc[j][4], 0, 0, 0);
        acc[j][5] = __builtin_amdgcn_mfma_f32_16x16x32_bf16(a0f, bx[p][j][1], acc[j][5], 0, 0, 0);
        acc[j][5] = __builtin_amdgcn_mfma_f32_16x16x32_bf16(a1f, bx[p][j][0], acc[j][5], 0, 0, 0);
      }
      __builtin_amdgcn_sched_barrier(0);
    });
  };
  wait1(OFF_FKV + ((lane & 15) * 4 + cg) * 128);
  CTS(3);
  u_fetch(3);
  wait1(OFF_FY + (lane & 31) * 128);
  q_fetch(0, 2);
  const unsigned fm = OFF_FKV + ((16 + (lane & 31)) * 4 + cg) * 128;
  asm volatile("global_load_dword %0, %1, %2 sc1" : "=v"(f0) : "v"(fm), "s"(ws) : "memory");
  CTS(4);
#pragma unroll
  for (int i = 0; i < 4; ++i) acc[i >> 1][4 + (i & 1)] = (f32x4){0.f, 0.f, 0.f, 0.f};
  pair(own + bo + 512 * 6);
  finish(std::integral_constant<int, 0>{}, std::integral_constant<int, 2>{});
  u32x2v a0[8][2], a1[8][2];
  s16x8 b0[6], b1[6];
  unsigned pa[4], pb = own + bo;
#pragma unroll
  for (int i = 0; i < 4; ++i) pa[i] = own + ao + 1024 * i;
  auto rd1 = [&](int rr, int i, u32x2v (&ah)[8][2], s16x8 (&bfr)[6]) {
    const int m = i < 18 ? i / 3 : 6 + (i - 18) / 2, e = i < 18 ? i % 3 : i & 1;
    if (e < 2) asm volatile("ds_read2_b32 %0, %1 offset0:%2 offset1:%3" : "=v"(ah[m][e & 1]) : "v"(pa[m >> 1]), "n"(128 * (m & 1) + 2 * e + 16 * rr), "n"(128 * (m & 1) + 2 * e + 16 * rr + 1));
    else asm volatile("ds_read_b128 %0, %1 offset:%2" : "=v"(bfr[m < 6 ? m : 0]) : "v"(pb), "n"(512 * (7 - m) + 64 * rr));
  };
  auto round = [&](f32x4 (&ac)[6], u32x2v (&ah)[8][2], s16x8 (&bfr)[6], u32x2v (&ahn)[8][2], s16x8 (&bfn)[6], int rr, unsigned step, bool pf) {
    int k = 0;
#pragma unroll
    for (int s = 2; s <= 7; ++s)
#pragma unroll
      for (int m = 0; m <= s; ++m) {
        if (s - m > 5) continue;
        const s16x8 af = __builtin_bit_cast(s16x8, __builtin_shufflevector(ah[m][0], ah[m][1], 0, 1, 2, 3));
        ac[s - 2] = __builtin_amdgcn_mfma_f32_16x16x32_bf16(af, bfr[s - m > 5 ? 0 : s - m], ac[s - 2], 0, 0, 0);
        __builtin_amdgcn_sched_barrier(0);
        if (m == s) asm volatile("s_waitcnt lgkmcnt(12)" ::: "memory");
#pragma unroll
        for (int i = 0; i < 22; ++i)
          if (i * 4 / 3 == k) {
            rd1(rr, i, ahn, bfn);
            if (rr && (i == 4 || i == 10 || i == 16 || i == 21)) asm volatile("v_add_u32 %0, %1, %0" : "+v"(pa[i / 6]) : "s"(step));
            if (rr && i == 17) asm volatile("v_add_u32 %0, %1, %0" : "+v"(pb) : "s"(step));
          }
        if (k == 7 && rr && pf) asm volatile("global_load_lds_dword %0, off\n\tv_lshl_add_u64 %0, %0, 0, %1" : "+v"(wp) : "s"((unsigned long long)wstep) : "memory");
        ++k;
        __builtin_amdgcn_sched_barrier(0);
      }
  };
  if (!mid) { wait1(fm); u_fetch(2); u_fetch(1); WAIT_VM(0); }
  WAIT_VM(4);
#pragma unroll
  for (int i = 0; i < 22; ++i) rd1(0, i, a0, b0);
  WAIT_LGKM0();
  auto channel = [&](auto j_c) {
    constexpr int j = decltype(j_c)::value;
    asm volatile("s_mov_b32 m0, %0" :: "s"(lb + 163584) : "memory");
    __builtin_amdgcn_sched_barrier(0);
#pragma unroll
    for (int s = 2; s <= 7; ++s) acc[j][s - 2] = s < 6 ? __builtin_amdgcn_mfma_f32_16x16x32_bf16(ad[j], b0[s], (f32x4){0.f, 0.f, 0.f, 0.f}, 0, 0, 0) : (f32x4){0.f, 0.f, 0.f, 0.f};
#pragma unroll 1
    for (int r = 0; r < 8; r += 2) {
      if (j == 0 && r == 6) {
        asm volatile("global_load_dword %0, %1, %2 sc1" : "+v"(f0) : "v"(OFF_FY + (32 + lane) * 128), "s"(ws) : "memory");
        asm volatile("global_load_dword %0, %1, %2 sc1" : "+v"(f1) : "v"(lane < 32 ? OFF_FY + (96 + lane) * 128 : OFF_FKV + ((48 + (lane & 15)) * 4 + cg) * 128), "s"(ws) : "memory");
      }
      if (j == 0 && r == 2) {
        asm volatile("s_waitcnt vmcnt(0)\n\ts_barrier" ::: "memory");
        if (tid < 8) __hip_atomic_fetch_add((gu32*)(ws + OFF_FXP + (tid >> 2) * 16384 + (tid & 3) * 1024 + (g >> 4) * 128), 1u, __ATOMIC_RELAXED, __HIP_MEMORY_SCOPE_AGENT);
      }
      round(acc[j], a0, b0, a1, b1, 1, r < 6 ? 128 : j ? 0 : CV_CH - 384, PFL >> j & 1);
      round(acc[j], a1, b1, a0, b0, 0, 0, false);
    }
    WAIT_LGKM0();
    __builtin_amdgcn_sched_barrier(0);
  };
  f0 = 0;
  channel(std::integral_constant<int, 0>{});
  CTS(13);
  asm volatile("s_waitcnt vmcnt(%2)" : "+v"(f0), "+v"(f1) : "n"(PFL & 1) : "memory");
  if (!__all(f0 == ep && f1 == ep)) {
    wait1(OFF_FY + (32 + lane) * 128);
    wait1(lane < 32 ? OFF_FY + (96 + lane) * 128 : OFF_FKV + ((48 + (lane & 15)) * 4 + cg) * 128);
  }
  u_fetch(0);
  q_fetch(2, 6);
  touch_pol(wp, lb + 163584);
  touch_pol(wp + (tid < 192 ? 64 * 2048 : 0), lb + 163584);
  CTS(15);
  channel(std::integral_constant<int, 1>{});
  WAIT_VM(2 + (PFL & 2) * 2);
  CTS(14);
  pair(own + bo);
  CTS(11);
  finish(std::integral_constant<int, 2>{}, std::integral_constant<int, 6>{});
  count(2, 6);
}
__device__ __forceinline__ float sum2_seg(float s, float q) {
  s += __builtin_bit_cast(float, __builtin_amdgcn_update_dpp(0, __builtin_bit_cast(int, s), 0x128, 0xf, 0xf, true));
  q += __builtin_bit_cast(float, __builtin_amdgcn_update_dpp(0, __builtin_bit_cast(int, q), 0x128, 0xf, 0xf, true));
  asm volatile("s_nop 1\n\tv_permlane16_swap_b32 %0, %1" : "+v"(s), "+v"(q));
  s += q;
  q = s;
  asm volatile("s_nop 1\n\tv_permlane32_swap_b32 %0, %1" : "+v"(s), "+v"(q));
  return s + q;
}
#define LASTSLEEP 8
__device__ __forceinline__ void wait_flags(const char* p, int lane, int n, unsigned ep) {
  gu32* f = (gu32*)(p + (lane < n ? lane : 0) * 128);
#pragma unroll 1
  for (int it = 0; it < 10000; ++it) {
    if (__all(__hip_atomic_load(f, __ATOMIC_RELAXED, __HIP_MEMORY_SCOPE_AGENT) == ep)) break;
    __builtin_amdgcn_s_sleep(LASTSLEEP);
  }
  asm volatile("" ::: "memory");
}
struct QRegs { u32x4v a[4], b[4]; };
__device__ __forceinline__ void q_load(QRegs& R, const u16* __restrict__ q, int f, int lane, int wave) {
  const u16* p = q + (size_t)(f * 16 + (lane & 7)) * D_ + wave * 256 + (lane >> 3) * 8;
#pragma unroll
  for (int i = 0; i < 4; ++i) { LD16(R.a[i], p + i * 64, "nt"); LD16(R.b[i], p + 8 * D_ + i * 64, "nt"); }
}
template <int N>
__device__ __forceinline__ void q_wait(QRegs& R, unsigned& e) {
  asm volatile("s_waitcnt vmcnt(%9)" : "+v"(R.a[0]), "+v"(R.a[1]), "+v"(R.a[2]), "+v"(R.a[3]), "+v"(R.b[0]), "+v"(R.b[1]), "+v"(R.b[2]), "+v"(R.b[3]), "+v"(e) : "n"(N) : "memory");
}
__device__ __forceinline__ void row_stats(const u32x4v (&xq)[4], char* lds, int k, int lane, int wave, float& mean, float& var) {
  f32x2* red = (f32x2*)(lds + 16640);
  const int r8 = lane & 7, seg = lane >> 3;
  f32x2 s = {0.f, 0.f}, sq = {0.f, 0.f};
#pragma unroll
  for (int i = 0; i < 16; ++i) { const f32x2 x = up2(xq[i >> 2][i & 3]); s += x; sq = __builtin_elementwise_fma(x, x, sq); }
  const float a = sum2_seg(s[0] + s[1], sq[0] + sq[1]);
  if ((seg & 5) == 0) ((float*)red)[((k * 4 + wave) * 8 + r8) * 2 + (seg >> 1)] = a;
  BARRIER();
  const f32x2 t = (red[(k * 4) * 8 + r8] + red[(k * 4 + 1) * 8 + r8]) + (red[(k * 4 + 2) * 8 + r8] + red[(k * 4 + 3) * 8 + r8]);
  mean = t[0] * (1.0f / 1024.0f);
  var = fmaxf(t[1] * (1.0f / 1024.0f) - mean * mean, 0.f);
}
__device__ __forceinline__ void q_store(char* __restrict__ ws, int t, int lane, int wave, const u32x4v (&xq)[4], float mean, float var) {
  const __amdgpu_buffer_rsrc_t rs = __builtin_amdgcn_make_buffer_rsrc(ws, 0, WS_USED, 0x00020000);
  const int r8 = lane & 7, seg = lane >> 3;
  const unsigned fo = (t >> 1) * 32768 + (wave * 8 + (seg >> 2)) * 1024 + ((seg & 3) * 16 + (t & 1) * 8 + r8) * 16;
#pragma unroll
  for (int i = 0; i < 4; ++i) __builtin_amdgcn_raw_buffer_store_b128(xq[i], rs, fo + i * 2048, OFF_XP, 17);
  const float rd = __builtin_amdgcn_rsqf(var + 1e-5f);
  if (wave == 0 && seg == 0) __builtin_amdgcn_raw_buffer_store_b64((u32x2v){__float_as_uint(rd), __float_as_uint(-mean * rd)}, rs, t * 64 + r8 * 8, OFF_QS, 17);
}
__device__ __forceinline__ void row_last(char* __restrict__ ws, u16* __restrict__ out, int t, int lane, int wave, unsigned ep) {
  const __amdgpu_buffer_rsrc_t rs = __builtin_amdgcn_make_buffer_rsrc(ws, 0, WS_USED, 0x00020000);
  const int r8 = lane & 7, seg = lane >> 3, qt = t & 3, vbp = LB + (t >> 2);
  const unsigned fo = vbp * 65536 + (qt >> 1) * 32768 + (wave * 8 + (seg >> 2)) * 1024 + ((seg & 3) * 16 + (qt & 1) * 8 + r8) * 16;
  wait_flags(ws + OFF_FP + (2 * LB + (t >> 1)) * 2048, lane, 16, ep);
  u32x4v p0[4], p1[4];
#pragma unroll
  for (int i = 0; i < 4; ++i) {
    p0[i] = __builtin_bit_cast(u32x4v, __builtin_amdgcn_raw_buffer_load_b128(rs, fo + i * 2048, OFF_P0, 16));
    p1[i] = __builtin_bit_cast(u32x4v, __builtin_amdgcn_raw_buffer_load_b128(rs, fo + i * 2048, OFF_P1, 16));
  }
#pragma unroll
  for (int i = 0; i < 4; ++i) {
    u32x4v o;
#pragma unroll
    for (int e = 0; e < 4; ++e) o[e] = pk2(up2(p0[i][e]) + up2(p1[i][e]));
    st16(out + (size_t)(t * 8 + r8) * D_ + wave * 256 + i * 64 + seg * 8, __builtin_bit_cast(u16x8, o));
  }
}

__device__ __forceinline__ int swzB(int r) { return ((((r >> 1) & 1) | (((r >> 3) & 1) << 1)) << 1); }
#define FLW 1
#define MAP3 1
#define NRW 18
#define PFROWS 110
__global__ __launch_bounds__(256, 1) void flow_kernel(const u16* __restrict__ q, const u16* __restrict__ kin, const u16* __restrict__ vin,
                                                      const u16* __restrict__ shift_k, const u16* __restrict__ ssm_k, const u16* __restrict__ ssm_D,
                                                      const u16* __restrict__ w1, const u16* __restrict__ w2,
                                                      const u16* __restrict__ w3, const u16* __restrict__ w4, char* __restrict__ ws,
                                                      u16* __restrict__ out) {
  __shared__ __attribute__((aligned(16))) char lds[163840];
  const int tid = threadIdx.x, lane = tid & 63, wave = __builtin_amdgcn_readfirstlane(tid >> 6);
  const int b = blockIdx.x, xcd = b & 7, idx = b >> 3;
  const unsigned sLds = (unsigned)(size_t)lds;
  const int oct = idx >> 3, o8 = idx & 7;
  int kind, rep, slice, kh = 0;
  if (idx < 8) { kind = 1 + (xcd & 1); rep = xcd >> 1; slice = o8; }
  else if (idx < 20) {
    const int j = xcd * 12 + idx - 8;
    rep = (j * 57) >> 10;
    kind = j < 90 ? 3 : j < 94 ? 5 : 6; slice = j < 90 ? j - rep * 18 : j < 94 ? 18 : j - 96 + NRW; rep = j < 90 ? rep : j < 94 ? j - 90 : 0;
    if (j < 90) {
      const int i = idx - 8, hi = i >= 6;
      rep = xcd < 6 ? (xcd & 1) * 2 + hi : 4;
      slice = xcd < 6 ? (xcd >> 1) * 6 + i - 6 * hi : xcd == 6 ? i : 12 + i;
      const int nE = xcd < 7 ? 8 : 4;
      if (i < nE) { const int e = xcd * 8 + i, t = (e * 205) >> 11, k = e - 10 * t, k3 = k < 3 ? 0 : k < 6 ? 1 : 2; slice = 3 * t + k3; rep = k - 3 * k3; }
      else { const int l = xcd * 4 + i - nE, t = (l * 205) >> 10, k = l - 5 * t; slice = 3 * t + (k >> 1); rep = k == 4 ? 4 : 3 + (k & 1); }
    }
  } else if (idx < 30) {
    const int h = idx < 28 ? xcd : 8 + (xcd >> 2);
    kind = 4; rep = h >> 1; kh = h & 1; slice = idx < 28 ? idx - 20 : (xcd & 3) * 2 + idx - 28;
    kh = xcd & 1; rep = (idx - 20) >> 1; slice = (xcd & 6) + (idx & 1);
  } else { kind = 6; rep = 0; slice = xcd * 2 + idx - 30; }
  CTS_INIT();
  CTS(0);
  {
    CODE_RANGE("Lpc%=", "Lcode_g12_b", c0, n0) CODE_RANGE("Lcode_r_e", "Lcode_g4_b", c7, n7)
    touch_lds(tid < 224 ? c0 + (tid * 128 < n0 ? tid * 128 : 0) : c7 + ((tid - 224) * 128 < n7 ? (tid - 224) * 128 : 0), sLds + 163584);
  }
  const int j3 = xcd * 12 + idx - 8, j4 = xcd * 10 + idx - 20;
  const unsigned sScr = sLds + 163584;
  const char* cp;
  unsigned co0, co1;
  {
    unsigned lo, hi, n;
    asm volatile("s_getpc_b64 s[98:99]\nLpd%=:\n"
                 "s_mov_b32 %0, Lcode_r_b-Lpd%=\ns_mov_b32 %2, Lcode_r_e-Lcode_r_b\n"
                 "s_cmp_eq_u32 %3, 1\ns_cselect_b32 %0, Lcode_g12_b-Lpd%=, %0\ns_cselect_b32 %2, Lcode_g2_b-Lcode_g12_b, %2\n"
                 "s_cmp_eq_u32 %3, 2\ns_cselect_b32 %0, Lcode_g2_b-Lpd%=, %0\ns_cselect_b32 %2, Lcode_g12_e-Lcode_g2_b, %2\n"
                 "s_cmp_eq_u32 %3, 3\ns_cselect_b32 %0, Lcode_g3_b-Lpd%=, %0\ns_cselect_b32 %2, Lcode_g3_e-Lcode_g3_b, %2\n"
                 "s_cmp_eq_u32 %3, 4\ns_cselect_b32 %0, Lcode_g4_b-Lpd%=, %0\ns_cselect_b32 %2, Lcode_g4_e-Lcode_g4_b, %2\n"
                 "s_cmp_eq_u32 %3, 5\ns_cselect_b32 %0, Lcode_g3o_b-Lpd%=, %0\ns_cselect_b32 %2, Lcode_g3o_e-Lcode_g3o_b, %2\n"
                 "s_add_u32 %0, s98, %0\ns_addc_u32 %1, s99, 0"
                 : "=&s"(lo), "=&s"(hi), "=&s"(n) : "s"(kind) : "s98", "s99", "scc");
    cp = (const char*)(((unsigned long long)hi << 32) | lo);
    co0 = tid * 128 < n ? tid * 128 : 0;
    co1 = 32768 + tid * 128 < n ? 32768 + tid * 128 : 0;
  }
  auto code_touch = [&]() { touch_lds(cp + co0, sScr); touch_lds(cp + co1, sScr); };
  unsigned ep = 0;
#define EP_LOAD() asm volatile("global_load_dword %0, %1, %2 sc1" : "=v"(ep) : "v"(0u), "s"(ws + OFF_EP) : "memory")
  if (kind == 1) { code_touch(); prewrite(ws, OFF_FH + slice * 128, 1024, rep, tid); }
  else {
    const int cv = kind == 4 ? j4 : kind == 6 ? 80 + slice : kind != 3 ? -1 : xcd < 7 ? (idx >= 16 ? 98 + xcd * 4 + idx - 16 : -1) : (idx >= 12 ? 126 + idx - 12 : -1);
    auto epoch = [&]() { const unsigned e = __builtin_amdgcn_readfirstlane(ep) + 1u; return e ? e : 1u; };
    auto q_unit = [&](QRegs& QR, int f) {
      float ma, va, mb, vb_;
      row_stats(QR.a, lds + CV_STG, 0, lane, wave, ma, va);
      row_stats(QR.b, lds + CV_STG, 1, lane, wave, mb, vb_);
      q_store(ws, 2 * f, lane, wave, QR.a, ma, va);
      q_store(ws, 2 * f + 1, lane, wave, QR.b, mb, vb_);
    };
    auto publish = [&](unsigned off) {
      BARRIER();
      if (tid == 0) __hip_atomic_store((gu32*)(ws + off), ep, __ATOMIC_RELAXED, __HIP_MEMORY_SCOPE_AGENT);
    };
    KvRegs KR;
    SkRegs SK;
    if (cv >= 0) {
      u32x4v hv[8];
      unsigned hx, dD;
      EP_LOAD();
      h_load(hv, hx, dD, ssm_k, ssm_D, 8 * cv, tid);
      kv_load(KR, kin, vin, 64 + cv, tid);
      sk_load(SK, shift_k, cv, tid);
      code_touch();
      const int li = idx < 14 ? idx - 12 : idx - 16;
      touch_lds((const char*)((xcd & 1) ? w2 : w1) + (size_t)(li * 128 + (tid & 127)) * 1024 + 384, sScr);
      h_wait<18>(hv, hx, dD);
      asm volatile("" : "+v"(ep));
      ep = epoch();
      CTS(12);
      h_spread(hv, hx, lds, tid);
      kv_wait<3>(KR, SK);
      kv_store(KR, SK, ws, lds + CV_STG, 64 + cv, tid);
      CTS(1);
      WAIT_VM(0);
      publish(OFF_FKV + (64 + cv) * 128);
      CTS(2);
      const char* wp = kind == 4 ? (const char*)w4 + (size_t)(kh * 1792 + (tid >> 1)) * 2048 + slice * 256 + (tid & 1) * 128 :
                       kind == 3 ? (const char*)w3 + (size_t)(tid >> 2) * 7168 + slice * 384 + (tid & 3) % 3 * 128 : (const char*)w4;
      wp = (const char*)w4 + (size_t)((xcd & 1) * 1792 + li * 112 + (tid >> 2)) * 2048 + (xcd & 6) * 256 + (tid & 3) * 128;
      conv_part(ws, dD, lds, cv, tid, lane, wave, ep, w3, w4, wp, kind == 4 ? 128 * 2048 : kind == 3 ? 64 * 7168 : 0);
      CTS(8);
    } else {
      const int n = kind == 2 ? 64 + rep * 8 + slice : (idx < 14 || xcd < 7 ? idx - 8 : idx - 10) * 8 + xcd;
      QRegs QR;
      if (n < 64) {
        EP_LOAD();
        kv_load(KR, kin, vin, n, tid);
        sk_load(SK, shift_k, n, tid);
        kv_wait<0>(KR, SK);
        asm volatile("" : "+v"(ep));
        ep = epoch();
        CTS(12);
        KvRegs K2;
        kv_store(KR, SK, ws, lds, n, tid);
        pads(ws, n * 4 + wave, lane, ep);
        if (n < 8)
          st16(ws + OFF_FXP + (lane >> 3) * 16384 + wave * 1024 + (2 * (n & 3) + (n >> 2)) * 128 + (lane & 7) * 16, __builtin_bit_cast(u16x8, (u32x4v){ep - 16u, 0u, 0u, 0u}));
        CTS(1);
        kv_load(K2, kin, vin, 192 + n, tid);
        WAIT_VM(11);
        publish(OFF_FKV + n * 128);
        CTS(2);
        q_load(QR, q, 32 + n, lane, wave);
        kv_wait<8>(K2, SK);
        kv_store(K2, SK, ws, lds + 16896, 192 + n, tid);
        q_wait<0>(QR, ep);
        publish(OFF_FKV + (192 + n) * 128);
        CTS(3);
      } else {
        QRegs Q0;
        EP_LOAD();
        q_load(Q0, q, n - 64, lane, wave);
        q_wait<0>(Q0, ep);
        ep = epoch();
        CTS(12);
        q_load(QR, q, 32 + n, lane, wave);
        q_unit(Q0, n - 64);
        CTS(1);
        q_wait<0>(QR, ep);
        publish(OFF_FY + (n - 64) * 128);
        CTS(2);
      }
      q_unit(QR, 32 + n);
      {
        const char* wp = (const char*)w2 + (size_t)(n < 64 ? 0 : (n - 64) * 512 + tid) * 128;
        touch_lds(wp, sScr);
        touch_lds(wp + (n < 64 ? 0 : 32768), sScr);
      }
      WAIT_VM(2);
      publish(OFF_FY + (32 + n) * 128);
      CTS(9);
      if (n >= 64) {
        prewrite(ws, OFF_FX1 + slice * 128, 1024, rep, tid);
        prewrite(ws, OFF_FP + slice * 128, 2048, rep, tid);
        prewrite(ws, OFF_FP + (8 + slice) * 128, 2048, rep, tid);
        prewrite(ws, OFF_FG + slice * 128, 2560, rep, tid);
        prewrite(ws, OFF_FG + (8 + slice) * 128, 2560, rep, tid);
        if (slice < 3) prewrite(ws, OFF_FG + (16 + slice) * 128, 2560, rep, tid);
        st16(ws + OFF_FXP + slice * 128 + (132 + rep + 4 * (tid >> 3)) * 1024 + (tid & 7) * 16, __builtin_bit_cast(u16x8, (u32x4v){0u, 0u, 0u, 0u}));
      }
      code_touch();
      if (n < 64) {
#pragma unroll
        for (int i = 0; i < 4; ++i) {
          const unsigned x = rep * 1024 + i * 256 + tid, row = kind == 3 ? (x * 21846) >> 16 : x >> 1;
          if (rep < (kind == 3 ? 3 : 2)) touch_lds((const char*)w3 + (size_t)row * 7168 + slice * 384 + (x - row * (kind == 3 ? 3 : 2)) * 128, sScr);
        }
      }
    }
    BARRIER();
    CTS(10);
  }
  CTS_FLUSH();
  const unsigned sDbg0 = OFF_DBG + (b * 4 + wave) * 8192;
#define LANE_ID(x) int x; asm volatile("v_mbcnt_lo_u32_b32 %0, -1, 0\n\tv_mbcnt_hi_u32_b32 %0, -1, %0" : "=v"(x))
#define OOB 0x80000000u
#define COMMON(NF, FIN, FOUT, SN0, WS, LDW, NREP)                                                                                    \
  LANE_ID(ln);                                                                                                                       \
  const int l15 = ln & 15, lg = ln >> 4, lq = l15 >> 2, lp = ln & 3, rr = 8 * lg + lq;                                               \
  const unsigned vA = ln * 16, vFlag = (ln < FLW && wave == 3) ? (FOUT) + ln * 4 : OOB;                                                        \
  const unsigned vPollOff = wave < 2 && ln < (NF) ? (FIN) + ((NF) == 16 && ln >= 8 ? (ln & 2 ? ln : ln - 16) : ln) * 128 : OOB, vSnapOff = wave == 0 ? vPollOff : OOB;                   \
  const unsigned vSnapRd = (SN0) + (ln >> 5) * (WS) + ((ln & 31) < (NF) ? (ln & 31) : 0) * 4;                                        \
  const unsigned vWd0 = (ln >> 3) * (LDW) + ((ln & 7) ^ swzB(ln >> 3)) * 16, vWd1 = ((ln >> 3) + 8) * (LDW) + ((ln & 7) ^ swzB(8 + (ln >> 3))) * 16; \
  const unsigned vWr0 = (wave >> 1) * 8192 + rr * 128 + (((wave & 1) * 4 + lp) ^ swzB(rr)) * 16, vWr1 = vWr0 + 8;                    \
  const unsigned vWr2 = 16384 + rr * 128 + ((2 * wave + (lp >> 1)) ^ swzB(rr)) * 16 + (lp & 1) * 8;                                  \
                                            \
  const unsigned sStep = (NREP) << 15, sU0 = rep << 15, sX1 = ((NREP) == 4 ? 128 + rep : 128 + (rep + 2) % 5) << 15;                 \
  const unsigned sX2 = ((NREP) == 4 ? 260 + rep : 260 + (rep + 4) % 5) << 15, sDbg = sDbg0, sBuf = kind
  constexpr unsigned AUX = 131072;
  if (kind == 3 || kind == 5) {
    COMMON(8, OFF_FX1, OFF_FG + slice * 128, AUX + 1536, 2048, 7168, kind == 3 ? 5 : 4);
    const unsigned vSrc = OFF_X1 + wave * 4096 + vA, vOut = OFF_G + (slice * 6 + wave) * 1024 + vA;
    const unsigned vOut2 = OFF_G + (slice * 6 + 4 + (wave >> 1)) * 1024 + (((wave & 1) * 2 + (lg >> 1)) * 16 + l15) * 16 + (lg & 1) * 8;
    const unsigned vAuxS = OFF_ST + (ln & 7) * 128 + (wave * 8 + (ln >> 3)) * 4, vX = AUX + wave * 2048 + ln * 4;
    const unsigned vStW = (ln & 7) == 0 ? AUX + 1280 + (wave * 8 + (ln >> 3)) * 4 : AUX + 24576 + wave * 2048 + 1536 + ln * 4, vStR = AUX + 1280 + l15 * 8;
    const unsigned sWw = 16 * wave * 7168 + slice * 384;
    if (kind == 3) { asm volatile(R"(
Lcode_g3_b:
s_mov_b64 s[36:37],%0
s_mov_b32 s38,111193216
s_mov_b32 s39,0x00020000
s_mov_b32 s54,%1
s_mov_b32 s55,%2
s_mov_b32 s56,%3
s_mov_b32 s58,%4
s_mov_b32 s83,%5
s_mov_b32 s82,%6
s_mov_b32 s84,%7
v_mov_b32 v0,%8
v_mov_b32 v2,%9
v_mov_b32 v3,%10
v_mov_b32 v4,%11
v_mov_b32 v6,%12
v_mov_b32 v7,%13
v_mov_b32 v8,%14
v_mov_b32 v9,%15
v_mov_b32 v10,%16
v_mov_b32 v11,%17
v_mov_b32 v12,%18
v_mov_b32 v28,%19
v_mov_b32 v29,%20
v_mov_b32 v30,%21
v_mov_b32 v31,%22
v_mov_b32 v32,%23
s_mov_b64 s[40:41],%24
s_mov_b32 s42,7340032
s_mov_b32 s43,0x00020000
s_lshl_b32 s88,s54,11
s_mov_b32 s69,111193088
buffer_load_dword v5,off,s[36:39],s69 sc1
s_add_u32 m0,s88,0
s_add_u32 s61,s82,0
buffer_load_dwordx4 v28,s[40:43],s61 offen offset:0  lds
s_add_u32 m0,s88,1024
s_nop 0
buffer_load_dwordx4 v29,s[40:43],s61 offen offset:0  lds
s_add_u32 m0,s88,8064
s_nop 0
buffer_load_dwordx4 v28,s[40:43],s61 offen offset:128  lds
s_add_u32 m0,s88,9088
s_nop 0
buffer_load_dwordx4 v29,s[40:43],s61 offen offset:128  lds
s_add_u32 m0,s88,16128
s_nop 0
buffer_load_dwordx4 v28,s[40:43],s61 offen offset:256  lds
s_add_u32 m0,s88,17152
s_nop 0
buffer_load_dwordx4 v29,s[40:43],s61 offen offset:256  lds
s_add_u32 m0,s88,24576
s_add_u32 s61,s82,458752
buffer_load_dwordx4 v28,s[40:43],s61 offen offset:0  lds
s_add_u32 m0,s88,25600
s_nop 0
buffer_load_dwordx4 v29,s[40:43],s61 offen offset:0  lds
s_add_u32 m0,s88,32640
s_nop 0
buffer_load_dwordx4 v28,s[40:43],s61 offen offset:128  lds
s_add_u32 m0,s88,33664
s_nop 0
buffer_load_dwordx4 v29,s[40:43],s61 offen offset:128  lds
s_add_u32 m0,s88,40704
s_nop 0
buffer_load_dwordx4 v28,s[40:43],s61 offen offset:256  lds
s_add_u32 m0,s88,41728
s_nop 0
buffer_load_dwordx4 v29,s[40:43],s61 offen offset:256  lds
s_add_u32 m0,s88,49152
s_add_u32 s61,s82,917504
buffer_load_dwordx4 v28,s[40:43],s61 offen offset:0  lds
s_add_u32 m0,s88,50176
s_nop 0
buffer_load_dwordx4 v29,s[40:43],s61 offen offset:0  lds
s_add_u32 m0,s88,57216
s_nop 0
buffer_load_dwordx4 v28,s[40:43],s61 offen offset:128  lds
s_add_u32 m0,s88,58240
s_nop 0
buffer_load_dwordx4 v29,s[40:43],s61 offen offset:128  lds
s_add_u32 m0,s88,65280
s_nop 0
buffer_load_dwordx4 v28,s[40:43],s61 offen offset:256  lds
s_add_u32 m0,s88,66304
s_nop 0
buffer_load_dwordx4 v29,s[40:43],s61 offen offset:256  lds
s_add_u32 m0,s88,73728
s_add_u32 s61,s82,1376256
buffer_load_dwordx4 v28,s[40:43],s61 offen offset:0  lds
s_add_u32 m0,s88,74752
s_nop 0
buffer_load_dwordx4 v29,s[40:43],s61 offen offset:0  lds
s_add_u32 m0,s88,81792
s_nop 0
buffer_load_dwordx4 v28,s[40:43],s61 offen offset:128  lds
s_add_u32 m0,s88,82816
s_nop 0
buffer_load_dwordx4 v29,s[40:43],s61 offen offset:128  lds
s_add_u32 m0,s88,89856
s_nop 0
buffer_load_dwordx4 v28,s[40:43],s61 offen offset:256  lds
s_add_u32 m0,s88,90880
s_nop 0
buffer_load_dwordx4 v29,s[40:43],s61 offen offset:256  lds
v_add_u32 v40,49152,v30
v_add_u32 v41,49152,v31
v_add_u32 v42,49152,v32
v_add_u32 v43,98304,v30
v_add_u32 v44,98304,v31
v_add_u32 v45,98304,v32
v_mov_b32 v84,0x3f803f80
v_mov_b32 v85,0x3f803f80
v_mov_b32 v86,0x3f803f80
v_mov_b32 v87,0x3f803f80
s_waitcnt vmcnt(18)
s_barrier
s_add_u32 m0,s88,98304
s_add_u32 s61,s82,1835008
buffer_load_dwordx4 v28,s[40:43],s61 offen offset:0  lds
s_add_u32 m0,s88,99328
ds_read_b64_tr_b16 a[0:1],v30 offset:0
buffer_load_dwordx4 v29,s[40:43],s61 offen offset:0  lds
s_add_u32 m0,s88,106368
ds_read_b64_tr_b16 a[2:3],v30 offset:512
buffer_load_dwordx4 v28,s[40:43],s61 offen offset:128  lds
s_add_u32 m0,s88,107392
ds_read_b64_tr_b16 a[4:5],v31 offset:0
buffer_load_dwordx4 v29,s[40:43],s61 offen offset:128  lds
s_add_u32 m0,s88,114432
ds_read_b64_tr_b16 a[6:7],v31 offset:512
buffer_load_dwordx4 v28,s[40:43],s61 offen offset:256  lds
s_add_u32 m0,s88,115456
ds_read_b64_tr_b16 a[8:9],v32 offset:0
buffer_load_dwordx4 v29,s[40:43],s61 offen offset:256  lds
ds_read_b64_tr_b16 a[10:11],v32 offset:512
ds_read_b64_tr_b16 a[12:13],v30 offset:4096
ds_read_b64_tr_b16 a[14:15],v30 offset:4608
ds_read_b64_tr_b16 a[16:17],v31 offset:4096
ds_read_b64_tr_b16 a[18:19],v31 offset:4608
ds_read_b64_tr_b16 a[20:21],v32 offset:4096
ds_read_b64_tr_b16 a[22:23],v32 offset:4608
s_waitcnt vmcnt(18)
s_barrier
s_add_u32 m0,s88,122880
s_add_u32 s61,s82,2293760
buffer_load_dwordx4 v28,s[40:43],s61 offen offset:0  lds
s_add_u32 m0,s88,123904
ds_read_b64_tr_b16 a[24:25],v30 offset:24576
buffer_load_dwordx4 v29,s[40:43],s61 offen offset:0  lds
s_add_u32 m0,s88,130944
ds_read_b64_tr_b16 a[26:27],v30 offset:25088
buffer_load_dwordx4 v28,s[40:43],s61 offen offset:128  lds
s_add_u32 m0,s88,131968
ds_read_b64_tr_b16 a[28:29],v31 offset:24576
buffer_load_dwordx4 v29,s[40:43],s61 offen offset:128  lds
s_add_u32 m0,s88,139008
ds_read_b64_tr_b16 a[30:31],v31 offset:25088
buffer_load_dwordx4 v28,s[40:43],s61 offen offset:256  lds
s_add_u32 m0,s88,140032
ds_read_b64_tr_b16 a[32:33],v32 offset:24576
buffer_load_dwordx4 v29,s[40:43],s61 offen offset:256  lds
ds_read_b64_tr_b16 a[34:35],v32 offset:25088
ds_read_b64_tr_b16 a[36:37],v30 offset:28672
ds_read_b64_tr_b16 a[38:39],v30 offset:29184
ds_read_b64_tr_b16 a[40:41],v31 offset:28672
ds_read_b64_tr_b16 a[42:43],v31 offset:29184
ds_read_b64_tr_b16 a[44:45],v32 offset:28672
ds_read_b64_tr_b16 a[46:47],v32 offset:29184
s_waitcnt lgkmcnt(12)
v_mfma_f32_16x16x32_bf16 v[16:19],a[0:3],v[84:87],0
v_mfma_f32_16x16x32_bf16 v[20:23],a[4:7],v[84:87],0
v_mfma_f32_16x16x32_bf16 v[24:27],a[8:11],v[84:87],0
v_mfma_f32_16x16x32_bf16 v[16:19],a[12:15],v[84:87],v[16:19]
v_mfma_f32_16x16x32_bf16 v[20:23],a[16:19],v[84:87],v[20:23]
v_mfma_f32_16x16x32_bf16 v[24:27],a[20:23],v[84:87],v[24:27]
s_waitcnt vmcnt(18)
s_barrier
s_add_u32 m0,s88,0
s_add_u32 s61,s82,2752512
buffer_load_dwordx4 v28,s[40:43],s61 offen offset:0  lds
s_add_u32 m0,s88,1024
ds_read_b64_tr_b16 a[48:49],v40 offset:0
buffer_load_dwordx4 v29,s[40:43],s61 offen offset:0  lds
s_add_u32 m0,s88,8064
ds_read_b64_tr_b16 a[50:51],v40 offset:512
buffer_load_dwordx4 v28,s[40:43],s61 offen offset:128  lds
s_add_u32 m0,s88,9088
ds_read_b64_tr_b16 a[52:53],v41 offset:0
buffer_load_dwordx4 v29,s[40:43],s61 offen offset:128  lds
s_add_u32 m0,s88,16128
ds_read_b64_tr_b16 a[54:55],v41 offset:512
buffer_load_dwordx4 v28,s[40:43],s61 offen offset:256  lds
s_add_u32 m0,s88,17152
ds_read_b64_tr_b16 a[56:57],v42 offset:0
buffer_load_dwordx4 v29,s[40:43],s61 offen offset:256  lds
ds_read_b64_tr_b16 a[58:59],v42 offset:512
ds_read_b64_tr_b16 a[60:61],v40 offset:4096
ds_read_b64_tr_b16 a[62:63],v40 offset:4608
ds_read_b64_tr_b16 a[64:65],v41 offset:4096)"
R"(
ds_read_b64_tr_b16 a[66:67],v41 offset:4608
ds_read_b64_tr_b16 a[68:69],v42 offset:4096
ds_read_b64_tr_b16 a[70:71],v42 offset:4608
s_waitcnt lgkmcnt(12)
v_mfma_f32_16x16x32_bf16 v[16:19],a[24:27],v[84:87],v[16:19]
v_mfma_f32_16x16x32_bf16 v[20:23],a[28:31],v[84:87],v[20:23]
v_mfma_f32_16x16x32_bf16 v[24:27],a[32:35],v[84:87],v[24:27]
v_mfma_f32_16x16x32_bf16 v[16:19],a[36:39],v[84:87],v[16:19]
v_mfma_f32_16x16x32_bf16 v[20:23],a[40:43],v[84:87],v[20:23]
v_mfma_f32_16x16x32_bf16 v[24:27],a[44:47],v[84:87],v[24:27]
s_waitcnt vmcnt(18)
s_barrier
s_add_u32 m0,s88,24576
s_add_u32 s61,s82,3211264
buffer_load_dwordx4 v28,s[40:43],s61 offen offset:0  lds
s_add_u32 m0,s88,25600
ds_read_b64_tr_b16 a[72:73],v40 offset:24576
buffer_load_dwordx4 v29,s[40:43],s61 offen offset:0  lds
s_add_u32 m0,s88,32640
ds_read_b64_tr_b16 a[74:75],v40 offset:25088
buffer_load_dwordx4 v28,s[40:43],s61 offen offset:128  lds
s_add_u32 m0,s88,33664
ds_read_b64_tr_b16 a[76:77],v41 offset:24576
buffer_load_dwordx4 v29,s[40:43],s61 offen offset:128  lds
s_add_u32 m0,s88,40704
ds_read_b64_tr_b16 a[78:79],v41 offset:25088
buffer_load_dwordx4 v28,s[40:43],s61 offen offset:256  lds
s_add_u32 m0,s88,41728
ds_read_b64_tr_b16 a[80:81],v42 offset:24576
buffer_load_dwordx4 v29,s[40:43],s61 offen offset:256  lds
ds_read_b64_tr_b16 a[82:83],v42 offset:25088
ds_read_b64_tr_b16 a[84:85],v40 offset:28672
ds_read_b64_tr_b16 a[86:87],v40 offset:29184
ds_read_b64_tr_b16 a[88:89],v41 offset:28672
ds_read_b64_tr_b16 a[90:91],v41 offset:29184
ds_read_b64_tr_b16 a[92:93],v42 offset:28672
ds_read_b64_tr_b16 a[94:95],v42 offset:29184
s_waitcnt lgkmcnt(12)
v_mfma_f32_16x16x32_bf16 v[16:19],a[48:51],v[84:87],v[16:19]
v_mfma_f32_16x16x32_bf16 v[20:23],a[52:55],v[84:87],v[20:23]
v_mfma_f32_16x16x32_bf16 v[24:27],a[56:59],v[84:87],v[24:27]
v_mfma_f32_16x16x32_bf16 v[16:19],a[60:63],v[84:87],v[16:19]
v_mfma_f32_16x16x32_bf16 v[20:23],a[64:67],v[84:87],v[20:23]
v_mfma_f32_16x16x32_bf16 v[24:27],a[68:71],v[84:87],v[24:27]
s_waitcnt vmcnt(18)
s_barrier
s_add_u32 m0,s88,49152
s_add_u32 s61,s82,3670016
buffer_load_dwordx4 v28,s[40:43],s61 offen offset:0  lds
s_add_u32 m0,s88,50176
ds_read_b64_tr_b16 a[96:97],v43 offset:0
buffer_load_dwordx4 v29,s[40:43],s61 offen offset:0  lds
s_add_u32 m0,s88,57216
ds_read_b64_tr_b16 a[98:99],v43 offset:512
buffer_load_dwordx4 v28,s[40:43],s61 offen offset:128  lds
s_add_u32 m0,s88,58240
ds_read_b64_tr_b16 a[100:101],v44 offset:0
buffer_load_dwordx4 v29,s[40:43],s61 offen offset:128  lds
s_add_u32 m0,s88,65280
ds_read_b64_tr_b16 a[102:103],v44 offset:512
buffer_load_dwordx4 v28,s[40:43],s61 offen offset:256  lds
s_add_u32 m0,s88,66304
ds_read_b64_tr_b16 a[104:105],v45 offset:0
buffer_load_dwordx4 v29,s[40:43],s61 offen offset:256  lds
ds_read_b64_tr_b16 a[106:107],v45 offset:512
ds_read_b64_tr_b16 a[108:109],v43 offset:4096
ds_read_b64_tr_b16 a[110:111],v43 offset:4608
ds_read_b64_tr_b16 a[112:113],v44 offset:4096
ds_read_b64_tr_b16 a[114:115],v44 offset:4608
ds_read_b64_tr_b16 a[116:117],v45 offset:4096
ds_read_b64_tr_b16 a[118:119],v45 offset:4608
s_waitcnt lgkmcnt(12)
v_mfma_f32_16x16x32_bf16 v[16:19],a[72:75],v[84:87],v[16:19]
v_mfma_f32_16x16x32_bf16 v[20:23],a[76:79],v[84:87],v[20:23]
v_mfma_f32_16x16x32_bf16 v[24:27],a[80:83],v[84:87],v[24:27]
v_mfma_f32_16x16x32_bf16 v[16:19],a[84:87],v[84:87],v[16:19]
v_mfma_f32_16x16x32_bf16 v[20:23],a[88:91],v[84:87],v[20:23]
v_mfma_f32_16x16x32_bf16 v[24:27],a[92:95],v[84:87],v[24:27]
s_waitcnt vmcnt(18)
s_barrier
s_add_u32 m0,s88,73728
s_add_u32 s61,s82,4128768
buffer_load_dwordx4 v28,s[40:43],s61 offen offset:0  lds
s_add_u32 m0,s88,74752
ds_read_b64_tr_b16 a[120:121],v43 offset:24576
buffer_load_dwordx4 v29,s[40:43],s61 offen offset:0  lds
s_add_u32 m0,s88,81792
ds_read_b64_tr_b16 a[122:123],v43 offset:25088
buffer_load_dwordx4 v28,s[40:43],s61 offen offset:128  lds
s_add_u32 m0,s88,82816
ds_read_b64_tr_b16 a[124:125],v44 offset:24576
buffer_load_dwordx4 v29,s[40:43],s61 offen offset:128  lds
s_add_u32 m0,s88,89856
ds_read_b64_tr_b16 a[126:127],v44 offset:25088
buffer_load_dwordx4 v28,s[40:43],s61 offen offset:256  lds
s_add_u32 m0,s88,90880
ds_read_b64_tr_b16 a[128:129],v45 offset:24576
buffer_load_dwordx4 v29,s[40:43],s61 offen offset:256  lds
ds_read_b64_tr_b16 a[130:131],v45 offset:25088
ds_read_b64_tr_b16 a[132:133],v43 offset:28672
ds_read_b64_tr_b16 a[134:135],v43 offset:29184
ds_read_b64_tr_b16 a[136:137],v44 offset:28672
ds_read_b64_tr_b16 a[138:139],v44 offset:29184
ds_read_b64_tr_b16 a[140:141],v45 offset:28672
ds_read_b64_tr_b16 a[142:143],v45 offset:29184
s_waitcnt lgkmcnt(12)
v_mfma_f32_16x16x32_bf16 v[16:19],a[96:99],v[84:87],v[16:19]
v_mfma_f32_16x16x32_bf16 v[20:23],a[100:103],v[84:87],v[20:23]
v_mfma_f32_16x16x32_bf16 v[24:27],a[104:107],v[84:87],v[24:27]
v_mfma_f32_16x16x32_bf16 v[16:19],a[108:111],v[84:87],v[16:19]
v_mfma_f32_16x16x32_bf16 v[20:23],a[112:115],v[84:87],v[20:23]
v_mfma_f32_16x16x32_bf16 v[24:27],a[116:119],v[84:87],v[24:27]
s_waitcnt vmcnt(18)
s_barrier
s_add_u32 m0,s88,98304
s_add_u32 s61,s82,4587520
buffer_load_dwordx4 v28,s[40:43],s61 offen offset:0  lds
s_add_u32 m0,s88,99328
ds_read_b64_tr_b16 a[144:145],v30 offset:0
buffer_load_dwordx4 v29,s[40:43],s61 offen offset:0  lds
s_add_u32 m0,s88,106368
ds_read_b64_tr_b16 a[146:147],v30 offset:512
buffer_load_dwordx4 v28,s[40:43],s61 offen offset:128  lds
s_add_u32 m0,s88,107392
ds_read_b64_tr_b16 a[148:149],v31 offset:0
buffer_load_dwordx4 v29,s[40:43],s61 offen offset:128  lds
s_add_u32 m0,s88,114432
ds_read_b64_tr_b16 a[150:151],v31 offset:512
buffer_load_dwordx4 v28,s[40:43],s61 offen offset:256  lds
s_add_u32 m0,s88,115456
ds_read_b64_tr_b16 a[152:153],v32 offset:0
buffer_load_dwordx4 v29,s[40:43],s61 offen offset:256  lds
ds_read_b64_tr_b16 a[154:155],v32 offset:512
ds_read_b64_tr_b16 a[156:157],v30 offset:4096
ds_read_b64_tr_b16 a[158:159],v30 offset:4608
ds_read_b64_tr_b16 a[160:161],v31 offset:4096
ds_read_b64_tr_b16 a[162:163],v31 offset:4608
ds_read_b64_tr_b16 a[164:165],v32 offset:4096
ds_read_b64_tr_b16 a[166:167],v32 offset:4608
s_waitcnt lgkmcnt(12)
v_mfma_f32_16x16x32_bf16 v[16:19],a[120:123],v[84:87],v[16:19]
v_mfma_f32_16x16x32_bf16 v[20:23],a[124:127],v[84:87],v[20:23]
v_mfma_f32_16x16x32_bf16 v[24:27],a[128:131],v[84:87],v[24:27]
v_mfma_f32_16x16x32_bf16 v[16:19],a[132:135],v[84:87],v[16:19]
v_mfma_f32_16x16x32_bf16 v[20:23],a[136:139],v[84:87],v[20:23]
v_mfma_f32_16x16x32_bf16 v[24:27],a[140:143],v[84:87],v[24:27]
s_waitcnt vmcnt(18)
s_barrier
s_add_u32 m0,s88,122880
s_add_u32 s61,s82,5046272
buffer_load_dwordx4 v28,s[40:43],s61 offen offset:0  lds
s_add_u32 m0,s88,123904
ds_read_b64_tr_b16 a[168:169],v30 offset:24576
buffer_load_dwordx4 v29,s[40:43],s61 offen offset:0  lds
s_add_u32 m0,s88,130944
ds_read_b64_tr_b16 a[170:171],v30 offset:25088
buffer_load_dwordx4 v28,s[40:43],s61 offen offset:128  lds
s_add_u32 m0,s88,131968
ds_read_b64_tr_b16 a[172:173],v31 offset:24576
buffer_load_dwordx4 v29,s[40:43],s61 offen offset:128  lds
s_add_u32 m0,s88,139008
ds_read_b64_tr_b16 a[174:175],v31 offset:25088
buffer_load_dwordx4 v28,s[40:43],s61 offen offset:256  lds
s_add_u32 m0,s88,140032
ds_read_b64_tr_b16 a[176:177],v32 offset:24576
buffer_load_dwordx4 v29,s[40:43],s61 offen offset:256  lds
ds_read_b64_tr_b16 a[178:179],v32 offset:25088
ds_read_b64_tr_b16 a[180:181],v30 offset:28672
ds_read_b64_tr_b16 a[182:183],v30 offset:29184
ds_read_b64_tr_b16 a[184:185],v31 offset:28672
ds_read_b64_tr_b16 a[186:187],v31 offset:29184
ds_read_b64_tr_b16 a[188:189],v32 offset:28672
ds_read_b64_tr_b16 a[190:191],v32 offset:29184
s_waitcnt lgkmcnt(12)
v_mfma_f32_16x16x32_bf16 v[16:19],a[144:147],v[84:87],v[16:19]
v_mfma_f32_16x16x32_bf16 v[20:23],a[148:151],v[84:87],v[20:23]
v_mfma_f32_16x16x32_bf16 v[24:27],a[152:155],v[84:87],v[24:27]
v_mfma_f32_16x16x32_bf16 v[16:19],a[156:159],v[84:87],v[16:19]
v_mfma_f32_16x16x32_bf16 v[20:23],a[160:163],v[84:87],v[20:23]
v_mfma_f32_16x16x32_bf16 v[24:27],a[164:167],v[84:87],v[24:27]
s_waitcnt vmcnt(18)
s_barrier
s_add_u32 m0,s88,0
s_add_u32 s61,s82,5505024
buffer_load_dwordx4 v28,s[40:43],s61 offen offset:0  lds
s_add_u32 m0,s88,1024
ds_read_b64_tr_b16 a[192:193],v40 offset:0
buffer_load_dwordx4 v29,s[40:43],s61 offen offset:0  lds
s_add_u32 m0,s88,8064
ds_read_b64_tr_b16 a[194:195],v40 offset:512
buffer_load_dwordx4 v28,s[40:43],s61 offen offset:128  lds
s_add_u32 m0,s88,9088
ds_read_b64_tr_b16 a[196:197],v41 offset:0
buffer_load_dwordx4 v29,s[40:43],s61 offen offset:128  lds
s_add_u32 m0,s88,16128
ds_read_b64_tr_b16 a[198:199],v41 offset:512
buffer_load_dwordx4 v28,s[40:43],s61 offen offset:256  lds
s_add_u32 m0,s88,17152
ds_read_b64_tr_b16 a[200:201],v42 offset:0
buffer_load_dwordx4 v29,s[40:43],s61 offen offset:256  lds)"
R"(
ds_read_b64_tr_b16 a[202:203],v42 offset:512
ds_read_b64_tr_b16 a[204:205],v40 offset:4096
ds_read_b64_tr_b16 a[206:207],v40 offset:4608
ds_read_b64_tr_b16 a[208:209],v41 offset:4096
ds_read_b64_tr_b16 a[210:211],v41 offset:4608
ds_read_b64_tr_b16 a[212:213],v42 offset:4096
ds_read_b64_tr_b16 a[214:215],v42 offset:4608
s_waitcnt lgkmcnt(12)
v_mfma_f32_16x16x32_bf16 v[16:19],a[168:171],v[84:87],v[16:19]
v_mfma_f32_16x16x32_bf16 v[20:23],a[172:175],v[84:87],v[20:23]
v_mfma_f32_16x16x32_bf16 v[24:27],a[176:179],v[84:87],v[24:27]
v_mfma_f32_16x16x32_bf16 v[16:19],a[180:183],v[84:87],v[16:19]
v_mfma_f32_16x16x32_bf16 v[20:23],a[184:187],v[84:87],v[20:23]
v_mfma_f32_16x16x32_bf16 v[24:27],a[188:191],v[84:87],v[24:27]
s_waitcnt vmcnt(18)
s_barrier
s_add_u32 m0,s88,24576
s_add_u32 s61,s82,5963776
buffer_load_dwordx4 v28,s[40:43],s61 offen offset:0  lds
s_add_u32 m0,s88,25600
ds_read_b64_tr_b16 a[216:217],v40 offset:24576
buffer_load_dwordx4 v29,s[40:43],s61 offen offset:0  lds
s_add_u32 m0,s88,32640
ds_read_b64_tr_b16 a[218:219],v40 offset:25088
buffer_load_dwordx4 v28,s[40:43],s61 offen offset:128  lds
s_add_u32 m0,s88,33664
ds_read_b64_tr_b16 a[220:221],v41 offset:24576
buffer_load_dwordx4 v29,s[40:43],s61 offen offset:128  lds
s_add_u32 m0,s88,40704
ds_read_b64_tr_b16 a[222:223],v41 offset:25088
buffer_load_dwordx4 v28,s[40:43],s61 offen offset:256  lds
s_add_u32 m0,s88,41728
ds_read_b64_tr_b16 a[224:225],v42 offset:24576
buffer_load_dwordx4 v29,s[40:43],s61 offen offset:256  lds
ds_read_b64_tr_b16 a[226:227],v42 offset:25088
ds_read_b64_tr_b16 a[228:229],v40 offset:28672
ds_read_b64_tr_b16 a[230:231],v40 offset:29184
ds_read_b64_tr_b16 a[232:233],v41 offset:28672
ds_read_b64_tr_b16 a[234:235],v41 offset:29184
ds_read_b64_tr_b16 a[236:237],v42 offset:28672
ds_read_b64_tr_b16 a[238:239],v42 offset:29184
s_waitcnt lgkmcnt(12)
v_mfma_f32_16x16x32_bf16 v[16:19],a[192:195],v[84:87],v[16:19]
v_mfma_f32_16x16x32_bf16 v[20:23],a[196:199],v[84:87],v[20:23]
v_mfma_f32_16x16x32_bf16 v[24:27],a[200:203],v[84:87],v[24:27]
v_mfma_f32_16x16x32_bf16 v[16:19],a[204:207],v[84:87],v[16:19]
v_mfma_f32_16x16x32_bf16 v[20:23],a[208:211],v[84:87],v[20:23]
v_mfma_f32_16x16x32_bf16 v[24:27],a[212:215],v[84:87],v[24:27]
s_waitcnt vmcnt(18)
s_barrier
s_add_u32 m0,s88,49152
s_add_u32 s61,s82,6422528
buffer_load_dwordx4 v28,s[40:43],s61 offen offset:0  lds
s_add_u32 m0,s88,50176
ds_read_b64_tr_b16 a[240:241],v43 offset:0
buffer_load_dwordx4 v29,s[40:43],s61 offen offset:0  lds
s_add_u32 m0,s88,57216
ds_read_b64_tr_b16 a[242:243],v43 offset:512
buffer_load_dwordx4 v28,s[40:43],s61 offen offset:128  lds
s_add_u32 m0,s88,58240
ds_read_b64_tr_b16 a[244:245],v44 offset:0
buffer_load_dwordx4 v29,s[40:43],s61 offen offset:128  lds
s_add_u32 m0,s88,65280
ds_read_b64_tr_b16 a[246:247],v44 offset:512
buffer_load_dwordx4 v28,s[40:43],s61 offen offset:256  lds
s_add_u32 m0,s88,66304
ds_read_b64_tr_b16 a[248:249],v45 offset:0
buffer_load_dwordx4 v29,s[40:43],s61 offen offset:256  lds
ds_read_b64_tr_b16 a[250:251],v45 offset:512
ds_read_b64_tr_b16 a[252:253],v43 offset:4096
ds_read_b64_tr_b16 a[254:255],v43 offset:4608
ds_read_b64_tr_b16 v[128:129],v44 offset:4096
ds_read_b64_tr_b16 v[130:131],v44 offset:4608
ds_read_b64_tr_b16 v[132:133],v45 offset:4096
ds_read_b64_tr_b16 v[134:135],v45 offset:4608
s_waitcnt lgkmcnt(12)
v_mfma_f32_16x16x32_bf16 v[16:19],a[216:219],v[84:87],v[16:19]
v_mfma_f32_16x16x32_bf16 v[20:23],a[220:223],v[84:87],v[20:23]
v_mfma_f32_16x16x32_bf16 v[24:27],a[224:227],v[84:87],v[24:27]
v_mfma_f32_16x16x32_bf16 v[16:19],a[228:231],v[84:87],v[16:19]
v_mfma_f32_16x16x32_bf16 v[20:23],a[232:235],v[84:87],v[20:23]
v_mfma_f32_16x16x32_bf16 v[24:27],a[236:239],v[84:87],v[24:27]
s_waitcnt vmcnt(18)
s_barrier
s_add_u32 m0,s88,73728
s_add_u32 s61,s82,6881280
buffer_load_dwordx4 v28,s[40:43],s61 offen offset:0  lds
s_add_u32 m0,s88,74752
ds_read_b64_tr_b16 v[136:137],v43 offset:24576
buffer_load_dwordx4 v29,s[40:43],s61 offen offset:0  lds
s_add_u32 m0,s88,81792
ds_read_b64_tr_b16 v[138:139],v43 offset:25088
buffer_load_dwordx4 v28,s[40:43],s61 offen offset:128  lds
s_add_u32 m0,s88,82816
ds_read_b64_tr_b16 v[140:141],v44 offset:24576
buffer_load_dwordx4 v29,s[40:43],s61 offen offset:128  lds
s_add_u32 m0,s88,89856
ds_read_b64_tr_b16 v[142:143],v44 offset:25088
buffer_load_dwordx4 v28,s[40:43],s61 offen offset:256  lds
s_add_u32 m0,s88,90880
ds_read_b64_tr_b16 v[144:145],v45 offset:24576
buffer_load_dwordx4 v29,s[40:43],s61 offen offset:256  lds
ds_read_b64_tr_b16 v[146:147],v45 offset:25088
ds_read_b64_tr_b16 v[148:149],v43 offset:28672
ds_read_b64_tr_b16 v[150:151],v43 offset:29184
ds_read_b64_tr_b16 v[152:153],v44 offset:28672
ds_read_b64_tr_b16 v[154:155],v44 offset:29184
ds_read_b64_tr_b16 v[156:157],v45 offset:28672
ds_read_b64_tr_b16 v[158:159],v45 offset:29184
s_waitcnt lgkmcnt(12)
v_mfma_f32_16x16x32_bf16 v[16:19],a[240:243],v[84:87],v[16:19]
v_mfma_f32_16x16x32_bf16 v[20:23],a[244:247],v[84:87],v[20:23]
v_mfma_f32_16x16x32_bf16 v[24:27],a[248:251],v[84:87],v[24:27]
v_mfma_f32_16x16x32_bf16 v[16:19],a[252:255],v[84:87],v[16:19]
v_mfma_f32_16x16x32_bf16 v[20:23],v[128:131],v[84:87],v[20:23]
v_mfma_f32_16x16x32_bf16 v[24:27],v[132:135],v[84:87],v[24:27]
s_waitcnt vmcnt(18)
s_barrier
ds_read_b64_tr_b16 v[160:161],v30 offset:0
ds_read_b64_tr_b16 v[162:163],v30 offset:512
ds_read_b64_tr_b16 v[164:165],v31 offset:0
ds_read_b64_tr_b16 v[166:167],v31 offset:512
ds_read_b64_tr_b16 v[168:169],v32 offset:0
ds_read_b64_tr_b16 v[170:171],v32 offset:512
ds_read_b64_tr_b16 v[172:173],v30 offset:4096
ds_read_b64_tr_b16 v[174:175],v30 offset:4608
ds_read_b64_tr_b16 v[176:177],v31 offset:4096
ds_read_b64_tr_b16 v[178:179],v31 offset:4608
ds_read_b64_tr_b16 v[180:181],v32 offset:4096
ds_read_b64_tr_b16 v[182:183],v32 offset:4608
s_waitcnt lgkmcnt(12)
v_mfma_f32_16x16x32_bf16 v[16:19],v[136:139],v[84:87],v[16:19]
v_mfma_f32_16x16x32_bf16 v[20:23],v[140:143],v[84:87],v[20:23]
v_mfma_f32_16x16x32_bf16 v[24:27],v[144:147],v[84:87],v[24:27]
v_mfma_f32_16x16x32_bf16 v[16:19],v[148:151],v[84:87],v[16:19]
v_mfma_f32_16x16x32_bf16 v[20:23],v[152:155],v[84:87],v[20:23]
v_mfma_f32_16x16x32_bf16 v[24:27],v[156:159],v[84:87],v[24:27]
s_waitcnt vmcnt(12)
s_barrier
ds_read_b64_tr_b16 v[184:185],v30 offset:24576
ds_read_b64_tr_b16 v[186:187],v30 offset:25088
ds_read_b64_tr_b16 v[188:189],v31 offset:24576
ds_read_b64_tr_b16 v[190:191],v31 offset:25088
ds_read_b64_tr_b16 v[192:193],v32 offset:24576
ds_read_b64_tr_b16 v[194:195],v32 offset:25088
ds_read_b64_tr_b16 v[196:197],v30 offset:28672
ds_read_b64_tr_b16 v[198:199],v30 offset:29184
ds_read_b64_tr_b16 v[200:201],v31 offset:28672
ds_read_b64_tr_b16 v[202:203],v31 offset:29184
ds_read_b64_tr_b16 v[204:205],v32 offset:28672
ds_read_b64_tr_b16 v[206:207],v32 offset:29184
s_waitcnt lgkmcnt(12)
v_mfma_f32_16x16x32_bf16 v[16:19],v[160:163],v[84:87],v[16:19]
v_mfma_f32_16x16x32_bf16 v[20:23],v[164:167],v[84:87],v[20:23]
v_mfma_f32_16x16x32_bf16 v[24:27],v[168:171],v[84:87],v[24:27]
v_mfma_f32_16x16x32_bf16 v[16:19],v[172:175],v[84:87],v[16:19]
v_mfma_f32_16x16x32_bf16 v[20:23],v[176:179],v[84:87],v[20:23]
v_mfma_f32_16x16x32_bf16 v[24:27],v[180:183],v[84:87],v[24:27]
s_waitcnt vmcnt(6)
s_barrier
ds_read_b64_tr_b16 v[208:209],v40 offset:0
ds_read_b64_tr_b16 v[210:211],v40 offset:512
ds_read_b64_tr_b16 v[212:213],v41 offset:0
ds_read_b64_tr_b16 v[214:215],v41 offset:512
ds_read_b64_tr_b16 v[216:217],v42 offset:0
ds_read_b64_tr_b16 v[218:219],v42 offset:512
ds_read_b64_tr_b16 v[220:221],v40 offset:4096
ds_read_b64_tr_b16 v[222:223],v40 offset:4608
ds_read_b64_tr_b16 v[224:225],v41 offset:4096
ds_read_b64_tr_b16 v[226:227],v41 offset:4608
ds_read_b64_tr_b16 v[228:229],v42 offset:4096
ds_read_b64_tr_b16 v[230:231],v42 offset:4608
s_waitcnt lgkmcnt(12)
v_mfma_f32_16x16x32_bf16 v[16:19],v[184:187],v[84:87],v[16:19]
v_mfma_f32_16x16x32_bf16 v[20:23],v[188:191],v[84:87],v[20:23]
v_mfma_f32_16x16x32_bf16 v[24:27],v[192:195],v[84:87],v[24:27]
v_mfma_f32_16x16x32_bf16 v[16:19],v[196:199],v[84:87],v[16:19]
v_mfma_f32_16x16x32_bf16 v[20:23],v[200:203],v[84:87],v[20:23]
v_mfma_f32_16x16x32_bf16 v[24:27],v[204:207],v[84:87],v[24:27]
s_waitcnt vmcnt(0)
s_barrier
ds_read_b64_tr_b16 v[232:233],v40 offset:24576
ds_read_b64_tr_b16 v[234:235],v40 offset:25088
ds_read_b64_tr_b16 v[236:237],v41 offset:24576
ds_read_b64_tr_b16 v[238:239],v41 offset:25088
ds_read_b64_tr_b16 v[240:241],v42 offset:24576
ds_read_b64_tr_b16 v[242:243],v42 offset:25088
ds_read_b64_tr_b16 v[244:245],v40 offset:28672
ds_read_b64_tr_b16 v[246:247],v40 offset:29184
ds_read_b64_tr_b16 v[248:249],v41 offset:28672
ds_read_b64_tr_b16 v[250:251],v41 offset:29184
ds_read_b64_tr_b16 v[252:253],v42 offset:28672
ds_read_b64_tr_b16 v[254:255],v42 offset:29184
s_waitcnt lgkmcnt(12)
v_mfma_f32_16x16x32_bf16 v[16:19],v[208:211],v[84:87],v[16:19]
v_mfma_f32_16x16x32_bf16 v[20:23],v[212:215],v[84:87],v[20:23]
v_mfma_f32_16x16x32_bf16 v[24:27],v[216:219],v[84:87],v[24:27]
v_mfma_f32_16x16x32_bf16 v[16:19],v[220:223],v[84:87],v[16:19]
v_mfma_f32_16x16x32_bf16 v[20:23],v[224:227],v[84:87],v[20:23]
v_mfma_f32_16x16x32_bf16 v[24:27],v[228:231],v[84:87],v[24:27])"
R"(
s_waitcnt lgkmcnt(0)
v_mfma_f32_16x16x32_bf16 v[16:19],v[232:235],v[84:87],v[16:19]
v_mfma_f32_16x16x32_bf16 v[20:23],v[236:239],v[84:87],v[20:23]
v_mfma_f32_16x16x32_bf16 v[24:27],v[240:243],v[84:87],v[24:27]
v_mfma_f32_16x16x32_bf16 v[16:19],v[244:247],v[84:87],v[16:19]
v_mfma_f32_16x16x32_bf16 v[20:23],v[248:251],v[84:87],v[20:23]
v_mfma_f32_16x16x32_bf16 v[24:27],v[252:255],v[84:87],v[24:27]
s_waitcnt lgkmcnt(0)
s_barrier
v_readfirstlane_b32 s60,v5
s_nop 3
s_add_u32 s60,s60,1
s_cmp_eq_u32 s60,0
s_cselect_b32 s60,1,s60
v_mov_b32 v5,s60
s_mov_b32 s59,536870912
s_add_u32 s57,s56,131072
s_mov_b32 s79,0
s_mov_b32 s75,0x3a800000
s_mov_b32 s76,0xc0135761
s_mov_b32 s77,0x3d372713
s_mov_b32 s44,s59
s_mov_b32 s45,s59
s_mov_b32 s46,s59
s_mov_b32 s47,s59
s_mov_b32 s48,s83
s_add_u32 s49,s48,s55
s_cmp_eq_u32 s49,s56
s_cselect_b32 s49,s57,s49
s_cmp_eq_u32 s49,s58
s_cselect_b32 s49,s59,s49
s_add_u32 s50,s49,s55
s_cmp_eq_u32 s50,s56
s_cselect_b32 s50,s57,s50
s_cmp_eq_u32 s50,s58
s_cselect_b32 s50,s59,s50
s_add_u32 s51,s50,s55
s_cmp_eq_u32 s51,s56
s_cselect_b32 s51,s57,s51
s_cmp_eq_u32 s51,s58
s_cselect_b32 s51,s59,s51
s_add_u32 s52,s51,s55
s_cmp_eq_u32 s52,s56
s_cselect_b32 s52,s57,s52
s_cmp_eq_u32 s52,s58
s_cselect_b32 s52,s59,s52
s_lshl_b32 s63,s54,12
s_add_u32 s63,s63,65536
s_add_u32 s64,s63,32768
s_lshl_b32 s88,s54,10
s_add_u32 s88,s88,131072
s_lshl_b32 s78,s54,11
s_add_u32 s78,s78,132608
v_add_u32 v1,16384,v0
v_mov_b32 v13,0x3727c5ac
s_getpc_b64 s[90:91]
Lg3_pc_%=:
s_mov_b32 s68,Lg3_d1w0_%=-Lg3_pc_%=
s_cmp_eq_u32 s54,1
s_cselect_b32 s68,Lg3_d1w1_%=-Lg3_pc_%=,s68
s_cmp_eq_u32 s54,2
s_cselect_b32 s68,Lg3_d1w2_%=-Lg3_pc_%=,s68
s_cmp_eq_u32 s54,3
s_cselect_b32 s68,Lg3_d1w3_%=-Lg3_pc_%=,s68
s_add_u32 s92,s90,s68
s_addc_u32 s93,s91,0
s_mov_b32 s68,Lg3_d0w0_%=-Lg3_pc_%=
s_cmp_eq_u32 s54,1
s_cselect_b32 s68,Lg3_d0w1_%=-Lg3_pc_%=,s68
s_cmp_eq_u32 s54,2
s_cselect_b32 s68,Lg3_d0w2_%=-Lg3_pc_%=,s68
s_cmp_eq_u32 s54,3
s_cselect_b32 s68,Lg3_d0w3_%=-Lg3_pc_%=,s68
s_add_u32 s90,s90,s68
s_addc_u32 s91,s91,0
s_mov_b32 s66,s59
s_mov_b32 s65,0
s_mov_b32 s62,s63
s_branch Lg3_slow0_%=
Lg3_d0w0_%=:
v_mfma_f32_16x16x32_bf16 v[28:31],a[0:3],v[52:55],0
s_mov_b32 m0,s62
ds_read_b128 v[72:75],v2 offset:5120
v_mul_f32 v116,s75,v112
v_mfma_f32_16x16x32_bf16 v[32:35],a[4:7],v[52:55],0
buffer_load_dwordx4 v0,s[36:39],s61 offen offset:0 sc1 lds
v_mfma_f32_16x16x32_bf16 v[36:39],a[8:11],v[52:55],0
s_mul_i32 s73,s47,7
v_fma_f32 v113,v113,s75,v13
v_fma_f32 v113,-v116,v116,v113
v_mfma_f32_16x16x32_bf16 v[28:31],a[12:15],v[56:59],v[28:31]
s_lshr_b32 s73,s73,1
ds_read_b128 v[76:79],v2 offset:6144
v_max_f32 v113,v113,v13
v_mfma_f32_16x16x32_bf16 v[32:35],a[16:19],v[56:59],v[32:35]
s_add_u32 s53,s52,s55
v_rsq_f32 v112,v113
v_mfma_f32_16x16x32_bf16 v[36:39],a[20:23],v[56:59],v[36:39]
s_waitcnt lgkmcnt(3)
buffer_load_dwordx4 v0,s[36:39],s61 offen offset:1024 sc1 lds
v_mfma_f32_16x16x32_bf16 v[28:31],a[24:27],v[60:63],v[28:31]
s_cmp_eq_u32 s53,s56
ds_read_b128 v[80:83],v2 offset:7168
v_mul_f32 v117,s76,v112
v_mfma_f32_16x16x32_bf16 v[32:35],a[28:31],v[60:63],v[32:35]
s_cselect_b32 s53,s57,s53
v_mul_f32 v118,v112,v112
v_mul_f32 v119,v113,v112
v_mfma_f32_16x16x32_bf16 v[36:39],a[32:35],v[60:63],v[36:39]
s_cmp_eq_u32 s53,s58
v_mul_f32 v118,s77,v118
v_mul_f32 v118,v118,v117
v_mfma_f32_16x16x32_bf16 v[28:31],a[36:39],v[64:67],v[28:31]
buffer_load_dwordx4 v0,s[36:39],s61 offen offset:2048 sc1 lds
v_mfma_f32_16x16x32_bf16 v[32:35],a[40:43],v[64:67],v[32:35]
s_cselect_b32 s53,s59,s53
ds_read_b128 v[52:55],v2 offset:8192
v_fma_f32 v40,-v116,v16,v40
v_mfma_f32_16x16x32_bf16 v[36:39],a[44:47],v[64:67],v[36:39]
s_waitcnt lgkmcnt(3)
v_fma_f32 v41,-v116,v17,v41
v_fma_f32 v42,-v116,v18,v42
v_mfma_f32_16x16x32_bf16 v[28:31],a[48:51],v[68:71],v[28:31]
s_cmp_ge_u32 s66,s59
ds_read_b128 v[56:59],v2 offset:9216
v_fma_f32 v43,-v116,v19,v43
v_mfma_f32_16x16x32_bf16 v[32:35],a[52:55],v[68:71],v[32:35]
buffer_load_dwordx4 v0,s[36:39],s61 offen offset:3072 sc1 lds
v_mfma_f32_16x16x32_bf16 v[36:39],a[56:59],v[68:71],v[36:39]
s_cselect_b32 s86,s49,s50
v_mul_f32 v84,v40,v40
v_mul_f32 v85,v41,v41
v_mfma_f32_16x16x32_bf16 v[28:31],a[60:63],v[72:75],v[28:31]
s_lshr_b32 s68,s86,5
ds_read_b128 v[60:63],v2 offset:10240
v_mul_f32 v86,v42,v42
v_mfma_f32_16x16x32_bf16 v[32:35],a[64:67],v[72:75],v[32:35]
s_add_u32 m0,s78,0
v_mul_f32 v87,v43,v43
v_fma_f32 v84,v118,v84,v117
v_mfma_f32_16x16x32_bf16 v[36:39],a[68:71],v[72:75],v[36:39]
s_waitcnt lgkmcnt(3)
v_mul_f32 v84,v40,v84
v_fma_f32 v85,v118,v85,v117
v_mfma_f32_16x16x32_bf16 v[28:31],a[72:75],v[76:79],v[28:31]
ds_read_b128 v[64:67],v2 offset:11264
v_mul_f32 v85,v41,v85
v_mfma_f32_16x16x32_bf16 v[32:35],a[76:79],v[76:79],v[32:35]
v_exp_f32 v84,v84
v_mfma_f32_16x16x32_bf16 v[36:39],a[80:83],v[76:79],v[36:39]
v_exp_f32 v85,v85
v_mfma_f32_16x16x32_bf16 v[28:31],a[84:87],v[80:83],v[28:31]
ds_read_b128 v[68:71],v2 offset:12288
v_fma_f32 v84,v84,v119,v119
v_mfma_f32_16x16x32_bf16 v[32:35],a[88:91],v[80:83],v[32:35]
v_rcp_f32 v84,v84
v_mfma_f32_16x16x32_bf16 v[36:39],a[92:95],v[80:83],v[36:39]
s_waitcnt lgkmcnt(3)
v_fma_f32 v85,v85,v119,v119
v_fma_f32 v86,v118,v86,v117
v_mfma_f32_16x16x32_bf16 v[28:31],a[96:99],v[52:55],v[28:31]
ds_read_b128 v[72:75],v2 offset:13312
v_mul_f32 v86,v42,v86
v_mfma_f32_16x16x32_bf16 v[32:35],a[100:103],v[52:55],v[32:35]
v_exp_f32 v86,v86
v_mfma_f32_16x16x32_bf16 v[36:39],a[104:107],v[52:55],v[36:39]
v_rcp_f32 v85,v85
v_mfma_f32_16x16x32_bf16 v[28:31],a[108:111],v[56:59],v[28:31]
ds_read_b128 v[76:79],v2 offset:14336
v_fma_f32 v86,v86,v119,v119
v_mfma_f32_16x16x32_bf16 v[32:35],a[112:115],v[56:59],v[32:35]
v_rcp_f32 v86,v86
v_mfma_f32_16x16x32_bf16 v[36:39],a[116:119],v[56:59],v[36:39]
s_waitcnt lgkmcnt(3)
v_fma_f32 v87,v118,v87,v117
v_mul_f32 v87,v43,v87
v_mfma_f32_16x16x32_bf16 v[28:31],a[120:123],v[60:63],v[28:31]
ds_read_b128 v[80:83],v2 offset:15360
v_mul_f32 v40,v40,v84
v_mfma_f32_16x16x32_bf16 v[32:35],a[124:127],v[60:63],v[32:35]
s_waitcnt vmcnt(6)
v_exp_f32 v87,v87
v_mfma_f32_16x16x32_bf16 v[36:39],a[128:131],v[60:63],v[36:39]
s_barrier
v_mfma_f32_16x16x32_bf16 v[28:31],a[132:135],v[64:67],v[28:31]
ds_read_b128 v[52:55],v2 offset:16384
v_fma_f32 v87,v87,v119,v119
v_mfma_f32_16x16x32_bf16 v[32:35],a[136:139],v[64:67],v[32:35]
v_rcp_f32 v87,v87
v_mfma_f32_16x16x32_bf16 v[36:39],a[140:143],v[64:67],v[36:39]
s_waitcnt lgkmcnt(3)
v_add_f32_dpp v104,v104,v104 quad_perm:[1,0,3,2] row_mask:0xf bank_mask:0xf
v_mul_f32 v41,v41,v85
v_mfma_f32_16x16x32_bf16 v[28:31],a[144:147],v[68:71],v[28:31]
ds_read_b128 v[56:59],v2 offset:17408
v_add_f32_dpp v104,v104,v104 quad_perm:[2,3,0,1] row_mask:0xf bank_mask:0xf
v_mfma_f32_16x16x32_bf16 v[32:35],a[148:151],v[68:71],v[32:35]
s_nop 0
v_add_f32_dpp v104,v104,v104 row_half_mirror row_mask:0xf bank_mask:0xf
v_mul_f32 v42,v42,v86)"
R"(
v_mfma_f32_16x16x32_bf16 v[36:39],a[152:155],v[68:71],v[36:39]
ds_write_b32 v11,v104 offset:0
v_mfma_f32_16x16x32_bf16 v[28:31],a[156:159],v[72:75],v[28:31]
ds_read_b128 v[60:63],v2 offset:18432
v_mul_f32 v43,v43,v87
v_mfma_f32_16x16x32_bf16 v[32:35],a[160:163],v[72:75],v[32:35]
buffer_load_dword v6,s[36:39],s68 offen offset:0 sc1 lds
v_mfma_f32_16x16x32_bf16 v[36:39],a[164:167],v[72:75],v[36:39]
s_waitcnt lgkmcnt(4)
v_cvt_pk_bf16_f32 v96,v40,v41
v_cvt_pk_bf16_f32 v97,v42,v43
v_mfma_f32_16x16x32_bf16 v[28:31],a[168:171],v[76:79],v[28:31]
s_add_u32 m0,s62,16384
ds_read_b128 v[64:67],v2 offset:19456
v_fma_f32 v44,-v116,v20,v44
v_mfma_f32_16x16x32_bf16 v[32:35],a[172:175],v[76:79],v[32:35]
buffer_load_dwordx4 v1,s[36:39],s61 offen offset:0 sc1 lds
v_mfma_f32_16x16x32_bf16 v[36:39],a[176:179],v[76:79],v[36:39]
s_lshr_b32 s72,s61,5
v_fma_f32 v45,-v116,v21,v45
v_fma_f32 v46,-v116,v22,v46
v_mfma_f32_16x16x32_bf16 v[28:31],a[180:183],v[80:83],v[28:31]
s_mov_b32 s62,s63
ds_read_b128 v[68:71],v2 offset:20480
v_fma_f32 v47,-v116,v23,v47
v_mfma_f32_16x16x32_bf16 v[32:35],a[184:187],v[80:83],v[32:35]
v_mul_f32 v88,v44,v44
v_mul_f32 v89,v45,v45
v_mfma_f32_16x16x32_bf16 v[36:39],a[188:191],v[80:83],v[36:39]
s_waitcnt lgkmcnt(4)
buffer_load_dwordx4 v1,s[36:39],s61 offen offset:1024 sc1 lds
v_mfma_f32_16x16x32_bf16 v[28:31],a[192:195],v[52:55],v[28:31]
ds_read_b128 v[72:75],v2 offset:21504
v_mul_f32 v90,v46,v46
v_mfma_f32_16x16x32_bf16 v[32:35],a[196:199],v[52:55],v[32:35]
v_mul_f32 v91,v47,v47
v_fma_f32 v88,v118,v88,v117
v_mfma_f32_16x16x32_bf16 v[36:39],a[200:203],v[52:55],v[36:39]
v_mul_f32 v88,v44,v88
v_fma_f32 v89,v118,v89,v117
v_mfma_f32_16x16x32_bf16 v[28:31],a[204:207],v[56:59],v[28:31]
buffer_load_dwordx4 v1,s[36:39],s61 offen offset:2048 sc1 lds
v_mfma_f32_16x16x32_bf16 v[32:35],a[208:211],v[56:59],v[32:35]
ds_read_b128 v[76:79],v2 offset:22528
v_mul_f32 v89,v45,v89
v_mfma_f32_16x16x32_bf16 v[36:39],a[212:215],v[56:59],v[36:39]
s_waitcnt lgkmcnt(3)
v_exp_f32 v88,v88
v_mfma_f32_16x16x32_bf16 v[28:31],a[216:219],v[60:63],v[28:31]
ds_read_b128 v[80:83],v2 offset:23552
v_fma_f32 v88,v88,v119,v119
v_mfma_f32_16x16x32_bf16 v[32:35],a[220:223],v[60:63],v[32:35]
buffer_load_dwordx4 v1,s[36:39],s61 offen offset:3072 sc1 lds
v_mfma_f32_16x16x32_bf16 v[36:39],a[224:227],v[60:63],v[36:39]
buffer_load_dword v108,v9,s[36:39],s72 offen offset:0 sc1
v_mfma_f32_16x16x32_bf16 v[28:31],a[228:231],v[64:67],v[28:31]
ds_read_b128 v[52:55],v2 offset:24576
v_fma_f32 v90,v118,v90,v117
v_mfma_f32_16x16x32_bf16 v[32:35],a[232:235],v[64:67],v[32:35]
v_exp_f32 v89,v89
v_mfma_f32_16x16x32_bf16 v[36:39],a[236:239],v[64:67],v[36:39]
s_waitcnt lgkmcnt(3)
v_rcp_f32 v88,v88
v_mfma_f32_16x16x32_bf16 v[28:31],a[240:243],v[68:71],v[28:31]
ds_read_b128 v[56:59],v2 offset:25600
v_mul_f32 v90,v46,v90
v_mfma_f32_16x16x32_bf16 v[32:35],a[244:247],v[68:71],v[32:35]
v_exp_f32 v90,v90
v_mfma_f32_16x16x32_bf16 v[36:39],a[248:251],v[68:71],v[36:39]
v_fma_f32 v89,v89,v119,v119
v_fma_f32 v90,v90,v119,v119
v_mfma_f32_16x16x32_bf16 v[28:31],a[252:255],v[72:75],v[28:31]
ds_read_b128 v[60:63],v2 offset:26624
v_fma_f32 v91,v118,v91,v117
v_mfma_f32_16x16x32_bf16 v[32:35],v[128:131],v[72:75],v[32:35]
v_rcp_f32 v89,v89
v_mfma_f32_16x16x32_bf16 v[36:39],v[132:135],v[72:75],v[36:39]
s_waitcnt lgkmcnt(3)
v_rcp_f32 v90,v90
v_mfma_f32_16x16x32_bf16 v[28:31],v[136:139],v[76:79],v[28:31]
ds_read_b128 v[64:67],v2 offset:27648
v_mul_f32 v91,v47,v91
v_mfma_f32_16x16x32_bf16 v[32:35],v[140:143],v[76:79],v[32:35]
v_exp_f32 v91,v91
v_mfma_f32_16x16x32_bf16 v[36:39],v[144:147],v[76:79],v[36:39]
v_fma_f32 v91,v91,v119,v119
v_mul_f32 v44,v44,v88
v_mfma_f32_16x16x32_bf16 v[28:31],v[148:151],v[80:83],v[28:31]
ds_read_b128 v[68:71],v2 offset:28672
v_mul_f32 v45,v45,v89
v_mfma_f32_16x16x32_bf16 v[32:35],v[152:155],v[80:83],v[32:35]
v_rcp_f32 v91,v91
v_mfma_f32_16x16x32_bf16 v[36:39],v[156:159],v[80:83],v[36:39]
s_waitcnt lgkmcnt(3)
v_mul_f32 v46,v46,v90
v_mul_f32 v47,v47,v91
v_cvt_pk_bf16_f32 v98,v44,v45
v_cvt_pk_bf16_f32 v99,v46,v47
v_mfma_f32_16x16x32_bf16 v[28:31],v[160:163],v[52:55],v[28:31]
buffer_store_dwordx4 v[96:99],v3,s[36:39],s73 offen offset:0 sc0 sc1
ds_read_b128 v[72:75],v2 offset:29696
v_fma_f32 v48,-v116,v24,v48
v_fma_f32 v49,-v116,v25,v49
v_mfma_f32_16x16x32_bf16 v[32:35],v[164:167],v[52:55],v[32:35]
v_fma_f32 v50,-v116,v26,v50
v_fma_f32 v51,-v116,v27,v51
v_mfma_f32_16x16x32_bf16 v[36:39],v[168:171],v[52:55],v[36:39]
v_mul_f32 v92,v48,v48
v_mul_f32 v93,v49,v49
v_mfma_f32_16x16x32_bf16 v[28:31],v[172:175],v[56:59],v[28:31]
ds_read_b128 v[76:79],v2 offset:30720
v_mul_f32 v94,v50,v50
v_mfma_f32_16x16x32_bf16 v[32:35],v[176:179],v[56:59],v[32:35]
v_mul_f32 v95,v51,v51
v_fma_f32 v92,v118,v92,v117
v_mfma_f32_16x16x32_bf16 v[36:39],v[180:183],v[56:59],v[36:39]
s_waitcnt lgkmcnt(3)
v_mul_f32 v92,v48,v92
v_fma_f32 v93,v118,v93,v117
v_mfma_f32_16x16x32_bf16 v[28:31],v[184:187],v[60:63],v[28:31]
ds_read_b128 v[80:83],v2 offset:31744
v_mul_f32 v93,v49,v93
v_mfma_f32_16x16x32_bf16 v[32:35],v[188:191],v[60:63],v[32:35]
s_waitcnt vmcnt(6)
v_exp_f32 v92,v92
v_mfma_f32_16x16x32_bf16 v[36:39],v[192:195],v[60:63],v[36:39]
s_barrier
v_mfma_f32_16x16x32_bf16 v[28:31],v[196:199],v[64:67],v[28:31]
ds_read_b128 v[52:55],v2 offset:32768
ds_read_b32 v14,v8 offset:0
v_mfma_f32_16x16x32_bf16 v[32:35],v[200:203],v[64:67],v[32:35]
ds_read_b64 v[112:113],v12 offset:0
v_fma_f32 v92,v92,v119,v119
v_mfma_f32_16x16x32_bf16 v[36:39],v[204:207],v[64:67],v[36:39]
s_waitcnt lgkmcnt(5)
v_exp_f32 v93,v93
v_mfma_f32_16x16x32_bf16 v[28:31],v[208:211],v[68:71],v[28:31]
ds_read_b128 v[56:59],v2 offset:33792
v_fma_f32 v93,v93,v119,v119
v_mfma_f32_16x16x32_bf16 v[32:35],v[212:215],v[68:71],v[32:35]
s_waitcnt lgkmcnt(2)
v_rcp_f32 v92,v92
v_mfma_f32_16x16x32_bf16 v[36:39],v[216:219],v[68:71],v[36:39]
s_waitcnt lgkmcnt(1)
v_cmp_eq_u32 vcc,s60,v14
v_rcp_f32 v93,v93
v_fma_f32 v94,v118,v94,v117
v_mfma_f32_16x16x32_bf16 v[28:31],v[220:223],v[72:75],v[28:31]
s_cmp_eq_u32 vcc_lo,-1
ds_read_b128 v[60:63],v2 offset:34816
v_mul_f32 v94,v50,v94
v_mfma_f32_16x16x32_bf16 v[32:35],v[224:227],v[72:75],v[32:35]
s_cselect_b32 s67,s86,s59
v_fma_f32 v95,v118,v95,v117
v_mul_f32 v95,v51,v95
v_exp_f32 v94,v94
v_exp_f32 v95,v95
v_mfma_f32_16x16x32_bf16 v[36:39],v[228:231],v[72:75],v[36:39]
v_fma_f32 v94,v94,v119,v119
v_fma_f32 v95,v95,v119,v119
v_rcp_f32 v94,v94
v_rcp_f32 v95,v95
v_mfma_f32_16x16x32_bf16 v[28:31],v[232:235],v[76:79],v[28:31]
s_lshr_b32 s61,s67,0
v_mul_f32 v48,v48,v92
v_mul_f32 v49,v49,v93
v_mul_f32 v50,v50,v94
v_mul_f32 v51,v51,v95
v_cvt_pk_bf16_f32 v100,v48,v49
v_cvt_pk_bf16_f32 v101,v50,v51
v_mfma_f32_16x16x32_bf16 v[32:35],v[236:239],v[76:79],v[32:35]
buffer_store_dwordx2 v[100:101],v10,s[36:39],s73 offen offset:0 sc0 sc1
ds_read_b128 v[64:67],v2 offset:35840
v_mfma_f32_16x16x32_bf16 v[36:39],v[240:243],v[76:79],v[36:39]
s_xor_b32 s63,s63,65536
s_cmp_ge_u32 s66,s59
v_mfma_f32_16x16x32_bf16 v[28:31],v[244:247],v[80:83],v[28:31]
ds_read_b128 v[68:71],v2 offset:36864
v_mfma_f32_16x16x32_bf16 v[32:35],v[248:251],v[80:83],v[32:35]
s_waitcnt lgkmcnt(3)
v_mfma_f32_16x16x32_bf16 v[36:39],v[252:255],v[80:83],v[36:39]
s_cbranch_scc1 Lg3_slow1_%=
Lg3_d1w0_%=:
v_mfma_f32_16x16x32_bf16 v[40:43],a[0:3],v[52:55],0
s_mov_b32 m0,s62
ds_read_b128 v[72:75],v2 offset:37888
v_mul_f32 v116,s75,v112
v_mfma_f32_16x16x32_bf16 v[44:47],a[4:7],v[52:55],0
buffer_load_dwordx4 v0,s[36:39],s61 offen offset:0 sc1 lds
v_mfma_f32_16x16x32_bf16 v[48:51],a[8:11],v[52:55],0
s_mul_i32 s73,s48,7
v_fma_f32 v113,v113,s75,v13
v_fma_f32 v113,-v116,v116,v113
v_mfma_f32_16x16x32_bf16 v[40:43],a[12:15],v[56:59],v[40:43]
s_lshr_b32 s73,s73,1
ds_read_b128 v[76:79],v2 offset:38912
v_max_f32 v113,v113,v13
v_mfma_f32_16x16x32_bf16 v[44:47],a[16:19],v[56:59],v[44:47]
s_add_u32 s74,s53,s55
v_rsq_f32 v112,v113)"
R"(
v_mfma_f32_16x16x32_bf16 v[48:51],a[20:23],v[56:59],v[48:51]
s_waitcnt lgkmcnt(3)
buffer_load_dwordx4 v0,s[36:39],s61 offen offset:1024 sc1 lds
v_mfma_f32_16x16x32_bf16 v[40:43],a[24:27],v[60:63],v[40:43]
s_cmp_eq_u32 s74,s56
ds_read_b128 v[80:83],v2 offset:39936
v_mul_f32 v117,s76,v112
v_mfma_f32_16x16x32_bf16 v[44:47],a[28:31],v[60:63],v[44:47]
s_cselect_b32 s74,s57,s74
v_mul_f32 v118,v112,v112
v_mul_f32 v119,v113,v112
v_mfma_f32_16x16x32_bf16 v[48:51],a[32:35],v[60:63],v[48:51]
s_cmp_eq_u32 s74,s58
v_mul_f32 v118,s77,v118
v_mul_f32 v118,v118,v117
v_mfma_f32_16x16x32_bf16 v[40:43],a[36:39],v[64:67],v[40:43]
buffer_load_dwordx4 v0,s[36:39],s61 offen offset:2048 sc1 lds
v_mfma_f32_16x16x32_bf16 v[44:47],a[40:43],v[64:67],v[44:47]
s_cselect_b32 s74,s59,s74
ds_read_b128 v[52:55],v2 offset:40960
v_fma_f32 v28,-v116,v16,v28
v_mfma_f32_16x16x32_bf16 v[48:51],a[44:47],v[64:67],v[48:51]
s_waitcnt lgkmcnt(3)
v_fma_f32 v29,-v116,v17,v29
v_fma_f32 v30,-v116,v18,v30
v_mfma_f32_16x16x32_bf16 v[40:43],a[48:51],v[68:71],v[40:43]
s_cmp_ge_u32 s67,s59
ds_read_b128 v[56:59],v2 offset:41984
v_fma_f32 v31,-v116,v19,v31
v_mfma_f32_16x16x32_bf16 v[44:47],a[52:55],v[68:71],v[44:47]
buffer_load_dwordx4 v0,s[36:39],s61 offen offset:3072 sc1 lds
v_mfma_f32_16x16x32_bf16 v[48:51],a[56:59],v[68:71],v[48:51]
s_cselect_b32 s86,s50,s51
v_mul_f32 v84,v28,v28
v_mul_f32 v85,v29,v29
v_mfma_f32_16x16x32_bf16 v[40:43],a[60:63],v[72:75],v[40:43]
s_lshr_b32 s68,s86,5
ds_read_b128 v[60:63],v2 offset:43008
v_mul_f32 v86,v30,v30
v_mfma_f32_16x16x32_bf16 v[44:47],a[64:67],v[72:75],v[44:47]
s_add_u32 m0,s78,256
v_mul_f32 v87,v31,v31
v_fma_f32 v84,v118,v84,v117
v_mfma_f32_16x16x32_bf16 v[48:51],a[68:71],v[72:75],v[48:51]
s_waitcnt lgkmcnt(3)
v_mul_f32 v84,v28,v84
v_fma_f32 v85,v118,v85,v117
v_mfma_f32_16x16x32_bf16 v[40:43],a[72:75],v[76:79],v[40:43]
s_mov_b64 s[44:45],s[46:47]
ds_read_b128 v[64:67],v2 offset:44032
v_mul_f32 v85,v29,v85
v_mfma_f32_16x16x32_bf16 v[44:47],a[76:79],v[76:79],v[44:47]
s_mov_b64 s[46:47],s[48:49]
v_exp_f32 v84,v84
v_mfma_f32_16x16x32_bf16 v[48:51],a[80:83],v[76:79],v[48:51]
s_mov_b64 s[48:49],s[50:51]
v_exp_f32 v85,v85
v_mfma_f32_16x16x32_bf16 v[40:43],a[84:87],v[80:83],v[40:43]
s_mov_b64 s[50:51],s[52:53]
ds_read_b128 v[68:71],v2 offset:45056
v_fma_f32 v84,v84,v119,v119
v_mfma_f32_16x16x32_bf16 v[44:47],a[88:91],v[80:83],v[44:47]
s_mov_b32 s52,s74
v_rcp_f32 v84,v84
v_mfma_f32_16x16x32_bf16 v[48:51],a[92:95],v[80:83],v[48:51]
s_waitcnt lgkmcnt(3)
v_fma_f32 v85,v85,v119,v119
v_fma_f32 v86,v118,v86,v117
v_mfma_f32_16x16x32_bf16 v[40:43],a[96:99],v[52:55],v[40:43]
ds_read_b128 v[72:75],v2 offset:46080
v_mul_f32 v86,v30,v86
v_mfma_f32_16x16x32_bf16 v[44:47],a[100:103],v[52:55],v[44:47]
v_exp_f32 v86,v86
v_mfma_f32_16x16x32_bf16 v[48:51],a[104:107],v[52:55],v[48:51]
v_rcp_f32 v85,v85
v_mfma_f32_16x16x32_bf16 v[40:43],a[108:111],v[56:59],v[40:43]
ds_read_b128 v[76:79],v2 offset:47104
v_fma_f32 v86,v86,v119,v119
v_mfma_f32_16x16x32_bf16 v[44:47],a[112:115],v[56:59],v[44:47]
v_rcp_f32 v86,v86
v_mfma_f32_16x16x32_bf16 v[48:51],a[116:119],v[56:59],v[48:51]
s_waitcnt lgkmcnt(3)
v_fma_f32 v87,v118,v87,v117
v_mul_f32 v87,v31,v87
v_mfma_f32_16x16x32_bf16 v[40:43],a[120:123],v[60:63],v[40:43]
ds_read_b128 v[80:83],v2 offset:48128
v_mul_f32 v28,v28,v84
v_mfma_f32_16x16x32_bf16 v[44:47],a[124:127],v[60:63],v[44:47]
s_waitcnt vmcnt(6)
v_exp_f32 v87,v87
v_mfma_f32_16x16x32_bf16 v[48:51],a[128:131],v[60:63],v[48:51]
s_barrier
v_mfma_f32_16x16x32_bf16 v[40:43],a[132:135],v[64:67],v[40:43]
ds_read_b128 v[52:55],v2 offset:49152
v_fma_f32 v87,v87,v119,v119
v_mfma_f32_16x16x32_bf16 v[44:47],a[136:139],v[64:67],v[44:47]
v_rcp_f32 v87,v87
v_mfma_f32_16x16x32_bf16 v[48:51],a[140:143],v[64:67],v[48:51]
s_waitcnt lgkmcnt(3)
v_add_f32_dpp v108,v108,v108 quad_perm:[1,0,3,2] row_mask:0xf bank_mask:0xf
v_mul_f32 v29,v29,v85
v_mfma_f32_16x16x32_bf16 v[40:43],a[144:147],v[68:71],v[40:43]
ds_read_b128 v[56:59],v2 offset:50176
v_add_f32_dpp v108,v108,v108 quad_perm:[2,3,0,1] row_mask:0xf bank_mask:0xf
v_mfma_f32_16x16x32_bf16 v[44:47],a[148:151],v[68:71],v[44:47]
s_nop 0
v_add_f32_dpp v108,v108,v108 row_half_mirror row_mask:0xf bank_mask:0xf
v_mul_f32 v30,v30,v86
v_mfma_f32_16x16x32_bf16 v[48:51],a[152:155],v[68:71],v[48:51]
ds_write_b32 v11,v108 offset:128
v_mfma_f32_16x16x32_bf16 v[40:43],a[156:159],v[72:75],v[40:43]
ds_read_b128 v[60:63],v2 offset:51200
v_mul_f32 v31,v31,v87
v_mfma_f32_16x16x32_bf16 v[44:47],a[160:163],v[72:75],v[44:47]
buffer_load_dword v6,s[36:39],s68 offen offset:0 sc1 lds
v_mfma_f32_16x16x32_bf16 v[48:51],a[164:167],v[72:75],v[48:51]
s_waitcnt lgkmcnt(4)
v_cvt_pk_bf16_f32 v96,v28,v29
v_cvt_pk_bf16_f32 v97,v30,v31
v_mfma_f32_16x16x32_bf16 v[40:43],a[168:171],v[76:79],v[40:43]
s_add_u32 m0,s62,16384
ds_read_b128 v[64:67],v2 offset:52224
v_fma_f32 v32,-v116,v20,v32
v_mfma_f32_16x16x32_bf16 v[44:47],a[172:175],v[76:79],v[44:47]
buffer_load_dwordx4 v1,s[36:39],s61 offen offset:0 sc1 lds
v_mfma_f32_16x16x32_bf16 v[48:51],a[176:179],v[76:79],v[48:51]
s_lshr_b32 s72,s61,5
v_fma_f32 v33,-v116,v21,v33
v_fma_f32 v34,-v116,v22,v34
v_mfma_f32_16x16x32_bf16 v[40:43],a[180:183],v[80:83],v[40:43]
s_mov_b32 s62,s64
ds_read_b128 v[68:71],v2 offset:53248
v_fma_f32 v35,-v116,v23,v35
v_mfma_f32_16x16x32_bf16 v[44:47],a[184:187],v[80:83],v[44:47]
v_mul_f32 v88,v32,v32
v_mul_f32 v89,v33,v33
v_mfma_f32_16x16x32_bf16 v[48:51],a[188:191],v[80:83],v[48:51]
s_waitcnt lgkmcnt(4)
buffer_load_dwordx4 v1,s[36:39],s61 offen offset:1024 sc1 lds
v_mfma_f32_16x16x32_bf16 v[40:43],a[192:195],v[52:55],v[40:43]
ds_read_b128 v[72:75],v2 offset:54272
v_mul_f32 v90,v34,v34
v_mfma_f32_16x16x32_bf16 v[44:47],a[196:199],v[52:55],v[44:47]
v_mul_f32 v91,v35,v35
v_fma_f32 v88,v118,v88,v117
v_mfma_f32_16x16x32_bf16 v[48:51],a[200:203],v[52:55],v[48:51]
v_mul_f32 v88,v32,v88
v_fma_f32 v89,v118,v89,v117
v_mfma_f32_16x16x32_bf16 v[40:43],a[204:207],v[56:59],v[40:43]
buffer_load_dwordx4 v1,s[36:39],s61 offen offset:2048 sc1 lds
v_mfma_f32_16x16x32_bf16 v[44:47],a[208:211],v[56:59],v[44:47]
ds_read_b128 v[76:79],v2 offset:55296
v_mul_f32 v89,v33,v89
v_mfma_f32_16x16x32_bf16 v[48:51],a[212:215],v[56:59],v[48:51]
s_waitcnt lgkmcnt(3)
v_exp_f32 v88,v88
v_mfma_f32_16x16x32_bf16 v[40:43],a[216:219],v[60:63],v[40:43]
ds_read_b128 v[80:83],v2 offset:56320
v_fma_f32 v88,v88,v119,v119
v_mfma_f32_16x16x32_bf16 v[44:47],a[220:223],v[60:63],v[44:47]
buffer_load_dwordx4 v1,s[36:39],s61 offen offset:3072 sc1 lds
v_mfma_f32_16x16x32_bf16 v[48:51],a[224:227],v[60:63],v[48:51]
buffer_load_dword v104,v9,s[36:39],s72 offen offset:0 sc1
v_mfma_f32_16x16x32_bf16 v[40:43],a[228:231],v[64:67],v[40:43]
ds_read_b128 v[52:55],v2 offset:57344
v_fma_f32 v90,v118,v90,v117
v_mfma_f32_16x16x32_bf16 v[44:47],a[232:235],v[64:67],v[44:47]
v_exp_f32 v89,v89
v_mfma_f32_16x16x32_bf16 v[48:51],a[236:239],v[64:67],v[48:51]
s_waitcnt lgkmcnt(3)
v_rcp_f32 v88,v88
v_mfma_f32_16x16x32_bf16 v[40:43],a[240:243],v[68:71],v[40:43]
ds_read_b128 v[56:59],v2 offset:58368
v_mul_f32 v90,v34,v90
v_mfma_f32_16x16x32_bf16 v[44:47],a[244:247],v[68:71],v[44:47]
v_exp_f32 v90,v90
v_mfma_f32_16x16x32_bf16 v[48:51],a[248:251],v[68:71],v[48:51]
v_fma_f32 v89,v89,v119,v119
v_fma_f32 v90,v90,v119,v119
v_mfma_f32_16x16x32_bf16 v[40:43],a[252:255],v[72:75],v[40:43]
ds_read_b128 v[60:63],v2 offset:59392
v_fma_f32 v91,v118,v91,v117
v_mfma_f32_16x16x32_bf16 v[44:47],v[128:131],v[72:75],v[44:47]
v_rcp_f32 v89,v89
v_mfma_f32_16x16x32_bf16 v[48:51],v[132:135],v[72:75],v[48:51]
s_waitcnt lgkmcnt(3)
v_rcp_f32 v90,v90
v_mfma_f32_16x16x32_bf16 v[40:43],v[136:139],v[76:79],v[40:43]
ds_read_b128 v[64:67],v2 offset:60416
v_mul_f32 v91,v35,v91
v_mfma_f32_16x16x32_bf16 v[44:47],v[140:143],v[76:79],v[44:47]
v_exp_f32 v91,v91
v_mfma_f32_16x16x32_bf16 v[48:51],v[144:147],v[76:79],v[48:51]
v_fma_f32 v91,v91,v119,v119
v_mul_f32 v32,v32,v88
v_mfma_f32_16x16x32_bf16 v[40:43],v[148:151],v[80:83],v[40:43]
ds_read_b128 v[68:71],v2 offset:61440
v_mul_f32 v33,v33,v89
v_mfma_f32_16x16x32_bf16 v[44:47],v[152:155],v[80:83],v[44:47]
v_rcp_f32 v91,v91)"
R"(
v_mfma_f32_16x16x32_bf16 v[48:51],v[156:159],v[80:83],v[48:51]
s_waitcnt lgkmcnt(3)
v_mul_f32 v34,v34,v90
v_mul_f32 v35,v35,v91
v_cvt_pk_bf16_f32 v98,v32,v33
v_cvt_pk_bf16_f32 v99,v34,v35
v_mfma_f32_16x16x32_bf16 v[40:43],v[160:163],v[52:55],v[40:43]
buffer_store_dwordx4 v[96:99],v3,s[36:39],s73 offen offset:0 sc0 sc1
ds_read_b128 v[72:75],v2 offset:62464
v_fma_f32 v36,-v116,v24,v36
v_fma_f32 v37,-v116,v25,v37
v_mfma_f32_16x16x32_bf16 v[44:47],v[164:167],v[52:55],v[44:47]
v_fma_f32 v38,-v116,v26,v38
v_fma_f32 v39,-v116,v27,v39
v_mfma_f32_16x16x32_bf16 v[48:51],v[168:171],v[52:55],v[48:51]
v_mul_f32 v92,v36,v36
v_mul_f32 v93,v37,v37
v_mfma_f32_16x16x32_bf16 v[40:43],v[172:175],v[56:59],v[40:43]
ds_read_b128 v[76:79],v2 offset:63488
v_mul_f32 v94,v38,v38
v_mfma_f32_16x16x32_bf16 v[44:47],v[176:179],v[56:59],v[44:47]
v_mul_f32 v95,v39,v39
v_fma_f32 v92,v118,v92,v117
v_mfma_f32_16x16x32_bf16 v[48:51],v[180:183],v[56:59],v[48:51]
s_waitcnt lgkmcnt(3)
v_mul_f32 v92,v36,v92
v_fma_f32 v93,v118,v93,v117
v_mfma_f32_16x16x32_bf16 v[40:43],v[184:187],v[60:63],v[40:43]
ds_read_b128 v[80:83],v2 offset:64512
v_mul_f32 v93,v37,v93
v_mfma_f32_16x16x32_bf16 v[44:47],v[188:191],v[60:63],v[44:47]
s_waitcnt vmcnt(6)
v_xor_b32 v2,65536,v2
v_fma_f32 v94,v118,v94,v117
v_mfma_f32_16x16x32_bf16 v[48:51],v[192:195],v[60:63],v[48:51]
s_barrier
v_mfma_f32_16x16x32_bf16 v[40:43],v[196:199],v[64:67],v[40:43]
ds_read_b128 v[52:55],v2 offset:0
ds_read_b32 v14,v8 offset:256
v_mfma_f32_16x16x32_bf16 v[44:47],v[200:203],v[64:67],v[44:47]
ds_read_b64 v[112:113],v12 offset:128
v_fma_f32 v95,v118,v95,v117
v_mfma_f32_16x16x32_bf16 v[48:51],v[204:207],v[64:67],v[48:51]
s_waitcnt lgkmcnt(5)
v_exp_f32 v92,v92
v_mfma_f32_16x16x32_bf16 v[40:43],v[208:211],v[68:71],v[40:43]
ds_read_b128 v[56:59],v2 offset:1024
v_mul_f32 v94,v38,v94
v_mfma_f32_16x16x32_bf16 v[44:47],v[212:215],v[68:71],v[44:47]
s_waitcnt lgkmcnt(2)
v_exp_f32 v93,v93
v_mfma_f32_16x16x32_bf16 v[48:51],v[216:219],v[68:71],v[48:51]
s_waitcnt lgkmcnt(1)
v_cmp_eq_u32 vcc,s60,v14
v_exp_f32 v94,v94
v_mul_f32 v95,v39,v95
v_mfma_f32_16x16x32_bf16 v[40:43],v[220:223],v[72:75],v[40:43]
s_cmp_eq_u32 vcc_lo,-1
ds_read_b128 v[60:63],v2 offset:2048
v_fma_f32 v92,v92,v119,v119
v_mfma_f32_16x16x32_bf16 v[44:47],v[224:227],v[72:75],v[44:47]
s_cselect_b32 s66,s86,s59
v_exp_f32 v95,v95
v_mfma_f32_16x16x32_bf16 v[48:51],v[228:231],v[72:75],v[48:51]
v_fma_f32 v93,v93,v119,v119
v_fma_f32 v94,v94,v119,v119
v_fma_f32 v95,v95,v119,v119
v_rcp_f32 v92,v92
v_rcp_f32 v93,v93
v_rcp_f32 v94,v94
v_rcp_f32 v95,v95
v_mfma_f32_16x16x32_bf16 v[40:43],v[232:235],v[76:79],v[40:43]
s_lshr_b32 s61,s66,0
v_mul_f32 v36,v36,v92
v_mul_f32 v37,v37,v93
v_mul_f32 v38,v38,v94
v_mul_f32 v39,v39,v95
v_cvt_pk_bf16_f32 v100,v36,v37
v_cvt_pk_bf16_f32 v101,v38,v39
v_mfma_f32_16x16x32_bf16 v[44:47],v[236:239],v[76:79],v[44:47]
buffer_store_dwordx2 v[100:101],v10,s[36:39],s73 offen offset:0 sc0 sc1
ds_read_b128 v[64:67],v2 offset:3072
v_mfma_f32_16x16x32_bf16 v[48:51],v[240:243],v[76:79],v[48:51]
s_xor_b32 s64,s64,65536
s_cmp_lt_u32 s67,s59
v_mfma_f32_16x16x32_bf16 v[40:43],v[244:247],v[80:83],v[40:43]
ds_read_b128 v[68:71],v2 offset:4096
v_mfma_f32_16x16x32_bf16 v[44:47],v[248:251],v[80:83],v[44:47]
s_waitcnt lgkmcnt(3)
v_mfma_f32_16x16x32_bf16 v[48:51],v[252:255],v[80:83],v[48:51]
s_cbranch_scc1 Lg3_d0w0_%=
s_branch Lg3_slow0_%=
Lg3_d0w1_%=:
v_mfma_f32_16x16x32_bf16 v[28:31],a[0:3],v[52:55],0
s_mov_b32 m0,s62
ds_read_b128 v[72:75],v2 offset:5120
v_mul_f32 v116,s75,v112
v_mfma_f32_16x16x32_bf16 v[32:35],a[4:7],v[52:55],0
s_mul_i32 s73,s47,7
v_fma_f32 v113,v113,s75,v13
v_fma_f32 v113,-v116,v116,v113
v_mfma_f32_16x16x32_bf16 v[36:39],a[8:11],v[52:55],0
buffer_load_dwordx4 v0,s[36:39],s61 offen offset:0 sc1 lds
v_mfma_f32_16x16x32_bf16 v[28:31],a[12:15],v[56:59],v[28:31]
s_lshr_b32 s73,s73,1
ds_read_b128 v[76:79],v2 offset:6144
v_max_f32 v113,v113,v13
v_mfma_f32_16x16x32_bf16 v[32:35],a[16:19],v[56:59],v[32:35]
s_add_u32 s53,s52,s55
v_rsq_f32 v112,v113
v_mfma_f32_16x16x32_bf16 v[36:39],a[20:23],v[56:59],v[36:39]
s_waitcnt lgkmcnt(3)
v_mul_f32 v117,s76,v112
v_mul_f32 v118,v112,v112
v_mfma_f32_16x16x32_bf16 v[28:31],a[24:27],v[60:63],v[28:31]
buffer_load_dwordx4 v0,s[36:39],s61 offen offset:1024 sc1 lds
v_mfma_f32_16x16x32_bf16 v[32:35],a[28:31],v[60:63],v[32:35]
s_cmp_eq_u32 s53,s56
ds_read_b128 v[80:83],v2 offset:7168
v_mul_f32 v119,v113,v112
v_mfma_f32_16x16x32_bf16 v[36:39],a[32:35],v[60:63],v[36:39]
s_cselect_b32 s53,s57,s53
v_mul_f32 v118,s77,v118
v_mul_f32 v118,v118,v117
v_mfma_f32_16x16x32_bf16 v[28:31],a[36:39],v[64:67],v[28:31]
s_cmp_eq_u32 s53,s58
ds_read_b128 v[52:55],v2 offset:8192
v_fma_f32 v40,-v116,v16,v40
v_mfma_f32_16x16x32_bf16 v[32:35],a[40:43],v[64:67],v[32:35]
buffer_load_dwordx4 v0,s[36:39],s61 offen offset:2048 sc1 lds
v_mfma_f32_16x16x32_bf16 v[36:39],a[44:47],v[64:67],v[36:39]
s_waitcnt lgkmcnt(3)
v_fma_f32 v41,-v116,v17,v41
v_fma_f32 v42,-v116,v18,v42
v_mfma_f32_16x16x32_bf16 v[28:31],a[48:51],v[68:71],v[28:31]
s_cselect_b32 s53,s59,s53
ds_read_b128 v[56:59],v2 offset:9216
v_fma_f32 v43,-v116,v19,v43
v_mfma_f32_16x16x32_bf16 v[32:35],a[52:55],v[68:71],v[32:35]
s_cmp_ge_u32 s66,s59
v_mul_f32 v84,v40,v40
v_mul_f32 v85,v41,v41
v_mfma_f32_16x16x32_bf16 v[36:39],a[56:59],v[68:71],v[36:39]
buffer_load_dwordx4 v0,s[36:39],s61 offen offset:3072 sc1 lds
v_mfma_f32_16x16x32_bf16 v[28:31],a[60:63],v[72:75],v[28:31]
s_cselect_b32 s86,s49,s50
ds_read_b128 v[60:63],v2 offset:10240
v_mul_f32 v86,v42,v42
v_mfma_f32_16x16x32_bf16 v[32:35],a[64:67],v[72:75],v[32:35]
s_add_u32 m0,s62,16384
v_mul_f32 v87,v43,v43
v_fma_f32 v84,v118,v84,v117
v_mfma_f32_16x16x32_bf16 v[36:39],a[68:71],v[72:75],v[36:39]
s_waitcnt lgkmcnt(3)
v_mul_f32 v84,v40,v84
v_fma_f32 v85,v118,v85,v117
v_mfma_f32_16x16x32_bf16 v[28:31],a[72:75],v[76:79],v[28:31]
s_lshr_b32 s72,s61,5
ds_read_b128 v[64:67],v2 offset:11264
v_mul_f32 v85,v41,v85
v_mfma_f32_16x16x32_bf16 v[32:35],a[76:79],v[76:79],v[32:35]
s_mov_b32 s62,s63
v_exp_f32 v84,v84
v_mfma_f32_16x16x32_bf16 v[36:39],a[80:83],v[76:79],v[36:39]
v_exp_f32 v85,v85
v_mfma_f32_16x16x32_bf16 v[28:31],a[84:87],v[80:83],v[28:31]
ds_read_b128 v[68:71],v2 offset:12288
v_fma_f32 v84,v84,v119,v119
v_mfma_f32_16x16x32_bf16 v[32:35],a[88:91],v[80:83],v[32:35]
v_rcp_f32 v84,v84
v_mfma_f32_16x16x32_bf16 v[36:39],a[92:95],v[80:83],v[36:39]
s_waitcnt lgkmcnt(3)
v_fma_f32 v85,v85,v119,v119
v_fma_f32 v86,v118,v86,v117
v_mfma_f32_16x16x32_bf16 v[28:31],a[96:99],v[52:55],v[28:31]
ds_read_b128 v[72:75],v2 offset:13312
v_mul_f32 v86,v42,v86
v_mfma_f32_16x16x32_bf16 v[32:35],a[100:103],v[52:55],v[32:35]
v_exp_f32 v86,v86
v_mfma_f32_16x16x32_bf16 v[36:39],a[104:107],v[52:55],v[36:39]
v_rcp_f32 v85,v85
v_mfma_f32_16x16x32_bf16 v[28:31],a[108:111],v[56:59],v[28:31]
ds_read_b128 v[76:79],v2 offset:14336
v_fma_f32 v86,v86,v119,v119
v_mfma_f32_16x16x32_bf16 v[32:35],a[112:115],v[56:59],v[32:35]
v_rcp_f32 v86,v86
v_mfma_f32_16x16x32_bf16 v[36:39],a[116:119],v[56:59],v[36:39]
s_waitcnt lgkmcnt(3)
v_fma_f32 v87,v118,v87,v117
v_mul_f32 v87,v43,v87
v_mfma_f32_16x16x32_bf16 v[28:31],a[120:123],v[60:63],v[28:31]
ds_read_b128 v[80:83],v2 offset:15360
v_mul_f32 v40,v40,v84
v_mfma_f32_16x16x32_bf16 v[32:35],a[124:127],v[60:63],v[32:35]
s_waitcnt vmcnt(6)
v_exp_f32 v87,v87
v_mfma_f32_16x16x32_bf16 v[36:39],a[128:131],v[60:63],v[36:39]
s_barrier
v_mfma_f32_16x16x32_bf16 v[28:31],a[132:135],v[64:67],v[28:31]
ds_read_b128 v[52:55],v2 offset:16384)"
R"(
v_fma_f32 v87,v87,v119,v119
v_mfma_f32_16x16x32_bf16 v[32:35],a[136:139],v[64:67],v[32:35]
v_rcp_f32 v87,v87
v_mfma_f32_16x16x32_bf16 v[36:39],a[140:143],v[64:67],v[36:39]
s_waitcnt lgkmcnt(3)
v_add_f32_dpp v104,v104,v104 quad_perm:[1,0,3,2] row_mask:0xf bank_mask:0xf
v_mul_f32 v41,v41,v85
v_mfma_f32_16x16x32_bf16 v[28:31],a[144:147],v[68:71],v[28:31]
ds_read_b128 v[56:59],v2 offset:17408
v_add_f32_dpp v104,v104,v104 quad_perm:[2,3,0,1] row_mask:0xf bank_mask:0xf
v_mfma_f32_16x16x32_bf16 v[32:35],a[148:151],v[68:71],v[32:35]
s_nop 0
v_add_f32_dpp v104,v104,v104 row_half_mirror row_mask:0xf bank_mask:0xf
v_mul_f32 v42,v42,v86
v_mfma_f32_16x16x32_bf16 v[36:39],a[152:155],v[68:71],v[36:39]
ds_write_b32 v11,v104 offset:0
v_mfma_f32_16x16x32_bf16 v[28:31],a[156:159],v[72:75],v[28:31]
ds_read_b128 v[60:63],v2 offset:18432
v_mul_f32 v43,v43,v87
v_mfma_f32_16x16x32_bf16 v[32:35],a[160:163],v[72:75],v[32:35]
v_cvt_pk_bf16_f32 v96,v40,v41
v_cvt_pk_bf16_f32 v97,v42,v43
v_mfma_f32_16x16x32_bf16 v[36:39],a[164:167],v[72:75],v[36:39]
s_waitcnt lgkmcnt(4)
v_fma_f32 v44,-v116,v20,v44
v_fma_f32 v45,-v116,v21,v45
v_mfma_f32_16x16x32_bf16 v[28:31],a[168:171],v[76:79],v[28:31]
ds_read_b128 v[64:67],v2 offset:19456
v_fma_f32 v46,-v116,v22,v46
v_mfma_f32_16x16x32_bf16 v[32:35],a[172:175],v[76:79],v[32:35]
v_fma_f32 v47,-v116,v23,v47
v_mul_f32 v88,v44,v44
v_mfma_f32_16x16x32_bf16 v[36:39],a[176:179],v[76:79],v[36:39]
buffer_load_dwordx4 v1,s[36:39],s61 offen offset:0 sc1 lds
v_mfma_f32_16x16x32_bf16 v[28:31],a[180:183],v[80:83],v[28:31]
ds_read_b128 v[68:71],v2 offset:20480
v_mul_f32 v89,v45,v45
v_mfma_f32_16x16x32_bf16 v[32:35],a[184:187],v[80:83],v[32:35]
v_mul_f32 v90,v46,v46
v_mul_f32 v91,v47,v47
v_mfma_f32_16x16x32_bf16 v[36:39],a[188:191],v[80:83],v[36:39]
s_waitcnt lgkmcnt(4)
v_fma_f32 v88,v118,v88,v117
v_mul_f32 v88,v44,v88
v_mfma_f32_16x16x32_bf16 v[28:31],a[192:195],v[52:55],v[28:31]
buffer_load_dwordx4 v1,s[36:39],s61 offen offset:1024 sc1 lds
v_mfma_f32_16x16x32_bf16 v[32:35],a[196:199],v[52:55],v[32:35]
ds_read_b128 v[72:75],v2 offset:21504
v_fma_f32 v89,v118,v89,v117
v_mfma_f32_16x16x32_bf16 v[36:39],a[200:203],v[52:55],v[36:39]
v_exp_f32 v88,v88
v_mfma_f32_16x16x32_bf16 v[28:31],a[204:207],v[56:59],v[28:31]
ds_read_b128 v[76:79],v2 offset:22528
v_mul_f32 v89,v45,v89
v_mfma_f32_16x16x32_bf16 v[32:35],a[208:211],v[56:59],v[32:35]
buffer_load_dwordx4 v1,s[36:39],s61 offen offset:2048 sc1 lds
v_mfma_f32_16x16x32_bf16 v[36:39],a[212:215],v[56:59],v[36:39]
s_waitcnt lgkmcnt(3)
v_exp_f32 v89,v89
v_mfma_f32_16x16x32_bf16 v[28:31],a[216:219],v[60:63],v[28:31]
ds_read_b128 v[80:83],v2 offset:23552
v_fma_f32 v88,v88,v119,v119
v_mfma_f32_16x16x32_bf16 v[32:35],a[220:223],v[60:63],v[32:35]
v_rcp_f32 v88,v88
v_mfma_f32_16x16x32_bf16 v[36:39],a[224:227],v[60:63],v[36:39]
buffer_load_dwordx4 v1,s[36:39],s61 offen offset:3072 sc1 lds
v_mfma_f32_16x16x32_bf16 v[28:31],a[228:231],v[64:67],v[28:31]
buffer_load_dword v108,v9,s[36:39],s72 offen offset:0 sc1
v_mfma_f32_16x16x32_bf16 v[32:35],a[232:235],v[64:67],v[32:35]
ds_read_b128 v[52:55],v2 offset:24576
v_fma_f32 v89,v89,v119,v119
v_mfma_f32_16x16x32_bf16 v[36:39],a[236:239],v[64:67],v[36:39]
s_waitcnt lgkmcnt(3)
v_rcp_f32 v89,v89
v_mfma_f32_16x16x32_bf16 v[28:31],a[240:243],v[68:71],v[28:31]
ds_read_b128 v[56:59],v2 offset:25600
v_fma_f32 v90,v118,v90,v117
v_mfma_f32_16x16x32_bf16 v[32:35],a[244:247],v[68:71],v[32:35]
v_mul_f32 v90,v46,v90
v_fma_f32 v91,v118,v91,v117
v_mfma_f32_16x16x32_bf16 v[36:39],a[248:251],v[68:71],v[36:39]
v_exp_f32 v90,v90
v_mfma_f32_16x16x32_bf16 v[28:31],a[252:255],v[72:75],v[28:31]
ds_read_b128 v[60:63],v2 offset:26624
v_mul_f32 v91,v47,v91
v_mfma_f32_16x16x32_bf16 v[32:35],v[128:131],v[72:75],v[32:35]
v_exp_f32 v91,v91
v_mfma_f32_16x16x32_bf16 v[36:39],v[132:135],v[72:75],v[36:39]
s_waitcnt lgkmcnt(3)
v_fma_f32 v90,v90,v119,v119
v_fma_f32 v91,v91,v119,v119
v_mfma_f32_16x16x32_bf16 v[28:31],v[136:139],v[76:79],v[28:31]
ds_read_b128 v[64:67],v2 offset:27648
v_mul_f32 v44,v44,v88
v_mfma_f32_16x16x32_bf16 v[32:35],v[140:143],v[76:79],v[32:35]
v_rcp_f32 v90,v90
v_mfma_f32_16x16x32_bf16 v[36:39],v[144:147],v[76:79],v[36:39]
v_rcp_f32 v91,v91
v_mfma_f32_16x16x32_bf16 v[28:31],v[148:151],v[80:83],v[28:31]
ds_read_b128 v[68:71],v2 offset:28672
v_mul_f32 v45,v45,v89
v_mfma_f32_16x16x32_bf16 v[32:35],v[152:155],v[80:83],v[32:35]
v_mul_f32 v46,v46,v90
v_mul_f32 v47,v47,v91
v_mfma_f32_16x16x32_bf16 v[36:39],v[156:159],v[80:83],v[36:39]
s_waitcnt lgkmcnt(3)
v_cvt_pk_bf16_f32 v98,v44,v45
v_cvt_pk_bf16_f32 v99,v46,v47
v_mfma_f32_16x16x32_bf16 v[28:31],v[160:163],v[52:55],v[28:31]
ds_read_b128 v[72:75],v2 offset:29696
v_fma_f32 v48,-v116,v24,v48
v_mfma_f32_16x16x32_bf16 v[32:35],v[164:167],v[52:55],v[32:35]
buffer_store_dwordx4 v[96:99],v3,s[36:39],s73 offen offset:0 sc0 sc1
v_fma_f32 v49,-v116,v25,v49
v_fma_f32 v50,-v116,v26,v50
v_fma_f32 v51,-v116,v27,v51
v_mfma_f32_16x16x32_bf16 v[36:39],v[168:171],v[52:55],v[36:39]
v_mul_f32 v92,v48,v48
v_mul_f32 v93,v49,v49
v_mfma_f32_16x16x32_bf16 v[28:31],v[172:175],v[56:59],v[28:31]
ds_read_b128 v[76:79],v2 offset:30720
v_mul_f32 v94,v50,v50
v_mfma_f32_16x16x32_bf16 v[32:35],v[176:179],v[56:59],v[32:35]
v_mul_f32 v95,v51,v51
v_fma_f32 v92,v118,v92,v117
v_mfma_f32_16x16x32_bf16 v[36:39],v[180:183],v[56:59],v[36:39]
s_waitcnt lgkmcnt(3)
v_mul_f32 v92,v48,v92
v_fma_f32 v93,v118,v93,v117
v_mfma_f32_16x16x32_bf16 v[28:31],v[184:187],v[60:63],v[28:31]
ds_read_b128 v[80:83],v2 offset:31744
v_mul_f32 v93,v49,v93
v_mfma_f32_16x16x32_bf16 v[32:35],v[188:191],v[60:63],v[32:35]
s_waitcnt vmcnt(6)
v_exp_f32 v92,v92
v_mfma_f32_16x16x32_bf16 v[36:39],v[192:195],v[60:63],v[36:39]
s_barrier
v_mfma_f32_16x16x32_bf16 v[28:31],v[196:199],v[64:67],v[28:31]
ds_read_b128 v[52:55],v2 offset:32768
ds_read_b32 v14,v8 offset:0
v_mfma_f32_16x16x32_bf16 v[32:35],v[200:203],v[64:67],v[32:35]
ds_read_b64 v[112:113],v12 offset:0
v_fma_f32 v92,v92,v119,v119
v_mfma_f32_16x16x32_bf16 v[36:39],v[204:207],v[64:67],v[36:39]
s_waitcnt lgkmcnt(5)
v_exp_f32 v93,v93
v_mfma_f32_16x16x32_bf16 v[28:31],v[208:211],v[68:71],v[28:31]
ds_read_b128 v[56:59],v2 offset:33792
v_fma_f32 v93,v93,v119,v119
v_mfma_f32_16x16x32_bf16 v[32:35],v[212:215],v[68:71],v[32:35]
s_waitcnt lgkmcnt(2)
v_rcp_f32 v92,v92
v_mfma_f32_16x16x32_bf16 v[36:39],v[216:219],v[68:71],v[36:39]
s_waitcnt lgkmcnt(1)
v_cmp_eq_u32 vcc,s60,v14
v_rcp_f32 v93,v93
v_fma_f32 v94,v118,v94,v117
v_mfma_f32_16x16x32_bf16 v[28:31],v[220:223],v[72:75],v[28:31]
s_cmp_eq_u32 vcc_lo,-1
ds_read_b128 v[60:63],v2 offset:34816
v_mul_f32 v94,v50,v94
v_mfma_f32_16x16x32_bf16 v[32:35],v[224:227],v[72:75],v[32:35]
s_cselect_b32 s67,s86,s59
v_exp_f32 v94,v94
v_mfma_f32_16x16x32_bf16 v[36:39],v[228:231],v[72:75],v[36:39]
v_fma_f32 v95,v118,v95,v117
v_mul_f32 v95,v51,v95
v_exp_f32 v95,v95
v_mfma_f32_16x16x32_bf16 v[28:31],v[232:235],v[76:79],v[28:31]
s_lshr_b32 s61,s67,0
v_fma_f32 v94,v94,v119,v119
v_fma_f32 v95,v95,v119,v119
v_rcp_f32 v94,v94
v_rcp_f32 v95,v95
v_mfma_f32_16x16x32_bf16 v[32:35],v[236:239],v[76:79],v[32:35]
s_xor_b32 s63,s63,65536
v_mul_f32 v48,v48,v92
v_mul_f32 v49,v49,v93
v_mul_f32 v50,v50,v94
v_mul_f32 v51,v51,v95
v_cvt_pk_bf16_f32 v100,v48,v49
v_cvt_pk_bf16_f32 v101,v50,v51
v_mfma_f32_16x16x32_bf16 v[36:39],v[240:243],v[76:79],v[36:39]
s_cmp_ge_u32 s66,s59
ds_read_b128 v[64:67],v2 offset:35840
buffer_store_dwordx2 v[100:101],v10,s[36:39],s73 offen offset:0 sc0 sc1
v_mfma_f32_16x16x32_bf16 v[28:31],v[244:247],v[80:83],v[28:31]
ds_read_b128 v[68:71],v2 offset:36864
v_mfma_f32_16x16x32_bf16 v[32:35],v[248:251],v[80:83],v[32:35]
s_waitcnt lgkmcnt(3)
v_mfma_f32_16x16x32_bf16 v[36:39],v[252:255],v[80:83],v[36:39]
s_cbranch_scc1 Lg3_slow1_%=
Lg3_d1w1_%=:
v_mfma_f32_16x16x32_bf16 v[40:43],a[0:3],v[52:55],0
s_mov_b32 m0,s62
ds_read_b128 v[72:75],v2 offset:37888
v_mul_f32 v116,s75,v112
v_mfma_f32_16x16x32_bf16 v[44:47],a[4:7],v[52:55],0
s_mul_i32 s73,s48,7
v_fma_f32 v113,v113,s75,v13)"
R"(
v_fma_f32 v113,-v116,v116,v113
v_mfma_f32_16x16x32_bf16 v[48:51],a[8:11],v[52:55],0
buffer_load_dwordx4 v0,s[36:39],s61 offen offset:0 sc1 lds
v_mfma_f32_16x16x32_bf16 v[40:43],a[12:15],v[56:59],v[40:43]
s_lshr_b32 s73,s73,1
ds_read_b128 v[76:79],v2 offset:38912
v_max_f32 v113,v113,v13
v_mfma_f32_16x16x32_bf16 v[44:47],a[16:19],v[56:59],v[44:47]
s_add_u32 s74,s53,s55
v_rsq_f32 v112,v113
v_mfma_f32_16x16x32_bf16 v[48:51],a[20:23],v[56:59],v[48:51]
s_waitcnt lgkmcnt(3)
v_mul_f32 v117,s76,v112
v_mul_f32 v118,v112,v112
v_mfma_f32_16x16x32_bf16 v[40:43],a[24:27],v[60:63],v[40:43]
buffer_load_dwordx4 v0,s[36:39],s61 offen offset:1024 sc1 lds
v_mfma_f32_16x16x32_bf16 v[44:47],a[28:31],v[60:63],v[44:47]
s_cmp_eq_u32 s74,s56
ds_read_b128 v[80:83],v2 offset:39936
v_mul_f32 v119,v113,v112
v_mfma_f32_16x16x32_bf16 v[48:51],a[32:35],v[60:63],v[48:51]
s_cselect_b32 s74,s57,s74
v_mul_f32 v118,s77,v118
v_mul_f32 v118,v118,v117
v_mfma_f32_16x16x32_bf16 v[40:43],a[36:39],v[64:67],v[40:43]
s_cmp_eq_u32 s74,s58
ds_read_b128 v[52:55],v2 offset:40960
v_fma_f32 v28,-v116,v16,v28
v_mfma_f32_16x16x32_bf16 v[44:47],a[40:43],v[64:67],v[44:47]
buffer_load_dwordx4 v0,s[36:39],s61 offen offset:2048 sc1 lds
v_mfma_f32_16x16x32_bf16 v[48:51],a[44:47],v[64:67],v[48:51]
s_waitcnt lgkmcnt(3)
v_fma_f32 v29,-v116,v17,v29
v_fma_f32 v30,-v116,v18,v30
v_mfma_f32_16x16x32_bf16 v[40:43],a[48:51],v[68:71],v[40:43]
s_cselect_b32 s74,s59,s74
ds_read_b128 v[56:59],v2 offset:41984
v_fma_f32 v31,-v116,v19,v31
v_mfma_f32_16x16x32_bf16 v[44:47],a[52:55],v[68:71],v[44:47]
s_cmp_ge_u32 s67,s59
v_mul_f32 v84,v28,v28
v_mul_f32 v85,v29,v29
v_mfma_f32_16x16x32_bf16 v[48:51],a[56:59],v[68:71],v[48:51]
buffer_load_dwordx4 v0,s[36:39],s61 offen offset:3072 sc1 lds
v_mfma_f32_16x16x32_bf16 v[40:43],a[60:63],v[72:75],v[40:43]
s_cselect_b32 s86,s50,s51
ds_read_b128 v[60:63],v2 offset:43008
v_mul_f32 v86,v30,v30
v_mfma_f32_16x16x32_bf16 v[44:47],a[64:67],v[72:75],v[44:47]
s_add_u32 m0,s62,16384
v_mul_f32 v87,v31,v31
v_fma_f32 v84,v118,v84,v117
v_mfma_f32_16x16x32_bf16 v[48:51],a[68:71],v[72:75],v[48:51]
s_waitcnt lgkmcnt(3)
v_mul_f32 v84,v28,v84
v_fma_f32 v85,v118,v85,v117
v_mfma_f32_16x16x32_bf16 v[40:43],a[72:75],v[76:79],v[40:43]
s_lshr_b32 s72,s61,5
ds_read_b128 v[64:67],v2 offset:44032
v_mul_f32 v85,v29,v85
v_mfma_f32_16x16x32_bf16 v[44:47],a[76:79],v[76:79],v[44:47]
s_mov_b32 s62,s64
v_exp_f32 v84,v84
v_mfma_f32_16x16x32_bf16 v[48:51],a[80:83],v[76:79],v[48:51]
s_mov_b64 s[44:45],s[46:47]
v_exp_f32 v85,v85
v_mfma_f32_16x16x32_bf16 v[40:43],a[84:87],v[80:83],v[40:43]
s_mov_b64 s[46:47],s[48:49]
ds_read_b128 v[68:71],v2 offset:45056
v_fma_f32 v84,v84,v119,v119
v_mfma_f32_16x16x32_bf16 v[44:47],a[88:91],v[80:83],v[44:47]
s_mov_b64 s[48:49],s[50:51]
v_rcp_f32 v84,v84
v_mfma_f32_16x16x32_bf16 v[48:51],a[92:95],v[80:83],v[48:51]
s_waitcnt lgkmcnt(3)
v_fma_f32 v85,v85,v119,v119
v_fma_f32 v86,v118,v86,v117
v_mfma_f32_16x16x32_bf16 v[40:43],a[96:99],v[52:55],v[40:43]
s_mov_b64 s[50:51],s[52:53]
ds_read_b128 v[72:75],v2 offset:46080
v_mul_f32 v86,v30,v86
v_mfma_f32_16x16x32_bf16 v[44:47],a[100:103],v[52:55],v[44:47]
s_mov_b32 s52,s74
v_exp_f32 v86,v86
v_mfma_f32_16x16x32_bf16 v[48:51],a[104:107],v[52:55],v[48:51]
v_rcp_f32 v85,v85
v_mfma_f32_16x16x32_bf16 v[40:43],a[108:111],v[56:59],v[40:43]
ds_read_b128 v[76:79],v2 offset:47104
v_fma_f32 v86,v86,v119,v119
v_mfma_f32_16x16x32_bf16 v[44:47],a[112:115],v[56:59],v[44:47]
v_rcp_f32 v86,v86
v_mfma_f32_16x16x32_bf16 v[48:51],a[116:119],v[56:59],v[48:51]
s_waitcnt lgkmcnt(3)
v_fma_f32 v87,v118,v87,v117
v_mul_f32 v87,v31,v87
v_mfma_f32_16x16x32_bf16 v[40:43],a[120:123],v[60:63],v[40:43]
ds_read_b128 v[80:83],v2 offset:48128
v_mul_f32 v28,v28,v84
v_mfma_f32_16x16x32_bf16 v[44:47],a[124:127],v[60:63],v[44:47]
s_waitcnt vmcnt(6)
v_exp_f32 v87,v87
v_mfma_f32_16x16x32_bf16 v[48:51],a[128:131],v[60:63],v[48:51]
s_barrier
v_mfma_f32_16x16x32_bf16 v[40:43],a[132:135],v[64:67],v[40:43]
ds_read_b128 v[52:55],v2 offset:49152
v_fma_f32 v87,v87,v119,v119
v_mfma_f32_16x16x32_bf16 v[44:47],a[136:139],v[64:67],v[44:47]
v_rcp_f32 v87,v87
v_mfma_f32_16x16x32_bf16 v[48:51],a[140:143],v[64:67],v[48:51]
s_waitcnt lgkmcnt(3)
v_add_f32_dpp v108,v108,v108 quad_perm:[1,0,3,2] row_mask:0xf bank_mask:0xf
v_mul_f32 v29,v29,v85
v_mfma_f32_16x16x32_bf16 v[40:43],a[144:147],v[68:71],v[40:43]
ds_read_b128 v[56:59],v2 offset:50176
v_add_f32_dpp v108,v108,v108 quad_perm:[2,3,0,1] row_mask:0xf bank_mask:0xf
v_mfma_f32_16x16x32_bf16 v[44:47],a[148:151],v[68:71],v[44:47]
s_nop 0
v_add_f32_dpp v108,v108,v108 row_half_mirror row_mask:0xf bank_mask:0xf
v_mul_f32 v30,v30,v86
v_mfma_f32_16x16x32_bf16 v[48:51],a[152:155],v[68:71],v[48:51]
ds_write_b32 v11,v108 offset:128
v_mfma_f32_16x16x32_bf16 v[40:43],a[156:159],v[72:75],v[40:43]
ds_read_b128 v[60:63],v2 offset:51200
v_mul_f32 v31,v31,v87
v_mfma_f32_16x16x32_bf16 v[44:47],a[160:163],v[72:75],v[44:47]
v_cvt_pk_bf16_f32 v96,v28,v29
v_cvt_pk_bf16_f32 v97,v30,v31
v_mfma_f32_16x16x32_bf16 v[48:51],a[164:167],v[72:75],v[48:51]
s_waitcnt lgkmcnt(4)
v_fma_f32 v32,-v116,v20,v32
v_fma_f32 v33,-v116,v21,v33
v_mfma_f32_16x16x32_bf16 v[40:43],a[168:171],v[76:79],v[40:43]
ds_read_b128 v[64:67],v2 offset:52224
v_fma_f32 v34,-v116,v22,v34
v_mfma_f32_16x16x32_bf16 v[44:47],a[172:175],v[76:79],v[44:47]
v_fma_f32 v35,-v116,v23,v35
v_mul_f32 v88,v32,v32
v_mfma_f32_16x16x32_bf16 v[48:51],a[176:179],v[76:79],v[48:51]
buffer_load_dwordx4 v1,s[36:39],s61 offen offset:0 sc1 lds
v_mfma_f32_16x16x32_bf16 v[40:43],a[180:183],v[80:83],v[40:43]
ds_read_b128 v[68:71],v2 offset:53248
v_mul_f32 v89,v33,v33
v_mfma_f32_16x16x32_bf16 v[44:47],a[184:187],v[80:83],v[44:47]
v_mul_f32 v90,v34,v34
v_mul_f32 v91,v35,v35
v_mfma_f32_16x16x32_bf16 v[48:51],a[188:191],v[80:83],v[48:51]
s_waitcnt lgkmcnt(4)
v_fma_f32 v88,v118,v88,v117
v_mul_f32 v88,v32,v88
v_mfma_f32_16x16x32_bf16 v[40:43],a[192:195],v[52:55],v[40:43]
buffer_load_dwordx4 v1,s[36:39],s61 offen offset:1024 sc1 lds
v_mfma_f32_16x16x32_bf16 v[44:47],a[196:199],v[52:55],v[44:47]
ds_read_b128 v[72:75],v2 offset:54272
v_fma_f32 v89,v118,v89,v117
v_mfma_f32_16x16x32_bf16 v[48:51],a[200:203],v[52:55],v[48:51]
v_exp_f32 v88,v88
v_mfma_f32_16x16x32_bf16 v[40:43],a[204:207],v[56:59],v[40:43]
ds_read_b128 v[76:79],v2 offset:55296
v_mul_f32 v89,v33,v89
v_mfma_f32_16x16x32_bf16 v[44:47],a[208:211],v[56:59],v[44:47]
buffer_load_dwordx4 v1,s[36:39],s61 offen offset:2048 sc1 lds
v_mfma_f32_16x16x32_bf16 v[48:51],a[212:215],v[56:59],v[48:51]
s_waitcnt lgkmcnt(3)
v_exp_f32 v89,v89
v_mfma_f32_16x16x32_bf16 v[40:43],a[216:219],v[60:63],v[40:43]
ds_read_b128 v[80:83],v2 offset:56320
v_fma_f32 v88,v88,v119,v119
v_mfma_f32_16x16x32_bf16 v[44:47],a[220:223],v[60:63],v[44:47]
v_rcp_f32 v88,v88
v_mfma_f32_16x16x32_bf16 v[48:51],a[224:227],v[60:63],v[48:51]
buffer_load_dwordx4 v1,s[36:39],s61 offen offset:3072 sc1 lds
v_mfma_f32_16x16x32_bf16 v[40:43],a[228:231],v[64:67],v[40:43]
buffer_load_dword v104,v9,s[36:39],s72 offen offset:0 sc1
v_mfma_f32_16x16x32_bf16 v[44:47],a[232:235],v[64:67],v[44:47]
ds_read_b128 v[52:55],v2 offset:57344
v_fma_f32 v89,v89,v119,v119
v_mfma_f32_16x16x32_bf16 v[48:51],a[236:239],v[64:67],v[48:51]
s_waitcnt lgkmcnt(3)
v_rcp_f32 v89,v89
v_mfma_f32_16x16x32_bf16 v[40:43],a[240:243],v[68:71],v[40:43]
ds_read_b128 v[56:59],v2 offset:58368
v_fma_f32 v90,v118,v90,v117
v_mfma_f32_16x16x32_bf16 v[44:47],a[244:247],v[68:71],v[44:47]
v_mul_f32 v90,v34,v90
v_fma_f32 v91,v118,v91,v117
v_mfma_f32_16x16x32_bf16 v[48:51],a[248:251],v[68:71],v[48:51]
v_exp_f32 v90,v90
v_mfma_f32_16x16x32_bf16 v[40:43],a[252:255],v[72:75],v[40:43]
ds_read_b128 v[60:63],v2 offset:59392
v_mul_f32 v91,v35,v91
v_mfma_f32_16x16x32_bf16 v[44:47],v[128:131],v[72:75],v[44:47]
v_exp_f32 v91,v91
v_mfma_f32_16x16x32_bf16 v[48:51],v[132:135],v[72:75],v[48:51]
s_waitcnt lgkmcnt(3)
v_fma_f32 v90,v90,v119,v119
v_fma_f32 v91,v91,v119,v119
v_mfma_f32_16x16x32_bf16 v[40:43],v[136:139],v[76:79],v[40:43]
ds_read_b128 v[64:67],v2 offset:60416
v_mul_f32 v32,v32,v88
v_mfma_f32_16x16x32_bf16 v[44:47],v[140:143],v[76:79],v[44:47])"
R"(
v_rcp_f32 v90,v90
v_mfma_f32_16x16x32_bf16 v[48:51],v[144:147],v[76:79],v[48:51]
v_rcp_f32 v91,v91
v_mfma_f32_16x16x32_bf16 v[40:43],v[148:151],v[80:83],v[40:43]
ds_read_b128 v[68:71],v2 offset:61440
v_mul_f32 v33,v33,v89
v_mfma_f32_16x16x32_bf16 v[44:47],v[152:155],v[80:83],v[44:47]
v_mul_f32 v34,v34,v90
v_mul_f32 v35,v35,v91
v_mfma_f32_16x16x32_bf16 v[48:51],v[156:159],v[80:83],v[48:51]
s_waitcnt lgkmcnt(3)
v_cvt_pk_bf16_f32 v98,v32,v33
v_cvt_pk_bf16_f32 v99,v34,v35
v_mfma_f32_16x16x32_bf16 v[40:43],v[160:163],v[52:55],v[40:43]
ds_read_b128 v[72:75],v2 offset:62464
v_fma_f32 v36,-v116,v24,v36
v_mfma_f32_16x16x32_bf16 v[44:47],v[164:167],v[52:55],v[44:47]
buffer_store_dwordx4 v[96:99],v3,s[36:39],s73 offen offset:0 sc0 sc1
v_fma_f32 v37,-v116,v25,v37
v_fma_f32 v38,-v116,v26,v38
v_fma_f32 v39,-v116,v27,v39
v_mfma_f32_16x16x32_bf16 v[48:51],v[168:171],v[52:55],v[48:51]
v_mul_f32 v92,v36,v36
v_mul_f32 v93,v37,v37
v_mfma_f32_16x16x32_bf16 v[40:43],v[172:175],v[56:59],v[40:43]
ds_read_b128 v[76:79],v2 offset:63488
v_mul_f32 v94,v38,v38
v_mfma_f32_16x16x32_bf16 v[44:47],v[176:179],v[56:59],v[44:47]
v_mul_f32 v95,v39,v39
v_fma_f32 v92,v118,v92,v117
v_mfma_f32_16x16x32_bf16 v[48:51],v[180:183],v[56:59],v[48:51]
s_waitcnt lgkmcnt(3)
v_mul_f32 v92,v36,v92
v_fma_f32 v93,v118,v93,v117
v_mfma_f32_16x16x32_bf16 v[40:43],v[184:187],v[60:63],v[40:43]
ds_read_b128 v[80:83],v2 offset:64512
v_mul_f32 v93,v37,v93
v_mfma_f32_16x16x32_bf16 v[44:47],v[188:191],v[60:63],v[44:47]
s_waitcnt vmcnt(6)
v_xor_b32 v2,65536,v2
v_fma_f32 v94,v118,v94,v117
v_mfma_f32_16x16x32_bf16 v[48:51],v[192:195],v[60:63],v[48:51]
s_barrier
v_mfma_f32_16x16x32_bf16 v[40:43],v[196:199],v[64:67],v[40:43]
ds_read_b128 v[52:55],v2 offset:0
ds_read_b32 v14,v8 offset:256
v_mfma_f32_16x16x32_bf16 v[44:47],v[200:203],v[64:67],v[44:47]
ds_read_b64 v[112:113],v12 offset:128
v_fma_f32 v95,v118,v95,v117
v_mfma_f32_16x16x32_bf16 v[48:51],v[204:207],v[64:67],v[48:51]
s_waitcnt lgkmcnt(5)
v_exp_f32 v92,v92
v_mfma_f32_16x16x32_bf16 v[40:43],v[208:211],v[68:71],v[40:43]
ds_read_b128 v[56:59],v2 offset:1024
v_mul_f32 v94,v38,v94
v_mfma_f32_16x16x32_bf16 v[44:47],v[212:215],v[68:71],v[44:47]
s_waitcnt lgkmcnt(2)
v_exp_f32 v93,v93
v_mfma_f32_16x16x32_bf16 v[48:51],v[216:219],v[68:71],v[48:51]
s_waitcnt lgkmcnt(1)
v_cmp_eq_u32 vcc,s60,v14
v_exp_f32 v94,v94
v_mul_f32 v95,v39,v95
v_mfma_f32_16x16x32_bf16 v[40:43],v[220:223],v[72:75],v[40:43]
s_cmp_eq_u32 vcc_lo,-1
ds_read_b128 v[60:63],v2 offset:2048
v_fma_f32 v92,v92,v119,v119
v_mfma_f32_16x16x32_bf16 v[44:47],v[224:227],v[72:75],v[44:47]
s_cselect_b32 s66,s86,s59
v_exp_f32 v95,v95
v_mfma_f32_16x16x32_bf16 v[48:51],v[228:231],v[72:75],v[48:51]
v_rcp_f32 v92,v92
v_mfma_f32_16x16x32_bf16 v[40:43],v[232:235],v[76:79],v[40:43]
s_lshr_b32 s61,s66,0
v_fma_f32 v93,v93,v119,v119
v_fma_f32 v94,v94,v119,v119
v_fma_f32 v95,v95,v119,v119
v_rcp_f32 v93,v93
v_rcp_f32 v94,v94
v_rcp_f32 v95,v95
v_mfma_f32_16x16x32_bf16 v[44:47],v[236:239],v[76:79],v[44:47]
s_xor_b32 s64,s64,65536
v_mul_f32 v36,v36,v92
v_mul_f32 v37,v37,v93
v_mul_f32 v38,v38,v94
v_mul_f32 v39,v39,v95
v_cvt_pk_bf16_f32 v100,v36,v37
v_cvt_pk_bf16_f32 v101,v38,v39
v_mfma_f32_16x16x32_bf16 v[48:51],v[240:243],v[76:79],v[48:51]
s_cmp_lt_u32 s67,s59
ds_read_b128 v[64:67],v2 offset:3072
buffer_store_dwordx2 v[100:101],v10,s[36:39],s73 offen offset:0 sc0 sc1
v_mfma_f32_16x16x32_bf16 v[40:43],v[244:247],v[80:83],v[40:43]
ds_read_b128 v[68:71],v2 offset:4096
v_mfma_f32_16x16x32_bf16 v[44:47],v[248:251],v[80:83],v[44:47]
s_waitcnt lgkmcnt(3)
v_mfma_f32_16x16x32_bf16 v[48:51],v[252:255],v[80:83],v[48:51]
s_cbranch_scc1 Lg3_d0w1_%=
s_branch Lg3_slow0_%=
Lg3_d0w2_%=:
v_mfma_f32_16x16x32_bf16 v[28:31],a[0:3],v[52:55],0
s_mov_b32 m0,s62
ds_read_b128 v[72:75],v2 offset:5120
v_mul_f32 v116,s75,v112
v_mfma_f32_16x16x32_bf16 v[32:35],a[4:7],v[52:55],0
s_mul_i32 s73,s47,7
v_fma_f32 v113,v113,s75,v13
v_fma_f32 v113,-v116,v116,v113
v_mfma_f32_16x16x32_bf16 v[36:39],a[8:11],v[52:55],0
s_lshr_b32 s73,s73,1
v_max_f32 v113,v113,v13
v_rsq_f32 v112,v113
v_mfma_f32_16x16x32_bf16 v[28:31],a[12:15],v[56:59],v[28:31]
buffer_load_dwordx4 v0,s[36:39],s61 offen offset:0 sc1 lds
v_mfma_f32_16x16x32_bf16 v[32:35],a[16:19],v[56:59],v[32:35]
s_add_u32 s53,s52,s55
ds_read_b128 v[76:79],v2 offset:6144
v_mul_f32 v117,s76,v112
v_mfma_f32_16x16x32_bf16 v[36:39],a[20:23],v[56:59],v[36:39]
s_waitcnt lgkmcnt(3)
v_mul_f32 v118,v112,v112
v_mul_f32 v119,v113,v112
v_mfma_f32_16x16x32_bf16 v[28:31],a[24:27],v[60:63],v[28:31]
s_cmp_eq_u32 s53,s56
ds_read_b128 v[80:83],v2 offset:7168
v_mul_f32 v118,s77,v118
v_mfma_f32_16x16x32_bf16 v[32:35],a[28:31],v[60:63],v[32:35]
buffer_load_dwordx4 v0,s[36:39],s61 offen offset:1024 sc1 lds
v_mfma_f32_16x16x32_bf16 v[36:39],a[32:35],v[60:63],v[36:39]
s_cselect_b32 s53,s57,s53
v_mul_f32 v118,v118,v117
v_fma_f32 v40,-v116,v16,v40
v_mfma_f32_16x16x32_bf16 v[28:31],a[36:39],v[64:67],v[28:31]
s_cmp_eq_u32 s53,s58
ds_read_b128 v[52:55],v2 offset:8192
v_fma_f32 v41,-v116,v17,v41
v_mfma_f32_16x16x32_bf16 v[32:35],a[40:43],v[64:67],v[32:35]
s_cselect_b32 s53,s59,s53
v_fma_f32 v42,-v116,v18,v42
v_fma_f32 v43,-v116,v19,v43
v_mfma_f32_16x16x32_bf16 v[36:39],a[44:47],v[64:67],v[36:39]
s_waitcnt lgkmcnt(3)
buffer_load_dwordx4 v0,s[36:39],s61 offen offset:2048 sc1 lds
v_mfma_f32_16x16x32_bf16 v[28:31],a[48:51],v[68:71],v[28:31]
s_cmp_ge_u32 s66,s59
ds_read_b128 v[56:59],v2 offset:9216
v_mul_f32 v84,v40,v40
v_mfma_f32_16x16x32_bf16 v[32:35],a[52:55],v[68:71],v[32:35]
s_cselect_b32 s86,s49,s50
v_mul_f32 v85,v41,v41
v_mul_f32 v86,v42,v42
v_mfma_f32_16x16x32_bf16 v[36:39],a[56:59],v[68:71],v[36:39]
v_mul_f32 v87,v43,v43
v_fma_f32 v84,v118,v84,v117
v_mfma_f32_16x16x32_bf16 v[28:31],a[60:63],v[72:75],v[28:31]
buffer_load_dwordx4 v0,s[36:39],s61 offen offset:3072 sc1 lds
v_mfma_f32_16x16x32_bf16 v[32:35],a[64:67],v[72:75],v[32:35]
s_add_u32 m0,s62,16384
ds_read_b128 v[60:63],v2 offset:10240
v_mul_f32 v84,v40,v84
v_mfma_f32_16x16x32_bf16 v[36:39],a[68:71],v[72:75],v[36:39]
s_waitcnt lgkmcnt(3)
v_exp_f32 v84,v84
v_mfma_f32_16x16x32_bf16 v[28:31],a[72:75],v[76:79],v[28:31]
s_lshr_b32 s72,s61,5
ds_read_b128 v[64:67],v2 offset:11264
v_fma_f32 v84,v84,v119,v119
v_mfma_f32_16x16x32_bf16 v[32:35],a[76:79],v[76:79],v[32:35]
s_mov_b32 s62,s63
v_rcp_f32 v84,v84
v_mfma_f32_16x16x32_bf16 v[36:39],a[80:83],v[76:79],v[36:39]
v_fma_f32 v85,v118,v85,v117
v_mul_f32 v85,v41,v85
v_mfma_f32_16x16x32_bf16 v[28:31],a[84:87],v[80:83],v[28:31]
ds_read_b128 v[68:71],v2 offset:12288
v_fma_f32 v86,v118,v86,v117
v_mfma_f32_16x16x32_bf16 v[32:35],a[88:91],v[80:83],v[32:35]
v_exp_f32 v85,v85
v_mfma_f32_16x16x32_bf16 v[36:39],a[92:95],v[80:83],v[36:39]
s_waitcnt lgkmcnt(3)
v_mul_f32 v86,v42,v86
v_fma_f32 v85,v85,v119,v119
v_mfma_f32_16x16x32_bf16 v[28:31],a[96:99],v[52:55],v[28:31]
ds_read_b128 v[72:75],v2 offset:13312
v_fma_f32 v87,v118,v87,v117
v_mfma_f32_16x16x32_bf16 v[32:35],a[100:103],v[52:55],v[32:35]
v_exp_f32 v86,v86
v_mfma_f32_16x16x32_bf16 v[36:39],a[104:107],v[52:55],v[36:39]
v_rcp_f32 v85,v85
v_mfma_f32_16x16x32_bf16 v[28:31],a[108:111],v[56:59],v[28:31]
ds_read_b128 v[76:79],v2 offset:14336
v_mul_f32 v87,v43,v87
v_mfma_f32_16x16x32_bf16 v[32:35],a[112:115],v[56:59],v[32:35]
v_exp_f32 v87,v87
v_mfma_f32_16x16x32_bf16 v[36:39],a[116:119],v[56:59],v[36:39]
s_waitcnt lgkmcnt(3)
v_fma_f32 v86,v86,v119,v119
v_fma_f32 v87,v87,v119,v119
v_mfma_f32_16x16x32_bf16 v[28:31],a[120:123],v[60:63],v[28:31]
ds_read_b128 v[80:83],v2 offset:15360)"
R"(
v_mul_f32 v40,v40,v84
v_mfma_f32_16x16x32_bf16 v[32:35],a[124:127],v[60:63],v[32:35]
s_waitcnt vmcnt(6)
v_rcp_f32 v86,v86
v_mfma_f32_16x16x32_bf16 v[36:39],a[128:131],v[60:63],v[36:39]
s_barrier
v_mfma_f32_16x16x32_bf16 v[28:31],a[132:135],v[64:67],v[28:31]
ds_read_b128 v[52:55],v2 offset:16384
v_add_f32_dpp v104,v104,v104 quad_perm:[1,0,3,2] row_mask:0xf bank_mask:0xf
v_mfma_f32_16x16x32_bf16 v[32:35],a[136:139],v[64:67],v[32:35]
v_rcp_f32 v87,v87
v_mfma_f32_16x16x32_bf16 v[36:39],a[140:143],v[64:67],v[36:39]
s_waitcnt lgkmcnt(3)
v_add_f32_dpp v104,v104,v104 quad_perm:[2,3,0,1] row_mask:0xf bank_mask:0xf
v_mul_f32 v41,v41,v85
v_mfma_f32_16x16x32_bf16 v[28:31],a[144:147],v[68:71],v[28:31]
ds_read_b128 v[56:59],v2 offset:17408
v_add_f32_dpp v104,v104,v104 row_half_mirror row_mask:0xf bank_mask:0xf
v_mfma_f32_16x16x32_bf16 v[32:35],a[148:151],v[68:71],v[32:35]
ds_write_b32 v11,v104 offset:0
v_mfma_f32_16x16x32_bf16 v[36:39],a[152:155],v[68:71],v[36:39]
v_mul_f32 v42,v42,v86
v_mul_f32 v43,v43,v87
v_mfma_f32_16x16x32_bf16 v[28:31],a[156:159],v[72:75],v[28:31]
ds_read_b128 v[60:63],v2 offset:18432
v_cvt_pk_bf16_f32 v96,v40,v41
v_mfma_f32_16x16x32_bf16 v[32:35],a[160:163],v[72:75],v[32:35]
v_cvt_pk_bf16_f32 v97,v42,v43
v_fma_f32 v44,-v116,v20,v44
v_mfma_f32_16x16x32_bf16 v[36:39],a[164:167],v[72:75],v[36:39]
s_waitcnt lgkmcnt(4)
v_fma_f32 v45,-v116,v21,v45
v_fma_f32 v46,-v116,v22,v46
v_mfma_f32_16x16x32_bf16 v[28:31],a[168:171],v[76:79],v[28:31]
ds_read_b128 v[64:67],v2 offset:19456
v_fma_f32 v47,-v116,v23,v47
v_mfma_f32_16x16x32_bf16 v[32:35],a[172:175],v[76:79],v[32:35]
v_mul_f32 v88,v44,v44
v_mul_f32 v89,v45,v45
v_mfma_f32_16x16x32_bf16 v[36:39],a[176:179],v[76:79],v[36:39]
v_mul_f32 v90,v46,v46
v_mul_f32 v91,v47,v47
v_mfma_f32_16x16x32_bf16 v[28:31],a[180:183],v[80:83],v[28:31]
buffer_load_dwordx4 v1,s[36:39],s61 offen offset:0 sc1 lds
v_mfma_f32_16x16x32_bf16 v[32:35],a[184:187],v[80:83],v[32:35]
ds_read_b128 v[68:71],v2 offset:20480
v_fma_f32 v88,v118,v88,v117
v_mfma_f32_16x16x32_bf16 v[36:39],a[188:191],v[80:83],v[36:39]
s_waitcnt lgkmcnt(4)
v_mul_f32 v88,v44,v88
v_exp_f32 v88,v88
v_mfma_f32_16x16x32_bf16 v[28:31],a[192:195],v[52:55],v[28:31]
ds_read_b128 v[72:75],v2 offset:21504
v_fma_f32 v88,v88,v119,v119
v_mfma_f32_16x16x32_bf16 v[32:35],a[196:199],v[52:55],v[32:35]
buffer_load_dwordx4 v1,s[36:39],s61 offen offset:1024 sc1 lds
v_mfma_f32_16x16x32_bf16 v[36:39],a[200:203],v[52:55],v[36:39]
v_rcp_f32 v88,v88
v_mfma_f32_16x16x32_bf16 v[28:31],a[204:207],v[56:59],v[28:31]
ds_read_b128 v[76:79],v2 offset:22528
v_fma_f32 v89,v118,v89,v117
v_mfma_f32_16x16x32_bf16 v[32:35],a[208:211],v[56:59],v[32:35]
v_mul_f32 v89,v45,v89
v_fma_f32 v90,v118,v90,v117
v_mfma_f32_16x16x32_bf16 v[36:39],a[212:215],v[56:59],v[36:39]
s_waitcnt lgkmcnt(3)
buffer_load_dwordx4 v1,s[36:39],s61 offen offset:2048 sc1 lds
v_mfma_f32_16x16x32_bf16 v[28:31],a[216:219],v[60:63],v[28:31]
ds_read_b128 v[80:83],v2 offset:23552
v_mul_f32 v90,v46,v90
v_mfma_f32_16x16x32_bf16 v[32:35],a[220:223],v[60:63],v[32:35]
v_exp_f32 v89,v89
v_mfma_f32_16x16x32_bf16 v[36:39],a[224:227],v[60:63],v[36:39]
v_exp_f32 v90,v90
v_mfma_f32_16x16x32_bf16 v[28:31],a[228:231],v[64:67],v[28:31]
buffer_load_dwordx4 v1,s[36:39],s61 offen offset:3072 sc1 lds
v_mfma_f32_16x16x32_bf16 v[32:35],a[232:235],v[64:67],v[32:35]
buffer_load_dword v108,v9,s[36:39],s72 offen offset:0 sc1
v_mfma_f32_16x16x32_bf16 v[36:39],a[236:239],v[64:67],v[36:39]
s_waitcnt lgkmcnt(2)
ds_read_b128 v[52:55],v2 offset:24576
v_fma_f32 v89,v89,v119,v119
v_mfma_f32_16x16x32_bf16 v[28:31],a[240:243],v[68:71],v[28:31]
ds_read_b128 v[56:59],v2 offset:25600
v_fma_f32 v90,v90,v119,v119
v_mfma_f32_16x16x32_bf16 v[32:35],a[244:247],v[68:71],v[32:35]
v_rcp_f32 v89,v89
v_mfma_f32_16x16x32_bf16 v[36:39],a[248:251],v[68:71],v[36:39]
v_rcp_f32 v90,v90
v_mfma_f32_16x16x32_bf16 v[28:31],a[252:255],v[72:75],v[28:31]
ds_read_b128 v[60:63],v2 offset:26624
v_fma_f32 v91,v118,v91,v117
v_mfma_f32_16x16x32_bf16 v[32:35],v[128:131],v[72:75],v[32:35]
v_mul_f32 v91,v47,v91
v_mul_f32 v44,v44,v88
v_mfma_f32_16x16x32_bf16 v[36:39],v[132:135],v[72:75],v[36:39]
s_waitcnt lgkmcnt(3)
v_exp_f32 v91,v91
v_mfma_f32_16x16x32_bf16 v[28:31],v[136:139],v[76:79],v[28:31]
ds_read_b128 v[64:67],v2 offset:27648
v_fma_f32 v91,v91,v119,v119
v_mfma_f32_16x16x32_bf16 v[32:35],v[140:143],v[76:79],v[32:35]
v_rcp_f32 v91,v91
v_mfma_f32_16x16x32_bf16 v[36:39],v[144:147],v[76:79],v[36:39]
v_mul_f32 v45,v45,v89
v_mul_f32 v46,v46,v90
v_mfma_f32_16x16x32_bf16 v[28:31],v[148:151],v[80:83],v[28:31]
ds_read_b128 v[68:71],v2 offset:28672
v_mul_f32 v47,v47,v91
v_mfma_f32_16x16x32_bf16 v[32:35],v[152:155],v[80:83],v[32:35]
v_cvt_pk_bf16_f32 v98,v44,v45
v_cvt_pk_bf16_f32 v99,v46,v47
v_mfma_f32_16x16x32_bf16 v[36:39],v[156:159],v[80:83],v[36:39]
s_waitcnt lgkmcnt(3)
v_fma_f32 v48,-v116,v24,v48
v_fma_f32 v49,-v116,v25,v49
v_mfma_f32_16x16x32_bf16 v[28:31],v[160:163],v[52:55],v[28:31]
ds_read_b128 v[72:75],v2 offset:29696
v_fma_f32 v50,-v116,v26,v50
v_mfma_f32_16x16x32_bf16 v[32:35],v[164:167],v[52:55],v[32:35]
v_fma_f32 v51,-v116,v27,v51
v_mul_f32 v92,v48,v48
v_mfma_f32_16x16x32_bf16 v[36:39],v[168:171],v[52:55],v[36:39]
buffer_store_dwordx4 v[96:99],v3,s[36:39],s73 offen offset:0 sc0 sc1
v_mul_f32 v93,v49,v49
v_mul_f32 v94,v50,v50
v_mul_f32 v95,v51,v51
v_mfma_f32_16x16x32_bf16 v[28:31],v[172:175],v[56:59],v[28:31]
ds_read_b128 v[76:79],v2 offset:30720
v_fma_f32 v92,v118,v92,v117
v_mfma_f32_16x16x32_bf16 v[32:35],v[176:179],v[56:59],v[32:35]
v_mul_f32 v92,v48,v92
v_fma_f32 v93,v118,v93,v117
v_mfma_f32_16x16x32_bf16 v[36:39],v[180:183],v[56:59],v[36:39]
s_waitcnt lgkmcnt(3)
v_exp_f32 v92,v92
v_mfma_f32_16x16x32_bf16 v[28:31],v[184:187],v[60:63],v[28:31]
ds_read_b128 v[80:83],v2 offset:31744
v_mul_f32 v93,v49,v93
v_mfma_f32_16x16x32_bf16 v[32:35],v[188:191],v[60:63],v[32:35]
s_waitcnt vmcnt(6)
v_exp_f32 v93,v93
v_mfma_f32_16x16x32_bf16 v[36:39],v[192:195],v[60:63],v[36:39]
s_barrier
v_mfma_f32_16x16x32_bf16 v[28:31],v[196:199],v[64:67],v[28:31]
ds_read_b128 v[52:55],v2 offset:32768
ds_read_b32 v14,v8 offset:0
v_mfma_f32_16x16x32_bf16 v[32:35],v[200:203],v[64:67],v[32:35]
ds_read_b64 v[112:113],v12 offset:0
v_fma_f32 v92,v92,v119,v119
v_mfma_f32_16x16x32_bf16 v[36:39],v[204:207],v[64:67],v[36:39]
s_waitcnt lgkmcnt(5)
v_rcp_f32 v92,v92
v_mfma_f32_16x16x32_bf16 v[28:31],v[208:211],v[68:71],v[28:31]
ds_read_b128 v[56:59],v2 offset:33792
v_fma_f32 v93,v93,v119,v119
v_mfma_f32_16x16x32_bf16 v[32:35],v[212:215],v[68:71],v[32:35]
s_waitcnt lgkmcnt(2)
v_rcp_f32 v93,v93
v_mfma_f32_16x16x32_bf16 v[36:39],v[216:219],v[68:71],v[36:39]
s_waitcnt lgkmcnt(1)
v_cmp_eq_u32 vcc,s60,v14
v_fma_f32 v94,v118,v94,v117
v_mul_f32 v94,v50,v94
v_fma_f32 v95,v118,v95,v117
v_mfma_f32_16x16x32_bf16 v[28:31],v[220:223],v[72:75],v[28:31]
s_cmp_eq_u32 vcc_lo,-1
ds_read_b128 v[60:63],v2 offset:34816
v_mul_f32 v95,v51,v95
v_mfma_f32_16x16x32_bf16 v[32:35],v[224:227],v[72:75],v[32:35]
s_cselect_b32 s67,s86,s59
v_exp_f32 v94,v94
v_mfma_f32_16x16x32_bf16 v[36:39],v[228:231],v[72:75],v[36:39]
v_exp_f32 v95,v95
v_mfma_f32_16x16x32_bf16 v[28:31],v[232:235],v[76:79],v[28:31]
s_lshr_b32 s61,s67,0
ds_read_b128 v[64:67],v2 offset:35840
v_fma_f32 v94,v94,v119,v119
v_mfma_f32_16x16x32_bf16 v[32:35],v[236:239],v[76:79],v[32:35]
s_xor_b32 s63,s63,65536
v_fma_f32 v95,v95,v119,v119
v_rcp_f32 v94,v94
v_rcp_f32 v95,v95
v_mfma_f32_16x16x32_bf16 v[36:39],v[240:243],v[76:79],v[36:39]
s_cmp_ge_u32 s66,s59
v_mul_f32 v48,v48,v92
v_mul_f32 v49,v49,v93
v_mul_f32 v50,v50,v94
v_mul_f32 v51,v51,v95
v_cvt_pk_bf16_f32 v100,v48,v49
v_cvt_pk_bf16_f32 v101,v50,v51
v_mfma_f32_16x16x32_bf16 v[28:31],v[244:247],v[80:83],v[28:31]
buffer_store_dwordx2 v[100:101],v10,s[36:39],s73 offen offset:0 sc0 sc1
ds_read_b128 v[68:71],v2 offset:36864
v_mfma_f32_16x16x32_bf16 v[32:35],v[248:251],v[80:83],v[32:35]
s_waitcnt lgkmcnt(3)
v_mfma_f32_16x16x32_bf16 v[36:39],v[252:255],v[80:83],v[36:39]
s_cbranch_scc1 Lg3_slow1_%=
Lg3_d1w2_%=:
v_mfma_f32_16x16x32_bf16 v[40:43],a[0:3],v[52:55],0)"
R"(
s_mov_b32 m0,s62
ds_read_b128 v[72:75],v2 offset:37888
v_mul_f32 v116,s75,v112
v_mfma_f32_16x16x32_bf16 v[44:47],a[4:7],v[52:55],0
s_mul_i32 s73,s48,7
v_fma_f32 v113,v113,s75,v13
v_fma_f32 v113,-v116,v116,v113
v_mfma_f32_16x16x32_bf16 v[48:51],a[8:11],v[52:55],0
s_lshr_b32 s73,s73,1
v_max_f32 v113,v113,v13
v_rsq_f32 v112,v113
v_mfma_f32_16x16x32_bf16 v[40:43],a[12:15],v[56:59],v[40:43]
buffer_load_dwordx4 v0,s[36:39],s61 offen offset:0 sc1 lds
v_mfma_f32_16x16x32_bf16 v[44:47],a[16:19],v[56:59],v[44:47]
s_add_u32 s74,s53,s55
ds_read_b128 v[76:79],v2 offset:38912
v_mul_f32 v117,s76,v112
v_mfma_f32_16x16x32_bf16 v[48:51],a[20:23],v[56:59],v[48:51]
s_waitcnt lgkmcnt(3)
v_mul_f32 v118,v112,v112
v_mul_f32 v119,v113,v112
v_mfma_f32_16x16x32_bf16 v[40:43],a[24:27],v[60:63],v[40:43]
s_cmp_eq_u32 s74,s56
ds_read_b128 v[80:83],v2 offset:39936
v_mul_f32 v118,s77,v118
v_mfma_f32_16x16x32_bf16 v[44:47],a[28:31],v[60:63],v[44:47]
buffer_load_dwordx4 v0,s[36:39],s61 offen offset:1024 sc1 lds
v_mfma_f32_16x16x32_bf16 v[48:51],a[32:35],v[60:63],v[48:51]
s_cselect_b32 s74,s57,s74
v_mul_f32 v118,v118,v117
v_fma_f32 v28,-v116,v16,v28
v_mfma_f32_16x16x32_bf16 v[40:43],a[36:39],v[64:67],v[40:43]
s_cmp_eq_u32 s74,s58
ds_read_b128 v[52:55],v2 offset:40960
v_fma_f32 v29,-v116,v17,v29
v_mfma_f32_16x16x32_bf16 v[44:47],a[40:43],v[64:67],v[44:47]
s_cselect_b32 s74,s59,s74
v_fma_f32 v30,-v116,v18,v30
v_fma_f32 v31,-v116,v19,v31
v_mfma_f32_16x16x32_bf16 v[48:51],a[44:47],v[64:67],v[48:51]
s_waitcnt lgkmcnt(3)
buffer_load_dwordx4 v0,s[36:39],s61 offen offset:2048 sc1 lds
v_mfma_f32_16x16x32_bf16 v[40:43],a[48:51],v[68:71],v[40:43]
s_cmp_ge_u32 s67,s59
ds_read_b128 v[56:59],v2 offset:41984
v_mul_f32 v84,v28,v28
v_mfma_f32_16x16x32_bf16 v[44:47],a[52:55],v[68:71],v[44:47]
s_cselect_b32 s86,s50,s51
v_mul_f32 v85,v29,v29
v_mul_f32 v86,v30,v30
v_mfma_f32_16x16x32_bf16 v[48:51],a[56:59],v[68:71],v[48:51]
s_mov_b64 s[44:45],s[46:47]
v_mul_f32 v87,v31,v31
v_fma_f32 v84,v118,v84,v117
v_mfma_f32_16x16x32_bf16 v[40:43],a[60:63],v[72:75],v[40:43]
buffer_load_dwordx4 v0,s[36:39],s61 offen offset:3072 sc1 lds
v_mfma_f32_16x16x32_bf16 v[44:47],a[64:67],v[72:75],v[44:47]
s_add_u32 m0,s62,16384
ds_read_b128 v[60:63],v2 offset:43008
v_mul_f32 v84,v28,v84
v_mfma_f32_16x16x32_bf16 v[48:51],a[68:71],v[72:75],v[48:51]
s_waitcnt lgkmcnt(3)
v_exp_f32 v84,v84
v_mfma_f32_16x16x32_bf16 v[40:43],a[72:75],v[76:79],v[40:43]
s_lshr_b32 s72,s61,5
ds_read_b128 v[64:67],v2 offset:44032
v_fma_f32 v84,v84,v119,v119
v_mfma_f32_16x16x32_bf16 v[44:47],a[76:79],v[76:79],v[44:47]
s_mov_b32 s62,s64
v_rcp_f32 v84,v84
v_mfma_f32_16x16x32_bf16 v[48:51],a[80:83],v[76:79],v[48:51]
s_mov_b64 s[46:47],s[48:49]
v_fma_f32 v85,v118,v85,v117
v_mul_f32 v85,v29,v85
v_mfma_f32_16x16x32_bf16 v[40:43],a[84:87],v[80:83],v[40:43]
s_mov_b64 s[48:49],s[50:51]
ds_read_b128 v[68:71],v2 offset:45056
v_fma_f32 v86,v118,v86,v117
v_mfma_f32_16x16x32_bf16 v[44:47],a[88:91],v[80:83],v[44:47]
s_mov_b64 s[50:51],s[52:53]
v_exp_f32 v85,v85
v_mfma_f32_16x16x32_bf16 v[48:51],a[92:95],v[80:83],v[48:51]
s_waitcnt lgkmcnt(3)
v_mul_f32 v86,v30,v86
v_fma_f32 v85,v85,v119,v119
v_mfma_f32_16x16x32_bf16 v[40:43],a[96:99],v[52:55],v[40:43]
s_mov_b32 s52,s74
ds_read_b128 v[72:75],v2 offset:46080
v_fma_f32 v87,v118,v87,v117
v_mfma_f32_16x16x32_bf16 v[44:47],a[100:103],v[52:55],v[44:47]
v_exp_f32 v86,v86
v_mfma_f32_16x16x32_bf16 v[48:51],a[104:107],v[52:55],v[48:51]
v_rcp_f32 v85,v85
v_mfma_f32_16x16x32_bf16 v[40:43],a[108:111],v[56:59],v[40:43]
ds_read_b128 v[76:79],v2 offset:47104
v_mul_f32 v87,v31,v87
v_mfma_f32_16x16x32_bf16 v[44:47],a[112:115],v[56:59],v[44:47]
v_exp_f32 v87,v87
v_mfma_f32_16x16x32_bf16 v[48:51],a[116:119],v[56:59],v[48:51]
s_waitcnt lgkmcnt(3)
v_fma_f32 v86,v86,v119,v119
v_fma_f32 v87,v87,v119,v119
v_mfma_f32_16x16x32_bf16 v[40:43],a[120:123],v[60:63],v[40:43]
ds_read_b128 v[80:83],v2 offset:48128
v_mul_f32 v28,v28,v84
v_mfma_f32_16x16x32_bf16 v[44:47],a[124:127],v[60:63],v[44:47]
s_waitcnt vmcnt(6)
v_rcp_f32 v86,v86
v_mfma_f32_16x16x32_bf16 v[48:51],a[128:131],v[60:63],v[48:51]
s_barrier
v_mfma_f32_16x16x32_bf16 v[40:43],a[132:135],v[64:67],v[40:43]
ds_read_b128 v[52:55],v2 offset:49152
v_add_f32_dpp v108,v108,v108 quad_perm:[1,0,3,2] row_mask:0xf bank_mask:0xf
v_mfma_f32_16x16x32_bf16 v[44:47],a[136:139],v[64:67],v[44:47]
v_rcp_f32 v87,v87
v_mfma_f32_16x16x32_bf16 v[48:51],a[140:143],v[64:67],v[48:51]
s_waitcnt lgkmcnt(3)
v_add_f32_dpp v108,v108,v108 quad_perm:[2,3,0,1] row_mask:0xf bank_mask:0xf
v_mul_f32 v29,v29,v85
v_mfma_f32_16x16x32_bf16 v[40:43],a[144:147],v[68:71],v[40:43]
ds_read_b128 v[56:59],v2 offset:50176
v_add_f32_dpp v108,v108,v108 row_half_mirror row_mask:0xf bank_mask:0xf
v_mfma_f32_16x16x32_bf16 v[44:47],a[148:151],v[68:71],v[44:47]
ds_write_b32 v11,v108 offset:128
v_mfma_f32_16x16x32_bf16 v[48:51],a[152:155],v[68:71],v[48:51]
v_mul_f32 v30,v30,v86
v_mul_f32 v31,v31,v87
v_mfma_f32_16x16x32_bf16 v[40:43],a[156:159],v[72:75],v[40:43]
ds_read_b128 v[60:63],v2 offset:51200
v_cvt_pk_bf16_f32 v96,v28,v29
v_mfma_f32_16x16x32_bf16 v[44:47],a[160:163],v[72:75],v[44:47]
v_cvt_pk_bf16_f32 v97,v30,v31
v_fma_f32 v32,-v116,v20,v32
v_mfma_f32_16x16x32_bf16 v[48:51],a[164:167],v[72:75],v[48:51]
s_waitcnt lgkmcnt(4)
v_fma_f32 v33,-v116,v21,v33
v_fma_f32 v34,-v116,v22,v34
v_mfma_f32_16x16x32_bf16 v[40:43],a[168:171],v[76:79],v[40:43]
ds_read_b128 v[64:67],v2 offset:52224
v_fma_f32 v35,-v116,v23,v35
v_mfma_f32_16x16x32_bf16 v[44:47],a[172:175],v[76:79],v[44:47]
v_mul_f32 v88,v32,v32
v_mul_f32 v89,v33,v33
v_mfma_f32_16x16x32_bf16 v[48:51],a[176:179],v[76:79],v[48:51]
v_mul_f32 v90,v34,v34
v_mul_f32 v91,v35,v35
v_mfma_f32_16x16x32_bf16 v[40:43],a[180:183],v[80:83],v[40:43]
buffer_load_dwordx4 v1,s[36:39],s61 offen offset:0 sc1 lds
v_mfma_f32_16x16x32_bf16 v[44:47],a[184:187],v[80:83],v[44:47]
ds_read_b128 v[68:71],v2 offset:53248
v_fma_f32 v88,v118,v88,v117
v_mfma_f32_16x16x32_bf16 v[48:51],a[188:191],v[80:83],v[48:51]
s_waitcnt lgkmcnt(4)
v_mul_f32 v88,v32,v88
v_fma_f32 v89,v118,v89,v117
v_mfma_f32_16x16x32_bf16 v[40:43],a[192:195],v[52:55],v[40:43]
ds_read_b128 v[72:75],v2 offset:54272
v_mul_f32 v89,v33,v89
v_mfma_f32_16x16x32_bf16 v[44:47],a[196:199],v[52:55],v[44:47]
buffer_load_dwordx4 v1,s[36:39],s61 offen offset:1024 sc1 lds
v_mfma_f32_16x16x32_bf16 v[48:51],a[200:203],v[52:55],v[48:51]
v_exp_f32 v88,v88
v_mfma_f32_16x16x32_bf16 v[40:43],a[204:207],v[56:59],v[40:43]
ds_read_b128 v[76:79],v2 offset:55296
v_fma_f32 v88,v88,v119,v119
v_mfma_f32_16x16x32_bf16 v[44:47],a[208:211],v[56:59],v[44:47]
v_exp_f32 v89,v89
v_mfma_f32_16x16x32_bf16 v[48:51],a[212:215],v[56:59],v[48:51]
s_waitcnt lgkmcnt(3)
buffer_load_dwordx4 v1,s[36:39],s61 offen offset:2048 sc1 lds
v_mfma_f32_16x16x32_bf16 v[40:43],a[216:219],v[60:63],v[40:43]
ds_read_b128 v[80:83],v2 offset:56320
v_fma_f32 v89,v89,v119,v119
v_mfma_f32_16x16x32_bf16 v[44:47],a[220:223],v[60:63],v[44:47]
v_rcp_f32 v88,v88
v_mfma_f32_16x16x32_bf16 v[48:51],a[224:227],v[60:63],v[48:51]
v_rcp_f32 v89,v89
v_mfma_f32_16x16x32_bf16 v[40:43],a[228:231],v[64:67],v[40:43]
buffer_load_dwordx4 v1,s[36:39],s61 offen offset:3072 sc1 lds
v_mfma_f32_16x16x32_bf16 v[44:47],a[232:235],v[64:67],v[44:47]
buffer_load_dword v104,v9,s[36:39],s72 offen offset:0 sc1
v_mfma_f32_16x16x32_bf16 v[48:51],a[236:239],v[64:67],v[48:51]
s_waitcnt lgkmcnt(2)
ds_read_b128 v[52:55],v2 offset:57344
v_fma_f32 v90,v118,v90,v117
v_mfma_f32_16x16x32_bf16 v[40:43],a[240:243],v[68:71],v[40:43]
ds_read_b128 v[56:59],v2 offset:58368
v_mul_f32 v90,v34,v90
v_mfma_f32_16x16x32_bf16 v[44:47],a[244:247],v[68:71],v[44:47]
v_exp_f32 v90,v90
v_mfma_f32_16x16x32_bf16 v[48:51],a[248:251],v[68:71],v[48:51]
v_fma_f32 v90,v90,v119,v119
v_rcp_f32 v90,v90
v_mfma_f32_16x16x32_bf16 v[40:43],a[252:255],v[72:75],v[40:43]
ds_read_b128 v[60:63],v2 offset:59392
v_fma_f32 v91,v118,v91,v117
v_mfma_f32_16x16x32_bf16 v[44:47],v[128:131],v[72:75],v[44:47]
v_mul_f32 v91,v35,v91
v_mul_f32 v32,v32,v88
v_mfma_f32_16x16x32_bf16 v[48:51],v[132:135],v[72:75],v[48:51])"
R"(
s_waitcnt lgkmcnt(3)
v_exp_f32 v91,v91
v_mfma_f32_16x16x32_bf16 v[40:43],v[136:139],v[76:79],v[40:43]
ds_read_b128 v[64:67],v2 offset:60416
v_fma_f32 v91,v91,v119,v119
v_mfma_f32_16x16x32_bf16 v[44:47],v[140:143],v[76:79],v[44:47]
v_rcp_f32 v91,v91
v_mfma_f32_16x16x32_bf16 v[48:51],v[144:147],v[76:79],v[48:51]
v_mul_f32 v33,v33,v89
v_mul_f32 v34,v34,v90
v_mfma_f32_16x16x32_bf16 v[40:43],v[148:151],v[80:83],v[40:43]
ds_read_b128 v[68:71],v2 offset:61440
v_mul_f32 v35,v35,v91
v_mfma_f32_16x16x32_bf16 v[44:47],v[152:155],v[80:83],v[44:47]
v_cvt_pk_bf16_f32 v98,v32,v33
v_cvt_pk_bf16_f32 v99,v34,v35
v_mfma_f32_16x16x32_bf16 v[48:51],v[156:159],v[80:83],v[48:51]
s_waitcnt lgkmcnt(3)
v_fma_f32 v36,-v116,v24,v36
v_fma_f32 v37,-v116,v25,v37
v_mfma_f32_16x16x32_bf16 v[40:43],v[160:163],v[52:55],v[40:43]
ds_read_b128 v[72:75],v2 offset:62464
v_fma_f32 v38,-v116,v26,v38
v_mfma_f32_16x16x32_bf16 v[44:47],v[164:167],v[52:55],v[44:47]
v_fma_f32 v39,-v116,v27,v39
v_mul_f32 v92,v36,v36
v_mfma_f32_16x16x32_bf16 v[48:51],v[168:171],v[52:55],v[48:51]
buffer_store_dwordx4 v[96:99],v3,s[36:39],s73 offen offset:0 sc0 sc1
v_mul_f32 v93,v37,v37
v_mul_f32 v94,v38,v38
v_mul_f32 v95,v39,v39
v_mfma_f32_16x16x32_bf16 v[40:43],v[172:175],v[56:59],v[40:43]
ds_read_b128 v[76:79],v2 offset:63488
v_fma_f32 v92,v118,v92,v117
v_mfma_f32_16x16x32_bf16 v[44:47],v[176:179],v[56:59],v[44:47]
v_mul_f32 v92,v36,v92
v_fma_f32 v93,v118,v93,v117
v_mfma_f32_16x16x32_bf16 v[48:51],v[180:183],v[56:59],v[48:51]
s_waitcnt lgkmcnt(3)
v_exp_f32 v92,v92
v_mfma_f32_16x16x32_bf16 v[40:43],v[184:187],v[60:63],v[40:43]
ds_read_b128 v[80:83],v2 offset:64512
v_mul_f32 v93,v37,v93
v_mfma_f32_16x16x32_bf16 v[44:47],v[188:191],v[60:63],v[44:47]
s_waitcnt vmcnt(6)
v_xor_b32 v2,65536,v2
v_fma_f32 v92,v92,v119,v119
v_mfma_f32_16x16x32_bf16 v[48:51],v[192:195],v[60:63],v[48:51]
s_barrier
v_mfma_f32_16x16x32_bf16 v[40:43],v[196:199],v[64:67],v[40:43]
ds_read_b128 v[52:55],v2 offset:0
ds_read_b32 v14,v8 offset:256
v_mfma_f32_16x16x32_bf16 v[44:47],v[200:203],v[64:67],v[44:47]
ds_read_b64 v[112:113],v12 offset:128
v_fma_f32 v94,v118,v94,v117
v_mfma_f32_16x16x32_bf16 v[48:51],v[204:207],v[64:67],v[48:51]
s_waitcnt lgkmcnt(5)
v_exp_f32 v93,v93
v_mfma_f32_16x16x32_bf16 v[40:43],v[208:211],v[68:71],v[40:43]
ds_read_b128 v[56:59],v2 offset:1024
v_mul_f32 v94,v38,v94
v_mfma_f32_16x16x32_bf16 v[44:47],v[212:215],v[68:71],v[44:47]
s_waitcnt lgkmcnt(2)
v_exp_f32 v94,v94
v_mfma_f32_16x16x32_bf16 v[48:51],v[216:219],v[68:71],v[48:51]
s_waitcnt lgkmcnt(1)
v_cmp_eq_u32 vcc,s60,v14
v_rcp_f32 v92,v92
v_fma_f32 v93,v93,v119,v119
v_mfma_f32_16x16x32_bf16 v[40:43],v[220:223],v[72:75],v[40:43]
s_cmp_eq_u32 vcc_lo,-1
ds_read_b128 v[60:63],v2 offset:2048
v_fma_f32 v94,v94,v119,v119
v_mfma_f32_16x16x32_bf16 v[44:47],v[224:227],v[72:75],v[44:47]
s_cselect_b32 s66,s86,s59
v_rcp_f32 v93,v93
v_mfma_f32_16x16x32_bf16 v[48:51],v[228:231],v[72:75],v[48:51]
v_rcp_f32 v94,v94
v_mfma_f32_16x16x32_bf16 v[40:43],v[232:235],v[76:79],v[40:43]
s_lshr_b32 s61,s66,0
v_fma_f32 v95,v118,v95,v117
v_mul_f32 v95,v39,v95
v_exp_f32 v95,v95
v_mfma_f32_16x16x32_bf16 v[44:47],v[236:239],v[76:79],v[44:47]
s_xor_b32 s64,s64,65536
v_fma_f32 v95,v95,v119,v119
v_rcp_f32 v95,v95
v_mfma_f32_16x16x32_bf16 v[48:51],v[240:243],v[76:79],v[48:51]
s_cmp_lt_u32 s67,s59
ds_read_b128 v[64:67],v2 offset:3072
v_mul_f32 v36,v36,v92
v_mul_f32 v37,v37,v93
v_mul_f32 v38,v38,v94
v_mul_f32 v39,v39,v95
v_cvt_pk_bf16_f32 v100,v36,v37
v_cvt_pk_bf16_f32 v101,v38,v39
v_mfma_f32_16x16x32_bf16 v[40:43],v[244:247],v[80:83],v[40:43]
buffer_store_dwordx2 v[100:101],v10,s[36:39],s73 offen offset:0 sc0 sc1
ds_read_b128 v[68:71],v2 offset:4096
v_mfma_f32_16x16x32_bf16 v[44:47],v[248:251],v[80:83],v[44:47]
s_waitcnt lgkmcnt(3)
v_mfma_f32_16x16x32_bf16 v[48:51],v[252:255],v[80:83],v[48:51]
s_cbranch_scc1 Lg3_d0w2_%=
s_branch Lg3_slow0_%=
Lg3_d0w3_%=:
v_mfma_f32_16x16x32_bf16 v[28:31],a[0:3],v[52:55],0
s_mov_b32 m0,s62
ds_read_b128 v[72:75],v2 offset:5120
v_mul_f32 v116,s75,v112
v_mfma_f32_16x16x32_bf16 v[32:35],a[4:7],v[52:55],0
s_mul_i32 s73,s47,7
v_fma_f32 v113,v113,s75,v13
v_fma_f32 v113,-v116,v116,v113
v_mfma_f32_16x16x32_bf16 v[36:39],a[8:11],v[52:55],0
s_lshr_b32 s73,s73,1
v_max_f32 v113,v113,v13
v_rsq_f32 v112,v113
v_mfma_f32_16x16x32_bf16 v[28:31],a[12:15],v[56:59],v[28:31]
s_add_u32 s53,s52,s55
ds_read_b128 v[76:79],v2 offset:6144
v_mul_f32 v117,s76,v112
v_mfma_f32_16x16x32_bf16 v[32:35],a[16:19],v[56:59],v[32:35]
buffer_load_dwordx4 v0,s[36:39],s61 offen offset:0 sc1 lds
v_mfma_f32_16x16x32_bf16 v[36:39],a[20:23],v[56:59],v[36:39]
s_waitcnt lgkmcnt(3)
v_mul_f32 v118,v112,v112
v_mul_f32 v119,v113,v112
v_mfma_f32_16x16x32_bf16 v[28:31],a[24:27],v[60:63],v[28:31]
s_cmp_eq_u32 s53,s56
ds_read_b128 v[80:83],v2 offset:7168
v_mul_f32 v118,s77,v118
v_mfma_f32_16x16x32_bf16 v[32:35],a[28:31],v[60:63],v[32:35]
s_cselect_b32 s53,s57,s53
v_mul_f32 v118,v118,v117
v_fma_f32 v40,-v116,v16,v40
v_mfma_f32_16x16x32_bf16 v[36:39],a[32:35],v[60:63],v[36:39]
buffer_load_dwordx4 v0,s[36:39],s61 offen offset:1024 sc1 lds
v_mfma_f32_16x16x32_bf16 v[28:31],a[36:39],v[64:67],v[28:31]
s_cmp_eq_u32 s53,s58
ds_read_b128 v[52:55],v2 offset:8192
v_fma_f32 v41,-v116,v17,v41
v_mfma_f32_16x16x32_bf16 v[32:35],a[40:43],v[64:67],v[32:35]
s_cselect_b32 s53,s59,s53
v_fma_f32 v42,-v116,v18,v42
v_fma_f32 v43,-v116,v19,v43
v_mfma_f32_16x16x32_bf16 v[36:39],a[44:47],v[64:67],v[36:39]
s_waitcnt lgkmcnt(3)
v_mul_f32 v84,v40,v40
v_mul_f32 v85,v41,v41
v_mfma_f32_16x16x32_bf16 v[28:31],a[48:51],v[68:71],v[28:31]
buffer_load_dwordx4 v0,s[36:39],s61 offen offset:2048 sc1 lds
v_mfma_f32_16x16x32_bf16 v[32:35],a[52:55],v[68:71],v[32:35]
s_cmp_ge_u32 s66,s59
ds_read_b128 v[56:59],v2 offset:9216
v_mul_f32 v86,v42,v42
v_mfma_f32_16x16x32_bf16 v[36:39],a[56:59],v[68:71],v[36:39]
s_cselect_b32 s86,s49,s50
v_mul_f32 v87,v43,v43
v_fma_f32 v84,v118,v84,v117
v_mfma_f32_16x16x32_bf16 v[28:31],a[60:63],v[72:75],v[28:31]
s_mul_i32 s70,s46,5
ds_read_b128 v[60:63],v2 offset:10240
v_mul_f32 v84,v40,v84
v_mfma_f32_16x16x32_bf16 v[32:35],a[64:67],v[72:75],v[32:35]
buffer_load_dwordx4 v0,s[36:39],s61 offen offset:3072 sc1 lds
v_mfma_f32_16x16x32_bf16 v[36:39],a[68:71],v[72:75],v[36:39]
s_waitcnt lgkmcnt(3)
v_exp_f32 v84,v84
v_mfma_f32_16x16x32_bf16 v[28:31],a[72:75],v[76:79],v[28:31]
s_lshr_b32 s70,s70,6
ds_read_b128 v[64:67],v2 offset:11264
v_fma_f32 v84,v84,v119,v119
v_mfma_f32_16x16x32_bf16 v[32:35],a[76:79],v[76:79],v[32:35]
s_add_u32 m0,s62,16384
v_rcp_f32 v84,v84
v_mfma_f32_16x16x32_bf16 v[36:39],a[80:83],v[76:79],v[36:39]
s_lshr_b32 s72,s61,5
v_fma_f32 v85,v118,v85,v117
v_mul_f32 v85,v41,v85
v_mfma_f32_16x16x32_bf16 v[28:31],a[84:87],v[80:83],v[28:31]
s_mov_b32 s62,s63
ds_read_b128 v[68:71],v2 offset:12288
v_fma_f32 v86,v118,v86,v117
v_mfma_f32_16x16x32_bf16 v[32:35],a[88:91],v[80:83],v[32:35]
v_exp_f32 v85,v85
v_mfma_f32_16x16x32_bf16 v[36:39],a[92:95],v[80:83],v[36:39]
s_waitcnt lgkmcnt(3)
v_mul_f32 v86,v42,v86
v_fma_f32 v85,v85,v119,v119
v_mfma_f32_16x16x32_bf16 v[28:31],a[96:99],v[52:55],v[28:31]
ds_read_b128 v[72:75],v2 offset:13312
v_fma_f32 v87,v118,v87,v117
v_mfma_f32_16x16x32_bf16 v[32:35],a[100:103],v[52:55],v[32:35]
v_exp_f32 v86,v86
v_mfma_f32_16x16x32_bf16 v[36:39],a[104:107],v[52:55],v[36:39]
v_rcp_f32 v85,v85
v_mfma_f32_16x16x32_bf16 v[28:31],a[108:111],v[56:59],v[28:31]
ds_read_b128 v[76:79],v2 offset:14336
v_mul_f32 v87,v43,v87
v_mfma_f32_16x16x32_bf16 v[32:35],a[112:115],v[56:59],v[32:35])"
R"(
v_exp_f32 v87,v87
v_mfma_f32_16x16x32_bf16 v[36:39],a[116:119],v[56:59],v[36:39]
s_waitcnt lgkmcnt(3)
v_fma_f32 v86,v86,v119,v119
v_fma_f32 v87,v87,v119,v119
v_mfma_f32_16x16x32_bf16 v[28:31],a[120:123],v[60:63],v[28:31]
ds_read_b128 v[80:83],v2 offset:15360
v_mul_f32 v40,v40,v84
v_mfma_f32_16x16x32_bf16 v[32:35],a[124:127],v[60:63],v[32:35]
s_waitcnt vmcnt(7)
v_rcp_f32 v86,v86
v_mfma_f32_16x16x32_bf16 v[36:39],a[128:131],v[60:63],v[36:39]
s_barrier
v_mfma_f32_16x16x32_bf16 v[28:31],a[132:135],v[64:67],v[28:31]
ds_read_b128 v[52:55],v2 offset:16384
v_add_f32_dpp v104,v104,v104 quad_perm:[1,0,3,2] row_mask:0xf bank_mask:0xf
v_mfma_f32_16x16x32_bf16 v[32:35],a[136:139],v[64:67],v[32:35]
v_rcp_f32 v87,v87
v_mfma_f32_16x16x32_bf16 v[36:39],a[140:143],v[64:67],v[36:39]
s_waitcnt lgkmcnt(3)
v_add_f32_dpp v104,v104,v104 quad_perm:[2,3,0,1] row_mask:0xf bank_mask:0xf
v_mul_f32 v41,v41,v85
v_mfma_f32_16x16x32_bf16 v[28:31],a[144:147],v[68:71],v[28:31]
ds_read_b128 v[56:59],v2 offset:17408
v_add_f32_dpp v104,v104,v104 row_half_mirror row_mask:0xf bank_mask:0xf
v_mfma_f32_16x16x32_bf16 v[32:35],a[148:151],v[68:71],v[32:35]
ds_write_b32 v11,v104 offset:0
v_mfma_f32_16x16x32_bf16 v[36:39],a[152:155],v[68:71],v[36:39]
v_mul_f32 v42,v42,v86
v_mul_f32 v43,v43,v87
v_mfma_f32_16x16x32_bf16 v[28:31],a[156:159],v[72:75],v[28:31]
ds_read_b128 v[60:63],v2 offset:18432
v_cvt_pk_bf16_f32 v96,v40,v41
v_mfma_f32_16x16x32_bf16 v[32:35],a[160:163],v[72:75],v[32:35]
v_cvt_pk_bf16_f32 v97,v42,v43
v_fma_f32 v44,-v116,v20,v44
v_mfma_f32_16x16x32_bf16 v[36:39],a[164:167],v[72:75],v[36:39]
s_waitcnt lgkmcnt(4)
v_fma_f32 v45,-v116,v21,v45
v_fma_f32 v46,-v116,v22,v46
v_mfma_f32_16x16x32_bf16 v[28:31],a[168:171],v[76:79],v[28:31]
ds_read_b128 v[64:67],v2 offset:19456
v_fma_f32 v47,-v116,v23,v47
v_mfma_f32_16x16x32_bf16 v[32:35],a[172:175],v[76:79],v[32:35]
v_mul_f32 v88,v44,v44
v_mul_f32 v89,v45,v45
v_mfma_f32_16x16x32_bf16 v[36:39],a[176:179],v[76:79],v[36:39]
v_mul_f32 v90,v46,v46
v_mul_f32 v91,v47,v47
v_mfma_f32_16x16x32_bf16 v[28:31],a[180:183],v[80:83],v[28:31]
ds_read_b128 v[68:71],v2 offset:20480
v_fma_f32 v88,v118,v88,v117
v_mfma_f32_16x16x32_bf16 v[32:35],a[184:187],v[80:83],v[32:35]
buffer_load_dwordx4 v1,s[36:39],s61 offen offset:0 sc1 lds
v_mfma_f32_16x16x32_bf16 v[36:39],a[188:191],v[80:83],v[36:39]
s_waitcnt lgkmcnt(4)
v_mul_f32 v88,v44,v88
v_fma_f32 v89,v118,v89,v117
v_mfma_f32_16x16x32_bf16 v[28:31],a[192:195],v[52:55],v[28:31]
ds_read_b128 v[72:75],v2 offset:21504
v_mul_f32 v89,v45,v89
v_mfma_f32_16x16x32_bf16 v[32:35],a[196:199],v[52:55],v[32:35]
v_exp_f32 v88,v88
v_mfma_f32_16x16x32_bf16 v[36:39],a[200:203],v[52:55],v[36:39]
buffer_load_dwordx4 v1,s[36:39],s61 offen offset:1024 sc1 lds
v_mfma_f32_16x16x32_bf16 v[28:31],a[204:207],v[56:59],v[28:31]
ds_read_b128 v[76:79],v2 offset:22528
v_fma_f32 v88,v88,v119,v119
v_mfma_f32_16x16x32_bf16 v[32:35],a[208:211],v[56:59],v[32:35]
v_exp_f32 v89,v89
v_mfma_f32_16x16x32_bf16 v[36:39],a[212:215],v[56:59],v[36:39]
s_waitcnt lgkmcnt(3)
v_rcp_f32 v88,v88
v_mfma_f32_16x16x32_bf16 v[28:31],a[216:219],v[60:63],v[28:31]
buffer_load_dwordx4 v1,s[36:39],s61 offen offset:2048 sc1 lds
v_mfma_f32_16x16x32_bf16 v[32:35],a[220:223],v[60:63],v[32:35]
ds_read_b128 v[80:83],v2 offset:23552
v_fma_f32 v89,v89,v119,v119
v_mfma_f32_16x16x32_bf16 v[36:39],a[224:227],v[60:63],v[36:39]
v_rcp_f32 v89,v89
v_mfma_f32_16x16x32_bf16 v[28:31],a[228:231],v[64:67],v[28:31]
ds_read_b128 v[52:55],v2 offset:24576
v_fma_f32 v90,v118,v90,v117
v_mfma_f32_16x16x32_bf16 v[32:35],a[232:235],v[64:67],v[32:35]
buffer_load_dwordx4 v1,s[36:39],s61 offen offset:3072 sc1 lds
v_mfma_f32_16x16x32_bf16 v[36:39],a[236:239],v[64:67],v[36:39]
s_waitcnt lgkmcnt(3)
buffer_load_dword v108,v9,s[36:39],s72 offen offset:0 sc1
v_mfma_f32_16x16x32_bf16 v[28:31],a[240:243],v[68:71],v[28:31]
ds_read_b128 v[56:59],v2 offset:25600
v_mul_f32 v90,v46,v90
v_mfma_f32_16x16x32_bf16 v[32:35],a[244:247],v[68:71],v[32:35]
v_exp_f32 v90,v90
v_mfma_f32_16x16x32_bf16 v[36:39],a[248:251],v[68:71],v[36:39]
v_fma_f32 v90,v90,v119,v119
v_fma_f32 v91,v118,v91,v117
v_mfma_f32_16x16x32_bf16 v[28:31],a[252:255],v[72:75],v[28:31]
ds_read_b128 v[60:63],v2 offset:26624
v_mul_f32 v91,v47,v91
v_mfma_f32_16x16x32_bf16 v[32:35],v[128:131],v[72:75],v[32:35]
v_exp_f32 v91,v91
v_mfma_f32_16x16x32_bf16 v[36:39],v[132:135],v[72:75],v[36:39]
s_waitcnt lgkmcnt(3)
v_rcp_f32 v90,v90
v_mfma_f32_16x16x32_bf16 v[28:31],v[136:139],v[76:79],v[28:31]
ds_read_b128 v[64:67],v2 offset:27648
v_fma_f32 v91,v91,v119,v119
v_mfma_f32_16x16x32_bf16 v[32:35],v[140:143],v[76:79],v[32:35]
v_rcp_f32 v91,v91
v_mfma_f32_16x16x32_bf16 v[36:39],v[144:147],v[76:79],v[36:39]
v_mul_f32 v44,v44,v88
v_mul_f32 v45,v45,v89
v_mfma_f32_16x16x32_bf16 v[28:31],v[148:151],v[80:83],v[28:31]
ds_read_b128 v[68:71],v2 offset:28672
v_mul_f32 v46,v46,v90
v_mfma_f32_16x16x32_bf16 v[32:35],v[152:155],v[80:83],v[32:35]
v_mul_f32 v47,v47,v91
v_cvt_pk_bf16_f32 v98,v44,v45
v_mfma_f32_16x16x32_bf16 v[36:39],v[156:159],v[80:83],v[36:39]
s_waitcnt lgkmcnt(3)
v_cvt_pk_bf16_f32 v99,v46,v47
v_fma_f32 v48,-v116,v24,v48
v_mfma_f32_16x16x32_bf16 v[28:31],v[160:163],v[52:55],v[28:31]
ds_read_b128 v[72:75],v2 offset:29696
v_fma_f32 v49,-v116,v25,v49
v_mfma_f32_16x16x32_bf16 v[32:35],v[164:167],v[52:55],v[32:35]
v_fma_f32 v50,-v116,v26,v50
v_fma_f32 v51,-v116,v27,v51
v_mfma_f32_16x16x32_bf16 v[36:39],v[168:171],v[52:55],v[36:39]
v_mul_f32 v92,v48,v48
v_mul_f32 v93,v49,v49
v_mfma_f32_16x16x32_bf16 v[28:31],v[172:175],v[56:59],v[28:31]
buffer_store_dwordx4 v[96:99],v3,s[36:39],s73 offen offset:0 sc0 sc1
ds_read_b128 v[76:79],v2 offset:30720
v_mul_f32 v94,v50,v50
v_mul_f32 v95,v51,v51
v_mfma_f32_16x16x32_bf16 v[32:35],v[176:179],v[56:59],v[32:35]
v_fma_f32 v92,v118,v92,v117
v_mul_f32 v92,v48,v92
v_mfma_f32_16x16x32_bf16 v[36:39],v[180:183],v[56:59],v[36:39]
s_waitcnt lgkmcnt(3)
v_exp_f32 v92,v92
v_mfma_f32_16x16x32_bf16 v[28:31],v[184:187],v[60:63],v[28:31]
ds_read_b128 v[80:83],v2 offset:31744
v_fma_f32 v92,v92,v119,v119
v_mfma_f32_16x16x32_bf16 v[32:35],v[188:191],v[60:63],v[32:35]
s_waitcnt vmcnt(6)
v_rcp_f32 v92,v92
v_mfma_f32_16x16x32_bf16 v[36:39],v[192:195],v[60:63],v[36:39]
s_barrier
v_mfma_f32_16x16x32_bf16 v[28:31],v[196:199],v[64:67],v[28:31]
buffer_store_dword v5,v4,s[36:39],s70 offen offset:0 sc0 sc1
ds_read_b128 v[52:55],v2 offset:32768
ds_read_b32 v14,v8 offset:0
ds_read_b64 v[112:113],v12 offset:0
v_mfma_f32_16x16x32_bf16 v[32:35],v[200:203],v[64:67],v[32:35]
v_fma_f32 v93,v118,v93,v117
v_mul_f32 v93,v49,v93
v_mfma_f32_16x16x32_bf16 v[36:39],v[204:207],v[64:67],v[36:39]
s_waitcnt lgkmcnt(5)
v_exp_f32 v93,v93
v_mfma_f32_16x16x32_bf16 v[28:31],v[208:211],v[68:71],v[28:31]
ds_read_b128 v[56:59],v2 offset:33792
v_fma_f32 v93,v93,v119,v119
v_mfma_f32_16x16x32_bf16 v[32:35],v[212:215],v[68:71],v[32:35]
s_waitcnt lgkmcnt(2)
v_rcp_f32 v93,v93
v_mfma_f32_16x16x32_bf16 v[36:39],v[216:219],v[68:71],v[36:39]
s_waitcnt lgkmcnt(1)
v_cmp_eq_u32 vcc,s60,v14
v_fma_f32 v94,v118,v94,v117
v_mul_f32 v94,v50,v94
v_fma_f32 v95,v118,v95,v117
v_mfma_f32_16x16x32_bf16 v[28:31],v[220:223],v[72:75],v[28:31]
s_cmp_eq_u32 vcc_lo,-1
ds_read_b128 v[60:63],v2 offset:34816
v_mul_f32 v95,v51,v95
v_mfma_f32_16x16x32_bf16 v[32:35],v[224:227],v[72:75],v[32:35]
s_cselect_b32 s67,s86,s59
v_exp_f32 v94,v94
v_mfma_f32_16x16x32_bf16 v[36:39],v[228:231],v[72:75],v[36:39]
v_exp_f32 v95,v95
v_mfma_f32_16x16x32_bf16 v[28:31],v[232:235],v[76:79],v[28:31]
s_lshr_b32 s61,s67,0
ds_read_b128 v[64:67],v2 offset:35840
v_fma_f32 v94,v94,v119,v119
v_mfma_f32_16x16x32_bf16 v[32:35],v[236:239],v[76:79],v[32:35]
s_xor_b32 s63,s63,65536
v_rcp_f32 v94,v94
v_mfma_f32_16x16x32_bf16 v[36:39],v[240:243],v[76:79],v[36:39]
s_cmp_ge_u32 s66,s59
v_fma_f32 v95,v95,v119,v119
v_rcp_f32 v95,v95
v_mfma_f32_16x16x32_bf16 v[28:31],v[244:247],v[80:83],v[28:31]
v_mul_f32 v48,v48,v92
v_mul_f32 v49,v49,v93
v_mul_f32 v50,v50,v94
v_mul_f32 v51,v51,v95
v_cvt_pk_bf16_f32 v100,v48,v49
v_cvt_pk_bf16_f32 v101,v50,v51)"
R"(
v_mfma_f32_16x16x32_bf16 v[32:35],v[248:251],v[80:83],v[32:35]
s_waitcnt lgkmcnt(2)
ds_read_b128 v[68:71],v2 offset:36864
buffer_store_dwordx2 v[100:101],v10,s[36:39],s73 offen offset:0 sc0 sc1
v_mfma_f32_16x16x32_bf16 v[36:39],v[252:255],v[80:83],v[36:39]
s_cbranch_scc1 Lg3_slow1_%=
Lg3_d1w3_%=:
v_mfma_f32_16x16x32_bf16 v[40:43],a[0:3],v[52:55],0
s_mov_b32 m0,s62
ds_read_b128 v[72:75],v2 offset:37888
v_mul_f32 v116,s75,v112
v_mfma_f32_16x16x32_bf16 v[44:47],a[4:7],v[52:55],0
s_mul_i32 s73,s48,7
v_fma_f32 v113,v113,s75,v13
v_fma_f32 v113,-v116,v116,v113
v_mfma_f32_16x16x32_bf16 v[48:51],a[8:11],v[52:55],0
s_lshr_b32 s73,s73,1
v_max_f32 v113,v113,v13
v_rsq_f32 v112,v113
v_mfma_f32_16x16x32_bf16 v[40:43],a[12:15],v[56:59],v[40:43]
s_add_u32 s74,s53,s55
ds_read_b128 v[76:79],v2 offset:38912
v_mul_f32 v117,s76,v112
v_mfma_f32_16x16x32_bf16 v[44:47],a[16:19],v[56:59],v[44:47]
buffer_load_dwordx4 v0,s[36:39],s61 offen offset:0 sc1 lds
v_mfma_f32_16x16x32_bf16 v[48:51],a[20:23],v[56:59],v[48:51]
s_waitcnt lgkmcnt(3)
v_mul_f32 v118,v112,v112
v_mul_f32 v119,v113,v112
v_mfma_f32_16x16x32_bf16 v[40:43],a[24:27],v[60:63],v[40:43]
s_cmp_eq_u32 s74,s56
ds_read_b128 v[80:83],v2 offset:39936
v_mul_f32 v118,s77,v118
v_mfma_f32_16x16x32_bf16 v[44:47],a[28:31],v[60:63],v[44:47]
s_cselect_b32 s74,s57,s74
v_mul_f32 v118,v118,v117
v_fma_f32 v28,-v116,v16,v28
v_mfma_f32_16x16x32_bf16 v[48:51],a[32:35],v[60:63],v[48:51]
buffer_load_dwordx4 v0,s[36:39],s61 offen offset:1024 sc1 lds
v_mfma_f32_16x16x32_bf16 v[40:43],a[36:39],v[64:67],v[40:43]
s_cmp_eq_u32 s74,s58
ds_read_b128 v[52:55],v2 offset:40960
v_fma_f32 v29,-v116,v17,v29
v_mfma_f32_16x16x32_bf16 v[44:47],a[40:43],v[64:67],v[44:47]
s_cselect_b32 s74,s59,s74
v_fma_f32 v30,-v116,v18,v30
v_fma_f32 v31,-v116,v19,v31
v_mfma_f32_16x16x32_bf16 v[48:51],a[44:47],v[64:67],v[48:51]
s_waitcnt lgkmcnt(3)
v_mul_f32 v84,v28,v28
v_mul_f32 v85,v29,v29
v_mfma_f32_16x16x32_bf16 v[40:43],a[48:51],v[68:71],v[40:43]
buffer_load_dwordx4 v0,s[36:39],s61 offen offset:2048 sc1 lds
v_mfma_f32_16x16x32_bf16 v[44:47],a[52:55],v[68:71],v[44:47]
s_cmp_ge_u32 s67,s59
ds_read_b128 v[56:59],v2 offset:41984
v_mul_f32 v86,v30,v30
v_mfma_f32_16x16x32_bf16 v[48:51],a[56:59],v[68:71],v[48:51]
s_cselect_b32 s86,s50,s51
v_mul_f32 v87,v31,v31
v_fma_f32 v84,v118,v84,v117
v_mfma_f32_16x16x32_bf16 v[40:43],a[60:63],v[72:75],v[40:43]
s_mul_i32 s70,s47,5
ds_read_b128 v[60:63],v2 offset:43008
v_mul_f32 v84,v28,v84
v_mfma_f32_16x16x32_bf16 v[44:47],a[64:67],v[72:75],v[44:47]
buffer_load_dwordx4 v0,s[36:39],s61 offen offset:3072 sc1 lds
v_mfma_f32_16x16x32_bf16 v[48:51],a[68:71],v[72:75],v[48:51]
s_waitcnt lgkmcnt(3)
v_exp_f32 v84,v84
v_mfma_f32_16x16x32_bf16 v[40:43],a[72:75],v[76:79],v[40:43]
s_lshr_b32 s70,s70,6
ds_read_b128 v[64:67],v2 offset:44032
v_fma_f32 v84,v84,v119,v119
v_mfma_f32_16x16x32_bf16 v[44:47],a[76:79],v[76:79],v[44:47]
s_add_u32 m0,s62,16384
v_rcp_f32 v84,v84
v_mfma_f32_16x16x32_bf16 v[48:51],a[80:83],v[76:79],v[48:51]
s_lshr_b32 s72,s61,5
v_fma_f32 v85,v118,v85,v117
v_mul_f32 v85,v29,v85
v_mfma_f32_16x16x32_bf16 v[40:43],a[84:87],v[80:83],v[40:43]
s_mov_b32 s62,s64
ds_read_b128 v[68:71],v2 offset:45056
v_fma_f32 v86,v118,v86,v117
v_mfma_f32_16x16x32_bf16 v[44:47],a[88:91],v[80:83],v[44:47]
s_mov_b64 s[44:45],s[46:47]
v_exp_f32 v85,v85
v_mfma_f32_16x16x32_bf16 v[48:51],a[92:95],v[80:83],v[48:51]
s_waitcnt lgkmcnt(3)
v_mul_f32 v86,v30,v86
v_fma_f32 v85,v85,v119,v119
v_mfma_f32_16x16x32_bf16 v[40:43],a[96:99],v[52:55],v[40:43]
s_mov_b64 s[46:47],s[48:49]
ds_read_b128 v[72:75],v2 offset:46080
v_fma_f32 v87,v118,v87,v117
v_mfma_f32_16x16x32_bf16 v[44:47],a[100:103],v[52:55],v[44:47]
s_mov_b64 s[48:49],s[50:51]
v_exp_f32 v86,v86
v_mfma_f32_16x16x32_bf16 v[48:51],a[104:107],v[52:55],v[48:51]
s_mov_b64 s[50:51],s[52:53]
v_rcp_f32 v85,v85
v_mfma_f32_16x16x32_bf16 v[40:43],a[108:111],v[56:59],v[40:43]
s_mov_b32 s52,s74
ds_read_b128 v[76:79],v2 offset:47104
v_mul_f32 v87,v31,v87
v_mfma_f32_16x16x32_bf16 v[44:47],a[112:115],v[56:59],v[44:47]
v_exp_f32 v87,v87
v_mfma_f32_16x16x32_bf16 v[48:51],a[116:119],v[56:59],v[48:51]
s_waitcnt lgkmcnt(3)
v_fma_f32 v86,v86,v119,v119
v_fma_f32 v87,v87,v119,v119
v_mfma_f32_16x16x32_bf16 v[40:43],a[120:123],v[60:63],v[40:43]
ds_read_b128 v[80:83],v2 offset:48128
v_mul_f32 v28,v28,v84
v_mfma_f32_16x16x32_bf16 v[44:47],a[124:127],v[60:63],v[44:47]
s_waitcnt vmcnt(7)
v_rcp_f32 v86,v86
v_mfma_f32_16x16x32_bf16 v[48:51],a[128:131],v[60:63],v[48:51]
s_barrier
v_mfma_f32_16x16x32_bf16 v[40:43],a[132:135],v[64:67],v[40:43]
ds_read_b128 v[52:55],v2 offset:49152
v_add_f32_dpp v108,v108,v108 quad_perm:[1,0,3,2] row_mask:0xf bank_mask:0xf
v_mfma_f32_16x16x32_bf16 v[44:47],a[136:139],v[64:67],v[44:47]
v_rcp_f32 v87,v87
v_mfma_f32_16x16x32_bf16 v[48:51],a[140:143],v[64:67],v[48:51]
s_waitcnt lgkmcnt(3)
v_add_f32_dpp v108,v108,v108 quad_perm:[2,3,0,1] row_mask:0xf bank_mask:0xf
v_mul_f32 v29,v29,v85
v_mfma_f32_16x16x32_bf16 v[40:43],a[144:147],v[68:71],v[40:43]
ds_read_b128 v[56:59],v2 offset:50176
v_add_f32_dpp v108,v108,v108 row_half_mirror row_mask:0xf bank_mask:0xf
v_mfma_f32_16x16x32_bf16 v[44:47],a[148:151],v[68:71],v[44:47]
ds_write_b32 v11,v108 offset:128
v_mfma_f32_16x16x32_bf16 v[48:51],a[152:155],v[68:71],v[48:51]
v_mul_f32 v30,v30,v86
v_mul_f32 v31,v31,v87
v_mfma_f32_16x16x32_bf16 v[40:43],a[156:159],v[72:75],v[40:43]
ds_read_b128 v[60:63],v2 offset:51200
v_cvt_pk_bf16_f32 v96,v28,v29
v_mfma_f32_16x16x32_bf16 v[44:47],a[160:163],v[72:75],v[44:47]
v_cvt_pk_bf16_f32 v97,v30,v31
v_fma_f32 v32,-v116,v20,v32
v_mfma_f32_16x16x32_bf16 v[48:51],a[164:167],v[72:75],v[48:51]
s_waitcnt lgkmcnt(4)
v_fma_f32 v33,-v116,v21,v33
v_fma_f32 v34,-v116,v22,v34
v_mfma_f32_16x16x32_bf16 v[40:43],a[168:171],v[76:79],v[40:43]
ds_read_b128 v[64:67],v2 offset:52224
v_fma_f32 v35,-v116,v23,v35
v_mfma_f32_16x16x32_bf16 v[44:47],a[172:175],v[76:79],v[44:47]
v_mul_f32 v88,v32,v32
v_mul_f32 v89,v33,v33
v_mfma_f32_16x16x32_bf16 v[48:51],a[176:179],v[76:79],v[48:51]
v_mul_f32 v90,v34,v34
v_mul_f32 v91,v35,v35
v_mfma_f32_16x16x32_bf16 v[40:43],a[180:183],v[80:83],v[40:43]
ds_read_b128 v[68:71],v2 offset:53248
v_fma_f32 v88,v118,v88,v117
v_mfma_f32_16x16x32_bf16 v[44:47],a[184:187],v[80:83],v[44:47]
buffer_load_dwordx4 v1,s[36:39],s61 offen offset:0 sc1 lds
v_mfma_f32_16x16x32_bf16 v[48:51],a[188:191],v[80:83],v[48:51]
s_waitcnt lgkmcnt(4)
v_mul_f32 v88,v32,v88
v_fma_f32 v89,v118,v89,v117
v_mfma_f32_16x16x32_bf16 v[40:43],a[192:195],v[52:55],v[40:43]
ds_read_b128 v[72:75],v2 offset:54272
v_mul_f32 v89,v33,v89
v_mfma_f32_16x16x32_bf16 v[44:47],a[196:199],v[52:55],v[44:47]
v_exp_f32 v88,v88
v_mfma_f32_16x16x32_bf16 v[48:51],a[200:203],v[52:55],v[48:51]
buffer_load_dwordx4 v1,s[36:39],s61 offen offset:1024 sc1 lds
v_mfma_f32_16x16x32_bf16 v[40:43],a[204:207],v[56:59],v[40:43]
ds_read_b128 v[76:79],v2 offset:55296
v_fma_f32 v88,v88,v119,v119
v_mfma_f32_16x16x32_bf16 v[44:47],a[208:211],v[56:59],v[44:47]
v_exp_f32 v89,v89
v_mfma_f32_16x16x32_bf16 v[48:51],a[212:215],v[56:59],v[48:51]
s_waitcnt lgkmcnt(3)
v_rcp_f32 v88,v88
v_mfma_f32_16x16x32_bf16 v[40:43],a[216:219],v[60:63],v[40:43]
buffer_load_dwordx4 v1,s[36:39],s61 offen offset:2048 sc1 lds
v_mfma_f32_16x16x32_bf16 v[44:47],a[220:223],v[60:63],v[44:47]
ds_read_b128 v[80:83],v2 offset:56320
v_fma_f32 v89,v89,v119,v119
v_mfma_f32_16x16x32_bf16 v[48:51],a[224:227],v[60:63],v[48:51]
v_rcp_f32 v89,v89
v_mfma_f32_16x16x32_bf16 v[40:43],a[228:231],v[64:67],v[40:43]
ds_read_b128 v[52:55],v2 offset:57344
v_fma_f32 v90,v118,v90,v117
v_mfma_f32_16x16x32_bf16 v[44:47],a[232:235],v[64:67],v[44:47]
buffer_load_dwordx4 v1,s[36:39],s61 offen offset:3072 sc1 lds
v_mfma_f32_16x16x32_bf16 v[48:51],a[236:239],v[64:67],v[48:51]
s_waitcnt lgkmcnt(3)
buffer_load_dword v104,v9,s[36:39],s72 offen offset:0 sc1
v_mfma_f32_16x16x32_bf16 v[40:43],a[240:243],v[68:71],v[40:43]
ds_read_b128 v[56:59],v2 offset:58368
v_mul_f32 v90,v34,v90
v_mfma_f32_16x16x32_bf16 v[44:47],a[244:247],v[68:71],v[44:47]
v_exp_f32 v90,v90)"
R"(
v_mfma_f32_16x16x32_bf16 v[48:51],a[248:251],v[68:71],v[48:51]
v_fma_f32 v90,v90,v119,v119
v_fma_f32 v91,v118,v91,v117
v_mfma_f32_16x16x32_bf16 v[40:43],a[252:255],v[72:75],v[40:43]
ds_read_b128 v[60:63],v2 offset:59392
v_mul_f32 v91,v35,v91
v_mfma_f32_16x16x32_bf16 v[44:47],v[128:131],v[72:75],v[44:47]
v_exp_f32 v91,v91
v_mfma_f32_16x16x32_bf16 v[48:51],v[132:135],v[72:75],v[48:51]
s_waitcnt lgkmcnt(3)
v_rcp_f32 v90,v90
v_mfma_f32_16x16x32_bf16 v[40:43],v[136:139],v[76:79],v[40:43]
ds_read_b128 v[64:67],v2 offset:60416
v_fma_f32 v91,v91,v119,v119
v_mfma_f32_16x16x32_bf16 v[44:47],v[140:143],v[76:79],v[44:47]
v_rcp_f32 v91,v91
v_mfma_f32_16x16x32_bf16 v[48:51],v[144:147],v[76:79],v[48:51]
v_mul_f32 v32,v32,v88
v_mul_f32 v33,v33,v89
v_mfma_f32_16x16x32_bf16 v[40:43],v[148:151],v[80:83],v[40:43]
ds_read_b128 v[68:71],v2 offset:61440
v_mul_f32 v34,v34,v90
v_mfma_f32_16x16x32_bf16 v[44:47],v[152:155],v[80:83],v[44:47]
v_mul_f32 v35,v35,v91
v_cvt_pk_bf16_f32 v98,v32,v33
v_mfma_f32_16x16x32_bf16 v[48:51],v[156:159],v[80:83],v[48:51]
s_waitcnt lgkmcnt(3)
v_cvt_pk_bf16_f32 v99,v34,v35
v_fma_f32 v36,-v116,v24,v36
v_mfma_f32_16x16x32_bf16 v[40:43],v[160:163],v[52:55],v[40:43]
ds_read_b128 v[72:75],v2 offset:62464
v_fma_f32 v37,-v116,v25,v37
v_mfma_f32_16x16x32_bf16 v[44:47],v[164:167],v[52:55],v[44:47]
v_fma_f32 v38,-v116,v26,v38
v_fma_f32 v39,-v116,v27,v39
v_mfma_f32_16x16x32_bf16 v[48:51],v[168:171],v[52:55],v[48:51]
v_mul_f32 v92,v36,v36
v_mul_f32 v93,v37,v37
v_mfma_f32_16x16x32_bf16 v[40:43],v[172:175],v[56:59],v[40:43]
buffer_store_dwordx4 v[96:99],v3,s[36:39],s73 offen offset:0 sc0 sc1
ds_read_b128 v[76:79],v2 offset:63488
v_mul_f32 v94,v38,v38
v_mul_f32 v95,v39,v39
v_mfma_f32_16x16x32_bf16 v[44:47],v[176:179],v[56:59],v[44:47]
v_fma_f32 v92,v118,v92,v117
v_mul_f32 v92,v36,v92
v_mfma_f32_16x16x32_bf16 v[48:51],v[180:183],v[56:59],v[48:51]
s_waitcnt lgkmcnt(3)
v_exp_f32 v92,v92
v_mfma_f32_16x16x32_bf16 v[40:43],v[184:187],v[60:63],v[40:43]
ds_read_b128 v[80:83],v2 offset:64512
v_fma_f32 v92,v92,v119,v119
v_mfma_f32_16x16x32_bf16 v[44:47],v[188:191],v[60:63],v[44:47]
s_waitcnt vmcnt(6)
v_xor_b32 v2,65536,v2
v_fma_f32 v93,v118,v93,v117
v_mfma_f32_16x16x32_bf16 v[48:51],v[192:195],v[60:63],v[48:51]
s_barrier
v_mfma_f32_16x16x32_bf16 v[40:43],v[196:199],v[64:67],v[40:43]
buffer_store_dword v5,v4,s[36:39],s70 offen offset:0 sc0 sc1
ds_read_b128 v[52:55],v2 offset:0
ds_read_b32 v14,v8 offset:256
ds_read_b64 v[112:113],v12 offset:128
v_mfma_f32_16x16x32_bf16 v[44:47],v[200:203],v[64:67],v[44:47]
v_rcp_f32 v92,v92
v_mfma_f32_16x16x32_bf16 v[48:51],v[204:207],v[64:67],v[48:51]
s_waitcnt lgkmcnt(5)
v_mul_f32 v93,v37,v93
v_fma_f32 v94,v118,v94,v117
v_mfma_f32_16x16x32_bf16 v[40:43],v[208:211],v[68:71],v[40:43]
ds_read_b128 v[56:59],v2 offset:1024
v_mul_f32 v94,v38,v94
v_mfma_f32_16x16x32_bf16 v[44:47],v[212:215],v[68:71],v[44:47]
s_waitcnt lgkmcnt(2)
v_exp_f32 v93,v93
v_mfma_f32_16x16x32_bf16 v[48:51],v[216:219],v[68:71],v[48:51]
s_waitcnt lgkmcnt(1)
v_cmp_eq_u32 vcc,s60,v14
v_exp_f32 v94,v94
v_fma_f32 v93,v93,v119,v119
v_mfma_f32_16x16x32_bf16 v[40:43],v[220:223],v[72:75],v[40:43]
s_cmp_eq_u32 vcc_lo,-1
ds_read_b128 v[60:63],v2 offset:2048
v_fma_f32 v94,v94,v119,v119
v_mfma_f32_16x16x32_bf16 v[44:47],v[224:227],v[72:75],v[44:47]
s_cselect_b32 s66,s86,s59
v_rcp_f32 v93,v93
v_mfma_f32_16x16x32_bf16 v[48:51],v[228:231],v[72:75],v[48:51]
v_rcp_f32 v94,v94
v_mfma_f32_16x16x32_bf16 v[40:43],v[232:235],v[76:79],v[40:43]
s_lshr_b32 s61,s66,0
ds_read_b128 v[64:67],v2 offset:3072
v_fma_f32 v95,v118,v95,v117
v_mfma_f32_16x16x32_bf16 v[44:47],v[236:239],v[76:79],v[44:47]
s_xor_b32 s64,s64,65536
v_mul_f32 v95,v39,v95
v_exp_f32 v95,v95
v_mfma_f32_16x16x32_bf16 v[48:51],v[240:243],v[76:79],v[48:51]
s_cmp_lt_u32 s67,s59
v_fma_f32 v95,v95,v119,v119
v_rcp_f32 v95,v95
v_mfma_f32_16x16x32_bf16 v[40:43],v[244:247],v[80:83],v[40:43]
v_mul_f32 v36,v36,v92
v_mul_f32 v37,v37,v93
v_mul_f32 v38,v38,v94
v_mul_f32 v39,v39,v95
v_cvt_pk_bf16_f32 v100,v36,v37
v_cvt_pk_bf16_f32 v101,v38,v39
v_mfma_f32_16x16x32_bf16 v[44:47],v[248:251],v[80:83],v[44:47]
s_waitcnt lgkmcnt(2)
ds_read_b128 v[68:71],v2 offset:4096
buffer_store_dwordx2 v[100:101],v10,s[36:39],s73 offen offset:0 sc0 sc1
v_mfma_f32_16x16x32_bf16 v[48:51],v[252:255],v[80:83],v[48:51]
s_cbranch_scc1 Lg3_d0w3_%=
s_branch Lg3_slow0_%=
Lg3_slow0_%=:
s_xor_b32 s62,s63,65536
s_lshr_b32 s61,s48,0
s_cmp_ge_u32 s66,s59
s_cbranch_scc1 Lg3_se0_%=
s_mov_b32 m0,s62
s_nop 0
buffer_load_dwordx4 v0,s[36:39],s61 offen offset:0 sc1 lds
buffer_load_dwordx4 v0,s[36:39],s61 offen offset:1024 sc1 lds
buffer_load_dwordx4 v0,s[36:39],s61 offen offset:2048 sc1 lds
buffer_load_dwordx4 v0,s[36:39],s61 offen offset:3072 sc1 lds
s_add_u32 m0,s62,16384
s_lshr_b32 s72,s61,5
buffer_load_dwordx4 v1,s[36:39],s61 offen offset:0 sc1 lds
buffer_load_dwordx4 v1,s[36:39],s61 offen offset:1024 sc1 lds
buffer_load_dwordx4 v1,s[36:39],s61 offen offset:2048 sc1 lds
buffer_load_dwordx4 v1,s[36:39],s61 offen offset:3072 sc1 lds
buffer_load_dword v104,v9,s[36:39],s72 offen offset:0 sc1
Lg3_se0_%=:
s_cmp_eq_u32 s54,1
s_cselect_b32 s68,s49,s48
s_lshr_b32 s68,s68,5
s_add_u32 m0,s78,8192
s_nop 0
buffer_load_dword v7,s[36:39],s68 offen offset:0 sc1 lds
s_mul_i32 s73,s47,7
s_lshr_b32 s73,s73,1
v_mul_f32 v116,s75,v112
v_fma_f32 v113,v113,s75,v13
v_fma_f32 v113,-v116,v116,v113
v_max_f32 v113,v113,v13
v_rsq_f32 v112,v113
s_nop 0
v_mul_f32 v117,s76,v112
v_mul_f32 v118,v112,v112
v_mul_f32 v119,v113,v112
v_mul_f32 v118,s77,v118
v_mul_f32 v118,v118,v117
v_fma_f32 v40,-v116,v16,v40
v_fma_f32 v41,-v116,v17,v41
v_fma_f32 v42,-v116,v18,v42
v_fma_f32 v43,-v116,v19,v43
v_mul_f32 v84,v40,v40
v_mul_f32 v85,v41,v41
v_mul_f32 v86,v42,v42
v_mul_f32 v87,v43,v43
v_fma_f32 v84,v118,v84,v117
v_fma_f32 v85,v118,v85,v117
v_fma_f32 v86,v118,v86,v117
v_fma_f32 v87,v118,v87,v117
v_mul_f32 v84,v40,v84
v_mul_f32 v85,v41,v85
v_mul_f32 v86,v42,v86
v_mul_f32 v87,v43,v87
v_exp_f32 v84,v84
v_exp_f32 v85,v85
v_exp_f32 v86,v86
v_exp_f32 v87,v87
v_fma_f32 v84,v84,v119,v119
v_fma_f32 v85,v85,v119,v119
v_fma_f32 v86,v86,v119,v119
v_fma_f32 v87,v87,v119,v119
v_rcp_f32 v84,v84
v_rcp_f32 v85,v85
v_rcp_f32 v86,v86
v_rcp_f32 v87,v87
v_mul_f32 v40,v40,v84
v_mul_f32 v41,v41,v85
v_mul_f32 v42,v42,v86
v_mul_f32 v43,v43,v87
v_cvt_pk_bf16_f32 v96,v40,v41
v_cvt_pk_bf16_f32 v97,v42,v43
v_fma_f32 v44,-v116,v20,v44
v_fma_f32 v45,-v116,v21,v45
v_fma_f32 v46,-v116,v22,v46
v_fma_f32 v47,-v116,v23,v47
v_mul_f32 v88,v44,v44
v_mul_f32 v89,v45,v45
v_mul_f32 v90,v46,v46
v_mul_f32 v91,v47,v47
v_fma_f32 v88,v118,v88,v117
v_fma_f32 v89,v118,v89,v117
v_fma_f32 v90,v118,v90,v117
v_fma_f32 v91,v118,v91,v117
v_mul_f32 v88,v44,v88)"
R"(
s_add_u32 m0,s78,8448
s_nop 0
buffer_load_dword v7,s[36:39],s68 offen offset:0 sc1 lds
v_mul_f32 v89,v45,v89
v_mul_f32 v90,v46,v90
v_mul_f32 v91,v47,v91
v_exp_f32 v88,v88
v_exp_f32 v89,v89
v_exp_f32 v90,v90
v_exp_f32 v91,v91
v_fma_f32 v88,v88,v119,v119
v_fma_f32 v89,v89,v119,v119
v_fma_f32 v90,v90,v119,v119
v_fma_f32 v91,v91,v119,v119
v_rcp_f32 v88,v88
v_rcp_f32 v89,v89
v_rcp_f32 v90,v90
v_rcp_f32 v91,v91
v_mul_f32 v44,v44,v88
v_mul_f32 v45,v45,v89
v_mul_f32 v46,v46,v90
v_mul_f32 v47,v47,v91
v_cvt_pk_bf16_f32 v98,v44,v45
v_cvt_pk_bf16_f32 v99,v46,v47
buffer_store_dwordx4 v[96:99],v3,s[36:39],s73 offen offset:0 sc0 sc1
v_fma_f32 v48,-v116,v24,v48
v_fma_f32 v49,-v116,v25,v49
v_fma_f32 v50,-v116,v26,v50
v_fma_f32 v51,-v116,v27,v51
v_mul_f32 v92,v48,v48
v_mul_f32 v93,v49,v49
v_mul_f32 v94,v50,v50
v_mul_f32 v95,v51,v51
v_fma_f32 v92,v118,v92,v117
v_fma_f32 v93,v118,v93,v117
v_fma_f32 v94,v118,v94,v117
v_fma_f32 v95,v118,v95,v117
v_mul_f32 v92,v48,v92
v_mul_f32 v93,v49,v93
v_mul_f32 v94,v50,v94
v_mul_f32 v95,v51,v95
v_exp_f32 v92,v92
v_exp_f32 v93,v93
v_exp_f32 v94,v94
v_exp_f32 v95,v95
v_fma_f32 v92,v92,v119,v119
v_fma_f32 v93,v93,v119,v119
v_fma_f32 v94,v94,v119,v119
v_fma_f32 v95,v95,v119,v119
v_rcp_f32 v92,v92
v_rcp_f32 v93,v93
v_rcp_f32 v94,v94
v_rcp_f32 v95,v95
v_mul_f32 v48,v48,v92
v_mul_f32 v49,v49,v93
v_mul_f32 v50,v50,v94
v_mul_f32 v51,v51,v95
v_cvt_pk_bf16_f32 v100,v48,v49
v_cvt_pk_bf16_f32 v101,v50,v51
buffer_store_dwordx2 v[100:101],v10,s[36:39],s73 offen offset:0 sc0 sc1
s_cmp_ge_u32 s48,s59
s_cbranch_scc1 Lg3_done0_%=
s_add_u32 m0,s78,16384
s_nop 0
buffer_load_dword v7,s[36:39],s68 offen offset:0 sc1 lds
s_waitcnt vmcnt(4)
s_barrier
ds_read_b32 v14,v8 offset:8192
s_mul_i32 s70,s46,5
s_lshr_b32 s70,s70,6
buffer_store_dword v5,v4,s[36:39],s70 offen offset:0 sc0 sc1
s_mov_b32 s46,s59
s_mov_b32 s69,18432
s_waitcnt lgkmcnt(0)
v_cmp_eq_u32 vcc,s60,v14
s_nop 0
s_cmp_eq_u32 vcc_lo,-1
s_cbranch_scc1 Lg3_go0_%=
s_add_u32 m0,s78,16640
s_nop 0
buffer_load_dword v7,s[36:39],s68 offen offset:0 sc1 lds
s_waitcnt vmcnt(5)
s_barrier
ds_read_b32 v14,v8 offset:8448
s_mov_b32 s69,18688
s_waitcnt lgkmcnt(0)
v_cmp_eq_u32 vcc,s60,v14
s_nop 0
s_cmp_eq_u32 vcc_lo,-1
s_cbranch_scc1 Lg3_go0_%=
s_add_u32 m0,s78,8192
s_nop 0
buffer_load_dword v7,s[36:39],s68 offen offset:0 sc1 lds
s_waitcnt vmcnt(3)
s_barrier
ds_read_b32 v14,v8 offset:16384
s_mul_i32 s70,s47,5
s_lshr_b32 s70,s70,6
buffer_store_dword v5,v4,s[36:39],s70 offen offset:0 sc0 sc1
s_mov_b32 s47,s59
s_mov_b32 s69,10240
s_waitcnt lgkmcnt(0)
v_cmp_eq_u32 vcc,s60,v14
s_nop 0
s_cmp_eq_u32 vcc_lo,-1
s_cbranch_scc1 Lg3_go0_%=
s_add_u32 m0,s78,8448
s_nop 0
buffer_load_dword v7,s[36:39],s68 offen offset:0 sc1 lds
s_waitcnt vmcnt(3)
s_barrier
ds_read_b32 v14,v8 offset:16640
s_mov_b32 s69,10496
s_waitcnt lgkmcnt(0)
v_cmp_eq_u32 vcc,s60,v14
s_nop 0
s_cmp_eq_u32 vcc_lo,-1
s_cbranch_scc1 Lg3_go0_%=
Lg3_poll0_%=:
s_add_u32 m0,s78,16384
s_nop 0
buffer_load_dword v7,s[36:39],s68 offen offset:0 sc1 lds
s_waitcnt vmcnt(2)
s_barrier
ds_read_b32 v14,v8 offset:8192
s_mov_b32 s69,18432
s_waitcnt lgkmcnt(0)
v_cmp_eq_u32 vcc,s60,v14
s_nop 0
s_cmp_eq_u32 vcc_lo,-1
s_cbranch_scc1 Lg3_go0_%=
s_sleep 4
s_add_u32 m0,s78,16640
s_nop 0
buffer_load_dword v7,s[36:39],s68 offen offset:0 sc1 lds
s_waitcnt vmcnt(2)
s_barrier
ds_read_b32 v14,v8 offset:8448
s_mov_b32 s69,18688
s_waitcnt lgkmcnt(0)
v_cmp_eq_u32 vcc,s60,v14
s_nop 0
s_cmp_eq_u32 vcc_lo,-1
s_cbranch_scc1 Lg3_go0_%=
s_sleep 4
s_add_u32 m0,s78,8192
s_nop 0
buffer_load_dword v7,s[36:39],s68 offen offset:0 sc1 lds
s_waitcnt vmcnt(2)
s_barrier
ds_read_b32 v14,v8 offset:16384
s_mov_b32 s69,10240
s_waitcnt lgkmcnt(0)
v_cmp_eq_u32 vcc,s60,v14
s_nop 0
s_cmp_eq_u32 vcc_lo,-1
s_cbranch_scc1 Lg3_go0_%=
s_sleep 4
s_add_u32 m0,s78,8448
s_nop 0
buffer_load_dword v7,s[36:39],s68 offen offset:0 sc1 lds
s_waitcnt vmcnt(2)
s_barrier
ds_read_b32 v14,v8 offset:16640
s_mov_b32 s69,10496
s_waitcnt lgkmcnt(0)
v_cmp_eq_u32 vcc,s60,v14
s_nop 0
s_cmp_eq_u32 vcc_lo,-1
s_cbranch_scc1 Lg3_go0_%=
s_sleep 4
s_add_u32 s79,s79,1
s_cmp_lt_u32 s79,20000
s_cbranch_scc1 Lg3_poll0_%=
Lg3_go0_%=:
s_xor_b32 s62,s63,65536
s_lshr_b32 s61,s48,0
s_cmp_lt_u32 s66,s59
s_cbranch_scc1 Lg3_gs0_%=
s_mov_b32 m0,s62
s_nop 0
buffer_load_dwordx4 v0,s[36:39],s61 offen offset:0 sc1 lds
buffer_load_dwordx4 v0,s[36:39],s61 offen offset:1024 sc1 lds
buffer_load_dwordx4 v0,s[36:39],s61 offen offset:2048 sc1 lds
buffer_load_dwordx4 v0,s[36:39],s61 offen offset:3072 sc1 lds
s_add_u32 m0,s62,16384
s_lshr_b32 s72,s61,5
buffer_load_dwordx4 v1,s[36:39],s61 offen offset:0 sc1 lds
buffer_load_dwordx4 v1,s[36:39],s61 offen offset:1024 sc1 lds
buffer_load_dwordx4 v1,s[36:39],s61 offen offset:2048 sc1 lds
buffer_load_dwordx4 v1,s[36:39],s61 offen offset:3072 sc1 lds
buffer_load_dword v104,v9,s[36:39],s72 offen offset:0 sc1
v_add_u32 v84,s69,v8
s_waitcnt vmcnt(5)
s_barrier
ds_read_b32 v14,v84 offset:0
ds_read_b128 v[52:55],v2 offset:0
ds_read_b128 v[56:59],v2 offset:1024
ds_read_b128 v[60:63],v2 offset:2048
ds_read_b128 v[64:67],v2 offset:3072)"
R"(
ds_read_b128 v[68:71],v2 offset:4096
s_mul_i32 s70,s46,5
s_lshr_b32 s70,s70,6
buffer_store_dword v5,v4,s[36:39],s70 offen offset:0 sc0 sc1
s_mov_b32 s46,s59
s_mul_i32 s70,s47,5
s_lshr_b32 s70,s70,6
buffer_store_dword v5,v4,s[36:39],s70 offen offset:0 sc0 sc1
s_mov_b32 s47,s59
s_mul_i32 s70,s59,5
s_lshr_b32 s70,s70,6
buffer_store_dword v5,v4,s[36:39],s70 offen offset:0 sc0 sc1
s_waitcnt lgkmcnt(5)
v_cmp_eq_u32 vcc,s60,v14
s_nop 0
s_cmp_eq_u32 vcc_lo,-1
s_cselect_b32 s66,s49,s59
s_lshr_b32 s61,s66,0
s_xor_b32 s62,s64,65536
s_waitcnt lgkmcnt(3)
s_setpc_b64 s[90:91]
Lg3_gs0_%=:
ds_read_b128 v[52:55],v2 offset:0
ds_read_b128 v[56:59],v2 offset:1024
ds_read_b128 v[60:63],v2 offset:2048
ds_read_b128 v[64:67],v2 offset:3072
ds_read_b128 v[68:71],v2 offset:4096
s_cmp_eq_u32 vcc_hi,-1
s_cselect_b32 s66,s49,s59
s_lshr_b32 s61,s66,0
s_xor_b32 s62,s64,65536
s_mov_b32 s46,s47
s_mov_b32 s47,s59
s_waitcnt lgkmcnt(3)
s_setpc_b64 s[90:91]
Lg3_done0_%=:
s_waitcnt vmcnt(0)
s_barrier
s_mul_i32 s70,s46,5
s_lshr_b32 s70,s70,6
buffer_store_dword v5,v4,s[36:39],s70 offen offset:0 sc0 sc1
s_mul_i32 s70,s47,5
s_lshr_b32 s70,s70,6
buffer_store_dword v5,v4,s[36:39],s70 offen offset:0 sc0 sc1
s_branch Lg3_done_%=
Lg3_slow1_%=:
s_xor_b32 s62,s64,65536
s_lshr_b32 s61,s49,0
s_cmp_ge_u32 s67,s59
s_cbranch_scc1 Lg3_se1_%=
s_mov_b32 m0,s62
s_nop 0
buffer_load_dwordx4 v0,s[36:39],s61 offen offset:0 sc1 lds
buffer_load_dwordx4 v0,s[36:39],s61 offen offset:1024 sc1 lds
buffer_load_dwordx4 v0,s[36:39],s61 offen offset:2048 sc1 lds
buffer_load_dwordx4 v0,s[36:39],s61 offen offset:3072 sc1 lds
s_add_u32 m0,s62,16384
s_lshr_b32 s72,s61,5
buffer_load_dwordx4 v1,s[36:39],s61 offen offset:0 sc1 lds
buffer_load_dwordx4 v1,s[36:39],s61 offen offset:1024 sc1 lds
buffer_load_dwordx4 v1,s[36:39],s61 offen offset:2048 sc1 lds
buffer_load_dwordx4 v1,s[36:39],s61 offen offset:3072 sc1 lds
buffer_load_dword v108,v9,s[36:39],s72 offen offset:0 sc1
Lg3_se1_%=:
s_cmp_eq_u32 s54,1
s_cselect_b32 s68,s50,s49
s_lshr_b32 s68,s68,5
s_add_u32 m0,s78,8192
s_nop 0
buffer_load_dword v7,s[36:39],s68 offen offset:0 sc1 lds
s_mul_i32 s73,s48,7
s_lshr_b32 s73,s73,1
v_mul_f32 v116,s75,v112
v_fma_f32 v113,v113,s75,v13
v_fma_f32 v113,-v116,v116,v113
v_max_f32 v113,v113,v13
v_rsq_f32 v112,v113
s_nop 0
v_mul_f32 v117,s76,v112
v_mul_f32 v118,v112,v112
v_mul_f32 v119,v113,v112
v_mul_f32 v118,s77,v118
v_mul_f32 v118,v118,v117
v_fma_f32 v28,-v116,v16,v28
v_fma_f32 v29,-v116,v17,v29
v_fma_f32 v30,-v116,v18,v30
v_fma_f32 v31,-v116,v19,v31
v_mul_f32 v84,v28,v28
v_mul_f32 v85,v29,v29
v_mul_f32 v86,v30,v30
v_mul_f32 v87,v31,v31
v_fma_f32 v84,v118,v84,v117
v_fma_f32 v85,v118,v85,v117
v_fma_f32 v86,v118,v86,v117
v_fma_f32 v87,v118,v87,v117
v_mul_f32 v84,v28,v84
v_mul_f32 v85,v29,v85
v_mul_f32 v86,v30,v86
v_mul_f32 v87,v31,v87
v_exp_f32 v84,v84
v_exp_f32 v85,v85
v_exp_f32 v86,v86
v_exp_f32 v87,v87
v_fma_f32 v84,v84,v119,v119
v_fma_f32 v85,v85,v119,v119
v_fma_f32 v86,v86,v119,v119
v_fma_f32 v87,v87,v119,v119
v_rcp_f32 v84,v84
v_rcp_f32 v85,v85
v_rcp_f32 v86,v86
v_rcp_f32 v87,v87
v_mul_f32 v28,v28,v84
v_mul_f32 v29,v29,v85
v_mul_f32 v30,v30,v86
v_mul_f32 v31,v31,v87
v_cvt_pk_bf16_f32 v96,v28,v29
v_cvt_pk_bf16_f32 v97,v30,v31
v_fma_f32 v32,-v116,v20,v32
v_fma_f32 v33,-v116,v21,v33
v_fma_f32 v34,-v116,v22,v34
v_fma_f32 v35,-v116,v23,v35
v_mul_f32 v88,v32,v32
v_mul_f32 v89,v33,v33
v_mul_f32 v90,v34,v34
v_mul_f32 v91,v35,v35
v_fma_f32 v88,v118,v88,v117
v_fma_f32 v89,v118,v89,v117
v_fma_f32 v90,v118,v90,v117
v_fma_f32 v91,v118,v91,v117
v_mul_f32 v88,v32,v88
s_add_u32 m0,s78,8448
s_nop 0
buffer_load_dword v7,s[36:39],s68 offen offset:0 sc1 lds
v_mul_f32 v89,v33,v89
v_mul_f32 v90,v34,v90
v_mul_f32 v91,v35,v91
v_exp_f32 v88,v88
v_exp_f32 v89,v89
v_exp_f32 v90,v90
v_exp_f32 v91,v91
v_fma_f32 v88,v88,v119,v119
v_fma_f32 v89,v89,v119,v119
v_fma_f32 v90,v90,v119,v119
v_fma_f32 v91,v91,v119,v119
v_rcp_f32 v88,v88
v_rcp_f32 v89,v89
v_rcp_f32 v90,v90
v_rcp_f32 v91,v91
v_mul_f32 v32,v32,v88
v_mul_f32 v33,v33,v89
v_mul_f32 v34,v34,v90
v_mul_f32 v35,v35,v91
v_cvt_pk_bf16_f32 v98,v32,v33
v_cvt_pk_bf16_f32 v99,v34,v35
buffer_store_dwordx4 v[96:99],v3,s[36:39],s73 offen offset:0 sc0 sc1
v_fma_f32 v36,-v116,v24,v36
v_fma_f32 v37,-v116,v25,v37
v_fma_f32 v38,-v116,v26,v38
v_fma_f32 v39,-v116,v27,v39
v_mul_f32 v92,v36,v36
v_mul_f32 v93,v37,v37
v_mul_f32 v94,v38,v38
v_mul_f32 v95,v39,v39
v_fma_f32 v92,v118,v92,v117
v_fma_f32 v93,v118,v93,v117
v_fma_f32 v94,v118,v94,v117
v_fma_f32 v95,v118,v95,v117
v_mul_f32 v92,v36,v92
v_mul_f32 v93,v37,v93
v_mul_f32 v94,v38,v94
v_mul_f32 v95,v39,v95
v_exp_f32 v92,v92
v_exp_f32 v93,v93
v_exp_f32 v94,v94
v_exp_f32 v95,v95
v_fma_f32 v92,v92,v119,v119
v_fma_f32 v93,v93,v119,v119
v_fma_f32 v94,v94,v119,v119
v_fma_f32 v95,v95,v119,v119
v_rcp_f32 v92,v92
v_rcp_f32 v93,v93
v_rcp_f32 v94,v94
v_rcp_f32 v95,v95
v_mul_f32 v36,v36,v92
v_mul_f32 v37,v37,v93
v_mul_f32 v38,v38,v94
v_mul_f32 v39,v39,v95
v_cvt_pk_bf16_f32 v100,v36,v37
v_cvt_pk_bf16_f32 v101,v38,v39
buffer_store_dwordx2 v[100:101],v10,s[36:39],s73 offen offset:0 sc0 sc1
s_cmp_ge_u32 s49,s59
s_cbranch_scc1 Lg3_done1_%=
s_add_u32 m0,s78,16384
s_nop 0
buffer_load_dword v7,s[36:39],s68 offen offset:0 sc1 lds
s_waitcnt vmcnt(4)
s_barrier
ds_read_b32 v14,v8 offset:8192
s_mul_i32 s70,s47,5
s_lshr_b32 s70,s70,6)"
R"(
buffer_store_dword v5,v4,s[36:39],s70 offen offset:0 sc0 sc1
s_mov_b32 s47,s59
s_mov_b32 s69,18432
s_waitcnt lgkmcnt(0)
v_cmp_eq_u32 vcc,s60,v14
s_nop 0
s_cmp_eq_u32 vcc_lo,-1
s_cbranch_scc1 Lg3_go1_%=
s_add_u32 m0,s78,16640
s_nop 0
buffer_load_dword v7,s[36:39],s68 offen offset:0 sc1 lds
s_waitcnt vmcnt(5)
s_barrier
ds_read_b32 v14,v8 offset:8448
s_mov_b32 s69,18688
s_waitcnt lgkmcnt(0)
v_cmp_eq_u32 vcc,s60,v14
s_nop 0
s_cmp_eq_u32 vcc_lo,-1
s_cbranch_scc1 Lg3_go1_%=
s_add_u32 m0,s78,8192
s_nop 0
buffer_load_dword v7,s[36:39],s68 offen offset:0 sc1 lds
s_waitcnt vmcnt(3)
s_barrier
ds_read_b32 v14,v8 offset:16384
s_mul_i32 s70,s48,5
s_lshr_b32 s70,s70,6
buffer_store_dword v5,v4,s[36:39],s70 offen offset:0 sc0 sc1
s_mov_b32 s48,s59
s_mov_b32 s69,10240
s_waitcnt lgkmcnt(0)
v_cmp_eq_u32 vcc,s60,v14
s_nop 0
s_cmp_eq_u32 vcc_lo,-1
s_cbranch_scc1 Lg3_go1_%=
s_add_u32 m0,s78,8448
s_nop 0
buffer_load_dword v7,s[36:39],s68 offen offset:0 sc1 lds
s_waitcnt vmcnt(3)
s_barrier
ds_read_b32 v14,v8 offset:16640
s_mov_b32 s69,10496
s_waitcnt lgkmcnt(0)
v_cmp_eq_u32 vcc,s60,v14
s_nop 0
s_cmp_eq_u32 vcc_lo,-1
s_cbranch_scc1 Lg3_go1_%=
Lg3_poll1_%=:
s_add_u32 m0,s78,16384
s_nop 0
buffer_load_dword v7,s[36:39],s68 offen offset:0 sc1 lds
s_waitcnt vmcnt(2)
s_barrier
ds_read_b32 v14,v8 offset:8192
s_mov_b32 s69,18432
s_waitcnt lgkmcnt(0)
v_cmp_eq_u32 vcc,s60,v14
s_nop 0
s_cmp_eq_u32 vcc_lo,-1
s_cbranch_scc1 Lg3_go1_%=
s_sleep 4
s_add_u32 m0,s78,16640
s_nop 0
buffer_load_dword v7,s[36:39],s68 offen offset:0 sc1 lds
s_waitcnt vmcnt(2)
s_barrier
ds_read_b32 v14,v8 offset:8448
s_mov_b32 s69,18688
s_waitcnt lgkmcnt(0)
v_cmp_eq_u32 vcc,s60,v14
s_nop 0
s_cmp_eq_u32 vcc_lo,-1
s_cbranch_scc1 Lg3_go1_%=
s_sleep 4
s_add_u32 m0,s78,8192
s_nop 0
buffer_load_dword v7,s[36:39],s68 offen offset:0 sc1 lds
s_waitcnt vmcnt(2)
s_barrier
ds_read_b32 v14,v8 offset:16384
s_mov_b32 s69,10240
s_waitcnt lgkmcnt(0)
v_cmp_eq_u32 vcc,s60,v14
s_nop 0
s_cmp_eq_u32 vcc_lo,-1
s_cbranch_scc1 Lg3_go1_%=
s_sleep 4
s_add_u32 m0,s78,8448
s_nop 0
buffer_load_dword v7,s[36:39],s68 offen offset:0 sc1 lds
s_waitcnt vmcnt(2)
s_barrier
ds_read_b32 v14,v8 offset:16640
s_mov_b32 s69,10496
s_waitcnt lgkmcnt(0)
v_cmp_eq_u32 vcc,s60,v14
s_nop 0
s_cmp_eq_u32 vcc_lo,-1
s_cbranch_scc1 Lg3_go1_%=
s_sleep 4
s_add_u32 s79,s79,1
s_cmp_lt_u32 s79,20000
s_cbranch_scc1 Lg3_poll1_%=
Lg3_go1_%=:
s_xor_b32 s62,s64,65536
s_lshr_b32 s61,s49,0
s_cmp_lt_u32 s67,s59
s_cbranch_scc1 Lg3_gs1_%=
s_mov_b32 m0,s62
s_nop 0
buffer_load_dwordx4 v0,s[36:39],s61 offen offset:0 sc1 lds
buffer_load_dwordx4 v0,s[36:39],s61 offen offset:1024 sc1 lds
buffer_load_dwordx4 v0,s[36:39],s61 offen offset:2048 sc1 lds
buffer_load_dwordx4 v0,s[36:39],s61 offen offset:3072 sc1 lds
s_add_u32 m0,s62,16384
s_lshr_b32 s72,s61,5
buffer_load_dwordx4 v1,s[36:39],s61 offen offset:0 sc1 lds
buffer_load_dwordx4 v1,s[36:39],s61 offen offset:1024 sc1 lds
buffer_load_dwordx4 v1,s[36:39],s61 offen offset:2048 sc1 lds
buffer_load_dwordx4 v1,s[36:39],s61 offen offset:3072 sc1 lds
buffer_load_dword v108,v9,s[36:39],s72 offen offset:0 sc1
v_add_u32 v84,s69,v8
s_waitcnt vmcnt(5)
s_barrier
ds_read_b32 v14,v84 offset:0
ds_read_b128 v[52:55],v2 offset:32768
ds_read_b128 v[56:59],v2 offset:33792
ds_read_b128 v[60:63],v2 offset:34816
ds_read_b128 v[64:67],v2 offset:35840
ds_read_b128 v[68:71],v2 offset:36864
s_mul_i32 s70,s47,5
s_lshr_b32 s70,s70,6
buffer_store_dword v5,v4,s[36:39],s70 offen offset:0 sc0 sc1
s_mov_b32 s47,s59
s_mul_i32 s70,s48,5
s_lshr_b32 s70,s70,6
buffer_store_dword v5,v4,s[36:39],s70 offen offset:0 sc0 sc1
s_mov_b32 s48,s59
s_mul_i32 s70,s59,5
s_lshr_b32 s70,s70,6
buffer_store_dword v5,v4,s[36:39],s70 offen offset:0 sc0 sc1
s_waitcnt lgkmcnt(5)
v_cmp_eq_u32 vcc,s60,v14
s_nop 0
s_cmp_eq_u32 vcc_lo,-1
s_cselect_b32 s67,s50,s59
s_lshr_b32 s61,s67,0
s_xor_b32 s62,s63,65536
s_waitcnt lgkmcnt(3)
s_setpc_b64 s[92:93]
Lg3_gs1_%=:
ds_read_b128 v[52:55],v2 offset:32768
ds_read_b128 v[56:59],v2 offset:33792
ds_read_b128 v[60:63],v2 offset:34816
ds_read_b128 v[64:67],v2 offset:35840
ds_read_b128 v[68:71],v2 offset:36864
s_cmp_eq_u32 vcc_hi,-1
s_cselect_b32 s67,s50,s59
s_lshr_b32 s61,s67,0
s_xor_b32 s62,s63,65536
s_mov_b32 s47,s48
s_mov_b32 s48,s59
s_waitcnt lgkmcnt(3)
s_setpc_b64 s[92:93]
Lg3_done1_%=:
s_waitcnt vmcnt(0)
s_barrier
s_mul_i32 s70,s47,5
s_lshr_b32 s70,s70,6
buffer_store_dword v5,v4,s[36:39],s70 offen offset:0 sc0 sc1
s_mul_i32 s70,s48,5
s_lshr_b32 s70,s70,6
buffer_store_dword v5,v4,s[36:39],s70 offen offset:0 sc0 sc1
s_branch Lg3_done_%=
Lg3_done_%=:
s_waitcnt vmcnt(0) lgkmcnt(0)
s_barrier
s_endpgm
Lcode_g3_e:)"
  :: "s"(ws), "s"(wave), "s"(sStep), "s"(sX1), "s"(sX2), "s"(sU0), "s"(sWw), "s"(sBuf), "v"(vSrc), "v"(vA), "v"(vOut), "v"(vFlag), "v"(vSnapOff), "v"(vPollOff), "v"(vSnapRd), "v"(vAuxS), "v"(vOut2), "v"(vStW), "v"(vStR), "v"(vWd0), "v"(vWd1), "v"(vWr0), "v"(vWr1), "v"(vWr2), "s"(w3) : "v0", "v1", "v2", "v3", "v4", "v5", "v6", "v7", "v8", "v9", "v10", "v11", "v12", "v13", "v14", "v15", "v16", "v17", "v18", "v19", "v20", "v21", "v22", "v23", "v24", "v25", "v26", "v27", "v28", "v29", "v30", "v31", "v32", "v33", "v34", "v35", "v36", "v37", "v38", "v39", "v40", "v41", "v42", "v43", "v44", "v45", "v46", "v47", "v48", "v49", "v50", "v51", "v52", "v53", "v54", "v55", "v56", "v57", "v58", "v59", "v60", "v61", "v62", "v63", "v64", "v65", "v66", "v67", "v68", "v69", "v70", "v71", "v72", "v73", "v74", "v75", "v76", "v77", "v78", "v79", "v80", "v81", "v82", "v83", "v84", "v85", "v86", "v87", "v88", "v89", "v90", "v91", "v92", "v93", "v94", "v95", "v96", "v97", "v98", "v99", "v100", "v101", "v102", "v103", "v104", "v105", "v106", "v107", "v108", "v109", "v110", "v111", "v112", "v113", "v114", "v115", "v116", "v117", "v118", "v119", "v120", "v121", "v122", "v123", "v124", "v125", "v126", "v127", "v128", "v129", "v130", "v131", "v132", "v133", "v134", "v135", "v136", "v137", "v138", "v139", "v140", "v141", "v142", "v143", "v144", "v145", "v146", "v147", "v148", "v149", "v150", "v151", "v152", "v153", "v154", "v155", "v156", "v157", "v158", "v159", "v160", "v161", "v162", "v163", "v164", "v165", "v166", "v167", "v168", "v169", "v170", "v171", "v172", "v173", "v174", "v175", "v176", "v177", "v178", "v179", "v180", "v181", "v182", "v183", "v184", "v185", "v186", "v187", "v188", "v189", "v190", "v191", "v192", "v193", "v194", "v195", "v196", "v197", "v198", "v199", "v200", "v201", "v202", "v203", "v204", "v205", "v206", "v207", "v208", "v209", "v210", "v211", "v212", "v213", "v214", "v215", "v216", "v217", "v218", "v219", "v220", "v221", "v222", "v223", "v224", "v225", "v226", "v227", "v228", "v229", "v230", "v231", "v255", "a0", "a1", "a2", "a3", "a4", "a5", "a6", "a7", "a8", "a9", "a10", "a11", "a12", "a13", "a14", "a15", "a16", "a17", "a18", "a19", "a20", "a21", "a22", "a23", "a24", "a25", "a26", "a27", "a28", "a29", "a30", "a31", "a32", "a33", "a34", "a35", "a36", "a37", "a38", "a39", "a40", "a41", "a42", "a43", "a44", "a45", "a46", "a47", "a48", "a49", "a50", "a51", "a52", "a53", "a54", "a55", "a56", "a57", "a58", "a59", "a60", "a61", "a62", "a63", "a64", "a65", "a66", "a67", "a68", "a69", "a70", "a71", "a72", "a73", "a74", "a75", "a76", "a77", "a78", "a79", "a80", "a81", "a82", "a83", "a84", "a85", "a86", "a87", "a88", "a89", "a90", "a91", "a92", "a93", "a94", "a95", "a96", "a97", "a98", "a99", "a100", "a101", "a102", "a103", "a104", "a105", "a106", "a107", "a108", "a109", "a110", "a111", "a112", "a113", "a114", "a115", "a116", "a117", "a118", "a119", "a120", "a121", "a122", "a123", "a124", "a125", "a126", "a127", "a128", "a129", "a130", "a131", "a132", "a133", "a134", "a135", "a136", "a137", "a138", "a139", "a140", "a141", "a142", "a143", "a144", "a145", "a146", "a147", "a148", "a149", "a150", "a151", "a152", "a153", "a154", "a155", "a156", "a157", "a158", "a159", "a160", "a161", "a162", "a163", "a164", "a165", "a166", "a167", "a168", "a169", "a170", "a171", "a172", "a173", "a174", "a175", "a176", "a177", "a178", "a179", "a180", "a181", "a182", "a183", "a184", "a185", "a186", "a187", "a188", "a189", "a190", "a191", "a192", "a193", "a194", "a195", "a196", "a197", "a198", "a199", "a200", "a201", "a202", "a203", "a204", "a205", "a206", "a207", "a208", "a209", "a210", "a211", "a212", "a213", "a214", "a215", "a216", "a217", "a218", "a219", "a220", "a221", "a222", "a223", "a224", "a225", "a226", "a227", "a228", "a229", "a230", "a231", "a232", "a233", "a234", "a235", "a236", "a237", "a238", "a239", "a240", "a241", "a242", "a243", "a244", "a245", "a246", "a247", "a248", "a249", "a250", "a251", "a252", "a253", "a254", "a255", "s36", "s37", "s38", "s39", "s40", "s41", "s42", "s43", "s44", "s45", "s46", "s47", "s48", "s49", "s50", "s51", "s52", "s53", "s54", "s55", "s56", "s57", "s58", "s59", "s60", "s61", "s62", "s63", "s64", "s65", "s66", "s67", "s68", "s69", "s70", "s71", "s72", "s73", "s74", "s75", "s76", "s77", "s78", "s79", "s80", "s81", "s82", "s83", "s84", "s85", "s86", "s87", "s88", "s89", "s90", "s91", "s92", "s93", "s94", "s95", "s96", "s97", "s98", "s99", "vcc", "scc", "memory"); } else { asm volatile(R"(
Lcode_g3o_b:
s_mov_b64 s[36:37],%0
s_mov_b32 s38,111193216
s_mov_b32 s39,0x00020000
s_mov_b32 s54,%1
s_mov_b32 s55,%2
s_mov_b32 s56,%3
s_mov_b32 s58,%4
s_mov_b32 s83,%5
s_mov_b32 s82,%6
s_mov_b32 s84,%7
v_mov_b32 v0,%8
v_mov_b32 v2,%9
v_mov_b32 v3,%10
v_mov_b32 v4,%11
v_mov_b32 v6,%12
v_mov_b32 v7,%13
v_mov_b32 v8,%14
v_mov_b32 v9,%15
v_mov_b32 v10,%16
v_mov_b32 v11,%17
v_mov_b32 v24,%18
v_mov_b32 v25,%19
v_mov_b32 v26,%20
v_mov_b32 v27,%21
v_mov_b32 v28,%22
s_mov_b64 s[40:41],%23
s_mov_b32 s42,7340032
s_mov_b32 s43,0x00020000
s_lshl_b32 s88,s54,11
s_mov_b32 s69,111193088
buffer_load_dword v5,off,s[36:39],s69 sc1
s_add_u32 m0,s88,0
s_add_u32 s61,s82,0
buffer_load_dwordx4 v24,s[40:43],s61 offen offset:0  lds
s_add_u32 m0,s88,1024
s_nop 0
buffer_load_dwordx4 v25,s[40:43],s61 offen offset:0  lds
s_add_u32 m0,s88,8064
s_nop 0
buffer_load_dwordx4 v24,s[40:43],s61 offen offset:128  lds
s_add_u32 m0,s88,9088
s_nop 0
buffer_load_dwordx4 v25,s[40:43],s61 offen offset:128  lds
s_add_u32 m0,s88,16384
s_add_u32 s61,s82,458752
buffer_load_dwordx4 v24,s[40:43],s61 offen offset:0  lds
s_add_u32 m0,s88,17408
s_nop 0
buffer_load_dwordx4 v25,s[40:43],s61 offen offset:0  lds
s_add_u32 m0,s88,24448
s_nop 0
buffer_load_dwordx4 v24,s[40:43],s61 offen offset:128  lds
s_add_u32 m0,s88,25472
s_nop 0
buffer_load_dwordx4 v25,s[40:43],s61 offen offset:128  lds
s_add_u32 m0,s88,32768
s_add_u32 s61,s82,917504
buffer_load_dwordx4 v24,s[40:43],s61 offen offset:0  lds
s_add_u32 m0,s88,33792
s_nop 0
buffer_load_dwordx4 v25,s[40:43],s61 offen offset:0  lds
s_add_u32 m0,s88,40832
s_nop 0
buffer_load_dwordx4 v24,s[40:43],s61 offen offset:128  lds
s_add_u32 m0,s88,41856
s_nop 0
buffer_load_dwordx4 v25,s[40:43],s61 offen offset:128  lds
s_add_u32 m0,s88,49152
s_add_u32 s61,s82,1376256
buffer_load_dwordx4 v24,s[40:43],s61 offen offset:0  lds
s_add_u32 m0,s88,50176
s_nop 0
buffer_load_dwordx4 v25,s[40:43],s61 offen offset:0  lds
s_add_u32 m0,s88,57216
s_nop 0
buffer_load_dwordx4 v24,s[40:43],s61 offen offset:128  lds
s_add_u32 m0,s88,58240
s_nop 0
buffer_load_dwordx4 v25,s[40:43],s61 offen offset:128  lds
s_add_u32 m0,s88,65536
s_add_u32 s61,s82,1835008
buffer_load_dwordx4 v24,s[40:43],s61 offen offset:0  lds
s_add_u32 m0,s88,66560
s_nop 0
buffer_load_dwordx4 v25,s[40:43],s61 offen offset:0  lds
s_add_u32 m0,s88,73600
s_nop 0
buffer_load_dwordx4 v24,s[40:43],s61 offen offset:128  lds
s_add_u32 m0,s88,74624
s_nop 0
buffer_load_dwordx4 v25,s[40:43],s61 offen offset:128  lds
s_add_u32 m0,s88,81920
s_add_u32 s61,s82,2293760
buffer_load_dwordx4 v24,s[40:43],s61 offen offset:0  lds
s_add_u32 m0,s88,82944
s_nop 0
buffer_load_dwordx4 v25,s[40:43],s61 offen offset:0  lds
s_add_u32 m0,s88,89984
s_nop 0
buffer_load_dwordx4 v24,s[40:43],s61 offen offset:128  lds
s_add_u32 m0,s88,91008
s_nop 0
buffer_load_dwordx4 v25,s[40:43],s61 offen offset:128  lds
s_add_u32 m0,s88,98304
s_add_u32 s61,s82,2752512
buffer_load_dwordx4 v24,s[40:43],s61 offen offset:0  lds
s_add_u32 m0,s88,99328
s_nop 0
buffer_load_dwordx4 v25,s[40:43],s61 offen offset:0  lds
s_add_u32 m0,s88,106368
s_nop 0
buffer_load_dwordx4 v24,s[40:43],s61 offen offset:128  lds
s_add_u32 m0,s88,107392
s_nop 0
buffer_load_dwordx4 v25,s[40:43],s61 offen offset:128  lds
v_add_u32 v32,49152,v26
v_add_u32 v33,49152,v27
v_add_u32 v34,98304,v26
v_add_u32 v35,98304,v27
v_mov_b32 v72,0x3f803f80
v_mov_b32 v73,0x3f803f80
v_mov_b32 v74,0x3f803f80
v_mov_b32 v75,0x3f803f80
s_waitcnt vmcnt(24)
s_barrier
s_add_u32 m0,s88,114688
s_add_u32 s61,s82,3211264
buffer_load_dwordx4 v24,s[40:43],s61 offen offset:0  lds
s_add_u32 m0,s88,115712
ds_read_b64_tr_b16 a[0:1],v26 offset:0
buffer_load_dwordx4 v25,s[40:43],s61 offen offset:0  lds
s_add_u32 m0,s88,122752
ds_read_b64_tr_b16 a[2:3],v26 offset:512
buffer_load_dwordx4 v24,s[40:43],s61 offen offset:128  lds
s_add_u32 m0,s88,123776
ds_read_b64_tr_b16 a[4:5],v27 offset:0
buffer_load_dwordx4 v25,s[40:43],s61 offen offset:128  lds
ds_read_b64_tr_b16 a[6:7],v27 offset:512
ds_read_b64_tr_b16 a[8:9],v26 offset:4096
ds_read_b64_tr_b16 a[10:11],v26 offset:4608
ds_read_b64_tr_b16 a[12:13],v27 offset:4096
ds_read_b64_tr_b16 a[14:15],v27 offset:4608
s_waitcnt vmcnt(24)
s_barrier
s_add_u32 m0,s88,131072
s_add_u32 s61,s82,3670016
buffer_load_dwordx4 v24,s[40:43],s61 offen offset:0  lds
s_add_u32 m0,s88,132096
ds_read_b64_tr_b16 a[16:17],v26 offset:16384
buffer_load_dwordx4 v25,s[40:43],s61 offen offset:0  lds
s_add_u32 m0,s88,139136
ds_read_b64_tr_b16 a[18:19],v26 offset:16896
buffer_load_dwordx4 v24,s[40:43],s61 offen offset:128  lds
s_add_u32 m0,s88,140160
ds_read_b64_tr_b16 a[20:21],v27 offset:16384
buffer_load_dwordx4 v25,s[40:43],s61 offen offset:128  lds
ds_read_b64_tr_b16 a[22:23],v27 offset:16896
ds_read_b64_tr_b16 a[24:25],v26 offset:20480
ds_read_b64_tr_b16 a[26:27],v26 offset:20992
ds_read_b64_tr_b16 a[28:29],v27 offset:20480
ds_read_b64_tr_b16 a[30:31],v27 offset:20992
s_waitcnt lgkmcnt(8)
v_mfma_f32_16x16x32_bf16 v[16:19],a[0:3],v[72:75],0
v_mfma_f32_16x16x32_bf16 v[20:23],a[4:7],v[72:75],0
v_mfma_f32_16x16x32_bf16 v[16:19],a[8:11],v[72:75],v[16:19]
v_mfma_f32_16x16x32_bf16 v[20:23],a[12:15],v[72:75],v[20:23]
s_waitcnt vmcnt(24)
s_barrier
s_add_u32 m0,s88,0
s_add_u32 s61,s82,4128768
buffer_load_dwordx4 v24,s[40:43],s61 offen offset:0  lds
s_add_u32 m0,s88,1024
ds_read_b64_tr_b16 a[32:33],v26 offset:32768
buffer_load_dwordx4 v25,s[40:43],s61 offen offset:0  lds
s_add_u32 m0,s88,8064
ds_read_b64_tr_b16 a[34:35],v26 offset:33280
buffer_load_dwordx4 v24,s[40:43],s61 offen offset:128  lds
s_add_u32 m0,s88,9088
ds_read_b64_tr_b16 a[36:37],v27 offset:32768
buffer_load_dwordx4 v25,s[40:43],s61 offen offset:128  lds
ds_read_b64_tr_b16 a[38:39],v27 offset:33280
ds_read_b64_tr_b16 a[40:41],v26 offset:36864
ds_read_b64_tr_b16 a[42:43],v26 offset:37376
ds_read_b64_tr_b16 a[44:45],v27 offset:36864
ds_read_b64_tr_b16 a[46:47],v27 offset:37376
s_waitcnt lgkmcnt(8)
v_mfma_f32_16x16x32_bf16 v[16:19],a[16:19],v[72:75],v[16:19]
v_mfma_f32_16x16x32_bf16 v[20:23],a[20:23],v[72:75],v[20:23]
v_mfma_f32_16x16x32_bf16 v[16:19],a[24:27],v[72:75],v[16:19]
v_mfma_f32_16x16x32_bf16 v[20:23],a[28:31],v[72:75],v[20:23]
s_waitcnt vmcnt(24)
s_barrier
s_add_u32 m0,s88,16384
s_add_u32 s61,s82,4587520
buffer_load_dwordx4 v24,s[40:43],s61 offen offset:0  lds
s_add_u32 m0,s88,17408
ds_read_b64_tr_b16 a[48:49],v32 offset:0
buffer_load_dwordx4 v25,s[40:43],s61 offen offset:0  lds
s_add_u32 m0,s88,24448)"
R"(
ds_read_b64_tr_b16 a[50:51],v32 offset:512
buffer_load_dwordx4 v24,s[40:43],s61 offen offset:128  lds
s_add_u32 m0,s88,25472
ds_read_b64_tr_b16 a[52:53],v33 offset:0
buffer_load_dwordx4 v25,s[40:43],s61 offen offset:128  lds
ds_read_b64_tr_b16 a[54:55],v33 offset:512
ds_read_b64_tr_b16 a[56:57],v32 offset:4096
ds_read_b64_tr_b16 a[58:59],v32 offset:4608
ds_read_b64_tr_b16 a[60:61],v33 offset:4096
ds_read_b64_tr_b16 a[62:63],v33 offset:4608
s_waitcnt lgkmcnt(8)
v_mfma_f32_16x16x32_bf16 v[16:19],a[32:35],v[72:75],v[16:19]
v_mfma_f32_16x16x32_bf16 v[20:23],a[36:39],v[72:75],v[20:23]
v_mfma_f32_16x16x32_bf16 v[16:19],a[40:43],v[72:75],v[16:19]
v_mfma_f32_16x16x32_bf16 v[20:23],a[44:47],v[72:75],v[20:23]
s_waitcnt vmcnt(24)
s_barrier
s_add_u32 m0,s88,32768
s_add_u32 s61,s82,5046272
buffer_load_dwordx4 v24,s[40:43],s61 offen offset:0  lds
s_add_u32 m0,s88,33792
ds_read_b64_tr_b16 a[64:65],v32 offset:16384
buffer_load_dwordx4 v25,s[40:43],s61 offen offset:0  lds
s_add_u32 m0,s88,40832
ds_read_b64_tr_b16 a[66:67],v32 offset:16896
buffer_load_dwordx4 v24,s[40:43],s61 offen offset:128  lds
s_add_u32 m0,s88,41856
ds_read_b64_tr_b16 a[68:69],v33 offset:16384
buffer_load_dwordx4 v25,s[40:43],s61 offen offset:128  lds
ds_read_b64_tr_b16 a[70:71],v33 offset:16896
ds_read_b64_tr_b16 a[72:73],v32 offset:20480
ds_read_b64_tr_b16 a[74:75],v32 offset:20992
ds_read_b64_tr_b16 a[76:77],v33 offset:20480
ds_read_b64_tr_b16 a[78:79],v33 offset:20992
s_waitcnt lgkmcnt(8)
v_mfma_f32_16x16x32_bf16 v[16:19],a[48:51],v[72:75],v[16:19]
v_mfma_f32_16x16x32_bf16 v[20:23],a[52:55],v[72:75],v[20:23]
v_mfma_f32_16x16x32_bf16 v[16:19],a[56:59],v[72:75],v[16:19]
v_mfma_f32_16x16x32_bf16 v[20:23],a[60:63],v[72:75],v[20:23]
s_waitcnt vmcnt(24)
s_barrier
s_add_u32 m0,s88,49152
s_add_u32 s61,s82,5505024
buffer_load_dwordx4 v24,s[40:43],s61 offen offset:0  lds
s_add_u32 m0,s88,50176
ds_read_b64_tr_b16 a[80:81],v32 offset:32768
buffer_load_dwordx4 v25,s[40:43],s61 offen offset:0  lds
s_add_u32 m0,s88,57216
ds_read_b64_tr_b16 a[82:83],v32 offset:33280
buffer_load_dwordx4 v24,s[40:43],s61 offen offset:128  lds
s_add_u32 m0,s88,58240
ds_read_b64_tr_b16 a[84:85],v33 offset:32768
buffer_load_dwordx4 v25,s[40:43],s61 offen offset:128  lds
ds_read_b64_tr_b16 a[86:87],v33 offset:33280
ds_read_b64_tr_b16 a[88:89],v32 offset:36864
ds_read_b64_tr_b16 a[90:91],v32 offset:37376
ds_read_b64_tr_b16 a[92:93],v33 offset:36864
ds_read_b64_tr_b16 a[94:95],v33 offset:37376
s_waitcnt lgkmcnt(8)
v_mfma_f32_16x16x32_bf16 v[16:19],a[64:67],v[72:75],v[16:19]
v_mfma_f32_16x16x32_bf16 v[20:23],a[68:71],v[72:75],v[20:23]
v_mfma_f32_16x16x32_bf16 v[16:19],a[72:75],v[72:75],v[16:19]
v_mfma_f32_16x16x32_bf16 v[20:23],a[76:79],v[72:75],v[20:23]
s_waitcnt vmcnt(24)
s_barrier
s_add_u32 m0,s88,65536
s_add_u32 s61,s82,5963776
buffer_load_dwordx4 v24,s[40:43],s61 offen offset:0  lds
s_add_u32 m0,s88,66560
ds_read_b64_tr_b16 a[96:97],v34 offset:0
buffer_load_dwordx4 v25,s[40:43],s61 offen offset:0  lds
s_add_u32 m0,s88,73600
ds_read_b64_tr_b16 a[98:99],v34 offset:512
buffer_load_dwordx4 v24,s[40:43],s61 offen offset:128  lds
s_add_u32 m0,s88,74624
ds_read_b64_tr_b16 a[100:101],v35 offset:0
buffer_load_dwordx4 v25,s[40:43],s61 offen offset:128  lds
ds_read_b64_tr_b16 a[102:103],v35 offset:512
ds_read_b64_tr_b16 a[104:105],v34 offset:4096
ds_read_b64_tr_b16 a[106:107],v34 offset:4608
ds_read_b64_tr_b16 a[108:109],v35 offset:4096
ds_read_b64_tr_b16 a[110:111],v35 offset:4608
s_waitcnt lgkmcnt(8)
v_mfma_f32_16x16x32_bf16 v[16:19],a[80:83],v[72:75],v[16:19]
v_mfma_f32_16x16x32_bf16 v[20:23],a[84:87],v[72:75],v[20:23]
v_mfma_f32_16x16x32_bf16 v[16:19],a[88:91],v[72:75],v[16:19]
v_mfma_f32_16x16x32_bf16 v[20:23],a[92:95],v[72:75],v[20:23]
s_waitcnt vmcnt(24)
s_barrier
s_add_u32 m0,s88,81920
s_add_u32 s61,s82,6422528
buffer_load_dwordx4 v24,s[40:43],s61 offen offset:0  lds
s_add_u32 m0,s88,82944
ds_read_b64_tr_b16 a[112:113],v34 offset:16384
buffer_load_dwordx4 v25,s[40:43],s61 offen offset:0  lds
s_add_u32 m0,s88,89984
ds_read_b64_tr_b16 a[114:115],v34 offset:16896
buffer_load_dwordx4 v24,s[40:43],s61 offen offset:128  lds
s_add_u32 m0,s88,91008
ds_read_b64_tr_b16 a[116:117],v35 offset:16384
buffer_load_dwordx4 v25,s[40:43],s61 offen offset:128  lds
ds_read_b64_tr_b16 a[118:119],v35 offset:16896
ds_read_b64_tr_b16 a[120:121],v34 offset:20480
ds_read_b64_tr_b16 a[122:123],v34 offset:20992
ds_read_b64_tr_b16 a[124:125],v35 offset:20480
ds_read_b64_tr_b16 a[126:127],v35 offset:20992
s_waitcnt lgkmcnt(8)
v_mfma_f32_16x16x32_bf16 v[16:19],a[96:99],v[72:75],v[16:19]
v_mfma_f32_16x16x32_bf16 v[20:23],a[100:103],v[72:75],v[20:23]
v_mfma_f32_16x16x32_bf16 v[16:19],a[104:107],v[72:75],v[16:19]
v_mfma_f32_16x16x32_bf16 v[20:23],a[108:111],v[72:75],v[20:23]
s_waitcnt vmcnt(24)
s_barrier
s_add_u32 m0,s88,98304
s_add_u32 s61,s82,6881280
buffer_load_dwordx4 v24,s[40:43],s61 offen offset:0  lds
s_add_u32 m0,s88,99328
ds_read_b64_tr_b16 a[128:129],v34 offset:32768
buffer_load_dwordx4 v25,s[40:43],s61 offen offset:0  lds
s_add_u32 m0,s88,106368
ds_read_b64_tr_b16 a[130:131],v34 offset:33280
buffer_load_dwordx4 v24,s[40:43],s61 offen offset:128  lds
s_add_u32 m0,s88,107392
ds_read_b64_tr_b16 a[132:133],v35 offset:32768
buffer_load_dwordx4 v25,s[40:43],s61 offen offset:128  lds
ds_read_b64_tr_b16 a[134:135],v35 offset:33280
ds_read_b64_tr_b16 a[136:137],v34 offset:36864
ds_read_b64_tr_b16 a[138:139],v34 offset:37376
ds_read_b64_tr_b16 a[140:141],v35 offset:36864
ds_read_b64_tr_b16 a[142:143],v35 offset:37376
s_waitcnt lgkmcnt(8)
v_mfma_f32_16x16x32_bf16 v[16:19],a[112:115],v[72:75],v[16:19]
v_mfma_f32_16x16x32_bf16 v[20:23],a[116:119],v[72:75],v[20:23]
v_mfma_f32_16x16x32_bf16 v[16:19],a[120:123],v[72:75],v[16:19]
v_mfma_f32_16x16x32_bf16 v[20:23],a[124:127],v[72:75],v[20:23]
s_waitcnt vmcnt(24)
s_barrier
ds_read_b64_tr_b16 a[144:145],v26 offset:0
ds_read_b64_tr_b16 a[146:147],v26 offset:512
ds_read_b64_tr_b16 a[148:149],v27 offset:0
ds_read_b64_tr_b16 a[150:151],v27 offset:512
ds_read_b64_tr_b16 a[152:153],v26 offset:4096
ds_read_b64_tr_b16 a[154:155],v26 offset:4608
ds_read_b64_tr_b16 a[156:157],v27 offset:4096
ds_read_b64_tr_b16 a[158:159],v27 offset:4608
s_waitcnt lgkmcnt(8)
v_mfma_f32_16x16x32_bf16 v[16:19],a[128:131],v[72:75],v[16:19]
v_mfma_f32_16x16x32_bf16 v[20:23],a[132:135],v[72:75],v[20:23]
v_mfma_f32_16x16x32_bf16 v[16:19],a[136:139],v[72:75],v[16:19]
v_mfma_f32_16x16x32_bf16 v[20:23],a[140:143],v[72:75],v[20:23]
s_waitcnt vmcnt(20)
s_barrier
ds_read_b64_tr_b16 a[160:161],v26 offset:16384
ds_read_b64_tr_b16 a[162:163],v26 offset:16896
ds_read_b64_tr_b16 a[164:165],v27 offset:16384
ds_read_b64_tr_b16 a[166:167],v27 offset:16896
ds_read_b64_tr_b16 a[168:169],v26 offset:20480
ds_read_b64_tr_b16 a[170:171],v26 offset:20992
ds_read_b64_tr_b16 a[172:173],v27 offset:20480
ds_read_b64_tr_b16 a[174:175],v27 offset:20992
s_waitcnt lgkmcnt(8)
v_mfma_f32_16x16x32_bf16 v[16:19],a[144:147],v[72:75],v[16:19]
v_mfma_f32_16x16x32_bf16 v[20:23],a[148:151],v[72:75],v[20:23]
v_mfma_f32_16x16x32_bf16 v[16:19],a[152:155],v[72:75],v[16:19]
v_mfma_f32_16x16x32_bf16 v[20:23],a[156:159],v[72:75],v[20:23]
s_waitcnt vmcnt(16)
s_barrier
ds_read_b64_tr_b16 a[176:177],v26 offset:32768
ds_read_b64_tr_b16 a[178:179],v26 offset:33280
ds_read_b64_tr_b16 a[180:181],v27 offset:32768
ds_read_b64_tr_b16 a[182:183],v27 offset:33280
ds_read_b64_tr_b16 a[184:185],v26 offset:36864
ds_read_b64_tr_b16 a[186:187],v26 offset:37376
ds_read_b64_tr_b16 a[188:189],v27 offset:36864
ds_read_b64_tr_b16 a[190:191],v27 offset:37376
s_waitcnt lgkmcnt(8)
v_mfma_f32_16x16x32_bf16 v[16:19],a[160:163],v[72:75],v[16:19]
v_mfma_f32_16x16x32_bf16 v[20:23],a[164:167],v[72:75],v[20:23]
v_mfma_f32_16x16x32_bf16 v[16:19],a[168:171],v[72:75],v[16:19]
v_mfma_f32_16x16x32_bf16 v[20:23],a[172:175],v[72:75],v[20:23]
s_waitcnt vmcnt(12)
s_barrier
ds_read_b64_tr_b16 a[192:193],v32 offset:0
ds_read_b64_tr_b16 a[194:195],v32 offset:512
ds_read_b64_tr_b16 a[196:197],v33 offset:0
ds_read_b64_tr_b16 a[198:199],v33 offset:512
ds_read_b64_tr_b16 a[200:201],v32 offset:4096
ds_read_b64_tr_b16 a[202:203],v32 offset:4608
ds_read_b64_tr_b16 a[204:205],v33 offset:4096
ds_read_b64_tr_b16 a[206:207],v33 offset:4608
s_waitcnt lgkmcnt(8)
v_mfma_f32_16x16x32_bf16 v[16:19],a[176:179],v[72:75],v[16:19]
v_mfma_f32_16x16x32_bf16 v[20:23],a[180:183],v[72:75],v[20:23]
v_mfma_f32_16x16x32_bf16 v[16:19],a[184:187],v[72:75],v[16:19]
v_mfma_f32_16x16x32_bf16 v[20:23],a[188:191],v[72:75],v[20:23]
s_waitcnt vmcnt(8)
s_barrier
ds_read_b64_tr_b16 a[208:209],v32 offset:16384
ds_read_b64_tr_b16 a[210:211],v32 offset:16896
ds_read_b64_tr_b16 a[212:213],v33 offset:16384)"
R"(
ds_read_b64_tr_b16 a[214:215],v33 offset:16896
ds_read_b64_tr_b16 a[216:217],v32 offset:20480
ds_read_b64_tr_b16 a[218:219],v32 offset:20992
ds_read_b64_tr_b16 a[220:221],v33 offset:20480
ds_read_b64_tr_b16 a[222:223],v33 offset:20992
s_waitcnt lgkmcnt(8)
v_mfma_f32_16x16x32_bf16 v[16:19],a[192:195],v[72:75],v[16:19]
v_mfma_f32_16x16x32_bf16 v[20:23],a[196:199],v[72:75],v[20:23]
v_mfma_f32_16x16x32_bf16 v[16:19],a[200:203],v[72:75],v[16:19]
v_mfma_f32_16x16x32_bf16 v[20:23],a[204:207],v[72:75],v[20:23]
s_waitcnt vmcnt(4)
s_barrier
ds_read_b64_tr_b16 a[224:225],v32 offset:32768
ds_read_b64_tr_b16 a[226:227],v32 offset:33280
ds_read_b64_tr_b16 a[228:229],v33 offset:32768
ds_read_b64_tr_b16 a[230:231],v33 offset:33280
ds_read_b64_tr_b16 a[232:233],v32 offset:36864
ds_read_b64_tr_b16 a[234:235],v32 offset:37376
ds_read_b64_tr_b16 a[236:237],v33 offset:36864
ds_read_b64_tr_b16 a[238:239],v33 offset:37376
s_waitcnt lgkmcnt(8)
v_mfma_f32_16x16x32_bf16 v[16:19],a[208:211],v[72:75],v[16:19]
v_mfma_f32_16x16x32_bf16 v[20:23],a[212:215],v[72:75],v[20:23]
v_mfma_f32_16x16x32_bf16 v[16:19],a[216:219],v[72:75],v[16:19]
v_mfma_f32_16x16x32_bf16 v[20:23],a[220:223],v[72:75],v[20:23]
s_waitcnt vmcnt(0)
s_barrier
ds_read_b64_tr_b16 a[240:241],v34 offset:0
ds_read_b64_tr_b16 a[242:243],v34 offset:512
ds_read_b64_tr_b16 a[244:245],v35 offset:0
ds_read_b64_tr_b16 a[246:247],v35 offset:512
ds_read_b64_tr_b16 a[248:249],v34 offset:4096
ds_read_b64_tr_b16 a[250:251],v34 offset:4608
ds_read_b64_tr_b16 a[252:253],v35 offset:4096
ds_read_b64_tr_b16 a[254:255],v35 offset:4608
s_waitcnt lgkmcnt(8)
v_mfma_f32_16x16x32_bf16 v[16:19],a[224:227],v[72:75],v[16:19]
v_mfma_f32_16x16x32_bf16 v[20:23],a[228:231],v[72:75],v[20:23]
v_mfma_f32_16x16x32_bf16 v[16:19],a[232:235],v[72:75],v[16:19]
v_mfma_f32_16x16x32_bf16 v[20:23],a[236:239],v[72:75],v[20:23]
s_waitcnt lgkmcnt(0)
v_mfma_f32_16x16x32_bf16 v[16:19],a[240:243],v[72:75],v[16:19]
v_mfma_f32_16x16x32_bf16 v[20:23],a[244:247],v[72:75],v[20:23]
v_mfma_f32_16x16x32_bf16 v[16:19],a[248:251],v[72:75],v[16:19]
v_mfma_f32_16x16x32_bf16 v[20:23],a[252:255],v[72:75],v[20:23]
s_waitcnt lgkmcnt(0)
s_barrier
v_readfirstlane_b32 s60,v5
s_nop 3
s_add_u32 s60,s60,1
s_cmp_eq_u32 s60,0
s_cselect_b32 s60,1,s60
v_mov_b32 v5,s60
s_mov_b32 s59,536870912
s_add_u32 s57,s56,131072
s_mov_b32 s79,0
s_mov_b32 s75,0x3a800000
s_mov_b32 s76,0xc0135761
s_mov_b32 s77,0x3d372713
s_mov_b32 s44,s59
s_mov_b32 s45,s59
s_mov_b32 s46,s59
s_mov_b32 s47,s59
s_mov_b32 s48,s83
s_add_u32 s49,s48,s55
s_cmp_eq_u32 s49,s56
s_cselect_b32 s49,s57,s49
s_cmp_eq_u32 s49,s58
s_cselect_b32 s49,s59,s49
s_add_u32 s50,s49,s55
s_cmp_eq_u32 s50,s56
s_cselect_b32 s50,s57,s50
s_cmp_eq_u32 s50,s58
s_cselect_b32 s50,s59,s50
s_add_u32 s51,s50,s55
s_cmp_eq_u32 s51,s56
s_cselect_b32 s51,s57,s51
s_cmp_eq_u32 s51,s58
s_cselect_b32 s51,s59,s51
s_add_u32 s52,s51,s55
s_cmp_eq_u32 s52,s56
s_cselect_b32 s52,s57,s52
s_cmp_eq_u32 s52,s58
s_cselect_b32 s52,s59,s52
s_lshl_b32 s63,s54,12
s_add_u32 s63,s63,65536
s_add_u32 s64,s63,32768
s_lshl_b32 s88,s54,10
s_add_u32 s88,s88,131072
s_lshl_b32 s78,s54,11
s_add_u32 s78,s78,132608
v_add_u32 v1,16384,v0
v_mov_b32 v12,0x3727c5ac
s_getpc_b64 s[90:91]
Lg3o_pc_%=:
s_mov_b32 s68,Lg3o_d1w0_%=-Lg3o_pc_%=
s_cmp_eq_u32 s54,1
s_cselect_b32 s68,Lg3o_d1w1_%=-Lg3o_pc_%=,s68
s_cmp_eq_u32 s54,2
s_cselect_b32 s68,Lg3o_d1w2_%=-Lg3o_pc_%=,s68
s_cmp_eq_u32 s54,3
s_cselect_b32 s68,Lg3o_d1w3_%=-Lg3o_pc_%=,s68
s_add_u32 s92,s90,s68
s_addc_u32 s93,s91,0
s_mov_b32 s68,Lg3o_d0w0_%=-Lg3o_pc_%=
s_cmp_eq_u32 s54,1
s_cselect_b32 s68,Lg3o_d0w1_%=-Lg3o_pc_%=,s68
s_cmp_eq_u32 s54,2
s_cselect_b32 s68,Lg3o_d0w2_%=-Lg3o_pc_%=,s68
s_cmp_eq_u32 s54,3
s_cselect_b32 s68,Lg3o_d0w3_%=-Lg3o_pc_%=,s68
s_add_u32 s90,s90,s68
s_addc_u32 s91,s91,0
s_mov_b32 s66,s59
s_mov_b32 s65,0
s_mov_b32 s62,s63
s_branch Lg3o_slow0_%=
Lg3o_d0w0_%=:
v_mfma_f32_16x16x32_bf16 v[24:27],a[0:3],v[40:43],0
s_add_u32 m0,s62,16384
ds_read_b128 v[68:71],v2 offset:7168
v_mul_f32 v96,s75,v92
v_mfma_f32_16x16x32_bf16 v[28:31],a[4:7],v[40:43],0
buffer_load_dwordx4 v1,s[36:39],s66 offen offset:2048 sc1 lds
v_mfma_f32_16x16x32_bf16 v[24:27],a[8:11],v[44:47],v[24:27]
s_lshr_b32 s72,s66,5
ds_read_b128 v[40:43],v2 offset:8192
v_fma_f32 v93,v93,s75,v12
v_mfma_f32_16x16x32_bf16 v[28:31],a[12:15],v[44:47],v[28:31]
s_lshr_b32 s71,s62,2
v_fma_f32 v93,-v96,v96,v93
v_max_f32 v93,v93,v12
v_mfma_f32_16x16x32_bf16 v[24:27],a[16:19],v[48:51],v[24:27]
s_mul_i32 s73,s47,7
ds_read_b128 v[44:47],v2 offset:9216
v_fma_f32 v32,-v96,v16,v32
v_mfma_f32_16x16x32_bf16 v[28:31],a[20:23],v[48:51],v[28:31]
buffer_load_dwordx4 v1,s[36:39],s66 offen offset:3072 sc1 lds
v_mfma_f32_16x16x32_bf16 v[24:27],a[24:27],v[52:55],v[24:27]
s_add_u32 m0,s71,s88
ds_read_b128 v[48:51],v2 offset:10240
v_fma_f32 v33,-v96,v17,v33
v_mfma_f32_16x16x32_bf16 v[28:31],a[28:31],v[52:55],v[28:31]
s_waitcnt lgkmcnt(3)
buffer_load_dword v9,s[36:39],s72 offen offset:0 sc1 lds
v_mfma_f32_16x16x32_bf16 v[24:27],a[32:35],v[56:59],v[24:27]
s_lshr_b32 s73,s73,1
ds_read_b128 v[52:55],v2 offset:11264
v_fma_f32 v34,-v96,v18,v34
v_mfma_f32_16x16x32_bf16 v[28:31],a[36:39],v[56:59],v[28:31]
s_lshr_b32 s68,s50,5
v_rsq_f32 v92,v93
v_mfma_f32_16x16x32_bf16 v[24:27],a[40:43],v[60:63],v[24:27]
s_add_u32 m0,s78,0
ds_read_b128 v[56:59],v2 offset:12288
v_mul_f32 v97,s76,v92
v_mfma_f32_16x16x32_bf16 v[28:31],a[44:47],v[60:63],v[28:31]
s_xor_b32 s69,s64,65536
v_mul_f32 v98,v92,v92
v_mul_f32 v99,v93,v92
v_mfma_f32_16x16x32_bf16 v[24:27],a[48:51],v[64:67],v[24:27]
s_cmp_ge_u32 s66,s59
ds_read_b128 v[60:63],v2 offset:13312
v_mul_f32 v98,s77,v98
v_mfma_f32_16x16x32_bf16 v[28:31],a[52:55],v[64:67],v[28:31]
s_cselect_b32 s62,s69,s63
v_mul_f32 v98,v98,v97
v_fma_f32 v35,-v96,v19,v35
v_mfma_f32_16x16x32_bf16 v[24:27],a[56:59],v[68:71],v[24:27]
s_cselect_b32 s86,s49,s50
ds_read_b128 v[64:67],v2 offset:14336
v_mul_f32 v72,v32,v32
v_mfma_f32_16x16x32_bf16 v[28:31],a[60:63],v[68:71],v[28:31]
s_waitcnt lgkmcnt(3)
v_mul_f32 v73,v33,v33
v_mul_f32 v74,v34,v34
v_mfma_f32_16x16x32_bf16 v[24:27],a[64:67],v[40:43],v[24:27]
s_waitcnt vmcnt(10)
ds_read_b128 v[68:71],v2 offset:15360
v_mul_f32 v75,v35,v35
v_mfma_f32_16x16x32_bf16 v[28:31],a[68:71],v[40:43],v[28:31]
s_barrier
v_mfma_f32_16x16x32_bf16 v[24:27],a[72:75],v[44:47],v[24:27]
ds_read_b128 v[40:43],v2 offset:16384
ds_read_b32 v13,v7 offset:256
v_mfma_f32_16x16x32_bf16 v[28:31],a[76:79],v[44:47],v[28:31]
buffer_load_dword v6,s[36:39],s68 offen offset:0 sc1 lds
v_mfma_f32_16x16x32_bf16 v[24:27],a[80:83],v[48:51],v[24:27]
s_mov_b32 m0,s62
ds_read_b128 v[44:47],v2 offset:17408
v_fma_f32 v72,v98,v72,v97
v_mfma_f32_16x16x32_bf16 v[28:31],a[84:87],v[48:51],v[28:31]
v_mul_f32 v72,v32,v72
v_fma_f32 v73,v98,v73,v97
v_mfma_f32_16x16x32_bf16 v[24:27],a[88:91],v[52:55],v[24:27]
s_waitcnt lgkmcnt(1)
ds_read_b128 v[48:51],v2 offset:18432
ds_read_b32 v88,v8 offset:0
v_mfma_f32_16x16x32_bf16 v[28:31],a[92:95],v[52:55],v[28:31]
v_cmp_eq_u32 vcc,s60,v13)"
R"(
v_exp_f32 v72,v72
v_mul_f32 v73,v33,v73
v_mfma_f32_16x16x32_bf16 v[24:27],a[96:99],v[56:59],v[24:27]
s_cselect_b32 s87,vcc_lo,vcc_hi
ds_read_b128 v[52:55],v2 offset:19456
v_fma_f32 v72,v72,v99,v99
v_mfma_f32_16x16x32_bf16 v[28:31],a[100:103],v[56:59],v[28:31]
s_cmp_eq_u32 s87,-1
v_exp_f32 v73,v73
v_mfma_f32_16x16x32_bf16 v[24:27],a[104:107],v[60:63],v[24:27]
s_cselect_b32 s67,s86,s59
ds_read_b128 v[56:59],v2 offset:20480
v_fma_f32 v73,v73,v99,v99
v_mfma_f32_16x16x32_bf16 v[28:31],a[108:111],v[60:63],v[28:31]
buffer_load_dwordx4 v0,s[36:39],s67 offen offset:0 sc1 lds
v_mfma_f32_16x16x32_bf16 v[24:27],a[112:115],v[64:67],v[24:27]
s_waitcnt lgkmcnt(2)
ds_read_b128 v[60:63],v2 offset:21504
v_fma_f32 v74,v98,v74,v97
v_mfma_f32_16x16x32_bf16 v[28:31],a[116:119],v[64:67],v[28:31]
s_xor_b32 s63,s63,65536
v_rcp_f32 v72,v72
v_mfma_f32_16x16x32_bf16 v[24:27],a[120:123],v[68:71],v[24:27]
s_add_u32 s53,s52,s55
ds_read_b128 v[64:67],v2 offset:22528
v_mul_f32 v74,v34,v74
v_mfma_f32_16x16x32_bf16 v[28:31],a[124:127],v[68:71],v[28:31]
s_waitcnt lgkmcnt(3)
buffer_load_dwordx4 v0,s[36:39],s67 offen offset:1024 sc1 lds
v_mfma_f32_16x16x32_bf16 v[24:27],a[128:131],v[40:43],v[24:27]
s_cmp_eq_u32 s53,s56
ds_read_b128 v[68:71],v2 offset:23552
v_add_f32_dpp v88,v88,v88 quad_perm:[1,0,3,2] row_mask:0xf bank_mask:0xf
v_mfma_f32_16x16x32_bf16 v[28:31],a[132:135],v[40:43],v[28:31]
s_cselect_b32 s53,s57,s53
v_exp_f32 v74,v74
v_mfma_f32_16x16x32_bf16 v[24:27],a[136:139],v[44:47],v[24:27]
s_cmp_eq_u32 s53,s58
ds_read_b128 v[40:43],v2 offset:24576
v_fma_f32 v74,v74,v99,v99
v_mfma_f32_16x16x32_bf16 v[28:31],a[140:143],v[44:47],v[28:31]
buffer_load_dwordx4 v0,s[36:39],s67 offen offset:2048 sc1 lds
v_mfma_f32_16x16x32_bf16 v[24:27],a[144:147],v[48:51],v[24:27]
s_cselect_b32 s53,s59,s53
ds_read_b128 v[44:47],v2 offset:25600
v_add_f32_dpp v88,v88,v88 quad_perm:[2,3,0,1] row_mask:0xf bank_mask:0xf
v_mfma_f32_16x16x32_bf16 v[28:31],a[148:151],v[48:51],v[28:31]
v_rcp_f32 v73,v73
v_mfma_f32_16x16x32_bf16 v[24:27],a[152:155],v[52:55],v[24:27]
ds_read_b128 v[48:51],v2 offset:26624
v_add_f32_dpp v88,v88,v88 row_half_mirror row_mask:0xf bank_mask:0xf
v_mfma_f32_16x16x32_bf16 v[28:31],a[156:159],v[52:55],v[28:31]
s_waitcnt lgkmcnt(3)
buffer_load_dwordx4 v0,s[36:39],s67 offen offset:3072 sc1 lds
v_mfma_f32_16x16x32_bf16 v[24:27],a[160:163],v[56:59],v[24:27]
s_add_u32 m0,s62,16384
ds_write_b32 v10,v88 offset:0
v_mfma_f32_16x16x32_bf16 v[28:31],a[164:167],v[56:59],v[28:31]
s_cmp_ge_u32 s66,s59
ds_read_b128 v[52:55],v2 offset:27648
v_fma_f32 v75,v98,v75,v97
v_mfma_f32_16x16x32_bf16 v[24:27],a[168:171],v[60:63],v[24:27]
ds_read_b128 v[56:59],v2 offset:28672
v_mul_f32 v75,v35,v75
v_mfma_f32_16x16x32_bf16 v[28:31],a[172:175],v[60:63],v[28:31]
buffer_load_dwordx4 v1,s[36:39],s67 offen offset:0 sc1 lds
v_mfma_f32_16x16x32_bf16 v[24:27],a[176:179],v[64:67],v[24:27]
ds_read_b128 v[60:63],v2 offset:29696
v_mul_f32 v32,v32,v72
v_mfma_f32_16x16x32_bf16 v[28:31],a[180:183],v[64:67],v[28:31]
v_exp_f32 v75,v75
v_mfma_f32_16x16x32_bf16 v[24:27],a[184:187],v[68:71],v[24:27]
ds_read_b128 v[64:67],v2 offset:30720
v_fma_f32 v75,v75,v99,v99
v_mfma_f32_16x16x32_bf16 v[28:31],a[188:191],v[68:71],v[28:31]
s_waitcnt lgkmcnt(3)
buffer_load_dwordx4 v1,s[36:39],s67 offen offset:1024 sc1 lds
v_mfma_f32_16x16x32_bf16 v[24:27],a[192:195],v[40:43],v[24:27]
s_waitcnt vmcnt(13)
ds_read_b128 v[68:71],v2 offset:31744
v_mul_f32 v33,v33,v73
v_mfma_f32_16x16x32_bf16 v[28:31],a[196:199],v[40:43],v[28:31]
s_barrier
v_mfma_f32_16x16x32_bf16 v[24:27],a[200:203],v[44:47],v[24:27]
ds_read_b128 v[40:43],v2 offset:32768
ds_read_b64 v[92:93],v11 offset:0
v_mfma_f32_16x16x32_bf16 v[28:31],a[204:207],v[44:47],v[28:31]
v_rcp_f32 v74,v74
v_mfma_f32_16x16x32_bf16 v[24:27],a[208:211],v[48:51],v[24:27]
ds_read_b128 v[44:47],v2 offset:33792
v_mul_f32 v34,v34,v74
v_mfma_f32_16x16x32_bf16 v[28:31],a[212:215],v[48:51],v[28:31]
v_rcp_f32 v75,v75
v_mfma_f32_16x16x32_bf16 v[24:27],a[216:219],v[52:55],v[24:27]
s_waitcnt lgkmcnt(1)
ds_read_b128 v[48:51],v2 offset:34816
v_mul_f32 v35,v35,v75
v_mfma_f32_16x16x32_bf16 v[28:31],a[220:223],v[52:55],v[28:31]
v_cvt_pk_bf16_f32 v80,v32,v33
v_cvt_pk_bf16_f32 v81,v34,v35
v_mfma_f32_16x16x32_bf16 v[24:27],a[224:227],v[56:59],v[24:27]
v_fma_f32 v36,-v96,v20,v36
v_fma_f32 v37,-v96,v21,v37
v_fma_f32 v38,-v96,v22,v38
v_fma_f32 v39,-v96,v23,v39
v_mul_f32 v76,v36,v36
v_mul_f32 v77,v37,v37
v_mul_f32 v78,v38,v38
v_mul_f32 v79,v39,v39
v_fma_f32 v76,v98,v76,v97
v_fma_f32 v77,v98,v77,v97
v_fma_f32 v78,v98,v78,v97
v_fma_f32 v79,v98,v79,v97
v_mul_f32 v76,v36,v76
v_mul_f32 v77,v37,v77
v_mul_f32 v78,v38,v78
v_mul_f32 v79,v39,v79
v_exp_f32 v76,v76
v_exp_f32 v77,v77
v_exp_f32 v78,v78
v_exp_f32 v79,v79
v_mfma_f32_16x16x32_bf16 v[28:31],a[228:231],v[56:59],v[28:31]
ds_read_b128 v[52:55],v2 offset:35840
v_fma_f32 v76,v76,v99,v99
v_fma_f32 v77,v77,v99,v99
v_fma_f32 v78,v78,v99,v99
v_fma_f32 v79,v79,v99,v99
v_rcp_f32 v76,v76
v_rcp_f32 v77,v77
v_rcp_f32 v78,v78
v_rcp_f32 v79,v79
v_mfma_f32_16x16x32_bf16 v[24:27],a[232:235],v[60:63],v[24:27]
v_mul_f32 v36,v36,v76
v_mul_f32 v37,v37,v77
v_mul_f32 v38,v38,v78
v_mul_f32 v39,v39,v79
v_cvt_pk_bf16_f32 v82,v36,v37
v_cvt_pk_bf16_f32 v83,v38,v39
v_mfma_f32_16x16x32_bf16 v[28:31],a[236:239],v[60:63],v[28:31]
ds_read_b128 v[56:59],v2 offset:36864
buffer_store_dwordx4 v[80:83],v3,s[36:39],s73 offen offset:0 sc0 sc1
v_mfma_f32_16x16x32_bf16 v[24:27],a[240:243],v[64:67],v[24:27]
ds_read_b128 v[60:63],v2 offset:37888
v_mfma_f32_16x16x32_bf16 v[28:31],a[244:247],v[64:67],v[28:31]
v_mfma_f32_16x16x32_bf16 v[24:27],a[248:251],v[68:71],v[24:27]
s_waitcnt lgkmcnt(2)
ds_read_b128 v[64:67],v2 offset:38912
v_mfma_f32_16x16x32_bf16 v[28:31],a[252:255],v[68:71],v[28:31]
s_cbranch_scc1 Lg3o_slow1_%=
Lg3o_d1w0_%=:
v_mfma_f32_16x16x32_bf16 v[32:35],a[0:3],v[40:43],0
s_add_u32 m0,s62,16384
ds_read_b128 v[68:71],v2 offset:39936
v_mul_f32 v96,s75,v92
v_mfma_f32_16x16x32_bf16 v[36:39],a[4:7],v[40:43],0
buffer_load_dwordx4 v1,s[36:39],s67 offen offset:2048 sc1 lds
v_mfma_f32_16x16x32_bf16 v[32:35],a[8:11],v[44:47],v[32:35]
s_lshr_b32 s72,s67,5
ds_read_b128 v[40:43],v2 offset:40960
v_fma_f32 v93,v93,s75,v12
v_mfma_f32_16x16x32_bf16 v[36:39],a[12:15],v[44:47],v[36:39]
s_lshr_b32 s71,s62,2
v_fma_f32 v93,-v96,v96,v93
v_max_f32 v93,v93,v12
v_mfma_f32_16x16x32_bf16 v[32:35],a[16:19],v[48:51],v[32:35]
s_mul_i32 s73,s48,7
ds_read_b128 v[44:47],v2 offset:41984
v_fma_f32 v24,-v96,v16,v24
v_mfma_f32_16x16x32_bf16 v[36:39],a[20:23],v[48:51],v[36:39]
buffer_load_dwordx4 v1,s[36:39],s67 offen offset:3072 sc1 lds
v_mfma_f32_16x16x32_bf16 v[32:35],a[24:27],v[52:55],v[32:35]
s_add_u32 m0,s71,s88
ds_read_b128 v[48:51],v2 offset:43008
v_fma_f32 v25,-v96,v17,v25
v_mfma_f32_16x16x32_bf16 v[36:39],a[28:31],v[52:55],v[36:39]
s_waitcnt lgkmcnt(3)
buffer_load_dword v9,s[36:39],s72 offen offset:0 sc1 lds
v_mfma_f32_16x16x32_bf16 v[32:35],a[32:35],v[56:59],v[32:35]
s_lshr_b32 s73,s73,1
ds_read_b128 v[52:55],v2 offset:44032
v_fma_f32 v26,-v96,v18,v26
v_mfma_f32_16x16x32_bf16 v[36:39],a[36:39],v[56:59],v[36:39]
s_lshr_b32 s68,s51,5
v_rsq_f32 v92,v93
v_mfma_f32_16x16x32_bf16 v[32:35],a[40:43],v[60:63],v[32:35]
s_add_u32 m0,s78,256
ds_read_b128 v[56:59],v2 offset:45056
v_mul_f32 v97,s76,v92
v_mfma_f32_16x16x32_bf16 v[36:39],a[44:47],v[60:63],v[36:39]
s_xor_b32 s69,s63,65536
v_mul_f32 v98,v92,v92
v_mul_f32 v99,v93,v92
v_mfma_f32_16x16x32_bf16 v[32:35],a[48:51],v[64:67],v[32:35]
s_cmp_ge_u32 s67,s59
ds_read_b128 v[60:63],v2 offset:46080
v_mul_f32 v98,s77,v98
v_mfma_f32_16x16x32_bf16 v[36:39],a[52:55],v[64:67],v[36:39]
s_cselect_b32 s62,s69,s64
v_mul_f32 v98,v98,v97
v_fma_f32 v27,-v96,v19,v27)"
R"(
v_mfma_f32_16x16x32_bf16 v[32:35],a[56:59],v[68:71],v[32:35]
s_cselect_b32 s86,s50,s51
ds_read_b128 v[64:67],v2 offset:47104
v_mul_f32 v72,v24,v24
v_mfma_f32_16x16x32_bf16 v[36:39],a[60:63],v[68:71],v[36:39]
s_waitcnt lgkmcnt(3)
v_mul_f32 v73,v25,v25
v_mul_f32 v74,v26,v26
v_mfma_f32_16x16x32_bf16 v[32:35],a[64:67],v[40:43],v[32:35]
s_waitcnt vmcnt(10)
ds_read_b128 v[68:71],v2 offset:48128
v_mul_f32 v75,v27,v27
v_mfma_f32_16x16x32_bf16 v[36:39],a[68:71],v[40:43],v[36:39]
s_barrier
v_mfma_f32_16x16x32_bf16 v[32:35],a[72:75],v[44:47],v[32:35]
s_mov_b64 s[44:45],s[46:47]
ds_read_b128 v[40:43],v2 offset:49152
ds_read_b32 v13,v7 offset:0
v_mfma_f32_16x16x32_bf16 v[36:39],a[76:79],v[44:47],v[36:39]
buffer_load_dword v6,s[36:39],s68 offen offset:0 sc1 lds
v_mfma_f32_16x16x32_bf16 v[32:35],a[80:83],v[48:51],v[32:35]
s_mov_b32 m0,s62
ds_read_b128 v[44:47],v2 offset:50176
v_fma_f32 v72,v98,v72,v97
v_mfma_f32_16x16x32_bf16 v[36:39],a[84:87],v[48:51],v[36:39]
s_mov_b64 s[46:47],s[48:49]
v_mul_f32 v72,v24,v72
v_fma_f32 v73,v98,v73,v97
v_mfma_f32_16x16x32_bf16 v[32:35],a[88:91],v[52:55],v[32:35]
s_waitcnt lgkmcnt(1)
ds_read_b128 v[48:51],v2 offset:51200
ds_read_b32 v88,v8 offset:8192
v_mfma_f32_16x16x32_bf16 v[36:39],a[92:95],v[52:55],v[36:39]
v_cmp_eq_u32 vcc,s60,v13
v_exp_f32 v72,v72
v_mul_f32 v73,v25,v73
v_mfma_f32_16x16x32_bf16 v[32:35],a[96:99],v[56:59],v[32:35]
s_cselect_b32 s87,vcc_lo,vcc_hi
ds_read_b128 v[52:55],v2 offset:52224
v_fma_f32 v72,v72,v99,v99
v_mfma_f32_16x16x32_bf16 v[36:39],a[100:103],v[56:59],v[36:39]
s_cmp_eq_u32 s87,-1
v_exp_f32 v73,v73
v_mfma_f32_16x16x32_bf16 v[32:35],a[104:107],v[60:63],v[32:35]
s_cselect_b32 s66,s86,s59
ds_read_b128 v[56:59],v2 offset:53248
v_fma_f32 v73,v73,v99,v99
v_mfma_f32_16x16x32_bf16 v[36:39],a[108:111],v[60:63],v[36:39]
buffer_load_dwordx4 v0,s[36:39],s66 offen offset:0 sc1 lds
v_mfma_f32_16x16x32_bf16 v[32:35],a[112:115],v[64:67],v[32:35]
s_waitcnt lgkmcnt(2)
ds_read_b128 v[60:63],v2 offset:54272
v_fma_f32 v74,v98,v74,v97
v_mfma_f32_16x16x32_bf16 v[36:39],a[116:119],v[64:67],v[36:39]
s_xor_b32 s64,s64,65536
v_rcp_f32 v72,v72
v_mfma_f32_16x16x32_bf16 v[32:35],a[120:123],v[68:71],v[32:35]
s_add_u32 s74,s53,s55
ds_read_b128 v[64:67],v2 offset:55296
v_mul_f32 v74,v26,v74
v_mfma_f32_16x16x32_bf16 v[36:39],a[124:127],v[68:71],v[36:39]
s_waitcnt lgkmcnt(3)
buffer_load_dwordx4 v0,s[36:39],s66 offen offset:1024 sc1 lds
v_mfma_f32_16x16x32_bf16 v[32:35],a[128:131],v[40:43],v[32:35]
s_cmp_eq_u32 s74,s56
ds_read_b128 v[68:71],v2 offset:56320
v_add_f32_dpp v88,v88,v88 quad_perm:[1,0,3,2] row_mask:0xf bank_mask:0xf
v_mfma_f32_16x16x32_bf16 v[36:39],a[132:135],v[40:43],v[36:39]
s_cselect_b32 s74,s57,s74
v_exp_f32 v74,v74
v_mfma_f32_16x16x32_bf16 v[32:35],a[136:139],v[44:47],v[32:35]
s_cmp_eq_u32 s74,s58
ds_read_b128 v[40:43],v2 offset:57344
v_fma_f32 v74,v74,v99,v99
v_mfma_f32_16x16x32_bf16 v[36:39],a[140:143],v[44:47],v[36:39]
buffer_load_dwordx4 v0,s[36:39],s66 offen offset:2048 sc1 lds
v_mfma_f32_16x16x32_bf16 v[32:35],a[144:147],v[48:51],v[32:35]
s_cselect_b32 s74,s59,s74
ds_read_b128 v[44:47],v2 offset:58368
v_add_f32_dpp v88,v88,v88 quad_perm:[2,3,0,1] row_mask:0xf bank_mask:0xf
v_mfma_f32_16x16x32_bf16 v[36:39],a[148:151],v[48:51],v[36:39]
s_mov_b64 s[48:49],s[50:51]
v_rcp_f32 v73,v73
v_mfma_f32_16x16x32_bf16 v[32:35],a[152:155],v[52:55],v[32:35]
s_mov_b64 s[50:51],s[52:53]
ds_read_b128 v[48:51],v2 offset:59392
v_add_f32_dpp v88,v88,v88 row_half_mirror row_mask:0xf bank_mask:0xf
v_mfma_f32_16x16x32_bf16 v[36:39],a[156:159],v[52:55],v[36:39]
s_waitcnt lgkmcnt(3)
buffer_load_dwordx4 v0,s[36:39],s66 offen offset:3072 sc1 lds
v_mfma_f32_16x16x32_bf16 v[32:35],a[160:163],v[56:59],v[32:35]
s_add_u32 m0,s62,16384
ds_write_b32 v10,v88 offset:128
v_mfma_f32_16x16x32_bf16 v[36:39],a[164:167],v[56:59],v[36:39]
s_mov_b32 s52,s74
ds_read_b128 v[52:55],v2 offset:60416
v_fma_f32 v75,v98,v75,v97
v_mfma_f32_16x16x32_bf16 v[32:35],a[168:171],v[60:63],v[32:35]
s_cmp_lt_u32 s67,s59
ds_read_b128 v[56:59],v2 offset:61440
v_mul_f32 v75,v27,v75
v_mfma_f32_16x16x32_bf16 v[36:39],a[172:175],v[60:63],v[36:39]
buffer_load_dwordx4 v1,s[36:39],s66 offen offset:0 sc1 lds
v_mfma_f32_16x16x32_bf16 v[32:35],a[176:179],v[64:67],v[32:35]
ds_read_b128 v[60:63],v2 offset:62464
v_mul_f32 v24,v24,v72
v_mfma_f32_16x16x32_bf16 v[36:39],a[180:183],v[64:67],v[36:39]
v_exp_f32 v75,v75
v_mfma_f32_16x16x32_bf16 v[32:35],a[184:187],v[68:71],v[32:35]
ds_read_b128 v[64:67],v2 offset:63488
v_fma_f32 v75,v75,v99,v99
v_mfma_f32_16x16x32_bf16 v[36:39],a[188:191],v[68:71],v[36:39]
s_waitcnt lgkmcnt(3)
buffer_load_dwordx4 v1,s[36:39],s66 offen offset:1024 sc1 lds
v_mfma_f32_16x16x32_bf16 v[32:35],a[192:195],v[40:43],v[32:35]
s_waitcnt vmcnt(13)
ds_read_b128 v[68:71],v2 offset:64512
v_xor_b32 v2,65536,v2
v_mfma_f32_16x16x32_bf16 v[36:39],a[196:199],v[40:43],v[36:39]
s_barrier
v_mfma_f32_16x16x32_bf16 v[32:35],a[200:203],v[44:47],v[32:35]
ds_read_b128 v[40:43],v2 offset:0
ds_read_b64 v[92:93],v11 offset:128
v_mfma_f32_16x16x32_bf16 v[36:39],a[204:207],v[44:47],v[36:39]
v_rcp_f32 v74,v74
v_mfma_f32_16x16x32_bf16 v[32:35],a[208:211],v[48:51],v[32:35]
ds_read_b128 v[44:47],v2 offset:1024
v_mul_f32 v25,v25,v73
v_mfma_f32_16x16x32_bf16 v[36:39],a[212:215],v[48:51],v[36:39]
v_rcp_f32 v75,v75
v_mfma_f32_16x16x32_bf16 v[32:35],a[216:219],v[52:55],v[32:35]
s_waitcnt lgkmcnt(1)
ds_read_b128 v[48:51],v2 offset:2048
v_mul_f32 v26,v26,v74
v_mfma_f32_16x16x32_bf16 v[36:39],a[220:223],v[52:55],v[36:39]
v_mul_f32 v27,v27,v75
v_cvt_pk_bf16_f32 v80,v24,v25
v_mfma_f32_16x16x32_bf16 v[32:35],a[224:227],v[56:59],v[32:35]
v_fma_f32 v28,-v96,v20,v28
v_fma_f32 v29,-v96,v21,v29
v_fma_f32 v30,-v96,v22,v30
v_fma_f32 v31,-v96,v23,v31
v_mul_f32 v76,v28,v28
v_mul_f32 v77,v29,v29
v_mul_f32 v78,v30,v30
v_mul_f32 v79,v31,v31
v_fma_f32 v76,v98,v76,v97
v_fma_f32 v77,v98,v77,v97
v_fma_f32 v78,v98,v78,v97
v_fma_f32 v79,v98,v79,v97
v_mul_f32 v76,v28,v76
v_mul_f32 v77,v29,v77
v_mul_f32 v78,v30,v78
v_mul_f32 v79,v31,v79
v_exp_f32 v76,v76
v_exp_f32 v77,v77
v_exp_f32 v78,v78
v_exp_f32 v79,v79
v_mfma_f32_16x16x32_bf16 v[36:39],a[228:231],v[56:59],v[36:39]
ds_read_b128 v[52:55],v2 offset:3072
v_fma_f32 v76,v76,v99,v99
v_fma_f32 v77,v77,v99,v99
v_fma_f32 v78,v78,v99,v99
v_fma_f32 v79,v79,v99,v99
v_rcp_f32 v76,v76
v_rcp_f32 v77,v77
v_rcp_f32 v78,v78
v_rcp_f32 v79,v79
v_mfma_f32_16x16x32_bf16 v[32:35],a[232:235],v[60:63],v[32:35]
v_cvt_pk_bf16_f32 v81,v26,v27
v_mul_f32 v28,v28,v76
v_mul_f32 v29,v29,v77
v_mul_f32 v30,v30,v78
v_mul_f32 v31,v31,v79
v_cvt_pk_bf16_f32 v82,v28,v29
v_cvt_pk_bf16_f32 v83,v30,v31
v_mfma_f32_16x16x32_bf16 v[36:39],a[236:239],v[60:63],v[36:39]
ds_read_b128 v[56:59],v2 offset:4096
buffer_store_dwordx4 v[80:83],v3,s[36:39],s73 offen offset:0 sc0 sc1
v_xor_b32 v8,16384,v8
v_mfma_f32_16x16x32_bf16 v[32:35],a[240:243],v[64:67],v[32:35]
ds_read_b128 v[60:63],v2 offset:5120
v_mfma_f32_16x16x32_bf16 v[36:39],a[244:247],v[64:67],v[36:39]
v_mfma_f32_16x16x32_bf16 v[32:35],a[248:251],v[68:71],v[32:35]
s_waitcnt lgkmcnt(2)
ds_read_b128 v[64:67],v2 offset:6144
v_mfma_f32_16x16x32_bf16 v[36:39],a[252:255],v[68:71],v[36:39]
s_cbranch_scc1 Lg3o_d0w0_%=
s_branch Lg3o_slow0_%=
Lg3o_d0w1_%=:
v_mfma_f32_16x16x32_bf16 v[24:27],a[0:3],v[40:43],0
s_add_u32 m0,s62,16384
ds_read_b128 v[68:71],v2 offset:7168
v_mul_f32 v96,s75,v92
v_mfma_f32_16x16x32_bf16 v[28:31],a[4:7],v[40:43],0
s_lshr_b32 s72,s66,5
v_fma_f32 v93,v93,s75,v12
v_fma_f32 v93,-v96,v96,v93
v_mfma_f32_16x16x32_bf16 v[24:27],a[8:11],v[44:47],v[24:27]
buffer_load_dwordx4 v1,s[36:39],s66 offen offset:2048 sc1 lds)"
R"(
v_mfma_f32_16x16x32_bf16 v[28:31],a[12:15],v[44:47],v[28:31]
s_lshr_b32 s71,s62,2
ds_read_b128 v[40:43],v2 offset:8192
v_max_f32 v93,v93,v12
v_mfma_f32_16x16x32_bf16 v[24:27],a[16:19],v[48:51],v[24:27]
s_mul_i32 s73,s47,7
ds_read_b128 v[44:47],v2 offset:9216
v_fma_f32 v32,-v96,v16,v32
v_mfma_f32_16x16x32_bf16 v[28:31],a[20:23],v[48:51],v[28:31]
v_rsq_f32 v92,v93
v_mfma_f32_16x16x32_bf16 v[24:27],a[24:27],v[52:55],v[24:27]
buffer_load_dwordx4 v1,s[36:39],s66 offen offset:3072 sc1 lds
v_mfma_f32_16x16x32_bf16 v[28:31],a[28:31],v[52:55],v[28:31]
s_waitcnt lgkmcnt(2)
ds_read_b128 v[48:51],v2 offset:10240
v_mul_f32 v97,s76,v92
v_mfma_f32_16x16x32_bf16 v[24:27],a[32:35],v[56:59],v[24:27]
s_add_u32 m0,s71,s88
ds_read_b128 v[52:55],v2 offset:11264
v_mul_f32 v98,v92,v92
v_mfma_f32_16x16x32_bf16 v[28:31],a[36:39],v[56:59],v[28:31]
buffer_load_dword v9,s[36:39],s72 offen offset:0 sc1 lds
v_mfma_f32_16x16x32_bf16 v[24:27],a[40:43],v[60:63],v[24:27]
s_lshr_b32 s73,s73,1
ds_read_b128 v[56:59],v2 offset:12288
v_mul_f32 v99,v93,v92
v_mfma_f32_16x16x32_bf16 v[28:31],a[44:47],v[60:63],v[28:31]
s_lshr_b32 s68,s51,5
v_mul_f32 v98,s77,v98
v_mul_f32 v98,v98,v97
v_mfma_f32_16x16x32_bf16 v[24:27],a[48:51],v[64:67],v[24:27]
s_add_u32 m0,s78,0
ds_read_b128 v[60:63],v2 offset:13312
v_fma_f32 v33,-v96,v17,v33
v_mfma_f32_16x16x32_bf16 v[28:31],a[52:55],v[64:67],v[28:31]
s_xor_b32 s69,s64,65536
v_fma_f32 v34,-v96,v18,v34
v_fma_f32 v35,-v96,v19,v35
v_mfma_f32_16x16x32_bf16 v[24:27],a[56:59],v[68:71],v[24:27]
s_cmp_ge_u32 s66,s59
ds_read_b128 v[64:67],v2 offset:14336
v_mul_f32 v72,v32,v32
v_mfma_f32_16x16x32_bf16 v[28:31],a[60:63],v[68:71],v[28:31]
s_waitcnt lgkmcnt(3)
v_mul_f32 v73,v33,v33
v_mul_f32 v74,v34,v34
v_mfma_f32_16x16x32_bf16 v[24:27],a[64:67],v[40:43],v[24:27]
s_waitcnt vmcnt(10)
ds_read_b128 v[68:71],v2 offset:15360
v_mul_f32 v75,v35,v35
v_mfma_f32_16x16x32_bf16 v[28:31],a[68:71],v[40:43],v[28:31]
s_barrier
v_mfma_f32_16x16x32_bf16 v[24:27],a[72:75],v[44:47],v[24:27]
s_cselect_b32 s62,s69,s63
ds_read_b128 v[40:43],v2 offset:16384
ds_read_b32 v13,v7 offset:256
v_mfma_f32_16x16x32_bf16 v[28:31],a[76:79],v[44:47],v[28:31]
s_cselect_b32 s86,s49,s50
v_fma_f32 v72,v98,v72,v97
v_mul_f32 v72,v32,v72
v_mfma_f32_16x16x32_bf16 v[24:27],a[80:83],v[48:51],v[24:27]
ds_read_b128 v[44:47],v2 offset:17408
v_fma_f32 v73,v98,v73,v97
v_mfma_f32_16x16x32_bf16 v[28:31],a[84:87],v[48:51],v[28:31]
buffer_load_dword v6,s[36:39],s68 offen offset:0 sc1 lds
v_mfma_f32_16x16x32_bf16 v[24:27],a[88:91],v[52:55],v[24:27]
s_waitcnt lgkmcnt(1)
ds_read_b128 v[48:51],v2 offset:18432
ds_read_b32 v88,v8 offset:0
v_mfma_f32_16x16x32_bf16 v[28:31],a[92:95],v[52:55],v[28:31]
v_cmp_eq_u32 vcc,s60,v13
v_exp_f32 v72,v72
v_mul_f32 v73,v33,v73
v_mfma_f32_16x16x32_bf16 v[24:27],a[96:99],v[56:59],v[24:27]
s_mov_b32 m0,s62
ds_read_b128 v[52:55],v2 offset:19456
v_fma_f32 v72,v72,v99,v99
v_mfma_f32_16x16x32_bf16 v[28:31],a[100:103],v[56:59],v[28:31]
s_cselect_b32 s87,vcc_lo,vcc_hi
v_exp_f32 v73,v73
v_mfma_f32_16x16x32_bf16 v[24:27],a[104:107],v[60:63],v[24:27]
s_cmp_eq_u32 s87,-1
ds_read_b128 v[56:59],v2 offset:20480
v_fma_f32 v73,v73,v99,v99
v_mfma_f32_16x16x32_bf16 v[28:31],a[108:111],v[60:63],v[28:31]
s_cselect_b32 s67,s86,s59
v_rcp_f32 v72,v72
v_mfma_f32_16x16x32_bf16 v[24:27],a[112:115],v[64:67],v[24:27]
s_waitcnt lgkmcnt(2)
buffer_load_dwordx4 v0,s[36:39],s67 offen offset:0 sc1 lds
v_mfma_f32_16x16x32_bf16 v[28:31],a[116:119],v[64:67],v[28:31]
s_xor_b32 s63,s63,65536
ds_read_b128 v[60:63],v2 offset:21504
v_add_f32_dpp v88,v88,v88 quad_perm:[1,0,3,2] row_mask:0xf bank_mask:0xf
v_mfma_f32_16x16x32_bf16 v[24:27],a[120:123],v[68:71],v[24:27]
s_add_u32 s53,s52,s55
ds_read_b128 v[64:67],v2 offset:22528
v_add_f32_dpp v88,v88,v88 quad_perm:[2,3,0,1] row_mask:0xf bank_mask:0xf
v_mfma_f32_16x16x32_bf16 v[28:31],a[124:127],v[68:71],v[28:31]
s_waitcnt lgkmcnt(3)
v_rcp_f32 v73,v73
v_mfma_f32_16x16x32_bf16 v[24:27],a[128:131],v[40:43],v[24:27]
buffer_load_dwordx4 v0,s[36:39],s67 offen offset:1024 sc1 lds
v_mfma_f32_16x16x32_bf16 v[28:31],a[132:135],v[40:43],v[28:31]
s_cmp_eq_u32 s53,s56
ds_read_b128 v[68:71],v2 offset:23552
v_add_f32_dpp v88,v88,v88 row_half_mirror row_mask:0xf bank_mask:0xf
v_mfma_f32_16x16x32_bf16 v[24:27],a[136:139],v[44:47],v[24:27]
s_cselect_b32 s53,s57,s53
ds_write_b32 v10,v88 offset:0
v_mfma_f32_16x16x32_bf16 v[28:31],a[140:143],v[44:47],v[28:31]
s_cmp_eq_u32 s53,s58
ds_read_b128 v[40:43],v2 offset:24576
v_fma_f32 v74,v98,v74,v97
v_mfma_f32_16x16x32_bf16 v[24:27],a[144:147],v[48:51],v[24:27]
buffer_load_dwordx4 v0,s[36:39],s67 offen offset:2048 sc1 lds
v_mfma_f32_16x16x32_bf16 v[28:31],a[148:151],v[48:51],v[28:31]
s_cselect_b32 s53,s59,s53
ds_read_b128 v[44:47],v2 offset:25600
v_mul_f32 v74,v34,v74
v_mfma_f32_16x16x32_bf16 v[24:27],a[152:155],v[52:55],v[24:27]
ds_read_b128 v[48:51],v2 offset:26624
v_fma_f32 v75,v98,v75,v97
v_mfma_f32_16x16x32_bf16 v[28:31],a[156:159],v[52:55],v[28:31]
s_waitcnt lgkmcnt(4)
v_exp_f32 v74,v74
v_mfma_f32_16x16x32_bf16 v[24:27],a[160:163],v[56:59],v[24:27]
buffer_load_dwordx4 v0,s[36:39],s67 offen offset:3072 sc1 lds
v_mfma_f32_16x16x32_bf16 v[28:31],a[164:167],v[56:59],v[28:31]
s_add_u32 m0,s62,16384
ds_read_b128 v[52:55],v2 offset:27648
v_mul_f32 v75,v35,v75
v_mfma_f32_16x16x32_bf16 v[24:27],a[168:171],v[60:63],v[24:27]
s_cmp_ge_u32 s66,s59
ds_read_b128 v[56:59],v2 offset:28672
v_fma_f32 v74,v74,v99,v99
v_mfma_f32_16x16x32_bf16 v[28:31],a[172:175],v[60:63],v[28:31]
v_exp_f32 v75,v75
v_mfma_f32_16x16x32_bf16 v[24:27],a[176:179],v[64:67],v[24:27]
buffer_load_dwordx4 v1,s[36:39],s67 offen offset:0 sc1 lds
v_mfma_f32_16x16x32_bf16 v[28:31],a[180:183],v[64:67],v[28:31]
ds_read_b128 v[60:63],v2 offset:29696
v_fma_f32 v75,v75,v99,v99
v_mfma_f32_16x16x32_bf16 v[24:27],a[184:187],v[68:71],v[24:27]
ds_read_b128 v[64:67],v2 offset:30720
v_mul_f32 v32,v32,v72
v_mfma_f32_16x16x32_bf16 v[28:31],a[188:191],v[68:71],v[28:31]
s_waitcnt lgkmcnt(3)
v_rcp_f32 v74,v74
v_mfma_f32_16x16x32_bf16 v[24:27],a[192:195],v[40:43],v[24:27]
s_waitcnt vmcnt(12)
ds_read_b128 v[68:71],v2 offset:31744
v_mul_f32 v33,v33,v73
v_mfma_f32_16x16x32_bf16 v[28:31],a[196:199],v[40:43],v[28:31]
s_barrier
v_mfma_f32_16x16x32_bf16 v[24:27],a[200:203],v[44:47],v[24:27]
buffer_load_dwordx4 v1,s[36:39],s67 offen offset:1024 sc1 lds
v_mfma_f32_16x16x32_bf16 v[28:31],a[204:207],v[44:47],v[28:31]
ds_read_b128 v[40:43],v2 offset:32768
ds_read_b64 v[92:93],v11 offset:0
v_mfma_f32_16x16x32_bf16 v[24:27],a[208:211],v[48:51],v[24:27]
ds_read_b128 v[44:47],v2 offset:33792
v_mul_f32 v34,v34,v74
v_mfma_f32_16x16x32_bf16 v[28:31],a[212:215],v[48:51],v[28:31]
v_rcp_f32 v75,v75
v_mfma_f32_16x16x32_bf16 v[24:27],a[216:219],v[52:55],v[24:27]
ds_read_b128 v[48:51],v2 offset:34816
v_mul_f32 v35,v35,v75
v_mfma_f32_16x16x32_bf16 v[28:31],a[220:223],v[52:55],v[28:31]
s_waitcnt lgkmcnt(4)
v_cvt_pk_bf16_f32 v80,v32,v33
v_cvt_pk_bf16_f32 v81,v34,v35
v_mfma_f32_16x16x32_bf16 v[24:27],a[224:227],v[56:59],v[24:27]
s_waitcnt lgkmcnt(2)
ds_read_b128 v[52:55],v2 offset:35840
v_fma_f32 v36,-v96,v20,v36
v_mfma_f32_16x16x32_bf16 v[28:31],a[228:231],v[56:59],v[28:31]
v_fma_f32 v37,-v96,v21,v37
v_fma_f32 v38,-v96,v22,v38
v_fma_f32 v39,-v96,v23,v39
v_mul_f32 v76,v36,v36
v_mul_f32 v77,v37,v37
v_mul_f32 v78,v38,v38
v_mul_f32 v79,v39,v39
v_fma_f32 v76,v98,v76,v97
v_fma_f32 v77,v98,v77,v97
v_fma_f32 v78,v98,v78,v97
v_fma_f32 v79,v98,v79,v97
v_mul_f32 v76,v36,v76
v_mul_f32 v77,v37,v77
v_mul_f32 v78,v38,v78
v_mul_f32 v79,v39,v79
v_exp_f32 v76,v76
v_exp_f32 v77,v77
v_exp_f32 v78,v78
v_exp_f32 v79,v79
v_mfma_f32_16x16x32_bf16 v[24:27],a[232:235],v[60:63],v[24:27]
v_fma_f32 v76,v76,v99,v99
v_fma_f32 v77,v77,v99,v99
v_fma_f32 v78,v78,v99,v99)"
R"(
v_fma_f32 v79,v79,v99,v99
v_rcp_f32 v76,v76
v_rcp_f32 v77,v77
v_rcp_f32 v78,v78
v_rcp_f32 v79,v79
v_mfma_f32_16x16x32_bf16 v[28:31],a[236:239],v[60:63],v[28:31]
ds_read_b128 v[56:59],v2 offset:36864
v_mul_f32 v36,v36,v76
v_mul_f32 v37,v37,v77
v_mul_f32 v38,v38,v78
v_mul_f32 v39,v39,v79
v_cvt_pk_bf16_f32 v82,v36,v37
v_cvt_pk_bf16_f32 v83,v38,v39
v_mfma_f32_16x16x32_bf16 v[24:27],a[240:243],v[64:67],v[24:27]
buffer_store_dwordx4 v[80:83],v3,s[36:39],s73 offen offset:0 sc0 sc1
ds_read_b128 v[60:63],v2 offset:37888
v_mfma_f32_16x16x32_bf16 v[28:31],a[244:247],v[64:67],v[28:31]
v_mfma_f32_16x16x32_bf16 v[24:27],a[248:251],v[68:71],v[24:27]
s_waitcnt lgkmcnt(2)
ds_read_b128 v[64:67],v2 offset:38912
v_mfma_f32_16x16x32_bf16 v[28:31],a[252:255],v[68:71],v[28:31]
s_cbranch_scc1 Lg3o_slow1_%=
Lg3o_d1w1_%=:
v_mfma_f32_16x16x32_bf16 v[32:35],a[0:3],v[40:43],0
s_add_u32 m0,s62,16384
ds_read_b128 v[68:71],v2 offset:39936
v_mul_f32 v96,s75,v92
v_mfma_f32_16x16x32_bf16 v[36:39],a[4:7],v[40:43],0
s_lshr_b32 s72,s67,5
v_fma_f32 v93,v93,s75,v12
v_fma_f32 v93,-v96,v96,v93
v_mfma_f32_16x16x32_bf16 v[32:35],a[8:11],v[44:47],v[32:35]
buffer_load_dwordx4 v1,s[36:39],s67 offen offset:2048 sc1 lds
v_mfma_f32_16x16x32_bf16 v[36:39],a[12:15],v[44:47],v[36:39]
s_lshr_b32 s71,s62,2
ds_read_b128 v[40:43],v2 offset:40960
v_max_f32 v93,v93,v12
v_mfma_f32_16x16x32_bf16 v[32:35],a[16:19],v[48:51],v[32:35]
s_mul_i32 s73,s48,7
ds_read_b128 v[44:47],v2 offset:41984
v_fma_f32 v24,-v96,v16,v24
v_mfma_f32_16x16x32_bf16 v[36:39],a[20:23],v[48:51],v[36:39]
s_mov_b64 s[44:45],s[46:47]
v_rsq_f32 v92,v93
v_mfma_f32_16x16x32_bf16 v[32:35],a[24:27],v[52:55],v[32:35]
buffer_load_dwordx4 v1,s[36:39],s67 offen offset:3072 sc1 lds
v_mfma_f32_16x16x32_bf16 v[36:39],a[28:31],v[52:55],v[36:39]
s_waitcnt lgkmcnt(2)
ds_read_b128 v[48:51],v2 offset:43008
v_mul_f32 v97,s76,v92
v_mfma_f32_16x16x32_bf16 v[32:35],a[32:35],v[56:59],v[32:35]
s_add_u32 m0,s71,s88
ds_read_b128 v[52:55],v2 offset:44032
v_mul_f32 v98,v92,v92
v_mfma_f32_16x16x32_bf16 v[36:39],a[36:39],v[56:59],v[36:39]
buffer_load_dword v9,s[36:39],s72 offen offset:0 sc1 lds
v_mfma_f32_16x16x32_bf16 v[32:35],a[40:43],v[60:63],v[32:35]
s_lshr_b32 s73,s73,1
ds_read_b128 v[56:59],v2 offset:45056
v_mul_f32 v99,v93,v92
v_mfma_f32_16x16x32_bf16 v[36:39],a[44:47],v[60:63],v[36:39]
s_lshr_b32 s68,s52,5
v_mul_f32 v98,s77,v98
v_mul_f32 v98,v98,v97
v_mfma_f32_16x16x32_bf16 v[32:35],a[48:51],v[64:67],v[32:35]
s_add_u32 m0,s78,256
ds_read_b128 v[60:63],v2 offset:46080
v_fma_f32 v25,-v96,v17,v25
v_mfma_f32_16x16x32_bf16 v[36:39],a[52:55],v[64:67],v[36:39]
s_xor_b32 s69,s63,65536
v_fma_f32 v26,-v96,v18,v26
v_fma_f32 v27,-v96,v19,v27
v_mfma_f32_16x16x32_bf16 v[32:35],a[56:59],v[68:71],v[32:35]
s_cmp_ge_u32 s67,s59
ds_read_b128 v[64:67],v2 offset:47104
v_mul_f32 v72,v24,v24
v_mfma_f32_16x16x32_bf16 v[36:39],a[60:63],v[68:71],v[36:39]
s_waitcnt lgkmcnt(3)
v_mul_f32 v73,v25,v25
v_mul_f32 v74,v26,v26
v_mfma_f32_16x16x32_bf16 v[32:35],a[64:67],v[40:43],v[32:35]
s_waitcnt vmcnt(10)
ds_read_b128 v[68:71],v2 offset:48128
v_mul_f32 v75,v27,v27
v_mfma_f32_16x16x32_bf16 v[36:39],a[68:71],v[40:43],v[36:39]
s_barrier
v_mfma_f32_16x16x32_bf16 v[32:35],a[72:75],v[44:47],v[32:35]
s_cselect_b32 s62,s69,s64
ds_read_b128 v[40:43],v2 offset:49152
ds_read_b32 v13,v7 offset:0
v_mfma_f32_16x16x32_bf16 v[36:39],a[76:79],v[44:47],v[36:39]
s_cselect_b32 s86,s50,s51
v_fma_f32 v72,v98,v72,v97
v_mul_f32 v72,v24,v72
v_mfma_f32_16x16x32_bf16 v[32:35],a[80:83],v[48:51],v[32:35]
s_mov_b64 s[46:47],s[48:49]
ds_read_b128 v[44:47],v2 offset:50176
v_fma_f32 v73,v98,v73,v97
v_mfma_f32_16x16x32_bf16 v[36:39],a[84:87],v[48:51],v[36:39]
buffer_load_dword v6,s[36:39],s68 offen offset:0 sc1 lds
v_mfma_f32_16x16x32_bf16 v[32:35],a[88:91],v[52:55],v[32:35]
s_waitcnt lgkmcnt(1)
ds_read_b128 v[48:51],v2 offset:51200
ds_read_b32 v88,v8 offset:8192
v_mfma_f32_16x16x32_bf16 v[36:39],a[92:95],v[52:55],v[36:39]
v_cmp_eq_u32 vcc,s60,v13
v_exp_f32 v72,v72
v_mul_f32 v73,v25,v73
v_mfma_f32_16x16x32_bf16 v[32:35],a[96:99],v[56:59],v[32:35]
s_mov_b32 m0,s62
ds_read_b128 v[52:55],v2 offset:52224
v_fma_f32 v72,v72,v99,v99
v_mfma_f32_16x16x32_bf16 v[36:39],a[100:103],v[56:59],v[36:39]
s_cselect_b32 s87,vcc_lo,vcc_hi
v_exp_f32 v73,v73
v_mfma_f32_16x16x32_bf16 v[32:35],a[104:107],v[60:63],v[32:35]
s_cmp_eq_u32 s87,-1
ds_read_b128 v[56:59],v2 offset:53248
v_fma_f32 v73,v73,v99,v99
v_mfma_f32_16x16x32_bf16 v[36:39],a[108:111],v[60:63],v[36:39]
s_cselect_b32 s66,s86,s59
v_rcp_f32 v72,v72
v_mfma_f32_16x16x32_bf16 v[32:35],a[112:115],v[64:67],v[32:35]
s_waitcnt lgkmcnt(2)
buffer_load_dwordx4 v0,s[36:39],s66 offen offset:0 sc1 lds
v_mfma_f32_16x16x32_bf16 v[36:39],a[116:119],v[64:67],v[36:39]
s_xor_b32 s64,s64,65536
ds_read_b128 v[60:63],v2 offset:54272
v_add_f32_dpp v88,v88,v88 quad_perm:[1,0,3,2] row_mask:0xf bank_mask:0xf
v_mfma_f32_16x16x32_bf16 v[32:35],a[120:123],v[68:71],v[32:35]
s_add_u32 s74,s53,s55
ds_read_b128 v[64:67],v2 offset:55296
v_add_f32_dpp v88,v88,v88 quad_perm:[2,3,0,1] row_mask:0xf bank_mask:0xf
v_mfma_f32_16x16x32_bf16 v[36:39],a[124:127],v[68:71],v[36:39]
s_waitcnt lgkmcnt(3)
v_rcp_f32 v73,v73
v_mfma_f32_16x16x32_bf16 v[32:35],a[128:131],v[40:43],v[32:35]
buffer_load_dwordx4 v0,s[36:39],s66 offen offset:1024 sc1 lds
v_mfma_f32_16x16x32_bf16 v[36:39],a[132:135],v[40:43],v[36:39]
s_cmp_eq_u32 s74,s56
ds_read_b128 v[68:71],v2 offset:56320
v_add_f32_dpp v88,v88,v88 row_half_mirror row_mask:0xf bank_mask:0xf
v_mfma_f32_16x16x32_bf16 v[32:35],a[136:139],v[44:47],v[32:35]
s_cselect_b32 s74,s57,s74
ds_write_b32 v10,v88 offset:128
v_mfma_f32_16x16x32_bf16 v[36:39],a[140:143],v[44:47],v[36:39]
s_cmp_eq_u32 s74,s58
ds_read_b128 v[40:43],v2 offset:57344
v_fma_f32 v74,v98,v74,v97
v_mfma_f32_16x16x32_bf16 v[32:35],a[144:147],v[48:51],v[32:35]
buffer_load_dwordx4 v0,s[36:39],s66 offen offset:2048 sc1 lds
v_mfma_f32_16x16x32_bf16 v[36:39],a[148:151],v[48:51],v[36:39]
s_cselect_b32 s74,s59,s74
ds_read_b128 v[44:47],v2 offset:58368
v_mul_f32 v74,v26,v74
v_mfma_f32_16x16x32_bf16 v[32:35],a[152:155],v[52:55],v[32:35]
s_mov_b64 s[48:49],s[50:51]
ds_read_b128 v[48:51],v2 offset:59392
v_fma_f32 v75,v98,v75,v97
v_mfma_f32_16x16x32_bf16 v[36:39],a[156:159],v[52:55],v[36:39]
s_waitcnt lgkmcnt(4)
v_exp_f32 v74,v74
v_mfma_f32_16x16x32_bf16 v[32:35],a[160:163],v[56:59],v[32:35]
buffer_load_dwordx4 v0,s[36:39],s66 offen offset:3072 sc1 lds
v_mfma_f32_16x16x32_bf16 v[36:39],a[164:167],v[56:59],v[36:39]
s_add_u32 m0,s62,16384
ds_read_b128 v[52:55],v2 offset:60416
v_mul_f32 v75,v27,v75
v_mfma_f32_16x16x32_bf16 v[32:35],a[168:171],v[60:63],v[32:35]
s_mov_b64 s[50:51],s[52:53]
ds_read_b128 v[56:59],v2 offset:61440
v_fma_f32 v74,v74,v99,v99
v_mfma_f32_16x16x32_bf16 v[36:39],a[172:175],v[60:63],v[36:39]
s_mov_b32 s52,s74
v_exp_f32 v75,v75
v_mfma_f32_16x16x32_bf16 v[32:35],a[176:179],v[64:67],v[32:35]
buffer_load_dwordx4 v1,s[36:39],s66 offen offset:0 sc1 lds
v_mfma_f32_16x16x32_bf16 v[36:39],a[180:183],v[64:67],v[36:39]
s_cmp_lt_u32 s67,s59
ds_read_b128 v[60:63],v2 offset:62464
v_fma_f32 v75,v75,v99,v99
v_mfma_f32_16x16x32_bf16 v[32:35],a[184:187],v[68:71],v[32:35]
ds_read_b128 v[64:67],v2 offset:63488
v_mul_f32 v24,v24,v72
v_mfma_f32_16x16x32_bf16 v[36:39],a[188:191],v[68:71],v[36:39]
s_waitcnt lgkmcnt(3)
v_rcp_f32 v74,v74
v_mfma_f32_16x16x32_bf16 v[32:35],a[192:195],v[40:43],v[32:35]
s_waitcnt vmcnt(12)
ds_read_b128 v[68:71],v2 offset:64512
v_xor_b32 v2,65536,v2
v_mfma_f32_16x16x32_bf16 v[36:39],a[196:199],v[40:43],v[36:39]
s_barrier
v_mfma_f32_16x16x32_bf16 v[32:35],a[200:203],v[44:47],v[32:35]
buffer_load_dwordx4 v1,s[36:39],s66 offen offset:1024 sc1 lds
v_mfma_f32_16x16x32_bf16 v[36:39],a[204:207],v[44:47],v[36:39]
ds_read_b128 v[40:43],v2 offset:0
ds_read_b64 v[92:93],v11 offset:128
v_mfma_f32_16x16x32_bf16 v[32:35],a[208:211],v[48:51],v[32:35]
ds_read_b128 v[44:47],v2 offset:1024)"
R"(
v_mul_f32 v25,v25,v73
v_mfma_f32_16x16x32_bf16 v[36:39],a[212:215],v[48:51],v[36:39]
v_rcp_f32 v75,v75
v_mfma_f32_16x16x32_bf16 v[32:35],a[216:219],v[52:55],v[32:35]
ds_read_b128 v[48:51],v2 offset:2048
v_mul_f32 v26,v26,v74
v_mfma_f32_16x16x32_bf16 v[36:39],a[220:223],v[52:55],v[36:39]
s_waitcnt lgkmcnt(4)
v_mul_f32 v27,v27,v75
v_cvt_pk_bf16_f32 v80,v24,v25
v_mfma_f32_16x16x32_bf16 v[32:35],a[224:227],v[56:59],v[32:35]
s_waitcnt lgkmcnt(2)
ds_read_b128 v[52:55],v2 offset:3072
v_cvt_pk_bf16_f32 v81,v26,v27
v_mfma_f32_16x16x32_bf16 v[36:39],a[228:231],v[56:59],v[36:39]
v_fma_f32 v28,-v96,v20,v28
v_fma_f32 v29,-v96,v21,v29
v_fma_f32 v30,-v96,v22,v30
v_fma_f32 v31,-v96,v23,v31
v_mul_f32 v76,v28,v28
v_mul_f32 v77,v29,v29
v_mul_f32 v78,v30,v30
v_mul_f32 v79,v31,v31
v_fma_f32 v76,v98,v76,v97
v_fma_f32 v77,v98,v77,v97
v_fma_f32 v78,v98,v78,v97
v_fma_f32 v79,v98,v79,v97
v_mul_f32 v76,v28,v76
v_mul_f32 v77,v29,v77
v_mul_f32 v78,v30,v78
v_mul_f32 v79,v31,v79
v_exp_f32 v76,v76
v_exp_f32 v77,v77
v_exp_f32 v78,v78
v_exp_f32 v79,v79
v_mfma_f32_16x16x32_bf16 v[32:35],a[232:235],v[60:63],v[32:35]
v_fma_f32 v76,v76,v99,v99
v_fma_f32 v77,v77,v99,v99
v_fma_f32 v78,v78,v99,v99
v_fma_f32 v79,v79,v99,v99
v_rcp_f32 v76,v76
v_rcp_f32 v77,v77
v_rcp_f32 v78,v78
v_rcp_f32 v79,v79
v_mfma_f32_16x16x32_bf16 v[36:39],a[236:239],v[60:63],v[36:39]
ds_read_b128 v[56:59],v2 offset:4096
v_mul_f32 v28,v28,v76
v_mul_f32 v29,v29,v77
v_mul_f32 v30,v30,v78
v_mul_f32 v31,v31,v79
v_cvt_pk_bf16_f32 v82,v28,v29
v_cvt_pk_bf16_f32 v83,v30,v31
v_mfma_f32_16x16x32_bf16 v[32:35],a[240:243],v[64:67],v[32:35]
buffer_store_dwordx4 v[80:83],v3,s[36:39],s73 offen offset:0 sc0 sc1
ds_read_b128 v[60:63],v2 offset:5120
v_xor_b32 v8,16384,v8
v_mfma_f32_16x16x32_bf16 v[36:39],a[244:247],v[64:67],v[36:39]
v_mfma_f32_16x16x32_bf16 v[32:35],a[248:251],v[68:71],v[32:35]
s_waitcnt lgkmcnt(2)
ds_read_b128 v[64:67],v2 offset:6144
v_mfma_f32_16x16x32_bf16 v[36:39],a[252:255],v[68:71],v[36:39]
s_cbranch_scc1 Lg3o_d0w1_%=
s_branch Lg3o_slow0_%=
Lg3o_d0w2_%=:
v_mfma_f32_16x16x32_bf16 v[24:27],a[0:3],v[40:43],0
s_add_u32 m0,s62,16384
ds_read_b128 v[68:71],v2 offset:7168
v_mul_f32 v96,s75,v92
v_mfma_f32_16x16x32_bf16 v[28:31],a[4:7],v[40:43],0
s_lshr_b32 s72,s66,5
v_fma_f32 v93,v93,s75,v12
v_fma_f32 v93,-v96,v96,v93
v_mfma_f32_16x16x32_bf16 v[24:27],a[8:11],v[44:47],v[24:27]
s_lshr_b32 s71,s62,2
ds_read_b128 v[40:43],v2 offset:8192
v_max_f32 v93,v93,v12
v_mfma_f32_16x16x32_bf16 v[28:31],a[12:15],v[44:47],v[28:31]
buffer_load_dwordx4 v1,s[36:39],s66 offen offset:2048 sc1 lds
v_mfma_f32_16x16x32_bf16 v[24:27],a[16:19],v[48:51],v[24:27]
s_mul_i32 s73,s47,7
ds_read_b128 v[44:47],v2 offset:9216
v_fma_f32 v32,-v96,v16,v32
v_mfma_f32_16x16x32_bf16 v[28:31],a[20:23],v[48:51],v[28:31]
v_rsq_f32 v92,v93
v_mfma_f32_16x16x32_bf16 v[24:27],a[24:27],v[52:55],v[24:27]
ds_read_b128 v[48:51],v2 offset:10240
v_mul_f32 v97,s76,v92
v_mfma_f32_16x16x32_bf16 v[28:31],a[28:31],v[52:55],v[28:31]
s_waitcnt lgkmcnt(3)
buffer_load_dwordx4 v1,s[36:39],s66 offen offset:3072 sc1 lds
v_mfma_f32_16x16x32_bf16 v[24:27],a[32:35],v[56:59],v[24:27]
s_add_u32 m0,s71,s88
ds_read_b128 v[52:55],v2 offset:11264
v_mul_f32 v98,v92,v92
v_mfma_f32_16x16x32_bf16 v[28:31],a[36:39],v[56:59],v[28:31]
buffer_load_dword v9,s[36:39],s72 offen offset:0 sc1 lds
v_mfma_f32_16x16x32_bf16 v[24:27],a[40:43],v[60:63],v[24:27]
s_lshr_b32 s73,s73,1
ds_read_b128 v[56:59],v2 offset:12288
v_mul_f32 v99,v93,v92
v_mfma_f32_16x16x32_bf16 v[28:31],a[44:47],v[60:63],v[28:31]
s_xor_b32 s69,s64,65536
v_mul_f32 v98,s77,v98
v_mul_f32 v98,v98,v97
v_mfma_f32_16x16x32_bf16 v[24:27],a[48:51],v[64:67],v[24:27]
s_cmp_ge_u32 s66,s59
ds_read_b128 v[60:63],v2 offset:13312
v_fma_f32 v33,-v96,v17,v33
v_mfma_f32_16x16x32_bf16 v[28:31],a[52:55],v[64:67],v[28:31]
s_cselect_b32 s62,s69,s63
v_fma_f32 v34,-v96,v18,v34
v_fma_f32 v35,-v96,v19,v35
v_mfma_f32_16x16x32_bf16 v[24:27],a[56:59],v[68:71],v[24:27]
s_cselect_b32 s86,s49,s50
ds_read_b128 v[64:67],v2 offset:14336
v_mul_f32 v72,v32,v32
v_mfma_f32_16x16x32_bf16 v[28:31],a[60:63],v[68:71],v[28:31]
s_waitcnt lgkmcnt(3)
v_mul_f32 v73,v33,v33
v_mul_f32 v74,v34,v34
v_mfma_f32_16x16x32_bf16 v[24:27],a[64:67],v[40:43],v[24:27]
s_waitcnt vmcnt(10)
ds_read_b128 v[68:71],v2 offset:15360
v_mul_f32 v75,v35,v35
v_mfma_f32_16x16x32_bf16 v[28:31],a[68:71],v[40:43],v[28:31]
s_barrier
v_mfma_f32_16x16x32_bf16 v[24:27],a[72:75],v[44:47],v[24:27]
s_mov_b32 m0,s62
ds_read_b128 v[40:43],v2 offset:16384
ds_read_b32 v13,v7 offset:256
v_mfma_f32_16x16x32_bf16 v[28:31],a[76:79],v[44:47],v[28:31]
v_fma_f32 v72,v98,v72,v97
v_mul_f32 v72,v32,v72
v_mfma_f32_16x16x32_bf16 v[24:27],a[80:83],v[48:51],v[24:27]
ds_read_b128 v[44:47],v2 offset:17408
v_fma_f32 v73,v98,v73,v97
v_mfma_f32_16x16x32_bf16 v[28:31],a[84:87],v[48:51],v[28:31]
v_exp_f32 v72,v72
v_mfma_f32_16x16x32_bf16 v[24:27],a[88:91],v[52:55],v[24:27]
s_waitcnt lgkmcnt(1)
ds_read_b128 v[48:51],v2 offset:18432
ds_read_b32 v88,v8 offset:0
v_mfma_f32_16x16x32_bf16 v[28:31],a[92:95],v[52:55],v[28:31]
v_cmp_eq_u32 vcc,s60,v13
v_mul_f32 v73,v33,v73
v_exp_f32 v73,v73
v_mfma_f32_16x16x32_bf16 v[24:27],a[96:99],v[56:59],v[24:27]
s_cselect_b32 s87,vcc_lo,vcc_hi
ds_read_b128 v[52:55],v2 offset:19456
v_fma_f32 v72,v72,v99,v99
v_mfma_f32_16x16x32_bf16 v[28:31],a[100:103],v[56:59],v[28:31]
s_cmp_eq_u32 s87,-1
v_rcp_f32 v72,v72
v_mfma_f32_16x16x32_bf16 v[24:27],a[104:107],v[60:63],v[24:27]
s_cselect_b32 s67,s86,s59
ds_read_b128 v[56:59],v2 offset:20480
v_fma_f32 v73,v73,v99,v99
v_mfma_f32_16x16x32_bf16 v[28:31],a[108:111],v[60:63],v[28:31]
s_xor_b32 s63,s63,65536
v_rcp_f32 v73,v73
v_mfma_f32_16x16x32_bf16 v[24:27],a[112:115],v[64:67],v[24:27]
s_waitcnt lgkmcnt(2)
ds_read_b128 v[60:63],v2 offset:21504
v_fma_f32 v74,v98,v74,v97
v_mfma_f32_16x16x32_bf16 v[28:31],a[116:119],v[64:67],v[28:31]
buffer_load_dwordx4 v0,s[36:39],s67 offen offset:0 sc1 lds
v_mfma_f32_16x16x32_bf16 v[24:27],a[120:123],v[68:71],v[24:27]
s_add_u32 s53,s52,s55
ds_read_b128 v[64:67],v2 offset:22528
v_mul_f32 v74,v34,v74
v_mfma_f32_16x16x32_bf16 v[28:31],a[124:127],v[68:71],v[28:31]
s_waitcnt lgkmcnt(3)
v_exp_f32 v74,v74
v_mfma_f32_16x16x32_bf16 v[24:27],a[128:131],v[40:43],v[24:27]
s_cmp_eq_u32 s53,s56
ds_read_b128 v[68:71],v2 offset:23552
v_fma_f32 v74,v74,v99,v99
v_mfma_f32_16x16x32_bf16 v[28:31],a[132:135],v[40:43],v[28:31]
buffer_load_dwordx4 v0,s[36:39],s67 offen offset:1024 sc1 lds
v_mfma_f32_16x16x32_bf16 v[24:27],a[136:139],v[44:47],v[24:27]
s_cselect_b32 s53,s57,s53
ds_read_b128 v[40:43],v2 offset:24576
v_add_f32_dpp v88,v88,v88 quad_perm:[1,0,3,2] row_mask:0xf bank_mask:0xf
v_mfma_f32_16x16x32_bf16 v[28:31],a[140:143],v[44:47],v[28:31]
s_cmp_eq_u32 s53,s58
v_rcp_f32 v74,v74
v_mfma_f32_16x16x32_bf16 v[24:27],a[144:147],v[48:51],v[24:27]
s_cselect_b32 s53,s59,s53
ds_read_b128 v[44:47],v2 offset:25600
v_add_f32_dpp v88,v88,v88 quad_perm:[2,3,0,1] row_mask:0xf bank_mask:0xf
v_mfma_f32_16x16x32_bf16 v[28:31],a[148:151],v[48:51],v[28:31]
buffer_load_dwordx4 v0,s[36:39],s67 offen offset:2048 sc1 lds
v_mfma_f32_16x16x32_bf16 v[24:27],a[152:155],v[52:55],v[24:27]
ds_read_b128 v[48:51],v2 offset:26624
v_add_f32_dpp v88,v88,v88 row_half_mirror row_mask:0xf bank_mask:0xf
v_mfma_f32_16x16x32_bf16 v[28:31],a[156:159],v[52:55],v[28:31]
s_waitcnt lgkmcnt(3)
ds_write_b32 v10,v88 offset:0
v_mfma_f32_16x16x32_bf16 v[24:27],a[160:163],v[56:59],v[24:27]
ds_read_b128 v[52:55],v2 offset:27648)"
R"(
v_fma_f32 v75,v98,v75,v97
v_mfma_f32_16x16x32_bf16 v[28:31],a[164:167],v[56:59],v[28:31]
buffer_load_dwordx4 v0,s[36:39],s67 offen offset:3072 sc1 lds
v_mfma_f32_16x16x32_bf16 v[24:27],a[168:171],v[60:63],v[24:27]
s_add_u32 m0,s62,16384
ds_read_b128 v[56:59],v2 offset:28672
v_mul_f32 v75,v35,v75
v_mfma_f32_16x16x32_bf16 v[28:31],a[172:175],v[60:63],v[28:31]
s_cmp_ge_u32 s66,s59
v_exp_f32 v75,v75
v_mfma_f32_16x16x32_bf16 v[24:27],a[176:179],v[64:67],v[24:27]
ds_read_b128 v[60:63],v2 offset:29696
v_fma_f32 v75,v75,v99,v99
v_mfma_f32_16x16x32_bf16 v[28:31],a[180:183],v[64:67],v[28:31]
buffer_load_dwordx4 v1,s[36:39],s67 offen offset:0 sc1 lds
v_mfma_f32_16x16x32_bf16 v[24:27],a[184:187],v[68:71],v[24:27]
ds_read_b128 v[64:67],v2 offset:30720
v_mul_f32 v32,v32,v72
v_mfma_f32_16x16x32_bf16 v[28:31],a[188:191],v[68:71],v[28:31]
s_waitcnt lgkmcnt(3)
v_rcp_f32 v75,v75
v_mfma_f32_16x16x32_bf16 v[24:27],a[192:195],v[40:43],v[24:27]
s_waitcnt vmcnt(11)
ds_read_b128 v[68:71],v2 offset:31744
v_mul_f32 v33,v33,v73
v_mfma_f32_16x16x32_bf16 v[28:31],a[196:199],v[40:43],v[28:31]
s_barrier
v_mfma_f32_16x16x32_bf16 v[24:27],a[200:203],v[44:47],v[24:27]
ds_read_b128 v[40:43],v2 offset:32768
ds_read_b64 v[92:93],v11 offset:0
v_mfma_f32_16x16x32_bf16 v[28:31],a[204:207],v[44:47],v[28:31]
buffer_load_dwordx4 v1,s[36:39],s67 offen offset:1024 sc1 lds
v_mfma_f32_16x16x32_bf16 v[24:27],a[208:211],v[48:51],v[24:27]
ds_read_b128 v[44:47],v2 offset:33792
v_mul_f32 v34,v34,v74
v_mfma_f32_16x16x32_bf16 v[28:31],a[212:215],v[48:51],v[28:31]
v_mul_f32 v35,v35,v75
v_cvt_pk_bf16_f32 v80,v32,v33
v_mfma_f32_16x16x32_bf16 v[24:27],a[216:219],v[52:55],v[24:27]
s_waitcnt lgkmcnt(1)
ds_read_b128 v[48:51],v2 offset:34816
v_cvt_pk_bf16_f32 v81,v34,v35
v_mfma_f32_16x16x32_bf16 v[28:31],a[220:223],v[52:55],v[28:31]
v_fma_f32 v36,-v96,v20,v36
v_fma_f32 v37,-v96,v21,v37
v_mfma_f32_16x16x32_bf16 v[24:27],a[224:227],v[56:59],v[24:27]
ds_read_b128 v[52:55],v2 offset:35840
v_fma_f32 v38,-v96,v22,v38
v_mfma_f32_16x16x32_bf16 v[28:31],a[228:231],v[56:59],v[28:31]
v_fma_f32 v39,-v96,v23,v39
v_mul_f32 v76,v36,v36
v_mfma_f32_16x16x32_bf16 v[24:27],a[232:235],v[60:63],v[24:27]
v_mul_f32 v77,v37,v37
v_mul_f32 v78,v38,v38
v_mul_f32 v79,v39,v39
v_fma_f32 v76,v98,v76,v97
v_fma_f32 v77,v98,v77,v97
v_fma_f32 v78,v98,v78,v97
v_fma_f32 v79,v98,v79,v97
v_mul_f32 v76,v36,v76
v_mul_f32 v77,v37,v77
v_mul_f32 v78,v38,v78
v_mul_f32 v79,v39,v79
v_exp_f32 v76,v76
v_exp_f32 v77,v77
v_exp_f32 v78,v78
v_exp_f32 v79,v79
v_mfma_f32_16x16x32_bf16 v[28:31],a[236:239],v[60:63],v[28:31]
ds_read_b128 v[56:59],v2 offset:36864
v_fma_f32 v76,v76,v99,v99
v_fma_f32 v77,v77,v99,v99
v_fma_f32 v78,v78,v99,v99
v_fma_f32 v79,v79,v99,v99
v_rcp_f32 v76,v76
v_rcp_f32 v77,v77
v_rcp_f32 v78,v78
v_rcp_f32 v79,v79
v_mfma_f32_16x16x32_bf16 v[24:27],a[240:243],v[64:67],v[24:27]
v_mul_f32 v36,v36,v76
v_mul_f32 v37,v37,v77
v_mul_f32 v38,v38,v78
v_mul_f32 v39,v39,v79
v_cvt_pk_bf16_f32 v82,v36,v37
v_cvt_pk_bf16_f32 v83,v38,v39
v_mfma_f32_16x16x32_bf16 v[28:31],a[244:247],v[64:67],v[28:31]
ds_read_b128 v[60:63],v2 offset:37888
buffer_store_dwordx4 v[80:83],v3,s[36:39],s73 offen offset:0 sc0 sc1
v_mfma_f32_16x16x32_bf16 v[24:27],a[248:251],v[68:71],v[24:27]
s_waitcnt lgkmcnt(2)
ds_read_b128 v[64:67],v2 offset:38912
v_mfma_f32_16x16x32_bf16 v[28:31],a[252:255],v[68:71],v[28:31]
s_cbranch_scc1 Lg3o_slow1_%=
Lg3o_d1w2_%=:
v_mfma_f32_16x16x32_bf16 v[32:35],a[0:3],v[40:43],0
s_add_u32 m0,s62,16384
ds_read_b128 v[68:71],v2 offset:39936
v_mul_f32 v96,s75,v92
v_mfma_f32_16x16x32_bf16 v[36:39],a[4:7],v[40:43],0
s_lshr_b32 s72,s67,5
v_fma_f32 v93,v93,s75,v12
v_fma_f32 v93,-v96,v96,v93
v_mfma_f32_16x16x32_bf16 v[32:35],a[8:11],v[44:47],v[32:35]
s_lshr_b32 s71,s62,2
ds_read_b128 v[40:43],v2 offset:40960
v_max_f32 v93,v93,v12
v_mfma_f32_16x16x32_bf16 v[36:39],a[12:15],v[44:47],v[36:39]
buffer_load_dwordx4 v1,s[36:39],s67 offen offset:2048 sc1 lds
v_mfma_f32_16x16x32_bf16 v[32:35],a[16:19],v[48:51],v[32:35]
s_mul_i32 s73,s48,7
ds_read_b128 v[44:47],v2 offset:41984
v_fma_f32 v24,-v96,v16,v24
v_mfma_f32_16x16x32_bf16 v[36:39],a[20:23],v[48:51],v[36:39]
s_mov_b64 s[44:45],s[46:47]
v_rsq_f32 v92,v93
v_mfma_f32_16x16x32_bf16 v[32:35],a[24:27],v[52:55],v[32:35]
s_mov_b64 s[46:47],s[48:49]
ds_read_b128 v[48:51],v2 offset:43008
v_mul_f32 v97,s76,v92
v_mfma_f32_16x16x32_bf16 v[36:39],a[28:31],v[52:55],v[36:39]
s_waitcnt lgkmcnt(3)
buffer_load_dwordx4 v1,s[36:39],s67 offen offset:3072 sc1 lds
v_mfma_f32_16x16x32_bf16 v[32:35],a[32:35],v[56:59],v[32:35]
s_add_u32 m0,s71,s88
ds_read_b128 v[52:55],v2 offset:44032
v_mul_f32 v98,v92,v92
v_mfma_f32_16x16x32_bf16 v[36:39],a[36:39],v[56:59],v[36:39]
buffer_load_dword v9,s[36:39],s72 offen offset:0 sc1 lds
v_mfma_f32_16x16x32_bf16 v[32:35],a[40:43],v[60:63],v[32:35]
s_lshr_b32 s73,s73,1
ds_read_b128 v[56:59],v2 offset:45056
v_mul_f32 v99,v93,v92
v_mfma_f32_16x16x32_bf16 v[36:39],a[44:47],v[60:63],v[36:39]
s_xor_b32 s69,s63,65536
v_mul_f32 v98,s77,v98
v_mul_f32 v98,v98,v97
v_mfma_f32_16x16x32_bf16 v[32:35],a[48:51],v[64:67],v[32:35]
s_cmp_ge_u32 s67,s59
ds_read_b128 v[60:63],v2 offset:46080
v_fma_f32 v25,-v96,v17,v25
v_mfma_f32_16x16x32_bf16 v[36:39],a[52:55],v[64:67],v[36:39]
s_cselect_b32 s62,s69,s64
v_fma_f32 v26,-v96,v18,v26
v_fma_f32 v27,-v96,v19,v27
v_mfma_f32_16x16x32_bf16 v[32:35],a[56:59],v[68:71],v[32:35]
s_cselect_b32 s86,s50,s51
ds_read_b128 v[64:67],v2 offset:47104
v_mul_f32 v72,v24,v24
v_mfma_f32_16x16x32_bf16 v[36:39],a[60:63],v[68:71],v[36:39]
s_waitcnt lgkmcnt(3)
v_mul_f32 v73,v25,v25
v_mul_f32 v74,v26,v26
v_mfma_f32_16x16x32_bf16 v[32:35],a[64:67],v[40:43],v[32:35]
s_waitcnt vmcnt(10)
ds_read_b128 v[68:71],v2 offset:48128
v_mul_f32 v75,v27,v27
v_mfma_f32_16x16x32_bf16 v[36:39],a[68:71],v[40:43],v[36:39]
s_barrier
v_mfma_f32_16x16x32_bf16 v[32:35],a[72:75],v[44:47],v[32:35]
s_mov_b32 m0,s62
ds_read_b128 v[40:43],v2 offset:49152
ds_read_b32 v13,v7 offset:0
v_mfma_f32_16x16x32_bf16 v[36:39],a[76:79],v[44:47],v[36:39]
s_mov_b64 s[48:49],s[50:51]
v_fma_f32 v72,v98,v72,v97
v_mul_f32 v72,v24,v72
v_mfma_f32_16x16x32_bf16 v[32:35],a[80:83],v[48:51],v[32:35]
s_mov_b64 s[50:51],s[52:53]
ds_read_b128 v[44:47],v2 offset:50176
v_fma_f32 v73,v98,v73,v97
v_mfma_f32_16x16x32_bf16 v[36:39],a[84:87],v[48:51],v[36:39]
v_exp_f32 v72,v72
v_mfma_f32_16x16x32_bf16 v[32:35],a[88:91],v[52:55],v[32:35]
s_waitcnt lgkmcnt(1)
ds_read_b128 v[48:51],v2 offset:51200
ds_read_b32 v88,v8 offset:8192
v_mfma_f32_16x16x32_bf16 v[36:39],a[92:95],v[52:55],v[36:39]
v_cmp_eq_u32 vcc,s60,v13
v_mul_f32 v73,v25,v73
v_exp_f32 v73,v73
v_mfma_f32_16x16x32_bf16 v[32:35],a[96:99],v[56:59],v[32:35]
s_cselect_b32 s87,vcc_lo,vcc_hi
ds_read_b128 v[52:55],v2 offset:52224
v_fma_f32 v72,v72,v99,v99
v_mfma_f32_16x16x32_bf16 v[36:39],a[100:103],v[56:59],v[36:39]
s_cmp_eq_u32 s87,-1
v_rcp_f32 v72,v72
v_mfma_f32_16x16x32_bf16 v[32:35],a[104:107],v[60:63],v[32:35]
s_cselect_b32 s66,s86,s59
ds_read_b128 v[56:59],v2 offset:53248
v_fma_f32 v73,v73,v99,v99
v_mfma_f32_16x16x32_bf16 v[36:39],a[108:111],v[60:63],v[36:39]
s_xor_b32 s64,s64,65536
v_rcp_f32 v73,v73
v_mfma_f32_16x16x32_bf16 v[32:35],a[112:115],v[64:67],v[32:35]
s_waitcnt lgkmcnt(2)
ds_read_b128 v[60:63],v2 offset:54272
v_fma_f32 v74,v98,v74,v97
v_mfma_f32_16x16x32_bf16 v[36:39],a[116:119],v[64:67],v[36:39]
buffer_load_dwordx4 v0,s[36:39],s66 offen offset:0 sc1 lds
v_mfma_f32_16x16x32_bf16 v[32:35],a[120:123],v[68:71],v[32:35])"
R"(
s_add_u32 s74,s53,s55
ds_read_b128 v[64:67],v2 offset:55296
v_mul_f32 v74,v26,v74
v_mfma_f32_16x16x32_bf16 v[36:39],a[124:127],v[68:71],v[36:39]
s_waitcnt lgkmcnt(3)
v_exp_f32 v74,v74
v_mfma_f32_16x16x32_bf16 v[32:35],a[128:131],v[40:43],v[32:35]
s_cmp_eq_u32 s74,s56
ds_read_b128 v[68:71],v2 offset:56320
v_fma_f32 v74,v74,v99,v99
v_mfma_f32_16x16x32_bf16 v[36:39],a[132:135],v[40:43],v[36:39]
buffer_load_dwordx4 v0,s[36:39],s66 offen offset:1024 sc1 lds
v_mfma_f32_16x16x32_bf16 v[32:35],a[136:139],v[44:47],v[32:35]
s_cselect_b32 s74,s57,s74
ds_read_b128 v[40:43],v2 offset:57344
v_add_f32_dpp v88,v88,v88 quad_perm:[1,0,3,2] row_mask:0xf bank_mask:0xf
v_mfma_f32_16x16x32_bf16 v[36:39],a[140:143],v[44:47],v[36:39]
s_cmp_eq_u32 s74,s58
v_rcp_f32 v74,v74
v_mfma_f32_16x16x32_bf16 v[32:35],a[144:147],v[48:51],v[32:35]
s_cselect_b32 s74,s59,s74
ds_read_b128 v[44:47],v2 offset:58368
v_add_f32_dpp v88,v88,v88 quad_perm:[2,3,0,1] row_mask:0xf bank_mask:0xf
v_mfma_f32_16x16x32_bf16 v[36:39],a[148:151],v[48:51],v[36:39]
buffer_load_dwordx4 v0,s[36:39],s66 offen offset:2048 sc1 lds
v_mfma_f32_16x16x32_bf16 v[32:35],a[152:155],v[52:55],v[32:35]
s_mov_b32 s52,s74
ds_read_b128 v[48:51],v2 offset:59392
v_add_f32_dpp v88,v88,v88 row_half_mirror row_mask:0xf bank_mask:0xf
v_mfma_f32_16x16x32_bf16 v[36:39],a[156:159],v[52:55],v[36:39]
s_waitcnt lgkmcnt(3)
ds_write_b32 v10,v88 offset:128
v_mfma_f32_16x16x32_bf16 v[32:35],a[160:163],v[56:59],v[32:35]
ds_read_b128 v[52:55],v2 offset:60416
v_fma_f32 v75,v98,v75,v97
v_mfma_f32_16x16x32_bf16 v[36:39],a[164:167],v[56:59],v[36:39]
buffer_load_dwordx4 v0,s[36:39],s66 offen offset:3072 sc1 lds
v_mfma_f32_16x16x32_bf16 v[32:35],a[168:171],v[60:63],v[32:35]
s_add_u32 m0,s62,16384
ds_read_b128 v[56:59],v2 offset:61440
v_mul_f32 v75,v27,v75
v_mfma_f32_16x16x32_bf16 v[36:39],a[172:175],v[60:63],v[36:39]
s_cmp_lt_u32 s67,s59
v_exp_f32 v75,v75
v_mfma_f32_16x16x32_bf16 v[32:35],a[176:179],v[64:67],v[32:35]
ds_read_b128 v[60:63],v2 offset:62464
v_fma_f32 v75,v75,v99,v99
v_mfma_f32_16x16x32_bf16 v[36:39],a[180:183],v[64:67],v[36:39]
buffer_load_dwordx4 v1,s[36:39],s66 offen offset:0 sc1 lds
v_mfma_f32_16x16x32_bf16 v[32:35],a[184:187],v[68:71],v[32:35]
ds_read_b128 v[64:67],v2 offset:63488
v_mul_f32 v24,v24,v72
v_mfma_f32_16x16x32_bf16 v[36:39],a[188:191],v[68:71],v[36:39]
s_waitcnt lgkmcnt(3)
v_rcp_f32 v75,v75
v_mfma_f32_16x16x32_bf16 v[32:35],a[192:195],v[40:43],v[32:35]
s_waitcnt vmcnt(11)
ds_read_b128 v[68:71],v2 offset:64512
v_xor_b32 v2,65536,v2
v_mfma_f32_16x16x32_bf16 v[36:39],a[196:199],v[40:43],v[36:39]
s_barrier
v_mfma_f32_16x16x32_bf16 v[32:35],a[200:203],v[44:47],v[32:35]
ds_read_b128 v[40:43],v2 offset:0
ds_read_b64 v[92:93],v11 offset:128
v_mfma_f32_16x16x32_bf16 v[36:39],a[204:207],v[44:47],v[36:39]
buffer_load_dwordx4 v1,s[36:39],s66 offen offset:1024 sc1 lds
v_mfma_f32_16x16x32_bf16 v[32:35],a[208:211],v[48:51],v[32:35]
ds_read_b128 v[44:47],v2 offset:1024
v_mul_f32 v25,v25,v73
v_mfma_f32_16x16x32_bf16 v[36:39],a[212:215],v[48:51],v[36:39]
v_mul_f32 v26,v26,v74
v_mul_f32 v27,v27,v75
v_mfma_f32_16x16x32_bf16 v[32:35],a[216:219],v[52:55],v[32:35]
s_waitcnt lgkmcnt(1)
ds_read_b128 v[48:51],v2 offset:2048
v_cvt_pk_bf16_f32 v80,v24,v25
v_mfma_f32_16x16x32_bf16 v[36:39],a[220:223],v[52:55],v[36:39]
v_cvt_pk_bf16_f32 v81,v26,v27
v_fma_f32 v28,-v96,v20,v28
v_mfma_f32_16x16x32_bf16 v[32:35],a[224:227],v[56:59],v[32:35]
ds_read_b128 v[52:55],v2 offset:3072
v_fma_f32 v29,-v96,v21,v29
v_mfma_f32_16x16x32_bf16 v[36:39],a[228:231],v[56:59],v[36:39]
v_fma_f32 v30,-v96,v22,v30
v_fma_f32 v31,-v96,v23,v31
v_mfma_f32_16x16x32_bf16 v[32:35],a[232:235],v[60:63],v[32:35]
v_mul_f32 v76,v28,v28
v_mul_f32 v77,v29,v29
v_mul_f32 v78,v30,v30
v_mul_f32 v79,v31,v31
v_fma_f32 v76,v98,v76,v97
v_fma_f32 v77,v98,v77,v97
v_fma_f32 v78,v98,v78,v97
v_fma_f32 v79,v98,v79,v97
v_mul_f32 v76,v28,v76
v_mul_f32 v77,v29,v77
v_mul_f32 v78,v30,v78
v_mul_f32 v79,v31,v79
v_exp_f32 v76,v76
v_exp_f32 v77,v77
v_exp_f32 v78,v78
v_exp_f32 v79,v79
v_mfma_f32_16x16x32_bf16 v[36:39],a[236:239],v[60:63],v[36:39]
ds_read_b128 v[56:59],v2 offset:4096
v_fma_f32 v76,v76,v99,v99
v_fma_f32 v77,v77,v99,v99
v_fma_f32 v78,v78,v99,v99
v_fma_f32 v79,v79,v99,v99
v_rcp_f32 v76,v76
v_rcp_f32 v77,v77
v_rcp_f32 v78,v78
v_rcp_f32 v79,v79
v_mfma_f32_16x16x32_bf16 v[32:35],a[240:243],v[64:67],v[32:35]
v_mul_f32 v28,v28,v76
v_mul_f32 v29,v29,v77
v_mul_f32 v30,v30,v78
v_mul_f32 v31,v31,v79
v_cvt_pk_bf16_f32 v82,v28,v29
v_cvt_pk_bf16_f32 v83,v30,v31
v_mfma_f32_16x16x32_bf16 v[36:39],a[244:247],v[64:67],v[36:39]
ds_read_b128 v[60:63],v2 offset:5120
buffer_store_dwordx4 v[80:83],v3,s[36:39],s73 offen offset:0 sc0 sc1
v_xor_b32 v8,16384,v8
v_mfma_f32_16x16x32_bf16 v[32:35],a[248:251],v[68:71],v[32:35]
s_waitcnt lgkmcnt(2)
ds_read_b128 v[64:67],v2 offset:6144
v_mfma_f32_16x16x32_bf16 v[36:39],a[252:255],v[68:71],v[36:39]
s_cbranch_scc1 Lg3o_d0w2_%=
s_branch Lg3o_slow0_%=
Lg3o_d0w3_%=:
v_mfma_f32_16x16x32_bf16 v[24:27],a[0:3],v[40:43],0
s_add_u32 m0,s62,16384
ds_read_b128 v[68:71],v2 offset:7168
v_mul_f32 v96,s75,v92
v_mfma_f32_16x16x32_bf16 v[28:31],a[4:7],v[40:43],0
s_lshr_b32 s72,s66,5
v_fma_f32 v93,v93,s75,v12
v_fma_f32 v93,-v96,v96,v93
v_mfma_f32_16x16x32_bf16 v[24:27],a[8:11],v[44:47],v[24:27]
s_lshr_b32 s71,s62,2
ds_read_b128 v[40:43],v2 offset:8192
v_max_f32 v93,v93,v12
v_mfma_f32_16x16x32_bf16 v[28:31],a[12:15],v[44:47],v[28:31]
s_mul_i32 s73,s47,7
v_rsq_f32 v92,v93
v_mfma_f32_16x16x32_bf16 v[24:27],a[16:19],v[48:51],v[24:27]
buffer_load_dwordx4 v1,s[36:39],s66 offen offset:2048 sc1 lds
v_mfma_f32_16x16x32_bf16 v[28:31],a[20:23],v[48:51],v[28:31]
s_mul_i32 s70,s45,5
ds_read_b128 v[44:47],v2 offset:9216
v_mul_f32 v97,s76,v92
v_mfma_f32_16x16x32_bf16 v[24:27],a[24:27],v[52:55],v[24:27]
ds_read_b128 v[48:51],v2 offset:10240
v_mul_f32 v98,v92,v92
v_mfma_f32_16x16x32_bf16 v[28:31],a[28:31],v[52:55],v[28:31]
s_waitcnt lgkmcnt(3)
v_mul_f32 v99,v93,v92
v_mul_f32 v98,s77,v98
v_mfma_f32_16x16x32_bf16 v[24:27],a[32:35],v[56:59],v[24:27]
buffer_load_dwordx4 v1,s[36:39],s66 offen offset:3072 sc1 lds
v_mfma_f32_16x16x32_bf16 v[28:31],a[36:39],v[56:59],v[28:31]
s_add_u32 m0,s71,s88
ds_read_b128 v[52:55],v2 offset:11264
v_mul_f32 v98,v98,v97
v_mfma_f32_16x16x32_bf16 v[24:27],a[40:43],v[60:63],v[24:27]
buffer_load_dword v9,s[36:39],s72 offen offset:0 sc1 lds
v_mfma_f32_16x16x32_bf16 v[28:31],a[44:47],v[60:63],v[28:31]
s_lshr_b32 s73,s73,1
ds_read_b128 v[56:59],v2 offset:12288
v_fma_f32 v32,-v96,v16,v32
v_mfma_f32_16x16x32_bf16 v[24:27],a[48:51],v[64:67],v[24:27]
s_lshr_b32 s70,s70,6
ds_read_b128 v[60:63],v2 offset:13312
v_fma_f32 v33,-v96,v17,v33
v_mfma_f32_16x16x32_bf16 v[28:31],a[52:55],v[64:67],v[28:31]
s_xor_b32 s69,s64,65536
v_fma_f32 v34,-v96,v18,v34
v_fma_f32 v35,-v96,v19,v35
v_mfma_f32_16x16x32_bf16 v[24:27],a[56:59],v[68:71],v[24:27]
s_cmp_ge_u32 s66,s59
ds_read_b128 v[64:67],v2 offset:14336
v_mul_f32 v72,v32,v32
v_mfma_f32_16x16x32_bf16 v[28:31],a[60:63],v[68:71],v[28:31]
s_waitcnt lgkmcnt(3)
v_mul_f32 v73,v33,v33
v_mul_f32 v74,v34,v34
v_mfma_f32_16x16x32_bf16 v[24:27],a[64:67],v[40:43],v[24:27]
s_waitcnt vmcnt(11)
ds_read_b128 v[68:71],v2 offset:15360
v_mul_f32 v75,v35,v35
v_mfma_f32_16x16x32_bf16 v[28:31],a[68:71],v[40:43],v[28:31]
s_barrier
v_mfma_f32_16x16x32_bf16 v[24:27],a[72:75],v[44:47],v[24:27]
buffer_store_dword v5,v4,s[36:39],s70 offen offset:0 sc0 sc1
ds_read_b128 v[40:43],v2 offset:16384
ds_read_b32 v13,v7 offset:256
v_fma_f32 v72,v98,v72,v97
v_mfma_f32_16x16x32_bf16 v[28:31],a[76:79],v[44:47],v[28:31]
s_cselect_b32 s62,s69,s63
v_mul_f32 v72,v32,v72)"
R"(
v_fma_f32 v73,v98,v73,v97
v_mfma_f32_16x16x32_bf16 v[24:27],a[80:83],v[48:51],v[24:27]
s_cselect_b32 s86,s49,s50
ds_read_b128 v[44:47],v2 offset:17408
v_mul_f32 v73,v33,v73
v_mfma_f32_16x16x32_bf16 v[28:31],a[84:87],v[48:51],v[28:31]
s_mov_b32 m0,s62
v_exp_f32 v72,v72
v_mfma_f32_16x16x32_bf16 v[24:27],a[88:91],v[52:55],v[24:27]
s_waitcnt lgkmcnt(1)
ds_read_b128 v[48:51],v2 offset:18432
ds_read_b32 v88,v8 offset:0
v_mfma_f32_16x16x32_bf16 v[28:31],a[92:95],v[52:55],v[28:31]
v_cmp_eq_u32 vcc,s60,v13
v_exp_f32 v73,v73
v_fma_f32 v72,v72,v99,v99
v_mfma_f32_16x16x32_bf16 v[24:27],a[96:99],v[56:59],v[24:27]
s_cselect_b32 s87,vcc_lo,vcc_hi
ds_read_b128 v[52:55],v2 offset:19456
v_fma_f32 v73,v73,v99,v99
v_mfma_f32_16x16x32_bf16 v[28:31],a[100:103],v[56:59],v[28:31]
s_cmp_eq_u32 s87,-1
v_rcp_f32 v72,v72
v_mfma_f32_16x16x32_bf16 v[24:27],a[104:107],v[60:63],v[24:27]
s_cselect_b32 s67,s86,s59
ds_read_b128 v[56:59],v2 offset:20480
v_fma_f32 v74,v98,v74,v97
v_mfma_f32_16x16x32_bf16 v[28:31],a[108:111],v[60:63],v[28:31]
s_xor_b32 s63,s63,65536
v_rcp_f32 v73,v73
v_mfma_f32_16x16x32_bf16 v[24:27],a[112:115],v[64:67],v[24:27]
s_waitcnt lgkmcnt(2)
ds_read_b128 v[60:63],v2 offset:21504
v_mul_f32 v74,v34,v74
v_mfma_f32_16x16x32_bf16 v[28:31],a[116:119],v[64:67],v[28:31]
s_add_u32 s53,s52,s55
v_exp_f32 v74,v74
v_mfma_f32_16x16x32_bf16 v[24:27],a[120:123],v[68:71],v[24:27]
buffer_load_dwordx4 v0,s[36:39],s67 offen offset:0 sc1 lds
v_mfma_f32_16x16x32_bf16 v[28:31],a[124:127],v[68:71],v[28:31]
s_waitcnt lgkmcnt(2)
ds_read_b128 v[64:67],v2 offset:22528
v_fma_f32 v74,v74,v99,v99
v_mfma_f32_16x16x32_bf16 v[24:27],a[128:131],v[40:43],v[24:27]
s_cmp_eq_u32 s53,s56
ds_read_b128 v[68:71],v2 offset:23552
v_add_f32_dpp v88,v88,v88 quad_perm:[1,0,3,2] row_mask:0xf bank_mask:0xf
v_mfma_f32_16x16x32_bf16 v[28:31],a[132:135],v[40:43],v[28:31]
s_cselect_b32 s53,s57,s53
v_rcp_f32 v74,v74
v_mfma_f32_16x16x32_bf16 v[24:27],a[136:139],v[44:47],v[24:27]
buffer_load_dwordx4 v0,s[36:39],s67 offen offset:1024 sc1 lds
v_mfma_f32_16x16x32_bf16 v[28:31],a[140:143],v[44:47],v[28:31]
s_cmp_eq_u32 s53,s58
ds_read_b128 v[40:43],v2 offset:24576
v_add_f32_dpp v88,v88,v88 quad_perm:[2,3,0,1] row_mask:0xf bank_mask:0xf
v_mfma_f32_16x16x32_bf16 v[24:27],a[144:147],v[48:51],v[24:27]
s_cselect_b32 s53,s59,s53
ds_read_b128 v[44:47],v2 offset:25600
v_add_f32_dpp v88,v88,v88 row_half_mirror row_mask:0xf bank_mask:0xf
v_mfma_f32_16x16x32_bf16 v[28:31],a[148:151],v[48:51],v[28:31]
ds_write_b32 v10,v88 offset:0
v_mfma_f32_16x16x32_bf16 v[24:27],a[152:155],v[52:55],v[24:27]
buffer_load_dwordx4 v0,s[36:39],s67 offen offset:2048 sc1 lds
v_mfma_f32_16x16x32_bf16 v[28:31],a[156:159],v[52:55],v[28:31]
s_waitcnt lgkmcnt(3)
ds_read_b128 v[48:51],v2 offset:26624
v_fma_f32 v75,v98,v75,v97
v_mfma_f32_16x16x32_bf16 v[24:27],a[160:163],v[56:59],v[24:27]
ds_read_b128 v[52:55],v2 offset:27648
v_mul_f32 v75,v35,v75
v_mfma_f32_16x16x32_bf16 v[28:31],a[164:167],v[56:59],v[28:31]
v_exp_f32 v75,v75
v_mfma_f32_16x16x32_bf16 v[24:27],a[168:171],v[60:63],v[24:27]
buffer_load_dwordx4 v0,s[36:39],s67 offen offset:3072 sc1 lds
v_mfma_f32_16x16x32_bf16 v[28:31],a[172:175],v[60:63],v[28:31]
s_add_u32 m0,s62,16384
ds_read_b128 v[56:59],v2 offset:28672
v_fma_f32 v75,v75,v99,v99
v_mfma_f32_16x16x32_bf16 v[24:27],a[176:179],v[64:67],v[24:27]
s_cmp_ge_u32 s66,s59
ds_read_b128 v[60:63],v2 offset:29696
v_mul_f32 v32,v32,v72
v_mfma_f32_16x16x32_bf16 v[28:31],a[180:183],v[64:67],v[28:31]
v_rcp_f32 v75,v75
v_mfma_f32_16x16x32_bf16 v[24:27],a[184:187],v[68:71],v[24:27]
buffer_load_dwordx4 v1,s[36:39],s67 offen offset:0 sc1 lds
v_mfma_f32_16x16x32_bf16 v[28:31],a[188:191],v[68:71],v[28:31]
s_waitcnt lgkmcnt(2)
ds_read_b128 v[64:67],v2 offset:30720
v_mul_f32 v33,v33,v73
v_mfma_f32_16x16x32_bf16 v[24:27],a[192:195],v[40:43],v[24:27]
s_waitcnt vmcnt(12)
ds_read_b128 v[68:71],v2 offset:31744
v_mul_f32 v34,v34,v74
v_mfma_f32_16x16x32_bf16 v[28:31],a[196:199],v[40:43],v[28:31]
s_barrier
v_mfma_f32_16x16x32_bf16 v[24:27],a[200:203],v[44:47],v[24:27]
ds_read_b128 v[40:43],v2 offset:32768
ds_read_b64 v[92:93],v11 offset:0
v_mfma_f32_16x16x32_bf16 v[28:31],a[204:207],v[44:47],v[28:31]
v_mul_f32 v35,v35,v75
v_cvt_pk_bf16_f32 v80,v32,v33
v_mfma_f32_16x16x32_bf16 v[24:27],a[208:211],v[48:51],v[24:27]
buffer_load_dwordx4 v1,s[36:39],s67 offen offset:1024 sc1 lds
v_mfma_f32_16x16x32_bf16 v[28:31],a[212:215],v[48:51],v[28:31]
ds_read_b128 v[44:47],v2 offset:33792
v_cvt_pk_bf16_f32 v81,v34,v35
v_mfma_f32_16x16x32_bf16 v[24:27],a[216:219],v[52:55],v[24:27]
s_waitcnt lgkmcnt(1)
ds_read_b128 v[48:51],v2 offset:34816
v_fma_f32 v36,-v96,v20,v36
v_mfma_f32_16x16x32_bf16 v[28:31],a[220:223],v[52:55],v[28:31]
v_fma_f32 v37,-v96,v21,v37
v_fma_f32 v38,-v96,v22,v38
v_mfma_f32_16x16x32_bf16 v[24:27],a[224:227],v[56:59],v[24:27]
ds_read_b128 v[52:55],v2 offset:35840
v_fma_f32 v39,-v96,v23,v39
v_mfma_f32_16x16x32_bf16 v[28:31],a[228:231],v[56:59],v[28:31]
v_mul_f32 v76,v36,v36
v_mul_f32 v77,v37,v37
v_mfma_f32_16x16x32_bf16 v[24:27],a[232:235],v[60:63],v[24:27]
ds_read_b128 v[56:59],v2 offset:36864
v_mul_f32 v78,v38,v38
v_mfma_f32_16x16x32_bf16 v[28:31],a[236:239],v[60:63],v[28:31]
v_mul_f32 v79,v39,v39
v_fma_f32 v76,v98,v76,v97
v_fma_f32 v77,v98,v77,v97
v_fma_f32 v78,v98,v78,v97
v_fma_f32 v79,v98,v79,v97
v_mul_f32 v76,v36,v76
v_mul_f32 v77,v37,v77
v_mul_f32 v78,v38,v78
v_mul_f32 v79,v39,v79
v_exp_f32 v76,v76
v_exp_f32 v77,v77
v_exp_f32 v78,v78
v_exp_f32 v79,v79
v_mfma_f32_16x16x32_bf16 v[24:27],a[240:243],v[64:67],v[24:27]
v_fma_f32 v76,v76,v99,v99
v_fma_f32 v77,v77,v99,v99
v_fma_f32 v78,v78,v99,v99
v_fma_f32 v79,v79,v99,v99
v_rcp_f32 v76,v76
v_rcp_f32 v77,v77
v_rcp_f32 v78,v78
v_rcp_f32 v79,v79
v_mfma_f32_16x16x32_bf16 v[28:31],a[244:247],v[64:67],v[28:31]
ds_read_b128 v[60:63],v2 offset:37888
v_mul_f32 v36,v36,v76
v_mul_f32 v37,v37,v77
v_mul_f32 v38,v38,v78
v_mul_f32 v39,v39,v79
v_cvt_pk_bf16_f32 v82,v36,v37
v_cvt_pk_bf16_f32 v83,v38,v39
v_mfma_f32_16x16x32_bf16 v[24:27],a[248:251],v[68:71],v[24:27]
s_waitcnt lgkmcnt(2)
ds_read_b128 v[64:67],v2 offset:38912
buffer_store_dwordx4 v[80:83],v3,s[36:39],s73 offen offset:0 sc0 sc1
v_mfma_f32_16x16x32_bf16 v[28:31],a[252:255],v[68:71],v[28:31]
s_cbranch_scc1 Lg3o_slow1_%=
Lg3o_d1w3_%=:
v_mfma_f32_16x16x32_bf16 v[32:35],a[0:3],v[40:43],0
s_add_u32 m0,s62,16384
ds_read_b128 v[68:71],v2 offset:39936
v_mul_f32 v96,s75,v92
v_mfma_f32_16x16x32_bf16 v[36:39],a[4:7],v[40:43],0
s_lshr_b32 s72,s67,5
v_fma_f32 v93,v93,s75,v12
v_fma_f32 v93,-v96,v96,v93
v_mfma_f32_16x16x32_bf16 v[32:35],a[8:11],v[44:47],v[32:35]
s_lshr_b32 s71,s62,2
ds_read_b128 v[40:43],v2 offset:40960
v_max_f32 v93,v93,v12
v_mfma_f32_16x16x32_bf16 v[36:39],a[12:15],v[44:47],v[36:39]
s_mul_i32 s73,s48,7
v_rsq_f32 v92,v93
v_mfma_f32_16x16x32_bf16 v[32:35],a[16:19],v[48:51],v[32:35]
buffer_load_dwordx4 v1,s[36:39],s67 offen offset:2048 sc1 lds
v_mfma_f32_16x16x32_bf16 v[36:39],a[20:23],v[48:51],v[36:39]
s_mul_i32 s70,s46,5
ds_read_b128 v[44:47],v2 offset:41984
v_mul_f32 v97,s76,v92
v_mfma_f32_16x16x32_bf16 v[32:35],a[24:27],v[52:55],v[32:35]
s_mov_b64 s[44:45],s[46:47]
ds_read_b128 v[48:51],v2 offset:43008
v_mul_f32 v98,v92,v92
v_mfma_f32_16x16x32_bf16 v[36:39],a[28:31],v[52:55],v[36:39]
s_waitcnt lgkmcnt(3)
v_mul_f32 v99,v93,v92
v_mul_f32 v98,s77,v98
v_mfma_f32_16x16x32_bf16 v[32:35],a[32:35],v[56:59],v[32:35]
buffer_load_dwordx4 v1,s[36:39],s67 offen offset:3072 sc1 lds
v_mfma_f32_16x16x32_bf16 v[36:39],a[36:39],v[56:59],v[36:39]
s_add_u32 m0,s71,s88
ds_read_b128 v[52:55],v2 offset:44032
v_mul_f32 v98,v98,v97
v_mfma_f32_16x16x32_bf16 v[32:35],a[40:43],v[60:63],v[32:35]
buffer_load_dword v9,s[36:39],s72 offen offset:0 sc1 lds
v_mfma_f32_16x16x32_bf16 v[36:39],a[44:47],v[60:63],v[36:39])"
R"(
s_lshr_b32 s73,s73,1
ds_read_b128 v[56:59],v2 offset:45056
v_fma_f32 v24,-v96,v16,v24
v_mfma_f32_16x16x32_bf16 v[32:35],a[48:51],v[64:67],v[32:35]
s_lshr_b32 s70,s70,6
ds_read_b128 v[60:63],v2 offset:46080
v_fma_f32 v25,-v96,v17,v25
v_mfma_f32_16x16x32_bf16 v[36:39],a[52:55],v[64:67],v[36:39]
s_xor_b32 s69,s63,65536
v_fma_f32 v26,-v96,v18,v26
v_fma_f32 v27,-v96,v19,v27
v_mfma_f32_16x16x32_bf16 v[32:35],a[56:59],v[68:71],v[32:35]
s_cmp_ge_u32 s67,s59
ds_read_b128 v[64:67],v2 offset:47104
v_mul_f32 v72,v24,v24
v_mfma_f32_16x16x32_bf16 v[36:39],a[60:63],v[68:71],v[36:39]
s_waitcnt lgkmcnt(3)
v_mul_f32 v73,v25,v25
v_mul_f32 v74,v26,v26
v_mfma_f32_16x16x32_bf16 v[32:35],a[64:67],v[40:43],v[32:35]
s_waitcnt vmcnt(11)
ds_read_b128 v[68:71],v2 offset:48128
v_mul_f32 v75,v27,v27
v_mfma_f32_16x16x32_bf16 v[36:39],a[68:71],v[40:43],v[36:39]
s_barrier
v_mfma_f32_16x16x32_bf16 v[32:35],a[72:75],v[44:47],v[32:35]
buffer_store_dword v5,v4,s[36:39],s70 offen offset:0 sc0 sc1
ds_read_b128 v[40:43],v2 offset:49152
ds_read_b32 v13,v7 offset:0
v_fma_f32 v72,v98,v72,v97
v_mfma_f32_16x16x32_bf16 v[36:39],a[76:79],v[44:47],v[36:39]
s_cselect_b32 s62,s69,s64
v_mul_f32 v72,v24,v72
v_fma_f32 v73,v98,v73,v97
v_mfma_f32_16x16x32_bf16 v[32:35],a[80:83],v[48:51],v[32:35]
s_cselect_b32 s86,s50,s51
ds_read_b128 v[44:47],v2 offset:50176
v_mul_f32 v73,v25,v73
v_mfma_f32_16x16x32_bf16 v[36:39],a[84:87],v[48:51],v[36:39]
s_mov_b32 m0,s62
v_exp_f32 v72,v72
v_mfma_f32_16x16x32_bf16 v[32:35],a[88:91],v[52:55],v[32:35]
s_waitcnt lgkmcnt(1)
ds_read_b128 v[48:51],v2 offset:51200
ds_read_b32 v88,v8 offset:8192
v_mfma_f32_16x16x32_bf16 v[36:39],a[92:95],v[52:55],v[36:39]
v_cmp_eq_u32 vcc,s60,v13
v_exp_f32 v73,v73
v_fma_f32 v72,v72,v99,v99
v_mfma_f32_16x16x32_bf16 v[32:35],a[96:99],v[56:59],v[32:35]
s_cselect_b32 s87,vcc_lo,vcc_hi
ds_read_b128 v[52:55],v2 offset:52224
v_fma_f32 v73,v73,v99,v99
v_mfma_f32_16x16x32_bf16 v[36:39],a[100:103],v[56:59],v[36:39]
s_cmp_eq_u32 s87,-1
v_rcp_f32 v72,v72
v_mfma_f32_16x16x32_bf16 v[32:35],a[104:107],v[60:63],v[32:35]
s_cselect_b32 s66,s86,s59
ds_read_b128 v[56:59],v2 offset:53248
v_fma_f32 v74,v98,v74,v97
v_mfma_f32_16x16x32_bf16 v[36:39],a[108:111],v[60:63],v[36:39]
s_xor_b32 s64,s64,65536
v_rcp_f32 v73,v73
v_mfma_f32_16x16x32_bf16 v[32:35],a[112:115],v[64:67],v[32:35]
s_waitcnt lgkmcnt(2)
ds_read_b128 v[60:63],v2 offset:54272
v_mul_f32 v74,v26,v74
v_mfma_f32_16x16x32_bf16 v[36:39],a[116:119],v[64:67],v[36:39]
s_add_u32 s74,s53,s55
v_exp_f32 v74,v74
v_mfma_f32_16x16x32_bf16 v[32:35],a[120:123],v[68:71],v[32:35]
buffer_load_dwordx4 v0,s[36:39],s66 offen offset:0 sc1 lds
v_mfma_f32_16x16x32_bf16 v[36:39],a[124:127],v[68:71],v[36:39]
s_waitcnt lgkmcnt(2)
ds_read_b128 v[64:67],v2 offset:55296
v_fma_f32 v74,v74,v99,v99
v_mfma_f32_16x16x32_bf16 v[32:35],a[128:131],v[40:43],v[32:35]
s_cmp_eq_u32 s74,s56
ds_read_b128 v[68:71],v2 offset:56320
v_add_f32_dpp v88,v88,v88 quad_perm:[1,0,3,2] row_mask:0xf bank_mask:0xf
v_mfma_f32_16x16x32_bf16 v[36:39],a[132:135],v[40:43],v[36:39]
s_cselect_b32 s74,s57,s74
v_rcp_f32 v74,v74
v_mfma_f32_16x16x32_bf16 v[32:35],a[136:139],v[44:47],v[32:35]
buffer_load_dwordx4 v0,s[36:39],s66 offen offset:1024 sc1 lds
v_mfma_f32_16x16x32_bf16 v[36:39],a[140:143],v[44:47],v[36:39]
s_cmp_eq_u32 s74,s58
ds_read_b128 v[40:43],v2 offset:57344
v_add_f32_dpp v88,v88,v88 quad_perm:[2,3,0,1] row_mask:0xf bank_mask:0xf
v_mfma_f32_16x16x32_bf16 v[32:35],a[144:147],v[48:51],v[32:35]
s_cselect_b32 s74,s59,s74
ds_read_b128 v[44:47],v2 offset:58368
v_add_f32_dpp v88,v88,v88 row_half_mirror row_mask:0xf bank_mask:0xf
v_mfma_f32_16x16x32_bf16 v[36:39],a[148:151],v[48:51],v[36:39]
s_mov_b64 s[46:47],s[48:49]
ds_write_b32 v10,v88 offset:128
v_mfma_f32_16x16x32_bf16 v[32:35],a[152:155],v[52:55],v[32:35]
buffer_load_dwordx4 v0,s[36:39],s66 offen offset:2048 sc1 lds
v_mfma_f32_16x16x32_bf16 v[36:39],a[156:159],v[52:55],v[36:39]
s_waitcnt lgkmcnt(3)
ds_read_b128 v[48:51],v2 offset:59392
v_fma_f32 v75,v98,v75,v97
v_mfma_f32_16x16x32_bf16 v[32:35],a[160:163],v[56:59],v[32:35]
s_mov_b64 s[48:49],s[50:51]
ds_read_b128 v[52:55],v2 offset:60416
v_mul_f32 v75,v27,v75
v_mfma_f32_16x16x32_bf16 v[36:39],a[164:167],v[56:59],v[36:39]
s_mov_b64 s[50:51],s[52:53]
v_exp_f32 v75,v75
v_mfma_f32_16x16x32_bf16 v[32:35],a[168:171],v[60:63],v[32:35]
buffer_load_dwordx4 v0,s[36:39],s66 offen offset:3072 sc1 lds
v_mfma_f32_16x16x32_bf16 v[36:39],a[172:175],v[60:63],v[36:39]
s_add_u32 m0,s62,16384
ds_read_b128 v[56:59],v2 offset:61440
v_fma_f32 v75,v75,v99,v99
v_mfma_f32_16x16x32_bf16 v[32:35],a[176:179],v[64:67],v[32:35]
s_mov_b32 s52,s74
ds_read_b128 v[60:63],v2 offset:62464
v_mul_f32 v24,v24,v72
v_mfma_f32_16x16x32_bf16 v[36:39],a[180:183],v[64:67],v[36:39]
s_cmp_lt_u32 s67,s59
v_rcp_f32 v75,v75
v_mfma_f32_16x16x32_bf16 v[32:35],a[184:187],v[68:71],v[32:35]
buffer_load_dwordx4 v1,s[36:39],s66 offen offset:0 sc1 lds
v_mfma_f32_16x16x32_bf16 v[36:39],a[188:191],v[68:71],v[36:39]
s_waitcnt lgkmcnt(2)
ds_read_b128 v[64:67],v2 offset:63488
v_mul_f32 v25,v25,v73
v_mfma_f32_16x16x32_bf16 v[32:35],a[192:195],v[40:43],v[32:35]
s_waitcnt vmcnt(12)
ds_read_b128 v[68:71],v2 offset:64512
v_xor_b32 v2,65536,v2
v_mfma_f32_16x16x32_bf16 v[36:39],a[196:199],v[40:43],v[36:39]
s_barrier
v_mfma_f32_16x16x32_bf16 v[32:35],a[200:203],v[44:47],v[32:35]
ds_read_b128 v[40:43],v2 offset:0
ds_read_b64 v[92:93],v11 offset:128
v_mfma_f32_16x16x32_bf16 v[36:39],a[204:207],v[44:47],v[36:39]
v_mul_f32 v26,v26,v74
v_mul_f32 v27,v27,v75
v_mfma_f32_16x16x32_bf16 v[32:35],a[208:211],v[48:51],v[32:35]
buffer_load_dwordx4 v1,s[36:39],s66 offen offset:1024 sc1 lds
v_mfma_f32_16x16x32_bf16 v[36:39],a[212:215],v[48:51],v[36:39]
ds_read_b128 v[44:47],v2 offset:1024
v_cvt_pk_bf16_f32 v80,v24,v25
v_mfma_f32_16x16x32_bf16 v[32:35],a[216:219],v[52:55],v[32:35]
s_waitcnt lgkmcnt(1)
ds_read_b128 v[48:51],v2 offset:2048
v_cvt_pk_bf16_f32 v81,v26,v27
v_mfma_f32_16x16x32_bf16 v[36:39],a[220:223],v[52:55],v[36:39]
v_fma_f32 v28,-v96,v20,v28
v_fma_f32 v29,-v96,v21,v29
v_mfma_f32_16x16x32_bf16 v[32:35],a[224:227],v[56:59],v[32:35]
ds_read_b128 v[52:55],v2 offset:3072
v_fma_f32 v30,-v96,v22,v30
v_mfma_f32_16x16x32_bf16 v[36:39],a[228:231],v[56:59],v[36:39]
v_fma_f32 v31,-v96,v23,v31
v_mul_f32 v76,v28,v28
v_mfma_f32_16x16x32_bf16 v[32:35],a[232:235],v[60:63],v[32:35]
ds_read_b128 v[56:59],v2 offset:4096
v_mul_f32 v77,v29,v29
v_mfma_f32_16x16x32_bf16 v[36:39],a[236:239],v[60:63],v[36:39]
v_mul_f32 v78,v30,v30
v_mul_f32 v79,v31,v31
v_fma_f32 v76,v98,v76,v97
v_fma_f32 v77,v98,v77,v97
v_fma_f32 v78,v98,v78,v97
v_fma_f32 v79,v98,v79,v97
v_mul_f32 v76,v28,v76
v_mul_f32 v77,v29,v77
v_mul_f32 v78,v30,v78
v_mul_f32 v79,v31,v79
v_exp_f32 v76,v76
v_exp_f32 v77,v77
v_exp_f32 v78,v78
v_exp_f32 v79,v79
v_mfma_f32_16x16x32_bf16 v[32:35],a[240:243],v[64:67],v[32:35]
v_fma_f32 v76,v76,v99,v99
v_fma_f32 v77,v77,v99,v99
v_fma_f32 v78,v78,v99,v99
v_fma_f32 v79,v79,v99,v99
v_rcp_f32 v76,v76
v_rcp_f32 v77,v77
v_rcp_f32 v78,v78
v_rcp_f32 v79,v79
v_mfma_f32_16x16x32_bf16 v[36:39],a[244:247],v[64:67],v[36:39]
ds_read_b128 v[60:63],v2 offset:5120
v_mul_f32 v28,v28,v76
v_mul_f32 v29,v29,v77
v_mul_f32 v30,v30,v78
v_mul_f32 v31,v31,v79
v_cvt_pk_bf16_f32 v82,v28,v29
v_cvt_pk_bf16_f32 v83,v30,v31
v_mfma_f32_16x16x32_bf16 v[32:35],a[248:251],v[68:71],v[32:35]
s_waitcnt lgkmcnt(2)
ds_read_b128 v[64:67],v2 offset:6144
buffer_store_dwordx4 v[80:83],v3,s[36:39],s73 offen offset:0 sc0 sc1
v_xor_b32 v8,16384,v8
v_mfma_f32_16x16x32_bf16 v[36:39],a[252:255],v[68:71],v[36:39]
s_cbranch_scc1 Lg3o_d0w3_%=)"
R"(
s_branch Lg3o_slow0_%=
Lg3o_slow0_%=:
s_add_u32 m0,s62,16384
s_lshr_b32 s72,s66,5
s_lshr_b32 s71,s62,2
buffer_load_dwordx4 v1,s[36:39],s66 offen offset:2048 sc1 lds
buffer_load_dwordx4 v1,s[36:39],s66 offen offset:3072 sc1 lds
s_add_u32 m0,s71,s88
s_nop 0
buffer_load_dword v9,s[36:39],s72 offen offset:0 sc1 lds
s_cmp_eq_u32 s54,1
s_cselect_b32 s68,s49,s48
s_lshr_b32 s68,s68,5
s_add_u32 m0,s78,8192
s_nop 0
buffer_load_dword v6,s[36:39],s68 offen offset:0 sc1 lds
s_mul_i32 s73,s47,7
s_lshr_b32 s73,s73,1
v_mul_f32 v96,s75,v92
v_fma_f32 v93,v93,s75,v12
v_fma_f32 v93,-v96,v96,v93
v_max_f32 v93,v93,v12
v_rsq_f32 v92,v93
s_nop 0
v_mul_f32 v97,s76,v92
v_mul_f32 v98,v92,v92
v_mul_f32 v99,v93,v92
v_mul_f32 v98,s77,v98
v_mul_f32 v98,v98,v97
v_fma_f32 v32,-v96,v16,v32
v_fma_f32 v33,-v96,v17,v33
v_fma_f32 v34,-v96,v18,v34
v_fma_f32 v35,-v96,v19,v35
v_mul_f32 v72,v32,v32
v_mul_f32 v73,v33,v33
v_mul_f32 v74,v34,v34
v_mul_f32 v75,v35,v35
v_fma_f32 v72,v98,v72,v97
v_fma_f32 v73,v98,v73,v97
v_fma_f32 v74,v98,v74,v97
v_fma_f32 v75,v98,v75,v97
v_mul_f32 v72,v32,v72
v_mul_f32 v73,v33,v73
v_mul_f32 v74,v34,v74
v_mul_f32 v75,v35,v75
v_exp_f32 v72,v72
v_exp_f32 v73,v73
v_exp_f32 v74,v74
v_exp_f32 v75,v75
v_fma_f32 v72,v72,v99,v99
v_fma_f32 v73,v73,v99,v99
v_fma_f32 v74,v74,v99,v99
v_fma_f32 v75,v75,v99,v99
v_rcp_f32 v72,v72
v_rcp_f32 v73,v73
v_rcp_f32 v74,v74
v_rcp_f32 v75,v75
v_mul_f32 v32,v32,v72
s_add_u32 m0,s78,8448
s_nop 0
buffer_load_dword v6,s[36:39],s68 offen offset:0 sc1 lds
v_mul_f32 v33,v33,v73
v_mul_f32 v34,v34,v74
v_mul_f32 v35,v35,v75
v_cvt_pk_bf16_f32 v80,v32,v33
v_cvt_pk_bf16_f32 v81,v34,v35
v_fma_f32 v36,-v96,v20,v36
v_fma_f32 v37,-v96,v21,v37
v_fma_f32 v38,-v96,v22,v38
v_fma_f32 v39,-v96,v23,v39
v_mul_f32 v76,v36,v36
v_mul_f32 v77,v37,v37
v_mul_f32 v78,v38,v38
v_mul_f32 v79,v39,v39
v_fma_f32 v76,v98,v76,v97
v_fma_f32 v77,v98,v77,v97
v_fma_f32 v78,v98,v78,v97
v_fma_f32 v79,v98,v79,v97
v_mul_f32 v76,v36,v76
v_mul_f32 v77,v37,v77
v_mul_f32 v78,v38,v78
v_mul_f32 v79,v39,v79
v_exp_f32 v76,v76
v_exp_f32 v77,v77
v_exp_f32 v78,v78
v_exp_f32 v79,v79
v_fma_f32 v76,v76,v99,v99
v_fma_f32 v77,v77,v99,v99
v_fma_f32 v78,v78,v99,v99
v_fma_f32 v79,v79,v99,v99
v_rcp_f32 v76,v76
v_rcp_f32 v77,v77
v_rcp_f32 v78,v78
v_rcp_f32 v79,v79
v_mul_f32 v36,v36,v76
v_mul_f32 v37,v37,v77
v_mul_f32 v38,v38,v78
v_mul_f32 v39,v39,v79
v_cvt_pk_bf16_f32 v82,v36,v37
v_cvt_pk_bf16_f32 v83,v38,v39
buffer_store_dwordx4 v[80:83],v3,s[36:39],s73 offen offset:0 sc0 sc1
s_cmp_ge_u32 s48,s59
s_cbranch_scc1 Lg3o_done0_%=
s_add_u32 m0,s78,16384
s_nop 0
buffer_load_dword v6,s[36:39],s68 offen offset:0 sc1 lds
s_waitcnt vmcnt(3)
s_barrier
ds_read_b32 v13,v7 offset:8192
s_mul_i32 s70,s45,5
s_lshr_b32 s70,s70,6
buffer_store_dword v5,v4,s[36:39],s70 offen offset:0 sc0 sc1
s_mov_b32 s45,s59
s_mul_i32 s70,s46,5
s_lshr_b32 s70,s70,6
buffer_store_dword v5,v4,s[36:39],s70 offen offset:0 sc0 sc1
s_mov_b32 s46,s59
s_mov_b32 s69,18432
s_waitcnt lgkmcnt(0)
v_cmp_eq_u32 vcc,s60,v13
s_nop 0
s_cmp_eq_u32 vcc_lo,-1
s_cbranch_scc1 Lg3o_go0_%=
s_add_u32 m0,s78,16640
s_nop 0
buffer_load_dword v6,s[36:39],s68 offen offset:0 sc1 lds
s_waitcnt vmcnt(5)
s_barrier
ds_read_b32 v13,v7 offset:8448
s_mov_b32 s69,18688
s_waitcnt lgkmcnt(0)
v_cmp_eq_u32 vcc,s60,v13
s_nop 0
s_cmp_eq_u32 vcc_lo,-1
s_cbranch_scc1 Lg3o_go0_%=
s_add_u32 m0,s78,8192
s_nop 0
buffer_load_dword v6,s[36:39],s68 offen offset:0 sc1 lds
s_waitcnt vmcnt(4)
s_barrier
ds_read_b32 v13,v7 offset:16384
s_mul_i32 s70,s47,5
s_lshr_b32 s70,s70,6
buffer_store_dword v5,v4,s[36:39],s70 offen offset:0 sc0 sc1
s_mov_b32 s47,s59
s_mov_b32 s69,10240
s_waitcnt lgkmcnt(0)
v_cmp_eq_u32 vcc,s60,v13
s_nop 0
s_cmp_eq_u32 vcc_lo,-1
s_cbranch_scc1 Lg3o_go0_%=
s_add_u32 m0,s78,8448
s_nop 0
buffer_load_dword v6,s[36:39],s68 offen offset:0 sc1 lds
s_waitcnt vmcnt(3)
s_barrier
ds_read_b32 v13,v7 offset:16640
s_mov_b32 s69,10496
s_waitcnt lgkmcnt(0)
v_cmp_eq_u32 vcc,s60,v13
s_nop 0
s_cmp_eq_u32 vcc_lo,-1
s_cbranch_scc1 Lg3o_go0_%=
Lg3o_poll0_%=:
s_add_u32 m0,s78,16384
s_nop 0
buffer_load_dword v6,s[36:39],s68 offen offset:0 sc1 lds
s_waitcnt vmcnt(2)
s_barrier
ds_read_b32 v13,v7 offset:8192
s_mov_b32 s69,18432
s_waitcnt lgkmcnt(0)
v_cmp_eq_u32 vcc,s60,v13
s_nop 0
s_cmp_eq_u32 vcc_lo,-1
s_cbranch_scc1 Lg3o_go0_%=
s_sleep 4
s_add_u32 m0,s78,16640
s_nop 0
buffer_load_dword v6,s[36:39],s68 offen offset:0 sc1 lds
s_waitcnt vmcnt(2)
s_barrier
ds_read_b32 v13,v7 offset:8448
s_mov_b32 s69,18688
s_waitcnt lgkmcnt(0)
v_cmp_eq_u32 vcc,s60,v13
s_nop 0
s_cmp_eq_u32 vcc_lo,-1
s_cbranch_scc1 Lg3o_go0_%=
s_sleep 4
s_add_u32 m0,s78,8192
s_nop 0
buffer_load_dword v6,s[36:39],s68 offen offset:0 sc1 lds
s_waitcnt vmcnt(2)
s_barrier
ds_read_b32 v13,v7 offset:16384
s_mov_b32 s69,10240
s_waitcnt lgkmcnt(0)
v_cmp_eq_u32 vcc,s60,v13
s_nop 0)"
R"(
s_cmp_eq_u32 vcc_lo,-1
s_cbranch_scc1 Lg3o_go0_%=
s_sleep 4
s_add_u32 m0,s78,8448
s_nop 0
buffer_load_dword v6,s[36:39],s68 offen offset:0 sc1 lds
s_waitcnt vmcnt(2)
s_barrier
ds_read_b32 v13,v7 offset:16640
s_mov_b32 s69,10496
s_waitcnt lgkmcnt(0)
v_cmp_eq_u32 vcc,s60,v13
s_nop 0
s_cmp_eq_u32 vcc_lo,-1
s_cbranch_scc1 Lg3o_go0_%=
s_sleep 4
s_add_u32 s79,s79,1
s_cmp_lt_u32 s79,20000
s_cbranch_scc1 Lg3o_poll0_%=
Lg3o_go0_%=:
s_xor_b32 s62,s63,65536
s_cmp_lt_u32 s66,s59
s_cbranch_scc1 Lg3o_gs0_%=
s_mov_b32 m0,s62
s_nop 0
buffer_load_dwordx4 v0,s[36:39],s48 offen offset:0 sc1 lds
buffer_load_dwordx4 v0,s[36:39],s48 offen offset:1024 sc1 lds
buffer_load_dwordx4 v0,s[36:39],s48 offen offset:2048 sc1 lds
buffer_load_dwordx4 v0,s[36:39],s48 offen offset:3072 sc1 lds
s_add_u32 m0,s62,16384
s_lshr_b32 s72,s48,5
s_lshr_b32 s71,s62,2
buffer_load_dwordx4 v1,s[36:39],s48 offen offset:0 sc1 lds
buffer_load_dwordx4 v1,s[36:39],s48 offen offset:1024 sc1 lds
buffer_load_dwordx4 v1,s[36:39],s48 offen offset:2048 sc1 lds
buffer_load_dwordx4 v1,s[36:39],s48 offen offset:3072 sc1 lds
s_add_u32 m0,s71,s88
s_nop 0
buffer_load_dword v9,s[36:39],s72 offen offset:0 sc1 lds
s_cmp_eq_u32 s54,1
s_cselect_b32 s68,s50,s49
s_lshr_b32 s68,s68,5
s_add_u32 m0,s78,256
s_nop 0
buffer_load_dword v6,s[36:39],s68 offen offset:0 sc1 lds
s_cmp_eq_u32 vcc_hi,-1
s_cselect_b32 s66,s49,s59
s_xor_b32 s62,s64,65536
s_mov_b32 m0,s62
s_nop 0
buffer_load_dwordx4 v0,s[36:39],s66 offen offset:0 sc1 lds
buffer_load_dwordx4 v0,s[36:39],s66 offen offset:1024 sc1 lds
buffer_load_dwordx4 v0,s[36:39],s66 offen offset:2048 sc1 lds
buffer_load_dwordx4 v0,s[36:39],s66 offen offset:3072 sc1 lds
s_add_u32 m0,s62,16384
s_nop 0
buffer_load_dwordx4 v1,s[36:39],s66 offen offset:0 sc1 lds
buffer_load_dwordx4 v1,s[36:39],s66 offen offset:1024 sc1 lds
s_waitcnt vmcnt(12)
s_barrier
ds_read_b128 v[40:43],v2 offset:0
ds_read_b128 v[44:47],v2 offset:1024
ds_read_b128 v[48:51],v2 offset:2048
ds_read_b128 v[52:55],v2 offset:3072
ds_read_b128 v[56:59],v2 offset:4096
ds_read_b128 v[60:63],v2 offset:5120
ds_read_b128 v[64:67],v2 offset:6144
s_mul_i32 s70,s45,5
s_lshr_b32 s70,s70,6
buffer_store_dword v5,v4,s[36:39],s70 offen offset:0 sc0 sc1
s_mov_b32 s45,s59
s_mul_i32 s70,s46,5
s_lshr_b32 s70,s70,6
buffer_store_dword v5,v4,s[36:39],s70 offen offset:0 sc0 sc1
s_mov_b32 s46,s59
s_mul_i32 s70,s47,5
s_lshr_b32 s70,s70,6
buffer_store_dword v5,v4,s[36:39],s70 offen offset:0 sc0 sc1
s_mov_b32 s47,s59
s_waitcnt lgkmcnt(3)
s_setpc_b64 s[90:91]
Lg3o_gs0_%=:
ds_read_b128 v[40:43],v2 offset:0
ds_read_b128 v[44:47],v2 offset:1024
ds_read_b128 v[48:51],v2 offset:2048
ds_read_b128 v[52:55],v2 offset:3072
ds_read_b128 v[56:59],v2 offset:4096
ds_read_b128 v[60:63],v2 offset:5120
ds_read_b128 v[64:67],v2 offset:6144
s_cmp_eq_u32 s54,1
s_cselect_b32 s68,s50,s49
s_lshr_b32 s68,s68,5
s_add_u32 m0,s78,256
s_nop 0
buffer_load_dword v6,s[36:39],s68 offen offset:0 sc1 lds
s_cmp_eq_u32 vcc_hi,-1
s_cselect_b32 s66,s49,s59
s_xor_b32 s62,s64,65536
s_mov_b32 m0,s62
s_nop 0
buffer_load_dwordx4 v0,s[36:39],s66 offen offset:0 sc1 lds
buffer_load_dwordx4 v0,s[36:39],s66 offen offset:1024 sc1 lds
buffer_load_dwordx4 v0,s[36:39],s66 offen offset:2048 sc1 lds
buffer_load_dwordx4 v0,s[36:39],s66 offen offset:3072 sc1 lds
s_add_u32 m0,s62,16384
s_nop 0
buffer_load_dwordx4 v1,s[36:39],s66 offen offset:0 sc1 lds
buffer_load_dwordx4 v1,s[36:39],s66 offen offset:1024 sc1 lds
s_mov_b32 s45,s47
s_mov_b32 s47,s59
s_mul_i32 s70,s59,5
s_lshr_b32 s70,s70,6
buffer_store_dword v5,v4,s[36:39],s70 offen offset:0 sc0 sc1
s_waitcnt lgkmcnt(3)
s_setpc_b64 s[90:91]
Lg3o_done0_%=:
s_waitcnt vmcnt(0)
s_barrier
s_mul_i32 s70,s45,5
s_lshr_b32 s70,s70,6
buffer_store_dword v5,v4,s[36:39],s70 offen offset:0 sc0 sc1
s_mul_i32 s70,s46,5
s_lshr_b32 s70,s70,6
buffer_store_dword v5,v4,s[36:39],s70 offen offset:0 sc0 sc1
s_mul_i32 s70,s47,5
s_lshr_b32 s70,s70,6
buffer_store_dword v5,v4,s[36:39],s70 offen offset:0 sc0 sc1
s_branch Lg3o_done_%=
Lg3o_slow1_%=:
s_add_u32 m0,s62,16384
s_lshr_b32 s72,s67,5
s_lshr_b32 s71,s62,2
buffer_load_dwordx4 v1,s[36:39],s67 offen offset:2048 sc1 lds
buffer_load_dwordx4 v1,s[36:39],s67 offen offset:3072 sc1 lds
s_add_u32 m0,s71,s88
s_nop 0
buffer_load_dword v9,s[36:39],s72 offen offset:0 sc1 lds
s_cmp_eq_u32 s54,1
s_cselect_b32 s68,s50,s49
s_lshr_b32 s68,s68,5
s_add_u32 m0,s78,8192
s_nop 0
buffer_load_dword v6,s[36:39],s68 offen offset:0 sc1 lds
s_mul_i32 s73,s48,7
s_lshr_b32 s73,s73,1
v_mul_f32 v96,s75,v92
v_fma_f32 v93,v93,s75,v12
v_fma_f32 v93,-v96,v96,v93
v_max_f32 v93,v93,v12
v_rsq_f32 v92,v93
s_nop 0
v_mul_f32 v97,s76,v92
v_mul_f32 v98,v92,v92
v_mul_f32 v99,v93,v92
v_mul_f32 v98,s77,v98
v_mul_f32 v98,v98,v97
v_fma_f32 v24,-v96,v16,v24
v_fma_f32 v25,-v96,v17,v25
v_fma_f32 v26,-v96,v18,v26
v_fma_f32 v27,-v96,v19,v27
v_mul_f32 v72,v24,v24
v_mul_f32 v73,v25,v25
v_mul_f32 v74,v26,v26
v_mul_f32 v75,v27,v27
v_fma_f32 v72,v98,v72,v97
v_fma_f32 v73,v98,v73,v97
v_fma_f32 v74,v98,v74,v97
v_fma_f32 v75,v98,v75,v97
v_mul_f32 v72,v24,v72
v_mul_f32 v73,v25,v73
v_mul_f32 v74,v26,v74
v_mul_f32 v75,v27,v75
v_exp_f32 v72,v72
v_exp_f32 v73,v73
v_exp_f32 v74,v74
v_exp_f32 v75,v75
v_fma_f32 v72,v72,v99,v99
v_fma_f32 v73,v73,v99,v99
v_fma_f32 v74,v74,v99,v99
v_fma_f32 v75,v75,v99,v99
v_rcp_f32 v72,v72
v_rcp_f32 v73,v73
v_rcp_f32 v74,v74
v_rcp_f32 v75,v75
v_mul_f32 v24,v24,v72
s_add_u32 m0,s78,8448
s_nop 0
buffer_load_dword v6,s[36:39],s68 offen offset:0 sc1 lds
v_mul_f32 v25,v25,v73
v_mul_f32 v26,v26,v74
v_mul_f32 v27,v27,v75
v_cvt_pk_bf16_f32 v80,v24,v25
v_cvt_pk_bf16_f32 v81,v26,v27
v_fma_f32 v28,-v96,v20,v28
v_fma_f32 v29,-v96,v21,v29
v_fma_f32 v30,-v96,v22,v30
v_fma_f32 v31,-v96,v23,v31
v_mul_f32 v76,v28,v28
v_mul_f32 v77,v29,v29
v_mul_f32 v78,v30,v30)"
R"(
v_mul_f32 v79,v31,v31
v_fma_f32 v76,v98,v76,v97
v_fma_f32 v77,v98,v77,v97
v_fma_f32 v78,v98,v78,v97
v_fma_f32 v79,v98,v79,v97
v_mul_f32 v76,v28,v76
v_mul_f32 v77,v29,v77
v_mul_f32 v78,v30,v78
v_mul_f32 v79,v31,v79
v_exp_f32 v76,v76
v_exp_f32 v77,v77
v_exp_f32 v78,v78
v_exp_f32 v79,v79
v_fma_f32 v76,v76,v99,v99
v_fma_f32 v77,v77,v99,v99
v_fma_f32 v78,v78,v99,v99
v_fma_f32 v79,v79,v99,v99
v_rcp_f32 v76,v76
v_rcp_f32 v77,v77
v_rcp_f32 v78,v78
v_rcp_f32 v79,v79
v_mul_f32 v28,v28,v76
v_mul_f32 v29,v29,v77
v_mul_f32 v30,v30,v78
v_mul_f32 v31,v31,v79
v_cvt_pk_bf16_f32 v82,v28,v29
v_cvt_pk_bf16_f32 v83,v30,v31
buffer_store_dwordx4 v[80:83],v3,s[36:39],s73 offen offset:0 sc0 sc1
s_cmp_ge_u32 s49,s59
s_cbranch_scc1 Lg3o_done1_%=
s_add_u32 m0,s78,16384
s_nop 0
buffer_load_dword v6,s[36:39],s68 offen offset:0 sc1 lds
s_waitcnt vmcnt(3)
s_barrier
ds_read_b32 v13,v7 offset:8192
s_mul_i32 s70,s46,5
s_lshr_b32 s70,s70,6
buffer_store_dword v5,v4,s[36:39],s70 offen offset:0 sc0 sc1
s_mov_b32 s46,s59
s_mul_i32 s70,s47,5
s_lshr_b32 s70,s70,6
buffer_store_dword v5,v4,s[36:39],s70 offen offset:0 sc0 sc1
s_mov_b32 s47,s59
s_mov_b32 s69,18432
s_waitcnt lgkmcnt(0)
v_cmp_eq_u32 vcc,s60,v13
s_nop 0
s_cmp_eq_u32 vcc_lo,-1
s_cbranch_scc1 Lg3o_go1_%=
s_add_u32 m0,s78,16640
s_nop 0
buffer_load_dword v6,s[36:39],s68 offen offset:0 sc1 lds
s_waitcnt vmcnt(5)
s_barrier
ds_read_b32 v13,v7 offset:8448
s_mov_b32 s69,18688
s_waitcnt lgkmcnt(0)
v_cmp_eq_u32 vcc,s60,v13
s_nop 0
s_cmp_eq_u32 vcc_lo,-1
s_cbranch_scc1 Lg3o_go1_%=
s_add_u32 m0,s78,8192
s_nop 0
buffer_load_dword v6,s[36:39],s68 offen offset:0 sc1 lds
s_waitcnt vmcnt(4)
s_barrier
ds_read_b32 v13,v7 offset:16384
s_mul_i32 s70,s48,5
s_lshr_b32 s70,s70,6
buffer_store_dword v5,v4,s[36:39],s70 offen offset:0 sc0 sc1
s_mov_b32 s48,s59
s_mov_b32 s69,10240
s_waitcnt lgkmcnt(0)
v_cmp_eq_u32 vcc,s60,v13
s_nop 0
s_cmp_eq_u32 vcc_lo,-1
s_cbranch_scc1 Lg3o_go1_%=
s_add_u32 m0,s78,8448
s_nop 0
buffer_load_dword v6,s[36:39],s68 offen offset:0 sc1 lds
s_waitcnt vmcnt(3)
s_barrier
ds_read_b32 v13,v7 offset:16640
s_mov_b32 s69,10496
s_waitcnt lgkmcnt(0)
v_cmp_eq_u32 vcc,s60,v13
s_nop 0
s_cmp_eq_u32 vcc_lo,-1
s_cbranch_scc1 Lg3o_go1_%=
Lg3o_poll1_%=:
s_add_u32 m0,s78,16384
s_nop 0
buffer_load_dword v6,s[36:39],s68 offen offset:0 sc1 lds
s_waitcnt vmcnt(2)
s_barrier
ds_read_b32 v13,v7 offset:8192
s_mov_b32 s69,18432
s_waitcnt lgkmcnt(0)
v_cmp_eq_u32 vcc,s60,v13
s_nop 0
s_cmp_eq_u32 vcc_lo,-1
s_cbranch_scc1 Lg3o_go1_%=
s_sleep 4
s_add_u32 m0,s78,16640
s_nop 0
buffer_load_dword v6,s[36:39],s68 offen offset:0 sc1 lds
s_waitcnt vmcnt(2)
s_barrier
ds_read_b32 v13,v7 offset:8448
s_mov_b32 s69,18688
s_waitcnt lgkmcnt(0)
v_cmp_eq_u32 vcc,s60,v13
s_nop 0
s_cmp_eq_u32 vcc_lo,-1
s_cbranch_scc1 Lg3o_go1_%=
s_sleep 4
s_add_u32 m0,s78,8192
s_nop 0
buffer_load_dword v6,s[36:39],s68 offen offset:0 sc1 lds
s_waitcnt vmcnt(2)
s_barrier
ds_read_b32 v13,v7 offset:16384
s_mov_b32 s69,10240
s_waitcnt lgkmcnt(0)
v_cmp_eq_u32 vcc,s60,v13
s_nop 0
s_cmp_eq_u32 vcc_lo,-1
s_cbranch_scc1 Lg3o_go1_%=
s_sleep 4
s_add_u32 m0,s78,8448
s_nop 0
buffer_load_dword v6,s[36:39],s68 offen offset:0 sc1 lds
s_waitcnt vmcnt(2)
s_barrier
ds_read_b32 v13,v7 offset:16640
s_mov_b32 s69,10496
s_waitcnt lgkmcnt(0)
v_cmp_eq_u32 vcc,s60,v13
s_nop 0
s_cmp_eq_u32 vcc_lo,-1
s_cbranch_scc1 Lg3o_go1_%=
s_sleep 4
s_add_u32 s79,s79,1
s_cmp_lt_u32 s79,20000
s_cbranch_scc1 Lg3o_poll1_%=
Lg3o_go1_%=:
s_xor_b32 s62,s64,65536
s_cmp_lt_u32 s67,s59
s_cbranch_scc1 Lg3o_gs1_%=
s_mov_b32 m0,s62
s_nop 0
buffer_load_dwordx4 v0,s[36:39],s49 offen offset:0 sc1 lds
buffer_load_dwordx4 v0,s[36:39],s49 offen offset:1024 sc1 lds
buffer_load_dwordx4 v0,s[36:39],s49 offen offset:2048 sc1 lds
buffer_load_dwordx4 v0,s[36:39],s49 offen offset:3072 sc1 lds
s_add_u32 m0,s62,16384
s_lshr_b32 s72,s49,5
s_lshr_b32 s71,s62,2
buffer_load_dwordx4 v1,s[36:39],s49 offen offset:0 sc1 lds
buffer_load_dwordx4 v1,s[36:39],s49 offen offset:1024 sc1 lds
buffer_load_dwordx4 v1,s[36:39],s49 offen offset:2048 sc1 lds
buffer_load_dwordx4 v1,s[36:39],s49 offen offset:3072 sc1 lds
s_add_u32 m0,s71,s88
s_nop 0
buffer_load_dword v9,s[36:39],s72 offen offset:0 sc1 lds
s_cmp_eq_u32 s54,1
s_cselect_b32 s68,s51,s50
s_lshr_b32 s68,s68,5
s_add_u32 m0,s78,0
s_nop 0
buffer_load_dword v6,s[36:39],s68 offen offset:0 sc1 lds
s_cmp_eq_u32 vcc_hi,-1
s_cselect_b32 s67,s50,s59
s_xor_b32 s62,s63,65536
s_mov_b32 m0,s62
s_nop 0
buffer_load_dwordx4 v0,s[36:39],s67 offen offset:0 sc1 lds
buffer_load_dwordx4 v0,s[36:39],s67 offen offset:1024 sc1 lds
buffer_load_dwordx4 v0,s[36:39],s67 offen offset:2048 sc1 lds
buffer_load_dwordx4 v0,s[36:39],s67 offen offset:3072 sc1 lds
s_add_u32 m0,s62,16384
s_nop 0
buffer_load_dwordx4 v1,s[36:39],s67 offen offset:0 sc1 lds
buffer_load_dwordx4 v1,s[36:39],s67 offen offset:1024 sc1 lds
s_waitcnt vmcnt(12)
s_barrier
ds_read_b128 v[40:43],v2 offset:32768
ds_read_b128 v[44:47],v2 offset:33792
ds_read_b128 v[48:51],v2 offset:34816
ds_read_b128 v[52:55],v2 offset:35840
ds_read_b128 v[56:59],v2 offset:36864
ds_read_b128 v[60:63],v2 offset:37888
ds_read_b128 v[64:67],v2 offset:38912
s_mul_i32 s70,s46,5
s_lshr_b32 s70,s70,6
buffer_store_dword v5,v4,s[36:39],s70 offen offset:0 sc0 sc1
s_mov_b32 s46,s59
s_mul_i32 s70,s47,5
s_lshr_b32 s70,s70,6)"
R"(
buffer_store_dword v5,v4,s[36:39],s70 offen offset:0 sc0 sc1
s_mov_b32 s47,s59
s_mul_i32 s70,s48,5
s_lshr_b32 s70,s70,6
buffer_store_dword v5,v4,s[36:39],s70 offen offset:0 sc0 sc1
s_mov_b32 s48,s59
s_waitcnt lgkmcnt(3)
s_setpc_b64 s[92:93]
Lg3o_gs1_%=:
ds_read_b128 v[40:43],v2 offset:32768
ds_read_b128 v[44:47],v2 offset:33792
ds_read_b128 v[48:51],v2 offset:34816
ds_read_b128 v[52:55],v2 offset:35840
ds_read_b128 v[56:59],v2 offset:36864
ds_read_b128 v[60:63],v2 offset:37888
ds_read_b128 v[64:67],v2 offset:38912
s_cmp_eq_u32 s54,1
s_cselect_b32 s68,s51,s50
s_lshr_b32 s68,s68,5
s_add_u32 m0,s78,0
s_nop 0
buffer_load_dword v6,s[36:39],s68 offen offset:0 sc1 lds
s_cmp_eq_u32 vcc_hi,-1
s_cselect_b32 s67,s50,s59
s_xor_b32 s62,s63,65536
s_mov_b32 m0,s62
s_nop 0
buffer_load_dwordx4 v0,s[36:39],s67 offen offset:0 sc1 lds
buffer_load_dwordx4 v0,s[36:39],s67 offen offset:1024 sc1 lds
buffer_load_dwordx4 v0,s[36:39],s67 offen offset:2048 sc1 lds
buffer_load_dwordx4 v0,s[36:39],s67 offen offset:3072 sc1 lds
s_add_u32 m0,s62,16384
s_nop 0
buffer_load_dwordx4 v1,s[36:39],s67 offen offset:0 sc1 lds
buffer_load_dwordx4 v1,s[36:39],s67 offen offset:1024 sc1 lds
s_mov_b32 s46,s48
s_mov_b32 s48,s59
s_mul_i32 s70,s59,5
s_lshr_b32 s70,s70,6
buffer_store_dword v5,v4,s[36:39],s70 offen offset:0 sc0 sc1
s_waitcnt lgkmcnt(3)
s_setpc_b64 s[92:93]
Lg3o_done1_%=:
s_waitcnt vmcnt(0)
s_barrier
s_mul_i32 s70,s46,5
s_lshr_b32 s70,s70,6
buffer_store_dword v5,v4,s[36:39],s70 offen offset:0 sc0 sc1
s_mul_i32 s70,s47,5
s_lshr_b32 s70,s70,6
buffer_store_dword v5,v4,s[36:39],s70 offen offset:0 sc0 sc1
s_mul_i32 s70,s48,5
s_lshr_b32 s70,s70,6
buffer_store_dword v5,v4,s[36:39],s70 offen offset:0 sc0 sc1
s_branch Lg3o_done_%=
Lg3o_done_%=:
s_waitcnt vmcnt(0) lgkmcnt(0)
s_barrier
s_endpgm
Lcode_g3o_e:)"
  :: "s"(ws), "s"(wave), "s"(sStep), "s"(sX1), "s"(sX2), "s"(sU0), "s"(sWw), "s"(sBuf), "v"(vSrc), "v"(vA), "v"(vOut), "v"(vFlag), "v"(vPollOff), "v"(vSnapRd), "v"(vX), "v"(vAuxS), "v"(vStW), "v"(vStR), "v"(vWd0), "v"(vWd1), "v"(vWr0), "v"(vWr1), "v"(vWr2), "s"(w3) : "v0", "v1", "v2", "v3", "v4", "v5", "v6", "v7", "v8", "v9", "v10", "v11", "v12", "v13", "v14", "v15", "v16", "v17", "v18", "v19", "v20", "v21", "v22", "v23", "v24", "v25", "v26", "v27", "v28", "v29", "v30", "v31", "v32", "v33", "v34", "v35", "v36", "v37", "v38", "v39", "v40", "v41", "v42", "v43", "v44", "v45", "v46", "v47", "v48", "v49", "v50", "v51", "v52", "v53", "v54", "v55", "v56", "v57", "v58", "v59", "v60", "v61", "v62", "v63", "v64", "v65", "v66", "v67", "v68", "v69", "v70", "v71", "v72", "v73", "v74", "v75", "v76", "v77", "v78", "v79", "v80", "v81", "v82", "v83", "v84", "v85", "v86", "v87", "v88", "v89", "v90", "v91", "v92", "v93", "v94", "v95", "v96", "v97", "v98", "v99", "v100", "v101", "v102", "v103", "v104", "v105", "v106", "v107", "v108", "v109", "v110", "v111", "v112", "v113", "v114", "v115", "v116", "v117", "v118", "v119", "v120", "v121", "v122", "v123", "v124", "v125", "v126", "v127", "v128", "v129", "v130", "v131", "v132", "v133", "v134", "v135", "v136", "v137", "v138", "v139", "v140", "v141", "v142", "v143", "v144", "v145", "v146", "v147", "v148", "v149", "v150", "v151", "v152", "v153", "v154", "v155", "v156", "v157", "v158", "v159", "v160", "v161", "v162", "v163", "v164", "v165", "v166", "v167", "v168", "v169", "v170", "v171", "v172", "v173", "v174", "v175", "v176", "v177", "v178", "v179", "v180", "v181", "v182", "v183", "v184", "v185", "v186", "v187", "v188", "v189", "v190", "v191", "v192", "v193", "v194", "v195", "v196", "v197", "v198", "v199", "v200", "v201", "v202", "v203", "v204", "v205", "v206", "v207", "v208", "v209", "v210", "v211", "v212", "v213", "v214", "v215", "v216", "v217", "v218", "v219", "v220", "v221", "v222", "v223", "v224", "v225", "v226", "v227", "v228", "v229", "v230", "v231", "v255", "a0", "a1", "a2", "a3", "a4", "a5", "a6", "a7", "a8", "a9", "a10", "a11", "a12", "a13", "a14", "a15", "a16", "a17", "a18", "a19", "a20", "a21", "a22", "a23", "a24", "a25", "a26", "a27", "a28", "a29", "a30", "a31", "a32", "a33", "a34", "a35", "a36", "a37", "a38", "a39", "a40", "a41", "a42", "a43", "a44", "a45", "a46", "a47", "a48", "a49", "a50", "a51", "a52", "a53", "a54", "a55", "a56", "a57", "a58", "a59", "a60", "a61", "a62", "a63", "a64", "a65", "a66", "a67", "a68", "a69", "a70", "a71", "a72", "a73", "a74", "a75", "a76", "a77", "a78", "a79", "a80", "a81", "a82", "a83", "a84", "a85", "a86", "a87", "a88", "a89", "a90", "a91", "a92", "a93", "a94", "a95", "a96", "a97", "a98", "a99", "a100", "a101", "a102", "a103", "a104", "a105", "a106", "a107", "a108", "a109", "a110", "a111", "a112", "a113", "a114", "a115", "a116", "a117", "a118", "a119", "a120", "a121", "a122", "a123", "a124", "a125", "a126", "a127", "a128", "a129", "a130", "a131", "a132", "a133", "a134", "a135", "a136", "a137", "a138", "a139", "a140", "a141", "a142", "a143", "a144", "a145", "a146", "a147", "a148", "a149", "a150", "a151", "a152", "a153", "a154", "a155", "a156", "a157", "a158", "a159", "a160", "a161", "a162", "a163", "a164", "a165", "a166", "a167", "a168", "a169", "a170", "a171", "a172", "a173", "a174", "a175", "a176", "a177", "a178", "a179", "a180", "a181", "a182", "a183", "a184", "a185", "a186", "a187", "a188", "a189", "a190", "a191", "a192", "a193", "a194", "a195", "a196", "a197", "a198", "a199", "a200", "a201", "a202", "a203", "a204", "a205", "a206", "a207", "a208", "a209", "a210", "a211", "a212", "a213", "a214", "a215", "a216", "a217", "a218", "a219", "a220", "a221", "a222", "a223", "a224", "a225", "a226", "a227", "a228", "a229", "a230", "a231", "a232", "a233", "a234", "a235", "a236", "a237", "a238", "a239", "a240", "a241", "a242", "a243", "a244", "a245", "a246", "a247", "a248", "a249", "a250", "a251", "a252", "a253", "a254", "a255", "s36", "s37", "s38", "s39", "s40", "s41", "s42", "s43", "s44", "s45", "s46", "s47", "s48", "s49", "s50", "s51", "s52", "s53", "s54", "s55", "s56", "s57", "s58", "s59", "s60", "s61", "s62", "s63", "s64", "s65", "s66", "s67", "s68", "s69", "s70", "s71", "s72", "s73", "s74", "s75", "s76", "s77", "s78", "s79", "s80", "s81", "s82", "s83", "s84", "s85", "s86", "s87", "s88", "s89", "s90", "s91", "s92", "s93", "s94", "s95", "s96", "s97", "s98", "s99", "vcc", "scc", "memory"); }
    __builtin_unreachable();
  } else if (kind == 4) {
    COMMON(10, OFF_FG + kh * 9 * 128, OFF_FP + (kh * 8 + slice) * 128, 159744, 256, 2048, 5);
    const unsigned vSrc = OFF_G + kh * 57344 + wave * 7168 + vA, vOut = (kh ? OFF_P1 : OFF_P0) + (slice * 4 + wave) * 1024 + vA;
    const unsigned vAuxS = (kh ? OFF_XP : OFF_X1) + (slice * 4 + wave) * 1024 + vA;
    const unsigned sWw = (kh * 1792 + 16 * wave) * 2048 + slice * 256;
    asm volatile(R"(
Lcode_g4_b:
s_mov_b64 s[36:37],%0
s_mov_b32 s38,111193216
s_mov_b32 s39,0x00020000
s_mov_b32 s54,%1
s_mov_b32 s55,%2
s_mov_b32 s56,%3
s_mov_b32 s58,%4
s_mov_b32 s83,%5
s_mov_b32 s82,%6
s_mov_b32 s84,%7
v_mov_b32 v0,%8
v_mov_b32 v1,%9
v_mov_b32 v3,%10
v_mov_b32 v4,%11
v_mov_b32 v6,%12
v_mov_b32 v7,%13
v_mov_b32 v8,%14
v_mov_b32 v9,%15
v_mov_b32 v12,%16
v_mov_b32 v13,%17
v_mov_b32 v14,%18
v_mov_b32 v15,%19
v_mov_b32 v16,%20
s_mov_b64 s[40:41],%21
s_mov_b32 s42,7340032
s_mov_b32 s43,0x00020000
s_lshl_b32 s88,s54,11
s_mul_i32 s68,s54,11264
s_add_u32 s68,s68,114688
v_add_u32 v10,s68,v1
s_mov_b32 s69,111193088
buffer_load_dword v5,off,s[36:39],s69 sc1
s_add_u32 m0,s88,0
s_add_u32 s61,s82,0
buffer_load_dwordx4 v12,s[40:43],s61 offen offset:0  lds
s_add_u32 m0,s88,1024
s_nop 0
buffer_load_dwordx4 v13,s[40:43],s61 offen offset:0  lds
s_add_u32 m0,s88,8064
s_nop 0
buffer_load_dwordx4 v12,s[40:43],s61 offen offset:128  lds
s_add_u32 m0,s88,9088
s_nop 0
buffer_load_dwordx4 v13,s[40:43],s61 offen offset:128  lds
s_add_u32 m0,s88,16384
s_add_u32 s61,s82,131072
buffer_load_dwordx4 v12,s[40:43],s61 offen offset:0  lds
s_add_u32 m0,s88,17408
s_nop 0
buffer_load_dwordx4 v13,s[40:43],s61 offen offset:0  lds
s_add_u32 m0,s88,24448
s_nop 0
buffer_load_dwordx4 v12,s[40:43],s61 offen offset:128  lds
s_add_u32 m0,s88,25472
s_nop 0
buffer_load_dwordx4 v13,s[40:43],s61 offen offset:128  lds
s_add_u32 m0,s88,32768
s_add_u32 s61,s82,262144
buffer_load_dwordx4 v12,s[40:43],s61 offen offset:0  lds
s_add_u32 m0,s88,33792
s_nop 0
buffer_load_dwordx4 v13,s[40:43],s61 offen offset:0  lds
s_add_u32 m0,s88,40832
s_nop 0
buffer_load_dwordx4 v12,s[40:43],s61 offen offset:128  lds
s_add_u32 m0,s88,41856
s_nop 0
buffer_load_dwordx4 v13,s[40:43],s61 offen offset:128  lds
s_add_u32 m0,s88,49152
s_add_u32 s61,s82,393216
buffer_load_dwordx4 v12,s[40:43],s61 offen offset:0  lds
s_add_u32 m0,s88,50176
s_nop 0
buffer_load_dwordx4 v13,s[40:43],s61 offen offset:0  lds
s_add_u32 m0,s88,57216
s_nop 0
buffer_load_dwordx4 v12,s[40:43],s61 offen offset:128  lds
s_add_u32 m0,s88,58240
s_nop 0
buffer_load_dwordx4 v13,s[40:43],s61 offen offset:128  lds
s_add_u32 m0,s88,65536
s_add_u32 s61,s82,524288
buffer_load_dwordx4 v12,s[40:43],s61 offen offset:0  lds
s_add_u32 m0,s88,66560
s_nop 0
buffer_load_dwordx4 v13,s[40:43],s61 offen offset:0  lds
s_add_u32 m0,s88,73600
s_nop 0
buffer_load_dwordx4 v12,s[40:43],s61 offen offset:128  lds
s_add_u32 m0,s88,74624
s_nop 0
buffer_load_dwordx4 v13,s[40:43],s61 offen offset:128  lds
v_add_u32 v20,49152,v14
v_add_u32 v21,49152,v15
v_add_u32 v22,98304,v14
v_add_u32 v23,98304,v15
s_waitcnt vmcnt(16)
s_barrier
s_add_u32 m0,s88,81920
s_add_u32 s61,s82,655360
buffer_load_dwordx4 v12,s[40:43],s61 offen offset:0  lds
s_add_u32 m0,s88,82944
ds_read_b64_tr_b16 a[0:1],v14 offset:0
buffer_load_dwordx4 v13,s[40:43],s61 offen offset:0  lds
s_add_u32 m0,s88,89984
ds_read_b64_tr_b16 a[2:3],v14 offset:512
buffer_load_dwordx4 v12,s[40:43],s61 offen offset:128  lds
s_add_u32 m0,s88,91008
ds_read_b64_tr_b16 a[4:5],v15 offset:0
buffer_load_dwordx4 v13,s[40:43],s61 offen offset:128  lds
ds_read_b64_tr_b16 a[6:7],v15 offset:512
ds_read_b64_tr_b16 a[8:9],v14 offset:4096
ds_read_b64_tr_b16 a[10:11],v14 offset:4608
ds_read_b64_tr_b16 a[12:13],v15 offset:4096
ds_read_b64_tr_b16 a[14:15],v15 offset:4608
s_waitcnt vmcnt(16)
s_barrier
s_add_u32 m0,s88,98304
s_add_u32 s61,s82,786432
buffer_load_dwordx4 v12,s[40:43],s61 offen offset:0  lds
s_add_u32 m0,s88,99328
ds_read_b64_tr_b16 a[16:17],v14 offset:16384
buffer_load_dwordx4 v13,s[40:43],s61 offen offset:0  lds
s_add_u32 m0,s88,106368
ds_read_b64_tr_b16 a[18:19],v14 offset:16896
buffer_load_dwordx4 v12,s[40:43],s61 offen offset:128  lds
s_add_u32 m0,s88,107392
ds_read_b64_tr_b16 a[20:21],v15 offset:16384
buffer_load_dwordx4 v13,s[40:43],s61 offen offset:128  lds
ds_read_b64_tr_b16 a[22:23],v15 offset:16896
ds_read_b64_tr_b16 a[24:25],v14 offset:20480
ds_read_b64_tr_b16 a[26:27],v14 offset:20992
ds_read_b64_tr_b16 a[28:29],v15 offset:20480
ds_read_b64_tr_b16 a[30:31],v15 offset:20992
s_waitcnt lgkmcnt(8)
s_waitcnt vmcnt(16)
s_barrier
s_add_u32 m0,s88,0
s_add_u32 s61,s82,917504
buffer_load_dwordx4 v12,s[40:43],s61 offen offset:0  lds
s_add_u32 m0,s88,1024
ds_read_b64_tr_b16 a[32:33],v14 offset:32768
buffer_load_dwordx4 v13,s[40:43],s61 offen offset:0  lds
s_add_u32 m0,s88,8064
ds_read_b64_tr_b16 a[34:35],v14 offset:33280
buffer_load_dwordx4 v12,s[40:43],s61 offen offset:128  lds
s_add_u32 m0,s88,9088
ds_read_b64_tr_b16 a[36:37],v15 offset:32768
buffer_load_dwordx4 v13,s[40:43],s61 offen offset:128  lds
ds_read_b64_tr_b16 a[38:39],v15 offset:33280
ds_read_b64_tr_b16 a[40:41],v14 offset:36864
ds_read_b64_tr_b16 a[42:43],v14 offset:37376
ds_read_b64_tr_b16 a[44:45],v15 offset:36864
ds_read_b64_tr_b16 a[46:47],v15 offset:37376
s_waitcnt lgkmcnt(8)
s_waitcnt vmcnt(16)
s_barrier
s_add_u32 m0,s88,16384
s_add_u32 s61,s82,1048576
buffer_load_dwordx4 v12,s[40:43],s61 offen offset:0  lds
s_add_u32 m0,s88,17408
ds_read_b64_tr_b16 a[48:49],v20 offset:0
buffer_load_dwordx4 v13,s[40:43],s61 offen offset:0  lds
s_add_u32 m0,s88,24448
ds_read_b64_tr_b16 a[50:51],v20 offset:512
buffer_load_dwordx4 v12,s[40:43],s61 offen offset:128  lds
s_add_u32 m0,s88,25472
ds_read_b64_tr_b16 a[52:53],v21 offset:0
buffer_load_dwordx4 v13,s[40:43],s61 offen offset:128  lds
ds_read_b64_tr_b16 a[54:55],v21 offset:512
ds_read_b64_tr_b16 a[56:57],v20 offset:4096
ds_read_b64_tr_b16 a[58:59],v20 offset:4608
ds_read_b64_tr_b16 a[60:61],v21 offset:4096
ds_read_b64_tr_b16 a[62:63],v21 offset:4608
s_waitcnt lgkmcnt(8)
s_waitcnt vmcnt(16)
s_barrier
s_add_u32 m0,s88,32768
s_add_u32 s61,s82,1179648
buffer_load_dwordx4 v12,s[40:43],s61 offen offset:0  lds
s_add_u32 m0,s88,33792
ds_read_b64_tr_b16 a[64:65],v20 offset:16384
buffer_load_dwordx4 v13,s[40:43],s61 offen offset:0  lds
s_add_u32 m0,s88,40832
ds_read_b64_tr_b16 a[66:67],v20 offset:16896
buffer_load_dwordx4 v12,s[40:43],s61 offen offset:128  lds
s_add_u32 m0,s88,41856
ds_read_b64_tr_b16 a[68:69],v21 offset:16384
buffer_load_dwordx4 v13,s[40:43],s61 offen offset:128  lds
ds_read_b64_tr_b16 a[70:71],v21 offset:16896
ds_read_b64_tr_b16 a[72:73],v20 offset:20480
ds_read_b64_tr_b16 a[74:75],v20 offset:20992
ds_read_b64_tr_b16 a[76:77],v21 offset:20480
ds_read_b64_tr_b16 a[78:79],v21 offset:20992
s_waitcnt lgkmcnt(8)
s_waitcnt vmcnt(16)
s_barrier
s_add_u32 m0,s88,49152
s_add_u32 s61,s82,1310720)"
R"(
buffer_load_dwordx4 v12,s[40:43],s61 offen offset:0  lds
s_add_u32 m0,s88,50176
ds_read_b64_tr_b16 a[80:81],v20 offset:32768
buffer_load_dwordx4 v13,s[40:43],s61 offen offset:0  lds
s_add_u32 m0,s88,57216
ds_read_b64_tr_b16 a[82:83],v20 offset:33280
buffer_load_dwordx4 v12,s[40:43],s61 offen offset:128  lds
s_add_u32 m0,s88,58240
ds_read_b64_tr_b16 a[84:85],v21 offset:32768
buffer_load_dwordx4 v13,s[40:43],s61 offen offset:128  lds
ds_read_b64_tr_b16 a[86:87],v21 offset:33280
ds_read_b64_tr_b16 a[88:89],v20 offset:36864
ds_read_b64_tr_b16 a[90:91],v20 offset:37376
ds_read_b64_tr_b16 a[92:93],v21 offset:36864
ds_read_b64_tr_b16 a[94:95],v21 offset:37376
s_waitcnt lgkmcnt(8)
s_waitcnt vmcnt(16)
s_barrier
s_add_u32 m0,s88,65536
s_add_u32 s61,s82,1441792
buffer_load_dwordx4 v12,s[40:43],s61 offen offset:0  lds
s_add_u32 m0,s88,66560
ds_read_b64_tr_b16 a[96:97],v22 offset:0
buffer_load_dwordx4 v13,s[40:43],s61 offen offset:0  lds
s_add_u32 m0,s88,73600
ds_read_b64_tr_b16 a[98:99],v22 offset:512
buffer_load_dwordx4 v12,s[40:43],s61 offen offset:128  lds
s_add_u32 m0,s88,74624
ds_read_b64_tr_b16 a[100:101],v23 offset:0
buffer_load_dwordx4 v13,s[40:43],s61 offen offset:128  lds
ds_read_b64_tr_b16 a[102:103],v23 offset:512
ds_read_b64_tr_b16 a[104:105],v22 offset:4096
ds_read_b64_tr_b16 a[106:107],v22 offset:4608
ds_read_b64_tr_b16 a[108:109],v23 offset:4096
ds_read_b64_tr_b16 a[110:111],v23 offset:4608
s_waitcnt lgkmcnt(8)
s_waitcnt vmcnt(16)
s_barrier
s_add_u32 m0,s88,81920
s_add_u32 s61,s82,1572864
buffer_load_dwordx4 v12,s[40:43],s61 offen offset:0  lds
s_add_u32 m0,s88,82944
ds_read_b64_tr_b16 a[112:113],v14 offset:0
buffer_load_dwordx4 v13,s[40:43],s61 offen offset:0  lds
s_add_u32 m0,s88,89984
ds_read_b64_tr_b16 a[114:115],v14 offset:512
buffer_load_dwordx4 v12,s[40:43],s61 offen offset:128  lds
s_add_u32 m0,s88,91008
ds_read_b64_tr_b16 a[116:117],v15 offset:0
buffer_load_dwordx4 v13,s[40:43],s61 offen offset:128  lds
ds_read_b64_tr_b16 a[118:119],v15 offset:512
ds_read_b64_tr_b16 a[120:121],v14 offset:4096
ds_read_b64_tr_b16 a[122:123],v14 offset:4608
ds_read_b64_tr_b16 a[124:125],v15 offset:4096
ds_read_b64_tr_b16 a[126:127],v15 offset:4608
s_waitcnt lgkmcnt(8)
s_waitcnt vmcnt(16)
s_barrier
s_add_u32 m0,s88,98304
s_add_u32 s61,s82,1703936
buffer_load_dwordx4 v12,s[40:43],s61 offen offset:0  lds
s_add_u32 m0,s88,99328
ds_read_b64_tr_b16 a[128:129],v14 offset:16384
buffer_load_dwordx4 v13,s[40:43],s61 offen offset:0  lds
s_add_u32 m0,s88,106368
ds_read_b64_tr_b16 a[130:131],v14 offset:16896
buffer_load_dwordx4 v12,s[40:43],s61 offen offset:128  lds
s_add_u32 m0,s88,107392
ds_read_b64_tr_b16 a[132:133],v15 offset:16384
buffer_load_dwordx4 v13,s[40:43],s61 offen offset:128  lds
ds_read_b64_tr_b16 a[134:135],v15 offset:16896
ds_read_b64_tr_b16 a[136:137],v14 offset:20480
ds_read_b64_tr_b16 a[138:139],v14 offset:20992
ds_read_b64_tr_b16 a[140:141],v15 offset:20480
ds_read_b64_tr_b16 a[142:143],v15 offset:20992
s_waitcnt lgkmcnt(8)
s_waitcnt vmcnt(16)
s_barrier
s_add_u32 m0,s88,0
s_add_u32 s61,s82,1835008
buffer_load_dwordx4 v12,s[40:43],s61 offen offset:0  lds
s_add_u32 m0,s88,1024
ds_read_b64_tr_b16 a[144:145],v14 offset:32768
buffer_load_dwordx4 v13,s[40:43],s61 offen offset:0  lds
s_add_u32 m0,s88,8064
ds_read_b64_tr_b16 a[146:147],v14 offset:33280
buffer_load_dwordx4 v12,s[40:43],s61 offen offset:128  lds
s_add_u32 m0,s88,9088
ds_read_b64_tr_b16 a[148:149],v15 offset:32768
buffer_load_dwordx4 v13,s[40:43],s61 offen offset:128  lds
ds_read_b64_tr_b16 a[150:151],v15 offset:33280
ds_read_b64_tr_b16 a[152:153],v14 offset:36864
ds_read_b64_tr_b16 a[154:155],v14 offset:37376
ds_read_b64_tr_b16 a[156:157],v15 offset:36864
ds_read_b64_tr_b16 a[158:159],v15 offset:37376
s_waitcnt lgkmcnt(8)
s_waitcnt vmcnt(16)
s_barrier
s_add_u32 m0,s88,16384
s_add_u32 s61,s82,1966080
buffer_load_dwordx4 v12,s[40:43],s61 offen offset:0  lds
s_add_u32 m0,s88,17408
ds_read_b64_tr_b16 a[160:161],v20 offset:0
buffer_load_dwordx4 v13,s[40:43],s61 offen offset:0  lds
s_add_u32 m0,s88,24448
ds_read_b64_tr_b16 a[162:163],v20 offset:512
buffer_load_dwordx4 v12,s[40:43],s61 offen offset:128  lds
s_add_u32 m0,s88,25472
ds_read_b64_tr_b16 a[164:165],v21 offset:0
buffer_load_dwordx4 v13,s[40:43],s61 offen offset:128  lds
ds_read_b64_tr_b16 a[166:167],v21 offset:512
ds_read_b64_tr_b16 a[168:169],v20 offset:4096
ds_read_b64_tr_b16 a[170:171],v20 offset:4608
ds_read_b64_tr_b16 a[172:173],v21 offset:4096
ds_read_b64_tr_b16 a[174:175],v21 offset:4608
s_waitcnt lgkmcnt(8)
s_waitcnt vmcnt(16)
s_barrier
s_add_u32 m0,s88,32768
s_add_u32 s61,s82,2097152
buffer_load_dwordx4 v12,s[40:43],s61 offen offset:0  lds
s_add_u32 m0,s88,33792
ds_read_b64_tr_b16 a[176:177],v20 offset:16384
buffer_load_dwordx4 v13,s[40:43],s61 offen offset:0  lds
s_add_u32 m0,s88,40832
ds_read_b64_tr_b16 a[178:179],v20 offset:16896
buffer_load_dwordx4 v12,s[40:43],s61 offen offset:128  lds
s_add_u32 m0,s88,41856
ds_read_b64_tr_b16 a[180:181],v21 offset:16384
buffer_load_dwordx4 v13,s[40:43],s61 offen offset:128  lds
ds_read_b64_tr_b16 a[182:183],v21 offset:16896
ds_read_b64_tr_b16 a[184:185],v20 offset:20480
ds_read_b64_tr_b16 a[186:187],v20 offset:20992
ds_read_b64_tr_b16 a[188:189],v21 offset:20480
ds_read_b64_tr_b16 a[190:191],v21 offset:20992
s_waitcnt lgkmcnt(8)
s_waitcnt vmcnt(16)
s_barrier
s_add_u32 m0,s88,49152
s_add_u32 s61,s82,2228224
buffer_load_dwordx4 v12,s[40:43],s61 offen offset:0  lds
s_add_u32 m0,s88,50176
ds_read_b64_tr_b16 a[192:193],v20 offset:32768
buffer_load_dwordx4 v13,s[40:43],s61 offen offset:0  lds
s_add_u32 m0,s88,57216
ds_read_b64_tr_b16 a[194:195],v20 offset:33280
buffer_load_dwordx4 v12,s[40:43],s61 offen offset:128  lds
s_add_u32 m0,s88,58240
ds_read_b64_tr_b16 a[196:197],v21 offset:32768
buffer_load_dwordx4 v13,s[40:43],s61 offen offset:128  lds
ds_read_b64_tr_b16 a[198:199],v21 offset:33280
ds_read_b64_tr_b16 a[200:201],v20 offset:36864
ds_read_b64_tr_b16 a[202:203],v20 offset:37376
ds_read_b64_tr_b16 a[204:205],v21 offset:36864
ds_read_b64_tr_b16 a[206:207],v21 offset:37376
s_waitcnt lgkmcnt(8)
s_waitcnt vmcnt(16)
s_barrier
s_add_u32 m0,s88,65536
s_add_u32 s61,s82,2359296
buffer_load_dwordx4 v12,s[40:43],s61 offen offset:0  lds
s_add_u32 m0,s88,66560
ds_read_b64_tr_b16 a[208:209],v22 offset:0
buffer_load_dwordx4 v13,s[40:43],s61 offen offset:0  lds
s_add_u32 m0,s88,73600
ds_read_b64_tr_b16 a[210:211],v22 offset:512
buffer_load_dwordx4 v12,s[40:43],s61 offen offset:128  lds
s_add_u32 m0,s88,74624
ds_read_b64_tr_b16 a[212:213],v23 offset:0
buffer_load_dwordx4 v13,s[40:43],s61 offen offset:128  lds
ds_read_b64_tr_b16 a[214:215],v23 offset:512
ds_read_b64_tr_b16 a[216:217],v22 offset:4096
ds_read_b64_tr_b16 a[218:219],v22 offset:4608
ds_read_b64_tr_b16 a[220:221],v23 offset:4096
ds_read_b64_tr_b16 a[222:223],v23 offset:4608
s_waitcnt lgkmcnt(8)
s_waitcnt vmcnt(16)
s_barrier
s_add_u32 m0,s88,81920
s_add_u32 s61,s82,2490368
buffer_load_dwordx4 v12,s[40:43],s61 offen offset:0  lds
s_add_u32 m0,s88,82944
ds_read_b64_tr_b16 a[224:225],v14 offset:0
buffer_load_dwordx4 v13,s[40:43],s61 offen offset:0  lds
s_add_u32 m0,s88,89984
ds_read_b64_tr_b16 a[226:227],v14 offset:512
buffer_load_dwordx4 v12,s[40:43],s61 offen offset:128  lds
s_add_u32 m0,s88,91008
ds_read_b64_tr_b16 a[228:229],v15 offset:0
buffer_load_dwordx4 v13,s[40:43],s61 offen offset:128  lds
ds_read_b64_tr_b16 a[230:231],v15 offset:512
ds_read_b64_tr_b16 a[232:233],v14 offset:4096
ds_read_b64_tr_b16 a[234:235],v14 offset:4608
ds_read_b64_tr_b16 a[236:237],v15 offset:4096
ds_read_b64_tr_b16 a[238:239],v15 offset:4608
s_waitcnt lgkmcnt(8)
s_waitcnt vmcnt(16)
s_barrier
s_add_u32 m0,s88,98304
s_add_u32 s61,s82,2621440)"
R"(
buffer_load_dwordx4 v12,s[40:43],s61 offen offset:0  lds
s_add_u32 m0,s88,99328
ds_read_b64_tr_b16 a[240:241],v14 offset:16384
buffer_load_dwordx4 v13,s[40:43],s61 offen offset:0  lds
s_add_u32 m0,s88,106368
ds_read_b64_tr_b16 a[242:243],v14 offset:16896
buffer_load_dwordx4 v12,s[40:43],s61 offen offset:128  lds
s_add_u32 m0,s88,107392
ds_read_b64_tr_b16 a[244:245],v15 offset:16384
buffer_load_dwordx4 v13,s[40:43],s61 offen offset:128  lds
ds_read_b64_tr_b16 a[246:247],v15 offset:16896
ds_read_b64_tr_b16 a[248:249],v14 offset:20480
ds_read_b64_tr_b16 a[250:251],v14 offset:20992
ds_read_b64_tr_b16 a[252:253],v15 offset:20480
ds_read_b64_tr_b16 a[254:255],v15 offset:20992
s_waitcnt lgkmcnt(8)
s_waitcnt vmcnt(16)
s_barrier
s_add_u32 m0,s88,0
s_add_u32 s61,s82,2752512
buffer_load_dwordx4 v12,s[40:43],s61 offen offset:0  lds
s_add_u32 m0,s88,1024
ds_read_b64_tr_b16 v[108:109],v14 offset:32768
buffer_load_dwordx4 v13,s[40:43],s61 offen offset:0  lds
s_add_u32 m0,s88,8064
ds_read_b64_tr_b16 v[110:111],v14 offset:33280
buffer_load_dwordx4 v12,s[40:43],s61 offen offset:128  lds
s_add_u32 m0,s88,9088
ds_read_b64_tr_b16 v[112:113],v15 offset:32768
buffer_load_dwordx4 v13,s[40:43],s61 offen offset:128  lds
ds_read_b64_tr_b16 v[114:115],v15 offset:33280
ds_read_b64_tr_b16 v[116:117],v14 offset:36864
ds_read_b64_tr_b16 v[118:119],v14 offset:37376
ds_read_b64_tr_b16 v[120:121],v15 offset:36864
ds_read_b64_tr_b16 v[122:123],v15 offset:37376
s_waitcnt lgkmcnt(8)
s_waitcnt vmcnt(16)
s_barrier
s_add_u32 m0,s88,16384
s_add_u32 s61,s82,2883584
buffer_load_dwordx4 v12,s[40:43],s61 offen offset:0  lds
s_add_u32 m0,s88,17408
ds_read_b64_tr_b16 v[124:125],v20 offset:0
buffer_load_dwordx4 v13,s[40:43],s61 offen offset:0  lds
s_add_u32 m0,s88,24448
ds_read_b64_tr_b16 v[126:127],v20 offset:512
buffer_load_dwordx4 v12,s[40:43],s61 offen offset:128  lds
s_add_u32 m0,s88,25472
ds_read_b64_tr_b16 v[128:129],v21 offset:0
buffer_load_dwordx4 v13,s[40:43],s61 offen offset:128  lds
ds_read_b64_tr_b16 v[130:131],v21 offset:512
ds_read_b64_tr_b16 v[132:133],v20 offset:4096
ds_read_b64_tr_b16 v[134:135],v20 offset:4608
ds_read_b64_tr_b16 v[136:137],v21 offset:4096
ds_read_b64_tr_b16 v[138:139],v21 offset:4608
s_waitcnt lgkmcnt(8)
s_waitcnt vmcnt(16)
s_barrier
s_add_u32 m0,s88,32768
s_add_u32 s61,s82,3014656
buffer_load_dwordx4 v12,s[40:43],s61 offen offset:0  lds
s_add_u32 m0,s88,33792
ds_read_b64_tr_b16 v[140:141],v20 offset:16384
buffer_load_dwordx4 v13,s[40:43],s61 offen offset:0  lds
s_add_u32 m0,s88,40832
ds_read_b64_tr_b16 v[142:143],v20 offset:16896
buffer_load_dwordx4 v12,s[40:43],s61 offen offset:128  lds
s_add_u32 m0,s88,41856
ds_read_b64_tr_b16 v[144:145],v21 offset:16384
buffer_load_dwordx4 v13,s[40:43],s61 offen offset:128  lds
ds_read_b64_tr_b16 v[146:147],v21 offset:16896
ds_read_b64_tr_b16 v[148:149],v20 offset:20480
ds_read_b64_tr_b16 v[150:151],v20 offset:20992
ds_read_b64_tr_b16 v[152:153],v21 offset:20480
ds_read_b64_tr_b16 v[154:155],v21 offset:20992
s_waitcnt lgkmcnt(8)
s_waitcnt vmcnt(16)
s_barrier
s_add_u32 m0,s88,49152
s_add_u32 s61,s82,3145728
buffer_load_dwordx4 v12,s[40:43],s61 offen offset:0  lds
s_add_u32 m0,s88,50176
ds_read_b64_tr_b16 v[156:157],v20 offset:32768
buffer_load_dwordx4 v13,s[40:43],s61 offen offset:0  lds
s_add_u32 m0,s88,57216
ds_read_b64_tr_b16 v[158:159],v20 offset:33280
buffer_load_dwordx4 v12,s[40:43],s61 offen offset:128  lds
s_add_u32 m0,s88,58240
ds_read_b64_tr_b16 v[160:161],v21 offset:32768
buffer_load_dwordx4 v13,s[40:43],s61 offen offset:128  lds
ds_read_b64_tr_b16 v[162:163],v21 offset:33280
ds_read_b64_tr_b16 v[164:165],v20 offset:36864
ds_read_b64_tr_b16 v[166:167],v20 offset:37376
ds_read_b64_tr_b16 v[168:169],v21 offset:36864
ds_read_b64_tr_b16 v[170:171],v21 offset:37376
s_waitcnt lgkmcnt(8)
s_waitcnt vmcnt(16)
s_barrier
s_add_u32 m0,s88,65536
s_add_u32 s61,s82,3276800
buffer_load_dwordx4 v12,s[40:43],s61 offen offset:0  lds
s_add_u32 m0,s88,66560
ds_read_b64_tr_b16 v[172:173],v22 offset:0
buffer_load_dwordx4 v13,s[40:43],s61 offen offset:0  lds
s_add_u32 m0,s88,73600
ds_read_b64_tr_b16 v[174:175],v22 offset:512
buffer_load_dwordx4 v12,s[40:43],s61 offen offset:128  lds
s_add_u32 m0,s88,74624
ds_read_b64_tr_b16 v[176:177],v23 offset:0
buffer_load_dwordx4 v13,s[40:43],s61 offen offset:128  lds
ds_read_b64_tr_b16 v[178:179],v23 offset:512
ds_read_b64_tr_b16 v[180:181],v22 offset:4096
ds_read_b64_tr_b16 v[182:183],v22 offset:4608
ds_read_b64_tr_b16 v[184:185],v23 offset:4096
ds_read_b64_tr_b16 v[186:187],v23 offset:4608
s_waitcnt lgkmcnt(8)
s_waitcnt vmcnt(16)
s_barrier
s_add_u32 m0,s88,81920
s_add_u32 s61,s82,3407872
buffer_load_dwordx4 v12,s[40:43],s61 offen offset:0  lds
s_add_u32 m0,s88,82944
ds_read_b64_tr_b16 v[188:189],v14 offset:0
buffer_load_dwordx4 v13,s[40:43],s61 offen offset:0  lds
s_add_u32 m0,s88,89984
ds_read_b64_tr_b16 v[190:191],v14 offset:512
buffer_load_dwordx4 v12,s[40:43],s61 offen offset:128  lds
s_add_u32 m0,s88,91008
ds_read_b64_tr_b16 v[192:193],v15 offset:0
buffer_load_dwordx4 v13,s[40:43],s61 offen offset:128  lds
ds_read_b64_tr_b16 v[194:195],v15 offset:512
ds_read_b64_tr_b16 v[196:197],v14 offset:4096
ds_read_b64_tr_b16 v[198:199],v14 offset:4608
ds_read_b64_tr_b16 v[200:201],v15 offset:4096
ds_read_b64_tr_b16 v[202:203],v15 offset:4608
s_waitcnt lgkmcnt(8)
s_waitcnt vmcnt(16)
s_barrier
s_add_u32 m0,s88,98304
s_add_u32 s61,s82,3538944
buffer_load_dwordx4 v12,s[40:43],s61 offen offset:0  lds
s_add_u32 m0,s88,99328
ds_read_b64_tr_b16 v[204:205],v14 offset:16384
buffer_load_dwordx4 v13,s[40:43],s61 offen offset:0  lds
s_add_u32 m0,s88,106368
ds_read_b64_tr_b16 v[206:207],v14 offset:16896
buffer_load_dwordx4 v12,s[40:43],s61 offen offset:128  lds
s_add_u32 m0,s88,107392
ds_read_b64_tr_b16 v[208:209],v15 offset:16384
buffer_load_dwordx4 v13,s[40:43],s61 offen offset:128  lds
ds_read_b64_tr_b16 v[210:211],v15 offset:16896
ds_read_b64_tr_b16 v[212:213],v14 offset:20480
ds_read_b64_tr_b16 v[214:215],v14 offset:20992
ds_read_b64_tr_b16 v[32:33],v15 offset:20480
ds_read_b64_tr_b16 v[34:35],v15 offset:20992
s_waitcnt lgkmcnt(8)
s_waitcnt vmcnt(16)
s_barrier
ds_read_b64_tr_b16 v[216:217],v14 offset:32768
ds_read_b64_tr_b16 v[218:219],v14 offset:33280
ds_read_b64_tr_b16 v[36:37],v15 offset:32768
ds_read_b64_tr_b16 v[38:39],v15 offset:33280
ds_read_b64_tr_b16 v[220:221],v14 offset:36864
ds_read_b64_tr_b16 v[222:223],v14 offset:37376
ds_read_b64_tr_b16 v[40:41],v15 offset:36864
ds_read_b64_tr_b16 v[42:43],v15 offset:37376
s_waitcnt lgkmcnt(8)
ds_write_b128 v10,v[32:35] offset:0
s_waitcnt vmcnt(12)
s_barrier
ds_read_b64_tr_b16 v[224:225],v20 offset:0
ds_read_b64_tr_b16 v[226:227],v20 offset:512
ds_read_b64_tr_b16 v[28:29],v21 offset:0
ds_read_b64_tr_b16 v[30:31],v21 offset:512
ds_read_b64_tr_b16 v[228:229],v20 offset:4096
ds_read_b64_tr_b16 v[230:231],v20 offset:4608
ds_read_b64_tr_b16 v[32:33],v21 offset:4096
ds_read_b64_tr_b16 v[34:35],v21 offset:4608
s_waitcnt lgkmcnt(8)
ds_write_b128 v10,v[36:39] offset:1024
ds_write_b128 v10,v[40:43] offset:2048
s_waitcnt vmcnt(8)
s_barrier
ds_read_b64_tr_b16 v[232:233],v20 offset:16384
ds_read_b64_tr_b16 v[234:235],v20 offset:16896
ds_read_b64_tr_b16 v[36:37],v21 offset:16384
ds_read_b64_tr_b16 v[38:39],v21 offset:16896
ds_read_b64_tr_b16 v[236:237],v20 offset:20480
ds_read_b64_tr_b16 v[238:239],v20 offset:20992
ds_read_b64_tr_b16 v[40:41],v21 offset:20480
ds_read_b64_tr_b16 v[42:43],v21 offset:20992
s_waitcnt lgkmcnt(8)
ds_write_b128 v10,v[28:31] offset:3072
ds_write_b128 v10,v[32:35] offset:4096
s_waitcnt vmcnt(4)
s_barrier
ds_read_b64_tr_b16 v[240:241],v20 offset:32768
ds_read_b64_tr_b16 v[242:243],v20 offset:33280
ds_read_b64_tr_b16 v[28:29],v21 offset:32768
ds_read_b64_tr_b16 v[30:31],v21 offset:33280)"
R"(
ds_read_b64_tr_b16 v[244:245],v20 offset:36864
ds_read_b64_tr_b16 v[246:247],v20 offset:37376
ds_read_b64_tr_b16 v[32:33],v21 offset:36864
ds_read_b64_tr_b16 v[34:35],v21 offset:37376
s_waitcnt lgkmcnt(8)
ds_write_b128 v10,v[36:39] offset:5120
ds_write_b128 v10,v[40:43] offset:6144
s_waitcnt vmcnt(0)
s_barrier
ds_read_b64_tr_b16 v[248:249],v22 offset:0
ds_read_b64_tr_b16 v[250:251],v22 offset:512
ds_read_b64_tr_b16 v[36:37],v23 offset:0
ds_read_b64_tr_b16 v[38:39],v23 offset:512
ds_read_b64_tr_b16 v[252:253],v22 offset:4096
ds_read_b64_tr_b16 v[254:255],v22 offset:4608
ds_read_b64_tr_b16 v[40:41],v23 offset:4096
ds_read_b64_tr_b16 v[42:43],v23 offset:4608
s_waitcnt lgkmcnt(8)
ds_write_b128 v10,v[28:31] offset:7168
ds_write_b128 v10,v[32:35] offset:8192
s_waitcnt lgkmcnt(0)
ds_write_b128 v10,v[36:39] offset:9216
ds_write_b128 v10,v[40:43] offset:10240
s_waitcnt lgkmcnt(0)
s_barrier
v_readfirstlane_b32 s60,v5
s_nop 3
s_add_u32 s60,s60,1
s_cmp_eq_u32 s60,0
s_cselect_b32 s60,1,s60
v_mov_b32 v5,s60
s_mov_b32 s59,536870912
s_add_u32 s57,s56,131072
s_mov_b32 s79,0
s_mov_b32 s75,0x3a800000
s_mov_b32 s76,0xc0135761
s_mov_b32 s77,0x3d372713
s_mov_b32 s44,s59
s_mov_b32 s45,s59
s_mov_b32 s46,s59
s_mov_b32 s47,s59
s_mov_b32 s48,s83
s_add_u32 s49,s48,s55
s_cmp_eq_u32 s49,s56
s_cselect_b32 s49,s57,s49
s_cmp_eq_u32 s49,s58
s_cselect_b32 s49,s59,s49
s_add_u32 s50,s49,s55
s_cmp_eq_u32 s50,s56
s_cselect_b32 s50,s57,s50
s_cmp_eq_u32 s50,s58
s_cselect_b32 s50,s59,s50
s_add_u32 s51,s50,s55
s_cmp_eq_u32 s51,s56
s_cselect_b32 s51,s57,s51
s_cmp_eq_u32 s51,s58
s_cselect_b32 s51,s59,s51
s_add_u32 s52,s51,s55
s_cmp_eq_u32 s52,s56
s_cselect_b32 s52,s57,s52
s_cmp_eq_u32 s52,s58
s_cselect_b32 s52,s59,s52
s_mul_i32 s88,s54,7168
v_add_u32 v2,57344,v1
s_lshl_b32 s78,s54,8
s_add_u32 s78,s78,159744
s_getpc_b64 s[90:91]
Lg4_pc_%=:
s_mov_b32 s68,Lg4_d1w0_%=-Lg4_pc_%=
s_cmp_eq_u32 s54,1
s_cselect_b32 s68,Lg4_d1w1_%=-Lg4_pc_%=,s68
s_cmp_eq_u32 s54,2
s_cselect_b32 s68,Lg4_d1w2_%=-Lg4_pc_%=,s68
s_cmp_eq_u32 s54,3
s_cselect_b32 s68,Lg4_d1w3_%=-Lg4_pc_%=,s68
s_add_u32 s92,s90,s68
s_addc_u32 s93,s91,0
s_mov_b32 s68,Lg4_d0w0_%=-Lg4_pc_%=
s_cmp_eq_u32 s54,1
s_cselect_b32 s68,Lg4_d0w1_%=-Lg4_pc_%=,s68
s_cmp_eq_u32 s54,2
s_cselect_b32 s68,Lg4_d0w2_%=-Lg4_pc_%=,s68
s_cmp_eq_u32 s54,3
s_cselect_b32 s68,Lg4_d0w3_%=-Lg4_pc_%=,s68
s_add_u32 s90,s90,s68
s_addc_u32 s91,s91,0
s_mov_b32 s66,s59
s_mov_b32 s65,0
s_mov_b32 s62,s63
s_branch Lg4_slow0_%=
Lg4_d0w0_%=:
v_mfma_f32_16x16x32_bf16 v[12:15],a[0:3],v[28:31],0
s_add_u32 m0,s88,57344
ds_read_b128 v[56:59],v1 offset:7168
v_lshlrev_b32 v60,16,v72
v_mfma_f32_16x16x32_bf16 v[16:19],a[4:7],v[28:31],0
buffer_load_dwordx4 v0,s[36:39],s61 offen offset:0 sc1 lds
v_mfma_f32_16x16x32_bf16 v[12:15],a[8:11],v[32:35],v[12:15]
s_max_u32 s73,s47,s65
ds_read_b128 v[28:31],v1 offset:8192
v_and_b32 v61,0xffff0000,v72
v_mfma_f32_16x16x32_bf16 v[16:19],a[12:15],v[32:35],v[16:19]
s_waitcnt lgkmcnt(5)
v_lshlrev_b32 v62,16,v73
v_and_b32 v63,0xffff0000,v73
v_mfma_f32_16x16x32_bf16 v[12:15],a[16:19],v[36:39],v[12:15]
s_mov_b32 s65,0
ds_read_b128 v[32:35],v1 offset:9216
v_lshlrev_b32 v64,16,v74
v_mfma_f32_16x16x32_bf16 v[16:19],a[20:23],v[36:39],v[16:19]
buffer_load_dwordx4 v0,s[36:39],s61 offen offset:1024 sc1 lds
v_mfma_f32_16x16x32_bf16 v[12:15],a[24:27],v[40:43],v[12:15]
ds_read_b128 v[36:39],v1 offset:10240
v_and_b32 v65,0xffff0000,v74
v_mfma_f32_16x16x32_bf16 v[16:19],a[28:31],v[40:43],v[16:19]
s_waitcnt lgkmcnt(5)
v_lshlrev_b32 v66,16,v75
v_and_b32 v67,0xffff0000,v75
v_mfma_f32_16x16x32_bf16 v[12:15],a[32:35],v[44:47],v[12:15]
ds_read_b128 v[40:43],v1 offset:11264
v_add_f32 v20,v20,v60
v_mfma_f32_16x16x32_bf16 v[16:19],a[36:39],v[44:47],v[16:19]
buffer_load_dwordx4 v0,s[36:39],s61 offen offset:2048 sc1 lds
v_mfma_f32_16x16x32_bf16 v[12:15],a[40:43],v[48:51],v[12:15]
ds_read_b128 v[44:47],v1 offset:12288
v_add_f32 v21,v21,v61
v_mfma_f32_16x16x32_bf16 v[16:19],a[44:47],v[48:51],v[16:19]
s_waitcnt lgkmcnt(5)
v_add_f32 v22,v22,v62
v_add_f32 v23,v23,v63
v_mfma_f32_16x16x32_bf16 v[12:15],a[48:51],v[52:55],v[12:15]
ds_read_b128 v[48:51],v1 offset:13312
v_add_f32 v24,v24,v64
v_mfma_f32_16x16x32_bf16 v[16:19],a[52:55],v[52:55],v[16:19]
buffer_load_dwordx4 v0,s[36:39],s61 offen offset:3072 sc1 lds
v_mfma_f32_16x16x32_bf16 v[12:15],a[56:59],v[56:59],v[12:15]
s_add_u32 s61,s61,4096
ds_read_b128 v[52:55],v1 offset:14336
v_add_f32 v25,v25,v65
v_mfma_f32_16x16x32_bf16 v[16:19],a[60:63],v[56:59],v[16:19]
s_waitcnt lgkmcnt(5)
v_add_f32 v26,v26,v66
v_add_f32 v27,v27,v67
v_mfma_f32_16x16x32_bf16 v[12:15],a[64:67],v[28:31],v[12:15]
s_add_u32 m0,s88,61440
v_cvt_pk_bf16_f32 v60,v20,v21
v_cvt_pk_bf16_f32 v61,v22,v23
v_cvt_pk_bf16_f32 v62,v24,v25
v_cvt_pk_bf16_f32 v63,v26,v27
v_mfma_f32_16x16x32_bf16 v[16:19],a[68:71],v[28:31],v[16:19]
s_add_u32 s53,s52,s55
ds_read_b128 v[56:59],v1 offset:15360
buffer_store_dwordx4 v[60:63],v3,s[36:39],s73 offen offset:0 sc0 sc1
s_cmp_eq_u32 s53,s56
v_mfma_f32_16x16x32_bf16 v[12:15],a[72:75],v[32:35],v[12:15]
s_cselect_b32 s53,s57,s53
ds_read_b128 v[28:31],v1 offset:16384
s_cmp_eq_u32 s53,s58
v_mfma_f32_16x16x32_bf16 v[16:19],a[76:79],v[32:35],v[16:19]
s_waitcnt lgkmcnt(5)
s_cselect_b32 s53,s59,s53
s_cmp_ge_u32 s66,s59
v_mfma_f32_16x16x32_bf16 v[12:15],a[80:83],v[36:39],v[12:15]
s_cselect_b32 s86,s49,s50
ds_read_b128 v[32:35],v1 offset:17408
s_mul_i32 s68,s86,5
v_mfma_f32_16x16x32_bf16 v[16:19],a[84:87],v[36:39],v[16:19]
buffer_load_dwordx4 v0,s[36:39],s61 offen offset:0 sc1 lds
v_mfma_f32_16x16x32_bf16 v[12:15],a[88:91],v[40:43],v[12:15]
s_lshr_b32 s68,s68,6
ds_read_b128 v[36:39],v1 offset:18432
v_mfma_f32_16x16x32_bf16 v[16:19],a[92:95],v[40:43],v[16:19]
s_waitcnt lgkmcnt(5)
v_mfma_f32_16x16x32_bf16 v[12:15],a[96:99],v[44:47],v[12:15]
ds_read_b128 v[40:43],v1 offset:19456
v_mfma_f32_16x16x32_bf16 v[16:19],a[100:103],v[44:47],v[16:19]
buffer_load_dwordx4 v0,s[36:39],s61 offen offset:1024 sc1 lds
v_mfma_f32_16x16x32_bf16 v[12:15],a[104:107],v[48:51],v[12:15]
ds_read_b128 v[44:47],v1 offset:20480
v_mfma_f32_16x16x32_bf16 v[16:19],a[108:111],v[48:51],v[16:19]
s_waitcnt lgkmcnt(5)
v_mfma_f32_16x16x32_bf16 v[12:15],a[112:115],v[52:55],v[12:15]
ds_read_b128 v[48:51],v1 offset:21504
v_mfma_f32_16x16x32_bf16 v[16:19],a[116:119],v[52:55],v[16:19]
buffer_load_dwordx4 v0,s[36:39],s61 offen offset:2048 sc1 lds
v_mfma_f32_16x16x32_bf16 v[12:15],a[120:123],v[56:59],v[12:15]
s_add_u32 m0,s78,3072
ds_read_b128 v[52:55],v1 offset:22528
s_add_u32 s61,s61,24576
v_mfma_f32_16x16x32_bf16 v[16:19],a[124:127],v[56:59],v[16:19]
s_waitcnt lgkmcnt(5)
v_mfma_f32_16x16x32_bf16 v[12:15],a[128:131],v[28:31],v[12:15]
ds_read_b128 v[56:59],v1 offset:23552
v_mfma_f32_16x16x32_bf16 v[16:19],a[132:135],v[28:31],v[16:19]
buffer_load_dwordx4 v[68:71],v9,s[36:39],s48 offen offset:0 sc1
v_mfma_f32_16x16x32_bf16 v[12:15],a[136:139],v[32:35],v[12:15]
ds_read_b128 v[28:31],v1 offset:24576
v_mfma_f32_16x16x32_bf16 v[16:19],a[140:143],v[32:35],v[16:19]
s_waitcnt lgkmcnt(5)
v_mfma_f32_16x16x32_bf16 v[12:15],a[144:147],v[36:39],v[12:15])"
R"(
ds_read_b128 v[32:35],v1 offset:25600
v_mfma_f32_16x16x32_bf16 v[16:19],a[148:151],v[36:39],v[16:19]
v_mfma_f32_16x16x32_bf16 v[12:15],a[152:155],v[40:43],v[12:15]
ds_read_b128 v[36:39],v1 offset:26624
v_mfma_f32_16x16x32_bf16 v[16:19],a[156:159],v[40:43],v[16:19]
s_waitcnt lgkmcnt(5)
v_mfma_f32_16x16x32_bf16 v[12:15],a[160:163],v[44:47],v[12:15]
s_waitcnt vmcnt(9)
ds_read_b128 v[40:43],v1 offset:27648
v_mfma_f32_16x16x32_bf16 v[16:19],a[164:167],v[44:47],v[16:19]
s_barrier
v_mfma_f32_16x16x32_bf16 v[12:15],a[168:171],v[48:51],v[12:15]
buffer_load_dword v6,s[36:39],s68 offen offset:0 sc1 lds
v_mfma_f32_16x16x32_bf16 v[16:19],a[172:175],v[48:51],v[16:19]
s_waitcnt lgkmcnt(4)
ds_read_b128 v[44:47],v1 offset:28672
s_add_u32 m0,s88,86016
v_mfma_f32_16x16x32_bf16 v[12:15],a[176:179],v[52:55],v[12:15]
ds_read_b128 v[48:51],v1 offset:29696
v_mfma_f32_16x16x32_bf16 v[16:19],a[180:183],v[52:55],v[16:19]
buffer_load_dwordx4 v0,s[36:39],s61 offen offset:0 sc1 lds
v_mfma_f32_16x16x32_bf16 v[12:15],a[184:187],v[56:59],v[12:15]
ds_read_b128 v[52:55],v1 offset:30720
v_mfma_f32_16x16x32_bf16 v[16:19],a[188:191],v[56:59],v[16:19]
s_waitcnt lgkmcnt(5)
v_mfma_f32_16x16x32_bf16 v[12:15],a[192:195],v[28:31],v[12:15]
ds_read_b128 v[56:59],v1 offset:31744
v_mfma_f32_16x16x32_bf16 v[16:19],a[196:199],v[28:31],v[16:19]
v_mfma_f32_16x16x32_bf16 v[12:15],a[200:203],v[32:35],v[12:15]
buffer_load_dwordx4 v0,s[36:39],s61 offen offset:1024 sc1 lds
v_mfma_f32_16x16x32_bf16 v[16:19],a[204:207],v[32:35],v[16:19]
s_waitcnt lgkmcnt(4)
ds_read_b128 v[28:31],v1 offset:32768
v_mfma_f32_16x16x32_bf16 v[12:15],a[208:211],v[36:39],v[12:15]
ds_read_b128 v[32:35],v1 offset:33792
v_mfma_f32_16x16x32_bf16 v[16:19],a[212:215],v[36:39],v[16:19]
v_mfma_f32_16x16x32_bf16 v[12:15],a[216:219],v[40:43],v[12:15]
ds_read_b128 v[36:39],v1 offset:34816
v_mfma_f32_16x16x32_bf16 v[16:19],a[220:223],v[40:43],v[16:19]
s_waitcnt lgkmcnt(5)
buffer_load_dwordx4 v0,s[36:39],s61 offen offset:2048 sc1 lds
v_mfma_f32_16x16x32_bf16 v[12:15],a[224:227],v[44:47],v[12:15]
ds_read_b128 v[40:43],v1 offset:35840
v_mfma_f32_16x16x32_bf16 v[16:19],a[228:231],v[44:47],v[16:19]
v_mfma_f32_16x16x32_bf16 v[12:15],a[232:235],v[48:51],v[12:15]
ds_read_b128 v[44:47],v1 offset:36864
v_mfma_f32_16x16x32_bf16 v[16:19],a[236:239],v[48:51],v[16:19]
s_waitcnt lgkmcnt(5)
v_mfma_f32_16x16x32_bf16 v[12:15],a[240:243],v[52:55],v[12:15]
buffer_load_dwordx4 v0,s[36:39],s61 offen offset:3072 sc1 lds
v_mfma_f32_16x16x32_bf16 v[16:19],a[244:247],v[52:55],v[16:19]
s_add_u32 s61,s61,4096
ds_read_b128 v[48:51],v1 offset:37888
s_add_u32 m0,s88,90112
v_mfma_f32_16x16x32_bf16 v[12:15],a[248:251],v[56:59],v[12:15]
ds_read_b128 v[52:55],v1 offset:38912
v_mfma_f32_16x16x32_bf16 v[16:19],a[252:255],v[56:59],v[16:19]
s_waitcnt lgkmcnt(5)
v_mfma_f32_16x16x32_bf16 v[12:15],v[108:111],v[28:31],v[12:15]
ds_read_b128 v[56:59],v1 offset:39936
v_mfma_f32_16x16x32_bf16 v[16:19],v[112:115],v[28:31],v[16:19]
buffer_load_dwordx4 v0,s[36:39],s61 offen offset:0 sc1 lds
v_mfma_f32_16x16x32_bf16 v[12:15],v[116:119],v[32:35],v[12:15]
ds_read_b128 v[28:31],v1 offset:40960
v_mfma_f32_16x16x32_bf16 v[16:19],v[120:123],v[32:35],v[16:19]
s_waitcnt lgkmcnt(5)
v_mfma_f32_16x16x32_bf16 v[12:15],v[124:127],v[36:39],v[12:15]
ds_read_b128 v[32:35],v1 offset:41984
v_mfma_f32_16x16x32_bf16 v[16:19],v[128:131],v[36:39],v[16:19]
v_mfma_f32_16x16x32_bf16 v[12:15],v[132:135],v[40:43],v[12:15]
buffer_load_dwordx4 v0,s[36:39],s61 offen offset:1024 sc1 lds
v_mfma_f32_16x16x32_bf16 v[16:19],v[136:139],v[40:43],v[16:19]
s_waitcnt lgkmcnt(4)
ds_read_b128 v[36:39],v1 offset:43008
v_mfma_f32_16x16x32_bf16 v[12:15],v[140:143],v[44:47],v[12:15]
ds_read_b128 v[40:43],v1 offset:44032
v_mfma_f32_16x16x32_bf16 v[16:19],v[144:147],v[44:47],v[16:19]
v_mfma_f32_16x16x32_bf16 v[12:15],v[148:151],v[48:51],v[12:15]
ds_read_b128 v[44:47],v1 offset:45056
v_mfma_f32_16x16x32_bf16 v[16:19],v[152:155],v[48:51],v[16:19]
s_waitcnt lgkmcnt(5)
buffer_load_dwordx4 v0,s[36:39],s61 offen offset:2048 sc1 lds
v_mfma_f32_16x16x32_bf16 v[12:15],v[156:159],v[52:55],v[12:15]
ds_read_b128 v[48:51],v1 offset:46080
v_mfma_f32_16x16x32_bf16 v[16:19],v[160:163],v[52:55],v[16:19]
v_mfma_f32_16x16x32_bf16 v[12:15],v[164:167],v[56:59],v[12:15]
ds_read_b128 v[52:55],v1 offset:47104
v_mfma_f32_16x16x32_bf16 v[16:19],v[168:171],v[56:59],v[16:19]
s_waitcnt lgkmcnt(5)
v_mfma_f32_16x16x32_bf16 v[12:15],v[172:175],v[28:31],v[12:15]
ds_read_b128 v[56:59],v1 offset:48128
v_mfma_f32_16x16x32_bf16 v[16:19],v[176:179],v[28:31],v[16:19]
ds_read_b128 v[88:91],v10 offset:0
v_mfma_f32_16x16x32_bf16 v[12:15],v[180:183],v[32:35],v[12:15]
ds_read_b128 v[28:31],v1 offset:49152
v_mfma_f32_16x16x32_bf16 v[16:19],v[184:187],v[32:35],v[16:19]
s_waitcnt lgkmcnt(6)
ds_read_b128 v[92:95],v10 offset:1024
v_mfma_f32_16x16x32_bf16 v[12:15],v[188:191],v[36:39],v[12:15]
ds_read_b128 v[32:35],v1 offset:50176
v_mfma_f32_16x16x32_bf16 v[16:19],v[192:195],v[36:39],v[16:19]
ds_read_b128 v[96:99],v10 offset:2048
v_mfma_f32_16x16x32_bf16 v[12:15],v[196:199],v[40:43],v[12:15]
ds_read_b128 v[36:39],v1 offset:51200
v_mfma_f32_16x16x32_bf16 v[16:19],v[200:203],v[40:43],v[16:19]
s_waitcnt lgkmcnt(5)
ds_read_b128 v[76:79],v10 offset:3072
v_mfma_f32_16x16x32_bf16 v[12:15],v[204:207],v[44:47],v[12:15]
ds_read_b128 v[40:43],v1 offset:52224
v_mfma_f32_16x16x32_bf16 v[16:19],v[208:211],v[44:47],v[16:19]
ds_read_b128 v[80:83],v10 offset:4096
v_mfma_f32_16x16x32_bf16 v[12:15],v[212:215],v[48:51],v[12:15]
ds_read_b128 v[44:47],v1 offset:53248
v_mfma_f32_16x16x32_bf16 v[16:19],v[88:91],v[48:51],v[16:19]
s_waitcnt lgkmcnt(5)
ds_read_b128 v[84:87],v10 offset:5120
v_mfma_f32_16x16x32_bf16 v[12:15],v[216:219],v[52:55],v[12:15]
ds_read_b128 v[48:51],v1 offset:54272
v_mfma_f32_16x16x32_bf16 v[16:19],v[92:95],v[52:55],v[16:19]
ds_read_b128 v[88:91],v10 offset:6144
v_mfma_f32_16x16x32_bf16 v[12:15],v[220:223],v[56:59],v[12:15]
ds_read_b128 v[52:55],v1 offset:55296
v_mfma_f32_16x16x32_bf16 v[16:19],v[96:99],v[56:59],v[16:19]
s_waitcnt lgkmcnt(5)
ds_read_b128 v[92:95],v10 offset:7168
v_mfma_f32_16x16x32_bf16 v[12:15],v[224:227],v[28:31],v[12:15]
s_waitcnt vmcnt(7)
ds_read_b128 v[56:59],v1 offset:56320
ds_read_b128 v[96:99],v10 offset:8192
v_mfma_f32_16x16x32_bf16 v[16:19],v[76:79],v[28:31],v[16:19]
s_barrier
v_mfma_f32_16x16x32_bf16 v[12:15],v[228:231],v[32:35],v[12:15]
ds_read_b128 v[28:31],v2 offset:0
ds_read_b32 v11,v8 offset:3072
v_mfma_f32_16x16x32_bf16 v[16:19],v[80:83],v[32:35],v[16:19]
s_waitcnt lgkmcnt(6)
ds_read_b128 v[76:79],v10 offset:9216
v_mfma_f32_16x16x32_bf16 v[12:15],v[232:235],v[36:39],v[12:15]
ds_read_b128 v[32:35],v2 offset:1024
v_mfma_f32_16x16x32_bf16 v[16:19],v[84:87],v[36:39],v[16:19]
ds_read_b128 v[80:83],v10 offset:10240
v_mfma_f32_16x16x32_bf16 v[12:15],v[236:239],v[40:43],v[12:15]
s_waitcnt lgkmcnt(3)
ds_read_b128 v[36:39],v2 offset:2048
v_mfma_f32_16x16x32_bf16 v[16:19],v[88:91],v[40:43],v[16:19]
v_cmp_eq_u32 vcc,s60,v11
v_mfma_f32_16x16x32_bf16 v[12:15],v[240:243],v[44:47],v[12:15]
s_cmp_eq_u32 vcc_lo,-1
ds_read_b128 v[40:43],v2 offset:3072
s_cselect_b32 s67,s86,s59
v_mfma_f32_16x16x32_bf16 v[16:19],v[92:95],v[44:47],v[16:19]
s_mul_i32 s61,s67,7
s_lshr_b32 s61,s61,1
s_cmp_ge_u32 s66,s59
v_mfma_f32_16x16x32_bf16 v[12:15],v[244:247],v[48:51],v[12:15]
ds_read_b128 v[44:47],v2 offset:4096
v_mfma_f32_16x16x32_bf16 v[16:19],v[96:99],v[48:51],v[16:19]
s_waitcnt lgkmcnt(3)
v_mfma_f32_16x16x32_bf16 v[12:15],v[248:251],v[52:55],v[12:15]
ds_read_b128 v[48:51],v2 offset:5120
v_mfma_f32_16x16x32_bf16 v[16:19],v[76:79],v[52:55],v[16:19]
v_mfma_f32_16x16x32_bf16 v[12:15],v[252:255],v[56:59],v[12:15]
ds_read_b128 v[52:55],v2 offset:6144
v_mfma_f32_16x16x32_bf16 v[16:19],v[80:83],v[56:59],v[16:19]
s_cbranch_scc1 Lg4_slow1_%=
Lg4_d1w0_%=:
v_mfma_f32_16x16x32_bf16 v[20:23],a[0:3],v[28:31],0
s_add_u32 m0,s88,0
ds_read_b128 v[56:59],v2 offset:7168
v_lshlrev_b32 v60,16,v68
v_mfma_f32_16x16x32_bf16 v[24:27],a[4:7],v[28:31],0
buffer_load_dwordx4 v0,s[36:39],s61 offen offset:0 sc1 lds
v_mfma_f32_16x16x32_bf16 v[20:23],a[8:11],v[32:35],v[20:23]
s_max_u32 s73,s48,s65
ds_read_b128 v[28:31],v2 offset:8192
v_and_b32 v61,0xffff0000,v68
v_mfma_f32_16x16x32_bf16 v[24:27],a[12:15],v[32:35],v[24:27]
s_waitcnt lgkmcnt(5)
v_lshlrev_b32 v62,16,v69
v_and_b32 v63,0xffff0000,v69
v_mfma_f32_16x16x32_bf16 v[20:23],a[16:19],v[36:39],v[20:23]
s_mov_b32 s65,0
ds_read_b128 v[32:35],v2 offset:9216
v_lshlrev_b32 v64,16,v70
v_mfma_f32_16x16x32_bf16 v[24:27],a[20:23],v[36:39],v[24:27]
buffer_load_dwordx4 v0,s[36:39],s61 offen offset:1024 sc1 lds
v_mfma_f32_16x16x32_bf16 v[20:23],a[24:27],v[40:43],v[20:23]
s_mov_b64 s[44:45],s[46:47]
ds_read_b128 v[36:39],v2 offset:10240
v_and_b32 v65,0xffff0000,v70
v_mfma_f32_16x16x32_bf16 v[24:27],a[28:31],v[40:43],v[24:27]
s_waitcnt lgkmcnt(5)
v_lshlrev_b32 v66,16,v71
v_and_b32 v67,0xffff0000,v71
v_mfma_f32_16x16x32_bf16 v[20:23],a[32:35],v[44:47],v[20:23]
s_mov_b64 s[46:47],s[48:49]
ds_read_b128 v[40:43],v2 offset:11264
v_add_f32 v12,v12,v60
v_mfma_f32_16x16x32_bf16 v[24:27],a[36:39],v[44:47],v[24:27]
buffer_load_dwordx4 v0,s[36:39],s61 offen offset:2048 sc1 lds)"
R"(
v_mfma_f32_16x16x32_bf16 v[20:23],a[40:43],v[48:51],v[20:23]
ds_read_b128 v[44:47],v2 offset:12288
v_add_f32 v13,v13,v61
v_mfma_f32_16x16x32_bf16 v[24:27],a[44:47],v[48:51],v[24:27]
s_waitcnt lgkmcnt(5)
v_add_f32 v14,v14,v62
v_add_f32 v15,v15,v63
v_mfma_f32_16x16x32_bf16 v[20:23],a[48:51],v[52:55],v[20:23]
ds_read_b128 v[48:51],v2 offset:13312
v_add_f32 v16,v16,v64
v_mfma_f32_16x16x32_bf16 v[24:27],a[52:55],v[52:55],v[24:27]
buffer_load_dwordx4 v0,s[36:39],s61 offen offset:3072 sc1 lds
v_mfma_f32_16x16x32_bf16 v[20:23],a[56:59],v[56:59],v[20:23]
s_add_u32 s61,s61,4096
ds_read_b128 v[52:55],v2 offset:14336
v_add_f32 v17,v17,v65
v_mfma_f32_16x16x32_bf16 v[24:27],a[60:63],v[56:59],v[24:27]
s_waitcnt lgkmcnt(5)
v_add_f32 v18,v18,v66
v_add_f32 v19,v19,v67
v_mfma_f32_16x16x32_bf16 v[20:23],a[64:67],v[28:31],v[20:23]
s_add_u32 m0,s88,4096
v_cvt_pk_bf16_f32 v60,v12,v13
v_cvt_pk_bf16_f32 v61,v14,v15
v_cvt_pk_bf16_f32 v62,v16,v17
v_cvt_pk_bf16_f32 v63,v18,v19
v_mfma_f32_16x16x32_bf16 v[24:27],a[68:71],v[28:31],v[24:27]
s_add_u32 s74,s53,s55
ds_read_b128 v[56:59],v2 offset:15360
buffer_store_dwordx4 v[60:63],v3,s[36:39],s73 offen offset:0 sc0 sc1
s_cmp_eq_u32 s74,s56
v_mfma_f32_16x16x32_bf16 v[20:23],a[72:75],v[32:35],v[20:23]
s_cselect_b32 s74,s57,s74
ds_read_b128 v[28:31],v2 offset:16384
s_cmp_eq_u32 s74,s58
v_mfma_f32_16x16x32_bf16 v[24:27],a[76:79],v[32:35],v[24:27]
s_waitcnt lgkmcnt(5)
s_cselect_b32 s74,s59,s74
s_cmp_ge_u32 s67,s59
v_mfma_f32_16x16x32_bf16 v[20:23],a[80:83],v[36:39],v[20:23]
s_cselect_b32 s86,s50,s51
ds_read_b128 v[32:35],v2 offset:17408
s_mul_i32 s68,s86,5
v_mfma_f32_16x16x32_bf16 v[24:27],a[84:87],v[36:39],v[24:27]
buffer_load_dwordx4 v0,s[36:39],s61 offen offset:0 sc1 lds
v_mfma_f32_16x16x32_bf16 v[20:23],a[88:91],v[40:43],v[20:23]
s_lshr_b32 s68,s68,6
ds_read_b128 v[36:39],v2 offset:18432
v_mfma_f32_16x16x32_bf16 v[24:27],a[92:95],v[40:43],v[24:27]
s_waitcnt lgkmcnt(5)
v_mfma_f32_16x16x32_bf16 v[20:23],a[96:99],v[44:47],v[20:23]
ds_read_b128 v[40:43],v2 offset:19456
v_mfma_f32_16x16x32_bf16 v[24:27],a[100:103],v[44:47],v[24:27]
buffer_load_dwordx4 v0,s[36:39],s61 offen offset:1024 sc1 lds
v_mfma_f32_16x16x32_bf16 v[20:23],a[104:107],v[48:51],v[20:23]
ds_read_b128 v[44:47],v2 offset:20480
v_mfma_f32_16x16x32_bf16 v[24:27],a[108:111],v[48:51],v[24:27]
s_waitcnt lgkmcnt(5)
v_mfma_f32_16x16x32_bf16 v[20:23],a[112:115],v[52:55],v[20:23]
ds_read_b128 v[48:51],v2 offset:21504
v_mfma_f32_16x16x32_bf16 v[24:27],a[116:119],v[52:55],v[24:27]
buffer_load_dwordx4 v0,s[36:39],s61 offen offset:2048 sc1 lds
v_mfma_f32_16x16x32_bf16 v[20:23],a[120:123],v[56:59],v[20:23]
s_add_u32 m0,s78,3072
ds_read_b128 v[52:55],v2 offset:22528
s_add_u32 s61,s61,24576
v_mfma_f32_16x16x32_bf16 v[24:27],a[124:127],v[56:59],v[24:27]
s_waitcnt lgkmcnt(5)
v_mfma_f32_16x16x32_bf16 v[20:23],a[128:131],v[28:31],v[20:23]
ds_read_b128 v[56:59],v2 offset:23552
v_mfma_f32_16x16x32_bf16 v[24:27],a[132:135],v[28:31],v[24:27]
buffer_load_dwordx4 v[72:75],v9,s[36:39],s49 offen offset:0 sc1
v_mfma_f32_16x16x32_bf16 v[20:23],a[136:139],v[32:35],v[20:23]
s_mov_b64 s[48:49],s[50:51]
ds_read_b128 v[28:31],v2 offset:24576
s_mov_b64 s[50:51],s[52:53]
v_mfma_f32_16x16x32_bf16 v[24:27],a[140:143],v[32:35],v[24:27]
s_waitcnt lgkmcnt(5)
s_mov_b32 s52,s74
v_mfma_f32_16x16x32_bf16 v[20:23],a[144:147],v[36:39],v[20:23]
ds_read_b128 v[32:35],v2 offset:25600
v_mfma_f32_16x16x32_bf16 v[24:27],a[148:151],v[36:39],v[24:27]
v_mfma_f32_16x16x32_bf16 v[20:23],a[152:155],v[40:43],v[20:23]
ds_read_b128 v[36:39],v2 offset:26624
v_mfma_f32_16x16x32_bf16 v[24:27],a[156:159],v[40:43],v[24:27]
s_waitcnt lgkmcnt(5)
v_mfma_f32_16x16x32_bf16 v[20:23],a[160:163],v[44:47],v[20:23]
s_waitcnt vmcnt(9)
ds_read_b128 v[40:43],v2 offset:27648
v_mfma_f32_16x16x32_bf16 v[24:27],a[164:167],v[44:47],v[24:27]
s_barrier
v_mfma_f32_16x16x32_bf16 v[20:23],a[168:171],v[48:51],v[20:23]
buffer_load_dword v6,s[36:39],s68 offen offset:0 sc1 lds
v_mfma_f32_16x16x32_bf16 v[24:27],a[172:175],v[48:51],v[24:27]
s_waitcnt lgkmcnt(4)
ds_read_b128 v[44:47],v2 offset:28672
s_add_u32 m0,s88,28672
v_mfma_f32_16x16x32_bf16 v[20:23],a[176:179],v[52:55],v[20:23]
ds_read_b128 v[48:51],v2 offset:29696
v_mfma_f32_16x16x32_bf16 v[24:27],a[180:183],v[52:55],v[24:27]
buffer_load_dwordx4 v0,s[36:39],s61 offen offset:0 sc1 lds
v_mfma_f32_16x16x32_bf16 v[20:23],a[184:187],v[56:59],v[20:23]
ds_read_b128 v[52:55],v2 offset:30720
v_mfma_f32_16x16x32_bf16 v[24:27],a[188:191],v[56:59],v[24:27]
s_waitcnt lgkmcnt(5)
v_mfma_f32_16x16x32_bf16 v[20:23],a[192:195],v[28:31],v[20:23]
ds_read_b128 v[56:59],v2 offset:31744
v_mfma_f32_16x16x32_bf16 v[24:27],a[196:199],v[28:31],v[24:27]
v_mfma_f32_16x16x32_bf16 v[20:23],a[200:203],v[32:35],v[20:23]
buffer_load_dwordx4 v0,s[36:39],s61 offen offset:1024 sc1 lds
v_mfma_f32_16x16x32_bf16 v[24:27],a[204:207],v[32:35],v[24:27]
s_waitcnt lgkmcnt(4)
ds_read_b128 v[28:31],v2 offset:32768
v_mfma_f32_16x16x32_bf16 v[20:23],a[208:211],v[36:39],v[20:23]
ds_read_b128 v[32:35],v2 offset:33792
v_mfma_f32_16x16x32_bf16 v[24:27],a[212:215],v[36:39],v[24:27]
v_mfma_f32_16x16x32_bf16 v[20:23],a[216:219],v[40:43],v[20:23]
ds_read_b128 v[36:39],v2 offset:34816
v_mfma_f32_16x16x32_bf16 v[24:27],a[220:223],v[40:43],v[24:27]
s_waitcnt lgkmcnt(5)
buffer_load_dwordx4 v0,s[36:39],s61 offen offset:2048 sc1 lds
v_mfma_f32_16x16x32_bf16 v[20:23],a[224:227],v[44:47],v[20:23]
ds_read_b128 v[40:43],v2 offset:35840
v_mfma_f32_16x16x32_bf16 v[24:27],a[228:231],v[44:47],v[24:27]
v_mfma_f32_16x16x32_bf16 v[20:23],a[232:235],v[48:51],v[20:23]
ds_read_b128 v[44:47],v2 offset:36864
v_mfma_f32_16x16x32_bf16 v[24:27],a[236:239],v[48:51],v[24:27]
s_waitcnt lgkmcnt(5)
v_mfma_f32_16x16x32_bf16 v[20:23],a[240:243],v[52:55],v[20:23]
buffer_load_dwordx4 v0,s[36:39],s61 offen offset:3072 sc1 lds
v_mfma_f32_16x16x32_bf16 v[24:27],a[244:247],v[52:55],v[24:27]
s_add_u32 s61,s61,4096
ds_read_b128 v[48:51],v2 offset:37888
s_add_u32 m0,s88,32768
v_mfma_f32_16x16x32_bf16 v[20:23],a[248:251],v[56:59],v[20:23]
ds_read_b128 v[52:55],v2 offset:38912
v_mfma_f32_16x16x32_bf16 v[24:27],a[252:255],v[56:59],v[24:27]
s_waitcnt lgkmcnt(5)
v_mfma_f32_16x16x32_bf16 v[20:23],v[108:111],v[28:31],v[20:23]
ds_read_b128 v[56:59],v2 offset:39936
v_mfma_f32_16x16x32_bf16 v[24:27],v[112:115],v[28:31],v[24:27]
buffer_load_dwordx4 v0,s[36:39],s61 offen offset:0 sc1 lds
v_mfma_f32_16x16x32_bf16 v[20:23],v[116:119],v[32:35],v[20:23]
ds_read_b128 v[28:31],v2 offset:40960
v_mfma_f32_16x16x32_bf16 v[24:27],v[120:123],v[32:35],v[24:27]
s_waitcnt lgkmcnt(5)
v_mfma_f32_16x16x32_bf16 v[20:23],v[124:127],v[36:39],v[20:23]
ds_read_b128 v[32:35],v2 offset:41984
v_mfma_f32_16x16x32_bf16 v[24:27],v[128:131],v[36:39],v[24:27]
v_mfma_f32_16x16x32_bf16 v[20:23],v[132:135],v[40:43],v[20:23]
buffer_load_dwordx4 v0,s[36:39],s61 offen offset:1024 sc1 lds
v_mfma_f32_16x16x32_bf16 v[24:27],v[136:139],v[40:43],v[24:27]
s_waitcnt lgkmcnt(4)
ds_read_b128 v[36:39],v2 offset:43008
v_mfma_f32_16x16x32_bf16 v[20:23],v[140:143],v[44:47],v[20:23]
ds_read_b128 v[40:43],v2 offset:44032
v_mfma_f32_16x16x32_bf16 v[24:27],v[144:147],v[44:47],v[24:27]
v_mfma_f32_16x16x32_bf16 v[20:23],v[148:151],v[48:51],v[20:23]
ds_read_b128 v[44:47],v2 offset:45056
v_mfma_f32_16x16x32_bf16 v[24:27],v[152:155],v[48:51],v[24:27]
s_waitcnt lgkmcnt(5)
buffer_load_dwordx4 v0,s[36:39],s61 offen offset:2048 sc1 lds
v_mfma_f32_16x16x32_bf16 v[20:23],v[156:159],v[52:55],v[20:23]
ds_read_b128 v[48:51],v2 offset:46080
v_mfma_f32_16x16x32_bf16 v[24:27],v[160:163],v[52:55],v[24:27]
v_mfma_f32_16x16x32_bf16 v[20:23],v[164:167],v[56:59],v[20:23]
ds_read_b128 v[52:55],v2 offset:47104
v_mfma_f32_16x16x32_bf16 v[24:27],v[168:171],v[56:59],v[24:27]
s_waitcnt lgkmcnt(5)
v_mfma_f32_16x16x32_bf16 v[20:23],v[172:175],v[28:31],v[20:23]
ds_read_b128 v[56:59],v2 offset:48128
v_mfma_f32_16x16x32_bf16 v[24:27],v[176:179],v[28:31],v[24:27]
ds_read_b128 v[88:91],v10 offset:0
v_mfma_f32_16x16x32_bf16 v[20:23],v[180:183],v[32:35],v[20:23]
ds_read_b128 v[28:31],v2 offset:49152
v_mfma_f32_16x16x32_bf16 v[24:27],v[184:187],v[32:35],v[24:27]
s_waitcnt lgkmcnt(6)
ds_read_b128 v[92:95],v10 offset:1024
v_mfma_f32_16x16x32_bf16 v[20:23],v[188:191],v[36:39],v[20:23]
ds_read_b128 v[32:35],v2 offset:50176
v_mfma_f32_16x16x32_bf16 v[24:27],v[192:195],v[36:39],v[24:27]
ds_read_b128 v[96:99],v10 offset:2048
v_mfma_f32_16x16x32_bf16 v[20:23],v[196:199],v[40:43],v[20:23]
ds_read_b128 v[36:39],v2 offset:51200
v_mfma_f32_16x16x32_bf16 v[24:27],v[200:203],v[40:43],v[24:27]
s_waitcnt lgkmcnt(5)
ds_read_b128 v[76:79],v10 offset:3072
v_mfma_f32_16x16x32_bf16 v[20:23],v[204:207],v[44:47],v[20:23]
ds_read_b128 v[40:43],v2 offset:52224
v_mfma_f32_16x16x32_bf16 v[24:27],v[208:211],v[44:47],v[24:27]
ds_read_b128 v[80:83],v10 offset:4096
v_mfma_f32_16x16x32_bf16 v[20:23],v[212:215],v[48:51],v[20:23]
ds_read_b128 v[44:47],v2 offset:53248
v_mfma_f32_16x16x32_bf16 v[24:27],v[88:91],v[48:51],v[24:27]
s_waitcnt lgkmcnt(5)
ds_read_b128 v[84:87],v10 offset:5120
v_mfma_f32_16x16x32_bf16 v[20:23],v[216:219],v[52:55],v[20:23]
ds_read_b128 v[48:51],v2 offset:54272
v_mfma_f32_16x16x32_bf16 v[24:27],v[92:95],v[52:55],v[24:27]
ds_read_b128 v[88:91],v10 offset:6144)"
R"(
v_mfma_f32_16x16x32_bf16 v[20:23],v[220:223],v[56:59],v[20:23]
ds_read_b128 v[52:55],v2 offset:55296
v_mfma_f32_16x16x32_bf16 v[24:27],v[96:99],v[56:59],v[24:27]
s_waitcnt lgkmcnt(5)
ds_read_b128 v[92:95],v10 offset:7168
v_mfma_f32_16x16x32_bf16 v[20:23],v[224:227],v[28:31],v[20:23]
s_waitcnt vmcnt(7)
ds_read_b128 v[56:59],v2 offset:56320
ds_read_b128 v[96:99],v10 offset:8192
v_mfma_f32_16x16x32_bf16 v[24:27],v[76:79],v[28:31],v[24:27]
s_barrier
v_mfma_f32_16x16x32_bf16 v[20:23],v[228:231],v[32:35],v[20:23]
ds_read_b128 v[28:31],v1 offset:0
ds_read_b32 v11,v8 offset:3072
v_mfma_f32_16x16x32_bf16 v[24:27],v[80:83],v[32:35],v[24:27]
s_waitcnt lgkmcnt(6)
ds_read_b128 v[76:79],v10 offset:9216
v_mfma_f32_16x16x32_bf16 v[20:23],v[232:235],v[36:39],v[20:23]
ds_read_b128 v[32:35],v1 offset:1024
v_mfma_f32_16x16x32_bf16 v[24:27],v[84:87],v[36:39],v[24:27]
ds_read_b128 v[80:83],v10 offset:10240
v_mfma_f32_16x16x32_bf16 v[20:23],v[236:239],v[40:43],v[20:23]
s_waitcnt lgkmcnt(3)
ds_read_b128 v[36:39],v1 offset:2048
v_mfma_f32_16x16x32_bf16 v[24:27],v[88:91],v[40:43],v[24:27]
v_cmp_eq_u32 vcc,s60,v11
v_mfma_f32_16x16x32_bf16 v[20:23],v[240:243],v[44:47],v[20:23]
s_cmp_eq_u32 vcc_lo,-1
ds_read_b128 v[40:43],v1 offset:3072
s_cselect_b32 s66,s86,s59
v_mfma_f32_16x16x32_bf16 v[24:27],v[92:95],v[44:47],v[24:27]
s_mul_i32 s61,s66,7
s_lshr_b32 s61,s61,1
s_cmp_lt_u32 s67,s59
v_mfma_f32_16x16x32_bf16 v[20:23],v[244:247],v[48:51],v[20:23]
ds_read_b128 v[44:47],v1 offset:4096
v_mfma_f32_16x16x32_bf16 v[24:27],v[96:99],v[48:51],v[24:27]
s_waitcnt lgkmcnt(3)
v_mfma_f32_16x16x32_bf16 v[20:23],v[248:251],v[52:55],v[20:23]
ds_read_b128 v[48:51],v1 offset:5120
v_mfma_f32_16x16x32_bf16 v[24:27],v[76:79],v[52:55],v[24:27]
v_mfma_f32_16x16x32_bf16 v[20:23],v[252:255],v[56:59],v[20:23]
ds_read_b128 v[52:55],v1 offset:6144
v_mfma_f32_16x16x32_bf16 v[24:27],v[80:83],v[56:59],v[24:27]
s_cbranch_scc1 Lg4_d0w0_%=
s_branch Lg4_slow0_%=
Lg4_d0w1_%=:
v_mfma_f32_16x16x32_bf16 v[12:15],a[0:3],v[28:31],0
s_add_u32 m0,s88,57344
ds_read_b128 v[56:59],v1 offset:7168
v_lshlrev_b32 v60,16,v72
v_mfma_f32_16x16x32_bf16 v[16:19],a[4:7],v[28:31],0
s_max_u32 s73,s47,s65
v_and_b32 v61,0xffff0000,v72
v_lshlrev_b32 v62,16,v73
v_mfma_f32_16x16x32_bf16 v[12:15],a[8:11],v[32:35],v[12:15]
buffer_load_dwordx4 v0,s[36:39],s61 offen offset:0 sc1 lds
v_mfma_f32_16x16x32_bf16 v[16:19],a[12:15],v[32:35],v[16:19]
s_waitcnt lgkmcnt(4)
ds_read_b128 v[28:31],v1 offset:8192
v_and_b32 v63,0xffff0000,v73
v_mfma_f32_16x16x32_bf16 v[12:15],a[16:19],v[36:39],v[12:15]
s_mov_b32 s65,0
ds_read_b128 v[32:35],v1 offset:9216
v_lshlrev_b32 v64,16,v74
v_mfma_f32_16x16x32_bf16 v[16:19],a[20:23],v[36:39],v[16:19]
v_and_b32 v65,0xffff0000,v74
v_lshlrev_b32 v66,16,v75
v_mfma_f32_16x16x32_bf16 v[12:15],a[24:27],v[40:43],v[12:15]
buffer_load_dwordx4 v0,s[36:39],s61 offen offset:1024 sc1 lds
v_mfma_f32_16x16x32_bf16 v[16:19],a[28:31],v[40:43],v[16:19]
s_waitcnt lgkmcnt(4)
ds_read_b128 v[36:39],v1 offset:10240
v_and_b32 v67,0xffff0000,v75
v_mfma_f32_16x16x32_bf16 v[12:15],a[32:35],v[44:47],v[12:15]
ds_read_b128 v[40:43],v1 offset:11264
v_add_f32 v20,v20,v60
v_mfma_f32_16x16x32_bf16 v[16:19],a[36:39],v[44:47],v[16:19]
v_add_f32 v21,v21,v61
v_add_f32 v22,v22,v62
v_mfma_f32_16x16x32_bf16 v[12:15],a[40:43],v[48:51],v[12:15]
buffer_load_dwordx4 v0,s[36:39],s61 offen offset:2048 sc1 lds
v_mfma_f32_16x16x32_bf16 v[16:19],a[44:47],v[48:51],v[16:19]
s_waitcnt lgkmcnt(4)
ds_read_b128 v[44:47],v1 offset:12288
v_add_f32 v23,v23,v63
v_mfma_f32_16x16x32_bf16 v[12:15],a[48:51],v[52:55],v[12:15]
ds_read_b128 v[48:51],v1 offset:13312
v_add_f32 v24,v24,v64
v_mfma_f32_16x16x32_bf16 v[16:19],a[52:55],v[52:55],v[16:19]
v_add_f32 v25,v25,v65
v_add_f32 v26,v26,v66
v_mfma_f32_16x16x32_bf16 v[12:15],a[56:59],v[56:59],v[12:15]
buffer_load_dwordx4 v0,s[36:39],s61 offen offset:3072 sc1 lds
v_mfma_f32_16x16x32_bf16 v[16:19],a[60:63],v[56:59],v[16:19]
s_waitcnt lgkmcnt(4)
ds_read_b128 v[52:55],v1 offset:14336
v_add_f32 v27,v27,v67
v_mfma_f32_16x16x32_bf16 v[12:15],a[64:67],v[28:31],v[12:15]
s_add_u32 s61,s61,4096
ds_read_b128 v[56:59],v1 offset:15360
v_cvt_pk_bf16_f32 v60,v20,v21
v_mfma_f32_16x16x32_bf16 v[16:19],a[68:71],v[28:31],v[16:19]
s_add_u32 m0,s88,61440
v_cvt_pk_bf16_f32 v61,v22,v23
v_cvt_pk_bf16_f32 v62,v24,v25
v_cvt_pk_bf16_f32 v63,v26,v27
v_mfma_f32_16x16x32_bf16 v[12:15],a[72:75],v[32:35],v[12:15]
buffer_store_dwordx4 v[60:63],v3,s[36:39],s73 offen offset:0 sc0 sc1
ds_read_b128 v[28:31],v1 offset:16384
v_mfma_f32_16x16x32_bf16 v[16:19],a[76:79],v[32:35],v[16:19]
s_waitcnt lgkmcnt(5)
s_add_u32 s53,s52,s55
s_cmp_eq_u32 s53,s56
v_mfma_f32_16x16x32_bf16 v[12:15],a[80:83],v[36:39],v[12:15]
s_cselect_b32 s53,s57,s53
ds_read_b128 v[32:35],v1 offset:17408
s_cmp_eq_u32 s53,s58
v_mfma_f32_16x16x32_bf16 v[16:19],a[84:87],v[36:39],v[16:19]
s_cselect_b32 s53,s59,s53
s_cmp_ge_u32 s66,s59
s_cselect_b32 s86,s49,s50
v_mfma_f32_16x16x32_bf16 v[12:15],a[88:91],v[40:43],v[12:15]
buffer_load_dwordx4 v0,s[36:39],s61 offen offset:0 sc1 lds
v_mfma_f32_16x16x32_bf16 v[16:19],a[92:95],v[40:43],v[16:19]
s_waitcnt lgkmcnt(4)
ds_read_b128 v[36:39],v1 offset:18432
v_mfma_f32_16x16x32_bf16 v[12:15],a[96:99],v[44:47],v[12:15]
ds_read_b128 v[40:43],v1 offset:19456
v_mfma_f32_16x16x32_bf16 v[16:19],a[100:103],v[44:47],v[16:19]
v_mfma_f32_16x16x32_bf16 v[12:15],a[104:107],v[48:51],v[12:15]
buffer_load_dwordx4 v0,s[36:39],s61 offen offset:1024 sc1 lds
v_mfma_f32_16x16x32_bf16 v[16:19],a[108:111],v[48:51],v[16:19]
s_waitcnt lgkmcnt(4)
ds_read_b128 v[44:47],v1 offset:20480
v_mfma_f32_16x16x32_bf16 v[12:15],a[112:115],v[52:55],v[12:15]
ds_read_b128 v[48:51],v1 offset:21504
v_mfma_f32_16x16x32_bf16 v[16:19],a[116:119],v[52:55],v[16:19]
v_mfma_f32_16x16x32_bf16 v[12:15],a[120:123],v[56:59],v[12:15]
buffer_load_dwordx4 v0,s[36:39],s61 offen offset:2048 sc1 lds
v_mfma_f32_16x16x32_bf16 v[16:19],a[124:127],v[56:59],v[16:19]
s_waitcnt lgkmcnt(4)
ds_read_b128 v[52:55],v1 offset:22528
s_add_u32 s61,s61,24576
v_mfma_f32_16x16x32_bf16 v[12:15],a[128:131],v[28:31],v[12:15]
s_add_u32 m0,s88,86016
ds_read_b128 v[56:59],v1 offset:23552
v_mfma_f32_16x16x32_bf16 v[16:19],a[132:135],v[28:31],v[16:19]
v_mfma_f32_16x16x32_bf16 v[12:15],a[136:139],v[32:35],v[12:15]
buffer_load_dwordx4 v[68:71],v9,s[36:39],s48 offen offset:0 sc1
v_mfma_f32_16x16x32_bf16 v[16:19],a[140:143],v[32:35],v[16:19]
s_waitcnt lgkmcnt(4)
ds_read_b128 v[28:31],v1 offset:24576
v_mfma_f32_16x16x32_bf16 v[12:15],a[144:147],v[36:39],v[12:15]
ds_read_b128 v[32:35],v1 offset:25600
v_mfma_f32_16x16x32_bf16 v[16:19],a[148:151],v[36:39],v[16:19]
v_mfma_f32_16x16x32_bf16 v[12:15],a[152:155],v[40:43],v[12:15]
ds_read_b128 v[36:39],v1 offset:26624
v_mfma_f32_16x16x32_bf16 v[16:19],a[156:159],v[40:43],v[16:19]
s_waitcnt lgkmcnt(5)
v_mfma_f32_16x16x32_bf16 v[12:15],a[160:163],v[44:47],v[12:15]
s_waitcnt vmcnt(9)
ds_read_b128 v[40:43],v1 offset:27648
v_mfma_f32_16x16x32_bf16 v[16:19],a[164:167],v[44:47],v[16:19]
s_barrier
v_mfma_f32_16x16x32_bf16 v[12:15],a[168:171],v[48:51],v[12:15]
ds_read_b128 v[44:47],v1 offset:28672
v_mfma_f32_16x16x32_bf16 v[16:19],a[172:175],v[48:51],v[16:19]
s_waitcnt lgkmcnt(5)
v_mfma_f32_16x16x32_bf16 v[12:15],a[176:179],v[52:55],v[12:15]
ds_read_b128 v[48:51],v1 offset:29696
v_mfma_f32_16x16x32_bf16 v[16:19],a[180:183],v[52:55],v[16:19]
v_mfma_f32_16x16x32_bf16 v[12:15],a[184:187],v[56:59],v[12:15]
buffer_load_dwordx4 v0,s[36:39],s61 offen offset:0 sc1 lds
v_mfma_f32_16x16x32_bf16 v[16:19],a[188:191],v[56:59],v[16:19]
s_waitcnt lgkmcnt(4)
ds_read_b128 v[52:55],v1 offset:30720
v_mfma_f32_16x16x32_bf16 v[12:15],a[192:195],v[28:31],v[12:15]
ds_read_b128 v[56:59],v1 offset:31744
v_mfma_f32_16x16x32_bf16 v[16:19],a[196:199],v[28:31],v[16:19]
v_mfma_f32_16x16x32_bf16 v[12:15],a[200:203],v[32:35],v[12:15]
ds_read_b128 v[28:31],v1 offset:32768
v_mfma_f32_16x16x32_bf16 v[16:19],a[204:207],v[32:35],v[16:19]
s_waitcnt lgkmcnt(5)
buffer_load_dwordx4 v0,s[36:39],s61 offen offset:1024 sc1 lds
v_mfma_f32_16x16x32_bf16 v[12:15],a[208:211],v[36:39],v[12:15]
ds_read_b128 v[32:35],v1 offset:33792
v_mfma_f32_16x16x32_bf16 v[16:19],a[212:215],v[36:39],v[16:19]
v_mfma_f32_16x16x32_bf16 v[12:15],a[216:219],v[40:43],v[12:15]
ds_read_b128 v[36:39],v1 offset:34816
v_mfma_f32_16x16x32_bf16 v[16:19],a[220:223],v[40:43],v[16:19]
s_waitcnt lgkmcnt(5)
v_mfma_f32_16x16x32_bf16 v[12:15],a[224:227],v[44:47],v[12:15]
buffer_load_dwordx4 v0,s[36:39],s61 offen offset:2048 sc1 lds
v_mfma_f32_16x16x32_bf16 v[16:19],a[228:231],v[44:47],v[16:19]
ds_read_b128 v[40:43],v1 offset:35840
v_mfma_f32_16x16x32_bf16 v[12:15],a[232:235],v[48:51],v[12:15]
ds_read_b128 v[44:47],v1 offset:36864
v_mfma_f32_16x16x32_bf16 v[16:19],a[236:239],v[48:51],v[16:19]
s_waitcnt lgkmcnt(5))"
R"(
v_mfma_f32_16x16x32_bf16 v[12:15],a[240:243],v[52:55],v[12:15]
ds_read_b128 v[48:51],v1 offset:37888
v_mfma_f32_16x16x32_bf16 v[16:19],a[244:247],v[52:55],v[16:19]
buffer_load_dwordx4 v0,s[36:39],s61 offen offset:3072 sc1 lds
v_mfma_f32_16x16x32_bf16 v[12:15],a[248:251],v[56:59],v[12:15]
s_add_u32 s61,s61,4096
ds_read_b128 v[52:55],v1 offset:38912
s_add_u32 m0,s88,90112
v_mfma_f32_16x16x32_bf16 v[16:19],a[252:255],v[56:59],v[16:19]
s_waitcnt lgkmcnt(5)
v_mfma_f32_16x16x32_bf16 v[12:15],v[108:111],v[28:31],v[12:15]
ds_read_b128 v[56:59],v1 offset:39936
v_mfma_f32_16x16x32_bf16 v[16:19],v[112:115],v[28:31],v[16:19]
v_mfma_f32_16x16x32_bf16 v[12:15],v[116:119],v[32:35],v[12:15]
buffer_load_dwordx4 v0,s[36:39],s61 offen offset:0 sc1 lds
v_mfma_f32_16x16x32_bf16 v[16:19],v[120:123],v[32:35],v[16:19]
s_waitcnt lgkmcnt(4)
ds_read_b128 v[28:31],v1 offset:40960
v_mfma_f32_16x16x32_bf16 v[12:15],v[124:127],v[36:39],v[12:15]
ds_read_b128 v[32:35],v1 offset:41984
v_mfma_f32_16x16x32_bf16 v[16:19],v[128:131],v[36:39],v[16:19]
v_mfma_f32_16x16x32_bf16 v[12:15],v[132:135],v[40:43],v[12:15]
ds_read_b128 v[36:39],v1 offset:43008
v_mfma_f32_16x16x32_bf16 v[16:19],v[136:139],v[40:43],v[16:19]
s_waitcnt lgkmcnt(5)
buffer_load_dwordx4 v0,s[36:39],s61 offen offset:1024 sc1 lds
v_mfma_f32_16x16x32_bf16 v[12:15],v[140:143],v[44:47],v[12:15]
ds_read_b128 v[40:43],v1 offset:44032
v_mfma_f32_16x16x32_bf16 v[16:19],v[144:147],v[44:47],v[16:19]
v_mfma_f32_16x16x32_bf16 v[12:15],v[148:151],v[48:51],v[12:15]
ds_read_b128 v[44:47],v1 offset:45056
v_mfma_f32_16x16x32_bf16 v[16:19],v[152:155],v[48:51],v[16:19]
s_waitcnt lgkmcnt(5)
v_mfma_f32_16x16x32_bf16 v[12:15],v[156:159],v[52:55],v[12:15]
buffer_load_dwordx4 v0,s[36:39],s61 offen offset:2048 sc1 lds
v_mfma_f32_16x16x32_bf16 v[16:19],v[160:163],v[52:55],v[16:19]
ds_read_b128 v[48:51],v1 offset:46080
v_mfma_f32_16x16x32_bf16 v[12:15],v[164:167],v[56:59],v[12:15]
ds_read_b128 v[52:55],v1 offset:47104
v_mfma_f32_16x16x32_bf16 v[16:19],v[168:171],v[56:59],v[16:19]
s_waitcnt lgkmcnt(5)
v_mfma_f32_16x16x32_bf16 v[12:15],v[172:175],v[28:31],v[12:15]
ds_read_b128 v[56:59],v1 offset:48128
v_mfma_f32_16x16x32_bf16 v[16:19],v[176:179],v[28:31],v[16:19]
ds_read_b128 v[88:91],v10 offset:0
v_mfma_f32_16x16x32_bf16 v[12:15],v[180:183],v[32:35],v[12:15]
ds_read_b128 v[28:31],v1 offset:49152
v_mfma_f32_16x16x32_bf16 v[16:19],v[184:187],v[32:35],v[16:19]
s_waitcnt lgkmcnt(6)
ds_read_b128 v[92:95],v10 offset:1024
v_mfma_f32_16x16x32_bf16 v[12:15],v[188:191],v[36:39],v[12:15]
ds_read_b128 v[32:35],v1 offset:50176
v_mfma_f32_16x16x32_bf16 v[16:19],v[192:195],v[36:39],v[16:19]
ds_read_b128 v[96:99],v10 offset:2048
v_mfma_f32_16x16x32_bf16 v[12:15],v[196:199],v[40:43],v[12:15]
ds_read_b128 v[36:39],v1 offset:51200
v_mfma_f32_16x16x32_bf16 v[16:19],v[200:203],v[40:43],v[16:19]
s_waitcnt lgkmcnt(5)
ds_read_b128 v[76:79],v10 offset:3072
v_mfma_f32_16x16x32_bf16 v[12:15],v[204:207],v[44:47],v[12:15]
ds_read_b128 v[40:43],v1 offset:52224
v_mfma_f32_16x16x32_bf16 v[16:19],v[208:211],v[44:47],v[16:19]
ds_read_b128 v[80:83],v10 offset:4096
v_mfma_f32_16x16x32_bf16 v[12:15],v[212:215],v[48:51],v[12:15]
ds_read_b128 v[44:47],v1 offset:53248
v_mfma_f32_16x16x32_bf16 v[16:19],v[88:91],v[48:51],v[16:19]
s_waitcnt lgkmcnt(5)
ds_read_b128 v[84:87],v10 offset:5120
v_mfma_f32_16x16x32_bf16 v[12:15],v[216:219],v[52:55],v[12:15]
ds_read_b128 v[48:51],v1 offset:54272
v_mfma_f32_16x16x32_bf16 v[16:19],v[92:95],v[52:55],v[16:19]
ds_read_b128 v[88:91],v10 offset:6144
v_mfma_f32_16x16x32_bf16 v[12:15],v[220:223],v[56:59],v[12:15]
ds_read_b128 v[52:55],v1 offset:55296
v_mfma_f32_16x16x32_bf16 v[16:19],v[96:99],v[56:59],v[16:19]
s_waitcnt lgkmcnt(5)
ds_read_b128 v[92:95],v10 offset:7168
v_mfma_f32_16x16x32_bf16 v[12:15],v[224:227],v[28:31],v[12:15]
s_waitcnt vmcnt(7)
ds_read_b128 v[56:59],v1 offset:56320
ds_read_b128 v[96:99],v10 offset:8192
v_mfma_f32_16x16x32_bf16 v[16:19],v[76:79],v[28:31],v[16:19]
s_barrier
v_mfma_f32_16x16x32_bf16 v[12:15],v[228:231],v[32:35],v[12:15]
ds_read_b128 v[28:31],v2 offset:0
ds_read_b32 v11,v8 offset:3072
v_mfma_f32_16x16x32_bf16 v[16:19],v[80:83],v[32:35],v[16:19]
s_waitcnt lgkmcnt(6)
ds_read_b128 v[76:79],v10 offset:9216
v_mfma_f32_16x16x32_bf16 v[12:15],v[232:235],v[36:39],v[12:15]
ds_read_b128 v[32:35],v2 offset:1024
v_mfma_f32_16x16x32_bf16 v[16:19],v[84:87],v[36:39],v[16:19]
ds_read_b128 v[80:83],v10 offset:10240
v_mfma_f32_16x16x32_bf16 v[12:15],v[236:239],v[40:43],v[12:15]
s_waitcnt lgkmcnt(3)
ds_read_b128 v[36:39],v2 offset:2048
v_mfma_f32_16x16x32_bf16 v[16:19],v[88:91],v[40:43],v[16:19]
v_cmp_eq_u32 vcc,s60,v11
v_mfma_f32_16x16x32_bf16 v[12:15],v[240:243],v[44:47],v[12:15]
s_cmp_eq_u32 vcc_lo,-1
ds_read_b128 v[40:43],v2 offset:3072
s_cselect_b32 s67,s86,s59
v_mfma_f32_16x16x32_bf16 v[16:19],v[92:95],v[44:47],v[16:19]
s_mul_i32 s61,s67,7
s_lshr_b32 s61,s61,1
s_cmp_ge_u32 s66,s59
v_mfma_f32_16x16x32_bf16 v[12:15],v[244:247],v[48:51],v[12:15]
ds_read_b128 v[44:47],v2 offset:4096
v_mfma_f32_16x16x32_bf16 v[16:19],v[96:99],v[48:51],v[16:19]
s_waitcnt lgkmcnt(3)
v_mfma_f32_16x16x32_bf16 v[12:15],v[248:251],v[52:55],v[12:15]
ds_read_b128 v[48:51],v2 offset:5120
v_mfma_f32_16x16x32_bf16 v[16:19],v[76:79],v[52:55],v[16:19]
v_mfma_f32_16x16x32_bf16 v[12:15],v[252:255],v[56:59],v[12:15]
ds_read_b128 v[52:55],v2 offset:6144
v_mfma_f32_16x16x32_bf16 v[16:19],v[80:83],v[56:59],v[16:19]
s_cbranch_scc1 Lg4_slow1_%=
Lg4_d1w1_%=:
v_mfma_f32_16x16x32_bf16 v[20:23],a[0:3],v[28:31],0
s_add_u32 m0,s88,0
ds_read_b128 v[56:59],v2 offset:7168
v_lshlrev_b32 v60,16,v68
v_mfma_f32_16x16x32_bf16 v[24:27],a[4:7],v[28:31],0
s_max_u32 s73,s48,s65
v_and_b32 v61,0xffff0000,v68
v_lshlrev_b32 v62,16,v69
v_mfma_f32_16x16x32_bf16 v[20:23],a[8:11],v[32:35],v[20:23]
buffer_load_dwordx4 v0,s[36:39],s61 offen offset:0 sc1 lds
v_mfma_f32_16x16x32_bf16 v[24:27],a[12:15],v[32:35],v[24:27]
s_waitcnt lgkmcnt(4)
ds_read_b128 v[28:31],v2 offset:8192
v_and_b32 v63,0xffff0000,v69
v_mfma_f32_16x16x32_bf16 v[20:23],a[16:19],v[36:39],v[20:23]
s_mov_b32 s65,0
ds_read_b128 v[32:35],v2 offset:9216
v_lshlrev_b32 v64,16,v70
v_mfma_f32_16x16x32_bf16 v[24:27],a[20:23],v[36:39],v[24:27]
s_mov_b64 s[44:45],s[46:47]
v_and_b32 v65,0xffff0000,v70
v_lshlrev_b32 v66,16,v71
v_mfma_f32_16x16x32_bf16 v[20:23],a[24:27],v[40:43],v[20:23]
buffer_load_dwordx4 v0,s[36:39],s61 offen offset:1024 sc1 lds
v_mfma_f32_16x16x32_bf16 v[24:27],a[28:31],v[40:43],v[24:27]
s_waitcnt lgkmcnt(4)
ds_read_b128 v[36:39],v2 offset:10240
v_and_b32 v67,0xffff0000,v71
v_mfma_f32_16x16x32_bf16 v[20:23],a[32:35],v[44:47],v[20:23]
s_mov_b64 s[46:47],s[48:49]
ds_read_b128 v[40:43],v2 offset:11264
v_add_f32 v12,v12,v60
v_mfma_f32_16x16x32_bf16 v[24:27],a[36:39],v[44:47],v[24:27]
v_add_f32 v13,v13,v61
v_add_f32 v14,v14,v62
v_mfma_f32_16x16x32_bf16 v[20:23],a[40:43],v[48:51],v[20:23]
buffer_load_dwordx4 v0,s[36:39],s61 offen offset:2048 sc1 lds
v_mfma_f32_16x16x32_bf16 v[24:27],a[44:47],v[48:51],v[24:27]
s_waitcnt lgkmcnt(4)
ds_read_b128 v[44:47],v2 offset:12288
v_add_f32 v15,v15,v63
v_mfma_f32_16x16x32_bf16 v[20:23],a[48:51],v[52:55],v[20:23]
ds_read_b128 v[48:51],v2 offset:13312
v_add_f32 v16,v16,v64
v_mfma_f32_16x16x32_bf16 v[24:27],a[52:55],v[52:55],v[24:27]
v_add_f32 v17,v17,v65
v_add_f32 v18,v18,v66
v_mfma_f32_16x16x32_bf16 v[20:23],a[56:59],v[56:59],v[20:23]
buffer_load_dwordx4 v0,s[36:39],s61 offen offset:3072 sc1 lds
v_mfma_f32_16x16x32_bf16 v[24:27],a[60:63],v[56:59],v[24:27]
s_waitcnt lgkmcnt(4)
ds_read_b128 v[52:55],v2 offset:14336
v_add_f32 v19,v19,v67
v_mfma_f32_16x16x32_bf16 v[20:23],a[64:67],v[28:31],v[20:23]
s_add_u32 s61,s61,4096
ds_read_b128 v[56:59],v2 offset:15360
v_cvt_pk_bf16_f32 v60,v12,v13
v_mfma_f32_16x16x32_bf16 v[24:27],a[68:71],v[28:31],v[24:27]
s_add_u32 m0,s88,4096
v_cvt_pk_bf16_f32 v61,v14,v15
v_cvt_pk_bf16_f32 v62,v16,v17
v_cvt_pk_bf16_f32 v63,v18,v19
v_mfma_f32_16x16x32_bf16 v[20:23],a[72:75],v[32:35],v[20:23]
buffer_store_dwordx4 v[60:63],v3,s[36:39],s73 offen offset:0 sc0 sc1
ds_read_b128 v[28:31],v2 offset:16384
v_mfma_f32_16x16x32_bf16 v[24:27],a[76:79],v[32:35],v[24:27]
s_waitcnt lgkmcnt(5)
s_add_u32 s74,s53,s55
s_cmp_eq_u32 s74,s56
v_mfma_f32_16x16x32_bf16 v[20:23],a[80:83],v[36:39],v[20:23]
s_cselect_b32 s74,s57,s74
ds_read_b128 v[32:35],v2 offset:17408
s_cmp_eq_u32 s74,s58
v_mfma_f32_16x16x32_bf16 v[24:27],a[84:87],v[36:39],v[24:27]
s_cselect_b32 s74,s59,s74
s_cmp_ge_u32 s67,s59
s_cselect_b32 s86,s50,s51
v_mfma_f32_16x16x32_bf16 v[20:23],a[88:91],v[40:43],v[20:23]
buffer_load_dwordx4 v0,s[36:39],s61 offen offset:0 sc1 lds
v_mfma_f32_16x16x32_bf16 v[24:27],a[92:95],v[40:43],v[24:27]
s_waitcnt lgkmcnt(4)
ds_read_b128 v[36:39],v2 offset:18432)"
R"(
v_mfma_f32_16x16x32_bf16 v[20:23],a[96:99],v[44:47],v[20:23]
ds_read_b128 v[40:43],v2 offset:19456
v_mfma_f32_16x16x32_bf16 v[24:27],a[100:103],v[44:47],v[24:27]
v_mfma_f32_16x16x32_bf16 v[20:23],a[104:107],v[48:51],v[20:23]
buffer_load_dwordx4 v0,s[36:39],s61 offen offset:1024 sc1 lds
v_mfma_f32_16x16x32_bf16 v[24:27],a[108:111],v[48:51],v[24:27]
s_waitcnt lgkmcnt(4)
ds_read_b128 v[44:47],v2 offset:20480
v_mfma_f32_16x16x32_bf16 v[20:23],a[112:115],v[52:55],v[20:23]
ds_read_b128 v[48:51],v2 offset:21504
v_mfma_f32_16x16x32_bf16 v[24:27],a[116:119],v[52:55],v[24:27]
v_mfma_f32_16x16x32_bf16 v[20:23],a[120:123],v[56:59],v[20:23]
buffer_load_dwordx4 v0,s[36:39],s61 offen offset:2048 sc1 lds
v_mfma_f32_16x16x32_bf16 v[24:27],a[124:127],v[56:59],v[24:27]
s_waitcnt lgkmcnt(4)
ds_read_b128 v[52:55],v2 offset:22528
s_add_u32 s61,s61,24576
v_mfma_f32_16x16x32_bf16 v[20:23],a[128:131],v[28:31],v[20:23]
s_add_u32 m0,s88,28672
ds_read_b128 v[56:59],v2 offset:23552
v_mfma_f32_16x16x32_bf16 v[24:27],a[132:135],v[28:31],v[24:27]
v_mfma_f32_16x16x32_bf16 v[20:23],a[136:139],v[32:35],v[20:23]
buffer_load_dwordx4 v[72:75],v9,s[36:39],s49 offen offset:0 sc1
v_mfma_f32_16x16x32_bf16 v[24:27],a[140:143],v[32:35],v[24:27]
s_waitcnt lgkmcnt(4)
ds_read_b128 v[28:31],v2 offset:24576
s_mov_b64 s[48:49],s[50:51]
v_mfma_f32_16x16x32_bf16 v[20:23],a[144:147],v[36:39],v[20:23]
s_mov_b64 s[50:51],s[52:53]
ds_read_b128 v[32:35],v2 offset:25600
s_mov_b32 s52,s74
v_mfma_f32_16x16x32_bf16 v[24:27],a[148:151],v[36:39],v[24:27]
v_mfma_f32_16x16x32_bf16 v[20:23],a[152:155],v[40:43],v[20:23]
ds_read_b128 v[36:39],v2 offset:26624
v_mfma_f32_16x16x32_bf16 v[24:27],a[156:159],v[40:43],v[24:27]
s_waitcnt lgkmcnt(5)
v_mfma_f32_16x16x32_bf16 v[20:23],a[160:163],v[44:47],v[20:23]
s_waitcnt vmcnt(9)
ds_read_b128 v[40:43],v2 offset:27648
v_mfma_f32_16x16x32_bf16 v[24:27],a[164:167],v[44:47],v[24:27]
s_barrier
v_mfma_f32_16x16x32_bf16 v[20:23],a[168:171],v[48:51],v[20:23]
ds_read_b128 v[44:47],v2 offset:28672
v_mfma_f32_16x16x32_bf16 v[24:27],a[172:175],v[48:51],v[24:27]
s_waitcnt lgkmcnt(5)
v_mfma_f32_16x16x32_bf16 v[20:23],a[176:179],v[52:55],v[20:23]
ds_read_b128 v[48:51],v2 offset:29696
v_mfma_f32_16x16x32_bf16 v[24:27],a[180:183],v[52:55],v[24:27]
v_mfma_f32_16x16x32_bf16 v[20:23],a[184:187],v[56:59],v[20:23]
buffer_load_dwordx4 v0,s[36:39],s61 offen offset:0 sc1 lds
v_mfma_f32_16x16x32_bf16 v[24:27],a[188:191],v[56:59],v[24:27]
s_waitcnt lgkmcnt(4)
ds_read_b128 v[52:55],v2 offset:30720
v_mfma_f32_16x16x32_bf16 v[20:23],a[192:195],v[28:31],v[20:23]
ds_read_b128 v[56:59],v2 offset:31744
v_mfma_f32_16x16x32_bf16 v[24:27],a[196:199],v[28:31],v[24:27]
v_mfma_f32_16x16x32_bf16 v[20:23],a[200:203],v[32:35],v[20:23]
ds_read_b128 v[28:31],v2 offset:32768
v_mfma_f32_16x16x32_bf16 v[24:27],a[204:207],v[32:35],v[24:27]
s_waitcnt lgkmcnt(5)
buffer_load_dwordx4 v0,s[36:39],s61 offen offset:1024 sc1 lds
v_mfma_f32_16x16x32_bf16 v[20:23],a[208:211],v[36:39],v[20:23]
ds_read_b128 v[32:35],v2 offset:33792
v_mfma_f32_16x16x32_bf16 v[24:27],a[212:215],v[36:39],v[24:27]
v_mfma_f32_16x16x32_bf16 v[20:23],a[216:219],v[40:43],v[20:23]
ds_read_b128 v[36:39],v2 offset:34816
v_mfma_f32_16x16x32_bf16 v[24:27],a[220:223],v[40:43],v[24:27]
s_waitcnt lgkmcnt(5)
v_mfma_f32_16x16x32_bf16 v[20:23],a[224:227],v[44:47],v[20:23]
buffer_load_dwordx4 v0,s[36:39],s61 offen offset:2048 sc1 lds
v_mfma_f32_16x16x32_bf16 v[24:27],a[228:231],v[44:47],v[24:27]
ds_read_b128 v[40:43],v2 offset:35840
v_mfma_f32_16x16x32_bf16 v[20:23],a[232:235],v[48:51],v[20:23]
ds_read_b128 v[44:47],v2 offset:36864
v_mfma_f32_16x16x32_bf16 v[24:27],a[236:239],v[48:51],v[24:27]
s_waitcnt lgkmcnt(5)
v_mfma_f32_16x16x32_bf16 v[20:23],a[240:243],v[52:55],v[20:23]
ds_read_b128 v[48:51],v2 offset:37888
v_mfma_f32_16x16x32_bf16 v[24:27],a[244:247],v[52:55],v[24:27]
buffer_load_dwordx4 v0,s[36:39],s61 offen offset:3072 sc1 lds
v_mfma_f32_16x16x32_bf16 v[20:23],a[248:251],v[56:59],v[20:23]
s_add_u32 s61,s61,4096
ds_read_b128 v[52:55],v2 offset:38912
s_add_u32 m0,s88,32768
v_mfma_f32_16x16x32_bf16 v[24:27],a[252:255],v[56:59],v[24:27]
s_waitcnt lgkmcnt(5)
v_mfma_f32_16x16x32_bf16 v[20:23],v[108:111],v[28:31],v[20:23]
ds_read_b128 v[56:59],v2 offset:39936
v_mfma_f32_16x16x32_bf16 v[24:27],v[112:115],v[28:31],v[24:27]
v_mfma_f32_16x16x32_bf16 v[20:23],v[116:119],v[32:35],v[20:23]
buffer_load_dwordx4 v0,s[36:39],s61 offen offset:0 sc1 lds
v_mfma_f32_16x16x32_bf16 v[24:27],v[120:123],v[32:35],v[24:27]
s_waitcnt lgkmcnt(4)
ds_read_b128 v[28:31],v2 offset:40960
v_mfma_f32_16x16x32_bf16 v[20:23],v[124:127],v[36:39],v[20:23]
ds_read_b128 v[32:35],v2 offset:41984
v_mfma_f32_16x16x32_bf16 v[24:27],v[128:131],v[36:39],v[24:27]
v_mfma_f32_16x16x32_bf16 v[20:23],v[132:135],v[40:43],v[20:23]
ds_read_b128 v[36:39],v2 offset:43008
v_mfma_f32_16x16x32_bf16 v[24:27],v[136:139],v[40:43],v[24:27]
s_waitcnt lgkmcnt(5)
buffer_load_dwordx4 v0,s[36:39],s61 offen offset:1024 sc1 lds
v_mfma_f32_16x16x32_bf16 v[20:23],v[140:143],v[44:47],v[20:23]
ds_read_b128 v[40:43],v2 offset:44032
v_mfma_f32_16x16x32_bf16 v[24:27],v[144:147],v[44:47],v[24:27]
v_mfma_f32_16x16x32_bf16 v[20:23],v[148:151],v[48:51],v[20:23]
ds_read_b128 v[44:47],v2 offset:45056
v_mfma_f32_16x16x32_bf16 v[24:27],v[152:155],v[48:51],v[24:27]
s_waitcnt lgkmcnt(5)
v_mfma_f32_16x16x32_bf16 v[20:23],v[156:159],v[52:55],v[20:23]
buffer_load_dwordx4 v0,s[36:39],s61 offen offset:2048 sc1 lds
v_mfma_f32_16x16x32_bf16 v[24:27],v[160:163],v[52:55],v[24:27]
ds_read_b128 v[48:51],v2 offset:46080
v_mfma_f32_16x16x32_bf16 v[20:23],v[164:167],v[56:59],v[20:23]
ds_read_b128 v[52:55],v2 offset:47104
v_mfma_f32_16x16x32_bf16 v[24:27],v[168:171],v[56:59],v[24:27]
s_waitcnt lgkmcnt(5)
v_mfma_f32_16x16x32_bf16 v[20:23],v[172:175],v[28:31],v[20:23]
ds_read_b128 v[56:59],v2 offset:48128
v_mfma_f32_16x16x32_bf16 v[24:27],v[176:179],v[28:31],v[24:27]
ds_read_b128 v[88:91],v10 offset:0
v_mfma_f32_16x16x32_bf16 v[20:23],v[180:183],v[32:35],v[20:23]
ds_read_b128 v[28:31],v2 offset:49152
v_mfma_f32_16x16x32_bf16 v[24:27],v[184:187],v[32:35],v[24:27]
s_waitcnt lgkmcnt(6)
ds_read_b128 v[92:95],v10 offset:1024
v_mfma_f32_16x16x32_bf16 v[20:23],v[188:191],v[36:39],v[20:23]
ds_read_b128 v[32:35],v2 offset:50176
v_mfma_f32_16x16x32_bf16 v[24:27],v[192:195],v[36:39],v[24:27]
ds_read_b128 v[96:99],v10 offset:2048
v_mfma_f32_16x16x32_bf16 v[20:23],v[196:199],v[40:43],v[20:23]
ds_read_b128 v[36:39],v2 offset:51200
v_mfma_f32_16x16x32_bf16 v[24:27],v[200:203],v[40:43],v[24:27]
s_waitcnt lgkmcnt(5)
ds_read_b128 v[76:79],v10 offset:3072
v_mfma_f32_16x16x32_bf16 v[20:23],v[204:207],v[44:47],v[20:23]
ds_read_b128 v[40:43],v2 offset:52224
v_mfma_f32_16x16x32_bf16 v[24:27],v[208:211],v[44:47],v[24:27]
ds_read_b128 v[80:83],v10 offset:4096
v_mfma_f32_16x16x32_bf16 v[20:23],v[212:215],v[48:51],v[20:23]
ds_read_b128 v[44:47],v2 offset:53248
v_mfma_f32_16x16x32_bf16 v[24:27],v[88:91],v[48:51],v[24:27]
s_waitcnt lgkmcnt(5)
ds_read_b128 v[84:87],v10 offset:5120
v_mfma_f32_16x16x32_bf16 v[20:23],v[216:219],v[52:55],v[20:23]
ds_read_b128 v[48:51],v2 offset:54272
v_mfma_f32_16x16x32_bf16 v[24:27],v[92:95],v[52:55],v[24:27]
ds_read_b128 v[88:91],v10 offset:6144
v_mfma_f32_16x16x32_bf16 v[20:23],v[220:223],v[56:59],v[20:23]
ds_read_b128 v[52:55],v2 offset:55296
v_mfma_f32_16x16x32_bf16 v[24:27],v[96:99],v[56:59],v[24:27]
s_waitcnt lgkmcnt(5)
ds_read_b128 v[92:95],v10 offset:7168
v_mfma_f32_16x16x32_bf16 v[20:23],v[224:227],v[28:31],v[20:23]
s_waitcnt vmcnt(7)
ds_read_b128 v[56:59],v2 offset:56320
ds_read_b128 v[96:99],v10 offset:8192
v_mfma_f32_16x16x32_bf16 v[24:27],v[76:79],v[28:31],v[24:27]
s_barrier
v_mfma_f32_16x16x32_bf16 v[20:23],v[228:231],v[32:35],v[20:23]
ds_read_b128 v[28:31],v1 offset:0
ds_read_b32 v11,v8 offset:3072
v_mfma_f32_16x16x32_bf16 v[24:27],v[80:83],v[32:35],v[24:27]
s_waitcnt lgkmcnt(6)
ds_read_b128 v[76:79],v10 offset:9216
v_mfma_f32_16x16x32_bf16 v[20:23],v[232:235],v[36:39],v[20:23]
ds_read_b128 v[32:35],v1 offset:1024
v_mfma_f32_16x16x32_bf16 v[24:27],v[84:87],v[36:39],v[24:27]
ds_read_b128 v[80:83],v10 offset:10240
v_mfma_f32_16x16x32_bf16 v[20:23],v[236:239],v[40:43],v[20:23]
s_waitcnt lgkmcnt(3)
ds_read_b128 v[36:39],v1 offset:2048
v_mfma_f32_16x16x32_bf16 v[24:27],v[88:91],v[40:43],v[24:27]
v_cmp_eq_u32 vcc,s60,v11
v_mfma_f32_16x16x32_bf16 v[20:23],v[240:243],v[44:47],v[20:23]
s_cmp_eq_u32 vcc_lo,-1
ds_read_b128 v[40:43],v1 offset:3072
s_cselect_b32 s66,s86,s59
v_mfma_f32_16x16x32_bf16 v[24:27],v[92:95],v[44:47],v[24:27]
s_mul_i32 s61,s66,7
s_lshr_b32 s61,s61,1
s_cmp_lt_u32 s67,s59
v_mfma_f32_16x16x32_bf16 v[20:23],v[244:247],v[48:51],v[20:23]
ds_read_b128 v[44:47],v1 offset:4096
v_mfma_f32_16x16x32_bf16 v[24:27],v[96:99],v[48:51],v[24:27]
s_waitcnt lgkmcnt(3)
v_mfma_f32_16x16x32_bf16 v[20:23],v[248:251],v[52:55],v[20:23]
ds_read_b128 v[48:51],v1 offset:5120
v_mfma_f32_16x16x32_bf16 v[24:27],v[76:79],v[52:55],v[24:27]
v_mfma_f32_16x16x32_bf16 v[20:23],v[252:255],v[56:59],v[20:23]
ds_read_b128 v[52:55],v1 offset:6144
v_mfma_f32_16x16x32_bf16 v[24:27],v[80:83],v[56:59],v[24:27]
s_cbranch_scc1 Lg4_d0w1_%=
s_branch Lg4_slow0_%=
Lg4_d0w2_%=:
v_mfma_f32_16x16x32_bf16 v[12:15],a[0:3],v[28:31],0
s_add_u32 m0,s88,57344
ds_read_b128 v[56:59],v1 offset:7168
v_lshlrev_b32 v60,16,v72
v_mfma_f32_16x16x32_bf16 v[16:19],a[4:7],v[28:31],0)"
R"(
s_max_u32 s73,s47,s65
v_and_b32 v61,0xffff0000,v72
v_lshlrev_b32 v62,16,v73
v_mfma_f32_16x16x32_bf16 v[12:15],a[8:11],v[32:35],v[12:15]
s_mov_b32 s65,0
ds_read_b128 v[28:31],v1 offset:8192
v_and_b32 v63,0xffff0000,v73
v_mfma_f32_16x16x32_bf16 v[16:19],a[12:15],v[32:35],v[16:19]
s_waitcnt lgkmcnt(5)
buffer_load_dwordx4 v0,s[36:39],s61 offen offset:0 sc1 lds
v_mfma_f32_16x16x32_bf16 v[12:15],a[16:19],v[36:39],v[12:15]
ds_read_b128 v[32:35],v1 offset:9216
v_lshlrev_b32 v64,16,v74
v_mfma_f32_16x16x32_bf16 v[16:19],a[20:23],v[36:39],v[16:19]
v_and_b32 v65,0xffff0000,v74
v_lshlrev_b32 v66,16,v75
v_mfma_f32_16x16x32_bf16 v[12:15],a[24:27],v[40:43],v[12:15]
ds_read_b128 v[36:39],v1 offset:10240
v_and_b32 v67,0xffff0000,v75
v_mfma_f32_16x16x32_bf16 v[16:19],a[28:31],v[40:43],v[16:19]
s_waitcnt lgkmcnt(5)
buffer_load_dwordx4 v0,s[36:39],s61 offen offset:1024 sc1 lds
v_mfma_f32_16x16x32_bf16 v[12:15],a[32:35],v[44:47],v[12:15]
ds_read_b128 v[40:43],v1 offset:11264
v_add_f32 v20,v20,v60
v_mfma_f32_16x16x32_bf16 v[16:19],a[36:39],v[44:47],v[16:19]
v_add_f32 v21,v21,v61
v_add_f32 v22,v22,v62
v_mfma_f32_16x16x32_bf16 v[12:15],a[40:43],v[48:51],v[12:15]
ds_read_b128 v[44:47],v1 offset:12288
v_add_f32 v23,v23,v63
v_mfma_f32_16x16x32_bf16 v[16:19],a[44:47],v[48:51],v[16:19]
s_waitcnt lgkmcnt(5)
buffer_load_dwordx4 v0,s[36:39],s61 offen offset:2048 sc1 lds
v_mfma_f32_16x16x32_bf16 v[12:15],a[48:51],v[52:55],v[12:15]
ds_read_b128 v[48:51],v1 offset:13312
v_add_f32 v24,v24,v64
v_mfma_f32_16x16x32_bf16 v[16:19],a[52:55],v[52:55],v[16:19]
v_add_f32 v25,v25,v65
v_add_f32 v26,v26,v66
v_mfma_f32_16x16x32_bf16 v[12:15],a[56:59],v[56:59],v[12:15]
ds_read_b128 v[52:55],v1 offset:14336
v_add_f32 v27,v27,v67
v_mfma_f32_16x16x32_bf16 v[16:19],a[60:63],v[56:59],v[16:19]
s_waitcnt lgkmcnt(5)
buffer_load_dwordx4 v0,s[36:39],s61 offen offset:3072 sc1 lds
v_mfma_f32_16x16x32_bf16 v[12:15],a[64:67],v[28:31],v[12:15]
s_add_u32 s61,s61,4096
ds_read_b128 v[56:59],v1 offset:15360
v_cvt_pk_bf16_f32 v60,v20,v21
v_mfma_f32_16x16x32_bf16 v[16:19],a[68:71],v[28:31],v[16:19]
s_add_u32 m0,s88,61440
v_cvt_pk_bf16_f32 v61,v22,v23
v_cvt_pk_bf16_f32 v62,v24,v25
v_mfma_f32_16x16x32_bf16 v[12:15],a[72:75],v[32:35],v[12:15]
s_add_u32 s53,s52,s55
ds_read_b128 v[28:31],v1 offset:16384
v_cvt_pk_bf16_f32 v63,v26,v27
v_mfma_f32_16x16x32_bf16 v[16:19],a[76:79],v[32:35],v[16:19]
s_waitcnt lgkmcnt(5)
buffer_store_dwordx4 v[60:63],v3,s[36:39],s73 offen offset:0 sc0 sc1
s_cmp_eq_u32 s53,s56
v_mfma_f32_16x16x32_bf16 v[12:15],a[80:83],v[36:39],v[12:15]
s_cselect_b32 s53,s57,s53
ds_read_b128 v[32:35],v1 offset:17408
s_cmp_eq_u32 s53,s58
v_mfma_f32_16x16x32_bf16 v[16:19],a[84:87],v[36:39],v[16:19]
s_cselect_b32 s53,s59,s53
s_cmp_ge_u32 s66,s59
s_cselect_b32 s86,s49,s50
v_mfma_f32_16x16x32_bf16 v[12:15],a[88:91],v[40:43],v[12:15]
ds_read_b128 v[36:39],v1 offset:18432
v_mfma_f32_16x16x32_bf16 v[16:19],a[92:95],v[40:43],v[16:19]
s_waitcnt lgkmcnt(5)
buffer_load_dwordx4 v0,s[36:39],s61 offen offset:0 sc1 lds
v_mfma_f32_16x16x32_bf16 v[12:15],a[96:99],v[44:47],v[12:15]
ds_read_b128 v[40:43],v1 offset:19456
v_mfma_f32_16x16x32_bf16 v[16:19],a[100:103],v[44:47],v[16:19]
v_mfma_f32_16x16x32_bf16 v[12:15],a[104:107],v[48:51],v[12:15]
ds_read_b128 v[44:47],v1 offset:20480
v_mfma_f32_16x16x32_bf16 v[16:19],a[108:111],v[48:51],v[16:19]
s_waitcnt lgkmcnt(5)
buffer_load_dwordx4 v0,s[36:39],s61 offen offset:1024 sc1 lds
v_mfma_f32_16x16x32_bf16 v[12:15],a[112:115],v[52:55],v[12:15]
ds_read_b128 v[48:51],v1 offset:21504
v_mfma_f32_16x16x32_bf16 v[16:19],a[116:119],v[52:55],v[16:19]
v_mfma_f32_16x16x32_bf16 v[12:15],a[120:123],v[56:59],v[12:15]
ds_read_b128 v[52:55],v1 offset:22528
v_mfma_f32_16x16x32_bf16 v[16:19],a[124:127],v[56:59],v[16:19]
s_waitcnt lgkmcnt(5)
buffer_load_dwordx4 v0,s[36:39],s61 offen offset:2048 sc1 lds
v_mfma_f32_16x16x32_bf16 v[12:15],a[128:131],v[28:31],v[12:15]
s_add_u32 s61,s61,24576
ds_read_b128 v[56:59],v1 offset:23552
s_add_u32 m0,s88,86016
v_mfma_f32_16x16x32_bf16 v[16:19],a[132:135],v[28:31],v[16:19]
v_mfma_f32_16x16x32_bf16 v[12:15],a[136:139],v[32:35],v[12:15]
ds_read_b128 v[28:31],v1 offset:24576
v_mfma_f32_16x16x32_bf16 v[16:19],a[140:143],v[32:35],v[16:19]
s_waitcnt lgkmcnt(5)
buffer_load_dwordx4 v[68:71],v9,s[36:39],s48 offen offset:0 sc1
v_mfma_f32_16x16x32_bf16 v[12:15],a[144:147],v[36:39],v[12:15]
ds_read_b128 v[32:35],v1 offset:25600
v_mfma_f32_16x16x32_bf16 v[16:19],a[148:151],v[36:39],v[16:19]
v_mfma_f32_16x16x32_bf16 v[12:15],a[152:155],v[40:43],v[12:15]
ds_read_b128 v[36:39],v1 offset:26624
v_mfma_f32_16x16x32_bf16 v[16:19],a[156:159],v[40:43],v[16:19]
s_waitcnt lgkmcnt(5)
v_mfma_f32_16x16x32_bf16 v[12:15],a[160:163],v[44:47],v[12:15]
s_waitcnt vmcnt(9)
ds_read_b128 v[40:43],v1 offset:27648
v_mfma_f32_16x16x32_bf16 v[16:19],a[164:167],v[44:47],v[16:19]
s_barrier
v_mfma_f32_16x16x32_bf16 v[12:15],a[168:171],v[48:51],v[12:15]
ds_read_b128 v[44:47],v1 offset:28672
v_mfma_f32_16x16x32_bf16 v[16:19],a[172:175],v[48:51],v[16:19]
s_waitcnt lgkmcnt(5)
v_mfma_f32_16x16x32_bf16 v[12:15],a[176:179],v[52:55],v[12:15]
ds_read_b128 v[48:51],v1 offset:29696
v_mfma_f32_16x16x32_bf16 v[16:19],a[180:183],v[52:55],v[16:19]
v_mfma_f32_16x16x32_bf16 v[12:15],a[184:187],v[56:59],v[12:15]
ds_read_b128 v[52:55],v1 offset:30720
v_mfma_f32_16x16x32_bf16 v[16:19],a[188:191],v[56:59],v[16:19]
s_waitcnt lgkmcnt(5)
buffer_load_dwordx4 v0,s[36:39],s61 offen offset:0 sc1 lds
v_mfma_f32_16x16x32_bf16 v[12:15],a[192:195],v[28:31],v[12:15]
ds_read_b128 v[56:59],v1 offset:31744
v_mfma_f32_16x16x32_bf16 v[16:19],a[196:199],v[28:31],v[16:19]
v_mfma_f32_16x16x32_bf16 v[12:15],a[200:203],v[32:35],v[12:15]
ds_read_b128 v[28:31],v1 offset:32768
v_mfma_f32_16x16x32_bf16 v[16:19],a[204:207],v[32:35],v[16:19]
s_waitcnt lgkmcnt(5)
v_mfma_f32_16x16x32_bf16 v[12:15],a[208:211],v[36:39],v[12:15]
buffer_load_dwordx4 v0,s[36:39],s61 offen offset:1024 sc1 lds
v_mfma_f32_16x16x32_bf16 v[16:19],a[212:215],v[36:39],v[16:19]
ds_read_b128 v[32:35],v1 offset:33792
v_mfma_f32_16x16x32_bf16 v[12:15],a[216:219],v[40:43],v[12:15]
ds_read_b128 v[36:39],v1 offset:34816
v_mfma_f32_16x16x32_bf16 v[16:19],a[220:223],v[40:43],v[16:19]
s_waitcnt lgkmcnt(5)
v_mfma_f32_16x16x32_bf16 v[12:15],a[224:227],v[44:47],v[12:15]
ds_read_b128 v[40:43],v1 offset:35840
v_mfma_f32_16x16x32_bf16 v[16:19],a[228:231],v[44:47],v[16:19]
buffer_load_dwordx4 v0,s[36:39],s61 offen offset:2048 sc1 lds
v_mfma_f32_16x16x32_bf16 v[12:15],a[232:235],v[48:51],v[12:15]
ds_read_b128 v[44:47],v1 offset:36864
v_mfma_f32_16x16x32_bf16 v[16:19],a[236:239],v[48:51],v[16:19]
s_waitcnt lgkmcnt(5)
v_mfma_f32_16x16x32_bf16 v[12:15],a[240:243],v[52:55],v[12:15]
ds_read_b128 v[48:51],v1 offset:37888
v_mfma_f32_16x16x32_bf16 v[16:19],a[244:247],v[52:55],v[16:19]
v_mfma_f32_16x16x32_bf16 v[12:15],a[248:251],v[56:59],v[12:15]
buffer_load_dwordx4 v0,s[36:39],s61 offen offset:3072 sc1 lds
v_mfma_f32_16x16x32_bf16 v[16:19],a[252:255],v[56:59],v[16:19]
s_waitcnt lgkmcnt(4)
ds_read_b128 v[52:55],v1 offset:38912
s_add_u32 s61,s61,4096
v_mfma_f32_16x16x32_bf16 v[12:15],v[108:111],v[28:31],v[12:15]
s_add_u32 m0,s88,90112
ds_read_b128 v[56:59],v1 offset:39936
v_mfma_f32_16x16x32_bf16 v[16:19],v[112:115],v[28:31],v[16:19]
v_mfma_f32_16x16x32_bf16 v[12:15],v[116:119],v[32:35],v[12:15]
ds_read_b128 v[28:31],v1 offset:40960
v_mfma_f32_16x16x32_bf16 v[16:19],v[120:123],v[32:35],v[16:19]
s_waitcnt lgkmcnt(5)
buffer_load_dwordx4 v0,s[36:39],s61 offen offset:0 sc1 lds
v_mfma_f32_16x16x32_bf16 v[12:15],v[124:127],v[36:39],v[12:15]
ds_read_b128 v[32:35],v1 offset:41984
v_mfma_f32_16x16x32_bf16 v[16:19],v[128:131],v[36:39],v[16:19]
v_mfma_f32_16x16x32_bf16 v[12:15],v[132:135],v[40:43],v[12:15]
ds_read_b128 v[36:39],v1 offset:43008
v_mfma_f32_16x16x32_bf16 v[16:19],v[136:139],v[40:43],v[16:19]
s_waitcnt lgkmcnt(5)
v_mfma_f32_16x16x32_bf16 v[12:15],v[140:143],v[44:47],v[12:15]
buffer_load_dwordx4 v0,s[36:39],s61 offen offset:1024 sc1 lds
v_mfma_f32_16x16x32_bf16 v[16:19],v[144:147],v[44:47],v[16:19]
ds_read_b128 v[40:43],v1 offset:44032
v_mfma_f32_16x16x32_bf16 v[12:15],v[148:151],v[48:51],v[12:15]
ds_read_b128 v[44:47],v1 offset:45056
v_mfma_f32_16x16x32_bf16 v[16:19],v[152:155],v[48:51],v[16:19]
s_waitcnt lgkmcnt(5)
v_mfma_f32_16x16x32_bf16 v[12:15],v[156:159],v[52:55],v[12:15]
ds_read_b128 v[48:51],v1 offset:46080
v_mfma_f32_16x16x32_bf16 v[16:19],v[160:163],v[52:55],v[16:19]
buffer_load_dwordx4 v0,s[36:39],s61 offen offset:2048 sc1 lds
v_mfma_f32_16x16x32_bf16 v[12:15],v[164:167],v[56:59],v[12:15]
ds_read_b128 v[52:55],v1 offset:47104
v_mfma_f32_16x16x32_bf16 v[16:19],v[168:171],v[56:59],v[16:19]
s_waitcnt lgkmcnt(5)
v_mfma_f32_16x16x32_bf16 v[12:15],v[172:175],v[28:31],v[12:15]
ds_read_b128 v[56:59],v1 offset:48128
v_mfma_f32_16x16x32_bf16 v[16:19],v[176:179],v[28:31],v[16:19]
ds_read_b128 v[88:91],v10 offset:0
v_mfma_f32_16x16x32_bf16 v[12:15],v[180:183],v[32:35],v[12:15]
ds_read_b128 v[28:31],v1 offset:49152
v_mfma_f32_16x16x32_bf16 v[16:19],v[184:187],v[32:35],v[16:19]
s_waitcnt lgkmcnt(6)
ds_read_b128 v[92:95],v10 offset:1024
v_mfma_f32_16x16x32_bf16 v[12:15],v[188:191],v[36:39],v[12:15]
ds_read_b128 v[32:35],v1 offset:50176)"
R"(
v_mfma_f32_16x16x32_bf16 v[16:19],v[192:195],v[36:39],v[16:19]
ds_read_b128 v[96:99],v10 offset:2048
v_mfma_f32_16x16x32_bf16 v[12:15],v[196:199],v[40:43],v[12:15]
ds_read_b128 v[36:39],v1 offset:51200
v_mfma_f32_16x16x32_bf16 v[16:19],v[200:203],v[40:43],v[16:19]
s_waitcnt lgkmcnt(5)
ds_read_b128 v[76:79],v10 offset:3072
v_mfma_f32_16x16x32_bf16 v[12:15],v[204:207],v[44:47],v[12:15]
ds_read_b128 v[40:43],v1 offset:52224
v_mfma_f32_16x16x32_bf16 v[16:19],v[208:211],v[44:47],v[16:19]
ds_read_b128 v[80:83],v10 offset:4096
v_mfma_f32_16x16x32_bf16 v[12:15],v[212:215],v[48:51],v[12:15]
ds_read_b128 v[44:47],v1 offset:53248
v_mfma_f32_16x16x32_bf16 v[16:19],v[88:91],v[48:51],v[16:19]
s_waitcnt lgkmcnt(5)
ds_read_b128 v[84:87],v10 offset:5120
v_mfma_f32_16x16x32_bf16 v[12:15],v[216:219],v[52:55],v[12:15]
ds_read_b128 v[48:51],v1 offset:54272
v_mfma_f32_16x16x32_bf16 v[16:19],v[92:95],v[52:55],v[16:19]
ds_read_b128 v[88:91],v10 offset:6144
v_mfma_f32_16x16x32_bf16 v[12:15],v[220:223],v[56:59],v[12:15]
ds_read_b128 v[52:55],v1 offset:55296
v_mfma_f32_16x16x32_bf16 v[16:19],v[96:99],v[56:59],v[16:19]
s_waitcnt lgkmcnt(5)
ds_read_b128 v[92:95],v10 offset:7168
v_mfma_f32_16x16x32_bf16 v[12:15],v[224:227],v[28:31],v[12:15]
s_waitcnt vmcnt(7)
ds_read_b128 v[56:59],v1 offset:56320
ds_read_b128 v[96:99],v10 offset:8192
v_mfma_f32_16x16x32_bf16 v[16:19],v[76:79],v[28:31],v[16:19]
s_barrier
v_mfma_f32_16x16x32_bf16 v[12:15],v[228:231],v[32:35],v[12:15]
ds_read_b128 v[28:31],v2 offset:0
ds_read_b32 v11,v8 offset:3072
v_mfma_f32_16x16x32_bf16 v[16:19],v[80:83],v[32:35],v[16:19]
s_waitcnt lgkmcnt(6)
ds_read_b128 v[76:79],v10 offset:9216
v_mfma_f32_16x16x32_bf16 v[12:15],v[232:235],v[36:39],v[12:15]
ds_read_b128 v[32:35],v2 offset:1024
v_mfma_f32_16x16x32_bf16 v[16:19],v[84:87],v[36:39],v[16:19]
ds_read_b128 v[80:83],v10 offset:10240
v_mfma_f32_16x16x32_bf16 v[12:15],v[236:239],v[40:43],v[12:15]
s_waitcnt lgkmcnt(3)
ds_read_b128 v[36:39],v2 offset:2048
v_mfma_f32_16x16x32_bf16 v[16:19],v[88:91],v[40:43],v[16:19]
v_cmp_eq_u32 vcc,s60,v11
v_mfma_f32_16x16x32_bf16 v[12:15],v[240:243],v[44:47],v[12:15]
s_cmp_eq_u32 vcc_lo,-1
ds_read_b128 v[40:43],v2 offset:3072
s_cselect_b32 s67,s86,s59
v_mfma_f32_16x16x32_bf16 v[16:19],v[92:95],v[44:47],v[16:19]
s_mul_i32 s61,s67,7
s_lshr_b32 s61,s61,1
s_cmp_ge_u32 s66,s59
v_mfma_f32_16x16x32_bf16 v[12:15],v[244:247],v[48:51],v[12:15]
ds_read_b128 v[44:47],v2 offset:4096
v_mfma_f32_16x16x32_bf16 v[16:19],v[96:99],v[48:51],v[16:19]
s_waitcnt lgkmcnt(3)
v_mfma_f32_16x16x32_bf16 v[12:15],v[248:251],v[52:55],v[12:15]
ds_read_b128 v[48:51],v2 offset:5120
v_mfma_f32_16x16x32_bf16 v[16:19],v[76:79],v[52:55],v[16:19]
v_mfma_f32_16x16x32_bf16 v[12:15],v[252:255],v[56:59],v[12:15]
ds_read_b128 v[52:55],v2 offset:6144
v_mfma_f32_16x16x32_bf16 v[16:19],v[80:83],v[56:59],v[16:19]
s_cbranch_scc1 Lg4_slow1_%=
Lg4_d1w2_%=:
v_mfma_f32_16x16x32_bf16 v[20:23],a[0:3],v[28:31],0
s_add_u32 m0,s88,0
ds_read_b128 v[56:59],v2 offset:7168
v_lshlrev_b32 v60,16,v68
v_mfma_f32_16x16x32_bf16 v[24:27],a[4:7],v[28:31],0
s_max_u32 s73,s48,s65
v_and_b32 v61,0xffff0000,v68
v_lshlrev_b32 v62,16,v69
v_mfma_f32_16x16x32_bf16 v[20:23],a[8:11],v[32:35],v[20:23]
s_mov_b32 s65,0
ds_read_b128 v[28:31],v2 offset:8192
v_and_b32 v63,0xffff0000,v69
v_mfma_f32_16x16x32_bf16 v[24:27],a[12:15],v[32:35],v[24:27]
s_waitcnt lgkmcnt(5)
buffer_load_dwordx4 v0,s[36:39],s61 offen offset:0 sc1 lds
v_mfma_f32_16x16x32_bf16 v[20:23],a[16:19],v[36:39],v[20:23]
s_mov_b64 s[44:45],s[46:47]
ds_read_b128 v[32:35],v2 offset:9216
v_lshlrev_b32 v64,16,v70
v_mfma_f32_16x16x32_bf16 v[24:27],a[20:23],v[36:39],v[24:27]
s_mov_b64 s[46:47],s[48:49]
v_and_b32 v65,0xffff0000,v70
v_lshlrev_b32 v66,16,v71
v_mfma_f32_16x16x32_bf16 v[20:23],a[24:27],v[40:43],v[20:23]
ds_read_b128 v[36:39],v2 offset:10240
v_and_b32 v67,0xffff0000,v71
v_mfma_f32_16x16x32_bf16 v[24:27],a[28:31],v[40:43],v[24:27]
s_waitcnt lgkmcnt(5)
buffer_load_dwordx4 v0,s[36:39],s61 offen offset:1024 sc1 lds
v_mfma_f32_16x16x32_bf16 v[20:23],a[32:35],v[44:47],v[20:23]
ds_read_b128 v[40:43],v2 offset:11264
v_add_f32 v12,v12,v60
v_mfma_f32_16x16x32_bf16 v[24:27],a[36:39],v[44:47],v[24:27]
v_add_f32 v13,v13,v61
v_add_f32 v14,v14,v62
v_mfma_f32_16x16x32_bf16 v[20:23],a[40:43],v[48:51],v[20:23]
ds_read_b128 v[44:47],v2 offset:12288
v_add_f32 v15,v15,v63
v_mfma_f32_16x16x32_bf16 v[24:27],a[44:47],v[48:51],v[24:27]
s_waitcnt lgkmcnt(5)
buffer_load_dwordx4 v0,s[36:39],s61 offen offset:2048 sc1 lds
v_mfma_f32_16x16x32_bf16 v[20:23],a[48:51],v[52:55],v[20:23]
ds_read_b128 v[48:51],v2 offset:13312
v_add_f32 v16,v16,v64
v_mfma_f32_16x16x32_bf16 v[24:27],a[52:55],v[52:55],v[24:27]
v_add_f32 v17,v17,v65
v_add_f32 v18,v18,v66
v_mfma_f32_16x16x32_bf16 v[20:23],a[56:59],v[56:59],v[20:23]
ds_read_b128 v[52:55],v2 offset:14336
v_add_f32 v19,v19,v67
v_mfma_f32_16x16x32_bf16 v[24:27],a[60:63],v[56:59],v[24:27]
s_waitcnt lgkmcnt(5)
buffer_load_dwordx4 v0,s[36:39],s61 offen offset:3072 sc1 lds
v_mfma_f32_16x16x32_bf16 v[20:23],a[64:67],v[28:31],v[20:23]
s_add_u32 s61,s61,4096
ds_read_b128 v[56:59],v2 offset:15360
v_cvt_pk_bf16_f32 v60,v12,v13
v_mfma_f32_16x16x32_bf16 v[24:27],a[68:71],v[28:31],v[24:27]
s_add_u32 m0,s88,4096
v_cvt_pk_bf16_f32 v61,v14,v15
v_cvt_pk_bf16_f32 v62,v16,v17
v_mfma_f32_16x16x32_bf16 v[20:23],a[72:75],v[32:35],v[20:23]
s_add_u32 s74,s53,s55
ds_read_b128 v[28:31],v2 offset:16384
v_cvt_pk_bf16_f32 v63,v18,v19
v_mfma_f32_16x16x32_bf16 v[24:27],a[76:79],v[32:35],v[24:27]
s_waitcnt lgkmcnt(5)
buffer_store_dwordx4 v[60:63],v3,s[36:39],s73 offen offset:0 sc0 sc1
s_cmp_eq_u32 s74,s56
v_mfma_f32_16x16x32_bf16 v[20:23],a[80:83],v[36:39],v[20:23]
s_cselect_b32 s74,s57,s74
ds_read_b128 v[32:35],v2 offset:17408
s_cmp_eq_u32 s74,s58
v_mfma_f32_16x16x32_bf16 v[24:27],a[84:87],v[36:39],v[24:27]
s_cselect_b32 s74,s59,s74
s_cmp_ge_u32 s67,s59
s_cselect_b32 s86,s50,s51
v_mfma_f32_16x16x32_bf16 v[20:23],a[88:91],v[40:43],v[20:23]
ds_read_b128 v[36:39],v2 offset:18432
v_mfma_f32_16x16x32_bf16 v[24:27],a[92:95],v[40:43],v[24:27]
s_waitcnt lgkmcnt(5)
buffer_load_dwordx4 v0,s[36:39],s61 offen offset:0 sc1 lds
v_mfma_f32_16x16x32_bf16 v[20:23],a[96:99],v[44:47],v[20:23]
ds_read_b128 v[40:43],v2 offset:19456
v_mfma_f32_16x16x32_bf16 v[24:27],a[100:103],v[44:47],v[24:27]
v_mfma_f32_16x16x32_bf16 v[20:23],a[104:107],v[48:51],v[20:23]
ds_read_b128 v[44:47],v2 offset:20480
v_mfma_f32_16x16x32_bf16 v[24:27],a[108:111],v[48:51],v[24:27]
s_waitcnt lgkmcnt(5)
buffer_load_dwordx4 v0,s[36:39],s61 offen offset:1024 sc1 lds
v_mfma_f32_16x16x32_bf16 v[20:23],a[112:115],v[52:55],v[20:23]
ds_read_b128 v[48:51],v2 offset:21504
v_mfma_f32_16x16x32_bf16 v[24:27],a[116:119],v[52:55],v[24:27]
v_mfma_f32_16x16x32_bf16 v[20:23],a[120:123],v[56:59],v[20:23]
ds_read_b128 v[52:55],v2 offset:22528
v_mfma_f32_16x16x32_bf16 v[24:27],a[124:127],v[56:59],v[24:27]
s_waitcnt lgkmcnt(5)
buffer_load_dwordx4 v0,s[36:39],s61 offen offset:2048 sc1 lds
v_mfma_f32_16x16x32_bf16 v[20:23],a[128:131],v[28:31],v[20:23]
s_add_u32 s61,s61,24576
ds_read_b128 v[56:59],v2 offset:23552
s_add_u32 m0,s88,28672
v_mfma_f32_16x16x32_bf16 v[24:27],a[132:135],v[28:31],v[24:27]
v_mfma_f32_16x16x32_bf16 v[20:23],a[136:139],v[32:35],v[20:23]
ds_read_b128 v[28:31],v2 offset:24576
v_mfma_f32_16x16x32_bf16 v[24:27],a[140:143],v[32:35],v[24:27]
s_waitcnt lgkmcnt(5)
buffer_load_dwordx4 v[72:75],v9,s[36:39],s49 offen offset:0 sc1
v_mfma_f32_16x16x32_bf16 v[20:23],a[144:147],v[36:39],v[20:23]
s_mov_b64 s[48:49],s[50:51]
ds_read_b128 v[32:35],v2 offset:25600
s_mov_b64 s[50:51],s[52:53]
v_mfma_f32_16x16x32_bf16 v[24:27],a[148:151],v[36:39],v[24:27]
s_mov_b32 s52,s74
v_mfma_f32_16x16x32_bf16 v[20:23],a[152:155],v[40:43],v[20:23]
ds_read_b128 v[36:39],v2 offset:26624
v_mfma_f32_16x16x32_bf16 v[24:27],a[156:159],v[40:43],v[24:27]
s_waitcnt lgkmcnt(5)
v_mfma_f32_16x16x32_bf16 v[20:23],a[160:163],v[44:47],v[20:23]
s_waitcnt vmcnt(9)
ds_read_b128 v[40:43],v2 offset:27648
v_mfma_f32_16x16x32_bf16 v[24:27],a[164:167],v[44:47],v[24:27]
s_barrier
v_mfma_f32_16x16x32_bf16 v[20:23],a[168:171],v[48:51],v[20:23]
ds_read_b128 v[44:47],v2 offset:28672
v_mfma_f32_16x16x32_bf16 v[24:27],a[172:175],v[48:51],v[24:27]
s_waitcnt lgkmcnt(5)
v_mfma_f32_16x16x32_bf16 v[20:23],a[176:179],v[52:55],v[20:23]
ds_read_b128 v[48:51],v2 offset:29696
v_mfma_f32_16x16x32_bf16 v[24:27],a[180:183],v[52:55],v[24:27]
v_mfma_f32_16x16x32_bf16 v[20:23],a[184:187],v[56:59],v[20:23]
ds_read_b128 v[52:55],v2 offset:30720
v_mfma_f32_16x16x32_bf16 v[24:27],a[188:191],v[56:59],v[24:27]
s_waitcnt lgkmcnt(5))"
R"(
buffer_load_dwordx4 v0,s[36:39],s61 offen offset:0 sc1 lds
v_mfma_f32_16x16x32_bf16 v[20:23],a[192:195],v[28:31],v[20:23]
ds_read_b128 v[56:59],v2 offset:31744
v_mfma_f32_16x16x32_bf16 v[24:27],a[196:199],v[28:31],v[24:27]
v_mfma_f32_16x16x32_bf16 v[20:23],a[200:203],v[32:35],v[20:23]
ds_read_b128 v[28:31],v2 offset:32768
v_mfma_f32_16x16x32_bf16 v[24:27],a[204:207],v[32:35],v[24:27]
s_waitcnt lgkmcnt(5)
v_mfma_f32_16x16x32_bf16 v[20:23],a[208:211],v[36:39],v[20:23]
buffer_load_dwordx4 v0,s[36:39],s61 offen offset:1024 sc1 lds
v_mfma_f32_16x16x32_bf16 v[24:27],a[212:215],v[36:39],v[24:27]
ds_read_b128 v[32:35],v2 offset:33792
v_mfma_f32_16x16x32_bf16 v[20:23],a[216:219],v[40:43],v[20:23]
ds_read_b128 v[36:39],v2 offset:34816
v_mfma_f32_16x16x32_bf16 v[24:27],a[220:223],v[40:43],v[24:27]
s_waitcnt lgkmcnt(5)
v_mfma_f32_16x16x32_bf16 v[20:23],a[224:227],v[44:47],v[20:23]
ds_read_b128 v[40:43],v2 offset:35840
v_mfma_f32_16x16x32_bf16 v[24:27],a[228:231],v[44:47],v[24:27]
buffer_load_dwordx4 v0,s[36:39],s61 offen offset:2048 sc1 lds
v_mfma_f32_16x16x32_bf16 v[20:23],a[232:235],v[48:51],v[20:23]
ds_read_b128 v[44:47],v2 offset:36864
v_mfma_f32_16x16x32_bf16 v[24:27],a[236:239],v[48:51],v[24:27]
s_waitcnt lgkmcnt(5)
v_mfma_f32_16x16x32_bf16 v[20:23],a[240:243],v[52:55],v[20:23]
ds_read_b128 v[48:51],v2 offset:37888
v_mfma_f32_16x16x32_bf16 v[24:27],a[244:247],v[52:55],v[24:27]
v_mfma_f32_16x16x32_bf16 v[20:23],a[248:251],v[56:59],v[20:23]
buffer_load_dwordx4 v0,s[36:39],s61 offen offset:3072 sc1 lds
v_mfma_f32_16x16x32_bf16 v[24:27],a[252:255],v[56:59],v[24:27]
s_waitcnt lgkmcnt(4)
ds_read_b128 v[52:55],v2 offset:38912
s_add_u32 s61,s61,4096
v_mfma_f32_16x16x32_bf16 v[20:23],v[108:111],v[28:31],v[20:23]
s_add_u32 m0,s88,32768
ds_read_b128 v[56:59],v2 offset:39936
v_mfma_f32_16x16x32_bf16 v[24:27],v[112:115],v[28:31],v[24:27]
v_mfma_f32_16x16x32_bf16 v[20:23],v[116:119],v[32:35],v[20:23]
ds_read_b128 v[28:31],v2 offset:40960
v_mfma_f32_16x16x32_bf16 v[24:27],v[120:123],v[32:35],v[24:27]
s_waitcnt lgkmcnt(5)
buffer_load_dwordx4 v0,s[36:39],s61 offen offset:0 sc1 lds
v_mfma_f32_16x16x32_bf16 v[20:23],v[124:127],v[36:39],v[20:23]
ds_read_b128 v[32:35],v2 offset:41984
v_mfma_f32_16x16x32_bf16 v[24:27],v[128:131],v[36:39],v[24:27]
v_mfma_f32_16x16x32_bf16 v[20:23],v[132:135],v[40:43],v[20:23]
ds_read_b128 v[36:39],v2 offset:43008
v_mfma_f32_16x16x32_bf16 v[24:27],v[136:139],v[40:43],v[24:27]
s_waitcnt lgkmcnt(5)
v_mfma_f32_16x16x32_bf16 v[20:23],v[140:143],v[44:47],v[20:23]
buffer_load_dwordx4 v0,s[36:39],s61 offen offset:1024 sc1 lds
v_mfma_f32_16x16x32_bf16 v[24:27],v[144:147],v[44:47],v[24:27]
ds_read_b128 v[40:43],v2 offset:44032
v_mfma_f32_16x16x32_bf16 v[20:23],v[148:151],v[48:51],v[20:23]
ds_read_b128 v[44:47],v2 offset:45056
v_mfma_f32_16x16x32_bf16 v[24:27],v[152:155],v[48:51],v[24:27]
s_waitcnt lgkmcnt(5)
v_mfma_f32_16x16x32_bf16 v[20:23],v[156:159],v[52:55],v[20:23]
ds_read_b128 v[48:51],v2 offset:46080
v_mfma_f32_16x16x32_bf16 v[24:27],v[160:163],v[52:55],v[24:27]
buffer_load_dwordx4 v0,s[36:39],s61 offen offset:2048 sc1 lds
v_mfma_f32_16x16x32_bf16 v[20:23],v[164:167],v[56:59],v[20:23]
ds_read_b128 v[52:55],v2 offset:47104
v_mfma_f32_16x16x32_bf16 v[24:27],v[168:171],v[56:59],v[24:27]
s_waitcnt lgkmcnt(5)
v_mfma_f32_16x16x32_bf16 v[20:23],v[172:175],v[28:31],v[20:23]
ds_read_b128 v[56:59],v2 offset:48128
v_mfma_f32_16x16x32_bf16 v[24:27],v[176:179],v[28:31],v[24:27]
ds_read_b128 v[88:91],v10 offset:0
v_mfma_f32_16x16x32_bf16 v[20:23],v[180:183],v[32:35],v[20:23]
ds_read_b128 v[28:31],v2 offset:49152
v_mfma_f32_16x16x32_bf16 v[24:27],v[184:187],v[32:35],v[24:27]
s_waitcnt lgkmcnt(6)
ds_read_b128 v[92:95],v10 offset:1024
v_mfma_f32_16x16x32_bf16 v[20:23],v[188:191],v[36:39],v[20:23]
ds_read_b128 v[32:35],v2 offset:50176
v_mfma_f32_16x16x32_bf16 v[24:27],v[192:195],v[36:39],v[24:27]
ds_read_b128 v[96:99],v10 offset:2048
v_mfma_f32_16x16x32_bf16 v[20:23],v[196:199],v[40:43],v[20:23]
ds_read_b128 v[36:39],v2 offset:51200
v_mfma_f32_16x16x32_bf16 v[24:27],v[200:203],v[40:43],v[24:27]
s_waitcnt lgkmcnt(5)
ds_read_b128 v[76:79],v10 offset:3072
v_mfma_f32_16x16x32_bf16 v[20:23],v[204:207],v[44:47],v[20:23]
ds_read_b128 v[40:43],v2 offset:52224
v_mfma_f32_16x16x32_bf16 v[24:27],v[208:211],v[44:47],v[24:27]
ds_read_b128 v[80:83],v10 offset:4096
v_mfma_f32_16x16x32_bf16 v[20:23],v[212:215],v[48:51],v[20:23]
ds_read_b128 v[44:47],v2 offset:53248
v_mfma_f32_16x16x32_bf16 v[24:27],v[88:91],v[48:51],v[24:27]
s_waitcnt lgkmcnt(5)
ds_read_b128 v[84:87],v10 offset:5120
v_mfma_f32_16x16x32_bf16 v[20:23],v[216:219],v[52:55],v[20:23]
ds_read_b128 v[48:51],v2 offset:54272
v_mfma_f32_16x16x32_bf16 v[24:27],v[92:95],v[52:55],v[24:27]
ds_read_b128 v[88:91],v10 offset:6144
v_mfma_f32_16x16x32_bf16 v[20:23],v[220:223],v[56:59],v[20:23]
ds_read_b128 v[52:55],v2 offset:55296
v_mfma_f32_16x16x32_bf16 v[24:27],v[96:99],v[56:59],v[24:27]
s_waitcnt lgkmcnt(5)
ds_read_b128 v[92:95],v10 offset:7168
v_mfma_f32_16x16x32_bf16 v[20:23],v[224:227],v[28:31],v[20:23]
s_waitcnt vmcnt(7)
ds_read_b128 v[56:59],v2 offset:56320
ds_read_b128 v[96:99],v10 offset:8192
v_mfma_f32_16x16x32_bf16 v[24:27],v[76:79],v[28:31],v[24:27]
s_barrier
v_mfma_f32_16x16x32_bf16 v[20:23],v[228:231],v[32:35],v[20:23]
ds_read_b128 v[28:31],v1 offset:0
ds_read_b32 v11,v8 offset:3072
v_mfma_f32_16x16x32_bf16 v[24:27],v[80:83],v[32:35],v[24:27]
s_waitcnt lgkmcnt(6)
ds_read_b128 v[76:79],v10 offset:9216
v_mfma_f32_16x16x32_bf16 v[20:23],v[232:235],v[36:39],v[20:23]
ds_read_b128 v[32:35],v1 offset:1024
v_mfma_f32_16x16x32_bf16 v[24:27],v[84:87],v[36:39],v[24:27]
ds_read_b128 v[80:83],v10 offset:10240
v_mfma_f32_16x16x32_bf16 v[20:23],v[236:239],v[40:43],v[20:23]
s_waitcnt lgkmcnt(3)
ds_read_b128 v[36:39],v1 offset:2048
v_mfma_f32_16x16x32_bf16 v[24:27],v[88:91],v[40:43],v[24:27]
v_cmp_eq_u32 vcc,s60,v11
v_mfma_f32_16x16x32_bf16 v[20:23],v[240:243],v[44:47],v[20:23]
s_cmp_eq_u32 vcc_lo,-1
ds_read_b128 v[40:43],v1 offset:3072
s_cselect_b32 s66,s86,s59
v_mfma_f32_16x16x32_bf16 v[24:27],v[92:95],v[44:47],v[24:27]
s_mul_i32 s61,s66,7
s_lshr_b32 s61,s61,1
s_cmp_lt_u32 s67,s59
v_mfma_f32_16x16x32_bf16 v[20:23],v[244:247],v[48:51],v[20:23]
ds_read_b128 v[44:47],v1 offset:4096
v_mfma_f32_16x16x32_bf16 v[24:27],v[96:99],v[48:51],v[24:27]
s_waitcnt lgkmcnt(3)
v_mfma_f32_16x16x32_bf16 v[20:23],v[248:251],v[52:55],v[20:23]
ds_read_b128 v[48:51],v1 offset:5120
v_mfma_f32_16x16x32_bf16 v[24:27],v[76:79],v[52:55],v[24:27]
v_mfma_f32_16x16x32_bf16 v[20:23],v[252:255],v[56:59],v[20:23]
ds_read_b128 v[52:55],v1 offset:6144
v_mfma_f32_16x16x32_bf16 v[24:27],v[80:83],v[56:59],v[24:27]
s_cbranch_scc1 Lg4_d0w2_%=
s_branch Lg4_slow0_%=
Lg4_d0w3_%=:
v_mfma_f32_16x16x32_bf16 v[12:15],a[0:3],v[28:31],0
s_add_u32 m0,s88,57344
ds_read_b128 v[56:59],v1 offset:7168
v_lshlrev_b32 v60,16,v72
v_mfma_f32_16x16x32_bf16 v[16:19],a[4:7],v[28:31],0
s_max_u32 s73,s47,s65
v_and_b32 v61,0xffff0000,v72
v_lshlrev_b32 v62,16,v73
v_mfma_f32_16x16x32_bf16 v[12:15],a[8:11],v[32:35],v[12:15]
s_mov_b32 s65,0
ds_read_b128 v[28:31],v1 offset:8192
v_and_b32 v63,0xffff0000,v73
v_mfma_f32_16x16x32_bf16 v[16:19],a[12:15],v[32:35],v[16:19]
s_waitcnt lgkmcnt(5)
v_lshlrev_b32 v64,16,v74
v_and_b32 v65,0xffff0000,v74
v_mfma_f32_16x16x32_bf16 v[12:15],a[16:19],v[36:39],v[12:15]
buffer_load_dwordx4 v0,s[36:39],s61 offen offset:0 sc1 lds
v_mfma_f32_16x16x32_bf16 v[16:19],a[20:23],v[36:39],v[16:19]
ds_read_b128 v[32:35],v1 offset:9216
v_lshlrev_b32 v66,16,v75
v_mfma_f32_16x16x32_bf16 v[12:15],a[24:27],v[40:43],v[12:15]
ds_read_b128 v[36:39],v1 offset:10240
v_and_b32 v67,0xffff0000,v75
v_mfma_f32_16x16x32_bf16 v[16:19],a[28:31],v[40:43],v[16:19]
s_waitcnt lgkmcnt(5)
v_add_f32 v20,v20,v60
v_add_f32 v21,v21,v61
v_mfma_f32_16x16x32_bf16 v[12:15],a[32:35],v[44:47],v[12:15]
buffer_load_dwordx4 v0,s[36:39],s61 offen offset:1024 sc1 lds
v_mfma_f32_16x16x32_bf16 v[16:19],a[36:39],v[44:47],v[16:19]
ds_read_b128 v[40:43],v1 offset:11264
v_add_f32 v22,v22,v62
v_mfma_f32_16x16x32_bf16 v[12:15],a[40:43],v[48:51],v[12:15]
ds_read_b128 v[44:47],v1 offset:12288
v_add_f32 v23,v23,v63
v_mfma_f32_16x16x32_bf16 v[16:19],a[44:47],v[48:51],v[16:19]
s_waitcnt lgkmcnt(5)
v_add_f32 v24,v24,v64
v_add_f32 v25,v25,v65
v_mfma_f32_16x16x32_bf16 v[12:15],a[48:51],v[52:55],v[12:15]
buffer_load_dwordx4 v0,s[36:39],s61 offen offset:2048 sc1 lds
v_mfma_f32_16x16x32_bf16 v[16:19],a[52:55],v[52:55],v[16:19]
ds_read_b128 v[48:51],v1 offset:13312
v_add_f32 v26,v26,v66
v_mfma_f32_16x16x32_bf16 v[12:15],a[56:59],v[56:59],v[12:15]
ds_read_b128 v[52:55],v1 offset:14336
v_add_f32 v27,v27,v67
v_mfma_f32_16x16x32_bf16 v[16:19],a[60:63],v[56:59],v[16:19]
s_waitcnt lgkmcnt(5)
v_cvt_pk_bf16_f32 v60,v20,v21
v_cvt_pk_bf16_f32 v61,v22,v23
v_mfma_f32_16x16x32_bf16 v[12:15],a[64:67],v[28:31],v[12:15]
buffer_load_dwordx4 v0,s[36:39],s61 offen offset:3072 sc1 lds
v_mfma_f32_16x16x32_bf16 v[16:19],a[68:71],v[28:31],v[16:19]
s_add_u32 s61,s61,4096
ds_read_b128 v[56:59],v1 offset:15360)"
R"(
v_cvt_pk_bf16_f32 v62,v24,v25
v_mfma_f32_16x16x32_bf16 v[12:15],a[72:75],v[32:35],v[12:15]
s_add_u32 m0,s88,61440
ds_read_b128 v[28:31],v1 offset:16384
v_cvt_pk_bf16_f32 v63,v26,v27
v_mfma_f32_16x16x32_bf16 v[16:19],a[76:79],v[32:35],v[16:19]
s_waitcnt lgkmcnt(5)
s_add_u32 s53,s52,s55
s_cmp_eq_u32 s53,s56
v_mfma_f32_16x16x32_bf16 v[12:15],a[80:83],v[36:39],v[12:15]
buffer_store_dwordx4 v[60:63],v3,s[36:39],s73 offen offset:0 sc0 sc1
ds_read_b128 v[32:35],v1 offset:17408
v_mfma_f32_16x16x32_bf16 v[16:19],a[84:87],v[36:39],v[16:19]
s_cselect_b32 s53,s57,s53
s_cmp_eq_u32 s53,s58
s_cselect_b32 s53,s59,s53
v_mfma_f32_16x16x32_bf16 v[12:15],a[88:91],v[40:43],v[12:15]
s_cmp_ge_u32 s66,s59
ds_read_b128 v[36:39],v1 offset:18432
s_cselect_b32 s86,s49,s50
v_mfma_f32_16x16x32_bf16 v[16:19],a[92:95],v[40:43],v[16:19]
s_waitcnt lgkmcnt(5)
s_lshr_b32 s70,s47,4
v_mfma_f32_16x16x32_bf16 v[12:15],a[96:99],v[44:47],v[12:15]
buffer_load_dwordx4 v0,s[36:39],s61 offen offset:0 sc1 lds
v_mfma_f32_16x16x32_bf16 v[16:19],a[100:103],v[44:47],v[16:19]
ds_read_b128 v[40:43],v1 offset:19456
v_mfma_f32_16x16x32_bf16 v[12:15],a[104:107],v[48:51],v[12:15]
ds_read_b128 v[44:47],v1 offset:20480
v_mfma_f32_16x16x32_bf16 v[16:19],a[108:111],v[48:51],v[16:19]
s_waitcnt lgkmcnt(5)
v_mfma_f32_16x16x32_bf16 v[12:15],a[112:115],v[52:55],v[12:15]
buffer_load_dwordx4 v0,s[36:39],s61 offen offset:1024 sc1 lds
v_mfma_f32_16x16x32_bf16 v[16:19],a[116:119],v[52:55],v[16:19]
ds_read_b128 v[48:51],v1 offset:21504
v_mfma_f32_16x16x32_bf16 v[12:15],a[120:123],v[56:59],v[12:15]
ds_read_b128 v[52:55],v1 offset:22528
v_mfma_f32_16x16x32_bf16 v[16:19],a[124:127],v[56:59],v[16:19]
s_waitcnt lgkmcnt(5)
v_mfma_f32_16x16x32_bf16 v[12:15],a[128:131],v[28:31],v[12:15]
buffer_load_dwordx4 v0,s[36:39],s61 offen offset:2048 sc1 lds
v_mfma_f32_16x16x32_bf16 v[16:19],a[132:135],v[28:31],v[16:19]
s_add_u32 s61,s61,24576
ds_read_b128 v[56:59],v1 offset:23552
s_add_u32 m0,s88,86016
v_mfma_f32_16x16x32_bf16 v[12:15],a[136:139],v[32:35],v[12:15]
ds_read_b128 v[28:31],v1 offset:24576
v_mfma_f32_16x16x32_bf16 v[16:19],a[140:143],v[32:35],v[16:19]
s_waitcnt lgkmcnt(5)
v_mfma_f32_16x16x32_bf16 v[12:15],a[144:147],v[36:39],v[12:15]
buffer_load_dwordx4 v[68:71],v9,s[36:39],s48 offen offset:0 sc1
v_mfma_f32_16x16x32_bf16 v[16:19],a[148:151],v[36:39],v[16:19]
ds_read_b128 v[32:35],v1 offset:25600
v_mfma_f32_16x16x32_bf16 v[12:15],a[152:155],v[40:43],v[12:15]
ds_read_b128 v[36:39],v1 offset:26624
v_mfma_f32_16x16x32_bf16 v[16:19],a[156:159],v[40:43],v[16:19]
s_waitcnt lgkmcnt(5)
v_mfma_f32_16x16x32_bf16 v[12:15],a[160:163],v[44:47],v[12:15]
s_waitcnt vmcnt(10)
ds_read_b128 v[40:43],v1 offset:27648
v_mfma_f32_16x16x32_bf16 v[16:19],a[164:167],v[44:47],v[16:19]
s_barrier
v_mfma_f32_16x16x32_bf16 v[12:15],a[168:171],v[48:51],v[12:15]
ds_read_b128 v[44:47],v1 offset:28672
v_mfma_f32_16x16x32_bf16 v[16:19],a[172:175],v[48:51],v[16:19]
s_waitcnt lgkmcnt(5)
v_mfma_f32_16x16x32_bf16 v[12:15],a[176:179],v[52:55],v[12:15]
ds_read_b128 v[48:51],v1 offset:29696
v_mfma_f32_16x16x32_bf16 v[16:19],a[180:183],v[52:55],v[16:19]
v_mfma_f32_16x16x32_bf16 v[12:15],a[184:187],v[56:59],v[12:15]
ds_read_b128 v[52:55],v1 offset:30720
v_mfma_f32_16x16x32_bf16 v[16:19],a[188:191],v[56:59],v[16:19]
s_waitcnt lgkmcnt(5)
v_mfma_f32_16x16x32_bf16 v[12:15],a[192:195],v[28:31],v[12:15]
buffer_load_dwordx4 v0,s[36:39],s61 offen offset:0 sc1 lds
v_mfma_f32_16x16x32_bf16 v[16:19],a[196:199],v[28:31],v[16:19]
ds_read_b128 v[56:59],v1 offset:31744
v_mfma_f32_16x16x32_bf16 v[12:15],a[200:203],v[32:35],v[12:15]
ds_read_b128 v[28:31],v1 offset:32768
v_mfma_f32_16x16x32_bf16 v[16:19],a[204:207],v[32:35],v[16:19]
s_waitcnt lgkmcnt(5)
v_mfma_f32_16x16x32_bf16 v[12:15],a[208:211],v[36:39],v[12:15]
ds_read_b128 v[32:35],v1 offset:33792
v_mfma_f32_16x16x32_bf16 v[16:19],a[212:215],v[36:39],v[16:19]
buffer_load_dwordx4 v0,s[36:39],s61 offen offset:1024 sc1 lds
v_mfma_f32_16x16x32_bf16 v[12:15],a[216:219],v[40:43],v[12:15]
ds_read_b128 v[36:39],v1 offset:34816
v_mfma_f32_16x16x32_bf16 v[16:19],a[220:223],v[40:43],v[16:19]
s_waitcnt lgkmcnt(5)
v_mfma_f32_16x16x32_bf16 v[12:15],a[224:227],v[44:47],v[12:15]
ds_read_b128 v[40:43],v1 offset:35840
v_mfma_f32_16x16x32_bf16 v[16:19],a[228:231],v[44:47],v[16:19]
v_mfma_f32_16x16x32_bf16 v[12:15],a[232:235],v[48:51],v[12:15]
buffer_load_dwordx4 v0,s[36:39],s61 offen offset:2048 sc1 lds
v_mfma_f32_16x16x32_bf16 v[16:19],a[236:239],v[48:51],v[16:19]
s_waitcnt lgkmcnt(4)
ds_read_b128 v[44:47],v1 offset:36864
v_mfma_f32_16x16x32_bf16 v[12:15],a[240:243],v[52:55],v[12:15]
ds_read_b128 v[48:51],v1 offset:37888
v_mfma_f32_16x16x32_bf16 v[16:19],a[244:247],v[52:55],v[16:19]
v_mfma_f32_16x16x32_bf16 v[12:15],a[248:251],v[56:59],v[12:15]
ds_read_b128 v[52:55],v1 offset:38912
v_mfma_f32_16x16x32_bf16 v[16:19],a[252:255],v[56:59],v[16:19]
s_waitcnt lgkmcnt(5)
buffer_load_dwordx4 v0,s[36:39],s61 offen offset:3072 sc1 lds
v_mfma_f32_16x16x32_bf16 v[12:15],v[108:111],v[28:31],v[12:15]
s_add_u32 s61,s61,4096
ds_read_b128 v[56:59],v1 offset:39936
s_add_u32 m0,s88,90112
v_mfma_f32_16x16x32_bf16 v[16:19],v[112:115],v[28:31],v[16:19]
v_mfma_f32_16x16x32_bf16 v[12:15],v[116:119],v[32:35],v[12:15]
ds_read_b128 v[28:31],v1 offset:40960
v_mfma_f32_16x16x32_bf16 v[16:19],v[120:123],v[32:35],v[16:19]
s_waitcnt lgkmcnt(5)
v_mfma_f32_16x16x32_bf16 v[12:15],v[124:127],v[36:39],v[12:15]
buffer_load_dwordx4 v0,s[36:39],s61 offen offset:0 sc1 lds
v_mfma_f32_16x16x32_bf16 v[16:19],v[128:131],v[36:39],v[16:19]
ds_read_b128 v[32:35],v1 offset:41984
v_mfma_f32_16x16x32_bf16 v[12:15],v[132:135],v[40:43],v[12:15]
ds_read_b128 v[36:39],v1 offset:43008
v_mfma_f32_16x16x32_bf16 v[16:19],v[136:139],v[40:43],v[16:19]
s_waitcnt lgkmcnt(5)
v_mfma_f32_16x16x32_bf16 v[12:15],v[140:143],v[44:47],v[12:15]
ds_read_b128 v[40:43],v1 offset:44032
v_mfma_f32_16x16x32_bf16 v[16:19],v[144:147],v[44:47],v[16:19]
buffer_load_dwordx4 v0,s[36:39],s61 offen offset:1024 sc1 lds
v_mfma_f32_16x16x32_bf16 v[12:15],v[148:151],v[48:51],v[12:15]
ds_read_b128 v[44:47],v1 offset:45056
v_mfma_f32_16x16x32_bf16 v[16:19],v[152:155],v[48:51],v[16:19]
s_waitcnt lgkmcnt(5)
v_mfma_f32_16x16x32_bf16 v[12:15],v[156:159],v[52:55],v[12:15]
ds_read_b128 v[48:51],v1 offset:46080
v_mfma_f32_16x16x32_bf16 v[16:19],v[160:163],v[52:55],v[16:19]
v_mfma_f32_16x16x32_bf16 v[12:15],v[164:167],v[56:59],v[12:15]
buffer_load_dwordx4 v0,s[36:39],s61 offen offset:2048 sc1 lds
v_mfma_f32_16x16x32_bf16 v[16:19],v[168:171],v[56:59],v[16:19]
s_waitcnt lgkmcnt(4)
ds_read_b128 v[52:55],v1 offset:47104
v_mfma_f32_16x16x32_bf16 v[12:15],v[172:175],v[28:31],v[12:15]
ds_read_b128 v[56:59],v1 offset:48128
v_mfma_f32_16x16x32_bf16 v[16:19],v[176:179],v[28:31],v[16:19]
ds_read_b128 v[88:91],v10 offset:0
v_mfma_f32_16x16x32_bf16 v[12:15],v[180:183],v[32:35],v[12:15]
ds_read_b128 v[28:31],v1 offset:49152
v_mfma_f32_16x16x32_bf16 v[16:19],v[184:187],v[32:35],v[16:19]
s_waitcnt lgkmcnt(6)
ds_read_b128 v[92:95],v10 offset:1024
v_mfma_f32_16x16x32_bf16 v[12:15],v[188:191],v[36:39],v[12:15]
ds_read_b128 v[32:35],v1 offset:50176
v_mfma_f32_16x16x32_bf16 v[16:19],v[192:195],v[36:39],v[16:19]
ds_read_b128 v[96:99],v10 offset:2048
v_mfma_f32_16x16x32_bf16 v[12:15],v[196:199],v[40:43],v[12:15]
ds_read_b128 v[36:39],v1 offset:51200
v_mfma_f32_16x16x32_bf16 v[16:19],v[200:203],v[40:43],v[16:19]
s_waitcnt lgkmcnt(5)
ds_read_b128 v[76:79],v10 offset:3072
v_mfma_f32_16x16x32_bf16 v[12:15],v[204:207],v[44:47],v[12:15]
ds_read_b128 v[40:43],v1 offset:52224
v_mfma_f32_16x16x32_bf16 v[16:19],v[208:211],v[44:47],v[16:19]
ds_read_b128 v[80:83],v10 offset:4096
v_mfma_f32_16x16x32_bf16 v[12:15],v[212:215],v[48:51],v[12:15]
ds_read_b128 v[44:47],v1 offset:53248
v_mfma_f32_16x16x32_bf16 v[16:19],v[88:91],v[48:51],v[16:19]
s_waitcnt lgkmcnt(5)
ds_read_b128 v[84:87],v10 offset:5120
v_mfma_f32_16x16x32_bf16 v[12:15],v[216:219],v[52:55],v[12:15]
ds_read_b128 v[48:51],v1 offset:54272
v_mfma_f32_16x16x32_bf16 v[16:19],v[92:95],v[52:55],v[16:19]
ds_read_b128 v[88:91],v10 offset:6144
v_mfma_f32_16x16x32_bf16 v[12:15],v[220:223],v[56:59],v[12:15]
ds_read_b128 v[52:55],v1 offset:55296
v_mfma_f32_16x16x32_bf16 v[16:19],v[96:99],v[56:59],v[16:19]
s_waitcnt lgkmcnt(5)
ds_read_b128 v[92:95],v10 offset:7168
v_mfma_f32_16x16x32_bf16 v[12:15],v[224:227],v[28:31],v[12:15]
s_waitcnt vmcnt(7)
ds_read_b128 v[56:59],v1 offset:56320
ds_read_b128 v[96:99],v10 offset:8192
v_mfma_f32_16x16x32_bf16 v[16:19],v[76:79],v[28:31],v[16:19]
s_barrier
v_mfma_f32_16x16x32_bf16 v[12:15],v[228:231],v[32:35],v[12:15]
buffer_store_dword v5,v4,s[36:39],s70 offen offset:0 sc0 sc1
ds_read_b128 v[28:31],v2 offset:0
ds_read_b32 v11,v8 offset:3072
v_mfma_f32_16x16x32_bf16 v[16:19],v[80:83],v[32:35],v[16:19]
s_waitcnt lgkmcnt(6)
ds_read_b128 v[76:79],v10 offset:9216
v_mfma_f32_16x16x32_bf16 v[12:15],v[232:235],v[36:39],v[12:15]
ds_read_b128 v[32:35],v2 offset:1024
v_mfma_f32_16x16x32_bf16 v[16:19],v[84:87],v[36:39],v[16:19]
ds_read_b128 v[80:83],v10 offset:10240
v_mfma_f32_16x16x32_bf16 v[12:15],v[236:239],v[40:43],v[12:15]
s_waitcnt lgkmcnt(3)
ds_read_b128 v[36:39],v2 offset:2048
v_mfma_f32_16x16x32_bf16 v[16:19],v[88:91],v[40:43],v[16:19]
v_cmp_eq_u32 vcc,s60,v11
v_mfma_f32_16x16x32_bf16 v[12:15],v[240:243],v[44:47],v[12:15]
s_cmp_eq_u32 vcc_lo,-1
ds_read_b128 v[40:43],v2 offset:3072
s_cselect_b32 s67,s86,s59)"
R"(
v_mfma_f32_16x16x32_bf16 v[16:19],v[92:95],v[44:47],v[16:19]
s_mul_i32 s61,s67,7
s_lshr_b32 s61,s61,1
s_cmp_ge_u32 s66,s59
v_mfma_f32_16x16x32_bf16 v[12:15],v[244:247],v[48:51],v[12:15]
ds_read_b128 v[44:47],v2 offset:4096
v_mfma_f32_16x16x32_bf16 v[16:19],v[96:99],v[48:51],v[16:19]
s_waitcnt lgkmcnt(3)
v_mfma_f32_16x16x32_bf16 v[12:15],v[248:251],v[52:55],v[12:15]
ds_read_b128 v[48:51],v2 offset:5120
v_mfma_f32_16x16x32_bf16 v[16:19],v[76:79],v[52:55],v[16:19]
v_mfma_f32_16x16x32_bf16 v[12:15],v[252:255],v[56:59],v[12:15]
ds_read_b128 v[52:55],v2 offset:6144
v_mfma_f32_16x16x32_bf16 v[16:19],v[80:83],v[56:59],v[16:19]
s_cbranch_scc1 Lg4_slow1_%=
Lg4_d1w3_%=:
v_mfma_f32_16x16x32_bf16 v[20:23],a[0:3],v[28:31],0
s_add_u32 m0,s88,0
ds_read_b128 v[56:59],v2 offset:7168
v_lshlrev_b32 v60,16,v68
v_mfma_f32_16x16x32_bf16 v[24:27],a[4:7],v[28:31],0
s_max_u32 s73,s48,s65
v_and_b32 v61,0xffff0000,v68
v_lshlrev_b32 v62,16,v69
v_mfma_f32_16x16x32_bf16 v[20:23],a[8:11],v[32:35],v[20:23]
s_mov_b32 s65,0
ds_read_b128 v[28:31],v2 offset:8192
v_and_b32 v63,0xffff0000,v69
v_mfma_f32_16x16x32_bf16 v[24:27],a[12:15],v[32:35],v[24:27]
s_waitcnt lgkmcnt(5)
v_lshlrev_b32 v64,16,v70
v_and_b32 v65,0xffff0000,v70
v_mfma_f32_16x16x32_bf16 v[20:23],a[16:19],v[36:39],v[20:23]
buffer_load_dwordx4 v0,s[36:39],s61 offen offset:0 sc1 lds
v_mfma_f32_16x16x32_bf16 v[24:27],a[20:23],v[36:39],v[24:27]
s_mov_b64 s[44:45],s[46:47]
ds_read_b128 v[32:35],v2 offset:9216
v_lshlrev_b32 v66,16,v71
v_mfma_f32_16x16x32_bf16 v[20:23],a[24:27],v[40:43],v[20:23]
s_mov_b64 s[46:47],s[48:49]
ds_read_b128 v[36:39],v2 offset:10240
v_and_b32 v67,0xffff0000,v71
v_mfma_f32_16x16x32_bf16 v[24:27],a[28:31],v[40:43],v[24:27]
s_waitcnt lgkmcnt(5)
v_add_f32 v12,v12,v60
v_add_f32 v13,v13,v61
v_mfma_f32_16x16x32_bf16 v[20:23],a[32:35],v[44:47],v[20:23]
buffer_load_dwordx4 v0,s[36:39],s61 offen offset:1024 sc1 lds
v_mfma_f32_16x16x32_bf16 v[24:27],a[36:39],v[44:47],v[24:27]
ds_read_b128 v[40:43],v2 offset:11264
v_add_f32 v14,v14,v62
v_mfma_f32_16x16x32_bf16 v[20:23],a[40:43],v[48:51],v[20:23]
ds_read_b128 v[44:47],v2 offset:12288
v_add_f32 v15,v15,v63
v_mfma_f32_16x16x32_bf16 v[24:27],a[44:47],v[48:51],v[24:27]
s_waitcnt lgkmcnt(5)
v_add_f32 v16,v16,v64
v_add_f32 v17,v17,v65
v_mfma_f32_16x16x32_bf16 v[20:23],a[48:51],v[52:55],v[20:23]
buffer_load_dwordx4 v0,s[36:39],s61 offen offset:2048 sc1 lds
v_mfma_f32_16x16x32_bf16 v[24:27],a[52:55],v[52:55],v[24:27]
ds_read_b128 v[48:51],v2 offset:13312
v_add_f32 v18,v18,v66
v_mfma_f32_16x16x32_bf16 v[20:23],a[56:59],v[56:59],v[20:23]
ds_read_b128 v[52:55],v2 offset:14336
v_add_f32 v19,v19,v67
v_mfma_f32_16x16x32_bf16 v[24:27],a[60:63],v[56:59],v[24:27]
s_waitcnt lgkmcnt(5)
v_cvt_pk_bf16_f32 v60,v12,v13
v_cvt_pk_bf16_f32 v61,v14,v15
v_mfma_f32_16x16x32_bf16 v[20:23],a[64:67],v[28:31],v[20:23]
buffer_load_dwordx4 v0,s[36:39],s61 offen offset:3072 sc1 lds
v_mfma_f32_16x16x32_bf16 v[24:27],a[68:71],v[28:31],v[24:27]
s_add_u32 s61,s61,4096
ds_read_b128 v[56:59],v2 offset:15360
v_cvt_pk_bf16_f32 v62,v16,v17
v_mfma_f32_16x16x32_bf16 v[20:23],a[72:75],v[32:35],v[20:23]
s_add_u32 m0,s88,4096
ds_read_b128 v[28:31],v2 offset:16384
v_cvt_pk_bf16_f32 v63,v18,v19
v_mfma_f32_16x16x32_bf16 v[24:27],a[76:79],v[32:35],v[24:27]
s_waitcnt lgkmcnt(5)
s_add_u32 s74,s53,s55
s_cmp_eq_u32 s74,s56
v_mfma_f32_16x16x32_bf16 v[20:23],a[80:83],v[36:39],v[20:23]
buffer_store_dwordx4 v[60:63],v3,s[36:39],s73 offen offset:0 sc0 sc1
ds_read_b128 v[32:35],v2 offset:17408
v_mfma_f32_16x16x32_bf16 v[24:27],a[84:87],v[36:39],v[24:27]
s_cselect_b32 s74,s57,s74
s_cmp_eq_u32 s74,s58
s_cselect_b32 s74,s59,s74
v_mfma_f32_16x16x32_bf16 v[20:23],a[88:91],v[40:43],v[20:23]
s_cmp_ge_u32 s67,s59
ds_read_b128 v[36:39],v2 offset:18432
s_cselect_b32 s86,s50,s51
v_mfma_f32_16x16x32_bf16 v[24:27],a[92:95],v[40:43],v[24:27]
s_waitcnt lgkmcnt(5)
s_lshr_b32 s70,s48,4
v_mfma_f32_16x16x32_bf16 v[20:23],a[96:99],v[44:47],v[20:23]
buffer_load_dwordx4 v0,s[36:39],s61 offen offset:0 sc1 lds
v_mfma_f32_16x16x32_bf16 v[24:27],a[100:103],v[44:47],v[24:27]
ds_read_b128 v[40:43],v2 offset:19456
v_mfma_f32_16x16x32_bf16 v[20:23],a[104:107],v[48:51],v[20:23]
ds_read_b128 v[44:47],v2 offset:20480
v_mfma_f32_16x16x32_bf16 v[24:27],a[108:111],v[48:51],v[24:27]
s_waitcnt lgkmcnt(5)
v_mfma_f32_16x16x32_bf16 v[20:23],a[112:115],v[52:55],v[20:23]
buffer_load_dwordx4 v0,s[36:39],s61 offen offset:1024 sc1 lds
v_mfma_f32_16x16x32_bf16 v[24:27],a[116:119],v[52:55],v[24:27]
ds_read_b128 v[48:51],v2 offset:21504
v_mfma_f32_16x16x32_bf16 v[20:23],a[120:123],v[56:59],v[20:23]
ds_read_b128 v[52:55],v2 offset:22528
v_mfma_f32_16x16x32_bf16 v[24:27],a[124:127],v[56:59],v[24:27]
s_waitcnt lgkmcnt(5)
v_mfma_f32_16x16x32_bf16 v[20:23],a[128:131],v[28:31],v[20:23]
buffer_load_dwordx4 v0,s[36:39],s61 offen offset:2048 sc1 lds
v_mfma_f32_16x16x32_bf16 v[24:27],a[132:135],v[28:31],v[24:27]
s_add_u32 s61,s61,24576
ds_read_b128 v[56:59],v2 offset:23552
s_add_u32 m0,s88,28672
v_mfma_f32_16x16x32_bf16 v[20:23],a[136:139],v[32:35],v[20:23]
ds_read_b128 v[28:31],v2 offset:24576
v_mfma_f32_16x16x32_bf16 v[24:27],a[140:143],v[32:35],v[24:27]
s_waitcnt lgkmcnt(5)
v_mfma_f32_16x16x32_bf16 v[20:23],a[144:147],v[36:39],v[20:23]
buffer_load_dwordx4 v[72:75],v9,s[36:39],s49 offen offset:0 sc1
v_mfma_f32_16x16x32_bf16 v[24:27],a[148:151],v[36:39],v[24:27]
s_mov_b64 s[48:49],s[50:51]
ds_read_b128 v[32:35],v2 offset:25600
s_mov_b64 s[50:51],s[52:53]
v_mfma_f32_16x16x32_bf16 v[20:23],a[152:155],v[40:43],v[20:23]
s_mov_b32 s52,s74
ds_read_b128 v[36:39],v2 offset:26624
v_mfma_f32_16x16x32_bf16 v[24:27],a[156:159],v[40:43],v[24:27]
s_waitcnt lgkmcnt(5)
v_mfma_f32_16x16x32_bf16 v[20:23],a[160:163],v[44:47],v[20:23]
s_waitcnt vmcnt(10)
ds_read_b128 v[40:43],v2 offset:27648
v_mfma_f32_16x16x32_bf16 v[24:27],a[164:167],v[44:47],v[24:27]
s_barrier
v_mfma_f32_16x16x32_bf16 v[20:23],a[168:171],v[48:51],v[20:23]
ds_read_b128 v[44:47],v2 offset:28672
v_mfma_f32_16x16x32_bf16 v[24:27],a[172:175],v[48:51],v[24:27]
s_waitcnt lgkmcnt(5)
v_mfma_f32_16x16x32_bf16 v[20:23],a[176:179],v[52:55],v[20:23]
ds_read_b128 v[48:51],v2 offset:29696
v_mfma_f32_16x16x32_bf16 v[24:27],a[180:183],v[52:55],v[24:27]
v_mfma_f32_16x16x32_bf16 v[20:23],a[184:187],v[56:59],v[20:23]
ds_read_b128 v[52:55],v2 offset:30720
v_mfma_f32_16x16x32_bf16 v[24:27],a[188:191],v[56:59],v[24:27]
s_waitcnt lgkmcnt(5)
v_mfma_f32_16x16x32_bf16 v[20:23],a[192:195],v[28:31],v[20:23]
buffer_load_dwordx4 v0,s[36:39],s61 offen offset:0 sc1 lds
v_mfma_f32_16x16x32_bf16 v[24:27],a[196:199],v[28:31],v[24:27]
ds_read_b128 v[56:59],v2 offset:31744
v_mfma_f32_16x16x32_bf16 v[20:23],a[200:203],v[32:35],v[20:23]
ds_read_b128 v[28:31],v2 offset:32768
v_mfma_f32_16x16x32_bf16 v[24:27],a[204:207],v[32:35],v[24:27]
s_waitcnt lgkmcnt(5)
v_mfma_f32_16x16x32_bf16 v[20:23],a[208:211],v[36:39],v[20:23]
ds_read_b128 v[32:35],v2 offset:33792
v_mfma_f32_16x16x32_bf16 v[24:27],a[212:215],v[36:39],v[24:27]
buffer_load_dwordx4 v0,s[36:39],s61 offen offset:1024 sc1 lds
v_mfma_f32_16x16x32_bf16 v[20:23],a[216:219],v[40:43],v[20:23]
ds_read_b128 v[36:39],v2 offset:34816
v_mfma_f32_16x16x32_bf16 v[24:27],a[220:223],v[40:43],v[24:27]
s_waitcnt lgkmcnt(5)
v_mfma_f32_16x16x32_bf16 v[20:23],a[224:227],v[44:47],v[20:23]
ds_read_b128 v[40:43],v2 offset:35840
v_mfma_f32_16x16x32_bf16 v[24:27],a[228:231],v[44:47],v[24:27]
v_mfma_f32_16x16x32_bf16 v[20:23],a[232:235],v[48:51],v[20:23]
buffer_load_dwordx4 v0,s[36:39],s61 offen offset:2048 sc1 lds
v_mfma_f32_16x16x32_bf16 v[24:27],a[236:239],v[48:51],v[24:27]
s_waitcnt lgkmcnt(4)
ds_read_b128 v[44:47],v2 offset:36864
v_mfma_f32_16x16x32_bf16 v[20:23],a[240:243],v[52:55],v[20:23]
ds_read_b128 v[48:51],v2 offset:37888
v_mfma_f32_16x16x32_bf16 v[24:27],a[244:247],v[52:55],v[24:27]
v_mfma_f32_16x16x32_bf16 v[20:23],a[248:251],v[56:59],v[20:23]
ds_read_b128 v[52:55],v2 offset:38912
v_mfma_f32_16x16x32_bf16 v[24:27],a[252:255],v[56:59],v[24:27]
s_waitcnt lgkmcnt(5)
buffer_load_dwordx4 v0,s[36:39],s61 offen offset:3072 sc1 lds
v_mfma_f32_16x16x32_bf16 v[20:23],v[108:111],v[28:31],v[20:23]
s_add_u32 s61,s61,4096
ds_read_b128 v[56:59],v2 offset:39936
s_add_u32 m0,s88,32768
v_mfma_f32_16x16x32_bf16 v[24:27],v[112:115],v[28:31],v[24:27]
v_mfma_f32_16x16x32_bf16 v[20:23],v[116:119],v[32:35],v[20:23]
ds_read_b128 v[28:31],v2 offset:40960
v_mfma_f32_16x16x32_bf16 v[24:27],v[120:123],v[32:35],v[24:27]
s_waitcnt lgkmcnt(5)
v_mfma_f32_16x16x32_bf16 v[20:23],v[124:127],v[36:39],v[20:23]
buffer_load_dwordx4 v0,s[36:39],s61 offen offset:0 sc1 lds
v_mfma_f32_16x16x32_bf16 v[24:27],v[128:131],v[36:39],v[24:27]
ds_read_b128 v[32:35],v2 offset:41984
v_mfma_f32_16x16x32_bf16 v[20:23],v[132:135],v[40:43],v[20:23]
ds_read_b128 v[36:39],v2 offset:43008
v_mfma_f32_16x16x32_bf16 v[24:27],v[136:139],v[40:43],v[24:27]
s_waitcnt lgkmcnt(5))"
R"(
v_mfma_f32_16x16x32_bf16 v[20:23],v[140:143],v[44:47],v[20:23]
ds_read_b128 v[40:43],v2 offset:44032
v_mfma_f32_16x16x32_bf16 v[24:27],v[144:147],v[44:47],v[24:27]
buffer_load_dwordx4 v0,s[36:39],s61 offen offset:1024 sc1 lds
v_mfma_f32_16x16x32_bf16 v[20:23],v[148:151],v[48:51],v[20:23]
ds_read_b128 v[44:47],v2 offset:45056
v_mfma_f32_16x16x32_bf16 v[24:27],v[152:155],v[48:51],v[24:27]
s_waitcnt lgkmcnt(5)
v_mfma_f32_16x16x32_bf16 v[20:23],v[156:159],v[52:55],v[20:23]
ds_read_b128 v[48:51],v2 offset:46080
v_mfma_f32_16x16x32_bf16 v[24:27],v[160:163],v[52:55],v[24:27]
v_mfma_f32_16x16x32_bf16 v[20:23],v[164:167],v[56:59],v[20:23]
buffer_load_dwordx4 v0,s[36:39],s61 offen offset:2048 sc1 lds
v_mfma_f32_16x16x32_bf16 v[24:27],v[168:171],v[56:59],v[24:27]
s_waitcnt lgkmcnt(4)
ds_read_b128 v[52:55],v2 offset:47104
v_mfma_f32_16x16x32_bf16 v[20:23],v[172:175],v[28:31],v[20:23]
ds_read_b128 v[56:59],v2 offset:48128
v_mfma_f32_16x16x32_bf16 v[24:27],v[176:179],v[28:31],v[24:27]
ds_read_b128 v[88:91],v10 offset:0
v_mfma_f32_16x16x32_bf16 v[20:23],v[180:183],v[32:35],v[20:23]
ds_read_b128 v[28:31],v2 offset:49152
v_mfma_f32_16x16x32_bf16 v[24:27],v[184:187],v[32:35],v[24:27]
s_waitcnt lgkmcnt(6)
ds_read_b128 v[92:95],v10 offset:1024
v_mfma_f32_16x16x32_bf16 v[20:23],v[188:191],v[36:39],v[20:23]
ds_read_b128 v[32:35],v2 offset:50176
v_mfma_f32_16x16x32_bf16 v[24:27],v[192:195],v[36:39],v[24:27]
ds_read_b128 v[96:99],v10 offset:2048
v_mfma_f32_16x16x32_bf16 v[20:23],v[196:199],v[40:43],v[20:23]
ds_read_b128 v[36:39],v2 offset:51200
v_mfma_f32_16x16x32_bf16 v[24:27],v[200:203],v[40:43],v[24:27]
s_waitcnt lgkmcnt(5)
ds_read_b128 v[76:79],v10 offset:3072
v_mfma_f32_16x16x32_bf16 v[20:23],v[204:207],v[44:47],v[20:23]
ds_read_b128 v[40:43],v2 offset:52224
v_mfma_f32_16x16x32_bf16 v[24:27],v[208:211],v[44:47],v[24:27]
ds_read_b128 v[80:83],v10 offset:4096
v_mfma_f32_16x16x32_bf16 v[20:23],v[212:215],v[48:51],v[20:23]
ds_read_b128 v[44:47],v2 offset:53248
v_mfma_f32_16x16x32_bf16 v[24:27],v[88:91],v[48:51],v[24:27]
s_waitcnt lgkmcnt(5)
ds_read_b128 v[84:87],v10 offset:5120
v_mfma_f32_16x16x32_bf16 v[20:23],v[216:219],v[52:55],v[20:23]
ds_read_b128 v[48:51],v2 offset:54272
v_mfma_f32_16x16x32_bf16 v[24:27],v[92:95],v[52:55],v[24:27]
ds_read_b128 v[88:91],v10 offset:6144
v_mfma_f32_16x16x32_bf16 v[20:23],v[220:223],v[56:59],v[20:23]
ds_read_b128 v[52:55],v2 offset:55296
v_mfma_f32_16x16x32_bf16 v[24:27],v[96:99],v[56:59],v[24:27]
s_waitcnt lgkmcnt(5)
ds_read_b128 v[92:95],v10 offset:7168
v_mfma_f32_16x16x32_bf16 v[20:23],v[224:227],v[28:31],v[20:23]
s_waitcnt vmcnt(7)
ds_read_b128 v[56:59],v2 offset:56320
ds_read_b128 v[96:99],v10 offset:8192
v_mfma_f32_16x16x32_bf16 v[24:27],v[76:79],v[28:31],v[24:27]
s_barrier
v_mfma_f32_16x16x32_bf16 v[20:23],v[228:231],v[32:35],v[20:23]
buffer_store_dword v5,v4,s[36:39],s70 offen offset:0 sc0 sc1
ds_read_b128 v[28:31],v1 offset:0
ds_read_b32 v11,v8 offset:3072
v_mfma_f32_16x16x32_bf16 v[24:27],v[80:83],v[32:35],v[24:27]
s_waitcnt lgkmcnt(6)
ds_read_b128 v[76:79],v10 offset:9216
v_mfma_f32_16x16x32_bf16 v[20:23],v[232:235],v[36:39],v[20:23]
ds_read_b128 v[32:35],v1 offset:1024
v_mfma_f32_16x16x32_bf16 v[24:27],v[84:87],v[36:39],v[24:27]
ds_read_b128 v[80:83],v10 offset:10240
v_mfma_f32_16x16x32_bf16 v[20:23],v[236:239],v[40:43],v[20:23]
s_waitcnt lgkmcnt(3)
ds_read_b128 v[36:39],v1 offset:2048
v_mfma_f32_16x16x32_bf16 v[24:27],v[88:91],v[40:43],v[24:27]
v_cmp_eq_u32 vcc,s60,v11
v_mfma_f32_16x16x32_bf16 v[20:23],v[240:243],v[44:47],v[20:23]
s_cmp_eq_u32 vcc_lo,-1
ds_read_b128 v[40:43],v1 offset:3072
s_cselect_b32 s66,s86,s59
v_mfma_f32_16x16x32_bf16 v[24:27],v[92:95],v[44:47],v[24:27]
s_mul_i32 s61,s66,7
s_lshr_b32 s61,s61,1
s_cmp_lt_u32 s67,s59
v_mfma_f32_16x16x32_bf16 v[20:23],v[244:247],v[48:51],v[20:23]
ds_read_b128 v[44:47],v1 offset:4096
v_mfma_f32_16x16x32_bf16 v[24:27],v[96:99],v[48:51],v[24:27]
s_waitcnt lgkmcnt(3)
v_mfma_f32_16x16x32_bf16 v[20:23],v[248:251],v[52:55],v[20:23]
ds_read_b128 v[48:51],v1 offset:5120
v_mfma_f32_16x16x32_bf16 v[24:27],v[76:79],v[52:55],v[24:27]
v_mfma_f32_16x16x32_bf16 v[20:23],v[252:255],v[56:59],v[20:23]
ds_read_b128 v[52:55],v1 offset:6144
v_mfma_f32_16x16x32_bf16 v[24:27],v[80:83],v[56:59],v[24:27]
s_cbranch_scc1 Lg4_d0w3_%=
s_branch Lg4_slow0_%=
Lg4_slow0_%=:
s_mul_i32 s61,s48,7
s_lshr_b32 s61,s61,1
s_cmp_ge_u32 s66,s59
s_cbranch_scc1 Lg4_se0_%=
s_add_u32 m0,s88,0
s_nop 0
buffer_load_dwordx4 v0,s[36:39],s61 offen offset:0 sc1 lds
buffer_load_dwordx4 v0,s[36:39],s61 offen offset:1024 sc1 lds
buffer_load_dwordx4 v0,s[36:39],s61 offen offset:2048 sc1 lds
buffer_load_dwordx4 v0,s[36:39],s61 offen offset:3072 sc1 lds
s_add_u32 s61,s61,4096
s_add_u32 m0,s88,4096
s_nop 0
buffer_load_dwordx4 v0,s[36:39],s61 offen offset:0 sc1 lds
buffer_load_dwordx4 v0,s[36:39],s61 offen offset:1024 sc1 lds
buffer_load_dwordx4 v0,s[36:39],s61 offen offset:2048 sc1 lds
s_add_u32 s61,s61,24576
s_add_u32 m0,s88,28672
s_nop 0
buffer_load_dwordx4 v0,s[36:39],s61 offen offset:0 sc1 lds
buffer_load_dwordx4 v0,s[36:39],s61 offen offset:1024 sc1 lds
buffer_load_dwordx4 v0,s[36:39],s61 offen offset:2048 sc1 lds
buffer_load_dwordx4 v0,s[36:39],s61 offen offset:3072 sc1 lds
s_add_u32 s61,s61,4096
s_add_u32 m0,s88,32768
s_nop 0
buffer_load_dwordx4 v0,s[36:39],s61 offen offset:0 sc1 lds
buffer_load_dwordx4 v0,s[36:39],s61 offen offset:1024 sc1 lds
buffer_load_dwordx4 v0,s[36:39],s61 offen offset:2048 sc1 lds
Lg4_se0_%=:
s_cmp_eq_u32 s54,1
s_cselect_b32 s68,s49,s48
s_mul_i32 s68,s68,5
s_lshr_b32 s68,s68,6
s_add_u32 m0,s78,0
s_nop 0
buffer_load_dword v7,s[36:39],s68 offen offset:0 sc1 lds
s_lshr_b32 s73,s47,0
v_lshlrev_b32 v60,16,v72
v_and_b32 v61,0xffff0000,v72
v_lshlrev_b32 v62,16,v73
v_and_b32 v63,0xffff0000,v73
v_lshlrev_b32 v64,16,v74
v_and_b32 v65,0xffff0000,v74
v_lshlrev_b32 v66,16,v75
v_and_b32 v67,0xffff0000,v75
v_add_f32 v20,v20,v60
v_add_f32 v21,v21,v61
s_add_u32 m0,s78,1024
s_nop 0
buffer_load_dword v7,s[36:39],s68 offen offset:0 sc1 lds
v_add_f32 v22,v22,v62
v_add_f32 v23,v23,v63
v_add_f32 v24,v24,v64
v_add_f32 v25,v25,v65
v_add_f32 v26,v26,v66
v_add_f32 v27,v27,v67
v_cvt_pk_bf16_f32 v60,v20,v21
v_cvt_pk_bf16_f32 v61,v22,v23
v_cvt_pk_bf16_f32 v62,v24,v25
v_cvt_pk_bf16_f32 v63,v26,v27
buffer_store_dwordx4 v[60:63],v3,s[36:39],s73 offen offset:0 sc0 sc1
s_cmp_ge_u32 s48,s59
s_cbranch_scc1 Lg4_done0_%=
s_add_u32 m0,s78,2048
s_nop 0
buffer_load_dword v7,s[36:39],s68 offen offset:0 sc1 lds
s_waitcnt vmcnt(3)
s_barrier
ds_read_b32 v11,v8 offset:0
s_mov_b32 s69,2304
s_waitcnt lgkmcnt(0)
v_cmp_eq_u32 vcc,s60,v11
s_nop 0
s_cmp_eq_u32 vcc_lo,-1
s_cbranch_scc1 Lg4_go0_%=
s_add_u32 m0,s78,3072
s_nop 0
buffer_load_dword v7,s[36:39],s68 offen offset:0 sc1 lds
s_waitcnt vmcnt(3)
s_barrier
ds_read_b32 v11,v8 offset:1024
s_mov_b32 s69,3328
s_waitcnt lgkmcnt(0)
v_cmp_eq_u32 vcc,s60,v11
s_nop 0
s_cmp_eq_u32 vcc_lo,-1
s_cbranch_scc1 Lg4_go0_%=
s_add_u32 m0,s78,0
s_nop 0
buffer_load_dword v7,s[36:39],s68 offen offset:0 sc1 lds
s_waitcnt vmcnt(2)
s_barrier
ds_read_b32 v11,v8 offset:2048
s_lshr_b32 s70,s47,4
buffer_store_dword v5,v4,s[36:39],s70 offen offset:0 sc0 sc1
s_mov_b32 s47,s59
s_mov_b32 s69,256
s_waitcnt lgkmcnt(0)
v_cmp_eq_u32 vcc,s60,v11
s_nop 0
s_cmp_eq_u32 vcc_lo,-1
s_cbranch_scc1 Lg4_go0_%=
s_add_u32 m0,s78,1024
s_nop 0)"
R"(
buffer_load_dword v7,s[36:39],s68 offen offset:0 sc1 lds
s_waitcnt vmcnt(3)
s_barrier
ds_read_b32 v11,v8 offset:3072
s_mov_b32 s69,1280
s_waitcnt lgkmcnt(0)
v_cmp_eq_u32 vcc,s60,v11
s_nop 0
s_cmp_eq_u32 vcc_lo,-1
s_cbranch_scc1 Lg4_go0_%=
Lg4_poll0_%=:
s_add_u32 m0,s78,2048
s_nop 0
buffer_load_dword v7,s[36:39],s68 offen offset:0 sc1 lds
s_waitcnt vmcnt(2)
s_barrier
ds_read_b32 v11,v8 offset:0
s_mov_b32 s69,2304
s_waitcnt lgkmcnt(0)
v_cmp_eq_u32 vcc,s60,v11
s_nop 0
s_cmp_eq_u32 vcc_lo,-1
s_cbranch_scc1 Lg4_go0_%=
s_sleep 4
s_add_u32 m0,s78,3072
s_nop 0
buffer_load_dword v7,s[36:39],s68 offen offset:0 sc1 lds
s_waitcnt vmcnt(2)
s_barrier
ds_read_b32 v11,v8 offset:1024
s_mov_b32 s69,3328
s_waitcnt lgkmcnt(0)
v_cmp_eq_u32 vcc,s60,v11
s_nop 0
s_cmp_eq_u32 vcc_lo,-1
s_cbranch_scc1 Lg4_go0_%=
s_sleep 4
s_add_u32 m0,s78,0
s_nop 0
buffer_load_dword v7,s[36:39],s68 offen offset:0 sc1 lds
s_waitcnt vmcnt(2)
s_barrier
ds_read_b32 v11,v8 offset:2048
s_mov_b32 s69,256
s_waitcnt lgkmcnt(0)
v_cmp_eq_u32 vcc,s60,v11
s_nop 0
s_cmp_eq_u32 vcc_lo,-1
s_cbranch_scc1 Lg4_go0_%=
s_sleep 4
s_add_u32 m0,s78,1024
s_nop 0
buffer_load_dword v7,s[36:39],s68 offen offset:0 sc1 lds
s_waitcnt vmcnt(2)
s_barrier
ds_read_b32 v11,v8 offset:3072
s_mov_b32 s69,1280
s_waitcnt lgkmcnt(0)
v_cmp_eq_u32 vcc,s60,v11
s_nop 0
s_cmp_eq_u32 vcc_lo,-1
s_cbranch_scc1 Lg4_go0_%=
s_sleep 4
s_add_u32 s79,s79,1
s_cmp_lt_u32 s79,20000
s_cbranch_scc1 Lg4_poll0_%=
Lg4_go0_%=:
s_mul_i32 s61,s48,7
s_lshr_b32 s61,s61,1
s_cmp_lt_u32 s66,s59
s_cbranch_scc1 Lg4_gs0_%=
s_add_u32 m0,s88,0
s_nop 0
buffer_load_dwordx4 v0,s[36:39],s61 offen offset:0 sc1 lds
buffer_load_dwordx4 v0,s[36:39],s61 offen offset:1024 sc1 lds
buffer_load_dwordx4 v0,s[36:39],s61 offen offset:2048 sc1 lds
buffer_load_dwordx4 v0,s[36:39],s61 offen offset:3072 sc1 lds
s_add_u32 s61,s61,4096
s_add_u32 m0,s88,4096
s_nop 0
buffer_load_dwordx4 v0,s[36:39],s61 offen offset:0 sc1 lds
buffer_load_dwordx4 v0,s[36:39],s61 offen offset:1024 sc1 lds
buffer_load_dwordx4 v0,s[36:39],s61 offen offset:2048 sc1 lds
s_add_u32 s61,s61,24576
s_add_u32 m0,s88,28672
s_nop 0
buffer_load_dwordx4 v0,s[36:39],s61 offen offset:0 sc1 lds
buffer_load_dwordx4 v0,s[36:39],s61 offen offset:1024 sc1 lds
buffer_load_dwordx4 v0,s[36:39],s61 offen offset:2048 sc1 lds
buffer_load_dwordx4 v0,s[36:39],s61 offen offset:3072 sc1 lds
s_add_u32 s61,s61,4096
s_add_u32 m0,s88,32768
s_nop 0
buffer_load_dwordx4 v0,s[36:39],s61 offen offset:0 sc1 lds
buffer_load_dwordx4 v0,s[36:39],s61 offen offset:1024 sc1 lds
buffer_load_dwordx4 v0,s[36:39],s61 offen offset:2048 sc1 lds
v_add_u32 v60,s69,v8
s_waitcnt vmcnt(7)
s_barrier
ds_read_b32 v11,v60 offset:0
ds_read_b128 v[28:31],v1 offset:0
ds_read_b128 v[32:35],v1 offset:1024
ds_read_b128 v[36:39],v1 offset:2048
ds_read_b128 v[40:43],v1 offset:3072
ds_read_b128 v[44:47],v1 offset:4096
ds_read_b128 v[48:51],v1 offset:5120
ds_read_b128 v[52:55],v1 offset:6144
s_lshr_b32 s70,s47,4
buffer_store_dword v5,v4,s[36:39],s70 offen offset:0 sc0 sc1
s_mov_b32 s47,s59
s_waitcnt lgkmcnt(7)
v_cmp_eq_u32 vcc,s60,v11
s_nop 0
s_cmp_eq_u32 vcc_lo,-1
s_cselect_b32 s66,s49,s59
s_mul_i32 s61,s66,7
s_lshr_b32 s61,s61,1
s_waitcnt lgkmcnt(5)
s_setpc_b64 s[90:91]
Lg4_gs0_%=:
ds_read_b128 v[28:31],v1 offset:0
ds_read_b128 v[32:35],v1 offset:1024
ds_read_b128 v[36:39],v1 offset:2048
ds_read_b128 v[40:43],v1 offset:3072
ds_read_b128 v[44:47],v1 offset:4096
ds_read_b128 v[48:51],v1 offset:5120
ds_read_b128 v[52:55],v1 offset:6144
s_cmp_eq_u32 vcc_hi,-1
s_cselect_b32 s66,s49,s59
s_mul_i32 s61,s66,7
s_lshr_b32 s61,s61,1
s_mov_b32 s65,s59
s_waitcnt lgkmcnt(5)
s_setpc_b64 s[90:91]
Lg4_done0_%=:
s_waitcnt vmcnt(0)
s_barrier
s_lshr_b32 s70,s47,4
buffer_store_dword v5,v4,s[36:39],s70 offen offset:0 sc0 sc1
s_branch Lg4_done_%=
Lg4_slow1_%=:
s_mul_i32 s61,s49,7
s_lshr_b32 s61,s61,1
s_cmp_ge_u32 s67,s59
s_cbranch_scc1 Lg4_se1_%=
s_add_u32 m0,s88,57344
s_nop 0
buffer_load_dwordx4 v0,s[36:39],s61 offen offset:0 sc1 lds
buffer_load_dwordx4 v0,s[36:39],s61 offen offset:1024 sc1 lds
buffer_load_dwordx4 v0,s[36:39],s61 offen offset:2048 sc1 lds
buffer_load_dwordx4 v0,s[36:39],s61 offen offset:3072 sc1 lds
s_add_u32 s61,s61,4096
s_add_u32 m0,s88,61440
s_nop 0
buffer_load_dwordx4 v0,s[36:39],s61 offen offset:0 sc1 lds
buffer_load_dwordx4 v0,s[36:39],s61 offen offset:1024 sc1 lds
buffer_load_dwordx4 v0,s[36:39],s61 offen offset:2048 sc1 lds
s_add_u32 s61,s61,24576
s_add_u32 m0,s88,86016
s_nop 0
buffer_load_dwordx4 v0,s[36:39],s61 offen offset:0 sc1 lds
buffer_load_dwordx4 v0,s[36:39],s61 offen offset:1024 sc1 lds
buffer_load_dwordx4 v0,s[36:39],s61 offen offset:2048 sc1 lds
buffer_load_dwordx4 v0,s[36:39],s61 offen offset:3072 sc1 lds
s_add_u32 s61,s61,4096
s_add_u32 m0,s88,90112
s_nop 0
buffer_load_dwordx4 v0,s[36:39],s61 offen offset:0 sc1 lds
buffer_load_dwordx4 v0,s[36:39],s61 offen offset:1024 sc1 lds
buffer_load_dwordx4 v0,s[36:39],s61 offen offset:2048 sc1 lds
Lg4_se1_%=:
s_cmp_eq_u32 s54,1
s_cselect_b32 s68,s50,s49
s_mul_i32 s68,s68,5
s_lshr_b32 s68,s68,6
s_add_u32 m0,s78,0
s_nop 0
buffer_load_dword v7,s[36:39],s68 offen offset:0 sc1 lds
s_lshr_b32 s73,s48,0
v_lshlrev_b32 v60,16,v68
v_and_b32 v61,0xffff0000,v68
v_lshlrev_b32 v62,16,v69
v_and_b32 v63,0xffff0000,v69
v_lshlrev_b32 v64,16,v70
v_and_b32 v65,0xffff0000,v70
v_lshlrev_b32 v66,16,v71
v_and_b32 v67,0xffff0000,v71
v_add_f32 v12,v12,v60
v_add_f32 v13,v13,v61
s_add_u32 m0,s78,1024
s_nop 0
buffer_load_dword v7,s[36:39],s68 offen offset:0 sc1 lds
v_add_f32 v14,v14,v62
v_add_f32 v15,v15,v63
v_add_f32 v16,v16,v64
v_add_f32 v17,v17,v65
v_add_f32 v18,v18,v66
v_add_f32 v19,v19,v67
v_cvt_pk_bf16_f32 v60,v12,v13
v_cvt_pk_bf16_f32 v61,v14,v15)"
R"(
v_cvt_pk_bf16_f32 v62,v16,v17
v_cvt_pk_bf16_f32 v63,v18,v19
buffer_store_dwordx4 v[60:63],v3,s[36:39],s73 offen offset:0 sc0 sc1
s_cmp_ge_u32 s49,s59
s_cbranch_scc1 Lg4_done1_%=
s_add_u32 m0,s78,2048
s_nop 0
buffer_load_dword v7,s[36:39],s68 offen offset:0 sc1 lds
s_waitcnt vmcnt(3)
s_barrier
ds_read_b32 v11,v8 offset:0
s_mov_b32 s69,2304
s_waitcnt lgkmcnt(0)
v_cmp_eq_u32 vcc,s60,v11
s_nop 0
s_cmp_eq_u32 vcc_lo,-1
s_cbranch_scc1 Lg4_go1_%=
s_add_u32 m0,s78,3072
s_nop 0
buffer_load_dword v7,s[36:39],s68 offen offset:0 sc1 lds
s_waitcnt vmcnt(3)
s_barrier
ds_read_b32 v11,v8 offset:1024
s_mov_b32 s69,3328
s_waitcnt lgkmcnt(0)
v_cmp_eq_u32 vcc,s60,v11
s_nop 0
s_cmp_eq_u32 vcc_lo,-1
s_cbranch_scc1 Lg4_go1_%=
s_add_u32 m0,s78,0
s_nop 0
buffer_load_dword v7,s[36:39],s68 offen offset:0 sc1 lds
s_waitcnt vmcnt(2)
s_barrier
ds_read_b32 v11,v8 offset:2048
s_lshr_b32 s70,s48,4
buffer_store_dword v5,v4,s[36:39],s70 offen offset:0 sc0 sc1
s_mov_b32 s48,s59
s_mov_b32 s69,256
s_waitcnt lgkmcnt(0)
v_cmp_eq_u32 vcc,s60,v11
s_nop 0
s_cmp_eq_u32 vcc_lo,-1
s_cbranch_scc1 Lg4_go1_%=
s_add_u32 m0,s78,1024
s_nop 0
buffer_load_dword v7,s[36:39],s68 offen offset:0 sc1 lds
s_waitcnt vmcnt(3)
s_barrier
ds_read_b32 v11,v8 offset:3072
s_mov_b32 s69,1280
s_waitcnt lgkmcnt(0)
v_cmp_eq_u32 vcc,s60,v11
s_nop 0
s_cmp_eq_u32 vcc_lo,-1
s_cbranch_scc1 Lg4_go1_%=
Lg4_poll1_%=:
s_add_u32 m0,s78,2048
s_nop 0
buffer_load_dword v7,s[36:39],s68 offen offset:0 sc1 lds
s_waitcnt vmcnt(2)
s_barrier
ds_read_b32 v11,v8 offset:0
s_mov_b32 s69,2304
s_waitcnt lgkmcnt(0)
v_cmp_eq_u32 vcc,s60,v11
s_nop 0
s_cmp_eq_u32 vcc_lo,-1
s_cbranch_scc1 Lg4_go1_%=
s_sleep 4
s_add_u32 m0,s78,3072
s_nop 0
buffer_load_dword v7,s[36:39],s68 offen offset:0 sc1 lds
s_waitcnt vmcnt(2)
s_barrier
ds_read_b32 v11,v8 offset:1024
s_mov_b32 s69,3328
s_waitcnt lgkmcnt(0)
v_cmp_eq_u32 vcc,s60,v11
s_nop 0
s_cmp_eq_u32 vcc_lo,-1
s_cbranch_scc1 Lg4_go1_%=
s_sleep 4
s_add_u32 m0,s78,0
s_nop 0
buffer_load_dword v7,s[36:39],s68 offen offset:0 sc1 lds
s_waitcnt vmcnt(2)
s_barrier
ds_read_b32 v11,v8 offset:2048
s_mov_b32 s69,256
s_waitcnt lgkmcnt(0)
v_cmp_eq_u32 vcc,s60,v11
s_nop 0
s_cmp_eq_u32 vcc_lo,-1
s_cbranch_scc1 Lg4_go1_%=
s_sleep 4
s_add_u32 m0,s78,1024
s_nop 0
buffer_load_dword v7,s[36:39],s68 offen offset:0 sc1 lds
s_waitcnt vmcnt(2)
s_barrier
ds_read_b32 v11,v8 offset:3072
s_mov_b32 s69,1280
s_waitcnt lgkmcnt(0)
v_cmp_eq_u32 vcc,s60,v11
s_nop 0
s_cmp_eq_u32 vcc_lo,-1
s_cbranch_scc1 Lg4_go1_%=
s_sleep 4
s_add_u32 s79,s79,1
s_cmp_lt_u32 s79,20000
s_cbranch_scc1 Lg4_poll1_%=
Lg4_go1_%=:
s_mul_i32 s61,s49,7
s_lshr_b32 s61,s61,1
s_cmp_lt_u32 s67,s59
s_cbranch_scc1 Lg4_gs1_%=
s_add_u32 m0,s88,57344
s_nop 0
buffer_load_dwordx4 v0,s[36:39],s61 offen offset:0 sc1 lds
buffer_load_dwordx4 v0,s[36:39],s61 offen offset:1024 sc1 lds
buffer_load_dwordx4 v0,s[36:39],s61 offen offset:2048 sc1 lds
buffer_load_dwordx4 v0,s[36:39],s61 offen offset:3072 sc1 lds
s_add_u32 s61,s61,4096
s_add_u32 m0,s88,61440
s_nop 0
buffer_load_dwordx4 v0,s[36:39],s61 offen offset:0 sc1 lds
buffer_load_dwordx4 v0,s[36:39],s61 offen offset:1024 sc1 lds
buffer_load_dwordx4 v0,s[36:39],s61 offen offset:2048 sc1 lds
s_add_u32 s61,s61,24576
s_add_u32 m0,s88,86016
s_nop 0
buffer_load_dwordx4 v0,s[36:39],s61 offen offset:0 sc1 lds
buffer_load_dwordx4 v0,s[36:39],s61 offen offset:1024 sc1 lds
buffer_load_dwordx4 v0,s[36:39],s61 offen offset:2048 sc1 lds
buffer_load_dwordx4 v0,s[36:39],s61 offen offset:3072 sc1 lds
s_add_u32 s61,s61,4096
s_add_u32 m0,s88,90112
s_nop 0
buffer_load_dwordx4 v0,s[36:39],s61 offen offset:0 sc1 lds
buffer_load_dwordx4 v0,s[36:39],s61 offen offset:1024 sc1 lds
buffer_load_dwordx4 v0,s[36:39],s61 offen offset:2048 sc1 lds
v_add_u32 v60,s69,v8
s_waitcnt vmcnt(7)
s_barrier
ds_read_b32 v11,v60 offset:0
ds_read_b128 v[28:31],v2 offset:0
ds_read_b128 v[32:35],v2 offset:1024
ds_read_b128 v[36:39],v2 offset:2048
ds_read_b128 v[40:43],v2 offset:3072
ds_read_b128 v[44:47],v2 offset:4096
ds_read_b128 v[48:51],v2 offset:5120
ds_read_b128 v[52:55],v2 offset:6144
s_lshr_b32 s70,s48,4
buffer_store_dword v5,v4,s[36:39],s70 offen offset:0 sc0 sc1
s_mov_b32 s48,s59
s_waitcnt lgkmcnt(7)
v_cmp_eq_u32 vcc,s60,v11
s_nop 0
s_cmp_eq_u32 vcc_lo,-1
s_cselect_b32 s67,s50,s59
s_mul_i32 s61,s67,7
s_lshr_b32 s61,s61,1
s_waitcnt lgkmcnt(5)
s_setpc_b64 s[92:93]
Lg4_gs1_%=:
ds_read_b128 v[28:31],v2 offset:0
ds_read_b128 v[32:35],v2 offset:1024
ds_read_b128 v[36:39],v2 offset:2048
ds_read_b128 v[40:43],v2 offset:3072
ds_read_b128 v[44:47],v2 offset:4096
ds_read_b128 v[48:51],v2 offset:5120
ds_read_b128 v[52:55],v2 offset:6144
s_cmp_eq_u32 vcc_hi,-1
s_cselect_b32 s67,s50,s59
s_mul_i32 s61,s67,7
s_lshr_b32 s61,s61,1
s_mov_b32 s65,s59
s_waitcnt lgkmcnt(5)
s_setpc_b64 s[92:93]
Lg4_done1_%=:
s_waitcnt vmcnt(0)
s_barrier
s_lshr_b32 s70,s48,4
buffer_store_dword v5,v4,s[36:39],s70 offen offset:0 sc0 sc1
s_branch Lg4_done_%=
Lg4_done_%=:
s_waitcnt vmcnt(0) lgkmcnt(0)
s_barrier
s_endpgm
Lcode_g4_e:)"
  :: "s"(ws), "s"(wave), "s"(sStep), "s"(sX1), "s"(sX2), "s"(sU0), "s"(sWw), "s"(sBuf), "v"(vSrc), "v"(vA), "v"(vOut), "v"(vFlag), "v"(vSnapOff), "v"(vPollOff), "v"(vSnapRd), "v"(vAuxS), "v"(vWd0), "v"(vWd1), "v"(vWr0), "v"(vWr1), "v"(vWr2), "s"(w4) : "v0", "v1", "v2", "v3", "v4", "v5", "v6", "v7", "v8", "v9", "v10", "v11", "v12", "v13", "v14", "v15", "v16", "v17", "v18", "v19", "v20", "v21", "v22", "v23", "v24", "v25", "v26", "v27", "v28", "v29", "v30", "v31", "v32", "v33", "v34", "v35", "v36", "v37", "v38", "v39", "v40", "v41", "v42", "v43", "v44", "v45", "v46", "v47", "v48", "v49", "v50", "v51", "v52", "v53", "v54", "v55", "v56", "v57", "v58", "v59", "v60", "v61", "v62", "v63", "v64", "v65", "v66", "v67", "v68", "v69", "v70", "v71", "v72", "v73", "v74", "v75", "v76", "v77", "v78", "v79", "v80", "v81", "v82", "v83", "v84", "v85", "v86", "v87", "v88", "v89", "v90", "v91", "v92", "v93", "v94", "v95", "v96", "v97", "v98", "v99", "v100", "v101", "v102", "v103", "v104", "v105", "v106", "v107", "v108", "v109", "v110", "v111", "v112", "v113", "v114", "v115", "v116", "v117", "v118", "v119", "v120", "v121", "v122", "v123", "v124", "v125", "v126", "v127", "v128", "v129", "v130", "v131", "v132", "v133", "v134", "v135", "v136", "v137", "v138", "v139", "v140", "v141", "v142", "v143", "v144", "v145", "v146", "v147", "v148", "v149", "v150", "v151", "v152", "v153", "v154", "v155", "v156", "v157", "v158", "v159", "v160", "v161", "v162", "v163", "v164", "v165", "v166", "v167", "v168", "v169", "v170", "v171", "v172", "v173", "v174", "v175", "v176", "v177", "v178", "v179", "v180", "v181", "v182", "v183", "v184", "v185", "v186", "v187", "v188", "v189", "v190", "v191", "v192", "v193", "v194", "v195", "v196", "v197", "v198", "v199", "v200", "v201", "v202", "v203", "v204", "v205", "v206", "v207", "v208", "v209", "v210", "v211", "v212", "v213", "v214", "v215", "v216", "v217", "v218", "v219", "v220", "v221", "v222", "v223", "v224", "v225", "v226", "v227", "v228", "v229", "v230", "v231", "v255", "a0", "a1", "a2", "a3", "a4", "a5", "a6", "a7", "a8", "a9", "a10", "a11", "a12", "a13", "a14", "a15", "a16", "a17", "a18", "a19", "a20", "a21", "a22", "a23", "a24", "a25", "a26", "a27", "a28", "a29", "a30", "a31", "a32", "a33", "a34", "a35", "a36", "a37", "a38", "a39", "a40", "a41", "a42", "a43", "a44", "a45", "a46", "a47", "a48", "a49", "a50", "a51", "a52", "a53", "a54", "a55", "a56", "a57", "a58", "a59", "a60", "a61", "a62", "a63", "a64", "a65", "a66", "a67", "a68", "a69", "a70", "a71", "a72", "a73", "a74", "a75", "a76", "a77", "a78", "a79", "a80", "a81", "a82", "a83", "a84", "a85", "a86", "a87", "a88", "a89", "a90", "a91", "a92", "a93", "a94", "a95", "a96", "a97", "a98", "a99", "a100", "a101", "a102", "a103", "a104", "a105", "a106", "a107", "a108", "a109", "a110", "a111", "a112", "a113", "a114", "a115", "a116", "a117", "a118", "a119", "a120", "a121", "a122", "a123", "a124", "a125", "a126", "a127", "a128", "a129", "a130", "a131", "a132", "a133", "a134", "a135", "a136", "a137", "a138", "a139", "a140", "a141", "a142", "a143", "a144", "a145", "a146", "a147", "a148", "a149", "a150", "a151", "a152", "a153", "a154", "a155", "a156", "a157", "a158", "a159", "a160", "a161", "a162", "a163", "a164", "a165", "a166", "a167", "a168", "a169", "a170", "a171", "a172", "a173", "a174", "a175", "a176", "a177", "a178", "a179", "a180", "a181", "a182", "a183", "a184", "a185", "a186", "a187", "a188", "a189", "a190", "a191", "a192", "a193", "a194", "a195", "a196", "a197", "a198", "a199", "a200", "a201", "a202", "a203", "a204", "a205", "a206", "a207", "a208", "a209", "a210", "a211", "a212", "a213", "a214", "a215", "a216", "a217", "a218", "a219", "a220", "a221", "a222", "a223", "a224", "a225", "a226", "a227", "a228", "a229", "a230", "a231", "a232", "a233", "a234", "a235", "a236", "a237", "a238", "a239", "a240", "a241", "a242", "a243", "a244", "a245", "a246", "a247", "a248", "a249", "a250", "a251", "a252", "a253", "a254", "a255", "s36", "s37", "s38", "s39", "s40", "s41", "s42", "s43", "s44", "s45", "s46", "s47", "s48", "s49", "s50", "s51", "s52", "s53", "s54", "s55", "s56", "s57", "s58", "s59", "s60", "s61", "s62", "s63", "s64", "s65", "s66", "s67", "s68", "s69", "s70", "s71", "s72", "s73", "s74", "s75", "s76", "s77", "s78", "s79", "s80", "s81", "s82", "s83", "s84", "s85", "s86", "s87", "s88", "s89", "s90", "s91", "s92", "s93", "s94", "s95", "s96", "s97", "s98", "s99", "vcc", "scc", "memory");
    __builtin_unreachable();
  } else {
   if (kind != 6) {
    COMMON(kind == 1 ? 8 : 16, kind == 1 ? OFF_FXP : OFF_FH, (kind == 1 ? OFF_FH : OFF_FX1) + slice * 128, AUX + 1536, 2048, 2048, 4);
    const int dl = kind == 2 ? ((wave & 2) ? -1 : 1) : 0, r0 = 16 + l15 - dl, r1 = 16 + l15 - 2 * dl;
    const unsigned sb = (kind == 1 ? OFF_XP : OFF_HS) - 32768 + wave * 4096 + lg * 256;
    const unsigned vSrc = sb + (r0 >> 4) * 32768 + (r0 & 15) * 16, vSrc2 = sb + 16384 + (r1 >> 4) * 32768 + (r1 & 15) * 16;
    const unsigned vOut = (kind == 1 ? OFF_HS : OFF_X1) + (slice * 4 + wave) * 1024 + vA;
    const unsigned vX = AUX + wave * 2048 + (kind == 1 ? l15 * 16 : vA), vX2 = AUX + wave * 2048 + l15 * 8;
    const unsigned vAuxS = kind == 1 ? OFF_S1 + ln * 4 : OFF_XP + (slice * 4 + wave) * 1024 + vA;
    const unsigned vAuxS2 = ln < 32 ? OFF_S1 - 1024 + 256 + ln * 4 : OOB;
    constexpr unsigned RED = AUX + 24576 + 1536;
    const unsigned vOut2 = wave != 2 ? OOB : kind == 1 ? (slice == 0 && ln < 16 ? OFF_S1 + 256 + ln * 8 : OOB) : ln < 32 ? OFF_ST + slice * 128 + ln * 4 : OOB;
    const unsigned vStW = RED + wave * 4 + (kind == 1 ? l15 * 32 + (lg == lq ? 16 : 0) : (l15 * 2 + (lg & 1)) * 16), vStR = RED + (kind == 1 ? l15 * 32 : (ln & 31) * 16);
    const unsigned sWw = 16 * wave * 2048 + slice * 256;
    const u16* w12 = kind == 1 ? w1 : w2;
    unsigned epOut;
    asm volatile(R"(
Lcode_g12_b:
s_cmp_eq_u32 %8,2
s_cbranch_scc1 Lg2_begin_%=
s_mov_b64 s[36:37],%1
s_mov_b32 s38,111193216
s_mov_b32 s39,0x00020000
s_mov_b32 s54,%2
s_mov_b32 s55,%3
s_mov_b32 s56,%4
s_mov_b32 s58,%5
s_mov_b32 s83,%6
s_mov_b32 s82,%7
s_mov_b32 s84,%8
v_mov_b32 v0,%9
v_mov_b32 v1,%10
v_mov_b32 v2,%11
v_mov_b32 v3,%12
v_mov_b32 v4,%13
v_mov_b32 v6,%14
v_mov_b32 v7,%15
v_mov_b32 v8,%16
v_mov_b32 v9,%17
v_mov_b32 v10,%18
v_mov_b32 v11,%19
v_mov_b32 v12,%20
v_mov_b32 v13,%21
v_mov_b32 v14,%22
v_mov_b32 v28,%23
v_mov_b32 v29,%24
v_mov_b32 v30,%25
v_mov_b32 v31,%26
v_mov_b32 v32,%27
s_mov_b64 s[40:41],%28
s_mov_b32 s42,2097152
s_mov_b32 s43,0x00020000
s_lshl_b32 s88,s54,11
s_mov_b32 s69,111193088
buffer_load_dword v5,off,s[36:39],s69 sc1
s_add_u32 m0,s88,0
s_add_u32 s61,s82,0
buffer_load_dwordx4 v28,s[40:43],s61 offen offset:0  lds
s_add_u32 m0,s88,1024
s_nop 0
buffer_load_dwordx4 v29,s[40:43],s61 offen offset:0  lds
s_add_u32 m0,s88,8064
s_nop 0
buffer_load_dwordx4 v28,s[40:43],s61 offen offset:128  lds
s_add_u32 m0,s88,9088
s_nop 0
buffer_load_dwordx4 v29,s[40:43],s61 offen offset:128  lds
s_add_u32 m0,s88,16384
s_add_u32 s61,s82,131072
buffer_load_dwordx4 v28,s[40:43],s61 offen offset:0  lds
s_add_u32 m0,s88,17408
s_nop 0
buffer_load_dwordx4 v29,s[40:43],s61 offen offset:0  lds
s_add_u32 m0,s88,24448
s_nop 0
buffer_load_dwordx4 v28,s[40:43],s61 offen offset:128  lds
s_add_u32 m0,s88,25472
s_nop 0
buffer_load_dwordx4 v29,s[40:43],s61 offen offset:128  lds
s_add_u32 m0,s88,32768
s_add_u32 s61,s82,262144
buffer_load_dwordx4 v28,s[40:43],s61 offen offset:0  lds
s_add_u32 m0,s88,33792
s_nop 0
buffer_load_dwordx4 v29,s[40:43],s61 offen offset:0  lds
s_add_u32 m0,s88,40832
s_nop 0
buffer_load_dwordx4 v28,s[40:43],s61 offen offset:128  lds
s_add_u32 m0,s88,41856
s_nop 0
buffer_load_dwordx4 v29,s[40:43],s61 offen offset:128  lds
s_add_u32 m0,s88,49152
s_add_u32 s61,s82,393216
buffer_load_dwordx4 v28,s[40:43],s61 offen offset:0  lds
s_add_u32 m0,s88,50176
s_nop 0
buffer_load_dwordx4 v29,s[40:43],s61 offen offset:0  lds
s_add_u32 m0,s88,57216
s_nop 0
buffer_load_dwordx4 v28,s[40:43],s61 offen offset:128  lds
s_add_u32 m0,s88,58240
s_nop 0
buffer_load_dwordx4 v29,s[40:43],s61 offen offset:128  lds
s_add_u32 m0,s88,65536
s_add_u32 s61,s82,524288
buffer_load_dwordx4 v28,s[40:43],s61 offen offset:0  lds
s_add_u32 m0,s88,66560
s_nop 0
buffer_load_dwordx4 v29,s[40:43],s61 offen offset:0  lds
s_add_u32 m0,s88,73600
s_nop 0
buffer_load_dwordx4 v28,s[40:43],s61 offen offset:128  lds
s_add_u32 m0,s88,74624
s_nop 0
buffer_load_dwordx4 v29,s[40:43],s61 offen offset:128  lds
s_add_u32 m0,s88,81920
s_add_u32 s61,s82,655360
buffer_load_dwordx4 v28,s[40:43],s61 offen offset:0  lds
s_add_u32 m0,s88,82944
s_nop 0
buffer_load_dwordx4 v29,s[40:43],s61 offen offset:0  lds
s_add_u32 m0,s88,89984
s_nop 0
buffer_load_dwordx4 v28,s[40:43],s61 offen offset:128  lds
s_add_u32 m0,s88,91008
s_nop 0
buffer_load_dwordx4 v29,s[40:43],s61 offen offset:128  lds
s_add_u32 m0,s88,98304
s_add_u32 s61,s82,786432
buffer_load_dwordx4 v28,s[40:43],s61 offen offset:0  lds
s_add_u32 m0,s88,99328
s_nop 0
buffer_load_dwordx4 v29,s[40:43],s61 offen offset:0  lds
s_add_u32 m0,s88,106368
s_nop 0
buffer_load_dwordx4 v28,s[40:43],s61 offen offset:128  lds
s_add_u32 m0,s88,107392
s_nop 0
buffer_load_dwordx4 v29,s[40:43],s61 offen offset:128  lds
v_add_u32 v36,49152,v30
v_add_u32 v37,49152,v31
v_add_u32 v38,98304,v30
v_add_u32 v39,98304,v31
v_mov_b32 v76,0x3f803f80
v_mov_b32 v77,0x3f803f80
v_mov_b32 v78,0x3f803f80
v_mov_b32 v79,0x3f803f80
s_waitcnt vmcnt(24)
s_barrier
s_add_u32 m0,s88,114688
s_add_u32 s61,s82,917504
buffer_load_dwordx4 v28,s[40:43],s61 offen offset:0  lds
s_add_u32 m0,s88,115712
ds_read_b64_tr_b16 a[0:1],v30 offset:0
buffer_load_dwordx4 v29,s[40:43],s61 offen offset:0  lds
s_add_u32 m0,s88,122752
ds_read_b64_tr_b16 a[2:3],v30 offset:512
buffer_load_dwordx4 v28,s[40:43],s61 offen offset:128  lds
s_add_u32 m0,s88,123776
ds_read_b64_tr_b16 a[4:5],v31 offset:0
buffer_load_dwordx4 v29,s[40:43],s61 offen offset:128  lds
ds_read_b64_tr_b16 a[6:7],v31 offset:512
ds_read_b64_tr_b16 a[8:9],v30 offset:4096
ds_read_b64_tr_b16 a[10:11],v30 offset:4608
ds_read_b64_tr_b16 a[12:13],v31 offset:4096
ds_read_b64_tr_b16 a[14:15],v31 offset:4608
s_waitcnt vmcnt(24)
s_barrier
s_add_u32 m0,s88,131072
s_add_u32 s61,s82,1048576
buffer_load_dwordx4 v28,s[40:43],s61 offen offset:0  lds
s_add_u32 m0,s88,132096
ds_read_b64_tr_b16 a[16:17],v30 offset:16384
buffer_load_dwordx4 v29,s[40:43],s61 offen offset:0  lds
s_add_u32 m0,s88,139136
ds_read_b64_tr_b16 a[18:19],v30 offset:16896
buffer_load_dwordx4 v28,s[40:43],s61 offen offset:128  lds
s_add_u32 m0,s88,140160
ds_read_b64_tr_b16 a[20:21],v31 offset:16384
buffer_load_dwordx4 v29,s[40:43],s61 offen offset:128  lds
ds_read_b64_tr_b16 a[22:23],v31 offset:16896
ds_read_b64_tr_b16 a[24:25],v30 offset:20480
ds_read_b64_tr_b16 a[26:27],v30 offset:20992
ds_read_b64_tr_b16 a[28:29],v31 offset:20480
ds_read_b64_tr_b16 a[30:31],v31 offset:20992
s_waitcnt lgkmcnt(8)
v_mfma_f32_16x16x32_bf16 v[20:23],a[0:3],v[76:79],0
v_mfma_f32_16x16x32_bf16 v[24:27],a[4:7],v[76:79],0
v_mfma_f32_16x16x32_bf16 v[20:23],a[8:11],v[76:79],v[20:23]
v_mfma_f32_16x16x32_bf16 v[24:27],a[12:15],v[76:79],v[24:27]
s_waitcnt vmcnt(24)
s_barrier
s_add_u32 m0,s88,0
s_add_u32 s61,s82,1179648
buffer_load_dwordx4 v28,s[40:43],s61 offen offset:0  lds
s_add_u32 m0,s88,1024
ds_read_b64_tr_b16 a[32:33],v30 offset:32768
buffer_load_dwordx4 v29,s[40:43],s61 offen offset:0  lds
s_add_u32 m0,s88,8064
ds_read_b64_tr_b16 a[34:35],v30 offset:33280
buffer_load_dwordx4 v28,s[40:43],s61 offen offset:128  lds
s_add_u32 m0,s88,9088
ds_read_b64_tr_b16 a[36:37],v31 offset:32768
buffer_load_dwordx4 v29,s[40:43],s61 offen offset:128  lds
ds_read_b64_tr_b16 a[38:39],v31 offset:33280
ds_read_b64_tr_b16 a[40:41],v30 offset:36864
ds_read_b64_tr_b16 a[42:43],v30 offset:37376
ds_read_b64_tr_b16 a[44:45],v31 offset:36864
ds_read_b64_tr_b16 a[46:47],v31 offset:37376
s_waitcnt lgkmcnt(8)
v_mfma_f32_16x16x32_bf16 v[20:23],a[16:19],v[76:79],v[20:23]
v_mfma_f32_16x16x32_bf16 v[24:27],a[20:23],v[76:79],v[24:27]
v_mfma_f32_16x16x32_bf16 v[20:23],a[24:27],v[76:79],v[20:23]
v_mfma_f32_16x16x32_bf16 v[24:27],a[28:31],v[76:79],v[24:27]
s_waitcnt vmcnt(24)
s_barrier
s_add_u32 m0,s88,16384)"
R"(
s_add_u32 s61,s82,1310720
buffer_load_dwordx4 v28,s[40:43],s61 offen offset:0  lds
s_add_u32 m0,s88,17408
ds_read_b64_tr_b16 a[48:49],v36 offset:0
buffer_load_dwordx4 v29,s[40:43],s61 offen offset:0  lds
s_add_u32 m0,s88,24448
ds_read_b64_tr_b16 a[50:51],v36 offset:512
buffer_load_dwordx4 v28,s[40:43],s61 offen offset:128  lds
s_add_u32 m0,s88,25472
ds_read_b64_tr_b16 a[52:53],v37 offset:0
buffer_load_dwordx4 v29,s[40:43],s61 offen offset:128  lds
ds_read_b64_tr_b16 a[54:55],v37 offset:512
ds_read_b64_tr_b16 a[56:57],v36 offset:4096
ds_read_b64_tr_b16 a[58:59],v36 offset:4608
ds_read_b64_tr_b16 a[60:61],v37 offset:4096
ds_read_b64_tr_b16 a[62:63],v37 offset:4608
s_waitcnt lgkmcnt(8)
v_mfma_f32_16x16x32_bf16 v[20:23],a[32:35],v[76:79],v[20:23]
v_mfma_f32_16x16x32_bf16 v[24:27],a[36:39],v[76:79],v[24:27]
v_mfma_f32_16x16x32_bf16 v[20:23],a[40:43],v[76:79],v[20:23]
v_mfma_f32_16x16x32_bf16 v[24:27],a[44:47],v[76:79],v[24:27]
s_waitcnt vmcnt(24)
s_barrier
s_add_u32 m0,s88,32768
s_add_u32 s61,s82,1441792
buffer_load_dwordx4 v28,s[40:43],s61 offen offset:0  lds
s_add_u32 m0,s88,33792
ds_read_b64_tr_b16 a[64:65],v36 offset:16384
buffer_load_dwordx4 v29,s[40:43],s61 offen offset:0  lds
s_add_u32 m0,s88,40832
ds_read_b64_tr_b16 a[66:67],v36 offset:16896
buffer_load_dwordx4 v28,s[40:43],s61 offen offset:128  lds
s_add_u32 m0,s88,41856
ds_read_b64_tr_b16 a[68:69],v37 offset:16384
buffer_load_dwordx4 v29,s[40:43],s61 offen offset:128  lds
ds_read_b64_tr_b16 a[70:71],v37 offset:16896
ds_read_b64_tr_b16 a[72:73],v36 offset:20480
ds_read_b64_tr_b16 a[74:75],v36 offset:20992
ds_read_b64_tr_b16 a[76:77],v37 offset:20480
ds_read_b64_tr_b16 a[78:79],v37 offset:20992
s_waitcnt lgkmcnt(8)
v_mfma_f32_16x16x32_bf16 v[20:23],a[48:51],v[76:79],v[20:23]
v_mfma_f32_16x16x32_bf16 v[24:27],a[52:55],v[76:79],v[24:27]
v_mfma_f32_16x16x32_bf16 v[20:23],a[56:59],v[76:79],v[20:23]
v_mfma_f32_16x16x32_bf16 v[24:27],a[60:63],v[76:79],v[24:27]
s_waitcnt vmcnt(24)
s_barrier
s_add_u32 m0,s88,49152
s_add_u32 s61,s82,1572864
buffer_load_dwordx4 v28,s[40:43],s61 offen offset:0  lds
s_add_u32 m0,s88,50176
ds_read_b64_tr_b16 a[80:81],v36 offset:32768
buffer_load_dwordx4 v29,s[40:43],s61 offen offset:0  lds
s_add_u32 m0,s88,57216
ds_read_b64_tr_b16 a[82:83],v36 offset:33280
buffer_load_dwordx4 v28,s[40:43],s61 offen offset:128  lds
s_add_u32 m0,s88,58240
ds_read_b64_tr_b16 a[84:85],v37 offset:32768
buffer_load_dwordx4 v29,s[40:43],s61 offen offset:128  lds
ds_read_b64_tr_b16 a[86:87],v37 offset:33280
ds_read_b64_tr_b16 a[88:89],v36 offset:36864
ds_read_b64_tr_b16 a[90:91],v36 offset:37376
ds_read_b64_tr_b16 a[92:93],v37 offset:36864
ds_read_b64_tr_b16 a[94:95],v37 offset:37376
s_waitcnt lgkmcnt(8)
v_mfma_f32_16x16x32_bf16 v[20:23],a[64:67],v[76:79],v[20:23]
v_mfma_f32_16x16x32_bf16 v[24:27],a[68:71],v[76:79],v[24:27]
v_mfma_f32_16x16x32_bf16 v[20:23],a[72:75],v[76:79],v[20:23]
v_mfma_f32_16x16x32_bf16 v[24:27],a[76:79],v[76:79],v[24:27]
s_waitcnt vmcnt(24)
s_barrier
s_add_u32 m0,s88,65536
s_add_u32 s61,s82,1703936
buffer_load_dwordx4 v28,s[40:43],s61 offen offset:0  lds
s_add_u32 m0,s88,66560
ds_read_b64_tr_b16 a[96:97],v38 offset:0
buffer_load_dwordx4 v29,s[40:43],s61 offen offset:0  lds
s_add_u32 m0,s88,73600
ds_read_b64_tr_b16 a[98:99],v38 offset:512
buffer_load_dwordx4 v28,s[40:43],s61 offen offset:128  lds
s_add_u32 m0,s88,74624
ds_read_b64_tr_b16 a[100:101],v39 offset:0
buffer_load_dwordx4 v29,s[40:43],s61 offen offset:128  lds
ds_read_b64_tr_b16 a[102:103],v39 offset:512
ds_read_b64_tr_b16 a[104:105],v38 offset:4096
ds_read_b64_tr_b16 a[106:107],v38 offset:4608
ds_read_b64_tr_b16 a[108:109],v39 offset:4096
ds_read_b64_tr_b16 a[110:111],v39 offset:4608
s_waitcnt lgkmcnt(8)
v_mfma_f32_16x16x32_bf16 v[20:23],a[80:83],v[76:79],v[20:23]
v_mfma_f32_16x16x32_bf16 v[24:27],a[84:87],v[76:79],v[24:27]
v_mfma_f32_16x16x32_bf16 v[20:23],a[88:91],v[76:79],v[20:23]
v_mfma_f32_16x16x32_bf16 v[24:27],a[92:95],v[76:79],v[24:27]
s_waitcnt vmcnt(24)
s_barrier
s_add_u32 m0,s88,81920
s_add_u32 s61,s82,1835008
buffer_load_dwordx4 v28,s[40:43],s61 offen offset:0  lds
s_add_u32 m0,s88,82944
ds_read_b64_tr_b16 a[112:113],v38 offset:16384
buffer_load_dwordx4 v29,s[40:43],s61 offen offset:0  lds
s_add_u32 m0,s88,89984
ds_read_b64_tr_b16 a[114:115],v38 offset:16896
buffer_load_dwordx4 v28,s[40:43],s61 offen offset:128  lds
s_add_u32 m0,s88,91008
ds_read_b64_tr_b16 a[116:117],v39 offset:16384
buffer_load_dwordx4 v29,s[40:43],s61 offen offset:128  lds
ds_read_b64_tr_b16 a[118:119],v39 offset:16896
ds_read_b64_tr_b16 a[120:121],v38 offset:20480
ds_read_b64_tr_b16 a[122:123],v38 offset:20992
ds_read_b64_tr_b16 a[124:125],v39 offset:20480
ds_read_b64_tr_b16 a[126:127],v39 offset:20992
s_waitcnt lgkmcnt(8)
v_mfma_f32_16x16x32_bf16 v[20:23],a[96:99],v[76:79],v[20:23]
v_mfma_f32_16x16x32_bf16 v[24:27],a[100:103],v[76:79],v[24:27]
v_mfma_f32_16x16x32_bf16 v[20:23],a[104:107],v[76:79],v[20:23]
v_mfma_f32_16x16x32_bf16 v[24:27],a[108:111],v[76:79],v[24:27]
s_waitcnt vmcnt(24)
s_barrier
s_add_u32 m0,s88,98304
s_add_u32 s61,s82,1966080
buffer_load_dwordx4 v28,s[40:43],s61 offen offset:0  lds
s_add_u32 m0,s88,99328
ds_read_b64_tr_b16 a[128:129],v38 offset:32768
buffer_load_dwordx4 v29,s[40:43],s61 offen offset:0  lds
s_add_u32 m0,s88,106368
ds_read_b64_tr_b16 a[130:131],v38 offset:33280
buffer_load_dwordx4 v28,s[40:43],s61 offen offset:128  lds
s_add_u32 m0,s88,107392
ds_read_b64_tr_b16 a[132:133],v39 offset:32768
buffer_load_dwordx4 v29,s[40:43],s61 offen offset:128  lds
ds_read_b64_tr_b16 a[134:135],v39 offset:33280
ds_read_b64_tr_b16 a[136:137],v38 offset:36864
ds_read_b64_tr_b16 a[138:139],v38 offset:37376
ds_read_b64_tr_b16 a[140:141],v39 offset:36864
ds_read_b64_tr_b16 a[142:143],v39 offset:37376
s_waitcnt lgkmcnt(8)
v_mfma_f32_16x16x32_bf16 v[20:23],a[112:115],v[76:79],v[20:23]
v_mfma_f32_16x16x32_bf16 v[24:27],a[116:119],v[76:79],v[24:27]
v_mfma_f32_16x16x32_bf16 v[20:23],a[120:123],v[76:79],v[20:23]
v_mfma_f32_16x16x32_bf16 v[24:27],a[124:127],v[76:79],v[24:27]
s_waitcnt vmcnt(24)
s_barrier
ds_read_b64_tr_b16 a[144:145],v30 offset:0
ds_read_b64_tr_b16 a[146:147],v30 offset:512
ds_read_b64_tr_b16 a[148:149],v31 offset:0
ds_read_b64_tr_b16 a[150:151],v31 offset:512
ds_read_b64_tr_b16 a[152:153],v30 offset:4096
ds_read_b64_tr_b16 a[154:155],v30 offset:4608
ds_read_b64_tr_b16 a[156:157],v31 offset:4096
ds_read_b64_tr_b16 a[158:159],v31 offset:4608
s_waitcnt lgkmcnt(8)
v_mfma_f32_16x16x32_bf16 v[20:23],a[128:131],v[76:79],v[20:23]
v_mfma_f32_16x16x32_bf16 v[24:27],a[132:135],v[76:79],v[24:27]
v_mfma_f32_16x16x32_bf16 v[20:23],a[136:139],v[76:79],v[20:23]
v_mfma_f32_16x16x32_bf16 v[24:27],a[140:143],v[76:79],v[24:27]
s_waitcnt vmcnt(20)
s_barrier
ds_read_b64_tr_b16 a[160:161],v30 offset:16384
ds_read_b64_tr_b16 a[162:163],v30 offset:16896
ds_read_b64_tr_b16 a[164:165],v31 offset:16384
ds_read_b64_tr_b16 a[166:167],v31 offset:16896
ds_read_b64_tr_b16 a[168:169],v30 offset:20480
ds_read_b64_tr_b16 a[170:171],v30 offset:20992
ds_read_b64_tr_b16 a[172:173],v31 offset:20480
ds_read_b64_tr_b16 a[174:175],v31 offset:20992
s_waitcnt lgkmcnt(8)
v_mfma_f32_16x16x32_bf16 v[20:23],a[144:147],v[76:79],v[20:23]
v_mfma_f32_16x16x32_bf16 v[24:27],a[148:151],v[76:79],v[24:27]
v_mfma_f32_16x16x32_bf16 v[20:23],a[152:155],v[76:79],v[20:23]
v_mfma_f32_16x16x32_bf16 v[24:27],a[156:159],v[76:79],v[24:27]
s_waitcnt vmcnt(16)
s_barrier
ds_read_b64_tr_b16 a[176:177],v30 offset:32768
ds_read_b64_tr_b16 a[178:179],v30 offset:33280
ds_read_b64_tr_b16 a[180:181],v31 offset:32768
ds_read_b64_tr_b16 a[182:183],v31 offset:33280
ds_read_b64_tr_b16 a[184:185],v30 offset:36864
ds_read_b64_tr_b16 a[186:187],v30 offset:37376
ds_read_b64_tr_b16 a[188:189],v31 offset:36864
ds_read_b64_tr_b16 a[190:191],v31 offset:37376
s_waitcnt lgkmcnt(8)
v_mfma_f32_16x16x32_bf16 v[20:23],a[160:163],v[76:79],v[20:23]
v_mfma_f32_16x16x32_bf16 v[24:27],a[164:167],v[76:79],v[24:27]
v_mfma_f32_16x16x32_bf16 v[20:23],a[168:171],v[76:79],v[20:23]
v_mfma_f32_16x16x32_bf16 v[24:27],a[172:175],v[76:79],v[24:27]
s_waitcnt vmcnt(12)
s_barrier
ds_read_b64_tr_b16 a[192:193],v36 offset:0
ds_read_b64_tr_b16 a[194:195],v36 offset:512
ds_read_b64_tr_b16 a[196:197],v37 offset:0
ds_read_b64_tr_b16 a[198:199],v37 offset:512
ds_read_b64_tr_b16 a[200:201],v36 offset:4096
ds_read_b64_tr_b16 a[202:203],v36 offset:4608
ds_read_b64_tr_b16 a[204:205],v37 offset:4096
ds_read_b64_tr_b16 a[206:207],v37 offset:4608
s_waitcnt lgkmcnt(8)
v_mfma_f32_16x16x32_bf16 v[20:23],a[176:179],v[76:79],v[20:23]
v_mfma_f32_16x16x32_bf16 v[24:27],a[180:183],v[76:79],v[24:27]
v_mfma_f32_16x16x32_bf16 v[20:23],a[184:187],v[76:79],v[20:23])"
R"(
v_mfma_f32_16x16x32_bf16 v[24:27],a[188:191],v[76:79],v[24:27]
s_waitcnt vmcnt(8)
s_barrier
ds_read_b64_tr_b16 a[208:209],v36 offset:16384
ds_read_b64_tr_b16 a[210:211],v36 offset:16896
ds_read_b64_tr_b16 a[212:213],v37 offset:16384
ds_read_b64_tr_b16 a[214:215],v37 offset:16896
ds_read_b64_tr_b16 a[216:217],v36 offset:20480
ds_read_b64_tr_b16 a[218:219],v36 offset:20992
ds_read_b64_tr_b16 a[220:221],v37 offset:20480
ds_read_b64_tr_b16 a[222:223],v37 offset:20992
s_waitcnt lgkmcnt(8)
v_mfma_f32_16x16x32_bf16 v[20:23],a[192:195],v[76:79],v[20:23]
v_mfma_f32_16x16x32_bf16 v[24:27],a[196:199],v[76:79],v[24:27]
v_mfma_f32_16x16x32_bf16 v[20:23],a[200:203],v[76:79],v[20:23]
v_mfma_f32_16x16x32_bf16 v[24:27],a[204:207],v[76:79],v[24:27]
s_waitcnt vmcnt(4)
s_barrier
ds_read_b64_tr_b16 a[224:225],v36 offset:32768
ds_read_b64_tr_b16 a[226:227],v36 offset:33280
ds_read_b64_tr_b16 a[228:229],v37 offset:32768
ds_read_b64_tr_b16 a[230:231],v37 offset:33280
ds_read_b64_tr_b16 a[232:233],v36 offset:36864
ds_read_b64_tr_b16 a[234:235],v36 offset:37376
ds_read_b64_tr_b16 a[236:237],v37 offset:36864
ds_read_b64_tr_b16 a[238:239],v37 offset:37376
s_waitcnt lgkmcnt(8)
v_mfma_f32_16x16x32_bf16 v[20:23],a[208:211],v[76:79],v[20:23]
v_mfma_f32_16x16x32_bf16 v[24:27],a[212:215],v[76:79],v[24:27]
v_mfma_f32_16x16x32_bf16 v[20:23],a[216:219],v[76:79],v[20:23]
v_mfma_f32_16x16x32_bf16 v[24:27],a[220:223],v[76:79],v[24:27]
s_waitcnt vmcnt(0)
s_barrier
ds_read_b64_tr_b16 a[240:241],v38 offset:0
ds_read_b64_tr_b16 a[242:243],v38 offset:512
ds_read_b64_tr_b16 a[244:245],v39 offset:0
ds_read_b64_tr_b16 a[246:247],v39 offset:512
ds_read_b64_tr_b16 a[248:249],v38 offset:4096
ds_read_b64_tr_b16 a[250:251],v38 offset:4608
ds_read_b64_tr_b16 a[252:253],v39 offset:4096
ds_read_b64_tr_b16 a[254:255],v39 offset:4608
s_waitcnt lgkmcnt(8)
v_mfma_f32_16x16x32_bf16 v[20:23],a[224:227],v[76:79],v[20:23]
v_mfma_f32_16x16x32_bf16 v[24:27],a[228:231],v[76:79],v[24:27]
v_mfma_f32_16x16x32_bf16 v[20:23],a[232:235],v[76:79],v[20:23]
v_mfma_f32_16x16x32_bf16 v[24:27],a[236:239],v[76:79],v[24:27]
s_waitcnt lgkmcnt(0)
v_mfma_f32_16x16x32_bf16 v[20:23],a[240:243],v[76:79],v[20:23]
v_mfma_f32_16x16x32_bf16 v[24:27],a[244:247],v[76:79],v[24:27]
v_mfma_f32_16x16x32_bf16 v[20:23],a[248:251],v[76:79],v[20:23]
v_mfma_f32_16x16x32_bf16 v[24:27],a[252:255],v[76:79],v[24:27]
s_waitcnt lgkmcnt(0)
s_barrier
v_readfirstlane_b32 s60,v5
s_nop 3
s_add_u32 s60,s60,1
s_cmp_eq_u32 s60,0
s_cselect_b32 s60,1,s60
v_mov_b32 v5,s60
s_mov_b32 s59,536870912
s_add_u32 s57,s56,131072
s_mov_b32 s79,0
s_mov_b32 s75,0x3a800000
s_mov_b32 s76,0xc0135761
s_mov_b32 s77,0x3d372713
s_mov_b32 s44,s59
s_mov_b32 s45,s59
s_mov_b32 s46,s59
s_mov_b32 s47,s59
s_mov_b32 s48,s83
s_add_u32 s49,s48,s55
s_cmp_eq_u32 s49,s56
s_cselect_b32 s49,s57,s49
s_cmp_eq_u32 s49,s58
s_cselect_b32 s49,s59,s49
s_add_u32 s50,s49,s55
s_cmp_eq_u32 s50,s56
s_cselect_b32 s50,s57,s50
s_cmp_eq_u32 s50,s58
s_cselect_b32 s50,s59,s50
s_add_u32 s51,s50,s55
s_cmp_eq_u32 s51,s56
s_cselect_b32 s51,s57,s51
s_cmp_eq_u32 s51,s58
s_cselect_b32 s51,s59,s51
s_add_u32 s52,s51,s55
s_cmp_eq_u32 s52,s56
s_cselect_b32 s52,s57,s52
s_cmp_eq_u32 s52,s58
s_cselect_b32 s52,s59,s52
s_lshl_b32 s63,s54,12
s_add_u32 s63,s63,65536
s_add_u32 s64,s63,32768
s_lshl_b32 s88,s54,10
s_add_u32 s88,s88,131072
s_lshl_b32 s78,s54,11
s_add_u32 s78,s78,132608
v_mov_b32 v15,0x3727c5ac
v_mov_b32 v100,0x3f803f80
v_mov_b32 v101,0x3f803f80
v_mov_b32 v102,0x3f803f80
v_mov_b32 v103,0x3f803f80
s_mov_b32 s89,1048576
s_mov_b32 s82,0xaaaaaaaa
s_mov_b32 s83,0xaaaaaaaa
s_mov_b32 s84,0xcccccccc
s_mov_b32 s85,0xcccccccc
s_mov_b32 s98,0x00f0000f
s_mov_b32 s99,0xf0000f00
s_getpc_b64 s[90:91]
Lg1_pc_%=:
s_mov_b32 s68,Lg1_d1w0_%=-Lg1_pc_%=
s_cmp_eq_u32 s54,1
s_cselect_b32 s68,Lg1_d1w1_%=-Lg1_pc_%=,s68
s_cmp_eq_u32 s54,2
s_cselect_b32 s68,Lg1_d1w2_%=-Lg1_pc_%=,s68
s_cmp_eq_u32 s54,3
s_cselect_b32 s68,Lg1_d1w3_%=-Lg1_pc_%=,s68
s_add_u32 s96,s90,s68
s_addc_u32 s97,s91,0
s_mov_b32 s68,Lg1_d0w0_%=-Lg1_pc_%=
s_cmp_eq_u32 s54,1
s_cselect_b32 s68,Lg1_d0w1_%=-Lg1_pc_%=,s68
s_cmp_eq_u32 s54,2
s_cselect_b32 s68,Lg1_d0w2_%=-Lg1_pc_%=,s68
s_cmp_eq_u32 s54,3
s_cselect_b32 s68,Lg1_d0w3_%=-Lg1_pc_%=,s68
s_add_u32 s94,s90,s68
s_addc_u32 s95,s91,0
s_mov_b32 s68,Lg1_a1w0_%=-Lg1_pc_%=
s_cmp_eq_u32 s54,1
s_cselect_b32 s68,Lg1_a1w1_%=-Lg1_pc_%=,s68
s_cmp_eq_u32 s54,2
s_cselect_b32 s68,Lg1_a1w2_%=-Lg1_pc_%=,s68
s_cmp_eq_u32 s54,3
s_cselect_b32 s68,Lg1_a1w3_%=-Lg1_pc_%=,s68
s_add_u32 s92,s90,s68
s_addc_u32 s93,s91,0
s_mov_b32 s68,Lg1_a0w0_%=-Lg1_pc_%=
s_cmp_eq_u32 s54,1
s_cselect_b32 s68,Lg1_a0w1_%=-Lg1_pc_%=,s68
s_cmp_eq_u32 s54,2
s_cselect_b32 s68,Lg1_a0w2_%=-Lg1_pc_%=,s68
s_cmp_eq_u32 s54,3
s_cselect_b32 s68,Lg1_a0w3_%=-Lg1_pc_%=,s68
s_add_u32 s90,s90,s68
s_addc_u32 s91,s91,0
s_mov_b32 s66,s59
s_mov_b32 s65,0
s_mov_b32 s62,s63
s_branch Lg1_slow0_%=
Lg1_a0w0_%=:
v_mfma_f32_16x16x32_bf16 v[28:31],a[0:3],v[44:47],0
s_add_u32 m0,s62,16384
ds_read_b128 v[72:75],v2 offset:7168
s_lshr_b32 s72,s66,6
v_mfma_f32_16x16x32_bf16 v[32:35],a[4:7],v[44:47],0
buffer_load_dwordx4 v1,s[36:39],s66 offen offset:1024 sc1 lds
v_mfma_f32_16x16x32_bf16 v[104:107],v[100:103],v[44:47],0
s_lshr_b32 s71,s62,2
s_cmp_lt_u32 s66,4194304
s_cselect_b32 s72,s59,s72
v_mfma_f32_16x16x32_bf16 v[108:111],v[44:47],v[44:47],0
v_fma_f32 v36,-v96,v20,v36
v_fma_f32 v37,-v96,v21,v37
v_mfma_f32_16x16x32_bf16 v[28:31],a[8:11],v[48:51],v[28:31]
ds_read_b128 v[44:47],v2 offset:8192
v_fma_f32 v38,-v96,v22,v38
v_mfma_f32_16x16x32_bf16 v[32:35],a[12:15],v[48:51],v[32:35]
buffer_load_dwordx4 v1,s[36:39],s66 offen offset:2048 sc1 lds
v_mfma_f32_16x16x32_bf16 v[104:107],v[100:103],v[48:51],v[104:107]
v_fma_f32 v39,-v96,v23,v39
v_mul_f32 v76,v36,v36
v_mfma_f32_16x16x32_bf16 v[108:111],v[48:51],v[48:51],v[108:111]
v_mul_f32 v77,v37,v37
v_mul_f32 v78,v38,v38
v_mfma_f32_16x16x32_bf16 v[28:31],a[16:19],v[52:55],v[28:31]
ds_read_b128 v[48:51],v2 offset:9216
v_mul_f32 v79,v39,v39
v_mfma_f32_16x16x32_bf16 v[32:35],a[20:23],v[52:55],v[32:35]
buffer_load_dwordx4 v1,s[36:39],s66 offen offset:3072 sc1 lds
v_mfma_f32_16x16x32_bf16 v[104:107],v[100:103],v[52:55],v[104:107]
s_add_u32 m0,s71,s88
v_fma_f32 v76,v98,v76,v97
v_mul_f32 v76,v36,v76
v_mfma_f32_16x16x32_bf16 v[108:111],v[52:55],v[52:55],v[108:111]
buffer_load_dword v10,s[36:39],s72 offen offset:0 sc1 lds
v_mfma_f32_16x16x32_bf16 v[28:31],a[24:27],v[56:59],v[28:31]
s_lshr_b32 s73,s47,0
ds_read_b128 v[52:55],v2 offset:10240
v_fma_f32 v77,v98,v77,v97
v_mfma_f32_16x16x32_bf16 v[32:35],a[28:31],v[56:59],v[32:35]
s_lshr_b32 s69,s50,5
v_exp_f32 v76,v76
v_mfma_f32_16x16x32_bf16 v[104:107],v[100:103],v[56:59],v[104:107]
s_cmp_lt_u32 s50,4194304
v_mul_f32 v77,v37,v77
v_fma_f32 v76,v76,v99,v99
v_mfma_f32_16x16x32_bf16 v[108:111],v[56:59],v[56:59],v[108:111]
s_waitcnt lgkmcnt(3))"
R"(
v_exp_f32 v77,v77
v_mfma_f32_16x16x32_bf16 v[28:31],a[32:35],v[60:63],v[28:31]
s_cselect_b32 s68,0xffffcfff,-1
ds_read_b128 v[56:59],v2 offset:11264
v_fma_f32 v77,v77,v99,v99
v_mfma_f32_16x16x32_bf16 v[32:35],a[36:39],v[60:63],v[32:35]
s_and_b32 s68,s68,s69
v_rcp_f32 v76,v76
v_mfma_f32_16x16x32_bf16 v[28:31],a[40:43],v[64:67],v[28:31]
s_cmp_lt_u32 s50,s89
ds_read_b128 v[60:63],v2 offset:12288
v_fma_f32 v78,v98,v78,v97
v_mfma_f32_16x16x32_bf16 v[32:35],a[44:47],v[64:67],v[32:35]
s_cselect_b32 s68,s59,s68
v_rcp_f32 v77,v77
v_mfma_f32_16x16x32_bf16 v[28:31],a[48:51],v[68:71],v[28:31]
s_add_u32 m0,s78,0
ds_read_b128 v[64:67],v2 offset:13312
v_mul_f32 v78,v38,v78
v_mfma_f32_16x16x32_bf16 v[32:35],a[52:55],v[68:71],v[32:35]
s_xor_b32 s69,s64,65536
v_exp_f32 v78,v78
v_mfma_f32_16x16x32_bf16 v[28:31],a[56:59],v[72:75],v[28:31]
s_cmp_ge_u32 s66,s59
ds_read_b128 v[68:71],v2 offset:14336
v_fma_f32 v78,v78,v99,v99
v_mfma_f32_16x16x32_bf16 v[32:35],a[60:63],v[72:75],v[32:35]
s_waitcnt lgkmcnt(3)
v_rcp_f32 v78,v78
v_mfma_f32_16x16x32_bf16 v[28:31],a[64:67],v[44:47],v[28:31]
s_waitcnt vmcnt(10)
ds_read_b128 v[72:75],v2 offset:15360
v_fma_f32 v79,v98,v79,v97
v_mfma_f32_16x16x32_bf16 v[32:35],a[68:71],v[44:47],v[32:35]
s_barrier
v_mfma_f32_16x16x32_bf16 v[28:31],a[72:75],v[48:51],v[28:31]
s_cselect_b32 s62,s69,s63
ds_read_b128 v[44:47],v2 offset:16384
ds_read_b32 v16,v7 offset:256
v_mfma_f32_16x16x32_bf16 v[32:35],a[76:79],v[48:51],v[32:35]
buffer_load_dword v6,s[36:39],s68 offen offset:0 sc1 lds
v_mfma_f32_16x16x32_bf16 v[28:31],a[80:83],v[52:55],v[28:31]
s_cselect_b32 s86,s49,s50
ds_read_b128 v[48:51],v2 offset:17408
v_mul_f32 v79,v39,v79
v_mfma_f32_16x16x32_bf16 v[32:35],a[84:87],v[52:55],v[32:35]
s_mov_b32 m0,s62
v_exp_f32 v79,v79
v_mfma_f32_16x16x32_bf16 v[28:31],a[88:91],v[56:59],v[28:31]
s_waitcnt lgkmcnt(1)
ds_read_b128 v[52:55],v2 offset:18432
v_fma_f32 v79,v79,v99,v99
v_mfma_f32_16x16x32_bf16 v[32:35],a[92:95],v[56:59],v[32:35]
v_cmp_eq_u32 vcc,s60,v16
v_rcp_f32 v79,v79
v_mul_f32 v36,v36,v76
v_mfma_f32_16x16x32_bf16 v[28:31],a[96:99],v[60:63],v[28:31]
s_cselect_b32 s87,vcc_lo,vcc_hi
ds_read_b128 v[56:59],v2 offset:19456
v_mul_f32 v37,v37,v77
v_mfma_f32_16x16x32_bf16 v[32:35],a[100:103],v[60:63],v[32:35]
s_cmp_lt_u32 s86,s89
v_mul_f32 v38,v38,v78
v_mul_f32 v39,v39,v79
v_mfma_f32_16x16x32_bf16 v[28:31],a[104:107],v[64:67],v[28:31]
s_cselect_b32 s87,-1,s87
ds_read_b128 v[60:63],v2 offset:20480
v_cvt_pk_bf16_f32 v84,v36,v37
v_mfma_f32_16x16x32_bf16 v[32:35],a[108:111],v[64:67],v[32:35]
s_cmp_eq_u32 s87,-1
v_cvt_pk_bf16_f32 v85,v38,v39
v_fma_f32 v40,-v96,v24,v40
v_mfma_f32_16x16x32_bf16 v[28:31],a[112:115],v[68:71],v[28:31]
s_cselect_b32 s67,s86,s59
ds_read_b128 v[64:67],v2 offset:21504
v_fma_f32 v41,-v96,v25,v41
v_mfma_f32_16x16x32_bf16 v[32:35],a[116:119],v[68:71],v[32:35]
s_xor_b32 s63,s63,65536
v_fma_f32 v42,-v96,v26,v42
v_fma_f32 v43,-v96,v27,v43
v_mfma_f32_16x16x32_bf16 v[28:31],a[120:123],v[72:75],v[28:31]
s_sub_u32 s69,s67,1048576
ds_read_b128 v[68:71],v2 offset:22528
v_mul_f32 v80,v40,v40
v_mfma_f32_16x16x32_bf16 v[32:35],a[124:127],v[72:75],v[32:35]
s_waitcnt lgkmcnt(3)
v_mul_f32 v81,v41,v41
v_mul_f32 v82,v42,v42
v_mfma_f32_16x16x32_bf16 v[28:31],a[128:131],v[44:47],v[28:31]
s_cmp_lt_u32 s69,3145728
ds_read_b128 v[72:75],v2 offset:23552
v_mul_f32 v83,v43,v43
v_mfma_f32_16x16x32_bf16 v[32:35],a[132:135],v[44:47],v[32:35]
s_cselect_b32 s89,4194304,s89
v_fma_f32 v80,v98,v80,v97
v_mul_f32 v80,v40,v80
v_mfma_f32_16x16x32_bf16 v[104:107],v[100:103],v[44:47],v[104:107]
s_add_u32 s53,s52,s55
v_exp_f32 v80,v80
v_mfma_f32_16x16x32_bf16 v[108:111],v[44:47],v[44:47],v[108:111]
s_cmp_eq_u32 s53,s56
v_fma_f32 v80,v80,v99,v99
v_fma_f32 v81,v98,v81,v97
v_mfma_f32_16x16x32_bf16 v[28:31],a[136:139],v[48:51],v[28:31]
s_cselect_b32 s53,s57,s53
ds_read_b128 v[44:47],v2 offset:24576
v_mul_f32 v81,v41,v81
v_mfma_f32_16x16x32_bf16 v[32:35],a[140:143],v[48:51],v[32:35]
s_cmp_eq_u32 s53,s58
v_exp_f32 v81,v81
v_mfma_f32_16x16x32_bf16 v[104:107],v[100:103],v[48:51],v[104:107]
s_cselect_b32 s53,s59,s53
v_rcp_f32 v80,v80
v_mfma_f32_16x16x32_bf16 v[108:111],v[48:51],v[48:51],v[108:111]
v_fma_f32 v81,v81,v99,v99
v_fma_f32 v82,v98,v82,v97
v_mfma_f32_16x16x32_bf16 v[28:31],a[144:147],v[52:55],v[28:31]
ds_read_b128 v[48:51],v2 offset:25600
v_mul_f32 v82,v42,v82
v_mfma_f32_16x16x32_bf16 v[32:35],a[148:151],v[52:55],v[32:35]
v_exp_f32 v82,v82
v_mfma_f32_16x16x32_bf16 v[104:107],v[100:103],v[52:55],v[104:107]
v_rcp_f32 v81,v81
v_mfma_f32_16x16x32_bf16 v[108:111],v[52:55],v[52:55],v[108:111]
v_fma_f32 v82,v82,v99,v99
v_fma_f32 v83,v98,v83,v97
v_mfma_f32_16x16x32_bf16 v[28:31],a[152:155],v[56:59],v[28:31]
buffer_load_dwordx4 v0,s[36:39],s67 offen offset:0 sc1 lds
v_mfma_f32_16x16x32_bf16 v[32:35],a[156:159],v[56:59],v[32:35]
ds_read_b128 v[52:55],v2 offset:26624
v_mul_f32 v83,v43,v83
v_mfma_f32_16x16x32_bf16 v[104:107],v[100:103],v[56:59],v[104:107]
v_exp_f32 v83,v83
v_mfma_f32_16x16x32_bf16 v[108:111],v[56:59],v[56:59],v[108:111]
s_waitcnt lgkmcnt(3)
v_rcp_f32 v82,v82
v_mfma_f32_16x16x32_bf16 v[28:31],a[160:163],v[60:63],v[28:31]
buffer_load_dwordx4 v0,s[36:39],s67 offen offset:1024 sc1 lds
v_mfma_f32_16x16x32_bf16 v[32:35],a[164:167],v[60:63],v[32:35]
ds_read_b128 v[56:59],v2 offset:27648
v_fma_f32 v83,v83,v99,v99
v_mfma_f32_16x16x32_bf16 v[28:31],a[168:171],v[64:67],v[28:31]
v_cndmask_b32 v112,v108,v109,s[82:83]
v_cndmask_b32 v113,v110,v111,s[82:83]
v_cndmask_b32 v112,v112,v113,s[84:85]
v_cndmask_b32 v112,v104,v112,s[98:99]
v_mfma_f32_16x16x32_bf16 v[32:35],a[172:175],v[64:67],v[32:35]
ds_read_b128 v[60:63],v2 offset:28672
ds_write_b32 v13,v112 offset:0
v_mfma_f32_16x16x32_bf16 v[28:31],a[176:179],v[68:71],v[28:31]
buffer_load_dwordx4 v0,s[36:39],s67 offen offset:2048 sc1 lds
v_mfma_f32_16x16x32_bf16 v[32:35],a[180:183],v[68:71],v[32:35]
ds_read_b128 v[64:67],v2 offset:29696
v_mul_f32 v40,v40,v80
v_mfma_f32_16x16x32_bf16 v[28:31],a[184:187],v[72:75],v[28:31]
ds_read_b128 v[68:71],v2 offset:30720
v_mul_f32 v41,v41,v81
v_mfma_f32_16x16x32_bf16 v[32:35],a[188:191],v[72:75],v[32:35]
s_waitcnt lgkmcnt(4)
v_rcp_f32 v83,v83
v_mfma_f32_16x16x32_bf16 v[28:31],a[192:195],v[44:47],v[28:31]
s_waitcnt vmcnt(10) lgkmcnt(2)
ds_read_b128 v[72:75],v2 offset:31744
v_mul_f32 v42,v42,v82
v_mfma_f32_16x16x32_bf16 v[32:35],a[196:199],v[44:47],v[32:35]
s_barrier
v_mfma_f32_16x16x32_bf16 v[28:31],a[200:203],v[48:51],v[28:31]
buffer_load_dwordx4 v0,s[36:39],s67 offen offset:3072 sc1 lds
v_mfma_f32_16x16x32_bf16 v[32:35],a[204:207],v[48:51],v[32:35]
s_add_u32 m0,s62,16384
ds_read_b128 v[44:47],v2 offset:32768
ds_read_b128 v[116:119],v14 offset:0
v_mfma_f32_16x16x32_bf16 v[28:31],a[208:211],v[52:55],v[28:31]
s_cmp_ge_u32 s66,s59
ds_read_b128 v[120:123],v14 offset:16
ds_read_b128 v[48:51],v2 offset:33792
v_mfma_f32_16x16x32_bf16 v[32:35],a[212:215],v[52:55],v[32:35]
v_mul_f32 v43,v43,v83
v_cvt_pk_bf16_f32 v86,v40,v41
v_mfma_f32_16x16x32_bf16 v[28:31],a[216:219],v[56:59],v[28:31]
buffer_load_dwordx4 v1,s[36:39],s67 offen offset:0 sc1 lds
v_mfma_f32_16x16x32_bf16 v[32:35],a[220:223],v[56:59],v[32:35]
s_waitcnt lgkmcnt(4)
ds_read_b128 v[52:55],v2 offset:34816
v_cvt_pk_bf16_f32 v87,v42,v43
v_mfma_f32_16x16x32_bf16 v[28:31],a[224:227],v[60:63],v[28:31]
s_waitcnt lgkmcnt(2)
ds_read_b128 v[56:59],v2 offset:35840
v_add_f32 v116,v116,v117
v_mfma_f32_16x16x32_bf16 v[32:35],a[228:231],v[60:63],v[32:35]
v_add_f32 v118,v118,v119
v_add_f32 v116,v116,v118
v_mfma_f32_16x16x32_bf16 v[28:31],a[232:235],v[64:67],v[28:31]
ds_read_b128 v[60:63],v2 offset:36864
v_mul_f32 v92,s75,v116
v_mfma_f32_16x16x32_bf16 v[32:35],a[236:239],v[64:67],v[32:35]
buffer_store_dwordx4 v[84:87],v3,s[36:39],s73 offen offset:0 sc0 sc1
v_add_f32 v120,v120,v121
v_add_f32 v122,v122,v123
v_add_f32 v120,v120,v122)"
R"(
v_mfma_f32_16x16x32_bf16 v[28:31],a[240:243],v[68:71],v[28:31]
ds_read_b128 v[64:67],v2 offset:37888
v_mul_f32 v120,s75,v120
v_mfma_f32_16x16x32_bf16 v[32:35],a[244:247],v[68:71],v[32:35]
v_fma_f32 v120,-v92,v92,v120
v_max_f32 v120,0,v120
v_add_f32 v115,v15,v120
v_fma_f32 v120,v15,v115,v120
v_rsq_f32 v116,v120
v_mfma_f32_16x16x32_bf16 v[28:31],a[248:251],v[72:75],v[28:31]
s_waitcnt lgkmcnt(2)
ds_read_b128 v[68:71],v2 offset:38912
v_mul_f32 v93,s76,v116
v_mul_f32 v94,v116,v116
v_mul_f32 v95,v120,v116
v_mul_f32 v94,s77,v94
v_mul_f32 v94,v94,v93
v_mfma_f32_16x16x32_bf16 v[32:35],a[252:255],v[72:75],v[32:35]
s_cbranch_scc1 Lg1_slow1_%=
Lg1_a1w0_%=:
v_mfma_f32_16x16x32_bf16 v[36:39],a[0:3],v[44:47],0
s_add_u32 m0,s62,16384
ds_read_b128 v[72:75],v2 offset:39936
s_lshr_b32 s72,s67,6
v_mfma_f32_16x16x32_bf16 v[40:43],a[4:7],v[44:47],0
buffer_load_dwordx4 v1,s[36:39],s67 offen offset:1024 sc1 lds
v_mfma_f32_16x16x32_bf16 v[104:107],v[100:103],v[44:47],0
s_lshr_b32 s71,s62,2
s_cmp_lt_u32 s67,4194304
s_cselect_b32 s72,s59,s72
v_mfma_f32_16x16x32_bf16 v[108:111],v[44:47],v[44:47],0
s_mov_b64 s[44:45],s[46:47]
v_fma_f32 v28,-v92,v20,v28
v_fma_f32 v29,-v92,v21,v29
v_mfma_f32_16x16x32_bf16 v[36:39],a[8:11],v[48:51],v[36:39]
s_mov_b64 s[46:47],s[48:49]
ds_read_b128 v[44:47],v2 offset:40960
v_fma_f32 v30,-v92,v22,v30
v_mfma_f32_16x16x32_bf16 v[40:43],a[12:15],v[48:51],v[40:43]
buffer_load_dwordx4 v1,s[36:39],s67 offen offset:2048 sc1 lds
v_mfma_f32_16x16x32_bf16 v[104:107],v[100:103],v[48:51],v[104:107]
v_fma_f32 v31,-v92,v23,v31
v_mul_f32 v76,v28,v28
v_mfma_f32_16x16x32_bf16 v[108:111],v[48:51],v[48:51],v[108:111]
v_mul_f32 v77,v29,v29
v_mul_f32 v78,v30,v30
v_mfma_f32_16x16x32_bf16 v[36:39],a[16:19],v[52:55],v[36:39]
ds_read_b128 v[48:51],v2 offset:41984
v_mul_f32 v79,v31,v31
v_mfma_f32_16x16x32_bf16 v[40:43],a[20:23],v[52:55],v[40:43]
buffer_load_dwordx4 v1,s[36:39],s67 offen offset:3072 sc1 lds
v_mfma_f32_16x16x32_bf16 v[104:107],v[100:103],v[52:55],v[104:107]
s_add_u32 m0,s71,s88
v_fma_f32 v76,v94,v76,v93
v_mul_f32 v76,v28,v76
v_mfma_f32_16x16x32_bf16 v[108:111],v[52:55],v[52:55],v[108:111]
buffer_load_dword v10,s[36:39],s72 offen offset:0 sc1 lds
v_mfma_f32_16x16x32_bf16 v[36:39],a[24:27],v[56:59],v[36:39]
s_lshr_b32 s73,s48,0
ds_read_b128 v[52:55],v2 offset:43008
v_fma_f32 v77,v94,v77,v93
v_mfma_f32_16x16x32_bf16 v[40:43],a[28:31],v[56:59],v[40:43]
s_lshr_b32 s69,s51,5
v_exp_f32 v76,v76
v_mfma_f32_16x16x32_bf16 v[104:107],v[100:103],v[56:59],v[104:107]
s_cmp_lt_u32 s51,4194304
v_mul_f32 v77,v29,v77
v_fma_f32 v76,v76,v95,v95
v_mfma_f32_16x16x32_bf16 v[108:111],v[56:59],v[56:59],v[108:111]
s_waitcnt lgkmcnt(3)
v_exp_f32 v77,v77
v_mfma_f32_16x16x32_bf16 v[36:39],a[32:35],v[60:63],v[36:39]
s_cselect_b32 s68,0xffffcfff,-1
ds_read_b128 v[56:59],v2 offset:44032
v_fma_f32 v77,v77,v95,v95
v_mfma_f32_16x16x32_bf16 v[40:43],a[36:39],v[60:63],v[40:43]
s_and_b32 s68,s68,s69
v_rcp_f32 v76,v76
v_mfma_f32_16x16x32_bf16 v[36:39],a[40:43],v[64:67],v[36:39]
s_cmp_lt_u32 s51,s89
ds_read_b128 v[60:63],v2 offset:45056
v_fma_f32 v78,v94,v78,v93
v_mfma_f32_16x16x32_bf16 v[40:43],a[44:47],v[64:67],v[40:43]
s_cselect_b32 s68,s59,s68
v_rcp_f32 v77,v77
v_mfma_f32_16x16x32_bf16 v[36:39],a[48:51],v[68:71],v[36:39]
s_add_u32 m0,s78,256
ds_read_b128 v[64:67],v2 offset:46080
v_mul_f32 v78,v30,v78
v_mfma_f32_16x16x32_bf16 v[40:43],a[52:55],v[68:71],v[40:43]
s_xor_b32 s69,s63,65536
v_exp_f32 v78,v78
v_mfma_f32_16x16x32_bf16 v[36:39],a[56:59],v[72:75],v[36:39]
s_cmp_ge_u32 s67,s59
ds_read_b128 v[68:71],v2 offset:47104
v_fma_f32 v78,v78,v95,v95
v_mfma_f32_16x16x32_bf16 v[40:43],a[60:63],v[72:75],v[40:43]
s_waitcnt lgkmcnt(3)
v_rcp_f32 v78,v78
v_mfma_f32_16x16x32_bf16 v[36:39],a[64:67],v[44:47],v[36:39]
s_waitcnt vmcnt(10)
ds_read_b128 v[72:75],v2 offset:48128
v_fma_f32 v79,v94,v79,v93
v_mfma_f32_16x16x32_bf16 v[40:43],a[68:71],v[44:47],v[40:43]
s_barrier
v_mfma_f32_16x16x32_bf16 v[36:39],a[72:75],v[48:51],v[36:39]
s_cselect_b32 s62,s69,s64
ds_read_b128 v[44:47],v2 offset:49152
ds_read_b32 v16,v7 offset:0
v_mfma_f32_16x16x32_bf16 v[40:43],a[76:79],v[48:51],v[40:43]
buffer_load_dword v6,s[36:39],s68 offen offset:0 sc1 lds
v_mfma_f32_16x16x32_bf16 v[36:39],a[80:83],v[52:55],v[36:39]
s_cselect_b32 s86,s50,s51
ds_read_b128 v[48:51],v2 offset:50176
v_mul_f32 v79,v31,v79
v_mfma_f32_16x16x32_bf16 v[40:43],a[84:87],v[52:55],v[40:43]
s_mov_b32 m0,s62
v_exp_f32 v79,v79
v_mfma_f32_16x16x32_bf16 v[36:39],a[88:91],v[56:59],v[36:39]
s_waitcnt lgkmcnt(1)
ds_read_b128 v[52:55],v2 offset:51200
v_fma_f32 v79,v79,v95,v95
v_mfma_f32_16x16x32_bf16 v[40:43],a[92:95],v[56:59],v[40:43]
v_cmp_eq_u32 vcc,s60,v16
v_rcp_f32 v79,v79
v_mul_f32 v28,v28,v76
v_mfma_f32_16x16x32_bf16 v[36:39],a[96:99],v[60:63],v[36:39]
s_cselect_b32 s87,vcc_lo,vcc_hi
ds_read_b128 v[56:59],v2 offset:52224
v_mul_f32 v29,v29,v77
v_mfma_f32_16x16x32_bf16 v[40:43],a[100:103],v[60:63],v[40:43]
s_cmp_lt_u32 s86,s89
v_mul_f32 v30,v30,v78
v_mul_f32 v31,v31,v79
v_mfma_f32_16x16x32_bf16 v[36:39],a[104:107],v[64:67],v[36:39]
s_cselect_b32 s87,-1,s87
ds_read_b128 v[60:63],v2 offset:53248
v_cvt_pk_bf16_f32 v84,v28,v29
v_mfma_f32_16x16x32_bf16 v[40:43],a[108:111],v[64:67],v[40:43]
s_cmp_eq_u32 s87,-1
v_cvt_pk_bf16_f32 v85,v30,v31
v_fma_f32 v32,-v92,v24,v32
v_mfma_f32_16x16x32_bf16 v[36:39],a[112:115],v[68:71],v[36:39]
s_cselect_b32 s66,s86,s59
ds_read_b128 v[64:67],v2 offset:54272
v_fma_f32 v33,-v92,v25,v33
v_mfma_f32_16x16x32_bf16 v[40:43],a[116:119],v[68:71],v[40:43]
s_xor_b32 s64,s64,65536
v_fma_f32 v34,-v92,v26,v34
v_fma_f32 v35,-v92,v27,v35
v_mfma_f32_16x16x32_bf16 v[36:39],a[120:123],v[72:75],v[36:39]
s_sub_u32 s69,s66,1048576
ds_read_b128 v[68:71],v2 offset:55296
v_mul_f32 v80,v32,v32
v_mfma_f32_16x16x32_bf16 v[40:43],a[124:127],v[72:75],v[40:43]
s_waitcnt lgkmcnt(3)
v_mul_f32 v81,v33,v33
v_mul_f32 v82,v34,v34
v_mfma_f32_16x16x32_bf16 v[36:39],a[128:131],v[44:47],v[36:39]
s_cmp_lt_u32 s69,3145728
ds_read_b128 v[72:75],v2 offset:56320
v_mul_f32 v83,v35,v35
v_mfma_f32_16x16x32_bf16 v[40:43],a[132:135],v[44:47],v[40:43]
s_cselect_b32 s89,4194304,s89
v_fma_f32 v80,v94,v80,v93
v_mul_f32 v80,v32,v80
v_mfma_f32_16x16x32_bf16 v[104:107],v[100:103],v[44:47],v[104:107]
s_add_u32 s74,s53,s55
v_exp_f32 v80,v80
v_mfma_f32_16x16x32_bf16 v[108:111],v[44:47],v[44:47],v[108:111]
s_cmp_eq_u32 s74,s56
v_fma_f32 v80,v80,v95,v95
v_fma_f32 v81,v94,v81,v93
v_mfma_f32_16x16x32_bf16 v[36:39],a[136:139],v[48:51],v[36:39]
s_cselect_b32 s74,s57,s74
ds_read_b128 v[44:47],v2 offset:57344
v_mul_f32 v81,v33,v81
v_mfma_f32_16x16x32_bf16 v[40:43],a[140:143],v[48:51],v[40:43]
s_cmp_eq_u32 s74,s58
v_exp_f32 v81,v81
v_mfma_f32_16x16x32_bf16 v[104:107],v[100:103],v[48:51],v[104:107]
s_cselect_b32 s74,s59,s74
v_rcp_f32 v80,v80
v_mfma_f32_16x16x32_bf16 v[108:111],v[48:51],v[48:51],v[108:111]
s_mov_b64 s[48:49],s[50:51]
v_fma_f32 v81,v81,v95,v95
v_fma_f32 v82,v94,v82,v93
v_mfma_f32_16x16x32_bf16 v[36:39],a[144:147],v[52:55],v[36:39]
s_mov_b64 s[50:51],s[52:53]
ds_read_b128 v[48:51],v2 offset:58368
v_mul_f32 v82,v34,v82
v_mfma_f32_16x16x32_bf16 v[40:43],a[148:151],v[52:55],v[40:43]
s_mov_b32 s52,s74
v_exp_f32 v82,v82
v_mfma_f32_16x16x32_bf16 v[104:107],v[100:103],v[52:55],v[104:107]
v_rcp_f32 v81,v81
v_mfma_f32_16x16x32_bf16 v[108:111],v[52:55],v[52:55],v[108:111]
v_fma_f32 v82,v82,v95,v95
v_fma_f32 v83,v94,v83,v93
v_mfma_f32_16x16x32_bf16 v[36:39],a[152:155],v[56:59],v[36:39])"
R"(
buffer_load_dwordx4 v0,s[36:39],s66 offen offset:0 sc1 lds
v_mfma_f32_16x16x32_bf16 v[40:43],a[156:159],v[56:59],v[40:43]
ds_read_b128 v[52:55],v2 offset:59392
v_mul_f32 v83,v35,v83
v_mfma_f32_16x16x32_bf16 v[104:107],v[100:103],v[56:59],v[104:107]
v_exp_f32 v83,v83
v_mfma_f32_16x16x32_bf16 v[108:111],v[56:59],v[56:59],v[108:111]
s_waitcnt lgkmcnt(3)
v_rcp_f32 v82,v82
v_mfma_f32_16x16x32_bf16 v[36:39],a[160:163],v[60:63],v[36:39]
buffer_load_dwordx4 v0,s[36:39],s66 offen offset:1024 sc1 lds
v_mfma_f32_16x16x32_bf16 v[40:43],a[164:167],v[60:63],v[40:43]
ds_read_b128 v[56:59],v2 offset:60416
v_fma_f32 v83,v83,v95,v95
v_mfma_f32_16x16x32_bf16 v[36:39],a[168:171],v[64:67],v[36:39]
v_cndmask_b32 v112,v108,v109,s[82:83]
v_cndmask_b32 v113,v110,v111,s[82:83]
v_cndmask_b32 v112,v112,v113,s[84:85]
v_cndmask_b32 v112,v104,v112,s[98:99]
v_mfma_f32_16x16x32_bf16 v[40:43],a[172:175],v[64:67],v[40:43]
ds_read_b128 v[60:63],v2 offset:61440
ds_write_b32 v13,v112 offset:2048
v_mfma_f32_16x16x32_bf16 v[36:39],a[176:179],v[68:71],v[36:39]
buffer_load_dwordx4 v0,s[36:39],s66 offen offset:2048 sc1 lds
v_mfma_f32_16x16x32_bf16 v[40:43],a[180:183],v[68:71],v[40:43]
ds_read_b128 v[64:67],v2 offset:62464
v_mul_f32 v32,v32,v80
v_mfma_f32_16x16x32_bf16 v[36:39],a[184:187],v[72:75],v[36:39]
ds_read_b128 v[68:71],v2 offset:63488
v_mul_f32 v33,v33,v81
v_mfma_f32_16x16x32_bf16 v[40:43],a[188:191],v[72:75],v[40:43]
s_waitcnt lgkmcnt(4)
v_rcp_f32 v83,v83
v_mfma_f32_16x16x32_bf16 v[36:39],a[192:195],v[44:47],v[36:39]
s_waitcnt vmcnt(10) lgkmcnt(2)
ds_read_b128 v[72:75],v2 offset:64512
v_xor_b32 v2,65536,v2
v_mfma_f32_16x16x32_bf16 v[40:43],a[196:199],v[44:47],v[40:43]
s_barrier
v_mfma_f32_16x16x32_bf16 v[36:39],a[200:203],v[48:51],v[36:39]
buffer_load_dwordx4 v0,s[36:39],s66 offen offset:3072 sc1 lds
v_mfma_f32_16x16x32_bf16 v[40:43],a[204:207],v[48:51],v[40:43]
s_add_u32 m0,s62,16384
ds_read_b128 v[44:47],v2 offset:0
ds_read_b128 v[116:119],v14 offset:2048
v_mfma_f32_16x16x32_bf16 v[36:39],a[208:211],v[52:55],v[36:39]
s_cmp_lt_u32 s67,4194304
ds_read_b128 v[120:123],v14 offset:2064
ds_read_b128 v[48:51],v2 offset:1024
v_mfma_f32_16x16x32_bf16 v[40:43],a[212:215],v[52:55],v[40:43]
v_mul_f32 v34,v34,v82
v_mul_f32 v35,v35,v83
v_mfma_f32_16x16x32_bf16 v[36:39],a[216:219],v[56:59],v[36:39]
buffer_load_dwordx4 v1,s[36:39],s66 offen offset:0 sc1 lds
v_mfma_f32_16x16x32_bf16 v[40:43],a[220:223],v[56:59],v[40:43]
s_waitcnt lgkmcnt(4)
ds_read_b128 v[52:55],v2 offset:2048
v_cvt_pk_bf16_f32 v86,v32,v33
v_mfma_f32_16x16x32_bf16 v[36:39],a[224:227],v[60:63],v[36:39]
s_waitcnt lgkmcnt(2)
ds_read_b128 v[56:59],v2 offset:3072
v_cvt_pk_bf16_f32 v87,v34,v35
v_mfma_f32_16x16x32_bf16 v[40:43],a[228:231],v[60:63],v[40:43]
v_add_f32 v116,v116,v117
v_add_f32 v118,v118,v119
v_mfma_f32_16x16x32_bf16 v[36:39],a[232:235],v[64:67],v[36:39]
ds_read_b128 v[60:63],v2 offset:4096
v_add_f32 v116,v116,v118
v_mfma_f32_16x16x32_bf16 v[40:43],a[236:239],v[64:67],v[40:43]
buffer_store_dwordx4 v[84:87],v3,s[36:39],s73 offen offset:0 sc0 sc1
v_mul_f32 v96,s75,v116
v_add_f32 v120,v120,v121
v_add_f32 v122,v122,v123
v_mfma_f32_16x16x32_bf16 v[36:39],a[240:243],v[68:71],v[36:39]
ds_read_b128 v[64:67],v2 offset:5120
v_add_f32 v120,v120,v122
v_mfma_f32_16x16x32_bf16 v[40:43],a[244:247],v[68:71],v[40:43]
v_mul_f32 v120,s75,v120
v_fma_f32 v120,-v96,v96,v120
v_max_f32 v120,0,v120
v_add_f32 v115,v15,v120
v_fma_f32 v120,v15,v115,v120
v_rsq_f32 v116,v120
v_mfma_f32_16x16x32_bf16 v[36:39],a[248:251],v[72:75],v[36:39]
s_waitcnt lgkmcnt(2)
ds_read_b128 v[68:71],v2 offset:6144
v_xor_b32 v8,16384,v8
v_mul_f32 v97,s76,v116
v_mul_f32 v98,v116,v116
v_mul_f32 v99,v120,v116
v_mul_f32 v98,s77,v98
v_mul_f32 v98,v98,v97
v_mfma_f32_16x16x32_bf16 v[40:43],a[252:255],v[72:75],v[40:43]
s_cbranch_scc1 Lg1_a0w0_%=
s_cmp_ge_u32 s48,4194304
s_cselect_b64 s[90:91],s[94:95],s[90:91]
s_cselect_b64 s[92:93],s[96:97],s[92:93]
s_cmp_lt_u32 s67,s59
s_cbranch_scc1 Lg1_d0w0_%=
s_branch Lg1_slow0_%=
Lg1_a0w1_%=:
v_mfma_f32_16x16x32_bf16 v[28:31],a[32:35],v[44:47],0
s_add_u32 m0,s62,16384
ds_read_b128 v[72:75],v2 offset:3072
s_lshr_b32 s72,s66,6
v_mfma_f32_16x16x32_bf16 v[32:35],a[36:39],v[44:47],0
s_lshr_b32 s71,s62,2
s_cmp_lt_u32 s66,4194304
s_cselect_b32 s72,s59,s72
v_mfma_f32_16x16x32_bf16 v[104:107],v[100:103],v[44:47],0
buffer_load_dwordx4 v1,s[36:39],s66 offen offset:1024 sc1 lds
v_mfma_f32_16x16x32_bf16 v[108:111],v[44:47],v[44:47],0
v_fma_f32 v36,-v96,v20,v36
v_fma_f32 v37,-v96,v21,v37
v_mfma_f32_16x16x32_bf16 v[28:31],a[40:43],v[48:51],v[28:31]
ds_read_b128 v[44:47],v2 offset:12288
v_fma_f32 v38,-v96,v22,v38
v_mfma_f32_16x16x32_bf16 v[32:35],a[44:47],v[48:51],v[32:35]
v_fma_f32 v39,-v96,v23,v39
v_mul_f32 v76,v36,v36
v_mfma_f32_16x16x32_bf16 v[104:107],v[100:103],v[48:51],v[104:107]
buffer_load_dwordx4 v1,s[36:39],s66 offen offset:2048 sc1 lds
v_mfma_f32_16x16x32_bf16 v[108:111],v[48:51],v[48:51],v[108:111]
v_mul_f32 v77,v37,v37
v_mul_f32 v78,v38,v38
v_mfma_f32_16x16x32_bf16 v[28:31],a[48:51],v[52:55],v[28:31]
ds_read_b128 v[48:51],v2 offset:13312
v_mul_f32 v79,v39,v39
v_mfma_f32_16x16x32_bf16 v[32:35],a[52:55],v[52:55],v[32:35]
v_fma_f32 v76,v98,v76,v97
v_mul_f32 v76,v36,v76
v_mfma_f32_16x16x32_bf16 v[104:107],v[100:103],v[52:55],v[104:107]
buffer_load_dwordx4 v1,s[36:39],s66 offen offset:3072 sc1 lds
v_mfma_f32_16x16x32_bf16 v[108:111],v[52:55],v[52:55],v[108:111]
s_add_u32 m0,s71,s88
v_exp_f32 v76,v76
v_mfma_f32_16x16x32_bf16 v[28:31],a[56:59],v[56:59],v[28:31]
buffer_load_dword v10,s[36:39],s72 offen offset:0 sc1 lds
v_mfma_f32_16x16x32_bf16 v[32:35],a[60:63],v[56:59],v[32:35]
s_lshr_b32 s73,s47,0
ds_read_b128 v[52:55],v2 offset:14336
v_fma_f32 v76,v76,v99,v99
v_mfma_f32_16x16x32_bf16 v[104:107],v[100:103],v[56:59],v[104:107]
s_lshr_b32 s69,s51,5
v_rcp_f32 v76,v76
v_mfma_f32_16x16x32_bf16 v[108:111],v[56:59],v[56:59],v[108:111]
s_waitcnt lgkmcnt(3)
v_fma_f32 v77,v98,v77,v97
v_mul_f32 v77,v37,v77
v_mfma_f32_16x16x32_bf16 v[28:31],a[0:3],v[60:63],v[28:31]
s_cmp_lt_u32 s51,4194304
ds_read_b128 v[56:59],v2 offset:15360
v_fma_f32 v78,v98,v78,v97
v_mfma_f32_16x16x32_bf16 v[32:35],a[4:7],v[60:63],v[32:35]
s_cselect_b32 s68,0xffffcfff,-1
v_exp_f32 v77,v77
v_mfma_f32_16x16x32_bf16 v[28:31],a[8:11],v[64:67],v[28:31]
s_and_b32 s68,s68,s69
ds_read_b128 v[60:63],v2 offset:8192
v_mul_f32 v78,v38,v78
v_mfma_f32_16x16x32_bf16 v[32:35],a[12:15],v[64:67],v[32:35]
s_cmp_lt_u32 s51,s89
v_exp_f32 v78,v78
v_mfma_f32_16x16x32_bf16 v[28:31],a[16:19],v[68:71],v[28:31]
s_cselect_b32 s68,s59,s68
ds_read_b128 v[64:67],v2 offset:9216
v_fma_f32 v77,v77,v99,v99
v_mfma_f32_16x16x32_bf16 v[32:35],a[20:23],v[68:71],v[32:35]
s_add_u32 m0,s78,0
v_rcp_f32 v77,v77
v_mfma_f32_16x16x32_bf16 v[28:31],a[24:27],v[72:75],v[28:31]
s_xor_b32 s69,s64,65536
ds_read_b128 v[68:71],v2 offset:10240
v_fma_f32 v78,v78,v99,v99
v_mfma_f32_16x16x32_bf16 v[32:35],a[28:31],v[72:75],v[32:35]
s_waitcnt lgkmcnt(3)
v_rcp_f32 v78,v78
v_mfma_f32_16x16x32_bf16 v[28:31],a[96:99],v[44:47],v[28:31]
s_waitcnt vmcnt(10)
ds_read_b128 v[72:75],v2 offset:11264
v_fma_f32 v79,v98,v79,v97
v_mfma_f32_16x16x32_bf16 v[32:35],a[100:103],v[44:47],v[32:35]
s_barrier
v_mfma_f32_16x16x32_bf16 v[28:31],a[104:107],v[48:51],v[28:31]
s_cmp_ge_u32 s66,s59
ds_read_b128 v[44:47],v2 offset:20480
ds_read_b32 v16,v7 offset:256
v_mfma_f32_16x16x32_bf16 v[32:35],a[108:111],v[48:51],v[32:35]
s_cselect_b32 s62,s69,s63
v_mul_f32 v79,v39,v79
v_mul_f32 v36,v36,v76
v_mfma_f32_16x16x32_bf16 v[28:31],a[112:115],v[52:55],v[28:31]
s_cselect_b32 s86,s49,s50
ds_read_b128 v[48:51],v2 offset:21504
v_mul_f32 v37,v37,v77
v_mfma_f32_16x16x32_bf16 v[32:35],a[116:119],v[52:55],v[32:35]
buffer_load_dword v6,s[36:39],s68 offen offset:0 sc1 lds
v_mfma_f32_16x16x32_bf16 v[28:31],a[120:123],v[56:59],v[28:31]
s_waitcnt lgkmcnt(1)
ds_read_b128 v[52:55],v2 offset:22528)"
R"(
v_mul_f32 v38,v38,v78
v_mfma_f32_16x16x32_bf16 v[32:35],a[124:127],v[56:59],v[32:35]
v_cmp_eq_u32 vcc,s60,v16
v_exp_f32 v79,v79
v_cvt_pk_bf16_f32 v84,v36,v37
v_mfma_f32_16x16x32_bf16 v[28:31],a[64:67],v[60:63],v[28:31]
s_mov_b32 m0,s62
ds_read_b128 v[56:59],v2 offset:23552
v_fma_f32 v79,v79,v99,v99
v_mfma_f32_16x16x32_bf16 v[32:35],a[68:71],v[60:63],v[32:35]
s_cselect_b32 s87,vcc_lo,vcc_hi
v_rcp_f32 v79,v79
v_mfma_f32_16x16x32_bf16 v[28:31],a[72:75],v[64:67],v[28:31]
s_cmp_lt_u32 s86,s89
ds_read_b128 v[60:63],v2 offset:16384
v_mul_f32 v39,v39,v79
v_mfma_f32_16x16x32_bf16 v[32:35],a[76:79],v[64:67],v[32:35]
s_cselect_b32 s87,-1,s87
v_cvt_pk_bf16_f32 v85,v38,v39
v_fma_f32 v40,-v96,v24,v40
v_mfma_f32_16x16x32_bf16 v[28:31],a[80:83],v[68:71],v[28:31]
s_cmp_eq_u32 s87,-1
ds_read_b128 v[64:67],v2 offset:17408
v_fma_f32 v41,-v96,v25,v41
v_mfma_f32_16x16x32_bf16 v[32:35],a[84:87],v[68:71],v[32:35]
s_cselect_b32 s67,s86,s59
v_fma_f32 v42,-v96,v26,v42
v_fma_f32 v43,-v96,v27,v43
v_mfma_f32_16x16x32_bf16 v[28:31],a[88:91],v[72:75],v[28:31]
s_xor_b32 s63,s63,65536
ds_read_b128 v[68:71],v2 offset:18432
v_mul_f32 v80,v40,v40
v_mfma_f32_16x16x32_bf16 v[32:35],a[92:95],v[72:75],v[32:35]
s_waitcnt lgkmcnt(3)
v_mul_f32 v81,v41,v41
v_mul_f32 v82,v42,v42
v_mfma_f32_16x16x32_bf16 v[28:31],a[160:163],v[44:47],v[28:31]
s_sub_u32 s69,s67,1048576
ds_read_b128 v[72:75],v2 offset:19456
v_mul_f32 v83,v43,v43
v_mfma_f32_16x16x32_bf16 v[32:35],a[164:167],v[44:47],v[32:35]
s_cmp_lt_u32 s69,3145728
v_fma_f32 v80,v98,v80,v97
v_mul_f32 v80,v40,v80
v_mfma_f32_16x16x32_bf16 v[104:107],v[100:103],v[44:47],v[104:107]
s_cselect_b32 s89,4194304,s89
v_exp_f32 v80,v80
v_mfma_f32_16x16x32_bf16 v[108:111],v[44:47],v[44:47],v[108:111]
s_add_u32 s53,s52,s55
v_fma_f32 v80,v80,v99,v99
v_fma_f32 v81,v98,v81,v97
v_mfma_f32_16x16x32_bf16 v[28:31],a[168:171],v[48:51],v[28:31]
s_cmp_eq_u32 s53,s56
ds_read_b128 v[44:47],v2 offset:28672
v_mul_f32 v81,v41,v81
v_mfma_f32_16x16x32_bf16 v[32:35],a[172:175],v[48:51],v[32:35]
s_cselect_b32 s53,s57,s53
v_exp_f32 v81,v81
v_mfma_f32_16x16x32_bf16 v[104:107],v[100:103],v[48:51],v[104:107]
s_cmp_eq_u32 s53,s58
v_rcp_f32 v80,v80
v_mfma_f32_16x16x32_bf16 v[108:111],v[48:51],v[48:51],v[108:111]
s_cselect_b32 s53,s59,s53
v_fma_f32 v81,v81,v99,v99
v_fma_f32 v82,v98,v82,v97
v_mfma_f32_16x16x32_bf16 v[28:31],a[176:179],v[52:55],v[28:31]
ds_read_b128 v[48:51],v2 offset:29696
v_mul_f32 v82,v42,v82
v_mfma_f32_16x16x32_bf16 v[32:35],a[180:183],v[52:55],v[32:35]
v_exp_f32 v82,v82
v_mfma_f32_16x16x32_bf16 v[104:107],v[100:103],v[52:55],v[104:107]
v_rcp_f32 v81,v81
v_mfma_f32_16x16x32_bf16 v[108:111],v[52:55],v[52:55],v[108:111]
v_fma_f32 v82,v82,v99,v99
v_fma_f32 v83,v98,v83,v97
v_mfma_f32_16x16x32_bf16 v[28:31],a[184:187],v[56:59],v[28:31]
ds_read_b128 v[52:55],v2 offset:30720
v_mul_f32 v83,v43,v83
v_mfma_f32_16x16x32_bf16 v[32:35],a[188:191],v[56:59],v[32:35]
buffer_load_dwordx4 v0,s[36:39],s67 offen offset:0 sc1 lds
v_mfma_f32_16x16x32_bf16 v[104:107],v[100:103],v[56:59],v[104:107]
v_exp_f32 v83,v83
v_mfma_f32_16x16x32_bf16 v[108:111],v[56:59],v[56:59],v[108:111]
s_waitcnt lgkmcnt(3)
v_rcp_f32 v82,v82
v_mfma_f32_16x16x32_bf16 v[28:31],a[128:131],v[60:63],v[28:31]
ds_read_b128 v[56:59],v2 offset:31744
v_fma_f32 v83,v83,v99,v99
v_mfma_f32_16x16x32_bf16 v[32:35],a[132:135],v[60:63],v[32:35]
buffer_load_dwordx4 v0,s[36:39],s67 offen offset:1024 sc1 lds
v_mfma_f32_16x16x32_bf16 v[28:31],a[136:139],v[64:67],v[28:31]
v_cndmask_b32 v112,v108,v109,s[82:83]
v_cndmask_b32 v113,v110,v111,s[82:83]
v_cndmask_b32 v112,v112,v113,s[84:85]
v_cndmask_b32 v112,v104,v112,s[98:99]
v_mfma_f32_16x16x32_bf16 v[32:35],a[140:143],v[64:67],v[32:35]
ds_read_b128 v[60:63],v2 offset:24576
ds_write_b32 v13,v112 offset:0
v_mfma_f32_16x16x32_bf16 v[28:31],a[144:147],v[68:71],v[28:31]
ds_read_b128 v[64:67],v2 offset:25600
v_mul_f32 v40,v40,v80
v_mfma_f32_16x16x32_bf16 v[32:35],a[148:151],v[68:71],v[32:35]
buffer_load_dwordx4 v0,s[36:39],s67 offen offset:2048 sc1 lds
v_mfma_f32_16x16x32_bf16 v[28:31],a[152:155],v[72:75],v[28:31]
ds_read_b128 v[68:71],v2 offset:26624
v_mul_f32 v41,v41,v81
v_mfma_f32_16x16x32_bf16 v[32:35],a[156:159],v[72:75],v[32:35]
s_waitcnt lgkmcnt(4)
v_rcp_f32 v83,v83
v_mfma_f32_16x16x32_bf16 v[28:31],a[224:227],v[44:47],v[28:31]
s_waitcnt vmcnt(10) lgkmcnt(2)
ds_read_b128 v[72:75],v2 offset:27648
v_mul_f32 v42,v42,v82
v_mfma_f32_16x16x32_bf16 v[32:35],a[228:231],v[44:47],v[32:35]
s_barrier
v_mfma_f32_16x16x32_bf16 v[28:31],a[232:235],v[48:51],v[28:31]
ds_read_b128 v[116:119],v14 offset:0
ds_read_b128 v[120:123],v14 offset:16
v_mfma_f32_16x16x32_bf16 v[32:35],a[236:239],v[48:51],v[32:35]
ds_read_b128 v[44:47],v2 offset:36864
buffer_load_dwordx4 v0,s[36:39],s67 offen offset:3072 sc1 lds
v_mfma_f32_16x16x32_bf16 v[28:31],a[240:243],v[52:55],v[28:31]
s_add_u32 m0,s62,16384
ds_read_b128 v[48:51],v2 offset:37888
v_mul_f32 v43,v43,v83
v_mfma_f32_16x16x32_bf16 v[32:35],a[244:247],v[52:55],v[32:35]
s_cmp_ge_u32 s66,s59
v_cvt_pk_bf16_f32 v86,v40,v41
v_cvt_pk_bf16_f32 v87,v42,v43
v_mfma_f32_16x16x32_bf16 v[28:31],a[248:251],v[56:59],v[28:31]
s_waitcnt lgkmcnt(2)
ds_read_b128 v[52:55],v2 offset:38912
v_add_f32 v116,v116,v117
v_mfma_f32_16x16x32_bf16 v[32:35],a[252:255],v[56:59],v[32:35]
buffer_load_dwordx4 v1,s[36:39],s67 offen offset:0 sc1 lds
v_mfma_f32_16x16x32_bf16 v[28:31],a[192:195],v[60:63],v[28:31]
ds_read_b128 v[56:59],v2 offset:39936
v_add_f32 v118,v118,v119
v_mfma_f32_16x16x32_bf16 v[32:35],a[196:199],v[60:63],v[32:35]
v_add_f32 v116,v116,v118
v_mul_f32 v92,s75,v116
v_mfma_f32_16x16x32_bf16 v[28:31],a[200:203],v[64:67],v[28:31]
ds_read_b128 v[60:63],v2 offset:32768
v_add_f32 v120,v120,v121
v_mfma_f32_16x16x32_bf16 v[32:35],a[204:207],v[64:67],v[32:35]
v_add_f32 v122,v122,v123
v_add_f32 v120,v120,v122
v_mfma_f32_16x16x32_bf16 v[28:31],a[208:211],v[68:71],v[28:31]
buffer_store_dwordx4 v[84:87],v3,s[36:39],s73 offen offset:0 sc0 sc1
ds_read_b128 v[64:67],v2 offset:33792
v_mul_f32 v120,s75,v120
v_fma_f32 v120,-v92,v92,v120
v_mfma_f32_16x16x32_bf16 v[32:35],a[212:215],v[68:71],v[32:35]
v_max_f32 v120,0,v120
v_add_f32 v115,v15,v120
v_fma_f32 v120,v15,v115,v120
v_rsq_f32 v116,v120
v_mfma_f32_16x16x32_bf16 v[28:31],a[216:219],v[72:75],v[28:31]
s_waitcnt lgkmcnt(2)
ds_read_b128 v[68:71],v2 offset:34816
v_mul_f32 v93,s76,v116
v_mul_f32 v94,v116,v116
v_mul_f32 v95,v120,v116
v_mul_f32 v94,s77,v94
v_mul_f32 v94,v94,v93
v_mfma_f32_16x16x32_bf16 v[32:35],a[220:223],v[72:75],v[32:35]
s_cbranch_scc1 Lg1_slow1_%=
Lg1_a1w1_%=:
v_mfma_f32_16x16x32_bf16 v[36:39],a[32:35],v[44:47],0
s_add_u32 m0,s62,16384
ds_read_b128 v[72:75],v2 offset:35840
s_lshr_b32 s72,s67,6
v_mfma_f32_16x16x32_bf16 v[40:43],a[36:39],v[44:47],0
s_lshr_b32 s71,s62,2
s_cmp_lt_u32 s67,4194304
s_cselect_b32 s72,s59,s72
v_mfma_f32_16x16x32_bf16 v[104:107],v[100:103],v[44:47],0
buffer_load_dwordx4 v1,s[36:39],s67 offen offset:1024 sc1 lds
v_mfma_f32_16x16x32_bf16 v[108:111],v[44:47],v[44:47],0
s_mov_b64 s[44:45],s[46:47]
v_fma_f32 v28,-v92,v20,v28
v_fma_f32 v29,-v92,v21,v29
v_mfma_f32_16x16x32_bf16 v[36:39],a[40:43],v[48:51],v[36:39]
s_mov_b64 s[46:47],s[48:49]
ds_read_b128 v[44:47],v2 offset:45056
v_fma_f32 v30,-v92,v22,v30
v_mfma_f32_16x16x32_bf16 v[40:43],a[44:47],v[48:51],v[40:43]
v_fma_f32 v31,-v92,v23,v31
v_mul_f32 v76,v28,v28
v_mfma_f32_16x16x32_bf16 v[104:107],v[100:103],v[48:51],v[104:107]
buffer_load_dwordx4 v1,s[36:39],s67 offen offset:2048 sc1 lds
v_mfma_f32_16x16x32_bf16 v[108:111],v[48:51],v[48:51],v[108:111]
v_mul_f32 v77,v29,v29
v_mul_f32 v78,v30,v30
v_mfma_f32_16x16x32_bf16 v[36:39],a[48:51],v[52:55],v[36:39]
ds_read_b128 v[48:51],v2 offset:46080
v_mul_f32 v79,v31,v31
v_mfma_f32_16x16x32_bf16 v[40:43],a[52:55],v[52:55],v[40:43]
v_fma_f32 v76,v94,v76,v93
v_mul_f32 v76,v28,v76)"
R"(
v_mfma_f32_16x16x32_bf16 v[104:107],v[100:103],v[52:55],v[104:107]
buffer_load_dwordx4 v1,s[36:39],s67 offen offset:3072 sc1 lds
v_mfma_f32_16x16x32_bf16 v[108:111],v[52:55],v[52:55],v[108:111]
s_add_u32 m0,s71,s88
v_exp_f32 v76,v76
v_mfma_f32_16x16x32_bf16 v[36:39],a[56:59],v[56:59],v[36:39]
buffer_load_dword v10,s[36:39],s72 offen offset:0 sc1 lds
v_mfma_f32_16x16x32_bf16 v[40:43],a[60:63],v[56:59],v[40:43]
s_lshr_b32 s73,s48,0
ds_read_b128 v[52:55],v2 offset:47104
v_fma_f32 v76,v76,v95,v95
v_mfma_f32_16x16x32_bf16 v[104:107],v[100:103],v[56:59],v[104:107]
s_lshr_b32 s69,s52,5
v_rcp_f32 v76,v76
v_mfma_f32_16x16x32_bf16 v[108:111],v[56:59],v[56:59],v[108:111]
s_waitcnt lgkmcnt(3)
v_fma_f32 v77,v94,v77,v93
v_mul_f32 v77,v29,v77
v_mfma_f32_16x16x32_bf16 v[36:39],a[0:3],v[60:63],v[36:39]
s_cmp_lt_u32 s52,4194304
ds_read_b128 v[56:59],v2 offset:48128
v_fma_f32 v78,v94,v78,v93
v_mfma_f32_16x16x32_bf16 v[40:43],a[4:7],v[60:63],v[40:43]
s_cselect_b32 s68,0xffffcfff,-1
v_exp_f32 v77,v77
v_mfma_f32_16x16x32_bf16 v[36:39],a[8:11],v[64:67],v[36:39]
s_and_b32 s68,s68,s69
ds_read_b128 v[60:63],v2 offset:40960
v_mul_f32 v78,v30,v78
v_mfma_f32_16x16x32_bf16 v[40:43],a[12:15],v[64:67],v[40:43]
s_cmp_lt_u32 s52,s89
v_exp_f32 v78,v78
v_mfma_f32_16x16x32_bf16 v[36:39],a[16:19],v[68:71],v[36:39]
s_cselect_b32 s68,s59,s68
ds_read_b128 v[64:67],v2 offset:41984
v_fma_f32 v77,v77,v95,v95
v_mfma_f32_16x16x32_bf16 v[40:43],a[20:23],v[68:71],v[40:43]
s_add_u32 m0,s78,256
v_rcp_f32 v77,v77
v_mfma_f32_16x16x32_bf16 v[36:39],a[24:27],v[72:75],v[36:39]
s_xor_b32 s69,s63,65536
ds_read_b128 v[68:71],v2 offset:43008
v_fma_f32 v78,v78,v95,v95
v_mfma_f32_16x16x32_bf16 v[40:43],a[28:31],v[72:75],v[40:43]
s_waitcnt lgkmcnt(3)
v_rcp_f32 v78,v78
v_mfma_f32_16x16x32_bf16 v[36:39],a[96:99],v[44:47],v[36:39]
s_waitcnt vmcnt(10)
ds_read_b128 v[72:75],v2 offset:44032
v_fma_f32 v79,v94,v79,v93
v_mfma_f32_16x16x32_bf16 v[40:43],a[100:103],v[44:47],v[40:43]
s_barrier
v_mfma_f32_16x16x32_bf16 v[36:39],a[104:107],v[48:51],v[36:39]
s_cmp_ge_u32 s67,s59
ds_read_b128 v[44:47],v2 offset:53248
ds_read_b32 v16,v7 offset:0
v_mfma_f32_16x16x32_bf16 v[40:43],a[108:111],v[48:51],v[40:43]
s_cselect_b32 s62,s69,s64
v_mul_f32 v79,v31,v79
v_mul_f32 v28,v28,v76
v_mfma_f32_16x16x32_bf16 v[36:39],a[112:115],v[52:55],v[36:39]
s_cselect_b32 s86,s50,s51
ds_read_b128 v[48:51],v2 offset:54272
v_mul_f32 v29,v29,v77
v_mfma_f32_16x16x32_bf16 v[40:43],a[116:119],v[52:55],v[40:43]
buffer_load_dword v6,s[36:39],s68 offen offset:0 sc1 lds
v_mfma_f32_16x16x32_bf16 v[36:39],a[120:123],v[56:59],v[36:39]
s_waitcnt lgkmcnt(1)
ds_read_b128 v[52:55],v2 offset:55296
v_mul_f32 v30,v30,v78
v_mfma_f32_16x16x32_bf16 v[40:43],a[124:127],v[56:59],v[40:43]
v_cmp_eq_u32 vcc,s60,v16
v_exp_f32 v79,v79
v_cvt_pk_bf16_f32 v84,v28,v29
v_mfma_f32_16x16x32_bf16 v[36:39],a[64:67],v[60:63],v[36:39]
s_mov_b32 m0,s62
ds_read_b128 v[56:59],v2 offset:56320
v_fma_f32 v79,v79,v95,v95
v_mfma_f32_16x16x32_bf16 v[40:43],a[68:71],v[60:63],v[40:43]
s_cselect_b32 s87,vcc_lo,vcc_hi
v_rcp_f32 v79,v79
v_mfma_f32_16x16x32_bf16 v[36:39],a[72:75],v[64:67],v[36:39]
s_cmp_lt_u32 s86,s89
ds_read_b128 v[60:63],v2 offset:49152
v_mul_f32 v31,v31,v79
v_mfma_f32_16x16x32_bf16 v[40:43],a[76:79],v[64:67],v[40:43]
s_cselect_b32 s87,-1,s87
v_cvt_pk_bf16_f32 v85,v30,v31
v_fma_f32 v32,-v92,v24,v32
v_mfma_f32_16x16x32_bf16 v[36:39],a[80:83],v[68:71],v[36:39]
s_cmp_eq_u32 s87,-1
ds_read_b128 v[64:67],v2 offset:50176
v_fma_f32 v33,-v92,v25,v33
v_mfma_f32_16x16x32_bf16 v[40:43],a[84:87],v[68:71],v[40:43]
s_cselect_b32 s66,s86,s59
v_fma_f32 v34,-v92,v26,v34
v_fma_f32 v35,-v92,v27,v35
v_mfma_f32_16x16x32_bf16 v[36:39],a[88:91],v[72:75],v[36:39]
s_xor_b32 s64,s64,65536
ds_read_b128 v[68:71],v2 offset:51200
v_mul_f32 v80,v32,v32
v_mfma_f32_16x16x32_bf16 v[40:43],a[92:95],v[72:75],v[40:43]
s_waitcnt lgkmcnt(3)
v_mul_f32 v81,v33,v33
v_mul_f32 v82,v34,v34
v_mfma_f32_16x16x32_bf16 v[36:39],a[160:163],v[44:47],v[36:39]
s_sub_u32 s69,s66,1048576
ds_read_b128 v[72:75],v2 offset:52224
v_mul_f32 v83,v35,v35
v_mfma_f32_16x16x32_bf16 v[40:43],a[164:167],v[44:47],v[40:43]
s_cmp_lt_u32 s69,3145728
v_fma_f32 v80,v94,v80,v93
v_mul_f32 v80,v32,v80
v_mfma_f32_16x16x32_bf16 v[104:107],v[100:103],v[44:47],v[104:107]
s_cselect_b32 s89,4194304,s89
v_exp_f32 v80,v80
v_mfma_f32_16x16x32_bf16 v[108:111],v[44:47],v[44:47],v[108:111]
s_add_u32 s74,s53,s55
v_fma_f32 v80,v80,v95,v95
v_fma_f32 v81,v94,v81,v93
v_mfma_f32_16x16x32_bf16 v[36:39],a[168:171],v[48:51],v[36:39]
s_cmp_eq_u32 s74,s56
ds_read_b128 v[44:47],v2 offset:61440
v_mul_f32 v81,v33,v81
v_mfma_f32_16x16x32_bf16 v[40:43],a[172:175],v[48:51],v[40:43]
s_cselect_b32 s74,s57,s74
v_exp_f32 v81,v81
v_mfma_f32_16x16x32_bf16 v[104:107],v[100:103],v[48:51],v[104:107]
s_cmp_eq_u32 s74,s58
v_rcp_f32 v80,v80
v_mfma_f32_16x16x32_bf16 v[108:111],v[48:51],v[48:51],v[108:111]
s_cselect_b32 s74,s59,s74
v_fma_f32 v81,v81,v95,v95
v_fma_f32 v82,v94,v82,v93
v_mfma_f32_16x16x32_bf16 v[36:39],a[176:179],v[52:55],v[36:39]
s_mov_b64 s[48:49],s[50:51]
ds_read_b128 v[48:51],v2 offset:62464
v_mul_f32 v82,v34,v82
v_mfma_f32_16x16x32_bf16 v[40:43],a[180:183],v[52:55],v[40:43]
s_mov_b64 s[50:51],s[52:53]
v_exp_f32 v82,v82
v_mfma_f32_16x16x32_bf16 v[104:107],v[100:103],v[52:55],v[104:107]
s_mov_b32 s52,s74
v_rcp_f32 v81,v81
v_mfma_f32_16x16x32_bf16 v[108:111],v[52:55],v[52:55],v[108:111]
v_fma_f32 v82,v82,v95,v95
v_fma_f32 v83,v94,v83,v93
v_mfma_f32_16x16x32_bf16 v[36:39],a[184:187],v[56:59],v[36:39]
ds_read_b128 v[52:55],v2 offset:63488
v_mul_f32 v83,v35,v83
v_mfma_f32_16x16x32_bf16 v[40:43],a[188:191],v[56:59],v[40:43]
buffer_load_dwordx4 v0,s[36:39],s66 offen offset:0 sc1 lds
v_mfma_f32_16x16x32_bf16 v[104:107],v[100:103],v[56:59],v[104:107]
v_exp_f32 v83,v83
v_mfma_f32_16x16x32_bf16 v[108:111],v[56:59],v[56:59],v[108:111]
s_waitcnt lgkmcnt(3)
v_rcp_f32 v82,v82
v_mfma_f32_16x16x32_bf16 v[36:39],a[128:131],v[60:63],v[36:39]
ds_read_b128 v[56:59],v2 offset:64512
v_fma_f32 v83,v83,v95,v95
v_mfma_f32_16x16x32_bf16 v[40:43],a[132:135],v[60:63],v[40:43]
buffer_load_dwordx4 v0,s[36:39],s66 offen offset:1024 sc1 lds
v_mfma_f32_16x16x32_bf16 v[36:39],a[136:139],v[64:67],v[36:39]
v_cndmask_b32 v112,v108,v109,s[82:83]
v_cndmask_b32 v113,v110,v111,s[82:83]
v_cndmask_b32 v112,v112,v113,s[84:85]
v_cndmask_b32 v112,v104,v112,s[98:99]
v_mfma_f32_16x16x32_bf16 v[40:43],a[140:143],v[64:67],v[40:43]
ds_read_b128 v[60:63],v2 offset:57344
ds_write_b32 v13,v112 offset:2048
v_mfma_f32_16x16x32_bf16 v[36:39],a[144:147],v[68:71],v[36:39]
ds_read_b128 v[64:67],v2 offset:58368
v_mul_f32 v32,v32,v80
v_mfma_f32_16x16x32_bf16 v[40:43],a[148:151],v[68:71],v[40:43]
buffer_load_dwordx4 v0,s[36:39],s66 offen offset:2048 sc1 lds
v_mfma_f32_16x16x32_bf16 v[36:39],a[152:155],v[72:75],v[36:39]
ds_read_b128 v[68:71],v2 offset:59392
v_mul_f32 v33,v33,v81
v_mfma_f32_16x16x32_bf16 v[40:43],a[156:159],v[72:75],v[40:43]
s_waitcnt lgkmcnt(4)
v_rcp_f32 v83,v83
v_mfma_f32_16x16x32_bf16 v[36:39],a[224:227],v[44:47],v[36:39]
s_waitcnt vmcnt(10) lgkmcnt(2)
ds_read_b128 v[72:75],v2 offset:60416
v_xor_b32 v2,65536,v2
v_mfma_f32_16x16x32_bf16 v[40:43],a[228:231],v[44:47],v[40:43]
s_barrier
v_mfma_f32_16x16x32_bf16 v[36:39],a[232:235],v[48:51],v[36:39]
ds_read_b128 v[116:119],v14 offset:2048
ds_read_b128 v[120:123],v14 offset:2064
v_mfma_f32_16x16x32_bf16 v[40:43],a[236:239],v[48:51],v[40:43]
ds_read_b128 v[44:47],v2 offset:4096
buffer_load_dwordx4 v0,s[36:39],s66 offen offset:3072 sc1 lds
v_mfma_f32_16x16x32_bf16 v[36:39],a[240:243],v[52:55],v[36:39]
s_add_u32 m0,s62,16384
ds_read_b128 v[48:51],v2 offset:5120
v_mul_f32 v34,v34,v82
v_mfma_f32_16x16x32_bf16 v[40:43],a[244:247],v[52:55],v[40:43]
s_cmp_lt_u32 s67,4194304
v_mul_f32 v35,v35,v83)"
R"(
v_cvt_pk_bf16_f32 v86,v32,v33
v_mfma_f32_16x16x32_bf16 v[36:39],a[248:251],v[56:59],v[36:39]
s_waitcnt lgkmcnt(2)
ds_read_b128 v[52:55],v2 offset:6144
v_cvt_pk_bf16_f32 v87,v34,v35
v_mfma_f32_16x16x32_bf16 v[40:43],a[252:255],v[56:59],v[40:43]
buffer_load_dwordx4 v1,s[36:39],s66 offen offset:0 sc1 lds
v_mfma_f32_16x16x32_bf16 v[36:39],a[192:195],v[60:63],v[36:39]
ds_read_b128 v[56:59],v2 offset:7168
v_add_f32 v116,v116,v117
v_mfma_f32_16x16x32_bf16 v[40:43],a[196:199],v[60:63],v[40:43]
v_add_f32 v118,v118,v119
v_add_f32 v116,v116,v118
v_mfma_f32_16x16x32_bf16 v[36:39],a[200:203],v[64:67],v[36:39]
ds_read_b128 v[60:63],v2 offset:0
v_mul_f32 v96,s75,v116
v_mfma_f32_16x16x32_bf16 v[40:43],a[204:207],v[64:67],v[40:43]
v_add_f32 v120,v120,v121
v_add_f32 v122,v122,v123
v_mfma_f32_16x16x32_bf16 v[36:39],a[208:211],v[68:71],v[36:39]
buffer_store_dwordx4 v[84:87],v3,s[36:39],s73 offen offset:0 sc0 sc1
ds_read_b128 v[64:67],v2 offset:1024
v_add_f32 v120,v120,v122
v_mul_f32 v120,s75,v120
v_mfma_f32_16x16x32_bf16 v[40:43],a[212:215],v[68:71],v[40:43]
v_fma_f32 v120,-v96,v96,v120
v_max_f32 v120,0,v120
v_add_f32 v115,v15,v120
v_fma_f32 v120,v15,v115,v120
v_rsq_f32 v116,v120
v_mfma_f32_16x16x32_bf16 v[36:39],a[216:219],v[72:75],v[36:39]
s_waitcnt lgkmcnt(2)
ds_read_b128 v[68:71],v2 offset:2048
v_xor_b32 v8,16384,v8
v_mul_f32 v97,s76,v116
v_mul_f32 v98,v116,v116
v_mul_f32 v99,v120,v116
v_mul_f32 v98,s77,v98
v_mul_f32 v98,v98,v97
v_mfma_f32_16x16x32_bf16 v[40:43],a[220:223],v[72:75],v[40:43]
s_cbranch_scc1 Lg1_a0w1_%=
s_cmp_ge_u32 s48,4194304
s_cselect_b64 s[90:91],s[94:95],s[90:91]
s_cselect_b64 s[92:93],s[96:97],s[92:93]
s_cmp_lt_u32 s67,s59
s_cbranch_scc1 Lg1_d0w1_%=
s_branch Lg1_slow0_%=
Lg1_a0w2_%=:
v_mfma_f32_16x16x32_bf16 v[28:31],a[64:67],v[44:47],0
s_add_u32 m0,s62,16384
ds_read_b128 v[72:75],v2 offset:15360
s_lshr_b32 s72,s66,6
v_mfma_f32_16x16x32_bf16 v[32:35],a[68:71],v[44:47],0
s_lshr_b32 s71,s62,2
s_cmp_lt_u32 s66,4194304
s_cselect_b32 s72,s59,s72
v_mfma_f32_16x16x32_bf16 v[104:107],v[100:103],v[44:47],0
v_mfma_f32_16x16x32_bf16 v[108:111],v[44:47],v[44:47],0
buffer_load_dwordx4 v1,s[36:39],s66 offen offset:1024 sc1 lds
v_mfma_f32_16x16x32_bf16 v[28:31],a[72:75],v[48:51],v[28:31]
ds_read_b128 v[44:47],v2 offset:0
v_fma_f32 v36,-v96,v20,v36
v_mfma_f32_16x16x32_bf16 v[32:35],a[76:79],v[48:51],v[32:35]
v_fma_f32 v37,-v96,v21,v37
v_fma_f32 v38,-v96,v22,v38
v_mfma_f32_16x16x32_bf16 v[104:107],v[100:103],v[48:51],v[104:107]
v_fma_f32 v39,-v96,v23,v39
v_mul_f32 v76,v36,v36
v_mfma_f32_16x16x32_bf16 v[108:111],v[48:51],v[48:51],v[108:111]
buffer_load_dwordx4 v1,s[36:39],s66 offen offset:2048 sc1 lds
v_mfma_f32_16x16x32_bf16 v[28:31],a[80:83],v[52:55],v[28:31]
ds_read_b128 v[48:51],v2 offset:1024
v_mul_f32 v77,v37,v37
v_mfma_f32_16x16x32_bf16 v[32:35],a[84:87],v[52:55],v[32:35]
v_mul_f32 v78,v38,v38
v_mul_f32 v79,v39,v39
v_mfma_f32_16x16x32_bf16 v[104:107],v[100:103],v[52:55],v[104:107]
v_fma_f32 v76,v98,v76,v97
v_mul_f32 v76,v36,v76
v_mfma_f32_16x16x32_bf16 v[108:111],v[52:55],v[52:55],v[108:111]
buffer_load_dwordx4 v1,s[36:39],s66 offen offset:3072 sc1 lds
v_mfma_f32_16x16x32_bf16 v[28:31],a[88:91],v[56:59],v[28:31]
s_add_u32 m0,s71,s88
ds_read_b128 v[52:55],v2 offset:2048
v_fma_f32 v77,v98,v77,v97
v_mfma_f32_16x16x32_bf16 v[32:35],a[92:95],v[56:59],v[32:35]
buffer_load_dword v10,s[36:39],s72 offen offset:0 sc1 lds
v_mfma_f32_16x16x32_bf16 v[104:107],v[100:103],v[56:59],v[104:107]
s_lshr_b32 s73,s47,0
v_exp_f32 v76,v76
v_mfma_f32_16x16x32_bf16 v[108:111],v[56:59],v[56:59],v[108:111]
s_waitcnt lgkmcnt(3)
v_mul_f32 v77,v37,v77
v_fma_f32 v76,v76,v99,v99
v_mfma_f32_16x16x32_bf16 v[28:31],a[96:99],v[60:63],v[28:31]
s_lshr_b32 s65,s48,6
ds_read_b128 v[56:59],v2 offset:3072
v_fma_f32 v78,v98,v78,v97
v_mfma_f32_16x16x32_bf16 v[32:35],a[100:103],v[60:63],v[32:35]
s_xor_b32 s69,s64,65536
v_exp_f32 v77,v77
v_mfma_f32_16x16x32_bf16 v[28:31],a[104:107],v[64:67],v[28:31]
s_cmp_ge_u32 s66,s59
ds_read_b128 v[60:63],v2 offset:4096
v_mul_f32 v78,v38,v78
v_mfma_f32_16x16x32_bf16 v[32:35],a[108:111],v[64:67],v[32:35]
s_cselect_b32 s62,s69,s63
v_exp_f32 v78,v78
v_mfma_f32_16x16x32_bf16 v[28:31],a[112:115],v[68:71],v[28:31]
s_cselect_b32 s86,s49,s50
ds_read_b128 v[64:67],v2 offset:5120
v_fma_f32 v77,v77,v99,v99
v_mfma_f32_16x16x32_bf16 v[32:35],a[116:119],v[68:71],v[32:35]
s_mov_b32 m0,s62
v_rcp_f32 v76,v76
v_mfma_f32_16x16x32_bf16 v[28:31],a[120:123],v[72:75],v[28:31]
ds_read_b128 v[68:71],v2 offset:6144
v_fma_f32 v78,v78,v99,v99
v_mfma_f32_16x16x32_bf16 v[32:35],a[124:127],v[72:75],v[32:35]
s_waitcnt lgkmcnt(3)
v_rcp_f32 v77,v77
v_mfma_f32_16x16x32_bf16 v[28:31],a[0:3],v[44:47],v[28:31]
s_waitcnt vmcnt(10)
ds_read_b128 v[72:75],v2 offset:7168
v_fma_f32 v79,v98,v79,v97
v_mfma_f32_16x16x32_bf16 v[32:35],a[4:7],v[44:47],v[32:35]
s_barrier
v_mfma_f32_16x16x32_bf16 v[28:31],a[8:11],v[48:51],v[28:31]
ds_read_b128 v[44:47],v2 offset:24576
ds_read_b32 v16,v7 offset:256
v_mfma_f32_16x16x32_bf16 v[32:35],a[12:15],v[48:51],v[32:35]
v_rcp_f32 v78,v78
v_mfma_f32_16x16x32_bf16 v[28:31],a[16:19],v[52:55],v[28:31]
ds_read_b128 v[48:51],v2 offset:25600
v_mul_f32 v79,v39,v79
v_mfma_f32_16x16x32_bf16 v[32:35],a[20:23],v[52:55],v[32:35]
v_exp_f32 v79,v79
v_mfma_f32_16x16x32_bf16 v[28:31],a[24:27],v[56:59],v[28:31]
s_waitcnt lgkmcnt(1)
ds_read_b128 v[52:55],v2 offset:26624
v_fma_f32 v79,v79,v99,v99
v_mfma_f32_16x16x32_bf16 v[32:35],a[28:31],v[56:59],v[32:35]
v_cmp_eq_u32 vcc,s60,v16
v_rcp_f32 v79,v79
v_mul_f32 v36,v36,v76
v_mfma_f32_16x16x32_bf16 v[28:31],a[32:35],v[60:63],v[28:31]
s_cselect_b32 s87,vcc_lo,vcc_hi
ds_read_b128 v[56:59],v2 offset:27648
v_mul_f32 v37,v37,v77
v_mfma_f32_16x16x32_bf16 v[32:35],a[36:39],v[60:63],v[32:35]
s_cmp_lt_u32 s86,s89
v_mul_f32 v38,v38,v78
v_mul_f32 v39,v39,v79
v_mfma_f32_16x16x32_bf16 v[28:31],a[40:43],v[64:67],v[28:31]
s_cselect_b32 s87,-1,s87
ds_read_b128 v[60:63],v2 offset:28672
v_cvt_pk_bf16_f32 v84,v36,v37
v_mfma_f32_16x16x32_bf16 v[32:35],a[44:47],v[64:67],v[32:35]
s_cmp_eq_u32 s87,-1
v_cvt_pk_bf16_f32 v85,v38,v39
v_fma_f32 v40,-v96,v24,v40
v_mfma_f32_16x16x32_bf16 v[28:31],a[48:51],v[68:71],v[28:31]
s_cselect_b32 s67,s86,s59
ds_read_b128 v[64:67],v2 offset:29696
v_fma_f32 v41,-v96,v25,v41
v_mfma_f32_16x16x32_bf16 v[32:35],a[52:55],v[68:71],v[32:35]
s_xor_b32 s63,s63,65536
v_fma_f32 v42,-v96,v26,v42
v_fma_f32 v43,-v96,v27,v43
v_mfma_f32_16x16x32_bf16 v[28:31],a[56:59],v[72:75],v[28:31]
s_sub_u32 s69,s67,1048576
ds_read_b128 v[68:71],v2 offset:30720
v_mul_f32 v80,v40,v40
v_mfma_f32_16x16x32_bf16 v[32:35],a[60:63],v[72:75],v[32:35]
s_waitcnt lgkmcnt(3)
v_mul_f32 v81,v41,v41
v_mul_f32 v82,v42,v42
v_mfma_f32_16x16x32_bf16 v[28:31],a[192:195],v[44:47],v[28:31]
s_cmp_lt_u32 s69,3145728
ds_read_b128 v[72:75],v2 offset:31744
v_mul_f32 v83,v43,v43
v_mfma_f32_16x16x32_bf16 v[32:35],a[196:199],v[44:47],v[32:35]
s_cselect_b32 s89,4194304,s89
v_fma_f32 v80,v98,v80,v97
v_mul_f32 v80,v40,v80
v_mfma_f32_16x16x32_bf16 v[104:107],v[100:103],v[44:47],v[104:107]
s_add_u32 s53,s52,s55
v_exp_f32 v80,v80
v_mfma_f32_16x16x32_bf16 v[108:111],v[44:47],v[44:47],v[108:111]
s_cmp_eq_u32 s53,s56
v_fma_f32 v80,v80,v99,v99
v_fma_f32 v81,v98,v81,v97
v_mfma_f32_16x16x32_bf16 v[28:31],a[200:203],v[48:51],v[28:31]
s_cselect_b32 s53,s57,s53
ds_read_b128 v[44:47],v2 offset:16384
v_mul_f32 v81,v41,v81
v_mfma_f32_16x16x32_bf16 v[32:35],a[204:207],v[48:51],v[32:35]
s_cmp_eq_u32 s53,s58
v_exp_f32 v81,v81
v_mfma_f32_16x16x32_bf16 v[104:107],v[100:103],v[48:51],v[104:107])"
R"(
s_cselect_b32 s53,s59,s53
v_rcp_f32 v80,v80
v_mfma_f32_16x16x32_bf16 v[108:111],v[48:51],v[48:51],v[108:111]
v_fma_f32 v81,v81,v99,v99
v_fma_f32 v82,v98,v82,v97
v_mfma_f32_16x16x32_bf16 v[28:31],a[208:211],v[52:55],v[28:31]
ds_read_b128 v[48:51],v2 offset:17408
v_mul_f32 v82,v42,v82
v_mfma_f32_16x16x32_bf16 v[32:35],a[212:215],v[52:55],v[32:35]
v_exp_f32 v82,v82
v_mfma_f32_16x16x32_bf16 v[104:107],v[100:103],v[52:55],v[104:107]
v_rcp_f32 v81,v81
v_mfma_f32_16x16x32_bf16 v[108:111],v[52:55],v[52:55],v[108:111]
v_fma_f32 v82,v82,v99,v99
v_fma_f32 v83,v98,v83,v97
v_mfma_f32_16x16x32_bf16 v[28:31],a[216:219],v[56:59],v[28:31]
ds_read_b128 v[52:55],v2 offset:18432
v_mul_f32 v83,v43,v83
v_mfma_f32_16x16x32_bf16 v[32:35],a[220:223],v[56:59],v[32:35]
v_exp_f32 v83,v83
v_mfma_f32_16x16x32_bf16 v[104:107],v[100:103],v[56:59],v[104:107]
buffer_load_dwordx4 v0,s[36:39],s67 offen offset:0 sc1 lds
v_mfma_f32_16x16x32_bf16 v[108:111],v[56:59],v[56:59],v[108:111]
s_waitcnt lgkmcnt(3)
v_rcp_f32 v82,v82
v_mfma_f32_16x16x32_bf16 v[28:31],a[224:227],v[60:63],v[28:31]
ds_read_b128 v[56:59],v2 offset:19456
v_fma_f32 v83,v83,v99,v99
v_mfma_f32_16x16x32_bf16 v[32:35],a[228:231],v[60:63],v[32:35]
v_rcp_f32 v83,v83
v_mfma_f32_16x16x32_bf16 v[28:31],a[232:235],v[64:67],v[28:31]
buffer_load_dwordx4 v0,s[36:39],s67 offen offset:1024 sc1 lds
v_cndmask_b32 v112,v108,v109,s[82:83]
v_cndmask_b32 v113,v110,v111,s[82:83]
v_cndmask_b32 v112,v112,v113,s[84:85]
v_cndmask_b32 v112,v104,v112,s[98:99]
v_mfma_f32_16x16x32_bf16 v[32:35],a[236:239],v[64:67],v[32:35]
ds_read_b128 v[60:63],v2 offset:20480
ds_write_b32 v13,v112 offset:0
v_mfma_f32_16x16x32_bf16 v[28:31],a[240:243],v[68:71],v[28:31]
ds_read_b128 v[64:67],v2 offset:21504
v_mul_f32 v40,v40,v80
v_mfma_f32_16x16x32_bf16 v[32:35],a[244:247],v[68:71],v[32:35]
v_mul_f32 v41,v41,v81
v_mul_f32 v42,v42,v82
v_mfma_f32_16x16x32_bf16 v[28:31],a[248:251],v[72:75],v[28:31]
buffer_load_dwordx4 v0,s[36:39],s67 offen offset:2048 sc1 lds
v_mfma_f32_16x16x32_bf16 v[32:35],a[252:255],v[72:75],v[32:35]
s_waitcnt lgkmcnt(3)
ds_read_b128 v[68:71],v2 offset:22528
v_mul_f32 v43,v43,v83
v_mfma_f32_16x16x32_bf16 v[28:31],a[128:131],v[44:47],v[28:31]
s_waitcnt vmcnt(9) lgkmcnt(2)
ds_read_b128 v[72:75],v2 offset:23552
v_cvt_pk_bf16_f32 v86,v40,v41
v_mfma_f32_16x16x32_bf16 v[32:35],a[132:135],v[44:47],v[32:35]
s_barrier
v_mfma_f32_16x16x32_bf16 v[28:31],a[136:139],v[48:51],v[28:31]
ds_read_b128 v[116:119],v14 offset:0
ds_read_b128 v[120:123],v14 offset:16
v_mfma_f32_16x16x32_bf16 v[32:35],a[140:143],v[48:51],v[32:35]
ds_read_b128 v[44:47],v2 offset:40960
v_cvt_pk_bf16_f32 v87,v42,v43
v_mfma_f32_16x16x32_bf16 v[28:31],a[144:147],v[52:55],v[28:31]
buffer_load_dwordx4 v0,s[36:39],s67 offen offset:3072 sc1 lds
v_mfma_f32_16x16x32_bf16 v[32:35],a[148:151],v[52:55],v[32:35]
s_add_u32 m0,s62,16384
ds_read_b128 v[48:51],v2 offset:41984
s_cmp_ge_u32 s66,s59
v_mfma_f32_16x16x32_bf16 v[28:31],a[152:155],v[56:59],v[28:31]
s_waitcnt lgkmcnt(2)
ds_read_b128 v[52:55],v2 offset:43008
v_add_f32 v116,v116,v117
v_mfma_f32_16x16x32_bf16 v[32:35],a[156:159],v[56:59],v[32:35]
v_add_f32 v118,v118,v119
v_add_f32 v116,v116,v118
v_mfma_f32_16x16x32_bf16 v[28:31],a[160:163],v[60:63],v[28:31]
buffer_load_dwordx4 v1,s[36:39],s67 offen offset:0 sc1 lds
v_mfma_f32_16x16x32_bf16 v[32:35],a[164:167],v[60:63],v[32:35]
ds_read_b128 v[56:59],v2 offset:44032
v_mul_f32 v92,s75,v116
v_mfma_f32_16x16x32_bf16 v[28:31],a[168:171],v[64:67],v[28:31]
ds_read_b128 v[60:63],v2 offset:45056
v_add_f32 v120,v120,v121
v_mfma_f32_16x16x32_bf16 v[32:35],a[172:175],v[64:67],v[32:35]
v_add_f32 v122,v122,v123
v_add_f32 v120,v120,v122
v_mfma_f32_16x16x32_bf16 v[28:31],a[176:179],v[68:71],v[28:31]
v_mul_f32 v120,s75,v120
v_fma_f32 v120,-v92,v92,v120
v_max_f32 v120,0,v120
v_add_f32 v115,v15,v120
v_fma_f32 v120,v15,v115,v120
v_rsq_f32 v115,v115
v_mfma_f32_16x16x32_bf16 v[32:35],a[180:183],v[68:71],v[32:35]
ds_read_b128 v[64:67],v2 offset:46080
buffer_store_dwordx4 v[84:87],v3,s[36:39],s73 offen offset:0 sc0 sc1
v_rsq_f32 v116,v120
v_mul_f32_e64 v114,-v92,v115
v_mfma_f32_16x16x32_bf16 v[28:31],a[184:187],v[72:75],v[28:31]
s_waitcnt lgkmcnt(2)
ds_read_b128 v[68:71],v2 offset:47104
buffer_store_dwordx2 v[114:115],v12,s[36:39],s65 offen offset:0 sc0 sc1
v_mul_f32 v93,s76,v116
v_mul_f32 v94,v116,v116
v_mul_f32 v95,v120,v116
v_mul_f32 v94,s77,v94
v_mul_f32 v94,v94,v93
v_mfma_f32_16x16x32_bf16 v[32:35],a[188:191],v[72:75],v[32:35]
s_cbranch_scc1 Lg1_slow1_%=
Lg1_a1w2_%=:
v_mfma_f32_16x16x32_bf16 v[36:39],a[64:67],v[44:47],0
s_add_u32 m0,s62,16384
ds_read_b128 v[72:75],v2 offset:48128
s_lshr_b32 s72,s67,6
v_mfma_f32_16x16x32_bf16 v[40:43],a[68:71],v[44:47],0
s_lshr_b32 s71,s62,2
s_cmp_lt_u32 s67,4194304
s_cselect_b32 s72,s59,s72
v_mfma_f32_16x16x32_bf16 v[104:107],v[100:103],v[44:47],0
s_mov_b64 s[44:45],s[46:47]
s_mov_b64 s[46:47],s[48:49]
v_mfma_f32_16x16x32_bf16 v[108:111],v[44:47],v[44:47],0
buffer_load_dwordx4 v1,s[36:39],s67 offen offset:1024 sc1 lds
v_mfma_f32_16x16x32_bf16 v[36:39],a[72:75],v[48:51],v[36:39]
ds_read_b128 v[44:47],v2 offset:32768
v_fma_f32 v28,-v92,v20,v28
v_mfma_f32_16x16x32_bf16 v[40:43],a[76:79],v[48:51],v[40:43]
v_fma_f32 v29,-v92,v21,v29
v_fma_f32 v30,-v92,v22,v30
v_mfma_f32_16x16x32_bf16 v[104:107],v[100:103],v[48:51],v[104:107]
v_fma_f32 v31,-v92,v23,v31
v_mul_f32 v76,v28,v28
v_mfma_f32_16x16x32_bf16 v[108:111],v[48:51],v[48:51],v[108:111]
buffer_load_dwordx4 v1,s[36:39],s67 offen offset:2048 sc1 lds
v_mfma_f32_16x16x32_bf16 v[36:39],a[80:83],v[52:55],v[36:39]
ds_read_b128 v[48:51],v2 offset:33792
v_mul_f32 v77,v29,v29
v_mfma_f32_16x16x32_bf16 v[40:43],a[84:87],v[52:55],v[40:43]
v_mul_f32 v78,v30,v30
v_mul_f32 v79,v31,v31
v_mfma_f32_16x16x32_bf16 v[104:107],v[100:103],v[52:55],v[104:107]
v_fma_f32 v76,v94,v76,v93
v_mul_f32 v76,v28,v76
v_mfma_f32_16x16x32_bf16 v[108:111],v[52:55],v[52:55],v[108:111]
buffer_load_dwordx4 v1,s[36:39],s67 offen offset:3072 sc1 lds
v_mfma_f32_16x16x32_bf16 v[36:39],a[88:91],v[56:59],v[36:39]
s_add_u32 m0,s71,s88
ds_read_b128 v[52:55],v2 offset:34816
v_fma_f32 v77,v94,v77,v93
v_mfma_f32_16x16x32_bf16 v[40:43],a[92:95],v[56:59],v[40:43]
buffer_load_dword v10,s[36:39],s72 offen offset:0 sc1 lds
v_mfma_f32_16x16x32_bf16 v[104:107],v[100:103],v[56:59],v[104:107]
s_lshr_b32 s73,s48,0
v_exp_f32 v76,v76
v_mfma_f32_16x16x32_bf16 v[108:111],v[56:59],v[56:59],v[108:111]
s_waitcnt lgkmcnt(3)
v_mul_f32 v77,v29,v77
v_fma_f32 v76,v76,v95,v95
v_mfma_f32_16x16x32_bf16 v[36:39],a[96:99],v[60:63],v[36:39]
s_lshr_b32 s65,s49,6
ds_read_b128 v[56:59],v2 offset:35840
v_fma_f32 v78,v94,v78,v93
v_mfma_f32_16x16x32_bf16 v[40:43],a[100:103],v[60:63],v[40:43]
s_xor_b32 s69,s63,65536
v_exp_f32 v77,v77
v_mfma_f32_16x16x32_bf16 v[36:39],a[104:107],v[64:67],v[36:39]
s_cmp_ge_u32 s67,s59
ds_read_b128 v[60:63],v2 offset:36864
v_mul_f32 v78,v30,v78
v_mfma_f32_16x16x32_bf16 v[40:43],a[108:111],v[64:67],v[40:43]
s_cselect_b32 s62,s69,s64
v_exp_f32 v78,v78
v_mfma_f32_16x16x32_bf16 v[36:39],a[112:115],v[68:71],v[36:39]
s_cselect_b32 s86,s50,s51
ds_read_b128 v[64:67],v2 offset:37888
v_fma_f32 v77,v77,v95,v95
v_mfma_f32_16x16x32_bf16 v[40:43],a[116:119],v[68:71],v[40:43]
s_mov_b32 m0,s62
v_rcp_f32 v76,v76
v_mfma_f32_16x16x32_bf16 v[36:39],a[120:123],v[72:75],v[36:39]
s_mov_b64 s[48:49],s[50:51]
ds_read_b128 v[68:71],v2 offset:38912
v_fma_f32 v78,v78,v95,v95
v_mfma_f32_16x16x32_bf16 v[40:43],a[124:127],v[72:75],v[40:43]
s_waitcnt lgkmcnt(3)
v_rcp_f32 v77,v77
v_mfma_f32_16x16x32_bf16 v[36:39],a[0:3],v[44:47],v[36:39]
s_waitcnt vmcnt(10)
ds_read_b128 v[72:75],v2 offset:39936
v_fma_f32 v79,v94,v79,v93
v_mfma_f32_16x16x32_bf16 v[40:43],a[4:7],v[44:47],v[40:43]
s_barrier
v_mfma_f32_16x16x32_bf16 v[36:39],a[8:11],v[48:51],v[36:39]
s_mov_b64 s[50:51],s[52:53]
ds_read_b128 v[44:47],v2 offset:57344
ds_read_b32 v16,v7 offset:0
v_mfma_f32_16x16x32_bf16 v[40:43],a[12:15],v[48:51],v[40:43]
v_rcp_f32 v78,v78
v_mfma_f32_16x16x32_bf16 v[36:39],a[16:19],v[52:55],v[36:39])"
R"(
ds_read_b128 v[48:51],v2 offset:58368
v_mul_f32 v79,v31,v79
v_mfma_f32_16x16x32_bf16 v[40:43],a[20:23],v[52:55],v[40:43]
v_exp_f32 v79,v79
v_mfma_f32_16x16x32_bf16 v[36:39],a[24:27],v[56:59],v[36:39]
s_waitcnt lgkmcnt(1)
ds_read_b128 v[52:55],v2 offset:59392
v_fma_f32 v79,v79,v95,v95
v_mfma_f32_16x16x32_bf16 v[40:43],a[28:31],v[56:59],v[40:43]
v_cmp_eq_u32 vcc,s60,v16
v_rcp_f32 v79,v79
v_mul_f32 v28,v28,v76
v_mfma_f32_16x16x32_bf16 v[36:39],a[32:35],v[60:63],v[36:39]
s_cselect_b32 s87,vcc_lo,vcc_hi
ds_read_b128 v[56:59],v2 offset:60416
v_mul_f32 v29,v29,v77
v_mfma_f32_16x16x32_bf16 v[40:43],a[36:39],v[60:63],v[40:43]
s_cmp_lt_u32 s86,s89
v_mul_f32 v30,v30,v78
v_mul_f32 v31,v31,v79
v_mfma_f32_16x16x32_bf16 v[36:39],a[40:43],v[64:67],v[36:39]
s_cselect_b32 s87,-1,s87
ds_read_b128 v[60:63],v2 offset:61440
v_cvt_pk_bf16_f32 v84,v28,v29
v_mfma_f32_16x16x32_bf16 v[40:43],a[44:47],v[64:67],v[40:43]
s_cmp_eq_u32 s87,-1
v_cvt_pk_bf16_f32 v85,v30,v31
v_fma_f32 v32,-v92,v24,v32
v_mfma_f32_16x16x32_bf16 v[36:39],a[48:51],v[68:71],v[36:39]
s_cselect_b32 s66,s86,s59
ds_read_b128 v[64:67],v2 offset:62464
v_fma_f32 v33,-v92,v25,v33
v_mfma_f32_16x16x32_bf16 v[40:43],a[52:55],v[68:71],v[40:43]
s_xor_b32 s64,s64,65536
v_fma_f32 v34,-v92,v26,v34
v_fma_f32 v35,-v92,v27,v35
v_mfma_f32_16x16x32_bf16 v[36:39],a[56:59],v[72:75],v[36:39]
s_sub_u32 s69,s66,1048576
ds_read_b128 v[68:71],v2 offset:63488
v_mul_f32 v80,v32,v32
v_mfma_f32_16x16x32_bf16 v[40:43],a[60:63],v[72:75],v[40:43]
s_waitcnt lgkmcnt(3)
v_mul_f32 v81,v33,v33
v_mul_f32 v82,v34,v34
v_mfma_f32_16x16x32_bf16 v[36:39],a[192:195],v[44:47],v[36:39]
s_cmp_lt_u32 s69,3145728
ds_read_b128 v[72:75],v2 offset:64512
v_mul_f32 v83,v35,v35
v_mfma_f32_16x16x32_bf16 v[40:43],a[196:199],v[44:47],v[40:43]
s_cselect_b32 s89,4194304,s89
v_fma_f32 v80,v94,v80,v93
v_mul_f32 v80,v32,v80
v_mfma_f32_16x16x32_bf16 v[104:107],v[100:103],v[44:47],v[104:107]
s_add_u32 s74,s53,s55
v_exp_f32 v80,v80
v_mfma_f32_16x16x32_bf16 v[108:111],v[44:47],v[44:47],v[108:111]
s_cmp_eq_u32 s74,s56
v_fma_f32 v80,v80,v95,v95
v_fma_f32 v81,v94,v81,v93
v_mfma_f32_16x16x32_bf16 v[36:39],a[200:203],v[48:51],v[36:39]
s_cselect_b32 s74,s57,s74
ds_read_b128 v[44:47],v2 offset:49152
v_mul_f32 v81,v33,v81
v_mfma_f32_16x16x32_bf16 v[40:43],a[204:207],v[48:51],v[40:43]
s_cmp_eq_u32 s74,s58
v_exp_f32 v81,v81
v_mfma_f32_16x16x32_bf16 v[104:107],v[100:103],v[48:51],v[104:107]
s_cselect_b32 s74,s59,s74
v_rcp_f32 v80,v80
v_mfma_f32_16x16x32_bf16 v[108:111],v[48:51],v[48:51],v[108:111]
s_mov_b32 s52,s74
v_fma_f32 v81,v81,v95,v95
v_fma_f32 v82,v94,v82,v93
v_mfma_f32_16x16x32_bf16 v[36:39],a[208:211],v[52:55],v[36:39]
ds_read_b128 v[48:51],v2 offset:50176
v_mul_f32 v82,v34,v82
v_mfma_f32_16x16x32_bf16 v[40:43],a[212:215],v[52:55],v[40:43]
v_exp_f32 v82,v82
v_mfma_f32_16x16x32_bf16 v[104:107],v[100:103],v[52:55],v[104:107]
v_rcp_f32 v81,v81
v_mfma_f32_16x16x32_bf16 v[108:111],v[52:55],v[52:55],v[108:111]
v_fma_f32 v82,v82,v95,v95
v_fma_f32 v83,v94,v83,v93
v_mfma_f32_16x16x32_bf16 v[36:39],a[216:219],v[56:59],v[36:39]
ds_read_b128 v[52:55],v2 offset:51200
v_mul_f32 v83,v35,v83
v_mfma_f32_16x16x32_bf16 v[40:43],a[220:223],v[56:59],v[40:43]
v_exp_f32 v83,v83
v_mfma_f32_16x16x32_bf16 v[104:107],v[100:103],v[56:59],v[104:107]
buffer_load_dwordx4 v0,s[36:39],s66 offen offset:0 sc1 lds
v_mfma_f32_16x16x32_bf16 v[108:111],v[56:59],v[56:59],v[108:111]
s_waitcnt lgkmcnt(3)
v_rcp_f32 v82,v82
v_mfma_f32_16x16x32_bf16 v[36:39],a[224:227],v[60:63],v[36:39]
ds_read_b128 v[56:59],v2 offset:52224
v_fma_f32 v83,v83,v95,v95
v_mfma_f32_16x16x32_bf16 v[40:43],a[228:231],v[60:63],v[40:43]
v_rcp_f32 v83,v83
v_mfma_f32_16x16x32_bf16 v[36:39],a[232:235],v[64:67],v[36:39]
buffer_load_dwordx4 v0,s[36:39],s66 offen offset:1024 sc1 lds
v_cndmask_b32 v112,v108,v109,s[82:83]
v_cndmask_b32 v113,v110,v111,s[82:83]
v_cndmask_b32 v112,v112,v113,s[84:85]
v_cndmask_b32 v112,v104,v112,s[98:99]
v_mfma_f32_16x16x32_bf16 v[40:43],a[236:239],v[64:67],v[40:43]
ds_read_b128 v[60:63],v2 offset:53248
ds_write_b32 v13,v112 offset:2048
v_mfma_f32_16x16x32_bf16 v[36:39],a[240:243],v[68:71],v[36:39]
ds_read_b128 v[64:67],v2 offset:54272
v_mul_f32 v32,v32,v80
v_mfma_f32_16x16x32_bf16 v[40:43],a[244:247],v[68:71],v[40:43]
v_mul_f32 v33,v33,v81
v_mul_f32 v34,v34,v82
v_mfma_f32_16x16x32_bf16 v[36:39],a[248:251],v[72:75],v[36:39]
buffer_load_dwordx4 v0,s[36:39],s66 offen offset:2048 sc1 lds
v_mfma_f32_16x16x32_bf16 v[40:43],a[252:255],v[72:75],v[40:43]
s_waitcnt lgkmcnt(3)
ds_read_b128 v[68:71],v2 offset:55296
v_mul_f32 v35,v35,v83
v_mfma_f32_16x16x32_bf16 v[36:39],a[128:131],v[44:47],v[36:39]
s_waitcnt vmcnt(9) lgkmcnt(2)
ds_read_b128 v[72:75],v2 offset:56320
v_xor_b32 v2,65536,v2
v_mfma_f32_16x16x32_bf16 v[40:43],a[132:135],v[44:47],v[40:43]
s_barrier
v_mfma_f32_16x16x32_bf16 v[36:39],a[136:139],v[48:51],v[36:39]
ds_read_b128 v[116:119],v14 offset:2048
ds_read_b128 v[120:123],v14 offset:2064
v_mfma_f32_16x16x32_bf16 v[40:43],a[140:143],v[48:51],v[40:43]
ds_read_b128 v[44:47],v2 offset:8192
v_cvt_pk_bf16_f32 v86,v32,v33
v_mfma_f32_16x16x32_bf16 v[36:39],a[144:147],v[52:55],v[36:39]
buffer_load_dwordx4 v0,s[36:39],s66 offen offset:3072 sc1 lds
v_mfma_f32_16x16x32_bf16 v[40:43],a[148:151],v[52:55],v[40:43]
s_add_u32 m0,s62,16384
ds_read_b128 v[48:51],v2 offset:9216
v_cvt_pk_bf16_f32 v87,v34,v35
v_mfma_f32_16x16x32_bf16 v[36:39],a[152:155],v[56:59],v[36:39]
s_waitcnt lgkmcnt(2)
ds_read_b128 v[52:55],v2 offset:10240
v_xor_b32 v8,16384,v8
v_mfma_f32_16x16x32_bf16 v[40:43],a[156:159],v[56:59],v[40:43]
v_add_f32 v116,v116,v117
v_add_f32 v118,v118,v119
v_mfma_f32_16x16x32_bf16 v[36:39],a[160:163],v[60:63],v[36:39]
buffer_load_dwordx4 v1,s[36:39],s66 offen offset:0 sc1 lds
v_mfma_f32_16x16x32_bf16 v[40:43],a[164:167],v[60:63],v[40:43]
s_cmp_lt_u32 s67,4194304
ds_read_b128 v[56:59],v2 offset:11264
v_add_f32 v116,v116,v118
v_mfma_f32_16x16x32_bf16 v[36:39],a[168:171],v[64:67],v[36:39]
ds_read_b128 v[60:63],v2 offset:12288
v_mul_f32 v96,s75,v116
v_mfma_f32_16x16x32_bf16 v[40:43],a[172:175],v[64:67],v[40:43]
v_add_f32 v120,v120,v121
v_add_f32 v122,v122,v123
v_mfma_f32_16x16x32_bf16 v[36:39],a[176:179],v[68:71],v[36:39]
v_add_f32 v120,v120,v122
v_mul_f32 v120,s75,v120
v_fma_f32 v120,-v96,v96,v120
v_max_f32 v120,0,v120
v_add_f32 v115,v15,v120
v_fma_f32 v120,v15,v115,v120
v_rsq_f32 v115,v115
v_mfma_f32_16x16x32_bf16 v[40:43],a[180:183],v[68:71],v[40:43]
ds_read_b128 v[64:67],v2 offset:13312
buffer_store_dwordx4 v[84:87],v3,s[36:39],s73 offen offset:0 sc0 sc1
v_rsq_f32 v116,v120
v_mul_f32_e64 v114,-v96,v115
v_mfma_f32_16x16x32_bf16 v[36:39],a[184:187],v[72:75],v[36:39]
s_waitcnt lgkmcnt(2)
ds_read_b128 v[68:71],v2 offset:14336
buffer_store_dwordx2 v[114:115],v12,s[36:39],s65 offen offset:0 sc0 sc1
v_mul_f32 v97,s76,v116
v_mul_f32 v98,v116,v116
v_mul_f32 v99,v120,v116
v_mul_f32 v98,s77,v98
v_mul_f32 v98,v98,v97
v_mfma_f32_16x16x32_bf16 v[40:43],a[188:191],v[72:75],v[40:43]
s_cbranch_scc1 Lg1_a0w2_%=
s_cmp_ge_u32 s48,4194304
s_cselect_b64 s[90:91],s[94:95],s[90:91]
s_cselect_b64 s[92:93],s[96:97],s[92:93]
s_cmp_lt_u32 s67,s59
s_cbranch_scc1 Lg1_d0w2_%=
s_branch Lg1_slow0_%=
Lg1_a0w3_%=:
v_mfma_f32_16x16x32_bf16 v[28:31],a[96:99],v[44:47],0
s_add_u32 m0,s62,16384
ds_read_b128 v[72:75],v2 offset:11264
s_lshr_b32 s72,s66,6
v_mfma_f32_16x16x32_bf16 v[32:35],a[100:103],v[44:47],0
s_lshr_b32 s71,s62,2
s_cmp_lt_u32 s66,4194304
s_cselect_b32 s72,s59,s72
v_mfma_f32_16x16x32_bf16 v[104:107],v[100:103],v[44:47],0
v_mfma_f32_16x16x32_bf16 v[108:111],v[44:47],v[44:47],0
v_fma_f32 v36,-v96,v20,v36
v_fma_f32 v37,-v96,v21,v37
v_mfma_f32_16x16x32_bf16 v[28:31],a[104:107],v[48:51],v[28:31])"
R"(
buffer_load_dwordx4 v1,s[36:39],s66 offen offset:1024 sc1 lds
v_mfma_f32_16x16x32_bf16 v[32:35],a[108:111],v[48:51],v[32:35]
ds_read_b128 v[44:47],v2 offset:4096
v_fma_f32 v38,-v96,v22,v38
v_mfma_f32_16x16x32_bf16 v[104:107],v[100:103],v[48:51],v[104:107]
v_fma_f32 v39,-v96,v23,v39
v_mul_f32 v76,v36,v36
v_mfma_f32_16x16x32_bf16 v[108:111],v[48:51],v[48:51],v[108:111]
v_mul_f32 v77,v37,v37
v_mul_f32 v78,v38,v38
v_mfma_f32_16x16x32_bf16 v[28:31],a[112:115],v[52:55],v[28:31]
buffer_load_dwordx4 v1,s[36:39],s66 offen offset:2048 sc1 lds
v_mfma_f32_16x16x32_bf16 v[32:35],a[116:119],v[52:55],v[32:35]
ds_read_b128 v[48:51],v2 offset:5120
v_mul_f32 v79,v39,v39
v_mfma_f32_16x16x32_bf16 v[104:107],v[100:103],v[52:55],v[104:107]
v_fma_f32 v76,v98,v76,v97
v_mul_f32 v76,v36,v76
v_mfma_f32_16x16x32_bf16 v[108:111],v[52:55],v[52:55],v[108:111]
v_exp_f32 v76,v76
v_mfma_f32_16x16x32_bf16 v[28:31],a[120:123],v[56:59],v[28:31]
buffer_load_dwordx4 v1,s[36:39],s66 offen offset:3072 sc1 lds
v_mfma_f32_16x16x32_bf16 v[32:35],a[124:127],v[56:59],v[32:35]
s_add_u32 m0,s71,s88
ds_read_b128 v[52:55],v2 offset:6144
v_fma_f32 v76,v76,v99,v99
v_mfma_f32_16x16x32_bf16 v[104:107],v[100:103],v[56:59],v[104:107]
buffer_load_dword v10,s[36:39],s72 offen offset:0 sc1 lds
v_mfma_f32_16x16x32_bf16 v[108:111],v[56:59],v[56:59],v[108:111]
s_waitcnt lgkmcnt(3)
v_rcp_f32 v76,v76
v_mfma_f32_16x16x32_bf16 v[28:31],a[64:67],v[60:63],v[28:31]
s_lshr_b32 s73,s47,0
ds_read_b128 v[56:59],v2 offset:7168
v_fma_f32 v77,v98,v77,v97
v_mfma_f32_16x16x32_bf16 v[32:35],a[68:71],v[60:63],v[32:35]
s_lshr_b32 s70,s45,5
v_mul_f32 v77,v37,v77
v_fma_f32 v78,v98,v78,v97
v_mfma_f32_16x16x32_bf16 v[28:31],a[72:75],v[64:67],v[28:31]
s_xor_b32 s69,s64,65536
ds_read_b128 v[60:63],v2 offset:0
v_mul_f32 v78,v38,v78
v_mfma_f32_16x16x32_bf16 v[32:35],a[76:79],v[64:67],v[32:35]
s_cmp_ge_u32 s66,s59
v_exp_f32 v77,v77
v_mfma_f32_16x16x32_bf16 v[28:31],a[80:83],v[68:71],v[28:31]
s_cselect_b32 s62,s69,s63
ds_read_b128 v[64:67],v2 offset:1024
v_fma_f32 v77,v77,v99,v99
v_mfma_f32_16x16x32_bf16 v[32:35],a[84:87],v[68:71],v[32:35]
s_cselect_b32 s86,s49,s50
v_exp_f32 v78,v78
v_mfma_f32_16x16x32_bf16 v[28:31],a[88:91],v[72:75],v[28:31]
s_mov_b32 m0,s62
ds_read_b128 v[68:71],v2 offset:2048
v_fma_f32 v78,v78,v99,v99
v_mfma_f32_16x16x32_bf16 v[32:35],a[92:95],v[72:75],v[32:35]
s_waitcnt lgkmcnt(3)
v_rcp_f32 v77,v77
v_mfma_f32_16x16x32_bf16 v[28:31],a[32:35],v[44:47],v[28:31]
s_waitcnt vmcnt(11)
ds_read_b128 v[72:75],v2 offset:3072
v_fma_f32 v79,v98,v79,v97
v_mfma_f32_16x16x32_bf16 v[32:35],a[36:39],v[44:47],v[32:35]
s_barrier
v_mfma_f32_16x16x32_bf16 v[28:31],a[40:43],v[48:51],v[28:31]
buffer_store_dword v5,v4,s[36:39],s70 offen offset:0 sc0 sc1
ds_read_b128 v[44:47],v2 offset:28672
ds_read_b32 v16,v7 offset:256
v_mul_f32 v79,v39,v79
v_mfma_f32_16x16x32_bf16 v[32:35],a[44:47],v[48:51],v[32:35]
v_exp_f32 v79,v79
v_mfma_f32_16x16x32_bf16 v[28:31],a[48:51],v[52:55],v[28:31]
ds_read_b128 v[48:51],v2 offset:29696
v_fma_f32 v79,v79,v99,v99
v_mfma_f32_16x16x32_bf16 v[32:35],a[52:55],v[52:55],v[32:35]
v_rcp_f32 v78,v78
v_mfma_f32_16x16x32_bf16 v[28:31],a[56:59],v[56:59],v[28:31]
s_waitcnt lgkmcnt(1)
ds_read_b128 v[52:55],v2 offset:30720
v_mul_f32 v36,v36,v76
v_mfma_f32_16x16x32_bf16 v[32:35],a[60:63],v[56:59],v[32:35]
v_cmp_eq_u32 vcc,s60,v16
v_rcp_f32 v79,v79
v_mul_f32 v37,v37,v77
v_mfma_f32_16x16x32_bf16 v[28:31],a[0:3],v[60:63],v[28:31]
s_cselect_b32 s87,vcc_lo,vcc_hi
ds_read_b128 v[56:59],v2 offset:31744
v_mul_f32 v38,v38,v78
v_mfma_f32_16x16x32_bf16 v[32:35],a[4:7],v[60:63],v[32:35]
s_cmp_lt_u32 s86,s89
v_mul_f32 v39,v39,v79
v_cvt_pk_bf16_f32 v84,v36,v37
v_mfma_f32_16x16x32_bf16 v[28:31],a[8:11],v[64:67],v[28:31]
s_cselect_b32 s87,-1,s87
ds_read_b128 v[60:63],v2 offset:24576
v_cvt_pk_bf16_f32 v85,v38,v39
v_mfma_f32_16x16x32_bf16 v[32:35],a[12:15],v[64:67],v[32:35]
s_cmp_eq_u32 s87,-1
v_fma_f32 v40,-v96,v24,v40
v_fma_f32 v41,-v96,v25,v41
v_mfma_f32_16x16x32_bf16 v[28:31],a[16:19],v[68:71],v[28:31]
s_cselect_b32 s67,s86,s59
ds_read_b128 v[64:67],v2 offset:25600
v_fma_f32 v42,-v96,v26,v42
v_mfma_f32_16x16x32_bf16 v[32:35],a[20:23],v[68:71],v[32:35]
s_xor_b32 s63,s63,65536
v_fma_f32 v43,-v96,v27,v43
v_mul_f32 v80,v40,v40
v_mfma_f32_16x16x32_bf16 v[28:31],a[24:27],v[72:75],v[28:31]
s_sub_u32 s69,s67,1048576
ds_read_b128 v[68:71],v2 offset:26624
v_mul_f32 v81,v41,v41
v_mfma_f32_16x16x32_bf16 v[32:35],a[28:31],v[72:75],v[32:35]
s_waitcnt lgkmcnt(3)
v_mul_f32 v82,v42,v42
v_mul_f32 v83,v43,v43
v_mfma_f32_16x16x32_bf16 v[28:31],a[224:227],v[44:47],v[28:31]
s_cmp_lt_u32 s69,3145728
ds_read_b128 v[72:75],v2 offset:27648
v_fma_f32 v80,v98,v80,v97
v_mfma_f32_16x16x32_bf16 v[32:35],a[228:231],v[44:47],v[32:35]
s_cselect_b32 s89,4194304,s89
v_mul_f32 v80,v40,v80
v_fma_f32 v81,v98,v81,v97
v_mfma_f32_16x16x32_bf16 v[104:107],v[100:103],v[44:47],v[104:107]
s_add_u32 s53,s52,s55
v_exp_f32 v80,v80
v_mfma_f32_16x16x32_bf16 v[108:111],v[44:47],v[44:47],v[108:111]
s_cmp_eq_u32 s53,s56
v_mul_f32 v81,v41,v81
v_fma_f32 v80,v80,v99,v99
v_mfma_f32_16x16x32_bf16 v[28:31],a[232:235],v[48:51],v[28:31]
s_cselect_b32 s53,s57,s53
ds_read_b128 v[44:47],v2 offset:20480
v_fma_f32 v82,v98,v82,v97
v_mfma_f32_16x16x32_bf16 v[32:35],a[236:239],v[48:51],v[32:35]
s_cmp_eq_u32 s53,s58
v_exp_f32 v81,v81
v_mfma_f32_16x16x32_bf16 v[104:107],v[100:103],v[48:51],v[104:107]
s_cselect_b32 s53,s59,s53
v_rcp_f32 v80,v80
v_mfma_f32_16x16x32_bf16 v[108:111],v[48:51],v[48:51],v[108:111]
v_mul_f32 v82,v42,v82
v_fma_f32 v81,v81,v99,v99
v_mfma_f32_16x16x32_bf16 v[28:31],a[240:243],v[52:55],v[28:31]
ds_read_b128 v[48:51],v2 offset:21504
v_fma_f32 v83,v98,v83,v97
v_mfma_f32_16x16x32_bf16 v[32:35],a[244:247],v[52:55],v[32:35]
v_exp_f32 v82,v82
v_mfma_f32_16x16x32_bf16 v[104:107],v[100:103],v[52:55],v[104:107]
v_rcp_f32 v81,v81
v_mfma_f32_16x16x32_bf16 v[108:111],v[52:55],v[52:55],v[108:111]
v_mul_f32 v83,v43,v83
v_fma_f32 v82,v82,v99,v99
v_mfma_f32_16x16x32_bf16 v[28:31],a[248:251],v[56:59],v[28:31]
ds_read_b128 v[52:55],v2 offset:22528
v_mul_f32 v40,v40,v80
v_mfma_f32_16x16x32_bf16 v[32:35],a[252:255],v[56:59],v[32:35]
v_exp_f32 v83,v83
v_mfma_f32_16x16x32_bf16 v[104:107],v[100:103],v[56:59],v[104:107]
v_rcp_f32 v82,v82
v_mfma_f32_16x16x32_bf16 v[108:111],v[56:59],v[56:59],v[108:111]
s_waitcnt lgkmcnt(3)
buffer_load_dwordx4 v0,s[36:39],s67 offen offset:0 sc1 lds
v_mfma_f32_16x16x32_bf16 v[28:31],a[192:195],v[60:63],v[28:31]
ds_read_b128 v[56:59],v2 offset:23552
v_fma_f32 v83,v83,v99,v99
v_mfma_f32_16x16x32_bf16 v[32:35],a[196:199],v[60:63],v[32:35]
v_rcp_f32 v83,v83
v_mfma_f32_16x16x32_bf16 v[28:31],a[200:203],v[64:67],v[28:31]
v_cndmask_b32 v112,v108,v109,s[82:83]
v_cndmask_b32 v113,v110,v111,s[82:83]
v_cndmask_b32 v112,v112,v113,s[84:85]
v_cndmask_b32 v112,v104,v112,s[98:99]
v_mfma_f32_16x16x32_bf16 v[32:35],a[204:207],v[64:67],v[32:35]
ds_read_b128 v[60:63],v2 offset:16384
ds_write_b32 v13,v112 offset:0
buffer_load_dwordx4 v0,s[36:39],s67 offen offset:1024 sc1 lds
v_mfma_f32_16x16x32_bf16 v[28:31],a[208:211],v[68:71],v[28:31]
ds_read_b128 v[64:67],v2 offset:17408
v_mul_f32 v41,v41,v81
v_mfma_f32_16x16x32_bf16 v[32:35],a[212:215],v[68:71],v[32:35]
v_mul_f32 v42,v42,v82
v_mul_f32 v43,v43,v83
v_mfma_f32_16x16x32_bf16 v[28:31],a[216:219],v[72:75],v[28:31]
ds_read_b128 v[68:71],v2 offset:18432
v_cvt_pk_bf16_f32 v86,v40,v41
v_mfma_f32_16x16x32_bf16 v[32:35],a[220:223],v[72:75],v[32:35]
s_waitcnt lgkmcnt(4)
buffer_load_dwordx4 v0,s[36:39],s67 offen offset:2048 sc1 lds
v_mfma_f32_16x16x32_bf16 v[28:31],a[160:163],v[44:47],v[28:31]
s_waitcnt vmcnt(10) lgkmcnt(2)
ds_read_b128 v[72:75],v2 offset:19456
v_cvt_pk_bf16_f32 v87,v42,v43
v_mfma_f32_16x16x32_bf16 v[32:35],a[164:167],v[44:47],v[32:35]
s_barrier
v_mfma_f32_16x16x32_bf16 v[28:31],a[168:171],v[48:51],v[28:31]
ds_read_b128 v[116:119],v14 offset:0)"
R"(
ds_read_b128 v[120:123],v14 offset:16
v_mfma_f32_16x16x32_bf16 v[32:35],a[172:175],v[48:51],v[32:35]
ds_read_b128 v[44:47],v2 offset:45056
v_mfma_f32_16x16x32_bf16 v[28:31],a[176:179],v[52:55],v[28:31]
ds_read_b128 v[48:51],v2 offset:46080
v_mfma_f32_16x16x32_bf16 v[32:35],a[180:183],v[52:55],v[32:35]
buffer_load_dwordx4 v0,s[36:39],s67 offen offset:3072 sc1 lds
v_mfma_f32_16x16x32_bf16 v[28:31],a[184:187],v[56:59],v[28:31]
s_waitcnt lgkmcnt(2)
ds_read_b128 v[52:55],v2 offset:47104
v_add_f32 v116,v116,v117
v_mfma_f32_16x16x32_bf16 v[32:35],a[188:191],v[56:59],v[32:35]
v_add_f32 v118,v118,v119
v_add_f32 v116,v116,v118
v_mfma_f32_16x16x32_bf16 v[28:31],a[128:131],v[60:63],v[28:31]
s_add_u32 m0,s62,16384
ds_read_b128 v[56:59],v2 offset:48128
v_mul_f32 v92,s75,v116
v_mfma_f32_16x16x32_bf16 v[32:35],a[132:135],v[60:63],v[32:35]
buffer_load_dwordx4 v1,s[36:39],s67 offen offset:0 sc1 lds
v_mfma_f32_16x16x32_bf16 v[28:31],a[136:139],v[64:67],v[28:31]
s_cmp_ge_u32 s66,s59
ds_read_b128 v[60:63],v2 offset:40960
v_add_f32 v120,v120,v121
v_mfma_f32_16x16x32_bf16 v[32:35],a[140:143],v[64:67],v[32:35]
v_add_f32 v122,v122,v123
v_add_f32 v120,v120,v122
v_mfma_f32_16x16x32_bf16 v[28:31],a[144:147],v[68:71],v[28:31]
ds_read_b128 v[64:67],v2 offset:41984
v_mul_f32 v120,s75,v120
v_mfma_f32_16x16x32_bf16 v[32:35],a[148:151],v[68:71],v[32:35]
v_fma_f32 v120,-v92,v92,v120
v_max_f32 v120,0,v120
v_add_f32 v115,v15,v120
v_fma_f32 v120,v15,v115,v120
v_rsq_f32 v116,v120
v_mfma_f32_16x16x32_bf16 v[28:31],a[152:155],v[72:75],v[28:31]
s_waitcnt lgkmcnt(2)
ds_read_b128 v[68:71],v2 offset:43008
buffer_store_dwordx4 v[84:87],v3,s[36:39],s73 offen offset:0 sc0 sc1
v_mul_f32 v93,s76,v116
v_mul_f32 v94,v116,v116
v_mul_f32 v95,v120,v116
v_mul_f32 v94,s77,v94
v_mul_f32 v94,v94,v93
v_mfma_f32_16x16x32_bf16 v[32:35],a[156:159],v[72:75],v[32:35]
s_cbranch_scc1 Lg1_slow1_%=
Lg1_a1w3_%=:
v_mfma_f32_16x16x32_bf16 v[36:39],a[96:99],v[44:47],0
s_add_u32 m0,s62,16384
ds_read_b128 v[72:75],v2 offset:44032
s_lshr_b32 s72,s67,6
v_mfma_f32_16x16x32_bf16 v[40:43],a[100:103],v[44:47],0
s_lshr_b32 s71,s62,2
s_cmp_lt_u32 s67,4194304
s_cselect_b32 s72,s59,s72
v_mfma_f32_16x16x32_bf16 v[104:107],v[100:103],v[44:47],0
s_mov_b64 s[44:45],s[46:47]
v_mfma_f32_16x16x32_bf16 v[108:111],v[44:47],v[44:47],0
v_fma_f32 v28,-v92,v20,v28
v_fma_f32 v29,-v92,v21,v29
v_mfma_f32_16x16x32_bf16 v[36:39],a[104:107],v[48:51],v[36:39]
buffer_load_dwordx4 v1,s[36:39],s67 offen offset:1024 sc1 lds
v_mfma_f32_16x16x32_bf16 v[40:43],a[108:111],v[48:51],v[40:43]
ds_read_b128 v[44:47],v2 offset:36864
v_fma_f32 v30,-v92,v22,v30
v_mfma_f32_16x16x32_bf16 v[104:107],v[100:103],v[48:51],v[104:107]
v_fma_f32 v31,-v92,v23,v31
v_mul_f32 v76,v28,v28
v_mfma_f32_16x16x32_bf16 v[108:111],v[48:51],v[48:51],v[108:111]
v_mul_f32 v77,v29,v29
v_mul_f32 v78,v30,v30
v_mfma_f32_16x16x32_bf16 v[36:39],a[112:115],v[52:55],v[36:39]
buffer_load_dwordx4 v1,s[36:39],s67 offen offset:2048 sc1 lds
v_mfma_f32_16x16x32_bf16 v[40:43],a[116:119],v[52:55],v[40:43]
ds_read_b128 v[48:51],v2 offset:37888
v_mul_f32 v79,v31,v31
v_mfma_f32_16x16x32_bf16 v[104:107],v[100:103],v[52:55],v[104:107]
v_fma_f32 v76,v94,v76,v93
v_mul_f32 v76,v28,v76
v_mfma_f32_16x16x32_bf16 v[108:111],v[52:55],v[52:55],v[108:111]
v_exp_f32 v76,v76
v_mfma_f32_16x16x32_bf16 v[36:39],a[120:123],v[56:59],v[36:39]
buffer_load_dwordx4 v1,s[36:39],s67 offen offset:3072 sc1 lds
v_mfma_f32_16x16x32_bf16 v[40:43],a[124:127],v[56:59],v[40:43]
s_add_u32 m0,s71,s88
ds_read_b128 v[52:55],v2 offset:38912
v_fma_f32 v76,v76,v95,v95
v_mfma_f32_16x16x32_bf16 v[104:107],v[100:103],v[56:59],v[104:107]
buffer_load_dword v10,s[36:39],s72 offen offset:0 sc1 lds
v_mfma_f32_16x16x32_bf16 v[108:111],v[56:59],v[56:59],v[108:111]
s_waitcnt lgkmcnt(3)
v_rcp_f32 v76,v76
v_mfma_f32_16x16x32_bf16 v[36:39],a[64:67],v[60:63],v[36:39]
s_lshr_b32 s73,s48,0
ds_read_b128 v[56:59],v2 offset:39936
v_fma_f32 v77,v94,v77,v93
v_mfma_f32_16x16x32_bf16 v[40:43],a[68:71],v[60:63],v[40:43]
s_lshr_b32 s70,s46,5
v_mul_f32 v77,v29,v77
v_fma_f32 v78,v94,v78,v93
v_mfma_f32_16x16x32_bf16 v[36:39],a[72:75],v[64:67],v[36:39]
s_xor_b32 s69,s63,65536
ds_read_b128 v[60:63],v2 offset:32768
v_mul_f32 v78,v30,v78
v_mfma_f32_16x16x32_bf16 v[40:43],a[76:79],v[64:67],v[40:43]
s_cmp_ge_u32 s67,s59
v_exp_f32 v77,v77
v_mfma_f32_16x16x32_bf16 v[36:39],a[80:83],v[68:71],v[36:39]
s_cselect_b32 s62,s69,s64
ds_read_b128 v[64:67],v2 offset:33792
v_fma_f32 v77,v77,v95,v95
v_mfma_f32_16x16x32_bf16 v[40:43],a[84:87],v[68:71],v[40:43]
s_cselect_b32 s86,s50,s51
v_exp_f32 v78,v78
v_mfma_f32_16x16x32_bf16 v[36:39],a[88:91],v[72:75],v[36:39]
s_mov_b32 m0,s62
ds_read_b128 v[68:71],v2 offset:34816
v_fma_f32 v78,v78,v95,v95
v_mfma_f32_16x16x32_bf16 v[40:43],a[92:95],v[72:75],v[40:43]
s_waitcnt lgkmcnt(3)
v_rcp_f32 v77,v77
v_mfma_f32_16x16x32_bf16 v[36:39],a[32:35],v[44:47],v[36:39]
s_waitcnt vmcnt(11)
ds_read_b128 v[72:75],v2 offset:35840
v_fma_f32 v79,v94,v79,v93
v_mfma_f32_16x16x32_bf16 v[40:43],a[36:39],v[44:47],v[40:43]
s_barrier
v_mfma_f32_16x16x32_bf16 v[36:39],a[40:43],v[48:51],v[36:39]
buffer_store_dword v5,v4,s[36:39],s70 offen offset:0 sc0 sc1
ds_read_b128 v[44:47],v2 offset:61440
ds_read_b32 v16,v7 offset:0
v_mul_f32 v79,v31,v79
v_mfma_f32_16x16x32_bf16 v[40:43],a[44:47],v[48:51],v[40:43]
s_mov_b64 s[46:47],s[48:49]
v_exp_f32 v79,v79
v_mfma_f32_16x16x32_bf16 v[36:39],a[48:51],v[52:55],v[36:39]
s_mov_b64 s[48:49],s[50:51]
ds_read_b128 v[48:51],v2 offset:62464
v_fma_f32 v79,v79,v95,v95
v_mfma_f32_16x16x32_bf16 v[40:43],a[52:55],v[52:55],v[40:43]
s_mov_b64 s[50:51],s[52:53]
v_rcp_f32 v78,v78
v_mfma_f32_16x16x32_bf16 v[36:39],a[56:59],v[56:59],v[36:39]
s_waitcnt lgkmcnt(1)
ds_read_b128 v[52:55],v2 offset:63488
v_mul_f32 v28,v28,v76
v_mfma_f32_16x16x32_bf16 v[40:43],a[60:63],v[56:59],v[40:43]
v_cmp_eq_u32 vcc,s60,v16
v_rcp_f32 v79,v79
v_mul_f32 v29,v29,v77
v_mfma_f32_16x16x32_bf16 v[36:39],a[0:3],v[60:63],v[36:39]
s_cselect_b32 s87,vcc_lo,vcc_hi
ds_read_b128 v[56:59],v2 offset:64512
v_mul_f32 v30,v30,v78
v_mfma_f32_16x16x32_bf16 v[40:43],a[4:7],v[60:63],v[40:43]
s_cmp_lt_u32 s86,s89
v_mul_f32 v31,v31,v79
v_cvt_pk_bf16_f32 v84,v28,v29
v_mfma_f32_16x16x32_bf16 v[36:39],a[8:11],v[64:67],v[36:39]
s_cselect_b32 s87,-1,s87
ds_read_b128 v[60:63],v2 offset:57344
v_cvt_pk_bf16_f32 v85,v30,v31
v_mfma_f32_16x16x32_bf16 v[40:43],a[12:15],v[64:67],v[40:43]
s_cmp_eq_u32 s87,-1
v_fma_f32 v32,-v92,v24,v32
v_fma_f32 v33,-v92,v25,v33
v_mfma_f32_16x16x32_bf16 v[36:39],a[16:19],v[68:71],v[36:39]
s_cselect_b32 s66,s86,s59
ds_read_b128 v[64:67],v2 offset:58368
v_fma_f32 v34,-v92,v26,v34
v_mfma_f32_16x16x32_bf16 v[40:43],a[20:23],v[68:71],v[40:43]
s_xor_b32 s64,s64,65536
v_fma_f32 v35,-v92,v27,v35
v_mul_f32 v80,v32,v32
v_mfma_f32_16x16x32_bf16 v[36:39],a[24:27],v[72:75],v[36:39]
s_sub_u32 s69,s66,1048576
ds_read_b128 v[68:71],v2 offset:59392
v_mul_f32 v81,v33,v33
v_mfma_f32_16x16x32_bf16 v[40:43],a[28:31],v[72:75],v[40:43]
s_waitcnt lgkmcnt(3)
v_mul_f32 v82,v34,v34
v_mul_f32 v83,v35,v35
v_mfma_f32_16x16x32_bf16 v[36:39],a[224:227],v[44:47],v[36:39]
s_cmp_lt_u32 s69,3145728
ds_read_b128 v[72:75],v2 offset:60416
v_fma_f32 v80,v94,v80,v93
v_mfma_f32_16x16x32_bf16 v[40:43],a[228:231],v[44:47],v[40:43]
s_cselect_b32 s89,4194304,s89
v_mul_f32 v80,v32,v80
v_fma_f32 v81,v94,v81,v93
v_mfma_f32_16x16x32_bf16 v[104:107],v[100:103],v[44:47],v[104:107]
s_add_u32 s74,s53,s55
v_exp_f32 v80,v80
v_mfma_f32_16x16x32_bf16 v[108:111],v[44:47],v[44:47],v[108:111]
s_cmp_eq_u32 s74,s56
v_mul_f32 v81,v33,v81
v_fma_f32 v80,v80,v95,v95
v_mfma_f32_16x16x32_bf16 v[36:39],a[232:235],v[48:51],v[36:39]
s_cselect_b32 s74,s57,s74)"
R"(
ds_read_b128 v[44:47],v2 offset:53248
v_fma_f32 v82,v94,v82,v93
v_mfma_f32_16x16x32_bf16 v[40:43],a[236:239],v[48:51],v[40:43]
s_cmp_eq_u32 s74,s58
v_exp_f32 v81,v81
v_mfma_f32_16x16x32_bf16 v[104:107],v[100:103],v[48:51],v[104:107]
s_cselect_b32 s74,s59,s74
v_rcp_f32 v80,v80
v_mfma_f32_16x16x32_bf16 v[108:111],v[48:51],v[48:51],v[108:111]
s_mov_b32 s52,s74
v_mul_f32 v82,v34,v82
v_fma_f32 v81,v81,v95,v95
v_mfma_f32_16x16x32_bf16 v[36:39],a[240:243],v[52:55],v[36:39]
ds_read_b128 v[48:51],v2 offset:54272
v_fma_f32 v83,v94,v83,v93
v_mfma_f32_16x16x32_bf16 v[40:43],a[244:247],v[52:55],v[40:43]
v_exp_f32 v82,v82
v_mfma_f32_16x16x32_bf16 v[104:107],v[100:103],v[52:55],v[104:107]
v_rcp_f32 v81,v81
v_mfma_f32_16x16x32_bf16 v[108:111],v[52:55],v[52:55],v[108:111]
v_mul_f32 v83,v35,v83
v_fma_f32 v82,v82,v95,v95
v_mfma_f32_16x16x32_bf16 v[36:39],a[248:251],v[56:59],v[36:39]
ds_read_b128 v[52:55],v2 offset:55296
v_mul_f32 v32,v32,v80
v_mfma_f32_16x16x32_bf16 v[40:43],a[252:255],v[56:59],v[40:43]
v_exp_f32 v83,v83
v_mfma_f32_16x16x32_bf16 v[104:107],v[100:103],v[56:59],v[104:107]
v_rcp_f32 v82,v82
v_mfma_f32_16x16x32_bf16 v[108:111],v[56:59],v[56:59],v[108:111]
s_waitcnt lgkmcnt(3)
buffer_load_dwordx4 v0,s[36:39],s66 offen offset:0 sc1 lds
v_mfma_f32_16x16x32_bf16 v[36:39],a[192:195],v[60:63],v[36:39]
ds_read_b128 v[56:59],v2 offset:56320
v_fma_f32 v83,v83,v95,v95
v_mfma_f32_16x16x32_bf16 v[40:43],a[196:199],v[60:63],v[40:43]
v_rcp_f32 v83,v83
v_mfma_f32_16x16x32_bf16 v[36:39],a[200:203],v[64:67],v[36:39]
v_cndmask_b32 v112,v108,v109,s[82:83]
v_cndmask_b32 v113,v110,v111,s[82:83]
v_cndmask_b32 v112,v112,v113,s[84:85]
v_cndmask_b32 v112,v104,v112,s[98:99]
v_mfma_f32_16x16x32_bf16 v[40:43],a[204:207],v[64:67],v[40:43]
ds_read_b128 v[60:63],v2 offset:49152
ds_write_b32 v13,v112 offset:2048
buffer_load_dwordx4 v0,s[36:39],s66 offen offset:1024 sc1 lds
v_mfma_f32_16x16x32_bf16 v[36:39],a[208:211],v[68:71],v[36:39]
ds_read_b128 v[64:67],v2 offset:50176
v_mul_f32 v33,v33,v81
v_mfma_f32_16x16x32_bf16 v[40:43],a[212:215],v[68:71],v[40:43]
v_mul_f32 v34,v34,v82
v_mul_f32 v35,v35,v83
v_mfma_f32_16x16x32_bf16 v[36:39],a[216:219],v[72:75],v[36:39]
ds_read_b128 v[68:71],v2 offset:51200
v_cvt_pk_bf16_f32 v86,v32,v33
v_mfma_f32_16x16x32_bf16 v[40:43],a[220:223],v[72:75],v[40:43]
s_waitcnt lgkmcnt(4)
buffer_load_dwordx4 v0,s[36:39],s66 offen offset:2048 sc1 lds
v_mfma_f32_16x16x32_bf16 v[36:39],a[160:163],v[44:47],v[36:39]
s_waitcnt vmcnt(10) lgkmcnt(2)
ds_read_b128 v[72:75],v2 offset:52224
v_xor_b32 v2,65536,v2
v_mfma_f32_16x16x32_bf16 v[40:43],a[164:167],v[44:47],v[40:43]
s_barrier
v_mfma_f32_16x16x32_bf16 v[36:39],a[168:171],v[48:51],v[36:39]
ds_read_b128 v[116:119],v14 offset:2048
ds_read_b128 v[120:123],v14 offset:2064
v_mfma_f32_16x16x32_bf16 v[40:43],a[172:175],v[48:51],v[40:43]
ds_read_b128 v[44:47],v2 offset:12288
v_cvt_pk_bf16_f32 v87,v34,v35
v_mfma_f32_16x16x32_bf16 v[36:39],a[176:179],v[52:55],v[36:39]
ds_read_b128 v[48:51],v2 offset:13312
v_xor_b32 v8,16384,v8
v_mfma_f32_16x16x32_bf16 v[40:43],a[180:183],v[52:55],v[40:43]
buffer_load_dwordx4 v0,s[36:39],s66 offen offset:3072 sc1 lds
v_mfma_f32_16x16x32_bf16 v[36:39],a[184:187],v[56:59],v[36:39]
s_waitcnt lgkmcnt(2)
ds_read_b128 v[52:55],v2 offset:14336
v_add_f32 v116,v116,v117
v_mfma_f32_16x16x32_bf16 v[40:43],a[188:191],v[56:59],v[40:43]
v_add_f32 v118,v118,v119
v_add_f32 v116,v116,v118
v_mfma_f32_16x16x32_bf16 v[36:39],a[128:131],v[60:63],v[36:39]
s_add_u32 m0,s62,16384
ds_read_b128 v[56:59],v2 offset:15360
v_mul_f32 v96,s75,v116
v_mfma_f32_16x16x32_bf16 v[40:43],a[132:135],v[60:63],v[40:43]
buffer_load_dwordx4 v1,s[36:39],s66 offen offset:0 sc1 lds
v_mfma_f32_16x16x32_bf16 v[36:39],a[136:139],v[64:67],v[36:39]
s_cmp_lt_u32 s67,4194304
ds_read_b128 v[60:63],v2 offset:8192
v_add_f32 v120,v120,v121
v_mfma_f32_16x16x32_bf16 v[40:43],a[140:143],v[64:67],v[40:43]
v_add_f32 v122,v122,v123
v_add_f32 v120,v120,v122
v_mfma_f32_16x16x32_bf16 v[36:39],a[144:147],v[68:71],v[36:39]
ds_read_b128 v[64:67],v2 offset:9216
v_mul_f32 v120,s75,v120
v_mfma_f32_16x16x32_bf16 v[40:43],a[148:151],v[68:71],v[40:43]
v_fma_f32 v120,-v96,v96,v120
v_max_f32 v120,0,v120
v_add_f32 v115,v15,v120
v_fma_f32 v120,v15,v115,v120
v_rsq_f32 v116,v120
v_mfma_f32_16x16x32_bf16 v[36:39],a[152:155],v[72:75],v[36:39]
s_waitcnt lgkmcnt(2)
ds_read_b128 v[68:71],v2 offset:10240
buffer_store_dwordx4 v[84:87],v3,s[36:39],s73 offen offset:0 sc0 sc1
v_mul_f32 v97,s76,v116
v_mul_f32 v98,v116,v116
v_mul_f32 v99,v120,v116
v_mul_f32 v98,s77,v98
v_mul_f32 v98,v98,v97
v_mfma_f32_16x16x32_bf16 v[40:43],a[156:159],v[72:75],v[40:43]
s_cbranch_scc1 Lg1_a0w3_%=
s_cmp_ge_u32 s48,4194304
s_cselect_b64 s[90:91],s[94:95],s[90:91]
s_cselect_b64 s[92:93],s[96:97],s[92:93]
s_cmp_lt_u32 s67,s59
s_cbranch_scc1 Lg1_d0w3_%=
s_branch Lg1_slow0_%=
Lg1_d0w0_%=:
v_mfma_f32_16x16x32_bf16 v[28:31],a[0:3],v[44:47],0
s_add_u32 m0,s62,16384
ds_read_b128 v[72:75],v2 offset:7168
s_lshr_b32 s72,s66,6
v_mfma_f32_16x16x32_bf16 v[32:35],a[4:7],v[44:47],0
buffer_load_dwordx4 v1,s[36:39],s66 offen offset:1024 sc1 lds
v_mfma_f32_16x16x32_bf16 v[28:31],a[8:11],v[48:51],v[28:31]
s_lshr_b32 s71,s62,2
ds_read_b128 v[44:47],v2 offset:8192
s_cmp_lt_u32 s66,4194304
v_mfma_f32_16x16x32_bf16 v[32:35],a[12:15],v[48:51],v[32:35]
s_cselect_b32 s72,s59,s72
v_fma_f32 v36,-v96,v20,v36
v_fma_f32 v37,-v96,v21,v37
v_mfma_f32_16x16x32_bf16 v[28:31],a[16:19],v[52:55],v[28:31]
ds_read_b128 v[48:51],v2 offset:9216
v_fma_f32 v38,-v96,v22,v38
v_mfma_f32_16x16x32_bf16 v[32:35],a[20:23],v[52:55],v[32:35]
buffer_load_dwordx4 v1,s[36:39],s66 offen offset:2048 sc1 lds
v_mfma_f32_16x16x32_bf16 v[28:31],a[24:27],v[56:59],v[28:31]
ds_read_b128 v[52:55],v2 offset:10240
v_fma_f32 v39,-v96,v23,v39
v_mfma_f32_16x16x32_bf16 v[32:35],a[28:31],v[56:59],v[32:35]
s_waitcnt lgkmcnt(3)
v_mul_f32 v76,v36,v36
v_mul_f32 v77,v37,v37
v_mfma_f32_16x16x32_bf16 v[28:31],a[32:35],v[60:63],v[28:31]
ds_read_b128 v[56:59],v2 offset:11264
v_mul_f32 v78,v38,v38
v_mfma_f32_16x16x32_bf16 v[32:35],a[36:39],v[60:63],v[32:35]
buffer_load_dwordx4 v1,s[36:39],s66 offen offset:3072 sc1 lds
v_mfma_f32_16x16x32_bf16 v[28:31],a[40:43],v[64:67],v[28:31]
s_add_u32 m0,s71,s88
ds_read_b128 v[60:63],v2 offset:12288
v_mul_f32 v79,v39,v39
v_mfma_f32_16x16x32_bf16 v[32:35],a[44:47],v[64:67],v[32:35]
buffer_load_dword v10,s[36:39],s72 offen offset:0 sc1 lds
v_mfma_f32_16x16x32_bf16 v[28:31],a[48:51],v[68:71],v[28:31]
s_lshr_b32 s73,s47,0
ds_read_b128 v[64:67],v2 offset:13312
v_fma_f32 v76,v98,v76,v97
v_mfma_f32_16x16x32_bf16 v[32:35],a[52:55],v[68:71],v[32:35]
s_lshr_b32 s69,s50,5
v_mul_f32 v76,v36,v76
v_fma_f32 v77,v98,v77,v97
v_mfma_f32_16x16x32_bf16 v[28:31],a[56:59],v[72:75],v[28:31]
s_cmp_lt_u32 s50,4194304
ds_read_b128 v[68:71],v2 offset:14336
v_mul_f32 v77,v37,v77
v_mfma_f32_16x16x32_bf16 v[32:35],a[60:63],v[72:75],v[32:35]
s_waitcnt lgkmcnt(3)
v_exp_f32 v76,v76
v_mfma_f32_16x16x32_bf16 v[28:31],a[64:67],v[44:47],v[28:31]
s_waitcnt vmcnt(10)
ds_read_b128 v[72:75],v2 offset:15360
v_fma_f32 v76,v76,v99,v99
v_mfma_f32_16x16x32_bf16 v[32:35],a[68:71],v[44:47],v[32:35]
s_barrier
v_mfma_f32_16x16x32_bf16 v[28:31],a[72:75],v[48:51],v[28:31]
s_cselect_b32 s68,0xffffcfff,-1
ds_read_b128 v[44:47],v2 offset:16384
ds_read_b32 v16,v7 offset:256
v_mfma_f32_16x16x32_bf16 v[32:35],a[76:79],v[48:51],v[32:35]
s_and_b32 s68,s68,s69
v_exp_f32 v77,v77
v_mfma_f32_16x16x32_bf16 v[28:31],a[80:83],v[52:55],v[28:31]
s_add_u32 m0,s78,0
ds_read_b128 v[48:51],v2 offset:17408
v_fma_f32 v77,v77,v99,v99
v_mfma_f32_16x16x32_bf16 v[32:35],a[84:87],v[52:55],v[32:35]
buffer_load_dword v6,s[36:39],s68 offen offset:0 sc1 lds
v_mfma_f32_16x16x32_bf16 v[28:31],a[88:91],v[56:59],v[28:31]
s_waitcnt lgkmcnt(1)
ds_read_b128 v[52:55],v2 offset:18432
v_fma_f32 v78,v98,v78,v97
v_mfma_f32_16x16x32_bf16 v[32:35],a[92:95],v[56:59],v[32:35]
v_cmp_eq_u32 vcc,s60,v16
v_rcp_f32 v76,v76)"
R"(
v_mul_f32 v78,v38,v78
v_mfma_f32_16x16x32_bf16 v[28:31],a[96:99],v[60:63],v[28:31]
s_xor_b32 s69,s64,65536
ds_read_b128 v[56:59],v2 offset:19456
v_fma_f32 v79,v98,v79,v97
v_mfma_f32_16x16x32_bf16 v[32:35],a[100:103],v[60:63],v[32:35]
s_cmp_ge_u32 s66,s59
v_exp_f32 v78,v78
v_mfma_f32_16x16x32_bf16 v[28:31],a[104:107],v[64:67],v[28:31]
s_cselect_b32 s62,s69,s63
s_cselect_b32 s86,s49,s50
s_cselect_b32 s87,vcc_lo,vcc_hi
s_cmp_eq_u32 s87,-1
s_mov_b32 m0,s62
s_cselect_b32 s67,s86,s59
v_mfma_f32_16x16x32_bf16 v[32:35],a[108:111],v[64:67],v[32:35]
s_xor_b32 s63,s63,65536
ds_read_b128 v[60:63],v2 offset:20480
buffer_load_dwordx4 v0,s[36:39],s67 offen offset:0 sc1 lds
v_mfma_f32_16x16x32_bf16 v[28:31],a[112:115],v[68:71],v[28:31]
s_add_u32 s53,s52,s55
ds_read_b128 v[64:67],v2 offset:21504
ds_read_b128 v[92:95],v8 offset:0
v_mfma_f32_16x16x32_bf16 v[32:35],a[116:119],v[68:71],v[32:35]
s_cmp_eq_u32 s53,s56
v_rcp_f32 v77,v77
v_mfma_f32_16x16x32_bf16 v[28:31],a[120:123],v[72:75],v[28:31]
s_cselect_b32 s53,s57,s53
ds_read_b128 v[68:71],v2 offset:22528
v_mul_f32 v79,v39,v79
v_mfma_f32_16x16x32_bf16 v[32:35],a[124:127],v[72:75],v[32:35]
s_waitcnt lgkmcnt(4)
buffer_load_dwordx4 v0,s[36:39],s67 offen offset:1024 sc1 lds
v_mfma_f32_16x16x32_bf16 v[28:31],a[128:131],v[44:47],v[28:31]
s_cmp_eq_u32 s53,s58
ds_read_b128 v[72:75],v2 offset:23552
v_fma_f32 v78,v78,v99,v99
v_mfma_f32_16x16x32_bf16 v[32:35],a[132:135],v[44:47],v[32:35]
s_cselect_b32 s53,s59,s53
v_exp_f32 v79,v79
v_mfma_f32_16x16x32_bf16 v[28:31],a[136:139],v[48:51],v[28:31]
s_waitcnt lgkmcnt(2)
ds_read_b128 v[44:47],v2 offset:24576
v_fma_f32 v79,v79,v99,v99
v_mfma_f32_16x16x32_bf16 v[32:35],a[140:143],v[48:51],v[32:35]
buffer_load_dwordx4 v0,s[36:39],s67 offen offset:2048 sc1 lds
v_mfma_f32_16x16x32_bf16 v[28:31],a[144:147],v[52:55],v[28:31]
ds_read_b128 v[48:51],v2 offset:25600
v_mul_f32 v36,v36,v76
v_mfma_f32_16x16x32_bf16 v[32:35],a[148:151],v[52:55],v[32:35]
v_rcp_f32 v78,v78
v_mfma_f32_16x16x32_bf16 v[28:31],a[152:155],v[56:59],v[28:31]
ds_read_b128 v[52:55],v2 offset:26624
v_mul_f32 v37,v37,v77
v_mfma_f32_16x16x32_bf16 v[32:35],a[156:159],v[56:59],v[32:35]
s_waitcnt lgkmcnt(3)
buffer_load_dwordx4 v0,s[36:39],s67 offen offset:3072 sc1 lds
v_mfma_f32_16x16x32_bf16 v[28:31],a[160:163],v[60:63],v[28:31]
s_add_u32 m0,s62,16384
ds_read_b128 v[56:59],v2 offset:27648
v_mul_f32 v38,v38,v78
v_mfma_f32_16x16x32_bf16 v[32:35],a[164:167],v[60:63],v[32:35]
s_cmp_ge_u32 s66,s59
v_rcp_f32 v79,v79
v_mfma_f32_16x16x32_bf16 v[28:31],a[168:171],v[64:67],v[28:31]
ds_read_b128 v[60:63],v2 offset:28672
v_mul_f32 v39,v39,v79
v_mfma_f32_16x16x32_bf16 v[32:35],a[172:175],v[64:67],v[32:35]
buffer_load_dwordx4 v1,s[36:39],s67 offen offset:0 sc1 lds
v_mfma_f32_16x16x32_bf16 v[28:31],a[176:179],v[68:71],v[28:31]
ds_read_b128 v[64:67],v2 offset:29696
v_cvt_pk_bf16_f32 v84,v36,v37
v_mfma_f32_16x16x32_bf16 v[32:35],a[180:183],v[68:71],v[32:35]
v_cvt_pk_bf16_f32 v85,v38,v39
v_fma_f32 v40,-v96,v24,v40
v_mfma_f32_16x16x32_bf16 v[28:31],a[184:187],v[72:75],v[28:31]
ds_read_b128 v[68:71],v2 offset:30720
v_fma_f32 v41,-v96,v25,v41
v_mfma_f32_16x16x32_bf16 v[32:35],a[188:191],v[72:75],v[32:35]
s_waitcnt lgkmcnt(3)
v_fma_f32 v42,-v96,v26,v42
v_fma_f32 v43,-v96,v27,v43
v_mfma_f32_16x16x32_bf16 v[28:31],a[192:195],v[44:47],v[28:31]
s_waitcnt vmcnt(12)
ds_read_b128 v[72:75],v2 offset:31744
v_mul_f32 v80,v40,v40
v_mfma_f32_16x16x32_bf16 v[32:35],a[196:199],v[44:47],v[32:35]
s_barrier
v_mfma_f32_16x16x32_bf16 v[28:31],a[200:203],v[48:51],v[28:31]
ds_read_b128 v[44:47],v2 offset:32768
v_mul_f32 v81,v41,v41
v_mfma_f32_16x16x32_bf16 v[32:35],a[204:207],v[48:51],v[32:35]
v_mul_f32 v82,v42,v42
v_mul_f32 v83,v43,v43
v_mfma_f32_16x16x32_bf16 v[28:31],a[208:211],v[52:55],v[28:31]
ds_read_b128 v[48:51],v2 offset:33792
v_fma_f32 v80,v98,v80,v97
v_mfma_f32_16x16x32_bf16 v[32:35],a[212:215],v[52:55],v[32:35]
v_mul_f32 v80,v40,v80
v_fma_f32 v81,v98,v81,v97
v_mfma_f32_16x16x32_bf16 v[28:31],a[216:219],v[56:59],v[28:31]
ds_read_b128 v[52:55],v2 offset:34816
v_mul_f32 v81,v41,v81
v_mfma_f32_16x16x32_bf16 v[32:35],a[220:223],v[56:59],v[32:35]
s_waitcnt lgkmcnt(3)
v_exp_f32 v80,v80
v_mfma_f32_16x16x32_bf16 v[28:31],a[224:227],v[60:63],v[28:31]
v_fma_f32 v82,v98,v82,v97
v_fma_f32 v83,v98,v83,v97
v_mul_f32 v82,v42,v82
v_mul_f32 v83,v43,v83
v_exp_f32 v81,v81
v_exp_f32 v82,v82
v_exp_f32 v83,v83
v_mfma_f32_16x16x32_bf16 v[32:35],a[228:231],v[60:63],v[32:35]
ds_read_b128 v[56:59],v2 offset:35840
v_fma_f32 v80,v80,v99,v99
v_fma_f32 v81,v81,v99,v99
v_fma_f32 v82,v82,v99,v99
v_fma_f32 v83,v83,v99,v99
v_rcp_f32 v80,v80
v_rcp_f32 v81,v81
v_rcp_f32 v82,v82
v_rcp_f32 v83,v83
v_mfma_f32_16x16x32_bf16 v[28:31],a[232:235],v[64:67],v[28:31]
v_mul_f32 v40,v40,v80
v_mul_f32 v41,v41,v81
v_mul_f32 v42,v42,v82
v_mul_f32 v43,v43,v83
v_cvt_pk_bf16_f32 v86,v40,v41
v_cvt_pk_bf16_f32 v87,v42,v43
v_mfma_f32_16x16x32_bf16 v[32:35],a[236:239],v[64:67],v[32:35]
ds_read_b128 v[60:63],v2 offset:36864
buffer_store_dwordx4 v[84:87],v3,s[36:39],s73 offen offset:0 sc0 sc1
v_mfma_f32_16x16x32_bf16 v[28:31],a[240:243],v[68:71],v[28:31]
ds_read_b128 v[64:67],v2 offset:37888
v_mfma_f32_16x16x32_bf16 v[32:35],a[244:247],v[68:71],v[32:35]
v_mfma_f32_16x16x32_bf16 v[28:31],a[248:251],v[72:75],v[28:31]
s_waitcnt lgkmcnt(2)
ds_read_b128 v[68:71],v2 offset:38912
v_mfma_f32_16x16x32_bf16 v[32:35],a[252:255],v[72:75],v[32:35]
s_cbranch_scc1 Lg1_slow1_%=
Lg1_d1w0_%=:
v_mfma_f32_16x16x32_bf16 v[36:39],a[0:3],v[44:47],0
s_add_u32 m0,s62,16384
ds_read_b128 v[72:75],v2 offset:39936
s_lshr_b32 s72,s67,6
v_mfma_f32_16x16x32_bf16 v[40:43],a[4:7],v[44:47],0
buffer_load_dwordx4 v1,s[36:39],s67 offen offset:1024 sc1 lds
v_mfma_f32_16x16x32_bf16 v[36:39],a[8:11],v[48:51],v[36:39]
s_lshr_b32 s71,s62,2
ds_read_b128 v[44:47],v2 offset:40960
s_cmp_lt_u32 s67,4194304
v_mfma_f32_16x16x32_bf16 v[40:43],a[12:15],v[48:51],v[40:43]
s_cselect_b32 s72,s59,s72
v_fma_f32 v28,-v92,v20,v28
v_fma_f32 v29,-v92,v21,v29
v_mfma_f32_16x16x32_bf16 v[36:39],a[16:19],v[52:55],v[36:39]
s_mov_b64 s[44:45],s[46:47]
ds_read_b128 v[48:51],v2 offset:41984
v_fma_f32 v30,-v92,v22,v30
v_mfma_f32_16x16x32_bf16 v[40:43],a[20:23],v[52:55],v[40:43]
buffer_load_dwordx4 v1,s[36:39],s67 offen offset:2048 sc1 lds
v_mfma_f32_16x16x32_bf16 v[36:39],a[24:27],v[56:59],v[36:39]
s_mov_b64 s[46:47],s[48:49]
ds_read_b128 v[52:55],v2 offset:43008
v_fma_f32 v31,-v92,v23,v31
v_mfma_f32_16x16x32_bf16 v[40:43],a[28:31],v[56:59],v[40:43]
s_waitcnt lgkmcnt(3)
v_mul_f32 v76,v28,v28
v_mul_f32 v77,v29,v29
v_mfma_f32_16x16x32_bf16 v[36:39],a[32:35],v[60:63],v[36:39]
ds_read_b128 v[56:59],v2 offset:44032
v_mul_f32 v78,v30,v30
v_mfma_f32_16x16x32_bf16 v[40:43],a[36:39],v[60:63],v[40:43]
buffer_load_dwordx4 v1,s[36:39],s67 offen offset:3072 sc1 lds
v_mfma_f32_16x16x32_bf16 v[36:39],a[40:43],v[64:67],v[36:39]
s_add_u32 m0,s71,s88
ds_read_b128 v[60:63],v2 offset:45056
v_mul_f32 v79,v31,v31
v_mfma_f32_16x16x32_bf16 v[40:43],a[44:47],v[64:67],v[40:43]
buffer_load_dword v10,s[36:39],s72 offen offset:0 sc1 lds
v_mfma_f32_16x16x32_bf16 v[36:39],a[48:51],v[68:71],v[36:39]
s_lshr_b32 s73,s48,0
ds_read_b128 v[64:67],v2 offset:46080
v_fma_f32 v76,v94,v76,v93
v_mfma_f32_16x16x32_bf16 v[40:43],a[52:55],v[68:71],v[40:43]
s_lshr_b32 s69,s51,5
v_mul_f32 v76,v28,v76
v_fma_f32 v77,v94,v77,v93
v_mfma_f32_16x16x32_bf16 v[36:39],a[56:59],v[72:75],v[36:39]
s_cmp_lt_u32 s51,4194304
ds_read_b128 v[68:71],v2 offset:47104
v_mul_f32 v77,v29,v77
v_mfma_f32_16x16x32_bf16 v[40:43],a[60:63],v[72:75],v[40:43]
s_waitcnt lgkmcnt(3)
v_exp_f32 v76,v76
v_mfma_f32_16x16x32_bf16 v[36:39],a[64:67],v[44:47],v[36:39]
s_waitcnt vmcnt(10)
ds_read_b128 v[72:75],v2 offset:48128)"
R"(
v_fma_f32 v76,v76,v95,v95
v_mfma_f32_16x16x32_bf16 v[40:43],a[68:71],v[44:47],v[40:43]
s_barrier
v_mfma_f32_16x16x32_bf16 v[36:39],a[72:75],v[48:51],v[36:39]
s_cselect_b32 s68,0xffffcfff,-1
ds_read_b128 v[44:47],v2 offset:49152
ds_read_b32 v16,v7 offset:0
v_mfma_f32_16x16x32_bf16 v[40:43],a[76:79],v[48:51],v[40:43]
s_and_b32 s68,s68,s69
v_exp_f32 v77,v77
v_mfma_f32_16x16x32_bf16 v[36:39],a[80:83],v[52:55],v[36:39]
s_add_u32 m0,s78,256
ds_read_b128 v[48:51],v2 offset:50176
v_fma_f32 v77,v77,v95,v95
v_mfma_f32_16x16x32_bf16 v[40:43],a[84:87],v[52:55],v[40:43]
buffer_load_dword v6,s[36:39],s68 offen offset:0 sc1 lds
v_mfma_f32_16x16x32_bf16 v[36:39],a[88:91],v[56:59],v[36:39]
s_waitcnt lgkmcnt(1)
ds_read_b128 v[52:55],v2 offset:51200
v_fma_f32 v78,v94,v78,v93
v_mfma_f32_16x16x32_bf16 v[40:43],a[92:95],v[56:59],v[40:43]
v_cmp_eq_u32 vcc,s60,v16
v_rcp_f32 v76,v76
v_mul_f32 v78,v30,v78
v_mfma_f32_16x16x32_bf16 v[36:39],a[96:99],v[60:63],v[36:39]
s_xor_b32 s69,s63,65536
ds_read_b128 v[56:59],v2 offset:52224
v_fma_f32 v79,v94,v79,v93
v_mfma_f32_16x16x32_bf16 v[40:43],a[100:103],v[60:63],v[40:43]
s_cmp_ge_u32 s67,s59
v_exp_f32 v78,v78
v_mfma_f32_16x16x32_bf16 v[36:39],a[104:107],v[64:67],v[36:39]
s_cselect_b32 s62,s69,s64
s_cselect_b32 s86,s50,s51
s_cselect_b32 s87,vcc_lo,vcc_hi
s_cmp_eq_u32 s87,-1
s_mov_b32 m0,s62
s_cselect_b32 s66,s86,s59
v_mfma_f32_16x16x32_bf16 v[40:43],a[108:111],v[64:67],v[40:43]
s_xor_b32 s64,s64,65536
ds_read_b128 v[60:63],v2 offset:53248
buffer_load_dwordx4 v0,s[36:39],s66 offen offset:0 sc1 lds
v_mfma_f32_16x16x32_bf16 v[36:39],a[112:115],v[68:71],v[36:39]
s_add_u32 s74,s53,s55
ds_read_b128 v[64:67],v2 offset:54272
ds_read_b128 v[96:99],v8 offset:8192
v_mfma_f32_16x16x32_bf16 v[40:43],a[116:119],v[68:71],v[40:43]
s_cmp_eq_u32 s74,s56
v_rcp_f32 v77,v77
v_mfma_f32_16x16x32_bf16 v[36:39],a[120:123],v[72:75],v[36:39]
s_cselect_b32 s74,s57,s74
ds_read_b128 v[68:71],v2 offset:55296
v_mul_f32 v79,v31,v79
v_mfma_f32_16x16x32_bf16 v[40:43],a[124:127],v[72:75],v[40:43]
s_waitcnt lgkmcnt(4)
buffer_load_dwordx4 v0,s[36:39],s66 offen offset:1024 sc1 lds
v_mfma_f32_16x16x32_bf16 v[36:39],a[128:131],v[44:47],v[36:39]
s_cmp_eq_u32 s74,s58
ds_read_b128 v[72:75],v2 offset:56320
v_fma_f32 v78,v78,v95,v95
v_mfma_f32_16x16x32_bf16 v[40:43],a[132:135],v[44:47],v[40:43]
s_cselect_b32 s74,s59,s74
v_exp_f32 v79,v79
v_mfma_f32_16x16x32_bf16 v[36:39],a[136:139],v[48:51],v[36:39]
s_waitcnt lgkmcnt(2)
ds_read_b128 v[44:47],v2 offset:57344
v_fma_f32 v79,v79,v95,v95
v_mfma_f32_16x16x32_bf16 v[40:43],a[140:143],v[48:51],v[40:43]
buffer_load_dwordx4 v0,s[36:39],s66 offen offset:2048 sc1 lds
v_mfma_f32_16x16x32_bf16 v[36:39],a[144:147],v[52:55],v[36:39]
s_mov_b64 s[48:49],s[50:51]
ds_read_b128 v[48:51],v2 offset:58368
v_mul_f32 v28,v28,v76
v_mfma_f32_16x16x32_bf16 v[40:43],a[148:151],v[52:55],v[40:43]
s_mov_b64 s[50:51],s[52:53]
v_rcp_f32 v78,v78
v_mfma_f32_16x16x32_bf16 v[36:39],a[152:155],v[56:59],v[36:39]
s_mov_b32 s52,s74
ds_read_b128 v[52:55],v2 offset:59392
v_mul_f32 v29,v29,v77
v_mfma_f32_16x16x32_bf16 v[40:43],a[156:159],v[56:59],v[40:43]
s_waitcnt lgkmcnt(3)
buffer_load_dwordx4 v0,s[36:39],s66 offen offset:3072 sc1 lds
v_mfma_f32_16x16x32_bf16 v[36:39],a[160:163],v[60:63],v[36:39]
s_add_u32 m0,s62,16384
ds_read_b128 v[56:59],v2 offset:60416
v_mul_f32 v30,v30,v78
v_mfma_f32_16x16x32_bf16 v[40:43],a[164:167],v[60:63],v[40:43]
s_cmp_lt_u32 s67,s59
v_rcp_f32 v79,v79
v_mfma_f32_16x16x32_bf16 v[36:39],a[168:171],v[64:67],v[36:39]
ds_read_b128 v[60:63],v2 offset:61440
v_mul_f32 v31,v31,v79
v_mfma_f32_16x16x32_bf16 v[40:43],a[172:175],v[64:67],v[40:43]
buffer_load_dwordx4 v1,s[36:39],s66 offen offset:0 sc1 lds
v_mfma_f32_16x16x32_bf16 v[36:39],a[176:179],v[68:71],v[36:39]
ds_read_b128 v[64:67],v2 offset:62464
v_cvt_pk_bf16_f32 v84,v28,v29
v_mfma_f32_16x16x32_bf16 v[40:43],a[180:183],v[68:71],v[40:43]
v_cvt_pk_bf16_f32 v85,v30,v31
v_fma_f32 v32,-v92,v24,v32
v_mfma_f32_16x16x32_bf16 v[36:39],a[184:187],v[72:75],v[36:39]
ds_read_b128 v[68:71],v2 offset:63488
v_fma_f32 v33,-v92,v25,v33
v_mfma_f32_16x16x32_bf16 v[40:43],a[188:191],v[72:75],v[40:43]
s_waitcnt lgkmcnt(3)
v_fma_f32 v34,-v92,v26,v34
v_fma_f32 v35,-v92,v27,v35
v_mfma_f32_16x16x32_bf16 v[36:39],a[192:195],v[44:47],v[36:39]
s_waitcnt vmcnt(12)
ds_read_b128 v[72:75],v2 offset:64512
v_xor_b32 v2,65536,v2
v_mfma_f32_16x16x32_bf16 v[40:43],a[196:199],v[44:47],v[40:43]
s_barrier
v_mfma_f32_16x16x32_bf16 v[36:39],a[200:203],v[48:51],v[36:39]
ds_read_b128 v[44:47],v2 offset:0
v_mul_f32 v80,v32,v32
v_mfma_f32_16x16x32_bf16 v[40:43],a[204:207],v[48:51],v[40:43]
v_mul_f32 v81,v33,v33
v_mul_f32 v82,v34,v34
v_mfma_f32_16x16x32_bf16 v[36:39],a[208:211],v[52:55],v[36:39]
ds_read_b128 v[48:51],v2 offset:1024
v_mul_f32 v83,v35,v35
v_mfma_f32_16x16x32_bf16 v[40:43],a[212:215],v[52:55],v[40:43]
v_fma_f32 v80,v94,v80,v93
v_mul_f32 v80,v32,v80
v_mfma_f32_16x16x32_bf16 v[36:39],a[216:219],v[56:59],v[36:39]
ds_read_b128 v[52:55],v2 offset:2048
v_fma_f32 v81,v94,v81,v93
v_mfma_f32_16x16x32_bf16 v[40:43],a[220:223],v[56:59],v[40:43]
s_waitcnt lgkmcnt(3)
v_exp_f32 v80,v80
v_mfma_f32_16x16x32_bf16 v[36:39],a[224:227],v[60:63],v[36:39]
v_fma_f32 v82,v94,v82,v93
v_fma_f32 v83,v94,v83,v93
v_mul_f32 v81,v33,v81
v_mul_f32 v82,v34,v82
v_mul_f32 v83,v35,v83
v_exp_f32 v81,v81
v_exp_f32 v82,v82
v_exp_f32 v83,v83
v_mfma_f32_16x16x32_bf16 v[40:43],a[228:231],v[60:63],v[40:43]
ds_read_b128 v[56:59],v2 offset:3072
v_fma_f32 v80,v80,v95,v95
v_fma_f32 v81,v81,v95,v95
v_fma_f32 v82,v82,v95,v95
v_fma_f32 v83,v83,v95,v95
v_rcp_f32 v80,v80
v_rcp_f32 v81,v81
v_rcp_f32 v82,v82
v_rcp_f32 v83,v83
v_mfma_f32_16x16x32_bf16 v[36:39],a[232:235],v[64:67],v[36:39]
v_mul_f32 v32,v32,v80
v_mul_f32 v33,v33,v81
v_mul_f32 v34,v34,v82
v_mul_f32 v35,v35,v83
v_cvt_pk_bf16_f32 v86,v32,v33
v_cvt_pk_bf16_f32 v87,v34,v35
v_mfma_f32_16x16x32_bf16 v[40:43],a[236:239],v[64:67],v[40:43]
ds_read_b128 v[60:63],v2 offset:4096
buffer_store_dwordx4 v[84:87],v3,s[36:39],s73 offen offset:0 sc0 sc1
v_xor_b32 v8,16384,v8
v_mfma_f32_16x16x32_bf16 v[36:39],a[240:243],v[68:71],v[36:39]
ds_read_b128 v[64:67],v2 offset:5120
v_mfma_f32_16x16x32_bf16 v[40:43],a[244:247],v[68:71],v[40:43]
v_mfma_f32_16x16x32_bf16 v[36:39],a[248:251],v[72:75],v[36:39]
s_waitcnt lgkmcnt(2)
ds_read_b128 v[68:71],v2 offset:6144
v_mfma_f32_16x16x32_bf16 v[40:43],a[252:255],v[72:75],v[40:43]
s_cbranch_scc1 Lg1_d0w0_%=
s_branch Lg1_slow0_%=
Lg1_d0w1_%=:
v_mfma_f32_16x16x32_bf16 v[28:31],a[32:35],v[44:47],0
s_add_u32 m0,s62,16384
ds_read_b128 v[72:75],v2 offset:3072
s_lshr_b32 s72,s66,6
v_mfma_f32_16x16x32_bf16 v[32:35],a[36:39],v[44:47],0
s_lshr_b32 s71,s62,2
s_cmp_lt_u32 s66,4194304
s_cselect_b32 s72,s59,s72
v_mfma_f32_16x16x32_bf16 v[28:31],a[40:43],v[48:51],v[28:31]
buffer_load_dwordx4 v1,s[36:39],s66 offen offset:1024 sc1 lds
v_mfma_f32_16x16x32_bf16 v[32:35],a[44:47],v[48:51],v[32:35]
ds_read_b128 v[44:47],v2 offset:12288
v_fma_f32 v36,-v96,v20,v36
v_mfma_f32_16x16x32_bf16 v[28:31],a[48:51],v[52:55],v[28:31]
ds_read_b128 v[48:51],v2 offset:13312
v_fma_f32 v37,-v96,v21,v37
v_mfma_f32_16x16x32_bf16 v[32:35],a[52:55],v[52:55],v[32:35]
v_fma_f32 v38,-v96,v22,v38
v_fma_f32 v39,-v96,v23,v39
v_mfma_f32_16x16x32_bf16 v[28:31],a[56:59],v[56:59],v[28:31]
buffer_load_dwordx4 v1,s[36:39],s66 offen offset:2048 sc1 lds
v_mfma_f32_16x16x32_bf16 v[32:35],a[60:63],v[56:59],v[32:35]
s_waitcnt lgkmcnt(2)
ds_read_b128 v[52:55],v2 offset:14336
v_mul_f32 v76,v36,v36
v_mfma_f32_16x16x32_bf16 v[28:31],a[0:3],v[60:63],v[28:31]
ds_read_b128 v[56:59],v2 offset:15360
v_mul_f32 v77,v37,v37)"
R"(
v_mfma_f32_16x16x32_bf16 v[32:35],a[4:7],v[60:63],v[32:35]
v_mul_f32 v78,v38,v38
v_mul_f32 v79,v39,v39
v_mfma_f32_16x16x32_bf16 v[28:31],a[8:11],v[64:67],v[28:31]
buffer_load_dwordx4 v1,s[36:39],s66 offen offset:3072 sc1 lds
v_mfma_f32_16x16x32_bf16 v[32:35],a[12:15],v[64:67],v[32:35]
s_add_u32 m0,s71,s88
ds_read_b128 v[60:63],v2 offset:8192
v_fma_f32 v76,v98,v76,v97
v_mfma_f32_16x16x32_bf16 v[28:31],a[16:19],v[68:71],v[28:31]
buffer_load_dword v10,s[36:39],s72 offen offset:0 sc1 lds
v_mfma_f32_16x16x32_bf16 v[32:35],a[20:23],v[68:71],v[32:35]
s_lshr_b32 s73,s47,0
ds_read_b128 v[64:67],v2 offset:9216
v_mul_f32 v76,v36,v76
v_mfma_f32_16x16x32_bf16 v[28:31],a[24:27],v[72:75],v[28:31]
s_lshr_b32 s69,s51,5
ds_read_b128 v[68:71],v2 offset:10240
v_fma_f32 v77,v98,v77,v97
v_mfma_f32_16x16x32_bf16 v[32:35],a[28:31],v[72:75],v[32:35]
s_waitcnt lgkmcnt(3)
v_exp_f32 v76,v76
v_mfma_f32_16x16x32_bf16 v[28:31],a[96:99],v[44:47],v[28:31]
s_waitcnt vmcnt(10)
ds_read_b128 v[72:75],v2 offset:11264
v_mul_f32 v77,v37,v77
v_mfma_f32_16x16x32_bf16 v[32:35],a[100:103],v[44:47],v[32:35]
s_barrier
v_mfma_f32_16x16x32_bf16 v[28:31],a[104:107],v[48:51],v[28:31]
s_cmp_lt_u32 s51,4194304
ds_read_b128 v[44:47],v2 offset:20480
ds_read_b32 v16,v7 offset:256
v_mfma_f32_16x16x32_bf16 v[32:35],a[108:111],v[48:51],v[32:35]
s_cselect_b32 s68,0xffffcfff,-1
v_exp_f32 v77,v77
v_mfma_f32_16x16x32_bf16 v[28:31],a[112:115],v[52:55],v[28:31]
s_and_b32 s68,s68,s69
ds_read_b128 v[48:51],v2 offset:21504
v_fma_f32 v76,v76,v99,v99
v_mfma_f32_16x16x32_bf16 v[32:35],a[116:119],v[52:55],v[32:35]
s_add_u32 m0,s78,0
v_rcp_f32 v76,v76
v_mfma_f32_16x16x32_bf16 v[28:31],a[120:123],v[56:59],v[28:31]
s_waitcnt lgkmcnt(1)
buffer_load_dword v6,s[36:39],s68 offen offset:0 sc1 lds
v_mfma_f32_16x16x32_bf16 v[32:35],a[124:127],v[56:59],v[32:35]
ds_read_b128 v[52:55],v2 offset:22528
v_fma_f32 v77,v77,v99,v99
v_mfma_f32_16x16x32_bf16 v[28:31],a[64:67],v[60:63],v[28:31]
s_xor_b32 s69,s64,65536
v_cmp_eq_u32 vcc,s60,v16
v_rcp_f32 v77,v77
v_fma_f32 v78,v98,v78,v97
v_mfma_f32_16x16x32_bf16 v[32:35],a[68:71],v[60:63],v[32:35]
s_cmp_ge_u32 s66,s59
ds_read_b128 v[56:59],v2 offset:23552
v_mul_f32 v78,v38,v78
v_mfma_f32_16x16x32_bf16 v[28:31],a[72:75],v[64:67],v[28:31]
s_cselect_b32 s62,s69,s63
ds_read_b128 v[60:63],v2 offset:16384
v_fma_f32 v79,v98,v79,v97
v_mfma_f32_16x16x32_bf16 v[32:35],a[76:79],v[64:67],v[32:35]
s_cselect_b32 s86,s49,s50
s_cselect_b32 s87,vcc_lo,vcc_hi
s_cmp_eq_u32 s87,-1
s_mov_b32 m0,s62
s_cselect_b32 s67,s86,s59
v_mfma_f32_16x16x32_bf16 v[28:31],a[80:83],v[68:71],v[28:31]
buffer_load_dwordx4 v0,s[36:39],s67 offen offset:0 sc1 lds
v_mfma_f32_16x16x32_bf16 v[32:35],a[84:87],v[68:71],v[32:35]
s_xor_b32 s63,s63,65536
ds_read_b128 v[64:67],v2 offset:17408
ds_read_b128 v[92:95],v8 offset:0
v_mfma_f32_16x16x32_bf16 v[28:31],a[88:91],v[72:75],v[28:31]
s_add_u32 s53,s52,s55
ds_read_b128 v[68:71],v2 offset:18432
v_mul_f32 v79,v39,v79
v_mfma_f32_16x16x32_bf16 v[32:35],a[92:95],v[72:75],v[32:35]
s_waitcnt lgkmcnt(4)
v_exp_f32 v78,v78
v_mfma_f32_16x16x32_bf16 v[28:31],a[160:163],v[44:47],v[28:31]
buffer_load_dwordx4 v0,s[36:39],s67 offen offset:1024 sc1 lds
v_mfma_f32_16x16x32_bf16 v[32:35],a[164:167],v[44:47],v[32:35]
s_cmp_eq_u32 s53,s56
ds_read_b128 v[72:75],v2 offset:19456
v_fma_f32 v78,v78,v99,v99
v_mfma_f32_16x16x32_bf16 v[28:31],a[168:171],v[48:51],v[28:31]
s_cselect_b32 s53,s57,s53
ds_read_b128 v[44:47],v2 offset:28672
v_mul_f32 v36,v36,v76
v_mfma_f32_16x16x32_bf16 v[32:35],a[172:175],v[48:51],v[32:35]
s_waitcnt lgkmcnt(3)
v_exp_f32 v79,v79
v_mfma_f32_16x16x32_bf16 v[28:31],a[176:179],v[52:55],v[28:31]
buffer_load_dwordx4 v0,s[36:39],s67 offen offset:2048 sc1 lds
v_mfma_f32_16x16x32_bf16 v[32:35],a[180:183],v[52:55],v[32:35]
s_cmp_eq_u32 s53,s58
ds_read_b128 v[48:51],v2 offset:29696
v_fma_f32 v79,v79,v99,v99
v_mfma_f32_16x16x32_bf16 v[28:31],a[184:187],v[56:59],v[28:31]
s_cselect_b32 s53,s59,s53
ds_read_b128 v[52:55],v2 offset:30720
v_mul_f32 v37,v37,v77
v_mfma_f32_16x16x32_bf16 v[32:35],a[188:191],v[56:59],v[32:35]
s_waitcnt lgkmcnt(3)
v_rcp_f32 v78,v78
v_mfma_f32_16x16x32_bf16 v[28:31],a[128:131],v[60:63],v[28:31]
buffer_load_dwordx4 v0,s[36:39],s67 offen offset:3072 sc1 lds
v_mfma_f32_16x16x32_bf16 v[32:35],a[132:135],v[60:63],v[32:35]
s_add_u32 m0,s62,16384
ds_read_b128 v[56:59],v2 offset:31744
v_mul_f32 v38,v38,v78
v_mfma_f32_16x16x32_bf16 v[28:31],a[136:139],v[64:67],v[28:31]
s_cmp_ge_u32 s66,s59
ds_read_b128 v[60:63],v2 offset:24576
v_cvt_pk_bf16_f32 v84,v36,v37
v_mfma_f32_16x16x32_bf16 v[32:35],a[140:143],v[64:67],v[32:35]
v_rcp_f32 v79,v79
v_mfma_f32_16x16x32_bf16 v[28:31],a[144:147],v[68:71],v[28:31]
buffer_load_dwordx4 v1,s[36:39],s67 offen offset:0 sc1 lds
v_mfma_f32_16x16x32_bf16 v[32:35],a[148:151],v[68:71],v[32:35]
ds_read_b128 v[64:67],v2 offset:25600
v_mul_f32 v39,v39,v79
v_mfma_f32_16x16x32_bf16 v[28:31],a[152:155],v[72:75],v[28:31]
ds_read_b128 v[68:71],v2 offset:26624
v_cvt_pk_bf16_f32 v85,v38,v39
v_mfma_f32_16x16x32_bf16 v[32:35],a[156:159],v[72:75],v[32:35]
s_waitcnt lgkmcnt(3)
v_fma_f32 v40,-v96,v24,v40
v_fma_f32 v41,-v96,v25,v41
v_mfma_f32_16x16x32_bf16 v[28:31],a[224:227],v[44:47],v[28:31]
s_waitcnt vmcnt(12)
ds_read_b128 v[72:75],v2 offset:27648
v_fma_f32 v42,-v96,v26,v42
v_mfma_f32_16x16x32_bf16 v[32:35],a[228:231],v[44:47],v[32:35]
s_barrier
v_mfma_f32_16x16x32_bf16 v[28:31],a[232:235],v[48:51],v[28:31]
ds_read_b128 v[44:47],v2 offset:36864
v_fma_f32 v43,-v96,v27,v43
v_mfma_f32_16x16x32_bf16 v[32:35],a[236:239],v[48:51],v[32:35]
v_mul_f32 v80,v40,v40
v_mul_f32 v81,v41,v41
v_mfma_f32_16x16x32_bf16 v[28:31],a[240:243],v[52:55],v[28:31]
ds_read_b128 v[48:51],v2 offset:37888
v_mul_f32 v82,v42,v42
v_mfma_f32_16x16x32_bf16 v[32:35],a[244:247],v[52:55],v[32:35]
v_mul_f32 v83,v43,v43
v_fma_f32 v80,v98,v80,v97
v_mfma_f32_16x16x32_bf16 v[28:31],a[248:251],v[56:59],v[28:31]
ds_read_b128 v[52:55],v2 offset:38912
v_mul_f32 v80,v40,v80
v_mfma_f32_16x16x32_bf16 v[32:35],a[252:255],v[56:59],v[32:35]
s_waitcnt lgkmcnt(3)
v_exp_f32 v80,v80
v_mfma_f32_16x16x32_bf16 v[28:31],a[192:195],v[60:63],v[28:31]
ds_read_b128 v[56:59],v2 offset:39936
v_fma_f32 v80,v80,v99,v99
v_mfma_f32_16x16x32_bf16 v[32:35],a[196:199],v[60:63],v[32:35]
v_fma_f32 v81,v98,v81,v97
v_fma_f32 v82,v98,v82,v97
v_fma_f32 v83,v98,v83,v97
v_mul_f32 v81,v41,v81
v_mul_f32 v82,v42,v82
v_mul_f32 v83,v43,v83
v_exp_f32 v81,v81
v_exp_f32 v82,v82
v_exp_f32 v83,v83
v_mfma_f32_16x16x32_bf16 v[28:31],a[200:203],v[64:67],v[28:31]
v_fma_f32 v81,v81,v99,v99
v_fma_f32 v82,v82,v99,v99
v_fma_f32 v83,v83,v99,v99
v_rcp_f32 v80,v80
v_rcp_f32 v81,v81
v_rcp_f32 v82,v82
v_rcp_f32 v83,v83
v_mfma_f32_16x16x32_bf16 v[32:35],a[204:207],v[64:67],v[32:35]
ds_read_b128 v[60:63],v2 offset:32768
v_mul_f32 v40,v40,v80
v_mul_f32 v41,v41,v81
v_mul_f32 v42,v42,v82
v_mul_f32 v43,v43,v83
v_cvt_pk_bf16_f32 v86,v40,v41
v_cvt_pk_bf16_f32 v87,v42,v43
v_mfma_f32_16x16x32_bf16 v[28:31],a[208:211],v[68:71],v[28:31]
buffer_store_dwordx4 v[84:87],v3,s[36:39],s73 offen offset:0 sc0 sc1
ds_read_b128 v[64:67],v2 offset:33792
v_mfma_f32_16x16x32_bf16 v[32:35],a[212:215],v[68:71],v[32:35]
v_mfma_f32_16x16x32_bf16 v[28:31],a[216:219],v[72:75],v[28:31]
s_waitcnt lgkmcnt(2)
ds_read_b128 v[68:71],v2 offset:34816
v_mfma_f32_16x16x32_bf16 v[32:35],a[220:223],v[72:75],v[32:35]
s_cbranch_scc1 Lg1_slow1_%=
Lg1_d1w1_%=:
v_mfma_f32_16x16x32_bf16 v[36:39],a[32:35],v[44:47],0
s_add_u32 m0,s62,16384
ds_read_b128 v[72:75],v2 offset:35840
s_lshr_b32 s72,s67,6
v_mfma_f32_16x16x32_bf16 v[40:43],a[36:39],v[44:47],0
s_lshr_b32 s71,s62,2
s_cmp_lt_u32 s67,4194304)"
R"(
s_cselect_b32 s72,s59,s72
v_mfma_f32_16x16x32_bf16 v[36:39],a[40:43],v[48:51],v[36:39]
buffer_load_dwordx4 v1,s[36:39],s67 offen offset:1024 sc1 lds
v_mfma_f32_16x16x32_bf16 v[40:43],a[44:47],v[48:51],v[40:43]
s_mov_b64 s[44:45],s[46:47]
ds_read_b128 v[44:47],v2 offset:45056
v_fma_f32 v28,-v92,v20,v28
v_mfma_f32_16x16x32_bf16 v[36:39],a[48:51],v[52:55],v[36:39]
s_mov_b64 s[46:47],s[48:49]
ds_read_b128 v[48:51],v2 offset:46080
v_fma_f32 v29,-v92,v21,v29
v_mfma_f32_16x16x32_bf16 v[40:43],a[52:55],v[52:55],v[40:43]
v_fma_f32 v30,-v92,v22,v30
v_fma_f32 v31,-v92,v23,v31
v_mfma_f32_16x16x32_bf16 v[36:39],a[56:59],v[56:59],v[36:39]
buffer_load_dwordx4 v1,s[36:39],s67 offen offset:2048 sc1 lds
v_mfma_f32_16x16x32_bf16 v[40:43],a[60:63],v[56:59],v[40:43]
s_waitcnt lgkmcnt(2)
ds_read_b128 v[52:55],v2 offset:47104
v_mul_f32 v76,v28,v28
v_mfma_f32_16x16x32_bf16 v[36:39],a[0:3],v[60:63],v[36:39]
ds_read_b128 v[56:59],v2 offset:48128
v_mul_f32 v77,v29,v29
v_mfma_f32_16x16x32_bf16 v[40:43],a[4:7],v[60:63],v[40:43]
v_mul_f32 v78,v30,v30
v_mul_f32 v79,v31,v31
v_mfma_f32_16x16x32_bf16 v[36:39],a[8:11],v[64:67],v[36:39]
buffer_load_dwordx4 v1,s[36:39],s67 offen offset:3072 sc1 lds
v_mfma_f32_16x16x32_bf16 v[40:43],a[12:15],v[64:67],v[40:43]
s_add_u32 m0,s71,s88
ds_read_b128 v[60:63],v2 offset:40960
v_fma_f32 v76,v94,v76,v93
v_mfma_f32_16x16x32_bf16 v[36:39],a[16:19],v[68:71],v[36:39]
buffer_load_dword v10,s[36:39],s72 offen offset:0 sc1 lds
v_mfma_f32_16x16x32_bf16 v[40:43],a[20:23],v[68:71],v[40:43]
s_lshr_b32 s73,s48,0
ds_read_b128 v[64:67],v2 offset:41984
v_mul_f32 v76,v28,v76
v_mfma_f32_16x16x32_bf16 v[36:39],a[24:27],v[72:75],v[36:39]
s_lshr_b32 s69,s52,5
ds_read_b128 v[68:71],v2 offset:43008
v_fma_f32 v77,v94,v77,v93
v_mfma_f32_16x16x32_bf16 v[40:43],a[28:31],v[72:75],v[40:43]
s_waitcnt lgkmcnt(3)
v_exp_f32 v76,v76
v_mfma_f32_16x16x32_bf16 v[36:39],a[96:99],v[44:47],v[36:39]
s_waitcnt vmcnt(10)
ds_read_b128 v[72:75],v2 offset:44032
v_mul_f32 v77,v29,v77
v_mfma_f32_16x16x32_bf16 v[40:43],a[100:103],v[44:47],v[40:43]
s_barrier
v_mfma_f32_16x16x32_bf16 v[36:39],a[104:107],v[48:51],v[36:39]
s_cmp_lt_u32 s52,4194304
ds_read_b128 v[44:47],v2 offset:53248
ds_read_b32 v16,v7 offset:0
v_mfma_f32_16x16x32_bf16 v[40:43],a[108:111],v[48:51],v[40:43]
s_cselect_b32 s68,0xffffcfff,-1
v_exp_f32 v77,v77
v_mfma_f32_16x16x32_bf16 v[36:39],a[112:115],v[52:55],v[36:39]
s_and_b32 s68,s68,s69
ds_read_b128 v[48:51],v2 offset:54272
v_fma_f32 v76,v76,v95,v95
v_mfma_f32_16x16x32_bf16 v[40:43],a[116:119],v[52:55],v[40:43]
s_add_u32 m0,s78,256
v_rcp_f32 v76,v76
v_mfma_f32_16x16x32_bf16 v[36:39],a[120:123],v[56:59],v[36:39]
s_waitcnt lgkmcnt(1)
buffer_load_dword v6,s[36:39],s68 offen offset:0 sc1 lds
v_mfma_f32_16x16x32_bf16 v[40:43],a[124:127],v[56:59],v[40:43]
ds_read_b128 v[52:55],v2 offset:55296
v_fma_f32 v77,v77,v95,v95
v_mfma_f32_16x16x32_bf16 v[36:39],a[64:67],v[60:63],v[36:39]
s_xor_b32 s69,s63,65536
v_cmp_eq_u32 vcc,s60,v16
v_rcp_f32 v77,v77
v_fma_f32 v78,v94,v78,v93
v_mfma_f32_16x16x32_bf16 v[40:43],a[68:71],v[60:63],v[40:43]
s_cmp_ge_u32 s67,s59
ds_read_b128 v[56:59],v2 offset:56320
v_mul_f32 v78,v30,v78
v_mfma_f32_16x16x32_bf16 v[36:39],a[72:75],v[64:67],v[36:39]
s_cselect_b32 s62,s69,s64
ds_read_b128 v[60:63],v2 offset:49152
v_fma_f32 v79,v94,v79,v93
v_mfma_f32_16x16x32_bf16 v[40:43],a[76:79],v[64:67],v[40:43]
s_cselect_b32 s86,s50,s51
s_cselect_b32 s87,vcc_lo,vcc_hi
s_cmp_eq_u32 s87,-1
s_mov_b32 m0,s62
s_cselect_b32 s66,s86,s59
v_mfma_f32_16x16x32_bf16 v[36:39],a[80:83],v[68:71],v[36:39]
buffer_load_dwordx4 v0,s[36:39],s66 offen offset:0 sc1 lds
v_mfma_f32_16x16x32_bf16 v[40:43],a[84:87],v[68:71],v[40:43]
s_xor_b32 s64,s64,65536
ds_read_b128 v[64:67],v2 offset:50176
ds_read_b128 v[96:99],v8 offset:8192
v_mfma_f32_16x16x32_bf16 v[36:39],a[88:91],v[72:75],v[36:39]
s_add_u32 s74,s53,s55
ds_read_b128 v[68:71],v2 offset:51200
v_mul_f32 v79,v31,v79
v_mfma_f32_16x16x32_bf16 v[40:43],a[92:95],v[72:75],v[40:43]
s_waitcnt lgkmcnt(4)
v_exp_f32 v78,v78
v_mfma_f32_16x16x32_bf16 v[36:39],a[160:163],v[44:47],v[36:39]
buffer_load_dwordx4 v0,s[36:39],s66 offen offset:1024 sc1 lds
v_mfma_f32_16x16x32_bf16 v[40:43],a[164:167],v[44:47],v[40:43]
s_cmp_eq_u32 s74,s56
ds_read_b128 v[72:75],v2 offset:52224
v_fma_f32 v78,v78,v95,v95
v_mfma_f32_16x16x32_bf16 v[36:39],a[168:171],v[48:51],v[36:39]
s_cselect_b32 s74,s57,s74
ds_read_b128 v[44:47],v2 offset:61440
v_mul_f32 v28,v28,v76
v_mfma_f32_16x16x32_bf16 v[40:43],a[172:175],v[48:51],v[40:43]
s_waitcnt lgkmcnt(3)
v_exp_f32 v79,v79
v_mfma_f32_16x16x32_bf16 v[36:39],a[176:179],v[52:55],v[36:39]
buffer_load_dwordx4 v0,s[36:39],s66 offen offset:2048 sc1 lds
v_mfma_f32_16x16x32_bf16 v[40:43],a[180:183],v[52:55],v[40:43]
s_cmp_eq_u32 s74,s58
ds_read_b128 v[48:51],v2 offset:62464
v_fma_f32 v79,v79,v95,v95
v_mfma_f32_16x16x32_bf16 v[36:39],a[184:187],v[56:59],v[36:39]
s_cselect_b32 s74,s59,s74
ds_read_b128 v[52:55],v2 offset:63488
v_mul_f32 v29,v29,v77
v_mfma_f32_16x16x32_bf16 v[40:43],a[188:191],v[56:59],v[40:43]
s_waitcnt lgkmcnt(3)
v_rcp_f32 v78,v78
v_mfma_f32_16x16x32_bf16 v[36:39],a[128:131],v[60:63],v[36:39]
buffer_load_dwordx4 v0,s[36:39],s66 offen offset:3072 sc1 lds
v_mfma_f32_16x16x32_bf16 v[40:43],a[132:135],v[60:63],v[40:43]
s_add_u32 m0,s62,16384
ds_read_b128 v[56:59],v2 offset:64512
v_mul_f32 v30,v30,v78
v_mfma_f32_16x16x32_bf16 v[36:39],a[136:139],v[64:67],v[36:39]
s_mov_b64 s[48:49],s[50:51]
ds_read_b128 v[60:63],v2 offset:57344
v_cvt_pk_bf16_f32 v84,v28,v29
v_mfma_f32_16x16x32_bf16 v[40:43],a[140:143],v[64:67],v[40:43]
s_mov_b64 s[50:51],s[52:53]
v_rcp_f32 v79,v79
v_mfma_f32_16x16x32_bf16 v[36:39],a[144:147],v[68:71],v[36:39]
buffer_load_dwordx4 v1,s[36:39],s66 offen offset:0 sc1 lds
v_mfma_f32_16x16x32_bf16 v[40:43],a[148:151],v[68:71],v[40:43]
s_mov_b32 s52,s74
ds_read_b128 v[64:67],v2 offset:58368
v_mul_f32 v31,v31,v79
v_mfma_f32_16x16x32_bf16 v[36:39],a[152:155],v[72:75],v[36:39]
s_cmp_lt_u32 s67,s59
ds_read_b128 v[68:71],v2 offset:59392
v_cvt_pk_bf16_f32 v85,v30,v31
v_mfma_f32_16x16x32_bf16 v[40:43],a[156:159],v[72:75],v[40:43]
s_waitcnt lgkmcnt(3)
v_fma_f32 v32,-v92,v24,v32
v_fma_f32 v33,-v92,v25,v33
v_mfma_f32_16x16x32_bf16 v[36:39],a[224:227],v[44:47],v[36:39]
s_waitcnt vmcnt(12)
ds_read_b128 v[72:75],v2 offset:60416
v_xor_b32 v2,65536,v2
v_mfma_f32_16x16x32_bf16 v[40:43],a[228:231],v[44:47],v[40:43]
s_barrier
v_mfma_f32_16x16x32_bf16 v[36:39],a[232:235],v[48:51],v[36:39]
ds_read_b128 v[44:47],v2 offset:4096
v_fma_f32 v34,-v92,v26,v34
v_mfma_f32_16x16x32_bf16 v[40:43],a[236:239],v[48:51],v[40:43]
v_fma_f32 v35,-v92,v27,v35
v_mul_f32 v80,v32,v32
v_mfma_f32_16x16x32_bf16 v[36:39],a[240:243],v[52:55],v[36:39]
ds_read_b128 v[48:51],v2 offset:5120
v_mul_f32 v81,v33,v33
v_mfma_f32_16x16x32_bf16 v[40:43],a[244:247],v[52:55],v[40:43]
v_mul_f32 v82,v34,v34
v_mul_f32 v83,v35,v35
v_mfma_f32_16x16x32_bf16 v[36:39],a[248:251],v[56:59],v[36:39]
ds_read_b128 v[52:55],v2 offset:6144
v_fma_f32 v80,v94,v80,v93
v_mfma_f32_16x16x32_bf16 v[40:43],a[252:255],v[56:59],v[40:43]
s_waitcnt lgkmcnt(3)
v_mul_f32 v80,v32,v80
v_fma_f32 v81,v94,v81,v93
v_mfma_f32_16x16x32_bf16 v[36:39],a[192:195],v[60:63],v[36:39]
ds_read_b128 v[56:59],v2 offset:7168
v_mul_f32 v81,v33,v81
v_mfma_f32_16x16x32_bf16 v[40:43],a[196:199],v[60:63],v[40:43]
v_fma_f32 v82,v94,v82,v93
v_fma_f32 v83,v94,v83,v93
v_mul_f32 v82,v34,v82
v_mul_f32 v83,v35,v83
v_exp_f32 v80,v80
v_exp_f32 v81,v81
v_exp_f32 v82,v82
v_exp_f32 v83,v83
v_mfma_f32_16x16x32_bf16 v[36:39],a[200:203],v[64:67],v[36:39]
v_fma_f32 v80,v80,v95,v95
v_fma_f32 v81,v81,v95,v95
v_fma_f32 v82,v82,v95,v95
v_fma_f32 v83,v83,v95,v95
v_rcp_f32 v80,v80
v_rcp_f32 v81,v81)"
R"(
v_rcp_f32 v82,v82
v_rcp_f32 v83,v83
v_mfma_f32_16x16x32_bf16 v[40:43],a[204:207],v[64:67],v[40:43]
ds_read_b128 v[60:63],v2 offset:0
v_mul_f32 v32,v32,v80
v_mul_f32 v33,v33,v81
v_mul_f32 v34,v34,v82
v_mul_f32 v35,v35,v83
v_cvt_pk_bf16_f32 v86,v32,v33
v_cvt_pk_bf16_f32 v87,v34,v35
v_mfma_f32_16x16x32_bf16 v[36:39],a[208:211],v[68:71],v[36:39]
buffer_store_dwordx4 v[84:87],v3,s[36:39],s73 offen offset:0 sc0 sc1
ds_read_b128 v[64:67],v2 offset:1024
v_xor_b32 v8,16384,v8
v_mfma_f32_16x16x32_bf16 v[40:43],a[212:215],v[68:71],v[40:43]
v_mfma_f32_16x16x32_bf16 v[36:39],a[216:219],v[72:75],v[36:39]
s_waitcnt lgkmcnt(2)
ds_read_b128 v[68:71],v2 offset:2048
v_mfma_f32_16x16x32_bf16 v[40:43],a[220:223],v[72:75],v[40:43]
s_cbranch_scc1 Lg1_d0w1_%=
s_branch Lg1_slow0_%=
Lg1_d0w2_%=:
v_mfma_f32_16x16x32_bf16 v[28:31],a[64:67],v[44:47],0
s_add_u32 m0,s62,16384
ds_read_b128 v[72:75],v2 offset:15360
s_lshr_b32 s72,s66,6
v_mfma_f32_16x16x32_bf16 v[32:35],a[68:71],v[44:47],0
s_lshr_b32 s71,s62,2
s_cmp_lt_u32 s66,4194304
s_cselect_b32 s72,s59,s72
v_mfma_f32_16x16x32_bf16 v[28:31],a[72:75],v[48:51],v[28:31]
ds_read_b128 v[44:47],v2 offset:0
v_mfma_f32_16x16x32_bf16 v[32:35],a[76:79],v[48:51],v[32:35]
buffer_load_dwordx4 v1,s[36:39],s66 offen offset:1024 sc1 lds
v_mfma_f32_16x16x32_bf16 v[28:31],a[80:83],v[52:55],v[28:31]
ds_read_b128 v[48:51],v2 offset:1024
v_fma_f32 v36,-v96,v20,v36
v_mfma_f32_16x16x32_bf16 v[32:35],a[84:87],v[52:55],v[32:35]
v_fma_f32 v37,-v96,v21,v37
v_fma_f32 v38,-v96,v22,v38
v_mfma_f32_16x16x32_bf16 v[28:31],a[88:91],v[56:59],v[28:31]
ds_read_b128 v[52:55],v2 offset:2048
v_fma_f32 v39,-v96,v23,v39
v_mfma_f32_16x16x32_bf16 v[32:35],a[92:95],v[56:59],v[32:35]
s_waitcnt lgkmcnt(3)
buffer_load_dwordx4 v1,s[36:39],s66 offen offset:2048 sc1 lds
v_mfma_f32_16x16x32_bf16 v[28:31],a[96:99],v[60:63],v[28:31]
ds_read_b128 v[56:59],v2 offset:3072
v_mul_f32 v76,v36,v36
v_mfma_f32_16x16x32_bf16 v[32:35],a[100:103],v[60:63],v[32:35]
v_mul_f32 v77,v37,v37
v_mul_f32 v78,v38,v38
v_mfma_f32_16x16x32_bf16 v[28:31],a[104:107],v[64:67],v[28:31]
ds_read_b128 v[60:63],v2 offset:4096
v_mul_f32 v79,v39,v39
v_mfma_f32_16x16x32_bf16 v[32:35],a[108:111],v[64:67],v[32:35]
buffer_load_dwordx4 v1,s[36:39],s66 offen offset:3072 sc1 lds
v_mfma_f32_16x16x32_bf16 v[28:31],a[112:115],v[68:71],v[28:31]
s_add_u32 m0,s71,s88
ds_read_b128 v[64:67],v2 offset:5120
v_fma_f32 v76,v98,v76,v97
v_mfma_f32_16x16x32_bf16 v[32:35],a[116:119],v[68:71],v[32:35]
buffer_load_dword v10,s[36:39],s72 offen offset:0 sc1 lds
v_mfma_f32_16x16x32_bf16 v[28:31],a[120:123],v[72:75],v[28:31]
s_lshr_b32 s73,s47,0
ds_read_b128 v[68:71],v2 offset:6144
v_mul_f32 v76,v36,v76
v_mfma_f32_16x16x32_bf16 v[32:35],a[124:127],v[72:75],v[32:35]
s_waitcnt lgkmcnt(3)
v_exp_f32 v76,v76
v_mfma_f32_16x16x32_bf16 v[28:31],a[0:3],v[44:47],v[28:31]
s_waitcnt vmcnt(10)
ds_read_b128 v[72:75],v2 offset:7168
v_fma_f32 v76,v76,v99,v99
v_mfma_f32_16x16x32_bf16 v[32:35],a[4:7],v[44:47],v[32:35]
s_barrier
v_mfma_f32_16x16x32_bf16 v[28:31],a[8:11],v[48:51],v[28:31]
s_xor_b32 s69,s64,65536
ds_read_b128 v[44:47],v2 offset:24576
ds_read_b32 v16,v7 offset:256
v_mfma_f32_16x16x32_bf16 v[32:35],a[12:15],v[48:51],v[32:35]
s_cmp_ge_u32 s66,s59
v_rcp_f32 v76,v76
v_mfma_f32_16x16x32_bf16 v[28:31],a[16:19],v[52:55],v[28:31]
s_cselect_b32 s62,s69,s63
ds_read_b128 v[48:51],v2 offset:25600
v_fma_f32 v77,v98,v77,v97
v_mfma_f32_16x16x32_bf16 v[32:35],a[20:23],v[52:55],v[32:35]
s_cselect_b32 s86,s49,s50
v_mul_f32 v77,v37,v77
v_fma_f32 v78,v98,v78,v97
v_mfma_f32_16x16x32_bf16 v[28:31],a[24:27],v[56:59],v[28:31]
s_waitcnt lgkmcnt(1)
ds_read_b128 v[52:55],v2 offset:26624
v_mul_f32 v78,v38,v78
v_mfma_f32_16x16x32_bf16 v[32:35],a[28:31],v[56:59],v[32:35]
v_cmp_eq_u32 vcc,s60,v16
v_exp_f32 v77,v77
v_fma_f32 v79,v98,v79,v97
v_mfma_f32_16x16x32_bf16 v[28:31],a[32:35],v[60:63],v[28:31]
s_mov_b32 m0,s62
ds_read_b128 v[56:59],v2 offset:27648
v_mul_f32 v79,v39,v79
v_mfma_f32_16x16x32_bf16 v[32:35],a[36:39],v[60:63],v[32:35]
s_cselect_b32 s87,vcc_lo,vcc_hi
v_exp_f32 v78,v78
v_mfma_f32_16x16x32_bf16 v[28:31],a[40:43],v[64:67],v[28:31]
s_cmp_eq_u32 s87,-1
ds_read_b128 v[60:63],v2 offset:28672
v_fma_f32 v77,v77,v99,v99
v_mfma_f32_16x16x32_bf16 v[32:35],a[44:47],v[64:67],v[32:35]
s_cselect_b32 s67,s86,s59
v_exp_f32 v79,v79
v_mfma_f32_16x16x32_bf16 v[28:31],a[48:51],v[68:71],v[28:31]
s_xor_b32 s63,s63,65536
ds_read_b128 v[64:67],v2 offset:29696
ds_read_b128 v[92:95],v8 offset:0
v_mfma_f32_16x16x32_bf16 v[32:35],a[52:55],v[68:71],v[32:35]
buffer_load_dwordx4 v0,s[36:39],s67 offen offset:0 sc1 lds
v_mfma_f32_16x16x32_bf16 v[28:31],a[56:59],v[72:75],v[28:31]
s_add_u32 s53,s52,s55
ds_read_b128 v[68:71],v2 offset:30720
v_fma_f32 v78,v78,v99,v99
v_mfma_f32_16x16x32_bf16 v[32:35],a[60:63],v[72:75],v[32:35]
s_waitcnt lgkmcnt(4)
v_rcp_f32 v77,v77
v_mfma_f32_16x16x32_bf16 v[28:31],a[192:195],v[44:47],v[28:31]
s_cmp_eq_u32 s53,s56
ds_read_b128 v[72:75],v2 offset:31744
v_fma_f32 v79,v79,v99,v99
v_mfma_f32_16x16x32_bf16 v[32:35],a[196:199],v[44:47],v[32:35]
buffer_load_dwordx4 v0,s[36:39],s67 offen offset:1024 sc1 lds
v_mfma_f32_16x16x32_bf16 v[28:31],a[200:203],v[48:51],v[28:31]
s_waitcnt lgkmcnt(2)
ds_read_b128 v[44:47],v2 offset:16384
v_mul_f32 v36,v36,v76
v_mfma_f32_16x16x32_bf16 v[32:35],a[204:207],v[48:51],v[32:35]
s_cselect_b32 s53,s57,s53
v_rcp_f32 v78,v78
v_mfma_f32_16x16x32_bf16 v[28:31],a[208:211],v[52:55],v[28:31]
s_cmp_eq_u32 s53,s58
ds_read_b128 v[48:51],v2 offset:17408
v_mul_f32 v37,v37,v77
v_mfma_f32_16x16x32_bf16 v[32:35],a[212:215],v[52:55],v[32:35]
buffer_load_dwordx4 v0,s[36:39],s67 offen offset:2048 sc1 lds
v_mfma_f32_16x16x32_bf16 v[28:31],a[216:219],v[56:59],v[28:31]
s_cselect_b32 s53,s59,s53
ds_read_b128 v[52:55],v2 offset:18432
v_mul_f32 v38,v38,v78
v_mfma_f32_16x16x32_bf16 v[32:35],a[220:223],v[56:59],v[32:35]
s_waitcnt lgkmcnt(3)
v_rcp_f32 v79,v79
v_mfma_f32_16x16x32_bf16 v[28:31],a[224:227],v[60:63],v[28:31]
ds_read_b128 v[56:59],v2 offset:19456
v_mul_f32 v39,v39,v79
v_mfma_f32_16x16x32_bf16 v[32:35],a[228:231],v[60:63],v[32:35]
buffer_load_dwordx4 v0,s[36:39],s67 offen offset:3072 sc1 lds
v_mfma_f32_16x16x32_bf16 v[28:31],a[232:235],v[64:67],v[28:31]
s_add_u32 m0,s62,16384
ds_read_b128 v[60:63],v2 offset:20480
v_cvt_pk_bf16_f32 v84,v36,v37
v_mfma_f32_16x16x32_bf16 v[32:35],a[236:239],v[64:67],v[32:35]
s_cmp_ge_u32 s66,s59
v_cvt_pk_bf16_f32 v85,v38,v39
v_fma_f32 v40,-v96,v24,v40
v_mfma_f32_16x16x32_bf16 v[28:31],a[240:243],v[68:71],v[28:31]
ds_read_b128 v[64:67],v2 offset:21504
v_fma_f32 v41,-v96,v25,v41
v_mfma_f32_16x16x32_bf16 v[32:35],a[244:247],v[68:71],v[32:35]
buffer_load_dwordx4 v1,s[36:39],s67 offen offset:0 sc1 lds
v_mfma_f32_16x16x32_bf16 v[28:31],a[248:251],v[72:75],v[28:31]
ds_read_b128 v[68:71],v2 offset:22528
v_fma_f32 v42,-v96,v26,v42
v_mfma_f32_16x16x32_bf16 v[32:35],a[252:255],v[72:75],v[32:35]
s_waitcnt lgkmcnt(3)
v_fma_f32 v43,-v96,v27,v43
v_mul_f32 v80,v40,v40
v_mfma_f32_16x16x32_bf16 v[28:31],a[128:131],v[44:47],v[28:31]
s_waitcnt vmcnt(11)
ds_read_b128 v[72:75],v2 offset:23552
v_mul_f32 v81,v41,v41
v_mfma_f32_16x16x32_bf16 v[32:35],a[132:135],v[44:47],v[32:35]
s_barrier
v_mfma_f32_16x16x32_bf16 v[28:31],a[136:139],v[48:51],v[28:31]
ds_read_b128 v[44:47],v2 offset:40960
v_mul_f32 v82,v42,v42
v_mfma_f32_16x16x32_bf16 v[32:35],a[140:143],v[48:51],v[32:35]
v_mul_f32 v83,v43,v43
v_fma_f32 v80,v98,v80,v97
v_mfma_f32_16x16x32_bf16 v[28:31],a[144:147],v[52:55],v[28:31]
ds_read_b128 v[48:51],v2 offset:41984
v_mul_f32 v80,v40,v80
v_mfma_f32_16x16x32_bf16 v[32:35],a[148:151],v[52:55],v[32:35]
v_exp_f32 v80,v80
v_mfma_f32_16x16x32_bf16 v[28:31],a[152:155],v[56:59],v[28:31]
ds_read_b128 v[52:55],v2 offset:43008
v_fma_f32 v80,v80,v99,v99
v_mfma_f32_16x16x32_bf16 v[32:35],a[156:159],v[56:59],v[32:35]
s_waitcnt lgkmcnt(3)
v_rcp_f32 v80,v80)"
R"(
v_mfma_f32_16x16x32_bf16 v[28:31],a[160:163],v[60:63],v[28:31]
ds_read_b128 v[56:59],v2 offset:44032
v_fma_f32 v81,v98,v81,v97
v_mfma_f32_16x16x32_bf16 v[32:35],a[164:167],v[60:63],v[32:35]
v_mul_f32 v81,v41,v81
v_fma_f32 v82,v98,v82,v97
v_mfma_f32_16x16x32_bf16 v[28:31],a[168:171],v[64:67],v[28:31]
v_fma_f32 v83,v98,v83,v97
v_mul_f32 v82,v42,v82
v_mul_f32 v83,v43,v83
v_exp_f32 v81,v81
v_exp_f32 v82,v82
v_exp_f32 v83,v83
v_mfma_f32_16x16x32_bf16 v[32:35],a[172:175],v[64:67],v[32:35]
ds_read_b128 v[60:63],v2 offset:45056
v_fma_f32 v81,v81,v99,v99
v_fma_f32 v82,v82,v99,v99
v_fma_f32 v83,v83,v99,v99
v_rcp_f32 v81,v81
v_rcp_f32 v82,v82
v_rcp_f32 v83,v83
v_mfma_f32_16x16x32_bf16 v[28:31],a[176:179],v[68:71],v[28:31]
v_mul_f32 v40,v40,v80
v_mul_f32 v41,v41,v81
v_mul_f32 v42,v42,v82
v_mul_f32 v43,v43,v83
v_cvt_pk_bf16_f32 v86,v40,v41
v_cvt_pk_bf16_f32 v87,v42,v43
v_mfma_f32_16x16x32_bf16 v[32:35],a[180:183],v[68:71],v[32:35]
ds_read_b128 v[64:67],v2 offset:46080
buffer_store_dwordx4 v[84:87],v3,s[36:39],s73 offen offset:0 sc0 sc1
v_mfma_f32_16x16x32_bf16 v[28:31],a[184:187],v[72:75],v[28:31]
s_waitcnt lgkmcnt(2)
ds_read_b128 v[68:71],v2 offset:47104
v_mfma_f32_16x16x32_bf16 v[32:35],a[188:191],v[72:75],v[32:35]
s_cbranch_scc1 Lg1_slow1_%=
Lg1_d1w2_%=:
v_mfma_f32_16x16x32_bf16 v[36:39],a[64:67],v[44:47],0
s_add_u32 m0,s62,16384
ds_read_b128 v[72:75],v2 offset:48128
s_lshr_b32 s72,s67,6
v_mfma_f32_16x16x32_bf16 v[40:43],a[68:71],v[44:47],0
s_lshr_b32 s71,s62,2
s_cmp_lt_u32 s67,4194304
s_cselect_b32 s72,s59,s72
v_mfma_f32_16x16x32_bf16 v[36:39],a[72:75],v[48:51],v[36:39]
s_mov_b64 s[44:45],s[46:47]
ds_read_b128 v[44:47],v2 offset:32768
s_mov_b64 s[46:47],s[48:49]
v_mfma_f32_16x16x32_bf16 v[40:43],a[76:79],v[48:51],v[40:43]
buffer_load_dwordx4 v1,s[36:39],s67 offen offset:1024 sc1 lds
v_mfma_f32_16x16x32_bf16 v[36:39],a[80:83],v[52:55],v[36:39]
ds_read_b128 v[48:51],v2 offset:33792
v_fma_f32 v28,-v92,v20,v28
v_mfma_f32_16x16x32_bf16 v[40:43],a[84:87],v[52:55],v[40:43]
v_fma_f32 v29,-v92,v21,v29
v_fma_f32 v30,-v92,v22,v30
v_mfma_f32_16x16x32_bf16 v[36:39],a[88:91],v[56:59],v[36:39]
ds_read_b128 v[52:55],v2 offset:34816
v_fma_f32 v31,-v92,v23,v31
v_mfma_f32_16x16x32_bf16 v[40:43],a[92:95],v[56:59],v[40:43]
s_waitcnt lgkmcnt(3)
buffer_load_dwordx4 v1,s[36:39],s67 offen offset:2048 sc1 lds
v_mfma_f32_16x16x32_bf16 v[36:39],a[96:99],v[60:63],v[36:39]
ds_read_b128 v[56:59],v2 offset:35840
v_mul_f32 v76,v28,v28
v_mfma_f32_16x16x32_bf16 v[40:43],a[100:103],v[60:63],v[40:43]
v_mul_f32 v77,v29,v29
v_mul_f32 v78,v30,v30
v_mfma_f32_16x16x32_bf16 v[36:39],a[104:107],v[64:67],v[36:39]
ds_read_b128 v[60:63],v2 offset:36864
v_mul_f32 v79,v31,v31
v_mfma_f32_16x16x32_bf16 v[40:43],a[108:111],v[64:67],v[40:43]
buffer_load_dwordx4 v1,s[36:39],s67 offen offset:3072 sc1 lds
v_mfma_f32_16x16x32_bf16 v[36:39],a[112:115],v[68:71],v[36:39]
s_add_u32 m0,s71,s88
ds_read_b128 v[64:67],v2 offset:37888
v_fma_f32 v76,v94,v76,v93
v_mfma_f32_16x16x32_bf16 v[40:43],a[116:119],v[68:71],v[40:43]
buffer_load_dword v10,s[36:39],s72 offen offset:0 sc1 lds
v_mfma_f32_16x16x32_bf16 v[36:39],a[120:123],v[72:75],v[36:39]
s_lshr_b32 s73,s48,0
ds_read_b128 v[68:71],v2 offset:38912
v_mul_f32 v76,v28,v76
v_mfma_f32_16x16x32_bf16 v[40:43],a[124:127],v[72:75],v[40:43]
s_waitcnt lgkmcnt(3)
v_exp_f32 v76,v76
v_mfma_f32_16x16x32_bf16 v[36:39],a[0:3],v[44:47],v[36:39]
s_waitcnt vmcnt(10)
ds_read_b128 v[72:75],v2 offset:39936
v_fma_f32 v76,v76,v95,v95
v_mfma_f32_16x16x32_bf16 v[40:43],a[4:7],v[44:47],v[40:43]
s_barrier
v_mfma_f32_16x16x32_bf16 v[36:39],a[8:11],v[48:51],v[36:39]
s_xor_b32 s69,s63,65536
ds_read_b128 v[44:47],v2 offset:57344
ds_read_b32 v16,v7 offset:0
v_mfma_f32_16x16x32_bf16 v[40:43],a[12:15],v[48:51],v[40:43]
s_cmp_ge_u32 s67,s59
v_rcp_f32 v76,v76
v_mfma_f32_16x16x32_bf16 v[36:39],a[16:19],v[52:55],v[36:39]
s_cselect_b32 s62,s69,s64
ds_read_b128 v[48:51],v2 offset:58368
v_fma_f32 v77,v94,v77,v93
v_mfma_f32_16x16x32_bf16 v[40:43],a[20:23],v[52:55],v[40:43]
s_cselect_b32 s86,s50,s51
v_mul_f32 v77,v29,v77
v_fma_f32 v78,v94,v78,v93
v_mfma_f32_16x16x32_bf16 v[36:39],a[24:27],v[56:59],v[36:39]
s_waitcnt lgkmcnt(1)
ds_read_b128 v[52:55],v2 offset:59392
v_mul_f32 v78,v30,v78
v_mfma_f32_16x16x32_bf16 v[40:43],a[28:31],v[56:59],v[40:43]
v_cmp_eq_u32 vcc,s60,v16
v_exp_f32 v77,v77
v_fma_f32 v79,v94,v79,v93
v_mfma_f32_16x16x32_bf16 v[36:39],a[32:35],v[60:63],v[36:39]
s_mov_b32 m0,s62
ds_read_b128 v[56:59],v2 offset:60416
v_mul_f32 v79,v31,v79
v_mfma_f32_16x16x32_bf16 v[40:43],a[36:39],v[60:63],v[40:43]
s_cselect_b32 s87,vcc_lo,vcc_hi
v_exp_f32 v78,v78
v_mfma_f32_16x16x32_bf16 v[36:39],a[40:43],v[64:67],v[36:39]
s_cmp_eq_u32 s87,-1
ds_read_b128 v[60:63],v2 offset:61440
v_fma_f32 v77,v77,v95,v95
v_mfma_f32_16x16x32_bf16 v[40:43],a[44:47],v[64:67],v[40:43]
s_cselect_b32 s66,s86,s59
v_exp_f32 v79,v79
v_mfma_f32_16x16x32_bf16 v[36:39],a[48:51],v[68:71],v[36:39]
s_xor_b32 s64,s64,65536
ds_read_b128 v[64:67],v2 offset:62464
ds_read_b128 v[96:99],v8 offset:8192
v_mfma_f32_16x16x32_bf16 v[40:43],a[52:55],v[68:71],v[40:43]
buffer_load_dwordx4 v0,s[36:39],s66 offen offset:0 sc1 lds
v_mfma_f32_16x16x32_bf16 v[36:39],a[56:59],v[72:75],v[36:39]
s_add_u32 s74,s53,s55
ds_read_b128 v[68:71],v2 offset:63488
v_fma_f32 v78,v78,v95,v95
v_mfma_f32_16x16x32_bf16 v[40:43],a[60:63],v[72:75],v[40:43]
s_waitcnt lgkmcnt(4)
v_rcp_f32 v77,v77
v_mfma_f32_16x16x32_bf16 v[36:39],a[192:195],v[44:47],v[36:39]
s_cmp_eq_u32 s74,s56
ds_read_b128 v[72:75],v2 offset:64512
v_fma_f32 v79,v79,v95,v95
v_mfma_f32_16x16x32_bf16 v[40:43],a[196:199],v[44:47],v[40:43]
buffer_load_dwordx4 v0,s[36:39],s66 offen offset:1024 sc1 lds
v_mfma_f32_16x16x32_bf16 v[36:39],a[200:203],v[48:51],v[36:39]
s_waitcnt lgkmcnt(2)
ds_read_b128 v[44:47],v2 offset:49152
v_mul_f32 v28,v28,v76
v_mfma_f32_16x16x32_bf16 v[40:43],a[204:207],v[48:51],v[40:43]
s_cselect_b32 s74,s57,s74
v_rcp_f32 v78,v78
v_mfma_f32_16x16x32_bf16 v[36:39],a[208:211],v[52:55],v[36:39]
s_cmp_eq_u32 s74,s58
ds_read_b128 v[48:51],v2 offset:50176
v_mul_f32 v29,v29,v77
v_mfma_f32_16x16x32_bf16 v[40:43],a[212:215],v[52:55],v[40:43]
buffer_load_dwordx4 v0,s[36:39],s66 offen offset:2048 sc1 lds
v_mfma_f32_16x16x32_bf16 v[36:39],a[216:219],v[56:59],v[36:39]
s_cselect_b32 s74,s59,s74
ds_read_b128 v[52:55],v2 offset:51200
v_mul_f32 v30,v30,v78
v_mfma_f32_16x16x32_bf16 v[40:43],a[220:223],v[56:59],v[40:43]
s_waitcnt lgkmcnt(3)
v_rcp_f32 v79,v79
v_mfma_f32_16x16x32_bf16 v[36:39],a[224:227],v[60:63],v[36:39]
s_mov_b64 s[48:49],s[50:51]
ds_read_b128 v[56:59],v2 offset:52224
v_mul_f32 v31,v31,v79
v_mfma_f32_16x16x32_bf16 v[40:43],a[228:231],v[60:63],v[40:43]
buffer_load_dwordx4 v0,s[36:39],s66 offen offset:3072 sc1 lds
v_mfma_f32_16x16x32_bf16 v[36:39],a[232:235],v[64:67],v[36:39]
s_add_u32 m0,s62,16384
ds_read_b128 v[60:63],v2 offset:53248
v_cvt_pk_bf16_f32 v84,v28,v29
v_mfma_f32_16x16x32_bf16 v[40:43],a[236:239],v[64:67],v[40:43]
s_mov_b64 s[50:51],s[52:53]
v_cvt_pk_bf16_f32 v85,v30,v31
v_fma_f32 v32,-v92,v24,v32
v_mfma_f32_16x16x32_bf16 v[36:39],a[240:243],v[68:71],v[36:39]
s_mov_b32 s52,s74
ds_read_b128 v[64:67],v2 offset:54272
v_fma_f32 v33,-v92,v25,v33
v_mfma_f32_16x16x32_bf16 v[40:43],a[244:247],v[68:71],v[40:43]
buffer_load_dwordx4 v1,s[36:39],s66 offen offset:0 sc1 lds
v_mfma_f32_16x16x32_bf16 v[36:39],a[248:251],v[72:75],v[36:39]
s_cmp_lt_u32 s67,s59
ds_read_b128 v[68:71],v2 offset:55296
v_fma_f32 v34,-v92,v26,v34
v_mfma_f32_16x16x32_bf16 v[40:43],a[252:255],v[72:75],v[40:43]
s_waitcnt lgkmcnt(3)
v_fma_f32 v35,-v92,v27,v35
v_mul_f32 v80,v32,v32
v_mfma_f32_16x16x32_bf16 v[36:39],a[128:131],v[44:47],v[36:39]
s_waitcnt vmcnt(11)
ds_read_b128 v[72:75],v2 offset:56320)"
R"(
v_xor_b32 v2,65536,v2
v_mfma_f32_16x16x32_bf16 v[40:43],a[132:135],v[44:47],v[40:43]
s_barrier
v_mfma_f32_16x16x32_bf16 v[36:39],a[136:139],v[48:51],v[36:39]
ds_read_b128 v[44:47],v2 offset:8192
v_mul_f32 v81,v33,v33
v_mfma_f32_16x16x32_bf16 v[40:43],a[140:143],v[48:51],v[40:43]
v_mul_f32 v82,v34,v34
v_mul_f32 v83,v35,v35
v_mfma_f32_16x16x32_bf16 v[36:39],a[144:147],v[52:55],v[36:39]
ds_read_b128 v[48:51],v2 offset:9216
v_fma_f32 v80,v94,v80,v93
v_mfma_f32_16x16x32_bf16 v[40:43],a[148:151],v[52:55],v[40:43]
v_mul_f32 v80,v32,v80
v_fma_f32 v81,v94,v81,v93
v_mfma_f32_16x16x32_bf16 v[36:39],a[152:155],v[56:59],v[36:39]
ds_read_b128 v[52:55],v2 offset:10240
v_mul_f32 v81,v33,v81
v_mfma_f32_16x16x32_bf16 v[40:43],a[156:159],v[56:59],v[40:43]
s_waitcnt lgkmcnt(3)
v_exp_f32 v80,v80
v_mfma_f32_16x16x32_bf16 v[36:39],a[160:163],v[60:63],v[36:39]
ds_read_b128 v[56:59],v2 offset:11264
v_fma_f32 v80,v80,v95,v95
v_mfma_f32_16x16x32_bf16 v[40:43],a[164:167],v[60:63],v[40:43]
v_exp_f32 v81,v81
v_mfma_f32_16x16x32_bf16 v[36:39],a[168:171],v[64:67],v[36:39]
v_fma_f32 v82,v94,v82,v93
v_fma_f32 v83,v94,v83,v93
v_mul_f32 v82,v34,v82
v_mul_f32 v83,v35,v83
v_exp_f32 v82,v82
v_exp_f32 v83,v83
v_mfma_f32_16x16x32_bf16 v[40:43],a[172:175],v[64:67],v[40:43]
ds_read_b128 v[60:63],v2 offset:12288
v_fma_f32 v81,v81,v95,v95
v_fma_f32 v82,v82,v95,v95
v_fma_f32 v83,v83,v95,v95
v_rcp_f32 v80,v80
v_rcp_f32 v81,v81
v_rcp_f32 v82,v82
v_rcp_f32 v83,v83
v_mfma_f32_16x16x32_bf16 v[36:39],a[176:179],v[68:71],v[36:39]
v_mul_f32 v32,v32,v80
v_mul_f32 v33,v33,v81
v_mul_f32 v34,v34,v82
v_mul_f32 v35,v35,v83
v_cvt_pk_bf16_f32 v86,v32,v33
v_cvt_pk_bf16_f32 v87,v34,v35
v_mfma_f32_16x16x32_bf16 v[40:43],a[180:183],v[68:71],v[40:43]
ds_read_b128 v[64:67],v2 offset:13312
buffer_store_dwordx4 v[84:87],v3,s[36:39],s73 offen offset:0 sc0 sc1
v_xor_b32 v8,16384,v8
v_mfma_f32_16x16x32_bf16 v[36:39],a[184:187],v[72:75],v[36:39]
s_waitcnt lgkmcnt(2)
ds_read_b128 v[68:71],v2 offset:14336
v_mfma_f32_16x16x32_bf16 v[40:43],a[188:191],v[72:75],v[40:43]
s_cbranch_scc1 Lg1_d0w2_%=
s_branch Lg1_slow0_%=
Lg1_d0w3_%=:
v_mfma_f32_16x16x32_bf16 v[28:31],a[96:99],v[44:47],0
s_add_u32 m0,s62,16384
ds_read_b128 v[72:75],v2 offset:11264
s_lshr_b32 s72,s66,6
v_mfma_f32_16x16x32_bf16 v[32:35],a[100:103],v[44:47],0
s_lshr_b32 s71,s62,2
s_cmp_lt_u32 s66,4194304
s_cselect_b32 s72,s59,s72
v_mfma_f32_16x16x32_bf16 v[28:31],a[104:107],v[48:51],v[28:31]
ds_read_b128 v[44:47],v2 offset:4096
v_mfma_f32_16x16x32_bf16 v[32:35],a[108:111],v[48:51],v[32:35]
v_fma_f32 v36,-v96,v20,v36
v_fma_f32 v37,-v96,v21,v37
v_mfma_f32_16x16x32_bf16 v[28:31],a[112:115],v[52:55],v[28:31]
buffer_load_dwordx4 v1,s[36:39],s66 offen offset:1024 sc1 lds
v_mfma_f32_16x16x32_bf16 v[32:35],a[116:119],v[52:55],v[32:35]
ds_read_b128 v[48:51],v2 offset:5120
v_fma_f32 v38,-v96,v22,v38
v_mfma_f32_16x16x32_bf16 v[28:31],a[120:123],v[56:59],v[28:31]
ds_read_b128 v[52:55],v2 offset:6144
v_fma_f32 v39,-v96,v23,v39
v_mfma_f32_16x16x32_bf16 v[32:35],a[124:127],v[56:59],v[32:35]
s_waitcnt lgkmcnt(3)
v_mul_f32 v76,v36,v36
v_mul_f32 v77,v37,v37
v_mfma_f32_16x16x32_bf16 v[28:31],a[64:67],v[60:63],v[28:31]
buffer_load_dwordx4 v1,s[36:39],s66 offen offset:2048 sc1 lds
v_mfma_f32_16x16x32_bf16 v[32:35],a[68:71],v[60:63],v[32:35]
ds_read_b128 v[56:59],v2 offset:7168
v_mul_f32 v78,v38,v38
v_mfma_f32_16x16x32_bf16 v[28:31],a[72:75],v[64:67],v[28:31]
ds_read_b128 v[60:63],v2 offset:0
v_mul_f32 v79,v39,v39
v_mfma_f32_16x16x32_bf16 v[32:35],a[76:79],v[64:67],v[32:35]
v_fma_f32 v76,v98,v76,v97
v_mul_f32 v76,v36,v76
v_mfma_f32_16x16x32_bf16 v[28:31],a[80:83],v[68:71],v[28:31]
buffer_load_dwordx4 v1,s[36:39],s66 offen offset:3072 sc1 lds
v_mfma_f32_16x16x32_bf16 v[32:35],a[84:87],v[68:71],v[32:35]
s_add_u32 m0,s71,s88
ds_read_b128 v[64:67],v2 offset:1024
v_fma_f32 v77,v98,v77,v97
v_mfma_f32_16x16x32_bf16 v[28:31],a[88:91],v[72:75],v[28:31]
buffer_load_dword v10,s[36:39],s72 offen offset:0 sc1 lds
v_mfma_f32_16x16x32_bf16 v[32:35],a[92:95],v[72:75],v[32:35]
s_waitcnt lgkmcnt(2)
ds_read_b128 v[68:71],v2 offset:2048
v_mul_f32 v77,v37,v77
v_mfma_f32_16x16x32_bf16 v[28:31],a[32:35],v[44:47],v[28:31]
s_waitcnt vmcnt(11)
ds_read_b128 v[72:75],v2 offset:3072
v_fma_f32 v78,v98,v78,v97
v_mfma_f32_16x16x32_bf16 v[32:35],a[36:39],v[44:47],v[32:35]
s_barrier
v_mfma_f32_16x16x32_bf16 v[28:31],a[40:43],v[48:51],v[28:31]
s_lshr_b32 s73,s47,0
ds_read_b128 v[44:47],v2 offset:28672
ds_read_b32 v16,v7 offset:256
v_mfma_f32_16x16x32_bf16 v[32:35],a[44:47],v[48:51],v[32:35]
s_lshr_b32 s70,s45,5
v_exp_f32 v76,v76
v_mfma_f32_16x16x32_bf16 v[28:31],a[48:51],v[52:55],v[28:31]
buffer_store_dword v5,v4,s[36:39],s70 offen offset:0 sc0 sc1
ds_read_b128 v[48:51],v2 offset:29696
v_exp_f32 v77,v77
v_mfma_f32_16x16x32_bf16 v[32:35],a[52:55],v[52:55],v[32:35]
s_xor_b32 s69,s64,65536
v_mul_f32 v78,v38,v78
v_fma_f32 v76,v76,v99,v99
v_mfma_f32_16x16x32_bf16 v[28:31],a[56:59],v[56:59],v[28:31]
s_waitcnt lgkmcnt(1)
ds_read_b128 v[52:55],v2 offset:30720
v_fma_f32 v79,v98,v79,v97
v_mfma_f32_16x16x32_bf16 v[32:35],a[60:63],v[56:59],v[32:35]
v_cmp_eq_u32 vcc,s60,v16
v_exp_f32 v78,v78
v_mul_f32 v79,v39,v79
v_mfma_f32_16x16x32_bf16 v[28:31],a[0:3],v[60:63],v[28:31]
s_cmp_ge_u32 s66,s59
ds_read_b128 v[56:59],v2 offset:31744
v_fma_f32 v77,v77,v99,v99
v_mfma_f32_16x16x32_bf16 v[32:35],a[4:7],v[60:63],v[32:35]
s_cselect_b32 s62,s69,s63
v_exp_f32 v79,v79
v_mfma_f32_16x16x32_bf16 v[28:31],a[8:11],v[64:67],v[28:31]
s_cselect_b32 s86,s49,s50
ds_read_b128 v[60:63],v2 offset:24576
v_fma_f32 v78,v78,v99,v99
v_mfma_f32_16x16x32_bf16 v[32:35],a[12:15],v[64:67],v[32:35]
s_mov_b32 m0,s62
v_rcp_f32 v76,v76
v_mfma_f32_16x16x32_bf16 v[28:31],a[16:19],v[68:71],v[28:31]
s_cselect_b32 s87,vcc_lo,vcc_hi
ds_read_b128 v[64:67],v2 offset:25600
ds_read_b128 v[92:95],v8 offset:0
v_mfma_f32_16x16x32_bf16 v[32:35],a[20:23],v[68:71],v[32:35]
s_cmp_eq_u32 s87,-1
v_fma_f32 v79,v79,v99,v99
s_cselect_b32 s67,s86,s59
v_mfma_f32_16x16x32_bf16 v[28:31],a[24:27],v[72:75],v[28:31]
buffer_load_dwordx4 v0,s[36:39],s67 offen offset:0 sc1 lds
v_mfma_f32_16x16x32_bf16 v[32:35],a[28:31],v[72:75],v[32:35]
s_waitcnt lgkmcnt(3)
ds_read_b128 v[68:71],v2 offset:26624
v_mul_f32 v36,v36,v76
v_mfma_f32_16x16x32_bf16 v[28:31],a[224:227],v[44:47],v[28:31]
s_xor_b32 s63,s63,65536
ds_read_b128 v[72:75],v2 offset:27648
v_fma_f32 v40,-v96,v24,v40
v_mfma_f32_16x16x32_bf16 v[32:35],a[228:231],v[44:47],v[32:35]
s_add_u32 s53,s52,s55
v_rcp_f32 v77,v77
v_mfma_f32_16x16x32_bf16 v[28:31],a[232:235],v[48:51],v[28:31]
s_waitcnt lgkmcnt(2)
buffer_load_dwordx4 v0,s[36:39],s67 offen offset:1024 sc1 lds
v_mfma_f32_16x16x32_bf16 v[32:35],a[236:239],v[48:51],v[32:35]
s_cmp_eq_u32 s53,s56
ds_read_b128 v[44:47],v2 offset:20480
v_mul_f32 v37,v37,v77
v_mfma_f32_16x16x32_bf16 v[28:31],a[240:243],v[52:55],v[28:31]
s_cselect_b32 s53,s57,s53
ds_read_b128 v[48:51],v2 offset:21504
v_cvt_pk_bf16_f32 v84,v36,v37
v_mfma_f32_16x16x32_bf16 v[32:35],a[244:247],v[52:55],v[32:35]
s_cmp_eq_u32 s53,s58
v_rcp_f32 v78,v78
v_mfma_f32_16x16x32_bf16 v[28:31],a[248:251],v[56:59],v[28:31]
buffer_load_dwordx4 v0,s[36:39],s67 offen offset:2048 sc1 lds
v_mfma_f32_16x16x32_bf16 v[32:35],a[252:255],v[56:59],v[32:35]
s_waitcnt lgkmcnt(2)
ds_read_b128 v[52:55],v2 offset:22528
v_mul_f32 v38,v38,v78
v_mfma_f32_16x16x32_bf16 v[28:31],a[192:195],v[60:63],v[28:31]
s_cselect_b32 s53,s59,s53
ds_read_b128 v[56:59],v2 offset:23552
v_fma_f32 v41,-v96,v25,v41
v_mfma_f32_16x16x32_bf16 v[32:35],a[196:199],v[60:63],v[32:35]
v_rcp_f32 v79,v79
v_mfma_f32_16x16x32_bf16 v[28:31],a[200:203],v[64:67],v[28:31]
buffer_load_dwordx4 v0,s[36:39],s67 offen offset:3072 sc1 lds)"
R"(
v_mfma_f32_16x16x32_bf16 v[32:35],a[204:207],v[64:67],v[32:35]
s_add_u32 m0,s62,16384
ds_read_b128 v[60:63],v2 offset:16384
v_mul_f32 v39,v39,v79
v_mfma_f32_16x16x32_bf16 v[28:31],a[208:211],v[68:71],v[28:31]
s_cmp_ge_u32 s66,s59
ds_read_b128 v[64:67],v2 offset:17408
v_cvt_pk_bf16_f32 v85,v38,v39
v_mfma_f32_16x16x32_bf16 v[32:35],a[212:215],v[68:71],v[32:35]
v_fma_f32 v42,-v96,v26,v42
v_fma_f32 v43,-v96,v27,v43
v_mfma_f32_16x16x32_bf16 v[28:31],a[216:219],v[72:75],v[28:31]
buffer_load_dwordx4 v1,s[36:39],s67 offen offset:0 sc1 lds
v_mfma_f32_16x16x32_bf16 v[32:35],a[220:223],v[72:75],v[32:35]
s_waitcnt lgkmcnt(2)
ds_read_b128 v[68:71],v2 offset:18432
v_mul_f32 v80,v40,v40
v_mfma_f32_16x16x32_bf16 v[28:31],a[160:163],v[44:47],v[28:31]
s_waitcnt vmcnt(12)
ds_read_b128 v[72:75],v2 offset:19456
v_mul_f32 v81,v41,v41
v_mfma_f32_16x16x32_bf16 v[32:35],a[164:167],v[44:47],v[32:35]
s_barrier
v_mfma_f32_16x16x32_bf16 v[28:31],a[168:171],v[48:51],v[28:31]
ds_read_b128 v[44:47],v2 offset:45056
v_mul_f32 v82,v42,v42
v_mfma_f32_16x16x32_bf16 v[32:35],a[172:175],v[48:51],v[32:35]
v_mul_f32 v83,v43,v43
v_fma_f32 v80,v98,v80,v97
v_mfma_f32_16x16x32_bf16 v[28:31],a[176:179],v[52:55],v[28:31]
ds_read_b128 v[48:51],v2 offset:46080
v_mul_f32 v80,v40,v80
v_mfma_f32_16x16x32_bf16 v[32:35],a[180:183],v[52:55],v[32:35]
v_exp_f32 v80,v80
v_mfma_f32_16x16x32_bf16 v[28:31],a[184:187],v[56:59],v[28:31]
ds_read_b128 v[52:55],v2 offset:47104
v_fma_f32 v80,v80,v99,v99
v_mfma_f32_16x16x32_bf16 v[32:35],a[188:191],v[56:59],v[32:35]
s_waitcnt lgkmcnt(3)
v_rcp_f32 v80,v80
v_mfma_f32_16x16x32_bf16 v[28:31],a[128:131],v[60:63],v[28:31]
ds_read_b128 v[56:59],v2 offset:48128
v_fma_f32 v81,v98,v81,v97
v_mfma_f32_16x16x32_bf16 v[32:35],a[132:135],v[60:63],v[32:35]
v_mul_f32 v81,v41,v81
v_fma_f32 v82,v98,v82,v97
v_mfma_f32_16x16x32_bf16 v[28:31],a[136:139],v[64:67],v[28:31]
ds_read_b128 v[60:63],v2 offset:40960
v_mul_f32 v82,v42,v82
v_mfma_f32_16x16x32_bf16 v[32:35],a[140:143],v[64:67],v[32:35]
v_fma_f32 v83,v98,v83,v97
v_mul_f32 v83,v43,v83
v_exp_f32 v81,v81
v_exp_f32 v82,v82
v_exp_f32 v83,v83
v_mfma_f32_16x16x32_bf16 v[28:31],a[144:147],v[68:71],v[28:31]
v_fma_f32 v81,v81,v99,v99
v_fma_f32 v82,v82,v99,v99
v_fma_f32 v83,v83,v99,v99
v_rcp_f32 v81,v81
v_rcp_f32 v82,v82
v_rcp_f32 v83,v83
v_mfma_f32_16x16x32_bf16 v[32:35],a[148:151],v[68:71],v[32:35]
ds_read_b128 v[64:67],v2 offset:41984
v_mul_f32 v40,v40,v80
v_mul_f32 v41,v41,v81
v_mul_f32 v42,v42,v82
v_mul_f32 v43,v43,v83
v_cvt_pk_bf16_f32 v86,v40,v41
v_cvt_pk_bf16_f32 v87,v42,v43
v_mfma_f32_16x16x32_bf16 v[28:31],a[152:155],v[72:75],v[28:31]
s_waitcnt lgkmcnt(2)
ds_read_b128 v[68:71],v2 offset:43008
buffer_store_dwordx4 v[84:87],v3,s[36:39],s73 offen offset:0 sc0 sc1
v_mfma_f32_16x16x32_bf16 v[32:35],a[156:159],v[72:75],v[32:35]
s_cbranch_scc1 Lg1_slow1_%=
Lg1_d1w3_%=:
v_mfma_f32_16x16x32_bf16 v[36:39],a[96:99],v[44:47],0
s_add_u32 m0,s62,16384
ds_read_b128 v[72:75],v2 offset:44032
s_lshr_b32 s72,s67,6
v_mfma_f32_16x16x32_bf16 v[40:43],a[100:103],v[44:47],0
s_lshr_b32 s71,s62,2
s_cmp_lt_u32 s67,4194304
s_cselect_b32 s72,s59,s72
v_mfma_f32_16x16x32_bf16 v[36:39],a[104:107],v[48:51],v[36:39]
s_mov_b64 s[44:45],s[46:47]
ds_read_b128 v[44:47],v2 offset:36864
v_mfma_f32_16x16x32_bf16 v[40:43],a[108:111],v[48:51],v[40:43]
v_fma_f32 v28,-v92,v20,v28
v_fma_f32 v29,-v92,v21,v29
v_mfma_f32_16x16x32_bf16 v[36:39],a[112:115],v[52:55],v[36:39]
buffer_load_dwordx4 v1,s[36:39],s67 offen offset:1024 sc1 lds
v_mfma_f32_16x16x32_bf16 v[40:43],a[116:119],v[52:55],v[40:43]
ds_read_b128 v[48:51],v2 offset:37888
v_fma_f32 v30,-v92,v22,v30
v_mfma_f32_16x16x32_bf16 v[36:39],a[120:123],v[56:59],v[36:39]
ds_read_b128 v[52:55],v2 offset:38912
v_fma_f32 v31,-v92,v23,v31
v_mfma_f32_16x16x32_bf16 v[40:43],a[124:127],v[56:59],v[40:43]
s_waitcnt lgkmcnt(3)
v_mul_f32 v76,v28,v28
v_mul_f32 v77,v29,v29
v_mfma_f32_16x16x32_bf16 v[36:39],a[64:67],v[60:63],v[36:39]
buffer_load_dwordx4 v1,s[36:39],s67 offen offset:2048 sc1 lds
v_mfma_f32_16x16x32_bf16 v[40:43],a[68:71],v[60:63],v[40:43]
ds_read_b128 v[56:59],v2 offset:39936
v_mul_f32 v78,v30,v30
v_mfma_f32_16x16x32_bf16 v[36:39],a[72:75],v[64:67],v[36:39]
ds_read_b128 v[60:63],v2 offset:32768
v_mul_f32 v79,v31,v31
v_mfma_f32_16x16x32_bf16 v[40:43],a[76:79],v[64:67],v[40:43]
v_fma_f32 v76,v94,v76,v93
v_mul_f32 v76,v28,v76
v_mfma_f32_16x16x32_bf16 v[36:39],a[80:83],v[68:71],v[36:39]
buffer_load_dwordx4 v1,s[36:39],s67 offen offset:3072 sc1 lds
v_mfma_f32_16x16x32_bf16 v[40:43],a[84:87],v[68:71],v[40:43]
s_add_u32 m0,s71,s88
ds_read_b128 v[64:67],v2 offset:33792
v_fma_f32 v77,v94,v77,v93
v_mfma_f32_16x16x32_bf16 v[36:39],a[88:91],v[72:75],v[36:39]
buffer_load_dword v10,s[36:39],s72 offen offset:0 sc1 lds
v_mfma_f32_16x16x32_bf16 v[40:43],a[92:95],v[72:75],v[40:43]
s_waitcnt lgkmcnt(2)
ds_read_b128 v[68:71],v2 offset:34816
v_mul_f32 v77,v29,v77
v_mfma_f32_16x16x32_bf16 v[36:39],a[32:35],v[44:47],v[36:39]
s_waitcnt vmcnt(11)
ds_read_b128 v[72:75],v2 offset:35840
v_fma_f32 v78,v94,v78,v93
v_mfma_f32_16x16x32_bf16 v[40:43],a[36:39],v[44:47],v[40:43]
s_barrier
v_mfma_f32_16x16x32_bf16 v[36:39],a[40:43],v[48:51],v[36:39]
s_lshr_b32 s73,s48,0
ds_read_b128 v[44:47],v2 offset:61440
ds_read_b32 v16,v7 offset:0
v_mfma_f32_16x16x32_bf16 v[40:43],a[44:47],v[48:51],v[40:43]
s_lshr_b32 s70,s46,5
v_exp_f32 v76,v76
v_mfma_f32_16x16x32_bf16 v[36:39],a[48:51],v[52:55],v[36:39]
buffer_store_dword v5,v4,s[36:39],s70 offen offset:0 sc0 sc1
ds_read_b128 v[48:51],v2 offset:62464
v_exp_f32 v77,v77
v_mfma_f32_16x16x32_bf16 v[40:43],a[52:55],v[52:55],v[40:43]
s_xor_b32 s69,s63,65536
v_mul_f32 v78,v30,v78
v_fma_f32 v76,v76,v95,v95
v_mfma_f32_16x16x32_bf16 v[36:39],a[56:59],v[56:59],v[36:39]
s_waitcnt lgkmcnt(1)
ds_read_b128 v[52:55],v2 offset:63488
v_fma_f32 v79,v94,v79,v93
v_mfma_f32_16x16x32_bf16 v[40:43],a[60:63],v[56:59],v[40:43]
v_cmp_eq_u32 vcc,s60,v16
v_exp_f32 v78,v78
v_mul_f32 v79,v31,v79
v_mfma_f32_16x16x32_bf16 v[36:39],a[0:3],v[60:63],v[36:39]
s_cmp_ge_u32 s67,s59
ds_read_b128 v[56:59],v2 offset:64512
v_fma_f32 v77,v77,v95,v95
v_mfma_f32_16x16x32_bf16 v[40:43],a[4:7],v[60:63],v[40:43]
s_cselect_b32 s62,s69,s64
v_exp_f32 v79,v79
v_mfma_f32_16x16x32_bf16 v[36:39],a[8:11],v[64:67],v[36:39]
s_cselect_b32 s86,s50,s51
ds_read_b128 v[60:63],v2 offset:57344
v_fma_f32 v78,v78,v95,v95
v_mfma_f32_16x16x32_bf16 v[40:43],a[12:15],v[64:67],v[40:43]
s_mov_b32 m0,s62
v_rcp_f32 v76,v76
v_mfma_f32_16x16x32_bf16 v[36:39],a[16:19],v[68:71],v[36:39]
s_cselect_b32 s87,vcc_lo,vcc_hi
ds_read_b128 v[64:67],v2 offset:58368
ds_read_b128 v[96:99],v8 offset:8192
v_mfma_f32_16x16x32_bf16 v[40:43],a[20:23],v[68:71],v[40:43]
s_cmp_eq_u32 s87,-1
v_fma_f32 v79,v79,v95,v95
s_cselect_b32 s66,s86,s59
v_mfma_f32_16x16x32_bf16 v[36:39],a[24:27],v[72:75],v[36:39]
buffer_load_dwordx4 v0,s[36:39],s66 offen offset:0 sc1 lds
v_mfma_f32_16x16x32_bf16 v[40:43],a[28:31],v[72:75],v[40:43]
s_waitcnt lgkmcnt(3)
ds_read_b128 v[68:71],v2 offset:59392
v_mul_f32 v28,v28,v76
v_mfma_f32_16x16x32_bf16 v[36:39],a[224:227],v[44:47],v[36:39]
s_xor_b32 s64,s64,65536
ds_read_b128 v[72:75],v2 offset:60416
v_fma_f32 v32,-v92,v24,v32
v_mfma_f32_16x16x32_bf16 v[40:43],a[228:231],v[44:47],v[40:43]
s_add_u32 s74,s53,s55
v_rcp_f32 v77,v77
v_mfma_f32_16x16x32_bf16 v[36:39],a[232:235],v[48:51],v[36:39]
s_waitcnt lgkmcnt(2)
buffer_load_dwordx4 v0,s[36:39],s66 offen offset:1024 sc1 lds
v_mfma_f32_16x16x32_bf16 v[40:43],a[236:239],v[48:51],v[40:43]
s_cmp_eq_u32 s74,s56
ds_read_b128 v[44:47],v2 offset:53248
v_mul_f32 v29,v29,v77
v_mfma_f32_16x16x32_bf16 v[36:39],a[240:243],v[52:55],v[36:39]
s_cselect_b32 s74,s57,s74
ds_read_b128 v[48:51],v2 offset:54272)"
R"(
v_cvt_pk_bf16_f32 v84,v28,v29
v_mfma_f32_16x16x32_bf16 v[40:43],a[244:247],v[52:55],v[40:43]
s_cmp_eq_u32 s74,s58
v_rcp_f32 v78,v78
v_mfma_f32_16x16x32_bf16 v[36:39],a[248:251],v[56:59],v[36:39]
buffer_load_dwordx4 v0,s[36:39],s66 offen offset:2048 sc1 lds
v_mfma_f32_16x16x32_bf16 v[40:43],a[252:255],v[56:59],v[40:43]
s_waitcnt lgkmcnt(2)
ds_read_b128 v[52:55],v2 offset:55296
v_mul_f32 v30,v30,v78
v_mfma_f32_16x16x32_bf16 v[36:39],a[192:195],v[60:63],v[36:39]
s_cselect_b32 s74,s59,s74
ds_read_b128 v[56:59],v2 offset:56320
v_fma_f32 v33,-v92,v25,v33
v_mfma_f32_16x16x32_bf16 v[40:43],a[196:199],v[60:63],v[40:43]
s_mov_b64 s[46:47],s[48:49]
v_rcp_f32 v79,v79
v_mfma_f32_16x16x32_bf16 v[36:39],a[200:203],v[64:67],v[36:39]
buffer_load_dwordx4 v0,s[36:39],s66 offen offset:3072 sc1 lds
v_mfma_f32_16x16x32_bf16 v[40:43],a[204:207],v[64:67],v[40:43]
s_add_u32 m0,s62,16384
ds_read_b128 v[60:63],v2 offset:49152
v_mul_f32 v31,v31,v79
v_mfma_f32_16x16x32_bf16 v[36:39],a[208:211],v[68:71],v[36:39]
s_mov_b64 s[48:49],s[50:51]
ds_read_b128 v[64:67],v2 offset:50176
v_cvt_pk_bf16_f32 v85,v30,v31
v_mfma_f32_16x16x32_bf16 v[40:43],a[212:215],v[68:71],v[40:43]
s_mov_b64 s[50:51],s[52:53]
v_fma_f32 v34,-v92,v26,v34
v_fma_f32 v35,-v92,v27,v35
v_mfma_f32_16x16x32_bf16 v[36:39],a[216:219],v[72:75],v[36:39]
buffer_load_dwordx4 v1,s[36:39],s66 offen offset:0 sc1 lds
v_mfma_f32_16x16x32_bf16 v[40:43],a[220:223],v[72:75],v[40:43]
s_waitcnt lgkmcnt(2)
ds_read_b128 v[68:71],v2 offset:51200
v_mul_f32 v80,v32,v32
v_mfma_f32_16x16x32_bf16 v[36:39],a[160:163],v[44:47],v[36:39]
s_waitcnt vmcnt(12)
ds_read_b128 v[72:75],v2 offset:52224
v_xor_b32 v2,65536,v2
v_mfma_f32_16x16x32_bf16 v[40:43],a[164:167],v[44:47],v[40:43]
s_barrier
v_mfma_f32_16x16x32_bf16 v[36:39],a[168:171],v[48:51],v[36:39]
s_mov_b32 s52,s74
ds_read_b128 v[44:47],v2 offset:12288
v_mul_f32 v81,v33,v33
v_mfma_f32_16x16x32_bf16 v[40:43],a[172:175],v[48:51],v[40:43]
s_cmp_lt_u32 s67,s59
v_mul_f32 v82,v34,v34
v_mul_f32 v83,v35,v35
v_mfma_f32_16x16x32_bf16 v[36:39],a[176:179],v[52:55],v[36:39]
ds_read_b128 v[48:51],v2 offset:13312
v_fma_f32 v80,v94,v80,v93
v_mfma_f32_16x16x32_bf16 v[40:43],a[180:183],v[52:55],v[40:43]
v_mul_f32 v80,v32,v80
v_fma_f32 v81,v94,v81,v93
v_mfma_f32_16x16x32_bf16 v[36:39],a[184:187],v[56:59],v[36:39]
ds_read_b128 v[52:55],v2 offset:14336
v_mul_f32 v81,v33,v81
v_mfma_f32_16x16x32_bf16 v[40:43],a[188:191],v[56:59],v[40:43]
s_waitcnt lgkmcnt(3)
v_exp_f32 v80,v80
v_mfma_f32_16x16x32_bf16 v[36:39],a[128:131],v[60:63],v[36:39]
ds_read_b128 v[56:59],v2 offset:15360
v_fma_f32 v80,v80,v95,v95
v_mfma_f32_16x16x32_bf16 v[40:43],a[132:135],v[60:63],v[40:43]
v_exp_f32 v81,v81
v_mfma_f32_16x16x32_bf16 v[36:39],a[136:139],v[64:67],v[36:39]
ds_read_b128 v[60:63],v2 offset:8192
v_fma_f32 v81,v81,v95,v95
v_mfma_f32_16x16x32_bf16 v[40:43],a[140:143],v[64:67],v[40:43]
v_fma_f32 v82,v94,v82,v93
v_fma_f32 v83,v94,v83,v93
v_mul_f32 v82,v34,v82
v_mul_f32 v83,v35,v83
v_exp_f32 v82,v82
v_exp_f32 v83,v83
v_mfma_f32_16x16x32_bf16 v[36:39],a[144:147],v[68:71],v[36:39]
v_fma_f32 v82,v82,v95,v95
v_fma_f32 v83,v83,v95,v95
v_rcp_f32 v80,v80
v_rcp_f32 v81,v81
v_rcp_f32 v82,v82
v_rcp_f32 v83,v83
v_mfma_f32_16x16x32_bf16 v[40:43],a[148:151],v[68:71],v[40:43]
ds_read_b128 v[64:67],v2 offset:9216
v_mul_f32 v32,v32,v80
v_mul_f32 v33,v33,v81
v_mul_f32 v34,v34,v82
v_mul_f32 v35,v35,v83
v_cvt_pk_bf16_f32 v86,v32,v33
v_cvt_pk_bf16_f32 v87,v34,v35
v_mfma_f32_16x16x32_bf16 v[36:39],a[152:155],v[72:75],v[36:39]
s_waitcnt lgkmcnt(2)
ds_read_b128 v[68:71],v2 offset:10240
buffer_store_dwordx4 v[84:87],v3,s[36:39],s73 offen offset:0 sc0 sc1
v_xor_b32 v8,16384,v8
v_mfma_f32_16x16x32_bf16 v[40:43],a[156:159],v[72:75],v[40:43]
s_cbranch_scc1 Lg1_d0w3_%=
s_branch Lg1_slow0_%=
Lg1_slow0_%=:
s_add_u32 m0,s62,16384
s_lshr_b32 s72,s66,6
s_lshr_b32 s71,s62,2
s_cmp_lt_u32 s66,4194304
s_cselect_b32 s72,s59,s72
buffer_load_dwordx4 v1,s[36:39],s66 offen offset:1024 sc1 lds
buffer_load_dwordx4 v1,s[36:39],s66 offen offset:2048 sc1 lds
buffer_load_dwordx4 v1,s[36:39],s66 offen offset:3072 sc1 lds
s_add_u32 m0,s71,s88
s_nop 0
buffer_load_dword v10,s[36:39],s72 offen offset:0 sc1 lds
s_cmp_eq_u32 s54,1
s_cselect_b32 s68,s49,s48
s_lshr_b32 s69,s68,5
s_cmp_lt_u32 s68,4194304
s_cselect_b32 s68,0xffffcfff,-1
s_and_b32 s68,s68,s69
s_add_u32 m0,s78,8192
s_nop 0
buffer_load_dword v6,s[36:39],s68 offen offset:0 sc1 lds
s_lshr_b32 s73,s47,0
v_fma_f32 v36,-v96,v20,v36
v_fma_f32 v37,-v96,v21,v37
v_fma_f32 v38,-v96,v22,v38
v_fma_f32 v39,-v96,v23,v39
v_mul_f32 v76,v36,v36
v_mul_f32 v77,v37,v37
v_mul_f32 v78,v38,v38
v_mul_f32 v79,v39,v39
v_fma_f32 v76,v98,v76,v97
v_fma_f32 v77,v98,v77,v97
v_fma_f32 v78,v98,v78,v97
v_fma_f32 v79,v98,v79,v97
v_mul_f32 v76,v36,v76
v_mul_f32 v77,v37,v77
v_mul_f32 v78,v38,v78
v_mul_f32 v79,v39,v79
v_exp_f32 v76,v76
v_exp_f32 v77,v77
v_exp_f32 v78,v78
v_exp_f32 v79,v79
v_fma_f32 v76,v76,v99,v99
v_fma_f32 v77,v77,v99,v99
v_fma_f32 v78,v78,v99,v99
v_fma_f32 v79,v79,v99,v99
v_rcp_f32 v76,v76
v_rcp_f32 v77,v77
v_rcp_f32 v78,v78
v_rcp_f32 v79,v79
v_mul_f32 v36,v36,v76
v_mul_f32 v37,v37,v77
v_mul_f32 v38,v38,v78
v_mul_f32 v39,v39,v79
v_cvt_pk_bf16_f32 v84,v36,v37
v_cvt_pk_bf16_f32 v85,v38,v39
s_add_u32 m0,s78,8448
s_nop 0
buffer_load_dword v6,s[36:39],s68 offen offset:0 sc1 lds
v_fma_f32 v40,-v96,v24,v40
v_fma_f32 v41,-v96,v25,v41
v_fma_f32 v42,-v96,v26,v42
v_fma_f32 v43,-v96,v27,v43
v_mul_f32 v80,v40,v40
v_mul_f32 v81,v41,v41
v_mul_f32 v82,v42,v42
v_mul_f32 v83,v43,v43
v_fma_f32 v80,v98,v80,v97
v_fma_f32 v81,v98,v81,v97
v_fma_f32 v82,v98,v82,v97
v_fma_f32 v83,v98,v83,v97
v_mul_f32 v80,v40,v80
v_mul_f32 v81,v41,v81
v_mul_f32 v82,v42,v82
v_mul_f32 v83,v43,v83
v_exp_f32 v80,v80
v_exp_f32 v81,v81
v_exp_f32 v82,v82
v_exp_f32 v83,v83
v_fma_f32 v80,v80,v99,v99
v_fma_f32 v81,v81,v99,v99
v_fma_f32 v82,v82,v99,v99
v_fma_f32 v83,v83,v99,v99
v_rcp_f32 v80,v80
v_rcp_f32 v81,v81
v_rcp_f32 v82,v82
v_rcp_f32 v83,v83
v_mul_f32 v40,v40,v80
v_mul_f32 v41,v41,v81
v_mul_f32 v42,v42,v82
v_mul_f32 v43,v43,v83
v_cvt_pk_bf16_f32 v86,v40,v41
v_cvt_pk_bf16_f32 v87,v42,v43
buffer_store_dwordx4 v[84:87],v3,s[36:39],s73 offen offset:0 sc0 sc1
s_cmp_ge_u32 s48,s59
s_cbranch_scc1 Lg1_done0_%=
s_add_u32 m0,s78,16384
s_nop 0
buffer_load_dword v6,s[36:39],s68 offen offset:0 sc1 lds)"
R"(
s_waitcnt vmcnt(3)
s_barrier
ds_read_b32 v16,v7 offset:8192
s_lshr_b32 s70,s45,5
buffer_store_dword v5,v4,s[36:39],s70 offen offset:0 sc0 sc1
s_mov_b32 s45,s59
s_lshr_b32 s70,s46,5
buffer_store_dword v5,v4,s[36:39],s70 offen offset:0 sc0 sc1
s_mov_b32 s46,s59
s_mov_b32 s69,18432
s_waitcnt lgkmcnt(0)
v_cmp_eq_u32 vcc,s60,v16
s_nop 0
s_cmp_eq_u32 vcc_lo,-1
s_cbranch_scc1 Lg1_go0_%=
s_add_u32 m0,s78,16640
s_nop 0
buffer_load_dword v6,s[36:39],s68 offen offset:0 sc1 lds
s_waitcnt vmcnt(5)
s_barrier
ds_read_b32 v16,v7 offset:8448
s_mov_b32 s69,18688
s_waitcnt lgkmcnt(0)
v_cmp_eq_u32 vcc,s60,v16
s_nop 0
s_cmp_eq_u32 vcc_lo,-1
s_cbranch_scc1 Lg1_go0_%=
s_add_u32 m0,s78,8192
s_nop 0
buffer_load_dword v6,s[36:39],s68 offen offset:0 sc1 lds
s_waitcnt vmcnt(4)
s_barrier
ds_read_b32 v16,v7 offset:16384
s_lshr_b32 s70,s47,5
buffer_store_dword v5,v4,s[36:39],s70 offen offset:0 sc0 sc1
s_mov_b32 s47,s59
s_mov_b32 s69,10240
s_waitcnt lgkmcnt(0)
v_cmp_eq_u32 vcc,s60,v16
s_nop 0
s_cmp_eq_u32 vcc_lo,-1
s_cbranch_scc1 Lg1_go0_%=
s_add_u32 m0,s78,8448
s_nop 0
buffer_load_dword v6,s[36:39],s68 offen offset:0 sc1 lds
s_waitcnt vmcnt(3)
s_barrier
ds_read_b32 v16,v7 offset:16640
s_mov_b32 s69,10496
s_waitcnt lgkmcnt(0)
v_cmp_eq_u32 vcc,s60,v16
s_nop 0
s_cmp_eq_u32 vcc_lo,-1
s_cbranch_scc1 Lg1_go0_%=
Lg1_poll0_%=:
s_add_u32 m0,s78,16384
s_nop 0
buffer_load_dword v6,s[36:39],s68 offen offset:0 sc1 lds
s_waitcnt vmcnt(2)
s_barrier
ds_read_b32 v16,v7 offset:8192
s_mov_b32 s69,18432
s_waitcnt lgkmcnt(0)
v_cmp_eq_u32 vcc,s60,v16
s_nop 0
s_cmp_eq_u32 vcc_lo,-1
s_cbranch_scc1 Lg1_go0_%=
s_sleep 4
s_add_u32 m0,s78,16640
s_nop 0
buffer_load_dword v6,s[36:39],s68 offen offset:0 sc1 lds
s_waitcnt vmcnt(2)
s_barrier
ds_read_b32 v16,v7 offset:8448
s_mov_b32 s69,18688
s_waitcnt lgkmcnt(0)
v_cmp_eq_u32 vcc,s60,v16
s_nop 0
s_cmp_eq_u32 vcc_lo,-1
s_cbranch_scc1 Lg1_go0_%=
s_sleep 4
s_add_u32 m0,s78,8192
s_nop 0
buffer_load_dword v6,s[36:39],s68 offen offset:0 sc1 lds
s_waitcnt vmcnt(2)
s_barrier
ds_read_b32 v16,v7 offset:16384
s_mov_b32 s69,10240
s_waitcnt lgkmcnt(0)
v_cmp_eq_u32 vcc,s60,v16
s_nop 0
s_cmp_eq_u32 vcc_lo,-1
s_cbranch_scc1 Lg1_go0_%=
s_sleep 4
s_add_u32 m0,s78,8448
s_nop 0
buffer_load_dword v6,s[36:39],s68 offen offset:0 sc1 lds
s_waitcnt vmcnt(2)
s_barrier
ds_read_b32 v16,v7 offset:16640
s_mov_b32 s69,10496
s_waitcnt lgkmcnt(0)
v_cmp_eq_u32 vcc,s60,v16
s_nop 0
s_cmp_eq_u32 vcc_lo,-1
s_cbranch_scc1 Lg1_go0_%=
s_sleep 4
s_add_u32 s79,s79,1
s_cmp_lt_u32 s79,20000
s_cbranch_scc1 Lg1_poll0_%=
Lg1_go0_%=:
s_xor_b32 s62,s63,65536
s_cmp_lt_u32 s66,s59
s_cbranch_scc1 Lg1_gs0_%=
s_mov_b32 m0,s62
s_nop 0
buffer_load_dwordx4 v0,s[36:39],s48 offen offset:0 sc1 lds
buffer_load_dwordx4 v0,s[36:39],s48 offen offset:1024 sc1 lds
buffer_load_dwordx4 v0,s[36:39],s48 offen offset:2048 sc1 lds
buffer_load_dwordx4 v0,s[36:39],s48 offen offset:3072 sc1 lds
s_add_u32 m0,s62,16384
s_lshr_b32 s72,s48,6
s_lshr_b32 s71,s62,2
s_cmp_lt_u32 s48,4194304
s_cselect_b32 s72,s59,s72
buffer_load_dwordx4 v1,s[36:39],s48 offen offset:0 sc1 lds
buffer_load_dwordx4 v1,s[36:39],s48 offen offset:1024 sc1 lds
buffer_load_dwordx4 v1,s[36:39],s48 offen offset:2048 sc1 lds
buffer_load_dwordx4 v1,s[36:39],s48 offen offset:3072 sc1 lds
s_add_u32 m0,s71,s88
s_nop 0
buffer_load_dword v10,s[36:39],s72 offen offset:0 sc1 lds
s_cmp_eq_u32 s54,1
s_cselect_b32 s68,s50,s49
s_lshr_b32 s69,s68,5
s_cmp_lt_u32 s68,4194304
s_cselect_b32 s68,0xffffcfff,-1
s_and_b32 s68,s68,s69
s_add_u32 m0,s78,256
s_nop 0
buffer_load_dword v6,s[36:39],s68 offen offset:0 sc1 lds
s_cmp_eq_u32 vcc_hi,-1
s_cselect_b32 s66,s49,s59
s_xor_b32 s62,s64,65536
s_mov_b32 m0,s62
s_nop 0
buffer_load_dwordx4 v0,s[36:39],s66 offen offset:0 sc1 lds
buffer_load_dwordx4 v0,s[36:39],s66 offen offset:1024 sc1 lds
buffer_load_dwordx4 v0,s[36:39],s66 offen offset:2048 sc1 lds
buffer_load_dwordx4 v0,s[36:39],s66 offen offset:3072 sc1 lds
s_add_u32 m0,s62,16384
s_nop 0
buffer_load_dwordx4 v1,s[36:39],s66 offen offset:0 sc1 lds
s_waitcnt vmcnt(11)
s_barrier
s_lshl_b32 s70,s54,12
v_add_u32 v76,s70,v2
v_xor_b32 v77,4096,v76
ds_read_b128 v[44:47],v76 offset:0
ds_read_b128 v[48:51],v76 offset:1024
ds_read_b128 v[52:55],v76 offset:2048
ds_read_b128 v[56:59],v76 offset:3072
ds_read_b128 v[60:63],v77 offset:0
ds_read_b128 v[64:67],v77 offset:1024
ds_read_b128 v[68:71],v77 offset:2048
s_lshr_b32 s70,s45,5
buffer_store_dword v5,v4,s[36:39],s70 offen offset:0 sc0 sc1
s_mov_b32 s45,s59
s_lshr_b32 s70,s46,5
buffer_store_dword v5,v4,s[36:39],s70 offen offset:0 sc0 sc1
s_mov_b32 s46,s59
s_lshr_b32 s70,s47,5
buffer_store_dword v5,v4,s[36:39],s70 offen offset:0 sc0 sc1
s_mov_b32 s47,s59
s_waitcnt lgkmcnt(3)
s_setpc_b64 s[90:91]
Lg1_gs0_%=:
s_lshl_b32 s70,s54,12
v_add_u32 v76,s70,v2
v_xor_b32 v77,4096,v76
ds_read_b128 v[44:47],v76 offset:0
ds_read_b128 v[48:51],v76 offset:1024
ds_read_b128 v[52:55],v76 offset:2048
ds_read_b128 v[56:59],v76 offset:3072
ds_read_b128 v[60:63],v77 offset:0
ds_read_b128 v[64:67],v77 offset:1024
ds_read_b128 v[68:71],v77 offset:2048
s_cmp_eq_u32 s54,1
s_cselect_b32 s68,s50,s49
s_lshr_b32 s69,s68,5
s_cmp_lt_u32 s68,4194304
s_cselect_b32 s68,0xffffcfff,-1
s_and_b32 s68,s68,s69
s_add_u32 m0,s78,256
s_nop 0
buffer_load_dword v6,s[36:39],s68 offen offset:0 sc1 lds
s_cmp_eq_u32 vcc_hi,-1
s_cselect_b32 s66,s49,s59
s_xor_b32 s62,s64,65536
s_mov_b32 m0,s62)"
R"(
s_nop 0
buffer_load_dwordx4 v0,s[36:39],s66 offen offset:0 sc1 lds
buffer_load_dwordx4 v0,s[36:39],s66 offen offset:1024 sc1 lds
buffer_load_dwordx4 v0,s[36:39],s66 offen offset:2048 sc1 lds
buffer_load_dwordx4 v0,s[36:39],s66 offen offset:3072 sc1 lds
s_add_u32 m0,s62,16384
s_nop 0
buffer_load_dwordx4 v1,s[36:39],s66 offen offset:0 sc1 lds
s_mov_b32 s45,s47
s_mov_b32 s47,s59
s_lshr_b32 s70,s59,5
buffer_store_dword v5,v4,s[36:39],s70 offen offset:0 sc0 sc1
s_waitcnt lgkmcnt(3)
s_setpc_b64 s[90:91]
Lg1_done0_%=:
s_waitcnt vmcnt(0)
s_barrier
s_lshr_b32 s70,s45,5
buffer_store_dword v5,v4,s[36:39],s70 offen offset:0 sc0 sc1
s_lshr_b32 s70,s46,5
buffer_store_dword v5,v4,s[36:39],s70 offen offset:0 sc0 sc1
s_lshr_b32 s70,s47,5
buffer_store_dword v5,v4,s[36:39],s70 offen offset:0 sc0 sc1
s_branch Lg1_done_%=
Lg1_slow1_%=:
s_add_u32 m0,s62,16384
s_lshr_b32 s72,s67,6
s_lshr_b32 s71,s62,2
s_cmp_lt_u32 s67,4194304
s_cselect_b32 s72,s59,s72
buffer_load_dwordx4 v1,s[36:39],s67 offen offset:1024 sc1 lds
buffer_load_dwordx4 v1,s[36:39],s67 offen offset:2048 sc1 lds
buffer_load_dwordx4 v1,s[36:39],s67 offen offset:3072 sc1 lds
s_add_u32 m0,s71,s88
s_nop 0
buffer_load_dword v10,s[36:39],s72 offen offset:0 sc1 lds
s_cmp_eq_u32 s54,1
s_cselect_b32 s68,s50,s49
s_lshr_b32 s69,s68,5
s_cmp_lt_u32 s68,4194304
s_cselect_b32 s68,0xffffcfff,-1
s_and_b32 s68,s68,s69
s_add_u32 m0,s78,8192
s_nop 0
buffer_load_dword v6,s[36:39],s68 offen offset:0 sc1 lds
s_lshr_b32 s73,s48,0
v_fma_f32 v28,-v92,v20,v28
v_fma_f32 v29,-v92,v21,v29
v_fma_f32 v30,-v92,v22,v30
v_fma_f32 v31,-v92,v23,v31
v_mul_f32 v76,v28,v28
v_mul_f32 v77,v29,v29
v_mul_f32 v78,v30,v30
v_mul_f32 v79,v31,v31
v_fma_f32 v76,v94,v76,v93
v_fma_f32 v77,v94,v77,v93
v_fma_f32 v78,v94,v78,v93
v_fma_f32 v79,v94,v79,v93
v_mul_f32 v76,v28,v76
v_mul_f32 v77,v29,v77
v_mul_f32 v78,v30,v78
v_mul_f32 v79,v31,v79
v_exp_f32 v76,v76
v_exp_f32 v77,v77
v_exp_f32 v78,v78
v_exp_f32 v79,v79
v_fma_f32 v76,v76,v95,v95
v_fma_f32 v77,v77,v95,v95
v_fma_f32 v78,v78,v95,v95
v_fma_f32 v79,v79,v95,v95
v_rcp_f32 v76,v76
v_rcp_f32 v77,v77
v_rcp_f32 v78,v78
v_rcp_f32 v79,v79
v_mul_f32 v28,v28,v76
v_mul_f32 v29,v29,v77
v_mul_f32 v30,v30,v78
v_mul_f32 v31,v31,v79
v_cvt_pk_bf16_f32 v84,v28,v29
v_cvt_pk_bf16_f32 v85,v30,v31
s_add_u32 m0,s78,8448
s_nop 0
buffer_load_dword v6,s[36:39],s68 offen offset:0 sc1 lds
v_fma_f32 v32,-v92,v24,v32
v_fma_f32 v33,-v92,v25,v33
v_fma_f32 v34,-v92,v26,v34
v_fma_f32 v35,-v92,v27,v35
v_mul_f32 v80,v32,v32
v_mul_f32 v81,v33,v33
v_mul_f32 v82,v34,v34
v_mul_f32 v83,v35,v35
v_fma_f32 v80,v94,v80,v93
v_fma_f32 v81,v94,v81,v93
v_fma_f32 v82,v94,v82,v93
v_fma_f32 v83,v94,v83,v93
v_mul_f32 v80,v32,v80
v_mul_f32 v81,v33,v81
v_mul_f32 v82,v34,v82
v_mul_f32 v83,v35,v83
v_exp_f32 v80,v80
v_exp_f32 v81,v81
v_exp_f32 v82,v82
v_exp_f32 v83,v83
v_fma_f32 v80,v80,v95,v95
v_fma_f32 v81,v81,v95,v95
v_fma_f32 v82,v82,v95,v95
v_fma_f32 v83,v83,v95,v95
v_rcp_f32 v80,v80
v_rcp_f32 v81,v81
v_rcp_f32 v82,v82
v_rcp_f32 v83,v83
v_mul_f32 v32,v32,v80
v_mul_f32 v33,v33,v81
v_mul_f32 v34,v34,v82
v_mul_f32 v35,v35,v83
v_cvt_pk_bf16_f32 v86,v32,v33
v_cvt_pk_bf16_f32 v87,v34,v35
buffer_store_dwordx4 v[84:87],v3,s[36:39],s73 offen offset:0 sc0 sc1
s_cmp_ge_u32 s49,s59
s_cbranch_scc1 Lg1_done1_%=
s_add_u32 m0,s78,16384
s_nop 0
buffer_load_dword v6,s[36:39],s68 offen offset:0 sc1 lds
s_waitcnt vmcnt(3)
s_barrier
ds_read_b32 v16,v7 offset:8192
s_lshr_b32 s70,s46,5
buffer_store_dword v5,v4,s[36:39],s70 offen offset:0 sc0 sc1
s_mov_b32 s46,s59
s_lshr_b32 s70,s47,5
buffer_store_dword v5,v4,s[36:39],s70 offen offset:0 sc0 sc1
s_mov_b32 s47,s59
s_mov_b32 s69,18432
s_waitcnt lgkmcnt(0)
v_cmp_eq_u32 vcc,s60,v16
s_nop 0
s_cmp_eq_u32 vcc_lo,-1
s_cbranch_scc1 Lg1_go1_%=
s_add_u32 m0,s78,16640
s_nop 0
buffer_load_dword v6,s[36:39],s68 offen offset:0 sc1 lds
s_waitcnt vmcnt(5)
s_barrier
ds_read_b32 v16,v7 offset:8448
s_mov_b32 s69,18688
s_waitcnt lgkmcnt(0)
v_cmp_eq_u32 vcc,s60,v16
s_nop 0
s_cmp_eq_u32 vcc_lo,-1
s_cbranch_scc1 Lg1_go1_%=
s_add_u32 m0,s78,8192
s_nop 0
buffer_load_dword v6,s[36:39],s68 offen offset:0 sc1 lds
s_waitcnt vmcnt(4)
s_barrier
ds_read_b32 v16,v7 offset:16384
s_lshr_b32 s70,s48,5
buffer_store_dword v5,v4,s[36:39],s70 offen offset:0 sc0 sc1
s_mov_b32 s48,s59
s_mov_b32 s69,10240
s_waitcnt lgkmcnt(0)
v_cmp_eq_u32 vcc,s60,v16
s_nop 0
s_cmp_eq_u32 vcc_lo,-1
s_cbranch_scc1 Lg1_go1_%=
s_add_u32 m0,s78,8448
s_nop 0
buffer_load_dword v6,s[36:39],s68 offen offset:0 sc1 lds
s_waitcnt vmcnt(3)
s_barrier
ds_read_b32 v16,v7 offset:16640
s_mov_b32 s69,10496
s_waitcnt lgkmcnt(0)
v_cmp_eq_u32 vcc,s60,v16
s_nop 0
s_cmp_eq_u32 vcc_lo,-1
s_cbranch_scc1 Lg1_go1_%=
Lg1_poll1_%=:
s_add_u32 m0,s78,16384
s_nop 0
buffer_load_dword v6,s[36:39],s68 offen offset:0 sc1 lds
s_waitcnt vmcnt(2)
s_barrier
ds_read_b32 v16,v7 offset:8192
s_mov_b32 s69,18432
s_waitcnt lgkmcnt(0)
v_cmp_eq_u32 vcc,s60,v16
s_nop 0
s_cmp_eq_u32 vcc_lo,-1
s_cbranch_scc1 Lg1_go1_%=
s_sleep 4
s_add_u32 m0,s78,16640
s_nop 0
buffer_load_dword v6,s[36:39],s68 offen offset:0 sc1 lds
s_waitcnt vmcnt(2)
s_barrier
ds_read_b32 v16,v7 offset:8448
s_mov_b32 s69,18688
s_waitcnt lgkmcnt(0)
v_cmp_eq_u32 vcc,s60,v16)"
R"(
s_nop 0
s_cmp_eq_u32 vcc_lo,-1
s_cbranch_scc1 Lg1_go1_%=
s_sleep 4
s_add_u32 m0,s78,8192
s_nop 0
buffer_load_dword v6,s[36:39],s68 offen offset:0 sc1 lds
s_waitcnt vmcnt(2)
s_barrier
ds_read_b32 v16,v7 offset:16384
s_mov_b32 s69,10240
s_waitcnt lgkmcnt(0)
v_cmp_eq_u32 vcc,s60,v16
s_nop 0
s_cmp_eq_u32 vcc_lo,-1
s_cbranch_scc1 Lg1_go1_%=
s_sleep 4
s_add_u32 m0,s78,8448
s_nop 0
buffer_load_dword v6,s[36:39],s68 offen offset:0 sc1 lds
s_waitcnt vmcnt(2)
s_barrier
ds_read_b32 v16,v7 offset:16640
s_mov_b32 s69,10496
s_waitcnt lgkmcnt(0)
v_cmp_eq_u32 vcc,s60,v16
s_nop 0
s_cmp_eq_u32 vcc_lo,-1
s_cbranch_scc1 Lg1_go1_%=
s_sleep 4
s_add_u32 s79,s79,1
s_cmp_lt_u32 s79,20000
s_cbranch_scc1 Lg1_poll1_%=
Lg1_go1_%=:
s_xor_b32 s62,s64,65536
s_cmp_lt_u32 s67,s59
s_cbranch_scc1 Lg1_gs1_%=
s_mov_b32 m0,s62
s_nop 0
buffer_load_dwordx4 v0,s[36:39],s49 offen offset:0 sc1 lds
buffer_load_dwordx4 v0,s[36:39],s49 offen offset:1024 sc1 lds
buffer_load_dwordx4 v0,s[36:39],s49 offen offset:2048 sc1 lds
buffer_load_dwordx4 v0,s[36:39],s49 offen offset:3072 sc1 lds
s_add_u32 m0,s62,16384
s_lshr_b32 s72,s49,6
s_lshr_b32 s71,s62,2
s_cmp_lt_u32 s49,4194304
s_cselect_b32 s72,s59,s72
buffer_load_dwordx4 v1,s[36:39],s49 offen offset:0 sc1 lds
buffer_load_dwordx4 v1,s[36:39],s49 offen offset:1024 sc1 lds
buffer_load_dwordx4 v1,s[36:39],s49 offen offset:2048 sc1 lds
buffer_load_dwordx4 v1,s[36:39],s49 offen offset:3072 sc1 lds
s_add_u32 m0,s71,s88
s_nop 0
buffer_load_dword v10,s[36:39],s72 offen offset:0 sc1 lds
s_cmp_eq_u32 s54,1
s_cselect_b32 s68,s51,s50
s_lshr_b32 s69,s68,5
s_cmp_lt_u32 s68,4194304
s_cselect_b32 s68,0xffffcfff,-1
s_and_b32 s68,s68,s69
s_add_u32 m0,s78,0
s_nop 0
buffer_load_dword v6,s[36:39],s68 offen offset:0 sc1 lds
s_cmp_eq_u32 vcc_hi,-1
s_cselect_b32 s67,s50,s59
s_xor_b32 s62,s63,65536
s_mov_b32 m0,s62
s_nop 0
buffer_load_dwordx4 v0,s[36:39],s67 offen offset:0 sc1 lds
buffer_load_dwordx4 v0,s[36:39],s67 offen offset:1024 sc1 lds
buffer_load_dwordx4 v0,s[36:39],s67 offen offset:2048 sc1 lds
buffer_load_dwordx4 v0,s[36:39],s67 offen offset:3072 sc1 lds
s_add_u32 m0,s62,16384
s_nop 0
buffer_load_dwordx4 v1,s[36:39],s67 offen offset:0 sc1 lds
s_waitcnt vmcnt(11)
s_barrier
s_lshl_b32 s70,s54,12
v_add_u32 v76,s70,v2
v_xor_b32 v77,4096,v76
ds_read_b128 v[44:47],v76 offset:32768
ds_read_b128 v[48:51],v76 offset:33792
ds_read_b128 v[52:55],v76 offset:34816
ds_read_b128 v[56:59],v76 offset:35840
ds_read_b128 v[60:63],v77 offset:32768
ds_read_b128 v[64:67],v77 offset:33792
ds_read_b128 v[68:71],v77 offset:34816
s_lshr_b32 s70,s46,5
buffer_store_dword v5,v4,s[36:39],s70 offen offset:0 sc0 sc1
s_mov_b32 s46,s59
s_lshr_b32 s70,s47,5
buffer_store_dword v5,v4,s[36:39],s70 offen offset:0 sc0 sc1
s_mov_b32 s47,s59
s_lshr_b32 s70,s48,5
buffer_store_dword v5,v4,s[36:39],s70 offen offset:0 sc0 sc1
s_mov_b32 s48,s59
s_waitcnt lgkmcnt(3)
s_setpc_b64 s[92:93]
Lg1_gs1_%=:
s_lshl_b32 s70,s54,12
v_add_u32 v76,s70,v2
v_xor_b32 v77,4096,v76
ds_read_b128 v[44:47],v76 offset:32768
ds_read_b128 v[48:51],v76 offset:33792
ds_read_b128 v[52:55],v76 offset:34816
ds_read_b128 v[56:59],v76 offset:35840
ds_read_b128 v[60:63],v77 offset:32768
ds_read_b128 v[64:67],v77 offset:33792
ds_read_b128 v[68:71],v77 offset:34816
s_cmp_eq_u32 s54,1
s_cselect_b32 s68,s51,s50
s_lshr_b32 s69,s68,5
s_cmp_lt_u32 s68,4194304
s_cselect_b32 s68,0xffffcfff,-1
s_and_b32 s68,s68,s69
s_add_u32 m0,s78,0
s_nop 0
buffer_load_dword v6,s[36:39],s68 offen offset:0 sc1 lds
s_cmp_eq_u32 vcc_hi,-1
s_cselect_b32 s67,s50,s59
s_xor_b32 s62,s63,65536
s_mov_b32 m0,s62
s_nop 0
buffer_load_dwordx4 v0,s[36:39],s67 offen offset:0 sc1 lds
buffer_load_dwordx4 v0,s[36:39],s67 offen offset:1024 sc1 lds
buffer_load_dwordx4 v0,s[36:39],s67 offen offset:2048 sc1 lds
buffer_load_dwordx4 v0,s[36:39],s67 offen offset:3072 sc1 lds
s_add_u32 m0,s62,16384
s_nop 0
buffer_load_dwordx4 v1,s[36:39],s67 offen offset:0 sc1 lds
s_mov_b32 s46,s48
s_mov_b32 s48,s59
s_lshr_b32 s70,s59,5
buffer_store_dword v5,v4,s[36:39],s70 offen offset:0 sc0 sc1
s_waitcnt lgkmcnt(3)
s_setpc_b64 s[92:93]
Lg1_done1_%=:
s_waitcnt vmcnt(0)
s_barrier
s_lshr_b32 s70,s46,5
buffer_store_dword v5,v4,s[36:39],s70 offen offset:0 sc0 sc1
s_lshr_b32 s70,s47,5
buffer_store_dword v5,v4,s[36:39],s70 offen offset:0 sc0 sc1
s_lshr_b32 s70,s48,5
buffer_store_dword v5,v4,s[36:39],s70 offen offset:0 sc0 sc1
s_branch Lg1_done_%=
Lg1_done_%=:
s_waitcnt vmcnt(0) lgkmcnt(0)
s_barrier
s_branch Lg2_end_%=
Lg2_begin_%=:
Lcode_g2_b:
s_mov_b64 s[36:37],%1
s_mov_b32 s38,111193216
s_mov_b32 s39,0x00020000
s_mov_b32 s54,%2
s_mov_b32 s55,%3
s_mov_b32 s56,%4
s_mov_b32 s58,%5
s_mov_b32 s83,%6
s_mov_b32 s82,%7
s_mov_b32 s84,%8
v_mov_b32 v0,%9
v_mov_b32 v1,%10
v_mov_b32 v2,%11
v_mov_b32 v3,%12
v_mov_b32 v4,%13
v_mov_b32 v6,%14
v_mov_b32 v7,%15
v_mov_b32 v8,%16
v_mov_b32 v9,%17
v_mov_b32 v10,%18
v_mov_b32 v11,%19
v_mov_b32 v12,%20
v_mov_b32 v13,%21
v_mov_b32 v14,%22
v_mov_b32 v16,%23
v_mov_b32 v17,%24
v_mov_b32 v18,%25
v_mov_b32 v19,%26
v_mov_b32 v20,%27
s_mov_b64 s[40:41],%28
s_mov_b32 s42,2097152
s_mov_b32 s43,0x00020000
s_lshl_b32 s88,s54,11
s_mov_b32 s69,111193088
buffer_load_dword v5,off,s[36:39],s69 sc1
s_add_u32 m0,s88,0
s_add_u32 s61,s82,0
buffer_load_dwordx4 v16,s[40:43],s61 offen offset:0  lds
s_add_u32 m0,s88,1024
s_nop 0
buffer_load_dwordx4 v17,s[40:43],s61 offen offset:0  lds
s_add_u32 m0,s88,8064
s_nop 0
buffer_load_dwordx4 v16,s[40:43],s61 offen offset:128  lds
s_add_u32 m0,s88,9088
s_nop 0
buffer_load_dwordx4 v17,s[40:43],s61 offen offset:128  lds)"
R"(
s_add_u32 m0,s88,16384
s_add_u32 s61,s82,131072
buffer_load_dwordx4 v16,s[40:43],s61 offen offset:0  lds
s_add_u32 m0,s88,17408
s_nop 0
buffer_load_dwordx4 v17,s[40:43],s61 offen offset:0  lds
s_add_u32 m0,s88,24448
s_nop 0
buffer_load_dwordx4 v16,s[40:43],s61 offen offset:128  lds
s_add_u32 m0,s88,25472
s_nop 0
buffer_load_dwordx4 v17,s[40:43],s61 offen offset:128  lds
s_add_u32 m0,s88,32768
s_add_u32 s61,s82,262144
buffer_load_dwordx4 v16,s[40:43],s61 offen offset:0  lds
s_add_u32 m0,s88,33792
s_nop 0
buffer_load_dwordx4 v17,s[40:43],s61 offen offset:0  lds
s_add_u32 m0,s88,40832
s_nop 0
buffer_load_dwordx4 v16,s[40:43],s61 offen offset:128  lds
s_add_u32 m0,s88,41856
s_nop 0
buffer_load_dwordx4 v17,s[40:43],s61 offen offset:128  lds
s_add_u32 m0,s88,49152
s_add_u32 s61,s82,393216
buffer_load_dwordx4 v16,s[40:43],s61 offen offset:0  lds
s_add_u32 m0,s88,50176
s_nop 0
buffer_load_dwordx4 v17,s[40:43],s61 offen offset:0  lds
s_add_u32 m0,s88,57216
s_nop 0
buffer_load_dwordx4 v16,s[40:43],s61 offen offset:128  lds
s_add_u32 m0,s88,58240
s_nop 0
buffer_load_dwordx4 v17,s[40:43],s61 offen offset:128  lds
s_add_u32 m0,s88,65536
s_add_u32 s61,s82,524288
buffer_load_dwordx4 v16,s[40:43],s61 offen offset:0  lds
s_add_u32 m0,s88,66560
s_nop 0
buffer_load_dwordx4 v17,s[40:43],s61 offen offset:0  lds
s_add_u32 m0,s88,73600
s_nop 0
buffer_load_dwordx4 v16,s[40:43],s61 offen offset:128  lds
s_add_u32 m0,s88,74624
s_nop 0
buffer_load_dwordx4 v17,s[40:43],s61 offen offset:128  lds
s_add_u32 m0,s88,81920
s_add_u32 s61,s82,655360
buffer_load_dwordx4 v16,s[40:43],s61 offen offset:0  lds
s_add_u32 m0,s88,82944
s_nop 0
buffer_load_dwordx4 v17,s[40:43],s61 offen offset:0  lds
s_add_u32 m0,s88,89984
s_nop 0
buffer_load_dwordx4 v16,s[40:43],s61 offen offset:128  lds
s_add_u32 m0,s88,91008
s_nop 0
buffer_load_dwordx4 v17,s[40:43],s61 offen offset:128  lds
s_add_u32 m0,s88,98304
s_add_u32 s61,s82,786432
buffer_load_dwordx4 v16,s[40:43],s61 offen offset:0  lds
s_add_u32 m0,s88,99328
s_nop 0
buffer_load_dwordx4 v17,s[40:43],s61 offen offset:0  lds
s_add_u32 m0,s88,106368
s_nop 0
buffer_load_dwordx4 v16,s[40:43],s61 offen offset:128  lds
s_add_u32 m0,s88,107392
s_nop 0
buffer_load_dwordx4 v17,s[40:43],s61 offen offset:128  lds
v_add_u32 v24,49152,v18
v_add_u32 v25,49152,v19
v_add_u32 v26,98304,v18
v_add_u32 v27,98304,v19
s_waitcnt vmcnt(24)
s_barrier
s_add_u32 m0,s88,114688
s_add_u32 s61,s82,917504
buffer_load_dwordx4 v16,s[40:43],s61 offen offset:0  lds
s_add_u32 m0,s88,115712
ds_read_b64_tr_b16 a[0:1],v18 offset:0
buffer_load_dwordx4 v17,s[40:43],s61 offen offset:0  lds
s_add_u32 m0,s88,122752
ds_read_b64_tr_b16 a[2:3],v18 offset:512
buffer_load_dwordx4 v16,s[40:43],s61 offen offset:128  lds
s_add_u32 m0,s88,123776
ds_read_b64_tr_b16 a[4:5],v19 offset:0
buffer_load_dwordx4 v17,s[40:43],s61 offen offset:128  lds
ds_read_b64_tr_b16 a[6:7],v19 offset:512
ds_read_b64_tr_b16 a[8:9],v18 offset:4096
ds_read_b64_tr_b16 a[10:11],v18 offset:4608
ds_read_b64_tr_b16 a[12:13],v19 offset:4096
ds_read_b64_tr_b16 a[14:15],v19 offset:4608
s_waitcnt vmcnt(24)
s_barrier
s_add_u32 m0,s88,131072
s_add_u32 s61,s82,1048576
buffer_load_dwordx4 v16,s[40:43],s61 offen offset:0  lds
s_add_u32 m0,s88,132096
ds_read_b64_tr_b16 a[16:17],v18 offset:16384
buffer_load_dwordx4 v17,s[40:43],s61 offen offset:0  lds
s_add_u32 m0,s88,139136
ds_read_b64_tr_b16 a[18:19],v18 offset:16896
buffer_load_dwordx4 v16,s[40:43],s61 offen offset:128  lds
s_add_u32 m0,s88,140160
ds_read_b64_tr_b16 a[20:21],v19 offset:16384
buffer_load_dwordx4 v17,s[40:43],s61 offen offset:128  lds
ds_read_b64_tr_b16 a[22:23],v19 offset:16896
ds_read_b64_tr_b16 a[24:25],v18 offset:20480
ds_read_b64_tr_b16 a[26:27],v18 offset:20992
ds_read_b64_tr_b16 a[28:29],v19 offset:20480
ds_read_b64_tr_b16 a[30:31],v19 offset:20992
s_waitcnt lgkmcnt(8)
s_waitcnt vmcnt(24)
s_barrier
s_add_u32 m0,s88,0
s_add_u32 s61,s82,1179648
buffer_load_dwordx4 v16,s[40:43],s61 offen offset:0  lds
s_add_u32 m0,s88,1024
ds_read_b64_tr_b16 a[32:33],v18 offset:32768
buffer_load_dwordx4 v17,s[40:43],s61 offen offset:0  lds
s_add_u32 m0,s88,8064
ds_read_b64_tr_b16 a[34:35],v18 offset:33280
buffer_load_dwordx4 v16,s[40:43],s61 offen offset:128  lds
s_add_u32 m0,s88,9088
ds_read_b64_tr_b16 a[36:37],v19 offset:32768
buffer_load_dwordx4 v17,s[40:43],s61 offen offset:128  lds
ds_read_b64_tr_b16 a[38:39],v19 offset:33280
ds_read_b64_tr_b16 a[40:41],v18 offset:36864
ds_read_b64_tr_b16 a[42:43],v18 offset:37376
ds_read_b64_tr_b16 a[44:45],v19 offset:36864
ds_read_b64_tr_b16 a[46:47],v19 offset:37376
s_waitcnt lgkmcnt(8)
s_waitcnt vmcnt(24)
s_barrier
s_add_u32 m0,s88,16384
s_add_u32 s61,s82,1310720
buffer_load_dwordx4 v16,s[40:43],s61 offen offset:0  lds
s_add_u32 m0,s88,17408
ds_read_b64_tr_b16 a[48:49],v24 offset:0
buffer_load_dwordx4 v17,s[40:43],s61 offen offset:0  lds
s_add_u32 m0,s88,24448
ds_read_b64_tr_b16 a[50:51],v24 offset:512
buffer_load_dwordx4 v16,s[40:43],s61 offen offset:128  lds
s_add_u32 m0,s88,25472
ds_read_b64_tr_b16 a[52:53],v25 offset:0
buffer_load_dwordx4 v17,s[40:43],s61 offen offset:128  lds
ds_read_b64_tr_b16 a[54:55],v25 offset:512
ds_read_b64_tr_b16 a[56:57],v24 offset:4096
ds_read_b64_tr_b16 a[58:59],v24 offset:4608
ds_read_b64_tr_b16 a[60:61],v25 offset:4096
ds_read_b64_tr_b16 a[62:63],v25 offset:4608
s_waitcnt lgkmcnt(8)
s_waitcnt vmcnt(24)
s_barrier
s_add_u32 m0,s88,32768
s_add_u32 s61,s82,1441792
buffer_load_dwordx4 v16,s[40:43],s61 offen offset:0  lds
s_add_u32 m0,s88,33792
ds_read_b64_tr_b16 a[64:65],v24 offset:16384
buffer_load_dwordx4 v17,s[40:43],s61 offen offset:0  lds
s_add_u32 m0,s88,40832
ds_read_b64_tr_b16 a[66:67],v24 offset:16896
buffer_load_dwordx4 v16,s[40:43],s61 offen offset:128  lds
s_add_u32 m0,s88,41856
ds_read_b64_tr_b16 a[68:69],v25 offset:16384
buffer_load_dwordx4 v17,s[40:43],s61 offen offset:128  lds
ds_read_b64_tr_b16 a[70:71],v25 offset:16896
ds_read_b64_tr_b16 a[72:73],v24 offset:20480
ds_read_b64_tr_b16 a[74:75],v24 offset:20992
ds_read_b64_tr_b16 a[76:77],v25 offset:20480
ds_read_b64_tr_b16 a[78:79],v25 offset:20992
s_waitcnt lgkmcnt(8)
s_waitcnt vmcnt(24)
s_barrier
s_add_u32 m0,s88,49152
s_add_u32 s61,s82,1572864
buffer_load_dwordx4 v16,s[40:43],s61 offen offset:0  lds
s_add_u32 m0,s88,50176
ds_read_b64_tr_b16 a[80:81],v24 offset:32768
buffer_load_dwordx4 v17,s[40:43],s61 offen offset:0  lds
s_add_u32 m0,s88,57216
ds_read_b64_tr_b16 a[82:83],v24 offset:33280
buffer_load_dwordx4 v16,s[40:43],s61 offen offset:128  lds
s_add_u32 m0,s88,58240
ds_read_b64_tr_b16 a[84:85],v25 offset:32768
buffer_load_dwordx4 v17,s[40:43],s61 offen offset:128  lds
ds_read_b64_tr_b16 a[86:87],v25 offset:33280
ds_read_b64_tr_b16 a[88:89],v24 offset:36864
ds_read_b64_tr_b16 a[90:91],v24 offset:37376
ds_read_b64_tr_b16 a[92:93],v25 offset:36864
ds_read_b64_tr_b16 a[94:95],v25 offset:37376
s_waitcnt lgkmcnt(8)
s_waitcnt vmcnt(24)
s_barrier
s_add_u32 m0,s88,65536
s_add_u32 s61,s82,1703936
buffer_load_dwordx4 v16,s[40:43],s61 offen offset:0  lds)"
R"(
s_add_u32 m0,s88,66560
ds_read_b64_tr_b16 a[96:97],v26 offset:0
buffer_load_dwordx4 v17,s[40:43],s61 offen offset:0  lds
s_add_u32 m0,s88,73600
ds_read_b64_tr_b16 a[98:99],v26 offset:512
buffer_load_dwordx4 v16,s[40:43],s61 offen offset:128  lds
s_add_u32 m0,s88,74624
ds_read_b64_tr_b16 a[100:101],v27 offset:0
buffer_load_dwordx4 v17,s[40:43],s61 offen offset:128  lds
ds_read_b64_tr_b16 a[102:103],v27 offset:512
ds_read_b64_tr_b16 a[104:105],v26 offset:4096
ds_read_b64_tr_b16 a[106:107],v26 offset:4608
ds_read_b64_tr_b16 a[108:109],v27 offset:4096
ds_read_b64_tr_b16 a[110:111],v27 offset:4608
s_waitcnt lgkmcnt(8)
s_waitcnt vmcnt(24)
s_barrier
s_add_u32 m0,s88,81920
s_add_u32 s61,s82,1835008
buffer_load_dwordx4 v16,s[40:43],s61 offen offset:0  lds
s_add_u32 m0,s88,82944
ds_read_b64_tr_b16 a[112:113],v26 offset:16384
buffer_load_dwordx4 v17,s[40:43],s61 offen offset:0  lds
s_add_u32 m0,s88,89984
ds_read_b64_tr_b16 a[114:115],v26 offset:16896
buffer_load_dwordx4 v16,s[40:43],s61 offen offset:128  lds
s_add_u32 m0,s88,91008
ds_read_b64_tr_b16 a[116:117],v27 offset:16384
buffer_load_dwordx4 v17,s[40:43],s61 offen offset:128  lds
ds_read_b64_tr_b16 a[118:119],v27 offset:16896
ds_read_b64_tr_b16 a[120:121],v26 offset:20480
ds_read_b64_tr_b16 a[122:123],v26 offset:20992
ds_read_b64_tr_b16 a[124:125],v27 offset:20480
ds_read_b64_tr_b16 a[126:127],v27 offset:20992
s_waitcnt lgkmcnt(8)
s_waitcnt vmcnt(24)
s_barrier
s_add_u32 m0,s88,98304
s_add_u32 s61,s82,1966080
buffer_load_dwordx4 v16,s[40:43],s61 offen offset:0  lds
s_add_u32 m0,s88,99328
ds_read_b64_tr_b16 a[128:129],v26 offset:32768
buffer_load_dwordx4 v17,s[40:43],s61 offen offset:0  lds
s_add_u32 m0,s88,106368
ds_read_b64_tr_b16 a[130:131],v26 offset:33280
buffer_load_dwordx4 v16,s[40:43],s61 offen offset:128  lds
s_add_u32 m0,s88,107392
ds_read_b64_tr_b16 a[132:133],v27 offset:32768
buffer_load_dwordx4 v17,s[40:43],s61 offen offset:128  lds
ds_read_b64_tr_b16 a[134:135],v27 offset:33280
ds_read_b64_tr_b16 a[136:137],v26 offset:36864
ds_read_b64_tr_b16 a[138:139],v26 offset:37376
ds_read_b64_tr_b16 a[140:141],v27 offset:36864
ds_read_b64_tr_b16 a[142:143],v27 offset:37376
s_waitcnt lgkmcnt(8)
s_waitcnt vmcnt(24)
s_barrier
ds_read_b64_tr_b16 a[144:145],v18 offset:0
ds_read_b64_tr_b16 a[146:147],v18 offset:512
ds_read_b64_tr_b16 a[148:149],v19 offset:0
ds_read_b64_tr_b16 a[150:151],v19 offset:512
ds_read_b64_tr_b16 a[152:153],v18 offset:4096
ds_read_b64_tr_b16 a[154:155],v18 offset:4608
ds_read_b64_tr_b16 a[156:157],v19 offset:4096
ds_read_b64_tr_b16 a[158:159],v19 offset:4608
s_waitcnt lgkmcnt(8)
s_waitcnt vmcnt(20)
s_barrier
ds_read_b64_tr_b16 a[160:161],v18 offset:16384
ds_read_b64_tr_b16 a[162:163],v18 offset:16896
ds_read_b64_tr_b16 a[164:165],v19 offset:16384
ds_read_b64_tr_b16 a[166:167],v19 offset:16896
ds_read_b64_tr_b16 a[168:169],v18 offset:20480
ds_read_b64_tr_b16 a[170:171],v18 offset:20992
ds_read_b64_tr_b16 a[172:173],v19 offset:20480
ds_read_b64_tr_b16 a[174:175],v19 offset:20992
s_waitcnt lgkmcnt(8)
s_waitcnt vmcnt(16)
s_barrier
ds_read_b64_tr_b16 a[176:177],v18 offset:32768
ds_read_b64_tr_b16 a[178:179],v18 offset:33280
ds_read_b64_tr_b16 a[180:181],v19 offset:32768
ds_read_b64_tr_b16 a[182:183],v19 offset:33280
ds_read_b64_tr_b16 a[184:185],v18 offset:36864
ds_read_b64_tr_b16 a[186:187],v18 offset:37376
ds_read_b64_tr_b16 a[188:189],v19 offset:36864
ds_read_b64_tr_b16 a[190:191],v19 offset:37376
s_waitcnt lgkmcnt(8)
s_waitcnt vmcnt(12)
s_barrier
ds_read_b64_tr_b16 a[192:193],v24 offset:0
ds_read_b64_tr_b16 a[194:195],v24 offset:512
ds_read_b64_tr_b16 a[196:197],v25 offset:0
ds_read_b64_tr_b16 a[198:199],v25 offset:512
ds_read_b64_tr_b16 a[200:201],v24 offset:4096
ds_read_b64_tr_b16 a[202:203],v24 offset:4608
ds_read_b64_tr_b16 a[204:205],v25 offset:4096
ds_read_b64_tr_b16 a[206:207],v25 offset:4608
s_waitcnt lgkmcnt(8)
s_waitcnt vmcnt(8)
s_barrier
ds_read_b64_tr_b16 a[208:209],v24 offset:16384
ds_read_b64_tr_b16 a[210:211],v24 offset:16896
ds_read_b64_tr_b16 a[212:213],v25 offset:16384
ds_read_b64_tr_b16 a[214:215],v25 offset:16896
ds_read_b64_tr_b16 a[216:217],v24 offset:20480
ds_read_b64_tr_b16 a[218:219],v24 offset:20992
ds_read_b64_tr_b16 a[220:221],v25 offset:20480
ds_read_b64_tr_b16 a[222:223],v25 offset:20992
s_waitcnt lgkmcnt(8)
s_waitcnt vmcnt(4)
s_barrier
ds_read_b64_tr_b16 a[224:225],v24 offset:32768
ds_read_b64_tr_b16 a[226:227],v24 offset:33280
ds_read_b64_tr_b16 a[228:229],v25 offset:32768
ds_read_b64_tr_b16 a[230:231],v25 offset:33280
ds_read_b64_tr_b16 a[232:233],v24 offset:36864
ds_read_b64_tr_b16 a[234:235],v24 offset:37376
ds_read_b64_tr_b16 a[236:237],v25 offset:36864
ds_read_b64_tr_b16 a[238:239],v25 offset:37376
s_waitcnt lgkmcnt(8)
s_waitcnt vmcnt(0)
s_barrier
ds_read_b64_tr_b16 a[240:241],v26 offset:0
ds_read_b64_tr_b16 a[242:243],v26 offset:512
ds_read_b64_tr_b16 a[244:245],v27 offset:0
ds_read_b64_tr_b16 a[246:247],v27 offset:512
ds_read_b64_tr_b16 a[248:249],v26 offset:4096
ds_read_b64_tr_b16 a[250:251],v26 offset:4608
ds_read_b64_tr_b16 a[252:253],v27 offset:4096
ds_read_b64_tr_b16 a[254:255],v27 offset:4608
s_waitcnt lgkmcnt(8)
s_waitcnt lgkmcnt(0)
s_waitcnt lgkmcnt(0)
s_barrier
v_readfirstlane_b32 s60,v5
s_nop 3
s_add_u32 s60,s60,1
s_cmp_eq_u32 s60,0
s_cselect_b32 s60,1,s60
v_mov_b32 v5,s60
s_mov_b32 s59,536870912
s_add_u32 s57,s56,131072
s_mov_b32 s79,0
s_mov_b32 s75,0x3a800000
s_mov_b32 s76,0xc0135761
s_mov_b32 s77,0x3d372713
s_mov_b32 s44,s59
s_mov_b32 s45,s59
s_mov_b32 s46,s59
s_mov_b32 s47,s59
s_mov_b32 s48,s83
s_add_u32 s49,s48,s55
s_cmp_eq_u32 s49,s56
s_cselect_b32 s49,s57,s49
s_cmp_eq_u32 s49,s58
s_cselect_b32 s49,s59,s49
s_add_u32 s50,s49,s55
s_cmp_eq_u32 s50,s56
s_cselect_b32 s50,s57,s50
s_cmp_eq_u32 s50,s58
s_cselect_b32 s50,s59,s50
s_add_u32 s51,s50,s55
s_cmp_eq_u32 s51,s56
s_cselect_b32 s51,s57,s51
s_cmp_eq_u32 s51,s58
s_cselect_b32 s51,s59,s51
s_add_u32 s52,s51,s55
s_cmp_eq_u32 s52,s56
s_cselect_b32 s52,s57,s52
s_cmp_eq_u32 s52,s58
s_cselect_b32 s52,s59,s52
s_lshl_b32 s63,s54,12
s_add_u32 s63,s63,65536
s_add_u32 s64,s63,32768
s_lshl_b32 s88,s54,10
s_add_u32 s88,s88,131072
s_lshl_b32 s78,s54,11
s_add_u32 s78,s78,132608
s_getpc_b64 s[90:91]
Lg2_pc_%=:
s_mov_b32 s68,Lg2_d1w0_%=-Lg2_pc_%=
s_cmp_eq_u32 s54,1
s_cselect_b32 s68,Lg2_d1w1_%=-Lg2_pc_%=,s68
s_cmp_eq_u32 s54,2
s_cselect_b32 s68,Lg2_d1w2_%=-Lg2_pc_%=,s68
s_cmp_eq_u32 s54,3
s_cselect_b32 s68,Lg2_d1w3_%=-Lg2_pc_%=,s68
s_add_u32 s92,s90,s68
s_addc_u32 s93,s91,0
s_mov_b32 s68,Lg2_d0w0_%=-Lg2_pc_%=
s_cmp_eq_u32 s54,1
s_cselect_b32 s68,Lg2_d0w1_%=-Lg2_pc_%=,s68
s_cmp_eq_u32 s54,2
s_cselect_b32 s68,Lg2_d0w2_%=-Lg2_pc_%=,s68
s_cmp_eq_u32 s54,3
s_cselect_b32 s68,Lg2_d0w3_%=-Lg2_pc_%=,s68
s_add_u32 s90,s90,s68
s_addc_u32 s91,s91,0
s_mov_b32 s66,s59)"
R"(
s_mov_b32 s65,0
s_mov_b32 s62,s63
s_branch Lg2_slow0_%=
Lg2_d0w0_%=:
v_mfma_f32_16x16x32_bf16 v[16:19],a[0:3],v[32:35],0
s_add_u32 m0,s62,16384
ds_read_b128 v[60:63],v2 offset:7168
v_lshlrev_b32 v64,16,v84
v_mfma_f32_16x16x32_bf16 v[20:23],a[4:7],v[32:35],0
buffer_load_dwordx4 v1,s[36:39],s66 offen offset:2048 sc1 lds
v_mfma_f32_16x16x32_bf16 v[16:19],a[8:11],v[36:39],v[16:19]
s_lshr_b32 s72,s66,6
ds_read_b128 v[32:35],v2 offset:8192
v_and_b32 v65,0xffff0000,v84
v_mfma_f32_16x16x32_bf16 v[20:23],a[12:15],v[36:39],v[20:23]
s_lshr_b32 s71,s62,2
v_lshlrev_b32 v66,16,v85
v_and_b32 v67,0xffff0000,v85
v_mfma_f32_16x16x32_bf16 v[16:19],a[16:19],v[40:43],v[16:19]
ds_read_b128 v[36:39],v2 offset:9216
v_lshlrev_b32 v68,16,v86
v_mfma_f32_16x16x32_bf16 v[20:23],a[20:23],v[40:43],v[20:23]
buffer_load_dwordx4 v1,s[36:39],s66 offen offset:3072 sc1 lds
v_mfma_f32_16x16x32_bf16 v[16:19],a[24:27],v[44:47],v[16:19]
s_add_u32 m0,s71,s88
ds_read_b128 v[40:43],v2 offset:10240
v_and_b32 v69,0xffff0000,v86
v_mfma_f32_16x16x32_bf16 v[20:23],a[28:31],v[44:47],v[20:23]
s_waitcnt lgkmcnt(3)
v_lshlrev_b32 v70,16,v87
v_and_b32 v71,0xffff0000,v87
v_mfma_f32_16x16x32_bf16 v[16:19],a[32:35],v[48:51],v[16:19]
s_lshr_b32 s73,s47,0
ds_read_b128 v[44:47],v2 offset:11264
v_fma_f32 v64,v64,v91,v90
v_mfma_f32_16x16x32_bf16 v[20:23],a[36:39],v[48:51],v[20:23]
buffer_load_dwordx4 v10,s[36:39],s66 offen offset:0 sc1 lds
v_mfma_f32_16x16x32_bf16 v[16:19],a[40:43],v[52:55],v[16:19]
buffer_load_dword v11,s[36:39],s72 offen offset:1024 sc1 lds
v_mfma_f32_16x16x32_bf16 v[20:23],a[44:47],v[52:55],v[20:23]
s_lshr_b32 s68,s50,5
ds_read_b128 v[48:51],v2 offset:12288
v_fma_f32 v65,v65,v91,v90
v_mfma_f32_16x16x32_bf16 v[16:19],a[48:51],v[56:59],v[16:19]
s_add_u32 m0,s78,0
ds_read_b128 v[52:55],v2 offset:13312
v_fma_f32 v66,v66,v91,v90
v_mfma_f32_16x16x32_bf16 v[20:23],a[52:55],v[56:59],v[20:23]
s_xor_b32 s69,s64,65536
v_fma_f32 v67,v67,v91,v90
v_fma_f32 v68,v68,v91,v90
v_mfma_f32_16x16x32_bf16 v[16:19],a[56:59],v[60:63],v[16:19]
s_cmp_ge_u32 s66,s59
ds_read_b128 v[56:59],v2 offset:14336
v_fma_f32 v69,v69,v91,v90
v_mfma_f32_16x16x32_bf16 v[20:23],a[60:63],v[60:63],v[20:23]
s_waitcnt lgkmcnt(3)
v_fma_f32 v70,v70,v91,v90
v_fma_f32 v71,v71,v91,v90
v_mfma_f32_16x16x32_bf16 v[16:19],a[64:67],v[32:35],v[16:19]
s_waitcnt vmcnt(11)
ds_read_b128 v[60:63],v2 offset:15360
v_add_f32 v24,v24,v64
v_mfma_f32_16x16x32_bf16 v[20:23],a[68:71],v[32:35],v[20:23]
s_barrier
v_mfma_f32_16x16x32_bf16 v[16:19],a[72:75],v[36:39],v[16:19]
s_cselect_b32 s62,s69,s63
ds_read_b128 v[32:35],v2 offset:16384
ds_read_b32 v15,v7 offset:256
v_mfma_f32_16x16x32_bf16 v[20:23],a[76:79],v[36:39],v[20:23]
buffer_load_dword v6,s[36:39],s68 offen offset:0 sc1 lds
v_mfma_f32_16x16x32_bf16 v[16:19],a[80:83],v[40:43],v[16:19]
s_cselect_b32 s86,s49,s50
ds_read_b128 v[36:39],v2 offset:17408
v_add_f32 v25,v25,v65
v_mfma_f32_16x16x32_bf16 v[20:23],a[84:87],v[40:43],v[20:23]
s_mov_b32 m0,s62
v_add_f32 v26,v26,v66
v_add_f32 v27,v27,v67
v_mfma_f32_16x16x32_bf16 v[16:19],a[88:91],v[44:47],v[16:19]
s_waitcnt lgkmcnt(1)
ds_read_b128 v[40:43],v2 offset:18432
v_add_f32 v28,v28,v68
v_mfma_f32_16x16x32_bf16 v[20:23],a[92:95],v[44:47],v[20:23]
v_cmp_eq_u32 vcc,s60,v15
v_add_f32 v29,v29,v69
v_add_f32 v30,v30,v70
v_add_f32 v31,v31,v71
v_mfma_f32_16x16x32_bf16 v[16:19],a[96:99],v[48:51],v[16:19]
s_cselect_b32 s87,vcc_lo,vcc_hi
ds_read_b128 v[44:47],v2 offset:19456
v_cvt_pk_bf16_f32 v72,v24,v25
v_mfma_f32_16x16x32_bf16 v[20:23],a[100:103],v[48:51],v[20:23]
s_cmp_eq_u32 s87,-1
v_cvt_pk_bf16_f32 v73,v26,v27
v_cvt_pk_bf16_f32 v74,v28,v29
v_mfma_f32_16x16x32_bf16 v[16:19],a[104:107],v[52:55],v[16:19]
s_cselect_b32 s67,s86,s59
ds_read_b128 v[48:51],v2 offset:20480
v_cvt_pk_bf16_f32 v75,v30,v31
v_mfma_f32_16x16x32_bf16 v[20:23],a[108:111],v[52:55],v[20:23]
buffer_load_dwordx4 v0,s[36:39],s67 offen offset:0 sc1 lds
v_mfma_f32_16x16x32_bf16 v[16:19],a[112:115],v[56:59],v[16:19]
s_xor_b32 s63,s63,65536
ds_read_b128 v[52:55],v2 offset:21504
ds_read_b128 v[80:83],v8 offset:0
v_mfma_f32_16x16x32_bf16 v[20:23],a[116:119],v[56:59],v[20:23]
s_add_u32 s53,s52,s55
ds_read_b64 v[88:89],v9 offset:1024
v_add_f32 v64,v24,v25
v_mfma_f32_16x16x32_bf16 v[16:19],a[120:123],v[60:63],v[16:19]
s_cmp_eq_u32 s53,s56
ds_read_b128 v[56:59],v2 offset:22528
v_mul_f32 v65,v24,v24
v_mfma_f32_16x16x32_bf16 v[20:23],a[124:127],v[60:63],v[20:23]
s_waitcnt lgkmcnt(5)
buffer_load_dwordx4 v0,s[36:39],s67 offen offset:1024 sc1 lds
v_mfma_f32_16x16x32_bf16 v[16:19],a[128:131],v[32:35],v[16:19]
s_cselect_b32 s53,s57,s53
ds_read_b128 v[60:63],v2 offset:23552
v_fma_f32 v65,v25,v25,v65
v_mfma_f32_16x16x32_bf16 v[20:23],a[132:135],v[32:35],v[20:23]
s_cmp_eq_u32 s53,s58
v_add_f32 v64,v64,v26
v_fma_f32 v65,v26,v26,v65
v_mfma_f32_16x16x32_bf16 v[16:19],a[136:139],v[36:39],v[16:19]
s_cselect_b32 s53,s59,s53
ds_read_b128 v[32:35],v2 offset:24576
v_add_f32 v64,v64,v27
v_mfma_f32_16x16x32_bf16 v[20:23],a[140:143],v[36:39],v[20:23]
s_waitcnt lgkmcnt(3)
buffer_load_dwordx4 v0,s[36:39],s67 offen offset:2048 sc1 lds
v_mfma_f32_16x16x32_bf16 v[16:19],a[144:147],v[40:43],v[16:19]
ds_read_b128 v[36:39],v2 offset:25600
v_fma_f32 v65,v27,v27,v65
v_mfma_f32_16x16x32_bf16 v[20:23],a[148:151],v[40:43],v[20:23]
v_add_f32 v64,v64,v28
v_fma_f32 v65,v28,v28,v65
v_mfma_f32_16x16x32_bf16 v[16:19],a[152:155],v[44:47],v[16:19]
ds_read_b128 v[40:43],v2 offset:26624
v_add_f32 v64,v64,v29
v_mfma_f32_16x16x32_bf16 v[20:23],a[156:159],v[44:47],v[20:23]
s_waitcnt lgkmcnt(3)
buffer_load_dwordx4 v0,s[36:39],s67 offen offset:3072 sc1 lds
v_mfma_f32_16x16x32_bf16 v[16:19],a[160:163],v[48:51],v[16:19]
s_add_u32 m0,s62,16384
ds_read_b128 v[44:47],v2 offset:27648
v_fma_f32 v65,v29,v29,v65
v_mfma_f32_16x16x32_bf16 v[20:23],a[164:167],v[48:51],v[20:23]
s_cmp_ge_u32 s66,s59
v_add_f32 v64,v64,v30
v_fma_f32 v65,v30,v30,v65
v_add_f32 v64,v64,v31
v_fma_f32 v65,v31,v31,v65
v_mfma_f32_16x16x32_bf16 v[16:19],a[168:171],v[52:55],v[16:19]
s_nop 0
v_permlane16_swap_b32 v64,v65
v_add_f32 v64,v64,v65
v_mov_b32 v65,v64
v_mfma_f32_16x16x32_bf16 v[20:23],a[172:175],v[52:55],v[20:23]
ds_read_b128 v[48:51],v2 offset:28672
buffer_load_dwordx4 v1,s[36:39],s67 offen offset:0 sc1 lds
v_permlane32_swap_b32 v64,v65
v_add_f32 v64,v64,v65
v_mfma_f32_16x16x32_bf16 v[16:19],a[176:179],v[56:59],v[16:19]
ds_write_b32 v13,v64 offset:0
v_mfma_f32_16x16x32_bf16 v[20:23],a[180:183],v[56:59],v[20:23]
ds_read_b128 v[52:55],v2 offset:29696
v_mfma_f32_16x16x32_bf16 v[16:19],a[184:187],v[60:63],v[16:19]
ds_read_b128 v[56:59],v2 offset:30720
v_mfma_f32_16x16x32_bf16 v[20:23],a[188:191],v[60:63],v[20:23]
s_waitcnt lgkmcnt(4)
buffer_load_dwordx4 v1,s[36:39],s67 offen offset:1024 sc1 lds
v_mfma_f32_16x16x32_bf16 v[16:19],a[192:195],v[32:35],v[16:19]
s_waitcnt vmcnt(14) lgkmcnt(2)
ds_read_b128 v[60:63],v2 offset:31744
v_mfma_f32_16x16x32_bf16 v[20:23],a[196:199],v[32:35],v[20:23]
s_barrier
v_mfma_f32_16x16x32_bf16 v[16:19],a[200:203],v[36:39],v[16:19]
ds_read_b128 v[32:35],v2 offset:32768
v_mfma_f32_16x16x32_bf16 v[20:23],a[204:207],v[36:39],v[20:23]
v_mfma_f32_16x16x32_bf16 v[16:19],a[208:211],v[40:43],v[16:19]
ds_read_b128 v[36:39],v2 offset:33792
v_mfma_f32_16x16x32_bf16 v[20:23],a[212:215],v[40:43],v[20:23]
v_mfma_f32_16x16x32_bf16 v[16:19],a[216:219],v[44:47],v[16:19]
ds_read_b128 v[40:43],v2 offset:34816
v_mfma_f32_16x16x32_bf16 v[20:23],a[220:223],v[44:47],v[20:23]
s_waitcnt lgkmcnt(3)
buffer_store_dwordx4 v[72:75],v3,s[36:39],s73 offen offset:0 sc0 sc1
v_mfma_f32_16x16x32_bf16 v[16:19],a[224:227],v[48:51],v[16:19]
ds_read_b128 v[44:47],v2 offset:35840
v_mfma_f32_16x16x32_bf16 v[20:23],a[228:231],v[48:51],v[20:23]
v_mfma_f32_16x16x32_bf16 v[16:19],a[232:235],v[52:55],v[16:19]
ds_read_b128 v[48:51],v2 offset:36864
v_mfma_f32_16x16x32_bf16 v[20:23],a[236:239],v[52:55],v[20:23]
v_mfma_f32_16x16x32_bf16 v[16:19],a[240:243],v[56:59],v[16:19]
ds_read_b128 v[52:55],v2 offset:37888
v_mfma_f32_16x16x32_bf16 v[20:23],a[244:247],v[56:59],v[20:23]
v_mfma_f32_16x16x32_bf16 v[16:19],a[248:251],v[60:63],v[16:19]
s_waitcnt lgkmcnt(2))"
R"(
ds_read_b128 v[56:59],v2 offset:38912
v_mfma_f32_16x16x32_bf16 v[20:23],a[252:255],v[60:63],v[20:23]
s_cbranch_scc1 Lg2_slow1_%=
Lg2_d1w0_%=:
v_mfma_f32_16x16x32_bf16 v[24:27],a[0:3],v[32:35],0
s_add_u32 m0,s62,16384
ds_read_b128 v[60:63],v2 offset:39936
v_lshlrev_b32 v64,16,v80
v_mfma_f32_16x16x32_bf16 v[28:31],a[4:7],v[32:35],0
buffer_load_dwordx4 v1,s[36:39],s67 offen offset:2048 sc1 lds
v_mfma_f32_16x16x32_bf16 v[24:27],a[8:11],v[36:39],v[24:27]
s_lshr_b32 s72,s67,6
ds_read_b128 v[32:35],v2 offset:40960
v_and_b32 v65,0xffff0000,v80
v_mfma_f32_16x16x32_bf16 v[28:31],a[12:15],v[36:39],v[28:31]
s_lshr_b32 s71,s62,2
v_lshlrev_b32 v66,16,v81
v_and_b32 v67,0xffff0000,v81
v_mfma_f32_16x16x32_bf16 v[24:27],a[16:19],v[40:43],v[24:27]
s_mov_b64 s[44:45],s[46:47]
ds_read_b128 v[36:39],v2 offset:41984
v_lshlrev_b32 v68,16,v82
v_mfma_f32_16x16x32_bf16 v[28:31],a[20:23],v[40:43],v[28:31]
buffer_load_dwordx4 v1,s[36:39],s67 offen offset:3072 sc1 lds
v_mfma_f32_16x16x32_bf16 v[24:27],a[24:27],v[44:47],v[24:27]
s_add_u32 m0,s71,s88
ds_read_b128 v[40:43],v2 offset:43008
v_and_b32 v69,0xffff0000,v82
v_mfma_f32_16x16x32_bf16 v[28:31],a[28:31],v[44:47],v[28:31]
s_waitcnt lgkmcnt(3)
v_lshlrev_b32 v70,16,v83
v_and_b32 v71,0xffff0000,v83
v_mfma_f32_16x16x32_bf16 v[24:27],a[32:35],v[48:51],v[24:27]
s_lshr_b32 s73,s48,0
ds_read_b128 v[44:47],v2 offset:44032
v_fma_f32 v64,v64,v89,v88
v_mfma_f32_16x16x32_bf16 v[28:31],a[36:39],v[48:51],v[28:31]
buffer_load_dwordx4 v10,s[36:39],s67 offen offset:0 sc1 lds
v_mfma_f32_16x16x32_bf16 v[24:27],a[40:43],v[52:55],v[24:27]
buffer_load_dword v11,s[36:39],s72 offen offset:1024 sc1 lds
v_mfma_f32_16x16x32_bf16 v[28:31],a[44:47],v[52:55],v[28:31]
s_lshr_b32 s68,s51,5
ds_read_b128 v[48:51],v2 offset:45056
v_fma_f32 v65,v65,v89,v88
v_mfma_f32_16x16x32_bf16 v[24:27],a[48:51],v[56:59],v[24:27]
s_add_u32 m0,s78,256
ds_read_b128 v[52:55],v2 offset:46080
v_fma_f32 v66,v66,v89,v88
v_mfma_f32_16x16x32_bf16 v[28:31],a[52:55],v[56:59],v[28:31]
s_xor_b32 s69,s63,65536
v_fma_f32 v67,v67,v89,v88
v_fma_f32 v68,v68,v89,v88
v_mfma_f32_16x16x32_bf16 v[24:27],a[56:59],v[60:63],v[24:27]
s_cmp_ge_u32 s67,s59
ds_read_b128 v[56:59],v2 offset:47104
v_fma_f32 v69,v69,v89,v88
v_mfma_f32_16x16x32_bf16 v[28:31],a[60:63],v[60:63],v[28:31]
s_waitcnt lgkmcnt(3)
v_fma_f32 v70,v70,v89,v88
v_fma_f32 v71,v71,v89,v88
v_mfma_f32_16x16x32_bf16 v[24:27],a[64:67],v[32:35],v[24:27]
s_waitcnt vmcnt(11)
ds_read_b128 v[60:63],v2 offset:48128
v_add_f32 v16,v16,v64
v_mfma_f32_16x16x32_bf16 v[28:31],a[68:71],v[32:35],v[28:31]
s_barrier
v_mfma_f32_16x16x32_bf16 v[24:27],a[72:75],v[36:39],v[24:27]
s_cselect_b32 s62,s69,s64
ds_read_b128 v[32:35],v2 offset:49152
ds_read_b32 v15,v7 offset:0
v_mfma_f32_16x16x32_bf16 v[28:31],a[76:79],v[36:39],v[28:31]
buffer_load_dword v6,s[36:39],s68 offen offset:0 sc1 lds
v_mfma_f32_16x16x32_bf16 v[24:27],a[80:83],v[40:43],v[24:27]
s_cselect_b32 s86,s50,s51
ds_read_b128 v[36:39],v2 offset:50176
v_add_f32 v17,v17,v65
v_mfma_f32_16x16x32_bf16 v[28:31],a[84:87],v[40:43],v[28:31]
s_mov_b32 m0,s62
v_add_f32 v18,v18,v66
v_add_f32 v19,v19,v67
v_mfma_f32_16x16x32_bf16 v[24:27],a[88:91],v[44:47],v[24:27]
s_waitcnt lgkmcnt(1)
ds_read_b128 v[40:43],v2 offset:51200
v_add_f32 v20,v20,v68
v_mfma_f32_16x16x32_bf16 v[28:31],a[92:95],v[44:47],v[28:31]
v_cmp_eq_u32 vcc,s60,v15
v_add_f32 v21,v21,v69
v_add_f32 v22,v22,v70
v_add_f32 v23,v23,v71
v_mfma_f32_16x16x32_bf16 v[24:27],a[96:99],v[48:51],v[24:27]
s_cselect_b32 s87,vcc_lo,vcc_hi
ds_read_b128 v[44:47],v2 offset:52224
v_cvt_pk_bf16_f32 v72,v16,v17
v_mfma_f32_16x16x32_bf16 v[28:31],a[100:103],v[48:51],v[28:31]
s_cmp_eq_u32 s87,-1
v_cvt_pk_bf16_f32 v73,v18,v19
v_cvt_pk_bf16_f32 v74,v20,v21
v_mfma_f32_16x16x32_bf16 v[24:27],a[104:107],v[52:55],v[24:27]
s_cselect_b32 s66,s86,s59
ds_read_b128 v[48:51],v2 offset:53248
v_cvt_pk_bf16_f32 v75,v22,v23
v_mfma_f32_16x16x32_bf16 v[28:31],a[108:111],v[52:55],v[28:31]
buffer_load_dwordx4 v0,s[36:39],s66 offen offset:0 sc1 lds
v_mfma_f32_16x16x32_bf16 v[24:27],a[112:115],v[56:59],v[24:27]
s_xor_b32 s64,s64,65536
ds_read_b128 v[52:55],v2 offset:54272
ds_read_b128 v[84:87],v8 offset:8192
v_mfma_f32_16x16x32_bf16 v[28:31],a[116:119],v[56:59],v[28:31]
s_add_u32 s74,s53,s55
ds_read_b64 v[90:91],v9 offset:9216
v_add_f32 v64,v16,v17
v_mfma_f32_16x16x32_bf16 v[24:27],a[120:123],v[60:63],v[24:27]
s_cmp_eq_u32 s74,s56
ds_read_b128 v[56:59],v2 offset:55296
v_mul_f32 v65,v16,v16
v_mfma_f32_16x16x32_bf16 v[28:31],a[124:127],v[60:63],v[28:31]
s_waitcnt lgkmcnt(5)
buffer_load_dwordx4 v0,s[36:39],s66 offen offset:1024 sc1 lds
v_mfma_f32_16x16x32_bf16 v[24:27],a[128:131],v[32:35],v[24:27]
s_cselect_b32 s74,s57,s74
ds_read_b128 v[60:63],v2 offset:56320
v_fma_f32 v65,v17,v17,v65
v_mfma_f32_16x16x32_bf16 v[28:31],a[132:135],v[32:35],v[28:31]
s_cmp_eq_u32 s74,s58
v_add_f32 v64,v64,v18
v_fma_f32 v65,v18,v18,v65
v_mfma_f32_16x16x32_bf16 v[24:27],a[136:139],v[36:39],v[24:27]
s_cselect_b32 s74,s59,s74
ds_read_b128 v[32:35],v2 offset:57344
v_add_f32 v64,v64,v19
v_mfma_f32_16x16x32_bf16 v[28:31],a[140:143],v[36:39],v[28:31]
s_waitcnt lgkmcnt(3)
buffer_load_dwordx4 v0,s[36:39],s66 offen offset:2048 sc1 lds
v_mfma_f32_16x16x32_bf16 v[24:27],a[144:147],v[40:43],v[24:27]
s_mov_b64 s[46:47],s[48:49]
ds_read_b128 v[36:39],v2 offset:58368
v_fma_f32 v65,v19,v19,v65
v_mfma_f32_16x16x32_bf16 v[28:31],a[148:151],v[40:43],v[28:31]
s_mov_b64 s[48:49],s[50:51]
v_add_f32 v64,v64,v20
v_fma_f32 v65,v20,v20,v65
v_mfma_f32_16x16x32_bf16 v[24:27],a[152:155],v[44:47],v[24:27]
s_mov_b64 s[50:51],s[52:53]
ds_read_b128 v[40:43],v2 offset:59392
v_add_f32 v64,v64,v21
v_mfma_f32_16x16x32_bf16 v[28:31],a[156:159],v[44:47],v[28:31]
s_waitcnt lgkmcnt(3)
buffer_load_dwordx4 v0,s[36:39],s66 offen offset:3072 sc1 lds
v_mfma_f32_16x16x32_bf16 v[24:27],a[160:163],v[48:51],v[24:27]
s_add_u32 m0,s62,16384
ds_read_b128 v[44:47],v2 offset:60416
v_fma_f32 v65,v21,v21,v65
v_mfma_f32_16x16x32_bf16 v[28:31],a[164:167],v[48:51],v[28:31]
s_mov_b32 s52,s74
v_add_f32 v64,v64,v22
v_fma_f32 v65,v22,v22,v65
v_add_f32 v64,v64,v23
v_fma_f32 v65,v23,v23,v65
v_mfma_f32_16x16x32_bf16 v[24:27],a[168:171],v[52:55],v[24:27]
s_cmp_lt_u32 s67,s59
v_permlane16_swap_b32 v64,v65
v_add_f32 v64,v64,v65
v_mov_b32 v65,v64
v_mfma_f32_16x16x32_bf16 v[28:31],a[172:175],v[52:55],v[28:31]
ds_read_b128 v[48:51],v2 offset:61440
buffer_load_dwordx4 v1,s[36:39],s66 offen offset:0 sc1 lds
v_permlane32_swap_b32 v64,v65
v_add_f32 v64,v64,v65
v_mfma_f32_16x16x32_bf16 v[24:27],a[176:179],v[56:59],v[24:27]
ds_write_b32 v13,v64 offset:2048
v_mfma_f32_16x16x32_bf16 v[28:31],a[180:183],v[56:59],v[28:31]
ds_read_b128 v[52:55],v2 offset:62464
v_xor_b32 v8,16384,v8
v_mfma_f32_16x16x32_bf16 v[24:27],a[184:187],v[60:63],v[24:27]
ds_read_b128 v[56:59],v2 offset:63488
v_xor_b32 v9,16384,v9
v_mfma_f32_16x16x32_bf16 v[28:31],a[188:191],v[60:63],v[28:31]
s_waitcnt lgkmcnt(4)
buffer_load_dwordx4 v1,s[36:39],s66 offen offset:1024 sc1 lds
v_mfma_f32_16x16x32_bf16 v[24:27],a[192:195],v[32:35],v[24:27]
s_waitcnt vmcnt(14) lgkmcnt(2)
ds_read_b128 v[60:63],v2 offset:64512
v_xor_b32 v2,65536,v2
v_mfma_f32_16x16x32_bf16 v[28:31],a[196:199],v[32:35],v[28:31]
s_barrier
v_mfma_f32_16x16x32_bf16 v[24:27],a[200:203],v[36:39],v[24:27]
ds_read_b128 v[32:35],v2 offset:0
v_mfma_f32_16x16x32_bf16 v[28:31],a[204:207],v[36:39],v[28:31]
v_mfma_f32_16x16x32_bf16 v[24:27],a[208:211],v[40:43],v[24:27]
ds_read_b128 v[36:39],v2 offset:1024
v_mfma_f32_16x16x32_bf16 v[28:31],a[212:215],v[40:43],v[28:31]
v_mfma_f32_16x16x32_bf16 v[24:27],a[216:219],v[44:47],v[24:27]
ds_read_b128 v[40:43],v2 offset:2048
v_mfma_f32_16x16x32_bf16 v[28:31],a[220:223],v[44:47],v[28:31]
s_waitcnt lgkmcnt(3)
buffer_store_dwordx4 v[72:75],v3,s[36:39],s73 offen offset:0 sc0 sc1
v_mfma_f32_16x16x32_bf16 v[24:27],a[224:227],v[48:51],v[24:27]
ds_read_b128 v[44:47],v2 offset:3072
v_mfma_f32_16x16x32_bf16 v[28:31],a[228:231],v[48:51],v[28:31]
v_mfma_f32_16x16x32_bf16 v[24:27],a[232:235],v[52:55],v[24:27])"
R"(
ds_read_b128 v[48:51],v2 offset:4096
v_mfma_f32_16x16x32_bf16 v[28:31],a[236:239],v[52:55],v[28:31]
v_mfma_f32_16x16x32_bf16 v[24:27],a[240:243],v[56:59],v[24:27]
ds_read_b128 v[52:55],v2 offset:5120
v_mfma_f32_16x16x32_bf16 v[28:31],a[244:247],v[56:59],v[28:31]
v_mfma_f32_16x16x32_bf16 v[24:27],a[248:251],v[60:63],v[24:27]
s_waitcnt lgkmcnt(2)
ds_read_b128 v[56:59],v2 offset:6144
v_mfma_f32_16x16x32_bf16 v[28:31],a[252:255],v[60:63],v[28:31]
s_cbranch_scc1 Lg2_d0w0_%=
s_branch Lg2_slow0_%=
Lg2_d0w1_%=:
v_mfma_f32_16x16x32_bf16 v[16:19],a[0:3],v[32:35],0
s_add_u32 m0,s62,16384
ds_read_b128 v[60:63],v2 offset:7168
v_lshlrev_b32 v64,16,v84
v_mfma_f32_16x16x32_bf16 v[20:23],a[4:7],v[32:35],0
s_lshr_b32 s72,s66,6
v_and_b32 v65,0xffff0000,v84
v_lshlrev_b32 v66,16,v85
v_mfma_f32_16x16x32_bf16 v[16:19],a[8:11],v[36:39],v[16:19]
buffer_load_dwordx4 v1,s[36:39],s66 offen offset:2048 sc1 lds
v_mfma_f32_16x16x32_bf16 v[20:23],a[12:15],v[36:39],v[20:23]
s_lshr_b32 s71,s62,2
ds_read_b128 v[32:35],v2 offset:8192
v_and_b32 v67,0xffff0000,v85
v_mfma_f32_16x16x32_bf16 v[16:19],a[16:19],v[40:43],v[16:19]
ds_read_b128 v[36:39],v2 offset:9216
v_lshlrev_b32 v68,16,v86
v_mfma_f32_16x16x32_bf16 v[20:23],a[20:23],v[40:43],v[20:23]
v_and_b32 v69,0xffff0000,v86
v_lshlrev_b32 v70,16,v87
v_mfma_f32_16x16x32_bf16 v[16:19],a[24:27],v[44:47],v[16:19]
buffer_load_dwordx4 v1,s[36:39],s66 offen offset:3072 sc1 lds
v_mfma_f32_16x16x32_bf16 v[20:23],a[28:31],v[44:47],v[20:23]
s_waitcnt lgkmcnt(2)
ds_read_b128 v[40:43],v2 offset:10240
v_and_b32 v71,0xffff0000,v87
v_mfma_f32_16x16x32_bf16 v[16:19],a[32:35],v[48:51],v[16:19]
s_add_u32 m0,s71,s88
ds_read_b128 v[44:47],v2 offset:11264
v_fma_f32 v64,v64,v91,v90
v_mfma_f32_16x16x32_bf16 v[20:23],a[36:39],v[48:51],v[20:23]
s_lshr_b32 s73,s47,0
v_fma_f32 v65,v65,v91,v90
v_fma_f32 v66,v66,v91,v90
v_mfma_f32_16x16x32_bf16 v[16:19],a[40:43],v[52:55],v[16:19]
buffer_load_dwordx4 v10,s[36:39],s66 offen offset:0 sc1 lds
v_mfma_f32_16x16x32_bf16 v[20:23],a[44:47],v[52:55],v[20:23]
s_lshr_b32 s68,s51,5
ds_read_b128 v[48:51],v2 offset:12288
v_fma_f32 v67,v67,v91,v90
v_mfma_f32_16x16x32_bf16 v[16:19],a[48:51],v[56:59],v[16:19]
buffer_load_dword v11,s[36:39],s72 offen offset:1024 sc1 lds
v_mfma_f32_16x16x32_bf16 v[20:23],a[52:55],v[56:59],v[20:23]
s_add_u32 m0,s78,0
ds_read_b128 v[52:55],v2 offset:13312
v_fma_f32 v68,v68,v91,v90
v_mfma_f32_16x16x32_bf16 v[16:19],a[56:59],v[60:63],v[16:19]
s_xor_b32 s69,s64,65536
ds_read_b128 v[56:59],v2 offset:14336
v_fma_f32 v69,v69,v91,v90
v_mfma_f32_16x16x32_bf16 v[20:23],a[60:63],v[60:63],v[20:23]
s_waitcnt lgkmcnt(3)
v_fma_f32 v70,v70,v91,v90
v_fma_f32 v71,v71,v91,v90
v_mfma_f32_16x16x32_bf16 v[16:19],a[64:67],v[32:35],v[16:19]
s_waitcnt vmcnt(11)
ds_read_b128 v[60:63],v2 offset:15360
v_add_f32 v24,v24,v64
v_mfma_f32_16x16x32_bf16 v[20:23],a[68:71],v[32:35],v[20:23]
s_barrier
v_mfma_f32_16x16x32_bf16 v[16:19],a[72:75],v[36:39],v[16:19]
s_cmp_ge_u32 s66,s59
ds_read_b128 v[32:35],v2 offset:16384
ds_read_b32 v15,v7 offset:256
v_mfma_f32_16x16x32_bf16 v[20:23],a[76:79],v[36:39],v[20:23]
s_cselect_b32 s62,s69,s63
v_add_f32 v25,v25,v65
v_add_f32 v26,v26,v66
v_mfma_f32_16x16x32_bf16 v[16:19],a[80:83],v[40:43],v[16:19]
s_cselect_b32 s86,s49,s50
ds_read_b128 v[36:39],v2 offset:17408
v_add_f32 v27,v27,v67
v_mfma_f32_16x16x32_bf16 v[20:23],a[84:87],v[40:43],v[20:23]
buffer_load_dword v6,s[36:39],s68 offen offset:0 sc1 lds
v_mfma_f32_16x16x32_bf16 v[16:19],a[88:91],v[44:47],v[16:19]
s_waitcnt lgkmcnt(1)
ds_read_b128 v[40:43],v2 offset:18432
v_add_f32 v28,v28,v68
v_mfma_f32_16x16x32_bf16 v[20:23],a[92:95],v[44:47],v[20:23]
v_cmp_eq_u32 vcc,s60,v15
v_add_f32 v29,v29,v69
v_add_f32 v30,v30,v70
v_add_f32 v31,v31,v71
v_mfma_f32_16x16x32_bf16 v[16:19],a[96:99],v[48:51],v[16:19]
s_mov_b32 m0,s62
ds_read_b128 v[44:47],v2 offset:19456
v_cvt_pk_bf16_f32 v72,v24,v25
v_mfma_f32_16x16x32_bf16 v[20:23],a[100:103],v[48:51],v[20:23]
s_cselect_b32 s87,vcc_lo,vcc_hi
v_cvt_pk_bf16_f32 v73,v26,v27
v_cvt_pk_bf16_f32 v74,v28,v29
v_mfma_f32_16x16x32_bf16 v[16:19],a[104:107],v[52:55],v[16:19]
s_cmp_eq_u32 s87,-1
ds_read_b128 v[48:51],v2 offset:20480
v_cvt_pk_bf16_f32 v75,v30,v31
v_mfma_f32_16x16x32_bf16 v[20:23],a[108:111],v[52:55],v[20:23]
s_cselect_b32 s67,s86,s59
v_add_f32 v64,v24,v25
v_mul_f32 v65,v24,v24
v_mfma_f32_16x16x32_bf16 v[16:19],a[112:115],v[56:59],v[16:19]
buffer_load_dwordx4 v0,s[36:39],s67 offen offset:0 sc1 lds
v_mfma_f32_16x16x32_bf16 v[20:23],a[116:119],v[56:59],v[20:23]
s_xor_b32 s63,s63,65536
ds_read_b128 v[52:55],v2 offset:21504
ds_read_b128 v[80:83],v8 offset:0
v_mfma_f32_16x16x32_bf16 v[16:19],a[120:123],v[60:63],v[16:19]
s_add_u32 s53,s52,s55
ds_read_b128 v[56:59],v2 offset:22528
ds_read_b64 v[88:89],v9 offset:1024
v_mfma_f32_16x16x32_bf16 v[20:23],a[124:127],v[60:63],v[20:23]
s_waitcnt lgkmcnt(5)
v_fma_f32 v65,v25,v25,v65
v_add_f32 v64,v64,v26
v_mfma_f32_16x16x32_bf16 v[16:19],a[128:131],v[32:35],v[16:19]
buffer_load_dwordx4 v0,s[36:39],s67 offen offset:1024 sc1 lds
v_mfma_f32_16x16x32_bf16 v[20:23],a[132:135],v[32:35],v[20:23]
s_cmp_eq_u32 s53,s56
ds_read_b128 v[60:63],v2 offset:23552
v_fma_f32 v65,v26,v26,v65
v_mfma_f32_16x16x32_bf16 v[16:19],a[136:139],v[36:39],v[16:19]
s_cselect_b32 s53,s57,s53
ds_read_b128 v[32:35],v2 offset:24576
v_add_f32 v64,v64,v27
v_mfma_f32_16x16x32_bf16 v[20:23],a[140:143],v[36:39],v[20:23]
s_cmp_eq_u32 s53,s58
v_fma_f32 v65,v27,v27,v65
v_add_f32 v64,v64,v28
v_mfma_f32_16x16x32_bf16 v[16:19],a[144:147],v[40:43],v[16:19]
s_waitcnt lgkmcnt(2)
buffer_load_dwordx4 v0,s[36:39],s67 offen offset:2048 sc1 lds
v_mfma_f32_16x16x32_bf16 v[20:23],a[148:151],v[40:43],v[20:23]
s_cselect_b32 s53,s59,s53
ds_read_b128 v[36:39],v2 offset:25600
v_fma_f32 v65,v28,v28,v65
v_mfma_f32_16x16x32_bf16 v[16:19],a[152:155],v[44:47],v[16:19]
ds_read_b128 v[40:43],v2 offset:26624
v_add_f32 v64,v64,v29
v_mfma_f32_16x16x32_bf16 v[20:23],a[156:159],v[44:47],v[20:23]
s_waitcnt lgkmcnt(3)
v_fma_f32 v65,v29,v29,v65
v_add_f32 v64,v64,v30
v_mfma_f32_16x16x32_bf16 v[16:19],a[160:163],v[48:51],v[16:19]
buffer_load_dwordx4 v0,s[36:39],s67 offen offset:3072 sc1 lds
v_mfma_f32_16x16x32_bf16 v[20:23],a[164:167],v[48:51],v[20:23]
s_add_u32 m0,s62,16384
ds_read_b128 v[44:47],v2 offset:27648
v_fma_f32 v65,v30,v30,v65
v_add_f32 v64,v64,v31
v_fma_f32 v65,v31,v31,v65
v_mfma_f32_16x16x32_bf16 v[16:19],a[168:171],v[52:55],v[16:19]
s_cmp_ge_u32 s66,s59
v_permlane16_swap_b32 v64,v65
v_add_f32 v64,v64,v65
v_mov_b32 v65,v64
v_mfma_f32_16x16x32_bf16 v[20:23],a[172:175],v[52:55],v[20:23]
ds_read_b128 v[48:51],v2 offset:28672
v_permlane32_swap_b32 v64,v65
v_add_f32 v64,v64,v65
v_mfma_f32_16x16x32_bf16 v[16:19],a[176:179],v[56:59],v[16:19]
ds_write_b32 v13,v64 offset:0
buffer_load_dwordx4 v1,s[36:39],s67 offen offset:0 sc1 lds
v_mfma_f32_16x16x32_bf16 v[20:23],a[180:183],v[56:59],v[20:23]
ds_read_b128 v[52:55],v2 offset:29696
v_mfma_f32_16x16x32_bf16 v[16:19],a[184:187],v[60:63],v[16:19]
ds_read_b128 v[56:59],v2 offset:30720
v_mfma_f32_16x16x32_bf16 v[20:23],a[188:191],v[60:63],v[20:23]
s_waitcnt lgkmcnt(4)
v_mfma_f32_16x16x32_bf16 v[16:19],a[192:195],v[32:35],v[16:19]
s_waitcnt vmcnt(13) lgkmcnt(2)
ds_read_b128 v[60:63],v2 offset:31744
v_mfma_f32_16x16x32_bf16 v[20:23],a[196:199],v[32:35],v[20:23]
s_barrier
v_mfma_f32_16x16x32_bf16 v[16:19],a[200:203],v[36:39],v[16:19]
buffer_load_dwordx4 v1,s[36:39],s67 offen offset:1024 sc1 lds
v_mfma_f32_16x16x32_bf16 v[20:23],a[204:207],v[36:39],v[20:23]
ds_read_b128 v[32:35],v2 offset:32768
v_mfma_f32_16x16x32_bf16 v[16:19],a[208:211],v[40:43],v[16:19]
ds_read_b128 v[36:39],v2 offset:33792
v_mfma_f32_16x16x32_bf16 v[20:23],a[212:215],v[40:43],v[20:23]
v_mfma_f32_16x16x32_bf16 v[16:19],a[216:219],v[44:47],v[16:19]
ds_read_b128 v[40:43],v2 offset:34816
v_mfma_f32_16x16x32_bf16 v[20:23],a[220:223],v[44:47],v[20:23]
s_waitcnt lgkmcnt(3)
v_mfma_f32_16x16x32_bf16 v[16:19],a[224:227],v[48:51],v[16:19]
buffer_store_dwordx4 v[72:75],v3,s[36:39],s73 offen offset:0 sc0 sc1
ds_read_b128 v[44:47],v2 offset:35840
v_mfma_f32_16x16x32_bf16 v[20:23],a[228:231],v[48:51],v[20:23]
v_mfma_f32_16x16x32_bf16 v[16:19],a[232:235],v[52:55],v[16:19])"
R"(
ds_read_b128 v[48:51],v2 offset:36864
v_mfma_f32_16x16x32_bf16 v[20:23],a[236:239],v[52:55],v[20:23]
v_mfma_f32_16x16x32_bf16 v[16:19],a[240:243],v[56:59],v[16:19]
ds_read_b128 v[52:55],v2 offset:37888
v_mfma_f32_16x16x32_bf16 v[20:23],a[244:247],v[56:59],v[20:23]
v_mfma_f32_16x16x32_bf16 v[16:19],a[248:251],v[60:63],v[16:19]
s_waitcnt lgkmcnt(2)
ds_read_b128 v[56:59],v2 offset:38912
v_mfma_f32_16x16x32_bf16 v[20:23],a[252:255],v[60:63],v[20:23]
s_cbranch_scc1 Lg2_slow1_%=
Lg2_d1w1_%=:
v_mfma_f32_16x16x32_bf16 v[24:27],a[0:3],v[32:35],0
s_add_u32 m0,s62,16384
ds_read_b128 v[60:63],v2 offset:39936
v_lshlrev_b32 v64,16,v80
v_mfma_f32_16x16x32_bf16 v[28:31],a[4:7],v[32:35],0
s_lshr_b32 s72,s67,6
v_and_b32 v65,0xffff0000,v80
v_lshlrev_b32 v66,16,v81
v_mfma_f32_16x16x32_bf16 v[24:27],a[8:11],v[36:39],v[24:27]
buffer_load_dwordx4 v1,s[36:39],s67 offen offset:2048 sc1 lds
v_mfma_f32_16x16x32_bf16 v[28:31],a[12:15],v[36:39],v[28:31]
s_lshr_b32 s71,s62,2
ds_read_b128 v[32:35],v2 offset:40960
v_and_b32 v67,0xffff0000,v81
v_mfma_f32_16x16x32_bf16 v[24:27],a[16:19],v[40:43],v[24:27]
s_mov_b64 s[44:45],s[46:47]
ds_read_b128 v[36:39],v2 offset:41984
v_lshlrev_b32 v68,16,v82
v_mfma_f32_16x16x32_bf16 v[28:31],a[20:23],v[40:43],v[28:31]
s_mov_b64 s[46:47],s[48:49]
v_and_b32 v69,0xffff0000,v82
v_lshlrev_b32 v70,16,v83
v_mfma_f32_16x16x32_bf16 v[24:27],a[24:27],v[44:47],v[24:27]
buffer_load_dwordx4 v1,s[36:39],s67 offen offset:3072 sc1 lds
v_mfma_f32_16x16x32_bf16 v[28:31],a[28:31],v[44:47],v[28:31]
s_waitcnt lgkmcnt(2)
ds_read_b128 v[40:43],v2 offset:43008
v_and_b32 v71,0xffff0000,v83
v_mfma_f32_16x16x32_bf16 v[24:27],a[32:35],v[48:51],v[24:27]
s_add_u32 m0,s71,s88
ds_read_b128 v[44:47],v2 offset:44032
v_fma_f32 v64,v64,v89,v88
v_mfma_f32_16x16x32_bf16 v[28:31],a[36:39],v[48:51],v[28:31]
s_lshr_b32 s73,s48,0
v_fma_f32 v65,v65,v89,v88
v_fma_f32 v66,v66,v89,v88
v_mfma_f32_16x16x32_bf16 v[24:27],a[40:43],v[52:55],v[24:27]
buffer_load_dwordx4 v10,s[36:39],s67 offen offset:0 sc1 lds
v_mfma_f32_16x16x32_bf16 v[28:31],a[44:47],v[52:55],v[28:31]
s_lshr_b32 s68,s52,5
ds_read_b128 v[48:51],v2 offset:45056
v_fma_f32 v67,v67,v89,v88
v_mfma_f32_16x16x32_bf16 v[24:27],a[48:51],v[56:59],v[24:27]
buffer_load_dword v11,s[36:39],s72 offen offset:1024 sc1 lds
v_mfma_f32_16x16x32_bf16 v[28:31],a[52:55],v[56:59],v[28:31]
s_add_u32 m0,s78,256
ds_read_b128 v[52:55],v2 offset:46080
v_fma_f32 v68,v68,v89,v88
v_mfma_f32_16x16x32_bf16 v[24:27],a[56:59],v[60:63],v[24:27]
s_xor_b32 s69,s63,65536
ds_read_b128 v[56:59],v2 offset:47104
v_fma_f32 v69,v69,v89,v88
v_mfma_f32_16x16x32_bf16 v[28:31],a[60:63],v[60:63],v[28:31]
s_waitcnt lgkmcnt(3)
v_fma_f32 v70,v70,v89,v88
v_fma_f32 v71,v71,v89,v88
v_mfma_f32_16x16x32_bf16 v[24:27],a[64:67],v[32:35],v[24:27]
s_waitcnt vmcnt(11)
ds_read_b128 v[60:63],v2 offset:48128
v_add_f32 v16,v16,v64
v_mfma_f32_16x16x32_bf16 v[28:31],a[68:71],v[32:35],v[28:31]
s_barrier
v_mfma_f32_16x16x32_bf16 v[24:27],a[72:75],v[36:39],v[24:27]
s_cmp_ge_u32 s67,s59
ds_read_b128 v[32:35],v2 offset:49152
ds_read_b32 v15,v7 offset:0
v_mfma_f32_16x16x32_bf16 v[28:31],a[76:79],v[36:39],v[28:31]
s_cselect_b32 s62,s69,s64
v_add_f32 v17,v17,v65
v_add_f32 v18,v18,v66
v_mfma_f32_16x16x32_bf16 v[24:27],a[80:83],v[40:43],v[24:27]
s_cselect_b32 s86,s50,s51
ds_read_b128 v[36:39],v2 offset:50176
v_add_f32 v19,v19,v67
v_mfma_f32_16x16x32_bf16 v[28:31],a[84:87],v[40:43],v[28:31]
buffer_load_dword v6,s[36:39],s68 offen offset:0 sc1 lds
v_mfma_f32_16x16x32_bf16 v[24:27],a[88:91],v[44:47],v[24:27]
s_waitcnt lgkmcnt(1)
ds_read_b128 v[40:43],v2 offset:51200
v_add_f32 v20,v20,v68
v_mfma_f32_16x16x32_bf16 v[28:31],a[92:95],v[44:47],v[28:31]
v_cmp_eq_u32 vcc,s60,v15
v_add_f32 v21,v21,v69
v_add_f32 v22,v22,v70
v_add_f32 v23,v23,v71
v_mfma_f32_16x16x32_bf16 v[24:27],a[96:99],v[48:51],v[24:27]
s_mov_b32 m0,s62
ds_read_b128 v[44:47],v2 offset:52224
v_cvt_pk_bf16_f32 v72,v16,v17
v_mfma_f32_16x16x32_bf16 v[28:31],a[100:103],v[48:51],v[28:31]
s_cselect_b32 s87,vcc_lo,vcc_hi
v_cvt_pk_bf16_f32 v73,v18,v19
v_cvt_pk_bf16_f32 v74,v20,v21
v_mfma_f32_16x16x32_bf16 v[24:27],a[104:107],v[52:55],v[24:27]
s_cmp_eq_u32 s87,-1
ds_read_b128 v[48:51],v2 offset:53248
v_cvt_pk_bf16_f32 v75,v22,v23
v_mfma_f32_16x16x32_bf16 v[28:31],a[108:111],v[52:55],v[28:31]
s_cselect_b32 s66,s86,s59
v_add_f32 v64,v16,v17
v_mul_f32 v65,v16,v16
v_mfma_f32_16x16x32_bf16 v[24:27],a[112:115],v[56:59],v[24:27]
buffer_load_dwordx4 v0,s[36:39],s66 offen offset:0 sc1 lds
v_mfma_f32_16x16x32_bf16 v[28:31],a[116:119],v[56:59],v[28:31]
s_xor_b32 s64,s64,65536
ds_read_b128 v[52:55],v2 offset:54272
ds_read_b128 v[84:87],v8 offset:8192
v_mfma_f32_16x16x32_bf16 v[24:27],a[120:123],v[60:63],v[24:27]
s_add_u32 s74,s53,s55
ds_read_b128 v[56:59],v2 offset:55296
ds_read_b64 v[90:91],v9 offset:9216
v_mfma_f32_16x16x32_bf16 v[28:31],a[124:127],v[60:63],v[28:31]
s_waitcnt lgkmcnt(5)
v_fma_f32 v65,v17,v17,v65
v_add_f32 v64,v64,v18
v_mfma_f32_16x16x32_bf16 v[24:27],a[128:131],v[32:35],v[24:27]
buffer_load_dwordx4 v0,s[36:39],s66 offen offset:1024 sc1 lds
v_mfma_f32_16x16x32_bf16 v[28:31],a[132:135],v[32:35],v[28:31]
s_cmp_eq_u32 s74,s56
ds_read_b128 v[60:63],v2 offset:56320
v_fma_f32 v65,v18,v18,v65
v_mfma_f32_16x16x32_bf16 v[24:27],a[136:139],v[36:39],v[24:27]
s_cselect_b32 s74,s57,s74
ds_read_b128 v[32:35],v2 offset:57344
v_add_f32 v64,v64,v19
v_mfma_f32_16x16x32_bf16 v[28:31],a[140:143],v[36:39],v[28:31]
s_cmp_eq_u32 s74,s58
v_fma_f32 v65,v19,v19,v65
v_add_f32 v64,v64,v20
v_mfma_f32_16x16x32_bf16 v[24:27],a[144:147],v[40:43],v[24:27]
s_waitcnt lgkmcnt(2)
buffer_load_dwordx4 v0,s[36:39],s66 offen offset:2048 sc1 lds
v_mfma_f32_16x16x32_bf16 v[28:31],a[148:151],v[40:43],v[28:31]
s_cselect_b32 s74,s59,s74
ds_read_b128 v[36:39],v2 offset:58368
v_fma_f32 v65,v20,v20,v65
v_mfma_f32_16x16x32_bf16 v[24:27],a[152:155],v[44:47],v[24:27]
s_mov_b64 s[48:49],s[50:51]
ds_read_b128 v[40:43],v2 offset:59392
v_add_f32 v64,v64,v21
v_mfma_f32_16x16x32_bf16 v[28:31],a[156:159],v[44:47],v[28:31]
s_waitcnt lgkmcnt(3)
v_fma_f32 v65,v21,v21,v65
v_add_f32 v64,v64,v22
v_mfma_f32_16x16x32_bf16 v[24:27],a[160:163],v[48:51],v[24:27]
buffer_load_dwordx4 v0,s[36:39],s66 offen offset:3072 sc1 lds
v_mfma_f32_16x16x32_bf16 v[28:31],a[164:167],v[48:51],v[28:31]
s_add_u32 m0,s62,16384
ds_read_b128 v[44:47],v2 offset:60416
v_fma_f32 v65,v22,v22,v65
v_add_f32 v64,v64,v23
v_fma_f32 v65,v23,v23,v65
v_mfma_f32_16x16x32_bf16 v[24:27],a[168:171],v[52:55],v[24:27]
s_mov_b64 s[50:51],s[52:53]
v_permlane16_swap_b32 v64,v65
v_add_f32 v64,v64,v65
v_mov_b32 v65,v64
v_mfma_f32_16x16x32_bf16 v[28:31],a[172:175],v[52:55],v[28:31]
s_mov_b32 s52,s74
ds_read_b128 v[48:51],v2 offset:61440
v_permlane32_swap_b32 v64,v65
v_add_f32 v64,v64,v65
v_mfma_f32_16x16x32_bf16 v[24:27],a[176:179],v[56:59],v[24:27]
s_cmp_lt_u32 s67,s59
ds_write_b32 v13,v64 offset:2048
buffer_load_dwordx4 v1,s[36:39],s66 offen offset:0 sc1 lds
v_mfma_f32_16x16x32_bf16 v[28:31],a[180:183],v[56:59],v[28:31]
ds_read_b128 v[52:55],v2 offset:62464
v_xor_b32 v8,16384,v8
v_mfma_f32_16x16x32_bf16 v[24:27],a[184:187],v[60:63],v[24:27]
ds_read_b128 v[56:59],v2 offset:63488
v_xor_b32 v9,16384,v9
v_mfma_f32_16x16x32_bf16 v[28:31],a[188:191],v[60:63],v[28:31]
s_waitcnt lgkmcnt(4)
v_mfma_f32_16x16x32_bf16 v[24:27],a[192:195],v[32:35],v[24:27]
s_waitcnt vmcnt(13) lgkmcnt(2)
ds_read_b128 v[60:63],v2 offset:64512
v_xor_b32 v2,65536,v2
v_mfma_f32_16x16x32_bf16 v[28:31],a[196:199],v[32:35],v[28:31]
s_barrier
v_mfma_f32_16x16x32_bf16 v[24:27],a[200:203],v[36:39],v[24:27]
buffer_load_dwordx4 v1,s[36:39],s66 offen offset:1024 sc1 lds
v_mfma_f32_16x16x32_bf16 v[28:31],a[204:207],v[36:39],v[28:31]
ds_read_b128 v[32:35],v2 offset:0
v_mfma_f32_16x16x32_bf16 v[24:27],a[208:211],v[40:43],v[24:27]
ds_read_b128 v[36:39],v2 offset:1024
v_mfma_f32_16x16x32_bf16 v[28:31],a[212:215],v[40:43],v[28:31]
v_mfma_f32_16x16x32_bf16 v[24:27],a[216:219],v[44:47],v[24:27]
ds_read_b128 v[40:43],v2 offset:2048)"
R"(
v_mfma_f32_16x16x32_bf16 v[28:31],a[220:223],v[44:47],v[28:31]
s_waitcnt lgkmcnt(3)
v_mfma_f32_16x16x32_bf16 v[24:27],a[224:227],v[48:51],v[24:27]
buffer_store_dwordx4 v[72:75],v3,s[36:39],s73 offen offset:0 sc0 sc1
ds_read_b128 v[44:47],v2 offset:3072
v_mfma_f32_16x16x32_bf16 v[28:31],a[228:231],v[48:51],v[28:31]
v_mfma_f32_16x16x32_bf16 v[24:27],a[232:235],v[52:55],v[24:27]
ds_read_b128 v[48:51],v2 offset:4096
v_mfma_f32_16x16x32_bf16 v[28:31],a[236:239],v[52:55],v[28:31]
v_mfma_f32_16x16x32_bf16 v[24:27],a[240:243],v[56:59],v[24:27]
ds_read_b128 v[52:55],v2 offset:5120
v_mfma_f32_16x16x32_bf16 v[28:31],a[244:247],v[56:59],v[28:31]
v_mfma_f32_16x16x32_bf16 v[24:27],a[248:251],v[60:63],v[24:27]
s_waitcnt lgkmcnt(2)
ds_read_b128 v[56:59],v2 offset:6144
v_mfma_f32_16x16x32_bf16 v[28:31],a[252:255],v[60:63],v[28:31]
s_cbranch_scc1 Lg2_d0w1_%=
s_branch Lg2_slow0_%=
Lg2_d0w2_%=:
v_mfma_f32_16x16x32_bf16 v[16:19],a[0:3],v[32:35],0
s_add_u32 m0,s62,16384
ds_read_b128 v[60:63],v2 offset:7168
v_lshlrev_b32 v64,16,v84
v_mfma_f32_16x16x32_bf16 v[20:23],a[4:7],v[32:35],0
s_lshr_b32 s72,s66,6
v_and_b32 v65,0xffff0000,v84
v_lshlrev_b32 v66,16,v85
v_mfma_f32_16x16x32_bf16 v[16:19],a[8:11],v[36:39],v[16:19]
s_lshr_b32 s71,s62,2
ds_read_b128 v[32:35],v2 offset:8192
v_and_b32 v67,0xffff0000,v85
v_mfma_f32_16x16x32_bf16 v[20:23],a[12:15],v[36:39],v[20:23]
buffer_load_dwordx4 v1,s[36:39],s66 offen offset:2048 sc1 lds
v_mfma_f32_16x16x32_bf16 v[16:19],a[16:19],v[40:43],v[16:19]
ds_read_b128 v[36:39],v2 offset:9216
v_lshlrev_b32 v68,16,v86
v_mfma_f32_16x16x32_bf16 v[20:23],a[20:23],v[40:43],v[20:23]
v_and_b32 v69,0xffff0000,v86
v_lshlrev_b32 v70,16,v87
v_mfma_f32_16x16x32_bf16 v[16:19],a[24:27],v[44:47],v[16:19]
ds_read_b128 v[40:43],v2 offset:10240
v_and_b32 v71,0xffff0000,v87
v_mfma_f32_16x16x32_bf16 v[20:23],a[28:31],v[44:47],v[20:23]
s_waitcnt lgkmcnt(3)
buffer_load_dwordx4 v1,s[36:39],s66 offen offset:3072 sc1 lds
v_mfma_f32_16x16x32_bf16 v[16:19],a[32:35],v[48:51],v[16:19]
s_add_u32 m0,s71,s88
ds_read_b128 v[44:47],v2 offset:11264
v_fma_f32 v64,v64,v91,v90
v_mfma_f32_16x16x32_bf16 v[20:23],a[36:39],v[48:51],v[20:23]
s_lshr_b32 s73,s47,0
v_fma_f32 v65,v65,v91,v90
v_fma_f32 v66,v66,v91,v90
v_mfma_f32_16x16x32_bf16 v[16:19],a[40:43],v[52:55],v[16:19]
s_lshr_b32 s85,s47,5
ds_read_b128 v[48:51],v2 offset:12288
v_fma_f32 v67,v67,v91,v90
v_mfma_f32_16x16x32_bf16 v[20:23],a[44:47],v[52:55],v[20:23]
buffer_load_dwordx4 v10,s[36:39],s66 offen offset:0 sc1 lds
v_mfma_f32_16x16x32_bf16 v[16:19],a[48:51],v[56:59],v[16:19]
buffer_load_dword v11,s[36:39],s72 offen offset:1024 sc1 lds
v_mfma_f32_16x16x32_bf16 v[20:23],a[52:55],v[56:59],v[20:23]
s_xor_b32 s69,s64,65536
ds_read_b128 v[52:55],v2 offset:13312
v_fma_f32 v68,v68,v91,v90
v_mfma_f32_16x16x32_bf16 v[16:19],a[56:59],v[60:63],v[16:19]
s_cmp_ge_u32 s66,s59
ds_read_b128 v[56:59],v2 offset:14336
v_fma_f32 v69,v69,v91,v90
v_mfma_f32_16x16x32_bf16 v[20:23],a[60:63],v[60:63],v[20:23]
s_waitcnt lgkmcnt(3)
v_fma_f32 v70,v70,v91,v90
v_fma_f32 v71,v71,v91,v90
v_mfma_f32_16x16x32_bf16 v[16:19],a[64:67],v[32:35],v[16:19]
s_waitcnt vmcnt(12)
ds_read_b128 v[60:63],v2 offset:15360
v_add_f32 v24,v24,v64
v_mfma_f32_16x16x32_bf16 v[20:23],a[68:71],v[32:35],v[20:23]
s_barrier
v_mfma_f32_16x16x32_bf16 v[16:19],a[72:75],v[36:39],v[16:19]
s_cselect_b32 s62,s69,s63
ds_read_b128 v[32:35],v2 offset:16384
ds_read_b32 v15,v7 offset:256
v_mfma_f32_16x16x32_bf16 v[20:23],a[76:79],v[36:39],v[20:23]
s_cselect_b32 s86,s49,s50
v_add_f32 v25,v25,v65
v_add_f32 v26,v26,v66
v_mfma_f32_16x16x32_bf16 v[16:19],a[80:83],v[40:43],v[16:19]
s_mov_b32 m0,s62
ds_read_b128 v[36:39],v2 offset:17408
v_add_f32 v27,v27,v67
v_mfma_f32_16x16x32_bf16 v[20:23],a[84:87],v[40:43],v[20:23]
v_add_f32 v28,v28,v68
v_add_f32 v29,v29,v69
v_mfma_f32_16x16x32_bf16 v[16:19],a[88:91],v[44:47],v[16:19]
s_waitcnt lgkmcnt(1)
ds_read_b128 v[40:43],v2 offset:18432
v_add_f32 v30,v30,v70
v_mfma_f32_16x16x32_bf16 v[20:23],a[92:95],v[44:47],v[20:23]
v_cmp_eq_u32 vcc,s60,v15
v_add_f32 v31,v31,v71
v_cvt_pk_bf16_f32 v72,v24,v25
v_cvt_pk_bf16_f32 v73,v26,v27
v_mfma_f32_16x16x32_bf16 v[16:19],a[96:99],v[48:51],v[16:19]
s_cselect_b32 s87,vcc_lo,vcc_hi
ds_read_b128 v[44:47],v2 offset:19456
v_cvt_pk_bf16_f32 v74,v28,v29
v_mfma_f32_16x16x32_bf16 v[20:23],a[100:103],v[48:51],v[20:23]
s_cmp_eq_u32 s87,-1
v_cvt_pk_bf16_f32 v75,v30,v31
v_add_f32 v64,v24,v25
v_mfma_f32_16x16x32_bf16 v[16:19],a[104:107],v[52:55],v[16:19]
s_cselect_b32 s67,s86,s59
ds_read_b128 v[48:51],v2 offset:20480
v_mul_f32 v65,v24,v24
v_mfma_f32_16x16x32_bf16 v[20:23],a[108:111],v[52:55],v[20:23]
s_xor_b32 s63,s63,65536
v_fma_f32 v65,v25,v25,v65
v_add_f32 v64,v64,v26
v_mfma_f32_16x16x32_bf16 v[16:19],a[112:115],v[56:59],v[16:19]
s_add_u32 s53,s52,s55
ds_read_b128 v[52:55],v2 offset:21504
ds_read_b128 v[80:83],v8 offset:0
v_mfma_f32_16x16x32_bf16 v[20:23],a[116:119],v[56:59],v[20:23]
buffer_load_dwordx4 v0,s[36:39],s67 offen offset:0 sc1 lds
v_mfma_f32_16x16x32_bf16 v[16:19],a[120:123],v[60:63],v[16:19]
s_cmp_eq_u32 s53,s56
ds_read_b128 v[56:59],v2 offset:22528
ds_read_b64 v[88:89],v9 offset:1024
v_mfma_f32_16x16x32_bf16 v[20:23],a[124:127],v[60:63],v[20:23]
s_waitcnt lgkmcnt(5)
v_fma_f32 v65,v26,v26,v65
v_add_f32 v64,v64,v27
v_mfma_f32_16x16x32_bf16 v[16:19],a[128:131],v[32:35],v[16:19]
s_cselect_b32 s53,s57,s53
ds_read_b128 v[60:63],v2 offset:23552
v_fma_f32 v65,v27,v27,v65
v_mfma_f32_16x16x32_bf16 v[20:23],a[132:135],v[32:35],v[20:23]
buffer_load_dwordx4 v0,s[36:39],s67 offen offset:1024 sc1 lds
v_mfma_f32_16x16x32_bf16 v[16:19],a[136:139],v[36:39],v[16:19]
s_cmp_eq_u32 s53,s58
ds_read_b128 v[32:35],v2 offset:24576
v_add_f32 v64,v64,v28
v_mfma_f32_16x16x32_bf16 v[20:23],a[140:143],v[36:39],v[20:23]
s_cselect_b32 s53,s59,s53
v_fma_f32 v65,v28,v28,v65
v_add_f32 v64,v64,v29
v_mfma_f32_16x16x32_bf16 v[16:19],a[144:147],v[40:43],v[16:19]
s_waitcnt lgkmcnt(2)
ds_read_b128 v[36:39],v2 offset:25600
v_fma_f32 v65,v29,v29,v65
v_mfma_f32_16x16x32_bf16 v[20:23],a[148:151],v[40:43],v[20:23]
buffer_load_dwordx4 v0,s[36:39],s67 offen offset:2048 sc1 lds
v_mfma_f32_16x16x32_bf16 v[16:19],a[152:155],v[44:47],v[16:19]
ds_read_b128 v[40:43],v2 offset:26624
v_add_f32 v64,v64,v30
v_mfma_f32_16x16x32_bf16 v[20:23],a[156:159],v[44:47],v[20:23]
s_waitcnt lgkmcnt(3)
v_add_f32 v64,v64,v31
v_fma_f32 v65,v30,v30,v65
v_mfma_f32_16x16x32_bf16 v[16:19],a[160:163],v[48:51],v[16:19]
ds_read_b128 v[44:47],v2 offset:27648
v_fma_f32 v65,v31,v31,v65
v_mfma_f32_16x16x32_bf16 v[20:23],a[164:167],v[48:51],v[20:23]
buffer_load_dwordx4 v0,s[36:39],s67 offen offset:3072 sc1 lds
v_mfma_f32_16x16x32_bf16 v[16:19],a[168:171],v[52:55],v[16:19]
s_add_u32 m0,s62,16384
v_permlane16_swap_b32 v64,v65
v_add_f32 v64,v64,v65
v_mov_b32 v65,v64
v_mfma_f32_16x16x32_bf16 v[20:23],a[172:175],v[52:55],v[20:23]
s_cmp_ge_u32 s66,s59
ds_read_b128 v[48:51],v2 offset:28672
v_permlane32_swap_b32 v64,v65
v_add_f32 v64,v64,v65
v_mfma_f32_16x16x32_bf16 v[16:19],a[176:179],v[56:59],v[16:19]
ds_write_b32 v13,v64 offset:0
v_mfma_f32_16x16x32_bf16 v[20:23],a[180:183],v[56:59],v[20:23]
ds_read_b128 v[52:55],v2 offset:29696
buffer_load_dwordx4 v1,s[36:39],s67 offen offset:0 sc1 lds
v_mfma_f32_16x16x32_bf16 v[16:19],a[184:187],v[60:63],v[16:19]
ds_read_b128 v[56:59],v2 offset:30720
v_mfma_f32_16x16x32_bf16 v[20:23],a[188:191],v[60:63],v[20:23]
s_waitcnt lgkmcnt(4)
v_mfma_f32_16x16x32_bf16 v[16:19],a[192:195],v[32:35],v[16:19]
s_waitcnt vmcnt(13) lgkmcnt(2)
ds_read_b128 v[60:63],v2 offset:31744
v_mfma_f32_16x16x32_bf16 v[20:23],a[196:199],v[32:35],v[20:23]
s_barrier
v_mfma_f32_16x16x32_bf16 v[16:19],a[200:203],v[36:39],v[16:19]
ds_read_b128 v[32:35],v2 offset:32768
ds_read_b128 v[76:79],v14 offset:0
v_mfma_f32_16x16x32_bf16 v[20:23],a[204:207],v[36:39],v[20:23]
buffer_load_dwordx4 v1,s[36:39],s67 offen offset:1024 sc1 lds
v_mfma_f32_16x16x32_bf16 v[16:19],a[208:211],v[40:43],v[16:19]
ds_read_b128 v[36:39],v2 offset:33792
v_mfma_f32_16x16x32_bf16 v[20:23],a[212:215],v[40:43],v[20:23]
v_mfma_f32_16x16x32_bf16 v[16:19],a[216:219],v[44:47],v[16:19]
s_waitcnt lgkmcnt(1)
ds_read_b128 v[40:43],v2 offset:34816)"
R"(
v_add_f32 v76,v76,v77
v_mfma_f32_16x16x32_bf16 v[20:23],a[220:223],v[44:47],v[20:23]
v_add_f32 v78,v78,v79
v_add_f32 v76,v76,v78
v_mfma_f32_16x16x32_bf16 v[16:19],a[224:227],v[48:51],v[16:19]
ds_read_b128 v[44:47],v2 offset:35840
v_mfma_f32_16x16x32_bf16 v[20:23],a[228:231],v[48:51],v[20:23]
buffer_store_dwordx4 v[72:75],v3,s[36:39],s73 offen offset:0 sc0 sc1
v_mfma_f32_16x16x32_bf16 v[16:19],a[232:235],v[52:55],v[16:19]
buffer_store_dword v76,v12,s[36:39],s85 offen offset:0 sc0 sc1
ds_read_b128 v[48:51],v2 offset:36864
v_mfma_f32_16x16x32_bf16 v[20:23],a[236:239],v[52:55],v[20:23]
v_mfma_f32_16x16x32_bf16 v[16:19],a[240:243],v[56:59],v[16:19]
ds_read_b128 v[52:55],v2 offset:37888
v_mfma_f32_16x16x32_bf16 v[20:23],a[244:247],v[56:59],v[20:23]
v_mfma_f32_16x16x32_bf16 v[16:19],a[248:251],v[60:63],v[16:19]
s_waitcnt lgkmcnt(2)
ds_read_b128 v[56:59],v2 offset:38912
v_mfma_f32_16x16x32_bf16 v[20:23],a[252:255],v[60:63],v[20:23]
s_cbranch_scc1 Lg2_slow1_%=
Lg2_d1w2_%=:
v_mfma_f32_16x16x32_bf16 v[24:27],a[0:3],v[32:35],0
s_add_u32 m0,s62,16384
ds_read_b128 v[60:63],v2 offset:39936
v_lshlrev_b32 v64,16,v80
v_mfma_f32_16x16x32_bf16 v[28:31],a[4:7],v[32:35],0
s_lshr_b32 s72,s67,6
v_and_b32 v65,0xffff0000,v80
v_lshlrev_b32 v66,16,v81
v_mfma_f32_16x16x32_bf16 v[24:27],a[8:11],v[36:39],v[24:27]
s_lshr_b32 s71,s62,2
ds_read_b128 v[32:35],v2 offset:40960
v_and_b32 v67,0xffff0000,v81
v_mfma_f32_16x16x32_bf16 v[28:31],a[12:15],v[36:39],v[28:31]
buffer_load_dwordx4 v1,s[36:39],s67 offen offset:2048 sc1 lds
v_mfma_f32_16x16x32_bf16 v[24:27],a[16:19],v[40:43],v[24:27]
s_mov_b64 s[44:45],s[46:47]
ds_read_b128 v[36:39],v2 offset:41984
v_lshlrev_b32 v68,16,v82
v_mfma_f32_16x16x32_bf16 v[28:31],a[20:23],v[40:43],v[28:31]
s_mov_b64 s[46:47],s[48:49]
v_and_b32 v69,0xffff0000,v82
v_lshlrev_b32 v70,16,v83
v_mfma_f32_16x16x32_bf16 v[24:27],a[24:27],v[44:47],v[24:27]
ds_read_b128 v[40:43],v2 offset:43008
v_and_b32 v71,0xffff0000,v83
v_mfma_f32_16x16x32_bf16 v[28:31],a[28:31],v[44:47],v[28:31]
s_waitcnt lgkmcnt(3)
buffer_load_dwordx4 v1,s[36:39],s67 offen offset:3072 sc1 lds
v_mfma_f32_16x16x32_bf16 v[24:27],a[32:35],v[48:51],v[24:27]
s_add_u32 m0,s71,s88
ds_read_b128 v[44:47],v2 offset:44032
v_fma_f32 v64,v64,v89,v88
v_mfma_f32_16x16x32_bf16 v[28:31],a[36:39],v[48:51],v[28:31]
s_lshr_b32 s73,s48,0
v_fma_f32 v65,v65,v89,v88
v_fma_f32 v66,v66,v89,v88
v_mfma_f32_16x16x32_bf16 v[24:27],a[40:43],v[52:55],v[24:27]
s_lshr_b32 s85,s48,5
ds_read_b128 v[48:51],v2 offset:45056
v_fma_f32 v67,v67,v89,v88
v_mfma_f32_16x16x32_bf16 v[28:31],a[44:47],v[52:55],v[28:31]
buffer_load_dwordx4 v10,s[36:39],s67 offen offset:0 sc1 lds
v_mfma_f32_16x16x32_bf16 v[24:27],a[48:51],v[56:59],v[24:27]
buffer_load_dword v11,s[36:39],s72 offen offset:1024 sc1 lds
v_mfma_f32_16x16x32_bf16 v[28:31],a[52:55],v[56:59],v[28:31]
s_xor_b32 s69,s63,65536
ds_read_b128 v[52:55],v2 offset:46080
v_fma_f32 v68,v68,v89,v88
v_mfma_f32_16x16x32_bf16 v[24:27],a[56:59],v[60:63],v[24:27]
s_cmp_ge_u32 s67,s59
ds_read_b128 v[56:59],v2 offset:47104
v_fma_f32 v69,v69,v89,v88
v_mfma_f32_16x16x32_bf16 v[28:31],a[60:63],v[60:63],v[28:31]
s_waitcnt lgkmcnt(3)
v_fma_f32 v70,v70,v89,v88
v_fma_f32 v71,v71,v89,v88
v_mfma_f32_16x16x32_bf16 v[24:27],a[64:67],v[32:35],v[24:27]
s_waitcnt vmcnt(12)
ds_read_b128 v[60:63],v2 offset:48128
v_add_f32 v16,v16,v64
v_mfma_f32_16x16x32_bf16 v[28:31],a[68:71],v[32:35],v[28:31]
s_barrier
v_mfma_f32_16x16x32_bf16 v[24:27],a[72:75],v[36:39],v[24:27]
s_cselect_b32 s62,s69,s64
ds_read_b128 v[32:35],v2 offset:49152
ds_read_b32 v15,v7 offset:0
v_mfma_f32_16x16x32_bf16 v[28:31],a[76:79],v[36:39],v[28:31]
s_cselect_b32 s86,s50,s51
v_add_f32 v17,v17,v65
v_add_f32 v18,v18,v66
v_mfma_f32_16x16x32_bf16 v[24:27],a[80:83],v[40:43],v[24:27]
s_mov_b32 m0,s62
ds_read_b128 v[36:39],v2 offset:50176
v_add_f32 v19,v19,v67
v_mfma_f32_16x16x32_bf16 v[28:31],a[84:87],v[40:43],v[28:31]
s_mov_b64 s[48:49],s[50:51]
v_add_f32 v20,v20,v68
v_add_f32 v21,v21,v69
v_mfma_f32_16x16x32_bf16 v[24:27],a[88:91],v[44:47],v[24:27]
s_waitcnt lgkmcnt(1)
ds_read_b128 v[40:43],v2 offset:51200
v_add_f32 v22,v22,v70
v_mfma_f32_16x16x32_bf16 v[28:31],a[92:95],v[44:47],v[28:31]
v_cmp_eq_u32 vcc,s60,v15
v_add_f32 v23,v23,v71
v_cvt_pk_bf16_f32 v72,v16,v17
v_cvt_pk_bf16_f32 v73,v18,v19
v_mfma_f32_16x16x32_bf16 v[24:27],a[96:99],v[48:51],v[24:27]
s_cselect_b32 s87,vcc_lo,vcc_hi
ds_read_b128 v[44:47],v2 offset:52224
v_cvt_pk_bf16_f32 v74,v20,v21
v_mfma_f32_16x16x32_bf16 v[28:31],a[100:103],v[48:51],v[28:31]
s_cmp_eq_u32 s87,-1
v_cvt_pk_bf16_f32 v75,v22,v23
v_add_f32 v64,v16,v17
v_mfma_f32_16x16x32_bf16 v[24:27],a[104:107],v[52:55],v[24:27]
s_cselect_b32 s66,s86,s59
ds_read_b128 v[48:51],v2 offset:53248
v_mul_f32 v65,v16,v16
v_mfma_f32_16x16x32_bf16 v[28:31],a[108:111],v[52:55],v[28:31]
s_xor_b32 s64,s64,65536
v_fma_f32 v65,v17,v17,v65
v_add_f32 v64,v64,v18
v_mfma_f32_16x16x32_bf16 v[24:27],a[112:115],v[56:59],v[24:27]
s_add_u32 s74,s53,s55
ds_read_b128 v[52:55],v2 offset:54272
ds_read_b128 v[84:87],v8 offset:8192
v_mfma_f32_16x16x32_bf16 v[28:31],a[116:119],v[56:59],v[28:31]
buffer_load_dwordx4 v0,s[36:39],s66 offen offset:0 sc1 lds
v_mfma_f32_16x16x32_bf16 v[24:27],a[120:123],v[60:63],v[24:27]
s_cmp_eq_u32 s74,s56
ds_read_b128 v[56:59],v2 offset:55296
ds_read_b64 v[90:91],v9 offset:9216
v_mfma_f32_16x16x32_bf16 v[28:31],a[124:127],v[60:63],v[28:31]
s_waitcnt lgkmcnt(5)
v_fma_f32 v65,v18,v18,v65
v_add_f32 v64,v64,v19
v_mfma_f32_16x16x32_bf16 v[24:27],a[128:131],v[32:35],v[24:27]
s_cselect_b32 s74,s57,s74
ds_read_b128 v[60:63],v2 offset:56320
v_fma_f32 v65,v19,v19,v65
v_mfma_f32_16x16x32_bf16 v[28:31],a[132:135],v[32:35],v[28:31]
buffer_load_dwordx4 v0,s[36:39],s66 offen offset:1024 sc1 lds
v_mfma_f32_16x16x32_bf16 v[24:27],a[136:139],v[36:39],v[24:27]
s_cmp_eq_u32 s74,s58
ds_read_b128 v[32:35],v2 offset:57344
v_add_f32 v64,v64,v20
v_mfma_f32_16x16x32_bf16 v[28:31],a[140:143],v[36:39],v[28:31]
s_cselect_b32 s74,s59,s74
v_fma_f32 v65,v20,v20,v65
v_add_f32 v64,v64,v21
v_mfma_f32_16x16x32_bf16 v[24:27],a[144:147],v[40:43],v[24:27]
s_waitcnt lgkmcnt(2)
ds_read_b128 v[36:39],v2 offset:58368
v_fma_f32 v65,v21,v21,v65
v_mfma_f32_16x16x32_bf16 v[28:31],a[148:151],v[40:43],v[28:31]
buffer_load_dwordx4 v0,s[36:39],s66 offen offset:2048 sc1 lds
v_mfma_f32_16x16x32_bf16 v[24:27],a[152:155],v[44:47],v[24:27]
s_mov_b64 s[50:51],s[52:53]
ds_read_b128 v[40:43],v2 offset:59392
v_add_f32 v64,v64,v22
v_mfma_f32_16x16x32_bf16 v[28:31],a[156:159],v[44:47],v[28:31]
s_waitcnt lgkmcnt(3)
v_add_f32 v64,v64,v23
v_fma_f32 v65,v22,v22,v65
v_mfma_f32_16x16x32_bf16 v[24:27],a[160:163],v[48:51],v[24:27]
s_mov_b32 s52,s74
ds_read_b128 v[44:47],v2 offset:60416
v_fma_f32 v65,v23,v23,v65
v_mfma_f32_16x16x32_bf16 v[28:31],a[164:167],v[48:51],v[28:31]
buffer_load_dwordx4 v0,s[36:39],s66 offen offset:3072 sc1 lds
v_mfma_f32_16x16x32_bf16 v[24:27],a[168:171],v[52:55],v[24:27]
s_add_u32 m0,s62,16384
v_permlane16_swap_b32 v64,v65
v_add_f32 v64,v64,v65
v_mov_b32 v65,v64
v_mfma_f32_16x16x32_bf16 v[28:31],a[172:175],v[52:55],v[28:31]
s_cmp_lt_u32 s67,s59
ds_read_b128 v[48:51],v2 offset:61440
v_permlane32_swap_b32 v64,v65
v_add_f32 v64,v64,v65
v_mfma_f32_16x16x32_bf16 v[24:27],a[176:179],v[56:59],v[24:27]
ds_write_b32 v13,v64 offset:2048
v_mfma_f32_16x16x32_bf16 v[28:31],a[180:183],v[56:59],v[28:31]
ds_read_b128 v[52:55],v2 offset:62464
buffer_load_dwordx4 v1,s[36:39],s66 offen offset:0 sc1 lds
v_mfma_f32_16x16x32_bf16 v[24:27],a[184:187],v[60:63],v[24:27]
ds_read_b128 v[56:59],v2 offset:63488
v_xor_b32 v8,16384,v8
v_mfma_f32_16x16x32_bf16 v[28:31],a[188:191],v[60:63],v[28:31]
s_waitcnt lgkmcnt(4)
v_xor_b32 v9,16384,v9
v_mfma_f32_16x16x32_bf16 v[24:27],a[192:195],v[32:35],v[24:27]
s_waitcnt vmcnt(13) lgkmcnt(2)
ds_read_b128 v[60:63],v2 offset:64512
v_xor_b32 v2,65536,v2
v_mfma_f32_16x16x32_bf16 v[28:31],a[196:199],v[32:35],v[28:31]
s_barrier
v_mfma_f32_16x16x32_bf16 v[24:27],a[200:203],v[36:39],v[24:27])"
R"(
ds_read_b128 v[32:35],v2 offset:0
ds_read_b128 v[76:79],v14 offset:2048
v_mfma_f32_16x16x32_bf16 v[28:31],a[204:207],v[36:39],v[28:31]
buffer_load_dwordx4 v1,s[36:39],s66 offen offset:1024 sc1 lds
v_mfma_f32_16x16x32_bf16 v[24:27],a[208:211],v[40:43],v[24:27]
ds_read_b128 v[36:39],v2 offset:1024
v_mfma_f32_16x16x32_bf16 v[28:31],a[212:215],v[40:43],v[28:31]
v_mfma_f32_16x16x32_bf16 v[24:27],a[216:219],v[44:47],v[24:27]
s_waitcnt lgkmcnt(1)
ds_read_b128 v[40:43],v2 offset:2048
v_add_f32 v76,v76,v77
v_mfma_f32_16x16x32_bf16 v[28:31],a[220:223],v[44:47],v[28:31]
v_add_f32 v78,v78,v79
v_add_f32 v76,v76,v78
v_mfma_f32_16x16x32_bf16 v[24:27],a[224:227],v[48:51],v[24:27]
ds_read_b128 v[44:47],v2 offset:3072
v_mfma_f32_16x16x32_bf16 v[28:31],a[228:231],v[48:51],v[28:31]
buffer_store_dwordx4 v[72:75],v3,s[36:39],s73 offen offset:0 sc0 sc1
v_mfma_f32_16x16x32_bf16 v[24:27],a[232:235],v[52:55],v[24:27]
buffer_store_dword v76,v12,s[36:39],s85 offen offset:0 sc0 sc1
ds_read_b128 v[48:51],v2 offset:4096
v_mfma_f32_16x16x32_bf16 v[28:31],a[236:239],v[52:55],v[28:31]
v_mfma_f32_16x16x32_bf16 v[24:27],a[240:243],v[56:59],v[24:27]
ds_read_b128 v[52:55],v2 offset:5120
v_mfma_f32_16x16x32_bf16 v[28:31],a[244:247],v[56:59],v[28:31]
v_mfma_f32_16x16x32_bf16 v[24:27],a[248:251],v[60:63],v[24:27]
s_waitcnt lgkmcnt(2)
ds_read_b128 v[56:59],v2 offset:6144
v_mfma_f32_16x16x32_bf16 v[28:31],a[252:255],v[60:63],v[28:31]
s_cbranch_scc1 Lg2_d0w2_%=
s_branch Lg2_slow0_%=
Lg2_d0w3_%=:
v_mfma_f32_16x16x32_bf16 v[16:19],a[0:3],v[32:35],0
s_add_u32 m0,s62,16384
ds_read_b128 v[60:63],v2 offset:7168
v_lshlrev_b32 v64,16,v84
v_mfma_f32_16x16x32_bf16 v[20:23],a[4:7],v[32:35],0
s_lshr_b32 s72,s66,6
v_and_b32 v65,0xffff0000,v84
v_lshlrev_b32 v66,16,v85
v_mfma_f32_16x16x32_bf16 v[16:19],a[8:11],v[36:39],v[16:19]
s_lshr_b32 s71,s62,2
ds_read_b128 v[32:35],v2 offset:8192
v_and_b32 v67,0xffff0000,v85
v_mfma_f32_16x16x32_bf16 v[20:23],a[12:15],v[36:39],v[20:23]
v_lshlrev_b32 v68,16,v86
v_and_b32 v69,0xffff0000,v86
v_mfma_f32_16x16x32_bf16 v[16:19],a[16:19],v[40:43],v[16:19]
buffer_load_dwordx4 v1,s[36:39],s66 offen offset:2048 sc1 lds
v_mfma_f32_16x16x32_bf16 v[20:23],a[20:23],v[40:43],v[20:23]
ds_read_b128 v[36:39],v2 offset:9216
v_lshlrev_b32 v70,16,v87
v_mfma_f32_16x16x32_bf16 v[16:19],a[24:27],v[44:47],v[16:19]
ds_read_b128 v[40:43],v2 offset:10240
v_and_b32 v71,0xffff0000,v87
v_mfma_f32_16x16x32_bf16 v[20:23],a[28:31],v[44:47],v[20:23]
s_waitcnt lgkmcnt(3)
v_fma_f32 v64,v64,v91,v90
v_fma_f32 v65,v65,v91,v90
v_mfma_f32_16x16x32_bf16 v[16:19],a[32:35],v[48:51],v[16:19]
buffer_load_dwordx4 v1,s[36:39],s66 offen offset:3072 sc1 lds
v_mfma_f32_16x16x32_bf16 v[20:23],a[36:39],v[48:51],v[20:23]
s_add_u32 m0,s71,s88
ds_read_b128 v[44:47],v2 offset:11264
v_fma_f32 v66,v66,v91,v90
v_mfma_f32_16x16x32_bf16 v[16:19],a[40:43],v[52:55],v[16:19]
s_lshr_b32 s73,s47,0
ds_read_b128 v[48:51],v2 offset:12288
v_fma_f32 v67,v67,v91,v90
v_mfma_f32_16x16x32_bf16 v[20:23],a[44:47],v[52:55],v[20:23]
s_lshr_b32 s70,s45,5
v_fma_f32 v68,v68,v91,v90
v_fma_f32 v69,v69,v91,v90
v_mfma_f32_16x16x32_bf16 v[16:19],a[48:51],v[56:59],v[16:19]
buffer_load_dwordx4 v10,s[36:39],s66 offen offset:0 sc1 lds
v_mfma_f32_16x16x32_bf16 v[20:23],a[52:55],v[56:59],v[20:23]
s_xor_b32 s69,s64,65536
ds_read_b128 v[52:55],v2 offset:13312
v_fma_f32 v70,v70,v91,v90
v_mfma_f32_16x16x32_bf16 v[16:19],a[56:59],v[60:63],v[16:19]
buffer_load_dword v11,s[36:39],s72 offen offset:1024 sc1 lds
v_mfma_f32_16x16x32_bf16 v[20:23],a[60:63],v[60:63],v[20:23]
s_waitcnt lgkmcnt(2)
ds_read_b128 v[56:59],v2 offset:14336
v_fma_f32 v71,v71,v91,v90
v_mfma_f32_16x16x32_bf16 v[16:19],a[64:67],v[32:35],v[16:19]
s_waitcnt vmcnt(12)
ds_read_b128 v[60:63],v2 offset:15360
v_add_f32 v24,v24,v64
v_mfma_f32_16x16x32_bf16 v[20:23],a[68:71],v[32:35],v[20:23]
s_barrier
v_mfma_f32_16x16x32_bf16 v[16:19],a[72:75],v[36:39],v[16:19]
buffer_store_dword v5,v4,s[36:39],s70 offen offset:0 sc0 sc1
ds_read_b128 v[32:35],v2 offset:16384
ds_read_b32 v15,v7 offset:256
v_add_f32 v25,v25,v65
v_mfma_f32_16x16x32_bf16 v[20:23],a[76:79],v[36:39],v[20:23]
s_cmp_ge_u32 s66,s59
v_add_f32 v26,v26,v66
v_add_f32 v27,v27,v67
v_mfma_f32_16x16x32_bf16 v[16:19],a[80:83],v[40:43],v[16:19]
s_cselect_b32 s62,s69,s63
ds_read_b128 v[36:39],v2 offset:17408
v_add_f32 v28,v28,v68
v_mfma_f32_16x16x32_bf16 v[20:23],a[84:87],v[40:43],v[20:23]
s_cselect_b32 s86,s49,s50
v_add_f32 v29,v29,v69
v_add_f32 v30,v30,v70
v_mfma_f32_16x16x32_bf16 v[16:19],a[88:91],v[44:47],v[16:19]
s_waitcnt lgkmcnt(1)
ds_read_b128 v[40:43],v2 offset:18432
v_add_f32 v31,v31,v71
v_mfma_f32_16x16x32_bf16 v[20:23],a[92:95],v[44:47],v[20:23]
v_cmp_eq_u32 vcc,s60,v15
v_cvt_pk_bf16_f32 v72,v24,v25
v_cvt_pk_bf16_f32 v73,v26,v27
v_cvt_pk_bf16_f32 v74,v28,v29
v_mfma_f32_16x16x32_bf16 v[16:19],a[96:99],v[48:51],v[16:19]
s_mov_b32 m0,s62
ds_read_b128 v[44:47],v2 offset:19456
v_cvt_pk_bf16_f32 v75,v30,v31
v_mfma_f32_16x16x32_bf16 v[20:23],a[100:103],v[48:51],v[20:23]
s_cselect_b32 s87,vcc_lo,vcc_hi
v_add_f32 v64,v24,v25
v_mul_f32 v65,v24,v24
v_mfma_f32_16x16x32_bf16 v[16:19],a[104:107],v[52:55],v[16:19]
s_cmp_eq_u32 s87,-1
ds_read_b128 v[48:51],v2 offset:20480
v_fma_f32 v65,v25,v25,v65
v_mfma_f32_16x16x32_bf16 v[20:23],a[108:111],v[52:55],v[20:23]
s_cselect_b32 s67,s86,s59
v_add_f32 v64,v64,v26
v_fma_f32 v65,v26,v26,v65
v_mfma_f32_16x16x32_bf16 v[16:19],a[112:115],v[56:59],v[16:19]
s_xor_b32 s63,s63,65536
ds_read_b128 v[52:55],v2 offset:21504
ds_read_b128 v[80:83],v8 offset:0
v_mfma_f32_16x16x32_bf16 v[20:23],a[116:119],v[56:59],v[20:23]
s_add_u32 s53,s52,s55
ds_read_b64 v[88:89],v9 offset:1024
v_add_f32 v64,v64,v27
v_mfma_f32_16x16x32_bf16 v[16:19],a[120:123],v[60:63],v[16:19]
buffer_load_dwordx4 v0,s[36:39],s67 offen offset:0 sc1 lds
v_mfma_f32_16x16x32_bf16 v[20:23],a[124:127],v[60:63],v[20:23]
s_waitcnt lgkmcnt(4)
ds_read_b128 v[56:59],v2 offset:22528
v_fma_f32 v65,v27,v27,v65
v_mfma_f32_16x16x32_bf16 v[16:19],a[128:131],v[32:35],v[16:19]
s_cmp_eq_u32 s53,s56
ds_read_b128 v[60:63],v2 offset:23552
v_add_f32 v64,v64,v28
v_mfma_f32_16x16x32_bf16 v[20:23],a[132:135],v[32:35],v[20:23]
s_cselect_b32 s53,s57,s53
v_fma_f32 v65,v28,v28,v65
v_add_f32 v64,v64,v29
v_mfma_f32_16x16x32_bf16 v[16:19],a[136:139],v[36:39],v[16:19]
buffer_load_dwordx4 v0,s[36:39],s67 offen offset:1024 sc1 lds
v_mfma_f32_16x16x32_bf16 v[20:23],a[140:143],v[36:39],v[20:23]
s_waitcnt lgkmcnt(2)
ds_read_b128 v[32:35],v2 offset:24576
v_fma_f32 v65,v29,v29,v65
v_mfma_f32_16x16x32_bf16 v[16:19],a[144:147],v[40:43],v[16:19]
s_cmp_eq_u32 s53,s58
ds_read_b128 v[36:39],v2 offset:25600
v_add_f32 v64,v64,v30
v_mfma_f32_16x16x32_bf16 v[20:23],a[148:151],v[40:43],v[20:23]
s_cselect_b32 s53,s59,s53
v_add_f32 v64,v64,v31
v_fma_f32 v65,v30,v30,v65
v_mfma_f32_16x16x32_bf16 v[16:19],a[152:155],v[44:47],v[16:19]
buffer_load_dwordx4 v0,s[36:39],s67 offen offset:2048 sc1 lds
v_mfma_f32_16x16x32_bf16 v[20:23],a[156:159],v[44:47],v[20:23]
s_waitcnt lgkmcnt(2)
ds_read_b128 v[40:43],v2 offset:26624
v_fma_f32 v65,v31,v31,v65
v_mfma_f32_16x16x32_bf16 v[16:19],a[160:163],v[48:51],v[16:19]
ds_read_b128 v[44:47],v2 offset:27648
v_mfma_f32_16x16x32_bf16 v[20:23],a[164:167],v[48:51],v[20:23]
v_permlane16_swap_b32 v64,v65
v_mfma_f32_16x16x32_bf16 v[16:19],a[168:171],v[52:55],v[16:19]
buffer_load_dwordx4 v0,s[36:39],s67 offen offset:3072 sc1 lds
v_add_f32 v64,v64,v65
v_mov_b32 v65,v64
v_mfma_f32_16x16x32_bf16 v[20:23],a[172:175],v[52:55],v[20:23]
s_add_u32 m0,s62,16384
ds_read_b128 v[48:51],v2 offset:28672
v_permlane32_swap_b32 v64,v65
v_add_f32 v64,v64,v65
v_mfma_f32_16x16x32_bf16 v[16:19],a[176:179],v[56:59],v[16:19]
s_cmp_ge_u32 s66,s59
ds_write_b32 v13,v64 offset:0
v_mfma_f32_16x16x32_bf16 v[20:23],a[180:183],v[56:59],v[20:23]
ds_read_b128 v[52:55],v2 offset:29696
v_mfma_f32_16x16x32_bf16 v[16:19],a[184:187],v[60:63],v[16:19]
buffer_load_dwordx4 v1,s[36:39],s67 offen offset:0 sc1 lds
v_mfma_f32_16x16x32_bf16 v[20:23],a[188:191],v[60:63],v[20:23]
s_waitcnt lgkmcnt(3)
ds_read_b128 v[56:59],v2 offset:30720
v_mfma_f32_16x16x32_bf16 v[16:19],a[192:195],v[32:35],v[16:19]
s_waitcnt vmcnt(13) lgkmcnt(2))"
R"(
ds_read_b128 v[60:63],v2 offset:31744
v_mfma_f32_16x16x32_bf16 v[20:23],a[196:199],v[32:35],v[20:23]
s_barrier
v_mfma_f32_16x16x32_bf16 v[16:19],a[200:203],v[36:39],v[16:19]
ds_read_b128 v[32:35],v2 offset:32768
v_mfma_f32_16x16x32_bf16 v[20:23],a[204:207],v[36:39],v[20:23]
v_mfma_f32_16x16x32_bf16 v[16:19],a[208:211],v[40:43],v[16:19]
buffer_load_dwordx4 v1,s[36:39],s67 offen offset:1024 sc1 lds
v_mfma_f32_16x16x32_bf16 v[20:23],a[212:215],v[40:43],v[20:23]
ds_read_b128 v[36:39],v2 offset:33792
v_mfma_f32_16x16x32_bf16 v[16:19],a[216:219],v[44:47],v[16:19]
ds_read_b128 v[40:43],v2 offset:34816
v_mfma_f32_16x16x32_bf16 v[20:23],a[220:223],v[44:47],v[20:23]
s_waitcnt lgkmcnt(3)
v_mfma_f32_16x16x32_bf16 v[16:19],a[224:227],v[48:51],v[16:19]
ds_read_b128 v[44:47],v2 offset:35840
v_mfma_f32_16x16x32_bf16 v[20:23],a[228:231],v[48:51],v[20:23]
v_mfma_f32_16x16x32_bf16 v[16:19],a[232:235],v[52:55],v[16:19]
buffer_store_dwordx4 v[72:75],v3,s[36:39],s73 offen offset:0 sc0 sc1
ds_read_b128 v[48:51],v2 offset:36864
v_mfma_f32_16x16x32_bf16 v[20:23],a[236:239],v[52:55],v[20:23]
v_mfma_f32_16x16x32_bf16 v[16:19],a[240:243],v[56:59],v[16:19]
ds_read_b128 v[52:55],v2 offset:37888
v_mfma_f32_16x16x32_bf16 v[20:23],a[244:247],v[56:59],v[20:23]
v_mfma_f32_16x16x32_bf16 v[16:19],a[248:251],v[60:63],v[16:19]
s_waitcnt lgkmcnt(2)
ds_read_b128 v[56:59],v2 offset:38912
v_mfma_f32_16x16x32_bf16 v[20:23],a[252:255],v[60:63],v[20:23]
s_cbranch_scc1 Lg2_slow1_%=
Lg2_d1w3_%=:
v_mfma_f32_16x16x32_bf16 v[24:27],a[0:3],v[32:35],0
s_add_u32 m0,s62,16384
ds_read_b128 v[60:63],v2 offset:39936
v_lshlrev_b32 v64,16,v80
v_mfma_f32_16x16x32_bf16 v[28:31],a[4:7],v[32:35],0
s_lshr_b32 s72,s67,6
v_and_b32 v65,0xffff0000,v80
v_lshlrev_b32 v66,16,v81
v_mfma_f32_16x16x32_bf16 v[24:27],a[8:11],v[36:39],v[24:27]
s_lshr_b32 s71,s62,2
ds_read_b128 v[32:35],v2 offset:40960
v_and_b32 v67,0xffff0000,v81
v_mfma_f32_16x16x32_bf16 v[28:31],a[12:15],v[36:39],v[28:31]
s_mov_b64 s[44:45],s[46:47]
v_lshlrev_b32 v68,16,v82
v_and_b32 v69,0xffff0000,v82
v_mfma_f32_16x16x32_bf16 v[24:27],a[16:19],v[40:43],v[24:27]
buffer_load_dwordx4 v1,s[36:39],s67 offen offset:2048 sc1 lds
v_mfma_f32_16x16x32_bf16 v[28:31],a[20:23],v[40:43],v[28:31]
ds_read_b128 v[36:39],v2 offset:41984
v_lshlrev_b32 v70,16,v83
v_mfma_f32_16x16x32_bf16 v[24:27],a[24:27],v[44:47],v[24:27]
ds_read_b128 v[40:43],v2 offset:43008
v_and_b32 v71,0xffff0000,v83
v_mfma_f32_16x16x32_bf16 v[28:31],a[28:31],v[44:47],v[28:31]
s_waitcnt lgkmcnt(3)
v_fma_f32 v64,v64,v89,v88
v_fma_f32 v65,v65,v89,v88
v_mfma_f32_16x16x32_bf16 v[24:27],a[32:35],v[48:51],v[24:27]
buffer_load_dwordx4 v1,s[36:39],s67 offen offset:3072 sc1 lds
v_mfma_f32_16x16x32_bf16 v[28:31],a[36:39],v[48:51],v[28:31]
s_add_u32 m0,s71,s88
ds_read_b128 v[44:47],v2 offset:44032
v_fma_f32 v66,v66,v89,v88
v_mfma_f32_16x16x32_bf16 v[24:27],a[40:43],v[52:55],v[24:27]
s_lshr_b32 s73,s48,0
ds_read_b128 v[48:51],v2 offset:45056
v_fma_f32 v67,v67,v89,v88
v_mfma_f32_16x16x32_bf16 v[28:31],a[44:47],v[52:55],v[28:31]
s_lshr_b32 s70,s46,5
v_fma_f32 v68,v68,v89,v88
v_fma_f32 v69,v69,v89,v88
v_mfma_f32_16x16x32_bf16 v[24:27],a[48:51],v[56:59],v[24:27]
buffer_load_dwordx4 v10,s[36:39],s67 offen offset:0 sc1 lds
v_mfma_f32_16x16x32_bf16 v[28:31],a[52:55],v[56:59],v[28:31]
s_xor_b32 s69,s63,65536
ds_read_b128 v[52:55],v2 offset:46080
v_fma_f32 v70,v70,v89,v88
v_mfma_f32_16x16x32_bf16 v[24:27],a[56:59],v[60:63],v[24:27]
buffer_load_dword v11,s[36:39],s72 offen offset:1024 sc1 lds
v_mfma_f32_16x16x32_bf16 v[28:31],a[60:63],v[60:63],v[28:31]
s_waitcnt lgkmcnt(2)
ds_read_b128 v[56:59],v2 offset:47104
v_fma_f32 v71,v71,v89,v88
v_mfma_f32_16x16x32_bf16 v[24:27],a[64:67],v[32:35],v[24:27]
s_waitcnt vmcnt(12)
ds_read_b128 v[60:63],v2 offset:48128
v_add_f32 v16,v16,v64
v_mfma_f32_16x16x32_bf16 v[28:31],a[68:71],v[32:35],v[28:31]
s_barrier
v_mfma_f32_16x16x32_bf16 v[24:27],a[72:75],v[36:39],v[24:27]
buffer_store_dword v5,v4,s[36:39],s70 offen offset:0 sc0 sc1
ds_read_b128 v[32:35],v2 offset:49152
ds_read_b32 v15,v7 offset:0
v_add_f32 v17,v17,v65
v_mfma_f32_16x16x32_bf16 v[28:31],a[76:79],v[36:39],v[28:31]
s_cmp_ge_u32 s67,s59
v_add_f32 v18,v18,v66
v_add_f32 v19,v19,v67
v_mfma_f32_16x16x32_bf16 v[24:27],a[80:83],v[40:43],v[24:27]
s_cselect_b32 s62,s69,s64
ds_read_b128 v[36:39],v2 offset:50176
v_add_f32 v20,v20,v68
v_mfma_f32_16x16x32_bf16 v[28:31],a[84:87],v[40:43],v[28:31]
s_cselect_b32 s86,s50,s51
v_add_f32 v21,v21,v69
v_add_f32 v22,v22,v70
v_mfma_f32_16x16x32_bf16 v[24:27],a[88:91],v[44:47],v[24:27]
s_waitcnt lgkmcnt(1)
ds_read_b128 v[40:43],v2 offset:51200
v_add_f32 v23,v23,v71
v_mfma_f32_16x16x32_bf16 v[28:31],a[92:95],v[44:47],v[28:31]
v_cmp_eq_u32 vcc,s60,v15
v_cvt_pk_bf16_f32 v72,v16,v17
v_cvt_pk_bf16_f32 v73,v18,v19
v_cvt_pk_bf16_f32 v74,v20,v21
v_mfma_f32_16x16x32_bf16 v[24:27],a[96:99],v[48:51],v[24:27]
s_mov_b32 m0,s62
ds_read_b128 v[44:47],v2 offset:52224
v_cvt_pk_bf16_f32 v75,v22,v23
v_mfma_f32_16x16x32_bf16 v[28:31],a[100:103],v[48:51],v[28:31]
s_cselect_b32 s87,vcc_lo,vcc_hi
v_add_f32 v64,v16,v17
v_mul_f32 v65,v16,v16
v_mfma_f32_16x16x32_bf16 v[24:27],a[104:107],v[52:55],v[24:27]
s_cmp_eq_u32 s87,-1
ds_read_b128 v[48:51],v2 offset:53248
v_fma_f32 v65,v17,v17,v65
v_mfma_f32_16x16x32_bf16 v[28:31],a[108:111],v[52:55],v[28:31]
s_cselect_b32 s66,s86,s59
v_add_f32 v64,v64,v18
v_fma_f32 v65,v18,v18,v65
v_mfma_f32_16x16x32_bf16 v[24:27],a[112:115],v[56:59],v[24:27]
s_xor_b32 s64,s64,65536
ds_read_b128 v[52:55],v2 offset:54272
ds_read_b128 v[84:87],v8 offset:8192
v_mfma_f32_16x16x32_bf16 v[28:31],a[116:119],v[56:59],v[28:31]
s_add_u32 s74,s53,s55
ds_read_b64 v[90:91],v9 offset:9216
v_add_f32 v64,v64,v19
v_mfma_f32_16x16x32_bf16 v[24:27],a[120:123],v[60:63],v[24:27]
buffer_load_dwordx4 v0,s[36:39],s66 offen offset:0 sc1 lds
v_mfma_f32_16x16x32_bf16 v[28:31],a[124:127],v[60:63],v[28:31]
s_waitcnt lgkmcnt(4)
ds_read_b128 v[56:59],v2 offset:55296
v_fma_f32 v65,v19,v19,v65
v_mfma_f32_16x16x32_bf16 v[24:27],a[128:131],v[32:35],v[24:27]
s_cmp_eq_u32 s74,s56
ds_read_b128 v[60:63],v2 offset:56320
v_add_f32 v64,v64,v20
v_mfma_f32_16x16x32_bf16 v[28:31],a[132:135],v[32:35],v[28:31]
s_cselect_b32 s74,s57,s74
v_fma_f32 v65,v20,v20,v65
v_add_f32 v64,v64,v21
v_mfma_f32_16x16x32_bf16 v[24:27],a[136:139],v[36:39],v[24:27]
buffer_load_dwordx4 v0,s[36:39],s66 offen offset:1024 sc1 lds
v_mfma_f32_16x16x32_bf16 v[28:31],a[140:143],v[36:39],v[28:31]
s_waitcnt lgkmcnt(2)
ds_read_b128 v[32:35],v2 offset:57344
v_fma_f32 v65,v21,v21,v65
v_mfma_f32_16x16x32_bf16 v[24:27],a[144:147],v[40:43],v[24:27]
s_cmp_eq_u32 s74,s58
ds_read_b128 v[36:39],v2 offset:58368
v_add_f32 v64,v64,v22
v_mfma_f32_16x16x32_bf16 v[28:31],a[148:151],v[40:43],v[28:31]
s_cselect_b32 s74,s59,s74
v_add_f32 v64,v64,v23
v_fma_f32 v65,v22,v22,v65
v_mfma_f32_16x16x32_bf16 v[24:27],a[152:155],v[44:47],v[24:27]
buffer_load_dwordx4 v0,s[36:39],s66 offen offset:2048 sc1 lds
v_mfma_f32_16x16x32_bf16 v[28:31],a[156:159],v[44:47],v[28:31]
s_waitcnt lgkmcnt(2)
ds_read_b128 v[40:43],v2 offset:59392
v_fma_f32 v65,v23,v23,v65
v_mfma_f32_16x16x32_bf16 v[24:27],a[160:163],v[48:51],v[24:27]
s_mov_b64 s[46:47],s[48:49]
ds_read_b128 v[44:47],v2 offset:60416
v_xor_b32 v8,16384,v8
v_mfma_f32_16x16x32_bf16 v[28:31],a[164:167],v[48:51],v[28:31]
s_mov_b64 s[48:49],s[50:51]
v_permlane16_swap_b32 v64,v65
v_mfma_f32_16x16x32_bf16 v[24:27],a[168:171],v[52:55],v[24:27]
buffer_load_dwordx4 v0,s[36:39],s66 offen offset:3072 sc1 lds
v_add_f32 v64,v64,v65
v_mov_b32 v65,v64
v_mfma_f32_16x16x32_bf16 v[28:31],a[172:175],v[52:55],v[28:31]
s_add_u32 m0,s62,16384
ds_read_b128 v[48:51],v2 offset:61440
v_permlane32_swap_b32 v64,v65
v_add_f32 v64,v64,v65
v_mfma_f32_16x16x32_bf16 v[24:27],a[176:179],v[56:59],v[24:27]
s_mov_b64 s[50:51],s[52:53]
ds_write_b32 v13,v64 offset:2048
v_mfma_f32_16x16x32_bf16 v[28:31],a[180:183],v[56:59],v[28:31]
s_mov_b32 s52,s74
ds_read_b128 v[52:55],v2 offset:62464
v_xor_b32 v9,16384,v9
v_mfma_f32_16x16x32_bf16 v[24:27],a[184:187],v[60:63],v[24:27]
buffer_load_dwordx4 v1,s[36:39],s66 offen offset:0 sc1 lds
v_mfma_f32_16x16x32_bf16 v[28:31],a[188:191],v[60:63],v[28:31])"
R"(
s_waitcnt lgkmcnt(3)
ds_read_b128 v[56:59],v2 offset:63488
s_cmp_lt_u32 s67,s59
v_mfma_f32_16x16x32_bf16 v[24:27],a[192:195],v[32:35],v[24:27]
s_waitcnt vmcnt(13) lgkmcnt(2)
ds_read_b128 v[60:63],v2 offset:64512
v_xor_b32 v2,65536,v2
v_mfma_f32_16x16x32_bf16 v[28:31],a[196:199],v[32:35],v[28:31]
s_barrier
v_mfma_f32_16x16x32_bf16 v[24:27],a[200:203],v[36:39],v[24:27]
ds_read_b128 v[32:35],v2 offset:0
v_mfma_f32_16x16x32_bf16 v[28:31],a[204:207],v[36:39],v[28:31]
v_mfma_f32_16x16x32_bf16 v[24:27],a[208:211],v[40:43],v[24:27]
buffer_load_dwordx4 v1,s[36:39],s66 offen offset:1024 sc1 lds
v_mfma_f32_16x16x32_bf16 v[28:31],a[212:215],v[40:43],v[28:31]
ds_read_b128 v[36:39],v2 offset:1024
v_mfma_f32_16x16x32_bf16 v[24:27],a[216:219],v[44:47],v[24:27]
ds_read_b128 v[40:43],v2 offset:2048
v_mfma_f32_16x16x32_bf16 v[28:31],a[220:223],v[44:47],v[28:31]
s_waitcnt lgkmcnt(3)
v_mfma_f32_16x16x32_bf16 v[24:27],a[224:227],v[48:51],v[24:27]
ds_read_b128 v[44:47],v2 offset:3072
v_mfma_f32_16x16x32_bf16 v[28:31],a[228:231],v[48:51],v[28:31]
v_mfma_f32_16x16x32_bf16 v[24:27],a[232:235],v[52:55],v[24:27]
buffer_store_dwordx4 v[72:75],v3,s[36:39],s73 offen offset:0 sc0 sc1
ds_read_b128 v[48:51],v2 offset:4096
v_mfma_f32_16x16x32_bf16 v[28:31],a[236:239],v[52:55],v[28:31]
v_mfma_f32_16x16x32_bf16 v[24:27],a[240:243],v[56:59],v[24:27]
ds_read_b128 v[52:55],v2 offset:5120
v_mfma_f32_16x16x32_bf16 v[28:31],a[244:247],v[56:59],v[28:31]
v_mfma_f32_16x16x32_bf16 v[24:27],a[248:251],v[60:63],v[24:27]
s_waitcnt lgkmcnt(2)
ds_read_b128 v[56:59],v2 offset:6144
v_mfma_f32_16x16x32_bf16 v[28:31],a[252:255],v[60:63],v[28:31]
s_cbranch_scc1 Lg2_d0w3_%=
s_branch Lg2_slow0_%=
Lg2_slow0_%=:
s_add_u32 m0,s62,16384
s_lshr_b32 s72,s66,6
s_lshr_b32 s71,s62,2
buffer_load_dwordx4 v1,s[36:39],s66 offen offset:2048 sc1 lds
buffer_load_dwordx4 v1,s[36:39],s66 offen offset:3072 sc1 lds
s_add_u32 m0,s71,s88
s_nop 0
buffer_load_dwordx4 v10,s[36:39],s66 offen offset:0 sc1 lds
buffer_load_dword v11,s[36:39],s72 offen offset:1024 sc1 lds
s_cmp_eq_u32 s54,1
s_cselect_b32 s68,s49,s48
s_lshr_b32 s68,s68,5
s_add_u32 m0,s78,8192
s_nop 0
buffer_load_dword v6,s[36:39],s68 offen offset:0 sc1 lds
s_lshr_b32 s73,s47,0
s_lshr_b32 s85,s47,5
v_lshlrev_b32 v64,16,v84
v_and_b32 v65,0xffff0000,v84
v_lshlrev_b32 v66,16,v85
v_and_b32 v67,0xffff0000,v85
v_lshlrev_b32 v68,16,v86
v_and_b32 v69,0xffff0000,v86
v_lshlrev_b32 v70,16,v87
v_and_b32 v71,0xffff0000,v87
v_fma_f32 v64,v64,v91,v90
v_fma_f32 v65,v65,v91,v90
v_fma_f32 v66,v66,v91,v90
v_fma_f32 v67,v67,v91,v90
v_fma_f32 v68,v68,v91,v90
v_fma_f32 v69,v69,v91,v90
v_fma_f32 v70,v70,v91,v90
v_fma_f32 v71,v71,v91,v90
v_add_f32 v24,v24,v64
v_add_f32 v25,v25,v65
v_add_f32 v26,v26,v66
v_add_f32 v27,v27,v67
v_add_f32 v28,v28,v68
v_add_f32 v29,v29,v69
v_add_f32 v30,v30,v70
v_add_f32 v31,v31,v71
v_cvt_pk_bf16_f32 v72,v24,v25
v_cvt_pk_bf16_f32 v73,v26,v27
v_cvt_pk_bf16_f32 v74,v28,v29
v_cvt_pk_bf16_f32 v75,v30,v31
s_add_u32 m0,s78,8448
s_nop 0
buffer_load_dword v6,s[36:39],s68 offen offset:0 sc1 lds
buffer_store_dwordx4 v[72:75],v3,s[36:39],s73 offen offset:0 sc0 sc1
v_add_f32 v64,v24,v25
v_mul_f32 v65,v24,v24
v_fma_f32 v65,v25,v25,v65
v_add_f32 v64,v64,v26
v_fma_f32 v65,v26,v26,v65
v_add_f32 v64,v64,v27
v_fma_f32 v65,v27,v27,v65
v_add_f32 v64,v64,v28
v_fma_f32 v65,v28,v28,v65
v_add_f32 v64,v64,v29
v_fma_f32 v65,v29,v29,v65
v_add_f32 v64,v64,v30
v_fma_f32 v65,v30,v30,v65
v_add_f32 v64,v64,v31
v_fma_f32 v65,v31,v31,v65
s_nop 1
v_permlane16_swap_b32 v64,v65
v_add_f32 v64,v64,v65
v_mov_b32 v65,v64
s_nop 1
v_permlane32_swap_b32 v64,v65
v_add_f32 v64,v64,v65
ds_write_b32 v13,v64 offset:0
s_waitcnt lgkmcnt(0)
s_barrier
ds_read_b128 v[76:79],v14 offset:0
s_waitcnt lgkmcnt(0)
v_add_f32 v76,v76,v77
v_add_f32 v78,v78,v79
v_add_f32 v76,v76,v78
buffer_store_dword v76,v12,s[36:39],s85 offen offset:0 sc0 sc1
s_cmp_ge_u32 s48,s59
s_cbranch_scc1 Lg2_done0_%=
s_add_u32 m0,s78,16384
s_nop 0
buffer_load_dword v6,s[36:39],s68 offen offset:0 sc1 lds
s_waitcnt vmcnt(4)
s_barrier
ds_read_b32 v15,v7 offset:8192
s_lshr_b32 s70,s45,5
buffer_store_dword v5,v4,s[36:39],s70 offen offset:0 sc0 sc1
s_mov_b32 s45,s59
s_lshr_b32 s70,s46,5
buffer_store_dword v5,v4,s[36:39],s70 offen offset:0 sc0 sc1
s_mov_b32 s46,s59
s_mov_b32 s69,18432
s_waitcnt lgkmcnt(0)
v_cmp_eq_u32 vcc,s60,v15
s_nop 0
s_cmp_eq_u32 vcc_lo,-1
s_cbranch_scc1 Lg2_go0_%=
s_add_u32 m0,s78,16640
s_nop 0
buffer_load_dword v6,s[36:39],s68 offen offset:0 sc1 lds
s_waitcnt vmcnt(6)
s_barrier
ds_read_b32 v15,v7 offset:8448
s_mov_b32 s69,18688
s_waitcnt lgkmcnt(0)
v_cmp_eq_u32 vcc,s60,v15
s_nop 0
s_cmp_eq_u32 vcc_lo,-1
s_cbranch_scc1 Lg2_go0_%=
s_add_u32 m0,s78,8192
s_nop 0
buffer_load_dword v6,s[36:39],s68 offen offset:0 sc1 lds
s_waitcnt vmcnt(4)
s_barrier
ds_read_b32 v15,v7 offset:16384
s_lshr_b32 s70,s47,5
buffer_store_dword v5,v4,s[36:39],s70 offen offset:0 sc0 sc1
s_mov_b32 s47,s59
s_mov_b32 s69,10240
s_waitcnt lgkmcnt(0)
v_cmp_eq_u32 vcc,s60,v15
s_nop 0
s_cmp_eq_u32 vcc_lo,-1
s_cbranch_scc1 Lg2_go0_%=
s_add_u32 m0,s78,8448
s_nop 0
buffer_load_dword v6,s[36:39],s68 offen offset:0 sc1 lds
s_waitcnt vmcnt(3)
s_barrier
ds_read_b32 v15,v7 offset:16640
s_mov_b32 s69,10496
s_waitcnt lgkmcnt(0)
v_cmp_eq_u32 vcc,s60,v15
s_nop 0
s_cmp_eq_u32 vcc_lo,-1
s_cbranch_scc1 Lg2_go0_%=
Lg2_poll0_%=:
s_add_u32 m0,s78,16384
s_nop 0
buffer_load_dword v6,s[36:39],s68 offen offset:0 sc1 lds
s_waitcnt vmcnt(2)
s_barrier
ds_read_b32 v15,v7 offset:8192
s_mov_b32 s69,18432
s_waitcnt lgkmcnt(0)
v_cmp_eq_u32 vcc,s60,v15
s_nop 0
s_cmp_eq_u32 vcc_lo,-1
s_cbranch_scc1 Lg2_go0_%=
s_sleep 4
s_add_u32 m0,s78,16640
s_nop 0
buffer_load_dword v6,s[36:39],s68 offen offset:0 sc1 lds
s_waitcnt vmcnt(2)
s_barrier
ds_read_b32 v15,v7 offset:8448
s_mov_b32 s69,18688
s_waitcnt lgkmcnt(0)
v_cmp_eq_u32 vcc,s60,v15
s_nop 0)"
R"(
s_cmp_eq_u32 vcc_lo,-1
s_cbranch_scc1 Lg2_go0_%=
s_sleep 4
s_add_u32 m0,s78,8192
s_nop 0
buffer_load_dword v6,s[36:39],s68 offen offset:0 sc1 lds
s_waitcnt vmcnt(2)
s_barrier
ds_read_b32 v15,v7 offset:16384
s_mov_b32 s69,10240
s_waitcnt lgkmcnt(0)
v_cmp_eq_u32 vcc,s60,v15
s_nop 0
s_cmp_eq_u32 vcc_lo,-1
s_cbranch_scc1 Lg2_go0_%=
s_sleep 4
s_add_u32 m0,s78,8448
s_nop 0
buffer_load_dword v6,s[36:39],s68 offen offset:0 sc1 lds
s_waitcnt vmcnt(2)
s_barrier
ds_read_b32 v15,v7 offset:16640
s_mov_b32 s69,10496
s_waitcnt lgkmcnt(0)
v_cmp_eq_u32 vcc,s60,v15
s_nop 0
s_cmp_eq_u32 vcc_lo,-1
s_cbranch_scc1 Lg2_go0_%=
s_sleep 4
s_add_u32 s79,s79,1
s_cmp_lt_u32 s79,20000
s_cbranch_scc1 Lg2_poll0_%=
Lg2_go0_%=:
s_xor_b32 s62,s63,65536
s_cmp_lt_u32 s66,s59
s_cbranch_scc1 Lg2_gs0_%=
s_mov_b32 m0,s62
s_nop 0
buffer_load_dwordx4 v0,s[36:39],s48 offen offset:0 sc1 lds
buffer_load_dwordx4 v0,s[36:39],s48 offen offset:1024 sc1 lds
buffer_load_dwordx4 v0,s[36:39],s48 offen offset:2048 sc1 lds
buffer_load_dwordx4 v0,s[36:39],s48 offen offset:3072 sc1 lds
s_add_u32 m0,s62,16384
s_lshr_b32 s72,s48,6
s_lshr_b32 s71,s62,2
buffer_load_dwordx4 v1,s[36:39],s48 offen offset:0 sc1 lds
buffer_load_dwordx4 v1,s[36:39],s48 offen offset:1024 sc1 lds
buffer_load_dwordx4 v1,s[36:39],s48 offen offset:2048 sc1 lds
buffer_load_dwordx4 v1,s[36:39],s48 offen offset:3072 sc1 lds
s_add_u32 m0,s71,s88
s_nop 0
buffer_load_dwordx4 v10,s[36:39],s48 offen offset:0 sc1 lds
buffer_load_dword v11,s[36:39],s72 offen offset:1024 sc1 lds
s_cmp_eq_u32 s54,1
s_cselect_b32 s68,s50,s49
s_lshr_b32 s68,s68,5
s_add_u32 m0,s78,256
s_nop 0
buffer_load_dword v6,s[36:39],s68 offen offset:0 sc1 lds
s_cmp_eq_u32 vcc_hi,-1
s_cselect_b32 s66,s49,s59
s_xor_b32 s62,s64,65536
s_mov_b32 m0,s62
s_nop 0
buffer_load_dwordx4 v0,s[36:39],s66 offen offset:0 sc1 lds
buffer_load_dwordx4 v0,s[36:39],s66 offen offset:1024 sc1 lds
buffer_load_dwordx4 v0,s[36:39],s66 offen offset:2048 sc1 lds
buffer_load_dwordx4 v0,s[36:39],s66 offen offset:3072 sc1 lds
s_add_u32 m0,s62,16384
s_nop 0
buffer_load_dwordx4 v1,s[36:39],s66 offen offset:0 sc1 lds
buffer_load_dwordx4 v1,s[36:39],s66 offen offset:1024 sc1 lds
s_waitcnt vmcnt(13)
s_barrier
ds_read_b128 v[32:35],v2 offset:0
ds_read_b128 v[36:39],v2 offset:1024
ds_read_b128 v[40:43],v2 offset:2048
ds_read_b128 v[44:47],v2 offset:3072
ds_read_b128 v[48:51],v2 offset:4096
ds_read_b128 v[52:55],v2 offset:5120
ds_read_b128 v[56:59],v2 offset:6144
s_lshr_b32 s70,s45,5
buffer_store_dword v5,v4,s[36:39],s70 offen offset:0 sc0 sc1
s_mov_b32 s45,s59
s_lshr_b32 s70,s46,5
buffer_store_dword v5,v4,s[36:39],s70 offen offset:0 sc0 sc1
s_mov_b32 s46,s59
s_lshr_b32 s70,s47,5
buffer_store_dword v5,v4,s[36:39],s70 offen offset:0 sc0 sc1
s_mov_b32 s47,s59
s_waitcnt lgkmcnt(3)
s_setpc_b64 s[90:91]
Lg2_gs0_%=:
ds_read_b128 v[32:35],v2 offset:0
ds_read_b128 v[36:39],v2 offset:1024
ds_read_b128 v[40:43],v2 offset:2048
ds_read_b128 v[44:47],v2 offset:3072
ds_read_b128 v[48:51],v2 offset:4096
ds_read_b128 v[52:55],v2 offset:5120
ds_read_b128 v[56:59],v2 offset:6144
s_cmp_eq_u32 s54,1
s_cselect_b32 s68,s50,s49
s_lshr_b32 s68,s68,5
s_add_u32 m0,s78,256
s_nop 0
buffer_load_dword v6,s[36:39],s68 offen offset:0 sc1 lds
s_cmp_eq_u32 vcc_hi,-1
s_cselect_b32 s66,s49,s59
s_xor_b32 s62,s64,65536
s_mov_b32 m0,s62
s_nop 0
buffer_load_dwordx4 v0,s[36:39],s66 offen offset:0 sc1 lds
buffer_load_dwordx4 v0,s[36:39],s66 offen offset:1024 sc1 lds
buffer_load_dwordx4 v0,s[36:39],s66 offen offset:2048 sc1 lds
buffer_load_dwordx4 v0,s[36:39],s66 offen offset:3072 sc1 lds
s_add_u32 m0,s62,16384
s_nop 0
buffer_load_dwordx4 v1,s[36:39],s66 offen offset:0 sc1 lds
buffer_load_dwordx4 v1,s[36:39],s66 offen offset:1024 sc1 lds
s_mov_b32 s45,s47
s_mov_b32 s47,s59
s_lshr_b32 s70,s59,5
buffer_store_dword v5,v4,s[36:39],s70 offen offset:0 sc0 sc1
s_lshr_b32 s70,s59,5
buffer_store_dword v5,v4,s[36:39],s70 offen offset:0 sc0 sc1
s_waitcnt lgkmcnt(3)
s_setpc_b64 s[90:91]
Lg2_done0_%=:
s_waitcnt vmcnt(0)
s_barrier
s_lshr_b32 s70,s45,5
buffer_store_dword v5,v4,s[36:39],s70 offen offset:0 sc0 sc1
s_lshr_b32 s70,s46,5
buffer_store_dword v5,v4,s[36:39],s70 offen offset:0 sc0 sc1
s_lshr_b32 s70,s47,5
buffer_store_dword v5,v4,s[36:39],s70 offen offset:0 sc0 sc1
s_branch Lg2_done_%=
Lg2_slow1_%=:
s_add_u32 m0,s62,16384
s_lshr_b32 s72,s67,6
s_lshr_b32 s71,s62,2
buffer_load_dwordx4 v1,s[36:39],s67 offen offset:2048 sc1 lds
buffer_load_dwordx4 v1,s[36:39],s67 offen offset:3072 sc1 lds
s_add_u32 m0,s71,s88
s_nop 0
buffer_load_dwordx4 v10,s[36:39],s67 offen offset:0 sc1 lds
buffer_load_dword v11,s[36:39],s72 offen offset:1024 sc1 lds
s_cmp_eq_u32 s54,1
s_cselect_b32 s68,s50,s49
s_lshr_b32 s68,s68,5
s_add_u32 m0,s78,8192
s_nop 0
buffer_load_dword v6,s[36:39],s68 offen offset:0 sc1 lds
s_lshr_b32 s73,s48,0
s_lshr_b32 s85,s48,5
v_lshlrev_b32 v64,16,v80
v_and_b32 v65,0xffff0000,v80
v_lshlrev_b32 v66,16,v81
v_and_b32 v67,0xffff0000,v81
v_lshlrev_b32 v68,16,v82
v_and_b32 v69,0xffff0000,v82
v_lshlrev_b32 v70,16,v83
v_and_b32 v71,0xffff0000,v83
v_fma_f32 v64,v64,v89,v88
v_fma_f32 v65,v65,v89,v88
v_fma_f32 v66,v66,v89,v88
v_fma_f32 v67,v67,v89,v88
v_fma_f32 v68,v68,v89,v88
v_fma_f32 v69,v69,v89,v88
v_fma_f32 v70,v70,v89,v88
v_fma_f32 v71,v71,v89,v88
v_add_f32 v16,v16,v64
v_add_f32 v17,v17,v65
v_add_f32 v18,v18,v66
v_add_f32 v19,v19,v67
v_add_f32 v20,v20,v68
v_add_f32 v21,v21,v69
v_add_f32 v22,v22,v70
v_add_f32 v23,v23,v71
v_cvt_pk_bf16_f32 v72,v16,v17
v_cvt_pk_bf16_f32 v73,v18,v19
v_cvt_pk_bf16_f32 v74,v20,v21
v_cvt_pk_bf16_f32 v75,v22,v23
s_add_u32 m0,s78,8448
s_nop 0
buffer_load_dword v6,s[36:39],s68 offen offset:0 sc1 lds
buffer_store_dwordx4 v[72:75],v3,s[36:39],s73 offen offset:0 sc0 sc1
v_add_f32 v64,v16,v17
v_mul_f32 v65,v16,v16
v_fma_f32 v65,v17,v17,v65
v_add_f32 v64,v64,v18
v_fma_f32 v65,v18,v18,v65
v_add_f32 v64,v64,v19
v_fma_f32 v65,v19,v19,v65
v_add_f32 v64,v64,v20
v_fma_f32 v65,v20,v20,v65
v_add_f32 v64,v64,v21
v_fma_f32 v65,v21,v21,v65
v_add_f32 v64,v64,v22
v_fma_f32 v65,v22,v22,v65)"
R"(
v_add_f32 v64,v64,v23
v_fma_f32 v65,v23,v23,v65
s_nop 1
v_permlane16_swap_b32 v64,v65
v_add_f32 v64,v64,v65
v_mov_b32 v65,v64
s_nop 1
v_permlane32_swap_b32 v64,v65
v_add_f32 v64,v64,v65
ds_write_b32 v13,v64 offset:2048
s_waitcnt lgkmcnt(0)
s_barrier
ds_read_b128 v[76:79],v14 offset:2048
s_waitcnt lgkmcnt(0)
v_add_f32 v76,v76,v77
v_add_f32 v78,v78,v79
v_add_f32 v76,v76,v78
buffer_store_dword v76,v12,s[36:39],s85 offen offset:0 sc0 sc1
s_cmp_ge_u32 s49,s59
s_cbranch_scc1 Lg2_done1_%=
s_add_u32 m0,s78,16384
s_nop 0
buffer_load_dword v6,s[36:39],s68 offen offset:0 sc1 lds
s_waitcnt vmcnt(4)
s_barrier
ds_read_b32 v15,v7 offset:8192
s_lshr_b32 s70,s46,5
buffer_store_dword v5,v4,s[36:39],s70 offen offset:0 sc0 sc1
s_mov_b32 s46,s59
s_lshr_b32 s70,s47,5
buffer_store_dword v5,v4,s[36:39],s70 offen offset:0 sc0 sc1
s_mov_b32 s47,s59
s_mov_b32 s69,18432
s_waitcnt lgkmcnt(0)
v_cmp_eq_u32 vcc,s60,v15
s_nop 0
s_cmp_eq_u32 vcc_lo,-1
s_cbranch_scc1 Lg2_go1_%=
s_add_u32 m0,s78,16640
s_nop 0
buffer_load_dword v6,s[36:39],s68 offen offset:0 sc1 lds
s_waitcnt vmcnt(6)
s_barrier
ds_read_b32 v15,v7 offset:8448
s_mov_b32 s69,18688
s_waitcnt lgkmcnt(0)
v_cmp_eq_u32 vcc,s60,v15
s_nop 0
s_cmp_eq_u32 vcc_lo,-1
s_cbranch_scc1 Lg2_go1_%=
s_add_u32 m0,s78,8192
s_nop 0
buffer_load_dword v6,s[36:39],s68 offen offset:0 sc1 lds
s_waitcnt vmcnt(4)
s_barrier
ds_read_b32 v15,v7 offset:16384
s_lshr_b32 s70,s48,5
buffer_store_dword v5,v4,s[36:39],s70 offen offset:0 sc0 sc1
s_mov_b32 s48,s59
s_mov_b32 s69,10240
s_waitcnt lgkmcnt(0)
v_cmp_eq_u32 vcc,s60,v15
s_nop 0
s_cmp_eq_u32 vcc_lo,-1
s_cbranch_scc1 Lg2_go1_%=
s_add_u32 m0,s78,8448
s_nop 0
buffer_load_dword v6,s[36:39],s68 offen offset:0 sc1 lds
s_waitcnt vmcnt(3)
s_barrier
ds_read_b32 v15,v7 offset:16640
s_mov_b32 s69,10496
s_waitcnt lgkmcnt(0)
v_cmp_eq_u32 vcc,s60,v15
s_nop 0
s_cmp_eq_u32 vcc_lo,-1
s_cbranch_scc1 Lg2_go1_%=
Lg2_poll1_%=:
s_add_u32 m0,s78,16384
s_nop 0
buffer_load_dword v6,s[36:39],s68 offen offset:0 sc1 lds
s_waitcnt vmcnt(2)
s_barrier
ds_read_b32 v15,v7 offset:8192
s_mov_b32 s69,18432
s_waitcnt lgkmcnt(0)
v_cmp_eq_u32 vcc,s60,v15
s_nop 0
s_cmp_eq_u32 vcc_lo,-1
s_cbranch_scc1 Lg2_go1_%=
s_sleep 4
s_add_u32 m0,s78,16640
s_nop 0
buffer_load_dword v6,s[36:39],s68 offen offset:0 sc1 lds
s_waitcnt vmcnt(2)
s_barrier
ds_read_b32 v15,v7 offset:8448
s_mov_b32 s69,18688
s_waitcnt lgkmcnt(0)
v_cmp_eq_u32 vcc,s60,v15
s_nop 0
s_cmp_eq_u32 vcc_lo,-1
s_cbranch_scc1 Lg2_go1_%=
s_sleep 4
s_add_u32 m0,s78,8192
s_nop 0
buffer_load_dword v6,s[36:39],s68 offen offset:0 sc1 lds
s_waitcnt vmcnt(2)
s_barrier
ds_read_b32 v15,v7 offset:16384
s_mov_b32 s69,10240
s_waitcnt lgkmcnt(0)
v_cmp_eq_u32 vcc,s60,v15
s_nop 0
s_cmp_eq_u32 vcc_lo,-1
s_cbranch_scc1 Lg2_go1_%=
s_sleep 4
s_add_u32 m0,s78,8448
s_nop 0
buffer_load_dword v6,s[36:39],s68 offen offset:0 sc1 lds
s_waitcnt vmcnt(2)
s_barrier
ds_read_b32 v15,v7 offset:16640
s_mov_b32 s69,10496
s_waitcnt lgkmcnt(0)
v_cmp_eq_u32 vcc,s60,v15
s_nop 0
s_cmp_eq_u32 vcc_lo,-1
s_cbranch_scc1 Lg2_go1_%=
s_sleep 4
s_add_u32 s79,s79,1
s_cmp_lt_u32 s79,20000
s_cbranch_scc1 Lg2_poll1_%=
Lg2_go1_%=:
s_xor_b32 s62,s64,65536
s_cmp_lt_u32 s67,s59
s_cbranch_scc1 Lg2_gs1_%=
s_mov_b32 m0,s62
s_nop 0
buffer_load_dwordx4 v0,s[36:39],s49 offen offset:0 sc1 lds
buffer_load_dwordx4 v0,s[36:39],s49 offen offset:1024 sc1 lds
buffer_load_dwordx4 v0,s[36:39],s49 offen offset:2048 sc1 lds
buffer_load_dwordx4 v0,s[36:39],s49 offen offset:3072 sc1 lds
s_add_u32 m0,s62,16384
s_lshr_b32 s72,s49,6
s_lshr_b32 s71,s62,2
buffer_load_dwordx4 v1,s[36:39],s49 offen offset:0 sc1 lds
buffer_load_dwordx4 v1,s[36:39],s49 offen offset:1024 sc1 lds
buffer_load_dwordx4 v1,s[36:39],s49 offen offset:2048 sc1 lds
buffer_load_dwordx4 v1,s[36:39],s49 offen offset:3072 sc1 lds
s_add_u32 m0,s71,s88
s_nop 0
buffer_load_dwordx4 v10,s[36:39],s49 offen offset:0 sc1 lds
buffer_load_dword v11,s[36:39],s72 offen offset:1024 sc1 lds
s_cmp_eq_u32 s54,1
s_cselect_b32 s68,s51,s50
s_lshr_b32 s68,s68,5
s_add_u32 m0,s78,0
s_nop 0
buffer_load_dword v6,s[36:39],s68 offen offset:0 sc1 lds
s_cmp_eq_u32 vcc_hi,-1
s_cselect_b32 s67,s50,s59
s_xor_b32 s62,s63,65536
s_mov_b32 m0,s62
s_nop 0
buffer_load_dwordx4 v0,s[36:39],s67 offen offset:0 sc1 lds
buffer_load_dwordx4 v0,s[36:39],s67 offen offset:1024 sc1 lds
buffer_load_dwordx4 v0,s[36:39],s67 offen offset:2048 sc1 lds
buffer_load_dwordx4 v0,s[36:39],s67 offen offset:3072 sc1 lds
s_add_u32 m0,s62,16384
s_nop 0
buffer_load_dwordx4 v1,s[36:39],s67 offen offset:0 sc1 lds
buffer_load_dwordx4 v1,s[36:39],s67 offen offset:1024 sc1 lds
s_waitcnt vmcnt(13)
s_barrier
ds_read_b128 v[32:35],v2 offset:32768
ds_read_b128 v[36:39],v2 offset:33792
ds_read_b128 v[40:43],v2 offset:34816
ds_read_b128 v[44:47],v2 offset:35840
ds_read_b128 v[48:51],v2 offset:36864
ds_read_b128 v[52:55],v2 offset:37888
ds_read_b128 v[56:59],v2 offset:38912
s_lshr_b32 s70,s46,5
buffer_store_dword v5,v4,s[36:39],s70 offen offset:0 sc0 sc1
s_mov_b32 s46,s59
s_lshr_b32 s70,s47,5
buffer_store_dword v5,v4,s[36:39],s70 offen offset:0 sc0 sc1
s_mov_b32 s47,s59
s_lshr_b32 s70,s48,5
buffer_store_dword v5,v4,s[36:39],s70 offen offset:0 sc0 sc1
s_mov_b32 s48,s59
s_waitcnt lgkmcnt(3)
s_setpc_b64 s[92:93]
Lg2_gs1_%=:
ds_read_b128 v[32:35],v2 offset:32768
ds_read_b128 v[36:39],v2 offset:33792
ds_read_b128 v[40:43],v2 offset:34816
ds_read_b128 v[44:47],v2 offset:35840
ds_read_b128 v[48:51],v2 offset:36864
ds_read_b128 v[52:55],v2 offset:37888)"
R"(
ds_read_b128 v[56:59],v2 offset:38912
s_cmp_eq_u32 s54,1
s_cselect_b32 s68,s51,s50
s_lshr_b32 s68,s68,5
s_add_u32 m0,s78,0
s_nop 0
buffer_load_dword v6,s[36:39],s68 offen offset:0 sc1 lds
s_cmp_eq_u32 vcc_hi,-1
s_cselect_b32 s67,s50,s59
s_xor_b32 s62,s63,65536
s_mov_b32 m0,s62
s_nop 0
buffer_load_dwordx4 v0,s[36:39],s67 offen offset:0 sc1 lds
buffer_load_dwordx4 v0,s[36:39],s67 offen offset:1024 sc1 lds
buffer_load_dwordx4 v0,s[36:39],s67 offen offset:2048 sc1 lds
buffer_load_dwordx4 v0,s[36:39],s67 offen offset:3072 sc1 lds
s_add_u32 m0,s62,16384
s_nop 0
buffer_load_dwordx4 v1,s[36:39],s67 offen offset:0 sc1 lds
buffer_load_dwordx4 v1,s[36:39],s67 offen offset:1024 sc1 lds
s_mov_b32 s46,s48
s_mov_b32 s48,s59
s_lshr_b32 s70,s59,5
buffer_store_dword v5,v4,s[36:39],s70 offen offset:0 sc0 sc1
s_lshr_b32 s70,s59,5
buffer_store_dword v5,v4,s[36:39],s70 offen offset:0 sc0 sc1
s_waitcnt lgkmcnt(3)
s_setpc_b64 s[92:93]
Lg2_done1_%=:
s_waitcnt vmcnt(0)
s_barrier
s_lshr_b32 s70,s46,5
buffer_store_dword v5,v4,s[36:39],s70 offen offset:0 sc0 sc1
s_lshr_b32 s70,s47,5
buffer_store_dword v5,v4,s[36:39],s70 offen offset:0 sc0 sc1
s_lshr_b32 s70,s48,5
buffer_store_dword v5,v4,s[36:39],s70 offen offset:0 sc0 sc1
s_branch Lg2_done_%=
Lg2_done_%=:
s_waitcnt vmcnt(0) lgkmcnt(0)
s_barrier
Lg2_end_%=:
s_mov_b32 %0,s60
Lcode_g12_e:)"
  : "=&s"(epOut) : "s"(ws), "s"(wave), "s"(sStep), "s"(sX1), "s"(sX2), "s"(sU0), "s"(sWw), "s"(sBuf), "v"(vSrc), "v"(vSrc2), "v"(vA), "v"(vOut), "v"(vFlag), "v"(vPollOff), "v"(vSnapRd), "v"(vX), "v"(vX2), "v"(vAuxS), "v"(vAuxS2), "v"(vOut2), "v"(vStW), "v"(vStR), "v"(vWd0), "v"(vWd1), "v"(vWr0), "v"(vWr1), "v"(vWr2), "s"(w12) : "v0", "v1", "v2", "v3", "v4", "v5", "v6", "v7", "v8", "v9", "v10", "v11", "v12", "v13", "v14", "v15", "v16", "v17", "v18", "v19", "v20", "v21", "v22", "v23", "v24", "v25", "v26", "v27", "v28", "v29", "v30", "v31", "v32", "v33", "v34", "v35", "v36", "v37", "v38", "v39", "v40", "v41", "v42", "v43", "v44", "v45", "v46", "v47", "v48", "v49", "v50", "v51", "v52", "v53", "v54", "v55", "v56", "v57", "v58", "v59", "v60", "v61", "v62", "v63", "v64", "v65", "v66", "v67", "v68", "v69", "v70", "v71", "v72", "v73", "v74", "v75", "v76", "v77", "v78", "v79", "v80", "v81", "v82", "v83", "v84", "v85", "v86", "v87", "v88", "v89", "v90", "v91", "v92", "v93", "v94", "v95", "v96", "v97", "v98", "v99", "v100", "v101", "v102", "v103", "v104", "v105", "v106", "v107", "v108", "v109", "v110", "v111", "v112", "v113", "v114", "v115", "v116", "v117", "v118", "v119", "v120", "v121", "v122", "v123", "v124", "v125", "v126", "v127", "v128", "v129", "v130", "v131", "v132", "v133", "v134", "v135", "v136", "v137", "v138", "v139", "v140", "v141", "v142", "v143", "v144", "v145", "v146", "v147", "v148", "v149", "v150", "v151", "v152", "v153", "v154", "v155", "v156", "v157", "v158", "v159", "v160", "v161", "v162", "v163", "v164", "v165", "v166", "v167", "v168", "v169", "v170", "v171", "v172", "v173", "v174", "v175", "v176", "v177", "v178", "v179", "v180", "v181", "v182", "v183", "v184", "v185", "v186", "v187", "v188", "v189", "v190", "v191", "v192", "v193", "v194", "v195", "v196", "v197", "v198", "v199", "v200", "v201", "v202", "v203", "v204", "v205", "v206", "v207", "v208", "v209", "v210", "v211", "v212", "v213", "v214", "v215", "v216", "v217", "v218", "v219", "v220", "v221", "v222", "v223", "v224", "v225", "v226", "v227", "v228", "v229", "v230", "v231", "v255", "a0", "a1", "a2", "a3", "a4", "a5", "a6", "a7", "a8", "a9", "a10", "a11", "a12", "a13", "a14", "a15", "a16", "a17", "a18", "a19", "a20", "a21", "a22", "a23", "a24", "a25", "a26", "a27", "a28", "a29", "a30", "a31", "a32", "a33", "a34", "a35", "a36", "a37", "a38", "a39", "a40", "a41", "a42", "a43", "a44", "a45", "a46", "a47", "a48", "a49", "a50", "a51", "a52", "a53", "a54", "a55", "a56", "a57", "a58", "a59", "a60", "a61", "a62", "a63", "a64", "a65", "a66", "a67", "a68", "a69", "a70", "a71", "a72", "a73", "a74", "a75", "a76", "a77", "a78", "a79", "a80", "a81", "a82", "a83", "a84", "a85", "a86", "a87", "a88", "a89", "a90", "a91", "a92", "a93", "a94", "a95", "a96", "a97", "a98", "a99", "a100", "a101", "a102", "a103", "a104", "a105", "a106", "a107", "a108", "a109", "a110", "a111", "a112", "a113", "a114", "a115", "a116", "a117", "a118", "a119", "a120", "a121", "a122", "a123", "a124", "a125", "a126", "a127", "a128", "a129", "a130", "a131", "a132", "a133", "a134", "a135", "a136", "a137", "a138", "a139", "a140", "a141", "a142", "a143", "a144", "a145", "a146", "a147", "a148", "a149", "a150", "a151", "a152", "a153", "a154", "a155", "a156", "a157", "a158", "a159", "a160", "a161", "a162", "a163", "a164", "a165", "a166", "a167", "a168", "a169", "a170", "a171", "a172", "a173", "a174", "a175", "a176", "a177", "a178", "a179", "a180", "a181", "a182", "a183", "a184", "a185", "a186", "a187", "a188", "a189", "a190", "a191", "a192", "a193", "a194", "a195", "a196", "a197", "a198", "a199", "a200", "a201", "a202", "a203", "a204", "a205", "a206", "a207", "a208", "a209", "a210", "a211", "a212", "a213", "a214", "a215", "a216", "a217", "a218", "a219", "a220", "a221", "a222", "a223", "a224", "a225", "a226", "a227", "a228", "a229", "a230", "a231", "a232", "a233", "a234", "a235", "a236", "a237", "a238", "a239", "a240", "a241", "a242", "a243", "a244", "a245", "a246", "a247", "a248", "a249", "a250", "a251", "a252", "a253", "a254", "a255", "s36", "s37", "s38", "s39", "s40", "s41", "s42", "s43", "s44", "s45", "s46", "s47", "s48", "s49", "s50", "s51", "s52", "s53", "s54", "s55", "s56", "s57", "s58", "s59", "s60", "s61", "s62", "s63", "s64", "s65", "s66", "s67", "s68", "s69", "s70", "s71", "s72", "s73", "s74", "s75", "s76", "s77", "s78", "s79", "s80", "s81", "s82", "s83", "s84", "s85", "s86", "s87", "s88", "s89", "s90", "s91", "s92", "s93", "s94", "s95", "s96", "s97", "s98", "s99", "vcc", "scc", "memory");
    ep = epOut;
   }
    if (kind == 6)
    {
      LANE_ID(lr);
      const int r8 = lr & 7, seg = lr >> 3;
      const unsigned sEpR = __builtin_amdgcn_readfirstlane(ep), sK = slice, sStep = NRW, sK1 = RT1 + slice, sRq = OFF_FXP + LB * 2048 + wave * 128, sDbg = sDbg0 + (kind == 6 ? 0 : 1024);
      const unsigned vRF = (wave * 8 + (seg >> 2)) * 1024 + ((seg & 3) * 16 + r8) * 16;
      const unsigned vRwr = sLds + (seg == 0 ? r8 * 64 + wave * 4 : 512 + lr * 4), vRrd = sLds + r8 * 64;
      const unsigned vRP = sLds + ((lr >> 4) == ((lr & 15) >> 2) ? r8 * 64 + 16 + wave * 8 + (seg & 1) * 4 : 768 + lr * 4);
      const unsigned vRS = (wave == 0 && seg == 0) ? r8 * 16 : 0x80000000u, vFlag = lr == 0 ? 0u : 0x80000000u, vL4 = (lr & 15) * 128;
      asm volatile(R"(
Lcode_r_b:
s_mov_b64 s[36:37],%0
s_mov_b32 s38,111193216
s_mov_b32 s39,0x00020000
s_mov_b32 s54,%1
s_mov_b32 s50,%2
s_mov_b32 s92,%3
s_mov_b32 s65,%4
s_mov_b32 s48,%5
v_mov_b32 v0,%6
v_mov_b32 v1,%7
v_mov_b32 v2,%8
v_mov_b32 v3,%9
v_mov_b32 v4,%10
v_mov_b32 v5,%11
v_mov_b32 v6,%12
v_lshrrev_b32 v10,1,v4
v_mov_b32 v7,s48
v_mbcnt_lo_u32_b32 v9,-1,0
v_mbcnt_hi_u32_b32 v9,-1,v9
v_lshlrev_b32 v9,4,v9
s_mov_b32 s74,0
s_mov_b32 s96,0
s_mov_b32 s95,0
s_mov_b32 s51,0x00003f80
s_mov_b32 s52,0x3f800000
s_mov_b32 s46,0xaaaaaaaa
s_mov_b32 s47,0xaaaaaaaa
s_mov_b32 s56,0xcccccccc
s_mov_b32 s57,0xcccccccc
s_mov_b32 s76,0x3a800000
s_mov_b32 s77,0x3727c5ac
s_mov_b32 s78,0xc0135761
s_mov_b32 s79,0x3d372713
v_mov_b32 v176,0x3f803f80
v_mov_b32 v177,0x3f803f80
v_mov_b32 v178,0x3f803f80
v_mov_b32 v179,0x3f803f80
s_lshr_b32 s63,s54,2
s_and_b32 s64,s54,1
s_lshl_b32 s64,s64,7
s_lshl_b32 s87,s63,16
s_add_u32 s87,s87,s64
s_and_b32 s64,s54,2
s_lshl_b32 s62,s64,14
s_add_u32 s87,s87,s62
s_add_u32 s58,s87,16908288
s_lshl_b32 s59,s54,8
s_and_b32 s64,s54,1
s_lshl_b32 s64,s64,7
s_sub_u32 s59,s59,s64
s_add_u32 s59,s59,107022336
s_lshl_b32 s49,s54,9
s_add_u32 s49,s49,s92
s_lshr_b32 s63,s54,1
s_lshl_b32 s91,s63,11
s_add_u32 s91,s91,109871104
s_add_u32 s70,s54,18
s_add_u32 s63,s54,s50
s_cmp_lt_u32 s54,0
s_cselect_b32 s70,s65,s70
s_cmp_lt_u32 s63,0
s_cselect_b32 s70,s63,s70
s_lshr_b32 s63,s70,1
s_lshl_b32 s69,s63,11
s_add_u32 s69,s69,109871104
Lr_bp9_%=:
buffer_load_dword v8,v6,s[36:39],s91 offen offset:0 sc1
s_waitcnt vmcnt(0)
v_cmp_eq_u32 vcc,s48,v8
s_nop 0
s_cmp_eq_u64 vcc,exec
s_cselect_b32 s63,1,s96
s_cmp_lg_u32 s63,0
s_cbranch_scc1 Lr_bq9_%=
s_add_u32 s74,s74,1
s_cmp_ge_u32 s74,20000
s_cselect_b32 s96,1,0
s_sleep 4
s_branch Lr_bp9_%=
Lr_bq9_%=:
s_add_u32 s63,s87,71303168
buffer_load_dwordx4 v[32:35],v1,s[36:39],s63 offen offset:0 sc1
buffer_load_dwordx4 v[36:39],v1,s[36:39],s63 offen offset:2048 sc1
s_add_u32 s63,s87,71307264
buffer_load_dwordx4 v[40:43],v1,s[36:39],s63 offen offset:0 sc1
buffer_load_dwordx4 v[44:47],v1,s[36:39],s63 offen offset:2048 sc1
s_add_u32 s63,s87,89128960
buffer_load_dwordx4 v[48:51],v1,s[36:39],s63 offen offset:0 sc1
buffer_load_dwordx4 v[52:55],v1,s[36:39],s63 offen offset:2048 sc1
s_add_u32 s63,s87,89133056
buffer_load_dwordx4 v[56:59],v1,s[36:39],s63 offen offset:0 sc1
buffer_load_dwordx4 v[60:63],v1,s[36:39],s63 offen offset:2048 sc1
s_add_u32 s63,s87,8388608
buffer_load_dwordx4 v[64:67],v1,s[36:39],s63 offen offset:0 sc1
buffer_load_dwordx4 v[68:71],v1,s[36:39],s63 offen offset:2048 sc1
s_add_u32 s63,s87,8392704
buffer_load_dwordx4 v[72:75],v1,s[36:39],s63 offen offset:0 sc1
buffer_load_dwordx4 v[76:79],v1,s[36:39],s63 offen offset:2048 sc1
buffer_load_dword v8,v6,s[36:39],s69 offen offset:0 sc1
s_waitcnt vmcnt(0)
Lr_it0_%=:
s_mov_b32 s88,s58
s_mov_b32 s89,s59
s_mov_b32 s90,s49
s_mov_b32 s54,s70
s_lshr_b32 s63,s54,2
s_and_b32 s64,s54,1
s_lshl_b32 s64,s64,7
s_lshl_b32 s87,s63,16
s_add_u32 s87,s87,s64
s_and_b32 s64,s54,2
s_lshl_b32 s62,s64,14
s_add_u32 s87,s87,s62
s_add_u32 s58,s87,16908288
s_lshl_b32 s59,s54,8
s_and_b32 s64,s54,1
s_lshl_b32 s64,s64,7
s_sub_u32 s59,s59,s64
s_add_u32 s59,s59,107022336
s_lshl_b32 s49,s54,9
s_add_u32 s49,s49,s92
s_lshr_b32 s63,s54,1
s_lshl_b32 s91,s63,11
s_add_u32 s91,s91,109871104
s_add_u32 s70,s54,18
s_add_u32 s63,s54,s50
s_cmp_lt_u32 s54,0
s_cselect_b32 s70,s65,s70
s_cmp_lt_u32 s63,0
s_cselect_b32 s70,s63,s70
s_lshr_b32 s63,s70,1
s_lshl_b32 s69,s63,11
s_add_u32 s69,s69,109871104
v_cmp_eq_u32 vcc,s48,v8
s_nop 0
s_cmp_eq_u64 vcc,exec
s_cselect_b32 s63,s54,256
s_cmp_lt_u32 s63,256
s_cbranch_scc1 Lr_ta0_%=
buffer_load_dword v8,v6,s[36:39],s91 offen offset:0 sc1
v_dot2_f32_bf16 v128,v32,s51,0
v_dot2_f32_bf16 v129,v32,s52,0
v_dot2_f32_bf16 v130,v33,s51,0
v_dot2_f32_bf16 v131,v33,s52,0
v_dot2_f32_bf16 v132,v34,s51,0
v_dot2_f32_bf16 v133,v34,s52,0
v_dot2_f32_bf16 v134,v35,s51,0
v_dot2_f32_bf16 v135,v35,s52,0
v_dot2_f32_bf16 v128,v48,s51,v128
v_dot2_f32_bf16 v129,v48,s52,v129
v_dot2_f32_bf16 v130,v49,s51,v130
v_dot2_f32_bf16 v131,v49,s52,v131
v_dot2_f32_bf16 v132,v50,s51,v132
v_dot2_f32_bf16 v133,v50,s52,v133
v_dot2_f32_bf16 v134,v51,s51,v134
v_dot2_f32_bf16 v135,v51,s52,v135
v_dot2_f32_bf16 v136,v36,s51,0
v_dot2_f32_bf16 v137,v36,s52,0
v_dot2_f32_bf16 v138,v37,s51,0
v_dot2_f32_bf16 v139,v37,s52,0
v_dot2_f32_bf16 v140,v38,s51,0
v_dot2_f32_bf16 v141,v38,s52,0
v_dot2_f32_bf16 v142,v39,s51,0
v_dot2_f32_bf16 v143,v39,s52,0
v_dot2_f32_bf16 v136,v52,s51,v136
v_dot2_f32_bf16 v137,v52,s52,v137
v_dot2_f32_bf16 v138,v53,s51,v138
v_dot2_f32_bf16 v139,v53,s52,v139
v_dot2_f32_bf16 v140,v54,s51,v140
v_dot2_f32_bf16 v141,v54,s52,v141
v_dot2_f32_bf16 v142,v55,s51,v142
v_dot2_f32_bf16 v143,v55,s52,v143
v_cvt_pk_bf16_f32 v208,v128,v129
v_cvt_pk_bf16_f32 v209,v130,v131
v_cvt_pk_bf16_f32 v210,v132,v133
v_cvt_pk_bf16_f32 v211,v134,v135
s_nop 1
v_mfma_f32_16x16x32_bf16 v[180:183],v[176:179],v[208:211],0
v_mfma_f32_16x16x32_bf16 v[184:187],v[208:211],v[208:211],0
v_dot2_f32_bf16 v144,v40,s51,0
v_dot2_f32_bf16 v145,v40,s52,0
v_dot2_f32_bf16 v146,v41,s51,0
v_dot2_f32_bf16 v147,v41,s52,0
v_dot2_f32_bf16 v148,v42,s51,0
v_dot2_f32_bf16 v149,v42,s52,0
v_dot2_f32_bf16 v150,v43,s51,0
v_dot2_f32_bf16 v151,v43,s52,0
v_dot2_f32_bf16 v144,v56,s51,v144
v_dot2_f32_bf16 v145,v56,s52,v145
v_dot2_f32_bf16 v146,v57,s51,v146
v_dot2_f32_bf16 v147,v57,s52,v147
v_dot2_f32_bf16 v148,v58,s51,v148
v_dot2_f32_bf16 v149,v58,s52,v149
v_dot2_f32_bf16 v150,v59,s51,v150
v_dot2_f32_bf16 v151,v59,s52,v151
v_cvt_pk_bf16_f32 v212,v136,v137
v_cvt_pk_bf16_f32 v213,v138,v139
v_cvt_pk_bf16_f32 v214,v140,v141
v_cvt_pk_bf16_f32 v215,v142,v143)"
R"(
s_nop 1
v_mfma_f32_16x16x32_bf16 v[180:183],v[176:179],v[212:215],v[180:183]
v_mfma_f32_16x16x32_bf16 v[184:187],v[212:215],v[212:215],v[184:187]
v_dot2_f32_bf16 v152,v44,s51,0
v_dot2_f32_bf16 v153,v44,s52,0
v_dot2_f32_bf16 v154,v45,s51,0
v_dot2_f32_bf16 v155,v45,s52,0
v_dot2_f32_bf16 v156,v46,s51,0
v_dot2_f32_bf16 v157,v46,s52,0
v_dot2_f32_bf16 v158,v47,s51,0
v_dot2_f32_bf16 v159,v47,s52,0
v_dot2_f32_bf16 v152,v60,s51,v152
v_dot2_f32_bf16 v153,v60,s52,v153
v_dot2_f32_bf16 v154,v61,s51,v154
v_dot2_f32_bf16 v155,v61,s52,v155
v_dot2_f32_bf16 v156,v62,s51,v156
v_dot2_f32_bf16 v157,v62,s52,v157
v_dot2_f32_bf16 v158,v63,s51,v158
v_dot2_f32_bf16 v159,v63,s52,v159
v_cvt_pk_bf16_f32 v216,v144,v145
v_cvt_pk_bf16_f32 v217,v146,v147
v_cvt_pk_bf16_f32 v218,v148,v149
v_cvt_pk_bf16_f32 v219,v150,v151
s_nop 1
v_mfma_f32_16x16x32_bf16 v[180:183],v[176:179],v[216:219],v[180:183]
v_mfma_f32_16x16x32_bf16 v[184:187],v[216:219],v[216:219],v[184:187]
v_cvt_pk_bf16_f32 v220,v152,v153
v_cvt_pk_bf16_f32 v221,v154,v155
v_cvt_pk_bf16_f32 v222,v156,v157
v_cvt_pk_bf16_f32 v223,v158,v159
s_nop 1
v_mfma_f32_16x16x32_bf16 v[180:183],v[176:179],v[220:223],v[180:183]
v_mfma_f32_16x16x32_bf16 v[184:187],v[220:223],v[220:223],v[184:187]
s_nop 6
v_add_f32_dpp v224,v180,v180 row_ror:8 row_mask:0xf bank_mask:0xf
v_cndmask_b32 v225,v184,v185,s[46:47]
v_cndmask_b32 v226,v186,v187,s[46:47]
ds_write_b32 v2,v224 offset:0
v_cndmask_b32 v225,v225,v226,s[56:57]
ds_write_b32 v0,v225 offset:0
s_waitcnt lgkmcnt(0)
s_barrier
ds_read_b128 v[160:163],v3 offset:0
ds_read_b128 v[164:167],v3 offset:16
ds_read_b128 v[168:171],v3 offset:32
s_waitcnt lgkmcnt(0)
v_pk_add_f32 v[160:161],v[160:161],v[162:163]
v_pk_add_f32 v[164:165],v[164:165],v[166:167]
v_pk_add_f32 v[168:169],v[168:169],v[170:171]
v_add_f32 v160,v160,v161
v_pk_add_f32 v[164:165],v[164:165],v[168:169]
v_mul_f32 v160,s76,v160
v_add_f32 v161,v164,v165
v_mul_f32 v161,s76,v161
v_fma_f32 v161,-v160,v160,v161
v_max_f32 v161,0,v161
s_waitcnt vmcnt(0)
s_cmp_eq_u32 s95,0
s_cbranch_scc1 Lr_pb1_%=
buffer_store_dword v7,v5,s[36:39],s60 offen offset:0 sc0 sc1
s_mov_b32 s95,0
Lr_pb1_%=:
v_cmp_eq_u32 vcc,s48,v8
s_nop 0
s_cmp_eq_u64 vcc,exec
s_cselect_b32 s63,s54,256
s_cmp_lt_u32 s63,256
s_cbranch_scc1 Lr_tb0_%=
buffer_load_dword v8,v6,s[36:39],s91 offen offset:0 sc1
v_add_f32 v161,s77,v161
v_rsq_f32 v228,v161
s_nop 0
v_mul_f32_e64 v230,-v160,v228
v_mov_b32 v229,v228
v_mov_b32 v231,v230
v_lshlrev_b32 v160,16,v64
v_and_b32 v161,0xffff0000,v64
v_pk_fma_f32 v[162:163],v[128:129],v[228:229],v[230:231]
v_pk_fma_f32 v[128:129],v[162:163],v[160:161],v[128:129]
v_lshlrev_b32 v164,16,v65
v_and_b32 v165,0xffff0000,v65
v_pk_fma_f32 v[166:167],v[130:131],v[228:229],v[230:231]
v_pk_fma_f32 v[130:131],v[166:167],v[164:165],v[130:131]
v_lshlrev_b32 v168,16,v66
v_and_b32 v169,0xffff0000,v66
v_pk_fma_f32 v[170:171],v[132:133],v[228:229],v[230:231]
v_pk_fma_f32 v[132:133],v[170:171],v[168:169],v[132:133]
v_lshlrev_b32 v172,16,v67
v_and_b32 v173,0xffff0000,v67
v_pk_fma_f32 v[174:175],v[134:135],v[228:229],v[230:231]
v_pk_fma_f32 v[134:135],v[174:175],v[172:173],v[134:135]
v_lshlrev_b32 v160,16,v68
v_and_b32 v161,0xffff0000,v68
v_pk_fma_f32 v[162:163],v[136:137],v[228:229],v[230:231]
v_pk_fma_f32 v[136:137],v[162:163],v[160:161],v[136:137]
v_lshlrev_b32 v164,16,v69
v_and_b32 v165,0xffff0000,v69
v_pk_fma_f32 v[166:167],v[138:139],v[228:229],v[230:231]
v_pk_fma_f32 v[138:139],v[166:167],v[164:165],v[138:139]
v_lshlrev_b32 v168,16,v70
v_and_b32 v169,0xffff0000,v70
v_pk_fma_f32 v[170:171],v[140:141],v[228:229],v[230:231]
v_pk_fma_f32 v[140:141],v[170:171],v[168:169],v[140:141]
v_lshlrev_b32 v172,16,v71
v_and_b32 v173,0xffff0000,v71
v_pk_fma_f32 v[174:175],v[142:143],v[228:229],v[230:231]
v_pk_fma_f32 v[142:143],v[174:175],v[172:173],v[142:143]
v_lshlrev_b32 v160,16,v72
v_and_b32 v161,0xffff0000,v72
v_pk_fma_f32 v[162:163],v[144:145],v[228:229],v[230:231]
v_pk_fma_f32 v[144:145],v[162:163],v[160:161],v[144:145]
v_lshlrev_b32 v164,16,v73
v_and_b32 v165,0xffff0000,v73
v_pk_fma_f32 v[166:167],v[146:147],v[228:229],v[230:231]
v_pk_fma_f32 v[146:147],v[166:167],v[164:165],v[146:147]
v_lshlrev_b32 v168,16,v74
v_and_b32 v169,0xffff0000,v74
v_pk_fma_f32 v[170:171],v[148:149],v[228:229],v[230:231]
v_pk_fma_f32 v[148:149],v[170:171],v[168:169],v[148:149]
v_lshlrev_b32 v172,16,v75
v_and_b32 v173,0xffff0000,v75
v_pk_fma_f32 v[174:175],v[150:151],v[228:229],v[230:231]
v_pk_fma_f32 v[150:151],v[174:175],v[172:173],v[150:151]
v_lshlrev_b32 v160,16,v76
v_and_b32 v161,0xffff0000,v76
v_pk_fma_f32 v[162:163],v[152:153],v[228:229],v[230:231]
v_pk_fma_f32 v[152:153],v[162:163],v[160:161],v[152:153]
v_lshlrev_b32 v164,16,v77
v_and_b32 v165,0xffff0000,v77
v_pk_fma_f32 v[166:167],v[154:155],v[228:229],v[230:231]
v_pk_fma_f32 v[154:155],v[166:167],v[164:165],v[154:155]
v_lshlrev_b32 v168,16,v78
v_and_b32 v169,0xffff0000,v78
v_pk_fma_f32 v[170:171],v[156:157],v[228:229],v[230:231]
v_pk_fma_f32 v[156:157],v[170:171],v[168:169],v[156:157]
v_lshlrev_b32 v172,16,v79
v_and_b32 v173,0xffff0000,v79
v_pk_fma_f32 v[174:175],v[158:159],v[228:229],v[230:231]
v_pk_fma_f32 v[158:159],v[174:175],v[172:173],v[158:159]
v_cvt_pk_bf16_f32 v208,v128,v129
v_cvt_pk_bf16_f32 v209,v130,v131
v_cvt_pk_bf16_f32 v210,v132,v133
v_cvt_pk_bf16_f32 v211,v134,v135
s_nop 1
v_mfma_f32_16x16x32_bf16 v[180:183],v[176:179],v[208:211],0
v_mfma_f32_16x16x32_bf16 v[184:187],v[208:211],v[208:211],0
buffer_store_dwordx4 v[208:211],v1,s[36:39],s88 offen offset:0 sc0 sc1
v_cvt_pk_bf16_f32 v212,v136,v137
v_cvt_pk_bf16_f32 v213,v138,v139
v_cvt_pk_bf16_f32 v214,v140,v141
v_cvt_pk_bf16_f32 v215,v142,v143
s_nop 1
v_mfma_f32_16x16x32_bf16 v[180:183],v[176:179],v[212:215],v[180:183]
v_mfma_f32_16x16x32_bf16 v[184:187],v[212:215],v[212:215],v[184:187]
buffer_store_dwordx4 v[212:215],v1,s[36:39],s88 offen offset:2048 sc0 sc1
v_cvt_pk_bf16_f32 v216,v144,v145
v_cvt_pk_bf16_f32 v217,v146,v147
v_cvt_pk_bf16_f32 v218,v148,v149
v_cvt_pk_bf16_f32 v219,v150,v151
s_nop 1
v_mfma_f32_16x16x32_bf16 v[180:183],v[176:179],v[216:219],v[180:183]
v_mfma_f32_16x16x32_bf16 v[184:187],v[216:219],v[216:219],v[184:187]
s_add_u32 s88,s88,4096
buffer_store_dwordx4 v[216:219],v1,s[36:39],s88 offen offset:0 sc0 sc1
v_cvt_pk_bf16_f32 v220,v152,v153
v_cvt_pk_bf16_f32 v221,v154,v155
v_cvt_pk_bf16_f32 v222,v156,v157
v_cvt_pk_bf16_f32 v223,v158,v159
s_nop 1
v_mfma_f32_16x16x32_bf16 v[180:183],v[176:179],v[220:223],v[180:183]
v_mfma_f32_16x16x32_bf16 v[184:187],v[220:223],v[220:223],v[184:187]
buffer_store_dwordx4 v[220:223],v1,s[36:39],s88 offen offset:2048 sc0 sc1
s_nop 5
v_add_f32_dpp v224,v180,v180 row_ror:8 row_mask:0xf bank_mask:0xf
v_cndmask_b32 v225,v184,v185,s[46:47]
v_cndmask_b32 v226,v186,v187,s[46:47]
ds_write_b32 v2,v224 offset:1024
v_cndmask_b32 v225,v225,v226,s[56:57]
ds_write_b32 v0,v225 offset:1024
s_waitcnt lgkmcnt(0)
s_barrier
ds_read_b128 v[160:163],v3 offset:1024
ds_read_b128 v[164:167],v3 offset:1040
ds_read_b128 v[168:171],v3 offset:1056
s_waitcnt lgkmcnt(0)
v_pk_add_f32 v[160:161],v[160:161],v[162:163]
v_pk_add_f32 v[164:165],v[164:165],v[166:167]
v_pk_add_f32 v[168:169],v[168:169],v[170:171]
v_add_f32 v160,v160,v161
v_pk_add_f32 v[164:165],v[164:165],v[168:169]
v_mul_f32 v160,s76,v160
v_add_f32 v161,v164,v165
v_mul_f32 v161,s76,v161
v_fma_f32 v161,-v160,v160,v161
v_max_f32 v161,0,v161
v_add_f32 v162,s77,v161
v_fma_f32 v163,s77,v162,v161
v_rsq_f32 v173,v162
v_sqrt_f32 v171,v163
v_rsq_f32 v162,v163)"
R"(
v_mov_b32 v168,v160
v_mul_f32_e64 v172,-v160,v173
v_mul_f32 v169,s78,v162
v_mul_f32 v163,v162,v162
v_mul_f32 v170,s79,v163
v_mul_f32 v170,v170,v169
buffer_store_dwordx4 v[168:171],v4,s[36:39],s89 offen offset:0 sc0 sc1
s_and_b32 s63,s89,128
s_lshr_b32 s63,s63,1
s_sub_u32 s89,s89,s63
buffer_store_dwordx2 v[172:173],v10,s[36:39],s89 offen offset:256 sc0 sc1
s_mov_b32 s60,s90
s_mov_b32 s95,1
s_waitcnt vmcnt(6)
v_cmp_eq_u32 vcc,s48,v8
s_nop 0
s_cmp_eq_u64 vcc,exec
s_cselect_b32 s63,s54,256
s_cmp_lt_u32 s63,256
s_cbranch_scc1 Lr_tc0_%=
s_waitcnt vmcnt(0)
s_cmp_eq_u32 s95,0
s_cbranch_scc1 Lr_pb2_%=
buffer_store_dword v7,v5,s[36:39],s60 offen offset:0 sc0 sc1
s_mov_b32 s95,0
Lr_pb2_%=:
s_cmp_ge_u32 s54,256
s_cbranch_scc1 Lr_done_%=
Lr_bp0_%=:
buffer_load_dword v8,v6,s[36:39],s91 offen offset:0 sc1
s_waitcnt vmcnt(0)
v_cmp_eq_u32 vcc,s48,v8
s_nop 0
s_cmp_eq_u64 vcc,exec
s_cselect_b32 s63,1,s96
s_cmp_lg_u32 s63,0
s_cbranch_scc1 Lr_bq0_%=
s_add_u32 s74,s74,1
s_cmp_ge_u32 s74,20000
s_cselect_b32 s96,1,0
s_sleep 4
s_branch Lr_bp0_%=
Lr_bq0_%=:
s_add_u32 s63,s87,71303168
buffer_load_dwordx4 v[80:83],v1,s[36:39],s63 offen offset:0 sc1
buffer_load_dwordx4 v[84:87],v1,s[36:39],s63 offen offset:2048 sc1
s_add_u32 s63,s87,71307264
buffer_load_dwordx4 v[88:91],v1,s[36:39],s63 offen offset:0 sc1
buffer_load_dwordx4 v[92:95],v1,s[36:39],s63 offen offset:2048 sc1
s_add_u32 s63,s87,89128960
buffer_load_dwordx4 v[96:99],v1,s[36:39],s63 offen offset:0 sc1
buffer_load_dwordx4 v[100:103],v1,s[36:39],s63 offen offset:2048 sc1
s_add_u32 s63,s87,89133056
buffer_load_dwordx4 v[104:107],v1,s[36:39],s63 offen offset:0 sc1
buffer_load_dwordx4 v[108:111],v1,s[36:39],s63 offen offset:2048 sc1
s_add_u32 s63,s87,8388608
buffer_load_dwordx4 v[112:115],v1,s[36:39],s63 offen offset:0 sc1
buffer_load_dwordx4 v[116:119],v1,s[36:39],s63 offen offset:2048 sc1
s_add_u32 s63,s87,8392704
buffer_load_dwordx4 v[120:123],v1,s[36:39],s63 offen offset:0 sc1
buffer_load_dwordx4 v[124:127],v1,s[36:39],s63 offen offset:2048 sc1
buffer_load_dword v8,v6,s[36:39],s69 offen offset:0 sc1
s_waitcnt vmcnt(0)
s_branch Lr_it1_%=
Lr_ta0_%=:
v_dot2_f32_bf16 v128,v32,s51,0
v_dot2_f32_bf16 v129,v32,s52,0
v_dot2_f32_bf16 v130,v33,s51,0
v_dot2_f32_bf16 v131,v33,s52,0
v_dot2_f32_bf16 v132,v34,s51,0
v_dot2_f32_bf16 v133,v34,s52,0
s_add_u32 s63,s87,71303168
buffer_load_dwordx4 v[80:83],v1,s[36:39],s63 offen offset:0 sc1
v_dot2_f32_bf16 v134,v35,s51,0
v_dot2_f32_bf16 v135,v35,s52,0
v_dot2_f32_bf16 v128,v48,s51,v128
v_dot2_f32_bf16 v129,v48,s52,v129
v_dot2_f32_bf16 v130,v49,s51,v130
v_dot2_f32_bf16 v131,v49,s52,v131
buffer_load_dwordx4 v[84:87],v1,s[36:39],s63 offen offset:2048 sc1
v_dot2_f32_bf16 v132,v50,s51,v132
v_dot2_f32_bf16 v133,v50,s52,v133
v_dot2_f32_bf16 v134,v51,s51,v134
v_dot2_f32_bf16 v135,v51,s52,v135
v_dot2_f32_bf16 v136,v36,s51,0
v_dot2_f32_bf16 v137,v36,s52,0
s_add_u32 s63,s87,71307264
buffer_load_dwordx4 v[88:91],v1,s[36:39],s63 offen offset:0 sc1
v_dot2_f32_bf16 v138,v37,s51,0
v_dot2_f32_bf16 v139,v37,s52,0
v_dot2_f32_bf16 v140,v38,s51,0
v_dot2_f32_bf16 v141,v38,s52,0
v_dot2_f32_bf16 v142,v39,s51,0
v_dot2_f32_bf16 v143,v39,s52,0
buffer_load_dwordx4 v[92:95],v1,s[36:39],s63 offen offset:2048 sc1
v_dot2_f32_bf16 v136,v52,s51,v136
v_dot2_f32_bf16 v137,v52,s52,v137
v_dot2_f32_bf16 v138,v53,s51,v138
v_dot2_f32_bf16 v139,v53,s52,v139
v_dot2_f32_bf16 v140,v54,s51,v140
v_dot2_f32_bf16 v141,v54,s52,v141
s_add_u32 s63,s87,89128960
buffer_load_dwordx4 v[96:99],v1,s[36:39],s63 offen offset:0 sc1
v_dot2_f32_bf16 v142,v55,s51,v142
v_dot2_f32_bf16 v143,v55,s52,v143
v_cvt_pk_bf16_f32 v208,v128,v129
v_cvt_pk_bf16_f32 v209,v130,v131
v_cvt_pk_bf16_f32 v210,v132,v133
v_cvt_pk_bf16_f32 v211,v134,v135
buffer_load_dwordx4 v[100:103],v1,s[36:39],s63 offen offset:2048 sc1
s_nop 0
v_mfma_f32_16x16x32_bf16 v[180:183],v[176:179],v[208:211],0
v_mfma_f32_16x16x32_bf16 v[184:187],v[208:211],v[208:211],0
v_dot2_f32_bf16 v144,v40,s51,0
v_dot2_f32_bf16 v145,v40,s52,0
v_dot2_f32_bf16 v146,v41,s51,0
v_dot2_f32_bf16 v147,v41,s52,0
s_add_u32 s63,s87,89133056
buffer_load_dwordx4 v[104:107],v1,s[36:39],s63 offen offset:0 sc1
v_dot2_f32_bf16 v148,v42,s51,0
v_dot2_f32_bf16 v149,v42,s52,0
v_dot2_f32_bf16 v150,v43,s51,0
v_dot2_f32_bf16 v151,v43,s52,0
v_dot2_f32_bf16 v144,v56,s51,v144
v_dot2_f32_bf16 v145,v56,s52,v145
buffer_load_dwordx4 v[108:111],v1,s[36:39],s63 offen offset:2048 sc1
v_dot2_f32_bf16 v146,v57,s51,v146
v_dot2_f32_bf16 v147,v57,s52,v147
v_dot2_f32_bf16 v148,v58,s51,v148
v_dot2_f32_bf16 v149,v58,s52,v149
v_dot2_f32_bf16 v150,v59,s51,v150
v_dot2_f32_bf16 v151,v59,s52,v151
s_add_u32 s63,s87,8388608
buffer_load_dwordx4 v[112:115],v1,s[36:39],s63 offen offset:0 sc1
v_cvt_pk_bf16_f32 v212,v136,v137
v_cvt_pk_bf16_f32 v213,v138,v139
v_cvt_pk_bf16_f32 v214,v140,v141
v_cvt_pk_bf16_f32 v215,v142,v143
s_nop 1
v_mfma_f32_16x16x32_bf16 v[180:183],v[176:179],v[212:215],v[180:183]
v_mfma_f32_16x16x32_bf16 v[184:187],v[212:215],v[212:215],v[184:187]
buffer_load_dwordx4 v[116:119],v1,s[36:39],s63 offen offset:2048 sc1
v_dot2_f32_bf16 v152,v44,s51,0
v_dot2_f32_bf16 v153,v44,s52,0
v_dot2_f32_bf16 v154,v45,s51,0
v_dot2_f32_bf16 v155,v45,s52,0
v_dot2_f32_bf16 v156,v46,s51,0
v_dot2_f32_bf16 v157,v46,s52,0
s_add_u32 s63,s87,8392704
buffer_load_dwordx4 v[120:123],v1,s[36:39],s63 offen offset:0 sc1
v_dot2_f32_bf16 v158,v47,s51,0
v_dot2_f32_bf16 v159,v47,s52,0
v_dot2_f32_bf16 v152,v60,s51,v152
v_dot2_f32_bf16 v153,v60,s52,v153
v_dot2_f32_bf16 v154,v61,s51,v154
v_dot2_f32_bf16 v155,v61,s52,v155
buffer_load_dwordx4 v[124:127],v1,s[36:39],s63 offen offset:2048 sc1
v_dot2_f32_bf16 v156,v62,s51,v156
v_dot2_f32_bf16 v157,v62,s52,v157
v_dot2_f32_bf16 v158,v63,s51,v158
v_dot2_f32_bf16 v159,v63,s52,v159
v_cvt_pk_bf16_f32 v216,v144,v145
v_cvt_pk_bf16_f32 v217,v146,v147
v_cvt_pk_bf16_f32 v218,v148,v149
v_cvt_pk_bf16_f32 v219,v150,v151
s_nop 1
v_mfma_f32_16x16x32_bf16 v[180:183],v[176:179],v[216:219],v[180:183]
v_mfma_f32_16x16x32_bf16 v[184:187],v[216:219],v[216:219],v[184:187]
v_cvt_pk_bf16_f32 v220,v152,v153
v_cvt_pk_bf16_f32 v221,v154,v155
v_cvt_pk_bf16_f32 v222,v156,v157
v_cvt_pk_bf16_f32 v223,v158,v159
s_nop 1
v_mfma_f32_16x16x32_bf16 v[180:183],v[176:179],v[220:223],v[180:183]
v_mfma_f32_16x16x32_bf16 v[184:187],v[220:223],v[220:223],v[184:187]
s_nop 6
v_add_f32_dpp v224,v180,v180 row_ror:8 row_mask:0xf bank_mask:0xf
v_cndmask_b32 v225,v184,v185,s[46:47]
v_cndmask_b32 v226,v186,v187,s[46:47]
ds_write_b32 v2,v224 offset:0
v_cndmask_b32 v225,v225,v226,s[56:57]
ds_write_b32 v0,v225 offset:0
s_waitcnt lgkmcnt(0)
s_barrier
ds_read_b128 v[160:163],v3 offset:0
ds_read_b128 v[164:167],v3 offset:16
ds_read_b128 v[168:171],v3 offset:32
s_waitcnt lgkmcnt(0)
v_pk_add_f32 v[160:161],v[160:161],v[162:163]
v_pk_add_f32 v[164:165],v[164:165],v[166:167]
v_pk_add_f32 v[168:169],v[168:169],v[170:171]
v_add_f32 v160,v160,v161
v_pk_add_f32 v[164:165],v[164:165],v[168:169]
v_mul_f32 v160,s76,v160
v_add_f32 v161,v164,v165
v_mul_f32 v161,s76,v161
v_fma_f32 v161,-v160,v160,v161
v_max_f32 v161,0,v161
s_waitcnt vmcnt(12)
s_cmp_eq_u32 s95,0)"
R"(
s_cbranch_scc1 Lr_pb3_%=
buffer_store_dword v7,v5,s[36:39],s60 offen offset:0 sc0 sc1
s_mov_b32 s95,0
Lr_pb3_%=:
v_add_f32 v161,s77,v161
v_rsq_f32 v228,v161
s_nop 0
v_mul_f32_e64 v230,-v160,v228
v_mov_b32 v229,v228
v_mov_b32 v231,v230
v_lshlrev_b32 v160,16,v64
v_and_b32 v161,0xffff0000,v64
v_pk_fma_f32 v[162:163],v[128:129],v[228:229],v[230:231]
v_pk_fma_f32 v[128:129],v[162:163],v[160:161],v[128:129]
v_lshlrev_b32 v164,16,v65
v_and_b32 v165,0xffff0000,v65
v_pk_fma_f32 v[166:167],v[130:131],v[228:229],v[230:231]
v_pk_fma_f32 v[130:131],v[166:167],v[164:165],v[130:131]
v_lshlrev_b32 v168,16,v66
v_and_b32 v169,0xffff0000,v66
v_pk_fma_f32 v[170:171],v[132:133],v[228:229],v[230:231]
v_pk_fma_f32 v[132:133],v[170:171],v[168:169],v[132:133]
v_lshlrev_b32 v172,16,v67
v_and_b32 v173,0xffff0000,v67
v_pk_fma_f32 v[174:175],v[134:135],v[228:229],v[230:231]
v_pk_fma_f32 v[134:135],v[174:175],v[172:173],v[134:135]
v_lshlrev_b32 v160,16,v68
v_and_b32 v161,0xffff0000,v68
v_pk_fma_f32 v[162:163],v[136:137],v[228:229],v[230:231]
v_pk_fma_f32 v[136:137],v[162:163],v[160:161],v[136:137]
v_lshlrev_b32 v164,16,v69
v_and_b32 v165,0xffff0000,v69
v_pk_fma_f32 v[166:167],v[138:139],v[228:229],v[230:231]
v_pk_fma_f32 v[138:139],v[166:167],v[164:165],v[138:139]
v_lshlrev_b32 v168,16,v70
v_and_b32 v169,0xffff0000,v70
v_pk_fma_f32 v[170:171],v[140:141],v[228:229],v[230:231]
v_pk_fma_f32 v[140:141],v[170:171],v[168:169],v[140:141]
v_lshlrev_b32 v172,16,v71
v_and_b32 v173,0xffff0000,v71
v_pk_fma_f32 v[174:175],v[142:143],v[228:229],v[230:231]
v_pk_fma_f32 v[142:143],v[174:175],v[172:173],v[142:143]
v_lshlrev_b32 v160,16,v72
v_and_b32 v161,0xffff0000,v72
v_pk_fma_f32 v[162:163],v[144:145],v[228:229],v[230:231]
v_pk_fma_f32 v[144:145],v[162:163],v[160:161],v[144:145]
v_lshlrev_b32 v164,16,v73
v_and_b32 v165,0xffff0000,v73
v_pk_fma_f32 v[166:167],v[146:147],v[228:229],v[230:231]
v_pk_fma_f32 v[146:147],v[166:167],v[164:165],v[146:147]
v_lshlrev_b32 v168,16,v74
v_and_b32 v169,0xffff0000,v74
v_pk_fma_f32 v[170:171],v[148:149],v[228:229],v[230:231]
v_pk_fma_f32 v[148:149],v[170:171],v[168:169],v[148:149]
v_lshlrev_b32 v172,16,v75
v_and_b32 v173,0xffff0000,v75
v_pk_fma_f32 v[174:175],v[150:151],v[228:229],v[230:231]
v_pk_fma_f32 v[150:151],v[174:175],v[172:173],v[150:151]
v_lshlrev_b32 v160,16,v76
v_and_b32 v161,0xffff0000,v76
v_pk_fma_f32 v[162:163],v[152:153],v[228:229],v[230:231]
v_pk_fma_f32 v[152:153],v[162:163],v[160:161],v[152:153]
v_lshlrev_b32 v164,16,v77
v_and_b32 v165,0xffff0000,v77
v_pk_fma_f32 v[166:167],v[154:155],v[228:229],v[230:231]
v_pk_fma_f32 v[154:155],v[166:167],v[164:165],v[154:155]
v_lshlrev_b32 v168,16,v78
v_and_b32 v169,0xffff0000,v78
v_pk_fma_f32 v[170:171],v[156:157],v[228:229],v[230:231]
v_pk_fma_f32 v[156:157],v[170:171],v[168:169],v[156:157]
v_lshlrev_b32 v172,16,v79
v_and_b32 v173,0xffff0000,v79
v_pk_fma_f32 v[174:175],v[158:159],v[228:229],v[230:231]
v_pk_fma_f32 v[158:159],v[174:175],v[172:173],v[158:159]
buffer_load_dword v8,v6,s[36:39],s69 offen offset:0 sc1
v_cvt_pk_bf16_f32 v208,v128,v129
v_cvt_pk_bf16_f32 v209,v130,v131
v_cvt_pk_bf16_f32 v210,v132,v133
v_cvt_pk_bf16_f32 v211,v134,v135
s_nop 1
v_mfma_f32_16x16x32_bf16 v[180:183],v[176:179],v[208:211],0
v_mfma_f32_16x16x32_bf16 v[184:187],v[208:211],v[208:211],0
buffer_store_dwordx4 v[208:211],v1,s[36:39],s88 offen offset:0 sc0 sc1
v_cvt_pk_bf16_f32 v212,v136,v137
v_cvt_pk_bf16_f32 v213,v138,v139
v_cvt_pk_bf16_f32 v214,v140,v141
v_cvt_pk_bf16_f32 v215,v142,v143
s_nop 1
v_mfma_f32_16x16x32_bf16 v[180:183],v[176:179],v[212:215],v[180:183]
v_mfma_f32_16x16x32_bf16 v[184:187],v[212:215],v[212:215],v[184:187]
buffer_store_dwordx4 v[212:215],v1,s[36:39],s88 offen offset:2048 sc0 sc1
v_cvt_pk_bf16_f32 v216,v144,v145
v_cvt_pk_bf16_f32 v217,v146,v147
v_cvt_pk_bf16_f32 v218,v148,v149
v_cvt_pk_bf16_f32 v219,v150,v151
s_nop 1
v_mfma_f32_16x16x32_bf16 v[180:183],v[176:179],v[216:219],v[180:183]
v_mfma_f32_16x16x32_bf16 v[184:187],v[216:219],v[216:219],v[184:187]
s_add_u32 s88,s88,4096
buffer_store_dwordx4 v[216:219],v1,s[36:39],s88 offen offset:0 sc0 sc1
v_cvt_pk_bf16_f32 v220,v152,v153
v_cvt_pk_bf16_f32 v221,v154,v155
v_cvt_pk_bf16_f32 v222,v156,v157
v_cvt_pk_bf16_f32 v223,v158,v159
s_nop 1
v_mfma_f32_16x16x32_bf16 v[180:183],v[176:179],v[220:223],v[180:183]
v_mfma_f32_16x16x32_bf16 v[184:187],v[220:223],v[220:223],v[184:187]
buffer_store_dwordx4 v[220:223],v1,s[36:39],s88 offen offset:2048 sc0 sc1
s_nop 5
v_add_f32_dpp v224,v180,v180 row_ror:8 row_mask:0xf bank_mask:0xf
v_cndmask_b32 v225,v184,v185,s[46:47]
v_cndmask_b32 v226,v186,v187,s[46:47]
ds_write_b32 v2,v224 offset:1024
v_cndmask_b32 v225,v225,v226,s[56:57]
ds_write_b32 v0,v225 offset:1024
s_waitcnt lgkmcnt(0)
s_barrier
ds_read_b128 v[160:163],v3 offset:1024
ds_read_b128 v[164:167],v3 offset:1040
ds_read_b128 v[168:171],v3 offset:1056
s_waitcnt lgkmcnt(0)
v_pk_add_f32 v[160:161],v[160:161],v[162:163]
v_pk_add_f32 v[164:165],v[164:165],v[166:167]
v_pk_add_f32 v[168:169],v[168:169],v[170:171]
v_add_f32 v160,v160,v161
v_pk_add_f32 v[164:165],v[164:165],v[168:169]
v_mul_f32 v160,s76,v160
v_add_f32 v161,v164,v165
v_mul_f32 v161,s76,v161
v_fma_f32 v161,-v160,v160,v161
v_max_f32 v161,0,v161
v_add_f32 v162,s77,v161
v_fma_f32 v163,s77,v162,v161
v_rsq_f32 v173,v162
v_sqrt_f32 v171,v163
v_rsq_f32 v162,v163
v_mov_b32 v168,v160
v_mul_f32_e64 v172,-v160,v173
v_mul_f32 v169,s78,v162
v_mul_f32 v163,v162,v162
v_mul_f32 v170,s79,v163
v_mul_f32 v170,v170,v169
buffer_store_dwordx4 v[168:171],v4,s[36:39],s89 offen offset:0 sc0 sc1
s_and_b32 s63,s89,128
s_lshr_b32 s63,s63,1
s_sub_u32 s89,s89,s63
buffer_store_dwordx2 v[172:173],v10,s[36:39],s89 offen offset:256 sc0 sc1
s_mov_b32 s60,s90
s_mov_b32 s95,1
s_waitcnt vmcnt(6)
s_branch Lr_it1_%=
Lr_tb0_%=:
v_add_f32 v161,s77,v161
v_rsq_f32 v228,v161
s_nop 0
v_mul_f32_e64 v230,-v160,v228
v_mov_b32 v229,v228
v_mov_b32 v231,v230
s_add_u32 s63,s87,71303168
buffer_load_dwordx4 v[80:83],v1,s[36:39],s63 offen offset:0 sc1
v_lshlrev_b32 v160,16,v64
v_and_b32 v161,0xffff0000,v64
v_pk_fma_f32 v[162:163],v[128:129],v[228:229],v[230:231]
v_pk_fma_f32 v[128:129],v[162:163],v[160:161],v[128:129]
v_lshlrev_b32 v164,16,v65
buffer_load_dwordx4 v[84:87],v1,s[36:39],s63 offen offset:2048 sc1
v_and_b32 v165,0xffff0000,v65
v_pk_fma_f32 v[166:167],v[130:131],v[228:229],v[230:231]
v_pk_fma_f32 v[130:131],v[166:167],v[164:165],v[130:131]
v_lshlrev_b32 v168,16,v66
v_and_b32 v169,0xffff0000,v66
s_add_u32 s63,s87,71307264
buffer_load_dwordx4 v[88:91],v1,s[36:39],s63 offen offset:0 sc1
v_pk_fma_f32 v[170:171],v[132:133],v[228:229],v[230:231]
v_pk_fma_f32 v[132:133],v[170:171],v[168:169],v[132:133]
v_lshlrev_b32 v172,16,v67
v_and_b32 v173,0xffff0000,v67
v_pk_fma_f32 v[174:175],v[134:135],v[228:229],v[230:231]
buffer_load_dwordx4 v[92:95],v1,s[36:39],s63 offen offset:2048 sc1
v_pk_fma_f32 v[134:135],v[174:175],v[172:173],v[134:135]
v_lshlrev_b32 v160,16,v68
v_and_b32 v161,0xffff0000,v68
v_pk_fma_f32 v[162:163],v[136:137],v[228:229],v[230:231]
v_pk_fma_f32 v[136:137],v[162:163],v[160:161],v[136:137]
s_add_u32 s63,s87,89128960
buffer_load_dwordx4 v[96:99],v1,s[36:39],s63 offen offset:0 sc1
v_lshlrev_b32 v164,16,v69
v_and_b32 v165,0xffff0000,v69
v_pk_fma_f32 v[166:167],v[138:139],v[228:229],v[230:231]
v_pk_fma_f32 v[138:139],v[166:167],v[164:165],v[138:139]
v_lshlrev_b32 v168,16,v70
buffer_load_dwordx4 v[100:103],v1,s[36:39],s63 offen offset:2048 sc1
v_and_b32 v169,0xffff0000,v70
v_pk_fma_f32 v[170:171],v[140:141],v[228:229],v[230:231]
v_pk_fma_f32 v[140:141],v[170:171],v[168:169],v[140:141]
v_lshlrev_b32 v172,16,v71
v_and_b32 v173,0xffff0000,v71
s_add_u32 s63,s87,89133056
buffer_load_dwordx4 v[104:107],v1,s[36:39],s63 offen offset:0 sc1
v_pk_fma_f32 v[174:175],v[142:143],v[228:229],v[230:231])"
R"(
v_pk_fma_f32 v[142:143],v[174:175],v[172:173],v[142:143]
v_lshlrev_b32 v160,16,v72
v_and_b32 v161,0xffff0000,v72
v_pk_fma_f32 v[162:163],v[144:145],v[228:229],v[230:231]
buffer_load_dwordx4 v[108:111],v1,s[36:39],s63 offen offset:2048 sc1
v_pk_fma_f32 v[144:145],v[162:163],v[160:161],v[144:145]
v_lshlrev_b32 v164,16,v73
v_and_b32 v165,0xffff0000,v73
v_pk_fma_f32 v[166:167],v[146:147],v[228:229],v[230:231]
v_pk_fma_f32 v[146:147],v[166:167],v[164:165],v[146:147]
s_add_u32 s63,s87,8388608
buffer_load_dwordx4 v[112:115],v1,s[36:39],s63 offen offset:0 sc1
v_lshlrev_b32 v168,16,v74
v_and_b32 v169,0xffff0000,v74
v_pk_fma_f32 v[170:171],v[148:149],v[228:229],v[230:231]
v_pk_fma_f32 v[148:149],v[170:171],v[168:169],v[148:149]
v_lshlrev_b32 v172,16,v75
buffer_load_dwordx4 v[116:119],v1,s[36:39],s63 offen offset:2048 sc1
v_and_b32 v173,0xffff0000,v75
v_pk_fma_f32 v[174:175],v[150:151],v[228:229],v[230:231]
v_pk_fma_f32 v[150:151],v[174:175],v[172:173],v[150:151]
v_lshlrev_b32 v160,16,v76
v_and_b32 v161,0xffff0000,v76
s_add_u32 s63,s87,8392704
buffer_load_dwordx4 v[120:123],v1,s[36:39],s63 offen offset:0 sc1
v_pk_fma_f32 v[162:163],v[152:153],v[228:229],v[230:231]
v_pk_fma_f32 v[152:153],v[162:163],v[160:161],v[152:153]
v_lshlrev_b32 v164,16,v77
v_and_b32 v165,0xffff0000,v77
v_pk_fma_f32 v[166:167],v[154:155],v[228:229],v[230:231]
buffer_load_dwordx4 v[124:127],v1,s[36:39],s63 offen offset:2048 sc1
v_pk_fma_f32 v[154:155],v[166:167],v[164:165],v[154:155]
v_lshlrev_b32 v168,16,v78
v_and_b32 v169,0xffff0000,v78
v_pk_fma_f32 v[170:171],v[156:157],v[228:229],v[230:231]
v_pk_fma_f32 v[156:157],v[170:171],v[168:169],v[156:157]
v_lshlrev_b32 v172,16,v79
v_and_b32 v173,0xffff0000,v79
v_pk_fma_f32 v[174:175],v[158:159],v[228:229],v[230:231]
v_pk_fma_f32 v[158:159],v[174:175],v[172:173],v[158:159]
buffer_load_dword v8,v6,s[36:39],s69 offen offset:0 sc1
v_cvt_pk_bf16_f32 v208,v128,v129
v_cvt_pk_bf16_f32 v209,v130,v131
v_cvt_pk_bf16_f32 v210,v132,v133
v_cvt_pk_bf16_f32 v211,v134,v135
s_nop 1
v_mfma_f32_16x16x32_bf16 v[180:183],v[176:179],v[208:211],0
v_mfma_f32_16x16x32_bf16 v[184:187],v[208:211],v[208:211],0
buffer_store_dwordx4 v[208:211],v1,s[36:39],s88 offen offset:0 sc0 sc1
v_cvt_pk_bf16_f32 v212,v136,v137
v_cvt_pk_bf16_f32 v213,v138,v139
v_cvt_pk_bf16_f32 v214,v140,v141
v_cvt_pk_bf16_f32 v215,v142,v143
s_nop 1
v_mfma_f32_16x16x32_bf16 v[180:183],v[176:179],v[212:215],v[180:183]
v_mfma_f32_16x16x32_bf16 v[184:187],v[212:215],v[212:215],v[184:187]
buffer_store_dwordx4 v[212:215],v1,s[36:39],s88 offen offset:2048 sc0 sc1
v_cvt_pk_bf16_f32 v216,v144,v145
v_cvt_pk_bf16_f32 v217,v146,v147
v_cvt_pk_bf16_f32 v218,v148,v149
v_cvt_pk_bf16_f32 v219,v150,v151
s_nop 1
v_mfma_f32_16x16x32_bf16 v[180:183],v[176:179],v[216:219],v[180:183]
v_mfma_f32_16x16x32_bf16 v[184:187],v[216:219],v[216:219],v[184:187]
s_add_u32 s88,s88,4096
buffer_store_dwordx4 v[216:219],v1,s[36:39],s88 offen offset:0 sc0 sc1
v_cvt_pk_bf16_f32 v220,v152,v153
v_cvt_pk_bf16_f32 v221,v154,v155
v_cvt_pk_bf16_f32 v222,v156,v157
v_cvt_pk_bf16_f32 v223,v158,v159
s_nop 1
v_mfma_f32_16x16x32_bf16 v[180:183],v[176:179],v[220:223],v[180:183]
v_mfma_f32_16x16x32_bf16 v[184:187],v[220:223],v[220:223],v[184:187]
buffer_store_dwordx4 v[220:223],v1,s[36:39],s88 offen offset:2048 sc0 sc1
s_nop 5
v_add_f32_dpp v224,v180,v180 row_ror:8 row_mask:0xf bank_mask:0xf
v_cndmask_b32 v225,v184,v185,s[46:47]
v_cndmask_b32 v226,v186,v187,s[46:47]
ds_write_b32 v2,v224 offset:1024
v_cndmask_b32 v225,v225,v226,s[56:57]
ds_write_b32 v0,v225 offset:1024
s_waitcnt lgkmcnt(0)
s_barrier
ds_read_b128 v[160:163],v3 offset:1024
ds_read_b128 v[164:167],v3 offset:1040
ds_read_b128 v[168:171],v3 offset:1056
s_waitcnt lgkmcnt(0)
v_pk_add_f32 v[160:161],v[160:161],v[162:163]
v_pk_add_f32 v[164:165],v[164:165],v[166:167]
v_pk_add_f32 v[168:169],v[168:169],v[170:171]
v_add_f32 v160,v160,v161
v_pk_add_f32 v[164:165],v[164:165],v[168:169]
v_mul_f32 v160,s76,v160
v_add_f32 v161,v164,v165
v_mul_f32 v161,s76,v161
v_fma_f32 v161,-v160,v160,v161
v_max_f32 v161,0,v161
v_add_f32 v162,s77,v161
v_fma_f32 v163,s77,v162,v161
v_rsq_f32 v173,v162
v_sqrt_f32 v171,v163
v_rsq_f32 v162,v163
v_mov_b32 v168,v160
v_mul_f32_e64 v172,-v160,v173
v_mul_f32 v169,s78,v162
v_mul_f32 v163,v162,v162
v_mul_f32 v170,s79,v163
v_mul_f32 v170,v170,v169
buffer_store_dwordx4 v[168:171],v4,s[36:39],s89 offen offset:0 sc0 sc1
s_and_b32 s63,s89,128
s_lshr_b32 s63,s63,1
s_sub_u32 s89,s89,s63
buffer_store_dwordx2 v[172:173],v10,s[36:39],s89 offen offset:256 sc0 sc1
s_mov_b32 s60,s90
s_mov_b32 s95,1
s_waitcnt vmcnt(6)
s_branch Lr_it1_%=
Lr_tc0_%=:
s_add_u32 s63,s87,71303168
buffer_load_dwordx4 v[80:83],v1,s[36:39],s63 offen offset:0 sc1
buffer_load_dwordx4 v[84:87],v1,s[36:39],s63 offen offset:2048 sc1
s_add_u32 s63,s87,71307264
buffer_load_dwordx4 v[88:91],v1,s[36:39],s63 offen offset:0 sc1
buffer_load_dwordx4 v[92:95],v1,s[36:39],s63 offen offset:2048 sc1
s_add_u32 s63,s87,89128960
buffer_load_dwordx4 v[96:99],v1,s[36:39],s63 offen offset:0 sc1
buffer_load_dwordx4 v[100:103],v1,s[36:39],s63 offen offset:2048 sc1
s_add_u32 s63,s87,89133056
buffer_load_dwordx4 v[104:107],v1,s[36:39],s63 offen offset:0 sc1
buffer_load_dwordx4 v[108:111],v1,s[36:39],s63 offen offset:2048 sc1
s_add_u32 s63,s87,8388608
buffer_load_dwordx4 v[112:115],v1,s[36:39],s63 offen offset:0 sc1
buffer_load_dwordx4 v[116:119],v1,s[36:39],s63 offen offset:2048 sc1
s_add_u32 s63,s87,8392704
buffer_load_dwordx4 v[120:123],v1,s[36:39],s63 offen offset:0 sc1
buffer_load_dwordx4 v[124:127],v1,s[36:39],s63 offen offset:2048 sc1
buffer_load_dword v8,v6,s[36:39],s69 offen offset:0 sc1
s_waitcnt vmcnt(13)
s_cmp_eq_u32 s95,0
s_cbranch_scc1 Lr_pb4_%=
buffer_store_dword v7,v5,s[36:39],s60 offen offset:0 sc0 sc1
s_mov_b32 s95,0
Lr_pb4_%=:
s_waitcnt vmcnt(0)
s_branch Lr_it1_%=
Lr_it1_%=:
s_mov_b32 s88,s58
s_mov_b32 s89,s59
s_mov_b32 s90,s49
s_mov_b32 s54,s70
s_lshr_b32 s63,s54,2
s_and_b32 s64,s54,1
s_lshl_b32 s64,s64,7
s_lshl_b32 s87,s63,16
s_add_u32 s87,s87,s64
s_and_b32 s64,s54,2
s_lshl_b32 s62,s64,14
s_add_u32 s87,s87,s62
s_add_u32 s58,s87,16908288
s_lshl_b32 s59,s54,8
s_and_b32 s64,s54,1
s_lshl_b32 s64,s64,7
s_sub_u32 s59,s59,s64
s_add_u32 s59,s59,107022336
s_lshl_b32 s49,s54,9
s_add_u32 s49,s49,s92
s_lshr_b32 s63,s54,1
s_lshl_b32 s91,s63,11
s_add_u32 s91,s91,109871104
s_add_u32 s70,s54,18
s_add_u32 s63,s54,s50
s_cmp_lt_u32 s54,0
s_cselect_b32 s70,s65,s70
s_cmp_lt_u32 s63,0
s_cselect_b32 s70,s63,s70
s_lshr_b32 s63,s70,1
s_lshl_b32 s69,s63,11
s_add_u32 s69,s69,109871104
v_cmp_eq_u32 vcc,s48,v8
s_nop 0
s_cmp_eq_u64 vcc,exec
s_cselect_b32 s63,s54,256
s_cmp_lt_u32 s63,256
s_cbranch_scc1 Lr_ta1_%=
buffer_load_dword v8,v6,s[36:39],s91 offen offset:0 sc1
v_dot2_f32_bf16 v128,v80,s51,0
v_dot2_f32_bf16 v129,v80,s52,0
v_dot2_f32_bf16 v130,v81,s51,0
v_dot2_f32_bf16 v131,v81,s52,0
v_dot2_f32_bf16 v132,v82,s51,0
v_dot2_f32_bf16 v133,v82,s52,0
v_dot2_f32_bf16 v134,v83,s51,0
v_dot2_f32_bf16 v135,v83,s52,0
v_dot2_f32_bf16 v128,v96,s51,v128
v_dot2_f32_bf16 v129,v96,s52,v129
v_dot2_f32_bf16 v130,v97,s51,v130
v_dot2_f32_bf16 v131,v97,s52,v131
v_dot2_f32_bf16 v132,v98,s51,v132
v_dot2_f32_bf16 v133,v98,s52,v133
v_dot2_f32_bf16 v134,v99,s51,v134)"
R"(
v_dot2_f32_bf16 v135,v99,s52,v135
v_dot2_f32_bf16 v136,v84,s51,0
v_dot2_f32_bf16 v137,v84,s52,0
v_dot2_f32_bf16 v138,v85,s51,0
v_dot2_f32_bf16 v139,v85,s52,0
v_dot2_f32_bf16 v140,v86,s51,0
v_dot2_f32_bf16 v141,v86,s52,0
v_dot2_f32_bf16 v142,v87,s51,0
v_dot2_f32_bf16 v143,v87,s52,0
v_dot2_f32_bf16 v136,v100,s51,v136
v_dot2_f32_bf16 v137,v100,s52,v137
v_dot2_f32_bf16 v138,v101,s51,v138
v_dot2_f32_bf16 v139,v101,s52,v139
v_dot2_f32_bf16 v140,v102,s51,v140
v_dot2_f32_bf16 v141,v102,s52,v141
v_dot2_f32_bf16 v142,v103,s51,v142
v_dot2_f32_bf16 v143,v103,s52,v143
v_cvt_pk_bf16_f32 v208,v128,v129
v_cvt_pk_bf16_f32 v209,v130,v131
v_cvt_pk_bf16_f32 v210,v132,v133
v_cvt_pk_bf16_f32 v211,v134,v135
s_nop 1
v_mfma_f32_16x16x32_bf16 v[180:183],v[176:179],v[208:211],0
v_mfma_f32_16x16x32_bf16 v[184:187],v[208:211],v[208:211],0
v_dot2_f32_bf16 v144,v88,s51,0
v_dot2_f32_bf16 v145,v88,s52,0
v_dot2_f32_bf16 v146,v89,s51,0
v_dot2_f32_bf16 v147,v89,s52,0
v_dot2_f32_bf16 v148,v90,s51,0
v_dot2_f32_bf16 v149,v90,s52,0
v_dot2_f32_bf16 v150,v91,s51,0
v_dot2_f32_bf16 v151,v91,s52,0
v_dot2_f32_bf16 v144,v104,s51,v144
v_dot2_f32_bf16 v145,v104,s52,v145
v_dot2_f32_bf16 v146,v105,s51,v146
v_dot2_f32_bf16 v147,v105,s52,v147
v_dot2_f32_bf16 v148,v106,s51,v148
v_dot2_f32_bf16 v149,v106,s52,v149
v_dot2_f32_bf16 v150,v107,s51,v150
v_dot2_f32_bf16 v151,v107,s52,v151
v_cvt_pk_bf16_f32 v212,v136,v137
v_cvt_pk_bf16_f32 v213,v138,v139
v_cvt_pk_bf16_f32 v214,v140,v141
v_cvt_pk_bf16_f32 v215,v142,v143
s_nop 1
v_mfma_f32_16x16x32_bf16 v[180:183],v[176:179],v[212:215],v[180:183]
v_mfma_f32_16x16x32_bf16 v[184:187],v[212:215],v[212:215],v[184:187]
v_dot2_f32_bf16 v152,v92,s51,0
v_dot2_f32_bf16 v153,v92,s52,0
v_dot2_f32_bf16 v154,v93,s51,0
v_dot2_f32_bf16 v155,v93,s52,0
v_dot2_f32_bf16 v156,v94,s51,0
v_dot2_f32_bf16 v157,v94,s52,0
v_dot2_f32_bf16 v158,v95,s51,0
v_dot2_f32_bf16 v159,v95,s52,0
v_dot2_f32_bf16 v152,v108,s51,v152
v_dot2_f32_bf16 v153,v108,s52,v153
v_dot2_f32_bf16 v154,v109,s51,v154
v_dot2_f32_bf16 v155,v109,s52,v155
v_dot2_f32_bf16 v156,v110,s51,v156
v_dot2_f32_bf16 v157,v110,s52,v157
v_dot2_f32_bf16 v158,v111,s51,v158
v_dot2_f32_bf16 v159,v111,s52,v159
v_cvt_pk_bf16_f32 v216,v144,v145
v_cvt_pk_bf16_f32 v217,v146,v147
v_cvt_pk_bf16_f32 v218,v148,v149
v_cvt_pk_bf16_f32 v219,v150,v151
s_nop 1
v_mfma_f32_16x16x32_bf16 v[180:183],v[176:179],v[216:219],v[180:183]
v_mfma_f32_16x16x32_bf16 v[184:187],v[216:219],v[216:219],v[184:187]
v_cvt_pk_bf16_f32 v220,v152,v153
v_cvt_pk_bf16_f32 v221,v154,v155
v_cvt_pk_bf16_f32 v222,v156,v157
v_cvt_pk_bf16_f32 v223,v158,v159
s_nop 1
v_mfma_f32_16x16x32_bf16 v[180:183],v[176:179],v[220:223],v[180:183]
v_mfma_f32_16x16x32_bf16 v[184:187],v[220:223],v[220:223],v[184:187]
s_nop 6
v_add_f32_dpp v224,v180,v180 row_ror:8 row_mask:0xf bank_mask:0xf
v_cndmask_b32 v225,v184,v185,s[46:47]
v_cndmask_b32 v226,v186,v187,s[46:47]
ds_write_b32 v2,v224 offset:0
v_cndmask_b32 v225,v225,v226,s[56:57]
ds_write_b32 v0,v225 offset:0
s_waitcnt lgkmcnt(0)
s_barrier
ds_read_b128 v[160:163],v3 offset:0
ds_read_b128 v[164:167],v3 offset:16
ds_read_b128 v[168:171],v3 offset:32
s_waitcnt lgkmcnt(0)
v_pk_add_f32 v[160:161],v[160:161],v[162:163]
v_pk_add_f32 v[164:165],v[164:165],v[166:167]
v_pk_add_f32 v[168:169],v[168:169],v[170:171]
v_add_f32 v160,v160,v161
v_pk_add_f32 v[164:165],v[164:165],v[168:169]
v_mul_f32 v160,s76,v160
v_add_f32 v161,v164,v165
v_mul_f32 v161,s76,v161
v_fma_f32 v161,-v160,v160,v161
v_max_f32 v161,0,v161
s_waitcnt vmcnt(0)
s_cmp_eq_u32 s95,0
s_cbranch_scc1 Lr_pb5_%=
buffer_store_dword v7,v5,s[36:39],s60 offen offset:0 sc0 sc1
s_mov_b32 s95,0
Lr_pb5_%=:
v_cmp_eq_u32 vcc,s48,v8
s_nop 0
s_cmp_eq_u64 vcc,exec
s_cselect_b32 s63,s54,256
s_cmp_lt_u32 s63,256
s_cbranch_scc1 Lr_tb1_%=
buffer_load_dword v8,v6,s[36:39],s91 offen offset:0 sc1
v_add_f32 v161,s77,v161
v_rsq_f32 v228,v161
s_nop 0
v_mul_f32_e64 v230,-v160,v228
v_mov_b32 v229,v228
v_mov_b32 v231,v230
v_lshlrev_b32 v160,16,v112
v_and_b32 v161,0xffff0000,v112
v_pk_fma_f32 v[162:163],v[128:129],v[228:229],v[230:231]
v_pk_fma_f32 v[128:129],v[162:163],v[160:161],v[128:129]
v_lshlrev_b32 v164,16,v113
v_and_b32 v165,0xffff0000,v113
v_pk_fma_f32 v[166:167],v[130:131],v[228:229],v[230:231]
v_pk_fma_f32 v[130:131],v[166:167],v[164:165],v[130:131]
v_lshlrev_b32 v168,16,v114
v_and_b32 v169,0xffff0000,v114
v_pk_fma_f32 v[170:171],v[132:133],v[228:229],v[230:231]
v_pk_fma_f32 v[132:133],v[170:171],v[168:169],v[132:133]
v_lshlrev_b32 v172,16,v115
v_and_b32 v173,0xffff0000,v115
v_pk_fma_f32 v[174:175],v[134:135],v[228:229],v[230:231]
v_pk_fma_f32 v[134:135],v[174:175],v[172:173],v[134:135]
v_lshlrev_b32 v160,16,v116
v_and_b32 v161,0xffff0000,v116
v_pk_fma_f32 v[162:163],v[136:137],v[228:229],v[230:231]
v_pk_fma_f32 v[136:137],v[162:163],v[160:161],v[136:137]
v_lshlrev_b32 v164,16,v117
v_and_b32 v165,0xffff0000,v117
v_pk_fma_f32 v[166:167],v[138:139],v[228:229],v[230:231]
v_pk_fma_f32 v[138:139],v[166:167],v[164:165],v[138:139]
v_lshlrev_b32 v168,16,v118
v_and_b32 v169,0xffff0000,v118
v_pk_fma_f32 v[170:171],v[140:141],v[228:229],v[230:231]
v_pk_fma_f32 v[140:141],v[170:171],v[168:169],v[140:141]
v_lshlrev_b32 v172,16,v119
v_and_b32 v173,0xffff0000,v119
v_pk_fma_f32 v[174:175],v[142:143],v[228:229],v[230:231]
v_pk_fma_f32 v[142:143],v[174:175],v[172:173],v[142:143]
v_lshlrev_b32 v160,16,v120
v_and_b32 v161,0xffff0000,v120
v_pk_fma_f32 v[162:163],v[144:145],v[228:229],v[230:231]
v_pk_fma_f32 v[144:145],v[162:163],v[160:161],v[144:145]
v_lshlrev_b32 v164,16,v121
v_and_b32 v165,0xffff0000,v121
v_pk_fma_f32 v[166:167],v[146:147],v[228:229],v[230:231]
v_pk_fma_f32 v[146:147],v[166:167],v[164:165],v[146:147]
v_lshlrev_b32 v168,16,v122
v_and_b32 v169,0xffff0000,v122
v_pk_fma_f32 v[170:171],v[148:149],v[228:229],v[230:231]
v_pk_fma_f32 v[148:149],v[170:171],v[168:169],v[148:149]
v_lshlrev_b32 v172,16,v123
v_and_b32 v173,0xffff0000,v123
v_pk_fma_f32 v[174:175],v[150:151],v[228:229],v[230:231]
v_pk_fma_f32 v[150:151],v[174:175],v[172:173],v[150:151]
v_lshlrev_b32 v160,16,v124
v_and_b32 v161,0xffff0000,v124
v_pk_fma_f32 v[162:163],v[152:153],v[228:229],v[230:231]
v_pk_fma_f32 v[152:153],v[162:163],v[160:161],v[152:153]
v_lshlrev_b32 v164,16,v125
v_and_b32 v165,0xffff0000,v125
v_pk_fma_f32 v[166:167],v[154:155],v[228:229],v[230:231]
v_pk_fma_f32 v[154:155],v[166:167],v[164:165],v[154:155]
v_lshlrev_b32 v168,16,v126
v_and_b32 v169,0xffff0000,v126
v_pk_fma_f32 v[170:171],v[156:157],v[228:229],v[230:231]
v_pk_fma_f32 v[156:157],v[170:171],v[168:169],v[156:157]
v_lshlrev_b32 v172,16,v127
v_and_b32 v173,0xffff0000,v127
v_pk_fma_f32 v[174:175],v[158:159],v[228:229],v[230:231]
v_pk_fma_f32 v[158:159],v[174:175],v[172:173],v[158:159]
v_cvt_pk_bf16_f32 v208,v128,v129
v_cvt_pk_bf16_f32 v209,v130,v131
v_cvt_pk_bf16_f32 v210,v132,v133
v_cvt_pk_bf16_f32 v211,v134,v135
s_nop 1
v_mfma_f32_16x16x32_bf16 v[180:183],v[176:179],v[208:211],0
v_mfma_f32_16x16x32_bf16 v[184:187],v[208:211],v[208:211],0
buffer_store_dwordx4 v[208:211],v1,s[36:39],s88 offen offset:0 sc0 sc1
v_cvt_pk_bf16_f32 v212,v136,v137
v_cvt_pk_bf16_f32 v213,v138,v139
v_cvt_pk_bf16_f32 v214,v140,v141
v_cvt_pk_bf16_f32 v215,v142,v143
s_nop 1
v_mfma_f32_16x16x32_bf16 v[180:183],v[176:179],v[212:215],v[180:183]
v_mfma_f32_16x16x32_bf16 v[184:187],v[212:215],v[212:215],v[184:187]
buffer_store_dwordx4 v[212:215],v1,s[36:39],s88 offen offset:2048 sc0 sc1
v_cvt_pk_bf16_f32 v216,v144,v145)"
R"(
v_cvt_pk_bf16_f32 v217,v146,v147
v_cvt_pk_bf16_f32 v218,v148,v149
v_cvt_pk_bf16_f32 v219,v150,v151
s_nop 1
v_mfma_f32_16x16x32_bf16 v[180:183],v[176:179],v[216:219],v[180:183]
v_mfma_f32_16x16x32_bf16 v[184:187],v[216:219],v[216:219],v[184:187]
s_add_u32 s88,s88,4096
buffer_store_dwordx4 v[216:219],v1,s[36:39],s88 offen offset:0 sc0 sc1
v_cvt_pk_bf16_f32 v220,v152,v153
v_cvt_pk_bf16_f32 v221,v154,v155
v_cvt_pk_bf16_f32 v222,v156,v157
v_cvt_pk_bf16_f32 v223,v158,v159
s_nop 1
v_mfma_f32_16x16x32_bf16 v[180:183],v[176:179],v[220:223],v[180:183]
v_mfma_f32_16x16x32_bf16 v[184:187],v[220:223],v[220:223],v[184:187]
buffer_store_dwordx4 v[220:223],v1,s[36:39],s88 offen offset:2048 sc0 sc1
s_nop 5
v_add_f32_dpp v224,v180,v180 row_ror:8 row_mask:0xf bank_mask:0xf
v_cndmask_b32 v225,v184,v185,s[46:47]
v_cndmask_b32 v226,v186,v187,s[46:47]
ds_write_b32 v2,v224 offset:1024
v_cndmask_b32 v225,v225,v226,s[56:57]
ds_write_b32 v0,v225 offset:1024
s_waitcnt lgkmcnt(0)
s_barrier
ds_read_b128 v[160:163],v3 offset:1024
ds_read_b128 v[164:167],v3 offset:1040
ds_read_b128 v[168:171],v3 offset:1056
s_waitcnt lgkmcnt(0)
v_pk_add_f32 v[160:161],v[160:161],v[162:163]
v_pk_add_f32 v[164:165],v[164:165],v[166:167]
v_pk_add_f32 v[168:169],v[168:169],v[170:171]
v_add_f32 v160,v160,v161
v_pk_add_f32 v[164:165],v[164:165],v[168:169]
v_mul_f32 v160,s76,v160
v_add_f32 v161,v164,v165
v_mul_f32 v161,s76,v161
v_fma_f32 v161,-v160,v160,v161
v_max_f32 v161,0,v161
v_add_f32 v162,s77,v161
v_fma_f32 v163,s77,v162,v161
v_rsq_f32 v173,v162
v_sqrt_f32 v171,v163
v_rsq_f32 v162,v163
v_mov_b32 v168,v160
v_mul_f32_e64 v172,-v160,v173
v_mul_f32 v169,s78,v162
v_mul_f32 v163,v162,v162
v_mul_f32 v170,s79,v163
v_mul_f32 v170,v170,v169
buffer_store_dwordx4 v[168:171],v4,s[36:39],s89 offen offset:0 sc0 sc1
s_and_b32 s63,s89,128
s_lshr_b32 s63,s63,1
s_sub_u32 s89,s89,s63
buffer_store_dwordx2 v[172:173],v10,s[36:39],s89 offen offset:256 sc0 sc1
s_mov_b32 s60,s90
s_mov_b32 s95,1
s_waitcnt vmcnt(6)
v_cmp_eq_u32 vcc,s48,v8
s_nop 0
s_cmp_eq_u64 vcc,exec
s_cselect_b32 s63,s54,256
s_cmp_lt_u32 s63,256
s_cbranch_scc1 Lr_tc1_%=
s_waitcnt vmcnt(0)
s_cmp_eq_u32 s95,0
s_cbranch_scc1 Lr_pb6_%=
buffer_store_dword v7,v5,s[36:39],s60 offen offset:0 sc0 sc1
s_mov_b32 s95,0
Lr_pb6_%=:
s_cmp_ge_u32 s54,256
s_cbranch_scc1 Lr_done_%=
Lr_bp1_%=:
buffer_load_dword v8,v6,s[36:39],s91 offen offset:0 sc1
s_waitcnt vmcnt(0)
v_cmp_eq_u32 vcc,s48,v8
s_nop 0
s_cmp_eq_u64 vcc,exec
s_cselect_b32 s63,1,s96
s_cmp_lg_u32 s63,0
s_cbranch_scc1 Lr_bq1_%=
s_add_u32 s74,s74,1
s_cmp_ge_u32 s74,20000
s_cselect_b32 s96,1,0
s_sleep 4
s_branch Lr_bp1_%=
Lr_bq1_%=:
s_add_u32 s63,s87,71303168
buffer_load_dwordx4 v[32:35],v1,s[36:39],s63 offen offset:0 sc1
buffer_load_dwordx4 v[36:39],v1,s[36:39],s63 offen offset:2048 sc1
s_add_u32 s63,s87,71307264
buffer_load_dwordx4 v[40:43],v1,s[36:39],s63 offen offset:0 sc1
buffer_load_dwordx4 v[44:47],v1,s[36:39],s63 offen offset:2048 sc1
s_add_u32 s63,s87,89128960
buffer_load_dwordx4 v[48:51],v1,s[36:39],s63 offen offset:0 sc1
buffer_load_dwordx4 v[52:55],v1,s[36:39],s63 offen offset:2048 sc1
s_add_u32 s63,s87,89133056
buffer_load_dwordx4 v[56:59],v1,s[36:39],s63 offen offset:0 sc1
buffer_load_dwordx4 v[60:63],v1,s[36:39],s63 offen offset:2048 sc1
s_add_u32 s63,s87,8388608
buffer_load_dwordx4 v[64:67],v1,s[36:39],s63 offen offset:0 sc1
buffer_load_dwordx4 v[68:71],v1,s[36:39],s63 offen offset:2048 sc1
s_add_u32 s63,s87,8392704
buffer_load_dwordx4 v[72:75],v1,s[36:39],s63 offen offset:0 sc1
buffer_load_dwordx4 v[76:79],v1,s[36:39],s63 offen offset:2048 sc1
buffer_load_dword v8,v6,s[36:39],s69 offen offset:0 sc1
s_waitcnt vmcnt(0)
s_branch Lr_it0_%=
Lr_ta1_%=:
v_dot2_f32_bf16 v128,v80,s51,0
v_dot2_f32_bf16 v129,v80,s52,0
v_dot2_f32_bf16 v130,v81,s51,0
v_dot2_f32_bf16 v131,v81,s52,0
v_dot2_f32_bf16 v132,v82,s51,0
v_dot2_f32_bf16 v133,v82,s52,0
s_add_u32 s63,s87,71303168
buffer_load_dwordx4 v[32:35],v1,s[36:39],s63 offen offset:0 sc1
v_dot2_f32_bf16 v134,v83,s51,0
v_dot2_f32_bf16 v135,v83,s52,0
v_dot2_f32_bf16 v128,v96,s51,v128
v_dot2_f32_bf16 v129,v96,s52,v129
v_dot2_f32_bf16 v130,v97,s51,v130
v_dot2_f32_bf16 v131,v97,s52,v131
buffer_load_dwordx4 v[36:39],v1,s[36:39],s63 offen offset:2048 sc1
v_dot2_f32_bf16 v132,v98,s51,v132
v_dot2_f32_bf16 v133,v98,s52,v133
v_dot2_f32_bf16 v134,v99,s51,v134
v_dot2_f32_bf16 v135,v99,s52,v135
v_dot2_f32_bf16 v136,v84,s51,0
v_dot2_f32_bf16 v137,v84,s52,0
s_add_u32 s63,s87,71307264
buffer_load_dwordx4 v[40:43],v1,s[36:39],s63 offen offset:0 sc1
v_dot2_f32_bf16 v138,v85,s51,0
v_dot2_f32_bf16 v139,v85,s52,0
v_dot2_f32_bf16 v140,v86,s51,0
v_dot2_f32_bf16 v141,v86,s52,0
v_dot2_f32_bf16 v142,v87,s51,0
v_dot2_f32_bf16 v143,v87,s52,0
buffer_load_dwordx4 v[44:47],v1,s[36:39],s63 offen offset:2048 sc1
v_dot2_f32_bf16 v136,v100,s51,v136
v_dot2_f32_bf16 v137,v100,s52,v137
v_dot2_f32_bf16 v138,v101,s51,v138
v_dot2_f32_bf16 v139,v101,s52,v139
v_dot2_f32_bf16 v140,v102,s51,v140
v_dot2_f32_bf16 v141,v102,s52,v141
s_add_u32 s63,s87,89128960
buffer_load_dwordx4 v[48:51],v1,s[36:39],s63 offen offset:0 sc1
v_dot2_f32_bf16 v142,v103,s51,v142
v_dot2_f32_bf16 v143,v103,s52,v143
v_cvt_pk_bf16_f32 v208,v128,v129
v_cvt_pk_bf16_f32 v209,v130,v131
v_cvt_pk_bf16_f32 v210,v132,v133
v_cvt_pk_bf16_f32 v211,v134,v135
buffer_load_dwordx4 v[52:55],v1,s[36:39],s63 offen offset:2048 sc1
s_nop 0
v_mfma_f32_16x16x32_bf16 v[180:183],v[176:179],v[208:211],0
v_mfma_f32_16x16x32_bf16 v[184:187],v[208:211],v[208:211],0
v_dot2_f32_bf16 v144,v88,s51,0
v_dot2_f32_bf16 v145,v88,s52,0
v_dot2_f32_bf16 v146,v89,s51,0
v_dot2_f32_bf16 v147,v89,s52,0
s_add_u32 s63,s87,89133056
buffer_load_dwordx4 v[56:59],v1,s[36:39],s63 offen offset:0 sc1
v_dot2_f32_bf16 v148,v90,s51,0
v_dot2_f32_bf16 v149,v90,s52,0
v_dot2_f32_bf16 v150,v91,s51,0
v_dot2_f32_bf16 v151,v91,s52,0
v_dot2_f32_bf16 v144,v104,s51,v144
v_dot2_f32_bf16 v145,v104,s52,v145
buffer_load_dwordx4 v[60:63],v1,s[36:39],s63 offen offset:2048 sc1
v_dot2_f32_bf16 v146,v105,s51,v146
v_dot2_f32_bf16 v147,v105,s52,v147
v_dot2_f32_bf16 v148,v106,s51,v148
v_dot2_f32_bf16 v149,v106,s52,v149
v_dot2_f32_bf16 v150,v107,s51,v150
v_dot2_f32_bf16 v151,v107,s52,v151
s_add_u32 s63,s87,8388608
buffer_load_dwordx4 v[64:67],v1,s[36:39],s63 offen offset:0 sc1
v_cvt_pk_bf16_f32 v212,v136,v137
v_cvt_pk_bf16_f32 v213,v138,v139
v_cvt_pk_bf16_f32 v214,v140,v141
v_cvt_pk_bf16_f32 v215,v142,v143
s_nop 1
v_mfma_f32_16x16x32_bf16 v[180:183],v[176:179],v[212:215],v[180:183]
v_mfma_f32_16x16x32_bf16 v[184:187],v[212:215],v[212:215],v[184:187]
buffer_load_dwordx4 v[68:71],v1,s[36:39],s63 offen offset:2048 sc1
v_dot2_f32_bf16 v152,v92,s51,0
v_dot2_f32_bf16 v153,v92,s52,0
v_dot2_f32_bf16 v154,v93,s51,0
v_dot2_f32_bf16 v155,v93,s52,0
v_dot2_f32_bf16 v156,v94,s51,0
v_dot2_f32_bf16 v157,v94,s52,0
s_add_u32 s63,s87,8392704
buffer_load_dwordx4 v[72:75],v1,s[36:39],s63 offen offset:0 sc1
v_dot2_f32_bf16 v158,v95,s51,0
v_dot2_f32_bf16 v159,v95,s52,0
v_dot2_f32_bf16 v152,v108,s51,v152
v_dot2_f32_bf16 v153,v108,s52,v153
v_dot2_f32_bf16 v154,v109,s51,v154
v_dot2_f32_bf16 v155,v109,s52,v155)"
R"(
buffer_load_dwordx4 v[76:79],v1,s[36:39],s63 offen offset:2048 sc1
v_dot2_f32_bf16 v156,v110,s51,v156
v_dot2_f32_bf16 v157,v110,s52,v157
v_dot2_f32_bf16 v158,v111,s51,v158
v_dot2_f32_bf16 v159,v111,s52,v159
v_cvt_pk_bf16_f32 v216,v144,v145
v_cvt_pk_bf16_f32 v217,v146,v147
v_cvt_pk_bf16_f32 v218,v148,v149
v_cvt_pk_bf16_f32 v219,v150,v151
s_nop 1
v_mfma_f32_16x16x32_bf16 v[180:183],v[176:179],v[216:219],v[180:183]
v_mfma_f32_16x16x32_bf16 v[184:187],v[216:219],v[216:219],v[184:187]
v_cvt_pk_bf16_f32 v220,v152,v153
v_cvt_pk_bf16_f32 v221,v154,v155
v_cvt_pk_bf16_f32 v222,v156,v157
v_cvt_pk_bf16_f32 v223,v158,v159
s_nop 1
v_mfma_f32_16x16x32_bf16 v[180:183],v[176:179],v[220:223],v[180:183]
v_mfma_f32_16x16x32_bf16 v[184:187],v[220:223],v[220:223],v[184:187]
s_nop 6
v_add_f32_dpp v224,v180,v180 row_ror:8 row_mask:0xf bank_mask:0xf
v_cndmask_b32 v225,v184,v185,s[46:47]
v_cndmask_b32 v226,v186,v187,s[46:47]
ds_write_b32 v2,v224 offset:0
v_cndmask_b32 v225,v225,v226,s[56:57]
ds_write_b32 v0,v225 offset:0
s_waitcnt lgkmcnt(0)
s_barrier
ds_read_b128 v[160:163],v3 offset:0
ds_read_b128 v[164:167],v3 offset:16
ds_read_b128 v[168:171],v3 offset:32
s_waitcnt lgkmcnt(0)
v_pk_add_f32 v[160:161],v[160:161],v[162:163]
v_pk_add_f32 v[164:165],v[164:165],v[166:167]
v_pk_add_f32 v[168:169],v[168:169],v[170:171]
v_add_f32 v160,v160,v161
v_pk_add_f32 v[164:165],v[164:165],v[168:169]
v_mul_f32 v160,s76,v160
v_add_f32 v161,v164,v165
v_mul_f32 v161,s76,v161
v_fma_f32 v161,-v160,v160,v161
v_max_f32 v161,0,v161
s_waitcnt vmcnt(12)
s_cmp_eq_u32 s95,0
s_cbranch_scc1 Lr_pb7_%=
buffer_store_dword v7,v5,s[36:39],s60 offen offset:0 sc0 sc1
s_mov_b32 s95,0
Lr_pb7_%=:
v_add_f32 v161,s77,v161
v_rsq_f32 v228,v161
s_nop 0
v_mul_f32_e64 v230,-v160,v228
v_mov_b32 v229,v228
v_mov_b32 v231,v230
v_lshlrev_b32 v160,16,v112
v_and_b32 v161,0xffff0000,v112
v_pk_fma_f32 v[162:163],v[128:129],v[228:229],v[230:231]
v_pk_fma_f32 v[128:129],v[162:163],v[160:161],v[128:129]
v_lshlrev_b32 v164,16,v113
v_and_b32 v165,0xffff0000,v113
v_pk_fma_f32 v[166:167],v[130:131],v[228:229],v[230:231]
v_pk_fma_f32 v[130:131],v[166:167],v[164:165],v[130:131]
v_lshlrev_b32 v168,16,v114
v_and_b32 v169,0xffff0000,v114
v_pk_fma_f32 v[170:171],v[132:133],v[228:229],v[230:231]
v_pk_fma_f32 v[132:133],v[170:171],v[168:169],v[132:133]
v_lshlrev_b32 v172,16,v115
v_and_b32 v173,0xffff0000,v115
v_pk_fma_f32 v[174:175],v[134:135],v[228:229],v[230:231]
v_pk_fma_f32 v[134:135],v[174:175],v[172:173],v[134:135]
v_lshlrev_b32 v160,16,v116
v_and_b32 v161,0xffff0000,v116
v_pk_fma_f32 v[162:163],v[136:137],v[228:229],v[230:231]
v_pk_fma_f32 v[136:137],v[162:163],v[160:161],v[136:137]
v_lshlrev_b32 v164,16,v117
v_and_b32 v165,0xffff0000,v117
v_pk_fma_f32 v[166:167],v[138:139],v[228:229],v[230:231]
v_pk_fma_f32 v[138:139],v[166:167],v[164:165],v[138:139]
v_lshlrev_b32 v168,16,v118
v_and_b32 v169,0xffff0000,v118
v_pk_fma_f32 v[170:171],v[140:141],v[228:229],v[230:231]
v_pk_fma_f32 v[140:141],v[170:171],v[168:169],v[140:141]
v_lshlrev_b32 v172,16,v119
v_and_b32 v173,0xffff0000,v119
v_pk_fma_f32 v[174:175],v[142:143],v[228:229],v[230:231]
v_pk_fma_f32 v[142:143],v[174:175],v[172:173],v[142:143]
v_lshlrev_b32 v160,16,v120
v_and_b32 v161,0xffff0000,v120
v_pk_fma_f32 v[162:163],v[144:145],v[228:229],v[230:231]
v_pk_fma_f32 v[144:145],v[162:163],v[160:161],v[144:145]
v_lshlrev_b32 v164,16,v121
v_and_b32 v165,0xffff0000,v121
v_pk_fma_f32 v[166:167],v[146:147],v[228:229],v[230:231]
v_pk_fma_f32 v[146:147],v[166:167],v[164:165],v[146:147]
v_lshlrev_b32 v168,16,v122
v_and_b32 v169,0xffff0000,v122
v_pk_fma_f32 v[170:171],v[148:149],v[228:229],v[230:231]
v_pk_fma_f32 v[148:149],v[170:171],v[168:169],v[148:149]
v_lshlrev_b32 v172,16,v123
v_and_b32 v173,0xffff0000,v123
v_pk_fma_f32 v[174:175],v[150:151],v[228:229],v[230:231]
v_pk_fma_f32 v[150:151],v[174:175],v[172:173],v[150:151]
v_lshlrev_b32 v160,16,v124
v_and_b32 v161,0xffff0000,v124
v_pk_fma_f32 v[162:163],v[152:153],v[228:229],v[230:231]
v_pk_fma_f32 v[152:153],v[162:163],v[160:161],v[152:153]
v_lshlrev_b32 v164,16,v125
v_and_b32 v165,0xffff0000,v125
v_pk_fma_f32 v[166:167],v[154:155],v[228:229],v[230:231]
v_pk_fma_f32 v[154:155],v[166:167],v[164:165],v[154:155]
v_lshlrev_b32 v168,16,v126
v_and_b32 v169,0xffff0000,v126
v_pk_fma_f32 v[170:171],v[156:157],v[228:229],v[230:231]
v_pk_fma_f32 v[156:157],v[170:171],v[168:169],v[156:157]
v_lshlrev_b32 v172,16,v127
v_and_b32 v173,0xffff0000,v127
v_pk_fma_f32 v[174:175],v[158:159],v[228:229],v[230:231]
v_pk_fma_f32 v[158:159],v[174:175],v[172:173],v[158:159]
buffer_load_dword v8,v6,s[36:39],s69 offen offset:0 sc1
v_cvt_pk_bf16_f32 v208,v128,v129
v_cvt_pk_bf16_f32 v209,v130,v131
v_cvt_pk_bf16_f32 v210,v132,v133
v_cvt_pk_bf16_f32 v211,v134,v135
s_nop 1
v_mfma_f32_16x16x32_bf16 v[180:183],v[176:179],v[208:211],0
v_mfma_f32_16x16x32_bf16 v[184:187],v[208:211],v[208:211],0
buffer_store_dwordx4 v[208:211],v1,s[36:39],s88 offen offset:0 sc0 sc1
v_cvt_pk_bf16_f32 v212,v136,v137
v_cvt_pk_bf16_f32 v213,v138,v139
v_cvt_pk_bf16_f32 v214,v140,v141
v_cvt_pk_bf16_f32 v215,v142,v143
s_nop 1
v_mfma_f32_16x16x32_bf16 v[180:183],v[176:179],v[212:215],v[180:183]
v_mfma_f32_16x16x32_bf16 v[184:187],v[212:215],v[212:215],v[184:187]
buffer_store_dwordx4 v[212:215],v1,s[36:39],s88 offen offset:2048 sc0 sc1
v_cvt_pk_bf16_f32 v216,v144,v145
v_cvt_pk_bf16_f32 v217,v146,v147
v_cvt_pk_bf16_f32 v218,v148,v149
v_cvt_pk_bf16_f32 v219,v150,v151
s_nop 1
v_mfma_f32_16x16x32_bf16 v[180:183],v[176:179],v[216:219],v[180:183]
v_mfma_f32_16x16x32_bf16 v[184:187],v[216:219],v[216:219],v[184:187]
s_add_u32 s88,s88,4096
buffer_store_dwordx4 v[216:219],v1,s[36:39],s88 offen offset:0 sc0 sc1
v_cvt_pk_bf16_f32 v220,v152,v153
v_cvt_pk_bf16_f32 v221,v154,v155
v_cvt_pk_bf16_f32 v222,v156,v157
v_cvt_pk_bf16_f32 v223,v158,v159
s_nop 1
v_mfma_f32_16x16x32_bf16 v[180:183],v[176:179],v[220:223],v[180:183]
v_mfma_f32_16x16x32_bf16 v[184:187],v[220:223],v[220:223],v[184:187]
buffer_store_dwordx4 v[220:223],v1,s[36:39],s88 offen offset:2048 sc0 sc1
s_nop 5
v_add_f32_dpp v224,v180,v180 row_ror:8 row_mask:0xf bank_mask:0xf
v_cndmask_b32 v225,v184,v185,s[46:47]
v_cndmask_b32 v226,v186,v187,s[46:47]
ds_write_b32 v2,v224 offset:1024
v_cndmask_b32 v225,v225,v226,s[56:57]
ds_write_b32 v0,v225 offset:1024
s_waitcnt lgkmcnt(0)
s_barrier
ds_read_b128 v[160:163],v3 offset:1024
ds_read_b128 v[164:167],v3 offset:1040
ds_read_b128 v[168:171],v3 offset:1056
s_waitcnt lgkmcnt(0)
v_pk_add_f32 v[160:161],v[160:161],v[162:163]
v_pk_add_f32 v[164:165],v[164:165],v[166:167]
v_pk_add_f32 v[168:169],v[168:169],v[170:171]
v_add_f32 v160,v160,v161
v_pk_add_f32 v[164:165],v[164:165],v[168:169]
v_mul_f32 v160,s76,v160
v_add_f32 v161,v164,v165
v_mul_f32 v161,s76,v161
v_fma_f32 v161,-v160,v160,v161
v_max_f32 v161,0,v161
v_add_f32 v162,s77,v161
v_fma_f32 v163,s77,v162,v161
v_rsq_f32 v173,v162
v_sqrt_f32 v171,v163
v_rsq_f32 v162,v163
v_mov_b32 v168,v160
v_mul_f32_e64 v172,-v160,v173
v_mul_f32 v169,s78,v162
v_mul_f32 v163,v162,v162
v_mul_f32 v170,s79,v163
v_mul_f32 v170,v170,v169
buffer_store_dwordx4 v[168:171],v4,s[36:39],s89 offen offset:0 sc0 sc1
s_and_b32 s63,s89,128
s_lshr_b32 s63,s63,1
s_sub_u32 s89,s89,s63
buffer_store_dwordx2 v[172:173],v10,s[36:39],s89 offen offset:256 sc0 sc1
s_mov_b32 s60,s90
s_mov_b32 s95,1
s_waitcnt vmcnt(6)
s_branch Lr_it0_%=
Lr_tb1_%=:
v_add_f32 v161,s77,v161
v_rsq_f32 v228,v161
s_nop 0
v_mul_f32_e64 v230,-v160,v228)"
R"(
v_mov_b32 v229,v228
v_mov_b32 v231,v230
s_add_u32 s63,s87,71303168
buffer_load_dwordx4 v[32:35],v1,s[36:39],s63 offen offset:0 sc1
v_lshlrev_b32 v160,16,v112
v_and_b32 v161,0xffff0000,v112
v_pk_fma_f32 v[162:163],v[128:129],v[228:229],v[230:231]
v_pk_fma_f32 v[128:129],v[162:163],v[160:161],v[128:129]
v_lshlrev_b32 v164,16,v113
buffer_load_dwordx4 v[36:39],v1,s[36:39],s63 offen offset:2048 sc1
v_and_b32 v165,0xffff0000,v113
v_pk_fma_f32 v[166:167],v[130:131],v[228:229],v[230:231]
v_pk_fma_f32 v[130:131],v[166:167],v[164:165],v[130:131]
v_lshlrev_b32 v168,16,v114
v_and_b32 v169,0xffff0000,v114
s_add_u32 s63,s87,71307264
buffer_load_dwordx4 v[40:43],v1,s[36:39],s63 offen offset:0 sc1
v_pk_fma_f32 v[170:171],v[132:133],v[228:229],v[230:231]
v_pk_fma_f32 v[132:133],v[170:171],v[168:169],v[132:133]
v_lshlrev_b32 v172,16,v115
v_and_b32 v173,0xffff0000,v115
v_pk_fma_f32 v[174:175],v[134:135],v[228:229],v[230:231]
buffer_load_dwordx4 v[44:47],v1,s[36:39],s63 offen offset:2048 sc1
v_pk_fma_f32 v[134:135],v[174:175],v[172:173],v[134:135]
v_lshlrev_b32 v160,16,v116
v_and_b32 v161,0xffff0000,v116
v_pk_fma_f32 v[162:163],v[136:137],v[228:229],v[230:231]
v_pk_fma_f32 v[136:137],v[162:163],v[160:161],v[136:137]
s_add_u32 s63,s87,89128960
buffer_load_dwordx4 v[48:51],v1,s[36:39],s63 offen offset:0 sc1
v_lshlrev_b32 v164,16,v117
v_and_b32 v165,0xffff0000,v117
v_pk_fma_f32 v[166:167],v[138:139],v[228:229],v[230:231]
v_pk_fma_f32 v[138:139],v[166:167],v[164:165],v[138:139]
v_lshlrev_b32 v168,16,v118
buffer_load_dwordx4 v[52:55],v1,s[36:39],s63 offen offset:2048 sc1
v_and_b32 v169,0xffff0000,v118
v_pk_fma_f32 v[170:171],v[140:141],v[228:229],v[230:231]
v_pk_fma_f32 v[140:141],v[170:171],v[168:169],v[140:141]
v_lshlrev_b32 v172,16,v119
v_and_b32 v173,0xffff0000,v119
s_add_u32 s63,s87,89133056
buffer_load_dwordx4 v[56:59],v1,s[36:39],s63 offen offset:0 sc1
v_pk_fma_f32 v[174:175],v[142:143],v[228:229],v[230:231]
v_pk_fma_f32 v[142:143],v[174:175],v[172:173],v[142:143]
v_lshlrev_b32 v160,16,v120
v_and_b32 v161,0xffff0000,v120
v_pk_fma_f32 v[162:163],v[144:145],v[228:229],v[230:231]
buffer_load_dwordx4 v[60:63],v1,s[36:39],s63 offen offset:2048 sc1
v_pk_fma_f32 v[144:145],v[162:163],v[160:161],v[144:145]
v_lshlrev_b32 v164,16,v121
v_and_b32 v165,0xffff0000,v121
v_pk_fma_f32 v[166:167],v[146:147],v[228:229],v[230:231]
v_pk_fma_f32 v[146:147],v[166:167],v[164:165],v[146:147]
s_add_u32 s63,s87,8388608
buffer_load_dwordx4 v[64:67],v1,s[36:39],s63 offen offset:0 sc1
v_lshlrev_b32 v168,16,v122
v_and_b32 v169,0xffff0000,v122
v_pk_fma_f32 v[170:171],v[148:149],v[228:229],v[230:231]
v_pk_fma_f32 v[148:149],v[170:171],v[168:169],v[148:149]
v_lshlrev_b32 v172,16,v123
buffer_load_dwordx4 v[68:71],v1,s[36:39],s63 offen offset:2048 sc1
v_and_b32 v173,0xffff0000,v123
v_pk_fma_f32 v[174:175],v[150:151],v[228:229],v[230:231]
v_pk_fma_f32 v[150:151],v[174:175],v[172:173],v[150:151]
v_lshlrev_b32 v160,16,v124
v_and_b32 v161,0xffff0000,v124
s_add_u32 s63,s87,8392704
buffer_load_dwordx4 v[72:75],v1,s[36:39],s63 offen offset:0 sc1
v_pk_fma_f32 v[162:163],v[152:153],v[228:229],v[230:231]
v_pk_fma_f32 v[152:153],v[162:163],v[160:161],v[152:153]
v_lshlrev_b32 v164,16,v125
v_and_b32 v165,0xffff0000,v125
v_pk_fma_f32 v[166:167],v[154:155],v[228:229],v[230:231]
buffer_load_dwordx4 v[76:79],v1,s[36:39],s63 offen offset:2048 sc1
v_pk_fma_f32 v[154:155],v[166:167],v[164:165],v[154:155]
v_lshlrev_b32 v168,16,v126
v_and_b32 v169,0xffff0000,v126
v_pk_fma_f32 v[170:171],v[156:157],v[228:229],v[230:231]
v_pk_fma_f32 v[156:157],v[170:171],v[168:169],v[156:157]
v_lshlrev_b32 v172,16,v127
v_and_b32 v173,0xffff0000,v127
v_pk_fma_f32 v[174:175],v[158:159],v[228:229],v[230:231]
v_pk_fma_f32 v[158:159],v[174:175],v[172:173],v[158:159]
buffer_load_dword v8,v6,s[36:39],s69 offen offset:0 sc1
v_cvt_pk_bf16_f32 v208,v128,v129
v_cvt_pk_bf16_f32 v209,v130,v131
v_cvt_pk_bf16_f32 v210,v132,v133
v_cvt_pk_bf16_f32 v211,v134,v135
s_nop 1
v_mfma_f32_16x16x32_bf16 v[180:183],v[176:179],v[208:211],0
v_mfma_f32_16x16x32_bf16 v[184:187],v[208:211],v[208:211],0
buffer_store_dwordx4 v[208:211],v1,s[36:39],s88 offen offset:0 sc0 sc1
v_cvt_pk_bf16_f32 v212,v136,v137
v_cvt_pk_bf16_f32 v213,v138,v139
v_cvt_pk_bf16_f32 v214,v140,v141
v_cvt_pk_bf16_f32 v215,v142,v143
s_nop 1
v_mfma_f32_16x16x32_bf16 v[180:183],v[176:179],v[212:215],v[180:183]
v_mfma_f32_16x16x32_bf16 v[184:187],v[212:215],v[212:215],v[184:187]
buffer_store_dwordx4 v[212:215],v1,s[36:39],s88 offen offset:2048 sc0 sc1
v_cvt_pk_bf16_f32 v216,v144,v145
v_cvt_pk_bf16_f32 v217,v146,v147
v_cvt_pk_bf16_f32 v218,v148,v149
v_cvt_pk_bf16_f32 v219,v150,v151
s_nop 1
v_mfma_f32_16x16x32_bf16 v[180:183],v[176:179],v[216:219],v[180:183]
v_mfma_f32_16x16x32_bf16 v[184:187],v[216:219],v[216:219],v[184:187]
s_add_u32 s88,s88,4096
buffer_store_dwordx4 v[216:219],v1,s[36:39],s88 offen offset:0 sc0 sc1
v_cvt_pk_bf16_f32 v220,v152,v153
v_cvt_pk_bf16_f32 v221,v154,v155
v_cvt_pk_bf16_f32 v222,v156,v157
v_cvt_pk_bf16_f32 v223,v158,v159
s_nop 1
v_mfma_f32_16x16x32_bf16 v[180:183],v[176:179],v[220:223],v[180:183]
v_mfma_f32_16x16x32_bf16 v[184:187],v[220:223],v[220:223],v[184:187]
buffer_store_dwordx4 v[220:223],v1,s[36:39],s88 offen offset:2048 sc0 sc1
s_nop 5
v_add_f32_dpp v224,v180,v180 row_ror:8 row_mask:0xf bank_mask:0xf
v_cndmask_b32 v225,v184,v185,s[46:47]
v_cndmask_b32 v226,v186,v187,s[46:47]
ds_write_b32 v2,v224 offset:1024
v_cndmask_b32 v225,v225,v226,s[56:57]
ds_write_b32 v0,v225 offset:1024
s_waitcnt lgkmcnt(0)
s_barrier
ds_read_b128 v[160:163],v3 offset:1024
ds_read_b128 v[164:167],v3 offset:1040
ds_read_b128 v[168:171],v3 offset:1056
s_waitcnt lgkmcnt(0)
v_pk_add_f32 v[160:161],v[160:161],v[162:163]
v_pk_add_f32 v[164:165],v[164:165],v[166:167]
v_pk_add_f32 v[168:169],v[168:169],v[170:171]
v_add_f32 v160,v160,v161
v_pk_add_f32 v[164:165],v[164:165],v[168:169]
v_mul_f32 v160,s76,v160
v_add_f32 v161,v164,v165
v_mul_f32 v161,s76,v161
v_fma_f32 v161,-v160,v160,v161
v_max_f32 v161,0,v161
v_add_f32 v162,s77,v161
v_fma_f32 v163,s77,v162,v161
v_rsq_f32 v173,v162
v_sqrt_f32 v171,v163
v_rsq_f32 v162,v163
v_mov_b32 v168,v160
v_mul_f32_e64 v172,-v160,v173
v_mul_f32 v169,s78,v162
v_mul_f32 v163,v162,v162
v_mul_f32 v170,s79,v163
v_mul_f32 v170,v170,v169
buffer_store_dwordx4 v[168:171],v4,s[36:39],s89 offen offset:0 sc0 sc1
s_and_b32 s63,s89,128
s_lshr_b32 s63,s63,1
s_sub_u32 s89,s89,s63
buffer_store_dwordx2 v[172:173],v10,s[36:39],s89 offen offset:256 sc0 sc1
s_mov_b32 s60,s90
s_mov_b32 s95,1
s_waitcnt vmcnt(6)
s_branch Lr_it0_%=
Lr_tc1_%=:
s_add_u32 s63,s87,71303168
buffer_load_dwordx4 v[32:35],v1,s[36:39],s63 offen offset:0 sc1
buffer_load_dwordx4 v[36:39],v1,s[36:39],s63 offen offset:2048 sc1
s_add_u32 s63,s87,71307264
buffer_load_dwordx4 v[40:43],v1,s[36:39],s63 offen offset:0 sc1
buffer_load_dwordx4 v[44:47],v1,s[36:39],s63 offen offset:2048 sc1
s_add_u32 s63,s87,89128960
buffer_load_dwordx4 v[48:51],v1,s[36:39],s63 offen offset:0 sc1
buffer_load_dwordx4 v[52:55],v1,s[36:39],s63 offen offset:2048 sc1
s_add_u32 s63,s87,89133056
buffer_load_dwordx4 v[56:59],v1,s[36:39],s63 offen offset:0 sc1
buffer_load_dwordx4 v[60:63],v1,s[36:39],s63 offen offset:2048 sc1
s_add_u32 s63,s87,8388608
buffer_load_dwordx4 v[64:67],v1,s[36:39],s63 offen offset:0 sc1
buffer_load_dwordx4 v[68:71],v1,s[36:39],s63 offen offset:2048 sc1
s_add_u32 s63,s87,8392704
buffer_load_dwordx4 v[72:75],v1,s[36:39],s63 offen offset:0 sc1
buffer_load_dwordx4 v[76:79],v1,s[36:39],s63 offen offset:2048 sc1
buffer_load_dword v8,v6,s[36:39],s69 offen offset:0 sc1
s_waitcnt vmcnt(13)
s_cmp_eq_u32 s95,0
s_cbranch_scc1 Lr_pb8_%=
buffer_store_dword v7,v5,s[36:39],s60 offen offset:0 sc0 sc1
s_mov_b32 s95,0
Lr_pb8_%=:
s_waitcnt vmcnt(0)
s_branch Lr_it0_%=
Lr_done_%=:
s_waitcnt vmcnt(0) lgkmcnt(0)
s_barrier
Lcode_r_e:)"
  :: "s"(ws), "s"(sK), "s"(sStep), "s"(sRq), "s"(sK1), "s"(sEpR), "v"(vRP), "v"(vRF), "v"(vRwr), "v"(vRrd), "v"(vRS), "v"(vFlag), "v"(vL4) : "v0", "v1", "v2", "v3", "v4", "v5", "v6", "v7", "v8", "v9", "v10", "v11", "v12", "v13", "v14", "v15", "v16", "v17", "v18", "v19", "v20", "v21", "v22", "v23", "v24", "v25", "v26", "v27", "v28", "v29", "v30", "v31", "v32", "v33", "v34", "v35", "v36", "v37", "v38", "v39", "v40", "v41", "v42", "v43", "v44", "v45", "v46", "v47", "v48", "v49", "v50", "v51", "v52", "v53", "v54", "v55", "v56", "v57", "v58", "v59", "v60", "v61", "v62", "v63", "v64", "v65", "v66", "v67", "v68", "v69", "v70", "v71", "v72", "v73", "v74", "v75", "v76", "v77", "v78", "v79", "v80", "v81", "v82", "v83", "v84", "v85", "v86", "v87", "v88", "v89", "v90", "v91", "v92", "v93", "v94", "v95", "v96", "v97", "v98", "v99", "v100", "v101", "v102", "v103", "v104", "v105", "v106", "v107", "v108", "v109", "v110", "v111", "v112", "v113", "v114", "v115", "v116", "v117", "v118", "v119", "v120", "v121", "v122", "v123", "v124", "v125", "v126", "v127", "v128", "v129", "v130", "v131", "v132", "v133", "v134", "v135", "v136", "v137", "v138", "v139", "v140", "v141", "v142", "v143", "v144", "v145", "v146", "v147", "v148", "v149", "v150", "v151", "v152", "v153", "v154", "v155", "v156", "v157", "v158", "v159", "v160", "v161", "v162", "v163", "v164", "v165", "v166", "v167", "v168", "v169", "v170", "v171", "v172", "v173", "v174", "v175", "v176", "v177", "v178", "v179", "v180", "v181", "v182", "v183", "v184", "v185", "v186", "v187", "v188", "v189", "v190", "v191", "v192", "v193", "v194", "v195", "v196", "v197", "v198", "v199", "v200", "v201", "v202", "v203", "v204", "v205", "v206", "v207", "v208", "v209", "v210", "v211", "v212", "v213", "v214", "v215", "v216", "v217", "v218", "v219", "v220", "v221", "v222", "v223", "v224", "v225", "v226", "v227", "v228", "v229", "v230", "v231", "v255", "s36", "s37", "s38", "s39", "s40", "s41", "s42", "s43", "s44", "s45", "s46", "s47", "s48", "s49", "s50", "s51", "s52", "s53", "s54", "s55", "s56", "s57", "s58", "s59", "s60", "s61", "s62", "s63", "s64", "s65", "s66", "s67", "s68", "s69", "s70", "s71", "s72", "s73", "s74", "s75", "s76", "s77", "s78", "s79", "s80", "s81", "s82", "s83", "s84", "s85", "s86", "s87", "s88", "s89", "s90", "s91", "s92", "s93", "s94", "s95", "s96", "s97", "s98", "s99", "vcc", "scc", "memory");
    }
    LANE_ID(lane2);
    for (int t = kind == 6 ? 82 - NRW + slice : (kind - 1) * 32 + rep * 8 + slice; t < 256; t += 82)
      row_last(ws, out, t, lane2, wave, ep);
    if (b == 0 && lane2 == 0 && wave == 0) __hip_atomic_store((gu32*)(ws + OFF_EP), ep, __ATOMIC_RELAXED, __HIP_MEMORY_SCOPE_AGENT);
    asm volatile("s_endpgm" ::: "memory");
    __builtin_unreachable();
  }
}

extern "C" void kernel_launch(void* const* d_in, const int* in_sizes, int n_in, void* d_out, int out_size,
                              void* d_ws, size_t ws_size, hipStream_t stream) {
  const u16* q = (const u16*)d_in[0];
  const u16* k = (const u16*)d_in[1];
  const u16* v = (const u16*)d_in[2];
  const u16* shift_k = (const u16*)d_in[3];
  const u16* ssm_k = (const u16*)d_in[4];
  const u16* ssm_D = (const u16*)d_in[5];
  const u16* w1 = (const u16*)d_in[7];
  const u16* w2 = (const u16*)d_in[8];
  const u16* w3 = (const u16*)d_in[9];
  const u16* w4 = (const u16*)d_in[10];
  flow_kernel<<<256, 256, 0, stream>>>(q, k, v, shift_k, ssm_k, ssm_D, w1, w2, w3, w4, (char*)d_ws, (u16*)d_out);
}
```

```cpp
#include <hip/hip_runtime.h>
#include <hip/hip_bf16.h>
#include <stdint.h>
#include <type_traits>

typedef short s16x8 __attribute__((ext_vector_type(8)));
typedef short s16x4 __attribute__((ext_vector_type(4)));
typedef float f32x4 __attribute__((ext_vector_type(4)));
typedef unsigned short u16;
typedef unsigned short u16x8 __attribute__((ext_vector_type(8)));
typedef unsigned short u16x4 __attribute__((ext_vector_type(4)));
typedef float f32x2 __attribute__((ext_vector_type(2)));
typedef __bf16 bf16x2 __attribute__((ext_vector_type(2)));
typedef unsigned u32x4v __attribute__((ext_vector_type(4)));
typedef __attribute__((address_space(1))) unsigned gu32;

constexpr int S_ = 2048, D_ = 1024, F_ = 3584;
#define PF 1
#define MAP12 1

constexpr int KE = 1, RT1 = 0, LB = 66, FL_BYTES = 2141312, WS_USED = 111193216;
constexpr int OFF_KVR = 4194304, OFF_YF = 8388608, OFF_XP = 12582912, OFF_HS = 22085632, OFF_X1 = 31457280, OFF_G = 40894464, OFF_P0 = 71303168, OFF_DBG = 80740352, OFF_P1 = 89128960, OFF_QS = 98566144, OFF_S1 = 106954752, OFF_ST = 107479040, OFF_FL = 109051904, OFF_FXP = 109052928, OFF_FH = 109325312, OFF_FX1 = 109597696, OFF_FP = 109871104, OFF_FG = 110416384, OFF_FKV = 111094784, OFF_FY = 111127552, OFF_EP = 111193088;

#define WS_DIAG 0

__device__ __forceinline__ float bf2f(u16 v) { return __uint_as_float(((unsigned)v) << 16); }
__device__ __forceinline__ u16 f2bf(float f) { return __builtin_bit_cast(u16, (__bf16)f); }
__device__ __forceinline__ float rbf(float f) { return bf2f(f2bf(f)); }
__device__ __forceinline__ void st16(void* p, u16x8 v) {
  asm volatile("global_store_dwordx4 %0, %1, off sc0 sc1\n\ts_nop 1" :: "v"(p), "v"(v) : "memory");
}
#define WAIT_VM(n) asm volatile("s_waitcnt vmcnt(%0)" ::"n"(n) : "memory")
#define WAIT_LGKM0() asm volatile("s_waitcnt lgkmcnt(0)" ::: "memory")

#define CODE_RANGE(B, E, p, n)                                                                                   \
  const char* p; unsigned n;                                                                                     \
  { unsigned lo_, hi_;                                                                                           \
    asm volatile("s_getpc_b64 s[98:99]\nLpc%=:\n\ts_add_u32 %0, s98, (" B "-Lpc%=)&4294967295\n\ts_addc_u32 %1, s99, (" B "-Lpc%=)>>32\n\t" \
                 "s_mov_b32 %2, " E "-" B : "=s"(lo_), "=s"(hi_), "=s"(n) :: "s98", "s99", "scc");               \
    p = (const char*)(((unsigned long long)hi_ << 32) | lo_); }
__device__ __forceinline__ unsigned touch(const char* p, unsigned n, unsigned off) {
  unsigned t;
  asm volatile("global_load_dword %0, %1, off" : "=v"(t) : "v"(p + (off < n ? off : 0u)) : "memory");
  return t;
}

__device__ __forceinline__ void touch_lds(const char* p, unsigned a) {
  asm volatile("s_mov_b32 m0, %1\n\ts_nop 0\n\tglobal_load_lds_dword %0, off" :: "v"(p), "s"(a) : "memory");
}
#define PFPOL ""
__device__ __forceinline__ void touch_pol(const char* p, unsigned a) {
  asm volatile("s_mov_b32 m0, %1\n\ts_nop 0\n\tglobal_load_lds_dword %0, off " PFPOL :: "v"(p), "s"(a) : "memory");
}
#define YSLEEP 4
#define KSLEEP 2
#define CTS(i)
#define CTS_INIT()
#define CTS_FLUSH()
__device__ __forceinline__ f32x2 up2(unsigned p) { return (f32x2){__uint_as_float(p << 16), __uint_as_float(p & 0xffff0000u)}; }
__device__ __forceinline__ unsigned pk2(f32x2 v) { return __builtin_bit_cast(unsigned, __builtin_convertvector(v, bf16x2)); }
__device__ __forceinline__ f32x2 rbf2(f32x2 v) { return up2(pk2(v)); }
#define BARRIER() asm volatile("s_waitcnt lgkmcnt(0)\n\ts_barrier" ::: "memory")
template <int N, class F> __device__ __forceinline__ void sfor(F&& f) {
  if constexpr (N > 0) { sfor<N - 1>(f); f(std::integral_constant<int, N - 1>{}); }
}
template <int NL>
__device__ __forceinline__ void poll_flags(const char* p, int n, int lane, unsigned ep) {
#pragma unroll 1
  for (int it = 0; it < 20000; ++it) {
    bool ok = true;
#pragma unroll
    for (int i = 0; i < NL; ++i) {
      const int f = lane + 64 * i;
      ok &= __hip_atomic_load((gu32*)(p + (f < n ? f : 0) * 128), __ATOMIC_RELAXED, __HIP_MEMORY_SCOPE_AGENT) == ep;
    }
    if (__all(ok)) break;
    __builtin_amdgcn_s_sleep(YSLEEP);
  }
  asm volatile("" ::: "memory");
}

constexpr int CV_HP = (2048 + 32) * 2;
constexpr int CV_UR = (2048 + 256) * 2;
constexpr int CV_CH = 2 * CV_HP + CV_UR;
#define MAP4 2
#define CNTL
#define PFL 0
constexpr int CV_STG = 8 * CV_CH;

struct KvRegs { u32x4v k0, k1, k2, k3, k4, k5, k6, v0, v1, v2, v3; };
struct SkRegs { u32x4v s0, s1, s2, s3; };
#define LD16(d, p, pol) asm volatile("global_load_dwordx4 %0, %1, off " pol : "=v"(d) : "v"(p) : "memory")
__device__ __forceinline__ void kv_load(KvRegs& R, const u16* __restrict__ kin, const u16* __restrict__ vin, int b, int tid) {
  const int tr = (b >> 2) * 32 + 4 * (tid >> 5), cc = (b & 3) * 256 + 8 * (tid & 31);
  const u16* kp = kin + (size_t)tr * D_ + cc;
  const u16* vp = vin + (size_t)tr * D_ + cc;
  LD16(R.k3, kp, "nt"); LD16(R.k4, kp + D_, "nt"); LD16(R.k5, kp + 2 * D_, "nt"); LD16(R.k6, kp + 3 * D_, "nt");
  LD16(R.v0, vp, "nt"); LD16(R.v1, vp + D_, "nt"); LD16(R.v2, vp + 2 * D_, "nt"); LD16(R.v3, vp + 3 * D_, "nt");
  const u16* kq = tr ? kp - 3 * D_ : kp;
  LD16(R.k0, kq, "nt"); LD16(R.k1, kq + D_, "nt"); LD16(R.k2, kq + 2 * D_, "nt");
}
__device__ __forceinline__ void sk_load(SkRegs& R, const u16* __restrict__ shift_k, int b, int tid) {
  const u16* sp = shift_k + (size_t)((b & 3) * 256 + 8 * (tid & 31)) * 4;
  LD16(R.s0, sp, ""); LD16(R.s1, sp + 8, ""); LD16(R.s2, sp + 16, ""); LD16(R.s3, sp + 24, "");
}
template <int N>
__device__ __forceinline__ void kv_wait(KvRegs& R, SkRegs& Q) {
  asm volatile("s_waitcnt vmcnt(%15)" : "+v"(R.k0), "+v"(R.k1), "+v"(R.k2), "+v"(R.k3), "+v"(R.k4), "+v"(R.k5), "+v"(R.k6), "+v"(R.v0), "+v"(R.v1), "+v"(R.v2),
               "+v"(R.v3), "+v"(Q.s0), "+v"(Q.s1), "+v"(Q.s2), "+v"(Q.s3) : "n"(N) : "memory");
}
__device__ __forceinline__ void kv_store(KvRegs& R, const SkRegs& Q, char* __restrict__ ws, char* T, int b, int tid) {
  const int t0 = (b >> 2) * 32, c0 = (b & 3) * 256, ch8 = tid & 31, rg = tid >> 5;
  if (t0 + 4 * rg < 3) R.k0 = R.k1 = R.k2 = (u32x4v){0u, 0u, 0u, 0u};
  typedef unsigned u32x2 __attribute__((ext_vector_type(2)));
  const u32x4v kr[7] = {R.k0, R.k1, R.k2, R.k3, R.k4, R.k5, R.k6}, vr[4] = {R.v0, R.v1, R.v2, R.v3}, sk[4] = {Q.s0, Q.s1, Q.s2, Q.s3};
#pragma unroll
  for (int j2 = 0; j2 < 4; ++j2) {
    const f32x2 sa = up2(sk[j2][0]), sb = up2(sk[j2][1]), sc = up2(sk[j2][2]), sd = up2(sk[j2][3]);
    const f32x2 s0 = {sa[0], sc[0]}, s1 = {sa[1], sc[1]}, s2 = {sb[0], sd[0]}, s3 = {sb[1], sd[1]};
    f32x2 kf[7], o[4];
#pragma unroll
    for (int r = 0; r < 7; ++r) kf[r] = up2(kr[r][j2]);
#pragma unroll
    for (int r = 0; r < 4; ++r)
      o[r] = __builtin_elementwise_fma(kf[r + 3], s0, __builtin_elementwise_fma(kf[r + 2], s1, __builtin_elementwise_fma(kf[r + 1], s2, kf[r] * s3))) * up2(vr[r][j2]);
#pragma unroll
    for (int e = 0; e < 2; ++e)
      *(u32x2*)(T + ch8 * 520 + (2 * j2 + e) * 64 + (28 - 4 * rg) * 2) = (u32x2){pk2((f32x2){o[3][e], o[2][e]}), pk2((f32x2){o[1][e], o[0][e]})};
  }
  BARRIER();
  u16* kvr = (u16*)(ws + OFF_KVR);
#pragma unroll
  for (int i = 0; i < 4; ++i) {
    const int ch = (tid >> 2) + 64 * i, cb = tid & 3;
    const char* p = T + ch * 64 + (ch >> 3) * 8 + cb * 16;
    const u16x4 lo = *(const u16x4*)p, hi = *(const u16x4*)(p + 8);
    st16(kvr + (size_t)(c0 + ch) * S_ + (2016 - t0) + cb * 8, __builtin_shufflevector(lo, hi, 0, 1, 2, 3, 4, 5, 6, 7));
  }
}

typedef unsigned u32x2v __attribute__((ext_vector_type(2)));
__device__ __forceinline__ void h_load(u32x4v (&hv)[8], unsigned& hx, unsigned& dD, const u16* __restrict__ ssm_k, const u16* __restrict__ ssm_D, int c0, int tid) {
  const int lane = tid & 63, wave = tid >> 6;
#pragma unroll
  for (int i = 0; i < 8; ++i) LD16(hv[i], ssm_k + (size_t)(c0 + i) * S_ + tid * 8, "nt");
  asm volatile("global_load_dword %0, %1, off" : "=v"(hx) : "v"(ssm_k + (size_t)(c0 + (lane & 7)) * S_ + (wave < 3 ? (wave + 1) * 512 : 0)) : "memory");
  asm volatile("global_load_dword %0, %1, off" : "=v"(dD) : "v"(ssm_D + c0 + 2 * wave) : "memory");
}
template <int N>
__device__ __forceinline__ void h_wait(u32x4v (&hv)[8], unsigned& hx, unsigned& dD) {
  asm volatile("s_waitcnt vmcnt(%10)" : "+v"(hv[0]), "+v"(hv[1]), "+v"(hv[2]), "+v"(hv[3]), "+v"(hv[4]), "+v"(hv[5]), "+v"(hv[6]), "+v"(hv[7]), "+v"(hx), "+v"(dD) : "n"(N) : "memory");
}
__device__ __forceinline__ void h_spread(const u32x4v (&hv)[8], unsigned hx, char* lds, int tid) {
  if (tid < 32 && (tid & 3) != 3) *(u32x4v*)(lds + (tid >> 2) * CV_CH + ((tid >> 1) & 1) * CV_HP + (tid & 1) * 16) = (u32x4v){0u, 0u, 0u, 0u};
  *(u32x4v*)(lds + (tid >> 5) * CV_CH + 2 * CV_HP + 4096 + (tid & 31) * 16) = (u32x4v){0u, 0u, 0u, 0u};
#pragma unroll
  for (int i = 0; i < 8; ++i) {
    char* base = lds + i * CV_CH + 32 + tid * 16;
    unsigned hn = __builtin_amdgcn_update_dpp(0, (int)hv[i][0], 0x130, 0xf, 0xf, true);
    const unsigned last = __builtin_amdgcn_readlane(hx, i);
    if ((tid & 63) == 63) hn = last;
#define AB(a, b) __builtin_amdgcn_alignbyte(a, b, 2)
    if (tid == 0) *(u32x4v*)(lds + i * CV_CH + CV_HP + 16) = (u32x4v){0u, 0u, 0u, hv[i][0] << 16};
    *(u32x4v*)(base) = hv[i];
    *(u32x4v*)(base + CV_HP) = (u32x4v){AB(hv[i][1], hv[i][0]), AB(hv[i][2], hv[i][1]), AB(hv[i][3], hv[i][2]), AB(hn, hv[i][3])};
#undef AB
  }
}

__device__ __forceinline__ void pads(char* __restrict__ ws, int w, int lane, unsigned ep) {
  if (w < 132) {
    const int k = w < 128 ? w >> 5 : w - 128, u = k == 0 ? -1 : k == 1 ? 128 : k == 2 ? 131 : 260;
    st16(ws + (w < 128 ? OFF_HS + u * 32768 + (w & 31) * 1024 : OFF_FH + u * 1024) + lane * 16, __builtin_bit_cast(u16x8, (u32x4v){w >= 128 && !(lane & 7) ? ep : 0u, 0u, 0u, 0u}));
  }
}
__device__ __forceinline__ void prewrite(char* __restrict__ ws, unsigned off, unsigned str, int rep, int tid) {
#pragma unroll
  for (int i = 0; i < 2; ++i) st16(ws + off + (rep + 4 * (tid >> 3) + 132 * i) * str + (tid & 7) * 16, __builtin_bit_cast(u16x8, (u32x4v){0u, 0u, 0u, 0u}));
}

__device__ __forceinline__ void conv_part(char* __restrict__ ws, unsigned dD, char* lds, int g, int tid, int lane, int wave, unsigned ep, const char* wp, unsigned wstep) {
  const __amdgpu_buffer_rsrc_t rs = __builtin_amdgcn_make_buffer_rsrc(ws, 0, WS_USED, 0x00020000);
  const u16* kvr = (const u16*)(ws + OFF_KVR) + (size_t)(8 * g + 2 * wave) * S_;
  auto wait1 = [&](unsigned off) {
#pragma unroll 1
    for (int it = 0; it < 20000; ++it) {
      if (__all(__hip_atomic_load((gu32*)(ws + off), __ATOMIC_RELAXED, __HIP_MEMORY_SCOPE_AGENT) == ep)) break;
      __builtin_amdgcn_s_sleep(KSLEEP);
    }
    asm volatile("" ::: "memory");
  };
  const unsigned lb = (unsigned)(size_t)lds;
  auto u_fetch = [&](int part) {
#pragma unroll
    for (int j = 0; j < 2; ++j)
      asm volatile("s_mov_b32 m0, %1\n\ts_nop 0\n\tglobal_load_lds_dwordx4 %0, off sc1" :: "v"(kvr + j * S_ + part * 512 + lane * 8),
                   "s"(lb + (2 * wave + j) * CV_CH + 2 * CV_HP + part * 1024) : "memory");
  };
  const unsigned xo = (tid >> 4) * 32768 + g * 256 + (tid & 15) * 16;
  u32x4v qv[6];
  u32x2v qs[6];
  auto q_fetch = [&](int s0, int ns) {
#pragma unroll
    for (int i = 0; i < 6; ++i)
      if (i < ns) {
        asm volatile("buffer_load_dwordx4 %0, %1, %2, %3 offen sc1" : "=v"(qv[i]) : "v"(xo), "s"(rs), "s"(OFF_XP + (s0 + i) * 524288) : "memory");
        asm volatile("buffer_load_dwordx2 %0, %1, %2, %3 offen sc1" : "=v"(qs[i]) : "v"(tid * 8), "s"(rs), "s"(OFF_QS + (s0 + i) * 2048) : "memory");
      }
  };
  const int cg = g >> 5;
  const int l15 = lane & 15, lg = lane >> 4, cp = (l15 + 1) & 1;
  const unsigned ao = cp * CV_HP + 2 * (8 * lg + (l15 + 1 - cp)), bo = 2 * CV_HP + 2 * (2032 - 16 * l15 + 8 * lg) - 2 * 256 * 7;
  const unsigned own = lb + 2 * wave * CV_CH;
  char* stg = lds + CV_STG;
  s16x8 ad[2];
#pragma unroll
  for (int j = 0; j < 2; ++j) {
    const int e = 15 - l15 - 8 * lg;
    const unsigned d = (j ? dD >> 16 : dD & 0xffffu) << ((e & 1) * 16);
    ad[j] = __builtin_bit_cast(s16x8, (u32x4v){e >> 1 == 0 ? d : 0u, e >> 1 == 1 ? d : 0u, e >> 1 == 2 ? d : 0u, e >> 1 == 3 ? d : 0u});
  }
  f32x4 acc[2][6];
  unsigned f0, f1 = 0;
  bool mid;
  auto finish = [&](auto s0_c, auto ns_c) {
    constexpr int S0 = decltype(s0_c)::value, NS = decltype(ns_c)::value, A0 = 6 - NS;
#pragma unroll
    for (int s = 0; s < NS; ++s)
#pragma unroll
      for (int r = 0; r < 4; ++r) *(unsigned*)(stg + s * 4352 + l15 * 272 + (4 * lg + r) * 16 + wave * 4) = pk2((f32x2){acc[0][A0 + s][r], acc[1][A0 + s][r]});
    if (NS == 2) {
      asm volatile("s_waitcnt vmcnt(0)" : "+v"(qv[0]), "+v"(qv[1]), "+v"(qs[0]), "+v"(qs[1]), "+v"(f0) :: "memory");
      mid = __all(f0 == ep);
      if (mid) { u_fetch(2); u_fetch(1); }
    } else asm volatile("" : "+v"(qv[0]), "+v"(qv[1]), "+v"(qv[2]), "+v"(qv[3]), "+v"(qv[4]), "+v"(qv[5]), "+v"(qs[0]), "+v"(qs[1]), "+v"(qs[2]), "+v"(qs[3]), "+v"(qs[4]), "+v"(qs[5]) :: "memory");
    BARRIER();
#pragma unroll
    for (int s = 0; s < NS; ++s) {
      const u32x4v y4 = *(const u32x4v*)(stg + s * 4352 + (tid >> 4) * 272 + (tid & 15) * 16);
      const f32x2 rd2 = {__uint_as_float(qs[s][0]), __uint_as_float(qs[s][0])}, nm2 = {__uint_as_float(qs[s][1]), __uint_as_float(qs[s][1])};
      u32x4v o;
#pragma unroll
      for (int e = 0; e < 4; ++e) {
        const f32x2 x = up2(qv[s][e]);
        o[e] = pk2(__builtin_elementwise_fma(__builtin_elementwise_fma(x, rd2, nm2), up2(y4[e]), x));
      }
      __builtin_amdgcn_raw_buffer_store_b128(o, rs, xo, OFF_XP + (S0 + s) * 524288, 17);
      __builtin_amdgcn_raw_buffer_store_b128(y4, rs, (tid >> 3) * 16384 + g * 128 + (tid & 7) * 16, OFF_YF + (S0 + s) * 524288, 17);
    }
    CTS(S0 ? 7 : 5);
  };
  auto count = [&](int s0, int ns) {
    WAIT_VM(0);
    BARRIER();
    if (tid < 4 * ns) __hip_atomic_fetch_add((gu32*)(ws + OFF_FXP + (s0 + (tid >> 2)) * 16384 + (tid & 3) * 1024 + (g >> 4) * 128), 1u, __ATOMIC_RELAXED, __HIP_MEMORY_SCOPE_AGENT);
  };
  auto pair = [&](unsigned bb) {
    u32x2v ax[2][2][2][2];
    s16x8 bx[2][2][2];
    auto rdA = [&](int q) {
#pragma unroll
      for (int j = 0; j < 2; ++j)
#pragma unroll
        for (int m = 0; m < 2; ++m) {
          asm volatile("ds_read2_b32 %0, %1 offset0:%2 offset1:%3" : "=v"(ax[q & 1][j][m][0]) : "v"(own + j * CV_CH + ao), "n"(128 * m + 16 * q), "n"(128 * m + 16 * q + 1));
          asm volatile("ds_read2_b32 %0, %1 offset0:%2 offset1:%3" : "=v"(ax[q & 1][j][m][1]) : "v"(own + j * CV_CH + ao), "n"(128 * m + 16 * q + 2), "n"(128 * m + 16 * q + 3));
          asm volatile("ds_read_b128 %0, %1 offset:%2" : "=v"(bx[q & 1][j][m]) : "v"(bb), "n"(j * CV_CH + 512 * (1 - m) + 64 * q));
        }
    };
    rdA(0);
    sfor<8>([&](auto q_c) {
      constexpr int q = decltype(q_c)::value, p = q & 1;
      if (q + 1 < 8) rdA(q + 1);
      asm volatile("s_waitcnt lgkmcnt(%0)" :: "n"(q + 1 < 8 ? 12 : 0) : "memory");
      __builtin_amdgcn_sched_barrier(0);
#pragma unroll
      for (int j = 0; j < 2; ++j) {
        const s16x8 a0f = __builtin_bit_cast(s16x8, __builtin_shufflevector(ax[p][j][0][0], ax[p][j][0][1], 0, 1, 2, 3)), a1f = __builtin_bit_cast(s16x8, __builtin_shufflevector(ax[p][j][1][0], ax[p][j][1][1], 0, 1, 2, 3));
        if (q == 0) {
          acc[j][4] = __builtin_amdgcn_mfma_f32_16x16x32_bf16(ad[j], bx[p][j][0], acc[j][4], 0, 0, 0);
          acc[j][5] = __builtin_amdgcn_mfma_f32_16x16x32_bf16(ad[j], bx[p][j][1], acc[j][5], 0, 0, 0);
        }
        acc[j][4] = __builtin_amdgcn_mfma_f32_16x16x32_bf16(a0f, bx[p][j][0], acc[j][4], 0, 0, 0);
        acc[j][5] = __builtin_amdgcn_mfma_f32_16x16x32_bf16(a0f, bx[p][j][1], acc[j][5], 0, 0, 0);
        acc[j][5] = __builtin_amdgcn_mfma_f32_16x16x32_bf16(a1f, bx[p][j][0], acc[j][5], 0, 0, 0);
      }
      __builtin_amdgcn_sched_barrier(0);
    });
  };
  wait1(OFF_FKV + ((lane & 15) * 4 + cg) * 128);
  CTS(3);
  u_fetch(3);
  wait1(OFF_FY + (lane & 31) * 128);
  q_fetch(0, 2);
  const unsigned fm = OFF_FKV + ((16 + (lane & 31)) * 4 + cg) * 128;
  asm volatile("global_load_dword %0, %1, %2 sc1" : "=v"(f0) : "v"(fm), "s"(ws) : "memory");
  CTS(4);
#pragma unroll
  for (int i = 0; i < 4; ++i) acc[i >> 1][4 + (i & 1)] = (f32x4){0.f, 0.f, 0.f, 0.f};
  pair(own + bo + 512 * 6);
  finish(std::integral_constant<int, 0>{}, std::integral_constant<int, 2>{});
  u32x2v a0[8][2], a1[8][2];
  s16x8 b0[6], b1[6];
  unsigned pa[4], pb = own + bo;
#pragma unroll
  for (int i = 0; i < 4; ++i) pa[i] = own + ao + 1024 * i;
  auto rd1 = [&](int rr, int i, u32x2v (&ah)[8][2], s16x8 (&bfr)[6]) {
    const int m = i < 18 ? i / 3 : 6 + (i - 18) / 2, e = i < 18 ? i % 3 : i & 1;
    if (e < 2) asm volatile("ds_read2_b32 %0, %1 offset0:%2 offset1:%3" : "=v"(ah[m][e & 1]) : "v"(pa[m >> 1]), "n"(128 * (m & 1) + 2 * e + 16 * rr), "n"(128 * (m & 1) + 2 * e + 16 * rr + 1));
    else asm volatile("ds_read_b128 %0, %1 offset:%2" : "=v"(bfr[m < 6 ? m : 0]) : "v"(pb), "n"(512 * (7 - m) + 64 * rr));
  };
  auto round = [&](f32x4 (&ac)[6], u32x2v (&ah)[8][2], s16x8 (&bfr)[6], u32x2v (&ahn)[8][2], s16x8 (&bfn)[6], int rr, unsigned step, bool pf) {
    int k = 0;
#pragma unroll
    for (int s = 2; s <= 7; ++s)
#pragma unroll
      for (int m = 0; m <= s; ++m) {
        if (s - m > 5) continue;
        const s16x8 af = __builtin_bit_cast(s16x8, __builtin_shufflevector(ah[m][0], ah[m][1], 0, 1, 2, 3));
        ac[s - 2] = __builtin_amdgcn_mfma_f32_16x16x32_bf16(af, bfr[s - m > 5 ? 0 : s - m], ac[s - 2], 0, 0, 0);
        __builtin_amdgcn_sched_barrier(0);
        if (m == s) asm volatile("s_waitcnt lgkmcnt(12)" ::: "memory");
#pragma unroll
        for (int i = 0; i < 22; ++i)
          if (i * 4 / 3 == k) {
            rd1(rr, i, ahn, bfn);
            if (rr && (i == 4 || i == 10 || i == 16 || i == 21)) asm volatile("v_add_u32 %0, %1, %0" : "+v"(pa[i / 6]) : "s"(step));
            if (rr && i == 17) asm volatile("v_add_u32 %0, %1, %0" : "+v"(pb) : "s"(step));
          }
        if (k == 7 && rr && pf) asm volatile("global_load_lds_dword %0, off\n\tv_lshl_add_u64 %0, %0, 0, %1" : "+v"(wp) : "s"((unsigned long long)wstep) : "memory");
        ++k;
        __builtin_amdgcn_sched_barrier(0);
      }
  };
  if (!mid) { wait1(fm); u_fetch(2); u_fetch(1); WAIT_VM(0); }
  WAIT_VM(4);
#pragma unroll
  for (int i = 0; i < 22; ++i) rd1(0, i, a0, b0);
  WAIT_LGKM0();
  auto channel = [&](auto j_c) {
    constexpr int j = decltype(j_c)::value;
    asm volatile("s_mov_b32 m0, %0" :: "s"(lb + 163584) : "memory");
    __builtin_amdgcn_sched_barrier(0);
#pragma unroll
    for (int s = 2; s <= 7; ++s) acc[j][s - 2] = s < 6 ? __builtin_amdgcn_mfma_f32_16x16x32_bf16(ad[j], b0[s], (f32x4){0.f, 0.f, 0.f, 0.f}, 0, 0, 0) : (f32x4){0.f, 0.f, 0.f, 0.f};
#pragma unroll 1
    for (int r = 0; r < 8; r += 2) {
      if (j == 0 && r == 6) {
        asm volatile("global_load_dword %0, %1, %2 sc1" : "+v"(f0) : "v"(OFF_FY + (32 + lane) * 128), "s"(ws) : "memory");
        asm volatile("global_load_dword %0, %1, %2 sc1" : "+v"(f1) : "v"(lane < 32 ? OFF_FY + (96 + lane) * 128 : OFF_FKV + ((48 + (lane & 15)) * 4 + cg) * 128), "s"(ws) : "memory");
      }
      if (j == 0 && r == 2) {
        asm volatile("s_waitcnt vmcnt(%0)\n\ts_barrier" :: "n"(PFL & 1) : "memory");
        if (tid < 8) __hip_atomic_fetch_add((gu32*)(ws + OFF_FXP + (tid >> 2) * 16384 + (tid & 3) * 1024 + (g >> 4) * 128), 1u, __ATOMIC_RELAXED, __HIP_MEMORY_SCOPE_AGENT);
      }
      round(acc[j], a0, b0, a1, b1, 1, r < 6 ? 128 : j ? 0 : CV_CH - 384, PFL >> j & 1);
      round(acc[j], a1, b1, a0, b0, 0, 0, false);
    }
    WAIT_LGKM0();
    __builtin_amdgcn_sched_barrier(0);
  };
  f0 = 0;
  channel(std::integral_constant<int, 0>{});
  CTS(13);
  asm volatile("s_waitcnt vmcnt(%2)" : "+v"(f0), "+v"(f1) : "n"(PFL & 1) : "memory");
  if (!__all(f0 == ep && f1 == ep)) {
    wait1(OFF_FY + (32 + lane) * 128);
    wait1(lane < 32 ? OFF_FY + (96 + lane) * 128 : OFF_FKV + ((48 + (lane & 15)) * 4 + cg) * 128);
  }
  u_fetch(0);
  q_fetch(2, 6);
#pragma unroll
  for (int i = 0; i < 3; ++i) touch_lds(wp + (size_t)i * wstep, lb + 163584);
  CTS(15);
  channel(std::integral_constant<int, 1>{});
  WAIT_VM(3);
  CTS(14);
  pair(own + bo);
  CTS(11);
  finish(std::integral_constant<int, 2>{}, std::integral_constant<int, 6>{});
  count(2, 6);
}
__device__ __forceinline__ float sum2_seg(float s, float q) {
  s += __builtin_bit_cast(float, __builtin_amdgcn_update_dpp(0, __builtin_bit_cast(int, s), 0x128, 0xf, 0xf, true));
  q += __builtin_bit_cast(float, __builtin_amdgcn_update_dpp(0, __builtin_bit_cast(int, q), 0x128, 0xf, 0xf, true));
  asm volatile("s_nop 1\n\tv_permlane16_swap_b32 %0, %1" : "+v"(s), "+v"(q));
  s += q;
  q = s;
  asm volatile("s_nop 1\n\tv_permlane32_swap_b32 %0, %1" : "+v"(s), "+v"(q));
  return s + q;
}
#define LASTSLEEP 8
__device__ __forceinline__ void wait_flags(const char* p, int lane, int n, unsigned ep) {
  gu32* f = (gu32*)(p + (lane < n ? lane : 0) * 128);
#pragma unroll 1
  for (int it = 0; it < 10000; ++it) {
    if (__all(__hip_atomic_load(f, __ATOMIC_RELAXED, __HIP_MEMORY_SCOPE_AGENT) == ep)) break;
    __builtin_amdgcn_s_sleep(LASTSLEEP);
  }
  asm volatile("" ::: "memory");
}
struct QRegs { u32x4v a[4], b[4]; };
__device__ __forceinline__ void q_load(QRegs& R, const u16* __restrict__ q, int f, int lane, int wave) {
  const u16* p = q + (size_t)(f * 16 + (lane & 7)) * D_ + wave * 256 + (lane >> 3) * 8;
#pragma unroll
  for (int i = 0; i < 4; ++i) { LD16(R.a[i], p + i * 64, "nt"); LD16(R.b[i], p + 8 * D_ + i * 64, "nt"); }
}
template <int N>
__device__ __forceinline__ void q_wait(QRegs& R, unsigned& e) {
  asm volatile("s_waitcnt vmcnt(%9)" : "+v"(R.a[0]), "+v"(R.a[1]), "+v"(R.a[2]), "+v"(R.a[3]), "+v"(R.b[0]), "+v"(R.b[1]), "+v"(R.b[2]), "+v"(R.b[3]), "+v"(e) : "n"(N) : "memory");
}
__device__ __forceinline__ void row_stats(const u32x4v (&xq)[4], char* lds, int k, int lane, int wave, float& mean, float& var) {
  f32x2* red = (f32x2*)(lds + 16640);
  const int r8 = lane & 7, seg = lane >> 3;
  f32x2 s = {0.f, 0.f}, sq = {0.f, 0.f};
#pragma unroll
  for (int i = 0; i < 16; ++i) { const f32x2 x = up2(xq[i >> 2][i & 3]); s += x; sq = __builtin_elementwise_fma(x, x, sq); }
  const float a = sum2_seg(s[0] + s[1], sq[0] + sq[1]);
  if ((seg & 5) == 0) ((float*)red)[((k * 4 + wave) * 8 + r8) * 2 + (seg >> 1)] = a;
  BARRIER();
  const f32x2 t = (red[(k * 4) * 8 + r8] + red[(k * 4 + 1) * 8 + r8]) + (red[(k * 4 + 2) * 8 + r8] + red[(k * 4 + 3) * 8 + r8]);
  mean = t[0] * (1.0f / 1024.0f);
  var = fmaxf(t[1] * (1.0f / 1024.0f) - mean * mean, 0.f);
}
__device__ __forceinline__ void q_store(char* __restrict__ ws, int t, int lane, int wave, const u32x4v (&xq)[4], float mean, float var) {
  const __amdgpu_buffer_rsrc_t rs = __builtin_amdgcn_make_buffer_rsrc(ws, 0, WS_USED, 0x00020000);
  const int r8 = lane & 7, seg = lane >> 3;
  const unsigned fo = (t >> 1) * 32768 + (wave * 8 + (seg >> 2)) * 1024 + ((seg & 3) * 16 + (t & 1) * 8 + r8) * 16;
#pragma unroll
  for (int i = 0; i < 4; ++i) __builtin_amdgcn_raw_buffer_store_b128(xq[i], rs, fo + i * 2048, OFF_XP, 17);
  const float rd = __builtin_amdgcn_rsqf(var + 1e-5f);
  if (wave == 0 && seg == 0) __builtin_amdgcn_raw_buffer_store_b64((u32x2v){__float_as_uint(rd), __float_as_uint(-mean * rd)}, rs, t * 64 + r8 * 8, OFF_QS, 17);
}
__device__ __forceinline__ void row_last(char* __restrict__ ws, u16* __restrict__ out, int t, int lane, int wave, unsigned ep) {
  const __amdgpu_buffer_rsrc_t rs = __builtin_amdgcn_make_buffer_rsrc(ws, 0, WS_USED, 0x00020000);
  const int r8 = lane & 7, seg = lane >> 3;
  const unsigned fo = (4 * LB + t) * 16384 + wave * 4096 + lane * 16;
  wait_flags(ws + OFF_FP + (2 * LB + (t >> 1)) * 2048, lane, 16, ep);
  u32x4v p0[4], p1[4];
#pragma unroll
  for (int i = 0; i < 4; ++i) {
    p0[i] = __builtin_bit_cast(u32x4v, __builtin_amdgcn_raw_buffer_load_b128(rs, fo + i * 1024, OFF_P0, 16));
    p1[i] = __builtin_bit_cast(u32x4v, __builtin_amdgcn_raw_buffer_load_b128(rs, fo + i * 1024, OFF_P1, 16));
  }
#pragma unroll
  for (int i = 0; i < 4; ++i) {
    u32x4v o;
#pragma unroll
    for (int e = 0; e < 4; ++e) o[e] = pk2(up2(p0[i][e]) + up2(p1[i][e]));
    st16(out + (size_t)(t * 8 + r8) * D_ + wave * 256 + i * 64 + seg * 8, __builtin_bit_cast(u16x8, o));
  }
}

__device__ __forceinline__ int swzB(int r) { return ((((r >> 1) & 1) | (((r >> 3) & 1) << 1)) << 1); }
#define FLW 1
#define MAP3 1
#define FLIP 0
#define NRW 18
#define PFROWS 110
__global__ __launch_bounds__(256, 1) void flow_kernel(const u16* __restrict__ q, const u16* __restrict__ kin, const u16* __restrict__ vin,
                                                      const u16* __restrict__ shift_k, const u16* __restrict__ ssm_k, const u16* __restrict__ ssm_D,
                                                      const u16* __restrict__ w1, const u16* __restrict__ w2,
                                                      const u16* __restrict__ w3, const u16* __restrict__ w4, char* __restrict__ ws,
                                                      u16* __restrict__ out) {
  __shared__ __attribute__((aligned(16))) char lds[163840];
  const int tid = threadIdx.x, lane = tid & 63, wave = __builtin_amdgcn_readfirstlane(tid >> 6);
  const int b = blockIdx.x, xcd = b & 7, idx = b >> 3;
  const unsigned sLds = (unsigned)(size_t)lds;
  int kind, rep, slice, kh = 0;
  const int P = xcd >> 1, par = (xcd & 1) ^ FLIP, nE = par ? 20 : 12, li = idx - nE;
  const int qq = (par ? 0 : 12) + idx - 8;
  if (idx < 8) { kind = 1 + (par ^ MAP12); rep = P; slice = idx; }
  else if (li < 0) {
    const int e = 15 * P + qq, t = (e * 205) >> 11, k = e - 10 * t, k3 = k < 3 ? 0 : k < 6 ? 1 : 2;
    kind = qq < 15 ? 3 : 5; slice = qq < 15 ? 3 * t + k3 : 18; rep = qq < 15 ? k - 3 * k3 : P;
  } else if (!par) { kind = 4; kh = P & 1; rep = li >> 2; slice = 2 * (P & 2) + (li & 3); }
  else {
    const int nl = 8 - (P & 1), l = ((15 * P + 1) >> 1) + li, t = (l * 205) >> 10, k = l - 5 * t;
    kind = li < nl ? 3 : 6; rep = li < nl ? (k == 4 ? 4 : 3 + (k & 1)) : 0;
    slice = li < nl ? 3 * t + (k >> 1) : ((9 * P) >> 1) + li - nl;
  }
  CTS_INIT();
  CTS(0);
  {
    CODE_RANGE("Lpc%=", "Lcode_g12_b", c0, n0) CODE_RANGE("Lcode_r_e", "Lcode_g4_b", c7, n7)
    touch_lds(tid < 224 ? c0 + (tid * 128 < n0 ? tid * 128 : 0) : c7 + ((tid - 224) * 128 < n7 ? (tid - 224) * 128 : 0), sLds + 163584);
  }
  const unsigned sScr = sLds + 163584;
  const char* cp;
  unsigned co0, co1;
  {
    unsigned lo, hi, n;
    asm volatile("s_getpc_b64 s[98:99]\nLpd%=:\n"
                 "s_mov_b32 %0, Lcode_r_b-Lpd%=\ns_mov_b32 %2, Lcode_r_e-Lcode_r_b\n"
                 "s_cmp_eq_u32 %3, 1\ns_cselect_b32 %0, Lcode_g12_b-Lpd%=, %0\ns_cselect_b32 %2, Lcode_g2_b-Lcode_g12_b, %2\n"
                 "s_cmp_eq_u32 %3, 2\ns_cselect_b32 %0, Lcode_g2_b-Lpd%=, %0\ns_cselect_b32 %2, Lcode_g12_e-Lcode_g2_b, %2\n"
                 "s_cmp_eq_u32 %3, 3\ns_cselect_b32 %0, Lcode_g3_b-Lpd%=, %0\ns_cselect_b32 %2, Lcode_g3_e-Lcode_g3_b, %2\n"
                 "s_cmp_eq_u32 %3, 4\ns_cselect_b32 %0, Lcode_g4_b-Lpd%=, %0\ns_cselect_b32 %2, Lcode_g4_e-Lcode_g4_b, %2\n"
                 "s_cmp_eq_u32 %3, 5\ns_cselect_b32 %0, Lcode_g3o_b-Lpd%=, %0\ns_cselect_b32 %2, Lcode_g3o_e-Lcode_g3o_b, %2\n"
                 "s_add_u32 %0, s98, %0\ns_addc_u32 %1, s99, 0"
                 : "=&s"(lo), "=&s"(hi), "=&s"(n) : "s"(kind) : "s98", "s99", "scc");
    cp = (const char*)(((unsigned long long)hi << 32) | lo);
    co0 = tid * 128 < n ? tid * 128 : 0;
    co1 = 32768 + tid * 128 < n ? 32768 + tid * 128 : 0;
  }
  auto code_touch = [&]() { touch_lds(cp + co0, sScr); touch_lds(cp + co1, sScr); };
  unsigned ep = 0;
#define EP_LOAD() asm volatile("global_load_dword %0, %1, %2 sc1" : "=v"(ep) : "v"(0u), "s"(ws + OFF_EP) : "memory")
  if (kind == 1) {
    code_touch();
    prewrite(ws, OFF_FH + slice * 128, 1024, rep, tid);
  }
  else {
    const int cv = li < 0 ? -1 : (li + (par ? 20 : 0)) * 4 + P;
    auto epoch = [&]() { const unsigned e = __builtin_amdgcn_readfirstlane(ep) + 1u; return e ? e : 1u; };
    auto q_unit = [&](QRegs& QR, int f) {
      float ma, va, mb, vb_;
      row_stats(QR.a, lds + CV_STG, 0, lane, wave, ma, va);
      row_stats(QR.b, lds + CV_STG, 1, lane, wave, mb, vb_);
      q_store(ws, 2 * f, lane, wave, QR.a, ma, va);
      q_store(ws, 2 * f + 1, lane, wave, QR.b, mb, vb_);
    };
    auto publish = [&](unsigned off) {
      BARRIER();
      if (tid == 0) __hip_atomic_store((gu32*)(ws + off), ep, __ATOMIC_RELAXED, __HIP_MEMORY_SCOPE_AGENT);
    };
    KvRegs KR;
    SkRegs SK;
    if (cv >= 0) {
      u32x4v hv[8];
      unsigned hx, dD;
      EP_LOAD();
      h_load(hv, hx, dD, ssm_k, ssm_D, 8 * cv, tid);
      kv_load(KR, kin, vin, 64 + cv, tid);
      sk_load(SK, shift_k, cv, tid);
      code_touch();
      touch_lds((const char*)((par ^ MAP12) ? w2 : w1) + (size_t)(par ? (li < 8 ? li * 256 + tid : 0) : (li < 16 ? li * 128 + (tid & 127) : 0)) * 1024 + 384, sScr);
      h_wait<18>(hv, hx, dD);
      asm volatile("" : "+v"(ep));
      ep = epoch();
      CTS(12);
      h_spread(hv, hx, lds, tid);
      kv_wait<3>(KR, SK);
      kv_store(KR, SK, ws, lds + CV_STG, 64 + cv, tid);
      CTS(1);
      WAIT_VM(0);
      publish(OFF_FKV + (64 + cv) * 128);
      CTS(2);
      const char* wp = (const char*)w4 + (par ? 0 : (size_t)(kh * 1792 + min(li * 90, 1696) + (tid >> 3)) * 2048 + (P & 2) * 512 + (tid & 7) * 128);
      conv_part(ws, dD, lds, cv, tid, lane, wave, ep, wp, par ? 0 : 32 * 2048);
      CTS(8);
    } else {
      const int n = kind == 2 ? 64 + rep * 8 + slice : qq * 4 + P;
      QRegs QR;
      if (n < 64) {
        EP_LOAD();
        kv_load(KR, kin, vin, n, tid);
        sk_load(SK, shift_k, n, tid);
        kv_wait<0>(KR, SK);
        asm volatile("" : "+v"(ep));
        ep = epoch();
        CTS(12);
        KvRegs K2;
        kv_store(KR, SK, ws, lds, n, tid);
        pads(ws, n * 4 + wave, lane, ep);
        if (n < 8)
          st16(ws + OFF_FXP + (lane >> 3) * 16384 + wave * 1024 + (2 * (n & 3) + (n >> 2)) * 128 + (lane & 7) * 16, __builtin_bit_cast(u16x8, (u32x4v){ep - 16u, 0u, 0u, 0u}));
        CTS(1);
        kv_load(K2, kin, vin, 192 + n, tid);
        WAIT_VM(11);
        publish(OFF_FKV + n * 128);
        CTS(2);
        q_load(QR, q, 32 + n, lane, wave);
        kv_wait<8>(K2, SK);
        kv_store(K2, SK, ws, lds + 16896, 192 + n, tid);
        q_wait<0>(QR, ep);
        publish(OFF_FKV + (192 + n) * 128);
        CTS(3);
      } else {
        QRegs Q0;
        EP_LOAD();
        q_load(Q0, q, n - 64, lane, wave);
        q_wait<0>(Q0, ep);
        ep = epoch();
        CTS(12);
        q_load(QR, q, 32 + n, lane, wave);
        q_unit(Q0, n - 64);
        CTS(1);
        q_wait<0>(QR, ep);
        publish(OFF_FY + (n - 64) * 128);
        CTS(2);
      }
      q_unit(QR, 32 + n);
      {
        const char* wp = (const char*)w2 + (size_t)(n < 64 ? 0 : (n - 64) * 512 + tid) * 128;
        touch_lds(wp, sScr);
        touch_lds(wp + (n < 64 ? 0 : 32768), sScr);
      }
      WAIT_VM(2);
      publish(OFF_FY + (32 + n) * 128);
      CTS(9);
      if (n >= 64) {
        prewrite(ws, OFF_FX1 + slice * 128, 1024, rep, tid);
        prewrite(ws, OFF_FP + slice * 128, 2048, rep, tid);
        prewrite(ws, OFF_FP + (8 + slice) * 128, 2048, rep, tid);
        prewrite(ws, OFF_FG + slice * 128, 2560, rep, tid);
        prewrite(ws, OFF_FG + (8 + slice) * 128, 2560, rep, tid);
        if (slice < 3) prewrite(ws, OFF_FG + (16 + slice) * 128, 2560, rep, tid);
        st16(ws + OFF_FXP + slice * 128 + (132 + rep + 4 * (tid >> 3)) * 1024 + (tid & 7) * 16, __builtin_bit_cast(u16x8, (u32x4v){0u, 0u, 0u, 0u}));
      }
      code_touch();
      if (n < 64) {
#pragma unroll
        for (int i = 0; i < 4; ++i) {
          const unsigned x = rep * 1024 + i * 256 + tid, row = kind == 3 ? (x * 21846) >> 16 : x >> 1;
          if (rep < (kind == 3 ? 3 : 2)) touch_lds((const char*)w3 + (size_t)row * 7168 + slice * 384 + (x - row * (kind == 3 ? 3 : 2)) * 128, sScr);
        }
      }
    }
    BARRIER();
    CTS(10);
  }
  CTS_FLUSH();
  const unsigned sDbg0 = OFF_DBG + (b * 4 + wave) * 8192;
#define LANE_ID(x) int x; asm volatile("v_mbcnt_lo_u32_b32 %0, -1, 0\n\tv_mbcnt_hi_u32_b32 %0, -1, %0" : "=v"(x))
#define OOB 0x80000000u
#define COMMON(NF, FIN, FOUT, SN0, WS, LDW, NREP)                                                                                    \
  LANE_ID(ln);                                                                                                                       \
  const int l15 = ln & 15, lg = ln >> 4, lq = l15 >> 2, lp = ln & 3, rr = 8 * lg + lq;                                               \
  const unsigned vA = ln * 16, vFlag = (ln < FLW && wave == 3) ? (FOUT) + ln * 4 : OOB;                                                        \
  const unsigned vPollOff = wave < 2 && ln < (NF) ? (FIN) + ((NF) == 16 && ln >= 8 ? (ln & 2 ? ln : ln - 16) : ln) * 128 : OOB, vSnapOff = wave == 0 ? vPollOff : OOB;                   \
  const unsigned vSnapRd = (SN0) + (ln >> 5) * (WS) + ((ln & 31) < (NF) ? (ln & 31) : 0) * 4;                                        \
  const unsigned vWd0 = (ln >> 3) * (LDW) + ((ln & 7) ^ swzB(ln >> 3)) * 16, vWd1 = ((ln >> 3) + 8) * (LDW) + ((ln & 7) ^ swzB(8 + (ln >> 3))) * 16; \
  const unsigned vWr0 = (wave >> 1) * 8192 + rr * 128 + (((wave & 1) * 4 + lp) ^ swzB(rr)) * 16, vWr1 = vWr0 + 8;                    \
  const unsigned vWr2 = 16384 + rr * 128 + ((2 * wave + (lp >> 1)) ^ swzB(rr)) * 16 + (lp & 1) * 8;                                  \
                                            \
  const int res = kind == 3 ? rep + 3 * KE : rep;                     \
  const unsigned sStep = (NREP) << 15, sU0 = (rep < 3 ? rep : res) << 15, sX1 = ((NREP) == 4 ? 128 + rep : 128 + (res + 2) % 5) << 15; \
  const unsigned sX2 = ((NREP) == 4 ? 260 + rep : 260 + (res + 4) % 5) << 15, sDbg = sDbg0, sBuf = kind != 3 ? kind : (rep < 3 ? 3 : 5) << 15
  constexpr unsigned AUX = 131072;
  if (kind == 3 || kind == 5) {
    COMMON(8, OFF_FX1, OFF_FG + slice * 128, AUX + 1536, 2048, 7168, kind == 3 ? 5 : 4);
    const unsigned vSrc = OFF_X1 + wave * 4096 + vA, vOut = OFF_G + (slice * 6 + wave) * 1024 + vA;
    const unsigned vOut2 = OFF_G + (slice * 6 + 4 + (wave >> 1)) * 1024 + (((wave & 1) * 2 + (lg >> 1)) * 16 + l15) * 16 + (lg & 1) * 8;
    const unsigned vAuxS = OFF_ST + (ln & 7) * 128 + (wave * 8 + (ln >> 3)) * 4, vX = AUX + wave * 2048 + ln * 4;
    const unsigned vStW = (ln & 7) == 0 ? AUX + 1280 + (wave * 8 + (ln >> 3)) * 4 : AUX + 24576 + wave * 2048 + 1536 + ln * 4, vStR = AUX + 1280 + l15 * 8;
    const unsigned sWw = 16 * wave * 7168 + slice * 384;
    if (kind == 3) { asm volatile(R"(
Lcode_g3_b:
s_mov_b64 s[36:37],%0
s_mov_b32 s38,111193216
s_mov_b32 s39,0x00020000
s_mov_b32 s54,%1
s_mov_b32 s55,%2
s_mov_b32 s56,%3
s_mov_b32 s58,%4
s_mov_b32 s83,%5
s_mov_b32 s82,%6
s_mov_b32 s84,%7
v_mov_b32 v0,%8
v_mov_b32 v2,%9
v_mov_b32 v3,%10
v_mov_b32 v4,%11
v_mov_b32 v6,%12
v_mov_b32 v7,%13
v_mov_b32 v8,%14
v_mov_b32 v9,%15
v_mov_b32 v10,%16
v_mov_b32 v11,%17
v_mov_b32 v28,%18
v_mov_b32 v29,%19
v_mov_b32 v30,%20
v_mov_b32 v31,%21
v_mov_b32 v32,%22
s_mov_b64 s[40:41],%23
s_mov_b32 s42,7340032
s_mov_b32 s43,0x00020000
s_lshl_b32 s88,s54,11
s_mov_b32 s69,111193088
buffer_load_dword v5,off,s[36:39],s69 sc1
s_add_u32 m0,s88,0
s_add_u32 s61,s82,0
buffer_load_dwordx4 v28,s[40:43],s61 offen offset:0  lds
s_add_u32 m0,s88,1024
s_nop 0
buffer_load_dwordx4 v29,s[40:43],s61 offen offset:0  lds
s_add_u32 m0,s88,8064
s_nop 0
buffer_load_dwordx4 v28,s[40:43],s61 offen offset:128  lds
s_add_u32 m0,s88,9088
s_nop 0
buffer_load_dwordx4 v29,s[40:43],s61 offen offset:128  lds
s_add_u32 m0,s88,16128
s_nop 0
buffer_load_dwordx4 v28,s[40:43],s61 offen offset:256  lds
s_add_u32 m0,s88,17152
s_nop 0
buffer_load_dwordx4 v29,s[40:43],s61 offen offset:256  lds
s_add_u32 m0,s88,24576
s_add_u32 s61,s82,458752
buffer_load_dwordx4 v28,s[40:43],s61 offen offset:0  lds
s_add_u32 m0,s88,25600
s_nop 0
buffer_load_dwordx4 v29,s[40:43],s61 offen offset:0  lds
s_add_u32 m0,s88,32640
s_nop 0
buffer_load_dwordx4 v28,s[40:43],s61 offen offset:128  lds
s_add_u32 m0,s88,33664
s_nop 0
buffer_load_dwordx4 v29,s[40:43],s61 offen offset:128  lds
s_add_u32 m0,s88,40704
s_nop 0
buffer_load_dwordx4 v28,s[40:43],s61 offen offset:256  lds
s_add_u32 m0,s88,41728
s_nop 0
buffer_load_dwordx4 v29,s[40:43],s61 offen offset:256  lds
s_add_u32 m0,s88,49152
s_add_u32 s61,s82,917504
buffer_load_dwordx4 v28,s[40:43],s61 offen offset:0  lds
s_add_u32 m0,s88,50176
s_nop 0
buffer_load_dwordx4 v29,s[40:43],s61 offen offset:0  lds
s_add_u32 m0,s88,57216
s_nop 0
buffer_load_dwordx4 v28,s[40:43],s61 offen offset:128  lds
s_add_u32 m0,s88,58240
s_nop 0
buffer_load_dwordx4 v29,s[40:43],s61 offen offset:128  lds
s_add_u32 m0,s88,65280
s_nop 0
buffer_load_dwordx4 v28,s[40:43],s61 offen offset:256  lds
s_add_u32 m0,s88,66304
s_nop 0
buffer_load_dwordx4 v29,s[40:43],s61 offen offset:256  lds
s_add_u32 m0,s88,73728
s_add_u32 s61,s82,1376256
buffer_load_dwordx4 v28,s[40:43],s61 offen offset:0  lds
s_add_u32 m0,s88,74752
s_nop 0
buffer_load_dwordx4 v29,s[40:43],s61 offen offset:0  lds
s_add_u32 m0,s88,81792
s_nop 0
buffer_load_dwordx4 v28,s[40:43],s61 offen offset:128  lds
s_add_u32 m0,s88,82816
s_nop 0
buffer_load_dwordx4 v29,s[40:43],s61 offen offset:128  lds
s_add_u32 m0,s88,89856
s_nop 0
buffer_load_dwordx4 v28,s[40:43],s61 offen offset:256  lds
s_add_u32 m0,s88,90880
s_nop 0
buffer_load_dwordx4 v29,s[40:43],s61 offen offset:256  lds
v_add_u32 v40,49152,v30
v_add_u32 v41,49152,v31
v_add_u32 v42,49152,v32
v_add_u32 v43,98304,v30
v_add_u32 v44,98304,v31
v_add_u32 v45,98304,v32
v_mov_b32 v84,0x3f803f80
v_mov_b32 v85,0x3f803f80
v_mov_b32 v86,0x3f803f80
v_mov_b32 v87,0x3f803f80
s_waitcnt vmcnt(18)
s_barrier
s_add_u32 m0,s88,98304
s_add_u32 s61,s82,1835008
buffer_load_dwordx4 v28,s[40:43],s61 offen offset:0  lds
s_add_u32 m0,s88,99328
ds_read_b64_tr_b16 a[0:1],v30 offset:0
buffer_load_dwordx4 v29,s[40:43],s61 offen offset:0  lds
s_add_u32 m0,s88,106368
ds_read_b64_tr_b16 a[2:3],v30 offset:512
buffer_load_dwordx4 v28,s[40:43],s61 offen offset:128  lds
s_add_u32 m0,s88,107392
ds_read_b64_tr_b16 a[4:5],v31 offset:0
buffer_load_dwordx4 v29,s[40:43],s61 offen offset:128  lds
s_add_u32 m0,s88,114432
ds_read_b64_tr_b16 a[6:7],v31 offset:512
buffer_load_dwordx4 v28,s[40:43],s61 offen offset:256  lds
s_add_u32 m0,s88,115456
ds_read_b64_tr_b16 a[8:9],v32 offset:0
buffer_load_dwordx4 v29,s[40:43],s61 offen offset:256  lds
ds_read_b64_tr_b16 a[10:11],v32 offset:512
ds_read_b64_tr_b16 a[12:13],v30 offset:4096
ds_read_b64_tr_b16 a[14:15],v30 offset:4608
ds_read_b64_tr_b16 a[16:17],v31 offset:4096
ds_read_b64_tr_b16 a[18:19],v31 offset:4608
ds_read_b64_tr_b16 a[20:21],v32 offset:4096
ds_read_b64_tr_b16 a[22:23],v32 offset:4608
s_waitcnt vmcnt(18)
s_barrier
s_add_u32 m0,s88,122880
s_add_u32 s61,s82,2293760
buffer_load_dwordx4 v28,s[40:43],s61 offen offset:0  lds
s_add_u32 m0,s88,123904
ds_read_b64_tr_b16 a[24:25],v30 offset:24576
buffer_load_dwordx4 v29,s[40:43],s61 offen offset:0  lds
s_add_u32 m0,s88,130944
ds_read_b64_tr_b16 a[26:27],v30 offset:25088
buffer_load_dwordx4 v28,s[40:43],s61 offen offset:128  lds
s_add_u32 m0,s88,131968
ds_read_b64_tr_b16 a[28:29],v31 offset:24576
buffer_load_dwordx4 v29,s[40:43],s61 offen offset:128  lds
s_add_u32 m0,s88,139008
ds_read_b64_tr_b16 a[30:31],v31 offset:25088
buffer_load_dwordx4 v28,s[40:43],s61 offen offset:256  lds
s_add_u32 m0,s88,140032
ds_read_b64_tr_b16 a[32:33],v32 offset:24576
buffer_load_dwordx4 v29,s[40:43],s61 offen offset:256  lds
ds_read_b64_tr_b16 a[34:35],v32 offset:25088
ds_read_b64_tr_b16 a[36:37],v30 offset:28672
ds_read_b64_tr_b16 a[38:39],v30 offset:29184
ds_read_b64_tr_b16 a[40:41],v31 offset:28672
ds_read_b64_tr_b16 a[42:43],v31 offset:29184
ds_read_b64_tr_b16 a[44:45],v32 offset:28672
ds_read_b64_tr_b16 a[46:47],v32 offset:29184
s_waitcnt lgkmcnt(12)
v_mfma_f32_16x16x32_bf16 v[16:19],a[0:3],v[84:87],0
v_mfma_f32_16x16x32_bf16 v[20:23],a[4:7],v[84:87],0
v_mfma_f32_16x16x32_bf16 v[24:27],a[8:11],v[84:87],0
v_mfma_f32_16x16x32_bf16 v[16:19],a[12:15],v[84:87],v[16:19]
v_mfma_f32_16x16x32_bf16 v[20:23],a[16:19],v[84:87],v[20:23]
v_mfma_f32_16x16x32_bf16 v[24:27],a[20:23],v[84:87],v[24:27]
s_waitcnt vmcnt(18)
s_barrier
s_add_u32 m0,s88,0
s_add_u32 s61,s82,2752512
buffer_load_dwordx4 v28,s[40:43],s61 offen offset:0  lds
s_add_u32 m0,s88,1024
ds_read_b64_tr_b16 a[48:49],v40 offset:0
buffer_load_dwordx4 v29,s[40:43],s61 offen offset:0  lds
s_add_u32 m0,s88,8064
ds_read_b64_tr_b16 a[50:51],v40 offset:512
buffer_load_dwordx4 v28,s[40:43],s61 offen offset:128  lds
s_add_u32 m0,s88,9088
ds_read_b64_tr_b16 a[52:53],v41 offset:0
buffer_load_dwordx4 v29,s[40:43],s61 offen offset:128  lds
s_add_u32 m0,s88,16128
ds_read_b64_tr_b16 a[54:55],v41 offset:512
buffer_load_dwordx4 v28,s[40:43],s61 offen offset:256  lds
s_add_u32 m0,s88,17152
ds_read_b64_tr_b16 a[56:57],v42 offset:0
buffer_load_dwordx4 v29,s[40:43],s61 offen offset:256  lds
ds_read_b64_tr_b16 a[58:59],v42 offset:512
ds_read_b64_tr_b16 a[60:61],v40 offset:4096
ds_read_b64_tr_b16 a[62:63],v40 offset:4608
ds_read_b64_tr_b16 a[64:65],v41 offset:4096
ds_read_b64_tr_b16 a[66:67],v41 offset:4608)"
R"(
ds_read_b64_tr_b16 a[68:69],v42 offset:4096
ds_read_b64_tr_b16 a[70:71],v42 offset:4608
s_waitcnt lgkmcnt(12)
v_mfma_f32_16x16x32_bf16 v[16:19],a[24:27],v[84:87],v[16:19]
v_mfma_f32_16x16x32_bf16 v[20:23],a[28:31],v[84:87],v[20:23]
v_mfma_f32_16x16x32_bf16 v[24:27],a[32:35],v[84:87],v[24:27]
v_mfma_f32_16x16x32_bf16 v[16:19],a[36:39],v[84:87],v[16:19]
v_mfma_f32_16x16x32_bf16 v[20:23],a[40:43],v[84:87],v[20:23]
v_mfma_f32_16x16x32_bf16 v[24:27],a[44:47],v[84:87],v[24:27]
s_waitcnt vmcnt(18)
s_barrier
s_add_u32 m0,s88,24576
s_add_u32 s61,s82,3211264
buffer_load_dwordx4 v28,s[40:43],s61 offen offset:0  lds
s_add_u32 m0,s88,25600
ds_read_b64_tr_b16 a[72:73],v40 offset:24576
buffer_load_dwordx4 v29,s[40:43],s61 offen offset:0  lds
s_add_u32 m0,s88,32640
ds_read_b64_tr_b16 a[74:75],v40 offset:25088
buffer_load_dwordx4 v28,s[40:43],s61 offen offset:128  lds
s_add_u32 m0,s88,33664
ds_read_b64_tr_b16 a[76:77],v41 offset:24576
buffer_load_dwordx4 v29,s[40:43],s61 offen offset:128  lds
s_add_u32 m0,s88,40704
ds_read_b64_tr_b16 a[78:79],v41 offset:25088
buffer_load_dwordx4 v28,s[40:43],s61 offen offset:256  lds
s_add_u32 m0,s88,41728
ds_read_b64_tr_b16 a[80:81],v42 offset:24576
buffer_load_dwordx4 v29,s[40:43],s61 offen offset:256  lds
ds_read_b64_tr_b16 a[82:83],v42 offset:25088
ds_read_b64_tr_b16 a[84:85],v40 offset:28672
ds_read_b64_tr_b16 a[86:87],v40 offset:29184
ds_read_b64_tr_b16 a[88:89],v41 offset:28672
ds_read_b64_tr_b16 a[90:91],v41 offset:29184
ds_read_b64_tr_b16 a[92:93],v42 offset:28672
ds_read_b64_tr_b16 a[94:95],v42 offset:29184
s_waitcnt lgkmcnt(12)
v_mfma_f32_16x16x32_bf16 v[16:19],a[48:51],v[84:87],v[16:19]
v_mfma_f32_16x16x32_bf16 v[20:23],a[52:55],v[84:87],v[20:23]
v_mfma_f32_16x16x32_bf16 v[24:27],a[56:59],v[84:87],v[24:27]
v_mfma_f32_16x16x32_bf16 v[16:19],a[60:63],v[84:87],v[16:19]
v_mfma_f32_16x16x32_bf16 v[20:23],a[64:67],v[84:87],v[20:23]
v_mfma_f32_16x16x32_bf16 v[24:27],a[68:71],v[84:87],v[24:27]
s_waitcnt vmcnt(18)
s_barrier
s_add_u32 m0,s88,49152
s_add_u32 s61,s82,3670016
buffer_load_dwordx4 v28,s[40:43],s61 offen offset:0  lds
s_add_u32 m0,s88,50176
ds_read_b64_tr_b16 a[96:97],v43 offset:0
buffer_load_dwordx4 v29,s[40:43],s61 offen offset:0  lds
s_add_u32 m0,s88,57216
ds_read_b64_tr_b16 a[98:99],v43 offset:512
buffer_load_dwordx4 v28,s[40:43],s61 offen offset:128  lds
s_add_u32 m0,s88,58240
ds_read_b64_tr_b16 a[100:101],v44 offset:0
buffer_load_dwordx4 v29,s[40:43],s61 offen offset:128  lds
s_add_u32 m0,s88,65280
ds_read_b64_tr_b16 a[102:103],v44 offset:512
buffer_load_dwordx4 v28,s[40:43],s61 offen offset:256  lds
s_add_u32 m0,s88,66304
ds_read_b64_tr_b16 a[104:105],v45 offset:0
buffer_load_dwordx4 v29,s[40:43],s61 offen offset:256  lds
ds_read_b64_tr_b16 a[106:107],v45 offset:512
ds_read_b64_tr_b16 a[108:109],v43 offset:4096
ds_read_b64_tr_b16 a[110:111],v43 offset:4608
ds_read_b64_tr_b16 a[112:113],v44 offset:4096
ds_read_b64_tr_b16 a[114:115],v44 offset:4608
ds_read_b64_tr_b16 a[116:117],v45 offset:4096
ds_read_b64_tr_b16 a[118:119],v45 offset:4608
s_waitcnt lgkmcnt(12)
v_mfma_f32_16x16x32_bf16 v[16:19],a[72:75],v[84:87],v[16:19]
v_mfma_f32_16x16x32_bf16 v[20:23],a[76:79],v[84:87],v[20:23]
v_mfma_f32_16x16x32_bf16 v[24:27],a[80:83],v[84:87],v[24:27]
v_mfma_f32_16x16x32_bf16 v[16:19],a[84:87],v[84:87],v[16:19]
v_mfma_f32_16x16x32_bf16 v[20:23],a[88:91],v[84:87],v[20:23]
v_mfma_f32_16x16x32_bf16 v[24:27],a[92:95],v[84:87],v[24:27]
s_waitcnt vmcnt(18)
s_barrier
s_add_u32 m0,s88,73728
s_add_u32 s61,s82,4128768
buffer_load_dwordx4 v28,s[40:43],s61 offen offset:0  lds
s_add_u32 m0,s88,74752
ds_read_b64_tr_b16 a[120:121],v43 offset:24576
buffer_load_dwordx4 v29,s[40:43],s61 offen offset:0  lds
s_add_u32 m0,s88,81792
ds_read_b64_tr_b16 a[122:123],v43 offset:25088
buffer_load_dwordx4 v28,s[40:43],s61 offen offset:128  lds
s_add_u32 m0,s88,82816
ds_read_b64_tr_b16 a[124:125],v44 offset:24576
buffer_load_dwordx4 v29,s[40:43],s61 offen offset:128  lds
s_add_u32 m0,s88,89856
ds_read_b64_tr_b16 a[126:127],v44 offset:25088
buffer_load_dwordx4 v28,s[40:43],s61 offen offset:256  lds
s_add_u32 m0,s88,90880
ds_read_b64_tr_b16 a[128:129],v45 offset:24576
buffer_load_dwordx4 v29,s[40:43],s61 offen offset:256  lds
ds_read_b64_tr_b16 a[130:131],v45 offset:25088
ds_read_b64_tr_b16 a[132:133],v43 offset:28672
ds_read_b64_tr_b16 a[134:135],v43 offset:29184
ds_read_b64_tr_b16 a[136:137],v44 offset:28672
ds_read_b64_tr_b16 a[138:139],v44 offset:29184
ds_read_b64_tr_b16 a[140:141],v45 offset:28672
ds_read_b64_tr_b16 a[142:143],v45 offset:29184
s_waitcnt lgkmcnt(12)
v_mfma_f32_16x16x32_bf16 v[16:19],a[96:99],v[84:87],v[16:19]
v_mfma_f32_16x16x32_bf16 v[20:23],a[100:103],v[84:87],v[20:23]
v_mfma_f32_16x16x32_bf16 v[24:27],a[104:107],v[84:87],v[24:27]
v_mfma_f32_16x16x32_bf16 v[16:19],a[108:111],v[84:87],v[16:19]
v_mfma_f32_16x16x32_bf16 v[20:23],a[112:115],v[84:87],v[20:23]
v_mfma_f32_16x16x32_bf16 v[24:27],a[116:119],v[84:87],v[24:27]
s_waitcnt vmcnt(18)
s_barrier
s_add_u32 m0,s88,98304
s_add_u32 s61,s82,4587520
buffer_load_dwordx4 v28,s[40:43],s61 offen offset:0  lds
s_add_u32 m0,s88,99328
ds_read_b64_tr_b16 a[144:145],v30 offset:0
buffer_load_dwordx4 v29,s[40:43],s61 offen offset:0  lds
s_add_u32 m0,s88,106368
ds_read_b64_tr_b16 a[146:147],v30 offset:512
buffer_load_dwordx4 v28,s[40:43],s61 offen offset:128  lds
s_add_u32 m0,s88,107392
ds_read_b64_tr_b16 a[148:149],v31 offset:0
buffer_load_dwordx4 v29,s[40:43],s61 offen offset:128  lds
s_add_u32 m0,s88,114432
ds_read_b64_tr_b16 a[150:151],v31 offset:512
buffer_load_dwordx4 v28,s[40:43],s61 offen offset:256  lds
s_add_u32 m0,s88,115456
ds_read_b64_tr_b16 a[152:153],v32 offset:0
buffer_load_dwordx4 v29,s[40:43],s61 offen offset:256  lds
ds_read_b64_tr_b16 a[154:155],v32 offset:512
ds_read_b64_tr_b16 a[156:157],v30 offset:4096
ds_read_b64_tr_b16 a[158:159],v30 offset:4608
ds_read_b64_tr_b16 a[160:161],v31 offset:4096
ds_read_b64_tr_b16 a[162:163],v31 offset:4608
ds_read_b64_tr_b16 a[164:165],v32 offset:4096
ds_read_b64_tr_b16 a[166:167],v32 offset:4608
s_waitcnt lgkmcnt(12)
v_mfma_f32_16x16x32_bf16 v[16:19],a[120:123],v[84:87],v[16:19]
v_mfma_f32_16x16x32_bf16 v[20:23],a[124:127],v[84:87],v[20:23]
v_mfma_f32_16x16x32_bf16 v[24:27],a[128:131],v[84:87],v[24:27]
v_mfma_f32_16x16x32_bf16 v[16:19],a[132:135],v[84:87],v[16:19]
v_mfma_f32_16x16x32_bf16 v[20:23],a[136:139],v[84:87],v[20:23]
v_mfma_f32_16x16x32_bf16 v[24:27],a[140:143],v[84:87],v[24:27]
s_waitcnt vmcnt(18)
s_barrier
s_add_u32 m0,s88,122880
s_add_u32 s61,s82,5046272
buffer_load_dwordx4 v28,s[40:43],s61 offen offset:0  lds
s_add_u32 m0,s88,123904
ds_read_b64_tr_b16 a[168:169],v30 offset:24576
buffer_load_dwordx4 v29,s[40:43],s61 offen offset:0  lds
s_add_u32 m0,s88,130944
ds_read_b64_tr_b16 a[170:171],v30 offset:25088
buffer_load_dwordx4 v28,s[40:43],s61 offen offset:128  lds
s_add_u32 m0,s88,131968
ds_read_b64_tr_b16 a[172:173],v31 offset:24576
buffer_load_dwordx4 v29,s[40:43],s61 offen offset:128  lds
s_add_u32 m0,s88,139008
ds_read_b64_tr_b16 a[174:175],v31 offset:25088
buffer_load_dwordx4 v28,s[40:43],s61 offen offset:256  lds
s_add_u32 m0,s88,140032
ds_read_b64_tr_b16 a[176:177],v32 offset:24576
buffer_load_dwordx4 v29,s[40:43],s61 offen offset:256  lds
ds_read_b64_tr_b16 a[178:179],v32 offset:25088
ds_read_b64_tr_b16 a[180:181],v30 offset:28672
ds_read_b64_tr_b16 a[182:183],v30 offset:29184
ds_read_b64_tr_b16 a[184:185],v31 offset:28672
ds_read_b64_tr_b16 a[186:187],v31 offset:29184
ds_read_b64_tr_b16 a[188:189],v32 offset:28672
ds_read_b64_tr_b16 a[190:191],v32 offset:29184
s_waitcnt lgkmcnt(12)
v_mfma_f32_16x16x32_bf16 v[16:19],a[144:147],v[84:87],v[16:19]
v_mfma_f32_16x16x32_bf16 v[20:23],a[148:151],v[84:87],v[20:23]
v_mfma_f32_16x16x32_bf16 v[24:27],a[152:155],v[84:87],v[24:27]
v_mfma_f32_16x16x32_bf16 v[16:19],a[156:159],v[84:87],v[16:19]
v_mfma_f32_16x16x32_bf16 v[20:23],a[160:163],v[84:87],v[20:23]
v_mfma_f32_16x16x32_bf16 v[24:27],a[164:167],v[84:87],v[24:27]
s_waitcnt vmcnt(18)
s_barrier
s_add_u32 m0,s88,0
s_add_u32 s61,s82,5505024
buffer_load_dwordx4 v28,s[40:43],s61 offen offset:0  lds
s_add_u32 m0,s88,1024
ds_read_b64_tr_b16 a[192:193],v40 offset:0
buffer_load_dwordx4 v29,s[40:43],s61 offen offset:0  lds
s_add_u32 m0,s88,8064
ds_read_b64_tr_b16 a[194:195],v40 offset:512
buffer_load_dwordx4 v28,s[40:43],s61 offen offset:128  lds
s_add_u32 m0,s88,9088
ds_read_b64_tr_b16 a[196:197],v41 offset:0
buffer_load_dwordx4 v29,s[40:43],s61 offen offset:128  lds
s_add_u32 m0,s88,16128
ds_read_b64_tr_b16 a[198:199],v41 offset:512
buffer_load_dwordx4 v28,s[40:43],s61 offen offset:256  lds
s_add_u32 m0,s88,17152
ds_read_b64_tr_b16 a[200:201],v42 offset:0
buffer_load_dwordx4 v29,s[40:43],s61 offen offset:256  lds
ds_read_b64_tr_b16 a[202:203],v42 offset:512)"
R"(
ds_read_b64_tr_b16 a[204:205],v40 offset:4096
ds_read_b64_tr_b16 a[206:207],v40 offset:4608
ds_read_b64_tr_b16 a[208:209],v41 offset:4096
ds_read_b64_tr_b16 a[210:211],v41 offset:4608
ds_read_b64_tr_b16 a[212:213],v42 offset:4096
ds_read_b64_tr_b16 a[214:215],v42 offset:4608
s_waitcnt lgkmcnt(12)
v_mfma_f32_16x16x32_bf16 v[16:19],a[168:171],v[84:87],v[16:19]
v_mfma_f32_16x16x32_bf16 v[20:23],a[172:175],v[84:87],v[20:23]
v_mfma_f32_16x16x32_bf16 v[24:27],a[176:179],v[84:87],v[24:27]
v_mfma_f32_16x16x32_bf16 v[16:19],a[180:183],v[84:87],v[16:19]
v_mfma_f32_16x16x32_bf16 v[20:23],a[184:187],v[84:87],v[20:23]
v_mfma_f32_16x16x32_bf16 v[24:27],a[188:191],v[84:87],v[24:27]
s_waitcnt vmcnt(18)
s_barrier
s_add_u32 m0,s88,24576
s_add_u32 s61,s82,5963776
buffer_load_dwordx4 v28,s[40:43],s61 offen offset:0  lds
s_add_u32 m0,s88,25600
ds_read_b64_tr_b16 a[216:217],v40 offset:24576
buffer_load_dwordx4 v29,s[40:43],s61 offen offset:0  lds
s_add_u32 m0,s88,32640
ds_read_b64_tr_b16 a[218:219],v40 offset:25088
buffer_load_dwordx4 v28,s[40:43],s61 offen offset:128  lds
s_add_u32 m0,s88,33664
ds_read_b64_tr_b16 a[220:221],v41 offset:24576
buffer_load_dwordx4 v29,s[40:43],s61 offen offset:128  lds
s_add_u32 m0,s88,40704
ds_read_b64_tr_b16 a[222:223],v41 offset:25088
buffer_load_dwordx4 v28,s[40:43],s61 offen offset:256  lds
s_add_u32 m0,s88,41728
ds_read_b64_tr_b16 a[224:225],v42 offset:24576
buffer_load_dwordx4 v29,s[40:43],s61 offen offset:256  lds
ds_read_b64_tr_b16 a[226:227],v42 offset:25088
ds_read_b64_tr_b16 a[228:229],v40 offset:28672
ds_read_b64_tr_b16 a[230:231],v40 offset:29184
ds_read_b64_tr_b16 a[232:233],v41 offset:28672
ds_read_b64_tr_b16 a[234:235],v41 offset:29184
ds_read_b64_tr_b16 a[236:237],v42 offset:28672
ds_read_b64_tr_b16 a[238:239],v42 offset:29184
s_waitcnt lgkmcnt(12)
v_mfma_f32_16x16x32_bf16 v[16:19],a[192:195],v[84:87],v[16:19]
v_mfma_f32_16x16x32_bf16 v[20:23],a[196:199],v[84:87],v[20:23]
v_mfma_f32_16x16x32_bf16 v[24:27],a[200:203],v[84:87],v[24:27]
v_mfma_f32_16x16x32_bf16 v[16:19],a[204:207],v[84:87],v[16:19]
v_mfma_f32_16x16x32_bf16 v[20:23],a[208:211],v[84:87],v[20:23]
v_mfma_f32_16x16x32_bf16 v[24:27],a[212:215],v[84:87],v[24:27]
s_waitcnt vmcnt(18)
s_barrier
s_add_u32 m0,s88,49152
s_add_u32 s61,s82,6422528
buffer_load_dwordx4 v28,s[40:43],s61 offen offset:0  lds
s_add_u32 m0,s88,50176
ds_read_b64_tr_b16 a[240:241],v43 offset:0
buffer_load_dwordx4 v29,s[40:43],s61 offen offset:0  lds
s_add_u32 m0,s88,57216
ds_read_b64_tr_b16 a[242:243],v43 offset:512
buffer_load_dwordx4 v28,s[40:43],s61 offen offset:128  lds
s_add_u32 m0,s88,58240
ds_read_b64_tr_b16 a[244:245],v44 offset:0
buffer_load_dwordx4 v29,s[40:43],s61 offen offset:128  lds
s_add_u32 m0,s88,65280
ds_read_b64_tr_b16 a[246:247],v44 offset:512
buffer_load_dwordx4 v28,s[40:43],s61 offen offset:256  lds
s_add_u32 m0,s88,66304
ds_read_b64_tr_b16 a[248:249],v45 offset:0
buffer_load_dwordx4 v29,s[40:43],s61 offen offset:256  lds
ds_read_b64_tr_b16 a[250:251],v45 offset:512
ds_read_b64_tr_b16 a[252:253],v43 offset:4096
ds_read_b64_tr_b16 a[254:255],v43 offset:4608
ds_read_b64_tr_b16 v[128:129],v44 offset:4096
ds_read_b64_tr_b16 v[130:131],v44 offset:4608
ds_read_b64_tr_b16 v[132:133],v45 offset:4096
ds_read_b64_tr_b16 v[134:135],v45 offset:4608
s_waitcnt lgkmcnt(12)
v_mfma_f32_16x16x32_bf16 v[16:19],a[216:219],v[84:87],v[16:19]
v_mfma_f32_16x16x32_bf16 v[20:23],a[220:223],v[84:87],v[20:23]
v_mfma_f32_16x16x32_bf16 v[24:27],a[224:227],v[84:87],v[24:27]
v_mfma_f32_16x16x32_bf16 v[16:19],a[228:231],v[84:87],v[16:19]
v_mfma_f32_16x16x32_bf16 v[20:23],a[232:235],v[84:87],v[20:23]
v_mfma_f32_16x16x32_bf16 v[24:27],a[236:239],v[84:87],v[24:27]
s_waitcnt vmcnt(18)
s_barrier
s_add_u32 m0,s88,73728
s_add_u32 s61,s82,6881280
buffer_load_dwordx4 v28,s[40:43],s61 offen offset:0  lds
s_add_u32 m0,s88,74752
ds_read_b64_tr_b16 v[136:137],v43 offset:24576
buffer_load_dwordx4 v29,s[40:43],s61 offen offset:0  lds
s_add_u32 m0,s88,81792
ds_read_b64_tr_b16 v[138:139],v43 offset:25088
buffer_load_dwordx4 v28,s[40:43],s61 offen offset:128  lds
s_add_u32 m0,s88,82816
ds_read_b64_tr_b16 v[140:141],v44 offset:24576
buffer_load_dwordx4 v29,s[40:43],s61 offen offset:128  lds
s_add_u32 m0,s88,89856
ds_read_b64_tr_b16 v[142:143],v44 offset:25088
buffer_load_dwordx4 v28,s[40:43],s61 offen offset:256  lds
s_add_u32 m0,s88,90880
ds_read_b64_tr_b16 v[144:145],v45 offset:24576
buffer_load_dwordx4 v29,s[40:43],s61 offen offset:256  lds
ds_read_b64_tr_b16 v[146:147],v45 offset:25088
ds_read_b64_tr_b16 v[148:149],v43 offset:28672
ds_read_b64_tr_b16 v[150:151],v43 offset:29184
ds_read_b64_tr_b16 v[152:153],v44 offset:28672
ds_read_b64_tr_b16 v[154:155],v44 offset:29184
ds_read_b64_tr_b16 v[156:157],v45 offset:28672
ds_read_b64_tr_b16 v[158:159],v45 offset:29184
s_waitcnt lgkmcnt(12)
v_mfma_f32_16x16x32_bf16 v[16:19],a[240:243],v[84:87],v[16:19]
v_mfma_f32_16x16x32_bf16 v[20:23],a[244:247],v[84:87],v[20:23]
v_mfma_f32_16x16x32_bf16 v[24:27],a[248:251],v[84:87],v[24:27]
v_mfma_f32_16x16x32_bf16 v[16:19],a[252:255],v[84:87],v[16:19]
v_mfma_f32_16x16x32_bf16 v[20:23],v[128:131],v[84:87],v[20:23]
v_mfma_f32_16x16x32_bf16 v[24:27],v[132:135],v[84:87],v[24:27]
s_waitcnt vmcnt(18)
s_barrier
ds_read_b64_tr_b16 v[160:161],v30 offset:0
ds_read_b64_tr_b16 v[162:163],v30 offset:512
ds_read_b64_tr_b16 v[164:165],v31 offset:0
ds_read_b64_tr_b16 v[166:167],v31 offset:512
ds_read_b64_tr_b16 v[168:169],v32 offset:0
ds_read_b64_tr_b16 v[170:171],v32 offset:512
ds_read_b64_tr_b16 v[172:173],v30 offset:4096
ds_read_b64_tr_b16 v[174:175],v30 offset:4608
ds_read_b64_tr_b16 v[176:177],v31 offset:4096
ds_read_b64_tr_b16 v[178:179],v31 offset:4608
ds_read_b64_tr_b16 v[180:181],v32 offset:4096
ds_read_b64_tr_b16 v[182:183],v32 offset:4608
s_waitcnt lgkmcnt(12)
v_mfma_f32_16x16x32_bf16 v[16:19],v[136:139],v[84:87],v[16:19]
v_mfma_f32_16x16x32_bf16 v[20:23],v[140:143],v[84:87],v[20:23]
v_mfma_f32_16x16x32_bf16 v[24:27],v[144:147],v[84:87],v[24:27]
v_mfma_f32_16x16x32_bf16 v[16:19],v[148:151],v[84:87],v[16:19]
v_mfma_f32_16x16x32_bf16 v[20:23],v[152:155],v[84:87],v[20:23]
v_mfma_f32_16x16x32_bf16 v[24:27],v[156:159],v[84:87],v[24:27]
s_waitcnt vmcnt(12)
s_barrier
ds_read_b64_tr_b16 v[184:185],v30 offset:24576
ds_read_b64_tr_b16 v[186:187],v30 offset:25088
ds_read_b64_tr_b16 v[188:189],v31 offset:24576
ds_read_b64_tr_b16 v[190:191],v31 offset:25088
ds_read_b64_tr_b16 v[192:193],v32 offset:24576
ds_read_b64_tr_b16 v[194:195],v32 offset:25088
ds_read_b64_tr_b16 v[196:197],v30 offset:28672
ds_read_b64_tr_b16 v[198:199],v30 offset:29184
ds_read_b64_tr_b16 v[200:201],v31 offset:28672
ds_read_b64_tr_b16 v[202:203],v31 offset:29184
ds_read_b64_tr_b16 v[204:205],v32 offset:28672
ds_read_b64_tr_b16 v[206:207],v32 offset:29184
s_waitcnt lgkmcnt(12)
v_mfma_f32_16x16x32_bf16 v[16:19],v[160:163],v[84:87],v[16:19]
v_mfma_f32_16x16x32_bf16 v[20:23],v[164:167],v[84:87],v[20:23]
v_mfma_f32_16x16x32_bf16 v[24:27],v[168:171],v[84:87],v[24:27]
v_mfma_f32_16x16x32_bf16 v[16:19],v[172:175],v[84:87],v[16:19]
v_mfma_f32_16x16x32_bf16 v[20:23],v[176:179],v[84:87],v[20:23]
v_mfma_f32_16x16x32_bf16 v[24:27],v[180:183],v[84:87],v[24:27]
s_waitcnt vmcnt(6)
s_barrier
ds_read_b64_tr_b16 v[208:209],v40 offset:0
ds_read_b64_tr_b16 v[210:211],v40 offset:512
ds_read_b64_tr_b16 v[212:213],v41 offset:0
ds_read_b64_tr_b16 v[214:215],v41 offset:512
ds_read_b64_tr_b16 v[216:217],v42 offset:0
ds_read_b64_tr_b16 v[218:219],v42 offset:512
ds_read_b64_tr_b16 v[220:221],v40 offset:4096
ds_read_b64_tr_b16 v[222:223],v40 offset:4608
ds_read_b64_tr_b16 v[224:225],v41 offset:4096
ds_read_b64_tr_b16 v[226:227],v41 offset:4608
ds_read_b64_tr_b16 v[228:229],v42 offset:4096
ds_read_b64_tr_b16 v[230:231],v42 offset:4608
s_waitcnt lgkmcnt(12)
v_mfma_f32_16x16x32_bf16 v[16:19],v[184:187],v[84:87],v[16:19]
v_mfma_f32_16x16x32_bf16 v[20:23],v[188:191],v[84:87],v[20:23]
v_mfma_f32_16x16x32_bf16 v[24:27],v[192:195],v[84:87],v[24:27]
v_mfma_f32_16x16x32_bf16 v[16:19],v[196:199],v[84:87],v[16:19]
v_mfma_f32_16x16x32_bf16 v[20:23],v[200:203],v[84:87],v[20:23]
v_mfma_f32_16x16x32_bf16 v[24:27],v[204:207],v[84:87],v[24:27]
s_waitcnt vmcnt(0)
s_barrier
ds_read_b64_tr_b16 v[232:233],v40 offset:24576
ds_read_b64_tr_b16 v[234:235],v40 offset:25088
ds_read_b64_tr_b16 v[236:237],v41 offset:24576
ds_read_b64_tr_b16 v[238:239],v41 offset:25088
ds_read_b64_tr_b16 v[240:241],v42 offset:24576
ds_read_b64_tr_b16 v[242:243],v42 offset:25088
ds_read_b64_tr_b16 v[244:245],v40 offset:28672
ds_read_b64_tr_b16 v[246:247],v40 offset:29184
ds_read_b64_tr_b16 v[248:249],v41 offset:28672
ds_read_b64_tr_b16 v[250:251],v41 offset:29184
ds_read_b64_tr_b16 v[252:253],v42 offset:28672
ds_read_b64_tr_b16 v[254:255],v42 offset:29184
s_waitcnt lgkmcnt(12)
v_mfma_f32_16x16x32_bf16 v[16:19],v[208:211],v[84:87],v[16:19]
v_mfma_f32_16x16x32_bf16 v[20:23],v[212:215],v[84:87],v[20:23]
v_mfma_f32_16x16x32_bf16 v[24:27],v[216:219],v[84:87],v[24:27]
v_mfma_f32_16x16x32_bf16 v[16:19],v[220:223],v[84:87],v[16:19]
v_mfma_f32_16x16x32_bf16 v[20:23],v[224:227],v[84:87],v[20:23]
v_mfma_f32_16x16x32_bf16 v[24:27],v[228:231],v[84:87],v[24:27]
s_waitcnt lgkmcnt(0))"
R"(
v_mfma_f32_16x16x32_bf16 v[16:19],v[232:235],v[84:87],v[16:19]
v_mfma_f32_16x16x32_bf16 v[20:23],v[236:239],v[84:87],v[20:23]
v_mfma_f32_16x16x32_bf16 v[24:27],v[240:243],v[84:87],v[24:27]
v_mfma_f32_16x16x32_bf16 v[16:19],v[244:247],v[84:87],v[16:19]
v_mfma_f32_16x16x32_bf16 v[20:23],v[248:251],v[84:87],v[20:23]
v_mfma_f32_16x16x32_bf16 v[24:27],v[252:255],v[84:87],v[24:27]
s_waitcnt lgkmcnt(0)
s_barrier
v_readfirstlane_b32 s60,v5
s_nop 3
s_add_u32 s60,s60,1
s_cmp_eq_u32 s60,0
s_cselect_b32 s60,1,s60
Lg3_init_%=:
v_mov_b32 v5,s60
s_mov_b32 s59,536870912
s_add_u32 s57,s56,131072
s_mov_b32 s79,0
s_mov_b32 s75,0x3a800000
s_mov_b32 s76,0xc0135761
s_mov_b32 s77,0x3d372713
s_mov_b32 s44,s59
s_mov_b32 s45,s59
s_mov_b32 s46,s59
s_mov_b32 s47,s59
s_mov_b32 s48,s83
s_add_u32 s49,s48,s84
s_add_u32 s50,s49,s55
s_cmp_eq_u32 s50,s56
s_cselect_b32 s50,s57,s50
s_cmp_eq_u32 s50,s58
s_cselect_b32 s50,s59,s50
s_add_u32 s51,s50,s55
s_cmp_eq_u32 s51,s56
s_cselect_b32 s51,s57,s51
s_cmp_eq_u32 s51,s58
s_cselect_b32 s51,s59,s51
s_add_u32 s52,s51,s55
s_cmp_eq_u32 s52,s56
s_cselect_b32 s52,s57,s52
s_cmp_eq_u32 s52,s58
s_cselect_b32 s52,s59,s52
s_lshl_b32 s63,s54,12
s_add_u32 s63,s63,65536
s_add_u32 s64,s63,32768
s_lshl_b32 s88,s54,10
s_add_u32 s88,s88,131072
s_lshl_b32 s78,s54,11
s_add_u32 s78,s78,132608
v_add_u32 v1,16384,v0
v_mov_b32 v12,0x3727c5ac
s_getpc_b64 s[90:91]
Lg3_pc_%=:
s_mov_b32 s68,Lg3_d1w0_%=-Lg3_pc_%=
s_cmp_eq_u32 s54,1
s_cselect_b32 s68,Lg3_d1w1_%=-Lg3_pc_%=,s68
s_cmp_eq_u32 s54,2
s_cselect_b32 s68,Lg3_d1w2_%=-Lg3_pc_%=,s68
s_cmp_eq_u32 s54,3
s_cselect_b32 s68,Lg3_d1w3_%=-Lg3_pc_%=,s68
s_add_u32 s92,s90,s68
s_addc_u32 s93,s91,0
s_mov_b32 s68,Lg3_d0w0_%=-Lg3_pc_%=
s_cmp_eq_u32 s54,1
s_cselect_b32 s68,Lg3_d0w1_%=-Lg3_pc_%=,s68
s_cmp_eq_u32 s54,2
s_cselect_b32 s68,Lg3_d0w2_%=-Lg3_pc_%=,s68
s_cmp_eq_u32 s54,3
s_cselect_b32 s68,Lg3_d0w3_%=-Lg3_pc_%=,s68
s_add_u32 s90,s90,s68
s_addc_u32 s91,s91,0
s_mov_b32 s66,s59
s_mov_b32 s65,0
s_mov_b32 s62,s63
s_branch Lg3_slow0_%=
Lg3_d0w0_%=:
v_mfma_f32_16x16x32_bf16 v[28:31],a[0:3],v[52:55],0
s_mov_b32 m0,s62
ds_read_b128 v[72:75],v2 offset:5120
v_mul_f32 v116,s75,v112
v_mfma_f32_16x16x32_bf16 v[32:35],a[4:7],v[52:55],0
buffer_load_dwordx4 v0,s[36:39],s61 offen offset:0 sc1 lds
v_mfma_f32_16x16x32_bf16 v[36:39],a[8:11],v[52:55],0
s_mul_i32 s73,s47,7
v_fma_f32 v113,v113,s75,v12
v_fma_f32 v113,-v116,v116,v113
v_mfma_f32_16x16x32_bf16 v[28:31],a[12:15],v[56:59],v[28:31]
s_lshr_b32 s73,s73,1
ds_read_b128 v[76:79],v2 offset:6144
v_max_f32 v113,v113,v12
v_mfma_f32_16x16x32_bf16 v[32:35],a[16:19],v[56:59],v[32:35]
s_add_u32 s53,s52,s55
v_rsq_f32 v112,v113
v_mfma_f32_16x16x32_bf16 v[36:39],a[20:23],v[56:59],v[36:39]
s_waitcnt lgkmcnt(3)
buffer_load_dwordx4 v0,s[36:39],s61 offen offset:1024 sc1 lds
v_mfma_f32_16x16x32_bf16 v[28:31],a[24:27],v[60:63],v[28:31]
s_cmp_eq_u32 s53,s56
ds_read_b128 v[80:83],v2 offset:7168
v_mul_f32 v117,s76,v112
v_mfma_f32_16x16x32_bf16 v[32:35],a[28:31],v[60:63],v[32:35]
s_cselect_b32 s53,s57,s53
v_mul_f32 v118,v112,v112
v_mul_f32 v119,v113,v112
v_mfma_f32_16x16x32_bf16 v[36:39],a[32:35],v[60:63],v[36:39]
s_cmp_eq_u32 s53,s58
v_mul_f32 v118,s77,v118
v_mul_f32 v118,v118,v117
v_mfma_f32_16x16x32_bf16 v[28:31],a[36:39],v[64:67],v[28:31]
buffer_load_dwordx4 v0,s[36:39],s61 offen offset:2048 sc1 lds
v_mfma_f32_16x16x32_bf16 v[32:35],a[40:43],v[64:67],v[32:35]
s_cselect_b32 s53,s59,s53
ds_read_b128 v[52:55],v2 offset:8192
v_fma_f32 v40,-v116,v16,v40
v_mfma_f32_16x16x32_bf16 v[36:39],a[44:47],v[64:67],v[36:39]
s_waitcnt lgkmcnt(3)
v_fma_f32 v41,-v116,v17,v41
v_fma_f32 v42,-v116,v18,v42
v_mfma_f32_16x16x32_bf16 v[28:31],a[48:51],v[68:71],v[28:31]
s_cmp_ge_u32 s66,s59
ds_read_b128 v[56:59],v2 offset:9216
v_fma_f32 v43,-v116,v19,v43
v_mfma_f32_16x16x32_bf16 v[32:35],a[52:55],v[68:71],v[32:35]
buffer_load_dwordx4 v0,s[36:39],s61 offen offset:3072 sc1 lds
v_mfma_f32_16x16x32_bf16 v[36:39],a[56:59],v[68:71],v[36:39]
s_cselect_b32 s86,s49,s50
v_mul_f32 v84,v40,v40
v_mul_f32 v85,v41,v41
v_mfma_f32_16x16x32_bf16 v[28:31],a[60:63],v[72:75],v[28:31]
s_lshr_b32 s68,s86,5
ds_read_b128 v[60:63],v2 offset:10240
v_mul_f32 v86,v42,v42
v_mfma_f32_16x16x32_bf16 v[32:35],a[64:67],v[72:75],v[32:35]
s_add_u32 m0,s78,0
v_mul_f32 v87,v43,v43
v_fma_f32 v84,v118,v84,v117
v_mfma_f32_16x16x32_bf16 v[36:39],a[68:71],v[72:75],v[36:39]
s_waitcnt lgkmcnt(3)
v_mul_f32 v84,v40,v84
v_fma_f32 v85,v118,v85,v117
v_mfma_f32_16x16x32_bf16 v[28:31],a[72:75],v[76:79],v[28:31]
ds_read_b128 v[64:67],v2 offset:11264
v_mul_f32 v85,v41,v85
v_mfma_f32_16x16x32_bf16 v[32:35],a[76:79],v[76:79],v[32:35]
v_exp_f32 v84,v84
v_mfma_f32_16x16x32_bf16 v[36:39],a[80:83],v[76:79],v[36:39]
v_exp_f32 v85,v85
v_mfma_f32_16x16x32_bf16 v[28:31],a[84:87],v[80:83],v[28:31]
ds_read_b128 v[68:71],v2 offset:12288
v_fma_f32 v84,v84,v119,v119
v_mfma_f32_16x16x32_bf16 v[32:35],a[88:91],v[80:83],v[32:35]
v_rcp_f32 v84,v84
v_mfma_f32_16x16x32_bf16 v[36:39],a[92:95],v[80:83],v[36:39]
s_waitcnt lgkmcnt(3)
v_fma_f32 v85,v85,v119,v119
v_fma_f32 v86,v118,v86,v117
v_mfma_f32_16x16x32_bf16 v[28:31],a[96:99],v[52:55],v[28:31]
ds_read_b128 v[72:75],v2 offset:13312
v_mul_f32 v86,v42,v86
v_mfma_f32_16x16x32_bf16 v[32:35],a[100:103],v[52:55],v[32:35]
v_exp_f32 v86,v86
v_mfma_f32_16x16x32_bf16 v[36:39],a[104:107],v[52:55],v[36:39]
v_rcp_f32 v85,v85
v_mfma_f32_16x16x32_bf16 v[28:31],a[108:111],v[56:59],v[28:31]
ds_read_b128 v[76:79],v2 offset:14336
v_fma_f32 v86,v86,v119,v119
v_mfma_f32_16x16x32_bf16 v[32:35],a[112:115],v[56:59],v[32:35]
v_rcp_f32 v86,v86
v_mfma_f32_16x16x32_bf16 v[36:39],a[116:119],v[56:59],v[36:39]
s_waitcnt lgkmcnt(3)
v_fma_f32 v87,v118,v87,v117
v_mul_f32 v87,v43,v87
v_mfma_f32_16x16x32_bf16 v[28:31],a[120:123],v[60:63],v[28:31]
ds_read_b128 v[80:83],v2 offset:15360
v_mul_f32 v40,v40,v84
v_mfma_f32_16x16x32_bf16 v[32:35],a[124:127],v[60:63],v[32:35]
s_waitcnt vmcnt(6)
v_exp_f32 v87,v87
v_mfma_f32_16x16x32_bf16 v[36:39],a[128:131],v[60:63],v[36:39]
s_barrier
v_mfma_f32_16x16x32_bf16 v[28:31],a[132:135],v[64:67],v[28:31]
ds_read_b128 v[52:55],v2 offset:16384
v_fma_f32 v87,v87,v119,v119
v_mfma_f32_16x16x32_bf16 v[32:35],a[136:139],v[64:67],v[32:35]
v_rcp_f32 v87,v87
v_mfma_f32_16x16x32_bf16 v[36:39],a[140:143],v[64:67],v[36:39]
s_waitcnt lgkmcnt(3)
v_add_f32_dpp v104,v104,v104 quad_perm:[1,0,3,2] row_mask:0xf bank_mask:0xf
v_mul_f32 v41,v41,v85
v_mfma_f32_16x16x32_bf16 v[28:31],a[144:147],v[68:71],v[28:31]
ds_read_b128 v[56:59],v2 offset:17408
v_add_f32_dpp v104,v104,v104 quad_perm:[2,3,0,1] row_mask:0xf bank_mask:0xf
v_mfma_f32_16x16x32_bf16 v[32:35],a[148:151],v[68:71],v[32:35]
s_nop 0
v_add_f32_dpp v104,v104,v104 row_half_mirror row_mask:0xf bank_mask:0xf
v_mul_f32 v42,v42,v86
v_mfma_f32_16x16x32_bf16 v[36:39],a[152:155],v[68:71],v[36:39]
ds_write_b32 v10,v104 offset:0
v_mfma_f32_16x16x32_bf16 v[28:31],a[156:159],v[72:75],v[28:31]
ds_read_b128 v[60:63],v2 offset:18432)"
R"(
v_mul_f32 v43,v43,v87
v_mfma_f32_16x16x32_bf16 v[32:35],a[160:163],v[72:75],v[32:35]
buffer_load_dword v6,s[36:39],s68 offen offset:0 sc1 lds
v_mfma_f32_16x16x32_bf16 v[36:39],a[164:167],v[72:75],v[36:39]
s_waitcnt lgkmcnt(4)
v_cvt_pk_bf16_f32 v96,v40,v41
v_cvt_pk_bf16_f32 v97,v42,v43
v_mfma_f32_16x16x32_bf16 v[28:31],a[168:171],v[76:79],v[28:31]
s_add_u32 m0,s62,16384
ds_read_b128 v[64:67],v2 offset:19456
v_fma_f32 v44,-v116,v20,v44
v_mfma_f32_16x16x32_bf16 v[32:35],a[172:175],v[76:79],v[32:35]
buffer_load_dwordx4 v1,s[36:39],s61 offen offset:0 sc1 lds
v_mfma_f32_16x16x32_bf16 v[36:39],a[176:179],v[76:79],v[36:39]
s_lshr_b32 s72,s61,5
v_fma_f32 v45,-v116,v21,v45
v_fma_f32 v46,-v116,v22,v46
v_mfma_f32_16x16x32_bf16 v[28:31],a[180:183],v[80:83],v[28:31]
s_mov_b32 s62,s63
ds_read_b128 v[68:71],v2 offset:20480
v_fma_f32 v47,-v116,v23,v47
v_mfma_f32_16x16x32_bf16 v[32:35],a[184:187],v[80:83],v[32:35]
v_mul_f32 v88,v44,v44
v_mul_f32 v89,v45,v45
v_mfma_f32_16x16x32_bf16 v[36:39],a[188:191],v[80:83],v[36:39]
s_waitcnt lgkmcnt(4)
buffer_load_dwordx4 v1,s[36:39],s61 offen offset:1024 sc1 lds
v_mfma_f32_16x16x32_bf16 v[28:31],a[192:195],v[52:55],v[28:31]
ds_read_b128 v[72:75],v2 offset:21504
v_mul_f32 v90,v46,v46
v_mfma_f32_16x16x32_bf16 v[32:35],a[196:199],v[52:55],v[32:35]
v_mul_f32 v91,v47,v47
v_fma_f32 v88,v118,v88,v117
v_mfma_f32_16x16x32_bf16 v[36:39],a[200:203],v[52:55],v[36:39]
v_mul_f32 v88,v44,v88
v_fma_f32 v89,v118,v89,v117
v_mfma_f32_16x16x32_bf16 v[28:31],a[204:207],v[56:59],v[28:31]
buffer_load_dwordx4 v1,s[36:39],s61 offen offset:2048 sc1 lds
v_mfma_f32_16x16x32_bf16 v[32:35],a[208:211],v[56:59],v[32:35]
ds_read_b128 v[76:79],v2 offset:22528
v_mul_f32 v89,v45,v89
v_mfma_f32_16x16x32_bf16 v[36:39],a[212:215],v[56:59],v[36:39]
s_waitcnt lgkmcnt(3)
v_exp_f32 v88,v88
v_mfma_f32_16x16x32_bf16 v[28:31],a[216:219],v[60:63],v[28:31]
ds_read_b128 v[80:83],v2 offset:23552
v_fma_f32 v88,v88,v119,v119
v_mfma_f32_16x16x32_bf16 v[32:35],a[220:223],v[60:63],v[32:35]
s_waitcnt vmcnt(8)
v_exp_f32 v89,v89
v_mfma_f32_16x16x32_bf16 v[36:39],a[224:227],v[60:63],v[36:39]
s_barrier
v_mfma_f32_16x16x32_bf16 v[28:31],a[228:231],v[64:67],v[28:31]
buffer_load_dwordx4 v1,s[36:39],s61 offen offset:3072 sc1 lds
v_mfma_f32_16x16x32_bf16 v[32:35],a[232:235],v[64:67],v[32:35]
buffer_load_dword v108,v8,s[36:39],s72 offen offset:0 sc1
v_mfma_f32_16x16x32_bf16 v[36:39],a[236:239],v[64:67],v[36:39]
s_waitcnt lgkmcnt(2)
ds_read_b128 v[52:55],v2 offset:24576
v_fma_f32 v89,v89,v119,v119
v_mfma_f32_16x16x32_bf16 v[28:31],a[240:243],v[68:71],v[28:31]
ds_read_b128 v[56:59],v2 offset:25600
v_fma_f32 v90,v118,v90,v117
v_mfma_f32_16x16x32_bf16 v[32:35],a[244:247],v[68:71],v[32:35]
v_rcp_f32 v88,v88
v_mfma_f32_16x16x32_bf16 v[36:39],a[248:251],v[68:71],v[36:39]
v_rcp_f32 v89,v89
v_mfma_f32_16x16x32_bf16 v[28:31],a[252:255],v[72:75],v[28:31]
ds_read_b128 v[60:63],v2 offset:26624
v_mul_f32 v90,v46,v90
v_mfma_f32_16x16x32_bf16 v[32:35],v[128:131],v[72:75],v[32:35]
v_exp_f32 v90,v90
v_mfma_f32_16x16x32_bf16 v[36:39],v[132:135],v[72:75],v[36:39]
s_waitcnt lgkmcnt(3)
v_fma_f32 v90,v90,v119,v119
v_fma_f32 v91,v118,v91,v117
v_mfma_f32_16x16x32_bf16 v[28:31],v[136:139],v[76:79],v[28:31]
ds_read_b128 v[64:67],v2 offset:27648
v_mul_f32 v91,v47,v91
v_mfma_f32_16x16x32_bf16 v[32:35],v[140:143],v[76:79],v[32:35]
v_exp_f32 v91,v91
v_mfma_f32_16x16x32_bf16 v[36:39],v[144:147],v[76:79],v[36:39]
v_rcp_f32 v90,v90
v_mfma_f32_16x16x32_bf16 v[28:31],v[148:151],v[80:83],v[28:31]
ds_read_b128 v[68:71],v2 offset:28672
v_fma_f32 v91,v91,v119,v119
v_mfma_f32_16x16x32_bf16 v[32:35],v[152:155],v[80:83],v[32:35]
v_rcp_f32 v91,v91
v_mfma_f32_16x16x32_bf16 v[36:39],v[156:159],v[80:83],v[36:39]
s_waitcnt lgkmcnt(3)
v_mul_f32 v44,v44,v88
v_mul_f32 v45,v45,v89
v_mul_f32 v46,v46,v90
v_mul_f32 v47,v47,v91
v_cvt_pk_bf16_f32 v98,v44,v45
v_cvt_pk_bf16_f32 v99,v46,v47
v_mfma_f32_16x16x32_bf16 v[28:31],v[160:163],v[52:55],v[28:31]
buffer_store_dwordx4 v[96:99],v3,s[36:39],s73 offen offset:0 sc0 sc1
ds_read_b128 v[72:75],v2 offset:29696
v_fma_f32 v48,-v116,v24,v48
v_fma_f32 v49,-v116,v25,v49
v_mfma_f32_16x16x32_bf16 v[32:35],v[164:167],v[52:55],v[32:35]
v_fma_f32 v50,-v116,v26,v50
v_fma_f32 v51,-v116,v27,v51
v_mfma_f32_16x16x32_bf16 v[36:39],v[168:171],v[52:55],v[36:39]
v_mul_f32 v92,v48,v48
v_mul_f32 v93,v49,v49
v_mfma_f32_16x16x32_bf16 v[28:31],v[172:175],v[56:59],v[28:31]
ds_read_b128 v[76:79],v2 offset:30720
v_mul_f32 v94,v50,v50
v_mfma_f32_16x16x32_bf16 v[32:35],v[176:179],v[56:59],v[32:35]
v_mul_f32 v95,v51,v51
v_fma_f32 v92,v118,v92,v117
v_mfma_f32_16x16x32_bf16 v[36:39],v[180:183],v[56:59],v[36:39]
s_waitcnt lgkmcnt(3)
v_mul_f32 v92,v48,v92
v_fma_f32 v93,v118,v93,v117
v_mfma_f32_16x16x32_bf16 v[28:31],v[184:187],v[60:63],v[28:31]
ds_read_b128 v[80:83],v2 offset:31744
v_mul_f32 v93,v49,v93
v_mfma_f32_16x16x32_bf16 v[32:35],v[188:191],v[60:63],v[32:35]
s_waitcnt vmcnt(6)
v_exp_f32 v92,v92
v_mfma_f32_16x16x32_bf16 v[36:39],v[192:195],v[60:63],v[36:39]
s_barrier
v_mfma_f32_16x16x32_bf16 v[28:31],v[196:199],v[64:67],v[28:31]
ds_read_b128 v[52:55],v2 offset:32768
ds_read_b32 v13,v7 offset:0
v_mfma_f32_16x16x32_bf16 v[32:35],v[200:203],v[64:67],v[32:35]
ds_read_b64 v[112:113],v11 offset:0
v_fma_f32 v92,v92,v119,v119
v_mfma_f32_16x16x32_bf16 v[36:39],v[204:207],v[64:67],v[36:39]
s_waitcnt lgkmcnt(5)
v_exp_f32 v93,v93
v_mfma_f32_16x16x32_bf16 v[28:31],v[208:211],v[68:71],v[28:31]
ds_read_b128 v[56:59],v2 offset:33792
v_fma_f32 v93,v93,v119,v119
v_mfma_f32_16x16x32_bf16 v[32:35],v[212:215],v[68:71],v[32:35]
s_waitcnt lgkmcnt(2)
v_rcp_f32 v92,v92
v_mfma_f32_16x16x32_bf16 v[36:39],v[216:219],v[68:71],v[36:39]
s_waitcnt lgkmcnt(1)
v_cmp_eq_u32 vcc,s60,v13
v_rcp_f32 v93,v93
v_fma_f32 v94,v118,v94,v117
v_mfma_f32_16x16x32_bf16 v[28:31],v[220:223],v[72:75],v[28:31]
s_cmp_eq_u32 vcc_lo,-1
ds_read_b128 v[60:63],v2 offset:34816
v_mul_f32 v94,v50,v94
v_mfma_f32_16x16x32_bf16 v[32:35],v[224:227],v[72:75],v[32:35]
s_cselect_b32 s67,s86,s59
v_fma_f32 v95,v118,v95,v117
v_mul_f32 v95,v51,v95
v_exp_f32 v94,v94
v_exp_f32 v95,v95
v_mfma_f32_16x16x32_bf16 v[36:39],v[228:231],v[72:75],v[36:39]
v_fma_f32 v94,v94,v119,v119
v_fma_f32 v95,v95,v119,v119
v_rcp_f32 v94,v94
v_rcp_f32 v95,v95
v_mfma_f32_16x16x32_bf16 v[28:31],v[232:235],v[76:79],v[28:31]
s_lshr_b32 s61,s67,0
v_mul_f32 v48,v48,v92
v_mul_f32 v49,v49,v93
v_mul_f32 v50,v50,v94
v_mul_f32 v51,v51,v95
v_cvt_pk_bf16_f32 v100,v48,v49
v_cvt_pk_bf16_f32 v101,v50,v51
v_mfma_f32_16x16x32_bf16 v[32:35],v[236:239],v[76:79],v[32:35]
buffer_store_dwordx2 v[100:101],v9,s[36:39],s73 offen offset:0 sc0 sc1
ds_read_b128 v[64:67],v2 offset:35840
v_mfma_f32_16x16x32_bf16 v[36:39],v[240:243],v[76:79],v[36:39]
s_xor_b32 s63,s63,65536
s_cmp_ge_u32 s66,s59
v_mfma_f32_16x16x32_bf16 v[28:31],v[244:247],v[80:83],v[28:31]
ds_read_b128 v[68:71],v2 offset:36864
v_mfma_f32_16x16x32_bf16 v[32:35],v[248:251],v[80:83],v[32:35]
s_waitcnt lgkmcnt(3)
v_mfma_f32_16x16x32_bf16 v[36:39],v[252:255],v[80:83],v[36:39]
s_cbranch_scc1 Lg3_slow1_%=
Lg3_d1w0_%=:
v_mfma_f32_16x16x32_bf16 v[40:43],a[0:3],v[52:55],0
s_mov_b32 m0,s62
ds_read_b128 v[72:75],v2 offset:37888
v_mul_f32 v116,s75,v112
v_mfma_f32_16x16x32_bf16 v[44:47],a[4:7],v[52:55],0
buffer_load_dwordx4 v0,s[36:39],s61 offen offset:0 sc1 lds
v_mfma_f32_16x16x32_bf16 v[48:51],a[8:11],v[52:55],0
s_mul_i32 s73,s48,7
v_fma_f32 v113,v113,s75,v12
v_fma_f32 v113,-v116,v116,v113
v_mfma_f32_16x16x32_bf16 v[40:43],a[12:15],v[56:59],v[40:43]
s_lshr_b32 s73,s73,1
ds_read_b128 v[76:79],v2 offset:38912
v_max_f32 v113,v113,v12
v_mfma_f32_16x16x32_bf16 v[44:47],a[16:19],v[56:59],v[44:47]
s_add_u32 s74,s53,s55
v_rsq_f32 v112,v113
v_mfma_f32_16x16x32_bf16 v[48:51],a[20:23],v[56:59],v[48:51]
s_waitcnt lgkmcnt(3))"
R"(
buffer_load_dwordx4 v0,s[36:39],s61 offen offset:1024 sc1 lds
v_mfma_f32_16x16x32_bf16 v[40:43],a[24:27],v[60:63],v[40:43]
s_cmp_eq_u32 s74,s56
ds_read_b128 v[80:83],v2 offset:39936
v_mul_f32 v117,s76,v112
v_mfma_f32_16x16x32_bf16 v[44:47],a[28:31],v[60:63],v[44:47]
s_cselect_b32 s74,s57,s74
v_mul_f32 v118,v112,v112
v_mul_f32 v119,v113,v112
v_mfma_f32_16x16x32_bf16 v[48:51],a[32:35],v[60:63],v[48:51]
s_cmp_eq_u32 s74,s58
v_mul_f32 v118,s77,v118
v_mul_f32 v118,v118,v117
v_mfma_f32_16x16x32_bf16 v[40:43],a[36:39],v[64:67],v[40:43]
buffer_load_dwordx4 v0,s[36:39],s61 offen offset:2048 sc1 lds
v_mfma_f32_16x16x32_bf16 v[44:47],a[40:43],v[64:67],v[44:47]
s_cselect_b32 s74,s59,s74
ds_read_b128 v[52:55],v2 offset:40960
v_fma_f32 v28,-v116,v16,v28
v_mfma_f32_16x16x32_bf16 v[48:51],a[44:47],v[64:67],v[48:51]
s_waitcnt lgkmcnt(3)
v_fma_f32 v29,-v116,v17,v29
v_fma_f32 v30,-v116,v18,v30
v_mfma_f32_16x16x32_bf16 v[40:43],a[48:51],v[68:71],v[40:43]
s_cmp_ge_u32 s67,s59
ds_read_b128 v[56:59],v2 offset:41984
v_fma_f32 v31,-v116,v19,v31
v_mfma_f32_16x16x32_bf16 v[44:47],a[52:55],v[68:71],v[44:47]
buffer_load_dwordx4 v0,s[36:39],s61 offen offset:3072 sc1 lds
v_mfma_f32_16x16x32_bf16 v[48:51],a[56:59],v[68:71],v[48:51]
s_cselect_b32 s86,s50,s51
v_mul_f32 v84,v28,v28
v_mul_f32 v85,v29,v29
v_mfma_f32_16x16x32_bf16 v[40:43],a[60:63],v[72:75],v[40:43]
s_lshr_b32 s68,s86,5
ds_read_b128 v[60:63],v2 offset:43008
v_mul_f32 v86,v30,v30
v_mfma_f32_16x16x32_bf16 v[44:47],a[64:67],v[72:75],v[44:47]
s_add_u32 m0,s78,256
v_mul_f32 v87,v31,v31
v_fma_f32 v84,v118,v84,v117
v_mfma_f32_16x16x32_bf16 v[48:51],a[68:71],v[72:75],v[48:51]
s_waitcnt lgkmcnt(3)
v_mul_f32 v84,v28,v84
v_fma_f32 v85,v118,v85,v117
v_mfma_f32_16x16x32_bf16 v[40:43],a[72:75],v[76:79],v[40:43]
s_mov_b64 s[44:45],s[46:47]
ds_read_b128 v[64:67],v2 offset:44032
v_mul_f32 v85,v29,v85
v_mfma_f32_16x16x32_bf16 v[44:47],a[76:79],v[76:79],v[44:47]
s_mov_b64 s[46:47],s[48:49]
v_exp_f32 v84,v84
v_mfma_f32_16x16x32_bf16 v[48:51],a[80:83],v[76:79],v[48:51]
s_mov_b64 s[48:49],s[50:51]
v_exp_f32 v85,v85
v_mfma_f32_16x16x32_bf16 v[40:43],a[84:87],v[80:83],v[40:43]
s_mov_b64 s[50:51],s[52:53]
ds_read_b128 v[68:71],v2 offset:45056
v_fma_f32 v84,v84,v119,v119
v_mfma_f32_16x16x32_bf16 v[44:47],a[88:91],v[80:83],v[44:47]
s_mov_b32 s52,s74
v_rcp_f32 v84,v84
v_mfma_f32_16x16x32_bf16 v[48:51],a[92:95],v[80:83],v[48:51]
s_waitcnt lgkmcnt(3)
v_fma_f32 v85,v85,v119,v119
v_fma_f32 v86,v118,v86,v117
v_mfma_f32_16x16x32_bf16 v[40:43],a[96:99],v[52:55],v[40:43]
ds_read_b128 v[72:75],v2 offset:46080
v_mul_f32 v86,v30,v86
v_mfma_f32_16x16x32_bf16 v[44:47],a[100:103],v[52:55],v[44:47]
v_exp_f32 v86,v86
v_mfma_f32_16x16x32_bf16 v[48:51],a[104:107],v[52:55],v[48:51]
v_rcp_f32 v85,v85
v_mfma_f32_16x16x32_bf16 v[40:43],a[108:111],v[56:59],v[40:43]
ds_read_b128 v[76:79],v2 offset:47104
v_fma_f32 v86,v86,v119,v119
v_mfma_f32_16x16x32_bf16 v[44:47],a[112:115],v[56:59],v[44:47]
v_rcp_f32 v86,v86
v_mfma_f32_16x16x32_bf16 v[48:51],a[116:119],v[56:59],v[48:51]
s_waitcnt lgkmcnt(3)
v_fma_f32 v87,v118,v87,v117
v_mul_f32 v87,v31,v87
v_mfma_f32_16x16x32_bf16 v[40:43],a[120:123],v[60:63],v[40:43]
ds_read_b128 v[80:83],v2 offset:48128
v_mul_f32 v28,v28,v84
v_mfma_f32_16x16x32_bf16 v[44:47],a[124:127],v[60:63],v[44:47]
s_waitcnt vmcnt(6)
v_exp_f32 v87,v87
v_mfma_f32_16x16x32_bf16 v[48:51],a[128:131],v[60:63],v[48:51]
s_barrier
v_mfma_f32_16x16x32_bf16 v[40:43],a[132:135],v[64:67],v[40:43]
ds_read_b128 v[52:55],v2 offset:49152
v_fma_f32 v87,v87,v119,v119
v_mfma_f32_16x16x32_bf16 v[44:47],a[136:139],v[64:67],v[44:47]
v_rcp_f32 v87,v87
v_mfma_f32_16x16x32_bf16 v[48:51],a[140:143],v[64:67],v[48:51]
s_waitcnt lgkmcnt(3)
v_add_f32_dpp v108,v108,v108 quad_perm:[1,0,3,2] row_mask:0xf bank_mask:0xf
v_mul_f32 v29,v29,v85
v_mfma_f32_16x16x32_bf16 v[40:43],a[144:147],v[68:71],v[40:43]
ds_read_b128 v[56:59],v2 offset:50176
v_add_f32_dpp v108,v108,v108 quad_perm:[2,3,0,1] row_mask:0xf bank_mask:0xf
v_mfma_f32_16x16x32_bf16 v[44:47],a[148:151],v[68:71],v[44:47]
s_nop 0
v_add_f32_dpp v108,v108,v108 row_half_mirror row_mask:0xf bank_mask:0xf
v_mul_f32 v30,v30,v86
v_mfma_f32_16x16x32_bf16 v[48:51],a[152:155],v[68:71],v[48:51]
ds_write_b32 v10,v108 offset:128
v_mfma_f32_16x16x32_bf16 v[40:43],a[156:159],v[72:75],v[40:43]
ds_read_b128 v[60:63],v2 offset:51200
v_mul_f32 v31,v31,v87
v_mfma_f32_16x16x32_bf16 v[44:47],a[160:163],v[72:75],v[44:47]
buffer_load_dword v6,s[36:39],s68 offen offset:0 sc1 lds
v_mfma_f32_16x16x32_bf16 v[48:51],a[164:167],v[72:75],v[48:51]
s_waitcnt lgkmcnt(4)
v_cvt_pk_bf16_f32 v96,v28,v29
v_cvt_pk_bf16_f32 v97,v30,v31
v_mfma_f32_16x16x32_bf16 v[40:43],a[168:171],v[76:79],v[40:43]
s_add_u32 m0,s62,16384
ds_read_b128 v[64:67],v2 offset:52224
v_fma_f32 v32,-v116,v20,v32
v_mfma_f32_16x16x32_bf16 v[44:47],a[172:175],v[76:79],v[44:47]
buffer_load_dwordx4 v1,s[36:39],s61 offen offset:0 sc1 lds
v_mfma_f32_16x16x32_bf16 v[48:51],a[176:179],v[76:79],v[48:51]
s_lshr_b32 s72,s61,5
v_fma_f32 v33,-v116,v21,v33
v_fma_f32 v34,-v116,v22,v34
v_mfma_f32_16x16x32_bf16 v[40:43],a[180:183],v[80:83],v[40:43]
s_mov_b32 s62,s64
ds_read_b128 v[68:71],v2 offset:53248
v_fma_f32 v35,-v116,v23,v35
v_mfma_f32_16x16x32_bf16 v[44:47],a[184:187],v[80:83],v[44:47]
v_mul_f32 v88,v32,v32
v_mul_f32 v89,v33,v33
v_mfma_f32_16x16x32_bf16 v[48:51],a[188:191],v[80:83],v[48:51]
s_waitcnt lgkmcnt(4)
buffer_load_dwordx4 v1,s[36:39],s61 offen offset:1024 sc1 lds
v_mfma_f32_16x16x32_bf16 v[40:43],a[192:195],v[52:55],v[40:43]
ds_read_b128 v[72:75],v2 offset:54272
v_mul_f32 v90,v34,v34
v_mfma_f32_16x16x32_bf16 v[44:47],a[196:199],v[52:55],v[44:47]
v_mul_f32 v91,v35,v35
v_fma_f32 v88,v118,v88,v117
v_mfma_f32_16x16x32_bf16 v[48:51],a[200:203],v[52:55],v[48:51]
v_mul_f32 v88,v32,v88
v_fma_f32 v89,v118,v89,v117
v_mfma_f32_16x16x32_bf16 v[40:43],a[204:207],v[56:59],v[40:43]
buffer_load_dwordx4 v1,s[36:39],s61 offen offset:2048 sc1 lds
v_mfma_f32_16x16x32_bf16 v[44:47],a[208:211],v[56:59],v[44:47]
ds_read_b128 v[76:79],v2 offset:55296
v_mul_f32 v89,v33,v89
v_mfma_f32_16x16x32_bf16 v[48:51],a[212:215],v[56:59],v[48:51]
s_waitcnt lgkmcnt(3)
v_exp_f32 v88,v88
v_mfma_f32_16x16x32_bf16 v[40:43],a[216:219],v[60:63],v[40:43]
ds_read_b128 v[80:83],v2 offset:56320
v_fma_f32 v88,v88,v119,v119
v_mfma_f32_16x16x32_bf16 v[44:47],a[220:223],v[60:63],v[44:47]
s_waitcnt vmcnt(8)
v_exp_f32 v89,v89
v_mfma_f32_16x16x32_bf16 v[48:51],a[224:227],v[60:63],v[48:51]
s_barrier
v_mfma_f32_16x16x32_bf16 v[40:43],a[228:231],v[64:67],v[40:43]
buffer_load_dwordx4 v1,s[36:39],s61 offen offset:3072 sc1 lds
v_mfma_f32_16x16x32_bf16 v[44:47],a[232:235],v[64:67],v[44:47]
buffer_load_dword v104,v8,s[36:39],s72 offen offset:0 sc1
v_mfma_f32_16x16x32_bf16 v[48:51],a[236:239],v[64:67],v[48:51]
s_waitcnt lgkmcnt(2)
ds_read_b128 v[52:55],v2 offset:57344
v_fma_f32 v89,v89,v119,v119
v_mfma_f32_16x16x32_bf16 v[40:43],a[240:243],v[68:71],v[40:43]
ds_read_b128 v[56:59],v2 offset:58368
v_fma_f32 v90,v118,v90,v117
v_mfma_f32_16x16x32_bf16 v[44:47],a[244:247],v[68:71],v[44:47]
v_rcp_f32 v88,v88
v_mfma_f32_16x16x32_bf16 v[48:51],a[248:251],v[68:71],v[48:51]
v_rcp_f32 v89,v89
v_mfma_f32_16x16x32_bf16 v[40:43],a[252:255],v[72:75],v[40:43]
ds_read_b128 v[60:63],v2 offset:59392
v_mul_f32 v90,v34,v90
v_mfma_f32_16x16x32_bf16 v[44:47],v[128:131],v[72:75],v[44:47]
v_exp_f32 v90,v90
v_mfma_f32_16x16x32_bf16 v[48:51],v[132:135],v[72:75],v[48:51]
s_waitcnt lgkmcnt(3)
v_fma_f32 v90,v90,v119,v119
v_fma_f32 v91,v118,v91,v117
v_mfma_f32_16x16x32_bf16 v[40:43],v[136:139],v[76:79],v[40:43]
ds_read_b128 v[64:67],v2 offset:60416
v_mul_f32 v91,v35,v91
v_mfma_f32_16x16x32_bf16 v[44:47],v[140:143],v[76:79],v[44:47]
v_exp_f32 v91,v91
v_mfma_f32_16x16x32_bf16 v[48:51],v[144:147],v[76:79],v[48:51]
v_rcp_f32 v90,v90
v_mfma_f32_16x16x32_bf16 v[40:43],v[148:151],v[80:83],v[40:43]
ds_read_b128 v[68:71],v2 offset:61440
v_fma_f32 v91,v91,v119,v119
v_mfma_f32_16x16x32_bf16 v[44:47],v[152:155],v[80:83],v[44:47]
v_rcp_f32 v91,v91
v_mfma_f32_16x16x32_bf16 v[48:51],v[156:159],v[80:83],v[48:51]
s_waitcnt lgkmcnt(3))"
R"(
v_mul_f32 v32,v32,v88
v_mul_f32 v33,v33,v89
v_mul_f32 v34,v34,v90
v_mul_f32 v35,v35,v91
v_cvt_pk_bf16_f32 v98,v32,v33
v_cvt_pk_bf16_f32 v99,v34,v35
v_mfma_f32_16x16x32_bf16 v[40:43],v[160:163],v[52:55],v[40:43]
buffer_store_dwordx4 v[96:99],v3,s[36:39],s73 offen offset:0 sc0 sc1
ds_read_b128 v[72:75],v2 offset:62464
v_fma_f32 v36,-v116,v24,v36
v_fma_f32 v37,-v116,v25,v37
v_mfma_f32_16x16x32_bf16 v[44:47],v[164:167],v[52:55],v[44:47]
v_fma_f32 v38,-v116,v26,v38
v_fma_f32 v39,-v116,v27,v39
v_mfma_f32_16x16x32_bf16 v[48:51],v[168:171],v[52:55],v[48:51]
v_mul_f32 v92,v36,v36
v_mul_f32 v93,v37,v37
v_mfma_f32_16x16x32_bf16 v[40:43],v[172:175],v[56:59],v[40:43]
ds_read_b128 v[76:79],v2 offset:63488
v_mul_f32 v94,v38,v38
v_mfma_f32_16x16x32_bf16 v[44:47],v[176:179],v[56:59],v[44:47]
v_mul_f32 v95,v39,v39
v_fma_f32 v92,v118,v92,v117
v_mfma_f32_16x16x32_bf16 v[48:51],v[180:183],v[56:59],v[48:51]
s_waitcnt lgkmcnt(3)
v_mul_f32 v92,v36,v92
v_fma_f32 v93,v118,v93,v117
v_mfma_f32_16x16x32_bf16 v[40:43],v[184:187],v[60:63],v[40:43]
ds_read_b128 v[80:83],v2 offset:64512
v_mul_f32 v93,v37,v93
v_mfma_f32_16x16x32_bf16 v[44:47],v[188:191],v[60:63],v[44:47]
s_waitcnt vmcnt(6)
v_xor_b32 v2,65536,v2
v_fma_f32 v94,v118,v94,v117
v_mfma_f32_16x16x32_bf16 v[48:51],v[192:195],v[60:63],v[48:51]
s_barrier
v_mfma_f32_16x16x32_bf16 v[40:43],v[196:199],v[64:67],v[40:43]
ds_read_b128 v[52:55],v2 offset:0
ds_read_b32 v13,v7 offset:256
v_mfma_f32_16x16x32_bf16 v[44:47],v[200:203],v[64:67],v[44:47]
ds_read_b64 v[112:113],v11 offset:128
v_fma_f32 v95,v118,v95,v117
v_mfma_f32_16x16x32_bf16 v[48:51],v[204:207],v[64:67],v[48:51]
s_waitcnt lgkmcnt(5)
v_exp_f32 v92,v92
v_mfma_f32_16x16x32_bf16 v[40:43],v[208:211],v[68:71],v[40:43]
ds_read_b128 v[56:59],v2 offset:1024
v_mul_f32 v94,v38,v94
v_mfma_f32_16x16x32_bf16 v[44:47],v[212:215],v[68:71],v[44:47]
s_waitcnt lgkmcnt(2)
v_exp_f32 v93,v93
v_mfma_f32_16x16x32_bf16 v[48:51],v[216:219],v[68:71],v[48:51]
s_waitcnt lgkmcnt(1)
v_cmp_eq_u32 vcc,s60,v13
v_exp_f32 v94,v94
v_mul_f32 v95,v39,v95
v_mfma_f32_16x16x32_bf16 v[40:43],v[220:223],v[72:75],v[40:43]
s_cmp_eq_u32 vcc_lo,-1
ds_read_b128 v[60:63],v2 offset:2048
v_fma_f32 v92,v92,v119,v119
v_mfma_f32_16x16x32_bf16 v[44:47],v[224:227],v[72:75],v[44:47]
s_cselect_b32 s66,s86,s59
v_exp_f32 v95,v95
v_mfma_f32_16x16x32_bf16 v[48:51],v[228:231],v[72:75],v[48:51]
v_fma_f32 v93,v93,v119,v119
v_fma_f32 v94,v94,v119,v119
v_fma_f32 v95,v95,v119,v119
v_rcp_f32 v92,v92
v_rcp_f32 v93,v93
v_rcp_f32 v94,v94
v_rcp_f32 v95,v95
v_mfma_f32_16x16x32_bf16 v[40:43],v[232:235],v[76:79],v[40:43]
s_lshr_b32 s61,s66,0
v_mul_f32 v36,v36,v92
v_mul_f32 v37,v37,v93
v_mul_f32 v38,v38,v94
v_mul_f32 v39,v39,v95
v_cvt_pk_bf16_f32 v100,v36,v37
v_cvt_pk_bf16_f32 v101,v38,v39
v_mfma_f32_16x16x32_bf16 v[44:47],v[236:239],v[76:79],v[44:47]
buffer_store_dwordx2 v[100:101],v9,s[36:39],s73 offen offset:0 sc0 sc1
ds_read_b128 v[64:67],v2 offset:3072
v_mfma_f32_16x16x32_bf16 v[48:51],v[240:243],v[76:79],v[48:51]
s_xor_b32 s64,s64,65536
s_cmp_lt_u32 s67,s59
v_mfma_f32_16x16x32_bf16 v[40:43],v[244:247],v[80:83],v[40:43]
ds_read_b128 v[68:71],v2 offset:4096
v_mfma_f32_16x16x32_bf16 v[44:47],v[248:251],v[80:83],v[44:47]
s_waitcnt lgkmcnt(3)
v_mfma_f32_16x16x32_bf16 v[48:51],v[252:255],v[80:83],v[48:51]
s_cbranch_scc1 Lg3_d0w0_%=
s_branch Lg3_slow0_%=
Lg3_d0w1_%=:
v_mfma_f32_16x16x32_bf16 v[28:31],a[0:3],v[52:55],0
s_mov_b32 m0,s62
ds_read_b128 v[72:75],v2 offset:5120
v_mul_f32 v116,s75,v112
v_mfma_f32_16x16x32_bf16 v[32:35],a[4:7],v[52:55],0
s_mul_i32 s73,s47,7
v_fma_f32 v113,v113,s75,v12
v_fma_f32 v113,-v116,v116,v113
v_mfma_f32_16x16x32_bf16 v[36:39],a[8:11],v[52:55],0
buffer_load_dwordx4 v0,s[36:39],s61 offen offset:0 sc1 lds
v_mfma_f32_16x16x32_bf16 v[28:31],a[12:15],v[56:59],v[28:31]
s_lshr_b32 s73,s73,1
ds_read_b128 v[76:79],v2 offset:6144
v_max_f32 v113,v113,v12
v_mfma_f32_16x16x32_bf16 v[32:35],a[16:19],v[56:59],v[32:35]
s_add_u32 s53,s52,s55
v_rsq_f32 v112,v113
v_mfma_f32_16x16x32_bf16 v[36:39],a[20:23],v[56:59],v[36:39]
s_waitcnt lgkmcnt(3)
v_mul_f32 v117,s76,v112
v_mul_f32 v118,v112,v112
v_mfma_f32_16x16x32_bf16 v[28:31],a[24:27],v[60:63],v[28:31]
buffer_load_dwordx4 v0,s[36:39],s61 offen offset:1024 sc1 lds
v_mfma_f32_16x16x32_bf16 v[32:35],a[28:31],v[60:63],v[32:35]
s_cmp_eq_u32 s53,s56
ds_read_b128 v[80:83],v2 offset:7168
v_mul_f32 v119,v113,v112
v_mfma_f32_16x16x32_bf16 v[36:39],a[32:35],v[60:63],v[36:39]
s_cselect_b32 s53,s57,s53
v_mul_f32 v118,s77,v118
v_mul_f32 v118,v118,v117
v_mfma_f32_16x16x32_bf16 v[28:31],a[36:39],v[64:67],v[28:31]
s_cmp_eq_u32 s53,s58
ds_read_b128 v[52:55],v2 offset:8192
v_fma_f32 v40,-v116,v16,v40
v_mfma_f32_16x16x32_bf16 v[32:35],a[40:43],v[64:67],v[32:35]
buffer_load_dwordx4 v0,s[36:39],s61 offen offset:2048 sc1 lds
v_mfma_f32_16x16x32_bf16 v[36:39],a[44:47],v[64:67],v[36:39]
s_waitcnt lgkmcnt(3)
v_fma_f32 v41,-v116,v17,v41
v_fma_f32 v42,-v116,v18,v42
v_mfma_f32_16x16x32_bf16 v[28:31],a[48:51],v[68:71],v[28:31]
s_cselect_b32 s53,s59,s53
ds_read_b128 v[56:59],v2 offset:9216
v_fma_f32 v43,-v116,v19,v43
v_mfma_f32_16x16x32_bf16 v[32:35],a[52:55],v[68:71],v[32:35]
s_cmp_ge_u32 s66,s59
v_mul_f32 v84,v40,v40
v_mul_f32 v85,v41,v41
v_mfma_f32_16x16x32_bf16 v[36:39],a[56:59],v[68:71],v[36:39]
buffer_load_dwordx4 v0,s[36:39],s61 offen offset:3072 sc1 lds
v_mfma_f32_16x16x32_bf16 v[28:31],a[60:63],v[72:75],v[28:31]
s_cselect_b32 s86,s49,s50
ds_read_b128 v[60:63],v2 offset:10240
v_mul_f32 v86,v42,v42
v_mfma_f32_16x16x32_bf16 v[32:35],a[64:67],v[72:75],v[32:35]
s_add_u32 m0,s62,16384
v_mul_f32 v87,v43,v43
v_fma_f32 v84,v118,v84,v117
v_mfma_f32_16x16x32_bf16 v[36:39],a[68:71],v[72:75],v[36:39]
s_waitcnt lgkmcnt(3)
v_mul_f32 v84,v40,v84
v_fma_f32 v85,v118,v85,v117
v_mfma_f32_16x16x32_bf16 v[28:31],a[72:75],v[76:79],v[28:31]
s_lshr_b32 s72,s61,5
ds_read_b128 v[64:67],v2 offset:11264
v_mul_f32 v85,v41,v85
v_mfma_f32_16x16x32_bf16 v[32:35],a[76:79],v[76:79],v[32:35]
s_mov_b32 s62,s63
v_exp_f32 v84,v84
v_mfma_f32_16x16x32_bf16 v[36:39],a[80:83],v[76:79],v[36:39]
v_exp_f32 v85,v85
v_mfma_f32_16x16x32_bf16 v[28:31],a[84:87],v[80:83],v[28:31]
ds_read_b128 v[68:71],v2 offset:12288
v_fma_f32 v84,v84,v119,v119
v_mfma_f32_16x16x32_bf16 v[32:35],a[88:91],v[80:83],v[32:35]
v_rcp_f32 v84,v84
v_mfma_f32_16x16x32_bf16 v[36:39],a[92:95],v[80:83],v[36:39]
s_waitcnt lgkmcnt(3)
v_fma_f32 v85,v85,v119,v119
v_fma_f32 v86,v118,v86,v117
v_mfma_f32_16x16x32_bf16 v[28:31],a[96:99],v[52:55],v[28:31]
ds_read_b128 v[72:75],v2 offset:13312
v_mul_f32 v86,v42,v86
v_mfma_f32_16x16x32_bf16 v[32:35],a[100:103],v[52:55],v[32:35]
v_exp_f32 v86,v86
v_mfma_f32_16x16x32_bf16 v[36:39],a[104:107],v[52:55],v[36:39]
v_rcp_f32 v85,v85
v_mfma_f32_16x16x32_bf16 v[28:31],a[108:111],v[56:59],v[28:31]
ds_read_b128 v[76:79],v2 offset:14336
v_fma_f32 v86,v86,v119,v119
v_mfma_f32_16x16x32_bf16 v[32:35],a[112:115],v[56:59],v[32:35]
v_rcp_f32 v86,v86
v_mfma_f32_16x16x32_bf16 v[36:39],a[116:119],v[56:59],v[36:39]
s_waitcnt lgkmcnt(3)
v_fma_f32 v87,v118,v87,v117
v_mul_f32 v87,v43,v87
v_mfma_f32_16x16x32_bf16 v[28:31],a[120:123],v[60:63],v[28:31]
ds_read_b128 v[80:83],v2 offset:15360
v_mul_f32 v40,v40,v84
v_mfma_f32_16x16x32_bf16 v[32:35],a[124:127],v[60:63],v[32:35]
s_waitcnt vmcnt(6)
v_exp_f32 v87,v87
v_mfma_f32_16x16x32_bf16 v[36:39],a[128:131],v[60:63],v[36:39]
s_barrier
v_mfma_f32_16x16x32_bf16 v[28:31],a[132:135],v[64:67],v[28:31]
ds_read_b128 v[52:55],v2 offset:16384)"
R"(
v_fma_f32 v87,v87,v119,v119
v_mfma_f32_16x16x32_bf16 v[32:35],a[136:139],v[64:67],v[32:35]
v_rcp_f32 v87,v87
v_mfma_f32_16x16x32_bf16 v[36:39],a[140:143],v[64:67],v[36:39]
s_waitcnt lgkmcnt(3)
v_add_f32_dpp v104,v104,v104 quad_perm:[1,0,3,2] row_mask:0xf bank_mask:0xf
v_mul_f32 v41,v41,v85
v_mfma_f32_16x16x32_bf16 v[28:31],a[144:147],v[68:71],v[28:31]
ds_read_b128 v[56:59],v2 offset:17408
v_add_f32_dpp v104,v104,v104 quad_perm:[2,3,0,1] row_mask:0xf bank_mask:0xf
v_mfma_f32_16x16x32_bf16 v[32:35],a[148:151],v[68:71],v[32:35]
s_nop 0
v_add_f32_dpp v104,v104,v104 row_half_mirror row_mask:0xf bank_mask:0xf
v_mul_f32 v42,v42,v86
v_mfma_f32_16x16x32_bf16 v[36:39],a[152:155],v[68:71],v[36:39]
ds_write_b32 v10,v104 offset:0
v_mfma_f32_16x16x32_bf16 v[28:31],a[156:159],v[72:75],v[28:31]
ds_read_b128 v[60:63],v2 offset:18432
v_mul_f32 v43,v43,v87
v_mfma_f32_16x16x32_bf16 v[32:35],a[160:163],v[72:75],v[32:35]
v_cvt_pk_bf16_f32 v96,v40,v41
v_cvt_pk_bf16_f32 v97,v42,v43
v_mfma_f32_16x16x32_bf16 v[36:39],a[164:167],v[72:75],v[36:39]
s_waitcnt lgkmcnt(4)
v_fma_f32 v44,-v116,v20,v44
v_fma_f32 v45,-v116,v21,v45
v_mfma_f32_16x16x32_bf16 v[28:31],a[168:171],v[76:79],v[28:31]
ds_read_b128 v[64:67],v2 offset:19456
v_fma_f32 v46,-v116,v22,v46
v_mfma_f32_16x16x32_bf16 v[32:35],a[172:175],v[76:79],v[32:35]
v_fma_f32 v47,-v116,v23,v47
v_mul_f32 v88,v44,v44
v_mfma_f32_16x16x32_bf16 v[36:39],a[176:179],v[76:79],v[36:39]
buffer_load_dwordx4 v1,s[36:39],s61 offen offset:0 sc1 lds
v_mfma_f32_16x16x32_bf16 v[28:31],a[180:183],v[80:83],v[28:31]
ds_read_b128 v[68:71],v2 offset:20480
v_mul_f32 v89,v45,v45
v_mfma_f32_16x16x32_bf16 v[32:35],a[184:187],v[80:83],v[32:35]
v_mul_f32 v90,v46,v46
v_mul_f32 v91,v47,v47
v_mfma_f32_16x16x32_bf16 v[36:39],a[188:191],v[80:83],v[36:39]
s_waitcnt lgkmcnt(4)
v_fma_f32 v88,v118,v88,v117
v_mul_f32 v88,v44,v88
v_mfma_f32_16x16x32_bf16 v[28:31],a[192:195],v[52:55],v[28:31]
buffer_load_dwordx4 v1,s[36:39],s61 offen offset:1024 sc1 lds
v_mfma_f32_16x16x32_bf16 v[32:35],a[196:199],v[52:55],v[32:35]
ds_read_b128 v[72:75],v2 offset:21504
v_fma_f32 v89,v118,v89,v117
v_mfma_f32_16x16x32_bf16 v[36:39],a[200:203],v[52:55],v[36:39]
v_exp_f32 v88,v88
v_mfma_f32_16x16x32_bf16 v[28:31],a[204:207],v[56:59],v[28:31]
ds_read_b128 v[76:79],v2 offset:22528
v_mul_f32 v89,v45,v89
v_mfma_f32_16x16x32_bf16 v[32:35],a[208:211],v[56:59],v[32:35]
buffer_load_dwordx4 v1,s[36:39],s61 offen offset:2048 sc1 lds
v_mfma_f32_16x16x32_bf16 v[36:39],a[212:215],v[56:59],v[36:39]
s_waitcnt lgkmcnt(3)
v_exp_f32 v89,v89
v_mfma_f32_16x16x32_bf16 v[28:31],a[216:219],v[60:63],v[28:31]
ds_read_b128 v[80:83],v2 offset:23552
v_fma_f32 v88,v88,v119,v119
v_mfma_f32_16x16x32_bf16 v[32:35],a[220:223],v[60:63],v[32:35]
s_waitcnt vmcnt(7)
v_rcp_f32 v88,v88
v_mfma_f32_16x16x32_bf16 v[36:39],a[224:227],v[60:63],v[36:39]
s_barrier
v_mfma_f32_16x16x32_bf16 v[28:31],a[228:231],v[64:67],v[28:31]
ds_read_b128 v[52:55],v2 offset:24576
v_fma_f32 v89,v89,v119,v119
v_mfma_f32_16x16x32_bf16 v[32:35],a[232:235],v[64:67],v[32:35]
buffer_load_dwordx4 v1,s[36:39],s61 offen offset:3072 sc1 lds
v_mfma_f32_16x16x32_bf16 v[36:39],a[236:239],v[64:67],v[36:39]
s_waitcnt lgkmcnt(3)
buffer_load_dword v108,v8,s[36:39],s72 offen offset:0 sc1
v_mfma_f32_16x16x32_bf16 v[28:31],a[240:243],v[68:71],v[28:31]
ds_read_b128 v[56:59],v2 offset:25600
v_fma_f32 v90,v118,v90,v117
v_mfma_f32_16x16x32_bf16 v[32:35],a[244:247],v[68:71],v[32:35]
v_rcp_f32 v89,v89
v_mfma_f32_16x16x32_bf16 v[36:39],a[248:251],v[68:71],v[36:39]
v_mul_f32 v90,v46,v90
v_fma_f32 v91,v118,v91,v117
v_mfma_f32_16x16x32_bf16 v[28:31],a[252:255],v[72:75],v[28:31]
ds_read_b128 v[60:63],v2 offset:26624
v_mul_f32 v91,v47,v91
v_mfma_f32_16x16x32_bf16 v[32:35],v[128:131],v[72:75],v[32:35]
v_exp_f32 v90,v90
v_mfma_f32_16x16x32_bf16 v[36:39],v[132:135],v[72:75],v[36:39]
s_waitcnt lgkmcnt(3)
v_exp_f32 v91,v91
v_mfma_f32_16x16x32_bf16 v[28:31],v[136:139],v[76:79],v[28:31]
ds_read_b128 v[64:67],v2 offset:27648
v_fma_f32 v90,v90,v119,v119
v_mfma_f32_16x16x32_bf16 v[32:35],v[140:143],v[76:79],v[32:35]
v_rcp_f32 v90,v90
v_mfma_f32_16x16x32_bf16 v[36:39],v[144:147],v[76:79],v[36:39]
v_fma_f32 v91,v91,v119,v119
v_mul_f32 v44,v44,v88
v_mfma_f32_16x16x32_bf16 v[28:31],v[148:151],v[80:83],v[28:31]
ds_read_b128 v[68:71],v2 offset:28672
v_mul_f32 v45,v45,v89
v_mfma_f32_16x16x32_bf16 v[32:35],v[152:155],v[80:83],v[32:35]
v_rcp_f32 v91,v91
v_mfma_f32_16x16x32_bf16 v[36:39],v[156:159],v[80:83],v[36:39]
s_waitcnt lgkmcnt(3)
v_mul_f32 v46,v46,v90
v_mul_f32 v47,v47,v91
v_mfma_f32_16x16x32_bf16 v[28:31],v[160:163],v[52:55],v[28:31]
v_cvt_pk_bf16_f32 v98,v44,v45
v_cvt_pk_bf16_f32 v99,v46,v47
v_mfma_f32_16x16x32_bf16 v[32:35],v[164:167],v[52:55],v[32:35]
buffer_store_dwordx4 v[96:99],v3,s[36:39],s73 offen offset:0 sc0 sc1
ds_read_b128 v[72:75],v2 offset:29696
v_fma_f32 v48,-v116,v24,v48
v_fma_f32 v49,-v116,v25,v49
v_mfma_f32_16x16x32_bf16 v[36:39],v[168:171],v[52:55],v[36:39]
v_fma_f32 v50,-v116,v26,v50
v_fma_f32 v51,-v116,v27,v51
v_mfma_f32_16x16x32_bf16 v[28:31],v[172:175],v[56:59],v[28:31]
ds_read_b128 v[76:79],v2 offset:30720
v_mul_f32 v92,v48,v48
v_mfma_f32_16x16x32_bf16 v[32:35],v[176:179],v[56:59],v[32:35]
v_mul_f32 v93,v49,v49
v_mul_f32 v94,v50,v50
v_mfma_f32_16x16x32_bf16 v[36:39],v[180:183],v[56:59],v[36:39]
s_waitcnt lgkmcnt(3)
v_mul_f32 v95,v51,v51
v_fma_f32 v92,v118,v92,v117
v_mfma_f32_16x16x32_bf16 v[28:31],v[184:187],v[60:63],v[28:31]
ds_read_b128 v[80:83],v2 offset:31744
v_mul_f32 v92,v48,v92
v_mfma_f32_16x16x32_bf16 v[32:35],v[188:191],v[60:63],v[32:35]
s_waitcnt vmcnt(6)
v_exp_f32 v92,v92
v_mfma_f32_16x16x32_bf16 v[36:39],v[192:195],v[60:63],v[36:39]
s_barrier
v_mfma_f32_16x16x32_bf16 v[28:31],v[196:199],v[64:67],v[28:31]
ds_read_b128 v[52:55],v2 offset:32768
ds_read_b32 v13,v7 offset:0
v_mfma_f32_16x16x32_bf16 v[32:35],v[200:203],v[64:67],v[32:35]
ds_read_b64 v[112:113],v11 offset:0
v_fma_f32 v92,v92,v119,v119
v_mfma_f32_16x16x32_bf16 v[36:39],v[204:207],v[64:67],v[36:39]
s_waitcnt lgkmcnt(5)
v_rcp_f32 v92,v92
v_mfma_f32_16x16x32_bf16 v[28:31],v[208:211],v[68:71],v[28:31]
ds_read_b128 v[56:59],v2 offset:33792
v_fma_f32 v93,v118,v93,v117
v_mfma_f32_16x16x32_bf16 v[32:35],v[212:215],v[68:71],v[32:35]
s_waitcnt lgkmcnt(2)
v_mul_f32 v93,v49,v93
v_fma_f32 v94,v118,v94,v117
v_mfma_f32_16x16x32_bf16 v[36:39],v[216:219],v[68:71],v[36:39]
s_waitcnt lgkmcnt(1)
v_cmp_eq_u32 vcc,s60,v13
v_exp_f32 v93,v93
v_mul_f32 v94,v50,v94
v_mfma_f32_16x16x32_bf16 v[28:31],v[220:223],v[72:75],v[28:31]
s_cmp_eq_u32 vcc_lo,-1
ds_read_b128 v[60:63],v2 offset:34816
v_fma_f32 v93,v93,v119,v119
v_mfma_f32_16x16x32_bf16 v[32:35],v[224:227],v[72:75],v[32:35]
s_cselect_b32 s67,s86,s59
v_exp_f32 v94,v94
v_mfma_f32_16x16x32_bf16 v[36:39],v[228:231],v[72:75],v[36:39]
v_fma_f32 v95,v118,v95,v117
v_mul_f32 v95,v51,v95
v_exp_f32 v95,v95
v_mfma_f32_16x16x32_bf16 v[28:31],v[232:235],v[76:79],v[28:31]
s_lshr_b32 s61,s67,0
v_fma_f32 v94,v94,v119,v119
v_fma_f32 v95,v95,v119,v119
v_rcp_f32 v93,v93
v_rcp_f32 v94,v94
v_rcp_f32 v95,v95
v_mfma_f32_16x16x32_bf16 v[32:35],v[236:239],v[76:79],v[32:35]
s_xor_b32 s63,s63,65536
v_mul_f32 v48,v48,v92
v_mul_f32 v49,v49,v93
v_mul_f32 v50,v50,v94
v_mul_f32 v51,v51,v95
v_cvt_pk_bf16_f32 v100,v48,v49
v_cvt_pk_bf16_f32 v101,v50,v51
v_mfma_f32_16x16x32_bf16 v[36:39],v[240:243],v[76:79],v[36:39]
s_cmp_ge_u32 s66,s59
ds_read_b128 v[64:67],v2 offset:35840
buffer_store_dwordx2 v[100:101],v9,s[36:39],s73 offen offset:0 sc0 sc1
v_mfma_f32_16x16x32_bf16 v[28:31],v[244:247],v[80:83],v[28:31]
ds_read_b128 v[68:71],v2 offset:36864
v_mfma_f32_16x16x32_bf16 v[32:35],v[248:251],v[80:83],v[32:35]
s_waitcnt lgkmcnt(3)
v_mfma_f32_16x16x32_bf16 v[36:39],v[252:255],v[80:83],v[36:39]
s_cbranch_scc1 Lg3_slow1_%=
Lg3_d1w1_%=:
v_mfma_f32_16x16x32_bf16 v[40:43],a[0:3],v[52:55],0
s_mov_b32 m0,s62
ds_read_b128 v[72:75],v2 offset:37888
v_mul_f32 v116,s75,v112
v_mfma_f32_16x16x32_bf16 v[44:47],a[4:7],v[52:55],0)"
R"(
s_mul_i32 s73,s48,7
v_fma_f32 v113,v113,s75,v12
v_fma_f32 v113,-v116,v116,v113
v_mfma_f32_16x16x32_bf16 v[48:51],a[8:11],v[52:55],0
buffer_load_dwordx4 v0,s[36:39],s61 offen offset:0 sc1 lds
v_mfma_f32_16x16x32_bf16 v[40:43],a[12:15],v[56:59],v[40:43]
s_lshr_b32 s73,s73,1
ds_read_b128 v[76:79],v2 offset:38912
v_max_f32 v113,v113,v12
v_mfma_f32_16x16x32_bf16 v[44:47],a[16:19],v[56:59],v[44:47]
s_add_u32 s74,s53,s55
v_rsq_f32 v112,v113
v_mfma_f32_16x16x32_bf16 v[48:51],a[20:23],v[56:59],v[48:51]
s_waitcnt lgkmcnt(3)
v_mul_f32 v117,s76,v112
v_mul_f32 v118,v112,v112
v_mfma_f32_16x16x32_bf16 v[40:43],a[24:27],v[60:63],v[40:43]
buffer_load_dwordx4 v0,s[36:39],s61 offen offset:1024 sc1 lds
v_mfma_f32_16x16x32_bf16 v[44:47],a[28:31],v[60:63],v[44:47]
s_cmp_eq_u32 s74,s56
ds_read_b128 v[80:83],v2 offset:39936
v_mul_f32 v119,v113,v112
v_mfma_f32_16x16x32_bf16 v[48:51],a[32:35],v[60:63],v[48:51]
s_cselect_b32 s74,s57,s74
v_mul_f32 v118,s77,v118
v_mul_f32 v118,v118,v117
v_mfma_f32_16x16x32_bf16 v[40:43],a[36:39],v[64:67],v[40:43]
s_cmp_eq_u32 s74,s58
ds_read_b128 v[52:55],v2 offset:40960
v_fma_f32 v28,-v116,v16,v28
v_mfma_f32_16x16x32_bf16 v[44:47],a[40:43],v[64:67],v[44:47]
buffer_load_dwordx4 v0,s[36:39],s61 offen offset:2048 sc1 lds
v_mfma_f32_16x16x32_bf16 v[48:51],a[44:47],v[64:67],v[48:51]
s_waitcnt lgkmcnt(3)
v_fma_f32 v29,-v116,v17,v29
v_fma_f32 v30,-v116,v18,v30
v_mfma_f32_16x16x32_bf16 v[40:43],a[48:51],v[68:71],v[40:43]
s_cselect_b32 s74,s59,s74
ds_read_b128 v[56:59],v2 offset:41984
v_fma_f32 v31,-v116,v19,v31
v_mfma_f32_16x16x32_bf16 v[44:47],a[52:55],v[68:71],v[44:47]
s_cmp_ge_u32 s67,s59
v_mul_f32 v84,v28,v28
v_mul_f32 v85,v29,v29
v_mfma_f32_16x16x32_bf16 v[48:51],a[56:59],v[68:71],v[48:51]
buffer_load_dwordx4 v0,s[36:39],s61 offen offset:3072 sc1 lds
v_mfma_f32_16x16x32_bf16 v[40:43],a[60:63],v[72:75],v[40:43]
s_cselect_b32 s86,s50,s51
ds_read_b128 v[60:63],v2 offset:43008
v_mul_f32 v86,v30,v30
v_mfma_f32_16x16x32_bf16 v[44:47],a[64:67],v[72:75],v[44:47]
s_add_u32 m0,s62,16384
v_mul_f32 v87,v31,v31
v_fma_f32 v84,v118,v84,v117
v_mfma_f32_16x16x32_bf16 v[48:51],a[68:71],v[72:75],v[48:51]
s_waitcnt lgkmcnt(3)
v_mul_f32 v84,v28,v84
v_fma_f32 v85,v118,v85,v117
v_mfma_f32_16x16x32_bf16 v[40:43],a[72:75],v[76:79],v[40:43]
s_lshr_b32 s72,s61,5
ds_read_b128 v[64:67],v2 offset:44032
v_mul_f32 v85,v29,v85
v_mfma_f32_16x16x32_bf16 v[44:47],a[76:79],v[76:79],v[44:47]
s_mov_b32 s62,s64
v_exp_f32 v84,v84
v_mfma_f32_16x16x32_bf16 v[48:51],a[80:83],v[76:79],v[48:51]
s_mov_b64 s[44:45],s[46:47]
v_exp_f32 v85,v85
v_mfma_f32_16x16x32_bf16 v[40:43],a[84:87],v[80:83],v[40:43]
s_mov_b64 s[46:47],s[48:49]
ds_read_b128 v[68:71],v2 offset:45056
v_fma_f32 v84,v84,v119,v119
v_mfma_f32_16x16x32_bf16 v[44:47],a[88:91],v[80:83],v[44:47]
s_mov_b64 s[48:49],s[50:51]
v_rcp_f32 v84,v84
v_mfma_f32_16x16x32_bf16 v[48:51],a[92:95],v[80:83],v[48:51]
s_waitcnt lgkmcnt(3)
v_fma_f32 v85,v85,v119,v119
v_fma_f32 v86,v118,v86,v117
v_mfma_f32_16x16x32_bf16 v[40:43],a[96:99],v[52:55],v[40:43]
s_mov_b64 s[50:51],s[52:53]
ds_read_b128 v[72:75],v2 offset:46080
v_mul_f32 v86,v30,v86
v_mfma_f32_16x16x32_bf16 v[44:47],a[100:103],v[52:55],v[44:47]
s_mov_b32 s52,s74
v_exp_f32 v86,v86
v_mfma_f32_16x16x32_bf16 v[48:51],a[104:107],v[52:55],v[48:51]
v_rcp_f32 v85,v85
v_mfma_f32_16x16x32_bf16 v[40:43],a[108:111],v[56:59],v[40:43]
ds_read_b128 v[76:79],v2 offset:47104
v_fma_f32 v86,v86,v119,v119
v_mfma_f32_16x16x32_bf16 v[44:47],a[112:115],v[56:59],v[44:47]
v_rcp_f32 v86,v86
v_mfma_f32_16x16x32_bf16 v[48:51],a[116:119],v[56:59],v[48:51]
s_waitcnt lgkmcnt(3)
v_fma_f32 v87,v118,v87,v117
v_mul_f32 v87,v31,v87
v_mfma_f32_16x16x32_bf16 v[40:43],a[120:123],v[60:63],v[40:43]
ds_read_b128 v[80:83],v2 offset:48128
v_mul_f32 v28,v28,v84
v_mfma_f32_16x16x32_bf16 v[44:47],a[124:127],v[60:63],v[44:47]
s_waitcnt vmcnt(6)
v_exp_f32 v87,v87
v_mfma_f32_16x16x32_bf16 v[48:51],a[128:131],v[60:63],v[48:51]
s_barrier
v_mfma_f32_16x16x32_bf16 v[40:43],a[132:135],v[64:67],v[40:43]
ds_read_b128 v[52:55],v2 offset:49152
v_fma_f32 v87,v87,v119,v119
v_mfma_f32_16x16x32_bf16 v[44:47],a[136:139],v[64:67],v[44:47]
v_rcp_f32 v87,v87
v_mfma_f32_16x16x32_bf16 v[48:51],a[140:143],v[64:67],v[48:51]
s_waitcnt lgkmcnt(3)
v_add_f32_dpp v108,v108,v108 quad_perm:[1,0,3,2] row_mask:0xf bank_mask:0xf
v_mul_f32 v29,v29,v85
v_mfma_f32_16x16x32_bf16 v[40:43],a[144:147],v[68:71],v[40:43]
ds_read_b128 v[56:59],v2 offset:50176
v_add_f32_dpp v108,v108,v108 quad_perm:[2,3,0,1] row_mask:0xf bank_mask:0xf
v_mfma_f32_16x16x32_bf16 v[44:47],a[148:151],v[68:71],v[44:47]
s_nop 0
v_add_f32_dpp v108,v108,v108 row_half_mirror row_mask:0xf bank_mask:0xf
v_mul_f32 v30,v30,v86
v_mfma_f32_16x16x32_bf16 v[48:51],a[152:155],v[68:71],v[48:51]
ds_write_b32 v10,v108 offset:128
v_mfma_f32_16x16x32_bf16 v[40:43],a[156:159],v[72:75],v[40:43]
ds_read_b128 v[60:63],v2 offset:51200
v_mul_f32 v31,v31,v87
v_mfma_f32_16x16x32_bf16 v[44:47],a[160:163],v[72:75],v[44:47]
v_cvt_pk_bf16_f32 v96,v28,v29
v_cvt_pk_bf16_f32 v97,v30,v31
v_mfma_f32_16x16x32_bf16 v[48:51],a[164:167],v[72:75],v[48:51]
s_waitcnt lgkmcnt(4)
v_fma_f32 v32,-v116,v20,v32
v_fma_f32 v33,-v116,v21,v33
v_mfma_f32_16x16x32_bf16 v[40:43],a[168:171],v[76:79],v[40:43]
ds_read_b128 v[64:67],v2 offset:52224
v_fma_f32 v34,-v116,v22,v34
v_mfma_f32_16x16x32_bf16 v[44:47],a[172:175],v[76:79],v[44:47]
v_fma_f32 v35,-v116,v23,v35
v_mul_f32 v88,v32,v32
v_mfma_f32_16x16x32_bf16 v[48:51],a[176:179],v[76:79],v[48:51]
buffer_load_dwordx4 v1,s[36:39],s61 offen offset:0 sc1 lds
v_mfma_f32_16x16x32_bf16 v[40:43],a[180:183],v[80:83],v[40:43]
ds_read_b128 v[68:71],v2 offset:53248
v_mul_f32 v89,v33,v33
v_mfma_f32_16x16x32_bf16 v[44:47],a[184:187],v[80:83],v[44:47]
v_mul_f32 v90,v34,v34
v_mul_f32 v91,v35,v35
v_mfma_f32_16x16x32_bf16 v[48:51],a[188:191],v[80:83],v[48:51]
s_waitcnt lgkmcnt(4)
v_fma_f32 v88,v118,v88,v117
v_mul_f32 v88,v32,v88
v_mfma_f32_16x16x32_bf16 v[40:43],a[192:195],v[52:55],v[40:43]
buffer_load_dwordx4 v1,s[36:39],s61 offen offset:1024 sc1 lds
v_mfma_f32_16x16x32_bf16 v[44:47],a[196:199],v[52:55],v[44:47]
ds_read_b128 v[72:75],v2 offset:54272
v_fma_f32 v89,v118,v89,v117
v_mfma_f32_16x16x32_bf16 v[48:51],a[200:203],v[52:55],v[48:51]
v_exp_f32 v88,v88
v_mfma_f32_16x16x32_bf16 v[40:43],a[204:207],v[56:59],v[40:43]
ds_read_b128 v[76:79],v2 offset:55296
v_mul_f32 v89,v33,v89
v_mfma_f32_16x16x32_bf16 v[44:47],a[208:211],v[56:59],v[44:47]
buffer_load_dwordx4 v1,s[36:39],s61 offen offset:2048 sc1 lds
v_mfma_f32_16x16x32_bf16 v[48:51],a[212:215],v[56:59],v[48:51]
s_waitcnt lgkmcnt(3)
v_exp_f32 v89,v89
v_mfma_f32_16x16x32_bf16 v[40:43],a[216:219],v[60:63],v[40:43]
ds_read_b128 v[80:83],v2 offset:56320
v_fma_f32 v88,v88,v119,v119
v_mfma_f32_16x16x32_bf16 v[44:47],a[220:223],v[60:63],v[44:47]
s_waitcnt vmcnt(7)
v_rcp_f32 v88,v88
v_mfma_f32_16x16x32_bf16 v[48:51],a[224:227],v[60:63],v[48:51]
s_barrier
v_mfma_f32_16x16x32_bf16 v[40:43],a[228:231],v[64:67],v[40:43]
ds_read_b128 v[52:55],v2 offset:57344
v_fma_f32 v89,v89,v119,v119
v_mfma_f32_16x16x32_bf16 v[44:47],a[232:235],v[64:67],v[44:47]
buffer_load_dwordx4 v1,s[36:39],s61 offen offset:3072 sc1 lds
v_mfma_f32_16x16x32_bf16 v[48:51],a[236:239],v[64:67],v[48:51]
s_waitcnt lgkmcnt(3)
buffer_load_dword v104,v8,s[36:39],s72 offen offset:0 sc1
v_mfma_f32_16x16x32_bf16 v[40:43],a[240:243],v[68:71],v[40:43]
ds_read_b128 v[56:59],v2 offset:58368
v_fma_f32 v90,v118,v90,v117
v_mfma_f32_16x16x32_bf16 v[44:47],a[244:247],v[68:71],v[44:47]
v_rcp_f32 v89,v89
v_mfma_f32_16x16x32_bf16 v[48:51],a[248:251],v[68:71],v[48:51]
v_mul_f32 v90,v34,v90
v_fma_f32 v91,v118,v91,v117
v_mfma_f32_16x16x32_bf16 v[40:43],a[252:255],v[72:75],v[40:43]
ds_read_b128 v[60:63],v2 offset:59392
v_mul_f32 v91,v35,v91
v_mfma_f32_16x16x32_bf16 v[44:47],v[128:131],v[72:75],v[44:47]
v_exp_f32 v90,v90
v_mfma_f32_16x16x32_bf16 v[48:51],v[132:135],v[72:75],v[48:51]
s_waitcnt lgkmcnt(3)
v_exp_f32 v91,v91
v_mfma_f32_16x16x32_bf16 v[40:43],v[136:139],v[76:79],v[40:43]
ds_read_b128 v[64:67],v2 offset:60416)"
R"(
v_fma_f32 v90,v90,v119,v119
v_mfma_f32_16x16x32_bf16 v[44:47],v[140:143],v[76:79],v[44:47]
v_rcp_f32 v90,v90
v_mfma_f32_16x16x32_bf16 v[48:51],v[144:147],v[76:79],v[48:51]
v_fma_f32 v91,v91,v119,v119
v_mul_f32 v32,v32,v88
v_mfma_f32_16x16x32_bf16 v[40:43],v[148:151],v[80:83],v[40:43]
ds_read_b128 v[68:71],v2 offset:61440
v_mul_f32 v33,v33,v89
v_mfma_f32_16x16x32_bf16 v[44:47],v[152:155],v[80:83],v[44:47]
v_rcp_f32 v91,v91
v_mfma_f32_16x16x32_bf16 v[48:51],v[156:159],v[80:83],v[48:51]
s_waitcnt lgkmcnt(3)
v_mul_f32 v34,v34,v90
v_mul_f32 v35,v35,v91
v_mfma_f32_16x16x32_bf16 v[40:43],v[160:163],v[52:55],v[40:43]
v_cvt_pk_bf16_f32 v98,v32,v33
v_cvt_pk_bf16_f32 v99,v34,v35
v_mfma_f32_16x16x32_bf16 v[44:47],v[164:167],v[52:55],v[44:47]
buffer_store_dwordx4 v[96:99],v3,s[36:39],s73 offen offset:0 sc0 sc1
ds_read_b128 v[72:75],v2 offset:62464
v_fma_f32 v36,-v116,v24,v36
v_fma_f32 v37,-v116,v25,v37
v_mfma_f32_16x16x32_bf16 v[48:51],v[168:171],v[52:55],v[48:51]
v_fma_f32 v38,-v116,v26,v38
v_fma_f32 v39,-v116,v27,v39
v_mfma_f32_16x16x32_bf16 v[40:43],v[172:175],v[56:59],v[40:43]
ds_read_b128 v[76:79],v2 offset:63488
v_mul_f32 v92,v36,v36
v_mfma_f32_16x16x32_bf16 v[44:47],v[176:179],v[56:59],v[44:47]
v_mul_f32 v93,v37,v37
v_mul_f32 v94,v38,v38
v_mfma_f32_16x16x32_bf16 v[48:51],v[180:183],v[56:59],v[48:51]
s_waitcnt lgkmcnt(3)
v_mul_f32 v95,v39,v39
v_fma_f32 v92,v118,v92,v117
v_mfma_f32_16x16x32_bf16 v[40:43],v[184:187],v[60:63],v[40:43]
ds_read_b128 v[80:83],v2 offset:64512
v_mul_f32 v92,v36,v92
v_mfma_f32_16x16x32_bf16 v[44:47],v[188:191],v[60:63],v[44:47]
s_waitcnt vmcnt(6)
v_xor_b32 v2,65536,v2
v_fma_f32 v93,v118,v93,v117
v_mfma_f32_16x16x32_bf16 v[48:51],v[192:195],v[60:63],v[48:51]
s_barrier
v_mfma_f32_16x16x32_bf16 v[40:43],v[196:199],v[64:67],v[40:43]
ds_read_b128 v[52:55],v2 offset:0
ds_read_b32 v13,v7 offset:256
v_mfma_f32_16x16x32_bf16 v[44:47],v[200:203],v[64:67],v[44:47]
ds_read_b64 v[112:113],v11 offset:128
v_mul_f32 v93,v37,v93
v_mfma_f32_16x16x32_bf16 v[48:51],v[204:207],v[64:67],v[48:51]
s_waitcnt lgkmcnt(5)
v_exp_f32 v92,v92
v_mfma_f32_16x16x32_bf16 v[40:43],v[208:211],v[68:71],v[40:43]
ds_read_b128 v[56:59],v2 offset:1024
v_fma_f32 v92,v92,v119,v119
v_mfma_f32_16x16x32_bf16 v[44:47],v[212:215],v[68:71],v[44:47]
s_waitcnt lgkmcnt(2)
v_exp_f32 v93,v93
v_mfma_f32_16x16x32_bf16 v[48:51],v[216:219],v[68:71],v[48:51]
s_waitcnt lgkmcnt(1)
v_cmp_eq_u32 vcc,s60,v13
v_rcp_f32 v92,v92
v_fma_f32 v93,v93,v119,v119
v_mfma_f32_16x16x32_bf16 v[40:43],v[220:223],v[72:75],v[40:43]
s_cmp_eq_u32 vcc_lo,-1
ds_read_b128 v[60:63],v2 offset:2048
v_fma_f32 v94,v118,v94,v117
v_mfma_f32_16x16x32_bf16 v[44:47],v[224:227],v[72:75],v[44:47]
s_cselect_b32 s66,s86,s59
v_rcp_f32 v93,v93
v_mfma_f32_16x16x32_bf16 v[48:51],v[228:231],v[72:75],v[48:51]
v_fma_f32 v95,v118,v95,v117
v_mul_f32 v94,v38,v94
v_mul_f32 v95,v39,v95
v_exp_f32 v94,v94
v_exp_f32 v95,v95
v_mfma_f32_16x16x32_bf16 v[40:43],v[232:235],v[76:79],v[40:43]
s_lshr_b32 s61,s66,0
v_fma_f32 v94,v94,v119,v119
v_fma_f32 v95,v95,v119,v119
v_rcp_f32 v94,v94
v_rcp_f32 v95,v95
v_mfma_f32_16x16x32_bf16 v[44:47],v[236:239],v[76:79],v[44:47]
s_xor_b32 s64,s64,65536
v_mul_f32 v36,v36,v92
v_mul_f32 v37,v37,v93
v_mul_f32 v38,v38,v94
v_mul_f32 v39,v39,v95
v_cvt_pk_bf16_f32 v100,v36,v37
v_cvt_pk_bf16_f32 v101,v38,v39
v_mfma_f32_16x16x32_bf16 v[48:51],v[240:243],v[76:79],v[48:51]
s_cmp_lt_u32 s67,s59
ds_read_b128 v[64:67],v2 offset:3072
buffer_store_dwordx2 v[100:101],v9,s[36:39],s73 offen offset:0 sc0 sc1
v_mfma_f32_16x16x32_bf16 v[40:43],v[244:247],v[80:83],v[40:43]
ds_read_b128 v[68:71],v2 offset:4096
v_mfma_f32_16x16x32_bf16 v[44:47],v[248:251],v[80:83],v[44:47]
s_waitcnt lgkmcnt(3)
v_mfma_f32_16x16x32_bf16 v[48:51],v[252:255],v[80:83],v[48:51]
s_cbranch_scc1 Lg3_d0w1_%=
s_branch Lg3_slow0_%=
Lg3_d0w2_%=:
v_mfma_f32_16x16x32_bf16 v[28:31],a[0:3],v[52:55],0
s_mov_b32 m0,s62
ds_read_b128 v[72:75],v2 offset:5120
v_mul_f32 v116,s75,v112
v_mfma_f32_16x16x32_bf16 v[32:35],a[4:7],v[52:55],0
s_mul_i32 s73,s47,7
v_fma_f32 v113,v113,s75,v12
v_fma_f32 v113,-v116,v116,v113
v_mfma_f32_16x16x32_bf16 v[36:39],a[8:11],v[52:55],0
s_lshr_b32 s73,s73,1
v_max_f32 v113,v113,v12
v_rsq_f32 v112,v113
v_mfma_f32_16x16x32_bf16 v[28:31],a[12:15],v[56:59],v[28:31]
buffer_load_dwordx4 v0,s[36:39],s61 offen offset:0 sc1 lds
v_mfma_f32_16x16x32_bf16 v[32:35],a[16:19],v[56:59],v[32:35]
s_add_u32 s53,s52,s55
ds_read_b128 v[76:79],v2 offset:6144
v_mul_f32 v117,s76,v112
v_mfma_f32_16x16x32_bf16 v[36:39],a[20:23],v[56:59],v[36:39]
s_waitcnt lgkmcnt(3)
v_mul_f32 v118,v112,v112
v_mul_f32 v119,v113,v112
v_mfma_f32_16x16x32_bf16 v[28:31],a[24:27],v[60:63],v[28:31]
s_cmp_eq_u32 s53,s56
ds_read_b128 v[80:83],v2 offset:7168
v_mul_f32 v118,s77,v118
v_mfma_f32_16x16x32_bf16 v[32:35],a[28:31],v[60:63],v[32:35]
buffer_load_dwordx4 v0,s[36:39],s61 offen offset:1024 sc1 lds
v_mfma_f32_16x16x32_bf16 v[36:39],a[32:35],v[60:63],v[36:39]
s_cselect_b32 s53,s57,s53
v_mul_f32 v118,v118,v117
v_fma_f32 v40,-v116,v16,v40
v_mfma_f32_16x16x32_bf16 v[28:31],a[36:39],v[64:67],v[28:31]
s_cmp_eq_u32 s53,s58
ds_read_b128 v[52:55],v2 offset:8192
v_fma_f32 v41,-v116,v17,v41
v_mfma_f32_16x16x32_bf16 v[32:35],a[40:43],v[64:67],v[32:35]
s_cselect_b32 s53,s59,s53
v_fma_f32 v42,-v116,v18,v42
v_fma_f32 v43,-v116,v19,v43
v_mfma_f32_16x16x32_bf16 v[36:39],a[44:47],v[64:67],v[36:39]
s_waitcnt lgkmcnt(3)
buffer_load_dwordx4 v0,s[36:39],s61 offen offset:2048 sc1 lds
v_mfma_f32_16x16x32_bf16 v[28:31],a[48:51],v[68:71],v[28:31]
s_cmp_ge_u32 s66,s59
ds_read_b128 v[56:59],v2 offset:9216
v_mul_f32 v84,v40,v40
v_mfma_f32_16x16x32_bf16 v[32:35],a[52:55],v[68:71],v[32:35]
s_cselect_b32 s86,s49,s50
v_mul_f32 v85,v41,v41
v_mul_f32 v86,v42,v42
v_mfma_f32_16x16x32_bf16 v[36:39],a[56:59],v[68:71],v[36:39]
v_mul_f32 v87,v43,v43
v_fma_f32 v84,v118,v84,v117
v_mfma_f32_16x16x32_bf16 v[28:31],a[60:63],v[72:75],v[28:31]
buffer_load_dwordx4 v0,s[36:39],s61 offen offset:3072 sc1 lds
v_mfma_f32_16x16x32_bf16 v[32:35],a[64:67],v[72:75],v[32:35]
s_add_u32 m0,s62,16384
ds_read_b128 v[60:63],v2 offset:10240
v_mul_f32 v84,v40,v84
v_mfma_f32_16x16x32_bf16 v[36:39],a[68:71],v[72:75],v[36:39]
s_waitcnt lgkmcnt(3)
v_exp_f32 v84,v84
v_mfma_f32_16x16x32_bf16 v[28:31],a[72:75],v[76:79],v[28:31]
s_lshr_b32 s72,s61,5
ds_read_b128 v[64:67],v2 offset:11264
v_fma_f32 v84,v84,v119,v119
v_mfma_f32_16x16x32_bf16 v[32:35],a[76:79],v[76:79],v[32:35]
s_mov_b32 s62,s63
v_rcp_f32 v84,v84
v_mfma_f32_16x16x32_bf16 v[36:39],a[80:83],v[76:79],v[36:39]
v_fma_f32 v85,v118,v85,v117
v_mul_f32 v85,v41,v85
v_mfma_f32_16x16x32_bf16 v[28:31],a[84:87],v[80:83],v[28:31]
ds_read_b128 v[68:71],v2 offset:12288
v_fma_f32 v86,v118,v86,v117
v_mfma_f32_16x16x32_bf16 v[32:35],a[88:91],v[80:83],v[32:35]
v_exp_f32 v85,v85
v_mfma_f32_16x16x32_bf16 v[36:39],a[92:95],v[80:83],v[36:39]
s_waitcnt lgkmcnt(3)
v_mul_f32 v86,v42,v86
v_fma_f32 v85,v85,v119,v119
v_mfma_f32_16x16x32_bf16 v[28:31],a[96:99],v[52:55],v[28:31]
ds_read_b128 v[72:75],v2 offset:13312
v_exp_f32 v86,v86
v_mfma_f32_16x16x32_bf16 v[32:35],a[100:103],v[52:55],v[32:35]
v_rcp_f32 v85,v85
v_mfma_f32_16x16x32_bf16 v[36:39],a[104:107],v[52:55],v[36:39]
v_fma_f32 v86,v86,v119,v119
v_fma_f32 v87,v118,v87,v117
v_mfma_f32_16x16x32_bf16 v[28:31],a[108:111],v[56:59],v[28:31]
ds_read_b128 v[76:79],v2 offset:14336
v_mul_f32 v87,v43,v87
v_mfma_f32_16x16x32_bf16 v[32:35],a[112:115],v[56:59],v[32:35]
v_exp_f32 v87,v87
v_mfma_f32_16x16x32_bf16 v[36:39],a[116:119],v[56:59],v[36:39])"
R"(
s_waitcnt lgkmcnt(3)
v_rcp_f32 v86,v86
v_mfma_f32_16x16x32_bf16 v[28:31],a[120:123],v[60:63],v[28:31]
ds_read_b128 v[80:83],v2 offset:15360
v_fma_f32 v87,v87,v119,v119
v_mfma_f32_16x16x32_bf16 v[32:35],a[124:127],v[60:63],v[32:35]
s_waitcnt vmcnt(6)
v_rcp_f32 v87,v87
v_mfma_f32_16x16x32_bf16 v[36:39],a[128:131],v[60:63],v[36:39]
s_barrier
v_mfma_f32_16x16x32_bf16 v[28:31],a[132:135],v[64:67],v[28:31]
ds_read_b128 v[52:55],v2 offset:16384
v_add_f32_dpp v104,v104,v104 quad_perm:[1,0,3,2] row_mask:0xf bank_mask:0xf
v_mfma_f32_16x16x32_bf16 v[32:35],a[136:139],v[64:67],v[32:35]
s_nop 0
v_add_f32_dpp v104,v104,v104 quad_perm:[2,3,0,1] row_mask:0xf bank_mask:0xf
v_mul_f32 v40,v40,v84
v_mfma_f32_16x16x32_bf16 v[36:39],a[140:143],v[64:67],v[36:39]
s_waitcnt lgkmcnt(3)
v_add_f32_dpp v104,v104,v104 row_half_mirror row_mask:0xf bank_mask:0xf
v_mul_f32 v41,v41,v85
v_mfma_f32_16x16x32_bf16 v[28:31],a[144:147],v[68:71],v[28:31]
ds_write_b32 v10,v104 offset:0
v_mfma_f32_16x16x32_bf16 v[32:35],a[148:151],v[68:71],v[32:35]
ds_read_b128 v[56:59],v2 offset:17408
v_mul_f32 v42,v42,v86
v_mfma_f32_16x16x32_bf16 v[36:39],a[152:155],v[68:71],v[36:39]
v_mul_f32 v43,v43,v87
v_cvt_pk_bf16_f32 v96,v40,v41
v_mfma_f32_16x16x32_bf16 v[28:31],a[156:159],v[72:75],v[28:31]
ds_read_b128 v[60:63],v2 offset:18432
v_cvt_pk_bf16_f32 v97,v42,v43
v_mfma_f32_16x16x32_bf16 v[32:35],a[160:163],v[72:75],v[32:35]
v_fma_f32 v44,-v116,v20,v44
v_fma_f32 v45,-v116,v21,v45
v_mfma_f32_16x16x32_bf16 v[36:39],a[164:167],v[72:75],v[36:39]
s_waitcnt lgkmcnt(4)
v_fma_f32 v46,-v116,v22,v46
v_fma_f32 v47,-v116,v23,v47
v_mfma_f32_16x16x32_bf16 v[28:31],a[168:171],v[76:79],v[28:31]
ds_read_b128 v[64:67],v2 offset:19456
v_mul_f32 v88,v44,v44
v_mfma_f32_16x16x32_bf16 v[32:35],a[172:175],v[76:79],v[32:35]
v_mul_f32 v89,v45,v45
v_mul_f32 v90,v46,v46
v_mfma_f32_16x16x32_bf16 v[36:39],a[176:179],v[76:79],v[36:39]
v_mul_f32 v91,v47,v47
v_fma_f32 v88,v118,v88,v117
v_mfma_f32_16x16x32_bf16 v[28:31],a[180:183],v[80:83],v[28:31]
buffer_load_dwordx4 v1,s[36:39],s61 offen offset:0 sc1 lds
v_mfma_f32_16x16x32_bf16 v[32:35],a[184:187],v[80:83],v[32:35]
ds_read_b128 v[68:71],v2 offset:20480
v_mul_f32 v88,v44,v88
v_mfma_f32_16x16x32_bf16 v[36:39],a[188:191],v[80:83],v[36:39]
s_waitcnt lgkmcnt(3)
v_exp_f32 v88,v88
v_fma_f32 v89,v118,v89,v117
v_mfma_f32_16x16x32_bf16 v[28:31],a[192:195],v[52:55],v[28:31]
ds_read_b128 v[72:75],v2 offset:21504
v_mul_f32 v89,v45,v89
v_mfma_f32_16x16x32_bf16 v[32:35],a[196:199],v[52:55],v[32:35]
buffer_load_dwordx4 v1,s[36:39],s61 offen offset:1024 sc1 lds
v_mfma_f32_16x16x32_bf16 v[36:39],a[200:203],v[52:55],v[36:39]
v_exp_f32 v89,v89
v_mfma_f32_16x16x32_bf16 v[28:31],a[204:207],v[56:59],v[28:31]
ds_read_b128 v[76:79],v2 offset:22528
v_fma_f32 v88,v88,v119,v119
v_mfma_f32_16x16x32_bf16 v[32:35],a[208:211],v[56:59],v[32:35]
v_rcp_f32 v88,v88
v_mfma_f32_16x16x32_bf16 v[36:39],a[212:215],v[56:59],v[36:39]
s_waitcnt lgkmcnt(3)
buffer_load_dwordx4 v1,s[36:39],s61 offen offset:2048 sc1 lds
v_mfma_f32_16x16x32_bf16 v[28:31],a[216:219],v[60:63],v[28:31]
ds_read_b128 v[80:83],v2 offset:23552
v_fma_f32 v89,v89,v119,v119
v_mfma_f32_16x16x32_bf16 v[32:35],a[220:223],v[60:63],v[32:35]
s_waitcnt vmcnt(7)
v_rcp_f32 v89,v89
v_mfma_f32_16x16x32_bf16 v[36:39],a[224:227],v[60:63],v[36:39]
s_barrier
v_mfma_f32_16x16x32_bf16 v[28:31],a[228:231],v[64:67],v[28:31]
ds_read_b128 v[52:55],v2 offset:24576
v_fma_f32 v90,v118,v90,v117
v_mfma_f32_16x16x32_bf16 v[32:35],a[232:235],v[64:67],v[32:35]
v_mul_f32 v90,v46,v90
v_fma_f32 v91,v118,v91,v117
v_mfma_f32_16x16x32_bf16 v[36:39],a[236:239],v[64:67],v[36:39]
s_waitcnt lgkmcnt(3)
buffer_load_dwordx4 v1,s[36:39],s61 offen offset:3072 sc1 lds
v_mfma_f32_16x16x32_bf16 v[28:31],a[240:243],v[68:71],v[28:31]
buffer_load_dword v108,v8,s[36:39],s72 offen offset:0 sc1
v_mfma_f32_16x16x32_bf16 v[32:35],a[244:247],v[68:71],v[32:35]
ds_read_b128 v[56:59],v2 offset:25600
v_mul_f32 v91,v47,v91
v_mfma_f32_16x16x32_bf16 v[36:39],a[248:251],v[68:71],v[36:39]
v_exp_f32 v90,v90
v_mfma_f32_16x16x32_bf16 v[28:31],a[252:255],v[72:75],v[28:31]
ds_read_b128 v[60:63],v2 offset:26624
v_fma_f32 v90,v90,v119,v119
v_mfma_f32_16x16x32_bf16 v[32:35],v[128:131],v[72:75],v[32:35]
v_exp_f32 v91,v91
v_mfma_f32_16x16x32_bf16 v[36:39],v[132:135],v[72:75],v[36:39]
s_waitcnt lgkmcnt(3)
v_rcp_f32 v90,v90
v_mfma_f32_16x16x32_bf16 v[28:31],v[136:139],v[76:79],v[28:31]
ds_read_b128 v[64:67],v2 offset:27648
v_fma_f32 v91,v91,v119,v119
v_mfma_f32_16x16x32_bf16 v[32:35],v[140:143],v[76:79],v[32:35]
v_rcp_f32 v91,v91
v_mfma_f32_16x16x32_bf16 v[36:39],v[144:147],v[76:79],v[36:39]
v_mul_f32 v44,v44,v88
v_mul_f32 v45,v45,v89
v_mfma_f32_16x16x32_bf16 v[28:31],v[148:151],v[80:83],v[28:31]
ds_read_b128 v[68:71],v2 offset:28672
v_mul_f32 v46,v46,v90
v_mfma_f32_16x16x32_bf16 v[32:35],v[152:155],v[80:83],v[32:35]
v_mul_f32 v47,v47,v91
v_cvt_pk_bf16_f32 v98,v44,v45
v_cvt_pk_bf16_f32 v99,v46,v47
v_mfma_f32_16x16x32_bf16 v[36:39],v[156:159],v[80:83],v[36:39]
s_waitcnt lgkmcnt(3)
v_fma_f32 v48,-v116,v24,v48
v_fma_f32 v49,-v116,v25,v49
v_mfma_f32_16x16x32_bf16 v[28:31],v[160:163],v[52:55],v[28:31]
ds_read_b128 v[72:75],v2 offset:29696
v_fma_f32 v50,-v116,v26,v50
v_mfma_f32_16x16x32_bf16 v[32:35],v[164:167],v[52:55],v[32:35]
v_fma_f32 v51,-v116,v27,v51
v_mul_f32 v92,v48,v48
v_mfma_f32_16x16x32_bf16 v[36:39],v[168:171],v[52:55],v[36:39]
buffer_store_dwordx4 v[96:99],v3,s[36:39],s73 offen offset:0 sc0 sc1
v_mul_f32 v93,v49,v49
v_mul_f32 v94,v50,v50
v_mul_f32 v95,v51,v51
v_mfma_f32_16x16x32_bf16 v[28:31],v[172:175],v[56:59],v[28:31]
ds_read_b128 v[76:79],v2 offset:30720
v_fma_f32 v92,v118,v92,v117
v_mfma_f32_16x16x32_bf16 v[32:35],v[176:179],v[56:59],v[32:35]
v_mul_f32 v92,v48,v92
v_fma_f32 v93,v118,v93,v117
v_mfma_f32_16x16x32_bf16 v[36:39],v[180:183],v[56:59],v[36:39]
s_waitcnt lgkmcnt(3)
v_exp_f32 v92,v92
v_mfma_f32_16x16x32_bf16 v[28:31],v[184:187],v[60:63],v[28:31]
ds_read_b128 v[80:83],v2 offset:31744
v_mul_f32 v93,v49,v93
v_mfma_f32_16x16x32_bf16 v[32:35],v[188:191],v[60:63],v[32:35]
s_waitcnt vmcnt(6)
v_exp_f32 v93,v93
v_mfma_f32_16x16x32_bf16 v[36:39],v[192:195],v[60:63],v[36:39]
s_barrier
v_mfma_f32_16x16x32_bf16 v[28:31],v[196:199],v[64:67],v[28:31]
ds_read_b128 v[52:55],v2 offset:32768
ds_read_b32 v13,v7 offset:0
v_mfma_f32_16x16x32_bf16 v[32:35],v[200:203],v[64:67],v[32:35]
ds_read_b64 v[112:113],v11 offset:0
v_fma_f32 v92,v92,v119,v119
v_mfma_f32_16x16x32_bf16 v[36:39],v[204:207],v[64:67],v[36:39]
s_waitcnt lgkmcnt(5)
v_rcp_f32 v92,v92
v_mfma_f32_16x16x32_bf16 v[28:31],v[208:211],v[68:71],v[28:31]
ds_read_b128 v[56:59],v2 offset:33792
v_fma_f32 v93,v93,v119,v119
v_mfma_f32_16x16x32_bf16 v[32:35],v[212:215],v[68:71],v[32:35]
s_waitcnt lgkmcnt(2)
v_rcp_f32 v93,v93
v_mfma_f32_16x16x32_bf16 v[36:39],v[216:219],v[68:71],v[36:39]
s_waitcnt lgkmcnt(1)
v_cmp_eq_u32 vcc,s60,v13
v_fma_f32 v94,v118,v94,v117
v_mul_f32 v94,v50,v94
v_fma_f32 v95,v118,v95,v117
v_mfma_f32_16x16x32_bf16 v[28:31],v[220:223],v[72:75],v[28:31]
s_cmp_eq_u32 vcc_lo,-1
ds_read_b128 v[60:63],v2 offset:34816
v_mul_f32 v95,v51,v95
v_mfma_f32_16x16x32_bf16 v[32:35],v[224:227],v[72:75],v[32:35]
s_cselect_b32 s67,s86,s59
v_exp_f32 v94,v94
v_mfma_f32_16x16x32_bf16 v[36:39],v[228:231],v[72:75],v[36:39]
v_exp_f32 v95,v95
v_mfma_f32_16x16x32_bf16 v[28:31],v[232:235],v[76:79],v[28:31]
s_lshr_b32 s61,s67,0
ds_read_b128 v[64:67],v2 offset:35840
v_fma_f32 v94,v94,v119,v119
v_mfma_f32_16x16x32_bf16 v[32:35],v[236:239],v[76:79],v[32:35]
s_xor_b32 s63,s63,65536
v_fma_f32 v95,v95,v119,v119
v_rcp_f32 v94,v94
v_rcp_f32 v95,v95
v_mfma_f32_16x16x32_bf16 v[36:39],v[240:243],v[76:79],v[36:39]
s_cmp_ge_u32 s66,s59
v_mul_f32 v48,v48,v92
v_mul_f32 v49,v49,v93
v_mul_f32 v50,v50,v94
v_mul_f32 v51,v51,v95
v_cvt_pk_bf16_f32 v100,v48,v49
v_cvt_pk_bf16_f32 v101,v50,v51
v_mfma_f32_16x16x32_bf16 v[28:31],v[244:247],v[80:83],v[28:31]
buffer_store_dwordx2 v[100:101],v9,s[36:39],s73 offen offset:0 sc0 sc1)"
R"(
ds_read_b128 v[68:71],v2 offset:36864
v_mfma_f32_16x16x32_bf16 v[32:35],v[248:251],v[80:83],v[32:35]
s_waitcnt lgkmcnt(3)
v_mfma_f32_16x16x32_bf16 v[36:39],v[252:255],v[80:83],v[36:39]
s_cbranch_scc1 Lg3_slow1_%=
Lg3_d1w2_%=:
v_mfma_f32_16x16x32_bf16 v[40:43],a[0:3],v[52:55],0
s_mov_b32 m0,s62
ds_read_b128 v[72:75],v2 offset:37888
v_mul_f32 v116,s75,v112
v_mfma_f32_16x16x32_bf16 v[44:47],a[4:7],v[52:55],0
s_mul_i32 s73,s48,7
v_fma_f32 v113,v113,s75,v12
v_fma_f32 v113,-v116,v116,v113
v_mfma_f32_16x16x32_bf16 v[48:51],a[8:11],v[52:55],0
s_lshr_b32 s73,s73,1
v_max_f32 v113,v113,v12
v_rsq_f32 v112,v113
v_mfma_f32_16x16x32_bf16 v[40:43],a[12:15],v[56:59],v[40:43]
buffer_load_dwordx4 v0,s[36:39],s61 offen offset:0 sc1 lds
v_mfma_f32_16x16x32_bf16 v[44:47],a[16:19],v[56:59],v[44:47]
s_add_u32 s74,s53,s55
ds_read_b128 v[76:79],v2 offset:38912
v_mul_f32 v117,s76,v112
v_mfma_f32_16x16x32_bf16 v[48:51],a[20:23],v[56:59],v[48:51]
s_waitcnt lgkmcnt(3)
v_mul_f32 v118,v112,v112
v_mul_f32 v119,v113,v112
v_mfma_f32_16x16x32_bf16 v[40:43],a[24:27],v[60:63],v[40:43]
s_cmp_eq_u32 s74,s56
ds_read_b128 v[80:83],v2 offset:39936
v_mul_f32 v118,s77,v118
v_mfma_f32_16x16x32_bf16 v[44:47],a[28:31],v[60:63],v[44:47]
buffer_load_dwordx4 v0,s[36:39],s61 offen offset:1024 sc1 lds
v_mfma_f32_16x16x32_bf16 v[48:51],a[32:35],v[60:63],v[48:51]
s_cselect_b32 s74,s57,s74
v_mul_f32 v118,v118,v117
v_fma_f32 v28,-v116,v16,v28
v_mfma_f32_16x16x32_bf16 v[40:43],a[36:39],v[64:67],v[40:43]
s_cmp_eq_u32 s74,s58
ds_read_b128 v[52:55],v2 offset:40960
v_fma_f32 v29,-v116,v17,v29
v_mfma_f32_16x16x32_bf16 v[44:47],a[40:43],v[64:67],v[44:47]
s_cselect_b32 s74,s59,s74
v_fma_f32 v30,-v116,v18,v30
v_fma_f32 v31,-v116,v19,v31
v_mfma_f32_16x16x32_bf16 v[48:51],a[44:47],v[64:67],v[48:51]
s_waitcnt lgkmcnt(3)
buffer_load_dwordx4 v0,s[36:39],s61 offen offset:2048 sc1 lds
v_mfma_f32_16x16x32_bf16 v[40:43],a[48:51],v[68:71],v[40:43]
s_cmp_ge_u32 s67,s59
ds_read_b128 v[56:59],v2 offset:41984
v_mul_f32 v84,v28,v28
v_mfma_f32_16x16x32_bf16 v[44:47],a[52:55],v[68:71],v[44:47]
s_cselect_b32 s86,s50,s51
v_mul_f32 v85,v29,v29
v_mul_f32 v86,v30,v30
v_mfma_f32_16x16x32_bf16 v[48:51],a[56:59],v[68:71],v[48:51]
s_mov_b64 s[44:45],s[46:47]
v_mul_f32 v87,v31,v31
v_fma_f32 v84,v118,v84,v117
v_mfma_f32_16x16x32_bf16 v[40:43],a[60:63],v[72:75],v[40:43]
buffer_load_dwordx4 v0,s[36:39],s61 offen offset:3072 sc1 lds
v_mfma_f32_16x16x32_bf16 v[44:47],a[64:67],v[72:75],v[44:47]
s_add_u32 m0,s62,16384
ds_read_b128 v[60:63],v2 offset:43008
v_mul_f32 v84,v28,v84
v_mfma_f32_16x16x32_bf16 v[48:51],a[68:71],v[72:75],v[48:51]
s_waitcnt lgkmcnt(3)
v_exp_f32 v84,v84
v_mfma_f32_16x16x32_bf16 v[40:43],a[72:75],v[76:79],v[40:43]
s_lshr_b32 s72,s61,5
ds_read_b128 v[64:67],v2 offset:44032
v_fma_f32 v84,v84,v119,v119
v_mfma_f32_16x16x32_bf16 v[44:47],a[76:79],v[76:79],v[44:47]
s_mov_b32 s62,s64
v_rcp_f32 v84,v84
v_mfma_f32_16x16x32_bf16 v[48:51],a[80:83],v[76:79],v[48:51]
s_mov_b64 s[46:47],s[48:49]
v_fma_f32 v85,v118,v85,v117
v_mul_f32 v85,v29,v85
v_fma_f32 v86,v118,v86,v117
v_mfma_f32_16x16x32_bf16 v[40:43],a[84:87],v[80:83],v[40:43]
s_mov_b64 s[48:49],s[50:51]
ds_read_b128 v[68:71],v2 offset:45056
v_mul_f32 v86,v30,v86
v_mfma_f32_16x16x32_bf16 v[44:47],a[88:91],v[80:83],v[44:47]
s_mov_b64 s[50:51],s[52:53]
v_exp_f32 v85,v85
v_mfma_f32_16x16x32_bf16 v[48:51],a[92:95],v[80:83],v[48:51]
s_waitcnt lgkmcnt(3)
v_exp_f32 v86,v86
v_mfma_f32_16x16x32_bf16 v[40:43],a[96:99],v[52:55],v[40:43]
s_mov_b32 s52,s74
ds_read_b128 v[72:75],v2 offset:46080
v_fma_f32 v85,v85,v119,v119
v_mfma_f32_16x16x32_bf16 v[44:47],a[100:103],v[52:55],v[44:47]
v_rcp_f32 v85,v85
v_mfma_f32_16x16x32_bf16 v[48:51],a[104:107],v[52:55],v[48:51]
v_fma_f32 v86,v86,v119,v119
v_fma_f32 v87,v118,v87,v117
v_mfma_f32_16x16x32_bf16 v[40:43],a[108:111],v[56:59],v[40:43]
ds_read_b128 v[76:79],v2 offset:47104
v_mul_f32 v87,v31,v87
v_mfma_f32_16x16x32_bf16 v[44:47],a[112:115],v[56:59],v[44:47]
v_exp_f32 v87,v87
v_mfma_f32_16x16x32_bf16 v[48:51],a[116:119],v[56:59],v[48:51]
s_waitcnt lgkmcnt(3)
v_rcp_f32 v86,v86
v_mfma_f32_16x16x32_bf16 v[40:43],a[120:123],v[60:63],v[40:43]
ds_read_b128 v[80:83],v2 offset:48128
v_fma_f32 v87,v87,v119,v119
v_mfma_f32_16x16x32_bf16 v[44:47],a[124:127],v[60:63],v[44:47]
s_waitcnt vmcnt(6)
v_rcp_f32 v87,v87
v_mfma_f32_16x16x32_bf16 v[48:51],a[128:131],v[60:63],v[48:51]
s_barrier
v_mfma_f32_16x16x32_bf16 v[40:43],a[132:135],v[64:67],v[40:43]
ds_read_b128 v[52:55],v2 offset:49152
v_add_f32_dpp v108,v108,v108 quad_perm:[1,0,3,2] row_mask:0xf bank_mask:0xf
v_mfma_f32_16x16x32_bf16 v[44:47],a[136:139],v[64:67],v[44:47]
s_nop 0
v_add_f32_dpp v108,v108,v108 quad_perm:[2,3,0,1] row_mask:0xf bank_mask:0xf
v_mul_f32 v28,v28,v84
v_mfma_f32_16x16x32_bf16 v[48:51],a[140:143],v[64:67],v[48:51]
s_waitcnt lgkmcnt(3)
v_add_f32_dpp v108,v108,v108 row_half_mirror row_mask:0xf bank_mask:0xf
v_mul_f32 v29,v29,v85
v_mfma_f32_16x16x32_bf16 v[40:43],a[144:147],v[68:71],v[40:43]
ds_write_b32 v10,v108 offset:128
v_mfma_f32_16x16x32_bf16 v[44:47],a[148:151],v[68:71],v[44:47]
ds_read_b128 v[56:59],v2 offset:50176
v_mul_f32 v30,v30,v86
v_mfma_f32_16x16x32_bf16 v[48:51],a[152:155],v[68:71],v[48:51]
v_mul_f32 v31,v31,v87
v_cvt_pk_bf16_f32 v96,v28,v29
v_cvt_pk_bf16_f32 v97,v30,v31
v_mfma_f32_16x16x32_bf16 v[40:43],a[156:159],v[72:75],v[40:43]
ds_read_b128 v[60:63],v2 offset:51200
v_fma_f32 v32,-v116,v20,v32
v_mfma_f32_16x16x32_bf16 v[44:47],a[160:163],v[72:75],v[44:47]
v_fma_f32 v33,-v116,v21,v33
v_fma_f32 v34,-v116,v22,v34
v_mfma_f32_16x16x32_bf16 v[48:51],a[164:167],v[72:75],v[48:51]
s_waitcnt lgkmcnt(4)
v_fma_f32 v35,-v116,v23,v35
v_mul_f32 v88,v32,v32
v_mfma_f32_16x16x32_bf16 v[40:43],a[168:171],v[76:79],v[40:43]
ds_read_b128 v[64:67],v2 offset:52224
v_mul_f32 v89,v33,v33
v_mfma_f32_16x16x32_bf16 v[44:47],a[172:175],v[76:79],v[44:47]
v_mul_f32 v90,v34,v34
v_mul_f32 v91,v35,v35
v_mfma_f32_16x16x32_bf16 v[48:51],a[176:179],v[76:79],v[48:51]
v_fma_f32 v88,v118,v88,v117
v_mul_f32 v88,v32,v88
v_mfma_f32_16x16x32_bf16 v[40:43],a[180:183],v[80:83],v[40:43]
buffer_load_dwordx4 v1,s[36:39],s61 offen offset:0 sc1 lds
v_mfma_f32_16x16x32_bf16 v[44:47],a[184:187],v[80:83],v[44:47]
ds_read_b128 v[68:71],v2 offset:53248
v_fma_f32 v89,v118,v89,v117
v_mfma_f32_16x16x32_bf16 v[48:51],a[188:191],v[80:83],v[48:51]
s_waitcnt lgkmcnt(3)
v_exp_f32 v88,v88
v_mfma_f32_16x16x32_bf16 v[40:43],a[192:195],v[52:55],v[40:43]
ds_read_b128 v[72:75],v2 offset:54272
v_mul_f32 v89,v33,v89
v_mfma_f32_16x16x32_bf16 v[44:47],a[196:199],v[52:55],v[44:47]
buffer_load_dwordx4 v1,s[36:39],s61 offen offset:1024 sc1 lds
v_mfma_f32_16x16x32_bf16 v[48:51],a[200:203],v[52:55],v[48:51]
v_exp_f32 v89,v89
v_mfma_f32_16x16x32_bf16 v[40:43],a[204:207],v[56:59],v[40:43]
ds_read_b128 v[76:79],v2 offset:55296
v_fma_f32 v88,v88,v119,v119
v_mfma_f32_16x16x32_bf16 v[44:47],a[208:211],v[56:59],v[44:47]
v_rcp_f32 v88,v88
v_mfma_f32_16x16x32_bf16 v[48:51],a[212:215],v[56:59],v[48:51]
s_waitcnt lgkmcnt(3)
buffer_load_dwordx4 v1,s[36:39],s61 offen offset:2048 sc1 lds
v_mfma_f32_16x16x32_bf16 v[40:43],a[216:219],v[60:63],v[40:43]
ds_read_b128 v[80:83],v2 offset:56320
v_fma_f32 v89,v89,v119,v119
v_mfma_f32_16x16x32_bf16 v[44:47],a[220:223],v[60:63],v[44:47]
s_waitcnt vmcnt(7)
v_rcp_f32 v89,v89
v_mfma_f32_16x16x32_bf16 v[48:51],a[224:227],v[60:63],v[48:51]
s_barrier
v_mfma_f32_16x16x32_bf16 v[40:43],a[228:231],v[64:67],v[40:43]
ds_read_b128 v[52:55],v2 offset:57344
v_fma_f32 v90,v118,v90,v117
v_mfma_f32_16x16x32_bf16 v[44:47],a[232:235],v[64:67],v[44:47]
v_mul_f32 v90,v34,v90
v_fma_f32 v91,v118,v91,v117
v_mfma_f32_16x16x32_bf16 v[48:51],a[236:239],v[64:67],v[48:51]
s_waitcnt lgkmcnt(3)
buffer_load_dwordx4 v1,s[36:39],s61 offen offset:3072 sc1 lds
v_mfma_f32_16x16x32_bf16 v[40:43],a[240:243],v[68:71],v[40:43]
buffer_load_dword v104,v8,s[36:39],s72 offen offset:0 sc1
v_mfma_f32_16x16x32_bf16 v[44:47],a[244:247],v[68:71],v[44:47]
ds_read_b128 v[56:59],v2 offset:58368)"
R"(
v_mul_f32 v91,v35,v91
v_mfma_f32_16x16x32_bf16 v[48:51],a[248:251],v[68:71],v[48:51]
v_exp_f32 v90,v90
v_mfma_f32_16x16x32_bf16 v[40:43],a[252:255],v[72:75],v[40:43]
ds_read_b128 v[60:63],v2 offset:59392
v_fma_f32 v90,v90,v119,v119
v_mfma_f32_16x16x32_bf16 v[44:47],v[128:131],v[72:75],v[44:47]
v_exp_f32 v91,v91
v_mfma_f32_16x16x32_bf16 v[48:51],v[132:135],v[72:75],v[48:51]
s_waitcnt lgkmcnt(3)
v_rcp_f32 v90,v90
v_mfma_f32_16x16x32_bf16 v[40:43],v[136:139],v[76:79],v[40:43]
ds_read_b128 v[64:67],v2 offset:60416
v_fma_f32 v91,v91,v119,v119
v_mfma_f32_16x16x32_bf16 v[44:47],v[140:143],v[76:79],v[44:47]
v_rcp_f32 v91,v91
v_mfma_f32_16x16x32_bf16 v[48:51],v[144:147],v[76:79],v[48:51]
v_mul_f32 v32,v32,v88
v_mul_f32 v33,v33,v89
v_mfma_f32_16x16x32_bf16 v[40:43],v[148:151],v[80:83],v[40:43]
ds_read_b128 v[68:71],v2 offset:61440
v_mul_f32 v34,v34,v90
v_mfma_f32_16x16x32_bf16 v[44:47],v[152:155],v[80:83],v[44:47]
v_mul_f32 v35,v35,v91
v_cvt_pk_bf16_f32 v98,v32,v33
v_mfma_f32_16x16x32_bf16 v[48:51],v[156:159],v[80:83],v[48:51]
s_waitcnt lgkmcnt(3)
v_cvt_pk_bf16_f32 v99,v34,v35
v_fma_f32 v36,-v116,v24,v36
v_mfma_f32_16x16x32_bf16 v[40:43],v[160:163],v[52:55],v[40:43]
ds_read_b128 v[72:75],v2 offset:62464
v_fma_f32 v37,-v116,v25,v37
v_mfma_f32_16x16x32_bf16 v[44:47],v[164:167],v[52:55],v[44:47]
v_fma_f32 v38,-v116,v26,v38
v_fma_f32 v39,-v116,v27,v39
v_mfma_f32_16x16x32_bf16 v[48:51],v[168:171],v[52:55],v[48:51]
buffer_store_dwordx4 v[96:99],v3,s[36:39],s73 offen offset:0 sc0 sc1
v_mul_f32 v92,v36,v36
v_mul_f32 v93,v37,v37
v_mul_f32 v94,v38,v38
v_mfma_f32_16x16x32_bf16 v[40:43],v[172:175],v[56:59],v[40:43]
ds_read_b128 v[76:79],v2 offset:63488
v_mul_f32 v95,v39,v39
v_mfma_f32_16x16x32_bf16 v[44:47],v[176:179],v[56:59],v[44:47]
v_fma_f32 v92,v118,v92,v117
v_mul_f32 v92,v36,v92
v_mfma_f32_16x16x32_bf16 v[48:51],v[180:183],v[56:59],v[48:51]
s_waitcnt lgkmcnt(3)
v_exp_f32 v92,v92
v_mfma_f32_16x16x32_bf16 v[40:43],v[184:187],v[60:63],v[40:43]
ds_read_b128 v[80:83],v2 offset:64512
v_fma_f32 v92,v92,v119,v119
v_mfma_f32_16x16x32_bf16 v[44:47],v[188:191],v[60:63],v[44:47]
s_waitcnt vmcnt(6)
v_xor_b32 v2,65536,v2
v_fma_f32 v93,v118,v93,v117
v_mfma_f32_16x16x32_bf16 v[48:51],v[192:195],v[60:63],v[48:51]
s_barrier
v_mfma_f32_16x16x32_bf16 v[40:43],v[196:199],v[64:67],v[40:43]
ds_read_b128 v[52:55],v2 offset:0
ds_read_b32 v13,v7 offset:256
v_mfma_f32_16x16x32_bf16 v[44:47],v[200:203],v[64:67],v[44:47]
ds_read_b64 v[112:113],v11 offset:128
v_mul_f32 v93,v37,v93
v_mfma_f32_16x16x32_bf16 v[48:51],v[204:207],v[64:67],v[48:51]
s_waitcnt lgkmcnt(5)
v_exp_f32 v93,v93
v_mfma_f32_16x16x32_bf16 v[40:43],v[208:211],v[68:71],v[40:43]
ds_read_b128 v[56:59],v2 offset:1024
v_fma_f32 v93,v93,v119,v119
v_mfma_f32_16x16x32_bf16 v[44:47],v[212:215],v[68:71],v[44:47]
s_waitcnt lgkmcnt(2)
v_rcp_f32 v92,v92
v_mfma_f32_16x16x32_bf16 v[48:51],v[216:219],v[68:71],v[48:51]
s_waitcnt lgkmcnt(1)
v_cmp_eq_u32 vcc,s60,v13
v_rcp_f32 v93,v93
v_fma_f32 v94,v118,v94,v117
v_mfma_f32_16x16x32_bf16 v[40:43],v[220:223],v[72:75],v[40:43]
s_cmp_eq_u32 vcc_lo,-1
ds_read_b128 v[60:63],v2 offset:2048
v_mul_f32 v94,v38,v94
v_mfma_f32_16x16x32_bf16 v[44:47],v[224:227],v[72:75],v[44:47]
s_cselect_b32 s66,s86,s59
v_exp_f32 v94,v94
v_mfma_f32_16x16x32_bf16 v[48:51],v[228:231],v[72:75],v[48:51]
v_fma_f32 v94,v94,v119,v119
v_fma_f32 v95,v118,v95,v117
v_mfma_f32_16x16x32_bf16 v[40:43],v[232:235],v[76:79],v[40:43]
s_lshr_b32 s61,s66,0
v_mul_f32 v95,v39,v95
v_exp_f32 v95,v95
v_mfma_f32_16x16x32_bf16 v[44:47],v[236:239],v[76:79],v[44:47]
s_xor_b32 s64,s64,65536
v_fma_f32 v95,v95,v119,v119
v_rcp_f32 v94,v94
v_rcp_f32 v95,v95
v_mfma_f32_16x16x32_bf16 v[48:51],v[240:243],v[76:79],v[48:51]
s_cmp_lt_u32 s67,s59
ds_read_b128 v[64:67],v2 offset:3072
v_mul_f32 v36,v36,v92
v_mul_f32 v37,v37,v93
v_mul_f32 v38,v38,v94
v_mul_f32 v39,v39,v95
v_cvt_pk_bf16_f32 v100,v36,v37
v_cvt_pk_bf16_f32 v101,v38,v39
v_mfma_f32_16x16x32_bf16 v[40:43],v[244:247],v[80:83],v[40:43]
buffer_store_dwordx2 v[100:101],v9,s[36:39],s73 offen offset:0 sc0 sc1
ds_read_b128 v[68:71],v2 offset:4096
v_mfma_f32_16x16x32_bf16 v[44:47],v[248:251],v[80:83],v[44:47]
s_waitcnt lgkmcnt(3)
v_mfma_f32_16x16x32_bf16 v[48:51],v[252:255],v[80:83],v[48:51]
s_cbranch_scc1 Lg3_d0w2_%=
s_branch Lg3_slow0_%=
Lg3_d0w3_%=:
v_mfma_f32_16x16x32_bf16 v[28:31],a[0:3],v[52:55],0
s_mov_b32 m0,s62
ds_read_b128 v[72:75],v2 offset:5120
v_mul_f32 v116,s75,v112
v_mfma_f32_16x16x32_bf16 v[32:35],a[4:7],v[52:55],0
s_mul_i32 s73,s47,7
v_fma_f32 v113,v113,s75,v12
v_fma_f32 v113,-v116,v116,v113
v_mfma_f32_16x16x32_bf16 v[36:39],a[8:11],v[52:55],0
s_lshr_b32 s73,s73,1
v_max_f32 v113,v113,v12
v_rsq_f32 v112,v113
v_mfma_f32_16x16x32_bf16 v[28:31],a[12:15],v[56:59],v[28:31]
s_add_u32 s53,s52,s55
ds_read_b128 v[76:79],v2 offset:6144
v_mul_f32 v117,s76,v112
v_mfma_f32_16x16x32_bf16 v[32:35],a[16:19],v[56:59],v[32:35]
buffer_load_dwordx4 v0,s[36:39],s61 offen offset:0 sc1 lds
v_mfma_f32_16x16x32_bf16 v[36:39],a[20:23],v[56:59],v[36:39]
s_waitcnt lgkmcnt(3)
v_mul_f32 v118,v112,v112
v_mul_f32 v119,v113,v112
v_mfma_f32_16x16x32_bf16 v[28:31],a[24:27],v[60:63],v[28:31]
s_cmp_eq_u32 s53,s56
ds_read_b128 v[80:83],v2 offset:7168
v_mul_f32 v118,s77,v118
v_mfma_f32_16x16x32_bf16 v[32:35],a[28:31],v[60:63],v[32:35]
s_cselect_b32 s53,s57,s53
v_mul_f32 v118,v118,v117
v_fma_f32 v40,-v116,v16,v40
v_mfma_f32_16x16x32_bf16 v[36:39],a[32:35],v[60:63],v[36:39]
buffer_load_dwordx4 v0,s[36:39],s61 offen offset:1024 sc1 lds
v_mfma_f32_16x16x32_bf16 v[28:31],a[36:39],v[64:67],v[28:31]
s_cmp_eq_u32 s53,s58
ds_read_b128 v[52:55],v2 offset:8192
v_fma_f32 v41,-v116,v17,v41
v_mfma_f32_16x16x32_bf16 v[32:35],a[40:43],v[64:67],v[32:35]
s_cselect_b32 s53,s59,s53
v_fma_f32 v42,-v116,v18,v42
v_fma_f32 v43,-v116,v19,v43
v_mfma_f32_16x16x32_bf16 v[36:39],a[44:47],v[64:67],v[36:39]
s_waitcnt lgkmcnt(3)
v_mul_f32 v84,v40,v40
v_mul_f32 v85,v41,v41
v_mfma_f32_16x16x32_bf16 v[28:31],a[48:51],v[68:71],v[28:31]
buffer_load_dwordx4 v0,s[36:39],s61 offen offset:2048 sc1 lds
v_mfma_f32_16x16x32_bf16 v[32:35],a[52:55],v[68:71],v[32:35]
s_cmp_ge_u32 s66,s59
ds_read_b128 v[56:59],v2 offset:9216
v_mul_f32 v86,v42,v42
v_mfma_f32_16x16x32_bf16 v[36:39],a[56:59],v[68:71],v[36:39]
s_cselect_b32 s86,s49,s50
v_mul_f32 v87,v43,v43
v_fma_f32 v84,v118,v84,v117
v_mfma_f32_16x16x32_bf16 v[28:31],a[60:63],v[72:75],v[28:31]
s_mul_i32 s70,s46,5
ds_read_b128 v[60:63],v2 offset:10240
v_mul_f32 v84,v40,v84
v_mfma_f32_16x16x32_bf16 v[32:35],a[64:67],v[72:75],v[32:35]
buffer_load_dwordx4 v0,s[36:39],s61 offen offset:3072 sc1 lds
v_mfma_f32_16x16x32_bf16 v[36:39],a[68:71],v[72:75],v[36:39]
s_waitcnt lgkmcnt(3)
v_exp_f32 v84,v84
v_mfma_f32_16x16x32_bf16 v[28:31],a[72:75],v[76:79],v[28:31]
s_add_u32 m0,s62,16384
ds_read_b128 v[64:67],v2 offset:11264
v_fma_f32 v84,v84,v119,v119
v_mfma_f32_16x16x32_bf16 v[32:35],a[76:79],v[76:79],v[32:35]
s_lshr_b32 s70,s70,6
v_rcp_f32 v84,v84
v_mfma_f32_16x16x32_bf16 v[36:39],a[80:83],v[76:79],v[36:39]
s_lshr_b32 s72,s61,5
v_fma_f32 v85,v118,v85,v117
v_mul_f32 v85,v41,v85
v_mfma_f32_16x16x32_bf16 v[28:31],a[84:87],v[80:83],v[28:31]
s_mov_b32 s62,s63
ds_read_b128 v[68:71],v2 offset:12288
v_fma_f32 v86,v118,v86,v117
v_mfma_f32_16x16x32_bf16 v[32:35],a[88:91],v[80:83],v[32:35]
v_exp_f32 v85,v85
v_mfma_f32_16x16x32_bf16 v[36:39],a[92:95],v[80:83],v[36:39]
s_waitcnt lgkmcnt(3)
v_mul_f32 v86,v42,v86
v_fma_f32 v85,v85,v119,v119
v_mfma_f32_16x16x32_bf16 v[28:31],a[96:99],v[52:55],v[28:31])"
R"(
ds_read_b128 v[72:75],v2 offset:13312
v_fma_f32 v87,v118,v87,v117
v_mfma_f32_16x16x32_bf16 v[32:35],a[100:103],v[52:55],v[32:35]
v_exp_f32 v86,v86
v_mfma_f32_16x16x32_bf16 v[36:39],a[104:107],v[52:55],v[36:39]
v_rcp_f32 v85,v85
v_mfma_f32_16x16x32_bf16 v[28:31],a[108:111],v[56:59],v[28:31]
ds_read_b128 v[76:79],v2 offset:14336
v_mul_f32 v87,v43,v87
v_mfma_f32_16x16x32_bf16 v[32:35],a[112:115],v[56:59],v[32:35]
v_exp_f32 v87,v87
v_mfma_f32_16x16x32_bf16 v[36:39],a[116:119],v[56:59],v[36:39]
s_waitcnt lgkmcnt(3)
v_fma_f32 v86,v86,v119,v119
v_fma_f32 v87,v87,v119,v119
v_mfma_f32_16x16x32_bf16 v[28:31],a[120:123],v[60:63],v[28:31]
ds_read_b128 v[80:83],v2 offset:15360
v_mul_f32 v40,v40,v84
v_mfma_f32_16x16x32_bf16 v[32:35],a[124:127],v[60:63],v[32:35]
s_waitcnt vmcnt(6)
v_rcp_f32 v86,v86
v_mfma_f32_16x16x32_bf16 v[36:39],a[128:131],v[60:63],v[36:39]
s_barrier
v_mfma_f32_16x16x32_bf16 v[28:31],a[132:135],v[64:67],v[28:31]
ds_read_b128 v[52:55],v2 offset:16384
v_add_f32_dpp v104,v104,v104 quad_perm:[1,0,3,2] row_mask:0xf bank_mask:0xf
v_mfma_f32_16x16x32_bf16 v[32:35],a[136:139],v[64:67],v[32:35]
v_rcp_f32 v87,v87
v_mfma_f32_16x16x32_bf16 v[36:39],a[140:143],v[64:67],v[36:39]
s_waitcnt lgkmcnt(3)
v_add_f32_dpp v104,v104,v104 quad_perm:[2,3,0,1] row_mask:0xf bank_mask:0xf
v_mul_f32 v41,v41,v85
v_mfma_f32_16x16x32_bf16 v[28:31],a[144:147],v[68:71],v[28:31]
ds_read_b128 v[56:59],v2 offset:17408
v_add_f32_dpp v104,v104,v104 row_half_mirror row_mask:0xf bank_mask:0xf
v_mfma_f32_16x16x32_bf16 v[32:35],a[148:151],v[68:71],v[32:35]
ds_write_b32 v10,v104 offset:0
v_mfma_f32_16x16x32_bf16 v[36:39],a[152:155],v[68:71],v[36:39]
v_mul_f32 v42,v42,v86
v_mul_f32 v43,v43,v87
v_mfma_f32_16x16x32_bf16 v[28:31],a[156:159],v[72:75],v[28:31]
ds_read_b128 v[60:63],v2 offset:18432
v_cvt_pk_bf16_f32 v96,v40,v41
v_mfma_f32_16x16x32_bf16 v[32:35],a[160:163],v[72:75],v[32:35]
v_cvt_pk_bf16_f32 v97,v42,v43
v_fma_f32 v44,-v116,v20,v44
v_mfma_f32_16x16x32_bf16 v[36:39],a[164:167],v[72:75],v[36:39]
s_waitcnt lgkmcnt(4)
v_fma_f32 v45,-v116,v21,v45
v_fma_f32 v46,-v116,v22,v46
v_mfma_f32_16x16x32_bf16 v[28:31],a[168:171],v[76:79],v[28:31]
ds_read_b128 v[64:67],v2 offset:19456
v_fma_f32 v47,-v116,v23,v47
v_mfma_f32_16x16x32_bf16 v[32:35],a[172:175],v[76:79],v[32:35]
v_mul_f32 v88,v44,v44
v_mul_f32 v89,v45,v45
v_mfma_f32_16x16x32_bf16 v[36:39],a[176:179],v[76:79],v[36:39]
v_mul_f32 v90,v46,v46
v_mul_f32 v91,v47,v47
v_mfma_f32_16x16x32_bf16 v[28:31],a[180:183],v[80:83],v[28:31]
ds_read_b128 v[68:71],v2 offset:20480
v_fma_f32 v88,v118,v88,v117
v_mfma_f32_16x16x32_bf16 v[32:35],a[184:187],v[80:83],v[32:35]
buffer_load_dwordx4 v1,s[36:39],s61 offen offset:0 sc1 lds
v_mfma_f32_16x16x32_bf16 v[36:39],a[188:191],v[80:83],v[36:39]
s_waitcnt lgkmcnt(4)
v_mul_f32 v88,v44,v88
v_fma_f32 v89,v118,v89,v117
v_mfma_f32_16x16x32_bf16 v[28:31],a[192:195],v[52:55],v[28:31]
ds_read_b128 v[72:75],v2 offset:21504
v_mul_f32 v89,v45,v89
v_mfma_f32_16x16x32_bf16 v[32:35],a[196:199],v[52:55],v[32:35]
v_exp_f32 v88,v88
v_mfma_f32_16x16x32_bf16 v[36:39],a[200:203],v[52:55],v[36:39]
buffer_load_dwordx4 v1,s[36:39],s61 offen offset:1024 sc1 lds
v_mfma_f32_16x16x32_bf16 v[28:31],a[204:207],v[56:59],v[28:31]
ds_read_b128 v[76:79],v2 offset:22528
v_fma_f32 v88,v88,v119,v119
v_mfma_f32_16x16x32_bf16 v[32:35],a[208:211],v[56:59],v[32:35]
v_exp_f32 v89,v89
v_mfma_f32_16x16x32_bf16 v[36:39],a[212:215],v[56:59],v[36:39]
s_waitcnt lgkmcnt(3)
v_rcp_f32 v88,v88
v_mfma_f32_16x16x32_bf16 v[28:31],a[216:219],v[60:63],v[28:31]
buffer_load_dwordx4 v1,s[36:39],s61 offen offset:2048 sc1 lds
v_mfma_f32_16x16x32_bf16 v[32:35],a[220:223],v[60:63],v[32:35]
s_waitcnt vmcnt(7)
ds_read_b128 v[80:83],v2 offset:23552
v_fma_f32 v89,v89,v119,v119
v_mfma_f32_16x16x32_bf16 v[36:39],a[224:227],v[60:63],v[36:39]
s_barrier
v_mfma_f32_16x16x32_bf16 v[28:31],a[228:231],v[64:67],v[28:31]
buffer_store_dword v5,v4,s[36:39],s70 offen offset:0 sc0 sc1
ds_read_b128 v[52:55],v2 offset:24576
v_rcp_f32 v89,v89
v_mfma_f32_16x16x32_bf16 v[32:35],a[232:235],v[64:67],v[32:35]
v_fma_f32 v90,v118,v90,v117
v_mul_f32 v90,v46,v90
v_mfma_f32_16x16x32_bf16 v[36:39],a[236:239],v[64:67],v[36:39]
s_waitcnt lgkmcnt(3)
v_exp_f32 v90,v90
v_mfma_f32_16x16x32_bf16 v[28:31],a[240:243],v[68:71],v[28:31]
buffer_load_dwordx4 v1,s[36:39],s61 offen offset:3072 sc1 lds
v_mfma_f32_16x16x32_bf16 v[32:35],a[244:247],v[68:71],v[32:35]
buffer_load_dword v108,v8,s[36:39],s72 offen offset:0 sc1
v_mfma_f32_16x16x32_bf16 v[36:39],a[248:251],v[68:71],v[36:39]
ds_read_b128 v[56:59],v2 offset:25600
v_fma_f32 v90,v90,v119,v119
v_mfma_f32_16x16x32_bf16 v[28:31],a[252:255],v[72:75],v[28:31]
ds_read_b128 v[60:63],v2 offset:26624
v_fma_f32 v91,v118,v91,v117
v_mfma_f32_16x16x32_bf16 v[32:35],v[128:131],v[72:75],v[32:35]
v_rcp_f32 v90,v90
v_mfma_f32_16x16x32_bf16 v[36:39],v[132:135],v[72:75],v[36:39]
s_waitcnt lgkmcnt(3)
v_mul_f32 v91,v47,v91
v_mul_f32 v44,v44,v88
v_mfma_f32_16x16x32_bf16 v[28:31],v[136:139],v[76:79],v[28:31]
ds_read_b128 v[64:67],v2 offset:27648
v_mul_f32 v45,v45,v89
v_mfma_f32_16x16x32_bf16 v[32:35],v[140:143],v[76:79],v[32:35]
v_exp_f32 v91,v91
v_mfma_f32_16x16x32_bf16 v[36:39],v[144:147],v[76:79],v[36:39]
v_fma_f32 v91,v91,v119,v119
v_mul_f32 v46,v46,v90
v_mfma_f32_16x16x32_bf16 v[28:31],v[148:151],v[80:83],v[28:31]
ds_read_b128 v[68:71],v2 offset:28672
v_cvt_pk_bf16_f32 v98,v44,v45
v_mfma_f32_16x16x32_bf16 v[32:35],v[152:155],v[80:83],v[32:35]
v_rcp_f32 v91,v91
v_mfma_f32_16x16x32_bf16 v[36:39],v[156:159],v[80:83],v[36:39]
s_waitcnt lgkmcnt(3)
v_mul_f32 v47,v47,v91
v_cvt_pk_bf16_f32 v99,v46,v47
v_mfma_f32_16x16x32_bf16 v[28:31],v[160:163],v[52:55],v[28:31]
ds_read_b128 v[72:75],v2 offset:29696
v_fma_f32 v48,-v116,v24,v48
v_mfma_f32_16x16x32_bf16 v[32:35],v[164:167],v[52:55],v[32:35]
v_fma_f32 v49,-v116,v25,v49
v_fma_f32 v50,-v116,v26,v50
v_mfma_f32_16x16x32_bf16 v[36:39],v[168:171],v[52:55],v[36:39]
v_fma_f32 v51,-v116,v27,v51
v_mul_f32 v92,v48,v48
v_mfma_f32_16x16x32_bf16 v[28:31],v[172:175],v[56:59],v[28:31]
buffer_store_dwordx4 v[96:99],v3,s[36:39],s73 offen offset:0 sc0 sc1
ds_read_b128 v[76:79],v2 offset:30720
v_mul_f32 v93,v49,v49
v_mul_f32 v94,v50,v50
v_mfma_f32_16x16x32_bf16 v[32:35],v[176:179],v[56:59],v[32:35]
v_mul_f32 v95,v51,v51
v_fma_f32 v92,v118,v92,v117
v_mfma_f32_16x16x32_bf16 v[36:39],v[180:183],v[56:59],v[36:39]
s_waitcnt lgkmcnt(3)
v_mul_f32 v92,v48,v92
v_fma_f32 v93,v118,v93,v117
v_mfma_f32_16x16x32_bf16 v[28:31],v[184:187],v[60:63],v[28:31]
ds_read_b128 v[80:83],v2 offset:31744
v_mul_f32 v93,v49,v93
v_mfma_f32_16x16x32_bf16 v[32:35],v[188:191],v[60:63],v[32:35]
s_waitcnt vmcnt(7)
v_exp_f32 v92,v92
v_mfma_f32_16x16x32_bf16 v[36:39],v[192:195],v[60:63],v[36:39]
s_barrier
v_mfma_f32_16x16x32_bf16 v[28:31],v[196:199],v[64:67],v[28:31]
ds_read_b128 v[52:55],v2 offset:32768
ds_read_b32 v13,v7 offset:0
v_mfma_f32_16x16x32_bf16 v[32:35],v[200:203],v[64:67],v[32:35]
ds_read_b64 v[112:113],v11 offset:0
v_fma_f32 v92,v92,v119,v119
v_mfma_f32_16x16x32_bf16 v[36:39],v[204:207],v[64:67],v[36:39]
s_waitcnt lgkmcnt(5)
v_exp_f32 v93,v93
v_mfma_f32_16x16x32_bf16 v[28:31],v[208:211],v[68:71],v[28:31]
ds_read_b128 v[56:59],v2 offset:33792
v_fma_f32 v93,v93,v119,v119
v_mfma_f32_16x16x32_bf16 v[32:35],v[212:215],v[68:71],v[32:35]
s_waitcnt lgkmcnt(2)
v_rcp_f32 v92,v92
v_mfma_f32_16x16x32_bf16 v[36:39],v[216:219],v[68:71],v[36:39]
s_waitcnt lgkmcnt(1)
v_cmp_eq_u32 vcc,s60,v13
v_rcp_f32 v93,v93
v_fma_f32 v94,v118,v94,v117
v_mfma_f32_16x16x32_bf16 v[28:31],v[220:223],v[72:75],v[28:31]
s_cmp_eq_u32 vcc_lo,-1
ds_read_b128 v[60:63],v2 offset:34816
v_mul_f32 v94,v50,v94
v_mfma_f32_16x16x32_bf16 v[32:35],v[224:227],v[72:75],v[32:35]
s_cselect_b32 s67,s86,s59
v_exp_f32 v94,v94
v_mfma_f32_16x16x32_bf16 v[36:39],v[228:231],v[72:75],v[36:39]
v_fma_f32 v94,v94,v119,v119
v_fma_f32 v95,v118,v95,v117
v_mfma_f32_16x16x32_bf16 v[28:31],v[232:235],v[76:79],v[28:31]
s_lshr_b32 s61,s67,0
ds_read_b128 v[64:67],v2 offset:35840
v_mul_f32 v95,v51,v95
v_mfma_f32_16x16x32_bf16 v[32:35],v[236:239],v[76:79],v[32:35]
s_xor_b32 s63,s63,65536
v_exp_f32 v95,v95)"
R"(
v_mfma_f32_16x16x32_bf16 v[36:39],v[240:243],v[76:79],v[36:39]
s_cmp_ge_u32 s66,s59
v_fma_f32 v95,v95,v119,v119
v_rcp_f32 v94,v94
v_rcp_f32 v95,v95
v_mfma_f32_16x16x32_bf16 v[28:31],v[244:247],v[80:83],v[28:31]
v_mul_f32 v48,v48,v92
v_mul_f32 v49,v49,v93
v_mul_f32 v50,v50,v94
v_mul_f32 v51,v51,v95
v_cvt_pk_bf16_f32 v100,v48,v49
v_cvt_pk_bf16_f32 v101,v50,v51
v_mfma_f32_16x16x32_bf16 v[32:35],v[248:251],v[80:83],v[32:35]
s_waitcnt lgkmcnt(2)
ds_read_b128 v[68:71],v2 offset:36864
buffer_store_dwordx2 v[100:101],v9,s[36:39],s73 offen offset:0 sc0 sc1
v_mfma_f32_16x16x32_bf16 v[36:39],v[252:255],v[80:83],v[36:39]
s_cbranch_scc1 Lg3_slow1_%=
Lg3_d1w3_%=:
v_mfma_f32_16x16x32_bf16 v[40:43],a[0:3],v[52:55],0
s_mov_b32 m0,s62
ds_read_b128 v[72:75],v2 offset:37888
v_mul_f32 v116,s75,v112
v_mfma_f32_16x16x32_bf16 v[44:47],a[4:7],v[52:55],0
s_mul_i32 s73,s48,7
v_fma_f32 v113,v113,s75,v12
v_fma_f32 v113,-v116,v116,v113
v_mfma_f32_16x16x32_bf16 v[48:51],a[8:11],v[52:55],0
s_lshr_b32 s73,s73,1
v_max_f32 v113,v113,v12
v_rsq_f32 v112,v113
v_mfma_f32_16x16x32_bf16 v[40:43],a[12:15],v[56:59],v[40:43]
s_add_u32 s74,s53,s55
ds_read_b128 v[76:79],v2 offset:38912
v_mul_f32 v117,s76,v112
v_mfma_f32_16x16x32_bf16 v[44:47],a[16:19],v[56:59],v[44:47]
buffer_load_dwordx4 v0,s[36:39],s61 offen offset:0 sc1 lds
v_mfma_f32_16x16x32_bf16 v[48:51],a[20:23],v[56:59],v[48:51]
s_waitcnt lgkmcnt(3)
v_mul_f32 v118,v112,v112
v_mul_f32 v119,v113,v112
v_mfma_f32_16x16x32_bf16 v[40:43],a[24:27],v[60:63],v[40:43]
s_cmp_eq_u32 s74,s56
ds_read_b128 v[80:83],v2 offset:39936
v_mul_f32 v118,s77,v118
v_mfma_f32_16x16x32_bf16 v[44:47],a[28:31],v[60:63],v[44:47]
s_cselect_b32 s74,s57,s74
v_mul_f32 v118,v118,v117
v_fma_f32 v28,-v116,v16,v28
v_mfma_f32_16x16x32_bf16 v[48:51],a[32:35],v[60:63],v[48:51]
buffer_load_dwordx4 v0,s[36:39],s61 offen offset:1024 sc1 lds
v_mfma_f32_16x16x32_bf16 v[40:43],a[36:39],v[64:67],v[40:43]
s_cmp_eq_u32 s74,s58
ds_read_b128 v[52:55],v2 offset:40960
v_fma_f32 v29,-v116,v17,v29
v_mfma_f32_16x16x32_bf16 v[44:47],a[40:43],v[64:67],v[44:47]
s_cselect_b32 s74,s59,s74
v_fma_f32 v30,-v116,v18,v30
v_fma_f32 v31,-v116,v19,v31
v_mfma_f32_16x16x32_bf16 v[48:51],a[44:47],v[64:67],v[48:51]
s_waitcnt lgkmcnt(3)
v_mul_f32 v84,v28,v28
v_mul_f32 v85,v29,v29
v_mfma_f32_16x16x32_bf16 v[40:43],a[48:51],v[68:71],v[40:43]
buffer_load_dwordx4 v0,s[36:39],s61 offen offset:2048 sc1 lds
v_mfma_f32_16x16x32_bf16 v[44:47],a[52:55],v[68:71],v[44:47]
s_cmp_ge_u32 s67,s59
ds_read_b128 v[56:59],v2 offset:41984
v_mul_f32 v86,v30,v30
v_mfma_f32_16x16x32_bf16 v[48:51],a[56:59],v[68:71],v[48:51]
s_cselect_b32 s86,s50,s51
v_mul_f32 v87,v31,v31
v_fma_f32 v84,v118,v84,v117
v_mfma_f32_16x16x32_bf16 v[40:43],a[60:63],v[72:75],v[40:43]
s_mul_i32 s70,s47,5
ds_read_b128 v[60:63],v2 offset:43008
v_mul_f32 v84,v28,v84
v_mfma_f32_16x16x32_bf16 v[44:47],a[64:67],v[72:75],v[44:47]
buffer_load_dwordx4 v0,s[36:39],s61 offen offset:3072 sc1 lds
v_mfma_f32_16x16x32_bf16 v[48:51],a[68:71],v[72:75],v[48:51]
s_waitcnt lgkmcnt(3)
v_exp_f32 v84,v84
v_mfma_f32_16x16x32_bf16 v[40:43],a[72:75],v[76:79],v[40:43]
s_add_u32 m0,s62,16384
ds_read_b128 v[64:67],v2 offset:44032
v_fma_f32 v84,v84,v119,v119
v_mfma_f32_16x16x32_bf16 v[44:47],a[76:79],v[76:79],v[44:47]
s_lshr_b32 s70,s70,6
v_rcp_f32 v84,v84
v_mfma_f32_16x16x32_bf16 v[48:51],a[80:83],v[76:79],v[48:51]
s_lshr_b32 s72,s61,5
v_fma_f32 v85,v118,v85,v117
v_mul_f32 v85,v29,v85
v_mfma_f32_16x16x32_bf16 v[40:43],a[84:87],v[80:83],v[40:43]
s_mov_b32 s62,s64
ds_read_b128 v[68:71],v2 offset:45056
v_fma_f32 v86,v118,v86,v117
v_mfma_f32_16x16x32_bf16 v[44:47],a[88:91],v[80:83],v[44:47]
s_mov_b64 s[44:45],s[46:47]
v_exp_f32 v85,v85
v_mfma_f32_16x16x32_bf16 v[48:51],a[92:95],v[80:83],v[48:51]
s_waitcnt lgkmcnt(3)
v_mul_f32 v86,v30,v86
v_fma_f32 v85,v85,v119,v119
v_mfma_f32_16x16x32_bf16 v[40:43],a[96:99],v[52:55],v[40:43]
s_mov_b64 s[46:47],s[48:49]
ds_read_b128 v[72:75],v2 offset:46080
v_fma_f32 v87,v118,v87,v117
v_mfma_f32_16x16x32_bf16 v[44:47],a[100:103],v[52:55],v[44:47]
s_mov_b64 s[48:49],s[50:51]
v_exp_f32 v86,v86
v_mfma_f32_16x16x32_bf16 v[48:51],a[104:107],v[52:55],v[48:51]
s_mov_b64 s[50:51],s[52:53]
v_rcp_f32 v85,v85
v_mfma_f32_16x16x32_bf16 v[40:43],a[108:111],v[56:59],v[40:43]
s_mov_b32 s52,s74
ds_read_b128 v[76:79],v2 offset:47104
v_mul_f32 v87,v31,v87
v_mfma_f32_16x16x32_bf16 v[44:47],a[112:115],v[56:59],v[44:47]
v_exp_f32 v87,v87
v_mfma_f32_16x16x32_bf16 v[48:51],a[116:119],v[56:59],v[48:51]
s_waitcnt lgkmcnt(3)
v_fma_f32 v86,v86,v119,v119
v_fma_f32 v87,v87,v119,v119
v_mfma_f32_16x16x32_bf16 v[40:43],a[120:123],v[60:63],v[40:43]
ds_read_b128 v[80:83],v2 offset:48128
v_mul_f32 v28,v28,v84
v_mfma_f32_16x16x32_bf16 v[44:47],a[124:127],v[60:63],v[44:47]
s_waitcnt vmcnt(6)
v_rcp_f32 v86,v86
v_mfma_f32_16x16x32_bf16 v[48:51],a[128:131],v[60:63],v[48:51]
s_barrier
v_mfma_f32_16x16x32_bf16 v[40:43],a[132:135],v[64:67],v[40:43]
ds_read_b128 v[52:55],v2 offset:49152
v_add_f32_dpp v108,v108,v108 quad_perm:[1,0,3,2] row_mask:0xf bank_mask:0xf
v_mfma_f32_16x16x32_bf16 v[44:47],a[136:139],v[64:67],v[44:47]
v_rcp_f32 v87,v87
v_mfma_f32_16x16x32_bf16 v[48:51],a[140:143],v[64:67],v[48:51]
s_waitcnt lgkmcnt(3)
v_add_f32_dpp v108,v108,v108 quad_perm:[2,3,0,1] row_mask:0xf bank_mask:0xf
v_mul_f32 v29,v29,v85
v_mfma_f32_16x16x32_bf16 v[40:43],a[144:147],v[68:71],v[40:43]
ds_read_b128 v[56:59],v2 offset:50176
v_add_f32_dpp v108,v108,v108 row_half_mirror row_mask:0xf bank_mask:0xf
v_mfma_f32_16x16x32_bf16 v[44:47],a[148:151],v[68:71],v[44:47]
ds_write_b32 v10,v108 offset:128
v_mfma_f32_16x16x32_bf16 v[48:51],a[152:155],v[68:71],v[48:51]
v_mul_f32 v30,v30,v86
v_mul_f32 v31,v31,v87
v_mfma_f32_16x16x32_bf16 v[40:43],a[156:159],v[72:75],v[40:43]
ds_read_b128 v[60:63],v2 offset:51200
v_cvt_pk_bf16_f32 v96,v28,v29
v_mfma_f32_16x16x32_bf16 v[44:47],a[160:163],v[72:75],v[44:47]
v_cvt_pk_bf16_f32 v97,v30,v31
v_fma_f32 v32,-v116,v20,v32
v_mfma_f32_16x16x32_bf16 v[48:51],a[164:167],v[72:75],v[48:51]
s_waitcnt lgkmcnt(4)
v_fma_f32 v33,-v116,v21,v33
v_fma_f32 v34,-v116,v22,v34
v_mfma_f32_16x16x32_bf16 v[40:43],a[168:171],v[76:79],v[40:43]
ds_read_b128 v[64:67],v2 offset:52224
v_fma_f32 v35,-v116,v23,v35
v_mfma_f32_16x16x32_bf16 v[44:47],a[172:175],v[76:79],v[44:47]
v_mul_f32 v88,v32,v32
v_mul_f32 v89,v33,v33
v_mfma_f32_16x16x32_bf16 v[48:51],a[176:179],v[76:79],v[48:51]
v_mul_f32 v90,v34,v34
v_mul_f32 v91,v35,v35
v_mfma_f32_16x16x32_bf16 v[40:43],a[180:183],v[80:83],v[40:43]
ds_read_b128 v[68:71],v2 offset:53248
v_fma_f32 v88,v118,v88,v117
v_mfma_f32_16x16x32_bf16 v[44:47],a[184:187],v[80:83],v[44:47]
buffer_load_dwordx4 v1,s[36:39],s61 offen offset:0 sc1 lds
v_mfma_f32_16x16x32_bf16 v[48:51],a[188:191],v[80:83],v[48:51]
s_waitcnt lgkmcnt(4)
v_mul_f32 v88,v32,v88
v_exp_f32 v88,v88
v_mfma_f32_16x16x32_bf16 v[40:43],a[192:195],v[52:55],v[40:43]
ds_read_b128 v[72:75],v2 offset:54272
v_fma_f32 v88,v88,v119,v119
v_mfma_f32_16x16x32_bf16 v[44:47],a[196:199],v[52:55],v[44:47]
v_rcp_f32 v88,v88
v_mfma_f32_16x16x32_bf16 v[48:51],a[200:203],v[52:55],v[48:51]
buffer_load_dwordx4 v1,s[36:39],s61 offen offset:1024 sc1 lds
v_mfma_f32_16x16x32_bf16 v[40:43],a[204:207],v[56:59],v[40:43]
ds_read_b128 v[76:79],v2 offset:55296
v_fma_f32 v89,v118,v89,v117
v_mfma_f32_16x16x32_bf16 v[44:47],a[208:211],v[56:59],v[44:47]
v_mul_f32 v89,v33,v89
v_fma_f32 v90,v118,v90,v117
v_mfma_f32_16x16x32_bf16 v[48:51],a[212:215],v[56:59],v[48:51]
s_waitcnt lgkmcnt(3)
v_exp_f32 v89,v89
v_mfma_f32_16x16x32_bf16 v[40:43],a[216:219],v[60:63],v[40:43]
buffer_load_dwordx4 v1,s[36:39],s61 offen offset:2048 sc1 lds
v_mfma_f32_16x16x32_bf16 v[44:47],a[220:223],v[60:63],v[44:47]
s_waitcnt vmcnt(7)
ds_read_b128 v[80:83],v2 offset:56320
v_mul_f32 v90,v34,v90
v_mfma_f32_16x16x32_bf16 v[48:51],a[224:227],v[60:63],v[48:51])"
R"(
s_barrier
v_mfma_f32_16x16x32_bf16 v[40:43],a[228:231],v[64:67],v[40:43]
buffer_store_dword v5,v4,s[36:39],s70 offen offset:0 sc0 sc1
ds_read_b128 v[52:55],v2 offset:57344
v_exp_f32 v90,v90
v_mfma_f32_16x16x32_bf16 v[44:47],a[232:235],v[64:67],v[44:47]
v_fma_f32 v89,v89,v119,v119
v_fma_f32 v90,v90,v119,v119
v_mfma_f32_16x16x32_bf16 v[48:51],a[236:239],v[64:67],v[48:51]
s_waitcnt lgkmcnt(3)
v_rcp_f32 v89,v89
v_mfma_f32_16x16x32_bf16 v[40:43],a[240:243],v[68:71],v[40:43]
buffer_load_dwordx4 v1,s[36:39],s61 offen offset:3072 sc1 lds
v_mfma_f32_16x16x32_bf16 v[44:47],a[244:247],v[68:71],v[44:47]
buffer_load_dword v104,v8,s[36:39],s72 offen offset:0 sc1
v_mfma_f32_16x16x32_bf16 v[48:51],a[248:251],v[68:71],v[48:51]
ds_read_b128 v[56:59],v2 offset:58368
v_fma_f32 v91,v118,v91,v117
v_mfma_f32_16x16x32_bf16 v[40:43],a[252:255],v[72:75],v[40:43]
ds_read_b128 v[60:63],v2 offset:59392
v_mul_f32 v91,v35,v91
v_mfma_f32_16x16x32_bf16 v[44:47],v[128:131],v[72:75],v[44:47]
v_exp_f32 v91,v91
v_mfma_f32_16x16x32_bf16 v[48:51],v[132:135],v[72:75],v[48:51]
s_waitcnt lgkmcnt(3)
v_rcp_f32 v90,v90
v_mfma_f32_16x16x32_bf16 v[40:43],v[136:139],v[76:79],v[40:43]
ds_read_b128 v[64:67],v2 offset:60416
v_fma_f32 v91,v91,v119,v119
v_mfma_f32_16x16x32_bf16 v[44:47],v[140:143],v[76:79],v[44:47]
v_rcp_f32 v91,v91
v_mfma_f32_16x16x32_bf16 v[48:51],v[144:147],v[76:79],v[48:51]
v_mul_f32 v32,v32,v88
v_mul_f32 v33,v33,v89
v_mfma_f32_16x16x32_bf16 v[40:43],v[148:151],v[80:83],v[40:43]
ds_read_b128 v[68:71],v2 offset:61440
v_mul_f32 v34,v34,v90
v_mfma_f32_16x16x32_bf16 v[44:47],v[152:155],v[80:83],v[44:47]
v_mul_f32 v35,v35,v91
v_cvt_pk_bf16_f32 v98,v32,v33
v_mfma_f32_16x16x32_bf16 v[48:51],v[156:159],v[80:83],v[48:51]
s_waitcnt lgkmcnt(3)
v_cvt_pk_bf16_f32 v99,v34,v35
v_fma_f32 v36,-v116,v24,v36
v_mfma_f32_16x16x32_bf16 v[40:43],v[160:163],v[52:55],v[40:43]
ds_read_b128 v[72:75],v2 offset:62464
v_fma_f32 v37,-v116,v25,v37
v_mfma_f32_16x16x32_bf16 v[44:47],v[164:167],v[52:55],v[44:47]
v_fma_f32 v38,-v116,v26,v38
v_fma_f32 v39,-v116,v27,v39
v_mfma_f32_16x16x32_bf16 v[48:51],v[168:171],v[52:55],v[48:51]
v_mul_f32 v92,v36,v36
v_mul_f32 v93,v37,v37
v_mfma_f32_16x16x32_bf16 v[40:43],v[172:175],v[56:59],v[40:43]
buffer_store_dwordx4 v[96:99],v3,s[36:39],s73 offen offset:0 sc0 sc1
ds_read_b128 v[76:79],v2 offset:63488
v_mul_f32 v94,v38,v38
v_mul_f32 v95,v39,v39
v_mfma_f32_16x16x32_bf16 v[44:47],v[176:179],v[56:59],v[44:47]
v_fma_f32 v92,v118,v92,v117
v_mul_f32 v92,v36,v92
v_mfma_f32_16x16x32_bf16 v[48:51],v[180:183],v[56:59],v[48:51]
s_waitcnt lgkmcnt(3)
v_exp_f32 v92,v92
v_mfma_f32_16x16x32_bf16 v[40:43],v[184:187],v[60:63],v[40:43]
ds_read_b128 v[80:83],v2 offset:64512
v_fma_f32 v92,v92,v119,v119
v_mfma_f32_16x16x32_bf16 v[44:47],v[188:191],v[60:63],v[44:47]
s_waitcnt vmcnt(7)
v_xor_b32 v2,65536,v2
v_fma_f32 v93,v118,v93,v117
v_mfma_f32_16x16x32_bf16 v[48:51],v[192:195],v[60:63],v[48:51]
s_barrier
v_mfma_f32_16x16x32_bf16 v[40:43],v[196:199],v[64:67],v[40:43]
ds_read_b128 v[52:55],v2 offset:0
ds_read_b32 v13,v7 offset:256
v_mfma_f32_16x16x32_bf16 v[44:47],v[200:203],v[64:67],v[44:47]
ds_read_b64 v[112:113],v11 offset:128
v_mul_f32 v93,v37,v93
v_mfma_f32_16x16x32_bf16 v[48:51],v[204:207],v[64:67],v[48:51]
s_waitcnt lgkmcnt(5)
v_exp_f32 v93,v93
v_mfma_f32_16x16x32_bf16 v[40:43],v[208:211],v[68:71],v[40:43]
ds_read_b128 v[56:59],v2 offset:1024
v_fma_f32 v93,v93,v119,v119
v_mfma_f32_16x16x32_bf16 v[44:47],v[212:215],v[68:71],v[44:47]
s_waitcnt lgkmcnt(2)
v_rcp_f32 v92,v92
v_mfma_f32_16x16x32_bf16 v[48:51],v[216:219],v[68:71],v[48:51]
s_waitcnt lgkmcnt(1)
v_cmp_eq_u32 vcc,s60,v13
v_rcp_f32 v93,v93
v_fma_f32 v94,v118,v94,v117
v_mfma_f32_16x16x32_bf16 v[40:43],v[220:223],v[72:75],v[40:43]
s_cmp_eq_u32 vcc_lo,-1
ds_read_b128 v[60:63],v2 offset:2048
v_mul_f32 v94,v38,v94
v_mfma_f32_16x16x32_bf16 v[44:47],v[224:227],v[72:75],v[44:47]
s_cselect_b32 s66,s86,s59
v_exp_f32 v94,v94
v_mfma_f32_16x16x32_bf16 v[48:51],v[228:231],v[72:75],v[48:51]
v_fma_f32 v94,v94,v119,v119
v_fma_f32 v95,v118,v95,v117
v_mfma_f32_16x16x32_bf16 v[40:43],v[232:235],v[76:79],v[40:43]
s_lshr_b32 s61,s66,0
ds_read_b128 v[64:67],v2 offset:3072
v_mul_f32 v95,v39,v95
v_mfma_f32_16x16x32_bf16 v[44:47],v[236:239],v[76:79],v[44:47]
s_xor_b32 s64,s64,65536
v_exp_f32 v95,v95
v_mfma_f32_16x16x32_bf16 v[48:51],v[240:243],v[76:79],v[48:51]
s_cmp_lt_u32 s67,s59
v_fma_f32 v95,v95,v119,v119
v_rcp_f32 v94,v94
v_rcp_f32 v95,v95
v_mfma_f32_16x16x32_bf16 v[40:43],v[244:247],v[80:83],v[40:43]
v_mul_f32 v36,v36,v92
v_mul_f32 v37,v37,v93
v_mul_f32 v38,v38,v94
v_mul_f32 v39,v39,v95
v_cvt_pk_bf16_f32 v100,v36,v37
v_cvt_pk_bf16_f32 v101,v38,v39
v_mfma_f32_16x16x32_bf16 v[44:47],v[248:251],v[80:83],v[44:47]
s_waitcnt lgkmcnt(2)
ds_read_b128 v[68:71],v2 offset:4096
buffer_store_dwordx2 v[100:101],v9,s[36:39],s73 offen offset:0 sc0 sc1
v_mfma_f32_16x16x32_bf16 v[48:51],v[252:255],v[80:83],v[48:51]
s_cbranch_scc1 Lg3_d0w3_%=
s_branch Lg3_slow0_%=
Lg3_slow0_%=:
s_cmp_lt_u32 s66,s59
s_cbranch_scc1 Lg3_gs0_%=
s_cmp_eq_u32 s54,1
s_cselect_b32 s68,s49,s48
s_lshr_b32 s68,s68,5
s_add_u32 m0,s78,8192
s_nop 0
buffer_load_dword v6,s[36:39],s68 offen offset:0 sc1 lds
s_mul_i32 s73,s47,7
s_lshr_b32 s73,s73,1
v_mul_f32 v116,s75,v112
v_fma_f32 v113,v113,s75,v12
v_fma_f32 v113,-v116,v116,v113
v_max_f32 v113,v113,v12
v_rsq_f32 v112,v113
s_nop 0
v_mul_f32 v117,s76,v112
v_mul_f32 v118,v112,v112
v_mul_f32 v119,v113,v112
v_mul_f32 v118,s77,v118
v_mul_f32 v118,v118,v117
v_fma_f32 v40,-v116,v16,v40
v_fma_f32 v41,-v116,v17,v41
v_fma_f32 v42,-v116,v18,v42
v_fma_f32 v43,-v116,v19,v43
v_mul_f32 v84,v40,v40
v_mul_f32 v85,v41,v41
v_mul_f32 v86,v42,v42
v_mul_f32 v87,v43,v43
v_fma_f32 v84,v118,v84,v117
v_fma_f32 v85,v118,v85,v117
v_fma_f32 v86,v118,v86,v117
v_fma_f32 v87,v118,v87,v117
v_mul_f32 v84,v40,v84
v_mul_f32 v85,v41,v85
v_mul_f32 v86,v42,v86
v_mul_f32 v87,v43,v87
v_exp_f32 v84,v84
v_exp_f32 v85,v85
v_exp_f32 v86,v86
v_exp_f32 v87,v87
v_fma_f32 v84,v84,v119,v119
v_fma_f32 v85,v85,v119,v119
v_fma_f32 v86,v86,v119,v119
v_fma_f32 v87,v87,v119,v119
v_rcp_f32 v84,v84
v_rcp_f32 v85,v85
v_rcp_f32 v86,v86
v_rcp_f32 v87,v87
v_mul_f32 v40,v40,v84
v_mul_f32 v41,v41,v85
v_mul_f32 v42,v42,v86
v_mul_f32 v43,v43,v87
v_cvt_pk_bf16_f32 v96,v40,v41
v_cvt_pk_bf16_f32 v97,v42,v43
v_fma_f32 v44,-v116,v20,v44
v_fma_f32 v45,-v116,v21,v45
v_fma_f32 v46,-v116,v22,v46
v_fma_f32 v47,-v116,v23,v47
v_mul_f32 v88,v44,v44
v_mul_f32 v89,v45,v45
v_mul_f32 v90,v46,v46
v_mul_f32 v91,v47,v47
v_fma_f32 v88,v118,v88,v117
v_fma_f32 v89,v118,v89,v117
v_fma_f32 v90,v118,v90,v117
v_fma_f32 v91,v118,v91,v117
v_mul_f32 v88,v44,v88
s_add_u32 m0,s78,8448
s_nop 0)"
R"(
buffer_load_dword v6,s[36:39],s68 offen offset:0 sc1 lds
v_mul_f32 v89,v45,v89
v_mul_f32 v90,v46,v90
v_mul_f32 v91,v47,v91
v_exp_f32 v88,v88
v_exp_f32 v89,v89
v_exp_f32 v90,v90
v_exp_f32 v91,v91
v_fma_f32 v88,v88,v119,v119
v_fma_f32 v89,v89,v119,v119
v_fma_f32 v90,v90,v119,v119
v_fma_f32 v91,v91,v119,v119
v_rcp_f32 v88,v88
v_rcp_f32 v89,v89
v_rcp_f32 v90,v90
v_rcp_f32 v91,v91
v_mul_f32 v44,v44,v88
v_mul_f32 v45,v45,v89
v_mul_f32 v46,v46,v90
v_mul_f32 v47,v47,v91
v_cvt_pk_bf16_f32 v98,v44,v45
v_cvt_pk_bf16_f32 v99,v46,v47
buffer_store_dwordx4 v[96:99],v3,s[36:39],s73 offen offset:0 sc0 sc1
v_fma_f32 v48,-v116,v24,v48
v_fma_f32 v49,-v116,v25,v49
v_fma_f32 v50,-v116,v26,v50
v_fma_f32 v51,-v116,v27,v51
v_mul_f32 v92,v48,v48
v_mul_f32 v93,v49,v49
v_mul_f32 v94,v50,v50
v_mul_f32 v95,v51,v51
v_fma_f32 v92,v118,v92,v117
v_fma_f32 v93,v118,v93,v117
v_fma_f32 v94,v118,v94,v117
v_fma_f32 v95,v118,v95,v117
v_mul_f32 v92,v48,v92
v_mul_f32 v93,v49,v93
v_mul_f32 v94,v50,v94
v_mul_f32 v95,v51,v95
v_exp_f32 v92,v92
v_exp_f32 v93,v93
v_exp_f32 v94,v94
v_exp_f32 v95,v95
v_fma_f32 v92,v92,v119,v119
v_fma_f32 v93,v93,v119,v119
v_fma_f32 v94,v94,v119,v119
v_fma_f32 v95,v95,v119,v119
v_rcp_f32 v92,v92
v_rcp_f32 v93,v93
v_rcp_f32 v94,v94
v_rcp_f32 v95,v95
v_mul_f32 v48,v48,v92
v_mul_f32 v49,v49,v93
v_mul_f32 v50,v50,v94
v_mul_f32 v51,v51,v95
v_cvt_pk_bf16_f32 v100,v48,v49
v_cvt_pk_bf16_f32 v101,v50,v51
buffer_store_dwordx2 v[100:101],v9,s[36:39],s73 offen offset:0 sc0 sc1
s_cmp_ge_u32 s48,s59
s_cbranch_scc1 Lg3_done0_%=
s_add_u32 m0,s78,16384
s_nop 0
buffer_load_dword v6,s[36:39],s68 offen offset:0 sc1 lds
s_waitcnt vmcnt(4)
s_barrier
ds_read_b32 v13,v7 offset:8192
s_mul_i32 s70,s46,5
s_lshr_b32 s70,s70,6
buffer_store_dword v5,v4,s[36:39],s70 offen offset:0 sc0 sc1
s_mov_b32 s46,s59
s_mov_b32 s69,18432
s_waitcnt lgkmcnt(0)
v_cmp_eq_u32 vcc,s60,v13
s_nop 0
s_cmp_eq_u32 vcc_lo,-1
s_cbranch_scc1 Lg3_go0_%=
s_add_u32 m0,s78,16640
s_nop 0
buffer_load_dword v6,s[36:39],s68 offen offset:0 sc1 lds
s_waitcnt vmcnt(5)
s_barrier
ds_read_b32 v13,v7 offset:8448
s_mov_b32 s69,18688
s_waitcnt lgkmcnt(0)
v_cmp_eq_u32 vcc,s60,v13
s_nop 0
s_cmp_eq_u32 vcc_lo,-1
s_cbranch_scc1 Lg3_go0_%=
s_add_u32 m0,s78,8192
s_nop 0
buffer_load_dword v6,s[36:39],s68 offen offset:0 sc1 lds
s_waitcnt vmcnt(3)
s_barrier
ds_read_b32 v13,v7 offset:16384
s_mul_i32 s70,s47,5
s_lshr_b32 s70,s70,6
buffer_store_dword v5,v4,s[36:39],s70 offen offset:0 sc0 sc1
s_mov_b32 s47,s59
s_mov_b32 s69,10240
s_waitcnt lgkmcnt(0)
v_cmp_eq_u32 vcc,s60,v13
s_nop 0
s_cmp_eq_u32 vcc_lo,-1
s_cbranch_scc1 Lg3_go0_%=
s_add_u32 m0,s78,8448
s_nop 0
buffer_load_dword v6,s[36:39],s68 offen offset:0 sc1 lds
s_waitcnt vmcnt(3)
s_barrier
ds_read_b32 v13,v7 offset:16640
s_mov_b32 s69,10496
s_waitcnt lgkmcnt(0)
v_cmp_eq_u32 vcc,s60,v13
s_nop 0
s_cmp_eq_u32 vcc_lo,-1
s_cbranch_scc1 Lg3_go0_%=
Lg3_poll0_%=:
s_add_u32 m0,s78,16384
s_nop 0
buffer_load_dword v6,s[36:39],s68 offen offset:0 sc1 lds
s_waitcnt vmcnt(2)
s_barrier
ds_read_b32 v13,v7 offset:8192
s_mov_b32 s69,18432
s_waitcnt lgkmcnt(0)
v_cmp_eq_u32 vcc,s60,v13
s_nop 0
s_cmp_eq_u32 vcc_lo,-1
s_cbranch_scc1 Lg3_go0_%=
s_sleep 4
s_add_u32 m0,s78,16640
s_nop 0
buffer_load_dword v6,s[36:39],s68 offen offset:0 sc1 lds
s_waitcnt vmcnt(2)
s_barrier
ds_read_b32 v13,v7 offset:8448
s_mov_b32 s69,18688
s_waitcnt lgkmcnt(0)
v_cmp_eq_u32 vcc,s60,v13
s_nop 0
s_cmp_eq_u32 vcc_lo,-1
s_cbranch_scc1 Lg3_go0_%=
s_sleep 4
s_add_u32 m0,s78,8192
s_nop 0
buffer_load_dword v6,s[36:39],s68 offen offset:0 sc1 lds
s_waitcnt vmcnt(2)
s_barrier
ds_read_b32 v13,v7 offset:16384
s_mov_b32 s69,10240
s_waitcnt lgkmcnt(0)
v_cmp_eq_u32 vcc,s60,v13
s_nop 0
s_cmp_eq_u32 vcc_lo,-1
s_cbranch_scc1 Lg3_go0_%=
s_sleep 4
s_add_u32 m0,s78,8448
s_nop 0
buffer_load_dword v6,s[36:39],s68 offen offset:0 sc1 lds
s_waitcnt vmcnt(2)
s_barrier
ds_read_b32 v13,v7 offset:16640
s_mov_b32 s69,10496
s_waitcnt lgkmcnt(0)
v_cmp_eq_u32 vcc,s60,v13
s_nop 0
s_cmp_eq_u32 vcc_lo,-1
s_cbranch_scc1 Lg3_go0_%=
s_sleep 4
s_add_u32 s79,s79,1
s_cmp_lt_u32 s79,20000
s_cbranch_scc1 Lg3_poll0_%=
Lg3_go0_%=:
s_xor_b32 s62,s63,65536
s_lshr_b32 s61,s48,0
s_mov_b32 m0,s62
s_nop 0
buffer_load_dwordx4 v0,s[36:39],s61 offen offset:0 sc1 lds
buffer_load_dwordx4 v0,s[36:39],s61 offen offset:1024 sc1 lds
buffer_load_dwordx4 v0,s[36:39],s61 offen offset:2048 sc1 lds
buffer_load_dwordx4 v0,s[36:39],s61 offen offset:3072 sc1 lds
s_add_u32 m0,s62,16384
s_nop 0
buffer_load_dwordx4 v1,s[36:39],s61 offen offset:0 sc1 lds
buffer_load_dwordx4 v1,s[36:39],s61 offen offset:1024 sc1 lds
buffer_load_dwordx4 v1,s[36:39],s61 offen offset:2048 sc1 lds
buffer_load_dwordx4 v1,s[36:39],s61 offen offset:3072 sc1 lds
s_lshr_b32 s72,s61,5
buffer_load_dword v104,v8,s[36:39],s72 offen offset:0 sc1
v_add_u32 v84,s69,v7
s_waitcnt vmcnt(5)
s_barrier
ds_read_b32 v13,v84 offset:0
ds_read_b128 v[52:55],v2 offset:0
ds_read_b128 v[56:59],v2 offset:1024
ds_read_b128 v[60:63],v2 offset:2048
ds_read_b128 v[64:67],v2 offset:3072
ds_read_b128 v[68:71],v2 offset:4096
s_mul_i32 s70,s46,5
s_lshr_b32 s70,s70,6)"
R"(
buffer_store_dword v5,v4,s[36:39],s70 offen offset:0 sc0 sc1
s_mov_b32 s46,s59
s_mul_i32 s70,s47,5
s_lshr_b32 s70,s70,6
buffer_store_dword v5,v4,s[36:39],s70 offen offset:0 sc0 sc1
s_mov_b32 s47,s59
s_mul_i32 s70,s59,5
s_lshr_b32 s70,s70,6
buffer_store_dword v5,v4,s[36:39],s70 offen offset:0 sc0 sc1
s_waitcnt lgkmcnt(5)
v_cmp_eq_u32 vcc,s60,v13
s_nop 0
s_cmp_eq_u32 vcc_lo,-1
s_cselect_b32 s66,s49,s59
s_lshr_b32 s61,s66,0
s_xor_b32 s62,s64,65536
s_waitcnt lgkmcnt(3)
s_setpc_b64 s[90:91]
Lg3_gs0_%=:
s_xor_b32 s62,s63,65536
s_lshr_b32 s61,s48,0
s_cmp_eq_u32 s54,1
s_cselect_b32 s68,s49,s48
s_lshr_b32 s68,s68,5
s_mul_i32 s73,s47,7
s_lshr_b32 s73,s73,1
s_mov_b32 m0,s62
s_nop 0
buffer_load_dwordx4 v0,s[36:39],s61 offen offset:0 sc1 lds
v_mul_f32 v116,s75,v112
v_fma_f32 v113,v113,s75,v12
v_fma_f32 v113,-v116,v116,v113
v_max_f32 v113,v113,v12
v_rsq_f32 v112,v113
s_nop 0
v_mul_f32 v117,s76,v112
v_mul_f32 v118,v112,v112
v_mul_f32 v119,v113,v112
v_mul_f32 v118,s77,v118
v_mul_f32 v118,v118,v117
v_fma_f32 v40,-v116,v16,v40
buffer_load_dwordx4 v0,s[36:39],s61 offen offset:1024 sc1 lds
v_fma_f32 v41,-v116,v17,v41
v_fma_f32 v42,-v116,v18,v42
v_fma_f32 v43,-v116,v19,v43
v_mul_f32 v84,v40,v40
v_mul_f32 v85,v41,v41
v_mul_f32 v86,v42,v42
v_mul_f32 v87,v43,v43
v_fma_f32 v84,v118,v84,v117
v_fma_f32 v85,v118,v85,v117
v_fma_f32 v86,v118,v86,v117
v_fma_f32 v87,v118,v87,v117
buffer_load_dwordx4 v0,s[36:39],s61 offen offset:2048 sc1 lds
v_mul_f32 v84,v40,v84
v_mul_f32 v85,v41,v85
v_mul_f32 v86,v42,v86
v_mul_f32 v87,v43,v87
v_exp_f32 v84,v84
v_exp_f32 v85,v85
v_exp_f32 v86,v86
v_exp_f32 v87,v87
v_fma_f32 v84,v84,v119,v119
v_fma_f32 v85,v85,v119,v119
v_fma_f32 v86,v86,v119,v119
buffer_load_dwordx4 v0,s[36:39],s61 offen offset:3072 sc1 lds
v_fma_f32 v87,v87,v119,v119
v_rcp_f32 v84,v84
v_rcp_f32 v85,v85
v_rcp_f32 v86,v86
v_rcp_f32 v87,v87
v_mul_f32 v40,v40,v84
v_mul_f32 v41,v41,v85
v_mul_f32 v42,v42,v86
v_mul_f32 v43,v43,v87
v_cvt_pk_bf16_f32 v96,v40,v41
v_cvt_pk_bf16_f32 v97,v42,v43
s_add_u32 m0,s78,8192
s_nop 0
buffer_load_dword v6,s[36:39],s68 offen offset:0 sc1 lds
v_fma_f32 v44,-v116,v20,v44
v_fma_f32 v45,-v116,v21,v45
v_fma_f32 v46,-v116,v22,v46
v_fma_f32 v47,-v116,v23,v47
v_mul_f32 v88,v44,v44
v_mul_f32 v89,v45,v45
v_mul_f32 v90,v46,v46
v_mul_f32 v91,v47,v47
v_fma_f32 v88,v118,v88,v117
v_fma_f32 v89,v118,v89,v117
v_fma_f32 v90,v118,v90,v117
s_add_u32 m0,s62,16384
s_nop 0
buffer_load_dwordx4 v1,s[36:39],s61 offen offset:0 sc1 lds
v_fma_f32 v91,v118,v91,v117
v_mul_f32 v88,v44,v88
v_mul_f32 v89,v45,v89
v_mul_f32 v90,v46,v90
v_mul_f32 v91,v47,v91
v_exp_f32 v88,v88
v_exp_f32 v89,v89
v_exp_f32 v90,v90
v_exp_f32 v91,v91
v_fma_f32 v88,v88,v119,v119
v_fma_f32 v89,v89,v119,v119
buffer_load_dwordx4 v1,s[36:39],s61 offen offset:1024 sc1 lds
v_fma_f32 v90,v90,v119,v119
v_fma_f32 v91,v91,v119,v119
v_rcp_f32 v88,v88
v_rcp_f32 v89,v89
v_rcp_f32 v90,v90
v_rcp_f32 v91,v91
v_mul_f32 v44,v44,v88
v_mul_f32 v45,v45,v89
v_mul_f32 v46,v46,v90
v_mul_f32 v47,v47,v91
v_cvt_pk_bf16_f32 v98,v44,v45
buffer_load_dwordx4 v1,s[36:39],s61 offen offset:2048 sc1 lds
v_cvt_pk_bf16_f32 v99,v46,v47
buffer_store_dwordx4 v[96:99],v3,s[36:39],s73 offen offset:0 sc0 sc1
v_fma_f32 v48,-v116,v24,v48
v_fma_f32 v49,-v116,v25,v49
v_fma_f32 v50,-v116,v26,v50
v_fma_f32 v51,-v116,v27,v51
v_mul_f32 v92,v48,v48
v_mul_f32 v93,v49,v49
v_mul_f32 v94,v50,v50
v_mul_f32 v95,v51,v51
v_fma_f32 v92,v118,v92,v117
buffer_load_dwordx4 v1,s[36:39],s61 offen offset:3072 sc1 lds
v_fma_f32 v93,v118,v93,v117
v_fma_f32 v94,v118,v94,v117
v_fma_f32 v95,v118,v95,v117
v_mul_f32 v92,v48,v92
v_mul_f32 v93,v49,v93
v_mul_f32 v94,v50,v94
v_mul_f32 v95,v51,v95
v_exp_f32 v92,v92
v_exp_f32 v93,v93
v_exp_f32 v94,v94
v_exp_f32 v95,v95
s_lshr_b32 s72,s61,5
buffer_load_dword v104,v8,s[36:39],s72 offen offset:0 sc1
v_fma_f32 v92,v92,v119,v119
v_fma_f32 v93,v93,v119,v119
v_fma_f32 v94,v94,v119,v119
v_fma_f32 v95,v95,v119,v119
v_rcp_f32 v92,v92
v_rcp_f32 v93,v93
v_rcp_f32 v94,v94
v_rcp_f32 v95,v95
v_mul_f32 v48,v48,v92
v_mul_f32 v49,v49,v93
v_mul_f32 v50,v50,v94
v_mul_f32 v51,v51,v95
v_cvt_pk_bf16_f32 v100,v48,v49
v_cvt_pk_bf16_f32 v101,v50,v51
buffer_store_dwordx2 v[100:101],v9,s[36:39],s73 offen offset:0 sc0 sc1
s_waitcnt vmcnt(7)
s_barrier
ds_read_b32 v13,v7 offset:8192
ds_read_b128 v[52:55],v2 offset:0
ds_read_b128 v[56:59],v2 offset:1024
ds_read_b128 v[60:63],v2 offset:2048
ds_read_b128 v[64:67],v2 offset:3072
ds_read_b128 v[68:71],v2 offset:4096
s_mul_i32 s70,s46,5
s_lshr_b32 s70,s70,6
buffer_store_dword v5,v4,s[36:39],s70 offen offset:0 sc0 sc1
s_waitcnt lgkmcnt(5)
v_cmp_eq_u32 vcc,s60,v13
s_nop 0
s_cmp_eq_u32 vcc_hi,-1
s_cselect_b32 s66,s49,s59
s_lshr_b32 s61,s66,0
s_xor_b32 s62,s64,65536
s_mov_b32 s46,s47
s_mov_b32 s47,s59
s_mul_i32 s70,s59,5
s_lshr_b32 s70,s70,6
buffer_store_dword v5,v4,s[36:39],s70 offen offset:0 sc0 sc1
s_waitcnt lgkmcnt(3)
s_setpc_b64 s[90:91]
Lg3_done0_%=:
s_waitcnt vmcnt(0)
s_barrier
s_mul_i32 s70,s46,5
s_lshr_b32 s70,s70,6
buffer_store_dword v5,v4,s[36:39],s70 offen offset:0 sc0 sc1
s_mul_i32 s70,s47,5
s_lshr_b32 s70,s70,6
buffer_store_dword v5,v4,s[36:39],s70 offen offset:0 sc0 sc1
s_branch Lg3_done_%=
Lg3_slow1_%=:
s_cmp_lt_u32 s67,s59
s_cbranch_scc1 Lg3_gs1_%=
s_cmp_eq_u32 s54,1
s_cselect_b32 s68,s50,s49
s_lshr_b32 s68,s68,5
s_add_u32 m0,s78,8192)"
R"(
s_nop 0
buffer_load_dword v6,s[36:39],s68 offen offset:0 sc1 lds
s_mul_i32 s73,s48,7
s_lshr_b32 s73,s73,1
v_mul_f32 v116,s75,v112
v_fma_f32 v113,v113,s75,v12
v_fma_f32 v113,-v116,v116,v113
v_max_f32 v113,v113,v12
v_rsq_f32 v112,v113
s_nop 0
v_mul_f32 v117,s76,v112
v_mul_f32 v118,v112,v112
v_mul_f32 v119,v113,v112
v_mul_f32 v118,s77,v118
v_mul_f32 v118,v118,v117
v_fma_f32 v28,-v116,v16,v28
v_fma_f32 v29,-v116,v17,v29
v_fma_f32 v30,-v116,v18,v30
v_fma_f32 v31,-v116,v19,v31
v_mul_f32 v84,v28,v28
v_mul_f32 v85,v29,v29
v_mul_f32 v86,v30,v30
v_mul_f32 v87,v31,v31
v_fma_f32 v84,v118,v84,v117
v_fma_f32 v85,v118,v85,v117
v_fma_f32 v86,v118,v86,v117
v_fma_f32 v87,v118,v87,v117
v_mul_f32 v84,v28,v84
v_mul_f32 v85,v29,v85
v_mul_f32 v86,v30,v86
v_mul_f32 v87,v31,v87
v_exp_f32 v84,v84
v_exp_f32 v85,v85
v_exp_f32 v86,v86
v_exp_f32 v87,v87
v_fma_f32 v84,v84,v119,v119
v_fma_f32 v85,v85,v119,v119
v_fma_f32 v86,v86,v119,v119
v_fma_f32 v87,v87,v119,v119
v_rcp_f32 v84,v84
v_rcp_f32 v85,v85
v_rcp_f32 v86,v86
v_rcp_f32 v87,v87
v_mul_f32 v28,v28,v84
v_mul_f32 v29,v29,v85
v_mul_f32 v30,v30,v86
v_mul_f32 v31,v31,v87
v_cvt_pk_bf16_f32 v96,v28,v29
v_cvt_pk_bf16_f32 v97,v30,v31
v_fma_f32 v32,-v116,v20,v32
v_fma_f32 v33,-v116,v21,v33
v_fma_f32 v34,-v116,v22,v34
v_fma_f32 v35,-v116,v23,v35
v_mul_f32 v88,v32,v32
v_mul_f32 v89,v33,v33
v_mul_f32 v90,v34,v34
v_mul_f32 v91,v35,v35
v_fma_f32 v88,v118,v88,v117
v_fma_f32 v89,v118,v89,v117
v_fma_f32 v90,v118,v90,v117
v_fma_f32 v91,v118,v91,v117
v_mul_f32 v88,v32,v88
s_add_u32 m0,s78,8448
s_nop 0
buffer_load_dword v6,s[36:39],s68 offen offset:0 sc1 lds
v_mul_f32 v89,v33,v89
v_mul_f32 v90,v34,v90
v_mul_f32 v91,v35,v91
v_exp_f32 v88,v88
v_exp_f32 v89,v89
v_exp_f32 v90,v90
v_exp_f32 v91,v91
v_fma_f32 v88,v88,v119,v119
v_fma_f32 v89,v89,v119,v119
v_fma_f32 v90,v90,v119,v119
v_fma_f32 v91,v91,v119,v119
v_rcp_f32 v88,v88
v_rcp_f32 v89,v89
v_rcp_f32 v90,v90
v_rcp_f32 v91,v91
v_mul_f32 v32,v32,v88
v_mul_f32 v33,v33,v89
v_mul_f32 v34,v34,v90
v_mul_f32 v35,v35,v91
v_cvt_pk_bf16_f32 v98,v32,v33
v_cvt_pk_bf16_f32 v99,v34,v35
buffer_store_dwordx4 v[96:99],v3,s[36:39],s73 offen offset:0 sc0 sc1
v_fma_f32 v36,-v116,v24,v36
v_fma_f32 v37,-v116,v25,v37
v_fma_f32 v38,-v116,v26,v38
v_fma_f32 v39,-v116,v27,v39
v_mul_f32 v92,v36,v36
v_mul_f32 v93,v37,v37
v_mul_f32 v94,v38,v38
v_mul_f32 v95,v39,v39
v_fma_f32 v92,v118,v92,v117
v_fma_f32 v93,v118,v93,v117
v_fma_f32 v94,v118,v94,v117
v_fma_f32 v95,v118,v95,v117
v_mul_f32 v92,v36,v92
v_mul_f32 v93,v37,v93
v_mul_f32 v94,v38,v94
v_mul_f32 v95,v39,v95
v_exp_f32 v92,v92
v_exp_f32 v93,v93
v_exp_f32 v94,v94
v_exp_f32 v95,v95
v_fma_f32 v92,v92,v119,v119
v_fma_f32 v93,v93,v119,v119
v_fma_f32 v94,v94,v119,v119
v_fma_f32 v95,v95,v119,v119
v_rcp_f32 v92,v92
v_rcp_f32 v93,v93
v_rcp_f32 v94,v94
v_rcp_f32 v95,v95
v_mul_f32 v36,v36,v92
v_mul_f32 v37,v37,v93
v_mul_f32 v38,v38,v94
v_mul_f32 v39,v39,v95
v_cvt_pk_bf16_f32 v100,v36,v37
v_cvt_pk_bf16_f32 v101,v38,v39
buffer_store_dwordx2 v[100:101],v9,s[36:39],s73 offen offset:0 sc0 sc1
s_cmp_ge_u32 s49,s59
s_cbranch_scc1 Lg3_done1_%=
s_add_u32 m0,s78,16384
s_nop 0
buffer_load_dword v6,s[36:39],s68 offen offset:0 sc1 lds
s_waitcnt vmcnt(4)
s_barrier
ds_read_b32 v13,v7 offset:8192
s_mul_i32 s70,s47,5
s_lshr_b32 s70,s70,6
buffer_store_dword v5,v4,s[36:39],s70 offen offset:0 sc0 sc1
s_mov_b32 s47,s59
s_mov_b32 s69,18432
s_waitcnt lgkmcnt(0)
v_cmp_eq_u32 vcc,s60,v13
s_nop 0
s_cmp_eq_u32 vcc_lo,-1
s_cbranch_scc1 Lg3_go1_%=
s_add_u32 m0,s78,16640
s_nop 0
buffer_load_dword v6,s[36:39],s68 offen offset:0 sc1 lds
s_waitcnt vmcnt(5)
s_barrier
ds_read_b32 v13,v7 offset:8448
s_mov_b32 s69,18688
s_waitcnt lgkmcnt(0)
v_cmp_eq_u32 vcc,s60,v13
s_nop 0
s_cmp_eq_u32 vcc_lo,-1
s_cbranch_scc1 Lg3_go1_%=
s_add_u32 m0,s78,8192
s_nop 0
buffer_load_dword v6,s[36:39],s68 offen offset:0 sc1 lds
s_waitcnt vmcnt(3)
s_barrier
ds_read_b32 v13,v7 offset:16384
s_mul_i32 s70,s48,5
s_lshr_b32 s70,s70,6
buffer_store_dword v5,v4,s[36:39],s70 offen offset:0 sc0 sc1
s_mov_b32 s48,s59
s_mov_b32 s69,10240
s_waitcnt lgkmcnt(0)
v_cmp_eq_u32 vcc,s60,v13
s_nop 0
s_cmp_eq_u32 vcc_lo,-1
s_cbranch_scc1 Lg3_go1_%=
s_add_u32 m0,s78,8448
s_nop 0
buffer_load_dword v6,s[36:39],s68 offen offset:0 sc1 lds
s_waitcnt vmcnt(3)
s_barrier
ds_read_b32 v13,v7 offset:16640
s_mov_b32 s69,10496
s_waitcnt lgkmcnt(0)
v_cmp_eq_u32 vcc,s60,v13
s_nop 0
s_cmp_eq_u32 vcc_lo,-1
s_cbranch_scc1 Lg3_go1_%=
Lg3_poll1_%=:
s_add_u32 m0,s78,16384
s_nop 0
buffer_load_dword v6,s[36:39],s68 offen offset:0 sc1 lds
s_waitcnt vmcnt(2)
s_barrier
ds_read_b32 v13,v7 offset:8192
s_mov_b32 s69,18432
s_waitcnt lgkmcnt(0)
v_cmp_eq_u32 vcc,s60,v13
s_nop 0
s_cmp_eq_u32 vcc_lo,-1
s_cbranch_scc1 Lg3_go1_%=
s_sleep 4
s_add_u32 m0,s78,16640
s_nop 0
buffer_load_dword v6,s[36:39],s68 offen offset:0 sc1 lds
s_waitcnt vmcnt(2)
s_barrier
ds_read_b32 v13,v7 offset:8448)"
R"(
s_mov_b32 s69,18688
s_waitcnt lgkmcnt(0)
v_cmp_eq_u32 vcc,s60,v13
s_nop 0
s_cmp_eq_u32 vcc_lo,-1
s_cbranch_scc1 Lg3_go1_%=
s_sleep 4
s_add_u32 m0,s78,8192
s_nop 0
buffer_load_dword v6,s[36:39],s68 offen offset:0 sc1 lds
s_waitcnt vmcnt(2)
s_barrier
ds_read_b32 v13,v7 offset:16384
s_mov_b32 s69,10240
s_waitcnt lgkmcnt(0)
v_cmp_eq_u32 vcc,s60,v13
s_nop 0
s_cmp_eq_u32 vcc_lo,-1
s_cbranch_scc1 Lg3_go1_%=
s_sleep 4
s_add_u32 m0,s78,8448
s_nop 0
buffer_load_dword v6,s[36:39],s68 offen offset:0 sc1 lds
s_waitcnt vmcnt(2)
s_barrier
ds_read_b32 v13,v7 offset:16640
s_mov_b32 s69,10496
s_waitcnt lgkmcnt(0)
v_cmp_eq_u32 vcc,s60,v13
s_nop 0
s_cmp_eq_u32 vcc_lo,-1
s_cbranch_scc1 Lg3_go1_%=
s_sleep 4
s_add_u32 s79,s79,1
s_cmp_lt_u32 s79,20000
s_cbranch_scc1 Lg3_poll1_%=
Lg3_go1_%=:
s_xor_b32 s62,s64,65536
s_lshr_b32 s61,s49,0
s_mov_b32 m0,s62
s_nop 0
buffer_load_dwordx4 v0,s[36:39],s61 offen offset:0 sc1 lds
buffer_load_dwordx4 v0,s[36:39],s61 offen offset:1024 sc1 lds
buffer_load_dwordx4 v0,s[36:39],s61 offen offset:2048 sc1 lds
buffer_load_dwordx4 v0,s[36:39],s61 offen offset:3072 sc1 lds
s_add_u32 m0,s62,16384
s_nop 0
buffer_load_dwordx4 v1,s[36:39],s61 offen offset:0 sc1 lds
buffer_load_dwordx4 v1,s[36:39],s61 offen offset:1024 sc1 lds
buffer_load_dwordx4 v1,s[36:39],s61 offen offset:2048 sc1 lds
buffer_load_dwordx4 v1,s[36:39],s61 offen offset:3072 sc1 lds
s_lshr_b32 s72,s61,5
buffer_load_dword v108,v8,s[36:39],s72 offen offset:0 sc1
v_add_u32 v84,s69,v7
s_waitcnt vmcnt(5)
s_barrier
ds_read_b32 v13,v84 offset:0
ds_read_b128 v[52:55],v2 offset:32768
ds_read_b128 v[56:59],v2 offset:33792
ds_read_b128 v[60:63],v2 offset:34816
ds_read_b128 v[64:67],v2 offset:35840
ds_read_b128 v[68:71],v2 offset:36864
s_mul_i32 s70,s47,5
s_lshr_b32 s70,s70,6
buffer_store_dword v5,v4,s[36:39],s70 offen offset:0 sc0 sc1
s_mov_b32 s47,s59
s_mul_i32 s70,s48,5
s_lshr_b32 s70,s70,6
buffer_store_dword v5,v4,s[36:39],s70 offen offset:0 sc0 sc1
s_mov_b32 s48,s59
s_mul_i32 s70,s59,5
s_lshr_b32 s70,s70,6
buffer_store_dword v5,v4,s[36:39],s70 offen offset:0 sc0 sc1
s_waitcnt lgkmcnt(5)
v_cmp_eq_u32 vcc,s60,v13
s_nop 0
s_cmp_eq_u32 vcc_lo,-1
s_cselect_b32 s67,s50,s59
s_lshr_b32 s61,s67,0
s_xor_b32 s62,s63,65536
s_waitcnt lgkmcnt(3)
s_setpc_b64 s[92:93]
Lg3_gs1_%=:
s_xor_b32 s62,s64,65536
s_lshr_b32 s61,s49,0
s_cmp_eq_u32 s54,1
s_cselect_b32 s68,s50,s49
s_lshr_b32 s68,s68,5
s_mul_i32 s73,s48,7
s_lshr_b32 s73,s73,1
s_mov_b32 m0,s62
s_nop 0
buffer_load_dwordx4 v0,s[36:39],s61 offen offset:0 sc1 lds
v_mul_f32 v116,s75,v112
v_fma_f32 v113,v113,s75,v12
v_fma_f32 v113,-v116,v116,v113
v_max_f32 v113,v113,v12
v_rsq_f32 v112,v113
s_nop 0
v_mul_f32 v117,s76,v112
v_mul_f32 v118,v112,v112
v_mul_f32 v119,v113,v112
v_mul_f32 v118,s77,v118
v_mul_f32 v118,v118,v117
v_fma_f32 v28,-v116,v16,v28
buffer_load_dwordx4 v0,s[36:39],s61 offen offset:1024 sc1 lds
v_fma_f32 v29,-v116,v17,v29
v_fma_f32 v30,-v116,v18,v30
v_fma_f32 v31,-v116,v19,v31
v_mul_f32 v84,v28,v28
v_mul_f32 v85,v29,v29
v_mul_f32 v86,v30,v30
v_mul_f32 v87,v31,v31
v_fma_f32 v84,v118,v84,v117
v_fma_f32 v85,v118,v85,v117
v_fma_f32 v86,v118,v86,v117
v_fma_f32 v87,v118,v87,v117
buffer_load_dwordx4 v0,s[36:39],s61 offen offset:2048 sc1 lds
v_mul_f32 v84,v28,v84
v_mul_f32 v85,v29,v85
v_mul_f32 v86,v30,v86
v_mul_f32 v87,v31,v87
v_exp_f32 v84,v84
v_exp_f32 v85,v85
v_exp_f32 v86,v86
v_exp_f32 v87,v87
v_fma_f32 v84,v84,v119,v119
v_fma_f32 v85,v85,v119,v119
v_fma_f32 v86,v86,v119,v119
buffer_load_dwordx4 v0,s[36:39],s61 offen offset:3072 sc1 lds
v_fma_f32 v87,v87,v119,v119
v_rcp_f32 v84,v84
v_rcp_f32 v85,v85
v_rcp_f32 v86,v86
v_rcp_f32 v87,v87
v_mul_f32 v28,v28,v84
v_mul_f32 v29,v29,v85
v_mul_f32 v30,v30,v86
v_mul_f32 v31,v31,v87
v_cvt_pk_bf16_f32 v96,v28,v29
v_cvt_pk_bf16_f32 v97,v30,v31
s_add_u32 m0,s78,8192
s_nop 0
buffer_load_dword v6,s[36:39],s68 offen offset:0 sc1 lds
v_fma_f32 v32,-v116,v20,v32
v_fma_f32 v33,-v116,v21,v33
v_fma_f32 v34,-v116,v22,v34
v_fma_f32 v35,-v116,v23,v35
v_mul_f32 v88,v32,v32
v_mul_f32 v89,v33,v33
v_mul_f32 v90,v34,v34
v_mul_f32 v91,v35,v35
v_fma_f32 v88,v118,v88,v117
v_fma_f32 v89,v118,v89,v117
v_fma_f32 v90,v118,v90,v117
s_add_u32 m0,s62,16384
s_nop 0
buffer_load_dwordx4 v1,s[36:39],s61 offen offset:0 sc1 lds
v_fma_f32 v91,v118,v91,v117
v_mul_f32 v88,v32,v88
v_mul_f32 v89,v33,v89
v_mul_f32 v90,v34,v90
v_mul_f32 v91,v35,v91
v_exp_f32 v88,v88
v_exp_f32 v89,v89
v_exp_f32 v90,v90
v_exp_f32 v91,v91
v_fma_f32 v88,v88,v119,v119
v_fma_f32 v89,v89,v119,v119
buffer_load_dwordx4 v1,s[36:39],s61 offen offset:1024 sc1 lds
v_fma_f32 v90,v90,v119,v119
v_fma_f32 v91,v91,v119,v119
v_rcp_f32 v88,v88
v_rcp_f32 v89,v89
v_rcp_f32 v90,v90
v_rcp_f32 v91,v91
v_mul_f32 v32,v32,v88
v_mul_f32 v33,v33,v89
v_mul_f32 v34,v34,v90
v_mul_f32 v35,v35,v91
v_cvt_pk_bf16_f32 v98,v32,v33
buffer_load_dwordx4 v1,s[36:39],s61 offen offset:2048 sc1 lds
v_cvt_pk_bf16_f32 v99,v34,v35
buffer_store_dwordx4 v[96:99],v3,s[36:39],s73 offen offset:0 sc0 sc1
v_fma_f32 v36,-v116,v24,v36
v_fma_f32 v37,-v116,v25,v37
v_fma_f32 v38,-v116,v26,v38
v_fma_f32 v39,-v116,v27,v39
v_mul_f32 v92,v36,v36
v_mul_f32 v93,v37,v37
v_mul_f32 v94,v38,v38
v_mul_f32 v95,v39,v39
v_fma_f32 v92,v118,v92,v117
buffer_load_dwordx4 v1,s[36:39],s61 offen offset:3072 sc1 lds
v_fma_f32 v93,v118,v93,v117
v_fma_f32 v94,v118,v94,v117
v_fma_f32 v95,v118,v95,v117
v_mul_f32 v92,v36,v92
v_mul_f32 v93,v37,v93
v_mul_f32 v94,v38,v94)"
R"(
v_mul_f32 v95,v39,v95
v_exp_f32 v92,v92
v_exp_f32 v93,v93
v_exp_f32 v94,v94
v_exp_f32 v95,v95
s_lshr_b32 s72,s61,5
buffer_load_dword v108,v8,s[36:39],s72 offen offset:0 sc1
v_fma_f32 v92,v92,v119,v119
v_fma_f32 v93,v93,v119,v119
v_fma_f32 v94,v94,v119,v119
v_fma_f32 v95,v95,v119,v119
v_rcp_f32 v92,v92
v_rcp_f32 v93,v93
v_rcp_f32 v94,v94
v_rcp_f32 v95,v95
v_mul_f32 v36,v36,v92
v_mul_f32 v37,v37,v93
v_mul_f32 v38,v38,v94
v_mul_f32 v39,v39,v95
v_cvt_pk_bf16_f32 v100,v36,v37
v_cvt_pk_bf16_f32 v101,v38,v39
buffer_store_dwordx2 v[100:101],v9,s[36:39],s73 offen offset:0 sc0 sc1
s_waitcnt vmcnt(7)
s_barrier
ds_read_b32 v13,v7 offset:8192
ds_read_b128 v[52:55],v2 offset:32768
ds_read_b128 v[56:59],v2 offset:33792
ds_read_b128 v[60:63],v2 offset:34816
ds_read_b128 v[64:67],v2 offset:35840
ds_read_b128 v[68:71],v2 offset:36864
s_mul_i32 s70,s47,5
s_lshr_b32 s70,s70,6
buffer_store_dword v5,v4,s[36:39],s70 offen offset:0 sc0 sc1
s_waitcnt lgkmcnt(5)
v_cmp_eq_u32 vcc,s60,v13
s_nop 0
s_cmp_eq_u32 vcc_hi,-1
s_cselect_b32 s67,s50,s59
s_lshr_b32 s61,s67,0
s_xor_b32 s62,s63,65536
s_mov_b32 s47,s48
s_mov_b32 s48,s59
s_mul_i32 s70,s59,5
s_lshr_b32 s70,s70,6
buffer_store_dword v5,v4,s[36:39],s70 offen offset:0 sc0 sc1
s_waitcnt lgkmcnt(3)
s_setpc_b64 s[92:93]
Lg3_done1_%=:
s_waitcnt vmcnt(0)
s_barrier
s_mul_i32 s70,s47,5
s_lshr_b32 s70,s70,6
buffer_store_dword v5,v4,s[36:39],s70 offen offset:0 sc0 sc1
s_mul_i32 s70,s48,5
s_lshr_b32 s70,s70,6
buffer_store_dword v5,v4,s[36:39],s70 offen offset:0 sc0 sc1
s_branch Lg3_done_%=
Lg3_done_%=:
s_waitcnt vmcnt(0) lgkmcnt(0)
s_barrier
s_endpgm
Lcode_g3_e:)"
  :: "s"(ws), "s"(wave), "s"(sStep), "s"(sX1), "s"(sX2), "s"(sU0), "s"(sWw), "s"(sBuf), "v"(vSrc), "v"(vA), "v"(vOut), "v"(vFlag), "v"(vPollOff), "v"(vSnapRd), "v"(vAuxS), "v"(vOut2), "v"(vStW), "v"(vStR), "v"(vWd0), "v"(vWd1), "v"(vWr0), "v"(vWr1), "v"(vWr2), "s"(w3) : "v0", "v1", "v2", "v3", "v4", "v5", "v6", "v7", "v8", "v9", "v10", "v11", "v12", "v13", "v14", "v15", "v16", "v17", "v18", "v19", "v20", "v21", "v22", "v23", "v24", "v25", "v26", "v27", "v28", "v29", "v30", "v31", "v32", "v33", "v34", "v35", "v36", "v37", "v38", "v39", "v40", "v41", "v42", "v43", "v44", "v45", "v46", "v47", "v48", "v49", "v50", "v51", "v52", "v53", "v54", "v55", "v56", "v57", "v58", "v59", "v60", "v61", "v62", "v63", "v64", "v65", "v66", "v67", "v68", "v69", "v70", "v71", "v72", "v73", "v74", "v75", "v76", "v77", "v78", "v79", "v80", "v81", "v82", "v83", "v84", "v85", "v86", "v87", "v88", "v89", "v90", "v91", "v92", "v93", "v94", "v95", "v96", "v97", "v98", "v99", "v100", "v101", "v102", "v103", "v104", "v105", "v106", "v107", "v108", "v109", "v110", "v111", "v112", "v113", "v114", "v115", "v116", "v117", "v118", "v119", "v120", "v121", "v122", "v123", "v124", "v125", "v126", "v127", "v128", "v129", "v130", "v131", "v132", "v133", "v134", "v135", "v136", "v137", "v138", "v139", "v140", "v141", "v142", "v143", "v144", "v145", "v146", "v147", "v148", "v149", "v150", "v151", "v152", "v153", "v154", "v155", "v156", "v157", "v158", "v159", "v160", "v161", "v162", "v163", "v164", "v165", "v166", "v167", "v168", "v169", "v170", "v171", "v172", "v173", "v174", "v175", "v176", "v177", "v178", "v179", "v180", "v181", "v182", "v183", "v184", "v185", "v186", "v187", "v188", "v189", "v190", "v191", "v192", "v193", "v194", "v195", "v196", "v197", "v198", "v199", "v200", "v201", "v202", "v203", "v204", "v205", "v206", "v207", "v208", "v209", "v210", "v211", "v212", "v213", "v214", "v215", "v216", "v217", "v218", "v219", "v220", "v221", "v222", "v223", "v224", "v225", "v226", "v227", "v228", "v229", "v230", "v231", "v255", "a0", "a1", "a2", "a3", "a4", "a5", "a6", "a7", "a8", "a9", "a10", "a11", "a12", "a13", "a14", "a15", "a16", "a17", "a18", "a19", "a20", "a21", "a22", "a23", "a24", "a25", "a26", "a27", "a28", "a29", "a30", "a31", "a32", "a33", "a34", "a35", "a36", "a37", "a38", "a39", "a40", "a41", "a42", "a43", "a44", "a45", "a46", "a47", "a48", "a49", "a50", "a51", "a52", "a53", "a54", "a55", "a56", "a57", "a58", "a59", "a60", "a61", "a62", "a63", "a64", "a65", "a66", "a67", "a68", "a69", "a70", "a71", "a72", "a73", "a74", "a75", "a76", "a77", "a78", "a79", "a80", "a81", "a82", "a83", "a84", "a85", "a86", "a87", "a88", "a89", "a90", "a91", "a92", "a93", "a94", "a95", "a96", "a97", "a98", "a99", "a100", "a101", "a102", "a103", "a104", "a105", "a106", "a107", "a108", "a109", "a110", "a111", "a112", "a113", "a114", "a115", "a116", "a117", "a118", "a119", "a120", "a121", "a122", "a123", "a124", "a125", "a126", "a127", "a128", "a129", "a130", "a131", "a132", "a133", "a134", "a135", "a136", "a137", "a138", "a139", "a140", "a141", "a142", "a143", "a144", "a145", "a146", "a147", "a148", "a149", "a150", "a151", "a152", "a153", "a154", "a155", "a156", "a157", "a158", "a159", "a160", "a161", "a162", "a163", "a164", "a165", "a166", "a167", "a168", "a169", "a170", "a171", "a172", "a173", "a174", "a175", "a176", "a177", "a178", "a179", "a180", "a181", "a182", "a183", "a184", "a185", "a186", "a187", "a188", "a189", "a190", "a191", "a192", "a193", "a194", "a195", "a196", "a197", "a198", "a199", "a200", "a201", "a202", "a203", "a204", "a205", "a206", "a207", "a208", "a209", "a210", "a211", "a212", "a213", "a214", "a215", "a216", "a217", "a218", "a219", "a220", "a221", "a222", "a223", "a224", "a225", "a226", "a227", "a228", "a229", "a230", "a231", "a232", "a233", "a234", "a235", "a236", "a237", "a238", "a239", "a240", "a241", "a242", "a243", "a244", "a245", "a246", "a247", "a248", "a249", "a250", "a251", "a252", "a253", "a254", "a255", "s36", "s37", "s38", "s39", "s40", "s41", "s42", "s43", "s44", "s45", "s46", "s47", "s48", "s49", "s50", "s51", "s52", "s53", "s54", "s55", "s56", "s57", "s58", "s59", "s60", "s61", "s62", "s63", "s64", "s65", "s66", "s67", "s68", "s69", "s70", "s71", "s72", "s73", "s74", "s75", "s76", "s77", "s78", "s79", "s80", "s81", "s82", "s83", "s84", "s85", "s86", "s87", "s88", "s89", "s90", "s91", "s92", "s93", "s94", "s95", "s96", "s97", "s98", "s99", "vcc", "scc", "memory"); } else { asm volatile(R"(
Lcode_g3o_b:
s_mov_b64 s[36:37],%0
s_mov_b32 s38,111193216
s_mov_b32 s39,0x00020000
s_mov_b32 s54,%1
s_mov_b32 s55,%2
s_mov_b32 s56,%3
s_mov_b32 s58,%4
s_mov_b32 s83,%5
s_mov_b32 s82,%6
s_mov_b32 s84,%7
v_mov_b32 v0,%8
v_mov_b32 v2,%9
v_mov_b32 v3,%10
v_mov_b32 v4,%11
v_mov_b32 v6,%12
v_mov_b32 v7,%13
v_mov_b32 v8,%14
v_mov_b32 v9,%15
v_mov_b32 v10,%16
v_mov_b32 v11,%17
v_mov_b32 v24,%18
v_mov_b32 v25,%19
v_mov_b32 v26,%20
v_mov_b32 v27,%21
v_mov_b32 v28,%22
s_mov_b64 s[40:41],%23
s_mov_b32 s42,7340032
s_mov_b32 s43,0x00020000
s_lshl_b32 s88,s54,11
s_mov_b32 s69,111193088
buffer_load_dword v5,off,s[36:39],s69 sc1
s_add_u32 m0,s88,0
s_add_u32 s61,s82,0
buffer_load_dwordx4 v24,s[40:43],s61 offen offset:0  lds
s_add_u32 m0,s88,1024
s_nop 0
buffer_load_dwordx4 v25,s[40:43],s61 offen offset:0  lds
s_add_u32 m0,s88,8064
s_nop 0
buffer_load_dwordx4 v24,s[40:43],s61 offen offset:128  lds
s_add_u32 m0,s88,9088
s_nop 0
buffer_load_dwordx4 v25,s[40:43],s61 offen offset:128  lds
s_add_u32 m0,s88,16384
s_add_u32 s61,s82,458752
buffer_load_dwordx4 v24,s[40:43],s61 offen offset:0  lds
s_add_u32 m0,s88,17408
s_nop 0
buffer_load_dwordx4 v25,s[40:43],s61 offen offset:0  lds
s_add_u32 m0,s88,24448
s_nop 0
buffer_load_dwordx4 v24,s[40:43],s61 offen offset:128  lds
s_add_u32 m0,s88,25472
s_nop 0
buffer_load_dwordx4 v25,s[40:43],s61 offen offset:128  lds
s_add_u32 m0,s88,32768
s_add_u32 s61,s82,917504
buffer_load_dwordx4 v24,s[40:43],s61 offen offset:0  lds
s_add_u32 m0,s88,33792
s_nop 0
buffer_load_dwordx4 v25,s[40:43],s61 offen offset:0  lds
s_add_u32 m0,s88,40832
s_nop 0
buffer_load_dwordx4 v24,s[40:43],s61 offen offset:128  lds
s_add_u32 m0,s88,41856
s_nop 0
buffer_load_dwordx4 v25,s[40:43],s61 offen offset:128  lds
s_add_u32 m0,s88,49152
s_add_u32 s61,s82,1376256
buffer_load_dwordx4 v24,s[40:43],s61 offen offset:0  lds
s_add_u32 m0,s88,50176
s_nop 0
buffer_load_dwordx4 v25,s[40:43],s61 offen offset:0  lds
s_add_u32 m0,s88,57216
s_nop 0
buffer_load_dwordx4 v24,s[40:43],s61 offen offset:128  lds
s_add_u32 m0,s88,58240
s_nop 0
buffer_load_dwordx4 v25,s[40:43],s61 offen offset:128  lds
s_add_u32 m0,s88,65536
s_add_u32 s61,s82,1835008
buffer_load_dwordx4 v24,s[40:43],s61 offen offset:0  lds
s_add_u32 m0,s88,66560
s_nop 0
buffer_load_dwordx4 v25,s[40:43],s61 offen offset:0  lds
s_add_u32 m0,s88,73600
s_nop 0
buffer_load_dwordx4 v24,s[40:43],s61 offen offset:128  lds
s_add_u32 m0,s88,74624
s_nop 0
buffer_load_dwordx4 v25,s[40:43],s61 offen offset:128  lds
s_add_u32 m0,s88,81920
s_add_u32 s61,s82,2293760
buffer_load_dwordx4 v24,s[40:43],s61 offen offset:0  lds
s_add_u32 m0,s88,82944
s_nop 0
buffer_load_dwordx4 v25,s[40:43],s61 offen offset:0  lds
s_add_u32 m0,s88,89984
s_nop 0
buffer_load_dwordx4 v24,s[40:43],s61 offen offset:128  lds
s_add_u32 m0,s88,91008
s_nop 0
buffer_load_dwordx4 v25,s[40:43],s61 offen offset:128  lds
s_add_u32 m0,s88,98304
s_add_u32 s61,s82,2752512
buffer_load_dwordx4 v24,s[40:43],s61 offen offset:0  lds
s_add_u32 m0,s88,99328
s_nop 0
buffer_load_dwordx4 v25,s[40:43],s61 offen offset:0  lds
s_add_u32 m0,s88,106368
s_nop 0
buffer_load_dwordx4 v24,s[40:43],s61 offen offset:128  lds
s_add_u32 m0,s88,107392
s_nop 0
buffer_load_dwordx4 v25,s[40:43],s61 offen offset:128  lds
v_add_u32 v32,49152,v26
v_add_u32 v33,49152,v27
v_add_u32 v34,98304,v26
v_add_u32 v35,98304,v27
v_mov_b32 v72,0x3f803f80
v_mov_b32 v73,0x3f803f80
v_mov_b32 v74,0x3f803f80
v_mov_b32 v75,0x3f803f80
s_waitcnt vmcnt(24)
s_barrier
s_add_u32 m0,s88,114688
s_add_u32 s61,s82,3211264
buffer_load_dwordx4 v24,s[40:43],s61 offen offset:0  lds
s_add_u32 m0,s88,115712
ds_read_b64_tr_b16 a[0:1],v26 offset:0
buffer_load_dwordx4 v25,s[40:43],s61 offen offset:0  lds
s_add_u32 m0,s88,122752
ds_read_b64_tr_b16 a[2:3],v26 offset:512
buffer_load_dwordx4 v24,s[40:43],s61 offen offset:128  lds
s_add_u32 m0,s88,123776
ds_read_b64_tr_b16 a[4:5],v27 offset:0
buffer_load_dwordx4 v25,s[40:43],s61 offen offset:128  lds
ds_read_b64_tr_b16 a[6:7],v27 offset:512
ds_read_b64_tr_b16 a[8:9],v26 offset:4096
ds_read_b64_tr_b16 a[10:11],v26 offset:4608
ds_read_b64_tr_b16 a[12:13],v27 offset:4096
ds_read_b64_tr_b16 a[14:15],v27 offset:4608
s_waitcnt vmcnt(24)
s_barrier
s_add_u32 m0,s88,131072
s_add_u32 s61,s82,3670016
buffer_load_dwordx4 v24,s[40:43],s61 offen offset:0  lds
s_add_u32 m0,s88,132096
ds_read_b64_tr_b16 a[16:17],v26 offset:16384
buffer_load_dwordx4 v25,s[40:43],s61 offen offset:0  lds
s_add_u32 m0,s88,139136
ds_read_b64_tr_b16 a[18:19],v26 offset:16896
buffer_load_dwordx4 v24,s[40:43],s61 offen offset:128  lds
s_add_u32 m0,s88,140160
ds_read_b64_tr_b16 a[20:21],v27 offset:16384
buffer_load_dwordx4 v25,s[40:43],s61 offen offset:128  lds
ds_read_b64_tr_b16 a[22:23],v27 offset:16896
ds_read_b64_tr_b16 a[24:25],v26 offset:20480
ds_read_b64_tr_b16 a[26:27],v26 offset:20992
ds_read_b64_tr_b16 a[28:29],v27 offset:20480
ds_read_b64_tr_b16 a[30:31],v27 offset:20992
s_waitcnt lgkmcnt(8)
v_mfma_f32_16x16x32_bf16 v[16:19],a[0:3],v[72:75],0
v_mfma_f32_16x16x32_bf16 v[20:23],a[4:7],v[72:75],0
v_mfma_f32_16x16x32_bf16 v[16:19],a[8:11],v[72:75],v[16:19]
v_mfma_f32_16x16x32_bf16 v[20:23],a[12:15],v[72:75],v[20:23]
s_waitcnt vmcnt(24)
s_barrier
s_add_u32 m0,s88,0
s_add_u32 s61,s82,4128768
buffer_load_dwordx4 v24,s[40:43],s61 offen offset:0  lds
s_add_u32 m0,s88,1024
ds_read_b64_tr_b16 a[32:33],v26 offset:32768
buffer_load_dwordx4 v25,s[40:43],s61 offen offset:0  lds
s_add_u32 m0,s88,8064
ds_read_b64_tr_b16 a[34:35],v26 offset:33280
buffer_load_dwordx4 v24,s[40:43],s61 offen offset:128  lds
s_add_u32 m0,s88,9088
ds_read_b64_tr_b16 a[36:37],v27 offset:32768
buffer_load_dwordx4 v25,s[40:43],s61 offen offset:128  lds
ds_read_b64_tr_b16 a[38:39],v27 offset:33280
ds_read_b64_tr_b16 a[40:41],v26 offset:36864
ds_read_b64_tr_b16 a[42:43],v26 offset:37376
ds_read_b64_tr_b16 a[44:45],v27 offset:36864
ds_read_b64_tr_b16 a[46:47],v27 offset:37376
s_waitcnt lgkmcnt(8)
v_mfma_f32_16x16x32_bf16 v[16:19],a[16:19],v[72:75],v[16:19]
v_mfma_f32_16x16x32_bf16 v[20:23],a[20:23],v[72:75],v[20:23]
v_mfma_f32_16x16x32_bf16 v[16:19],a[24:27],v[72:75],v[16:19]
v_mfma_f32_16x16x32_bf16 v[20:23],a[28:31],v[72:75],v[20:23]
s_waitcnt vmcnt(24)
s_barrier
s_add_u32 m0,s88,16384
s_add_u32 s61,s82,4587520
buffer_load_dwordx4 v24,s[40:43],s61 offen offset:0  lds
s_add_u32 m0,s88,17408
ds_read_b64_tr_b16 a[48:49],v32 offset:0
buffer_load_dwordx4 v25,s[40:43],s61 offen offset:0  lds
s_add_u32 m0,s88,24448)"
R"(
ds_read_b64_tr_b16 a[50:51],v32 offset:512
buffer_load_dwordx4 v24,s[40:43],s61 offen offset:128  lds
s_add_u32 m0,s88,25472
ds_read_b64_tr_b16 a[52:53],v33 offset:0
buffer_load_dwordx4 v25,s[40:43],s61 offen offset:128  lds
ds_read_b64_tr_b16 a[54:55],v33 offset:512
ds_read_b64_tr_b16 a[56:57],v32 offset:4096
ds_read_b64_tr_b16 a[58:59],v32 offset:4608
ds_read_b64_tr_b16 a[60:61],v33 offset:4096
ds_read_b64_tr_b16 a[62:63],v33 offset:4608
s_waitcnt lgkmcnt(8)
v_mfma_f32_16x16x32_bf16 v[16:19],a[32:35],v[72:75],v[16:19]
v_mfma_f32_16x16x32_bf16 v[20:23],a[36:39],v[72:75],v[20:23]
v_mfma_f32_16x16x32_bf16 v[16:19],a[40:43],v[72:75],v[16:19]
v_mfma_f32_16x16x32_bf16 v[20:23],a[44:47],v[72:75],v[20:23]
s_waitcnt vmcnt(24)
s_barrier
s_add_u32 m0,s88,32768
s_add_u32 s61,s82,5046272
buffer_load_dwordx4 v24,s[40:43],s61 offen offset:0  lds
s_add_u32 m0,s88,33792
ds_read_b64_tr_b16 a[64:65],v32 offset:16384
buffer_load_dwordx4 v25,s[40:43],s61 offen offset:0  lds
s_add_u32 m0,s88,40832
ds_read_b64_tr_b16 a[66:67],v32 offset:16896
buffer_load_dwordx4 v24,s[40:43],s61 offen offset:128  lds
s_add_u32 m0,s88,41856
ds_read_b64_tr_b16 a[68:69],v33 offset:16384
buffer_load_dwordx4 v25,s[40:43],s61 offen offset:128  lds
ds_read_b64_tr_b16 a[70:71],v33 offset:16896
ds_read_b64_tr_b16 a[72:73],v32 offset:20480
ds_read_b64_tr_b16 a[74:75],v32 offset:20992
ds_read_b64_tr_b16 a[76:77],v33 offset:20480
ds_read_b64_tr_b16 a[78:79],v33 offset:20992
s_waitcnt lgkmcnt(8)
v_mfma_f32_16x16x32_bf16 v[16:19],a[48:51],v[72:75],v[16:19]
v_mfma_f32_16x16x32_bf16 v[20:23],a[52:55],v[72:75],v[20:23]
v_mfma_f32_16x16x32_bf16 v[16:19],a[56:59],v[72:75],v[16:19]
v_mfma_f32_16x16x32_bf16 v[20:23],a[60:63],v[72:75],v[20:23]
s_waitcnt vmcnt(24)
s_barrier
s_add_u32 m0,s88,49152
s_add_u32 s61,s82,5505024
buffer_load_dwordx4 v24,s[40:43],s61 offen offset:0  lds
s_add_u32 m0,s88,50176
ds_read_b64_tr_b16 a[80:81],v32 offset:32768
buffer_load_dwordx4 v25,s[40:43],s61 offen offset:0  lds
s_add_u32 m0,s88,57216
ds_read_b64_tr_b16 a[82:83],v32 offset:33280
buffer_load_dwordx4 v24,s[40:43],s61 offen offset:128  lds
s_add_u32 m0,s88,58240
ds_read_b64_tr_b16 a[84:85],v33 offset:32768
buffer_load_dwordx4 v25,s[40:43],s61 offen offset:128  lds
ds_read_b64_tr_b16 a[86:87],v33 offset:33280
ds_read_b64_tr_b16 a[88:89],v32 offset:36864
ds_read_b64_tr_b16 a[90:91],v32 offset:37376
ds_read_b64_tr_b16 a[92:93],v33 offset:36864
ds_read_b64_tr_b16 a[94:95],v33 offset:37376
s_waitcnt lgkmcnt(8)
v_mfma_f32_16x16x32_bf16 v[16:19],a[64:67],v[72:75],v[16:19]
v_mfma_f32_16x16x32_bf16 v[20:23],a[68:71],v[72:75],v[20:23]
v_mfma_f32_16x16x32_bf16 v[16:19],a[72:75],v[72:75],v[16:19]
v_mfma_f32_16x16x32_bf16 v[20:23],a[76:79],v[72:75],v[20:23]
s_waitcnt vmcnt(24)
s_barrier
s_add_u32 m0,s88,65536
s_add_u32 s61,s82,5963776
buffer_load_dwordx4 v24,s[40:43],s61 offen offset:0  lds
s_add_u32 m0,s88,66560
ds_read_b64_tr_b16 a[96:97],v34 offset:0
buffer_load_dwordx4 v25,s[40:43],s61 offen offset:0  lds
s_add_u32 m0,s88,73600
ds_read_b64_tr_b16 a[98:99],v34 offset:512
buffer_load_dwordx4 v24,s[40:43],s61 offen offset:128  lds
s_add_u32 m0,s88,74624
ds_read_b64_tr_b16 a[100:101],v35 offset:0
buffer_load_dwordx4 v25,s[40:43],s61 offen offset:128  lds
ds_read_b64_tr_b16 a[102:103],v35 offset:512
ds_read_b64_tr_b16 a[104:105],v34 offset:4096
ds_read_b64_tr_b16 a[106:107],v34 offset:4608
ds_read_b64_tr_b16 a[108:109],v35 offset:4096
ds_read_b64_tr_b16 a[110:111],v35 offset:4608
s_waitcnt lgkmcnt(8)
v_mfma_f32_16x16x32_bf16 v[16:19],a[80:83],v[72:75],v[16:19]
v_mfma_f32_16x16x32_bf16 v[20:23],a[84:87],v[72:75],v[20:23]
v_mfma_f32_16x16x32_bf16 v[16:19],a[88:91],v[72:75],v[16:19]
v_mfma_f32_16x16x32_bf16 v[20:23],a[92:95],v[72:75],v[20:23]
s_waitcnt vmcnt(24)
s_barrier
s_add_u32 m0,s88,81920
s_add_u32 s61,s82,6422528
buffer_load_dwordx4 v24,s[40:43],s61 offen offset:0  lds
s_add_u32 m0,s88,82944
ds_read_b64_tr_b16 a[112:113],v34 offset:16384
buffer_load_dwordx4 v25,s[40:43],s61 offen offset:0  lds
s_add_u32 m0,s88,89984
ds_read_b64_tr_b16 a[114:115],v34 offset:16896
buffer_load_dwordx4 v24,s[40:43],s61 offen offset:128  lds
s_add_u32 m0,s88,91008
ds_read_b64_tr_b16 a[116:117],v35 offset:16384
buffer_load_dwordx4 v25,s[40:43],s61 offen offset:128  lds
ds_read_b64_tr_b16 a[118:119],v35 offset:16896
ds_read_b64_tr_b16 a[120:121],v34 offset:20480
ds_read_b64_tr_b16 a[122:123],v34 offset:20992
ds_read_b64_tr_b16 a[124:125],v35 offset:20480
ds_read_b64_tr_b16 a[126:127],v35 offset:20992
s_waitcnt lgkmcnt(8)
v_mfma_f32_16x16x32_bf16 v[16:19],a[96:99],v[72:75],v[16:19]
v_mfma_f32_16x16x32_bf16 v[20:23],a[100:103],v[72:75],v[20:23]
v_mfma_f32_16x16x32_bf16 v[16:19],a[104:107],v[72:75],v[16:19]
v_mfma_f32_16x16x32_bf16 v[20:23],a[108:111],v[72:75],v[20:23]
s_waitcnt vmcnt(24)
s_barrier
s_add_u32 m0,s88,98304
s_add_u32 s61,s82,6881280
buffer_load_dwordx4 v24,s[40:43],s61 offen offset:0  lds
s_add_u32 m0,s88,99328
ds_read_b64_tr_b16 a[128:129],v34 offset:32768
buffer_load_dwordx4 v25,s[40:43],s61 offen offset:0  lds
s_add_u32 m0,s88,106368
ds_read_b64_tr_b16 a[130:131],v34 offset:33280
buffer_load_dwordx4 v24,s[40:43],s61 offen offset:128  lds
s_add_u32 m0,s88,107392
ds_read_b64_tr_b16 a[132:133],v35 offset:32768
buffer_load_dwordx4 v25,s[40:43],s61 offen offset:128  lds
ds_read_b64_tr_b16 a[134:135],v35 offset:33280
ds_read_b64_tr_b16 a[136:137],v34 offset:36864
ds_read_b64_tr_b16 a[138:139],v34 offset:37376
ds_read_b64_tr_b16 a[140:141],v35 offset:36864
ds_read_b64_tr_b16 a[142:143],v35 offset:37376
s_waitcnt lgkmcnt(8)
v_mfma_f32_16x16x32_bf16 v[16:19],a[112:115],v[72:75],v[16:19]
v_mfma_f32_16x16x32_bf16 v[20:23],a[116:119],v[72:75],v[20:23]
v_mfma_f32_16x16x32_bf16 v[16:19],a[120:123],v[72:75],v[16:19]
v_mfma_f32_16x16x32_bf16 v[20:23],a[124:127],v[72:75],v[20:23]
s_waitcnt vmcnt(24)
s_barrier
ds_read_b64_tr_b16 a[144:145],v26 offset:0
ds_read_b64_tr_b16 a[146:147],v26 offset:512
ds_read_b64_tr_b16 a[148:149],v27 offset:0
ds_read_b64_tr_b16 a[150:151],v27 offset:512
ds_read_b64_tr_b16 a[152:153],v26 offset:4096
ds_read_b64_tr_b16 a[154:155],v26 offset:4608
ds_read_b64_tr_b16 a[156:157],v27 offset:4096
ds_read_b64_tr_b16 a[158:159],v27 offset:4608
s_waitcnt lgkmcnt(8)
v_mfma_f32_16x16x32_bf16 v[16:19],a[128:131],v[72:75],v[16:19]
v_mfma_f32_16x16x32_bf16 v[20:23],a[132:135],v[72:75],v[20:23]
v_mfma_f32_16x16x32_bf16 v[16:19],a[136:139],v[72:75],v[16:19]
v_mfma_f32_16x16x32_bf16 v[20:23],a[140:143],v[72:75],v[20:23]
s_waitcnt vmcnt(20)
s_barrier
ds_read_b64_tr_b16 a[160:161],v26 offset:16384
ds_read_b64_tr_b16 a[162:163],v26 offset:16896
ds_read_b64_tr_b16 a[164:165],v27 offset:16384
ds_read_b64_tr_b16 a[166:167],v27 offset:16896
ds_read_b64_tr_b16 a[168:169],v26 offset:20480
ds_read_b64_tr_b16 a[170:171],v26 offset:20992
ds_read_b64_tr_b16 a[172:173],v27 offset:20480
ds_read_b64_tr_b16 a[174:175],v27 offset:20992
s_waitcnt lgkmcnt(8)
v_mfma_f32_16x16x32_bf16 v[16:19],a[144:147],v[72:75],v[16:19]
v_mfma_f32_16x16x32_bf16 v[20:23],a[148:151],v[72:75],v[20:23]
v_mfma_f32_16x16x32_bf16 v[16:19],a[152:155],v[72:75],v[16:19]
v_mfma_f32_16x16x32_bf16 v[20:23],a[156:159],v[72:75],v[20:23]
s_waitcnt vmcnt(16)
s_barrier
ds_read_b64_tr_b16 a[176:177],v26 offset:32768
ds_read_b64_tr_b16 a[178:179],v26 offset:33280
ds_read_b64_tr_b16 a[180:181],v27 offset:32768
ds_read_b64_tr_b16 a[182:183],v27 offset:33280
ds_read_b64_tr_b16 a[184:185],v26 offset:36864
ds_read_b64_tr_b16 a[186:187],v26 offset:37376
ds_read_b64_tr_b16 a[188:189],v27 offset:36864
ds_read_b64_tr_b16 a[190:191],v27 offset:37376
s_waitcnt lgkmcnt(8)
v_mfma_f32_16x16x32_bf16 v[16:19],a[160:163],v[72:75],v[16:19]
v_mfma_f32_16x16x32_bf16 v[20:23],a[164:167],v[72:75],v[20:23]
v_mfma_f32_16x16x32_bf16 v[16:19],a[168:171],v[72:75],v[16:19]
v_mfma_f32_16x16x32_bf16 v[20:23],a[172:175],v[72:75],v[20:23]
s_waitcnt vmcnt(12)
s_barrier
ds_read_b64_tr_b16 a[192:193],v32 offset:0
ds_read_b64_tr_b16 a[194:195],v32 offset:512
ds_read_b64_tr_b16 a[196:197],v33 offset:0
ds_read_b64_tr_b16 a[198:199],v33 offset:512
ds_read_b64_tr_b16 a[200:201],v32 offset:4096
ds_read_b64_tr_b16 a[202:203],v32 offset:4608
ds_read_b64_tr_b16 a[204:205],v33 offset:4096
ds_read_b64_tr_b16 a[206:207],v33 offset:4608
s_waitcnt lgkmcnt(8)
v_mfma_f32_16x16x32_bf16 v[16:19],a[176:179],v[72:75],v[16:19]
v_mfma_f32_16x16x32_bf16 v[20:23],a[180:183],v[72:75],v[20:23]
v_mfma_f32_16x16x32_bf16 v[16:19],a[184:187],v[72:75],v[16:19]
v_mfma_f32_16x16x32_bf16 v[20:23],a[188:191],v[72:75],v[20:23]
s_waitcnt vmcnt(8)
s_barrier
ds_read_b64_tr_b16 a[208:209],v32 offset:16384
ds_read_b64_tr_b16 a[210:211],v32 offset:16896
ds_read_b64_tr_b16 a[212:213],v33 offset:16384)"
R"(
ds_read_b64_tr_b16 a[214:215],v33 offset:16896
ds_read_b64_tr_b16 a[216:217],v32 offset:20480
ds_read_b64_tr_b16 a[218:219],v32 offset:20992
ds_read_b64_tr_b16 a[220:221],v33 offset:20480
ds_read_b64_tr_b16 a[222:223],v33 offset:20992
s_waitcnt lgkmcnt(8)
v_mfma_f32_16x16x32_bf16 v[16:19],a[192:195],v[72:75],v[16:19]
v_mfma_f32_16x16x32_bf16 v[20:23],a[196:199],v[72:75],v[20:23]
v_mfma_f32_16x16x32_bf16 v[16:19],a[200:203],v[72:75],v[16:19]
v_mfma_f32_16x16x32_bf16 v[20:23],a[204:207],v[72:75],v[20:23]
s_waitcnt vmcnt(4)
s_barrier
ds_read_b64_tr_b16 a[224:225],v32 offset:32768
ds_read_b64_tr_b16 a[226:227],v32 offset:33280
ds_read_b64_tr_b16 a[228:229],v33 offset:32768
ds_read_b64_tr_b16 a[230:231],v33 offset:33280
ds_read_b64_tr_b16 a[232:233],v32 offset:36864
ds_read_b64_tr_b16 a[234:235],v32 offset:37376
ds_read_b64_tr_b16 a[236:237],v33 offset:36864
ds_read_b64_tr_b16 a[238:239],v33 offset:37376
s_waitcnt lgkmcnt(8)
v_mfma_f32_16x16x32_bf16 v[16:19],a[208:211],v[72:75],v[16:19]
v_mfma_f32_16x16x32_bf16 v[20:23],a[212:215],v[72:75],v[20:23]
v_mfma_f32_16x16x32_bf16 v[16:19],a[216:219],v[72:75],v[16:19]
v_mfma_f32_16x16x32_bf16 v[20:23],a[220:223],v[72:75],v[20:23]
s_waitcnt vmcnt(0)
s_barrier
ds_read_b64_tr_b16 a[240:241],v34 offset:0
ds_read_b64_tr_b16 a[242:243],v34 offset:512
ds_read_b64_tr_b16 a[244:245],v35 offset:0
ds_read_b64_tr_b16 a[246:247],v35 offset:512
ds_read_b64_tr_b16 a[248:249],v34 offset:4096
ds_read_b64_tr_b16 a[250:251],v34 offset:4608
ds_read_b64_tr_b16 a[252:253],v35 offset:4096
ds_read_b64_tr_b16 a[254:255],v35 offset:4608
s_waitcnt lgkmcnt(8)
v_mfma_f32_16x16x32_bf16 v[16:19],a[224:227],v[72:75],v[16:19]
v_mfma_f32_16x16x32_bf16 v[20:23],a[228:231],v[72:75],v[20:23]
v_mfma_f32_16x16x32_bf16 v[16:19],a[232:235],v[72:75],v[16:19]
v_mfma_f32_16x16x32_bf16 v[20:23],a[236:239],v[72:75],v[20:23]
s_waitcnt lgkmcnt(0)
v_mfma_f32_16x16x32_bf16 v[16:19],a[240:243],v[72:75],v[16:19]
v_mfma_f32_16x16x32_bf16 v[20:23],a[244:247],v[72:75],v[20:23]
v_mfma_f32_16x16x32_bf16 v[16:19],a[248:251],v[72:75],v[16:19]
v_mfma_f32_16x16x32_bf16 v[20:23],a[252:255],v[72:75],v[20:23]
s_waitcnt lgkmcnt(0)
s_barrier
v_readfirstlane_b32 s60,v5
s_nop 3
s_add_u32 s60,s60,1
s_cmp_eq_u32 s60,0
s_cselect_b32 s60,1,s60
Lg3o_init_%=:
v_mov_b32 v5,s60
s_mov_b32 s59,536870912
s_add_u32 s57,s56,131072
s_mov_b32 s79,0
s_mov_b32 s75,0x3a800000
s_mov_b32 s76,0xc0135761
s_mov_b32 s77,0x3d372713
s_mov_b32 s44,s59
s_mov_b32 s45,s59
s_mov_b32 s46,s59
s_mov_b32 s47,s59
s_mov_b32 s48,s83
s_add_u32 s49,s48,s55
s_cmp_eq_u32 s49,s56
s_cselect_b32 s49,s57,s49
s_cmp_eq_u32 s49,s58
s_cselect_b32 s49,s59,s49
s_add_u32 s50,s49,s55
s_cmp_eq_u32 s50,s56
s_cselect_b32 s50,s57,s50
s_cmp_eq_u32 s50,s58
s_cselect_b32 s50,s59,s50
s_add_u32 s51,s50,s55
s_cmp_eq_u32 s51,s56
s_cselect_b32 s51,s57,s51
s_cmp_eq_u32 s51,s58
s_cselect_b32 s51,s59,s51
s_add_u32 s52,s51,s55
s_cmp_eq_u32 s52,s56
s_cselect_b32 s52,s57,s52
s_cmp_eq_u32 s52,s58
s_cselect_b32 s52,s59,s52
s_lshl_b32 s63,s54,12
s_add_u32 s63,s63,65536
s_add_u32 s64,s63,32768
s_lshl_b32 s88,s54,10
s_add_u32 s88,s88,131072
s_lshl_b32 s78,s54,11
s_add_u32 s78,s78,132608
v_add_u32 v1,16384,v0
v_mov_b32 v12,0x3727c5ac
s_getpc_b64 s[90:91]
Lg3o_pc_%=:
s_mov_b32 s68,Lg3o_d1w0_%=-Lg3o_pc_%=
s_cmp_eq_u32 s54,1
s_cselect_b32 s68,Lg3o_d1w1_%=-Lg3o_pc_%=,s68
s_cmp_eq_u32 s54,2
s_cselect_b32 s68,Lg3o_d1w2_%=-Lg3o_pc_%=,s68
s_cmp_eq_u32 s54,3
s_cselect_b32 s68,Lg3o_d1w3_%=-Lg3o_pc_%=,s68
s_add_u32 s92,s90,s68
s_addc_u32 s93,s91,0
s_mov_b32 s68,Lg3o_d0w0_%=-Lg3o_pc_%=
s_cmp_eq_u32 s54,1
s_cselect_b32 s68,Lg3o_d0w1_%=-Lg3o_pc_%=,s68
s_cmp_eq_u32 s54,2
s_cselect_b32 s68,Lg3o_d0w2_%=-Lg3o_pc_%=,s68
s_cmp_eq_u32 s54,3
s_cselect_b32 s68,Lg3o_d0w3_%=-Lg3o_pc_%=,s68
s_add_u32 s90,s90,s68
s_addc_u32 s91,s91,0
s_mov_b32 s66,s59
s_mov_b32 s65,0
s_mov_b32 s62,s63
s_branch Lg3o_slow0_%=
Lg3o_d0w0_%=:
v_mfma_f32_16x16x32_bf16 v[24:27],a[0:3],v[40:43],0
s_add_u32 m0,s62,16384
ds_read_b128 v[68:71],v2 offset:7168
v_mul_f32 v96,s75,v92
v_mfma_f32_16x16x32_bf16 v[28:31],a[4:7],v[40:43],0
buffer_load_dwordx4 v1,s[36:39],s66 offen offset:0 sc1 lds
v_mfma_f32_16x16x32_bf16 v[24:27],a[8:11],v[44:47],v[24:27]
s_mul_i32 s73,s47,7
ds_read_b128 v[40:43],v2 offset:8192
v_fma_f32 v93,v93,s75,v12
v_fma_f32 v93,-v96,v96,v93
v_mfma_f32_16x16x32_bf16 v[28:31],a[12:15],v[44:47],v[28:31]
s_lshr_b32 s73,s73,1
v_max_f32 v93,v93,v12
v_rsq_f32 v92,v93
v_mfma_f32_16x16x32_bf16 v[24:27],a[16:19],v[48:51],v[24:27]
s_lshr_b32 s72,s66,5
ds_read_b128 v[44:47],v2 offset:9216
v_mul_f32 v97,s76,v92
v_mul_f32 v98,v92,v92
v_mfma_f32_16x16x32_bf16 v[28:31],a[20:23],v[48:51],v[28:31]
buffer_load_dwordx4 v1,s[36:39],s66 offen offset:1024 sc1 lds
v_mfma_f32_16x16x32_bf16 v[24:27],a[24:27],v[52:55],v[24:27]
s_lshr_b32 s71,s62,2
ds_read_b128 v[48:51],v2 offset:10240
v_mul_f32 v99,v93,v92
v_mul_f32 v98,s77,v98
v_mfma_f32_16x16x32_bf16 v[28:31],a[28:31],v[52:55],v[28:31]
s_waitcnt lgkmcnt(3)
v_mul_f32 v98,v98,v97
v_fma_f32 v32,-v96,v16,v32
v_fma_f32 v33,-v96,v17,v33
v_mfma_f32_16x16x32_bf16 v[24:27],a[32:35],v[56:59],v[24:27]
ds_read_b128 v[52:55],v2 offset:11264
v_fma_f32 v34,-v96,v18,v34
v_fma_f32 v35,-v96,v19,v35
v_mfma_f32_16x16x32_bf16 v[28:31],a[36:39],v[56:59],v[28:31]
buffer_load_dwordx4 v1,s[36:39],s66 offen offset:2048 sc1 lds
v_mfma_f32_16x16x32_bf16 v[24:27],a[40:43],v[60:63],v[24:27]
ds_read_b128 v[56:59],v2 offset:12288
v_mul_f32 v72,v32,v32
v_mul_f32 v73,v33,v33
v_mfma_f32_16x16x32_bf16 v[28:31],a[44:47],v[60:63],v[28:31]
v_mul_f32 v74,v34,v34
v_mul_f32 v75,v35,v35
v_fma_f32 v72,v98,v72,v97
v_mfma_f32_16x16x32_bf16 v[24:27],a[48:51],v[64:67],v[24:27]
ds_read_b128 v[60:63],v2 offset:13312
v_mul_f32 v72,v32,v72
v_fma_f32 v73,v98,v73,v97
v_mfma_f32_16x16x32_bf16 v[28:31],a[52:55],v[64:67],v[28:31]
buffer_load_dwordx4 v1,s[36:39],s66 offen offset:3072 sc1 lds
v_mfma_f32_16x16x32_bf16 v[24:27],a[56:59],v[68:71],v[24:27]
s_add_u32 m0,s71,s88
ds_read_b128 v[64:67],v2 offset:14336
v_exp_f32 v72,v72
v_mfma_f32_16x16x32_bf16 v[28:31],a[60:63],v[68:71],v[28:31]
s_waitcnt lgkmcnt(3)
buffer_load_dword v9,s[36:39],s72 offen offset:0 sc1 lds
v_mul_f32 v73,v33,v73
v_mfma_f32_16x16x32_bf16 v[24:27],a[64:67],v[40:43],v[24:27]
s_waitcnt vmcnt(11)
ds_read_b128 v[68:71],v2 offset:15360
v_exp_f32 v73,v73
v_mfma_f32_16x16x32_bf16 v[28:31],a[68:71],v[40:43],v[28:31]
s_barrier
v_mfma_f32_16x16x32_bf16 v[24:27],a[72:75],v[44:47],v[24:27]
s_xor_b32 s87,s64,65536
ds_read_b128 v[40:43],v2 offset:16384
ds_read_b32 v88,v8 offset:0
v_fma_f32 v72,v72,v99,v99
v_mfma_f32_16x16x32_bf16 v[28:31],a[76:79],v[44:47],v[28:31]
s_cmp_ge_u32 s66,s59
v_rcp_f32 v72,v72
v_fma_f32 v73,v73,v99,v99
v_mfma_f32_16x16x32_bf16 v[24:27],a[80:83],v[48:51],v[24:27]
s_cselect_b32 s86,s49,s50
ds_read_b128 v[44:47],v2 offset:17408
v_rcp_f32 v73,v73
v_mfma_f32_16x16x32_bf16 v[28:31],a[84:87],v[48:51],v[28:31]
s_cselect_b32 s87,s87,s63)"
R"(
v_fma_f32 v74,v98,v74,v97
v_mul_f32 v74,v34,v74
v_mfma_f32_16x16x32_bf16 v[24:27],a[88:91],v[52:55],v[24:27]
s_lshr_b32 s68,s50,5
ds_read_b128 v[48:51],v2 offset:18432
v_exp_f32 v74,v74
v_mfma_f32_16x16x32_bf16 v[28:31],a[92:95],v[52:55],v[28:31]
s_waitcnt lgkmcnt(4)
v_fma_f32 v74,v74,v99,v99
v_rcp_f32 v74,v74
v_mfma_f32_16x16x32_bf16 v[24:27],a[96:99],v[56:59],v[24:27]
s_waitcnt lgkmcnt(2)
ds_read_b128 v[52:55],v2 offset:19456
v_fma_f32 v75,v98,v75,v97
v_mul_f32 v75,v35,v75
v_mfma_f32_16x16x32_bf16 v[28:31],a[100:103],v[56:59],v[28:31]
s_lshr_b32 s70,s51,5
v_exp_f32 v75,v75
v_mfma_f32_16x16x32_bf16 v[24:27],a[104:107],v[60:63],v[24:27]
s_mov_b32 s62,s87
ds_read_b128 v[56:59],v2 offset:20480
v_fma_f32 v75,v75,v99,v99
v_add_f32_dpp v88,v88,v88 quad_perm:[1,0,3,2] row_mask:0xf bank_mask:0xf
v_mfma_f32_16x16x32_bf16 v[28:31],a[108:111],v[60:63],v[28:31]
v_rcp_f32 v75,v75
v_add_f32_dpp v88,v88,v88 quad_perm:[2,3,0,1] row_mask:0xf bank_mask:0xf
v_mfma_f32_16x16x32_bf16 v[24:27],a[112:115],v[64:67],v[24:27]
ds_read_b128 v[60:63],v2 offset:21504
v_add_f32_dpp v88,v88,v88 row_half_mirror row_mask:0xf bank_mask:0xf
v_mul_f32 v32,v32,v72
v_mfma_f32_16x16x32_bf16 v[28:31],a[116:119],v[64:67],v[28:31]
ds_write_b32 v10,v88 offset:0
v_mfma_f32_16x16x32_bf16 v[24:27],a[120:123],v[68:71],v[24:27]
ds_read_b128 v[64:67],v2 offset:22528
v_mul_f32 v33,v33,v73
v_mul_f32 v34,v34,v74
v_mfma_f32_16x16x32_bf16 v[28:31],a[124:127],v[68:71],v[28:31]
s_waitcnt lgkmcnt(4)
v_mul_f32 v35,v35,v75
v_cvt_pk_bf16_f32 v80,v32,v33
v_cvt_pk_bf16_f32 v81,v34,v35
v_mfma_f32_16x16x32_bf16 v[24:27],a[128:131],v[40:43],v[24:27]
s_waitcnt vmcnt(10)
ds_read_b128 v[68:71],v2 offset:23552
v_fma_f32 v36,-v96,v20,v36
v_fma_f32 v37,-v96,v21,v37
v_mfma_f32_16x16x32_bf16 v[28:31],a[132:135],v[40:43],v[28:31]
s_barrier
v_mfma_f32_16x16x32_bf16 v[24:27],a[136:139],v[44:47],v[24:27]
ds_read_b128 v[40:43],v2 offset:24576
ds_read_b32 v13,v7 offset:0
v_fma_f32 v38,-v96,v22,v38
v_mfma_f32_16x16x32_bf16 v[28:31],a[140:143],v[44:47],v[28:31]
v_fma_f32 v39,-v96,v23,v39
v_mul_f32 v76,v36,v36
v_mul_f32 v77,v37,v37
v_mfma_f32_16x16x32_bf16 v[24:27],a[144:147],v[48:51],v[24:27]
ds_read_b128 v[44:47],v2 offset:25600
v_mul_f32 v78,v38,v38
v_mul_f32 v79,v39,v39
v_mfma_f32_16x16x32_bf16 v[28:31],a[148:151],v[48:51],v[28:31]
v_fma_f32 v76,v98,v76,v97
v_mul_f32 v76,v36,v76
v_mfma_f32_16x16x32_bf16 v[24:27],a[152:155],v[52:55],v[24:27]
s_waitcnt lgkmcnt(1)
ds_read_b128 v[48:51],v2 offset:26624
v_exp_f32 v76,v76
v_mfma_f32_16x16x32_bf16 v[28:31],a[156:159],v[52:55],v[28:31]
v_cmp_eq_u32 vcc,s60,v13
v_fma_f32 v76,v76,v99,v99
v_rcp_f32 v76,v76
v_mfma_f32_16x16x32_bf16 v[24:27],a[160:163],v[56:59],v[24:27]
s_cmp_eq_u32 vcc_lo,-1
ds_read_b128 v[52:55],v2 offset:27648
v_fma_f32 v77,v98,v77,v97
v_mul_f32 v77,v37,v77
v_mfma_f32_16x16x32_bf16 v[28:31],a[164:167],v[56:59],v[28:31]
s_cselect_b32 s67,s86,s59
s_cmp_ge_u32 s67,s59
s_cselect_b32 s68,s68,s70
s_add_u32 m0,s78,256
v_mfma_f32_16x16x32_bf16 v[24:27],a[168:171],v[60:63],v[24:27]
buffer_load_dword v6,s[36:39],s68 offen offset:0 sc1 lds
ds_read_b128 v[56:59],v2 offset:28672
v_mfma_f32_16x16x32_bf16 v[28:31],a[172:175],v[60:63],v[28:31]
s_mov_b32 m0,s62
v_exp_f32 v77,v77
v_fma_f32 v78,v98,v78,v97
v_mfma_f32_16x16x32_bf16 v[24:27],a[176:179],v[64:67],v[24:27]
buffer_load_dwordx4 v0,s[36:39],s67 offen offset:0 sc1 lds
ds_read_b128 v[60:63],v2 offset:29696
v_mfma_f32_16x16x32_bf16 v[28:31],a[180:183],v[64:67],v[28:31]
s_xor_b32 s63,s63,65536
v_mul_f32 v78,v38,v78
v_fma_f32 v77,v77,v99,v99
v_mfma_f32_16x16x32_bf16 v[24:27],a[184:187],v[68:71],v[24:27]
s_add_u32 s53,s52,s55
ds_read_b128 v[64:67],v2 offset:30720
v_exp_f32 v78,v78
v_mfma_f32_16x16x32_bf16 v[28:31],a[188:191],v[68:71],v[28:31]
s_waitcnt lgkmcnt(3)
v_rcp_f32 v77,v77
v_fma_f32 v78,v78,v99,v99
v_mfma_f32_16x16x32_bf16 v[24:27],a[192:195],v[40:43],v[24:27]
s_waitcnt vmcnt(7)
ds_read_b128 v[68:71],v2 offset:31744
v_rcp_f32 v78,v78
v_mfma_f32_16x16x32_bf16 v[28:31],a[196:199],v[40:43],v[28:31]
s_barrier
v_mfma_f32_16x16x32_bf16 v[24:27],a[200:203],v[44:47],v[24:27]
buffer_load_dwordx4 v0,s[36:39],s67 offen offset:1024 sc1 lds
ds_read_b128 v[40:43],v2 offset:32768
v_mfma_f32_16x16x32_bf16 v[28:31],a[204:207],v[44:47],v[28:31]
s_cmp_eq_u32 s53,s56
ds_read_b64 v[92:93],v11 offset:0
v_fma_f32 v79,v98,v79,v97
v_mul_f32 v79,v39,v79
v_mfma_f32_16x16x32_bf16 v[24:27],a[208:211],v[48:51],v[24:27]
s_cselect_b32 s53,s57,s53
ds_read_b128 v[44:47],v2 offset:33792
v_exp_f32 v79,v79
v_mfma_f32_16x16x32_bf16 v[28:31],a[212:215],v[48:51],v[28:31]
s_cmp_eq_u32 s53,s58
v_fma_f32 v79,v79,v99,v99
v_mul_f32 v36,v36,v76
v_mfma_f32_16x16x32_bf16 v[24:27],a[216:219],v[52:55],v[24:27]
buffer_load_dwordx4 v0,s[36:39],s67 offen offset:2048 sc1 lds
v_rcp_f32 v79,v79
v_mfma_f32_16x16x32_bf16 v[28:31],a[220:223],v[52:55],v[28:31]
s_waitcnt lgkmcnt(3)
ds_read_b128 v[48:51],v2 offset:34816
v_mul_f32 v37,v37,v77
v_mul_f32 v38,v38,v78
v_mul_f32 v39,v39,v79
v_cvt_pk_bf16_f32 v82,v36,v37
v_cvt_pk_bf16_f32 v83,v38,v39
v_mfma_f32_16x16x32_bf16 v[24:27],a[224:227],v[56:59],v[24:27]
s_waitcnt lgkmcnt(2)
buffer_store_dwordx4 v[80:83],v3,s[36:39],s73 offen offset:0 sc0 sc1
ds_read_b128 v[52:55],v2 offset:35840
s_cselect_b32 s53,s59,s53
v_mfma_f32_16x16x32_bf16 v[28:31],a[228:231],v[56:59],v[28:31]
s_cmp_ge_u32 s66,s59
v_mfma_f32_16x16x32_bf16 v[24:27],a[232:235],v[60:63],v[24:27]
buffer_load_dwordx4 v0,s[36:39],s67 offen offset:3072 sc1 lds
v_mfma_f32_16x16x32_bf16 v[28:31],a[236:239],v[60:63],v[28:31]
ds_read_b128 v[56:59],v2 offset:36864
v_mfma_f32_16x16x32_bf16 v[24:27],a[240:243],v[64:67],v[24:27]
ds_read_b128 v[60:63],v2 offset:37888
v_mfma_f32_16x16x32_bf16 v[28:31],a[244:247],v[64:67],v[28:31]
v_mfma_f32_16x16x32_bf16 v[24:27],a[248:251],v[68:71],v[24:27]
s_waitcnt lgkmcnt(2)
ds_read_b128 v[64:67],v2 offset:38912
v_mfma_f32_16x16x32_bf16 v[28:31],a[252:255],v[68:71],v[28:31]
s_cbranch_scc1 Lg3o_slow1_%=
Lg3o_d1w0_%=:
v_mfma_f32_16x16x32_bf16 v[32:35],a[0:3],v[40:43],0
s_add_u32 m0,s62,16384
ds_read_b128 v[68:71],v2 offset:39936
v_mul_f32 v96,s75,v92
v_mfma_f32_16x16x32_bf16 v[36:39],a[4:7],v[40:43],0
buffer_load_dwordx4 v1,s[36:39],s67 offen offset:0 sc1 lds
v_mfma_f32_16x16x32_bf16 v[32:35],a[8:11],v[44:47],v[32:35]
s_mul_i32 s73,s48,7
ds_read_b128 v[40:43],v2 offset:40960
v_fma_f32 v93,v93,s75,v12
v_fma_f32 v93,-v96,v96,v93
v_mfma_f32_16x16x32_bf16 v[36:39],a[12:15],v[44:47],v[36:39]
s_lshr_b32 s73,s73,1
v_max_f32 v93,v93,v12
v_rsq_f32 v92,v93
v_mfma_f32_16x16x32_bf16 v[32:35],a[16:19],v[48:51],v[32:35]
s_lshr_b32 s72,s67,5
ds_read_b128 v[44:47],v2 offset:41984
v_mul_f32 v97,s76,v92
v_mul_f32 v98,v92,v92
v_mfma_f32_16x16x32_bf16 v[36:39],a[20:23],v[48:51],v[36:39]
buffer_load_dwordx4 v1,s[36:39],s67 offen offset:1024 sc1 lds
v_mfma_f32_16x16x32_bf16 v[32:35],a[24:27],v[52:55],v[32:35]
s_lshr_b32 s71,s62,2
ds_read_b128 v[48:51],v2 offset:43008
v_mul_f32 v99,v93,v92
v_mul_f32 v98,s77,v98
v_mfma_f32_16x16x32_bf16 v[36:39],a[28:31],v[52:55],v[36:39]
s_waitcnt lgkmcnt(3)
v_mul_f32 v98,v98,v97
v_fma_f32 v24,-v96,v16,v24
v_fma_f32 v25,-v96,v17,v25
v_mfma_f32_16x16x32_bf16 v[32:35],a[32:35],v[56:59],v[32:35]
s_mov_b64 s[44:45],s[46:47]
ds_read_b128 v[52:55],v2 offset:44032
v_fma_f32 v26,-v96,v18,v26
v_fma_f32 v27,-v96,v19,v27
v_mfma_f32_16x16x32_bf16 v[36:39],a[36:39],v[56:59],v[36:39]
buffer_load_dwordx4 v1,s[36:39],s67 offen offset:2048 sc1 lds
v_mfma_f32_16x16x32_bf16 v[32:35],a[40:43],v[60:63],v[32:35]
s_mov_b64 s[46:47],s[48:49]
ds_read_b128 v[56:59],v2 offset:45056
v_mul_f32 v72,v24,v24
v_mul_f32 v73,v25,v25)"
R"(
v_mfma_f32_16x16x32_bf16 v[36:39],a[44:47],v[60:63],v[36:39]
s_mov_b64 s[48:49],s[50:51]
v_mul_f32 v74,v26,v26
v_mul_f32 v75,v27,v27
v_fma_f32 v72,v98,v72,v97
v_mfma_f32_16x16x32_bf16 v[32:35],a[48:51],v[64:67],v[32:35]
ds_read_b128 v[60:63],v2 offset:46080
v_mul_f32 v72,v24,v72
v_fma_f32 v73,v98,v73,v97
v_mfma_f32_16x16x32_bf16 v[36:39],a[52:55],v[64:67],v[36:39]
buffer_load_dwordx4 v1,s[36:39],s67 offen offset:3072 sc1 lds
v_mfma_f32_16x16x32_bf16 v[32:35],a[56:59],v[68:71],v[32:35]
s_add_u32 m0,s71,s88
ds_read_b128 v[64:67],v2 offset:47104
v_exp_f32 v72,v72
v_mfma_f32_16x16x32_bf16 v[36:39],a[60:63],v[68:71],v[36:39]
s_waitcnt lgkmcnt(3)
buffer_load_dword v9,s[36:39],s72 offen offset:0 sc1 lds
v_mul_f32 v73,v25,v73
v_mfma_f32_16x16x32_bf16 v[32:35],a[64:67],v[40:43],v[32:35]
s_waitcnt vmcnt(11)
ds_read_b128 v[68:71],v2 offset:48128
v_exp_f32 v73,v73
v_mfma_f32_16x16x32_bf16 v[36:39],a[68:71],v[40:43],v[36:39]
s_barrier
v_mfma_f32_16x16x32_bf16 v[32:35],a[72:75],v[44:47],v[32:35]
s_xor_b32 s87,s63,65536
ds_read_b128 v[40:43],v2 offset:49152
ds_read_b32 v88,v8 offset:8192
v_fma_f32 v72,v72,v99,v99
v_mfma_f32_16x16x32_bf16 v[36:39],a[76:79],v[44:47],v[36:39]
s_cmp_ge_u32 s67,s59
v_rcp_f32 v72,v72
v_fma_f32 v73,v73,v99,v99
v_mfma_f32_16x16x32_bf16 v[32:35],a[80:83],v[48:51],v[32:35]
s_cselect_b32 s86,s50,s51
ds_read_b128 v[44:47],v2 offset:50176
v_rcp_f32 v73,v73
v_mfma_f32_16x16x32_bf16 v[36:39],a[84:87],v[48:51],v[36:39]
s_cselect_b32 s87,s87,s64
v_fma_f32 v74,v98,v74,v97
v_mul_f32 v74,v26,v74
v_mfma_f32_16x16x32_bf16 v[32:35],a[88:91],v[52:55],v[32:35]
s_lshr_b32 s68,s51,5
ds_read_b128 v[48:51],v2 offset:51200
v_exp_f32 v74,v74
v_mfma_f32_16x16x32_bf16 v[36:39],a[92:95],v[52:55],v[36:39]
s_waitcnt lgkmcnt(4)
v_fma_f32 v74,v74,v99,v99
v_rcp_f32 v74,v74
v_mfma_f32_16x16x32_bf16 v[32:35],a[96:99],v[56:59],v[32:35]
s_waitcnt lgkmcnt(2)
ds_read_b128 v[52:55],v2 offset:52224
v_fma_f32 v75,v98,v75,v97
v_mul_f32 v75,v27,v75
v_mfma_f32_16x16x32_bf16 v[36:39],a[100:103],v[56:59],v[36:39]
s_lshr_b32 s70,s52,5
v_exp_f32 v75,v75
v_mfma_f32_16x16x32_bf16 v[32:35],a[104:107],v[60:63],v[32:35]
s_mov_b32 s62,s87
ds_read_b128 v[56:59],v2 offset:53248
v_fma_f32 v75,v75,v99,v99
v_add_f32_dpp v88,v88,v88 quad_perm:[1,0,3,2] row_mask:0xf bank_mask:0xf
v_mfma_f32_16x16x32_bf16 v[36:39],a[108:111],v[60:63],v[36:39]
s_mov_b64 s[50:51],s[52:53]
v_rcp_f32 v75,v75
v_add_f32_dpp v88,v88,v88 quad_perm:[2,3,0,1] row_mask:0xf bank_mask:0xf
v_mfma_f32_16x16x32_bf16 v[32:35],a[112:115],v[64:67],v[32:35]
ds_read_b128 v[60:63],v2 offset:54272
v_add_f32_dpp v88,v88,v88 row_half_mirror row_mask:0xf bank_mask:0xf
v_mul_f32 v24,v24,v72
v_mfma_f32_16x16x32_bf16 v[36:39],a[116:119],v[64:67],v[36:39]
ds_write_b32 v10,v88 offset:128
v_mfma_f32_16x16x32_bf16 v[32:35],a[120:123],v[68:71],v[32:35]
ds_read_b128 v[64:67],v2 offset:55296
v_mul_f32 v25,v25,v73
v_mul_f32 v26,v26,v74
v_mfma_f32_16x16x32_bf16 v[36:39],a[124:127],v[68:71],v[36:39]
s_waitcnt lgkmcnt(4)
v_mul_f32 v27,v27,v75
v_cvt_pk_bf16_f32 v80,v24,v25
v_cvt_pk_bf16_f32 v81,v26,v27
v_mfma_f32_16x16x32_bf16 v[32:35],a[128:131],v[40:43],v[32:35]
s_waitcnt vmcnt(10)
ds_read_b128 v[68:71],v2 offset:56320
v_fma_f32 v28,-v96,v20,v28
v_fma_f32 v29,-v96,v21,v29
v_mfma_f32_16x16x32_bf16 v[36:39],a[132:135],v[40:43],v[36:39]
s_barrier
v_mfma_f32_16x16x32_bf16 v[32:35],a[136:139],v[44:47],v[32:35]
ds_read_b128 v[40:43],v2 offset:57344
ds_read_b32 v13,v7 offset:256
v_fma_f32 v30,-v96,v22,v30
v_mfma_f32_16x16x32_bf16 v[36:39],a[140:143],v[44:47],v[36:39]
v_fma_f32 v31,-v96,v23,v31
v_mul_f32 v76,v28,v28
v_mul_f32 v77,v29,v29
v_mfma_f32_16x16x32_bf16 v[32:35],a[144:147],v[48:51],v[32:35]
ds_read_b128 v[44:47],v2 offset:58368
v_mul_f32 v78,v30,v30
v_mul_f32 v79,v31,v31
v_mfma_f32_16x16x32_bf16 v[36:39],a[148:151],v[48:51],v[36:39]
v_fma_f32 v76,v98,v76,v97
v_mul_f32 v76,v28,v76
v_mfma_f32_16x16x32_bf16 v[32:35],a[152:155],v[52:55],v[32:35]
s_waitcnt lgkmcnt(1)
ds_read_b128 v[48:51],v2 offset:59392
v_exp_f32 v76,v76
v_mfma_f32_16x16x32_bf16 v[36:39],a[156:159],v[52:55],v[36:39]
v_cmp_eq_u32 vcc,s60,v13
v_fma_f32 v76,v76,v99,v99
v_rcp_f32 v76,v76
v_mfma_f32_16x16x32_bf16 v[32:35],a[160:163],v[56:59],v[32:35]
s_cmp_eq_u32 vcc_lo,-1
ds_read_b128 v[52:55],v2 offset:60416
v_fma_f32 v77,v98,v77,v97
v_mul_f32 v77,v29,v77
v_mfma_f32_16x16x32_bf16 v[36:39],a[164:167],v[56:59],v[36:39]
s_cselect_b32 s66,s86,s59
s_cmp_ge_u32 s66,s59
s_cselect_b32 s68,s68,s70
s_add_u32 m0,s78,0
v_mfma_f32_16x16x32_bf16 v[32:35],a[168:171],v[60:63],v[32:35]
buffer_load_dword v6,s[36:39],s68 offen offset:0 sc1 lds
ds_read_b128 v[56:59],v2 offset:61440
v_mfma_f32_16x16x32_bf16 v[36:39],a[172:175],v[60:63],v[36:39]
s_mov_b32 m0,s62
v_exp_f32 v77,v77
v_fma_f32 v78,v98,v78,v97
v_mfma_f32_16x16x32_bf16 v[32:35],a[176:179],v[64:67],v[32:35]
buffer_load_dwordx4 v0,s[36:39],s66 offen offset:0 sc1 lds
ds_read_b128 v[60:63],v2 offset:62464
v_mfma_f32_16x16x32_bf16 v[36:39],a[180:183],v[64:67],v[36:39]
s_xor_b32 s64,s64,65536
v_mul_f32 v78,v30,v78
v_fma_f32 v77,v77,v99,v99
v_mfma_f32_16x16x32_bf16 v[32:35],a[184:187],v[68:71],v[32:35]
s_add_u32 s74,s53,s55
ds_read_b128 v[64:67],v2 offset:63488
v_exp_f32 v78,v78
v_mfma_f32_16x16x32_bf16 v[36:39],a[188:191],v[68:71],v[36:39]
s_waitcnt lgkmcnt(3)
v_rcp_f32 v77,v77
v_fma_f32 v78,v78,v99,v99
v_mfma_f32_16x16x32_bf16 v[32:35],a[192:195],v[40:43],v[32:35]
s_waitcnt vmcnt(7)
ds_read_b128 v[68:71],v2 offset:64512
v_xor_b32 v2,65536,v2
v_fma_f32 v79,v98,v79,v97
v_mfma_f32_16x16x32_bf16 v[36:39],a[196:199],v[40:43],v[36:39]
s_barrier
v_mfma_f32_16x16x32_bf16 v[32:35],a[200:203],v[44:47],v[32:35]
buffer_load_dwordx4 v0,s[36:39],s66 offen offset:1024 sc1 lds
ds_read_b128 v[40:43],v2 offset:0
v_mfma_f32_16x16x32_bf16 v[36:39],a[204:207],v[44:47],v[36:39]
s_cmp_eq_u32 s74,s56
ds_read_b64 v[92:93],v11 offset:128
v_rcp_f32 v78,v78
v_mfma_f32_16x16x32_bf16 v[32:35],a[208:211],v[48:51],v[32:35]
s_cselect_b32 s74,s57,s74
ds_read_b128 v[44:47],v2 offset:1024
v_mul_f32 v79,v31,v79
v_mul_f32 v28,v28,v76
v_mfma_f32_16x16x32_bf16 v[36:39],a[212:215],v[48:51],v[36:39]
s_cmp_eq_u32 s74,s58
v_exp_f32 v79,v79
v_mfma_f32_16x16x32_bf16 v[32:35],a[216:219],v[52:55],v[32:35]
buffer_load_dwordx4 v0,s[36:39],s66 offen offset:2048 sc1 lds
v_fma_f32 v79,v79,v99,v99
v_rcp_f32 v79,v79
v_mfma_f32_16x16x32_bf16 v[36:39],a[220:223],v[52:55],v[36:39]
s_waitcnt lgkmcnt(3)
ds_read_b128 v[48:51],v2 offset:2048
v_mul_f32 v29,v29,v77
v_mul_f32 v30,v30,v78
v_mul_f32 v31,v31,v79
v_cvt_pk_bf16_f32 v82,v28,v29
v_cvt_pk_bf16_f32 v83,v30,v31
v_mfma_f32_16x16x32_bf16 v[32:35],a[224:227],v[56:59],v[32:35]
s_waitcnt lgkmcnt(2)
buffer_store_dwordx4 v[80:83],v3,s[36:39],s73 offen offset:0 sc0 sc1
ds_read_b128 v[52:55],v2 offset:3072
v_xor_b32 v8,16384,v8
s_cselect_b32 s74,s59,s74
v_mfma_f32_16x16x32_bf16 v[36:39],a[228:231],v[56:59],v[36:39]
s_mov_b32 s52,s74
s_cmp_lt_u32 s67,s59
v_mfma_f32_16x16x32_bf16 v[32:35],a[232:235],v[60:63],v[32:35]
buffer_load_dwordx4 v0,s[36:39],s66 offen offset:3072 sc1 lds
v_mfma_f32_16x16x32_bf16 v[36:39],a[236:239],v[60:63],v[36:39]
ds_read_b128 v[56:59],v2 offset:4096
v_mfma_f32_16x16x32_bf16 v[32:35],a[240:243],v[64:67],v[32:35]
ds_read_b128 v[60:63],v2 offset:5120
v_mfma_f32_16x16x32_bf16 v[36:39],a[244:247],v[64:67],v[36:39]
v_mfma_f32_16x16x32_bf16 v[32:35],a[248:251],v[68:71],v[32:35]
s_waitcnt lgkmcnt(2)
ds_read_b128 v[64:67],v2 offset:6144
v_mfma_f32_16x16x32_bf16 v[36:39],a[252:255],v[68:71],v[36:39]
s_cbranch_scc1 Lg3o_d0w0_%=
s_branch Lg3o_slow0_%=)"
R"(
Lg3o_d0w1_%=:
v_mfma_f32_16x16x32_bf16 v[24:27],a[0:3],v[40:43],0
s_add_u32 m0,s62,16384
ds_read_b128 v[68:71],v2 offset:7168
v_mul_f32 v96,s75,v92
v_mfma_f32_16x16x32_bf16 v[28:31],a[4:7],v[40:43],0
s_mul_i32 s73,s47,7
v_fma_f32 v93,v93,s75,v12
v_fma_f32 v93,-v96,v96,v93
v_mfma_f32_16x16x32_bf16 v[24:27],a[8:11],v[44:47],v[24:27]
buffer_load_dwordx4 v1,s[36:39],s66 offen offset:0 sc1 lds
ds_read_b128 v[40:43],v2 offset:8192
v_mfma_f32_16x16x32_bf16 v[28:31],a[12:15],v[44:47],v[28:31]
s_lshr_b32 s73,s73,1
v_max_f32 v93,v93,v12
v_rsq_f32 v92,v93
v_mfma_f32_16x16x32_bf16 v[24:27],a[16:19],v[48:51],v[24:27]
s_lshr_b32 s72,s66,5
ds_read_b128 v[44:47],v2 offset:9216
v_mul_f32 v97,s76,v92
v_mfma_f32_16x16x32_bf16 v[28:31],a[20:23],v[48:51],v[28:31]
s_lshr_b32 s71,s62,2
v_mul_f32 v98,v92,v92
v_mul_f32 v99,v93,v92
v_mul_f32 v98,s77,v98
v_mfma_f32_16x16x32_bf16 v[24:27],a[24:27],v[52:55],v[24:27]
buffer_load_dwordx4 v1,s[36:39],s66 offen offset:1024 sc1 lds
ds_read_b128 v[48:51],v2 offset:10240
v_mfma_f32_16x16x32_bf16 v[28:31],a[28:31],v[52:55],v[28:31]
s_waitcnt lgkmcnt(3)
v_mul_f32 v98,v98,v97
v_fma_f32 v32,-v96,v16,v32
v_mfma_f32_16x16x32_bf16 v[24:27],a[32:35],v[56:59],v[24:27]
ds_read_b128 v[52:55],v2 offset:11264
v_fma_f32 v33,-v96,v17,v33
v_fma_f32 v34,-v96,v18,v34
v_mfma_f32_16x16x32_bf16 v[28:31],a[36:39],v[56:59],v[28:31]
v_fma_f32 v35,-v96,v19,v35
v_mul_f32 v72,v32,v32
v_mfma_f32_16x16x32_bf16 v[24:27],a[40:43],v[60:63],v[24:27]
buffer_load_dwordx4 v1,s[36:39],s66 offen offset:2048 sc1 lds
ds_read_b128 v[56:59],v2 offset:12288
v_mfma_f32_16x16x32_bf16 v[28:31],a[44:47],v[60:63],v[28:31]
v_mul_f32 v73,v33,v33
v_mul_f32 v74,v34,v34
v_mul_f32 v75,v35,v35
v_mfma_f32_16x16x32_bf16 v[24:27],a[48:51],v[64:67],v[24:27]
ds_read_b128 v[60:63],v2 offset:13312
v_fma_f32 v72,v98,v72,v97
v_mfma_f32_16x16x32_bf16 v[28:31],a[52:55],v[64:67],v[28:31]
v_mul_f32 v72,v32,v72
v_exp_f32 v72,v72
v_mfma_f32_16x16x32_bf16 v[24:27],a[56:59],v[68:71],v[24:27]
buffer_load_dwordx4 v1,s[36:39],s66 offen offset:3072 sc1 lds
ds_read_b128 v[64:67],v2 offset:14336
v_mfma_f32_16x16x32_bf16 v[28:31],a[60:63],v[68:71],v[28:31]
s_waitcnt lgkmcnt(3)
v_fma_f32 v72,v72,v99,v99
s_add_u32 m0,s71,s88
v_mfma_f32_16x16x32_bf16 v[24:27],a[64:67],v[40:43],v[24:27]
s_xor_b32 s87,s64,65536
ds_read_b128 v[68:71],v2 offset:15360
buffer_load_dword v9,s[36:39],s72 offen offset:0 sc1 lds
s_waitcnt vmcnt(10)
v_mfma_f32_16x16x32_bf16 v[28:31],a[68:71],v[40:43],v[28:31]
s_barrier
v_mfma_f32_16x16x32_bf16 v[24:27],a[72:75],v[44:47],v[24:27]
s_cmp_ge_u32 s66,s59
ds_read_b128 v[40:43],v2 offset:16384
ds_read_b32 v88,v8 offset:0
v_fma_f32 v73,v98,v73,v97
v_mfma_f32_16x16x32_bf16 v[28:31],a[76:79],v[44:47],v[28:31]
s_cselect_b32 s86,s49,s50
v_rcp_f32 v72,v72
v_mul_f32 v73,v33,v73
v_mfma_f32_16x16x32_bf16 v[24:27],a[80:83],v[48:51],v[24:27]
s_cselect_b32 s87,s87,s63
ds_read_b128 v[44:47],v2 offset:17408
v_fma_f32 v74,v98,v74,v97
v_mfma_f32_16x16x32_bf16 v[28:31],a[84:87],v[48:51],v[28:31]
s_mov_b32 s62,s87
v_exp_f32 v73,v73
v_mul_f32 v74,v34,v74
v_mfma_f32_16x16x32_bf16 v[24:27],a[88:91],v[52:55],v[24:27]
s_mov_b32 m0,s62
ds_read_b128 v[48:51],v2 offset:18432
v_exp_f32 v74,v74
v_mfma_f32_16x16x32_bf16 v[28:31],a[92:95],v[52:55],v[28:31]
s_waitcnt lgkmcnt(4)
v_fma_f32 v73,v73,v99,v99
v_fma_f32 v74,v74,v99,v99
v_mfma_f32_16x16x32_bf16 v[24:27],a[96:99],v[56:59],v[24:27]
s_waitcnt lgkmcnt(2)
ds_read_b128 v[52:55],v2 offset:19456
v_rcp_f32 v73,v73
v_mfma_f32_16x16x32_bf16 v[28:31],a[100:103],v[56:59],v[28:31]
v_rcp_f32 v74,v74
v_mfma_f32_16x16x32_bf16 v[24:27],a[104:107],v[60:63],v[24:27]
ds_read_b128 v[56:59],v2 offset:20480
v_add_f32_dpp v88,v88,v88 quad_perm:[1,0,3,2] row_mask:0xf bank_mask:0xf
v_fma_f32 v75,v98,v75,v97
v_mfma_f32_16x16x32_bf16 v[28:31],a[108:111],v[60:63],v[28:31]
v_mul_f32 v75,v35,v75
v_exp_f32 v75,v75
v_mfma_f32_16x16x32_bf16 v[24:27],a[112:115],v[64:67],v[24:27]
ds_read_b128 v[60:63],v2 offset:21504
v_fma_f32 v75,v75,v99,v99
v_mfma_f32_16x16x32_bf16 v[28:31],a[116:119],v[64:67],v[28:31]
v_rcp_f32 v75,v75
v_add_f32_dpp v88,v88,v88 quad_perm:[2,3,0,1] row_mask:0xf bank_mask:0xf
v_mfma_f32_16x16x32_bf16 v[24:27],a[120:123],v[68:71],v[24:27]
ds_read_b128 v[64:67],v2 offset:22528
v_add_f32_dpp v88,v88,v88 row_half_mirror row_mask:0xf bank_mask:0xf
v_mul_f32 v32,v32,v72
v_mfma_f32_16x16x32_bf16 v[28:31],a[124:127],v[68:71],v[28:31]
s_waitcnt lgkmcnt(3)
ds_write_b32 v10,v88 offset:0
v_mfma_f32_16x16x32_bf16 v[24:27],a[128:131],v[40:43],v[24:27]
s_nop 0
ds_read_b128 v[68:71],v2 offset:23552
v_mul_f32 v33,v33,v73
v_mul_f32 v34,v34,v74
v_mfma_f32_16x16x32_bf16 v[28:31],a[132:135],v[40:43],v[28:31]
s_barrier
v_mfma_f32_16x16x32_bf16 v[24:27],a[136:139],v[44:47],v[24:27]
ds_read_b128 v[40:43],v2 offset:24576
ds_read_b32 v13,v7 offset:0
v_mul_f32 v35,v35,v75
v_mfma_f32_16x16x32_bf16 v[28:31],a[140:143],v[44:47],v[28:31]
v_cvt_pk_bf16_f32 v80,v32,v33
v_cvt_pk_bf16_f32 v81,v34,v35
v_fma_f32 v36,-v96,v20,v36
v_mfma_f32_16x16x32_bf16 v[24:27],a[144:147],v[48:51],v[24:27]
ds_read_b128 v[44:47],v2 offset:25600
v_fma_f32 v37,-v96,v21,v37
v_mfma_f32_16x16x32_bf16 v[28:31],a[148:151],v[48:51],v[28:31]
v_fma_f32 v38,-v96,v22,v38
v_fma_f32 v39,-v96,v23,v39
v_mul_f32 v76,v36,v36
v_mfma_f32_16x16x32_bf16 v[24:27],a[152:155],v[52:55],v[24:27]
s_waitcnt lgkmcnt(1)
ds_read_b128 v[48:51],v2 offset:26624
v_mul_f32 v77,v37,v37
v_mul_f32 v78,v38,v38
v_mfma_f32_16x16x32_bf16 v[28:31],a[156:159],v[52:55],v[28:31]
v_cmp_eq_u32 vcc,s60,v13
v_mul_f32 v79,v39,v39
v_fma_f32 v76,v98,v76,v97
v_mul_f32 v76,v36,v76
v_mfma_f32_16x16x32_bf16 v[24:27],a[160:163],v[56:59],v[24:27]
s_cmp_eq_u32 vcc_lo,-1
ds_read_b128 v[52:55],v2 offset:27648
v_fma_f32 v77,v98,v77,v97
s_cselect_b32 s67,s86,s59
v_mfma_f32_16x16x32_bf16 v[28:31],a[164:167],v[56:59],v[28:31]
buffer_load_dwordx4 v0,s[36:39],s67 offen offset:0 sc1 lds
v_mfma_f32_16x16x32_bf16 v[24:27],a[168:171],v[60:63],v[24:27]
s_xor_b32 s63,s63,65536
ds_read_b128 v[56:59],v2 offset:28672
v_exp_f32 v76,v76
v_mfma_f32_16x16x32_bf16 v[28:31],a[172:175],v[60:63],v[28:31]
s_add_u32 s53,s52,s55
v_mul_f32 v77,v37,v77
v_exp_f32 v77,v77
v_mfma_f32_16x16x32_bf16 v[24:27],a[176:179],v[64:67],v[24:27]
s_cmp_eq_u32 s53,s56
ds_read_b128 v[60:63],v2 offset:29696
v_fma_f32 v76,v76,v99,v99
v_mfma_f32_16x16x32_bf16 v[28:31],a[180:183],v[64:67],v[28:31]
buffer_load_dwordx4 v0,s[36:39],s67 offen offset:1024 sc1 lds
v_fma_f32 v77,v77,v99,v99
v_mfma_f32_16x16x32_bf16 v[24:27],a[184:187],v[68:71],v[24:27]
s_cselect_b32 s53,s57,s53
ds_read_b128 v[64:67],v2 offset:30720
v_rcp_f32 v76,v76
v_mfma_f32_16x16x32_bf16 v[28:31],a[188:191],v[68:71],v[28:31]
s_waitcnt lgkmcnt(3)
v_rcp_f32 v77,v77
v_mfma_f32_16x16x32_bf16 v[24:27],a[192:195],v[40:43],v[24:27]
s_waitcnt vmcnt(7)
ds_read_b128 v[68:71],v2 offset:31744
v_fma_f32 v78,v98,v78,v97
v_mul_f32 v78,v38,v78
v_mfma_f32_16x16x32_bf16 v[28:31],a[196:199],v[40:43],v[28:31]
s_barrier
v_mfma_f32_16x16x32_bf16 v[24:27],a[200:203],v[44:47],v[24:27]
s_cmp_eq_u32 s53,s58
ds_read_b128 v[40:43],v2 offset:32768
ds_read_b64 v[92:93],v11 offset:0
v_fma_f32 v79,v98,v79,v97
v_mfma_f32_16x16x32_bf16 v[28:31],a[204:207],v[44:47],v[28:31]
buffer_load_dwordx4 v0,s[36:39],s67 offen offset:2048 sc1 lds
v_mul_f32 v79,v39,v79
v_mfma_f32_16x16x32_bf16 v[24:27],a[208:211],v[48:51],v[24:27]
s_cselect_b32 s53,s59,s53
ds_read_b128 v[44:47],v2 offset:33792
v_mul_f32 v36,v36,v76
v_mfma_f32_16x16x32_bf16 v[28:31],a[212:215],v[48:51],v[28:31]
s_cmp_ge_u32 s66,s59
v_exp_f32 v78,v78)"
R"(
v_mul_f32 v37,v37,v77
v_mfma_f32_16x16x32_bf16 v[24:27],a[216:219],v[52:55],v[24:27]
s_waitcnt lgkmcnt(1)
ds_read_b128 v[48:51],v2 offset:34816
v_exp_f32 v79,v79
v_mfma_f32_16x16x32_bf16 v[28:31],a[220:223],v[52:55],v[28:31]
buffer_load_dwordx4 v0,s[36:39],s67 offen offset:3072 sc1 lds
v_fma_f32 v78,v78,v99,v99
v_fma_f32 v79,v79,v99,v99
v_rcp_f32 v78,v78
v_rcp_f32 v79,v79
v_mfma_f32_16x16x32_bf16 v[24:27],a[224:227],v[56:59],v[24:27]
v_mul_f32 v38,v38,v78
v_mul_f32 v39,v39,v79
v_cvt_pk_bf16_f32 v82,v36,v37
v_cvt_pk_bf16_f32 v83,v38,v39
v_mfma_f32_16x16x32_bf16 v[28:31],a[228:231],v[56:59],v[28:31]
ds_read_b128 v[52:55],v2 offset:35840
buffer_store_dwordx4 v[80:83],v3,s[36:39],s73 offen offset:0 sc0 sc1
v_mfma_f32_16x16x32_bf16 v[24:27],a[232:235],v[60:63],v[24:27]
ds_read_b128 v[56:59],v2 offset:36864
v_mfma_f32_16x16x32_bf16 v[28:31],a[236:239],v[60:63],v[28:31]
v_mfma_f32_16x16x32_bf16 v[24:27],a[240:243],v[64:67],v[24:27]
ds_read_b128 v[60:63],v2 offset:37888
v_mfma_f32_16x16x32_bf16 v[28:31],a[244:247],v[64:67],v[28:31]
v_mfma_f32_16x16x32_bf16 v[24:27],a[248:251],v[68:71],v[24:27]
s_waitcnt lgkmcnt(2)
ds_read_b128 v[64:67],v2 offset:38912
v_mfma_f32_16x16x32_bf16 v[28:31],a[252:255],v[68:71],v[28:31]
s_cbranch_scc1 Lg3o_slow1_%=
Lg3o_d1w1_%=:
v_mfma_f32_16x16x32_bf16 v[32:35],a[0:3],v[40:43],0
s_add_u32 m0,s62,16384
ds_read_b128 v[68:71],v2 offset:39936
v_mul_f32 v96,s75,v92
v_mfma_f32_16x16x32_bf16 v[36:39],a[4:7],v[40:43],0
s_mul_i32 s73,s48,7
v_fma_f32 v93,v93,s75,v12
v_fma_f32 v93,-v96,v96,v93
v_mfma_f32_16x16x32_bf16 v[32:35],a[8:11],v[44:47],v[32:35]
buffer_load_dwordx4 v1,s[36:39],s67 offen offset:0 sc1 lds
ds_read_b128 v[40:43],v2 offset:40960
v_mfma_f32_16x16x32_bf16 v[36:39],a[12:15],v[44:47],v[36:39]
s_lshr_b32 s73,s73,1
v_max_f32 v93,v93,v12
v_rsq_f32 v92,v93
v_mfma_f32_16x16x32_bf16 v[32:35],a[16:19],v[48:51],v[32:35]
s_lshr_b32 s72,s67,5
ds_read_b128 v[44:47],v2 offset:41984
v_mul_f32 v97,s76,v92
v_mfma_f32_16x16x32_bf16 v[36:39],a[20:23],v[48:51],v[36:39]
s_lshr_b32 s71,s62,2
v_mul_f32 v98,v92,v92
v_mul_f32 v99,v93,v92
v_mul_f32 v98,s77,v98
v_mfma_f32_16x16x32_bf16 v[32:35],a[24:27],v[52:55],v[32:35]
buffer_load_dwordx4 v1,s[36:39],s67 offen offset:1024 sc1 lds
ds_read_b128 v[48:51],v2 offset:43008
v_mfma_f32_16x16x32_bf16 v[36:39],a[28:31],v[52:55],v[36:39]
s_waitcnt lgkmcnt(3)
v_mul_f32 v98,v98,v97
v_fma_f32 v24,-v96,v16,v24
v_mfma_f32_16x16x32_bf16 v[32:35],a[32:35],v[56:59],v[32:35]
s_mov_b64 s[44:45],s[46:47]
ds_read_b128 v[52:55],v2 offset:44032
v_fma_f32 v25,-v96,v17,v25
v_fma_f32 v26,-v96,v18,v26
v_mfma_f32_16x16x32_bf16 v[36:39],a[36:39],v[56:59],v[36:39]
s_mov_b64 s[46:47],s[48:49]
v_fma_f32 v27,-v96,v19,v27
v_mul_f32 v72,v24,v24
v_mfma_f32_16x16x32_bf16 v[32:35],a[40:43],v[60:63],v[32:35]
buffer_load_dwordx4 v1,s[36:39],s67 offen offset:2048 sc1 lds
ds_read_b128 v[56:59],v2 offset:45056
v_mfma_f32_16x16x32_bf16 v[36:39],a[44:47],v[60:63],v[36:39]
s_mov_b64 s[48:49],s[50:51]
v_mul_f32 v73,v25,v25
v_mul_f32 v74,v26,v26
v_mul_f32 v75,v27,v27
v_mfma_f32_16x16x32_bf16 v[32:35],a[48:51],v[64:67],v[32:35]
ds_read_b128 v[60:63],v2 offset:46080
v_fma_f32 v72,v98,v72,v97
v_mfma_f32_16x16x32_bf16 v[36:39],a[52:55],v[64:67],v[36:39]
v_mul_f32 v72,v24,v72
v_exp_f32 v72,v72
v_mfma_f32_16x16x32_bf16 v[32:35],a[56:59],v[68:71],v[32:35]
buffer_load_dwordx4 v1,s[36:39],s67 offen offset:3072 sc1 lds
ds_read_b128 v[64:67],v2 offset:47104
v_mfma_f32_16x16x32_bf16 v[36:39],a[60:63],v[68:71],v[36:39]
s_waitcnt lgkmcnt(3)
v_fma_f32 v72,v72,v99,v99
s_add_u32 m0,s71,s88
v_mfma_f32_16x16x32_bf16 v[32:35],a[64:67],v[40:43],v[32:35]
s_xor_b32 s87,s63,65536
ds_read_b128 v[68:71],v2 offset:48128
buffer_load_dword v9,s[36:39],s72 offen offset:0 sc1 lds
s_waitcnt vmcnt(10)
v_mfma_f32_16x16x32_bf16 v[36:39],a[68:71],v[40:43],v[36:39]
s_barrier
v_mfma_f32_16x16x32_bf16 v[32:35],a[72:75],v[44:47],v[32:35]
s_cmp_ge_u32 s67,s59
ds_read_b128 v[40:43],v2 offset:49152
ds_read_b32 v88,v8 offset:8192
v_fma_f32 v73,v98,v73,v97
v_mfma_f32_16x16x32_bf16 v[36:39],a[76:79],v[44:47],v[36:39]
s_cselect_b32 s86,s50,s51
v_rcp_f32 v72,v72
v_mul_f32 v73,v25,v73
v_mfma_f32_16x16x32_bf16 v[32:35],a[80:83],v[48:51],v[32:35]
s_cselect_b32 s87,s87,s64
ds_read_b128 v[44:47],v2 offset:50176
v_fma_f32 v74,v98,v74,v97
v_mfma_f32_16x16x32_bf16 v[36:39],a[84:87],v[48:51],v[36:39]
s_mov_b32 s62,s87
v_exp_f32 v73,v73
v_mul_f32 v74,v26,v74
v_mfma_f32_16x16x32_bf16 v[32:35],a[88:91],v[52:55],v[32:35]
s_mov_b32 m0,s62
ds_read_b128 v[48:51],v2 offset:51200
v_exp_f32 v74,v74
v_mfma_f32_16x16x32_bf16 v[36:39],a[92:95],v[52:55],v[36:39]
s_waitcnt lgkmcnt(4)
v_fma_f32 v73,v73,v99,v99
v_fma_f32 v74,v74,v99,v99
v_mfma_f32_16x16x32_bf16 v[32:35],a[96:99],v[56:59],v[32:35]
s_waitcnt lgkmcnt(2)
ds_read_b128 v[52:55],v2 offset:52224
v_rcp_f32 v73,v73
v_mfma_f32_16x16x32_bf16 v[36:39],a[100:103],v[56:59],v[36:39]
s_mov_b64 s[50:51],s[52:53]
v_rcp_f32 v74,v74
v_mfma_f32_16x16x32_bf16 v[32:35],a[104:107],v[60:63],v[32:35]
ds_read_b128 v[56:59],v2 offset:53248
v_add_f32_dpp v88,v88,v88 quad_perm:[1,0,3,2] row_mask:0xf bank_mask:0xf
v_fma_f32 v75,v98,v75,v97
v_mfma_f32_16x16x32_bf16 v[36:39],a[108:111],v[60:63],v[36:39]
v_mul_f32 v75,v27,v75
v_exp_f32 v75,v75
v_mfma_f32_16x16x32_bf16 v[32:35],a[112:115],v[64:67],v[32:35]
ds_read_b128 v[60:63],v2 offset:54272
v_fma_f32 v75,v75,v99,v99
v_mfma_f32_16x16x32_bf16 v[36:39],a[116:119],v[64:67],v[36:39]
v_rcp_f32 v75,v75
v_add_f32_dpp v88,v88,v88 quad_perm:[2,3,0,1] row_mask:0xf bank_mask:0xf
v_mfma_f32_16x16x32_bf16 v[32:35],a[120:123],v[68:71],v[32:35]
ds_read_b128 v[64:67],v2 offset:55296
v_add_f32_dpp v88,v88,v88 row_half_mirror row_mask:0xf bank_mask:0xf
v_mul_f32 v24,v24,v72
v_mfma_f32_16x16x32_bf16 v[36:39],a[124:127],v[68:71],v[36:39]
s_waitcnt lgkmcnt(3)
ds_write_b32 v10,v88 offset:128
v_mfma_f32_16x16x32_bf16 v[32:35],a[128:131],v[40:43],v[32:35]
s_nop 0
ds_read_b128 v[68:71],v2 offset:56320
v_mul_f32 v25,v25,v73
v_mul_f32 v26,v26,v74
v_mfma_f32_16x16x32_bf16 v[36:39],a[132:135],v[40:43],v[36:39]
s_barrier
v_mfma_f32_16x16x32_bf16 v[32:35],a[136:139],v[44:47],v[32:35]
ds_read_b128 v[40:43],v2 offset:57344
ds_read_b32 v13,v7 offset:256
v_mul_f32 v27,v27,v75
v_mfma_f32_16x16x32_bf16 v[36:39],a[140:143],v[44:47],v[36:39]
v_cvt_pk_bf16_f32 v80,v24,v25
v_cvt_pk_bf16_f32 v81,v26,v27
v_fma_f32 v28,-v96,v20,v28
v_mfma_f32_16x16x32_bf16 v[32:35],a[144:147],v[48:51],v[32:35]
ds_read_b128 v[44:47],v2 offset:58368
v_fma_f32 v29,-v96,v21,v29
v_mfma_f32_16x16x32_bf16 v[36:39],a[148:151],v[48:51],v[36:39]
v_fma_f32 v30,-v96,v22,v30
v_fma_f32 v31,-v96,v23,v31
v_mul_f32 v76,v28,v28
v_mfma_f32_16x16x32_bf16 v[32:35],a[152:155],v[52:55],v[32:35]
s_waitcnt lgkmcnt(1)
ds_read_b128 v[48:51],v2 offset:59392
v_mul_f32 v77,v29,v29
v_mul_f32 v78,v30,v30
v_mfma_f32_16x16x32_bf16 v[36:39],a[156:159],v[52:55],v[36:39]
v_cmp_eq_u32 vcc,s60,v13
v_mul_f32 v79,v31,v31
v_fma_f32 v76,v98,v76,v97
v_mul_f32 v76,v28,v76
v_mfma_f32_16x16x32_bf16 v[32:35],a[160:163],v[56:59],v[32:35]
s_cmp_eq_u32 vcc_lo,-1
ds_read_b128 v[52:55],v2 offset:60416
v_fma_f32 v77,v98,v77,v97
s_cselect_b32 s66,s86,s59
v_mfma_f32_16x16x32_bf16 v[36:39],a[164:167],v[56:59],v[36:39]
buffer_load_dwordx4 v0,s[36:39],s66 offen offset:0 sc1 lds
v_mfma_f32_16x16x32_bf16 v[32:35],a[168:171],v[60:63],v[32:35]
s_xor_b32 s64,s64,65536
ds_read_b128 v[56:59],v2 offset:61440
v_exp_f32 v76,v76
v_mfma_f32_16x16x32_bf16 v[36:39],a[172:175],v[60:63],v[36:39]
s_add_u32 s74,s53,s55
v_mul_f32 v77,v29,v77
v_exp_f32 v77,v77
v_mfma_f32_16x16x32_bf16 v[32:35],a[176:179],v[64:67],v[32:35]
s_cmp_eq_u32 s74,s56)"
R"(
ds_read_b128 v[60:63],v2 offset:62464
v_fma_f32 v76,v76,v99,v99
v_mfma_f32_16x16x32_bf16 v[36:39],a[180:183],v[64:67],v[36:39]
buffer_load_dwordx4 v0,s[36:39],s66 offen offset:1024 sc1 lds
v_fma_f32 v77,v77,v99,v99
v_mfma_f32_16x16x32_bf16 v[32:35],a[184:187],v[68:71],v[32:35]
s_cselect_b32 s74,s57,s74
ds_read_b128 v[64:67],v2 offset:63488
v_rcp_f32 v76,v76
v_mfma_f32_16x16x32_bf16 v[36:39],a[188:191],v[68:71],v[36:39]
s_waitcnt lgkmcnt(3)
v_rcp_f32 v77,v77
v_mfma_f32_16x16x32_bf16 v[32:35],a[192:195],v[40:43],v[32:35]
s_waitcnt vmcnt(7)
ds_read_b128 v[68:71],v2 offset:64512
v_xor_b32 v2,65536,v2
v_fma_f32 v78,v98,v78,v97
v_mfma_f32_16x16x32_bf16 v[36:39],a[196:199],v[40:43],v[36:39]
s_barrier
v_mfma_f32_16x16x32_bf16 v[32:35],a[200:203],v[44:47],v[32:35]
s_cmp_eq_u32 s74,s58
ds_read_b128 v[40:43],v2 offset:0
ds_read_b64 v[92:93],v11 offset:128
v_mul_f32 v78,v30,v78
v_mfma_f32_16x16x32_bf16 v[36:39],a[204:207],v[44:47],v[36:39]
buffer_load_dwordx4 v0,s[36:39],s66 offen offset:2048 sc1 lds
v_fma_f32 v79,v98,v79,v97
v_mfma_f32_16x16x32_bf16 v[32:35],a[208:211],v[48:51],v[32:35]
s_cselect_b32 s74,s59,s74
ds_read_b128 v[44:47],v2 offset:1024
v_mul_f32 v79,v31,v79
v_mfma_f32_16x16x32_bf16 v[36:39],a[212:215],v[48:51],v[36:39]
s_mov_b32 s52,s74
v_exp_f32 v78,v78
v_mul_f32 v28,v28,v76
v_mfma_f32_16x16x32_bf16 v[32:35],a[216:219],v[52:55],v[32:35]
s_waitcnt lgkmcnt(1)
ds_read_b128 v[48:51],v2 offset:2048
v_exp_f32 v79,v79
v_mfma_f32_16x16x32_bf16 v[36:39],a[220:223],v[52:55],v[36:39]
buffer_load_dwordx4 v0,s[36:39],s66 offen offset:3072 sc1 lds
v_fma_f32 v78,v78,v99,v99
v_fma_f32 v79,v79,v99,v99
v_rcp_f32 v78,v78
v_rcp_f32 v79,v79
v_mfma_f32_16x16x32_bf16 v[32:35],a[224:227],v[56:59],v[32:35]
s_cmp_lt_u32 s67,s59
v_mul_f32 v29,v29,v77
v_mul_f32 v30,v30,v78
v_mul_f32 v31,v31,v79
v_cvt_pk_bf16_f32 v82,v28,v29
v_cvt_pk_bf16_f32 v83,v30,v31
v_mfma_f32_16x16x32_bf16 v[36:39],a[228:231],v[56:59],v[36:39]
ds_read_b128 v[52:55],v2 offset:3072
buffer_store_dwordx4 v[80:83],v3,s[36:39],s73 offen offset:0 sc0 sc1
v_xor_b32 v8,16384,v8
v_mfma_f32_16x16x32_bf16 v[32:35],a[232:235],v[60:63],v[32:35]
ds_read_b128 v[56:59],v2 offset:4096
v_mfma_f32_16x16x32_bf16 v[36:39],a[236:239],v[60:63],v[36:39]
v_mfma_f32_16x16x32_bf16 v[32:35],a[240:243],v[64:67],v[32:35]
ds_read_b128 v[60:63],v2 offset:5120
v_mfma_f32_16x16x32_bf16 v[36:39],a[244:247],v[64:67],v[36:39]
v_mfma_f32_16x16x32_bf16 v[32:35],a[248:251],v[68:71],v[32:35]
s_waitcnt lgkmcnt(2)
ds_read_b128 v[64:67],v2 offset:6144
v_mfma_f32_16x16x32_bf16 v[36:39],a[252:255],v[68:71],v[36:39]
s_cbranch_scc1 Lg3o_d0w1_%=
s_branch Lg3o_slow0_%=
Lg3o_d0w2_%=:
v_mfma_f32_16x16x32_bf16 v[24:27],a[0:3],v[40:43],0
s_add_u32 m0,s62,16384
ds_read_b128 v[68:71],v2 offset:7168
v_mul_f32 v96,s75,v92
v_mfma_f32_16x16x32_bf16 v[28:31],a[4:7],v[40:43],0
s_mul_i32 s73,s47,7
v_fma_f32 v93,v93,s75,v12
v_fma_f32 v93,-v96,v96,v93
v_mfma_f32_16x16x32_bf16 v[24:27],a[8:11],v[44:47],v[24:27]
s_lshr_b32 s73,s73,1
ds_read_b128 v[40:43],v2 offset:8192
v_max_f32 v93,v93,v12
v_mfma_f32_16x16x32_bf16 v[28:31],a[12:15],v[44:47],v[28:31]
buffer_load_dwordx4 v1,s[36:39],s66 offen offset:0 sc1 lds
v_mfma_f32_16x16x32_bf16 v[24:27],a[16:19],v[48:51],v[24:27]
s_lshr_b32 s72,s66,5
ds_read_b128 v[44:47],v2 offset:9216
v_fma_f32 v32,-v96,v16,v32
v_mfma_f32_16x16x32_bf16 v[28:31],a[20:23],v[48:51],v[28:31]
s_lshr_b32 s71,s62,2
v_rsq_f32 v92,v93
v_mfma_f32_16x16x32_bf16 v[24:27],a[24:27],v[52:55],v[24:27]
ds_read_b128 v[48:51],v2 offset:10240
v_mul_f32 v97,s76,v92
v_mfma_f32_16x16x32_bf16 v[28:31],a[28:31],v[52:55],v[28:31]
s_waitcnt lgkmcnt(3)
buffer_load_dwordx4 v1,s[36:39],s66 offen offset:1024 sc1 lds
v_mfma_f32_16x16x32_bf16 v[24:27],a[32:35],v[56:59],v[24:27]
ds_read_b128 v[52:55],v2 offset:11264
v_mul_f32 v98,v92,v92
v_mfma_f32_16x16x32_bf16 v[28:31],a[36:39],v[56:59],v[28:31]
v_mul_f32 v99,v93,v92
v_mul_f32 v98,s77,v98
v_mfma_f32_16x16x32_bf16 v[24:27],a[40:43],v[60:63],v[24:27]
ds_read_b128 v[56:59],v2 offset:12288
v_mul_f32 v98,v98,v97
v_mfma_f32_16x16x32_bf16 v[28:31],a[44:47],v[60:63],v[28:31]
buffer_load_dwordx4 v1,s[36:39],s66 offen offset:2048 sc1 lds
v_mfma_f32_16x16x32_bf16 v[24:27],a[48:51],v[64:67],v[24:27]
ds_read_b128 v[60:63],v2 offset:13312
v_fma_f32 v33,-v96,v17,v33
v_mfma_f32_16x16x32_bf16 v[28:31],a[52:55],v[64:67],v[28:31]
v_fma_f32 v34,-v96,v18,v34
v_fma_f32 v35,-v96,v19,v35
v_mfma_f32_16x16x32_bf16 v[24:27],a[56:59],v[68:71],v[24:27]
ds_read_b128 v[64:67],v2 offset:14336
v_mul_f32 v72,v32,v32
v_mfma_f32_16x16x32_bf16 v[28:31],a[60:63],v[68:71],v[28:31]
s_waitcnt lgkmcnt(3)
buffer_load_dwordx4 v1,s[36:39],s66 offen offset:3072 sc1 lds
s_add_u32 m0,s71,s88
v_mfma_f32_16x16x32_bf16 v[24:27],a[64:67],v[40:43],v[24:27]
s_xor_b32 s87,s64,65536
ds_read_b128 v[68:71],v2 offset:15360
buffer_load_dword v9,s[36:39],s72 offen offset:0 sc1 lds
s_waitcnt vmcnt(10)
v_mfma_f32_16x16x32_bf16 v[28:31],a[68:71],v[40:43],v[28:31]
s_barrier
v_mfma_f32_16x16x32_bf16 v[24:27],a[72:75],v[44:47],v[24:27]
s_cmp_ge_u32 s66,s59
ds_read_b128 v[40:43],v2 offset:16384
ds_read_b32 v88,v8 offset:0
v_mfma_f32_16x16x32_bf16 v[28:31],a[76:79],v[44:47],v[28:31]
s_cselect_b32 s86,s49,s50
v_mul_f32 v73,v33,v33
v_mul_f32 v74,v34,v34
v_mfma_f32_16x16x32_bf16 v[24:27],a[80:83],v[48:51],v[24:27]
s_cselect_b32 s87,s87,s63
ds_read_b128 v[44:47],v2 offset:17408
v_mul_f32 v75,v35,v35
v_mfma_f32_16x16x32_bf16 v[28:31],a[84:87],v[48:51],v[28:31]
s_mov_b32 s62,s87
v_fma_f32 v72,v98,v72,v97
v_mul_f32 v72,v32,v72
v_mfma_f32_16x16x32_bf16 v[24:27],a[88:91],v[52:55],v[24:27]
s_mov_b32 m0,s62
ds_read_b128 v[48:51],v2 offset:18432
v_fma_f32 v73,v98,v73,v97
v_mfma_f32_16x16x32_bf16 v[28:31],a[92:95],v[52:55],v[28:31]
s_waitcnt lgkmcnt(4)
v_exp_f32 v72,v72
v_mfma_f32_16x16x32_bf16 v[24:27],a[96:99],v[56:59],v[24:27]
s_waitcnt lgkmcnt(2)
ds_read_b128 v[52:55],v2 offset:19456
v_mul_f32 v73,v33,v73
v_mfma_f32_16x16x32_bf16 v[28:31],a[100:103],v[56:59],v[28:31]
v_exp_f32 v73,v73
v_mfma_f32_16x16x32_bf16 v[24:27],a[104:107],v[60:63],v[24:27]
ds_read_b128 v[56:59],v2 offset:20480
v_fma_f32 v72,v72,v99,v99
v_mfma_f32_16x16x32_bf16 v[28:31],a[108:111],v[60:63],v[28:31]
v_rcp_f32 v72,v72
v_mfma_f32_16x16x32_bf16 v[24:27],a[112:115],v[64:67],v[24:27]
ds_read_b128 v[60:63],v2 offset:21504
v_fma_f32 v73,v73,v99,v99
v_mfma_f32_16x16x32_bf16 v[28:31],a[116:119],v[64:67],v[28:31]
v_rcp_f32 v73,v73
v_mfma_f32_16x16x32_bf16 v[24:27],a[120:123],v[68:71],v[24:27]
ds_read_b128 v[64:67],v2 offset:22528
v_add_f32_dpp v88,v88,v88 quad_perm:[1,0,3,2] row_mask:0xf bank_mask:0xf
v_mfma_f32_16x16x32_bf16 v[28:31],a[124:127],v[68:71],v[28:31]
s_waitcnt lgkmcnt(3)
v_add_f32_dpp v88,v88,v88 quad_perm:[2,3,0,1] row_mask:0xf bank_mask:0xf
v_fma_f32 v74,v98,v74,v97
v_mfma_f32_16x16x32_bf16 v[24:27],a[128:131],v[40:43],v[24:27]
s_nop 0
ds_read_b128 v[68:71],v2 offset:23552
v_mul_f32 v74,v34,v74
v_mfma_f32_16x16x32_bf16 v[28:31],a[132:135],v[40:43],v[28:31]
s_barrier
v_mfma_f32_16x16x32_bf16 v[24:27],a[136:139],v[44:47],v[24:27]
ds_read_b128 v[40:43],v2 offset:24576
ds_read_b32 v13,v7 offset:0
v_mfma_f32_16x16x32_bf16 v[28:31],a[140:143],v[44:47],v[28:31]
v_exp_f32 v74,v74
v_mfma_f32_16x16x32_bf16 v[24:27],a[144:147],v[48:51],v[24:27]
ds_read_b128 v[44:47],v2 offset:25600
v_fma_f32 v74,v74,v99,v99
v_mfma_f32_16x16x32_bf16 v[28:31],a[148:151],v[48:51],v[28:31]
v_rcp_f32 v74,v74
v_mfma_f32_16x16x32_bf16 v[24:27],a[152:155],v[52:55],v[24:27]
s_waitcnt lgkmcnt(1)
ds_read_b128 v[48:51],v2 offset:26624
v_add_f32_dpp v88,v88,v88 row_half_mirror row_mask:0xf bank_mask:0xf
v_mfma_f32_16x16x32_bf16 v[28:31],a[156:159],v[52:55],v[28:31]
v_cmp_eq_u32 vcc,s60,v13
v_fma_f32 v75,v98,v75,v97
v_mul_f32 v75,v35,v75
v_mul_f32 v32,v32,v72
v_mfma_f32_16x16x32_bf16 v[24:27],a[160:163],v[56:59],v[24:27]
s_cmp_eq_u32 vcc_lo,-1)"
R"(
ds_write_b32 v10,v88 offset:0
v_mfma_f32_16x16x32_bf16 v[28:31],a[164:167],v[56:59],v[28:31]
s_cselect_b32 s67,s86,s59
ds_read_b128 v[52:55],v2 offset:27648
v_mul_f32 v33,v33,v73
v_mfma_f32_16x16x32_bf16 v[24:27],a[168:171],v[60:63],v[24:27]
buffer_load_dwordx4 v0,s[36:39],s67 offen offset:0 sc1 lds
v_mfma_f32_16x16x32_bf16 v[28:31],a[172:175],v[60:63],v[28:31]
s_xor_b32 s63,s63,65536
ds_read_b128 v[56:59],v2 offset:28672
v_mul_f32 v34,v34,v74
v_mfma_f32_16x16x32_bf16 v[24:27],a[176:179],v[64:67],v[24:27]
s_add_u32 s53,s52,s55
ds_read_b128 v[60:63],v2 offset:29696
v_cvt_pk_bf16_f32 v80,v32,v33
v_mfma_f32_16x16x32_bf16 v[28:31],a[180:183],v[64:67],v[28:31]
s_cmp_eq_u32 s53,s56
v_exp_f32 v75,v75
v_mfma_f32_16x16x32_bf16 v[24:27],a[184:187],v[68:71],v[24:27]
buffer_load_dwordx4 v0,s[36:39],s67 offen offset:1024 sc1 lds
v_mfma_f32_16x16x32_bf16 v[28:31],a[188:191],v[68:71],v[28:31]
s_waitcnt lgkmcnt(2)
ds_read_b128 v[64:67],v2 offset:30720
v_fma_f32 v75,v75,v99,v99
v_mfma_f32_16x16x32_bf16 v[24:27],a[192:195],v[40:43],v[24:27]
s_waitcnt vmcnt(7)
ds_read_b128 v[68:71],v2 offset:31744
v_fma_f32 v36,-v96,v20,v36
v_mfma_f32_16x16x32_bf16 v[28:31],a[196:199],v[40:43],v[28:31]
s_barrier
v_mfma_f32_16x16x32_bf16 v[24:27],a[200:203],v[44:47],v[24:27]
s_cselect_b32 s53,s57,s53
ds_read_b128 v[40:43],v2 offset:32768
ds_read_b64 v[92:93],v11 offset:0
v_mfma_f32_16x16x32_bf16 v[28:31],a[204:207],v[44:47],v[28:31]
s_cmp_eq_u32 s53,s58
v_rcp_f32 v75,v75
v_mfma_f32_16x16x32_bf16 v[24:27],a[208:211],v[48:51],v[24:27]
buffer_load_dwordx4 v0,s[36:39],s67 offen offset:2048 sc1 lds
v_mfma_f32_16x16x32_bf16 v[28:31],a[212:215],v[48:51],v[28:31]
s_cselect_b32 s53,s59,s53
ds_read_b128 v[44:47],v2 offset:33792
v_mul_f32 v35,v35,v75
v_mfma_f32_16x16x32_bf16 v[24:27],a[216:219],v[52:55],v[24:27]
s_waitcnt lgkmcnt(1)
ds_read_b128 v[48:51],v2 offset:34816
v_cvt_pk_bf16_f32 v81,v34,v35
v_mfma_f32_16x16x32_bf16 v[28:31],a[220:223],v[52:55],v[28:31]
v_fma_f32 v37,-v96,v21,v37
v_fma_f32 v38,-v96,v22,v38
v_fma_f32 v39,-v96,v23,v39
v_mul_f32 v76,v36,v36
v_mul_f32 v77,v37,v37
v_mul_f32 v78,v38,v38
v_mul_f32 v79,v39,v39
v_fma_f32 v76,v98,v76,v97
v_fma_f32 v77,v98,v77,v97
v_fma_f32 v78,v98,v78,v97
v_fma_f32 v79,v98,v79,v97
v_mul_f32 v76,v36,v76
v_mul_f32 v77,v37,v77
v_mul_f32 v78,v38,v78
v_mul_f32 v79,v39,v79
v_exp_f32 v76,v76
v_exp_f32 v77,v77
v_exp_f32 v78,v78
v_exp_f32 v79,v79
v_mfma_f32_16x16x32_bf16 v[24:27],a[224:227],v[56:59],v[24:27]
buffer_load_dwordx4 v0,s[36:39],s67 offen offset:3072 sc1 lds
v_fma_f32 v76,v76,v99,v99
v_fma_f32 v77,v77,v99,v99
v_fma_f32 v78,v78,v99,v99
v_fma_f32 v79,v79,v99,v99
v_rcp_f32 v76,v76
v_rcp_f32 v77,v77
v_rcp_f32 v78,v78
v_rcp_f32 v79,v79
v_mfma_f32_16x16x32_bf16 v[28:31],a[228:231],v[56:59],v[28:31]
s_cmp_ge_u32 s66,s59
ds_read_b128 v[52:55],v2 offset:35840
v_mul_f32 v36,v36,v76
v_mul_f32 v37,v37,v77
v_mul_f32 v38,v38,v78
v_mul_f32 v39,v39,v79
v_cvt_pk_bf16_f32 v82,v36,v37
v_cvt_pk_bf16_f32 v83,v38,v39
v_mfma_f32_16x16x32_bf16 v[24:27],a[232:235],v[60:63],v[24:27]
buffer_store_dwordx4 v[80:83],v3,s[36:39],s73 offen offset:0 sc0 sc1
ds_read_b128 v[56:59],v2 offset:36864
v_mfma_f32_16x16x32_bf16 v[28:31],a[236:239],v[60:63],v[28:31]
v_mfma_f32_16x16x32_bf16 v[24:27],a[240:243],v[64:67],v[24:27]
ds_read_b128 v[60:63],v2 offset:37888
v_mfma_f32_16x16x32_bf16 v[28:31],a[244:247],v[64:67],v[28:31]
v_mfma_f32_16x16x32_bf16 v[24:27],a[248:251],v[68:71],v[24:27]
s_waitcnt lgkmcnt(2)
ds_read_b128 v[64:67],v2 offset:38912
v_mfma_f32_16x16x32_bf16 v[28:31],a[252:255],v[68:71],v[28:31]
s_cbranch_scc1 Lg3o_slow1_%=
Lg3o_d1w2_%=:
v_mfma_f32_16x16x32_bf16 v[32:35],a[0:3],v[40:43],0
s_add_u32 m0,s62,16384
ds_read_b128 v[68:71],v2 offset:39936
v_mul_f32 v96,s75,v92
v_mfma_f32_16x16x32_bf16 v[36:39],a[4:7],v[40:43],0
s_mul_i32 s73,s48,7
v_fma_f32 v93,v93,s75,v12
v_fma_f32 v93,-v96,v96,v93
v_mfma_f32_16x16x32_bf16 v[32:35],a[8:11],v[44:47],v[32:35]
s_lshr_b32 s73,s73,1
ds_read_b128 v[40:43],v2 offset:40960
v_max_f32 v93,v93,v12
v_mfma_f32_16x16x32_bf16 v[36:39],a[12:15],v[44:47],v[36:39]
buffer_load_dwordx4 v1,s[36:39],s67 offen offset:0 sc1 lds
v_mfma_f32_16x16x32_bf16 v[32:35],a[16:19],v[48:51],v[32:35]
s_lshr_b32 s72,s67,5
ds_read_b128 v[44:47],v2 offset:41984
v_fma_f32 v24,-v96,v16,v24
v_mfma_f32_16x16x32_bf16 v[36:39],a[20:23],v[48:51],v[36:39]
s_lshr_b32 s71,s62,2
v_rsq_f32 v92,v93
v_mfma_f32_16x16x32_bf16 v[32:35],a[24:27],v[52:55],v[32:35]
s_mov_b64 s[44:45],s[46:47]
ds_read_b128 v[48:51],v2 offset:43008
v_mul_f32 v97,s76,v92
v_mfma_f32_16x16x32_bf16 v[36:39],a[28:31],v[52:55],v[36:39]
s_waitcnt lgkmcnt(3)
buffer_load_dwordx4 v1,s[36:39],s67 offen offset:1024 sc1 lds
v_mfma_f32_16x16x32_bf16 v[32:35],a[32:35],v[56:59],v[32:35]
s_mov_b64 s[46:47],s[48:49]
ds_read_b128 v[52:55],v2 offset:44032
v_mul_f32 v98,v92,v92
v_mfma_f32_16x16x32_bf16 v[36:39],a[36:39],v[56:59],v[36:39]
s_mov_b64 s[48:49],s[50:51]
v_mul_f32 v99,v93,v92
v_mul_f32 v98,s77,v98
v_mfma_f32_16x16x32_bf16 v[32:35],a[40:43],v[60:63],v[32:35]
ds_read_b128 v[56:59],v2 offset:45056
v_mul_f32 v98,v98,v97
v_mfma_f32_16x16x32_bf16 v[36:39],a[44:47],v[60:63],v[36:39]
buffer_load_dwordx4 v1,s[36:39],s67 offen offset:2048 sc1 lds
v_mfma_f32_16x16x32_bf16 v[32:35],a[48:51],v[64:67],v[32:35]
ds_read_b128 v[60:63],v2 offset:46080
v_fma_f32 v25,-v96,v17,v25
v_mfma_f32_16x16x32_bf16 v[36:39],a[52:55],v[64:67],v[36:39]
v_fma_f32 v26,-v96,v18,v26
v_fma_f32 v27,-v96,v19,v27
v_mfma_f32_16x16x32_bf16 v[32:35],a[56:59],v[68:71],v[32:35]
ds_read_b128 v[64:67],v2 offset:47104
v_mul_f32 v72,v24,v24
v_mfma_f32_16x16x32_bf16 v[36:39],a[60:63],v[68:71],v[36:39]
s_waitcnt lgkmcnt(3)
buffer_load_dwordx4 v1,s[36:39],s67 offen offset:3072 sc1 lds
s_add_u32 m0,s71,s88
v_mfma_f32_16x16x32_bf16 v[32:35],a[64:67],v[40:43],v[32:35]
s_xor_b32 s87,s63,65536
ds_read_b128 v[68:71],v2 offset:48128
buffer_load_dword v9,s[36:39],s72 offen offset:0 sc1 lds
s_waitcnt vmcnt(10)
v_mfma_f32_16x16x32_bf16 v[36:39],a[68:71],v[40:43],v[36:39]
s_barrier
v_mfma_f32_16x16x32_bf16 v[32:35],a[72:75],v[44:47],v[32:35]
s_cmp_ge_u32 s67,s59
ds_read_b128 v[40:43],v2 offset:49152
ds_read_b32 v88,v8 offset:8192
v_mfma_f32_16x16x32_bf16 v[36:39],a[76:79],v[44:47],v[36:39]
s_cselect_b32 s86,s50,s51
v_mul_f32 v73,v25,v25
v_mul_f32 v74,v26,v26
v_mfma_f32_16x16x32_bf16 v[32:35],a[80:83],v[48:51],v[32:35]
s_cselect_b32 s87,s87,s64
ds_read_b128 v[44:47],v2 offset:50176
v_mul_f32 v75,v27,v27
v_mfma_f32_16x16x32_bf16 v[36:39],a[84:87],v[48:51],v[36:39]
s_mov_b32 s62,s87
v_fma_f32 v72,v98,v72,v97
v_mul_f32 v72,v24,v72
v_mfma_f32_16x16x32_bf16 v[32:35],a[88:91],v[52:55],v[32:35]
s_mov_b32 m0,s62
ds_read_b128 v[48:51],v2 offset:51200
v_fma_f32 v73,v98,v73,v97
v_mfma_f32_16x16x32_bf16 v[36:39],a[92:95],v[52:55],v[36:39]
s_waitcnt lgkmcnt(4)
v_exp_f32 v72,v72
v_mfma_f32_16x16x32_bf16 v[32:35],a[96:99],v[56:59],v[32:35]
s_waitcnt lgkmcnt(2)
ds_read_b128 v[52:55],v2 offset:52224
v_mul_f32 v73,v25,v73
v_mfma_f32_16x16x32_bf16 v[36:39],a[100:103],v[56:59],v[36:39]
s_mov_b64 s[50:51],s[52:53]
v_exp_f32 v73,v73
v_mfma_f32_16x16x32_bf16 v[32:35],a[104:107],v[60:63],v[32:35]
ds_read_b128 v[56:59],v2 offset:53248
v_fma_f32 v72,v72,v99,v99
v_mfma_f32_16x16x32_bf16 v[36:39],a[108:111],v[60:63],v[36:39]
v_rcp_f32 v72,v72
v_mfma_f32_16x16x32_bf16 v[32:35],a[112:115],v[64:67],v[32:35]
ds_read_b128 v[60:63],v2 offset:54272
v_fma_f32 v73,v73,v99,v99
v_mfma_f32_16x16x32_bf16 v[36:39],a[116:119],v[64:67],v[36:39]
v_rcp_f32 v73,v73)"
R"(
v_mfma_f32_16x16x32_bf16 v[32:35],a[120:123],v[68:71],v[32:35]
ds_read_b128 v[64:67],v2 offset:55296
v_add_f32_dpp v88,v88,v88 quad_perm:[1,0,3,2] row_mask:0xf bank_mask:0xf
v_mfma_f32_16x16x32_bf16 v[36:39],a[124:127],v[68:71],v[36:39]
s_waitcnt lgkmcnt(3)
v_add_f32_dpp v88,v88,v88 quad_perm:[2,3,0,1] row_mask:0xf bank_mask:0xf
v_fma_f32 v74,v98,v74,v97
v_mfma_f32_16x16x32_bf16 v[32:35],a[128:131],v[40:43],v[32:35]
s_nop 0
ds_read_b128 v[68:71],v2 offset:56320
v_mul_f32 v74,v26,v74
v_mfma_f32_16x16x32_bf16 v[36:39],a[132:135],v[40:43],v[36:39]
s_barrier
v_mfma_f32_16x16x32_bf16 v[32:35],a[136:139],v[44:47],v[32:35]
ds_read_b128 v[40:43],v2 offset:57344
ds_read_b32 v13,v7 offset:256
v_mfma_f32_16x16x32_bf16 v[36:39],a[140:143],v[44:47],v[36:39]
v_exp_f32 v74,v74
v_mfma_f32_16x16x32_bf16 v[32:35],a[144:147],v[48:51],v[32:35]
ds_read_b128 v[44:47],v2 offset:58368
v_fma_f32 v74,v74,v99,v99
v_mfma_f32_16x16x32_bf16 v[36:39],a[148:151],v[48:51],v[36:39]
v_rcp_f32 v74,v74
v_mfma_f32_16x16x32_bf16 v[32:35],a[152:155],v[52:55],v[32:35]
s_waitcnt lgkmcnt(1)
ds_read_b128 v[48:51],v2 offset:59392
v_add_f32_dpp v88,v88,v88 row_half_mirror row_mask:0xf bank_mask:0xf
v_mfma_f32_16x16x32_bf16 v[36:39],a[156:159],v[52:55],v[36:39]
v_cmp_eq_u32 vcc,s60,v13
v_fma_f32 v75,v98,v75,v97
v_mul_f32 v75,v27,v75
v_mul_f32 v24,v24,v72
v_mfma_f32_16x16x32_bf16 v[32:35],a[160:163],v[56:59],v[32:35]
s_cmp_eq_u32 vcc_lo,-1
ds_write_b32 v10,v88 offset:128
v_mfma_f32_16x16x32_bf16 v[36:39],a[164:167],v[56:59],v[36:39]
s_cselect_b32 s66,s86,s59
ds_read_b128 v[52:55],v2 offset:60416
v_mul_f32 v25,v25,v73
v_mfma_f32_16x16x32_bf16 v[32:35],a[168:171],v[60:63],v[32:35]
buffer_load_dwordx4 v0,s[36:39],s66 offen offset:0 sc1 lds
v_mfma_f32_16x16x32_bf16 v[36:39],a[172:175],v[60:63],v[36:39]
s_xor_b32 s64,s64,65536
ds_read_b128 v[56:59],v2 offset:61440
v_mul_f32 v26,v26,v74
v_mfma_f32_16x16x32_bf16 v[32:35],a[176:179],v[64:67],v[32:35]
s_add_u32 s74,s53,s55
ds_read_b128 v[60:63],v2 offset:62464
v_cvt_pk_bf16_f32 v80,v24,v25
v_mfma_f32_16x16x32_bf16 v[36:39],a[180:183],v[64:67],v[36:39]
s_cmp_eq_u32 s74,s56
v_exp_f32 v75,v75
v_mfma_f32_16x16x32_bf16 v[32:35],a[184:187],v[68:71],v[32:35]
buffer_load_dwordx4 v0,s[36:39],s66 offen offset:1024 sc1 lds
v_mfma_f32_16x16x32_bf16 v[36:39],a[188:191],v[68:71],v[36:39]
s_waitcnt lgkmcnt(2)
ds_read_b128 v[64:67],v2 offset:63488
v_fma_f32 v75,v75,v99,v99
v_mfma_f32_16x16x32_bf16 v[32:35],a[192:195],v[40:43],v[32:35]
s_waitcnt vmcnt(7)
ds_read_b128 v[68:71],v2 offset:64512
v_xor_b32 v2,65536,v2
v_mfma_f32_16x16x32_bf16 v[36:39],a[196:199],v[40:43],v[36:39]
s_barrier
v_mfma_f32_16x16x32_bf16 v[32:35],a[200:203],v[44:47],v[32:35]
s_cselect_b32 s74,s57,s74
ds_read_b128 v[40:43],v2 offset:0
ds_read_b64 v[92:93],v11 offset:128
v_mfma_f32_16x16x32_bf16 v[36:39],a[204:207],v[44:47],v[36:39]
s_cmp_eq_u32 s74,s58
v_rcp_f32 v75,v75
v_mfma_f32_16x16x32_bf16 v[32:35],a[208:211],v[48:51],v[32:35]
buffer_load_dwordx4 v0,s[36:39],s66 offen offset:2048 sc1 lds
v_mfma_f32_16x16x32_bf16 v[36:39],a[212:215],v[48:51],v[36:39]
s_cselect_b32 s74,s59,s74
ds_read_b128 v[44:47],v2 offset:1024
v_mul_f32 v27,v27,v75
v_mfma_f32_16x16x32_bf16 v[32:35],a[216:219],v[52:55],v[32:35]
s_waitcnt lgkmcnt(1)
ds_read_b128 v[48:51],v2 offset:2048
v_cvt_pk_bf16_f32 v81,v26,v27
v_mfma_f32_16x16x32_bf16 v[36:39],a[220:223],v[52:55],v[36:39]
v_fma_f32 v28,-v96,v20,v28
v_fma_f32 v29,-v96,v21,v29
v_fma_f32 v30,-v96,v22,v30
v_fma_f32 v31,-v96,v23,v31
v_mul_f32 v76,v28,v28
v_mul_f32 v77,v29,v29
v_mul_f32 v78,v30,v30
v_mul_f32 v79,v31,v31
v_fma_f32 v76,v98,v76,v97
v_fma_f32 v77,v98,v77,v97
v_fma_f32 v78,v98,v78,v97
v_fma_f32 v79,v98,v79,v97
v_mul_f32 v76,v28,v76
v_mul_f32 v77,v29,v77
v_mul_f32 v78,v30,v78
v_mul_f32 v79,v31,v79
v_exp_f32 v76,v76
v_exp_f32 v77,v77
v_exp_f32 v78,v78
v_exp_f32 v79,v79
v_mfma_f32_16x16x32_bf16 v[32:35],a[224:227],v[56:59],v[32:35]
buffer_load_dwordx4 v0,s[36:39],s66 offen offset:3072 sc1 lds
v_fma_f32 v76,v76,v99,v99
v_fma_f32 v77,v77,v99,v99
v_fma_f32 v78,v78,v99,v99
v_fma_f32 v79,v79,v99,v99
v_rcp_f32 v76,v76
v_rcp_f32 v77,v77
v_rcp_f32 v78,v78
v_rcp_f32 v79,v79
v_mfma_f32_16x16x32_bf16 v[36:39],a[228:231],v[56:59],v[36:39]
s_mov_b32 s52,s74
ds_read_b128 v[52:55],v2 offset:3072
v_mul_f32 v28,v28,v76
v_mul_f32 v29,v29,v77
v_mul_f32 v30,v30,v78
v_mul_f32 v31,v31,v79
v_cvt_pk_bf16_f32 v82,v28,v29
v_cvt_pk_bf16_f32 v83,v30,v31
v_mfma_f32_16x16x32_bf16 v[32:35],a[232:235],v[60:63],v[32:35]
buffer_store_dwordx4 v[80:83],v3,s[36:39],s73 offen offset:0 sc0 sc1
ds_read_b128 v[56:59],v2 offset:4096
v_xor_b32 v8,16384,v8
v_mfma_f32_16x16x32_bf16 v[36:39],a[236:239],v[60:63],v[36:39]
s_cmp_lt_u32 s67,s59
v_mfma_f32_16x16x32_bf16 v[32:35],a[240:243],v[64:67],v[32:35]
ds_read_b128 v[60:63],v2 offset:5120
v_mfma_f32_16x16x32_bf16 v[36:39],a[244:247],v[64:67],v[36:39]
v_mfma_f32_16x16x32_bf16 v[32:35],a[248:251],v[68:71],v[32:35]
s_waitcnt lgkmcnt(2)
ds_read_b128 v[64:67],v2 offset:6144
v_mfma_f32_16x16x32_bf16 v[36:39],a[252:255],v[68:71],v[36:39]
s_cbranch_scc1 Lg3o_d0w2_%=
s_branch Lg3o_slow0_%=
Lg3o_d0w3_%=:
v_mfma_f32_16x16x32_bf16 v[24:27],a[0:3],v[40:43],0
s_add_u32 m0,s62,16384
ds_read_b128 v[68:71],v2 offset:7168
v_mul_f32 v96,s75,v92
v_mfma_f32_16x16x32_bf16 v[28:31],a[4:7],v[40:43],0
s_mul_i32 s73,s47,7
v_fma_f32 v93,v93,s75,v12
v_fma_f32 v93,-v96,v96,v93
v_mfma_f32_16x16x32_bf16 v[24:27],a[8:11],v[44:47],v[24:27]
s_lshr_b32 s73,s73,1
ds_read_b128 v[40:43],v2 offset:8192
v_max_f32 v93,v93,v12
v_mfma_f32_16x16x32_bf16 v[28:31],a[12:15],v[44:47],v[28:31]
s_lshr_b32 s72,s66,5
v_rsq_f32 v92,v93
v_mfma_f32_16x16x32_bf16 v[24:27],a[16:19],v[48:51],v[24:27]
buffer_load_dwordx4 v1,s[36:39],s66 offen offset:0 sc1 lds
v_mfma_f32_16x16x32_bf16 v[28:31],a[20:23],v[48:51],v[28:31]
s_lshr_b32 s71,s62,2
ds_read_b128 v[44:47],v2 offset:9216
v_mul_f32 v97,s76,v92
v_mfma_f32_16x16x32_bf16 v[24:27],a[24:27],v[52:55],v[24:27]
s_mul_i32 s70,s46,5
ds_read_b128 v[48:51],v2 offset:10240
v_mul_f32 v98,v92,v92
v_mfma_f32_16x16x32_bf16 v[28:31],a[28:31],v[52:55],v[28:31]
s_waitcnt lgkmcnt(3)
v_mul_f32 v99,v93,v92
v_mul_f32 v98,s77,v98
v_mfma_f32_16x16x32_bf16 v[24:27],a[32:35],v[56:59],v[24:27]
buffer_load_dwordx4 v1,s[36:39],s66 offen offset:1024 sc1 lds
v_mfma_f32_16x16x32_bf16 v[28:31],a[36:39],v[56:59],v[28:31]
ds_read_b128 v[52:55],v2 offset:11264
v_mul_f32 v98,v98,v97
v_mfma_f32_16x16x32_bf16 v[24:27],a[40:43],v[60:63],v[24:27]
ds_read_b128 v[56:59],v2 offset:12288
v_fma_f32 v32,-v96,v16,v32
v_mfma_f32_16x16x32_bf16 v[28:31],a[44:47],v[60:63],v[28:31]
v_fma_f32 v33,-v96,v17,v33
v_fma_f32 v34,-v96,v18,v34
v_mfma_f32_16x16x32_bf16 v[24:27],a[48:51],v[64:67],v[24:27]
buffer_load_dwordx4 v1,s[36:39],s66 offen offset:2048 sc1 lds
v_mfma_f32_16x16x32_bf16 v[28:31],a[52:55],v[64:67],v[28:31]
ds_read_b128 v[60:63],v2 offset:13312
v_fma_f32 v35,-v96,v19,v35
v_mfma_f32_16x16x32_bf16 v[24:27],a[56:59],v[68:71],v[24:27]
ds_read_b128 v[64:67],v2 offset:14336
v_mul_f32 v72,v32,v32
v_mfma_f32_16x16x32_bf16 v[28:31],a[60:63],v[68:71],v[28:31]
s_waitcnt lgkmcnt(3)
v_mul_f32 v73,v33,v33
v_mul_f32 v74,v34,v34
v_mfma_f32_16x16x32_bf16 v[24:27],a[64:67],v[40:43],v[24:27]
s_waitcnt vmcnt(9)
ds_read_b128 v[68:71],v2 offset:15360
v_mul_f32 v75,v35,v35
v_mfma_f32_16x16x32_bf16 v[28:31],a[68:71],v[40:43],v[28:31]
s_barrier
v_mfma_f32_16x16x32_bf16 v[24:27],a[72:75],v[44:47],v[24:27]
buffer_load_dwordx4 v1,s[36:39],s66 offen offset:3072 sc1 lds
v_mfma_f32_16x16x32_bf16 v[28:31],a[76:79],v[44:47],v[28:31]
s_add_u32 m0,s71,s88
ds_read_b128 v[40:43],v2 offset:16384)"
R"(
ds_read_b32 v88,v8 offset:0
v_mfma_f32_16x16x32_bf16 v[24:27],a[80:83],v[48:51],v[24:27]
buffer_load_dword v9,s[36:39],s72 offen offset:0 sc1 lds
v_mfma_f32_16x16x32_bf16 v[28:31],a[84:87],v[48:51],v[28:31]
s_xor_b32 s87,s64,65536
ds_read_b128 v[44:47],v2 offset:17408
v_fma_f32 v72,v98,v72,v97
v_mfma_f32_16x16x32_bf16 v[24:27],a[88:91],v[52:55],v[24:27]
s_cmp_ge_u32 s66,s59
ds_read_b128 v[48:51],v2 offset:18432
v_mul_f32 v72,v32,v72
v_mfma_f32_16x16x32_bf16 v[28:31],a[92:95],v[52:55],v[28:31]
s_waitcnt lgkmcnt(4)
v_exp_f32 v72,v72
v_mfma_f32_16x16x32_bf16 v[24:27],a[96:99],v[56:59],v[24:27]
s_cselect_b32 s86,s49,s50
ds_read_b128 v[52:55],v2 offset:19456
v_fma_f32 v72,v72,v99,v99
v_mfma_f32_16x16x32_bf16 v[28:31],a[100:103],v[56:59],v[28:31]
s_waitcnt lgkmcnt(3)
v_rcp_f32 v72,v72
v_mfma_f32_16x16x32_bf16 v[24:27],a[104:107],v[60:63],v[24:27]
s_cselect_b32 s87,s87,s63
ds_read_b128 v[56:59],v2 offset:20480
v_add_f32_dpp v88,v88,v88 quad_perm:[1,0,3,2] row_mask:0xf bank_mask:0xf
v_mfma_f32_16x16x32_bf16 v[28:31],a[108:111],v[60:63],v[28:31]
s_mov_b32 s62,s87
v_add_f32_dpp v88,v88,v88 quad_perm:[2,3,0,1] row_mask:0xf bank_mask:0xf
v_fma_f32 v73,v98,v73,v97
v_mfma_f32_16x16x32_bf16 v[24:27],a[112:115],v[64:67],v[24:27]
s_mov_b32 m0,s62
ds_read_b128 v[60:63],v2 offset:21504
v_mul_f32 v73,v33,v73
v_mfma_f32_16x16x32_bf16 v[28:31],a[116:119],v[64:67],v[28:31]
v_exp_f32 v73,v73
v_mfma_f32_16x16x32_bf16 v[24:27],a[120:123],v[68:71],v[24:27]
ds_read_b128 v[64:67],v2 offset:22528
v_fma_f32 v73,v73,v99,v99
v_mfma_f32_16x16x32_bf16 v[28:31],a[124:127],v[68:71],v[28:31]
s_waitcnt lgkmcnt(3)
v_rcp_f32 v73,v73
v_mfma_f32_16x16x32_bf16 v[24:27],a[128:131],v[40:43],v[24:27]
s_nop 0
ds_read_b128 v[68:71],v2 offset:23552
v_add_f32_dpp v88,v88,v88 row_half_mirror row_mask:0xf bank_mask:0xf
v_mfma_f32_16x16x32_bf16 v[28:31],a[132:135],v[40:43],v[28:31]
s_barrier
v_mfma_f32_16x16x32_bf16 v[24:27],a[136:139],v[44:47],v[24:27]
ds_write_b32 v10,v88 offset:0
v_mfma_f32_16x16x32_bf16 v[28:31],a[140:143],v[44:47],v[28:31]
ds_read_b128 v[40:43],v2 offset:24576
ds_read_b32 v13,v7 offset:0
v_mfma_f32_16x16x32_bf16 v[24:27],a[144:147],v[48:51],v[24:27]
ds_read_b128 v[44:47],v2 offset:25600
v_fma_f32 v74,v98,v74,v97
v_mfma_f32_16x16x32_bf16 v[28:31],a[148:151],v[48:51],v[28:31]
v_mul_f32 v74,v34,v74
v_fma_f32 v75,v98,v75,v97
v_mfma_f32_16x16x32_bf16 v[24:27],a[152:155],v[52:55],v[24:27]
ds_read_b128 v[48:51],v2 offset:26624
v_mul_f32 v75,v35,v75
v_mfma_f32_16x16x32_bf16 v[28:31],a[156:159],v[52:55],v[28:31]
s_waitcnt lgkmcnt(5)
v_exp_f32 v74,v74
v_mfma_f32_16x16x32_bf16 v[24:27],a[160:163],v[56:59],v[24:27]
s_waitcnt lgkmcnt(2)
ds_read_b128 v[52:55],v2 offset:27648
v_fma_f32 v74,v74,v99,v99
v_mfma_f32_16x16x32_bf16 v[28:31],a[164:167],v[56:59],v[28:31]
v_cmp_eq_u32 vcc,s60,v13
v_exp_f32 v75,v75
v_mul_f32 v32,v32,v72
v_mfma_f32_16x16x32_bf16 v[24:27],a[168:171],v[60:63],v[24:27]
s_cmp_eq_u32 vcc_lo,-1
ds_read_b128 v[56:59],v2 offset:28672
s_cselect_b32 s67,s86,s59
v_mfma_f32_16x16x32_bf16 v[28:31],a[172:175],v[60:63],v[28:31]
buffer_load_dwordx4 v0,s[36:39],s67 offen offset:0 sc1 lds
v_mfma_f32_16x16x32_bf16 v[24:27],a[176:179],v[64:67],v[24:27]
s_lshr_b32 s70,s70,6
ds_read_b128 v[60:63],v2 offset:29696
v_fma_f32 v75,v75,v99,v99
v_mfma_f32_16x16x32_bf16 v[28:31],a[180:183],v[64:67],v[28:31]
s_xor_b32 s63,s63,65536
v_rcp_f32 v74,v74
v_mfma_f32_16x16x32_bf16 v[24:27],a[184:187],v[68:71],v[24:27]
s_add_u32 s53,s52,s55
ds_read_b128 v[64:67],v2 offset:30720
v_mul_f32 v33,v33,v73
v_mfma_f32_16x16x32_bf16 v[28:31],a[188:191],v[68:71],v[28:31]
s_waitcnt lgkmcnt(3)
buffer_load_dwordx4 v0,s[36:39],s67 offen offset:1024 sc1 lds
v_mfma_f32_16x16x32_bf16 v[24:27],a[192:195],v[40:43],v[24:27]
s_waitcnt vmcnt(7)
ds_read_b128 v[68:71],v2 offset:31744
v_mul_f32 v34,v34,v74
v_mfma_f32_16x16x32_bf16 v[28:31],a[196:199],v[40:43],v[28:31]
s_barrier
v_mfma_f32_16x16x32_bf16 v[24:27],a[200:203],v[44:47],v[24:27]
buffer_store_dword v5,v4,s[36:39],s70 offen offset:0 sc0 sc1
ds_read_b128 v[40:43],v2 offset:32768
ds_read_b64 v[92:93],v11 offset:0
v_cvt_pk_bf16_f32 v80,v32,v33
v_mfma_f32_16x16x32_bf16 v[28:31],a[204:207],v[44:47],v[28:31]
s_cmp_eq_u32 s53,s56
v_rcp_f32 v75,v75
v_mfma_f32_16x16x32_bf16 v[24:27],a[208:211],v[48:51],v[24:27]
s_cselect_b32 s53,s57,s53
ds_read_b128 v[44:47],v2 offset:33792
v_mul_f32 v35,v35,v75
v_mfma_f32_16x16x32_bf16 v[28:31],a[212:215],v[48:51],v[28:31]
buffer_load_dwordx4 v0,s[36:39],s67 offen offset:2048 sc1 lds
v_mfma_f32_16x16x32_bf16 v[24:27],a[216:219],v[52:55],v[24:27]
s_cmp_eq_u32 s53,s58
ds_read_b128 v[48:51],v2 offset:34816
v_cvt_pk_bf16_f32 v81,v34,v35
v_mfma_f32_16x16x32_bf16 v[28:31],a[220:223],v[52:55],v[28:31]
s_waitcnt lgkmcnt(4)
v_fma_f32 v36,-v96,v20,v36
v_fma_f32 v37,-v96,v21,v37
v_mfma_f32_16x16x32_bf16 v[24:27],a[224:227],v[56:59],v[24:27]
s_cselect_b32 s53,s59,s53
v_fma_f32 v38,-v96,v22,v38
v_fma_f32 v39,-v96,v23,v39
v_mul_f32 v76,v36,v36
v_mul_f32 v77,v37,v37
v_mul_f32 v78,v38,v38
v_mul_f32 v79,v39,v39
v_fma_f32 v76,v98,v76,v97
v_fma_f32 v77,v98,v77,v97
v_fma_f32 v78,v98,v78,v97
v_fma_f32 v79,v98,v79,v97
v_mul_f32 v76,v36,v76
v_mul_f32 v77,v37,v77
v_mul_f32 v78,v38,v78
v_mul_f32 v79,v39,v79
v_exp_f32 v76,v76
v_exp_f32 v77,v77
v_exp_f32 v78,v78
v_exp_f32 v79,v79
v_mfma_f32_16x16x32_bf16 v[28:31],a[228:231],v[56:59],v[28:31]
s_waitcnt lgkmcnt(2)
ds_read_b128 v[52:55],v2 offset:35840
buffer_load_dwordx4 v0,s[36:39],s67 offen offset:3072 sc1 lds
v_fma_f32 v76,v76,v99,v99
v_fma_f32 v77,v77,v99,v99
v_fma_f32 v78,v78,v99,v99
v_fma_f32 v79,v79,v99,v99
v_rcp_f32 v76,v76
v_rcp_f32 v77,v77
v_rcp_f32 v78,v78
v_rcp_f32 v79,v79
v_mfma_f32_16x16x32_bf16 v[24:27],a[232:235],v[60:63],v[24:27]
s_cmp_ge_u32 s66,s59
v_mul_f32 v36,v36,v76
v_mul_f32 v37,v37,v77
v_mul_f32 v38,v38,v78
v_mul_f32 v39,v39,v79
v_cvt_pk_bf16_f32 v82,v36,v37
v_cvt_pk_bf16_f32 v83,v38,v39
v_mfma_f32_16x16x32_bf16 v[28:31],a[236:239],v[60:63],v[28:31]
ds_read_b128 v[56:59],v2 offset:36864
buffer_store_dwordx4 v[80:83],v3,s[36:39],s73 offen offset:0 sc0 sc1
v_mfma_f32_16x16x32_bf16 v[24:27],a[240:243],v[64:67],v[24:27]
ds_read_b128 v[60:63],v2 offset:37888
v_mfma_f32_16x16x32_bf16 v[28:31],a[244:247],v[64:67],v[28:31]
v_mfma_f32_16x16x32_bf16 v[24:27],a[248:251],v[68:71],v[24:27]
s_waitcnt lgkmcnt(2)
ds_read_b128 v[64:67],v2 offset:38912
v_mfma_f32_16x16x32_bf16 v[28:31],a[252:255],v[68:71],v[28:31]
s_cbranch_scc1 Lg3o_slow1_%=
Lg3o_d1w3_%=:
v_mfma_f32_16x16x32_bf16 v[32:35],a[0:3],v[40:43],0
s_add_u32 m0,s62,16384
ds_read_b128 v[68:71],v2 offset:39936
v_mul_f32 v96,s75,v92
v_mfma_f32_16x16x32_bf16 v[36:39],a[4:7],v[40:43],0
s_mul_i32 s73,s48,7
v_fma_f32 v93,v93,s75,v12
v_fma_f32 v93,-v96,v96,v93
v_mfma_f32_16x16x32_bf16 v[32:35],a[8:11],v[44:47],v[32:35]
s_lshr_b32 s73,s73,1
ds_read_b128 v[40:43],v2 offset:40960
v_max_f32 v93,v93,v12
v_mfma_f32_16x16x32_bf16 v[36:39],a[12:15],v[44:47],v[36:39]
s_lshr_b32 s72,s67,5
v_rsq_f32 v92,v93
v_mfma_f32_16x16x32_bf16 v[32:35],a[16:19],v[48:51],v[32:35]
buffer_load_dwordx4 v1,s[36:39],s67 offen offset:0 sc1 lds
v_mfma_f32_16x16x32_bf16 v[36:39],a[20:23],v[48:51],v[36:39]
s_lshr_b32 s71,s62,2
ds_read_b128 v[44:47],v2 offset:41984
v_mul_f32 v97,s76,v92
v_mfma_f32_16x16x32_bf16 v[32:35],a[24:27],v[52:55],v[32:35]
s_mul_i32 s70,s47,5
ds_read_b128 v[48:51],v2 offset:43008
v_mul_f32 v98,v92,v92
v_mfma_f32_16x16x32_bf16 v[36:39],a[28:31],v[52:55],v[36:39]
s_waitcnt lgkmcnt(3)
v_mul_f32 v99,v93,v92)"
R"(
v_mul_f32 v98,s77,v98
v_mfma_f32_16x16x32_bf16 v[32:35],a[32:35],v[56:59],v[32:35]
buffer_load_dwordx4 v1,s[36:39],s67 offen offset:1024 sc1 lds
v_mfma_f32_16x16x32_bf16 v[36:39],a[36:39],v[56:59],v[36:39]
s_mov_b64 s[44:45],s[46:47]
ds_read_b128 v[52:55],v2 offset:44032
v_mul_f32 v98,v98,v97
v_mfma_f32_16x16x32_bf16 v[32:35],a[40:43],v[60:63],v[32:35]
s_mov_b64 s[46:47],s[48:49]
ds_read_b128 v[56:59],v2 offset:45056
v_fma_f32 v24,-v96,v16,v24
v_mfma_f32_16x16x32_bf16 v[36:39],a[44:47],v[60:63],v[36:39]
s_mov_b64 s[48:49],s[50:51]
v_fma_f32 v25,-v96,v17,v25
v_fma_f32 v26,-v96,v18,v26
v_mfma_f32_16x16x32_bf16 v[32:35],a[48:51],v[64:67],v[32:35]
buffer_load_dwordx4 v1,s[36:39],s67 offen offset:2048 sc1 lds
v_mfma_f32_16x16x32_bf16 v[36:39],a[52:55],v[64:67],v[36:39]
ds_read_b128 v[60:63],v2 offset:46080
v_fma_f32 v27,-v96,v19,v27
v_mfma_f32_16x16x32_bf16 v[32:35],a[56:59],v[68:71],v[32:35]
ds_read_b128 v[64:67],v2 offset:47104
v_mul_f32 v72,v24,v24
v_mfma_f32_16x16x32_bf16 v[36:39],a[60:63],v[68:71],v[36:39]
s_waitcnt lgkmcnt(3)
v_mul_f32 v73,v25,v25
v_mul_f32 v74,v26,v26
v_mfma_f32_16x16x32_bf16 v[32:35],a[64:67],v[40:43],v[32:35]
s_waitcnt vmcnt(9)
ds_read_b128 v[68:71],v2 offset:48128
v_mul_f32 v75,v27,v27
v_mfma_f32_16x16x32_bf16 v[36:39],a[68:71],v[40:43],v[36:39]
s_barrier
v_mfma_f32_16x16x32_bf16 v[32:35],a[72:75],v[44:47],v[32:35]
buffer_load_dwordx4 v1,s[36:39],s67 offen offset:3072 sc1 lds
v_mfma_f32_16x16x32_bf16 v[36:39],a[76:79],v[44:47],v[36:39]
s_add_u32 m0,s71,s88
ds_read_b128 v[40:43],v2 offset:49152
ds_read_b32 v88,v8 offset:8192
v_mfma_f32_16x16x32_bf16 v[32:35],a[80:83],v[48:51],v[32:35]
buffer_load_dword v9,s[36:39],s72 offen offset:0 sc1 lds
v_mfma_f32_16x16x32_bf16 v[36:39],a[84:87],v[48:51],v[36:39]
s_xor_b32 s87,s63,65536
ds_read_b128 v[44:47],v2 offset:50176
v_fma_f32 v72,v98,v72,v97
v_mfma_f32_16x16x32_bf16 v[32:35],a[88:91],v[52:55],v[32:35]
s_cmp_ge_u32 s67,s59
ds_read_b128 v[48:51],v2 offset:51200
v_mul_f32 v72,v24,v72
v_mfma_f32_16x16x32_bf16 v[36:39],a[92:95],v[52:55],v[36:39]
s_waitcnt lgkmcnt(4)
v_exp_f32 v72,v72
v_mfma_f32_16x16x32_bf16 v[32:35],a[96:99],v[56:59],v[32:35]
s_cselect_b32 s86,s50,s51
ds_read_b128 v[52:55],v2 offset:52224
v_fma_f32 v72,v72,v99,v99
v_mfma_f32_16x16x32_bf16 v[36:39],a[100:103],v[56:59],v[36:39]
s_waitcnt lgkmcnt(3)
v_rcp_f32 v72,v72
v_mfma_f32_16x16x32_bf16 v[32:35],a[104:107],v[60:63],v[32:35]
s_cselect_b32 s87,s87,s64
ds_read_b128 v[56:59],v2 offset:53248
v_add_f32_dpp v88,v88,v88 quad_perm:[1,0,3,2] row_mask:0xf bank_mask:0xf
v_mfma_f32_16x16x32_bf16 v[36:39],a[108:111],v[60:63],v[36:39]
s_mov_b32 s62,s87
v_add_f32_dpp v88,v88,v88 quad_perm:[2,3,0,1] row_mask:0xf bank_mask:0xf
v_fma_f32 v73,v98,v73,v97
v_mfma_f32_16x16x32_bf16 v[32:35],a[112:115],v[64:67],v[32:35]
s_mov_b32 m0,s62
ds_read_b128 v[60:63],v2 offset:54272
v_mul_f32 v73,v25,v73
v_mfma_f32_16x16x32_bf16 v[36:39],a[116:119],v[64:67],v[36:39]
s_mov_b64 s[50:51],s[52:53]
v_exp_f32 v73,v73
v_mfma_f32_16x16x32_bf16 v[32:35],a[120:123],v[68:71],v[32:35]
ds_read_b128 v[64:67],v2 offset:55296
v_fma_f32 v73,v73,v99,v99
v_mfma_f32_16x16x32_bf16 v[36:39],a[124:127],v[68:71],v[36:39]
s_waitcnt lgkmcnt(3)
v_rcp_f32 v73,v73
v_mfma_f32_16x16x32_bf16 v[32:35],a[128:131],v[40:43],v[32:35]
s_nop 0
ds_read_b128 v[68:71],v2 offset:56320
v_add_f32_dpp v88,v88,v88 row_half_mirror row_mask:0xf bank_mask:0xf
v_mfma_f32_16x16x32_bf16 v[36:39],a[132:135],v[40:43],v[36:39]
s_barrier
v_mfma_f32_16x16x32_bf16 v[32:35],a[136:139],v[44:47],v[32:35]
ds_write_b32 v10,v88 offset:128
v_mfma_f32_16x16x32_bf16 v[36:39],a[140:143],v[44:47],v[36:39]
ds_read_b128 v[40:43],v2 offset:57344
ds_read_b32 v13,v7 offset:256
v_mfma_f32_16x16x32_bf16 v[32:35],a[144:147],v[48:51],v[32:35]
ds_read_b128 v[44:47],v2 offset:58368
v_fma_f32 v74,v98,v74,v97
v_mfma_f32_16x16x32_bf16 v[36:39],a[148:151],v[48:51],v[36:39]
v_mul_f32 v74,v26,v74
v_fma_f32 v75,v98,v75,v97
v_mfma_f32_16x16x32_bf16 v[32:35],a[152:155],v[52:55],v[32:35]
ds_read_b128 v[48:51],v2 offset:59392
v_mul_f32 v75,v27,v75
v_mfma_f32_16x16x32_bf16 v[36:39],a[156:159],v[52:55],v[36:39]
s_waitcnt lgkmcnt(5)
v_exp_f32 v74,v74
v_mfma_f32_16x16x32_bf16 v[32:35],a[160:163],v[56:59],v[32:35]
s_waitcnt lgkmcnt(2)
ds_read_b128 v[52:55],v2 offset:60416
v_fma_f32 v74,v74,v99,v99
v_mfma_f32_16x16x32_bf16 v[36:39],a[164:167],v[56:59],v[36:39]
v_cmp_eq_u32 vcc,s60,v13
v_exp_f32 v75,v75
v_mul_f32 v24,v24,v72
v_mfma_f32_16x16x32_bf16 v[32:35],a[168:171],v[60:63],v[32:35]
s_cmp_eq_u32 vcc_lo,-1
ds_read_b128 v[56:59],v2 offset:61440
s_cselect_b32 s66,s86,s59
v_mfma_f32_16x16x32_bf16 v[36:39],a[172:175],v[60:63],v[36:39]
buffer_load_dwordx4 v0,s[36:39],s66 offen offset:0 sc1 lds
v_mfma_f32_16x16x32_bf16 v[32:35],a[176:179],v[64:67],v[32:35]
s_lshr_b32 s70,s70,6
ds_read_b128 v[60:63],v2 offset:62464
v_fma_f32 v75,v75,v99,v99
v_mfma_f32_16x16x32_bf16 v[36:39],a[180:183],v[64:67],v[36:39]
s_xor_b32 s64,s64,65536
v_rcp_f32 v74,v74
v_mfma_f32_16x16x32_bf16 v[32:35],a[184:187],v[68:71],v[32:35]
s_add_u32 s74,s53,s55
ds_read_b128 v[64:67],v2 offset:63488
v_mul_f32 v25,v25,v73
v_mfma_f32_16x16x32_bf16 v[36:39],a[188:191],v[68:71],v[36:39]
s_waitcnt lgkmcnt(3)
buffer_load_dwordx4 v0,s[36:39],s66 offen offset:1024 sc1 lds
v_mfma_f32_16x16x32_bf16 v[32:35],a[192:195],v[40:43],v[32:35]
s_waitcnt vmcnt(7)
ds_read_b128 v[68:71],v2 offset:64512
v_xor_b32 v2,65536,v2
v_mfma_f32_16x16x32_bf16 v[36:39],a[196:199],v[40:43],v[36:39]
s_barrier
v_mfma_f32_16x16x32_bf16 v[32:35],a[200:203],v[44:47],v[32:35]
buffer_store_dword v5,v4,s[36:39],s70 offen offset:0 sc0 sc1
ds_read_b128 v[40:43],v2 offset:0
ds_read_b64 v[92:93],v11 offset:128
v_mul_f32 v26,v26,v74
v_mfma_f32_16x16x32_bf16 v[36:39],a[204:207],v[44:47],v[36:39]
s_cmp_eq_u32 s74,s56
v_rcp_f32 v75,v75
v_mfma_f32_16x16x32_bf16 v[32:35],a[208:211],v[48:51],v[32:35]
s_cselect_b32 s74,s57,s74
ds_read_b128 v[44:47],v2 offset:1024
v_mul_f32 v27,v27,v75
v_mfma_f32_16x16x32_bf16 v[36:39],a[212:215],v[48:51],v[36:39]
buffer_load_dwordx4 v0,s[36:39],s66 offen offset:2048 sc1 lds
v_mfma_f32_16x16x32_bf16 v[32:35],a[216:219],v[52:55],v[32:35]
s_cmp_eq_u32 s74,s58
ds_read_b128 v[48:51],v2 offset:2048
v_cvt_pk_bf16_f32 v80,v24,v25
v_mfma_f32_16x16x32_bf16 v[36:39],a[220:223],v[52:55],v[36:39]
s_waitcnt lgkmcnt(4)
v_cvt_pk_bf16_f32 v81,v26,v27
v_fma_f32 v28,-v96,v20,v28
v_mfma_f32_16x16x32_bf16 v[32:35],a[224:227],v[56:59],v[32:35]
s_cselect_b32 s74,s59,s74
v_fma_f32 v29,-v96,v21,v29
v_fma_f32 v30,-v96,v22,v30
v_fma_f32 v31,-v96,v23,v31
v_mul_f32 v76,v28,v28
v_mul_f32 v77,v29,v29
v_mul_f32 v78,v30,v30
v_mul_f32 v79,v31,v31
v_fma_f32 v76,v98,v76,v97
v_fma_f32 v77,v98,v77,v97
v_fma_f32 v78,v98,v78,v97
v_fma_f32 v79,v98,v79,v97
v_mul_f32 v76,v28,v76
v_mul_f32 v77,v29,v77
v_mul_f32 v78,v30,v78
v_mul_f32 v79,v31,v79
v_exp_f32 v76,v76
v_exp_f32 v77,v77
v_exp_f32 v78,v78
v_exp_f32 v79,v79
v_mfma_f32_16x16x32_bf16 v[36:39],a[228:231],v[56:59],v[36:39]
s_waitcnt lgkmcnt(2)
ds_read_b128 v[52:55],v2 offset:3072
buffer_load_dwordx4 v0,s[36:39],s66 offen offset:3072 sc1 lds
v_fma_f32 v76,v76,v99,v99
v_fma_f32 v77,v77,v99,v99
v_fma_f32 v78,v78,v99,v99
v_fma_f32 v79,v79,v99,v99
v_rcp_f32 v76,v76
v_rcp_f32 v77,v77
v_rcp_f32 v78,v78
v_rcp_f32 v79,v79
v_mfma_f32_16x16x32_bf16 v[32:35],a[232:235],v[60:63],v[32:35]
s_mov_b32 s52,s74
v_mul_f32 v28,v28,v76
v_mul_f32 v29,v29,v77
v_mul_f32 v30,v30,v78
v_mul_f32 v31,v31,v79
v_cvt_pk_bf16_f32 v82,v28,v29
v_cvt_pk_bf16_f32 v83,v30,v31)"
R"(
v_mfma_f32_16x16x32_bf16 v[36:39],a[236:239],v[60:63],v[36:39]
s_cmp_lt_u32 s67,s59
ds_read_b128 v[56:59],v2 offset:4096
buffer_store_dwordx4 v[80:83],v3,s[36:39],s73 offen offset:0 sc0 sc1
v_xor_b32 v8,16384,v8
v_mfma_f32_16x16x32_bf16 v[32:35],a[240:243],v[64:67],v[32:35]
ds_read_b128 v[60:63],v2 offset:5120
v_mfma_f32_16x16x32_bf16 v[36:39],a[244:247],v[64:67],v[36:39]
v_mfma_f32_16x16x32_bf16 v[32:35],a[248:251],v[68:71],v[32:35]
s_waitcnt lgkmcnt(2)
ds_read_b128 v[64:67],v2 offset:6144
v_mfma_f32_16x16x32_bf16 v[36:39],a[252:255],v[68:71],v[36:39]
s_cbranch_scc1 Lg3o_d0w3_%=
s_branch Lg3o_slow0_%=
Lg3o_slow0_%=:
s_cmp_lt_u32 s66,s59
s_cbranch_scc1 Lg3o_gs0_%=
s_cmp_eq_u32 s54,1
s_cselect_b32 s68,s49,s48
s_lshr_b32 s68,s68,5
s_add_u32 m0,s78,8192
s_nop 0
buffer_load_dword v6,s[36:39],s68 offen offset:0 sc1 lds
s_mul_i32 s73,s47,7
s_lshr_b32 s73,s73,1
v_mul_f32 v96,s75,v92
v_fma_f32 v93,v93,s75,v12
v_fma_f32 v93,-v96,v96,v93
v_max_f32 v93,v93,v12
v_rsq_f32 v92,v93
s_nop 0
v_mul_f32 v97,s76,v92
v_mul_f32 v98,v92,v92
v_mul_f32 v99,v93,v92
v_mul_f32 v98,s77,v98
v_mul_f32 v98,v98,v97
v_fma_f32 v32,-v96,v16,v32
v_fma_f32 v33,-v96,v17,v33
v_fma_f32 v34,-v96,v18,v34
v_fma_f32 v35,-v96,v19,v35
v_mul_f32 v72,v32,v32
v_mul_f32 v73,v33,v33
v_mul_f32 v74,v34,v34
v_mul_f32 v75,v35,v35
v_fma_f32 v72,v98,v72,v97
v_fma_f32 v73,v98,v73,v97
v_fma_f32 v74,v98,v74,v97
v_fma_f32 v75,v98,v75,v97
v_mul_f32 v72,v32,v72
v_mul_f32 v73,v33,v73
v_mul_f32 v74,v34,v74
v_mul_f32 v75,v35,v75
v_exp_f32 v72,v72
v_exp_f32 v73,v73
v_exp_f32 v74,v74
v_exp_f32 v75,v75
v_fma_f32 v72,v72,v99,v99
v_fma_f32 v73,v73,v99,v99
v_fma_f32 v74,v74,v99,v99
v_fma_f32 v75,v75,v99,v99
v_rcp_f32 v72,v72
v_rcp_f32 v73,v73
v_rcp_f32 v74,v74
v_rcp_f32 v75,v75
v_mul_f32 v32,v32,v72
s_add_u32 m0,s78,8448
s_nop 0
buffer_load_dword v6,s[36:39],s68 offen offset:0 sc1 lds
v_mul_f32 v33,v33,v73
v_mul_f32 v34,v34,v74
v_mul_f32 v35,v35,v75
v_cvt_pk_bf16_f32 v80,v32,v33
v_cvt_pk_bf16_f32 v81,v34,v35
v_fma_f32 v36,-v96,v20,v36
v_fma_f32 v37,-v96,v21,v37
v_fma_f32 v38,-v96,v22,v38
v_fma_f32 v39,-v96,v23,v39
v_mul_f32 v76,v36,v36
v_mul_f32 v77,v37,v37
v_mul_f32 v78,v38,v38
v_mul_f32 v79,v39,v39
v_fma_f32 v76,v98,v76,v97
v_fma_f32 v77,v98,v77,v97
v_fma_f32 v78,v98,v78,v97
v_fma_f32 v79,v98,v79,v97
v_mul_f32 v76,v36,v76
v_mul_f32 v77,v37,v77
v_mul_f32 v78,v38,v78
v_mul_f32 v79,v39,v79
v_exp_f32 v76,v76
v_exp_f32 v77,v77
v_exp_f32 v78,v78
v_exp_f32 v79,v79
v_fma_f32 v76,v76,v99,v99
v_fma_f32 v77,v77,v99,v99
v_fma_f32 v78,v78,v99,v99
v_fma_f32 v79,v79,v99,v99
v_rcp_f32 v76,v76
v_rcp_f32 v77,v77
v_rcp_f32 v78,v78
v_rcp_f32 v79,v79
v_mul_f32 v36,v36,v76
v_mul_f32 v37,v37,v77
v_mul_f32 v38,v38,v78
v_mul_f32 v39,v39,v79
v_cvt_pk_bf16_f32 v82,v36,v37
v_cvt_pk_bf16_f32 v83,v38,v39
buffer_store_dwordx4 v[80:83],v3,s[36:39],s73 offen offset:0 sc0 sc1
s_cmp_ge_u32 s48,s59
s_cbranch_scc1 Lg3o_done0_%=
s_add_u32 m0,s78,16384
s_nop 0
buffer_load_dword v6,s[36:39],s68 offen offset:0 sc1 lds
s_waitcnt vmcnt(3)
s_barrier
ds_read_b32 v13,v7 offset:8192
s_mul_i32 s70,s46,5
s_lshr_b32 s70,s70,6
buffer_store_dword v5,v4,s[36:39],s70 offen offset:0 sc0 sc1
s_mov_b32 s46,s59
s_mov_b32 s69,18432
s_waitcnt lgkmcnt(0)
v_cmp_eq_u32 vcc,s60,v13
s_nop 0
s_cmp_eq_u32 vcc_lo,-1
s_cbranch_scc1 Lg3o_go0_%=
s_add_u32 m0,s78,16640
s_nop 0
buffer_load_dword v6,s[36:39],s68 offen offset:0 sc1 lds
s_waitcnt vmcnt(4)
s_barrier
ds_read_b32 v13,v7 offset:8448
s_mov_b32 s69,18688
s_waitcnt lgkmcnt(0)
v_cmp_eq_u32 vcc,s60,v13
s_nop 0
s_cmp_eq_u32 vcc_lo,-1
s_cbranch_scc1 Lg3o_go0_%=
s_add_u32 m0,s78,8192
s_nop 0
buffer_load_dword v6,s[36:39],s68 offen offset:0 sc1 lds
s_waitcnt vmcnt(3)
s_barrier
ds_read_b32 v13,v7 offset:16384
s_mul_i32 s70,s47,5
s_lshr_b32 s70,s70,6
buffer_store_dword v5,v4,s[36:39],s70 offen offset:0 sc0 sc1
s_mov_b32 s47,s59
s_mov_b32 s69,10240
s_waitcnt lgkmcnt(0)
v_cmp_eq_u32 vcc,s60,v13
s_nop 0
s_cmp_eq_u32 vcc_lo,-1
s_cbranch_scc1 Lg3o_go0_%=
s_add_u32 m0,s78,8448
s_nop 0
buffer_load_dword v6,s[36:39],s68 offen offset:0 sc1 lds
s_waitcnt vmcnt(3)
s_barrier
ds_read_b32 v13,v7 offset:16640
s_mov_b32 s69,10496
s_waitcnt lgkmcnt(0)
v_cmp_eq_u32 vcc,s60,v13
s_nop 0
s_cmp_eq_u32 vcc_lo,-1
s_cbranch_scc1 Lg3o_go0_%=
Lg3o_poll0_%=:
s_add_u32 m0,s78,16384
s_nop 0
buffer_load_dword v6,s[36:39],s68 offen offset:0 sc1 lds
s_waitcnt vmcnt(2)
s_barrier
ds_read_b32 v13,v7 offset:8192
s_mov_b32 s69,18432
s_waitcnt lgkmcnt(0)
v_cmp_eq_u32 vcc,s60,v13
s_nop 0
s_cmp_eq_u32 vcc_lo,-1
s_cbranch_scc1 Lg3o_go0_%=
s_sleep 4
s_add_u32 m0,s78,16640
s_nop 0
buffer_load_dword v6,s[36:39],s68 offen offset:0 sc1 lds
s_waitcnt vmcnt(2)
s_barrier
ds_read_b32 v13,v7 offset:8448
s_mov_b32 s69,18688
s_waitcnt lgkmcnt(0)
v_cmp_eq_u32 vcc,s60,v13
s_nop 0
s_cmp_eq_u32 vcc_lo,-1
s_cbranch_scc1 Lg3o_go0_%=
s_sleep 4
s_add_u32 m0,s78,8192
s_nop 0
buffer_load_dword v6,s[36:39],s68 offen offset:0 sc1 lds
s_waitcnt vmcnt(2)
s_barrier
ds_read_b32 v13,v7 offset:16384
s_mov_b32 s69,10240)"
R"(
s_waitcnt lgkmcnt(0)
v_cmp_eq_u32 vcc,s60,v13
s_nop 0
s_cmp_eq_u32 vcc_lo,-1
s_cbranch_scc1 Lg3o_go0_%=
s_sleep 4
s_add_u32 m0,s78,8448
s_nop 0
buffer_load_dword v6,s[36:39],s68 offen offset:0 sc1 lds
s_waitcnt vmcnt(2)
s_barrier
ds_read_b32 v13,v7 offset:16640
s_mov_b32 s69,10496
s_waitcnt lgkmcnt(0)
v_cmp_eq_u32 vcc,s60,v13
s_nop 0
s_cmp_eq_u32 vcc_lo,-1
s_cbranch_scc1 Lg3o_go0_%=
s_sleep 4
s_add_u32 s79,s79,1
s_cmp_lt_u32 s79,20000
s_cbranch_scc1 Lg3o_poll0_%=
Lg3o_go0_%=:
s_xor_b32 s62,s63,65536
s_mov_b32 m0,s62
s_nop 0
buffer_load_dwordx4 v0,s[36:39],s48 offen offset:0 sc1 lds
buffer_load_dwordx4 v0,s[36:39],s48 offen offset:1024 sc1 lds
buffer_load_dwordx4 v0,s[36:39],s48 offen offset:2048 sc1 lds
buffer_load_dwordx4 v0,s[36:39],s48 offen offset:3072 sc1 lds
s_add_u32 m0,s62,16384
s_nop 0
buffer_load_dwordx4 v1,s[36:39],s48 offen offset:0 sc1 lds
buffer_load_dwordx4 v1,s[36:39],s48 offen offset:1024 sc1 lds
buffer_load_dwordx4 v1,s[36:39],s48 offen offset:2048 sc1 lds
buffer_load_dwordx4 v1,s[36:39],s48 offen offset:3072 sc1 lds
s_lshr_b32 s72,s48,5
s_lshr_b32 s71,s62,2
s_add_u32 m0,s71,s88
s_nop 0
buffer_load_dword v9,s[36:39],s72 offen offset:0 sc1 lds
s_cmp_eq_u32 vcc_hi,-1
s_cselect_b32 s66,s49,s59
s_xor_b32 s62,s64,65536
s_cmp_ge_u32 s66,s59
s_cselect_b32 s68,s49,s50
s_lshr_b32 s68,s68,5
s_add_u32 m0,s78,0
s_nop 0
buffer_load_dword v6,s[36:39],s68 offen offset:0 sc1 lds
s_mov_b32 m0,s62
s_nop 0
buffer_load_dwordx4 v0,s[36:39],s66 offen offset:0 sc1 lds
buffer_load_dwordx4 v0,s[36:39],s66 offen offset:1024 sc1 lds
buffer_load_dwordx4 v0,s[36:39],s66 offen offset:2048 sc1 lds
buffer_load_dwordx4 v0,s[36:39],s66 offen offset:3072 sc1 lds
s_waitcnt vmcnt(10)
s_barrier
ds_read_b128 v[40:43],v2 offset:0
ds_read_b128 v[44:47],v2 offset:1024
ds_read_b128 v[48:51],v2 offset:2048
ds_read_b128 v[52:55],v2 offset:3072
ds_read_b128 v[56:59],v2 offset:4096
ds_read_b128 v[60:63],v2 offset:5120
ds_read_b128 v[64:67],v2 offset:6144
s_mul_i32 s70,s46,5
s_lshr_b32 s70,s70,6
buffer_store_dword v5,v4,s[36:39],s70 offen offset:0 sc0 sc1
s_mov_b32 s46,s59
s_mul_i32 s70,s47,5
s_lshr_b32 s70,s70,6
buffer_store_dword v5,v4,s[36:39],s70 offen offset:0 sc0 sc1
s_mov_b32 s47,s59
s_mul_i32 s70,s59,5
s_lshr_b32 s70,s70,6
buffer_store_dword v5,v4,s[36:39],s70 offen offset:0 sc0 sc1
s_waitcnt lgkmcnt(3)
s_setpc_b64 s[90:91]
Lg3o_gs0_%=:
s_cmp_eq_u32 s54,1
s_cselect_b32 s68,s49,s48
s_lshr_b32 s68,s68,5
s_mul_i32 s73,s47,7
s_lshr_b32 s73,s73,1
s_add_u32 m0,s78,8192
s_nop 0
buffer_load_dword v6,s[36:39],s68 offen offset:0 sc1 lds
v_mul_f32 v96,s75,v92
v_fma_f32 v93,v93,s75,v12
v_fma_f32 v93,-v96,v96,v93
v_max_f32 v93,v93,v12
v_rsq_f32 v92,v93
s_nop 0
v_mul_f32 v97,s76,v92
v_mul_f32 v98,v92,v92
v_mul_f32 v99,v93,v92
v_mul_f32 v98,s77,v98
v_mul_f32 v98,v98,v97
v_fma_f32 v32,-v96,v16,v32
v_fma_f32 v33,-v96,v17,v33
v_fma_f32 v34,-v96,v18,v34
s_add_u32 m0,s62,16384
s_nop 0
buffer_load_dwordx4 v1,s[36:39],s66 offen offset:0 sc1 lds
v_fma_f32 v35,-v96,v19,v35
v_mul_f32 v72,v32,v32
v_mul_f32 v73,v33,v33
v_mul_f32 v74,v34,v34
v_mul_f32 v75,v35,v35
v_fma_f32 v72,v98,v72,v97
v_fma_f32 v73,v98,v73,v97
v_fma_f32 v74,v98,v74,v97
v_fma_f32 v75,v98,v75,v97
v_mul_f32 v72,v32,v72
v_mul_f32 v73,v33,v73
v_mul_f32 v74,v34,v74
v_mul_f32 v75,v35,v75
buffer_load_dwordx4 v1,s[36:39],s66 offen offset:1024 sc1 lds
v_exp_f32 v72,v72
v_exp_f32 v73,v73
v_exp_f32 v74,v74
v_exp_f32 v75,v75
v_fma_f32 v72,v72,v99,v99
v_fma_f32 v73,v73,v99,v99
v_fma_f32 v74,v74,v99,v99
v_fma_f32 v75,v75,v99,v99
v_rcp_f32 v72,v72
v_rcp_f32 v73,v73
v_rcp_f32 v74,v74
v_rcp_f32 v75,v75
v_mul_f32 v32,v32,v72
buffer_load_dwordx4 v1,s[36:39],s66 offen offset:2048 sc1 lds
v_mul_f32 v33,v33,v73
v_mul_f32 v34,v34,v74
v_mul_f32 v35,v35,v75
v_cvt_pk_bf16_f32 v80,v32,v33
v_cvt_pk_bf16_f32 v81,v34,v35
v_fma_f32 v36,-v96,v20,v36
v_fma_f32 v37,-v96,v21,v37
v_fma_f32 v38,-v96,v22,v38
v_fma_f32 v39,-v96,v23,v39
v_mul_f32 v76,v36,v36
v_mul_f32 v77,v37,v37
v_mul_f32 v78,v38,v38
v_mul_f32 v79,v39,v39
buffer_load_dwordx4 v1,s[36:39],s66 offen offset:3072 sc1 lds
v_fma_f32 v76,v98,v76,v97
v_fma_f32 v77,v98,v77,v97
v_fma_f32 v78,v98,v78,v97
v_fma_f32 v79,v98,v79,v97
v_mul_f32 v76,v36,v76
v_mul_f32 v77,v37,v77
v_mul_f32 v78,v38,v78
v_mul_f32 v79,v39,v79
v_exp_f32 v76,v76
v_exp_f32 v77,v77
v_exp_f32 v78,v78
v_exp_f32 v79,v79
v_fma_f32 v76,v76,v99,v99
s_lshr_b32 s72,s66,5
s_lshr_b32 s71,s62,2
s_add_u32 m0,s71,s88
s_nop 0
buffer_load_dword v9,s[36:39],s72 offen offset:0 sc1 lds
v_fma_f32 v77,v77,v99,v99
v_fma_f32 v78,v78,v99,v99
v_fma_f32 v79,v79,v99,v99
v_rcp_f32 v76,v76
v_rcp_f32 v77,v77
v_rcp_f32 v78,v78
v_rcp_f32 v79,v79
v_mul_f32 v36,v36,v76
v_mul_f32 v37,v37,v77
v_mul_f32 v38,v38,v78
v_mul_f32 v39,v39,v79
v_cvt_pk_bf16_f32 v82,v36,v37
v_cvt_pk_bf16_f32 v83,v38,v39
buffer_store_dwordx4 v[80:83],v3,s[36:39],s73 offen offset:0 sc0 sc1
s_waitcnt vmcnt(6)
s_barrier
ds_read_b32 v13,v7 offset:8192
ds_read_b128 v[40:43],v2 offset:0
ds_read_b128 v[44:47],v2 offset:1024
ds_read_b128 v[48:51],v2 offset:2048
ds_read_b128 v[52:55],v2 offset:3072
ds_read_b128 v[56:59],v2 offset:4096
ds_read_b128 v[60:63],v2 offset:5120
ds_read_b128 v[64:67],v2 offset:6144
s_mul_i32 s70,s46,5
s_lshr_b32 s70,s70,6
buffer_store_dword v5,v4,s[36:39],s70 offen offset:0 sc0 sc1
s_waitcnt lgkmcnt(7)
v_cmp_eq_u32 vcc,s60,v13
s_nop 0
s_cmp_eq_u32 vcc_hi,-1
s_cselect_b32 s66,s49,s59
s_xor_b32 s62,s64,65536
s_cmp_ge_u32 s66,s59
s_cselect_b32 s68,s49,s50
s_lshr_b32 s68,s68,5)"
R"(
s_add_u32 m0,s78,0
s_nop 0
buffer_load_dword v6,s[36:39],s68 offen offset:0 sc1 lds
s_mov_b32 m0,s62
s_nop 0
buffer_load_dwordx4 v0,s[36:39],s66 offen offset:0 sc1 lds
buffer_load_dwordx4 v0,s[36:39],s66 offen offset:1024 sc1 lds
buffer_load_dwordx4 v0,s[36:39],s66 offen offset:2048 sc1 lds
buffer_load_dwordx4 v0,s[36:39],s66 offen offset:3072 sc1 lds
s_mov_b32 s46,s47
s_mov_b32 s47,s59
s_mul_i32 s70,s59,5
s_lshr_b32 s70,s70,6
buffer_store_dword v5,v4,s[36:39],s70 offen offset:0 sc0 sc1
s_waitcnt lgkmcnt(3)
s_setpc_b64 s[90:91]
Lg3o_done0_%=:
s_waitcnt vmcnt(0)
s_barrier
s_mul_i32 s70,s46,5
s_lshr_b32 s70,s70,6
buffer_store_dword v5,v4,s[36:39],s70 offen offset:0 sc0 sc1
s_mul_i32 s70,s47,5
s_lshr_b32 s70,s70,6
buffer_store_dword v5,v4,s[36:39],s70 offen offset:0 sc0 sc1
s_branch Lg3o_done_%=
Lg3o_slow1_%=:
s_cmp_lt_u32 s67,s59
s_cbranch_scc1 Lg3o_gs1_%=
s_cmp_eq_u32 s54,1
s_cselect_b32 s68,s50,s49
s_lshr_b32 s68,s68,5
s_add_u32 m0,s78,8192
s_nop 0
buffer_load_dword v6,s[36:39],s68 offen offset:0 sc1 lds
s_mul_i32 s73,s48,7
s_lshr_b32 s73,s73,1
v_mul_f32 v96,s75,v92
v_fma_f32 v93,v93,s75,v12
v_fma_f32 v93,-v96,v96,v93
v_max_f32 v93,v93,v12
v_rsq_f32 v92,v93
s_nop 0
v_mul_f32 v97,s76,v92
v_mul_f32 v98,v92,v92
v_mul_f32 v99,v93,v92
v_mul_f32 v98,s77,v98
v_mul_f32 v98,v98,v97
v_fma_f32 v24,-v96,v16,v24
v_fma_f32 v25,-v96,v17,v25
v_fma_f32 v26,-v96,v18,v26
v_fma_f32 v27,-v96,v19,v27
v_mul_f32 v72,v24,v24
v_mul_f32 v73,v25,v25
v_mul_f32 v74,v26,v26
v_mul_f32 v75,v27,v27
v_fma_f32 v72,v98,v72,v97
v_fma_f32 v73,v98,v73,v97
v_fma_f32 v74,v98,v74,v97
v_fma_f32 v75,v98,v75,v97
v_mul_f32 v72,v24,v72
v_mul_f32 v73,v25,v73
v_mul_f32 v74,v26,v74
v_mul_f32 v75,v27,v75
v_exp_f32 v72,v72
v_exp_f32 v73,v73
v_exp_f32 v74,v74
v_exp_f32 v75,v75
v_fma_f32 v72,v72,v99,v99
v_fma_f32 v73,v73,v99,v99
v_fma_f32 v74,v74,v99,v99
v_fma_f32 v75,v75,v99,v99
v_rcp_f32 v72,v72
v_rcp_f32 v73,v73
v_rcp_f32 v74,v74
v_rcp_f32 v75,v75
v_mul_f32 v24,v24,v72
s_add_u32 m0,s78,8448
s_nop 0
buffer_load_dword v6,s[36:39],s68 offen offset:0 sc1 lds
v_mul_f32 v25,v25,v73
v_mul_f32 v26,v26,v74
v_mul_f32 v27,v27,v75
v_cvt_pk_bf16_f32 v80,v24,v25
v_cvt_pk_bf16_f32 v81,v26,v27
v_fma_f32 v28,-v96,v20,v28
v_fma_f32 v29,-v96,v21,v29
v_fma_f32 v30,-v96,v22,v30
v_fma_f32 v31,-v96,v23,v31
v_mul_f32 v76,v28,v28
v_mul_f32 v77,v29,v29
v_mul_f32 v78,v30,v30
v_mul_f32 v79,v31,v31
v_fma_f32 v76,v98,v76,v97
v_fma_f32 v77,v98,v77,v97
v_fma_f32 v78,v98,v78,v97
v_fma_f32 v79,v98,v79,v97
v_mul_f32 v76,v28,v76
v_mul_f32 v77,v29,v77
v_mul_f32 v78,v30,v78
v_mul_f32 v79,v31,v79
v_exp_f32 v76,v76
v_exp_f32 v77,v77
v_exp_f32 v78,v78
v_exp_f32 v79,v79
v_fma_f32 v76,v76,v99,v99
v_fma_f32 v77,v77,v99,v99
v_fma_f32 v78,v78,v99,v99
v_fma_f32 v79,v79,v99,v99
v_rcp_f32 v76,v76
v_rcp_f32 v77,v77
v_rcp_f32 v78,v78
v_rcp_f32 v79,v79
v_mul_f32 v28,v28,v76
v_mul_f32 v29,v29,v77
v_mul_f32 v30,v30,v78
v_mul_f32 v31,v31,v79
v_cvt_pk_bf16_f32 v82,v28,v29
v_cvt_pk_bf16_f32 v83,v30,v31
buffer_store_dwordx4 v[80:83],v3,s[36:39],s73 offen offset:0 sc0 sc1
s_cmp_ge_u32 s49,s59
s_cbranch_scc1 Lg3o_done1_%=
s_add_u32 m0,s78,16384
s_nop 0
buffer_load_dword v6,s[36:39],s68 offen offset:0 sc1 lds
s_waitcnt vmcnt(3)
s_barrier
ds_read_b32 v13,v7 offset:8192
s_mul_i32 s70,s47,5
s_lshr_b32 s70,s70,6
buffer_store_dword v5,v4,s[36:39],s70 offen offset:0 sc0 sc1
s_mov_b32 s47,s59
s_mov_b32 s69,18432
s_waitcnt lgkmcnt(0)
v_cmp_eq_u32 vcc,s60,v13
s_nop 0
s_cmp_eq_u32 vcc_lo,-1
s_cbranch_scc1 Lg3o_go1_%=
s_add_u32 m0,s78,16640
s_nop 0
buffer_load_dword v6,s[36:39],s68 offen offset:0 sc1 lds
s_waitcnt vmcnt(4)
s_barrier
ds_read_b32 v13,v7 offset:8448
s_mov_b32 s69,18688
s_waitcnt lgkmcnt(0)
v_cmp_eq_u32 vcc,s60,v13
s_nop 0
s_cmp_eq_u32 vcc_lo,-1
s_cbranch_scc1 Lg3o_go1_%=
s_add_u32 m0,s78,8192
s_nop 0
buffer_load_dword v6,s[36:39],s68 offen offset:0 sc1 lds
s_waitcnt vmcnt(3)
s_barrier
ds_read_b32 v13,v7 offset:16384
s_mul_i32 s70,s48,5
s_lshr_b32 s70,s70,6
buffer_store_dword v5,v4,s[36:39],s70 offen offset:0 sc0 sc1
s_mov_b32 s48,s59
s_mov_b32 s69,10240
s_waitcnt lgkmcnt(0)
v_cmp_eq_u32 vcc,s60,v13
s_nop 0
s_cmp_eq_u32 vcc_lo,-1
s_cbranch_scc1 Lg3o_go1_%=
s_add_u32 m0,s78,8448
s_nop 0
buffer_load_dword v6,s[36:39],s68 offen offset:0 sc1 lds
s_waitcnt vmcnt(3)
s_barrier
ds_read_b32 v13,v7 offset:16640
s_mov_b32 s69,10496
s_waitcnt lgkmcnt(0)
v_cmp_eq_u32 vcc,s60,v13
s_nop 0
s_cmp_eq_u32 vcc_lo,-1
s_cbranch_scc1 Lg3o_go1_%=
Lg3o_poll1_%=:
s_add_u32 m0,s78,16384
s_nop 0
buffer_load_dword v6,s[36:39],s68 offen offset:0 sc1 lds
s_waitcnt vmcnt(2)
s_barrier
ds_read_b32 v13,v7 offset:8192
s_mov_b32 s69,18432
s_waitcnt lgkmcnt(0)
v_cmp_eq_u32 vcc,s60,v13
s_nop 0
s_cmp_eq_u32 vcc_lo,-1
s_cbranch_scc1 Lg3o_go1_%=
s_sleep 4
s_add_u32 m0,s78,16640
s_nop 0
buffer_load_dword v6,s[36:39],s68 offen offset:0 sc1 lds
s_waitcnt vmcnt(2)
s_barrier
ds_read_b32 v13,v7 offset:8448
s_mov_b32 s69,18688
s_waitcnt lgkmcnt(0))"
R"(
v_cmp_eq_u32 vcc,s60,v13
s_nop 0
s_cmp_eq_u32 vcc_lo,-1
s_cbranch_scc1 Lg3o_go1_%=
s_sleep 4
s_add_u32 m0,s78,8192
s_nop 0
buffer_load_dword v6,s[36:39],s68 offen offset:0 sc1 lds
s_waitcnt vmcnt(2)
s_barrier
ds_read_b32 v13,v7 offset:16384
s_mov_b32 s69,10240
s_waitcnt lgkmcnt(0)
v_cmp_eq_u32 vcc,s60,v13
s_nop 0
s_cmp_eq_u32 vcc_lo,-1
s_cbranch_scc1 Lg3o_go1_%=
s_sleep 4
s_add_u32 m0,s78,8448
s_nop 0
buffer_load_dword v6,s[36:39],s68 offen offset:0 sc1 lds
s_waitcnt vmcnt(2)
s_barrier
ds_read_b32 v13,v7 offset:16640
s_mov_b32 s69,10496
s_waitcnt lgkmcnt(0)
v_cmp_eq_u32 vcc,s60,v13
s_nop 0
s_cmp_eq_u32 vcc_lo,-1
s_cbranch_scc1 Lg3o_go1_%=
s_sleep 4
s_add_u32 s79,s79,1
s_cmp_lt_u32 s79,20000
s_cbranch_scc1 Lg3o_poll1_%=
Lg3o_go1_%=:
s_xor_b32 s62,s64,65536
s_mov_b32 m0,s62
s_nop 0
buffer_load_dwordx4 v0,s[36:39],s49 offen offset:0 sc1 lds
buffer_load_dwordx4 v0,s[36:39],s49 offen offset:1024 sc1 lds
buffer_load_dwordx4 v0,s[36:39],s49 offen offset:2048 sc1 lds
buffer_load_dwordx4 v0,s[36:39],s49 offen offset:3072 sc1 lds
s_add_u32 m0,s62,16384
s_nop 0
buffer_load_dwordx4 v1,s[36:39],s49 offen offset:0 sc1 lds
buffer_load_dwordx4 v1,s[36:39],s49 offen offset:1024 sc1 lds
buffer_load_dwordx4 v1,s[36:39],s49 offen offset:2048 sc1 lds
buffer_load_dwordx4 v1,s[36:39],s49 offen offset:3072 sc1 lds
s_lshr_b32 s72,s49,5
s_lshr_b32 s71,s62,2
s_add_u32 m0,s71,s88
s_nop 0
buffer_load_dword v9,s[36:39],s72 offen offset:0 sc1 lds
s_cmp_eq_u32 vcc_hi,-1
s_cselect_b32 s67,s50,s59
s_xor_b32 s62,s63,65536
s_cmp_ge_u32 s67,s59
s_cselect_b32 s68,s50,s51
s_lshr_b32 s68,s68,5
s_add_u32 m0,s78,256
s_nop 0
buffer_load_dword v6,s[36:39],s68 offen offset:0 sc1 lds
s_mov_b32 m0,s62
s_nop 0
buffer_load_dwordx4 v0,s[36:39],s67 offen offset:0 sc1 lds
buffer_load_dwordx4 v0,s[36:39],s67 offen offset:1024 sc1 lds
buffer_load_dwordx4 v0,s[36:39],s67 offen offset:2048 sc1 lds
buffer_load_dwordx4 v0,s[36:39],s67 offen offset:3072 sc1 lds
s_waitcnt vmcnt(10)
s_barrier
ds_read_b128 v[40:43],v2 offset:32768
ds_read_b128 v[44:47],v2 offset:33792
ds_read_b128 v[48:51],v2 offset:34816
ds_read_b128 v[52:55],v2 offset:35840
ds_read_b128 v[56:59],v2 offset:36864
ds_read_b128 v[60:63],v2 offset:37888
ds_read_b128 v[64:67],v2 offset:38912
s_mul_i32 s70,s47,5
s_lshr_b32 s70,s70,6
buffer_store_dword v5,v4,s[36:39],s70 offen offset:0 sc0 sc1
s_mov_b32 s47,s59
s_mul_i32 s70,s48,5
s_lshr_b32 s70,s70,6
buffer_store_dword v5,v4,s[36:39],s70 offen offset:0 sc0 sc1
s_mov_b32 s48,s59
s_mul_i32 s70,s59,5
s_lshr_b32 s70,s70,6
buffer_store_dword v5,v4,s[36:39],s70 offen offset:0 sc0 sc1
s_waitcnt lgkmcnt(3)
s_setpc_b64 s[92:93]
Lg3o_gs1_%=:
s_cmp_eq_u32 s54,1
s_cselect_b32 s68,s50,s49
s_lshr_b32 s68,s68,5
s_mul_i32 s73,s48,7
s_lshr_b32 s73,s73,1
s_add_u32 m0,s78,8192
s_nop 0
buffer_load_dword v6,s[36:39],s68 offen offset:0 sc1 lds
v_mul_f32 v96,s75,v92
v_fma_f32 v93,v93,s75,v12
v_fma_f32 v93,-v96,v96,v93
v_max_f32 v93,v93,v12
v_rsq_f32 v92,v93
s_nop 0
v_mul_f32 v97,s76,v92
v_mul_f32 v98,v92,v92
v_mul_f32 v99,v93,v92
v_mul_f32 v98,s77,v98
v_mul_f32 v98,v98,v97
v_fma_f32 v24,-v96,v16,v24
v_fma_f32 v25,-v96,v17,v25
v_fma_f32 v26,-v96,v18,v26
s_add_u32 m0,s62,16384
s_nop 0
buffer_load_dwordx4 v1,s[36:39],s67 offen offset:0 sc1 lds
v_fma_f32 v27,-v96,v19,v27
v_mul_f32 v72,v24,v24
v_mul_f32 v73,v25,v25
v_mul_f32 v74,v26,v26
v_mul_f32 v75,v27,v27
v_fma_f32 v72,v98,v72,v97
v_fma_f32 v73,v98,v73,v97
v_fma_f32 v74,v98,v74,v97
v_fma_f32 v75,v98,v75,v97
v_mul_f32 v72,v24,v72
v_mul_f32 v73,v25,v73
v_mul_f32 v74,v26,v74
v_mul_f32 v75,v27,v75
buffer_load_dwordx4 v1,s[36:39],s67 offen offset:1024 sc1 lds
v_exp_f32 v72,v72
v_exp_f32 v73,v73
v_exp_f32 v74,v74
v_exp_f32 v75,v75
v_fma_f32 v72,v72,v99,v99
v_fma_f32 v73,v73,v99,v99
v_fma_f32 v74,v74,v99,v99
v_fma_f32 v75,v75,v99,v99
v_rcp_f32 v72,v72
v_rcp_f32 v73,v73
v_rcp_f32 v74,v74
v_rcp_f32 v75,v75
v_mul_f32 v24,v24,v72
buffer_load_dwordx4 v1,s[36:39],s67 offen offset:2048 sc1 lds
v_mul_f32 v25,v25,v73
v_mul_f32 v26,v26,v74
v_mul_f32 v27,v27,v75
v_cvt_pk_bf16_f32 v80,v24,v25
v_cvt_pk_bf16_f32 v81,v26,v27
v_fma_f32 v28,-v96,v20,v28
v_fma_f32 v29,-v96,v21,v29
v_fma_f32 v30,-v96,v22,v30
v_fma_f32 v31,-v96,v23,v31
v_mul_f32 v76,v28,v28
v_mul_f32 v77,v29,v29
v_mul_f32 v78,v30,v30
v_mul_f32 v79,v31,v31
buffer_load_dwordx4 v1,s[36:39],s67 offen offset:3072 sc1 lds
v_fma_f32 v76,v98,v76,v97
v_fma_f32 v77,v98,v77,v97
v_fma_f32 v78,v98,v78,v97
v_fma_f32 v79,v98,v79,v97
v_mul_f32 v76,v28,v76
v_mul_f32 v77,v29,v77
v_mul_f32 v78,v30,v78
v_mul_f32 v79,v31,v79
v_exp_f32 v76,v76
v_exp_f32 v77,v77
v_exp_f32 v78,v78
v_exp_f32 v79,v79
v_fma_f32 v76,v76,v99,v99
s_lshr_b32 s72,s67,5
s_lshr_b32 s71,s62,2
s_add_u32 m0,s71,s88
s_nop 0
buffer_load_dword v9,s[36:39],s72 offen offset:0 sc1 lds
v_fma_f32 v77,v77,v99,v99
v_fma_f32 v78,v78,v99,v99
v_fma_f32 v79,v79,v99,v99
v_rcp_f32 v76,v76
v_rcp_f32 v77,v77
v_rcp_f32 v78,v78
v_rcp_f32 v79,v79
v_mul_f32 v28,v28,v76
v_mul_f32 v29,v29,v77
v_mul_f32 v30,v30,v78
v_mul_f32 v31,v31,v79
v_cvt_pk_bf16_f32 v82,v28,v29
v_cvt_pk_bf16_f32 v83,v30,v31
buffer_store_dwordx4 v[80:83],v3,s[36:39],s73 offen offset:0 sc0 sc1
s_waitcnt vmcnt(6)
s_barrier
ds_read_b32 v13,v7 offset:8192
ds_read_b128 v[40:43],v2 offset:32768
ds_read_b128 v[44:47],v2 offset:33792
ds_read_b128 v[48:51],v2 offset:34816
ds_read_b128 v[52:55],v2 offset:35840
ds_read_b128 v[56:59],v2 offset:36864
ds_read_b128 v[60:63],v2 offset:37888
ds_read_b128 v[64:67],v2 offset:38912)"
R"(
s_mul_i32 s70,s47,5
s_lshr_b32 s70,s70,6
buffer_store_dword v5,v4,s[36:39],s70 offen offset:0 sc0 sc1
s_waitcnt lgkmcnt(7)
v_cmp_eq_u32 vcc,s60,v13
s_nop 0
s_cmp_eq_u32 vcc_hi,-1
s_cselect_b32 s67,s50,s59
s_xor_b32 s62,s63,65536
s_cmp_ge_u32 s67,s59
s_cselect_b32 s68,s50,s51
s_lshr_b32 s68,s68,5
s_add_u32 m0,s78,256
s_nop 0
buffer_load_dword v6,s[36:39],s68 offen offset:0 sc1 lds
s_mov_b32 m0,s62
s_nop 0
buffer_load_dwordx4 v0,s[36:39],s67 offen offset:0 sc1 lds
buffer_load_dwordx4 v0,s[36:39],s67 offen offset:1024 sc1 lds
buffer_load_dwordx4 v0,s[36:39],s67 offen offset:2048 sc1 lds
buffer_load_dwordx4 v0,s[36:39],s67 offen offset:3072 sc1 lds
s_mov_b32 s47,s48
s_mov_b32 s48,s59
s_mul_i32 s70,s59,5
s_lshr_b32 s70,s70,6
buffer_store_dword v5,v4,s[36:39],s70 offen offset:0 sc0 sc1
s_waitcnt lgkmcnt(3)
s_setpc_b64 s[92:93]
Lg3o_done1_%=:
s_waitcnt vmcnt(0)
s_barrier
s_mul_i32 s70,s47,5
s_lshr_b32 s70,s70,6
buffer_store_dword v5,v4,s[36:39],s70 offen offset:0 sc0 sc1
s_mul_i32 s70,s48,5
s_lshr_b32 s70,s70,6
buffer_store_dword v5,v4,s[36:39],s70 offen offset:0 sc0 sc1
s_branch Lg3o_done_%=
Lg3o_done_%=:
s_waitcnt vmcnt(0) lgkmcnt(0)
s_barrier
s_endpgm
Lcode_g3o_e:)"
  :: "s"(ws), "s"(wave), "s"(sStep), "s"(sX1), "s"(sX2), "s"(sU0), "s"(sWw), "s"(sBuf), "v"(vSrc), "v"(vA), "v"(vOut), "v"(vFlag), "v"(vPollOff), "v"(vSnapRd), "v"(vX), "v"(vAuxS), "v"(vStW), "v"(vStR), "v"(vWd0), "v"(vWd1), "v"(vWr0), "v"(vWr1), "v"(vWr2), "s"(w3) : "v0", "v1", "v2", "v3", "v4", "v5", "v6", "v7", "v8", "v9", "v10", "v11", "v12", "v13", "v14", "v15", "v16", "v17", "v18", "v19", "v20", "v21", "v22", "v23", "v24", "v25", "v26", "v27", "v28", "v29", "v30", "v31", "v32", "v33", "v34", "v35", "v36", "v37", "v38", "v39", "v40", "v41", "v42", "v43", "v44", "v45", "v46", "v47", "v48", "v49", "v50", "v51", "v52", "v53", "v54", "v55", "v56", "v57", "v58", "v59", "v60", "v61", "v62", "v63", "v64", "v65", "v66", "v67", "v68", "v69", "v70", "v71", "v72", "v73", "v74", "v75", "v76", "v77", "v78", "v79", "v80", "v81", "v82", "v83", "v84", "v85", "v86", "v87", "v88", "v89", "v90", "v91", "v92", "v93", "v94", "v95", "v96", "v97", "v98", "v99", "v100", "v101", "v102", "v103", "v104", "v105", "v106", "v107", "v108", "v109", "v110", "v111", "v112", "v113", "v114", "v115", "v116", "v117", "v118", "v119", "v120", "v121", "v122", "v123", "v124", "v125", "v126", "v127", "v128", "v129", "v130", "v131", "v132", "v133", "v134", "v135", "v136", "v137", "v138", "v139", "v140", "v141", "v142", "v143", "v144", "v145", "v146", "v147", "v148", "v149", "v150", "v151", "v152", "v153", "v154", "v155", "v156", "v157", "v158", "v159", "v160", "v161", "v162", "v163", "v164", "v165", "v166", "v167", "v168", "v169", "v170", "v171", "v172", "v173", "v174", "v175", "v176", "v177", "v178", "v179", "v180", "v181", "v182", "v183", "v184", "v185", "v186", "v187", "v188", "v189", "v190", "v191", "v192", "v193", "v194", "v195", "v196", "v197", "v198", "v199", "v200", "v201", "v202", "v203", "v204", "v205", "v206", "v207", "v208", "v209", "v210", "v211", "v212", "v213", "v214", "v215", "v216", "v217", "v218", "v219", "v220", "v221", "v222", "v223", "v224", "v225", "v226", "v227", "v228", "v229", "v230", "v231", "v255", "a0", "a1", "a2", "a3", "a4", "a5", "a6", "a7", "a8", "a9", "a10", "a11", "a12", "a13", "a14", "a15", "a16", "a17", "a18", "a19", "a20", "a21", "a22", "a23", "a24", "a25", "a26", "a27", "a28", "a29", "a30", "a31", "a32", "a33", "a34", "a35", "a36", "a37", "a38", "a39", "a40", "a41", "a42", "a43", "a44", "a45", "a46", "a47", "a48", "a49", "a50", "a51", "a52", "a53", "a54", "a55", "a56", "a57", "a58", "a59", "a60", "a61", "a62", "a63", "a64", "a65", "a66", "a67", "a68", "a69", "a70", "a71", "a72", "a73", "a74", "a75", "a76", "a77", "a78", "a79", "a80", "a81", "a82", "a83", "a84", "a85", "a86", "a87", "a88", "a89", "a90", "a91", "a92", "a93", "a94", "a95", "a96", "a97", "a98", "a99", "a100", "a101", "a102", "a103", "a104", "a105", "a106", "a107", "a108", "a109", "a110", "a111", "a112", "a113", "a114", "a115", "a116", "a117", "a118", "a119", "a120", "a121", "a122", "a123", "a124", "a125", "a126", "a127", "a128", "a129", "a130", "a131", "a132", "a133", "a134", "a135", "a136", "a137", "a138", "a139", "a140", "a141", "a142", "a143", "a144", "a145", "a146", "a147", "a148", "a149", "a150", "a151", "a152", "a153", "a154", "a155", "a156", "a157", "a158", "a159", "a160", "a161", "a162", "a163", "a164", "a165", "a166", "a167", "a168", "a169", "a170", "a171", "a172", "a173", "a174", "a175", "a176", "a177", "a178", "a179", "a180", "a181", "a182", "a183", "a184", "a185", "a186", "a187", "a188", "a189", "a190", "a191", "a192", "a193", "a194", "a195", "a196", "a197", "a198", "a199", "a200", "a201", "a202", "a203", "a204", "a205", "a206", "a207", "a208", "a209", "a210", "a211", "a212", "a213", "a214", "a215", "a216", "a217", "a218", "a219", "a220", "a221", "a222", "a223", "a224", "a225", "a226", "a227", "a228", "a229", "a230", "a231", "a232", "a233", "a234", "a235", "a236", "a237", "a238", "a239", "a240", "a241", "a242", "a243", "a244", "a245", "a246", "a247", "a248", "a249", "a250", "a251", "a252", "a253", "a254", "a255", "s36", "s37", "s38", "s39", "s40", "s41", "s42", "s43", "s44", "s45", "s46", "s47", "s48", "s49", "s50", "s51", "s52", "s53", "s54", "s55", "s56", "s57", "s58", "s59", "s60", "s61", "s62", "s63", "s64", "s65", "s66", "s67", "s68", "s69", "s70", "s71", "s72", "s73", "s74", "s75", "s76", "s77", "s78", "s79", "s80", "s81", "s82", "s83", "s84", "s85", "s86", "s87", "s88", "s89", "s90", "s91", "s92", "s93", "s94", "s95", "s96", "s97", "s98", "s99", "vcc", "scc", "memory"); }
    __builtin_unreachable();
  } else if (kind == 4) {
    COMMON(10, OFF_FG + kh * 9 * 128, OFF_FP + (kh * 8 + slice) * 128, 159744, 256, 2048, 5);
    const unsigned vSrc = OFF_G + kh * 57344 + wave * 7168 + vA, vOut = (kh ? OFF_P1 : OFF_P0) + (l15 >> 3) * 16384 + (slice * 4 + wave) * 512 + lg * 128 + (l15 & 7) * 16;
    const unsigned vAuxS = (kh ? OFF_XP : OFF_X1) + (slice * 4 + wave) * 1024 + vA;
    const unsigned sWw = (kh * 1792 + 16 * wave) * 2048 + slice * 256;
    asm volatile(R"(
Lcode_g4_b:
s_mov_b64 s[36:37],%0
s_mov_b32 s38,111193216
s_mov_b32 s39,0x00020000
s_mov_b32 s54,%1
s_mov_b32 s55,%2
s_mov_b32 s56,%3
s_mov_b32 s58,%4
s_mov_b32 s83,%5
s_mov_b32 s82,%6
s_mov_b32 s84,%7
v_mov_b32 v0,%8
v_mov_b32 v1,%9
v_mov_b32 v3,%10
v_mov_b32 v4,%11
v_mov_b32 v6,%12
v_mov_b32 v7,%13
v_mov_b32 v8,%14
v_mov_b32 v12,%15
v_mov_b32 v13,%16
v_mov_b32 v14,%17
v_mov_b32 v15,%18
v_mov_b32 v16,%19
s_mov_b64 s[40:41],%20
s_mov_b32 s42,7340032
s_mov_b32 s43,0x00020000
s_lshl_b32 s88,s54,11
s_mul_i32 s68,s54,11264
s_add_u32 s68,s68,114688
v_add_u32 v9,s68,v1
s_mov_b32 s69,111193088
buffer_load_dword v5,off,s[36:39],s69 sc1
s_add_u32 m0,s88,0
s_add_u32 s61,s82,0
buffer_load_dwordx4 v12,s[40:43],s61 offen offset:0  lds
s_add_u32 m0,s88,1024
s_nop 0
buffer_load_dwordx4 v13,s[40:43],s61 offen offset:0  lds
s_add_u32 m0,s88,8064
s_nop 0
buffer_load_dwordx4 v12,s[40:43],s61 offen offset:128  lds
s_add_u32 m0,s88,9088
s_nop 0
buffer_load_dwordx4 v13,s[40:43],s61 offen offset:128  lds
s_add_u32 m0,s88,16384
s_add_u32 s61,s82,131072
buffer_load_dwordx4 v12,s[40:43],s61 offen offset:0  lds
s_add_u32 m0,s88,17408
s_nop 0
buffer_load_dwordx4 v13,s[40:43],s61 offen offset:0  lds
s_add_u32 m0,s88,24448
s_nop 0
buffer_load_dwordx4 v12,s[40:43],s61 offen offset:128  lds
s_add_u32 m0,s88,25472
s_nop 0
buffer_load_dwordx4 v13,s[40:43],s61 offen offset:128  lds
s_add_u32 m0,s88,32768
s_add_u32 s61,s82,262144
buffer_load_dwordx4 v12,s[40:43],s61 offen offset:0  lds
s_add_u32 m0,s88,33792
s_nop 0
buffer_load_dwordx4 v13,s[40:43],s61 offen offset:0  lds
s_add_u32 m0,s88,40832
s_nop 0
buffer_load_dwordx4 v12,s[40:43],s61 offen offset:128  lds
s_add_u32 m0,s88,41856
s_nop 0
buffer_load_dwordx4 v13,s[40:43],s61 offen offset:128  lds
s_add_u32 m0,s88,49152
s_add_u32 s61,s82,393216
buffer_load_dwordx4 v12,s[40:43],s61 offen offset:0  lds
s_add_u32 m0,s88,50176
s_nop 0
buffer_load_dwordx4 v13,s[40:43],s61 offen offset:0  lds
s_add_u32 m0,s88,57216
s_nop 0
buffer_load_dwordx4 v12,s[40:43],s61 offen offset:128  lds
s_add_u32 m0,s88,58240
s_nop 0
buffer_load_dwordx4 v13,s[40:43],s61 offen offset:128  lds
s_add_u32 m0,s88,65536
s_add_u32 s61,s82,524288
buffer_load_dwordx4 v12,s[40:43],s61 offen offset:0  lds
s_add_u32 m0,s88,66560
s_nop 0
buffer_load_dwordx4 v13,s[40:43],s61 offen offset:0  lds
s_add_u32 m0,s88,73600
s_nop 0
buffer_load_dwordx4 v12,s[40:43],s61 offen offset:128  lds
s_add_u32 m0,s88,74624
s_nop 0
buffer_load_dwordx4 v13,s[40:43],s61 offen offset:128  lds
v_add_u32 v20,49152,v14
v_add_u32 v21,49152,v15
v_add_u32 v22,98304,v14
v_add_u32 v23,98304,v15
s_waitcnt vmcnt(16)
s_barrier
s_add_u32 m0,s88,81920
s_add_u32 s61,s82,655360
buffer_load_dwordx4 v12,s[40:43],s61 offen offset:0  lds
s_add_u32 m0,s88,82944
ds_read_b64_tr_b16 a[0:1],v14 offset:0
buffer_load_dwordx4 v13,s[40:43],s61 offen offset:0  lds
s_add_u32 m0,s88,89984
ds_read_b64_tr_b16 a[2:3],v14 offset:512
buffer_load_dwordx4 v12,s[40:43],s61 offen offset:128  lds
s_add_u32 m0,s88,91008
ds_read_b64_tr_b16 a[4:5],v15 offset:0
buffer_load_dwordx4 v13,s[40:43],s61 offen offset:128  lds
ds_read_b64_tr_b16 a[6:7],v15 offset:512
ds_read_b64_tr_b16 a[8:9],v14 offset:4096
ds_read_b64_tr_b16 a[10:11],v14 offset:4608
ds_read_b64_tr_b16 a[12:13],v15 offset:4096
ds_read_b64_tr_b16 a[14:15],v15 offset:4608
s_waitcnt vmcnt(16)
s_barrier
s_add_u32 m0,s88,98304
s_add_u32 s61,s82,786432
buffer_load_dwordx4 v12,s[40:43],s61 offen offset:0  lds
s_add_u32 m0,s88,99328
ds_read_b64_tr_b16 a[16:17],v14 offset:16384
buffer_load_dwordx4 v13,s[40:43],s61 offen offset:0  lds
s_add_u32 m0,s88,106368
ds_read_b64_tr_b16 a[18:19],v14 offset:16896
buffer_load_dwordx4 v12,s[40:43],s61 offen offset:128  lds
s_add_u32 m0,s88,107392
ds_read_b64_tr_b16 a[20:21],v15 offset:16384
buffer_load_dwordx4 v13,s[40:43],s61 offen offset:128  lds
ds_read_b64_tr_b16 a[22:23],v15 offset:16896
ds_read_b64_tr_b16 a[24:25],v14 offset:20480
ds_read_b64_tr_b16 a[26:27],v14 offset:20992
ds_read_b64_tr_b16 a[28:29],v15 offset:20480
ds_read_b64_tr_b16 a[30:31],v15 offset:20992
s_waitcnt lgkmcnt(8)
s_waitcnt vmcnt(16)
s_barrier
s_add_u32 m0,s88,0
s_add_u32 s61,s82,917504
buffer_load_dwordx4 v12,s[40:43],s61 offen offset:0  lds
s_add_u32 m0,s88,1024
ds_read_b64_tr_b16 a[32:33],v14 offset:32768
buffer_load_dwordx4 v13,s[40:43],s61 offen offset:0  lds
s_add_u32 m0,s88,8064
ds_read_b64_tr_b16 a[34:35],v14 offset:33280
buffer_load_dwordx4 v12,s[40:43],s61 offen offset:128  lds
s_add_u32 m0,s88,9088
ds_read_b64_tr_b16 a[36:37],v15 offset:32768
buffer_load_dwordx4 v13,s[40:43],s61 offen offset:128  lds
ds_read_b64_tr_b16 a[38:39],v15 offset:33280
ds_read_b64_tr_b16 a[40:41],v14 offset:36864
ds_read_b64_tr_b16 a[42:43],v14 offset:37376
ds_read_b64_tr_b16 a[44:45],v15 offset:36864
ds_read_b64_tr_b16 a[46:47],v15 offset:37376
s_waitcnt lgkmcnt(8)
s_waitcnt vmcnt(16)
s_barrier
s_add_u32 m0,s88,16384
s_add_u32 s61,s82,1048576
buffer_load_dwordx4 v12,s[40:43],s61 offen offset:0  lds
s_add_u32 m0,s88,17408
ds_read_b64_tr_b16 a[48:49],v20 offset:0
buffer_load_dwordx4 v13,s[40:43],s61 offen offset:0  lds
s_add_u32 m0,s88,24448
ds_read_b64_tr_b16 a[50:51],v20 offset:512
buffer_load_dwordx4 v12,s[40:43],s61 offen offset:128  lds
s_add_u32 m0,s88,25472
ds_read_b64_tr_b16 a[52:53],v21 offset:0
buffer_load_dwordx4 v13,s[40:43],s61 offen offset:128  lds
ds_read_b64_tr_b16 a[54:55],v21 offset:512
ds_read_b64_tr_b16 a[56:57],v20 offset:4096
ds_read_b64_tr_b16 a[58:59],v20 offset:4608
ds_read_b64_tr_b16 a[60:61],v21 offset:4096
ds_read_b64_tr_b16 a[62:63],v21 offset:4608
s_waitcnt lgkmcnt(8)
s_waitcnt vmcnt(16)
s_barrier
s_add_u32 m0,s88,32768
s_add_u32 s61,s82,1179648
buffer_load_dwordx4 v12,s[40:43],s61 offen offset:0  lds
s_add_u32 m0,s88,33792
ds_read_b64_tr_b16 a[64:65],v20 offset:16384
buffer_load_dwordx4 v13,s[40:43],s61 offen offset:0  lds
s_add_u32 m0,s88,40832
ds_read_b64_tr_b16 a[66:67],v20 offset:16896
buffer_load_dwordx4 v12,s[40:43],s61 offen offset:128  lds
s_add_u32 m0,s88,41856
ds_read_b64_tr_b16 a[68:69],v21 offset:16384
buffer_load_dwordx4 v13,s[40:43],s61 offen offset:128  lds
ds_read_b64_tr_b16 a[70:71],v21 offset:16896
ds_read_b64_tr_b16 a[72:73],v20 offset:20480
ds_read_b64_tr_b16 a[74:75],v20 offset:20992
ds_read_b64_tr_b16 a[76:77],v21 offset:20480
ds_read_b64_tr_b16 a[78:79],v21 offset:20992
s_waitcnt lgkmcnt(8)
s_waitcnt vmcnt(16)
s_barrier
s_add_u32 m0,s88,49152
s_add_u32 s61,s82,1310720
buffer_load_dwordx4 v12,s[40:43],s61 offen offset:0  lds)"
R"(
s_add_u32 m0,s88,50176
ds_read_b64_tr_b16 a[80:81],v20 offset:32768
buffer_load_dwordx4 v13,s[40:43],s61 offen offset:0  lds
s_add_u32 m0,s88,57216
ds_read_b64_tr_b16 a[82:83],v20 offset:33280
buffer_load_dwordx4 v12,s[40:43],s61 offen offset:128  lds
s_add_u32 m0,s88,58240
ds_read_b64_tr_b16 a[84:85],v21 offset:32768
buffer_load_dwordx4 v13,s[40:43],s61 offen offset:128  lds
ds_read_b64_tr_b16 a[86:87],v21 offset:33280
ds_read_b64_tr_b16 a[88:89],v20 offset:36864
ds_read_b64_tr_b16 a[90:91],v20 offset:37376
ds_read_b64_tr_b16 a[92:93],v21 offset:36864
ds_read_b64_tr_b16 a[94:95],v21 offset:37376
s_waitcnt lgkmcnt(8)
s_waitcnt vmcnt(16)
s_barrier
s_add_u32 m0,s88,65536
s_add_u32 s61,s82,1441792
buffer_load_dwordx4 v12,s[40:43],s61 offen offset:0  lds
s_add_u32 m0,s88,66560
ds_read_b64_tr_b16 a[96:97],v22 offset:0
buffer_load_dwordx4 v13,s[40:43],s61 offen offset:0  lds
s_add_u32 m0,s88,73600
ds_read_b64_tr_b16 a[98:99],v22 offset:512
buffer_load_dwordx4 v12,s[40:43],s61 offen offset:128  lds
s_add_u32 m0,s88,74624
ds_read_b64_tr_b16 a[100:101],v23 offset:0
buffer_load_dwordx4 v13,s[40:43],s61 offen offset:128  lds
ds_read_b64_tr_b16 a[102:103],v23 offset:512
ds_read_b64_tr_b16 a[104:105],v22 offset:4096
ds_read_b64_tr_b16 a[106:107],v22 offset:4608
ds_read_b64_tr_b16 a[108:109],v23 offset:4096
ds_read_b64_tr_b16 a[110:111],v23 offset:4608
s_waitcnt lgkmcnt(8)
s_waitcnt vmcnt(16)
s_barrier
s_add_u32 m0,s88,81920
s_add_u32 s61,s82,1572864
buffer_load_dwordx4 v12,s[40:43],s61 offen offset:0  lds
s_add_u32 m0,s88,82944
ds_read_b64_tr_b16 a[112:113],v14 offset:0
buffer_load_dwordx4 v13,s[40:43],s61 offen offset:0  lds
s_add_u32 m0,s88,89984
ds_read_b64_tr_b16 a[114:115],v14 offset:512
buffer_load_dwordx4 v12,s[40:43],s61 offen offset:128  lds
s_add_u32 m0,s88,91008
ds_read_b64_tr_b16 a[116:117],v15 offset:0
buffer_load_dwordx4 v13,s[40:43],s61 offen offset:128  lds
ds_read_b64_tr_b16 a[118:119],v15 offset:512
ds_read_b64_tr_b16 a[120:121],v14 offset:4096
ds_read_b64_tr_b16 a[122:123],v14 offset:4608
ds_read_b64_tr_b16 a[124:125],v15 offset:4096
ds_read_b64_tr_b16 a[126:127],v15 offset:4608
s_waitcnt lgkmcnt(8)
s_waitcnt vmcnt(16)
s_barrier
s_add_u32 m0,s88,98304
s_add_u32 s61,s82,1703936
buffer_load_dwordx4 v12,s[40:43],s61 offen offset:0  lds
s_add_u32 m0,s88,99328
ds_read_b64_tr_b16 a[128:129],v14 offset:16384
buffer_load_dwordx4 v13,s[40:43],s61 offen offset:0  lds
s_add_u32 m0,s88,106368
ds_read_b64_tr_b16 a[130:131],v14 offset:16896
buffer_load_dwordx4 v12,s[40:43],s61 offen offset:128  lds
s_add_u32 m0,s88,107392
ds_read_b64_tr_b16 a[132:133],v15 offset:16384
buffer_load_dwordx4 v13,s[40:43],s61 offen offset:128  lds
ds_read_b64_tr_b16 a[134:135],v15 offset:16896
ds_read_b64_tr_b16 a[136:137],v14 offset:20480
ds_read_b64_tr_b16 a[138:139],v14 offset:20992
ds_read_b64_tr_b16 a[140:141],v15 offset:20480
ds_read_b64_tr_b16 a[142:143],v15 offset:20992
s_waitcnt lgkmcnt(8)
s_waitcnt vmcnt(16)
s_barrier
s_add_u32 m0,s88,0
s_add_u32 s61,s82,1835008
buffer_load_dwordx4 v12,s[40:43],s61 offen offset:0  lds
s_add_u32 m0,s88,1024
ds_read_b64_tr_b16 a[144:145],v14 offset:32768
buffer_load_dwordx4 v13,s[40:43],s61 offen offset:0  lds
s_add_u32 m0,s88,8064
ds_read_b64_tr_b16 a[146:147],v14 offset:33280
buffer_load_dwordx4 v12,s[40:43],s61 offen offset:128  lds
s_add_u32 m0,s88,9088
ds_read_b64_tr_b16 a[148:149],v15 offset:32768
buffer_load_dwordx4 v13,s[40:43],s61 offen offset:128  lds
ds_read_b64_tr_b16 a[150:151],v15 offset:33280
ds_read_b64_tr_b16 a[152:153],v14 offset:36864
ds_read_b64_tr_b16 a[154:155],v14 offset:37376
ds_read_b64_tr_b16 a[156:157],v15 offset:36864
ds_read_b64_tr_b16 a[158:159],v15 offset:37376
s_waitcnt lgkmcnt(8)
s_waitcnt vmcnt(16)
s_barrier
s_add_u32 m0,s88,16384
s_add_u32 s61,s82,1966080
buffer_load_dwordx4 v12,s[40:43],s61 offen offset:0  lds
s_add_u32 m0,s88,17408
ds_read_b64_tr_b16 a[160:161],v20 offset:0
buffer_load_dwordx4 v13,s[40:43],s61 offen offset:0  lds
s_add_u32 m0,s88,24448
ds_read_b64_tr_b16 a[162:163],v20 offset:512
buffer_load_dwordx4 v12,s[40:43],s61 offen offset:128  lds
s_add_u32 m0,s88,25472
ds_read_b64_tr_b16 a[164:165],v21 offset:0
buffer_load_dwordx4 v13,s[40:43],s61 offen offset:128  lds
ds_read_b64_tr_b16 a[166:167],v21 offset:512
ds_read_b64_tr_b16 a[168:169],v20 offset:4096
ds_read_b64_tr_b16 a[170:171],v20 offset:4608
ds_read_b64_tr_b16 a[172:173],v21 offset:4096
ds_read_b64_tr_b16 a[174:175],v21 offset:4608
s_waitcnt lgkmcnt(8)
s_waitcnt vmcnt(16)
s_barrier
s_add_u32 m0,s88,32768
s_add_u32 s61,s82,2097152
buffer_load_dwordx4 v12,s[40:43],s61 offen offset:0  lds
s_add_u32 m0,s88,33792
ds_read_b64_tr_b16 a[176:177],v20 offset:16384
buffer_load_dwordx4 v13,s[40:43],s61 offen offset:0  lds
s_add_u32 m0,s88,40832
ds_read_b64_tr_b16 a[178:179],v20 offset:16896
buffer_load_dwordx4 v12,s[40:43],s61 offen offset:128  lds
s_add_u32 m0,s88,41856
ds_read_b64_tr_b16 a[180:181],v21 offset:16384
buffer_load_dwordx4 v13,s[40:43],s61 offen offset:128  lds
ds_read_b64_tr_b16 a[182:183],v21 offset:16896
ds_read_b64_tr_b16 a[184:185],v20 offset:20480
ds_read_b64_tr_b16 a[186:187],v20 offset:20992
ds_read_b64_tr_b16 a[188:189],v21 offset:20480
ds_read_b64_tr_b16 a[190:191],v21 offset:20992
s_waitcnt lgkmcnt(8)
s_waitcnt vmcnt(16)
s_barrier
s_add_u32 m0,s88,49152
s_add_u32 s61,s82,2228224
buffer_load_dwordx4 v12,s[40:43],s61 offen offset:0  lds
s_add_u32 m0,s88,50176
ds_read_b64_tr_b16 a[192:193],v20 offset:32768
buffer_load_dwordx4 v13,s[40:43],s61 offen offset:0  lds
s_add_u32 m0,s88,57216
ds_read_b64_tr_b16 a[194:195],v20 offset:33280
buffer_load_dwordx4 v12,s[40:43],s61 offen offset:128  lds
s_add_u32 m0,s88,58240
ds_read_b64_tr_b16 a[196:197],v21 offset:32768
buffer_load_dwordx4 v13,s[40:43],s61 offen offset:128  lds
ds_read_b64_tr_b16 a[198:199],v21 offset:33280
ds_read_b64_tr_b16 a[200:201],v20 offset:36864
ds_read_b64_tr_b16 a[202:203],v20 offset:37376
ds_read_b64_tr_b16 a[204:205],v21 offset:36864
ds_read_b64_tr_b16 a[206:207],v21 offset:37376
s_waitcnt lgkmcnt(8)
s_waitcnt vmcnt(16)
s_barrier
s_add_u32 m0,s88,65536
s_add_u32 s61,s82,2359296
buffer_load_dwordx4 v12,s[40:43],s61 offen offset:0  lds
s_add_u32 m0,s88,66560
ds_read_b64_tr_b16 a[208:209],v22 offset:0
buffer_load_dwordx4 v13,s[40:43],s61 offen offset:0  lds
s_add_u32 m0,s88,73600
ds_read_b64_tr_b16 a[210:211],v22 offset:512
buffer_load_dwordx4 v12,s[40:43],s61 offen offset:128  lds
s_add_u32 m0,s88,74624
ds_read_b64_tr_b16 a[212:213],v23 offset:0
buffer_load_dwordx4 v13,s[40:43],s61 offen offset:128  lds
ds_read_b64_tr_b16 a[214:215],v23 offset:512
ds_read_b64_tr_b16 a[216:217],v22 offset:4096
ds_read_b64_tr_b16 a[218:219],v22 offset:4608
ds_read_b64_tr_b16 a[220:221],v23 offset:4096
ds_read_b64_tr_b16 a[222:223],v23 offset:4608
s_waitcnt lgkmcnt(8)
s_waitcnt vmcnt(16)
s_barrier
s_add_u32 m0,s88,81920
s_add_u32 s61,s82,2490368
buffer_load_dwordx4 v12,s[40:43],s61 offen offset:0  lds
s_add_u32 m0,s88,82944
ds_read_b64_tr_b16 a[224:225],v14 offset:0
buffer_load_dwordx4 v13,s[40:43],s61 offen offset:0  lds
s_add_u32 m0,s88,89984
ds_read_b64_tr_b16 a[226:227],v14 offset:512
buffer_load_dwordx4 v12,s[40:43],s61 offen offset:128  lds
s_add_u32 m0,s88,91008
ds_read_b64_tr_b16 a[228:229],v15 offset:0
buffer_load_dwordx4 v13,s[40:43],s61 offen offset:128  lds
ds_read_b64_tr_b16 a[230:231],v15 offset:512
ds_read_b64_tr_b16 a[232:233],v14 offset:4096
ds_read_b64_tr_b16 a[234:235],v14 offset:4608
ds_read_b64_tr_b16 a[236:237],v15 offset:4096
ds_read_b64_tr_b16 a[238:239],v15 offset:4608
s_waitcnt lgkmcnt(8)
s_waitcnt vmcnt(16)
s_barrier
s_add_u32 m0,s88,98304
s_add_u32 s61,s82,2621440
buffer_load_dwordx4 v12,s[40:43],s61 offen offset:0  lds)"
R"(
s_add_u32 m0,s88,99328
ds_read_b64_tr_b16 a[240:241],v14 offset:16384
buffer_load_dwordx4 v13,s[40:43],s61 offen offset:0  lds
s_add_u32 m0,s88,106368
ds_read_b64_tr_b16 a[242:243],v14 offset:16896
buffer_load_dwordx4 v12,s[40:43],s61 offen offset:128  lds
s_add_u32 m0,s88,107392
ds_read_b64_tr_b16 a[244:245],v15 offset:16384
buffer_load_dwordx4 v13,s[40:43],s61 offen offset:128  lds
ds_read_b64_tr_b16 a[246:247],v15 offset:16896
ds_read_b64_tr_b16 a[248:249],v14 offset:20480
ds_read_b64_tr_b16 a[250:251],v14 offset:20992
ds_read_b64_tr_b16 a[252:253],v15 offset:20480
ds_read_b64_tr_b16 a[254:255],v15 offset:20992
s_waitcnt lgkmcnt(8)
s_waitcnt vmcnt(16)
s_barrier
s_add_u32 m0,s88,0
s_add_u32 s61,s82,2752512
buffer_load_dwordx4 v12,s[40:43],s61 offen offset:0  lds
s_add_u32 m0,s88,1024
ds_read_b64_tr_b16 v[108:109],v14 offset:32768
buffer_load_dwordx4 v13,s[40:43],s61 offen offset:0  lds
s_add_u32 m0,s88,8064
ds_read_b64_tr_b16 v[110:111],v14 offset:33280
buffer_load_dwordx4 v12,s[40:43],s61 offen offset:128  lds
s_add_u32 m0,s88,9088
ds_read_b64_tr_b16 v[112:113],v15 offset:32768
buffer_load_dwordx4 v13,s[40:43],s61 offen offset:128  lds
ds_read_b64_tr_b16 v[114:115],v15 offset:33280
ds_read_b64_tr_b16 v[116:117],v14 offset:36864
ds_read_b64_tr_b16 v[118:119],v14 offset:37376
ds_read_b64_tr_b16 v[120:121],v15 offset:36864
ds_read_b64_tr_b16 v[122:123],v15 offset:37376
s_waitcnt lgkmcnt(8)
s_waitcnt vmcnt(16)
s_barrier
s_add_u32 m0,s88,16384
s_add_u32 s61,s82,2883584
buffer_load_dwordx4 v12,s[40:43],s61 offen offset:0  lds
s_add_u32 m0,s88,17408
ds_read_b64_tr_b16 v[124:125],v20 offset:0
buffer_load_dwordx4 v13,s[40:43],s61 offen offset:0  lds
s_add_u32 m0,s88,24448
ds_read_b64_tr_b16 v[126:127],v20 offset:512
buffer_load_dwordx4 v12,s[40:43],s61 offen offset:128  lds
s_add_u32 m0,s88,25472
ds_read_b64_tr_b16 v[128:129],v21 offset:0
buffer_load_dwordx4 v13,s[40:43],s61 offen offset:128  lds
ds_read_b64_tr_b16 v[130:131],v21 offset:512
ds_read_b64_tr_b16 v[132:133],v20 offset:4096
ds_read_b64_tr_b16 v[134:135],v20 offset:4608
ds_read_b64_tr_b16 v[136:137],v21 offset:4096
ds_read_b64_tr_b16 v[138:139],v21 offset:4608
s_waitcnt lgkmcnt(8)
s_waitcnt vmcnt(16)
s_barrier
s_add_u32 m0,s88,32768
s_add_u32 s61,s82,3014656
buffer_load_dwordx4 v12,s[40:43],s61 offen offset:0  lds
s_add_u32 m0,s88,33792
ds_read_b64_tr_b16 v[140:141],v20 offset:16384
buffer_load_dwordx4 v13,s[40:43],s61 offen offset:0  lds
s_add_u32 m0,s88,40832
ds_read_b64_tr_b16 v[142:143],v20 offset:16896
buffer_load_dwordx4 v12,s[40:43],s61 offen offset:128  lds
s_add_u32 m0,s88,41856
ds_read_b64_tr_b16 v[144:145],v21 offset:16384
buffer_load_dwordx4 v13,s[40:43],s61 offen offset:128  lds
ds_read_b64_tr_b16 v[146:147],v21 offset:16896
ds_read_b64_tr_b16 v[148:149],v20 offset:20480
ds_read_b64_tr_b16 v[150:151],v20 offset:20992
ds_read_b64_tr_b16 v[152:153],v21 offset:20480
ds_read_b64_tr_b16 v[154:155],v21 offset:20992
s_waitcnt lgkmcnt(8)
s_waitcnt vmcnt(16)
s_barrier
s_add_u32 m0,s88,49152
s_add_u32 s61,s82,3145728
buffer_load_dwordx4 v12,s[40:43],s61 offen offset:0  lds
s_add_u32 m0,s88,50176
ds_read_b64_tr_b16 v[156:157],v20 offset:32768
buffer_load_dwordx4 v13,s[40:43],s61 offen offset:0  lds
s_add_u32 m0,s88,57216
ds_read_b64_tr_b16 v[158:159],v20 offset:33280
buffer_load_dwordx4 v12,s[40:43],s61 offen offset:128  lds
s_add_u32 m0,s88,58240
ds_read_b64_tr_b16 v[160:161],v21 offset:32768
buffer_load_dwordx4 v13,s[40:43],s61 offen offset:128  lds
ds_read_b64_tr_b16 v[162:163],v21 offset:33280
ds_read_b64_tr_b16 v[164:165],v20 offset:36864
ds_read_b64_tr_b16 v[166:167],v20 offset:37376
ds_read_b64_tr_b16 v[168:169],v21 offset:36864
ds_read_b64_tr_b16 v[170:171],v21 offset:37376
s_waitcnt lgkmcnt(8)
s_waitcnt vmcnt(16)
s_barrier
s_add_u32 m0,s88,65536
s_add_u32 s61,s82,3276800
buffer_load_dwordx4 v12,s[40:43],s61 offen offset:0  lds
s_add_u32 m0,s88,66560
ds_read_b64_tr_b16 v[172:173],v22 offset:0
buffer_load_dwordx4 v13,s[40:43],s61 offen offset:0  lds
s_add_u32 m0,s88,73600
ds_read_b64_tr_b16 v[174:175],v22 offset:512
buffer_load_dwordx4 v12,s[40:43],s61 offen offset:128  lds
s_add_u32 m0,s88,74624
ds_read_b64_tr_b16 v[176:177],v23 offset:0
buffer_load_dwordx4 v13,s[40:43],s61 offen offset:128  lds
ds_read_b64_tr_b16 v[178:179],v23 offset:512
ds_read_b64_tr_b16 v[180:181],v22 offset:4096
ds_read_b64_tr_b16 v[182:183],v22 offset:4608
ds_read_b64_tr_b16 v[184:185],v23 offset:4096
ds_read_b64_tr_b16 v[186:187],v23 offset:4608
s_waitcnt lgkmcnt(8)
s_waitcnt vmcnt(16)
s_barrier
s_add_u32 m0,s88,81920
s_add_u32 s61,s82,3407872
buffer_load_dwordx4 v12,s[40:43],s61 offen offset:0  lds
s_add_u32 m0,s88,82944
ds_read_b64_tr_b16 v[188:189],v14 offset:0
buffer_load_dwordx4 v13,s[40:43],s61 offen offset:0  lds
s_add_u32 m0,s88,89984
ds_read_b64_tr_b16 v[190:191],v14 offset:512
buffer_load_dwordx4 v12,s[40:43],s61 offen offset:128  lds
s_add_u32 m0,s88,91008
ds_read_b64_tr_b16 v[192:193],v15 offset:0
buffer_load_dwordx4 v13,s[40:43],s61 offen offset:128  lds
ds_read_b64_tr_b16 v[194:195],v15 offset:512
ds_read_b64_tr_b16 v[196:197],v14 offset:4096
ds_read_b64_tr_b16 v[198:199],v14 offset:4608
ds_read_b64_tr_b16 v[200:201],v15 offset:4096
ds_read_b64_tr_b16 v[202:203],v15 offset:4608
s_waitcnt lgkmcnt(8)
s_waitcnt vmcnt(16)
s_barrier
s_add_u32 m0,s88,98304
s_add_u32 s61,s82,3538944
buffer_load_dwordx4 v12,s[40:43],s61 offen offset:0  lds
s_add_u32 m0,s88,99328
ds_read_b64_tr_b16 v[204:205],v14 offset:16384
buffer_load_dwordx4 v13,s[40:43],s61 offen offset:0  lds
s_add_u32 m0,s88,106368
ds_read_b64_tr_b16 v[206:207],v14 offset:16896
buffer_load_dwordx4 v12,s[40:43],s61 offen offset:128  lds
s_add_u32 m0,s88,107392
ds_read_b64_tr_b16 v[208:209],v15 offset:16384
buffer_load_dwordx4 v13,s[40:43],s61 offen offset:128  lds
ds_read_b64_tr_b16 v[210:211],v15 offset:16896
ds_read_b64_tr_b16 v[212:213],v14 offset:20480
ds_read_b64_tr_b16 v[214:215],v14 offset:20992
ds_read_b64_tr_b16 v[32:33],v15 offset:20480
ds_read_b64_tr_b16 v[34:35],v15 offset:20992
s_waitcnt lgkmcnt(8)
s_waitcnt vmcnt(16)
s_barrier
ds_read_b64_tr_b16 v[216:217],v14 offset:32768
ds_read_b64_tr_b16 v[218:219],v14 offset:33280
ds_read_b64_tr_b16 v[36:37],v15 offset:32768
ds_read_b64_tr_b16 v[38:39],v15 offset:33280
ds_read_b64_tr_b16 v[220:221],v14 offset:36864
ds_read_b64_tr_b16 v[222:223],v14 offset:37376
ds_read_b64_tr_b16 v[40:41],v15 offset:36864
ds_read_b64_tr_b16 v[42:43],v15 offset:37376
s_waitcnt lgkmcnt(8)
ds_write_b128 v9,v[32:35] offset:0
s_waitcnt vmcnt(12)
s_barrier
ds_read_b64_tr_b16 v[224:225],v20 offset:0
ds_read_b64_tr_b16 v[226:227],v20 offset:512
ds_read_b64_tr_b16 v[28:29],v21 offset:0
ds_read_b64_tr_b16 v[30:31],v21 offset:512
ds_read_b64_tr_b16 v[228:229],v20 offset:4096
ds_read_b64_tr_b16 v[230:231],v20 offset:4608
ds_read_b64_tr_b16 v[32:33],v21 offset:4096
ds_read_b64_tr_b16 v[34:35],v21 offset:4608
s_waitcnt lgkmcnt(8)
ds_write_b128 v9,v[36:39] offset:1024
ds_write_b128 v9,v[40:43] offset:2048
s_waitcnt vmcnt(8)
s_barrier
ds_read_b64_tr_b16 v[232:233],v20 offset:16384
ds_read_b64_tr_b16 v[234:235],v20 offset:16896
ds_read_b64_tr_b16 v[36:37],v21 offset:16384
ds_read_b64_tr_b16 v[38:39],v21 offset:16896
ds_read_b64_tr_b16 v[236:237],v20 offset:20480
ds_read_b64_tr_b16 v[238:239],v20 offset:20992
ds_read_b64_tr_b16 v[40:41],v21 offset:20480
ds_read_b64_tr_b16 v[42:43],v21 offset:20992
s_waitcnt lgkmcnt(8)
ds_write_b128 v9,v[28:31] offset:3072
ds_write_b128 v9,v[32:35] offset:4096
s_waitcnt vmcnt(4)
s_barrier
ds_read_b64_tr_b16 v[240:241],v20 offset:32768
ds_read_b64_tr_b16 v[242:243],v20 offset:33280
ds_read_b64_tr_b16 v[28:29],v21 offset:32768
ds_read_b64_tr_b16 v[30:31],v21 offset:33280
ds_read_b64_tr_b16 v[244:245],v20 offset:36864)"
R"(
ds_read_b64_tr_b16 v[246:247],v20 offset:37376
ds_read_b64_tr_b16 v[32:33],v21 offset:36864
ds_read_b64_tr_b16 v[34:35],v21 offset:37376
s_waitcnt lgkmcnt(8)
ds_write_b128 v9,v[36:39] offset:5120
ds_write_b128 v9,v[40:43] offset:6144
s_waitcnt vmcnt(0)
s_barrier
ds_read_b64_tr_b16 v[248:249],v22 offset:0
ds_read_b64_tr_b16 v[250:251],v22 offset:512
ds_read_b64_tr_b16 v[36:37],v23 offset:0
ds_read_b64_tr_b16 v[38:39],v23 offset:512
ds_read_b64_tr_b16 v[252:253],v22 offset:4096
ds_read_b64_tr_b16 v[254:255],v22 offset:4608
ds_read_b64_tr_b16 v[40:41],v23 offset:4096
ds_read_b64_tr_b16 v[42:43],v23 offset:4608
s_waitcnt lgkmcnt(8)
ds_write_b128 v9,v[28:31] offset:7168
ds_write_b128 v9,v[32:35] offset:8192
s_waitcnt lgkmcnt(0)
ds_write_b128 v9,v[36:39] offset:9216
ds_write_b128 v9,v[40:43] offset:10240
s_waitcnt lgkmcnt(0)
s_barrier
v_readfirstlane_b32 s60,v5
s_nop 3
s_add_u32 s60,s60,1
s_cmp_eq_u32 s60,0
s_cselect_b32 s60,1,s60
Lg4_init_%=:
v_mov_b32 v5,s60
s_mov_b32 s59,536870912
s_add_u32 s57,s56,131072
s_mov_b32 s79,0
s_mov_b32 s75,0x3a800000
s_mov_b32 s76,0xc0135761
s_mov_b32 s77,0x3d372713
s_mov_b32 s44,s59
s_mov_b32 s45,s59
s_mov_b32 s46,s59
s_mov_b32 s47,s59
s_mov_b32 s48,s83
s_add_u32 s49,s48,s55
s_cmp_eq_u32 s49,s56
s_cselect_b32 s49,s57,s49
s_cmp_eq_u32 s49,s58
s_cselect_b32 s49,s59,s49
s_add_u32 s50,s49,s55
s_cmp_eq_u32 s50,s56
s_cselect_b32 s50,s57,s50
s_cmp_eq_u32 s50,s58
s_cselect_b32 s50,s59,s50
s_add_u32 s51,s50,s55
s_cmp_eq_u32 s51,s56
s_cselect_b32 s51,s57,s51
s_cmp_eq_u32 s51,s58
s_cselect_b32 s51,s59,s51
s_add_u32 s52,s51,s55
s_cmp_eq_u32 s52,s56
s_cselect_b32 s52,s57,s52
s_cmp_eq_u32 s52,s58
s_cselect_b32 s52,s59,s52
s_mul_i32 s88,s54,7168
v_add_u32 v2,57344,v1
s_lshl_b32 s78,s54,8
s_add_u32 s78,s78,159744
s_getpc_b64 s[90:91]
Lg4_pc_%=:
s_mov_b32 s68,Lg4_d1w0_%=-Lg4_pc_%=
s_cmp_eq_u32 s54,1
s_cselect_b32 s68,Lg4_d1w1_%=-Lg4_pc_%=,s68
s_cmp_eq_u32 s54,2
s_cselect_b32 s68,Lg4_d1w2_%=-Lg4_pc_%=,s68
s_cmp_eq_u32 s54,3
s_cselect_b32 s68,Lg4_d1w3_%=-Lg4_pc_%=,s68
s_add_u32 s92,s90,s68
s_addc_u32 s93,s91,0
s_mov_b32 s68,Lg4_d0w0_%=-Lg4_pc_%=
s_cmp_eq_u32 s54,1
s_cselect_b32 s68,Lg4_d0w1_%=-Lg4_pc_%=,s68
s_cmp_eq_u32 s54,2
s_cselect_b32 s68,Lg4_d0w2_%=-Lg4_pc_%=,s68
s_cmp_eq_u32 s54,3
s_cselect_b32 s68,Lg4_d0w3_%=-Lg4_pc_%=,s68
s_add_u32 s90,s90,s68
s_addc_u32 s91,s91,0
s_mov_b32 s66,s59
s_mov_b32 s65,0
s_mov_b32 s62,s63
s_branch Lg4_slow0_%=
Lg4_d0w0_%=:
v_mfma_f32_16x16x32_bf16 v[12:15],a[0:3],v[28:31],0
s_add_u32 m0,s88,57344
ds_read_b128 v[56:59],v1 offset:7168
v_lshlrev_b32 v60,16,v72
v_mfma_f32_16x16x32_bf16 v[16:19],a[4:7],v[28:31],0
buffer_load_dwordx4 v0,s[36:39],s61 offen offset:0 sc1 lds
v_mfma_f32_16x16x32_bf16 v[12:15],a[8:11],v[32:35],v[12:15]
s_max_u32 s73,s47,s65
ds_read_b128 v[28:31],v1 offset:8192
v_and_b32 v61,0xffff0000,v72
v_mfma_f32_16x16x32_bf16 v[16:19],a[12:15],v[32:35],v[16:19]
s_waitcnt lgkmcnt(5)
v_lshlrev_b32 v62,16,v73
v_and_b32 v63,0xffff0000,v73
v_mfma_f32_16x16x32_bf16 v[12:15],a[16:19],v[36:39],v[12:15]
s_mov_b32 s65,0
ds_read_b128 v[32:35],v1 offset:9216
v_lshlrev_b32 v64,16,v74
v_mfma_f32_16x16x32_bf16 v[16:19],a[20:23],v[36:39],v[16:19]
buffer_load_dwordx4 v0,s[36:39],s61 offen offset:1024 sc1 lds
v_mfma_f32_16x16x32_bf16 v[12:15],a[24:27],v[40:43],v[12:15]
s_add_u32 s53,s52,s55
ds_read_b128 v[36:39],v1 offset:10240
v_and_b32 v65,0xffff0000,v74
v_mfma_f32_16x16x32_bf16 v[16:19],a[28:31],v[40:43],v[16:19]
s_waitcnt lgkmcnt(5)
v_lshlrev_b32 v66,16,v75
v_and_b32 v67,0xffff0000,v75
v_mfma_f32_16x16x32_bf16 v[12:15],a[32:35],v[44:47],v[12:15]
s_cmp_eq_u32 s53,s56
ds_read_b128 v[40:43],v1 offset:11264
v_add_f32 v20,v20,v60
v_mfma_f32_16x16x32_bf16 v[16:19],a[36:39],v[44:47],v[16:19]
buffer_load_dwordx4 v0,s[36:39],s61 offen offset:2048 sc1 lds
v_mfma_f32_16x16x32_bf16 v[12:15],a[40:43],v[48:51],v[12:15]
s_cselect_b32 s53,s57,s53
ds_read_b128 v[44:47],v1 offset:12288
v_add_f32 v21,v21,v61
v_mfma_f32_16x16x32_bf16 v[16:19],a[44:47],v[48:51],v[16:19]
s_waitcnt lgkmcnt(5)
v_add_f32 v22,v22,v62
v_add_f32 v23,v23,v63
v_mfma_f32_16x16x32_bf16 v[12:15],a[48:51],v[52:55],v[12:15]
s_cmp_eq_u32 s53,s58
ds_read_b128 v[48:51],v1 offset:13312
v_add_f32 v24,v24,v64
v_mfma_f32_16x16x32_bf16 v[16:19],a[52:55],v[52:55],v[16:19]
buffer_load_dwordx4 v0,s[36:39],s61 offen offset:3072 sc1 lds
v_mfma_f32_16x16x32_bf16 v[12:15],a[56:59],v[56:59],v[12:15]
s_cselect_b32 s53,s59,s53
ds_read_b128 v[52:55],v1 offset:14336
v_add_f32 v25,v25,v65
v_mfma_f32_16x16x32_bf16 v[16:19],a[60:63],v[56:59],v[16:19]
s_waitcnt lgkmcnt(5)
v_add_f32 v26,v26,v66
v_add_f32 v27,v27,v67
v_mfma_f32_16x16x32_bf16 v[12:15],a[64:67],v[28:31],v[12:15]
s_add_u32 s61,s61,4096
v_cvt_pk_bf16_f32 v60,v20,v21
v_cvt_pk_bf16_f32 v61,v22,v23
v_cvt_pk_bf16_f32 v62,v24,v25
v_cvt_pk_bf16_f32 v63,v26,v27
v_mfma_f32_16x16x32_bf16 v[16:19],a[68:71],v[28:31],v[16:19]
s_add_u32 m0,s88,61440
ds_read_b128 v[56:59],v1 offset:15360
buffer_store_dwordx4 v[60:63],v3,s[36:39],s73 offen offset:0 sc0 sc1
s_cmp_ge_u32 s66,s59
v_mfma_f32_16x16x32_bf16 v[12:15],a[72:75],v[32:35],v[12:15]
s_cselect_b32 s86,s49,s50
ds_read_b128 v[28:31],v1 offset:16384
s_mul_i32 s68,s86,5
v_mfma_f32_16x16x32_bf16 v[16:19],a[76:79],v[32:35],v[16:19]
s_waitcnt lgkmcnt(5)
s_lshr_b32 s68,s68,6
v_mfma_f32_16x16x32_bf16 v[12:15],a[80:83],v[36:39],v[12:15]
ds_read_b128 v[32:35],v1 offset:17408
v_mfma_f32_16x16x32_bf16 v[16:19],a[84:87],v[36:39],v[16:19]
buffer_load_dwordx4 v0,s[36:39],s61 offen offset:0 sc1 lds
v_mfma_f32_16x16x32_bf16 v[12:15],a[88:91],v[40:43],v[12:15]
ds_read_b128 v[36:39],v1 offset:18432
v_mfma_f32_16x16x32_bf16 v[16:19],a[92:95],v[40:43],v[16:19]
s_waitcnt lgkmcnt(5)
v_mfma_f32_16x16x32_bf16 v[12:15],a[96:99],v[44:47],v[12:15]
ds_read_b128 v[40:43],v1 offset:19456
v_mfma_f32_16x16x32_bf16 v[16:19],a[100:103],v[44:47],v[16:19]
buffer_load_dwordx4 v0,s[36:39],s61 offen offset:1024 sc1 lds
v_mfma_f32_16x16x32_bf16 v[12:15],a[104:107],v[48:51],v[12:15]
ds_read_b128 v[44:47],v1 offset:20480
v_mfma_f32_16x16x32_bf16 v[16:19],a[108:111],v[48:51],v[16:19]
s_waitcnt lgkmcnt(5)
v_mfma_f32_16x16x32_bf16 v[12:15],a[112:115],v[52:55],v[12:15]
ds_read_b128 v[48:51],v1 offset:21504
v_mfma_f32_16x16x32_bf16 v[16:19],a[116:119],v[52:55],v[16:19]
buffer_load_dwordx4 v0,s[36:39],s61 offen offset:2048 sc1 lds
v_mfma_f32_16x16x32_bf16 v[12:15],a[120:123],v[56:59],v[12:15]
s_add_u32 m0,s78,3072
ds_read_b128 v[52:55],v1 offset:22528
s_add_u32 s61,s61,24576
v_mfma_f32_16x16x32_bf16 v[16:19],a[124:127],v[56:59],v[16:19]
s_waitcnt lgkmcnt(5)
v_mfma_f32_16x16x32_bf16 v[12:15],a[128:131],v[28:31],v[12:15]
ds_read_b128 v[56:59],v1 offset:23552
v_mfma_f32_16x16x32_bf16 v[16:19],a[132:135],v[28:31],v[16:19]
buffer_load_dwordx4 v[68:71],v8,s[36:39],s48 offen offset:0 sc1
v_mfma_f32_16x16x32_bf16 v[12:15],a[136:139],v[32:35],v[12:15]
ds_read_b128 v[28:31],v1 offset:24576
v_mfma_f32_16x16x32_bf16 v[16:19],a[140:143],v[32:35],v[16:19]
s_waitcnt lgkmcnt(5)
v_mfma_f32_16x16x32_bf16 v[12:15],a[144:147],v[36:39],v[12:15])"
R"(
ds_read_b128 v[32:35],v1 offset:25600
v_mfma_f32_16x16x32_bf16 v[16:19],a[148:151],v[36:39],v[16:19]
v_mfma_f32_16x16x32_bf16 v[12:15],a[152:155],v[40:43],v[12:15]
ds_read_b128 v[36:39],v1 offset:26624
v_mfma_f32_16x16x32_bf16 v[16:19],a[156:159],v[40:43],v[16:19]
s_waitcnt lgkmcnt(5)
v_mfma_f32_16x16x32_bf16 v[12:15],a[160:163],v[44:47],v[12:15]
s_waitcnt vmcnt(9)
ds_read_b128 v[40:43],v1 offset:27648
v_mfma_f32_16x16x32_bf16 v[16:19],a[164:167],v[44:47],v[16:19]
s_barrier
v_mfma_f32_16x16x32_bf16 v[12:15],a[168:171],v[48:51],v[12:15]
ds_read_b128 v[44:47],v1 offset:28672
v_mfma_f32_16x16x32_bf16 v[16:19],a[172:175],v[48:51],v[16:19]
s_waitcnt lgkmcnt(5)
buffer_load_dword v6,s[36:39],s68 offen offset:0 sc1 lds
v_mfma_f32_16x16x32_bf16 v[12:15],a[176:179],v[52:55],v[12:15]
s_add_u32 m0,s88,86016
ds_read_b128 v[48:51],v1 offset:29696
v_mfma_f32_16x16x32_bf16 v[16:19],a[180:183],v[52:55],v[16:19]
v_mfma_f32_16x16x32_bf16 v[12:15],a[184:187],v[56:59],v[12:15]
ds_read_b128 v[52:55],v1 offset:30720
v_mfma_f32_16x16x32_bf16 v[16:19],a[188:191],v[56:59],v[16:19]
s_waitcnt lgkmcnt(5)
buffer_load_dwordx4 v0,s[36:39],s61 offen offset:0 sc1 lds
v_mfma_f32_16x16x32_bf16 v[12:15],a[192:195],v[28:31],v[12:15]
ds_read_b128 v[56:59],v1 offset:31744
v_mfma_f32_16x16x32_bf16 v[16:19],a[196:199],v[28:31],v[16:19]
v_mfma_f32_16x16x32_bf16 v[12:15],a[200:203],v[32:35],v[12:15]
ds_read_b128 v[28:31],v1 offset:32768
v_mfma_f32_16x16x32_bf16 v[16:19],a[204:207],v[32:35],v[16:19]
s_waitcnt lgkmcnt(5)
buffer_load_dwordx4 v0,s[36:39],s61 offen offset:1024 sc1 lds
v_mfma_f32_16x16x32_bf16 v[12:15],a[208:211],v[36:39],v[12:15]
ds_read_b128 v[32:35],v1 offset:33792
v_mfma_f32_16x16x32_bf16 v[16:19],a[212:215],v[36:39],v[16:19]
v_mfma_f32_16x16x32_bf16 v[12:15],a[216:219],v[40:43],v[12:15]
ds_read_b128 v[36:39],v1 offset:34816
v_mfma_f32_16x16x32_bf16 v[16:19],a[220:223],v[40:43],v[16:19]
s_waitcnt lgkmcnt(5)
buffer_load_dwordx4 v0,s[36:39],s61 offen offset:2048 sc1 lds
v_mfma_f32_16x16x32_bf16 v[12:15],a[224:227],v[44:47],v[12:15]
ds_read_b128 v[40:43],v1 offset:35840
v_mfma_f32_16x16x32_bf16 v[16:19],a[228:231],v[44:47],v[16:19]
v_mfma_f32_16x16x32_bf16 v[12:15],a[232:235],v[48:51],v[12:15]
ds_read_b128 v[44:47],v1 offset:36864
v_mfma_f32_16x16x32_bf16 v[16:19],a[236:239],v[48:51],v[16:19]
s_waitcnt lgkmcnt(5)
buffer_load_dwordx4 v0,s[36:39],s61 offen offset:3072 sc1 lds
v_mfma_f32_16x16x32_bf16 v[12:15],a[240:243],v[52:55],v[12:15]
s_add_u32 s61,s61,4096
ds_read_b128 v[48:51],v1 offset:37888
s_add_u32 m0,s88,90112
v_mfma_f32_16x16x32_bf16 v[16:19],a[244:247],v[52:55],v[16:19]
v_mfma_f32_16x16x32_bf16 v[12:15],a[248:251],v[56:59],v[12:15]
ds_read_b128 v[52:55],v1 offset:38912
v_mfma_f32_16x16x32_bf16 v[16:19],a[252:255],v[56:59],v[16:19]
s_waitcnt lgkmcnt(5)
buffer_load_dwordx4 v0,s[36:39],s61 offen offset:0 sc1 lds
v_mfma_f32_16x16x32_bf16 v[12:15],v[108:111],v[28:31],v[12:15]
ds_read_b128 v[56:59],v1 offset:39936
v_mfma_f32_16x16x32_bf16 v[16:19],v[112:115],v[28:31],v[16:19]
v_mfma_f32_16x16x32_bf16 v[12:15],v[116:119],v[32:35],v[12:15]
ds_read_b128 v[28:31],v1 offset:40960
v_mfma_f32_16x16x32_bf16 v[16:19],v[120:123],v[32:35],v[16:19]
s_waitcnt lgkmcnt(5)
buffer_load_dwordx4 v0,s[36:39],s61 offen offset:1024 sc1 lds
v_mfma_f32_16x16x32_bf16 v[12:15],v[124:127],v[36:39],v[12:15]
ds_read_b128 v[32:35],v1 offset:41984
v_mfma_f32_16x16x32_bf16 v[16:19],v[128:131],v[36:39],v[16:19]
v_mfma_f32_16x16x32_bf16 v[12:15],v[132:135],v[40:43],v[12:15]
ds_read_b128 v[36:39],v1 offset:43008
v_mfma_f32_16x16x32_bf16 v[16:19],v[136:139],v[40:43],v[16:19]
s_waitcnt lgkmcnt(5)
buffer_load_dwordx4 v0,s[36:39],s61 offen offset:2048 sc1 lds
v_mfma_f32_16x16x32_bf16 v[12:15],v[140:143],v[44:47],v[12:15]
ds_read_b128 v[40:43],v1 offset:44032
v_mfma_f32_16x16x32_bf16 v[16:19],v[144:147],v[44:47],v[16:19]
v_mfma_f32_16x16x32_bf16 v[12:15],v[148:151],v[48:51],v[12:15]
ds_read_b128 v[44:47],v1 offset:45056
v_mfma_f32_16x16x32_bf16 v[16:19],v[152:155],v[48:51],v[16:19]
s_waitcnt lgkmcnt(5)
v_mfma_f32_16x16x32_bf16 v[12:15],v[156:159],v[52:55],v[12:15]
ds_read_b128 v[48:51],v1 offset:46080
v_mfma_f32_16x16x32_bf16 v[16:19],v[160:163],v[52:55],v[16:19]
v_mfma_f32_16x16x32_bf16 v[12:15],v[164:167],v[56:59],v[12:15]
ds_read_b128 v[52:55],v1 offset:47104
v_mfma_f32_16x16x32_bf16 v[16:19],v[168:171],v[56:59],v[16:19]
s_waitcnt lgkmcnt(5)
v_mfma_f32_16x16x32_bf16 v[12:15],v[172:175],v[28:31],v[12:15]
ds_read_b128 v[56:59],v1 offset:48128
v_mfma_f32_16x16x32_bf16 v[16:19],v[176:179],v[28:31],v[16:19]
ds_read_b128 v[88:91],v9 offset:0
v_mfma_f32_16x16x32_bf16 v[12:15],v[180:183],v[32:35],v[12:15]
ds_read_b128 v[28:31],v1 offset:49152
v_mfma_f32_16x16x32_bf16 v[16:19],v[184:187],v[32:35],v[16:19]
s_waitcnt lgkmcnt(6)
ds_read_b128 v[92:95],v9 offset:1024
v_mfma_f32_16x16x32_bf16 v[12:15],v[188:191],v[36:39],v[12:15]
ds_read_b128 v[32:35],v1 offset:50176
v_mfma_f32_16x16x32_bf16 v[16:19],v[192:195],v[36:39],v[16:19]
ds_read_b128 v[96:99],v9 offset:2048
v_mfma_f32_16x16x32_bf16 v[12:15],v[196:199],v[40:43],v[12:15]
ds_read_b128 v[36:39],v1 offset:51200
v_mfma_f32_16x16x32_bf16 v[16:19],v[200:203],v[40:43],v[16:19]
s_waitcnt lgkmcnt(5)
ds_read_b128 v[76:79],v9 offset:3072
v_mfma_f32_16x16x32_bf16 v[12:15],v[204:207],v[44:47],v[12:15]
ds_read_b128 v[40:43],v1 offset:52224
v_mfma_f32_16x16x32_bf16 v[16:19],v[208:211],v[44:47],v[16:19]
ds_read_b128 v[80:83],v9 offset:4096
v_mfma_f32_16x16x32_bf16 v[12:15],v[212:215],v[48:51],v[12:15]
ds_read_b128 v[44:47],v1 offset:53248
v_mfma_f32_16x16x32_bf16 v[16:19],v[88:91],v[48:51],v[16:19]
s_waitcnt lgkmcnt(5)
ds_read_b128 v[84:87],v9 offset:5120
v_mfma_f32_16x16x32_bf16 v[12:15],v[216:219],v[52:55],v[12:15]
ds_read_b128 v[48:51],v1 offset:54272
v_mfma_f32_16x16x32_bf16 v[16:19],v[92:95],v[52:55],v[16:19]
ds_read_b128 v[88:91],v9 offset:6144
v_mfma_f32_16x16x32_bf16 v[12:15],v[220:223],v[56:59],v[12:15]
ds_read_b128 v[52:55],v1 offset:55296
v_mfma_f32_16x16x32_bf16 v[16:19],v[96:99],v[56:59],v[16:19]
s_waitcnt lgkmcnt(5)
ds_read_b128 v[92:95],v9 offset:7168
v_mfma_f32_16x16x32_bf16 v[12:15],v[224:227],v[28:31],v[12:15]
s_waitcnt vmcnt(7)
ds_read_b128 v[56:59],v1 offset:56320
ds_read_b128 v[96:99],v9 offset:8192
v_mfma_f32_16x16x32_bf16 v[16:19],v[76:79],v[28:31],v[16:19]
s_barrier
v_mfma_f32_16x16x32_bf16 v[12:15],v[228:231],v[32:35],v[12:15]
ds_read_b128 v[28:31],v2 offset:0
ds_read_b32 v10,v7 offset:3072
v_mfma_f32_16x16x32_bf16 v[16:19],v[80:83],v[32:35],v[16:19]
s_waitcnt lgkmcnt(6)
ds_read_b128 v[76:79],v9 offset:9216
v_mfma_f32_16x16x32_bf16 v[12:15],v[232:235],v[36:39],v[12:15]
ds_read_b128 v[32:35],v2 offset:1024
v_mfma_f32_16x16x32_bf16 v[16:19],v[84:87],v[36:39],v[16:19]
ds_read_b128 v[80:83],v9 offset:10240
v_mfma_f32_16x16x32_bf16 v[12:15],v[236:239],v[40:43],v[12:15]
s_waitcnt lgkmcnt(3)
ds_read_b128 v[36:39],v2 offset:2048
v_mfma_f32_16x16x32_bf16 v[16:19],v[88:91],v[40:43],v[16:19]
v_cmp_eq_u32 vcc,s60,v10
v_mfma_f32_16x16x32_bf16 v[12:15],v[240:243],v[44:47],v[12:15]
s_cmp_eq_u32 vcc_lo,-1
ds_read_b128 v[40:43],v2 offset:3072
s_cselect_b32 s67,s86,s59
v_mfma_f32_16x16x32_bf16 v[16:19],v[92:95],v[44:47],v[16:19]
s_mul_i32 s61,s67,7
s_lshr_b32 s61,s61,1
s_cmp_ge_u32 s66,s59
v_mfma_f32_16x16x32_bf16 v[12:15],v[244:247],v[48:51],v[12:15]
ds_read_b128 v[44:47],v2 offset:4096
v_mfma_f32_16x16x32_bf16 v[16:19],v[96:99],v[48:51],v[16:19]
s_waitcnt lgkmcnt(3)
v_mfma_f32_16x16x32_bf16 v[12:15],v[248:251],v[52:55],v[12:15]
ds_read_b128 v[48:51],v2 offset:5120
v_mfma_f32_16x16x32_bf16 v[16:19],v[76:79],v[52:55],v[16:19]
v_mfma_f32_16x16x32_bf16 v[12:15],v[252:255],v[56:59],v[12:15]
ds_read_b128 v[52:55],v2 offset:6144
v_mfma_f32_16x16x32_bf16 v[16:19],v[80:83],v[56:59],v[16:19]
s_cbranch_scc1 Lg4_slow1_%=
Lg4_d1w0_%=:
v_mfma_f32_16x16x32_bf16 v[20:23],a[0:3],v[28:31],0
s_add_u32 m0,s88,0
ds_read_b128 v[56:59],v2 offset:7168
v_lshlrev_b32 v60,16,v68
v_mfma_f32_16x16x32_bf16 v[24:27],a[4:7],v[28:31],0
buffer_load_dwordx4 v0,s[36:39],s61 offen offset:0 sc1 lds
v_mfma_f32_16x16x32_bf16 v[20:23],a[8:11],v[32:35],v[20:23]
s_max_u32 s73,s48,s65
ds_read_b128 v[28:31],v2 offset:8192
v_and_b32 v61,0xffff0000,v68
v_mfma_f32_16x16x32_bf16 v[24:27],a[12:15],v[32:35],v[24:27]
s_waitcnt lgkmcnt(5)
v_lshlrev_b32 v62,16,v69
v_and_b32 v63,0xffff0000,v69
v_mfma_f32_16x16x32_bf16 v[20:23],a[16:19],v[36:39],v[20:23]
s_mov_b32 s65,0
ds_read_b128 v[32:35],v2 offset:9216
v_lshlrev_b32 v64,16,v70
v_mfma_f32_16x16x32_bf16 v[24:27],a[20:23],v[36:39],v[24:27]
buffer_load_dwordx4 v0,s[36:39],s61 offen offset:1024 sc1 lds
v_mfma_f32_16x16x32_bf16 v[20:23],a[24:27],v[40:43],v[20:23]
s_add_u32 s74,s53,s55
ds_read_b128 v[36:39],v2 offset:10240
v_and_b32 v65,0xffff0000,v70
v_mfma_f32_16x16x32_bf16 v[24:27],a[28:31],v[40:43],v[24:27]
s_waitcnt lgkmcnt(5)
v_lshlrev_b32 v66,16,v71
v_and_b32 v67,0xffff0000,v71
v_mfma_f32_16x16x32_bf16 v[20:23],a[32:35],v[44:47],v[20:23]
s_cmp_eq_u32 s74,s56
ds_read_b128 v[40:43],v2 offset:11264
v_add_f32 v12,v12,v60
v_mfma_f32_16x16x32_bf16 v[24:27],a[36:39],v[44:47],v[24:27]
buffer_load_dwordx4 v0,s[36:39],s61 offen offset:2048 sc1 lds)"
R"(
v_mfma_f32_16x16x32_bf16 v[20:23],a[40:43],v[48:51],v[20:23]
s_cselect_b32 s74,s57,s74
ds_read_b128 v[44:47],v2 offset:12288
v_add_f32 v13,v13,v61
v_mfma_f32_16x16x32_bf16 v[24:27],a[44:47],v[48:51],v[24:27]
s_waitcnt lgkmcnt(5)
v_add_f32 v14,v14,v62
v_add_f32 v15,v15,v63
v_mfma_f32_16x16x32_bf16 v[20:23],a[48:51],v[52:55],v[20:23]
s_cmp_eq_u32 s74,s58
ds_read_b128 v[48:51],v2 offset:13312
v_add_f32 v16,v16,v64
v_mfma_f32_16x16x32_bf16 v[24:27],a[52:55],v[52:55],v[24:27]
buffer_load_dwordx4 v0,s[36:39],s61 offen offset:3072 sc1 lds
v_mfma_f32_16x16x32_bf16 v[20:23],a[56:59],v[56:59],v[20:23]
s_cselect_b32 s74,s59,s74
ds_read_b128 v[52:55],v2 offset:14336
v_add_f32 v17,v17,v65
v_mfma_f32_16x16x32_bf16 v[24:27],a[60:63],v[56:59],v[24:27]
s_waitcnt lgkmcnt(5)
v_add_f32 v18,v18,v66
v_add_f32 v19,v19,v67
v_mfma_f32_16x16x32_bf16 v[20:23],a[64:67],v[28:31],v[20:23]
s_add_u32 s61,s61,4096
v_cvt_pk_bf16_f32 v60,v12,v13
v_cvt_pk_bf16_f32 v61,v14,v15
v_cvt_pk_bf16_f32 v62,v16,v17
v_cvt_pk_bf16_f32 v63,v18,v19
v_mfma_f32_16x16x32_bf16 v[24:27],a[68:71],v[28:31],v[24:27]
s_add_u32 m0,s88,4096
ds_read_b128 v[56:59],v2 offset:15360
buffer_store_dwordx4 v[60:63],v3,s[36:39],s73 offen offset:0 sc0 sc1
s_cmp_ge_u32 s67,s59
s_mov_b64 s[44:45],s[46:47]
v_mfma_f32_16x16x32_bf16 v[20:23],a[72:75],v[32:35],v[20:23]
s_cselect_b32 s86,s50,s51
ds_read_b128 v[28:31],v2 offset:16384
s_mul_i32 s68,s86,5
v_mfma_f32_16x16x32_bf16 v[24:27],a[76:79],v[32:35],v[24:27]
s_waitcnt lgkmcnt(5)
s_lshr_b32 s68,s68,6
s_mov_b64 s[46:47],s[48:49]
v_mfma_f32_16x16x32_bf16 v[20:23],a[80:83],v[36:39],v[20:23]
ds_read_b128 v[32:35],v2 offset:17408
v_mfma_f32_16x16x32_bf16 v[24:27],a[84:87],v[36:39],v[24:27]
buffer_load_dwordx4 v0,s[36:39],s61 offen offset:0 sc1 lds
v_mfma_f32_16x16x32_bf16 v[20:23],a[88:91],v[40:43],v[20:23]
ds_read_b128 v[36:39],v2 offset:18432
v_mfma_f32_16x16x32_bf16 v[24:27],a[92:95],v[40:43],v[24:27]
s_waitcnt lgkmcnt(5)
v_mfma_f32_16x16x32_bf16 v[20:23],a[96:99],v[44:47],v[20:23]
ds_read_b128 v[40:43],v2 offset:19456
v_mfma_f32_16x16x32_bf16 v[24:27],a[100:103],v[44:47],v[24:27]
buffer_load_dwordx4 v0,s[36:39],s61 offen offset:1024 sc1 lds
v_mfma_f32_16x16x32_bf16 v[20:23],a[104:107],v[48:51],v[20:23]
ds_read_b128 v[44:47],v2 offset:20480
v_mfma_f32_16x16x32_bf16 v[24:27],a[108:111],v[48:51],v[24:27]
s_waitcnt lgkmcnt(5)
v_mfma_f32_16x16x32_bf16 v[20:23],a[112:115],v[52:55],v[20:23]
ds_read_b128 v[48:51],v2 offset:21504
v_mfma_f32_16x16x32_bf16 v[24:27],a[116:119],v[52:55],v[24:27]
buffer_load_dwordx4 v0,s[36:39],s61 offen offset:2048 sc1 lds
v_mfma_f32_16x16x32_bf16 v[20:23],a[120:123],v[56:59],v[20:23]
s_add_u32 m0,s78,3072
ds_read_b128 v[52:55],v2 offset:22528
s_add_u32 s61,s61,24576
v_mfma_f32_16x16x32_bf16 v[24:27],a[124:127],v[56:59],v[24:27]
s_waitcnt lgkmcnt(5)
v_mfma_f32_16x16x32_bf16 v[20:23],a[128:131],v[28:31],v[20:23]
ds_read_b128 v[56:59],v2 offset:23552
v_mfma_f32_16x16x32_bf16 v[24:27],a[132:135],v[28:31],v[24:27]
buffer_load_dwordx4 v[72:75],v8,s[36:39],s49 offen offset:0 sc1
v_mfma_f32_16x16x32_bf16 v[20:23],a[136:139],v[32:35],v[20:23]
s_mov_b64 s[48:49],s[50:51]
ds_read_b128 v[28:31],v2 offset:24576
s_mov_b64 s[50:51],s[52:53]
v_mfma_f32_16x16x32_bf16 v[24:27],a[140:143],v[32:35],v[24:27]
s_waitcnt lgkmcnt(5)
s_mov_b32 s52,s74
v_mfma_f32_16x16x32_bf16 v[20:23],a[144:147],v[36:39],v[20:23]
ds_read_b128 v[32:35],v2 offset:25600
v_mfma_f32_16x16x32_bf16 v[24:27],a[148:151],v[36:39],v[24:27]
v_mfma_f32_16x16x32_bf16 v[20:23],a[152:155],v[40:43],v[20:23]
ds_read_b128 v[36:39],v2 offset:26624
v_mfma_f32_16x16x32_bf16 v[24:27],a[156:159],v[40:43],v[24:27]
s_waitcnt lgkmcnt(5)
v_mfma_f32_16x16x32_bf16 v[20:23],a[160:163],v[44:47],v[20:23]
s_waitcnt vmcnt(9)
ds_read_b128 v[40:43],v2 offset:27648
v_mfma_f32_16x16x32_bf16 v[24:27],a[164:167],v[44:47],v[24:27]
s_barrier
v_mfma_f32_16x16x32_bf16 v[20:23],a[168:171],v[48:51],v[20:23]
ds_read_b128 v[44:47],v2 offset:28672
v_mfma_f32_16x16x32_bf16 v[24:27],a[172:175],v[48:51],v[24:27]
s_waitcnt lgkmcnt(5)
buffer_load_dword v6,s[36:39],s68 offen offset:0 sc1 lds
v_mfma_f32_16x16x32_bf16 v[20:23],a[176:179],v[52:55],v[20:23]
s_add_u32 m0,s88,28672
ds_read_b128 v[48:51],v2 offset:29696
v_mfma_f32_16x16x32_bf16 v[24:27],a[180:183],v[52:55],v[24:27]
v_mfma_f32_16x16x32_bf16 v[20:23],a[184:187],v[56:59],v[20:23]
ds_read_b128 v[52:55],v2 offset:30720
v_mfma_f32_16x16x32_bf16 v[24:27],a[188:191],v[56:59],v[24:27]
s_waitcnt lgkmcnt(5)
buffer_load_dwordx4 v0,s[36:39],s61 offen offset:0 sc1 lds
v_mfma_f32_16x16x32_bf16 v[20:23],a[192:195],v[28:31],v[20:23]
ds_read_b128 v[56:59],v2 offset:31744
v_mfma_f32_16x16x32_bf16 v[24:27],a[196:199],v[28:31],v[24:27]
v_mfma_f32_16x16x32_bf16 v[20:23],a[200:203],v[32:35],v[20:23]
ds_read_b128 v[28:31],v2 offset:32768
v_mfma_f32_16x16x32_bf16 v[24:27],a[204:207],v[32:35],v[24:27]
s_waitcnt lgkmcnt(5)
buffer_load_dwordx4 v0,s[36:39],s61 offen offset:1024 sc1 lds
v_mfma_f32_16x16x32_bf16 v[20:23],a[208:211],v[36:39],v[20:23]
ds_read_b128 v[32:35],v2 offset:33792
v_mfma_f32_16x16x32_bf16 v[24:27],a[212:215],v[36:39],v[24:27]
v_mfma_f32_16x16x32_bf16 v[20:23],a[216:219],v[40:43],v[20:23]
ds_read_b128 v[36:39],v2 offset:34816
v_mfma_f32_16x16x32_bf16 v[24:27],a[220:223],v[40:43],v[24:27]
s_waitcnt lgkmcnt(5)
buffer_load_dwordx4 v0,s[36:39],s61 offen offset:2048 sc1 lds
v_mfma_f32_16x16x32_bf16 v[20:23],a[224:227],v[44:47],v[20:23]
ds_read_b128 v[40:43],v2 offset:35840
v_mfma_f32_16x16x32_bf16 v[24:27],a[228:231],v[44:47],v[24:27]
v_mfma_f32_16x16x32_bf16 v[20:23],a[232:235],v[48:51],v[20:23]
ds_read_b128 v[44:47],v2 offset:36864
v_mfma_f32_16x16x32_bf16 v[24:27],a[236:239],v[48:51],v[24:27]
s_waitcnt lgkmcnt(5)
buffer_load_dwordx4 v0,s[36:39],s61 offen offset:3072 sc1 lds
v_mfma_f32_16x16x32_bf16 v[20:23],a[240:243],v[52:55],v[20:23]
s_add_u32 s61,s61,4096
ds_read_b128 v[48:51],v2 offset:37888
s_add_u32 m0,s88,32768
v_mfma_f32_16x16x32_bf16 v[24:27],a[244:247],v[52:55],v[24:27]
v_mfma_f32_16x16x32_bf16 v[20:23],a[248:251],v[56:59],v[20:23]
ds_read_b128 v[52:55],v2 offset:38912
v_mfma_f32_16x16x32_bf16 v[24:27],a[252:255],v[56:59],v[24:27]
s_waitcnt lgkmcnt(5)
buffer_load_dwordx4 v0,s[36:39],s61 offen offset:0 sc1 lds
v_mfma_f32_16x16x32_bf16 v[20:23],v[108:111],v[28:31],v[20:23]
ds_read_b128 v[56:59],v2 offset:39936
v_mfma_f32_16x16x32_bf16 v[24:27],v[112:115],v[28:31],v[24:27]
v_mfma_f32_16x16x32_bf16 v[20:23],v[116:119],v[32:35],v[20:23]
ds_read_b128 v[28:31],v2 offset:40960
v_mfma_f32_16x16x32_bf16 v[24:27],v[120:123],v[32:35],v[24:27]
s_waitcnt lgkmcnt(5)
buffer_load_dwordx4 v0,s[36:39],s61 offen offset:1024 sc1 lds
v_mfma_f32_16x16x32_bf16 v[20:23],v[124:127],v[36:39],v[20:23]
ds_read_b128 v[32:35],v2 offset:41984
v_mfma_f32_16x16x32_bf16 v[24:27],v[128:131],v[36:39],v[24:27]
v_mfma_f32_16x16x32_bf16 v[20:23],v[132:135],v[40:43],v[20:23]
ds_read_b128 v[36:39],v2 offset:43008
v_mfma_f32_16x16x32_bf16 v[24:27],v[136:139],v[40:43],v[24:27]
s_waitcnt lgkmcnt(5)
buffer_load_dwordx4 v0,s[36:39],s61 offen offset:2048 sc1 lds
v_mfma_f32_16x16x32_bf16 v[20:23],v[140:143],v[44:47],v[20:23]
ds_read_b128 v[40:43],v2 offset:44032
v_mfma_f32_16x16x32_bf16 v[24:27],v[144:147],v[44:47],v[24:27]
v_mfma_f32_16x16x32_bf16 v[20:23],v[148:151],v[48:51],v[20:23]
ds_read_b128 v[44:47],v2 offset:45056
v_mfma_f32_16x16x32_bf16 v[24:27],v[152:155],v[48:51],v[24:27]
s_waitcnt lgkmcnt(5)
v_mfma_f32_16x16x32_bf16 v[20:23],v[156:159],v[52:55],v[20:23]
ds_read_b128 v[48:51],v2 offset:46080
v_mfma_f32_16x16x32_bf16 v[24:27],v[160:163],v[52:55],v[24:27]
v_mfma_f32_16x16x32_bf16 v[20:23],v[164:167],v[56:59],v[20:23]
ds_read_b128 v[52:55],v2 offset:47104
v_mfma_f32_16x16x32_bf16 v[24:27],v[168:171],v[56:59],v[24:27]
s_waitcnt lgkmcnt(5)
v_mfma_f32_16x16x32_bf16 v[20:23],v[172:175],v[28:31],v[20:23]
ds_read_b128 v[56:59],v2 offset:48128
v_mfma_f32_16x16x32_bf16 v[24:27],v[176:179],v[28:31],v[24:27]
ds_read_b128 v[88:91],v9 offset:0
v_mfma_f32_16x16x32_bf16 v[20:23],v[180:183],v[32:35],v[20:23]
ds_read_b128 v[28:31],v2 offset:49152
v_mfma_f32_16x16x32_bf16 v[24:27],v[184:187],v[32:35],v[24:27]
s_waitcnt lgkmcnt(6)
ds_read_b128 v[92:95],v9 offset:1024
v_mfma_f32_16x16x32_bf16 v[20:23],v[188:191],v[36:39],v[20:23]
ds_read_b128 v[32:35],v2 offset:50176
v_mfma_f32_16x16x32_bf16 v[24:27],v[192:195],v[36:39],v[24:27]
ds_read_b128 v[96:99],v9 offset:2048
v_mfma_f32_16x16x32_bf16 v[20:23],v[196:199],v[40:43],v[20:23]
ds_read_b128 v[36:39],v2 offset:51200
v_mfma_f32_16x16x32_bf16 v[24:27],v[200:203],v[40:43],v[24:27]
s_waitcnt lgkmcnt(5)
ds_read_b128 v[76:79],v9 offset:3072
v_mfma_f32_16x16x32_bf16 v[20:23],v[204:207],v[44:47],v[20:23]
ds_read_b128 v[40:43],v2 offset:52224
v_mfma_f32_16x16x32_bf16 v[24:27],v[208:211],v[44:47],v[24:27]
ds_read_b128 v[80:83],v9 offset:4096
v_mfma_f32_16x16x32_bf16 v[20:23],v[212:215],v[48:51],v[20:23]
ds_read_b128 v[44:47],v2 offset:53248
v_mfma_f32_16x16x32_bf16 v[24:27],v[88:91],v[48:51],v[24:27]
s_waitcnt lgkmcnt(5)
ds_read_b128 v[84:87],v9 offset:5120
v_mfma_f32_16x16x32_bf16 v[20:23],v[216:219],v[52:55],v[20:23]
ds_read_b128 v[48:51],v2 offset:54272
v_mfma_f32_16x16x32_bf16 v[24:27],v[92:95],v[52:55],v[24:27]
ds_read_b128 v[88:91],v9 offset:6144)"
R"(
v_mfma_f32_16x16x32_bf16 v[20:23],v[220:223],v[56:59],v[20:23]
ds_read_b128 v[52:55],v2 offset:55296
v_mfma_f32_16x16x32_bf16 v[24:27],v[96:99],v[56:59],v[24:27]
s_waitcnt lgkmcnt(5)
ds_read_b128 v[92:95],v9 offset:7168
v_mfma_f32_16x16x32_bf16 v[20:23],v[224:227],v[28:31],v[20:23]
s_waitcnt vmcnt(7)
ds_read_b128 v[56:59],v2 offset:56320
ds_read_b128 v[96:99],v9 offset:8192
v_mfma_f32_16x16x32_bf16 v[24:27],v[76:79],v[28:31],v[24:27]
s_barrier
v_mfma_f32_16x16x32_bf16 v[20:23],v[228:231],v[32:35],v[20:23]
ds_read_b128 v[28:31],v1 offset:0
ds_read_b32 v10,v7 offset:3072
v_mfma_f32_16x16x32_bf16 v[24:27],v[80:83],v[32:35],v[24:27]
s_waitcnt lgkmcnt(6)
ds_read_b128 v[76:79],v9 offset:9216
v_mfma_f32_16x16x32_bf16 v[20:23],v[232:235],v[36:39],v[20:23]
ds_read_b128 v[32:35],v1 offset:1024
v_mfma_f32_16x16x32_bf16 v[24:27],v[84:87],v[36:39],v[24:27]
ds_read_b128 v[80:83],v9 offset:10240
v_mfma_f32_16x16x32_bf16 v[20:23],v[236:239],v[40:43],v[20:23]
s_waitcnt lgkmcnt(3)
ds_read_b128 v[36:39],v1 offset:2048
v_mfma_f32_16x16x32_bf16 v[24:27],v[88:91],v[40:43],v[24:27]
v_cmp_eq_u32 vcc,s60,v10
v_mfma_f32_16x16x32_bf16 v[20:23],v[240:243],v[44:47],v[20:23]
s_cmp_eq_u32 vcc_lo,-1
ds_read_b128 v[40:43],v1 offset:3072
s_cselect_b32 s66,s86,s59
v_mfma_f32_16x16x32_bf16 v[24:27],v[92:95],v[44:47],v[24:27]
s_mul_i32 s61,s66,7
s_lshr_b32 s61,s61,1
s_cmp_lt_u32 s67,s59
v_mfma_f32_16x16x32_bf16 v[20:23],v[244:247],v[48:51],v[20:23]
ds_read_b128 v[44:47],v1 offset:4096
v_mfma_f32_16x16x32_bf16 v[24:27],v[96:99],v[48:51],v[24:27]
s_waitcnt lgkmcnt(3)
v_mfma_f32_16x16x32_bf16 v[20:23],v[248:251],v[52:55],v[20:23]
ds_read_b128 v[48:51],v1 offset:5120
v_mfma_f32_16x16x32_bf16 v[24:27],v[76:79],v[52:55],v[24:27]
v_mfma_f32_16x16x32_bf16 v[20:23],v[252:255],v[56:59],v[20:23]
ds_read_b128 v[52:55],v1 offset:6144
v_mfma_f32_16x16x32_bf16 v[24:27],v[80:83],v[56:59],v[24:27]
s_cbranch_scc1 Lg4_d0w0_%=
s_branch Lg4_slow0_%=
Lg4_d0w1_%=:
v_mfma_f32_16x16x32_bf16 v[12:15],a[0:3],v[28:31],0
s_add_u32 m0,s88,57344
ds_read_b128 v[56:59],v1 offset:7168
v_lshlrev_b32 v60,16,v72
v_mfma_f32_16x16x32_bf16 v[16:19],a[4:7],v[28:31],0
s_max_u32 s73,s47,s65
v_and_b32 v61,0xffff0000,v72
v_lshlrev_b32 v62,16,v73
v_mfma_f32_16x16x32_bf16 v[12:15],a[8:11],v[32:35],v[12:15]
buffer_load_dwordx4 v0,s[36:39],s61 offen offset:0 sc1 lds
v_mfma_f32_16x16x32_bf16 v[16:19],a[12:15],v[32:35],v[16:19]
s_waitcnt lgkmcnt(4)
ds_read_b128 v[28:31],v1 offset:8192
v_and_b32 v63,0xffff0000,v73
v_mfma_f32_16x16x32_bf16 v[12:15],a[16:19],v[36:39],v[12:15]
s_mov_b32 s65,0
ds_read_b128 v[32:35],v1 offset:9216
v_lshlrev_b32 v64,16,v74
v_mfma_f32_16x16x32_bf16 v[16:19],a[20:23],v[36:39],v[16:19]
s_add_u32 s53,s52,s55
v_and_b32 v65,0xffff0000,v74
v_lshlrev_b32 v66,16,v75
v_mfma_f32_16x16x32_bf16 v[12:15],a[24:27],v[40:43],v[12:15]
buffer_load_dwordx4 v0,s[36:39],s61 offen offset:1024 sc1 lds
v_mfma_f32_16x16x32_bf16 v[16:19],a[28:31],v[40:43],v[16:19]
s_waitcnt lgkmcnt(4)
ds_read_b128 v[36:39],v1 offset:10240
v_and_b32 v67,0xffff0000,v75
v_mfma_f32_16x16x32_bf16 v[12:15],a[32:35],v[44:47],v[12:15]
s_cmp_eq_u32 s53,s56
ds_read_b128 v[40:43],v1 offset:11264
v_add_f32 v20,v20,v60
v_mfma_f32_16x16x32_bf16 v[16:19],a[36:39],v[44:47],v[16:19]
s_cselect_b32 s53,s57,s53
v_add_f32 v21,v21,v61
v_add_f32 v22,v22,v62
v_mfma_f32_16x16x32_bf16 v[12:15],a[40:43],v[48:51],v[12:15]
buffer_load_dwordx4 v0,s[36:39],s61 offen offset:2048 sc1 lds
v_mfma_f32_16x16x32_bf16 v[16:19],a[44:47],v[48:51],v[16:19]
s_waitcnt lgkmcnt(4)
ds_read_b128 v[44:47],v1 offset:12288
v_add_f32 v23,v23,v63
v_mfma_f32_16x16x32_bf16 v[12:15],a[48:51],v[52:55],v[12:15]
s_cmp_eq_u32 s53,s58
ds_read_b128 v[48:51],v1 offset:13312
v_add_f32 v24,v24,v64
v_mfma_f32_16x16x32_bf16 v[16:19],a[52:55],v[52:55],v[16:19]
s_cselect_b32 s53,s59,s53
v_add_f32 v25,v25,v65
v_add_f32 v26,v26,v66
v_mfma_f32_16x16x32_bf16 v[12:15],a[56:59],v[56:59],v[12:15]
buffer_load_dwordx4 v0,s[36:39],s61 offen offset:3072 sc1 lds
v_mfma_f32_16x16x32_bf16 v[16:19],a[60:63],v[56:59],v[16:19]
s_waitcnt lgkmcnt(4)
ds_read_b128 v[52:55],v1 offset:14336
v_add_f32 v27,v27,v67
v_mfma_f32_16x16x32_bf16 v[12:15],a[64:67],v[28:31],v[12:15]
s_add_u32 s61,s61,4096
ds_read_b128 v[56:59],v1 offset:15360
v_cvt_pk_bf16_f32 v60,v20,v21
v_mfma_f32_16x16x32_bf16 v[16:19],a[68:71],v[28:31],v[16:19]
s_add_u32 m0,s88,61440
v_cvt_pk_bf16_f32 v61,v22,v23
v_cvt_pk_bf16_f32 v62,v24,v25
v_cvt_pk_bf16_f32 v63,v26,v27
v_mfma_f32_16x16x32_bf16 v[12:15],a[72:75],v[32:35],v[12:15]
buffer_store_dwordx4 v[60:63],v3,s[36:39],s73 offen offset:0 sc0 sc1
ds_read_b128 v[28:31],v1 offset:16384
v_mfma_f32_16x16x32_bf16 v[16:19],a[76:79],v[32:35],v[16:19]
s_waitcnt lgkmcnt(5)
s_cmp_ge_u32 s66,s59
s_cselect_b32 s86,s49,s50
v_mfma_f32_16x16x32_bf16 v[12:15],a[80:83],v[36:39],v[12:15]
ds_read_b128 v[32:35],v1 offset:17408
v_mfma_f32_16x16x32_bf16 v[16:19],a[84:87],v[36:39],v[16:19]
v_mfma_f32_16x16x32_bf16 v[12:15],a[88:91],v[40:43],v[12:15]
buffer_load_dwordx4 v0,s[36:39],s61 offen offset:0 sc1 lds
v_mfma_f32_16x16x32_bf16 v[16:19],a[92:95],v[40:43],v[16:19]
s_waitcnt lgkmcnt(4)
ds_read_b128 v[36:39],v1 offset:18432
v_mfma_f32_16x16x32_bf16 v[12:15],a[96:99],v[44:47],v[12:15]
ds_read_b128 v[40:43],v1 offset:19456
v_mfma_f32_16x16x32_bf16 v[16:19],a[100:103],v[44:47],v[16:19]
v_mfma_f32_16x16x32_bf16 v[12:15],a[104:107],v[48:51],v[12:15]
buffer_load_dwordx4 v0,s[36:39],s61 offen offset:1024 sc1 lds
v_mfma_f32_16x16x32_bf16 v[16:19],a[108:111],v[48:51],v[16:19]
s_waitcnt lgkmcnt(4)
ds_read_b128 v[44:47],v1 offset:20480
v_mfma_f32_16x16x32_bf16 v[12:15],a[112:115],v[52:55],v[12:15]
ds_read_b128 v[48:51],v1 offset:21504
v_mfma_f32_16x16x32_bf16 v[16:19],a[116:119],v[52:55],v[16:19]
v_mfma_f32_16x16x32_bf16 v[12:15],a[120:123],v[56:59],v[12:15]
buffer_load_dwordx4 v0,s[36:39],s61 offen offset:2048 sc1 lds
v_mfma_f32_16x16x32_bf16 v[16:19],a[124:127],v[56:59],v[16:19]
s_waitcnt lgkmcnt(4)
ds_read_b128 v[52:55],v1 offset:22528
s_add_u32 s61,s61,24576
v_mfma_f32_16x16x32_bf16 v[12:15],a[128:131],v[28:31],v[12:15]
s_add_u32 m0,s88,86016
ds_read_b128 v[56:59],v1 offset:23552
v_mfma_f32_16x16x32_bf16 v[16:19],a[132:135],v[28:31],v[16:19]
v_mfma_f32_16x16x32_bf16 v[12:15],a[136:139],v[32:35],v[12:15]
buffer_load_dwordx4 v[68:71],v8,s[36:39],s48 offen offset:0 sc1
v_mfma_f32_16x16x32_bf16 v[16:19],a[140:143],v[32:35],v[16:19]
s_waitcnt lgkmcnt(4)
ds_read_b128 v[28:31],v1 offset:24576
v_mfma_f32_16x16x32_bf16 v[12:15],a[144:147],v[36:39],v[12:15]
ds_read_b128 v[32:35],v1 offset:25600
v_mfma_f32_16x16x32_bf16 v[16:19],a[148:151],v[36:39],v[16:19]
v_mfma_f32_16x16x32_bf16 v[12:15],a[152:155],v[40:43],v[12:15]
ds_read_b128 v[36:39],v1 offset:26624
v_mfma_f32_16x16x32_bf16 v[16:19],a[156:159],v[40:43],v[16:19]
s_waitcnt lgkmcnt(5)
v_mfma_f32_16x16x32_bf16 v[12:15],a[160:163],v[44:47],v[12:15]
s_waitcnt vmcnt(9)
ds_read_b128 v[40:43],v1 offset:27648
v_mfma_f32_16x16x32_bf16 v[16:19],a[164:167],v[44:47],v[16:19]
s_barrier
v_mfma_f32_16x16x32_bf16 v[12:15],a[168:171],v[48:51],v[12:15]
ds_read_b128 v[44:47],v1 offset:28672
v_mfma_f32_16x16x32_bf16 v[16:19],a[172:175],v[48:51],v[16:19]
s_waitcnt lgkmcnt(5)
v_mfma_f32_16x16x32_bf16 v[12:15],a[176:179],v[52:55],v[12:15]
ds_read_b128 v[48:51],v1 offset:29696
v_mfma_f32_16x16x32_bf16 v[16:19],a[180:183],v[52:55],v[16:19]
buffer_load_dwordx4 v0,s[36:39],s61 offen offset:0 sc1 lds
v_mfma_f32_16x16x32_bf16 v[12:15],a[184:187],v[56:59],v[12:15]
ds_read_b128 v[52:55],v1 offset:30720
v_mfma_f32_16x16x32_bf16 v[16:19],a[188:191],v[56:59],v[16:19]
s_waitcnt lgkmcnt(5)
v_mfma_f32_16x16x32_bf16 v[12:15],a[192:195],v[28:31],v[12:15]
ds_read_b128 v[56:59],v1 offset:31744
v_mfma_f32_16x16x32_bf16 v[16:19],a[196:199],v[28:31],v[16:19]
buffer_load_dwordx4 v0,s[36:39],s61 offen offset:1024 sc1 lds
v_mfma_f32_16x16x32_bf16 v[12:15],a[200:203],v[32:35],v[12:15]
ds_read_b128 v[28:31],v1 offset:32768
v_mfma_f32_16x16x32_bf16 v[16:19],a[204:207],v[32:35],v[16:19]
s_waitcnt lgkmcnt(5)
v_mfma_f32_16x16x32_bf16 v[12:15],a[208:211],v[36:39],v[12:15]
ds_read_b128 v[32:35],v1 offset:33792
v_mfma_f32_16x16x32_bf16 v[16:19],a[212:215],v[36:39],v[16:19]
buffer_load_dwordx4 v0,s[36:39],s61 offen offset:2048 sc1 lds
v_mfma_f32_16x16x32_bf16 v[12:15],a[216:219],v[40:43],v[12:15]
ds_read_b128 v[36:39],v1 offset:34816
v_mfma_f32_16x16x32_bf16 v[16:19],a[220:223],v[40:43],v[16:19]
s_waitcnt lgkmcnt(5)
v_mfma_f32_16x16x32_bf16 v[12:15],a[224:227],v[44:47],v[12:15]
ds_read_b128 v[40:43],v1 offset:35840
v_mfma_f32_16x16x32_bf16 v[16:19],a[228:231],v[44:47],v[16:19]
buffer_load_dwordx4 v0,s[36:39],s61 offen offset:3072 sc1 lds
v_mfma_f32_16x16x32_bf16 v[12:15],a[232:235],v[48:51],v[12:15]
s_add_u32 s61,s61,4096
ds_read_b128 v[44:47],v1 offset:36864)"
R"(
s_add_u32 m0,s88,90112
v_mfma_f32_16x16x32_bf16 v[16:19],a[236:239],v[48:51],v[16:19]
s_waitcnt lgkmcnt(5)
v_mfma_f32_16x16x32_bf16 v[12:15],a[240:243],v[52:55],v[12:15]
ds_read_b128 v[48:51],v1 offset:37888
v_mfma_f32_16x16x32_bf16 v[16:19],a[244:247],v[52:55],v[16:19]
buffer_load_dwordx4 v0,s[36:39],s61 offen offset:0 sc1 lds
v_mfma_f32_16x16x32_bf16 v[12:15],a[248:251],v[56:59],v[12:15]
ds_read_b128 v[52:55],v1 offset:38912
v_mfma_f32_16x16x32_bf16 v[16:19],a[252:255],v[56:59],v[16:19]
s_waitcnt lgkmcnt(5)
v_mfma_f32_16x16x32_bf16 v[12:15],v[108:111],v[28:31],v[12:15]
ds_read_b128 v[56:59],v1 offset:39936
v_mfma_f32_16x16x32_bf16 v[16:19],v[112:115],v[28:31],v[16:19]
buffer_load_dwordx4 v0,s[36:39],s61 offen offset:1024 sc1 lds
v_mfma_f32_16x16x32_bf16 v[12:15],v[116:119],v[32:35],v[12:15]
ds_read_b128 v[28:31],v1 offset:40960
v_mfma_f32_16x16x32_bf16 v[16:19],v[120:123],v[32:35],v[16:19]
s_waitcnt lgkmcnt(5)
v_mfma_f32_16x16x32_bf16 v[12:15],v[124:127],v[36:39],v[12:15]
ds_read_b128 v[32:35],v1 offset:41984
v_mfma_f32_16x16x32_bf16 v[16:19],v[128:131],v[36:39],v[16:19]
buffer_load_dwordx4 v0,s[36:39],s61 offen offset:2048 sc1 lds
v_mfma_f32_16x16x32_bf16 v[12:15],v[132:135],v[40:43],v[12:15]
ds_read_b128 v[36:39],v1 offset:43008
v_mfma_f32_16x16x32_bf16 v[16:19],v[136:139],v[40:43],v[16:19]
s_waitcnt lgkmcnt(5)
v_mfma_f32_16x16x32_bf16 v[12:15],v[140:143],v[44:47],v[12:15]
ds_read_b128 v[40:43],v1 offset:44032
v_mfma_f32_16x16x32_bf16 v[16:19],v[144:147],v[44:47],v[16:19]
v_mfma_f32_16x16x32_bf16 v[12:15],v[148:151],v[48:51],v[12:15]
ds_read_b128 v[44:47],v1 offset:45056
v_mfma_f32_16x16x32_bf16 v[16:19],v[152:155],v[48:51],v[16:19]
s_waitcnt lgkmcnt(5)
v_mfma_f32_16x16x32_bf16 v[12:15],v[156:159],v[52:55],v[12:15]
ds_read_b128 v[48:51],v1 offset:46080
v_mfma_f32_16x16x32_bf16 v[16:19],v[160:163],v[52:55],v[16:19]
v_mfma_f32_16x16x32_bf16 v[12:15],v[164:167],v[56:59],v[12:15]
ds_read_b128 v[52:55],v1 offset:47104
v_mfma_f32_16x16x32_bf16 v[16:19],v[168:171],v[56:59],v[16:19]
s_waitcnt lgkmcnt(5)
v_mfma_f32_16x16x32_bf16 v[12:15],v[172:175],v[28:31],v[12:15]
ds_read_b128 v[56:59],v1 offset:48128
v_mfma_f32_16x16x32_bf16 v[16:19],v[176:179],v[28:31],v[16:19]
ds_read_b128 v[88:91],v9 offset:0
v_mfma_f32_16x16x32_bf16 v[12:15],v[180:183],v[32:35],v[12:15]
ds_read_b128 v[28:31],v1 offset:49152
v_mfma_f32_16x16x32_bf16 v[16:19],v[184:187],v[32:35],v[16:19]
s_waitcnt lgkmcnt(6)
ds_read_b128 v[92:95],v9 offset:1024
v_mfma_f32_16x16x32_bf16 v[12:15],v[188:191],v[36:39],v[12:15]
ds_read_b128 v[32:35],v1 offset:50176
v_mfma_f32_16x16x32_bf16 v[16:19],v[192:195],v[36:39],v[16:19]
ds_read_b128 v[96:99],v9 offset:2048
v_mfma_f32_16x16x32_bf16 v[12:15],v[196:199],v[40:43],v[12:15]
ds_read_b128 v[36:39],v1 offset:51200
v_mfma_f32_16x16x32_bf16 v[16:19],v[200:203],v[40:43],v[16:19]
s_waitcnt lgkmcnt(5)
ds_read_b128 v[76:79],v9 offset:3072
v_mfma_f32_16x16x32_bf16 v[12:15],v[204:207],v[44:47],v[12:15]
ds_read_b128 v[40:43],v1 offset:52224
v_mfma_f32_16x16x32_bf16 v[16:19],v[208:211],v[44:47],v[16:19]
ds_read_b128 v[80:83],v9 offset:4096
v_mfma_f32_16x16x32_bf16 v[12:15],v[212:215],v[48:51],v[12:15]
ds_read_b128 v[44:47],v1 offset:53248
v_mfma_f32_16x16x32_bf16 v[16:19],v[88:91],v[48:51],v[16:19]
s_waitcnt lgkmcnt(5)
ds_read_b128 v[84:87],v9 offset:5120
v_mfma_f32_16x16x32_bf16 v[12:15],v[216:219],v[52:55],v[12:15]
ds_read_b128 v[48:51],v1 offset:54272
v_mfma_f32_16x16x32_bf16 v[16:19],v[92:95],v[52:55],v[16:19]
ds_read_b128 v[88:91],v9 offset:6144
v_mfma_f32_16x16x32_bf16 v[12:15],v[220:223],v[56:59],v[12:15]
ds_read_b128 v[52:55],v1 offset:55296
v_mfma_f32_16x16x32_bf16 v[16:19],v[96:99],v[56:59],v[16:19]
s_waitcnt lgkmcnt(5)
ds_read_b128 v[92:95],v9 offset:7168
v_mfma_f32_16x16x32_bf16 v[12:15],v[224:227],v[28:31],v[12:15]
s_waitcnt vmcnt(7)
ds_read_b128 v[56:59],v1 offset:56320
ds_read_b128 v[96:99],v9 offset:8192
v_mfma_f32_16x16x32_bf16 v[16:19],v[76:79],v[28:31],v[16:19]
s_barrier
v_mfma_f32_16x16x32_bf16 v[12:15],v[228:231],v[32:35],v[12:15]
ds_read_b128 v[28:31],v2 offset:0
ds_read_b32 v10,v7 offset:3072
v_mfma_f32_16x16x32_bf16 v[16:19],v[80:83],v[32:35],v[16:19]
s_waitcnt lgkmcnt(6)
ds_read_b128 v[76:79],v9 offset:9216
v_mfma_f32_16x16x32_bf16 v[12:15],v[232:235],v[36:39],v[12:15]
ds_read_b128 v[32:35],v2 offset:1024
v_mfma_f32_16x16x32_bf16 v[16:19],v[84:87],v[36:39],v[16:19]
ds_read_b128 v[80:83],v9 offset:10240
v_mfma_f32_16x16x32_bf16 v[12:15],v[236:239],v[40:43],v[12:15]
s_waitcnt lgkmcnt(3)
ds_read_b128 v[36:39],v2 offset:2048
v_mfma_f32_16x16x32_bf16 v[16:19],v[88:91],v[40:43],v[16:19]
v_cmp_eq_u32 vcc,s60,v10
v_mfma_f32_16x16x32_bf16 v[12:15],v[240:243],v[44:47],v[12:15]
s_cmp_eq_u32 vcc_lo,-1
ds_read_b128 v[40:43],v2 offset:3072
s_cselect_b32 s67,s86,s59
v_mfma_f32_16x16x32_bf16 v[16:19],v[92:95],v[44:47],v[16:19]
s_mul_i32 s61,s67,7
s_lshr_b32 s61,s61,1
s_cmp_ge_u32 s66,s59
v_mfma_f32_16x16x32_bf16 v[12:15],v[244:247],v[48:51],v[12:15]
ds_read_b128 v[44:47],v2 offset:4096
v_mfma_f32_16x16x32_bf16 v[16:19],v[96:99],v[48:51],v[16:19]
s_waitcnt lgkmcnt(3)
v_mfma_f32_16x16x32_bf16 v[12:15],v[248:251],v[52:55],v[12:15]
ds_read_b128 v[48:51],v2 offset:5120
v_mfma_f32_16x16x32_bf16 v[16:19],v[76:79],v[52:55],v[16:19]
v_mfma_f32_16x16x32_bf16 v[12:15],v[252:255],v[56:59],v[12:15]
ds_read_b128 v[52:55],v2 offset:6144
v_mfma_f32_16x16x32_bf16 v[16:19],v[80:83],v[56:59],v[16:19]
s_cbranch_scc1 Lg4_slow1_%=
Lg4_d1w1_%=:
v_mfma_f32_16x16x32_bf16 v[20:23],a[0:3],v[28:31],0
s_add_u32 m0,s88,0
ds_read_b128 v[56:59],v2 offset:7168
v_lshlrev_b32 v60,16,v68
v_mfma_f32_16x16x32_bf16 v[24:27],a[4:7],v[28:31],0
s_max_u32 s73,s48,s65
v_and_b32 v61,0xffff0000,v68
v_lshlrev_b32 v62,16,v69
v_mfma_f32_16x16x32_bf16 v[20:23],a[8:11],v[32:35],v[20:23]
buffer_load_dwordx4 v0,s[36:39],s61 offen offset:0 sc1 lds
v_mfma_f32_16x16x32_bf16 v[24:27],a[12:15],v[32:35],v[24:27]
s_waitcnt lgkmcnt(4)
ds_read_b128 v[28:31],v2 offset:8192
v_and_b32 v63,0xffff0000,v69
v_mfma_f32_16x16x32_bf16 v[20:23],a[16:19],v[36:39],v[20:23]
s_mov_b32 s65,0
ds_read_b128 v[32:35],v2 offset:9216
v_lshlrev_b32 v64,16,v70
v_mfma_f32_16x16x32_bf16 v[24:27],a[20:23],v[36:39],v[24:27]
s_add_u32 s74,s53,s55
v_and_b32 v65,0xffff0000,v70
v_lshlrev_b32 v66,16,v71
v_mfma_f32_16x16x32_bf16 v[20:23],a[24:27],v[40:43],v[20:23]
buffer_load_dwordx4 v0,s[36:39],s61 offen offset:1024 sc1 lds
v_mfma_f32_16x16x32_bf16 v[24:27],a[28:31],v[40:43],v[24:27]
s_waitcnt lgkmcnt(4)
ds_read_b128 v[36:39],v2 offset:10240
v_and_b32 v67,0xffff0000,v71
v_mfma_f32_16x16x32_bf16 v[20:23],a[32:35],v[44:47],v[20:23]
s_cmp_eq_u32 s74,s56
ds_read_b128 v[40:43],v2 offset:11264
v_add_f32 v12,v12,v60
v_mfma_f32_16x16x32_bf16 v[24:27],a[36:39],v[44:47],v[24:27]
s_cselect_b32 s74,s57,s74
v_add_f32 v13,v13,v61
v_add_f32 v14,v14,v62
v_mfma_f32_16x16x32_bf16 v[20:23],a[40:43],v[48:51],v[20:23]
buffer_load_dwordx4 v0,s[36:39],s61 offen offset:2048 sc1 lds
v_mfma_f32_16x16x32_bf16 v[24:27],a[44:47],v[48:51],v[24:27]
s_waitcnt lgkmcnt(4)
ds_read_b128 v[44:47],v2 offset:12288
v_add_f32 v15,v15,v63
v_mfma_f32_16x16x32_bf16 v[20:23],a[48:51],v[52:55],v[20:23]
s_cmp_eq_u32 s74,s58
ds_read_b128 v[48:51],v2 offset:13312
v_add_f32 v16,v16,v64
v_mfma_f32_16x16x32_bf16 v[24:27],a[52:55],v[52:55],v[24:27]
s_cselect_b32 s74,s59,s74
v_add_f32 v17,v17,v65
v_add_f32 v18,v18,v66
v_mfma_f32_16x16x32_bf16 v[20:23],a[56:59],v[56:59],v[20:23]
buffer_load_dwordx4 v0,s[36:39],s61 offen offset:3072 sc1 lds
v_mfma_f32_16x16x32_bf16 v[24:27],a[60:63],v[56:59],v[24:27]
s_waitcnt lgkmcnt(4)
ds_read_b128 v[52:55],v2 offset:14336
v_add_f32 v19,v19,v67
v_mfma_f32_16x16x32_bf16 v[20:23],a[64:67],v[28:31],v[20:23]
s_add_u32 s61,s61,4096
ds_read_b128 v[56:59],v2 offset:15360
v_cvt_pk_bf16_f32 v60,v12,v13
v_mfma_f32_16x16x32_bf16 v[24:27],a[68:71],v[28:31],v[24:27]
s_add_u32 m0,s88,4096
v_cvt_pk_bf16_f32 v61,v14,v15
v_cvt_pk_bf16_f32 v62,v16,v17
v_cvt_pk_bf16_f32 v63,v18,v19
v_mfma_f32_16x16x32_bf16 v[20:23],a[72:75],v[32:35],v[20:23]
buffer_store_dwordx4 v[60:63],v3,s[36:39],s73 offen offset:0 sc0 sc1
ds_read_b128 v[28:31],v2 offset:16384
v_mfma_f32_16x16x32_bf16 v[24:27],a[76:79],v[32:35],v[24:27]
s_waitcnt lgkmcnt(5)
s_cmp_ge_u32 s67,s59
s_cselect_b32 s86,s50,s51
v_mfma_f32_16x16x32_bf16 v[20:23],a[80:83],v[36:39],v[20:23]
s_mov_b64 s[44:45],s[46:47]
ds_read_b128 v[32:35],v2 offset:17408
s_mov_b64 s[46:47],s[48:49]
v_mfma_f32_16x16x32_bf16 v[24:27],a[84:87],v[36:39],v[24:27]
v_mfma_f32_16x16x32_bf16 v[20:23],a[88:91],v[40:43],v[20:23]
buffer_load_dwordx4 v0,s[36:39],s61 offen offset:0 sc1 lds
v_mfma_f32_16x16x32_bf16 v[24:27],a[92:95],v[40:43],v[24:27]
s_waitcnt lgkmcnt(4)
ds_read_b128 v[36:39],v2 offset:18432)"
R"(
v_mfma_f32_16x16x32_bf16 v[20:23],a[96:99],v[44:47],v[20:23]
ds_read_b128 v[40:43],v2 offset:19456
v_mfma_f32_16x16x32_bf16 v[24:27],a[100:103],v[44:47],v[24:27]
v_mfma_f32_16x16x32_bf16 v[20:23],a[104:107],v[48:51],v[20:23]
buffer_load_dwordx4 v0,s[36:39],s61 offen offset:1024 sc1 lds
v_mfma_f32_16x16x32_bf16 v[24:27],a[108:111],v[48:51],v[24:27]
s_waitcnt lgkmcnt(4)
ds_read_b128 v[44:47],v2 offset:20480
v_mfma_f32_16x16x32_bf16 v[20:23],a[112:115],v[52:55],v[20:23]
ds_read_b128 v[48:51],v2 offset:21504
v_mfma_f32_16x16x32_bf16 v[24:27],a[116:119],v[52:55],v[24:27]
v_mfma_f32_16x16x32_bf16 v[20:23],a[120:123],v[56:59],v[20:23]
buffer_load_dwordx4 v0,s[36:39],s61 offen offset:2048 sc1 lds
v_mfma_f32_16x16x32_bf16 v[24:27],a[124:127],v[56:59],v[24:27]
s_waitcnt lgkmcnt(4)
ds_read_b128 v[52:55],v2 offset:22528
s_add_u32 s61,s61,24576
v_mfma_f32_16x16x32_bf16 v[20:23],a[128:131],v[28:31],v[20:23]
s_add_u32 m0,s88,28672
ds_read_b128 v[56:59],v2 offset:23552
v_mfma_f32_16x16x32_bf16 v[24:27],a[132:135],v[28:31],v[24:27]
v_mfma_f32_16x16x32_bf16 v[20:23],a[136:139],v[32:35],v[20:23]
buffer_load_dwordx4 v[72:75],v8,s[36:39],s49 offen offset:0 sc1
v_mfma_f32_16x16x32_bf16 v[24:27],a[140:143],v[32:35],v[24:27]
s_waitcnt lgkmcnt(4)
ds_read_b128 v[28:31],v2 offset:24576
s_mov_b64 s[48:49],s[50:51]
v_mfma_f32_16x16x32_bf16 v[20:23],a[144:147],v[36:39],v[20:23]
s_mov_b64 s[50:51],s[52:53]
ds_read_b128 v[32:35],v2 offset:25600
s_mov_b32 s52,s74
v_mfma_f32_16x16x32_bf16 v[24:27],a[148:151],v[36:39],v[24:27]
v_mfma_f32_16x16x32_bf16 v[20:23],a[152:155],v[40:43],v[20:23]
ds_read_b128 v[36:39],v2 offset:26624
v_mfma_f32_16x16x32_bf16 v[24:27],a[156:159],v[40:43],v[24:27]
s_waitcnt lgkmcnt(5)
v_mfma_f32_16x16x32_bf16 v[20:23],a[160:163],v[44:47],v[20:23]
s_waitcnt vmcnt(9)
ds_read_b128 v[40:43],v2 offset:27648
v_mfma_f32_16x16x32_bf16 v[24:27],a[164:167],v[44:47],v[24:27]
s_barrier
v_mfma_f32_16x16x32_bf16 v[20:23],a[168:171],v[48:51],v[20:23]
ds_read_b128 v[44:47],v2 offset:28672
v_mfma_f32_16x16x32_bf16 v[24:27],a[172:175],v[48:51],v[24:27]
s_waitcnt lgkmcnt(5)
v_mfma_f32_16x16x32_bf16 v[20:23],a[176:179],v[52:55],v[20:23]
ds_read_b128 v[48:51],v2 offset:29696
v_mfma_f32_16x16x32_bf16 v[24:27],a[180:183],v[52:55],v[24:27]
buffer_load_dwordx4 v0,s[36:39],s61 offen offset:0 sc1 lds
v_mfma_f32_16x16x32_bf16 v[20:23],a[184:187],v[56:59],v[20:23]
ds_read_b128 v[52:55],v2 offset:30720
v_mfma_f32_16x16x32_bf16 v[24:27],a[188:191],v[56:59],v[24:27]
s_waitcnt lgkmcnt(5)
v_mfma_f32_16x16x32_bf16 v[20:23],a[192:195],v[28:31],v[20:23]
ds_read_b128 v[56:59],v2 offset:31744
v_mfma_f32_16x16x32_bf16 v[24:27],a[196:199],v[28:31],v[24:27]
buffer_load_dwordx4 v0,s[36:39],s61 offen offset:1024 sc1 lds
v_mfma_f32_16x16x32_bf16 v[20:23],a[200:203],v[32:35],v[20:23]
ds_read_b128 v[28:31],v2 offset:32768
v_mfma_f32_16x16x32_bf16 v[24:27],a[204:207],v[32:35],v[24:27]
s_waitcnt lgkmcnt(5)
v_mfma_f32_16x16x32_bf16 v[20:23],a[208:211],v[36:39],v[20:23]
ds_read_b128 v[32:35],v2 offset:33792
v_mfma_f32_16x16x32_bf16 v[24:27],a[212:215],v[36:39],v[24:27]
buffer_load_dwordx4 v0,s[36:39],s61 offen offset:2048 sc1 lds
v_mfma_f32_16x16x32_bf16 v[20:23],a[216:219],v[40:43],v[20:23]
ds_read_b128 v[36:39],v2 offset:34816
v_mfma_f32_16x16x32_bf16 v[24:27],a[220:223],v[40:43],v[24:27]
s_waitcnt lgkmcnt(5)
v_mfma_f32_16x16x32_bf16 v[20:23],a[224:227],v[44:47],v[20:23]
ds_read_b128 v[40:43],v2 offset:35840
v_mfma_f32_16x16x32_bf16 v[24:27],a[228:231],v[44:47],v[24:27]
buffer_load_dwordx4 v0,s[36:39],s61 offen offset:3072 sc1 lds
v_mfma_f32_16x16x32_bf16 v[20:23],a[232:235],v[48:51],v[20:23]
s_add_u32 s61,s61,4096
ds_read_b128 v[44:47],v2 offset:36864
s_add_u32 m0,s88,32768
v_mfma_f32_16x16x32_bf16 v[24:27],a[236:239],v[48:51],v[24:27]
s_waitcnt lgkmcnt(5)
v_mfma_f32_16x16x32_bf16 v[20:23],a[240:243],v[52:55],v[20:23]
ds_read_b128 v[48:51],v2 offset:37888
v_mfma_f32_16x16x32_bf16 v[24:27],a[244:247],v[52:55],v[24:27]
buffer_load_dwordx4 v0,s[36:39],s61 offen offset:0 sc1 lds
v_mfma_f32_16x16x32_bf16 v[20:23],a[248:251],v[56:59],v[20:23]
ds_read_b128 v[52:55],v2 offset:38912
v_mfma_f32_16x16x32_bf16 v[24:27],a[252:255],v[56:59],v[24:27]
s_waitcnt lgkmcnt(5)
v_mfma_f32_16x16x32_bf16 v[20:23],v[108:111],v[28:31],v[20:23]
ds_read_b128 v[56:59],v2 offset:39936
v_mfma_f32_16x16x32_bf16 v[24:27],v[112:115],v[28:31],v[24:27]
buffer_load_dwordx4 v0,s[36:39],s61 offen offset:1024 sc1 lds
v_mfma_f32_16x16x32_bf16 v[20:23],v[116:119],v[32:35],v[20:23]
ds_read_b128 v[28:31],v2 offset:40960
v_mfma_f32_16x16x32_bf16 v[24:27],v[120:123],v[32:35],v[24:27]
s_waitcnt lgkmcnt(5)
v_mfma_f32_16x16x32_bf16 v[20:23],v[124:127],v[36:39],v[20:23]
ds_read_b128 v[32:35],v2 offset:41984
v_mfma_f32_16x16x32_bf16 v[24:27],v[128:131],v[36:39],v[24:27]
buffer_load_dwordx4 v0,s[36:39],s61 offen offset:2048 sc1 lds
v_mfma_f32_16x16x32_bf16 v[20:23],v[132:135],v[40:43],v[20:23]
ds_read_b128 v[36:39],v2 offset:43008
v_mfma_f32_16x16x32_bf16 v[24:27],v[136:139],v[40:43],v[24:27]
s_waitcnt lgkmcnt(5)
v_mfma_f32_16x16x32_bf16 v[20:23],v[140:143],v[44:47],v[20:23]
ds_read_b128 v[40:43],v2 offset:44032
v_mfma_f32_16x16x32_bf16 v[24:27],v[144:147],v[44:47],v[24:27]
v_mfma_f32_16x16x32_bf16 v[20:23],v[148:151],v[48:51],v[20:23]
ds_read_b128 v[44:47],v2 offset:45056
v_mfma_f32_16x16x32_bf16 v[24:27],v[152:155],v[48:51],v[24:27]
s_waitcnt lgkmcnt(5)
v_mfma_f32_16x16x32_bf16 v[20:23],v[156:159],v[52:55],v[20:23]
ds_read_b128 v[48:51],v2 offset:46080
v_mfma_f32_16x16x32_bf16 v[24:27],v[160:163],v[52:55],v[24:27]
v_mfma_f32_16x16x32_bf16 v[20:23],v[164:167],v[56:59],v[20:23]
ds_read_b128 v[52:55],v2 offset:47104
v_mfma_f32_16x16x32_bf16 v[24:27],v[168:171],v[56:59],v[24:27]
s_waitcnt lgkmcnt(5)
v_mfma_f32_16x16x32_bf16 v[20:23],v[172:175],v[28:31],v[20:23]
ds_read_b128 v[56:59],v2 offset:48128
v_mfma_f32_16x16x32_bf16 v[24:27],v[176:179],v[28:31],v[24:27]
ds_read_b128 v[88:91],v9 offset:0
v_mfma_f32_16x16x32_bf16 v[20:23],v[180:183],v[32:35],v[20:23]
ds_read_b128 v[28:31],v2 offset:49152
v_mfma_f32_16x16x32_bf16 v[24:27],v[184:187],v[32:35],v[24:27]
s_waitcnt lgkmcnt(6)
ds_read_b128 v[92:95],v9 offset:1024
v_mfma_f32_16x16x32_bf16 v[20:23],v[188:191],v[36:39],v[20:23]
ds_read_b128 v[32:35],v2 offset:50176
v_mfma_f32_16x16x32_bf16 v[24:27],v[192:195],v[36:39],v[24:27]
ds_read_b128 v[96:99],v9 offset:2048
v_mfma_f32_16x16x32_bf16 v[20:23],v[196:199],v[40:43],v[20:23]
ds_read_b128 v[36:39],v2 offset:51200
v_mfma_f32_16x16x32_bf16 v[24:27],v[200:203],v[40:43],v[24:27]
s_waitcnt lgkmcnt(5)
ds_read_b128 v[76:79],v9 offset:3072
v_mfma_f32_16x16x32_bf16 v[20:23],v[204:207],v[44:47],v[20:23]
ds_read_b128 v[40:43],v2 offset:52224
v_mfma_f32_16x16x32_bf16 v[24:27],v[208:211],v[44:47],v[24:27]
ds_read_b128 v[80:83],v9 offset:4096
v_mfma_f32_16x16x32_bf16 v[20:23],v[212:215],v[48:51],v[20:23]
ds_read_b128 v[44:47],v2 offset:53248
v_mfma_f32_16x16x32_bf16 v[24:27],v[88:91],v[48:51],v[24:27]
s_waitcnt lgkmcnt(5)
ds_read_b128 v[84:87],v9 offset:5120
v_mfma_f32_16x16x32_bf16 v[20:23],v[216:219],v[52:55],v[20:23]
ds_read_b128 v[48:51],v2 offset:54272
v_mfma_f32_16x16x32_bf16 v[24:27],v[92:95],v[52:55],v[24:27]
ds_read_b128 v[88:91],v9 offset:6144
v_mfma_f32_16x16x32_bf16 v[20:23],v[220:223],v[56:59],v[20:23]
ds_read_b128 v[52:55],v2 offset:55296
v_mfma_f32_16x16x32_bf16 v[24:27],v[96:99],v[56:59],v[24:27]
s_waitcnt lgkmcnt(5)
ds_read_b128 v[92:95],v9 offset:7168
v_mfma_f32_16x16x32_bf16 v[20:23],v[224:227],v[28:31],v[20:23]
s_waitcnt vmcnt(7)
ds_read_b128 v[56:59],v2 offset:56320
ds_read_b128 v[96:99],v9 offset:8192
v_mfma_f32_16x16x32_bf16 v[24:27],v[76:79],v[28:31],v[24:27]
s_barrier
v_mfma_f32_16x16x32_bf16 v[20:23],v[228:231],v[32:35],v[20:23]
ds_read_b128 v[28:31],v1 offset:0
ds_read_b32 v10,v7 offset:3072
v_mfma_f32_16x16x32_bf16 v[24:27],v[80:83],v[32:35],v[24:27]
s_waitcnt lgkmcnt(6)
ds_read_b128 v[76:79],v9 offset:9216
v_mfma_f32_16x16x32_bf16 v[20:23],v[232:235],v[36:39],v[20:23]
ds_read_b128 v[32:35],v1 offset:1024
v_mfma_f32_16x16x32_bf16 v[24:27],v[84:87],v[36:39],v[24:27]
ds_read_b128 v[80:83],v9 offset:10240
v_mfma_f32_16x16x32_bf16 v[20:23],v[236:239],v[40:43],v[20:23]
s_waitcnt lgkmcnt(3)
ds_read_b128 v[36:39],v1 offset:2048
v_mfma_f32_16x16x32_bf16 v[24:27],v[88:91],v[40:43],v[24:27]
v_cmp_eq_u32 vcc,s60,v10
v_mfma_f32_16x16x32_bf16 v[20:23],v[240:243],v[44:47],v[20:23]
s_cmp_eq_u32 vcc_lo,-1
ds_read_b128 v[40:43],v1 offset:3072
s_cselect_b32 s66,s86,s59
v_mfma_f32_16x16x32_bf16 v[24:27],v[92:95],v[44:47],v[24:27]
s_mul_i32 s61,s66,7
s_lshr_b32 s61,s61,1
s_cmp_lt_u32 s67,s59
v_mfma_f32_16x16x32_bf16 v[20:23],v[244:247],v[48:51],v[20:23]
ds_read_b128 v[44:47],v1 offset:4096
v_mfma_f32_16x16x32_bf16 v[24:27],v[96:99],v[48:51],v[24:27]
s_waitcnt lgkmcnt(3)
v_mfma_f32_16x16x32_bf16 v[20:23],v[248:251],v[52:55],v[20:23]
ds_read_b128 v[48:51],v1 offset:5120
v_mfma_f32_16x16x32_bf16 v[24:27],v[76:79],v[52:55],v[24:27]
v_mfma_f32_16x16x32_bf16 v[20:23],v[252:255],v[56:59],v[20:23]
ds_read_b128 v[52:55],v1 offset:6144
v_mfma_f32_16x16x32_bf16 v[24:27],v[80:83],v[56:59],v[24:27]
s_cbranch_scc1 Lg4_d0w1_%=
s_branch Lg4_slow0_%=
Lg4_d0w2_%=:
v_mfma_f32_16x16x32_bf16 v[12:15],a[0:3],v[28:31],0
s_add_u32 m0,s88,57344
ds_read_b128 v[56:59],v1 offset:7168
v_lshlrev_b32 v60,16,v72
v_mfma_f32_16x16x32_bf16 v[16:19],a[4:7],v[28:31],0)"
R"(
s_max_u32 s73,s47,s65
v_and_b32 v61,0xffff0000,v72
v_lshlrev_b32 v62,16,v73
v_mfma_f32_16x16x32_bf16 v[12:15],a[8:11],v[32:35],v[12:15]
s_mov_b32 s65,0
ds_read_b128 v[28:31],v1 offset:8192
v_and_b32 v63,0xffff0000,v73
v_mfma_f32_16x16x32_bf16 v[16:19],a[12:15],v[32:35],v[16:19]
s_waitcnt lgkmcnt(5)
buffer_load_dwordx4 v0,s[36:39],s61 offen offset:0 sc1 lds
v_mfma_f32_16x16x32_bf16 v[12:15],a[16:19],v[36:39],v[12:15]
s_add_u32 s53,s52,s55
ds_read_b128 v[32:35],v1 offset:9216
v_lshlrev_b32 v64,16,v74
v_mfma_f32_16x16x32_bf16 v[16:19],a[20:23],v[36:39],v[16:19]
s_cmp_eq_u32 s53,s56
v_and_b32 v65,0xffff0000,v74
v_lshlrev_b32 v66,16,v75
v_mfma_f32_16x16x32_bf16 v[12:15],a[24:27],v[40:43],v[12:15]
s_cselect_b32 s53,s57,s53
ds_read_b128 v[36:39],v1 offset:10240
v_and_b32 v67,0xffff0000,v75
v_mfma_f32_16x16x32_bf16 v[16:19],a[28:31],v[40:43],v[16:19]
s_waitcnt lgkmcnt(5)
buffer_load_dwordx4 v0,s[36:39],s61 offen offset:1024 sc1 lds
v_mfma_f32_16x16x32_bf16 v[12:15],a[32:35],v[44:47],v[12:15]
s_cmp_eq_u32 s53,s58
ds_read_b128 v[40:43],v1 offset:11264
v_add_f32 v20,v20,v60
v_mfma_f32_16x16x32_bf16 v[16:19],a[36:39],v[44:47],v[16:19]
s_cselect_b32 s53,s59,s53
v_add_f32 v21,v21,v61
v_add_f32 v22,v22,v62
v_mfma_f32_16x16x32_bf16 v[12:15],a[40:43],v[48:51],v[12:15]
ds_read_b128 v[44:47],v1 offset:12288
v_add_f32 v23,v23,v63
v_mfma_f32_16x16x32_bf16 v[16:19],a[44:47],v[48:51],v[16:19]
s_waitcnt lgkmcnt(5)
buffer_load_dwordx4 v0,s[36:39],s61 offen offset:2048 sc1 lds
v_mfma_f32_16x16x32_bf16 v[12:15],a[48:51],v[52:55],v[12:15]
ds_read_b128 v[48:51],v1 offset:13312
v_add_f32 v24,v24,v64
v_mfma_f32_16x16x32_bf16 v[16:19],a[52:55],v[52:55],v[16:19]
v_add_f32 v25,v25,v65
v_add_f32 v26,v26,v66
v_mfma_f32_16x16x32_bf16 v[12:15],a[56:59],v[56:59],v[12:15]
ds_read_b128 v[52:55],v1 offset:14336
v_add_f32 v27,v27,v67
v_mfma_f32_16x16x32_bf16 v[16:19],a[60:63],v[56:59],v[16:19]
s_waitcnt lgkmcnt(5)
buffer_load_dwordx4 v0,s[36:39],s61 offen offset:3072 sc1 lds
v_mfma_f32_16x16x32_bf16 v[12:15],a[64:67],v[28:31],v[12:15]
s_add_u32 s61,s61,4096
ds_read_b128 v[56:59],v1 offset:15360
v_cvt_pk_bf16_f32 v60,v20,v21
v_mfma_f32_16x16x32_bf16 v[16:19],a[68:71],v[28:31],v[16:19]
s_add_u32 m0,s88,61440
v_cvt_pk_bf16_f32 v61,v22,v23
v_cvt_pk_bf16_f32 v62,v24,v25
v_mfma_f32_16x16x32_bf16 v[12:15],a[72:75],v[32:35],v[12:15]
s_cmp_ge_u32 s66,s59
ds_read_b128 v[28:31],v1 offset:16384
v_cvt_pk_bf16_f32 v63,v26,v27
v_mfma_f32_16x16x32_bf16 v[16:19],a[76:79],v[32:35],v[16:19]
s_waitcnt lgkmcnt(5)
buffer_store_dwordx4 v[60:63],v3,s[36:39],s73 offen offset:0 sc0 sc1
s_cselect_b32 s86,s49,s50
v_mfma_f32_16x16x32_bf16 v[12:15],a[80:83],v[36:39],v[12:15]
ds_read_b128 v[32:35],v1 offset:17408
v_mfma_f32_16x16x32_bf16 v[16:19],a[84:87],v[36:39],v[16:19]
v_mfma_f32_16x16x32_bf16 v[12:15],a[88:91],v[40:43],v[12:15]
ds_read_b128 v[36:39],v1 offset:18432
v_mfma_f32_16x16x32_bf16 v[16:19],a[92:95],v[40:43],v[16:19]
s_waitcnt lgkmcnt(5)
buffer_load_dwordx4 v0,s[36:39],s61 offen offset:0 sc1 lds
v_mfma_f32_16x16x32_bf16 v[12:15],a[96:99],v[44:47],v[12:15]
ds_read_b128 v[40:43],v1 offset:19456
v_mfma_f32_16x16x32_bf16 v[16:19],a[100:103],v[44:47],v[16:19]
v_mfma_f32_16x16x32_bf16 v[12:15],a[104:107],v[48:51],v[12:15]
ds_read_b128 v[44:47],v1 offset:20480
v_mfma_f32_16x16x32_bf16 v[16:19],a[108:111],v[48:51],v[16:19]
s_waitcnt lgkmcnt(5)
buffer_load_dwordx4 v0,s[36:39],s61 offen offset:1024 sc1 lds
v_mfma_f32_16x16x32_bf16 v[12:15],a[112:115],v[52:55],v[12:15]
ds_read_b128 v[48:51],v1 offset:21504
v_mfma_f32_16x16x32_bf16 v[16:19],a[116:119],v[52:55],v[16:19]
v_mfma_f32_16x16x32_bf16 v[12:15],a[120:123],v[56:59],v[12:15]
ds_read_b128 v[52:55],v1 offset:22528
v_mfma_f32_16x16x32_bf16 v[16:19],a[124:127],v[56:59],v[16:19]
s_waitcnt lgkmcnt(5)
buffer_load_dwordx4 v0,s[36:39],s61 offen offset:2048 sc1 lds
v_mfma_f32_16x16x32_bf16 v[12:15],a[128:131],v[28:31],v[12:15]
s_add_u32 s61,s61,24576
ds_read_b128 v[56:59],v1 offset:23552
s_add_u32 m0,s88,86016
v_mfma_f32_16x16x32_bf16 v[16:19],a[132:135],v[28:31],v[16:19]
v_mfma_f32_16x16x32_bf16 v[12:15],a[136:139],v[32:35],v[12:15]
ds_read_b128 v[28:31],v1 offset:24576
v_mfma_f32_16x16x32_bf16 v[16:19],a[140:143],v[32:35],v[16:19]
s_waitcnt lgkmcnt(5)
buffer_load_dwordx4 v[68:71],v8,s[36:39],s48 offen offset:0 sc1
v_mfma_f32_16x16x32_bf16 v[12:15],a[144:147],v[36:39],v[12:15]
ds_read_b128 v[32:35],v1 offset:25600
v_mfma_f32_16x16x32_bf16 v[16:19],a[148:151],v[36:39],v[16:19]
v_mfma_f32_16x16x32_bf16 v[12:15],a[152:155],v[40:43],v[12:15]
ds_read_b128 v[36:39],v1 offset:26624
v_mfma_f32_16x16x32_bf16 v[16:19],a[156:159],v[40:43],v[16:19]
s_waitcnt lgkmcnt(5)
v_mfma_f32_16x16x32_bf16 v[12:15],a[160:163],v[44:47],v[12:15]
s_waitcnt vmcnt(9)
ds_read_b128 v[40:43],v1 offset:27648
v_mfma_f32_16x16x32_bf16 v[16:19],a[164:167],v[44:47],v[16:19]
s_barrier
v_mfma_f32_16x16x32_bf16 v[12:15],a[168:171],v[48:51],v[12:15]
ds_read_b128 v[44:47],v1 offset:28672
v_mfma_f32_16x16x32_bf16 v[16:19],a[172:175],v[48:51],v[16:19]
s_waitcnt lgkmcnt(5)
v_mfma_f32_16x16x32_bf16 v[12:15],a[176:179],v[52:55],v[12:15]
ds_read_b128 v[48:51],v1 offset:29696
v_mfma_f32_16x16x32_bf16 v[16:19],a[180:183],v[52:55],v[16:19]
v_mfma_f32_16x16x32_bf16 v[12:15],a[184:187],v[56:59],v[12:15]
buffer_load_dwordx4 v0,s[36:39],s61 offen offset:0 sc1 lds
v_mfma_f32_16x16x32_bf16 v[16:19],a[188:191],v[56:59],v[16:19]
s_waitcnt lgkmcnt(4)
ds_read_b128 v[52:55],v1 offset:30720
v_mfma_f32_16x16x32_bf16 v[12:15],a[192:195],v[28:31],v[12:15]
ds_read_b128 v[56:59],v1 offset:31744
v_mfma_f32_16x16x32_bf16 v[16:19],a[196:199],v[28:31],v[16:19]
v_mfma_f32_16x16x32_bf16 v[12:15],a[200:203],v[32:35],v[12:15]
buffer_load_dwordx4 v0,s[36:39],s61 offen offset:1024 sc1 lds
v_mfma_f32_16x16x32_bf16 v[16:19],a[204:207],v[32:35],v[16:19]
s_waitcnt lgkmcnt(4)
ds_read_b128 v[28:31],v1 offset:32768
v_mfma_f32_16x16x32_bf16 v[12:15],a[208:211],v[36:39],v[12:15]
ds_read_b128 v[32:35],v1 offset:33792
v_mfma_f32_16x16x32_bf16 v[16:19],a[212:215],v[36:39],v[16:19]
v_mfma_f32_16x16x32_bf16 v[12:15],a[216:219],v[40:43],v[12:15]
buffer_load_dwordx4 v0,s[36:39],s61 offen offset:2048 sc1 lds
v_mfma_f32_16x16x32_bf16 v[16:19],a[220:223],v[40:43],v[16:19]
s_waitcnt lgkmcnt(4)
ds_read_b128 v[36:39],v1 offset:34816
v_mfma_f32_16x16x32_bf16 v[12:15],a[224:227],v[44:47],v[12:15]
ds_read_b128 v[40:43],v1 offset:35840
v_mfma_f32_16x16x32_bf16 v[16:19],a[228:231],v[44:47],v[16:19]
v_mfma_f32_16x16x32_bf16 v[12:15],a[232:235],v[48:51],v[12:15]
buffer_load_dwordx4 v0,s[36:39],s61 offen offset:3072 sc1 lds
v_mfma_f32_16x16x32_bf16 v[16:19],a[236:239],v[48:51],v[16:19]
s_waitcnt lgkmcnt(4)
ds_read_b128 v[44:47],v1 offset:36864
s_add_u32 s61,s61,4096
v_mfma_f32_16x16x32_bf16 v[12:15],a[240:243],v[52:55],v[12:15]
s_add_u32 m0,s88,90112
ds_read_b128 v[48:51],v1 offset:37888
v_mfma_f32_16x16x32_bf16 v[16:19],a[244:247],v[52:55],v[16:19]
v_mfma_f32_16x16x32_bf16 v[12:15],a[248:251],v[56:59],v[12:15]
buffer_load_dwordx4 v0,s[36:39],s61 offen offset:0 sc1 lds
v_mfma_f32_16x16x32_bf16 v[16:19],a[252:255],v[56:59],v[16:19]
s_waitcnt lgkmcnt(4)
ds_read_b128 v[52:55],v1 offset:38912
v_mfma_f32_16x16x32_bf16 v[12:15],v[108:111],v[28:31],v[12:15]
ds_read_b128 v[56:59],v1 offset:39936
v_mfma_f32_16x16x32_bf16 v[16:19],v[112:115],v[28:31],v[16:19]
v_mfma_f32_16x16x32_bf16 v[12:15],v[116:119],v[32:35],v[12:15]
buffer_load_dwordx4 v0,s[36:39],s61 offen offset:1024 sc1 lds
v_mfma_f32_16x16x32_bf16 v[16:19],v[120:123],v[32:35],v[16:19]
s_waitcnt lgkmcnt(4)
ds_read_b128 v[28:31],v1 offset:40960
v_mfma_f32_16x16x32_bf16 v[12:15],v[124:127],v[36:39],v[12:15]
ds_read_b128 v[32:35],v1 offset:41984
v_mfma_f32_16x16x32_bf16 v[16:19],v[128:131],v[36:39],v[16:19]
v_mfma_f32_16x16x32_bf16 v[12:15],v[132:135],v[40:43],v[12:15]
buffer_load_dwordx4 v0,s[36:39],s61 offen offset:2048 sc1 lds
v_mfma_f32_16x16x32_bf16 v[16:19],v[136:139],v[40:43],v[16:19]
s_waitcnt lgkmcnt(4)
ds_read_b128 v[36:39],v1 offset:43008
v_mfma_f32_16x16x32_bf16 v[12:15],v[140:143],v[44:47],v[12:15]
ds_read_b128 v[40:43],v1 offset:44032
v_mfma_f32_16x16x32_bf16 v[16:19],v[144:147],v[44:47],v[16:19]
v_mfma_f32_16x16x32_bf16 v[12:15],v[148:151],v[48:51],v[12:15]
ds_read_b128 v[44:47],v1 offset:45056
v_mfma_f32_16x16x32_bf16 v[16:19],v[152:155],v[48:51],v[16:19]
s_waitcnt lgkmcnt(5)
v_mfma_f32_16x16x32_bf16 v[12:15],v[156:159],v[52:55],v[12:15]
ds_read_b128 v[48:51],v1 offset:46080
v_mfma_f32_16x16x32_bf16 v[16:19],v[160:163],v[52:55],v[16:19]
v_mfma_f32_16x16x32_bf16 v[12:15],v[164:167],v[56:59],v[12:15]
ds_read_b128 v[52:55],v1 offset:47104
v_mfma_f32_16x16x32_bf16 v[16:19],v[168:171],v[56:59],v[16:19]
s_waitcnt lgkmcnt(5)
v_mfma_f32_16x16x32_bf16 v[12:15],v[172:175],v[28:31],v[12:15]
ds_read_b128 v[56:59],v1 offset:48128
v_mfma_f32_16x16x32_bf16 v[16:19],v[176:179],v[28:31],v[16:19]
ds_read_b128 v[88:91],v9 offset:0
v_mfma_f32_16x16x32_bf16 v[12:15],v[180:183],v[32:35],v[12:15]
ds_read_b128 v[28:31],v1 offset:49152
v_mfma_f32_16x16x32_bf16 v[16:19],v[184:187],v[32:35],v[16:19]
s_waitcnt lgkmcnt(6)
ds_read_b128 v[92:95],v9 offset:1024
v_mfma_f32_16x16x32_bf16 v[12:15],v[188:191],v[36:39],v[12:15]
ds_read_b128 v[32:35],v1 offset:50176)"
R"(
v_mfma_f32_16x16x32_bf16 v[16:19],v[192:195],v[36:39],v[16:19]
ds_read_b128 v[96:99],v9 offset:2048
v_mfma_f32_16x16x32_bf16 v[12:15],v[196:199],v[40:43],v[12:15]
ds_read_b128 v[36:39],v1 offset:51200
v_mfma_f32_16x16x32_bf16 v[16:19],v[200:203],v[40:43],v[16:19]
s_waitcnt lgkmcnt(5)
ds_read_b128 v[76:79],v9 offset:3072
v_mfma_f32_16x16x32_bf16 v[12:15],v[204:207],v[44:47],v[12:15]
ds_read_b128 v[40:43],v1 offset:52224
v_mfma_f32_16x16x32_bf16 v[16:19],v[208:211],v[44:47],v[16:19]
ds_read_b128 v[80:83],v9 offset:4096
v_mfma_f32_16x16x32_bf16 v[12:15],v[212:215],v[48:51],v[12:15]
ds_read_b128 v[44:47],v1 offset:53248
v_mfma_f32_16x16x32_bf16 v[16:19],v[88:91],v[48:51],v[16:19]
s_waitcnt lgkmcnt(5)
ds_read_b128 v[84:87],v9 offset:5120
v_mfma_f32_16x16x32_bf16 v[12:15],v[216:219],v[52:55],v[12:15]
ds_read_b128 v[48:51],v1 offset:54272
v_mfma_f32_16x16x32_bf16 v[16:19],v[92:95],v[52:55],v[16:19]
ds_read_b128 v[88:91],v9 offset:6144
v_mfma_f32_16x16x32_bf16 v[12:15],v[220:223],v[56:59],v[12:15]
ds_read_b128 v[52:55],v1 offset:55296
v_mfma_f32_16x16x32_bf16 v[16:19],v[96:99],v[56:59],v[16:19]
s_waitcnt lgkmcnt(5)
ds_read_b128 v[92:95],v9 offset:7168
v_mfma_f32_16x16x32_bf16 v[12:15],v[224:227],v[28:31],v[12:15]
s_waitcnt vmcnt(7)
ds_read_b128 v[56:59],v1 offset:56320
ds_read_b128 v[96:99],v9 offset:8192
v_mfma_f32_16x16x32_bf16 v[16:19],v[76:79],v[28:31],v[16:19]
s_barrier
v_mfma_f32_16x16x32_bf16 v[12:15],v[228:231],v[32:35],v[12:15]
ds_read_b128 v[28:31],v2 offset:0
ds_read_b32 v10,v7 offset:3072
v_mfma_f32_16x16x32_bf16 v[16:19],v[80:83],v[32:35],v[16:19]
s_waitcnt lgkmcnt(6)
ds_read_b128 v[76:79],v9 offset:9216
v_mfma_f32_16x16x32_bf16 v[12:15],v[232:235],v[36:39],v[12:15]
ds_read_b128 v[32:35],v2 offset:1024
v_mfma_f32_16x16x32_bf16 v[16:19],v[84:87],v[36:39],v[16:19]
ds_read_b128 v[80:83],v9 offset:10240
v_mfma_f32_16x16x32_bf16 v[12:15],v[236:239],v[40:43],v[12:15]
s_waitcnt lgkmcnt(3)
ds_read_b128 v[36:39],v2 offset:2048
v_mfma_f32_16x16x32_bf16 v[16:19],v[88:91],v[40:43],v[16:19]
v_cmp_eq_u32 vcc,s60,v10
v_mfma_f32_16x16x32_bf16 v[12:15],v[240:243],v[44:47],v[12:15]
s_cmp_eq_u32 vcc_lo,-1
ds_read_b128 v[40:43],v2 offset:3072
s_cselect_b32 s67,s86,s59
v_mfma_f32_16x16x32_bf16 v[16:19],v[92:95],v[44:47],v[16:19]
s_mul_i32 s61,s67,7
s_lshr_b32 s61,s61,1
s_cmp_ge_u32 s66,s59
v_mfma_f32_16x16x32_bf16 v[12:15],v[244:247],v[48:51],v[12:15]
ds_read_b128 v[44:47],v2 offset:4096
v_mfma_f32_16x16x32_bf16 v[16:19],v[96:99],v[48:51],v[16:19]
s_waitcnt lgkmcnt(3)
v_mfma_f32_16x16x32_bf16 v[12:15],v[248:251],v[52:55],v[12:15]
ds_read_b128 v[48:51],v2 offset:5120
v_mfma_f32_16x16x32_bf16 v[16:19],v[76:79],v[52:55],v[16:19]
v_mfma_f32_16x16x32_bf16 v[12:15],v[252:255],v[56:59],v[12:15]
ds_read_b128 v[52:55],v2 offset:6144
v_mfma_f32_16x16x32_bf16 v[16:19],v[80:83],v[56:59],v[16:19]
s_cbranch_scc1 Lg4_slow1_%=
Lg4_d1w2_%=:
v_mfma_f32_16x16x32_bf16 v[20:23],a[0:3],v[28:31],0
s_add_u32 m0,s88,0
ds_read_b128 v[56:59],v2 offset:7168
v_lshlrev_b32 v60,16,v68
v_mfma_f32_16x16x32_bf16 v[24:27],a[4:7],v[28:31],0
s_max_u32 s73,s48,s65
v_and_b32 v61,0xffff0000,v68
v_lshlrev_b32 v62,16,v69
v_mfma_f32_16x16x32_bf16 v[20:23],a[8:11],v[32:35],v[20:23]
s_mov_b32 s65,0
ds_read_b128 v[28:31],v2 offset:8192
v_and_b32 v63,0xffff0000,v69
v_mfma_f32_16x16x32_bf16 v[24:27],a[12:15],v[32:35],v[24:27]
s_waitcnt lgkmcnt(5)
buffer_load_dwordx4 v0,s[36:39],s61 offen offset:0 sc1 lds
v_mfma_f32_16x16x32_bf16 v[20:23],a[16:19],v[36:39],v[20:23]
s_add_u32 s74,s53,s55
ds_read_b128 v[32:35],v2 offset:9216
v_lshlrev_b32 v64,16,v70
v_mfma_f32_16x16x32_bf16 v[24:27],a[20:23],v[36:39],v[24:27]
s_cmp_eq_u32 s74,s56
v_and_b32 v65,0xffff0000,v70
v_lshlrev_b32 v66,16,v71
v_mfma_f32_16x16x32_bf16 v[20:23],a[24:27],v[40:43],v[20:23]
s_cselect_b32 s74,s57,s74
ds_read_b128 v[36:39],v2 offset:10240
v_and_b32 v67,0xffff0000,v71
v_mfma_f32_16x16x32_bf16 v[24:27],a[28:31],v[40:43],v[24:27]
s_waitcnt lgkmcnt(5)
buffer_load_dwordx4 v0,s[36:39],s61 offen offset:1024 sc1 lds
v_mfma_f32_16x16x32_bf16 v[20:23],a[32:35],v[44:47],v[20:23]
s_cmp_eq_u32 s74,s58
ds_read_b128 v[40:43],v2 offset:11264
v_add_f32 v12,v12,v60
v_mfma_f32_16x16x32_bf16 v[24:27],a[36:39],v[44:47],v[24:27]
s_cselect_b32 s74,s59,s74
v_add_f32 v13,v13,v61
v_add_f32 v14,v14,v62
v_mfma_f32_16x16x32_bf16 v[20:23],a[40:43],v[48:51],v[20:23]
s_mov_b64 s[44:45],s[46:47]
ds_read_b128 v[44:47],v2 offset:12288
v_add_f32 v15,v15,v63
v_mfma_f32_16x16x32_bf16 v[24:27],a[44:47],v[48:51],v[24:27]
s_waitcnt lgkmcnt(5)
buffer_load_dwordx4 v0,s[36:39],s61 offen offset:2048 sc1 lds
v_mfma_f32_16x16x32_bf16 v[20:23],a[48:51],v[52:55],v[20:23]
s_mov_b64 s[46:47],s[48:49]
ds_read_b128 v[48:51],v2 offset:13312
v_add_f32 v16,v16,v64
v_mfma_f32_16x16x32_bf16 v[24:27],a[52:55],v[52:55],v[24:27]
v_add_f32 v17,v17,v65
v_add_f32 v18,v18,v66
v_mfma_f32_16x16x32_bf16 v[20:23],a[56:59],v[56:59],v[20:23]
ds_read_b128 v[52:55],v2 offset:14336
v_add_f32 v19,v19,v67
v_mfma_f32_16x16x32_bf16 v[24:27],a[60:63],v[56:59],v[24:27]
s_waitcnt lgkmcnt(5)
buffer_load_dwordx4 v0,s[36:39],s61 offen offset:3072 sc1 lds
v_mfma_f32_16x16x32_bf16 v[20:23],a[64:67],v[28:31],v[20:23]
s_add_u32 s61,s61,4096
ds_read_b128 v[56:59],v2 offset:15360
v_cvt_pk_bf16_f32 v60,v12,v13
v_mfma_f32_16x16x32_bf16 v[24:27],a[68:71],v[28:31],v[24:27]
s_add_u32 m0,s88,4096
v_cvt_pk_bf16_f32 v61,v14,v15
v_cvt_pk_bf16_f32 v62,v16,v17
v_mfma_f32_16x16x32_bf16 v[20:23],a[72:75],v[32:35],v[20:23]
s_cmp_ge_u32 s67,s59
ds_read_b128 v[28:31],v2 offset:16384
v_cvt_pk_bf16_f32 v63,v18,v19
v_mfma_f32_16x16x32_bf16 v[24:27],a[76:79],v[32:35],v[24:27]
s_waitcnt lgkmcnt(5)
buffer_store_dwordx4 v[60:63],v3,s[36:39],s73 offen offset:0 sc0 sc1
s_cselect_b32 s86,s50,s51
v_mfma_f32_16x16x32_bf16 v[20:23],a[80:83],v[36:39],v[20:23]
ds_read_b128 v[32:35],v2 offset:17408
v_mfma_f32_16x16x32_bf16 v[24:27],a[84:87],v[36:39],v[24:27]
v_mfma_f32_16x16x32_bf16 v[20:23],a[88:91],v[40:43],v[20:23]
ds_read_b128 v[36:39],v2 offset:18432
v_mfma_f32_16x16x32_bf16 v[24:27],a[92:95],v[40:43],v[24:27]
s_waitcnt lgkmcnt(5)
buffer_load_dwordx4 v0,s[36:39],s61 offen offset:0 sc1 lds
v_mfma_f32_16x16x32_bf16 v[20:23],a[96:99],v[44:47],v[20:23]
ds_read_b128 v[40:43],v2 offset:19456
v_mfma_f32_16x16x32_bf16 v[24:27],a[100:103],v[44:47],v[24:27]
v_mfma_f32_16x16x32_bf16 v[20:23],a[104:107],v[48:51],v[20:23]
ds_read_b128 v[44:47],v2 offset:20480
v_mfma_f32_16x16x32_bf16 v[24:27],a[108:111],v[48:51],v[24:27]
s_waitcnt lgkmcnt(5)
buffer_load_dwordx4 v0,s[36:39],s61 offen offset:1024 sc1 lds
v_mfma_f32_16x16x32_bf16 v[20:23],a[112:115],v[52:55],v[20:23]
ds_read_b128 v[48:51],v2 offset:21504
v_mfma_f32_16x16x32_bf16 v[24:27],a[116:119],v[52:55],v[24:27]
v_mfma_f32_16x16x32_bf16 v[20:23],a[120:123],v[56:59],v[20:23]
ds_read_b128 v[52:55],v2 offset:22528
v_mfma_f32_16x16x32_bf16 v[24:27],a[124:127],v[56:59],v[24:27]
s_waitcnt lgkmcnt(5)
buffer_load_dwordx4 v0,s[36:39],s61 offen offset:2048 sc1 lds
v_mfma_f32_16x16x32_bf16 v[20:23],a[128:131],v[28:31],v[20:23]
s_add_u32 s61,s61,24576
ds_read_b128 v[56:59],v2 offset:23552
s_add_u32 m0,s88,28672
v_mfma_f32_16x16x32_bf16 v[24:27],a[132:135],v[28:31],v[24:27]
v_mfma_f32_16x16x32_bf16 v[20:23],a[136:139],v[32:35],v[20:23]
ds_read_b128 v[28:31],v2 offset:24576
v_mfma_f32_16x16x32_bf16 v[24:27],a[140:143],v[32:35],v[24:27]
s_waitcnt lgkmcnt(5)
buffer_load_dwordx4 v[72:75],v8,s[36:39],s49 offen offset:0 sc1
v_mfma_f32_16x16x32_bf16 v[20:23],a[144:147],v[36:39],v[20:23]
s_mov_b64 s[48:49],s[50:51]
ds_read_b128 v[32:35],v2 offset:25600
s_mov_b64 s[50:51],s[52:53]
v_mfma_f32_16x16x32_bf16 v[24:27],a[148:151],v[36:39],v[24:27]
s_mov_b32 s52,s74
v_mfma_f32_16x16x32_bf16 v[20:23],a[152:155],v[40:43],v[20:23]
ds_read_b128 v[36:39],v2 offset:26624
v_mfma_f32_16x16x32_bf16 v[24:27],a[156:159],v[40:43],v[24:27]
s_waitcnt lgkmcnt(5)
v_mfma_f32_16x16x32_bf16 v[20:23],a[160:163],v[44:47],v[20:23]
s_waitcnt vmcnt(9)
ds_read_b128 v[40:43],v2 offset:27648
v_mfma_f32_16x16x32_bf16 v[24:27],a[164:167],v[44:47],v[24:27]
s_barrier
v_mfma_f32_16x16x32_bf16 v[20:23],a[168:171],v[48:51],v[20:23]
ds_read_b128 v[44:47],v2 offset:28672
v_mfma_f32_16x16x32_bf16 v[24:27],a[172:175],v[48:51],v[24:27]
s_waitcnt lgkmcnt(5)
v_mfma_f32_16x16x32_bf16 v[20:23],a[176:179],v[52:55],v[20:23]
ds_read_b128 v[48:51],v2 offset:29696
v_mfma_f32_16x16x32_bf16 v[24:27],a[180:183],v[52:55],v[24:27]
v_mfma_f32_16x16x32_bf16 v[20:23],a[184:187],v[56:59],v[20:23]
buffer_load_dwordx4 v0,s[36:39],s61 offen offset:0 sc1 lds
v_mfma_f32_16x16x32_bf16 v[24:27],a[188:191],v[56:59],v[24:27]
s_waitcnt lgkmcnt(4))"
R"(
ds_read_b128 v[52:55],v2 offset:30720
v_mfma_f32_16x16x32_bf16 v[20:23],a[192:195],v[28:31],v[20:23]
ds_read_b128 v[56:59],v2 offset:31744
v_mfma_f32_16x16x32_bf16 v[24:27],a[196:199],v[28:31],v[24:27]
v_mfma_f32_16x16x32_bf16 v[20:23],a[200:203],v[32:35],v[20:23]
buffer_load_dwordx4 v0,s[36:39],s61 offen offset:1024 sc1 lds
v_mfma_f32_16x16x32_bf16 v[24:27],a[204:207],v[32:35],v[24:27]
s_waitcnt lgkmcnt(4)
ds_read_b128 v[28:31],v2 offset:32768
v_mfma_f32_16x16x32_bf16 v[20:23],a[208:211],v[36:39],v[20:23]
ds_read_b128 v[32:35],v2 offset:33792
v_mfma_f32_16x16x32_bf16 v[24:27],a[212:215],v[36:39],v[24:27]
v_mfma_f32_16x16x32_bf16 v[20:23],a[216:219],v[40:43],v[20:23]
buffer_load_dwordx4 v0,s[36:39],s61 offen offset:2048 sc1 lds
v_mfma_f32_16x16x32_bf16 v[24:27],a[220:223],v[40:43],v[24:27]
s_waitcnt lgkmcnt(4)
ds_read_b128 v[36:39],v2 offset:34816
v_mfma_f32_16x16x32_bf16 v[20:23],a[224:227],v[44:47],v[20:23]
ds_read_b128 v[40:43],v2 offset:35840
v_mfma_f32_16x16x32_bf16 v[24:27],a[228:231],v[44:47],v[24:27]
v_mfma_f32_16x16x32_bf16 v[20:23],a[232:235],v[48:51],v[20:23]
buffer_load_dwordx4 v0,s[36:39],s61 offen offset:3072 sc1 lds
v_mfma_f32_16x16x32_bf16 v[24:27],a[236:239],v[48:51],v[24:27]
s_waitcnt lgkmcnt(4)
ds_read_b128 v[44:47],v2 offset:36864
s_add_u32 s61,s61,4096
v_mfma_f32_16x16x32_bf16 v[20:23],a[240:243],v[52:55],v[20:23]
s_add_u32 m0,s88,32768
ds_read_b128 v[48:51],v2 offset:37888
v_mfma_f32_16x16x32_bf16 v[24:27],a[244:247],v[52:55],v[24:27]
v_mfma_f32_16x16x32_bf16 v[20:23],a[248:251],v[56:59],v[20:23]
buffer_load_dwordx4 v0,s[36:39],s61 offen offset:0 sc1 lds
v_mfma_f32_16x16x32_bf16 v[24:27],a[252:255],v[56:59],v[24:27]
s_waitcnt lgkmcnt(4)
ds_read_b128 v[52:55],v2 offset:38912
v_mfma_f32_16x16x32_bf16 v[20:23],v[108:111],v[28:31],v[20:23]
ds_read_b128 v[56:59],v2 offset:39936
v_mfma_f32_16x16x32_bf16 v[24:27],v[112:115],v[28:31],v[24:27]
v_mfma_f32_16x16x32_bf16 v[20:23],v[116:119],v[32:35],v[20:23]
buffer_load_dwordx4 v0,s[36:39],s61 offen offset:1024 sc1 lds
v_mfma_f32_16x16x32_bf16 v[24:27],v[120:123],v[32:35],v[24:27]
s_waitcnt lgkmcnt(4)
ds_read_b128 v[28:31],v2 offset:40960
v_mfma_f32_16x16x32_bf16 v[20:23],v[124:127],v[36:39],v[20:23]
ds_read_b128 v[32:35],v2 offset:41984
v_mfma_f32_16x16x32_bf16 v[24:27],v[128:131],v[36:39],v[24:27]
v_mfma_f32_16x16x32_bf16 v[20:23],v[132:135],v[40:43],v[20:23]
buffer_load_dwordx4 v0,s[36:39],s61 offen offset:2048 sc1 lds
v_mfma_f32_16x16x32_bf16 v[24:27],v[136:139],v[40:43],v[24:27]
s_waitcnt lgkmcnt(4)
ds_read_b128 v[36:39],v2 offset:43008
v_mfma_f32_16x16x32_bf16 v[20:23],v[140:143],v[44:47],v[20:23]
ds_read_b128 v[40:43],v2 offset:44032
v_mfma_f32_16x16x32_bf16 v[24:27],v[144:147],v[44:47],v[24:27]
v_mfma_f32_16x16x32_bf16 v[20:23],v[148:151],v[48:51],v[20:23]
ds_read_b128 v[44:47],v2 offset:45056
v_mfma_f32_16x16x32_bf16 v[24:27],v[152:155],v[48:51],v[24:27]
s_waitcnt lgkmcnt(5)
v_mfma_f32_16x16x32_bf16 v[20:23],v[156:159],v[52:55],v[20:23]
ds_read_b128 v[48:51],v2 offset:46080
v_mfma_f32_16x16x32_bf16 v[24:27],v[160:163],v[52:55],v[24:27]
v_mfma_f32_16x16x32_bf16 v[20:23],v[164:167],v[56:59],v[20:23]
ds_read_b128 v[52:55],v2 offset:47104
v_mfma_f32_16x16x32_bf16 v[24:27],v[168:171],v[56:59],v[24:27]
s_waitcnt lgkmcnt(5)
v_mfma_f32_16x16x32_bf16 v[20:23],v[172:175],v[28:31],v[20:23]
ds_read_b128 v[56:59],v2 offset:48128
v_mfma_f32_16x16x32_bf16 v[24:27],v[176:179],v[28:31],v[24:27]
ds_read_b128 v[88:91],v9 offset:0
v_mfma_f32_16x16x32_bf16 v[20:23],v[180:183],v[32:35],v[20:23]
ds_read_b128 v[28:31],v2 offset:49152
v_mfma_f32_16x16x32_bf16 v[24:27],v[184:187],v[32:35],v[24:27]
s_waitcnt lgkmcnt(6)
ds_read_b128 v[92:95],v9 offset:1024
v_mfma_f32_16x16x32_bf16 v[20:23],v[188:191],v[36:39],v[20:23]
ds_read_b128 v[32:35],v2 offset:50176
v_mfma_f32_16x16x32_bf16 v[24:27],v[192:195],v[36:39],v[24:27]
ds_read_b128 v[96:99],v9 offset:2048
v_mfma_f32_16x16x32_bf16 v[20:23],v[196:199],v[40:43],v[20:23]
ds_read_b128 v[36:39],v2 offset:51200
v_mfma_f32_16x16x32_bf16 v[24:27],v[200:203],v[40:43],v[24:27]
s_waitcnt lgkmcnt(5)
ds_read_b128 v[76:79],v9 offset:3072
v_mfma_f32_16x16x32_bf16 v[20:23],v[204:207],v[44:47],v[20:23]
ds_read_b128 v[40:43],v2 offset:52224
v_mfma_f32_16x16x32_bf16 v[24:27],v[208:211],v[44:47],v[24:27]
ds_read_b128 v[80:83],v9 offset:4096
v_mfma_f32_16x16x32_bf16 v[20:23],v[212:215],v[48:51],v[20:23]
ds_read_b128 v[44:47],v2 offset:53248
v_mfma_f32_16x16x32_bf16 v[24:27],v[88:91],v[48:51],v[24:27]
s_waitcnt lgkmcnt(5)
ds_read_b128 v[84:87],v9 offset:5120
v_mfma_f32_16x16x32_bf16 v[20:23],v[216:219],v[52:55],v[20:23]
ds_read_b128 v[48:51],v2 offset:54272
v_mfma_f32_16x16x32_bf16 v[24:27],v[92:95],v[52:55],v[24:27]
ds_read_b128 v[88:91],v9 offset:6144
v_mfma_f32_16x16x32_bf16 v[20:23],v[220:223],v[56:59],v[20:23]
ds_read_b128 v[52:55],v2 offset:55296
v_mfma_f32_16x16x32_bf16 v[24:27],v[96:99],v[56:59],v[24:27]
s_waitcnt lgkmcnt(5)
ds_read_b128 v[92:95],v9 offset:7168
v_mfma_f32_16x16x32_bf16 v[20:23],v[224:227],v[28:31],v[20:23]
s_waitcnt vmcnt(7)
ds_read_b128 v[56:59],v2 offset:56320
ds_read_b128 v[96:99],v9 offset:8192
v_mfma_f32_16x16x32_bf16 v[24:27],v[76:79],v[28:31],v[24:27]
s_barrier
v_mfma_f32_16x16x32_bf16 v[20:23],v[228:231],v[32:35],v[20:23]
ds_read_b128 v[28:31],v1 offset:0
ds_read_b32 v10,v7 offset:3072
v_mfma_f32_16x16x32_bf16 v[24:27],v[80:83],v[32:35],v[24:27]
s_waitcnt lgkmcnt(6)
ds_read_b128 v[76:79],v9 offset:9216
v_mfma_f32_16x16x32_bf16 v[20:23],v[232:235],v[36:39],v[20:23]
ds_read_b128 v[32:35],v1 offset:1024
v_mfma_f32_16x16x32_bf16 v[24:27],v[84:87],v[36:39],v[24:27]
ds_read_b128 v[80:83],v9 offset:10240
v_mfma_f32_16x16x32_bf16 v[20:23],v[236:239],v[40:43],v[20:23]
s_waitcnt lgkmcnt(3)
ds_read_b128 v[36:39],v1 offset:2048
v_mfma_f32_16x16x32_bf16 v[24:27],v[88:91],v[40:43],v[24:27]
v_cmp_eq_u32 vcc,s60,v10
v_mfma_f32_16x16x32_bf16 v[20:23],v[240:243],v[44:47],v[20:23]
s_cmp_eq_u32 vcc_lo,-1
ds_read_b128 v[40:43],v1 offset:3072
s_cselect_b32 s66,s86,s59
v_mfma_f32_16x16x32_bf16 v[24:27],v[92:95],v[44:47],v[24:27]
s_mul_i32 s61,s66,7
s_lshr_b32 s61,s61,1
s_cmp_lt_u32 s67,s59
v_mfma_f32_16x16x32_bf16 v[20:23],v[244:247],v[48:51],v[20:23]
ds_read_b128 v[44:47],v1 offset:4096
v_mfma_f32_16x16x32_bf16 v[24:27],v[96:99],v[48:51],v[24:27]
s_waitcnt lgkmcnt(3)
v_mfma_f32_16x16x32_bf16 v[20:23],v[248:251],v[52:55],v[20:23]
ds_read_b128 v[48:51],v1 offset:5120
v_mfma_f32_16x16x32_bf16 v[24:27],v[76:79],v[52:55],v[24:27]
v_mfma_f32_16x16x32_bf16 v[20:23],v[252:255],v[56:59],v[20:23]
ds_read_b128 v[52:55],v1 offset:6144
v_mfma_f32_16x16x32_bf16 v[24:27],v[80:83],v[56:59],v[24:27]
s_cbranch_scc1 Lg4_d0w2_%=
s_branch Lg4_slow0_%=
Lg4_d0w3_%=:
v_mfma_f32_16x16x32_bf16 v[12:15],a[0:3],v[28:31],0
s_add_u32 m0,s88,57344
ds_read_b128 v[56:59],v1 offset:7168
v_lshlrev_b32 v60,16,v72
v_mfma_f32_16x16x32_bf16 v[16:19],a[4:7],v[28:31],0
s_max_u32 s73,s47,s65
v_and_b32 v61,0xffff0000,v72
v_lshlrev_b32 v62,16,v73
v_mfma_f32_16x16x32_bf16 v[12:15],a[8:11],v[32:35],v[12:15]
s_mov_b32 s65,0
ds_read_b128 v[28:31],v1 offset:8192
v_and_b32 v63,0xffff0000,v73
v_mfma_f32_16x16x32_bf16 v[16:19],a[12:15],v[32:35],v[16:19]
s_waitcnt lgkmcnt(5)
v_lshlrev_b32 v64,16,v74
v_and_b32 v65,0xffff0000,v74
v_mfma_f32_16x16x32_bf16 v[12:15],a[16:19],v[36:39],v[12:15]
buffer_load_dwordx4 v0,s[36:39],s61 offen offset:0 sc1 lds
v_mfma_f32_16x16x32_bf16 v[16:19],a[20:23],v[36:39],v[16:19]
s_add_u32 s53,s52,s55
ds_read_b128 v[32:35],v1 offset:9216
v_lshlrev_b32 v66,16,v75
v_mfma_f32_16x16x32_bf16 v[12:15],a[24:27],v[40:43],v[12:15]
s_cmp_eq_u32 s53,s56
ds_read_b128 v[36:39],v1 offset:10240
v_and_b32 v67,0xffff0000,v75
v_mfma_f32_16x16x32_bf16 v[16:19],a[28:31],v[40:43],v[16:19]
s_waitcnt lgkmcnt(5)
v_add_f32 v20,v20,v60
v_add_f32 v21,v21,v61
v_mfma_f32_16x16x32_bf16 v[12:15],a[32:35],v[44:47],v[12:15]
buffer_load_dwordx4 v0,s[36:39],s61 offen offset:1024 sc1 lds
v_mfma_f32_16x16x32_bf16 v[16:19],a[36:39],v[44:47],v[16:19]
s_cselect_b32 s53,s57,s53
ds_read_b128 v[40:43],v1 offset:11264
v_add_f32 v22,v22,v62
v_mfma_f32_16x16x32_bf16 v[12:15],a[40:43],v[48:51],v[12:15]
s_cmp_eq_u32 s53,s58
ds_read_b128 v[44:47],v1 offset:12288
v_add_f32 v23,v23,v63
v_mfma_f32_16x16x32_bf16 v[16:19],a[44:47],v[48:51],v[16:19]
s_waitcnt lgkmcnt(5)
v_add_f32 v24,v24,v64
v_add_f32 v25,v25,v65
v_mfma_f32_16x16x32_bf16 v[12:15],a[48:51],v[52:55],v[12:15]
buffer_load_dwordx4 v0,s[36:39],s61 offen offset:2048 sc1 lds
v_mfma_f32_16x16x32_bf16 v[16:19],a[52:55],v[52:55],v[16:19]
s_cselect_b32 s53,s59,s53
ds_read_b128 v[48:51],v1 offset:13312
v_add_f32 v26,v26,v66
v_mfma_f32_16x16x32_bf16 v[12:15],a[56:59],v[56:59],v[12:15]
ds_read_b128 v[52:55],v1 offset:14336
v_add_f32 v27,v27,v67
v_mfma_f32_16x16x32_bf16 v[16:19],a[60:63],v[56:59],v[16:19]
s_waitcnt lgkmcnt(5)
v_cvt_pk_bf16_f32 v60,v20,v21
v_cvt_pk_bf16_f32 v61,v22,v23)"
R"(
v_mfma_f32_16x16x32_bf16 v[12:15],a[64:67],v[28:31],v[12:15]
buffer_load_dwordx4 v0,s[36:39],s61 offen offset:3072 sc1 lds
v_mfma_f32_16x16x32_bf16 v[16:19],a[68:71],v[28:31],v[16:19]
s_add_u32 s61,s61,4096
ds_read_b128 v[56:59],v1 offset:15360
v_cvt_pk_bf16_f32 v62,v24,v25
v_mfma_f32_16x16x32_bf16 v[12:15],a[72:75],v[32:35],v[12:15]
s_add_u32 m0,s88,61440
ds_read_b128 v[28:31],v1 offset:16384
v_cvt_pk_bf16_f32 v63,v26,v27
v_mfma_f32_16x16x32_bf16 v[16:19],a[76:79],v[32:35],v[16:19]
s_waitcnt lgkmcnt(5)
s_cmp_ge_u32 s66,s59
s_cselect_b32 s86,s49,s50
v_mfma_f32_16x16x32_bf16 v[12:15],a[80:83],v[36:39],v[12:15]
buffer_store_dwordx4 v[60:63],v3,s[36:39],s73 offen offset:0 sc0 sc1
ds_read_b128 v[32:35],v1 offset:17408
v_mfma_f32_16x16x32_bf16 v[16:19],a[84:87],v[36:39],v[16:19]
v_mfma_f32_16x16x32_bf16 v[12:15],a[88:91],v[40:43],v[12:15]
ds_read_b128 v[36:39],v1 offset:18432
v_mfma_f32_16x16x32_bf16 v[16:19],a[92:95],v[40:43],v[16:19]
s_waitcnt lgkmcnt(5)
v_mfma_f32_16x16x32_bf16 v[12:15],a[96:99],v[44:47],v[12:15]
buffer_load_dwordx4 v0,s[36:39],s61 offen offset:0 sc1 lds
v_mfma_f32_16x16x32_bf16 v[16:19],a[100:103],v[44:47],v[16:19]
ds_read_b128 v[40:43],v1 offset:19456
v_mfma_f32_16x16x32_bf16 v[12:15],a[104:107],v[48:51],v[12:15]
ds_read_b128 v[44:47],v1 offset:20480
v_mfma_f32_16x16x32_bf16 v[16:19],a[108:111],v[48:51],v[16:19]
s_waitcnt lgkmcnt(5)
v_mfma_f32_16x16x32_bf16 v[12:15],a[112:115],v[52:55],v[12:15]
buffer_load_dwordx4 v0,s[36:39],s61 offen offset:1024 sc1 lds
v_mfma_f32_16x16x32_bf16 v[16:19],a[116:119],v[52:55],v[16:19]
ds_read_b128 v[48:51],v1 offset:21504
v_mfma_f32_16x16x32_bf16 v[12:15],a[120:123],v[56:59],v[12:15]
ds_read_b128 v[52:55],v1 offset:22528
v_mfma_f32_16x16x32_bf16 v[16:19],a[124:127],v[56:59],v[16:19]
s_waitcnt lgkmcnt(5)
v_mfma_f32_16x16x32_bf16 v[12:15],a[128:131],v[28:31],v[12:15]
buffer_load_dwordx4 v0,s[36:39],s61 offen offset:2048 sc1 lds
v_mfma_f32_16x16x32_bf16 v[16:19],a[132:135],v[28:31],v[16:19]
s_add_u32 s61,s61,24576
ds_read_b128 v[56:59],v1 offset:23552
s_add_u32 m0,s88,86016
v_mfma_f32_16x16x32_bf16 v[12:15],a[136:139],v[32:35],v[12:15]
ds_read_b128 v[28:31],v1 offset:24576
v_mfma_f32_16x16x32_bf16 v[16:19],a[140:143],v[32:35],v[16:19]
s_waitcnt lgkmcnt(5)
v_mfma_f32_16x16x32_bf16 v[12:15],a[144:147],v[36:39],v[12:15]
buffer_load_dwordx4 v[68:71],v8,s[36:39],s48 offen offset:0 sc1
v_mfma_f32_16x16x32_bf16 v[16:19],a[148:151],v[36:39],v[16:19]
ds_read_b128 v[32:35],v1 offset:25600
v_mfma_f32_16x16x32_bf16 v[12:15],a[152:155],v[40:43],v[12:15]
ds_read_b128 v[36:39],v1 offset:26624
v_mfma_f32_16x16x32_bf16 v[16:19],a[156:159],v[40:43],v[16:19]
s_waitcnt lgkmcnt(5)
v_mfma_f32_16x16x32_bf16 v[12:15],a[160:163],v[44:47],v[12:15]
s_waitcnt vmcnt(10)
ds_read_b128 v[40:43],v1 offset:27648
v_mfma_f32_16x16x32_bf16 v[16:19],a[164:167],v[44:47],v[16:19]
s_barrier
v_mfma_f32_16x16x32_bf16 v[12:15],a[168:171],v[48:51],v[12:15]
ds_read_b128 v[44:47],v1 offset:28672
v_mfma_f32_16x16x32_bf16 v[16:19],a[172:175],v[48:51],v[16:19]
s_waitcnt lgkmcnt(5)
v_mfma_f32_16x16x32_bf16 v[12:15],a[176:179],v[52:55],v[12:15]
ds_read_b128 v[48:51],v1 offset:29696
v_mfma_f32_16x16x32_bf16 v[16:19],a[180:183],v[52:55],v[16:19]
v_mfma_f32_16x16x32_bf16 v[12:15],a[184:187],v[56:59],v[12:15]
ds_read_b128 v[52:55],v1 offset:30720
v_mfma_f32_16x16x32_bf16 v[16:19],a[188:191],v[56:59],v[16:19]
s_waitcnt lgkmcnt(5)
buffer_load_dwordx4 v0,s[36:39],s61 offen offset:0 sc1 lds
v_mfma_f32_16x16x32_bf16 v[12:15],a[192:195],v[28:31],v[12:15]
ds_read_b128 v[56:59],v1 offset:31744
v_mfma_f32_16x16x32_bf16 v[16:19],a[196:199],v[28:31],v[16:19]
v_mfma_f32_16x16x32_bf16 v[12:15],a[200:203],v[32:35],v[12:15]
ds_read_b128 v[28:31],v1 offset:32768
v_mfma_f32_16x16x32_bf16 v[16:19],a[204:207],v[32:35],v[16:19]
s_waitcnt lgkmcnt(5)
buffer_load_dwordx4 v0,s[36:39],s61 offen offset:1024 sc1 lds
v_mfma_f32_16x16x32_bf16 v[12:15],a[208:211],v[36:39],v[12:15]
ds_read_b128 v[32:35],v1 offset:33792
v_mfma_f32_16x16x32_bf16 v[16:19],a[212:215],v[36:39],v[16:19]
v_mfma_f32_16x16x32_bf16 v[12:15],a[216:219],v[40:43],v[12:15]
ds_read_b128 v[36:39],v1 offset:34816
v_mfma_f32_16x16x32_bf16 v[16:19],a[220:223],v[40:43],v[16:19]
s_waitcnt lgkmcnt(5)
buffer_load_dwordx4 v0,s[36:39],s61 offen offset:2048 sc1 lds
v_mfma_f32_16x16x32_bf16 v[12:15],a[224:227],v[44:47],v[12:15]
ds_read_b128 v[40:43],v1 offset:35840
v_mfma_f32_16x16x32_bf16 v[16:19],a[228:231],v[44:47],v[16:19]
v_mfma_f32_16x16x32_bf16 v[12:15],a[232:235],v[48:51],v[12:15]
ds_read_b128 v[44:47],v1 offset:36864
v_mfma_f32_16x16x32_bf16 v[16:19],a[236:239],v[48:51],v[16:19]
s_waitcnt lgkmcnt(5)
buffer_load_dwordx4 v0,s[36:39],s61 offen offset:3072 sc1 lds
v_mfma_f32_16x16x32_bf16 v[12:15],a[240:243],v[52:55],v[12:15]
s_add_u32 s61,s61,4096
ds_read_b128 v[48:51],v1 offset:37888
s_add_u32 m0,s88,90112
v_mfma_f32_16x16x32_bf16 v[16:19],a[244:247],v[52:55],v[16:19]
s_lshr_b32 s70,s47,4
v_mfma_f32_16x16x32_bf16 v[12:15],a[248:251],v[56:59],v[12:15]
ds_read_b128 v[52:55],v1 offset:38912
v_mfma_f32_16x16x32_bf16 v[16:19],a[252:255],v[56:59],v[16:19]
s_waitcnt lgkmcnt(5)
buffer_load_dwordx4 v0,s[36:39],s61 offen offset:0 sc1 lds
v_mfma_f32_16x16x32_bf16 v[12:15],v[108:111],v[28:31],v[12:15]
ds_read_b128 v[56:59],v1 offset:39936
v_mfma_f32_16x16x32_bf16 v[16:19],v[112:115],v[28:31],v[16:19]
v_mfma_f32_16x16x32_bf16 v[12:15],v[116:119],v[32:35],v[12:15]
ds_read_b128 v[28:31],v1 offset:40960
v_mfma_f32_16x16x32_bf16 v[16:19],v[120:123],v[32:35],v[16:19]
s_waitcnt lgkmcnt(5)
buffer_load_dwordx4 v0,s[36:39],s61 offen offset:1024 sc1 lds
v_mfma_f32_16x16x32_bf16 v[12:15],v[124:127],v[36:39],v[12:15]
ds_read_b128 v[32:35],v1 offset:41984
v_mfma_f32_16x16x32_bf16 v[16:19],v[128:131],v[36:39],v[16:19]
v_mfma_f32_16x16x32_bf16 v[12:15],v[132:135],v[40:43],v[12:15]
ds_read_b128 v[36:39],v1 offset:43008
v_mfma_f32_16x16x32_bf16 v[16:19],v[136:139],v[40:43],v[16:19]
s_waitcnt lgkmcnt(5)
buffer_load_dwordx4 v0,s[36:39],s61 offen offset:2048 sc1 lds
v_mfma_f32_16x16x32_bf16 v[12:15],v[140:143],v[44:47],v[12:15]
ds_read_b128 v[40:43],v1 offset:44032
v_mfma_f32_16x16x32_bf16 v[16:19],v[144:147],v[44:47],v[16:19]
v_mfma_f32_16x16x32_bf16 v[12:15],v[148:151],v[48:51],v[12:15]
ds_read_b128 v[44:47],v1 offset:45056
v_mfma_f32_16x16x32_bf16 v[16:19],v[152:155],v[48:51],v[16:19]
s_waitcnt lgkmcnt(5)
v_mfma_f32_16x16x32_bf16 v[12:15],v[156:159],v[52:55],v[12:15]
ds_read_b128 v[48:51],v1 offset:46080
v_mfma_f32_16x16x32_bf16 v[16:19],v[160:163],v[52:55],v[16:19]
v_mfma_f32_16x16x32_bf16 v[12:15],v[164:167],v[56:59],v[12:15]
ds_read_b128 v[52:55],v1 offset:47104
v_mfma_f32_16x16x32_bf16 v[16:19],v[168:171],v[56:59],v[16:19]
s_waitcnt lgkmcnt(5)
v_mfma_f32_16x16x32_bf16 v[12:15],v[172:175],v[28:31],v[12:15]
ds_read_b128 v[56:59],v1 offset:48128
v_mfma_f32_16x16x32_bf16 v[16:19],v[176:179],v[28:31],v[16:19]
ds_read_b128 v[88:91],v9 offset:0
v_mfma_f32_16x16x32_bf16 v[12:15],v[180:183],v[32:35],v[12:15]
ds_read_b128 v[28:31],v1 offset:49152
v_mfma_f32_16x16x32_bf16 v[16:19],v[184:187],v[32:35],v[16:19]
s_waitcnt lgkmcnt(6)
ds_read_b128 v[92:95],v9 offset:1024
v_mfma_f32_16x16x32_bf16 v[12:15],v[188:191],v[36:39],v[12:15]
ds_read_b128 v[32:35],v1 offset:50176
v_mfma_f32_16x16x32_bf16 v[16:19],v[192:195],v[36:39],v[16:19]
ds_read_b128 v[96:99],v9 offset:2048
v_mfma_f32_16x16x32_bf16 v[12:15],v[196:199],v[40:43],v[12:15]
ds_read_b128 v[36:39],v1 offset:51200
v_mfma_f32_16x16x32_bf16 v[16:19],v[200:203],v[40:43],v[16:19]
s_waitcnt lgkmcnt(5)
ds_read_b128 v[76:79],v9 offset:3072
v_mfma_f32_16x16x32_bf16 v[12:15],v[204:207],v[44:47],v[12:15]
ds_read_b128 v[40:43],v1 offset:52224
v_mfma_f32_16x16x32_bf16 v[16:19],v[208:211],v[44:47],v[16:19]
ds_read_b128 v[80:83],v9 offset:4096
v_mfma_f32_16x16x32_bf16 v[12:15],v[212:215],v[48:51],v[12:15]
ds_read_b128 v[44:47],v1 offset:53248
v_mfma_f32_16x16x32_bf16 v[16:19],v[88:91],v[48:51],v[16:19]
s_waitcnt lgkmcnt(5)
ds_read_b128 v[84:87],v9 offset:5120
v_mfma_f32_16x16x32_bf16 v[12:15],v[216:219],v[52:55],v[12:15]
ds_read_b128 v[48:51],v1 offset:54272
v_mfma_f32_16x16x32_bf16 v[16:19],v[92:95],v[52:55],v[16:19]
ds_read_b128 v[88:91],v9 offset:6144
v_mfma_f32_16x16x32_bf16 v[12:15],v[220:223],v[56:59],v[12:15]
ds_read_b128 v[52:55],v1 offset:55296
v_mfma_f32_16x16x32_bf16 v[16:19],v[96:99],v[56:59],v[16:19]
s_waitcnt lgkmcnt(5)
ds_read_b128 v[92:95],v9 offset:7168
v_mfma_f32_16x16x32_bf16 v[12:15],v[224:227],v[28:31],v[12:15]
s_waitcnt vmcnt(7)
ds_read_b128 v[56:59],v1 offset:56320
ds_read_b128 v[96:99],v9 offset:8192
v_mfma_f32_16x16x32_bf16 v[16:19],v[76:79],v[28:31],v[16:19]
s_barrier
v_mfma_f32_16x16x32_bf16 v[12:15],v[228:231],v[32:35],v[12:15]
buffer_store_dword v5,v4,s[36:39],s70 offen offset:0 sc0 sc1
ds_read_b128 v[28:31],v2 offset:0
ds_read_b32 v10,v7 offset:3072
v_mfma_f32_16x16x32_bf16 v[16:19],v[80:83],v[32:35],v[16:19]
s_waitcnt lgkmcnt(6)
ds_read_b128 v[76:79],v9 offset:9216
v_mfma_f32_16x16x32_bf16 v[12:15],v[232:235],v[36:39],v[12:15]
ds_read_b128 v[32:35],v2 offset:1024
v_mfma_f32_16x16x32_bf16 v[16:19],v[84:87],v[36:39],v[16:19]
ds_read_b128 v[80:83],v9 offset:10240
v_mfma_f32_16x16x32_bf16 v[12:15],v[236:239],v[40:43],v[12:15]
s_waitcnt lgkmcnt(3)
ds_read_b128 v[36:39],v2 offset:2048
v_mfma_f32_16x16x32_bf16 v[16:19],v[88:91],v[40:43],v[16:19]
v_cmp_eq_u32 vcc,s60,v10
v_mfma_f32_16x16x32_bf16 v[12:15],v[240:243],v[44:47],v[12:15]
s_cmp_eq_u32 vcc_lo,-1
ds_read_b128 v[40:43],v2 offset:3072
s_cselect_b32 s67,s86,s59)"
R"(
v_mfma_f32_16x16x32_bf16 v[16:19],v[92:95],v[44:47],v[16:19]
s_mul_i32 s61,s67,7
s_lshr_b32 s61,s61,1
s_cmp_ge_u32 s66,s59
v_mfma_f32_16x16x32_bf16 v[12:15],v[244:247],v[48:51],v[12:15]
ds_read_b128 v[44:47],v2 offset:4096
v_mfma_f32_16x16x32_bf16 v[16:19],v[96:99],v[48:51],v[16:19]
s_waitcnt lgkmcnt(3)
v_mfma_f32_16x16x32_bf16 v[12:15],v[248:251],v[52:55],v[12:15]
ds_read_b128 v[48:51],v2 offset:5120
v_mfma_f32_16x16x32_bf16 v[16:19],v[76:79],v[52:55],v[16:19]
v_mfma_f32_16x16x32_bf16 v[12:15],v[252:255],v[56:59],v[12:15]
ds_read_b128 v[52:55],v2 offset:6144
v_mfma_f32_16x16x32_bf16 v[16:19],v[80:83],v[56:59],v[16:19]
s_cbranch_scc1 Lg4_slow1_%=
Lg4_d1w3_%=:
v_mfma_f32_16x16x32_bf16 v[20:23],a[0:3],v[28:31],0
s_add_u32 m0,s88,0
ds_read_b128 v[56:59],v2 offset:7168
v_lshlrev_b32 v60,16,v68
v_mfma_f32_16x16x32_bf16 v[24:27],a[4:7],v[28:31],0
s_max_u32 s73,s48,s65
v_and_b32 v61,0xffff0000,v68
v_lshlrev_b32 v62,16,v69
v_mfma_f32_16x16x32_bf16 v[20:23],a[8:11],v[32:35],v[20:23]
s_mov_b32 s65,0
ds_read_b128 v[28:31],v2 offset:8192
v_and_b32 v63,0xffff0000,v69
v_mfma_f32_16x16x32_bf16 v[24:27],a[12:15],v[32:35],v[24:27]
s_waitcnt lgkmcnt(5)
v_lshlrev_b32 v64,16,v70
v_and_b32 v65,0xffff0000,v70
v_mfma_f32_16x16x32_bf16 v[20:23],a[16:19],v[36:39],v[20:23]
buffer_load_dwordx4 v0,s[36:39],s61 offen offset:0 sc1 lds
v_mfma_f32_16x16x32_bf16 v[24:27],a[20:23],v[36:39],v[24:27]
s_add_u32 s74,s53,s55
ds_read_b128 v[32:35],v2 offset:9216
v_lshlrev_b32 v66,16,v71
v_mfma_f32_16x16x32_bf16 v[20:23],a[24:27],v[40:43],v[20:23]
s_cmp_eq_u32 s74,s56
ds_read_b128 v[36:39],v2 offset:10240
v_and_b32 v67,0xffff0000,v71
v_mfma_f32_16x16x32_bf16 v[24:27],a[28:31],v[40:43],v[24:27]
s_waitcnt lgkmcnt(5)
v_add_f32 v12,v12,v60
v_add_f32 v13,v13,v61
v_mfma_f32_16x16x32_bf16 v[20:23],a[32:35],v[44:47],v[20:23]
buffer_load_dwordx4 v0,s[36:39],s61 offen offset:1024 sc1 lds
v_mfma_f32_16x16x32_bf16 v[24:27],a[36:39],v[44:47],v[24:27]
s_cselect_b32 s74,s57,s74
ds_read_b128 v[40:43],v2 offset:11264
v_add_f32 v14,v14,v62
v_mfma_f32_16x16x32_bf16 v[20:23],a[40:43],v[48:51],v[20:23]
s_cmp_eq_u32 s74,s58
ds_read_b128 v[44:47],v2 offset:12288
v_add_f32 v15,v15,v63
v_mfma_f32_16x16x32_bf16 v[24:27],a[44:47],v[48:51],v[24:27]
s_waitcnt lgkmcnt(5)
v_add_f32 v16,v16,v64
v_add_f32 v17,v17,v65
v_mfma_f32_16x16x32_bf16 v[20:23],a[48:51],v[52:55],v[20:23]
buffer_load_dwordx4 v0,s[36:39],s61 offen offset:2048 sc1 lds
v_mfma_f32_16x16x32_bf16 v[24:27],a[52:55],v[52:55],v[24:27]
s_cselect_b32 s74,s59,s74
ds_read_b128 v[48:51],v2 offset:13312
v_add_f32 v18,v18,v66
v_mfma_f32_16x16x32_bf16 v[20:23],a[56:59],v[56:59],v[20:23]
s_mov_b64 s[44:45],s[46:47]
ds_read_b128 v[52:55],v2 offset:14336
v_add_f32 v19,v19,v67
v_mfma_f32_16x16x32_bf16 v[24:27],a[60:63],v[56:59],v[24:27]
s_waitcnt lgkmcnt(5)
v_cvt_pk_bf16_f32 v60,v12,v13
v_cvt_pk_bf16_f32 v61,v14,v15
v_mfma_f32_16x16x32_bf16 v[20:23],a[64:67],v[28:31],v[20:23]
buffer_load_dwordx4 v0,s[36:39],s61 offen offset:3072 sc1 lds
v_mfma_f32_16x16x32_bf16 v[24:27],a[68:71],v[28:31],v[24:27]
s_add_u32 s61,s61,4096
ds_read_b128 v[56:59],v2 offset:15360
v_cvt_pk_bf16_f32 v62,v16,v17
v_mfma_f32_16x16x32_bf16 v[20:23],a[72:75],v[32:35],v[20:23]
s_add_u32 m0,s88,4096
ds_read_b128 v[28:31],v2 offset:16384
v_cvt_pk_bf16_f32 v63,v18,v19
v_mfma_f32_16x16x32_bf16 v[24:27],a[76:79],v[32:35],v[24:27]
s_waitcnt lgkmcnt(5)
s_cmp_ge_u32 s67,s59
s_cselect_b32 s86,s50,s51
v_mfma_f32_16x16x32_bf16 v[20:23],a[80:83],v[36:39],v[20:23]
buffer_store_dwordx4 v[60:63],v3,s[36:39],s73 offen offset:0 sc0 sc1
ds_read_b128 v[32:35],v2 offset:17408
v_mfma_f32_16x16x32_bf16 v[24:27],a[84:87],v[36:39],v[24:27]
s_mov_b64 s[46:47],s[48:49]
v_mfma_f32_16x16x32_bf16 v[20:23],a[88:91],v[40:43],v[20:23]
ds_read_b128 v[36:39],v2 offset:18432
v_mfma_f32_16x16x32_bf16 v[24:27],a[92:95],v[40:43],v[24:27]
s_waitcnt lgkmcnt(5)
v_mfma_f32_16x16x32_bf16 v[20:23],a[96:99],v[44:47],v[20:23]
buffer_load_dwordx4 v0,s[36:39],s61 offen offset:0 sc1 lds
v_mfma_f32_16x16x32_bf16 v[24:27],a[100:103],v[44:47],v[24:27]
ds_read_b128 v[40:43],v2 offset:19456
v_mfma_f32_16x16x32_bf16 v[20:23],a[104:107],v[48:51],v[20:23]
ds_read_b128 v[44:47],v2 offset:20480
v_mfma_f32_16x16x32_bf16 v[24:27],a[108:111],v[48:51],v[24:27]
s_waitcnt lgkmcnt(5)
v_mfma_f32_16x16x32_bf16 v[20:23],a[112:115],v[52:55],v[20:23]
buffer_load_dwordx4 v0,s[36:39],s61 offen offset:1024 sc1 lds
v_mfma_f32_16x16x32_bf16 v[24:27],a[116:119],v[52:55],v[24:27]
ds_read_b128 v[48:51],v2 offset:21504
v_mfma_f32_16x16x32_bf16 v[20:23],a[120:123],v[56:59],v[20:23]
ds_read_b128 v[52:55],v2 offset:22528
v_mfma_f32_16x16x32_bf16 v[24:27],a[124:127],v[56:59],v[24:27]
s_waitcnt lgkmcnt(5)
v_mfma_f32_16x16x32_bf16 v[20:23],a[128:131],v[28:31],v[20:23]
buffer_load_dwordx4 v0,s[36:39],s61 offen offset:2048 sc1 lds
v_mfma_f32_16x16x32_bf16 v[24:27],a[132:135],v[28:31],v[24:27]
s_add_u32 s61,s61,24576
ds_read_b128 v[56:59],v2 offset:23552
s_add_u32 m0,s88,28672
v_mfma_f32_16x16x32_bf16 v[20:23],a[136:139],v[32:35],v[20:23]
ds_read_b128 v[28:31],v2 offset:24576
v_mfma_f32_16x16x32_bf16 v[24:27],a[140:143],v[32:35],v[24:27]
s_waitcnt lgkmcnt(5)
v_mfma_f32_16x16x32_bf16 v[20:23],a[144:147],v[36:39],v[20:23]
buffer_load_dwordx4 v[72:75],v8,s[36:39],s49 offen offset:0 sc1
v_mfma_f32_16x16x32_bf16 v[24:27],a[148:151],v[36:39],v[24:27]
ds_read_b128 v[32:35],v2 offset:25600
v_mfma_f32_16x16x32_bf16 v[20:23],a[152:155],v[40:43],v[20:23]
ds_read_b128 v[36:39],v2 offset:26624
v_mfma_f32_16x16x32_bf16 v[24:27],a[156:159],v[40:43],v[24:27]
s_waitcnt lgkmcnt(5)
v_mfma_f32_16x16x32_bf16 v[20:23],a[160:163],v[44:47],v[20:23]
s_waitcnt vmcnt(10)
ds_read_b128 v[40:43],v2 offset:27648
v_mfma_f32_16x16x32_bf16 v[24:27],a[164:167],v[44:47],v[24:27]
s_barrier
v_mfma_f32_16x16x32_bf16 v[20:23],a[168:171],v[48:51],v[20:23]
ds_read_b128 v[44:47],v2 offset:28672
v_mfma_f32_16x16x32_bf16 v[24:27],a[172:175],v[48:51],v[24:27]
s_waitcnt lgkmcnt(5)
v_mfma_f32_16x16x32_bf16 v[20:23],a[176:179],v[52:55],v[20:23]
ds_read_b128 v[48:51],v2 offset:29696
v_mfma_f32_16x16x32_bf16 v[24:27],a[180:183],v[52:55],v[24:27]
v_mfma_f32_16x16x32_bf16 v[20:23],a[184:187],v[56:59],v[20:23]
ds_read_b128 v[52:55],v2 offset:30720
v_mfma_f32_16x16x32_bf16 v[24:27],a[188:191],v[56:59],v[24:27]
s_waitcnt lgkmcnt(5)
buffer_load_dwordx4 v0,s[36:39],s61 offen offset:0 sc1 lds
v_mfma_f32_16x16x32_bf16 v[20:23],a[192:195],v[28:31],v[20:23]
ds_read_b128 v[56:59],v2 offset:31744
v_mfma_f32_16x16x32_bf16 v[24:27],a[196:199],v[28:31],v[24:27]
v_mfma_f32_16x16x32_bf16 v[20:23],a[200:203],v[32:35],v[20:23]
ds_read_b128 v[28:31],v2 offset:32768
v_mfma_f32_16x16x32_bf16 v[24:27],a[204:207],v[32:35],v[24:27]
s_waitcnt lgkmcnt(5)
buffer_load_dwordx4 v0,s[36:39],s61 offen offset:1024 sc1 lds
v_mfma_f32_16x16x32_bf16 v[20:23],a[208:211],v[36:39],v[20:23]
ds_read_b128 v[32:35],v2 offset:33792
v_mfma_f32_16x16x32_bf16 v[24:27],a[212:215],v[36:39],v[24:27]
v_mfma_f32_16x16x32_bf16 v[20:23],a[216:219],v[40:43],v[20:23]
ds_read_b128 v[36:39],v2 offset:34816
v_mfma_f32_16x16x32_bf16 v[24:27],a[220:223],v[40:43],v[24:27]
s_waitcnt lgkmcnt(5)
buffer_load_dwordx4 v0,s[36:39],s61 offen offset:2048 sc1 lds
v_mfma_f32_16x16x32_bf16 v[20:23],a[224:227],v[44:47],v[20:23]
ds_read_b128 v[40:43],v2 offset:35840
v_mfma_f32_16x16x32_bf16 v[24:27],a[228:231],v[44:47],v[24:27]
v_mfma_f32_16x16x32_bf16 v[20:23],a[232:235],v[48:51],v[20:23]
ds_read_b128 v[44:47],v2 offset:36864
v_mfma_f32_16x16x32_bf16 v[24:27],a[236:239],v[48:51],v[24:27]
s_waitcnt lgkmcnt(5)
buffer_load_dwordx4 v0,s[36:39],s61 offen offset:3072 sc1 lds
v_mfma_f32_16x16x32_bf16 v[20:23],a[240:243],v[52:55],v[20:23]
s_add_u32 s61,s61,4096
ds_read_b128 v[48:51],v2 offset:37888
s_add_u32 m0,s88,32768
v_mfma_f32_16x16x32_bf16 v[24:27],a[244:247],v[52:55],v[24:27]
s_lshr_b32 s70,s48,4
s_mov_b64 s[48:49],s[50:51]
s_mov_b64 s[50:51],s[52:53]
v_mfma_f32_16x16x32_bf16 v[20:23],a[248:251],v[56:59],v[20:23]
s_mov_b32 s52,s74
ds_read_b128 v[52:55],v2 offset:38912
v_mfma_f32_16x16x32_bf16 v[24:27],a[252:255],v[56:59],v[24:27]
s_waitcnt lgkmcnt(5)
buffer_load_dwordx4 v0,s[36:39],s61 offen offset:0 sc1 lds
v_mfma_f32_16x16x32_bf16 v[20:23],v[108:111],v[28:31],v[20:23]
ds_read_b128 v[56:59],v2 offset:39936
v_mfma_f32_16x16x32_bf16 v[24:27],v[112:115],v[28:31],v[24:27]
v_mfma_f32_16x16x32_bf16 v[20:23],v[116:119],v[32:35],v[20:23]
ds_read_b128 v[28:31],v2 offset:40960
v_mfma_f32_16x16x32_bf16 v[24:27],v[120:123],v[32:35],v[24:27]
s_waitcnt lgkmcnt(5)
buffer_load_dwordx4 v0,s[36:39],s61 offen offset:1024 sc1 lds
v_mfma_f32_16x16x32_bf16 v[20:23],v[124:127],v[36:39],v[20:23]
ds_read_b128 v[32:35],v2 offset:41984
v_mfma_f32_16x16x32_bf16 v[24:27],v[128:131],v[36:39],v[24:27]
v_mfma_f32_16x16x32_bf16 v[20:23],v[132:135],v[40:43],v[20:23]
ds_read_b128 v[36:39],v2 offset:43008
v_mfma_f32_16x16x32_bf16 v[24:27],v[136:139],v[40:43],v[24:27])"
R"(
s_waitcnt lgkmcnt(5)
buffer_load_dwordx4 v0,s[36:39],s61 offen offset:2048 sc1 lds
v_mfma_f32_16x16x32_bf16 v[20:23],v[140:143],v[44:47],v[20:23]
ds_read_b128 v[40:43],v2 offset:44032
v_mfma_f32_16x16x32_bf16 v[24:27],v[144:147],v[44:47],v[24:27]
v_mfma_f32_16x16x32_bf16 v[20:23],v[148:151],v[48:51],v[20:23]
ds_read_b128 v[44:47],v2 offset:45056
v_mfma_f32_16x16x32_bf16 v[24:27],v[152:155],v[48:51],v[24:27]
s_waitcnt lgkmcnt(5)
v_mfma_f32_16x16x32_bf16 v[20:23],v[156:159],v[52:55],v[20:23]
ds_read_b128 v[48:51],v2 offset:46080
v_mfma_f32_16x16x32_bf16 v[24:27],v[160:163],v[52:55],v[24:27]
v_mfma_f32_16x16x32_bf16 v[20:23],v[164:167],v[56:59],v[20:23]
ds_read_b128 v[52:55],v2 offset:47104
v_mfma_f32_16x16x32_bf16 v[24:27],v[168:171],v[56:59],v[24:27]
s_waitcnt lgkmcnt(5)
v_mfma_f32_16x16x32_bf16 v[20:23],v[172:175],v[28:31],v[20:23]
ds_read_b128 v[56:59],v2 offset:48128
v_mfma_f32_16x16x32_bf16 v[24:27],v[176:179],v[28:31],v[24:27]
ds_read_b128 v[88:91],v9 offset:0
v_mfma_f32_16x16x32_bf16 v[20:23],v[180:183],v[32:35],v[20:23]
ds_read_b128 v[28:31],v2 offset:49152
v_mfma_f32_16x16x32_bf16 v[24:27],v[184:187],v[32:35],v[24:27]
s_waitcnt lgkmcnt(6)
ds_read_b128 v[92:95],v9 offset:1024
v_mfma_f32_16x16x32_bf16 v[20:23],v[188:191],v[36:39],v[20:23]
ds_read_b128 v[32:35],v2 offset:50176
v_mfma_f32_16x16x32_bf16 v[24:27],v[192:195],v[36:39],v[24:27]
ds_read_b128 v[96:99],v9 offset:2048
v_mfma_f32_16x16x32_bf16 v[20:23],v[196:199],v[40:43],v[20:23]
ds_read_b128 v[36:39],v2 offset:51200
v_mfma_f32_16x16x32_bf16 v[24:27],v[200:203],v[40:43],v[24:27]
s_waitcnt lgkmcnt(5)
ds_read_b128 v[76:79],v9 offset:3072
v_mfma_f32_16x16x32_bf16 v[20:23],v[204:207],v[44:47],v[20:23]
ds_read_b128 v[40:43],v2 offset:52224
v_mfma_f32_16x16x32_bf16 v[24:27],v[208:211],v[44:47],v[24:27]
ds_read_b128 v[80:83],v9 offset:4096
v_mfma_f32_16x16x32_bf16 v[20:23],v[212:215],v[48:51],v[20:23]
ds_read_b128 v[44:47],v2 offset:53248
v_mfma_f32_16x16x32_bf16 v[24:27],v[88:91],v[48:51],v[24:27]
s_waitcnt lgkmcnt(5)
ds_read_b128 v[84:87],v9 offset:5120
v_mfma_f32_16x16x32_bf16 v[20:23],v[216:219],v[52:55],v[20:23]
ds_read_b128 v[48:51],v2 offset:54272
v_mfma_f32_16x16x32_bf16 v[24:27],v[92:95],v[52:55],v[24:27]
ds_read_b128 v[88:91],v9 offset:6144
v_mfma_f32_16x16x32_bf16 v[20:23],v[220:223],v[56:59],v[20:23]
ds_read_b128 v[52:55],v2 offset:55296
v_mfma_f32_16x16x32_bf16 v[24:27],v[96:99],v[56:59],v[24:27]
s_waitcnt lgkmcnt(5)
ds_read_b128 v[92:95],v9 offset:7168
v_mfma_f32_16x16x32_bf16 v[20:23],v[224:227],v[28:31],v[20:23]
s_waitcnt vmcnt(7)
ds_read_b128 v[56:59],v2 offset:56320
ds_read_b128 v[96:99],v9 offset:8192
v_mfma_f32_16x16x32_bf16 v[24:27],v[76:79],v[28:31],v[24:27]
s_barrier
v_mfma_f32_16x16x32_bf16 v[20:23],v[228:231],v[32:35],v[20:23]
buffer_store_dword v5,v4,s[36:39],s70 offen offset:0 sc0 sc1
ds_read_b128 v[28:31],v1 offset:0
ds_read_b32 v10,v7 offset:3072
v_mfma_f32_16x16x32_bf16 v[24:27],v[80:83],v[32:35],v[24:27]
s_waitcnt lgkmcnt(6)
ds_read_b128 v[76:79],v9 offset:9216
v_mfma_f32_16x16x32_bf16 v[20:23],v[232:235],v[36:39],v[20:23]
ds_read_b128 v[32:35],v1 offset:1024
v_mfma_f32_16x16x32_bf16 v[24:27],v[84:87],v[36:39],v[24:27]
ds_read_b128 v[80:83],v9 offset:10240
v_mfma_f32_16x16x32_bf16 v[20:23],v[236:239],v[40:43],v[20:23]
s_waitcnt lgkmcnt(3)
ds_read_b128 v[36:39],v1 offset:2048
v_mfma_f32_16x16x32_bf16 v[24:27],v[88:91],v[40:43],v[24:27]
v_cmp_eq_u32 vcc,s60,v10
v_mfma_f32_16x16x32_bf16 v[20:23],v[240:243],v[44:47],v[20:23]
s_cmp_eq_u32 vcc_lo,-1
ds_read_b128 v[40:43],v1 offset:3072
s_cselect_b32 s66,s86,s59
v_mfma_f32_16x16x32_bf16 v[24:27],v[92:95],v[44:47],v[24:27]
s_mul_i32 s61,s66,7
s_lshr_b32 s61,s61,1
s_cmp_lt_u32 s67,s59
v_mfma_f32_16x16x32_bf16 v[20:23],v[244:247],v[48:51],v[20:23]
ds_read_b128 v[44:47],v1 offset:4096
v_mfma_f32_16x16x32_bf16 v[24:27],v[96:99],v[48:51],v[24:27]
s_waitcnt lgkmcnt(3)
v_mfma_f32_16x16x32_bf16 v[20:23],v[248:251],v[52:55],v[20:23]
ds_read_b128 v[48:51],v1 offset:5120
v_mfma_f32_16x16x32_bf16 v[24:27],v[76:79],v[52:55],v[24:27]
v_mfma_f32_16x16x32_bf16 v[20:23],v[252:255],v[56:59],v[20:23]
ds_read_b128 v[52:55],v1 offset:6144
v_mfma_f32_16x16x32_bf16 v[24:27],v[80:83],v[56:59],v[24:27]
s_cbranch_scc1 Lg4_d0w3_%=
s_branch Lg4_slow0_%=
Lg4_slow0_%=:
s_cmp_lt_u32 s66,s59
s_cbranch_scc1 Lg4_gs0_%=
s_cmp_eq_u32 s54,1
s_cselect_b32 s68,s49,s48
s_mul_i32 s68,s68,5
s_lshr_b32 s68,s68,6
s_add_u32 m0,s78,0
s_nop 0
buffer_load_dword v6,s[36:39],s68 offen offset:0 sc1 lds
s_lshr_b32 s73,s47,0
v_lshlrev_b32 v60,16,v72
v_and_b32 v61,0xffff0000,v72
v_lshlrev_b32 v62,16,v73
v_and_b32 v63,0xffff0000,v73
v_lshlrev_b32 v64,16,v74
v_and_b32 v65,0xffff0000,v74
v_lshlrev_b32 v66,16,v75
v_and_b32 v67,0xffff0000,v75
v_add_f32 v20,v20,v60
v_add_f32 v21,v21,v61
s_add_u32 m0,s78,1024
s_nop 0
buffer_load_dword v6,s[36:39],s68 offen offset:0 sc1 lds
v_add_f32 v22,v22,v62
v_add_f32 v23,v23,v63
v_add_f32 v24,v24,v64
v_add_f32 v25,v25,v65
v_add_f32 v26,v26,v66
v_add_f32 v27,v27,v67
v_cvt_pk_bf16_f32 v60,v20,v21
v_cvt_pk_bf16_f32 v61,v22,v23
v_cvt_pk_bf16_f32 v62,v24,v25
v_cvt_pk_bf16_f32 v63,v26,v27
buffer_store_dwordx4 v[60:63],v3,s[36:39],s73 offen offset:0 sc0 sc1
s_cmp_ge_u32 s48,s59
s_cbranch_scc1 Lg4_done0_%=
s_add_u32 m0,s78,2048
s_nop 0
buffer_load_dword v6,s[36:39],s68 offen offset:0 sc1 lds
s_waitcnt vmcnt(3)
s_barrier
ds_read_b32 v10,v7 offset:0
s_mov_b32 s69,2304
s_waitcnt lgkmcnt(0)
v_cmp_eq_u32 vcc,s60,v10
s_nop 0
s_cmp_eq_u32 vcc_lo,-1
s_cbranch_scc1 Lg4_go0_%=
s_add_u32 m0,s78,3072
s_nop 0
buffer_load_dword v6,s[36:39],s68 offen offset:0 sc1 lds
s_waitcnt vmcnt(3)
s_barrier
ds_read_b32 v10,v7 offset:1024
s_mov_b32 s69,3328
s_waitcnt lgkmcnt(0)
v_cmp_eq_u32 vcc,s60,v10
s_nop 0
s_cmp_eq_u32 vcc_lo,-1
s_cbranch_scc1 Lg4_go0_%=
s_add_u32 m0,s78,0
s_nop 0
buffer_load_dword v6,s[36:39],s68 offen offset:0 sc1 lds
s_waitcnt vmcnt(2)
s_barrier
ds_read_b32 v10,v7 offset:2048
s_lshr_b32 s70,s47,4
buffer_store_dword v5,v4,s[36:39],s70 offen offset:0 sc0 sc1
s_mov_b32 s47,s59
s_mov_b32 s69,256
s_waitcnt lgkmcnt(0)
v_cmp_eq_u32 vcc,s60,v10
s_nop 0
s_cmp_eq_u32 vcc_lo,-1
s_cbranch_scc1 Lg4_go0_%=
s_add_u32 m0,s78,1024
s_nop 0
buffer_load_dword v6,s[36:39],s68 offen offset:0 sc1 lds
s_waitcnt vmcnt(3)
s_barrier
ds_read_b32 v10,v7 offset:3072
s_mov_b32 s69,1280
s_waitcnt lgkmcnt(0)
v_cmp_eq_u32 vcc,s60,v10
s_nop 0
s_cmp_eq_u32 vcc_lo,-1
s_cbranch_scc1 Lg4_go0_%=
Lg4_poll0_%=:
s_add_u32 m0,s78,2048
s_nop 0
buffer_load_dword v6,s[36:39],s68 offen offset:0 sc1 lds
s_waitcnt vmcnt(2)
s_barrier
ds_read_b32 v10,v7 offset:0
s_mov_b32 s69,2304
s_waitcnt lgkmcnt(0)
v_cmp_eq_u32 vcc,s60,v10
s_nop 0
s_cmp_eq_u32 vcc_lo,-1
s_cbranch_scc1 Lg4_go0_%=
s_sleep 4
s_add_u32 m0,s78,3072
s_nop 0
buffer_load_dword v6,s[36:39],s68 offen offset:0 sc1 lds
s_waitcnt vmcnt(2))"
R"(
s_barrier
ds_read_b32 v10,v7 offset:1024
s_mov_b32 s69,3328
s_waitcnt lgkmcnt(0)
v_cmp_eq_u32 vcc,s60,v10
s_nop 0
s_cmp_eq_u32 vcc_lo,-1
s_cbranch_scc1 Lg4_go0_%=
s_sleep 4
s_add_u32 m0,s78,0
s_nop 0
buffer_load_dword v6,s[36:39],s68 offen offset:0 sc1 lds
s_waitcnt vmcnt(2)
s_barrier
ds_read_b32 v10,v7 offset:2048
s_mov_b32 s69,256
s_waitcnt lgkmcnt(0)
v_cmp_eq_u32 vcc,s60,v10
s_nop 0
s_cmp_eq_u32 vcc_lo,-1
s_cbranch_scc1 Lg4_go0_%=
s_sleep 4
s_add_u32 m0,s78,1024
s_nop 0
buffer_load_dword v6,s[36:39],s68 offen offset:0 sc1 lds
s_waitcnt vmcnt(2)
s_barrier
ds_read_b32 v10,v7 offset:3072
s_mov_b32 s69,1280
s_waitcnt lgkmcnt(0)
v_cmp_eq_u32 vcc,s60,v10
s_nop 0
s_cmp_eq_u32 vcc_lo,-1
s_cbranch_scc1 Lg4_go0_%=
s_sleep 4
s_add_u32 s79,s79,1
s_cmp_lt_u32 s79,20000
s_cbranch_scc1 Lg4_poll0_%=
Lg4_go0_%=:
s_mul_i32 s61,s48,7
s_lshr_b32 s61,s61,1
s_add_u32 m0,s88,0
s_nop 0
buffer_load_dwordx4 v0,s[36:39],s61 offen offset:0 sc1 lds
buffer_load_dwordx4 v0,s[36:39],s61 offen offset:1024 sc1 lds
buffer_load_dwordx4 v0,s[36:39],s61 offen offset:2048 sc1 lds
buffer_load_dwordx4 v0,s[36:39],s61 offen offset:3072 sc1 lds
s_add_u32 s61,s61,4096
s_add_u32 m0,s88,4096
s_nop 0
buffer_load_dwordx4 v0,s[36:39],s61 offen offset:0 sc1 lds
buffer_load_dwordx4 v0,s[36:39],s61 offen offset:1024 sc1 lds
buffer_load_dwordx4 v0,s[36:39],s61 offen offset:2048 sc1 lds
s_add_u32 s61,s61,24576
s_add_u32 m0,s88,28672
s_nop 0
buffer_load_dwordx4 v0,s[36:39],s61 offen offset:0 sc1 lds
buffer_load_dwordx4 v0,s[36:39],s61 offen offset:1024 sc1 lds
buffer_load_dwordx4 v0,s[36:39],s61 offen offset:2048 sc1 lds
buffer_load_dwordx4 v0,s[36:39],s61 offen offset:3072 sc1 lds
s_add_u32 s61,s61,4096
s_add_u32 m0,s88,32768
s_nop 0
buffer_load_dwordx4 v0,s[36:39],s61 offen offset:0 sc1 lds
buffer_load_dwordx4 v0,s[36:39],s61 offen offset:1024 sc1 lds
buffer_load_dwordx4 v0,s[36:39],s61 offen offset:2048 sc1 lds
v_add_u32 v60,s69,v7
s_waitcnt vmcnt(7)
s_barrier
ds_read_b32 v10,v60 offset:0
ds_read_b128 v[28:31],v1 offset:0
ds_read_b128 v[32:35],v1 offset:1024
ds_read_b128 v[36:39],v1 offset:2048
ds_read_b128 v[40:43],v1 offset:3072
ds_read_b128 v[44:47],v1 offset:4096
ds_read_b128 v[48:51],v1 offset:5120
ds_read_b128 v[52:55],v1 offset:6144
s_lshr_b32 s70,s47,4
buffer_store_dword v5,v4,s[36:39],s70 offen offset:0 sc0 sc1
s_mov_b32 s47,s59
s_waitcnt lgkmcnt(7)
v_cmp_eq_u32 vcc,s60,v10
s_nop 0
s_cmp_eq_u32 vcc_lo,-1
s_cselect_b32 s66,s49,s59
s_mul_i32 s61,s66,7
s_lshr_b32 s61,s61,1
s_waitcnt lgkmcnt(5)
s_setpc_b64 s[90:91]
Lg4_gs0_%=:
s_mul_i32 s61,s48,7
s_lshr_b32 s61,s61,1
s_cmp_eq_u32 s54,1
s_cselect_b32 s68,s49,s48
s_mul_i32 s68,s68,5
s_lshr_b32 s68,s68,6
s_lshr_b32 s73,s47,0
s_add_u32 m0,s88,0
s_nop 0
buffer_load_dwordx4 v0,s[36:39],s61 offen offset:0 sc1 lds
v_lshlrev_b32 v60,16,v72
v_and_b32 v61,0xffff0000,v72
v_lshlrev_b32 v62,16,v73
v_and_b32 v63,0xffff0000,v73
buffer_load_dwordx4 v0,s[36:39],s61 offen offset:1024 sc1 lds
v_lshlrev_b32 v64,16,v74
v_and_b32 v65,0xffff0000,v74
v_lshlrev_b32 v66,16,v75
v_and_b32 v67,0xffff0000,v75
buffer_load_dwordx4 v0,s[36:39],s61 offen offset:2048 sc1 lds
v_add_f32 v20,v20,v60
v_add_f32 v21,v21,v61
v_add_f32 v22,v22,v62
v_add_f32 v23,v23,v63
buffer_load_dwordx4 v0,s[36:39],s61 offen offset:3072 sc1 lds
v_add_f32 v24,v24,v64
v_add_f32 v25,v25,v65
v_add_f32 v26,v26,v66
v_add_f32 v27,v27,v67
s_add_u32 s61,s61,4096
s_add_u32 m0,s88,4096
s_nop 0
buffer_load_dwordx4 v0,s[36:39],s61 offen offset:0 sc1 lds
v_cvt_pk_bf16_f32 v60,v20,v21
v_cvt_pk_bf16_f32 v61,v22,v23
v_cvt_pk_bf16_f32 v62,v24,v25
v_cvt_pk_bf16_f32 v63,v26,v27
buffer_load_dwordx4 v0,s[36:39],s61 offen offset:1024 sc1 lds
buffer_store_dwordx4 v[60:63],v3,s[36:39],s73 offen offset:0 sc0 sc1
buffer_load_dwordx4 v0,s[36:39],s61 offen offset:2048 sc1 lds
s_add_u32 m0,s78,0
s_nop 0
buffer_load_dword v6,s[36:39],s68 offen offset:0 sc1 lds
s_add_u32 s61,s61,24576
s_add_u32 m0,s88,28672
s_nop 0
buffer_load_dwordx4 v0,s[36:39],s61 offen offset:0 sc1 lds
buffer_load_dwordx4 v0,s[36:39],s61 offen offset:1024 sc1 lds
buffer_load_dwordx4 v0,s[36:39],s61 offen offset:2048 sc1 lds
buffer_load_dwordx4 v0,s[36:39],s61 offen offset:3072 sc1 lds
s_add_u32 s61,s61,4096
s_add_u32 m0,s88,32768
s_nop 0
buffer_load_dwordx4 v0,s[36:39],s61 offen offset:0 sc1 lds
buffer_load_dwordx4 v0,s[36:39],s61 offen offset:1024 sc1 lds
buffer_load_dwordx4 v0,s[36:39],s61 offen offset:2048 sc1 lds
s_waitcnt vmcnt(7)
s_barrier
ds_read_b32 v10,v7 offset:0
ds_read_b128 v[28:31],v1 offset:0
ds_read_b128 v[32:35],v1 offset:1024
ds_read_b128 v[36:39],v1 offset:2048
ds_read_b128 v[40:43],v1 offset:3072
ds_read_b128 v[44:47],v1 offset:4096
ds_read_b128 v[48:51],v1 offset:5120
ds_read_b128 v[52:55],v1 offset:6144
s_waitcnt lgkmcnt(7)
v_cmp_eq_u32 vcc,s60,v10
s_nop 0
s_cmp_eq_u32 vcc_hi,-1
s_cselect_b32 s66,s49,s59
s_mul_i32 s61,s66,7
s_lshr_b32 s61,s61,1
s_mov_b32 s65,s59
s_lshr_b32 s70,s59,4
buffer_store_dword v5,v4,s[36:39],s70 offen offset:0 sc0 sc1
s_waitcnt lgkmcnt(5)
s_setpc_b64 s[90:91]
Lg4_done0_%=:
s_waitcnt vmcnt(0)
s_barrier
s_lshr_b32 s70,s47,4
buffer_store_dword v5,v4,s[36:39],s70 offen offset:0 sc0 sc1
s_branch Lg4_done_%=
Lg4_slow1_%=:
s_cmp_lt_u32 s67,s59
s_cbranch_scc1 Lg4_gs1_%=
s_cmp_eq_u32 s54,1
s_cselect_b32 s68,s50,s49
s_mul_i32 s68,s68,5
s_lshr_b32 s68,s68,6
s_add_u32 m0,s78,0
s_nop 0
buffer_load_dword v6,s[36:39],s68 offen offset:0 sc1 lds
s_lshr_b32 s73,s48,0
v_lshlrev_b32 v60,16,v68
v_and_b32 v61,0xffff0000,v68
v_lshlrev_b32 v62,16,v69
v_and_b32 v63,0xffff0000,v69
v_lshlrev_b32 v64,16,v70
v_and_b32 v65,0xffff0000,v70
v_lshlrev_b32 v66,16,v71
v_and_b32 v67,0xffff0000,v71
v_add_f32 v12,v12,v60
v_add_f32 v13,v13,v61
s_add_u32 m0,s78,1024
s_nop 0
buffer_load_dword v6,s[36:39],s68 offen offset:0 sc1 lds
v_add_f32 v14,v14,v62
v_add_f32 v15,v15,v63)"
R"(
v_add_f32 v16,v16,v64
v_add_f32 v17,v17,v65
v_add_f32 v18,v18,v66
v_add_f32 v19,v19,v67
v_cvt_pk_bf16_f32 v60,v12,v13
v_cvt_pk_bf16_f32 v61,v14,v15
v_cvt_pk_bf16_f32 v62,v16,v17
v_cvt_pk_bf16_f32 v63,v18,v19
buffer_store_dwordx4 v[60:63],v3,s[36:39],s73 offen offset:0 sc0 sc1
s_cmp_ge_u32 s49,s59
s_cbranch_scc1 Lg4_done1_%=
s_add_u32 m0,s78,2048
s_nop 0
buffer_load_dword v6,s[36:39],s68 offen offset:0 sc1 lds
s_waitcnt vmcnt(3)
s_barrier
ds_read_b32 v10,v7 offset:0
s_mov_b32 s69,2304
s_waitcnt lgkmcnt(0)
v_cmp_eq_u32 vcc,s60,v10
s_nop 0
s_cmp_eq_u32 vcc_lo,-1
s_cbranch_scc1 Lg4_go1_%=
s_add_u32 m0,s78,3072
s_nop 0
buffer_load_dword v6,s[36:39],s68 offen offset:0 sc1 lds
s_waitcnt vmcnt(3)
s_barrier
ds_read_b32 v10,v7 offset:1024
s_mov_b32 s69,3328
s_waitcnt lgkmcnt(0)
v_cmp_eq_u32 vcc,s60,v10
s_nop 0
s_cmp_eq_u32 vcc_lo,-1
s_cbranch_scc1 Lg4_go1_%=
s_add_u32 m0,s78,0
s_nop 0
buffer_load_dword v6,s[36:39],s68 offen offset:0 sc1 lds
s_waitcnt vmcnt(2)
s_barrier
ds_read_b32 v10,v7 offset:2048
s_lshr_b32 s70,s48,4
buffer_store_dword v5,v4,s[36:39],s70 offen offset:0 sc0 sc1
s_mov_b32 s48,s59
s_mov_b32 s69,256
s_waitcnt lgkmcnt(0)
v_cmp_eq_u32 vcc,s60,v10
s_nop 0
s_cmp_eq_u32 vcc_lo,-1
s_cbranch_scc1 Lg4_go1_%=
s_add_u32 m0,s78,1024
s_nop 0
buffer_load_dword v6,s[36:39],s68 offen offset:0 sc1 lds
s_waitcnt vmcnt(3)
s_barrier
ds_read_b32 v10,v7 offset:3072
s_mov_b32 s69,1280
s_waitcnt lgkmcnt(0)
v_cmp_eq_u32 vcc,s60,v10
s_nop 0
s_cmp_eq_u32 vcc_lo,-1
s_cbranch_scc1 Lg4_go1_%=
Lg4_poll1_%=:
s_add_u32 m0,s78,2048
s_nop 0
buffer_load_dword v6,s[36:39],s68 offen offset:0 sc1 lds
s_waitcnt vmcnt(2)
s_barrier
ds_read_b32 v10,v7 offset:0
s_mov_b32 s69,2304
s_waitcnt lgkmcnt(0)
v_cmp_eq_u32 vcc,s60,v10
s_nop 0
s_cmp_eq_u32 vcc_lo,-1
s_cbranch_scc1 Lg4_go1_%=
s_sleep 4
s_add_u32 m0,s78,3072
s_nop 0
buffer_load_dword v6,s[36:39],s68 offen offset:0 sc1 lds
s_waitcnt vmcnt(2)
s_barrier
ds_read_b32 v10,v7 offset:1024
s_mov_b32 s69,3328
s_waitcnt lgkmcnt(0)
v_cmp_eq_u32 vcc,s60,v10
s_nop 0
s_cmp_eq_u32 vcc_lo,-1
s_cbranch_scc1 Lg4_go1_%=
s_sleep 4
s_add_u32 m0,s78,0
s_nop 0
buffer_load_dword v6,s[36:39],s68 offen offset:0 sc1 lds
s_waitcnt vmcnt(2)
s_barrier
ds_read_b32 v10,v7 offset:2048
s_mov_b32 s69,256
s_waitcnt lgkmcnt(0)
v_cmp_eq_u32 vcc,s60,v10
s_nop 0
s_cmp_eq_u32 vcc_lo,-1
s_cbranch_scc1 Lg4_go1_%=
s_sleep 4
s_add_u32 m0,s78,1024
s_nop 0
buffer_load_dword v6,s[36:39],s68 offen offset:0 sc1 lds
s_waitcnt vmcnt(2)
s_barrier
ds_read_b32 v10,v7 offset:3072
s_mov_b32 s69,1280
s_waitcnt lgkmcnt(0)
v_cmp_eq_u32 vcc,s60,v10
s_nop 0
s_cmp_eq_u32 vcc_lo,-1
s_cbranch_scc1 Lg4_go1_%=
s_sleep 4
s_add_u32 s79,s79,1
s_cmp_lt_u32 s79,20000
s_cbranch_scc1 Lg4_poll1_%=
Lg4_go1_%=:
s_mul_i32 s61,s49,7
s_lshr_b32 s61,s61,1
s_add_u32 m0,s88,57344
s_nop 0
buffer_load_dwordx4 v0,s[36:39],s61 offen offset:0 sc1 lds
buffer_load_dwordx4 v0,s[36:39],s61 offen offset:1024 sc1 lds
buffer_load_dwordx4 v0,s[36:39],s61 offen offset:2048 sc1 lds
buffer_load_dwordx4 v0,s[36:39],s61 offen offset:3072 sc1 lds
s_add_u32 s61,s61,4096
s_add_u32 m0,s88,61440
s_nop 0
buffer_load_dwordx4 v0,s[36:39],s61 offen offset:0 sc1 lds
buffer_load_dwordx4 v0,s[36:39],s61 offen offset:1024 sc1 lds
buffer_load_dwordx4 v0,s[36:39],s61 offen offset:2048 sc1 lds
s_add_u32 s61,s61,24576
s_add_u32 m0,s88,86016
s_nop 0
buffer_load_dwordx4 v0,s[36:39],s61 offen offset:0 sc1 lds
buffer_load_dwordx4 v0,s[36:39],s61 offen offset:1024 sc1 lds
buffer_load_dwordx4 v0,s[36:39],s61 offen offset:2048 sc1 lds
buffer_load_dwordx4 v0,s[36:39],s61 offen offset:3072 sc1 lds
s_add_u32 s61,s61,4096
s_add_u32 m0,s88,90112
s_nop 0
buffer_load_dwordx4 v0,s[36:39],s61 offen offset:0 sc1 lds
buffer_load_dwordx4 v0,s[36:39],s61 offen offset:1024 sc1 lds
buffer_load_dwordx4 v0,s[36:39],s61 offen offset:2048 sc1 lds
v_add_u32 v60,s69,v7
s_waitcnt vmcnt(7)
s_barrier
ds_read_b32 v10,v60 offset:0
ds_read_b128 v[28:31],v2 offset:0
ds_read_b128 v[32:35],v2 offset:1024
ds_read_b128 v[36:39],v2 offset:2048
ds_read_b128 v[40:43],v2 offset:3072
ds_read_b128 v[44:47],v2 offset:4096
ds_read_b128 v[48:51],v2 offset:5120
ds_read_b128 v[52:55],v2 offset:6144
s_lshr_b32 s70,s48,4
buffer_store_dword v5,v4,s[36:39],s70 offen offset:0 sc0 sc1
s_mov_b32 s48,s59
s_waitcnt lgkmcnt(7)
v_cmp_eq_u32 vcc,s60,v10
s_nop 0
s_cmp_eq_u32 vcc_lo,-1
s_cselect_b32 s67,s50,s59
s_mul_i32 s61,s67,7
s_lshr_b32 s61,s61,1
s_waitcnt lgkmcnt(5)
s_setpc_b64 s[92:93]
Lg4_gs1_%=:
s_mul_i32 s61,s49,7
s_lshr_b32 s61,s61,1
s_cmp_eq_u32 s54,1
s_cselect_b32 s68,s50,s49
s_mul_i32 s68,s68,5
s_lshr_b32 s68,s68,6
s_lshr_b32 s73,s48,0
s_add_u32 m0,s88,57344
s_nop 0
buffer_load_dwordx4 v0,s[36:39],s61 offen offset:0 sc1 lds
v_lshlrev_b32 v60,16,v68
v_and_b32 v61,0xffff0000,v68
v_lshlrev_b32 v62,16,v69
v_and_b32 v63,0xffff0000,v69
buffer_load_dwordx4 v0,s[36:39],s61 offen offset:1024 sc1 lds
v_lshlrev_b32 v64,16,v70
v_and_b32 v65,0xffff0000,v70
v_lshlrev_b32 v66,16,v71
v_and_b32 v67,0xffff0000,v71
buffer_load_dwordx4 v0,s[36:39],s61 offen offset:2048 sc1 lds
v_add_f32 v12,v12,v60
v_add_f32 v13,v13,v61
v_add_f32 v14,v14,v62
v_add_f32 v15,v15,v63
buffer_load_dwordx4 v0,s[36:39],s61 offen offset:3072 sc1 lds
v_add_f32 v16,v16,v64
v_add_f32 v17,v17,v65
v_add_f32 v18,v18,v66
v_add_f32 v19,v19,v67
s_add_u32 s61,s61,4096)"
R"(
s_add_u32 m0,s88,61440
s_nop 0
buffer_load_dwordx4 v0,s[36:39],s61 offen offset:0 sc1 lds
v_cvt_pk_bf16_f32 v60,v12,v13
v_cvt_pk_bf16_f32 v61,v14,v15
v_cvt_pk_bf16_f32 v62,v16,v17
v_cvt_pk_bf16_f32 v63,v18,v19
buffer_load_dwordx4 v0,s[36:39],s61 offen offset:1024 sc1 lds
buffer_store_dwordx4 v[60:63],v3,s[36:39],s73 offen offset:0 sc0 sc1
buffer_load_dwordx4 v0,s[36:39],s61 offen offset:2048 sc1 lds
s_add_u32 m0,s78,0
s_nop 0
buffer_load_dword v6,s[36:39],s68 offen offset:0 sc1 lds
s_add_u32 s61,s61,24576
s_add_u32 m0,s88,86016
s_nop 0
buffer_load_dwordx4 v0,s[36:39],s61 offen offset:0 sc1 lds
buffer_load_dwordx4 v0,s[36:39],s61 offen offset:1024 sc1 lds
buffer_load_dwordx4 v0,s[36:39],s61 offen offset:2048 sc1 lds
buffer_load_dwordx4 v0,s[36:39],s61 offen offset:3072 sc1 lds
s_add_u32 s61,s61,4096
s_add_u32 m0,s88,90112
s_nop 0
buffer_load_dwordx4 v0,s[36:39],s61 offen offset:0 sc1 lds
buffer_load_dwordx4 v0,s[36:39],s61 offen offset:1024 sc1 lds
buffer_load_dwordx4 v0,s[36:39],s61 offen offset:2048 sc1 lds
s_waitcnt vmcnt(7)
s_barrier
ds_read_b32 v10,v7 offset:0
ds_read_b128 v[28:31],v2 offset:0
ds_read_b128 v[32:35],v2 offset:1024
ds_read_b128 v[36:39],v2 offset:2048
ds_read_b128 v[40:43],v2 offset:3072
ds_read_b128 v[44:47],v2 offset:4096
ds_read_b128 v[48:51],v2 offset:5120
ds_read_b128 v[52:55],v2 offset:6144
s_waitcnt lgkmcnt(7)
v_cmp_eq_u32 vcc,s60,v10
s_nop 0
s_cmp_eq_u32 vcc_hi,-1
s_cselect_b32 s67,s50,s59
s_mul_i32 s61,s67,7
s_lshr_b32 s61,s61,1
s_mov_b32 s65,s59
s_lshr_b32 s70,s59,4
buffer_store_dword v5,v4,s[36:39],s70 offen offset:0 sc0 sc1
s_waitcnt lgkmcnt(5)
s_setpc_b64 s[92:93]
Lg4_done1_%=:
s_waitcnt vmcnt(0)
s_barrier
s_lshr_b32 s70,s48,4
buffer_store_dword v5,v4,s[36:39],s70 offen offset:0 sc0 sc1
s_branch Lg4_done_%=
Lg4_done_%=:
s_waitcnt vmcnt(0) lgkmcnt(0)
s_barrier
s_endpgm
Lcode_g4_e:)"
  :: "s"(ws), "s"(wave), "s"(sStep), "s"(sX1), "s"(sX2), "s"(sU0), "s"(sWw), "s"(sBuf), "v"(vSrc), "v"(vA), "v"(vOut), "v"(vFlag), "v"(vPollOff), "v"(vSnapRd), "v"(vAuxS), "v"(vWd0), "v"(vWd1), "v"(vWr0), "v"(vWr1), "v"(vWr2), "s"(w4) : "v0", "v1", "v2", "v3", "v4", "v5", "v6", "v7", "v8", "v9", "v10", "v11", "v12", "v13", "v14", "v15", "v16", "v17", "v18", "v19", "v20", "v21", "v22", "v23", "v24", "v25", "v26", "v27", "v28", "v29", "v30", "v31", "v32", "v33", "v34", "v35", "v36", "v37", "v38", "v39", "v40", "v41", "v42", "v43", "v44", "v45", "v46", "v47", "v48", "v49", "v50", "v51", "v52", "v53", "v54", "v55", "v56", "v57", "v58", "v59", "v60", "v61", "v62", "v63", "v64", "v65", "v66", "v67", "v68", "v69", "v70", "v71", "v72", "v73", "v74", "v75", "v76", "v77", "v78", "v79", "v80", "v81", "v82", "v83", "v84", "v85", "v86", "v87", "v88", "v89", "v90", "v91", "v92", "v93", "v94", "v95", "v96", "v97", "v98", "v99", "v100", "v101", "v102", "v103", "v104", "v105", "v106", "v107", "v108", "v109", "v110", "v111", "v112", "v113", "v114", "v115", "v116", "v117", "v118", "v119", "v120", "v121", "v122", "v123", "v124", "v125", "v126", "v127", "v128", "v129", "v130", "v131", "v132", "v133", "v134", "v135", "v136", "v137", "v138", "v139", "v140", "v141", "v142", "v143", "v144", "v145", "v146", "v147", "v148", "v149", "v150", "v151", "v152", "v153", "v154", "v155", "v156", "v157", "v158", "v159", "v160", "v161", "v162", "v163", "v164", "v165", "v166", "v167", "v168", "v169", "v170", "v171", "v172", "v173", "v174", "v175", "v176", "v177", "v178", "v179", "v180", "v181", "v182", "v183", "v184", "v185", "v186", "v187", "v188", "v189", "v190", "v191", "v192", "v193", "v194", "v195", "v196", "v197", "v198", "v199", "v200", "v201", "v202", "v203", "v204", "v205", "v206", "v207", "v208", "v209", "v210", "v211", "v212", "v213", "v214", "v215", "v216", "v217", "v218", "v219", "v220", "v221", "v222", "v223", "v224", "v225", "v226", "v227", "v228", "v229", "v230", "v231", "v255", "a0", "a1", "a2", "a3", "a4", "a5", "a6", "a7", "a8", "a9", "a10", "a11", "a12", "a13", "a14", "a15", "a16", "a17", "a18", "a19", "a20", "a21", "a22", "a23", "a24", "a25", "a26", "a27", "a28", "a29", "a30", "a31", "a32", "a33", "a34", "a35", "a36", "a37", "a38", "a39", "a40", "a41", "a42", "a43", "a44", "a45", "a46", "a47", "a48", "a49", "a50", "a51", "a52", "a53", "a54", "a55", "a56", "a57", "a58", "a59", "a60", "a61", "a62", "a63", "a64", "a65", "a66", "a67", "a68", "a69", "a70", "a71", "a72", "a73", "a74", "a75", "a76", "a77", "a78", "a79", "a80", "a81", "a82", "a83", "a84", "a85", "a86", "a87", "a88", "a89", "a90", "a91", "a92", "a93", "a94", "a95", "a96", "a97", "a98", "a99", "a100", "a101", "a102", "a103", "a104", "a105", "a106", "a107", "a108", "a109", "a110", "a111", "a112", "a113", "a114", "a115", "a116", "a117", "a118", "a119", "a120", "a121", "a122", "a123", "a124", "a125", "a126", "a127", "a128", "a129", "a130", "a131", "a132", "a133", "a134", "a135", "a136", "a137", "a138", "a139", "a140", "a141", "a142", "a143", "a144", "a145", "a146", "a147", "a148", "a149", "a150", "a151", "a152", "a153", "a154", "a155", "a156", "a157", "a158", "a159", "a160", "a161", "a162", "a163", "a164", "a165", "a166", "a167", "a168", "a169", "a170", "a171", "a172", "a173", "a174", "a175", "a176", "a177", "a178", "a179", "a180", "a181", "a182", "a183", "a184", "a185", "a186", "a187", "a188", "a189", "a190", "a191", "a192", "a193", "a194", "a195", "a196", "a197", "a198", "a199", "a200", "a201", "a202", "a203", "a204", "a205", "a206", "a207", "a208", "a209", "a210", "a211", "a212", "a213", "a214", "a215", "a216", "a217", "a218", "a219", "a220", "a221", "a222", "a223", "a224", "a225", "a226", "a227", "a228", "a229", "a230", "a231", "a232", "a233", "a234", "a235", "a236", "a237", "a238", "a239", "a240", "a241", "a242", "a243", "a244", "a245", "a246", "a247", "a248", "a249", "a250", "a251", "a252", "a253", "a254", "a255", "s36", "s37", "s38", "s39", "s40", "s41", "s42", "s43", "s44", "s45", "s46", "s47", "s48", "s49", "s50", "s51", "s52", "s53", "s54", "s55", "s56", "s57", "s58", "s59", "s60", "s61", "s62", "s63", "s64", "s65", "s66", "s67", "s68", "s69", "s70", "s71", "s72", "s73", "s74", "s75", "s76", "s77", "s78", "s79", "s80", "s81", "s82", "s83", "s84", "s85", "s86", "s87", "s88", "s89", "s90", "s91", "s92", "s93", "s94", "s95", "s96", "s97", "s98", "s99", "vcc", "scc", "memory");
    __builtin_unreachable();
  } else {
   if (kind != 6) {
    COMMON(kind == 1 ? 8 : 16, kind == 1 ? OFF_FXP : OFF_FH, (kind == 1 ? OFF_FH : OFF_FX1) + slice * 128, AUX + 1536, 2048, 2048, 4);
    const int dl = kind == 2 ? ((wave & 2) ? -1 : 1) : 0, r0 = 16 + l15 - dl, r1 = 16 + l15 - 2 * dl;
    const unsigned sb = (kind == 1 ? OFF_XP : OFF_HS) - 32768 + wave * 4096 + lg * 256;
    const unsigned vSrc = sb + (r0 >> 4) * 32768 + (r0 & 15) * 16, vSrc2 = sb + 16384 + (r1 >> 4) * 32768 + (r1 & 15) * 16;
    const unsigned vOut = (kind == 1 ? OFF_HS : OFF_X1) + (slice * 4 + wave) * 1024 + vA;
    const unsigned vX = AUX + wave * 2048 + (kind == 1 ? l15 * 16 : vA), vX2 = AUX + wave * 2048 + l15 * 8;
    const unsigned vAuxS = kind == 1 ? OFF_S1 + ln * 4 : OFF_XP + (slice * 4 + wave) * 1024 + vA;
    const unsigned vAuxS2 = ln < 32 ? OFF_S1 - 1024 + 256 + ln * 4 : OOB;
    constexpr unsigned RED = AUX + 24576 + 1536;
    const unsigned vOut2 = wave != 2 ? OOB : kind == 1 ? (slice == 0 && ln < 16 ? OFF_S1 + 256 + ln * 8 : OOB) : ln < 32 ? OFF_ST + slice * 128 + ln * 4 : OOB;
    const unsigned vStW = RED + wave * 4 + (kind == 1 ? l15 * 32 + (lg == lq ? 16 : 0) : (l15 * 2 + (lg & 1)) * 16), vStR = RED + (kind == 1 ? l15 * 32 : (ln & 31) * 16);
    const unsigned sWw = 16 * wave * 2048 + slice * 256;
    const u16* w12 = kind == 1 ? w1 : w2;
    unsigned epOut;
    asm volatile(R"(
Lcode_g12_b:
s_cmp_eq_u32 %8,2
s_cbranch_scc1 Lg2_begin_%=
s_mov_b64 s[36:37],%1
s_mov_b32 s38,111193216
s_mov_b32 s39,0x00020000
s_mov_b32 s54,%2
s_mov_b32 s55,%3
s_mov_b32 s56,%4
s_mov_b32 s58,%5
s_mov_b32 s83,%6
s_mov_b32 s82,%7
s_mov_b32 s84,%8
v_mov_b32 v0,%9
v_mov_b32 v1,%10
v_mov_b32 v2,%11
v_mov_b32 v3,%12
v_mov_b32 v4,%13
v_mov_b32 v6,%14
v_mov_b32 v7,%15
v_mov_b32 v8,%16
v_mov_b32 v9,%17
v_mov_b32 v10,%18
v_mov_b32 v11,%19
v_mov_b32 v12,%20
v_mov_b32 v13,%21
v_mov_b32 v14,%22
v_mov_b32 v28,%23
v_mov_b32 v29,%24
v_mov_b32 v30,%25
v_mov_b32 v31,%26
v_mov_b32 v32,%27
s_mov_b64 s[40:41],%28
s_mov_b32 s42,2097152
s_mov_b32 s43,0x00020000
s_lshl_b32 s88,s54,11
s_mov_b32 s69,111193088
buffer_load_dword v5,off,s[36:39],s69 sc1
s_add_u32 m0,s88,0
s_add_u32 s61,s82,0
buffer_load_dwordx4 v28,s[40:43],s61 offen offset:0  lds
s_add_u32 m0,s88,1024
s_nop 0
buffer_load_dwordx4 v29,s[40:43],s61 offen offset:0  lds
s_add_u32 m0,s88,8064
s_nop 0
buffer_load_dwordx4 v28,s[40:43],s61 offen offset:128  lds
s_add_u32 m0,s88,9088
s_nop 0
buffer_load_dwordx4 v29,s[40:43],s61 offen offset:128  lds
s_add_u32 m0,s88,16384
s_add_u32 s61,s82,131072
buffer_load_dwordx4 v28,s[40:43],s61 offen offset:0  lds
s_add_u32 m0,s88,17408
s_nop 0
buffer_load_dwordx4 v29,s[40:43],s61 offen offset:0  lds
s_add_u32 m0,s88,24448
s_nop 0
buffer_load_dwordx4 v28,s[40:43],s61 offen offset:128  lds
s_add_u32 m0,s88,25472
s_nop 0
buffer_load_dwordx4 v29,s[40:43],s61 offen offset:128  lds
s_add_u32 m0,s88,32768
s_add_u32 s61,s82,262144
buffer_load_dwordx4 v28,s[40:43],s61 offen offset:0  lds
s_add_u32 m0,s88,33792
s_nop 0
buffer_load_dwordx4 v29,s[40:43],s61 offen offset:0  lds
s_add_u32 m0,s88,40832
s_nop 0
buffer_load_dwordx4 v28,s[40:43],s61 offen offset:128  lds
s_add_u32 m0,s88,41856
s_nop 0
buffer_load_dwordx4 v29,s[40:43],s61 offen offset:128  lds
s_add_u32 m0,s88,49152
s_add_u32 s61,s82,393216
buffer_load_dwordx4 v28,s[40:43],s61 offen offset:0  lds
s_add_u32 m0,s88,50176
s_nop 0
buffer_load_dwordx4 v29,s[40:43],s61 offen offset:0  lds
s_add_u32 m0,s88,57216
s_nop 0
buffer_load_dwordx4 v28,s[40:43],s61 offen offset:128  lds
s_add_u32 m0,s88,58240
s_nop 0
buffer_load_dwordx4 v29,s[40:43],s61 offen offset:128  lds
s_add_u32 m0,s88,65536
s_add_u32 s61,s82,524288
buffer_load_dwordx4 v28,s[40:43],s61 offen offset:0  lds
s_add_u32 m0,s88,66560
s_nop 0
buffer_load_dwordx4 v29,s[40:43],s61 offen offset:0  lds
s_add_u32 m0,s88,73600
s_nop 0
buffer_load_dwordx4 v28,s[40:43],s61 offen offset:128  lds
s_add_u32 m0,s88,74624
s_nop 0
buffer_load_dwordx4 v29,s[40:43],s61 offen offset:128  lds
s_add_u32 m0,s88,81920
s_add_u32 s61,s82,655360
buffer_load_dwordx4 v28,s[40:43],s61 offen offset:0  lds
s_add_u32 m0,s88,82944
s_nop 0
buffer_load_dwordx4 v29,s[40:43],s61 offen offset:0  lds
s_add_u32 m0,s88,89984
s_nop 0
buffer_load_dwordx4 v28,s[40:43],s61 offen offset:128  lds
s_add_u32 m0,s88,91008
s_nop 0
buffer_load_dwordx4 v29,s[40:43],s61 offen offset:128  lds
s_add_u32 m0,s88,98304
s_add_u32 s61,s82,786432
buffer_load_dwordx4 v28,s[40:43],s61 offen offset:0  lds
s_add_u32 m0,s88,99328
s_nop 0
buffer_load_dwordx4 v29,s[40:43],s61 offen offset:0  lds
s_add_u32 m0,s88,106368
s_nop 0
buffer_load_dwordx4 v28,s[40:43],s61 offen offset:128  lds
s_add_u32 m0,s88,107392
s_nop 0
buffer_load_dwordx4 v29,s[40:43],s61 offen offset:128  lds
v_add_u32 v36,49152,v30
v_add_u32 v37,49152,v31
v_add_u32 v38,98304,v30
v_add_u32 v39,98304,v31
v_mov_b32 v76,0x3f803f80
v_mov_b32 v77,0x3f803f80
v_mov_b32 v78,0x3f803f80
v_mov_b32 v79,0x3f803f80
s_waitcnt vmcnt(24)
s_barrier
s_add_u32 m0,s88,114688
s_add_u32 s61,s82,917504
buffer_load_dwordx4 v28,s[40:43],s61 offen offset:0  lds
s_add_u32 m0,s88,115712
ds_read_b64_tr_b16 a[0:1],v30 offset:0
buffer_load_dwordx4 v29,s[40:43],s61 offen offset:0  lds
s_add_u32 m0,s88,122752
ds_read_b64_tr_b16 a[2:3],v30 offset:512
buffer_load_dwordx4 v28,s[40:43],s61 offen offset:128  lds
s_add_u32 m0,s88,123776
ds_read_b64_tr_b16 a[4:5],v31 offset:0
buffer_load_dwordx4 v29,s[40:43],s61 offen offset:128  lds
ds_read_b64_tr_b16 a[6:7],v31 offset:512
ds_read_b64_tr_b16 a[8:9],v30 offset:4096
ds_read_b64_tr_b16 a[10:11],v30 offset:4608
ds_read_b64_tr_b16 a[12:13],v31 offset:4096
ds_read_b64_tr_b16 a[14:15],v31 offset:4608
s_waitcnt vmcnt(24)
s_barrier
s_add_u32 m0,s88,131072
s_add_u32 s61,s82,1048576
buffer_load_dwordx4 v28,s[40:43],s61 offen offset:0  lds
s_add_u32 m0,s88,132096
ds_read_b64_tr_b16 a[16:17],v30 offset:16384
buffer_load_dwordx4 v29,s[40:43],s61 offen offset:0  lds
s_add_u32 m0,s88,139136
ds_read_b64_tr_b16 a[18:19],v30 offset:16896
buffer_load_dwordx4 v28,s[40:43],s61 offen offset:128  lds
s_add_u32 m0,s88,140160
ds_read_b64_tr_b16 a[20:21],v31 offset:16384
buffer_load_dwordx4 v29,s[40:43],s61 offen offset:128  lds
ds_read_b64_tr_b16 a[22:23],v31 offset:16896
ds_read_b64_tr_b16 a[24:25],v30 offset:20480
ds_read_b64_tr_b16 a[26:27],v30 offset:20992
ds_read_b64_tr_b16 a[28:29],v31 offset:20480
ds_read_b64_tr_b16 a[30:31],v31 offset:20992
s_waitcnt lgkmcnt(8)
v_mfma_f32_16x16x32_bf16 v[20:23],a[0:3],v[76:79],0
v_mfma_f32_16x16x32_bf16 v[24:27],a[4:7],v[76:79],0
v_mfma_f32_16x16x32_bf16 v[20:23],a[8:11],v[76:79],v[20:23]
v_mfma_f32_16x16x32_bf16 v[24:27],a[12:15],v[76:79],v[24:27]
s_waitcnt vmcnt(24)
s_barrier
s_add_u32 m0,s88,0
s_add_u32 s61,s82,1179648
buffer_load_dwordx4 v28,s[40:43],s61 offen offset:0  lds
s_add_u32 m0,s88,1024
ds_read_b64_tr_b16 a[32:33],v30 offset:32768
buffer_load_dwordx4 v29,s[40:43],s61 offen offset:0  lds
s_add_u32 m0,s88,8064
ds_read_b64_tr_b16 a[34:35],v30 offset:33280
buffer_load_dwordx4 v28,s[40:43],s61 offen offset:128  lds
s_add_u32 m0,s88,9088
ds_read_b64_tr_b16 a[36:37],v31 offset:32768
buffer_load_dwordx4 v29,s[40:43],s61 offen offset:128  lds
ds_read_b64_tr_b16 a[38:39],v31 offset:33280
ds_read_b64_tr_b16 a[40:41],v30 offset:36864
ds_read_b64_tr_b16 a[42:43],v30 offset:37376
ds_read_b64_tr_b16 a[44:45],v31 offset:36864
ds_read_b64_tr_b16 a[46:47],v31 offset:37376
s_waitcnt lgkmcnt(8)
v_mfma_f32_16x16x32_bf16 v[20:23],a[16:19],v[76:79],v[20:23]
v_mfma_f32_16x16x32_bf16 v[24:27],a[20:23],v[76:79],v[24:27]
v_mfma_f32_16x16x32_bf16 v[20:23],a[24:27],v[76:79],v[20:23]
v_mfma_f32_16x16x32_bf16 v[24:27],a[28:31],v[76:79],v[24:27]
s_waitcnt vmcnt(24)
s_barrier
s_add_u32 m0,s88,16384)"
R"(
s_add_u32 s61,s82,1310720
buffer_load_dwordx4 v28,s[40:43],s61 offen offset:0  lds
s_add_u32 m0,s88,17408
ds_read_b64_tr_b16 a[48:49],v36 offset:0
buffer_load_dwordx4 v29,s[40:43],s61 offen offset:0  lds
s_add_u32 m0,s88,24448
ds_read_b64_tr_b16 a[50:51],v36 offset:512
buffer_load_dwordx4 v28,s[40:43],s61 offen offset:128  lds
s_add_u32 m0,s88,25472
ds_read_b64_tr_b16 a[52:53],v37 offset:0
buffer_load_dwordx4 v29,s[40:43],s61 offen offset:128  lds
ds_read_b64_tr_b16 a[54:55],v37 offset:512
ds_read_b64_tr_b16 a[56:57],v36 offset:4096
ds_read_b64_tr_b16 a[58:59],v36 offset:4608
ds_read_b64_tr_b16 a[60:61],v37 offset:4096
ds_read_b64_tr_b16 a[62:63],v37 offset:4608
s_waitcnt lgkmcnt(8)
v_mfma_f32_16x16x32_bf16 v[20:23],a[32:35],v[76:79],v[20:23]
v_mfma_f32_16x16x32_bf16 v[24:27],a[36:39],v[76:79],v[24:27]
v_mfma_f32_16x16x32_bf16 v[20:23],a[40:43],v[76:79],v[20:23]
v_mfma_f32_16x16x32_bf16 v[24:27],a[44:47],v[76:79],v[24:27]
s_waitcnt vmcnt(24)
s_barrier
s_add_u32 m0,s88,32768
s_add_u32 s61,s82,1441792
buffer_load_dwordx4 v28,s[40:43],s61 offen offset:0  lds
s_add_u32 m0,s88,33792
ds_read_b64_tr_b16 a[64:65],v36 offset:16384
buffer_load_dwordx4 v29,s[40:43],s61 offen offset:0  lds
s_add_u32 m0,s88,40832
ds_read_b64_tr_b16 a[66:67],v36 offset:16896
buffer_load_dwordx4 v28,s[40:43],s61 offen offset:128  lds
s_add_u32 m0,s88,41856
ds_read_b64_tr_b16 a[68:69],v37 offset:16384
buffer_load_dwordx4 v29,s[40:43],s61 offen offset:128  lds
ds_read_b64_tr_b16 a[70:71],v37 offset:16896
ds_read_b64_tr_b16 a[72:73],v36 offset:20480
ds_read_b64_tr_b16 a[74:75],v36 offset:20992
ds_read_b64_tr_b16 a[76:77],v37 offset:20480
ds_read_b64_tr_b16 a[78:79],v37 offset:20992
s_waitcnt lgkmcnt(8)
v_mfma_f32_16x16x32_bf16 v[20:23],a[48:51],v[76:79],v[20:23]
v_mfma_f32_16x16x32_bf16 v[24:27],a[52:55],v[76:79],v[24:27]
v_mfma_f32_16x16x32_bf16 v[20:23],a[56:59],v[76:79],v[20:23]
v_mfma_f32_16x16x32_bf16 v[24:27],a[60:63],v[76:79],v[24:27]
s_waitcnt vmcnt(24)
s_barrier
s_add_u32 m0,s88,49152
s_add_u32 s61,s82,1572864
buffer_load_dwordx4 v28,s[40:43],s61 offen offset:0  lds
s_add_u32 m0,s88,50176
ds_read_b64_tr_b16 a[80:81],v36 offset:32768
buffer_load_dwordx4 v29,s[40:43],s61 offen offset:0  lds
s_add_u32 m0,s88,57216
ds_read_b64_tr_b16 a[82:83],v36 offset:33280
buffer_load_dwordx4 v28,s[40:43],s61 offen offset:128  lds
s_add_u32 m0,s88,58240
ds_read_b64_tr_b16 a[84:85],v37 offset:32768
buffer_load_dwordx4 v29,s[40:43],s61 offen offset:128  lds
ds_read_b64_tr_b16 a[86:87],v37 offset:33280
ds_read_b64_tr_b16 a[88:89],v36 offset:36864
ds_read_b64_tr_b16 a[90:91],v36 offset:37376
ds_read_b64_tr_b16 a[92:93],v37 offset:36864
ds_read_b64_tr_b16 a[94:95],v37 offset:37376
s_waitcnt lgkmcnt(8)
v_mfma_f32_16x16x32_bf16 v[20:23],a[64:67],v[76:79],v[20:23]
v_mfma_f32_16x16x32_bf16 v[24:27],a[68:71],v[76:79],v[24:27]
v_mfma_f32_16x16x32_bf16 v[20:23],a[72:75],v[76:79],v[20:23]
v_mfma_f32_16x16x32_bf16 v[24:27],a[76:79],v[76:79],v[24:27]
s_waitcnt vmcnt(24)
s_barrier
s_add_u32 m0,s88,65536
s_add_u32 s61,s82,1703936
buffer_load_dwordx4 v28,s[40:43],s61 offen offset:0  lds
s_add_u32 m0,s88,66560
ds_read_b64_tr_b16 a[96:97],v38 offset:0
buffer_load_dwordx4 v29,s[40:43],s61 offen offset:0  lds
s_add_u32 m0,s88,73600
ds_read_b64_tr_b16 a[98:99],v38 offset:512
buffer_load_dwordx4 v28,s[40:43],s61 offen offset:128  lds
s_add_u32 m0,s88,74624
ds_read_b64_tr_b16 a[100:101],v39 offset:0
buffer_load_dwordx4 v29,s[40:43],s61 offen offset:128  lds
ds_read_b64_tr_b16 a[102:103],v39 offset:512
ds_read_b64_tr_b16 a[104:105],v38 offset:4096
ds_read_b64_tr_b16 a[106:107],v38 offset:4608
ds_read_b64_tr_b16 a[108:109],v39 offset:4096
ds_read_b64_tr_b16 a[110:111],v39 offset:4608
s_waitcnt lgkmcnt(8)
v_mfma_f32_16x16x32_bf16 v[20:23],a[80:83],v[76:79],v[20:23]
v_mfma_f32_16x16x32_bf16 v[24:27],a[84:87],v[76:79],v[24:27]
v_mfma_f32_16x16x32_bf16 v[20:23],a[88:91],v[76:79],v[20:23]
v_mfma_f32_16x16x32_bf16 v[24:27],a[92:95],v[76:79],v[24:27]
s_waitcnt vmcnt(24)
s_barrier
s_add_u32 m0,s88,81920
s_add_u32 s61,s82,1835008
buffer_load_dwordx4 v28,s[40:43],s61 offen offset:0  lds
s_add_u32 m0,s88,82944
ds_read_b64_tr_b16 a[112:113],v38 offset:16384
buffer_load_dwordx4 v29,s[40:43],s61 offen offset:0  lds
s_add_u32 m0,s88,89984
ds_read_b64_tr_b16 a[114:115],v38 offset:16896
buffer_load_dwordx4 v28,s[40:43],s61 offen offset:128  lds
s_add_u32 m0,s88,91008
ds_read_b64_tr_b16 a[116:117],v39 offset:16384
buffer_load_dwordx4 v29,s[40:43],s61 offen offset:128  lds
ds_read_b64_tr_b16 a[118:119],v39 offset:16896
ds_read_b64_tr_b16 a[120:121],v38 offset:20480
ds_read_b64_tr_b16 a[122:123],v38 offset:20992
ds_read_b64_tr_b16 a[124:125],v39 offset:20480
ds_read_b64_tr_b16 a[126:127],v39 offset:20992
s_waitcnt lgkmcnt(8)
v_mfma_f32_16x16x32_bf16 v[20:23],a[96:99],v[76:79],v[20:23]
v_mfma_f32_16x16x32_bf16 v[24:27],a[100:103],v[76:79],v[24:27]
v_mfma_f32_16x16x32_bf16 v[20:23],a[104:107],v[76:79],v[20:23]
v_mfma_f32_16x16x32_bf16 v[24:27],a[108:111],v[76:79],v[24:27]
s_waitcnt vmcnt(24)
s_barrier
s_add_u32 m0,s88,98304
s_add_u32 s61,s82,1966080
buffer_load_dwordx4 v28,s[40:43],s61 offen offset:0  lds
s_add_u32 m0,s88,99328
ds_read_b64_tr_b16 a[128:129],v38 offset:32768
buffer_load_dwordx4 v29,s[40:43],s61 offen offset:0  lds
s_add_u32 m0,s88,106368
ds_read_b64_tr_b16 a[130:131],v38 offset:33280
buffer_load_dwordx4 v28,s[40:43],s61 offen offset:128  lds
s_add_u32 m0,s88,107392
ds_read_b64_tr_b16 a[132:133],v39 offset:32768
buffer_load_dwordx4 v29,s[40:43],s61 offen offset:128  lds
ds_read_b64_tr_b16 a[134:135],v39 offset:33280
ds_read_b64_tr_b16 a[136:137],v38 offset:36864
ds_read_b64_tr_b16 a[138:139],v38 offset:37376
ds_read_b64_tr_b16 a[140:141],v39 offset:36864
ds_read_b64_tr_b16 a[142:143],v39 offset:37376
s_waitcnt lgkmcnt(8)
v_mfma_f32_16x16x32_bf16 v[20:23],a[112:115],v[76:79],v[20:23]
v_mfma_f32_16x16x32_bf16 v[24:27],a[116:119],v[76:79],v[24:27]
v_mfma_f32_16x16x32_bf16 v[20:23],a[120:123],v[76:79],v[20:23]
v_mfma_f32_16x16x32_bf16 v[24:27],a[124:127],v[76:79],v[24:27]
s_waitcnt vmcnt(24)
s_barrier
ds_read_b64_tr_b16 a[144:145],v30 offset:0
ds_read_b64_tr_b16 a[146:147],v30 offset:512
ds_read_b64_tr_b16 a[148:149],v31 offset:0
ds_read_b64_tr_b16 a[150:151],v31 offset:512
ds_read_b64_tr_b16 a[152:153],v30 offset:4096
ds_read_b64_tr_b16 a[154:155],v30 offset:4608
ds_read_b64_tr_b16 a[156:157],v31 offset:4096
ds_read_b64_tr_b16 a[158:159],v31 offset:4608
s_waitcnt lgkmcnt(8)
v_mfma_f32_16x16x32_bf16 v[20:23],a[128:131],v[76:79],v[20:23]
v_mfma_f32_16x16x32_bf16 v[24:27],a[132:135],v[76:79],v[24:27]
v_mfma_f32_16x16x32_bf16 v[20:23],a[136:139],v[76:79],v[20:23]
v_mfma_f32_16x16x32_bf16 v[24:27],a[140:143],v[76:79],v[24:27]
s_waitcnt vmcnt(20)
s_barrier
ds_read_b64_tr_b16 a[160:161],v30 offset:16384
ds_read_b64_tr_b16 a[162:163],v30 offset:16896
ds_read_b64_tr_b16 a[164:165],v31 offset:16384
ds_read_b64_tr_b16 a[166:167],v31 offset:16896
ds_read_b64_tr_b16 a[168:169],v30 offset:20480
ds_read_b64_tr_b16 a[170:171],v30 offset:20992
ds_read_b64_tr_b16 a[172:173],v31 offset:20480
ds_read_b64_tr_b16 a[174:175],v31 offset:20992
s_waitcnt lgkmcnt(8)
v_mfma_f32_16x16x32_bf16 v[20:23],a[144:147],v[76:79],v[20:23]
v_mfma_f32_16x16x32_bf16 v[24:27],a[148:151],v[76:79],v[24:27]
v_mfma_f32_16x16x32_bf16 v[20:23],a[152:155],v[76:79],v[20:23]
v_mfma_f32_16x16x32_bf16 v[24:27],a[156:159],v[76:79],v[24:27]
s_waitcnt vmcnt(16)
s_barrier
ds_read_b64_tr_b16 a[176:177],v30 offset:32768
ds_read_b64_tr_b16 a[178:179],v30 offset:33280
ds_read_b64_tr_b16 a[180:181],v31 offset:32768
ds_read_b64_tr_b16 a[182:183],v31 offset:33280
ds_read_b64_tr_b16 a[184:185],v30 offset:36864
ds_read_b64_tr_b16 a[186:187],v30 offset:37376
ds_read_b64_tr_b16 a[188:189],v31 offset:36864
ds_read_b64_tr_b16 a[190:191],v31 offset:37376
s_waitcnt lgkmcnt(8)
v_mfma_f32_16x16x32_bf16 v[20:23],a[160:163],v[76:79],v[20:23]
v_mfma_f32_16x16x32_bf16 v[24:27],a[164:167],v[76:79],v[24:27]
v_mfma_f32_16x16x32_bf16 v[20:23],a[168:171],v[76:79],v[20:23]
v_mfma_f32_16x16x32_bf16 v[24:27],a[172:175],v[76:79],v[24:27]
s_waitcnt vmcnt(12)
s_barrier
ds_read_b64_tr_b16 a[192:193],v36 offset:0
ds_read_b64_tr_b16 a[194:195],v36 offset:512
ds_read_b64_tr_b16 a[196:197],v37 offset:0
ds_read_b64_tr_b16 a[198:199],v37 offset:512
ds_read_b64_tr_b16 a[200:201],v36 offset:4096
ds_read_b64_tr_b16 a[202:203],v36 offset:4608
ds_read_b64_tr_b16 a[204:205],v37 offset:4096
ds_read_b64_tr_b16 a[206:207],v37 offset:4608
s_waitcnt lgkmcnt(8)
v_mfma_f32_16x16x32_bf16 v[20:23],a[176:179],v[76:79],v[20:23]
v_mfma_f32_16x16x32_bf16 v[24:27],a[180:183],v[76:79],v[24:27]
v_mfma_f32_16x16x32_bf16 v[20:23],a[184:187],v[76:79],v[20:23])"
R"(
v_mfma_f32_16x16x32_bf16 v[24:27],a[188:191],v[76:79],v[24:27]
s_waitcnt vmcnt(8)
s_barrier
ds_read_b64_tr_b16 a[208:209],v36 offset:16384
ds_read_b64_tr_b16 a[210:211],v36 offset:16896
ds_read_b64_tr_b16 a[212:213],v37 offset:16384
ds_read_b64_tr_b16 a[214:215],v37 offset:16896
ds_read_b64_tr_b16 a[216:217],v36 offset:20480
ds_read_b64_tr_b16 a[218:219],v36 offset:20992
ds_read_b64_tr_b16 a[220:221],v37 offset:20480
ds_read_b64_tr_b16 a[222:223],v37 offset:20992
s_waitcnt lgkmcnt(8)
v_mfma_f32_16x16x32_bf16 v[20:23],a[192:195],v[76:79],v[20:23]
v_mfma_f32_16x16x32_bf16 v[24:27],a[196:199],v[76:79],v[24:27]
v_mfma_f32_16x16x32_bf16 v[20:23],a[200:203],v[76:79],v[20:23]
v_mfma_f32_16x16x32_bf16 v[24:27],a[204:207],v[76:79],v[24:27]
s_waitcnt vmcnt(4)
s_barrier
ds_read_b64_tr_b16 a[224:225],v36 offset:32768
ds_read_b64_tr_b16 a[226:227],v36 offset:33280
ds_read_b64_tr_b16 a[228:229],v37 offset:32768
ds_read_b64_tr_b16 a[230:231],v37 offset:33280
ds_read_b64_tr_b16 a[232:233],v36 offset:36864
ds_read_b64_tr_b16 a[234:235],v36 offset:37376
ds_read_b64_tr_b16 a[236:237],v37 offset:36864
ds_read_b64_tr_b16 a[238:239],v37 offset:37376
s_waitcnt lgkmcnt(8)
v_mfma_f32_16x16x32_bf16 v[20:23],a[208:211],v[76:79],v[20:23]
v_mfma_f32_16x16x32_bf16 v[24:27],a[212:215],v[76:79],v[24:27]
v_mfma_f32_16x16x32_bf16 v[20:23],a[216:219],v[76:79],v[20:23]
v_mfma_f32_16x16x32_bf16 v[24:27],a[220:223],v[76:79],v[24:27]
s_waitcnt vmcnt(0)
s_barrier
ds_read_b64_tr_b16 a[240:241],v38 offset:0
ds_read_b64_tr_b16 a[242:243],v38 offset:512
ds_read_b64_tr_b16 a[244:245],v39 offset:0
ds_read_b64_tr_b16 a[246:247],v39 offset:512
ds_read_b64_tr_b16 a[248:249],v38 offset:4096
ds_read_b64_tr_b16 a[250:251],v38 offset:4608
ds_read_b64_tr_b16 a[252:253],v39 offset:4096
ds_read_b64_tr_b16 a[254:255],v39 offset:4608
s_waitcnt lgkmcnt(8)
v_mfma_f32_16x16x32_bf16 v[20:23],a[224:227],v[76:79],v[20:23]
v_mfma_f32_16x16x32_bf16 v[24:27],a[228:231],v[76:79],v[24:27]
v_mfma_f32_16x16x32_bf16 v[20:23],a[232:235],v[76:79],v[20:23]
v_mfma_f32_16x16x32_bf16 v[24:27],a[236:239],v[76:79],v[24:27]
s_waitcnt lgkmcnt(0)
v_mfma_f32_16x16x32_bf16 v[20:23],a[240:243],v[76:79],v[20:23]
v_mfma_f32_16x16x32_bf16 v[24:27],a[244:247],v[76:79],v[24:27]
v_mfma_f32_16x16x32_bf16 v[20:23],a[248:251],v[76:79],v[20:23]
v_mfma_f32_16x16x32_bf16 v[24:27],a[252:255],v[76:79],v[24:27]
s_waitcnt lgkmcnt(0)
s_barrier
v_readfirstlane_b32 s60,v5
s_nop 3
s_add_u32 s60,s60,1
s_cmp_eq_u32 s60,0
s_cselect_b32 s60,1,s60
Lg1_init_%=:
v_mov_b32 v5,s60
s_mov_b32 s59,536870912
s_add_u32 s57,s56,131072
s_mov_b32 s79,0
s_mov_b32 s75,0x3a800000
s_mov_b32 s76,0xc0135761
s_mov_b32 s77,0x3d372713
s_mov_b32 s44,s59
s_mov_b32 s45,s59
s_mov_b32 s46,s59
s_mov_b32 s47,s59
s_mov_b32 s48,s83
s_add_u32 s49,s48,s55
s_cmp_eq_u32 s49,s56
s_cselect_b32 s49,s57,s49
s_cmp_eq_u32 s49,s58
s_cselect_b32 s49,s59,s49
s_add_u32 s50,s49,s55
s_cmp_eq_u32 s50,s56
s_cselect_b32 s50,s57,s50
s_cmp_eq_u32 s50,s58
s_cselect_b32 s50,s59,s50
s_add_u32 s51,s50,s55
s_cmp_eq_u32 s51,s56
s_cselect_b32 s51,s57,s51
s_cmp_eq_u32 s51,s58
s_cselect_b32 s51,s59,s51
s_add_u32 s52,s51,s55
s_cmp_eq_u32 s52,s56
s_cselect_b32 s52,s57,s52
s_cmp_eq_u32 s52,s58
s_cselect_b32 s52,s59,s52
s_lshl_b32 s63,s54,12
s_add_u32 s63,s63,65536
s_add_u32 s64,s63,32768
s_lshl_b32 s88,s54,10
s_add_u32 s88,s88,131072
s_lshl_b32 s78,s54,11
s_add_u32 s78,s78,132608
v_mov_b32 v15,0x3727c5ac
v_mov_b32 v100,0x3f803f80
v_mov_b32 v101,0x3f803f80
v_mov_b32 v102,0x3f803f80
v_mov_b32 v103,0x3f803f80
s_mov_b32 s89,1048576
s_mov_b32 s82,0xaaaaaaaa
s_mov_b32 s83,0xaaaaaaaa
s_mov_b32 s84,0xcccccccc
s_mov_b32 s85,0xcccccccc
s_mov_b32 s98,0x00f0000f
s_mov_b32 s99,0xf0000f00
s_getpc_b64 s[90:91]
Lg1_pc_%=:
s_mov_b32 s68,Lg1_d1w0_%=-Lg1_pc_%=
s_cmp_eq_u32 s54,1
s_cselect_b32 s68,Lg1_d1w1_%=-Lg1_pc_%=,s68
s_cmp_eq_u32 s54,2
s_cselect_b32 s68,Lg1_d1w2_%=-Lg1_pc_%=,s68
s_cmp_eq_u32 s54,3
s_cselect_b32 s68,Lg1_d1w3_%=-Lg1_pc_%=,s68
s_add_u32 s96,s90,s68
s_addc_u32 s97,s91,0
s_mov_b32 s68,Lg1_d0w0_%=-Lg1_pc_%=
s_cmp_eq_u32 s54,1
s_cselect_b32 s68,Lg1_d0w1_%=-Lg1_pc_%=,s68
s_cmp_eq_u32 s54,2
s_cselect_b32 s68,Lg1_d0w2_%=-Lg1_pc_%=,s68
s_cmp_eq_u32 s54,3
s_cselect_b32 s68,Lg1_d0w3_%=-Lg1_pc_%=,s68
s_add_u32 s94,s90,s68
s_addc_u32 s95,s91,0
s_mov_b32 s68,Lg1_a1w0_%=-Lg1_pc_%=
s_cmp_eq_u32 s54,1
s_cselect_b32 s68,Lg1_a1w1_%=-Lg1_pc_%=,s68
s_cmp_eq_u32 s54,2
s_cselect_b32 s68,Lg1_a1w2_%=-Lg1_pc_%=,s68
s_cmp_eq_u32 s54,3
s_cselect_b32 s68,Lg1_a1w3_%=-Lg1_pc_%=,s68
s_add_u32 s92,s90,s68
s_addc_u32 s93,s91,0
s_mov_b32 s68,Lg1_a0w0_%=-Lg1_pc_%=
s_cmp_eq_u32 s54,1
s_cselect_b32 s68,Lg1_a0w1_%=-Lg1_pc_%=,s68
s_cmp_eq_u32 s54,2
s_cselect_b32 s68,Lg1_a0w2_%=-Lg1_pc_%=,s68
s_cmp_eq_u32 s54,3
s_cselect_b32 s68,Lg1_a0w3_%=-Lg1_pc_%=,s68
s_add_u32 s90,s90,s68
s_addc_u32 s91,s91,0
s_mov_b32 s66,s59
s_mov_b32 s65,0
s_mov_b32 s62,s63
s_branch Lg1_slow0_%=
Lg1_a0w0_%=:
v_mfma_f32_16x16x32_bf16 v[28:31],a[0:3],v[44:47],0
s_add_u32 m0,s62,16384
ds_read_b128 v[72:75],v2 offset:7168
s_lshr_b32 s73,s47,0
v_mfma_f32_16x16x32_bf16 v[32:35],a[4:7],v[44:47],0
buffer_load_dwordx4 v1,s[36:39],s66 offen offset:0 sc1 lds
v_mfma_f32_16x16x32_bf16 v[104:107],v[100:103],v[44:47],0
s_lshr_b32 s72,s66,6
s_lshr_b32 s71,s62,2
s_cmp_lt_u32 s66,4194304
v_mfma_f32_16x16x32_bf16 v[108:111],v[44:47],v[44:47],0
s_cselect_b32 s72,s59,s72
v_fma_f32 v36,-v96,v20,v36
v_fma_f32 v37,-v96,v21,v37
v_mfma_f32_16x16x32_bf16 v[28:31],a[8:11],v[48:51],v[28:31]
ds_read_b128 v[44:47],v2 offset:8192
v_fma_f32 v38,-v96,v22,v38
v_mfma_f32_16x16x32_bf16 v[32:35],a[12:15],v[48:51],v[32:35]
buffer_load_dwordx4 v1,s[36:39],s66 offen offset:1024 sc1 lds
v_mfma_f32_16x16x32_bf16 v[104:107],v[100:103],v[48:51],v[104:107]
v_fma_f32 v39,-v96,v23,v39
v_mul_f32 v76,v36,v36
v_mfma_f32_16x16x32_bf16 v[108:111],v[48:51],v[48:51],v[108:111]
v_mul_f32 v77,v37,v37
v_mul_f32 v78,v38,v38
v_mfma_f32_16x16x32_bf16 v[28:31],a[16:19],v[52:55],v[28:31]
ds_read_b128 v[48:51],v2 offset:9216
v_mul_f32 v79,v39,v39
v_mfma_f32_16x16x32_bf16 v[32:35],a[20:23],v[52:55],v[32:35]
buffer_load_dwordx4 v1,s[36:39],s66 offen offset:2048 sc1 lds
v_mfma_f32_16x16x32_bf16 v[104:107],v[100:103],v[52:55],v[104:107]
v_fma_f32 v76,v98,v76,v97
v_mul_f32 v76,v36,v76
v_mfma_f32_16x16x32_bf16 v[108:111],v[52:55],v[52:55],v[108:111]
v_exp_f32 v76,v76
v_mfma_f32_16x16x32_bf16 v[28:31],a[24:27],v[56:59],v[28:31]
ds_read_b128 v[52:55],v2 offset:10240
v_fma_f32 v76,v76,v99,v99
v_mfma_f32_16x16x32_bf16 v[32:35],a[28:31],v[56:59],v[32:35]
buffer_load_dwordx4 v1,s[36:39],s66 offen offset:3072 sc1 lds
v_mfma_f32_16x16x32_bf16 v[104:107],v[100:103],v[56:59],v[104:107]
s_add_u32 m0,s71,s88
v_rcp_f32 v76,v76
v_mfma_f32_16x16x32_bf16 v[108:111],v[56:59],v[56:59],v[108:111]
s_waitcnt lgkmcnt(3)
buffer_load_dword v10,s[36:39],s72 offen offset:0 sc1 lds
v_mfma_f32_16x16x32_bf16 v[28:31],a[32:35],v[60:63],v[28:31])"
R"(
s_xor_b32 s87,s64,65536
ds_read_b128 v[56:59],v2 offset:11264
v_fma_f32 v77,v98,v77,v97
v_mfma_f32_16x16x32_bf16 v[32:35],a[36:39],v[60:63],v[32:35]
s_cmp_ge_u32 s66,s59
v_mul_f32 v77,v37,v77
v_fma_f32 v78,v98,v78,v97
v_mfma_f32_16x16x32_bf16 v[28:31],a[40:43],v[64:67],v[28:31]
s_cselect_b32 s86,s49,s50
ds_read_b128 v[60:63],v2 offset:12288
v_mul_f32 v78,v38,v78
v_mfma_f32_16x16x32_bf16 v[32:35],a[44:47],v[64:67],v[32:35]
s_cselect_b32 s87,s87,s63
v_exp_f32 v77,v77
v_mfma_f32_16x16x32_bf16 v[28:31],a[48:51],v[68:71],v[28:31]
s_lshr_b32 s69,s50,5
ds_read_b128 v[64:67],v2 offset:13312
v_fma_f32 v77,v77,v99,v99
v_mfma_f32_16x16x32_bf16 v[32:35],a[52:55],v[68:71],v[32:35]
s_cmp_lt_u32 s50,4194304
v_exp_f32 v78,v78
v_mfma_f32_16x16x32_bf16 v[28:31],a[56:59],v[72:75],v[28:31]
s_cselect_b32 s68,0xffffcfff,-1
ds_read_b128 v[68:71],v2 offset:14336
v_fma_f32 v78,v78,v99,v99
v_mfma_f32_16x16x32_bf16 v[32:35],a[60:63],v[72:75],v[32:35]
s_waitcnt lgkmcnt(3)
v_rcp_f32 v77,v77
v_mfma_f32_16x16x32_bf16 v[28:31],a[64:67],v[44:47],v[28:31]
s_waitcnt vmcnt(11)
ds_read_b128 v[72:75],v2 offset:15360
v_fma_f32 v79,v98,v79,v97
v_mfma_f32_16x16x32_bf16 v[32:35],a[68:71],v[44:47],v[32:35]
s_barrier
v_mfma_f32_16x16x32_bf16 v[28:31],a[72:75],v[48:51],v[28:31]
s_and_b32 s68,s68,s69
ds_read_b128 v[44:47],v2 offset:16384
v_mul_f32 v79,v39,v79
v_mfma_f32_16x16x32_bf16 v[32:35],a[76:79],v[48:51],v[32:35]
s_cmp_lt_u32 s50,s89
v_exp_f32 v79,v79
v_mfma_f32_16x16x32_bf16 v[28:31],a[80:83],v[52:55],v[28:31]
s_cselect_b32 s68,s59,s68
ds_read_b128 v[48:51],v2 offset:17408
v_fma_f32 v79,v79,v99,v99
v_mfma_f32_16x16x32_bf16 v[32:35],a[84:87],v[52:55],v[32:35]
s_lshr_b32 s69,s51,5
v_rcp_f32 v78,v78
v_mfma_f32_16x16x32_bf16 v[28:31],a[88:91],v[56:59],v[28:31]
s_cmp_lt_u32 s51,4194304
ds_read_b128 v[52:55],v2 offset:18432
v_mul_f32 v36,v36,v76
v_mfma_f32_16x16x32_bf16 v[32:35],a[92:95],v[56:59],v[32:35]
s_waitcnt lgkmcnt(3)
v_rcp_f32 v79,v79
v_mfma_f32_16x16x32_bf16 v[28:31],a[96:99],v[60:63],v[28:31]
s_cselect_b32 s70,0xffffcfff,-1
ds_read_b128 v[56:59],v2 offset:19456
v_mul_f32 v37,v37,v77
v_mfma_f32_16x16x32_bf16 v[32:35],a[100:103],v[60:63],v[32:35]
s_and_b32 s70,s70,s69
v_mul_f32 v38,v38,v78
v_mul_f32 v39,v39,v79
v_mfma_f32_16x16x32_bf16 v[28:31],a[104:107],v[64:67],v[28:31]
s_cmp_lt_u32 s51,s89
ds_read_b128 v[60:63],v2 offset:20480
v_cvt_pk_bf16_f32 v84,v36,v37
v_mfma_f32_16x16x32_bf16 v[32:35],a[108:111],v[64:67],v[32:35]
s_cselect_b32 s70,s59,s70
v_cvt_pk_bf16_f32 v85,v38,v39
v_fma_f32 v40,-v96,v24,v40
v_mfma_f32_16x16x32_bf16 v[28:31],a[112:115],v[68:71],v[28:31]
s_mov_b32 s62,s87
ds_read_b128 v[64:67],v2 offset:21504
v_fma_f32 v41,-v96,v25,v41
v_mfma_f32_16x16x32_bf16 v[32:35],a[116:119],v[68:71],v[32:35]
s_cmp_lt_u32 s86,s89
v_fma_f32 v42,-v96,v26,v42
v_fma_f32 v43,-v96,v27,v43
v_mfma_f32_16x16x32_bf16 v[28:31],a[120:123],v[72:75],v[28:31]
ds_read_b128 v[68:71],v2 offset:22528
v_mul_f32 v80,v40,v40
v_mfma_f32_16x16x32_bf16 v[32:35],a[124:127],v[72:75],v[32:35]
s_waitcnt lgkmcnt(3)
v_mul_f32 v81,v41,v41
v_mul_f32 v82,v42,v42
v_mfma_f32_16x16x32_bf16 v[28:31],a[128:131],v[44:47],v[28:31]
ds_read_b128 v[72:75],v2 offset:23552
v_mul_f32 v83,v43,v43
v_mfma_f32_16x16x32_bf16 v[32:35],a[132:135],v[44:47],v[32:35]
v_fma_f32 v80,v98,v80,v97
v_mul_f32 v80,v40,v80
v_mfma_f32_16x16x32_bf16 v[104:107],v[100:103],v[44:47],v[104:107]
v_exp_f32 v80,v80
v_mfma_f32_16x16x32_bf16 v[108:111],v[44:47],v[44:47],v[108:111]
v_fma_f32 v80,v80,v99,v99
v_fma_f32 v81,v98,v81,v97
v_mfma_f32_16x16x32_bf16 v[28:31],a[136:139],v[48:51],v[28:31]
ds_read_b128 v[44:47],v2 offset:24576
v_mul_f32 v81,v41,v81
v_mfma_f32_16x16x32_bf16 v[32:35],a[140:143],v[48:51],v[32:35]
v_exp_f32 v81,v81
v_mfma_f32_16x16x32_bf16 v[104:107],v[100:103],v[48:51],v[104:107]
s_waitcnt vmcnt(10)
v_rcp_f32 v80,v80
v_mfma_f32_16x16x32_bf16 v[108:111],v[48:51],v[48:51],v[108:111]
s_barrier
v_mfma_f32_16x16x32_bf16 v[28:31],a[144:147],v[52:55],v[28:31]
ds_read_b128 v[48:51],v2 offset:25600
ds_read_b32 v16,v7 offset:0
v_mfma_f32_16x16x32_bf16 v[32:35],a[148:151],v[52:55],v[32:35]
v_fma_f32 v81,v81,v99,v99
v_fma_f32 v82,v98,v82,v97
v_mfma_f32_16x16x32_bf16 v[104:107],v[100:103],v[52:55],v[104:107]
v_rcp_f32 v81,v81
v_mfma_f32_16x16x32_bf16 v[108:111],v[52:55],v[52:55],v[108:111]
v_mul_f32 v82,v42,v82
v_fma_f32 v83,v98,v83,v97
v_mfma_f32_16x16x32_bf16 v[28:31],a[152:155],v[56:59],v[28:31]
s_waitcnt lgkmcnt(0)
ds_read_b128 v[52:55],v2 offset:26624
v_mul_f32 v83,v43,v83
v_mfma_f32_16x16x32_bf16 v[32:35],a[156:159],v[56:59],v[32:35]
v_cmp_eq_u32 vcc,s60,v16
v_exp_f32 v82,v82
v_mul_f32 v40,v40,v80
v_mfma_f32_16x16x32_bf16 v[104:107],v[100:103],v[56:59],v[104:107]
s_cselect_b32 vcc_lo,-1,vcc_lo
v_exp_f32 v83,v83
v_mfma_f32_16x16x32_bf16 v[108:111],v[56:59],v[56:59],v[108:111]
v_fma_f32 v82,v82,v99,v99
v_fma_f32 v83,v83,v99,v99
v_mfma_f32_16x16x32_bf16 v[28:31],a[160:163],v[60:63],v[28:31]
s_cmp_eq_u32 vcc_lo,-1
ds_read_b128 v[56:59],v2 offset:27648
v_mul_f32 v41,v41,v81
v_mfma_f32_16x16x32_bf16 v[32:35],a[164:167],v[60:63],v[32:35]
s_cselect_b32 s67,s86,s59
s_cmp_ge_u32 s67,s59
s_cselect_b32 s68,s68,s70
s_add_u32 m0,s78,256
v_mfma_f32_16x16x32_bf16 v[28:31],a[168:171],v[64:67],v[28:31]
buffer_load_dword v6,s[36:39],s68 offen offset:0 sc1 lds
v_cndmask_b32 v112,v108,v109,s[82:83]
v_cndmask_b32 v113,v110,v111,s[82:83]
v_cndmask_b32 v112,v112,v113,s[84:85]
v_cndmask_b32 v112,v104,v112,s[98:99]
v_mfma_f32_16x16x32_bf16 v[32:35],a[172:175],v[64:67],v[32:35]
s_mov_b32 m0,s62
ds_read_b128 v[60:63],v2 offset:28672
ds_write_b32 v13,v112 offset:0
v_mfma_f32_16x16x32_bf16 v[28:31],a[176:179],v[68:71],v[28:31]
buffer_load_dwordx4 v0,s[36:39],s67 offen offset:0 sc1 lds
v_mfma_f32_16x16x32_bf16 v[32:35],a[180:183],v[68:71],v[32:35]
s_xor_b32 s63,s63,65536
ds_read_b128 v[64:67],v2 offset:29696
v_cvt_pk_bf16_f32 v86,v40,v41
v_mfma_f32_16x16x32_bf16 v[28:31],a[184:187],v[72:75],v[28:31]
s_sub_u32 s69,s67,1048576
ds_read_b128 v[68:71],v2 offset:30720
s_cmp_lt_u32 s69,3145728
v_mfma_f32_16x16x32_bf16 v[32:35],a[188:191],v[72:75],v[32:35]
s_waitcnt lgkmcnt(4)
v_rcp_f32 v82,v82
v_mfma_f32_16x16x32_bf16 v[28:31],a[192:195],v[44:47],v[28:31]
s_waitcnt vmcnt(7) lgkmcnt(2)
ds_read_b128 v[72:75],v2 offset:31744
v_mul_f32 v42,v42,v82
v_mfma_f32_16x16x32_bf16 v[32:35],a[196:199],v[44:47],v[32:35]
s_barrier
v_mfma_f32_16x16x32_bf16 v[28:31],a[200:203],v[48:51],v[28:31]
buffer_load_dwordx4 v0,s[36:39],s67 offen offset:1024 sc1 lds
v_mfma_f32_16x16x32_bf16 v[32:35],a[204:207],v[48:51],v[32:35]
s_cselect_b32 s89,4194304,s89
ds_read_b128 v[44:47],v2 offset:32768
ds_read_b128 v[116:119],v14 offset:0
v_mfma_f32_16x16x32_bf16 v[28:31],a[208:211],v[52:55],v[28:31]
s_add_u32 s53,s52,s55
ds_read_b128 v[120:123],v14 offset:16
ds_read_b128 v[48:51],v2 offset:33792
v_mfma_f32_16x16x32_bf16 v[32:35],a[212:215],v[52:55],v[32:35]
s_cmp_eq_u32 s53,s56
v_rcp_f32 v83,v83
v_mfma_f32_16x16x32_bf16 v[28:31],a[216:219],v[56:59],v[28:31]
buffer_load_dwordx4 v0,s[36:39],s67 offen offset:2048 sc1 lds
v_mfma_f32_16x16x32_bf16 v[32:35],a[220:223],v[56:59],v[32:35]
s_waitcnt lgkmcnt(4)
ds_read_b128 v[52:55],v2 offset:34816
v_mul_f32 v43,v43,v83
v_mfma_f32_16x16x32_bf16 v[28:31],a[224:227],v[60:63],v[28:31]
s_waitcnt lgkmcnt(2)
ds_read_b128 v[56:59],v2 offset:35840
v_cvt_pk_bf16_f32 v87,v42,v43
v_mfma_f32_16x16x32_bf16 v[32:35],a[228:231],v[60:63],v[32:35]
s_cselect_b32 s53,s57,s53
v_add_f32 v116,v116,v117
v_add_f32 v118,v118,v119
v_mfma_f32_16x16x32_bf16 v[28:31],a[232:235],v[64:67],v[28:31]
buffer_load_dwordx4 v0,s[36:39],s67 offen offset:3072 sc1 lds
v_mfma_f32_16x16x32_bf16 v[32:35],a[236:239],v[64:67],v[32:35])"
R"(
s_cmp_eq_u32 s53,s58
ds_read_b128 v[60:63],v2 offset:36864
v_add_f32 v116,v116,v118
v_mfma_f32_16x16x32_bf16 v[28:31],a[240:243],v[68:71],v[28:31]
s_cselect_b32 s53,s59,s53
ds_read_b128 v[64:67],v2 offset:37888
v_mul_f32 v92,s75,v116
v_mfma_f32_16x16x32_bf16 v[32:35],a[244:247],v[68:71],v[32:35]
s_cmp_ge_u32 s66,s59
v_add_f32 v120,v120,v121
v_add_f32 v122,v122,v123
v_add_f32 v120,v120,v122
v_mul_f32 v120,s75,v120
v_fma_f32 v120,-v92,v92,v120
v_max_f32 v120,0,v120
v_add_f32 v115,v15,v120
v_fma_f32 v120,v15,v115,v120
v_rsq_f32 v116,v120
v_mfma_f32_16x16x32_bf16 v[28:31],a[248:251],v[72:75],v[28:31]
s_waitcnt lgkmcnt(2)
ds_read_b128 v[68:71],v2 offset:38912
buffer_store_dwordx4 v[84:87],v3,s[36:39],s73 offen offset:0 sc0 sc1
v_mul_f32 v93,s76,v116
v_mul_f32 v94,v116,v116
v_mul_f32 v95,v120,v116
v_mul_f32 v94,s77,v94
v_mul_f32 v94,v94,v93
v_mfma_f32_16x16x32_bf16 v[32:35],a[252:255],v[72:75],v[32:35]
s_cbranch_scc1 Lg1_slow1_%=
Lg1_a1w0_%=:
v_mfma_f32_16x16x32_bf16 v[36:39],a[0:3],v[44:47],0
s_add_u32 m0,s62,16384
ds_read_b128 v[72:75],v2 offset:39936
s_lshr_b32 s73,s48,0
v_mfma_f32_16x16x32_bf16 v[40:43],a[4:7],v[44:47],0
buffer_load_dwordx4 v1,s[36:39],s67 offen offset:0 sc1 lds
v_mfma_f32_16x16x32_bf16 v[104:107],v[100:103],v[44:47],0
s_lshr_b32 s72,s67,6
s_lshr_b32 s71,s62,2
s_cmp_lt_u32 s67,4194304
v_mfma_f32_16x16x32_bf16 v[108:111],v[44:47],v[44:47],0
s_cselect_b32 s72,s59,s72
v_fma_f32 v28,-v92,v20,v28
v_fma_f32 v29,-v92,v21,v29
v_mfma_f32_16x16x32_bf16 v[36:39],a[8:11],v[48:51],v[36:39]
s_mov_b64 s[44:45],s[46:47]
ds_read_b128 v[44:47],v2 offset:40960
v_fma_f32 v30,-v92,v22,v30
v_mfma_f32_16x16x32_bf16 v[40:43],a[12:15],v[48:51],v[40:43]
buffer_load_dwordx4 v1,s[36:39],s67 offen offset:1024 sc1 lds
v_mfma_f32_16x16x32_bf16 v[104:107],v[100:103],v[48:51],v[104:107]
s_mov_b64 s[46:47],s[48:49]
v_fma_f32 v31,-v92,v23,v31
v_mul_f32 v76,v28,v28
v_mfma_f32_16x16x32_bf16 v[108:111],v[48:51],v[48:51],v[108:111]
s_mov_b64 s[48:49],s[50:51]
v_mul_f32 v77,v29,v29
v_mul_f32 v78,v30,v30
v_mfma_f32_16x16x32_bf16 v[36:39],a[16:19],v[52:55],v[36:39]
ds_read_b128 v[48:51],v2 offset:41984
v_mul_f32 v79,v31,v31
v_mfma_f32_16x16x32_bf16 v[40:43],a[20:23],v[52:55],v[40:43]
buffer_load_dwordx4 v1,s[36:39],s67 offen offset:2048 sc1 lds
v_mfma_f32_16x16x32_bf16 v[104:107],v[100:103],v[52:55],v[104:107]
v_fma_f32 v76,v94,v76,v93
v_mul_f32 v76,v28,v76
v_mfma_f32_16x16x32_bf16 v[108:111],v[52:55],v[52:55],v[108:111]
v_exp_f32 v76,v76
v_mfma_f32_16x16x32_bf16 v[36:39],a[24:27],v[56:59],v[36:39]
ds_read_b128 v[52:55],v2 offset:43008
v_fma_f32 v76,v76,v95,v95
v_mfma_f32_16x16x32_bf16 v[40:43],a[28:31],v[56:59],v[40:43]
buffer_load_dwordx4 v1,s[36:39],s67 offen offset:3072 sc1 lds
v_mfma_f32_16x16x32_bf16 v[104:107],v[100:103],v[56:59],v[104:107]
s_add_u32 m0,s71,s88
v_rcp_f32 v76,v76
v_mfma_f32_16x16x32_bf16 v[108:111],v[56:59],v[56:59],v[108:111]
s_waitcnt lgkmcnt(3)
buffer_load_dword v10,s[36:39],s72 offen offset:0 sc1 lds
v_mfma_f32_16x16x32_bf16 v[36:39],a[32:35],v[60:63],v[36:39]
s_xor_b32 s87,s63,65536
ds_read_b128 v[56:59],v2 offset:44032
v_fma_f32 v77,v94,v77,v93
v_mfma_f32_16x16x32_bf16 v[40:43],a[36:39],v[60:63],v[40:43]
s_cmp_ge_u32 s67,s59
v_mul_f32 v77,v29,v77
v_fma_f32 v78,v94,v78,v93
v_mfma_f32_16x16x32_bf16 v[36:39],a[40:43],v[64:67],v[36:39]
s_cselect_b32 s86,s50,s51
ds_read_b128 v[60:63],v2 offset:45056
v_mul_f32 v78,v30,v78
v_mfma_f32_16x16x32_bf16 v[40:43],a[44:47],v[64:67],v[40:43]
s_cselect_b32 s87,s87,s64
v_exp_f32 v77,v77
v_mfma_f32_16x16x32_bf16 v[36:39],a[48:51],v[68:71],v[36:39]
s_lshr_b32 s69,s51,5
ds_read_b128 v[64:67],v2 offset:46080
v_fma_f32 v77,v77,v95,v95
v_mfma_f32_16x16x32_bf16 v[40:43],a[52:55],v[68:71],v[40:43]
s_cmp_lt_u32 s51,4194304
v_exp_f32 v78,v78
v_mfma_f32_16x16x32_bf16 v[36:39],a[56:59],v[72:75],v[36:39]
s_cselect_b32 s68,0xffffcfff,-1
ds_read_b128 v[68:71],v2 offset:47104
v_fma_f32 v78,v78,v95,v95
v_mfma_f32_16x16x32_bf16 v[40:43],a[60:63],v[72:75],v[40:43]
s_waitcnt lgkmcnt(3)
v_rcp_f32 v77,v77
v_mfma_f32_16x16x32_bf16 v[36:39],a[64:67],v[44:47],v[36:39]
s_waitcnt vmcnt(11)
ds_read_b128 v[72:75],v2 offset:48128
v_fma_f32 v79,v94,v79,v93
v_mfma_f32_16x16x32_bf16 v[40:43],a[68:71],v[44:47],v[40:43]
s_barrier
v_mfma_f32_16x16x32_bf16 v[36:39],a[72:75],v[48:51],v[36:39]
s_and_b32 s68,s68,s69
ds_read_b128 v[44:47],v2 offset:49152
v_mul_f32 v79,v31,v79
v_mfma_f32_16x16x32_bf16 v[40:43],a[76:79],v[48:51],v[40:43]
s_cmp_lt_u32 s51,s89
v_exp_f32 v79,v79
v_mfma_f32_16x16x32_bf16 v[36:39],a[80:83],v[52:55],v[36:39]
s_cselect_b32 s68,s59,s68
ds_read_b128 v[48:51],v2 offset:50176
v_fma_f32 v79,v79,v95,v95
v_mfma_f32_16x16x32_bf16 v[40:43],a[84:87],v[52:55],v[40:43]
s_lshr_b32 s69,s52,5
v_rcp_f32 v78,v78
v_mfma_f32_16x16x32_bf16 v[36:39],a[88:91],v[56:59],v[36:39]
s_cmp_lt_u32 s52,4194304
ds_read_b128 v[52:55],v2 offset:51200
v_mul_f32 v28,v28,v76
v_mfma_f32_16x16x32_bf16 v[40:43],a[92:95],v[56:59],v[40:43]
s_waitcnt lgkmcnt(3)
v_rcp_f32 v79,v79
v_mfma_f32_16x16x32_bf16 v[36:39],a[96:99],v[60:63],v[36:39]
s_cselect_b32 s70,0xffffcfff,-1
ds_read_b128 v[56:59],v2 offset:52224
v_mul_f32 v29,v29,v77
v_mfma_f32_16x16x32_bf16 v[40:43],a[100:103],v[60:63],v[40:43]
s_and_b32 s70,s70,s69
v_mul_f32 v30,v30,v78
v_mul_f32 v31,v31,v79
v_mfma_f32_16x16x32_bf16 v[36:39],a[104:107],v[64:67],v[36:39]
s_cmp_lt_u32 s52,s89
ds_read_b128 v[60:63],v2 offset:53248
v_cvt_pk_bf16_f32 v84,v28,v29
v_mfma_f32_16x16x32_bf16 v[40:43],a[108:111],v[64:67],v[40:43]
s_cselect_b32 s70,s59,s70
v_cvt_pk_bf16_f32 v85,v30,v31
v_fma_f32 v32,-v92,v24,v32
v_mfma_f32_16x16x32_bf16 v[36:39],a[112:115],v[68:71],v[36:39]
s_mov_b32 s62,s87
ds_read_b128 v[64:67],v2 offset:54272
v_fma_f32 v33,-v92,v25,v33
v_mfma_f32_16x16x32_bf16 v[40:43],a[116:119],v[68:71],v[40:43]
s_cmp_lt_u32 s86,s89
v_fma_f32 v34,-v92,v26,v34
v_fma_f32 v35,-v92,v27,v35
v_mfma_f32_16x16x32_bf16 v[36:39],a[120:123],v[72:75],v[36:39]
s_mov_b64 s[50:51],s[52:53]
ds_read_b128 v[68:71],v2 offset:55296
v_mul_f32 v80,v32,v32
v_mfma_f32_16x16x32_bf16 v[40:43],a[124:127],v[72:75],v[40:43]
s_waitcnt lgkmcnt(3)
v_mul_f32 v81,v33,v33
v_mul_f32 v82,v34,v34
v_mfma_f32_16x16x32_bf16 v[36:39],a[128:131],v[44:47],v[36:39]
ds_read_b128 v[72:75],v2 offset:56320
v_mul_f32 v83,v35,v35
v_mfma_f32_16x16x32_bf16 v[40:43],a[132:135],v[44:47],v[40:43]
v_fma_f32 v80,v94,v80,v93
v_mul_f32 v80,v32,v80
v_mfma_f32_16x16x32_bf16 v[104:107],v[100:103],v[44:47],v[104:107]
v_exp_f32 v80,v80
v_mfma_f32_16x16x32_bf16 v[108:111],v[44:47],v[44:47],v[108:111]
v_fma_f32 v80,v80,v95,v95
v_fma_f32 v81,v94,v81,v93
v_mfma_f32_16x16x32_bf16 v[36:39],a[136:139],v[48:51],v[36:39]
ds_read_b128 v[44:47],v2 offset:57344
v_mul_f32 v81,v33,v81
v_mfma_f32_16x16x32_bf16 v[40:43],a[140:143],v[48:51],v[40:43]
v_exp_f32 v81,v81
v_mfma_f32_16x16x32_bf16 v[104:107],v[100:103],v[48:51],v[104:107]
s_waitcnt vmcnt(10)
v_rcp_f32 v80,v80
v_mfma_f32_16x16x32_bf16 v[108:111],v[48:51],v[48:51],v[108:111]
s_barrier
v_mfma_f32_16x16x32_bf16 v[36:39],a[144:147],v[52:55],v[36:39]
ds_read_b128 v[48:51],v2 offset:58368
ds_read_b32 v16,v7 offset:256
v_mfma_f32_16x16x32_bf16 v[40:43],a[148:151],v[52:55],v[40:43]
v_fma_f32 v81,v81,v95,v95
v_fma_f32 v82,v94,v82,v93
v_mfma_f32_16x16x32_bf16 v[104:107],v[100:103],v[52:55],v[104:107]
v_rcp_f32 v81,v81
v_mfma_f32_16x16x32_bf16 v[108:111],v[52:55],v[52:55],v[108:111]
v_mul_f32 v82,v34,v82
v_fma_f32 v83,v94,v83,v93
v_mfma_f32_16x16x32_bf16 v[36:39],a[152:155],v[56:59],v[36:39])"
R"(
s_waitcnt lgkmcnt(0)
ds_read_b128 v[52:55],v2 offset:59392
v_mul_f32 v83,v35,v83
v_mfma_f32_16x16x32_bf16 v[40:43],a[156:159],v[56:59],v[40:43]
v_cmp_eq_u32 vcc,s60,v16
v_exp_f32 v82,v82
v_mul_f32 v32,v32,v80
v_mfma_f32_16x16x32_bf16 v[104:107],v[100:103],v[56:59],v[104:107]
s_cselect_b32 vcc_lo,-1,vcc_lo
v_exp_f32 v83,v83
v_mfma_f32_16x16x32_bf16 v[108:111],v[56:59],v[56:59],v[108:111]
v_fma_f32 v82,v82,v95,v95
v_fma_f32 v83,v83,v95,v95
v_mfma_f32_16x16x32_bf16 v[36:39],a[160:163],v[60:63],v[36:39]
s_cmp_eq_u32 vcc_lo,-1
ds_read_b128 v[56:59],v2 offset:60416
v_mul_f32 v33,v33,v81
v_mfma_f32_16x16x32_bf16 v[40:43],a[164:167],v[60:63],v[40:43]
s_cselect_b32 s66,s86,s59
s_cmp_ge_u32 s66,s59
s_cselect_b32 s68,s68,s70
s_add_u32 m0,s78,0
v_mfma_f32_16x16x32_bf16 v[36:39],a[168:171],v[64:67],v[36:39]
buffer_load_dword v6,s[36:39],s68 offen offset:0 sc1 lds
v_cndmask_b32 v112,v108,v109,s[82:83]
v_cndmask_b32 v113,v110,v111,s[82:83]
v_cndmask_b32 v112,v112,v113,s[84:85]
v_cndmask_b32 v112,v104,v112,s[98:99]
v_mfma_f32_16x16x32_bf16 v[40:43],a[172:175],v[64:67],v[40:43]
s_mov_b32 m0,s62
ds_read_b128 v[60:63],v2 offset:61440
ds_write_b32 v13,v112 offset:2048
v_mfma_f32_16x16x32_bf16 v[36:39],a[176:179],v[68:71],v[36:39]
buffer_load_dwordx4 v0,s[36:39],s66 offen offset:0 sc1 lds
v_mfma_f32_16x16x32_bf16 v[40:43],a[180:183],v[68:71],v[40:43]
s_xor_b32 s64,s64,65536
ds_read_b128 v[64:67],v2 offset:62464
v_cvt_pk_bf16_f32 v86,v32,v33
v_mfma_f32_16x16x32_bf16 v[36:39],a[184:187],v[72:75],v[36:39]
s_sub_u32 s69,s66,1048576
ds_read_b128 v[68:71],v2 offset:63488
s_cmp_lt_u32 s69,3145728
v_mfma_f32_16x16x32_bf16 v[40:43],a[188:191],v[72:75],v[40:43]
s_waitcnt lgkmcnt(4)
v_rcp_f32 v82,v82
v_mfma_f32_16x16x32_bf16 v[36:39],a[192:195],v[44:47],v[36:39]
s_waitcnt vmcnt(7) lgkmcnt(2)
ds_read_b128 v[72:75],v2 offset:64512
v_xor_b32 v2,65536,v2
v_mfma_f32_16x16x32_bf16 v[40:43],a[196:199],v[44:47],v[40:43]
s_barrier
v_mfma_f32_16x16x32_bf16 v[36:39],a[200:203],v[48:51],v[36:39]
buffer_load_dwordx4 v0,s[36:39],s66 offen offset:1024 sc1 lds
v_mfma_f32_16x16x32_bf16 v[40:43],a[204:207],v[48:51],v[40:43]
s_cselect_b32 s89,4194304,s89
ds_read_b128 v[44:47],v2 offset:0
ds_read_b128 v[116:119],v14 offset:2048
v_mfma_f32_16x16x32_bf16 v[36:39],a[208:211],v[52:55],v[36:39]
s_add_u32 s74,s53,s55
ds_read_b128 v[120:123],v14 offset:2064
ds_read_b128 v[48:51],v2 offset:1024
v_mfma_f32_16x16x32_bf16 v[40:43],a[212:215],v[52:55],v[40:43]
s_cmp_eq_u32 s74,s56
v_rcp_f32 v83,v83
v_mfma_f32_16x16x32_bf16 v[36:39],a[216:219],v[56:59],v[36:39]
buffer_load_dwordx4 v0,s[36:39],s66 offen offset:2048 sc1 lds
v_mfma_f32_16x16x32_bf16 v[40:43],a[220:223],v[56:59],v[40:43]
s_waitcnt lgkmcnt(4)
ds_read_b128 v[52:55],v2 offset:2048
v_mul_f32 v34,v34,v82
v_mfma_f32_16x16x32_bf16 v[36:39],a[224:227],v[60:63],v[36:39]
s_waitcnt lgkmcnt(2)
ds_read_b128 v[56:59],v2 offset:3072
v_mul_f32 v35,v35,v83
v_mfma_f32_16x16x32_bf16 v[40:43],a[228:231],v[60:63],v[40:43]
s_cselect_b32 s74,s57,s74
v_add_f32 v116,v116,v117
v_add_f32 v118,v118,v119
v_mfma_f32_16x16x32_bf16 v[36:39],a[232:235],v[64:67],v[36:39]
buffer_load_dwordx4 v0,s[36:39],s66 offen offset:3072 sc1 lds
v_mfma_f32_16x16x32_bf16 v[40:43],a[236:239],v[64:67],v[40:43]
s_cmp_eq_u32 s74,s58
ds_read_b128 v[60:63],v2 offset:4096
v_add_f32 v116,v116,v118
v_mfma_f32_16x16x32_bf16 v[36:39],a[240:243],v[68:71],v[36:39]
s_cselect_b32 s74,s59,s74
ds_read_b128 v[64:67],v2 offset:5120
v_mul_f32 v96,s75,v116
v_mfma_f32_16x16x32_bf16 v[40:43],a[244:247],v[68:71],v[40:43]
s_mov_b32 s52,s74
v_cvt_pk_bf16_f32 v87,v34,v35
v_add_f32 v120,v120,v121
v_add_f32 v122,v122,v123
v_add_f32 v120,v120,v122
v_mul_f32 v120,s75,v120
v_fma_f32 v120,-v96,v96,v120
v_max_f32 v120,0,v120
v_add_f32 v115,v15,v120
v_fma_f32 v120,v15,v115,v120
v_rsq_f32 v116,v120
v_mfma_f32_16x16x32_bf16 v[36:39],a[248:251],v[72:75],v[36:39]
s_waitcnt lgkmcnt(2)
ds_read_b128 v[68:71],v2 offset:6144
buffer_store_dwordx4 v[84:87],v3,s[36:39],s73 offen offset:0 sc0 sc1
v_xor_b32 v8,16384,v8
v_mul_f32 v97,s76,v116
v_mul_f32 v98,v116,v116
v_mul_f32 v99,v120,v116
v_mul_f32 v98,s77,v98
v_mul_f32 v98,v98,v97
s_cmp_lt_u32 s67,4194304
v_mfma_f32_16x16x32_bf16 v[40:43],a[252:255],v[72:75],v[40:43]
s_cbranch_scc1 Lg1_a0w0_%=
s_cmp_ge_u32 s48,4194304
s_cselect_b64 s[90:91],s[94:95],s[90:91]
s_cselect_b64 s[92:93],s[96:97],s[92:93]
s_cmp_lt_u32 s67,s59
s_cbranch_scc1 Lg1_d0w0_%=
s_branch Lg1_slow0_%=
Lg1_a0w1_%=:
v_mfma_f32_16x16x32_bf16 v[28:31],a[32:35],v[44:47],0
s_add_u32 m0,s62,16384
ds_read_b128 v[72:75],v2 offset:3072
s_lshr_b32 s73,s47,0
v_mfma_f32_16x16x32_bf16 v[32:35],a[36:39],v[44:47],0
s_lshr_b32 s72,s66,6
s_lshr_b32 s71,s62,2
s_cmp_lt_u32 s66,4194304
v_mfma_f32_16x16x32_bf16 v[104:107],v[100:103],v[44:47],0
buffer_load_dwordx4 v1,s[36:39],s66 offen offset:0 sc1 lds
v_mfma_f32_16x16x32_bf16 v[108:111],v[44:47],v[44:47],0
s_cselect_b32 s72,s59,s72
v_fma_f32 v36,-v96,v20,v36
v_fma_f32 v37,-v96,v21,v37
v_mfma_f32_16x16x32_bf16 v[28:31],a[40:43],v[48:51],v[28:31]
ds_read_b128 v[44:47],v2 offset:12288
v_fma_f32 v38,-v96,v22,v38
v_mfma_f32_16x16x32_bf16 v[32:35],a[44:47],v[48:51],v[32:35]
v_fma_f32 v39,-v96,v23,v39
v_mul_f32 v76,v36,v36
v_mfma_f32_16x16x32_bf16 v[104:107],v[100:103],v[48:51],v[104:107]
buffer_load_dwordx4 v1,s[36:39],s66 offen offset:1024 sc1 lds
v_mfma_f32_16x16x32_bf16 v[108:111],v[48:51],v[48:51],v[108:111]
v_mul_f32 v77,v37,v37
v_mul_f32 v78,v38,v38
v_mfma_f32_16x16x32_bf16 v[28:31],a[48:51],v[52:55],v[28:31]
ds_read_b128 v[48:51],v2 offset:13312
v_mul_f32 v79,v39,v39
v_mfma_f32_16x16x32_bf16 v[32:35],a[52:55],v[52:55],v[32:35]
v_fma_f32 v76,v98,v76,v97
v_mul_f32 v76,v36,v76
v_mfma_f32_16x16x32_bf16 v[104:107],v[100:103],v[52:55],v[104:107]
buffer_load_dwordx4 v1,s[36:39],s66 offen offset:2048 sc1 lds
v_mfma_f32_16x16x32_bf16 v[108:111],v[52:55],v[52:55],v[108:111]
v_exp_f32 v76,v76
v_mfma_f32_16x16x32_bf16 v[28:31],a[56:59],v[56:59],v[28:31]
ds_read_b128 v[52:55],v2 offset:14336
v_fma_f32 v76,v76,v99,v99
v_mfma_f32_16x16x32_bf16 v[32:35],a[60:63],v[56:59],v[32:35]
v_rcp_f32 v76,v76
v_mfma_f32_16x16x32_bf16 v[104:107],v[100:103],v[56:59],v[104:107]
buffer_load_dwordx4 v1,s[36:39],s66 offen offset:3072 sc1 lds
v_mfma_f32_16x16x32_bf16 v[108:111],v[56:59],v[56:59],v[108:111]
s_waitcnt lgkmcnt(3)
v_fma_f32 v77,v98,v77,v97
v_mul_f32 v77,v37,v77
v_mfma_f32_16x16x32_bf16 v[28:31],a[0:3],v[60:63],v[28:31]
s_add_u32 m0,s71,s88
ds_read_b128 v[56:59],v2 offset:15360
v_fma_f32 v78,v98,v78,v97
v_mfma_f32_16x16x32_bf16 v[32:35],a[4:7],v[60:63],v[32:35]
buffer_load_dword v10,s[36:39],s72 offen offset:0 sc1 lds
v_mfma_f32_16x16x32_bf16 v[28:31],a[8:11],v[64:67],v[28:31]
s_xor_b32 s87,s64,65536
ds_read_b128 v[60:63],v2 offset:8192
v_mul_f32 v78,v38,v78
v_mfma_f32_16x16x32_bf16 v[32:35],a[12:15],v[64:67],v[32:35]
s_cmp_ge_u32 s66,s59
v_exp_f32 v77,v77
v_mfma_f32_16x16x32_bf16 v[28:31],a[16:19],v[68:71],v[28:31]
s_cselect_b32 s86,s49,s50
ds_read_b128 v[64:67],v2 offset:9216
v_fma_f32 v77,v77,v99,v99
v_mfma_f32_16x16x32_bf16 v[32:35],a[20:23],v[68:71],v[32:35]
s_cselect_b32 s87,s87,s63
v_exp_f32 v78,v78
v_mfma_f32_16x16x32_bf16 v[28:31],a[24:27],v[72:75],v[28:31]
s_mov_b32 s62,s87
ds_read_b128 v[68:71],v2 offset:10240
v_fma_f32 v78,v78,v99,v99
v_mfma_f32_16x16x32_bf16 v[32:35],a[28:31],v[72:75],v[32:35]
s_waitcnt lgkmcnt(3)
v_rcp_f32 v77,v77
v_mfma_f32_16x16x32_bf16 v[28:31],a[96:99],v[44:47],v[28:31]
s_waitcnt vmcnt(10)
ds_read_b128 v[72:75],v2 offset:11264
v_fma_f32 v79,v98,v79,v97
v_mfma_f32_16x16x32_bf16 v[32:35],a[100:103],v[44:47],v[32:35]
s_barrier
v_mfma_f32_16x16x32_bf16 v[28:31],a[104:107],v[48:51],v[28:31])"
R"(
s_cmp_lt_u32 s86,s89
ds_read_b128 v[44:47],v2 offset:20480
v_mul_f32 v79,v39,v79
v_mfma_f32_16x16x32_bf16 v[32:35],a[108:111],v[48:51],v[32:35]
s_mov_b32 m0,s62
v_exp_f32 v79,v79
v_mfma_f32_16x16x32_bf16 v[28:31],a[112:115],v[52:55],v[28:31]
ds_read_b128 v[48:51],v2 offset:21504
v_fma_f32 v79,v79,v99,v99
v_mfma_f32_16x16x32_bf16 v[32:35],a[116:119],v[52:55],v[32:35]
v_rcp_f32 v78,v78
v_mfma_f32_16x16x32_bf16 v[28:31],a[120:123],v[56:59],v[28:31]
ds_read_b128 v[52:55],v2 offset:22528
v_mul_f32 v36,v36,v76
v_mfma_f32_16x16x32_bf16 v[32:35],a[124:127],v[56:59],v[32:35]
s_waitcnt lgkmcnt(3)
v_rcp_f32 v79,v79
v_mfma_f32_16x16x32_bf16 v[28:31],a[64:67],v[60:63],v[28:31]
ds_read_b128 v[56:59],v2 offset:23552
v_mul_f32 v37,v37,v77
v_mfma_f32_16x16x32_bf16 v[32:35],a[68:71],v[60:63],v[32:35]
v_mul_f32 v38,v38,v78
v_mul_f32 v39,v39,v79
v_mfma_f32_16x16x32_bf16 v[28:31],a[72:75],v[64:67],v[28:31]
ds_read_b128 v[60:63],v2 offset:16384
v_cvt_pk_bf16_f32 v84,v36,v37
v_mfma_f32_16x16x32_bf16 v[32:35],a[76:79],v[64:67],v[32:35]
v_cvt_pk_bf16_f32 v85,v38,v39
v_fma_f32 v40,-v96,v24,v40
v_mfma_f32_16x16x32_bf16 v[28:31],a[80:83],v[68:71],v[28:31]
ds_read_b128 v[64:67],v2 offset:17408
v_fma_f32 v41,-v96,v25,v41
v_mfma_f32_16x16x32_bf16 v[32:35],a[84:87],v[68:71],v[32:35]
v_fma_f32 v42,-v96,v26,v42
v_fma_f32 v43,-v96,v27,v43
v_mfma_f32_16x16x32_bf16 v[28:31],a[88:91],v[72:75],v[28:31]
ds_read_b128 v[68:71],v2 offset:18432
v_mul_f32 v80,v40,v40
v_mfma_f32_16x16x32_bf16 v[32:35],a[92:95],v[72:75],v[32:35]
s_waitcnt lgkmcnt(3)
v_mul_f32 v81,v41,v41
v_mul_f32 v82,v42,v42
v_mfma_f32_16x16x32_bf16 v[28:31],a[160:163],v[44:47],v[28:31]
ds_read_b128 v[72:75],v2 offset:19456
v_mul_f32 v83,v43,v43
v_mfma_f32_16x16x32_bf16 v[32:35],a[164:167],v[44:47],v[32:35]
v_fma_f32 v80,v98,v80,v97
v_mul_f32 v80,v40,v80
v_mfma_f32_16x16x32_bf16 v[104:107],v[100:103],v[44:47],v[104:107]
v_exp_f32 v80,v80
v_mfma_f32_16x16x32_bf16 v[108:111],v[44:47],v[44:47],v[108:111]
v_fma_f32 v80,v80,v99,v99
v_fma_f32 v81,v98,v81,v97
v_mfma_f32_16x16x32_bf16 v[28:31],a[168:171],v[48:51],v[28:31]
ds_read_b128 v[44:47],v2 offset:28672
v_mul_f32 v81,v41,v81
v_mfma_f32_16x16x32_bf16 v[32:35],a[172:175],v[48:51],v[32:35]
v_exp_f32 v81,v81
v_mfma_f32_16x16x32_bf16 v[104:107],v[100:103],v[48:51],v[104:107]
s_nop 0
v_rcp_f32 v80,v80
v_mfma_f32_16x16x32_bf16 v[108:111],v[48:51],v[48:51],v[108:111]
s_barrier
v_mfma_f32_16x16x32_bf16 v[28:31],a[176:179],v[52:55],v[28:31]
ds_read_b128 v[48:51],v2 offset:29696
ds_read_b32 v16,v7 offset:0
v_mfma_f32_16x16x32_bf16 v[32:35],a[180:183],v[52:55],v[32:35]
v_fma_f32 v81,v81,v99,v99
v_fma_f32 v82,v98,v82,v97
v_mfma_f32_16x16x32_bf16 v[104:107],v[100:103],v[52:55],v[104:107]
v_rcp_f32 v81,v81
v_mfma_f32_16x16x32_bf16 v[108:111],v[52:55],v[52:55],v[108:111]
v_mul_f32 v82,v42,v82
v_fma_f32 v83,v98,v83,v97
v_mfma_f32_16x16x32_bf16 v[28:31],a[184:187],v[56:59],v[28:31]
s_waitcnt lgkmcnt(0)
ds_read_b128 v[52:55],v2 offset:30720
v_mul_f32 v83,v43,v83
v_mfma_f32_16x16x32_bf16 v[32:35],a[188:191],v[56:59],v[32:35]
v_cmp_eq_u32 vcc,s60,v16
v_exp_f32 v82,v82
v_mul_f32 v40,v40,v80
v_mfma_f32_16x16x32_bf16 v[104:107],v[100:103],v[56:59],v[104:107]
s_cselect_b32 vcc_lo,-1,vcc_lo
v_exp_f32 v83,v83
v_mfma_f32_16x16x32_bf16 v[108:111],v[56:59],v[56:59],v[108:111]
v_fma_f32 v82,v82,v99,v99
v_fma_f32 v83,v83,v99,v99
v_mfma_f32_16x16x32_bf16 v[28:31],a[128:131],v[60:63],v[28:31]
s_cmp_eq_u32 vcc_lo,-1
ds_read_b128 v[56:59],v2 offset:31744
s_cselect_b32 s67,s86,s59
v_mfma_f32_16x16x32_bf16 v[32:35],a[132:135],v[60:63],v[32:35]
buffer_load_dwordx4 v0,s[36:39],s67 offen offset:0 sc1 lds
v_mfma_f32_16x16x32_bf16 v[28:31],a[136:139],v[64:67],v[28:31]
s_xor_b32 s63,s63,65536
v_cndmask_b32 v112,v108,v109,s[82:83]
v_cndmask_b32 v113,v110,v111,s[82:83]
v_cndmask_b32 v112,v112,v113,s[84:85]
v_cndmask_b32 v112,v104,v112,s[98:99]
v_mfma_f32_16x16x32_bf16 v[32:35],a[140:143],v[64:67],v[32:35]
s_sub_u32 s69,s67,1048576
ds_read_b128 v[60:63],v2 offset:24576
ds_write_b32 v13,v112 offset:0
v_mfma_f32_16x16x32_bf16 v[28:31],a[144:147],v[68:71],v[28:31]
s_cmp_lt_u32 s69,3145728
ds_read_b128 v[64:67],v2 offset:25600
v_mul_f32 v41,v41,v81
v_mfma_f32_16x16x32_bf16 v[32:35],a[148:151],v[68:71],v[32:35]
buffer_load_dwordx4 v0,s[36:39],s67 offen offset:1024 sc1 lds
v_mfma_f32_16x16x32_bf16 v[28:31],a[152:155],v[72:75],v[28:31]
s_cselect_b32 s89,4194304,s89
ds_read_b128 v[68:71],v2 offset:26624
v_cvt_pk_bf16_f32 v86,v40,v41
v_mfma_f32_16x16x32_bf16 v[32:35],a[156:159],v[72:75],v[32:35]
s_waitcnt lgkmcnt(4)
v_rcp_f32 v82,v82
v_mfma_f32_16x16x32_bf16 v[28:31],a[224:227],v[44:47],v[28:31]
s_waitcnt vmcnt(7) lgkmcnt(2)
ds_read_b128 v[72:75],v2 offset:27648
v_mul_f32 v42,v42,v82
v_mfma_f32_16x16x32_bf16 v[32:35],a[228:231],v[44:47],v[32:35]
s_barrier
v_mfma_f32_16x16x32_bf16 v[28:31],a[232:235],v[48:51],v[28:31]
s_add_u32 s53,s52,s55
ds_read_b128 v[116:119],v14 offset:0
ds_read_b128 v[120:123],v14 offset:16
v_mfma_f32_16x16x32_bf16 v[32:35],a[236:239],v[48:51],v[32:35]
s_cmp_eq_u32 s53,s56
ds_read_b128 v[44:47],v2 offset:36864
buffer_load_dwordx4 v0,s[36:39],s67 offen offset:2048 sc1 lds
v_mfma_f32_16x16x32_bf16 v[28:31],a[240:243],v[52:55],v[28:31]
s_cselect_b32 s53,s57,s53
ds_read_b128 v[48:51],v2 offset:37888
s_cmp_eq_u32 s53,s58
v_mfma_f32_16x16x32_bf16 v[32:35],a[244:247],v[52:55],v[32:35]
s_cselect_b32 s53,s59,s53
v_rcp_f32 v83,v83
v_mfma_f32_16x16x32_bf16 v[28:31],a[248:251],v[56:59],v[28:31]
s_waitcnt lgkmcnt(2)
ds_read_b128 v[52:55],v2 offset:38912
v_mul_f32 v43,v43,v83
v_mfma_f32_16x16x32_bf16 v[32:35],a[252:255],v[56:59],v[32:35]
buffer_load_dwordx4 v0,s[36:39],s67 offen offset:3072 sc1 lds
v_mfma_f32_16x16x32_bf16 v[28:31],a[192:195],v[60:63],v[28:31]
s_cmp_ge_u32 s66,s59
ds_read_b128 v[56:59],v2 offset:39936
v_add_f32 v116,v116,v117
v_mfma_f32_16x16x32_bf16 v[32:35],a[196:199],v[60:63],v[32:35]
v_add_f32 v118,v118,v119
v_add_f32 v116,v116,v118
v_mfma_f32_16x16x32_bf16 v[28:31],a[200:203],v[64:67],v[28:31]
ds_read_b128 v[60:63],v2 offset:32768
v_cvt_pk_bf16_f32 v87,v42,v43
v_mfma_f32_16x16x32_bf16 v[32:35],a[204:207],v[64:67],v[32:35]
buffer_store_dwordx4 v[84:87],v3,s[36:39],s73 offen offset:0 sc0 sc1
v_mul_f32 v92,s75,v116
v_add_f32 v120,v120,v121
v_add_f32 v122,v122,v123
v_mfma_f32_16x16x32_bf16 v[28:31],a[208:211],v[68:71],v[28:31]
ds_read_b128 v[64:67],v2 offset:33792
v_add_f32 v120,v120,v122
v_mfma_f32_16x16x32_bf16 v[32:35],a[212:215],v[68:71],v[32:35]
v_mul_f32 v120,s75,v120
v_fma_f32 v120,-v92,v92,v120
v_max_f32 v120,0,v120
v_add_f32 v115,v15,v120
v_fma_f32 v120,v15,v115,v120
v_rsq_f32 v116,v120
v_mfma_f32_16x16x32_bf16 v[28:31],a[216:219],v[72:75],v[28:31]
s_waitcnt lgkmcnt(2)
ds_read_b128 v[68:71],v2 offset:34816
v_mul_f32 v93,s76,v116
v_mul_f32 v94,v116,v116
v_mul_f32 v95,v120,v116
v_mul_f32 v94,s77,v94
v_mul_f32 v94,v94,v93
v_mfma_f32_16x16x32_bf16 v[32:35],a[220:223],v[72:75],v[32:35]
s_cbranch_scc1 Lg1_slow1_%=
Lg1_a1w1_%=:
v_mfma_f32_16x16x32_bf16 v[36:39],a[32:35],v[44:47],0
s_add_u32 m0,s62,16384
ds_read_b128 v[72:75],v2 offset:35840
s_lshr_b32 s73,s48,0
v_mfma_f32_16x16x32_bf16 v[40:43],a[36:39],v[44:47],0
s_lshr_b32 s72,s67,6
s_lshr_b32 s71,s62,2
s_cmp_lt_u32 s67,4194304
v_mfma_f32_16x16x32_bf16 v[104:107],v[100:103],v[44:47],0
buffer_load_dwordx4 v1,s[36:39],s67 offen offset:0 sc1 lds
v_mfma_f32_16x16x32_bf16 v[108:111],v[44:47],v[44:47],0
s_cselect_b32 s72,s59,s72
v_fma_f32 v28,-v92,v20,v28
v_fma_f32 v29,-v92,v21,v29
v_mfma_f32_16x16x32_bf16 v[36:39],a[40:43],v[48:51],v[36:39]
s_mov_b64 s[44:45],s[46:47]
ds_read_b128 v[44:47],v2 offset:45056
v_fma_f32 v30,-v92,v22,v30
v_mfma_f32_16x16x32_bf16 v[40:43],a[44:47],v[48:51],v[40:43]
s_mov_b64 s[46:47],s[48:49])"
R"(
v_fma_f32 v31,-v92,v23,v31
v_mul_f32 v76,v28,v28
v_mfma_f32_16x16x32_bf16 v[104:107],v[100:103],v[48:51],v[104:107]
buffer_load_dwordx4 v1,s[36:39],s67 offen offset:1024 sc1 lds
v_mfma_f32_16x16x32_bf16 v[108:111],v[48:51],v[48:51],v[108:111]
s_mov_b64 s[48:49],s[50:51]
v_mul_f32 v77,v29,v29
v_mul_f32 v78,v30,v30
v_mfma_f32_16x16x32_bf16 v[36:39],a[48:51],v[52:55],v[36:39]
ds_read_b128 v[48:51],v2 offset:46080
v_mul_f32 v79,v31,v31
v_mfma_f32_16x16x32_bf16 v[40:43],a[52:55],v[52:55],v[40:43]
v_fma_f32 v76,v94,v76,v93
v_mul_f32 v76,v28,v76
v_mfma_f32_16x16x32_bf16 v[104:107],v[100:103],v[52:55],v[104:107]
buffer_load_dwordx4 v1,s[36:39],s67 offen offset:2048 sc1 lds
v_mfma_f32_16x16x32_bf16 v[108:111],v[52:55],v[52:55],v[108:111]
v_exp_f32 v76,v76
v_mfma_f32_16x16x32_bf16 v[36:39],a[56:59],v[56:59],v[36:39]
ds_read_b128 v[52:55],v2 offset:47104
v_fma_f32 v76,v76,v95,v95
v_mfma_f32_16x16x32_bf16 v[40:43],a[60:63],v[56:59],v[40:43]
v_rcp_f32 v76,v76
v_mfma_f32_16x16x32_bf16 v[104:107],v[100:103],v[56:59],v[104:107]
buffer_load_dwordx4 v1,s[36:39],s67 offen offset:3072 sc1 lds
v_mfma_f32_16x16x32_bf16 v[108:111],v[56:59],v[56:59],v[108:111]
s_waitcnt lgkmcnt(3)
v_fma_f32 v77,v94,v77,v93
v_mul_f32 v77,v29,v77
v_mfma_f32_16x16x32_bf16 v[36:39],a[0:3],v[60:63],v[36:39]
s_add_u32 m0,s71,s88
ds_read_b128 v[56:59],v2 offset:48128
v_fma_f32 v78,v94,v78,v93
v_mfma_f32_16x16x32_bf16 v[40:43],a[4:7],v[60:63],v[40:43]
buffer_load_dword v10,s[36:39],s72 offen offset:0 sc1 lds
v_mfma_f32_16x16x32_bf16 v[36:39],a[8:11],v[64:67],v[36:39]
s_xor_b32 s87,s63,65536
ds_read_b128 v[60:63],v2 offset:40960
v_mul_f32 v78,v30,v78
v_mfma_f32_16x16x32_bf16 v[40:43],a[12:15],v[64:67],v[40:43]
s_cmp_ge_u32 s67,s59
v_exp_f32 v77,v77
v_mfma_f32_16x16x32_bf16 v[36:39],a[16:19],v[68:71],v[36:39]
s_cselect_b32 s86,s50,s51
ds_read_b128 v[64:67],v2 offset:41984
v_fma_f32 v77,v77,v95,v95
v_mfma_f32_16x16x32_bf16 v[40:43],a[20:23],v[68:71],v[40:43]
s_cselect_b32 s87,s87,s64
v_exp_f32 v78,v78
v_mfma_f32_16x16x32_bf16 v[36:39],a[24:27],v[72:75],v[36:39]
s_mov_b32 s62,s87
ds_read_b128 v[68:71],v2 offset:43008
v_fma_f32 v78,v78,v95,v95
v_mfma_f32_16x16x32_bf16 v[40:43],a[28:31],v[72:75],v[40:43]
s_waitcnt lgkmcnt(3)
v_rcp_f32 v77,v77
v_mfma_f32_16x16x32_bf16 v[36:39],a[96:99],v[44:47],v[36:39]
s_waitcnt vmcnt(10)
ds_read_b128 v[72:75],v2 offset:44032
v_fma_f32 v79,v94,v79,v93
v_mfma_f32_16x16x32_bf16 v[40:43],a[100:103],v[44:47],v[40:43]
s_barrier
v_mfma_f32_16x16x32_bf16 v[36:39],a[104:107],v[48:51],v[36:39]
s_cmp_lt_u32 s86,s89
ds_read_b128 v[44:47],v2 offset:53248
v_mul_f32 v79,v31,v79
v_mfma_f32_16x16x32_bf16 v[40:43],a[108:111],v[48:51],v[40:43]
s_mov_b32 m0,s62
v_exp_f32 v79,v79
v_mfma_f32_16x16x32_bf16 v[36:39],a[112:115],v[52:55],v[36:39]
s_mov_b64 s[50:51],s[52:53]
ds_read_b128 v[48:51],v2 offset:54272
v_fma_f32 v79,v79,v95,v95
v_mfma_f32_16x16x32_bf16 v[40:43],a[116:119],v[52:55],v[40:43]
v_rcp_f32 v78,v78
v_mfma_f32_16x16x32_bf16 v[36:39],a[120:123],v[56:59],v[36:39]
ds_read_b128 v[52:55],v2 offset:55296
v_mul_f32 v28,v28,v76
v_mfma_f32_16x16x32_bf16 v[40:43],a[124:127],v[56:59],v[40:43]
s_waitcnt lgkmcnt(3)
v_rcp_f32 v79,v79
v_mfma_f32_16x16x32_bf16 v[36:39],a[64:67],v[60:63],v[36:39]
ds_read_b128 v[56:59],v2 offset:56320
v_mul_f32 v29,v29,v77
v_mfma_f32_16x16x32_bf16 v[40:43],a[68:71],v[60:63],v[40:43]
v_mul_f32 v30,v30,v78
v_mul_f32 v31,v31,v79
v_mfma_f32_16x16x32_bf16 v[36:39],a[72:75],v[64:67],v[36:39]
ds_read_b128 v[60:63],v2 offset:49152
v_cvt_pk_bf16_f32 v84,v28,v29
v_mfma_f32_16x16x32_bf16 v[40:43],a[76:79],v[64:67],v[40:43]
v_cvt_pk_bf16_f32 v85,v30,v31
v_fma_f32 v32,-v92,v24,v32
v_mfma_f32_16x16x32_bf16 v[36:39],a[80:83],v[68:71],v[36:39]
ds_read_b128 v[64:67],v2 offset:50176
v_fma_f32 v33,-v92,v25,v33
v_mfma_f32_16x16x32_bf16 v[40:43],a[84:87],v[68:71],v[40:43]
v_fma_f32 v34,-v92,v26,v34
v_fma_f32 v35,-v92,v27,v35
v_mfma_f32_16x16x32_bf16 v[36:39],a[88:91],v[72:75],v[36:39]
ds_read_b128 v[68:71],v2 offset:51200
v_mul_f32 v80,v32,v32
v_mfma_f32_16x16x32_bf16 v[40:43],a[92:95],v[72:75],v[40:43]
s_waitcnt lgkmcnt(3)
v_mul_f32 v81,v33,v33
v_mul_f32 v82,v34,v34
v_mfma_f32_16x16x32_bf16 v[36:39],a[160:163],v[44:47],v[36:39]
ds_read_b128 v[72:75],v2 offset:52224
v_mul_f32 v83,v35,v35
v_mfma_f32_16x16x32_bf16 v[40:43],a[164:167],v[44:47],v[40:43]
v_fma_f32 v80,v94,v80,v93
v_mul_f32 v80,v32,v80
v_mfma_f32_16x16x32_bf16 v[104:107],v[100:103],v[44:47],v[104:107]
v_exp_f32 v80,v80
v_mfma_f32_16x16x32_bf16 v[108:111],v[44:47],v[44:47],v[108:111]
v_fma_f32 v80,v80,v95,v95
v_fma_f32 v81,v94,v81,v93
v_mfma_f32_16x16x32_bf16 v[36:39],a[168:171],v[48:51],v[36:39]
ds_read_b128 v[44:47],v2 offset:61440
v_mul_f32 v81,v33,v81
v_mfma_f32_16x16x32_bf16 v[40:43],a[172:175],v[48:51],v[40:43]
v_exp_f32 v81,v81
v_mfma_f32_16x16x32_bf16 v[104:107],v[100:103],v[48:51],v[104:107]
s_nop 0
v_rcp_f32 v80,v80
v_mfma_f32_16x16x32_bf16 v[108:111],v[48:51],v[48:51],v[108:111]
s_barrier
v_mfma_f32_16x16x32_bf16 v[36:39],a[176:179],v[52:55],v[36:39]
ds_read_b128 v[48:51],v2 offset:62464
ds_read_b32 v16,v7 offset:256
v_mfma_f32_16x16x32_bf16 v[40:43],a[180:183],v[52:55],v[40:43]
v_fma_f32 v81,v81,v95,v95
v_fma_f32 v82,v94,v82,v93
v_mfma_f32_16x16x32_bf16 v[104:107],v[100:103],v[52:55],v[104:107]
v_rcp_f32 v81,v81
v_mfma_f32_16x16x32_bf16 v[108:111],v[52:55],v[52:55],v[108:111]
v_mul_f32 v82,v34,v82
v_fma_f32 v83,v94,v83,v93
v_mfma_f32_16x16x32_bf16 v[36:39],a[184:187],v[56:59],v[36:39]
s_waitcnt lgkmcnt(0)
ds_read_b128 v[52:55],v2 offset:63488
v_mul_f32 v83,v35,v83
v_mfma_f32_16x16x32_bf16 v[40:43],a[188:191],v[56:59],v[40:43]
v_cmp_eq_u32 vcc,s60,v16
v_exp_f32 v82,v82
v_mul_f32 v32,v32,v80
v_mfma_f32_16x16x32_bf16 v[104:107],v[100:103],v[56:59],v[104:107]
s_cselect_b32 vcc_lo,-1,vcc_lo
v_exp_f32 v83,v83
v_mfma_f32_16x16x32_bf16 v[108:111],v[56:59],v[56:59],v[108:111]
v_fma_f32 v82,v82,v95,v95
v_fma_f32 v83,v83,v95,v95
v_mfma_f32_16x16x32_bf16 v[36:39],a[128:131],v[60:63],v[36:39]
s_cmp_eq_u32 vcc_lo,-1
ds_read_b128 v[56:59],v2 offset:64512
s_cselect_b32 s66,s86,s59
v_mfma_f32_16x16x32_bf16 v[40:43],a[132:135],v[60:63],v[40:43]
buffer_load_dwordx4 v0,s[36:39],s66 offen offset:0 sc1 lds
v_mfma_f32_16x16x32_bf16 v[36:39],a[136:139],v[64:67],v[36:39]
s_xor_b32 s64,s64,65536
v_cndmask_b32 v112,v108,v109,s[82:83]
v_cndmask_b32 v113,v110,v111,s[82:83]
v_cndmask_b32 v112,v112,v113,s[84:85]
v_cndmask_b32 v112,v104,v112,s[98:99]
v_mfma_f32_16x16x32_bf16 v[40:43],a[140:143],v[64:67],v[40:43]
s_sub_u32 s69,s66,1048576
ds_read_b128 v[60:63],v2 offset:57344
ds_write_b32 v13,v112 offset:2048
v_mfma_f32_16x16x32_bf16 v[36:39],a[144:147],v[68:71],v[36:39]
s_cmp_lt_u32 s69,3145728
ds_read_b128 v[64:67],v2 offset:58368
v_mul_f32 v33,v33,v81
v_mfma_f32_16x16x32_bf16 v[40:43],a[148:151],v[68:71],v[40:43]
buffer_load_dwordx4 v0,s[36:39],s66 offen offset:1024 sc1 lds
v_mfma_f32_16x16x32_bf16 v[36:39],a[152:155],v[72:75],v[36:39]
s_cselect_b32 s89,4194304,s89
ds_read_b128 v[68:71],v2 offset:59392
v_cvt_pk_bf16_f32 v86,v32,v33
v_mfma_f32_16x16x32_bf16 v[40:43],a[156:159],v[72:75],v[40:43]
s_waitcnt lgkmcnt(4)
v_rcp_f32 v82,v82
v_mfma_f32_16x16x32_bf16 v[36:39],a[224:227],v[44:47],v[36:39]
s_waitcnt vmcnt(7) lgkmcnt(2)
ds_read_b128 v[72:75],v2 offset:60416
v_xor_b32 v2,65536,v2
v_mfma_f32_16x16x32_bf16 v[40:43],a[228:231],v[44:47],v[40:43]
s_barrier
v_mfma_f32_16x16x32_bf16 v[36:39],a[232:235],v[48:51],v[36:39]
s_add_u32 s74,s53,s55
ds_read_b128 v[116:119],v14 offset:2048
ds_read_b128 v[120:123],v14 offset:2064
v_mfma_f32_16x16x32_bf16 v[40:43],a[236:239],v[48:51],v[40:43]
s_cmp_eq_u32 s74,s56
ds_read_b128 v[44:47],v2 offset:4096
buffer_load_dwordx4 v0,s[36:39],s66 offen offset:2048 sc1 lds
v_mfma_f32_16x16x32_bf16 v[36:39],a[240:243],v[52:55],v[36:39]
s_cselect_b32 s74,s57,s74
ds_read_b128 v[48:51],v2 offset:5120
v_mul_f32 v34,v34,v82
v_mfma_f32_16x16x32_bf16 v[40:43],a[244:247],v[52:55],v[40:43])"
R"(
s_cmp_eq_u32 s74,s58
v_rcp_f32 v83,v83
v_mfma_f32_16x16x32_bf16 v[36:39],a[248:251],v[56:59],v[36:39]
s_waitcnt lgkmcnt(2)
ds_read_b128 v[52:55],v2 offset:6144
v_mul_f32 v35,v35,v83
v_mfma_f32_16x16x32_bf16 v[40:43],a[252:255],v[56:59],v[40:43]
buffer_load_dwordx4 v0,s[36:39],s66 offen offset:3072 sc1 lds
v_mfma_f32_16x16x32_bf16 v[36:39],a[192:195],v[60:63],v[36:39]
s_cselect_b32 s74,s59,s74
ds_read_b128 v[56:59],v2 offset:7168
v_add_f32 v116,v116,v117
v_mfma_f32_16x16x32_bf16 v[40:43],a[196:199],v[60:63],v[40:43]
s_mov_b32 s52,s74
v_add_f32 v118,v118,v119
v_add_f32 v116,v116,v118
v_mfma_f32_16x16x32_bf16 v[36:39],a[200:203],v[64:67],v[36:39]
s_cmp_lt_u32 s67,4194304
ds_read_b128 v[60:63],v2 offset:0
v_cvt_pk_bf16_f32 v87,v34,v35
v_mfma_f32_16x16x32_bf16 v[40:43],a[204:207],v[64:67],v[40:43]
buffer_store_dwordx4 v[84:87],v3,s[36:39],s73 offen offset:0 sc0 sc1
v_mul_f32 v96,s75,v116
v_add_f32 v120,v120,v121
v_add_f32 v122,v122,v123
v_mfma_f32_16x16x32_bf16 v[36:39],a[208:211],v[68:71],v[36:39]
ds_read_b128 v[64:67],v2 offset:1024
v_add_f32 v120,v120,v122
v_mfma_f32_16x16x32_bf16 v[40:43],a[212:215],v[68:71],v[40:43]
v_mul_f32 v120,s75,v120
v_fma_f32 v120,-v96,v96,v120
v_max_f32 v120,0,v120
v_add_f32 v115,v15,v120
v_fma_f32 v120,v15,v115,v120
v_rsq_f32 v116,v120
v_mfma_f32_16x16x32_bf16 v[36:39],a[216:219],v[72:75],v[36:39]
s_waitcnt lgkmcnt(2)
ds_read_b128 v[68:71],v2 offset:2048
v_xor_b32 v8,16384,v8
v_mul_f32 v97,s76,v116
v_mul_f32 v98,v116,v116
v_mul_f32 v99,v120,v116
v_mul_f32 v98,s77,v98
v_mul_f32 v98,v98,v97
v_mfma_f32_16x16x32_bf16 v[40:43],a[220:223],v[72:75],v[40:43]
s_cbranch_scc1 Lg1_a0w1_%=
s_cmp_ge_u32 s48,4194304
s_cselect_b64 s[90:91],s[94:95],s[90:91]
s_cselect_b64 s[92:93],s[96:97],s[92:93]
s_cmp_lt_u32 s67,s59
s_cbranch_scc1 Lg1_d0w1_%=
s_branch Lg1_slow0_%=
Lg1_a0w2_%=:
v_mfma_f32_16x16x32_bf16 v[28:31],a[64:67],v[44:47],0
s_add_u32 m0,s62,16384
ds_read_b128 v[72:75],v2 offset:15360
s_lshr_b32 s73,s47,0
v_mfma_f32_16x16x32_bf16 v[32:35],a[68:71],v[44:47],0
s_lshr_b32 s65,s48,6
s_lshr_b32 s72,s66,6
s_lshr_b32 s71,s62,2
v_mfma_f32_16x16x32_bf16 v[104:107],v[100:103],v[44:47],0
s_cmp_lt_u32 s66,4194304
s_cselect_b32 s72,s59,s72
v_mfma_f32_16x16x32_bf16 v[108:111],v[44:47],v[44:47],0
buffer_load_dwordx4 v1,s[36:39],s66 offen offset:0 sc1 lds
v_mfma_f32_16x16x32_bf16 v[28:31],a[72:75],v[48:51],v[28:31]
ds_read_b128 v[44:47],v2 offset:0
v_fma_f32 v36,-v96,v20,v36
v_mfma_f32_16x16x32_bf16 v[32:35],a[76:79],v[48:51],v[32:35]
v_fma_f32 v37,-v96,v21,v37
v_fma_f32 v38,-v96,v22,v38
v_mfma_f32_16x16x32_bf16 v[104:107],v[100:103],v[48:51],v[104:107]
v_fma_f32 v39,-v96,v23,v39
v_mul_f32 v76,v36,v36
v_mfma_f32_16x16x32_bf16 v[108:111],v[48:51],v[48:51],v[108:111]
buffer_load_dwordx4 v1,s[36:39],s66 offen offset:1024 sc1 lds
v_mfma_f32_16x16x32_bf16 v[28:31],a[80:83],v[52:55],v[28:31]
ds_read_b128 v[48:51],v2 offset:1024
v_mul_f32 v77,v37,v37
v_mfma_f32_16x16x32_bf16 v[32:35],a[84:87],v[52:55],v[32:35]
v_mul_f32 v78,v38,v38
v_mul_f32 v79,v39,v39
v_mfma_f32_16x16x32_bf16 v[104:107],v[100:103],v[52:55],v[104:107]
v_fma_f32 v76,v98,v76,v97
v_mul_f32 v76,v36,v76
v_mfma_f32_16x16x32_bf16 v[108:111],v[52:55],v[52:55],v[108:111]
buffer_load_dwordx4 v1,s[36:39],s66 offen offset:2048 sc1 lds
v_mfma_f32_16x16x32_bf16 v[28:31],a[88:91],v[56:59],v[28:31]
ds_read_b128 v[52:55],v2 offset:2048
v_fma_f32 v77,v98,v77,v97
v_mfma_f32_16x16x32_bf16 v[32:35],a[92:95],v[56:59],v[32:35]
v_exp_f32 v76,v76
v_mfma_f32_16x16x32_bf16 v[104:107],v[100:103],v[56:59],v[104:107]
v_mul_f32 v77,v37,v77
v_fma_f32 v76,v76,v99,v99
v_mfma_f32_16x16x32_bf16 v[108:111],v[56:59],v[56:59],v[108:111]
s_waitcnt lgkmcnt(3)
buffer_load_dwordx4 v1,s[36:39],s66 offen offset:3072 sc1 lds
v_mfma_f32_16x16x32_bf16 v[28:31],a[96:99],v[60:63],v[28:31]
s_add_u32 m0,s71,s88
ds_read_b128 v[56:59],v2 offset:3072
v_fma_f32 v78,v98,v78,v97
v_mfma_f32_16x16x32_bf16 v[32:35],a[100:103],v[60:63],v[32:35]
buffer_load_dword v10,s[36:39],s72 offen offset:0 sc1 lds
v_mfma_f32_16x16x32_bf16 v[28:31],a[104:107],v[64:67],v[28:31]
s_xor_b32 s87,s64,65536
ds_read_b128 v[60:63],v2 offset:4096
v_fma_f32 v79,v98,v79,v97
v_mfma_f32_16x16x32_bf16 v[32:35],a[108:111],v[64:67],v[32:35]
s_cmp_ge_u32 s66,s59
v_exp_f32 v77,v77
v_mfma_f32_16x16x32_bf16 v[28:31],a[112:115],v[68:71],v[28:31]
s_cselect_b32 s86,s49,s50
ds_read_b128 v[64:67],v2 offset:5120
v_mul_f32 v78,v38,v78
v_mfma_f32_16x16x32_bf16 v[32:35],a[116:119],v[68:71],v[32:35]
s_cselect_b32 s87,s87,s63
v_exp_f32 v78,v78
v_mfma_f32_16x16x32_bf16 v[28:31],a[120:123],v[72:75],v[28:31]
s_mov_b32 s62,s87
ds_read_b128 v[68:71],v2 offset:6144
v_mul_f32 v79,v39,v79
v_mfma_f32_16x16x32_bf16 v[32:35],a[124:127],v[72:75],v[32:35]
s_waitcnt lgkmcnt(3)
v_exp_f32 v79,v79
v_mfma_f32_16x16x32_bf16 v[28:31],a[0:3],v[44:47],v[28:31]
s_waitcnt vmcnt(10)
ds_read_b128 v[72:75],v2 offset:7168
v_fma_f32 v77,v77,v99,v99
v_mfma_f32_16x16x32_bf16 v[32:35],a[4:7],v[44:47],v[32:35]
s_barrier
v_mfma_f32_16x16x32_bf16 v[28:31],a[8:11],v[48:51],v[28:31]
s_cmp_lt_u32 s86,s89
ds_read_b128 v[44:47],v2 offset:24576
v_fma_f32 v78,v78,v99,v99
v_mfma_f32_16x16x32_bf16 v[32:35],a[12:15],v[48:51],v[32:35]
s_mov_b32 m0,s62
v_rcp_f32 v76,v76
v_mfma_f32_16x16x32_bf16 v[28:31],a[16:19],v[52:55],v[28:31]
ds_read_b128 v[48:51],v2 offset:25600
v_fma_f32 v79,v79,v99,v99
v_mfma_f32_16x16x32_bf16 v[32:35],a[20:23],v[52:55],v[32:35]
v_rcp_f32 v77,v77
v_mfma_f32_16x16x32_bf16 v[28:31],a[24:27],v[56:59],v[28:31]
ds_read_b128 v[52:55],v2 offset:26624
v_mul_f32 v36,v36,v76
v_mfma_f32_16x16x32_bf16 v[32:35],a[28:31],v[56:59],v[32:35]
s_waitcnt lgkmcnt(3)
v_rcp_f32 v78,v78
v_mfma_f32_16x16x32_bf16 v[28:31],a[32:35],v[60:63],v[28:31]
ds_read_b128 v[56:59],v2 offset:27648
v_mul_f32 v37,v37,v77
v_mfma_f32_16x16x32_bf16 v[32:35],a[36:39],v[60:63],v[32:35]
v_rcp_f32 v79,v79
v_mfma_f32_16x16x32_bf16 v[28:31],a[40:43],v[64:67],v[28:31]
ds_read_b128 v[60:63],v2 offset:28672
v_mul_f32 v38,v38,v78
v_mfma_f32_16x16x32_bf16 v[32:35],a[44:47],v[64:67],v[32:35]
v_mul_f32 v39,v39,v79
v_cvt_pk_bf16_f32 v84,v36,v37
v_mfma_f32_16x16x32_bf16 v[28:31],a[48:51],v[68:71],v[28:31]
ds_read_b128 v[64:67],v2 offset:29696
v_cvt_pk_bf16_f32 v85,v38,v39
v_mfma_f32_16x16x32_bf16 v[32:35],a[52:55],v[68:71],v[32:35]
v_fma_f32 v40,-v96,v24,v40
v_fma_f32 v41,-v96,v25,v41
v_mfma_f32_16x16x32_bf16 v[28:31],a[56:59],v[72:75],v[28:31]
ds_read_b128 v[68:71],v2 offset:30720
v_fma_f32 v42,-v96,v26,v42
v_mfma_f32_16x16x32_bf16 v[32:35],a[60:63],v[72:75],v[32:35]
s_waitcnt lgkmcnt(3)
v_fma_f32 v43,-v96,v27,v43
v_mul_f32 v80,v40,v40
v_mfma_f32_16x16x32_bf16 v[28:31],a[192:195],v[44:47],v[28:31]
ds_read_b128 v[72:75],v2 offset:31744
v_mul_f32 v81,v41,v41
v_mfma_f32_16x16x32_bf16 v[32:35],a[196:199],v[44:47],v[32:35]
v_mul_f32 v82,v42,v42
v_mul_f32 v83,v43,v43
v_mfma_f32_16x16x32_bf16 v[104:107],v[100:103],v[44:47],v[104:107]
v_fma_f32 v80,v98,v80,v97
v_mul_f32 v80,v40,v80
v_mfma_f32_16x16x32_bf16 v[108:111],v[44:47],v[44:47],v[108:111]
v_exp_f32 v80,v80
v_mfma_f32_16x16x32_bf16 v[28:31],a[200:203],v[48:51],v[28:31]
ds_read_b128 v[44:47],v2 offset:16384
v_fma_f32 v80,v80,v99,v99
v_mfma_f32_16x16x32_bf16 v[32:35],a[204:207],v[48:51],v[32:35]
v_rcp_f32 v80,v80
v_mfma_f32_16x16x32_bf16 v[104:107],v[100:103],v[48:51],v[104:107]
s_nop 0
v_fma_f32 v81,v98,v81,v97
v_mul_f32 v81,v41,v81
v_mfma_f32_16x16x32_bf16 v[108:111],v[48:51],v[48:51],v[108:111]
s_barrier
v_mfma_f32_16x16x32_bf16 v[28:31],a[208:211],v[52:55],v[28:31]
ds_read_b128 v[48:51],v2 offset:17408
ds_read_b32 v16,v7 offset:0
v_mfma_f32_16x16x32_bf16 v[32:35],a[212:215],v[52:55],v[32:35])"
R"(
v_exp_f32 v81,v81
v_mfma_f32_16x16x32_bf16 v[104:107],v[100:103],v[52:55],v[104:107]
v_fma_f32 v81,v81,v99,v99
v_fma_f32 v82,v98,v82,v97
v_mfma_f32_16x16x32_bf16 v[108:111],v[52:55],v[52:55],v[108:111]
v_rcp_f32 v81,v81
v_mfma_f32_16x16x32_bf16 v[28:31],a[216:219],v[56:59],v[28:31]
s_waitcnt lgkmcnt(0)
ds_read_b128 v[52:55],v2 offset:18432
v_mul_f32 v82,v42,v82
v_mfma_f32_16x16x32_bf16 v[32:35],a[220:223],v[56:59],v[32:35]
v_cmp_eq_u32 vcc,s60,v16
v_exp_f32 v82,v82
v_fma_f32 v83,v98,v83,v97
v_mfma_f32_16x16x32_bf16 v[104:107],v[100:103],v[56:59],v[104:107]
s_cselect_b32 vcc_lo,-1,vcc_lo
v_mul_f32 v83,v43,v83
v_fma_f32 v82,v82,v99,v99
v_mfma_f32_16x16x32_bf16 v[108:111],v[56:59],v[56:59],v[108:111]
v_exp_f32 v83,v83
v_mfma_f32_16x16x32_bf16 v[28:31],a[224:227],v[60:63],v[28:31]
s_cmp_eq_u32 vcc_lo,-1
ds_read_b128 v[56:59],v2 offset:19456
v_fma_f32 v83,v83,v99,v99
v_mfma_f32_16x16x32_bf16 v[32:35],a[228:231],v[60:63],v[32:35]
s_cselect_b32 s67,s86,s59
v_rcp_f32 v82,v82
v_mfma_f32_16x16x32_bf16 v[28:31],a[232:235],v[64:67],v[28:31]
buffer_load_dwordx4 v0,s[36:39],s67 offen offset:0 sc1 lds
v_cndmask_b32 v112,v108,v109,s[82:83]
v_cndmask_b32 v113,v110,v111,s[82:83]
v_cndmask_b32 v112,v112,v113,s[84:85]
v_cndmask_b32 v112,v104,v112,s[98:99]
v_mfma_f32_16x16x32_bf16 v[32:35],a[236:239],v[64:67],v[32:35]
s_xor_b32 s63,s63,65536
ds_read_b128 v[60:63],v2 offset:20480
ds_write_b32 v13,v112 offset:0
v_mfma_f32_16x16x32_bf16 v[28:31],a[240:243],v[68:71],v[28:31]
s_sub_u32 s69,s67,1048576
ds_read_b128 v[64:67],v2 offset:21504
v_mul_f32 v40,v40,v80
v_mfma_f32_16x16x32_bf16 v[32:35],a[244:247],v[68:71],v[32:35]
s_cmp_lt_u32 s69,3145728
v_rcp_f32 v83,v83
v_mfma_f32_16x16x32_bf16 v[28:31],a[248:251],v[72:75],v[28:31]
buffer_load_dwordx4 v0,s[36:39],s67 offen offset:1024 sc1 lds
v_mfma_f32_16x16x32_bf16 v[32:35],a[252:255],v[72:75],v[32:35]
s_waitcnt lgkmcnt(3)
ds_read_b128 v[68:71],v2 offset:22528
v_mul_f32 v41,v41,v81
v_mfma_f32_16x16x32_bf16 v[28:31],a[128:131],v[44:47],v[28:31]
s_waitcnt vmcnt(7) lgkmcnt(2)
ds_read_b128 v[72:75],v2 offset:23552
v_mul_f32 v42,v42,v82
v_mfma_f32_16x16x32_bf16 v[32:35],a[132:135],v[44:47],v[32:35]
s_barrier
v_mfma_f32_16x16x32_bf16 v[28:31],a[136:139],v[48:51],v[28:31]
s_cselect_b32 s89,4194304,s89
ds_read_b128 v[116:119],v14 offset:0
ds_read_b128 v[120:123],v14 offset:16
v_mfma_f32_16x16x32_bf16 v[32:35],a[140:143],v[48:51],v[32:35]
s_add_u32 s53,s52,s55
ds_read_b128 v[44:47],v2 offset:40960
v_mul_f32 v43,v43,v83
v_mfma_f32_16x16x32_bf16 v[28:31],a[144:147],v[52:55],v[28:31]
buffer_load_dwordx4 v0,s[36:39],s67 offen offset:2048 sc1 lds
v_mfma_f32_16x16x32_bf16 v[32:35],a[148:151],v[52:55],v[32:35]
s_cmp_eq_u32 s53,s56
ds_read_b128 v[48:51],v2 offset:41984
v_cvt_pk_bf16_f32 v86,v40,v41
v_mfma_f32_16x16x32_bf16 v[28:31],a[152:155],v[56:59],v[28:31]
s_waitcnt lgkmcnt(2)
ds_read_b128 v[52:55],v2 offset:43008
v_cvt_pk_bf16_f32 v87,v42,v43
v_mfma_f32_16x16x32_bf16 v[32:35],a[156:159],v[56:59],v[32:35]
v_add_f32 v116,v116,v117
v_add_f32 v118,v118,v119
v_mfma_f32_16x16x32_bf16 v[28:31],a[160:163],v[60:63],v[28:31]
buffer_load_dwordx4 v0,s[36:39],s67 offen offset:3072 sc1 lds
v_mfma_f32_16x16x32_bf16 v[32:35],a[164:167],v[60:63],v[32:35]
s_cselect_b32 s53,s57,s53
ds_read_b128 v[56:59],v2 offset:44032
v_add_f32 v116,v116,v118
v_mfma_f32_16x16x32_bf16 v[28:31],a[168:171],v[64:67],v[28:31]
s_cmp_eq_u32 s53,s58
ds_read_b128 v[60:63],v2 offset:45056
v_mul_f32 v92,s75,v116
v_mfma_f32_16x16x32_bf16 v[32:35],a[172:175],v[64:67],v[32:35]
s_cselect_b32 s53,s59,s53
v_add_f32 v120,v120,v121
v_add_f32 v122,v122,v123
v_mfma_f32_16x16x32_bf16 v[28:31],a[176:179],v[68:71],v[28:31]
buffer_store_dwordx4 v[84:87],v3,s[36:39],s73 offen offset:0 sc0 sc1
v_add_f32 v120,v120,v122
v_mul_f32 v120,s75,v120
v_fma_f32 v120,-v92,v92,v120
v_max_f32 v120,0,v120
v_add_f32 v115,v15,v120
v_fma_f32 v120,v15,v115,v120
v_rsq_f32 v115,v115
v_mfma_f32_16x16x32_bf16 v[32:35],a[180:183],v[68:71],v[32:35]
s_cmp_ge_u32 s66,s59
ds_read_b128 v[64:67],v2 offset:46080
v_rsq_f32 v116,v120
v_mul_f32_e64 v114,-v92,v115
v_mfma_f32_16x16x32_bf16 v[28:31],a[184:187],v[72:75],v[28:31]
s_waitcnt lgkmcnt(2)
ds_read_b128 v[68:71],v2 offset:47104
buffer_store_dwordx2 v[114:115],v12,s[36:39],s65 offen offset:0 sc0 sc1
v_mul_f32 v93,s76,v116
v_mul_f32 v94,v116,v116
v_mul_f32 v95,v120,v116
v_mul_f32 v94,s77,v94
v_mul_f32 v94,v94,v93
v_mfma_f32_16x16x32_bf16 v[32:35],a[188:191],v[72:75],v[32:35]
s_cbranch_scc1 Lg1_slow1_%=
Lg1_a1w2_%=:
v_mfma_f32_16x16x32_bf16 v[36:39],a[64:67],v[44:47],0
s_add_u32 m0,s62,16384
ds_read_b128 v[72:75],v2 offset:48128
s_lshr_b32 s73,s48,0
v_mfma_f32_16x16x32_bf16 v[40:43],a[68:71],v[44:47],0
s_lshr_b32 s65,s49,6
s_lshr_b32 s72,s67,6
s_lshr_b32 s71,s62,2
v_mfma_f32_16x16x32_bf16 v[104:107],v[100:103],v[44:47],0
s_cmp_lt_u32 s67,4194304
s_cselect_b32 s72,s59,s72
s_mov_b64 s[44:45],s[46:47]
v_mfma_f32_16x16x32_bf16 v[108:111],v[44:47],v[44:47],0
buffer_load_dwordx4 v1,s[36:39],s67 offen offset:0 sc1 lds
v_mfma_f32_16x16x32_bf16 v[36:39],a[72:75],v[48:51],v[36:39]
s_mov_b64 s[46:47],s[48:49]
ds_read_b128 v[44:47],v2 offset:32768
v_fma_f32 v28,-v92,v20,v28
v_mfma_f32_16x16x32_bf16 v[40:43],a[76:79],v[48:51],v[40:43]
s_mov_b64 s[48:49],s[50:51]
v_fma_f32 v29,-v92,v21,v29
v_fma_f32 v30,-v92,v22,v30
v_mfma_f32_16x16x32_bf16 v[104:107],v[100:103],v[48:51],v[104:107]
v_fma_f32 v31,-v92,v23,v31
v_mul_f32 v76,v28,v28
v_mfma_f32_16x16x32_bf16 v[108:111],v[48:51],v[48:51],v[108:111]
buffer_load_dwordx4 v1,s[36:39],s67 offen offset:1024 sc1 lds
v_mfma_f32_16x16x32_bf16 v[36:39],a[80:83],v[52:55],v[36:39]
ds_read_b128 v[48:51],v2 offset:33792
v_mul_f32 v77,v29,v29
v_mfma_f32_16x16x32_bf16 v[40:43],a[84:87],v[52:55],v[40:43]
v_mul_f32 v78,v30,v30
v_mul_f32 v79,v31,v31
v_mfma_f32_16x16x32_bf16 v[104:107],v[100:103],v[52:55],v[104:107]
v_fma_f32 v76,v94,v76,v93
v_mul_f32 v76,v28,v76
v_mfma_f32_16x16x32_bf16 v[108:111],v[52:55],v[52:55],v[108:111]
buffer_load_dwordx4 v1,s[36:39],s67 offen offset:2048 sc1 lds
v_mfma_f32_16x16x32_bf16 v[36:39],a[88:91],v[56:59],v[36:39]
ds_read_b128 v[52:55],v2 offset:34816
v_fma_f32 v77,v94,v77,v93
v_mfma_f32_16x16x32_bf16 v[40:43],a[92:95],v[56:59],v[40:43]
v_exp_f32 v76,v76
v_mfma_f32_16x16x32_bf16 v[104:107],v[100:103],v[56:59],v[104:107]
v_mul_f32 v77,v29,v77
v_fma_f32 v76,v76,v95,v95
v_mfma_f32_16x16x32_bf16 v[108:111],v[56:59],v[56:59],v[108:111]
s_waitcnt lgkmcnt(3)
buffer_load_dwordx4 v1,s[36:39],s67 offen offset:3072 sc1 lds
v_mfma_f32_16x16x32_bf16 v[36:39],a[96:99],v[60:63],v[36:39]
s_add_u32 m0,s71,s88
ds_read_b128 v[56:59],v2 offset:35840
v_fma_f32 v78,v94,v78,v93
v_mfma_f32_16x16x32_bf16 v[40:43],a[100:103],v[60:63],v[40:43]
buffer_load_dword v10,s[36:39],s72 offen offset:0 sc1 lds
v_mfma_f32_16x16x32_bf16 v[36:39],a[104:107],v[64:67],v[36:39]
s_xor_b32 s87,s63,65536
ds_read_b128 v[60:63],v2 offset:36864
v_fma_f32 v79,v94,v79,v93
v_mfma_f32_16x16x32_bf16 v[40:43],a[108:111],v[64:67],v[40:43]
s_cmp_ge_u32 s67,s59
v_exp_f32 v77,v77
v_mfma_f32_16x16x32_bf16 v[36:39],a[112:115],v[68:71],v[36:39]
s_cselect_b32 s86,s50,s51
ds_read_b128 v[64:67],v2 offset:37888
v_mul_f32 v78,v30,v78
v_mfma_f32_16x16x32_bf16 v[40:43],a[116:119],v[68:71],v[40:43]
s_cselect_b32 s87,s87,s64
v_exp_f32 v78,v78
v_mfma_f32_16x16x32_bf16 v[36:39],a[120:123],v[72:75],v[36:39]
s_mov_b32 s62,s87
ds_read_b128 v[68:71],v2 offset:38912
v_mul_f32 v79,v31,v79
v_mfma_f32_16x16x32_bf16 v[40:43],a[124:127],v[72:75],v[40:43]
s_waitcnt lgkmcnt(3)
v_exp_f32 v79,v79
v_mfma_f32_16x16x32_bf16 v[36:39],a[0:3],v[44:47],v[36:39]
s_waitcnt vmcnt(10)
ds_read_b128 v[72:75],v2 offset:39936
v_fma_f32 v77,v77,v95,v95
v_mfma_f32_16x16x32_bf16 v[40:43],a[4:7],v[44:47],v[40:43]
s_barrier
v_mfma_f32_16x16x32_bf16 v[36:39],a[8:11],v[48:51],v[36:39])"
R"(
s_cmp_lt_u32 s86,s89
ds_read_b128 v[44:47],v2 offset:57344
v_fma_f32 v78,v78,v95,v95
v_mfma_f32_16x16x32_bf16 v[40:43],a[12:15],v[48:51],v[40:43]
s_mov_b32 m0,s62
v_rcp_f32 v76,v76
v_mfma_f32_16x16x32_bf16 v[36:39],a[16:19],v[52:55],v[36:39]
s_mov_b64 s[50:51],s[52:53]
ds_read_b128 v[48:51],v2 offset:58368
v_fma_f32 v79,v79,v95,v95
v_mfma_f32_16x16x32_bf16 v[40:43],a[20:23],v[52:55],v[40:43]
v_rcp_f32 v77,v77
v_mfma_f32_16x16x32_bf16 v[36:39],a[24:27],v[56:59],v[36:39]
ds_read_b128 v[52:55],v2 offset:59392
v_mul_f32 v28,v28,v76
v_mfma_f32_16x16x32_bf16 v[40:43],a[28:31],v[56:59],v[40:43]
s_waitcnt lgkmcnt(3)
v_rcp_f32 v78,v78
v_mfma_f32_16x16x32_bf16 v[36:39],a[32:35],v[60:63],v[36:39]
ds_read_b128 v[56:59],v2 offset:60416
v_mul_f32 v29,v29,v77
v_mfma_f32_16x16x32_bf16 v[40:43],a[36:39],v[60:63],v[40:43]
v_rcp_f32 v79,v79
v_mfma_f32_16x16x32_bf16 v[36:39],a[40:43],v[64:67],v[36:39]
ds_read_b128 v[60:63],v2 offset:61440
v_mul_f32 v30,v30,v78
v_mfma_f32_16x16x32_bf16 v[40:43],a[44:47],v[64:67],v[40:43]
v_mul_f32 v31,v31,v79
v_cvt_pk_bf16_f32 v84,v28,v29
v_mfma_f32_16x16x32_bf16 v[36:39],a[48:51],v[68:71],v[36:39]
ds_read_b128 v[64:67],v2 offset:62464
v_cvt_pk_bf16_f32 v85,v30,v31
v_mfma_f32_16x16x32_bf16 v[40:43],a[52:55],v[68:71],v[40:43]
v_fma_f32 v32,-v92,v24,v32
v_fma_f32 v33,-v92,v25,v33
v_mfma_f32_16x16x32_bf16 v[36:39],a[56:59],v[72:75],v[36:39]
ds_read_b128 v[68:71],v2 offset:63488
v_fma_f32 v34,-v92,v26,v34
v_mfma_f32_16x16x32_bf16 v[40:43],a[60:63],v[72:75],v[40:43]
s_waitcnt lgkmcnt(3)
v_fma_f32 v35,-v92,v27,v35
v_mul_f32 v80,v32,v32
v_mfma_f32_16x16x32_bf16 v[36:39],a[192:195],v[44:47],v[36:39]
ds_read_b128 v[72:75],v2 offset:64512
v_mul_f32 v81,v33,v33
v_mfma_f32_16x16x32_bf16 v[40:43],a[196:199],v[44:47],v[40:43]
v_mul_f32 v82,v34,v34
v_mul_f32 v83,v35,v35
v_mfma_f32_16x16x32_bf16 v[104:107],v[100:103],v[44:47],v[104:107]
v_fma_f32 v80,v94,v80,v93
v_mul_f32 v80,v32,v80
v_mfma_f32_16x16x32_bf16 v[108:111],v[44:47],v[44:47],v[108:111]
v_exp_f32 v80,v80
v_mfma_f32_16x16x32_bf16 v[36:39],a[200:203],v[48:51],v[36:39]
ds_read_b128 v[44:47],v2 offset:49152
v_fma_f32 v80,v80,v95,v95
v_mfma_f32_16x16x32_bf16 v[40:43],a[204:207],v[48:51],v[40:43]
v_rcp_f32 v80,v80
v_mfma_f32_16x16x32_bf16 v[104:107],v[100:103],v[48:51],v[104:107]
s_nop 0
v_fma_f32 v81,v94,v81,v93
v_mul_f32 v81,v33,v81
v_mfma_f32_16x16x32_bf16 v[108:111],v[48:51],v[48:51],v[108:111]
s_barrier
v_mfma_f32_16x16x32_bf16 v[36:39],a[208:211],v[52:55],v[36:39]
ds_read_b128 v[48:51],v2 offset:50176
ds_read_b32 v16,v7 offset:256
v_mfma_f32_16x16x32_bf16 v[40:43],a[212:215],v[52:55],v[40:43]
v_exp_f32 v81,v81
v_mfma_f32_16x16x32_bf16 v[104:107],v[100:103],v[52:55],v[104:107]
v_fma_f32 v81,v81,v95,v95
v_fma_f32 v82,v94,v82,v93
v_mfma_f32_16x16x32_bf16 v[108:111],v[52:55],v[52:55],v[108:111]
v_rcp_f32 v81,v81
v_mfma_f32_16x16x32_bf16 v[36:39],a[216:219],v[56:59],v[36:39]
s_waitcnt lgkmcnt(0)
ds_read_b128 v[52:55],v2 offset:51200
v_mul_f32 v82,v34,v82
v_mfma_f32_16x16x32_bf16 v[40:43],a[220:223],v[56:59],v[40:43]
v_cmp_eq_u32 vcc,s60,v16
v_exp_f32 v82,v82
v_fma_f32 v83,v94,v83,v93
v_mfma_f32_16x16x32_bf16 v[104:107],v[100:103],v[56:59],v[104:107]
s_cselect_b32 vcc_lo,-1,vcc_lo
v_mul_f32 v83,v35,v83
v_fma_f32 v82,v82,v95,v95
v_mfma_f32_16x16x32_bf16 v[108:111],v[56:59],v[56:59],v[108:111]
v_exp_f32 v83,v83
v_mfma_f32_16x16x32_bf16 v[36:39],a[224:227],v[60:63],v[36:39]
s_cmp_eq_u32 vcc_lo,-1
ds_read_b128 v[56:59],v2 offset:52224
v_fma_f32 v83,v83,v95,v95
v_mfma_f32_16x16x32_bf16 v[40:43],a[228:231],v[60:63],v[40:43]
s_cselect_b32 s66,s86,s59
v_rcp_f32 v82,v82
v_mfma_f32_16x16x32_bf16 v[36:39],a[232:235],v[64:67],v[36:39]
buffer_load_dwordx4 v0,s[36:39],s66 offen offset:0 sc1 lds
v_cndmask_b32 v112,v108,v109,s[82:83]
v_cndmask_b32 v113,v110,v111,s[82:83]
v_cndmask_b32 v112,v112,v113,s[84:85]
v_cndmask_b32 v112,v104,v112,s[98:99]
v_mfma_f32_16x16x32_bf16 v[40:43],a[236:239],v[64:67],v[40:43]
s_xor_b32 s64,s64,65536
ds_read_b128 v[60:63],v2 offset:53248
ds_write_b32 v13,v112 offset:2048
v_mfma_f32_16x16x32_bf16 v[36:39],a[240:243],v[68:71],v[36:39]
s_sub_u32 s69,s66,1048576
ds_read_b128 v[64:67],v2 offset:54272
v_mul_f32 v32,v32,v80
v_mfma_f32_16x16x32_bf16 v[40:43],a[244:247],v[68:71],v[40:43]
s_cmp_lt_u32 s69,3145728
v_rcp_f32 v83,v83
v_mfma_f32_16x16x32_bf16 v[36:39],a[248:251],v[72:75],v[36:39]
buffer_load_dwordx4 v0,s[36:39],s66 offen offset:1024 sc1 lds
v_mfma_f32_16x16x32_bf16 v[40:43],a[252:255],v[72:75],v[40:43]
s_waitcnt lgkmcnt(3)
ds_read_b128 v[68:71],v2 offset:55296
v_mul_f32 v33,v33,v81
v_mfma_f32_16x16x32_bf16 v[36:39],a[128:131],v[44:47],v[36:39]
s_waitcnt vmcnt(7) lgkmcnt(2)
ds_read_b128 v[72:75],v2 offset:56320
v_xor_b32 v2,65536,v2
v_mfma_f32_16x16x32_bf16 v[40:43],a[132:135],v[44:47],v[40:43]
s_barrier
v_mfma_f32_16x16x32_bf16 v[36:39],a[136:139],v[48:51],v[36:39]
s_cselect_b32 s89,4194304,s89
ds_read_b128 v[116:119],v14 offset:2048
ds_read_b128 v[120:123],v14 offset:2064
v_mfma_f32_16x16x32_bf16 v[40:43],a[140:143],v[48:51],v[40:43]
s_add_u32 s74,s53,s55
ds_read_b128 v[44:47],v2 offset:8192
v_mul_f32 v34,v34,v82
v_mfma_f32_16x16x32_bf16 v[36:39],a[144:147],v[52:55],v[36:39]
buffer_load_dwordx4 v0,s[36:39],s66 offen offset:2048 sc1 lds
v_mfma_f32_16x16x32_bf16 v[40:43],a[148:151],v[52:55],v[40:43]
s_cmp_eq_u32 s74,s56
ds_read_b128 v[48:51],v2 offset:9216
v_mul_f32 v35,v35,v83
v_mfma_f32_16x16x32_bf16 v[36:39],a[152:155],v[56:59],v[36:39]
s_waitcnt lgkmcnt(2)
ds_read_b128 v[52:55],v2 offset:10240
v_cvt_pk_bf16_f32 v86,v32,v33
v_mfma_f32_16x16x32_bf16 v[40:43],a[156:159],v[56:59],v[40:43]
v_add_f32 v116,v116,v117
v_add_f32 v118,v118,v119
v_mfma_f32_16x16x32_bf16 v[36:39],a[160:163],v[60:63],v[36:39]
buffer_load_dwordx4 v0,s[36:39],s66 offen offset:3072 sc1 lds
v_mfma_f32_16x16x32_bf16 v[40:43],a[164:167],v[60:63],v[40:43]
s_cselect_b32 s74,s57,s74
ds_read_b128 v[56:59],v2 offset:11264
v_add_f32 v116,v116,v118
v_mfma_f32_16x16x32_bf16 v[36:39],a[168:171],v[64:67],v[36:39]
s_cmp_eq_u32 s74,s58
ds_read_b128 v[60:63],v2 offset:12288
v_mul_f32 v96,s75,v116
v_mfma_f32_16x16x32_bf16 v[40:43],a[172:175],v[64:67],v[40:43]
s_cselect_b32 s74,s59,s74
v_cvt_pk_bf16_f32 v87,v34,v35
v_add_f32 v120,v120,v121
v_mfma_f32_16x16x32_bf16 v[36:39],a[176:179],v[68:71],v[36:39]
buffer_store_dwordx4 v[84:87],v3,s[36:39],s73 offen offset:0 sc0 sc1
v_add_f32 v122,v122,v123
v_add_f32 v120,v120,v122
v_mul_f32 v120,s75,v120
v_fma_f32 v120,-v96,v96,v120
v_max_f32 v120,0,v120
v_add_f32 v115,v15,v120
v_fma_f32 v120,v15,v115,v120
v_rsq_f32 v115,v115
v_mfma_f32_16x16x32_bf16 v[40:43],a[180:183],v[68:71],v[40:43]
s_mov_b32 s52,s74
ds_read_b128 v[64:67],v2 offset:13312
v_rsq_f32 v116,v120
v_mul_f32_e64 v114,-v96,v115
v_mfma_f32_16x16x32_bf16 v[36:39],a[184:187],v[72:75],v[36:39]
s_waitcnt lgkmcnt(2)
ds_read_b128 v[68:71],v2 offset:14336
buffer_store_dwordx2 v[114:115],v12,s[36:39],s65 offen offset:0 sc0 sc1
v_xor_b32 v8,16384,v8
v_mul_f32 v97,s76,v116
v_mul_f32 v98,v116,v116
v_mul_f32 v99,v120,v116
v_mul_f32 v98,s77,v98
v_mul_f32 v98,v98,v97
s_cmp_lt_u32 s67,4194304
v_mfma_f32_16x16x32_bf16 v[40:43],a[188:191],v[72:75],v[40:43]
s_cbranch_scc1 Lg1_a0w2_%=
s_cmp_ge_u32 s48,4194304
s_cselect_b64 s[90:91],s[94:95],s[90:91]
s_cselect_b64 s[92:93],s[96:97],s[92:93]
s_cmp_lt_u32 s67,s59
s_cbranch_scc1 Lg1_d0w2_%=
s_branch Lg1_slow0_%=
Lg1_a0w3_%=:
v_mfma_f32_16x16x32_bf16 v[28:31],a[96:99],v[44:47],0
s_add_u32 m0,s62,16384
ds_read_b128 v[72:75],v2 offset:11264
s_lshr_b32 s73,s47,0
v_mfma_f32_16x16x32_bf16 v[32:35],a[100:103],v[44:47],0
s_lshr_b32 s72,s66,6)"
R"(
s_lshr_b32 s71,s62,2
s_cmp_lt_u32 s66,4194304
v_mfma_f32_16x16x32_bf16 v[104:107],v[100:103],v[44:47],0
s_cselect_b32 s72,s59,s72
v_mfma_f32_16x16x32_bf16 v[108:111],v[44:47],v[44:47],0
v_fma_f32 v36,-v96,v20,v36
v_fma_f32 v37,-v96,v21,v37
v_mfma_f32_16x16x32_bf16 v[28:31],a[104:107],v[48:51],v[28:31]
buffer_load_dwordx4 v1,s[36:39],s66 offen offset:0 sc1 lds
v_mfma_f32_16x16x32_bf16 v[32:35],a[108:111],v[48:51],v[32:35]
ds_read_b128 v[44:47],v2 offset:4096
v_fma_f32 v38,-v96,v22,v38
v_mfma_f32_16x16x32_bf16 v[104:107],v[100:103],v[48:51],v[104:107]
v_fma_f32 v39,-v96,v23,v39
v_mul_f32 v76,v36,v36
v_mfma_f32_16x16x32_bf16 v[108:111],v[48:51],v[48:51],v[108:111]
v_mul_f32 v77,v37,v37
v_mul_f32 v78,v38,v38
v_mfma_f32_16x16x32_bf16 v[28:31],a[112:115],v[52:55],v[28:31]
buffer_load_dwordx4 v1,s[36:39],s66 offen offset:1024 sc1 lds
v_mfma_f32_16x16x32_bf16 v[32:35],a[116:119],v[52:55],v[32:35]
ds_read_b128 v[48:51],v2 offset:5120
v_mul_f32 v79,v39,v39
v_mfma_f32_16x16x32_bf16 v[104:107],v[100:103],v[52:55],v[104:107]
v_fma_f32 v76,v98,v76,v97
v_mul_f32 v76,v36,v76
v_mfma_f32_16x16x32_bf16 v[108:111],v[52:55],v[52:55],v[108:111]
v_exp_f32 v76,v76
v_mfma_f32_16x16x32_bf16 v[28:31],a[120:123],v[56:59],v[28:31]
buffer_load_dwordx4 v1,s[36:39],s66 offen offset:2048 sc1 lds
v_mfma_f32_16x16x32_bf16 v[32:35],a[124:127],v[56:59],v[32:35]
ds_read_b128 v[52:55],v2 offset:6144
v_fma_f32 v76,v76,v99,v99
v_mfma_f32_16x16x32_bf16 v[104:107],v[100:103],v[56:59],v[104:107]
v_rcp_f32 v76,v76
v_mfma_f32_16x16x32_bf16 v[108:111],v[56:59],v[56:59],v[108:111]
s_waitcnt lgkmcnt(3)
v_fma_f32 v77,v98,v77,v97
v_mul_f32 v77,v37,v77
v_mfma_f32_16x16x32_bf16 v[28:31],a[64:67],v[60:63],v[28:31]
buffer_load_dwordx4 v1,s[36:39],s66 offen offset:3072 sc1 lds
v_mfma_f32_16x16x32_bf16 v[32:35],a[68:71],v[60:63],v[32:35]
s_add_u32 m0,s71,s88
ds_read_b128 v[56:59],v2 offset:7168
v_fma_f32 v78,v98,v78,v97
v_mfma_f32_16x16x32_bf16 v[28:31],a[72:75],v[64:67],v[28:31]
buffer_load_dword v10,s[36:39],s72 offen offset:0 sc1 lds
v_mfma_f32_16x16x32_bf16 v[32:35],a[76:79],v[64:67],v[32:35]
s_xor_b32 s87,s64,65536
ds_read_b128 v[60:63],v2 offset:0
v_mul_f32 v78,v38,v78
v_mfma_f32_16x16x32_bf16 v[28:31],a[80:83],v[68:71],v[28:31]
s_cmp_ge_u32 s66,s59
ds_read_b128 v[64:67],v2 offset:1024
v_fma_f32 v79,v98,v79,v97
v_mfma_f32_16x16x32_bf16 v[32:35],a[84:87],v[68:71],v[32:35]
s_cselect_b32 s86,s49,s50
v_exp_f32 v77,v77
v_mfma_f32_16x16x32_bf16 v[28:31],a[88:91],v[72:75],v[28:31]
s_cselect_b32 s87,s87,s63
ds_read_b128 v[68:71],v2 offset:2048
v_mul_f32 v79,v39,v79
v_mfma_f32_16x16x32_bf16 v[32:35],a[92:95],v[72:75],v[32:35]
s_waitcnt lgkmcnt(3)
v_exp_f32 v78,v78
v_mfma_f32_16x16x32_bf16 v[28:31],a[32:35],v[44:47],v[28:31]
s_waitcnt vmcnt(11)
ds_read_b128 v[72:75],v2 offset:3072
v_fma_f32 v77,v77,v99,v99
v_mfma_f32_16x16x32_bf16 v[32:35],a[36:39],v[44:47],v[32:35]
s_barrier
v_mfma_f32_16x16x32_bf16 v[28:31],a[40:43],v[48:51],v[28:31]
s_mov_b32 s62,s87
ds_read_b128 v[44:47],v2 offset:28672
v_fma_f32 v78,v78,v99,v99
v_mfma_f32_16x16x32_bf16 v[32:35],a[44:47],v[48:51],v[32:35]
s_cmp_lt_u32 s86,s89
v_exp_f32 v79,v79
v_mfma_f32_16x16x32_bf16 v[28:31],a[48:51],v[52:55],v[28:31]
s_mov_b32 m0,s62
ds_read_b128 v[48:51],v2 offset:29696
v_fma_f32 v79,v79,v99,v99
v_mfma_f32_16x16x32_bf16 v[32:35],a[52:55],v[52:55],v[32:35]
v_rcp_f32 v77,v77
v_mfma_f32_16x16x32_bf16 v[28:31],a[56:59],v[56:59],v[28:31]
ds_read_b128 v[52:55],v2 offset:30720
v_mul_f32 v36,v36,v76
v_mfma_f32_16x16x32_bf16 v[32:35],a[60:63],v[56:59],v[32:35]
s_waitcnt lgkmcnt(3)
v_rcp_f32 v78,v78
v_mfma_f32_16x16x32_bf16 v[28:31],a[0:3],v[60:63],v[28:31]
ds_read_b128 v[56:59],v2 offset:31744
v_mul_f32 v37,v37,v77
v_mfma_f32_16x16x32_bf16 v[32:35],a[4:7],v[60:63],v[32:35]
v_rcp_f32 v79,v79
v_mfma_f32_16x16x32_bf16 v[28:31],a[8:11],v[64:67],v[28:31]
ds_read_b128 v[60:63],v2 offset:24576
v_mul_f32 v38,v38,v78
v_mfma_f32_16x16x32_bf16 v[32:35],a[12:15],v[64:67],v[32:35]
v_mul_f32 v39,v39,v79
v_cvt_pk_bf16_f32 v84,v36,v37
v_mfma_f32_16x16x32_bf16 v[28:31],a[16:19],v[68:71],v[28:31]
ds_read_b128 v[64:67],v2 offset:25600
v_cvt_pk_bf16_f32 v85,v38,v39
v_mfma_f32_16x16x32_bf16 v[32:35],a[20:23],v[68:71],v[32:35]
v_fma_f32 v40,-v96,v24,v40
v_fma_f32 v41,-v96,v25,v41
v_mfma_f32_16x16x32_bf16 v[28:31],a[24:27],v[72:75],v[28:31]
ds_read_b128 v[68:71],v2 offset:26624
v_fma_f32 v42,-v96,v26,v42
v_mfma_f32_16x16x32_bf16 v[32:35],a[28:31],v[72:75],v[32:35]
s_waitcnt lgkmcnt(3)
v_fma_f32 v43,-v96,v27,v43
v_mul_f32 v80,v40,v40
v_mfma_f32_16x16x32_bf16 v[28:31],a[224:227],v[44:47],v[28:31]
ds_read_b128 v[72:75],v2 offset:27648
v_mul_f32 v81,v41,v41
v_mfma_f32_16x16x32_bf16 v[32:35],a[228:231],v[44:47],v[32:35]
v_mul_f32 v82,v42,v42
v_mul_f32 v83,v43,v43
v_mfma_f32_16x16x32_bf16 v[104:107],v[100:103],v[44:47],v[104:107]
v_fma_f32 v80,v98,v80,v97
v_mul_f32 v80,v40,v80
v_mfma_f32_16x16x32_bf16 v[108:111],v[44:47],v[44:47],v[108:111]
v_exp_f32 v80,v80
v_mfma_f32_16x16x32_bf16 v[28:31],a[232:235],v[48:51],v[28:31]
ds_read_b128 v[44:47],v2 offset:20480
v_fma_f32 v80,v80,v99,v99
v_mfma_f32_16x16x32_bf16 v[32:35],a[236:239],v[48:51],v[32:35]
v_rcp_f32 v80,v80
v_mfma_f32_16x16x32_bf16 v[104:107],v[100:103],v[48:51],v[104:107]
s_nop 0
v_fma_f32 v81,v98,v81,v97
v_mul_f32 v81,v41,v81
v_mfma_f32_16x16x32_bf16 v[108:111],v[48:51],v[48:51],v[108:111]
s_barrier
v_mfma_f32_16x16x32_bf16 v[28:31],a[240:243],v[52:55],v[28:31]
ds_read_b128 v[48:51],v2 offset:21504
ds_read_b32 v16,v7 offset:0
v_mfma_f32_16x16x32_bf16 v[32:35],a[244:247],v[52:55],v[32:35]
v_exp_f32 v81,v81
v_mfma_f32_16x16x32_bf16 v[104:107],v[100:103],v[52:55],v[104:107]
v_fma_f32 v81,v81,v99,v99
v_fma_f32 v82,v98,v82,v97
v_mfma_f32_16x16x32_bf16 v[108:111],v[52:55],v[52:55],v[108:111]
v_rcp_f32 v81,v81
v_mfma_f32_16x16x32_bf16 v[28:31],a[248:251],v[56:59],v[28:31]
s_waitcnt lgkmcnt(0)
ds_read_b128 v[52:55],v2 offset:22528
v_mul_f32 v82,v42,v82
v_mfma_f32_16x16x32_bf16 v[32:35],a[252:255],v[56:59],v[32:35]
v_cmp_eq_u32 vcc,s60,v16
v_exp_f32 v82,v82
v_fma_f32 v83,v98,v83,v97
v_mfma_f32_16x16x32_bf16 v[104:107],v[100:103],v[56:59],v[104:107]
s_cselect_b32 vcc_lo,-1,vcc_lo
v_mul_f32 v83,v43,v83
v_fma_f32 v82,v82,v99,v99
v_mfma_f32_16x16x32_bf16 v[108:111],v[56:59],v[56:59],v[108:111]
v_exp_f32 v83,v83
v_mfma_f32_16x16x32_bf16 v[28:31],a[192:195],v[60:63],v[28:31]
s_cmp_eq_u32 vcc_lo,-1
ds_read_b128 v[56:59],v2 offset:23552
v_fma_f32 v83,v83,v99,v99
v_mfma_f32_16x16x32_bf16 v[32:35],a[196:199],v[60:63],v[32:35]
s_cselect_b32 s67,s86,s59
v_rcp_f32 v82,v82
v_mfma_f32_16x16x32_bf16 v[28:31],a[200:203],v[64:67],v[28:31]
s_lshr_b32 s70,s46,5
v_cndmask_b32 v112,v108,v109,s[82:83]
v_cndmask_b32 v113,v110,v111,s[82:83]
v_cndmask_b32 v112,v112,v113,s[84:85]
v_cndmask_b32 v112,v104,v112,s[98:99]
v_mfma_f32_16x16x32_bf16 v[32:35],a[204:207],v[64:67],v[32:35]
s_xor_b32 s63,s63,65536
ds_read_b128 v[60:63],v2 offset:16384
ds_write_b32 v13,v112 offset:0
buffer_load_dwordx4 v0,s[36:39],s67 offen offset:0 sc1 lds
v_mfma_f32_16x16x32_bf16 v[28:31],a[208:211],v[68:71],v[28:31]
s_sub_u32 s69,s67,1048576
ds_read_b128 v[64:67],v2 offset:17408
v_mul_f32 v40,v40,v80
v_mfma_f32_16x16x32_bf16 v[32:35],a[212:215],v[68:71],v[32:35]
s_cmp_lt_u32 s69,3145728
v_rcp_f32 v83,v83
v_mfma_f32_16x16x32_bf16 v[28:31],a[216:219],v[72:75],v[28:31]
s_cselect_b32 s89,4194304,s89
ds_read_b128 v[68:71],v2 offset:18432
v_mul_f32 v41,v41,v81
v_mfma_f32_16x16x32_bf16 v[32:35],a[220:223],v[72:75],v[32:35]
s_waitcnt lgkmcnt(4)
buffer_load_dwordx4 v0,s[36:39],s67 offen offset:1024 sc1 lds
v_mfma_f32_16x16x32_bf16 v[28:31],a[160:163],v[44:47],v[28:31]
s_waitcnt vmcnt(7) lgkmcnt(2)
ds_read_b128 v[72:75],v2 offset:19456
v_mul_f32 v42,v42,v82
v_mfma_f32_16x16x32_bf16 v[32:35],a[164:167],v[44:47],v[32:35]
s_barrier
v_mfma_f32_16x16x32_bf16 v[28:31],a[168:171],v[48:51],v[28:31]
buffer_store_dword v5,v4,s[36:39],s70 offen offset:0 sc0 sc1)"
R"(
ds_read_b128 v[116:119],v14 offset:0
ds_read_b128 v[120:123],v14 offset:16
ds_read_b128 v[44:47],v2 offset:45056
v_mfma_f32_16x16x32_bf16 v[32:35],a[172:175],v[48:51],v[32:35]
s_add_u32 s53,s52,s55
v_mul_f32 v43,v43,v83
v_cvt_pk_bf16_f32 v86,v40,v41
v_mfma_f32_16x16x32_bf16 v[28:31],a[176:179],v[52:55],v[28:31]
s_cmp_eq_u32 s53,s56
ds_read_b128 v[48:51],v2 offset:46080
v_cvt_pk_bf16_f32 v87,v42,v43
v_mfma_f32_16x16x32_bf16 v[32:35],a[180:183],v[52:55],v[32:35]
buffer_load_dwordx4 v0,s[36:39],s67 offen offset:2048 sc1 lds
v_mfma_f32_16x16x32_bf16 v[28:31],a[184:187],v[56:59],v[28:31]
s_waitcnt lgkmcnt(2)
ds_read_b128 v[52:55],v2 offset:47104
v_add_f32 v116,v116,v117
v_mfma_f32_16x16x32_bf16 v[32:35],a[188:191],v[56:59],v[32:35]
v_add_f32 v118,v118,v119
v_add_f32 v116,v116,v118
v_mfma_f32_16x16x32_bf16 v[28:31],a[128:131],v[60:63],v[28:31]
s_cselect_b32 s53,s57,s53
ds_read_b128 v[56:59],v2 offset:48128
v_mul_f32 v92,s75,v116
v_mfma_f32_16x16x32_bf16 v[32:35],a[132:135],v[60:63],v[32:35]
buffer_load_dwordx4 v0,s[36:39],s67 offen offset:3072 sc1 lds
v_mfma_f32_16x16x32_bf16 v[28:31],a[136:139],v[64:67],v[28:31]
s_cmp_eq_u32 s53,s58
ds_read_b128 v[60:63],v2 offset:40960
v_add_f32 v120,v120,v121
v_mfma_f32_16x16x32_bf16 v[32:35],a[140:143],v[64:67],v[32:35]
s_cselect_b32 s53,s59,s53
v_add_f32 v122,v122,v123
v_add_f32 v120,v120,v122
v_mfma_f32_16x16x32_bf16 v[28:31],a[144:147],v[68:71],v[28:31]
s_cmp_ge_u32 s66,s59
ds_read_b128 v[64:67],v2 offset:41984
v_mul_f32 v120,s75,v120
v_mfma_f32_16x16x32_bf16 v[32:35],a[148:151],v[68:71],v[32:35]
buffer_store_dwordx4 v[84:87],v3,s[36:39],s73 offen offset:0 sc0 sc1
v_fma_f32 v120,-v92,v92,v120
v_max_f32 v120,0,v120
v_add_f32 v115,v15,v120
v_fma_f32 v120,v15,v115,v120
v_rsq_f32 v116,v120
v_mfma_f32_16x16x32_bf16 v[28:31],a[152:155],v[72:75],v[28:31]
s_waitcnt lgkmcnt(2)
ds_read_b128 v[68:71],v2 offset:43008
v_mul_f32 v93,s76,v116
v_mul_f32 v94,v116,v116
v_mul_f32 v95,v120,v116
v_mul_f32 v94,s77,v94
v_mul_f32 v94,v94,v93
v_mfma_f32_16x16x32_bf16 v[32:35],a[156:159],v[72:75],v[32:35]
s_cbranch_scc1 Lg1_slow1_%=
Lg1_a1w3_%=:
v_mfma_f32_16x16x32_bf16 v[36:39],a[96:99],v[44:47],0
s_add_u32 m0,s62,16384
ds_read_b128 v[72:75],v2 offset:44032
s_lshr_b32 s73,s48,0
v_mfma_f32_16x16x32_bf16 v[40:43],a[100:103],v[44:47],0
s_lshr_b32 s72,s67,6
s_lshr_b32 s71,s62,2
s_cmp_lt_u32 s67,4194304
v_mfma_f32_16x16x32_bf16 v[104:107],v[100:103],v[44:47],0
s_cselect_b32 s72,s59,s72
s_mov_b64 s[44:45],s[46:47]
v_mfma_f32_16x16x32_bf16 v[108:111],v[44:47],v[44:47],0
v_fma_f32 v28,-v92,v20,v28
v_fma_f32 v29,-v92,v21,v29
v_mfma_f32_16x16x32_bf16 v[36:39],a[104:107],v[48:51],v[36:39]
buffer_load_dwordx4 v1,s[36:39],s67 offen offset:0 sc1 lds
v_mfma_f32_16x16x32_bf16 v[40:43],a[108:111],v[48:51],v[40:43]
ds_read_b128 v[44:47],v2 offset:36864
v_fma_f32 v30,-v92,v22,v30
v_mfma_f32_16x16x32_bf16 v[104:107],v[100:103],v[48:51],v[104:107]
v_fma_f32 v31,-v92,v23,v31
v_mul_f32 v76,v28,v28
v_mfma_f32_16x16x32_bf16 v[108:111],v[48:51],v[48:51],v[108:111]
v_mul_f32 v77,v29,v29
v_mul_f32 v78,v30,v30
v_mfma_f32_16x16x32_bf16 v[36:39],a[112:115],v[52:55],v[36:39]
buffer_load_dwordx4 v1,s[36:39],s67 offen offset:1024 sc1 lds
v_mfma_f32_16x16x32_bf16 v[40:43],a[116:119],v[52:55],v[40:43]
ds_read_b128 v[48:51],v2 offset:37888
v_mul_f32 v79,v31,v31
v_mfma_f32_16x16x32_bf16 v[104:107],v[100:103],v[52:55],v[104:107]
v_fma_f32 v76,v94,v76,v93
v_mul_f32 v76,v28,v76
v_mfma_f32_16x16x32_bf16 v[108:111],v[52:55],v[52:55],v[108:111]
v_exp_f32 v76,v76
v_mfma_f32_16x16x32_bf16 v[36:39],a[120:123],v[56:59],v[36:39]
buffer_load_dwordx4 v1,s[36:39],s67 offen offset:2048 sc1 lds
v_mfma_f32_16x16x32_bf16 v[40:43],a[124:127],v[56:59],v[40:43]
ds_read_b128 v[52:55],v2 offset:38912
v_fma_f32 v76,v76,v95,v95
v_mfma_f32_16x16x32_bf16 v[104:107],v[100:103],v[56:59],v[104:107]
v_rcp_f32 v76,v76
v_mfma_f32_16x16x32_bf16 v[108:111],v[56:59],v[56:59],v[108:111]
s_waitcnt lgkmcnt(3)
v_fma_f32 v77,v94,v77,v93
v_mul_f32 v77,v29,v77
v_mfma_f32_16x16x32_bf16 v[36:39],a[64:67],v[60:63],v[36:39]
buffer_load_dwordx4 v1,s[36:39],s67 offen offset:3072 sc1 lds
v_mfma_f32_16x16x32_bf16 v[40:43],a[68:71],v[60:63],v[40:43]
s_add_u32 m0,s71,s88
ds_read_b128 v[56:59],v2 offset:39936
v_fma_f32 v78,v94,v78,v93
v_mfma_f32_16x16x32_bf16 v[36:39],a[72:75],v[64:67],v[36:39]
buffer_load_dword v10,s[36:39],s72 offen offset:0 sc1 lds
v_mfma_f32_16x16x32_bf16 v[40:43],a[76:79],v[64:67],v[40:43]
s_xor_b32 s87,s63,65536
ds_read_b128 v[60:63],v2 offset:32768
v_mul_f32 v78,v30,v78
v_mfma_f32_16x16x32_bf16 v[36:39],a[80:83],v[68:71],v[36:39]
s_cmp_ge_u32 s67,s59
ds_read_b128 v[64:67],v2 offset:33792
v_fma_f32 v79,v94,v79,v93
v_mfma_f32_16x16x32_bf16 v[40:43],a[84:87],v[68:71],v[40:43]
s_cselect_b32 s86,s50,s51
v_exp_f32 v77,v77
v_mfma_f32_16x16x32_bf16 v[36:39],a[88:91],v[72:75],v[36:39]
s_cselect_b32 s87,s87,s64
ds_read_b128 v[68:71],v2 offset:34816
v_mul_f32 v79,v31,v79
v_mfma_f32_16x16x32_bf16 v[40:43],a[92:95],v[72:75],v[40:43]
s_waitcnt lgkmcnt(3)
v_exp_f32 v78,v78
v_mfma_f32_16x16x32_bf16 v[36:39],a[32:35],v[44:47],v[36:39]
s_waitcnt vmcnt(11)
ds_read_b128 v[72:75],v2 offset:35840
v_fma_f32 v77,v77,v95,v95
v_mfma_f32_16x16x32_bf16 v[40:43],a[36:39],v[44:47],v[40:43]
s_barrier
v_mfma_f32_16x16x32_bf16 v[36:39],a[40:43],v[48:51],v[36:39]
s_mov_b32 s62,s87
ds_read_b128 v[44:47],v2 offset:61440
v_fma_f32 v78,v78,v95,v95
v_mfma_f32_16x16x32_bf16 v[40:43],a[44:47],v[48:51],v[40:43]
s_cmp_lt_u32 s86,s89
v_exp_f32 v79,v79
v_mfma_f32_16x16x32_bf16 v[36:39],a[48:51],v[52:55],v[36:39]
s_mov_b32 m0,s62
ds_read_b128 v[48:51],v2 offset:62464
v_fma_f32 v79,v79,v95,v95
v_mfma_f32_16x16x32_bf16 v[40:43],a[52:55],v[52:55],v[40:43]
v_rcp_f32 v77,v77
v_mfma_f32_16x16x32_bf16 v[36:39],a[56:59],v[56:59],v[36:39]
ds_read_b128 v[52:55],v2 offset:63488
v_mul_f32 v28,v28,v76
v_mfma_f32_16x16x32_bf16 v[40:43],a[60:63],v[56:59],v[40:43]
s_waitcnt lgkmcnt(3)
v_rcp_f32 v78,v78
v_mfma_f32_16x16x32_bf16 v[36:39],a[0:3],v[60:63],v[36:39]
ds_read_b128 v[56:59],v2 offset:64512
v_mul_f32 v29,v29,v77
v_mfma_f32_16x16x32_bf16 v[40:43],a[4:7],v[60:63],v[40:43]
v_rcp_f32 v79,v79
v_mfma_f32_16x16x32_bf16 v[36:39],a[8:11],v[64:67],v[36:39]
ds_read_b128 v[60:63],v2 offset:57344
v_mul_f32 v30,v30,v78
v_mfma_f32_16x16x32_bf16 v[40:43],a[12:15],v[64:67],v[40:43]
v_mul_f32 v31,v31,v79
v_cvt_pk_bf16_f32 v84,v28,v29
v_mfma_f32_16x16x32_bf16 v[36:39],a[16:19],v[68:71],v[36:39]
ds_read_b128 v[64:67],v2 offset:58368
v_cvt_pk_bf16_f32 v85,v30,v31
v_mfma_f32_16x16x32_bf16 v[40:43],a[20:23],v[68:71],v[40:43]
v_fma_f32 v32,-v92,v24,v32
v_fma_f32 v33,-v92,v25,v33
v_mfma_f32_16x16x32_bf16 v[36:39],a[24:27],v[72:75],v[36:39]
ds_read_b128 v[68:71],v2 offset:59392
v_fma_f32 v34,-v92,v26,v34
v_mfma_f32_16x16x32_bf16 v[40:43],a[28:31],v[72:75],v[40:43]
s_waitcnt lgkmcnt(3)
v_fma_f32 v35,-v92,v27,v35
v_mul_f32 v80,v32,v32
v_mfma_f32_16x16x32_bf16 v[36:39],a[224:227],v[44:47],v[36:39]
ds_read_b128 v[72:75],v2 offset:60416
v_mul_f32 v81,v33,v33
v_mfma_f32_16x16x32_bf16 v[40:43],a[228:231],v[44:47],v[40:43]
v_mul_f32 v82,v34,v34
v_mul_f32 v83,v35,v35
v_mfma_f32_16x16x32_bf16 v[104:107],v[100:103],v[44:47],v[104:107]
v_fma_f32 v80,v94,v80,v93
v_mul_f32 v80,v32,v80
v_mfma_f32_16x16x32_bf16 v[108:111],v[44:47],v[44:47],v[108:111]
v_exp_f32 v80,v80
v_mfma_f32_16x16x32_bf16 v[36:39],a[232:235],v[48:51],v[36:39]
ds_read_b128 v[44:47],v2 offset:53248
v_fma_f32 v80,v80,v95,v95
v_mfma_f32_16x16x32_bf16 v[40:43],a[236:239],v[48:51],v[40:43]
v_rcp_f32 v80,v80
v_mfma_f32_16x16x32_bf16 v[104:107],v[100:103],v[48:51],v[104:107]
s_nop 0
v_fma_f32 v81,v94,v81,v93
v_mul_f32 v81,v33,v81
v_mfma_f32_16x16x32_bf16 v[108:111],v[48:51],v[48:51],v[108:111]
s_barrier
v_mfma_f32_16x16x32_bf16 v[36:39],a[240:243],v[52:55],v[36:39])"
R"(
ds_read_b128 v[48:51],v2 offset:54272
ds_read_b32 v16,v7 offset:256
v_mfma_f32_16x16x32_bf16 v[40:43],a[244:247],v[52:55],v[40:43]
v_exp_f32 v81,v81
v_mfma_f32_16x16x32_bf16 v[104:107],v[100:103],v[52:55],v[104:107]
v_fma_f32 v81,v81,v95,v95
v_fma_f32 v82,v94,v82,v93
v_mfma_f32_16x16x32_bf16 v[108:111],v[52:55],v[52:55],v[108:111]
v_rcp_f32 v81,v81
v_mfma_f32_16x16x32_bf16 v[36:39],a[248:251],v[56:59],v[36:39]
s_waitcnt lgkmcnt(0)
ds_read_b128 v[52:55],v2 offset:55296
v_mul_f32 v82,v34,v82
v_mfma_f32_16x16x32_bf16 v[40:43],a[252:255],v[56:59],v[40:43]
v_cmp_eq_u32 vcc,s60,v16
v_exp_f32 v82,v82
v_fma_f32 v83,v94,v83,v93
v_mfma_f32_16x16x32_bf16 v[104:107],v[100:103],v[56:59],v[104:107]
s_cselect_b32 vcc_lo,-1,vcc_lo
v_mul_f32 v83,v35,v83
v_fma_f32 v82,v82,v95,v95
v_mfma_f32_16x16x32_bf16 v[108:111],v[56:59],v[56:59],v[108:111]
v_exp_f32 v83,v83
v_mfma_f32_16x16x32_bf16 v[36:39],a[192:195],v[60:63],v[36:39]
s_cmp_eq_u32 vcc_lo,-1
ds_read_b128 v[56:59],v2 offset:56320
v_fma_f32 v83,v83,v95,v95
v_mfma_f32_16x16x32_bf16 v[40:43],a[196:199],v[60:63],v[40:43]
s_cselect_b32 s66,s86,s59
v_rcp_f32 v82,v82
v_mfma_f32_16x16x32_bf16 v[36:39],a[200:203],v[64:67],v[36:39]
s_lshr_b32 s70,s47,5
v_cndmask_b32 v112,v108,v109,s[82:83]
v_cndmask_b32 v113,v110,v111,s[82:83]
v_cndmask_b32 v112,v112,v113,s[84:85]
v_cndmask_b32 v112,v104,v112,s[98:99]
v_mfma_f32_16x16x32_bf16 v[40:43],a[204:207],v[64:67],v[40:43]
s_xor_b32 s64,s64,65536
ds_read_b128 v[60:63],v2 offset:49152
ds_write_b32 v13,v112 offset:2048
buffer_load_dwordx4 v0,s[36:39],s66 offen offset:0 sc1 lds
v_mfma_f32_16x16x32_bf16 v[36:39],a[208:211],v[68:71],v[36:39]
s_sub_u32 s69,s66,1048576
ds_read_b128 v[64:67],v2 offset:50176
v_mul_f32 v32,v32,v80
v_mfma_f32_16x16x32_bf16 v[40:43],a[212:215],v[68:71],v[40:43]
s_cmp_lt_u32 s69,3145728
v_rcp_f32 v83,v83
v_mfma_f32_16x16x32_bf16 v[36:39],a[216:219],v[72:75],v[36:39]
s_cselect_b32 s89,4194304,s89
ds_read_b128 v[68:71],v2 offset:51200
v_mul_f32 v33,v33,v81
v_mfma_f32_16x16x32_bf16 v[40:43],a[220:223],v[72:75],v[40:43]
s_waitcnt lgkmcnt(4)
buffer_load_dwordx4 v0,s[36:39],s66 offen offset:1024 sc1 lds
v_mfma_f32_16x16x32_bf16 v[36:39],a[160:163],v[44:47],v[36:39]
s_waitcnt vmcnt(7) lgkmcnt(2)
ds_read_b128 v[72:75],v2 offset:52224
v_xor_b32 v2,65536,v2
v_mfma_f32_16x16x32_bf16 v[40:43],a[164:167],v[44:47],v[40:43]
s_barrier
v_mfma_f32_16x16x32_bf16 v[36:39],a[168:171],v[48:51],v[36:39]
buffer_store_dword v5,v4,s[36:39],s70 offen offset:0 sc0 sc1
ds_read_b128 v[116:119],v14 offset:2048
ds_read_b128 v[120:123],v14 offset:2064
ds_read_b128 v[44:47],v2 offset:12288
v_mfma_f32_16x16x32_bf16 v[40:43],a[172:175],v[48:51],v[40:43]
s_add_u32 s74,s53,s55
v_mul_f32 v34,v34,v82
v_mul_f32 v35,v35,v83
v_mfma_f32_16x16x32_bf16 v[36:39],a[176:179],v[52:55],v[36:39]
s_cmp_eq_u32 s74,s56
ds_read_b128 v[48:51],v2 offset:13312
v_cvt_pk_bf16_f32 v86,v32,v33
v_mfma_f32_16x16x32_bf16 v[40:43],a[180:183],v[52:55],v[40:43]
buffer_load_dwordx4 v0,s[36:39],s66 offen offset:2048 sc1 lds
v_mfma_f32_16x16x32_bf16 v[36:39],a[184:187],v[56:59],v[36:39]
s_waitcnt lgkmcnt(2)
ds_read_b128 v[52:55],v2 offset:14336
v_cvt_pk_bf16_f32 v87,v34,v35
v_mfma_f32_16x16x32_bf16 v[40:43],a[188:191],v[56:59],v[40:43]
v_add_f32 v116,v116,v117
v_add_f32 v118,v118,v119
v_mfma_f32_16x16x32_bf16 v[36:39],a[128:131],v[60:63],v[36:39]
s_cselect_b32 s74,s57,s74
ds_read_b128 v[56:59],v2 offset:15360
v_add_f32 v116,v116,v118
v_mfma_f32_16x16x32_bf16 v[40:43],a[132:135],v[60:63],v[40:43]
buffer_load_dwordx4 v0,s[36:39],s66 offen offset:3072 sc1 lds
v_mfma_f32_16x16x32_bf16 v[36:39],a[136:139],v[64:67],v[36:39]
s_cmp_eq_u32 s74,s58
ds_read_b128 v[60:63],v2 offset:8192
v_mul_f32 v96,s75,v116
v_mfma_f32_16x16x32_bf16 v[40:43],a[140:143],v[64:67],v[40:43]
s_cselect_b32 s74,s59,s74
v_add_f32 v120,v120,v121
v_add_f32 v122,v122,v123
v_mfma_f32_16x16x32_bf16 v[36:39],a[144:147],v[68:71],v[36:39]
s_mov_b64 s[46:47],s[48:49]
ds_read_b128 v[64:67],v2 offset:9216
v_add_f32 v120,v120,v122
v_mfma_f32_16x16x32_bf16 v[40:43],a[148:151],v[68:71],v[40:43]
buffer_store_dwordx4 v[84:87],v3,s[36:39],s73 offen offset:0 sc0 sc1
v_mul_f32 v120,s75,v120
v_fma_f32 v120,-v96,v96,v120
v_max_f32 v120,0,v120
v_add_f32 v115,v15,v120
v_fma_f32 v120,v15,v115,v120
v_rsq_f32 v116,v120
v_mfma_f32_16x16x32_bf16 v[36:39],a[152:155],v[72:75],v[36:39]
s_waitcnt lgkmcnt(2)
s_mov_b64 s[48:49],s[50:51]
s_mov_b64 s[50:51],s[52:53]
ds_read_b128 v[68:71],v2 offset:10240
v_xor_b32 v8,16384,v8
v_mul_f32 v97,s76,v116
v_mul_f32 v98,v116,v116
v_mul_f32 v99,v120,v116
v_mul_f32 v98,s77,v98
v_mul_f32 v98,v98,v97
s_mov_b32 s52,s74
s_cmp_lt_u32 s67,4194304
v_mfma_f32_16x16x32_bf16 v[40:43],a[156:159],v[72:75],v[40:43]
s_cbranch_scc1 Lg1_a0w3_%=
s_cmp_ge_u32 s48,4194304
s_cselect_b64 s[90:91],s[94:95],s[90:91]
s_cselect_b64 s[92:93],s[96:97],s[92:93]
s_cmp_lt_u32 s67,s59
s_cbranch_scc1 Lg1_d0w3_%=
s_branch Lg1_slow0_%=
Lg1_d0w0_%=:
v_mfma_f32_16x16x32_bf16 v[28:31],a[0:3],v[44:47],0
s_add_u32 m0,s62,16384
ds_read_b128 v[72:75],v2 offset:7168
s_lshr_b32 s73,s47,0
v_mfma_f32_16x16x32_bf16 v[32:35],a[4:7],v[44:47],0
buffer_load_dwordx4 v1,s[36:39],s66 offen offset:0 sc1 lds
v_mfma_f32_16x16x32_bf16 v[28:31],a[8:11],v[48:51],v[28:31]
s_lshr_b32 s72,s66,6
ds_read_b128 v[44:47],v2 offset:8192
s_lshr_b32 s71,s62,2
v_mfma_f32_16x16x32_bf16 v[32:35],a[12:15],v[48:51],v[32:35]
s_cmp_lt_u32 s66,4194304
v_fma_f32 v36,-v96,v20,v36
v_fma_f32 v37,-v96,v21,v37
v_mfma_f32_16x16x32_bf16 v[28:31],a[16:19],v[52:55],v[28:31]
s_cselect_b32 s72,s59,s72
ds_read_b128 v[48:51],v2 offset:9216
v_fma_f32 v38,-v96,v22,v38
v_mfma_f32_16x16x32_bf16 v[32:35],a[20:23],v[52:55],v[32:35]
buffer_load_dwordx4 v1,s[36:39],s66 offen offset:1024 sc1 lds
v_mfma_f32_16x16x32_bf16 v[28:31],a[24:27],v[56:59],v[28:31]
ds_read_b128 v[52:55],v2 offset:10240
v_fma_f32 v39,-v96,v23,v39
v_mfma_f32_16x16x32_bf16 v[32:35],a[28:31],v[56:59],v[32:35]
s_waitcnt lgkmcnt(3)
v_mul_f32 v76,v36,v36
v_mul_f32 v77,v37,v37
v_mfma_f32_16x16x32_bf16 v[28:31],a[32:35],v[60:63],v[28:31]
ds_read_b128 v[56:59],v2 offset:11264
v_mul_f32 v78,v38,v38
v_mfma_f32_16x16x32_bf16 v[32:35],a[36:39],v[60:63],v[32:35]
buffer_load_dwordx4 v1,s[36:39],s66 offen offset:2048 sc1 lds
v_mfma_f32_16x16x32_bf16 v[28:31],a[40:43],v[64:67],v[28:31]
ds_read_b128 v[60:63],v2 offset:12288
v_mul_f32 v79,v39,v39
v_mfma_f32_16x16x32_bf16 v[32:35],a[44:47],v[64:67],v[32:35]
v_fma_f32 v76,v98,v76,v97
v_mul_f32 v76,v36,v76
v_mfma_f32_16x16x32_bf16 v[28:31],a[48:51],v[68:71],v[28:31]
ds_read_b128 v[64:67],v2 offset:13312
v_fma_f32 v77,v98,v77,v97
v_mfma_f32_16x16x32_bf16 v[32:35],a[52:55],v[68:71],v[32:35]
buffer_load_dwordx4 v1,s[36:39],s66 offen offset:3072 sc1 lds
v_mfma_f32_16x16x32_bf16 v[28:31],a[56:59],v[72:75],v[28:31]
s_add_u32 m0,s71,s88
ds_read_b128 v[68:71],v2 offset:14336
v_mul_f32 v77,v37,v77
v_mfma_f32_16x16x32_bf16 v[32:35],a[60:63],v[72:75],v[32:35]
s_waitcnt lgkmcnt(3)
buffer_load_dword v10,s[36:39],s72 offen offset:0 sc1 lds
v_mfma_f32_16x16x32_bf16 v[28:31],a[64:67],v[44:47],v[28:31]
s_waitcnt vmcnt(11)
ds_read_b128 v[72:75],v2 offset:15360
v_fma_f32 v78,v98,v78,v97
v_mfma_f32_16x16x32_bf16 v[32:35],a[68:71],v[44:47],v[32:35]
s_barrier
v_mfma_f32_16x16x32_bf16 v[28:31],a[72:75],v[48:51],v[28:31]
s_xor_b32 s87,s64,65536
ds_read_b128 v[44:47],v2 offset:16384
v_fma_f32 v79,v98,v79,v97
v_mfma_f32_16x16x32_bf16 v[32:35],a[76:79],v[48:51],v[32:35]
s_cmp_ge_u32 s66,s59
v_exp_f32 v76,v76
v_mfma_f32_16x16x32_bf16 v[28:31],a[80:83],v[52:55],v[28:31]
s_cselect_b32 s86,s49,s50
ds_read_b128 v[48:51],v2 offset:17408
v_mul_f32 v78,v38,v78
v_mfma_f32_16x16x32_bf16 v[32:35],a[84:87],v[52:55],v[32:35]
s_cselect_b32 s87,s87,s63)"
R"(
v_exp_f32 v77,v77
v_mfma_f32_16x16x32_bf16 v[28:31],a[88:91],v[56:59],v[28:31]
s_lshr_b32 s69,s50,5
ds_read_b128 v[52:55],v2 offset:18432
v_mul_f32 v79,v39,v79
v_mfma_f32_16x16x32_bf16 v[32:35],a[92:95],v[56:59],v[32:35]
s_waitcnt lgkmcnt(3)
v_exp_f32 v78,v78
v_mfma_f32_16x16x32_bf16 v[28:31],a[96:99],v[60:63],v[28:31]
s_cmp_lt_u32 s50,4194304
ds_read_b128 v[56:59],v2 offset:19456
v_fma_f32 v76,v76,v99,v99
v_mfma_f32_16x16x32_bf16 v[32:35],a[100:103],v[60:63],v[32:35]
s_cselect_b32 s68,0xffffcfff,-1
v_exp_f32 v79,v79
v_mfma_f32_16x16x32_bf16 v[28:31],a[104:107],v[64:67],v[28:31]
s_and_b32 s68,s68,s69
ds_read_b128 v[60:63],v2 offset:20480
v_fma_f32 v77,v77,v99,v99
v_mfma_f32_16x16x32_bf16 v[32:35],a[108:111],v[64:67],v[32:35]
s_lshr_b32 s69,s51,5
v_rcp_f32 v76,v76
v_mfma_f32_16x16x32_bf16 v[28:31],a[112:115],v[68:71],v[28:31]
s_cmp_lt_u32 s51,4194304
ds_read_b128 v[64:67],v2 offset:21504
v_fma_f32 v78,v78,v99,v99
v_mfma_f32_16x16x32_bf16 v[32:35],a[116:119],v[68:71],v[32:35]
s_cselect_b32 s70,0xffffcfff,-1
v_rcp_f32 v77,v77
v_mfma_f32_16x16x32_bf16 v[28:31],a[120:123],v[72:75],v[28:31]
s_and_b32 s70,s70,s69
ds_read_b128 v[68:71],v2 offset:22528
v_fma_f32 v79,v79,v99,v99
v_mfma_f32_16x16x32_bf16 v[32:35],a[124:127],v[72:75],v[32:35]
s_waitcnt lgkmcnt(3)
v_rcp_f32 v78,v78
v_mfma_f32_16x16x32_bf16 v[28:31],a[128:131],v[44:47],v[28:31]
s_waitcnt vmcnt(10)
ds_read_b128 v[72:75],v2 offset:23552
v_mul_f32 v36,v36,v76
v_mfma_f32_16x16x32_bf16 v[32:35],a[132:135],v[44:47],v[32:35]
s_barrier
v_mfma_f32_16x16x32_bf16 v[28:31],a[136:139],v[48:51],v[28:31]
s_mov_b32 s62,s87
ds_read_b128 v[44:47],v2 offset:24576
ds_read_b32 v16,v7 offset:0
v_mfma_f32_16x16x32_bf16 v[32:35],a[140:143],v[48:51],v[32:35]
v_rcp_f32 v79,v79
v_mfma_f32_16x16x32_bf16 v[28:31],a[144:147],v[52:55],v[28:31]
ds_read_b128 v[48:51],v2 offset:25600
v_mul_f32 v37,v37,v77
v_mfma_f32_16x16x32_bf16 v[32:35],a[148:151],v[52:55],v[32:35]
v_mul_f32 v38,v38,v78
v_mul_f32 v39,v39,v79
v_mfma_f32_16x16x32_bf16 v[28:31],a[152:155],v[56:59],v[28:31]
s_waitcnt lgkmcnt(1)
ds_read_b128 v[52:55],v2 offset:26624
v_cvt_pk_bf16_f32 v84,v36,v37
v_mfma_f32_16x16x32_bf16 v[32:35],a[156:159],v[56:59],v[32:35]
v_cmp_eq_u32 vcc,s60,v16
v_cvt_pk_bf16_f32 v85,v38,v39
v_fma_f32 v40,-v96,v24,v40
v_fma_f32 v41,-v96,v25,v41
v_mfma_f32_16x16x32_bf16 v[28:31],a[160:163],v[60:63],v[28:31]
s_cmp_eq_u32 vcc_lo,-1
ds_read_b128 v[56:59],v2 offset:27648
v_fma_f32 v42,-v96,v26,v42
v_mfma_f32_16x16x32_bf16 v[32:35],a[164:167],v[60:63],v[32:35]
s_cselect_b32 s67,s86,s59
s_cmp_ge_u32 s67,s59
s_cselect_b32 s68,s68,s70
s_add_u32 m0,s78,256
v_mfma_f32_16x16x32_bf16 v[28:31],a[168:171],v[64:67],v[28:31]
buffer_load_dword v6,s[36:39],s68 offen offset:0 sc1 lds
v_mfma_f32_16x16x32_bf16 v[32:35],a[172:175],v[64:67],v[32:35]
s_mov_b32 m0,s62
ds_read_b128 v[60:63],v2 offset:28672
v_fma_f32 v43,-v96,v27,v43
v_mfma_f32_16x16x32_bf16 v[28:31],a[176:179],v[68:71],v[28:31]
buffer_load_dwordx4 v0,s[36:39],s67 offen offset:0 sc1 lds
v_mfma_f32_16x16x32_bf16 v[32:35],a[180:183],v[68:71],v[32:35]
s_xor_b32 s63,s63,65536
ds_read_b128 v[64:67],v2 offset:29696
v_mul_f32 v80,v40,v40
v_mfma_f32_16x16x32_bf16 v[28:31],a[184:187],v[72:75],v[28:31]
s_add_u32 s53,s52,s55
ds_read_b128 v[68:71],v2 offset:30720
v_mul_f32 v81,v41,v41
v_mfma_f32_16x16x32_bf16 v[32:35],a[188:191],v[72:75],v[32:35]
s_waitcnt lgkmcnt(3)
v_mul_f32 v82,v42,v42
v_mul_f32 v83,v43,v43
v_mfma_f32_16x16x32_bf16 v[28:31],a[192:195],v[44:47],v[28:31]
s_waitcnt vmcnt(7)
ds_read_b128 v[72:75],v2 offset:31744
v_fma_f32 v80,v98,v80,v97
v_mfma_f32_16x16x32_bf16 v[32:35],a[196:199],v[44:47],v[32:35]
s_barrier
v_mfma_f32_16x16x32_bf16 v[28:31],a[200:203],v[48:51],v[28:31]
buffer_load_dwordx4 v0,s[36:39],s67 offen offset:1024 sc1 lds
v_mfma_f32_16x16x32_bf16 v[32:35],a[204:207],v[48:51],v[32:35]
s_cmp_eq_u32 s53,s56
ds_read_b128 v[44:47],v2 offset:32768
ds_read_b128 v[92:95],v8 offset:0
v_mfma_f32_16x16x32_bf16 v[28:31],a[208:211],v[52:55],v[28:31]
s_cselect_b32 s53,s57,s53
ds_read_b128 v[48:51],v2 offset:33792
v_mul_f32 v80,v40,v80
v_mfma_f32_16x16x32_bf16 v[32:35],a[212:215],v[52:55],v[32:35]
s_cmp_eq_u32 s53,s58
v_fma_f32 v81,v98,v81,v97
v_fma_f32 v82,v98,v82,v97
v_fma_f32 v83,v98,v83,v97
v_mul_f32 v81,v41,v81
v_mul_f32 v82,v42,v82
v_mul_f32 v83,v43,v83
v_exp_f32 v80,v80
v_exp_f32 v81,v81
v_exp_f32 v82,v82
v_exp_f32 v83,v83
v_mfma_f32_16x16x32_bf16 v[28:31],a[216:219],v[56:59],v[28:31]
buffer_load_dwordx4 v0,s[36:39],s67 offen offset:2048 sc1 lds
v_fma_f32 v80,v80,v99,v99
v_fma_f32 v81,v81,v99,v99
v_fma_f32 v82,v82,v99,v99
v_fma_f32 v83,v83,v99,v99
v_rcp_f32 v80,v80
v_rcp_f32 v81,v81
v_rcp_f32 v82,v82
v_rcp_f32 v83,v83
v_mfma_f32_16x16x32_bf16 v[32:35],a[220:223],v[56:59],v[32:35]
s_waitcnt lgkmcnt(3)
ds_read_b128 v[52:55],v2 offset:34816
v_mul_f32 v40,v40,v80
v_mul_f32 v41,v41,v81
v_mul_f32 v42,v42,v82
v_mul_f32 v43,v43,v83
v_cvt_pk_bf16_f32 v86,v40,v41
v_cvt_pk_bf16_f32 v87,v42,v43
v_mfma_f32_16x16x32_bf16 v[28:31],a[224:227],v[60:63],v[28:31]
buffer_store_dwordx4 v[84:87],v3,s[36:39],s73 offen offset:0 sc0 sc1
ds_read_b128 v[56:59],v2 offset:35840
v_mfma_f32_16x16x32_bf16 v[32:35],a[228:231],v[60:63],v[32:35]
s_waitcnt lgkmcnt(3)
s_cselect_b32 s53,s59,s53
s_cmp_ge_u32 s66,s59
v_mfma_f32_16x16x32_bf16 v[28:31],a[232:235],v[64:67],v[28:31]
buffer_load_dwordx4 v0,s[36:39],s67 offen offset:3072 sc1 lds
v_mfma_f32_16x16x32_bf16 v[32:35],a[236:239],v[64:67],v[32:35]
ds_read_b128 v[60:63],v2 offset:36864
v_mfma_f32_16x16x32_bf16 v[28:31],a[240:243],v[68:71],v[28:31]
ds_read_b128 v[64:67],v2 offset:37888
v_mfma_f32_16x16x32_bf16 v[32:35],a[244:247],v[68:71],v[32:35]
v_mfma_f32_16x16x32_bf16 v[28:31],a[248:251],v[72:75],v[28:31]
s_waitcnt lgkmcnt(2)
ds_read_b128 v[68:71],v2 offset:38912
v_mfma_f32_16x16x32_bf16 v[32:35],a[252:255],v[72:75],v[32:35]
s_cbranch_scc1 Lg1_slow1_%=
Lg1_d1w0_%=:
v_mfma_f32_16x16x32_bf16 v[36:39],a[0:3],v[44:47],0
s_add_u32 m0,s62,16384
ds_read_b128 v[72:75],v2 offset:39936
s_lshr_b32 s73,s48,0
v_mfma_f32_16x16x32_bf16 v[40:43],a[4:7],v[44:47],0
buffer_load_dwordx4 v1,s[36:39],s67 offen offset:0 sc1 lds
v_mfma_f32_16x16x32_bf16 v[36:39],a[8:11],v[48:51],v[36:39]
s_lshr_b32 s72,s67,6
ds_read_b128 v[44:47],v2 offset:40960
s_lshr_b32 s71,s62,2
v_mfma_f32_16x16x32_bf16 v[40:43],a[12:15],v[48:51],v[40:43]
s_cmp_lt_u32 s67,4194304
v_fma_f32 v28,-v92,v20,v28
v_fma_f32 v29,-v92,v21,v29
v_mfma_f32_16x16x32_bf16 v[36:39],a[16:19],v[52:55],v[36:39]
s_cselect_b32 s72,s59,s72
ds_read_b128 v[48:51],v2 offset:41984
v_fma_f32 v30,-v92,v22,v30
v_mfma_f32_16x16x32_bf16 v[40:43],a[20:23],v[52:55],v[40:43]
buffer_load_dwordx4 v1,s[36:39],s67 offen offset:1024 sc1 lds
v_mfma_f32_16x16x32_bf16 v[36:39],a[24:27],v[56:59],v[36:39]
s_mov_b64 s[44:45],s[46:47]
ds_read_b128 v[52:55],v2 offset:43008
v_fma_f32 v31,-v92,v23,v31
v_mfma_f32_16x16x32_bf16 v[40:43],a[28:31],v[56:59],v[40:43]
s_waitcnt lgkmcnt(3)
v_mul_f32 v76,v28,v28
v_mul_f32 v77,v29,v29
v_mfma_f32_16x16x32_bf16 v[36:39],a[32:35],v[60:63],v[36:39]
s_mov_b64 s[46:47],s[48:49]
ds_read_b128 v[56:59],v2 offset:44032
v_mul_f32 v78,v30,v30
v_mfma_f32_16x16x32_bf16 v[40:43],a[36:39],v[60:63],v[40:43]
buffer_load_dwordx4 v1,s[36:39],s67 offen offset:2048 sc1 lds
v_mfma_f32_16x16x32_bf16 v[36:39],a[40:43],v[64:67],v[36:39]
s_mov_b64 s[48:49],s[50:51]
ds_read_b128 v[60:63],v2 offset:45056
v_mul_f32 v79,v31,v31
v_mfma_f32_16x16x32_bf16 v[40:43],a[44:47],v[64:67],v[40:43]
v_fma_f32 v76,v94,v76,v93
v_mul_f32 v76,v28,v76)"
R"(
v_mfma_f32_16x16x32_bf16 v[36:39],a[48:51],v[68:71],v[36:39]
ds_read_b128 v[64:67],v2 offset:46080
v_fma_f32 v77,v94,v77,v93
v_mfma_f32_16x16x32_bf16 v[40:43],a[52:55],v[68:71],v[40:43]
buffer_load_dwordx4 v1,s[36:39],s67 offen offset:3072 sc1 lds
v_mfma_f32_16x16x32_bf16 v[36:39],a[56:59],v[72:75],v[36:39]
s_add_u32 m0,s71,s88
ds_read_b128 v[68:71],v2 offset:47104
v_mul_f32 v77,v29,v77
v_mfma_f32_16x16x32_bf16 v[40:43],a[60:63],v[72:75],v[40:43]
s_waitcnt lgkmcnt(3)
buffer_load_dword v10,s[36:39],s72 offen offset:0 sc1 lds
v_mfma_f32_16x16x32_bf16 v[36:39],a[64:67],v[44:47],v[36:39]
s_waitcnt vmcnt(11)
ds_read_b128 v[72:75],v2 offset:48128
v_fma_f32 v78,v94,v78,v93
v_mfma_f32_16x16x32_bf16 v[40:43],a[68:71],v[44:47],v[40:43]
s_barrier
v_mfma_f32_16x16x32_bf16 v[36:39],a[72:75],v[48:51],v[36:39]
s_xor_b32 s87,s63,65536
ds_read_b128 v[44:47],v2 offset:49152
v_fma_f32 v79,v94,v79,v93
v_mfma_f32_16x16x32_bf16 v[40:43],a[76:79],v[48:51],v[40:43]
s_cmp_ge_u32 s67,s59
v_exp_f32 v76,v76
v_mfma_f32_16x16x32_bf16 v[36:39],a[80:83],v[52:55],v[36:39]
s_cselect_b32 s86,s50,s51
ds_read_b128 v[48:51],v2 offset:50176
v_mul_f32 v78,v30,v78
v_mfma_f32_16x16x32_bf16 v[40:43],a[84:87],v[52:55],v[40:43]
s_cselect_b32 s87,s87,s64
v_exp_f32 v77,v77
v_mfma_f32_16x16x32_bf16 v[36:39],a[88:91],v[56:59],v[36:39]
s_lshr_b32 s69,s51,5
ds_read_b128 v[52:55],v2 offset:51200
v_mul_f32 v79,v31,v79
v_mfma_f32_16x16x32_bf16 v[40:43],a[92:95],v[56:59],v[40:43]
s_waitcnt lgkmcnt(3)
v_exp_f32 v78,v78
v_mfma_f32_16x16x32_bf16 v[36:39],a[96:99],v[60:63],v[36:39]
s_cmp_lt_u32 s51,4194304
ds_read_b128 v[56:59],v2 offset:52224
v_fma_f32 v76,v76,v95,v95
v_mfma_f32_16x16x32_bf16 v[40:43],a[100:103],v[60:63],v[40:43]
s_cselect_b32 s68,0xffffcfff,-1
v_exp_f32 v79,v79
v_mfma_f32_16x16x32_bf16 v[36:39],a[104:107],v[64:67],v[36:39]
s_and_b32 s68,s68,s69
ds_read_b128 v[60:63],v2 offset:53248
v_fma_f32 v77,v77,v95,v95
v_mfma_f32_16x16x32_bf16 v[40:43],a[108:111],v[64:67],v[40:43]
s_lshr_b32 s69,s52,5
v_rcp_f32 v76,v76
v_mfma_f32_16x16x32_bf16 v[36:39],a[112:115],v[68:71],v[36:39]
s_cmp_lt_u32 s52,4194304
ds_read_b128 v[64:67],v2 offset:54272
v_fma_f32 v78,v78,v95,v95
v_mfma_f32_16x16x32_bf16 v[40:43],a[116:119],v[68:71],v[40:43]
s_cselect_b32 s70,0xffffcfff,-1
v_rcp_f32 v77,v77
v_mfma_f32_16x16x32_bf16 v[36:39],a[120:123],v[72:75],v[36:39]
s_and_b32 s70,s70,s69
ds_read_b128 v[68:71],v2 offset:55296
v_fma_f32 v79,v79,v95,v95
v_mfma_f32_16x16x32_bf16 v[40:43],a[124:127],v[72:75],v[40:43]
s_waitcnt lgkmcnt(3)
v_rcp_f32 v78,v78
v_mfma_f32_16x16x32_bf16 v[36:39],a[128:131],v[44:47],v[36:39]
s_waitcnt vmcnt(10)
ds_read_b128 v[72:75],v2 offset:56320
v_mul_f32 v28,v28,v76
v_mfma_f32_16x16x32_bf16 v[40:43],a[132:135],v[44:47],v[40:43]
s_barrier
v_mfma_f32_16x16x32_bf16 v[36:39],a[136:139],v[48:51],v[36:39]
s_mov_b32 s62,s87
ds_read_b128 v[44:47],v2 offset:57344
ds_read_b32 v16,v7 offset:256
v_mfma_f32_16x16x32_bf16 v[40:43],a[140:143],v[48:51],v[40:43]
s_mov_b64 s[50:51],s[52:53]
v_rcp_f32 v79,v79
v_mfma_f32_16x16x32_bf16 v[36:39],a[144:147],v[52:55],v[36:39]
ds_read_b128 v[48:51],v2 offset:58368
v_mul_f32 v29,v29,v77
v_mfma_f32_16x16x32_bf16 v[40:43],a[148:151],v[52:55],v[40:43]
v_mul_f32 v30,v30,v78
v_mul_f32 v31,v31,v79
v_mfma_f32_16x16x32_bf16 v[36:39],a[152:155],v[56:59],v[36:39]
s_waitcnt lgkmcnt(1)
ds_read_b128 v[52:55],v2 offset:59392
v_cvt_pk_bf16_f32 v84,v28,v29
v_mfma_f32_16x16x32_bf16 v[40:43],a[156:159],v[56:59],v[40:43]
v_cmp_eq_u32 vcc,s60,v16
v_cvt_pk_bf16_f32 v85,v30,v31
v_fma_f32 v32,-v92,v24,v32
v_fma_f32 v33,-v92,v25,v33
v_mfma_f32_16x16x32_bf16 v[36:39],a[160:163],v[60:63],v[36:39]
s_cmp_eq_u32 vcc_lo,-1
ds_read_b128 v[56:59],v2 offset:60416
v_fma_f32 v34,-v92,v26,v34
v_mfma_f32_16x16x32_bf16 v[40:43],a[164:167],v[60:63],v[40:43]
s_cselect_b32 s66,s86,s59
s_cmp_ge_u32 s66,s59
s_cselect_b32 s68,s68,s70
s_add_u32 m0,s78,0
v_mfma_f32_16x16x32_bf16 v[36:39],a[168:171],v[64:67],v[36:39]
buffer_load_dword v6,s[36:39],s68 offen offset:0 sc1 lds
v_mfma_f32_16x16x32_bf16 v[40:43],a[172:175],v[64:67],v[40:43]
s_mov_b32 m0,s62
ds_read_b128 v[60:63],v2 offset:61440
v_fma_f32 v35,-v92,v27,v35
v_mfma_f32_16x16x32_bf16 v[36:39],a[176:179],v[68:71],v[36:39]
buffer_load_dwordx4 v0,s[36:39],s66 offen offset:0 sc1 lds
v_mfma_f32_16x16x32_bf16 v[40:43],a[180:183],v[68:71],v[40:43]
s_xor_b32 s64,s64,65536
ds_read_b128 v[64:67],v2 offset:62464
v_mul_f32 v80,v32,v32
v_mfma_f32_16x16x32_bf16 v[36:39],a[184:187],v[72:75],v[36:39]
s_add_u32 s74,s53,s55
ds_read_b128 v[68:71],v2 offset:63488
v_mul_f32 v81,v33,v33
v_mfma_f32_16x16x32_bf16 v[40:43],a[188:191],v[72:75],v[40:43]
s_waitcnt lgkmcnt(3)
v_mul_f32 v82,v34,v34
v_mul_f32 v83,v35,v35
v_mfma_f32_16x16x32_bf16 v[36:39],a[192:195],v[44:47],v[36:39]
s_waitcnt vmcnt(7)
ds_read_b128 v[72:75],v2 offset:64512
v_xor_b32 v2,65536,v2
v_mfma_f32_16x16x32_bf16 v[40:43],a[196:199],v[44:47],v[40:43]
s_barrier
v_mfma_f32_16x16x32_bf16 v[36:39],a[200:203],v[48:51],v[36:39]
buffer_load_dwordx4 v0,s[36:39],s66 offen offset:1024 sc1 lds
v_mfma_f32_16x16x32_bf16 v[40:43],a[204:207],v[48:51],v[40:43]
s_cmp_eq_u32 s74,s56
ds_read_b128 v[44:47],v2 offset:0
ds_read_b128 v[96:99],v8 offset:8192
v_mfma_f32_16x16x32_bf16 v[36:39],a[208:211],v[52:55],v[36:39]
s_cselect_b32 s74,s57,s74
ds_read_b128 v[48:51],v2 offset:1024
v_fma_f32 v80,v94,v80,v93
v_mfma_f32_16x16x32_bf16 v[40:43],a[212:215],v[52:55],v[40:43]
s_cmp_eq_u32 s74,s58
v_fma_f32 v81,v94,v81,v93
v_fma_f32 v82,v94,v82,v93
v_fma_f32 v83,v94,v83,v93
v_mul_f32 v80,v32,v80
v_mul_f32 v81,v33,v81
v_mul_f32 v82,v34,v82
v_mul_f32 v83,v35,v83
v_exp_f32 v80,v80
v_exp_f32 v81,v81
v_exp_f32 v82,v82
v_exp_f32 v83,v83
v_mfma_f32_16x16x32_bf16 v[36:39],a[216:219],v[56:59],v[36:39]
buffer_load_dwordx4 v0,s[36:39],s66 offen offset:2048 sc1 lds
v_fma_f32 v80,v80,v95,v95
v_fma_f32 v81,v81,v95,v95
v_fma_f32 v82,v82,v95,v95
v_fma_f32 v83,v83,v95,v95
v_rcp_f32 v80,v80
v_rcp_f32 v81,v81
v_rcp_f32 v82,v82
v_rcp_f32 v83,v83
v_mfma_f32_16x16x32_bf16 v[40:43],a[220:223],v[56:59],v[40:43]
s_waitcnt lgkmcnt(3)
ds_read_b128 v[52:55],v2 offset:2048
v_mul_f32 v32,v32,v80
v_mul_f32 v33,v33,v81
v_mul_f32 v34,v34,v82
v_mul_f32 v35,v35,v83
v_cvt_pk_bf16_f32 v86,v32,v33
v_cvt_pk_bf16_f32 v87,v34,v35
v_mfma_f32_16x16x32_bf16 v[36:39],a[224:227],v[60:63],v[36:39]
buffer_store_dwordx4 v[84:87],v3,s[36:39],s73 offen offset:0 sc0 sc1
ds_read_b128 v[56:59],v2 offset:3072
v_xor_b32 v8,16384,v8
v_mfma_f32_16x16x32_bf16 v[40:43],a[228:231],v[60:63],v[40:43]
s_waitcnt lgkmcnt(3)
s_cselect_b32 s74,s59,s74
s_mov_b32 s52,s74
v_mfma_f32_16x16x32_bf16 v[36:39],a[232:235],v[64:67],v[36:39]
buffer_load_dwordx4 v0,s[36:39],s66 offen offset:3072 sc1 lds
v_mfma_f32_16x16x32_bf16 v[40:43],a[236:239],v[64:67],v[40:43]
s_cmp_lt_u32 s67,s59
ds_read_b128 v[60:63],v2 offset:4096
v_mfma_f32_16x16x32_bf16 v[36:39],a[240:243],v[68:71],v[36:39]
ds_read_b128 v[64:67],v2 offset:5120
v_mfma_f32_16x16x32_bf16 v[40:43],a[244:247],v[68:71],v[40:43]
v_mfma_f32_16x16x32_bf16 v[36:39],a[248:251],v[72:75],v[36:39]
s_waitcnt lgkmcnt(2)
ds_read_b128 v[68:71],v2 offset:6144
v_mfma_f32_16x16x32_bf16 v[40:43],a[252:255],v[72:75],v[40:43]
s_cbranch_scc1 Lg1_d0w0_%=
s_branch Lg1_slow0_%=
Lg1_d0w1_%=:
v_mfma_f32_16x16x32_bf16 v[28:31],a[32:35],v[44:47],0
s_add_u32 m0,s62,16384
ds_read_b128 v[72:75],v2 offset:3072
s_lshr_b32 s73,s47,0
v_mfma_f32_16x16x32_bf16 v[32:35],a[36:39],v[44:47],0)"
R"(
s_lshr_b32 s72,s66,6
s_lshr_b32 s71,s62,2
s_cmp_lt_u32 s66,4194304
v_mfma_f32_16x16x32_bf16 v[28:31],a[40:43],v[48:51],v[28:31]
buffer_load_dwordx4 v1,s[36:39],s66 offen offset:0 sc1 lds
v_mfma_f32_16x16x32_bf16 v[32:35],a[44:47],v[48:51],v[32:35]
s_cselect_b32 s72,s59,s72
ds_read_b128 v[44:47],v2 offset:12288
v_fma_f32 v36,-v96,v20,v36
v_mfma_f32_16x16x32_bf16 v[28:31],a[48:51],v[52:55],v[28:31]
ds_read_b128 v[48:51],v2 offset:13312
v_fma_f32 v37,-v96,v21,v37
v_mfma_f32_16x16x32_bf16 v[32:35],a[52:55],v[52:55],v[32:35]
v_fma_f32 v38,-v96,v22,v38
v_fma_f32 v39,-v96,v23,v39
v_mfma_f32_16x16x32_bf16 v[28:31],a[56:59],v[56:59],v[28:31]
buffer_load_dwordx4 v1,s[36:39],s66 offen offset:1024 sc1 lds
v_mfma_f32_16x16x32_bf16 v[32:35],a[60:63],v[56:59],v[32:35]
s_waitcnt lgkmcnt(2)
ds_read_b128 v[52:55],v2 offset:14336
v_mul_f32 v76,v36,v36
v_mfma_f32_16x16x32_bf16 v[28:31],a[0:3],v[60:63],v[28:31]
ds_read_b128 v[56:59],v2 offset:15360
v_mul_f32 v77,v37,v37
v_mfma_f32_16x16x32_bf16 v[32:35],a[4:7],v[60:63],v[32:35]
v_mul_f32 v78,v38,v38
v_mul_f32 v79,v39,v39
v_mfma_f32_16x16x32_bf16 v[28:31],a[8:11],v[64:67],v[28:31]
buffer_load_dwordx4 v1,s[36:39],s66 offen offset:2048 sc1 lds
v_mfma_f32_16x16x32_bf16 v[32:35],a[12:15],v[64:67],v[32:35]
ds_read_b128 v[60:63],v2 offset:8192
v_fma_f32 v76,v98,v76,v97
v_mfma_f32_16x16x32_bf16 v[28:31],a[16:19],v[68:71],v[28:31]
ds_read_b128 v[64:67],v2 offset:9216
v_mul_f32 v76,v36,v76
v_mfma_f32_16x16x32_bf16 v[32:35],a[20:23],v[68:71],v[32:35]
v_exp_f32 v76,v76
v_mfma_f32_16x16x32_bf16 v[28:31],a[24:27],v[72:75],v[28:31]
buffer_load_dwordx4 v1,s[36:39],s66 offen offset:3072 sc1 lds
v_mfma_f32_16x16x32_bf16 v[32:35],a[28:31],v[72:75],v[32:35]
s_waitcnt lgkmcnt(2)
ds_read_b128 v[68:71],v2 offset:10240
s_add_u32 m0,s71,s88
v_mfma_f32_16x16x32_bf16 v[28:31],a[96:99],v[44:47],v[28:31]
s_xor_b32 s87,s64,65536
ds_read_b128 v[72:75],v2 offset:11264
buffer_load_dword v10,s[36:39],s72 offen offset:0 sc1 lds
s_waitcnt vmcnt(10)
v_mfma_f32_16x16x32_bf16 v[32:35],a[100:103],v[44:47],v[32:35]
s_barrier
v_mfma_f32_16x16x32_bf16 v[28:31],a[104:107],v[48:51],v[28:31]
s_cmp_ge_u32 s66,s59
ds_read_b128 v[44:47],v2 offset:20480
v_fma_f32 v76,v76,v99,v99
v_mfma_f32_16x16x32_bf16 v[32:35],a[108:111],v[48:51],v[32:35]
s_cselect_b32 s86,s49,s50
v_rcp_f32 v76,v76
v_mfma_f32_16x16x32_bf16 v[28:31],a[112:115],v[52:55],v[28:31]
s_cselect_b32 s87,s87,s63
ds_read_b128 v[48:51],v2 offset:21504
v_fma_f32 v77,v98,v77,v97
v_mfma_f32_16x16x32_bf16 v[32:35],a[116:119],v[52:55],v[32:35]
s_mov_b32 s62,s87
v_mul_f32 v77,v37,v77
v_fma_f32 v78,v98,v78,v97
v_mfma_f32_16x16x32_bf16 v[28:31],a[120:123],v[56:59],v[28:31]
s_mov_b32 m0,s62
ds_read_b128 v[52:55],v2 offset:22528
v_mul_f32 v78,v38,v78
v_mfma_f32_16x16x32_bf16 v[32:35],a[124:127],v[56:59],v[32:35]
s_waitcnt lgkmcnt(3)
v_exp_f32 v77,v77
v_mfma_f32_16x16x32_bf16 v[28:31],a[64:67],v[60:63],v[28:31]
ds_read_b128 v[56:59],v2 offset:23552
v_fma_f32 v77,v77,v99,v99
v_mfma_f32_16x16x32_bf16 v[32:35],a[68:71],v[60:63],v[32:35]
v_exp_f32 v78,v78
v_mfma_f32_16x16x32_bf16 v[28:31],a[72:75],v[64:67],v[28:31]
ds_read_b128 v[60:63],v2 offset:16384
v_fma_f32 v78,v78,v99,v99
v_mfma_f32_16x16x32_bf16 v[32:35],a[76:79],v[64:67],v[32:35]
v_rcp_f32 v77,v77
v_mfma_f32_16x16x32_bf16 v[28:31],a[80:83],v[68:71],v[28:31]
ds_read_b128 v[64:67],v2 offset:17408
v_fma_f32 v79,v98,v79,v97
v_mfma_f32_16x16x32_bf16 v[32:35],a[84:87],v[68:71],v[32:35]
v_rcp_f32 v78,v78
v_mfma_f32_16x16x32_bf16 v[28:31],a[88:91],v[72:75],v[28:31]
ds_read_b128 v[68:71],v2 offset:18432
v_mul_f32 v79,v39,v79
v_mfma_f32_16x16x32_bf16 v[32:35],a[92:95],v[72:75],v[32:35]
s_waitcnt lgkmcnt(3)
v_exp_f32 v79,v79
v_mfma_f32_16x16x32_bf16 v[28:31],a[160:163],v[44:47],v[28:31]
s_nop 0
ds_read_b128 v[72:75],v2 offset:19456
v_fma_f32 v79,v79,v99,v99
v_mfma_f32_16x16x32_bf16 v[32:35],a[164:167],v[44:47],v[32:35]
s_barrier
v_mfma_f32_16x16x32_bf16 v[28:31],a[168:171],v[48:51],v[28:31]
ds_read_b128 v[44:47],v2 offset:28672
ds_read_b32 v16,v7 offset:0
v_mfma_f32_16x16x32_bf16 v[32:35],a[172:175],v[48:51],v[32:35]
v_rcp_f32 v79,v79
v_mfma_f32_16x16x32_bf16 v[28:31],a[176:179],v[52:55],v[28:31]
ds_read_b128 v[48:51],v2 offset:29696
v_mul_f32 v36,v36,v76
v_mfma_f32_16x16x32_bf16 v[32:35],a[180:183],v[52:55],v[32:35]
v_mul_f32 v37,v37,v77
v_mul_f32 v38,v38,v78
v_mfma_f32_16x16x32_bf16 v[28:31],a[184:187],v[56:59],v[28:31]
s_waitcnt lgkmcnt(1)
ds_read_b128 v[52:55],v2 offset:30720
v_mul_f32 v39,v39,v79
v_mfma_f32_16x16x32_bf16 v[32:35],a[188:191],v[56:59],v[32:35]
v_cmp_eq_u32 vcc,s60,v16
v_cvt_pk_bf16_f32 v84,v36,v37
v_cvt_pk_bf16_f32 v85,v38,v39
v_fma_f32 v40,-v96,v24,v40
v_mfma_f32_16x16x32_bf16 v[28:31],a[128:131],v[60:63],v[28:31]
s_cmp_eq_u32 vcc_lo,-1
ds_read_b128 v[56:59],v2 offset:31744
s_cselect_b32 s67,s86,s59
v_mfma_f32_16x16x32_bf16 v[32:35],a[132:135],v[60:63],v[32:35]
buffer_load_dwordx4 v0,s[36:39],s67 offen offset:0 sc1 lds
v_mfma_f32_16x16x32_bf16 v[28:31],a[136:139],v[64:67],v[28:31]
s_xor_b32 s63,s63,65536
ds_read_b128 v[60:63],v2 offset:24576
v_fma_f32 v41,-v96,v25,v41
v_mfma_f32_16x16x32_bf16 v[32:35],a[140:143],v[64:67],v[32:35]
s_add_u32 s53,s52,s55
v_fma_f32 v42,-v96,v26,v42
v_fma_f32 v43,-v96,v27,v43
v_mfma_f32_16x16x32_bf16 v[28:31],a[144:147],v[68:71],v[28:31]
s_cmp_eq_u32 s53,s56
ds_read_b128 v[64:67],v2 offset:25600
v_mul_f32 v80,v40,v40
v_mfma_f32_16x16x32_bf16 v[32:35],a[148:151],v[68:71],v[32:35]
buffer_load_dwordx4 v0,s[36:39],s67 offen offset:1024 sc1 lds
v_mfma_f32_16x16x32_bf16 v[28:31],a[152:155],v[72:75],v[28:31]
s_cselect_b32 s53,s57,s53
ds_read_b128 v[68:71],v2 offset:26624
v_mul_f32 v81,v41,v41
v_mfma_f32_16x16x32_bf16 v[32:35],a[156:159],v[72:75],v[32:35]
s_waitcnt lgkmcnt(3)
v_mul_f32 v82,v42,v42
v_mul_f32 v83,v43,v43
v_mfma_f32_16x16x32_bf16 v[28:31],a[224:227],v[44:47],v[28:31]
s_waitcnt vmcnt(7)
ds_read_b128 v[72:75],v2 offset:27648
v_fma_f32 v80,v98,v80,v97
v_mfma_f32_16x16x32_bf16 v[32:35],a[228:231],v[44:47],v[32:35]
s_barrier
v_mfma_f32_16x16x32_bf16 v[28:31],a[232:235],v[48:51],v[28:31]
s_cmp_eq_u32 s53,s58
ds_read_b128 v[44:47],v2 offset:36864
ds_read_b128 v[92:95],v8 offset:0
v_mfma_f32_16x16x32_bf16 v[32:35],a[236:239],v[48:51],v[32:35]
buffer_load_dwordx4 v0,s[36:39],s67 offen offset:2048 sc1 lds
v_mfma_f32_16x16x32_bf16 v[28:31],a[240:243],v[52:55],v[28:31]
s_cselect_b32 s53,s59,s53
ds_read_b128 v[48:51],v2 offset:37888
v_mul_f32 v80,v40,v80
v_mfma_f32_16x16x32_bf16 v[32:35],a[244:247],v[52:55],v[32:35]
s_cmp_ge_u32 s66,s59
v_exp_f32 v80,v80
v_mfma_f32_16x16x32_bf16 v[28:31],a[248:251],v[56:59],v[28:31]
v_fma_f32 v81,v98,v81,v97
v_fma_f32 v82,v98,v82,v97
v_fma_f32 v83,v98,v83,v97
v_mul_f32 v81,v41,v81
v_mul_f32 v82,v42,v82
v_mul_f32 v83,v43,v83
v_exp_f32 v81,v81
v_exp_f32 v82,v82
v_exp_f32 v83,v83
v_mfma_f32_16x16x32_bf16 v[32:35],a[252:255],v[56:59],v[32:35]
s_waitcnt lgkmcnt(3)
ds_read_b128 v[52:55],v2 offset:38912
buffer_load_dwordx4 v0,s[36:39],s67 offen offset:3072 sc1 lds
v_fma_f32 v80,v80,v99,v99
v_fma_f32 v81,v81,v99,v99
v_fma_f32 v82,v82,v99,v99
v_fma_f32 v83,v83,v99,v99
v_rcp_f32 v80,v80
v_rcp_f32 v81,v81
v_rcp_f32 v82,v82
v_rcp_f32 v83,v83
v_mfma_f32_16x16x32_bf16 v[28:31],a[192:195],v[60:63],v[28:31]
s_waitcnt lgkmcnt(2)
v_mul_f32 v40,v40,v80
v_mul_f32 v41,v41,v81
v_mul_f32 v42,v42,v82
v_mul_f32 v43,v43,v83
v_cvt_pk_bf16_f32 v86,v40,v41
v_cvt_pk_bf16_f32 v87,v42,v43
v_mfma_f32_16x16x32_bf16 v[32:35],a[196:199],v[60:63],v[32:35]
ds_read_b128 v[56:59],v2 offset:39936
buffer_store_dwordx4 v[84:87],v3,s[36:39],s73 offen offset:0 sc0 sc1
v_mfma_f32_16x16x32_bf16 v[28:31],a[200:203],v[64:67],v[28:31])"
R"(
ds_read_b128 v[60:63],v2 offset:32768
v_mfma_f32_16x16x32_bf16 v[32:35],a[204:207],v[64:67],v[32:35]
v_mfma_f32_16x16x32_bf16 v[28:31],a[208:211],v[68:71],v[28:31]
ds_read_b128 v[64:67],v2 offset:33792
v_mfma_f32_16x16x32_bf16 v[32:35],a[212:215],v[68:71],v[32:35]
v_mfma_f32_16x16x32_bf16 v[28:31],a[216:219],v[72:75],v[28:31]
s_waitcnt lgkmcnt(2)
ds_read_b128 v[68:71],v2 offset:34816
v_mfma_f32_16x16x32_bf16 v[32:35],a[220:223],v[72:75],v[32:35]
s_cbranch_scc1 Lg1_slow1_%=
Lg1_d1w1_%=:
v_mfma_f32_16x16x32_bf16 v[36:39],a[32:35],v[44:47],0
s_add_u32 m0,s62,16384
ds_read_b128 v[72:75],v2 offset:35840
s_lshr_b32 s73,s48,0
v_mfma_f32_16x16x32_bf16 v[40:43],a[36:39],v[44:47],0
s_lshr_b32 s72,s67,6
s_lshr_b32 s71,s62,2
s_cmp_lt_u32 s67,4194304
v_mfma_f32_16x16x32_bf16 v[36:39],a[40:43],v[48:51],v[36:39]
buffer_load_dwordx4 v1,s[36:39],s67 offen offset:0 sc1 lds
v_mfma_f32_16x16x32_bf16 v[40:43],a[44:47],v[48:51],v[40:43]
s_cselect_b32 s72,s59,s72
ds_read_b128 v[44:47],v2 offset:45056
v_fma_f32 v28,-v92,v20,v28
v_mfma_f32_16x16x32_bf16 v[36:39],a[48:51],v[52:55],v[36:39]
s_mov_b64 s[44:45],s[46:47]
ds_read_b128 v[48:51],v2 offset:46080
v_fma_f32 v29,-v92,v21,v29
v_mfma_f32_16x16x32_bf16 v[40:43],a[52:55],v[52:55],v[40:43]
s_mov_b64 s[46:47],s[48:49]
v_fma_f32 v30,-v92,v22,v30
v_fma_f32 v31,-v92,v23,v31
v_mfma_f32_16x16x32_bf16 v[36:39],a[56:59],v[56:59],v[36:39]
buffer_load_dwordx4 v1,s[36:39],s67 offen offset:1024 sc1 lds
v_mfma_f32_16x16x32_bf16 v[40:43],a[60:63],v[56:59],v[40:43]
s_waitcnt lgkmcnt(2)
ds_read_b128 v[52:55],v2 offset:47104
v_mul_f32 v76,v28,v28
v_mfma_f32_16x16x32_bf16 v[36:39],a[0:3],v[60:63],v[36:39]
s_mov_b64 s[48:49],s[50:51]
ds_read_b128 v[56:59],v2 offset:48128
v_mul_f32 v77,v29,v29
v_mfma_f32_16x16x32_bf16 v[40:43],a[4:7],v[60:63],v[40:43]
v_mul_f32 v78,v30,v30
v_mul_f32 v79,v31,v31
v_mfma_f32_16x16x32_bf16 v[36:39],a[8:11],v[64:67],v[36:39]
buffer_load_dwordx4 v1,s[36:39],s67 offen offset:2048 sc1 lds
v_mfma_f32_16x16x32_bf16 v[40:43],a[12:15],v[64:67],v[40:43]
ds_read_b128 v[60:63],v2 offset:40960
v_fma_f32 v76,v94,v76,v93
v_mfma_f32_16x16x32_bf16 v[36:39],a[16:19],v[68:71],v[36:39]
ds_read_b128 v[64:67],v2 offset:41984
v_mul_f32 v76,v28,v76
v_mfma_f32_16x16x32_bf16 v[40:43],a[20:23],v[68:71],v[40:43]
v_exp_f32 v76,v76
v_mfma_f32_16x16x32_bf16 v[36:39],a[24:27],v[72:75],v[36:39]
buffer_load_dwordx4 v1,s[36:39],s67 offen offset:3072 sc1 lds
v_mfma_f32_16x16x32_bf16 v[40:43],a[28:31],v[72:75],v[40:43]
s_waitcnt lgkmcnt(2)
ds_read_b128 v[68:71],v2 offset:43008
s_add_u32 m0,s71,s88
v_mfma_f32_16x16x32_bf16 v[36:39],a[96:99],v[44:47],v[36:39]
s_xor_b32 s87,s63,65536
ds_read_b128 v[72:75],v2 offset:44032
buffer_load_dword v10,s[36:39],s72 offen offset:0 sc1 lds
s_waitcnt vmcnt(10)
v_mfma_f32_16x16x32_bf16 v[40:43],a[100:103],v[44:47],v[40:43]
s_barrier
v_mfma_f32_16x16x32_bf16 v[36:39],a[104:107],v[48:51],v[36:39]
s_cmp_ge_u32 s67,s59
ds_read_b128 v[44:47],v2 offset:53248
v_fma_f32 v76,v76,v95,v95
v_mfma_f32_16x16x32_bf16 v[40:43],a[108:111],v[48:51],v[40:43]
s_cselect_b32 s86,s50,s51
v_rcp_f32 v76,v76
v_mfma_f32_16x16x32_bf16 v[36:39],a[112:115],v[52:55],v[36:39]
s_cselect_b32 s87,s87,s64
ds_read_b128 v[48:51],v2 offset:54272
v_fma_f32 v77,v94,v77,v93
v_mfma_f32_16x16x32_bf16 v[40:43],a[116:119],v[52:55],v[40:43]
s_mov_b32 s62,s87
v_mul_f32 v77,v29,v77
v_fma_f32 v78,v94,v78,v93
v_mfma_f32_16x16x32_bf16 v[36:39],a[120:123],v[56:59],v[36:39]
s_mov_b32 m0,s62
ds_read_b128 v[52:55],v2 offset:55296
v_mul_f32 v78,v30,v78
v_mfma_f32_16x16x32_bf16 v[40:43],a[124:127],v[56:59],v[40:43]
s_waitcnt lgkmcnt(3)
v_exp_f32 v77,v77
v_mfma_f32_16x16x32_bf16 v[36:39],a[64:67],v[60:63],v[36:39]
s_mov_b64 s[50:51],s[52:53]
ds_read_b128 v[56:59],v2 offset:56320
v_fma_f32 v77,v77,v95,v95
v_mfma_f32_16x16x32_bf16 v[40:43],a[68:71],v[60:63],v[40:43]
v_exp_f32 v78,v78
v_mfma_f32_16x16x32_bf16 v[36:39],a[72:75],v[64:67],v[36:39]
ds_read_b128 v[60:63],v2 offset:49152
v_fma_f32 v78,v78,v95,v95
v_mfma_f32_16x16x32_bf16 v[40:43],a[76:79],v[64:67],v[40:43]
v_rcp_f32 v77,v77
v_mfma_f32_16x16x32_bf16 v[36:39],a[80:83],v[68:71],v[36:39]
ds_read_b128 v[64:67],v2 offset:50176
v_fma_f32 v79,v94,v79,v93
v_mfma_f32_16x16x32_bf16 v[40:43],a[84:87],v[68:71],v[40:43]
v_rcp_f32 v78,v78
v_mfma_f32_16x16x32_bf16 v[36:39],a[88:91],v[72:75],v[36:39]
ds_read_b128 v[68:71],v2 offset:51200
v_mul_f32 v79,v31,v79
v_mfma_f32_16x16x32_bf16 v[40:43],a[92:95],v[72:75],v[40:43]
s_waitcnt lgkmcnt(3)
v_exp_f32 v79,v79
v_mfma_f32_16x16x32_bf16 v[36:39],a[160:163],v[44:47],v[36:39]
s_nop 0
ds_read_b128 v[72:75],v2 offset:52224
v_fma_f32 v79,v79,v95,v95
v_mfma_f32_16x16x32_bf16 v[40:43],a[164:167],v[44:47],v[40:43]
s_barrier
v_mfma_f32_16x16x32_bf16 v[36:39],a[168:171],v[48:51],v[36:39]
ds_read_b128 v[44:47],v2 offset:61440
ds_read_b32 v16,v7 offset:256
v_mfma_f32_16x16x32_bf16 v[40:43],a[172:175],v[48:51],v[40:43]
v_rcp_f32 v79,v79
v_mfma_f32_16x16x32_bf16 v[36:39],a[176:179],v[52:55],v[36:39]
ds_read_b128 v[48:51],v2 offset:62464
v_mul_f32 v28,v28,v76
v_mfma_f32_16x16x32_bf16 v[40:43],a[180:183],v[52:55],v[40:43]
v_mul_f32 v29,v29,v77
v_mul_f32 v30,v30,v78
v_mfma_f32_16x16x32_bf16 v[36:39],a[184:187],v[56:59],v[36:39]
s_waitcnt lgkmcnt(1)
ds_read_b128 v[52:55],v2 offset:63488
v_mul_f32 v31,v31,v79
v_mfma_f32_16x16x32_bf16 v[40:43],a[188:191],v[56:59],v[40:43]
v_cmp_eq_u32 vcc,s60,v16
v_cvt_pk_bf16_f32 v84,v28,v29
v_cvt_pk_bf16_f32 v85,v30,v31
v_fma_f32 v32,-v92,v24,v32
v_mfma_f32_16x16x32_bf16 v[36:39],a[128:131],v[60:63],v[36:39]
s_cmp_eq_u32 vcc_lo,-1
ds_read_b128 v[56:59],v2 offset:64512
s_cselect_b32 s66,s86,s59
v_mfma_f32_16x16x32_bf16 v[40:43],a[132:135],v[60:63],v[40:43]
buffer_load_dwordx4 v0,s[36:39],s66 offen offset:0 sc1 lds
v_mfma_f32_16x16x32_bf16 v[36:39],a[136:139],v[64:67],v[36:39]
s_xor_b32 s64,s64,65536
ds_read_b128 v[60:63],v2 offset:57344
v_fma_f32 v33,-v92,v25,v33
v_mfma_f32_16x16x32_bf16 v[40:43],a[140:143],v[64:67],v[40:43]
s_add_u32 s74,s53,s55
v_fma_f32 v34,-v92,v26,v34
v_fma_f32 v35,-v92,v27,v35
v_mfma_f32_16x16x32_bf16 v[36:39],a[144:147],v[68:71],v[36:39]
s_cmp_eq_u32 s74,s56
ds_read_b128 v[64:67],v2 offset:58368
v_mul_f32 v80,v32,v32
v_mfma_f32_16x16x32_bf16 v[40:43],a[148:151],v[68:71],v[40:43]
buffer_load_dwordx4 v0,s[36:39],s66 offen offset:1024 sc1 lds
v_mfma_f32_16x16x32_bf16 v[36:39],a[152:155],v[72:75],v[36:39]
s_cselect_b32 s74,s57,s74
ds_read_b128 v[68:71],v2 offset:59392
v_mul_f32 v81,v33,v33
v_mfma_f32_16x16x32_bf16 v[40:43],a[156:159],v[72:75],v[40:43]
s_waitcnt lgkmcnt(3)
v_mul_f32 v82,v34,v34
v_mul_f32 v83,v35,v35
v_mfma_f32_16x16x32_bf16 v[36:39],a[224:227],v[44:47],v[36:39]
s_waitcnt vmcnt(7)
ds_read_b128 v[72:75],v2 offset:60416
v_xor_b32 v2,65536,v2
v_mfma_f32_16x16x32_bf16 v[40:43],a[228:231],v[44:47],v[40:43]
s_barrier
v_mfma_f32_16x16x32_bf16 v[36:39],a[232:235],v[48:51],v[36:39]
s_cmp_eq_u32 s74,s58
ds_read_b128 v[44:47],v2 offset:4096
ds_read_b128 v[96:99],v8 offset:8192
v_mfma_f32_16x16x32_bf16 v[40:43],a[236:239],v[48:51],v[40:43]
buffer_load_dwordx4 v0,s[36:39],s66 offen offset:2048 sc1 lds
v_mfma_f32_16x16x32_bf16 v[36:39],a[240:243],v[52:55],v[36:39]
s_cselect_b32 s74,s59,s74
ds_read_b128 v[48:51],v2 offset:5120
v_fma_f32 v80,v94,v80,v93
v_mfma_f32_16x16x32_bf16 v[40:43],a[244:247],v[52:55],v[40:43]
s_mov_b32 s52,s74
v_mul_f32 v80,v32,v80
v_fma_f32 v81,v94,v81,v93
v_mfma_f32_16x16x32_bf16 v[36:39],a[248:251],v[56:59],v[36:39]
s_cmp_lt_u32 s67,s59
v_fma_f32 v82,v94,v82,v93
v_fma_f32 v83,v94,v83,v93
v_mul_f32 v81,v33,v81
v_mul_f32 v82,v34,v82
v_mul_f32 v83,v35,v83
v_exp_f32 v80,v80
v_exp_f32 v81,v81
v_exp_f32 v82,v82
v_exp_f32 v83,v83
v_mfma_f32_16x16x32_bf16 v[40:43],a[252:255],v[56:59],v[40:43]
s_waitcnt lgkmcnt(3))"
R"(
ds_read_b128 v[52:55],v2 offset:6144
buffer_load_dwordx4 v0,s[36:39],s66 offen offset:3072 sc1 lds
v_fma_f32 v80,v80,v95,v95
v_fma_f32 v81,v81,v95,v95
v_fma_f32 v82,v82,v95,v95
v_fma_f32 v83,v83,v95,v95
v_rcp_f32 v80,v80
v_rcp_f32 v81,v81
v_rcp_f32 v82,v82
v_rcp_f32 v83,v83
v_mfma_f32_16x16x32_bf16 v[36:39],a[192:195],v[60:63],v[36:39]
s_waitcnt lgkmcnt(2)
v_mul_f32 v32,v32,v80
v_mul_f32 v33,v33,v81
v_mul_f32 v34,v34,v82
v_mul_f32 v35,v35,v83
v_cvt_pk_bf16_f32 v86,v32,v33
v_cvt_pk_bf16_f32 v87,v34,v35
v_mfma_f32_16x16x32_bf16 v[40:43],a[196:199],v[60:63],v[40:43]
ds_read_b128 v[56:59],v2 offset:7168
buffer_store_dwordx4 v[84:87],v3,s[36:39],s73 offen offset:0 sc0 sc1
v_xor_b32 v8,16384,v8
v_mfma_f32_16x16x32_bf16 v[36:39],a[200:203],v[64:67],v[36:39]
ds_read_b128 v[60:63],v2 offset:0
v_mfma_f32_16x16x32_bf16 v[40:43],a[204:207],v[64:67],v[40:43]
v_mfma_f32_16x16x32_bf16 v[36:39],a[208:211],v[68:71],v[36:39]
ds_read_b128 v[64:67],v2 offset:1024
v_mfma_f32_16x16x32_bf16 v[40:43],a[212:215],v[68:71],v[40:43]
v_mfma_f32_16x16x32_bf16 v[36:39],a[216:219],v[72:75],v[36:39]
s_waitcnt lgkmcnt(2)
ds_read_b128 v[68:71],v2 offset:2048
v_mfma_f32_16x16x32_bf16 v[40:43],a[220:223],v[72:75],v[40:43]
s_cbranch_scc1 Lg1_d0w1_%=
s_branch Lg1_slow0_%=
Lg1_d0w2_%=:
v_mfma_f32_16x16x32_bf16 v[28:31],a[64:67],v[44:47],0
s_add_u32 m0,s62,16384
ds_read_b128 v[72:75],v2 offset:15360
s_lshr_b32 s73,s47,0
v_mfma_f32_16x16x32_bf16 v[32:35],a[68:71],v[44:47],0
s_lshr_b32 s72,s66,6
s_lshr_b32 s71,s62,2
s_cmp_lt_u32 s66,4194304
v_mfma_f32_16x16x32_bf16 v[28:31],a[72:75],v[48:51],v[28:31]
s_cselect_b32 s72,s59,s72
ds_read_b128 v[44:47],v2 offset:0
v_mfma_f32_16x16x32_bf16 v[32:35],a[76:79],v[48:51],v[32:35]
buffer_load_dwordx4 v1,s[36:39],s66 offen offset:0 sc1 lds
v_mfma_f32_16x16x32_bf16 v[28:31],a[80:83],v[52:55],v[28:31]
ds_read_b128 v[48:51],v2 offset:1024
v_fma_f32 v36,-v96,v20,v36
v_mfma_f32_16x16x32_bf16 v[32:35],a[84:87],v[52:55],v[32:35]
v_fma_f32 v37,-v96,v21,v37
v_fma_f32 v38,-v96,v22,v38
v_mfma_f32_16x16x32_bf16 v[28:31],a[88:91],v[56:59],v[28:31]
ds_read_b128 v[52:55],v2 offset:2048
v_fma_f32 v39,-v96,v23,v39
v_mfma_f32_16x16x32_bf16 v[32:35],a[92:95],v[56:59],v[32:35]
s_waitcnt lgkmcnt(3)
buffer_load_dwordx4 v1,s[36:39],s66 offen offset:1024 sc1 lds
v_mfma_f32_16x16x32_bf16 v[28:31],a[96:99],v[60:63],v[28:31]
ds_read_b128 v[56:59],v2 offset:3072
v_mul_f32 v76,v36,v36
v_mfma_f32_16x16x32_bf16 v[32:35],a[100:103],v[60:63],v[32:35]
v_mul_f32 v77,v37,v37
v_mul_f32 v78,v38,v38
v_mfma_f32_16x16x32_bf16 v[28:31],a[104:107],v[64:67],v[28:31]
ds_read_b128 v[60:63],v2 offset:4096
v_mul_f32 v79,v39,v39
v_mfma_f32_16x16x32_bf16 v[32:35],a[108:111],v[64:67],v[32:35]
buffer_load_dwordx4 v1,s[36:39],s66 offen offset:2048 sc1 lds
v_mfma_f32_16x16x32_bf16 v[28:31],a[112:115],v[68:71],v[28:31]
ds_read_b128 v[64:67],v2 offset:5120
v_fma_f32 v76,v98,v76,v97
v_mfma_f32_16x16x32_bf16 v[32:35],a[116:119],v[68:71],v[32:35]
v_mul_f32 v76,v36,v76
v_fma_f32 v77,v98,v77,v97
v_mfma_f32_16x16x32_bf16 v[28:31],a[120:123],v[72:75],v[28:31]
ds_read_b128 v[68:71],v2 offset:6144
v_mul_f32 v77,v37,v77
v_mfma_f32_16x16x32_bf16 v[32:35],a[124:127],v[72:75],v[32:35]
s_waitcnt lgkmcnt(3)
buffer_load_dwordx4 v1,s[36:39],s66 offen offset:3072 sc1 lds
s_add_u32 m0,s71,s88
v_mfma_f32_16x16x32_bf16 v[28:31],a[0:3],v[44:47],v[28:31]
s_xor_b32 s87,s64,65536
ds_read_b128 v[72:75],v2 offset:7168
buffer_load_dword v10,s[36:39],s72 offen offset:0 sc1 lds
s_waitcnt vmcnt(10)
v_mfma_f32_16x16x32_bf16 v[32:35],a[4:7],v[44:47],v[32:35]
s_barrier
v_mfma_f32_16x16x32_bf16 v[28:31],a[8:11],v[48:51],v[28:31]
s_cmp_ge_u32 s66,s59
ds_read_b128 v[44:47],v2 offset:24576
v_fma_f32 v78,v98,v78,v97
v_mfma_f32_16x16x32_bf16 v[32:35],a[12:15],v[48:51],v[32:35]
s_cselect_b32 s86,s49,s50
v_exp_f32 v76,v76
v_mfma_f32_16x16x32_bf16 v[28:31],a[16:19],v[52:55],v[28:31]
s_cselect_b32 s87,s87,s63
ds_read_b128 v[48:51],v2 offset:25600
v_mul_f32 v78,v38,v78
v_mfma_f32_16x16x32_bf16 v[32:35],a[20:23],v[52:55],v[32:35]
s_mov_b32 s62,s87
v_exp_f32 v77,v77
v_mfma_f32_16x16x32_bf16 v[28:31],a[24:27],v[56:59],v[28:31]
s_mov_b32 m0,s62
ds_read_b128 v[52:55],v2 offset:26624
v_fma_f32 v76,v76,v99,v99
v_mfma_f32_16x16x32_bf16 v[32:35],a[28:31],v[56:59],v[32:35]
s_waitcnt lgkmcnt(3)
v_exp_f32 v78,v78
v_mfma_f32_16x16x32_bf16 v[28:31],a[32:35],v[60:63],v[28:31]
ds_read_b128 v[56:59],v2 offset:27648
v_fma_f32 v77,v77,v99,v99
v_mfma_f32_16x16x32_bf16 v[32:35],a[36:39],v[60:63],v[32:35]
v_rcp_f32 v76,v76
v_mfma_f32_16x16x32_bf16 v[28:31],a[40:43],v[64:67],v[28:31]
ds_read_b128 v[60:63],v2 offset:28672
v_fma_f32 v78,v78,v99,v99
v_mfma_f32_16x16x32_bf16 v[32:35],a[44:47],v[64:67],v[32:35]
v_rcp_f32 v77,v77
v_mfma_f32_16x16x32_bf16 v[28:31],a[48:51],v[68:71],v[28:31]
ds_read_b128 v[64:67],v2 offset:29696
v_fma_f32 v79,v98,v79,v97
v_mfma_f32_16x16x32_bf16 v[32:35],a[52:55],v[68:71],v[32:35]
v_rcp_f32 v78,v78
v_mfma_f32_16x16x32_bf16 v[28:31],a[56:59],v[72:75],v[28:31]
ds_read_b128 v[68:71],v2 offset:30720
v_mul_f32 v79,v39,v79
v_mfma_f32_16x16x32_bf16 v[32:35],a[60:63],v[72:75],v[32:35]
s_waitcnt lgkmcnt(3)
v_exp_f32 v79,v79
v_mfma_f32_16x16x32_bf16 v[28:31],a[192:195],v[44:47],v[28:31]
s_nop 0
ds_read_b128 v[72:75],v2 offset:31744
v_fma_f32 v79,v79,v99,v99
v_mfma_f32_16x16x32_bf16 v[32:35],a[196:199],v[44:47],v[32:35]
s_barrier
v_mfma_f32_16x16x32_bf16 v[28:31],a[200:203],v[48:51],v[28:31]
ds_read_b128 v[44:47],v2 offset:16384
ds_read_b32 v16,v7 offset:0
v_mfma_f32_16x16x32_bf16 v[32:35],a[204:207],v[48:51],v[32:35]
v_rcp_f32 v79,v79
v_mfma_f32_16x16x32_bf16 v[28:31],a[208:211],v[52:55],v[28:31]
ds_read_b128 v[48:51],v2 offset:17408
v_mul_f32 v36,v36,v76
v_mfma_f32_16x16x32_bf16 v[32:35],a[212:215],v[52:55],v[32:35]
v_mul_f32 v37,v37,v77
v_mul_f32 v38,v38,v78
v_mfma_f32_16x16x32_bf16 v[28:31],a[216:219],v[56:59],v[28:31]
s_waitcnt lgkmcnt(1)
ds_read_b128 v[52:55],v2 offset:18432
v_mul_f32 v39,v39,v79
v_mfma_f32_16x16x32_bf16 v[32:35],a[220:223],v[56:59],v[32:35]
v_cmp_eq_u32 vcc,s60,v16
v_cvt_pk_bf16_f32 v84,v36,v37
v_cvt_pk_bf16_f32 v85,v38,v39
v_fma_f32 v40,-v96,v24,v40
v_mfma_f32_16x16x32_bf16 v[28:31],a[224:227],v[60:63],v[28:31]
s_cmp_eq_u32 vcc_lo,-1
ds_read_b128 v[56:59],v2 offset:19456
v_fma_f32 v41,-v96,v25,v41
v_mfma_f32_16x16x32_bf16 v[32:35],a[228:231],v[60:63],v[32:35]
s_cselect_b32 s67,s86,s59
v_fma_f32 v42,-v96,v26,v42
v_fma_f32 v43,-v96,v27,v43
v_mfma_f32_16x16x32_bf16 v[28:31],a[232:235],v[64:67],v[28:31]
buffer_load_dwordx4 v0,s[36:39],s67 offen offset:0 sc1 lds
v_mfma_f32_16x16x32_bf16 v[32:35],a[236:239],v[64:67],v[32:35]
s_xor_b32 s63,s63,65536
ds_read_b128 v[60:63],v2 offset:20480
v_mul_f32 v80,v40,v40
v_mfma_f32_16x16x32_bf16 v[28:31],a[240:243],v[68:71],v[28:31]
s_add_u32 s53,s52,s55
ds_read_b128 v[64:67],v2 offset:21504
v_mul_f32 v81,v41,v41
v_mfma_f32_16x16x32_bf16 v[32:35],a[244:247],v[68:71],v[32:35]
s_cmp_eq_u32 s53,s56
v_mul_f32 v82,v42,v42
v_mul_f32 v83,v43,v43
v_mfma_f32_16x16x32_bf16 v[28:31],a[248:251],v[72:75],v[28:31]
buffer_load_dwordx4 v0,s[36:39],s67 offen offset:1024 sc1 lds
v_mfma_f32_16x16x32_bf16 v[32:35],a[252:255],v[72:75],v[32:35]
s_waitcnt lgkmcnt(2)
ds_read_b128 v[68:71],v2 offset:22528
v_fma_f32 v80,v98,v80,v97
v_mfma_f32_16x16x32_bf16 v[28:31],a[128:131],v[44:47],v[28:31]
s_waitcnt vmcnt(7)
ds_read_b128 v[72:75],v2 offset:23552
v_mul_f32 v80,v40,v80
v_mfma_f32_16x16x32_bf16 v[32:35],a[132:135],v[44:47],v[32:35]
s_barrier
v_mfma_f32_16x16x32_bf16 v[28:31],a[136:139],v[48:51],v[28:31]
s_cselect_b32 s53,s57,s53
ds_read_b128 v[44:47],v2 offset:40960
ds_read_b128 v[92:95],v8 offset:0
v_mfma_f32_16x16x32_bf16 v[32:35],a[140:143],v[48:51],v[32:35]
s_cmp_eq_u32 s53,s58
v_exp_f32 v80,v80)"
R"(
v_mfma_f32_16x16x32_bf16 v[28:31],a[144:147],v[52:55],v[28:31]
buffer_load_dwordx4 v0,s[36:39],s67 offen offset:2048 sc1 lds
v_mfma_f32_16x16x32_bf16 v[32:35],a[148:151],v[52:55],v[32:35]
s_cselect_b32 s53,s59,s53
ds_read_b128 v[48:51],v2 offset:41984
v_fma_f32 v80,v80,v99,v99
v_mfma_f32_16x16x32_bf16 v[28:31],a[152:155],v[56:59],v[28:31]
s_cmp_ge_u32 s66,s59
ds_read_b128 v[52:55],v2 offset:43008
v_fma_f32 v81,v98,v81,v97
v_mfma_f32_16x16x32_bf16 v[32:35],a[156:159],v[56:59],v[32:35]
s_waitcnt lgkmcnt(4)
v_fma_f32 v82,v98,v82,v97
v_fma_f32 v83,v98,v83,v97
v_mul_f32 v81,v41,v81
v_mul_f32 v82,v42,v82
v_mul_f32 v83,v43,v83
v_exp_f32 v81,v81
v_exp_f32 v82,v82
v_exp_f32 v83,v83
v_mfma_f32_16x16x32_bf16 v[28:31],a[160:163],v[60:63],v[28:31]
s_waitcnt lgkmcnt(2)
buffer_load_dwordx4 v0,s[36:39],s67 offen offset:3072 sc1 lds
v_fma_f32 v81,v81,v99,v99
v_fma_f32 v82,v82,v99,v99
v_fma_f32 v83,v83,v99,v99
v_rcp_f32 v80,v80
v_rcp_f32 v81,v81
v_rcp_f32 v82,v82
v_rcp_f32 v83,v83
v_mfma_f32_16x16x32_bf16 v[32:35],a[164:167],v[60:63],v[32:35]
ds_read_b128 v[56:59],v2 offset:44032
v_mul_f32 v40,v40,v80
v_mul_f32 v41,v41,v81
v_mul_f32 v42,v42,v82
v_mul_f32 v43,v43,v83
v_cvt_pk_bf16_f32 v86,v40,v41
v_cvt_pk_bf16_f32 v87,v42,v43
v_mfma_f32_16x16x32_bf16 v[28:31],a[168:171],v[64:67],v[28:31]
buffer_store_dwordx4 v[84:87],v3,s[36:39],s73 offen offset:0 sc0 sc1
ds_read_b128 v[60:63],v2 offset:45056
v_mfma_f32_16x16x32_bf16 v[32:35],a[172:175],v[64:67],v[32:35]
v_mfma_f32_16x16x32_bf16 v[28:31],a[176:179],v[68:71],v[28:31]
ds_read_b128 v[64:67],v2 offset:46080
v_mfma_f32_16x16x32_bf16 v[32:35],a[180:183],v[68:71],v[32:35]
v_mfma_f32_16x16x32_bf16 v[28:31],a[184:187],v[72:75],v[28:31]
s_waitcnt lgkmcnt(2)
ds_read_b128 v[68:71],v2 offset:47104
v_mfma_f32_16x16x32_bf16 v[32:35],a[188:191],v[72:75],v[32:35]
s_cbranch_scc1 Lg1_slow1_%=
Lg1_d1w2_%=:
v_mfma_f32_16x16x32_bf16 v[36:39],a[64:67],v[44:47],0
s_add_u32 m0,s62,16384
ds_read_b128 v[72:75],v2 offset:48128
s_lshr_b32 s73,s48,0
v_mfma_f32_16x16x32_bf16 v[40:43],a[68:71],v[44:47],0
s_lshr_b32 s72,s67,6
s_lshr_b32 s71,s62,2
s_cmp_lt_u32 s67,4194304
v_mfma_f32_16x16x32_bf16 v[36:39],a[72:75],v[48:51],v[36:39]
s_cselect_b32 s72,s59,s72
ds_read_b128 v[44:47],v2 offset:32768
s_mov_b64 s[44:45],s[46:47]
v_mfma_f32_16x16x32_bf16 v[40:43],a[76:79],v[48:51],v[40:43]
buffer_load_dwordx4 v1,s[36:39],s67 offen offset:0 sc1 lds
v_mfma_f32_16x16x32_bf16 v[36:39],a[80:83],v[52:55],v[36:39]
s_mov_b64 s[46:47],s[48:49]
ds_read_b128 v[48:51],v2 offset:33792
v_fma_f32 v28,-v92,v20,v28
v_mfma_f32_16x16x32_bf16 v[40:43],a[84:87],v[52:55],v[40:43]
s_mov_b64 s[48:49],s[50:51]
v_fma_f32 v29,-v92,v21,v29
v_fma_f32 v30,-v92,v22,v30
v_mfma_f32_16x16x32_bf16 v[36:39],a[88:91],v[56:59],v[36:39]
ds_read_b128 v[52:55],v2 offset:34816
v_fma_f32 v31,-v92,v23,v31
v_mfma_f32_16x16x32_bf16 v[40:43],a[92:95],v[56:59],v[40:43]
s_waitcnt lgkmcnt(3)
buffer_load_dwordx4 v1,s[36:39],s67 offen offset:1024 sc1 lds
v_mfma_f32_16x16x32_bf16 v[36:39],a[96:99],v[60:63],v[36:39]
ds_read_b128 v[56:59],v2 offset:35840
v_mul_f32 v76,v28,v28
v_mfma_f32_16x16x32_bf16 v[40:43],a[100:103],v[60:63],v[40:43]
v_mul_f32 v77,v29,v29
v_mul_f32 v78,v30,v30
v_mfma_f32_16x16x32_bf16 v[36:39],a[104:107],v[64:67],v[36:39]
ds_read_b128 v[60:63],v2 offset:36864
v_mul_f32 v79,v31,v31
v_mfma_f32_16x16x32_bf16 v[40:43],a[108:111],v[64:67],v[40:43]
buffer_load_dwordx4 v1,s[36:39],s67 offen offset:2048 sc1 lds
v_mfma_f32_16x16x32_bf16 v[36:39],a[112:115],v[68:71],v[36:39]
ds_read_b128 v[64:67],v2 offset:37888
v_fma_f32 v76,v94,v76,v93
v_mfma_f32_16x16x32_bf16 v[40:43],a[116:119],v[68:71],v[40:43]
v_mul_f32 v76,v28,v76
v_fma_f32 v77,v94,v77,v93
v_mfma_f32_16x16x32_bf16 v[36:39],a[120:123],v[72:75],v[36:39]
ds_read_b128 v[68:71],v2 offset:38912
v_mul_f32 v77,v29,v77
v_mfma_f32_16x16x32_bf16 v[40:43],a[124:127],v[72:75],v[40:43]
s_waitcnt lgkmcnt(3)
buffer_load_dwordx4 v1,s[36:39],s67 offen offset:3072 sc1 lds
s_add_u32 m0,s71,s88
v_mfma_f32_16x16x32_bf16 v[36:39],a[0:3],v[44:47],v[36:39]
s_xor_b32 s87,s63,65536
ds_read_b128 v[72:75],v2 offset:39936
buffer_load_dword v10,s[36:39],s72 offen offset:0 sc1 lds
s_waitcnt vmcnt(10)
v_mfma_f32_16x16x32_bf16 v[40:43],a[4:7],v[44:47],v[40:43]
s_barrier
v_mfma_f32_16x16x32_bf16 v[36:39],a[8:11],v[48:51],v[36:39]
s_cmp_ge_u32 s67,s59
ds_read_b128 v[44:47],v2 offset:57344
v_fma_f32 v78,v94,v78,v93
v_mfma_f32_16x16x32_bf16 v[40:43],a[12:15],v[48:51],v[40:43]
s_cselect_b32 s86,s50,s51
v_exp_f32 v76,v76
v_mfma_f32_16x16x32_bf16 v[36:39],a[16:19],v[52:55],v[36:39]
s_cselect_b32 s87,s87,s64
ds_read_b128 v[48:51],v2 offset:58368
v_mul_f32 v78,v30,v78
v_mfma_f32_16x16x32_bf16 v[40:43],a[20:23],v[52:55],v[40:43]
s_mov_b32 s62,s87
v_exp_f32 v77,v77
v_mfma_f32_16x16x32_bf16 v[36:39],a[24:27],v[56:59],v[36:39]
s_mov_b32 m0,s62
ds_read_b128 v[52:55],v2 offset:59392
v_fma_f32 v76,v76,v95,v95
v_mfma_f32_16x16x32_bf16 v[40:43],a[28:31],v[56:59],v[40:43]
s_waitcnt lgkmcnt(3)
v_exp_f32 v78,v78
v_mfma_f32_16x16x32_bf16 v[36:39],a[32:35],v[60:63],v[36:39]
s_mov_b64 s[50:51],s[52:53]
ds_read_b128 v[56:59],v2 offset:60416
v_fma_f32 v77,v77,v95,v95
v_mfma_f32_16x16x32_bf16 v[40:43],a[36:39],v[60:63],v[40:43]
v_rcp_f32 v76,v76
v_mfma_f32_16x16x32_bf16 v[36:39],a[40:43],v[64:67],v[36:39]
ds_read_b128 v[60:63],v2 offset:61440
v_fma_f32 v78,v78,v95,v95
v_mfma_f32_16x16x32_bf16 v[40:43],a[44:47],v[64:67],v[40:43]
v_rcp_f32 v77,v77
v_mfma_f32_16x16x32_bf16 v[36:39],a[48:51],v[68:71],v[36:39]
ds_read_b128 v[64:67],v2 offset:62464
v_fma_f32 v79,v94,v79,v93
v_mfma_f32_16x16x32_bf16 v[40:43],a[52:55],v[68:71],v[40:43]
v_rcp_f32 v78,v78
v_mfma_f32_16x16x32_bf16 v[36:39],a[56:59],v[72:75],v[36:39]
ds_read_b128 v[68:71],v2 offset:63488
v_mul_f32 v79,v31,v79
v_mfma_f32_16x16x32_bf16 v[40:43],a[60:63],v[72:75],v[40:43]
s_waitcnt lgkmcnt(3)
v_exp_f32 v79,v79
v_mfma_f32_16x16x32_bf16 v[36:39],a[192:195],v[44:47],v[36:39]
s_nop 0
ds_read_b128 v[72:75],v2 offset:64512
v_fma_f32 v79,v79,v95,v95
v_mfma_f32_16x16x32_bf16 v[40:43],a[196:199],v[44:47],v[40:43]
s_barrier
v_mfma_f32_16x16x32_bf16 v[36:39],a[200:203],v[48:51],v[36:39]
ds_read_b128 v[44:47],v2 offset:49152
ds_read_b32 v16,v7 offset:256
v_mfma_f32_16x16x32_bf16 v[40:43],a[204:207],v[48:51],v[40:43]
v_rcp_f32 v79,v79
v_mfma_f32_16x16x32_bf16 v[36:39],a[208:211],v[52:55],v[36:39]
ds_read_b128 v[48:51],v2 offset:50176
v_mul_f32 v28,v28,v76
v_mfma_f32_16x16x32_bf16 v[40:43],a[212:215],v[52:55],v[40:43]
v_mul_f32 v29,v29,v77
v_mul_f32 v30,v30,v78
v_mfma_f32_16x16x32_bf16 v[36:39],a[216:219],v[56:59],v[36:39]
s_waitcnt lgkmcnt(1)
ds_read_b128 v[52:55],v2 offset:51200
v_mul_f32 v31,v31,v79
v_mfma_f32_16x16x32_bf16 v[40:43],a[220:223],v[56:59],v[40:43]
v_cmp_eq_u32 vcc,s60,v16
v_cvt_pk_bf16_f32 v84,v28,v29
v_cvt_pk_bf16_f32 v85,v30,v31
v_fma_f32 v32,-v92,v24,v32
v_mfma_f32_16x16x32_bf16 v[36:39],a[224:227],v[60:63],v[36:39]
s_cmp_eq_u32 vcc_lo,-1
ds_read_b128 v[56:59],v2 offset:52224
v_fma_f32 v33,-v92,v25,v33
v_mfma_f32_16x16x32_bf16 v[40:43],a[228:231],v[60:63],v[40:43]
s_cselect_b32 s66,s86,s59
v_fma_f32 v34,-v92,v26,v34
v_fma_f32 v35,-v92,v27,v35
v_mfma_f32_16x16x32_bf16 v[36:39],a[232:235],v[64:67],v[36:39]
buffer_load_dwordx4 v0,s[36:39],s66 offen offset:0 sc1 lds
v_mfma_f32_16x16x32_bf16 v[40:43],a[236:239],v[64:67],v[40:43]
s_xor_b32 s64,s64,65536
ds_read_b128 v[60:63],v2 offset:53248
v_mul_f32 v80,v32,v32
v_mfma_f32_16x16x32_bf16 v[36:39],a[240:243],v[68:71],v[36:39]
s_add_u32 s74,s53,s55
ds_read_b128 v[64:67],v2 offset:54272
v_mul_f32 v81,v33,v33
v_mfma_f32_16x16x32_bf16 v[40:43],a[244:247],v[68:71],v[40:43]
s_cmp_eq_u32 s74,s56
v_mul_f32 v82,v34,v34)"
R"(
v_mul_f32 v83,v35,v35
v_mfma_f32_16x16x32_bf16 v[36:39],a[248:251],v[72:75],v[36:39]
buffer_load_dwordx4 v0,s[36:39],s66 offen offset:1024 sc1 lds
v_mfma_f32_16x16x32_bf16 v[40:43],a[252:255],v[72:75],v[40:43]
s_waitcnt lgkmcnt(2)
ds_read_b128 v[68:71],v2 offset:55296
v_fma_f32 v80,v94,v80,v93
v_mfma_f32_16x16x32_bf16 v[36:39],a[128:131],v[44:47],v[36:39]
s_waitcnt vmcnt(7)
ds_read_b128 v[72:75],v2 offset:56320
v_xor_b32 v2,65536,v2
v_mfma_f32_16x16x32_bf16 v[40:43],a[132:135],v[44:47],v[40:43]
s_barrier
v_mfma_f32_16x16x32_bf16 v[36:39],a[136:139],v[48:51],v[36:39]
s_cselect_b32 s74,s57,s74
ds_read_b128 v[44:47],v2 offset:8192
ds_read_b128 v[96:99],v8 offset:8192
v_mfma_f32_16x16x32_bf16 v[40:43],a[140:143],v[48:51],v[40:43]
s_cmp_eq_u32 s74,s58
v_mul_f32 v80,v32,v80
v_fma_f32 v81,v94,v81,v93
v_mfma_f32_16x16x32_bf16 v[36:39],a[144:147],v[52:55],v[36:39]
buffer_load_dwordx4 v0,s[36:39],s66 offen offset:2048 sc1 lds
v_mfma_f32_16x16x32_bf16 v[40:43],a[148:151],v[52:55],v[40:43]
s_cselect_b32 s74,s59,s74
ds_read_b128 v[48:51],v2 offset:9216
v_mul_f32 v81,v33,v81
v_mfma_f32_16x16x32_bf16 v[36:39],a[152:155],v[56:59],v[36:39]
s_mov_b32 s52,s74
ds_read_b128 v[52:55],v2 offset:10240
v_fma_f32 v82,v94,v82,v93
v_mfma_f32_16x16x32_bf16 v[40:43],a[156:159],v[56:59],v[40:43]
s_waitcnt lgkmcnt(4)
v_fma_f32 v83,v94,v83,v93
v_mul_f32 v82,v34,v82
v_mul_f32 v83,v35,v83
v_exp_f32 v80,v80
v_exp_f32 v81,v81
v_exp_f32 v82,v82
v_exp_f32 v83,v83
v_mfma_f32_16x16x32_bf16 v[36:39],a[160:163],v[60:63],v[36:39]
s_waitcnt lgkmcnt(2)
buffer_load_dwordx4 v0,s[36:39],s66 offen offset:3072 sc1 lds
v_fma_f32 v80,v80,v95,v95
v_fma_f32 v81,v81,v95,v95
v_fma_f32 v82,v82,v95,v95
v_fma_f32 v83,v83,v95,v95
v_rcp_f32 v80,v80
v_rcp_f32 v81,v81
v_rcp_f32 v82,v82
v_rcp_f32 v83,v83
v_mfma_f32_16x16x32_bf16 v[40:43],a[164:167],v[60:63],v[40:43]
s_cmp_lt_u32 s67,s59
ds_read_b128 v[56:59],v2 offset:11264
v_mul_f32 v32,v32,v80
v_mul_f32 v33,v33,v81
v_mul_f32 v34,v34,v82
v_mul_f32 v35,v35,v83
v_cvt_pk_bf16_f32 v86,v32,v33
v_cvt_pk_bf16_f32 v87,v34,v35
v_mfma_f32_16x16x32_bf16 v[36:39],a[168:171],v[64:67],v[36:39]
buffer_store_dwordx4 v[84:87],v3,s[36:39],s73 offen offset:0 sc0 sc1
ds_read_b128 v[60:63],v2 offset:12288
v_xor_b32 v8,16384,v8
v_mfma_f32_16x16x32_bf16 v[40:43],a[172:175],v[64:67],v[40:43]
v_mfma_f32_16x16x32_bf16 v[36:39],a[176:179],v[68:71],v[36:39]
ds_read_b128 v[64:67],v2 offset:13312
v_mfma_f32_16x16x32_bf16 v[40:43],a[180:183],v[68:71],v[40:43]
v_mfma_f32_16x16x32_bf16 v[36:39],a[184:187],v[72:75],v[36:39]
s_waitcnt lgkmcnt(2)
ds_read_b128 v[68:71],v2 offset:14336
v_mfma_f32_16x16x32_bf16 v[40:43],a[188:191],v[72:75],v[40:43]
s_cbranch_scc1 Lg1_d0w2_%=
s_branch Lg1_slow0_%=
Lg1_d0w3_%=:
v_mfma_f32_16x16x32_bf16 v[28:31],a[96:99],v[44:47],0
s_add_u32 m0,s62,16384
ds_read_b128 v[72:75],v2 offset:11264
s_lshr_b32 s73,s47,0
v_mfma_f32_16x16x32_bf16 v[32:35],a[100:103],v[44:47],0
s_lshr_b32 s72,s66,6
s_lshr_b32 s71,s62,2
s_cmp_lt_u32 s66,4194304
v_mfma_f32_16x16x32_bf16 v[28:31],a[104:107],v[48:51],v[28:31]
s_cselect_b32 s72,s59,s72
ds_read_b128 v[44:47],v2 offset:4096
v_mfma_f32_16x16x32_bf16 v[32:35],a[108:111],v[48:51],v[32:35]
v_fma_f32 v36,-v96,v20,v36
v_fma_f32 v37,-v96,v21,v37
v_mfma_f32_16x16x32_bf16 v[28:31],a[112:115],v[52:55],v[28:31]
buffer_load_dwordx4 v1,s[36:39],s66 offen offset:0 sc1 lds
v_mfma_f32_16x16x32_bf16 v[32:35],a[116:119],v[52:55],v[32:35]
ds_read_b128 v[48:51],v2 offset:5120
v_fma_f32 v38,-v96,v22,v38
v_mfma_f32_16x16x32_bf16 v[28:31],a[120:123],v[56:59],v[28:31]
ds_read_b128 v[52:55],v2 offset:6144
v_fma_f32 v39,-v96,v23,v39
v_mfma_f32_16x16x32_bf16 v[32:35],a[124:127],v[56:59],v[32:35]
s_waitcnt lgkmcnt(3)
v_mul_f32 v76,v36,v36
v_mul_f32 v77,v37,v37
v_mfma_f32_16x16x32_bf16 v[28:31],a[64:67],v[60:63],v[28:31]
buffer_load_dwordx4 v1,s[36:39],s66 offen offset:1024 sc1 lds
v_mfma_f32_16x16x32_bf16 v[32:35],a[68:71],v[60:63],v[32:35]
ds_read_b128 v[56:59],v2 offset:7168
v_mul_f32 v78,v38,v38
v_mfma_f32_16x16x32_bf16 v[28:31],a[72:75],v[64:67],v[28:31]
ds_read_b128 v[60:63],v2 offset:0
v_mul_f32 v79,v39,v39
v_mfma_f32_16x16x32_bf16 v[32:35],a[76:79],v[64:67],v[32:35]
v_fma_f32 v76,v98,v76,v97
v_mul_f32 v76,v36,v76
v_mfma_f32_16x16x32_bf16 v[28:31],a[80:83],v[68:71],v[28:31]
buffer_load_dwordx4 v1,s[36:39],s66 offen offset:2048 sc1 lds
v_mfma_f32_16x16x32_bf16 v[32:35],a[84:87],v[68:71],v[32:35]
ds_read_b128 v[64:67],v2 offset:1024
v_fma_f32 v77,v98,v77,v97
v_mfma_f32_16x16x32_bf16 v[28:31],a[88:91],v[72:75],v[28:31]
ds_read_b128 v[68:71],v2 offset:2048
v_mul_f32 v77,v37,v77
v_mfma_f32_16x16x32_bf16 v[32:35],a[92:95],v[72:75],v[32:35]
s_waitcnt lgkmcnt(3)
v_exp_f32 v76,v76
v_mfma_f32_16x16x32_bf16 v[28:31],a[32:35],v[44:47],v[28:31]
s_waitcnt vmcnt(9)
ds_read_b128 v[72:75],v2 offset:3072
v_fma_f32 v76,v76,v99,v99
v_mfma_f32_16x16x32_bf16 v[32:35],a[36:39],v[44:47],v[32:35]
s_barrier
v_mfma_f32_16x16x32_bf16 v[28:31],a[40:43],v[48:51],v[28:31]
buffer_load_dwordx4 v1,s[36:39],s66 offen offset:3072 sc1 lds
v_mfma_f32_16x16x32_bf16 v[32:35],a[44:47],v[48:51],v[32:35]
s_add_u32 m0,s71,s88
ds_read_b128 v[44:47],v2 offset:28672
v_fma_f32 v78,v98,v78,v97
v_mfma_f32_16x16x32_bf16 v[28:31],a[48:51],v[52:55],v[28:31]
buffer_load_dword v10,s[36:39],s72 offen offset:0 sc1 lds
v_mfma_f32_16x16x32_bf16 v[32:35],a[52:55],v[52:55],v[32:35]
s_xor_b32 s87,s64,65536
ds_read_b128 v[48:51],v2 offset:29696
v_fma_f32 v79,v98,v79,v97
v_mfma_f32_16x16x32_bf16 v[28:31],a[56:59],v[56:59],v[28:31]
s_cmp_ge_u32 s66,s59
ds_read_b128 v[52:55],v2 offset:30720
v_mul_f32 v78,v38,v78
v_mfma_f32_16x16x32_bf16 v[32:35],a[60:63],v[56:59],v[32:35]
s_waitcnt lgkmcnt(3)
v_exp_f32 v77,v77
v_mfma_f32_16x16x32_bf16 v[28:31],a[0:3],v[60:63],v[28:31]
s_cselect_b32 s86,s49,s50
ds_read_b128 v[56:59],v2 offset:31744
v_mul_f32 v79,v39,v79
v_mfma_f32_16x16x32_bf16 v[32:35],a[4:7],v[60:63],v[32:35]
s_cselect_b32 s87,s87,s63
v_exp_f32 v78,v78
v_mfma_f32_16x16x32_bf16 v[28:31],a[8:11],v[64:67],v[28:31]
s_mov_b32 s62,s87
ds_read_b128 v[60:63],v2 offset:24576
v_fma_f32 v77,v77,v99,v99
v_mfma_f32_16x16x32_bf16 v[32:35],a[12:15],v[64:67],v[32:35]
s_mov_b32 m0,s62
v_exp_f32 v79,v79
v_mfma_f32_16x16x32_bf16 v[28:31],a[16:19],v[68:71],v[28:31]
ds_read_b128 v[64:67],v2 offset:25600
v_fma_f32 v78,v78,v99,v99
v_mfma_f32_16x16x32_bf16 v[32:35],a[20:23],v[68:71],v[32:35]
v_rcp_f32 v76,v76
v_mfma_f32_16x16x32_bf16 v[28:31],a[24:27],v[72:75],v[28:31]
ds_read_b128 v[68:71],v2 offset:26624
v_fma_f32 v79,v79,v99,v99
v_mfma_f32_16x16x32_bf16 v[32:35],a[28:31],v[72:75],v[32:35]
s_waitcnt lgkmcnt(3)
v_rcp_f32 v77,v77
v_mfma_f32_16x16x32_bf16 v[28:31],a[224:227],v[44:47],v[28:31]
s_nop 0
ds_read_b128 v[72:75],v2 offset:27648
v_mul_f32 v36,v36,v76
v_mfma_f32_16x16x32_bf16 v[32:35],a[228:231],v[44:47],v[32:35]
s_barrier
v_mfma_f32_16x16x32_bf16 v[28:31],a[232:235],v[48:51],v[28:31]
ds_read_b128 v[44:47],v2 offset:20480
ds_read_b32 v16,v7 offset:0
v_mfma_f32_16x16x32_bf16 v[32:35],a[236:239],v[48:51],v[32:35]
v_rcp_f32 v78,v78
v_mfma_f32_16x16x32_bf16 v[28:31],a[240:243],v[52:55],v[28:31]
ds_read_b128 v[48:51],v2 offset:21504
v_mul_f32 v37,v37,v77
v_mfma_f32_16x16x32_bf16 v[32:35],a[244:247],v[52:55],v[32:35]
v_rcp_f32 v79,v79
v_mfma_f32_16x16x32_bf16 v[28:31],a[248:251],v[56:59],v[28:31]
s_waitcnt lgkmcnt(1)
ds_read_b128 v[52:55],v2 offset:22528
v_mul_f32 v38,v38,v78
v_mfma_f32_16x16x32_bf16 v[32:35],a[252:255],v[56:59],v[32:35]
v_cmp_eq_u32 vcc,s60,v16
v_mul_f32 v39,v39,v79
v_cvt_pk_bf16_f32 v84,v36,v37
v_cvt_pk_bf16_f32 v85,v38,v39
v_mfma_f32_16x16x32_bf16 v[28:31],a[192:195],v[60:63],v[28:31]
s_cmp_eq_u32 vcc_lo,-1)"
R"(
ds_read_b128 v[56:59],v2 offset:23552
v_fma_f32 v40,-v96,v24,v40
v_mfma_f32_16x16x32_bf16 v[32:35],a[196:199],v[60:63],v[32:35]
s_cselect_b32 s67,s86,s59
v_fma_f32 v41,-v96,v25,v41
v_fma_f32 v42,-v96,v26,v42
v_mfma_f32_16x16x32_bf16 v[28:31],a[200:203],v[64:67],v[28:31]
s_lshr_b32 s70,s46,5
ds_read_b128 v[60:63],v2 offset:16384
v_fma_f32 v43,-v96,v27,v43
v_mfma_f32_16x16x32_bf16 v[32:35],a[204:207],v[64:67],v[32:35]
buffer_load_dwordx4 v0,s[36:39],s67 offen offset:0 sc1 lds
v_mfma_f32_16x16x32_bf16 v[28:31],a[208:211],v[68:71],v[28:31]
s_xor_b32 s63,s63,65536
ds_read_b128 v[64:67],v2 offset:17408
v_mul_f32 v80,v40,v40
v_mfma_f32_16x16x32_bf16 v[32:35],a[212:215],v[68:71],v[32:35]
s_add_u32 s53,s52,s55
v_mul_f32 v81,v41,v41
v_mul_f32 v82,v42,v42
v_mfma_f32_16x16x32_bf16 v[28:31],a[216:219],v[72:75],v[28:31]
s_cmp_eq_u32 s53,s56
ds_read_b128 v[68:71],v2 offset:18432
v_mul_f32 v83,v43,v43
v_mfma_f32_16x16x32_bf16 v[32:35],a[220:223],v[72:75],v[32:35]
s_waitcnt lgkmcnt(3)
buffer_load_dwordx4 v0,s[36:39],s67 offen offset:1024 sc1 lds
v_mfma_f32_16x16x32_bf16 v[28:31],a[160:163],v[44:47],v[28:31]
s_waitcnt vmcnt(7)
ds_read_b128 v[72:75],v2 offset:19456
v_fma_f32 v80,v98,v80,v97
v_mfma_f32_16x16x32_bf16 v[32:35],a[164:167],v[44:47],v[32:35]
s_barrier
v_mfma_f32_16x16x32_bf16 v[28:31],a[168:171],v[48:51],v[28:31]
buffer_store_dword v5,v4,s[36:39],s70 offen offset:0 sc0 sc1
ds_read_b128 v[44:47],v2 offset:45056
ds_read_b128 v[92:95],v8 offset:0
v_mul_f32 v80,v40,v80
v_mfma_f32_16x16x32_bf16 v[32:35],a[172:175],v[48:51],v[32:35]
s_cselect_b32 s53,s57,s53
v_exp_f32 v80,v80
v_mfma_f32_16x16x32_bf16 v[28:31],a[176:179],v[52:55],v[28:31]
s_cmp_eq_u32 s53,s58
ds_read_b128 v[48:51],v2 offset:46080
v_fma_f32 v80,v80,v99,v99
v_mfma_f32_16x16x32_bf16 v[32:35],a[180:183],v[52:55],v[32:35]
buffer_load_dwordx4 v0,s[36:39],s67 offen offset:2048 sc1 lds
v_mfma_f32_16x16x32_bf16 v[28:31],a[184:187],v[56:59],v[28:31]
s_cselect_b32 s53,s59,s53
ds_read_b128 v[52:55],v2 offset:47104
v_fma_f32 v81,v98,v81,v97
v_mfma_f32_16x16x32_bf16 v[32:35],a[188:191],v[56:59],v[32:35]
s_waitcnt lgkmcnt(4)
v_rcp_f32 v80,v80
v_mfma_f32_16x16x32_bf16 v[28:31],a[128:131],v[60:63],v[28:31]
s_waitcnt lgkmcnt(2)
v_fma_f32 v82,v98,v82,v97
v_fma_f32 v83,v98,v83,v97
v_mul_f32 v81,v41,v81
v_mul_f32 v82,v42,v82
v_mul_f32 v83,v43,v83
v_exp_f32 v81,v81
v_exp_f32 v82,v82
v_exp_f32 v83,v83
v_mfma_f32_16x16x32_bf16 v[32:35],a[132:135],v[60:63],v[32:35]
s_cmp_ge_u32 s66,s59
ds_read_b128 v[56:59],v2 offset:48128
buffer_load_dwordx4 v0,s[36:39],s67 offen offset:3072 sc1 lds
v_fma_f32 v81,v81,v99,v99
v_fma_f32 v82,v82,v99,v99
v_fma_f32 v83,v83,v99,v99
v_rcp_f32 v81,v81
v_rcp_f32 v82,v82
v_rcp_f32 v83,v83
v_mfma_f32_16x16x32_bf16 v[28:31],a[136:139],v[64:67],v[28:31]
v_mul_f32 v40,v40,v80
v_mul_f32 v41,v41,v81
v_mul_f32 v42,v42,v82
v_mul_f32 v43,v43,v83
v_cvt_pk_bf16_f32 v86,v40,v41
v_cvt_pk_bf16_f32 v87,v42,v43
v_mfma_f32_16x16x32_bf16 v[32:35],a[140:143],v[64:67],v[32:35]
ds_read_b128 v[60:63],v2 offset:40960
buffer_store_dwordx4 v[84:87],v3,s[36:39],s73 offen offset:0 sc0 sc1
v_mfma_f32_16x16x32_bf16 v[28:31],a[144:147],v[68:71],v[28:31]
ds_read_b128 v[64:67],v2 offset:41984
v_mfma_f32_16x16x32_bf16 v[32:35],a[148:151],v[68:71],v[32:35]
v_mfma_f32_16x16x32_bf16 v[28:31],a[152:155],v[72:75],v[28:31]
s_waitcnt lgkmcnt(2)
ds_read_b128 v[68:71],v2 offset:43008
v_mfma_f32_16x16x32_bf16 v[32:35],a[156:159],v[72:75],v[32:35]
s_cbranch_scc1 Lg1_slow1_%=
Lg1_d1w3_%=:
v_mfma_f32_16x16x32_bf16 v[36:39],a[96:99],v[44:47],0
s_add_u32 m0,s62,16384
ds_read_b128 v[72:75],v2 offset:44032
s_lshr_b32 s73,s48,0
v_mfma_f32_16x16x32_bf16 v[40:43],a[100:103],v[44:47],0
s_lshr_b32 s72,s67,6
s_lshr_b32 s71,s62,2
s_cmp_lt_u32 s67,4194304
v_mfma_f32_16x16x32_bf16 v[36:39],a[104:107],v[48:51],v[36:39]
s_cselect_b32 s72,s59,s72
ds_read_b128 v[44:47],v2 offset:36864
s_mov_b64 s[44:45],s[46:47]
v_mfma_f32_16x16x32_bf16 v[40:43],a[108:111],v[48:51],v[40:43]
v_fma_f32 v28,-v92,v20,v28
v_fma_f32 v29,-v92,v21,v29
v_mfma_f32_16x16x32_bf16 v[36:39],a[112:115],v[52:55],v[36:39]
buffer_load_dwordx4 v1,s[36:39],s67 offen offset:0 sc1 lds
v_mfma_f32_16x16x32_bf16 v[40:43],a[116:119],v[52:55],v[40:43]
ds_read_b128 v[48:51],v2 offset:37888
v_fma_f32 v30,-v92,v22,v30
v_mfma_f32_16x16x32_bf16 v[36:39],a[120:123],v[56:59],v[36:39]
ds_read_b128 v[52:55],v2 offset:38912
v_fma_f32 v31,-v92,v23,v31
v_mfma_f32_16x16x32_bf16 v[40:43],a[124:127],v[56:59],v[40:43]
s_waitcnt lgkmcnt(3)
v_mul_f32 v76,v28,v28
v_mul_f32 v77,v29,v29
v_mfma_f32_16x16x32_bf16 v[36:39],a[64:67],v[60:63],v[36:39]
buffer_load_dwordx4 v1,s[36:39],s67 offen offset:1024 sc1 lds
v_mfma_f32_16x16x32_bf16 v[40:43],a[68:71],v[60:63],v[40:43]
ds_read_b128 v[56:59],v2 offset:39936
v_mul_f32 v78,v30,v30
v_mfma_f32_16x16x32_bf16 v[36:39],a[72:75],v[64:67],v[36:39]
ds_read_b128 v[60:63],v2 offset:32768
v_mul_f32 v79,v31,v31
v_mfma_f32_16x16x32_bf16 v[40:43],a[76:79],v[64:67],v[40:43]
v_fma_f32 v76,v94,v76,v93
v_mul_f32 v76,v28,v76
v_mfma_f32_16x16x32_bf16 v[36:39],a[80:83],v[68:71],v[36:39]
buffer_load_dwordx4 v1,s[36:39],s67 offen offset:2048 sc1 lds
v_mfma_f32_16x16x32_bf16 v[40:43],a[84:87],v[68:71],v[40:43]
ds_read_b128 v[64:67],v2 offset:33792
v_fma_f32 v77,v94,v77,v93
v_mfma_f32_16x16x32_bf16 v[36:39],a[88:91],v[72:75],v[36:39]
ds_read_b128 v[68:71],v2 offset:34816
v_mul_f32 v77,v29,v77
v_mfma_f32_16x16x32_bf16 v[40:43],a[92:95],v[72:75],v[40:43]
s_waitcnt lgkmcnt(3)
v_exp_f32 v76,v76
v_mfma_f32_16x16x32_bf16 v[36:39],a[32:35],v[44:47],v[36:39]
s_waitcnt vmcnt(9)
ds_read_b128 v[72:75],v2 offset:35840
v_fma_f32 v76,v76,v95,v95
v_mfma_f32_16x16x32_bf16 v[40:43],a[36:39],v[44:47],v[40:43]
s_barrier
v_mfma_f32_16x16x32_bf16 v[36:39],a[40:43],v[48:51],v[36:39]
buffer_load_dwordx4 v1,s[36:39],s67 offen offset:3072 sc1 lds
v_mfma_f32_16x16x32_bf16 v[40:43],a[44:47],v[48:51],v[40:43]
s_add_u32 m0,s71,s88
ds_read_b128 v[44:47],v2 offset:61440
v_fma_f32 v78,v94,v78,v93
v_mfma_f32_16x16x32_bf16 v[36:39],a[48:51],v[52:55],v[36:39]
buffer_load_dword v10,s[36:39],s72 offen offset:0 sc1 lds
v_mfma_f32_16x16x32_bf16 v[40:43],a[52:55],v[52:55],v[40:43]
s_xor_b32 s87,s63,65536
ds_read_b128 v[48:51],v2 offset:62464
v_fma_f32 v79,v94,v79,v93
v_mfma_f32_16x16x32_bf16 v[36:39],a[56:59],v[56:59],v[36:39]
s_cmp_ge_u32 s67,s59
ds_read_b128 v[52:55],v2 offset:63488
v_mul_f32 v78,v30,v78
v_mfma_f32_16x16x32_bf16 v[40:43],a[60:63],v[56:59],v[40:43]
s_waitcnt lgkmcnt(3)
v_exp_f32 v77,v77
v_mfma_f32_16x16x32_bf16 v[36:39],a[0:3],v[60:63],v[36:39]
s_cselect_b32 s86,s50,s51
ds_read_b128 v[56:59],v2 offset:64512
v_mul_f32 v79,v31,v79
v_mfma_f32_16x16x32_bf16 v[40:43],a[4:7],v[60:63],v[40:43]
s_cselect_b32 s87,s87,s64
v_exp_f32 v78,v78
v_mfma_f32_16x16x32_bf16 v[36:39],a[8:11],v[64:67],v[36:39]
s_mov_b32 s62,s87
ds_read_b128 v[60:63],v2 offset:57344
v_fma_f32 v77,v77,v95,v95
v_mfma_f32_16x16x32_bf16 v[40:43],a[12:15],v[64:67],v[40:43]
s_mov_b32 m0,s62
v_exp_f32 v79,v79
v_mfma_f32_16x16x32_bf16 v[36:39],a[16:19],v[68:71],v[36:39]
ds_read_b128 v[64:67],v2 offset:58368
v_fma_f32 v78,v78,v95,v95
v_mfma_f32_16x16x32_bf16 v[40:43],a[20:23],v[68:71],v[40:43]
v_rcp_f32 v76,v76
v_mfma_f32_16x16x32_bf16 v[36:39],a[24:27],v[72:75],v[36:39]
ds_read_b128 v[68:71],v2 offset:59392
v_fma_f32 v79,v79,v95,v95
v_mfma_f32_16x16x32_bf16 v[40:43],a[28:31],v[72:75],v[40:43]
s_waitcnt lgkmcnt(3)
v_rcp_f32 v77,v77
v_mfma_f32_16x16x32_bf16 v[36:39],a[224:227],v[44:47],v[36:39]
s_nop 0
ds_read_b128 v[72:75],v2 offset:60416
v_mul_f32 v28,v28,v76
v_mfma_f32_16x16x32_bf16 v[40:43],a[228:231],v[44:47],v[40:43]
s_barrier
v_mfma_f32_16x16x32_bf16 v[36:39],a[232:235],v[48:51],v[36:39]
ds_read_b128 v[44:47],v2 offset:53248)"
R"(
ds_read_b32 v16,v7 offset:256
v_mfma_f32_16x16x32_bf16 v[40:43],a[236:239],v[48:51],v[40:43]
v_rcp_f32 v78,v78
v_mfma_f32_16x16x32_bf16 v[36:39],a[240:243],v[52:55],v[36:39]
ds_read_b128 v[48:51],v2 offset:54272
v_mul_f32 v29,v29,v77
v_mfma_f32_16x16x32_bf16 v[40:43],a[244:247],v[52:55],v[40:43]
v_rcp_f32 v79,v79
v_mfma_f32_16x16x32_bf16 v[36:39],a[248:251],v[56:59],v[36:39]
s_waitcnt lgkmcnt(1)
ds_read_b128 v[52:55],v2 offset:55296
v_mul_f32 v30,v30,v78
v_mfma_f32_16x16x32_bf16 v[40:43],a[252:255],v[56:59],v[40:43]
v_cmp_eq_u32 vcc,s60,v16
v_mul_f32 v31,v31,v79
v_cvt_pk_bf16_f32 v84,v28,v29
v_cvt_pk_bf16_f32 v85,v30,v31
v_mfma_f32_16x16x32_bf16 v[36:39],a[192:195],v[60:63],v[36:39]
s_cmp_eq_u32 vcc_lo,-1
ds_read_b128 v[56:59],v2 offset:56320
v_fma_f32 v32,-v92,v24,v32
v_mfma_f32_16x16x32_bf16 v[40:43],a[196:199],v[60:63],v[40:43]
s_cselect_b32 s66,s86,s59
v_fma_f32 v33,-v92,v25,v33
v_fma_f32 v34,-v92,v26,v34
v_mfma_f32_16x16x32_bf16 v[36:39],a[200:203],v[64:67],v[36:39]
s_lshr_b32 s70,s47,5
ds_read_b128 v[60:63],v2 offset:49152
v_fma_f32 v35,-v92,v27,v35
v_mfma_f32_16x16x32_bf16 v[40:43],a[204:207],v[64:67],v[40:43]
buffer_load_dwordx4 v0,s[36:39],s66 offen offset:0 sc1 lds
v_mfma_f32_16x16x32_bf16 v[36:39],a[208:211],v[68:71],v[36:39]
s_xor_b32 s64,s64,65536
ds_read_b128 v[64:67],v2 offset:50176
v_mul_f32 v80,v32,v32
v_mfma_f32_16x16x32_bf16 v[40:43],a[212:215],v[68:71],v[40:43]
s_add_u32 s74,s53,s55
v_mul_f32 v81,v33,v33
v_mul_f32 v82,v34,v34
v_mfma_f32_16x16x32_bf16 v[36:39],a[216:219],v[72:75],v[36:39]
s_cmp_eq_u32 s74,s56
ds_read_b128 v[68:71],v2 offset:51200
v_mul_f32 v83,v35,v35
v_mfma_f32_16x16x32_bf16 v[40:43],a[220:223],v[72:75],v[40:43]
s_waitcnt lgkmcnt(3)
buffer_load_dwordx4 v0,s[36:39],s66 offen offset:1024 sc1 lds
v_mfma_f32_16x16x32_bf16 v[36:39],a[160:163],v[44:47],v[36:39]
s_waitcnt vmcnt(7)
ds_read_b128 v[72:75],v2 offset:52224
v_xor_b32 v2,65536,v2
v_mfma_f32_16x16x32_bf16 v[40:43],a[164:167],v[44:47],v[40:43]
s_barrier
v_mfma_f32_16x16x32_bf16 v[36:39],a[168:171],v[48:51],v[36:39]
buffer_store_dword v5,v4,s[36:39],s70 offen offset:0 sc0 sc1
ds_read_b128 v[44:47],v2 offset:12288
ds_read_b128 v[96:99],v8 offset:8192
v_fma_f32 v80,v94,v80,v93
v_mfma_f32_16x16x32_bf16 v[40:43],a[172:175],v[48:51],v[40:43]
s_cselect_b32 s74,s57,s74
v_mul_f32 v80,v32,v80
v_fma_f32 v81,v94,v81,v93
v_mfma_f32_16x16x32_bf16 v[36:39],a[176:179],v[52:55],v[36:39]
s_cmp_eq_u32 s74,s58
ds_read_b128 v[48:51],v2 offset:13312
v_mul_f32 v81,v33,v81
v_mfma_f32_16x16x32_bf16 v[40:43],a[180:183],v[52:55],v[40:43]
buffer_load_dwordx4 v0,s[36:39],s66 offen offset:2048 sc1 lds
v_mfma_f32_16x16x32_bf16 v[36:39],a[184:187],v[56:59],v[36:39]
s_cselect_b32 s74,s59,s74
ds_read_b128 v[52:55],v2 offset:14336
v_fma_f32 v82,v94,v82,v93
v_mfma_f32_16x16x32_bf16 v[40:43],a[188:191],v[56:59],v[40:43]
s_waitcnt lgkmcnt(4)
v_exp_f32 v80,v80
v_mfma_f32_16x16x32_bf16 v[36:39],a[128:131],v[60:63],v[36:39]
s_waitcnt lgkmcnt(2)
v_fma_f32 v83,v94,v83,v93
v_mul_f32 v82,v34,v82
v_mul_f32 v83,v35,v83
v_exp_f32 v81,v81
v_exp_f32 v82,v82
v_exp_f32 v83,v83
v_mfma_f32_16x16x32_bf16 v[40:43],a[132:135],v[60:63],v[40:43]
s_mov_b64 s[46:47],s[48:49]
ds_read_b128 v[56:59],v2 offset:15360
buffer_load_dwordx4 v0,s[36:39],s66 offen offset:3072 sc1 lds
v_fma_f32 v80,v80,v95,v95
v_fma_f32 v81,v81,v95,v95
v_fma_f32 v82,v82,v95,v95
v_fma_f32 v83,v83,v95,v95
v_rcp_f32 v80,v80
v_rcp_f32 v81,v81
v_rcp_f32 v82,v82
v_rcp_f32 v83,v83
v_mfma_f32_16x16x32_bf16 v[36:39],a[136:139],v[64:67],v[36:39]
s_mov_b64 s[48:49],s[50:51]
v_mul_f32 v32,v32,v80
v_mul_f32 v33,v33,v81
v_mul_f32 v34,v34,v82
v_mul_f32 v35,v35,v83
v_cvt_pk_bf16_f32 v86,v32,v33
v_cvt_pk_bf16_f32 v87,v34,v35
v_mfma_f32_16x16x32_bf16 v[40:43],a[140:143],v[64:67],v[40:43]
s_mov_b64 s[50:51],s[52:53]
ds_read_b128 v[60:63],v2 offset:8192
buffer_store_dwordx4 v[84:87],v3,s[36:39],s73 offen offset:0 sc0 sc1
v_xor_b32 v8,16384,v8
s_mov_b32 s52,s74
s_cmp_lt_u32 s67,s59
v_mfma_f32_16x16x32_bf16 v[36:39],a[144:147],v[68:71],v[36:39]
ds_read_b128 v[64:67],v2 offset:9216
v_mfma_f32_16x16x32_bf16 v[40:43],a[148:151],v[68:71],v[40:43]
v_mfma_f32_16x16x32_bf16 v[36:39],a[152:155],v[72:75],v[36:39]
s_waitcnt lgkmcnt(2)
ds_read_b128 v[68:71],v2 offset:10240
v_mfma_f32_16x16x32_bf16 v[40:43],a[156:159],v[72:75],v[40:43]
s_cbranch_scc1 Lg1_d0w3_%=
s_branch Lg1_slow0_%=
Lg1_slow0_%=:
s_cmp_lt_u32 s66,s59
s_cbranch_scc1 Lg1_gs0_%=
s_cmp_eq_u32 s54,1
s_cselect_b32 s68,s49,s48
s_lshr_b32 s69,s68,5
s_cmp_lt_u32 s68,4194304
s_cselect_b32 s68,0xffffcfff,-1
s_and_b32 s68,s68,s69
s_add_u32 m0,s78,8192
s_nop 0
buffer_load_dword v6,s[36:39],s68 offen offset:0 sc1 lds
s_lshr_b32 s73,s47,0
v_fma_f32 v36,-v96,v20,v36
v_fma_f32 v37,-v96,v21,v37
v_fma_f32 v38,-v96,v22,v38
v_fma_f32 v39,-v96,v23,v39
v_mul_f32 v76,v36,v36
v_mul_f32 v77,v37,v37
v_mul_f32 v78,v38,v38
v_mul_f32 v79,v39,v39
v_fma_f32 v76,v98,v76,v97
v_fma_f32 v77,v98,v77,v97
v_fma_f32 v78,v98,v78,v97
v_fma_f32 v79,v98,v79,v97
v_mul_f32 v76,v36,v76
v_mul_f32 v77,v37,v77
v_mul_f32 v78,v38,v78
v_mul_f32 v79,v39,v79
v_exp_f32 v76,v76
v_exp_f32 v77,v77
v_exp_f32 v78,v78
v_exp_f32 v79,v79
v_fma_f32 v76,v76,v99,v99
v_fma_f32 v77,v77,v99,v99
v_fma_f32 v78,v78,v99,v99
v_fma_f32 v79,v79,v99,v99
v_rcp_f32 v76,v76
v_rcp_f32 v77,v77
v_rcp_f32 v78,v78
v_rcp_f32 v79,v79
v_mul_f32 v36,v36,v76
v_mul_f32 v37,v37,v77
v_mul_f32 v38,v38,v78
v_mul_f32 v39,v39,v79
v_cvt_pk_bf16_f32 v84,v36,v37
v_cvt_pk_bf16_f32 v85,v38,v39
s_add_u32 m0,s78,8448
s_nop 0
buffer_load_dword v6,s[36:39],s68 offen offset:0 sc1 lds
v_fma_f32 v40,-v96,v24,v40
v_fma_f32 v41,-v96,v25,v41
v_fma_f32 v42,-v96,v26,v42
v_fma_f32 v43,-v96,v27,v43
v_mul_f32 v80,v40,v40
v_mul_f32 v81,v41,v41
v_mul_f32 v82,v42,v42
v_mul_f32 v83,v43,v43
v_fma_f32 v80,v98,v80,v97
v_fma_f32 v81,v98,v81,v97
v_fma_f32 v82,v98,v82,v97
v_fma_f32 v83,v98,v83,v97
v_mul_f32 v80,v40,v80
v_mul_f32 v81,v41,v81
v_mul_f32 v82,v42,v82
v_mul_f32 v83,v43,v83
v_exp_f32 v80,v80
v_exp_f32 v81,v81
v_exp_f32 v82,v82
v_exp_f32 v83,v83
v_fma_f32 v80,v80,v99,v99
v_fma_f32 v81,v81,v99,v99
v_fma_f32 v82,v82,v99,v99
v_fma_f32 v83,v83,v99,v99
v_rcp_f32 v80,v80
v_rcp_f32 v81,v81
v_rcp_f32 v82,v82
v_rcp_f32 v83,v83
v_mul_f32 v40,v40,v80
v_mul_f32 v41,v41,v81
v_mul_f32 v42,v42,v82
v_mul_f32 v43,v43,v83)"
R"(
v_cvt_pk_bf16_f32 v86,v40,v41
v_cvt_pk_bf16_f32 v87,v42,v43
buffer_store_dwordx4 v[84:87],v3,s[36:39],s73 offen offset:0 sc0 sc1
s_cmp_ge_u32 s48,s59
s_cbranch_scc1 Lg1_done0_%=
s_add_u32 m0,s78,16384
s_nop 0
buffer_load_dword v6,s[36:39],s68 offen offset:0 sc1 lds
s_waitcnt vmcnt(3)
s_barrier
ds_read_b32 v16,v7 offset:8192
s_lshr_b32 s70,s46,5
buffer_store_dword v5,v4,s[36:39],s70 offen offset:0 sc0 sc1
s_mov_b32 s46,s59
s_mov_b32 s69,18432
s_waitcnt lgkmcnt(0)
v_cmp_eq_u32 vcc,s60,v16
s_nop 0
s_cmp_eq_u32 vcc_lo,-1
s_cbranch_scc1 Lg1_go0_%=
s_add_u32 m0,s78,16640
s_nop 0
buffer_load_dword v6,s[36:39],s68 offen offset:0 sc1 lds
s_waitcnt vmcnt(4)
s_barrier
ds_read_b32 v16,v7 offset:8448
s_mov_b32 s69,18688
s_waitcnt lgkmcnt(0)
v_cmp_eq_u32 vcc,s60,v16
s_nop 0
s_cmp_eq_u32 vcc_lo,-1
s_cbranch_scc1 Lg1_go0_%=
s_add_u32 m0,s78,8192
s_nop 0
buffer_load_dword v6,s[36:39],s68 offen offset:0 sc1 lds
s_waitcnt vmcnt(3)
s_barrier
ds_read_b32 v16,v7 offset:16384
s_lshr_b32 s70,s47,5
buffer_store_dword v5,v4,s[36:39],s70 offen offset:0 sc0 sc1
s_mov_b32 s47,s59
s_mov_b32 s69,10240
s_waitcnt lgkmcnt(0)
v_cmp_eq_u32 vcc,s60,v16
s_nop 0
s_cmp_eq_u32 vcc_lo,-1
s_cbranch_scc1 Lg1_go0_%=
s_add_u32 m0,s78,8448
s_nop 0
buffer_load_dword v6,s[36:39],s68 offen offset:0 sc1 lds
s_waitcnt vmcnt(3)
s_barrier
ds_read_b32 v16,v7 offset:16640
s_mov_b32 s69,10496
s_waitcnt lgkmcnt(0)
v_cmp_eq_u32 vcc,s60,v16
s_nop 0
s_cmp_eq_u32 vcc_lo,-1
s_cbranch_scc1 Lg1_go0_%=
Lg1_poll0_%=:
s_add_u32 m0,s78,16384
s_nop 0
buffer_load_dword v6,s[36:39],s68 offen offset:0 sc1 lds
s_waitcnt vmcnt(2)
s_barrier
ds_read_b32 v16,v7 offset:8192
s_mov_b32 s69,18432
s_waitcnt lgkmcnt(0)
v_cmp_eq_u32 vcc,s60,v16
s_nop 0
s_cmp_eq_u32 vcc_lo,-1
s_cbranch_scc1 Lg1_go0_%=
s_sleep 4
s_add_u32 m0,s78,16640
s_nop 0
buffer_load_dword v6,s[36:39],s68 offen offset:0 sc1 lds
s_waitcnt vmcnt(2)
s_barrier
ds_read_b32 v16,v7 offset:8448
s_mov_b32 s69,18688
s_waitcnt lgkmcnt(0)
v_cmp_eq_u32 vcc,s60,v16
s_nop 0
s_cmp_eq_u32 vcc_lo,-1
s_cbranch_scc1 Lg1_go0_%=
s_sleep 4
s_add_u32 m0,s78,8192
s_nop 0
buffer_load_dword v6,s[36:39],s68 offen offset:0 sc1 lds
s_waitcnt vmcnt(2)
s_barrier
ds_read_b32 v16,v7 offset:16384
s_mov_b32 s69,10240
s_waitcnt lgkmcnt(0)
v_cmp_eq_u32 vcc,s60,v16
s_nop 0
s_cmp_eq_u32 vcc_lo,-1
s_cbranch_scc1 Lg1_go0_%=
s_sleep 4
s_add_u32 m0,s78,8448
s_nop 0
buffer_load_dword v6,s[36:39],s68 offen offset:0 sc1 lds
s_waitcnt vmcnt(2)
s_barrier
ds_read_b32 v16,v7 offset:16640
s_mov_b32 s69,10496
s_waitcnt lgkmcnt(0)
v_cmp_eq_u32 vcc,s60,v16
s_nop 0
s_cmp_eq_u32 vcc_lo,-1
s_cbranch_scc1 Lg1_go0_%=
s_sleep 4
s_add_u32 s79,s79,1
s_cmp_lt_u32 s79,20000
s_cbranch_scc1 Lg1_poll0_%=
Lg1_go0_%=:
s_xor_b32 s62,s63,65536
s_mov_b32 m0,s62
s_nop 0
buffer_load_dwordx4 v0,s[36:39],s48 offen offset:0 sc1 lds
buffer_load_dwordx4 v0,s[36:39],s48 offen offset:1024 sc1 lds
buffer_load_dwordx4 v0,s[36:39],s48 offen offset:2048 sc1 lds
buffer_load_dwordx4 v0,s[36:39],s48 offen offset:3072 sc1 lds
s_add_u32 m0,s62,16384
s_nop 0
buffer_load_dwordx4 v1,s[36:39],s48 offen offset:0 sc1 lds
buffer_load_dwordx4 v1,s[36:39],s48 offen offset:1024 sc1 lds
buffer_load_dwordx4 v1,s[36:39],s48 offen offset:2048 sc1 lds
buffer_load_dwordx4 v1,s[36:39],s48 offen offset:3072 sc1 lds
s_lshr_b32 s72,s48,6
s_lshr_b32 s71,s62,2
s_cmp_lt_u32 s48,4194304
s_cselect_b32 s72,s59,s72
s_add_u32 m0,s71,s88
s_nop 0
buffer_load_dword v10,s[36:39],s72 offen offset:0 sc1 lds
s_cmp_eq_u32 vcc_hi,-1
s_cselect_b32 s66,s49,s59
s_xor_b32 s62,s64,65536
s_cmp_ge_u32 s66,s59
s_cselect_b32 s68,s49,s50
s_lshr_b32 s69,s68,5
s_cmp_lt_u32 s68,4194304
s_cselect_b32 s68,0xffffcfff,-1
s_and_b32 s68,s68,s69
s_add_u32 m0,s78,0
s_nop 0
buffer_load_dword v6,s[36:39],s68 offen offset:0 sc1 lds
s_mov_b32 m0,s62
s_nop 0
buffer_load_dwordx4 v0,s[36:39],s66 offen offset:0 sc1 lds
buffer_load_dwordx4 v0,s[36:39],s66 offen offset:1024 sc1 lds
buffer_load_dwordx4 v0,s[36:39],s66 offen offset:2048 sc1 lds
buffer_load_dwordx4 v0,s[36:39],s66 offen offset:3072 sc1 lds
s_waitcnt vmcnt(10)
s_barrier
s_lshl_b32 s70,s54,12
v_add_u32 v76,s70,v2
v_xor_b32 v77,4096,v76
ds_read_b128 v[44:47],v76 offset:0
ds_read_b128 v[48:51],v76 offset:1024
ds_read_b128 v[52:55],v76 offset:2048
ds_read_b128 v[56:59],v76 offset:3072
ds_read_b128 v[60:63],v77 offset:0
ds_read_b128 v[64:67],v77 offset:1024
ds_read_b128 v[68:71],v77 offset:2048
s_lshr_b32 s70,s46,5
buffer_store_dword v5,v4,s[36:39],s70 offen offset:0 sc0 sc1
s_mov_b32 s46,s59
s_lshr_b32 s70,s47,5
buffer_store_dword v5,v4,s[36:39],s70 offen offset:0 sc0 sc1
s_mov_b32 s47,s59
s_lshr_b32 s70,s59,5
buffer_store_dword v5,v4,s[36:39],s70 offen offset:0 sc0 sc1
s_waitcnt lgkmcnt(3)
s_setpc_b64 s[90:91]
Lg1_gs0_%=:
s_cmp_eq_u32 s54,1
s_cselect_b32 s68,s49,s48
s_lshr_b32 s69,s68,5
s_cmp_lt_u32 s68,4194304
s_cselect_b32 s68,0xffffcfff,-1
s_and_b32 s68,s68,s69
s_lshr_b32 s73,s47,0
s_add_u32 m0,s78,8192
s_nop 0
buffer_load_dword v6,s[36:39],s68 offen offset:0 sc1 lds
v_fma_f32 v36,-v96,v20,v36
v_fma_f32 v37,-v96,v21,v37
v_fma_f32 v38,-v96,v22,v38
v_fma_f32 v39,-v96,v23,v39
v_mul_f32 v76,v36,v36
v_mul_f32 v77,v37,v37
v_mul_f32 v78,v38,v38
v_mul_f32 v79,v39,v39
v_fma_f32 v76,v98,v76,v97
v_fma_f32 v77,v98,v77,v97
v_fma_f32 v78,v98,v78,v97
s_add_u32 m0,s62,16384
s_nop 0)"
R"(
buffer_load_dwordx4 v1,s[36:39],s66 offen offset:0 sc1 lds
v_fma_f32 v79,v98,v79,v97
v_mul_f32 v76,v36,v76
v_mul_f32 v77,v37,v77
v_mul_f32 v78,v38,v78
v_mul_f32 v79,v39,v79
v_exp_f32 v76,v76
v_exp_f32 v77,v77
v_exp_f32 v78,v78
v_exp_f32 v79,v79
v_fma_f32 v76,v76,v99,v99
v_fma_f32 v77,v77,v99,v99
buffer_load_dwordx4 v1,s[36:39],s66 offen offset:1024 sc1 lds
v_fma_f32 v78,v78,v99,v99
v_fma_f32 v79,v79,v99,v99
v_rcp_f32 v76,v76
v_rcp_f32 v77,v77
v_rcp_f32 v78,v78
v_rcp_f32 v79,v79
v_mul_f32 v36,v36,v76
v_mul_f32 v37,v37,v77
v_mul_f32 v38,v38,v78
v_mul_f32 v39,v39,v79
v_cvt_pk_bf16_f32 v84,v36,v37
buffer_load_dwordx4 v1,s[36:39],s66 offen offset:2048 sc1 lds
v_cvt_pk_bf16_f32 v85,v38,v39
v_fma_f32 v40,-v96,v24,v40
v_fma_f32 v41,-v96,v25,v41
v_fma_f32 v42,-v96,v26,v42
v_fma_f32 v43,-v96,v27,v43
v_mul_f32 v80,v40,v40
v_mul_f32 v81,v41,v41
v_mul_f32 v82,v42,v42
v_mul_f32 v83,v43,v43
v_fma_f32 v80,v98,v80,v97
v_fma_f32 v81,v98,v81,v97
buffer_load_dwordx4 v1,s[36:39],s66 offen offset:3072 sc1 lds
v_fma_f32 v82,v98,v82,v97
v_fma_f32 v83,v98,v83,v97
v_mul_f32 v80,v40,v80
v_mul_f32 v81,v41,v81
v_mul_f32 v82,v42,v82
v_mul_f32 v83,v43,v83
v_exp_f32 v80,v80
v_exp_f32 v81,v81
v_exp_f32 v82,v82
v_exp_f32 v83,v83
v_fma_f32 v80,v80,v99,v99
s_lshr_b32 s72,s66,6
s_lshr_b32 s71,s62,2
s_cmp_lt_u32 s66,4194304
s_cselect_b32 s72,s59,s72
s_add_u32 m0,s71,s88
s_nop 0
buffer_load_dword v10,s[36:39],s72 offen offset:0 sc1 lds
v_fma_f32 v81,v81,v99,v99
v_fma_f32 v82,v82,v99,v99
v_fma_f32 v83,v83,v99,v99
v_rcp_f32 v80,v80
v_rcp_f32 v81,v81
v_rcp_f32 v82,v82
v_rcp_f32 v83,v83
v_mul_f32 v40,v40,v80
v_mul_f32 v41,v41,v81
v_mul_f32 v42,v42,v82
v_mul_f32 v43,v43,v83
v_cvt_pk_bf16_f32 v86,v40,v41
v_cvt_pk_bf16_f32 v87,v42,v43
buffer_store_dwordx4 v[84:87],v3,s[36:39],s73 offen offset:0 sc0 sc1
s_waitcnt vmcnt(6)
s_barrier
ds_read_b32 v16,v7 offset:8192
s_lshl_b32 s70,s54,12
v_add_u32 v76,s70,v2
v_xor_b32 v77,4096,v76
ds_read_b128 v[44:47],v76 offset:0
ds_read_b128 v[48:51],v76 offset:1024
ds_read_b128 v[52:55],v76 offset:2048
ds_read_b128 v[56:59],v76 offset:3072
ds_read_b128 v[60:63],v77 offset:0
ds_read_b128 v[64:67],v77 offset:1024
ds_read_b128 v[68:71],v77 offset:2048
s_lshr_b32 s70,s46,5
buffer_store_dword v5,v4,s[36:39],s70 offen offset:0 sc0 sc1
s_waitcnt lgkmcnt(7)
v_cmp_eq_u32 vcc,s60,v16
s_nop 0
s_cmp_eq_u32 vcc_hi,-1
s_cselect_b32 s66,s49,s59
s_xor_b32 s62,s64,65536
s_cmp_ge_u32 s66,s59
s_cselect_b32 s68,s49,s50
s_lshr_b32 s69,s68,5
s_cmp_lt_u32 s68,4194304
s_cselect_b32 s68,0xffffcfff,-1
s_and_b32 s68,s68,s69
s_add_u32 m0,s78,0
s_nop 0
buffer_load_dword v6,s[36:39],s68 offen offset:0 sc1 lds
s_mov_b32 m0,s62
s_nop 0
buffer_load_dwordx4 v0,s[36:39],s66 offen offset:0 sc1 lds
buffer_load_dwordx4 v0,s[36:39],s66 offen offset:1024 sc1 lds
buffer_load_dwordx4 v0,s[36:39],s66 offen offset:2048 sc1 lds
buffer_load_dwordx4 v0,s[36:39],s66 offen offset:3072 sc1 lds
s_mov_b32 s46,s47
s_mov_b32 s47,s59
s_lshr_b32 s70,s59,5
buffer_store_dword v5,v4,s[36:39],s70 offen offset:0 sc0 sc1
s_waitcnt lgkmcnt(3)
s_setpc_b64 s[90:91]
Lg1_done0_%=:
s_waitcnt vmcnt(0)
s_barrier
s_lshr_b32 s70,s46,5
buffer_store_dword v5,v4,s[36:39],s70 offen offset:0 sc0 sc1
s_lshr_b32 s70,s47,5
buffer_store_dword v5,v4,s[36:39],s70 offen offset:0 sc0 sc1
s_branch Lg1_done_%=
Lg1_slow1_%=:
s_cmp_lt_u32 s67,s59
s_cbranch_scc1 Lg1_gs1_%=
s_cmp_eq_u32 s54,1
s_cselect_b32 s68,s50,s49
s_lshr_b32 s69,s68,5
s_cmp_lt_u32 s68,4194304
s_cselect_b32 s68,0xffffcfff,-1
s_and_b32 s68,s68,s69
s_add_u32 m0,s78,8192
s_nop 0
buffer_load_dword v6,s[36:39],s68 offen offset:0 sc1 lds
s_lshr_b32 s73,s48,0
v_fma_f32 v28,-v92,v20,v28
v_fma_f32 v29,-v92,v21,v29
v_fma_f32 v30,-v92,v22,v30
v_fma_f32 v31,-v92,v23,v31
v_mul_f32 v76,v28,v28
v_mul_f32 v77,v29,v29
v_mul_f32 v78,v30,v30
v_mul_f32 v79,v31,v31
v_fma_f32 v76,v94,v76,v93
v_fma_f32 v77,v94,v77,v93
v_fma_f32 v78,v94,v78,v93
v_fma_f32 v79,v94,v79,v93
v_mul_f32 v76,v28,v76
v_mul_f32 v77,v29,v77
v_mul_f32 v78,v30,v78
v_mul_f32 v79,v31,v79
v_exp_f32 v76,v76
v_exp_f32 v77,v77
v_exp_f32 v78,v78
v_exp_f32 v79,v79
v_fma_f32 v76,v76,v95,v95
v_fma_f32 v77,v77,v95,v95
v_fma_f32 v78,v78,v95,v95
v_fma_f32 v79,v79,v95,v95
v_rcp_f32 v76,v76
v_rcp_f32 v77,v77
v_rcp_f32 v78,v78
v_rcp_f32 v79,v79
v_mul_f32 v28,v28,v76
v_mul_f32 v29,v29,v77
v_mul_f32 v30,v30,v78
v_mul_f32 v31,v31,v79
v_cvt_pk_bf16_f32 v84,v28,v29
v_cvt_pk_bf16_f32 v85,v30,v31
s_add_u32 m0,s78,8448
s_nop 0
buffer_load_dword v6,s[36:39],s68 offen offset:0 sc1 lds
v_fma_f32 v32,-v92,v24,v32
v_fma_f32 v33,-v92,v25,v33
v_fma_f32 v34,-v92,v26,v34
v_fma_f32 v35,-v92,v27,v35
v_mul_f32 v80,v32,v32
v_mul_f32 v81,v33,v33
v_mul_f32 v82,v34,v34
v_mul_f32 v83,v35,v35
v_fma_f32 v80,v94,v80,v93
v_fma_f32 v81,v94,v81,v93
v_fma_f32 v82,v94,v82,v93
v_fma_f32 v83,v94,v83,v93
v_mul_f32 v80,v32,v80
v_mul_f32 v81,v33,v81
v_mul_f32 v82,v34,v82
v_mul_f32 v83,v35,v83
v_exp_f32 v80,v80
v_exp_f32 v81,v81
v_exp_f32 v82,v82
v_exp_f32 v83,v83
v_fma_f32 v80,v80,v95,v95
v_fma_f32 v81,v81,v95,v95
v_fma_f32 v82,v82,v95,v95
v_fma_f32 v83,v83,v95,v95
v_rcp_f32 v80,v80
v_rcp_f32 v81,v81
v_rcp_f32 v82,v82
v_rcp_f32 v83,v83
v_mul_f32 v32,v32,v80
v_mul_f32 v33,v33,v81
v_mul_f32 v34,v34,v82)"
R"(
v_mul_f32 v35,v35,v83
v_cvt_pk_bf16_f32 v86,v32,v33
v_cvt_pk_bf16_f32 v87,v34,v35
buffer_store_dwordx4 v[84:87],v3,s[36:39],s73 offen offset:0 sc0 sc1
s_cmp_ge_u32 s49,s59
s_cbranch_scc1 Lg1_done1_%=
s_add_u32 m0,s78,16384
s_nop 0
buffer_load_dword v6,s[36:39],s68 offen offset:0 sc1 lds
s_waitcnt vmcnt(3)
s_barrier
ds_read_b32 v16,v7 offset:8192
s_lshr_b32 s70,s47,5
buffer_store_dword v5,v4,s[36:39],s70 offen offset:0 sc0 sc1
s_mov_b32 s47,s59
s_mov_b32 s69,18432
s_waitcnt lgkmcnt(0)
v_cmp_eq_u32 vcc,s60,v16
s_nop 0
s_cmp_eq_u32 vcc_lo,-1
s_cbranch_scc1 Lg1_go1_%=
s_add_u32 m0,s78,16640
s_nop 0
buffer_load_dword v6,s[36:39],s68 offen offset:0 sc1 lds
s_waitcnt vmcnt(4)
s_barrier
ds_read_b32 v16,v7 offset:8448
s_mov_b32 s69,18688
s_waitcnt lgkmcnt(0)
v_cmp_eq_u32 vcc,s60,v16
s_nop 0
s_cmp_eq_u32 vcc_lo,-1
s_cbranch_scc1 Lg1_go1_%=
s_add_u32 m0,s78,8192
s_nop 0
buffer_load_dword v6,s[36:39],s68 offen offset:0 sc1 lds
s_waitcnt vmcnt(3)
s_barrier
ds_read_b32 v16,v7 offset:16384
s_lshr_b32 s70,s48,5
buffer_store_dword v5,v4,s[36:39],s70 offen offset:0 sc0 sc1
s_mov_b32 s48,s59
s_mov_b32 s69,10240
s_waitcnt lgkmcnt(0)
v_cmp_eq_u32 vcc,s60,v16
s_nop 0
s_cmp_eq_u32 vcc_lo,-1
s_cbranch_scc1 Lg1_go1_%=
s_add_u32 m0,s78,8448
s_nop 0
buffer_load_dword v6,s[36:39],s68 offen offset:0 sc1 lds
s_waitcnt vmcnt(3)
s_barrier
ds_read_b32 v16,v7 offset:16640
s_mov_b32 s69,10496
s_waitcnt lgkmcnt(0)
v_cmp_eq_u32 vcc,s60,v16
s_nop 0
s_cmp_eq_u32 vcc_lo,-1
s_cbranch_scc1 Lg1_go1_%=
Lg1_poll1_%=:
s_add_u32 m0,s78,16384
s_nop 0
buffer_load_dword v6,s[36:39],s68 offen offset:0 sc1 lds
s_waitcnt vmcnt(2)
s_barrier
ds_read_b32 v16,v7 offset:8192
s_mov_b32 s69,18432
s_waitcnt lgkmcnt(0)
v_cmp_eq_u32 vcc,s60,v16
s_nop 0
s_cmp_eq_u32 vcc_lo,-1
s_cbranch_scc1 Lg1_go1_%=
s_sleep 4
s_add_u32 m0,s78,16640
s_nop 0
buffer_load_dword v6,s[36:39],s68 offen offset:0 sc1 lds
s_waitcnt vmcnt(2)
s_barrier
ds_read_b32 v16,v7 offset:8448
s_mov_b32 s69,18688
s_waitcnt lgkmcnt(0)
v_cmp_eq_u32 vcc,s60,v16
s_nop 0
s_cmp_eq_u32 vcc_lo,-1
s_cbranch_scc1 Lg1_go1_%=
s_sleep 4
s_add_u32 m0,s78,8192
s_nop 0
buffer_load_dword v6,s[36:39],s68 offen offset:0 sc1 lds
s_waitcnt vmcnt(2)
s_barrier
ds_read_b32 v16,v7 offset:16384
s_mov_b32 s69,10240
s_waitcnt lgkmcnt(0)
v_cmp_eq_u32 vcc,s60,v16
s_nop 0
s_cmp_eq_u32 vcc_lo,-1
s_cbranch_scc1 Lg1_go1_%=
s_sleep 4
s_add_u32 m0,s78,8448
s_nop 0
buffer_load_dword v6,s[36:39],s68 offen offset:0 sc1 lds
s_waitcnt vmcnt(2)
s_barrier
ds_read_b32 v16,v7 offset:16640
s_mov_b32 s69,10496
s_waitcnt lgkmcnt(0)
v_cmp_eq_u32 vcc,s60,v16
s_nop 0
s_cmp_eq_u32 vcc_lo,-1
s_cbranch_scc1 Lg1_go1_%=
s_sleep 4
s_add_u32 s79,s79,1
s_cmp_lt_u32 s79,20000
s_cbranch_scc1 Lg1_poll1_%=
Lg1_go1_%=:
s_xor_b32 s62,s64,65536
s_mov_b32 m0,s62
s_nop 0
buffer_load_dwordx4 v0,s[36:39],s49 offen offset:0 sc1 lds
buffer_load_dwordx4 v0,s[36:39],s49 offen offset:1024 sc1 lds
buffer_load_dwordx4 v0,s[36:39],s49 offen offset:2048 sc1 lds
buffer_load_dwordx4 v0,s[36:39],s49 offen offset:3072 sc1 lds
s_add_u32 m0,s62,16384
s_nop 0
buffer_load_dwordx4 v1,s[36:39],s49 offen offset:0 sc1 lds
buffer_load_dwordx4 v1,s[36:39],s49 offen offset:1024 sc1 lds
buffer_load_dwordx4 v1,s[36:39],s49 offen offset:2048 sc1 lds
buffer_load_dwordx4 v1,s[36:39],s49 offen offset:3072 sc1 lds
s_lshr_b32 s72,s49,6
s_lshr_b32 s71,s62,2
s_cmp_lt_u32 s49,4194304
s_cselect_b32 s72,s59,s72
s_add_u32 m0,s71,s88
s_nop 0
buffer_load_dword v10,s[36:39],s72 offen offset:0 sc1 lds
s_cmp_eq_u32 vcc_hi,-1
s_cselect_b32 s67,s50,s59
s_xor_b32 s62,s63,65536
s_cmp_ge_u32 s67,s59
s_cselect_b32 s68,s50,s51
s_lshr_b32 s69,s68,5
s_cmp_lt_u32 s68,4194304
s_cselect_b32 s68,0xffffcfff,-1
s_and_b32 s68,s68,s69
s_add_u32 m0,s78,256
s_nop 0
buffer_load_dword v6,s[36:39],s68 offen offset:0 sc1 lds
s_mov_b32 m0,s62
s_nop 0
buffer_load_dwordx4 v0,s[36:39],s67 offen offset:0 sc1 lds
buffer_load_dwordx4 v0,s[36:39],s67 offen offset:1024 sc1 lds
buffer_load_dwordx4 v0,s[36:39],s67 offen offset:2048 sc1 lds
buffer_load_dwordx4 v0,s[36:39],s67 offen offset:3072 sc1 lds
s_waitcnt vmcnt(10)
s_barrier
s_lshl_b32 s70,s54,12
v_add_u32 v76,s70,v2
v_xor_b32 v77,4096,v76
ds_read_b128 v[44:47],v76 offset:32768
ds_read_b128 v[48:51],v76 offset:33792
ds_read_b128 v[52:55],v76 offset:34816
ds_read_b128 v[56:59],v76 offset:35840
ds_read_b128 v[60:63],v77 offset:32768
ds_read_b128 v[64:67],v77 offset:33792
ds_read_b128 v[68:71],v77 offset:34816
s_lshr_b32 s70,s47,5
buffer_store_dword v5,v4,s[36:39],s70 offen offset:0 sc0 sc1
s_mov_b32 s47,s59
s_lshr_b32 s70,s48,5
buffer_store_dword v5,v4,s[36:39],s70 offen offset:0 sc0 sc1
s_mov_b32 s48,s59
s_lshr_b32 s70,s59,5
buffer_store_dword v5,v4,s[36:39],s70 offen offset:0 sc0 sc1
s_waitcnt lgkmcnt(3)
s_setpc_b64 s[92:93]
Lg1_gs1_%=:
s_cmp_eq_u32 s54,1
s_cselect_b32 s68,s50,s49
s_lshr_b32 s69,s68,5
s_cmp_lt_u32 s68,4194304
s_cselect_b32 s68,0xffffcfff,-1
s_and_b32 s68,s68,s69
s_lshr_b32 s73,s48,0
s_add_u32 m0,s78,8192
s_nop 0
buffer_load_dword v6,s[36:39],s68 offen offset:0 sc1 lds
v_fma_f32 v28,-v92,v20,v28
v_fma_f32 v29,-v92,v21,v29
v_fma_f32 v30,-v92,v22,v30
v_fma_f32 v31,-v92,v23,v31
v_mul_f32 v76,v28,v28
v_mul_f32 v77,v29,v29
v_mul_f32 v78,v30,v30
v_mul_f32 v79,v31,v31
v_fma_f32 v76,v94,v76,v93
v_fma_f32 v77,v94,v77,v93
v_fma_f32 v78,v94,v78,v93
s_add_u32 m0,s62,16384)"
R"(
s_nop 0
buffer_load_dwordx4 v1,s[36:39],s67 offen offset:0 sc1 lds
v_fma_f32 v79,v94,v79,v93
v_mul_f32 v76,v28,v76
v_mul_f32 v77,v29,v77
v_mul_f32 v78,v30,v78
v_mul_f32 v79,v31,v79
v_exp_f32 v76,v76
v_exp_f32 v77,v77
v_exp_f32 v78,v78
v_exp_f32 v79,v79
v_fma_f32 v76,v76,v95,v95
v_fma_f32 v77,v77,v95,v95
buffer_load_dwordx4 v1,s[36:39],s67 offen offset:1024 sc1 lds
v_fma_f32 v78,v78,v95,v95
v_fma_f32 v79,v79,v95,v95
v_rcp_f32 v76,v76
v_rcp_f32 v77,v77
v_rcp_f32 v78,v78
v_rcp_f32 v79,v79
v_mul_f32 v28,v28,v76
v_mul_f32 v29,v29,v77
v_mul_f32 v30,v30,v78
v_mul_f32 v31,v31,v79
v_cvt_pk_bf16_f32 v84,v28,v29
buffer_load_dwordx4 v1,s[36:39],s67 offen offset:2048 sc1 lds
v_cvt_pk_bf16_f32 v85,v30,v31
v_fma_f32 v32,-v92,v24,v32
v_fma_f32 v33,-v92,v25,v33
v_fma_f32 v34,-v92,v26,v34
v_fma_f32 v35,-v92,v27,v35
v_mul_f32 v80,v32,v32
v_mul_f32 v81,v33,v33
v_mul_f32 v82,v34,v34
v_mul_f32 v83,v35,v35
v_fma_f32 v80,v94,v80,v93
v_fma_f32 v81,v94,v81,v93
buffer_load_dwordx4 v1,s[36:39],s67 offen offset:3072 sc1 lds
v_fma_f32 v82,v94,v82,v93
v_fma_f32 v83,v94,v83,v93
v_mul_f32 v80,v32,v80
v_mul_f32 v81,v33,v81
v_mul_f32 v82,v34,v82
v_mul_f32 v83,v35,v83
v_exp_f32 v80,v80
v_exp_f32 v81,v81
v_exp_f32 v82,v82
v_exp_f32 v83,v83
v_fma_f32 v80,v80,v95,v95
s_lshr_b32 s72,s67,6
s_lshr_b32 s71,s62,2
s_cmp_lt_u32 s67,4194304
s_cselect_b32 s72,s59,s72
s_add_u32 m0,s71,s88
s_nop 0
buffer_load_dword v10,s[36:39],s72 offen offset:0 sc1 lds
v_fma_f32 v81,v81,v95,v95
v_fma_f32 v82,v82,v95,v95
v_fma_f32 v83,v83,v95,v95
v_rcp_f32 v80,v80
v_rcp_f32 v81,v81
v_rcp_f32 v82,v82
v_rcp_f32 v83,v83
v_mul_f32 v32,v32,v80
v_mul_f32 v33,v33,v81
v_mul_f32 v34,v34,v82
v_mul_f32 v35,v35,v83
v_cvt_pk_bf16_f32 v86,v32,v33
v_cvt_pk_bf16_f32 v87,v34,v35
buffer_store_dwordx4 v[84:87],v3,s[36:39],s73 offen offset:0 sc0 sc1
s_waitcnt vmcnt(6)
s_barrier
ds_read_b32 v16,v7 offset:8192
s_lshl_b32 s70,s54,12
v_add_u32 v76,s70,v2
v_xor_b32 v77,4096,v76
ds_read_b128 v[44:47],v76 offset:32768
ds_read_b128 v[48:51],v76 offset:33792
ds_read_b128 v[52:55],v76 offset:34816
ds_read_b128 v[56:59],v76 offset:35840
ds_read_b128 v[60:63],v77 offset:32768
ds_read_b128 v[64:67],v77 offset:33792
ds_read_b128 v[68:71],v77 offset:34816
s_lshr_b32 s70,s47,5
buffer_store_dword v5,v4,s[36:39],s70 offen offset:0 sc0 sc1
s_waitcnt lgkmcnt(7)
v_cmp_eq_u32 vcc,s60,v16
s_nop 0
s_cmp_eq_u32 vcc_hi,-1
s_cselect_b32 s67,s50,s59
s_xor_b32 s62,s63,65536
s_cmp_ge_u32 s67,s59
s_cselect_b32 s68,s50,s51
s_lshr_b32 s69,s68,5
s_cmp_lt_u32 s68,4194304
s_cselect_b32 s68,0xffffcfff,-1
s_and_b32 s68,s68,s69
s_add_u32 m0,s78,256
s_nop 0
buffer_load_dword v6,s[36:39],s68 offen offset:0 sc1 lds
s_mov_b32 m0,s62
s_nop 0
buffer_load_dwordx4 v0,s[36:39],s67 offen offset:0 sc1 lds
buffer_load_dwordx4 v0,s[36:39],s67 offen offset:1024 sc1 lds
buffer_load_dwordx4 v0,s[36:39],s67 offen offset:2048 sc1 lds
buffer_load_dwordx4 v0,s[36:39],s67 offen offset:3072 sc1 lds
s_mov_b32 s47,s48
s_mov_b32 s48,s59
s_lshr_b32 s70,s59,5
buffer_store_dword v5,v4,s[36:39],s70 offen offset:0 sc0 sc1
s_waitcnt lgkmcnt(3)
s_setpc_b64 s[92:93]
Lg1_done1_%=:
s_waitcnt vmcnt(0)
s_barrier
s_lshr_b32 s70,s47,5
buffer_store_dword v5,v4,s[36:39],s70 offen offset:0 sc0 sc1
s_lshr_b32 s70,s48,5
buffer_store_dword v5,v4,s[36:39],s70 offen offset:0 sc0 sc1
s_branch Lg1_done_%=
Lg1_done_%=:
s_waitcnt vmcnt(0) lgkmcnt(0)
s_barrier
s_branch Lg2_end_%=
Lg2_begin_%=:
Lcode_g2_b:
s_mov_b64 s[36:37],%1
s_mov_b32 s38,111193216
s_mov_b32 s39,0x00020000
s_mov_b32 s54,%2
s_mov_b32 s55,%3
s_mov_b32 s56,%4
s_mov_b32 s58,%5
s_mov_b32 s83,%6
s_mov_b32 s82,%7
s_mov_b32 s84,%8
v_mov_b32 v0,%9
v_mov_b32 v1,%10
v_mov_b32 v2,%11
v_mov_b32 v3,%12
v_mov_b32 v4,%13
v_mov_b32 v6,%14
v_mov_b32 v7,%15
v_mov_b32 v8,%16
v_mov_b32 v9,%17
v_mov_b32 v10,%18
v_mov_b32 v11,%19
v_mov_b32 v12,%20
v_mov_b32 v13,%21
v_mov_b32 v14,%22
v_mov_b32 v16,%23
v_mov_b32 v17,%24
v_mov_b32 v18,%25
v_mov_b32 v19,%26
v_mov_b32 v20,%27
s_mov_b64 s[40:41],%28
s_mov_b32 s42,2097152
s_mov_b32 s43,0x00020000
s_lshl_b32 s88,s54,11
s_mov_b32 s69,111193088
buffer_load_dword v5,off,s[36:39],s69 sc1
s_add_u32 m0,s88,0
s_add_u32 s61,s82,0
buffer_load_dwordx4 v16,s[40:43],s61 offen offset:0  lds
s_add_u32 m0,s88,1024
s_nop 0
buffer_load_dwordx4 v17,s[40:43],s61 offen offset:0  lds
s_add_u32 m0,s88,8064
s_nop 0
buffer_load_dwordx4 v16,s[40:43],s61 offen offset:128  lds
s_add_u32 m0,s88,9088
s_nop 0
buffer_load_dwordx4 v17,s[40:43],s61 offen offset:128  lds
s_add_u32 m0,s88,16384
s_add_u32 s61,s82,131072
buffer_load_dwordx4 v16,s[40:43],s61 offen offset:0  lds
s_add_u32 m0,s88,17408
s_nop 0
buffer_load_dwordx4 v17,s[40:43],s61 offen offset:0  lds
s_add_u32 m0,s88,24448
s_nop 0
buffer_load_dwordx4 v16,s[40:43],s61 offen offset:128  lds
s_add_u32 m0,s88,25472
s_nop 0
buffer_load_dwordx4 v17,s[40:43],s61 offen offset:128  lds
s_add_u32 m0,s88,32768
s_add_u32 s61,s82,262144
buffer_load_dwordx4 v16,s[40:43],s61 offen offset:0  lds
s_add_u32 m0,s88,33792
s_nop 0
buffer_load_dwordx4 v17,s[40:43],s61 offen offset:0  lds
s_add_u32 m0,s88,40832
s_nop 0
buffer_load_dwordx4 v16,s[40:43],s61 offen offset:128  lds
s_add_u32 m0,s88,41856
s_nop 0
buffer_load_dwordx4 v17,s[40:43],s61 offen offset:128  lds
s_add_u32 m0,s88,49152
s_add_u32 s61,s82,393216
buffer_load_dwordx4 v16,s[40:43],s61 offen offset:0  lds)"
R"(
s_add_u32 m0,s88,50176
s_nop 0
buffer_load_dwordx4 v17,s[40:43],s61 offen offset:0  lds
s_add_u32 m0,s88,57216
s_nop 0
buffer_load_dwordx4 v16,s[40:43],s61 offen offset:128  lds
s_add_u32 m0,s88,58240
s_nop 0
buffer_load_dwordx4 v17,s[40:43],s61 offen offset:128  lds
s_add_u32 m0,s88,65536
s_add_u32 s61,s82,524288
buffer_load_dwordx4 v16,s[40:43],s61 offen offset:0  lds
s_add_u32 m0,s88,66560
s_nop 0
buffer_load_dwordx4 v17,s[40:43],s61 offen offset:0  lds
s_add_u32 m0,s88,73600
s_nop 0
buffer_load_dwordx4 v16,s[40:43],s61 offen offset:128  lds
s_add_u32 m0,s88,74624
s_nop 0
buffer_load_dwordx4 v17,s[40:43],s61 offen offset:128  lds
s_add_u32 m0,s88,81920
s_add_u32 s61,s82,655360
buffer_load_dwordx4 v16,s[40:43],s61 offen offset:0  lds
s_add_u32 m0,s88,82944
s_nop 0
buffer_load_dwordx4 v17,s[40:43],s61 offen offset:0  lds
s_add_u32 m0,s88,89984
s_nop 0
buffer_load_dwordx4 v16,s[40:43],s61 offen offset:128  lds
s_add_u32 m0,s88,91008
s_nop 0
buffer_load_dwordx4 v17,s[40:43],s61 offen offset:128  lds
s_add_u32 m0,s88,98304
s_add_u32 s61,s82,786432
buffer_load_dwordx4 v16,s[40:43],s61 offen offset:0  lds
s_add_u32 m0,s88,99328
s_nop 0
buffer_load_dwordx4 v17,s[40:43],s61 offen offset:0  lds
s_add_u32 m0,s88,106368
s_nop 0
buffer_load_dwordx4 v16,s[40:43],s61 offen offset:128  lds
s_add_u32 m0,s88,107392
s_nop 0
buffer_load_dwordx4 v17,s[40:43],s61 offen offset:128  lds
v_add_u32 v24,49152,v18
v_add_u32 v25,49152,v19
v_add_u32 v26,98304,v18
v_add_u32 v27,98304,v19
s_waitcnt vmcnt(24)
s_barrier
s_add_u32 m0,s88,114688
s_add_u32 s61,s82,917504
buffer_load_dwordx4 v16,s[40:43],s61 offen offset:0  lds
s_add_u32 m0,s88,115712
ds_read_b64_tr_b16 a[0:1],v18 offset:0
buffer_load_dwordx4 v17,s[40:43],s61 offen offset:0  lds
s_add_u32 m0,s88,122752
ds_read_b64_tr_b16 a[2:3],v18 offset:512
buffer_load_dwordx4 v16,s[40:43],s61 offen offset:128  lds
s_add_u32 m0,s88,123776
ds_read_b64_tr_b16 a[4:5],v19 offset:0
buffer_load_dwordx4 v17,s[40:43],s61 offen offset:128  lds
ds_read_b64_tr_b16 a[6:7],v19 offset:512
ds_read_b64_tr_b16 a[8:9],v18 offset:4096
ds_read_b64_tr_b16 a[10:11],v18 offset:4608
ds_read_b64_tr_b16 a[12:13],v19 offset:4096
ds_read_b64_tr_b16 a[14:15],v19 offset:4608
s_waitcnt vmcnt(24)
s_barrier
s_add_u32 m0,s88,131072
s_add_u32 s61,s82,1048576
buffer_load_dwordx4 v16,s[40:43],s61 offen offset:0  lds
s_add_u32 m0,s88,132096
ds_read_b64_tr_b16 a[16:17],v18 offset:16384
buffer_load_dwordx4 v17,s[40:43],s61 offen offset:0  lds
s_add_u32 m0,s88,139136
ds_read_b64_tr_b16 a[18:19],v18 offset:16896
buffer_load_dwordx4 v16,s[40:43],s61 offen offset:128  lds
s_add_u32 m0,s88,140160
ds_read_b64_tr_b16 a[20:21],v19 offset:16384
buffer_load_dwordx4 v17,s[40:43],s61 offen offset:128  lds
ds_read_b64_tr_b16 a[22:23],v19 offset:16896
ds_read_b64_tr_b16 a[24:25],v18 offset:20480
ds_read_b64_tr_b16 a[26:27],v18 offset:20992
ds_read_b64_tr_b16 a[28:29],v19 offset:20480
ds_read_b64_tr_b16 a[30:31],v19 offset:20992
s_waitcnt lgkmcnt(8)
s_waitcnt vmcnt(24)
s_barrier
s_add_u32 m0,s88,0
s_add_u32 s61,s82,1179648
buffer_load_dwordx4 v16,s[40:43],s61 offen offset:0  lds
s_add_u32 m0,s88,1024
ds_read_b64_tr_b16 a[32:33],v18 offset:32768
buffer_load_dwordx4 v17,s[40:43],s61 offen offset:0  lds
s_add_u32 m0,s88,8064
ds_read_b64_tr_b16 a[34:35],v18 offset:33280
buffer_load_dwordx4 v16,s[40:43],s61 offen offset:128  lds
s_add_u32 m0,s88,9088
ds_read_b64_tr_b16 a[36:37],v19 offset:32768
buffer_load_dwordx4 v17,s[40:43],s61 offen offset:128  lds
ds_read_b64_tr_b16 a[38:39],v19 offset:33280
ds_read_b64_tr_b16 a[40:41],v18 offset:36864
ds_read_b64_tr_b16 a[42:43],v18 offset:37376
ds_read_b64_tr_b16 a[44:45],v19 offset:36864
ds_read_b64_tr_b16 a[46:47],v19 offset:37376
s_waitcnt lgkmcnt(8)
s_waitcnt vmcnt(24)
s_barrier
s_add_u32 m0,s88,16384
s_add_u32 s61,s82,1310720
buffer_load_dwordx4 v16,s[40:43],s61 offen offset:0  lds
s_add_u32 m0,s88,17408
ds_read_b64_tr_b16 a[48:49],v24 offset:0
buffer_load_dwordx4 v17,s[40:43],s61 offen offset:0  lds
s_add_u32 m0,s88,24448
ds_read_b64_tr_b16 a[50:51],v24 offset:512
buffer_load_dwordx4 v16,s[40:43],s61 offen offset:128  lds
s_add_u32 m0,s88,25472
ds_read_b64_tr_b16 a[52:53],v25 offset:0
buffer_load_dwordx4 v17,s[40:43],s61 offen offset:128  lds
ds_read_b64_tr_b16 a[54:55],v25 offset:512
ds_read_b64_tr_b16 a[56:57],v24 offset:4096
ds_read_b64_tr_b16 a[58:59],v24 offset:4608
ds_read_b64_tr_b16 a[60:61],v25 offset:4096
ds_read_b64_tr_b16 a[62:63],v25 offset:4608
s_waitcnt lgkmcnt(8)
s_waitcnt vmcnt(24)
s_barrier
s_add_u32 m0,s88,32768
s_add_u32 s61,s82,1441792
buffer_load_dwordx4 v16,s[40:43],s61 offen offset:0  lds
s_add_u32 m0,s88,33792
ds_read_b64_tr_b16 a[64:65],v24 offset:16384
buffer_load_dwordx4 v17,s[40:43],s61 offen offset:0  lds
s_add_u32 m0,s88,40832
ds_read_b64_tr_b16 a[66:67],v24 offset:16896
buffer_load_dwordx4 v16,s[40:43],s61 offen offset:128  lds
s_add_u32 m0,s88,41856
ds_read_b64_tr_b16 a[68:69],v25 offset:16384
buffer_load_dwordx4 v17,s[40:43],s61 offen offset:128  lds
ds_read_b64_tr_b16 a[70:71],v25 offset:16896
ds_read_b64_tr_b16 a[72:73],v24 offset:20480
ds_read_b64_tr_b16 a[74:75],v24 offset:20992
ds_read_b64_tr_b16 a[76:77],v25 offset:20480
ds_read_b64_tr_b16 a[78:79],v25 offset:20992
s_waitcnt lgkmcnt(8)
s_waitcnt vmcnt(24)
s_barrier
s_add_u32 m0,s88,49152
s_add_u32 s61,s82,1572864
buffer_load_dwordx4 v16,s[40:43],s61 offen offset:0  lds
s_add_u32 m0,s88,50176
ds_read_b64_tr_b16 a[80:81],v24 offset:32768
buffer_load_dwordx4 v17,s[40:43],s61 offen offset:0  lds
s_add_u32 m0,s88,57216
ds_read_b64_tr_b16 a[82:83],v24 offset:33280
buffer_load_dwordx4 v16,s[40:43],s61 offen offset:128  lds
s_add_u32 m0,s88,58240
ds_read_b64_tr_b16 a[84:85],v25 offset:32768
buffer_load_dwordx4 v17,s[40:43],s61 offen offset:128  lds
ds_read_b64_tr_b16 a[86:87],v25 offset:33280
ds_read_b64_tr_b16 a[88:89],v24 offset:36864
ds_read_b64_tr_b16 a[90:91],v24 offset:37376
ds_read_b64_tr_b16 a[92:93],v25 offset:36864
ds_read_b64_tr_b16 a[94:95],v25 offset:37376
s_waitcnt lgkmcnt(8)
s_waitcnt vmcnt(24)
s_barrier
s_add_u32 m0,s88,65536
s_add_u32 s61,s82,1703936
buffer_load_dwordx4 v16,s[40:43],s61 offen offset:0  lds
s_add_u32 m0,s88,66560
ds_read_b64_tr_b16 a[96:97],v26 offset:0
buffer_load_dwordx4 v17,s[40:43],s61 offen offset:0  lds
s_add_u32 m0,s88,73600
ds_read_b64_tr_b16 a[98:99],v26 offset:512
buffer_load_dwordx4 v16,s[40:43],s61 offen offset:128  lds
s_add_u32 m0,s88,74624
ds_read_b64_tr_b16 a[100:101],v27 offset:0
buffer_load_dwordx4 v17,s[40:43],s61 offen offset:128  lds
ds_read_b64_tr_b16 a[102:103],v27 offset:512
ds_read_b64_tr_b16 a[104:105],v26 offset:4096
ds_read_b64_tr_b16 a[106:107],v26 offset:4608
ds_read_b64_tr_b16 a[108:109],v27 offset:4096
ds_read_b64_tr_b16 a[110:111],v27 offset:4608
s_waitcnt lgkmcnt(8)
s_waitcnt vmcnt(24)
s_barrier
s_add_u32 m0,s88,81920
s_add_u32 s61,s82,1835008
buffer_load_dwordx4 v16,s[40:43],s61 offen offset:0  lds
s_add_u32 m0,s88,82944
ds_read_b64_tr_b16 a[112:113],v26 offset:16384
buffer_load_dwordx4 v17,s[40:43],s61 offen offset:0  lds
s_add_u32 m0,s88,89984
ds_read_b64_tr_b16 a[114:115],v26 offset:16896
buffer_load_dwordx4 v16,s[40:43],s61 offen offset:128  lds
s_add_u32 m0,s88,91008)"
R"(
ds_read_b64_tr_b16 a[116:117],v27 offset:16384
buffer_load_dwordx4 v17,s[40:43],s61 offen offset:128  lds
ds_read_b64_tr_b16 a[118:119],v27 offset:16896
ds_read_b64_tr_b16 a[120:121],v26 offset:20480
ds_read_b64_tr_b16 a[122:123],v26 offset:20992
ds_read_b64_tr_b16 a[124:125],v27 offset:20480
ds_read_b64_tr_b16 a[126:127],v27 offset:20992
s_waitcnt lgkmcnt(8)
s_waitcnt vmcnt(24)
s_barrier
s_add_u32 m0,s88,98304
s_add_u32 s61,s82,1966080
buffer_load_dwordx4 v16,s[40:43],s61 offen offset:0  lds
s_add_u32 m0,s88,99328
ds_read_b64_tr_b16 a[128:129],v26 offset:32768
buffer_load_dwordx4 v17,s[40:43],s61 offen offset:0  lds
s_add_u32 m0,s88,106368
ds_read_b64_tr_b16 a[130:131],v26 offset:33280
buffer_load_dwordx4 v16,s[40:43],s61 offen offset:128  lds
s_add_u32 m0,s88,107392
ds_read_b64_tr_b16 a[132:133],v27 offset:32768
buffer_load_dwordx4 v17,s[40:43],s61 offen offset:128  lds
ds_read_b64_tr_b16 a[134:135],v27 offset:33280
ds_read_b64_tr_b16 a[136:137],v26 offset:36864
ds_read_b64_tr_b16 a[138:139],v26 offset:37376
ds_read_b64_tr_b16 a[140:141],v27 offset:36864
ds_read_b64_tr_b16 a[142:143],v27 offset:37376
s_waitcnt lgkmcnt(8)
s_waitcnt vmcnt(24)
s_barrier
ds_read_b64_tr_b16 a[144:145],v18 offset:0
ds_read_b64_tr_b16 a[146:147],v18 offset:512
ds_read_b64_tr_b16 a[148:149],v19 offset:0
ds_read_b64_tr_b16 a[150:151],v19 offset:512
ds_read_b64_tr_b16 a[152:153],v18 offset:4096
ds_read_b64_tr_b16 a[154:155],v18 offset:4608
ds_read_b64_tr_b16 a[156:157],v19 offset:4096
ds_read_b64_tr_b16 a[158:159],v19 offset:4608
s_waitcnt lgkmcnt(8)
s_waitcnt vmcnt(20)
s_barrier
ds_read_b64_tr_b16 a[160:161],v18 offset:16384
ds_read_b64_tr_b16 a[162:163],v18 offset:16896
ds_read_b64_tr_b16 a[164:165],v19 offset:16384
ds_read_b64_tr_b16 a[166:167],v19 offset:16896
ds_read_b64_tr_b16 a[168:169],v18 offset:20480
ds_read_b64_tr_b16 a[170:171],v18 offset:20992
ds_read_b64_tr_b16 a[172:173],v19 offset:20480
ds_read_b64_tr_b16 a[174:175],v19 offset:20992
s_waitcnt lgkmcnt(8)
s_waitcnt vmcnt(16)
s_barrier
ds_read_b64_tr_b16 a[176:177],v18 offset:32768
ds_read_b64_tr_b16 a[178:179],v18 offset:33280
ds_read_b64_tr_b16 a[180:181],v19 offset:32768
ds_read_b64_tr_b16 a[182:183],v19 offset:33280
ds_read_b64_tr_b16 a[184:185],v18 offset:36864
ds_read_b64_tr_b16 a[186:187],v18 offset:37376
ds_read_b64_tr_b16 a[188:189],v19 offset:36864
ds_read_b64_tr_b16 a[190:191],v19 offset:37376
s_waitcnt lgkmcnt(8)
s_waitcnt vmcnt(12)
s_barrier
ds_read_b64_tr_b16 a[192:193],v24 offset:0
ds_read_b64_tr_b16 a[194:195],v24 offset:512
ds_read_b64_tr_b16 a[196:197],v25 offset:0
ds_read_b64_tr_b16 a[198:199],v25 offset:512
ds_read_b64_tr_b16 a[200:201],v24 offset:4096
ds_read_b64_tr_b16 a[202:203],v24 offset:4608
ds_read_b64_tr_b16 a[204:205],v25 offset:4096
ds_read_b64_tr_b16 a[206:207],v25 offset:4608
s_waitcnt lgkmcnt(8)
s_waitcnt vmcnt(8)
s_barrier
ds_read_b64_tr_b16 a[208:209],v24 offset:16384
ds_read_b64_tr_b16 a[210:211],v24 offset:16896
ds_read_b64_tr_b16 a[212:213],v25 offset:16384
ds_read_b64_tr_b16 a[214:215],v25 offset:16896
ds_read_b64_tr_b16 a[216:217],v24 offset:20480
ds_read_b64_tr_b16 a[218:219],v24 offset:20992
ds_read_b64_tr_b16 a[220:221],v25 offset:20480
ds_read_b64_tr_b16 a[222:223],v25 offset:20992
s_waitcnt lgkmcnt(8)
s_waitcnt vmcnt(4)
s_barrier
ds_read_b64_tr_b16 a[224:225],v24 offset:32768
ds_read_b64_tr_b16 a[226:227],v24 offset:33280
ds_read_b64_tr_b16 a[228:229],v25 offset:32768
ds_read_b64_tr_b16 a[230:231],v25 offset:33280
ds_read_b64_tr_b16 a[232:233],v24 offset:36864
ds_read_b64_tr_b16 a[234:235],v24 offset:37376
ds_read_b64_tr_b16 a[236:237],v25 offset:36864
ds_read_b64_tr_b16 a[238:239],v25 offset:37376
s_waitcnt lgkmcnt(8)
s_waitcnt vmcnt(0)
s_barrier
ds_read_b64_tr_b16 a[240:241],v26 offset:0
ds_read_b64_tr_b16 a[242:243],v26 offset:512
ds_read_b64_tr_b16 a[244:245],v27 offset:0
ds_read_b64_tr_b16 a[246:247],v27 offset:512
ds_read_b64_tr_b16 a[248:249],v26 offset:4096
ds_read_b64_tr_b16 a[250:251],v26 offset:4608
ds_read_b64_tr_b16 a[252:253],v27 offset:4096
ds_read_b64_tr_b16 a[254:255],v27 offset:4608
s_waitcnt lgkmcnt(8)
s_waitcnt lgkmcnt(0)
s_waitcnt lgkmcnt(0)
s_barrier
v_readfirstlane_b32 s60,v5
s_nop 3
s_add_u32 s60,s60,1
s_cmp_eq_u32 s60,0
s_cselect_b32 s60,1,s60
Lg2_init_%=:
v_mov_b32 v5,s60
s_mov_b32 s59,536870912
s_add_u32 s57,s56,131072
s_mov_b32 s79,0
s_mov_b32 s75,0x3a800000
s_mov_b32 s76,0xc0135761
s_mov_b32 s77,0x3d372713
s_mov_b32 s44,s59
s_mov_b32 s45,s59
s_mov_b32 s46,s59
s_mov_b32 s47,s59
s_mov_b32 s48,s83
s_add_u32 s49,s48,s55
s_cmp_eq_u32 s49,s56
s_cselect_b32 s49,s57,s49
s_cmp_eq_u32 s49,s58
s_cselect_b32 s49,s59,s49
s_add_u32 s50,s49,s55
s_cmp_eq_u32 s50,s56
s_cselect_b32 s50,s57,s50
s_cmp_eq_u32 s50,s58
s_cselect_b32 s50,s59,s50
s_add_u32 s51,s50,s55
s_cmp_eq_u32 s51,s56
s_cselect_b32 s51,s57,s51
s_cmp_eq_u32 s51,s58
s_cselect_b32 s51,s59,s51
s_add_u32 s52,s51,s55
s_cmp_eq_u32 s52,s56
s_cselect_b32 s52,s57,s52
s_cmp_eq_u32 s52,s58
s_cselect_b32 s52,s59,s52
s_lshl_b32 s63,s54,12
s_add_u32 s63,s63,65536
s_add_u32 s64,s63,32768
s_lshl_b32 s88,s54,10
s_add_u32 s88,s88,131072
s_lshl_b32 s78,s54,11
s_add_u32 s78,s78,132608
s_getpc_b64 s[90:91]
Lg2_pc_%=:
s_mov_b32 s68,Lg2_d1w0_%=-Lg2_pc_%=
s_cmp_eq_u32 s54,1
s_cselect_b32 s68,Lg2_d1w1_%=-Lg2_pc_%=,s68
s_cmp_eq_u32 s54,2
s_cselect_b32 s68,Lg2_d1w2_%=-Lg2_pc_%=,s68
s_cmp_eq_u32 s54,3
s_cselect_b32 s68,Lg2_d1w3_%=-Lg2_pc_%=,s68
s_add_u32 s92,s90,s68
s_addc_u32 s93,s91,0
s_mov_b32 s68,Lg2_d0w0_%=-Lg2_pc_%=
s_cmp_eq_u32 s54,1
s_cselect_b32 s68,Lg2_d0w1_%=-Lg2_pc_%=,s68
s_cmp_eq_u32 s54,2
s_cselect_b32 s68,Lg2_d0w2_%=-Lg2_pc_%=,s68
s_cmp_eq_u32 s54,3
s_cselect_b32 s68,Lg2_d0w3_%=-Lg2_pc_%=,s68
s_add_u32 s90,s90,s68
s_addc_u32 s91,s91,0
s_mov_b32 s66,s59
s_mov_b32 s65,0
s_mov_b32 s62,s63
s_branch Lg2_slow0_%=
Lg2_d0w0_%=:
v_mfma_f32_16x16x32_bf16 v[16:19],a[0:3],v[32:35],0
s_add_u32 m0,s62,16384
ds_read_b128 v[60:63],v2 offset:7168
v_lshlrev_b32 v64,16,v84
v_mfma_f32_16x16x32_bf16 v[20:23],a[4:7],v[32:35],0
buffer_load_dwordx4 v1,s[36:39],s66 offen offset:0 sc1 lds
v_mfma_f32_16x16x32_bf16 v[16:19],a[8:11],v[36:39],v[16:19]
s_lshr_b32 s73,s47,0
ds_read_b128 v[32:35],v2 offset:8192
v_and_b32 v65,0xffff0000,v84
v_mfma_f32_16x16x32_bf16 v[20:23],a[12:15],v[36:39],v[20:23]
s_xor_b32 s87,s64,65536
v_lshlrev_b32 v66,16,v85
v_and_b32 v67,0xffff0000,v85
v_mfma_f32_16x16x32_bf16 v[16:19],a[16:19],v[40:43],v[16:19]
s_cmp_ge_u32 s66,s59
ds_read_b128 v[36:39],v2 offset:9216
v_lshlrev_b32 v68,16,v86
v_mfma_f32_16x16x32_bf16 v[20:23],a[20:23],v[40:43],v[20:23]
buffer_load_dwordx4 v1,s[36:39],s66 offen offset:1024 sc1 lds
v_mfma_f32_16x16x32_bf16 v[16:19],a[24:27],v[44:47],v[16:19]
s_cselect_b32 s86,s49,s50)"
R"(
ds_read_b128 v[40:43],v2 offset:10240
v_and_b32 v69,0xffff0000,v86
v_mfma_f32_16x16x32_bf16 v[20:23],a[28:31],v[44:47],v[20:23]
s_waitcnt lgkmcnt(3)
v_lshlrev_b32 v70,16,v87
v_and_b32 v71,0xffff0000,v87
v_mfma_f32_16x16x32_bf16 v[16:19],a[32:35],v[48:51],v[16:19]
s_cselect_b32 s87,s87,s63
ds_read_b128 v[44:47],v2 offset:11264
v_fma_f32 v64,v64,v91,v90
v_mfma_f32_16x16x32_bf16 v[20:23],a[36:39],v[48:51],v[20:23]
buffer_load_dwordx4 v1,s[36:39],s66 offen offset:2048 sc1 lds
v_mfma_f32_16x16x32_bf16 v[16:19],a[40:43],v[52:55],v[16:19]
s_lshr_b32 s72,s66,6
ds_read_b128 v[48:51],v2 offset:12288
v_fma_f32 v65,v65,v91,v90
v_mfma_f32_16x16x32_bf16 v[20:23],a[44:47],v[52:55],v[20:23]
s_lshr_b32 s71,s62,2
v_fma_f32 v66,v66,v91,v90
v_fma_f32 v67,v67,v91,v90
v_mfma_f32_16x16x32_bf16 v[16:19],a[48:51],v[56:59],v[16:19]
s_mov_b32 s62,s87
ds_read_b128 v[52:55],v2 offset:13312
v_fma_f32 v68,v68,v91,v90
v_mfma_f32_16x16x32_bf16 v[20:23],a[52:55],v[56:59],v[20:23]
buffer_load_dwordx4 v1,s[36:39],s66 offen offset:3072 sc1 lds
v_mfma_f32_16x16x32_bf16 v[16:19],a[56:59],v[60:63],v[16:19]
s_add_u32 m0,s71,s88
ds_read_b128 v[56:59],v2 offset:14336
v_fma_f32 v69,v69,v91,v90
v_mfma_f32_16x16x32_bf16 v[20:23],a[60:63],v[60:63],v[20:23]
s_waitcnt lgkmcnt(3)
v_fma_f32 v70,v70,v91,v90
v_fma_f32 v71,v71,v91,v90
v_mfma_f32_16x16x32_bf16 v[16:19],a[64:67],v[32:35],v[16:19]
s_waitcnt vmcnt(10)
ds_read_b128 v[60:63],v2 offset:15360
v_add_f32 v24,v24,v64
v_mfma_f32_16x16x32_bf16 v[20:23],a[68:71],v[32:35],v[20:23]
s_barrier
v_mfma_f32_16x16x32_bf16 v[16:19],a[72:75],v[36:39],v[16:19]
s_lshr_b32 s68,s50,5
ds_read_b128 v[32:35],v2 offset:16384
v_add_f32 v25,v25,v65
v_mfma_f32_16x16x32_bf16 v[20:23],a[76:79],v[36:39],v[20:23]
buffer_load_dwordx4 v10,s[36:39],s66 offen offset:0 sc1 lds
v_mfma_f32_16x16x32_bf16 v[16:19],a[80:83],v[40:43],v[16:19]
buffer_load_dword v11,s[36:39],s72 offen offset:1024 sc1 lds
v_mfma_f32_16x16x32_bf16 v[20:23],a[84:87],v[40:43],v[20:23]
s_lshr_b32 s70,s51,5
ds_read_b128 v[36:39],v2 offset:17408
v_add_f32 v26,v26,v66
v_mfma_f32_16x16x32_bf16 v[16:19],a[88:91],v[44:47],v[16:19]
ds_read_b128 v[40:43],v2 offset:18432
v_add_f32 v27,v27,v67
v_mfma_f32_16x16x32_bf16 v[20:23],a[92:95],v[44:47],v[20:23]
s_waitcnt lgkmcnt(3)
v_add_f32 v28,v28,v68
v_add_f32 v29,v29,v69
v_add_f32 v30,v30,v70
v_add_f32 v31,v31,v71
v_cvt_pk_bf16_f32 v72,v24,v25
v_cvt_pk_bf16_f32 v73,v26,v27
v_cvt_pk_bf16_f32 v74,v28,v29
v_cvt_pk_bf16_f32 v75,v30,v31
v_mfma_f32_16x16x32_bf16 v[16:19],a[96:99],v[48:51],v[16:19]
buffer_store_dwordx4 v[72:75],v3,s[36:39],s73 offen offset:0 sc0 sc1
ds_read_b128 v[44:47],v2 offset:19456
v_add_f32 v64,v24,v25
v_mul_f32 v65,v24,v24
v_mfma_f32_16x16x32_bf16 v[20:23],a[100:103],v[48:51],v[20:23]
v_fma_f32 v65,v25,v25,v65
v_add_f32 v64,v64,v26
v_mfma_f32_16x16x32_bf16 v[16:19],a[104:107],v[52:55],v[16:19]
ds_read_b128 v[48:51],v2 offset:20480
v_fma_f32 v65,v26,v26,v65
v_mfma_f32_16x16x32_bf16 v[20:23],a[108:111],v[52:55],v[20:23]
v_add_f32 v64,v64,v27
v_fma_f32 v65,v27,v27,v65
v_mfma_f32_16x16x32_bf16 v[16:19],a[112:115],v[56:59],v[16:19]
ds_read_b128 v[52:55],v2 offset:21504
v_add_f32 v64,v64,v28
v_mfma_f32_16x16x32_bf16 v[20:23],a[116:119],v[56:59],v[20:23]
v_fma_f32 v65,v28,v28,v65
v_add_f32 v64,v64,v29
v_fma_f32 v65,v29,v29,v65
v_add_f32 v64,v64,v30
v_fma_f32 v65,v30,v30,v65
v_add_f32 v64,v64,v31
v_fma_f32 v65,v31,v31,v65
v_mfma_f32_16x16x32_bf16 v[16:19],a[120:123],v[60:63],v[16:19]
s_nop 0
v_permlane16_swap_b32 v64,v65
v_add_f32 v64,v64,v65
v_mov_b32 v65,v64
v_mfma_f32_16x16x32_bf16 v[20:23],a[124:127],v[60:63],v[20:23]
s_waitcnt lgkmcnt(2)
ds_read_b128 v[56:59],v2 offset:22528
v_permlane32_swap_b32 v64,v65
v_add_f32 v64,v64,v65
v_mfma_f32_16x16x32_bf16 v[16:19],a[128:131],v[32:35],v[16:19]
ds_write_b32 v13,v64 offset:0
v_mfma_f32_16x16x32_bf16 v[20:23],a[132:135],v[32:35],v[20:23]
ds_read_b128 v[60:63],v2 offset:23552
v_mfma_f32_16x16x32_bf16 v[16:19],a[136:139],v[36:39],v[16:19]
ds_read_b128 v[32:35],v2 offset:24576
v_mfma_f32_16x16x32_bf16 v[20:23],a[140:143],v[36:39],v[20:23]
v_mfma_f32_16x16x32_bf16 v[16:19],a[144:147],v[40:43],v[16:19]
s_waitcnt vmcnt(11) lgkmcnt(2)
ds_read_b128 v[36:39],v2 offset:25600
v_mfma_f32_16x16x32_bf16 v[20:23],a[148:151],v[40:43],v[20:23]
s_barrier
v_mfma_f32_16x16x32_bf16 v[16:19],a[152:155],v[44:47],v[16:19]
ds_read_b128 v[40:43],v2 offset:26624
ds_read_b32 v15,v7 offset:0
v_mfma_f32_16x16x32_bf16 v[20:23],a[156:159],v[44:47],v[20:23]
s_waitcnt lgkmcnt(4)
v_mfma_f32_16x16x32_bf16 v[16:19],a[160:163],v[48:51],v[16:19]
ds_read_b128 v[44:47],v2 offset:27648
v_mfma_f32_16x16x32_bf16 v[20:23],a[164:167],v[48:51],v[20:23]
v_mfma_f32_16x16x32_bf16 v[16:19],a[168:171],v[52:55],v[16:19]
s_waitcnt lgkmcnt(1)
ds_read_b128 v[48:51],v2 offset:28672
v_mfma_f32_16x16x32_bf16 v[20:23],a[172:175],v[52:55],v[20:23]
v_cmp_eq_u32 vcc,s60,v15
v_mfma_f32_16x16x32_bf16 v[16:19],a[176:179],v[56:59],v[16:19]
s_cmp_eq_u32 vcc_lo,-1
ds_read_b128 v[52:55],v2 offset:29696
s_cselect_b32 s67,s86,s59
v_mfma_f32_16x16x32_bf16 v[20:23],a[180:183],v[56:59],v[20:23]
s_cmp_ge_u32 s67,s59
s_cselect_b32 s68,s68,s70
s_add_u32 m0,s78,256
v_mfma_f32_16x16x32_bf16 v[16:19],a[184:187],v[60:63],v[16:19]
buffer_load_dword v6,s[36:39],s68 offen offset:0 sc1 lds
v_mfma_f32_16x16x32_bf16 v[20:23],a[188:191],v[60:63],v[20:23]
s_waitcnt lgkmcnt(2)
ds_read_b128 v[56:59],v2 offset:30720
s_xor_b32 s63,s63,65536
v_mfma_f32_16x16x32_bf16 v[16:19],a[192:195],v[32:35],v[16:19]
s_waitcnt vmcnt(8)
ds_read_b128 v[60:63],v2 offset:31744
s_mov_b32 m0,s62
v_mfma_f32_16x16x32_bf16 v[20:23],a[196:199],v[32:35],v[20:23]
s_barrier
v_mfma_f32_16x16x32_bf16 v[16:19],a[200:203],v[36:39],v[16:19]
buffer_load_dwordx4 v0,s[36:39],s67 offen offset:0 sc1 lds
v_mfma_f32_16x16x32_bf16 v[20:23],a[204:207],v[36:39],v[20:23]
s_add_u32 s53,s52,s55
ds_read_b128 v[32:35],v2 offset:32768
ds_read_b128 v[80:83],v8 offset:0
v_mfma_f32_16x16x32_bf16 v[16:19],a[208:211],v[40:43],v[16:19]
s_cmp_eq_u32 s53,s56
ds_read_b128 v[36:39],v2 offset:33792
ds_read_b64 v[88:89],v9 offset:1024
v_mfma_f32_16x16x32_bf16 v[20:23],a[212:215],v[40:43],v[20:23]
s_cselect_b32 s53,s57,s53
s_cmp_eq_u32 s53,s58
s_cselect_b32 s53,s59,s53
v_mfma_f32_16x16x32_bf16 v[16:19],a[216:219],v[44:47],v[16:19]
buffer_load_dwordx4 v0,s[36:39],s67 offen offset:1024 sc1 lds
v_mfma_f32_16x16x32_bf16 v[20:23],a[220:223],v[44:47],v[20:23]
s_waitcnt lgkmcnt(4)
ds_read_b128 v[40:43],v2 offset:34816
s_cmp_ge_u32 s66,s59
v_mfma_f32_16x16x32_bf16 v[16:19],a[224:227],v[48:51],v[16:19]
ds_read_b128 v[44:47],v2 offset:35840
v_mfma_f32_16x16x32_bf16 v[20:23],a[228:231],v[48:51],v[20:23]
v_mfma_f32_16x16x32_bf16 v[16:19],a[232:235],v[52:55],v[16:19]
s_waitcnt lgkmcnt(2)
buffer_load_dwordx4 v0,s[36:39],s67 offen offset:2048 sc1 lds
v_mfma_f32_16x16x32_bf16 v[20:23],a[236:239],v[52:55],v[20:23]
ds_read_b128 v[48:51],v2 offset:36864
v_mfma_f32_16x16x32_bf16 v[16:19],a[240:243],v[56:59],v[16:19]
ds_read_b128 v[52:55],v2 offset:37888
v_mfma_f32_16x16x32_bf16 v[20:23],a[244:247],v[56:59],v[20:23]
v_mfma_f32_16x16x32_bf16 v[16:19],a[248:251],v[60:63],v[16:19]
s_waitcnt lgkmcnt(2)
ds_read_b128 v[56:59],v2 offset:38912
buffer_load_dwordx4 v0,s[36:39],s67 offen offset:3072 sc1 lds
v_mfma_f32_16x16x32_bf16 v[20:23],a[252:255],v[60:63],v[20:23]
s_cbranch_scc1 Lg2_slow1_%=
Lg2_d1w0_%=:
v_mfma_f32_16x16x32_bf16 v[24:27],a[0:3],v[32:35],0
s_add_u32 m0,s62,16384
ds_read_b128 v[60:63],v2 offset:39936
v_lshlrev_b32 v64,16,v80
v_mfma_f32_16x16x32_bf16 v[28:31],a[4:7],v[32:35],0
buffer_load_dwordx4 v1,s[36:39],s67 offen offset:0 sc1 lds
v_mfma_f32_16x16x32_bf16 v[24:27],a[8:11],v[36:39],v[24:27]
s_lshr_b32 s73,s48,0
ds_read_b128 v[32:35],v2 offset:40960
v_and_b32 v65,0xffff0000,v80
v_mfma_f32_16x16x32_bf16 v[28:31],a[12:15],v[36:39],v[28:31]
s_xor_b32 s87,s63,65536
v_lshlrev_b32 v66,16,v81
v_and_b32 v67,0xffff0000,v81
v_mfma_f32_16x16x32_bf16 v[24:27],a[16:19],v[40:43],v[24:27]
s_cmp_ge_u32 s67,s59
ds_read_b128 v[36:39],v2 offset:41984)"
R"(
v_lshlrev_b32 v68,16,v82
v_mfma_f32_16x16x32_bf16 v[28:31],a[20:23],v[40:43],v[28:31]
buffer_load_dwordx4 v1,s[36:39],s67 offen offset:1024 sc1 lds
v_mfma_f32_16x16x32_bf16 v[24:27],a[24:27],v[44:47],v[24:27]
s_cselect_b32 s86,s50,s51
ds_read_b128 v[40:43],v2 offset:43008
v_and_b32 v69,0xffff0000,v82
v_mfma_f32_16x16x32_bf16 v[28:31],a[28:31],v[44:47],v[28:31]
s_waitcnt lgkmcnt(3)
v_lshlrev_b32 v70,16,v83
v_and_b32 v71,0xffff0000,v83
v_mfma_f32_16x16x32_bf16 v[24:27],a[32:35],v[48:51],v[24:27]
s_cselect_b32 s87,s87,s64
ds_read_b128 v[44:47],v2 offset:44032
v_fma_f32 v64,v64,v89,v88
v_mfma_f32_16x16x32_bf16 v[28:31],a[36:39],v[48:51],v[28:31]
buffer_load_dwordx4 v1,s[36:39],s67 offen offset:2048 sc1 lds
v_mfma_f32_16x16x32_bf16 v[24:27],a[40:43],v[52:55],v[24:27]
s_lshr_b32 s72,s67,6
ds_read_b128 v[48:51],v2 offset:45056
v_fma_f32 v65,v65,v89,v88
v_mfma_f32_16x16x32_bf16 v[28:31],a[44:47],v[52:55],v[28:31]
s_lshr_b32 s71,s62,2
v_fma_f32 v66,v66,v89,v88
v_fma_f32 v67,v67,v89,v88
v_mfma_f32_16x16x32_bf16 v[24:27],a[48:51],v[56:59],v[24:27]
s_mov_b32 s62,s87
ds_read_b128 v[52:55],v2 offset:46080
v_fma_f32 v68,v68,v89,v88
v_mfma_f32_16x16x32_bf16 v[28:31],a[52:55],v[56:59],v[28:31]
buffer_load_dwordx4 v1,s[36:39],s67 offen offset:3072 sc1 lds
v_mfma_f32_16x16x32_bf16 v[24:27],a[56:59],v[60:63],v[24:27]
s_add_u32 m0,s71,s88
ds_read_b128 v[56:59],v2 offset:47104
v_fma_f32 v69,v69,v89,v88
v_mfma_f32_16x16x32_bf16 v[28:31],a[60:63],v[60:63],v[28:31]
s_waitcnt lgkmcnt(3)
v_fma_f32 v70,v70,v89,v88
v_fma_f32 v71,v71,v89,v88
v_mfma_f32_16x16x32_bf16 v[24:27],a[64:67],v[32:35],v[24:27]
s_waitcnt vmcnt(10)
ds_read_b128 v[60:63],v2 offset:48128
v_add_f32 v16,v16,v64
v_mfma_f32_16x16x32_bf16 v[28:31],a[68:71],v[32:35],v[28:31]
s_barrier
v_mfma_f32_16x16x32_bf16 v[24:27],a[72:75],v[36:39],v[24:27]
s_lshr_b32 s68,s51,5
ds_read_b128 v[32:35],v2 offset:49152
v_add_f32 v17,v17,v65
v_mfma_f32_16x16x32_bf16 v[28:31],a[76:79],v[36:39],v[28:31]
buffer_load_dwordx4 v10,s[36:39],s67 offen offset:0 sc1 lds
v_mfma_f32_16x16x32_bf16 v[24:27],a[80:83],v[40:43],v[24:27]
buffer_load_dword v11,s[36:39],s72 offen offset:1024 sc1 lds
v_mfma_f32_16x16x32_bf16 v[28:31],a[84:87],v[40:43],v[28:31]
s_lshr_b32 s70,s52,5
ds_read_b128 v[36:39],v2 offset:50176
v_add_f32 v18,v18,v66
v_mfma_f32_16x16x32_bf16 v[24:27],a[88:91],v[44:47],v[24:27]
s_mov_b64 s[44:45],s[46:47]
ds_read_b128 v[40:43],v2 offset:51200
v_add_f32 v19,v19,v67
v_mfma_f32_16x16x32_bf16 v[28:31],a[92:95],v[44:47],v[28:31]
s_waitcnt lgkmcnt(3)
v_add_f32 v20,v20,v68
v_add_f32 v21,v21,v69
v_add_f32 v22,v22,v70
v_add_f32 v23,v23,v71
v_cvt_pk_bf16_f32 v72,v16,v17
v_cvt_pk_bf16_f32 v73,v18,v19
v_cvt_pk_bf16_f32 v74,v20,v21
v_cvt_pk_bf16_f32 v75,v22,v23
v_mfma_f32_16x16x32_bf16 v[24:27],a[96:99],v[48:51],v[24:27]
buffer_store_dwordx4 v[72:75],v3,s[36:39],s73 offen offset:0 sc0 sc1
ds_read_b128 v[44:47],v2 offset:52224
v_add_f32 v64,v16,v17
v_mul_f32 v65,v16,v16
v_mfma_f32_16x16x32_bf16 v[28:31],a[100:103],v[48:51],v[28:31]
s_mov_b64 s[46:47],s[48:49]
v_fma_f32 v65,v17,v17,v65
v_add_f32 v64,v64,v18
v_mfma_f32_16x16x32_bf16 v[24:27],a[104:107],v[52:55],v[24:27]
s_mov_b64 s[48:49],s[50:51]
ds_read_b128 v[48:51],v2 offset:53248
v_fma_f32 v65,v18,v18,v65
v_mfma_f32_16x16x32_bf16 v[28:31],a[108:111],v[52:55],v[28:31]
s_mov_b64 s[50:51],s[52:53]
v_add_f32 v64,v64,v19
v_fma_f32 v65,v19,v19,v65
v_mfma_f32_16x16x32_bf16 v[24:27],a[112:115],v[56:59],v[24:27]
ds_read_b128 v[52:55],v2 offset:54272
v_add_f32 v64,v64,v20
v_mfma_f32_16x16x32_bf16 v[28:31],a[116:119],v[56:59],v[28:31]
v_fma_f32 v65,v20,v20,v65
v_add_f32 v64,v64,v21
v_fma_f32 v65,v21,v21,v65
v_add_f32 v64,v64,v22
v_fma_f32 v65,v22,v22,v65
v_add_f32 v64,v64,v23
v_fma_f32 v65,v23,v23,v65
v_mfma_f32_16x16x32_bf16 v[24:27],a[120:123],v[60:63],v[24:27]
s_nop 0
v_permlane16_swap_b32 v64,v65
v_add_f32 v64,v64,v65
v_mov_b32 v65,v64
v_mfma_f32_16x16x32_bf16 v[28:31],a[124:127],v[60:63],v[28:31]
s_waitcnt lgkmcnt(2)
ds_read_b128 v[56:59],v2 offset:55296
v_permlane32_swap_b32 v64,v65
v_add_f32 v64,v64,v65
v_mfma_f32_16x16x32_bf16 v[24:27],a[128:131],v[32:35],v[24:27]
ds_write_b32 v13,v64 offset:2048
v_mfma_f32_16x16x32_bf16 v[28:31],a[132:135],v[32:35],v[28:31]
ds_read_b128 v[60:63],v2 offset:56320
v_mfma_f32_16x16x32_bf16 v[24:27],a[136:139],v[36:39],v[24:27]
ds_read_b128 v[32:35],v2 offset:57344
v_mfma_f32_16x16x32_bf16 v[28:31],a[140:143],v[36:39],v[28:31]
v_mfma_f32_16x16x32_bf16 v[24:27],a[144:147],v[40:43],v[24:27]
s_waitcnt vmcnt(11) lgkmcnt(2)
ds_read_b128 v[36:39],v2 offset:58368
v_mfma_f32_16x16x32_bf16 v[28:31],a[148:151],v[40:43],v[28:31]
s_barrier
v_mfma_f32_16x16x32_bf16 v[24:27],a[152:155],v[44:47],v[24:27]
ds_read_b128 v[40:43],v2 offset:59392
ds_read_b32 v15,v7 offset:256
v_mfma_f32_16x16x32_bf16 v[28:31],a[156:159],v[44:47],v[28:31]
s_waitcnt lgkmcnt(4)
v_mfma_f32_16x16x32_bf16 v[24:27],a[160:163],v[48:51],v[24:27]
ds_read_b128 v[44:47],v2 offset:60416
v_mfma_f32_16x16x32_bf16 v[28:31],a[164:167],v[48:51],v[28:31]
v_mfma_f32_16x16x32_bf16 v[24:27],a[168:171],v[52:55],v[24:27]
s_waitcnt lgkmcnt(1)
ds_read_b128 v[48:51],v2 offset:61440
v_mfma_f32_16x16x32_bf16 v[28:31],a[172:175],v[52:55],v[28:31]
v_cmp_eq_u32 vcc,s60,v15
v_mfma_f32_16x16x32_bf16 v[24:27],a[176:179],v[56:59],v[24:27]
s_cmp_eq_u32 vcc_lo,-1
ds_read_b128 v[52:55],v2 offset:62464
s_cselect_b32 s66,s86,s59
v_mfma_f32_16x16x32_bf16 v[28:31],a[180:183],v[56:59],v[28:31]
s_cmp_ge_u32 s66,s59
s_cselect_b32 s68,s68,s70
s_add_u32 m0,s78,0
v_mfma_f32_16x16x32_bf16 v[24:27],a[184:187],v[60:63],v[24:27]
buffer_load_dword v6,s[36:39],s68 offen offset:0 sc1 lds
v_mfma_f32_16x16x32_bf16 v[28:31],a[188:191],v[60:63],v[28:31]
s_waitcnt lgkmcnt(2)
ds_read_b128 v[56:59],v2 offset:63488
s_xor_b32 s64,s64,65536
v_mfma_f32_16x16x32_bf16 v[24:27],a[192:195],v[32:35],v[24:27]
s_waitcnt vmcnt(8)
ds_read_b128 v[60:63],v2 offset:64512
v_xor_b32 v2,65536,v2
v_mfma_f32_16x16x32_bf16 v[28:31],a[196:199],v[32:35],v[28:31]
s_barrier
s_mov_b32 m0,s62
v_mfma_f32_16x16x32_bf16 v[24:27],a[200:203],v[36:39],v[24:27]
buffer_load_dwordx4 v0,s[36:39],s66 offen offset:0 sc1 lds
v_mfma_f32_16x16x32_bf16 v[28:31],a[204:207],v[36:39],v[28:31]
s_add_u32 s74,s53,s55
ds_read_b128 v[32:35],v2 offset:0
ds_read_b128 v[84:87],v8 offset:8192
v_mfma_f32_16x16x32_bf16 v[24:27],a[208:211],v[40:43],v[24:27]
s_cmp_eq_u32 s74,s56
ds_read_b128 v[36:39],v2 offset:1024
ds_read_b64 v[90:91],v9 offset:9216
v_mfma_f32_16x16x32_bf16 v[28:31],a[212:215],v[40:43],v[28:31]
s_cselect_b32 s74,s57,s74
v_xor_b32 v8,16384,v8
v_xor_b32 v9,16384,v9
v_mfma_f32_16x16x32_bf16 v[24:27],a[216:219],v[44:47],v[24:27]
buffer_load_dwordx4 v0,s[36:39],s66 offen offset:1024 sc1 lds
v_mfma_f32_16x16x32_bf16 v[28:31],a[220:223],v[44:47],v[28:31]
s_waitcnt lgkmcnt(4)
ds_read_b128 v[40:43],v2 offset:2048
s_cmp_eq_u32 s74,s58
v_mfma_f32_16x16x32_bf16 v[24:27],a[224:227],v[48:51],v[24:27]
s_cselect_b32 s74,s59,s74
ds_read_b128 v[44:47],v2 offset:3072
s_mov_b32 s52,s74
v_mfma_f32_16x16x32_bf16 v[28:31],a[228:231],v[48:51],v[28:31]
s_cmp_lt_u32 s67,s59
v_mfma_f32_16x16x32_bf16 v[24:27],a[232:235],v[52:55],v[24:27]
s_waitcnt lgkmcnt(2)
buffer_load_dwordx4 v0,s[36:39],s66 offen offset:2048 sc1 lds
v_mfma_f32_16x16x32_bf16 v[28:31],a[236:239],v[52:55],v[28:31]
ds_read_b128 v[48:51],v2 offset:4096
v_mfma_f32_16x16x32_bf16 v[24:27],a[240:243],v[56:59],v[24:27]
ds_read_b128 v[52:55],v2 offset:5120
v_mfma_f32_16x16x32_bf16 v[28:31],a[244:247],v[56:59],v[28:31]
v_mfma_f32_16x16x32_bf16 v[24:27],a[248:251],v[60:63],v[24:27]
s_waitcnt lgkmcnt(2)
ds_read_b128 v[56:59],v2 offset:6144
buffer_load_dwordx4 v0,s[36:39],s66 offen offset:3072 sc1 lds
v_mfma_f32_16x16x32_bf16 v[28:31],a[252:255],v[60:63],v[28:31]
s_cbranch_scc1 Lg2_d0w0_%=
s_branch Lg2_slow0_%=
Lg2_d0w1_%=:
v_mfma_f32_16x16x32_bf16 v[16:19],a[0:3],v[32:35],0
s_add_u32 m0,s62,16384
ds_read_b128 v[60:63],v2 offset:7168)"
R"(
v_lshlrev_b32 v64,16,v84
v_mfma_f32_16x16x32_bf16 v[20:23],a[4:7],v[32:35],0
s_lshr_b32 s73,s47,0
v_and_b32 v65,0xffff0000,v84
v_lshlrev_b32 v66,16,v85
v_mfma_f32_16x16x32_bf16 v[16:19],a[8:11],v[36:39],v[16:19]
buffer_load_dwordx4 v1,s[36:39],s66 offen offset:0 sc1 lds
v_mfma_f32_16x16x32_bf16 v[20:23],a[12:15],v[36:39],v[20:23]
s_xor_b32 s87,s64,65536
ds_read_b128 v[32:35],v2 offset:8192
v_and_b32 v67,0xffff0000,v85
v_mfma_f32_16x16x32_bf16 v[16:19],a[16:19],v[40:43],v[16:19]
s_cmp_ge_u32 s66,s59
ds_read_b128 v[36:39],v2 offset:9216
v_lshlrev_b32 v68,16,v86
v_mfma_f32_16x16x32_bf16 v[20:23],a[20:23],v[40:43],v[20:23]
s_cselect_b32 s86,s49,s50
v_and_b32 v69,0xffff0000,v86
v_lshlrev_b32 v70,16,v87
v_mfma_f32_16x16x32_bf16 v[16:19],a[24:27],v[44:47],v[16:19]
buffer_load_dwordx4 v1,s[36:39],s66 offen offset:1024 sc1 lds
v_mfma_f32_16x16x32_bf16 v[20:23],a[28:31],v[44:47],v[20:23]
s_waitcnt lgkmcnt(2)
ds_read_b128 v[40:43],v2 offset:10240
v_and_b32 v71,0xffff0000,v87
v_mfma_f32_16x16x32_bf16 v[16:19],a[32:35],v[48:51],v[16:19]
s_cselect_b32 s87,s87,s63
ds_read_b128 v[44:47],v2 offset:11264
v_fma_f32 v64,v64,v91,v90
v_mfma_f32_16x16x32_bf16 v[20:23],a[36:39],v[48:51],v[20:23]
s_lshr_b32 s72,s66,6
v_fma_f32 v65,v65,v91,v90
v_fma_f32 v66,v66,v91,v90
v_mfma_f32_16x16x32_bf16 v[16:19],a[40:43],v[52:55],v[16:19]
buffer_load_dwordx4 v1,s[36:39],s66 offen offset:2048 sc1 lds
v_mfma_f32_16x16x32_bf16 v[20:23],a[44:47],v[52:55],v[20:23]
s_lshr_b32 s71,s62,2
ds_read_b128 v[48:51],v2 offset:12288
v_fma_f32 v67,v67,v91,v90
v_mfma_f32_16x16x32_bf16 v[16:19],a[48:51],v[56:59],v[16:19]
s_mov_b32 s62,s87
ds_read_b128 v[52:55],v2 offset:13312
v_fma_f32 v68,v68,v91,v90
v_mfma_f32_16x16x32_bf16 v[20:23],a[52:55],v[56:59],v[20:23]
v_fma_f32 v69,v69,v91,v90
v_fma_f32 v70,v70,v91,v90
v_mfma_f32_16x16x32_bf16 v[16:19],a[56:59],v[60:63],v[16:19]
buffer_load_dwordx4 v1,s[36:39],s66 offen offset:3072 sc1 lds
v_mfma_f32_16x16x32_bf16 v[20:23],a[60:63],v[60:63],v[20:23]
s_waitcnt lgkmcnt(2)
ds_read_b128 v[56:59],v2 offset:14336
v_fma_f32 v71,v71,v91,v90
v_mfma_f32_16x16x32_bf16 v[16:19],a[64:67],v[32:35],v[16:19]
s_waitcnt vmcnt(9)
ds_read_b128 v[60:63],v2 offset:15360
v_add_f32 v24,v24,v64
v_mfma_f32_16x16x32_bf16 v[20:23],a[68:71],v[32:35],v[20:23]
s_barrier
v_mfma_f32_16x16x32_bf16 v[16:19],a[72:75],v[36:39],v[16:19]
s_add_u32 m0,s71,s88
ds_read_b128 v[32:35],v2 offset:16384
v_add_f32 v25,v25,v65
v_mfma_f32_16x16x32_bf16 v[20:23],a[76:79],v[36:39],v[20:23]
v_add_f32 v26,v26,v66
v_add_f32 v27,v27,v67
v_mfma_f32_16x16x32_bf16 v[16:19],a[80:83],v[40:43],v[16:19]
buffer_load_dwordx4 v10,s[36:39],s66 offen offset:0 sc1 lds
v_mfma_f32_16x16x32_bf16 v[20:23],a[84:87],v[40:43],v[20:23]
ds_read_b128 v[36:39],v2 offset:17408
v_add_f32 v28,v28,v68
v_mfma_f32_16x16x32_bf16 v[16:19],a[88:91],v[44:47],v[16:19]
buffer_load_dword v11,s[36:39],s72 offen offset:1024 sc1 lds
v_mfma_f32_16x16x32_bf16 v[20:23],a[92:95],v[44:47],v[20:23]
s_waitcnt lgkmcnt(2)
ds_read_b128 v[40:43],v2 offset:18432
v_add_f32 v29,v29,v69
v_mfma_f32_16x16x32_bf16 v[16:19],a[96:99],v[48:51],v[16:19]
s_mov_b32 m0,s62
v_add_f32 v30,v30,v70
v_add_f32 v31,v31,v71
v_cvt_pk_bf16_f32 v72,v24,v25
v_cvt_pk_bf16_f32 v73,v26,v27
v_cvt_pk_bf16_f32 v74,v28,v29
v_cvt_pk_bf16_f32 v75,v30,v31
v_mfma_f32_16x16x32_bf16 v[20:23],a[100:103],v[48:51],v[20:23]
ds_read_b128 v[44:47],v2 offset:19456
buffer_store_dwordx4 v[72:75],v3,s[36:39],s73 offen offset:0 sc0 sc1
v_add_f32 v64,v24,v25
v_mul_f32 v65,v24,v24
v_fma_f32 v65,v25,v25,v65
v_mfma_f32_16x16x32_bf16 v[16:19],a[104:107],v[52:55],v[16:19]
ds_read_b128 v[48:51],v2 offset:20480
v_add_f32 v64,v64,v26
v_mfma_f32_16x16x32_bf16 v[20:23],a[108:111],v[52:55],v[20:23]
v_fma_f32 v65,v26,v26,v65
v_add_f32 v64,v64,v27
v_mfma_f32_16x16x32_bf16 v[16:19],a[112:115],v[56:59],v[16:19]
ds_read_b128 v[52:55],v2 offset:21504
v_fma_f32 v65,v27,v27,v65
v_mfma_f32_16x16x32_bf16 v[20:23],a[116:119],v[56:59],v[20:23]
v_add_f32 v64,v64,v28
v_fma_f32 v65,v28,v28,v65
v_add_f32 v64,v64,v29
v_fma_f32 v65,v29,v29,v65
v_add_f32 v64,v64,v30
v_fma_f32 v65,v30,v30,v65
v_add_f32 v64,v64,v31
v_fma_f32 v65,v31,v31,v65
v_mfma_f32_16x16x32_bf16 v[16:19],a[120:123],v[60:63],v[16:19]
s_nop 0
v_permlane16_swap_b32 v64,v65
v_add_f32 v64,v64,v65
v_mov_b32 v65,v64
v_mfma_f32_16x16x32_bf16 v[20:23],a[124:127],v[60:63],v[20:23]
s_waitcnt lgkmcnt(2)
ds_read_b128 v[56:59],v2 offset:22528
v_permlane32_swap_b32 v64,v65
v_add_f32 v64,v64,v65
v_mfma_f32_16x16x32_bf16 v[16:19],a[128:131],v[32:35],v[16:19]
ds_write_b32 v13,v64 offset:0
v_mfma_f32_16x16x32_bf16 v[20:23],a[132:135],v[32:35],v[20:23]
ds_read_b128 v[60:63],v2 offset:23552
v_mfma_f32_16x16x32_bf16 v[16:19],a[136:139],v[36:39],v[16:19]
ds_read_b128 v[32:35],v2 offset:24576
v_mfma_f32_16x16x32_bf16 v[20:23],a[140:143],v[36:39],v[20:23]
v_mfma_f32_16x16x32_bf16 v[16:19],a[144:147],v[40:43],v[16:19]
s_waitcnt vmcnt(11) lgkmcnt(2)
ds_read_b128 v[36:39],v2 offset:25600
v_mfma_f32_16x16x32_bf16 v[20:23],a[148:151],v[40:43],v[20:23]
s_barrier
v_mfma_f32_16x16x32_bf16 v[16:19],a[152:155],v[44:47],v[16:19]
ds_read_b128 v[40:43],v2 offset:26624
ds_read_b32 v15,v7 offset:0
v_mfma_f32_16x16x32_bf16 v[20:23],a[156:159],v[44:47],v[20:23]
s_waitcnt lgkmcnt(4)
v_mfma_f32_16x16x32_bf16 v[16:19],a[160:163],v[48:51],v[16:19]
ds_read_b128 v[44:47],v2 offset:27648
v_mfma_f32_16x16x32_bf16 v[20:23],a[164:167],v[48:51],v[20:23]
v_mfma_f32_16x16x32_bf16 v[16:19],a[168:171],v[52:55],v[16:19]
s_waitcnt lgkmcnt(1)
ds_read_b128 v[48:51],v2 offset:28672
v_mfma_f32_16x16x32_bf16 v[20:23],a[172:175],v[52:55],v[20:23]
v_cmp_eq_u32 vcc,s60,v15
v_mfma_f32_16x16x32_bf16 v[16:19],a[176:179],v[56:59],v[16:19]
s_cmp_eq_u32 vcc_lo,-1
ds_read_b128 v[52:55],v2 offset:29696
s_cselect_b32 s67,s86,s59
v_mfma_f32_16x16x32_bf16 v[20:23],a[180:183],v[56:59],v[20:23]
buffer_load_dwordx4 v0,s[36:39],s67 offen offset:0 sc1 lds
v_mfma_f32_16x16x32_bf16 v[16:19],a[184:187],v[60:63],v[16:19]
s_xor_b32 s63,s63,65536
ds_read_b128 v[56:59],v2 offset:30720
s_add_u32 s53,s52,s55
v_mfma_f32_16x16x32_bf16 v[20:23],a[188:191],v[60:63],v[20:23]
s_waitcnt lgkmcnt(3)
s_cmp_eq_u32 s53,s56
s_cselect_b32 s53,s57,s53
v_mfma_f32_16x16x32_bf16 v[16:19],a[192:195],v[32:35],v[16:19]
s_waitcnt vmcnt(8)
ds_read_b128 v[60:63],v2 offset:31744
s_cmp_eq_u32 s53,s58
v_mfma_f32_16x16x32_bf16 v[20:23],a[196:199],v[32:35],v[20:23]
s_barrier
v_mfma_f32_16x16x32_bf16 v[16:19],a[200:203],v[36:39],v[16:19]
s_cselect_b32 s53,s59,s53
ds_read_b128 v[32:35],v2 offset:32768
ds_read_b128 v[80:83],v8 offset:0
v_mfma_f32_16x16x32_bf16 v[20:23],a[204:207],v[36:39],v[20:23]
buffer_load_dwordx4 v0,s[36:39],s67 offen offset:1024 sc1 lds
v_mfma_f32_16x16x32_bf16 v[16:19],a[208:211],v[40:43],v[16:19]
s_cmp_ge_u32 s66,s59
ds_read_b128 v[36:39],v2 offset:33792
ds_read_b64 v[88:89],v9 offset:1024
v_mfma_f32_16x16x32_bf16 v[20:23],a[212:215],v[40:43],v[20:23]
v_mfma_f32_16x16x32_bf16 v[16:19],a[216:219],v[44:47],v[16:19]
ds_read_b128 v[40:43],v2 offset:34816
v_mfma_f32_16x16x32_bf16 v[20:23],a[220:223],v[44:47],v[20:23]
s_waitcnt lgkmcnt(5)
buffer_load_dwordx4 v0,s[36:39],s67 offen offset:2048 sc1 lds
v_mfma_f32_16x16x32_bf16 v[16:19],a[224:227],v[48:51],v[16:19]
ds_read_b128 v[44:47],v2 offset:35840
v_mfma_f32_16x16x32_bf16 v[20:23],a[228:231],v[48:51],v[20:23]
v_mfma_f32_16x16x32_bf16 v[16:19],a[232:235],v[52:55],v[16:19]
s_waitcnt lgkmcnt(2)
ds_read_b128 v[48:51],v2 offset:36864
v_mfma_f32_16x16x32_bf16 v[20:23],a[236:239],v[52:55],v[20:23]
buffer_load_dwordx4 v0,s[36:39],s67 offen offset:3072 sc1 lds
v_mfma_f32_16x16x32_bf16 v[16:19],a[240:243],v[56:59],v[16:19]
ds_read_b128 v[52:55],v2 offset:37888
v_mfma_f32_16x16x32_bf16 v[20:23],a[244:247],v[56:59],v[20:23]
v_mfma_f32_16x16x32_bf16 v[16:19],a[248:251],v[60:63],v[16:19]
s_waitcnt lgkmcnt(2)
ds_read_b128 v[56:59],v2 offset:38912
v_mfma_f32_16x16x32_bf16 v[20:23],a[252:255],v[60:63],v[20:23]
s_cbranch_scc1 Lg2_slow1_%=
Lg2_d1w1_%=:
v_mfma_f32_16x16x32_bf16 v[24:27],a[0:3],v[32:35],0
s_add_u32 m0,s62,16384
ds_read_b128 v[60:63],v2 offset:39936
v_lshlrev_b32 v64,16,v80)"
R"(
v_mfma_f32_16x16x32_bf16 v[28:31],a[4:7],v[32:35],0
s_lshr_b32 s73,s48,0
v_and_b32 v65,0xffff0000,v80
v_lshlrev_b32 v66,16,v81
v_mfma_f32_16x16x32_bf16 v[24:27],a[8:11],v[36:39],v[24:27]
buffer_load_dwordx4 v1,s[36:39],s67 offen offset:0 sc1 lds
v_mfma_f32_16x16x32_bf16 v[28:31],a[12:15],v[36:39],v[28:31]
s_xor_b32 s87,s63,65536
ds_read_b128 v[32:35],v2 offset:40960
v_and_b32 v67,0xffff0000,v81
v_mfma_f32_16x16x32_bf16 v[24:27],a[16:19],v[40:43],v[24:27]
s_cmp_ge_u32 s67,s59
ds_read_b128 v[36:39],v2 offset:41984
v_lshlrev_b32 v68,16,v82
v_mfma_f32_16x16x32_bf16 v[28:31],a[20:23],v[40:43],v[28:31]
s_cselect_b32 s86,s50,s51
v_and_b32 v69,0xffff0000,v82
v_lshlrev_b32 v70,16,v83
v_mfma_f32_16x16x32_bf16 v[24:27],a[24:27],v[44:47],v[24:27]
buffer_load_dwordx4 v1,s[36:39],s67 offen offset:1024 sc1 lds
v_mfma_f32_16x16x32_bf16 v[28:31],a[28:31],v[44:47],v[28:31]
s_waitcnt lgkmcnt(2)
ds_read_b128 v[40:43],v2 offset:43008
v_and_b32 v71,0xffff0000,v83
v_mfma_f32_16x16x32_bf16 v[24:27],a[32:35],v[48:51],v[24:27]
s_cselect_b32 s87,s87,s64
ds_read_b128 v[44:47],v2 offset:44032
v_fma_f32 v64,v64,v89,v88
v_mfma_f32_16x16x32_bf16 v[28:31],a[36:39],v[48:51],v[28:31]
s_lshr_b32 s72,s67,6
v_fma_f32 v65,v65,v89,v88
v_fma_f32 v66,v66,v89,v88
v_mfma_f32_16x16x32_bf16 v[24:27],a[40:43],v[52:55],v[24:27]
buffer_load_dwordx4 v1,s[36:39],s67 offen offset:2048 sc1 lds
v_mfma_f32_16x16x32_bf16 v[28:31],a[44:47],v[52:55],v[28:31]
s_lshr_b32 s71,s62,2
ds_read_b128 v[48:51],v2 offset:45056
v_fma_f32 v67,v67,v89,v88
v_mfma_f32_16x16x32_bf16 v[24:27],a[48:51],v[56:59],v[24:27]
s_mov_b32 s62,s87
ds_read_b128 v[52:55],v2 offset:46080
v_fma_f32 v68,v68,v89,v88
v_mfma_f32_16x16x32_bf16 v[28:31],a[52:55],v[56:59],v[28:31]
s_mov_b64 s[44:45],s[46:47]
v_fma_f32 v69,v69,v89,v88
v_fma_f32 v70,v70,v89,v88
v_mfma_f32_16x16x32_bf16 v[24:27],a[56:59],v[60:63],v[24:27]
buffer_load_dwordx4 v1,s[36:39],s67 offen offset:3072 sc1 lds
v_mfma_f32_16x16x32_bf16 v[28:31],a[60:63],v[60:63],v[28:31]
s_waitcnt lgkmcnt(2)
ds_read_b128 v[56:59],v2 offset:47104
v_fma_f32 v71,v71,v89,v88
v_mfma_f32_16x16x32_bf16 v[24:27],a[64:67],v[32:35],v[24:27]
s_waitcnt vmcnt(9)
ds_read_b128 v[60:63],v2 offset:48128
v_add_f32 v16,v16,v64
v_mfma_f32_16x16x32_bf16 v[28:31],a[68:71],v[32:35],v[28:31]
s_barrier
v_mfma_f32_16x16x32_bf16 v[24:27],a[72:75],v[36:39],v[24:27]
s_add_u32 m0,s71,s88
ds_read_b128 v[32:35],v2 offset:49152
v_add_f32 v17,v17,v65
v_mfma_f32_16x16x32_bf16 v[28:31],a[76:79],v[36:39],v[28:31]
s_mov_b64 s[46:47],s[48:49]
v_add_f32 v18,v18,v66
v_add_f32 v19,v19,v67
v_mfma_f32_16x16x32_bf16 v[24:27],a[80:83],v[40:43],v[24:27]
buffer_load_dwordx4 v10,s[36:39],s67 offen offset:0 sc1 lds
v_mfma_f32_16x16x32_bf16 v[28:31],a[84:87],v[40:43],v[28:31]
s_mov_b64 s[48:49],s[50:51]
ds_read_b128 v[36:39],v2 offset:50176
v_add_f32 v20,v20,v68
v_mfma_f32_16x16x32_bf16 v[24:27],a[88:91],v[44:47],v[24:27]
buffer_load_dword v11,s[36:39],s72 offen offset:1024 sc1 lds
v_mfma_f32_16x16x32_bf16 v[28:31],a[92:95],v[44:47],v[28:31]
s_waitcnt lgkmcnt(2)
ds_read_b128 v[40:43],v2 offset:51200
v_add_f32 v21,v21,v69
v_mfma_f32_16x16x32_bf16 v[24:27],a[96:99],v[48:51],v[24:27]
s_mov_b32 m0,s62
v_add_f32 v22,v22,v70
v_add_f32 v23,v23,v71
v_cvt_pk_bf16_f32 v72,v16,v17
v_cvt_pk_bf16_f32 v73,v18,v19
v_cvt_pk_bf16_f32 v74,v20,v21
v_cvt_pk_bf16_f32 v75,v22,v23
v_mfma_f32_16x16x32_bf16 v[28:31],a[100:103],v[48:51],v[28:31]
s_mov_b64 s[50:51],s[52:53]
ds_read_b128 v[44:47],v2 offset:52224
buffer_store_dwordx4 v[72:75],v3,s[36:39],s73 offen offset:0 sc0 sc1
v_add_f32 v64,v16,v17
v_mul_f32 v65,v16,v16
v_fma_f32 v65,v17,v17,v65
v_mfma_f32_16x16x32_bf16 v[24:27],a[104:107],v[52:55],v[24:27]
ds_read_b128 v[48:51],v2 offset:53248
v_add_f32 v64,v64,v18
v_mfma_f32_16x16x32_bf16 v[28:31],a[108:111],v[52:55],v[28:31]
v_fma_f32 v65,v18,v18,v65
v_add_f32 v64,v64,v19
v_mfma_f32_16x16x32_bf16 v[24:27],a[112:115],v[56:59],v[24:27]
ds_read_b128 v[52:55],v2 offset:54272
v_fma_f32 v65,v19,v19,v65
v_mfma_f32_16x16x32_bf16 v[28:31],a[116:119],v[56:59],v[28:31]
v_add_f32 v64,v64,v20
v_fma_f32 v65,v20,v20,v65
v_add_f32 v64,v64,v21
v_fma_f32 v65,v21,v21,v65
v_add_f32 v64,v64,v22
v_fma_f32 v65,v22,v22,v65
v_add_f32 v64,v64,v23
v_fma_f32 v65,v23,v23,v65
v_mfma_f32_16x16x32_bf16 v[24:27],a[120:123],v[60:63],v[24:27]
s_nop 0
v_permlane16_swap_b32 v64,v65
v_add_f32 v64,v64,v65
v_mov_b32 v65,v64
v_mfma_f32_16x16x32_bf16 v[28:31],a[124:127],v[60:63],v[28:31]
s_waitcnt lgkmcnt(2)
ds_read_b128 v[56:59],v2 offset:55296
v_permlane32_swap_b32 v64,v65
v_add_f32 v64,v64,v65
v_mfma_f32_16x16x32_bf16 v[24:27],a[128:131],v[32:35],v[24:27]
ds_write_b32 v13,v64 offset:2048
v_mfma_f32_16x16x32_bf16 v[28:31],a[132:135],v[32:35],v[28:31]
ds_read_b128 v[60:63],v2 offset:56320
v_mfma_f32_16x16x32_bf16 v[24:27],a[136:139],v[36:39],v[24:27]
ds_read_b128 v[32:35],v2 offset:57344
v_mfma_f32_16x16x32_bf16 v[28:31],a[140:143],v[36:39],v[28:31]
v_mfma_f32_16x16x32_bf16 v[24:27],a[144:147],v[40:43],v[24:27]
s_waitcnt vmcnt(11) lgkmcnt(2)
ds_read_b128 v[36:39],v2 offset:58368
v_mfma_f32_16x16x32_bf16 v[28:31],a[148:151],v[40:43],v[28:31]
s_barrier
v_mfma_f32_16x16x32_bf16 v[24:27],a[152:155],v[44:47],v[24:27]
ds_read_b128 v[40:43],v2 offset:59392
ds_read_b32 v15,v7 offset:256
v_mfma_f32_16x16x32_bf16 v[28:31],a[156:159],v[44:47],v[28:31]
s_waitcnt lgkmcnt(4)
v_mfma_f32_16x16x32_bf16 v[24:27],a[160:163],v[48:51],v[24:27]
ds_read_b128 v[44:47],v2 offset:60416
v_mfma_f32_16x16x32_bf16 v[28:31],a[164:167],v[48:51],v[28:31]
v_mfma_f32_16x16x32_bf16 v[24:27],a[168:171],v[52:55],v[24:27]
s_waitcnt lgkmcnt(1)
ds_read_b128 v[48:51],v2 offset:61440
v_mfma_f32_16x16x32_bf16 v[28:31],a[172:175],v[52:55],v[28:31]
v_cmp_eq_u32 vcc,s60,v15
v_mfma_f32_16x16x32_bf16 v[24:27],a[176:179],v[56:59],v[24:27]
s_cmp_eq_u32 vcc_lo,-1
ds_read_b128 v[52:55],v2 offset:62464
s_cselect_b32 s66,s86,s59
v_mfma_f32_16x16x32_bf16 v[28:31],a[180:183],v[56:59],v[28:31]
buffer_load_dwordx4 v0,s[36:39],s66 offen offset:0 sc1 lds
v_mfma_f32_16x16x32_bf16 v[24:27],a[184:187],v[60:63],v[24:27]
s_xor_b32 s64,s64,65536
ds_read_b128 v[56:59],v2 offset:63488
s_add_u32 s74,s53,s55
v_mfma_f32_16x16x32_bf16 v[28:31],a[188:191],v[60:63],v[28:31]
s_waitcnt lgkmcnt(3)
s_cmp_eq_u32 s74,s56
s_cselect_b32 s74,s57,s74
v_mfma_f32_16x16x32_bf16 v[24:27],a[192:195],v[32:35],v[24:27]
s_waitcnt vmcnt(8)
ds_read_b128 v[60:63],v2 offset:64512
v_xor_b32 v2,65536,v2
v_mfma_f32_16x16x32_bf16 v[28:31],a[196:199],v[32:35],v[28:31]
s_barrier
v_mfma_f32_16x16x32_bf16 v[24:27],a[200:203],v[36:39],v[24:27]
s_cmp_eq_u32 s74,s58
ds_read_b128 v[32:35],v2 offset:0
ds_read_b128 v[84:87],v8 offset:8192
v_mfma_f32_16x16x32_bf16 v[28:31],a[204:207],v[36:39],v[28:31]
buffer_load_dwordx4 v0,s[36:39],s66 offen offset:1024 sc1 lds
v_mfma_f32_16x16x32_bf16 v[24:27],a[208:211],v[40:43],v[24:27]
s_cselect_b32 s74,s59,s74
ds_read_b128 v[36:39],v2 offset:1024
ds_read_b64 v[90:91],v9 offset:9216
v_mfma_f32_16x16x32_bf16 v[28:31],a[212:215],v[40:43],v[28:31]
s_mov_b32 s52,s74
v_xor_b32 v8,16384,v8
v_xor_b32 v9,16384,v9
v_mfma_f32_16x16x32_bf16 v[24:27],a[216:219],v[44:47],v[24:27]
s_cmp_lt_u32 s67,s59
ds_read_b128 v[40:43],v2 offset:2048
v_mfma_f32_16x16x32_bf16 v[28:31],a[220:223],v[44:47],v[28:31]
s_waitcnt lgkmcnt(5)
buffer_load_dwordx4 v0,s[36:39],s66 offen offset:2048 sc1 lds
v_mfma_f32_16x16x32_bf16 v[24:27],a[224:227],v[48:51],v[24:27]
ds_read_b128 v[44:47],v2 offset:3072
v_mfma_f32_16x16x32_bf16 v[28:31],a[228:231],v[48:51],v[28:31]
v_mfma_f32_16x16x32_bf16 v[24:27],a[232:235],v[52:55],v[24:27]
s_waitcnt lgkmcnt(2)
ds_read_b128 v[48:51],v2 offset:4096
v_mfma_f32_16x16x32_bf16 v[28:31],a[236:239],v[52:55],v[28:31]
buffer_load_dwordx4 v0,s[36:39],s66 offen offset:3072 sc1 lds
v_mfma_f32_16x16x32_bf16 v[24:27],a[240:243],v[56:59],v[24:27]
ds_read_b128 v[52:55],v2 offset:5120
v_mfma_f32_16x16x32_bf16 v[28:31],a[244:247],v[56:59],v[28:31]
v_mfma_f32_16x16x32_bf16 v[24:27],a[248:251],v[60:63],v[24:27]
s_waitcnt lgkmcnt(2)
ds_read_b128 v[56:59],v2 offset:6144)"
R"(
v_mfma_f32_16x16x32_bf16 v[28:31],a[252:255],v[60:63],v[28:31]
s_cbranch_scc1 Lg2_d0w1_%=
s_branch Lg2_slow0_%=
Lg2_d0w2_%=:
v_mfma_f32_16x16x32_bf16 v[16:19],a[0:3],v[32:35],0
s_add_u32 m0,s62,16384
ds_read_b128 v[60:63],v2 offset:7168
v_lshlrev_b32 v64,16,v84
v_mfma_f32_16x16x32_bf16 v[20:23],a[4:7],v[32:35],0
s_lshr_b32 s73,s47,0
v_and_b32 v65,0xffff0000,v84
v_lshlrev_b32 v66,16,v85
v_mfma_f32_16x16x32_bf16 v[16:19],a[8:11],v[36:39],v[16:19]
s_lshr_b32 s85,s47,5
ds_read_b128 v[32:35],v2 offset:8192
v_and_b32 v67,0xffff0000,v85
v_mfma_f32_16x16x32_bf16 v[20:23],a[12:15],v[36:39],v[20:23]
buffer_load_dwordx4 v1,s[36:39],s66 offen offset:0 sc1 lds
v_mfma_f32_16x16x32_bf16 v[16:19],a[16:19],v[40:43],v[16:19]
s_xor_b32 s87,s64,65536
ds_read_b128 v[36:39],v2 offset:9216
v_lshlrev_b32 v68,16,v86
v_mfma_f32_16x16x32_bf16 v[20:23],a[20:23],v[40:43],v[20:23]
s_cmp_ge_u32 s66,s59
v_and_b32 v69,0xffff0000,v86
v_lshlrev_b32 v70,16,v87
v_mfma_f32_16x16x32_bf16 v[16:19],a[24:27],v[44:47],v[16:19]
s_cselect_b32 s86,s49,s50
ds_read_b128 v[40:43],v2 offset:10240
v_and_b32 v71,0xffff0000,v87
v_mfma_f32_16x16x32_bf16 v[20:23],a[28:31],v[44:47],v[20:23]
s_waitcnt lgkmcnt(3)
buffer_load_dwordx4 v1,s[36:39],s66 offen offset:1024 sc1 lds
v_mfma_f32_16x16x32_bf16 v[16:19],a[32:35],v[48:51],v[16:19]
s_cselect_b32 s87,s87,s63
ds_read_b128 v[44:47],v2 offset:11264
v_fma_f32 v64,v64,v91,v90
v_mfma_f32_16x16x32_bf16 v[20:23],a[36:39],v[48:51],v[20:23]
s_lshr_b32 s72,s66,6
v_fma_f32 v65,v65,v91,v90
v_fma_f32 v66,v66,v91,v90
v_mfma_f32_16x16x32_bf16 v[16:19],a[40:43],v[52:55],v[16:19]
s_lshr_b32 s71,s62,2
ds_read_b128 v[48:51],v2 offset:12288
v_fma_f32 v67,v67,v91,v90
v_mfma_f32_16x16x32_bf16 v[20:23],a[44:47],v[52:55],v[20:23]
buffer_load_dwordx4 v1,s[36:39],s66 offen offset:2048 sc1 lds
v_mfma_f32_16x16x32_bf16 v[16:19],a[48:51],v[56:59],v[16:19]
s_mov_b32 s62,s87
ds_read_b128 v[52:55],v2 offset:13312
v_fma_f32 v68,v68,v91,v90
v_mfma_f32_16x16x32_bf16 v[20:23],a[52:55],v[56:59],v[20:23]
v_fma_f32 v69,v69,v91,v90
v_fma_f32 v70,v70,v91,v90
v_mfma_f32_16x16x32_bf16 v[16:19],a[56:59],v[60:63],v[16:19]
ds_read_b128 v[56:59],v2 offset:14336
v_fma_f32 v71,v71,v91,v90
v_mfma_f32_16x16x32_bf16 v[20:23],a[60:63],v[60:63],v[20:23]
s_waitcnt lgkmcnt(3)
buffer_load_dwordx4 v1,s[36:39],s66 offen offset:3072 sc1 lds
v_mfma_f32_16x16x32_bf16 v[16:19],a[64:67],v[32:35],v[16:19]
s_waitcnt vmcnt(10)
ds_read_b128 v[60:63],v2 offset:15360
v_add_f32 v24,v24,v64
v_mfma_f32_16x16x32_bf16 v[20:23],a[68:71],v[32:35],v[20:23]
s_barrier
v_mfma_f32_16x16x32_bf16 v[16:19],a[72:75],v[36:39],v[16:19]
s_add_u32 m0,s71,s88
ds_read_b128 v[32:35],v2 offset:16384
v_add_f32 v25,v25,v65
v_mfma_f32_16x16x32_bf16 v[20:23],a[76:79],v[36:39],v[20:23]
v_add_f32 v26,v26,v66
v_add_f32 v27,v27,v67
v_mfma_f32_16x16x32_bf16 v[16:19],a[80:83],v[40:43],v[16:19]
ds_read_b128 v[36:39],v2 offset:17408
v_add_f32 v28,v28,v68
v_mfma_f32_16x16x32_bf16 v[20:23],a[84:87],v[40:43],v[20:23]
buffer_load_dwordx4 v10,s[36:39],s66 offen offset:0 sc1 lds
v_mfma_f32_16x16x32_bf16 v[16:19],a[88:91],v[44:47],v[16:19]
buffer_load_dword v11,s[36:39],s72 offen offset:1024 sc1 lds
v_mfma_f32_16x16x32_bf16 v[20:23],a[92:95],v[44:47],v[20:23]
s_waitcnt lgkmcnt(2)
ds_read_b128 v[40:43],v2 offset:18432
v_add_f32 v29,v29,v69
v_mfma_f32_16x16x32_bf16 v[16:19],a[96:99],v[48:51],v[16:19]
s_mov_b32 m0,s62
ds_read_b128 v[44:47],v2 offset:19456
v_add_f32 v30,v30,v70
v_mfma_f32_16x16x32_bf16 v[20:23],a[100:103],v[48:51],v[20:23]
v_add_f32 v31,v31,v71
v_cvt_pk_bf16_f32 v72,v24,v25
v_cvt_pk_bf16_f32 v73,v26,v27
v_cvt_pk_bf16_f32 v74,v28,v29
v_cvt_pk_bf16_f32 v75,v30,v31
v_mfma_f32_16x16x32_bf16 v[16:19],a[104:107],v[52:55],v[16:19]
buffer_store_dwordx4 v[72:75],v3,s[36:39],s73 offen offset:0 sc0 sc1
ds_read_b128 v[48:51],v2 offset:20480
v_add_f32 v64,v24,v25
v_mul_f32 v65,v24,v24
v_mfma_f32_16x16x32_bf16 v[20:23],a[108:111],v[52:55],v[20:23]
v_fma_f32 v65,v25,v25,v65
v_add_f32 v64,v64,v26
v_mfma_f32_16x16x32_bf16 v[16:19],a[112:115],v[56:59],v[16:19]
ds_read_b128 v[52:55],v2 offset:21504
v_fma_f32 v65,v26,v26,v65
v_mfma_f32_16x16x32_bf16 v[20:23],a[116:119],v[56:59],v[20:23]
v_add_f32 v64,v64,v27
v_fma_f32 v65,v27,v27,v65
v_add_f32 v64,v64,v28
v_fma_f32 v65,v28,v28,v65
v_add_f32 v64,v64,v29
v_fma_f32 v65,v29,v29,v65
v_add_f32 v64,v64,v30
v_fma_f32 v65,v30,v30,v65
v_add_f32 v64,v64,v31
v_fma_f32 v65,v31,v31,v65
v_mfma_f32_16x16x32_bf16 v[16:19],a[120:123],v[60:63],v[16:19]
s_nop 0
v_permlane16_swap_b32 v64,v65
v_add_f32 v64,v64,v65
v_mov_b32 v65,v64
v_mfma_f32_16x16x32_bf16 v[20:23],a[124:127],v[60:63],v[20:23]
s_waitcnt lgkmcnt(2)
ds_read_b128 v[56:59],v2 offset:22528
v_permlane32_swap_b32 v64,v65
v_add_f32 v64,v64,v65
v_mfma_f32_16x16x32_bf16 v[16:19],a[128:131],v[32:35],v[16:19]
ds_write_b32 v13,v64 offset:0
v_mfma_f32_16x16x32_bf16 v[20:23],a[132:135],v[32:35],v[20:23]
ds_read_b128 v[60:63],v2 offset:23552
v_mfma_f32_16x16x32_bf16 v[16:19],a[136:139],v[36:39],v[16:19]
ds_read_b128 v[32:35],v2 offset:24576
v_mfma_f32_16x16x32_bf16 v[20:23],a[140:143],v[36:39],v[20:23]
v_mfma_f32_16x16x32_bf16 v[16:19],a[144:147],v[40:43],v[16:19]
s_waitcnt vmcnt(11) lgkmcnt(2)
ds_read_b128 v[36:39],v2 offset:25600
v_mfma_f32_16x16x32_bf16 v[20:23],a[148:151],v[40:43],v[20:23]
s_barrier
v_mfma_f32_16x16x32_bf16 v[16:19],a[152:155],v[44:47],v[16:19]
ds_read_b128 v[40:43],v2 offset:26624
ds_read_b32 v15,v7 offset:0
v_mfma_f32_16x16x32_bf16 v[20:23],a[156:159],v[44:47],v[20:23]
s_waitcnt lgkmcnt(4)
ds_read_b128 v[76:79],v14 offset:0
v_mfma_f32_16x16x32_bf16 v[16:19],a[160:163],v[48:51],v[16:19]
ds_read_b128 v[44:47],v2 offset:27648
v_mfma_f32_16x16x32_bf16 v[20:23],a[164:167],v[48:51],v[20:23]
v_mfma_f32_16x16x32_bf16 v[16:19],a[168:171],v[52:55],v[16:19]
s_waitcnt lgkmcnt(2)
ds_read_b128 v[48:51],v2 offset:28672
v_mfma_f32_16x16x32_bf16 v[20:23],a[172:175],v[52:55],v[20:23]
s_waitcnt lgkmcnt(2)
v_cmp_eq_u32 vcc,s60,v15
v_add_f32 v76,v76,v77
v_add_f32 v78,v78,v79
v_add_f32 v76,v76,v78
v_mfma_f32_16x16x32_bf16 v[16:19],a[176:179],v[56:59],v[16:19]
buffer_store_dword v76,v12,s[36:39],s85 offen offset:0 sc0 sc1
ds_read_b128 v[52:55],v2 offset:29696
v_mfma_f32_16x16x32_bf16 v[20:23],a[180:183],v[56:59],v[20:23]
s_cmp_eq_u32 vcc_lo,-1
s_cselect_b32 s67,s86,s59
s_xor_b32 s63,s63,65536
v_mfma_f32_16x16x32_bf16 v[16:19],a[184:187],v[60:63],v[16:19]
buffer_load_dwordx4 v0,s[36:39],s67 offen offset:0 sc1 lds
v_mfma_f32_16x16x32_bf16 v[20:23],a[188:191],v[60:63],v[20:23]
s_waitcnt lgkmcnt(2)
ds_read_b128 v[56:59],v2 offset:30720
s_add_u32 s53,s52,s55
v_mfma_f32_16x16x32_bf16 v[16:19],a[192:195],v[32:35],v[16:19]
s_waitcnt vmcnt(9)
ds_read_b128 v[60:63],v2 offset:31744
s_cmp_eq_u32 s53,s56
v_mfma_f32_16x16x32_bf16 v[20:23],a[196:199],v[32:35],v[20:23]
s_barrier
v_mfma_f32_16x16x32_bf16 v[16:19],a[200:203],v[36:39],v[16:19]
s_cselect_b32 s53,s57,s53
ds_read_b128 v[32:35],v2 offset:32768
ds_read_b128 v[80:83],v8 offset:0
v_mfma_f32_16x16x32_bf16 v[20:23],a[204:207],v[36:39],v[20:23]
s_cmp_eq_u32 s53,s58
ds_read_b64 v[88:89],v9 offset:1024
s_cselect_b32 s53,s59,s53
v_mfma_f32_16x16x32_bf16 v[16:19],a[208:211],v[40:43],v[16:19]
buffer_load_dwordx4 v0,s[36:39],s67 offen offset:1024 sc1 lds
v_mfma_f32_16x16x32_bf16 v[20:23],a[212:215],v[40:43],v[20:23]
s_cmp_ge_u32 s66,s59
ds_read_b128 v[36:39],v2 offset:33792
v_mfma_f32_16x16x32_bf16 v[16:19],a[216:219],v[44:47],v[16:19]
ds_read_b128 v[40:43],v2 offset:34816
v_mfma_f32_16x16x32_bf16 v[20:23],a[220:223],v[44:47],v[20:23]
s_waitcnt lgkmcnt(5)
v_mfma_f32_16x16x32_bf16 v[16:19],a[224:227],v[48:51],v[16:19]
buffer_load_dwordx4 v0,s[36:39],s67 offen offset:2048 sc1 lds
v_mfma_f32_16x16x32_bf16 v[20:23],a[228:231],v[48:51],v[20:23]
s_waitcnt lgkmcnt(2)
ds_read_b128 v[44:47],v2 offset:35840
v_mfma_f32_16x16x32_bf16 v[16:19],a[232:235],v[52:55],v[16:19]
ds_read_b128 v[48:51],v2 offset:36864
v_mfma_f32_16x16x32_bf16 v[20:23],a[236:239],v[52:55],v[20:23])"
R"(
v_mfma_f32_16x16x32_bf16 v[16:19],a[240:243],v[56:59],v[16:19]
buffer_load_dwordx4 v0,s[36:39],s67 offen offset:3072 sc1 lds
v_mfma_f32_16x16x32_bf16 v[20:23],a[244:247],v[56:59],v[20:23]
ds_read_b128 v[52:55],v2 offset:37888
v_mfma_f32_16x16x32_bf16 v[16:19],a[248:251],v[60:63],v[16:19]
s_waitcnt lgkmcnt(2)
ds_read_b128 v[56:59],v2 offset:38912
v_mfma_f32_16x16x32_bf16 v[20:23],a[252:255],v[60:63],v[20:23]
s_cbranch_scc1 Lg2_slow1_%=
Lg2_d1w2_%=:
v_mfma_f32_16x16x32_bf16 v[24:27],a[0:3],v[32:35],0
s_add_u32 m0,s62,16384
ds_read_b128 v[60:63],v2 offset:39936
v_lshlrev_b32 v64,16,v80
v_mfma_f32_16x16x32_bf16 v[28:31],a[4:7],v[32:35],0
s_lshr_b32 s73,s48,0
v_and_b32 v65,0xffff0000,v80
v_lshlrev_b32 v66,16,v81
v_mfma_f32_16x16x32_bf16 v[24:27],a[8:11],v[36:39],v[24:27]
s_lshr_b32 s85,s48,5
ds_read_b128 v[32:35],v2 offset:40960
v_and_b32 v67,0xffff0000,v81
v_mfma_f32_16x16x32_bf16 v[28:31],a[12:15],v[36:39],v[28:31]
buffer_load_dwordx4 v1,s[36:39],s67 offen offset:0 sc1 lds
v_mfma_f32_16x16x32_bf16 v[24:27],a[16:19],v[40:43],v[24:27]
s_xor_b32 s87,s63,65536
ds_read_b128 v[36:39],v2 offset:41984
v_lshlrev_b32 v68,16,v82
v_mfma_f32_16x16x32_bf16 v[28:31],a[20:23],v[40:43],v[28:31]
s_cmp_ge_u32 s67,s59
v_and_b32 v69,0xffff0000,v82
v_lshlrev_b32 v70,16,v83
v_mfma_f32_16x16x32_bf16 v[24:27],a[24:27],v[44:47],v[24:27]
s_cselect_b32 s86,s50,s51
ds_read_b128 v[40:43],v2 offset:43008
v_and_b32 v71,0xffff0000,v83
v_mfma_f32_16x16x32_bf16 v[28:31],a[28:31],v[44:47],v[28:31]
s_waitcnt lgkmcnt(3)
buffer_load_dwordx4 v1,s[36:39],s67 offen offset:1024 sc1 lds
v_mfma_f32_16x16x32_bf16 v[24:27],a[32:35],v[48:51],v[24:27]
s_cselect_b32 s87,s87,s64
ds_read_b128 v[44:47],v2 offset:44032
v_fma_f32 v64,v64,v89,v88
v_mfma_f32_16x16x32_bf16 v[28:31],a[36:39],v[48:51],v[28:31]
s_lshr_b32 s72,s67,6
v_fma_f32 v65,v65,v89,v88
v_fma_f32 v66,v66,v89,v88
v_mfma_f32_16x16x32_bf16 v[24:27],a[40:43],v[52:55],v[24:27]
s_lshr_b32 s71,s62,2
ds_read_b128 v[48:51],v2 offset:45056
v_fma_f32 v67,v67,v89,v88
v_mfma_f32_16x16x32_bf16 v[28:31],a[44:47],v[52:55],v[28:31]
buffer_load_dwordx4 v1,s[36:39],s67 offen offset:2048 sc1 lds
v_mfma_f32_16x16x32_bf16 v[24:27],a[48:51],v[56:59],v[24:27]
s_mov_b32 s62,s87
ds_read_b128 v[52:55],v2 offset:46080
v_fma_f32 v68,v68,v89,v88
v_mfma_f32_16x16x32_bf16 v[28:31],a[52:55],v[56:59],v[28:31]
s_mov_b64 s[44:45],s[46:47]
v_fma_f32 v69,v69,v89,v88
v_fma_f32 v70,v70,v89,v88
v_mfma_f32_16x16x32_bf16 v[24:27],a[56:59],v[60:63],v[24:27]
s_mov_b64 s[46:47],s[48:49]
ds_read_b128 v[56:59],v2 offset:47104
v_fma_f32 v71,v71,v89,v88
v_mfma_f32_16x16x32_bf16 v[28:31],a[60:63],v[60:63],v[28:31]
s_waitcnt lgkmcnt(3)
buffer_load_dwordx4 v1,s[36:39],s67 offen offset:3072 sc1 lds
v_mfma_f32_16x16x32_bf16 v[24:27],a[64:67],v[32:35],v[24:27]
s_waitcnt vmcnt(10)
ds_read_b128 v[60:63],v2 offset:48128
v_add_f32 v16,v16,v64
v_mfma_f32_16x16x32_bf16 v[28:31],a[68:71],v[32:35],v[28:31]
s_barrier
v_mfma_f32_16x16x32_bf16 v[24:27],a[72:75],v[36:39],v[24:27]
s_add_u32 m0,s71,s88
ds_read_b128 v[32:35],v2 offset:49152
v_add_f32 v17,v17,v65
v_mfma_f32_16x16x32_bf16 v[28:31],a[76:79],v[36:39],v[28:31]
s_mov_b64 s[48:49],s[50:51]
v_add_f32 v18,v18,v66
v_add_f32 v19,v19,v67
v_mfma_f32_16x16x32_bf16 v[24:27],a[80:83],v[40:43],v[24:27]
s_mov_b64 s[50:51],s[52:53]
ds_read_b128 v[36:39],v2 offset:50176
v_add_f32 v20,v20,v68
v_mfma_f32_16x16x32_bf16 v[28:31],a[84:87],v[40:43],v[28:31]
buffer_load_dwordx4 v10,s[36:39],s67 offen offset:0 sc1 lds
v_mfma_f32_16x16x32_bf16 v[24:27],a[88:91],v[44:47],v[24:27]
buffer_load_dword v11,s[36:39],s72 offen offset:1024 sc1 lds
v_mfma_f32_16x16x32_bf16 v[28:31],a[92:95],v[44:47],v[28:31]
s_waitcnt lgkmcnt(2)
ds_read_b128 v[40:43],v2 offset:51200
v_add_f32 v21,v21,v69
v_mfma_f32_16x16x32_bf16 v[24:27],a[96:99],v[48:51],v[24:27]
s_mov_b32 m0,s62
ds_read_b128 v[44:47],v2 offset:52224
v_add_f32 v22,v22,v70
v_mfma_f32_16x16x32_bf16 v[28:31],a[100:103],v[48:51],v[28:31]
v_add_f32 v23,v23,v71
v_cvt_pk_bf16_f32 v72,v16,v17
v_cvt_pk_bf16_f32 v73,v18,v19
v_cvt_pk_bf16_f32 v74,v20,v21
v_cvt_pk_bf16_f32 v75,v22,v23
v_mfma_f32_16x16x32_bf16 v[24:27],a[104:107],v[52:55],v[24:27]
buffer_store_dwordx4 v[72:75],v3,s[36:39],s73 offen offset:0 sc0 sc1
ds_read_b128 v[48:51],v2 offset:53248
v_add_f32 v64,v16,v17
v_mul_f32 v65,v16,v16
v_mfma_f32_16x16x32_bf16 v[28:31],a[108:111],v[52:55],v[28:31]
v_fma_f32 v65,v17,v17,v65
v_add_f32 v64,v64,v18
v_mfma_f32_16x16x32_bf16 v[24:27],a[112:115],v[56:59],v[24:27]
ds_read_b128 v[52:55],v2 offset:54272
v_fma_f32 v65,v18,v18,v65
v_mfma_f32_16x16x32_bf16 v[28:31],a[116:119],v[56:59],v[28:31]
v_add_f32 v64,v64,v19
v_fma_f32 v65,v19,v19,v65
v_add_f32 v64,v64,v20
v_fma_f32 v65,v20,v20,v65
v_add_f32 v64,v64,v21
v_fma_f32 v65,v21,v21,v65
v_add_f32 v64,v64,v22
v_fma_f32 v65,v22,v22,v65
v_add_f32 v64,v64,v23
v_fma_f32 v65,v23,v23,v65
v_mfma_f32_16x16x32_bf16 v[24:27],a[120:123],v[60:63],v[24:27]
s_nop 0
v_permlane16_swap_b32 v64,v65
v_add_f32 v64,v64,v65
v_mov_b32 v65,v64
v_mfma_f32_16x16x32_bf16 v[28:31],a[124:127],v[60:63],v[28:31]
s_waitcnt lgkmcnt(2)
ds_read_b128 v[56:59],v2 offset:55296
v_permlane32_swap_b32 v64,v65
v_add_f32 v64,v64,v65
v_mfma_f32_16x16x32_bf16 v[24:27],a[128:131],v[32:35],v[24:27]
ds_write_b32 v13,v64 offset:2048
v_mfma_f32_16x16x32_bf16 v[28:31],a[132:135],v[32:35],v[28:31]
ds_read_b128 v[60:63],v2 offset:56320
v_mfma_f32_16x16x32_bf16 v[24:27],a[136:139],v[36:39],v[24:27]
ds_read_b128 v[32:35],v2 offset:57344
v_mfma_f32_16x16x32_bf16 v[28:31],a[140:143],v[36:39],v[28:31]
v_mfma_f32_16x16x32_bf16 v[24:27],a[144:147],v[40:43],v[24:27]
s_waitcnt vmcnt(11) lgkmcnt(2)
ds_read_b128 v[36:39],v2 offset:58368
v_mfma_f32_16x16x32_bf16 v[28:31],a[148:151],v[40:43],v[28:31]
s_barrier
v_mfma_f32_16x16x32_bf16 v[24:27],a[152:155],v[44:47],v[24:27]
ds_read_b128 v[40:43],v2 offset:59392
ds_read_b32 v15,v7 offset:256
v_mfma_f32_16x16x32_bf16 v[28:31],a[156:159],v[44:47],v[28:31]
s_waitcnt lgkmcnt(4)
ds_read_b128 v[76:79],v14 offset:2048
v_mfma_f32_16x16x32_bf16 v[24:27],a[160:163],v[48:51],v[24:27]
ds_read_b128 v[44:47],v2 offset:60416
v_mfma_f32_16x16x32_bf16 v[28:31],a[164:167],v[48:51],v[28:31]
v_mfma_f32_16x16x32_bf16 v[24:27],a[168:171],v[52:55],v[24:27]
s_waitcnt lgkmcnt(2)
ds_read_b128 v[48:51],v2 offset:61440
v_mfma_f32_16x16x32_bf16 v[28:31],a[172:175],v[52:55],v[28:31]
s_waitcnt lgkmcnt(2)
v_cmp_eq_u32 vcc,s60,v15
v_add_f32 v76,v76,v77
v_add_f32 v78,v78,v79
v_add_f32 v76,v76,v78
v_mfma_f32_16x16x32_bf16 v[24:27],a[176:179],v[56:59],v[24:27]
buffer_store_dword v76,v12,s[36:39],s85 offen offset:0 sc0 sc1
ds_read_b128 v[52:55],v2 offset:62464
v_mfma_f32_16x16x32_bf16 v[28:31],a[180:183],v[56:59],v[28:31]
s_cmp_eq_u32 vcc_lo,-1
s_cselect_b32 s66,s86,s59
s_xor_b32 s64,s64,65536
v_mfma_f32_16x16x32_bf16 v[24:27],a[184:187],v[60:63],v[24:27]
buffer_load_dwordx4 v0,s[36:39],s66 offen offset:0 sc1 lds
v_mfma_f32_16x16x32_bf16 v[28:31],a[188:191],v[60:63],v[28:31]
s_waitcnt lgkmcnt(2)
ds_read_b128 v[56:59],v2 offset:63488
s_add_u32 s74,s53,s55
v_mfma_f32_16x16x32_bf16 v[24:27],a[192:195],v[32:35],v[24:27]
s_waitcnt vmcnt(9)
ds_read_b128 v[60:63],v2 offset:64512
v_xor_b32 v2,65536,v2
v_mfma_f32_16x16x32_bf16 v[28:31],a[196:199],v[32:35],v[28:31]
s_barrier
v_mfma_f32_16x16x32_bf16 v[24:27],a[200:203],v[36:39],v[24:27]
s_cmp_eq_u32 s74,s56
ds_read_b128 v[32:35],v2 offset:0
ds_read_b128 v[84:87],v8 offset:8192
v_mfma_f32_16x16x32_bf16 v[28:31],a[204:207],v[36:39],v[28:31]
s_cselect_b32 s74,s57,s74
ds_read_b64 v[90:91],v9 offset:9216
v_xor_b32 v8,16384,v8
v_mfma_f32_16x16x32_bf16 v[24:27],a[208:211],v[40:43],v[24:27]
buffer_load_dwordx4 v0,s[36:39],s66 offen offset:1024 sc1 lds
v_mfma_f32_16x16x32_bf16 v[28:31],a[212:215],v[40:43],v[28:31]
s_cmp_eq_u32 s74,s58
ds_read_b128 v[36:39],v2 offset:1024
v_xor_b32 v9,16384,v9
v_mfma_f32_16x16x32_bf16 v[24:27],a[216:219],v[44:47],v[24:27])"
R"(
s_cselect_b32 s74,s59,s74
ds_read_b128 v[40:43],v2 offset:2048
s_mov_b32 s52,s74
v_mfma_f32_16x16x32_bf16 v[28:31],a[220:223],v[44:47],v[28:31]
s_waitcnt lgkmcnt(5)
s_cmp_lt_u32 s67,s59
v_mfma_f32_16x16x32_bf16 v[24:27],a[224:227],v[48:51],v[24:27]
buffer_load_dwordx4 v0,s[36:39],s66 offen offset:2048 sc1 lds
v_mfma_f32_16x16x32_bf16 v[28:31],a[228:231],v[48:51],v[28:31]
s_waitcnt lgkmcnt(2)
ds_read_b128 v[44:47],v2 offset:3072
v_mfma_f32_16x16x32_bf16 v[24:27],a[232:235],v[52:55],v[24:27]
ds_read_b128 v[48:51],v2 offset:4096
v_mfma_f32_16x16x32_bf16 v[28:31],a[236:239],v[52:55],v[28:31]
v_mfma_f32_16x16x32_bf16 v[24:27],a[240:243],v[56:59],v[24:27]
buffer_load_dwordx4 v0,s[36:39],s66 offen offset:3072 sc1 lds
v_mfma_f32_16x16x32_bf16 v[28:31],a[244:247],v[56:59],v[28:31]
ds_read_b128 v[52:55],v2 offset:5120
v_mfma_f32_16x16x32_bf16 v[24:27],a[248:251],v[60:63],v[24:27]
s_waitcnt lgkmcnt(2)
ds_read_b128 v[56:59],v2 offset:6144
v_mfma_f32_16x16x32_bf16 v[28:31],a[252:255],v[60:63],v[28:31]
s_cbranch_scc1 Lg2_d0w2_%=
s_branch Lg2_slow0_%=
Lg2_d0w3_%=:
v_mfma_f32_16x16x32_bf16 v[16:19],a[0:3],v[32:35],0
s_add_u32 m0,s62,16384
ds_read_b128 v[60:63],v2 offset:7168
v_lshlrev_b32 v64,16,v84
v_mfma_f32_16x16x32_bf16 v[20:23],a[4:7],v[32:35],0
s_lshr_b32 s73,s47,0
v_and_b32 v65,0xffff0000,v84
v_lshlrev_b32 v66,16,v85
v_mfma_f32_16x16x32_bf16 v[16:19],a[8:11],v[36:39],v[16:19]
s_xor_b32 s87,s64,65536
ds_read_b128 v[32:35],v2 offset:8192
v_and_b32 v67,0xffff0000,v85
v_mfma_f32_16x16x32_bf16 v[20:23],a[12:15],v[36:39],v[20:23]
s_cmp_ge_u32 s66,s59
v_lshlrev_b32 v68,16,v86
v_and_b32 v69,0xffff0000,v86
v_mfma_f32_16x16x32_bf16 v[16:19],a[16:19],v[40:43],v[16:19]
buffer_load_dwordx4 v1,s[36:39],s66 offen offset:0 sc1 lds
v_mfma_f32_16x16x32_bf16 v[20:23],a[20:23],v[40:43],v[20:23]
s_cselect_b32 s86,s49,s50
ds_read_b128 v[36:39],v2 offset:9216
v_lshlrev_b32 v70,16,v87
v_mfma_f32_16x16x32_bf16 v[16:19],a[24:27],v[44:47],v[16:19]
s_cselect_b32 s87,s87,s63
ds_read_b128 v[40:43],v2 offset:10240
v_and_b32 v71,0xffff0000,v87
v_mfma_f32_16x16x32_bf16 v[20:23],a[28:31],v[44:47],v[20:23]
s_waitcnt lgkmcnt(3)
v_fma_f32 v64,v64,v91,v90
v_fma_f32 v65,v65,v91,v90
v_mfma_f32_16x16x32_bf16 v[16:19],a[32:35],v[48:51],v[16:19]
buffer_load_dwordx4 v1,s[36:39],s66 offen offset:1024 sc1 lds
v_mfma_f32_16x16x32_bf16 v[20:23],a[36:39],v[48:51],v[20:23]
s_lshr_b32 s72,s66,6
ds_read_b128 v[44:47],v2 offset:11264
v_fma_f32 v66,v66,v91,v90
v_mfma_f32_16x16x32_bf16 v[16:19],a[40:43],v[52:55],v[16:19]
s_lshr_b32 s71,s62,2
ds_read_b128 v[48:51],v2 offset:12288
v_fma_f32 v67,v67,v91,v90
v_mfma_f32_16x16x32_bf16 v[20:23],a[44:47],v[52:55],v[20:23]
s_mov_b32 s62,s87
v_fma_f32 v68,v68,v91,v90
v_fma_f32 v69,v69,v91,v90
v_mfma_f32_16x16x32_bf16 v[16:19],a[48:51],v[56:59],v[16:19]
buffer_load_dwordx4 v1,s[36:39],s66 offen offset:2048 sc1 lds
v_mfma_f32_16x16x32_bf16 v[20:23],a[52:55],v[56:59],v[20:23]
ds_read_b128 v[52:55],v2 offset:13312
v_fma_f32 v70,v70,v91,v90
v_mfma_f32_16x16x32_bf16 v[16:19],a[56:59],v[60:63],v[16:19]
ds_read_b128 v[56:59],v2 offset:14336
v_fma_f32 v71,v71,v91,v90
v_mfma_f32_16x16x32_bf16 v[20:23],a[60:63],v[60:63],v[20:23]
s_waitcnt lgkmcnt(3)
v_add_f32 v24,v24,v64
v_add_f32 v25,v25,v65
v_mfma_f32_16x16x32_bf16 v[16:19],a[64:67],v[32:35],v[16:19]
s_waitcnt vmcnt(9)
ds_read_b128 v[60:63],v2 offset:15360
v_add_f32 v26,v26,v66
v_mfma_f32_16x16x32_bf16 v[20:23],a[68:71],v[32:35],v[20:23]
s_barrier
v_mfma_f32_16x16x32_bf16 v[16:19],a[72:75],v[36:39],v[16:19]
buffer_load_dwordx4 v1,s[36:39],s66 offen offset:3072 sc1 lds
v_mfma_f32_16x16x32_bf16 v[20:23],a[76:79],v[36:39],v[20:23]
s_add_u32 m0,s71,s88
ds_read_b128 v[32:35],v2 offset:16384
v_add_f32 v27,v27,v67
v_mfma_f32_16x16x32_bf16 v[16:19],a[80:83],v[40:43],v[16:19]
s_lshr_b32 s70,s46,5
ds_read_b128 v[36:39],v2 offset:17408
v_add_f32 v28,v28,v68
v_mfma_f32_16x16x32_bf16 v[20:23],a[84:87],v[40:43],v[20:23]
v_add_f32 v29,v29,v69
v_add_f32 v30,v30,v70
v_mfma_f32_16x16x32_bf16 v[16:19],a[88:91],v[44:47],v[16:19]
buffer_load_dwordx4 v10,s[36:39],s66 offen offset:0 sc1 lds
v_mfma_f32_16x16x32_bf16 v[20:23],a[92:95],v[44:47],v[20:23]
s_waitcnt lgkmcnt(2)
ds_read_b128 v[40:43],v2 offset:18432
v_add_f32 v31,v31,v71
v_mfma_f32_16x16x32_bf16 v[16:19],a[96:99],v[48:51],v[16:19]
buffer_load_dword v11,s[36:39],s72 offen offset:1024 sc1 lds
v_mfma_f32_16x16x32_bf16 v[20:23],a[100:103],v[48:51],v[20:23]
s_mov_b32 m0,s62
ds_read_b128 v[44:47],v2 offset:19456
v_cvt_pk_bf16_f32 v72,v24,v25
v_mfma_f32_16x16x32_bf16 v[16:19],a[104:107],v[52:55],v[16:19]
v_cvt_pk_bf16_f32 v73,v26,v27
v_cvt_pk_bf16_f32 v74,v28,v29
v_cvt_pk_bf16_f32 v75,v30,v31
v_mfma_f32_16x16x32_bf16 v[20:23],a[108:111],v[52:55],v[20:23]
ds_read_b128 v[48:51],v2 offset:20480
buffer_store_dwordx4 v[72:75],v3,s[36:39],s73 offen offset:0 sc0 sc1
v_add_f32 v64,v24,v25
v_mul_f32 v65,v24,v24
v_fma_f32 v65,v25,v25,v65
v_mfma_f32_16x16x32_bf16 v[16:19],a[112:115],v[56:59],v[16:19]
ds_read_b128 v[52:55],v2 offset:21504
v_add_f32 v64,v64,v26
v_mfma_f32_16x16x32_bf16 v[20:23],a[116:119],v[56:59],v[20:23]
v_fma_f32 v65,v26,v26,v65
v_add_f32 v64,v64,v27
v_fma_f32 v65,v27,v27,v65
v_add_f32 v64,v64,v28
v_fma_f32 v65,v28,v28,v65
v_add_f32 v64,v64,v29
v_fma_f32 v65,v29,v29,v65
v_add_f32 v64,v64,v30
v_fma_f32 v65,v30,v30,v65
v_add_f32 v64,v64,v31
v_fma_f32 v65,v31,v31,v65
v_mfma_f32_16x16x32_bf16 v[16:19],a[120:123],v[60:63],v[16:19]
s_nop 0
v_permlane16_swap_b32 v64,v65
v_add_f32 v64,v64,v65
v_mov_b32 v65,v64
v_mfma_f32_16x16x32_bf16 v[20:23],a[124:127],v[60:63],v[20:23]
s_waitcnt lgkmcnt(2)
ds_read_b128 v[56:59],v2 offset:22528
v_permlane32_swap_b32 v64,v65
v_add_f32 v64,v64,v65
v_mfma_f32_16x16x32_bf16 v[16:19],a[128:131],v[32:35],v[16:19]
ds_write_b32 v13,v64 offset:0
v_mfma_f32_16x16x32_bf16 v[20:23],a[132:135],v[32:35],v[20:23]
ds_read_b128 v[60:63],v2 offset:23552
v_mfma_f32_16x16x32_bf16 v[16:19],a[136:139],v[36:39],v[16:19]
ds_read_b128 v[32:35],v2 offset:24576
v_mfma_f32_16x16x32_bf16 v[20:23],a[140:143],v[36:39],v[20:23]
v_mfma_f32_16x16x32_bf16 v[16:19],a[144:147],v[40:43],v[16:19]
s_waitcnt vmcnt(12) lgkmcnt(2)
ds_read_b128 v[36:39],v2 offset:25600
v_mfma_f32_16x16x32_bf16 v[20:23],a[148:151],v[40:43],v[20:23]
s_barrier
v_mfma_f32_16x16x32_bf16 v[16:19],a[152:155],v[44:47],v[16:19]
buffer_store_dword v5,v4,s[36:39],s70 offen offset:0 sc0 sc1
ds_read_b128 v[40:43],v2 offset:26624
ds_read_b32 v15,v7 offset:0
v_mfma_f32_16x16x32_bf16 v[20:23],a[156:159],v[44:47],v[20:23]
s_waitcnt lgkmcnt(4)
v_mfma_f32_16x16x32_bf16 v[16:19],a[160:163],v[48:51],v[16:19]
ds_read_b128 v[44:47],v2 offset:27648
v_mfma_f32_16x16x32_bf16 v[20:23],a[164:167],v[48:51],v[20:23]
v_mfma_f32_16x16x32_bf16 v[16:19],a[168:171],v[52:55],v[16:19]
s_waitcnt lgkmcnt(1)
ds_read_b128 v[48:51],v2 offset:28672
v_mfma_f32_16x16x32_bf16 v[20:23],a[172:175],v[52:55],v[20:23]
v_cmp_eq_u32 vcc,s60,v15
v_mfma_f32_16x16x32_bf16 v[16:19],a[176:179],v[56:59],v[16:19]
s_cmp_eq_u32 vcc_lo,-1
ds_read_b128 v[52:55],v2 offset:29696
s_cselect_b32 s67,s86,s59
v_mfma_f32_16x16x32_bf16 v[20:23],a[180:183],v[56:59],v[20:23]
s_xor_b32 s63,s63,65536
s_add_u32 s53,s52,s55
s_cmp_eq_u32 s53,s56
v_mfma_f32_16x16x32_bf16 v[16:19],a[184:187],v[60:63],v[16:19]
s_cselect_b32 s53,s57,s53
ds_read_b128 v[56:59],v2 offset:30720
s_cmp_eq_u32 s53,s58
v_mfma_f32_16x16x32_bf16 v[20:23],a[188:191],v[60:63],v[20:23]
s_waitcnt lgkmcnt(3)
buffer_load_dwordx4 v0,s[36:39],s67 offen offset:0 sc1 lds
v_mfma_f32_16x16x32_bf16 v[16:19],a[192:195],v[32:35],v[16:19]
s_waitcnt vmcnt(9)
ds_read_b128 v[60:63],v2 offset:31744
s_cselect_b32 s53,s59,s53
v_mfma_f32_16x16x32_bf16 v[20:23],a[196:199],v[32:35],v[20:23]
s_barrier
v_mfma_f32_16x16x32_bf16 v[16:19],a[200:203],v[36:39],v[16:19]
s_cmp_ge_u32 s66,s59
ds_read_b128 v[32:35],v2 offset:32768
ds_read_b128 v[80:83],v8 offset:0
v_mfma_f32_16x16x32_bf16 v[20:23],a[204:207],v[36:39],v[20:23]
ds_read_b64 v[88:89],v9 offset:1024)"
R"(
v_mfma_f32_16x16x32_bf16 v[16:19],a[208:211],v[40:43],v[16:19]
ds_read_b128 v[36:39],v2 offset:33792
v_mfma_f32_16x16x32_bf16 v[20:23],a[212:215],v[40:43],v[20:23]
buffer_load_dwordx4 v0,s[36:39],s67 offen offset:1024 sc1 lds
v_mfma_f32_16x16x32_bf16 v[16:19],a[216:219],v[44:47],v[16:19]
ds_read_b128 v[40:43],v2 offset:34816
v_mfma_f32_16x16x32_bf16 v[20:23],a[220:223],v[44:47],v[20:23]
s_waitcnt lgkmcnt(5)
v_mfma_f32_16x16x32_bf16 v[16:19],a[224:227],v[48:51],v[16:19]
ds_read_b128 v[44:47],v2 offset:35840
v_mfma_f32_16x16x32_bf16 v[20:23],a[228:231],v[48:51],v[20:23]
s_waitcnt lgkmcnt(3)
buffer_load_dwordx4 v0,s[36:39],s67 offen offset:2048 sc1 lds
v_mfma_f32_16x16x32_bf16 v[16:19],a[232:235],v[52:55],v[16:19]
ds_read_b128 v[48:51],v2 offset:36864
v_mfma_f32_16x16x32_bf16 v[20:23],a[236:239],v[52:55],v[20:23]
v_mfma_f32_16x16x32_bf16 v[16:19],a[240:243],v[56:59],v[16:19]
ds_read_b128 v[52:55],v2 offset:37888
v_mfma_f32_16x16x32_bf16 v[20:23],a[244:247],v[56:59],v[20:23]
buffer_load_dwordx4 v0,s[36:39],s67 offen offset:3072 sc1 lds
v_mfma_f32_16x16x32_bf16 v[16:19],a[248:251],v[60:63],v[16:19]
s_waitcnt lgkmcnt(2)
ds_read_b128 v[56:59],v2 offset:38912
v_mfma_f32_16x16x32_bf16 v[20:23],a[252:255],v[60:63],v[20:23]
s_cbranch_scc1 Lg2_slow1_%=
Lg2_d1w3_%=:
v_mfma_f32_16x16x32_bf16 v[24:27],a[0:3],v[32:35],0
s_add_u32 m0,s62,16384
ds_read_b128 v[60:63],v2 offset:39936
v_lshlrev_b32 v64,16,v80
v_mfma_f32_16x16x32_bf16 v[28:31],a[4:7],v[32:35],0
s_lshr_b32 s73,s48,0
v_and_b32 v65,0xffff0000,v80
v_lshlrev_b32 v66,16,v81
v_mfma_f32_16x16x32_bf16 v[24:27],a[8:11],v[36:39],v[24:27]
s_xor_b32 s87,s63,65536
ds_read_b128 v[32:35],v2 offset:40960
v_and_b32 v67,0xffff0000,v81
v_mfma_f32_16x16x32_bf16 v[28:31],a[12:15],v[36:39],v[28:31]
s_cmp_ge_u32 s67,s59
v_lshlrev_b32 v68,16,v82
v_and_b32 v69,0xffff0000,v82
v_mfma_f32_16x16x32_bf16 v[24:27],a[16:19],v[40:43],v[24:27]
buffer_load_dwordx4 v1,s[36:39],s67 offen offset:0 sc1 lds
v_mfma_f32_16x16x32_bf16 v[28:31],a[20:23],v[40:43],v[28:31]
s_cselect_b32 s86,s50,s51
ds_read_b128 v[36:39],v2 offset:41984
v_lshlrev_b32 v70,16,v83
v_mfma_f32_16x16x32_bf16 v[24:27],a[24:27],v[44:47],v[24:27]
s_cselect_b32 s87,s87,s64
ds_read_b128 v[40:43],v2 offset:43008
v_and_b32 v71,0xffff0000,v83
v_mfma_f32_16x16x32_bf16 v[28:31],a[28:31],v[44:47],v[28:31]
s_waitcnt lgkmcnt(3)
v_fma_f32 v64,v64,v89,v88
v_fma_f32 v65,v65,v89,v88
v_mfma_f32_16x16x32_bf16 v[24:27],a[32:35],v[48:51],v[24:27]
buffer_load_dwordx4 v1,s[36:39],s67 offen offset:1024 sc1 lds
v_mfma_f32_16x16x32_bf16 v[28:31],a[36:39],v[48:51],v[28:31]
s_lshr_b32 s72,s67,6
ds_read_b128 v[44:47],v2 offset:44032
v_fma_f32 v66,v66,v89,v88
v_mfma_f32_16x16x32_bf16 v[24:27],a[40:43],v[52:55],v[24:27]
s_lshr_b32 s71,s62,2
ds_read_b128 v[48:51],v2 offset:45056
v_fma_f32 v67,v67,v89,v88
v_mfma_f32_16x16x32_bf16 v[28:31],a[44:47],v[52:55],v[28:31]
s_mov_b32 s62,s87
v_fma_f32 v68,v68,v89,v88
v_fma_f32 v69,v69,v89,v88
v_mfma_f32_16x16x32_bf16 v[24:27],a[48:51],v[56:59],v[24:27]
buffer_load_dwordx4 v1,s[36:39],s67 offen offset:2048 sc1 lds
v_mfma_f32_16x16x32_bf16 v[28:31],a[52:55],v[56:59],v[28:31]
s_mov_b64 s[44:45],s[46:47]
ds_read_b128 v[52:55],v2 offset:46080
v_fma_f32 v70,v70,v89,v88
v_mfma_f32_16x16x32_bf16 v[24:27],a[56:59],v[60:63],v[24:27]
ds_read_b128 v[56:59],v2 offset:47104
v_fma_f32 v71,v71,v89,v88
v_mfma_f32_16x16x32_bf16 v[28:31],a[60:63],v[60:63],v[28:31]
s_waitcnt lgkmcnt(3)
v_add_f32 v16,v16,v64
v_add_f32 v17,v17,v65
v_mfma_f32_16x16x32_bf16 v[24:27],a[64:67],v[32:35],v[24:27]
s_waitcnt vmcnt(9)
ds_read_b128 v[60:63],v2 offset:48128
v_add_f32 v18,v18,v66
v_mfma_f32_16x16x32_bf16 v[28:31],a[68:71],v[32:35],v[28:31]
s_barrier
v_mfma_f32_16x16x32_bf16 v[24:27],a[72:75],v[36:39],v[24:27]
buffer_load_dwordx4 v1,s[36:39],s67 offen offset:3072 sc1 lds
v_mfma_f32_16x16x32_bf16 v[28:31],a[76:79],v[36:39],v[28:31]
s_add_u32 m0,s71,s88
ds_read_b128 v[32:35],v2 offset:49152
v_add_f32 v19,v19,v67
v_mfma_f32_16x16x32_bf16 v[24:27],a[80:83],v[40:43],v[24:27]
s_lshr_b32 s70,s47,5
ds_read_b128 v[36:39],v2 offset:50176
v_add_f32 v20,v20,v68
v_mfma_f32_16x16x32_bf16 v[28:31],a[84:87],v[40:43],v[28:31]
s_mov_b64 s[46:47],s[48:49]
v_add_f32 v21,v21,v69
v_add_f32 v22,v22,v70
v_mfma_f32_16x16x32_bf16 v[24:27],a[88:91],v[44:47],v[24:27]
buffer_load_dwordx4 v10,s[36:39],s67 offen offset:0 sc1 lds
v_mfma_f32_16x16x32_bf16 v[28:31],a[92:95],v[44:47],v[28:31]
s_waitcnt lgkmcnt(2)
ds_read_b128 v[40:43],v2 offset:51200
v_add_f32 v23,v23,v71
v_mfma_f32_16x16x32_bf16 v[24:27],a[96:99],v[48:51],v[24:27]
buffer_load_dword v11,s[36:39],s72 offen offset:1024 sc1 lds
v_mfma_f32_16x16x32_bf16 v[28:31],a[100:103],v[48:51],v[28:31]
s_mov_b32 m0,s62
ds_read_b128 v[44:47],v2 offset:52224
v_cvt_pk_bf16_f32 v72,v16,v17
v_mfma_f32_16x16x32_bf16 v[24:27],a[104:107],v[52:55],v[24:27]
s_mov_b64 s[48:49],s[50:51]
v_cvt_pk_bf16_f32 v73,v18,v19
v_cvt_pk_bf16_f32 v74,v20,v21
v_cvt_pk_bf16_f32 v75,v22,v23
v_mfma_f32_16x16x32_bf16 v[28:31],a[108:111],v[52:55],v[28:31]
s_mov_b64 s[50:51],s[52:53]
ds_read_b128 v[48:51],v2 offset:53248
buffer_store_dwordx4 v[72:75],v3,s[36:39],s73 offen offset:0 sc0 sc1
v_add_f32 v64,v16,v17
v_mul_f32 v65,v16,v16
v_fma_f32 v65,v17,v17,v65
v_mfma_f32_16x16x32_bf16 v[24:27],a[112:115],v[56:59],v[24:27]
ds_read_b128 v[52:55],v2 offset:54272
v_add_f32 v64,v64,v18
v_mfma_f32_16x16x32_bf16 v[28:31],a[116:119],v[56:59],v[28:31]
v_fma_f32 v65,v18,v18,v65
v_add_f32 v64,v64,v19
v_fma_f32 v65,v19,v19,v65
v_add_f32 v64,v64,v20
v_fma_f32 v65,v20,v20,v65
v_add_f32 v64,v64,v21
v_fma_f32 v65,v21,v21,v65
v_add_f32 v64,v64,v22
v_fma_f32 v65,v22,v22,v65
v_add_f32 v64,v64,v23
v_fma_f32 v65,v23,v23,v65
v_mfma_f32_16x16x32_bf16 v[24:27],a[120:123],v[60:63],v[24:27]
s_nop 0
v_permlane16_swap_b32 v64,v65
v_add_f32 v64,v64,v65
v_mov_b32 v65,v64
v_mfma_f32_16x16x32_bf16 v[28:31],a[124:127],v[60:63],v[28:31]
s_waitcnt lgkmcnt(2)
ds_read_b128 v[56:59],v2 offset:55296
v_permlane32_swap_b32 v64,v65
v_add_f32 v64,v64,v65
v_mfma_f32_16x16x32_bf16 v[24:27],a[128:131],v[32:35],v[24:27]
ds_write_b32 v13,v64 offset:2048
v_mfma_f32_16x16x32_bf16 v[28:31],a[132:135],v[32:35],v[28:31]
ds_read_b128 v[60:63],v2 offset:56320
v_mfma_f32_16x16x32_bf16 v[24:27],a[136:139],v[36:39],v[24:27]
ds_read_b128 v[32:35],v2 offset:57344
v_mfma_f32_16x16x32_bf16 v[28:31],a[140:143],v[36:39],v[28:31]
v_mfma_f32_16x16x32_bf16 v[24:27],a[144:147],v[40:43],v[24:27]
s_waitcnt vmcnt(12) lgkmcnt(2)
ds_read_b128 v[36:39],v2 offset:58368
v_mfma_f32_16x16x32_bf16 v[28:31],a[148:151],v[40:43],v[28:31]
s_barrier
v_mfma_f32_16x16x32_bf16 v[24:27],a[152:155],v[44:47],v[24:27]
buffer_store_dword v5,v4,s[36:39],s70 offen offset:0 sc0 sc1
ds_read_b128 v[40:43],v2 offset:59392
ds_read_b32 v15,v7 offset:256
v_mfma_f32_16x16x32_bf16 v[28:31],a[156:159],v[44:47],v[28:31]
s_waitcnt lgkmcnt(4)
v_mfma_f32_16x16x32_bf16 v[24:27],a[160:163],v[48:51],v[24:27]
ds_read_b128 v[44:47],v2 offset:60416
v_mfma_f32_16x16x32_bf16 v[28:31],a[164:167],v[48:51],v[28:31]
v_mfma_f32_16x16x32_bf16 v[24:27],a[168:171],v[52:55],v[24:27]
s_waitcnt lgkmcnt(1)
ds_read_b128 v[48:51],v2 offset:61440
v_mfma_f32_16x16x32_bf16 v[28:31],a[172:175],v[52:55],v[28:31]
v_cmp_eq_u32 vcc,s60,v15
v_mfma_f32_16x16x32_bf16 v[24:27],a[176:179],v[56:59],v[24:27]
s_cmp_eq_u32 vcc_lo,-1
ds_read_b128 v[52:55],v2 offset:62464
s_cselect_b32 s66,s86,s59
v_mfma_f32_16x16x32_bf16 v[28:31],a[180:183],v[56:59],v[28:31]
s_xor_b32 s64,s64,65536
s_add_u32 s74,s53,s55
s_cmp_eq_u32 s74,s56
v_mfma_f32_16x16x32_bf16 v[24:27],a[184:187],v[60:63],v[24:27]
s_cselect_b32 s74,s57,s74
ds_read_b128 v[56:59],v2 offset:63488
s_cmp_eq_u32 s74,s58
v_mfma_f32_16x16x32_bf16 v[28:31],a[188:191],v[60:63],v[28:31]
s_waitcnt lgkmcnt(3)
buffer_load_dwordx4 v0,s[36:39],s66 offen offset:0 sc1 lds
v_mfma_f32_16x16x32_bf16 v[24:27],a[192:195],v[32:35],v[24:27]
s_waitcnt vmcnt(9)
ds_read_b128 v[60:63],v2 offset:64512
v_xor_b32 v2,65536,v2
v_mfma_f32_16x16x32_bf16 v[28:31],a[196:199],v[32:35],v[28:31]
s_barrier
v_mfma_f32_16x16x32_bf16 v[24:27],a[200:203],v[36:39],v[24:27])"
R"(
s_cselect_b32 s74,s59,s74
ds_read_b128 v[32:35],v2 offset:0
ds_read_b128 v[84:87],v8 offset:8192
v_mfma_f32_16x16x32_bf16 v[28:31],a[204:207],v[36:39],v[28:31]
s_mov_b32 s52,s74
ds_read_b64 v[90:91],v9 offset:9216
v_xor_b32 v8,16384,v8
v_mfma_f32_16x16x32_bf16 v[24:27],a[208:211],v[40:43],v[24:27]
s_cmp_lt_u32 s67,s59
ds_read_b128 v[36:39],v2 offset:1024
v_xor_b32 v9,16384,v9
v_mfma_f32_16x16x32_bf16 v[28:31],a[212:215],v[40:43],v[28:31]
buffer_load_dwordx4 v0,s[36:39],s66 offen offset:1024 sc1 lds
v_mfma_f32_16x16x32_bf16 v[24:27],a[216:219],v[44:47],v[24:27]
ds_read_b128 v[40:43],v2 offset:2048
v_mfma_f32_16x16x32_bf16 v[28:31],a[220:223],v[44:47],v[28:31]
s_waitcnt lgkmcnt(5)
v_mfma_f32_16x16x32_bf16 v[24:27],a[224:227],v[48:51],v[24:27]
ds_read_b128 v[44:47],v2 offset:3072
v_mfma_f32_16x16x32_bf16 v[28:31],a[228:231],v[48:51],v[28:31]
s_waitcnt lgkmcnt(3)
buffer_load_dwordx4 v0,s[36:39],s66 offen offset:2048 sc1 lds
v_mfma_f32_16x16x32_bf16 v[24:27],a[232:235],v[52:55],v[24:27]
ds_read_b128 v[48:51],v2 offset:4096
v_mfma_f32_16x16x32_bf16 v[28:31],a[236:239],v[52:55],v[28:31]
v_mfma_f32_16x16x32_bf16 v[24:27],a[240:243],v[56:59],v[24:27]
ds_read_b128 v[52:55],v2 offset:5120
v_mfma_f32_16x16x32_bf16 v[28:31],a[244:247],v[56:59],v[28:31]
buffer_load_dwordx4 v0,s[36:39],s66 offen offset:3072 sc1 lds
v_mfma_f32_16x16x32_bf16 v[24:27],a[248:251],v[60:63],v[24:27]
s_waitcnt lgkmcnt(2)
ds_read_b128 v[56:59],v2 offset:6144
v_mfma_f32_16x16x32_bf16 v[28:31],a[252:255],v[60:63],v[28:31]
s_cbranch_scc1 Lg2_d0w3_%=
s_branch Lg2_slow0_%=
Lg2_slow0_%=:
s_cmp_lt_u32 s66,s59
s_cbranch_scc1 Lg2_gs0_%=
s_cmp_eq_u32 s54,1
s_cselect_b32 s68,s49,s48
s_lshr_b32 s68,s68,5
s_add_u32 m0,s78,8192
s_nop 0
buffer_load_dword v6,s[36:39],s68 offen offset:0 sc1 lds
s_lshr_b32 s73,s47,0
s_lshr_b32 s85,s47,5
v_lshlrev_b32 v64,16,v84
v_and_b32 v65,0xffff0000,v84
v_lshlrev_b32 v66,16,v85
v_and_b32 v67,0xffff0000,v85
v_lshlrev_b32 v68,16,v86
v_and_b32 v69,0xffff0000,v86
v_lshlrev_b32 v70,16,v87
v_and_b32 v71,0xffff0000,v87
v_fma_f32 v64,v64,v91,v90
v_fma_f32 v65,v65,v91,v90
v_fma_f32 v66,v66,v91,v90
v_fma_f32 v67,v67,v91,v90
v_fma_f32 v68,v68,v91,v90
v_fma_f32 v69,v69,v91,v90
v_fma_f32 v70,v70,v91,v90
v_fma_f32 v71,v71,v91,v90
v_add_f32 v24,v24,v64
v_add_f32 v25,v25,v65
v_add_f32 v26,v26,v66
v_add_f32 v27,v27,v67
v_add_f32 v28,v28,v68
v_add_f32 v29,v29,v69
v_add_f32 v30,v30,v70
v_add_f32 v31,v31,v71
v_cvt_pk_bf16_f32 v72,v24,v25
v_cvt_pk_bf16_f32 v73,v26,v27
v_cvt_pk_bf16_f32 v74,v28,v29
v_cvt_pk_bf16_f32 v75,v30,v31
s_add_u32 m0,s78,8448
s_nop 0
buffer_load_dword v6,s[36:39],s68 offen offset:0 sc1 lds
buffer_store_dwordx4 v[72:75],v3,s[36:39],s73 offen offset:0 sc0 sc1
v_add_f32 v64,v24,v25
v_mul_f32 v65,v24,v24
v_fma_f32 v65,v25,v25,v65
v_add_f32 v64,v64,v26
v_fma_f32 v65,v26,v26,v65
v_add_f32 v64,v64,v27
v_fma_f32 v65,v27,v27,v65
v_add_f32 v64,v64,v28
v_fma_f32 v65,v28,v28,v65
v_add_f32 v64,v64,v29
v_fma_f32 v65,v29,v29,v65
v_add_f32 v64,v64,v30
v_fma_f32 v65,v30,v30,v65
v_add_f32 v64,v64,v31
v_fma_f32 v65,v31,v31,v65
s_nop 1
v_permlane16_swap_b32 v64,v65
v_add_f32 v64,v64,v65
v_mov_b32 v65,v64
s_nop 1
v_permlane32_swap_b32 v64,v65
v_add_f32 v64,v64,v65
ds_write_b32 v13,v64 offset:0
s_waitcnt lgkmcnt(0)
s_barrier
ds_read_b128 v[76:79],v14 offset:0
s_waitcnt lgkmcnt(0)
v_add_f32 v76,v76,v77
v_add_f32 v78,v78,v79
v_add_f32 v76,v76,v78
buffer_store_dword v76,v12,s[36:39],s85 offen offset:0 sc0 sc1
s_cmp_ge_u32 s48,s59
s_cbranch_scc1 Lg2_done0_%=
s_add_u32 m0,s78,16384
s_nop 0
buffer_load_dword v6,s[36:39],s68 offen offset:0 sc1 lds
s_waitcnt vmcnt(4)
s_barrier
ds_read_b32 v15,v7 offset:8192
s_lshr_b32 s70,s46,5
buffer_store_dword v5,v4,s[36:39],s70 offen offset:0 sc0 sc1
s_mov_b32 s46,s59
s_mov_b32 s69,18432
s_waitcnt lgkmcnt(0)
v_cmp_eq_u32 vcc,s60,v15
s_nop 0
s_cmp_eq_u32 vcc_lo,-1
s_cbranch_scc1 Lg2_go0_%=
s_add_u32 m0,s78,16640
s_nop 0
buffer_load_dword v6,s[36:39],s68 offen offset:0 sc1 lds
s_waitcnt vmcnt(5)
s_barrier
ds_read_b32 v15,v7 offset:8448
s_mov_b32 s69,18688
s_waitcnt lgkmcnt(0)
v_cmp_eq_u32 vcc,s60,v15
s_nop 0
s_cmp_eq_u32 vcc_lo,-1
s_cbranch_scc1 Lg2_go0_%=
s_add_u32 m0,s78,8192
s_nop 0
buffer_load_dword v6,s[36:39],s68 offen offset:0 sc1 lds
s_waitcnt vmcnt(3)
s_barrier
ds_read_b32 v15,v7 offset:16384
s_lshr_b32 s70,s47,5
buffer_store_dword v5,v4,s[36:39],s70 offen offset:0 sc0 sc1
s_mov_b32 s47,s59
s_mov_b32 s69,10240
s_waitcnt lgkmcnt(0)
v_cmp_eq_u32 vcc,s60,v15
s_nop 0
s_cmp_eq_u32 vcc_lo,-1
s_cbranch_scc1 Lg2_go0_%=
s_add_u32 m0,s78,8448
s_nop 0
buffer_load_dword v6,s[36:39],s68 offen offset:0 sc1 lds
s_waitcnt vmcnt(3)
s_barrier
ds_read_b32 v15,v7 offset:16640
s_mov_b32 s69,10496
s_waitcnt lgkmcnt(0)
v_cmp_eq_u32 vcc,s60,v15
s_nop 0
s_cmp_eq_u32 vcc_lo,-1
s_cbranch_scc1 Lg2_go0_%=
Lg2_poll0_%=:
s_add_u32 m0,s78,16384
s_nop 0
buffer_load_dword v6,s[36:39],s68 offen offset:0 sc1 lds
s_waitcnt vmcnt(2)
s_barrier
ds_read_b32 v15,v7 offset:8192
s_mov_b32 s69,18432
s_waitcnt lgkmcnt(0)
v_cmp_eq_u32 vcc,s60,v15
s_nop 0
s_cmp_eq_u32 vcc_lo,-1
s_cbranch_scc1 Lg2_go0_%=
s_sleep 4
s_add_u32 m0,s78,16640
s_nop 0
buffer_load_dword v6,s[36:39],s68 offen offset:0 sc1 lds
s_waitcnt vmcnt(2)
s_barrier
ds_read_b32 v15,v7 offset:8448
s_mov_b32 s69,18688
s_waitcnt lgkmcnt(0)
v_cmp_eq_u32 vcc,s60,v15
s_nop 0
s_cmp_eq_u32 vcc_lo,-1
s_cbranch_scc1 Lg2_go0_%=
s_sleep 4
s_add_u32 m0,s78,8192
s_nop 0
buffer_load_dword v6,s[36:39],s68 offen offset:0 sc1 lds
s_waitcnt vmcnt(2)
s_barrier
ds_read_b32 v15,v7 offset:16384
s_mov_b32 s69,10240
s_waitcnt lgkmcnt(0))"
R"(
v_cmp_eq_u32 vcc,s60,v15
s_nop 0
s_cmp_eq_u32 vcc_lo,-1
s_cbranch_scc1 Lg2_go0_%=
s_sleep 4
s_add_u32 m0,s78,8448
s_nop 0
buffer_load_dword v6,s[36:39],s68 offen offset:0 sc1 lds
s_waitcnt vmcnt(2)
s_barrier
ds_read_b32 v15,v7 offset:16640
s_mov_b32 s69,10496
s_waitcnt lgkmcnt(0)
v_cmp_eq_u32 vcc,s60,v15
s_nop 0
s_cmp_eq_u32 vcc_lo,-1
s_cbranch_scc1 Lg2_go0_%=
s_sleep 4
s_add_u32 s79,s79,1
s_cmp_lt_u32 s79,20000
s_cbranch_scc1 Lg2_poll0_%=
Lg2_go0_%=:
s_xor_b32 s62,s63,65536
s_mov_b32 m0,s62
s_nop 0
buffer_load_dwordx4 v0,s[36:39],s48 offen offset:0 sc1 lds
buffer_load_dwordx4 v0,s[36:39],s48 offen offset:1024 sc1 lds
buffer_load_dwordx4 v0,s[36:39],s48 offen offset:2048 sc1 lds
buffer_load_dwordx4 v0,s[36:39],s48 offen offset:3072 sc1 lds
s_add_u32 m0,s62,16384
s_nop 0
buffer_load_dwordx4 v1,s[36:39],s48 offen offset:0 sc1 lds
buffer_load_dwordx4 v1,s[36:39],s48 offen offset:1024 sc1 lds
buffer_load_dwordx4 v1,s[36:39],s48 offen offset:2048 sc1 lds
buffer_load_dwordx4 v1,s[36:39],s48 offen offset:3072 sc1 lds
s_lshr_b32 s72,s48,6
s_lshr_b32 s71,s62,2
s_add_u32 m0,s71,s88
s_nop 0
buffer_load_dwordx4 v10,s[36:39],s48 offen offset:0 sc1 lds
buffer_load_dword v11,s[36:39],s72 offen offset:1024 sc1 lds
s_cmp_eq_u32 vcc_hi,-1
s_cselect_b32 s66,s49,s59
s_xor_b32 s62,s64,65536
s_cmp_ge_u32 s66,s59
s_cselect_b32 s68,s49,s50
s_lshr_b32 s68,s68,5
s_add_u32 m0,s78,0
s_nop 0
buffer_load_dword v6,s[36:39],s68 offen offset:0 sc1 lds
s_mov_b32 m0,s62
s_nop 0
buffer_load_dwordx4 v0,s[36:39],s66 offen offset:0 sc1 lds
buffer_load_dwordx4 v0,s[36:39],s66 offen offset:1024 sc1 lds
buffer_load_dwordx4 v0,s[36:39],s66 offen offset:2048 sc1 lds
buffer_load_dwordx4 v0,s[36:39],s66 offen offset:3072 sc1 lds
s_waitcnt vmcnt(11)
s_barrier
ds_read_b128 v[32:35],v2 offset:0
ds_read_b128 v[36:39],v2 offset:1024
ds_read_b128 v[40:43],v2 offset:2048
ds_read_b128 v[44:47],v2 offset:3072
ds_read_b128 v[48:51],v2 offset:4096
ds_read_b128 v[52:55],v2 offset:5120
ds_read_b128 v[56:59],v2 offset:6144
s_lshr_b32 s70,s46,5
buffer_store_dword v5,v4,s[36:39],s70 offen offset:0 sc0 sc1
s_mov_b32 s46,s59
s_lshr_b32 s70,s47,5
buffer_store_dword v5,v4,s[36:39],s70 offen offset:0 sc0 sc1
s_mov_b32 s47,s59
s_lshr_b32 s70,s59,5
buffer_store_dword v5,v4,s[36:39],s70 offen offset:0 sc0 sc1
s_waitcnt lgkmcnt(3)
s_setpc_b64 s[90:91]
Lg2_gs0_%=:
s_cmp_eq_u32 s54,1
s_cselect_b32 s68,s49,s48
s_lshr_b32 s68,s68,5
s_lshr_b32 s73,s47,0
s_lshr_b32 s85,s47,5
s_add_u32 m0,s78,8192
s_nop 0
buffer_load_dword v6,s[36:39],s68 offen offset:0 sc1 lds
v_lshlrev_b32 v64,16,v84
v_and_b32 v65,0xffff0000,v84
v_lshlrev_b32 v66,16,v85
v_and_b32 v67,0xffff0000,v85
v_lshlrev_b32 v68,16,v86
v_and_b32 v69,0xffff0000,v86
v_lshlrev_b32 v70,16,v87
v_and_b32 v71,0xffff0000,v87
s_add_u32 m0,s62,16384
s_nop 0
buffer_load_dwordx4 v1,s[36:39],s66 offen offset:0 sc1 lds
v_fma_f32 v64,v64,v91,v90
v_fma_f32 v65,v65,v91,v90
v_fma_f32 v66,v66,v91,v90
v_fma_f32 v67,v67,v91,v90
v_fma_f32 v68,v68,v91,v90
v_fma_f32 v69,v69,v91,v90
v_fma_f32 v70,v70,v91,v90
v_fma_f32 v71,v71,v91,v90
buffer_load_dwordx4 v1,s[36:39],s66 offen offset:1024 sc1 lds
v_add_f32 v24,v24,v64
v_add_f32 v25,v25,v65
v_add_f32 v26,v26,v66
v_add_f32 v27,v27,v67
v_add_f32 v28,v28,v68
v_add_f32 v29,v29,v69
v_add_f32 v30,v30,v70
v_add_f32 v31,v31,v71
buffer_load_dwordx4 v1,s[36:39],s66 offen offset:2048 sc1 lds
v_cvt_pk_bf16_f32 v72,v24,v25
v_cvt_pk_bf16_f32 v73,v26,v27
v_cvt_pk_bf16_f32 v74,v28,v29
v_cvt_pk_bf16_f32 v75,v30,v31
buffer_store_dwordx4 v[72:75],v3,s[36:39],s73 offen offset:0 sc0 sc1
v_add_f32 v64,v24,v25
v_mul_f32 v65,v24,v24
v_fma_f32 v65,v25,v25,v65
buffer_load_dwordx4 v1,s[36:39],s66 offen offset:3072 sc1 lds
v_add_f32 v64,v64,v26
v_fma_f32 v65,v26,v26,v65
v_add_f32 v64,v64,v27
v_fma_f32 v65,v27,v27,v65
v_add_f32 v64,v64,v28
v_fma_f32 v65,v28,v28,v65
v_add_f32 v64,v64,v29
v_fma_f32 v65,v29,v29,v65
s_lshr_b32 s72,s66,6
s_lshr_b32 s71,s62,2
s_add_u32 m0,s71,s88
s_nop 0
buffer_load_dwordx4 v10,s[36:39],s66 offen offset:0 sc1 lds
v_add_f32 v64,v64,v30
v_fma_f32 v65,v30,v30,v65
v_add_f32 v64,v64,v31
v_fma_f32 v65,v31,v31,v65
s_nop 1
v_permlane16_swap_b32 v64,v65
v_add_f32 v64,v64,v65
v_mov_b32 v65,v64
s_nop 1
v_permlane32_swap_b32 v64,v65
buffer_load_dword v11,s[36:39],s72 offen offset:1024 sc1 lds
v_add_f32 v64,v64,v65
ds_write_b32 v13,v64 offset:0
s_waitcnt lgkmcnt(0)
s_barrier
ds_read_b128 v[76:79],v14 offset:0
s_waitcnt lgkmcnt(0)
v_add_f32 v76,v76,v77
v_add_f32 v78,v78,v79
v_add_f32 v76,v76,v78
buffer_store_dword v76,v12,s[36:39],s85 offen offset:0 sc0 sc1
s_waitcnt vmcnt(8)
s_barrier
ds_read_b32 v15,v7 offset:8192
ds_read_b128 v[32:35],v2 offset:0
ds_read_b128 v[36:39],v2 offset:1024
ds_read_b128 v[40:43],v2 offset:2048
ds_read_b128 v[44:47],v2 offset:3072
ds_read_b128 v[48:51],v2 offset:4096
ds_read_b128 v[52:55],v2 offset:5120
ds_read_b128 v[56:59],v2 offset:6144
s_lshr_b32 s70,s46,5
buffer_store_dword v5,v4,s[36:39],s70 offen offset:0 sc0 sc1
s_waitcnt lgkmcnt(7)
v_cmp_eq_u32 vcc,s60,v15
s_nop 0
s_cmp_eq_u32 vcc_hi,-1
s_cselect_b32 s66,s49,s59
s_xor_b32 s62,s64,65536
s_cmp_ge_u32 s66,s59
s_cselect_b32 s68,s49,s50
s_lshr_b32 s68,s68,5
s_add_u32 m0,s78,0
s_nop 0
buffer_load_dword v6,s[36:39],s68 offen offset:0 sc1 lds
s_mov_b32 m0,s62
s_nop 0
buffer_load_dwordx4 v0,s[36:39],s66 offen offset:0 sc1 lds
buffer_load_dwordx4 v0,s[36:39],s66 offen offset:1024 sc1 lds
buffer_load_dwordx4 v0,s[36:39],s66 offen offset:2048 sc1 lds
buffer_load_dwordx4 v0,s[36:39],s66 offen offset:3072 sc1 lds
s_mov_b32 s46,s47
s_mov_b32 s47,s59
s_lshr_b32 s70,s59,5
buffer_store_dword v5,v4,s[36:39],s70 offen offset:0 sc0 sc1
s_lshr_b32 s70,s59,5
buffer_store_dword v5,v4,s[36:39],s70 offen offset:0 sc0 sc1
s_waitcnt lgkmcnt(3)
s_setpc_b64 s[90:91]
Lg2_done0_%=:
s_waitcnt vmcnt(0)
s_barrier
s_lshr_b32 s70,s46,5
buffer_store_dword v5,v4,s[36:39],s70 offen offset:0 sc0 sc1
s_lshr_b32 s70,s47,5)"
R"(
buffer_store_dword v5,v4,s[36:39],s70 offen offset:0 sc0 sc1
s_branch Lg2_done_%=
Lg2_slow1_%=:
s_cmp_lt_u32 s67,s59
s_cbranch_scc1 Lg2_gs1_%=
s_cmp_eq_u32 s54,1
s_cselect_b32 s68,s50,s49
s_lshr_b32 s68,s68,5
s_add_u32 m0,s78,8192
s_nop 0
buffer_load_dword v6,s[36:39],s68 offen offset:0 sc1 lds
s_lshr_b32 s73,s48,0
s_lshr_b32 s85,s48,5
v_lshlrev_b32 v64,16,v80
v_and_b32 v65,0xffff0000,v80
v_lshlrev_b32 v66,16,v81
v_and_b32 v67,0xffff0000,v81
v_lshlrev_b32 v68,16,v82
v_and_b32 v69,0xffff0000,v82
v_lshlrev_b32 v70,16,v83
v_and_b32 v71,0xffff0000,v83
v_fma_f32 v64,v64,v89,v88
v_fma_f32 v65,v65,v89,v88
v_fma_f32 v66,v66,v89,v88
v_fma_f32 v67,v67,v89,v88
v_fma_f32 v68,v68,v89,v88
v_fma_f32 v69,v69,v89,v88
v_fma_f32 v70,v70,v89,v88
v_fma_f32 v71,v71,v89,v88
v_add_f32 v16,v16,v64
v_add_f32 v17,v17,v65
v_add_f32 v18,v18,v66
v_add_f32 v19,v19,v67
v_add_f32 v20,v20,v68
v_add_f32 v21,v21,v69
v_add_f32 v22,v22,v70
v_add_f32 v23,v23,v71
v_cvt_pk_bf16_f32 v72,v16,v17
v_cvt_pk_bf16_f32 v73,v18,v19
v_cvt_pk_bf16_f32 v74,v20,v21
v_cvt_pk_bf16_f32 v75,v22,v23
s_add_u32 m0,s78,8448
s_nop 0
buffer_load_dword v6,s[36:39],s68 offen offset:0 sc1 lds
buffer_store_dwordx4 v[72:75],v3,s[36:39],s73 offen offset:0 sc0 sc1
v_add_f32 v64,v16,v17
v_mul_f32 v65,v16,v16
v_fma_f32 v65,v17,v17,v65
v_add_f32 v64,v64,v18
v_fma_f32 v65,v18,v18,v65
v_add_f32 v64,v64,v19
v_fma_f32 v65,v19,v19,v65
v_add_f32 v64,v64,v20
v_fma_f32 v65,v20,v20,v65
v_add_f32 v64,v64,v21
v_fma_f32 v65,v21,v21,v65
v_add_f32 v64,v64,v22
v_fma_f32 v65,v22,v22,v65
v_add_f32 v64,v64,v23
v_fma_f32 v65,v23,v23,v65
s_nop 1
v_permlane16_swap_b32 v64,v65
v_add_f32 v64,v64,v65
v_mov_b32 v65,v64
s_nop 1
v_permlane32_swap_b32 v64,v65
v_add_f32 v64,v64,v65
ds_write_b32 v13,v64 offset:2048
s_waitcnt lgkmcnt(0)
s_barrier
ds_read_b128 v[76:79],v14 offset:2048
s_waitcnt lgkmcnt(0)
v_add_f32 v76,v76,v77
v_add_f32 v78,v78,v79
v_add_f32 v76,v76,v78
buffer_store_dword v76,v12,s[36:39],s85 offen offset:0 sc0 sc1
s_cmp_ge_u32 s49,s59
s_cbranch_scc1 Lg2_done1_%=
s_add_u32 m0,s78,16384
s_nop 0
buffer_load_dword v6,s[36:39],s68 offen offset:0 sc1 lds
s_waitcnt vmcnt(4)
s_barrier
ds_read_b32 v15,v7 offset:8192
s_lshr_b32 s70,s47,5
buffer_store_dword v5,v4,s[36:39],s70 offen offset:0 sc0 sc1
s_mov_b32 s47,s59
s_mov_b32 s69,18432
s_waitcnt lgkmcnt(0)
v_cmp_eq_u32 vcc,s60,v15
s_nop 0
s_cmp_eq_u32 vcc_lo,-1
s_cbranch_scc1 Lg2_go1_%=
s_add_u32 m0,s78,16640
s_nop 0
buffer_load_dword v6,s[36:39],s68 offen offset:0 sc1 lds
s_waitcnt vmcnt(5)
s_barrier
ds_read_b32 v15,v7 offset:8448
s_mov_b32 s69,18688
s_waitcnt lgkmcnt(0)
v_cmp_eq_u32 vcc,s60,v15
s_nop 0
s_cmp_eq_u32 vcc_lo,-1
s_cbranch_scc1 Lg2_go1_%=
s_add_u32 m0,s78,8192
s_nop 0
buffer_load_dword v6,s[36:39],s68 offen offset:0 sc1 lds
s_waitcnt vmcnt(3)
s_barrier
ds_read_b32 v15,v7 offset:16384
s_lshr_b32 s70,s48,5
buffer_store_dword v5,v4,s[36:39],s70 offen offset:0 sc0 sc1
s_mov_b32 s48,s59
s_mov_b32 s69,10240
s_waitcnt lgkmcnt(0)
v_cmp_eq_u32 vcc,s60,v15
s_nop 0
s_cmp_eq_u32 vcc_lo,-1
s_cbranch_scc1 Lg2_go1_%=
s_add_u32 m0,s78,8448
s_nop 0
buffer_load_dword v6,s[36:39],s68 offen offset:0 sc1 lds
s_waitcnt vmcnt(3)
s_barrier
ds_read_b32 v15,v7 offset:16640
s_mov_b32 s69,10496
s_waitcnt lgkmcnt(0)
v_cmp_eq_u32 vcc,s60,v15
s_nop 0
s_cmp_eq_u32 vcc_lo,-1
s_cbranch_scc1 Lg2_go1_%=
Lg2_poll1_%=:
s_add_u32 m0,s78,16384
s_nop 0
buffer_load_dword v6,s[36:39],s68 offen offset:0 sc1 lds
s_waitcnt vmcnt(2)
s_barrier
ds_read_b32 v15,v7 offset:8192
s_mov_b32 s69,18432
s_waitcnt lgkmcnt(0)
v_cmp_eq_u32 vcc,s60,v15
s_nop 0
s_cmp_eq_u32 vcc_lo,-1
s_cbranch_scc1 Lg2_go1_%=
s_sleep 4
s_add_u32 m0,s78,16640
s_nop 0
buffer_load_dword v6,s[36:39],s68 offen offset:0 sc1 lds
s_waitcnt vmcnt(2)
s_barrier
ds_read_b32 v15,v7 offset:8448
s_mov_b32 s69,18688
s_waitcnt lgkmcnt(0)
v_cmp_eq_u32 vcc,s60,v15
s_nop 0
s_cmp_eq_u32 vcc_lo,-1
s_cbranch_scc1 Lg2_go1_%=
s_sleep 4
s_add_u32 m0,s78,8192
s_nop 0
buffer_load_dword v6,s[36:39],s68 offen offset:0 sc1 lds
s_waitcnt vmcnt(2)
s_barrier
ds_read_b32 v15,v7 offset:16384
s_mov_b32 s69,10240
s_waitcnt lgkmcnt(0)
v_cmp_eq_u32 vcc,s60,v15
s_nop 0
s_cmp_eq_u32 vcc_lo,-1
s_cbranch_scc1 Lg2_go1_%=
s_sleep 4
s_add_u32 m0,s78,8448
s_nop 0
buffer_load_dword v6,s[36:39],s68 offen offset:0 sc1 lds
s_waitcnt vmcnt(2)
s_barrier
ds_read_b32 v15,v7 offset:16640
s_mov_b32 s69,10496
s_waitcnt lgkmcnt(0)
v_cmp_eq_u32 vcc,s60,v15
s_nop 0
s_cmp_eq_u32 vcc_lo,-1
s_cbranch_scc1 Lg2_go1_%=
s_sleep 4
s_add_u32 s79,s79,1
s_cmp_lt_u32 s79,20000
s_cbranch_scc1 Lg2_poll1_%=
Lg2_go1_%=:
s_xor_b32 s62,s64,65536
s_mov_b32 m0,s62
s_nop 0
buffer_load_dwordx4 v0,s[36:39],s49 offen offset:0 sc1 lds
buffer_load_dwordx4 v0,s[36:39],s49 offen offset:1024 sc1 lds
buffer_load_dwordx4 v0,s[36:39],s49 offen offset:2048 sc1 lds
buffer_load_dwordx4 v0,s[36:39],s49 offen offset:3072 sc1 lds
s_add_u32 m0,s62,16384
s_nop 0
buffer_load_dwordx4 v1,s[36:39],s49 offen offset:0 sc1 lds
buffer_load_dwordx4 v1,s[36:39],s49 offen offset:1024 sc1 lds)"
R"(
buffer_load_dwordx4 v1,s[36:39],s49 offen offset:2048 sc1 lds
buffer_load_dwordx4 v1,s[36:39],s49 offen offset:3072 sc1 lds
s_lshr_b32 s72,s49,6
s_lshr_b32 s71,s62,2
s_add_u32 m0,s71,s88
s_nop 0
buffer_load_dwordx4 v10,s[36:39],s49 offen offset:0 sc1 lds
buffer_load_dword v11,s[36:39],s72 offen offset:1024 sc1 lds
s_cmp_eq_u32 vcc_hi,-1
s_cselect_b32 s67,s50,s59
s_xor_b32 s62,s63,65536
s_cmp_ge_u32 s67,s59
s_cselect_b32 s68,s50,s51
s_lshr_b32 s68,s68,5
s_add_u32 m0,s78,256
s_nop 0
buffer_load_dword v6,s[36:39],s68 offen offset:0 sc1 lds
s_mov_b32 m0,s62
s_nop 0
buffer_load_dwordx4 v0,s[36:39],s67 offen offset:0 sc1 lds
buffer_load_dwordx4 v0,s[36:39],s67 offen offset:1024 sc1 lds
buffer_load_dwordx4 v0,s[36:39],s67 offen offset:2048 sc1 lds
buffer_load_dwordx4 v0,s[36:39],s67 offen offset:3072 sc1 lds
s_waitcnt vmcnt(11)
s_barrier
ds_read_b128 v[32:35],v2 offset:32768
ds_read_b128 v[36:39],v2 offset:33792
ds_read_b128 v[40:43],v2 offset:34816
ds_read_b128 v[44:47],v2 offset:35840
ds_read_b128 v[48:51],v2 offset:36864
ds_read_b128 v[52:55],v2 offset:37888
ds_read_b128 v[56:59],v2 offset:38912
s_lshr_b32 s70,s47,5
buffer_store_dword v5,v4,s[36:39],s70 offen offset:0 sc0 sc1
s_mov_b32 s47,s59
s_lshr_b32 s70,s48,5
buffer_store_dword v5,v4,s[36:39],s70 offen offset:0 sc0 sc1
s_mov_b32 s48,s59
s_lshr_b32 s70,s59,5
buffer_store_dword v5,v4,s[36:39],s70 offen offset:0 sc0 sc1
s_waitcnt lgkmcnt(3)
s_setpc_b64 s[92:93]
Lg2_gs1_%=:
s_cmp_eq_u32 s54,1
s_cselect_b32 s68,s50,s49
s_lshr_b32 s68,s68,5
s_lshr_b32 s73,s48,0
s_lshr_b32 s85,s48,5
s_add_u32 m0,s78,8192
s_nop 0
buffer_load_dword v6,s[36:39],s68 offen offset:0 sc1 lds
v_lshlrev_b32 v64,16,v80
v_and_b32 v65,0xffff0000,v80
v_lshlrev_b32 v66,16,v81
v_and_b32 v67,0xffff0000,v81
v_lshlrev_b32 v68,16,v82
v_and_b32 v69,0xffff0000,v82
v_lshlrev_b32 v70,16,v83
v_and_b32 v71,0xffff0000,v83
s_add_u32 m0,s62,16384
s_nop 0
buffer_load_dwordx4 v1,s[36:39],s67 offen offset:0 sc1 lds
v_fma_f32 v64,v64,v89,v88
v_fma_f32 v65,v65,v89,v88
v_fma_f32 v66,v66,v89,v88
v_fma_f32 v67,v67,v89,v88
v_fma_f32 v68,v68,v89,v88
v_fma_f32 v69,v69,v89,v88
v_fma_f32 v70,v70,v89,v88
v_fma_f32 v71,v71,v89,v88
buffer_load_dwordx4 v1,s[36:39],s67 offen offset:1024 sc1 lds
v_add_f32 v16,v16,v64
v_add_f32 v17,v17,v65
v_add_f32 v18,v18,v66
v_add_f32 v19,v19,v67
v_add_f32 v20,v20,v68
v_add_f32 v21,v21,v69
v_add_f32 v22,v22,v70
v_add_f32 v23,v23,v71
buffer_load_dwordx4 v1,s[36:39],s67 offen offset:2048 sc1 lds
v_cvt_pk_bf16_f32 v72,v16,v17
v_cvt_pk_bf16_f32 v73,v18,v19
v_cvt_pk_bf16_f32 v74,v20,v21
v_cvt_pk_bf16_f32 v75,v22,v23
buffer_store_dwordx4 v[72:75],v3,s[36:39],s73 offen offset:0 sc0 sc1
v_add_f32 v64,v16,v17
v_mul_f32 v65,v16,v16
v_fma_f32 v65,v17,v17,v65
buffer_load_dwordx4 v1,s[36:39],s67 offen offset:3072 sc1 lds
v_add_f32 v64,v64,v18
v_fma_f32 v65,v18,v18,v65
v_add_f32 v64,v64,v19
v_fma_f32 v65,v19,v19,v65
v_add_f32 v64,v64,v20
v_fma_f32 v65,v20,v20,v65
v_add_f32 v64,v64,v21
v_fma_f32 v65,v21,v21,v65
s_lshr_b32 s72,s67,6
s_lshr_b32 s71,s62,2
s_add_u32 m0,s71,s88
s_nop 0
buffer_load_dwordx4 v10,s[36:39],s67 offen offset:0 sc1 lds
v_add_f32 v64,v64,v22
v_fma_f32 v65,v22,v22,v65
v_add_f32 v64,v64,v23
v_fma_f32 v65,v23,v23,v65
s_nop 1
v_permlane16_swap_b32 v64,v65
v_add_f32 v64,v64,v65
v_mov_b32 v65,v64
s_nop 1
v_permlane32_swap_b32 v64,v65
buffer_load_dword v11,s[36:39],s72 offen offset:1024 sc1 lds
v_add_f32 v64,v64,v65
ds_write_b32 v13,v64 offset:2048
s_waitcnt lgkmcnt(0)
s_barrier
ds_read_b128 v[76:79],v14 offset:2048
s_waitcnt lgkmcnt(0)
v_add_f32 v76,v76,v77
v_add_f32 v78,v78,v79
v_add_f32 v76,v76,v78
buffer_store_dword v76,v12,s[36:39],s85 offen offset:0 sc0 sc1
s_waitcnt vmcnt(8)
s_barrier
ds_read_b32 v15,v7 offset:8192
ds_read_b128 v[32:35],v2 offset:32768
ds_read_b128 v[36:39],v2 offset:33792
ds_read_b128 v[40:43],v2 offset:34816
ds_read_b128 v[44:47],v2 offset:35840
ds_read_b128 v[48:51],v2 offset:36864
ds_read_b128 v[52:55],v2 offset:37888
ds_read_b128 v[56:59],v2 offset:38912
s_lshr_b32 s70,s47,5
buffer_store_dword v5,v4,s[36:39],s70 offen offset:0 sc0 sc1
s_waitcnt lgkmcnt(7)
v_cmp_eq_u32 vcc,s60,v15
s_nop 0
s_cmp_eq_u32 vcc_hi,-1
s_cselect_b32 s67,s50,s59
s_xor_b32 s62,s63,65536
s_cmp_ge_u32 s67,s59
s_cselect_b32 s68,s50,s51
s_lshr_b32 s68,s68,5
s_add_u32 m0,s78,256
s_nop 0
buffer_load_dword v6,s[36:39],s68 offen offset:0 sc1 lds
s_mov_b32 m0,s62
s_nop 0
buffer_load_dwordx4 v0,s[36:39],s67 offen offset:0 sc1 lds
buffer_load_dwordx4 v0,s[36:39],s67 offen offset:1024 sc1 lds
buffer_load_dwordx4 v0,s[36:39],s67 offen offset:2048 sc1 lds
buffer_load_dwordx4 v0,s[36:39],s67 offen offset:3072 sc1 lds
s_mov_b32 s47,s48
s_mov_b32 s48,s59
s_lshr_b32 s70,s59,5
buffer_store_dword v5,v4,s[36:39],s70 offen offset:0 sc0 sc1
s_lshr_b32 s70,s59,5
buffer_store_dword v5,v4,s[36:39],s70 offen offset:0 sc0 sc1
s_waitcnt lgkmcnt(3)
s_setpc_b64 s[92:93]
Lg2_done1_%=:
s_waitcnt vmcnt(0)
s_barrier
s_lshr_b32 s70,s47,5
buffer_store_dword v5,v4,s[36:39],s70 offen offset:0 sc0 sc1
s_lshr_b32 s70,s48,5
buffer_store_dword v5,v4,s[36:39],s70 offen offset:0 sc0 sc1
s_branch Lg2_done_%=
Lg2_done_%=:
s_waitcnt vmcnt(0) lgkmcnt(0)
s_barrier
Lg2_end_%=:
s_mov_b32 %0,s60
Lcode_g12_e:)"
  : "=&s"(epOut) : "s"(ws), "s"(wave), "s"(sStep), "s"(sX1), "s"(sX2), "s"(sU0), "s"(sWw), "s"(sBuf), "v"(vSrc), "v"(vSrc2), "v"(vA), "v"(vOut), "v"(vFlag), "v"(vPollOff), "v"(vSnapRd), "v"(vX), "v"(vX2), "v"(vAuxS), "v"(vAuxS2), "v"(vOut2), "v"(vStW), "v"(vStR), "v"(vWd0), "v"(vWd1), "v"(vWr0), "v"(vWr1), "v"(vWr2), "s"(w12) : "v0", "v1", "v2", "v3", "v4", "v5", "v6", "v7", "v8", "v9", "v10", "v11", "v12", "v13", "v14", "v15", "v16", "v17", "v18", "v19", "v20", "v21", "v22", "v23", "v24", "v25", "v26", "v27", "v28", "v29", "v30", "v31", "v32", "v33", "v34", "v35", "v36", "v37", "v38", "v39", "v40", "v41", "v42", "v43", "v44", "v45", "v46", "v47", "v48", "v49", "v50", "v51", "v52", "v53", "v54", "v55", "v56", "v57", "v58", "v59", "v60", "v61", "v62", "v63", "v64", "v65", "v66", "v67", "v68", "v69", "v70", "v71", "v72", "v73", "v74", "v75", "v76", "v77", "v78", "v79", "v80", "v81", "v82", "v83", "v84", "v85", "v86", "v87", "v88", "v89", "v90", "v91", "v92", "v93", "v94", "v95", "v96", "v97", "v98", "v99", "v100", "v101", "v102", "v103", "v104", "v105", "v106", "v107", "v108", "v109", "v110", "v111", "v112", "v113", "v114", "v115", "v116", "v117", "v118", "v119", "v120", "v121", "v122", "v123", "v124", "v125", "v126", "v127", "v128", "v129", "v130", "v131", "v132", "v133", "v134", "v135", "v136", "v137", "v138", "v139", "v140", "v141", "v142", "v143", "v144", "v145", "v146", "v147", "v148", "v149", "v150", "v151", "v152", "v153", "v154", "v155", "v156", "v157", "v158", "v159", "v160", "v161", "v162", "v163", "v164", "v165", "v166", "v167", "v168", "v169", "v170", "v171", "v172", "v173", "v174", "v175", "v176", "v177", "v178", "v179", "v180", "v181", "v182", "v183", "v184", "v185", "v186", "v187", "v188", "v189", "v190", "v191", "v192", "v193", "v194", "v195", "v196", "v197", "v198", "v199", "v200", "v201", "v202", "v203", "v204", "v205", "v206", "v207", "v208", "v209", "v210", "v211", "v212", "v213", "v214", "v215", "v216", "v217", "v218", "v219", "v220", "v221", "v222", "v223", "v224", "v225", "v226", "v227", "v228", "v229", "v230", "v231", "v255", "a0", "a1", "a2", "a3", "a4", "a5", "a6", "a7", "a8", "a9", "a10", "a11", "a12", "a13", "a14", "a15", "a16", "a17", "a18", "a19", "a20", "a21", "a22", "a23", "a24", "a25", "a26", "a27", "a28", "a29", "a30", "a31", "a32", "a33", "a34", "a35", "a36", "a37", "a38", "a39", "a40", "a41", "a42", "a43", "a44", "a45", "a46", "a47", "a48", "a49", "a50", "a51", "a52", "a53", "a54", "a55", "a56", "a57", "a58", "a59", "a60", "a61", "a62", "a63", "a64", "a65", "a66", "a67", "a68", "a69", "a70", "a71", "a72", "a73", "a74", "a75", "a76", "a77", "a78", "a79", "a80", "a81", "a82", "a83", "a84", "a85", "a86", "a87", "a88", "a89", "a90", "a91", "a92", "a93", "a94", "a95", "a96", "a97", "a98", "a99", "a100", "a101", "a102", "a103", "a104", "a105", "a106", "a107", "a108", "a109", "a110", "a111", "a112", "a113", "a114", "a115", "a116", "a117", "a118", "a119", "a120", "a121", "a122", "a123", "a124", "a125", "a126", "a127", "a128", "a129", "a130", "a131", "a132", "a133", "a134", "a135", "a136", "a137", "a138", "a139", "a140", "a141", "a142", "a143", "a144", "a145", "a146", "a147", "a148", "a149", "a150", "a151", "a152", "a153", "a154", "a155", "a156", "a157", "a158", "a159", "a160", "a161", "a162", "a163", "a164", "a165", "a166", "a167", "a168", "a169", "a170", "a171", "a172", "a173", "a174", "a175", "a176", "a177", "a178", "a179", "a180", "a181", "a182", "a183", "a184", "a185", "a186", "a187", "a188", "a189", "a190", "a191", "a192", "a193", "a194", "a195", "a196", "a197", "a198", "a199", "a200", "a201", "a202", "a203", "a204", "a205", "a206", "a207", "a208", "a209", "a210", "a211", "a212", "a213", "a214", "a215", "a216", "a217", "a218", "a219", "a220", "a221", "a222", "a223", "a224", "a225", "a226", "a227", "a228", "a229", "a230", "a231", "a232", "a233", "a234", "a235", "a236", "a237", "a238", "a239", "a240", "a241", "a242", "a243", "a244", "a245", "a246", "a247", "a248", "a249", "a250", "a251", "a252", "a253", "a254", "a255", "s36", "s37", "s38", "s39", "s40", "s41", "s42", "s43", "s44", "s45", "s46", "s47", "s48", "s49", "s50", "s51", "s52", "s53", "s54", "s55", "s56", "s57", "s58", "s59", "s60", "s61", "s62", "s63", "s64", "s65", "s66", "s67", "s68", "s69", "s70", "s71", "s72", "s73", "s74", "s75", "s76", "s77", "s78", "s79", "s80", "s81", "s82", "s83", "s84", "s85", "s86", "s87", "s88", "s89", "s90", "s91", "s92", "s93", "s94", "s95", "s96", "s97", "s98", "s99", "vcc", "scc", "memory");
    ep = epOut;
   }
    if (kind == 6)
    {
      LANE_ID(lr);
      const int r8 = lr & 7, seg = lr >> 3;
      const unsigned sEpR = __builtin_amdgcn_readfirstlane(ep), sK = slice, sStep = NRW, sK1 = RT1 + slice, sRq = OFF_FXP + LB * 2048 + wave * 128, sDbg = sDbg0 + (kind == 6 ? 0 : 1024);
      const unsigned vRF = (wave * 8 + (seg >> 2)) * 1024 + ((seg & 3) * 16 + r8) * 16;
      const unsigned vRwr = sLds + (seg == 0 ? r8 * 64 + wave * 4 : 512 + lr * 4), vRrd = sLds + r8 * 64;
      const unsigned vRP = sLds + ((lr >> 4) == ((lr & 15) >> 2) ? r8 * 64 + 16 + wave * 8 + (seg & 1) * 4 : 768 + lr * 4);
      const unsigned vRS = (wave == 0 && seg == 0) ? r8 * 16 : 0x80000000u, vFlag = lr == 0 ? 0u : 0x80000000u, vL4 = (lr & 15) * 128, vRL = wave * 4096 + lr * 16;
      asm volatile(R"(
Lcode_r_b:
s_mov_b64 s[36:37],%0
s_mov_b32 s38,111193216
s_mov_b32 s39,0x00020000
s_mov_b32 s54,%1
s_mov_b32 s50,%2
s_mov_b32 s92,%3
s_mov_b32 s48,%4
v_mov_b32 v0,%5
v_mov_b32 v1,%6
v_mov_b32 v2,%7
v_mov_b32 v3,%8
v_mov_b32 v4,%9
v_mov_b32 v5,%10
v_mov_b32 v6,%11
v_mov_b32 v11,%12
v_lshrrev_b32 v10,1,v4
v_mov_b32 v7,s48
v_mbcnt_lo_u32_b32 v9,-1,0
v_mbcnt_hi_u32_b32 v9,-1,v9
v_lshlrev_b32 v9,4,v9
s_mov_b32 s74,0
s_mov_b32 s96,0
s_mov_b32 s60,536870912
s_mov_b32 s46,0xaaaaaaaa
s_mov_b32 s47,0xaaaaaaaa
s_mov_b32 s56,0xcccccccc
s_mov_b32 s57,0xcccccccc
s_mov_b32 s76,0x3a800000
s_mov_b32 s77,0x3727c5ac
s_mov_b32 s78,0xc0135761
s_mov_b32 s79,0x3d372713
v_mov_b32 v192,0x3f803f80
v_mov_b32 v193,0x3f803f80
v_mov_b32 v194,0x3f803f80
v_mov_b32 v195,0x3f803f80
s_lshr_b32 s63,s54,1
s_and_b32 s64,s54,1
s_lshl_b32 s64,s64,7
s_lshl_b32 s87,s54,14
s_lshl_b32 s58,s63,15
s_add_u32 s58,s58,s64
s_add_u32 s58,s58,16908288
s_lshl_b32 s59,s63,9
s_add_u32 s59,s59,s64
s_add_u32 s59,s59,107022336
s_lshl_b32 s49,s54,9
s_add_u32 s49,s49,s92
s_lshl_b32 s91,s63,11
s_add_u32 s91,s91,109871104
s_lshl_b32 s69,s50,10
s_add_u32 s69,s69,s91
v_mov_b32 v20,0
v_mov_b32 v21,0
v_mov_b32 v22,0
v_mov_b32 v23,0
v_mov_b32 v24,0
v_mov_b32 v25,0
v_mov_b32 v26,0
v_mov_b32 v27,0
v_mov_b32 v16,0x3f80
v_mov_b32 v17,0x3f800000
v_cndmask_b32 v16,v16,v17,s[46:47]
s_mov_b32 s98,0x00300003
s_mov_b32 s99,0x30000300
v_cndmask_b32 v20,v20,v16,s[98:99]
s_lshl_b64 s[98:99],s[98:99],2
v_cndmask_b32 v21,v21,v16,s[98:99]
v_mov_b32 v26,v20
v_mov_b32 v27,v21
Lr_bp9_%=:
buffer_load_dword v8,v6,s[36:39],s91 offen offset:0 sc1
s_waitcnt vmcnt(0)
v_cmp_eq_u32 vcc,s48,v8
s_nop 0
s_cmp_eq_u64 vcc,exec
s_cselect_b32 s63,1,s96
s_cmp_lg_u32 s63,0
s_cbranch_scc1 Lr_bq9_%=
s_add_u32 s74,s74,1
s_cmp_ge_u32 s74,20000
s_cselect_b32 s96,1,0
s_sleep 4
s_branch Lr_bp9_%=
Lr_bq9_%=:
s_add_u32 s63,s87,71303168
buffer_load_dwordx4 v[32:35],v11,s[36:39],s63 offen offset:0 sc1
buffer_load_dwordx4 v[36:39],v11,s[36:39],s63 offen offset:1024 sc1
buffer_load_dwordx4 v[40:43],v11,s[36:39],s63 offen offset:2048 sc1
buffer_load_dwordx4 v[44:47],v11,s[36:39],s63 offen offset:3072 sc1
s_add_u32 s63,s87,89128960
buffer_load_dwordx4 v[48:51],v11,s[36:39],s63 offen offset:0 sc1
buffer_load_dwordx4 v[52:55],v11,s[36:39],s63 offen offset:1024 sc1
buffer_load_dwordx4 v[56:59],v11,s[36:39],s63 offen offset:2048 sc1
buffer_load_dwordx4 v[60:63],v11,s[36:39],s63 offen offset:3072 sc1
s_add_u32 s63,s87,8388608
buffer_load_dwordx4 v[64:67],v11,s[36:39],s63 offen offset:0 sc1
buffer_load_dwordx4 v[68:71],v11,s[36:39],s63 offen offset:1024 sc1
buffer_load_dwordx4 v[72:75],v11,s[36:39],s63 offen offset:2048 sc1
buffer_load_dwordx4 v[76:79],v11,s[36:39],s63 offen offset:3072 sc1
buffer_load_dword v8,v6,s[36:39],s69 offen offset:0 sc1
s_waitcnt vmcnt(0)
Lr_it0_%=:
s_mov_b32 s88,s58
s_mov_b32 s89,s59
s_mov_b32 s90,s49
s_add_u32 s54,s54,s50
s_lshr_b32 s63,s54,1
s_and_b32 s64,s54,1
s_lshl_b32 s64,s64,7
s_lshl_b32 s87,s54,14
s_lshl_b32 s58,s63,15
s_add_u32 s58,s58,s64
s_add_u32 s58,s58,16908288
s_lshl_b32 s59,s63,9
s_add_u32 s59,s59,s64
s_add_u32 s59,s59,107022336
s_lshl_b32 s49,s54,9
s_add_u32 s49,s49,s92
s_lshl_b32 s91,s63,11
s_add_u32 s91,s91,109871104
s_lshl_b32 s69,s50,10
s_add_u32 s69,s69,s91
v_cmp_eq_u32 vcc,s48,v8
s_nop 0
s_cmp_eq_u64 vcc,exec
s_cselect_b32 s63,s54,256
s_cmp_lt_u32 s63,256
s_cbranch_scc1 Lr_ta0_%=
v_mfma_f32_16x16x32_bf16 v[128:131],v[20:23],v[32:35],0
v_mfma_f32_16x16x32_bf16 v[132:135],v[24:27],v[32:35],0
v_mfma_f32_16x16x32_bf16 v[128:131],v[20:23],v[48:51],v[128:131]
v_mfma_f32_16x16x32_bf16 v[132:135],v[24:27],v[48:51],v[132:135]
v_mfma_f32_16x16x32_bf16 v[136:139],v[20:23],v[36:39],0
v_mfma_f32_16x16x32_bf16 v[140:143],v[24:27],v[36:39],0
v_mfma_f32_16x16x32_bf16 v[136:139],v[20:23],v[52:55],v[136:139]
s_nop 3
v_cvt_pk_bf16_f32 v208,v128,v129
v_cvt_pk_bf16_f32 v209,v130,v131
v_mfma_f32_16x16x32_bf16 v[140:143],v[24:27],v[52:55],v[140:143]
v_cvt_pk_bf16_f32 v210,v132,v133
v_cvt_pk_bf16_f32 v211,v134,v135
v_mfma_f32_16x16x32_bf16 v[144:147],v[20:23],v[40:43],0
v_mfma_f32_16x16x32_bf16 v[148:151],v[24:27],v[40:43],0
v_mfma_f32_16x16x32_bf16 v[144:147],v[20:23],v[56:59],v[144:147]
v_cvt_pk_bf16_f32 v212,v136,v137
v_cvt_pk_bf16_f32 v213,v138,v139
v_mfma_f32_16x16x32_bf16 v[148:151],v[24:27],v[56:59],v[148:151]
v_cvt_pk_bf16_f32 v214,v140,v141
v_cvt_pk_bf16_f32 v215,v142,v143
v_mfma_f32_16x16x32_bf16 v[28:31],v[192:195],v[208:211],0
v_mfma_f32_16x16x32_bf16 v[204:207],v[208:211],v[208:211],0
v_mfma_f32_16x16x32_bf16 v[152:155],v[20:23],v[44:47],0
v_mfma_f32_16x16x32_bf16 v[156:159],v[24:27],v[44:47],0
v_mfma_f32_16x16x32_bf16 v[152:155],v[20:23],v[60:63],v[152:155]
v_cvt_pk_bf16_f32 v216,v144,v145
v_cvt_pk_bf16_f32 v217,v146,v147
v_mfma_f32_16x16x32_bf16 v[156:159],v[24:27],v[60:63],v[156:159]
v_cvt_pk_bf16_f32 v218,v148,v149
v_cvt_pk_bf16_f32 v219,v150,v151
v_mfma_f32_16x16x32_bf16 v[28:31],v[192:195],v[212:215],v[28:31]
v_mfma_f32_16x16x32_bf16 v[204:207],v[212:215],v[212:215],v[204:207]
v_mfma_f32_16x16x32_bf16 v[28:31],v[192:195],v[216:219],v[28:31]
v_mfma_f32_16x16x32_bf16 v[204:207],v[216:219],v[216:219],v[204:207]
v_mfma_f32_16x16x32_bf16 v[160:163],v[20:23],v[64:67],0
v_cvt_pk_bf16_f32 v220,v152,v153
v_cvt_pk_bf16_f32 v221,v154,v155
v_mfma_f32_16x16x32_bf16 v[164:167],v[24:27],v[64:67],0
v_cvt_pk_bf16_f32 v222,v156,v157
v_cvt_pk_bf16_f32 v223,v158,v159
v_mfma_f32_16x16x32_bf16 v[168:171],v[20:23],v[68:71],0
s_nop 0
v_mfma_f32_16x16x32_bf16 v[28:31],v[192:195],v[220:223],v[28:31]
v_mfma_f32_16x16x32_bf16 v[204:207],v[220:223],v[220:223],v[204:207]
v_mfma_f32_16x16x32_bf16 v[172:175],v[24:27],v[68:71],0
v_mfma_f32_16x16x32_bf16 v[176:179],v[20:23],v[72:75],0
v_mfma_f32_16x16x32_bf16 v[180:183],v[24:27],v[72:75],0
s_nop 3
v_add_f32_dpp v224,v28,v28 row_ror:8 row_mask:0xf bank_mask:0xf
v_cndmask_b32 v225,v204,v205,s[46:47]
v_cndmask_b32 v226,v206,v207,s[46:47]
v_cndmask_b32 v225,v225,v226,s[56:57]
v_mfma_f32_16x16x32_bf16 v[184:187],v[20:23],v[76:79],0
ds_write_b32 v2,v224 offset:0
ds_write_b32 v0,v225 offset:0
v_mfma_f32_16x16x32_bf16 v[188:191],v[24:27],v[76:79],0
s_waitcnt lgkmcnt(0)
s_barrier
ds_read_b128 v[196:199],v3 offset:0
ds_read_b128 v[200:203],v3 offset:16
ds_read_b128 v[204:207],v3 offset:32
s_waitcnt lgkmcnt(0)
v_add_f32 v196,v196,v197
v_add_f32 v198,v198,v199
v_add_f32 v200,v200,v204
v_add_f32 v201,v201,v205
v_add_f32 v202,v202,v206
v_add_f32 v203,v203,v207
v_add_f32 v196,v196,v198
v_add_f32 v200,v200,v201)"
R"(
v_add_f32 v202,v202,v203
v_mul_f32 v196,s76,v196
v_add_f32 v197,v200,v202
v_mul_f32 v197,s76,v197
v_fma_f32 v197,-v196,v196,v197
v_max_f32 v197,0,v197
v_add_f32 v197,s77,v197
v_rsq_f32 v228,v197
s_nop 0
v_mul_f32_e64 v230,-v196,v228
v_mov_b32 v229,v228
v_mov_b32 v231,v230
v_pk_fma_f32 v[196:197],v[128:129],v[228:229],v[230:231]
v_pk_fma_f32 v[198:199],v[130:131],v[228:229],v[230:231]
v_pk_fma_f32 v[200:201],v[132:133],v[228:229],v[230:231]
v_pk_fma_f32 v[202:203],v[134:135],v[228:229],v[230:231]
v_pk_fma_f32 v[128:129],v[196:197],v[160:161],v[128:129]
v_pk_fma_f32 v[130:131],v[198:199],v[162:163],v[130:131]
v_pk_fma_f32 v[132:133],v[200:201],v[164:165],v[132:133]
v_pk_fma_f32 v[134:135],v[202:203],v[166:167],v[134:135]
v_pk_fma_f32 v[196:197],v[136:137],v[228:229],v[230:231]
v_pk_fma_f32 v[198:199],v[138:139],v[228:229],v[230:231]
v_pk_fma_f32 v[200:201],v[140:141],v[228:229],v[230:231]
v_pk_fma_f32 v[202:203],v[142:143],v[228:229],v[230:231]
v_pk_fma_f32 v[136:137],v[196:197],v[168:169],v[136:137]
v_pk_fma_f32 v[138:139],v[198:199],v[170:171],v[138:139]
v_pk_fma_f32 v[140:141],v[200:201],v[172:173],v[140:141]
v_pk_fma_f32 v[142:143],v[202:203],v[174:175],v[142:143]
v_pk_fma_f32 v[196:197],v[144:145],v[228:229],v[230:231]
v_pk_fma_f32 v[198:199],v[146:147],v[228:229],v[230:231]
v_pk_fma_f32 v[200:201],v[148:149],v[228:229],v[230:231]
v_pk_fma_f32 v[202:203],v[150:151],v[228:229],v[230:231]
v_pk_fma_f32 v[144:145],v[196:197],v[176:177],v[144:145]
v_pk_fma_f32 v[146:147],v[198:199],v[178:179],v[146:147]
v_pk_fma_f32 v[148:149],v[200:201],v[180:181],v[148:149]
v_pk_fma_f32 v[150:151],v[202:203],v[182:183],v[150:151]
v_pk_fma_f32 v[196:197],v[152:153],v[228:229],v[230:231]
v_pk_fma_f32 v[198:199],v[154:155],v[228:229],v[230:231]
v_pk_fma_f32 v[200:201],v[156:157],v[228:229],v[230:231]
v_pk_fma_f32 v[202:203],v[158:159],v[228:229],v[230:231]
v_pk_fma_f32 v[152:153],v[196:197],v[184:185],v[152:153]
v_pk_fma_f32 v[154:155],v[198:199],v[186:187],v[154:155]
v_pk_fma_f32 v[156:157],v[200:201],v[188:189],v[156:157]
v_pk_fma_f32 v[158:159],v[202:203],v[190:191],v[158:159]
v_cvt_pk_bf16_f32 v208,v128,v129
v_cvt_pk_bf16_f32 v209,v130,v131
v_cvt_pk_bf16_f32 v210,v132,v133
v_cvt_pk_bf16_f32 v211,v134,v135
v_cvt_pk_bf16_f32 v212,v136,v137
v_cvt_pk_bf16_f32 v213,v138,v139
v_cvt_pk_bf16_f32 v214,v140,v141
v_cvt_pk_bf16_f32 v215,v142,v143
v_mfma_f32_16x16x32_bf16 v[28:31],v[192:195],v[208:211],0
v_cvt_pk_bf16_f32 v216,v144,v145
v_cvt_pk_bf16_f32 v217,v146,v147
v_mfma_f32_16x16x32_bf16 v[204:207],v[208:211],v[208:211],0
v_cvt_pk_bf16_f32 v218,v148,v149
v_cvt_pk_bf16_f32 v219,v150,v151
v_mfma_f32_16x16x32_bf16 v[28:31],v[192:195],v[212:215],v[28:31]
v_cvt_pk_bf16_f32 v220,v152,v153
v_cvt_pk_bf16_f32 v221,v154,v155
v_mfma_f32_16x16x32_bf16 v[204:207],v[212:215],v[212:215],v[204:207]
v_cvt_pk_bf16_f32 v222,v156,v157
v_cvt_pk_bf16_f32 v223,v158,v159
v_mfma_f32_16x16x32_bf16 v[28:31],v[192:195],v[216:219],v[28:31]
v_mfma_f32_16x16x32_bf16 v[204:207],v[216:219],v[216:219],v[204:207]
v_mfma_f32_16x16x32_bf16 v[28:31],v[192:195],v[220:223],v[28:31]
v_mfma_f32_16x16x32_bf16 v[204:207],v[220:223],v[220:223],v[204:207]
s_waitcnt vmcnt(0)
buffer_store_dword v7,v5,s[36:39],s60 offen offset:0 sc0 sc1
s_mov_b32 s60,536870912
buffer_load_dword v8,v6,s[36:39],s91 offen offset:0 sc1
buffer_store_dwordx4 v[208:211],v1,s[36:39],s88 offen offset:0 sc0 sc1
buffer_store_dwordx4 v[212:215],v1,s[36:39],s88 offen offset:2048 sc0 sc1
s_add_u32 s88,s88,4096
v_add_f32_dpp v224,v28,v28 row_ror:8 row_mask:0xf bank_mask:0xf
v_cndmask_b32 v225,v204,v205,s[46:47]
v_cndmask_b32 v226,v206,v207,s[46:47]
v_cndmask_b32 v225,v225,v226,s[56:57]
s_and_b32 s63,s89,128
ds_write_b32 v2,v224 offset:1024
ds_write_b32 v0,v225 offset:1024
buffer_store_dwordx4 v[216:219],v1,s[36:39],s88 offen offset:0 sc0 sc1
buffer_store_dwordx4 v[220:223],v1,s[36:39],s88 offen offset:2048 sc0 sc1
s_waitcnt lgkmcnt(0)
s_barrier
ds_read_b128 v[196:199],v3 offset:1024
ds_read_b128 v[200:203],v3 offset:1040
ds_read_b128 v[204:207],v3 offset:1056
s_waitcnt lgkmcnt(0)
v_add_f32 v196,v196,v197
v_add_f32 v198,v198,v199
v_add_f32 v200,v200,v204
v_add_f32 v201,v201,v205
v_add_f32 v202,v202,v206
v_add_f32 v203,v203,v207
v_add_f32 v196,v196,v198
v_add_f32 v200,v200,v201
v_add_f32 v202,v202,v203
v_mul_f32 v196,s76,v196
v_add_f32 v197,v200,v202
v_mul_f32 v197,s76,v197
v_fma_f32 v197,-v196,v196,v197
v_max_f32 v197,0,v197
v_add_f32 v198,s77,v197
v_fma_f32 v199,s77,v198,v197
v_rsq_f32 v205,v198
v_rsq_f32 v198,v199
v_mov_b32 v200,v196
v_mul_f32_e64 v204,-v196,v205
v_mul_f32 v203,v199,v198
v_mul_f32 v201,s78,v198
v_mul_f32 v199,v198,v198
v_mul_f32 v202,s79,v199
v_mul_f32 v202,v202,v201
s_lshr_b32 s63,s63,1
s_sub_u32 s63,s89,s63
buffer_store_dwordx4 v[200:203],v4,s[36:39],s89 offen offset:0 sc0 sc1
buffer_store_dwordx2 v[204:205],v10,s[36:39],s63 offen offset:256 sc0 sc1
s_mov_b32 s60,s90
s_waitcnt vmcnt(6)
v_cmp_eq_u32 vcc,s48,v8
s_nop 0
s_cmp_eq_u64 vcc,exec
s_cselect_b32 s63,s54,256
s_cmp_lt_u32 s63,256
s_cbranch_scc1 Lr_tc0_%=
buffer_load_dword v8,v6,s[36:39],s91 offen offset:0 sc1
s_waitcnt vmcnt(0)
buffer_store_dword v7,v5,s[36:39],s60 offen offset:0 sc0 sc1
s_mov_b32 s60,536870912
v_cmp_eq_u32 vcc,s48,v8
s_nop 0
s_cmp_eq_u64 vcc,exec
s_cselect_b32 s63,s54,256
s_cmp_lt_u32 s63,256
s_cbranch_scc1 Lr_bq0_%=
s_cmp_ge_u32 s54,256
s_cbranch_scc1 Lr_done_%=
Lr_bp0_%=:
buffer_load_dword v8,v6,s[36:39],s91 offen offset:0 sc1
s_waitcnt vmcnt(0)
v_cmp_eq_u32 vcc,s48,v8
s_nop 0
s_cmp_eq_u64 vcc,exec
s_cselect_b32 s63,1,s96
s_cmp_lg_u32 s63,0
s_cbranch_scc1 Lr_bq0_%=
s_add_u32 s74,s74,1
s_cmp_ge_u32 s74,20000
s_cselect_b32 s96,1,0
s_sleep 4
s_branch Lr_bp0_%=
Lr_bq0_%=:
s_add_u32 s63,s87,71303168
buffer_load_dwordx4 v[80:83],v11,s[36:39],s63 offen offset:0 sc1
buffer_load_dwordx4 v[84:87],v11,s[36:39],s63 offen offset:1024 sc1
buffer_load_dwordx4 v[88:91],v11,s[36:39],s63 offen offset:2048 sc1
buffer_load_dwordx4 v[92:95],v11,s[36:39],s63 offen offset:3072 sc1
s_add_u32 s63,s87,89128960
buffer_load_dwordx4 v[96:99],v11,s[36:39],s63 offen offset:0 sc1
buffer_load_dwordx4 v[100:103],v11,s[36:39],s63 offen offset:1024 sc1
buffer_load_dwordx4 v[104:107],v11,s[36:39],s63 offen offset:2048 sc1
buffer_load_dwordx4 v[108:111],v11,s[36:39],s63 offen offset:3072 sc1
s_add_u32 s63,s87,8388608
buffer_load_dwordx4 v[112:115],v11,s[36:39],s63 offen offset:0 sc1
buffer_load_dwordx4 v[116:119],v11,s[36:39],s63 offen offset:1024 sc1
buffer_load_dwordx4 v[120:123],v11,s[36:39],s63 offen offset:2048 sc1
buffer_load_dwordx4 v[124:127],v11,s[36:39],s63 offen offset:3072 sc1
buffer_load_dword v8,v6,s[36:39],s69 offen offset:0 sc1
s_waitcnt vmcnt(0)
s_branch Lr_it1_%=
Lr_ta0_%=:
v_mfma_f32_16x16x32_bf16 v[128:131],v[20:23],v[32:35],0
s_add_u32 s63,s87,71303168
v_mfma_f32_16x16x32_bf16 v[132:135],v[24:27],v[32:35],0
buffer_load_dwordx4 v[80:83],v11,s[36:39],s63 offen offset:0 sc1
v_mfma_f32_16x16x32_bf16 v[128:131],v[20:23],v[48:51],v[128:131]
buffer_load_dwordx4 v[84:87],v11,s[36:39],s63 offen offset:1024 sc1
v_mfma_f32_16x16x32_bf16 v[132:135],v[24:27],v[48:51],v[132:135]
buffer_load_dwordx4 v[88:91],v11,s[36:39],s63 offen offset:2048 sc1
v_mfma_f32_16x16x32_bf16 v[136:139],v[20:23],v[36:39],0
buffer_load_dwordx4 v[92:95],v11,s[36:39],s63 offen offset:3072 sc1
v_mfma_f32_16x16x32_bf16 v[140:143],v[24:27],v[36:39],0
s_add_u32 s63,s87,89128960
v_mfma_f32_16x16x32_bf16 v[136:139],v[20:23],v[52:55],v[136:139]
v_cvt_pk_bf16_f32 v208,v128,v129
v_cvt_pk_bf16_f32 v209,v130,v131
v_mfma_f32_16x16x32_bf16 v[140:143],v[24:27],v[52:55],v[140:143]
v_cvt_pk_bf16_f32 v210,v132,v133
v_cvt_pk_bf16_f32 v211,v134,v135
v_mfma_f32_16x16x32_bf16 v[144:147],v[20:23],v[40:43],0
buffer_load_dwordx4 v[96:99],v11,s[36:39],s63 offen offset:0 sc1
v_mfma_f32_16x16x32_bf16 v[148:151],v[24:27],v[40:43],0
buffer_load_dwordx4 v[100:103],v11,s[36:39],s63 offen offset:1024 sc1
v_mfma_f32_16x16x32_bf16 v[144:147],v[20:23],v[56:59],v[144:147]
v_cvt_pk_bf16_f32 v212,v136,v137
v_cvt_pk_bf16_f32 v213,v138,v139
v_mfma_f32_16x16x32_bf16 v[148:151],v[24:27],v[56:59],v[148:151]
v_cvt_pk_bf16_f32 v214,v140,v141)"
R"(
v_cvt_pk_bf16_f32 v215,v142,v143
v_mfma_f32_16x16x32_bf16 v[28:31],v[192:195],v[208:211],0
buffer_load_dwordx4 v[104:107],v11,s[36:39],s63 offen offset:2048 sc1
v_mfma_f32_16x16x32_bf16 v[204:207],v[208:211],v[208:211],0
buffer_load_dwordx4 v[108:111],v11,s[36:39],s63 offen offset:3072 sc1
v_mfma_f32_16x16x32_bf16 v[152:155],v[20:23],v[44:47],0
s_add_u32 s63,s87,8388608
v_mfma_f32_16x16x32_bf16 v[156:159],v[24:27],v[44:47],0
buffer_load_dwordx4 v[112:115],v11,s[36:39],s63 offen offset:0 sc1
v_mfma_f32_16x16x32_bf16 v[152:155],v[20:23],v[60:63],v[152:155]
v_cvt_pk_bf16_f32 v216,v144,v145
v_cvt_pk_bf16_f32 v217,v146,v147
v_mfma_f32_16x16x32_bf16 v[156:159],v[24:27],v[60:63],v[156:159]
v_cvt_pk_bf16_f32 v218,v148,v149
v_cvt_pk_bf16_f32 v219,v150,v151
v_mfma_f32_16x16x32_bf16 v[28:31],v[192:195],v[212:215],v[28:31]
buffer_load_dwordx4 v[116:119],v11,s[36:39],s63 offen offset:1024 sc1
v_mfma_f32_16x16x32_bf16 v[204:207],v[212:215],v[212:215],v[204:207]
buffer_load_dwordx4 v[120:123],v11,s[36:39],s63 offen offset:2048 sc1
v_mfma_f32_16x16x32_bf16 v[28:31],v[192:195],v[216:219],v[28:31]
buffer_load_dwordx4 v[124:127],v11,s[36:39],s63 offen offset:3072 sc1
v_mfma_f32_16x16x32_bf16 v[204:207],v[216:219],v[216:219],v[204:207]
buffer_load_dword v8,v6,s[36:39],s69 offen offset:0 sc1
v_mfma_f32_16x16x32_bf16 v[160:163],v[20:23],v[64:67],0
v_cvt_pk_bf16_f32 v220,v152,v153
v_cvt_pk_bf16_f32 v221,v154,v155
v_mfma_f32_16x16x32_bf16 v[164:167],v[24:27],v[64:67],0
v_cvt_pk_bf16_f32 v222,v156,v157
v_cvt_pk_bf16_f32 v223,v158,v159
v_mfma_f32_16x16x32_bf16 v[168:171],v[20:23],v[68:71],0
s_nop 0
v_mfma_f32_16x16x32_bf16 v[28:31],v[192:195],v[220:223],v[28:31]
v_mfma_f32_16x16x32_bf16 v[204:207],v[220:223],v[220:223],v[204:207]
v_mfma_f32_16x16x32_bf16 v[172:175],v[24:27],v[68:71],0
v_mfma_f32_16x16x32_bf16 v[176:179],v[20:23],v[72:75],0
v_mfma_f32_16x16x32_bf16 v[180:183],v[24:27],v[72:75],0
s_nop 3
v_add_f32_dpp v224,v28,v28 row_ror:8 row_mask:0xf bank_mask:0xf
v_cndmask_b32 v225,v204,v205,s[46:47]
v_cndmask_b32 v226,v206,v207,s[46:47]
v_cndmask_b32 v225,v225,v226,s[56:57]
v_mfma_f32_16x16x32_bf16 v[184:187],v[20:23],v[76:79],0
ds_write_b32 v2,v224 offset:0
ds_write_b32 v0,v225 offset:0
v_mfma_f32_16x16x32_bf16 v[188:191],v[24:27],v[76:79],0
s_waitcnt lgkmcnt(0)
s_barrier
ds_read_b128 v[196:199],v3 offset:0
ds_read_b128 v[200:203],v3 offset:16
ds_read_b128 v[204:207],v3 offset:32
s_waitcnt lgkmcnt(0)
v_add_f32 v196,v196,v197
v_add_f32 v198,v198,v199
v_add_f32 v200,v200,v204
v_add_f32 v201,v201,v205
v_add_f32 v202,v202,v206
v_add_f32 v203,v203,v207
v_add_f32 v196,v196,v198
v_add_f32 v200,v200,v201
v_add_f32 v202,v202,v203
v_mul_f32 v196,s76,v196
v_add_f32 v197,v200,v202
v_mul_f32 v197,s76,v197
v_fma_f32 v197,-v196,v196,v197
v_max_f32 v197,0,v197
v_add_f32 v197,s77,v197
v_rsq_f32 v228,v197
s_nop 0
v_mul_f32_e64 v230,-v196,v228
v_mov_b32 v229,v228
v_mov_b32 v231,v230
v_pk_fma_f32 v[196:197],v[128:129],v[228:229],v[230:231]
v_pk_fma_f32 v[198:199],v[130:131],v[228:229],v[230:231]
v_pk_fma_f32 v[200:201],v[132:133],v[228:229],v[230:231]
v_pk_fma_f32 v[202:203],v[134:135],v[228:229],v[230:231]
v_pk_fma_f32 v[128:129],v[196:197],v[160:161],v[128:129]
v_pk_fma_f32 v[130:131],v[198:199],v[162:163],v[130:131]
v_pk_fma_f32 v[132:133],v[200:201],v[164:165],v[132:133]
v_pk_fma_f32 v[134:135],v[202:203],v[166:167],v[134:135]
v_pk_fma_f32 v[196:197],v[136:137],v[228:229],v[230:231]
v_pk_fma_f32 v[198:199],v[138:139],v[228:229],v[230:231]
v_pk_fma_f32 v[200:201],v[140:141],v[228:229],v[230:231]
v_pk_fma_f32 v[202:203],v[142:143],v[228:229],v[230:231]
v_pk_fma_f32 v[136:137],v[196:197],v[168:169],v[136:137]
v_pk_fma_f32 v[138:139],v[198:199],v[170:171],v[138:139]
v_pk_fma_f32 v[140:141],v[200:201],v[172:173],v[140:141]
v_pk_fma_f32 v[142:143],v[202:203],v[174:175],v[142:143]
v_pk_fma_f32 v[196:197],v[144:145],v[228:229],v[230:231]
v_pk_fma_f32 v[198:199],v[146:147],v[228:229],v[230:231]
v_pk_fma_f32 v[200:201],v[148:149],v[228:229],v[230:231]
v_pk_fma_f32 v[202:203],v[150:151],v[228:229],v[230:231]
v_pk_fma_f32 v[144:145],v[196:197],v[176:177],v[144:145]
v_pk_fma_f32 v[146:147],v[198:199],v[178:179],v[146:147]
v_pk_fma_f32 v[148:149],v[200:201],v[180:181],v[148:149]
v_pk_fma_f32 v[150:151],v[202:203],v[182:183],v[150:151]
v_pk_fma_f32 v[196:197],v[152:153],v[228:229],v[230:231]
v_pk_fma_f32 v[198:199],v[154:155],v[228:229],v[230:231]
v_pk_fma_f32 v[200:201],v[156:157],v[228:229],v[230:231]
v_pk_fma_f32 v[202:203],v[158:159],v[228:229],v[230:231]
v_pk_fma_f32 v[152:153],v[196:197],v[184:185],v[152:153]
v_pk_fma_f32 v[154:155],v[198:199],v[186:187],v[154:155]
v_pk_fma_f32 v[156:157],v[200:201],v[188:189],v[156:157]
v_pk_fma_f32 v[158:159],v[202:203],v[190:191],v[158:159]
v_cvt_pk_bf16_f32 v208,v128,v129
v_cvt_pk_bf16_f32 v209,v130,v131
v_cvt_pk_bf16_f32 v210,v132,v133
v_cvt_pk_bf16_f32 v211,v134,v135
v_cvt_pk_bf16_f32 v212,v136,v137
v_cvt_pk_bf16_f32 v213,v138,v139
v_cvt_pk_bf16_f32 v214,v140,v141
v_cvt_pk_bf16_f32 v215,v142,v143
v_mfma_f32_16x16x32_bf16 v[28:31],v[192:195],v[208:211],0
v_cvt_pk_bf16_f32 v216,v144,v145
v_cvt_pk_bf16_f32 v217,v146,v147
v_mfma_f32_16x16x32_bf16 v[204:207],v[208:211],v[208:211],0
v_cvt_pk_bf16_f32 v218,v148,v149
v_cvt_pk_bf16_f32 v219,v150,v151
v_mfma_f32_16x16x32_bf16 v[28:31],v[192:195],v[212:215],v[28:31]
v_cvt_pk_bf16_f32 v220,v152,v153
v_cvt_pk_bf16_f32 v221,v154,v155
v_mfma_f32_16x16x32_bf16 v[204:207],v[212:215],v[212:215],v[204:207]
v_cvt_pk_bf16_f32 v222,v156,v157
v_cvt_pk_bf16_f32 v223,v158,v159
v_mfma_f32_16x16x32_bf16 v[28:31],v[192:195],v[216:219],v[28:31]
v_mfma_f32_16x16x32_bf16 v[204:207],v[216:219],v[216:219],v[204:207]
v_mfma_f32_16x16x32_bf16 v[28:31],v[192:195],v[220:223],v[28:31]
v_mfma_f32_16x16x32_bf16 v[204:207],v[220:223],v[220:223],v[204:207]
s_waitcnt vmcnt(13)
buffer_store_dword v7,v5,s[36:39],s60 offen offset:0 sc0 sc1
s_mov_b32 s60,536870912
buffer_store_dwordx4 v[208:211],v1,s[36:39],s88 offen offset:0 sc0 sc1
buffer_store_dwordx4 v[212:215],v1,s[36:39],s88 offen offset:2048 sc0 sc1
s_add_u32 s88,s88,4096
s_nop 0
v_add_f32_dpp v224,v28,v28 row_ror:8 row_mask:0xf bank_mask:0xf
v_cndmask_b32 v225,v204,v205,s[46:47]
v_cndmask_b32 v226,v206,v207,s[46:47]
v_cndmask_b32 v225,v225,v226,s[56:57]
s_and_b32 s63,s89,128
ds_write_b32 v2,v224 offset:1024
ds_write_b32 v0,v225 offset:1024
buffer_store_dwordx4 v[216:219],v1,s[36:39],s88 offen offset:0 sc0 sc1
buffer_store_dwordx4 v[220:223],v1,s[36:39],s88 offen offset:2048 sc0 sc1
s_waitcnt lgkmcnt(0)
s_barrier
ds_read_b128 v[196:199],v3 offset:1024
ds_read_b128 v[200:203],v3 offset:1040
ds_read_b128 v[204:207],v3 offset:1056
s_waitcnt lgkmcnt(0)
v_add_f32 v196,v196,v197
v_add_f32 v198,v198,v199
v_add_f32 v200,v200,v204
v_add_f32 v201,v201,v205
v_add_f32 v202,v202,v206
v_add_f32 v203,v203,v207
v_add_f32 v196,v196,v198
v_add_f32 v200,v200,v201
v_add_f32 v202,v202,v203
v_mul_f32 v196,s76,v196
v_add_f32 v197,v200,v202
v_mul_f32 v197,s76,v197
v_fma_f32 v197,-v196,v196,v197
v_max_f32 v197,0,v197
v_add_f32 v198,s77,v197
v_fma_f32 v199,s77,v198,v197
v_rsq_f32 v205,v198
v_rsq_f32 v198,v199
v_mov_b32 v200,v196
v_mul_f32_e64 v204,-v196,v205
v_mul_f32 v203,v199,v198
v_mul_f32 v201,s78,v198
v_mul_f32 v199,v198,v198
v_mul_f32 v202,s79,v199
v_mul_f32 v202,v202,v201
s_lshr_b32 s63,s63,1
s_sub_u32 s63,s89,s63
buffer_store_dwordx4 v[200:203],v4,s[36:39],s89 offen offset:0 sc0 sc1
buffer_store_dwordx2 v[204:205],v10,s[36:39],s63 offen offset:256 sc0 sc1
s_mov_b32 s60,s90
s_waitcnt vmcnt(7)
s_branch Lr_it1_%=
Lr_tc0_%=:
s_add_u32 s63,s87,71303168
buffer_load_dwordx4 v[80:83],v11,s[36:39],s63 offen offset:0 sc1
buffer_load_dwordx4 v[84:87],v11,s[36:39],s63 offen offset:1024 sc1
buffer_load_dwordx4 v[88:91],v11,s[36:39],s63 offen offset:2048 sc1
buffer_load_dwordx4 v[92:95],v11,s[36:39],s63 offen offset:3072 sc1
s_add_u32 s63,s87,89128960
buffer_load_dwordx4 v[96:99],v11,s[36:39],s63 offen offset:0 sc1
buffer_load_dwordx4 v[100:103],v11,s[36:39],s63 offen offset:1024 sc1
buffer_load_dwordx4 v[104:107],v11,s[36:39],s63 offen offset:2048 sc1
buffer_load_dwordx4 v[108:111],v11,s[36:39],s63 offen offset:3072 sc1
s_add_u32 s63,s87,8388608
buffer_load_dwordx4 v[112:115],v11,s[36:39],s63 offen offset:0 sc1
buffer_load_dwordx4 v[116:119],v11,s[36:39],s63 offen offset:1024 sc1
buffer_load_dwordx4 v[120:123],v11,s[36:39],s63 offen offset:2048 sc1
buffer_load_dwordx4 v[124:127],v11,s[36:39],s63 offen offset:3072 sc1
buffer_load_dword v8,v6,s[36:39],s69 offen offset:0 sc1
s_waitcnt vmcnt(13)
buffer_store_dword v7,v5,s[36:39],s60 offen offset:0 sc0 sc1)"
R"(
s_mov_b32 s60,536870912
s_waitcnt vmcnt(1)
s_branch Lr_it1_%=
Lr_it1_%=:
s_mov_b32 s88,s58
s_mov_b32 s89,s59
s_mov_b32 s90,s49
s_add_u32 s54,s54,s50
s_lshr_b32 s63,s54,1
s_and_b32 s64,s54,1
s_lshl_b32 s64,s64,7
s_lshl_b32 s87,s54,14
s_lshl_b32 s58,s63,15
s_add_u32 s58,s58,s64
s_add_u32 s58,s58,16908288
s_lshl_b32 s59,s63,9
s_add_u32 s59,s59,s64
s_add_u32 s59,s59,107022336
s_lshl_b32 s49,s54,9
s_add_u32 s49,s49,s92
s_lshl_b32 s91,s63,11
s_add_u32 s91,s91,109871104
s_lshl_b32 s69,s50,10
s_add_u32 s69,s69,s91
v_cmp_eq_u32 vcc,s48,v8
s_nop 0
s_cmp_eq_u64 vcc,exec
s_cselect_b32 s63,s54,256
s_cmp_lt_u32 s63,256
s_cbranch_scc1 Lr_ta1_%=
v_mfma_f32_16x16x32_bf16 v[128:131],v[20:23],v[80:83],0
v_mfma_f32_16x16x32_bf16 v[132:135],v[24:27],v[80:83],0
v_mfma_f32_16x16x32_bf16 v[128:131],v[20:23],v[96:99],v[128:131]
v_mfma_f32_16x16x32_bf16 v[132:135],v[24:27],v[96:99],v[132:135]
v_mfma_f32_16x16x32_bf16 v[136:139],v[20:23],v[84:87],0
v_mfma_f32_16x16x32_bf16 v[140:143],v[24:27],v[84:87],0
v_mfma_f32_16x16x32_bf16 v[136:139],v[20:23],v[100:103],v[136:139]
s_nop 3
v_cvt_pk_bf16_f32 v208,v128,v129
v_cvt_pk_bf16_f32 v209,v130,v131
v_mfma_f32_16x16x32_bf16 v[140:143],v[24:27],v[100:103],v[140:143]
v_cvt_pk_bf16_f32 v210,v132,v133
v_cvt_pk_bf16_f32 v211,v134,v135
v_mfma_f32_16x16x32_bf16 v[144:147],v[20:23],v[88:91],0
v_mfma_f32_16x16x32_bf16 v[148:151],v[24:27],v[88:91],0
v_mfma_f32_16x16x32_bf16 v[144:147],v[20:23],v[104:107],v[144:147]
v_cvt_pk_bf16_f32 v212,v136,v137
v_cvt_pk_bf16_f32 v213,v138,v139
v_mfma_f32_16x16x32_bf16 v[148:151],v[24:27],v[104:107],v[148:151]
v_cvt_pk_bf16_f32 v214,v140,v141
v_cvt_pk_bf16_f32 v215,v142,v143
v_mfma_f32_16x16x32_bf16 v[28:31],v[192:195],v[208:211],0
v_mfma_f32_16x16x32_bf16 v[204:207],v[208:211],v[208:211],0
v_mfma_f32_16x16x32_bf16 v[152:155],v[20:23],v[92:95],0
v_mfma_f32_16x16x32_bf16 v[156:159],v[24:27],v[92:95],0
v_mfma_f32_16x16x32_bf16 v[152:155],v[20:23],v[108:111],v[152:155]
v_cvt_pk_bf16_f32 v216,v144,v145
v_cvt_pk_bf16_f32 v217,v146,v147
v_mfma_f32_16x16x32_bf16 v[156:159],v[24:27],v[108:111],v[156:159]
v_cvt_pk_bf16_f32 v218,v148,v149
v_cvt_pk_bf16_f32 v219,v150,v151
v_mfma_f32_16x16x32_bf16 v[28:31],v[192:195],v[212:215],v[28:31]
v_mfma_f32_16x16x32_bf16 v[204:207],v[212:215],v[212:215],v[204:207]
v_mfma_f32_16x16x32_bf16 v[28:31],v[192:195],v[216:219],v[28:31]
v_mfma_f32_16x16x32_bf16 v[204:207],v[216:219],v[216:219],v[204:207]
v_mfma_f32_16x16x32_bf16 v[160:163],v[20:23],v[112:115],0
v_cvt_pk_bf16_f32 v220,v152,v153
v_cvt_pk_bf16_f32 v221,v154,v155
v_mfma_f32_16x16x32_bf16 v[164:167],v[24:27],v[112:115],0
v_cvt_pk_bf16_f32 v222,v156,v157
v_cvt_pk_bf16_f32 v223,v158,v159
v_mfma_f32_16x16x32_bf16 v[168:171],v[20:23],v[116:119],0
s_nop 0
v_mfma_f32_16x16x32_bf16 v[28:31],v[192:195],v[220:223],v[28:31]
v_mfma_f32_16x16x32_bf16 v[204:207],v[220:223],v[220:223],v[204:207]
v_mfma_f32_16x16x32_bf16 v[172:175],v[24:27],v[116:119],0
v_mfma_f32_16x16x32_bf16 v[176:179],v[20:23],v[120:123],0
v_mfma_f32_16x16x32_bf16 v[180:183],v[24:27],v[120:123],0
s_nop 3
v_add_f32_dpp v224,v28,v28 row_ror:8 row_mask:0xf bank_mask:0xf
v_cndmask_b32 v225,v204,v205,s[46:47]
v_cndmask_b32 v226,v206,v207,s[46:47]
v_cndmask_b32 v225,v225,v226,s[56:57]
v_mfma_f32_16x16x32_bf16 v[184:187],v[20:23],v[124:127],0
ds_write_b32 v2,v224 offset:0
ds_write_b32 v0,v225 offset:0
v_mfma_f32_16x16x32_bf16 v[188:191],v[24:27],v[124:127],0
s_waitcnt lgkmcnt(0)
s_barrier
ds_read_b128 v[196:199],v3 offset:0
ds_read_b128 v[200:203],v3 offset:16
ds_read_b128 v[204:207],v3 offset:32
s_waitcnt lgkmcnt(0)
v_add_f32 v196,v196,v197
v_add_f32 v198,v198,v199
v_add_f32 v200,v200,v204
v_add_f32 v201,v201,v205
v_add_f32 v202,v202,v206
v_add_f32 v203,v203,v207
v_add_f32 v196,v196,v198
v_add_f32 v200,v200,v201
v_add_f32 v202,v202,v203
v_mul_f32 v196,s76,v196
v_add_f32 v197,v200,v202
v_mul_f32 v197,s76,v197
v_fma_f32 v197,-v196,v196,v197
v_max_f32 v197,0,v197
v_add_f32 v197,s77,v197
v_rsq_f32 v228,v197
s_nop 0
v_mul_f32_e64 v230,-v196,v228
v_mov_b32 v229,v228
v_mov_b32 v231,v230
v_pk_fma_f32 v[196:197],v[128:129],v[228:229],v[230:231]
v_pk_fma_f32 v[198:199],v[130:131],v[228:229],v[230:231]
v_pk_fma_f32 v[200:201],v[132:133],v[228:229],v[230:231]
v_pk_fma_f32 v[202:203],v[134:135],v[228:229],v[230:231]
v_pk_fma_f32 v[128:129],v[196:197],v[160:161],v[128:129]
v_pk_fma_f32 v[130:131],v[198:199],v[162:163],v[130:131]
v_pk_fma_f32 v[132:133],v[200:201],v[164:165],v[132:133]
v_pk_fma_f32 v[134:135],v[202:203],v[166:167],v[134:135]
v_pk_fma_f32 v[196:197],v[136:137],v[228:229],v[230:231]
v_pk_fma_f32 v[198:199],v[138:139],v[228:229],v[230:231]
v_pk_fma_f32 v[200:201],v[140:141],v[228:229],v[230:231]
v_pk_fma_f32 v[202:203],v[142:143],v[228:229],v[230:231]
v_pk_fma_f32 v[136:137],v[196:197],v[168:169],v[136:137]
v_pk_fma_f32 v[138:139],v[198:199],v[170:171],v[138:139]
v_pk_fma_f32 v[140:141],v[200:201],v[172:173],v[140:141]
v_pk_fma_f32 v[142:143],v[202:203],v[174:175],v[142:143]
v_pk_fma_f32 v[196:197],v[144:145],v[228:229],v[230:231]
v_pk_fma_f32 v[198:199],v[146:147],v[228:229],v[230:231]
v_pk_fma_f32 v[200:201],v[148:149],v[228:229],v[230:231]
v_pk_fma_f32 v[202:203],v[150:151],v[228:229],v[230:231]
v_pk_fma_f32 v[144:145],v[196:197],v[176:177],v[144:145]
v_pk_fma_f32 v[146:147],v[198:199],v[178:179],v[146:147]
v_pk_fma_f32 v[148:149],v[200:201],v[180:181],v[148:149]
v_pk_fma_f32 v[150:151],v[202:203],v[182:183],v[150:151]
v_pk_fma_f32 v[196:197],v[152:153],v[228:229],v[230:231]
v_pk_fma_f32 v[198:199],v[154:155],v[228:229],v[230:231]
v_pk_fma_f32 v[200:201],v[156:157],v[228:229],v[230:231]
v_pk_fma_f32 v[202:203],v[158:159],v[228:229],v[230:231]
v_pk_fma_f32 v[152:153],v[196:197],v[184:185],v[152:153]
v_pk_fma_f32 v[154:155],v[198:199],v[186:187],v[154:155]
v_pk_fma_f32 v[156:157],v[200:201],v[188:189],v[156:157]
v_pk_fma_f32 v[158:159],v[202:203],v[190:191],v[158:159]
v_cvt_pk_bf16_f32 v208,v128,v129
v_cvt_pk_bf16_f32 v209,v130,v131
v_cvt_pk_bf16_f32 v210,v132,v133
v_cvt_pk_bf16_f32 v211,v134,v135
v_cvt_pk_bf16_f32 v212,v136,v137
v_cvt_pk_bf16_f32 v213,v138,v139
v_cvt_pk_bf16_f32 v214,v140,v141
v_cvt_pk_bf16_f32 v215,v142,v143
v_mfma_f32_16x16x32_bf16 v[28:31],v[192:195],v[208:211],0
v_cvt_pk_bf16_f32 v216,v144,v145
v_cvt_pk_bf16_f32 v217,v146,v147
v_mfma_f32_16x16x32_bf16 v[204:207],v[208:211],v[208:211],0
v_cvt_pk_bf16_f32 v218,v148,v149
v_cvt_pk_bf16_f32 v219,v150,v151
v_mfma_f32_16x16x32_bf16 v[28:31],v[192:195],v[212:215],v[28:31]
v_cvt_pk_bf16_f32 v220,v152,v153
v_cvt_pk_bf16_f32 v221,v154,v155
v_mfma_f32_16x16x32_bf16 v[204:207],v[212:215],v[212:215],v[204:207]
v_cvt_pk_bf16_f32 v222,v156,v157
v_cvt_pk_bf16_f32 v223,v158,v159
v_mfma_f32_16x16x32_bf16 v[28:31],v[192:195],v[216:219],v[28:31]
v_mfma_f32_16x16x32_bf16 v[204:207],v[216:219],v[216:219],v[204:207]
v_mfma_f32_16x16x32_bf16 v[28:31],v[192:195],v[220:223],v[28:31]
v_mfma_f32_16x16x32_bf16 v[204:207],v[220:223],v[220:223],v[204:207]
s_waitcnt vmcnt(0)
buffer_store_dword v7,v5,s[36:39],s60 offen offset:0 sc0 sc1
s_mov_b32 s60,536870912
buffer_load_dword v8,v6,s[36:39],s91 offen offset:0 sc1
buffer_store_dwordx4 v[208:211],v1,s[36:39],s88 offen offset:0 sc0 sc1
buffer_store_dwordx4 v[212:215],v1,s[36:39],s88 offen offset:2048 sc0 sc1
s_add_u32 s88,s88,4096
v_add_f32_dpp v224,v28,v28 row_ror:8 row_mask:0xf bank_mask:0xf
v_cndmask_b32 v225,v204,v205,s[46:47]
v_cndmask_b32 v226,v206,v207,s[46:47]
v_cndmask_b32 v225,v225,v226,s[56:57]
s_and_b32 s63,s89,128
ds_write_b32 v2,v224 offset:1024
ds_write_b32 v0,v225 offset:1024
buffer_store_dwordx4 v[216:219],v1,s[36:39],s88 offen offset:0 sc0 sc1
buffer_store_dwordx4 v[220:223],v1,s[36:39],s88 offen offset:2048 sc0 sc1
s_waitcnt lgkmcnt(0)
s_barrier
ds_read_b128 v[196:199],v3 offset:1024
ds_read_b128 v[200:203],v3 offset:1040
ds_read_b128 v[204:207],v3 offset:1056
s_waitcnt lgkmcnt(0)
v_add_f32 v196,v196,v197
v_add_f32 v198,v198,v199
v_add_f32 v200,v200,v204
v_add_f32 v201,v201,v205
v_add_f32 v202,v202,v206
v_add_f32 v203,v203,v207
v_add_f32 v196,v196,v198
v_add_f32 v200,v200,v201
v_add_f32 v202,v202,v203)"
R"(
v_mul_f32 v196,s76,v196
v_add_f32 v197,v200,v202
v_mul_f32 v197,s76,v197
v_fma_f32 v197,-v196,v196,v197
v_max_f32 v197,0,v197
v_add_f32 v198,s77,v197
v_fma_f32 v199,s77,v198,v197
v_rsq_f32 v205,v198
v_rsq_f32 v198,v199
v_mov_b32 v200,v196
v_mul_f32_e64 v204,-v196,v205
v_mul_f32 v203,v199,v198
v_mul_f32 v201,s78,v198
v_mul_f32 v199,v198,v198
v_mul_f32 v202,s79,v199
v_mul_f32 v202,v202,v201
s_lshr_b32 s63,s63,1
s_sub_u32 s63,s89,s63
buffer_store_dwordx4 v[200:203],v4,s[36:39],s89 offen offset:0 sc0 sc1
buffer_store_dwordx2 v[204:205],v10,s[36:39],s63 offen offset:256 sc0 sc1
s_mov_b32 s60,s90
s_waitcnt vmcnt(6)
v_cmp_eq_u32 vcc,s48,v8
s_nop 0
s_cmp_eq_u64 vcc,exec
s_cselect_b32 s63,s54,256
s_cmp_lt_u32 s63,256
s_cbranch_scc1 Lr_tc1_%=
buffer_load_dword v8,v6,s[36:39],s91 offen offset:0 sc1
s_waitcnt vmcnt(0)
buffer_store_dword v7,v5,s[36:39],s60 offen offset:0 sc0 sc1
s_mov_b32 s60,536870912
v_cmp_eq_u32 vcc,s48,v8
s_nop 0
s_cmp_eq_u64 vcc,exec
s_cselect_b32 s63,s54,256
s_cmp_lt_u32 s63,256
s_cbranch_scc1 Lr_bq1_%=
s_cmp_ge_u32 s54,256
s_cbranch_scc1 Lr_done_%=
Lr_bp1_%=:
buffer_load_dword v8,v6,s[36:39],s91 offen offset:0 sc1
s_waitcnt vmcnt(0)
v_cmp_eq_u32 vcc,s48,v8
s_nop 0
s_cmp_eq_u64 vcc,exec
s_cselect_b32 s63,1,s96
s_cmp_lg_u32 s63,0
s_cbranch_scc1 Lr_bq1_%=
s_add_u32 s74,s74,1
s_cmp_ge_u32 s74,20000
s_cselect_b32 s96,1,0
s_sleep 4
s_branch Lr_bp1_%=
Lr_bq1_%=:
s_add_u32 s63,s87,71303168
buffer_load_dwordx4 v[32:35],v11,s[36:39],s63 offen offset:0 sc1
buffer_load_dwordx4 v[36:39],v11,s[36:39],s63 offen offset:1024 sc1
buffer_load_dwordx4 v[40:43],v11,s[36:39],s63 offen offset:2048 sc1
buffer_load_dwordx4 v[44:47],v11,s[36:39],s63 offen offset:3072 sc1
s_add_u32 s63,s87,89128960
buffer_load_dwordx4 v[48:51],v11,s[36:39],s63 offen offset:0 sc1
buffer_load_dwordx4 v[52:55],v11,s[36:39],s63 offen offset:1024 sc1
buffer_load_dwordx4 v[56:59],v11,s[36:39],s63 offen offset:2048 sc1
buffer_load_dwordx4 v[60:63],v11,s[36:39],s63 offen offset:3072 sc1
s_add_u32 s63,s87,8388608
buffer_load_dwordx4 v[64:67],v11,s[36:39],s63 offen offset:0 sc1
buffer_load_dwordx4 v[68:71],v11,s[36:39],s63 offen offset:1024 sc1
buffer_load_dwordx4 v[72:75],v11,s[36:39],s63 offen offset:2048 sc1
buffer_load_dwordx4 v[76:79],v11,s[36:39],s63 offen offset:3072 sc1
buffer_load_dword v8,v6,s[36:39],s69 offen offset:0 sc1
s_waitcnt vmcnt(0)
s_branch Lr_it0_%=
Lr_ta1_%=:
v_mfma_f32_16x16x32_bf16 v[128:131],v[20:23],v[80:83],0
s_add_u32 s63,s87,71303168
v_mfma_f32_16x16x32_bf16 v[132:135],v[24:27],v[80:83],0
buffer_load_dwordx4 v[32:35],v11,s[36:39],s63 offen offset:0 sc1
v_mfma_f32_16x16x32_bf16 v[128:131],v[20:23],v[96:99],v[128:131]
buffer_load_dwordx4 v[36:39],v11,s[36:39],s63 offen offset:1024 sc1
v_mfma_f32_16x16x32_bf16 v[132:135],v[24:27],v[96:99],v[132:135]
buffer_load_dwordx4 v[40:43],v11,s[36:39],s63 offen offset:2048 sc1
v_mfma_f32_16x16x32_bf16 v[136:139],v[20:23],v[84:87],0
buffer_load_dwordx4 v[44:47],v11,s[36:39],s63 offen offset:3072 sc1
v_mfma_f32_16x16x32_bf16 v[140:143],v[24:27],v[84:87],0
s_add_u32 s63,s87,89128960
v_mfma_f32_16x16x32_bf16 v[136:139],v[20:23],v[100:103],v[136:139]
v_cvt_pk_bf16_f32 v208,v128,v129
v_cvt_pk_bf16_f32 v209,v130,v131
v_mfma_f32_16x16x32_bf16 v[140:143],v[24:27],v[100:103],v[140:143]
v_cvt_pk_bf16_f32 v210,v132,v133
v_cvt_pk_bf16_f32 v211,v134,v135
v_mfma_f32_16x16x32_bf16 v[144:147],v[20:23],v[88:91],0
buffer_load_dwordx4 v[48:51],v11,s[36:39],s63 offen offset:0 sc1
v_mfma_f32_16x16x32_bf16 v[148:151],v[24:27],v[88:91],0
buffer_load_dwordx4 v[52:55],v11,s[36:39],s63 offen offset:1024 sc1
v_mfma_f32_16x16x32_bf16 v[144:147],v[20:23],v[104:107],v[144:147]
v_cvt_pk_bf16_f32 v212,v136,v137
v_cvt_pk_bf16_f32 v213,v138,v139
v_mfma_f32_16x16x32_bf16 v[148:151],v[24:27],v[104:107],v[148:151]
v_cvt_pk_bf16_f32 v214,v140,v141
v_cvt_pk_bf16_f32 v215,v142,v143
v_mfma_f32_16x16x32_bf16 v[28:31],v[192:195],v[208:211],0
buffer_load_dwordx4 v[56:59],v11,s[36:39],s63 offen offset:2048 sc1
v_mfma_f32_16x16x32_bf16 v[204:207],v[208:211],v[208:211],0
buffer_load_dwordx4 v[60:63],v11,s[36:39],s63 offen offset:3072 sc1
v_mfma_f32_16x16x32_bf16 v[152:155],v[20:23],v[92:95],0
s_add_u32 s63,s87,8388608
v_mfma_f32_16x16x32_bf16 v[156:159],v[24:27],v[92:95],0
buffer_load_dwordx4 v[64:67],v11,s[36:39],s63 offen offset:0 sc1
v_mfma_f32_16x16x32_bf16 v[152:155],v[20:23],v[108:111],v[152:155]
v_cvt_pk_bf16_f32 v216,v144,v145
v_cvt_pk_bf16_f32 v217,v146,v147
v_mfma_f32_16x16x32_bf16 v[156:159],v[24:27],v[108:111],v[156:159]
v_cvt_pk_bf16_f32 v218,v148,v149
v_cvt_pk_bf16_f32 v219,v150,v151
v_mfma_f32_16x16x32_bf16 v[28:31],v[192:195],v[212:215],v[28:31]
buffer_load_dwordx4 v[68:71],v11,s[36:39],s63 offen offset:1024 sc1
v_mfma_f32_16x16x32_bf16 v[204:207],v[212:215],v[212:215],v[204:207]
buffer_load_dwordx4 v[72:75],v11,s[36:39],s63 offen offset:2048 sc1
v_mfma_f32_16x16x32_bf16 v[28:31],v[192:195],v[216:219],v[28:31]
buffer_load_dwordx4 v[76:79],v11,s[36:39],s63 offen offset:3072 sc1
v_mfma_f32_16x16x32_bf16 v[204:207],v[216:219],v[216:219],v[204:207]
buffer_load_dword v8,v6,s[36:39],s69 offen offset:0 sc1
v_mfma_f32_16x16x32_bf16 v[160:163],v[20:23],v[112:115],0
v_cvt_pk_bf16_f32 v220,v152,v153
v_cvt_pk_bf16_f32 v221,v154,v155
v_mfma_f32_16x16x32_bf16 v[164:167],v[24:27],v[112:115],0
v_cvt_pk_bf16_f32 v222,v156,v157
v_cvt_pk_bf16_f32 v223,v158,v159
v_mfma_f32_16x16x32_bf16 v[168:171],v[20:23],v[116:119],0
s_nop 0
v_mfma_f32_16x16x32_bf16 v[28:31],v[192:195],v[220:223],v[28:31]
v_mfma_f32_16x16x32_bf16 v[204:207],v[220:223],v[220:223],v[204:207]
v_mfma_f32_16x16x32_bf16 v[172:175],v[24:27],v[116:119],0
v_mfma_f32_16x16x32_bf16 v[176:179],v[20:23],v[120:123],0
v_mfma_f32_16x16x32_bf16 v[180:183],v[24:27],v[120:123],0
s_nop 3
v_add_f32_dpp v224,v28,v28 row_ror:8 row_mask:0xf bank_mask:0xf
v_cndmask_b32 v225,v204,v205,s[46:47]
v_cndmask_b32 v226,v206,v207,s[46:47]
v_cndmask_b32 v225,v225,v226,s[56:57]
v_mfma_f32_16x16x32_bf16 v[184:187],v[20:23],v[124:127],0
ds_write_b32 v2,v224 offset:0
ds_write_b32 v0,v225 offset:0
v_mfma_f32_16x16x32_bf16 v[188:191],v[24:27],v[124:127],0
s_waitcnt lgkmcnt(0)
s_barrier
ds_read_b128 v[196:199],v3 offset:0
ds_read_b128 v[200:203],v3 offset:16
ds_read_b128 v[204:207],v3 offset:32
s_waitcnt lgkmcnt(0)
v_add_f32 v196,v196,v197
v_add_f32 v198,v198,v199
v_add_f32 v200,v200,v204
v_add_f32 v201,v201,v205
v_add_f32 v202,v202,v206
v_add_f32 v203,v203,v207
v_add_f32 v196,v196,v198
v_add_f32 v200,v200,v201
v_add_f32 v202,v202,v203
v_mul_f32 v196,s76,v196
v_add_f32 v197,v200,v202
v_mul_f32 v197,s76,v197
v_fma_f32 v197,-v196,v196,v197
v_max_f32 v197,0,v197
v_add_f32 v197,s77,v197
v_rsq_f32 v228,v197
s_nop 0
v_mul_f32_e64 v230,-v196,v228
v_mov_b32 v229,v228
v_mov_b32 v231,v230
v_pk_fma_f32 v[196:197],v[128:129],v[228:229],v[230:231]
v_pk_fma_f32 v[198:199],v[130:131],v[228:229],v[230:231]
v_pk_fma_f32 v[200:201],v[132:133],v[228:229],v[230:231]
v_pk_fma_f32 v[202:203],v[134:135],v[228:229],v[230:231]
v_pk_fma_f32 v[128:129],v[196:197],v[160:161],v[128:129]
v_pk_fma_f32 v[130:131],v[198:199],v[162:163],v[130:131]
v_pk_fma_f32 v[132:133],v[200:201],v[164:165],v[132:133]
v_pk_fma_f32 v[134:135],v[202:203],v[166:167],v[134:135]
v_pk_fma_f32 v[196:197],v[136:137],v[228:229],v[230:231]
v_pk_fma_f32 v[198:199],v[138:139],v[228:229],v[230:231]
v_pk_fma_f32 v[200:201],v[140:141],v[228:229],v[230:231]
v_pk_fma_f32 v[202:203],v[142:143],v[228:229],v[230:231]
v_pk_fma_f32 v[136:137],v[196:197],v[168:169],v[136:137]
v_pk_fma_f32 v[138:139],v[198:199],v[170:171],v[138:139]
v_pk_fma_f32 v[140:141],v[200:201],v[172:173],v[140:141]
v_pk_fma_f32 v[142:143],v[202:203],v[174:175],v[142:143]
v_pk_fma_f32 v[196:197],v[144:145],v[228:229],v[230:231]
v_pk_fma_f32 v[198:199],v[146:147],v[228:229],v[230:231]
v_pk_fma_f32 v[200:201],v[148:149],v[228:229],v[230:231]
v_pk_fma_f32 v[202:203],v[150:151],v[228:229],v[230:231]
v_pk_fma_f32 v[144:145],v[196:197],v[176:177],v[144:145]
v_pk_fma_f32 v[146:147],v[198:199],v[178:179],v[146:147]
v_pk_fma_f32 v[148:149],v[200:201],v[180:181],v[148:149]
v_pk_fma_f32 v[150:151],v[202:203],v[182:183],v[150:151]
v_pk_fma_f32 v[196:197],v[152:153],v[228:229],v[230:231]
v_pk_fma_f32 v[198:199],v[154:155],v[228:229],v[230:231]
v_pk_fma_f32 v[200:201],v[156:157],v[228:229],v[230:231]
v_pk_fma_f32 v[202:203],v[158:159],v[228:229],v[230:231])"
R"(
v_pk_fma_f32 v[152:153],v[196:197],v[184:185],v[152:153]
v_pk_fma_f32 v[154:155],v[198:199],v[186:187],v[154:155]
v_pk_fma_f32 v[156:157],v[200:201],v[188:189],v[156:157]
v_pk_fma_f32 v[158:159],v[202:203],v[190:191],v[158:159]
v_cvt_pk_bf16_f32 v208,v128,v129
v_cvt_pk_bf16_f32 v209,v130,v131
v_cvt_pk_bf16_f32 v210,v132,v133
v_cvt_pk_bf16_f32 v211,v134,v135
v_cvt_pk_bf16_f32 v212,v136,v137
v_cvt_pk_bf16_f32 v213,v138,v139
v_cvt_pk_bf16_f32 v214,v140,v141
v_cvt_pk_bf16_f32 v215,v142,v143
v_mfma_f32_16x16x32_bf16 v[28:31],v[192:195],v[208:211],0
v_cvt_pk_bf16_f32 v216,v144,v145
v_cvt_pk_bf16_f32 v217,v146,v147
v_mfma_f32_16x16x32_bf16 v[204:207],v[208:211],v[208:211],0
v_cvt_pk_bf16_f32 v218,v148,v149
v_cvt_pk_bf16_f32 v219,v150,v151
v_mfma_f32_16x16x32_bf16 v[28:31],v[192:195],v[212:215],v[28:31]
v_cvt_pk_bf16_f32 v220,v152,v153
v_cvt_pk_bf16_f32 v221,v154,v155
v_mfma_f32_16x16x32_bf16 v[204:207],v[212:215],v[212:215],v[204:207]
v_cvt_pk_bf16_f32 v222,v156,v157
v_cvt_pk_bf16_f32 v223,v158,v159
v_mfma_f32_16x16x32_bf16 v[28:31],v[192:195],v[216:219],v[28:31]
v_mfma_f32_16x16x32_bf16 v[204:207],v[216:219],v[216:219],v[204:207]
v_mfma_f32_16x16x32_bf16 v[28:31],v[192:195],v[220:223],v[28:31]
v_mfma_f32_16x16x32_bf16 v[204:207],v[220:223],v[220:223],v[204:207]
s_waitcnt vmcnt(13)
buffer_store_dword v7,v5,s[36:39],s60 offen offset:0 sc0 sc1
s_mov_b32 s60,536870912
buffer_store_dwordx4 v[208:211],v1,s[36:39],s88 offen offset:0 sc0 sc1
buffer_store_dwordx4 v[212:215],v1,s[36:39],s88 offen offset:2048 sc0 sc1
s_add_u32 s88,s88,4096
s_nop 0
v_add_f32_dpp v224,v28,v28 row_ror:8 row_mask:0xf bank_mask:0xf
v_cndmask_b32 v225,v204,v205,s[46:47]
v_cndmask_b32 v226,v206,v207,s[46:47]
v_cndmask_b32 v225,v225,v226,s[56:57]
s_and_b32 s63,s89,128
ds_write_b32 v2,v224 offset:1024
ds_write_b32 v0,v225 offset:1024
buffer_store_dwordx4 v[216:219],v1,s[36:39],s88 offen offset:0 sc0 sc1
buffer_store_dwordx4 v[220:223],v1,s[36:39],s88 offen offset:2048 sc0 sc1
s_waitcnt lgkmcnt(0)
s_barrier
ds_read_b128 v[196:199],v3 offset:1024
ds_read_b128 v[200:203],v3 offset:1040
ds_read_b128 v[204:207],v3 offset:1056
s_waitcnt lgkmcnt(0)
v_add_f32 v196,v196,v197
v_add_f32 v198,v198,v199
v_add_f32 v200,v200,v204
v_add_f32 v201,v201,v205
v_add_f32 v202,v202,v206
v_add_f32 v203,v203,v207
v_add_f32 v196,v196,v198
v_add_f32 v200,v200,v201
v_add_f32 v202,v202,v203
v_mul_f32 v196,s76,v196
v_add_f32 v197,v200,v202
v_mul_f32 v197,s76,v197
v_fma_f32 v197,-v196,v196,v197
v_max_f32 v197,0,v197
v_add_f32 v198,s77,v197
v_fma_f32 v199,s77,v198,v197
v_rsq_f32 v205,v198
v_rsq_f32 v198,v199
v_mov_b32 v200,v196
v_mul_f32_e64 v204,-v196,v205
v_mul_f32 v203,v199,v198
v_mul_f32 v201,s78,v198
v_mul_f32 v199,v198,v198
v_mul_f32 v202,s79,v199
v_mul_f32 v202,v202,v201
s_lshr_b32 s63,s63,1
s_sub_u32 s63,s89,s63
buffer_store_dwordx4 v[200:203],v4,s[36:39],s89 offen offset:0 sc0 sc1
buffer_store_dwordx2 v[204:205],v10,s[36:39],s63 offen offset:256 sc0 sc1
s_mov_b32 s60,s90
s_waitcnt vmcnt(7)
s_branch Lr_it0_%=
Lr_tc1_%=:
s_add_u32 s63,s87,71303168
buffer_load_dwordx4 v[32:35],v11,s[36:39],s63 offen offset:0 sc1
buffer_load_dwordx4 v[36:39],v11,s[36:39],s63 offen offset:1024 sc1
buffer_load_dwordx4 v[40:43],v11,s[36:39],s63 offen offset:2048 sc1
buffer_load_dwordx4 v[44:47],v11,s[36:39],s63 offen offset:3072 sc1
s_add_u32 s63,s87,89128960
buffer_load_dwordx4 v[48:51],v11,s[36:39],s63 offen offset:0 sc1
buffer_load_dwordx4 v[52:55],v11,s[36:39],s63 offen offset:1024 sc1
buffer_load_dwordx4 v[56:59],v11,s[36:39],s63 offen offset:2048 sc1
buffer_load_dwordx4 v[60:63],v11,s[36:39],s63 offen offset:3072 sc1
s_add_u32 s63,s87,8388608
buffer_load_dwordx4 v[64:67],v11,s[36:39],s63 offen offset:0 sc1
buffer_load_dwordx4 v[68:71],v11,s[36:39],s63 offen offset:1024 sc1
buffer_load_dwordx4 v[72:75],v11,s[36:39],s63 offen offset:2048 sc1
buffer_load_dwordx4 v[76:79],v11,s[36:39],s63 offen offset:3072 sc1
buffer_load_dword v8,v6,s[36:39],s69 offen offset:0 sc1
s_waitcnt vmcnt(13)
buffer_store_dword v7,v5,s[36:39],s60 offen offset:0 sc0 sc1
s_mov_b32 s60,536870912
s_waitcnt vmcnt(1)
s_branch Lr_it0_%=
Lr_done_%=:
s_waitcnt vmcnt(0) lgkmcnt(0)
s_barrier
Lcode_r_e:)"
  :: "s"(ws), "s"(sK), "s"(sStep), "s"(sRq), "s"(sEpR), "v"(vRP), "v"(vRF), "v"(vRwr), "v"(vRrd), "v"(vRS), "v"(vFlag), "v"(vL4), "v"(vRL) : "v0", "v1", "v2", "v3", "v4", "v5", "v6", "v7", "v8", "v9", "v10", "v11", "v12", "v13", "v14", "v15", "v16", "v17", "v18", "v19", "v20", "v21", "v22", "v23", "v24", "v25", "v26", "v27", "v28", "v29", "v30", "v31", "v32", "v33", "v34", "v35", "v36", "v37", "v38", "v39", "v40", "v41", "v42", "v43", "v44", "v45", "v46", "v47", "v48", "v49", "v50", "v51", "v52", "v53", "v54", "v55", "v56", "v57", "v58", "v59", "v60", "v61", "v62", "v63", "v64", "v65", "v66", "v67", "v68", "v69", "v70", "v71", "v72", "v73", "v74", "v75", "v76", "v77", "v78", "v79", "v80", "v81", "v82", "v83", "v84", "v85", "v86", "v87", "v88", "v89", "v90", "v91", "v92", "v93", "v94", "v95", "v96", "v97", "v98", "v99", "v100", "v101", "v102", "v103", "v104", "v105", "v106", "v107", "v108", "v109", "v110", "v111", "v112", "v113", "v114", "v115", "v116", "v117", "v118", "v119", "v120", "v121", "v122", "v123", "v124", "v125", "v126", "v127", "v128", "v129", "v130", "v131", "v132", "v133", "v134", "v135", "v136", "v137", "v138", "v139", "v140", "v141", "v142", "v143", "v144", "v145", "v146", "v147", "v148", "v149", "v150", "v151", "v152", "v153", "v154", "v155", "v156", "v157", "v158", "v159", "v160", "v161", "v162", "v163", "v164", "v165", "v166", "v167", "v168", "v169", "v170", "v171", "v172", "v173", "v174", "v175", "v176", "v177", "v178", "v179", "v180", "v181", "v182", "v183", "v184", "v185", "v186", "v187", "v188", "v189", "v190", "v191", "v192", "v193", "v194", "v195", "v196", "v197", "v198", "v199", "v200", "v201", "v202", "v203", "v204", "v205", "v206", "v207", "v208", "v209", "v210", "v211", "v212", "v213", "v214", "v215", "v216", "v217", "v218", "v219", "v220", "v221", "v222", "v223", "v224", "v225", "v226", "v227", "v228", "v229", "v230", "v231", "v255", "s36", "s37", "s38", "s39", "s40", "s41", "s42", "s43", "s44", "s45", "s46", "s47", "s48", "s49", "s50", "s51", "s52", "s53", "s54", "s55", "s56", "s57", "s58", "s59", "s60", "s61", "s62", "s63", "s64", "s65", "s66", "s67", "s68", "s69", "s70", "s71", "s72", "s73", "s74", "s75", "s76", "s77", "s78", "s79", "s80", "s81", "s82", "s83", "s84", "s85", "s86", "s87", "s88", "s89", "s90", "s91", "s92", "s93", "s94", "s95", "s96", "s97", "s98", "s99", "vcc", "scc", "memory");
    }
    LANE_ID(lane2);
    for (int t = kind == 6 ? 82 - NRW + slice : (kind - 1) * 32 + rep * 8 + slice; t < 256; t += 82)
      row_last(ws, out, t, lane2, wave, ep);
    if (b == 0 && lane2 == 0 && wave == 0) __hip_atomic_store((gu32*)(ws + OFF_EP), ep, __ATOMIC_RELAXED, __HIP_MEMORY_SCOPE_AGENT);
    asm volatile("s_endpgm" ::: "memory");
    __builtin_unreachable();
  }
}

extern "C" void kernel_launch(void* const* d_in, const int* in_sizes, int n_in, void* d_out, int out_size,
                              void* d_ws, size_t ws_size, hipStream_t stream) {
  const u16* q = (const u16*)d_in[0];
  const u16* k = (const u16*)d_in[1];
  const u16* v = (const u16*)d_in[2];
  const u16* shift_k = (const u16*)d_in[3];
  const u16* ssm_k = (const u16*)d_in[4];
  const u16* ssm_D = (const u16*)d_in[5];
  const u16* w1 = (const u16*)d_in[7];
  const u16* w2 = (const u16*)d_in[8];
  const u16* w3 = (const u16*)d_in[9];
  const u16* w4 = (const u16*)d_in[10];
  flow_kernel<<<256, 256, 0, stream>>>(q, k, v, shift_k, ssm_k, ssm_D, w1, w2, w3, w4, (char*)d_ws, (u16*)d_out);
}
```

```cpp
#include <hip/hip_runtime.h>
#include <hip/hip_bf16.h>
#include <stdint.h>
#include <type_traits>

typedef short s16x8 __attribute__((ext_vector_type(8)));
typedef short s16x4 __attribute__((ext_vector_type(4)));
typedef float f32x4 __attribute__((ext_vector_type(4)));
typedef unsigned short u16;
typedef unsigned short u16x8 __attribute__((ext_vector_type(8)));
typedef unsigned short u16x4 __attribute__((ext_vector_type(4)));
typedef float f32x2 __attribute__((ext_vector_type(2)));
typedef __bf16 bf16x2 __attribute__((ext_vector_type(2)));
typedef unsigned u32x4v __attribute__((ext_vector_type(4)));
typedef __attribute__((address_space(1))) unsigned gu32;

constexpr int S_ = 2048, D_ = 1024, F_ = 3584;
#define PF 1

constexpr int KE = 1, RT1 = 0, LB = 66, FL_BYTES = 2141312, WS_USED = 122727552;
constexpr int OFF_KVR = 4194304, OFF_YF = 8388608, OFF_XP = 12582912, OFF_HS = 22085632, OFF_X1 = 31457280, OFF_G = 40894464, OFF_P0 = 71303168, OFF_DBG = 80740352, OFF_P1 = 89128960, OFF_QS = 98566144, OFF_P2 = 99614720, OFF_P3 = 109051904, OFF_S1 = 118489088, OFF_ST = 119013376, OFF_FL = 120586240, OFF_FXP = 120587264, OFF_FH = 120859648, OFF_FX1 = 121132032, OFF_FP = 121405440, OFF_FG = 121950720, OFF_FKV = 122629120, OFF_FY = 122661888, OFF_EP = 122727424;

#define WS_DIAG 0

__device__ __forceinline__ float bf2f(u16 v) { return __uint_as_float(((unsigned)v) << 16); }
__device__ __forceinline__ u16 f2bf(float f) { return __builtin_bit_cast(u16, (__bf16)f); }
__device__ __forceinline__ float rbf(float f) { return bf2f(f2bf(f)); }
__device__ __forceinline__ void st16(void* p, u16x8 v) {
  asm volatile("global_store_dwordx4 %0, %1, off sc0 sc1\n\ts_nop 1" :: "v"(p), "v"(v) : "memory");
}
__device__ __forceinline__ void bst16(u32x4v v, __amdgpu_buffer_rsrc_t rs, unsigned vo, unsigned so) {
  asm volatile("buffer_store_dwordx4 %0, %1, %2, %3 offen sc0 sc1\n\ts_nop 1" :: "v"(v), "v"(vo), "s"(rs), "s"(so) : "memory");
}
#define WAIT_VM(n) asm volatile("s_waitcnt vmcnt(%0)" ::"n"(n) : "memory")
#define WAIT_LGKM0() asm volatile("s_waitcnt lgkmcnt(0)" ::: "memory")

#define CODE_RANGE(B, E, p, n)                                                                                   \
  const char* p; unsigned n;                                                                                     \
  { unsigned lo_, hi_;                                                                                           \
    asm volatile("s_getpc_b64 s[98:99]\nLpc%=:\n\ts_add_u32 %0, s98, (" B "-Lpc%=)&4294967295\n\ts_addc_u32 %1, s99, (" B "-Lpc%=)>>32\n\t" \
                 "s_mov_b32 %2, " E "-" B : "=s"(lo_), "=s"(hi_), "=s"(n) :: "s98", "s99", "scc");               \
    p = (const char*)(((unsigned long long)hi_ << 32) | lo_); }
__device__ __forceinline__ unsigned touch(const char* p, unsigned n, unsigned off) {
  unsigned t;
  asm volatile("global_load_dword %0, %1, off" : "=v"(t) : "v"(p + (off < n ? off : 0u)) : "memory");
  return t;
}

__device__ __forceinline__ void touch_lds(const char* p, unsigned a) {
  asm volatile("s_mov_b32 m0, %1\n\ts_nop 0\n\tglobal_load_lds_dword %0, off" :: "v"(p), "s"(a) : "memory");
}
#define PFPOL ""
__device__ __forceinline__ void touch_pol(const char* p, unsigned a) {
  asm volatile("s_mov_b32 m0, %1\n\ts_nop 0\n\tglobal_load_lds_dword %0, off " PFPOL :: "v"(p), "s"(a) : "memory");
}
#define YSLEEP 4
#define KSLEEP 2
#define CTS(i)
#define CTS_INIT()
#define CTS_FLUSH()
__device__ __forceinline__ f32x2 up2(unsigned p) { return (f32x2){__uint_as_float(p << 16), __uint_as_float(p & 0xffff0000u)}; }
__device__ __forceinline__ unsigned pk2(f32x2 v) { return __builtin_bit_cast(unsigned, __builtin_convertvector(v, bf16x2)); }
__device__ __forceinline__ f32x2 rbf2(f32x2 v) { return up2(pk2(v)); }
#define BARRIER() asm volatile("s_waitcnt lgkmcnt(0)\n\ts_barrier" ::: "memory")
template <int N, class F> __device__ __forceinline__ void sfor(F&& f) {
  if constexpr (N > 0) { sfor<N - 1>(f); f(std::integral_constant<int, N - 1>{}); }
}
template <int NL>
__device__ __forceinline__ void poll_flags(const char* p, int n, int lane, unsigned ep) {
#pragma unroll 1
  for (int it = 0; it < 20000; ++it) {
    bool ok = true;
#pragma unroll
    for (int i = 0; i < NL; ++i) {
      const int f = lane + 64 * i;
      ok &= __hip_atomic_load((gu32*)(p + (f < n ? f : 0) * 128), __ATOMIC_RELAXED, __HIP_MEMORY_SCOPE_AGENT) == ep;
    }
    if (__all(ok)) break;
    __builtin_amdgcn_s_sleep(YSLEEP);
  }
  asm volatile("" ::: "memory");
}

constexpr int CV_HP = (2048 + 32) * 2;
constexpr int CV_UR = (2048 + 256) * 2;
constexpr int CV_CH = 2 * CV_HP + CV_UR;
#define TPOS 4
#define LATEQ 4
constexpr int CV_STG = 8 * CV_CH;

struct KvRegs { u32x4v k0, k1, k2, k3, k4, k5, k6, v0, v1, v2, v3; };
struct SkRegs { u32x4v s0, s1, s2, s3; };
#define LD16(d, p, pol) asm volatile("global_load_dwordx4 %0, %1, off " pol : "=v"(d) : "v"(p) : "memory")
__device__ __forceinline__ void kv_load(KvRegs& R, const u16* __restrict__ kin, const u16* __restrict__ vin, int b, int tid) {
  const int tr = (b >> 2) * 32 + 4 * (tid >> 5), cc = (b & 3) * 256 + 8 * (tid & 31);
  const u16* kp = kin + (size_t)tr * D_ + cc;
  const u16* vp = vin + (size_t)tr * D_ + cc;
  LD16(R.k3, kp, "nt"); LD16(R.k4, kp + D_, "nt"); LD16(R.k5, kp + 2 * D_, "nt"); LD16(R.k6, kp + 3 * D_, "nt");
  LD16(R.v0, vp, "nt"); LD16(R.v1, vp + D_, "nt"); LD16(R.v2, vp + 2 * D_, "nt"); LD16(R.v3, vp + 3 * D_, "nt");
  const u16* kq = tr ? kp - 3 * D_ : kp;
  LD16(R.k0, kq, "nt"); LD16(R.k1, kq + D_, "nt"); LD16(R.k2, kq + 2 * D_, "nt");
}
__device__ __forceinline__ void sk_load(SkRegs& R, const u16* __restrict__ shift_k, int b, int tid) {
  const u16* sp = shift_k + (size_t)((b & 3) * 256 + 8 * (tid & 31)) * 4;
  LD16(R.s0, sp, ""); LD16(R.s1, sp + 8, ""); LD16(R.s2, sp + 16, ""); LD16(R.s3, sp + 24, "");
}
template <int N>
__device__ __forceinline__ void kv_wait(KvRegs& R, SkRegs& Q) {
  asm volatile("s_waitcnt vmcnt(%15)" : "+v"(R.k0), "+v"(R.k1), "+v"(R.k2), "+v"(R.k3), "+v"(R.k4), "+v"(R.k5), "+v"(R.k6), "+v"(R.v0), "+v"(R.v1), "+v"(R.v2),
               "+v"(R.v3), "+v"(Q.s0), "+v"(Q.s1), "+v"(Q.s2), "+v"(Q.s3) : "n"(N) : "memory");
}
__device__ __forceinline__ void kv_store(KvRegs& R, const SkRegs& Q, char* __restrict__ ws, char* T, int b, int tid) {
  const int t0 = (b >> 2) * 32, c0 = (b & 3) * 256, ch8 = tid & 31, rg = tid >> 5;
  if (t0 + 4 * rg < 3) R.k0 = R.k1 = R.k2 = (u32x4v){0u, 0u, 0u, 0u};
  typedef unsigned u32x2 __attribute__((ext_vector_type(2)));
  const u32x4v kr[7] = {R.k0, R.k1, R.k2, R.k3, R.k4, R.k5, R.k6}, vr[4] = {R.v0, R.v1, R.v2, R.v3}, sk[4] = {Q.s0, Q.s1, Q.s2, Q.s3};
#pragma unroll
  for (int j2 = 0; j2 < 4; ++j2) {
    const f32x2 sa = up2(sk[j2][0]), sb = up2(sk[j2][1]), sc = up2(sk[j2][2]), sd = up2(sk[j2][3]);
    const f32x2 s0 = {sa[0], sc[0]}, s1 = {sa[1], sc[1]}, s2 = {sb[0], sd[0]}, s3 = {sb[1], sd[1]};
    f32x2 kf[7], o[4];
#pragma unroll
    for (int r = 0; r < 7; ++r) kf[r] = up2(kr[r][j2]);
#pragma unroll
    for (int r = 0; r < 4; ++r)
      o[r] = __builtin_elementwise_fma(kf[r + 3], s0, __builtin_elementwise_fma(kf[r + 2], s1, __builtin_elementwise_fma(kf[r + 1], s2, kf[r] * s3))) * up2(vr[r][j2]);
#pragma unroll
    for (int e = 0; e < 2; ++e)
      *(u32x2*)(T + ch8 * 520 + (2 * j2 + e) * 64 + (28 - 4 * rg) * 2) = (u32x2){pk2((f32x2){o[3][e], o[2][e]}), pk2((f32x2){o[1][e], o[0][e]})};
  }
  BARRIER();
  u16* kvr = (u16*)(ws + OFF_KVR);
#pragma unroll
  for (int i = 0; i < 4; ++i) {
    const int ch = (tid >> 2) + 64 * i, cb = tid & 3;
    const char* p = T + ch * 64 + (ch >> 3) * 8 + cb * 16;
    const u16x4 lo = *(const u16x4*)p, hi = *(const u16x4*)(p + 8);
    st16(kvr + (size_t)(c0 + ch) * S_ + (2016 - t0) + cb * 8, __builtin_shufflevector(lo, hi, 0, 1, 2, 3, 4, 5, 6, 7));
  }
}

typedef unsigned u32x2v __attribute__((ext_vector_type(2)));
__device__ __forceinline__ void h_load(u32x4v (&hv)[8], unsigned& hx, unsigned& dD, const u16* __restrict__ ssm_k, const u16* __restrict__ ssm_D, int c0, int tid) {
  const int lane = tid & 63, wave = tid >> 6;
#pragma unroll
  for (int i = 0; i < 8; ++i) LD16(hv[i], ssm_k + (size_t)(c0 + i) * S_ + tid * 8, "nt");
  asm volatile("global_load_dword %0, %1, off" : "=v"(hx) : "v"(ssm_k + (size_t)(c0 + (lane & 7)) * S_ + (wave < 3 ? (wave + 1) * 512 : 0)) : "memory");
  asm volatile("global_load_dword %0, %1, off" : "=v"(dD) : "v"(ssm_D + c0 + 2 * wave) : "memory");
}
template <int N>
__device__ __forceinline__ void h_wait(u32x4v (&hv)[8], unsigned& hx, unsigned& dD) {
  asm volatile("s_waitcnt vmcnt(%10)" : "+v"(hv[0]), "+v"(hv[1]), "+v"(hv[2]), "+v"(hv[3]), "+v"(hv[4]), "+v"(hv[5]), "+v"(hv[6]), "+v"(hv[7]), "+v"(hx), "+v"(dD) : "n"(N) : "memory");
}
__device__ __forceinline__ void h_spread(const u32x4v (&hv)[8], unsigned hx, char* lds, int tid) {
  if (tid < 32 && (tid & 3) != 3) *(u32x4v*)(lds + (tid >> 2) * CV_CH + ((tid >> 1) & 1) * CV_HP + (tid & 1) * 16) = (u32x4v){0u, 0u, 0u, 0u};
  *(u32x4v*)(lds + (tid >> 5) * CV_CH + 2 * CV_HP + 4096 + (tid & 31) * 16) = (u32x4v){0u, 0u, 0u, 0u};
#pragma unroll
  for (int i = 0; i < 8; ++i) {
    char* base = lds + i * CV_CH + 32 + tid * 16;
    unsigned hn = __builtin_amdgcn_update_dpp(0, (int)hv[i][0], 0x130, 0xf, 0xf, true);
    const unsigned last = __builtin_amdgcn_readlane(hx, i);
    if ((tid & 63) == 63) hn = last;
#define AB(a, b) __builtin_amdgcn_alignbyte(a, b, 2)
    if (tid == 0) *(u32x4v*)(lds + i * CV_CH + CV_HP + 16) = (u32x4v){0u, 0u, 0u, hv[i][0] << 16};
    *(u32x4v*)(base) = hv[i];
    *(u32x4v*)(base + CV_HP) = (u32x4v){AB(hv[i][1], hv[i][0]), AB(hv[i][2], hv[i][1]), AB(hv[i][3], hv[i][2]), AB(hn, hv[i][3])};
#undef AB
  }
}

__device__ __forceinline__ void pads(char* __restrict__ ws, int w, int lane, unsigned ep) {
  if (w < 132) {
    const int k = w < 128 ? w >> 5 : w - 128, u = k == 0 ? -1 : k == 1 ? 128 : k == 2 ? 131 : 260;
    st16(ws + (w < 128 ? OFF_HS + u * 32768 + (w & 31) * 1024 : OFF_FH + u * 1024) + lane * 16, __builtin_bit_cast(u16x8, (u32x4v){w >= 128 && !(lane & 7) ? ep : 0u, 0u, 0u, 0u}));
  }
}
__device__ __forceinline__ void prewrite(char* __restrict__ ws, unsigned off, unsigned str, int rep, int tid) {
#pragma unroll
  for (int i = 0; i < 2; ++i) st16(ws + off + (rep + 4 * (tid >> 3) + 132 * i) * str + (tid & 7) * 16, __builtin_bit_cast(u16x8, (u32x4v){0u, 0u, 0u, 0u}));
}

__device__ __forceinline__ void conv_part(char* __restrict__ ws, unsigned dD, char* lds, int g, int tid, int lane, int wave, unsigned ep, const char* wp, unsigned wstep) {
  const __amdgpu_buffer_rsrc_t rs = __builtin_amdgcn_make_buffer_rsrc(ws, 0, WS_USED, 0x00020000);
  const u16* kvr = (const u16*)(ws + OFF_KVR) + (size_t)(8 * g + 2 * wave) * S_;
  auto wait1 = [&](unsigned off) {
#pragma unroll 1
    for (int it = 0; it < 20000; ++it) {
      if (__all(__hip_atomic_load((gu32*)(ws + off), __ATOMIC_RELAXED, __HIP_MEMORY_SCOPE_AGENT) == ep)) break;
      __builtin_amdgcn_s_sleep(KSLEEP);
    }
    asm volatile("" ::: "memory");
  };
  const unsigned lb = (unsigned)(size_t)lds;
  auto u_fetch = [&](int part) {
#pragma unroll
    for (int j = 0; j < 2; ++j)
      asm volatile("s_mov_b32 m0, %1\n\ts_nop 0\n\tglobal_load_lds_dwordx4 %0, off sc1" :: "v"(kvr + j * S_ + part * 512 + lane * 8),
                   "s"(lb + (2 * wave + j) * CV_CH + 2 * CV_HP + part * 1024) : "memory");
  };
  const unsigned xo = (tid >> 4) * 32768 + g * 256 + (tid & 15) * 16;
  u32x4v qv[6];
  u32x2v qs[6];
  auto q_fetch = [&](int s0, int ns) {
#pragma unroll
    for (int i = 0; i < 6; ++i)
      if (i < ns) {
        asm volatile("buffer_load_dwordx4 %0, %1, %2, %3 offen sc1" : "=v"(qv[i]) : "v"(xo), "s"(rs), "s"(OFF_XP + (s0 + i) * 524288) : "memory");
        asm volatile("buffer_load_dwordx2 %0, %1, %2, %3 offen sc1" : "=v"(qs[i]) : "v"(tid * 8), "s"(rs), "s"(OFF_QS + (s0 + i) * 2048) : "memory");
      }
  };
  const int cg = g >> 5;
  const int l15 = lane & 15, lg = lane >> 4, cp = (l15 + 1) & 1;
  const unsigned ao = cp * CV_HP + 2 * (8 * lg + (l15 + 1 - cp)), bo = 2 * CV_HP + 2 * (2032 - 16 * l15 + 8 * lg) - 2 * 256 * 7;
  const unsigned own = lb + 2 * wave * CV_CH;
  char* stg = lds + CV_STG;
  s16x8 ad[2];
#pragma unroll
  for (int j = 0; j < 2; ++j) {
    const int e = 15 - l15 - 8 * lg;
    const unsigned d = (j ? dD >> 16 : dD & 0xffffu) << ((e & 1) * 16);
    ad[j] = __builtin_bit_cast(s16x8, (u32x4v){e >> 1 == 0 ? d : 0u, e >> 1 == 1 ? d : 0u, e >> 1 == 2 ? d : 0u, e >> 1 == 3 ? d : 0u});
  }
  f32x4 acc[2][6];
  unsigned f0, f1;
  bool mid;
  auto finish = [&](auto s0_c, auto ns_c) {
    constexpr int S0 = decltype(s0_c)::value, NS = decltype(ns_c)::value, A0 = S0 == 2 ? 0 : 4, Q0 = S0 == 6 ? 4 : 0;
#pragma unroll
    for (int s = 0; s < NS; ++s)
#pragma unroll
      for (int r = 0; r < 4; ++r) *(unsigned*)(stg + (Q0 + s) * 4352 + l15 * 272 + (4 * lg + r) * 16 + wave * 4) = pk2((f32x2){acc[0][A0 + s][r], acc[1][A0 + s][r]});
    if (S0 == 0) {
      asm volatile("s_waitcnt vmcnt(0)" : "+v"(qv[0]), "+v"(qv[1]), "+v"(qs[0]), "+v"(qs[1]), "+v"(f0) :: "memory");
      mid = __all(f0 == ep);
      if (mid) { u_fetch(2); u_fetch(1); }
    } else if (S0 == 2) asm volatile("" : "+v"(qv[0]), "+v"(qv[1]), "+v"(qv[2]), "+v"(qv[3]), "+v"(qv[4]), "+v"(qv[5]), "+v"(qs[0]), "+v"(qs[1]), "+v"(qs[2]), "+v"(qs[3]), "+v"(qs[4]), "+v"(qs[5]) :: "memory");
    BARRIER();
    u32x4v y4[NS];
#pragma unroll
    for (int s = 0; s < NS; ++s) {
      y4[s] = *(const u32x4v*)(stg + (Q0 + s) * 4352 + (tid >> 4) * 272 + (tid & 15) * 16);
      const f32x2 rd2 = {__uint_as_float(qs[Q0 + s][0]), __uint_as_float(qs[Q0 + s][0])}, nm2 = {__uint_as_float(qs[Q0 + s][1]), __uint_as_float(qs[Q0 + s][1])};
      u32x4v o;
#pragma unroll
      for (int e = 0; e < 4; ++e) {
        const f32x2 x = up2(qv[Q0 + s][e]);
        o[e] = pk2(__builtin_elementwise_fma(__builtin_elementwise_fma(x, rd2, nm2), up2(y4[s][e]), x));
      }
      bst16(o, rs, xo, OFF_XP + (S0 + s) * 524288);
    }
#pragma unroll
    for (int s = 0; s < NS; ++s) bst16(y4[s], rs, (tid >> 3) * 16384 + g * 128 + (tid & 7) * 16, OFF_YF + (S0 + s) * 524288);
    CTS(S0 ? 7 : 5);
  };
  auto touches = [&]() {
#pragma unroll
    for (int i = 0; i < 3; ++i) touch_lds(wp + (size_t)i * wstep, lb + 163584);
  };
  auto count = [&](int s0, int ns, auto n_c) {
    WAIT_VM(decltype(n_c)::value);
    BARRIER();
    if (tid < 4 * ns) __hip_atomic_fetch_add((gu32*)(ws + OFF_FXP + (s0 + (tid >> 2)) * 16384 + (tid & 3) * 1024 + (g >> 4) * 128), 1u, __ATOMIC_RELAXED, __HIP_MEMORY_SCOPE_AGENT);
  };
  auto pair = [&](unsigned bb) {
    u32x2v ax[2][2][2][2];
    s16x8 bx[2][2][2];
    auto rdA = [&](int q) {
#pragma unroll
      for (int j = 0; j < 2; ++j)
#pragma unroll
        for (int m = 0; m < 2; ++m) {
          asm volatile("ds_read2_b32 %0, %1 offset0:%2 offset1:%3" : "=v"(ax[q & 1][j][m][0]) : "v"(own + j * CV_CH + ao), "n"(128 * m + 16 * q), "n"(128 * m + 16 * q + 1));
          asm volatile("ds_read2_b32 %0, %1 offset0:%2 offset1:%3" : "=v"(ax[q & 1][j][m][1]) : "v"(own + j * CV_CH + ao), "n"(128 * m + 16 * q + 2), "n"(128 * m + 16 * q + 3));
          asm volatile("ds_read_b128 %0, %1 offset:%2" : "=v"(bx[q & 1][j][m]) : "v"(bb), "n"(j * CV_CH + 512 * (1 - m) + 64 * q));
        }
    };
    rdA(0);
    sfor<8>([&](auto q_c) {
      constexpr int q = decltype(q_c)::value, p = q & 1;
      if (q + 1 < 8) rdA(q + 1);
      asm volatile("s_waitcnt lgkmcnt(%0)" :: "n"(q + 1 < 8 ? 12 : 0) : "memory");
      __builtin_amdgcn_sched_barrier(0);
#pragma unroll
      for (int j = 0; j < 2; ++j) {
        const s16x8 a0f = __builtin_bit_cast(s16x8, __builtin_shufflevector(ax[p][j][0][0], ax[p][j][0][1], 0, 1, 2, 3)), a1f = __builtin_bit_cast(s16x8, __builtin_shufflevector(ax[p][j][1][0], ax[p][j][1][1], 0, 1, 2, 3));
        if (q == 0) {
          acc[j][4] = __builtin_amdgcn_mfma_f32_16x16x32_bf16(ad[j], bx[p][j][0], acc[j][4], 0, 0, 0);
          acc[j][5] = __builtin_amdgcn_mfma_f32_16x16x32_bf16(ad[j], bx[p][j][1], acc[j][5], 0, 0, 0);
        }
        acc[j][4] = __builtin_amdgcn_mfma_f32_16x16x32_bf16(a0f, bx[p][j][0], acc[j][4], 0, 0, 0);
        acc[j][5] = __builtin_amdgcn_mfma_f32_16x16x32_bf16(a0f, bx[p][j][1], acc[j][5], 0, 0, 0);
        acc[j][5] = __builtin_amdgcn_mfma_f32_16x16x32_bf16(a1f, bx[p][j][0], acc[j][5], 0, 0, 0);
      }
      __builtin_amdgcn_sched_barrier(0);
    });
  };
  wait1(OFF_FKV + ((lane & 15) * 4 + cg) * 128);
  CTS(3);
  u_fetch(3);
  wait1(OFF_FY + (lane & 31) * 128);
  q_fetch(0, 2);
  const unsigned fm = OFF_FKV + ((16 + (lane & 31)) * 4 + cg) * 128;
  asm volatile("global_load_dword %0, %1, %2 sc1" : "=v"(f0) : "v"(fm), "s"(ws) : "memory");
  CTS(4);
#pragma unroll
  for (int i = 0; i < 4; ++i) acc[i >> 1][4 + (i & 1)] = (f32x4){0.f, 0.f, 0.f, 0.f};
  pair(own + bo + 512 * 6);
  finish(std::integral_constant<int, 0>{}, std::integral_constant<int, 2>{});
  u32x2v a0[8][2], a1[8][2];
  s16x8 b0[6], b1[6];
  unsigned pa[4], pb = own + bo;
#pragma unroll
  for (int i = 0; i < 4; ++i) pa[i] = own + ao + 1024 * i;
  auto rd1 = [&](int rr, int i, u32x2v (&ah)[8][2], s16x8 (&bfr)[6]) {
    const int m = i < 18 ? i / 3 : 6 + (i - 18) / 2, e = i < 18 ? i % 3 : i & 1;
    if (e < 2) asm volatile("ds_read2_b32 %0, %1 offset0:%2 offset1:%3" : "=v"(ah[m][e & 1]) : "v"(pa[m >> 1]), "n"(128 * (m & 1) + 2 * e + 16 * rr), "n"(128 * (m & 1) + 2 * e + 16 * rr + 1));
    else asm volatile("ds_read_b128 %0, %1 offset:%2" : "=v"(bfr[m < 6 ? m : 0]) : "v"(pb), "n"(512 * (7 - m) + 64 * rr));
  };
  auto round = [&](f32x4 (&ac)[6], u32x2v (&ah)[8][2], s16x8 (&bfr)[6], u32x2v (&ahn)[8][2], s16x8 (&bfn)[6], int rr, unsigned step) {
    int k = 0;
#pragma unroll
    for (int s = 2; s <= 7; ++s)
#pragma unroll
      for (int m = 0; m <= s; ++m) {
        if (s - m > 5) continue;
        const s16x8 af = __builtin_bit_cast(s16x8, __builtin_shufflevector(ah[m][0], ah[m][1], 0, 1, 2, 3));
        ac[s - 2] = __builtin_amdgcn_mfma_f32_16x16x32_bf16(af, bfr[s - m > 5 ? 0 : s - m], ac[s - 2], 0, 0, 0);
        __builtin_amdgcn_sched_barrier(0);
        if (m == s) asm volatile("s_waitcnt lgkmcnt(12)" ::: "memory");
#pragma unroll
        for (int i = 0; i < 22; ++i)
          if (i * 4 / 3 == k) {
            rd1(rr, i, ahn, bfn);
            if (rr && (i == 4 || i == 10 || i == 16 || i == 21)) asm volatile("v_add_u32 %0, %1, %0" : "+v"(pa[i / 6]) : "s"(step));
            if (rr && i == 17) asm volatile("v_add_u32 %0, %1, %0" : "+v"(pb) : "s"(step));
          }
        ++k;
        __builtin_amdgcn_sched_barrier(0);
      }
  };
  if (!mid) { wait1(fm); u_fetch(2); u_fetch(1); WAIT_VM(0); }
  WAIT_VM(4);
#pragma unroll
  for (int i = 0; i < 22; ++i) rd1(0, i, a0, b0);
  WAIT_LGKM0();
  auto channel = [&](auto j_c, auto r0_c, auto r1_c) {
    constexpr int j = decltype(j_c)::value, r0 = decltype(r0_c)::value, r1 = decltype(r1_c)::value;
    __builtin_amdgcn_sched_barrier(0);
    if (r0 == 0) {
#pragma unroll
      for (int s = 2; s <= 7; ++s) acc[j][s - 2] = s < 6 ? __builtin_amdgcn_mfma_f32_16x16x32_bf16(ad[j], b0[s], (f32x4){0.f, 0.f, 0.f, 0.f}, 0, 0, 0) : (f32x4){0.f, 0.f, 0.f, 0.f};
    }
#pragma unroll 1
    for (int r = r0; r < r1; r += 2) {
      if (j == 0 && r == 2) {
        asm volatile("s_waitcnt vmcnt(0)\n\ts_barrier" ::: "memory");
        if (tid < 8) __hip_atomic_fetch_add((gu32*)(ws + OFF_FXP + (tid >> 2) * 16384 + (tid & 3) * 1024 + (g >> 4) * 128), 1u, __ATOMIC_RELAXED, __HIP_MEMORY_SCOPE_AGENT);
      }
      if (TPOS == 0 && j == 0 && r >= 2) { touch_lds(wp, lb + 163584); wp += wstep; }
      round(acc[j], a0, b0, a1, b1, 1, r < 6 ? 128 : j ? 0 : CV_CH - 384);
      round(acc[j], a1, b1, a0, b0, 0, 0);
    }
    WAIT_LGKM0();
    __builtin_amdgcn_sched_barrier(0);
  };
  typedef std::integral_constant<int, 0> I0;
  typedef std::integral_constant<int, 1> I1;
  channel(I0{}, I0{}, std::integral_constant<int, 8>{});
  CTS(13);
  channel(I1{}, I0{}, std::integral_constant<int, LATEQ - 2>{});
  const unsigned fa = OFF_FY + (32 + lane) * 128, fb = lane < 32 ? OFF_FY + (96 + lane) * 128 : OFF_FKV + ((48 + (lane & 15)) * 4 + cg) * 128;
  asm volatile("global_load_dword %0, %1, %2 sc1" : "=v"(f0) : "v"(fa), "s"(ws) : "memory");
  asm volatile("global_load_dword %0, %1, %2 sc1" : "=v"(f1) : "v"(fb), "s"(ws) : "memory");
  channel(I1{}, std::integral_constant<int, LATEQ - 2>{}, std::integral_constant<int, LATEQ>{});
  asm volatile("s_waitcnt vmcnt(0)" : "+v"(f0), "+v"(f1) :: "memory");
  if (!__all(f0 == ep && f1 == ep)) { wait1(fa); wait1(fb); }
  u_fetch(0);
  q_fetch(2, 6);
  if (TPOS == 1) touches();
  CTS(15);
  channel(I1{}, std::integral_constant<int, LATEQ>{}, std::integral_constant<int, 8>{});
  WAIT_VM(TPOS == 1 ? 3 : 0);
  CTS(14);
  finish(std::integral_constant<int, 2>{}, std::integral_constant<int, 4>{});
  if (TPOS == 2) touches();
  pair(own + bo);
  count(2, 4, std::integral_constant<int, TPOS == 2 ? 3 : 0>{});
  CTS(11);
  if (TPOS == 3) touches();
  finish(std::integral_constant<int, 6>{}, std::integral_constant<int, 2>{});
  if (TPOS == 4) touches();
  count(6, 2, std::integral_constant<int, TPOS == 4 ? 3 : 0>{});
}
__device__ __forceinline__ float sum2_seg(float s, float q) {
  s += __builtin_bit_cast(float, __builtin_amdgcn_update_dpp(0, __builtin_bit_cast(int, s), 0x128, 0xf, 0xf, true));
  q += __builtin_bit_cast(float, __builtin_amdgcn_update_dpp(0, __builtin_bit_cast(int, q), 0x128, 0xf, 0xf, true));
  asm volatile("s_nop 1\n\tv_permlane16_swap_b32 %0, %1" : "+v"(s), "+v"(q));
  s += q;
  q = s;
  asm volatile("s_nop 1\n\tv_permlane32_swap_b32 %0, %1" : "+v"(s), "+v"(q));
  return s + q;
}
#define LASTSLEEP 8
__device__ __forceinline__ void wait_flags(const char* p, int lane, int n, unsigned ep) {
  gu32* f = (gu32*)(p + (lane < n ? lane : 0) * 128);
#pragma unroll 1
  for (int it = 0; it < 10000; ++it) {
    if (__all(__hip_atomic_load(f, __ATOMIC_RELAXED, __HIP_MEMORY_SCOPE_AGENT) == ep)) break;
    __builtin_amdgcn_s_sleep(LASTSLEEP);
  }
  asm volatile("" ::: "memory");
}
struct QRegs { u32x4v a[4], b[4]; };
__device__ __forceinline__ void q_load(QRegs& R, const u16* __restrict__ q, int f, int lane, int wave) {
  const u16* p = q + (size_t)(f * 16 + (lane & 7)) * D_ + wave * 256 + (lane >> 3) * 8;
#pragma unroll
  for (int i = 0; i < 4; ++i) { LD16(R.a[i], p + i * 64, "nt"); LD16(R.b[i], p + 8 * D_ + i * 64, "nt"); }
}
template <int N>
__device__ __forceinline__ void q_wait(QRegs& R, unsigned& e) {
  asm volatile("s_waitcnt vmcnt(%9)" : "+v"(R.a[0]), "+v"(R.a[1]), "+v"(R.a[2]), "+v"(R.a[3]), "+v"(R.b[0]), "+v"(R.b[1]), "+v"(R.b[2]), "+v"(R.b[3]), "+v"(e) : "n"(N) : "memory");
}
__device__ __forceinline__ void row_stats(const u32x4v (&xq)[4], char* lds, int k, int lane, int wave, float& mean, float& var) {
  f32x2* red = (f32x2*)(lds + 16640);
  const int r8 = lane & 7, seg = lane >> 3;
  f32x2 s = {0.f, 0.f}, sq = {0.f, 0.f};
#pragma unroll
  for (int i = 0; i < 16; ++i) { const f32x2 x = up2(xq[i >> 2][i & 3]); s += x; sq = __builtin_elementwise_fma(x, x, sq); }
  const float a = sum2_seg(s[0] + s[1], sq[0] + sq[1]);
  if ((seg & 5) == 0) ((float*)red)[((k * 4 + wave) * 8 + r8) * 2 + (seg >> 1)] = a;
  BARRIER();
  const f32x2 t = (red[(k * 4) * 8 + r8] + red[(k * 4 + 1) * 8 + r8]) + (red[(k * 4 + 2) * 8 + r8] + red[(k * 4 + 3) * 8 + r8]);
  mean = t[0] * (1.0f / 1024.0f);
  var = fmaxf(t[1] * (1.0f / 1024.0f) - mean * mean, 0.f);
}
__device__ __forceinline__ void q_store(char* __restrict__ ws, int t, int lane, int wave, const u32x4v (&xq)[4], float mean, float var) {
  const __amdgpu_buffer_rsrc_t rs = __builtin_amdgcn_make_buffer_rsrc(ws, 0, WS_USED, 0x00020000);
  const int r8 = lane & 7, seg = lane >> 3;
  const unsigned fo = (t >> 1) * 32768 + (wave * 8 + (seg >> 2)) * 1024 + ((seg & 3) * 16 + (t & 1) * 8 + r8) * 16;
#pragma unroll
  for (int i = 0; i < 4; ++i) bst16(xq[i], rs, fo + i * 2048, OFF_XP);
  const float rd = __builtin_amdgcn_rsqf(var + 1e-5f);
  if (wave == 0 && seg == 0) __builtin_amdgcn_raw_buffer_store_b64((u32x2v){__float_as_uint(rd), __float_as_uint(-mean * rd)}, rs, t * 64 + r8 * 8, OFF_QS, 17);
}
__device__ __forceinline__ void row_last(char* __restrict__ ws, u16* __restrict__ out, int t, int lane, int wave, unsigned ep) {
  const __amdgpu_buffer_rsrc_t rs = __builtin_amdgcn_make_buffer_rsrc(ws, 0, WS_USED, 0x00020000);
  const int r8 = lane & 7, seg = lane >> 3;
  const unsigned fo = (4 * LB + t) * 16384 + wave * 4096 + lane * 16;
  wait_flags(ws + OFF_FP + (2 * LB + (t >> 1)) * 2048, lane, 16, ep);
  u32x4v p0[4], p1[4], p2[4], p3[4];
#pragma unroll
  for (int i = 0; i < 4; ++i) {
    p0[i] = __builtin_bit_cast(u32x4v, __builtin_amdgcn_raw_buffer_load_b128(rs, fo + i * 1024, OFF_P0, 16));
    p1[i] = __builtin_bit_cast(u32x4v, __builtin_amdgcn_raw_buffer_load_b128(rs, fo + i * 1024, OFF_P1, 16));
    p2[i] = __builtin_bit_cast(u32x4v, __builtin_amdgcn_raw_buffer_load_b128(rs, fo + i * 1024, OFF_P2, 16));
    p3[i] = __builtin_bit_cast(u32x4v, __builtin_amdgcn_raw_buffer_load_b128(rs, fo + i * 1024, OFF_P3, 16));
  }
#pragma unroll
  for (int i = 0; i < 4; ++i) {
    u32x4v o;
#pragma unroll
    for (int e = 0; e < 4; ++e) o[e] = pk2((up2(p0[i][e]) + up2(p1[i][e])) + (up2(p2[i][e]) + up2(p3[i][e])));
    st16(out + (size_t)(t * 8 + r8) * D_ + wave * 256 + i * 64 + seg * 8, __builtin_bit_cast(u16x8, o));
  }
}

#define FINE 20
__device__ __forceinline__ void chunk_last(char* __restrict__ ws, u16* __restrict__ out, int c, int lane, unsigned ep) {
  const __amdgpu_buffer_rsrc_t rs = __builtin_amdgcn_make_buffer_rsrc(ws, 0, WS_USED, 0x00020000);
  const unsigned fo = 4 * LB * 16384 + c * 1024 + lane * 16;
  gu32* f = (gu32*)(ws + OFF_FP + (2 * LB + (c >> 5)) * 2048 + ((lane & 3) * 4 + ((c >> 2) & 3)) * 128);
#pragma unroll 1
  for (int it = 0; it < 20000; ++it) {
    if (__all(__hip_atomic_load(f, __ATOMIC_RELAXED, __HIP_MEMORY_SCOPE_AGENT) == ep)) break;
    __builtin_amdgcn_s_sleep(4);
  }
  asm volatile("" ::: "memory");
  const u32x4v p0 = __builtin_bit_cast(u32x4v, __builtin_amdgcn_raw_buffer_load_b128(rs, fo, OFF_P0, 16)), p1 = __builtin_bit_cast(u32x4v, __builtin_amdgcn_raw_buffer_load_b128(rs, fo, OFF_P1, 16));
  const u32x4v p2 = __builtin_bit_cast(u32x4v, __builtin_amdgcn_raw_buffer_load_b128(rs, fo, OFF_P2, 16)), p3 = __builtin_bit_cast(u32x4v, __builtin_amdgcn_raw_buffer_load_b128(rs, fo, OFF_P3, 16));
  u32x4v o;
#pragma unroll
  for (int e = 0; e < 4; ++e) o[e] = pk2((up2(p0[e]) + up2(p1[e])) + (up2(p2[e]) + up2(p3[e])));
  st16(out + (size_t)((c >> 4) * 8 + (lane & 7)) * D_ + (c & 15) * 64 + (lane >> 3) * 8, __builtin_bit_cast(u16x8, o));
}

__device__ __forceinline__ int swzB(int r) { return ((((r >> 1) & 1) | (((r >> 3) & 1) << 1)) << 1); }
#define FLW 1
#define MAP3 1
#define MAP12 1
#define FLIP 0
#define NRW 18
#define PFROWS 110
__global__ __launch_bounds__(256, 1) void flow_kernel(const u16* __restrict__ q, const u16* __restrict__ kin, const u16* __restrict__ vin,
                                                      const u16* __restrict__ shift_k, const u16* __restrict__ ssm_k, const u16* __restrict__ ssm_D,
                                                      const u16* __restrict__ w1, const u16* __restrict__ w2,
                                                      const u16* __restrict__ w3, const u16* __restrict__ w4, char* __restrict__ ws,
                                                      u16* __restrict__ out) {
  __shared__ __attribute__((aligned(16))) char lds[163840];
  const int tid = threadIdx.x, lane = tid & 63, wave = __builtin_amdgcn_readfirstlane(tid >> 6);
  const int b = blockIdx.x, xcd = b & 7, idx = b >> 3;
  const unsigned sLds = (unsigned)(size_t)lds;
  int kind, rep, slice, kh = 0;
  const int P = xcd >> 1, par = (xcd & 1) ^ FLIP, nE = par ? 20 : 12, li = idx - nE;
  const int qq = (par ? 0 : 12) + idx - 8;
  if (idx < 8) { kind = 1 + (par ^ MAP12); rep = P; slice = idx; }
  else if (li < 0) {
    const int e = 15 * P + qq, t = (e * 205) >> 11, k = e - 10 * t, k3 = k < 3 ? 0 : k < 6 ? 1 : 2;
    kind = qq < 15 ? 3 : 5; slice = qq < 15 ? 3 * t + k3 : 18; rep = qq < 15 ? k - 3 * k3 : P;
  } else if (!par) { kind = 4; kh = P; rep = li >> 2; slice = li & 3; }
  else {
    const int nl = 8 - (P & 1), l = ((15 * P + 1) >> 1) + li, t = (l * 205) >> 10, k = l - 5 * t;
    kind = li < nl ? 3 : 6; rep = li < nl ? (k == 4 ? 4 : 3 + (k & 1)) : 0;
    slice = li < nl ? 3 * t + (k >> 1) : ((9 * P) >> 1) + li - nl;
  }
  CTS_INIT();
  CTS(0);
  {
    CODE_RANGE("Lpc%=", "Lcode_g12_b", c0, n0) CODE_RANGE("Lcode_r_e", "Lcode_g4_b", c7, n7)
    touch_lds(tid < 224 ? c0 + (tid * 128 < n0 ? tid * 128 : 0) : c7 + ((tid - 224) * 128 < n7 ? (tid - 224) * 128 : 0), sLds + 163584);
  }
  const unsigned sScr = sLds + 163584;
  const char* cp;
  unsigned co0, co1;
  {
    unsigned lo, hi, n;
    asm volatile("s_getpc_b64 s[98:99]\nLpd%=:\n"
                 "s_mov_b32 %0, Lcode_r_b-Lpd%=\ns_mov_b32 %2, Lcode_r_e-Lcode_r_b\n"
                 "s_cmp_eq_u32 %3, 1\ns_cselect_b32 %0, Lcode_g12_b-Lpd%=, %0\ns_cselect_b32 %2, Lcode_g2_b-Lcode_g12_b, %2\n"
                 "s_cmp_eq_u32 %3, 2\ns_cselect_b32 %0, Lcode_g2_b-Lpd%=, %0\ns_cselect_b32 %2, Lcode_g12_e-Lcode_g2_b, %2\n"
                 "s_cmp_eq_u32 %3, 3\ns_cselect_b32 %0, Lcode_g3_b-Lpd%=, %0\ns_cselect_b32 %2, Lcode_g3_e-Lcode_g3_b, %2\n"
                 "s_cmp_eq_u32 %3, 4\ns_cselect_b32 %0, Lcode_g4_b-Lpd%=, %0\ns_cselect_b32 %2, Lcode_g4_e-Lcode_g4_b, %2\n"
                 "s_cmp_eq_u32 %3, 5\ns_cselect_b32 %0, Lcode_g3o_b-Lpd%=, %0\ns_cselect_b32 %2, Lcode_g3o_e-Lcode_g3o_b, %2\n"
                 "s_add_u32 %0, s98, %0\ns_addc_u32 %1, s99, 0"
                 : "=&s"(lo), "=&s"(hi), "=&s"(n) : "s"(kind) : "s98", "s99", "scc");
    cp = (const char*)(((unsigned long long)hi << 32) | lo);
    co0 = tid * 128 < n ? tid * 128 : 0;
    co1 = 32768 + tid * 128 < n ? 32768 + tid * 128 : 0;
  }
  auto code_touch = [&]() { touch_lds(cp + co0, sScr); touch_lds(cp + co1, sScr); };
  unsigned ep = 0;
#define EP_LOAD() asm volatile("global_load_dword %0, %1, %2 sc1" : "=v"(ep) : "v"(0u), "s"(ws + OFF_EP) : "memory")
  if (kind == 1) {
    code_touch();
    prewrite(ws, OFF_FH + slice * 128, 1024, rep, tid);
  }
  else {
    const int cv = li < 0 ? -1 : (li + (par ? 20 : 0)) * 4 + P;
    auto epoch = [&]() { const unsigned e = __builtin_amdgcn_readfirstlane(ep) + 1u; return e ? e : 1u; };
    auto q_unit = [&](QRegs& QR, int f) {
      float ma, va, mb, vb_;
      row_stats(QR.a, lds + CV_STG, 0, lane, wave, ma, va);
      row_stats(QR.b, lds + CV_STG, 1, lane, wave, mb, vb_);
      q_store(ws, 2 * f, lane, wave, QR.a, ma, va);
      q_store(ws, 2 * f + 1, lane, wave, QR.b, mb, vb_);
    };
    auto publish = [&](unsigned off) {
      BARRIER();
      if (tid == 0) __hip_atomic_store((gu32*)(ws + off), ep, __ATOMIC_RELAXED, __HIP_MEMORY_SCOPE_AGENT);
    };
    KvRegs KR;
    SkRegs SK;
    if (cv >= 0) {
      u32x4v hv[8];
      unsigned hx, dD;
      EP_LOAD();
      h_load(hv, hx, dD, ssm_k, ssm_D, 8 * cv, tid);
      kv_load(KR, kin, vin, 64 + cv, tid);
      sk_load(SK, shift_k, cv, tid);
      code_touch();
      touch_lds((const char*)((par ^ MAP12) ? w2 : w1) + (size_t)(par ? (li < 8 ? li * 256 + tid : 0) : (li < 16 ? li * 128 + (tid & 127) : 0)) * 1024 + 384, sScr);
      h_wait<18>(hv, hx, dD);
      asm volatile("" : "+v"(ep));
      ep = epoch();
      CTS(12);
      h_spread(hv, hx, lds, tid);
      kv_wait<3>(KR, SK);
      kv_store(KR, SK, ws, lds + CV_STG, 64 + cv, tid);
      CTS(1);
      WAIT_VM(0);
      publish(OFF_FKV + (64 + cv) * 128);
      CTS(2);
      const char* wp = (const char*)w4 + (par ? 0 : (size_t)(kh * 896 + min(li * 45, 848)) * 2048 + tid * 128);
      conv_part(ws, dD, lds, cv, tid, lane, wave, ep, wp, par ? 0 : 16 * 2048);
      CTS(8);
    } else {
      const int n = kind == 2 ? 64 + rep * 8 + slice : qq * 4 + P;
      QRegs QR;
      if (n < 64) {
        EP_LOAD();
        kv_load(KR, kin, vin, n, tid);
        sk_load(SK, shift_k, n, tid);
        kv_wait<0>(KR, SK);
        asm volatile("" : "+v"(ep));
        ep = epoch();
        CTS(12);
        KvRegs K2;
        kv_store(KR, SK, ws, lds, n, tid);
        pads(ws, n * 4 + wave, lane, ep);
        if (n < 8)
          st16(ws + OFF_FXP + (lane >> 3) * 16384 + wave * 1024 + (2 * (n & 3) + (n >> 2)) * 128 + (lane & 7) * 16, __builtin_bit_cast(u16x8, (u32x4v){ep - 16u, 0u, 0u, 0u}));
        CTS(1);
        kv_load(K2, kin, vin, 192 + n, tid);
        WAIT_VM(11);
        publish(OFF_FKV + n * 128);
        CTS(2);
        q_load(QR, q, 32 + n, lane, wave);
        kv_wait<8>(K2, SK);
        kv_store(K2, SK, ws, lds + 16896, 192 + n, tid);
        q_wait<0>(QR, ep);
        publish(OFF_FKV + (192 + n) * 128);
        CTS(3);
      } else {
        QRegs Q0;
        EP_LOAD();
        q_load(Q0, q, n - 64, lane, wave);
        q_wait<0>(Q0, ep);
        ep = epoch();
        CTS(12);
        q_load(QR, q, 32 + n, lane, wave);
        q_unit(Q0, n - 64);
        CTS(1);
        q_wait<0>(QR, ep);
        publish(OFF_FY + (n - 64) * 128);
        CTS(2);
      }
      q_unit(QR, 32 + n);
      {
        const char* wp = (const char*)w2 + (size_t)(n < 64 ? 0 : (n - 64) * 512 + tid) * 128;
        touch_lds(wp, sScr);
        touch_lds(wp + (n < 64 ? 0 : 32768), sScr);
      }
      WAIT_VM(2);
      publish(OFF_FY + (32 + n) * 128);
      CTS(9);
      if (n >= 64) {
        prewrite(ws, OFF_FX1 + slice * 128, 1024, rep, tid);
        prewrite(ws, OFF_FP + slice * 128, 2048, rep, tid);
        prewrite(ws, OFF_FP + (8 + slice) * 128, 2048, rep, tid);
        prewrite(ws, OFF_FG + slice * 128, 2560, rep, tid);
        prewrite(ws, OFF_FG + (8 + slice) * 128, 2560, rep, tid);
        if (slice < 3) prewrite(ws, OFF_FG + (16 + slice) * 128, 2560, rep, tid);
        st16(ws + OFF_FXP + slice * 128 + (132 + rep + 4 * (tid >> 3)) * 1024 + (tid & 7) * 16, __builtin_bit_cast(u16x8, (u32x4v){0u, 0u, 0u, 0u}));
      }
      code_touch();
      if (n < 64) {
#pragma unroll
        for (int i = 0; i < 4; ++i) {
          const unsigned x = rep * 1024 + i * 256 + tid, row = kind == 3 ? (x * 21846) >> 16 : x >> 1;
          if (rep < (kind == 3 ? 3 : 2)) touch_lds((const char*)w3 + (size_t)row * 7168 + slice * 384 + (x - row * (kind == 3 ? 3 : 2)) * 128, sScr);
        }
      }
    }
    BARRIER();
    CTS(10);
  }
  CTS_FLUSH();
  const unsigned sDbg0 = OFF_DBG + (b * 4 + wave) * 8192;
#define LANE_ID(x) int x; asm volatile("v_mbcnt_lo_u32_b32 %0, -1, 0\n\tv_mbcnt_hi_u32_b32 %0, -1, %0" : "=v"(x))
#define OOB 0x80000000u
#define COMMON(NF, FIN, FOUT, SN0, WS, LDW, NREP)                                                                                    \
  LANE_ID(ln);                                                                                                                       \
  const int l15 = ln & 15, lg = ln >> 4, lq = l15 >> 2, lp = ln & 3, rr = 8 * lg + lq;                                               \
  const unsigned vA = ln * 16, vFlag = (ln < FLW && wave == 3) ? (FOUT) + ln * 4 : OOB;                                                        \
  const unsigned vPollOff = wave < 2 && ln < (NF) ? (FIN) + ((NF) == 16 && ln >= 8 ? (ln & 2 ? ln : ln - 16) : ln) * 128 : OOB, vSnapOff = wave == 0 ? vPollOff : OOB;                   \
  const unsigned vSnapRd = (SN0) + (ln >> 5) * (WS) + ((ln & 31) < (NF) ? (ln & 31) : 0) * 4;                                        \
  const unsigned vWd0 = (ln >> 3) * (LDW) + ((ln & 7) ^ swzB(ln >> 3)) * 16, vWd1 = ((ln >> 3) + 8) * (LDW) + ((ln & 7) ^ swzB(8 + (ln >> 3))) * 16; \
  const unsigned vWr0 = (kind == 4 ? wave * 4096 : (wave >> 1) * 8192) + rr * 128 + (((kind == 4 ? 0 : (wave & 1) * 4) + lp) ^ swzB(rr)) * 16, vWr1 = vWr0 + 8;                  \
  const unsigned vWr2 = kind == 4 ? vWr0 ^ 64 : 16384 + rr * 128 + ((2 * wave + (lp >> 1)) ^ swzB(rr)) * 16 + (lp & 1) * 8;                                  \
                                            \
  const int res = kind == 3 ? rep + 3 * KE : rep;                     \
  const unsigned sStep = (NREP) << 15, sU0 = (rep < 3 ? rep : res) << 15, sX1 = ((NREP) == 4 ? 128 + rep : 128 + (res + 2) % 5) << 15; \
  const unsigned sX2 = ((NREP) == 4 ? 260 + rep : 260 + (res + 4) % 5) << 15, sDbg = sDbg0, sBuf = kind != 3 ? kind : (rep < 3 ? 3 : 5) << 15
  constexpr unsigned AUX = 131072;
  if (kind == 3 || kind == 5) {
    COMMON(8, OFF_FX1, OFF_FG + slice * 128, AUX + 1536, 2048, 7168, kind == 3 ? 5 : 4);
    const unsigned vSrc = OFF_X1 + wave * 4096 + vA, vOut = OFF_G + (slice * 6 + wave) * 1024 + vA;
    const unsigned vOut2 = OFF_G + (slice * 6 + 4 + (wave >> 1)) * 1024 + (((wave & 1) * 2 + (lg >> 1)) * 16 + l15) * 16 + (lg & 1) * 8;
    const unsigned vAuxS = OFF_ST + (ln & 7) * 128 + (wave * 8 + (ln >> 3)) * 4, vX = AUX + wave * 2048 + ln * 4;
    const unsigned vStW = (ln & 7) == 0 ? AUX + 1280 + (wave * 8 + (ln >> 3)) * 4 : AUX + 24576 + wave * 2048 + 1536 + ln * 4, vStR = AUX + 1280 + l15 * 8;
    const unsigned sWw = 16 * wave * 7168 + slice * 384;
    if (kind == 3) { asm volatile(R"(
Lcode_g3_b:
s_mov_b64 s[36:37],%0
s_mov_b32 s38,122727552
s_mov_b32 s39,0x00020000
s_mov_b32 s54,%1
s_mov_b32 s55,%2
s_mov_b32 s56,%3
s_mov_b32 s58,%4
s_mov_b32 s83,%5
s_mov_b32 s82,%6
s_mov_b32 s84,%7
v_mov_b32 v0,%8
v_mov_b32 v2,%9
v_mov_b32 v3,%10
v_mov_b32 v4,%11
v_mov_b32 v6,%12
v_mov_b32 v7,%13
v_mov_b32 v8,%14
v_mov_b32 v9,%15
v_mov_b32 v10,%16
v_mov_b32 v11,%17
v_mov_b32 v28,%18
v_mov_b32 v29,%19
v_mov_b32 v30,%20
v_mov_b32 v31,%21
v_mov_b32 v32,%22
s_mov_b64 s[40:41],%23
s_mov_b32 s42,7340032
s_mov_b32 s43,0x00020000
s_lshl_b32 s88,s54,11
s_mov_b32 s69,122727424
buffer_load_dword v5,off,s[36:39],s69 sc1
s_add_u32 m0,s88,0
s_add_u32 s61,s82,0
buffer_load_dwordx4 v28,s[40:43],s61 offen offset:0  lds
s_add_u32 m0,s88,1024
s_nop 0
buffer_load_dwordx4 v29,s[40:43],s61 offen offset:0  lds
s_add_u32 m0,s88,8064
s_nop 0
buffer_load_dwordx4 v28,s[40:43],s61 offen offset:128  lds
s_add_u32 m0,s88,9088
s_nop 0
buffer_load_dwordx4 v29,s[40:43],s61 offen offset:128  lds
s_add_u32 m0,s88,16128
s_nop 0
buffer_load_dwordx4 v28,s[40:43],s61 offen offset:256  lds
s_add_u32 m0,s88,17152
s_nop 0
buffer_load_dwordx4 v29,s[40:43],s61 offen offset:256  lds
s_add_u32 m0,s88,24576
s_add_u32 s61,s82,458752
buffer_load_dwordx4 v28,s[40:43],s61 offen offset:0  lds
s_add_u32 m0,s88,25600
s_nop 0
buffer_load_dwordx4 v29,s[40:43],s61 offen offset:0  lds
s_add_u32 m0,s88,32640
s_nop 0
buffer_load_dwordx4 v28,s[40:43],s61 offen offset:128  lds
s_add_u32 m0,s88,33664
s_nop 0
buffer_load_dwordx4 v29,s[40:43],s61 offen offset:128  lds
s_add_u32 m0,s88,40704
s_nop 0
buffer_load_dwordx4 v28,s[40:43],s61 offen offset:256  lds
s_add_u32 m0,s88,41728
s_nop 0
buffer_load_dwordx4 v29,s[40:43],s61 offen offset:256  lds
s_add_u32 m0,s88,49152
s_add_u32 s61,s82,917504
buffer_load_dwordx4 v28,s[40:43],s61 offen offset:0  lds
s_add_u32 m0,s88,50176
s_nop 0
buffer_load_dwordx4 v29,s[40:43],s61 offen offset:0  lds
s_add_u32 m0,s88,57216
s_nop 0
buffer_load_dwordx4 v28,s[40:43],s61 offen offset:128  lds
s_add_u32 m0,s88,58240
s_nop 0
buffer_load_dwordx4 v29,s[40:43],s61 offen offset:128  lds
s_add_u32 m0,s88,65280
s_nop 0
buffer_load_dwordx4 v28,s[40:43],s61 offen offset:256  lds
s_add_u32 m0,s88,66304
s_nop 0
buffer_load_dwordx4 v29,s[40:43],s61 offen offset:256  lds
s_add_u32 m0,s88,73728
s_add_u32 s61,s82,1376256
buffer_load_dwordx4 v28,s[40:43],s61 offen offset:0  lds
s_add_u32 m0,s88,74752
s_nop 0
buffer_load_dwordx4 v29,s[40:43],s61 offen offset:0  lds
s_add_u32 m0,s88,81792
s_nop 0
buffer_load_dwordx4 v28,s[40:43],s61 offen offset:128  lds
s_add_u32 m0,s88,82816
s_nop 0
buffer_load_dwordx4 v29,s[40:43],s61 offen offset:128  lds
s_add_u32 m0,s88,89856
s_nop 0
buffer_load_dwordx4 v28,s[40:43],s61 offen offset:256  lds
s_add_u32 m0,s88,90880
s_nop 0
buffer_load_dwordx4 v29,s[40:43],s61 offen offset:256  lds
v_add_u32 v40,49152,v30
v_add_u32 v41,49152,v31
v_add_u32 v42,49152,v32
v_add_u32 v43,98304,v30
v_add_u32 v44,98304,v31
v_add_u32 v45,98304,v32
v_mov_b32 v84,0x3f803f80
v_mov_b32 v85,0x3f803f80
v_mov_b32 v86,0x3f803f80
v_mov_b32 v87,0x3f803f80
s_waitcnt vmcnt(18)
s_barrier
s_add_u32 m0,s88,98304
s_add_u32 s61,s82,1835008
buffer_load_dwordx4 v28,s[40:43],s61 offen offset:0  lds
s_add_u32 m0,s88,99328
ds_read_b64_tr_b16 a[0:1],v30 offset:0
buffer_load_dwordx4 v29,s[40:43],s61 offen offset:0  lds
s_add_u32 m0,s88,106368
ds_read_b64_tr_b16 a[2:3],v30 offset:512
buffer_load_dwordx4 v28,s[40:43],s61 offen offset:128  lds
s_add_u32 m0,s88,107392
ds_read_b64_tr_b16 a[4:5],v31 offset:0
buffer_load_dwordx4 v29,s[40:43],s61 offen offset:128  lds
s_add_u32 m0,s88,114432
ds_read_b64_tr_b16 a[6:7],v31 offset:512
buffer_load_dwordx4 v28,s[40:43],s61 offen offset:256  lds
s_add_u32 m0,s88,115456
ds_read_b64_tr_b16 a[8:9],v32 offset:0
buffer_load_dwordx4 v29,s[40:43],s61 offen offset:256  lds
ds_read_b64_tr_b16 a[10:11],v32 offset:512
ds_read_b64_tr_b16 a[12:13],v30 offset:4096
ds_read_b64_tr_b16 a[14:15],v30 offset:4608
ds_read_b64_tr_b16 a[16:17],v31 offset:4096
ds_read_b64_tr_b16 a[18:19],v31 offset:4608
ds_read_b64_tr_b16 a[20:21],v32 offset:4096
ds_read_b64_tr_b16 a[22:23],v32 offset:4608
s_waitcnt vmcnt(18)
s_barrier
s_add_u32 m0,s88,122880
s_add_u32 s61,s82,2293760
buffer_load_dwordx4 v28,s[40:43],s61 offen offset:0  lds
s_add_u32 m0,s88,123904
ds_read_b64_tr_b16 a[24:25],v30 offset:24576
buffer_load_dwordx4 v29,s[40:43],s61 offen offset:0  lds
s_add_u32 m0,s88,130944
ds_read_b64_tr_b16 a[26:27],v30 offset:25088
buffer_load_dwordx4 v28,s[40:43],s61 offen offset:128  lds
s_add_u32 m0,s88,131968
ds_read_b64_tr_b16 a[28:29],v31 offset:24576
buffer_load_dwordx4 v29,s[40:43],s61 offen offset:128  lds
s_add_u32 m0,s88,139008
ds_read_b64_tr_b16 a[30:31],v31 offset:25088
buffer_load_dwordx4 v28,s[40:43],s61 offen offset:256  lds
s_add_u32 m0,s88,140032
ds_read_b64_tr_b16 a[32:33],v32 offset:24576
buffer_load_dwordx4 v29,s[40:43],s61 offen offset:256  lds
ds_read_b64_tr_b16 a[34:35],v32 offset:25088
ds_read_b64_tr_b16 a[36:37],v30 offset:28672
ds_read_b64_tr_b16 a[38:39],v30 offset:29184
ds_read_b64_tr_b16 a[40:41],v31 offset:28672
ds_read_b64_tr_b16 a[42:43],v31 offset:29184
ds_read_b64_tr_b16 a[44:45],v32 offset:28672
ds_read_b64_tr_b16 a[46:47],v32 offset:29184
s_waitcnt lgkmcnt(12)
v_mfma_f32_16x16x32_bf16 v[16:19],a[0:3],v[84:87],0
v_mfma_f32_16x16x32_bf16 v[20:23],a[4:7],v[84:87],0
v_mfma_f32_16x16x32_bf16 v[24:27],a[8:11],v[84:87],0
v_mfma_f32_16x16x32_bf16 v[16:19],a[12:15],v[84:87],v[16:19]
v_mfma_f32_16x16x32_bf16 v[20:23],a[16:19],v[84:87],v[20:23]
v_mfma_f32_16x16x32_bf16 v[24:27],a[20:23],v[84:87],v[24:27]
s_waitcnt vmcnt(18)
s_barrier
s_add_u32 m0,s88,0
s_add_u32 s61,s82,2752512
buffer_load_dwordx4 v28,s[40:43],s61 offen offset:0  lds
s_add_u32 m0,s88,1024
ds_read_b64_tr_b16 a[48:49],v40 offset:0
buffer_load_dwordx4 v29,s[40:43],s61 offen offset:0  lds
s_add_u32 m0,s88,8064
ds_read_b64_tr_b16 a[50:51],v40 offset:512
buffer_load_dwordx4 v28,s[40:43],s61 offen offset:128  lds
s_add_u32 m0,s88,9088
ds_read_b64_tr_b16 a[52:53],v41 offset:0
buffer_load_dwordx4 v29,s[40:43],s61 offen offset:128  lds
s_add_u32 m0,s88,16128
ds_read_b64_tr_b16 a[54:55],v41 offset:512
buffer_load_dwordx4 v28,s[40:43],s61 offen offset:256  lds
s_add_u32 m0,s88,17152
ds_read_b64_tr_b16 a[56:57],v42 offset:0
buffer_load_dwordx4 v29,s[40:43],s61 offen offset:256  lds
ds_read_b64_tr_b16 a[58:59],v42 offset:512
ds_read_b64_tr_b16 a[60:61],v40 offset:4096
ds_read_b64_tr_b16 a[62:63],v40 offset:4608
ds_read_b64_tr_b16 a[64:65],v41 offset:4096
ds_read_b64_tr_b16 a[66:67],v41 offset:4608)"
R"(
ds_read_b64_tr_b16 a[68:69],v42 offset:4096
ds_read_b64_tr_b16 a[70:71],v42 offset:4608
s_waitcnt lgkmcnt(12)
v_mfma_f32_16x16x32_bf16 v[16:19],a[24:27],v[84:87],v[16:19]
v_mfma_f32_16x16x32_bf16 v[20:23],a[28:31],v[84:87],v[20:23]
v_mfma_f32_16x16x32_bf16 v[24:27],a[32:35],v[84:87],v[24:27]
v_mfma_f32_16x16x32_bf16 v[16:19],a[36:39],v[84:87],v[16:19]
v_mfma_f32_16x16x32_bf16 v[20:23],a[40:43],v[84:87],v[20:23]
v_mfma_f32_16x16x32_bf16 v[24:27],a[44:47],v[84:87],v[24:27]
s_waitcnt vmcnt(18)
s_barrier
s_add_u32 m0,s88,24576
s_add_u32 s61,s82,3211264
buffer_load_dwordx4 v28,s[40:43],s61 offen offset:0  lds
s_add_u32 m0,s88,25600
ds_read_b64_tr_b16 a[72:73],v40 offset:24576
buffer_load_dwordx4 v29,s[40:43],s61 offen offset:0  lds
s_add_u32 m0,s88,32640
ds_read_b64_tr_b16 a[74:75],v40 offset:25088
buffer_load_dwordx4 v28,s[40:43],s61 offen offset:128  lds
s_add_u32 m0,s88,33664
ds_read_b64_tr_b16 a[76:77],v41 offset:24576
buffer_load_dwordx4 v29,s[40:43],s61 offen offset:128  lds
s_add_u32 m0,s88,40704
ds_read_b64_tr_b16 a[78:79],v41 offset:25088
buffer_load_dwordx4 v28,s[40:43],s61 offen offset:256  lds
s_add_u32 m0,s88,41728
ds_read_b64_tr_b16 a[80:81],v42 offset:24576
buffer_load_dwordx4 v29,s[40:43],s61 offen offset:256  lds
ds_read_b64_tr_b16 a[82:83],v42 offset:25088
ds_read_b64_tr_b16 a[84:85],v40 offset:28672
ds_read_b64_tr_b16 a[86:87],v40 offset:29184
ds_read_b64_tr_b16 a[88:89],v41 offset:28672
ds_read_b64_tr_b16 a[90:91],v41 offset:29184
ds_read_b64_tr_b16 a[92:93],v42 offset:28672
ds_read_b64_tr_b16 a[94:95],v42 offset:29184
s_waitcnt lgkmcnt(12)
v_mfma_f32_16x16x32_bf16 v[16:19],a[48:51],v[84:87],v[16:19]
v_mfma_f32_16x16x32_bf16 v[20:23],a[52:55],v[84:87],v[20:23]
v_mfma_f32_16x16x32_bf16 v[24:27],a[56:59],v[84:87],v[24:27]
v_mfma_f32_16x16x32_bf16 v[16:19],a[60:63],v[84:87],v[16:19]
v_mfma_f32_16x16x32_bf16 v[20:23],a[64:67],v[84:87],v[20:23]
v_mfma_f32_16x16x32_bf16 v[24:27],a[68:71],v[84:87],v[24:27]
s_waitcnt vmcnt(18)
s_barrier
s_add_u32 m0,s88,49152
s_add_u32 s61,s82,3670016
buffer_load_dwordx4 v28,s[40:43],s61 offen offset:0  lds
s_add_u32 m0,s88,50176
ds_read_b64_tr_b16 a[96:97],v43 offset:0
buffer_load_dwordx4 v29,s[40:43],s61 offen offset:0  lds
s_add_u32 m0,s88,57216
ds_read_b64_tr_b16 a[98:99],v43 offset:512
buffer_load_dwordx4 v28,s[40:43],s61 offen offset:128  lds
s_add_u32 m0,s88,58240
ds_read_b64_tr_b16 a[100:101],v44 offset:0
buffer_load_dwordx4 v29,s[40:43],s61 offen offset:128  lds
s_add_u32 m0,s88,65280
ds_read_b64_tr_b16 a[102:103],v44 offset:512
buffer_load_dwordx4 v28,s[40:43],s61 offen offset:256  lds
s_add_u32 m0,s88,66304
ds_read_b64_tr_b16 a[104:105],v45 offset:0
buffer_load_dwordx4 v29,s[40:43],s61 offen offset:256  lds
ds_read_b64_tr_b16 a[106:107],v45 offset:512
ds_read_b64_tr_b16 a[108:109],v43 offset:4096
ds_read_b64_tr_b16 a[110:111],v43 offset:4608
ds_read_b64_tr_b16 a[112:113],v44 offset:4096
ds_read_b64_tr_b16 a[114:115],v44 offset:4608
ds_read_b64_tr_b16 a[116:117],v45 offset:4096
ds_read_b64_tr_b16 a[118:119],v45 offset:4608
s_waitcnt lgkmcnt(12)
v_mfma_f32_16x16x32_bf16 v[16:19],a[72:75],v[84:87],v[16:19]
v_mfma_f32_16x16x32_bf16 v[20:23],a[76:79],v[84:87],v[20:23]
v_mfma_f32_16x16x32_bf16 v[24:27],a[80:83],v[84:87],v[24:27]
v_mfma_f32_16x16x32_bf16 v[16:19],a[84:87],v[84:87],v[16:19]
v_mfma_f32_16x16x32_bf16 v[20:23],a[88:91],v[84:87],v[20:23]
v_mfma_f32_16x16x32_bf16 v[24:27],a[92:95],v[84:87],v[24:27]
s_waitcnt vmcnt(18)
s_barrier
s_add_u32 m0,s88,73728
s_add_u32 s61,s82,4128768
buffer_load_dwordx4 v28,s[40:43],s61 offen offset:0  lds
s_add_u32 m0,s88,74752
ds_read_b64_tr_b16 a[120:121],v43 offset:24576
buffer_load_dwordx4 v29,s[40:43],s61 offen offset:0  lds
s_add_u32 m0,s88,81792
ds_read_b64_tr_b16 a[122:123],v43 offset:25088
buffer_load_dwordx4 v28,s[40:43],s61 offen offset:128  lds
s_add_u32 m0,s88,82816
ds_read_b64_tr_b16 a[124:125],v44 offset:24576
buffer_load_dwordx4 v29,s[40:43],s61 offen offset:128  lds
s_add_u32 m0,s88,89856
ds_read_b64_tr_b16 a[126:127],v44 offset:25088
buffer_load_dwordx4 v28,s[40:43],s61 offen offset:256  lds
s_add_u32 m0,s88,90880
ds_read_b64_tr_b16 a[128:129],v45 offset:24576
buffer_load_dwordx4 v29,s[40:43],s61 offen offset:256  lds
ds_read_b64_tr_b16 a[130:131],v45 offset:25088
ds_read_b64_tr_b16 a[132:133],v43 offset:28672
ds_read_b64_tr_b16 a[134:135],v43 offset:29184
ds_read_b64_tr_b16 a[136:137],v44 offset:28672
ds_read_b64_tr_b16 a[138:139],v44 offset:29184
ds_read_b64_tr_b16 a[140:141],v45 offset:28672
ds_read_b64_tr_b16 a[142:143],v45 offset:29184
s_waitcnt lgkmcnt(12)
v_mfma_f32_16x16x32_bf16 v[16:19],a[96:99],v[84:87],v[16:19]
v_mfma_f32_16x16x32_bf16 v[20:23],a[100:103],v[84:87],v[20:23]
v_mfma_f32_16x16x32_bf16 v[24:27],a[104:107],v[84:87],v[24:27]
v_mfma_f32_16x16x32_bf16 v[16:19],a[108:111],v[84:87],v[16:19]
v_mfma_f32_16x16x32_bf16 v[20:23],a[112:115],v[84:87],v[20:23]
v_mfma_f32_16x16x32_bf16 v[24:27],a[116:119],v[84:87],v[24:27]
s_waitcnt vmcnt(18)
s_barrier
s_add_u32 m0,s88,98304
s_add_u32 s61,s82,4587520
buffer_load_dwordx4 v28,s[40:43],s61 offen offset:0  lds
s_add_u32 m0,s88,99328
ds_read_b64_tr_b16 a[144:145],v30 offset:0
buffer_load_dwordx4 v29,s[40:43],s61 offen offset:0  lds
s_add_u32 m0,s88,106368
ds_read_b64_tr_b16 a[146:147],v30 offset:512
buffer_load_dwordx4 v28,s[40:43],s61 offen offset:128  lds
s_add_u32 m0,s88,107392
ds_read_b64_tr_b16 a[148:149],v31 offset:0
buffer_load_dwordx4 v29,s[40:43],s61 offen offset:128  lds
s_add_u32 m0,s88,114432
ds_read_b64_tr_b16 a[150:151],v31 offset:512
buffer_load_dwordx4 v28,s[40:43],s61 offen offset:256  lds
s_add_u32 m0,s88,115456
ds_read_b64_tr_b16 a[152:153],v32 offset:0
buffer_load_dwordx4 v29,s[40:43],s61 offen offset:256  lds
ds_read_b64_tr_b16 a[154:155],v32 offset:512
ds_read_b64_tr_b16 a[156:157],v30 offset:4096
ds_read_b64_tr_b16 a[158:159],v30 offset:4608
ds_read_b64_tr_b16 a[160:161],v31 offset:4096
ds_read_b64_tr_b16 a[162:163],v31 offset:4608
ds_read_b64_tr_b16 a[164:165],v32 offset:4096
ds_read_b64_tr_b16 a[166:167],v32 offset:4608
s_waitcnt lgkmcnt(12)
v_mfma_f32_16x16x32_bf16 v[16:19],a[120:123],v[84:87],v[16:19]
v_mfma_f32_16x16x32_bf16 v[20:23],a[124:127],v[84:87],v[20:23]
v_mfma_f32_16x16x32_bf16 v[24:27],a[128:131],v[84:87],v[24:27]
v_mfma_f32_16x16x32_bf16 v[16:19],a[132:135],v[84:87],v[16:19]
v_mfma_f32_16x16x32_bf16 v[20:23],a[136:139],v[84:87],v[20:23]
v_mfma_f32_16x16x32_bf16 v[24:27],a[140:143],v[84:87],v[24:27]
s_waitcnt vmcnt(18)
s_barrier
s_add_u32 m0,s88,122880
s_add_u32 s61,s82,5046272
buffer_load_dwordx4 v28,s[40:43],s61 offen offset:0  lds
s_add_u32 m0,s88,123904
ds_read_b64_tr_b16 a[168:169],v30 offset:24576
buffer_load_dwordx4 v29,s[40:43],s61 offen offset:0  lds
s_add_u32 m0,s88,130944
ds_read_b64_tr_b16 a[170:171],v30 offset:25088
buffer_load_dwordx4 v28,s[40:43],s61 offen offset:128  lds
s_add_u32 m0,s88,131968
ds_read_b64_tr_b16 a[172:173],v31 offset:24576
buffer_load_dwordx4 v29,s[40:43],s61 offen offset:128  lds
s_add_u32 m0,s88,139008
ds_read_b64_tr_b16 a[174:175],v31 offset:25088
buffer_load_dwordx4 v28,s[40:43],s61 offen offset:256  lds
s_add_u32 m0,s88,140032
ds_read_b64_tr_b16 a[176:177],v32 offset:24576
buffer_load_dwordx4 v29,s[40:43],s61 offen offset:256  lds
ds_read_b64_tr_b16 a[178:179],v32 offset:25088
ds_read_b64_tr_b16 a[180:181],v30 offset:28672
ds_read_b64_tr_b16 a[182:183],v30 offset:29184
ds_read_b64_tr_b16 a[184:185],v31 offset:28672
ds_read_b64_tr_b16 a[186:187],v31 offset:29184
ds_read_b64_tr_b16 a[188:189],v32 offset:28672
ds_read_b64_tr_b16 a[190:191],v32 offset:29184
s_waitcnt lgkmcnt(12)
v_mfma_f32_16x16x32_bf16 v[16:19],a[144:147],v[84:87],v[16:19]
v_mfma_f32_16x16x32_bf16 v[20:23],a[148:151],v[84:87],v[20:23]
v_mfma_f32_16x16x32_bf16 v[24:27],a[152:155],v[84:87],v[24:27]
v_mfma_f32_16x16x32_bf16 v[16:19],a[156:159],v[84:87],v[16:19]
v_mfma_f32_16x16x32_bf16 v[20:23],a[160:163],v[84:87],v[20:23]
v_mfma_f32_16x16x32_bf16 v[24:27],a[164:167],v[84:87],v[24:27]
s_waitcnt vmcnt(18)
s_barrier
s_add_u32 m0,s88,0
s_add_u32 s61,s82,5505024
buffer_load_dwordx4 v28,s[40:43],s61 offen offset:0  lds
s_add_u32 m0,s88,1024
ds_read_b64_tr_b16 a[192:193],v40 offset:0
buffer_load_dwordx4 v29,s[40:43],s61 offen offset:0  lds
s_add_u32 m0,s88,8064
ds_read_b64_tr_b16 a[194:195],v40 offset:512
buffer_load_dwordx4 v28,s[40:43],s61 offen offset:128  lds
s_add_u32 m0,s88,9088
ds_read_b64_tr_b16 a[196:197],v41 offset:0
buffer_load_dwordx4 v29,s[40:43],s61 offen offset:128  lds
s_add_u32 m0,s88,16128
ds_read_b64_tr_b16 a[198:199],v41 offset:512
buffer_load_dwordx4 v28,s[40:43],s61 offen offset:256  lds
s_add_u32 m0,s88,17152
ds_read_b64_tr_b16 a[200:201],v42 offset:0
buffer_load_dwordx4 v29,s[40:43],s61 offen offset:256  lds
ds_read_b64_tr_b16 a[202:203],v42 offset:512)"
R"(
ds_read_b64_tr_b16 a[204:205],v40 offset:4096
ds_read_b64_tr_b16 a[206:207],v40 offset:4608
ds_read_b64_tr_b16 a[208:209],v41 offset:4096
ds_read_b64_tr_b16 a[210:211],v41 offset:4608
ds_read_b64_tr_b16 a[212:213],v42 offset:4096
ds_read_b64_tr_b16 a[214:215],v42 offset:4608
s_waitcnt lgkmcnt(12)
v_mfma_f32_16x16x32_bf16 v[16:19],a[168:171],v[84:87],v[16:19]
v_mfma_f32_16x16x32_bf16 v[20:23],a[172:175],v[84:87],v[20:23]
v_mfma_f32_16x16x32_bf16 v[24:27],a[176:179],v[84:87],v[24:27]
v_mfma_f32_16x16x32_bf16 v[16:19],a[180:183],v[84:87],v[16:19]
v_mfma_f32_16x16x32_bf16 v[20:23],a[184:187],v[84:87],v[20:23]
v_mfma_f32_16x16x32_bf16 v[24:27],a[188:191],v[84:87],v[24:27]
s_waitcnt vmcnt(18)
s_barrier
s_add_u32 m0,s88,24576
s_add_u32 s61,s82,5963776
buffer_load_dwordx4 v28,s[40:43],s61 offen offset:0  lds
s_add_u32 m0,s88,25600
ds_read_b64_tr_b16 a[216:217],v40 offset:24576
buffer_load_dwordx4 v29,s[40:43],s61 offen offset:0  lds
s_add_u32 m0,s88,32640
ds_read_b64_tr_b16 a[218:219],v40 offset:25088
buffer_load_dwordx4 v28,s[40:43],s61 offen offset:128  lds
s_add_u32 m0,s88,33664
ds_read_b64_tr_b16 a[220:221],v41 offset:24576
buffer_load_dwordx4 v29,s[40:43],s61 offen offset:128  lds
s_add_u32 m0,s88,40704
ds_read_b64_tr_b16 a[222:223],v41 offset:25088
buffer_load_dwordx4 v28,s[40:43],s61 offen offset:256  lds
s_add_u32 m0,s88,41728
ds_read_b64_tr_b16 a[224:225],v42 offset:24576
buffer_load_dwordx4 v29,s[40:43],s61 offen offset:256  lds
ds_read_b64_tr_b16 a[226:227],v42 offset:25088
ds_read_b64_tr_b16 a[228:229],v40 offset:28672
ds_read_b64_tr_b16 a[230:231],v40 offset:29184
ds_read_b64_tr_b16 a[232:233],v41 offset:28672
ds_read_b64_tr_b16 a[234:235],v41 offset:29184
ds_read_b64_tr_b16 a[236:237],v42 offset:28672
ds_read_b64_tr_b16 a[238:239],v42 offset:29184
s_waitcnt lgkmcnt(12)
v_mfma_f32_16x16x32_bf16 v[16:19],a[192:195],v[84:87],v[16:19]
v_mfma_f32_16x16x32_bf16 v[20:23],a[196:199],v[84:87],v[20:23]
v_mfma_f32_16x16x32_bf16 v[24:27],a[200:203],v[84:87],v[24:27]
v_mfma_f32_16x16x32_bf16 v[16:19],a[204:207],v[84:87],v[16:19]
v_mfma_f32_16x16x32_bf16 v[20:23],a[208:211],v[84:87],v[20:23]
v_mfma_f32_16x16x32_bf16 v[24:27],a[212:215],v[84:87],v[24:27]
s_waitcnt vmcnt(18)
s_barrier
s_add_u32 m0,s88,49152
s_add_u32 s61,s82,6422528
buffer_load_dwordx4 v28,s[40:43],s61 offen offset:0  lds
s_add_u32 m0,s88,50176
ds_read_b64_tr_b16 a[240:241],v43 offset:0
buffer_load_dwordx4 v29,s[40:43],s61 offen offset:0  lds
s_add_u32 m0,s88,57216
ds_read_b64_tr_b16 a[242:243],v43 offset:512
buffer_load_dwordx4 v28,s[40:43],s61 offen offset:128  lds
s_add_u32 m0,s88,58240
ds_read_b64_tr_b16 a[244:245],v44 offset:0
buffer_load_dwordx4 v29,s[40:43],s61 offen offset:128  lds
s_add_u32 m0,s88,65280
ds_read_b64_tr_b16 a[246:247],v44 offset:512
buffer_load_dwordx4 v28,s[40:43],s61 offen offset:256  lds
s_add_u32 m0,s88,66304
ds_read_b64_tr_b16 a[248:249],v45 offset:0
buffer_load_dwordx4 v29,s[40:43],s61 offen offset:256  lds
ds_read_b64_tr_b16 a[250:251],v45 offset:512
ds_read_b64_tr_b16 a[252:253],v43 offset:4096
ds_read_b64_tr_b16 a[254:255],v43 offset:4608
ds_read_b64_tr_b16 v[128:129],v44 offset:4096
ds_read_b64_tr_b16 v[130:131],v44 offset:4608
ds_read_b64_tr_b16 v[132:133],v45 offset:4096
ds_read_b64_tr_b16 v[134:135],v45 offset:4608
s_waitcnt lgkmcnt(12)
v_mfma_f32_16x16x32_bf16 v[16:19],a[216:219],v[84:87],v[16:19]
v_mfma_f32_16x16x32_bf16 v[20:23],a[220:223],v[84:87],v[20:23]
v_mfma_f32_16x16x32_bf16 v[24:27],a[224:227],v[84:87],v[24:27]
v_mfma_f32_16x16x32_bf16 v[16:19],a[228:231],v[84:87],v[16:19]
v_mfma_f32_16x16x32_bf16 v[20:23],a[232:235],v[84:87],v[20:23]
v_mfma_f32_16x16x32_bf16 v[24:27],a[236:239],v[84:87],v[24:27]
s_waitcnt vmcnt(18)
s_barrier
s_add_u32 m0,s88,73728
s_add_u32 s61,s82,6881280
buffer_load_dwordx4 v28,s[40:43],s61 offen offset:0  lds
s_add_u32 m0,s88,74752
ds_read_b64_tr_b16 v[136:137],v43 offset:24576
buffer_load_dwordx4 v29,s[40:43],s61 offen offset:0  lds
s_add_u32 m0,s88,81792
ds_read_b64_tr_b16 v[138:139],v43 offset:25088
buffer_load_dwordx4 v28,s[40:43],s61 offen offset:128  lds
s_add_u32 m0,s88,82816
ds_read_b64_tr_b16 v[140:141],v44 offset:24576
buffer_load_dwordx4 v29,s[40:43],s61 offen offset:128  lds
s_add_u32 m0,s88,89856
ds_read_b64_tr_b16 v[142:143],v44 offset:25088
buffer_load_dwordx4 v28,s[40:43],s61 offen offset:256  lds
s_add_u32 m0,s88,90880
ds_read_b64_tr_b16 v[144:145],v45 offset:24576
buffer_load_dwordx4 v29,s[40:43],s61 offen offset:256  lds
ds_read_b64_tr_b16 v[146:147],v45 offset:25088
ds_read_b64_tr_b16 v[148:149],v43 offset:28672
ds_read_b64_tr_b16 v[150:151],v43 offset:29184
ds_read_b64_tr_b16 v[152:153],v44 offset:28672
ds_read_b64_tr_b16 v[154:155],v44 offset:29184
ds_read_b64_tr_b16 v[156:157],v45 offset:28672
ds_read_b64_tr_b16 v[158:159],v45 offset:29184
s_getpc_b64 s[46:47]
Lg3_cp_%=:
s_add_u32 s46,s46,Lg3_init_%=-Lg3_cp_%=
s_addc_u32 s47,s47,0
s_lshl_b32 s68,s54,13
v_lshl_add_u32 v68,v2,3,s68
v_min_u32 v69,Lg3_done_%=-Lg3_init_%=,v68
global_load_dword v72,v69,s[46:47]
s_waitcnt lgkmcnt(12)
v_mfma_f32_16x16x32_bf16 v[16:19],a[240:243],v[84:87],v[16:19]
v_mfma_f32_16x16x32_bf16 v[20:23],a[244:247],v[84:87],v[20:23]
v_mfma_f32_16x16x32_bf16 v[24:27],a[248:251],v[84:87],v[24:27]
v_mfma_f32_16x16x32_bf16 v[16:19],a[252:255],v[84:87],v[16:19]
v_mfma_f32_16x16x32_bf16 v[20:23],v[128:131],v[84:87],v[20:23]
v_mfma_f32_16x16x32_bf16 v[24:27],v[132:135],v[84:87],v[24:27]
s_waitcnt vmcnt(19)
s_barrier
ds_read_b64_tr_b16 v[160:161],v30 offset:0
ds_read_b64_tr_b16 v[162:163],v30 offset:512
ds_read_b64_tr_b16 v[164:165],v31 offset:0
ds_read_b64_tr_b16 v[166:167],v31 offset:512
ds_read_b64_tr_b16 v[168:169],v32 offset:0
ds_read_b64_tr_b16 v[170:171],v32 offset:512
ds_read_b64_tr_b16 v[172:173],v30 offset:4096
ds_read_b64_tr_b16 v[174:175],v30 offset:4608
ds_read_b64_tr_b16 v[176:177],v31 offset:4096
ds_read_b64_tr_b16 v[178:179],v31 offset:4608
ds_read_b64_tr_b16 v[180:181],v32 offset:4096
ds_read_b64_tr_b16 v[182:183],v32 offset:4608
s_waitcnt lgkmcnt(12)
v_mfma_f32_16x16x32_bf16 v[16:19],v[136:139],v[84:87],v[16:19]
v_mfma_f32_16x16x32_bf16 v[20:23],v[140:143],v[84:87],v[20:23]
v_mfma_f32_16x16x32_bf16 v[24:27],v[144:147],v[84:87],v[24:27]
v_mfma_f32_16x16x32_bf16 v[16:19],v[148:151],v[84:87],v[16:19]
v_mfma_f32_16x16x32_bf16 v[20:23],v[152:155],v[84:87],v[20:23]
v_mfma_f32_16x16x32_bf16 v[24:27],v[156:159],v[84:87],v[24:27]
s_waitcnt vmcnt(13)
s_barrier
ds_read_b64_tr_b16 v[184:185],v30 offset:24576
ds_read_b64_tr_b16 v[186:187],v30 offset:25088
ds_read_b64_tr_b16 v[188:189],v31 offset:24576
ds_read_b64_tr_b16 v[190:191],v31 offset:25088
ds_read_b64_tr_b16 v[192:193],v32 offset:24576
ds_read_b64_tr_b16 v[194:195],v32 offset:25088
ds_read_b64_tr_b16 v[196:197],v30 offset:28672
ds_read_b64_tr_b16 v[198:199],v30 offset:29184
ds_read_b64_tr_b16 v[200:201],v31 offset:28672
ds_read_b64_tr_b16 v[202:203],v31 offset:29184
ds_read_b64_tr_b16 v[204:205],v32 offset:28672
ds_read_b64_tr_b16 v[206:207],v32 offset:29184
s_waitcnt lgkmcnt(12)
v_mfma_f32_16x16x32_bf16 v[16:19],v[160:163],v[84:87],v[16:19]
v_mfma_f32_16x16x32_bf16 v[20:23],v[164:167],v[84:87],v[20:23]
v_mfma_f32_16x16x32_bf16 v[24:27],v[168:171],v[84:87],v[24:27]
v_mfma_f32_16x16x32_bf16 v[16:19],v[172:175],v[84:87],v[16:19]
v_mfma_f32_16x16x32_bf16 v[20:23],v[176:179],v[84:87],v[20:23]
v_mfma_f32_16x16x32_bf16 v[24:27],v[180:183],v[84:87],v[24:27]
s_waitcnt vmcnt(7)
s_barrier
ds_read_b64_tr_b16 v[208:209],v40 offset:0
ds_read_b64_tr_b16 v[210:211],v40 offset:512
ds_read_b64_tr_b16 v[212:213],v41 offset:0
ds_read_b64_tr_b16 v[214:215],v41 offset:512
ds_read_b64_tr_b16 v[216:217],v42 offset:0
ds_read_b64_tr_b16 v[218:219],v42 offset:512
ds_read_b64_tr_b16 v[220:221],v40 offset:4096
ds_read_b64_tr_b16 v[222:223],v40 offset:4608
ds_read_b64_tr_b16 v[224:225],v41 offset:4096
ds_read_b64_tr_b16 v[226:227],v41 offset:4608
ds_read_b64_tr_b16 v[228:229],v42 offset:4096
ds_read_b64_tr_b16 v[230:231],v42 offset:4608
s_waitcnt lgkmcnt(12)
v_mfma_f32_16x16x32_bf16 v[16:19],v[184:187],v[84:87],v[16:19]
v_mfma_f32_16x16x32_bf16 v[20:23],v[188:191],v[84:87],v[20:23]
v_mfma_f32_16x16x32_bf16 v[24:27],v[192:195],v[84:87],v[24:27]
v_mfma_f32_16x16x32_bf16 v[16:19],v[196:199],v[84:87],v[16:19]
v_mfma_f32_16x16x32_bf16 v[20:23],v[200:203],v[84:87],v[20:23]
v_mfma_f32_16x16x32_bf16 v[24:27],v[204:207],v[84:87],v[24:27]
s_waitcnt vmcnt(1)
s_barrier
ds_read_b64_tr_b16 v[232:233],v40 offset:24576
ds_read_b64_tr_b16 v[234:235],v40 offset:25088
ds_read_b64_tr_b16 v[236:237],v41 offset:24576
ds_read_b64_tr_b16 v[238:239],v41 offset:25088
ds_read_b64_tr_b16 v[240:241],v42 offset:24576
ds_read_b64_tr_b16 v[242:243],v42 offset:25088
ds_read_b64_tr_b16 v[244:245],v40 offset:28672
ds_read_b64_tr_b16 v[246:247],v40 offset:29184
ds_read_b64_tr_b16 v[248:249],v41 offset:28672
ds_read_b64_tr_b16 v[250:251],v41 offset:29184
ds_read_b64_tr_b16 v[252:253],v42 offset:28672
ds_read_b64_tr_b16 v[254:255],v42 offset:29184)"
R"(
s_waitcnt lgkmcnt(12)
v_mfma_f32_16x16x32_bf16 v[16:19],v[208:211],v[84:87],v[16:19]
v_mfma_f32_16x16x32_bf16 v[20:23],v[212:215],v[84:87],v[20:23]
v_mfma_f32_16x16x32_bf16 v[24:27],v[216:219],v[84:87],v[24:27]
v_mfma_f32_16x16x32_bf16 v[16:19],v[220:223],v[84:87],v[16:19]
v_mfma_f32_16x16x32_bf16 v[20:23],v[224:227],v[84:87],v[20:23]
v_mfma_f32_16x16x32_bf16 v[24:27],v[228:231],v[84:87],v[24:27]
s_waitcnt lgkmcnt(0)
v_mfma_f32_16x16x32_bf16 v[16:19],v[232:235],v[84:87],v[16:19]
v_mfma_f32_16x16x32_bf16 v[20:23],v[236:239],v[84:87],v[20:23]
v_mfma_f32_16x16x32_bf16 v[24:27],v[240:243],v[84:87],v[24:27]
v_mfma_f32_16x16x32_bf16 v[16:19],v[244:247],v[84:87],v[16:19]
v_mfma_f32_16x16x32_bf16 v[20:23],v[248:251],v[84:87],v[20:23]
v_mfma_f32_16x16x32_bf16 v[24:27],v[252:255],v[84:87],v[24:27]
s_waitcnt lgkmcnt(0)
s_barrier
v_readfirstlane_b32 s60,v5
s_nop 3
s_add_u32 s60,s60,1
s_cmp_eq_u32 s60,0
s_cselect_b32 s60,1,s60
Lg3_init_%=:
v_mov_b32 v5,s60
s_mov_b32 s59,536870912
s_add_u32 s57,s56,131072
s_mov_b32 s79,0
s_mov_b32 s75,0x3a800000
s_mov_b32 s76,0xc0135761
s_mov_b32 s77,0x3d372713
s_mov_b32 s44,s59
s_mov_b32 s45,s59
s_mov_b32 s46,s59
s_mov_b32 s47,s59
s_mov_b32 s48,s83
s_add_u32 s49,s48,s84
s_add_u32 s50,s49,s55
s_cmp_eq_u32 s50,s56
s_cselect_b32 s50,s57,s50
s_cmp_eq_u32 s50,s58
s_cselect_b32 s50,s59,s50
s_add_u32 s51,s50,s55
s_cmp_eq_u32 s51,s56
s_cselect_b32 s51,s57,s51
s_cmp_eq_u32 s51,s58
s_cselect_b32 s51,s59,s51
s_add_u32 s52,s51,s55
s_cmp_eq_u32 s52,s56
s_cselect_b32 s52,s57,s52
s_cmp_eq_u32 s52,s58
s_cselect_b32 s52,s59,s52
s_lshl_b32 s63,s54,12
s_add_u32 s63,s63,65536
s_add_u32 s64,s63,32768
s_lshl_b32 s88,s54,10
s_add_u32 s88,s88,131072
s_lshl_b32 s78,s54,11
s_add_u32 s78,s78,132608
v_add_u32 v1,16384,v0
v_mov_b32 v12,0x3727c5ac
s_getpc_b64 s[90:91]
Lg3_pc_%=:
s_mov_b32 s68,Lg3_d1w0_%=-Lg3_pc_%=
s_cmp_eq_u32 s54,1
s_cselect_b32 s68,Lg3_d1w1_%=-Lg3_pc_%=,s68
s_cmp_eq_u32 s54,2
s_cselect_b32 s68,Lg3_d1w2_%=-Lg3_pc_%=,s68
s_cmp_eq_u32 s54,3
s_cselect_b32 s68,Lg3_d1w3_%=-Lg3_pc_%=,s68
s_add_u32 s92,s90,s68
s_addc_u32 s93,s91,0
s_mov_b32 s68,Lg3_d0w0_%=-Lg3_pc_%=
s_cmp_eq_u32 s54,1
s_cselect_b32 s68,Lg3_d0w1_%=-Lg3_pc_%=,s68
s_cmp_eq_u32 s54,2
s_cselect_b32 s68,Lg3_d0w2_%=-Lg3_pc_%=,s68
s_cmp_eq_u32 s54,3
s_cselect_b32 s68,Lg3_d0w3_%=-Lg3_pc_%=,s68
s_add_u32 s90,s90,s68
s_addc_u32 s91,s91,0
s_mov_b32 s66,s59
s_mov_b32 s65,0
s_mov_b32 s62,s63
s_branch Lg3_slow0_%=
Lg3_d0w0_%=:
v_mfma_f32_16x16x32_bf16 v[28:31],a[0:3],v[52:55],0
s_mov_b32 m0,s62
ds_read_b128 v[72:75],v2 offset:5120
v_mul_f32 v116,s75,v112
v_mfma_f32_16x16x32_bf16 v[32:35],a[4:7],v[52:55],0
buffer_load_dwordx4 v0,s[36:39],s61 offen offset:0 sc1 lds
v_mfma_f32_16x16x32_bf16 v[36:39],a[8:11],v[52:55],0
s_mul_i32 s73,s47,7
v_fma_f32 v113,v113,s75,v12
v_fma_f32 v113,-v116,v116,v113
v_mfma_f32_16x16x32_bf16 v[28:31],a[12:15],v[56:59],v[28:31]
s_lshr_b32 s73,s73,1
ds_read_b128 v[76:79],v2 offset:6144
v_max_f32 v113,v113,v12
v_mfma_f32_16x16x32_bf16 v[32:35],a[16:19],v[56:59],v[32:35]
s_add_u32 s53,s52,s55
v_rsq_f32 v112,v113
v_mfma_f32_16x16x32_bf16 v[36:39],a[20:23],v[56:59],v[36:39]
s_waitcnt lgkmcnt(3)
buffer_load_dwordx4 v0,s[36:39],s61 offen offset:1024 sc1 lds
v_mfma_f32_16x16x32_bf16 v[28:31],a[24:27],v[60:63],v[28:31]
s_cmp_eq_u32 s53,s56
ds_read_b128 v[80:83],v2 offset:7168
v_mul_f32 v117,s76,v112
v_mfma_f32_16x16x32_bf16 v[32:35],a[28:31],v[60:63],v[32:35]
s_cselect_b32 s53,s57,s53
v_mul_f32 v118,v112,v112
v_mul_f32 v119,v113,v112
v_mfma_f32_16x16x32_bf16 v[36:39],a[32:35],v[60:63],v[36:39]
s_cmp_eq_u32 s53,s58
v_mul_f32 v118,s77,v118
v_mul_f32 v118,v118,v117
v_mfma_f32_16x16x32_bf16 v[28:31],a[36:39],v[64:67],v[28:31]
buffer_load_dwordx4 v0,s[36:39],s61 offen offset:2048 sc1 lds
v_mfma_f32_16x16x32_bf16 v[32:35],a[40:43],v[64:67],v[32:35]
s_cselect_b32 s53,s59,s53
ds_read_b128 v[52:55],v2 offset:8192
v_fma_f32 v40,-v116,v16,v40
v_mfma_f32_16x16x32_bf16 v[36:39],a[44:47],v[64:67],v[36:39]
s_waitcnt lgkmcnt(3)
v_fma_f32 v41,-v116,v17,v41
v_fma_f32 v42,-v116,v18,v42
v_mfma_f32_16x16x32_bf16 v[28:31],a[48:51],v[68:71],v[28:31]
s_cmp_ge_u32 s66,s59
ds_read_b128 v[56:59],v2 offset:9216
v_fma_f32 v43,-v116,v19,v43
v_mfma_f32_16x16x32_bf16 v[32:35],a[52:55],v[68:71],v[32:35]
buffer_load_dwordx4 v0,s[36:39],s61 offen offset:3072 sc1 lds
v_mfma_f32_16x16x32_bf16 v[36:39],a[56:59],v[68:71],v[36:39]
s_cselect_b32 s86,s49,s50
v_mul_f32 v84,v40,v40
v_mul_f32 v85,v41,v41
v_mfma_f32_16x16x32_bf16 v[28:31],a[60:63],v[72:75],v[28:31]
s_lshr_b32 s68,s86,5
ds_read_b128 v[60:63],v2 offset:10240
v_mul_f32 v86,v42,v42
v_mfma_f32_16x16x32_bf16 v[32:35],a[64:67],v[72:75],v[32:35]
s_add_u32 m0,s78,0
v_mul_f32 v87,v43,v43
v_fma_f32 v84,v118,v84,v117
v_mfma_f32_16x16x32_bf16 v[36:39],a[68:71],v[72:75],v[36:39]
s_waitcnt lgkmcnt(3)
v_mul_f32 v84,v40,v84
v_fma_f32 v85,v118,v85,v117
v_mfma_f32_16x16x32_bf16 v[28:31],a[72:75],v[76:79],v[28:31]
ds_read_b128 v[64:67],v2 offset:11264
v_mul_f32 v85,v41,v85
v_mfma_f32_16x16x32_bf16 v[32:35],a[76:79],v[76:79],v[32:35]
v_exp_f32 v84,v84
v_mfma_f32_16x16x32_bf16 v[36:39],a[80:83],v[76:79],v[36:39]
v_exp_f32 v85,v85
v_mfma_f32_16x16x32_bf16 v[28:31],a[84:87],v[80:83],v[28:31]
ds_read_b128 v[68:71],v2 offset:12288
v_fma_f32 v84,v84,v119,v119
v_mfma_f32_16x16x32_bf16 v[32:35],a[88:91],v[80:83],v[32:35]
v_rcp_f32 v84,v84
v_mfma_f32_16x16x32_bf16 v[36:39],a[92:95],v[80:83],v[36:39]
s_waitcnt lgkmcnt(3)
v_fma_f32 v85,v85,v119,v119
v_fma_f32 v86,v118,v86,v117
v_mfma_f32_16x16x32_bf16 v[28:31],a[96:99],v[52:55],v[28:31]
ds_read_b128 v[72:75],v2 offset:13312
v_mul_f32 v86,v42,v86
v_mfma_f32_16x16x32_bf16 v[32:35],a[100:103],v[52:55],v[32:35]
v_exp_f32 v86,v86
v_mfma_f32_16x16x32_bf16 v[36:39],a[104:107],v[52:55],v[36:39]
v_rcp_f32 v85,v85
v_mfma_f32_16x16x32_bf16 v[28:31],a[108:111],v[56:59],v[28:31]
ds_read_b128 v[76:79],v2 offset:14336
v_fma_f32 v86,v86,v119,v119
v_mfma_f32_16x16x32_bf16 v[32:35],a[112:115],v[56:59],v[32:35]
v_rcp_f32 v86,v86
v_mfma_f32_16x16x32_bf16 v[36:39],a[116:119],v[56:59],v[36:39]
s_waitcnt lgkmcnt(3)
v_fma_f32 v87,v118,v87,v117
v_mul_f32 v87,v43,v87
v_mfma_f32_16x16x32_bf16 v[28:31],a[120:123],v[60:63],v[28:31]
ds_read_b128 v[80:83],v2 offset:15360
v_mul_f32 v40,v40,v84
v_mfma_f32_16x16x32_bf16 v[32:35],a[124:127],v[60:63],v[32:35]
s_waitcnt vmcnt(6)
v_exp_f32 v87,v87
v_mfma_f32_16x16x32_bf16 v[36:39],a[128:131],v[60:63],v[36:39]
s_barrier
v_mfma_f32_16x16x32_bf16 v[28:31],a[132:135],v[64:67],v[28:31]
ds_read_b128 v[52:55],v2 offset:16384
v_fma_f32 v87,v87,v119,v119
v_mfma_f32_16x16x32_bf16 v[32:35],a[136:139],v[64:67],v[32:35]
v_rcp_f32 v87,v87
v_mfma_f32_16x16x32_bf16 v[36:39],a[140:143],v[64:67],v[36:39]
s_waitcnt lgkmcnt(3)
v_add_f32_dpp v104,v104,v104 quad_perm:[1,0,3,2] row_mask:0xf bank_mask:0xf
v_mul_f32 v41,v41,v85
v_mfma_f32_16x16x32_bf16 v[28:31],a[144:147],v[68:71],v[28:31]
ds_read_b128 v[56:59],v2 offset:17408
v_add_f32_dpp v104,v104,v104 quad_perm:[2,3,0,1] row_mask:0xf bank_mask:0xf)"
R"(
v_mfma_f32_16x16x32_bf16 v[32:35],a[148:151],v[68:71],v[32:35]
s_nop 0
v_add_f32_dpp v104,v104,v104 row_half_mirror row_mask:0xf bank_mask:0xf
v_mul_f32 v42,v42,v86
v_mfma_f32_16x16x32_bf16 v[36:39],a[152:155],v[68:71],v[36:39]
v_mul_f32 v43,v43,v87
v_cvt_pk_bf16_f32 v96,v40,v41
v_mfma_f32_16x16x32_bf16 v[28:31],a[156:159],v[72:75],v[28:31]
ds_read_b128 v[60:63],v2 offset:18432
v_cvt_pk_bf16_f32 v97,v42,v43
v_mfma_f32_16x16x32_bf16 v[32:35],a[160:163],v[72:75],v[32:35]
buffer_load_dword v6,s[36:39],s68 offen offset:0 sc1 lds
v_mfma_f32_16x16x32_bf16 v[36:39],a[164:167],v[72:75],v[36:39]
s_waitcnt lgkmcnt(3)
v_fma_f32 v44,-v116,v20,v44
v_fma_f32 v45,-v116,v21,v45
v_mfma_f32_16x16x32_bf16 v[28:31],a[168:171],v[76:79],v[28:31]
s_add_u32 m0,s62,16384
ds_read_b128 v[64:67],v2 offset:19456
v_fma_f32 v46,-v116,v22,v46
v_mfma_f32_16x16x32_bf16 v[32:35],a[172:175],v[76:79],v[32:35]
buffer_load_dwordx4 v1,s[36:39],s61 offen offset:0 sc1 lds
v_mfma_f32_16x16x32_bf16 v[36:39],a[176:179],v[76:79],v[36:39]
s_lshr_b32 s72,s61,5
v_fma_f32 v47,-v116,v23,v47
v_mul_f32 v88,v44,v44
v_mfma_f32_16x16x32_bf16 v[28:31],a[180:183],v[80:83],v[28:31]
s_mov_b32 s62,s63
ds_read_b128 v[68:71],v2 offset:20480
v_mul_f32 v89,v45,v45
v_mfma_f32_16x16x32_bf16 v[32:35],a[184:187],v[80:83],v[32:35]
ds_write_b32 v10,v104 offset:0
v_mfma_f32_16x16x32_bf16 v[36:39],a[188:191],v[80:83],v[36:39]
s_waitcnt lgkmcnt(4)
buffer_load_dwordx4 v1,s[36:39],s61 offen offset:1024 sc1 lds
v_mfma_f32_16x16x32_bf16 v[28:31],a[192:195],v[52:55],v[28:31]
ds_read_b128 v[72:75],v2 offset:21504
v_mul_f32 v90,v46,v46
v_mfma_f32_16x16x32_bf16 v[32:35],a[196:199],v[52:55],v[32:35]
v_mul_f32 v91,v47,v47
v_fma_f32 v88,v118,v88,v117
v_mfma_f32_16x16x32_bf16 v[36:39],a[200:203],v[52:55],v[36:39]
v_mul_f32 v88,v44,v88
v_fma_f32 v89,v118,v89,v117
v_mfma_f32_16x16x32_bf16 v[28:31],a[204:207],v[56:59],v[28:31]
buffer_load_dwordx4 v1,s[36:39],s61 offen offset:2048 sc1 lds
v_mfma_f32_16x16x32_bf16 v[32:35],a[208:211],v[56:59],v[32:35]
ds_read_b128 v[76:79],v2 offset:22528
v_mul_f32 v89,v45,v89
v_mfma_f32_16x16x32_bf16 v[36:39],a[212:215],v[56:59],v[36:39]
s_waitcnt lgkmcnt(4)
v_exp_f32 v88,v88
v_mfma_f32_16x16x32_bf16 v[28:31],a[216:219],v[60:63],v[28:31]
ds_read_b128 v[80:83],v2 offset:23552
v_fma_f32 v88,v88,v119,v119
v_mfma_f32_16x16x32_bf16 v[32:35],a[220:223],v[60:63],v[32:35]
buffer_load_dwordx4 v1,s[36:39],s61 offen offset:3072 sc1 lds
v_mfma_f32_16x16x32_bf16 v[36:39],a[224:227],v[60:63],v[36:39]
buffer_load_dword v108,v8,s[36:39],s72 offen offset:0 sc1
v_mfma_f32_16x16x32_bf16 v[28:31],a[228:231],v[64:67],v[28:31]
ds_read_b128 v[52:55],v2 offset:24576
v_fma_f32 v90,v118,v90,v117
v_mfma_f32_16x16x32_bf16 v[32:35],a[232:235],v[64:67],v[32:35]
v_exp_f32 v89,v89
v_mfma_f32_16x16x32_bf16 v[36:39],a[236:239],v[64:67],v[36:39]
s_waitcnt lgkmcnt(3)
v_rcp_f32 v88,v88
v_mfma_f32_16x16x32_bf16 v[28:31],a[240:243],v[68:71],v[28:31]
ds_read_b128 v[56:59],v2 offset:25600
v_mul_f32 v90,v46,v90
v_mfma_f32_16x16x32_bf16 v[32:35],a[244:247],v[68:71],v[32:35]
s_waitcnt vmcnt(10)
v_exp_f32 v90,v90
v_mfma_f32_16x16x32_bf16 v[36:39],a[248:251],v[68:71],v[36:39]
s_barrier
v_mfma_f32_16x16x32_bf16 v[28:31],a[252:255],v[72:75],v[28:31]
ds_read_b128 v[60:63],v2 offset:26624
v_fma_f32 v89,v89,v119,v119
v_mfma_f32_16x16x32_bf16 v[32:35],v[128:131],v[72:75],v[32:35]
v_rcp_f32 v89,v89
v_mfma_f32_16x16x32_bf16 v[36:39],v[132:135],v[72:75],v[36:39]
s_waitcnt lgkmcnt(3)
v_fma_f32 v90,v90,v119,v119
v_fma_f32 v91,v118,v91,v117
v_mfma_f32_16x16x32_bf16 v[28:31],v[136:139],v[76:79],v[28:31]
ds_read_b128 v[64:67],v2 offset:27648
v_mul_f32 v91,v47,v91
v_mfma_f32_16x16x32_bf16 v[32:35],v[140:143],v[76:79],v[32:35]
v_exp_f32 v91,v91
v_mfma_f32_16x16x32_bf16 v[36:39],v[144:147],v[76:79],v[36:39]
v_rcp_f32 v90,v90
v_mfma_f32_16x16x32_bf16 v[28:31],v[148:151],v[80:83],v[28:31]
ds_read_b128 v[68:71],v2 offset:28672
v_fma_f32 v91,v91,v119,v119
v_mfma_f32_16x16x32_bf16 v[32:35],v[152:155],v[80:83],v[32:35]
v_rcp_f32 v91,v91
v_mfma_f32_16x16x32_bf16 v[36:39],v[156:159],v[80:83],v[36:39]
s_waitcnt lgkmcnt(3)
v_mul_f32 v44,v44,v88
v_mul_f32 v45,v45,v89
v_mfma_f32_16x16x32_bf16 v[28:31],v[160:163],v[52:55],v[28:31]
ds_read_b128 v[72:75],v2 offset:29696
v_mul_f32 v46,v46,v90
v_mfma_f32_16x16x32_bf16 v[32:35],v[164:167],v[52:55],v[32:35]
v_mul_f32 v47,v47,v91
v_cvt_pk_bf16_f32 v98,v44,v45
v_mfma_f32_16x16x32_bf16 v[36:39],v[168:171],v[52:55],v[36:39]
v_cvt_pk_bf16_f32 v99,v46,v47
v_fma_f32 v48,-v116,v24,v48
v_mfma_f32_16x16x32_bf16 v[28:31],v[172:175],v[56:59],v[28:31]
ds_read_b128 v[76:79],v2 offset:30720
v_fma_f32 v49,-v116,v25,v49
v_mfma_f32_16x16x32_bf16 v[32:35],v[176:179],v[56:59],v[32:35]
v_fma_f32 v50,-v116,v26,v50
v_fma_f32 v51,-v116,v27,v51
v_mfma_f32_16x16x32_bf16 v[36:39],v[180:183],v[56:59],v[36:39]
s_waitcnt lgkmcnt(3)
buffer_store_dwordx4 v[96:99],v3,s[36:39],s73 offen offset:0 sc0 sc1
v_mul_f32 v92,v48,v48
v_mul_f32 v93,v49,v49
v_mul_f32 v94,v50,v50
v_mfma_f32_16x16x32_bf16 v[28:31],v[184:187],v[60:63],v[28:31]
ds_read_b128 v[80:83],v2 offset:31744
v_mul_f32 v95,v51,v51
v_mfma_f32_16x16x32_bf16 v[32:35],v[188:191],v[60:63],v[32:35]
s_waitcnt vmcnt(6)
v_fma_f32 v92,v118,v92,v117
v_mul_f32 v92,v48,v92
v_mfma_f32_16x16x32_bf16 v[36:39],v[192:195],v[60:63],v[36:39]
s_barrier
v_mfma_f32_16x16x32_bf16 v[28:31],v[196:199],v[64:67],v[28:31]
ds_read_b128 v[52:55],v2 offset:32768
ds_read_b32 v13,v7 offset:0
v_mfma_f32_16x16x32_bf16 v[32:35],v[200:203],v[64:67],v[32:35]
ds_read_b64 v[112:113],v11 offset:0
v_fma_f32 v93,v118,v93,v117
v_mfma_f32_16x16x32_bf16 v[36:39],v[204:207],v[64:67],v[36:39]
s_waitcnt lgkmcnt(5)
v_exp_f32 v92,v92
v_mfma_f32_16x16x32_bf16 v[28:31],v[208:211],v[68:71],v[28:31]
ds_read_b128 v[56:59],v2 offset:33792
v_mul_f32 v93,v49,v93
v_mfma_f32_16x16x32_bf16 v[32:35],v[212:215],v[68:71],v[32:35]
s_waitcnt lgkmcnt(2)
v_exp_f32 v93,v93
v_mfma_f32_16x16x32_bf16 v[36:39],v[216:219],v[68:71],v[36:39]
s_waitcnt lgkmcnt(1)
v_cmp_eq_u32 vcc,s60,v13
v_fma_f32 v92,v92,v119,v119
v_rcp_f32 v92,v92
v_mfma_f32_16x16x32_bf16 v[28:31],v[220:223],v[72:75],v[28:31]
s_cmp_eq_u32 vcc_lo,-1
ds_read_b128 v[60:63],v2 offset:34816
v_fma_f32 v93,v93,v119,v119
v_mfma_f32_16x16x32_bf16 v[32:35],v[224:227],v[72:75],v[32:35]
s_cselect_b32 s67,s86,s59
v_rcp_f32 v93,v93
v_mfma_f32_16x16x32_bf16 v[36:39],v[228:231],v[72:75],v[36:39]
v_fma_f32 v94,v118,v94,v117
v_mul_f32 v94,v50,v94
v_mfma_f32_16x16x32_bf16 v[28:31],v[232:235],v[76:79],v[28:31]
s_lshr_b32 s61,s67,0
ds_read_b128 v[64:67],v2 offset:35840
v_fma_f32 v95,v118,v95,v117
v_mfma_f32_16x16x32_bf16 v[32:35],v[236:239],v[76:79],v[32:35]
s_xor_b32 s63,s63,65536
v_mul_f32 v95,v51,v95
v_exp_f32 v94,v94
v_exp_f32 v95,v95
v_mfma_f32_16x16x32_bf16 v[36:39],v[240:243],v[76:79],v[36:39]
s_cmp_ge_u32 s66,s59
v_fma_f32 v94,v94,v119,v119
v_fma_f32 v95,v95,v119,v119
v_rcp_f32 v94,v94
v_rcp_f32 v95,v95
v_mfma_f32_16x16x32_bf16 v[28:31],v[244:247],v[80:83],v[28:31]
v_mul_f32 v48,v48,v92
v_mul_f32 v49,v49,v93
v_mul_f32 v50,v50,v94
v_mul_f32 v51,v51,v95
v_cvt_pk_bf16_f32 v100,v48,v49
v_cvt_pk_bf16_f32 v101,v50,v51
v_mfma_f32_16x16x32_bf16 v[32:35],v[248:251],v[80:83],v[32:35]
s_waitcnt lgkmcnt(2)
ds_read_b128 v[68:71],v2 offset:36864
buffer_store_dwordx2 v[100:101],v9,s[36:39],s73 offen offset:0 sc0 sc1
v_mfma_f32_16x16x32_bf16 v[36:39],v[252:255],v[80:83],v[36:39]
s_cbranch_scc1 Lg3_slow1_%=
Lg3_d1w0_%=:
v_mfma_f32_16x16x32_bf16 v[40:43],a[0:3],v[52:55],0
s_mov_b32 m0,s62
ds_read_b128 v[72:75],v2 offset:37888
v_mul_f32 v116,s75,v112
v_mfma_f32_16x16x32_bf16 v[44:47],a[4:7],v[52:55],0
buffer_load_dwordx4 v0,s[36:39],s61 offen offset:0 sc1 lds
v_mfma_f32_16x16x32_bf16 v[48:51],a[8:11],v[52:55],0
s_mul_i32 s73,s48,7
v_fma_f32 v113,v113,s75,v12
v_fma_f32 v113,-v116,v116,v113
v_mfma_f32_16x16x32_bf16 v[40:43],a[12:15],v[56:59],v[40:43])"
R"(
s_lshr_b32 s73,s73,1
ds_read_b128 v[76:79],v2 offset:38912
v_max_f32 v113,v113,v12
v_mfma_f32_16x16x32_bf16 v[44:47],a[16:19],v[56:59],v[44:47]
s_add_u32 s74,s53,s55
v_rsq_f32 v112,v113
v_mfma_f32_16x16x32_bf16 v[48:51],a[20:23],v[56:59],v[48:51]
s_waitcnt lgkmcnt(3)
buffer_load_dwordx4 v0,s[36:39],s61 offen offset:1024 sc1 lds
v_mfma_f32_16x16x32_bf16 v[40:43],a[24:27],v[60:63],v[40:43]
s_cmp_eq_u32 s74,s56
ds_read_b128 v[80:83],v2 offset:39936
v_mul_f32 v117,s76,v112
v_mfma_f32_16x16x32_bf16 v[44:47],a[28:31],v[60:63],v[44:47]
s_cselect_b32 s74,s57,s74
v_mul_f32 v118,v112,v112
v_mul_f32 v119,v113,v112
v_mfma_f32_16x16x32_bf16 v[48:51],a[32:35],v[60:63],v[48:51]
s_cmp_eq_u32 s74,s58
v_mul_f32 v118,s77,v118
v_mul_f32 v118,v118,v117
v_mfma_f32_16x16x32_bf16 v[40:43],a[36:39],v[64:67],v[40:43]
buffer_load_dwordx4 v0,s[36:39],s61 offen offset:2048 sc1 lds
v_mfma_f32_16x16x32_bf16 v[44:47],a[40:43],v[64:67],v[44:47]
s_cselect_b32 s74,s59,s74
ds_read_b128 v[52:55],v2 offset:40960
v_fma_f32 v28,-v116,v16,v28
v_mfma_f32_16x16x32_bf16 v[48:51],a[44:47],v[64:67],v[48:51]
s_waitcnt lgkmcnt(3)
v_fma_f32 v29,-v116,v17,v29
v_fma_f32 v30,-v116,v18,v30
v_mfma_f32_16x16x32_bf16 v[40:43],a[48:51],v[68:71],v[40:43]
s_cmp_ge_u32 s67,s59
ds_read_b128 v[56:59],v2 offset:41984
v_fma_f32 v31,-v116,v19,v31
v_mfma_f32_16x16x32_bf16 v[44:47],a[52:55],v[68:71],v[44:47]
buffer_load_dwordx4 v0,s[36:39],s61 offen offset:3072 sc1 lds
v_mfma_f32_16x16x32_bf16 v[48:51],a[56:59],v[68:71],v[48:51]
s_cselect_b32 s86,s50,s51
v_mul_f32 v84,v28,v28
v_mul_f32 v85,v29,v29
v_mfma_f32_16x16x32_bf16 v[40:43],a[60:63],v[72:75],v[40:43]
s_lshr_b32 s68,s86,5
ds_read_b128 v[60:63],v2 offset:43008
v_mul_f32 v86,v30,v30
v_mfma_f32_16x16x32_bf16 v[44:47],a[64:67],v[72:75],v[44:47]
s_add_u32 m0,s78,256
v_mul_f32 v87,v31,v31
v_fma_f32 v84,v118,v84,v117
v_mfma_f32_16x16x32_bf16 v[48:51],a[68:71],v[72:75],v[48:51]
s_waitcnt lgkmcnt(3)
v_mul_f32 v84,v28,v84
v_fma_f32 v85,v118,v85,v117
v_mfma_f32_16x16x32_bf16 v[40:43],a[72:75],v[76:79],v[40:43]
s_mov_b64 s[44:45],s[46:47]
ds_read_b128 v[64:67],v2 offset:44032
v_mul_f32 v85,v29,v85
v_mfma_f32_16x16x32_bf16 v[44:47],a[76:79],v[76:79],v[44:47]
s_mov_b64 s[46:47],s[48:49]
v_exp_f32 v84,v84
v_mfma_f32_16x16x32_bf16 v[48:51],a[80:83],v[76:79],v[48:51]
s_mov_b64 s[48:49],s[50:51]
v_exp_f32 v85,v85
v_mfma_f32_16x16x32_bf16 v[40:43],a[84:87],v[80:83],v[40:43]
s_mov_b64 s[50:51],s[52:53]
ds_read_b128 v[68:71],v2 offset:45056
v_fma_f32 v84,v84,v119,v119
v_mfma_f32_16x16x32_bf16 v[44:47],a[88:91],v[80:83],v[44:47]
s_mov_b32 s52,s74
v_rcp_f32 v84,v84
v_mfma_f32_16x16x32_bf16 v[48:51],a[92:95],v[80:83],v[48:51]
s_waitcnt lgkmcnt(3)
v_fma_f32 v85,v85,v119,v119
v_fma_f32 v86,v118,v86,v117
v_mfma_f32_16x16x32_bf16 v[40:43],a[96:99],v[52:55],v[40:43]
ds_read_b128 v[72:75],v2 offset:46080
v_mul_f32 v86,v30,v86
v_mfma_f32_16x16x32_bf16 v[44:47],a[100:103],v[52:55],v[44:47]
v_exp_f32 v86,v86
v_mfma_f32_16x16x32_bf16 v[48:51],a[104:107],v[52:55],v[48:51]
v_rcp_f32 v85,v85
v_mfma_f32_16x16x32_bf16 v[40:43],a[108:111],v[56:59],v[40:43]
ds_read_b128 v[76:79],v2 offset:47104
v_fma_f32 v86,v86,v119,v119
v_mfma_f32_16x16x32_bf16 v[44:47],a[112:115],v[56:59],v[44:47]
v_rcp_f32 v86,v86
v_mfma_f32_16x16x32_bf16 v[48:51],a[116:119],v[56:59],v[48:51]
s_waitcnt lgkmcnt(3)
v_fma_f32 v87,v118,v87,v117
v_mul_f32 v87,v31,v87
v_mfma_f32_16x16x32_bf16 v[40:43],a[120:123],v[60:63],v[40:43]
ds_read_b128 v[80:83],v2 offset:48128
v_mul_f32 v28,v28,v84
v_mfma_f32_16x16x32_bf16 v[44:47],a[124:127],v[60:63],v[44:47]
s_waitcnt vmcnt(6)
v_exp_f32 v87,v87
v_mfma_f32_16x16x32_bf16 v[48:51],a[128:131],v[60:63],v[48:51]
s_barrier
v_mfma_f32_16x16x32_bf16 v[40:43],a[132:135],v[64:67],v[40:43]
ds_read_b128 v[52:55],v2 offset:49152
v_fma_f32 v87,v87,v119,v119
v_mfma_f32_16x16x32_bf16 v[44:47],a[136:139],v[64:67],v[44:47]
v_rcp_f32 v87,v87
v_mfma_f32_16x16x32_bf16 v[48:51],a[140:143],v[64:67],v[48:51]
s_waitcnt lgkmcnt(3)
v_add_f32_dpp v108,v108,v108 quad_perm:[1,0,3,2] row_mask:0xf bank_mask:0xf
v_mul_f32 v29,v29,v85
v_mfma_f32_16x16x32_bf16 v[40:43],a[144:147],v[68:71],v[40:43]
ds_read_b128 v[56:59],v2 offset:50176
v_add_f32_dpp v108,v108,v108 quad_perm:[2,3,0,1] row_mask:0xf bank_mask:0xf
v_mfma_f32_16x16x32_bf16 v[44:47],a[148:151],v[68:71],v[44:47]
s_nop 0
v_add_f32_dpp v108,v108,v108 row_half_mirror row_mask:0xf bank_mask:0xf
v_mul_f32 v30,v30,v86
v_mfma_f32_16x16x32_bf16 v[48:51],a[152:155],v[68:71],v[48:51]
v_mul_f32 v31,v31,v87
v_cvt_pk_bf16_f32 v96,v28,v29
v_mfma_f32_16x16x32_bf16 v[40:43],a[156:159],v[72:75],v[40:43]
ds_read_b128 v[60:63],v2 offset:51200
v_cvt_pk_bf16_f32 v97,v30,v31
v_mfma_f32_16x16x32_bf16 v[44:47],a[160:163],v[72:75],v[44:47]
buffer_load_dword v6,s[36:39],s68 offen offset:0 sc1 lds
v_mfma_f32_16x16x32_bf16 v[48:51],a[164:167],v[72:75],v[48:51]
s_waitcnt lgkmcnt(3)
v_fma_f32 v32,-v116,v20,v32
v_fma_f32 v33,-v116,v21,v33
v_mfma_f32_16x16x32_bf16 v[40:43],a[168:171],v[76:79],v[40:43]
s_add_u32 m0,s62,16384
ds_read_b128 v[64:67],v2 offset:52224
v_fma_f32 v34,-v116,v22,v34
v_mfma_f32_16x16x32_bf16 v[44:47],a[172:175],v[76:79],v[44:47]
buffer_load_dwordx4 v1,s[36:39],s61 offen offset:0 sc1 lds
v_mfma_f32_16x16x32_bf16 v[48:51],a[176:179],v[76:79],v[48:51]
s_lshr_b32 s72,s61,5
v_fma_f32 v35,-v116,v23,v35
v_mul_f32 v88,v32,v32
v_mfma_f32_16x16x32_bf16 v[40:43],a[180:183],v[80:83],v[40:43]
s_mov_b32 s62,s64
ds_read_b128 v[68:71],v2 offset:53248
v_mul_f32 v89,v33,v33
v_mfma_f32_16x16x32_bf16 v[44:47],a[184:187],v[80:83],v[44:47]
ds_write_b32 v10,v108 offset:128
v_mfma_f32_16x16x32_bf16 v[48:51],a[188:191],v[80:83],v[48:51]
s_waitcnt lgkmcnt(4)
buffer_load_dwordx4 v1,s[36:39],s61 offen offset:1024 sc1 lds
v_mfma_f32_16x16x32_bf16 v[40:43],a[192:195],v[52:55],v[40:43]
ds_read_b128 v[72:75],v2 offset:54272
v_mul_f32 v90,v34,v34
v_mfma_f32_16x16x32_bf16 v[44:47],a[196:199],v[52:55],v[44:47]
v_mul_f32 v91,v35,v35
v_fma_f32 v88,v118,v88,v117
v_mfma_f32_16x16x32_bf16 v[48:51],a[200:203],v[52:55],v[48:51]
v_mul_f32 v88,v32,v88
v_fma_f32 v89,v118,v89,v117
v_mfma_f32_16x16x32_bf16 v[40:43],a[204:207],v[56:59],v[40:43]
buffer_load_dwordx4 v1,s[36:39],s61 offen offset:2048 sc1 lds
v_mfma_f32_16x16x32_bf16 v[44:47],a[208:211],v[56:59],v[44:47]
ds_read_b128 v[76:79],v2 offset:55296
v_mul_f32 v89,v33,v89
v_mfma_f32_16x16x32_bf16 v[48:51],a[212:215],v[56:59],v[48:51]
s_waitcnt lgkmcnt(4)
v_exp_f32 v88,v88
v_mfma_f32_16x16x32_bf16 v[40:43],a[216:219],v[60:63],v[40:43]
ds_read_b128 v[80:83],v2 offset:56320
v_fma_f32 v88,v88,v119,v119
v_mfma_f32_16x16x32_bf16 v[44:47],a[220:223],v[60:63],v[44:47]
buffer_load_dwordx4 v1,s[36:39],s61 offen offset:3072 sc1 lds
v_mfma_f32_16x16x32_bf16 v[48:51],a[224:227],v[60:63],v[48:51]
buffer_load_dword v104,v8,s[36:39],s72 offen offset:0 sc1
v_mfma_f32_16x16x32_bf16 v[40:43],a[228:231],v[64:67],v[40:43]
ds_read_b128 v[52:55],v2 offset:57344
v_fma_f32 v90,v118,v90,v117
v_mfma_f32_16x16x32_bf16 v[44:47],a[232:235],v[64:67],v[44:47]
v_exp_f32 v89,v89
v_mfma_f32_16x16x32_bf16 v[48:51],a[236:239],v[64:67],v[48:51]
s_waitcnt lgkmcnt(3)
v_rcp_f32 v88,v88
v_mfma_f32_16x16x32_bf16 v[40:43],a[240:243],v[68:71],v[40:43]
ds_read_b128 v[56:59],v2 offset:58368
v_mul_f32 v90,v34,v90
v_mfma_f32_16x16x32_bf16 v[44:47],a[244:247],v[68:71],v[44:47]
s_waitcnt vmcnt(10)
v_exp_f32 v90,v90
v_mfma_f32_16x16x32_bf16 v[48:51],a[248:251],v[68:71],v[48:51]
s_barrier
v_mfma_f32_16x16x32_bf16 v[40:43],a[252:255],v[72:75],v[40:43]
ds_read_b128 v[60:63],v2 offset:59392
v_fma_f32 v89,v89,v119,v119
v_mfma_f32_16x16x32_bf16 v[44:47],v[128:131],v[72:75],v[44:47]
v_rcp_f32 v89,v89
v_mfma_f32_16x16x32_bf16 v[48:51],v[132:135],v[72:75],v[48:51]
s_waitcnt lgkmcnt(3)
v_fma_f32 v90,v90,v119,v119
v_fma_f32 v91,v118,v91,v117
v_mfma_f32_16x16x32_bf16 v[40:43],v[136:139],v[76:79],v[40:43]
ds_read_b128 v[64:67],v2 offset:60416
v_mul_f32 v91,v35,v91
v_mfma_f32_16x16x32_bf16 v[44:47],v[140:143],v[76:79],v[44:47]
v_exp_f32 v91,v91
v_mfma_f32_16x16x32_bf16 v[48:51],v[144:147],v[76:79],v[48:51])"
R"(
v_rcp_f32 v90,v90
v_mfma_f32_16x16x32_bf16 v[40:43],v[148:151],v[80:83],v[40:43]
ds_read_b128 v[68:71],v2 offset:61440
v_fma_f32 v91,v91,v119,v119
v_mfma_f32_16x16x32_bf16 v[44:47],v[152:155],v[80:83],v[44:47]
v_rcp_f32 v91,v91
v_mfma_f32_16x16x32_bf16 v[48:51],v[156:159],v[80:83],v[48:51]
s_waitcnt lgkmcnt(3)
v_mul_f32 v32,v32,v88
v_mul_f32 v33,v33,v89
v_mfma_f32_16x16x32_bf16 v[40:43],v[160:163],v[52:55],v[40:43]
ds_read_b128 v[72:75],v2 offset:62464
v_mul_f32 v34,v34,v90
v_mfma_f32_16x16x32_bf16 v[44:47],v[164:167],v[52:55],v[44:47]
v_mul_f32 v35,v35,v91
v_cvt_pk_bf16_f32 v98,v32,v33
v_mfma_f32_16x16x32_bf16 v[48:51],v[168:171],v[52:55],v[48:51]
v_cvt_pk_bf16_f32 v99,v34,v35
v_fma_f32 v36,-v116,v24,v36
v_mfma_f32_16x16x32_bf16 v[40:43],v[172:175],v[56:59],v[40:43]
ds_read_b128 v[76:79],v2 offset:63488
v_fma_f32 v37,-v116,v25,v37
v_mfma_f32_16x16x32_bf16 v[44:47],v[176:179],v[56:59],v[44:47]
v_fma_f32 v38,-v116,v26,v38
v_fma_f32 v39,-v116,v27,v39
v_mfma_f32_16x16x32_bf16 v[48:51],v[180:183],v[56:59],v[48:51]
s_waitcnt lgkmcnt(3)
buffer_store_dwordx4 v[96:99],v3,s[36:39],s73 offen offset:0 sc0 sc1
v_mul_f32 v92,v36,v36
v_mul_f32 v93,v37,v37
v_mul_f32 v94,v38,v38
v_mfma_f32_16x16x32_bf16 v[40:43],v[184:187],v[60:63],v[40:43]
ds_read_b128 v[80:83],v2 offset:64512
v_xor_b32 v2,65536,v2
v_mfma_f32_16x16x32_bf16 v[44:47],v[188:191],v[60:63],v[44:47]
s_waitcnt vmcnt(6)
v_mul_f32 v95,v39,v39
v_fma_f32 v92,v118,v92,v117
v_mfma_f32_16x16x32_bf16 v[48:51],v[192:195],v[60:63],v[48:51]
s_barrier
v_mfma_f32_16x16x32_bf16 v[40:43],v[196:199],v[64:67],v[40:43]
ds_read_b128 v[52:55],v2 offset:0
ds_read_b32 v13,v7 offset:256
v_mfma_f32_16x16x32_bf16 v[44:47],v[200:203],v[64:67],v[44:47]
ds_read_b64 v[112:113],v11 offset:128
v_mul_f32 v92,v36,v92
v_mfma_f32_16x16x32_bf16 v[48:51],v[204:207],v[64:67],v[48:51]
s_waitcnt lgkmcnt(5)
v_exp_f32 v92,v92
v_mfma_f32_16x16x32_bf16 v[40:43],v[208:211],v[68:71],v[40:43]
ds_read_b128 v[56:59],v2 offset:1024
v_fma_f32 v92,v92,v119,v119
v_mfma_f32_16x16x32_bf16 v[44:47],v[212:215],v[68:71],v[44:47]
s_waitcnt lgkmcnt(2)
v_rcp_f32 v92,v92
v_mfma_f32_16x16x32_bf16 v[48:51],v[216:219],v[68:71],v[48:51]
s_waitcnt lgkmcnt(1)
v_cmp_eq_u32 vcc,s60,v13
v_fma_f32 v93,v118,v93,v117
v_mul_f32 v93,v37,v93
v_fma_f32 v94,v118,v94,v117
v_mfma_f32_16x16x32_bf16 v[40:43],v[220:223],v[72:75],v[40:43]
s_cmp_eq_u32 vcc_lo,-1
ds_read_b128 v[60:63],v2 offset:2048
v_mul_f32 v94,v38,v94
v_mfma_f32_16x16x32_bf16 v[44:47],v[224:227],v[72:75],v[44:47]
s_cselect_b32 s66,s86,s59
v_exp_f32 v93,v93
v_mfma_f32_16x16x32_bf16 v[48:51],v[228:231],v[72:75],v[48:51]
v_exp_f32 v94,v94
v_mfma_f32_16x16x32_bf16 v[40:43],v[232:235],v[76:79],v[40:43]
s_lshr_b32 s61,s66,0
ds_read_b128 v[64:67],v2 offset:3072
v_fma_f32 v93,v93,v119,v119
v_mfma_f32_16x16x32_bf16 v[44:47],v[236:239],v[76:79],v[44:47]
s_xor_b32 s64,s64,65536
v_fma_f32 v95,v118,v95,v117
v_mul_f32 v95,v39,v95
v_exp_f32 v95,v95
v_mfma_f32_16x16x32_bf16 v[48:51],v[240:243],v[76:79],v[48:51]
s_cmp_lt_u32 s67,s59
v_fma_f32 v94,v94,v119,v119
v_fma_f32 v95,v95,v119,v119
v_rcp_f32 v93,v93
v_rcp_f32 v94,v94
v_rcp_f32 v95,v95
v_mfma_f32_16x16x32_bf16 v[40:43],v[244:247],v[80:83],v[40:43]
v_mul_f32 v36,v36,v92
v_mul_f32 v37,v37,v93
v_mul_f32 v38,v38,v94
v_mul_f32 v39,v39,v95
v_cvt_pk_bf16_f32 v100,v36,v37
v_cvt_pk_bf16_f32 v101,v38,v39
v_mfma_f32_16x16x32_bf16 v[44:47],v[248:251],v[80:83],v[44:47]
s_waitcnt lgkmcnt(2)
ds_read_b128 v[68:71],v2 offset:4096
buffer_store_dwordx2 v[100:101],v9,s[36:39],s73 offen offset:0 sc0 sc1
v_mfma_f32_16x16x32_bf16 v[48:51],v[252:255],v[80:83],v[48:51]
s_cbranch_scc1 Lg3_d0w0_%=
s_branch Lg3_slow0_%=
Lg3_d0w1_%=:
v_mfma_f32_16x16x32_bf16 v[28:31],a[0:3],v[52:55],0
s_mov_b32 m0,s62
ds_read_b128 v[72:75],v2 offset:5120
v_mul_f32 v116,s75,v112
v_mfma_f32_16x16x32_bf16 v[32:35],a[4:7],v[52:55],0
s_mul_i32 s73,s47,7
v_fma_f32 v113,v113,s75,v12
v_fma_f32 v113,-v116,v116,v113
v_mfma_f32_16x16x32_bf16 v[36:39],a[8:11],v[52:55],0
buffer_load_dwordx4 v0,s[36:39],s61 offen offset:0 sc1 lds
v_mfma_f32_16x16x32_bf16 v[28:31],a[12:15],v[56:59],v[28:31]
s_lshr_b32 s73,s73,1
ds_read_b128 v[76:79],v2 offset:6144
v_max_f32 v113,v113,v12
v_mfma_f32_16x16x32_bf16 v[32:35],a[16:19],v[56:59],v[32:35]
s_add_u32 s53,s52,s55
v_rsq_f32 v112,v113
v_mfma_f32_16x16x32_bf16 v[36:39],a[20:23],v[56:59],v[36:39]
s_waitcnt lgkmcnt(3)
v_mul_f32 v117,s76,v112
v_mul_f32 v118,v112,v112
v_mfma_f32_16x16x32_bf16 v[28:31],a[24:27],v[60:63],v[28:31]
buffer_load_dwordx4 v0,s[36:39],s61 offen offset:1024 sc1 lds
v_mfma_f32_16x16x32_bf16 v[32:35],a[28:31],v[60:63],v[32:35]
s_cmp_eq_u32 s53,s56
ds_read_b128 v[80:83],v2 offset:7168
v_mul_f32 v119,v113,v112
v_mfma_f32_16x16x32_bf16 v[36:39],a[32:35],v[60:63],v[36:39]
s_cselect_b32 s53,s57,s53
v_mul_f32 v118,s77,v118
v_mul_f32 v118,v118,v117
v_mfma_f32_16x16x32_bf16 v[28:31],a[36:39],v[64:67],v[28:31]
s_cmp_eq_u32 s53,s58
ds_read_b128 v[52:55],v2 offset:8192
v_fma_f32 v40,-v116,v16,v40
v_mfma_f32_16x16x32_bf16 v[32:35],a[40:43],v[64:67],v[32:35]
buffer_load_dwordx4 v0,s[36:39],s61 offen offset:2048 sc1 lds
v_mfma_f32_16x16x32_bf16 v[36:39],a[44:47],v[64:67],v[36:39]
s_waitcnt lgkmcnt(3)
v_fma_f32 v41,-v116,v17,v41
v_fma_f32 v42,-v116,v18,v42
v_mfma_f32_16x16x32_bf16 v[28:31],a[48:51],v[68:71],v[28:31]
s_cselect_b32 s53,s59,s53
ds_read_b128 v[56:59],v2 offset:9216
v_fma_f32 v43,-v116,v19,v43
v_mfma_f32_16x16x32_bf16 v[32:35],a[52:55],v[68:71],v[32:35]
s_cmp_ge_u32 s66,s59
v_mul_f32 v84,v40,v40
v_mul_f32 v85,v41,v41
v_mfma_f32_16x16x32_bf16 v[36:39],a[56:59],v[68:71],v[36:39]
buffer_load_dwordx4 v0,s[36:39],s61 offen offset:3072 sc1 lds
v_mfma_f32_16x16x32_bf16 v[28:31],a[60:63],v[72:75],v[28:31]
s_cselect_b32 s86,s49,s50
ds_read_b128 v[60:63],v2 offset:10240
v_mul_f32 v86,v42,v42
v_mfma_f32_16x16x32_bf16 v[32:35],a[64:67],v[72:75],v[32:35]
s_add_u32 m0,s62,16384
v_mul_f32 v87,v43,v43
v_fma_f32 v84,v118,v84,v117
v_mfma_f32_16x16x32_bf16 v[36:39],a[68:71],v[72:75],v[36:39]
s_waitcnt lgkmcnt(3)
v_mul_f32 v84,v40,v84
v_fma_f32 v85,v118,v85,v117
v_mfma_f32_16x16x32_bf16 v[28:31],a[72:75],v[76:79],v[28:31]
s_lshr_b32 s72,s61,5
ds_read_b128 v[64:67],v2 offset:11264
v_mul_f32 v85,v41,v85
v_mfma_f32_16x16x32_bf16 v[32:35],a[76:79],v[76:79],v[32:35]
s_mov_b32 s62,s63
v_exp_f32 v84,v84
v_mfma_f32_16x16x32_bf16 v[36:39],a[80:83],v[76:79],v[36:39]
v_exp_f32 v85,v85
v_mfma_f32_16x16x32_bf16 v[28:31],a[84:87],v[80:83],v[28:31]
ds_read_b128 v[68:71],v2 offset:12288
v_fma_f32 v84,v84,v119,v119
v_mfma_f32_16x16x32_bf16 v[32:35],a[88:91],v[80:83],v[32:35]
v_rcp_f32 v84,v84
v_mfma_f32_16x16x32_bf16 v[36:39],a[92:95],v[80:83],v[36:39]
s_waitcnt lgkmcnt(3)
v_fma_f32 v85,v85,v119,v119
v_fma_f32 v86,v118,v86,v117
v_mfma_f32_16x16x32_bf16 v[28:31],a[96:99],v[52:55],v[28:31]
ds_read_b128 v[72:75],v2 offset:13312
v_mul_f32 v86,v42,v86
v_mfma_f32_16x16x32_bf16 v[32:35],a[100:103],v[52:55],v[32:35]
v_exp_f32 v86,v86
v_mfma_f32_16x16x32_bf16 v[36:39],a[104:107],v[52:55],v[36:39]
v_rcp_f32 v85,v85
v_mfma_f32_16x16x32_bf16 v[28:31],a[108:111],v[56:59],v[28:31]
ds_read_b128 v[76:79],v2 offset:14336
v_fma_f32 v86,v86,v119,v119
v_mfma_f32_16x16x32_bf16 v[32:35],a[112:115],v[56:59],v[32:35]
v_rcp_f32 v86,v86
v_mfma_f32_16x16x32_bf16 v[36:39],a[116:119],v[56:59],v[36:39]
s_waitcnt lgkmcnt(3)
v_fma_f32 v87,v118,v87,v117
v_mul_f32 v87,v43,v87
v_mfma_f32_16x16x32_bf16 v[28:31],a[120:123],v[60:63],v[28:31]
ds_read_b128 v[80:83],v2 offset:15360)"
R"(
v_mul_f32 v40,v40,v84
v_mfma_f32_16x16x32_bf16 v[32:35],a[124:127],v[60:63],v[32:35]
s_waitcnt vmcnt(6)
v_exp_f32 v87,v87
v_mfma_f32_16x16x32_bf16 v[36:39],a[128:131],v[60:63],v[36:39]
s_barrier
v_mfma_f32_16x16x32_bf16 v[28:31],a[132:135],v[64:67],v[28:31]
ds_read_b128 v[52:55],v2 offset:16384
v_fma_f32 v87,v87,v119,v119
v_mfma_f32_16x16x32_bf16 v[32:35],a[136:139],v[64:67],v[32:35]
v_rcp_f32 v87,v87
v_mfma_f32_16x16x32_bf16 v[36:39],a[140:143],v[64:67],v[36:39]
s_waitcnt lgkmcnt(3)
v_add_f32_dpp v104,v104,v104 quad_perm:[1,0,3,2] row_mask:0xf bank_mask:0xf
v_mul_f32 v41,v41,v85
v_mfma_f32_16x16x32_bf16 v[28:31],a[144:147],v[68:71],v[28:31]
ds_read_b128 v[56:59],v2 offset:17408
v_add_f32_dpp v104,v104,v104 quad_perm:[2,3,0,1] row_mask:0xf bank_mask:0xf
v_mfma_f32_16x16x32_bf16 v[32:35],a[148:151],v[68:71],v[32:35]
s_nop 0
v_add_f32_dpp v104,v104,v104 row_half_mirror row_mask:0xf bank_mask:0xf
v_mul_f32 v42,v42,v86
v_mfma_f32_16x16x32_bf16 v[36:39],a[152:155],v[68:71],v[36:39]
v_mul_f32 v43,v43,v87
v_cvt_pk_bf16_f32 v96,v40,v41
v_mfma_f32_16x16x32_bf16 v[28:31],a[156:159],v[72:75],v[28:31]
ds_read_b128 v[60:63],v2 offset:18432
v_cvt_pk_bf16_f32 v97,v42,v43
v_mfma_f32_16x16x32_bf16 v[32:35],a[160:163],v[72:75],v[32:35]
v_fma_f32 v44,-v116,v20,v44
v_fma_f32 v45,-v116,v21,v45
v_mfma_f32_16x16x32_bf16 v[36:39],a[164:167],v[72:75],v[36:39]
s_waitcnt lgkmcnt(3)
v_fma_f32 v46,-v116,v22,v46
v_fma_f32 v47,-v116,v23,v47
v_mfma_f32_16x16x32_bf16 v[28:31],a[168:171],v[76:79],v[28:31]
ds_read_b128 v[64:67],v2 offset:19456
v_mul_f32 v88,v44,v44
v_mfma_f32_16x16x32_bf16 v[32:35],a[172:175],v[76:79],v[32:35]
v_mul_f32 v89,v45,v45
v_mul_f32 v90,v46,v46
v_mfma_f32_16x16x32_bf16 v[36:39],a[176:179],v[76:79],v[36:39]
buffer_load_dwordx4 v1,s[36:39],s61 offen offset:0 sc1 lds
v_mfma_f32_16x16x32_bf16 v[28:31],a[180:183],v[80:83],v[28:31]
ds_read_b128 v[68:71],v2 offset:20480
v_mul_f32 v91,v47,v47
v_mfma_f32_16x16x32_bf16 v[32:35],a[184:187],v[80:83],v[32:35]
ds_write_b32 v10,v104 offset:0
v_mfma_f32_16x16x32_bf16 v[36:39],a[188:191],v[80:83],v[36:39]
s_waitcnt lgkmcnt(4)
v_fma_f32 v88,v118,v88,v117
v_mul_f32 v88,v44,v88
v_mfma_f32_16x16x32_bf16 v[28:31],a[192:195],v[52:55],v[28:31]
buffer_load_dwordx4 v1,s[36:39],s61 offen offset:1024 sc1 lds
v_mfma_f32_16x16x32_bf16 v[32:35],a[196:199],v[52:55],v[32:35]
ds_read_b128 v[72:75],v2 offset:21504
v_fma_f32 v89,v118,v89,v117
v_mfma_f32_16x16x32_bf16 v[36:39],a[200:203],v[52:55],v[36:39]
v_exp_f32 v88,v88
v_mfma_f32_16x16x32_bf16 v[28:31],a[204:207],v[56:59],v[28:31]
ds_read_b128 v[76:79],v2 offset:22528
v_mul_f32 v89,v45,v89
v_mfma_f32_16x16x32_bf16 v[32:35],a[208:211],v[56:59],v[32:35]
buffer_load_dwordx4 v1,s[36:39],s61 offen offset:2048 sc1 lds
v_mfma_f32_16x16x32_bf16 v[36:39],a[212:215],v[56:59],v[36:39]
s_waitcnt lgkmcnt(4)
v_exp_f32 v89,v89
v_mfma_f32_16x16x32_bf16 v[28:31],a[216:219],v[60:63],v[28:31]
ds_read_b128 v[80:83],v2 offset:23552
v_fma_f32 v88,v88,v119,v119
v_mfma_f32_16x16x32_bf16 v[32:35],a[220:223],v[60:63],v[32:35]
v_rcp_f32 v88,v88
v_mfma_f32_16x16x32_bf16 v[36:39],a[224:227],v[60:63],v[36:39]
buffer_load_dwordx4 v1,s[36:39],s61 offen offset:3072 sc1 lds
v_mfma_f32_16x16x32_bf16 v[28:31],a[228:231],v[64:67],v[28:31]
buffer_load_dword v108,v8,s[36:39],s72 offen offset:0 sc1
v_mfma_f32_16x16x32_bf16 v[32:35],a[232:235],v[64:67],v[32:35]
ds_read_b128 v[52:55],v2 offset:24576
v_fma_f32 v89,v89,v119,v119
v_mfma_f32_16x16x32_bf16 v[36:39],a[236:239],v[64:67],v[36:39]
s_waitcnt lgkmcnt(3)
v_rcp_f32 v89,v89
v_mfma_f32_16x16x32_bf16 v[28:31],a[240:243],v[68:71],v[28:31]
ds_read_b128 v[56:59],v2 offset:25600
v_fma_f32 v90,v118,v90,v117
v_mfma_f32_16x16x32_bf16 v[32:35],a[244:247],v[68:71],v[32:35]
s_waitcnt vmcnt(9)
v_mul_f32 v90,v46,v90
v_fma_f32 v91,v118,v91,v117
v_mfma_f32_16x16x32_bf16 v[36:39],a[248:251],v[68:71],v[36:39]
s_barrier
v_mfma_f32_16x16x32_bf16 v[28:31],a[252:255],v[72:75],v[28:31]
ds_read_b128 v[60:63],v2 offset:26624
v_mul_f32 v91,v47,v91
v_mfma_f32_16x16x32_bf16 v[32:35],v[128:131],v[72:75],v[32:35]
v_exp_f32 v90,v90
v_mfma_f32_16x16x32_bf16 v[36:39],v[132:135],v[72:75],v[36:39]
s_waitcnt lgkmcnt(3)
v_exp_f32 v91,v91
v_mfma_f32_16x16x32_bf16 v[28:31],v[136:139],v[76:79],v[28:31]
ds_read_b128 v[64:67],v2 offset:27648
v_fma_f32 v90,v90,v119,v119
v_mfma_f32_16x16x32_bf16 v[32:35],v[140:143],v[76:79],v[32:35]
v_rcp_f32 v90,v90
v_mfma_f32_16x16x32_bf16 v[36:39],v[144:147],v[76:79],v[36:39]
v_fma_f32 v91,v91,v119,v119
v_mul_f32 v44,v44,v88
v_mfma_f32_16x16x32_bf16 v[28:31],v[148:151],v[80:83],v[28:31]
ds_read_b128 v[68:71],v2 offset:28672
v_mul_f32 v45,v45,v89
v_mfma_f32_16x16x32_bf16 v[32:35],v[152:155],v[80:83],v[32:35]
v_rcp_f32 v91,v91
v_mfma_f32_16x16x32_bf16 v[36:39],v[156:159],v[80:83],v[36:39]
s_waitcnt lgkmcnt(3)
v_mul_f32 v46,v46,v90
v_mul_f32 v47,v47,v91
v_mfma_f32_16x16x32_bf16 v[28:31],v[160:163],v[52:55],v[28:31]
ds_read_b128 v[72:75],v2 offset:29696
v_cvt_pk_bf16_f32 v98,v44,v45
v_mfma_f32_16x16x32_bf16 v[32:35],v[164:167],v[52:55],v[32:35]
v_cvt_pk_bf16_f32 v99,v46,v47
v_fma_f32 v48,-v116,v24,v48
v_mfma_f32_16x16x32_bf16 v[36:39],v[168:171],v[52:55],v[36:39]
v_fma_f32 v49,-v116,v25,v49
v_fma_f32 v50,-v116,v26,v50
v_mfma_f32_16x16x32_bf16 v[28:31],v[172:175],v[56:59],v[28:31]
buffer_store_dwordx4 v[96:99],v3,s[36:39],s73 offen offset:0 sc0 sc1
ds_read_b128 v[76:79],v2 offset:30720
v_fma_f32 v51,-v116,v27,v51
v_mul_f32 v92,v48,v48
v_mfma_f32_16x16x32_bf16 v[32:35],v[176:179],v[56:59],v[32:35]
v_mul_f32 v93,v49,v49
v_mul_f32 v94,v50,v50
v_mfma_f32_16x16x32_bf16 v[36:39],v[180:183],v[56:59],v[36:39]
s_waitcnt lgkmcnt(3)
v_mul_f32 v95,v51,v51
v_fma_f32 v92,v118,v92,v117
v_mfma_f32_16x16x32_bf16 v[28:31],v[184:187],v[60:63],v[28:31]
ds_read_b128 v[80:83],v2 offset:31744
v_mul_f32 v92,v48,v92
v_mfma_f32_16x16x32_bf16 v[32:35],v[188:191],v[60:63],v[32:35]
s_waitcnt vmcnt(6)
v_exp_f32 v92,v92
v_mfma_f32_16x16x32_bf16 v[36:39],v[192:195],v[60:63],v[36:39]
s_barrier
v_mfma_f32_16x16x32_bf16 v[28:31],v[196:199],v[64:67],v[28:31]
ds_read_b128 v[52:55],v2 offset:32768
ds_read_b32 v13,v7 offset:0
v_mfma_f32_16x16x32_bf16 v[32:35],v[200:203],v[64:67],v[32:35]
ds_read_b64 v[112:113],v11 offset:0
v_fma_f32 v92,v92,v119,v119
v_mfma_f32_16x16x32_bf16 v[36:39],v[204:207],v[64:67],v[36:39]
s_waitcnt lgkmcnt(5)
v_rcp_f32 v92,v92
v_mfma_f32_16x16x32_bf16 v[28:31],v[208:211],v[68:71],v[28:31]
ds_read_b128 v[56:59],v2 offset:33792
v_fma_f32 v93,v118,v93,v117
v_mfma_f32_16x16x32_bf16 v[32:35],v[212:215],v[68:71],v[32:35]
s_waitcnt lgkmcnt(2)
v_mul_f32 v93,v49,v93
v_fma_f32 v94,v118,v94,v117
v_mfma_f32_16x16x32_bf16 v[36:39],v[216:219],v[68:71],v[36:39]
s_waitcnt lgkmcnt(1)
v_cmp_eq_u32 vcc,s60,v13
v_exp_f32 v93,v93
v_mul_f32 v94,v50,v94
v_mfma_f32_16x16x32_bf16 v[28:31],v[220:223],v[72:75],v[28:31]
s_cmp_eq_u32 vcc_lo,-1
ds_read_b128 v[60:63],v2 offset:34816
v_fma_f32 v93,v93,v119,v119
v_mfma_f32_16x16x32_bf16 v[32:35],v[224:227],v[72:75],v[32:35]
s_cselect_b32 s67,s86,s59
v_exp_f32 v94,v94
v_mfma_f32_16x16x32_bf16 v[36:39],v[228:231],v[72:75],v[36:39]
v_fma_f32 v95,v118,v95,v117
v_mul_f32 v95,v51,v95
v_exp_f32 v95,v95
v_mfma_f32_16x16x32_bf16 v[28:31],v[232:235],v[76:79],v[28:31]
s_lshr_b32 s61,s67,0
v_fma_f32 v94,v94,v119,v119
v_fma_f32 v95,v95,v119,v119
v_rcp_f32 v93,v93
v_rcp_f32 v94,v94
v_rcp_f32 v95,v95
v_mfma_f32_16x16x32_bf16 v[32:35],v[236:239],v[76:79],v[32:35]
s_xor_b32 s63,s63,65536
v_mul_f32 v48,v48,v92
v_mul_f32 v49,v49,v93
v_mul_f32 v50,v50,v94
v_mul_f32 v51,v51,v95
v_cvt_pk_bf16_f32 v100,v48,v49
v_cvt_pk_bf16_f32 v101,v50,v51
v_mfma_f32_16x16x32_bf16 v[36:39],v[240:243],v[76:79],v[36:39]
s_cmp_ge_u32 s66,s59
ds_read_b128 v[64:67],v2 offset:35840
buffer_store_dwordx2 v[100:101],v9,s[36:39],s73 offen offset:0 sc0 sc1
v_mfma_f32_16x16x32_bf16 v[28:31],v[244:247],v[80:83],v[28:31]
ds_read_b128 v[68:71],v2 offset:36864
v_mfma_f32_16x16x32_bf16 v[32:35],v[248:251],v[80:83],v[32:35]
s_waitcnt lgkmcnt(3))"
R"(
v_mfma_f32_16x16x32_bf16 v[36:39],v[252:255],v[80:83],v[36:39]
s_cbranch_scc1 Lg3_slow1_%=
Lg3_d1w1_%=:
v_mfma_f32_16x16x32_bf16 v[40:43],a[0:3],v[52:55],0
s_mov_b32 m0,s62
ds_read_b128 v[72:75],v2 offset:37888
v_mul_f32 v116,s75,v112
v_mfma_f32_16x16x32_bf16 v[44:47],a[4:7],v[52:55],0
s_mul_i32 s73,s48,7
v_fma_f32 v113,v113,s75,v12
v_fma_f32 v113,-v116,v116,v113
v_mfma_f32_16x16x32_bf16 v[48:51],a[8:11],v[52:55],0
buffer_load_dwordx4 v0,s[36:39],s61 offen offset:0 sc1 lds
v_mfma_f32_16x16x32_bf16 v[40:43],a[12:15],v[56:59],v[40:43]
s_lshr_b32 s73,s73,1
ds_read_b128 v[76:79],v2 offset:38912
v_max_f32 v113,v113,v12
v_mfma_f32_16x16x32_bf16 v[44:47],a[16:19],v[56:59],v[44:47]
s_add_u32 s74,s53,s55
v_rsq_f32 v112,v113
v_mfma_f32_16x16x32_bf16 v[48:51],a[20:23],v[56:59],v[48:51]
s_waitcnt lgkmcnt(3)
v_mul_f32 v117,s76,v112
v_mul_f32 v118,v112,v112
v_mfma_f32_16x16x32_bf16 v[40:43],a[24:27],v[60:63],v[40:43]
buffer_load_dwordx4 v0,s[36:39],s61 offen offset:1024 sc1 lds
v_mfma_f32_16x16x32_bf16 v[44:47],a[28:31],v[60:63],v[44:47]
s_cmp_eq_u32 s74,s56
ds_read_b128 v[80:83],v2 offset:39936
v_mul_f32 v119,v113,v112
v_mfma_f32_16x16x32_bf16 v[48:51],a[32:35],v[60:63],v[48:51]
s_cselect_b32 s74,s57,s74
v_mul_f32 v118,s77,v118
v_mul_f32 v118,v118,v117
v_mfma_f32_16x16x32_bf16 v[40:43],a[36:39],v[64:67],v[40:43]
s_cmp_eq_u32 s74,s58
ds_read_b128 v[52:55],v2 offset:40960
v_fma_f32 v28,-v116,v16,v28
v_mfma_f32_16x16x32_bf16 v[44:47],a[40:43],v[64:67],v[44:47]
buffer_load_dwordx4 v0,s[36:39],s61 offen offset:2048 sc1 lds
v_mfma_f32_16x16x32_bf16 v[48:51],a[44:47],v[64:67],v[48:51]
s_waitcnt lgkmcnt(3)
v_fma_f32 v29,-v116,v17,v29
v_fma_f32 v30,-v116,v18,v30
v_mfma_f32_16x16x32_bf16 v[40:43],a[48:51],v[68:71],v[40:43]
s_cselect_b32 s74,s59,s74
ds_read_b128 v[56:59],v2 offset:41984
v_fma_f32 v31,-v116,v19,v31
v_mfma_f32_16x16x32_bf16 v[44:47],a[52:55],v[68:71],v[44:47]
s_cmp_ge_u32 s67,s59
v_mul_f32 v84,v28,v28
v_mul_f32 v85,v29,v29
v_mfma_f32_16x16x32_bf16 v[48:51],a[56:59],v[68:71],v[48:51]
buffer_load_dwordx4 v0,s[36:39],s61 offen offset:3072 sc1 lds
v_mfma_f32_16x16x32_bf16 v[40:43],a[60:63],v[72:75],v[40:43]
s_cselect_b32 s86,s50,s51
ds_read_b128 v[60:63],v2 offset:43008
v_mul_f32 v86,v30,v30
v_mfma_f32_16x16x32_bf16 v[44:47],a[64:67],v[72:75],v[44:47]
s_add_u32 m0,s62,16384
v_mul_f32 v87,v31,v31
v_fma_f32 v84,v118,v84,v117
v_mfma_f32_16x16x32_bf16 v[48:51],a[68:71],v[72:75],v[48:51]
s_waitcnt lgkmcnt(3)
v_mul_f32 v84,v28,v84
v_fma_f32 v85,v118,v85,v117
v_mfma_f32_16x16x32_bf16 v[40:43],a[72:75],v[76:79],v[40:43]
s_lshr_b32 s72,s61,5
ds_read_b128 v[64:67],v2 offset:44032
v_mul_f32 v85,v29,v85
v_mfma_f32_16x16x32_bf16 v[44:47],a[76:79],v[76:79],v[44:47]
s_mov_b32 s62,s64
v_exp_f32 v84,v84
v_mfma_f32_16x16x32_bf16 v[48:51],a[80:83],v[76:79],v[48:51]
s_mov_b64 s[44:45],s[46:47]
v_exp_f32 v85,v85
v_mfma_f32_16x16x32_bf16 v[40:43],a[84:87],v[80:83],v[40:43]
s_mov_b64 s[46:47],s[48:49]
ds_read_b128 v[68:71],v2 offset:45056
v_fma_f32 v84,v84,v119,v119
v_mfma_f32_16x16x32_bf16 v[44:47],a[88:91],v[80:83],v[44:47]
s_mov_b64 s[48:49],s[50:51]
v_rcp_f32 v84,v84
v_mfma_f32_16x16x32_bf16 v[48:51],a[92:95],v[80:83],v[48:51]
s_waitcnt lgkmcnt(3)
v_fma_f32 v85,v85,v119,v119
v_fma_f32 v86,v118,v86,v117
v_mfma_f32_16x16x32_bf16 v[40:43],a[96:99],v[52:55],v[40:43]
s_mov_b64 s[50:51],s[52:53]
ds_read_b128 v[72:75],v2 offset:46080
v_mul_f32 v86,v30,v86
v_mfma_f32_16x16x32_bf16 v[44:47],a[100:103],v[52:55],v[44:47]
s_mov_b32 s52,s74
v_exp_f32 v86,v86
v_mfma_f32_16x16x32_bf16 v[48:51],a[104:107],v[52:55],v[48:51]
v_rcp_f32 v85,v85
v_mfma_f32_16x16x32_bf16 v[40:43],a[108:111],v[56:59],v[40:43]
ds_read_b128 v[76:79],v2 offset:47104
v_fma_f32 v86,v86,v119,v119
v_mfma_f32_16x16x32_bf16 v[44:47],a[112:115],v[56:59],v[44:47]
v_rcp_f32 v86,v86
v_mfma_f32_16x16x32_bf16 v[48:51],a[116:119],v[56:59],v[48:51]
s_waitcnt lgkmcnt(3)
v_fma_f32 v87,v118,v87,v117
v_mul_f32 v87,v31,v87
v_mfma_f32_16x16x32_bf16 v[40:43],a[120:123],v[60:63],v[40:43]
ds_read_b128 v[80:83],v2 offset:48128
v_mul_f32 v28,v28,v84
v_mfma_f32_16x16x32_bf16 v[44:47],a[124:127],v[60:63],v[44:47]
s_waitcnt vmcnt(6)
v_exp_f32 v87,v87
v_mfma_f32_16x16x32_bf16 v[48:51],a[128:131],v[60:63],v[48:51]
s_barrier
v_mfma_f32_16x16x32_bf16 v[40:43],a[132:135],v[64:67],v[40:43]
ds_read_b128 v[52:55],v2 offset:49152
v_fma_f32 v87,v87,v119,v119
v_mfma_f32_16x16x32_bf16 v[44:47],a[136:139],v[64:67],v[44:47]
v_rcp_f32 v87,v87
v_mfma_f32_16x16x32_bf16 v[48:51],a[140:143],v[64:67],v[48:51]
s_waitcnt lgkmcnt(3)
v_add_f32_dpp v108,v108,v108 quad_perm:[1,0,3,2] row_mask:0xf bank_mask:0xf
v_mul_f32 v29,v29,v85
v_mfma_f32_16x16x32_bf16 v[40:43],a[144:147],v[68:71],v[40:43]
ds_read_b128 v[56:59],v2 offset:50176
v_add_f32_dpp v108,v108,v108 quad_perm:[2,3,0,1] row_mask:0xf bank_mask:0xf
v_mfma_f32_16x16x32_bf16 v[44:47],a[148:151],v[68:71],v[44:47]
s_nop 0
v_add_f32_dpp v108,v108,v108 row_half_mirror row_mask:0xf bank_mask:0xf
v_mul_f32 v30,v30,v86
v_mfma_f32_16x16x32_bf16 v[48:51],a[152:155],v[68:71],v[48:51]
v_mul_f32 v31,v31,v87
v_cvt_pk_bf16_f32 v96,v28,v29
v_mfma_f32_16x16x32_bf16 v[40:43],a[156:159],v[72:75],v[40:43]
ds_read_b128 v[60:63],v2 offset:51200
v_cvt_pk_bf16_f32 v97,v30,v31
v_mfma_f32_16x16x32_bf16 v[44:47],a[160:163],v[72:75],v[44:47]
v_fma_f32 v32,-v116,v20,v32
v_fma_f32 v33,-v116,v21,v33
v_mfma_f32_16x16x32_bf16 v[48:51],a[164:167],v[72:75],v[48:51]
s_waitcnt lgkmcnt(3)
v_fma_f32 v34,-v116,v22,v34
v_fma_f32 v35,-v116,v23,v35
v_mfma_f32_16x16x32_bf16 v[40:43],a[168:171],v[76:79],v[40:43]
ds_read_b128 v[64:67],v2 offset:52224
v_mul_f32 v88,v32,v32
v_mfma_f32_16x16x32_bf16 v[44:47],a[172:175],v[76:79],v[44:47]
v_mul_f32 v89,v33,v33
v_mul_f32 v90,v34,v34
v_mfma_f32_16x16x32_bf16 v[48:51],a[176:179],v[76:79],v[48:51]
buffer_load_dwordx4 v1,s[36:39],s61 offen offset:0 sc1 lds
v_mfma_f32_16x16x32_bf16 v[40:43],a[180:183],v[80:83],v[40:43]
ds_read_b128 v[68:71],v2 offset:53248
v_mul_f32 v91,v35,v35
v_mfma_f32_16x16x32_bf16 v[44:47],a[184:187],v[80:83],v[44:47]
ds_write_b32 v10,v108 offset:128
v_mfma_f32_16x16x32_bf16 v[48:51],a[188:191],v[80:83],v[48:51]
s_waitcnt lgkmcnt(4)
v_fma_f32 v88,v118,v88,v117
v_mul_f32 v88,v32,v88
v_mfma_f32_16x16x32_bf16 v[40:43],a[192:195],v[52:55],v[40:43]
buffer_load_dwordx4 v1,s[36:39],s61 offen offset:1024 sc1 lds
v_mfma_f32_16x16x32_bf16 v[44:47],a[196:199],v[52:55],v[44:47]
ds_read_b128 v[72:75],v2 offset:54272
v_fma_f32 v89,v118,v89,v117
v_mfma_f32_16x16x32_bf16 v[48:51],a[200:203],v[52:55],v[48:51]
v_exp_f32 v88,v88
v_mfma_f32_16x16x32_bf16 v[40:43],a[204:207],v[56:59],v[40:43]
ds_read_b128 v[76:79],v2 offset:55296
v_mul_f32 v89,v33,v89
v_mfma_f32_16x16x32_bf16 v[44:47],a[208:211],v[56:59],v[44:47]
buffer_load_dwordx4 v1,s[36:39],s61 offen offset:2048 sc1 lds
v_mfma_f32_16x16x32_bf16 v[48:51],a[212:215],v[56:59],v[48:51]
s_waitcnt lgkmcnt(4)
v_exp_f32 v89,v89
v_mfma_f32_16x16x32_bf16 v[40:43],a[216:219],v[60:63],v[40:43]
ds_read_b128 v[80:83],v2 offset:56320
v_fma_f32 v88,v88,v119,v119
v_mfma_f32_16x16x32_bf16 v[44:47],a[220:223],v[60:63],v[44:47]
v_rcp_f32 v88,v88
v_mfma_f32_16x16x32_bf16 v[48:51],a[224:227],v[60:63],v[48:51]
buffer_load_dwordx4 v1,s[36:39],s61 offen offset:3072 sc1 lds
v_mfma_f32_16x16x32_bf16 v[40:43],a[228:231],v[64:67],v[40:43]
buffer_load_dword v104,v8,s[36:39],s72 offen offset:0 sc1
v_mfma_f32_16x16x32_bf16 v[44:47],a[232:235],v[64:67],v[44:47]
ds_read_b128 v[52:55],v2 offset:57344
v_fma_f32 v89,v89,v119,v119
v_mfma_f32_16x16x32_bf16 v[48:51],a[236:239],v[64:67],v[48:51]
s_waitcnt lgkmcnt(3)
v_rcp_f32 v89,v89
v_mfma_f32_16x16x32_bf16 v[40:43],a[240:243],v[68:71],v[40:43]
ds_read_b128 v[56:59],v2 offset:58368
v_fma_f32 v90,v118,v90,v117
v_mfma_f32_16x16x32_bf16 v[44:47],a[244:247],v[68:71],v[44:47]
s_waitcnt vmcnt(9)
v_mul_f32 v90,v34,v90
v_fma_f32 v91,v118,v91,v117
v_mfma_f32_16x16x32_bf16 v[48:51],a[248:251],v[68:71],v[48:51]
s_barrier
v_mfma_f32_16x16x32_bf16 v[40:43],a[252:255],v[72:75],v[40:43]
ds_read_b128 v[60:63],v2 offset:59392)"
R"(
v_mul_f32 v91,v35,v91
v_mfma_f32_16x16x32_bf16 v[44:47],v[128:131],v[72:75],v[44:47]
v_exp_f32 v90,v90
v_mfma_f32_16x16x32_bf16 v[48:51],v[132:135],v[72:75],v[48:51]
s_waitcnt lgkmcnt(3)
v_exp_f32 v91,v91
v_mfma_f32_16x16x32_bf16 v[40:43],v[136:139],v[76:79],v[40:43]
ds_read_b128 v[64:67],v2 offset:60416
v_fma_f32 v90,v90,v119,v119
v_mfma_f32_16x16x32_bf16 v[44:47],v[140:143],v[76:79],v[44:47]
v_rcp_f32 v90,v90
v_mfma_f32_16x16x32_bf16 v[48:51],v[144:147],v[76:79],v[48:51]
v_fma_f32 v91,v91,v119,v119
v_mul_f32 v32,v32,v88
v_mfma_f32_16x16x32_bf16 v[40:43],v[148:151],v[80:83],v[40:43]
ds_read_b128 v[68:71],v2 offset:61440
v_mul_f32 v33,v33,v89
v_mfma_f32_16x16x32_bf16 v[44:47],v[152:155],v[80:83],v[44:47]
v_rcp_f32 v91,v91
v_mfma_f32_16x16x32_bf16 v[48:51],v[156:159],v[80:83],v[48:51]
s_waitcnt lgkmcnt(3)
v_mul_f32 v34,v34,v90
v_mul_f32 v35,v35,v91
v_mfma_f32_16x16x32_bf16 v[40:43],v[160:163],v[52:55],v[40:43]
ds_read_b128 v[72:75],v2 offset:62464
v_cvt_pk_bf16_f32 v98,v32,v33
v_mfma_f32_16x16x32_bf16 v[44:47],v[164:167],v[52:55],v[44:47]
v_cvt_pk_bf16_f32 v99,v34,v35
v_fma_f32 v36,-v116,v24,v36
v_mfma_f32_16x16x32_bf16 v[48:51],v[168:171],v[52:55],v[48:51]
v_fma_f32 v37,-v116,v25,v37
v_fma_f32 v38,-v116,v26,v38
v_mfma_f32_16x16x32_bf16 v[40:43],v[172:175],v[56:59],v[40:43]
buffer_store_dwordx4 v[96:99],v3,s[36:39],s73 offen offset:0 sc0 sc1
ds_read_b128 v[76:79],v2 offset:63488
v_fma_f32 v39,-v116,v27,v39
v_mul_f32 v92,v36,v36
v_mfma_f32_16x16x32_bf16 v[44:47],v[176:179],v[56:59],v[44:47]
v_mul_f32 v93,v37,v37
v_mul_f32 v94,v38,v38
v_mfma_f32_16x16x32_bf16 v[48:51],v[180:183],v[56:59],v[48:51]
s_waitcnt lgkmcnt(3)
v_mul_f32 v95,v39,v39
v_fma_f32 v92,v118,v92,v117
v_mfma_f32_16x16x32_bf16 v[40:43],v[184:187],v[60:63],v[40:43]
ds_read_b128 v[80:83],v2 offset:64512
v_mul_f32 v92,v36,v92
v_mfma_f32_16x16x32_bf16 v[44:47],v[188:191],v[60:63],v[44:47]
s_waitcnt vmcnt(6)
v_xor_b32 v2,65536,v2
v_fma_f32 v93,v118,v93,v117
v_mfma_f32_16x16x32_bf16 v[48:51],v[192:195],v[60:63],v[48:51]
s_barrier
v_mfma_f32_16x16x32_bf16 v[40:43],v[196:199],v[64:67],v[40:43]
ds_read_b128 v[52:55],v2 offset:0
ds_read_b32 v13,v7 offset:256
v_mfma_f32_16x16x32_bf16 v[44:47],v[200:203],v[64:67],v[44:47]
ds_read_b64 v[112:113],v11 offset:128
v_mul_f32 v93,v37,v93
v_mfma_f32_16x16x32_bf16 v[48:51],v[204:207],v[64:67],v[48:51]
s_waitcnt lgkmcnt(5)
v_exp_f32 v92,v92
v_mfma_f32_16x16x32_bf16 v[40:43],v[208:211],v[68:71],v[40:43]
ds_read_b128 v[56:59],v2 offset:1024
v_fma_f32 v92,v92,v119,v119
v_mfma_f32_16x16x32_bf16 v[44:47],v[212:215],v[68:71],v[44:47]
s_waitcnt lgkmcnt(2)
v_exp_f32 v93,v93
v_mfma_f32_16x16x32_bf16 v[48:51],v[216:219],v[68:71],v[48:51]
s_waitcnt lgkmcnt(1)
v_cmp_eq_u32 vcc,s60,v13
v_rcp_f32 v92,v92
v_fma_f32 v93,v93,v119,v119
v_mfma_f32_16x16x32_bf16 v[40:43],v[220:223],v[72:75],v[40:43]
s_cmp_eq_u32 vcc_lo,-1
ds_read_b128 v[60:63],v2 offset:2048
v_fma_f32 v94,v118,v94,v117
v_mfma_f32_16x16x32_bf16 v[44:47],v[224:227],v[72:75],v[44:47]
s_cselect_b32 s66,s86,s59
v_rcp_f32 v93,v93
v_mfma_f32_16x16x32_bf16 v[48:51],v[228:231],v[72:75],v[48:51]
v_fma_f32 v95,v118,v95,v117
v_mul_f32 v94,v38,v94
v_mul_f32 v95,v39,v95
v_exp_f32 v94,v94
v_exp_f32 v95,v95
v_mfma_f32_16x16x32_bf16 v[40:43],v[232:235],v[76:79],v[40:43]
s_lshr_b32 s61,s66,0
v_fma_f32 v94,v94,v119,v119
v_fma_f32 v95,v95,v119,v119
v_rcp_f32 v94,v94
v_rcp_f32 v95,v95
v_mfma_f32_16x16x32_bf16 v[44:47],v[236:239],v[76:79],v[44:47]
s_xor_b32 s64,s64,65536
v_mul_f32 v36,v36,v92
v_mul_f32 v37,v37,v93
v_mul_f32 v38,v38,v94
v_mul_f32 v39,v39,v95
v_cvt_pk_bf16_f32 v100,v36,v37
v_cvt_pk_bf16_f32 v101,v38,v39
v_mfma_f32_16x16x32_bf16 v[48:51],v[240:243],v[76:79],v[48:51]
s_cmp_lt_u32 s67,s59
ds_read_b128 v[64:67],v2 offset:3072
buffer_store_dwordx2 v[100:101],v9,s[36:39],s73 offen offset:0 sc0 sc1
v_mfma_f32_16x16x32_bf16 v[40:43],v[244:247],v[80:83],v[40:43]
ds_read_b128 v[68:71],v2 offset:4096
v_mfma_f32_16x16x32_bf16 v[44:47],v[248:251],v[80:83],v[44:47]
s_waitcnt lgkmcnt(3)
v_mfma_f32_16x16x32_bf16 v[48:51],v[252:255],v[80:83],v[48:51]
s_cbranch_scc1 Lg3_d0w1_%=
s_branch Lg3_slow0_%=
Lg3_d0w2_%=:
v_mfma_f32_16x16x32_bf16 v[28:31],a[0:3],v[52:55],0
s_mov_b32 m0,s62
ds_read_b128 v[72:75],v2 offset:5120
v_mul_f32 v116,s75,v112
v_mfma_f32_16x16x32_bf16 v[32:35],a[4:7],v[52:55],0
s_mul_i32 s73,s47,7
v_fma_f32 v113,v113,s75,v12
v_fma_f32 v113,-v116,v116,v113
v_mfma_f32_16x16x32_bf16 v[36:39],a[8:11],v[52:55],0
s_lshr_b32 s73,s73,1
v_max_f32 v113,v113,v12
v_rsq_f32 v112,v113
v_mfma_f32_16x16x32_bf16 v[28:31],a[12:15],v[56:59],v[28:31]
buffer_load_dwordx4 v0,s[36:39],s61 offen offset:0 sc1 lds
v_mfma_f32_16x16x32_bf16 v[32:35],a[16:19],v[56:59],v[32:35]
s_add_u32 s53,s52,s55
ds_read_b128 v[76:79],v2 offset:6144
v_mul_f32 v117,s76,v112
v_mfma_f32_16x16x32_bf16 v[36:39],a[20:23],v[56:59],v[36:39]
s_waitcnt lgkmcnt(3)
v_mul_f32 v118,v112,v112
v_mul_f32 v119,v113,v112
v_mfma_f32_16x16x32_bf16 v[28:31],a[24:27],v[60:63],v[28:31]
s_cmp_eq_u32 s53,s56
ds_read_b128 v[80:83],v2 offset:7168
v_mul_f32 v118,s77,v118
v_mfma_f32_16x16x32_bf16 v[32:35],a[28:31],v[60:63],v[32:35]
buffer_load_dwordx4 v0,s[36:39],s61 offen offset:1024 sc1 lds
v_mfma_f32_16x16x32_bf16 v[36:39],a[32:35],v[60:63],v[36:39]
s_cselect_b32 s53,s57,s53
v_mul_f32 v118,v118,v117
v_fma_f32 v40,-v116,v16,v40
v_mfma_f32_16x16x32_bf16 v[28:31],a[36:39],v[64:67],v[28:31]
s_cmp_eq_u32 s53,s58
ds_read_b128 v[52:55],v2 offset:8192
v_fma_f32 v41,-v116,v17,v41
v_mfma_f32_16x16x32_bf16 v[32:35],a[40:43],v[64:67],v[32:35]
s_cselect_b32 s53,s59,s53
v_fma_f32 v42,-v116,v18,v42
v_fma_f32 v43,-v116,v19,v43
v_mfma_f32_16x16x32_bf16 v[36:39],a[44:47],v[64:67],v[36:39]
s_waitcnt lgkmcnt(3)
buffer_load_dwordx4 v0,s[36:39],s61 offen offset:2048 sc1 lds
v_mfma_f32_16x16x32_bf16 v[28:31],a[48:51],v[68:71],v[28:31]
s_cmp_ge_u32 s66,s59
ds_read_b128 v[56:59],v2 offset:9216
v_mul_f32 v84,v40,v40
v_mfma_f32_16x16x32_bf16 v[32:35],a[52:55],v[68:71],v[32:35]
s_cselect_b32 s86,s49,s50
v_mul_f32 v85,v41,v41
v_mul_f32 v86,v42,v42
v_mfma_f32_16x16x32_bf16 v[36:39],a[56:59],v[68:71],v[36:39]
v_mul_f32 v87,v43,v43
v_fma_f32 v84,v118,v84,v117
v_mul_f32 v84,v40,v84
v_mfma_f32_16x16x32_bf16 v[28:31],a[60:63],v[72:75],v[28:31]
buffer_load_dwordx4 v0,s[36:39],s61 offen offset:3072 sc1 lds
v_mfma_f32_16x16x32_bf16 v[32:35],a[64:67],v[72:75],v[32:35]
s_add_u32 m0,s62,16384
ds_read_b128 v[60:63],v2 offset:10240
v_fma_f32 v85,v118,v85,v117
v_mfma_f32_16x16x32_bf16 v[36:39],a[68:71],v[72:75],v[36:39]
s_waitcnt lgkmcnt(3)
v_exp_f32 v84,v84
v_mfma_f32_16x16x32_bf16 v[28:31],a[72:75],v[76:79],v[28:31]
s_lshr_b32 s72,s61,5
ds_read_b128 v[64:67],v2 offset:11264
v_mul_f32 v85,v41,v85
v_mfma_f32_16x16x32_bf16 v[32:35],a[76:79],v[76:79],v[32:35]
s_mov_b32 s62,s63
v_exp_f32 v85,v85
v_mfma_f32_16x16x32_bf16 v[36:39],a[80:83],v[76:79],v[36:39]
v_fma_f32 v84,v84,v119,v119
v_fma_f32 v85,v85,v119,v119
v_mfma_f32_16x16x32_bf16 v[28:31],a[84:87],v[80:83],v[28:31]
ds_read_b128 v[68:71],v2 offset:12288
v_fma_f32 v86,v118,v86,v117
v_mfma_f32_16x16x32_bf16 v[32:35],a[88:91],v[80:83],v[32:35]
v_rcp_f32 v84,v84
v_mfma_f32_16x16x32_bf16 v[36:39],a[92:95],v[80:83],v[36:39]
s_waitcnt lgkmcnt(3)
v_rcp_f32 v85,v85
v_mfma_f32_16x16x32_bf16 v[28:31],a[96:99],v[52:55],v[28:31]
ds_read_b128 v[72:75],v2 offset:13312
v_mul_f32 v86,v42,v86
v_mfma_f32_16x16x32_bf16 v[32:35],a[100:103],v[52:55],v[32:35]
v_exp_f32 v86,v86
v_mfma_f32_16x16x32_bf16 v[36:39],a[104:107],v[52:55],v[36:39])"
R"(
v_fma_f32 v86,v86,v119,v119
v_fma_f32 v87,v118,v87,v117
v_mfma_f32_16x16x32_bf16 v[28:31],a[108:111],v[56:59],v[28:31]
ds_read_b128 v[76:79],v2 offset:14336
v_rcp_f32 v86,v86
v_mfma_f32_16x16x32_bf16 v[32:35],a[112:115],v[56:59],v[32:35]
v_mul_f32 v87,v43,v87
v_mul_f32 v40,v40,v84
v_mfma_f32_16x16x32_bf16 v[36:39],a[116:119],v[56:59],v[36:39]
s_waitcnt lgkmcnt(3)
v_exp_f32 v87,v87
v_mfma_f32_16x16x32_bf16 v[28:31],a[120:123],v[60:63],v[28:31]
ds_read_b128 v[80:83],v2 offset:15360
v_fma_f32 v87,v87,v119,v119
v_mfma_f32_16x16x32_bf16 v[32:35],a[124:127],v[60:63],v[32:35]
s_waitcnt vmcnt(6)
v_rcp_f32 v87,v87
v_mfma_f32_16x16x32_bf16 v[36:39],a[128:131],v[60:63],v[36:39]
s_barrier
v_mfma_f32_16x16x32_bf16 v[28:31],a[132:135],v[64:67],v[28:31]
ds_read_b128 v[52:55],v2 offset:16384
v_add_f32_dpp v104,v104,v104 quad_perm:[1,0,3,2] row_mask:0xf bank_mask:0xf
v_mfma_f32_16x16x32_bf16 v[32:35],a[136:139],v[64:67],v[32:35]
s_nop 0
v_add_f32_dpp v104,v104,v104 quad_perm:[2,3,0,1] row_mask:0xf bank_mask:0xf
v_mul_f32 v41,v41,v85
v_mfma_f32_16x16x32_bf16 v[36:39],a[140:143],v[64:67],v[36:39]
s_waitcnt lgkmcnt(3)
v_add_f32_dpp v104,v104,v104 row_half_mirror row_mask:0xf bank_mask:0xf
v_mul_f32 v42,v42,v86
v_mfma_f32_16x16x32_bf16 v[28:31],a[144:147],v[68:71],v[28:31]
ds_read_b128 v[56:59],v2 offset:17408
v_mul_f32 v43,v43,v87
v_mfma_f32_16x16x32_bf16 v[32:35],a[148:151],v[68:71],v[32:35]
v_cvt_pk_bf16_f32 v96,v40,v41
v_cvt_pk_bf16_f32 v97,v42,v43
v_mfma_f32_16x16x32_bf16 v[36:39],a[152:155],v[68:71],v[36:39]
v_fma_f32 v44,-v116,v20,v44
v_fma_f32 v45,-v116,v21,v45
v_mfma_f32_16x16x32_bf16 v[28:31],a[156:159],v[72:75],v[28:31]
ds_read_b128 v[60:63],v2 offset:18432
v_fma_f32 v46,-v116,v22,v46
v_mfma_f32_16x16x32_bf16 v[32:35],a[160:163],v[72:75],v[32:35]
v_fma_f32 v47,-v116,v23,v47
v_mul_f32 v88,v44,v44
v_mul_f32 v89,v45,v45
v_mfma_f32_16x16x32_bf16 v[36:39],a[164:167],v[72:75],v[36:39]
s_waitcnt lgkmcnt(3)
v_mul_f32 v90,v46,v46
v_mul_f32 v91,v47,v47
v_mfma_f32_16x16x32_bf16 v[28:31],a[168:171],v[76:79],v[28:31]
ds_read_b128 v[64:67],v2 offset:19456
v_fma_f32 v88,v118,v88,v117
v_mfma_f32_16x16x32_bf16 v[32:35],a[172:175],v[76:79],v[32:35]
v_mul_f32 v88,v44,v88
v_fma_f32 v89,v118,v89,v117
v_mfma_f32_16x16x32_bf16 v[36:39],a[176:179],v[76:79],v[36:39]
v_exp_f32 v88,v88
v_mfma_f32_16x16x32_bf16 v[28:31],a[180:183],v[80:83],v[28:31]
buffer_load_dwordx4 v1,s[36:39],s61 offen offset:0 sc1 lds
v_mfma_f32_16x16x32_bf16 v[32:35],a[184:187],v[80:83],v[32:35]
ds_write_b32 v10,v104 offset:0
v_mfma_f32_16x16x32_bf16 v[36:39],a[188:191],v[80:83],v[36:39]
s_waitcnt lgkmcnt(3)
ds_read_b128 v[68:71],v2 offset:20480
v_mul_f32 v89,v45,v89
v_mfma_f32_16x16x32_bf16 v[28:31],a[192:195],v[52:55],v[28:31]
ds_read_b128 v[72:75],v2 offset:21504
v_fma_f32 v88,v88,v119,v119
v_mfma_f32_16x16x32_bf16 v[32:35],a[196:199],v[52:55],v[32:35]
buffer_load_dwordx4 v1,s[36:39],s61 offen offset:1024 sc1 lds
v_mfma_f32_16x16x32_bf16 v[36:39],a[200:203],v[52:55],v[36:39]
v_exp_f32 v89,v89
v_mfma_f32_16x16x32_bf16 v[28:31],a[204:207],v[56:59],v[28:31]
ds_read_b128 v[76:79],v2 offset:22528
v_fma_f32 v89,v89,v119,v119
v_mfma_f32_16x16x32_bf16 v[32:35],a[208:211],v[56:59],v[32:35]
v_rcp_f32 v88,v88
v_mfma_f32_16x16x32_bf16 v[36:39],a[212:215],v[56:59],v[36:39]
s_waitcnt lgkmcnt(4)
buffer_load_dwordx4 v1,s[36:39],s61 offen offset:2048 sc1 lds
v_fma_f32 v90,v118,v90,v117
v_mfma_f32_16x16x32_bf16 v[28:31],a[216:219],v[60:63],v[28:31]
ds_read_b128 v[80:83],v2 offset:23552
v_mul_f32 v90,v46,v90
v_mfma_f32_16x16x32_bf16 v[32:35],a[220:223],v[60:63],v[32:35]
v_exp_f32 v90,v90
v_mfma_f32_16x16x32_bf16 v[36:39],a[224:227],v[60:63],v[36:39]
v_rcp_f32 v89,v89
v_mfma_f32_16x16x32_bf16 v[28:31],a[228:231],v[64:67],v[28:31]
buffer_load_dwordx4 v1,s[36:39],s61 offen offset:3072 sc1 lds
v_mfma_f32_16x16x32_bf16 v[32:35],a[232:235],v[64:67],v[32:35]
buffer_load_dword v108,v8,s[36:39],s72 offen offset:0 sc1
v_mfma_f32_16x16x32_bf16 v[36:39],a[236:239],v[64:67],v[36:39]
s_waitcnt lgkmcnt(2)
ds_read_b128 v[52:55],v2 offset:24576
v_fma_f32 v90,v90,v119,v119
v_mfma_f32_16x16x32_bf16 v[28:31],a[240:243],v[68:71],v[28:31]
ds_read_b128 v[56:59],v2 offset:25600
v_fma_f32 v91,v118,v91,v117
v_mfma_f32_16x16x32_bf16 v[32:35],a[244:247],v[68:71],v[32:35]
s_waitcnt vmcnt(9)
v_rcp_f32 v90,v90
v_mfma_f32_16x16x32_bf16 v[36:39],a[248:251],v[68:71],v[36:39]
s_barrier
v_mfma_f32_16x16x32_bf16 v[28:31],a[252:255],v[72:75],v[28:31]
ds_read_b128 v[60:63],v2 offset:26624
v_mul_f32 v91,v47,v91
v_mfma_f32_16x16x32_bf16 v[32:35],v[128:131],v[72:75],v[32:35]
v_exp_f32 v91,v91
v_mfma_f32_16x16x32_bf16 v[36:39],v[132:135],v[72:75],v[36:39]
s_waitcnt lgkmcnt(3)
v_fma_f32 v91,v91,v119,v119
v_mul_f32 v44,v44,v88
v_mfma_f32_16x16x32_bf16 v[28:31],v[136:139],v[76:79],v[28:31]
ds_read_b128 v[64:67],v2 offset:27648
v_rcp_f32 v91,v91
v_mfma_f32_16x16x32_bf16 v[32:35],v[140:143],v[76:79],v[32:35]
v_mul_f32 v45,v45,v89
v_mul_f32 v46,v46,v90
v_mfma_f32_16x16x32_bf16 v[36:39],v[144:147],v[76:79],v[36:39]
v_mul_f32 v47,v47,v91
v_cvt_pk_bf16_f32 v98,v44,v45
v_mfma_f32_16x16x32_bf16 v[28:31],v[148:151],v[80:83],v[28:31]
ds_read_b128 v[68:71],v2 offset:28672
v_cvt_pk_bf16_f32 v99,v46,v47
v_mfma_f32_16x16x32_bf16 v[32:35],v[152:155],v[80:83],v[32:35]
v_fma_f32 v48,-v116,v24,v48
v_fma_f32 v49,-v116,v25,v49
v_mfma_f32_16x16x32_bf16 v[36:39],v[156:159],v[80:83],v[36:39]
s_waitcnt lgkmcnt(3)
v_fma_f32 v50,-v116,v26,v50
v_fma_f32 v51,-v116,v27,v51
v_mfma_f32_16x16x32_bf16 v[28:31],v[160:163],v[52:55],v[28:31]
ds_read_b128 v[72:75],v2 offset:29696
v_mul_f32 v92,v48,v48
v_mfma_f32_16x16x32_bf16 v[32:35],v[164:167],v[52:55],v[32:35]
v_mul_f32 v93,v49,v49
v_mul_f32 v94,v50,v50
v_mfma_f32_16x16x32_bf16 v[36:39],v[168:171],v[52:55],v[36:39]
buffer_store_dwordx4 v[96:99],v3,s[36:39],s73 offen offset:0 sc0 sc1
v_mul_f32 v95,v51,v51
v_fma_f32 v92,v118,v92,v117
v_mul_f32 v92,v48,v92
v_mfma_f32_16x16x32_bf16 v[28:31],v[172:175],v[56:59],v[28:31]
ds_read_b128 v[76:79],v2 offset:30720
v_fma_f32 v93,v118,v93,v117
v_mfma_f32_16x16x32_bf16 v[32:35],v[176:179],v[56:59],v[32:35]
v_exp_f32 v92,v92
v_mfma_f32_16x16x32_bf16 v[36:39],v[180:183],v[56:59],v[36:39]
s_waitcnt lgkmcnt(3)
v_mul_f32 v93,v49,v93
v_fma_f32 v92,v92,v119,v119
v_mfma_f32_16x16x32_bf16 v[28:31],v[184:187],v[60:63],v[28:31]
ds_read_b128 v[80:83],v2 offset:31744
v_fma_f32 v94,v118,v94,v117
v_mfma_f32_16x16x32_bf16 v[32:35],v[188:191],v[60:63],v[32:35]
s_waitcnt vmcnt(6)
v_exp_f32 v93,v93
v_mul_f32 v94,v50,v94
v_mfma_f32_16x16x32_bf16 v[36:39],v[192:195],v[60:63],v[36:39]
s_barrier
v_mfma_f32_16x16x32_bf16 v[28:31],v[196:199],v[64:67],v[28:31]
ds_read_b128 v[52:55],v2 offset:32768
ds_read_b32 v13,v7 offset:0
v_mfma_f32_16x16x32_bf16 v[32:35],v[200:203],v[64:67],v[32:35]
ds_read_b64 v[112:113],v11 offset:0
v_fma_f32 v95,v118,v95,v117
v_mfma_f32_16x16x32_bf16 v[36:39],v[204:207],v[64:67],v[36:39]
s_waitcnt lgkmcnt(5)
v_exp_f32 v94,v94
v_mfma_f32_16x16x32_bf16 v[28:31],v[208:211],v[68:71],v[28:31]
ds_read_b128 v[56:59],v2 offset:33792
v_mul_f32 v95,v51,v95
v_mfma_f32_16x16x32_bf16 v[32:35],v[212:215],v[68:71],v[32:35]
s_waitcnt lgkmcnt(2)
v_exp_f32 v95,v95
v_mfma_f32_16x16x32_bf16 v[36:39],v[216:219],v[68:71],v[36:39]
s_waitcnt lgkmcnt(1)
v_cmp_eq_u32 vcc,s60,v13
v_rcp_f32 v92,v92
v_fma_f32 v93,v93,v119,v119
v_mfma_f32_16x16x32_bf16 v[28:31],v[220:223],v[72:75],v[28:31]
s_cmp_eq_u32 vcc_lo,-1
ds_read_b128 v[60:63],v2 offset:34816
v_fma_f32 v94,v94,v119,v119
v_mfma_f32_16x16x32_bf16 v[32:35],v[224:227],v[72:75],v[32:35]
s_cselect_b32 s67,s86,s59
v_rcp_f32 v93,v93
v_mfma_f32_16x16x32_bf16 v[36:39],v[228:231],v[72:75],v[36:39]
v_fma_f32 v95,v95,v119,v119
v_rcp_f32 v94,v94
v_rcp_f32 v95,v95
v_mfma_f32_16x16x32_bf16 v[28:31],v[232:235],v[76:79],v[28:31]
s_lshr_b32 s61,s67,0
v_mul_f32 v48,v48,v92
v_mul_f32 v49,v49,v93
v_mul_f32 v50,v50,v94
v_mul_f32 v51,v51,v95
v_cvt_pk_bf16_f32 v100,v48,v49)"
R"(
v_cvt_pk_bf16_f32 v101,v50,v51
v_mfma_f32_16x16x32_bf16 v[32:35],v[236:239],v[76:79],v[32:35]
buffer_store_dwordx2 v[100:101],v9,s[36:39],s73 offen offset:0 sc0 sc1
ds_read_b128 v[64:67],v2 offset:35840
v_mfma_f32_16x16x32_bf16 v[36:39],v[240:243],v[76:79],v[36:39]
s_xor_b32 s63,s63,65536
s_cmp_ge_u32 s66,s59
v_mfma_f32_16x16x32_bf16 v[28:31],v[244:247],v[80:83],v[28:31]
ds_read_b128 v[68:71],v2 offset:36864
v_mfma_f32_16x16x32_bf16 v[32:35],v[248:251],v[80:83],v[32:35]
s_waitcnt lgkmcnt(3)
v_mfma_f32_16x16x32_bf16 v[36:39],v[252:255],v[80:83],v[36:39]
s_cbranch_scc1 Lg3_slow1_%=
Lg3_d1w2_%=:
v_mfma_f32_16x16x32_bf16 v[40:43],a[0:3],v[52:55],0
s_mov_b32 m0,s62
ds_read_b128 v[72:75],v2 offset:37888
v_mul_f32 v116,s75,v112
v_mfma_f32_16x16x32_bf16 v[44:47],a[4:7],v[52:55],0
s_mul_i32 s73,s48,7
v_fma_f32 v113,v113,s75,v12
v_fma_f32 v113,-v116,v116,v113
v_mfma_f32_16x16x32_bf16 v[48:51],a[8:11],v[52:55],0
s_lshr_b32 s73,s73,1
v_max_f32 v113,v113,v12
v_rsq_f32 v112,v113
v_mfma_f32_16x16x32_bf16 v[40:43],a[12:15],v[56:59],v[40:43]
buffer_load_dwordx4 v0,s[36:39],s61 offen offset:0 sc1 lds
v_mfma_f32_16x16x32_bf16 v[44:47],a[16:19],v[56:59],v[44:47]
s_add_u32 s74,s53,s55
ds_read_b128 v[76:79],v2 offset:38912
v_mul_f32 v117,s76,v112
v_mfma_f32_16x16x32_bf16 v[48:51],a[20:23],v[56:59],v[48:51]
s_waitcnt lgkmcnt(3)
v_mul_f32 v118,v112,v112
v_mul_f32 v119,v113,v112
v_mfma_f32_16x16x32_bf16 v[40:43],a[24:27],v[60:63],v[40:43]
s_cmp_eq_u32 s74,s56
ds_read_b128 v[80:83],v2 offset:39936
v_mul_f32 v118,s77,v118
v_mfma_f32_16x16x32_bf16 v[44:47],a[28:31],v[60:63],v[44:47]
buffer_load_dwordx4 v0,s[36:39],s61 offen offset:1024 sc1 lds
v_mfma_f32_16x16x32_bf16 v[48:51],a[32:35],v[60:63],v[48:51]
s_cselect_b32 s74,s57,s74
v_mul_f32 v118,v118,v117
v_fma_f32 v28,-v116,v16,v28
v_mfma_f32_16x16x32_bf16 v[40:43],a[36:39],v[64:67],v[40:43]
s_cmp_eq_u32 s74,s58
ds_read_b128 v[52:55],v2 offset:40960
v_fma_f32 v29,-v116,v17,v29
v_mfma_f32_16x16x32_bf16 v[44:47],a[40:43],v[64:67],v[44:47]
s_cselect_b32 s74,s59,s74
v_fma_f32 v30,-v116,v18,v30
v_fma_f32 v31,-v116,v19,v31
v_mfma_f32_16x16x32_bf16 v[48:51],a[44:47],v[64:67],v[48:51]
s_waitcnt lgkmcnt(3)
buffer_load_dwordx4 v0,s[36:39],s61 offen offset:2048 sc1 lds
v_mfma_f32_16x16x32_bf16 v[40:43],a[48:51],v[68:71],v[40:43]
s_cmp_ge_u32 s67,s59
ds_read_b128 v[56:59],v2 offset:41984
v_mul_f32 v84,v28,v28
v_mfma_f32_16x16x32_bf16 v[44:47],a[52:55],v[68:71],v[44:47]
s_cselect_b32 s86,s50,s51
v_mul_f32 v85,v29,v29
v_mul_f32 v86,v30,v30
v_mul_f32 v87,v31,v31
v_mfma_f32_16x16x32_bf16 v[48:51],a[56:59],v[68:71],v[48:51]
s_mov_b64 s[44:45],s[46:47]
v_fma_f32 v84,v118,v84,v117
v_mul_f32 v84,v28,v84
v_mfma_f32_16x16x32_bf16 v[40:43],a[60:63],v[72:75],v[40:43]
buffer_load_dwordx4 v0,s[36:39],s61 offen offset:3072 sc1 lds
v_mfma_f32_16x16x32_bf16 v[44:47],a[64:67],v[72:75],v[44:47]
s_add_u32 m0,s62,16384
ds_read_b128 v[60:63],v2 offset:43008
v_fma_f32 v85,v118,v85,v117
v_mfma_f32_16x16x32_bf16 v[48:51],a[68:71],v[72:75],v[48:51]
s_waitcnt lgkmcnt(3)
v_exp_f32 v84,v84
v_mfma_f32_16x16x32_bf16 v[40:43],a[72:75],v[76:79],v[40:43]
s_lshr_b32 s72,s61,5
ds_read_b128 v[64:67],v2 offset:44032
v_mul_f32 v85,v29,v85
v_mfma_f32_16x16x32_bf16 v[44:47],a[76:79],v[76:79],v[44:47]
s_mov_b32 s62,s64
v_exp_f32 v85,v85
v_mfma_f32_16x16x32_bf16 v[48:51],a[80:83],v[76:79],v[48:51]
s_mov_b64 s[46:47],s[48:49]
v_fma_f32 v84,v84,v119,v119
v_fma_f32 v85,v85,v119,v119
v_mfma_f32_16x16x32_bf16 v[40:43],a[84:87],v[80:83],v[40:43]
s_mov_b64 s[48:49],s[50:51]
ds_read_b128 v[68:71],v2 offset:45056
v_fma_f32 v86,v118,v86,v117
v_mfma_f32_16x16x32_bf16 v[44:47],a[88:91],v[80:83],v[44:47]
s_mov_b64 s[50:51],s[52:53]
v_rcp_f32 v84,v84
v_mfma_f32_16x16x32_bf16 v[48:51],a[92:95],v[80:83],v[48:51]
s_waitcnt lgkmcnt(3)
v_rcp_f32 v85,v85
v_mfma_f32_16x16x32_bf16 v[40:43],a[96:99],v[52:55],v[40:43]
s_mov_b32 s52,s74
ds_read_b128 v[72:75],v2 offset:46080
v_mul_f32 v86,v30,v86
v_fma_f32 v87,v118,v87,v117
v_mfma_f32_16x16x32_bf16 v[44:47],a[100:103],v[52:55],v[44:47]
v_exp_f32 v86,v86
v_mfma_f32_16x16x32_bf16 v[48:51],a[104:107],v[52:55],v[48:51]
v_mul_f32 v87,v31,v87
v_fma_f32 v86,v86,v119,v119
v_mfma_f32_16x16x32_bf16 v[40:43],a[108:111],v[56:59],v[40:43]
ds_read_b128 v[76:79],v2 offset:47104
v_mul_f32 v28,v28,v84
v_mfma_f32_16x16x32_bf16 v[44:47],a[112:115],v[56:59],v[44:47]
v_exp_f32 v87,v87
v_mfma_f32_16x16x32_bf16 v[48:51],a[116:119],v[56:59],v[48:51]
s_waitcnt lgkmcnt(3)
v_rcp_f32 v86,v86
v_mfma_f32_16x16x32_bf16 v[40:43],a[120:123],v[60:63],v[40:43]
ds_read_b128 v[80:83],v2 offset:48128
v_fma_f32 v87,v87,v119,v119
v_mfma_f32_16x16x32_bf16 v[44:47],a[124:127],v[60:63],v[44:47]
s_waitcnt vmcnt(6)
v_rcp_f32 v87,v87
v_mfma_f32_16x16x32_bf16 v[48:51],a[128:131],v[60:63],v[48:51]
s_barrier
v_mfma_f32_16x16x32_bf16 v[40:43],a[132:135],v[64:67],v[40:43]
ds_read_b128 v[52:55],v2 offset:49152
v_add_f32_dpp v108,v108,v108 quad_perm:[1,0,3,2] row_mask:0xf bank_mask:0xf
v_mfma_f32_16x16x32_bf16 v[44:47],a[136:139],v[64:67],v[44:47]
s_nop 0
v_add_f32_dpp v108,v108,v108 quad_perm:[2,3,0,1] row_mask:0xf bank_mask:0xf
v_mul_f32 v29,v29,v85
v_mfma_f32_16x16x32_bf16 v[48:51],a[140:143],v[64:67],v[48:51]
s_waitcnt lgkmcnt(3)
v_add_f32_dpp v108,v108,v108 row_half_mirror row_mask:0xf bank_mask:0xf
v_mul_f32 v30,v30,v86
v_mul_f32 v31,v31,v87
v_mfma_f32_16x16x32_bf16 v[40:43],a[144:147],v[68:71],v[40:43]
ds_read_b128 v[56:59],v2 offset:50176
v_cvt_pk_bf16_f32 v96,v28,v29
v_mfma_f32_16x16x32_bf16 v[44:47],a[148:151],v[68:71],v[44:47]
v_cvt_pk_bf16_f32 v97,v30,v31
v_fma_f32 v32,-v116,v20,v32
v_mfma_f32_16x16x32_bf16 v[48:51],a[152:155],v[68:71],v[48:51]
v_fma_f32 v33,-v116,v21,v33
v_fma_f32 v34,-v116,v22,v34
v_mfma_f32_16x16x32_bf16 v[40:43],a[156:159],v[72:75],v[40:43]
ds_read_b128 v[60:63],v2 offset:51200
v_fma_f32 v35,-v116,v23,v35
v_mfma_f32_16x16x32_bf16 v[44:47],a[160:163],v[72:75],v[44:47]
v_mul_f32 v88,v32,v32
v_mul_f32 v89,v33,v33
v_mfma_f32_16x16x32_bf16 v[48:51],a[164:167],v[72:75],v[48:51]
s_waitcnt lgkmcnt(3)
v_mul_f32 v90,v34,v34
v_mul_f32 v91,v35,v35
v_mfma_f32_16x16x32_bf16 v[40:43],a[168:171],v[76:79],v[40:43]
ds_read_b128 v[64:67],v2 offset:52224
v_fma_f32 v88,v118,v88,v117
v_mfma_f32_16x16x32_bf16 v[44:47],a[172:175],v[76:79],v[44:47]
v_mul_f32 v88,v32,v88
v_fma_f32 v89,v118,v89,v117
v_mfma_f32_16x16x32_bf16 v[48:51],a[176:179],v[76:79],v[48:51]
v_exp_f32 v88,v88
v_mfma_f32_16x16x32_bf16 v[40:43],a[180:183],v[80:83],v[40:43]
buffer_load_dwordx4 v1,s[36:39],s61 offen offset:0 sc1 lds
v_mfma_f32_16x16x32_bf16 v[44:47],a[184:187],v[80:83],v[44:47]
ds_write_b32 v10,v108 offset:128
v_mfma_f32_16x16x32_bf16 v[48:51],a[188:191],v[80:83],v[48:51]
s_waitcnt lgkmcnt(3)
ds_read_b128 v[68:71],v2 offset:53248
v_mul_f32 v89,v33,v89
v_fma_f32 v88,v88,v119,v119
v_mfma_f32_16x16x32_bf16 v[40:43],a[192:195],v[52:55],v[40:43]
ds_read_b128 v[72:75],v2 offset:54272
v_fma_f32 v90,v118,v90,v117
v_mfma_f32_16x16x32_bf16 v[44:47],a[196:199],v[52:55],v[44:47]
buffer_load_dwordx4 v1,s[36:39],s61 offen offset:1024 sc1 lds
v_mfma_f32_16x16x32_bf16 v[48:51],a[200:203],v[52:55],v[48:51]
v_exp_f32 v89,v89
v_mfma_f32_16x16x32_bf16 v[40:43],a[204:207],v[56:59],v[40:43]
ds_read_b128 v[76:79],v2 offset:55296
v_mul_f32 v90,v34,v90
v_mfma_f32_16x16x32_bf16 v[44:47],a[208:211],v[56:59],v[44:47]
v_exp_f32 v90,v90
v_mfma_f32_16x16x32_bf16 v[48:51],a[212:215],v[56:59],v[48:51]
s_waitcnt lgkmcnt(4)
buffer_load_dwordx4 v1,s[36:39],s61 offen offset:2048 sc1 lds
v_mfma_f32_16x16x32_bf16 v[40:43],a[216:219],v[60:63],v[40:43]
ds_read_b128 v[80:83],v2 offset:56320
v_fma_f32 v89,v89,v119,v119
v_mfma_f32_16x16x32_bf16 v[44:47],a[220:223],v[60:63],v[44:47]
v_rcp_f32 v88,v88
v_mfma_f32_16x16x32_bf16 v[48:51],a[224:227],v[60:63],v[48:51]
v_rcp_f32 v89,v89
v_mfma_f32_16x16x32_bf16 v[40:43],a[228:231],v[64:67],v[40:43]
buffer_load_dwordx4 v1,s[36:39],s61 offen offset:3072 sc1 lds
v_mfma_f32_16x16x32_bf16 v[44:47],a[232:235],v[64:67],v[44:47]
buffer_load_dword v104,v8,s[36:39],s72 offen offset:0 sc1)"
R"(
ds_read_b128 v[52:55],v2 offset:57344
v_mfma_f32_16x16x32_bf16 v[48:51],a[236:239],v[64:67],v[48:51]
s_waitcnt lgkmcnt(3)
v_fma_f32 v90,v90,v119,v119
v_fma_f32 v91,v118,v91,v117
v_mfma_f32_16x16x32_bf16 v[40:43],a[240:243],v[68:71],v[40:43]
ds_read_b128 v[56:59],v2 offset:58368
v_mul_f32 v91,v35,v91
v_mfma_f32_16x16x32_bf16 v[44:47],a[244:247],v[68:71],v[44:47]
s_waitcnt vmcnt(9)
v_exp_f32 v91,v91
v_mfma_f32_16x16x32_bf16 v[48:51],a[248:251],v[68:71],v[48:51]
s_barrier
v_mfma_f32_16x16x32_bf16 v[40:43],a[252:255],v[72:75],v[40:43]
ds_read_b128 v[60:63],v2 offset:59392
v_fma_f32 v91,v91,v119,v119
v_mfma_f32_16x16x32_bf16 v[44:47],v[128:131],v[72:75],v[44:47]
v_rcp_f32 v90,v90
v_mfma_f32_16x16x32_bf16 v[48:51],v[132:135],v[72:75],v[48:51]
s_waitcnt lgkmcnt(3)
v_rcp_f32 v91,v91
v_mfma_f32_16x16x32_bf16 v[40:43],v[136:139],v[76:79],v[40:43]
ds_read_b128 v[64:67],v2 offset:60416
v_mul_f32 v32,v32,v88
v_mfma_f32_16x16x32_bf16 v[44:47],v[140:143],v[76:79],v[44:47]
v_mul_f32 v33,v33,v89
v_mul_f32 v34,v34,v90
v_mfma_f32_16x16x32_bf16 v[48:51],v[144:147],v[76:79],v[48:51]
v_mul_f32 v35,v35,v91
v_cvt_pk_bf16_f32 v98,v32,v33
v_mfma_f32_16x16x32_bf16 v[40:43],v[148:151],v[80:83],v[40:43]
ds_read_b128 v[68:71],v2 offset:61440
v_cvt_pk_bf16_f32 v99,v34,v35
v_fma_f32 v36,-v116,v24,v36
v_mfma_f32_16x16x32_bf16 v[44:47],v[152:155],v[80:83],v[44:47]
v_fma_f32 v37,-v116,v25,v37
v_fma_f32 v38,-v116,v26,v38
v_mfma_f32_16x16x32_bf16 v[48:51],v[156:159],v[80:83],v[48:51]
s_waitcnt lgkmcnt(3)
v_fma_f32 v39,-v116,v27,v39
v_mul_f32 v92,v36,v36
v_mfma_f32_16x16x32_bf16 v[40:43],v[160:163],v[52:55],v[40:43]
ds_read_b128 v[72:75],v2 offset:62464
v_mul_f32 v93,v37,v37
v_mfma_f32_16x16x32_bf16 v[44:47],v[164:167],v[52:55],v[44:47]
v_mul_f32 v94,v38,v38
v_mul_f32 v95,v39,v39
v_mfma_f32_16x16x32_bf16 v[48:51],v[168:171],v[52:55],v[48:51]
buffer_store_dwordx4 v[96:99],v3,s[36:39],s73 offen offset:0 sc0 sc1
v_fma_f32 v92,v118,v92,v117
v_mul_f32 v92,v36,v92
v_fma_f32 v93,v118,v93,v117
v_mfma_f32_16x16x32_bf16 v[40:43],v[172:175],v[56:59],v[40:43]
ds_read_b128 v[76:79],v2 offset:63488
v_mul_f32 v93,v37,v93
v_mfma_f32_16x16x32_bf16 v[44:47],v[176:179],v[56:59],v[44:47]
v_exp_f32 v92,v92
v_mfma_f32_16x16x32_bf16 v[48:51],v[180:183],v[56:59],v[48:51]
s_waitcnt lgkmcnt(3)
v_exp_f32 v93,v93
v_mfma_f32_16x16x32_bf16 v[40:43],v[184:187],v[60:63],v[40:43]
ds_read_b128 v[80:83],v2 offset:64512
v_fma_f32 v92,v92,v119,v119
v_mfma_f32_16x16x32_bf16 v[44:47],v[188:191],v[60:63],v[44:47]
s_waitcnt vmcnt(6)
v_xor_b32 v2,65536,v2
v_fma_f32 v93,v93,v119,v119
v_mfma_f32_16x16x32_bf16 v[48:51],v[192:195],v[60:63],v[48:51]
s_barrier
v_mfma_f32_16x16x32_bf16 v[40:43],v[196:199],v[64:67],v[40:43]
ds_read_b128 v[52:55],v2 offset:0
ds_read_b32 v13,v7 offset:256
v_mfma_f32_16x16x32_bf16 v[44:47],v[200:203],v[64:67],v[44:47]
ds_read_b64 v[112:113],v11 offset:128
v_fma_f32 v94,v118,v94,v117
v_mfma_f32_16x16x32_bf16 v[48:51],v[204:207],v[64:67],v[48:51]
s_waitcnt lgkmcnt(5)
v_rcp_f32 v92,v92
v_mfma_f32_16x16x32_bf16 v[40:43],v[208:211],v[68:71],v[40:43]
ds_read_b128 v[56:59],v2 offset:1024
v_mul_f32 v94,v38,v94
v_mfma_f32_16x16x32_bf16 v[44:47],v[212:215],v[68:71],v[44:47]
s_waitcnt lgkmcnt(2)
v_exp_f32 v94,v94
v_mfma_f32_16x16x32_bf16 v[48:51],v[216:219],v[68:71],v[48:51]
s_waitcnt lgkmcnt(1)
v_cmp_eq_u32 vcc,s60,v13
v_rcp_f32 v93,v93
v_fma_f32 v94,v94,v119,v119
v_mfma_f32_16x16x32_bf16 v[40:43],v[220:223],v[72:75],v[40:43]
s_cmp_eq_u32 vcc_lo,-1
ds_read_b128 v[60:63],v2 offset:2048
v_fma_f32 v95,v118,v95,v117
v_mfma_f32_16x16x32_bf16 v[44:47],v[224:227],v[72:75],v[44:47]
s_cselect_b32 s66,s86,s59
v_mul_f32 v95,v39,v95
v_exp_f32 v95,v95
v_mfma_f32_16x16x32_bf16 v[48:51],v[228:231],v[72:75],v[48:51]
v_fma_f32 v95,v95,v119,v119
v_rcp_f32 v94,v94
v_rcp_f32 v95,v95
v_mfma_f32_16x16x32_bf16 v[40:43],v[232:235],v[76:79],v[40:43]
s_lshr_b32 s61,s66,0
v_mul_f32 v36,v36,v92
v_mul_f32 v37,v37,v93
v_mul_f32 v38,v38,v94
v_mul_f32 v39,v39,v95
v_cvt_pk_bf16_f32 v100,v36,v37
v_cvt_pk_bf16_f32 v101,v38,v39
v_mfma_f32_16x16x32_bf16 v[44:47],v[236:239],v[76:79],v[44:47]
buffer_store_dwordx2 v[100:101],v9,s[36:39],s73 offen offset:0 sc0 sc1
ds_read_b128 v[64:67],v2 offset:3072
v_mfma_f32_16x16x32_bf16 v[48:51],v[240:243],v[76:79],v[48:51]
s_xor_b32 s64,s64,65536
s_cmp_lt_u32 s67,s59
v_mfma_f32_16x16x32_bf16 v[40:43],v[244:247],v[80:83],v[40:43]
ds_read_b128 v[68:71],v2 offset:4096
v_mfma_f32_16x16x32_bf16 v[44:47],v[248:251],v[80:83],v[44:47]
s_waitcnt lgkmcnt(3)
v_mfma_f32_16x16x32_bf16 v[48:51],v[252:255],v[80:83],v[48:51]
s_cbranch_scc1 Lg3_d0w2_%=
s_branch Lg3_slow0_%=
Lg3_d0w3_%=:
v_mfma_f32_16x16x32_bf16 v[28:31],a[0:3],v[52:55],0
s_mov_b32 m0,s62
ds_read_b128 v[72:75],v2 offset:5120
v_mul_f32 v116,s75,v112
v_mfma_f32_16x16x32_bf16 v[32:35],a[4:7],v[52:55],0
s_mul_i32 s73,s47,7
v_fma_f32 v113,v113,s75,v12
v_fma_f32 v113,-v116,v116,v113
v_mfma_f32_16x16x32_bf16 v[36:39],a[8:11],v[52:55],0
s_lshr_b32 s73,s73,1
v_max_f32 v113,v113,v12
v_rsq_f32 v112,v113
v_mfma_f32_16x16x32_bf16 v[28:31],a[12:15],v[56:59],v[28:31]
s_add_u32 s53,s52,s55
ds_read_b128 v[76:79],v2 offset:6144
v_mul_f32 v117,s76,v112
v_mfma_f32_16x16x32_bf16 v[32:35],a[16:19],v[56:59],v[32:35]
buffer_load_dwordx4 v0,s[36:39],s61 offen offset:0 sc1 lds
v_mfma_f32_16x16x32_bf16 v[36:39],a[20:23],v[56:59],v[36:39]
s_waitcnt lgkmcnt(3)
v_mul_f32 v118,v112,v112
v_mul_f32 v119,v113,v112
v_mfma_f32_16x16x32_bf16 v[28:31],a[24:27],v[60:63],v[28:31]
s_cmp_eq_u32 s53,s56
ds_read_b128 v[80:83],v2 offset:7168
v_mul_f32 v118,s77,v118
v_mfma_f32_16x16x32_bf16 v[32:35],a[28:31],v[60:63],v[32:35]
s_cselect_b32 s53,s57,s53
v_mul_f32 v118,v118,v117
v_fma_f32 v40,-v116,v16,v40
v_mfma_f32_16x16x32_bf16 v[36:39],a[32:35],v[60:63],v[36:39]
buffer_load_dwordx4 v0,s[36:39],s61 offen offset:1024 sc1 lds
v_mfma_f32_16x16x32_bf16 v[28:31],a[36:39],v[64:67],v[28:31]
s_cmp_eq_u32 s53,s58
ds_read_b128 v[52:55],v2 offset:8192
v_fma_f32 v41,-v116,v17,v41
v_mfma_f32_16x16x32_bf16 v[32:35],a[40:43],v[64:67],v[32:35]
s_cselect_b32 s53,s59,s53
v_fma_f32 v42,-v116,v18,v42
v_fma_f32 v43,-v116,v19,v43
v_mfma_f32_16x16x32_bf16 v[36:39],a[44:47],v[64:67],v[36:39]
s_waitcnt lgkmcnt(3)
v_mul_f32 v84,v40,v40
v_mul_f32 v85,v41,v41
v_mfma_f32_16x16x32_bf16 v[28:31],a[48:51],v[68:71],v[28:31]
buffer_load_dwordx4 v0,s[36:39],s61 offen offset:2048 sc1 lds
v_mfma_f32_16x16x32_bf16 v[32:35],a[52:55],v[68:71],v[32:35]
s_cmp_ge_u32 s66,s59
ds_read_b128 v[56:59],v2 offset:9216
v_mul_f32 v86,v42,v42
v_mfma_f32_16x16x32_bf16 v[36:39],a[56:59],v[68:71],v[36:39]
s_cselect_b32 s86,s49,s50
v_mul_f32 v87,v43,v43
v_fma_f32 v84,v118,v84,v117
v_mfma_f32_16x16x32_bf16 v[28:31],a[60:63],v[72:75],v[28:31]
s_mul_i32 s70,s46,5
ds_read_b128 v[60:63],v2 offset:10240
v_mul_f32 v84,v40,v84
v_mfma_f32_16x16x32_bf16 v[32:35],a[64:67],v[72:75],v[32:35]
buffer_load_dwordx4 v0,s[36:39],s61 offen offset:3072 sc1 lds
v_mfma_f32_16x16x32_bf16 v[36:39],a[68:71],v[72:75],v[36:39]
s_waitcnt lgkmcnt(3)
v_exp_f32 v84,v84
v_mfma_f32_16x16x32_bf16 v[28:31],a[72:75],v[76:79],v[28:31]
s_add_u32 m0,s62,16384
ds_read_b128 v[64:67],v2 offset:11264
v_fma_f32 v84,v84,v119,v119
v_mfma_f32_16x16x32_bf16 v[32:35],a[76:79],v[76:79],v[32:35]
s_lshr_b32 s70,s70,6
v_rcp_f32 v84,v84
v_mfma_f32_16x16x32_bf16 v[36:39],a[80:83],v[76:79],v[36:39]
s_lshr_b32 s72,s61,5
v_fma_f32 v85,v118,v85,v117
v_mul_f32 v85,v41,v85
v_mfma_f32_16x16x32_bf16 v[28:31],a[84:87],v[80:83],v[28:31]
s_mov_b32 s62,s63
ds_read_b128 v[68:71],v2 offset:12288)"
R"(
v_fma_f32 v86,v118,v86,v117
v_mfma_f32_16x16x32_bf16 v[32:35],a[88:91],v[80:83],v[32:35]
v_exp_f32 v85,v85
v_mfma_f32_16x16x32_bf16 v[36:39],a[92:95],v[80:83],v[36:39]
s_waitcnt lgkmcnt(3)
v_mul_f32 v86,v42,v86
v_fma_f32 v85,v85,v119,v119
v_mfma_f32_16x16x32_bf16 v[28:31],a[96:99],v[52:55],v[28:31]
ds_read_b128 v[72:75],v2 offset:13312
v_fma_f32 v87,v118,v87,v117
v_mfma_f32_16x16x32_bf16 v[32:35],a[100:103],v[52:55],v[32:35]
v_exp_f32 v86,v86
v_mfma_f32_16x16x32_bf16 v[36:39],a[104:107],v[52:55],v[36:39]
v_rcp_f32 v85,v85
v_mfma_f32_16x16x32_bf16 v[28:31],a[108:111],v[56:59],v[28:31]
ds_read_b128 v[76:79],v2 offset:14336
v_mul_f32 v87,v43,v87
v_mfma_f32_16x16x32_bf16 v[32:35],a[112:115],v[56:59],v[32:35]
v_exp_f32 v87,v87
v_mfma_f32_16x16x32_bf16 v[36:39],a[116:119],v[56:59],v[36:39]
s_waitcnt lgkmcnt(3)
v_fma_f32 v86,v86,v119,v119
v_fma_f32 v87,v87,v119,v119
v_mfma_f32_16x16x32_bf16 v[28:31],a[120:123],v[60:63],v[28:31]
ds_read_b128 v[80:83],v2 offset:15360
v_mul_f32 v40,v40,v84
v_mfma_f32_16x16x32_bf16 v[32:35],a[124:127],v[60:63],v[32:35]
s_waitcnt vmcnt(7)
v_rcp_f32 v86,v86
v_mfma_f32_16x16x32_bf16 v[36:39],a[128:131],v[60:63],v[36:39]
s_barrier
v_mfma_f32_16x16x32_bf16 v[28:31],a[132:135],v[64:67],v[28:31]
ds_read_b128 v[52:55],v2 offset:16384
v_add_f32_dpp v104,v104,v104 quad_perm:[1,0,3,2] row_mask:0xf bank_mask:0xf
v_mfma_f32_16x16x32_bf16 v[32:35],a[136:139],v[64:67],v[32:35]
v_rcp_f32 v87,v87
v_mfma_f32_16x16x32_bf16 v[36:39],a[140:143],v[64:67],v[36:39]
s_waitcnt lgkmcnt(3)
v_add_f32_dpp v104,v104,v104 quad_perm:[2,3,0,1] row_mask:0xf bank_mask:0xf
v_mul_f32 v41,v41,v85
v_mfma_f32_16x16x32_bf16 v[28:31],a[144:147],v[68:71],v[28:31]
ds_read_b128 v[56:59],v2 offset:17408
v_add_f32_dpp v104,v104,v104 row_half_mirror row_mask:0xf bank_mask:0xf
v_mfma_f32_16x16x32_bf16 v[32:35],a[148:151],v[68:71],v[32:35]
v_mul_f32 v42,v42,v86
v_mul_f32 v43,v43,v87
v_mfma_f32_16x16x32_bf16 v[36:39],a[152:155],v[68:71],v[36:39]
v_cvt_pk_bf16_f32 v96,v40,v41
v_cvt_pk_bf16_f32 v97,v42,v43
v_mfma_f32_16x16x32_bf16 v[28:31],a[156:159],v[72:75],v[28:31]
ds_read_b128 v[60:63],v2 offset:18432
v_fma_f32 v44,-v116,v20,v44
v_mfma_f32_16x16x32_bf16 v[32:35],a[160:163],v[72:75],v[32:35]
v_fma_f32 v45,-v116,v21,v45
v_fma_f32 v46,-v116,v22,v46
v_mfma_f32_16x16x32_bf16 v[36:39],a[164:167],v[72:75],v[36:39]
s_waitcnt lgkmcnt(3)
v_fma_f32 v47,-v116,v23,v47
v_mul_f32 v88,v44,v44
v_mfma_f32_16x16x32_bf16 v[28:31],a[168:171],v[76:79],v[28:31]
ds_read_b128 v[64:67],v2 offset:19456
v_mul_f32 v89,v45,v45
v_mfma_f32_16x16x32_bf16 v[32:35],a[172:175],v[76:79],v[32:35]
v_mul_f32 v90,v46,v46
v_mul_f32 v91,v47,v47
v_mfma_f32_16x16x32_bf16 v[36:39],a[176:179],v[76:79],v[36:39]
v_fma_f32 v88,v118,v88,v117
v_mul_f32 v88,v44,v88
v_mfma_f32_16x16x32_bf16 v[28:31],a[180:183],v[80:83],v[28:31]
ds_read_b128 v[68:71],v2 offset:20480
v_fma_f32 v89,v118,v89,v117
v_mfma_f32_16x16x32_bf16 v[32:35],a[184:187],v[80:83],v[32:35]
buffer_load_dwordx4 v1,s[36:39],s61 offen offset:0 sc1 lds
v_mfma_f32_16x16x32_bf16 v[36:39],a[188:191],v[80:83],v[36:39]
s_waitcnt lgkmcnt(3)
ds_write_b32 v10,v104 offset:0
v_mfma_f32_16x16x32_bf16 v[28:31],a[192:195],v[52:55],v[28:31]
ds_read_b128 v[72:75],v2 offset:21504
v_mul_f32 v89,v45,v89
v_mfma_f32_16x16x32_bf16 v[32:35],a[196:199],v[52:55],v[32:35]
v_exp_f32 v88,v88
v_mfma_f32_16x16x32_bf16 v[36:39],a[200:203],v[52:55],v[36:39]
buffer_load_dwordx4 v1,s[36:39],s61 offen offset:1024 sc1 lds
v_mfma_f32_16x16x32_bf16 v[28:31],a[204:207],v[56:59],v[28:31]
ds_read_b128 v[76:79],v2 offset:22528
v_fma_f32 v88,v88,v119,v119
v_mfma_f32_16x16x32_bf16 v[32:35],a[208:211],v[56:59],v[32:35]
v_exp_f32 v89,v89
v_mfma_f32_16x16x32_bf16 v[36:39],a[212:215],v[56:59],v[36:39]
s_waitcnt lgkmcnt(4)
v_rcp_f32 v88,v88
v_mfma_f32_16x16x32_bf16 v[28:31],a[216:219],v[60:63],v[28:31]
buffer_load_dwordx4 v1,s[36:39],s61 offen offset:2048 sc1 lds
v_mfma_f32_16x16x32_bf16 v[32:35],a[220:223],v[60:63],v[32:35]
ds_read_b128 v[80:83],v2 offset:23552
v_fma_f32 v89,v89,v119,v119
v_mfma_f32_16x16x32_bf16 v[36:39],a[224:227],v[60:63],v[36:39]
v_rcp_f32 v89,v89
v_mfma_f32_16x16x32_bf16 v[28:31],a[228:231],v[64:67],v[28:31]
ds_read_b128 v[52:55],v2 offset:24576
v_fma_f32 v90,v118,v90,v117
v_mfma_f32_16x16x32_bf16 v[32:35],a[232:235],v[64:67],v[32:35]
buffer_load_dwordx4 v1,s[36:39],s61 offen offset:3072 sc1 lds
v_mfma_f32_16x16x32_bf16 v[36:39],a[236:239],v[64:67],v[36:39]
s_waitcnt lgkmcnt(3)
buffer_load_dword v108,v8,s[36:39],s72 offen offset:0 sc1
v_mfma_f32_16x16x32_bf16 v[28:31],a[240:243],v[68:71],v[28:31]
ds_read_b128 v[56:59],v2 offset:25600
v_mul_f32 v90,v46,v90
v_mfma_f32_16x16x32_bf16 v[32:35],a[244:247],v[68:71],v[32:35]
s_waitcnt vmcnt(9)
v_exp_f32 v90,v90
v_mfma_f32_16x16x32_bf16 v[36:39],a[248:251],v[68:71],v[36:39]
s_barrier
v_mfma_f32_16x16x32_bf16 v[28:31],a[252:255],v[72:75],v[28:31]
buffer_store_dword v5,v4,s[36:39],s70 offen offset:0 sc0 sc1
ds_read_b128 v[60:63],v2 offset:26624
v_fma_f32 v90,v90,v119,v119
v_fma_f32 v91,v118,v91,v117
v_mfma_f32_16x16x32_bf16 v[32:35],v[128:131],v[72:75],v[32:35]
v_rcp_f32 v90,v90
v_mfma_f32_16x16x32_bf16 v[36:39],v[132:135],v[72:75],v[36:39]
s_waitcnt lgkmcnt(3)
v_mul_f32 v91,v47,v91
v_mul_f32 v44,v44,v88
v_mfma_f32_16x16x32_bf16 v[28:31],v[136:139],v[76:79],v[28:31]
ds_read_b128 v[64:67],v2 offset:27648
v_mul_f32 v45,v45,v89
v_mfma_f32_16x16x32_bf16 v[32:35],v[140:143],v[76:79],v[32:35]
v_exp_f32 v91,v91
v_mfma_f32_16x16x32_bf16 v[36:39],v[144:147],v[76:79],v[36:39]
v_fma_f32 v91,v91,v119,v119
v_mul_f32 v46,v46,v90
v_mfma_f32_16x16x32_bf16 v[28:31],v[148:151],v[80:83],v[28:31]
ds_read_b128 v[68:71],v2 offset:28672
v_cvt_pk_bf16_f32 v98,v44,v45
v_mfma_f32_16x16x32_bf16 v[32:35],v[152:155],v[80:83],v[32:35]
v_rcp_f32 v91,v91
v_mfma_f32_16x16x32_bf16 v[36:39],v[156:159],v[80:83],v[36:39]
s_waitcnt lgkmcnt(3)
v_mul_f32 v47,v47,v91
v_cvt_pk_bf16_f32 v99,v46,v47
v_mfma_f32_16x16x32_bf16 v[28:31],v[160:163],v[52:55],v[28:31]
ds_read_b128 v[72:75],v2 offset:29696
v_fma_f32 v48,-v116,v24,v48
v_mfma_f32_16x16x32_bf16 v[32:35],v[164:167],v[52:55],v[32:35]
v_fma_f32 v49,-v116,v25,v49
v_fma_f32 v50,-v116,v26,v50
v_mfma_f32_16x16x32_bf16 v[36:39],v[168:171],v[52:55],v[36:39]
v_fma_f32 v51,-v116,v27,v51
v_mul_f32 v92,v48,v48
v_mfma_f32_16x16x32_bf16 v[28:31],v[172:175],v[56:59],v[28:31]
ds_read_b128 v[76:79],v2 offset:30720
v_mul_f32 v93,v49,v49
v_mfma_f32_16x16x32_bf16 v[32:35],v[176:179],v[56:59],v[32:35]
buffer_store_dwordx4 v[96:99],v3,s[36:39],s73 offen offset:0 sc0 sc1
v_mul_f32 v94,v50,v50
v_mul_f32 v95,v51,v51
v_fma_f32 v92,v118,v92,v117
v_mfma_f32_16x16x32_bf16 v[36:39],v[180:183],v[56:59],v[36:39]
s_waitcnt lgkmcnt(3)
v_mul_f32 v92,v48,v92
v_fma_f32 v93,v118,v93,v117
v_mfma_f32_16x16x32_bf16 v[28:31],v[184:187],v[60:63],v[28:31]
ds_read_b128 v[80:83],v2 offset:31744
v_mul_f32 v93,v49,v93
v_mfma_f32_16x16x32_bf16 v[32:35],v[188:191],v[60:63],v[32:35]
s_waitcnt vmcnt(7)
v_exp_f32 v92,v92
v_mfma_f32_16x16x32_bf16 v[36:39],v[192:195],v[60:63],v[36:39]
s_barrier
v_mfma_f32_16x16x32_bf16 v[28:31],v[196:199],v[64:67],v[28:31]
ds_read_b128 v[52:55],v2 offset:32768
ds_read_b32 v13,v7 offset:0
v_mfma_f32_16x16x32_bf16 v[32:35],v[200:203],v[64:67],v[32:35]
ds_read_b64 v[112:113],v11 offset:0
v_fma_f32 v92,v92,v119,v119
v_mfma_f32_16x16x32_bf16 v[36:39],v[204:207],v[64:67],v[36:39]
s_waitcnt lgkmcnt(5)
v_exp_f32 v93,v93
v_mfma_f32_16x16x32_bf16 v[28:31],v[208:211],v[68:71],v[28:31]
ds_read_b128 v[56:59],v2 offset:33792
v_fma_f32 v93,v93,v119,v119
v_mfma_f32_16x16x32_bf16 v[32:35],v[212:215],v[68:71],v[32:35]
s_waitcnt lgkmcnt(2)
v_rcp_f32 v92,v92
v_mfma_f32_16x16x32_bf16 v[36:39],v[216:219],v[68:71],v[36:39]
s_waitcnt lgkmcnt(1)
v_cmp_eq_u32 vcc,s60,v13
v_rcp_f32 v93,v93
v_fma_f32 v94,v118,v94,v117
v_mfma_f32_16x16x32_bf16 v[28:31],v[220:223],v[72:75],v[28:31]
s_cmp_eq_u32 vcc_lo,-1
ds_read_b128 v[60:63],v2 offset:34816
v_mul_f32 v94,v50,v94
v_mfma_f32_16x16x32_bf16 v[32:35],v[224:227],v[72:75],v[32:35]
s_cselect_b32 s67,s86,s59
v_exp_f32 v94,v94
v_mfma_f32_16x16x32_bf16 v[36:39],v[228:231],v[72:75],v[36:39]
v_fma_f32 v94,v94,v119,v119)"
R"(
v_fma_f32 v95,v118,v95,v117
v_mfma_f32_16x16x32_bf16 v[28:31],v[232:235],v[76:79],v[28:31]
s_lshr_b32 s61,s67,0
v_mul_f32 v95,v51,v95
v_exp_f32 v95,v95
v_mfma_f32_16x16x32_bf16 v[32:35],v[236:239],v[76:79],v[32:35]
s_xor_b32 s63,s63,65536
v_fma_f32 v95,v95,v119,v119
v_rcp_f32 v94,v94
v_rcp_f32 v95,v95
v_mfma_f32_16x16x32_bf16 v[36:39],v[240:243],v[76:79],v[36:39]
s_cmp_ge_u32 s66,s59
ds_read_b128 v[64:67],v2 offset:35840
v_mul_f32 v48,v48,v92
v_mul_f32 v49,v49,v93
v_mul_f32 v50,v50,v94
v_mul_f32 v51,v51,v95
v_cvt_pk_bf16_f32 v100,v48,v49
v_cvt_pk_bf16_f32 v101,v50,v51
v_mfma_f32_16x16x32_bf16 v[28:31],v[244:247],v[80:83],v[28:31]
buffer_store_dwordx2 v[100:101],v9,s[36:39],s73 offen offset:0 sc0 sc1
ds_read_b128 v[68:71],v2 offset:36864
v_mfma_f32_16x16x32_bf16 v[32:35],v[248:251],v[80:83],v[32:35]
s_waitcnt lgkmcnt(3)
v_mfma_f32_16x16x32_bf16 v[36:39],v[252:255],v[80:83],v[36:39]
s_cbranch_scc1 Lg3_slow1_%=
Lg3_d1w3_%=:
v_mfma_f32_16x16x32_bf16 v[40:43],a[0:3],v[52:55],0
s_mov_b32 m0,s62
ds_read_b128 v[72:75],v2 offset:37888
v_mul_f32 v116,s75,v112
v_mfma_f32_16x16x32_bf16 v[44:47],a[4:7],v[52:55],0
s_mul_i32 s73,s48,7
v_fma_f32 v113,v113,s75,v12
v_fma_f32 v113,-v116,v116,v113
v_mfma_f32_16x16x32_bf16 v[48:51],a[8:11],v[52:55],0
s_lshr_b32 s73,s73,1
v_max_f32 v113,v113,v12
v_rsq_f32 v112,v113
v_mfma_f32_16x16x32_bf16 v[40:43],a[12:15],v[56:59],v[40:43]
s_add_u32 s74,s53,s55
ds_read_b128 v[76:79],v2 offset:38912
v_mul_f32 v117,s76,v112
v_mfma_f32_16x16x32_bf16 v[44:47],a[16:19],v[56:59],v[44:47]
buffer_load_dwordx4 v0,s[36:39],s61 offen offset:0 sc1 lds
v_mfma_f32_16x16x32_bf16 v[48:51],a[20:23],v[56:59],v[48:51]
s_waitcnt lgkmcnt(3)
v_mul_f32 v118,v112,v112
v_mul_f32 v119,v113,v112
v_mfma_f32_16x16x32_bf16 v[40:43],a[24:27],v[60:63],v[40:43]
s_cmp_eq_u32 s74,s56
ds_read_b128 v[80:83],v2 offset:39936
v_mul_f32 v118,s77,v118
v_mfma_f32_16x16x32_bf16 v[44:47],a[28:31],v[60:63],v[44:47]
s_cselect_b32 s74,s57,s74
v_mul_f32 v118,v118,v117
v_fma_f32 v28,-v116,v16,v28
v_mfma_f32_16x16x32_bf16 v[48:51],a[32:35],v[60:63],v[48:51]
buffer_load_dwordx4 v0,s[36:39],s61 offen offset:1024 sc1 lds
v_mfma_f32_16x16x32_bf16 v[40:43],a[36:39],v[64:67],v[40:43]
s_cmp_eq_u32 s74,s58
ds_read_b128 v[52:55],v2 offset:40960
v_fma_f32 v29,-v116,v17,v29
v_mfma_f32_16x16x32_bf16 v[44:47],a[40:43],v[64:67],v[44:47]
s_cselect_b32 s74,s59,s74
v_fma_f32 v30,-v116,v18,v30
v_fma_f32 v31,-v116,v19,v31
v_mfma_f32_16x16x32_bf16 v[48:51],a[44:47],v[64:67],v[48:51]
s_waitcnt lgkmcnt(3)
v_mul_f32 v84,v28,v28
v_mul_f32 v85,v29,v29
v_mfma_f32_16x16x32_bf16 v[40:43],a[48:51],v[68:71],v[40:43]
buffer_load_dwordx4 v0,s[36:39],s61 offen offset:2048 sc1 lds
v_mfma_f32_16x16x32_bf16 v[44:47],a[52:55],v[68:71],v[44:47]
s_cmp_ge_u32 s67,s59
ds_read_b128 v[56:59],v2 offset:41984
v_mul_f32 v86,v30,v30
v_mfma_f32_16x16x32_bf16 v[48:51],a[56:59],v[68:71],v[48:51]
s_cselect_b32 s86,s50,s51
v_mul_f32 v87,v31,v31
v_fma_f32 v84,v118,v84,v117
v_mfma_f32_16x16x32_bf16 v[40:43],a[60:63],v[72:75],v[40:43]
s_mul_i32 s70,s47,5
ds_read_b128 v[60:63],v2 offset:43008
v_mul_f32 v84,v28,v84
v_mfma_f32_16x16x32_bf16 v[44:47],a[64:67],v[72:75],v[44:47]
buffer_load_dwordx4 v0,s[36:39],s61 offen offset:3072 sc1 lds
v_mfma_f32_16x16x32_bf16 v[48:51],a[68:71],v[72:75],v[48:51]
s_waitcnt lgkmcnt(3)
v_exp_f32 v84,v84
v_mfma_f32_16x16x32_bf16 v[40:43],a[72:75],v[76:79],v[40:43]
s_add_u32 m0,s62,16384
ds_read_b128 v[64:67],v2 offset:44032
v_fma_f32 v84,v84,v119,v119
v_mfma_f32_16x16x32_bf16 v[44:47],a[76:79],v[76:79],v[44:47]
s_lshr_b32 s70,s70,6
v_rcp_f32 v84,v84
v_mfma_f32_16x16x32_bf16 v[48:51],a[80:83],v[76:79],v[48:51]
s_lshr_b32 s72,s61,5
v_fma_f32 v85,v118,v85,v117
v_mul_f32 v85,v29,v85
v_mfma_f32_16x16x32_bf16 v[40:43],a[84:87],v[80:83],v[40:43]
s_mov_b32 s62,s64
ds_read_b128 v[68:71],v2 offset:45056
v_fma_f32 v86,v118,v86,v117
v_mfma_f32_16x16x32_bf16 v[44:47],a[88:91],v[80:83],v[44:47]
s_mov_b64 s[44:45],s[46:47]
v_exp_f32 v85,v85
v_mfma_f32_16x16x32_bf16 v[48:51],a[92:95],v[80:83],v[48:51]
s_waitcnt lgkmcnt(3)
v_mul_f32 v86,v30,v86
v_fma_f32 v85,v85,v119,v119
v_mfma_f32_16x16x32_bf16 v[40:43],a[96:99],v[52:55],v[40:43]
s_mov_b64 s[46:47],s[48:49]
ds_read_b128 v[72:75],v2 offset:46080
v_exp_f32 v86,v86
v_mfma_f32_16x16x32_bf16 v[44:47],a[100:103],v[52:55],v[44:47]
s_mov_b64 s[48:49],s[50:51]
v_rcp_f32 v85,v85
v_mfma_f32_16x16x32_bf16 v[48:51],a[104:107],v[52:55],v[48:51]
s_mov_b64 s[50:51],s[52:53]
v_fma_f32 v86,v86,v119,v119
v_fma_f32 v87,v118,v87,v117
v_mfma_f32_16x16x32_bf16 v[40:43],a[108:111],v[56:59],v[40:43]
s_mov_b32 s52,s74
ds_read_b128 v[76:79],v2 offset:47104
v_mul_f32 v87,v31,v87
v_mfma_f32_16x16x32_bf16 v[44:47],a[112:115],v[56:59],v[44:47]
v_exp_f32 v87,v87
v_mfma_f32_16x16x32_bf16 v[48:51],a[116:119],v[56:59],v[48:51]
s_waitcnt lgkmcnt(3)
v_rcp_f32 v86,v86
v_mfma_f32_16x16x32_bf16 v[40:43],a[120:123],v[60:63],v[40:43]
ds_read_b128 v[80:83],v2 offset:48128
v_fma_f32 v87,v87,v119,v119
v_mfma_f32_16x16x32_bf16 v[44:47],a[124:127],v[60:63],v[44:47]
s_waitcnt vmcnt(7)
v_rcp_f32 v87,v87
v_mfma_f32_16x16x32_bf16 v[48:51],a[128:131],v[60:63],v[48:51]
s_barrier
v_mfma_f32_16x16x32_bf16 v[40:43],a[132:135],v[64:67],v[40:43]
ds_read_b128 v[52:55],v2 offset:49152
v_add_f32_dpp v108,v108,v108 quad_perm:[1,0,3,2] row_mask:0xf bank_mask:0xf
v_mfma_f32_16x16x32_bf16 v[44:47],a[136:139],v[64:67],v[44:47]
s_nop 0
v_add_f32_dpp v108,v108,v108 quad_perm:[2,3,0,1] row_mask:0xf bank_mask:0xf
v_mul_f32 v28,v28,v84
v_mfma_f32_16x16x32_bf16 v[48:51],a[140:143],v[64:67],v[48:51]
s_waitcnt lgkmcnt(3)
v_add_f32_dpp v108,v108,v108 row_half_mirror row_mask:0xf bank_mask:0xf
v_mul_f32 v29,v29,v85
v_mfma_f32_16x16x32_bf16 v[40:43],a[144:147],v[68:71],v[40:43]
ds_read_b128 v[56:59],v2 offset:50176
v_mul_f32 v30,v30,v86
v_mfma_f32_16x16x32_bf16 v[44:47],a[148:151],v[68:71],v[44:47]
v_mul_f32 v31,v31,v87
v_cvt_pk_bf16_f32 v96,v28,v29
v_mfma_f32_16x16x32_bf16 v[48:51],a[152:155],v[68:71],v[48:51]
v_cvt_pk_bf16_f32 v97,v30,v31
v_fma_f32 v32,-v116,v20,v32
v_mfma_f32_16x16x32_bf16 v[40:43],a[156:159],v[72:75],v[40:43]
ds_read_b128 v[60:63],v2 offset:51200
v_fma_f32 v33,-v116,v21,v33
v_mfma_f32_16x16x32_bf16 v[44:47],a[160:163],v[72:75],v[44:47]
v_fma_f32 v34,-v116,v22,v34
v_fma_f32 v35,-v116,v23,v35
v_mfma_f32_16x16x32_bf16 v[48:51],a[164:167],v[72:75],v[48:51]
s_waitcnt lgkmcnt(3)
v_mul_f32 v88,v32,v32
v_mul_f32 v89,v33,v33
v_mfma_f32_16x16x32_bf16 v[40:43],a[168:171],v[76:79],v[40:43]
ds_read_b128 v[64:67],v2 offset:52224
v_mul_f32 v90,v34,v34
v_mfma_f32_16x16x32_bf16 v[44:47],a[172:175],v[76:79],v[44:47]
v_mul_f32 v91,v35,v35
v_fma_f32 v88,v118,v88,v117
v_mfma_f32_16x16x32_bf16 v[48:51],a[176:179],v[76:79],v[48:51]
v_mul_f32 v88,v32,v88
v_fma_f32 v89,v118,v89,v117
v_mfma_f32_16x16x32_bf16 v[40:43],a[180:183],v[80:83],v[40:43]
ds_read_b128 v[68:71],v2 offset:53248
v_mul_f32 v89,v33,v89
v_mfma_f32_16x16x32_bf16 v[44:47],a[184:187],v[80:83],v[44:47]
buffer_load_dwordx4 v1,s[36:39],s61 offen offset:0 sc1 lds
v_mfma_f32_16x16x32_bf16 v[48:51],a[188:191],v[80:83],v[48:51]
s_waitcnt lgkmcnt(3)
ds_write_b32 v10,v108 offset:128
v_fma_f32 v90,v118,v90,v117
v_mfma_f32_16x16x32_bf16 v[40:43],a[192:195],v[52:55],v[40:43]
ds_read_b128 v[72:75],v2 offset:54272
v_fma_f32 v91,v118,v91,v117
v_mfma_f32_16x16x32_bf16 v[44:47],a[196:199],v[52:55],v[44:47]
v_exp_f32 v88,v88
v_mfma_f32_16x16x32_bf16 v[48:51],a[200:203],v[52:55],v[48:51]
buffer_load_dwordx4 v1,s[36:39],s61 offen offset:1024 sc1 lds
v_mfma_f32_16x16x32_bf16 v[40:43],a[204:207],v[56:59],v[40:43]
ds_read_b128 v[76:79],v2 offset:55296
v_mul_f32 v90,v34,v90
v_mfma_f32_16x16x32_bf16 v[44:47],a[208:211],v[56:59],v[44:47]
v_exp_f32 v89,v89)"
R"(
v_mfma_f32_16x16x32_bf16 v[48:51],a[212:215],v[56:59],v[48:51]
s_waitcnt lgkmcnt(4)
v_exp_f32 v90,v90
v_mfma_f32_16x16x32_bf16 v[40:43],a[216:219],v[60:63],v[40:43]
buffer_load_dwordx4 v1,s[36:39],s61 offen offset:2048 sc1 lds
v_mfma_f32_16x16x32_bf16 v[44:47],a[220:223],v[60:63],v[44:47]
ds_read_b128 v[80:83],v2 offset:56320
v_mul_f32 v91,v35,v91
v_mfma_f32_16x16x32_bf16 v[48:51],a[224:227],v[60:63],v[48:51]
v_exp_f32 v91,v91
v_mfma_f32_16x16x32_bf16 v[40:43],a[228:231],v[64:67],v[40:43]
ds_read_b128 v[52:55],v2 offset:57344
v_fma_f32 v88,v88,v119,v119
v_mfma_f32_16x16x32_bf16 v[44:47],a[232:235],v[64:67],v[44:47]
buffer_load_dwordx4 v1,s[36:39],s61 offen offset:3072 sc1 lds
v_mfma_f32_16x16x32_bf16 v[48:51],a[236:239],v[64:67],v[48:51]
s_waitcnt lgkmcnt(3)
buffer_load_dword v104,v8,s[36:39],s72 offen offset:0 sc1
v_mfma_f32_16x16x32_bf16 v[40:43],a[240:243],v[68:71],v[40:43]
ds_read_b128 v[56:59],v2 offset:58368
v_fma_f32 v89,v89,v119,v119
v_mfma_f32_16x16x32_bf16 v[44:47],a[244:247],v[68:71],v[44:47]
s_waitcnt vmcnt(9)
v_rcp_f32 v88,v88
v_mfma_f32_16x16x32_bf16 v[48:51],a[248:251],v[68:71],v[48:51]
s_barrier
v_mfma_f32_16x16x32_bf16 v[40:43],a[252:255],v[72:75],v[40:43]
buffer_store_dword v5,v4,s[36:39],s70 offen offset:0 sc0 sc1
ds_read_b128 v[60:63],v2 offset:59392
v_rcp_f32 v89,v89
v_mfma_f32_16x16x32_bf16 v[44:47],v[128:131],v[72:75],v[44:47]
v_fma_f32 v90,v90,v119,v119
v_fma_f32 v91,v91,v119,v119
v_mfma_f32_16x16x32_bf16 v[48:51],v[132:135],v[72:75],v[48:51]
s_waitcnt lgkmcnt(3)
v_rcp_f32 v90,v90
v_mfma_f32_16x16x32_bf16 v[40:43],v[136:139],v[76:79],v[40:43]
ds_read_b128 v[64:67],v2 offset:60416
v_mul_f32 v32,v32,v88
v_mfma_f32_16x16x32_bf16 v[44:47],v[140:143],v[76:79],v[44:47]
v_rcp_f32 v91,v91
v_mfma_f32_16x16x32_bf16 v[48:51],v[144:147],v[76:79],v[48:51]
v_mul_f32 v33,v33,v89
v_mul_f32 v34,v34,v90
v_mfma_f32_16x16x32_bf16 v[40:43],v[148:151],v[80:83],v[40:43]
ds_read_b128 v[68:71],v2 offset:61440
v_mul_f32 v35,v35,v91
v_mfma_f32_16x16x32_bf16 v[44:47],v[152:155],v[80:83],v[44:47]
v_cvt_pk_bf16_f32 v98,v32,v33
v_cvt_pk_bf16_f32 v99,v34,v35
v_fma_f32 v36,-v116,v24,v36
v_mfma_f32_16x16x32_bf16 v[48:51],v[156:159],v[80:83],v[48:51]
s_waitcnt lgkmcnt(3)
v_fma_f32 v37,-v116,v25,v37
v_fma_f32 v38,-v116,v26,v38
v_mfma_f32_16x16x32_bf16 v[40:43],v[160:163],v[52:55],v[40:43]
ds_read_b128 v[72:75],v2 offset:62464
v_fma_f32 v39,-v116,v27,v39
v_mfma_f32_16x16x32_bf16 v[44:47],v[164:167],v[52:55],v[44:47]
v_mul_f32 v92,v36,v36
v_mul_f32 v93,v37,v37
v_mfma_f32_16x16x32_bf16 v[48:51],v[168:171],v[52:55],v[48:51]
v_mul_f32 v94,v38,v38
v_mul_f32 v95,v39,v39
v_mfma_f32_16x16x32_bf16 v[40:43],v[172:175],v[56:59],v[40:43]
ds_read_b128 v[76:79],v2 offset:63488
v_fma_f32 v92,v118,v92,v117
v_mfma_f32_16x16x32_bf16 v[44:47],v[176:179],v[56:59],v[44:47]
buffer_store_dwordx4 v[96:99],v3,s[36:39],s73 offen offset:0 sc0 sc1
v_mul_f32 v92,v36,v92
v_exp_f32 v92,v92
v_mfma_f32_16x16x32_bf16 v[48:51],v[180:183],v[56:59],v[48:51]
s_waitcnt lgkmcnt(3)
v_fma_f32 v92,v92,v119,v119
v_fma_f32 v93,v118,v93,v117
v_mfma_f32_16x16x32_bf16 v[40:43],v[184:187],v[60:63],v[40:43]
ds_read_b128 v[80:83],v2 offset:64512
v_mul_f32 v93,v37,v93
v_mfma_f32_16x16x32_bf16 v[44:47],v[188:191],v[60:63],v[44:47]
s_waitcnt vmcnt(7)
v_xor_b32 v2,65536,v2
v_fma_f32 v94,v118,v94,v117
v_mfma_f32_16x16x32_bf16 v[48:51],v[192:195],v[60:63],v[48:51]
s_barrier
v_mfma_f32_16x16x32_bf16 v[40:43],v[196:199],v[64:67],v[40:43]
ds_read_b128 v[52:55],v2 offset:0
ds_read_b32 v13,v7 offset:256
v_mfma_f32_16x16x32_bf16 v[44:47],v[200:203],v[64:67],v[44:47]
ds_read_b64 v[112:113],v11 offset:128
v_fma_f32 v95,v118,v95,v117
v_mfma_f32_16x16x32_bf16 v[48:51],v[204:207],v[64:67],v[48:51]
s_waitcnt lgkmcnt(5)
v_exp_f32 v93,v93
v_mfma_f32_16x16x32_bf16 v[40:43],v[208:211],v[68:71],v[40:43]
ds_read_b128 v[56:59],v2 offset:1024
v_mul_f32 v94,v38,v94
v_mfma_f32_16x16x32_bf16 v[44:47],v[212:215],v[68:71],v[44:47]
s_waitcnt lgkmcnt(2)
v_exp_f32 v94,v94
v_mfma_f32_16x16x32_bf16 v[48:51],v[216:219],v[68:71],v[48:51]
s_waitcnt lgkmcnt(1)
v_cmp_eq_u32 vcc,s60,v13
v_rcp_f32 v92,v92
v_mul_f32 v95,v39,v95
v_mfma_f32_16x16x32_bf16 v[40:43],v[220:223],v[72:75],v[40:43]
s_cmp_eq_u32 vcc_lo,-1
ds_read_b128 v[60:63],v2 offset:2048
v_fma_f32 v93,v93,v119,v119
v_mfma_f32_16x16x32_bf16 v[44:47],v[224:227],v[72:75],v[44:47]
s_cselect_b32 s66,s86,s59
v_exp_f32 v95,v95
v_mfma_f32_16x16x32_bf16 v[48:51],v[228:231],v[72:75],v[48:51]
v_rcp_f32 v93,v93
v_mfma_f32_16x16x32_bf16 v[40:43],v[232:235],v[76:79],v[40:43]
s_lshr_b32 s61,s66,0
ds_read_b128 v[64:67],v2 offset:3072
v_fma_f32 v94,v94,v119,v119
v_mfma_f32_16x16x32_bf16 v[44:47],v[236:239],v[76:79],v[44:47]
s_xor_b32 s64,s64,65536
v_fma_f32 v95,v95,v119,v119
v_rcp_f32 v94,v94
v_rcp_f32 v95,v95
v_mfma_f32_16x16x32_bf16 v[48:51],v[240:243],v[76:79],v[48:51]
s_cmp_lt_u32 s67,s59
v_mul_f32 v36,v36,v92
v_mul_f32 v37,v37,v93
v_mul_f32 v38,v38,v94
v_mul_f32 v39,v39,v95
v_cvt_pk_bf16_f32 v100,v36,v37
v_cvt_pk_bf16_f32 v101,v38,v39
v_mfma_f32_16x16x32_bf16 v[40:43],v[244:247],v[80:83],v[40:43]
buffer_store_dwordx2 v[100:101],v9,s[36:39],s73 offen offset:0 sc0 sc1
ds_read_b128 v[68:71],v2 offset:4096
v_mfma_f32_16x16x32_bf16 v[44:47],v[248:251],v[80:83],v[44:47]
s_waitcnt lgkmcnt(3)
v_mfma_f32_16x16x32_bf16 v[48:51],v[252:255],v[80:83],v[48:51]
s_cbranch_scc1 Lg3_d0w3_%=
s_branch Lg3_slow0_%=
Lg3_slow0_%=:
s_cmp_lt_u32 s66,s59
s_cbranch_scc1 Lg3_gs0_%=
s_cmp_eq_u32 s54,1
s_cselect_b32 s68,s49,s48
s_lshr_b32 s68,s68,5
s_add_u32 m0,s78,8192
s_nop 0
buffer_load_dword v6,s[36:39],s68 offen offset:0 sc1 lds
s_mul_i32 s73,s47,7
s_lshr_b32 s73,s73,1
v_mul_f32 v116,s75,v112
v_fma_f32 v113,v113,s75,v12
v_fma_f32 v113,-v116,v116,v113
v_max_f32 v113,v113,v12
v_rsq_f32 v112,v113
s_nop 0
v_mul_f32 v117,s76,v112
v_mul_f32 v118,v112,v112
v_mul_f32 v119,v113,v112
v_mul_f32 v118,s77,v118
v_mul_f32 v118,v118,v117
v_fma_f32 v40,-v116,v16,v40
v_fma_f32 v41,-v116,v17,v41
v_fma_f32 v42,-v116,v18,v42
v_fma_f32 v43,-v116,v19,v43
v_mul_f32 v84,v40,v40
v_mul_f32 v85,v41,v41
v_mul_f32 v86,v42,v42
v_mul_f32 v87,v43,v43
v_fma_f32 v84,v118,v84,v117
v_fma_f32 v85,v118,v85,v117
v_fma_f32 v86,v118,v86,v117
v_fma_f32 v87,v118,v87,v117
v_mul_f32 v84,v40,v84
v_mul_f32 v85,v41,v85
v_mul_f32 v86,v42,v86
v_mul_f32 v87,v43,v87
v_exp_f32 v84,v84
v_exp_f32 v85,v85
v_exp_f32 v86,v86
v_exp_f32 v87,v87
v_fma_f32 v84,v84,v119,v119
v_fma_f32 v85,v85,v119,v119
v_fma_f32 v86,v86,v119,v119
v_fma_f32 v87,v87,v119,v119
v_rcp_f32 v84,v84
v_rcp_f32 v85,v85
v_rcp_f32 v86,v86
v_rcp_f32 v87,v87
v_mul_f32 v40,v40,v84
v_mul_f32 v41,v41,v85
v_mul_f32 v42,v42,v86
v_mul_f32 v43,v43,v87
v_cvt_pk_bf16_f32 v96,v40,v41
v_cvt_pk_bf16_f32 v97,v42,v43
v_fma_f32 v44,-v116,v20,v44
v_fma_f32 v45,-v116,v21,v45
v_fma_f32 v46,-v116,v22,v46
v_fma_f32 v47,-v116,v23,v47
v_mul_f32 v88,v44,v44
v_mul_f32 v89,v45,v45)"
R"(
v_mul_f32 v90,v46,v46
v_mul_f32 v91,v47,v47
v_fma_f32 v88,v118,v88,v117
v_fma_f32 v89,v118,v89,v117
v_fma_f32 v90,v118,v90,v117
v_fma_f32 v91,v118,v91,v117
v_mul_f32 v88,v44,v88
s_add_u32 m0,s78,8448
s_nop 0
buffer_load_dword v6,s[36:39],s68 offen offset:0 sc1 lds
v_mul_f32 v89,v45,v89
v_mul_f32 v90,v46,v90
v_mul_f32 v91,v47,v91
v_exp_f32 v88,v88
v_exp_f32 v89,v89
v_exp_f32 v90,v90
v_exp_f32 v91,v91
v_fma_f32 v88,v88,v119,v119
v_fma_f32 v89,v89,v119,v119
v_fma_f32 v90,v90,v119,v119
v_fma_f32 v91,v91,v119,v119
v_rcp_f32 v88,v88
v_rcp_f32 v89,v89
v_rcp_f32 v90,v90
v_rcp_f32 v91,v91
v_mul_f32 v44,v44,v88
v_mul_f32 v45,v45,v89
v_mul_f32 v46,v46,v90
v_mul_f32 v47,v47,v91
v_cvt_pk_bf16_f32 v98,v44,v45
v_cvt_pk_bf16_f32 v99,v46,v47
buffer_store_dwordx4 v[96:99],v3,s[36:39],s73 offen offset:0 sc0 sc1
v_fma_f32 v48,-v116,v24,v48
v_fma_f32 v49,-v116,v25,v49
v_fma_f32 v50,-v116,v26,v50
v_fma_f32 v51,-v116,v27,v51
v_mul_f32 v92,v48,v48
v_mul_f32 v93,v49,v49
v_mul_f32 v94,v50,v50
v_mul_f32 v95,v51,v51
v_fma_f32 v92,v118,v92,v117
v_fma_f32 v93,v118,v93,v117
v_fma_f32 v94,v118,v94,v117
v_fma_f32 v95,v118,v95,v117
v_mul_f32 v92,v48,v92
v_mul_f32 v93,v49,v93
v_mul_f32 v94,v50,v94
v_mul_f32 v95,v51,v95
v_exp_f32 v92,v92
v_exp_f32 v93,v93
v_exp_f32 v94,v94
v_exp_f32 v95,v95
v_fma_f32 v92,v92,v119,v119
v_fma_f32 v93,v93,v119,v119
v_fma_f32 v94,v94,v119,v119
v_fma_f32 v95,v95,v119,v119
v_rcp_f32 v92,v92
v_rcp_f32 v93,v93
v_rcp_f32 v94,v94
v_rcp_f32 v95,v95
v_mul_f32 v48,v48,v92
v_mul_f32 v49,v49,v93
v_mul_f32 v50,v50,v94
v_mul_f32 v51,v51,v95
v_cvt_pk_bf16_f32 v100,v48,v49
v_cvt_pk_bf16_f32 v101,v50,v51
buffer_store_dwordx2 v[100:101],v9,s[36:39],s73 offen offset:0 sc0 sc1
s_cmp_ge_u32 s48,s59
s_cbranch_scc1 Lg3_done0_%=
s_add_u32 m0,s78,16384
s_nop 0
buffer_load_dword v6,s[36:39],s68 offen offset:0 sc1 lds
s_waitcnt vmcnt(4)
s_barrier
ds_read_b32 v13,v7 offset:8192
s_mul_i32 s70,s46,5
s_lshr_b32 s70,s70,6
buffer_store_dword v5,v4,s[36:39],s70 offen offset:0 sc0 sc1
s_mov_b32 s46,s59
s_mov_b32 s69,18432
s_waitcnt lgkmcnt(0)
v_cmp_eq_u32 vcc,s60,v13
s_nop 0
s_cmp_eq_u32 vcc_lo,-1
s_cbranch_scc1 Lg3_go0_%=
s_add_u32 m0,s78,16640
s_nop 0
buffer_load_dword v6,s[36:39],s68 offen offset:0 sc1 lds
s_waitcnt vmcnt(5)
s_barrier
ds_read_b32 v13,v7 offset:8448
s_mov_b32 s69,18688
s_waitcnt lgkmcnt(0)
v_cmp_eq_u32 vcc,s60,v13
s_nop 0
s_cmp_eq_u32 vcc_lo,-1
s_cbranch_scc1 Lg3_go0_%=
s_add_u32 m0,s78,8192
s_nop 0
buffer_load_dword v6,s[36:39],s68 offen offset:0 sc1 lds
s_waitcnt vmcnt(3)
s_barrier
ds_read_b32 v13,v7 offset:16384
s_mul_i32 s70,s47,5
s_lshr_b32 s70,s70,6
buffer_store_dword v5,v4,s[36:39],s70 offen offset:0 sc0 sc1
s_mov_b32 s47,s59
s_mov_b32 s69,10240
s_waitcnt lgkmcnt(0)
v_cmp_eq_u32 vcc,s60,v13
s_nop 0
s_cmp_eq_u32 vcc_lo,-1
s_cbranch_scc1 Lg3_go0_%=
s_add_u32 m0,s78,8448
s_nop 0
buffer_load_dword v6,s[36:39],s68 offen offset:0 sc1 lds
s_waitcnt vmcnt(3)
s_barrier
ds_read_b32 v13,v7 offset:16640
s_mov_b32 s69,10496
s_waitcnt lgkmcnt(0)
v_cmp_eq_u32 vcc,s60,v13
s_nop 0
s_cmp_eq_u32 vcc_lo,-1
s_cbranch_scc1 Lg3_go0_%=
Lg3_poll0_%=:
s_add_u32 m0,s78,16384
s_nop 0
buffer_load_dword v6,s[36:39],s68 offen offset:0 sc1 lds
s_waitcnt vmcnt(2)
s_barrier
ds_read_b32 v13,v7 offset:8192
s_mov_b32 s69,18432
s_waitcnt lgkmcnt(0)
v_cmp_eq_u32 vcc,s60,v13
s_nop 0
s_cmp_eq_u32 vcc_lo,-1
s_cbranch_scc1 Lg3_go0_%=
s_sleep 4
s_add_u32 m0,s78,16640
s_nop 0
buffer_load_dword v6,s[36:39],s68 offen offset:0 sc1 lds
s_waitcnt vmcnt(2)
s_barrier
ds_read_b32 v13,v7 offset:8448
s_mov_b32 s69,18688
s_waitcnt lgkmcnt(0)
v_cmp_eq_u32 vcc,s60,v13
s_nop 0
s_cmp_eq_u32 vcc_lo,-1
s_cbranch_scc1 Lg3_go0_%=
s_sleep 4
s_add_u32 m0,s78,8192
s_nop 0
buffer_load_dword v6,s[36:39],s68 offen offset:0 sc1 lds
s_waitcnt vmcnt(2)
s_barrier
ds_read_b32 v13,v7 offset:16384
s_mov_b32 s69,10240
s_waitcnt lgkmcnt(0)
v_cmp_eq_u32 vcc,s60,v13
s_nop 0
s_cmp_eq_u32 vcc_lo,-1
s_cbranch_scc1 Lg3_go0_%=
s_sleep 4
s_add_u32 m0,s78,8448
s_nop 0
buffer_load_dword v6,s[36:39],s68 offen offset:0 sc1 lds
s_waitcnt vmcnt(2)
s_barrier
ds_read_b32 v13,v7 offset:16640
s_mov_b32 s69,10496
s_waitcnt lgkmcnt(0)
v_cmp_eq_u32 vcc,s60,v13
s_nop 0
s_cmp_eq_u32 vcc_lo,-1
s_cbranch_scc1 Lg3_go0_%=
s_sleep 4
s_add_u32 s79,s79,1
s_cmp_lt_u32 s79,20000
s_cbranch_scc1 Lg3_poll0_%=
Lg3_go0_%=:
s_xor_b32 s62,s63,65536
s_lshr_b32 s61,s48,0
s_mov_b32 m0,s62
s_nop 0
buffer_load_dwordx4 v0,s[36:39],s61 offen offset:0 sc1 lds
buffer_load_dwordx4 v0,s[36:39],s61 offen offset:1024 sc1 lds
buffer_load_dwordx4 v0,s[36:39],s61 offen offset:2048 sc1 lds
buffer_load_dwordx4 v0,s[36:39],s61 offen offset:3072 sc1 lds
s_add_u32 m0,s62,16384
s_nop 0
buffer_load_dwordx4 v1,s[36:39],s61 offen offset:0 sc1 lds
buffer_load_dwordx4 v1,s[36:39],s61 offen offset:1024 sc1 lds
buffer_load_dwordx4 v1,s[36:39],s61 offen offset:2048 sc1 lds
buffer_load_dwordx4 v1,s[36:39],s61 offen offset:3072 sc1 lds
s_lshr_b32 s72,s61,5
buffer_load_dword v104,v8,s[36:39],s72 offen offset:0 sc1
v_add_u32 v84,s69,v7
s_waitcnt vmcnt(5))"
R"(
s_barrier
ds_read_b32 v13,v84 offset:0
ds_read_b128 v[52:55],v2 offset:0
ds_read_b128 v[56:59],v2 offset:1024
ds_read_b128 v[60:63],v2 offset:2048
ds_read_b128 v[64:67],v2 offset:3072
ds_read_b128 v[68:71],v2 offset:4096
s_mul_i32 s70,s46,5
s_lshr_b32 s70,s70,6
buffer_store_dword v5,v4,s[36:39],s70 offen offset:0 sc0 sc1
s_mov_b32 s46,s59
s_mul_i32 s70,s47,5
s_lshr_b32 s70,s70,6
buffer_store_dword v5,v4,s[36:39],s70 offen offset:0 sc0 sc1
s_mov_b32 s47,s59
s_mul_i32 s70,s59,5
s_lshr_b32 s70,s70,6
buffer_store_dword v5,v4,s[36:39],s70 offen offset:0 sc0 sc1
s_waitcnt lgkmcnt(5)
v_cmp_eq_u32 vcc,s60,v13
s_nop 0
s_cmp_eq_u32 vcc_lo,-1
s_cselect_b32 s66,s49,s59
s_lshr_b32 s61,s66,0
s_xor_b32 s62,s64,65536
s_waitcnt lgkmcnt(3)
s_setpc_b64 s[90:91]
Lg3_gs0_%=:
s_xor_b32 s62,s63,65536
s_lshr_b32 s61,s48,0
s_cmp_eq_u32 s54,1
s_cselect_b32 s68,s49,s48
s_lshr_b32 s68,s68,5
s_mul_i32 s73,s47,7
s_lshr_b32 s73,s73,1
s_mov_b32 m0,s62
s_nop 0
buffer_load_dwordx4 v0,s[36:39],s61 offen offset:0 sc1 lds
v_mul_f32 v116,s75,v112
v_fma_f32 v113,v113,s75,v12
v_fma_f32 v113,-v116,v116,v113
v_max_f32 v113,v113,v12
v_rsq_f32 v112,v113
s_nop 0
v_mul_f32 v117,s76,v112
v_mul_f32 v118,v112,v112
v_mul_f32 v119,v113,v112
v_mul_f32 v118,s77,v118
v_mul_f32 v118,v118,v117
v_fma_f32 v40,-v116,v16,v40
buffer_load_dwordx4 v0,s[36:39],s61 offen offset:1024 sc1 lds
v_fma_f32 v41,-v116,v17,v41
v_fma_f32 v42,-v116,v18,v42
v_fma_f32 v43,-v116,v19,v43
v_mul_f32 v84,v40,v40
v_mul_f32 v85,v41,v41
v_mul_f32 v86,v42,v42
v_mul_f32 v87,v43,v43
v_fma_f32 v84,v118,v84,v117
v_fma_f32 v85,v118,v85,v117
v_fma_f32 v86,v118,v86,v117
v_fma_f32 v87,v118,v87,v117
buffer_load_dwordx4 v0,s[36:39],s61 offen offset:2048 sc1 lds
v_mul_f32 v84,v40,v84
v_mul_f32 v85,v41,v85
v_mul_f32 v86,v42,v86
v_mul_f32 v87,v43,v87
v_exp_f32 v84,v84
v_exp_f32 v85,v85
v_exp_f32 v86,v86
v_exp_f32 v87,v87
v_fma_f32 v84,v84,v119,v119
v_fma_f32 v85,v85,v119,v119
v_fma_f32 v86,v86,v119,v119
buffer_load_dwordx4 v0,s[36:39],s61 offen offset:3072 sc1 lds
v_fma_f32 v87,v87,v119,v119
v_rcp_f32 v84,v84
v_rcp_f32 v85,v85
v_rcp_f32 v86,v86
v_rcp_f32 v87,v87
v_mul_f32 v40,v40,v84
v_mul_f32 v41,v41,v85
v_mul_f32 v42,v42,v86
v_mul_f32 v43,v43,v87
v_cvt_pk_bf16_f32 v96,v40,v41
v_cvt_pk_bf16_f32 v97,v42,v43
s_add_u32 m0,s78,8192
s_nop 0
buffer_load_dword v6,s[36:39],s68 offen offset:0 sc1 lds
v_fma_f32 v44,-v116,v20,v44
v_fma_f32 v45,-v116,v21,v45
v_fma_f32 v46,-v116,v22,v46
v_fma_f32 v47,-v116,v23,v47
v_mul_f32 v88,v44,v44
v_mul_f32 v89,v45,v45
v_mul_f32 v90,v46,v46
v_mul_f32 v91,v47,v47
v_fma_f32 v88,v118,v88,v117
v_fma_f32 v89,v118,v89,v117
v_fma_f32 v90,v118,v90,v117
s_add_u32 m0,s62,16384
s_nop 0
buffer_load_dwordx4 v1,s[36:39],s61 offen offset:0 sc1 lds
v_fma_f32 v91,v118,v91,v117
v_mul_f32 v88,v44,v88
v_mul_f32 v89,v45,v89
v_mul_f32 v90,v46,v90
v_mul_f32 v91,v47,v91
v_exp_f32 v88,v88
v_exp_f32 v89,v89
v_exp_f32 v90,v90
v_exp_f32 v91,v91
v_fma_f32 v88,v88,v119,v119
v_fma_f32 v89,v89,v119,v119
buffer_load_dwordx4 v1,s[36:39],s61 offen offset:1024 sc1 lds
v_fma_f32 v90,v90,v119,v119
v_fma_f32 v91,v91,v119,v119
v_rcp_f32 v88,v88
v_rcp_f32 v89,v89
v_rcp_f32 v90,v90
v_rcp_f32 v91,v91
v_mul_f32 v44,v44,v88
v_mul_f32 v45,v45,v89
v_mul_f32 v46,v46,v90
v_mul_f32 v47,v47,v91
v_cvt_pk_bf16_f32 v98,v44,v45
buffer_load_dwordx4 v1,s[36:39],s61 offen offset:2048 sc1 lds
v_cvt_pk_bf16_f32 v99,v46,v47
buffer_store_dwordx4 v[96:99],v3,s[36:39],s73 offen offset:0 sc0 sc1
v_fma_f32 v48,-v116,v24,v48
v_fma_f32 v49,-v116,v25,v49
v_fma_f32 v50,-v116,v26,v50
v_fma_f32 v51,-v116,v27,v51
v_mul_f32 v92,v48,v48
v_mul_f32 v93,v49,v49
v_mul_f32 v94,v50,v50
v_mul_f32 v95,v51,v51
v_fma_f32 v92,v118,v92,v117
buffer_load_dwordx4 v1,s[36:39],s61 offen offset:3072 sc1 lds
v_fma_f32 v93,v118,v93,v117
v_fma_f32 v94,v118,v94,v117
v_fma_f32 v95,v118,v95,v117
v_mul_f32 v92,v48,v92
v_mul_f32 v93,v49,v93
v_mul_f32 v94,v50,v94
v_mul_f32 v95,v51,v95
v_exp_f32 v92,v92
v_exp_f32 v93,v93
v_exp_f32 v94,v94
v_exp_f32 v95,v95
s_lshr_b32 s72,s61,5
buffer_load_dword v104,v8,s[36:39],s72 offen offset:0 sc1
v_fma_f32 v92,v92,v119,v119
v_fma_f32 v93,v93,v119,v119
v_fma_f32 v94,v94,v119,v119
v_fma_f32 v95,v95,v119,v119
v_rcp_f32 v92,v92
v_rcp_f32 v93,v93
v_rcp_f32 v94,v94
v_rcp_f32 v95,v95
v_mul_f32 v48,v48,v92
v_mul_f32 v49,v49,v93
v_mul_f32 v50,v50,v94
v_mul_f32 v51,v51,v95
v_cvt_pk_bf16_f32 v100,v48,v49
v_cvt_pk_bf16_f32 v101,v50,v51
buffer_store_dwordx2 v[100:101],v9,s[36:39],s73 offen offset:0 sc0 sc1
s_waitcnt vmcnt(7)
s_barrier
ds_read_b32 v13,v7 offset:8192
ds_read_b128 v[52:55],v2 offset:0
ds_read_b128 v[56:59],v2 offset:1024
ds_read_b128 v[60:63],v2 offset:2048
ds_read_b128 v[64:67],v2 offset:3072
ds_read_b128 v[68:71],v2 offset:4096
s_mul_i32 s70,s46,5
s_lshr_b32 s70,s70,6
buffer_store_dword v5,v4,s[36:39],s70 offen offset:0 sc0 sc1
s_waitcnt lgkmcnt(5)
v_cmp_eq_u32 vcc,s60,v13
s_nop 0
s_cmp_eq_u32 vcc_hi,-1
s_cselect_b32 s66,s49,s59
s_lshr_b32 s61,s66,0
s_xor_b32 s62,s64,65536
s_mov_b32 s46,s47
s_mov_b32 s47,s59
s_mul_i32 s70,s59,5
s_lshr_b32 s70,s70,6
buffer_store_dword v5,v4,s[36:39],s70 offen offset:0 sc0 sc1
s_waitcnt lgkmcnt(3)
s_setpc_b64 s[90:91]
Lg3_done0_%=:
s_waitcnt vmcnt(0)
s_barrier
s_mul_i32 s70,s46,5
s_lshr_b32 s70,s70,6
buffer_store_dword v5,v4,s[36:39],s70 offen offset:0 sc0 sc1
s_mul_i32 s70,s47,5
s_lshr_b32 s70,s70,6)"
R"(
buffer_store_dword v5,v4,s[36:39],s70 offen offset:0 sc0 sc1
s_branch Lg3_done_%=
Lg3_slow1_%=:
s_cmp_lt_u32 s67,s59
s_cbranch_scc1 Lg3_gs1_%=
s_cmp_eq_u32 s54,1
s_cselect_b32 s68,s50,s49
s_lshr_b32 s68,s68,5
s_add_u32 m0,s78,8192
s_nop 0
buffer_load_dword v6,s[36:39],s68 offen offset:0 sc1 lds
s_mul_i32 s73,s48,7
s_lshr_b32 s73,s73,1
v_mul_f32 v116,s75,v112
v_fma_f32 v113,v113,s75,v12
v_fma_f32 v113,-v116,v116,v113
v_max_f32 v113,v113,v12
v_rsq_f32 v112,v113
s_nop 0
v_mul_f32 v117,s76,v112
v_mul_f32 v118,v112,v112
v_mul_f32 v119,v113,v112
v_mul_f32 v118,s77,v118
v_mul_f32 v118,v118,v117
v_fma_f32 v28,-v116,v16,v28
v_fma_f32 v29,-v116,v17,v29
v_fma_f32 v30,-v116,v18,v30
v_fma_f32 v31,-v116,v19,v31
v_mul_f32 v84,v28,v28
v_mul_f32 v85,v29,v29
v_mul_f32 v86,v30,v30
v_mul_f32 v87,v31,v31
v_fma_f32 v84,v118,v84,v117
v_fma_f32 v85,v118,v85,v117
v_fma_f32 v86,v118,v86,v117
v_fma_f32 v87,v118,v87,v117
v_mul_f32 v84,v28,v84
v_mul_f32 v85,v29,v85
v_mul_f32 v86,v30,v86
v_mul_f32 v87,v31,v87
v_exp_f32 v84,v84
v_exp_f32 v85,v85
v_exp_f32 v86,v86
v_exp_f32 v87,v87
v_fma_f32 v84,v84,v119,v119
v_fma_f32 v85,v85,v119,v119
v_fma_f32 v86,v86,v119,v119
v_fma_f32 v87,v87,v119,v119
v_rcp_f32 v84,v84
v_rcp_f32 v85,v85
v_rcp_f32 v86,v86
v_rcp_f32 v87,v87
v_mul_f32 v28,v28,v84
v_mul_f32 v29,v29,v85
v_mul_f32 v30,v30,v86
v_mul_f32 v31,v31,v87
v_cvt_pk_bf16_f32 v96,v28,v29
v_cvt_pk_bf16_f32 v97,v30,v31
v_fma_f32 v32,-v116,v20,v32
v_fma_f32 v33,-v116,v21,v33
v_fma_f32 v34,-v116,v22,v34
v_fma_f32 v35,-v116,v23,v35
v_mul_f32 v88,v32,v32
v_mul_f32 v89,v33,v33
v_mul_f32 v90,v34,v34
v_mul_f32 v91,v35,v35
v_fma_f32 v88,v118,v88,v117
v_fma_f32 v89,v118,v89,v117
v_fma_f32 v90,v118,v90,v117
v_fma_f32 v91,v118,v91,v117
v_mul_f32 v88,v32,v88
s_add_u32 m0,s78,8448
s_nop 0
buffer_load_dword v6,s[36:39],s68 offen offset:0 sc1 lds
v_mul_f32 v89,v33,v89
v_mul_f32 v90,v34,v90
v_mul_f32 v91,v35,v91
v_exp_f32 v88,v88
v_exp_f32 v89,v89
v_exp_f32 v90,v90
v_exp_f32 v91,v91
v_fma_f32 v88,v88,v119,v119
v_fma_f32 v89,v89,v119,v119
v_fma_f32 v90,v90,v119,v119
v_fma_f32 v91,v91,v119,v119
v_rcp_f32 v88,v88
v_rcp_f32 v89,v89
v_rcp_f32 v90,v90
v_rcp_f32 v91,v91
v_mul_f32 v32,v32,v88
v_mul_f32 v33,v33,v89
v_mul_f32 v34,v34,v90
v_mul_f32 v35,v35,v91
v_cvt_pk_bf16_f32 v98,v32,v33
v_cvt_pk_bf16_f32 v99,v34,v35
buffer_store_dwordx4 v[96:99],v3,s[36:39],s73 offen offset:0 sc0 sc1
v_fma_f32 v36,-v116,v24,v36
v_fma_f32 v37,-v116,v25,v37
v_fma_f32 v38,-v116,v26,v38
v_fma_f32 v39,-v116,v27,v39
v_mul_f32 v92,v36,v36
v_mul_f32 v93,v37,v37
v_mul_f32 v94,v38,v38
v_mul_f32 v95,v39,v39
v_fma_f32 v92,v118,v92,v117
v_fma_f32 v93,v118,v93,v117
v_fma_f32 v94,v118,v94,v117
v_fma_f32 v95,v118,v95,v117
v_mul_f32 v92,v36,v92
v_mul_f32 v93,v37,v93
v_mul_f32 v94,v38,v94
v_mul_f32 v95,v39,v95
v_exp_f32 v92,v92
v_exp_f32 v93,v93
v_exp_f32 v94,v94
v_exp_f32 v95,v95
v_fma_f32 v92,v92,v119,v119
v_fma_f32 v93,v93,v119,v119
v_fma_f32 v94,v94,v119,v119
v_fma_f32 v95,v95,v119,v119
v_rcp_f32 v92,v92
v_rcp_f32 v93,v93
v_rcp_f32 v94,v94
v_rcp_f32 v95,v95
v_mul_f32 v36,v36,v92
v_mul_f32 v37,v37,v93
v_mul_f32 v38,v38,v94
v_mul_f32 v39,v39,v95
v_cvt_pk_bf16_f32 v100,v36,v37
v_cvt_pk_bf16_f32 v101,v38,v39
buffer_store_dwordx2 v[100:101],v9,s[36:39],s73 offen offset:0 sc0 sc1
s_cmp_ge_u32 s49,s59
s_cbranch_scc1 Lg3_done1_%=
s_add_u32 m0,s78,16384
s_nop 0
buffer_load_dword v6,s[36:39],s68 offen offset:0 sc1 lds
s_waitcnt vmcnt(4)
s_barrier
ds_read_b32 v13,v7 offset:8192
s_mul_i32 s70,s47,5
s_lshr_b32 s70,s70,6
buffer_store_dword v5,v4,s[36:39],s70 offen offset:0 sc0 sc1
s_mov_b32 s47,s59
s_mov_b32 s69,18432
s_waitcnt lgkmcnt(0)
v_cmp_eq_u32 vcc,s60,v13
s_nop 0
s_cmp_eq_u32 vcc_lo,-1
s_cbranch_scc1 Lg3_go1_%=
s_add_u32 m0,s78,16640
s_nop 0
buffer_load_dword v6,s[36:39],s68 offen offset:0 sc1 lds
s_waitcnt vmcnt(5)
s_barrier
ds_read_b32 v13,v7 offset:8448
s_mov_b32 s69,18688
s_waitcnt lgkmcnt(0)
v_cmp_eq_u32 vcc,s60,v13
s_nop 0
s_cmp_eq_u32 vcc_lo,-1
s_cbranch_scc1 Lg3_go1_%=
s_add_u32 m0,s78,8192
s_nop 0
buffer_load_dword v6,s[36:39],s68 offen offset:0 sc1 lds
s_waitcnt vmcnt(3)
s_barrier
ds_read_b32 v13,v7 offset:16384
s_mul_i32 s70,s48,5
s_lshr_b32 s70,s70,6
buffer_store_dword v5,v4,s[36:39],s70 offen offset:0 sc0 sc1
s_mov_b32 s48,s59
s_mov_b32 s69,10240
s_waitcnt lgkmcnt(0)
v_cmp_eq_u32 vcc,s60,v13
s_nop 0
s_cmp_eq_u32 vcc_lo,-1
s_cbranch_scc1 Lg3_go1_%=
s_add_u32 m0,s78,8448
s_nop 0
buffer_load_dword v6,s[36:39],s68 offen offset:0 sc1 lds
s_waitcnt vmcnt(3)
s_barrier
ds_read_b32 v13,v7 offset:16640
s_mov_b32 s69,10496
s_waitcnt lgkmcnt(0)
v_cmp_eq_u32 vcc,s60,v13
s_nop 0
s_cmp_eq_u32 vcc_lo,-1
s_cbranch_scc1 Lg3_go1_%=
Lg3_poll1_%=:
s_add_u32 m0,s78,16384
s_nop 0
buffer_load_dword v6,s[36:39],s68 offen offset:0 sc1 lds
s_waitcnt vmcnt(2)
s_barrier
ds_read_b32 v13,v7 offset:8192
s_mov_b32 s69,18432
s_waitcnt lgkmcnt(0)
v_cmp_eq_u32 vcc,s60,v13
s_nop 0)"
R"(
s_cmp_eq_u32 vcc_lo,-1
s_cbranch_scc1 Lg3_go1_%=
s_sleep 4
s_add_u32 m0,s78,16640
s_nop 0
buffer_load_dword v6,s[36:39],s68 offen offset:0 sc1 lds
s_waitcnt vmcnt(2)
s_barrier
ds_read_b32 v13,v7 offset:8448
s_mov_b32 s69,18688
s_waitcnt lgkmcnt(0)
v_cmp_eq_u32 vcc,s60,v13
s_nop 0
s_cmp_eq_u32 vcc_lo,-1
s_cbranch_scc1 Lg3_go1_%=
s_sleep 4
s_add_u32 m0,s78,8192
s_nop 0
buffer_load_dword v6,s[36:39],s68 offen offset:0 sc1 lds
s_waitcnt vmcnt(2)
s_barrier
ds_read_b32 v13,v7 offset:16384
s_mov_b32 s69,10240
s_waitcnt lgkmcnt(0)
v_cmp_eq_u32 vcc,s60,v13
s_nop 0
s_cmp_eq_u32 vcc_lo,-1
s_cbranch_scc1 Lg3_go1_%=
s_sleep 4
s_add_u32 m0,s78,8448
s_nop 0
buffer_load_dword v6,s[36:39],s68 offen offset:0 sc1 lds
s_waitcnt vmcnt(2)
s_barrier
ds_read_b32 v13,v7 offset:16640
s_mov_b32 s69,10496
s_waitcnt lgkmcnt(0)
v_cmp_eq_u32 vcc,s60,v13
s_nop 0
s_cmp_eq_u32 vcc_lo,-1
s_cbranch_scc1 Lg3_go1_%=
s_sleep 4
s_add_u32 s79,s79,1
s_cmp_lt_u32 s79,20000
s_cbranch_scc1 Lg3_poll1_%=
Lg3_go1_%=:
s_xor_b32 s62,s64,65536
s_lshr_b32 s61,s49,0
s_mov_b32 m0,s62
s_nop 0
buffer_load_dwordx4 v0,s[36:39],s61 offen offset:0 sc1 lds
buffer_load_dwordx4 v0,s[36:39],s61 offen offset:1024 sc1 lds
buffer_load_dwordx4 v0,s[36:39],s61 offen offset:2048 sc1 lds
buffer_load_dwordx4 v0,s[36:39],s61 offen offset:3072 sc1 lds
s_add_u32 m0,s62,16384
s_nop 0
buffer_load_dwordx4 v1,s[36:39],s61 offen offset:0 sc1 lds
buffer_load_dwordx4 v1,s[36:39],s61 offen offset:1024 sc1 lds
buffer_load_dwordx4 v1,s[36:39],s61 offen offset:2048 sc1 lds
buffer_load_dwordx4 v1,s[36:39],s61 offen offset:3072 sc1 lds
s_lshr_b32 s72,s61,5
buffer_load_dword v108,v8,s[36:39],s72 offen offset:0 sc1
v_add_u32 v84,s69,v7
s_waitcnt vmcnt(5)
s_barrier
ds_read_b32 v13,v84 offset:0
ds_read_b128 v[52:55],v2 offset:32768
ds_read_b128 v[56:59],v2 offset:33792
ds_read_b128 v[60:63],v2 offset:34816
ds_read_b128 v[64:67],v2 offset:35840
ds_read_b128 v[68:71],v2 offset:36864
s_mul_i32 s70,s47,5
s_lshr_b32 s70,s70,6
buffer_store_dword v5,v4,s[36:39],s70 offen offset:0 sc0 sc1
s_mov_b32 s47,s59
s_mul_i32 s70,s48,5
s_lshr_b32 s70,s70,6
buffer_store_dword v5,v4,s[36:39],s70 offen offset:0 sc0 sc1
s_mov_b32 s48,s59
s_mul_i32 s70,s59,5
s_lshr_b32 s70,s70,6
buffer_store_dword v5,v4,s[36:39],s70 offen offset:0 sc0 sc1
s_waitcnt lgkmcnt(5)
v_cmp_eq_u32 vcc,s60,v13
s_nop 0
s_cmp_eq_u32 vcc_lo,-1
s_cselect_b32 s67,s50,s59
s_lshr_b32 s61,s67,0
s_xor_b32 s62,s63,65536
s_waitcnt lgkmcnt(3)
s_setpc_b64 s[92:93]
Lg3_gs1_%=:
s_xor_b32 s62,s64,65536
s_lshr_b32 s61,s49,0
s_cmp_eq_u32 s54,1
s_cselect_b32 s68,s50,s49
s_lshr_b32 s68,s68,5
s_mul_i32 s73,s48,7
s_lshr_b32 s73,s73,1
s_mov_b32 m0,s62
s_nop 0
buffer_load_dwordx4 v0,s[36:39],s61 offen offset:0 sc1 lds
v_mul_f32 v116,s75,v112
v_fma_f32 v113,v113,s75,v12
v_fma_f32 v113,-v116,v116,v113
v_max_f32 v113,v113,v12
v_rsq_f32 v112,v113
s_nop 0
v_mul_f32 v117,s76,v112
v_mul_f32 v118,v112,v112
v_mul_f32 v119,v113,v112
v_mul_f32 v118,s77,v118
v_mul_f32 v118,v118,v117
v_fma_f32 v28,-v116,v16,v28
buffer_load_dwordx4 v0,s[36:39],s61 offen offset:1024 sc1 lds
v_fma_f32 v29,-v116,v17,v29
v_fma_f32 v30,-v116,v18,v30
v_fma_f32 v31,-v116,v19,v31
v_mul_f32 v84,v28,v28
v_mul_f32 v85,v29,v29
v_mul_f32 v86,v30,v30
v_mul_f32 v87,v31,v31
v_fma_f32 v84,v118,v84,v117
v_fma_f32 v85,v118,v85,v117
v_fma_f32 v86,v118,v86,v117
v_fma_f32 v87,v118,v87,v117
buffer_load_dwordx4 v0,s[36:39],s61 offen offset:2048 sc1 lds
v_mul_f32 v84,v28,v84
v_mul_f32 v85,v29,v85
v_mul_f32 v86,v30,v86
v_mul_f32 v87,v31,v87
v_exp_f32 v84,v84
v_exp_f32 v85,v85
v_exp_f32 v86,v86
v_exp_f32 v87,v87
v_fma_f32 v84,v84,v119,v119
v_fma_f32 v85,v85,v119,v119
v_fma_f32 v86,v86,v119,v119
buffer_load_dwordx4 v0,s[36:39],s61 offen offset:3072 sc1 lds
v_fma_f32 v87,v87,v119,v119
v_rcp_f32 v84,v84
v_rcp_f32 v85,v85
v_rcp_f32 v86,v86
v_rcp_f32 v87,v87
v_mul_f32 v28,v28,v84
v_mul_f32 v29,v29,v85
v_mul_f32 v30,v30,v86
v_mul_f32 v31,v31,v87
v_cvt_pk_bf16_f32 v96,v28,v29
v_cvt_pk_bf16_f32 v97,v30,v31
s_add_u32 m0,s78,8192
s_nop 0
buffer_load_dword v6,s[36:39],s68 offen offset:0 sc1 lds
v_fma_f32 v32,-v116,v20,v32
v_fma_f32 v33,-v116,v21,v33
v_fma_f32 v34,-v116,v22,v34
v_fma_f32 v35,-v116,v23,v35
v_mul_f32 v88,v32,v32
v_mul_f32 v89,v33,v33
v_mul_f32 v90,v34,v34
v_mul_f32 v91,v35,v35
v_fma_f32 v88,v118,v88,v117
v_fma_f32 v89,v118,v89,v117
v_fma_f32 v90,v118,v90,v117
s_add_u32 m0,s62,16384
s_nop 0
buffer_load_dwordx4 v1,s[36:39],s61 offen offset:0 sc1 lds
v_fma_f32 v91,v118,v91,v117
v_mul_f32 v88,v32,v88
v_mul_f32 v89,v33,v89
v_mul_f32 v90,v34,v90
v_mul_f32 v91,v35,v91
v_exp_f32 v88,v88
v_exp_f32 v89,v89
v_exp_f32 v90,v90
v_exp_f32 v91,v91
v_fma_f32 v88,v88,v119,v119
v_fma_f32 v89,v89,v119,v119
buffer_load_dwordx4 v1,s[36:39],s61 offen offset:1024 sc1 lds
v_fma_f32 v90,v90,v119,v119
v_fma_f32 v91,v91,v119,v119
v_rcp_f32 v88,v88
v_rcp_f32 v89,v89
v_rcp_f32 v90,v90
v_rcp_f32 v91,v91
v_mul_f32 v32,v32,v88
v_mul_f32 v33,v33,v89
v_mul_f32 v34,v34,v90
v_mul_f32 v35,v35,v91
v_cvt_pk_bf16_f32 v98,v32,v33
buffer_load_dwordx4 v1,s[36:39],s61 offen offset:2048 sc1 lds
v_cvt_pk_bf16_f32 v99,v34,v35
buffer_store_dwordx4 v[96:99],v3,s[36:39],s73 offen offset:0 sc0 sc1
v_fma_f32 v36,-v116,v24,v36
v_fma_f32 v37,-v116,v25,v37
v_fma_f32 v38,-v116,v26,v38
v_fma_f32 v39,-v116,v27,v39
v_mul_f32 v92,v36,v36
v_mul_f32 v93,v37,v37
v_mul_f32 v94,v38,v38)"
R"(
v_mul_f32 v95,v39,v39
v_fma_f32 v92,v118,v92,v117
buffer_load_dwordx4 v1,s[36:39],s61 offen offset:3072 sc1 lds
v_fma_f32 v93,v118,v93,v117
v_fma_f32 v94,v118,v94,v117
v_fma_f32 v95,v118,v95,v117
v_mul_f32 v92,v36,v92
v_mul_f32 v93,v37,v93
v_mul_f32 v94,v38,v94
v_mul_f32 v95,v39,v95
v_exp_f32 v92,v92
v_exp_f32 v93,v93
v_exp_f32 v94,v94
v_exp_f32 v95,v95
s_lshr_b32 s72,s61,5
buffer_load_dword v108,v8,s[36:39],s72 offen offset:0 sc1
v_fma_f32 v92,v92,v119,v119
v_fma_f32 v93,v93,v119,v119
v_fma_f32 v94,v94,v119,v119
v_fma_f32 v95,v95,v119,v119
v_rcp_f32 v92,v92
v_rcp_f32 v93,v93
v_rcp_f32 v94,v94
v_rcp_f32 v95,v95
v_mul_f32 v36,v36,v92
v_mul_f32 v37,v37,v93
v_mul_f32 v38,v38,v94
v_mul_f32 v39,v39,v95
v_cvt_pk_bf16_f32 v100,v36,v37
v_cvt_pk_bf16_f32 v101,v38,v39
buffer_store_dwordx2 v[100:101],v9,s[36:39],s73 offen offset:0 sc0 sc1
s_waitcnt vmcnt(7)
s_barrier
ds_read_b32 v13,v7 offset:8192
ds_read_b128 v[52:55],v2 offset:32768
ds_read_b128 v[56:59],v2 offset:33792
ds_read_b128 v[60:63],v2 offset:34816
ds_read_b128 v[64:67],v2 offset:35840
ds_read_b128 v[68:71],v2 offset:36864
s_mul_i32 s70,s47,5
s_lshr_b32 s70,s70,6
buffer_store_dword v5,v4,s[36:39],s70 offen offset:0 sc0 sc1
s_waitcnt lgkmcnt(5)
v_cmp_eq_u32 vcc,s60,v13
s_nop 0
s_cmp_eq_u32 vcc_hi,-1
s_cselect_b32 s67,s50,s59
s_lshr_b32 s61,s67,0
s_xor_b32 s62,s63,65536
s_mov_b32 s47,s48
s_mov_b32 s48,s59
s_mul_i32 s70,s59,5
s_lshr_b32 s70,s70,6
buffer_store_dword v5,v4,s[36:39],s70 offen offset:0 sc0 sc1
s_waitcnt lgkmcnt(3)
s_setpc_b64 s[92:93]
Lg3_done1_%=:
s_waitcnt vmcnt(0)
s_barrier
s_mul_i32 s70,s47,5
s_lshr_b32 s70,s70,6
buffer_store_dword v5,v4,s[36:39],s70 offen offset:0 sc0 sc1
s_mul_i32 s70,s48,5
s_lshr_b32 s70,s70,6
buffer_store_dword v5,v4,s[36:39],s70 offen offset:0 sc0 sc1
s_branch Lg3_done_%=
Lg3_done_%=:
s_waitcnt vmcnt(0) lgkmcnt(0)
s_barrier
s_endpgm
Lcode_g3_e:)"
  :: "s"(ws), "s"(wave), "s"(sStep), "s"(sX1), "s"(sX2), "s"(sU0), "s"(sWw), "s"(sBuf), "v"(vSrc), "v"(vA), "v"(vOut), "v"(vFlag), "v"(vPollOff), "v"(vSnapRd), "v"(vAuxS), "v"(vOut2), "v"(vStW), "v"(vStR), "v"(vWd0), "v"(vWd1), "v"(vWr0), "v"(vWr1), "v"(vWr2), "s"(w3) : "v0", "v1", "v2", "v3", "v4", "v5", "v6", "v7", "v8", "v9", "v10", "v11", "v12", "v13", "v14", "v15", "v16", "v17", "v18", "v19", "v20", "v21", "v22", "v23", "v24", "v25", "v26", "v27", "v28", "v29", "v30", "v31", "v32", "v33", "v34", "v35", "v36", "v37", "v38", "v39", "v40", "v41", "v42", "v43", "v44", "v45", "v46", "v47", "v48", "v49", "v50", "v51", "v52", "v53", "v54", "v55", "v56", "v57", "v58", "v59", "v60", "v61", "v62", "v63", "v64", "v65", "v66", "v67", "v68", "v69", "v70", "v71", "v72", "v73", "v74", "v75", "v76", "v77", "v78", "v79", "v80", "v81", "v82", "v83", "v84", "v85", "v86", "v87", "v88", "v89", "v90", "v91", "v92", "v93", "v94", "v95", "v96", "v97", "v98", "v99", "v100", "v101", "v102", "v103", "v104", "v105", "v106", "v107", "v108", "v109", "v110", "v111", "v112", "v113", "v114", "v115", "v116", "v117", "v118", "v119", "v120", "v121", "v122", "v123", "v124", "v125", "v126", "v127", "v128", "v129", "v130", "v131", "v132", "v133", "v134", "v135", "v136", "v137", "v138", "v139", "v140", "v141", "v142", "v143", "v144", "v145", "v146", "v147", "v148", "v149", "v150", "v151", "v152", "v153", "v154", "v155", "v156", "v157", "v158", "v159", "v160", "v161", "v162", "v163", "v164", "v165", "v166", "v167", "v168", "v169", "v170", "v171", "v172", "v173", "v174", "v175", "v176", "v177", "v178", "v179", "v180", "v181", "v182", "v183", "v184", "v185", "v186", "v187", "v188", "v189", "v190", "v191", "v192", "v193", "v194", "v195", "v196", "v197", "v198", "v199", "v200", "v201", "v202", "v203", "v204", "v205", "v206", "v207", "v208", "v209", "v210", "v211", "v212", "v213", "v214", "v215", "v216", "v217", "v218", "v219", "v220", "v221", "v222", "v223", "v224", "v225", "v226", "v227", "v228", "v229", "v230", "v231", "v255", "a0", "a1", "a2", "a3", "a4", "a5", "a6", "a7", "a8", "a9", "a10", "a11", "a12", "a13", "a14", "a15", "a16", "a17", "a18", "a19", "a20", "a21", "a22", "a23", "a24", "a25", "a26", "a27", "a28", "a29", "a30", "a31", "a32", "a33", "a34", "a35", "a36", "a37", "a38", "a39", "a40", "a41", "a42", "a43", "a44", "a45", "a46", "a47", "a48", "a49", "a50", "a51", "a52", "a53", "a54", "a55", "a56", "a57", "a58", "a59", "a60", "a61", "a62", "a63", "a64", "a65", "a66", "a67", "a68", "a69", "a70", "a71", "a72", "a73", "a74", "a75", "a76", "a77", "a78", "a79", "a80", "a81", "a82", "a83", "a84", "a85", "a86", "a87", "a88", "a89", "a90", "a91", "a92", "a93", "a94", "a95", "a96", "a97", "a98", "a99", "a100", "a101", "a102", "a103", "a104", "a105", "a106", "a107", "a108", "a109", "a110", "a111", "a112", "a113", "a114", "a115", "a116", "a117", "a118", "a119", "a120", "a121", "a122", "a123", "a124", "a125", "a126", "a127", "a128", "a129", "a130", "a131", "a132", "a133", "a134", "a135", "a136", "a137", "a138", "a139", "a140", "a141", "a142", "a143", "a144", "a145", "a146", "a147", "a148", "a149", "a150", "a151", "a152", "a153", "a154", "a155", "a156", "a157", "a158", "a159", "a160", "a161", "a162", "a163", "a164", "a165", "a166", "a167", "a168", "a169", "a170", "a171", "a172", "a173", "a174", "a175", "a176", "a177", "a178", "a179", "a180", "a181", "a182", "a183", "a184", "a185", "a186", "a187", "a188", "a189", "a190", "a191", "a192", "a193", "a194", "a195", "a196", "a197", "a198", "a199", "a200", "a201", "a202", "a203", "a204", "a205", "a206", "a207", "a208", "a209", "a210", "a211", "a212", "a213", "a214", "a215", "a216", "a217", "a218", "a219", "a220", "a221", "a222", "a223", "a224", "a225", "a226", "a227", "a228", "a229", "a230", "a231", "a232", "a233", "a234", "a235", "a236", "a237", "a238", "a239", "a240", "a241", "a242", "a243", "a244", "a245", "a246", "a247", "a248", "a249", "a250", "a251", "a252", "a253", "a254", "a255", "s36", "s37", "s38", "s39", "s40", "s41", "s42", "s43", "s44", "s45", "s46", "s47", "s48", "s49", "s50", "s51", "s52", "s53", "s54", "s55", "s56", "s57", "s58", "s59", "s60", "s61", "s62", "s63", "s64", "s65", "s66", "s67", "s68", "s69", "s70", "s71", "s72", "s73", "s74", "s75", "s76", "s77", "s78", "s79", "s80", "s81", "s82", "s83", "s84", "s85", "s86", "s87", "s88", "s89", "s90", "s91", "s92", "s93", "s94", "s95", "s96", "s97", "s98", "s99", "vcc", "scc", "memory"); } else { asm volatile(R"(
Lcode_g3o_b:
s_mov_b64 s[36:37],%0
s_mov_b32 s38,122727552
s_mov_b32 s39,0x00020000
s_mov_b32 s54,%1
s_mov_b32 s55,%2
s_mov_b32 s56,%3
s_mov_b32 s58,%4
s_mov_b32 s83,%5
s_mov_b32 s82,%6
s_mov_b32 s84,%7
v_mov_b32 v0,%8
v_mov_b32 v2,%9
v_mov_b32 v3,%10
v_mov_b32 v4,%11
v_mov_b32 v6,%12
v_mov_b32 v7,%13
v_mov_b32 v8,%14
v_mov_b32 v9,%15
v_mov_b32 v10,%16
v_mov_b32 v11,%17
v_mov_b32 v24,%18
v_mov_b32 v25,%19
v_mov_b32 v26,%20
v_mov_b32 v27,%21
v_mov_b32 v28,%22
s_mov_b64 s[40:41],%23
s_mov_b32 s42,7340032
s_mov_b32 s43,0x00020000
s_lshl_b32 s88,s54,11
s_mov_b32 s69,122727424
buffer_load_dword v5,off,s[36:39],s69 sc1
s_add_u32 m0,s88,0
s_add_u32 s61,s82,0
buffer_load_dwordx4 v24,s[40:43],s61 offen offset:0  lds
s_add_u32 m0,s88,1024
s_nop 0
buffer_load_dwordx4 v25,s[40:43],s61 offen offset:0  lds
s_add_u32 m0,s88,8064
s_nop 0
buffer_load_dwordx4 v24,s[40:43],s61 offen offset:128  lds
s_add_u32 m0,s88,9088
s_nop 0
buffer_load_dwordx4 v25,s[40:43],s61 offen offset:128  lds
s_add_u32 m0,s88,16384
s_add_u32 s61,s82,458752
buffer_load_dwordx4 v24,s[40:43],s61 offen offset:0  lds
s_add_u32 m0,s88,17408
s_nop 0
buffer_load_dwordx4 v25,s[40:43],s61 offen offset:0  lds
s_add_u32 m0,s88,24448
s_nop 0
buffer_load_dwordx4 v24,s[40:43],s61 offen offset:128  lds
s_add_u32 m0,s88,25472
s_nop 0
buffer_load_dwordx4 v25,s[40:43],s61 offen offset:128  lds
s_add_u32 m0,s88,32768
s_add_u32 s61,s82,917504
buffer_load_dwordx4 v24,s[40:43],s61 offen offset:0  lds
s_add_u32 m0,s88,33792
s_nop 0
buffer_load_dwordx4 v25,s[40:43],s61 offen offset:0  lds
s_add_u32 m0,s88,40832
s_nop 0
buffer_load_dwordx4 v24,s[40:43],s61 offen offset:128  lds
s_add_u32 m0,s88,41856
s_nop 0
buffer_load_dwordx4 v25,s[40:43],s61 offen offset:128  lds
s_add_u32 m0,s88,49152
s_add_u32 s61,s82,1376256
buffer_load_dwordx4 v24,s[40:43],s61 offen offset:0  lds
s_add_u32 m0,s88,50176
s_nop 0
buffer_load_dwordx4 v25,s[40:43],s61 offen offset:0  lds
s_add_u32 m0,s88,57216
s_nop 0
buffer_load_dwordx4 v24,s[40:43],s61 offen offset:128  lds
s_add_u32 m0,s88,58240
s_nop 0
buffer_load_dwordx4 v25,s[40:43],s61 offen offset:128  lds
s_add_u32 m0,s88,65536
s_add_u32 s61,s82,1835008
buffer_load_dwordx4 v24,s[40:43],s61 offen offset:0  lds
s_add_u32 m0,s88,66560
s_nop 0
buffer_load_dwordx4 v25,s[40:43],s61 offen offset:0  lds
s_add_u32 m0,s88,73600
s_nop 0
buffer_load_dwordx4 v24,s[40:43],s61 offen offset:128  lds
s_add_u32 m0,s88,74624
s_nop 0
buffer_load_dwordx4 v25,s[40:43],s61 offen offset:128  lds
s_add_u32 m0,s88,81920
s_add_u32 s61,s82,2293760
buffer_load_dwordx4 v24,s[40:43],s61 offen offset:0  lds
s_add_u32 m0,s88,82944
s_nop 0
buffer_load_dwordx4 v25,s[40:43],s61 offen offset:0  lds
s_add_u32 m0,s88,89984
s_nop 0
buffer_load_dwordx4 v24,s[40:43],s61 offen offset:128  lds
s_add_u32 m0,s88,91008
s_nop 0
buffer_load_dwordx4 v25,s[40:43],s61 offen offset:128  lds
s_add_u32 m0,s88,98304
s_add_u32 s61,s82,2752512
buffer_load_dwordx4 v24,s[40:43],s61 offen offset:0  lds
s_add_u32 m0,s88,99328
s_nop 0
buffer_load_dwordx4 v25,s[40:43],s61 offen offset:0  lds
s_add_u32 m0,s88,106368
s_nop 0
buffer_load_dwordx4 v24,s[40:43],s61 offen offset:128  lds
s_add_u32 m0,s88,107392
s_nop 0
buffer_load_dwordx4 v25,s[40:43],s61 offen offset:128  lds
v_add_u32 v32,49152,v26
v_add_u32 v33,49152,v27
v_add_u32 v34,98304,v26
v_add_u32 v35,98304,v27
v_mov_b32 v72,0x3f803f80
v_mov_b32 v73,0x3f803f80
v_mov_b32 v74,0x3f803f80
v_mov_b32 v75,0x3f803f80
s_waitcnt vmcnt(24)
s_barrier
s_add_u32 m0,s88,114688
s_add_u32 s61,s82,3211264
buffer_load_dwordx4 v24,s[40:43],s61 offen offset:0  lds
s_add_u32 m0,s88,115712
ds_read_b64_tr_b16 a[0:1],v26 offset:0
buffer_load_dwordx4 v25,s[40:43],s61 offen offset:0  lds
s_add_u32 m0,s88,122752
ds_read_b64_tr_b16 a[2:3],v26 offset:512
buffer_load_dwordx4 v24,s[40:43],s61 offen offset:128  lds
s_add_u32 m0,s88,123776
ds_read_b64_tr_b16 a[4:5],v27 offset:0
buffer_load_dwordx4 v25,s[40:43],s61 offen offset:128  lds
ds_read_b64_tr_b16 a[6:7],v27 offset:512
ds_read_b64_tr_b16 a[8:9],v26 offset:4096
ds_read_b64_tr_b16 a[10:11],v26 offset:4608
ds_read_b64_tr_b16 a[12:13],v27 offset:4096
ds_read_b64_tr_b16 a[14:15],v27 offset:4608
s_waitcnt vmcnt(24)
s_barrier
s_add_u32 m0,s88,131072
s_add_u32 s61,s82,3670016
buffer_load_dwordx4 v24,s[40:43],s61 offen offset:0  lds
s_add_u32 m0,s88,132096
ds_read_b64_tr_b16 a[16:17],v26 offset:16384
buffer_load_dwordx4 v25,s[40:43],s61 offen offset:0  lds
s_add_u32 m0,s88,139136
ds_read_b64_tr_b16 a[18:19],v26 offset:16896
buffer_load_dwordx4 v24,s[40:43],s61 offen offset:128  lds
s_add_u32 m0,s88,140160
ds_read_b64_tr_b16 a[20:21],v27 offset:16384
buffer_load_dwordx4 v25,s[40:43],s61 offen offset:128  lds
ds_read_b64_tr_b16 a[22:23],v27 offset:16896
ds_read_b64_tr_b16 a[24:25],v26 offset:20480
ds_read_b64_tr_b16 a[26:27],v26 offset:20992
ds_read_b64_tr_b16 a[28:29],v27 offset:20480
ds_read_b64_tr_b16 a[30:31],v27 offset:20992
s_waitcnt lgkmcnt(8)
v_mfma_f32_16x16x32_bf16 v[16:19],a[0:3],v[72:75],0
v_mfma_f32_16x16x32_bf16 v[20:23],a[4:7],v[72:75],0
v_mfma_f32_16x16x32_bf16 v[16:19],a[8:11],v[72:75],v[16:19]
v_mfma_f32_16x16x32_bf16 v[20:23],a[12:15],v[72:75],v[20:23]
s_waitcnt vmcnt(24)
s_barrier
s_add_u32 m0,s88,0
s_add_u32 s61,s82,4128768
buffer_load_dwordx4 v24,s[40:43],s61 offen offset:0  lds
s_add_u32 m0,s88,1024
ds_read_b64_tr_b16 a[32:33],v26 offset:32768
buffer_load_dwordx4 v25,s[40:43],s61 offen offset:0  lds
s_add_u32 m0,s88,8064
ds_read_b64_tr_b16 a[34:35],v26 offset:33280
buffer_load_dwordx4 v24,s[40:43],s61 offen offset:128  lds
s_add_u32 m0,s88,9088
ds_read_b64_tr_b16 a[36:37],v27 offset:32768
buffer_load_dwordx4 v25,s[40:43],s61 offen offset:128  lds
ds_read_b64_tr_b16 a[38:39],v27 offset:33280
ds_read_b64_tr_b16 a[40:41],v26 offset:36864
ds_read_b64_tr_b16 a[42:43],v26 offset:37376
ds_read_b64_tr_b16 a[44:45],v27 offset:36864
ds_read_b64_tr_b16 a[46:47],v27 offset:37376
s_waitcnt lgkmcnt(8)
v_mfma_f32_16x16x32_bf16 v[16:19],a[16:19],v[72:75],v[16:19]
v_mfma_f32_16x16x32_bf16 v[20:23],a[20:23],v[72:75],v[20:23]
v_mfma_f32_16x16x32_bf16 v[16:19],a[24:27],v[72:75],v[16:19]
v_mfma_f32_16x16x32_bf16 v[20:23],a[28:31],v[72:75],v[20:23]
s_waitcnt vmcnt(24)
s_barrier
s_add_u32 m0,s88,16384
s_add_u32 s61,s82,4587520
buffer_load_dwordx4 v24,s[40:43],s61 offen offset:0  lds
s_add_u32 m0,s88,17408
ds_read_b64_tr_b16 a[48:49],v32 offset:0
buffer_load_dwordx4 v25,s[40:43],s61 offen offset:0  lds
s_add_u32 m0,s88,24448)"
R"(
ds_read_b64_tr_b16 a[50:51],v32 offset:512
buffer_load_dwordx4 v24,s[40:43],s61 offen offset:128  lds
s_add_u32 m0,s88,25472
ds_read_b64_tr_b16 a[52:53],v33 offset:0
buffer_load_dwordx4 v25,s[40:43],s61 offen offset:128  lds
ds_read_b64_tr_b16 a[54:55],v33 offset:512
ds_read_b64_tr_b16 a[56:57],v32 offset:4096
ds_read_b64_tr_b16 a[58:59],v32 offset:4608
ds_read_b64_tr_b16 a[60:61],v33 offset:4096
ds_read_b64_tr_b16 a[62:63],v33 offset:4608
s_waitcnt lgkmcnt(8)
v_mfma_f32_16x16x32_bf16 v[16:19],a[32:35],v[72:75],v[16:19]
v_mfma_f32_16x16x32_bf16 v[20:23],a[36:39],v[72:75],v[20:23]
v_mfma_f32_16x16x32_bf16 v[16:19],a[40:43],v[72:75],v[16:19]
v_mfma_f32_16x16x32_bf16 v[20:23],a[44:47],v[72:75],v[20:23]
s_waitcnt vmcnt(24)
s_barrier
s_add_u32 m0,s88,32768
s_add_u32 s61,s82,5046272
buffer_load_dwordx4 v24,s[40:43],s61 offen offset:0  lds
s_add_u32 m0,s88,33792
ds_read_b64_tr_b16 a[64:65],v32 offset:16384
buffer_load_dwordx4 v25,s[40:43],s61 offen offset:0  lds
s_add_u32 m0,s88,40832
ds_read_b64_tr_b16 a[66:67],v32 offset:16896
buffer_load_dwordx4 v24,s[40:43],s61 offen offset:128  lds
s_add_u32 m0,s88,41856
ds_read_b64_tr_b16 a[68:69],v33 offset:16384
buffer_load_dwordx4 v25,s[40:43],s61 offen offset:128  lds
ds_read_b64_tr_b16 a[70:71],v33 offset:16896
ds_read_b64_tr_b16 a[72:73],v32 offset:20480
ds_read_b64_tr_b16 a[74:75],v32 offset:20992
ds_read_b64_tr_b16 a[76:77],v33 offset:20480
ds_read_b64_tr_b16 a[78:79],v33 offset:20992
s_waitcnt lgkmcnt(8)
v_mfma_f32_16x16x32_bf16 v[16:19],a[48:51],v[72:75],v[16:19]
v_mfma_f32_16x16x32_bf16 v[20:23],a[52:55],v[72:75],v[20:23]
v_mfma_f32_16x16x32_bf16 v[16:19],a[56:59],v[72:75],v[16:19]
v_mfma_f32_16x16x32_bf16 v[20:23],a[60:63],v[72:75],v[20:23]
s_waitcnt vmcnt(24)
s_barrier
s_add_u32 m0,s88,49152
s_add_u32 s61,s82,5505024
buffer_load_dwordx4 v24,s[40:43],s61 offen offset:0  lds
s_add_u32 m0,s88,50176
ds_read_b64_tr_b16 a[80:81],v32 offset:32768
buffer_load_dwordx4 v25,s[40:43],s61 offen offset:0  lds
s_add_u32 m0,s88,57216
ds_read_b64_tr_b16 a[82:83],v32 offset:33280
buffer_load_dwordx4 v24,s[40:43],s61 offen offset:128  lds
s_add_u32 m0,s88,58240
ds_read_b64_tr_b16 a[84:85],v33 offset:32768
buffer_load_dwordx4 v25,s[40:43],s61 offen offset:128  lds
ds_read_b64_tr_b16 a[86:87],v33 offset:33280
ds_read_b64_tr_b16 a[88:89],v32 offset:36864
ds_read_b64_tr_b16 a[90:91],v32 offset:37376
ds_read_b64_tr_b16 a[92:93],v33 offset:36864
ds_read_b64_tr_b16 a[94:95],v33 offset:37376
s_waitcnt lgkmcnt(8)
v_mfma_f32_16x16x32_bf16 v[16:19],a[64:67],v[72:75],v[16:19]
v_mfma_f32_16x16x32_bf16 v[20:23],a[68:71],v[72:75],v[20:23]
v_mfma_f32_16x16x32_bf16 v[16:19],a[72:75],v[72:75],v[16:19]
v_mfma_f32_16x16x32_bf16 v[20:23],a[76:79],v[72:75],v[20:23]
s_waitcnt vmcnt(24)
s_barrier
s_add_u32 m0,s88,65536
s_add_u32 s61,s82,5963776
buffer_load_dwordx4 v24,s[40:43],s61 offen offset:0  lds
s_add_u32 m0,s88,66560
ds_read_b64_tr_b16 a[96:97],v34 offset:0
buffer_load_dwordx4 v25,s[40:43],s61 offen offset:0  lds
s_add_u32 m0,s88,73600
ds_read_b64_tr_b16 a[98:99],v34 offset:512
buffer_load_dwordx4 v24,s[40:43],s61 offen offset:128  lds
s_add_u32 m0,s88,74624
ds_read_b64_tr_b16 a[100:101],v35 offset:0
buffer_load_dwordx4 v25,s[40:43],s61 offen offset:128  lds
ds_read_b64_tr_b16 a[102:103],v35 offset:512
ds_read_b64_tr_b16 a[104:105],v34 offset:4096
ds_read_b64_tr_b16 a[106:107],v34 offset:4608
ds_read_b64_tr_b16 a[108:109],v35 offset:4096
ds_read_b64_tr_b16 a[110:111],v35 offset:4608
s_waitcnt lgkmcnt(8)
v_mfma_f32_16x16x32_bf16 v[16:19],a[80:83],v[72:75],v[16:19]
v_mfma_f32_16x16x32_bf16 v[20:23],a[84:87],v[72:75],v[20:23]
v_mfma_f32_16x16x32_bf16 v[16:19],a[88:91],v[72:75],v[16:19]
v_mfma_f32_16x16x32_bf16 v[20:23],a[92:95],v[72:75],v[20:23]
s_waitcnt vmcnt(24)
s_barrier
s_add_u32 m0,s88,81920
s_add_u32 s61,s82,6422528
buffer_load_dwordx4 v24,s[40:43],s61 offen offset:0  lds
s_add_u32 m0,s88,82944
ds_read_b64_tr_b16 a[112:113],v34 offset:16384
buffer_load_dwordx4 v25,s[40:43],s61 offen offset:0  lds
s_add_u32 m0,s88,89984
ds_read_b64_tr_b16 a[114:115],v34 offset:16896
buffer_load_dwordx4 v24,s[40:43],s61 offen offset:128  lds
s_add_u32 m0,s88,91008
ds_read_b64_tr_b16 a[116:117],v35 offset:16384
buffer_load_dwordx4 v25,s[40:43],s61 offen offset:128  lds
ds_read_b64_tr_b16 a[118:119],v35 offset:16896
ds_read_b64_tr_b16 a[120:121],v34 offset:20480
ds_read_b64_tr_b16 a[122:123],v34 offset:20992
ds_read_b64_tr_b16 a[124:125],v35 offset:20480
ds_read_b64_tr_b16 a[126:127],v35 offset:20992
s_waitcnt lgkmcnt(8)
v_mfma_f32_16x16x32_bf16 v[16:19],a[96:99],v[72:75],v[16:19]
v_mfma_f32_16x16x32_bf16 v[20:23],a[100:103],v[72:75],v[20:23]
v_mfma_f32_16x16x32_bf16 v[16:19],a[104:107],v[72:75],v[16:19]
v_mfma_f32_16x16x32_bf16 v[20:23],a[108:111],v[72:75],v[20:23]
s_waitcnt vmcnt(24)
s_barrier
s_add_u32 m0,s88,98304
s_add_u32 s61,s82,6881280
buffer_load_dwordx4 v24,s[40:43],s61 offen offset:0  lds
s_add_u32 m0,s88,99328
ds_read_b64_tr_b16 a[128:129],v34 offset:32768
buffer_load_dwordx4 v25,s[40:43],s61 offen offset:0  lds
s_add_u32 m0,s88,106368
ds_read_b64_tr_b16 a[130:131],v34 offset:33280
buffer_load_dwordx4 v24,s[40:43],s61 offen offset:128  lds
s_add_u32 m0,s88,107392
ds_read_b64_tr_b16 a[132:133],v35 offset:32768
buffer_load_dwordx4 v25,s[40:43],s61 offen offset:128  lds
ds_read_b64_tr_b16 a[134:135],v35 offset:33280
ds_read_b64_tr_b16 a[136:137],v34 offset:36864
ds_read_b64_tr_b16 a[138:139],v34 offset:37376
ds_read_b64_tr_b16 a[140:141],v35 offset:36864
ds_read_b64_tr_b16 a[142:143],v35 offset:37376
s_waitcnt lgkmcnt(8)
v_mfma_f32_16x16x32_bf16 v[16:19],a[112:115],v[72:75],v[16:19]
v_mfma_f32_16x16x32_bf16 v[20:23],a[116:119],v[72:75],v[20:23]
v_mfma_f32_16x16x32_bf16 v[16:19],a[120:123],v[72:75],v[16:19]
v_mfma_f32_16x16x32_bf16 v[20:23],a[124:127],v[72:75],v[20:23]
s_waitcnt vmcnt(24)
s_barrier
ds_read_b64_tr_b16 a[144:145],v26 offset:0
ds_read_b64_tr_b16 a[146:147],v26 offset:512
ds_read_b64_tr_b16 a[148:149],v27 offset:0
ds_read_b64_tr_b16 a[150:151],v27 offset:512
ds_read_b64_tr_b16 a[152:153],v26 offset:4096
ds_read_b64_tr_b16 a[154:155],v26 offset:4608
ds_read_b64_tr_b16 a[156:157],v27 offset:4096
ds_read_b64_tr_b16 a[158:159],v27 offset:4608
s_waitcnt lgkmcnt(8)
v_mfma_f32_16x16x32_bf16 v[16:19],a[128:131],v[72:75],v[16:19]
v_mfma_f32_16x16x32_bf16 v[20:23],a[132:135],v[72:75],v[20:23]
v_mfma_f32_16x16x32_bf16 v[16:19],a[136:139],v[72:75],v[16:19]
v_mfma_f32_16x16x32_bf16 v[20:23],a[140:143],v[72:75],v[20:23]
s_waitcnt vmcnt(20)
s_barrier
ds_read_b64_tr_b16 a[160:161],v26 offset:16384
ds_read_b64_tr_b16 a[162:163],v26 offset:16896
ds_read_b64_tr_b16 a[164:165],v27 offset:16384
ds_read_b64_tr_b16 a[166:167],v27 offset:16896
ds_read_b64_tr_b16 a[168:169],v26 offset:20480
ds_read_b64_tr_b16 a[170:171],v26 offset:20992
ds_read_b64_tr_b16 a[172:173],v27 offset:20480
ds_read_b64_tr_b16 a[174:175],v27 offset:20992
s_waitcnt lgkmcnt(8)
v_mfma_f32_16x16x32_bf16 v[16:19],a[144:147],v[72:75],v[16:19]
v_mfma_f32_16x16x32_bf16 v[20:23],a[148:151],v[72:75],v[20:23]
v_mfma_f32_16x16x32_bf16 v[16:19],a[152:155],v[72:75],v[16:19]
v_mfma_f32_16x16x32_bf16 v[20:23],a[156:159],v[72:75],v[20:23]
s_waitcnt vmcnt(16)
s_barrier
ds_read_b64_tr_b16 a[176:177],v26 offset:32768
ds_read_b64_tr_b16 a[178:179],v26 offset:33280
ds_read_b64_tr_b16 a[180:181],v27 offset:32768
ds_read_b64_tr_b16 a[182:183],v27 offset:33280
ds_read_b64_tr_b16 a[184:185],v26 offset:36864
ds_read_b64_tr_b16 a[186:187],v26 offset:37376
ds_read_b64_tr_b16 a[188:189],v27 offset:36864
ds_read_b64_tr_b16 a[190:191],v27 offset:37376
s_getpc_b64 s[46:47]
Lg3o_cp_%=:
s_add_u32 s46,s46,Lg3o_init_%=-Lg3o_cp_%=
s_addc_u32 s47,s47,0
s_lshl_b32 s68,s54,13
v_lshl_add_u32 v56,v2,3,s68
v_min_u32 v57,Lg3o_done_%=-Lg3o_init_%=,v56
global_load_dword v60,v57,s[46:47]
s_waitcnt lgkmcnt(8)
v_mfma_f32_16x16x32_bf16 v[16:19],a[160:163],v[72:75],v[16:19]
v_mfma_f32_16x16x32_bf16 v[20:23],a[164:167],v[72:75],v[20:23]
v_mfma_f32_16x16x32_bf16 v[16:19],a[168:171],v[72:75],v[16:19]
v_mfma_f32_16x16x32_bf16 v[20:23],a[172:175],v[72:75],v[20:23]
s_waitcnt vmcnt(13)
s_barrier
ds_read_b64_tr_b16 a[192:193],v32 offset:0
ds_read_b64_tr_b16 a[194:195],v32 offset:512
ds_read_b64_tr_b16 a[196:197],v33 offset:0
ds_read_b64_tr_b16 a[198:199],v33 offset:512
ds_read_b64_tr_b16 a[200:201],v32 offset:4096
ds_read_b64_tr_b16 a[202:203],v32 offset:4608
ds_read_b64_tr_b16 a[204:205],v33 offset:4096
ds_read_b64_tr_b16 a[206:207],v33 offset:4608
s_waitcnt lgkmcnt(8)
v_mfma_f32_16x16x32_bf16 v[16:19],a[176:179],v[72:75],v[16:19])"
R"(
v_mfma_f32_16x16x32_bf16 v[20:23],a[180:183],v[72:75],v[20:23]
v_mfma_f32_16x16x32_bf16 v[16:19],a[184:187],v[72:75],v[16:19]
v_mfma_f32_16x16x32_bf16 v[20:23],a[188:191],v[72:75],v[20:23]
s_waitcnt vmcnt(9)
s_barrier
ds_read_b64_tr_b16 a[208:209],v32 offset:16384
ds_read_b64_tr_b16 a[210:211],v32 offset:16896
ds_read_b64_tr_b16 a[212:213],v33 offset:16384
ds_read_b64_tr_b16 a[214:215],v33 offset:16896
ds_read_b64_tr_b16 a[216:217],v32 offset:20480
ds_read_b64_tr_b16 a[218:219],v32 offset:20992
ds_read_b64_tr_b16 a[220:221],v33 offset:20480
ds_read_b64_tr_b16 a[222:223],v33 offset:20992
s_waitcnt lgkmcnt(8)
v_mfma_f32_16x16x32_bf16 v[16:19],a[192:195],v[72:75],v[16:19]
v_mfma_f32_16x16x32_bf16 v[20:23],a[196:199],v[72:75],v[20:23]
v_mfma_f32_16x16x32_bf16 v[16:19],a[200:203],v[72:75],v[16:19]
v_mfma_f32_16x16x32_bf16 v[20:23],a[204:207],v[72:75],v[20:23]
s_waitcnt vmcnt(5)
s_barrier
ds_read_b64_tr_b16 a[224:225],v32 offset:32768
ds_read_b64_tr_b16 a[226:227],v32 offset:33280
ds_read_b64_tr_b16 a[228:229],v33 offset:32768
ds_read_b64_tr_b16 a[230:231],v33 offset:33280
ds_read_b64_tr_b16 a[232:233],v32 offset:36864
ds_read_b64_tr_b16 a[234:235],v32 offset:37376
ds_read_b64_tr_b16 a[236:237],v33 offset:36864
ds_read_b64_tr_b16 a[238:239],v33 offset:37376
s_waitcnt lgkmcnt(8)
v_mfma_f32_16x16x32_bf16 v[16:19],a[208:211],v[72:75],v[16:19]
v_mfma_f32_16x16x32_bf16 v[20:23],a[212:215],v[72:75],v[20:23]
v_mfma_f32_16x16x32_bf16 v[16:19],a[216:219],v[72:75],v[16:19]
v_mfma_f32_16x16x32_bf16 v[20:23],a[220:223],v[72:75],v[20:23]
s_waitcnt vmcnt(1)
s_barrier
ds_read_b64_tr_b16 a[240:241],v34 offset:0
ds_read_b64_tr_b16 a[242:243],v34 offset:512
ds_read_b64_tr_b16 a[244:245],v35 offset:0
ds_read_b64_tr_b16 a[246:247],v35 offset:512
ds_read_b64_tr_b16 a[248:249],v34 offset:4096
ds_read_b64_tr_b16 a[250:251],v34 offset:4608
ds_read_b64_tr_b16 a[252:253],v35 offset:4096
ds_read_b64_tr_b16 a[254:255],v35 offset:4608
s_waitcnt lgkmcnt(8)
v_mfma_f32_16x16x32_bf16 v[16:19],a[224:227],v[72:75],v[16:19]
v_mfma_f32_16x16x32_bf16 v[20:23],a[228:231],v[72:75],v[20:23]
v_mfma_f32_16x16x32_bf16 v[16:19],a[232:235],v[72:75],v[16:19]
v_mfma_f32_16x16x32_bf16 v[20:23],a[236:239],v[72:75],v[20:23]
s_waitcnt lgkmcnt(0)
v_mfma_f32_16x16x32_bf16 v[16:19],a[240:243],v[72:75],v[16:19]
v_mfma_f32_16x16x32_bf16 v[20:23],a[244:247],v[72:75],v[20:23]
v_mfma_f32_16x16x32_bf16 v[16:19],a[248:251],v[72:75],v[16:19]
v_mfma_f32_16x16x32_bf16 v[20:23],a[252:255],v[72:75],v[20:23]
s_waitcnt lgkmcnt(0)
s_barrier
v_readfirstlane_b32 s60,v5
s_nop 3
s_add_u32 s60,s60,1
s_cmp_eq_u32 s60,0
s_cselect_b32 s60,1,s60
Lg3o_init_%=:
v_mov_b32 v5,s60
s_mov_b32 s59,536870912
s_add_u32 s57,s56,131072
s_mov_b32 s79,0
s_mov_b32 s75,0x3a800000
s_mov_b32 s76,0xc0135761
s_mov_b32 s77,0x3d372713
s_mov_b32 s44,s59
s_mov_b32 s45,s59
s_mov_b32 s46,s59
s_mov_b32 s47,s59
s_mov_b32 s48,s83
s_add_u32 s49,s48,s55
s_cmp_eq_u32 s49,s56
s_cselect_b32 s49,s57,s49
s_cmp_eq_u32 s49,s58
s_cselect_b32 s49,s59,s49
s_add_u32 s50,s49,s55
s_cmp_eq_u32 s50,s56
s_cselect_b32 s50,s57,s50
s_cmp_eq_u32 s50,s58
s_cselect_b32 s50,s59,s50
s_add_u32 s51,s50,s55
s_cmp_eq_u32 s51,s56
s_cselect_b32 s51,s57,s51
s_cmp_eq_u32 s51,s58
s_cselect_b32 s51,s59,s51
s_add_u32 s52,s51,s55
s_cmp_eq_u32 s52,s56
s_cselect_b32 s52,s57,s52
s_cmp_eq_u32 s52,s58
s_cselect_b32 s52,s59,s52
s_lshl_b32 s63,s54,12
s_add_u32 s63,s63,65536
s_add_u32 s64,s63,32768
s_lshl_b32 s88,s54,10
s_add_u32 s88,s88,131072
s_lshl_b32 s78,s54,11
s_add_u32 s78,s78,132608
v_add_u32 v1,16384,v0
v_mov_b32 v12,0x3727c5ac
s_getpc_b64 s[90:91]
Lg3o_pc_%=:
s_mov_b32 s68,Lg3o_d1w0_%=-Lg3o_pc_%=
s_cmp_eq_u32 s54,1
s_cselect_b32 s68,Lg3o_d1w1_%=-Lg3o_pc_%=,s68
s_cmp_eq_u32 s54,2
s_cselect_b32 s68,Lg3o_d1w2_%=-Lg3o_pc_%=,s68
s_cmp_eq_u32 s54,3
s_cselect_b32 s68,Lg3o_d1w3_%=-Lg3o_pc_%=,s68
s_add_u32 s92,s90,s68
s_addc_u32 s93,s91,0
s_mov_b32 s68,Lg3o_d0w0_%=-Lg3o_pc_%=
s_cmp_eq_u32 s54,1
s_cselect_b32 s68,Lg3o_d0w1_%=-Lg3o_pc_%=,s68
s_cmp_eq_u32 s54,2
s_cselect_b32 s68,Lg3o_d0w2_%=-Lg3o_pc_%=,s68
s_cmp_eq_u32 s54,3
s_cselect_b32 s68,Lg3o_d0w3_%=-Lg3o_pc_%=,s68
s_add_u32 s90,s90,s68
s_addc_u32 s91,s91,0
s_mov_b32 s66,s59
s_mov_b32 s65,0
s_mov_b32 s62,s63
s_branch Lg3o_slow0_%=
Lg3o_d0w0_%=:
v_mfma_f32_16x16x32_bf16 v[24:27],a[0:3],v[40:43],0
s_add_u32 m0,s62,16384
ds_read_b128 v[68:71],v2 offset:7168
v_mul_f32 v96,s75,v92
v_mfma_f32_16x16x32_bf16 v[28:31],a[4:7],v[40:43],0
buffer_load_dwordx4 v1,s[36:39],s66 offen offset:0 sc1 lds
v_mfma_f32_16x16x32_bf16 v[24:27],a[8:11],v[44:47],v[24:27]
s_mul_i32 s73,s47,7
ds_read_b128 v[40:43],v2 offset:8192
v_fma_f32 v93,v93,s75,v12
v_mfma_f32_16x16x32_bf16 v[28:31],a[12:15],v[44:47],v[28:31]
s_lshr_b32 s73,s73,1
v_fma_f32 v93,-v96,v96,v93
v_max_f32 v93,v93,v12
v_mfma_f32_16x16x32_bf16 v[24:27],a[16:19],v[48:51],v[24:27]
s_lshr_b32 s72,s66,5
ds_read_b128 v[44:47],v2 offset:9216
v_fma_f32 v32,-v96,v16,v32
v_mfma_f32_16x16x32_bf16 v[28:31],a[20:23],v[48:51],v[28:31]
buffer_load_dwordx4 v1,s[36:39],s66 offen offset:1024 sc1 lds
v_mfma_f32_16x16x32_bf16 v[24:27],a[24:27],v[52:55],v[24:27]
s_lshr_b32 s71,s62,2
ds_read_b128 v[48:51],v2 offset:10240
v_fma_f32 v33,-v96,v17,v33
v_mfma_f32_16x16x32_bf16 v[28:31],a[28:31],v[52:55],v[28:31]
s_waitcnt lgkmcnt(3)
v_rsq_f32 v92,v93
v_mfma_f32_16x16x32_bf16 v[24:27],a[32:35],v[56:59],v[24:27]
ds_read_b128 v[52:55],v2 offset:11264
v_mul_f32 v97,s76,v92
v_mfma_f32_16x16x32_bf16 v[28:31],a[36:39],v[56:59],v[28:31]
buffer_load_dwordx4 v1,s[36:39],s66 offen offset:2048 sc1 lds
v_mfma_f32_16x16x32_bf16 v[24:27],a[40:43],v[60:63],v[24:27]
ds_read_b128 v[56:59],v2 offset:12288
v_mul_f32 v98,v92,v92
v_mfma_f32_16x16x32_bf16 v[28:31],a[44:47],v[60:63],v[28:31]
v_mul_f32 v99,v93,v92
v_mul_f32 v98,s77,v98
v_mfma_f32_16x16x32_bf16 v[24:27],a[48:51],v[64:67],v[24:27]
ds_read_b128 v[60:63],v2 offset:13312
v_mul_f32 v98,v98,v97
v_mfma_f32_16x16x32_bf16 v[28:31],a[52:55],v[64:67],v[28:31]
buffer_load_dwordx4 v1,s[36:39],s66 offen offset:3072 sc1 lds
v_mfma_f32_16x16x32_bf16 v[24:27],a[56:59],v[68:71],v[24:27]
s_add_u32 m0,s71,s88
ds_read_b128 v[64:67],v2 offset:14336
v_fma_f32 v34,-v96,v18,v34
v_mfma_f32_16x16x32_bf16 v[28:31],a[60:63],v[68:71],v[28:31]
s_waitcnt lgkmcnt(3)
buffer_load_dword v9,s[36:39],s72 offen offset:0 sc1 lds
v_mfma_f32_16x16x32_bf16 v[24:27],a[64:67],v[40:43],v[24:27]
s_waitcnt vmcnt(11)
ds_read_b128 v[68:71],v2 offset:15360
v_fma_f32 v35,-v96,v19,v35
v_mfma_f32_16x16x32_bf16 v[28:31],a[68:71],v[40:43],v[28:31]
s_barrier
v_mfma_f32_16x16x32_bf16 v[24:27],a[72:75],v[44:47],v[24:27]
s_xor_b32 s87,s64,65536
ds_read_b128 v[40:43],v2 offset:16384
ds_read_b32 v88,v8 offset:0
v_mfma_f32_16x16x32_bf16 v[28:31],a[76:79],v[44:47],v[28:31]
s_cmp_ge_u32 s66,s59
v_mul_f32 v72,v32,v32
v_mul_f32 v73,v33,v33
v_mfma_f32_16x16x32_bf16 v[24:27],a[80:83],v[48:51],v[24:27]
s_cselect_b32 s86,s49,s50
ds_read_b128 v[44:47],v2 offset:17408
v_mul_f32 v74,v34,v34
v_mfma_f32_16x16x32_bf16 v[28:31],a[84:87],v[48:51],v[28:31]
s_cselect_b32 s87,s87,s63
v_mul_f32 v75,v35,v35
v_fma_f32 v72,v98,v72,v97
v_mfma_f32_16x16x32_bf16 v[24:27],a[88:91],v[52:55],v[24:27])"
R"(
s_lshr_b32 s68,s50,5
ds_read_b128 v[48:51],v2 offset:18432
v_mul_f32 v72,v32,v72
v_mfma_f32_16x16x32_bf16 v[28:31],a[92:95],v[52:55],v[28:31]
s_waitcnt lgkmcnt(4)
v_exp_f32 v72,v72
v_mfma_f32_16x16x32_bf16 v[24:27],a[96:99],v[56:59],v[24:27]
s_waitcnt vmcnt(10) lgkmcnt(2)
ds_read_b128 v[52:55],v2 offset:19456
v_fma_f32 v72,v72,v99,v99
v_mfma_f32_16x16x32_bf16 v[28:31],a[100:103],v[56:59],v[28:31]
s_barrier
v_mfma_f32_16x16x32_bf16 v[24:27],a[104:107],v[60:63],v[24:27]
s_lshr_b32 s70,s51,5
ds_read_b128 v[56:59],v2 offset:20480
ds_read_b32 v13,v7 offset:0
v_mfma_f32_16x16x32_bf16 v[28:31],a[108:111],v[60:63],v[28:31]
s_mov_b32 s62,s87
v_rcp_f32 v72,v72
v_mfma_f32_16x16x32_bf16 v[24:27],a[112:115],v[64:67],v[24:27]
ds_read_b128 v[60:63],v2 offset:21504
v_add_f32_dpp v88,v88,v88 quad_perm:[1,0,3,2] row_mask:0xf bank_mask:0xf
v_mfma_f32_16x16x32_bf16 v[28:31],a[116:119],v[64:67],v[28:31]
s_nop 0
v_add_f32_dpp v88,v88,v88 quad_perm:[2,3,0,1] row_mask:0xf bank_mask:0xf
v_fma_f32 v73,v98,v73,v97
v_mfma_f32_16x16x32_bf16 v[24:27],a[120:123],v[68:71],v[24:27]
s_waitcnt lgkmcnt(1)
ds_read_b128 v[64:67],v2 offset:22528
v_mul_f32 v73,v33,v73
v_mfma_f32_16x16x32_bf16 v[28:31],a[124:127],v[68:71],v[28:31]
v_cmp_eq_u32 vcc,s60,v13
v_exp_f32 v73,v73
v_add_f32_dpp v88,v88,v88 row_half_mirror row_mask:0xf bank_mask:0xf
v_mfma_f32_16x16x32_bf16 v[24:27],a[128:131],v[40:43],v[24:27]
s_cmp_eq_u32 vcc_lo,-1
ds_write_b32 v10,v88 offset:0
v_mfma_f32_16x16x32_bf16 v[28:31],a[132:135],v[40:43],v[28:31]
s_cselect_b32 s67,s86,s59
s_cmp_ge_u32 s67,s59
s_cselect_b32 s68,s68,s70
ds_read_b128 v[68:71],v2 offset:23552
s_add_u32 m0,s78,256
v_mfma_f32_16x16x32_bf16 v[24:27],a[136:139],v[44:47],v[24:27]
buffer_load_dword v6,s[36:39],s68 offen offset:0 sc1 lds
v_mfma_f32_16x16x32_bf16 v[28:31],a[140:143],v[44:47],v[28:31]
s_mov_b32 m0,s62
ds_read_b128 v[40:43],v2 offset:24576
v_fma_f32 v73,v73,v99,v99
v_mfma_f32_16x16x32_bf16 v[24:27],a[144:147],v[48:51],v[24:27]
buffer_load_dwordx4 v0,s[36:39],s67 offen offset:0 sc1 lds
v_mfma_f32_16x16x32_bf16 v[28:31],a[148:151],v[48:51],v[28:31]
s_xor_b32 s63,s63,65536
ds_read_b128 v[44:47],v2 offset:25600
v_fma_f32 v74,v98,v74,v97
v_mfma_f32_16x16x32_bf16 v[24:27],a[152:155],v[52:55],v[24:27]
s_add_u32 s53,s52,s55
ds_read_b128 v[48:51],v2 offset:26624
v_mul_f32 v74,v34,v74
v_mfma_f32_16x16x32_bf16 v[28:31],a[156:159],v[52:55],v[28:31]
s_waitcnt lgkmcnt(3)
v_exp_f32 v74,v74
v_mfma_f32_16x16x32_bf16 v[24:27],a[160:163],v[56:59],v[24:27]
buffer_load_dwordx4 v0,s[36:39],s67 offen offset:1024 sc1 lds
v_mfma_f32_16x16x32_bf16 v[28:31],a[164:167],v[56:59],v[28:31]
s_cmp_eq_u32 s53,s56
ds_read_b128 v[52:55],v2 offset:27648
v_fma_f32 v74,v74,v99,v99
v_mfma_f32_16x16x32_bf16 v[24:27],a[168:171],v[60:63],v[24:27]
s_cselect_b32 s53,s57,s53
ds_read_b128 v[56:59],v2 offset:28672
v_fma_f32 v75,v98,v75,v97
v_mfma_f32_16x16x32_bf16 v[28:31],a[172:175],v[60:63],v[28:31]
s_cmp_eq_u32 s53,s58
v_rcp_f32 v73,v73
v_mfma_f32_16x16x32_bf16 v[24:27],a[176:179],v[64:67],v[24:27]
buffer_load_dwordx4 v0,s[36:39],s67 offen offset:2048 sc1 lds
v_mfma_f32_16x16x32_bf16 v[28:31],a[180:183],v[64:67],v[28:31]
s_cselect_b32 s53,s59,s53
ds_read_b128 v[60:63],v2 offset:29696
v_mul_f32 v75,v35,v75
v_mfma_f32_16x16x32_bf16 v[24:27],a[184:187],v[68:71],v[24:27]
s_cmp_ge_u32 s66,s59
ds_read_b128 v[64:67],v2 offset:30720
v_mul_f32 v32,v32,v72
v_mfma_f32_16x16x32_bf16 v[28:31],a[188:191],v[68:71],v[28:31]
s_waitcnt lgkmcnt(3)
v_exp_f32 v75,v75
v_mfma_f32_16x16x32_bf16 v[24:27],a[192:195],v[40:43],v[24:27]
s_waitcnt vmcnt(9)
ds_read_b128 v[68:71],v2 offset:31744
v_fma_f32 v75,v75,v99,v99
v_mfma_f32_16x16x32_bf16 v[28:31],a[196:199],v[40:43],v[28:31]
s_barrier
v_fma_f32 v36,-v96,v20,v36
v_fma_f32 v37,-v96,v21,v37
v_fma_f32 v38,-v96,v22,v38
v_fma_f32 v39,-v96,v23,v39
v_mul_f32 v76,v36,v36
v_mul_f32 v77,v37,v37
v_mul_f32 v78,v38,v38
v_mul_f32 v79,v39,v39
v_fma_f32 v76,v98,v76,v97
v_fma_f32 v77,v98,v77,v97
v_fma_f32 v78,v98,v78,v97
v_fma_f32 v79,v98,v79,v97
v_mul_f32 v76,v36,v76
v_mul_f32 v77,v37,v77
v_mul_f32 v78,v38,v78
v_mul_f32 v79,v39,v79
v_exp_f32 v76,v76
v_exp_f32 v77,v77
v_exp_f32 v78,v78
v_exp_f32 v79,v79
v_mfma_f32_16x16x32_bf16 v[24:27],a[200:203],v[44:47],v[24:27]
buffer_load_dwordx4 v0,s[36:39],s67 offen offset:3072 sc1 lds
v_rcp_f32 v74,v74
v_rcp_f32 v75,v75
v_fma_f32 v76,v76,v99,v99
v_fma_f32 v77,v77,v99,v99
v_fma_f32 v78,v78,v99,v99
v_fma_f32 v79,v79,v99,v99
v_rcp_f32 v76,v76
v_rcp_f32 v77,v77
v_rcp_f32 v78,v78
v_rcp_f32 v79,v79
v_mfma_f32_16x16x32_bf16 v[28:31],a[204:207],v[44:47],v[28:31]
ds_read_b128 v[40:43],v2 offset:32768
v_mul_f32 v33,v33,v73
v_mul_f32 v34,v34,v74
v_mul_f32 v35,v35,v75
v_cvt_pk_bf16_f32 v80,v32,v33
v_cvt_pk_bf16_f32 v81,v34,v35
v_mul_f32 v36,v36,v76
v_mul_f32 v37,v37,v77
v_mul_f32 v38,v38,v78
v_mul_f32 v39,v39,v79
v_cvt_pk_bf16_f32 v82,v36,v37
v_cvt_pk_bf16_f32 v83,v38,v39
v_mfma_f32_16x16x32_bf16 v[24:27],a[208:211],v[48:51],v[24:27]
buffer_store_dwordx4 v[80:83],v3,s[36:39],s73 offen offset:0 sc0 sc1
ds_read_b128 v[44:47],v2 offset:33792
ds_read_b64 v[92:93],v11 offset:0
v_mfma_f32_16x16x32_bf16 v[28:31],a[212:215],v[48:51],v[28:31]
v_mfma_f32_16x16x32_bf16 v[24:27],a[216:219],v[52:55],v[24:27]
ds_read_b128 v[48:51],v2 offset:34816
v_mfma_f32_16x16x32_bf16 v[28:31],a[220:223],v[52:55],v[28:31]
s_waitcnt lgkmcnt(4)
v_mfma_f32_16x16x32_bf16 v[24:27],a[224:227],v[56:59],v[24:27]
s_waitcnt lgkmcnt(1)
ds_read_b128 v[52:55],v2 offset:35840
v_mfma_f32_16x16x32_bf16 v[28:31],a[228:231],v[56:59],v[28:31]
v_mfma_f32_16x16x32_bf16 v[24:27],a[232:235],v[60:63],v[24:27]
ds_read_b128 v[56:59],v2 offset:36864
v_mfma_f32_16x16x32_bf16 v[28:31],a[236:239],v[60:63],v[28:31]
v_mfma_f32_16x16x32_bf16 v[24:27],a[240:243],v[64:67],v[24:27]
ds_read_b128 v[60:63],v2 offset:37888
v_mfma_f32_16x16x32_bf16 v[28:31],a[244:247],v[64:67],v[28:31]
v_mfma_f32_16x16x32_bf16 v[24:27],a[248:251],v[68:71],v[24:27]
s_waitcnt lgkmcnt(2)
ds_read_b128 v[64:67],v2 offset:38912
v_mfma_f32_16x16x32_bf16 v[28:31],a[252:255],v[68:71],v[28:31]
s_cbranch_scc1 Lg3o_slow1_%=
Lg3o_d1w0_%=:
v_mfma_f32_16x16x32_bf16 v[32:35],a[0:3],v[40:43],0
s_add_u32 m0,s62,16384
ds_read_b128 v[68:71],v2 offset:39936
v_mul_f32 v96,s75,v92
v_mfma_f32_16x16x32_bf16 v[36:39],a[4:7],v[40:43],0
buffer_load_dwordx4 v1,s[36:39],s67 offen offset:0 sc1 lds
v_mfma_f32_16x16x32_bf16 v[32:35],a[8:11],v[44:47],v[32:35]
s_mul_i32 s73,s48,7
ds_read_b128 v[40:43],v2 offset:40960
v_fma_f32 v93,v93,s75,v12
v_mfma_f32_16x16x32_bf16 v[36:39],a[12:15],v[44:47],v[36:39]
s_lshr_b32 s73,s73,1
v_fma_f32 v93,-v96,v96,v93
v_max_f32 v93,v93,v12
v_mfma_f32_16x16x32_bf16 v[32:35],a[16:19],v[48:51],v[32:35]
s_lshr_b32 s72,s67,5
ds_read_b128 v[44:47],v2 offset:41984
v_fma_f32 v24,-v96,v16,v24
v_mfma_f32_16x16x32_bf16 v[36:39],a[20:23],v[48:51],v[36:39]
buffer_load_dwordx4 v1,s[36:39],s67 offen offset:1024 sc1 lds
v_mfma_f32_16x16x32_bf16 v[32:35],a[24:27],v[52:55],v[32:35]
s_lshr_b32 s71,s62,2
ds_read_b128 v[48:51],v2 offset:43008
v_fma_f32 v25,-v96,v17,v25
v_mfma_f32_16x16x32_bf16 v[36:39],a[28:31],v[52:55],v[36:39]
s_waitcnt lgkmcnt(3)
v_rsq_f32 v92,v93
v_mfma_f32_16x16x32_bf16 v[32:35],a[32:35],v[56:59],v[32:35]
s_mov_b64 s[44:45],s[46:47]
ds_read_b128 v[52:55],v2 offset:44032
v_mul_f32 v97,s76,v92
v_mfma_f32_16x16x32_bf16 v[36:39],a[36:39],v[56:59],v[36:39]
buffer_load_dwordx4 v1,s[36:39],s67 offen offset:2048 sc1 lds
v_mfma_f32_16x16x32_bf16 v[32:35],a[40:43],v[60:63],v[32:35]
s_mov_b64 s[46:47],s[48:49]
ds_read_b128 v[56:59],v2 offset:45056)"
R"(
v_mul_f32 v98,v92,v92
v_mfma_f32_16x16x32_bf16 v[36:39],a[44:47],v[60:63],v[36:39]
s_mov_b64 s[48:49],s[50:51]
v_mul_f32 v99,v93,v92
v_mul_f32 v98,s77,v98
v_mfma_f32_16x16x32_bf16 v[32:35],a[48:51],v[64:67],v[32:35]
ds_read_b128 v[60:63],v2 offset:46080
v_mul_f32 v98,v98,v97
v_mfma_f32_16x16x32_bf16 v[36:39],a[52:55],v[64:67],v[36:39]
buffer_load_dwordx4 v1,s[36:39],s67 offen offset:3072 sc1 lds
v_mfma_f32_16x16x32_bf16 v[32:35],a[56:59],v[68:71],v[32:35]
s_add_u32 m0,s71,s88
ds_read_b128 v[64:67],v2 offset:47104
v_fma_f32 v26,-v96,v18,v26
v_mfma_f32_16x16x32_bf16 v[36:39],a[60:63],v[68:71],v[36:39]
s_waitcnt lgkmcnt(3)
buffer_load_dword v9,s[36:39],s72 offen offset:0 sc1 lds
v_mfma_f32_16x16x32_bf16 v[32:35],a[64:67],v[40:43],v[32:35]
s_waitcnt vmcnt(11)
ds_read_b128 v[68:71],v2 offset:48128
v_fma_f32 v27,-v96,v19,v27
v_mfma_f32_16x16x32_bf16 v[36:39],a[68:71],v[40:43],v[36:39]
s_barrier
v_mfma_f32_16x16x32_bf16 v[32:35],a[72:75],v[44:47],v[32:35]
s_xor_b32 s87,s63,65536
ds_read_b128 v[40:43],v2 offset:49152
ds_read_b32 v88,v8 offset:8192
v_mfma_f32_16x16x32_bf16 v[36:39],a[76:79],v[44:47],v[36:39]
s_cmp_ge_u32 s67,s59
v_mul_f32 v72,v24,v24
v_mul_f32 v73,v25,v25
v_mfma_f32_16x16x32_bf16 v[32:35],a[80:83],v[48:51],v[32:35]
s_cselect_b32 s86,s50,s51
ds_read_b128 v[44:47],v2 offset:50176
v_mul_f32 v74,v26,v26
v_mfma_f32_16x16x32_bf16 v[36:39],a[84:87],v[48:51],v[36:39]
s_cselect_b32 s87,s87,s64
v_mul_f32 v75,v27,v27
v_fma_f32 v72,v98,v72,v97
v_mfma_f32_16x16x32_bf16 v[32:35],a[88:91],v[52:55],v[32:35]
s_lshr_b32 s68,s51,5
ds_read_b128 v[48:51],v2 offset:51200
v_mul_f32 v72,v24,v72
v_mfma_f32_16x16x32_bf16 v[36:39],a[92:95],v[52:55],v[36:39]
s_waitcnt lgkmcnt(4)
v_exp_f32 v72,v72
v_mfma_f32_16x16x32_bf16 v[32:35],a[96:99],v[56:59],v[32:35]
s_waitcnt vmcnt(10) lgkmcnt(2)
ds_read_b128 v[52:55],v2 offset:52224
v_fma_f32 v72,v72,v99,v99
v_mfma_f32_16x16x32_bf16 v[36:39],a[100:103],v[56:59],v[36:39]
s_barrier
v_mfma_f32_16x16x32_bf16 v[32:35],a[104:107],v[60:63],v[32:35]
s_lshr_b32 s70,s52,5
ds_read_b128 v[56:59],v2 offset:53248
ds_read_b32 v13,v7 offset:256
v_mfma_f32_16x16x32_bf16 v[36:39],a[108:111],v[60:63],v[36:39]
s_mov_b32 s62,s87
v_rcp_f32 v72,v72
v_mfma_f32_16x16x32_bf16 v[32:35],a[112:115],v[64:67],v[32:35]
s_mov_b64 s[50:51],s[52:53]
ds_read_b128 v[60:63],v2 offset:54272
v_add_f32_dpp v88,v88,v88 quad_perm:[1,0,3,2] row_mask:0xf bank_mask:0xf
v_mfma_f32_16x16x32_bf16 v[36:39],a[116:119],v[64:67],v[36:39]
s_nop 0
v_add_f32_dpp v88,v88,v88 quad_perm:[2,3,0,1] row_mask:0xf bank_mask:0xf
v_fma_f32 v73,v98,v73,v97
v_mfma_f32_16x16x32_bf16 v[32:35],a[120:123],v[68:71],v[32:35]
s_waitcnt lgkmcnt(1)
ds_read_b128 v[64:67],v2 offset:55296
v_mul_f32 v73,v25,v73
v_mfma_f32_16x16x32_bf16 v[36:39],a[124:127],v[68:71],v[36:39]
v_cmp_eq_u32 vcc,s60,v13
v_exp_f32 v73,v73
v_add_f32_dpp v88,v88,v88 row_half_mirror row_mask:0xf bank_mask:0xf
v_mfma_f32_16x16x32_bf16 v[32:35],a[128:131],v[40:43],v[32:35]
s_cmp_eq_u32 vcc_lo,-1
ds_write_b32 v10,v88 offset:128
v_mfma_f32_16x16x32_bf16 v[36:39],a[132:135],v[40:43],v[36:39]
s_cselect_b32 s66,s86,s59
s_cmp_ge_u32 s66,s59
s_cselect_b32 s68,s68,s70
ds_read_b128 v[68:71],v2 offset:56320
s_add_u32 m0,s78,0
v_mfma_f32_16x16x32_bf16 v[32:35],a[136:139],v[44:47],v[32:35]
buffer_load_dword v6,s[36:39],s68 offen offset:0 sc1 lds
v_mfma_f32_16x16x32_bf16 v[36:39],a[140:143],v[44:47],v[36:39]
s_mov_b32 m0,s62
ds_read_b128 v[40:43],v2 offset:57344
v_fma_f32 v73,v73,v99,v99
v_mfma_f32_16x16x32_bf16 v[32:35],a[144:147],v[48:51],v[32:35]
buffer_load_dwordx4 v0,s[36:39],s66 offen offset:0 sc1 lds
v_mfma_f32_16x16x32_bf16 v[36:39],a[148:151],v[48:51],v[36:39]
s_xor_b32 s64,s64,65536
ds_read_b128 v[44:47],v2 offset:58368
v_fma_f32 v74,v98,v74,v97
v_mfma_f32_16x16x32_bf16 v[32:35],a[152:155],v[52:55],v[32:35]
s_add_u32 s74,s53,s55
ds_read_b128 v[48:51],v2 offset:59392
v_mul_f32 v74,v26,v74
v_mfma_f32_16x16x32_bf16 v[36:39],a[156:159],v[52:55],v[36:39]
s_waitcnt lgkmcnt(3)
v_exp_f32 v74,v74
v_mfma_f32_16x16x32_bf16 v[32:35],a[160:163],v[56:59],v[32:35]
buffer_load_dwordx4 v0,s[36:39],s66 offen offset:1024 sc1 lds
v_mfma_f32_16x16x32_bf16 v[36:39],a[164:167],v[56:59],v[36:39]
s_cmp_eq_u32 s74,s56
ds_read_b128 v[52:55],v2 offset:60416
v_fma_f32 v74,v74,v99,v99
v_mfma_f32_16x16x32_bf16 v[32:35],a[168:171],v[60:63],v[32:35]
s_cselect_b32 s74,s57,s74
ds_read_b128 v[56:59],v2 offset:61440
v_fma_f32 v75,v98,v75,v97
v_mfma_f32_16x16x32_bf16 v[36:39],a[172:175],v[60:63],v[36:39]
s_cmp_eq_u32 s74,s58
v_rcp_f32 v73,v73
v_mfma_f32_16x16x32_bf16 v[32:35],a[176:179],v[64:67],v[32:35]
buffer_load_dwordx4 v0,s[36:39],s66 offen offset:2048 sc1 lds
v_mfma_f32_16x16x32_bf16 v[36:39],a[180:183],v[64:67],v[36:39]
s_cselect_b32 s74,s59,s74
ds_read_b128 v[60:63],v2 offset:62464
v_mul_f32 v75,v27,v75
v_mfma_f32_16x16x32_bf16 v[32:35],a[184:187],v[68:71],v[32:35]
s_mov_b32 s52,s74
ds_read_b128 v[64:67],v2 offset:63488
v_mul_f32 v24,v24,v72
v_mfma_f32_16x16x32_bf16 v[36:39],a[188:191],v[68:71],v[36:39]
s_waitcnt lgkmcnt(3)
v_exp_f32 v75,v75
v_mfma_f32_16x16x32_bf16 v[32:35],a[192:195],v[40:43],v[32:35]
s_waitcnt vmcnt(9)
ds_read_b128 v[68:71],v2 offset:64512
v_xor_b32 v2,65536,v2
v_mfma_f32_16x16x32_bf16 v[36:39],a[196:199],v[40:43],v[36:39]
s_barrier
v_fma_f32 v28,-v96,v20,v28
v_fma_f32 v29,-v96,v21,v29
v_fma_f32 v30,-v96,v22,v30
v_fma_f32 v31,-v96,v23,v31
v_mul_f32 v76,v28,v28
v_mul_f32 v77,v29,v29
v_mul_f32 v78,v30,v30
v_mul_f32 v79,v31,v31
v_fma_f32 v76,v98,v76,v97
v_fma_f32 v77,v98,v77,v97
v_fma_f32 v78,v98,v78,v97
v_fma_f32 v79,v98,v79,v97
v_mul_f32 v76,v28,v76
v_mul_f32 v77,v29,v77
v_mul_f32 v78,v30,v78
v_mul_f32 v79,v31,v79
v_exp_f32 v76,v76
v_exp_f32 v77,v77
v_exp_f32 v78,v78
v_exp_f32 v79,v79
v_mfma_f32_16x16x32_bf16 v[32:35],a[200:203],v[44:47],v[32:35]
buffer_load_dwordx4 v0,s[36:39],s66 offen offset:3072 sc1 lds
v_fma_f32 v75,v75,v99,v99
v_rcp_f32 v74,v74
v_rcp_f32 v75,v75
v_fma_f32 v76,v76,v99,v99
v_fma_f32 v77,v77,v99,v99
v_fma_f32 v78,v78,v99,v99
v_fma_f32 v79,v79,v99,v99
v_rcp_f32 v76,v76
v_rcp_f32 v77,v77
v_rcp_f32 v78,v78
v_rcp_f32 v79,v79
v_mfma_f32_16x16x32_bf16 v[36:39],a[204:207],v[44:47],v[36:39]
s_cmp_lt_u32 s67,s59
ds_read_b128 v[40:43],v2 offset:0
v_mul_f32 v25,v25,v73
v_mul_f32 v26,v26,v74
v_mul_f32 v27,v27,v75
v_cvt_pk_bf16_f32 v80,v24,v25
v_cvt_pk_bf16_f32 v81,v26,v27
v_mul_f32 v28,v28,v76
v_mul_f32 v29,v29,v77
v_mul_f32 v30,v30,v78
v_mul_f32 v31,v31,v79
v_cvt_pk_bf16_f32 v82,v28,v29
v_cvt_pk_bf16_f32 v83,v30,v31
v_mfma_f32_16x16x32_bf16 v[32:35],a[208:211],v[48:51],v[32:35]
buffer_store_dwordx4 v[80:83],v3,s[36:39],s73 offen offset:0 sc0 sc1
ds_read_b128 v[44:47],v2 offset:1024
ds_read_b64 v[92:93],v11 offset:128
v_xor_b32 v8,16384,v8
v_mfma_f32_16x16x32_bf16 v[36:39],a[212:215],v[48:51],v[36:39]
v_mfma_f32_16x16x32_bf16 v[32:35],a[216:219],v[52:55],v[32:35]
ds_read_b128 v[48:51],v2 offset:2048
v_mfma_f32_16x16x32_bf16 v[36:39],a[220:223],v[52:55],v[36:39]
s_waitcnt lgkmcnt(4)
v_mfma_f32_16x16x32_bf16 v[32:35],a[224:227],v[56:59],v[32:35]
s_waitcnt lgkmcnt(1)
ds_read_b128 v[52:55],v2 offset:3072
v_mfma_f32_16x16x32_bf16 v[36:39],a[228:231],v[56:59],v[36:39]
v_mfma_f32_16x16x32_bf16 v[32:35],a[232:235],v[60:63],v[32:35]
ds_read_b128 v[56:59],v2 offset:4096
v_mfma_f32_16x16x32_bf16 v[36:39],a[236:239],v[60:63],v[36:39]
v_mfma_f32_16x16x32_bf16 v[32:35],a[240:243],v[64:67],v[32:35])"
R"(
ds_read_b128 v[60:63],v2 offset:5120
v_mfma_f32_16x16x32_bf16 v[36:39],a[244:247],v[64:67],v[36:39]
v_mfma_f32_16x16x32_bf16 v[32:35],a[248:251],v[68:71],v[32:35]
s_waitcnt lgkmcnt(2)
ds_read_b128 v[64:67],v2 offset:6144
v_mfma_f32_16x16x32_bf16 v[36:39],a[252:255],v[68:71],v[36:39]
s_cbranch_scc1 Lg3o_d0w0_%=
s_branch Lg3o_slow0_%=
Lg3o_d0w1_%=:
v_mfma_f32_16x16x32_bf16 v[24:27],a[0:3],v[40:43],0
s_add_u32 m0,s62,16384
ds_read_b128 v[68:71],v2 offset:7168
v_mul_f32 v96,s75,v92
v_mfma_f32_16x16x32_bf16 v[28:31],a[4:7],v[40:43],0
s_mul_i32 s73,s47,7
v_fma_f32 v93,v93,s75,v12
v_fma_f32 v93,-v96,v96,v93
v_mfma_f32_16x16x32_bf16 v[24:27],a[8:11],v[44:47],v[24:27]
buffer_load_dwordx4 v1,s[36:39],s66 offen offset:0 sc1 lds
v_mfma_f32_16x16x32_bf16 v[28:31],a[12:15],v[44:47],v[28:31]
s_lshr_b32 s73,s73,1
ds_read_b128 v[40:43],v2 offset:8192
v_max_f32 v93,v93,v12
v_mfma_f32_16x16x32_bf16 v[24:27],a[16:19],v[48:51],v[24:27]
s_lshr_b32 s72,s66,5
ds_read_b128 v[44:47],v2 offset:9216
v_fma_f32 v32,-v96,v16,v32
v_mfma_f32_16x16x32_bf16 v[28:31],a[20:23],v[48:51],v[28:31]
s_lshr_b32 s71,s62,2
v_rsq_f32 v92,v93
v_mfma_f32_16x16x32_bf16 v[24:27],a[24:27],v[52:55],v[24:27]
buffer_load_dwordx4 v1,s[36:39],s66 offen offset:1024 sc1 lds
v_mfma_f32_16x16x32_bf16 v[28:31],a[28:31],v[52:55],v[28:31]
s_waitcnt lgkmcnt(2)
ds_read_b128 v[48:51],v2 offset:10240
v_mul_f32 v97,s76,v92
v_mfma_f32_16x16x32_bf16 v[24:27],a[32:35],v[56:59],v[24:27]
ds_read_b128 v[52:55],v2 offset:11264
v_mul_f32 v98,v92,v92
v_mfma_f32_16x16x32_bf16 v[28:31],a[36:39],v[56:59],v[28:31]
v_mul_f32 v99,v93,v92
v_mul_f32 v98,s77,v98
v_mfma_f32_16x16x32_bf16 v[24:27],a[40:43],v[60:63],v[24:27]
buffer_load_dwordx4 v1,s[36:39],s66 offen offset:2048 sc1 lds
v_mfma_f32_16x16x32_bf16 v[28:31],a[44:47],v[60:63],v[28:31]
ds_read_b128 v[56:59],v2 offset:12288
v_mul_f32 v98,v98,v97
v_mfma_f32_16x16x32_bf16 v[24:27],a[48:51],v[64:67],v[24:27]
ds_read_b128 v[60:63],v2 offset:13312
v_fma_f32 v33,-v96,v17,v33
v_mfma_f32_16x16x32_bf16 v[28:31],a[52:55],v[64:67],v[28:31]
v_fma_f32 v34,-v96,v18,v34
v_fma_f32 v35,-v96,v19,v35
v_mfma_f32_16x16x32_bf16 v[24:27],a[56:59],v[68:71],v[24:27]
buffer_load_dwordx4 v1,s[36:39],s66 offen offset:3072 sc1 lds
v_mfma_f32_16x16x32_bf16 v[28:31],a[60:63],v[68:71],v[28:31]
s_waitcnt lgkmcnt(2)
ds_read_b128 v[64:67],v2 offset:14336
s_add_u32 m0,s71,s88
v_mfma_f32_16x16x32_bf16 v[24:27],a[64:67],v[40:43],v[24:27]
s_xor_b32 s87,s64,65536
ds_read_b128 v[68:71],v2 offset:15360
buffer_load_dword v9,s[36:39],s72 offen offset:0 sc1 lds
s_waitcnt vmcnt(10)
v_mfma_f32_16x16x32_bf16 v[28:31],a[68:71],v[40:43],v[28:31]
s_barrier
v_mfma_f32_16x16x32_bf16 v[24:27],a[72:75],v[44:47],v[24:27]
s_cmp_ge_u32 s66,s59
ds_read_b128 v[40:43],v2 offset:16384
ds_read_b32 v88,v8 offset:0
v_mfma_f32_16x16x32_bf16 v[28:31],a[76:79],v[44:47],v[28:31]
s_cselect_b32 s86,s49,s50
v_mul_f32 v72,v32,v32
v_mul_f32 v73,v33,v33
v_mfma_f32_16x16x32_bf16 v[24:27],a[80:83],v[48:51],v[24:27]
s_cselect_b32 s87,s87,s63
ds_read_b128 v[44:47],v2 offset:17408
v_mul_f32 v74,v34,v34
v_mfma_f32_16x16x32_bf16 v[28:31],a[84:87],v[48:51],v[28:31]
s_mov_b32 s62,s87
v_mul_f32 v75,v35,v35
v_fma_f32 v72,v98,v72,v97
v_mfma_f32_16x16x32_bf16 v[24:27],a[88:91],v[52:55],v[24:27]
s_mov_b32 m0,s62
ds_read_b128 v[48:51],v2 offset:18432
v_mul_f32 v72,v32,v72
v_mfma_f32_16x16x32_bf16 v[28:31],a[92:95],v[52:55],v[28:31]
s_waitcnt lgkmcnt(4)
v_exp_f32 v72,v72
v_mfma_f32_16x16x32_bf16 v[24:27],a[96:99],v[56:59],v[24:27]
s_nop 0
s_waitcnt lgkmcnt(2)
ds_read_b128 v[52:55],v2 offset:19456
v_fma_f32 v72,v72,v99,v99
v_mfma_f32_16x16x32_bf16 v[28:31],a[100:103],v[56:59],v[28:31]
s_barrier
v_mfma_f32_16x16x32_bf16 v[24:27],a[104:107],v[60:63],v[24:27]
ds_read_b128 v[56:59],v2 offset:20480
ds_read_b32 v13,v7 offset:0
v_mfma_f32_16x16x32_bf16 v[28:31],a[108:111],v[60:63],v[28:31]
v_rcp_f32 v72,v72
v_mfma_f32_16x16x32_bf16 v[24:27],a[112:115],v[64:67],v[24:27]
ds_read_b128 v[60:63],v2 offset:21504
v_add_f32_dpp v88,v88,v88 quad_perm:[1,0,3,2] row_mask:0xf bank_mask:0xf
v_mfma_f32_16x16x32_bf16 v[28:31],a[116:119],v[64:67],v[28:31]
s_nop 0
v_add_f32_dpp v88,v88,v88 quad_perm:[2,3,0,1] row_mask:0xf bank_mask:0xf
v_fma_f32 v73,v98,v73,v97
v_mfma_f32_16x16x32_bf16 v[24:27],a[120:123],v[68:71],v[24:27]
s_waitcnt lgkmcnt(1)
ds_read_b128 v[64:67],v2 offset:22528
v_mul_f32 v73,v33,v73
v_mfma_f32_16x16x32_bf16 v[28:31],a[124:127],v[68:71],v[28:31]
v_cmp_eq_u32 vcc,s60,v13
v_exp_f32 v73,v73
v_add_f32_dpp v88,v88,v88 row_half_mirror row_mask:0xf bank_mask:0xf
v_mfma_f32_16x16x32_bf16 v[24:27],a[128:131],v[40:43],v[24:27]
s_cmp_eq_u32 vcc_lo,-1
ds_write_b32 v10,v88 offset:0
s_cselect_b32 s67,s86,s59
v_mfma_f32_16x16x32_bf16 v[28:31],a[132:135],v[40:43],v[28:31]
s_xor_b32 s63,s63,65536
ds_read_b128 v[68:71],v2 offset:23552
buffer_load_dwordx4 v0,s[36:39],s67 offen offset:0 sc1 lds
v_mfma_f32_16x16x32_bf16 v[24:27],a[136:139],v[44:47],v[24:27]
s_add_u32 s53,s52,s55
ds_read_b128 v[40:43],v2 offset:24576
v_fma_f32 v73,v73,v99,v99
v_mfma_f32_16x16x32_bf16 v[28:31],a[140:143],v[44:47],v[28:31]
s_cmp_eq_u32 s53,s56
v_rcp_f32 v73,v73
v_mfma_f32_16x16x32_bf16 v[24:27],a[144:147],v[48:51],v[24:27]
s_cselect_b32 s53,s57,s53
ds_read_b128 v[44:47],v2 offset:25600
v_fma_f32 v74,v98,v74,v97
v_mfma_f32_16x16x32_bf16 v[28:31],a[148:151],v[48:51],v[28:31]
buffer_load_dwordx4 v0,s[36:39],s67 offen offset:1024 sc1 lds
v_mfma_f32_16x16x32_bf16 v[24:27],a[152:155],v[52:55],v[24:27]
s_cmp_eq_u32 s53,s58
ds_read_b128 v[48:51],v2 offset:26624
v_mul_f32 v74,v34,v74
v_mfma_f32_16x16x32_bf16 v[28:31],a[156:159],v[52:55],v[28:31]
s_waitcnt lgkmcnt(3)
v_exp_f32 v74,v74
v_mfma_f32_16x16x32_bf16 v[24:27],a[160:163],v[56:59],v[24:27]
s_cselect_b32 s53,s59,s53
ds_read_b128 v[52:55],v2 offset:27648
v_fma_f32 v74,v74,v99,v99
v_mfma_f32_16x16x32_bf16 v[28:31],a[164:167],v[56:59],v[28:31]
buffer_load_dwordx4 v0,s[36:39],s67 offen offset:2048 sc1 lds
v_mfma_f32_16x16x32_bf16 v[24:27],a[168:171],v[60:63],v[24:27]
s_cmp_ge_u32 s66,s59
ds_read_b128 v[56:59],v2 offset:28672
v_fma_f32 v75,v98,v75,v97
v_mfma_f32_16x16x32_bf16 v[28:31],a[172:175],v[60:63],v[28:31]
v_rcp_f32 v74,v74
v_mfma_f32_16x16x32_bf16 v[24:27],a[176:179],v[64:67],v[24:27]
ds_read_b128 v[60:63],v2 offset:29696
v_mul_f32 v75,v35,v75
v_mfma_f32_16x16x32_bf16 v[28:31],a[180:183],v[64:67],v[28:31]
buffer_load_dwordx4 v0,s[36:39],s67 offen offset:3072 sc1 lds
v_mfma_f32_16x16x32_bf16 v[24:27],a[184:187],v[68:71],v[24:27]
ds_read_b128 v[64:67],v2 offset:30720
v_mul_f32 v32,v32,v72
v_mfma_f32_16x16x32_bf16 v[28:31],a[188:191],v[68:71],v[28:31]
s_waitcnt lgkmcnt(3)
v_exp_f32 v75,v75
v_mfma_f32_16x16x32_bf16 v[24:27],a[192:195],v[40:43],v[24:27]
s_waitcnt vmcnt(9)
ds_read_b128 v[68:71],v2 offset:31744
v_fma_f32 v75,v75,v99,v99
v_mfma_f32_16x16x32_bf16 v[28:31],a[196:199],v[40:43],v[28:31]
s_barrier
v_mfma_f32_16x16x32_bf16 v[24:27],a[200:203],v[44:47],v[24:27]
v_fma_f32 v36,-v96,v20,v36
v_fma_f32 v37,-v96,v21,v37
v_fma_f32 v38,-v96,v22,v38
v_fma_f32 v39,-v96,v23,v39
v_mul_f32 v76,v36,v36
v_mul_f32 v77,v37,v37
v_mul_f32 v78,v38,v38
v_mul_f32 v79,v39,v39
v_fma_f32 v76,v98,v76,v97
v_fma_f32 v77,v98,v77,v97
v_fma_f32 v78,v98,v78,v97
v_fma_f32 v79,v98,v79,v97
v_mul_f32 v76,v36,v76
v_mul_f32 v77,v37,v77
v_mul_f32 v78,v38,v78
v_mul_f32 v79,v39,v79
v_exp_f32 v76,v76
v_exp_f32 v77,v77
v_exp_f32 v78,v78
v_exp_f32 v79,v79
v_mfma_f32_16x16x32_bf16 v[28:31],a[204:207],v[44:47],v[28:31]
ds_read_b128 v[40:43],v2 offset:32768
v_rcp_f32 v75,v75
v_fma_f32 v76,v76,v99,v99
v_fma_f32 v77,v77,v99,v99
v_fma_f32 v78,v78,v99,v99)"
R"(
v_fma_f32 v79,v79,v99,v99
v_rcp_f32 v76,v76
v_rcp_f32 v77,v77
v_rcp_f32 v78,v78
v_rcp_f32 v79,v79
v_mfma_f32_16x16x32_bf16 v[24:27],a[208:211],v[48:51],v[24:27]
v_mul_f32 v33,v33,v73
v_mul_f32 v34,v34,v74
v_mul_f32 v35,v35,v75
v_cvt_pk_bf16_f32 v80,v32,v33
v_cvt_pk_bf16_f32 v81,v34,v35
v_mul_f32 v36,v36,v76
v_mul_f32 v37,v37,v77
v_mul_f32 v38,v38,v78
v_mul_f32 v39,v39,v79
v_cvt_pk_bf16_f32 v82,v36,v37
v_cvt_pk_bf16_f32 v83,v38,v39
v_mfma_f32_16x16x32_bf16 v[28:31],a[212:215],v[48:51],v[28:31]
ds_read_b128 v[44:47],v2 offset:33792
buffer_store_dwordx4 v[80:83],v3,s[36:39],s73 offen offset:0 sc0 sc1
ds_read_b64 v[92:93],v11 offset:0
v_mfma_f32_16x16x32_bf16 v[24:27],a[216:219],v[52:55],v[24:27]
ds_read_b128 v[48:51],v2 offset:34816
v_mfma_f32_16x16x32_bf16 v[28:31],a[220:223],v[52:55],v[28:31]
s_waitcnt lgkmcnt(4)
v_mfma_f32_16x16x32_bf16 v[24:27],a[224:227],v[56:59],v[24:27]
ds_read_b128 v[52:55],v2 offset:35840
v_mfma_f32_16x16x32_bf16 v[28:31],a[228:231],v[56:59],v[28:31]
s_waitcnt lgkmcnt(2)
v_mfma_f32_16x16x32_bf16 v[24:27],a[232:235],v[60:63],v[24:27]
ds_read_b128 v[56:59],v2 offset:36864
v_mfma_f32_16x16x32_bf16 v[28:31],a[236:239],v[60:63],v[28:31]
v_mfma_f32_16x16x32_bf16 v[24:27],a[240:243],v[64:67],v[24:27]
ds_read_b128 v[60:63],v2 offset:37888
v_mfma_f32_16x16x32_bf16 v[28:31],a[244:247],v[64:67],v[28:31]
v_mfma_f32_16x16x32_bf16 v[24:27],a[248:251],v[68:71],v[24:27]
s_waitcnt lgkmcnt(2)
ds_read_b128 v[64:67],v2 offset:38912
v_mfma_f32_16x16x32_bf16 v[28:31],a[252:255],v[68:71],v[28:31]
s_cbranch_scc1 Lg3o_slow1_%=
Lg3o_d1w1_%=:
v_mfma_f32_16x16x32_bf16 v[32:35],a[0:3],v[40:43],0
s_add_u32 m0,s62,16384
ds_read_b128 v[68:71],v2 offset:39936
v_mul_f32 v96,s75,v92
v_mfma_f32_16x16x32_bf16 v[36:39],a[4:7],v[40:43],0
s_mul_i32 s73,s48,7
v_fma_f32 v93,v93,s75,v12
v_fma_f32 v93,-v96,v96,v93
v_mfma_f32_16x16x32_bf16 v[32:35],a[8:11],v[44:47],v[32:35]
buffer_load_dwordx4 v1,s[36:39],s67 offen offset:0 sc1 lds
v_mfma_f32_16x16x32_bf16 v[36:39],a[12:15],v[44:47],v[36:39]
s_lshr_b32 s73,s73,1
ds_read_b128 v[40:43],v2 offset:40960
v_max_f32 v93,v93,v12
v_mfma_f32_16x16x32_bf16 v[32:35],a[16:19],v[48:51],v[32:35]
s_lshr_b32 s72,s67,5
ds_read_b128 v[44:47],v2 offset:41984
v_fma_f32 v24,-v96,v16,v24
v_mfma_f32_16x16x32_bf16 v[36:39],a[20:23],v[48:51],v[36:39]
s_lshr_b32 s71,s62,2
v_rsq_f32 v92,v93
v_mfma_f32_16x16x32_bf16 v[32:35],a[24:27],v[52:55],v[32:35]
buffer_load_dwordx4 v1,s[36:39],s67 offen offset:1024 sc1 lds
v_mfma_f32_16x16x32_bf16 v[36:39],a[28:31],v[52:55],v[36:39]
s_waitcnt lgkmcnt(2)
ds_read_b128 v[48:51],v2 offset:43008
v_mul_f32 v97,s76,v92
v_mfma_f32_16x16x32_bf16 v[32:35],a[32:35],v[56:59],v[32:35]
s_mov_b64 s[44:45],s[46:47]
ds_read_b128 v[52:55],v2 offset:44032
v_mul_f32 v98,v92,v92
v_mfma_f32_16x16x32_bf16 v[36:39],a[36:39],v[56:59],v[36:39]
s_mov_b64 s[46:47],s[48:49]
v_mul_f32 v99,v93,v92
v_mul_f32 v98,s77,v98
v_mfma_f32_16x16x32_bf16 v[32:35],a[40:43],v[60:63],v[32:35]
buffer_load_dwordx4 v1,s[36:39],s67 offen offset:2048 sc1 lds
v_mfma_f32_16x16x32_bf16 v[36:39],a[44:47],v[60:63],v[36:39]
s_mov_b64 s[48:49],s[50:51]
ds_read_b128 v[56:59],v2 offset:45056
v_mul_f32 v98,v98,v97
v_mfma_f32_16x16x32_bf16 v[32:35],a[48:51],v[64:67],v[32:35]
ds_read_b128 v[60:63],v2 offset:46080
v_fma_f32 v25,-v96,v17,v25
v_mfma_f32_16x16x32_bf16 v[36:39],a[52:55],v[64:67],v[36:39]
v_fma_f32 v26,-v96,v18,v26
v_fma_f32 v27,-v96,v19,v27
v_mfma_f32_16x16x32_bf16 v[32:35],a[56:59],v[68:71],v[32:35]
buffer_load_dwordx4 v1,s[36:39],s67 offen offset:3072 sc1 lds
v_mfma_f32_16x16x32_bf16 v[36:39],a[60:63],v[68:71],v[36:39]
s_waitcnt lgkmcnt(2)
ds_read_b128 v[64:67],v2 offset:47104
s_add_u32 m0,s71,s88
v_mfma_f32_16x16x32_bf16 v[32:35],a[64:67],v[40:43],v[32:35]
s_xor_b32 s87,s63,65536
ds_read_b128 v[68:71],v2 offset:48128
buffer_load_dword v9,s[36:39],s72 offen offset:0 sc1 lds
s_waitcnt vmcnt(10)
v_mfma_f32_16x16x32_bf16 v[36:39],a[68:71],v[40:43],v[36:39]
s_barrier
v_mfma_f32_16x16x32_bf16 v[32:35],a[72:75],v[44:47],v[32:35]
s_cmp_ge_u32 s67,s59
ds_read_b128 v[40:43],v2 offset:49152
ds_read_b32 v88,v8 offset:8192
v_mfma_f32_16x16x32_bf16 v[36:39],a[76:79],v[44:47],v[36:39]
s_cselect_b32 s86,s50,s51
v_mul_f32 v72,v24,v24
v_mul_f32 v73,v25,v25
v_mfma_f32_16x16x32_bf16 v[32:35],a[80:83],v[48:51],v[32:35]
s_cselect_b32 s87,s87,s64
ds_read_b128 v[44:47],v2 offset:50176
v_mul_f32 v74,v26,v26
v_mfma_f32_16x16x32_bf16 v[36:39],a[84:87],v[48:51],v[36:39]
s_mov_b32 s62,s87
v_mul_f32 v75,v27,v27
v_fma_f32 v72,v98,v72,v97
v_mfma_f32_16x16x32_bf16 v[32:35],a[88:91],v[52:55],v[32:35]
s_mov_b32 m0,s62
ds_read_b128 v[48:51],v2 offset:51200
v_mul_f32 v72,v24,v72
v_mfma_f32_16x16x32_bf16 v[36:39],a[92:95],v[52:55],v[36:39]
s_waitcnt lgkmcnt(4)
v_exp_f32 v72,v72
v_mfma_f32_16x16x32_bf16 v[32:35],a[96:99],v[56:59],v[32:35]
s_nop 0
s_waitcnt lgkmcnt(2)
ds_read_b128 v[52:55],v2 offset:52224
v_fma_f32 v72,v72,v99,v99
v_mfma_f32_16x16x32_bf16 v[36:39],a[100:103],v[56:59],v[36:39]
s_barrier
v_mfma_f32_16x16x32_bf16 v[32:35],a[104:107],v[60:63],v[32:35]
s_mov_b64 s[50:51],s[52:53]
ds_read_b128 v[56:59],v2 offset:53248
ds_read_b32 v13,v7 offset:256
v_mfma_f32_16x16x32_bf16 v[36:39],a[108:111],v[60:63],v[36:39]
v_rcp_f32 v72,v72
v_mfma_f32_16x16x32_bf16 v[32:35],a[112:115],v[64:67],v[32:35]
ds_read_b128 v[60:63],v2 offset:54272
v_add_f32_dpp v88,v88,v88 quad_perm:[1,0,3,2] row_mask:0xf bank_mask:0xf
v_mfma_f32_16x16x32_bf16 v[36:39],a[116:119],v[64:67],v[36:39]
s_nop 0
v_add_f32_dpp v88,v88,v88 quad_perm:[2,3,0,1] row_mask:0xf bank_mask:0xf
v_fma_f32 v73,v98,v73,v97
v_mfma_f32_16x16x32_bf16 v[32:35],a[120:123],v[68:71],v[32:35]
s_waitcnt lgkmcnt(1)
ds_read_b128 v[64:67],v2 offset:55296
v_mul_f32 v73,v25,v73
v_mfma_f32_16x16x32_bf16 v[36:39],a[124:127],v[68:71],v[36:39]
v_cmp_eq_u32 vcc,s60,v13
v_exp_f32 v73,v73
v_add_f32_dpp v88,v88,v88 row_half_mirror row_mask:0xf bank_mask:0xf
v_mfma_f32_16x16x32_bf16 v[32:35],a[128:131],v[40:43],v[32:35]
s_cmp_eq_u32 vcc_lo,-1
ds_write_b32 v10,v88 offset:128
s_cselect_b32 s66,s86,s59
v_mfma_f32_16x16x32_bf16 v[36:39],a[132:135],v[40:43],v[36:39]
s_xor_b32 s64,s64,65536
ds_read_b128 v[68:71],v2 offset:56320
buffer_load_dwordx4 v0,s[36:39],s66 offen offset:0 sc1 lds
v_mfma_f32_16x16x32_bf16 v[32:35],a[136:139],v[44:47],v[32:35]
s_add_u32 s74,s53,s55
ds_read_b128 v[40:43],v2 offset:57344
v_fma_f32 v73,v73,v99,v99
v_mfma_f32_16x16x32_bf16 v[36:39],a[140:143],v[44:47],v[36:39]
s_cmp_eq_u32 s74,s56
v_rcp_f32 v73,v73
v_mfma_f32_16x16x32_bf16 v[32:35],a[144:147],v[48:51],v[32:35]
s_cselect_b32 s74,s57,s74
ds_read_b128 v[44:47],v2 offset:58368
v_fma_f32 v74,v98,v74,v97
v_mfma_f32_16x16x32_bf16 v[36:39],a[148:151],v[48:51],v[36:39]
buffer_load_dwordx4 v0,s[36:39],s66 offen offset:1024 sc1 lds
v_mfma_f32_16x16x32_bf16 v[32:35],a[152:155],v[52:55],v[32:35]
s_cmp_eq_u32 s74,s58
ds_read_b128 v[48:51],v2 offset:59392
v_mul_f32 v74,v26,v74
v_mfma_f32_16x16x32_bf16 v[36:39],a[156:159],v[52:55],v[36:39]
s_waitcnt lgkmcnt(3)
v_exp_f32 v74,v74
v_mfma_f32_16x16x32_bf16 v[32:35],a[160:163],v[56:59],v[32:35]
s_cselect_b32 s74,s59,s74
ds_read_b128 v[52:55],v2 offset:60416
v_fma_f32 v74,v74,v99,v99
v_mfma_f32_16x16x32_bf16 v[36:39],a[164:167],v[56:59],v[36:39]
buffer_load_dwordx4 v0,s[36:39],s66 offen offset:2048 sc1 lds
v_mfma_f32_16x16x32_bf16 v[32:35],a[168:171],v[60:63],v[32:35]
s_mov_b32 s52,s74
ds_read_b128 v[56:59],v2 offset:61440
v_fma_f32 v75,v98,v75,v97
v_mfma_f32_16x16x32_bf16 v[36:39],a[172:175],v[60:63],v[36:39]
s_cmp_lt_u32 s67,s59
v_rcp_f32 v74,v74
v_mfma_f32_16x16x32_bf16 v[32:35],a[176:179],v[64:67],v[32:35]
ds_read_b128 v[60:63],v2 offset:62464
v_mul_f32 v75,v27,v75
v_mfma_f32_16x16x32_bf16 v[36:39],a[180:183],v[64:67],v[36:39]
buffer_load_dwordx4 v0,s[36:39],s66 offen offset:3072 sc1 lds
v_mfma_f32_16x16x32_bf16 v[32:35],a[184:187],v[68:71],v[32:35]
ds_read_b128 v[64:67],v2 offset:63488)"
R"(
v_mul_f32 v24,v24,v72
v_mfma_f32_16x16x32_bf16 v[36:39],a[188:191],v[68:71],v[36:39]
s_waitcnt lgkmcnt(3)
v_exp_f32 v75,v75
v_mfma_f32_16x16x32_bf16 v[32:35],a[192:195],v[40:43],v[32:35]
s_waitcnt vmcnt(9)
ds_read_b128 v[68:71],v2 offset:64512
v_xor_b32 v2,65536,v2
v_mfma_f32_16x16x32_bf16 v[36:39],a[196:199],v[40:43],v[36:39]
s_barrier
v_mfma_f32_16x16x32_bf16 v[32:35],a[200:203],v[44:47],v[32:35]
v_fma_f32 v28,-v96,v20,v28
v_fma_f32 v29,-v96,v21,v29
v_fma_f32 v30,-v96,v22,v30
v_fma_f32 v31,-v96,v23,v31
v_mul_f32 v76,v28,v28
v_mul_f32 v77,v29,v29
v_mul_f32 v78,v30,v30
v_mul_f32 v79,v31,v31
v_fma_f32 v76,v98,v76,v97
v_fma_f32 v77,v98,v77,v97
v_fma_f32 v78,v98,v78,v97
v_fma_f32 v79,v98,v79,v97
v_mul_f32 v76,v28,v76
v_mul_f32 v77,v29,v77
v_mul_f32 v78,v30,v78
v_mul_f32 v79,v31,v79
v_exp_f32 v76,v76
v_exp_f32 v77,v77
v_exp_f32 v78,v78
v_exp_f32 v79,v79
v_mfma_f32_16x16x32_bf16 v[36:39],a[204:207],v[44:47],v[36:39]
ds_read_b128 v[40:43],v2 offset:0
v_fma_f32 v75,v75,v99,v99
v_rcp_f32 v75,v75
v_fma_f32 v76,v76,v99,v99
v_fma_f32 v77,v77,v99,v99
v_fma_f32 v78,v78,v99,v99
v_fma_f32 v79,v79,v99,v99
v_rcp_f32 v76,v76
v_rcp_f32 v77,v77
v_rcp_f32 v78,v78
v_rcp_f32 v79,v79
v_mfma_f32_16x16x32_bf16 v[32:35],a[208:211],v[48:51],v[32:35]
v_mul_f32 v25,v25,v73
v_mul_f32 v26,v26,v74
v_mul_f32 v27,v27,v75
v_cvt_pk_bf16_f32 v80,v24,v25
v_cvt_pk_bf16_f32 v81,v26,v27
v_mul_f32 v28,v28,v76
v_mul_f32 v29,v29,v77
v_mul_f32 v30,v30,v78
v_mul_f32 v31,v31,v79
v_cvt_pk_bf16_f32 v82,v28,v29
v_cvt_pk_bf16_f32 v83,v30,v31
v_mfma_f32_16x16x32_bf16 v[36:39],a[212:215],v[48:51],v[36:39]
ds_read_b128 v[44:47],v2 offset:1024
buffer_store_dwordx4 v[80:83],v3,s[36:39],s73 offen offset:0 sc0 sc1
ds_read_b64 v[92:93],v11 offset:128
v_xor_b32 v8,16384,v8
v_mfma_f32_16x16x32_bf16 v[32:35],a[216:219],v[52:55],v[32:35]
ds_read_b128 v[48:51],v2 offset:2048
v_mfma_f32_16x16x32_bf16 v[36:39],a[220:223],v[52:55],v[36:39]
s_waitcnt lgkmcnt(4)
v_mfma_f32_16x16x32_bf16 v[32:35],a[224:227],v[56:59],v[32:35]
ds_read_b128 v[52:55],v2 offset:3072
v_mfma_f32_16x16x32_bf16 v[36:39],a[228:231],v[56:59],v[36:39]
s_waitcnt lgkmcnt(2)
v_mfma_f32_16x16x32_bf16 v[32:35],a[232:235],v[60:63],v[32:35]
ds_read_b128 v[56:59],v2 offset:4096
v_mfma_f32_16x16x32_bf16 v[36:39],a[236:239],v[60:63],v[36:39]
v_mfma_f32_16x16x32_bf16 v[32:35],a[240:243],v[64:67],v[32:35]
ds_read_b128 v[60:63],v2 offset:5120
v_mfma_f32_16x16x32_bf16 v[36:39],a[244:247],v[64:67],v[36:39]
v_mfma_f32_16x16x32_bf16 v[32:35],a[248:251],v[68:71],v[32:35]
s_waitcnt lgkmcnt(2)
ds_read_b128 v[64:67],v2 offset:6144
v_mfma_f32_16x16x32_bf16 v[36:39],a[252:255],v[68:71],v[36:39]
s_cbranch_scc1 Lg3o_d0w1_%=
s_branch Lg3o_slow0_%=
Lg3o_d0w2_%=:
v_mfma_f32_16x16x32_bf16 v[24:27],a[0:3],v[40:43],0
s_add_u32 m0,s62,16384
ds_read_b128 v[68:71],v2 offset:7168
v_mul_f32 v96,s75,v92
v_mfma_f32_16x16x32_bf16 v[28:31],a[4:7],v[40:43],0
s_mul_i32 s73,s47,7
v_fma_f32 v93,v93,s75,v12
v_fma_f32 v93,-v96,v96,v93
v_mfma_f32_16x16x32_bf16 v[24:27],a[8:11],v[44:47],v[24:27]
s_lshr_b32 s73,s73,1
ds_read_b128 v[40:43],v2 offset:8192
v_max_f32 v93,v93,v12
v_mfma_f32_16x16x32_bf16 v[28:31],a[12:15],v[44:47],v[28:31]
buffer_load_dwordx4 v1,s[36:39],s66 offen offset:0 sc1 lds
v_mfma_f32_16x16x32_bf16 v[24:27],a[16:19],v[48:51],v[24:27]
s_lshr_b32 s72,s66,5
ds_read_b128 v[44:47],v2 offset:9216
v_fma_f32 v32,-v96,v16,v32
v_mfma_f32_16x16x32_bf16 v[28:31],a[20:23],v[48:51],v[28:31]
s_lshr_b32 s71,s62,2
v_rsq_f32 v92,v93
v_mfma_f32_16x16x32_bf16 v[24:27],a[24:27],v[52:55],v[24:27]
ds_read_b128 v[48:51],v2 offset:10240
v_mul_f32 v97,s76,v92
v_mfma_f32_16x16x32_bf16 v[28:31],a[28:31],v[52:55],v[28:31]
s_waitcnt lgkmcnt(3)
buffer_load_dwordx4 v1,s[36:39],s66 offen offset:1024 sc1 lds
v_mfma_f32_16x16x32_bf16 v[24:27],a[32:35],v[56:59],v[24:27]
ds_read_b128 v[52:55],v2 offset:11264
v_mul_f32 v98,v92,v92
v_mfma_f32_16x16x32_bf16 v[28:31],a[36:39],v[56:59],v[28:31]
v_mul_f32 v99,v93,v92
v_mul_f32 v98,s77,v98
v_mfma_f32_16x16x32_bf16 v[24:27],a[40:43],v[60:63],v[24:27]
ds_read_b128 v[56:59],v2 offset:12288
v_mul_f32 v98,v98,v97
v_mfma_f32_16x16x32_bf16 v[28:31],a[44:47],v[60:63],v[28:31]
buffer_load_dwordx4 v1,s[36:39],s66 offen offset:2048 sc1 lds
v_mfma_f32_16x16x32_bf16 v[24:27],a[48:51],v[64:67],v[24:27]
ds_read_b128 v[60:63],v2 offset:13312
v_fma_f32 v33,-v96,v17,v33
v_mfma_f32_16x16x32_bf16 v[28:31],a[52:55],v[64:67],v[28:31]
v_fma_f32 v34,-v96,v18,v34
v_fma_f32 v35,-v96,v19,v35
v_mfma_f32_16x16x32_bf16 v[24:27],a[56:59],v[68:71],v[24:27]
ds_read_b128 v[64:67],v2 offset:14336
v_mul_f32 v72,v32,v32
v_mfma_f32_16x16x32_bf16 v[28:31],a[60:63],v[68:71],v[28:31]
s_waitcnt lgkmcnt(3)
buffer_load_dwordx4 v1,s[36:39],s66 offen offset:3072 sc1 lds
s_add_u32 m0,s71,s88
v_mfma_f32_16x16x32_bf16 v[24:27],a[64:67],v[40:43],v[24:27]
s_xor_b32 s87,s64,65536
ds_read_b128 v[68:71],v2 offset:15360
buffer_load_dword v9,s[36:39],s72 offen offset:0 sc1 lds
s_waitcnt vmcnt(10)
v_mfma_f32_16x16x32_bf16 v[28:31],a[68:71],v[40:43],v[28:31]
s_barrier
v_mfma_f32_16x16x32_bf16 v[24:27],a[72:75],v[44:47],v[24:27]
s_cmp_ge_u32 s66,s59
ds_read_b128 v[40:43],v2 offset:16384
ds_read_b32 v88,v8 offset:0
v_mfma_f32_16x16x32_bf16 v[28:31],a[76:79],v[44:47],v[28:31]
s_cselect_b32 s86,s49,s50
v_mul_f32 v73,v33,v33
v_mul_f32 v74,v34,v34
v_mfma_f32_16x16x32_bf16 v[24:27],a[80:83],v[48:51],v[24:27]
s_cselect_b32 s87,s87,s63
ds_read_b128 v[44:47],v2 offset:17408
v_mul_f32 v75,v35,v35
v_mfma_f32_16x16x32_bf16 v[28:31],a[84:87],v[48:51],v[28:31]
s_mov_b32 s62,s87
v_fma_f32 v72,v98,v72,v97
v_mul_f32 v72,v32,v72
v_mfma_f32_16x16x32_bf16 v[24:27],a[88:91],v[52:55],v[24:27]
s_mov_b32 m0,s62
ds_read_b128 v[48:51],v2 offset:18432
v_fma_f32 v73,v98,v73,v97
v_mfma_f32_16x16x32_bf16 v[28:31],a[92:95],v[52:55],v[28:31]
s_waitcnt lgkmcnt(4)
v_exp_f32 v72,v72
v_mfma_f32_16x16x32_bf16 v[24:27],a[96:99],v[56:59],v[24:27]
s_nop 0
s_waitcnt lgkmcnt(2)
ds_read_b128 v[52:55],v2 offset:19456
v_mul_f32 v73,v33,v73
v_mfma_f32_16x16x32_bf16 v[28:31],a[100:103],v[56:59],v[28:31]
s_barrier
v_mfma_f32_16x16x32_bf16 v[24:27],a[104:107],v[60:63],v[24:27]
ds_read_b128 v[56:59],v2 offset:20480
ds_read_b32 v13,v7 offset:0
v_mfma_f32_16x16x32_bf16 v[28:31],a[108:111],v[60:63],v[28:31]
v_exp_f32 v73,v73
v_mfma_f32_16x16x32_bf16 v[24:27],a[112:115],v[64:67],v[24:27]
ds_read_b128 v[60:63],v2 offset:21504
v_fma_f32 v72,v72,v99,v99
v_mfma_f32_16x16x32_bf16 v[28:31],a[116:119],v[64:67],v[28:31]
v_rcp_f32 v72,v72
v_mfma_f32_16x16x32_bf16 v[24:27],a[120:123],v[68:71],v[24:27]
s_waitcnt lgkmcnt(1)
ds_read_b128 v[64:67],v2 offset:22528
v_fma_f32 v73,v73,v99,v99
v_mfma_f32_16x16x32_bf16 v[28:31],a[124:127],v[68:71],v[28:31]
v_cmp_eq_u32 vcc,s60,v13
v_rcp_f32 v73,v73
v_add_f32_dpp v88,v88,v88 quad_perm:[1,0,3,2] row_mask:0xf bank_mask:0xf
v_mfma_f32_16x16x32_bf16 v[24:27],a[128:131],v[40:43],v[24:27]
s_cmp_eq_u32 vcc_lo,-1
ds_read_b128 v[68:71],v2 offset:23552
v_add_f32_dpp v88,v88,v88 quad_perm:[2,3,0,1] row_mask:0xf bank_mask:0xf
v_mfma_f32_16x16x32_bf16 v[28:31],a[132:135],v[40:43],v[28:31]
s_cselect_b32 s67,s86,s59
v_add_f32_dpp v88,v88,v88 row_half_mirror row_mask:0xf bank_mask:0xf
v_fma_f32 v74,v98,v74,v97
v_mfma_f32_16x16x32_bf16 v[24:27],a[136:139],v[44:47],v[24:27]
buffer_load_dwordx4 v0,s[36:39],s67 offen offset:0 sc1 lds
v_mfma_f32_16x16x32_bf16 v[28:31],a[140:143],v[44:47],v[28:31]
s_xor_b32 s63,s63,65536
ds_read_b128 v[40:43],v2 offset:24576)"
R"(
v_mul_f32 v74,v34,v74
v_mfma_f32_16x16x32_bf16 v[24:27],a[144:147],v[48:51],v[24:27]
s_add_u32 s53,s52,s55
ds_write_b32 v10,v88 offset:0
v_mfma_f32_16x16x32_bf16 v[28:31],a[148:151],v[48:51],v[28:31]
s_cmp_eq_u32 s53,s56
ds_read_b128 v[44:47],v2 offset:25600
v_fma_f32 v75,v98,v75,v97
v_mfma_f32_16x16x32_bf16 v[24:27],a[152:155],v[52:55],v[24:27]
buffer_load_dwordx4 v0,s[36:39],s67 offen offset:1024 sc1 lds
v_mfma_f32_16x16x32_bf16 v[28:31],a[156:159],v[52:55],v[28:31]
s_waitcnt lgkmcnt(3)
ds_read_b128 v[48:51],v2 offset:26624
v_mul_f32 v75,v35,v75
v_mfma_f32_16x16x32_bf16 v[24:27],a[160:163],v[56:59],v[24:27]
s_cselect_b32 s53,s57,s53
ds_read_b128 v[52:55],v2 offset:27648
v_mul_f32 v32,v32,v72
v_mfma_f32_16x16x32_bf16 v[28:31],a[164:167],v[56:59],v[28:31]
s_cmp_eq_u32 s53,s58
v_exp_f32 v74,v74
v_mfma_f32_16x16x32_bf16 v[24:27],a[168:171],v[60:63],v[24:27]
buffer_load_dwordx4 v0,s[36:39],s67 offen offset:2048 sc1 lds
v_mfma_f32_16x16x32_bf16 v[28:31],a[172:175],v[60:63],v[28:31]
s_cselect_b32 s53,s59,s53
ds_read_b128 v[56:59],v2 offset:28672
v_fma_f32 v74,v74,v99,v99
v_mfma_f32_16x16x32_bf16 v[24:27],a[176:179],v[64:67],v[24:27]
s_cmp_ge_u32 s66,s59
ds_read_b128 v[60:63],v2 offset:29696
v_mul_f32 v33,v33,v73
v_mfma_f32_16x16x32_bf16 v[28:31],a[180:183],v[64:67],v[28:31]
v_exp_f32 v75,v75
v_mfma_f32_16x16x32_bf16 v[24:27],a[184:187],v[68:71],v[24:27]
buffer_load_dwordx4 v0,s[36:39],s67 offen offset:3072 sc1 lds
v_mfma_f32_16x16x32_bf16 v[28:31],a[188:191],v[68:71],v[28:31]
s_waitcnt lgkmcnt(2)
ds_read_b128 v[64:67],v2 offset:30720
v_fma_f32 v75,v75,v99,v99
v_mfma_f32_16x16x32_bf16 v[24:27],a[192:195],v[40:43],v[24:27]
s_waitcnt vmcnt(9)
ds_read_b128 v[68:71],v2 offset:31744
v_cvt_pk_bf16_f32 v80,v32,v33
v_mfma_f32_16x16x32_bf16 v[28:31],a[196:199],v[40:43],v[28:31]
s_barrier
v_mfma_f32_16x16x32_bf16 v[24:27],a[200:203],v[44:47],v[24:27]
ds_read_b128 v[40:43],v2 offset:32768
ds_read_b64 v[92:93],v11 offset:0
v_mfma_f32_16x16x32_bf16 v[28:31],a[204:207],v[44:47],v[28:31]
v_fma_f32 v36,-v96,v20,v36
v_fma_f32 v37,-v96,v21,v37
v_fma_f32 v38,-v96,v22,v38
v_fma_f32 v39,-v96,v23,v39
v_mul_f32 v76,v36,v36
v_mul_f32 v77,v37,v37
v_mul_f32 v78,v38,v38
v_mul_f32 v79,v39,v39
v_fma_f32 v76,v98,v76,v97
v_fma_f32 v77,v98,v77,v97
v_fma_f32 v78,v98,v78,v97
v_fma_f32 v79,v98,v79,v97
v_mul_f32 v76,v36,v76
v_mul_f32 v77,v37,v77
v_mul_f32 v78,v38,v78
v_mul_f32 v79,v39,v79
v_exp_f32 v76,v76
v_exp_f32 v77,v77
v_exp_f32 v78,v78
v_exp_f32 v79,v79
v_mfma_f32_16x16x32_bf16 v[24:27],a[208:211],v[48:51],v[24:27]
v_rcp_f32 v74,v74
v_rcp_f32 v75,v75
v_fma_f32 v76,v76,v99,v99
v_fma_f32 v77,v77,v99,v99
v_fma_f32 v78,v78,v99,v99
v_fma_f32 v79,v79,v99,v99
v_rcp_f32 v76,v76
v_rcp_f32 v77,v77
v_rcp_f32 v78,v78
v_rcp_f32 v79,v79
v_mfma_f32_16x16x32_bf16 v[28:31],a[212:215],v[48:51],v[28:31]
ds_read_b128 v[44:47],v2 offset:33792
v_mul_f32 v34,v34,v74
v_mul_f32 v35,v35,v75
v_cvt_pk_bf16_f32 v81,v34,v35
v_mul_f32 v36,v36,v76
v_mul_f32 v37,v37,v77
v_mul_f32 v38,v38,v78
v_mul_f32 v39,v39,v79
v_cvt_pk_bf16_f32 v82,v36,v37
v_cvt_pk_bf16_f32 v83,v38,v39
v_mfma_f32_16x16x32_bf16 v[24:27],a[216:219],v[52:55],v[24:27]
s_waitcnt lgkmcnt(1)
buffer_store_dwordx4 v[80:83],v3,s[36:39],s73 offen offset:0 sc0 sc1
ds_read_b128 v[48:51],v2 offset:34816
v_mfma_f32_16x16x32_bf16 v[28:31],a[220:223],v[52:55],v[28:31]
v_mfma_f32_16x16x32_bf16 v[24:27],a[224:227],v[56:59],v[24:27]
ds_read_b128 v[52:55],v2 offset:35840
v_mfma_f32_16x16x32_bf16 v[28:31],a[228:231],v[56:59],v[28:31]
v_mfma_f32_16x16x32_bf16 v[24:27],a[232:235],v[60:63],v[24:27]
ds_read_b128 v[56:59],v2 offset:36864
v_mfma_f32_16x16x32_bf16 v[28:31],a[236:239],v[60:63],v[28:31]
v_mfma_f32_16x16x32_bf16 v[24:27],a[240:243],v[64:67],v[24:27]
ds_read_b128 v[60:63],v2 offset:37888
v_mfma_f32_16x16x32_bf16 v[28:31],a[244:247],v[64:67],v[28:31]
v_mfma_f32_16x16x32_bf16 v[24:27],a[248:251],v[68:71],v[24:27]
s_waitcnt lgkmcnt(2)
ds_read_b128 v[64:67],v2 offset:38912
v_mfma_f32_16x16x32_bf16 v[28:31],a[252:255],v[68:71],v[28:31]
s_cbranch_scc1 Lg3o_slow1_%=
Lg3o_d1w2_%=:
v_mfma_f32_16x16x32_bf16 v[32:35],a[0:3],v[40:43],0
s_add_u32 m0,s62,16384
ds_read_b128 v[68:71],v2 offset:39936
v_mul_f32 v96,s75,v92
v_mfma_f32_16x16x32_bf16 v[36:39],a[4:7],v[40:43],0
s_mul_i32 s73,s48,7
v_fma_f32 v93,v93,s75,v12
v_fma_f32 v93,-v96,v96,v93
v_mfma_f32_16x16x32_bf16 v[32:35],a[8:11],v[44:47],v[32:35]
s_lshr_b32 s73,s73,1
ds_read_b128 v[40:43],v2 offset:40960
v_max_f32 v93,v93,v12
v_mfma_f32_16x16x32_bf16 v[36:39],a[12:15],v[44:47],v[36:39]
buffer_load_dwordx4 v1,s[36:39],s67 offen offset:0 sc1 lds
v_mfma_f32_16x16x32_bf16 v[32:35],a[16:19],v[48:51],v[32:35]
s_lshr_b32 s72,s67,5
ds_read_b128 v[44:47],v2 offset:41984
v_fma_f32 v24,-v96,v16,v24
v_mfma_f32_16x16x32_bf16 v[36:39],a[20:23],v[48:51],v[36:39]
s_lshr_b32 s71,s62,2
v_rsq_f32 v92,v93
v_mfma_f32_16x16x32_bf16 v[32:35],a[24:27],v[52:55],v[32:35]
s_mov_b64 s[44:45],s[46:47]
ds_read_b128 v[48:51],v2 offset:43008
v_mul_f32 v97,s76,v92
v_mfma_f32_16x16x32_bf16 v[36:39],a[28:31],v[52:55],v[36:39]
s_waitcnt lgkmcnt(3)
buffer_load_dwordx4 v1,s[36:39],s67 offen offset:1024 sc1 lds
v_mfma_f32_16x16x32_bf16 v[32:35],a[32:35],v[56:59],v[32:35]
s_mov_b64 s[46:47],s[48:49]
ds_read_b128 v[52:55],v2 offset:44032
v_mul_f32 v98,v92,v92
v_mfma_f32_16x16x32_bf16 v[36:39],a[36:39],v[56:59],v[36:39]
s_mov_b64 s[48:49],s[50:51]
v_mul_f32 v99,v93,v92
v_mul_f32 v98,s77,v98
v_mfma_f32_16x16x32_bf16 v[32:35],a[40:43],v[60:63],v[32:35]
ds_read_b128 v[56:59],v2 offset:45056
v_mul_f32 v98,v98,v97
v_mfma_f32_16x16x32_bf16 v[36:39],a[44:47],v[60:63],v[36:39]
buffer_load_dwordx4 v1,s[36:39],s67 offen offset:2048 sc1 lds
v_mfma_f32_16x16x32_bf16 v[32:35],a[48:51],v[64:67],v[32:35]
ds_read_b128 v[60:63],v2 offset:46080
v_fma_f32 v25,-v96,v17,v25
v_mfma_f32_16x16x32_bf16 v[36:39],a[52:55],v[64:67],v[36:39]
v_fma_f32 v26,-v96,v18,v26
v_fma_f32 v27,-v96,v19,v27
v_mfma_f32_16x16x32_bf16 v[32:35],a[56:59],v[68:71],v[32:35]
ds_read_b128 v[64:67],v2 offset:47104
v_mul_f32 v72,v24,v24
v_mfma_f32_16x16x32_bf16 v[36:39],a[60:63],v[68:71],v[36:39]
s_waitcnt lgkmcnt(3)
buffer_load_dwordx4 v1,s[36:39],s67 offen offset:3072 sc1 lds
s_add_u32 m0,s71,s88
v_mfma_f32_16x16x32_bf16 v[32:35],a[64:67],v[40:43],v[32:35]
s_xor_b32 s87,s63,65536
ds_read_b128 v[68:71],v2 offset:48128
buffer_load_dword v9,s[36:39],s72 offen offset:0 sc1 lds
s_waitcnt vmcnt(10)
v_mfma_f32_16x16x32_bf16 v[36:39],a[68:71],v[40:43],v[36:39]
s_barrier
v_mfma_f32_16x16x32_bf16 v[32:35],a[72:75],v[44:47],v[32:35]
s_cmp_ge_u32 s67,s59
ds_read_b128 v[40:43],v2 offset:49152
ds_read_b32 v88,v8 offset:8192
v_mfma_f32_16x16x32_bf16 v[36:39],a[76:79],v[44:47],v[36:39]
s_cselect_b32 s86,s50,s51
v_mul_f32 v73,v25,v25
v_mul_f32 v74,v26,v26
v_mfma_f32_16x16x32_bf16 v[32:35],a[80:83],v[48:51],v[32:35]
s_cselect_b32 s87,s87,s64
ds_read_b128 v[44:47],v2 offset:50176
v_mul_f32 v75,v27,v27
v_mfma_f32_16x16x32_bf16 v[36:39],a[84:87],v[48:51],v[36:39]
s_mov_b32 s62,s87
v_fma_f32 v72,v98,v72,v97
v_mul_f32 v72,v24,v72
v_mfma_f32_16x16x32_bf16 v[32:35],a[88:91],v[52:55],v[32:35]
s_mov_b32 m0,s62
ds_read_b128 v[48:51],v2 offset:51200
v_fma_f32 v73,v98,v73,v97
v_mfma_f32_16x16x32_bf16 v[36:39],a[92:95],v[52:55],v[36:39]
s_waitcnt lgkmcnt(4)
v_exp_f32 v72,v72
v_mfma_f32_16x16x32_bf16 v[32:35],a[96:99],v[56:59],v[32:35]
s_nop 0
s_waitcnt lgkmcnt(2)
ds_read_b128 v[52:55],v2 offset:52224
v_mul_f32 v73,v25,v73)"
R"(
v_mfma_f32_16x16x32_bf16 v[36:39],a[100:103],v[56:59],v[36:39]
s_barrier
v_mfma_f32_16x16x32_bf16 v[32:35],a[104:107],v[60:63],v[32:35]
s_mov_b64 s[50:51],s[52:53]
ds_read_b128 v[56:59],v2 offset:53248
ds_read_b32 v13,v7 offset:256
v_mfma_f32_16x16x32_bf16 v[36:39],a[108:111],v[60:63],v[36:39]
v_exp_f32 v73,v73
v_mfma_f32_16x16x32_bf16 v[32:35],a[112:115],v[64:67],v[32:35]
ds_read_b128 v[60:63],v2 offset:54272
v_fma_f32 v72,v72,v99,v99
v_mfma_f32_16x16x32_bf16 v[36:39],a[116:119],v[64:67],v[36:39]
v_rcp_f32 v72,v72
v_mfma_f32_16x16x32_bf16 v[32:35],a[120:123],v[68:71],v[32:35]
s_waitcnt lgkmcnt(1)
ds_read_b128 v[64:67],v2 offset:55296
v_fma_f32 v73,v73,v99,v99
v_mfma_f32_16x16x32_bf16 v[36:39],a[124:127],v[68:71],v[36:39]
v_cmp_eq_u32 vcc,s60,v13
v_rcp_f32 v73,v73
v_add_f32_dpp v88,v88,v88 quad_perm:[1,0,3,2] row_mask:0xf bank_mask:0xf
v_mfma_f32_16x16x32_bf16 v[32:35],a[128:131],v[40:43],v[32:35]
s_cmp_eq_u32 vcc_lo,-1
ds_read_b128 v[68:71],v2 offset:56320
v_add_f32_dpp v88,v88,v88 quad_perm:[2,3,0,1] row_mask:0xf bank_mask:0xf
v_mfma_f32_16x16x32_bf16 v[36:39],a[132:135],v[40:43],v[36:39]
s_cselect_b32 s66,s86,s59
v_add_f32_dpp v88,v88,v88 row_half_mirror row_mask:0xf bank_mask:0xf
v_fma_f32 v74,v98,v74,v97
v_mfma_f32_16x16x32_bf16 v[32:35],a[136:139],v[44:47],v[32:35]
buffer_load_dwordx4 v0,s[36:39],s66 offen offset:0 sc1 lds
v_mfma_f32_16x16x32_bf16 v[36:39],a[140:143],v[44:47],v[36:39]
s_xor_b32 s64,s64,65536
ds_read_b128 v[40:43],v2 offset:57344
v_mul_f32 v74,v26,v74
v_mfma_f32_16x16x32_bf16 v[32:35],a[144:147],v[48:51],v[32:35]
s_add_u32 s74,s53,s55
ds_write_b32 v10,v88 offset:128
v_mfma_f32_16x16x32_bf16 v[36:39],a[148:151],v[48:51],v[36:39]
s_cmp_eq_u32 s74,s56
ds_read_b128 v[44:47],v2 offset:58368
v_fma_f32 v75,v98,v75,v97
v_mfma_f32_16x16x32_bf16 v[32:35],a[152:155],v[52:55],v[32:35]
buffer_load_dwordx4 v0,s[36:39],s66 offen offset:1024 sc1 lds
v_mfma_f32_16x16x32_bf16 v[36:39],a[156:159],v[52:55],v[36:39]
s_waitcnt lgkmcnt(3)
ds_read_b128 v[48:51],v2 offset:59392
v_mul_f32 v75,v27,v75
v_mfma_f32_16x16x32_bf16 v[32:35],a[160:163],v[56:59],v[32:35]
s_cselect_b32 s74,s57,s74
ds_read_b128 v[52:55],v2 offset:60416
v_mul_f32 v24,v24,v72
v_mfma_f32_16x16x32_bf16 v[36:39],a[164:167],v[56:59],v[36:39]
s_cmp_eq_u32 s74,s58
v_exp_f32 v74,v74
v_mfma_f32_16x16x32_bf16 v[32:35],a[168:171],v[60:63],v[32:35]
buffer_load_dwordx4 v0,s[36:39],s66 offen offset:2048 sc1 lds
v_mfma_f32_16x16x32_bf16 v[36:39],a[172:175],v[60:63],v[36:39]
s_cselect_b32 s74,s59,s74
ds_read_b128 v[56:59],v2 offset:61440
v_fma_f32 v74,v74,v99,v99
v_mfma_f32_16x16x32_bf16 v[32:35],a[176:179],v[64:67],v[32:35]
s_mov_b32 s52,s74
ds_read_b128 v[60:63],v2 offset:62464
v_mul_f32 v25,v25,v73
v_mfma_f32_16x16x32_bf16 v[36:39],a[180:183],v[64:67],v[36:39]
s_cmp_lt_u32 s67,s59
v_exp_f32 v75,v75
v_mfma_f32_16x16x32_bf16 v[32:35],a[184:187],v[68:71],v[32:35]
buffer_load_dwordx4 v0,s[36:39],s66 offen offset:3072 sc1 lds
v_mfma_f32_16x16x32_bf16 v[36:39],a[188:191],v[68:71],v[36:39]
s_waitcnt lgkmcnt(2)
ds_read_b128 v[64:67],v2 offset:63488
v_fma_f32 v75,v75,v99,v99
v_mfma_f32_16x16x32_bf16 v[32:35],a[192:195],v[40:43],v[32:35]
s_waitcnt vmcnt(9)
ds_read_b128 v[68:71],v2 offset:64512
v_xor_b32 v2,65536,v2
v_mfma_f32_16x16x32_bf16 v[36:39],a[196:199],v[40:43],v[36:39]
s_barrier
v_mfma_f32_16x16x32_bf16 v[32:35],a[200:203],v[44:47],v[32:35]
ds_read_b128 v[40:43],v2 offset:0
ds_read_b64 v[92:93],v11 offset:128
v_mfma_f32_16x16x32_bf16 v[36:39],a[204:207],v[44:47],v[36:39]
v_fma_f32 v28,-v96,v20,v28
v_fma_f32 v29,-v96,v21,v29
v_fma_f32 v30,-v96,v22,v30
v_fma_f32 v31,-v96,v23,v31
v_mul_f32 v76,v28,v28
v_mul_f32 v77,v29,v29
v_mul_f32 v78,v30,v30
v_mul_f32 v79,v31,v31
v_fma_f32 v76,v98,v76,v97
v_fma_f32 v77,v98,v77,v97
v_fma_f32 v78,v98,v78,v97
v_fma_f32 v79,v98,v79,v97
v_mul_f32 v76,v28,v76
v_mul_f32 v77,v29,v77
v_mul_f32 v78,v30,v78
v_mul_f32 v79,v31,v79
v_exp_f32 v76,v76
v_exp_f32 v77,v77
v_exp_f32 v78,v78
v_exp_f32 v79,v79
v_mfma_f32_16x16x32_bf16 v[32:35],a[208:211],v[48:51],v[32:35]
v_rcp_f32 v74,v74
v_rcp_f32 v75,v75
v_fma_f32 v76,v76,v99,v99
v_fma_f32 v77,v77,v99,v99
v_fma_f32 v78,v78,v99,v99
v_fma_f32 v79,v79,v99,v99
v_rcp_f32 v76,v76
v_rcp_f32 v77,v77
v_rcp_f32 v78,v78
v_rcp_f32 v79,v79
v_mfma_f32_16x16x32_bf16 v[36:39],a[212:215],v[48:51],v[36:39]
ds_read_b128 v[44:47],v2 offset:1024
v_mul_f32 v26,v26,v74
v_mul_f32 v27,v27,v75
v_cvt_pk_bf16_f32 v80,v24,v25
v_cvt_pk_bf16_f32 v81,v26,v27
v_mul_f32 v28,v28,v76
v_mul_f32 v29,v29,v77
v_mul_f32 v30,v30,v78
v_mul_f32 v31,v31,v79
v_cvt_pk_bf16_f32 v82,v28,v29
v_cvt_pk_bf16_f32 v83,v30,v31
v_mfma_f32_16x16x32_bf16 v[32:35],a[216:219],v[52:55],v[32:35]
s_waitcnt lgkmcnt(1)
buffer_store_dwordx4 v[80:83],v3,s[36:39],s73 offen offset:0 sc0 sc1
ds_read_b128 v[48:51],v2 offset:2048
v_xor_b32 v8,16384,v8
v_mfma_f32_16x16x32_bf16 v[36:39],a[220:223],v[52:55],v[36:39]
v_mfma_f32_16x16x32_bf16 v[32:35],a[224:227],v[56:59],v[32:35]
ds_read_b128 v[52:55],v2 offset:3072
v_mfma_f32_16x16x32_bf16 v[36:39],a[228:231],v[56:59],v[36:39]
v_mfma_f32_16x16x32_bf16 v[32:35],a[232:235],v[60:63],v[32:35]
ds_read_b128 v[56:59],v2 offset:4096
v_mfma_f32_16x16x32_bf16 v[36:39],a[236:239],v[60:63],v[36:39]
v_mfma_f32_16x16x32_bf16 v[32:35],a[240:243],v[64:67],v[32:35]
ds_read_b128 v[60:63],v2 offset:5120
v_mfma_f32_16x16x32_bf16 v[36:39],a[244:247],v[64:67],v[36:39]
v_mfma_f32_16x16x32_bf16 v[32:35],a[248:251],v[68:71],v[32:35]
s_waitcnt lgkmcnt(2)
ds_read_b128 v[64:67],v2 offset:6144
v_mfma_f32_16x16x32_bf16 v[36:39],a[252:255],v[68:71],v[36:39]
s_cbranch_scc1 Lg3o_d0w2_%=
s_branch Lg3o_slow0_%=
Lg3o_d0w3_%=:
v_mfma_f32_16x16x32_bf16 v[24:27],a[0:3],v[40:43],0
s_add_u32 m0,s62,16384
ds_read_b128 v[68:71],v2 offset:7168
v_mul_f32 v96,s75,v92
v_mfma_f32_16x16x32_bf16 v[28:31],a[4:7],v[40:43],0
s_mul_i32 s73,s47,7
v_fma_f32 v93,v93,s75,v12
v_fma_f32 v93,-v96,v96,v93
v_mfma_f32_16x16x32_bf16 v[24:27],a[8:11],v[44:47],v[24:27]
s_lshr_b32 s73,s73,1
ds_read_b128 v[40:43],v2 offset:8192
v_max_f32 v93,v93,v12
v_mfma_f32_16x16x32_bf16 v[28:31],a[12:15],v[44:47],v[28:31]
s_lshr_b32 s72,s66,5
v_rsq_f32 v92,v93
v_mfma_f32_16x16x32_bf16 v[24:27],a[16:19],v[48:51],v[24:27]
buffer_load_dwordx4 v1,s[36:39],s66 offen offset:0 sc1 lds
v_mfma_f32_16x16x32_bf16 v[28:31],a[20:23],v[48:51],v[28:31]
s_lshr_b32 s71,s62,2
ds_read_b128 v[44:47],v2 offset:9216
v_mul_f32 v97,s76,v92
v_mfma_f32_16x16x32_bf16 v[24:27],a[24:27],v[52:55],v[24:27]
s_mul_i32 s70,s46,5
ds_read_b128 v[48:51],v2 offset:10240
v_mul_f32 v98,v92,v92
v_mfma_f32_16x16x32_bf16 v[28:31],a[28:31],v[52:55],v[28:31]
s_waitcnt lgkmcnt(3)
v_mul_f32 v99,v93,v92
v_mul_f32 v98,s77,v98
v_mfma_f32_16x16x32_bf16 v[24:27],a[32:35],v[56:59],v[24:27]
buffer_load_dwordx4 v1,s[36:39],s66 offen offset:1024 sc1 lds
v_mfma_f32_16x16x32_bf16 v[28:31],a[36:39],v[56:59],v[28:31]
ds_read_b128 v[52:55],v2 offset:11264
v_mul_f32 v98,v98,v97
v_mfma_f32_16x16x32_bf16 v[24:27],a[40:43],v[60:63],v[24:27]
ds_read_b128 v[56:59],v2 offset:12288
v_fma_f32 v32,-v96,v16,v32
v_mfma_f32_16x16x32_bf16 v[28:31],a[44:47],v[60:63],v[28:31]
v_fma_f32 v33,-v96,v17,v33
v_fma_f32 v34,-v96,v18,v34
v_mfma_f32_16x16x32_bf16 v[24:27],a[48:51],v[64:67],v[24:27]
buffer_load_dwordx4 v1,s[36:39],s66 offen offset:2048 sc1 lds
v_mfma_f32_16x16x32_bf16 v[28:31],a[52:55],v[64:67],v[28:31]
ds_read_b128 v[60:63],v2 offset:13312
v_fma_f32 v35,-v96,v19,v35
v_mfma_f32_16x16x32_bf16 v[24:27],a[56:59],v[68:71],v[24:27]
ds_read_b128 v[64:67],v2 offset:14336
v_mul_f32 v72,v32,v32
v_mfma_f32_16x16x32_bf16 v[28:31],a[60:63],v[68:71],v[28:31]
s_waitcnt lgkmcnt(3)
v_mul_f32 v73,v33,v33)"
R"(
v_mul_f32 v74,v34,v34
v_mfma_f32_16x16x32_bf16 v[24:27],a[64:67],v[40:43],v[24:27]
s_waitcnt vmcnt(9)
ds_read_b128 v[68:71],v2 offset:15360
v_mul_f32 v75,v35,v35
v_mfma_f32_16x16x32_bf16 v[28:31],a[68:71],v[40:43],v[28:31]
s_barrier
v_mfma_f32_16x16x32_bf16 v[24:27],a[72:75],v[44:47],v[24:27]
buffer_load_dwordx4 v1,s[36:39],s66 offen offset:3072 sc1 lds
v_mfma_f32_16x16x32_bf16 v[28:31],a[76:79],v[44:47],v[28:31]
s_add_u32 m0,s71,s88
ds_read_b128 v[40:43],v2 offset:16384
ds_read_b32 v88,v8 offset:0
v_mfma_f32_16x16x32_bf16 v[24:27],a[80:83],v[48:51],v[24:27]
buffer_load_dword v9,s[36:39],s72 offen offset:0 sc1 lds
v_mfma_f32_16x16x32_bf16 v[28:31],a[84:87],v[48:51],v[28:31]
s_xor_b32 s87,s64,65536
ds_read_b128 v[44:47],v2 offset:17408
v_fma_f32 v72,v98,v72,v97
v_mfma_f32_16x16x32_bf16 v[24:27],a[88:91],v[52:55],v[24:27]
s_cmp_ge_u32 s66,s59
ds_read_b128 v[48:51],v2 offset:18432
v_mul_f32 v72,v32,v72
v_mfma_f32_16x16x32_bf16 v[28:31],a[92:95],v[52:55],v[28:31]
s_waitcnt lgkmcnt(4)
v_exp_f32 v72,v72
v_mfma_f32_16x16x32_bf16 v[24:27],a[96:99],v[56:59],v[24:27]
s_nop 0
ds_read_b128 v[52:55],v2 offset:19456
v_fma_f32 v72,v72,v99,v99
v_mfma_f32_16x16x32_bf16 v[28:31],a[100:103],v[56:59],v[28:31]
s_barrier
v_mfma_f32_16x16x32_bf16 v[24:27],a[104:107],v[60:63],v[24:27]
s_waitcnt lgkmcnt(3)
ds_read_b128 v[56:59],v2 offset:20480
ds_read_b32 v13,v7 offset:0
v_mfma_f32_16x16x32_bf16 v[28:31],a[108:111],v[60:63],v[28:31]
s_cselect_b32 s86,s49,s50
v_rcp_f32 v72,v72
v_mfma_f32_16x16x32_bf16 v[24:27],a[112:115],v[64:67],v[24:27]
s_cselect_b32 s87,s87,s63
ds_read_b128 v[60:63],v2 offset:21504
v_add_f32_dpp v88,v88,v88 quad_perm:[1,0,3,2] row_mask:0xf bank_mask:0xf
v_mfma_f32_16x16x32_bf16 v[28:31],a[116:119],v[64:67],v[28:31]
s_mov_b32 s62,s87
v_add_f32_dpp v88,v88,v88 quad_perm:[2,3,0,1] row_mask:0xf bank_mask:0xf
v_fma_f32 v73,v98,v73,v97
v_mfma_f32_16x16x32_bf16 v[24:27],a[120:123],v[68:71],v[24:27]
s_waitcnt lgkmcnt(1)
ds_read_b128 v[64:67],v2 offset:22528
v_mul_f32 v73,v33,v73
v_mfma_f32_16x16x32_bf16 v[28:31],a[124:127],v[68:71],v[28:31]
v_cmp_eq_u32 vcc,s60,v13
v_exp_f32 v73,v73
v_add_f32_dpp v88,v88,v88 row_half_mirror row_mask:0xf bank_mask:0xf
v_mfma_f32_16x16x32_bf16 v[24:27],a[128:131],v[40:43],v[24:27]
s_cmp_eq_u32 vcc_lo,-1
ds_write_b32 v10,v88 offset:0
v_mfma_f32_16x16x32_bf16 v[28:31],a[132:135],v[40:43],v[28:31]
s_cselect_b32 s67,s86,s59
ds_read_b128 v[68:71],v2 offset:23552
v_fma_f32 v73,v73,v99,v99
v_mfma_f32_16x16x32_bf16 v[24:27],a[136:139],v[44:47],v[24:27]
s_mov_b32 m0,s62
ds_read_b128 v[40:43],v2 offset:24576
v_fma_f32 v74,v98,v74,v97
v_mfma_f32_16x16x32_bf16 v[28:31],a[140:143],v[44:47],v[28:31]
buffer_load_dwordx4 v0,s[36:39],s67 offen offset:0 sc1 lds
v_mfma_f32_16x16x32_bf16 v[24:27],a[144:147],v[48:51],v[24:27]
s_xor_b32 s63,s63,65536
ds_read_b128 v[44:47],v2 offset:25600
v_mul_f32 v74,v34,v74
v_mfma_f32_16x16x32_bf16 v[28:31],a[148:151],v[48:51],v[28:31]
s_add_u32 s53,s52,s55
v_exp_f32 v74,v74
v_mfma_f32_16x16x32_bf16 v[24:27],a[152:155],v[52:55],v[24:27]
s_cmp_eq_u32 s53,s56
ds_read_b128 v[48:51],v2 offset:26624
v_fma_f32 v74,v74,v99,v99
v_mfma_f32_16x16x32_bf16 v[28:31],a[156:159],v[52:55],v[28:31]
s_waitcnt lgkmcnt(3)
buffer_load_dwordx4 v0,s[36:39],s67 offen offset:1024 sc1 lds
v_mfma_f32_16x16x32_bf16 v[24:27],a[160:163],v[56:59],v[24:27]
s_cselect_b32 s53,s57,s53
ds_read_b128 v[52:55],v2 offset:27648
v_fma_f32 v75,v98,v75,v97
v_mfma_f32_16x16x32_bf16 v[28:31],a[164:167],v[56:59],v[28:31]
s_cmp_eq_u32 s53,s58
v_rcp_f32 v73,v73
v_mfma_f32_16x16x32_bf16 v[24:27],a[168:171],v[60:63],v[24:27]
s_cselect_b32 s53,s59,s53
ds_read_b128 v[56:59],v2 offset:28672
v_mul_f32 v75,v35,v75
v_mfma_f32_16x16x32_bf16 v[28:31],a[172:175],v[60:63],v[28:31]
buffer_load_dwordx4 v0,s[36:39],s67 offen offset:2048 sc1 lds
v_mfma_f32_16x16x32_bf16 v[24:27],a[176:179],v[64:67],v[24:27]
s_lshr_b32 s70,s70,6
ds_read_b128 v[60:63],v2 offset:29696
v_mul_f32 v32,v32,v72
v_mfma_f32_16x16x32_bf16 v[28:31],a[180:183],v[64:67],v[28:31]
s_cmp_ge_u32 s66,s59
v_exp_f32 v75,v75
v_mfma_f32_16x16x32_bf16 v[24:27],a[184:187],v[68:71],v[24:27]
ds_read_b128 v[64:67],v2 offset:30720
v_fma_f32 v75,v75,v99,v99
v_mfma_f32_16x16x32_bf16 v[28:31],a[188:191],v[68:71],v[28:31]
s_waitcnt lgkmcnt(3)
buffer_load_dwordx4 v0,s[36:39],s67 offen offset:3072 sc1 lds
v_mfma_f32_16x16x32_bf16 v[24:27],a[192:195],v[40:43],v[24:27]
s_waitcnt vmcnt(9)
ds_read_b128 v[68:71],v2 offset:31744
v_mul_f32 v33,v33,v73
v_mfma_f32_16x16x32_bf16 v[28:31],a[196:199],v[40:43],v[28:31]
s_barrier
v_mfma_f32_16x16x32_bf16 v[24:27],a[200:203],v[44:47],v[24:27]
buffer_store_dword v5,v4,s[36:39],s70 offen offset:0 sc0 sc1
ds_read_b128 v[40:43],v2 offset:32768
ds_read_b64 v[92:93],v11 offset:0
v_cvt_pk_bf16_f32 v80,v32,v33
v_mfma_f32_16x16x32_bf16 v[28:31],a[204:207],v[44:47],v[28:31]
v_rcp_f32 v74,v74
v_mfma_f32_16x16x32_bf16 v[24:27],a[208:211],v[48:51],v[24:27]
v_fma_f32 v36,-v96,v20,v36
v_fma_f32 v37,-v96,v21,v37
v_fma_f32 v38,-v96,v22,v38
v_fma_f32 v39,-v96,v23,v39
v_mul_f32 v76,v36,v36
v_mul_f32 v77,v37,v37
v_mul_f32 v78,v38,v38
v_mul_f32 v79,v39,v39
v_fma_f32 v76,v98,v76,v97
v_fma_f32 v77,v98,v77,v97
v_fma_f32 v78,v98,v78,v97
v_fma_f32 v79,v98,v79,v97
v_mul_f32 v76,v36,v76
v_mul_f32 v77,v37,v77
v_mul_f32 v78,v38,v78
v_mul_f32 v79,v39,v79
v_exp_f32 v76,v76
v_exp_f32 v77,v77
v_exp_f32 v78,v78
v_exp_f32 v79,v79
v_mfma_f32_16x16x32_bf16 v[28:31],a[212:215],v[48:51],v[28:31]
ds_read_b128 v[44:47],v2 offset:33792
v_rcp_f32 v75,v75
v_fma_f32 v76,v76,v99,v99
v_fma_f32 v77,v77,v99,v99
v_fma_f32 v78,v78,v99,v99
v_fma_f32 v79,v79,v99,v99
v_rcp_f32 v76,v76
v_rcp_f32 v77,v77
v_rcp_f32 v78,v78
v_rcp_f32 v79,v79
v_mfma_f32_16x16x32_bf16 v[24:27],a[216:219],v[52:55],v[24:27]
s_waitcnt lgkmcnt(1)
v_mul_f32 v34,v34,v74
v_mul_f32 v35,v35,v75
v_cvt_pk_bf16_f32 v81,v34,v35
v_mul_f32 v36,v36,v76
v_mul_f32 v37,v37,v77
v_mul_f32 v38,v38,v78
v_mul_f32 v39,v39,v79
v_cvt_pk_bf16_f32 v82,v36,v37
v_cvt_pk_bf16_f32 v83,v38,v39
v_mfma_f32_16x16x32_bf16 v[28:31],a[220:223],v[52:55],v[28:31]
ds_read_b128 v[48:51],v2 offset:34816
buffer_store_dwordx4 v[80:83],v3,s[36:39],s73 offen offset:0 sc0 sc1
v_mfma_f32_16x16x32_bf16 v[24:27],a[224:227],v[56:59],v[24:27]
ds_read_b128 v[52:55],v2 offset:35840
v_mfma_f32_16x16x32_bf16 v[28:31],a[228:231],v[56:59],v[28:31]
v_mfma_f32_16x16x32_bf16 v[24:27],a[232:235],v[60:63],v[24:27]
ds_read_b128 v[56:59],v2 offset:36864
v_mfma_f32_16x16x32_bf16 v[28:31],a[236:239],v[60:63],v[28:31]
v_mfma_f32_16x16x32_bf16 v[24:27],a[240:243],v[64:67],v[24:27]
ds_read_b128 v[60:63],v2 offset:37888
v_mfma_f32_16x16x32_bf16 v[28:31],a[244:247],v[64:67],v[28:31]
v_mfma_f32_16x16x32_bf16 v[24:27],a[248:251],v[68:71],v[24:27]
s_waitcnt lgkmcnt(2)
ds_read_b128 v[64:67],v2 offset:38912
v_mfma_f32_16x16x32_bf16 v[28:31],a[252:255],v[68:71],v[28:31]
s_cbranch_scc1 Lg3o_slow1_%=
Lg3o_d1w3_%=:
v_mfma_f32_16x16x32_bf16 v[32:35],a[0:3],v[40:43],0
s_add_u32 m0,s62,16384
ds_read_b128 v[68:71],v2 offset:39936
v_mul_f32 v96,s75,v92
v_mfma_f32_16x16x32_bf16 v[36:39],a[4:7],v[40:43],0
s_mul_i32 s73,s48,7
v_fma_f32 v93,v93,s75,v12
v_fma_f32 v93,-v96,v96,v93
v_mfma_f32_16x16x32_bf16 v[32:35],a[8:11],v[44:47],v[32:35]
s_lshr_b32 s73,s73,1
ds_read_b128 v[40:43],v2 offset:40960
v_max_f32 v93,v93,v12
v_mfma_f32_16x16x32_bf16 v[36:39],a[12:15],v[44:47],v[36:39]
s_lshr_b32 s72,s67,5
v_rsq_f32 v92,v93
v_mfma_f32_16x16x32_bf16 v[32:35],a[16:19],v[48:51],v[32:35]
buffer_load_dwordx4 v1,s[36:39],s67 offen offset:0 sc1 lds
v_mfma_f32_16x16x32_bf16 v[36:39],a[20:23],v[48:51],v[36:39])"
R"(
s_lshr_b32 s71,s62,2
ds_read_b128 v[44:47],v2 offset:41984
v_mul_f32 v97,s76,v92
v_mfma_f32_16x16x32_bf16 v[32:35],a[24:27],v[52:55],v[32:35]
s_mul_i32 s70,s47,5
ds_read_b128 v[48:51],v2 offset:43008
v_mul_f32 v98,v92,v92
v_mfma_f32_16x16x32_bf16 v[36:39],a[28:31],v[52:55],v[36:39]
s_waitcnt lgkmcnt(3)
v_mul_f32 v99,v93,v92
v_mul_f32 v98,s77,v98
v_mfma_f32_16x16x32_bf16 v[32:35],a[32:35],v[56:59],v[32:35]
buffer_load_dwordx4 v1,s[36:39],s67 offen offset:1024 sc1 lds
v_mfma_f32_16x16x32_bf16 v[36:39],a[36:39],v[56:59],v[36:39]
s_mov_b64 s[44:45],s[46:47]
ds_read_b128 v[52:55],v2 offset:44032
v_mul_f32 v98,v98,v97
v_mfma_f32_16x16x32_bf16 v[32:35],a[40:43],v[60:63],v[32:35]
s_mov_b64 s[46:47],s[48:49]
ds_read_b128 v[56:59],v2 offset:45056
v_fma_f32 v24,-v96,v16,v24
v_mfma_f32_16x16x32_bf16 v[36:39],a[44:47],v[60:63],v[36:39]
s_mov_b64 s[48:49],s[50:51]
v_fma_f32 v25,-v96,v17,v25
v_fma_f32 v26,-v96,v18,v26
v_mfma_f32_16x16x32_bf16 v[32:35],a[48:51],v[64:67],v[32:35]
buffer_load_dwordx4 v1,s[36:39],s67 offen offset:2048 sc1 lds
v_mfma_f32_16x16x32_bf16 v[36:39],a[52:55],v[64:67],v[36:39]
ds_read_b128 v[60:63],v2 offset:46080
v_fma_f32 v27,-v96,v19,v27
v_mfma_f32_16x16x32_bf16 v[32:35],a[56:59],v[68:71],v[32:35]
ds_read_b128 v[64:67],v2 offset:47104
v_mul_f32 v72,v24,v24
v_mfma_f32_16x16x32_bf16 v[36:39],a[60:63],v[68:71],v[36:39]
s_waitcnt lgkmcnt(3)
v_mul_f32 v73,v25,v25
v_mul_f32 v74,v26,v26
v_mfma_f32_16x16x32_bf16 v[32:35],a[64:67],v[40:43],v[32:35]
s_waitcnt vmcnt(9)
ds_read_b128 v[68:71],v2 offset:48128
v_mul_f32 v75,v27,v27
v_mfma_f32_16x16x32_bf16 v[36:39],a[68:71],v[40:43],v[36:39]
s_barrier
v_mfma_f32_16x16x32_bf16 v[32:35],a[72:75],v[44:47],v[32:35]
buffer_load_dwordx4 v1,s[36:39],s67 offen offset:3072 sc1 lds
v_mfma_f32_16x16x32_bf16 v[36:39],a[76:79],v[44:47],v[36:39]
s_add_u32 m0,s71,s88
ds_read_b128 v[40:43],v2 offset:49152
ds_read_b32 v88,v8 offset:8192
v_mfma_f32_16x16x32_bf16 v[32:35],a[80:83],v[48:51],v[32:35]
buffer_load_dword v9,s[36:39],s72 offen offset:0 sc1 lds
v_mfma_f32_16x16x32_bf16 v[36:39],a[84:87],v[48:51],v[36:39]
s_xor_b32 s87,s63,65536
ds_read_b128 v[44:47],v2 offset:50176
v_fma_f32 v72,v98,v72,v97
v_mfma_f32_16x16x32_bf16 v[32:35],a[88:91],v[52:55],v[32:35]
s_cmp_ge_u32 s67,s59
ds_read_b128 v[48:51],v2 offset:51200
v_mul_f32 v72,v24,v72
v_mfma_f32_16x16x32_bf16 v[36:39],a[92:95],v[52:55],v[36:39]
s_waitcnt lgkmcnt(4)
v_exp_f32 v72,v72
v_mfma_f32_16x16x32_bf16 v[32:35],a[96:99],v[56:59],v[32:35]
s_nop 0
ds_read_b128 v[52:55],v2 offset:52224
v_fma_f32 v72,v72,v99,v99
v_mfma_f32_16x16x32_bf16 v[36:39],a[100:103],v[56:59],v[36:39]
s_barrier
v_mfma_f32_16x16x32_bf16 v[32:35],a[104:107],v[60:63],v[32:35]
s_waitcnt lgkmcnt(3)
ds_read_b128 v[56:59],v2 offset:53248
ds_read_b32 v13,v7 offset:256
v_mfma_f32_16x16x32_bf16 v[36:39],a[108:111],v[60:63],v[36:39]
s_cselect_b32 s86,s50,s51
v_rcp_f32 v72,v72
v_mfma_f32_16x16x32_bf16 v[32:35],a[112:115],v[64:67],v[32:35]
s_cselect_b32 s87,s87,s64
ds_read_b128 v[60:63],v2 offset:54272
v_add_f32_dpp v88,v88,v88 quad_perm:[1,0,3,2] row_mask:0xf bank_mask:0xf
v_mfma_f32_16x16x32_bf16 v[36:39],a[116:119],v[64:67],v[36:39]
s_mov_b32 s62,s87
v_add_f32_dpp v88,v88,v88 quad_perm:[2,3,0,1] row_mask:0xf bank_mask:0xf
v_fma_f32 v73,v98,v73,v97
v_mfma_f32_16x16x32_bf16 v[32:35],a[120:123],v[68:71],v[32:35]
s_waitcnt lgkmcnt(1)
ds_read_b128 v[64:67],v2 offset:55296
v_mul_f32 v73,v25,v73
v_mfma_f32_16x16x32_bf16 v[36:39],a[124:127],v[68:71],v[36:39]
v_cmp_eq_u32 vcc,s60,v13
v_exp_f32 v73,v73
v_add_f32_dpp v88,v88,v88 row_half_mirror row_mask:0xf bank_mask:0xf
v_mfma_f32_16x16x32_bf16 v[32:35],a[128:131],v[40:43],v[32:35]
s_cmp_eq_u32 vcc_lo,-1
ds_write_b32 v10,v88 offset:128
v_mfma_f32_16x16x32_bf16 v[36:39],a[132:135],v[40:43],v[36:39]
s_cselect_b32 s66,s86,s59
ds_read_b128 v[68:71],v2 offset:56320
v_fma_f32 v73,v73,v99,v99
v_mfma_f32_16x16x32_bf16 v[32:35],a[136:139],v[44:47],v[32:35]
s_mov_b32 m0,s62
ds_read_b128 v[40:43],v2 offset:57344
v_fma_f32 v74,v98,v74,v97
v_mfma_f32_16x16x32_bf16 v[36:39],a[140:143],v[44:47],v[36:39]
buffer_load_dwordx4 v0,s[36:39],s66 offen offset:0 sc1 lds
v_mfma_f32_16x16x32_bf16 v[32:35],a[144:147],v[48:51],v[32:35]
s_xor_b32 s64,s64,65536
ds_read_b128 v[44:47],v2 offset:58368
v_mul_f32 v74,v26,v74
v_mfma_f32_16x16x32_bf16 v[36:39],a[148:151],v[48:51],v[36:39]
s_add_u32 s74,s53,s55
v_exp_f32 v74,v74
v_mfma_f32_16x16x32_bf16 v[32:35],a[152:155],v[52:55],v[32:35]
s_cmp_eq_u32 s74,s56
ds_read_b128 v[48:51],v2 offset:59392
v_fma_f32 v74,v74,v99,v99
v_mfma_f32_16x16x32_bf16 v[36:39],a[156:159],v[52:55],v[36:39]
s_waitcnt lgkmcnt(3)
buffer_load_dwordx4 v0,s[36:39],s66 offen offset:1024 sc1 lds
v_mfma_f32_16x16x32_bf16 v[32:35],a[160:163],v[56:59],v[32:35]
s_cselect_b32 s74,s57,s74
ds_read_b128 v[52:55],v2 offset:60416
v_fma_f32 v75,v98,v75,v97
v_mfma_f32_16x16x32_bf16 v[36:39],a[164:167],v[56:59],v[36:39]
s_cmp_eq_u32 s74,s58
v_rcp_f32 v73,v73
v_mfma_f32_16x16x32_bf16 v[32:35],a[168:171],v[60:63],v[32:35]
s_cselect_b32 s74,s59,s74
ds_read_b128 v[56:59],v2 offset:61440
v_mul_f32 v75,v27,v75
v_mfma_f32_16x16x32_bf16 v[36:39],a[172:175],v[60:63],v[36:39]
buffer_load_dwordx4 v0,s[36:39],s66 offen offset:2048 sc1 lds
v_mfma_f32_16x16x32_bf16 v[32:35],a[176:179],v[64:67],v[32:35]
s_lshr_b32 s70,s70,6
ds_read_b128 v[60:63],v2 offset:62464
v_mul_f32 v24,v24,v72
v_mfma_f32_16x16x32_bf16 v[36:39],a[180:183],v[64:67],v[36:39]
s_mov_b64 s[50:51],s[52:53]
v_exp_f32 v75,v75
v_mfma_f32_16x16x32_bf16 v[32:35],a[184:187],v[68:71],v[32:35]
s_mov_b32 s52,s74
ds_read_b128 v[64:67],v2 offset:63488
v_fma_f32 v75,v75,v99,v99
v_mfma_f32_16x16x32_bf16 v[36:39],a[188:191],v[68:71],v[36:39]
s_waitcnt lgkmcnt(3)
buffer_load_dwordx4 v0,s[36:39],s66 offen offset:3072 sc1 lds
v_mfma_f32_16x16x32_bf16 v[32:35],a[192:195],v[40:43],v[32:35]
s_waitcnt vmcnt(9)
ds_read_b128 v[68:71],v2 offset:64512
v_xor_b32 v2,65536,v2
v_mfma_f32_16x16x32_bf16 v[36:39],a[196:199],v[40:43],v[36:39]
s_barrier
v_mfma_f32_16x16x32_bf16 v[32:35],a[200:203],v[44:47],v[32:35]
buffer_store_dword v5,v4,s[36:39],s70 offen offset:0 sc0 sc1
ds_read_b128 v[40:43],v2 offset:0
ds_read_b64 v[92:93],v11 offset:128
v_mul_f32 v25,v25,v73
v_mfma_f32_16x16x32_bf16 v[36:39],a[204:207],v[44:47],v[36:39]
s_cmp_lt_u32 s67,s59
v_rcp_f32 v74,v74
v_mfma_f32_16x16x32_bf16 v[32:35],a[208:211],v[48:51],v[32:35]
v_fma_f32 v28,-v96,v20,v28
v_fma_f32 v29,-v96,v21,v29
v_fma_f32 v30,-v96,v22,v30
v_fma_f32 v31,-v96,v23,v31
v_mul_f32 v76,v28,v28
v_mul_f32 v77,v29,v29
v_mul_f32 v78,v30,v30
v_mul_f32 v79,v31,v31
v_fma_f32 v76,v98,v76,v97
v_fma_f32 v77,v98,v77,v97
v_fma_f32 v78,v98,v78,v97
v_fma_f32 v79,v98,v79,v97
v_mul_f32 v76,v28,v76
v_mul_f32 v77,v29,v77
v_mul_f32 v78,v30,v78
v_mul_f32 v79,v31,v79
v_exp_f32 v76,v76
v_exp_f32 v77,v77
v_exp_f32 v78,v78
v_exp_f32 v79,v79
v_mfma_f32_16x16x32_bf16 v[36:39],a[212:215],v[48:51],v[36:39]
ds_read_b128 v[44:47],v2 offset:1024
v_rcp_f32 v75,v75
v_fma_f32 v76,v76,v99,v99
v_fma_f32 v77,v77,v99,v99
v_fma_f32 v78,v78,v99,v99
v_fma_f32 v79,v79,v99,v99
v_rcp_f32 v76,v76
v_rcp_f32 v77,v77
v_rcp_f32 v78,v78
v_rcp_f32 v79,v79
v_mfma_f32_16x16x32_bf16 v[32:35],a[216:219],v[52:55],v[32:35]
s_waitcnt lgkmcnt(1)
v_mul_f32 v26,v26,v74
v_mul_f32 v27,v27,v75
v_cvt_pk_bf16_f32 v80,v24,v25
v_cvt_pk_bf16_f32 v81,v26,v27
v_mul_f32 v28,v28,v76
v_mul_f32 v29,v29,v77
v_mul_f32 v30,v30,v78)"
R"(
v_mul_f32 v31,v31,v79
v_cvt_pk_bf16_f32 v82,v28,v29
v_cvt_pk_bf16_f32 v83,v30,v31
v_mfma_f32_16x16x32_bf16 v[36:39],a[220:223],v[52:55],v[36:39]
ds_read_b128 v[48:51],v2 offset:2048
buffer_store_dwordx4 v[80:83],v3,s[36:39],s73 offen offset:0 sc0 sc1
v_xor_b32 v8,16384,v8
v_mfma_f32_16x16x32_bf16 v[32:35],a[224:227],v[56:59],v[32:35]
ds_read_b128 v[52:55],v2 offset:3072
v_mfma_f32_16x16x32_bf16 v[36:39],a[228:231],v[56:59],v[36:39]
v_mfma_f32_16x16x32_bf16 v[32:35],a[232:235],v[60:63],v[32:35]
ds_read_b128 v[56:59],v2 offset:4096
v_mfma_f32_16x16x32_bf16 v[36:39],a[236:239],v[60:63],v[36:39]
v_mfma_f32_16x16x32_bf16 v[32:35],a[240:243],v[64:67],v[32:35]
ds_read_b128 v[60:63],v2 offset:5120
v_mfma_f32_16x16x32_bf16 v[36:39],a[244:247],v[64:67],v[36:39]
v_mfma_f32_16x16x32_bf16 v[32:35],a[248:251],v[68:71],v[32:35]
s_waitcnt lgkmcnt(2)
ds_read_b128 v[64:67],v2 offset:6144
v_mfma_f32_16x16x32_bf16 v[36:39],a[252:255],v[68:71],v[36:39]
s_cbranch_scc1 Lg3o_d0w3_%=
s_branch Lg3o_slow0_%=
Lg3o_slow0_%=:
s_cmp_lt_u32 s66,s59
s_cbranch_scc1 Lg3o_gs0_%=
s_cmp_eq_u32 s54,1
s_cselect_b32 s68,s49,s48
s_lshr_b32 s68,s68,5
s_add_u32 m0,s78,8192
s_nop 0
buffer_load_dword v6,s[36:39],s68 offen offset:0 sc1 lds
s_mul_i32 s73,s47,7
s_lshr_b32 s73,s73,1
v_mul_f32 v96,s75,v92
v_fma_f32 v93,v93,s75,v12
v_fma_f32 v93,-v96,v96,v93
v_max_f32 v93,v93,v12
v_rsq_f32 v92,v93
s_nop 0
v_mul_f32 v97,s76,v92
v_mul_f32 v98,v92,v92
v_mul_f32 v99,v93,v92
v_mul_f32 v98,s77,v98
v_mul_f32 v98,v98,v97
v_fma_f32 v32,-v96,v16,v32
v_fma_f32 v33,-v96,v17,v33
v_fma_f32 v34,-v96,v18,v34
v_fma_f32 v35,-v96,v19,v35
v_mul_f32 v72,v32,v32
v_mul_f32 v73,v33,v33
v_mul_f32 v74,v34,v34
v_mul_f32 v75,v35,v35
v_fma_f32 v72,v98,v72,v97
v_fma_f32 v73,v98,v73,v97
v_fma_f32 v74,v98,v74,v97
v_fma_f32 v75,v98,v75,v97
v_mul_f32 v72,v32,v72
v_mul_f32 v73,v33,v73
v_mul_f32 v74,v34,v74
v_mul_f32 v75,v35,v75
v_exp_f32 v72,v72
v_exp_f32 v73,v73
v_exp_f32 v74,v74
v_exp_f32 v75,v75
v_fma_f32 v72,v72,v99,v99
v_fma_f32 v73,v73,v99,v99
v_fma_f32 v74,v74,v99,v99
v_fma_f32 v75,v75,v99,v99
v_rcp_f32 v72,v72
v_rcp_f32 v73,v73
v_rcp_f32 v74,v74
v_rcp_f32 v75,v75
v_mul_f32 v32,v32,v72
s_add_u32 m0,s78,8448
s_nop 0
buffer_load_dword v6,s[36:39],s68 offen offset:0 sc1 lds
v_mul_f32 v33,v33,v73
v_mul_f32 v34,v34,v74
v_mul_f32 v35,v35,v75
v_cvt_pk_bf16_f32 v80,v32,v33
v_cvt_pk_bf16_f32 v81,v34,v35
v_fma_f32 v36,-v96,v20,v36
v_fma_f32 v37,-v96,v21,v37
v_fma_f32 v38,-v96,v22,v38
v_fma_f32 v39,-v96,v23,v39
v_mul_f32 v76,v36,v36
v_mul_f32 v77,v37,v37
v_mul_f32 v78,v38,v38
v_mul_f32 v79,v39,v39
v_fma_f32 v76,v98,v76,v97
v_fma_f32 v77,v98,v77,v97
v_fma_f32 v78,v98,v78,v97
v_fma_f32 v79,v98,v79,v97
v_mul_f32 v76,v36,v76
v_mul_f32 v77,v37,v77
v_mul_f32 v78,v38,v78
v_mul_f32 v79,v39,v79
v_exp_f32 v76,v76
v_exp_f32 v77,v77
v_exp_f32 v78,v78
v_exp_f32 v79,v79
v_fma_f32 v76,v76,v99,v99
v_fma_f32 v77,v77,v99,v99
v_fma_f32 v78,v78,v99,v99
v_fma_f32 v79,v79,v99,v99
v_rcp_f32 v76,v76
v_rcp_f32 v77,v77
v_rcp_f32 v78,v78
v_rcp_f32 v79,v79
v_mul_f32 v36,v36,v76
v_mul_f32 v37,v37,v77
v_mul_f32 v38,v38,v78
v_mul_f32 v39,v39,v79
v_cvt_pk_bf16_f32 v82,v36,v37
v_cvt_pk_bf16_f32 v83,v38,v39
buffer_store_dwordx4 v[80:83],v3,s[36:39],s73 offen offset:0 sc0 sc1
s_cmp_ge_u32 s48,s59
s_cbranch_scc1 Lg3o_done0_%=
s_add_u32 m0,s78,16384
s_nop 0
buffer_load_dword v6,s[36:39],s68 offen offset:0 sc1 lds
s_waitcnt vmcnt(3)
s_barrier
ds_read_b32 v13,v7 offset:8192
s_mul_i32 s70,s46,5
s_lshr_b32 s70,s70,6
buffer_store_dword v5,v4,s[36:39],s70 offen offset:0 sc0 sc1
s_mov_b32 s46,s59
s_mov_b32 s69,18432
s_waitcnt lgkmcnt(0)
v_cmp_eq_u32 vcc,s60,v13
s_nop 0
s_cmp_eq_u32 vcc_lo,-1
s_cbranch_scc1 Lg3o_go0_%=
s_add_u32 m0,s78,16640
s_nop 0
buffer_load_dword v6,s[36:39],s68 offen offset:0 sc1 lds
s_waitcnt vmcnt(4)
s_barrier
ds_read_b32 v13,v7 offset:8448
s_mov_b32 s69,18688
s_waitcnt lgkmcnt(0)
v_cmp_eq_u32 vcc,s60,v13
s_nop 0
s_cmp_eq_u32 vcc_lo,-1
s_cbranch_scc1 Lg3o_go0_%=
s_add_u32 m0,s78,8192
s_nop 0
buffer_load_dword v6,s[36:39],s68 offen offset:0 sc1 lds
s_waitcnt vmcnt(3)
s_barrier
ds_read_b32 v13,v7 offset:16384
s_mul_i32 s70,s47,5
s_lshr_b32 s70,s70,6
buffer_store_dword v5,v4,s[36:39],s70 offen offset:0 sc0 sc1
s_mov_b32 s47,s59
s_mov_b32 s69,10240
s_waitcnt lgkmcnt(0)
v_cmp_eq_u32 vcc,s60,v13
s_nop 0
s_cmp_eq_u32 vcc_lo,-1
s_cbranch_scc1 Lg3o_go0_%=
s_add_u32 m0,s78,8448
s_nop 0
buffer_load_dword v6,s[36:39],s68 offen offset:0 sc1 lds
s_waitcnt vmcnt(3)
s_barrier
ds_read_b32 v13,v7 offset:16640
s_mov_b32 s69,10496
s_waitcnt lgkmcnt(0)
v_cmp_eq_u32 vcc,s60,v13
s_nop 0
s_cmp_eq_u32 vcc_lo,-1
s_cbranch_scc1 Lg3o_go0_%=
Lg3o_poll0_%=:
s_add_u32 m0,s78,16384
s_nop 0
buffer_load_dword v6,s[36:39],s68 offen offset:0 sc1 lds
s_waitcnt vmcnt(2)
s_barrier
ds_read_b32 v13,v7 offset:8192
s_mov_b32 s69,18432
s_waitcnt lgkmcnt(0)
v_cmp_eq_u32 vcc,s60,v13
s_nop 0
s_cmp_eq_u32 vcc_lo,-1
s_cbranch_scc1 Lg3o_go0_%=
s_sleep 4
s_add_u32 m0,s78,16640
s_nop 0
buffer_load_dword v6,s[36:39],s68 offen offset:0 sc1 lds
s_waitcnt vmcnt(2)
s_barrier
ds_read_b32 v13,v7 offset:8448
s_mov_b32 s69,18688
s_waitcnt lgkmcnt(0)
v_cmp_eq_u32 vcc,s60,v13
s_nop 0
s_cmp_eq_u32 vcc_lo,-1
s_cbranch_scc1 Lg3o_go0_%=)"
R"(
s_sleep 4
s_add_u32 m0,s78,8192
s_nop 0
buffer_load_dword v6,s[36:39],s68 offen offset:0 sc1 lds
s_waitcnt vmcnt(2)
s_barrier
ds_read_b32 v13,v7 offset:16384
s_mov_b32 s69,10240
s_waitcnt lgkmcnt(0)
v_cmp_eq_u32 vcc,s60,v13
s_nop 0
s_cmp_eq_u32 vcc_lo,-1
s_cbranch_scc1 Lg3o_go0_%=
s_sleep 4
s_add_u32 m0,s78,8448
s_nop 0
buffer_load_dword v6,s[36:39],s68 offen offset:0 sc1 lds
s_waitcnt vmcnt(2)
s_barrier
ds_read_b32 v13,v7 offset:16640
s_mov_b32 s69,10496
s_waitcnt lgkmcnt(0)
v_cmp_eq_u32 vcc,s60,v13
s_nop 0
s_cmp_eq_u32 vcc_lo,-1
s_cbranch_scc1 Lg3o_go0_%=
s_sleep 4
s_add_u32 s79,s79,1
s_cmp_lt_u32 s79,20000
s_cbranch_scc1 Lg3o_poll0_%=
Lg3o_go0_%=:
s_xor_b32 s62,s63,65536
s_mov_b32 m0,s62
s_nop 0
buffer_load_dwordx4 v0,s[36:39],s48 offen offset:0 sc1 lds
buffer_load_dwordx4 v0,s[36:39],s48 offen offset:1024 sc1 lds
buffer_load_dwordx4 v0,s[36:39],s48 offen offset:2048 sc1 lds
buffer_load_dwordx4 v0,s[36:39],s48 offen offset:3072 sc1 lds
s_add_u32 m0,s62,16384
s_nop 0
buffer_load_dwordx4 v1,s[36:39],s48 offen offset:0 sc1 lds
buffer_load_dwordx4 v1,s[36:39],s48 offen offset:1024 sc1 lds
buffer_load_dwordx4 v1,s[36:39],s48 offen offset:2048 sc1 lds
buffer_load_dwordx4 v1,s[36:39],s48 offen offset:3072 sc1 lds
s_lshr_b32 s72,s48,5
s_lshr_b32 s71,s62,2
s_add_u32 m0,s71,s88
s_nop 0
buffer_load_dword v9,s[36:39],s72 offen offset:0 sc1 lds
s_cmp_eq_u32 vcc_hi,-1
s_cselect_b32 s66,s49,s59
s_xor_b32 s62,s64,65536
s_cmp_ge_u32 s66,s59
s_cselect_b32 s68,s49,s50
s_lshr_b32 s68,s68,5
s_add_u32 m0,s78,0
s_nop 0
buffer_load_dword v6,s[36:39],s68 offen offset:0 sc1 lds
s_mov_b32 m0,s62
s_nop 0
buffer_load_dwordx4 v0,s[36:39],s66 offen offset:0 sc1 lds
buffer_load_dwordx4 v0,s[36:39],s66 offen offset:1024 sc1 lds
buffer_load_dwordx4 v0,s[36:39],s66 offen offset:2048 sc1 lds
buffer_load_dwordx4 v0,s[36:39],s66 offen offset:3072 sc1 lds
s_waitcnt vmcnt(10)
s_barrier
ds_read_b128 v[40:43],v2 offset:0
ds_read_b128 v[44:47],v2 offset:1024
ds_read_b128 v[48:51],v2 offset:2048
ds_read_b128 v[52:55],v2 offset:3072
ds_read_b128 v[56:59],v2 offset:4096
ds_read_b128 v[60:63],v2 offset:5120
ds_read_b128 v[64:67],v2 offset:6144
s_mul_i32 s70,s46,5
s_lshr_b32 s70,s70,6
buffer_store_dword v5,v4,s[36:39],s70 offen offset:0 sc0 sc1
s_mov_b32 s46,s59
s_mul_i32 s70,s47,5
s_lshr_b32 s70,s70,6
buffer_store_dword v5,v4,s[36:39],s70 offen offset:0 sc0 sc1
s_mov_b32 s47,s59
s_mul_i32 s70,s59,5
s_lshr_b32 s70,s70,6
buffer_store_dword v5,v4,s[36:39],s70 offen offset:0 sc0 sc1
s_waitcnt lgkmcnt(3)
s_setpc_b64 s[90:91]
Lg3o_gs0_%=:
s_cmp_eq_u32 s54,1
s_cselect_b32 s68,s49,s48
s_lshr_b32 s68,s68,5
s_mul_i32 s73,s47,7
s_lshr_b32 s73,s73,1
s_add_u32 m0,s78,8192
s_nop 0
buffer_load_dword v6,s[36:39],s68 offen offset:0 sc1 lds
v_mul_f32 v96,s75,v92
v_fma_f32 v93,v93,s75,v12
v_fma_f32 v93,-v96,v96,v93
v_max_f32 v93,v93,v12
v_rsq_f32 v92,v93
s_nop 0
v_mul_f32 v97,s76,v92
v_mul_f32 v98,v92,v92
v_mul_f32 v99,v93,v92
v_mul_f32 v98,s77,v98
v_mul_f32 v98,v98,v97
v_fma_f32 v32,-v96,v16,v32
v_fma_f32 v33,-v96,v17,v33
v_fma_f32 v34,-v96,v18,v34
s_add_u32 m0,s62,16384
s_nop 0
buffer_load_dwordx4 v1,s[36:39],s66 offen offset:0 sc1 lds
v_fma_f32 v35,-v96,v19,v35
v_mul_f32 v72,v32,v32
v_mul_f32 v73,v33,v33
v_mul_f32 v74,v34,v34
v_mul_f32 v75,v35,v35
v_fma_f32 v72,v98,v72,v97
v_fma_f32 v73,v98,v73,v97
v_fma_f32 v74,v98,v74,v97
v_fma_f32 v75,v98,v75,v97
v_mul_f32 v72,v32,v72
v_mul_f32 v73,v33,v73
v_mul_f32 v74,v34,v74
v_mul_f32 v75,v35,v75
buffer_load_dwordx4 v1,s[36:39],s66 offen offset:1024 sc1 lds
v_exp_f32 v72,v72
v_exp_f32 v73,v73
v_exp_f32 v74,v74
v_exp_f32 v75,v75
v_fma_f32 v72,v72,v99,v99
v_fma_f32 v73,v73,v99,v99
v_fma_f32 v74,v74,v99,v99
v_fma_f32 v75,v75,v99,v99
v_rcp_f32 v72,v72
v_rcp_f32 v73,v73
v_rcp_f32 v74,v74
v_rcp_f32 v75,v75
v_mul_f32 v32,v32,v72
buffer_load_dwordx4 v1,s[36:39],s66 offen offset:2048 sc1 lds
v_mul_f32 v33,v33,v73
v_mul_f32 v34,v34,v74
v_mul_f32 v35,v35,v75
v_cvt_pk_bf16_f32 v80,v32,v33
v_cvt_pk_bf16_f32 v81,v34,v35
v_fma_f32 v36,-v96,v20,v36
v_fma_f32 v37,-v96,v21,v37
v_fma_f32 v38,-v96,v22,v38
v_fma_f32 v39,-v96,v23,v39
v_mul_f32 v76,v36,v36
v_mul_f32 v77,v37,v37
v_mul_f32 v78,v38,v38
v_mul_f32 v79,v39,v39
buffer_load_dwordx4 v1,s[36:39],s66 offen offset:3072 sc1 lds
v_fma_f32 v76,v98,v76,v97
v_fma_f32 v77,v98,v77,v97
v_fma_f32 v78,v98,v78,v97
v_fma_f32 v79,v98,v79,v97
v_mul_f32 v76,v36,v76
v_mul_f32 v77,v37,v77
v_mul_f32 v78,v38,v78
v_mul_f32 v79,v39,v79
v_exp_f32 v76,v76
v_exp_f32 v77,v77
v_exp_f32 v78,v78
v_exp_f32 v79,v79
v_fma_f32 v76,v76,v99,v99
s_lshr_b32 s72,s66,5
s_lshr_b32 s71,s62,2
s_add_u32 m0,s71,s88
s_nop 0
buffer_load_dword v9,s[36:39],s72 offen offset:0 sc1 lds
v_fma_f32 v77,v77,v99,v99
v_fma_f32 v78,v78,v99,v99
v_fma_f32 v79,v79,v99,v99
v_rcp_f32 v76,v76
v_rcp_f32 v77,v77
v_rcp_f32 v78,v78
v_rcp_f32 v79,v79
v_mul_f32 v36,v36,v76
v_mul_f32 v37,v37,v77
v_mul_f32 v38,v38,v78
v_mul_f32 v39,v39,v79
v_cvt_pk_bf16_f32 v82,v36,v37
v_cvt_pk_bf16_f32 v83,v38,v39
buffer_store_dwordx4 v[80:83],v3,s[36:39],s73 offen offset:0 sc0 sc1
s_waitcnt vmcnt(6)
s_barrier
ds_read_b32 v13,v7 offset:8192
ds_read_b128 v[40:43],v2 offset:0
ds_read_b128 v[44:47],v2 offset:1024
ds_read_b128 v[48:51],v2 offset:2048
ds_read_b128 v[52:55],v2 offset:3072
ds_read_b128 v[56:59],v2 offset:4096
ds_read_b128 v[60:63],v2 offset:5120
ds_read_b128 v[64:67],v2 offset:6144
s_mul_i32 s70,s46,5
s_lshr_b32 s70,s70,6
buffer_store_dword v5,v4,s[36:39],s70 offen offset:0 sc0 sc1
s_waitcnt lgkmcnt(7))"
R"(
v_cmp_eq_u32 vcc,s60,v13
s_nop 0
s_cmp_eq_u32 vcc_hi,-1
s_cselect_b32 s66,s49,s59
s_xor_b32 s62,s64,65536
s_cmp_ge_u32 s66,s59
s_cselect_b32 s68,s49,s50
s_lshr_b32 s68,s68,5
s_add_u32 m0,s78,0
s_nop 0
buffer_load_dword v6,s[36:39],s68 offen offset:0 sc1 lds
s_mov_b32 m0,s62
s_nop 0
buffer_load_dwordx4 v0,s[36:39],s66 offen offset:0 sc1 lds
buffer_load_dwordx4 v0,s[36:39],s66 offen offset:1024 sc1 lds
buffer_load_dwordx4 v0,s[36:39],s66 offen offset:2048 sc1 lds
buffer_load_dwordx4 v0,s[36:39],s66 offen offset:3072 sc1 lds
s_mov_b32 s46,s47
s_mov_b32 s47,s59
s_mul_i32 s70,s59,5
s_lshr_b32 s70,s70,6
buffer_store_dword v5,v4,s[36:39],s70 offen offset:0 sc0 sc1
s_waitcnt lgkmcnt(3)
s_setpc_b64 s[90:91]
Lg3o_done0_%=:
s_waitcnt vmcnt(0)
s_barrier
s_mul_i32 s70,s46,5
s_lshr_b32 s70,s70,6
buffer_store_dword v5,v4,s[36:39],s70 offen offset:0 sc0 sc1
s_mul_i32 s70,s47,5
s_lshr_b32 s70,s70,6
buffer_store_dword v5,v4,s[36:39],s70 offen offset:0 sc0 sc1
s_branch Lg3o_done_%=
Lg3o_slow1_%=:
s_cmp_lt_u32 s67,s59
s_cbranch_scc1 Lg3o_gs1_%=
s_cmp_eq_u32 s54,1
s_cselect_b32 s68,s50,s49
s_lshr_b32 s68,s68,5
s_add_u32 m0,s78,8192
s_nop 0
buffer_load_dword v6,s[36:39],s68 offen offset:0 sc1 lds
s_mul_i32 s73,s48,7
s_lshr_b32 s73,s73,1
v_mul_f32 v96,s75,v92
v_fma_f32 v93,v93,s75,v12
v_fma_f32 v93,-v96,v96,v93
v_max_f32 v93,v93,v12
v_rsq_f32 v92,v93
s_nop 0
v_mul_f32 v97,s76,v92
v_mul_f32 v98,v92,v92
v_mul_f32 v99,v93,v92
v_mul_f32 v98,s77,v98
v_mul_f32 v98,v98,v97
v_fma_f32 v24,-v96,v16,v24
v_fma_f32 v25,-v96,v17,v25
v_fma_f32 v26,-v96,v18,v26
v_fma_f32 v27,-v96,v19,v27
v_mul_f32 v72,v24,v24
v_mul_f32 v73,v25,v25
v_mul_f32 v74,v26,v26
v_mul_f32 v75,v27,v27
v_fma_f32 v72,v98,v72,v97
v_fma_f32 v73,v98,v73,v97
v_fma_f32 v74,v98,v74,v97
v_fma_f32 v75,v98,v75,v97
v_mul_f32 v72,v24,v72
v_mul_f32 v73,v25,v73
v_mul_f32 v74,v26,v74
v_mul_f32 v75,v27,v75
v_exp_f32 v72,v72
v_exp_f32 v73,v73
v_exp_f32 v74,v74
v_exp_f32 v75,v75
v_fma_f32 v72,v72,v99,v99
v_fma_f32 v73,v73,v99,v99
v_fma_f32 v74,v74,v99,v99
v_fma_f32 v75,v75,v99,v99
v_rcp_f32 v72,v72
v_rcp_f32 v73,v73
v_rcp_f32 v74,v74
v_rcp_f32 v75,v75
v_mul_f32 v24,v24,v72
s_add_u32 m0,s78,8448
s_nop 0
buffer_load_dword v6,s[36:39],s68 offen offset:0 sc1 lds
v_mul_f32 v25,v25,v73
v_mul_f32 v26,v26,v74
v_mul_f32 v27,v27,v75
v_cvt_pk_bf16_f32 v80,v24,v25
v_cvt_pk_bf16_f32 v81,v26,v27
v_fma_f32 v28,-v96,v20,v28
v_fma_f32 v29,-v96,v21,v29
v_fma_f32 v30,-v96,v22,v30
v_fma_f32 v31,-v96,v23,v31
v_mul_f32 v76,v28,v28
v_mul_f32 v77,v29,v29
v_mul_f32 v78,v30,v30
v_mul_f32 v79,v31,v31
v_fma_f32 v76,v98,v76,v97
v_fma_f32 v77,v98,v77,v97
v_fma_f32 v78,v98,v78,v97
v_fma_f32 v79,v98,v79,v97
v_mul_f32 v76,v28,v76
v_mul_f32 v77,v29,v77
v_mul_f32 v78,v30,v78
v_mul_f32 v79,v31,v79
v_exp_f32 v76,v76
v_exp_f32 v77,v77
v_exp_f32 v78,v78
v_exp_f32 v79,v79
v_fma_f32 v76,v76,v99,v99
v_fma_f32 v77,v77,v99,v99
v_fma_f32 v78,v78,v99,v99
v_fma_f32 v79,v79,v99,v99
v_rcp_f32 v76,v76
v_rcp_f32 v77,v77
v_rcp_f32 v78,v78
v_rcp_f32 v79,v79
v_mul_f32 v28,v28,v76
v_mul_f32 v29,v29,v77
v_mul_f32 v30,v30,v78
v_mul_f32 v31,v31,v79
v_cvt_pk_bf16_f32 v82,v28,v29
v_cvt_pk_bf16_f32 v83,v30,v31
buffer_store_dwordx4 v[80:83],v3,s[36:39],s73 offen offset:0 sc0 sc1
s_cmp_ge_u32 s49,s59
s_cbranch_scc1 Lg3o_done1_%=
s_add_u32 m0,s78,16384
s_nop 0
buffer_load_dword v6,s[36:39],s68 offen offset:0 sc1 lds
s_waitcnt vmcnt(3)
s_barrier
ds_read_b32 v13,v7 offset:8192
s_mul_i32 s70,s47,5
s_lshr_b32 s70,s70,6
buffer_store_dword v5,v4,s[36:39],s70 offen offset:0 sc0 sc1
s_mov_b32 s47,s59
s_mov_b32 s69,18432
s_waitcnt lgkmcnt(0)
v_cmp_eq_u32 vcc,s60,v13
s_nop 0
s_cmp_eq_u32 vcc_lo,-1
s_cbranch_scc1 Lg3o_go1_%=
s_add_u32 m0,s78,16640
s_nop 0
buffer_load_dword v6,s[36:39],s68 offen offset:0 sc1 lds
s_waitcnt vmcnt(4)
s_barrier
ds_read_b32 v13,v7 offset:8448
s_mov_b32 s69,18688
s_waitcnt lgkmcnt(0)
v_cmp_eq_u32 vcc,s60,v13
s_nop 0
s_cmp_eq_u32 vcc_lo,-1
s_cbranch_scc1 Lg3o_go1_%=
s_add_u32 m0,s78,8192
s_nop 0
buffer_load_dword v6,s[36:39],s68 offen offset:0 sc1 lds
s_waitcnt vmcnt(3)
s_barrier
ds_read_b32 v13,v7 offset:16384
s_mul_i32 s70,s48,5
s_lshr_b32 s70,s70,6
buffer_store_dword v5,v4,s[36:39],s70 offen offset:0 sc0 sc1
s_mov_b32 s48,s59
s_mov_b32 s69,10240
s_waitcnt lgkmcnt(0)
v_cmp_eq_u32 vcc,s60,v13
s_nop 0
s_cmp_eq_u32 vcc_lo,-1
s_cbranch_scc1 Lg3o_go1_%=
s_add_u32 m0,s78,8448
s_nop 0
buffer_load_dword v6,s[36:39],s68 offen offset:0 sc1 lds
s_waitcnt vmcnt(3)
s_barrier
ds_read_b32 v13,v7 offset:16640
s_mov_b32 s69,10496
s_waitcnt lgkmcnt(0)
v_cmp_eq_u32 vcc,s60,v13
s_nop 0
s_cmp_eq_u32 vcc_lo,-1
s_cbranch_scc1 Lg3o_go1_%=
Lg3o_poll1_%=:
s_add_u32 m0,s78,16384
s_nop 0
buffer_load_dword v6,s[36:39],s68 offen offset:0 sc1 lds
s_waitcnt vmcnt(2)
s_barrier
ds_read_b32 v13,v7 offset:8192
s_mov_b32 s69,18432
s_waitcnt lgkmcnt(0)
v_cmp_eq_u32 vcc,s60,v13
s_nop 0
s_cmp_eq_u32 vcc_lo,-1
s_cbranch_scc1 Lg3o_go1_%=
s_sleep 4)"
R"(
s_add_u32 m0,s78,16640
s_nop 0
buffer_load_dword v6,s[36:39],s68 offen offset:0 sc1 lds
s_waitcnt vmcnt(2)
s_barrier
ds_read_b32 v13,v7 offset:8448
s_mov_b32 s69,18688
s_waitcnt lgkmcnt(0)
v_cmp_eq_u32 vcc,s60,v13
s_nop 0
s_cmp_eq_u32 vcc_lo,-1
s_cbranch_scc1 Lg3o_go1_%=
s_sleep 4
s_add_u32 m0,s78,8192
s_nop 0
buffer_load_dword v6,s[36:39],s68 offen offset:0 sc1 lds
s_waitcnt vmcnt(2)
s_barrier
ds_read_b32 v13,v7 offset:16384
s_mov_b32 s69,10240
s_waitcnt lgkmcnt(0)
v_cmp_eq_u32 vcc,s60,v13
s_nop 0
s_cmp_eq_u32 vcc_lo,-1
s_cbranch_scc1 Lg3o_go1_%=
s_sleep 4
s_add_u32 m0,s78,8448
s_nop 0
buffer_load_dword v6,s[36:39],s68 offen offset:0 sc1 lds
s_waitcnt vmcnt(2)
s_barrier
ds_read_b32 v13,v7 offset:16640
s_mov_b32 s69,10496
s_waitcnt lgkmcnt(0)
v_cmp_eq_u32 vcc,s60,v13
s_nop 0
s_cmp_eq_u32 vcc_lo,-1
s_cbranch_scc1 Lg3o_go1_%=
s_sleep 4
s_add_u32 s79,s79,1
s_cmp_lt_u32 s79,20000
s_cbranch_scc1 Lg3o_poll1_%=
Lg3o_go1_%=:
s_xor_b32 s62,s64,65536
s_mov_b32 m0,s62
s_nop 0
buffer_load_dwordx4 v0,s[36:39],s49 offen offset:0 sc1 lds
buffer_load_dwordx4 v0,s[36:39],s49 offen offset:1024 sc1 lds
buffer_load_dwordx4 v0,s[36:39],s49 offen offset:2048 sc1 lds
buffer_load_dwordx4 v0,s[36:39],s49 offen offset:3072 sc1 lds
s_add_u32 m0,s62,16384
s_nop 0
buffer_load_dwordx4 v1,s[36:39],s49 offen offset:0 sc1 lds
buffer_load_dwordx4 v1,s[36:39],s49 offen offset:1024 sc1 lds
buffer_load_dwordx4 v1,s[36:39],s49 offen offset:2048 sc1 lds
buffer_load_dwordx4 v1,s[36:39],s49 offen offset:3072 sc1 lds
s_lshr_b32 s72,s49,5
s_lshr_b32 s71,s62,2
s_add_u32 m0,s71,s88
s_nop 0
buffer_load_dword v9,s[36:39],s72 offen offset:0 sc1 lds
s_cmp_eq_u32 vcc_hi,-1
s_cselect_b32 s67,s50,s59
s_xor_b32 s62,s63,65536
s_cmp_ge_u32 s67,s59
s_cselect_b32 s68,s50,s51
s_lshr_b32 s68,s68,5
s_add_u32 m0,s78,256
s_nop 0
buffer_load_dword v6,s[36:39],s68 offen offset:0 sc1 lds
s_mov_b32 m0,s62
s_nop 0
buffer_load_dwordx4 v0,s[36:39],s67 offen offset:0 sc1 lds
buffer_load_dwordx4 v0,s[36:39],s67 offen offset:1024 sc1 lds
buffer_load_dwordx4 v0,s[36:39],s67 offen offset:2048 sc1 lds
buffer_load_dwordx4 v0,s[36:39],s67 offen offset:3072 sc1 lds
s_waitcnt vmcnt(10)
s_barrier
ds_read_b128 v[40:43],v2 offset:32768
ds_read_b128 v[44:47],v2 offset:33792
ds_read_b128 v[48:51],v2 offset:34816
ds_read_b128 v[52:55],v2 offset:35840
ds_read_b128 v[56:59],v2 offset:36864
ds_read_b128 v[60:63],v2 offset:37888
ds_read_b128 v[64:67],v2 offset:38912
s_mul_i32 s70,s47,5
s_lshr_b32 s70,s70,6
buffer_store_dword v5,v4,s[36:39],s70 offen offset:0 sc0 sc1
s_mov_b32 s47,s59
s_mul_i32 s70,s48,5
s_lshr_b32 s70,s70,6
buffer_store_dword v5,v4,s[36:39],s70 offen offset:0 sc0 sc1
s_mov_b32 s48,s59
s_mul_i32 s70,s59,5
s_lshr_b32 s70,s70,6
buffer_store_dword v5,v4,s[36:39],s70 offen offset:0 sc0 sc1
s_waitcnt lgkmcnt(3)
s_setpc_b64 s[92:93]
Lg3o_gs1_%=:
s_cmp_eq_u32 s54,1
s_cselect_b32 s68,s50,s49
s_lshr_b32 s68,s68,5
s_mul_i32 s73,s48,7
s_lshr_b32 s73,s73,1
s_add_u32 m0,s78,8192
s_nop 0
buffer_load_dword v6,s[36:39],s68 offen offset:0 sc1 lds
v_mul_f32 v96,s75,v92
v_fma_f32 v93,v93,s75,v12
v_fma_f32 v93,-v96,v96,v93
v_max_f32 v93,v93,v12
v_rsq_f32 v92,v93
s_nop 0
v_mul_f32 v97,s76,v92
v_mul_f32 v98,v92,v92
v_mul_f32 v99,v93,v92
v_mul_f32 v98,s77,v98
v_mul_f32 v98,v98,v97
v_fma_f32 v24,-v96,v16,v24
v_fma_f32 v25,-v96,v17,v25
v_fma_f32 v26,-v96,v18,v26
s_add_u32 m0,s62,16384
s_nop 0
buffer_load_dwordx4 v1,s[36:39],s67 offen offset:0 sc1 lds
v_fma_f32 v27,-v96,v19,v27
v_mul_f32 v72,v24,v24
v_mul_f32 v73,v25,v25
v_mul_f32 v74,v26,v26
v_mul_f32 v75,v27,v27
v_fma_f32 v72,v98,v72,v97
v_fma_f32 v73,v98,v73,v97
v_fma_f32 v74,v98,v74,v97
v_fma_f32 v75,v98,v75,v97
v_mul_f32 v72,v24,v72
v_mul_f32 v73,v25,v73
v_mul_f32 v74,v26,v74
v_mul_f32 v75,v27,v75
buffer_load_dwordx4 v1,s[36:39],s67 offen offset:1024 sc1 lds
v_exp_f32 v72,v72
v_exp_f32 v73,v73
v_exp_f32 v74,v74
v_exp_f32 v75,v75
v_fma_f32 v72,v72,v99,v99
v_fma_f32 v73,v73,v99,v99
v_fma_f32 v74,v74,v99,v99
v_fma_f32 v75,v75,v99,v99
v_rcp_f32 v72,v72
v_rcp_f32 v73,v73
v_rcp_f32 v74,v74
v_rcp_f32 v75,v75
v_mul_f32 v24,v24,v72
buffer_load_dwordx4 v1,s[36:39],s67 offen offset:2048 sc1 lds
v_mul_f32 v25,v25,v73
v_mul_f32 v26,v26,v74
v_mul_f32 v27,v27,v75
v_cvt_pk_bf16_f32 v80,v24,v25
v_cvt_pk_bf16_f32 v81,v26,v27
v_fma_f32 v28,-v96,v20,v28
v_fma_f32 v29,-v96,v21,v29
v_fma_f32 v30,-v96,v22,v30
v_fma_f32 v31,-v96,v23,v31
v_mul_f32 v76,v28,v28
v_mul_f32 v77,v29,v29
v_mul_f32 v78,v30,v30
v_mul_f32 v79,v31,v31
buffer_load_dwordx4 v1,s[36:39],s67 offen offset:3072 sc1 lds
v_fma_f32 v76,v98,v76,v97
v_fma_f32 v77,v98,v77,v97
v_fma_f32 v78,v98,v78,v97
v_fma_f32 v79,v98,v79,v97
v_mul_f32 v76,v28,v76
v_mul_f32 v77,v29,v77
v_mul_f32 v78,v30,v78
v_mul_f32 v79,v31,v79
v_exp_f32 v76,v76
v_exp_f32 v77,v77
v_exp_f32 v78,v78
v_exp_f32 v79,v79
v_fma_f32 v76,v76,v99,v99
s_lshr_b32 s72,s67,5
s_lshr_b32 s71,s62,2
s_add_u32 m0,s71,s88
s_nop 0
buffer_load_dword v9,s[36:39],s72 offen offset:0 sc1 lds
v_fma_f32 v77,v77,v99,v99
v_fma_f32 v78,v78,v99,v99
v_fma_f32 v79,v79,v99,v99
v_rcp_f32 v76,v76
v_rcp_f32 v77,v77
v_rcp_f32 v78,v78
v_rcp_f32 v79,v79
v_mul_f32 v28,v28,v76
v_mul_f32 v29,v29,v77
v_mul_f32 v30,v30,v78
v_mul_f32 v31,v31,v79
v_cvt_pk_bf16_f32 v82,v28,v29
v_cvt_pk_bf16_f32 v83,v30,v31
buffer_store_dwordx4 v[80:83],v3,s[36:39],s73 offen offset:0 sc0 sc1
s_waitcnt vmcnt(6)
s_barrier)"
R"(
ds_read_b32 v13,v7 offset:8192
ds_read_b128 v[40:43],v2 offset:32768
ds_read_b128 v[44:47],v2 offset:33792
ds_read_b128 v[48:51],v2 offset:34816
ds_read_b128 v[52:55],v2 offset:35840
ds_read_b128 v[56:59],v2 offset:36864
ds_read_b128 v[60:63],v2 offset:37888
ds_read_b128 v[64:67],v2 offset:38912
s_mul_i32 s70,s47,5
s_lshr_b32 s70,s70,6
buffer_store_dword v5,v4,s[36:39],s70 offen offset:0 sc0 sc1
s_waitcnt lgkmcnt(7)
v_cmp_eq_u32 vcc,s60,v13
s_nop 0
s_cmp_eq_u32 vcc_hi,-1
s_cselect_b32 s67,s50,s59
s_xor_b32 s62,s63,65536
s_cmp_ge_u32 s67,s59
s_cselect_b32 s68,s50,s51
s_lshr_b32 s68,s68,5
s_add_u32 m0,s78,256
s_nop 0
buffer_load_dword v6,s[36:39],s68 offen offset:0 sc1 lds
s_mov_b32 m0,s62
s_nop 0
buffer_load_dwordx4 v0,s[36:39],s67 offen offset:0 sc1 lds
buffer_load_dwordx4 v0,s[36:39],s67 offen offset:1024 sc1 lds
buffer_load_dwordx4 v0,s[36:39],s67 offen offset:2048 sc1 lds
buffer_load_dwordx4 v0,s[36:39],s67 offen offset:3072 sc1 lds
s_mov_b32 s47,s48
s_mov_b32 s48,s59
s_mul_i32 s70,s59,5
s_lshr_b32 s70,s70,6
buffer_store_dword v5,v4,s[36:39],s70 offen offset:0 sc0 sc1
s_waitcnt lgkmcnt(3)
s_setpc_b64 s[92:93]
Lg3o_done1_%=:
s_waitcnt vmcnt(0)
s_barrier
s_mul_i32 s70,s47,5
s_lshr_b32 s70,s70,6
buffer_store_dword v5,v4,s[36:39],s70 offen offset:0 sc0 sc1
s_mul_i32 s70,s48,5
s_lshr_b32 s70,s70,6
buffer_store_dword v5,v4,s[36:39],s70 offen offset:0 sc0 sc1
s_branch Lg3o_done_%=
Lg3o_done_%=:
s_waitcnt vmcnt(0) lgkmcnt(0)
s_barrier
s_endpgm
Lcode_g3o_e:)"
  :: "s"(ws), "s"(wave), "s"(sStep), "s"(sX1), "s"(sX2), "s"(sU0), "s"(sWw), "s"(sBuf), "v"(vSrc), "v"(vA), "v"(vOut), "v"(vFlag), "v"(vPollOff), "v"(vSnapRd), "v"(vX), "v"(vAuxS), "v"(vStW), "v"(vStR), "v"(vWd0), "v"(vWd1), "v"(vWr0), "v"(vWr1), "v"(vWr2), "s"(w3) : "v0", "v1", "v2", "v3", "v4", "v5", "v6", "v7", "v8", "v9", "v10", "v11", "v12", "v13", "v14", "v15", "v16", "v17", "v18", "v19", "v20", "v21", "v22", "v23", "v24", "v25", "v26", "v27", "v28", "v29", "v30", "v31", "v32", "v33", "v34", "v35", "v36", "v37", "v38", "v39", "v40", "v41", "v42", "v43", "v44", "v45", "v46", "v47", "v48", "v49", "v50", "v51", "v52", "v53", "v54", "v55", "v56", "v57", "v58", "v59", "v60", "v61", "v62", "v63", "v64", "v65", "v66", "v67", "v68", "v69", "v70", "v71", "v72", "v73", "v74", "v75", "v76", "v77", "v78", "v79", "v80", "v81", "v82", "v83", "v84", "v85", "v86", "v87", "v88", "v89", "v90", "v91", "v92", "v93", "v94", "v95", "v96", "v97", "v98", "v99", "v100", "v101", "v102", "v103", "v104", "v105", "v106", "v107", "v108", "v109", "v110", "v111", "v112", "v113", "v114", "v115", "v116", "v117", "v118", "v119", "v120", "v121", "v122", "v123", "v124", "v125", "v126", "v127", "v128", "v129", "v130", "v131", "v132", "v133", "v134", "v135", "v136", "v137", "v138", "v139", "v140", "v141", "v142", "v143", "v144", "v145", "v146", "v147", "v148", "v149", "v150", "v151", "v152", "v153", "v154", "v155", "v156", "v157", "v158", "v159", "v160", "v161", "v162", "v163", "v164", "v165", "v166", "v167", "v168", "v169", "v170", "v171", "v172", "v173", "v174", "v175", "v176", "v177", "v178", "v179", "v180", "v181", "v182", "v183", "v184", "v185", "v186", "v187", "v188", "v189", "v190", "v191", "v192", "v193", "v194", "v195", "v196", "v197", "v198", "v199", "v200", "v201", "v202", "v203", "v204", "v205", "v206", "v207", "v208", "v209", "v210", "v211", "v212", "v213", "v214", "v215", "v216", "v217", "v218", "v219", "v220", "v221", "v222", "v223", "v224", "v225", "v226", "v227", "v228", "v229", "v230", "v231", "v255", "a0", "a1", "a2", "a3", "a4", "a5", "a6", "a7", "a8", "a9", "a10", "a11", "a12", "a13", "a14", "a15", "a16", "a17", "a18", "a19", "a20", "a21", "a22", "a23", "a24", "a25", "a26", "a27", "a28", "a29", "a30", "a31", "a32", "a33", "a34", "a35", "a36", "a37", "a38", "a39", "a40", "a41", "a42", "a43", "a44", "a45", "a46", "a47", "a48", "a49", "a50", "a51", "a52", "a53", "a54", "a55", "a56", "a57", "a58", "a59", "a60", "a61", "a62", "a63", "a64", "a65", "a66", "a67", "a68", "a69", "a70", "a71", "a72", "a73", "a74", "a75", "a76", "a77", "a78", "a79", "a80", "a81", "a82", "a83", "a84", "a85", "a86", "a87", "a88", "a89", "a90", "a91", "a92", "a93", "a94", "a95", "a96", "a97", "a98", "a99", "a100", "a101", "a102", "a103", "a104", "a105", "a106", "a107", "a108", "a109", "a110", "a111", "a112", "a113", "a114", "a115", "a116", "a117", "a118", "a119", "a120", "a121", "a122", "a123", "a124", "a125", "a126", "a127", "a128", "a129", "a130", "a131", "a132", "a133", "a134", "a135", "a136", "a137", "a138", "a139", "a140", "a141", "a142", "a143", "a144", "a145", "a146", "a147", "a148", "a149", "a150", "a151", "a152", "a153", "a154", "a155", "a156", "a157", "a158", "a159", "a160", "a161", "a162", "a163", "a164", "a165", "a166", "a167", "a168", "a169", "a170", "a171", "a172", "a173", "a174", "a175", "a176", "a177", "a178", "a179", "a180", "a181", "a182", "a183", "a184", "a185", "a186", "a187", "a188", "a189", "a190", "a191", "a192", "a193", "a194", "a195", "a196", "a197", "a198", "a199", "a200", "a201", "a202", "a203", "a204", "a205", "a206", "a207", "a208", "a209", "a210", "a211", "a212", "a213", "a214", "a215", "a216", "a217", "a218", "a219", "a220", "a221", "a222", "a223", "a224", "a225", "a226", "a227", "a228", "a229", "a230", "a231", "a232", "a233", "a234", "a235", "a236", "a237", "a238", "a239", "a240", "a241", "a242", "a243", "a244", "a245", "a246", "a247", "a248", "a249", "a250", "a251", "a252", "a253", "a254", "a255", "s36", "s37", "s38", "s39", "s40", "s41", "s42", "s43", "s44", "s45", "s46", "s47", "s48", "s49", "s50", "s51", "s52", "s53", "s54", "s55", "s56", "s57", "s58", "s59", "s60", "s61", "s62", "s63", "s64", "s65", "s66", "s67", "s68", "s69", "s70", "s71", "s72", "s73", "s74", "s75", "s76", "s77", "s78", "s79", "s80", "s81", "s82", "s83", "s84", "s85", "s86", "s87", "s88", "s89", "s90", "s91", "s92", "s93", "s94", "s95", "s96", "s97", "s98", "s99", "vcc", "scc", "memory"); }
    __builtin_unreachable();
  } else if (kind == 4) {
    COMMON(kh == 1 ? 6 : 5, OFF_FG + ((14 * kh) / 3) * 128, OFF_FP + (kh * 4 + slice) * 128, 65536, 256, 2048, 5);
    const unsigned vSrc = OFF_G + kh * 28672 + wave * 4096 + vA, vSrc2 = OFF_G + kh * 28672 + 16384 + wave * 3072 + vA;
    const unsigned vOut = (kh == 0 ? OFF_P0 : kh == 1 ? OFF_P1 : kh == 2 ? OFF_P2 : OFF_P3) + (l15 >> 3) * 16384 + (slice * 8 + wave * 2) * 512 + lg * 128 + (l15 & 7) * 16;
    const unsigned vAuxS = kh < 2 ? (kh ? OFF_XP : OFF_X1) + (slice * 8 + wave * 2) * 1024 + vA : OOB;
    const unsigned sWw = kh * 896 * 2048 + slice * 512 + wave * 128;
    asm volatile(R"(
Lcode_g4_b:
s_mov_b64 s[36:37],%0
s_mov_b32 s38,122727552
s_mov_b32 s39,0x00020000
s_mov_b32 s54,%1
s_mov_b32 s55,%2
s_mov_b32 s56,%3
s_mov_b32 s58,%4
s_mov_b32 s83,%5
s_mov_b32 s82,%6
s_mov_b32 s84,%7
v_mov_b32 v0,%8
v_mov_b32 v1,%9
v_mov_b32 v2,%10
v_mov_b32 v3,%11
v_mov_b32 v4,%12
v_mov_b32 v6,%13
v_mov_b32 v7,%14
v_mov_b32 v8,%15
v_mov_b32 v12,%16
v_mov_b32 v13,%17
v_mov_b32 v14,%18
v_mov_b32 v15,%19
v_mov_b32 v16,%20
s_mov_b64 s[40:41],%21
s_mov_b32 s42,7340032
s_mov_b32 s43,0x00020000
s_lshl_b32 s88,s54,12
s_mul_i32 s68,s54,9216
s_add_u32 s68,s68,122880
v_add_u32 v9,s68,v2
s_mov_b32 s69,122727424
buffer_load_dword v5,off,s[36:39],s69 sc1
s_add_u32 m0,s88,0
s_add_u32 s61,s82,0
buffer_load_dwordx4 v12,s[40:43],s61 offen offset:0  lds
s_add_u32 m0,s88,1024
s_nop 0
buffer_load_dwordx4 v13,s[40:43],s61 offen offset:0  lds
s_add_u32 m0,s88,2048
s_add_u32 s61,s82,32768
buffer_load_dwordx4 v12,s[40:43],s61 offen offset:0  lds
s_add_u32 m0,s88,3072
s_nop 0
buffer_load_dwordx4 v13,s[40:43],s61 offen offset:0  lds
s_add_u32 m0,s88,16384
s_add_u32 s61,s82,65536
buffer_load_dwordx4 v12,s[40:43],s61 offen offset:0  lds
s_add_u32 m0,s88,17408
s_nop 0
buffer_load_dwordx4 v13,s[40:43],s61 offen offset:0  lds
s_add_u32 m0,s88,18432
s_add_u32 s61,s82,98304
buffer_load_dwordx4 v12,s[40:43],s61 offen offset:0  lds
s_add_u32 m0,s88,19456
s_nop 0
buffer_load_dwordx4 v13,s[40:43],s61 offen offset:0  lds
s_add_u32 m0,s88,32768
s_add_u32 s61,s82,131072
buffer_load_dwordx4 v12,s[40:43],s61 offen offset:0  lds
s_add_u32 m0,s88,33792
s_nop 0
buffer_load_dwordx4 v13,s[40:43],s61 offen offset:0  lds
s_add_u32 m0,s88,34816
s_add_u32 s61,s82,163840
buffer_load_dwordx4 v12,s[40:43],s61 offen offset:0  lds
s_add_u32 m0,s88,35840
s_nop 0
buffer_load_dwordx4 v13,s[40:43],s61 offen offset:0  lds
s_add_u32 m0,s88,49152
s_add_u32 s61,s82,196608
buffer_load_dwordx4 v12,s[40:43],s61 offen offset:0  lds
s_add_u32 m0,s88,50176
s_nop 0
buffer_load_dwordx4 v13,s[40:43],s61 offen offset:0  lds
s_add_u32 m0,s88,51200
s_add_u32 s61,s82,229376
buffer_load_dwordx4 v12,s[40:43],s61 offen offset:0  lds
s_add_u32 m0,s88,52224
s_nop 0
buffer_load_dwordx4 v13,s[40:43],s61 offen offset:0  lds
s_add_u32 m0,s88,65536
s_add_u32 s61,s82,262144
buffer_load_dwordx4 v12,s[40:43],s61 offen offset:0  lds
s_add_u32 m0,s88,66560
s_nop 0
buffer_load_dwordx4 v13,s[40:43],s61 offen offset:0  lds
s_add_u32 m0,s88,67584
s_add_u32 s61,s82,294912
buffer_load_dwordx4 v12,s[40:43],s61 offen offset:0  lds
s_add_u32 m0,s88,68608
s_nop 0
buffer_load_dwordx4 v13,s[40:43],s61 offen offset:0  lds
v_add_u32 v28,49152,v14
v_add_u32 v30,49152,v16
v_add_u32 v32,98304,v14
v_add_u32 v34,98304,v16
s_waitcnt vmcnt(16)
s_add_u32 m0,s88,81920
s_add_u32 s61,s82,327680
buffer_load_dwordx4 v12,s[40:43],s61 offen offset:0  lds
s_add_u32 m0,s88,82944
ds_read_b64_tr_b16 a[0:1],v14 offset:0
buffer_load_dwordx4 v13,s[40:43],s61 offen offset:0  lds
s_add_u32 m0,s88,83968
s_add_u32 s61,s82,360448
buffer_load_dwordx4 v12,s[40:43],s61 offen offset:0  lds
s_add_u32 m0,s88,84992
ds_read_b64_tr_b16 a[2:3],v14 offset:512
buffer_load_dwordx4 v13,s[40:43],s61 offen offset:0  lds
ds_read_b64_tr_b16 a[4:5],v14 offset:8
ds_read_b64_tr_b16 a[6:7],v14 offset:520
ds_read_b64_tr_b16 a[8:9],v16 offset:0
ds_read_b64_tr_b16 a[10:11],v16 offset:512
ds_read_b64_tr_b16 a[12:13],v16 offset:8
ds_read_b64_tr_b16 a[14:15],v16 offset:520
s_waitcnt vmcnt(16)
s_add_u32 m0,s88,98304
s_add_u32 s61,s82,393216
buffer_load_dwordx4 v12,s[40:43],s61 offen offset:0  lds
s_add_u32 m0,s88,99328
ds_read_b64_tr_b16 a[16:17],v14 offset:16384
buffer_load_dwordx4 v13,s[40:43],s61 offen offset:0  lds
s_add_u32 m0,s88,100352
s_add_u32 s61,s82,425984
buffer_load_dwordx4 v12,s[40:43],s61 offen offset:0  lds
s_add_u32 m0,s88,101376
ds_read_b64_tr_b16 a[18:19],v14 offset:16896
buffer_load_dwordx4 v13,s[40:43],s61 offen offset:0  lds
ds_read_b64_tr_b16 a[20:21],v14 offset:16392
ds_read_b64_tr_b16 a[22:23],v14 offset:16904
ds_read_b64_tr_b16 a[24:25],v16 offset:16384
ds_read_b64_tr_b16 a[26:27],v16 offset:16896
ds_read_b64_tr_b16 a[28:29],v16 offset:16392
ds_read_b64_tr_b16 a[30:31],v16 offset:16904
s_waitcnt lgkmcnt(8)
s_waitcnt vmcnt(16)
s_add_u32 m0,s88,0
s_add_u32 s61,s82,458752
buffer_load_dwordx4 v12,s[40:43],s61 offen offset:0  lds
s_add_u32 m0,s88,1024
ds_read_b64_tr_b16 a[32:33],v14 offset:32768
buffer_load_dwordx4 v13,s[40:43],s61 offen offset:0  lds
s_add_u32 m0,s88,2048
s_add_u32 s61,s82,491520
buffer_load_dwordx4 v12,s[40:43],s61 offen offset:0  lds
s_add_u32 m0,s88,3072
ds_read_b64_tr_b16 a[34:35],v14 offset:33280
buffer_load_dwordx4 v13,s[40:43],s61 offen offset:0  lds
ds_read_b64_tr_b16 a[36:37],v14 offset:32776
ds_read_b64_tr_b16 a[38:39],v14 offset:33288
ds_read_b64_tr_b16 a[40:41],v16 offset:32768
ds_read_b64_tr_b16 a[42:43],v16 offset:33280
ds_read_b64_tr_b16 a[44:45],v16 offset:32776
ds_read_b64_tr_b16 a[46:47],v16 offset:33288
s_waitcnt lgkmcnt(8)
s_waitcnt vmcnt(16)
s_add_u32 m0,s88,16384
s_add_u32 s61,s82,524288
buffer_load_dwordx4 v12,s[40:43],s61 offen offset:0  lds
s_add_u32 m0,s88,17408
ds_read_b64_tr_b16 a[48:49],v28 offset:0
buffer_load_dwordx4 v13,s[40:43],s61 offen offset:0  lds
s_add_u32 m0,s88,18432
s_add_u32 s61,s82,557056
buffer_load_dwordx4 v12,s[40:43],s61 offen offset:0  lds
s_add_u32 m0,s88,19456
ds_read_b64_tr_b16 a[50:51],v28 offset:512
buffer_load_dwordx4 v13,s[40:43],s61 offen offset:0  lds
ds_read_b64_tr_b16 a[52:53],v28 offset:8
ds_read_b64_tr_b16 a[54:55],v28 offset:520
ds_read_b64_tr_b16 a[56:57],v30 offset:0
ds_read_b64_tr_b16 a[58:59],v30 offset:512
ds_read_b64_tr_b16 a[60:61],v30 offset:8
ds_read_b64_tr_b16 a[62:63],v30 offset:520
s_waitcnt lgkmcnt(8)
s_waitcnt vmcnt(16)
s_add_u32 m0,s88,32768
s_add_u32 s61,s82,589824
buffer_load_dwordx4 v12,s[40:43],s61 offen offset:0  lds
s_add_u32 m0,s88,33792
ds_read_b64_tr_b16 a[64:65],v28 offset:16384
buffer_load_dwordx4 v13,s[40:43],s61 offen offset:0  lds
s_add_u32 m0,s88,34816
s_add_u32 s61,s82,622592
buffer_load_dwordx4 v12,s[40:43],s61 offen offset:0  lds
s_add_u32 m0,s88,35840
ds_read_b64_tr_b16 a[66:67],v28 offset:16896
buffer_load_dwordx4 v13,s[40:43],s61 offen offset:0  lds
ds_read_b64_tr_b16 a[68:69],v28 offset:16392
ds_read_b64_tr_b16 a[70:71],v28 offset:16904
ds_read_b64_tr_b16 a[72:73],v30 offset:16384
ds_read_b64_tr_b16 a[74:75],v30 offset:16896
ds_read_b64_tr_b16 a[76:77],v30 offset:16392
ds_read_b64_tr_b16 a[78:79],v30 offset:16904
s_waitcnt lgkmcnt(8)
s_waitcnt vmcnt(16)
s_add_u32 m0,s88,49152
s_add_u32 s61,s82,655360
buffer_load_dwordx4 v12,s[40:43],s61 offen offset:0  lds)"
R"(
s_add_u32 m0,s88,50176
ds_read_b64_tr_b16 a[80:81],v28 offset:32768
buffer_load_dwordx4 v13,s[40:43],s61 offen offset:0  lds
s_add_u32 m0,s88,51200
s_add_u32 s61,s82,688128
buffer_load_dwordx4 v12,s[40:43],s61 offen offset:0  lds
s_add_u32 m0,s88,52224
ds_read_b64_tr_b16 a[82:83],v28 offset:33280
buffer_load_dwordx4 v13,s[40:43],s61 offen offset:0  lds
ds_read_b64_tr_b16 a[84:85],v28 offset:32776
ds_read_b64_tr_b16 a[86:87],v28 offset:33288
ds_read_b64_tr_b16 a[88:89],v30 offset:32768
ds_read_b64_tr_b16 a[90:91],v30 offset:33280
ds_read_b64_tr_b16 a[92:93],v30 offset:32776
ds_read_b64_tr_b16 a[94:95],v30 offset:33288
s_waitcnt lgkmcnt(8)
s_waitcnt vmcnt(16)
s_add_u32 m0,s88,65536
s_add_u32 s61,s82,720896
buffer_load_dwordx4 v12,s[40:43],s61 offen offset:0  lds
s_add_u32 m0,s88,66560
ds_read_b64_tr_b16 a[96:97],v32 offset:0
buffer_load_dwordx4 v13,s[40:43],s61 offen offset:0  lds
s_add_u32 m0,s88,67584
s_add_u32 s61,s82,753664
buffer_load_dwordx4 v12,s[40:43],s61 offen offset:0  lds
s_add_u32 m0,s88,68608
ds_read_b64_tr_b16 a[98:99],v32 offset:512
buffer_load_dwordx4 v13,s[40:43],s61 offen offset:0  lds
ds_read_b64_tr_b16 a[100:101],v32 offset:8
ds_read_b64_tr_b16 a[102:103],v32 offset:520
ds_read_b64_tr_b16 a[104:105],v34 offset:0
ds_read_b64_tr_b16 a[106:107],v34 offset:512
ds_read_b64_tr_b16 a[108:109],v34 offset:8
ds_read_b64_tr_b16 a[110:111],v34 offset:520
s_waitcnt lgkmcnt(8)
s_waitcnt vmcnt(16)
s_add_u32 m0,s88,81920
s_add_u32 s61,s82,786432
buffer_load_dwordx4 v12,s[40:43],s61 offen offset:0  lds
s_add_u32 m0,s88,82944
ds_read_b64_tr_b16 a[112:113],v14 offset:0
buffer_load_dwordx4 v13,s[40:43],s61 offen offset:0  lds
s_add_u32 m0,s88,83968
s_add_u32 s61,s82,819200
buffer_load_dwordx4 v12,s[40:43],s61 offen offset:0  lds
s_add_u32 m0,s88,84992
ds_read_b64_tr_b16 a[114:115],v14 offset:512
buffer_load_dwordx4 v13,s[40:43],s61 offen offset:0  lds
ds_read_b64_tr_b16 a[116:117],v14 offset:8
ds_read_b64_tr_b16 a[118:119],v14 offset:520
ds_read_b64_tr_b16 a[120:121],v16 offset:0
ds_read_b64_tr_b16 a[122:123],v16 offset:512
ds_read_b64_tr_b16 a[124:125],v16 offset:8
ds_read_b64_tr_b16 a[126:127],v16 offset:520
s_waitcnt lgkmcnt(8)
s_waitcnt vmcnt(16)
s_add_u32 m0,s88,98304
s_add_u32 s61,s82,851968
buffer_load_dwordx4 v12,s[40:43],s61 offen offset:0  lds
s_add_u32 m0,s88,99328
ds_read_b64_tr_b16 a[128:129],v14 offset:16384
buffer_load_dwordx4 v13,s[40:43],s61 offen offset:0  lds
s_add_u32 m0,s88,100352
s_add_u32 s61,s82,884736
buffer_load_dwordx4 v12,s[40:43],s61 offen offset:0  lds
s_add_u32 m0,s88,101376
ds_read_b64_tr_b16 a[130:131],v14 offset:16896
buffer_load_dwordx4 v13,s[40:43],s61 offen offset:0  lds
ds_read_b64_tr_b16 a[132:133],v14 offset:16392
ds_read_b64_tr_b16 a[134:135],v14 offset:16904
ds_read_b64_tr_b16 a[136:137],v16 offset:16384
ds_read_b64_tr_b16 a[138:139],v16 offset:16896
ds_read_b64_tr_b16 a[140:141],v16 offset:16392
ds_read_b64_tr_b16 a[142:143],v16 offset:16904
s_waitcnt lgkmcnt(8)
s_waitcnt vmcnt(16)
s_add_u32 m0,s88,0
s_add_u32 s61,s82,917504
buffer_load_dwordx4 v12,s[40:43],s61 offen offset:0  lds
s_add_u32 m0,s88,1024
ds_read_b64_tr_b16 a[144:145],v14 offset:32768
buffer_load_dwordx4 v13,s[40:43],s61 offen offset:0  lds
s_add_u32 m0,s88,2048
s_add_u32 s61,s82,950272
buffer_load_dwordx4 v12,s[40:43],s61 offen offset:0  lds
s_add_u32 m0,s88,3072
ds_read_b64_tr_b16 a[146:147],v14 offset:33280
buffer_load_dwordx4 v13,s[40:43],s61 offen offset:0  lds
ds_read_b64_tr_b16 a[148:149],v14 offset:32776
ds_read_b64_tr_b16 a[150:151],v14 offset:33288
ds_read_b64_tr_b16 a[152:153],v16 offset:32768
ds_read_b64_tr_b16 a[154:155],v16 offset:33280
ds_read_b64_tr_b16 a[156:157],v16 offset:32776
ds_read_b64_tr_b16 a[158:159],v16 offset:33288
s_waitcnt lgkmcnt(8)
s_waitcnt vmcnt(16)
s_add_u32 m0,s88,16384
s_add_u32 s61,s82,983040
buffer_load_dwordx4 v12,s[40:43],s61 offen offset:0  lds
s_add_u32 m0,s88,17408
ds_read_b64_tr_b16 a[160:161],v28 offset:0
buffer_load_dwordx4 v13,s[40:43],s61 offen offset:0  lds
s_add_u32 m0,s88,18432
s_add_u32 s61,s82,1015808
buffer_load_dwordx4 v12,s[40:43],s61 offen offset:0  lds
s_add_u32 m0,s88,19456
ds_read_b64_tr_b16 a[162:163],v28 offset:512
buffer_load_dwordx4 v13,s[40:43],s61 offen offset:0  lds
ds_read_b64_tr_b16 a[164:165],v28 offset:8
ds_read_b64_tr_b16 a[166:167],v28 offset:520
ds_read_b64_tr_b16 a[168:169],v30 offset:0
ds_read_b64_tr_b16 a[170:171],v30 offset:512
ds_read_b64_tr_b16 a[172:173],v30 offset:8
ds_read_b64_tr_b16 a[174:175],v30 offset:520
s_waitcnt lgkmcnt(8)
s_waitcnt vmcnt(16)
s_add_u32 m0,s88,32768
s_add_u32 s61,s82,1048576
buffer_load_dwordx4 v12,s[40:43],s61 offen offset:0  lds
s_add_u32 m0,s88,33792
ds_read_b64_tr_b16 a[176:177],v28 offset:16384
buffer_load_dwordx4 v13,s[40:43],s61 offen offset:0  lds
s_add_u32 m0,s88,34816
s_add_u32 s61,s82,1081344
buffer_load_dwordx4 v12,s[40:43],s61 offen offset:0  lds
s_add_u32 m0,s88,35840
ds_read_b64_tr_b16 a[178:179],v28 offset:16896
buffer_load_dwordx4 v13,s[40:43],s61 offen offset:0  lds
ds_read_b64_tr_b16 a[180:181],v28 offset:16392
ds_read_b64_tr_b16 a[182:183],v28 offset:16904
ds_read_b64_tr_b16 a[184:185],v30 offset:16384
ds_read_b64_tr_b16 a[186:187],v30 offset:16896
ds_read_b64_tr_b16 a[188:189],v30 offset:16392
ds_read_b64_tr_b16 a[190:191],v30 offset:16904
s_waitcnt lgkmcnt(8)
s_waitcnt vmcnt(16)
s_add_u32 m0,s88,49152
s_add_u32 s61,s82,1114112
buffer_load_dwordx4 v12,s[40:43],s61 offen offset:0  lds
s_add_u32 m0,s88,50176
ds_read_b64_tr_b16 a[192:193],v28 offset:32768
buffer_load_dwordx4 v13,s[40:43],s61 offen offset:0  lds
s_add_u32 m0,s88,51200
s_add_u32 s61,s82,1146880
buffer_load_dwordx4 v12,s[40:43],s61 offen offset:0  lds
s_add_u32 m0,s88,52224
ds_read_b64_tr_b16 a[194:195],v28 offset:33280
buffer_load_dwordx4 v13,s[40:43],s61 offen offset:0  lds
ds_read_b64_tr_b16 a[196:197],v28 offset:32776
ds_read_b64_tr_b16 a[198:199],v28 offset:33288
ds_read_b64_tr_b16 a[200:201],v30 offset:32768
ds_read_b64_tr_b16 a[202:203],v30 offset:33280
ds_read_b64_tr_b16 a[204:205],v30 offset:32776
ds_read_b64_tr_b16 a[206:207],v30 offset:33288
s_waitcnt lgkmcnt(8)
s_waitcnt vmcnt(16)
s_add_u32 m0,s88,65536
s_add_u32 s61,s82,1179648
buffer_load_dwordx4 v12,s[40:43],s61 offen offset:0  lds
s_add_u32 m0,s88,66560
ds_read_b64_tr_b16 a[208:209],v32 offset:0
buffer_load_dwordx4 v13,s[40:43],s61 offen offset:0  lds
s_add_u32 m0,s88,67584
s_add_u32 s61,s82,1212416
buffer_load_dwordx4 v12,s[40:43],s61 offen offset:0  lds
s_add_u32 m0,s88,68608
ds_read_b64_tr_b16 a[210:211],v32 offset:512
buffer_load_dwordx4 v13,s[40:43],s61 offen offset:0  lds
ds_read_b64_tr_b16 a[212:213],v32 offset:8
ds_read_b64_tr_b16 a[214:215],v32 offset:520
ds_read_b64_tr_b16 a[216:217],v34 offset:0
ds_read_b64_tr_b16 a[218:219],v34 offset:512
ds_read_b64_tr_b16 a[220:221],v34 offset:8
ds_read_b64_tr_b16 a[222:223],v34 offset:520
s_waitcnt lgkmcnt(8)
s_waitcnt vmcnt(16)
s_add_u32 m0,s88,81920
s_add_u32 s61,s82,1245184
buffer_load_dwordx4 v12,s[40:43],s61 offen offset:0  lds
s_add_u32 m0,s88,82944
ds_read_b64_tr_b16 a[224:225],v14 offset:0
buffer_load_dwordx4 v13,s[40:43],s61 offen offset:0  lds
s_add_u32 m0,s88,83968
s_add_u32 s61,s82,1277952
buffer_load_dwordx4 v12,s[40:43],s61 offen offset:0  lds
s_add_u32 m0,s88,84992
ds_read_b64_tr_b16 a[226:227],v14 offset:512
buffer_load_dwordx4 v13,s[40:43],s61 offen offset:0  lds
ds_read_b64_tr_b16 a[228:229],v14 offset:8
ds_read_b64_tr_b16 a[230:231],v14 offset:520
ds_read_b64_tr_b16 a[232:233],v16 offset:0
ds_read_b64_tr_b16 a[234:235],v16 offset:512
ds_read_b64_tr_b16 a[236:237],v16 offset:8
ds_read_b64_tr_b16 a[238:239],v16 offset:520
s_waitcnt lgkmcnt(8)
s_waitcnt vmcnt(16)
s_add_u32 m0,s88,98304
s_add_u32 s61,s82,1310720
buffer_load_dwordx4 v12,s[40:43],s61 offen offset:0  lds)"
R"(
s_add_u32 m0,s88,99328
ds_read_b64_tr_b16 a[240:241],v14 offset:16384
buffer_load_dwordx4 v13,s[40:43],s61 offen offset:0  lds
s_add_u32 m0,s88,100352
s_add_u32 s61,s82,1343488
buffer_load_dwordx4 v12,s[40:43],s61 offen offset:0  lds
s_add_u32 m0,s88,101376
ds_read_b64_tr_b16 a[242:243],v14 offset:16896
buffer_load_dwordx4 v13,s[40:43],s61 offen offset:0  lds
ds_read_b64_tr_b16 a[244:245],v14 offset:16392
ds_read_b64_tr_b16 a[246:247],v14 offset:16904
ds_read_b64_tr_b16 a[248:249],v16 offset:16384
ds_read_b64_tr_b16 a[250:251],v16 offset:16896
ds_read_b64_tr_b16 a[252:253],v16 offset:16392
ds_read_b64_tr_b16 a[254:255],v16 offset:16904
s_waitcnt lgkmcnt(8)
s_waitcnt vmcnt(16)
s_add_u32 m0,s88,0
s_add_u32 s61,s82,1376256
buffer_load_dwordx4 v12,s[40:43],s61 offen offset:0  lds
s_add_u32 m0,s88,1024
ds_read_b64_tr_b16 v[100:101],v14 offset:32768
buffer_load_dwordx4 v13,s[40:43],s61 offen offset:0  lds
s_add_u32 m0,s88,2048
s_add_u32 s61,s82,1409024
buffer_load_dwordx4 v12,s[40:43],s61 offen offset:0  lds
s_add_u32 m0,s88,3072
ds_read_b64_tr_b16 v[102:103],v14 offset:33280
buffer_load_dwordx4 v13,s[40:43],s61 offen offset:0  lds
ds_read_b64_tr_b16 v[104:105],v14 offset:32776
ds_read_b64_tr_b16 v[106:107],v14 offset:33288
ds_read_b64_tr_b16 v[108:109],v16 offset:32768
ds_read_b64_tr_b16 v[110:111],v16 offset:33280
ds_read_b64_tr_b16 v[112:113],v16 offset:32776
ds_read_b64_tr_b16 v[114:115],v16 offset:33288
s_waitcnt lgkmcnt(8)
s_waitcnt vmcnt(16)
s_add_u32 m0,s88,16384
s_add_u32 s61,s82,1441792
buffer_load_dwordx4 v12,s[40:43],s61 offen offset:0  lds
s_add_u32 m0,s88,17408
ds_read_b64_tr_b16 v[116:117],v28 offset:0
buffer_load_dwordx4 v13,s[40:43],s61 offen offset:0  lds
s_add_u32 m0,s88,18432
s_add_u32 s61,s82,1474560
buffer_load_dwordx4 v12,s[40:43],s61 offen offset:0  lds
s_add_u32 m0,s88,19456
ds_read_b64_tr_b16 v[118:119],v28 offset:512
buffer_load_dwordx4 v13,s[40:43],s61 offen offset:0  lds
ds_read_b64_tr_b16 v[120:121],v28 offset:8
ds_read_b64_tr_b16 v[122:123],v28 offset:520
ds_read_b64_tr_b16 v[124:125],v30 offset:0
ds_read_b64_tr_b16 v[126:127],v30 offset:512
ds_read_b64_tr_b16 v[128:129],v30 offset:8
ds_read_b64_tr_b16 v[130:131],v30 offset:520
s_waitcnt lgkmcnt(8)
s_waitcnt vmcnt(16)
s_add_u32 m0,s88,32768
s_add_u32 s61,s82,1507328
buffer_load_dwordx4 v12,s[40:43],s61 offen offset:0  lds
s_add_u32 m0,s88,33792
ds_read_b64_tr_b16 v[132:133],v28 offset:16384
buffer_load_dwordx4 v13,s[40:43],s61 offen offset:0  lds
s_add_u32 m0,s88,34816
s_add_u32 s61,s82,1540096
buffer_load_dwordx4 v12,s[40:43],s61 offen offset:0  lds
s_add_u32 m0,s88,35840
ds_read_b64_tr_b16 v[134:135],v28 offset:16896
buffer_load_dwordx4 v13,s[40:43],s61 offen offset:0  lds
ds_read_b64_tr_b16 v[136:137],v28 offset:16392
ds_read_b64_tr_b16 v[138:139],v28 offset:16904
ds_read_b64_tr_b16 v[140:141],v30 offset:16384
ds_read_b64_tr_b16 v[142:143],v30 offset:16896
ds_read_b64_tr_b16 v[144:145],v30 offset:16392
ds_read_b64_tr_b16 v[146:147],v30 offset:16904
s_waitcnt lgkmcnt(8)
s_waitcnt vmcnt(16)
s_add_u32 m0,s88,49152
s_add_u32 s61,s82,1572864
buffer_load_dwordx4 v12,s[40:43],s61 offen offset:0  lds
s_add_u32 m0,s88,50176
ds_read_b64_tr_b16 v[148:149],v28 offset:32768
buffer_load_dwordx4 v13,s[40:43],s61 offen offset:0  lds
s_add_u32 m0,s88,51200
s_add_u32 s61,s82,1605632
buffer_load_dwordx4 v12,s[40:43],s61 offen offset:0  lds
s_add_u32 m0,s88,52224
ds_read_b64_tr_b16 v[150:151],v28 offset:33280
buffer_load_dwordx4 v13,s[40:43],s61 offen offset:0  lds
ds_read_b64_tr_b16 v[152:153],v28 offset:32776
ds_read_b64_tr_b16 v[154:155],v28 offset:33288
ds_read_b64_tr_b16 v[156:157],v30 offset:32768
ds_read_b64_tr_b16 v[158:159],v30 offset:33280
ds_read_b64_tr_b16 v[52:53],v30 offset:32776
ds_read_b64_tr_b16 v[54:55],v30 offset:33288
s_waitcnt lgkmcnt(8)
s_waitcnt vmcnt(16)
s_add_u32 m0,s88,65536
s_add_u32 s61,s82,1638400
buffer_load_dwordx4 v12,s[40:43],s61 offen offset:0  lds
s_add_u32 m0,s88,66560
ds_read_b64_tr_b16 v[160:161],v32 offset:0
buffer_load_dwordx4 v13,s[40:43],s61 offen offset:0  lds
s_add_u32 m0,s88,67584
s_add_u32 s61,s82,1671168
buffer_load_dwordx4 v12,s[40:43],s61 offen offset:0  lds
s_add_u32 m0,s88,68608
ds_read_b64_tr_b16 v[162:163],v32 offset:512
buffer_load_dwordx4 v13,s[40:43],s61 offen offset:0  lds
ds_read_b64_tr_b16 v[164:165],v32 offset:8
ds_read_b64_tr_b16 v[166:167],v32 offset:520
ds_read_b64_tr_b16 v[168:169],v34 offset:0
ds_read_b64_tr_b16 v[170:171],v34 offset:512
ds_read_b64_tr_b16 v[44:45],v34 offset:8
ds_read_b64_tr_b16 v[46:47],v34 offset:520
s_waitcnt lgkmcnt(8)
ds_write_b128 v9,v[52:55] offset:0
s_waitcnt vmcnt(16)
s_add_u32 m0,s88,81920
s_add_u32 s61,s82,1703936
buffer_load_dwordx4 v12,s[40:43],s61 offen offset:0  lds
s_add_u32 m0,s88,82944
ds_read_b64_tr_b16 v[172:173],v14 offset:0
buffer_load_dwordx4 v13,s[40:43],s61 offen offset:0  lds
s_add_u32 m0,s88,83968
s_add_u32 s61,s82,1736704
buffer_load_dwordx4 v12,s[40:43],s61 offen offset:0  lds
s_add_u32 m0,s88,84992
ds_read_b64_tr_b16 v[174:175],v14 offset:512
buffer_load_dwordx4 v13,s[40:43],s61 offen offset:0  lds
ds_read_b64_tr_b16 v[176:177],v14 offset:8
ds_read_b64_tr_b16 v[178:179],v14 offset:520
ds_read_b64_tr_b16 v[180:181],v16 offset:0
ds_read_b64_tr_b16 v[182:183],v16 offset:512
ds_read_b64_tr_b16 v[52:53],v16 offset:8
ds_read_b64_tr_b16 v[54:55],v16 offset:520
s_waitcnt lgkmcnt(8)
ds_write_b128 v9,v[44:47] offset:1024
s_waitcnt vmcnt(16)
s_add_u32 m0,s88,98304
s_add_u32 s61,s82,1769472
buffer_load_dwordx4 v12,s[40:43],s61 offen offset:0  lds
s_add_u32 m0,s88,99328
ds_read_b64_tr_b16 v[184:185],v14 offset:16384
buffer_load_dwordx4 v13,s[40:43],s61 offen offset:0  lds
s_add_u32 m0,s88,100352
s_add_u32 s61,s82,1802240
buffer_load_dwordx4 v12,s[40:43],s61 offen offset:0  lds
s_add_u32 m0,s88,101376
ds_read_b64_tr_b16 v[186:187],v14 offset:16896
buffer_load_dwordx4 v13,s[40:43],s61 offen offset:0  lds
ds_read_b64_tr_b16 v[188:189],v14 offset:16392
ds_read_b64_tr_b16 v[190:191],v14 offset:16904
ds_read_b64_tr_b16 v[192:193],v16 offset:16384
ds_read_b64_tr_b16 v[194:195],v16 offset:16896
ds_read_b64_tr_b16 v[44:45],v16 offset:16392
ds_read_b64_tr_b16 v[46:47],v16 offset:16904
s_waitcnt lgkmcnt(8)
ds_write_b128 v9,v[52:55] offset:2048
s_waitcnt vmcnt(16)
ds_read_b64_tr_b16 v[196:197],v14 offset:32768
ds_read_b64_tr_b16 v[198:199],v14 offset:33280
ds_read_b64_tr_b16 v[200:201],v14 offset:32776
ds_read_b64_tr_b16 v[202:203],v14 offset:33288
ds_read_b64_tr_b16 v[204:205],v16 offset:32768
ds_read_b64_tr_b16 v[206:207],v16 offset:33280
ds_read_b64_tr_b16 v[52:53],v16 offset:32776
ds_read_b64_tr_b16 v[54:55],v16 offset:33288
s_getpc_b64 s[46:47]
Lg4_cp_%=:
s_add_u32 s46,s46,Lg4_init_%=-Lg4_cp_%=
s_addc_u32 s47,s47,0
s_lshl_b32 s68,s54,13
v_lshl_add_u32 v76,v2,3,s68
v_min_u32 v77,Lg4_done_%=-Lg4_init_%=,v76
global_load_dword v80,v77,s[46:47]
s_waitcnt lgkmcnt(8)
ds_write_b128 v9,v[44:47] offset:3072
s_waitcnt vmcnt(13)
ds_read_b64_tr_b16 v[208:209],v28 offset:0
ds_read_b64_tr_b16 v[210:211],v28 offset:512
ds_read_b64_tr_b16 v[212:213],v28 offset:8
ds_read_b64_tr_b16 v[214:215],v28 offset:520
ds_read_b64_tr_b16 v[216:217],v30 offset:0
ds_read_b64_tr_b16 v[218:219],v30 offset:512
ds_read_b64_tr_b16 v[44:45],v30 offset:8
ds_read_b64_tr_b16 v[46:47],v30 offset:520
s_waitcnt lgkmcnt(8)
ds_write_b128 v9,v[52:55] offset:4096
s_waitcnt vmcnt(9)
ds_read_b64_tr_b16 v[220:221],v28 offset:16384
ds_read_b64_tr_b16 v[222:223],v28 offset:16896
ds_read_b64_tr_b16 v[224:225],v28 offset:16392
ds_read_b64_tr_b16 v[226:227],v28 offset:16904
ds_read_b64_tr_b16 v[228:229],v30 offset:16384
ds_read_b64_tr_b16 v[230:231],v30 offset:16896
ds_read_b64_tr_b16 v[52:53],v30 offset:16392
ds_read_b64_tr_b16 v[54:55],v30 offset:16904
s_waitcnt lgkmcnt(8)
ds_write_b128 v9,v[44:47] offset:5120)"
R"(
s_waitcnt vmcnt(5)
ds_read_b64_tr_b16 v[232:233],v28 offset:32768
ds_read_b64_tr_b16 v[234:235],v28 offset:33280
ds_read_b64_tr_b16 v[236:237],v28 offset:32776
ds_read_b64_tr_b16 v[238:239],v28 offset:33288
ds_read_b64_tr_b16 v[240:241],v30 offset:32768
ds_read_b64_tr_b16 v[242:243],v30 offset:33280
ds_read_b64_tr_b16 v[44:45],v30 offset:32776
ds_read_b64_tr_b16 v[46:47],v30 offset:33288
s_waitcnt lgkmcnt(8)
ds_write_b128 v9,v[52:55] offset:6144
s_waitcnt vmcnt(1)
ds_read_b64_tr_b16 v[244:245],v32 offset:0
ds_read_b64_tr_b16 v[246:247],v32 offset:512
ds_read_b64_tr_b16 v[248:249],v32 offset:8
ds_read_b64_tr_b16 v[250:251],v32 offset:520
ds_read_b64_tr_b16 v[252:253],v34 offset:0
ds_read_b64_tr_b16 v[254:255],v34 offset:512
ds_read_b64_tr_b16 v[52:53],v34 offset:8
ds_read_b64_tr_b16 v[54:55],v34 offset:520
s_waitcnt lgkmcnt(8)
ds_write_b128 v9,v[44:47] offset:7168
s_waitcnt lgkmcnt(0)
ds_write_b128 v9,v[52:55] offset:8192
s_waitcnt lgkmcnt(0)
s_barrier
v_readfirstlane_b32 s60,v5
s_nop 3
s_add_u32 s60,s60,1
s_cmp_eq_u32 s60,0
s_cselect_b32 s60,1,s60
Lg4_init_%=:
v_mov_b32 v5,s60
s_mov_b32 s59,536870912
s_add_u32 s57,s56,131072
s_mov_b32 s79,0
s_mov_b32 s75,0x3a800000
s_mov_b32 s76,0xc0135761
s_mov_b32 s77,0x3d372713
s_mov_b32 s44,s59
s_mov_b32 s45,s59
s_mov_b32 s46,s59
s_mov_b32 s47,s59
s_mov_b32 s48,s83
s_add_u32 s49,s48,s55
s_cmp_eq_u32 s49,s56
s_cselect_b32 s49,s57,s49
s_cmp_eq_u32 s49,s58
s_cselect_b32 s49,s59,s49
s_add_u32 s50,s49,s55
s_cmp_eq_u32 s50,s56
s_cselect_b32 s50,s57,s50
s_cmp_eq_u32 s50,s58
s_cselect_b32 s50,s59,s50
s_add_u32 s51,s50,s55
s_cmp_eq_u32 s51,s56
s_cselect_b32 s51,s57,s51
s_cmp_eq_u32 s51,s58
s_cselect_b32 s51,s59,s51
s_add_u32 s52,s51,s55
s_cmp_eq_u32 s52,s56
s_cselect_b32 s52,s57,s52
s_cmp_eq_u32 s52,s58
s_cselect_b32 s52,s59,s52
s_lshl_b32 s63,s54,12
s_mul_i32 s64,s54,3072
s_add_u32 s64,s64,16384
s_lshl_b32 s78,s54,8
s_add_u32 s78,s78,65536
s_getpc_b64 s[90:91]
Lg4_pc_%=:
s_mov_b32 s68,Lg4_d1w0_%=-Lg4_pc_%=
s_cmp_eq_u32 s54,1
s_cselect_b32 s68,Lg4_d1w1_%=-Lg4_pc_%=,s68
s_cmp_eq_u32 s54,2
s_cselect_b32 s68,Lg4_d1w2_%=-Lg4_pc_%=,s68
s_cmp_eq_u32 s54,3
s_cselect_b32 s68,Lg4_d1w3_%=-Lg4_pc_%=,s68
s_add_u32 s92,s90,s68
s_addc_u32 s93,s91,0
s_mov_b32 s68,Lg4_d0w0_%=-Lg4_pc_%=
s_cmp_eq_u32 s54,1
s_cselect_b32 s68,Lg4_d0w1_%=-Lg4_pc_%=,s68
s_cmp_eq_u32 s54,2
s_cselect_b32 s68,Lg4_d0w2_%=-Lg4_pc_%=,s68
s_cmp_eq_u32 s54,3
s_cselect_b32 s68,Lg4_d0w3_%=-Lg4_pc_%=,s68
s_add_u32 s90,s90,s68
s_addc_u32 s91,s91,0
s_mov_b32 s66,s59
s_mov_b32 s65,0
s_mov_b32 s62,s63
s_branch Lg4_slow0_%=
Lg4_d0w0_%=:
v_mfma_f32_16x16x32_bf16 v[12:15],a[0:3],v[44:47],0
s_add_u32 m0,s63,28672
ds_read_b128 v[56:59],v2 offset:3072
v_lshlrev_b32 v60,16,v68
v_mfma_f32_16x16x32_bf16 v[16:19],a[4:7],v[44:47],0
buffer_load_dwordx4 v0,s[36:39],s61 offen offset:0 sc1 lds
v_mfma_f32_16x16x32_bf16 v[20:23],a[8:11],v[44:47],0
s_max_u32 s73,s47,s65
v_and_b32 v61,0xffff0000,v68
v_lshlrev_b32 v62,16,v69
v_mfma_f32_16x16x32_bf16 v[24:27],a[12:15],v[44:47],0
s_waitcnt lgkmcnt(2)
v_and_b32 v63,0xffff0000,v69
v_lshlrev_b32 v64,16,v70
v_mfma_f32_16x16x32_bf16 v[12:15],a[16:19],v[48:51],v[12:15]
s_mov_b32 s65,0
ds_read_b128 v[44:47],v2 offset:4096
v_and_b32 v65,0xffff0000,v70
v_mfma_f32_16x16x32_bf16 v[16:19],a[20:23],v[48:51],v[16:19]
buffer_load_dwordx4 v0,s[36:39],s61 offen offset:1024 sc1 lds
v_mfma_f32_16x16x32_bf16 v[20:23],a[24:27],v[48:51],v[20:23]
s_add_u32 s53,s52,s55
v_lshlrev_b32 v66,16,v71
v_and_b32 v67,0xffff0000,v71
v_mfma_f32_16x16x32_bf16 v[24:27],a[28:31],v[48:51],v[24:27]
s_waitcnt lgkmcnt(2)
v_add_f32 v28,v28,v60
v_add_f32 v29,v29,v61
v_mfma_f32_16x16x32_bf16 v[12:15],a[32:35],v[52:55],v[12:15]
s_cmp_eq_u32 s53,s56
ds_read_b128 v[48:51],v2 offset:5120
v_add_f32 v30,v30,v62
v_mfma_f32_16x16x32_bf16 v[16:19],a[36:39],v[52:55],v[16:19]
buffer_load_dwordx4 v0,s[36:39],s61 offen offset:2048 sc1 lds
v_mfma_f32_16x16x32_bf16 v[20:23],a[40:43],v[52:55],v[20:23]
s_cselect_b32 s53,s57,s53
v_add_f32 v31,v31,v63
v_add_f32 v32,v32,v64
v_mfma_f32_16x16x32_bf16 v[24:27],a[44:47],v[52:55],v[24:27]
s_waitcnt lgkmcnt(2)
v_add_f32 v33,v33,v65
v_add_f32 v34,v34,v66
v_mfma_f32_16x16x32_bf16 v[12:15],a[48:51],v[56:59],v[12:15]
s_cmp_eq_u32 s53,s58
ds_read_b128 v[52:55],v2 offset:6144
v_add_f32 v35,v35,v67
v_mfma_f32_16x16x32_bf16 v[16:19],a[52:55],v[56:59],v[16:19]
buffer_load_dwordx4 v0,s[36:39],s61 offen offset:3072 sc1 lds
v_mfma_f32_16x16x32_bf16 v[20:23],a[56:59],v[56:59],v[20:23]
s_cselect_b32 s53,s59,s53
v_cvt_pk_bf16_f32 v28,v28,v29
v_cvt_pk_bf16_f32 v29,v30,v31
v_mfma_f32_16x16x32_bf16 v[24:27],a[60:63],v[56:59],v[24:27]
s_waitcnt lgkmcnt(2)
v_cvt_pk_bf16_f32 v30,v32,v33
v_cvt_pk_bf16_f32 v31,v34,v35
v_mfma_f32_16x16x32_bf16 v[12:15],a[64:67],v[44:47],v[12:15]
s_cmp_ge_u32 s66,s59
ds_read_b128 v[56:59],v2 offset:7168
v_lshlrev_b32 v60,16,v72
v_mfma_f32_16x16x32_bf16 v[16:19],a[68:71],v[44:47],v[16:19]
s_cselect_b32 s86,s49,s50
v_and_b32 v61,0xffff0000,v72
v_lshlrev_b32 v62,16,v73
v_mfma_f32_16x16x32_bf16 v[20:23],a[72:75],v[44:47],v[20:23]
buffer_store_dwordx4 v[28:31],v3,s[36:39],s73 offen offset:0 sc0 sc1
v_and_b32 v63,0xffff0000,v73
v_lshlrev_b32 v64,16,v74
v_and_b32 v65,0xffff0000,v74
v_mfma_f32_16x16x32_bf16 v[24:27],a[76:79],v[44:47],v[24:27]
s_waitcnt lgkmcnt(2)
v_lshlrev_b32 v66,16,v75
v_and_b32 v67,0xffff0000,v75
v_mfma_f32_16x16x32_bf16 v[12:15],a[80:83],v[48:51],v[12:15]
s_mul_i32 s68,s86,5
ds_read_b128 v[44:47],v2 offset:8192
v_add_f32 v36,v36,v60
v_mfma_f32_16x16x32_bf16 v[16:19],a[84:87],v[48:51],v[16:19]
s_lshr_b32 s68,s68,6
v_add_f32 v37,v37,v61
v_add_f32 v38,v38,v62
v_mfma_f32_16x16x32_bf16 v[20:23],a[88:91],v[48:51],v[20:23]
s_add_u32 m0,s78,4096
v_add_f32 v39,v39,v63
v_add_f32 v40,v40,v64
v_mfma_f32_16x16x32_bf16 v[24:27],a[92:95],v[48:51],v[24:27]
s_waitcnt lgkmcnt(2)
v_add_f32 v41,v41,v65
v_add_f32 v42,v42,v66
v_mfma_f32_16x16x32_bf16 v[12:15],a[96:99],v[52:55],v[12:15]
ds_read_b128 v[48:51],v2 offset:9216
v_add_f32 v43,v43,v67
v_mfma_f32_16x16x32_bf16 v[16:19],a[100:103],v[52:55],v[16:19]
v_cvt_pk_bf16_f32 v36,v36,v37
v_cvt_pk_bf16_f32 v37,v38,v39
v_mfma_f32_16x16x32_bf16 v[20:23],a[104:107],v[52:55],v[20:23]
v_cvt_pk_bf16_f32 v38,v40,v41
v_cvt_pk_bf16_f32 v39,v42,v43
v_mfma_f32_16x16x32_bf16 v[24:27],a[108:111],v[52:55],v[24:27]
s_waitcnt lgkmcnt(2)
v_mfma_f32_16x16x32_bf16 v[12:15],a[112:115],v[56:59],v[12:15]
buffer_store_dwordx4 v[36:39],v3,s[36:39],s73 offen offset:512 sc0 sc1
ds_read_b128 v[52:55],v2 offset:10240
v_mfma_f32_16x16x32_bf16 v[16:19],a[116:119],v[56:59],v[16:19]
v_mfma_f32_16x16x32_bf16 v[20:23],a[120:123],v[56:59],v[20:23]
v_mfma_f32_16x16x32_bf16 v[24:27],a[124:127],v[56:59],v[24:27])"
R"(
s_waitcnt lgkmcnt(2)
v_mfma_f32_16x16x32_bf16 v[12:15],a[128:131],v[44:47],v[12:15]
ds_read_b128 v[56:59],v2 offset:11264
v_mfma_f32_16x16x32_bf16 v[16:19],a[132:135],v[44:47],v[16:19]
buffer_load_dwordx4 v[68:71],v8,s[36:39],s48 offen offset:0 sc1
v_mfma_f32_16x16x32_bf16 v[20:23],a[136:139],v[44:47],v[20:23]
v_mfma_f32_16x16x32_bf16 v[24:27],a[140:143],v[44:47],v[24:27]
s_waitcnt lgkmcnt(2)
v_mfma_f32_16x16x32_bf16 v[12:15],a[144:147],v[48:51],v[12:15]
ds_read_b128 v[44:47],v2 offset:12288
v_mfma_f32_16x16x32_bf16 v[16:19],a[148:151],v[48:51],v[16:19]
buffer_load_dwordx4 v[72:75],v8,s[36:39],s48 offen offset:1024 sc1
v_mfma_f32_16x16x32_bf16 v[20:23],a[152:155],v[48:51],v[20:23]
v_mfma_f32_16x16x32_bf16 v[24:27],a[156:159],v[48:51],v[24:27]
s_waitcnt lgkmcnt(2)
v_mfma_f32_16x16x32_bf16 v[12:15],a[160:163],v[52:55],v[12:15]
ds_read_b128 v[48:51],v2 offset:13312
v_mfma_f32_16x16x32_bf16 v[16:19],a[164:167],v[52:55],v[16:19]
v_mfma_f32_16x16x32_bf16 v[20:23],a[168:171],v[52:55],v[20:23]
v_mfma_f32_16x16x32_bf16 v[24:27],a[172:175],v[52:55],v[24:27]
s_waitcnt lgkmcnt(2)
buffer_load_dword v6,s[36:39],s68 offen offset:0 sc1 lds
v_mfma_f32_16x16x32_bf16 v[12:15],a[176:179],v[56:59],v[12:15]
s_add_u32 m0,s64,28672
ds_read_b128 v[52:55],v2 offset:14336
v_mfma_f32_16x16x32_bf16 v[16:19],a[180:183],v[56:59],v[16:19]
v_mfma_f32_16x16x32_bf16 v[20:23],a[184:187],v[56:59],v[20:23]
v_mfma_f32_16x16x32_bf16 v[24:27],a[188:191],v[56:59],v[24:27]
s_waitcnt lgkmcnt(2)
v_mfma_f32_16x16x32_bf16 v[12:15],a[192:195],v[44:47],v[12:15]
ds_read_b128 v[56:59],v2 offset:15360
v_mfma_f32_16x16x32_bf16 v[16:19],a[196:199],v[44:47],v[16:19]
v_mfma_f32_16x16x32_bf16 v[20:23],a[200:203],v[44:47],v[20:23]
s_waitcnt vmcnt(9)
v_mfma_f32_16x16x32_bf16 v[24:27],a[204:207],v[44:47],v[24:27]
s_waitcnt lgkmcnt(2)
s_barrier
v_mfma_f32_16x16x32_bf16 v[12:15],a[208:211],v[48:51],v[12:15]
ds_read_b128 v[44:47],v2 offset:16384
v_mfma_f32_16x16x32_bf16 v[16:19],a[212:215],v[48:51],v[16:19]
buffer_load_dwordx4 v1,s[36:39],s61 offen offset:0 sc1 lds
v_mfma_f32_16x16x32_bf16 v[20:23],a[216:219],v[48:51],v[20:23]
v_mfma_f32_16x16x32_bf16 v[24:27],a[220:223],v[48:51],v[24:27]
s_waitcnt lgkmcnt(2)
v_mfma_f32_16x16x32_bf16 v[12:15],a[224:227],v[52:55],v[12:15]
ds_read_b128 v[48:51],v2 offset:17408
v_mfma_f32_16x16x32_bf16 v[16:19],a[228:231],v[52:55],v[16:19]
v_mfma_f32_16x16x32_bf16 v[20:23],a[232:235],v[52:55],v[20:23]
v_mfma_f32_16x16x32_bf16 v[24:27],a[236:239],v[52:55],v[24:27]
s_waitcnt lgkmcnt(2)
buffer_load_dwordx4 v1,s[36:39],s61 offen offset:1024 sc1 lds
v_mfma_f32_16x16x32_bf16 v[12:15],a[240:243],v[56:59],v[12:15]
ds_read_b128 v[52:55],v2 offset:18432
v_mfma_f32_16x16x32_bf16 v[16:19],a[244:247],v[56:59],v[16:19]
v_mfma_f32_16x16x32_bf16 v[20:23],a[248:251],v[56:59],v[20:23]
v_mfma_f32_16x16x32_bf16 v[24:27],a[252:255],v[56:59],v[24:27]
s_waitcnt lgkmcnt(2)
v_mfma_f32_16x16x32_bf16 v[12:15],v[100:103],v[44:47],v[12:15]
ds_read_b128 v[56:59],v2 offset:19456
v_mfma_f32_16x16x32_bf16 v[16:19],v[104:107],v[44:47],v[16:19]
buffer_load_dwordx4 v1,s[36:39],s61 offen offset:2048 sc1 lds
v_mfma_f32_16x16x32_bf16 v[20:23],v[108:111],v[44:47],v[20:23]
ds_read_b128 v[88:91],v9 offset:0
v_mfma_f32_16x16x32_bf16 v[24:27],v[112:115],v[44:47],v[24:27]
s_waitcnt lgkmcnt(3)
v_mfma_f32_16x16x32_bf16 v[12:15],v[116:119],v[48:51],v[12:15]
ds_read_b128 v[44:47],v2 offset:20480
v_mfma_f32_16x16x32_bf16 v[16:19],v[120:123],v[48:51],v[16:19]
ds_read_b128 v[76:79],v9 offset:1024
v_mfma_f32_16x16x32_bf16 v[20:23],v[124:127],v[48:51],v[20:23]
v_mfma_f32_16x16x32_bf16 v[24:27],v[128:131],v[48:51],v[24:27]
s_waitcnt lgkmcnt(4)
v_mfma_f32_16x16x32_bf16 v[12:15],v[132:135],v[52:55],v[12:15]
ds_read_b128 v[48:51],v2 offset:21504
v_mfma_f32_16x16x32_bf16 v[16:19],v[136:139],v[52:55],v[16:19]
ds_read_b128 v[80:83],v9 offset:2048
v_mfma_f32_16x16x32_bf16 v[20:23],v[140:143],v[52:55],v[20:23]
v_mfma_f32_16x16x32_bf16 v[24:27],v[144:147],v[52:55],v[24:27]
s_waitcnt lgkmcnt(4)
v_mfma_f32_16x16x32_bf16 v[12:15],v[148:151],v[56:59],v[12:15]
ds_read_b128 v[52:55],v2 offset:22528
v_mfma_f32_16x16x32_bf16 v[16:19],v[152:155],v[56:59],v[16:19]
ds_read_b128 v[84:87],v9 offset:3072
v_mfma_f32_16x16x32_bf16 v[20:23],v[156:159],v[56:59],v[20:23]
v_mfma_f32_16x16x32_bf16 v[24:27],v[88:91],v[56:59],v[24:27]
s_waitcnt lgkmcnt(4)
v_mfma_f32_16x16x32_bf16 v[12:15],v[160:163],v[44:47],v[12:15]
ds_read_b128 v[56:59],v2 offset:23552
v_mfma_f32_16x16x32_bf16 v[16:19],v[164:167],v[44:47],v[16:19]
ds_read_b128 v[88:91],v9 offset:4096
v_mfma_f32_16x16x32_bf16 v[20:23],v[168:171],v[44:47],v[20:23]
v_mfma_f32_16x16x32_bf16 v[24:27],v[76:79],v[44:47],v[24:27]
s_waitcnt lgkmcnt(4)
v_mfma_f32_16x16x32_bf16 v[12:15],v[172:175],v[48:51],v[12:15]
ds_read_b128 v[44:47],v2 offset:24576
v_mfma_f32_16x16x32_bf16 v[16:19],v[176:179],v[48:51],v[16:19]
ds_read_b128 v[76:79],v9 offset:5120
v_mfma_f32_16x16x32_bf16 v[20:23],v[180:183],v[48:51],v[20:23]
v_mfma_f32_16x16x32_bf16 v[24:27],v[80:83],v[48:51],v[24:27]
s_waitcnt lgkmcnt(4)
v_mfma_f32_16x16x32_bf16 v[12:15],v[184:187],v[52:55],v[12:15]
ds_read_b128 v[48:51],v2 offset:25600
v_mfma_f32_16x16x32_bf16 v[16:19],v[188:191],v[52:55],v[16:19]
ds_read_b128 v[80:83],v9 offset:6144
v_mfma_f32_16x16x32_bf16 v[20:23],v[192:195],v[52:55],v[20:23]
v_mfma_f32_16x16x32_bf16 v[24:27],v[84:87],v[52:55],v[24:27]
s_waitcnt lgkmcnt(4)
v_mfma_f32_16x16x32_bf16 v[12:15],v[196:199],v[56:59],v[12:15]
ds_read_b128 v[52:55],v2 offset:26624
v_mfma_f32_16x16x32_bf16 v[16:19],v[200:203],v[56:59],v[16:19]
ds_read_b128 v[84:87],v9 offset:7168
v_mfma_f32_16x16x32_bf16 v[20:23],v[204:207],v[56:59],v[20:23]
v_mfma_f32_16x16x32_bf16 v[24:27],v[88:91],v[56:59],v[24:27]
s_waitcnt lgkmcnt(4)
v_mfma_f32_16x16x32_bf16 v[12:15],v[208:211],v[44:47],v[12:15]
ds_read_b128 v[56:59],v2 offset:27648
v_mfma_f32_16x16x32_bf16 v[16:19],v[212:215],v[44:47],v[16:19]
ds_read_b128 v[88:91],v9 offset:8192
v_mfma_f32_16x16x32_bf16 v[20:23],v[216:219],v[44:47],v[20:23]
s_waitcnt vmcnt(3)
v_mfma_f32_16x16x32_bf16 v[24:27],v[76:79],v[44:47],v[24:27]
s_waitcnt lgkmcnt(4)
s_barrier
v_mfma_f32_16x16x32_bf16 v[12:15],v[220:223],v[48:51],v[12:15]
ds_read_b128 v[44:47],v2 offset:28672
ds_read_b32 v10,v7 offset:4096
v_mfma_f32_16x16x32_bf16 v[16:19],v[224:227],v[48:51],v[16:19]
v_mfma_f32_16x16x32_bf16 v[20:23],v[228:231],v[48:51],v[20:23]
v_mfma_f32_16x16x32_bf16 v[24:27],v[80:83],v[48:51],v[24:27]
s_waitcnt lgkmcnt(4)
v_mfma_f32_16x16x32_bf16 v[12:15],v[232:235],v[52:55],v[12:15]
s_waitcnt lgkmcnt(0)
ds_read_b128 v[48:51],v2 offset:29696
v_mfma_f32_16x16x32_bf16 v[16:19],v[236:239],v[52:55],v[16:19]
v_cmp_eq_u32 vcc,s60,v10
v_mfma_f32_16x16x32_bf16 v[20:23],v[240:243],v[52:55],v[20:23]
s_cmp_eq_u32 vcc_lo,-1
s_cselect_b32 s67,s86,s59
s_mul_i32 s61,s67,7
v_mfma_f32_16x16x32_bf16 v[24:27],v[84:87],v[52:55],v[24:27]
s_lshr_b32 s61,s61,1
s_cmp_ge_u32 s66,s59
v_mfma_f32_16x16x32_bf16 v[12:15],v[244:247],v[56:59],v[12:15]
ds_read_b128 v[52:55],v2 offset:30720
v_mfma_f32_16x16x32_bf16 v[16:19],v[248:251],v[56:59],v[16:19]
v_mfma_f32_16x16x32_bf16 v[20:23],v[252:255],v[56:59],v[20:23]
v_mfma_f32_16x16x32_bf16 v[24:27],v[88:91],v[56:59],v[24:27]
s_cbranch_scc1 Lg4_slow1_%=
Lg4_d1w0_%=:
v_mfma_f32_16x16x32_bf16 v[28:31],a[0:3],v[44:47],0
s_add_u32 m0,s63,0
ds_read_b128 v[56:59],v2 offset:31744
v_lshlrev_b32 v60,16,v68
v_mfma_f32_16x16x32_bf16 v[32:35],a[4:7],v[44:47],0
buffer_load_dwordx4 v0,s[36:39],s61 offen offset:0 sc1 lds
v_mfma_f32_16x16x32_bf16 v[36:39],a[8:11],v[44:47],0
s_max_u32 s73,s48,s65
v_and_b32 v61,0xffff0000,v68
v_lshlrev_b32 v62,16,v69
v_mfma_f32_16x16x32_bf16 v[40:43],a[12:15],v[44:47],0
s_waitcnt lgkmcnt(2)
v_and_b32 v63,0xffff0000,v69
v_lshlrev_b32 v64,16,v70
v_mfma_f32_16x16x32_bf16 v[28:31],a[16:19],v[48:51],v[28:31]
s_mov_b32 s65,0
ds_read_b128 v[44:47],v2 offset:32768
v_and_b32 v65,0xffff0000,v70
v_mfma_f32_16x16x32_bf16 v[32:35],a[20:23],v[48:51],v[32:35]
buffer_load_dwordx4 v0,s[36:39],s61 offen offset:1024 sc1 lds
v_mfma_f32_16x16x32_bf16 v[36:39],a[24:27],v[48:51],v[36:39]
s_add_u32 s74,s53,s55
v_lshlrev_b32 v66,16,v71
v_and_b32 v67,0xffff0000,v71
v_mfma_f32_16x16x32_bf16 v[40:43],a[28:31],v[48:51],v[40:43]
s_waitcnt lgkmcnt(2)
v_add_f32 v12,v12,v60
v_add_f32 v13,v13,v61
v_mfma_f32_16x16x32_bf16 v[28:31],a[32:35],v[52:55],v[28:31]
s_cmp_eq_u32 s74,s56
ds_read_b128 v[48:51],v2 offset:33792
v_add_f32 v14,v14,v62
v_mfma_f32_16x16x32_bf16 v[32:35],a[36:39],v[52:55],v[32:35]
buffer_load_dwordx4 v0,s[36:39],s61 offen offset:2048 sc1 lds
v_mfma_f32_16x16x32_bf16 v[36:39],a[40:43],v[52:55],v[36:39]
s_cselect_b32 s74,s57,s74
v_add_f32 v15,v15,v63
v_add_f32 v16,v16,v64
v_mfma_f32_16x16x32_bf16 v[40:43],a[44:47],v[52:55],v[40:43]
s_waitcnt lgkmcnt(2)
v_add_f32 v17,v17,v65
v_add_f32 v18,v18,v66
v_mfma_f32_16x16x32_bf16 v[28:31],a[48:51],v[56:59],v[28:31]
s_cmp_eq_u32 s74,s58
ds_read_b128 v[52:55],v2 offset:34816
v_add_f32 v19,v19,v67
v_mfma_f32_16x16x32_bf16 v[32:35],a[52:55],v[56:59],v[32:35]
buffer_load_dwordx4 v0,s[36:39],s61 offen offset:3072 sc1 lds
v_mfma_f32_16x16x32_bf16 v[36:39],a[56:59],v[56:59],v[36:39]
s_cselect_b32 s74,s59,s74
v_cvt_pk_bf16_f32 v12,v12,v13)"
R"(
v_cvt_pk_bf16_f32 v13,v14,v15
v_mfma_f32_16x16x32_bf16 v[40:43],a[60:63],v[56:59],v[40:43]
s_waitcnt lgkmcnt(2)
v_cvt_pk_bf16_f32 v14,v16,v17
v_cvt_pk_bf16_f32 v15,v18,v19
v_mfma_f32_16x16x32_bf16 v[28:31],a[64:67],v[44:47],v[28:31]
s_cmp_ge_u32 s67,s59
ds_read_b128 v[56:59],v2 offset:35840
v_lshlrev_b32 v60,16,v72
v_mfma_f32_16x16x32_bf16 v[32:35],a[68:71],v[44:47],v[32:35]
s_cselect_b32 s86,s50,s51
v_and_b32 v61,0xffff0000,v72
v_lshlrev_b32 v62,16,v73
v_mfma_f32_16x16x32_bf16 v[36:39],a[72:75],v[44:47],v[36:39]
buffer_store_dwordx4 v[12:15],v3,s[36:39],s73 offen offset:0 sc0 sc1
v_and_b32 v63,0xffff0000,v73
v_lshlrev_b32 v64,16,v74
v_and_b32 v65,0xffff0000,v74
v_mfma_f32_16x16x32_bf16 v[40:43],a[76:79],v[44:47],v[40:43]
s_waitcnt lgkmcnt(2)
v_lshlrev_b32 v66,16,v75
v_and_b32 v67,0xffff0000,v75
v_mfma_f32_16x16x32_bf16 v[28:31],a[80:83],v[48:51],v[28:31]
s_mul_i32 s68,s86,5
ds_read_b128 v[44:47],v2 offset:36864
v_add_f32 v20,v20,v60
v_mfma_f32_16x16x32_bf16 v[32:35],a[84:87],v[48:51],v[32:35]
s_lshr_b32 s68,s68,6
v_add_f32 v21,v21,v61
v_add_f32 v22,v22,v62
v_mfma_f32_16x16x32_bf16 v[36:39],a[88:91],v[48:51],v[36:39]
s_add_u32 m0,s78,5120
v_add_f32 v23,v23,v63
v_add_f32 v24,v24,v64
v_mfma_f32_16x16x32_bf16 v[40:43],a[92:95],v[48:51],v[40:43]
s_waitcnt lgkmcnt(2)
v_add_f32 v25,v25,v65
v_add_f32 v26,v26,v66
v_mfma_f32_16x16x32_bf16 v[28:31],a[96:99],v[52:55],v[28:31]
s_mov_b64 s[44:45],s[46:47]
ds_read_b128 v[48:51],v2 offset:37888
v_add_f32 v27,v27,v67
v_mfma_f32_16x16x32_bf16 v[32:35],a[100:103],v[52:55],v[32:35]
s_mov_b64 s[46:47],s[48:49]
v_cvt_pk_bf16_f32 v20,v20,v21
v_cvt_pk_bf16_f32 v21,v22,v23
v_mfma_f32_16x16x32_bf16 v[36:39],a[104:107],v[52:55],v[36:39]
v_cvt_pk_bf16_f32 v22,v24,v25
v_cvt_pk_bf16_f32 v23,v26,v27
v_mfma_f32_16x16x32_bf16 v[40:43],a[108:111],v[52:55],v[40:43]
s_waitcnt lgkmcnt(2)
v_mfma_f32_16x16x32_bf16 v[28:31],a[112:115],v[56:59],v[28:31]
buffer_store_dwordx4 v[20:23],v3,s[36:39],s73 offen offset:512 sc0 sc1
ds_read_b128 v[52:55],v2 offset:38912
v_mfma_f32_16x16x32_bf16 v[32:35],a[116:119],v[56:59],v[32:35]
v_mfma_f32_16x16x32_bf16 v[36:39],a[120:123],v[56:59],v[36:39]
v_mfma_f32_16x16x32_bf16 v[40:43],a[124:127],v[56:59],v[40:43]
s_waitcnt lgkmcnt(2)
v_mfma_f32_16x16x32_bf16 v[28:31],a[128:131],v[44:47],v[28:31]
ds_read_b128 v[56:59],v2 offset:39936
v_mfma_f32_16x16x32_bf16 v[32:35],a[132:135],v[44:47],v[32:35]
buffer_load_dwordx4 v[68:71],v8,s[36:39],s49 offen offset:0 sc1
v_mfma_f32_16x16x32_bf16 v[36:39],a[136:139],v[44:47],v[36:39]
v_mfma_f32_16x16x32_bf16 v[40:43],a[140:143],v[44:47],v[40:43]
s_waitcnt lgkmcnt(2)
v_mfma_f32_16x16x32_bf16 v[28:31],a[144:147],v[48:51],v[28:31]
ds_read_b128 v[44:47],v2 offset:40960
v_mfma_f32_16x16x32_bf16 v[32:35],a[148:151],v[48:51],v[32:35]
buffer_load_dwordx4 v[72:75],v8,s[36:39],s49 offen offset:1024 sc1
v_mfma_f32_16x16x32_bf16 v[36:39],a[152:155],v[48:51],v[36:39]
s_mov_b64 s[48:49],s[50:51]
s_mov_b64 s[50:51],s[52:53]
s_mov_b32 s52,s74
v_mfma_f32_16x16x32_bf16 v[40:43],a[156:159],v[48:51],v[40:43]
s_waitcnt lgkmcnt(2)
v_mfma_f32_16x16x32_bf16 v[28:31],a[160:163],v[52:55],v[28:31]
ds_read_b128 v[48:51],v2 offset:41984
v_mfma_f32_16x16x32_bf16 v[32:35],a[164:167],v[52:55],v[32:35]
v_mfma_f32_16x16x32_bf16 v[36:39],a[168:171],v[52:55],v[36:39]
v_mfma_f32_16x16x32_bf16 v[40:43],a[172:175],v[52:55],v[40:43]
s_waitcnt lgkmcnt(2)
buffer_load_dword v6,s[36:39],s68 offen offset:0 sc1 lds
v_mfma_f32_16x16x32_bf16 v[28:31],a[176:179],v[56:59],v[28:31]
s_add_u32 m0,s64,0
ds_read_b128 v[52:55],v2 offset:43008
v_mfma_f32_16x16x32_bf16 v[32:35],a[180:183],v[56:59],v[32:35]
v_mfma_f32_16x16x32_bf16 v[36:39],a[184:187],v[56:59],v[36:39]
v_mfma_f32_16x16x32_bf16 v[40:43],a[188:191],v[56:59],v[40:43]
s_waitcnt lgkmcnt(2)
v_mfma_f32_16x16x32_bf16 v[28:31],a[192:195],v[44:47],v[28:31]
ds_read_b128 v[56:59],v2 offset:44032
v_mfma_f32_16x16x32_bf16 v[32:35],a[196:199],v[44:47],v[32:35]
v_mfma_f32_16x16x32_bf16 v[36:39],a[200:203],v[44:47],v[36:39]
s_waitcnt vmcnt(9)
v_mfma_f32_16x16x32_bf16 v[40:43],a[204:207],v[44:47],v[40:43]
s_waitcnt lgkmcnt(2)
s_barrier
v_mfma_f32_16x16x32_bf16 v[28:31],a[208:211],v[48:51],v[28:31]
ds_read_b128 v[44:47],v2 offset:45056
v_mfma_f32_16x16x32_bf16 v[32:35],a[212:215],v[48:51],v[32:35]
buffer_load_dwordx4 v1,s[36:39],s61 offen offset:0 sc1 lds
v_mfma_f32_16x16x32_bf16 v[36:39],a[216:219],v[48:51],v[36:39]
v_mfma_f32_16x16x32_bf16 v[40:43],a[220:223],v[48:51],v[40:43]
s_waitcnt lgkmcnt(2)
v_mfma_f32_16x16x32_bf16 v[28:31],a[224:227],v[52:55],v[28:31]
ds_read_b128 v[48:51],v2 offset:46080
v_mfma_f32_16x16x32_bf16 v[32:35],a[228:231],v[52:55],v[32:35]
v_mfma_f32_16x16x32_bf16 v[36:39],a[232:235],v[52:55],v[36:39]
v_mfma_f32_16x16x32_bf16 v[40:43],a[236:239],v[52:55],v[40:43]
s_waitcnt lgkmcnt(2)
buffer_load_dwordx4 v1,s[36:39],s61 offen offset:1024 sc1 lds
v_mfma_f32_16x16x32_bf16 v[28:31],a[240:243],v[56:59],v[28:31]
ds_read_b128 v[52:55],v2 offset:47104
v_mfma_f32_16x16x32_bf16 v[32:35],a[244:247],v[56:59],v[32:35]
v_mfma_f32_16x16x32_bf16 v[36:39],a[248:251],v[56:59],v[36:39]
v_mfma_f32_16x16x32_bf16 v[40:43],a[252:255],v[56:59],v[40:43]
s_waitcnt lgkmcnt(2)
v_mfma_f32_16x16x32_bf16 v[28:31],v[100:103],v[44:47],v[28:31]
ds_read_b128 v[56:59],v2 offset:48128
v_mfma_f32_16x16x32_bf16 v[32:35],v[104:107],v[44:47],v[32:35]
buffer_load_dwordx4 v1,s[36:39],s61 offen offset:2048 sc1 lds
v_mfma_f32_16x16x32_bf16 v[36:39],v[108:111],v[44:47],v[36:39]
ds_read_b128 v[88:91],v9 offset:0
v_mfma_f32_16x16x32_bf16 v[40:43],v[112:115],v[44:47],v[40:43]
s_waitcnt lgkmcnt(3)
v_mfma_f32_16x16x32_bf16 v[28:31],v[116:119],v[48:51],v[28:31]
ds_read_b128 v[44:47],v2 offset:49152
v_mfma_f32_16x16x32_bf16 v[32:35],v[120:123],v[48:51],v[32:35]
ds_read_b128 v[76:79],v9 offset:1024
v_mfma_f32_16x16x32_bf16 v[36:39],v[124:127],v[48:51],v[36:39]
v_mfma_f32_16x16x32_bf16 v[40:43],v[128:131],v[48:51],v[40:43]
s_waitcnt lgkmcnt(4)
v_mfma_f32_16x16x32_bf16 v[28:31],v[132:135],v[52:55],v[28:31]
ds_read_b128 v[48:51],v2 offset:50176
v_mfma_f32_16x16x32_bf16 v[32:35],v[136:139],v[52:55],v[32:35]
ds_read_b128 v[80:83],v9 offset:2048
v_mfma_f32_16x16x32_bf16 v[36:39],v[140:143],v[52:55],v[36:39]
v_mfma_f32_16x16x32_bf16 v[40:43],v[144:147],v[52:55],v[40:43]
s_waitcnt lgkmcnt(4)
v_mfma_f32_16x16x32_bf16 v[28:31],v[148:151],v[56:59],v[28:31]
ds_read_b128 v[52:55],v2 offset:51200
v_mfma_f32_16x16x32_bf16 v[32:35],v[152:155],v[56:59],v[32:35]
ds_read_b128 v[84:87],v9 offset:3072
v_mfma_f32_16x16x32_bf16 v[36:39],v[156:159],v[56:59],v[36:39]
v_mfma_f32_16x16x32_bf16 v[40:43],v[88:91],v[56:59],v[40:43]
s_waitcnt lgkmcnt(4)
v_mfma_f32_16x16x32_bf16 v[28:31],v[160:163],v[44:47],v[28:31]
ds_read_b128 v[56:59],v2 offset:52224
v_mfma_f32_16x16x32_bf16 v[32:35],v[164:167],v[44:47],v[32:35]
ds_read_b128 v[88:91],v9 offset:4096
v_mfma_f32_16x16x32_bf16 v[36:39],v[168:171],v[44:47],v[36:39]
v_mfma_f32_16x16x32_bf16 v[40:43],v[76:79],v[44:47],v[40:43]
s_waitcnt lgkmcnt(4)
v_mfma_f32_16x16x32_bf16 v[28:31],v[172:175],v[48:51],v[28:31]
ds_read_b128 v[44:47],v2 offset:53248
v_mfma_f32_16x16x32_bf16 v[32:35],v[176:179],v[48:51],v[32:35]
ds_read_b128 v[76:79],v9 offset:5120
v_mfma_f32_16x16x32_bf16 v[36:39],v[180:183],v[48:51],v[36:39]
v_mfma_f32_16x16x32_bf16 v[40:43],v[80:83],v[48:51],v[40:43]
s_waitcnt lgkmcnt(4)
v_mfma_f32_16x16x32_bf16 v[28:31],v[184:187],v[52:55],v[28:31]
ds_read_b128 v[48:51],v2 offset:54272
v_mfma_f32_16x16x32_bf16 v[32:35],v[188:191],v[52:55],v[32:35]
ds_read_b128 v[80:83],v9 offset:6144
v_mfma_f32_16x16x32_bf16 v[36:39],v[192:195],v[52:55],v[36:39]
v_mfma_f32_16x16x32_bf16 v[40:43],v[84:87],v[52:55],v[40:43]
s_waitcnt lgkmcnt(4)
v_mfma_f32_16x16x32_bf16 v[28:31],v[196:199],v[56:59],v[28:31]
ds_read_b128 v[52:55],v2 offset:55296
v_mfma_f32_16x16x32_bf16 v[32:35],v[200:203],v[56:59],v[32:35]
ds_read_b128 v[84:87],v9 offset:7168
v_mfma_f32_16x16x32_bf16 v[36:39],v[204:207],v[56:59],v[36:39]
v_mfma_f32_16x16x32_bf16 v[40:43],v[88:91],v[56:59],v[40:43]
s_waitcnt lgkmcnt(4)
v_mfma_f32_16x16x32_bf16 v[28:31],v[208:211],v[44:47],v[28:31]
ds_read_b128 v[56:59],v2 offset:56320
v_mfma_f32_16x16x32_bf16 v[32:35],v[212:215],v[44:47],v[32:35]
ds_read_b128 v[88:91],v9 offset:8192
v_mfma_f32_16x16x32_bf16 v[36:39],v[216:219],v[44:47],v[36:39]
s_waitcnt vmcnt(3)
v_mfma_f32_16x16x32_bf16 v[40:43],v[76:79],v[44:47],v[40:43]
s_waitcnt lgkmcnt(4)
s_barrier
v_mfma_f32_16x16x32_bf16 v[28:31],v[220:223],v[48:51],v[28:31]
ds_read_b128 v[44:47],v2 offset:0
ds_read_b32 v10,v7 offset:5120
v_mfma_f32_16x16x32_bf16 v[32:35],v[224:227],v[48:51],v[32:35]
v_mfma_f32_16x16x32_bf16 v[36:39],v[228:231],v[48:51],v[36:39]
v_mfma_f32_16x16x32_bf16 v[40:43],v[80:83],v[48:51],v[40:43]
s_waitcnt lgkmcnt(4)
v_mfma_f32_16x16x32_bf16 v[28:31],v[232:235],v[52:55],v[28:31]
s_waitcnt lgkmcnt(0)
ds_read_b128 v[48:51],v2 offset:1024
v_mfma_f32_16x16x32_bf16 v[32:35],v[236:239],v[52:55],v[32:35]
v_cmp_eq_u32 vcc,s60,v10
v_mfma_f32_16x16x32_bf16 v[36:39],v[240:243],v[52:55],v[36:39]
s_cmp_eq_u32 vcc_lo,-1
s_cselect_b32 s66,s86,s59
s_mul_i32 s61,s66,7
v_mfma_f32_16x16x32_bf16 v[40:43],v[84:87],v[52:55],v[40:43])"
R"(
s_lshr_b32 s61,s61,1
s_cmp_lt_u32 s67,s59
v_mfma_f32_16x16x32_bf16 v[28:31],v[244:247],v[56:59],v[28:31]
ds_read_b128 v[52:55],v2 offset:2048
v_mfma_f32_16x16x32_bf16 v[32:35],v[248:251],v[56:59],v[32:35]
v_mfma_f32_16x16x32_bf16 v[36:39],v[252:255],v[56:59],v[36:39]
v_mfma_f32_16x16x32_bf16 v[40:43],v[88:91],v[56:59],v[40:43]
s_cbranch_scc1 Lg4_d0w0_%=
s_branch Lg4_slow0_%=
Lg4_d0w1_%=:
v_mfma_f32_16x16x32_bf16 v[12:15],a[0:3],v[44:47],0
s_add_u32 m0,s63,28672
ds_read_b128 v[56:59],v2 offset:3072
v_lshlrev_b32 v60,16,v68
v_mfma_f32_16x16x32_bf16 v[16:19],a[4:7],v[44:47],0
s_max_u32 s73,s47,s65
v_and_b32 v61,0xffff0000,v68
v_lshlrev_b32 v62,16,v69
v_mfma_f32_16x16x32_bf16 v[20:23],a[8:11],v[44:47],0
buffer_load_dwordx4 v0,s[36:39],s61 offen offset:0 sc1 lds
v_mfma_f32_16x16x32_bf16 v[24:27],a[12:15],v[44:47],0
s_waitcnt lgkmcnt(2)
v_and_b32 v63,0xffff0000,v69
v_lshlrev_b32 v64,16,v70
v_mfma_f32_16x16x32_bf16 v[12:15],a[16:19],v[48:51],v[12:15]
s_mov_b32 s65,0
ds_read_b128 v[44:47],v2 offset:4096
v_and_b32 v65,0xffff0000,v70
v_mfma_f32_16x16x32_bf16 v[16:19],a[20:23],v[48:51],v[16:19]
s_add_u32 s53,s52,s55
v_lshlrev_b32 v66,16,v71
v_and_b32 v67,0xffff0000,v71
v_mfma_f32_16x16x32_bf16 v[20:23],a[24:27],v[48:51],v[20:23]
buffer_load_dwordx4 v0,s[36:39],s61 offen offset:1024 sc1 lds
v_mfma_f32_16x16x32_bf16 v[24:27],a[28:31],v[48:51],v[24:27]
s_waitcnt lgkmcnt(2)
v_add_f32 v28,v28,v60
v_add_f32 v29,v29,v61
v_mfma_f32_16x16x32_bf16 v[12:15],a[32:35],v[52:55],v[12:15]
s_cmp_eq_u32 s53,s56
ds_read_b128 v[48:51],v2 offset:5120
v_add_f32 v30,v30,v62
v_mfma_f32_16x16x32_bf16 v[16:19],a[36:39],v[52:55],v[16:19]
s_cselect_b32 s53,s57,s53
v_add_f32 v31,v31,v63
v_add_f32 v32,v32,v64
v_mfma_f32_16x16x32_bf16 v[20:23],a[40:43],v[52:55],v[20:23]
buffer_load_dwordx4 v0,s[36:39],s61 offen offset:2048 sc1 lds
v_mfma_f32_16x16x32_bf16 v[24:27],a[44:47],v[52:55],v[24:27]
s_waitcnt lgkmcnt(2)
v_add_f32 v33,v33,v65
v_add_f32 v34,v34,v66
v_mfma_f32_16x16x32_bf16 v[12:15],a[48:51],v[56:59],v[12:15]
s_cmp_eq_u32 s53,s58
ds_read_b128 v[52:55],v2 offset:6144
v_add_f32 v35,v35,v67
v_mfma_f32_16x16x32_bf16 v[16:19],a[52:55],v[56:59],v[16:19]
s_cselect_b32 s53,s59,s53
v_cvt_pk_bf16_f32 v28,v28,v29
v_cvt_pk_bf16_f32 v29,v30,v31
v_mfma_f32_16x16x32_bf16 v[20:23],a[56:59],v[56:59],v[20:23]
buffer_load_dwordx4 v0,s[36:39],s61 offen offset:3072 sc1 lds
v_mfma_f32_16x16x32_bf16 v[24:27],a[60:63],v[56:59],v[24:27]
s_waitcnt lgkmcnt(2)
v_cvt_pk_bf16_f32 v30,v32,v33
v_cvt_pk_bf16_f32 v31,v34,v35
v_mfma_f32_16x16x32_bf16 v[12:15],a[64:67],v[44:47],v[12:15]
s_cmp_ge_u32 s66,s59
ds_read_b128 v[56:59],v2 offset:7168
v_lshlrev_b32 v60,16,v72
v_mfma_f32_16x16x32_bf16 v[16:19],a[68:71],v[44:47],v[16:19]
s_cselect_b32 s86,s49,s50
v_and_b32 v61,0xffff0000,v72
v_lshlrev_b32 v62,16,v73
v_mfma_f32_16x16x32_bf16 v[20:23],a[72:75],v[44:47],v[20:23]
s_add_u32 m0,s64,28672
v_and_b32 v63,0xffff0000,v73
v_lshlrev_b32 v64,16,v74
v_mfma_f32_16x16x32_bf16 v[24:27],a[76:79],v[44:47],v[24:27]
s_waitcnt lgkmcnt(2)
buffer_store_dwordx4 v[28:31],v3,s[36:39],s73 offen offset:0 sc0 sc1
v_and_b32 v65,0xffff0000,v74
v_lshlrev_b32 v66,16,v75
v_and_b32 v67,0xffff0000,v75
v_mfma_f32_16x16x32_bf16 v[12:15],a[80:83],v[48:51],v[12:15]
ds_read_b128 v[44:47],v2 offset:8192
v_add_f32 v36,v36,v60
v_mfma_f32_16x16x32_bf16 v[16:19],a[84:87],v[48:51],v[16:19]
v_add_f32 v37,v37,v61
v_add_f32 v38,v38,v62
v_mfma_f32_16x16x32_bf16 v[20:23],a[88:91],v[48:51],v[20:23]
v_add_f32 v39,v39,v63
v_add_f32 v40,v40,v64
v_mfma_f32_16x16x32_bf16 v[24:27],a[92:95],v[48:51],v[24:27]
s_waitcnt lgkmcnt(2)
v_add_f32 v41,v41,v65
v_add_f32 v42,v42,v66
v_mfma_f32_16x16x32_bf16 v[12:15],a[96:99],v[52:55],v[12:15]
ds_read_b128 v[48:51],v2 offset:9216
v_add_f32 v43,v43,v67
v_mfma_f32_16x16x32_bf16 v[16:19],a[100:103],v[52:55],v[16:19]
v_cvt_pk_bf16_f32 v36,v36,v37
v_cvt_pk_bf16_f32 v37,v38,v39
v_mfma_f32_16x16x32_bf16 v[20:23],a[104:107],v[52:55],v[20:23]
v_cvt_pk_bf16_f32 v38,v40,v41
v_cvt_pk_bf16_f32 v39,v42,v43
v_mfma_f32_16x16x32_bf16 v[24:27],a[108:111],v[52:55],v[24:27]
s_waitcnt lgkmcnt(2)
v_mfma_f32_16x16x32_bf16 v[12:15],a[112:115],v[56:59],v[12:15]
ds_read_b128 v[52:55],v2 offset:10240
v_mfma_f32_16x16x32_bf16 v[16:19],a[116:119],v[56:59],v[16:19]
buffer_store_dwordx4 v[36:39],v3,s[36:39],s73 offen offset:512 sc0 sc1
v_mfma_f32_16x16x32_bf16 v[20:23],a[120:123],v[56:59],v[20:23]
v_mfma_f32_16x16x32_bf16 v[24:27],a[124:127],v[56:59],v[24:27]
s_waitcnt lgkmcnt(2)
v_mfma_f32_16x16x32_bf16 v[12:15],a[128:131],v[44:47],v[12:15]
ds_read_b128 v[56:59],v2 offset:11264
v_mfma_f32_16x16x32_bf16 v[16:19],a[132:135],v[44:47],v[16:19]
v_mfma_f32_16x16x32_bf16 v[20:23],a[136:139],v[44:47],v[20:23]
buffer_load_dwordx4 v[68:71],v8,s[36:39],s48 offen offset:0 sc1
v_mfma_f32_16x16x32_bf16 v[24:27],a[140:143],v[44:47],v[24:27]
s_waitcnt lgkmcnt(2)
v_mfma_f32_16x16x32_bf16 v[12:15],a[144:147],v[48:51],v[12:15]
ds_read_b128 v[44:47],v2 offset:12288
v_mfma_f32_16x16x32_bf16 v[16:19],a[148:151],v[48:51],v[16:19]
v_mfma_f32_16x16x32_bf16 v[20:23],a[152:155],v[48:51],v[20:23]
buffer_load_dwordx4 v[72:75],v8,s[36:39],s48 offen offset:1024 sc1
v_mfma_f32_16x16x32_bf16 v[24:27],a[156:159],v[48:51],v[24:27]
s_waitcnt lgkmcnt(2)
v_mfma_f32_16x16x32_bf16 v[12:15],a[160:163],v[52:55],v[12:15]
ds_read_b128 v[48:51],v2 offset:13312
v_mfma_f32_16x16x32_bf16 v[16:19],a[164:167],v[52:55],v[16:19]
v_mfma_f32_16x16x32_bf16 v[20:23],a[168:171],v[52:55],v[20:23]
v_mfma_f32_16x16x32_bf16 v[24:27],a[172:175],v[52:55],v[24:27]
s_waitcnt lgkmcnt(2)
v_mfma_f32_16x16x32_bf16 v[12:15],a[176:179],v[56:59],v[12:15]
ds_read_b128 v[52:55],v2 offset:14336
v_mfma_f32_16x16x32_bf16 v[16:19],a[180:183],v[56:59],v[16:19]
v_mfma_f32_16x16x32_bf16 v[20:23],a[184:187],v[56:59],v[20:23]
v_mfma_f32_16x16x32_bf16 v[24:27],a[188:191],v[56:59],v[24:27]
s_waitcnt lgkmcnt(2)
v_mfma_f32_16x16x32_bf16 v[12:15],a[192:195],v[44:47],v[12:15]
ds_read_b128 v[56:59],v2 offset:15360
v_mfma_f32_16x16x32_bf16 v[16:19],a[196:199],v[44:47],v[16:19]
v_mfma_f32_16x16x32_bf16 v[20:23],a[200:203],v[44:47],v[20:23]
s_waitcnt vmcnt(8)
v_mfma_f32_16x16x32_bf16 v[24:27],a[204:207],v[44:47],v[24:27]
s_waitcnt lgkmcnt(2)
s_barrier
v_mfma_f32_16x16x32_bf16 v[12:15],a[208:211],v[48:51],v[12:15]
ds_read_b128 v[44:47],v2 offset:16384
v_mfma_f32_16x16x32_bf16 v[16:19],a[212:215],v[48:51],v[16:19]
v_mfma_f32_16x16x32_bf16 v[20:23],a[216:219],v[48:51],v[20:23]
buffer_load_dwordx4 v1,s[36:39],s61 offen offset:0 sc1 lds
v_mfma_f32_16x16x32_bf16 v[24:27],a[220:223],v[48:51],v[24:27]
s_waitcnt lgkmcnt(2)
v_mfma_f32_16x16x32_bf16 v[12:15],a[224:227],v[52:55],v[12:15]
ds_read_b128 v[48:51],v2 offset:17408
v_mfma_f32_16x16x32_bf16 v[16:19],a[228:231],v[52:55],v[16:19]
v_mfma_f32_16x16x32_bf16 v[20:23],a[232:235],v[52:55],v[20:23]
v_mfma_f32_16x16x32_bf16 v[24:27],a[236:239],v[52:55],v[24:27]
s_waitcnt lgkmcnt(2)
v_mfma_f32_16x16x32_bf16 v[12:15],a[240:243],v[56:59],v[12:15]
buffer_load_dwordx4 v1,s[36:39],s61 offen offset:1024 sc1 lds
v_mfma_f32_16x16x32_bf16 v[16:19],a[244:247],v[56:59],v[16:19]
ds_read_b128 v[52:55],v2 offset:18432
v_mfma_f32_16x16x32_bf16 v[20:23],a[248:251],v[56:59],v[20:23]
v_mfma_f32_16x16x32_bf16 v[24:27],a[252:255],v[56:59],v[24:27]
s_waitcnt lgkmcnt(2)
v_mfma_f32_16x16x32_bf16 v[12:15],v[100:103],v[44:47],v[12:15]
ds_read_b128 v[56:59],v2 offset:19456
v_mfma_f32_16x16x32_bf16 v[16:19],v[104:107],v[44:47],v[16:19]
ds_read_b128 v[88:91],v9 offset:0
v_mfma_f32_16x16x32_bf16 v[20:23],v[108:111],v[44:47],v[20:23]
buffer_load_dwordx4 v1,s[36:39],s61 offen offset:2048 sc1 lds
v_mfma_f32_16x16x32_bf16 v[24:27],v[112:115],v[44:47],v[24:27]
s_waitcnt lgkmcnt(3)
v_mfma_f32_16x16x32_bf16 v[12:15],v[116:119],v[48:51],v[12:15]
ds_read_b128 v[44:47],v2 offset:20480
v_mfma_f32_16x16x32_bf16 v[16:19],v[120:123],v[48:51],v[16:19]
ds_read_b128 v[76:79],v9 offset:1024
v_mfma_f32_16x16x32_bf16 v[20:23],v[124:127],v[48:51],v[20:23]
v_mfma_f32_16x16x32_bf16 v[24:27],v[128:131],v[48:51],v[24:27]
s_waitcnt lgkmcnt(4)
v_mfma_f32_16x16x32_bf16 v[12:15],v[132:135],v[52:55],v[12:15]
ds_read_b128 v[48:51],v2 offset:21504
v_mfma_f32_16x16x32_bf16 v[16:19],v[136:139],v[52:55],v[16:19]
ds_read_b128 v[80:83],v9 offset:2048
v_mfma_f32_16x16x32_bf16 v[20:23],v[140:143],v[52:55],v[20:23]
v_mfma_f32_16x16x32_bf16 v[24:27],v[144:147],v[52:55],v[24:27]
s_waitcnt lgkmcnt(4)
v_mfma_f32_16x16x32_bf16 v[12:15],v[148:151],v[56:59],v[12:15]
ds_read_b128 v[52:55],v2 offset:22528
v_mfma_f32_16x16x32_bf16 v[16:19],v[152:155],v[56:59],v[16:19]
ds_read_b128 v[84:87],v9 offset:3072
v_mfma_f32_16x16x32_bf16 v[20:23],v[156:159],v[56:59],v[20:23]
v_mfma_f32_16x16x32_bf16 v[24:27],v[88:91],v[56:59],v[24:27]
s_waitcnt lgkmcnt(4)
v_mfma_f32_16x16x32_bf16 v[12:15],v[160:163],v[44:47],v[12:15]
ds_read_b128 v[56:59],v2 offset:23552)"
R"(
v_mfma_f32_16x16x32_bf16 v[16:19],v[164:167],v[44:47],v[16:19]
ds_read_b128 v[88:91],v9 offset:4096
v_mfma_f32_16x16x32_bf16 v[20:23],v[168:171],v[44:47],v[20:23]
v_mfma_f32_16x16x32_bf16 v[24:27],v[76:79],v[44:47],v[24:27]
s_waitcnt lgkmcnt(4)
v_mfma_f32_16x16x32_bf16 v[12:15],v[172:175],v[48:51],v[12:15]
ds_read_b128 v[44:47],v2 offset:24576
v_mfma_f32_16x16x32_bf16 v[16:19],v[176:179],v[48:51],v[16:19]
ds_read_b128 v[76:79],v9 offset:5120
v_mfma_f32_16x16x32_bf16 v[20:23],v[180:183],v[48:51],v[20:23]
v_mfma_f32_16x16x32_bf16 v[24:27],v[80:83],v[48:51],v[24:27]
s_waitcnt lgkmcnt(4)
v_mfma_f32_16x16x32_bf16 v[12:15],v[184:187],v[52:55],v[12:15]
ds_read_b128 v[48:51],v2 offset:25600
v_mfma_f32_16x16x32_bf16 v[16:19],v[188:191],v[52:55],v[16:19]
ds_read_b128 v[80:83],v9 offset:6144
v_mfma_f32_16x16x32_bf16 v[20:23],v[192:195],v[52:55],v[20:23]
v_mfma_f32_16x16x32_bf16 v[24:27],v[84:87],v[52:55],v[24:27]
s_waitcnt lgkmcnt(4)
v_mfma_f32_16x16x32_bf16 v[12:15],v[196:199],v[56:59],v[12:15]
ds_read_b128 v[52:55],v2 offset:26624
v_mfma_f32_16x16x32_bf16 v[16:19],v[200:203],v[56:59],v[16:19]
ds_read_b128 v[84:87],v9 offset:7168
v_mfma_f32_16x16x32_bf16 v[20:23],v[204:207],v[56:59],v[20:23]
v_mfma_f32_16x16x32_bf16 v[24:27],v[88:91],v[56:59],v[24:27]
s_waitcnt lgkmcnt(4)
v_mfma_f32_16x16x32_bf16 v[12:15],v[208:211],v[44:47],v[12:15]
ds_read_b128 v[56:59],v2 offset:27648
v_mfma_f32_16x16x32_bf16 v[16:19],v[212:215],v[44:47],v[16:19]
ds_read_b128 v[88:91],v9 offset:8192
v_mfma_f32_16x16x32_bf16 v[20:23],v[216:219],v[44:47],v[20:23]
s_waitcnt vmcnt(3)
v_mfma_f32_16x16x32_bf16 v[24:27],v[76:79],v[44:47],v[24:27]
s_waitcnt lgkmcnt(4)
s_barrier
v_mfma_f32_16x16x32_bf16 v[12:15],v[220:223],v[48:51],v[12:15]
ds_read_b128 v[44:47],v2 offset:28672
ds_read_b32 v10,v7 offset:4096
v_mfma_f32_16x16x32_bf16 v[16:19],v[224:227],v[48:51],v[16:19]
v_mfma_f32_16x16x32_bf16 v[20:23],v[228:231],v[48:51],v[20:23]
v_mfma_f32_16x16x32_bf16 v[24:27],v[80:83],v[48:51],v[24:27]
s_waitcnt lgkmcnt(4)
v_mfma_f32_16x16x32_bf16 v[12:15],v[232:235],v[52:55],v[12:15]
s_waitcnt lgkmcnt(0)
ds_read_b128 v[48:51],v2 offset:29696
v_mfma_f32_16x16x32_bf16 v[16:19],v[236:239],v[52:55],v[16:19]
v_cmp_eq_u32 vcc,s60,v10
v_mfma_f32_16x16x32_bf16 v[20:23],v[240:243],v[52:55],v[20:23]
s_cmp_eq_u32 vcc_lo,-1
s_cselect_b32 s67,s86,s59
s_mul_i32 s61,s67,7
v_mfma_f32_16x16x32_bf16 v[24:27],v[84:87],v[52:55],v[24:27]
s_lshr_b32 s61,s61,1
s_cmp_ge_u32 s66,s59
v_mfma_f32_16x16x32_bf16 v[12:15],v[244:247],v[56:59],v[12:15]
ds_read_b128 v[52:55],v2 offset:30720
v_mfma_f32_16x16x32_bf16 v[16:19],v[248:251],v[56:59],v[16:19]
v_mfma_f32_16x16x32_bf16 v[20:23],v[252:255],v[56:59],v[20:23]
v_mfma_f32_16x16x32_bf16 v[24:27],v[88:91],v[56:59],v[24:27]
s_cbranch_scc1 Lg4_slow1_%=
Lg4_d1w1_%=:
v_mfma_f32_16x16x32_bf16 v[28:31],a[0:3],v[44:47],0
s_add_u32 m0,s63,0
ds_read_b128 v[56:59],v2 offset:31744
v_lshlrev_b32 v60,16,v68
v_mfma_f32_16x16x32_bf16 v[32:35],a[4:7],v[44:47],0
s_max_u32 s73,s48,s65
v_and_b32 v61,0xffff0000,v68
v_lshlrev_b32 v62,16,v69
v_mfma_f32_16x16x32_bf16 v[36:39],a[8:11],v[44:47],0
buffer_load_dwordx4 v0,s[36:39],s61 offen offset:0 sc1 lds
v_mfma_f32_16x16x32_bf16 v[40:43],a[12:15],v[44:47],0
s_waitcnt lgkmcnt(2)
v_and_b32 v63,0xffff0000,v69
v_lshlrev_b32 v64,16,v70
v_mfma_f32_16x16x32_bf16 v[28:31],a[16:19],v[48:51],v[28:31]
s_mov_b32 s65,0
ds_read_b128 v[44:47],v2 offset:32768
v_and_b32 v65,0xffff0000,v70
v_mfma_f32_16x16x32_bf16 v[32:35],a[20:23],v[48:51],v[32:35]
s_add_u32 s74,s53,s55
v_lshlrev_b32 v66,16,v71
v_and_b32 v67,0xffff0000,v71
v_mfma_f32_16x16x32_bf16 v[36:39],a[24:27],v[48:51],v[36:39]
buffer_load_dwordx4 v0,s[36:39],s61 offen offset:1024 sc1 lds
v_mfma_f32_16x16x32_bf16 v[40:43],a[28:31],v[48:51],v[40:43]
s_waitcnt lgkmcnt(2)
v_add_f32 v12,v12,v60
v_add_f32 v13,v13,v61
v_mfma_f32_16x16x32_bf16 v[28:31],a[32:35],v[52:55],v[28:31]
s_cmp_eq_u32 s74,s56
ds_read_b128 v[48:51],v2 offset:33792
v_add_f32 v14,v14,v62
v_mfma_f32_16x16x32_bf16 v[32:35],a[36:39],v[52:55],v[32:35]
s_cselect_b32 s74,s57,s74
v_add_f32 v15,v15,v63
v_add_f32 v16,v16,v64
v_mfma_f32_16x16x32_bf16 v[36:39],a[40:43],v[52:55],v[36:39]
buffer_load_dwordx4 v0,s[36:39],s61 offen offset:2048 sc1 lds
v_mfma_f32_16x16x32_bf16 v[40:43],a[44:47],v[52:55],v[40:43]
s_waitcnt lgkmcnt(2)
v_add_f32 v17,v17,v65
v_add_f32 v18,v18,v66
v_mfma_f32_16x16x32_bf16 v[28:31],a[48:51],v[56:59],v[28:31]
s_cmp_eq_u32 s74,s58
ds_read_b128 v[52:55],v2 offset:34816
v_add_f32 v19,v19,v67
v_mfma_f32_16x16x32_bf16 v[32:35],a[52:55],v[56:59],v[32:35]
s_cselect_b32 s74,s59,s74
v_cvt_pk_bf16_f32 v12,v12,v13
v_cvt_pk_bf16_f32 v13,v14,v15
v_mfma_f32_16x16x32_bf16 v[36:39],a[56:59],v[56:59],v[36:39]
buffer_load_dwordx4 v0,s[36:39],s61 offen offset:3072 sc1 lds
v_mfma_f32_16x16x32_bf16 v[40:43],a[60:63],v[56:59],v[40:43]
s_waitcnt lgkmcnt(2)
v_cvt_pk_bf16_f32 v14,v16,v17
v_cvt_pk_bf16_f32 v15,v18,v19
v_mfma_f32_16x16x32_bf16 v[28:31],a[64:67],v[44:47],v[28:31]
s_cmp_ge_u32 s67,s59
ds_read_b128 v[56:59],v2 offset:35840
v_lshlrev_b32 v60,16,v72
v_mfma_f32_16x16x32_bf16 v[32:35],a[68:71],v[44:47],v[32:35]
s_cselect_b32 s86,s50,s51
v_and_b32 v61,0xffff0000,v72
v_lshlrev_b32 v62,16,v73
v_mfma_f32_16x16x32_bf16 v[36:39],a[72:75],v[44:47],v[36:39]
s_add_u32 m0,s64,0
v_and_b32 v63,0xffff0000,v73
v_lshlrev_b32 v64,16,v74
v_mfma_f32_16x16x32_bf16 v[40:43],a[76:79],v[44:47],v[40:43]
s_waitcnt lgkmcnt(2)
buffer_store_dwordx4 v[12:15],v3,s[36:39],s73 offen offset:0 sc0 sc1
v_and_b32 v65,0xffff0000,v74
v_lshlrev_b32 v66,16,v75
v_and_b32 v67,0xffff0000,v75
v_mfma_f32_16x16x32_bf16 v[28:31],a[80:83],v[48:51],v[28:31]
s_mov_b64 s[44:45],s[46:47]
ds_read_b128 v[44:47],v2 offset:36864
v_add_f32 v20,v20,v60
v_mfma_f32_16x16x32_bf16 v[32:35],a[84:87],v[48:51],v[32:35]
s_mov_b64 s[46:47],s[48:49]
v_add_f32 v21,v21,v61
v_add_f32 v22,v22,v62
v_mfma_f32_16x16x32_bf16 v[36:39],a[88:91],v[48:51],v[36:39]
v_add_f32 v23,v23,v63
v_add_f32 v24,v24,v64
v_mfma_f32_16x16x32_bf16 v[40:43],a[92:95],v[48:51],v[40:43]
s_waitcnt lgkmcnt(2)
v_add_f32 v25,v25,v65
v_add_f32 v26,v26,v66
v_mfma_f32_16x16x32_bf16 v[28:31],a[96:99],v[52:55],v[28:31]
ds_read_b128 v[48:51],v2 offset:37888
v_add_f32 v27,v27,v67
v_mfma_f32_16x16x32_bf16 v[32:35],a[100:103],v[52:55],v[32:35]
v_cvt_pk_bf16_f32 v20,v20,v21
v_cvt_pk_bf16_f32 v21,v22,v23
v_mfma_f32_16x16x32_bf16 v[36:39],a[104:107],v[52:55],v[36:39]
v_cvt_pk_bf16_f32 v22,v24,v25
v_cvt_pk_bf16_f32 v23,v26,v27
v_mfma_f32_16x16x32_bf16 v[40:43],a[108:111],v[52:55],v[40:43]
s_waitcnt lgkmcnt(2)
v_mfma_f32_16x16x32_bf16 v[28:31],a[112:115],v[56:59],v[28:31]
ds_read_b128 v[52:55],v2 offset:38912
v_mfma_f32_16x16x32_bf16 v[32:35],a[116:119],v[56:59],v[32:35]
buffer_store_dwordx4 v[20:23],v3,s[36:39],s73 offen offset:512 sc0 sc1
v_mfma_f32_16x16x32_bf16 v[36:39],a[120:123],v[56:59],v[36:39]
v_mfma_f32_16x16x32_bf16 v[40:43],a[124:127],v[56:59],v[40:43]
s_waitcnt lgkmcnt(2)
v_mfma_f32_16x16x32_bf16 v[28:31],a[128:131],v[44:47],v[28:31]
ds_read_b128 v[56:59],v2 offset:39936
v_mfma_f32_16x16x32_bf16 v[32:35],a[132:135],v[44:47],v[32:35]
v_mfma_f32_16x16x32_bf16 v[36:39],a[136:139],v[44:47],v[36:39]
buffer_load_dwordx4 v[68:71],v8,s[36:39],s49 offen offset:0 sc1
v_mfma_f32_16x16x32_bf16 v[40:43],a[140:143],v[44:47],v[40:43]
s_waitcnt lgkmcnt(2)
v_mfma_f32_16x16x32_bf16 v[28:31],a[144:147],v[48:51],v[28:31]
ds_read_b128 v[44:47],v2 offset:40960
v_mfma_f32_16x16x32_bf16 v[32:35],a[148:151],v[48:51],v[32:35]
v_mfma_f32_16x16x32_bf16 v[36:39],a[152:155],v[48:51],v[36:39]
buffer_load_dwordx4 v[72:75],v8,s[36:39],s49 offen offset:1024 sc1
v_mfma_f32_16x16x32_bf16 v[40:43],a[156:159],v[48:51],v[40:43]
s_waitcnt lgkmcnt(2)
s_mov_b64 s[48:49],s[50:51]
s_mov_b64 s[50:51],s[52:53]
v_mfma_f32_16x16x32_bf16 v[28:31],a[160:163],v[52:55],v[28:31]
s_mov_b32 s52,s74
ds_read_b128 v[48:51],v2 offset:41984
v_mfma_f32_16x16x32_bf16 v[32:35],a[164:167],v[52:55],v[32:35]
v_mfma_f32_16x16x32_bf16 v[36:39],a[168:171],v[52:55],v[36:39]
v_mfma_f32_16x16x32_bf16 v[40:43],a[172:175],v[52:55],v[40:43]
s_waitcnt lgkmcnt(2)
v_mfma_f32_16x16x32_bf16 v[28:31],a[176:179],v[56:59],v[28:31]
ds_read_b128 v[52:55],v2 offset:43008
v_mfma_f32_16x16x32_bf16 v[32:35],a[180:183],v[56:59],v[32:35]
v_mfma_f32_16x16x32_bf16 v[36:39],a[184:187],v[56:59],v[36:39]
v_mfma_f32_16x16x32_bf16 v[40:43],a[188:191],v[56:59],v[40:43]
s_waitcnt lgkmcnt(2)
v_mfma_f32_16x16x32_bf16 v[28:31],a[192:195],v[44:47],v[28:31]
ds_read_b128 v[56:59],v2 offset:44032
v_mfma_f32_16x16x32_bf16 v[32:35],a[196:199],v[44:47],v[32:35])"
R"(
v_mfma_f32_16x16x32_bf16 v[36:39],a[200:203],v[44:47],v[36:39]
s_waitcnt vmcnt(8)
v_mfma_f32_16x16x32_bf16 v[40:43],a[204:207],v[44:47],v[40:43]
s_waitcnt lgkmcnt(2)
s_barrier
v_mfma_f32_16x16x32_bf16 v[28:31],a[208:211],v[48:51],v[28:31]
ds_read_b128 v[44:47],v2 offset:45056
v_mfma_f32_16x16x32_bf16 v[32:35],a[212:215],v[48:51],v[32:35]
v_mfma_f32_16x16x32_bf16 v[36:39],a[216:219],v[48:51],v[36:39]
buffer_load_dwordx4 v1,s[36:39],s61 offen offset:0 sc1 lds
v_mfma_f32_16x16x32_bf16 v[40:43],a[220:223],v[48:51],v[40:43]
s_waitcnt lgkmcnt(2)
v_mfma_f32_16x16x32_bf16 v[28:31],a[224:227],v[52:55],v[28:31]
ds_read_b128 v[48:51],v2 offset:46080
v_mfma_f32_16x16x32_bf16 v[32:35],a[228:231],v[52:55],v[32:35]
v_mfma_f32_16x16x32_bf16 v[36:39],a[232:235],v[52:55],v[36:39]
v_mfma_f32_16x16x32_bf16 v[40:43],a[236:239],v[52:55],v[40:43]
s_waitcnt lgkmcnt(2)
v_mfma_f32_16x16x32_bf16 v[28:31],a[240:243],v[56:59],v[28:31]
buffer_load_dwordx4 v1,s[36:39],s61 offen offset:1024 sc1 lds
v_mfma_f32_16x16x32_bf16 v[32:35],a[244:247],v[56:59],v[32:35]
ds_read_b128 v[52:55],v2 offset:47104
v_mfma_f32_16x16x32_bf16 v[36:39],a[248:251],v[56:59],v[36:39]
v_mfma_f32_16x16x32_bf16 v[40:43],a[252:255],v[56:59],v[40:43]
s_waitcnt lgkmcnt(2)
v_mfma_f32_16x16x32_bf16 v[28:31],v[100:103],v[44:47],v[28:31]
ds_read_b128 v[56:59],v2 offset:48128
v_mfma_f32_16x16x32_bf16 v[32:35],v[104:107],v[44:47],v[32:35]
ds_read_b128 v[88:91],v9 offset:0
v_mfma_f32_16x16x32_bf16 v[36:39],v[108:111],v[44:47],v[36:39]
buffer_load_dwordx4 v1,s[36:39],s61 offen offset:2048 sc1 lds
v_mfma_f32_16x16x32_bf16 v[40:43],v[112:115],v[44:47],v[40:43]
s_waitcnt lgkmcnt(3)
v_mfma_f32_16x16x32_bf16 v[28:31],v[116:119],v[48:51],v[28:31]
ds_read_b128 v[44:47],v2 offset:49152
v_mfma_f32_16x16x32_bf16 v[32:35],v[120:123],v[48:51],v[32:35]
ds_read_b128 v[76:79],v9 offset:1024
v_mfma_f32_16x16x32_bf16 v[36:39],v[124:127],v[48:51],v[36:39]
v_mfma_f32_16x16x32_bf16 v[40:43],v[128:131],v[48:51],v[40:43]
s_waitcnt lgkmcnt(4)
v_mfma_f32_16x16x32_bf16 v[28:31],v[132:135],v[52:55],v[28:31]
ds_read_b128 v[48:51],v2 offset:50176
v_mfma_f32_16x16x32_bf16 v[32:35],v[136:139],v[52:55],v[32:35]
ds_read_b128 v[80:83],v9 offset:2048
v_mfma_f32_16x16x32_bf16 v[36:39],v[140:143],v[52:55],v[36:39]
v_mfma_f32_16x16x32_bf16 v[40:43],v[144:147],v[52:55],v[40:43]
s_waitcnt lgkmcnt(4)
v_mfma_f32_16x16x32_bf16 v[28:31],v[148:151],v[56:59],v[28:31]
ds_read_b128 v[52:55],v2 offset:51200
v_mfma_f32_16x16x32_bf16 v[32:35],v[152:155],v[56:59],v[32:35]
ds_read_b128 v[84:87],v9 offset:3072
v_mfma_f32_16x16x32_bf16 v[36:39],v[156:159],v[56:59],v[36:39]
v_mfma_f32_16x16x32_bf16 v[40:43],v[88:91],v[56:59],v[40:43]
s_waitcnt lgkmcnt(4)
v_mfma_f32_16x16x32_bf16 v[28:31],v[160:163],v[44:47],v[28:31]
ds_read_b128 v[56:59],v2 offset:52224
v_mfma_f32_16x16x32_bf16 v[32:35],v[164:167],v[44:47],v[32:35]
ds_read_b128 v[88:91],v9 offset:4096
v_mfma_f32_16x16x32_bf16 v[36:39],v[168:171],v[44:47],v[36:39]
v_mfma_f32_16x16x32_bf16 v[40:43],v[76:79],v[44:47],v[40:43]
s_waitcnt lgkmcnt(4)
v_mfma_f32_16x16x32_bf16 v[28:31],v[172:175],v[48:51],v[28:31]
ds_read_b128 v[44:47],v2 offset:53248
v_mfma_f32_16x16x32_bf16 v[32:35],v[176:179],v[48:51],v[32:35]
ds_read_b128 v[76:79],v9 offset:5120
v_mfma_f32_16x16x32_bf16 v[36:39],v[180:183],v[48:51],v[36:39]
v_mfma_f32_16x16x32_bf16 v[40:43],v[80:83],v[48:51],v[40:43]
s_waitcnt lgkmcnt(4)
v_mfma_f32_16x16x32_bf16 v[28:31],v[184:187],v[52:55],v[28:31]
ds_read_b128 v[48:51],v2 offset:54272
v_mfma_f32_16x16x32_bf16 v[32:35],v[188:191],v[52:55],v[32:35]
ds_read_b128 v[80:83],v9 offset:6144
v_mfma_f32_16x16x32_bf16 v[36:39],v[192:195],v[52:55],v[36:39]
v_mfma_f32_16x16x32_bf16 v[40:43],v[84:87],v[52:55],v[40:43]
s_waitcnt lgkmcnt(4)
v_mfma_f32_16x16x32_bf16 v[28:31],v[196:199],v[56:59],v[28:31]
ds_read_b128 v[52:55],v2 offset:55296
v_mfma_f32_16x16x32_bf16 v[32:35],v[200:203],v[56:59],v[32:35]
ds_read_b128 v[84:87],v9 offset:7168
v_mfma_f32_16x16x32_bf16 v[36:39],v[204:207],v[56:59],v[36:39]
v_mfma_f32_16x16x32_bf16 v[40:43],v[88:91],v[56:59],v[40:43]
s_waitcnt lgkmcnt(4)
v_mfma_f32_16x16x32_bf16 v[28:31],v[208:211],v[44:47],v[28:31]
ds_read_b128 v[56:59],v2 offset:56320
v_mfma_f32_16x16x32_bf16 v[32:35],v[212:215],v[44:47],v[32:35]
ds_read_b128 v[88:91],v9 offset:8192
v_mfma_f32_16x16x32_bf16 v[36:39],v[216:219],v[44:47],v[36:39]
s_waitcnt vmcnt(3)
v_mfma_f32_16x16x32_bf16 v[40:43],v[76:79],v[44:47],v[40:43]
s_waitcnt lgkmcnt(4)
s_barrier
v_mfma_f32_16x16x32_bf16 v[28:31],v[220:223],v[48:51],v[28:31]
ds_read_b128 v[44:47],v2 offset:0
ds_read_b32 v10,v7 offset:5120
v_mfma_f32_16x16x32_bf16 v[32:35],v[224:227],v[48:51],v[32:35]
v_mfma_f32_16x16x32_bf16 v[36:39],v[228:231],v[48:51],v[36:39]
v_mfma_f32_16x16x32_bf16 v[40:43],v[80:83],v[48:51],v[40:43]
s_waitcnt lgkmcnt(4)
v_mfma_f32_16x16x32_bf16 v[28:31],v[232:235],v[52:55],v[28:31]
s_waitcnt lgkmcnt(0)
ds_read_b128 v[48:51],v2 offset:1024
v_mfma_f32_16x16x32_bf16 v[32:35],v[236:239],v[52:55],v[32:35]
v_cmp_eq_u32 vcc,s60,v10
v_mfma_f32_16x16x32_bf16 v[36:39],v[240:243],v[52:55],v[36:39]
s_cmp_eq_u32 vcc_lo,-1
s_cselect_b32 s66,s86,s59
s_mul_i32 s61,s66,7
v_mfma_f32_16x16x32_bf16 v[40:43],v[84:87],v[52:55],v[40:43]
s_lshr_b32 s61,s61,1
s_cmp_lt_u32 s67,s59
v_mfma_f32_16x16x32_bf16 v[28:31],v[244:247],v[56:59],v[28:31]
ds_read_b128 v[52:55],v2 offset:2048
v_mfma_f32_16x16x32_bf16 v[32:35],v[248:251],v[56:59],v[32:35]
v_mfma_f32_16x16x32_bf16 v[36:39],v[252:255],v[56:59],v[36:39]
v_mfma_f32_16x16x32_bf16 v[40:43],v[88:91],v[56:59],v[40:43]
s_cbranch_scc1 Lg4_d0w1_%=
s_branch Lg4_slow0_%=
Lg4_d0w2_%=:
v_mfma_f32_16x16x32_bf16 v[12:15],a[0:3],v[44:47],0
s_add_u32 m0,s63,28672
ds_read_b128 v[56:59],v2 offset:3072
v_lshlrev_b32 v60,16,v68
v_mfma_f32_16x16x32_bf16 v[16:19],a[4:7],v[44:47],0
s_max_u32 s73,s47,s65
v_and_b32 v61,0xffff0000,v68
v_lshlrev_b32 v62,16,v69
v_mfma_f32_16x16x32_bf16 v[20:23],a[8:11],v[44:47],0
s_mov_b32 s65,0
v_and_b32 v63,0xffff0000,v69
v_lshlrev_b32 v64,16,v70
v_mfma_f32_16x16x32_bf16 v[24:27],a[12:15],v[44:47],0
s_waitcnt lgkmcnt(2)
buffer_load_dwordx4 v0,s[36:39],s61 offen offset:0 sc1 lds
v_mfma_f32_16x16x32_bf16 v[12:15],a[16:19],v[48:51],v[12:15]
s_add_u32 s53,s52,s55
ds_read_b128 v[44:47],v2 offset:4096
v_and_b32 v65,0xffff0000,v70
v_mfma_f32_16x16x32_bf16 v[16:19],a[20:23],v[48:51],v[16:19]
s_cmp_eq_u32 s53,s56
v_lshlrev_b32 v66,16,v71
v_and_b32 v67,0xffff0000,v71
v_mfma_f32_16x16x32_bf16 v[20:23],a[24:27],v[48:51],v[20:23]
s_cselect_b32 s53,s57,s53
v_add_f32 v28,v28,v60
v_add_f32 v29,v29,v61
v_mfma_f32_16x16x32_bf16 v[24:27],a[28:31],v[48:51],v[24:27]
s_waitcnt lgkmcnt(2)
buffer_load_dwordx4 v0,s[36:39],s61 offen offset:1024 sc1 lds
v_mfma_f32_16x16x32_bf16 v[12:15],a[32:35],v[52:55],v[12:15]
s_cmp_eq_u32 s53,s58
ds_read_b128 v[48:51],v2 offset:5120
v_add_f32 v30,v30,v62
v_mfma_f32_16x16x32_bf16 v[16:19],a[36:39],v[52:55],v[16:19]
s_cselect_b32 s53,s59,s53
v_add_f32 v31,v31,v63
v_add_f32 v32,v32,v64
v_mfma_f32_16x16x32_bf16 v[20:23],a[40:43],v[52:55],v[20:23]
s_cmp_ge_u32 s66,s59
v_add_f32 v33,v33,v65
v_add_f32 v34,v34,v66
v_mfma_f32_16x16x32_bf16 v[24:27],a[44:47],v[52:55],v[24:27]
s_waitcnt lgkmcnt(2)
buffer_load_dwordx4 v0,s[36:39],s61 offen offset:2048 sc1 lds
v_mfma_f32_16x16x32_bf16 v[12:15],a[48:51],v[56:59],v[12:15]
s_cselect_b32 s86,s49,s50
ds_read_b128 v[52:55],v2 offset:6144
v_add_f32 v35,v35,v67
v_mfma_f32_16x16x32_bf16 v[16:19],a[52:55],v[56:59],v[16:19]
v_cvt_pk_bf16_f32 v28,v28,v29
v_cvt_pk_bf16_f32 v29,v30,v31
v_mfma_f32_16x16x32_bf16 v[20:23],a[56:59],v[56:59],v[20:23]
v_cvt_pk_bf16_f32 v30,v32,v33
v_cvt_pk_bf16_f32 v31,v34,v35
v_mfma_f32_16x16x32_bf16 v[24:27],a[60:63],v[56:59],v[24:27]
s_waitcnt lgkmcnt(2)
buffer_load_dwordx4 v0,s[36:39],s61 offen offset:3072 sc1 lds
v_mfma_f32_16x16x32_bf16 v[12:15],a[64:67],v[44:47],v[12:15]
s_add_u32 m0,s64,28672
ds_read_b128 v[56:59],v2 offset:7168
v_lshlrev_b32 v60,16,v72
v_mfma_f32_16x16x32_bf16 v[16:19],a[68:71],v[44:47],v[16:19]
v_and_b32 v61,0xffff0000,v72
v_lshlrev_b32 v62,16,v73
v_mfma_f32_16x16x32_bf16 v[20:23],a[72:75],v[44:47],v[20:23]
v_and_b32 v63,0xffff0000,v73
v_lshlrev_b32 v64,16,v74
v_mfma_f32_16x16x32_bf16 v[24:27],a[76:79],v[44:47],v[24:27]
s_waitcnt lgkmcnt(2)
v_and_b32 v65,0xffff0000,v74
v_lshlrev_b32 v66,16,v75
v_mfma_f32_16x16x32_bf16 v[12:15],a[80:83],v[48:51],v[12:15]
buffer_store_dwordx4 v[28:31],v3,s[36:39],s73 offen offset:0 sc0 sc1
ds_read_b128 v[44:47],v2 offset:8192
v_and_b32 v67,0xffff0000,v75
v_add_f32 v36,v36,v60
v_mfma_f32_16x16x32_bf16 v[16:19],a[84:87],v[48:51],v[16:19]
v_add_f32 v37,v37,v61
v_add_f32 v38,v38,v62
v_mfma_f32_16x16x32_bf16 v[20:23],a[88:91],v[48:51],v[20:23]
v_add_f32 v39,v39,v63)"
R"(
v_add_f32 v40,v40,v64
v_mfma_f32_16x16x32_bf16 v[24:27],a[92:95],v[48:51],v[24:27]
s_waitcnt lgkmcnt(2)
v_add_f32 v41,v41,v65
v_add_f32 v42,v42,v66
v_mfma_f32_16x16x32_bf16 v[12:15],a[96:99],v[52:55],v[12:15]
ds_read_b128 v[48:51],v2 offset:9216
v_add_f32 v43,v43,v67
v_mfma_f32_16x16x32_bf16 v[16:19],a[100:103],v[52:55],v[16:19]
v_cvt_pk_bf16_f32 v36,v36,v37
v_cvt_pk_bf16_f32 v37,v38,v39
v_mfma_f32_16x16x32_bf16 v[20:23],a[104:107],v[52:55],v[20:23]
v_cvt_pk_bf16_f32 v38,v40,v41
v_cvt_pk_bf16_f32 v39,v42,v43
v_mfma_f32_16x16x32_bf16 v[24:27],a[108:111],v[52:55],v[24:27]
s_waitcnt lgkmcnt(2)
v_mfma_f32_16x16x32_bf16 v[12:15],a[112:115],v[56:59],v[12:15]
ds_read_b128 v[52:55],v2 offset:10240
v_mfma_f32_16x16x32_bf16 v[16:19],a[116:119],v[56:59],v[16:19]
v_mfma_f32_16x16x32_bf16 v[20:23],a[120:123],v[56:59],v[20:23]
buffer_store_dwordx4 v[36:39],v3,s[36:39],s73 offen offset:512 sc0 sc1
v_mfma_f32_16x16x32_bf16 v[24:27],a[124:127],v[56:59],v[24:27]
s_waitcnt lgkmcnt(2)
v_mfma_f32_16x16x32_bf16 v[12:15],a[128:131],v[44:47],v[12:15]
ds_read_b128 v[56:59],v2 offset:11264
v_mfma_f32_16x16x32_bf16 v[16:19],a[132:135],v[44:47],v[16:19]
v_mfma_f32_16x16x32_bf16 v[20:23],a[136:139],v[44:47],v[20:23]
v_mfma_f32_16x16x32_bf16 v[24:27],a[140:143],v[44:47],v[24:27]
s_waitcnt lgkmcnt(2)
buffer_load_dwordx4 v[68:71],v8,s[36:39],s48 offen offset:0 sc1
v_mfma_f32_16x16x32_bf16 v[12:15],a[144:147],v[48:51],v[12:15]
ds_read_b128 v[44:47],v2 offset:12288
v_mfma_f32_16x16x32_bf16 v[16:19],a[148:151],v[48:51],v[16:19]
v_mfma_f32_16x16x32_bf16 v[20:23],a[152:155],v[48:51],v[20:23]
v_mfma_f32_16x16x32_bf16 v[24:27],a[156:159],v[48:51],v[24:27]
s_waitcnt lgkmcnt(2)
buffer_load_dwordx4 v[72:75],v8,s[36:39],s48 offen offset:1024 sc1
v_mfma_f32_16x16x32_bf16 v[12:15],a[160:163],v[52:55],v[12:15]
ds_read_b128 v[48:51],v2 offset:13312
v_mfma_f32_16x16x32_bf16 v[16:19],a[164:167],v[52:55],v[16:19]
v_mfma_f32_16x16x32_bf16 v[20:23],a[168:171],v[52:55],v[20:23]
v_mfma_f32_16x16x32_bf16 v[24:27],a[172:175],v[52:55],v[24:27]
s_waitcnt lgkmcnt(2)
v_mfma_f32_16x16x32_bf16 v[12:15],a[176:179],v[56:59],v[12:15]
ds_read_b128 v[52:55],v2 offset:14336
v_mfma_f32_16x16x32_bf16 v[16:19],a[180:183],v[56:59],v[16:19]
v_mfma_f32_16x16x32_bf16 v[20:23],a[184:187],v[56:59],v[20:23]
v_mfma_f32_16x16x32_bf16 v[24:27],a[188:191],v[56:59],v[24:27]
s_waitcnt lgkmcnt(2)
v_mfma_f32_16x16x32_bf16 v[12:15],a[192:195],v[44:47],v[12:15]
ds_read_b128 v[56:59],v2 offset:15360
v_mfma_f32_16x16x32_bf16 v[16:19],a[196:199],v[44:47],v[16:19]
v_mfma_f32_16x16x32_bf16 v[20:23],a[200:203],v[44:47],v[20:23]
s_waitcnt vmcnt(8)
v_mfma_f32_16x16x32_bf16 v[24:27],a[204:207],v[44:47],v[24:27]
s_waitcnt lgkmcnt(2)
s_barrier
v_mfma_f32_16x16x32_bf16 v[12:15],a[208:211],v[48:51],v[12:15]
ds_read_b128 v[44:47],v2 offset:16384
v_mfma_f32_16x16x32_bf16 v[16:19],a[212:215],v[48:51],v[16:19]
v_mfma_f32_16x16x32_bf16 v[20:23],a[216:219],v[48:51],v[20:23]
v_mfma_f32_16x16x32_bf16 v[24:27],a[220:223],v[48:51],v[24:27]
s_waitcnt lgkmcnt(2)
buffer_load_dwordx4 v1,s[36:39],s61 offen offset:0 sc1 lds
v_mfma_f32_16x16x32_bf16 v[12:15],a[224:227],v[52:55],v[12:15]
ds_read_b128 v[48:51],v2 offset:17408
v_mfma_f32_16x16x32_bf16 v[16:19],a[228:231],v[52:55],v[16:19]
v_mfma_f32_16x16x32_bf16 v[20:23],a[232:235],v[52:55],v[20:23]
v_mfma_f32_16x16x32_bf16 v[24:27],a[236:239],v[52:55],v[24:27]
s_waitcnt lgkmcnt(2)
v_mfma_f32_16x16x32_bf16 v[12:15],a[240:243],v[56:59],v[12:15]
ds_read_b128 v[52:55],v2 offset:18432
v_mfma_f32_16x16x32_bf16 v[16:19],a[244:247],v[56:59],v[16:19]
buffer_load_dwordx4 v1,s[36:39],s61 offen offset:1024 sc1 lds
v_mfma_f32_16x16x32_bf16 v[20:23],a[248:251],v[56:59],v[20:23]
v_mfma_f32_16x16x32_bf16 v[24:27],a[252:255],v[56:59],v[24:27]
s_waitcnt lgkmcnt(2)
v_mfma_f32_16x16x32_bf16 v[12:15],v[100:103],v[44:47],v[12:15]
ds_read_b128 v[56:59],v2 offset:19456
v_mfma_f32_16x16x32_bf16 v[16:19],v[104:107],v[44:47],v[16:19]
ds_read_b128 v[88:91],v9 offset:0
v_mfma_f32_16x16x32_bf16 v[20:23],v[108:111],v[44:47],v[20:23]
v_mfma_f32_16x16x32_bf16 v[24:27],v[112:115],v[44:47],v[24:27]
s_waitcnt lgkmcnt(3)
buffer_load_dwordx4 v1,s[36:39],s61 offen offset:2048 sc1 lds
v_mfma_f32_16x16x32_bf16 v[12:15],v[116:119],v[48:51],v[12:15]
ds_read_b128 v[44:47],v2 offset:20480
v_mfma_f32_16x16x32_bf16 v[16:19],v[120:123],v[48:51],v[16:19]
ds_read_b128 v[76:79],v9 offset:1024
v_mfma_f32_16x16x32_bf16 v[20:23],v[124:127],v[48:51],v[20:23]
v_mfma_f32_16x16x32_bf16 v[24:27],v[128:131],v[48:51],v[24:27]
s_waitcnt lgkmcnt(4)
v_mfma_f32_16x16x32_bf16 v[12:15],v[132:135],v[52:55],v[12:15]
ds_read_b128 v[48:51],v2 offset:21504
v_mfma_f32_16x16x32_bf16 v[16:19],v[136:139],v[52:55],v[16:19]
ds_read_b128 v[80:83],v9 offset:2048
v_mfma_f32_16x16x32_bf16 v[20:23],v[140:143],v[52:55],v[20:23]
v_mfma_f32_16x16x32_bf16 v[24:27],v[144:147],v[52:55],v[24:27]
s_waitcnt lgkmcnt(4)
v_mfma_f32_16x16x32_bf16 v[12:15],v[148:151],v[56:59],v[12:15]
ds_read_b128 v[52:55],v2 offset:22528
v_mfma_f32_16x16x32_bf16 v[16:19],v[152:155],v[56:59],v[16:19]
ds_read_b128 v[84:87],v9 offset:3072
v_mfma_f32_16x16x32_bf16 v[20:23],v[156:159],v[56:59],v[20:23]
v_mfma_f32_16x16x32_bf16 v[24:27],v[88:91],v[56:59],v[24:27]
s_waitcnt lgkmcnt(4)
v_mfma_f32_16x16x32_bf16 v[12:15],v[160:163],v[44:47],v[12:15]
ds_read_b128 v[56:59],v2 offset:23552
v_mfma_f32_16x16x32_bf16 v[16:19],v[164:167],v[44:47],v[16:19]
ds_read_b128 v[88:91],v9 offset:4096
v_mfma_f32_16x16x32_bf16 v[20:23],v[168:171],v[44:47],v[20:23]
v_mfma_f32_16x16x32_bf16 v[24:27],v[76:79],v[44:47],v[24:27]
s_waitcnt lgkmcnt(4)
v_mfma_f32_16x16x32_bf16 v[12:15],v[172:175],v[48:51],v[12:15]
ds_read_b128 v[44:47],v2 offset:24576
v_mfma_f32_16x16x32_bf16 v[16:19],v[176:179],v[48:51],v[16:19]
ds_read_b128 v[76:79],v9 offset:5120
v_mfma_f32_16x16x32_bf16 v[20:23],v[180:183],v[48:51],v[20:23]
v_mfma_f32_16x16x32_bf16 v[24:27],v[80:83],v[48:51],v[24:27]
s_waitcnt lgkmcnt(4)
v_mfma_f32_16x16x32_bf16 v[12:15],v[184:187],v[52:55],v[12:15]
ds_read_b128 v[48:51],v2 offset:25600
v_mfma_f32_16x16x32_bf16 v[16:19],v[188:191],v[52:55],v[16:19]
ds_read_b128 v[80:83],v9 offset:6144
v_mfma_f32_16x16x32_bf16 v[20:23],v[192:195],v[52:55],v[20:23]
v_mfma_f32_16x16x32_bf16 v[24:27],v[84:87],v[52:55],v[24:27]
s_waitcnt lgkmcnt(4)
v_mfma_f32_16x16x32_bf16 v[12:15],v[196:199],v[56:59],v[12:15]
ds_read_b128 v[52:55],v2 offset:26624
v_mfma_f32_16x16x32_bf16 v[16:19],v[200:203],v[56:59],v[16:19]
ds_read_b128 v[84:87],v9 offset:7168
v_mfma_f32_16x16x32_bf16 v[20:23],v[204:207],v[56:59],v[20:23]
v_mfma_f32_16x16x32_bf16 v[24:27],v[88:91],v[56:59],v[24:27]
s_waitcnt lgkmcnt(4)
v_mfma_f32_16x16x32_bf16 v[12:15],v[208:211],v[44:47],v[12:15]
ds_read_b128 v[56:59],v2 offset:27648
v_mfma_f32_16x16x32_bf16 v[16:19],v[212:215],v[44:47],v[16:19]
ds_read_b128 v[88:91],v9 offset:8192
v_mfma_f32_16x16x32_bf16 v[20:23],v[216:219],v[44:47],v[20:23]
s_waitcnt vmcnt(3)
v_mfma_f32_16x16x32_bf16 v[24:27],v[76:79],v[44:47],v[24:27]
s_waitcnt lgkmcnt(4)
s_barrier
v_mfma_f32_16x16x32_bf16 v[12:15],v[220:223],v[48:51],v[12:15]
ds_read_b128 v[44:47],v2 offset:28672
ds_read_b32 v10,v7 offset:4096
v_mfma_f32_16x16x32_bf16 v[16:19],v[224:227],v[48:51],v[16:19]
v_mfma_f32_16x16x32_bf16 v[20:23],v[228:231],v[48:51],v[20:23]
v_mfma_f32_16x16x32_bf16 v[24:27],v[80:83],v[48:51],v[24:27]
s_waitcnt lgkmcnt(4)
v_mfma_f32_16x16x32_bf16 v[12:15],v[232:235],v[52:55],v[12:15]
s_waitcnt lgkmcnt(0)
ds_read_b128 v[48:51],v2 offset:29696
v_mfma_f32_16x16x32_bf16 v[16:19],v[236:239],v[52:55],v[16:19]
v_cmp_eq_u32 vcc,s60,v10
v_mfma_f32_16x16x32_bf16 v[20:23],v[240:243],v[52:55],v[20:23]
s_cmp_eq_u32 vcc_lo,-1
s_cselect_b32 s67,s86,s59
s_mul_i32 s61,s67,7
v_mfma_f32_16x16x32_bf16 v[24:27],v[84:87],v[52:55],v[24:27]
s_lshr_b32 s61,s61,1
s_cmp_ge_u32 s66,s59
v_mfma_f32_16x16x32_bf16 v[12:15],v[244:247],v[56:59],v[12:15]
ds_read_b128 v[52:55],v2 offset:30720
v_mfma_f32_16x16x32_bf16 v[16:19],v[248:251],v[56:59],v[16:19]
v_mfma_f32_16x16x32_bf16 v[20:23],v[252:255],v[56:59],v[20:23]
v_mfma_f32_16x16x32_bf16 v[24:27],v[88:91],v[56:59],v[24:27]
s_cbranch_scc1 Lg4_slow1_%=
Lg4_d1w2_%=:
v_mfma_f32_16x16x32_bf16 v[28:31],a[0:3],v[44:47],0
s_add_u32 m0,s63,0
ds_read_b128 v[56:59],v2 offset:31744
v_lshlrev_b32 v60,16,v68
v_mfma_f32_16x16x32_bf16 v[32:35],a[4:7],v[44:47],0
s_max_u32 s73,s48,s65
v_and_b32 v61,0xffff0000,v68
v_lshlrev_b32 v62,16,v69
v_mfma_f32_16x16x32_bf16 v[36:39],a[8:11],v[44:47],0
s_mov_b32 s65,0
v_and_b32 v63,0xffff0000,v69
v_lshlrev_b32 v64,16,v70
v_mfma_f32_16x16x32_bf16 v[40:43],a[12:15],v[44:47],0
s_waitcnt lgkmcnt(2)
buffer_load_dwordx4 v0,s[36:39],s61 offen offset:0 sc1 lds
v_mfma_f32_16x16x32_bf16 v[28:31],a[16:19],v[48:51],v[28:31]
s_add_u32 s74,s53,s55
ds_read_b128 v[44:47],v2 offset:32768
v_and_b32 v65,0xffff0000,v70
v_mfma_f32_16x16x32_bf16 v[32:35],a[20:23],v[48:51],v[32:35]
s_cmp_eq_u32 s74,s56
v_lshlrev_b32 v66,16,v71
v_and_b32 v67,0xffff0000,v71
v_mfma_f32_16x16x32_bf16 v[36:39],a[24:27],v[48:51],v[36:39]
s_cselect_b32 s74,s57,s74
v_add_f32 v12,v12,v60
v_add_f32 v13,v13,v61
v_mfma_f32_16x16x32_bf16 v[40:43],a[28:31],v[48:51],v[40:43]
s_waitcnt lgkmcnt(2)
buffer_load_dwordx4 v0,s[36:39],s61 offen offset:1024 sc1 lds
v_mfma_f32_16x16x32_bf16 v[28:31],a[32:35],v[52:55],v[28:31])"
R"(
s_cmp_eq_u32 s74,s58
ds_read_b128 v[48:51],v2 offset:33792
v_add_f32 v14,v14,v62
v_mfma_f32_16x16x32_bf16 v[32:35],a[36:39],v[52:55],v[32:35]
s_cselect_b32 s74,s59,s74
v_add_f32 v15,v15,v63
v_add_f32 v16,v16,v64
v_mfma_f32_16x16x32_bf16 v[36:39],a[40:43],v[52:55],v[36:39]
s_cmp_ge_u32 s67,s59
v_add_f32 v17,v17,v65
v_add_f32 v18,v18,v66
v_mfma_f32_16x16x32_bf16 v[40:43],a[44:47],v[52:55],v[40:43]
s_waitcnt lgkmcnt(2)
buffer_load_dwordx4 v0,s[36:39],s61 offen offset:2048 sc1 lds
v_mfma_f32_16x16x32_bf16 v[28:31],a[48:51],v[56:59],v[28:31]
s_cselect_b32 s86,s50,s51
ds_read_b128 v[52:55],v2 offset:34816
v_add_f32 v19,v19,v67
v_mfma_f32_16x16x32_bf16 v[32:35],a[52:55],v[56:59],v[32:35]
s_mov_b64 s[44:45],s[46:47]
v_cvt_pk_bf16_f32 v12,v12,v13
v_cvt_pk_bf16_f32 v13,v14,v15
v_mfma_f32_16x16x32_bf16 v[36:39],a[56:59],v[56:59],v[36:39]
s_mov_b64 s[46:47],s[48:49]
v_cvt_pk_bf16_f32 v14,v16,v17
v_cvt_pk_bf16_f32 v15,v18,v19
v_mfma_f32_16x16x32_bf16 v[40:43],a[60:63],v[56:59],v[40:43]
s_waitcnt lgkmcnt(2)
buffer_load_dwordx4 v0,s[36:39],s61 offen offset:3072 sc1 lds
v_mfma_f32_16x16x32_bf16 v[28:31],a[64:67],v[44:47],v[28:31]
s_add_u32 m0,s64,0
ds_read_b128 v[56:59],v2 offset:35840
v_lshlrev_b32 v60,16,v72
v_mfma_f32_16x16x32_bf16 v[32:35],a[68:71],v[44:47],v[32:35]
v_and_b32 v61,0xffff0000,v72
v_lshlrev_b32 v62,16,v73
v_mfma_f32_16x16x32_bf16 v[36:39],a[72:75],v[44:47],v[36:39]
v_and_b32 v63,0xffff0000,v73
v_lshlrev_b32 v64,16,v74
v_mfma_f32_16x16x32_bf16 v[40:43],a[76:79],v[44:47],v[40:43]
s_waitcnt lgkmcnt(2)
v_and_b32 v65,0xffff0000,v74
v_lshlrev_b32 v66,16,v75
v_mfma_f32_16x16x32_bf16 v[28:31],a[80:83],v[48:51],v[28:31]
buffer_store_dwordx4 v[12:15],v3,s[36:39],s73 offen offset:0 sc0 sc1
ds_read_b128 v[44:47],v2 offset:36864
v_and_b32 v67,0xffff0000,v75
v_add_f32 v20,v20,v60
v_mfma_f32_16x16x32_bf16 v[32:35],a[84:87],v[48:51],v[32:35]
v_add_f32 v21,v21,v61
v_add_f32 v22,v22,v62
v_mfma_f32_16x16x32_bf16 v[36:39],a[88:91],v[48:51],v[36:39]
v_add_f32 v23,v23,v63
v_add_f32 v24,v24,v64
v_mfma_f32_16x16x32_bf16 v[40:43],a[92:95],v[48:51],v[40:43]
s_waitcnt lgkmcnt(2)
v_add_f32 v25,v25,v65
v_add_f32 v26,v26,v66
v_mfma_f32_16x16x32_bf16 v[28:31],a[96:99],v[52:55],v[28:31]
ds_read_b128 v[48:51],v2 offset:37888
v_add_f32 v27,v27,v67
v_mfma_f32_16x16x32_bf16 v[32:35],a[100:103],v[52:55],v[32:35]
v_cvt_pk_bf16_f32 v20,v20,v21
v_cvt_pk_bf16_f32 v21,v22,v23
v_mfma_f32_16x16x32_bf16 v[36:39],a[104:107],v[52:55],v[36:39]
v_cvt_pk_bf16_f32 v22,v24,v25
v_cvt_pk_bf16_f32 v23,v26,v27
v_mfma_f32_16x16x32_bf16 v[40:43],a[108:111],v[52:55],v[40:43]
s_waitcnt lgkmcnt(2)
v_mfma_f32_16x16x32_bf16 v[28:31],a[112:115],v[56:59],v[28:31]
ds_read_b128 v[52:55],v2 offset:38912
v_mfma_f32_16x16x32_bf16 v[32:35],a[116:119],v[56:59],v[32:35]
v_mfma_f32_16x16x32_bf16 v[36:39],a[120:123],v[56:59],v[36:39]
buffer_store_dwordx4 v[20:23],v3,s[36:39],s73 offen offset:512 sc0 sc1
v_mfma_f32_16x16x32_bf16 v[40:43],a[124:127],v[56:59],v[40:43]
s_waitcnt lgkmcnt(2)
v_mfma_f32_16x16x32_bf16 v[28:31],a[128:131],v[44:47],v[28:31]
ds_read_b128 v[56:59],v2 offset:39936
v_mfma_f32_16x16x32_bf16 v[32:35],a[132:135],v[44:47],v[32:35]
v_mfma_f32_16x16x32_bf16 v[36:39],a[136:139],v[44:47],v[36:39]
v_mfma_f32_16x16x32_bf16 v[40:43],a[140:143],v[44:47],v[40:43]
s_waitcnt lgkmcnt(2)
buffer_load_dwordx4 v[68:71],v8,s[36:39],s49 offen offset:0 sc1
v_mfma_f32_16x16x32_bf16 v[28:31],a[144:147],v[48:51],v[28:31]
ds_read_b128 v[44:47],v2 offset:40960
v_mfma_f32_16x16x32_bf16 v[32:35],a[148:151],v[48:51],v[32:35]
v_mfma_f32_16x16x32_bf16 v[36:39],a[152:155],v[48:51],v[36:39]
v_mfma_f32_16x16x32_bf16 v[40:43],a[156:159],v[48:51],v[40:43]
s_waitcnt lgkmcnt(2)
buffer_load_dwordx4 v[72:75],v8,s[36:39],s49 offen offset:1024 sc1
v_mfma_f32_16x16x32_bf16 v[28:31],a[160:163],v[52:55],v[28:31]
s_mov_b64 s[48:49],s[50:51]
ds_read_b128 v[48:51],v2 offset:41984
s_mov_b64 s[50:51],s[52:53]
v_mfma_f32_16x16x32_bf16 v[32:35],a[164:167],v[52:55],v[32:35]
s_mov_b32 s52,s74
v_mfma_f32_16x16x32_bf16 v[36:39],a[168:171],v[52:55],v[36:39]
v_mfma_f32_16x16x32_bf16 v[40:43],a[172:175],v[52:55],v[40:43]
s_waitcnt lgkmcnt(2)
v_mfma_f32_16x16x32_bf16 v[28:31],a[176:179],v[56:59],v[28:31]
ds_read_b128 v[52:55],v2 offset:43008
v_mfma_f32_16x16x32_bf16 v[32:35],a[180:183],v[56:59],v[32:35]
v_mfma_f32_16x16x32_bf16 v[36:39],a[184:187],v[56:59],v[36:39]
v_mfma_f32_16x16x32_bf16 v[40:43],a[188:191],v[56:59],v[40:43]
s_waitcnt lgkmcnt(2)
v_mfma_f32_16x16x32_bf16 v[28:31],a[192:195],v[44:47],v[28:31]
ds_read_b128 v[56:59],v2 offset:44032
v_mfma_f32_16x16x32_bf16 v[32:35],a[196:199],v[44:47],v[32:35]
v_mfma_f32_16x16x32_bf16 v[36:39],a[200:203],v[44:47],v[36:39]
s_waitcnt vmcnt(8)
v_mfma_f32_16x16x32_bf16 v[40:43],a[204:207],v[44:47],v[40:43]
s_waitcnt lgkmcnt(2)
s_barrier
v_mfma_f32_16x16x32_bf16 v[28:31],a[208:211],v[48:51],v[28:31]
ds_read_b128 v[44:47],v2 offset:45056
v_mfma_f32_16x16x32_bf16 v[32:35],a[212:215],v[48:51],v[32:35]
v_mfma_f32_16x16x32_bf16 v[36:39],a[216:219],v[48:51],v[36:39]
v_mfma_f32_16x16x32_bf16 v[40:43],a[220:223],v[48:51],v[40:43]
s_waitcnt lgkmcnt(2)
buffer_load_dwordx4 v1,s[36:39],s61 offen offset:0 sc1 lds
v_mfma_f32_16x16x32_bf16 v[28:31],a[224:227],v[52:55],v[28:31]
ds_read_b128 v[48:51],v2 offset:46080
v_mfma_f32_16x16x32_bf16 v[32:35],a[228:231],v[52:55],v[32:35]
v_mfma_f32_16x16x32_bf16 v[36:39],a[232:235],v[52:55],v[36:39]
v_mfma_f32_16x16x32_bf16 v[40:43],a[236:239],v[52:55],v[40:43]
s_waitcnt lgkmcnt(2)
v_mfma_f32_16x16x32_bf16 v[28:31],a[240:243],v[56:59],v[28:31]
ds_read_b128 v[52:55],v2 offset:47104
v_mfma_f32_16x16x32_bf16 v[32:35],a[244:247],v[56:59],v[32:35]
buffer_load_dwordx4 v1,s[36:39],s61 offen offset:1024 sc1 lds
v_mfma_f32_16x16x32_bf16 v[36:39],a[248:251],v[56:59],v[36:39]
v_mfma_f32_16x16x32_bf16 v[40:43],a[252:255],v[56:59],v[40:43]
s_waitcnt lgkmcnt(2)
v_mfma_f32_16x16x32_bf16 v[28:31],v[100:103],v[44:47],v[28:31]
ds_read_b128 v[56:59],v2 offset:48128
v_mfma_f32_16x16x32_bf16 v[32:35],v[104:107],v[44:47],v[32:35]
ds_read_b128 v[88:91],v9 offset:0
v_mfma_f32_16x16x32_bf16 v[36:39],v[108:111],v[44:47],v[36:39]
v_mfma_f32_16x16x32_bf16 v[40:43],v[112:115],v[44:47],v[40:43]
s_waitcnt lgkmcnt(3)
buffer_load_dwordx4 v1,s[36:39],s61 offen offset:2048 sc1 lds
v_mfma_f32_16x16x32_bf16 v[28:31],v[116:119],v[48:51],v[28:31]
ds_read_b128 v[44:47],v2 offset:49152
v_mfma_f32_16x16x32_bf16 v[32:35],v[120:123],v[48:51],v[32:35]
ds_read_b128 v[76:79],v9 offset:1024
v_mfma_f32_16x16x32_bf16 v[36:39],v[124:127],v[48:51],v[36:39]
v_mfma_f32_16x16x32_bf16 v[40:43],v[128:131],v[48:51],v[40:43]
s_waitcnt lgkmcnt(4)
v_mfma_f32_16x16x32_bf16 v[28:31],v[132:135],v[52:55],v[28:31]
ds_read_b128 v[48:51],v2 offset:50176
v_mfma_f32_16x16x32_bf16 v[32:35],v[136:139],v[52:55],v[32:35]
ds_read_b128 v[80:83],v9 offset:2048
v_mfma_f32_16x16x32_bf16 v[36:39],v[140:143],v[52:55],v[36:39]
v_mfma_f32_16x16x32_bf16 v[40:43],v[144:147],v[52:55],v[40:43]
s_waitcnt lgkmcnt(4)
v_mfma_f32_16x16x32_bf16 v[28:31],v[148:151],v[56:59],v[28:31]
ds_read_b128 v[52:55],v2 offset:51200
v_mfma_f32_16x16x32_bf16 v[32:35],v[152:155],v[56:59],v[32:35]
ds_read_b128 v[84:87],v9 offset:3072
v_mfma_f32_16x16x32_bf16 v[36:39],v[156:159],v[56:59],v[36:39]
v_mfma_f32_16x16x32_bf16 v[40:43],v[88:91],v[56:59],v[40:43]
s_waitcnt lgkmcnt(4)
v_mfma_f32_16x16x32_bf16 v[28:31],v[160:163],v[44:47],v[28:31]
ds_read_b128 v[56:59],v2 offset:52224
v_mfma_f32_16x16x32_bf16 v[32:35],v[164:167],v[44:47],v[32:35]
ds_read_b128 v[88:91],v9 offset:4096
v_mfma_f32_16x16x32_bf16 v[36:39],v[168:171],v[44:47],v[36:39]
v_mfma_f32_16x16x32_bf16 v[40:43],v[76:79],v[44:47],v[40:43]
s_waitcnt lgkmcnt(4)
v_mfma_f32_16x16x32_bf16 v[28:31],v[172:175],v[48:51],v[28:31]
ds_read_b128 v[44:47],v2 offset:53248
v_mfma_f32_16x16x32_bf16 v[32:35],v[176:179],v[48:51],v[32:35]
ds_read_b128 v[76:79],v9 offset:5120
v_mfma_f32_16x16x32_bf16 v[36:39],v[180:183],v[48:51],v[36:39]
v_mfma_f32_16x16x32_bf16 v[40:43],v[80:83],v[48:51],v[40:43]
s_waitcnt lgkmcnt(4)
v_mfma_f32_16x16x32_bf16 v[28:31],v[184:187],v[52:55],v[28:31]
ds_read_b128 v[48:51],v2 offset:54272
v_mfma_f32_16x16x32_bf16 v[32:35],v[188:191],v[52:55],v[32:35]
ds_read_b128 v[80:83],v9 offset:6144
v_mfma_f32_16x16x32_bf16 v[36:39],v[192:195],v[52:55],v[36:39]
v_mfma_f32_16x16x32_bf16 v[40:43],v[84:87],v[52:55],v[40:43]
s_waitcnt lgkmcnt(4)
v_mfma_f32_16x16x32_bf16 v[28:31],v[196:199],v[56:59],v[28:31]
ds_read_b128 v[52:55],v2 offset:55296
v_mfma_f32_16x16x32_bf16 v[32:35],v[200:203],v[56:59],v[32:35]
ds_read_b128 v[84:87],v9 offset:7168
v_mfma_f32_16x16x32_bf16 v[36:39],v[204:207],v[56:59],v[36:39]
v_mfma_f32_16x16x32_bf16 v[40:43],v[88:91],v[56:59],v[40:43]
s_waitcnt lgkmcnt(4)
v_mfma_f32_16x16x32_bf16 v[28:31],v[208:211],v[44:47],v[28:31]
ds_read_b128 v[56:59],v2 offset:56320
v_mfma_f32_16x16x32_bf16 v[32:35],v[212:215],v[44:47],v[32:35]
ds_read_b128 v[88:91],v9 offset:8192
v_mfma_f32_16x16x32_bf16 v[36:39],v[216:219],v[44:47],v[36:39]
s_waitcnt vmcnt(3)
v_mfma_f32_16x16x32_bf16 v[40:43],v[76:79],v[44:47],v[40:43]
s_waitcnt lgkmcnt(4)
s_barrier
v_mfma_f32_16x16x32_bf16 v[28:31],v[220:223],v[48:51],v[28:31])"
R"(
ds_read_b128 v[44:47],v2 offset:0
ds_read_b32 v10,v7 offset:5120
v_mfma_f32_16x16x32_bf16 v[32:35],v[224:227],v[48:51],v[32:35]
v_mfma_f32_16x16x32_bf16 v[36:39],v[228:231],v[48:51],v[36:39]
v_mfma_f32_16x16x32_bf16 v[40:43],v[80:83],v[48:51],v[40:43]
s_waitcnt lgkmcnt(4)
v_mfma_f32_16x16x32_bf16 v[28:31],v[232:235],v[52:55],v[28:31]
s_waitcnt lgkmcnt(0)
ds_read_b128 v[48:51],v2 offset:1024
v_mfma_f32_16x16x32_bf16 v[32:35],v[236:239],v[52:55],v[32:35]
v_cmp_eq_u32 vcc,s60,v10
v_mfma_f32_16x16x32_bf16 v[36:39],v[240:243],v[52:55],v[36:39]
s_cmp_eq_u32 vcc_lo,-1
s_cselect_b32 s66,s86,s59
s_mul_i32 s61,s66,7
v_mfma_f32_16x16x32_bf16 v[40:43],v[84:87],v[52:55],v[40:43]
s_lshr_b32 s61,s61,1
s_cmp_lt_u32 s67,s59
v_mfma_f32_16x16x32_bf16 v[28:31],v[244:247],v[56:59],v[28:31]
ds_read_b128 v[52:55],v2 offset:2048
v_mfma_f32_16x16x32_bf16 v[32:35],v[248:251],v[56:59],v[32:35]
v_mfma_f32_16x16x32_bf16 v[36:39],v[252:255],v[56:59],v[36:39]
v_mfma_f32_16x16x32_bf16 v[40:43],v[88:91],v[56:59],v[40:43]
s_cbranch_scc1 Lg4_d0w2_%=
s_branch Lg4_slow0_%=
Lg4_d0w3_%=:
v_mfma_f32_16x16x32_bf16 v[12:15],a[0:3],v[44:47],0
s_add_u32 m0,s63,28672
ds_read_b128 v[56:59],v2 offset:3072
v_lshlrev_b32 v60,16,v68
v_mfma_f32_16x16x32_bf16 v[16:19],a[4:7],v[44:47],0
s_max_u32 s73,s47,s65
v_and_b32 v61,0xffff0000,v68
v_lshlrev_b32 v62,16,v69
v_mfma_f32_16x16x32_bf16 v[20:23],a[8:11],v[44:47],0
s_mov_b32 s65,0
v_and_b32 v63,0xffff0000,v69
v_lshlrev_b32 v64,16,v70
v_mfma_f32_16x16x32_bf16 v[24:27],a[12:15],v[44:47],0
s_waitcnt lgkmcnt(2)
v_and_b32 v65,0xffff0000,v70
v_lshlrev_b32 v66,16,v71
v_mfma_f32_16x16x32_bf16 v[12:15],a[16:19],v[48:51],v[12:15]
buffer_load_dwordx4 v0,s[36:39],s61 offen offset:0 sc1 lds
v_mfma_f32_16x16x32_bf16 v[16:19],a[20:23],v[48:51],v[16:19]
s_add_u32 s53,s52,s55
ds_read_b128 v[44:47],v2 offset:4096
v_and_b32 v67,0xffff0000,v71
v_mfma_f32_16x16x32_bf16 v[20:23],a[24:27],v[48:51],v[20:23]
s_cmp_eq_u32 s53,s56
v_add_f32 v28,v28,v60
v_add_f32 v29,v29,v61
v_mfma_f32_16x16x32_bf16 v[24:27],a[28:31],v[48:51],v[24:27]
s_waitcnt lgkmcnt(2)
v_add_f32 v30,v30,v62
v_add_f32 v31,v31,v63
v_mfma_f32_16x16x32_bf16 v[12:15],a[32:35],v[52:55],v[12:15]
buffer_load_dwordx4 v0,s[36:39],s61 offen offset:1024 sc1 lds
v_mfma_f32_16x16x32_bf16 v[16:19],a[36:39],v[52:55],v[16:19]
s_cselect_b32 s53,s57,s53
ds_read_b128 v[48:51],v2 offset:5120
v_add_f32 v32,v32,v64
v_mfma_f32_16x16x32_bf16 v[20:23],a[40:43],v[52:55],v[20:23]
s_cmp_eq_u32 s53,s58
v_add_f32 v33,v33,v65
v_add_f32 v34,v34,v66
v_mfma_f32_16x16x32_bf16 v[24:27],a[44:47],v[52:55],v[24:27]
s_waitcnt lgkmcnt(2)
v_add_f32 v35,v35,v67
v_cvt_pk_bf16_f32 v28,v28,v29
v_mfma_f32_16x16x32_bf16 v[12:15],a[48:51],v[56:59],v[12:15]
buffer_load_dwordx4 v0,s[36:39],s61 offen offset:2048 sc1 lds
v_mfma_f32_16x16x32_bf16 v[16:19],a[52:55],v[56:59],v[16:19]
s_cselect_b32 s53,s59,s53
ds_read_b128 v[52:55],v2 offset:6144
v_cvt_pk_bf16_f32 v29,v30,v31
v_mfma_f32_16x16x32_bf16 v[20:23],a[56:59],v[56:59],v[20:23]
s_cmp_ge_u32 s66,s59
v_cvt_pk_bf16_f32 v30,v32,v33
v_cvt_pk_bf16_f32 v31,v34,v35
v_mfma_f32_16x16x32_bf16 v[24:27],a[60:63],v[56:59],v[24:27]
s_waitcnt lgkmcnt(2)
v_lshlrev_b32 v60,16,v72
v_and_b32 v61,0xffff0000,v72
v_mfma_f32_16x16x32_bf16 v[12:15],a[64:67],v[44:47],v[12:15]
buffer_load_dwordx4 v0,s[36:39],s61 offen offset:3072 sc1 lds
v_mfma_f32_16x16x32_bf16 v[16:19],a[68:71],v[44:47],v[16:19]
s_cselect_b32 s86,s49,s50
ds_read_b128 v[56:59],v2 offset:7168
v_lshlrev_b32 v62,16,v73
v_mfma_f32_16x16x32_bf16 v[20:23],a[72:75],v[44:47],v[20:23]
s_add_u32 m0,s64,28672
v_and_b32 v63,0xffff0000,v73
v_lshlrev_b32 v64,16,v74
v_mfma_f32_16x16x32_bf16 v[24:27],a[76:79],v[44:47],v[24:27]
s_waitcnt lgkmcnt(2)
v_and_b32 v65,0xffff0000,v74
v_lshlrev_b32 v66,16,v75
v_mfma_f32_16x16x32_bf16 v[12:15],a[80:83],v[48:51],v[12:15]
s_lshr_b32 s70,s47,4
ds_read_b128 v[44:47],v2 offset:8192
v_and_b32 v67,0xffff0000,v75
v_mfma_f32_16x16x32_bf16 v[16:19],a[84:87],v[48:51],v[16:19]
buffer_store_dwordx4 v[28:31],v3,s[36:39],s73 offen offset:0 sc0 sc1
v_add_f32 v36,v36,v60
v_add_f32 v37,v37,v61
v_add_f32 v38,v38,v62
v_mfma_f32_16x16x32_bf16 v[20:23],a[88:91],v[48:51],v[20:23]
v_add_f32 v39,v39,v63
v_add_f32 v40,v40,v64
v_mfma_f32_16x16x32_bf16 v[24:27],a[92:95],v[48:51],v[24:27]
s_waitcnt lgkmcnt(2)
v_add_f32 v41,v41,v65
v_add_f32 v42,v42,v66
v_mfma_f32_16x16x32_bf16 v[12:15],a[96:99],v[52:55],v[12:15]
ds_read_b128 v[48:51],v2 offset:9216
v_add_f32 v43,v43,v67
v_mfma_f32_16x16x32_bf16 v[16:19],a[100:103],v[52:55],v[16:19]
v_cvt_pk_bf16_f32 v36,v36,v37
v_cvt_pk_bf16_f32 v37,v38,v39
v_mfma_f32_16x16x32_bf16 v[20:23],a[104:107],v[52:55],v[20:23]
v_cvt_pk_bf16_f32 v38,v40,v41
v_cvt_pk_bf16_f32 v39,v42,v43
v_mfma_f32_16x16x32_bf16 v[24:27],a[108:111],v[52:55],v[24:27]
s_waitcnt lgkmcnt(2)
v_mfma_f32_16x16x32_bf16 v[12:15],a[112:115],v[56:59],v[12:15]
ds_read_b128 v[52:55],v2 offset:10240
v_mfma_f32_16x16x32_bf16 v[16:19],a[116:119],v[56:59],v[16:19]
v_mfma_f32_16x16x32_bf16 v[20:23],a[120:123],v[56:59],v[20:23]
v_mfma_f32_16x16x32_bf16 v[24:27],a[124:127],v[56:59],v[24:27]
s_waitcnt lgkmcnt(2)
buffer_store_dwordx4 v[36:39],v3,s[36:39],s73 offen offset:512 sc0 sc1
v_mfma_f32_16x16x32_bf16 v[12:15],a[128:131],v[44:47],v[12:15]
ds_read_b128 v[56:59],v2 offset:11264
v_mfma_f32_16x16x32_bf16 v[16:19],a[132:135],v[44:47],v[16:19]
v_mfma_f32_16x16x32_bf16 v[20:23],a[136:139],v[44:47],v[20:23]
v_mfma_f32_16x16x32_bf16 v[24:27],a[140:143],v[44:47],v[24:27]
s_waitcnt lgkmcnt(2)
v_mfma_f32_16x16x32_bf16 v[12:15],a[144:147],v[48:51],v[12:15]
buffer_load_dwordx4 v[68:71],v8,s[36:39],s48 offen offset:0 sc1
v_mfma_f32_16x16x32_bf16 v[16:19],a[148:151],v[48:51],v[16:19]
ds_read_b128 v[44:47],v2 offset:12288
v_mfma_f32_16x16x32_bf16 v[20:23],a[152:155],v[48:51],v[20:23]
v_mfma_f32_16x16x32_bf16 v[24:27],a[156:159],v[48:51],v[24:27]
s_waitcnt lgkmcnt(2)
v_mfma_f32_16x16x32_bf16 v[12:15],a[160:163],v[52:55],v[12:15]
buffer_load_dwordx4 v[72:75],v8,s[36:39],s48 offen offset:1024 sc1
v_mfma_f32_16x16x32_bf16 v[16:19],a[164:167],v[52:55],v[16:19]
ds_read_b128 v[48:51],v2 offset:13312
v_mfma_f32_16x16x32_bf16 v[20:23],a[168:171],v[52:55],v[20:23]
v_mfma_f32_16x16x32_bf16 v[24:27],a[172:175],v[52:55],v[24:27]
s_waitcnt lgkmcnt(2)
v_mfma_f32_16x16x32_bf16 v[12:15],a[176:179],v[56:59],v[12:15]
ds_read_b128 v[52:55],v2 offset:14336
v_mfma_f32_16x16x32_bf16 v[16:19],a[180:183],v[56:59],v[16:19]
v_mfma_f32_16x16x32_bf16 v[20:23],a[184:187],v[56:59],v[20:23]
v_mfma_f32_16x16x32_bf16 v[24:27],a[188:191],v[56:59],v[24:27]
s_waitcnt lgkmcnt(2)
v_mfma_f32_16x16x32_bf16 v[12:15],a[192:195],v[44:47],v[12:15]
ds_read_b128 v[56:59],v2 offset:15360
v_mfma_f32_16x16x32_bf16 v[16:19],a[196:199],v[44:47],v[16:19]
v_mfma_f32_16x16x32_bf16 v[20:23],a[200:203],v[44:47],v[20:23]
s_waitcnt vmcnt(9)
v_mfma_f32_16x16x32_bf16 v[24:27],a[204:207],v[44:47],v[24:27]
s_waitcnt lgkmcnt(2)
s_barrier
v_mfma_f32_16x16x32_bf16 v[12:15],a[208:211],v[48:51],v[12:15]
ds_read_b128 v[44:47],v2 offset:16384
v_mfma_f32_16x16x32_bf16 v[16:19],a[212:215],v[48:51],v[16:19]
v_mfma_f32_16x16x32_bf16 v[20:23],a[216:219],v[48:51],v[20:23]
v_mfma_f32_16x16x32_bf16 v[24:27],a[220:223],v[48:51],v[24:27]
s_waitcnt lgkmcnt(2)
v_mfma_f32_16x16x32_bf16 v[12:15],a[224:227],v[52:55],v[12:15]
buffer_load_dwordx4 v1,s[36:39],s61 offen offset:0 sc1 lds
v_mfma_f32_16x16x32_bf16 v[16:19],a[228:231],v[52:55],v[16:19]
ds_read_b128 v[48:51],v2 offset:17408
v_mfma_f32_16x16x32_bf16 v[20:23],a[232:235],v[52:55],v[20:23]
v_mfma_f32_16x16x32_bf16 v[24:27],a[236:239],v[52:55],v[24:27]
s_waitcnt lgkmcnt(2)
v_mfma_f32_16x16x32_bf16 v[12:15],a[240:243],v[56:59],v[12:15]
ds_read_b128 v[52:55],v2 offset:18432
v_mfma_f32_16x16x32_bf16 v[16:19],a[244:247],v[56:59],v[16:19]
v_mfma_f32_16x16x32_bf16 v[20:23],a[248:251],v[56:59],v[20:23]
buffer_load_dwordx4 v1,s[36:39],s61 offen offset:1024 sc1 lds
v_mfma_f32_16x16x32_bf16 v[24:27],a[252:255],v[56:59],v[24:27]
s_waitcnt lgkmcnt(2)
v_mfma_f32_16x16x32_bf16 v[12:15],v[100:103],v[44:47],v[12:15]
ds_read_b128 v[56:59],v2 offset:19456
v_mfma_f32_16x16x32_bf16 v[16:19],v[104:107],v[44:47],v[16:19]
ds_read_b128 v[88:91],v9 offset:0
v_mfma_f32_16x16x32_bf16 v[20:23],v[108:111],v[44:47],v[20:23]
v_mfma_f32_16x16x32_bf16 v[24:27],v[112:115],v[44:47],v[24:27]
s_waitcnt lgkmcnt(3)
v_mfma_f32_16x16x32_bf16 v[12:15],v[116:119],v[48:51],v[12:15]
buffer_load_dwordx4 v1,s[36:39],s61 offen offset:2048 sc1 lds
v_mfma_f32_16x16x32_bf16 v[16:19],v[120:123],v[48:51],v[16:19]
ds_read_b128 v[44:47],v2 offset:20480
ds_read_b128 v[76:79],v9 offset:1024
v_mfma_f32_16x16x32_bf16 v[20:23],v[124:127],v[48:51],v[20:23]
v_mfma_f32_16x16x32_bf16 v[24:27],v[128:131],v[48:51],v[24:27])"
R"(
s_waitcnt lgkmcnt(4)
v_mfma_f32_16x16x32_bf16 v[12:15],v[132:135],v[52:55],v[12:15]
ds_read_b128 v[48:51],v2 offset:21504
v_mfma_f32_16x16x32_bf16 v[16:19],v[136:139],v[52:55],v[16:19]
ds_read_b128 v[80:83],v9 offset:2048
v_mfma_f32_16x16x32_bf16 v[20:23],v[140:143],v[52:55],v[20:23]
v_mfma_f32_16x16x32_bf16 v[24:27],v[144:147],v[52:55],v[24:27]
s_waitcnt lgkmcnt(4)
v_mfma_f32_16x16x32_bf16 v[12:15],v[148:151],v[56:59],v[12:15]
ds_read_b128 v[52:55],v2 offset:22528
v_mfma_f32_16x16x32_bf16 v[16:19],v[152:155],v[56:59],v[16:19]
ds_read_b128 v[84:87],v9 offset:3072
v_mfma_f32_16x16x32_bf16 v[20:23],v[156:159],v[56:59],v[20:23]
v_mfma_f32_16x16x32_bf16 v[24:27],v[88:91],v[56:59],v[24:27]
s_waitcnt lgkmcnt(4)
v_mfma_f32_16x16x32_bf16 v[12:15],v[160:163],v[44:47],v[12:15]
ds_read_b128 v[56:59],v2 offset:23552
v_mfma_f32_16x16x32_bf16 v[16:19],v[164:167],v[44:47],v[16:19]
ds_read_b128 v[88:91],v9 offset:4096
v_mfma_f32_16x16x32_bf16 v[20:23],v[168:171],v[44:47],v[20:23]
v_mfma_f32_16x16x32_bf16 v[24:27],v[76:79],v[44:47],v[24:27]
s_waitcnt lgkmcnt(4)
v_mfma_f32_16x16x32_bf16 v[12:15],v[172:175],v[48:51],v[12:15]
ds_read_b128 v[44:47],v2 offset:24576
v_mfma_f32_16x16x32_bf16 v[16:19],v[176:179],v[48:51],v[16:19]
ds_read_b128 v[76:79],v9 offset:5120
v_mfma_f32_16x16x32_bf16 v[20:23],v[180:183],v[48:51],v[20:23]
v_mfma_f32_16x16x32_bf16 v[24:27],v[80:83],v[48:51],v[24:27]
s_waitcnt lgkmcnt(4)
v_mfma_f32_16x16x32_bf16 v[12:15],v[184:187],v[52:55],v[12:15]
ds_read_b128 v[48:51],v2 offset:25600
v_mfma_f32_16x16x32_bf16 v[16:19],v[188:191],v[52:55],v[16:19]
ds_read_b128 v[80:83],v9 offset:6144
v_mfma_f32_16x16x32_bf16 v[20:23],v[192:195],v[52:55],v[20:23]
v_mfma_f32_16x16x32_bf16 v[24:27],v[84:87],v[52:55],v[24:27]
s_waitcnt lgkmcnt(4)
v_mfma_f32_16x16x32_bf16 v[12:15],v[196:199],v[56:59],v[12:15]
ds_read_b128 v[52:55],v2 offset:26624
v_mfma_f32_16x16x32_bf16 v[16:19],v[200:203],v[56:59],v[16:19]
ds_read_b128 v[84:87],v9 offset:7168
v_mfma_f32_16x16x32_bf16 v[20:23],v[204:207],v[56:59],v[20:23]
v_mfma_f32_16x16x32_bf16 v[24:27],v[88:91],v[56:59],v[24:27]
s_waitcnt lgkmcnt(4)
v_mfma_f32_16x16x32_bf16 v[12:15],v[208:211],v[44:47],v[12:15]
ds_read_b128 v[56:59],v2 offset:27648
v_mfma_f32_16x16x32_bf16 v[16:19],v[212:215],v[44:47],v[16:19]
ds_read_b128 v[88:91],v9 offset:8192
v_mfma_f32_16x16x32_bf16 v[20:23],v[216:219],v[44:47],v[20:23]
s_waitcnt vmcnt(3)
v_mfma_f32_16x16x32_bf16 v[24:27],v[76:79],v[44:47],v[24:27]
s_waitcnt lgkmcnt(4)
s_barrier
v_mfma_f32_16x16x32_bf16 v[12:15],v[220:223],v[48:51],v[12:15]
buffer_store_dword v5,v4,s[36:39],s70 offen offset:0 sc0 sc1
ds_read_b128 v[44:47],v2 offset:28672
ds_read_b32 v10,v7 offset:4096
v_mfma_f32_16x16x32_bf16 v[16:19],v[224:227],v[48:51],v[16:19]
v_mfma_f32_16x16x32_bf16 v[20:23],v[228:231],v[48:51],v[20:23]
v_mfma_f32_16x16x32_bf16 v[24:27],v[80:83],v[48:51],v[24:27]
s_waitcnt lgkmcnt(4)
v_mfma_f32_16x16x32_bf16 v[12:15],v[232:235],v[52:55],v[12:15]
s_waitcnt lgkmcnt(0)
ds_read_b128 v[48:51],v2 offset:29696
v_mfma_f32_16x16x32_bf16 v[16:19],v[236:239],v[52:55],v[16:19]
v_cmp_eq_u32 vcc,s60,v10
v_mfma_f32_16x16x32_bf16 v[20:23],v[240:243],v[52:55],v[20:23]
s_cmp_eq_u32 vcc_lo,-1
s_cselect_b32 s67,s86,s59
s_mul_i32 s61,s67,7
v_mfma_f32_16x16x32_bf16 v[24:27],v[84:87],v[52:55],v[24:27]
s_lshr_b32 s61,s61,1
s_cmp_ge_u32 s66,s59
v_mfma_f32_16x16x32_bf16 v[12:15],v[244:247],v[56:59],v[12:15]
ds_read_b128 v[52:55],v2 offset:30720
v_mfma_f32_16x16x32_bf16 v[16:19],v[248:251],v[56:59],v[16:19]
v_mfma_f32_16x16x32_bf16 v[20:23],v[252:255],v[56:59],v[20:23]
v_mfma_f32_16x16x32_bf16 v[24:27],v[88:91],v[56:59],v[24:27]
s_cbranch_scc1 Lg4_slow1_%=
Lg4_d1w3_%=:
v_mfma_f32_16x16x32_bf16 v[28:31],a[0:3],v[44:47],0
s_add_u32 m0,s63,0
ds_read_b128 v[56:59],v2 offset:31744
v_lshlrev_b32 v60,16,v68
v_mfma_f32_16x16x32_bf16 v[32:35],a[4:7],v[44:47],0
s_max_u32 s73,s48,s65
v_and_b32 v61,0xffff0000,v68
v_lshlrev_b32 v62,16,v69
v_mfma_f32_16x16x32_bf16 v[36:39],a[8:11],v[44:47],0
s_mov_b32 s65,0
v_and_b32 v63,0xffff0000,v69
v_lshlrev_b32 v64,16,v70
v_mfma_f32_16x16x32_bf16 v[40:43],a[12:15],v[44:47],0
s_waitcnt lgkmcnt(2)
v_and_b32 v65,0xffff0000,v70
v_lshlrev_b32 v66,16,v71
v_mfma_f32_16x16x32_bf16 v[28:31],a[16:19],v[48:51],v[28:31]
buffer_load_dwordx4 v0,s[36:39],s61 offen offset:0 sc1 lds
v_mfma_f32_16x16x32_bf16 v[32:35],a[20:23],v[48:51],v[32:35]
s_add_u32 s74,s53,s55
ds_read_b128 v[44:47],v2 offset:32768
v_and_b32 v67,0xffff0000,v71
v_mfma_f32_16x16x32_bf16 v[36:39],a[24:27],v[48:51],v[36:39]
s_cmp_eq_u32 s74,s56
v_add_f32 v12,v12,v60
v_add_f32 v13,v13,v61
v_mfma_f32_16x16x32_bf16 v[40:43],a[28:31],v[48:51],v[40:43]
s_waitcnt lgkmcnt(2)
v_add_f32 v14,v14,v62
v_add_f32 v15,v15,v63
v_mfma_f32_16x16x32_bf16 v[28:31],a[32:35],v[52:55],v[28:31]
buffer_load_dwordx4 v0,s[36:39],s61 offen offset:1024 sc1 lds
v_mfma_f32_16x16x32_bf16 v[32:35],a[36:39],v[52:55],v[32:35]
s_cselect_b32 s74,s57,s74
ds_read_b128 v[48:51],v2 offset:33792
v_add_f32 v16,v16,v64
v_mfma_f32_16x16x32_bf16 v[36:39],a[40:43],v[52:55],v[36:39]
s_cmp_eq_u32 s74,s58
v_add_f32 v17,v17,v65
v_add_f32 v18,v18,v66
v_mfma_f32_16x16x32_bf16 v[40:43],a[44:47],v[52:55],v[40:43]
s_waitcnt lgkmcnt(2)
v_add_f32 v19,v19,v67
v_cvt_pk_bf16_f32 v12,v12,v13
v_mfma_f32_16x16x32_bf16 v[28:31],a[48:51],v[56:59],v[28:31]
buffer_load_dwordx4 v0,s[36:39],s61 offen offset:2048 sc1 lds
v_mfma_f32_16x16x32_bf16 v[32:35],a[52:55],v[56:59],v[32:35]
s_cselect_b32 s74,s59,s74
ds_read_b128 v[52:55],v2 offset:34816
v_cvt_pk_bf16_f32 v13,v14,v15
v_mfma_f32_16x16x32_bf16 v[36:39],a[56:59],v[56:59],v[36:39]
s_cmp_ge_u32 s67,s59
v_cvt_pk_bf16_f32 v14,v16,v17
v_cvt_pk_bf16_f32 v15,v18,v19
v_mfma_f32_16x16x32_bf16 v[40:43],a[60:63],v[56:59],v[40:43]
s_waitcnt lgkmcnt(2)
v_lshlrev_b32 v60,16,v72
v_and_b32 v61,0xffff0000,v72
v_mfma_f32_16x16x32_bf16 v[28:31],a[64:67],v[44:47],v[28:31]
buffer_load_dwordx4 v0,s[36:39],s61 offen offset:3072 sc1 lds
v_mfma_f32_16x16x32_bf16 v[32:35],a[68:71],v[44:47],v[32:35]
s_cselect_b32 s86,s50,s51
ds_read_b128 v[56:59],v2 offset:35840
v_lshlrev_b32 v62,16,v73
v_mfma_f32_16x16x32_bf16 v[36:39],a[72:75],v[44:47],v[36:39]
s_add_u32 m0,s64,0
v_and_b32 v63,0xffff0000,v73
v_lshlrev_b32 v64,16,v74
v_mfma_f32_16x16x32_bf16 v[40:43],a[76:79],v[44:47],v[40:43]
s_waitcnt lgkmcnt(2)
v_and_b32 v65,0xffff0000,v74
v_lshlrev_b32 v66,16,v75
v_mfma_f32_16x16x32_bf16 v[28:31],a[80:83],v[48:51],v[28:31]
s_lshr_b32 s70,s48,4
ds_read_b128 v[44:47],v2 offset:36864
v_and_b32 v67,0xffff0000,v75
v_mfma_f32_16x16x32_bf16 v[32:35],a[84:87],v[48:51],v[32:35]
buffer_store_dwordx4 v[12:15],v3,s[36:39],s73 offen offset:0 sc0 sc1
v_add_f32 v20,v20,v60
v_add_f32 v21,v21,v61
v_add_f32 v22,v22,v62
v_mfma_f32_16x16x32_bf16 v[36:39],a[88:91],v[48:51],v[36:39]
s_mov_b64 s[44:45],s[46:47]
v_add_f32 v23,v23,v63
v_add_f32 v24,v24,v64
v_mfma_f32_16x16x32_bf16 v[40:43],a[92:95],v[48:51],v[40:43]
s_waitcnt lgkmcnt(2)
v_add_f32 v25,v25,v65
v_add_f32 v26,v26,v66
v_mfma_f32_16x16x32_bf16 v[28:31],a[96:99],v[52:55],v[28:31]
s_mov_b64 s[46:47],s[48:49]
ds_read_b128 v[48:51],v2 offset:37888
v_add_f32 v27,v27,v67
v_mfma_f32_16x16x32_bf16 v[32:35],a[100:103],v[52:55],v[32:35]
v_cvt_pk_bf16_f32 v20,v20,v21
v_cvt_pk_bf16_f32 v21,v22,v23
v_mfma_f32_16x16x32_bf16 v[36:39],a[104:107],v[52:55],v[36:39]
v_cvt_pk_bf16_f32 v22,v24,v25
v_cvt_pk_bf16_f32 v23,v26,v27
v_mfma_f32_16x16x32_bf16 v[40:43],a[108:111],v[52:55],v[40:43]
s_waitcnt lgkmcnt(2)
v_mfma_f32_16x16x32_bf16 v[28:31],a[112:115],v[56:59],v[28:31]
ds_read_b128 v[52:55],v2 offset:38912
v_mfma_f32_16x16x32_bf16 v[32:35],a[116:119],v[56:59],v[32:35]
v_mfma_f32_16x16x32_bf16 v[36:39],a[120:123],v[56:59],v[36:39]
v_mfma_f32_16x16x32_bf16 v[40:43],a[124:127],v[56:59],v[40:43]
s_waitcnt lgkmcnt(2)
buffer_store_dwordx4 v[20:23],v3,s[36:39],s73 offen offset:512 sc0 sc1
v_mfma_f32_16x16x32_bf16 v[28:31],a[128:131],v[44:47],v[28:31]
ds_read_b128 v[56:59],v2 offset:39936
v_mfma_f32_16x16x32_bf16 v[32:35],a[132:135],v[44:47],v[32:35]
v_mfma_f32_16x16x32_bf16 v[36:39],a[136:139],v[44:47],v[36:39]
v_mfma_f32_16x16x32_bf16 v[40:43],a[140:143],v[44:47],v[40:43]
s_waitcnt lgkmcnt(2)
v_mfma_f32_16x16x32_bf16 v[28:31],a[144:147],v[48:51],v[28:31]
buffer_load_dwordx4 v[68:71],v8,s[36:39],s49 offen offset:0 sc1
v_mfma_f32_16x16x32_bf16 v[32:35],a[148:151],v[48:51],v[32:35]
ds_read_b128 v[44:47],v2 offset:40960
v_mfma_f32_16x16x32_bf16 v[36:39],a[152:155],v[48:51],v[36:39]
v_mfma_f32_16x16x32_bf16 v[40:43],a[156:159],v[48:51],v[40:43]
s_waitcnt lgkmcnt(2))"
R"(
v_mfma_f32_16x16x32_bf16 v[28:31],a[160:163],v[52:55],v[28:31]
buffer_load_dwordx4 v[72:75],v8,s[36:39],s49 offen offset:1024 sc1
v_mfma_f32_16x16x32_bf16 v[32:35],a[164:167],v[52:55],v[32:35]
s_mov_b64 s[48:49],s[50:51]
ds_read_b128 v[48:51],v2 offset:41984
s_mov_b64 s[50:51],s[52:53]
v_mfma_f32_16x16x32_bf16 v[36:39],a[168:171],v[52:55],v[36:39]
s_mov_b32 s52,s74
v_mfma_f32_16x16x32_bf16 v[40:43],a[172:175],v[52:55],v[40:43]
s_waitcnt lgkmcnt(2)
v_mfma_f32_16x16x32_bf16 v[28:31],a[176:179],v[56:59],v[28:31]
ds_read_b128 v[52:55],v2 offset:43008
v_mfma_f32_16x16x32_bf16 v[32:35],a[180:183],v[56:59],v[32:35]
v_mfma_f32_16x16x32_bf16 v[36:39],a[184:187],v[56:59],v[36:39]
v_mfma_f32_16x16x32_bf16 v[40:43],a[188:191],v[56:59],v[40:43]
s_waitcnt lgkmcnt(2)
v_mfma_f32_16x16x32_bf16 v[28:31],a[192:195],v[44:47],v[28:31]
ds_read_b128 v[56:59],v2 offset:44032
v_mfma_f32_16x16x32_bf16 v[32:35],a[196:199],v[44:47],v[32:35]
v_mfma_f32_16x16x32_bf16 v[36:39],a[200:203],v[44:47],v[36:39]
s_waitcnt vmcnt(9)
v_mfma_f32_16x16x32_bf16 v[40:43],a[204:207],v[44:47],v[40:43]
s_waitcnt lgkmcnt(2)
s_barrier
v_mfma_f32_16x16x32_bf16 v[28:31],a[208:211],v[48:51],v[28:31]
ds_read_b128 v[44:47],v2 offset:45056
v_mfma_f32_16x16x32_bf16 v[32:35],a[212:215],v[48:51],v[32:35]
v_mfma_f32_16x16x32_bf16 v[36:39],a[216:219],v[48:51],v[36:39]
v_mfma_f32_16x16x32_bf16 v[40:43],a[220:223],v[48:51],v[40:43]
s_waitcnt lgkmcnt(2)
v_mfma_f32_16x16x32_bf16 v[28:31],a[224:227],v[52:55],v[28:31]
buffer_load_dwordx4 v1,s[36:39],s61 offen offset:0 sc1 lds
v_mfma_f32_16x16x32_bf16 v[32:35],a[228:231],v[52:55],v[32:35]
ds_read_b128 v[48:51],v2 offset:46080
v_mfma_f32_16x16x32_bf16 v[36:39],a[232:235],v[52:55],v[36:39]
v_mfma_f32_16x16x32_bf16 v[40:43],a[236:239],v[52:55],v[40:43]
s_waitcnt lgkmcnt(2)
v_mfma_f32_16x16x32_bf16 v[28:31],a[240:243],v[56:59],v[28:31]
ds_read_b128 v[52:55],v2 offset:47104
v_mfma_f32_16x16x32_bf16 v[32:35],a[244:247],v[56:59],v[32:35]
v_mfma_f32_16x16x32_bf16 v[36:39],a[248:251],v[56:59],v[36:39]
buffer_load_dwordx4 v1,s[36:39],s61 offen offset:1024 sc1 lds
v_mfma_f32_16x16x32_bf16 v[40:43],a[252:255],v[56:59],v[40:43]
s_waitcnt lgkmcnt(2)
v_mfma_f32_16x16x32_bf16 v[28:31],v[100:103],v[44:47],v[28:31]
ds_read_b128 v[56:59],v2 offset:48128
v_mfma_f32_16x16x32_bf16 v[32:35],v[104:107],v[44:47],v[32:35]
ds_read_b128 v[88:91],v9 offset:0
v_mfma_f32_16x16x32_bf16 v[36:39],v[108:111],v[44:47],v[36:39]
v_mfma_f32_16x16x32_bf16 v[40:43],v[112:115],v[44:47],v[40:43]
s_waitcnt lgkmcnt(3)
v_mfma_f32_16x16x32_bf16 v[28:31],v[116:119],v[48:51],v[28:31]
buffer_load_dwordx4 v1,s[36:39],s61 offen offset:2048 sc1 lds
v_mfma_f32_16x16x32_bf16 v[32:35],v[120:123],v[48:51],v[32:35]
ds_read_b128 v[44:47],v2 offset:49152
ds_read_b128 v[76:79],v9 offset:1024
v_mfma_f32_16x16x32_bf16 v[36:39],v[124:127],v[48:51],v[36:39]
v_mfma_f32_16x16x32_bf16 v[40:43],v[128:131],v[48:51],v[40:43]
s_waitcnt lgkmcnt(4)
v_mfma_f32_16x16x32_bf16 v[28:31],v[132:135],v[52:55],v[28:31]
ds_read_b128 v[48:51],v2 offset:50176
v_mfma_f32_16x16x32_bf16 v[32:35],v[136:139],v[52:55],v[32:35]
ds_read_b128 v[80:83],v9 offset:2048
v_mfma_f32_16x16x32_bf16 v[36:39],v[140:143],v[52:55],v[36:39]
v_mfma_f32_16x16x32_bf16 v[40:43],v[144:147],v[52:55],v[40:43]
s_waitcnt lgkmcnt(4)
v_mfma_f32_16x16x32_bf16 v[28:31],v[148:151],v[56:59],v[28:31]
ds_read_b128 v[52:55],v2 offset:51200
v_mfma_f32_16x16x32_bf16 v[32:35],v[152:155],v[56:59],v[32:35]
ds_read_b128 v[84:87],v9 offset:3072
v_mfma_f32_16x16x32_bf16 v[36:39],v[156:159],v[56:59],v[36:39]
v_mfma_f32_16x16x32_bf16 v[40:43],v[88:91],v[56:59],v[40:43]
s_waitcnt lgkmcnt(4)
v_mfma_f32_16x16x32_bf16 v[28:31],v[160:163],v[44:47],v[28:31]
ds_read_b128 v[56:59],v2 offset:52224
v_mfma_f32_16x16x32_bf16 v[32:35],v[164:167],v[44:47],v[32:35]
ds_read_b128 v[88:91],v9 offset:4096
v_mfma_f32_16x16x32_bf16 v[36:39],v[168:171],v[44:47],v[36:39]
v_mfma_f32_16x16x32_bf16 v[40:43],v[76:79],v[44:47],v[40:43]
s_waitcnt lgkmcnt(4)
v_mfma_f32_16x16x32_bf16 v[28:31],v[172:175],v[48:51],v[28:31]
ds_read_b128 v[44:47],v2 offset:53248
v_mfma_f32_16x16x32_bf16 v[32:35],v[176:179],v[48:51],v[32:35]
ds_read_b128 v[76:79],v9 offset:5120
v_mfma_f32_16x16x32_bf16 v[36:39],v[180:183],v[48:51],v[36:39]
v_mfma_f32_16x16x32_bf16 v[40:43],v[80:83],v[48:51],v[40:43]
s_waitcnt lgkmcnt(4)
v_mfma_f32_16x16x32_bf16 v[28:31],v[184:187],v[52:55],v[28:31]
ds_read_b128 v[48:51],v2 offset:54272
v_mfma_f32_16x16x32_bf16 v[32:35],v[188:191],v[52:55],v[32:35]
ds_read_b128 v[80:83],v9 offset:6144
v_mfma_f32_16x16x32_bf16 v[36:39],v[192:195],v[52:55],v[36:39]
v_mfma_f32_16x16x32_bf16 v[40:43],v[84:87],v[52:55],v[40:43]
s_waitcnt lgkmcnt(4)
v_mfma_f32_16x16x32_bf16 v[28:31],v[196:199],v[56:59],v[28:31]
ds_read_b128 v[52:55],v2 offset:55296
v_mfma_f32_16x16x32_bf16 v[32:35],v[200:203],v[56:59],v[32:35]
ds_read_b128 v[84:87],v9 offset:7168
v_mfma_f32_16x16x32_bf16 v[36:39],v[204:207],v[56:59],v[36:39]
v_mfma_f32_16x16x32_bf16 v[40:43],v[88:91],v[56:59],v[40:43]
s_waitcnt lgkmcnt(4)
v_mfma_f32_16x16x32_bf16 v[28:31],v[208:211],v[44:47],v[28:31]
ds_read_b128 v[56:59],v2 offset:56320
v_mfma_f32_16x16x32_bf16 v[32:35],v[212:215],v[44:47],v[32:35]
ds_read_b128 v[88:91],v9 offset:8192
v_mfma_f32_16x16x32_bf16 v[36:39],v[216:219],v[44:47],v[36:39]
s_waitcnt vmcnt(3)
v_mfma_f32_16x16x32_bf16 v[40:43],v[76:79],v[44:47],v[40:43]
s_waitcnt lgkmcnt(4)
s_barrier
v_mfma_f32_16x16x32_bf16 v[28:31],v[220:223],v[48:51],v[28:31]
buffer_store_dword v5,v4,s[36:39],s70 offen offset:0 sc0 sc1
ds_read_b128 v[44:47],v2 offset:0
ds_read_b32 v10,v7 offset:5120
v_mfma_f32_16x16x32_bf16 v[32:35],v[224:227],v[48:51],v[32:35]
v_mfma_f32_16x16x32_bf16 v[36:39],v[228:231],v[48:51],v[36:39]
v_mfma_f32_16x16x32_bf16 v[40:43],v[80:83],v[48:51],v[40:43]
s_waitcnt lgkmcnt(4)
v_mfma_f32_16x16x32_bf16 v[28:31],v[232:235],v[52:55],v[28:31]
s_waitcnt lgkmcnt(0)
ds_read_b128 v[48:51],v2 offset:1024
v_mfma_f32_16x16x32_bf16 v[32:35],v[236:239],v[52:55],v[32:35]
v_cmp_eq_u32 vcc,s60,v10
v_mfma_f32_16x16x32_bf16 v[36:39],v[240:243],v[52:55],v[36:39]
s_cmp_eq_u32 vcc_lo,-1
s_cselect_b32 s66,s86,s59
s_mul_i32 s61,s66,7
v_mfma_f32_16x16x32_bf16 v[40:43],v[84:87],v[52:55],v[40:43]
s_lshr_b32 s61,s61,1
s_cmp_lt_u32 s67,s59
v_mfma_f32_16x16x32_bf16 v[28:31],v[244:247],v[56:59],v[28:31]
ds_read_b128 v[52:55],v2 offset:2048
v_mfma_f32_16x16x32_bf16 v[32:35],v[248:251],v[56:59],v[32:35]
v_mfma_f32_16x16x32_bf16 v[36:39],v[252:255],v[56:59],v[36:39]
v_mfma_f32_16x16x32_bf16 v[40:43],v[88:91],v[56:59],v[40:43]
s_cbranch_scc1 Lg4_d0w3_%=
s_branch Lg4_slow0_%=
Lg4_slow0_%=:
s_cmp_lt_u32 s66,s59
s_cbranch_scc1 Lg4_gs0_%=
s_cmp_eq_u32 s54,1
s_cselect_b32 s68,s49,s48
s_mul_i32 s68,s68,5
s_lshr_b32 s68,s68,6
s_add_u32 m0,s78,0
s_nop 0
buffer_load_dword v6,s[36:39],s68 offen offset:0 sc1 lds
s_lshr_b32 s73,s47,0
v_lshlrev_b32 v60,16,v68
v_and_b32 v61,0xffff0000,v68
v_lshlrev_b32 v62,16,v69
v_and_b32 v63,0xffff0000,v69
v_lshlrev_b32 v64,16,v70
v_and_b32 v65,0xffff0000,v70
v_lshlrev_b32 v66,16,v71
v_and_b32 v67,0xffff0000,v71
v_add_f32 v28,v28,v60
v_add_f32 v29,v29,v61
v_add_f32 v30,v30,v62
v_add_f32 v31,v31,v63
v_add_f32 v32,v32,v64
v_add_f32 v33,v33,v65
v_add_f32 v34,v34,v66
v_add_f32 v35,v35,v67
v_cvt_pk_bf16_f32 v28,v28,v29
v_cvt_pk_bf16_f32 v29,v30,v31
v_cvt_pk_bf16_f32 v30,v32,v33
v_cvt_pk_bf16_f32 v31,v34,v35
s_add_u32 m0,s78,1024
s_nop 0
buffer_load_dword v6,s[36:39],s68 offen offset:0 sc1 lds
buffer_store_dwordx4 v[28:31],v3,s[36:39],s73 offen offset:0 sc0 sc1
v_lshlrev_b32 v60,16,v72
v_and_b32 v61,0xffff0000,v72
v_lshlrev_b32 v62,16,v73
v_and_b32 v63,0xffff0000,v73
v_lshlrev_b32 v64,16,v74
v_and_b32 v65,0xffff0000,v74
v_lshlrev_b32 v66,16,v75
v_and_b32 v67,0xffff0000,v75
v_add_f32 v36,v36,v60
v_add_f32 v37,v37,v61
v_add_f32 v38,v38,v62
v_add_f32 v39,v39,v63
v_add_f32 v40,v40,v64
v_add_f32 v41,v41,v65
v_add_f32 v42,v42,v66
v_add_f32 v43,v43,v67
v_cvt_pk_bf16_f32 v36,v36,v37
v_cvt_pk_bf16_f32 v37,v38,v39
v_cvt_pk_bf16_f32 v38,v40,v41
v_cvt_pk_bf16_f32 v39,v42,v43
buffer_store_dwordx4 v[36:39],v3,s[36:39],s73 offen offset:512 sc0 sc1
s_cmp_ge_u32 s48,s59
s_cbranch_scc1 Lg4_done0_%=
s_add_u32 m0,s78,2048
s_nop 0
buffer_load_dword v6,s[36:39],s68 offen offset:0 sc1 lds
s_waitcnt vmcnt(4)
s_barrier)"
R"(
ds_read_b32 v10,v7 offset:0
s_mov_b32 s69,2304
s_waitcnt lgkmcnt(0)
v_cmp_eq_u32 vcc,s60,v10
s_nop 0
s_cmp_eq_u32 vcc_lo,-1
s_cbranch_scc1 Lg4_go0_%=
s_add_u32 m0,s78,3072
s_nop 0
buffer_load_dword v6,s[36:39],s68 offen offset:0 sc1 lds
s_waitcnt vmcnt(4)
s_barrier
ds_read_b32 v10,v7 offset:1024
s_mov_b32 s69,3328
s_waitcnt lgkmcnt(0)
v_cmp_eq_u32 vcc,s60,v10
s_nop 0
s_cmp_eq_u32 vcc_lo,-1
s_cbranch_scc1 Lg4_go0_%=
s_add_u32 m0,s78,0
s_nop 0
buffer_load_dword v6,s[36:39],s68 offen offset:0 sc1 lds
s_waitcnt vmcnt(2)
s_barrier
ds_read_b32 v10,v7 offset:2048
s_lshr_b32 s70,s47,4
buffer_store_dword v5,v4,s[36:39],s70 offen offset:0 sc0 sc1
s_mov_b32 s47,s59
s_mov_b32 s69,256
s_waitcnt lgkmcnt(0)
v_cmp_eq_u32 vcc,s60,v10
s_nop 0
s_cmp_eq_u32 vcc_lo,-1
s_cbranch_scc1 Lg4_go0_%=
s_add_u32 m0,s78,1024
s_nop 0
buffer_load_dword v6,s[36:39],s68 offen offset:0 sc1 lds
s_waitcnt vmcnt(3)
s_barrier
ds_read_b32 v10,v7 offset:3072
s_mov_b32 s69,1280
s_waitcnt lgkmcnt(0)
v_cmp_eq_u32 vcc,s60,v10
s_nop 0
s_cmp_eq_u32 vcc_lo,-1
s_cbranch_scc1 Lg4_go0_%=
Lg4_poll0_%=:
s_add_u32 m0,s78,2048
s_nop 0
buffer_load_dword v6,s[36:39],s68 offen offset:0 sc1 lds
s_waitcnt vmcnt(2)
s_barrier
ds_read_b32 v10,v7 offset:0
s_mov_b32 s69,2304
s_waitcnt lgkmcnt(0)
v_cmp_eq_u32 vcc,s60,v10
s_nop 0
s_cmp_eq_u32 vcc_lo,-1
s_cbranch_scc1 Lg4_go0_%=
s_sleep 4
s_add_u32 m0,s78,3072
s_nop 0
buffer_load_dword v6,s[36:39],s68 offen offset:0 sc1 lds
s_waitcnt vmcnt(2)
s_barrier
ds_read_b32 v10,v7 offset:1024
s_mov_b32 s69,3328
s_waitcnt lgkmcnt(0)
v_cmp_eq_u32 vcc,s60,v10
s_nop 0
s_cmp_eq_u32 vcc_lo,-1
s_cbranch_scc1 Lg4_go0_%=
s_sleep 4
s_add_u32 m0,s78,0
s_nop 0
buffer_load_dword v6,s[36:39],s68 offen offset:0 sc1 lds
s_waitcnt vmcnt(2)
s_barrier
ds_read_b32 v10,v7 offset:2048
s_mov_b32 s69,256
s_waitcnt lgkmcnt(0)
v_cmp_eq_u32 vcc,s60,v10
s_nop 0
s_cmp_eq_u32 vcc_lo,-1
s_cbranch_scc1 Lg4_go0_%=
s_sleep 4
s_add_u32 m0,s78,1024
s_nop 0
buffer_load_dword v6,s[36:39],s68 offen offset:0 sc1 lds
s_waitcnt vmcnt(2)
s_barrier
ds_read_b32 v10,v7 offset:3072
s_mov_b32 s69,1280
s_waitcnt lgkmcnt(0)
v_cmp_eq_u32 vcc,s60,v10
s_nop 0
s_cmp_eq_u32 vcc_lo,-1
s_cbranch_scc1 Lg4_go0_%=
s_sleep 4
s_add_u32 s79,s79,1
s_cmp_lt_u32 s79,20000
s_cbranch_scc1 Lg4_poll0_%=
Lg4_go0_%=:
s_mul_i32 s61,s48,7
s_lshr_b32 s61,s61,1
s_add_u32 m0,s63,0
s_nop 0
buffer_load_dwordx4 v0,s[36:39],s61 offen offset:0 sc1 lds
buffer_load_dwordx4 v0,s[36:39],s61 offen offset:1024 sc1 lds
buffer_load_dwordx4 v0,s[36:39],s61 offen offset:2048 sc1 lds
buffer_load_dwordx4 v0,s[36:39],s61 offen offset:3072 sc1 lds
s_add_u32 m0,s64,0
s_nop 0
buffer_load_dwordx4 v1,s[36:39],s61 offen offset:0 sc1 lds
buffer_load_dwordx4 v1,s[36:39],s61 offen offset:1024 sc1 lds
buffer_load_dwordx4 v1,s[36:39],s61 offen offset:2048 sc1 lds
v_add_u32 v60,s69,v7
s_waitcnt vmcnt(3)
s_barrier
ds_read_b32 v10,v60 offset:0
ds_read_b128 v[44:47],v2 offset:0
ds_read_b128 v[48:51],v2 offset:1024
ds_read_b128 v[52:55],v2 offset:2048
s_lshr_b32 s70,s47,4
buffer_store_dword v5,v4,s[36:39],s70 offen offset:0 sc0 sc1
s_mov_b32 s47,s59
s_waitcnt lgkmcnt(3)
v_cmp_eq_u32 vcc,s60,v10
s_nop 0
s_cmp_eq_u32 vcc_lo,-1
s_cselect_b32 s66,s49,s59
s_mul_i32 s61,s66,7
s_lshr_b32 s61,s61,1
s_waitcnt lgkmcnt(2)
s_setpc_b64 s[90:91]
Lg4_gs0_%=:
s_mul_i32 s61,s48,7
s_lshr_b32 s61,s61,1
s_cmp_eq_u32 s54,1
s_cselect_b32 s68,s49,s48
s_mul_i32 s68,s68,5
s_lshr_b32 s68,s68,6
s_lshr_b32 s73,s47,0
s_add_u32 m0,s63,0
s_nop 0
buffer_load_dwordx4 v0,s[36:39],s61 offen offset:0 sc1 lds
v_lshlrev_b32 v60,16,v68
v_and_b32 v61,0xffff0000,v68
v_lshlrev_b32 v62,16,v69
v_and_b32 v63,0xffff0000,v69
v_lshlrev_b32 v64,16,v70
buffer_load_dwordx4 v0,s[36:39],s61 offen offset:1024 sc1 lds
v_and_b32 v65,0xffff0000,v70
v_lshlrev_b32 v66,16,v71
v_and_b32 v67,0xffff0000,v71
v_add_f32 v28,v28,v60
v_add_f32 v29,v29,v61
buffer_load_dwordx4 v0,s[36:39],s61 offen offset:2048 sc1 lds
v_add_f32 v30,v30,v62
v_add_f32 v31,v31,v63
v_add_f32 v32,v32,v64
v_add_f32 v33,v33,v65
v_add_f32 v34,v34,v66
buffer_load_dwordx4 v0,s[36:39],s61 offen offset:3072 sc1 lds
v_add_f32 v35,v35,v67
v_cvt_pk_bf16_f32 v28,v28,v29
v_cvt_pk_bf16_f32 v29,v30,v31
v_cvt_pk_bf16_f32 v30,v32,v33
v_cvt_pk_bf16_f32 v31,v34,v35
s_add_u32 m0,s78,0
s_nop 0
buffer_load_dword v6,s[36:39],s68 offen offset:0 sc1 lds
buffer_store_dwordx4 v[28:31],v3,s[36:39],s73 offen offset:0 sc0 sc1
v_lshlrev_b32 v60,16,v72
v_and_b32 v61,0xffff0000,v72
v_lshlrev_b32 v62,16,v73
v_and_b32 v63,0xffff0000,v73
s_add_u32 m0,s64,0
s_nop 0
buffer_load_dwordx4 v1,s[36:39],s61 offen offset:0 sc1 lds
v_lshlrev_b32 v64,16,v74
v_and_b32 v65,0xffff0000,v74
v_lshlrev_b32 v66,16,v75
v_and_b32 v67,0xffff0000,v75
v_add_f32 v36,v36,v60
buffer_load_dwordx4 v1,s[36:39],s61 offen offset:1024 sc1 lds
v_add_f32 v37,v37,v61
v_add_f32 v38,v38,v62
v_add_f32 v39,v39,v63
v_add_f32 v40,v40,v64
v_add_f32 v41,v41,v65
buffer_load_dwordx4 v1,s[36:39],s61 offen offset:2048 sc1 lds
v_add_f32 v42,v42,v66
v_add_f32 v43,v43,v67
v_cvt_pk_bf16_f32 v36,v36,v37
v_cvt_pk_bf16_f32 v37,v38,v39
v_cvt_pk_bf16_f32 v38,v40,v41
v_cvt_pk_bf16_f32 v39,v42,v43
buffer_store_dwordx4 v[36:39],v3,s[36:39],s73 offen offset:512 sc0 sc1
s_waitcnt vmcnt(5))"
R"(
s_barrier
ds_read_b32 v10,v7 offset:0
ds_read_b128 v[44:47],v2 offset:0
ds_read_b128 v[48:51],v2 offset:1024
ds_read_b128 v[52:55],v2 offset:2048
s_waitcnt lgkmcnt(3)
v_cmp_eq_u32 vcc,s60,v10
s_nop 0
s_cmp_eq_u32 vcc_hi,-1
s_cselect_b32 s66,s49,s59
s_mul_i32 s61,s66,7
s_lshr_b32 s61,s61,1
s_mov_b32 s65,s59
s_lshr_b32 s70,s59,4
buffer_store_dword v5,v4,s[36:39],s70 offen offset:0 sc0 sc1
s_waitcnt lgkmcnt(2)
s_setpc_b64 s[90:91]
Lg4_done0_%=:
s_waitcnt vmcnt(0)
s_barrier
s_lshr_b32 s70,s47,4
buffer_store_dword v5,v4,s[36:39],s70 offen offset:0 sc0 sc1
s_branch Lg4_done_%=
Lg4_slow1_%=:
s_cmp_lt_u32 s67,s59
s_cbranch_scc1 Lg4_gs1_%=
s_cmp_eq_u32 s54,1
s_cselect_b32 s68,s50,s49
s_mul_i32 s68,s68,5
s_lshr_b32 s68,s68,6
s_add_u32 m0,s78,0
s_nop 0
buffer_load_dword v6,s[36:39],s68 offen offset:0 sc1 lds
s_lshr_b32 s73,s48,0
v_lshlrev_b32 v60,16,v68
v_and_b32 v61,0xffff0000,v68
v_lshlrev_b32 v62,16,v69
v_and_b32 v63,0xffff0000,v69
v_lshlrev_b32 v64,16,v70
v_and_b32 v65,0xffff0000,v70
v_lshlrev_b32 v66,16,v71
v_and_b32 v67,0xffff0000,v71
v_add_f32 v12,v12,v60
v_add_f32 v13,v13,v61
v_add_f32 v14,v14,v62
v_add_f32 v15,v15,v63
v_add_f32 v16,v16,v64
v_add_f32 v17,v17,v65
v_add_f32 v18,v18,v66
v_add_f32 v19,v19,v67
v_cvt_pk_bf16_f32 v12,v12,v13
v_cvt_pk_bf16_f32 v13,v14,v15
v_cvt_pk_bf16_f32 v14,v16,v17
v_cvt_pk_bf16_f32 v15,v18,v19
s_add_u32 m0,s78,1024
s_nop 0
buffer_load_dword v6,s[36:39],s68 offen offset:0 sc1 lds
buffer_store_dwordx4 v[12:15],v3,s[36:39],s73 offen offset:0 sc0 sc1
v_lshlrev_b32 v60,16,v72
v_and_b32 v61,0xffff0000,v72
v_lshlrev_b32 v62,16,v73
v_and_b32 v63,0xffff0000,v73
v_lshlrev_b32 v64,16,v74
v_and_b32 v65,0xffff0000,v74
v_lshlrev_b32 v66,16,v75
v_and_b32 v67,0xffff0000,v75
v_add_f32 v20,v20,v60
v_add_f32 v21,v21,v61
v_add_f32 v22,v22,v62
v_add_f32 v23,v23,v63
v_add_f32 v24,v24,v64
v_add_f32 v25,v25,v65
v_add_f32 v26,v26,v66
v_add_f32 v27,v27,v67
v_cvt_pk_bf16_f32 v20,v20,v21
v_cvt_pk_bf16_f32 v21,v22,v23
v_cvt_pk_bf16_f32 v22,v24,v25
v_cvt_pk_bf16_f32 v23,v26,v27
buffer_store_dwordx4 v[20:23],v3,s[36:39],s73 offen offset:512 sc0 sc1
s_cmp_ge_u32 s49,s59
s_cbranch_scc1 Lg4_done1_%=
s_add_u32 m0,s78,2048
s_nop 0
buffer_load_dword v6,s[36:39],s68 offen offset:0 sc1 lds
s_waitcnt vmcnt(4)
s_barrier
ds_read_b32 v10,v7 offset:0
s_mov_b32 s69,2304
s_waitcnt lgkmcnt(0)
v_cmp_eq_u32 vcc,s60,v10
s_nop 0
s_cmp_eq_u32 vcc_lo,-1
s_cbranch_scc1 Lg4_go1_%=
s_add_u32 m0,s78,3072
s_nop 0
buffer_load_dword v6,s[36:39],s68 offen offset:0 sc1 lds
s_waitcnt vmcnt(4)
s_barrier
ds_read_b32 v10,v7 offset:1024
s_mov_b32 s69,3328
s_waitcnt lgkmcnt(0)
v_cmp_eq_u32 vcc,s60,v10
s_nop 0
s_cmp_eq_u32 vcc_lo,-1
s_cbranch_scc1 Lg4_go1_%=
s_add_u32 m0,s78,0
s_nop 0
buffer_load_dword v6,s[36:39],s68 offen offset:0 sc1 lds
s_waitcnt vmcnt(2)
s_barrier
ds_read_b32 v10,v7 offset:2048
s_lshr_b32 s70,s48,4
buffer_store_dword v5,v4,s[36:39],s70 offen offset:0 sc0 sc1
s_mov_b32 s48,s59
s_mov_b32 s69,256
s_waitcnt lgkmcnt(0)
v_cmp_eq_u32 vcc,s60,v10
s_nop 0
s_cmp_eq_u32 vcc_lo,-1
s_cbranch_scc1 Lg4_go1_%=
s_add_u32 m0,s78,1024
s_nop 0
buffer_load_dword v6,s[36:39],s68 offen offset:0 sc1 lds
s_waitcnt vmcnt(3)
s_barrier
ds_read_b32 v10,v7 offset:3072
s_mov_b32 s69,1280
s_waitcnt lgkmcnt(0)
v_cmp_eq_u32 vcc,s60,v10
s_nop 0
s_cmp_eq_u32 vcc_lo,-1
s_cbranch_scc1 Lg4_go1_%=
Lg4_poll1_%=:
s_add_u32 m0,s78,2048
s_nop 0
buffer_load_dword v6,s[36:39],s68 offen offset:0 sc1 lds
s_waitcnt vmcnt(2)
s_barrier
ds_read_b32 v10,v7 offset:0
s_mov_b32 s69,2304
s_waitcnt lgkmcnt(0)
v_cmp_eq_u32 vcc,s60,v10
s_nop 0
s_cmp_eq_u32 vcc_lo,-1
s_cbranch_scc1 Lg4_go1_%=
s_sleep 4
s_add_u32 m0,s78,3072
s_nop 0
buffer_load_dword v6,s[36:39],s68 offen offset:0 sc1 lds
s_waitcnt vmcnt(2)
s_barrier
ds_read_b32 v10,v7 offset:1024
s_mov_b32 s69,3328
s_waitcnt lgkmcnt(0)
v_cmp_eq_u32 vcc,s60,v10
s_nop 0
s_cmp_eq_u32 vcc_lo,-1
s_cbranch_scc1 Lg4_go1_%=
s_sleep 4
s_add_u32 m0,s78,0
s_nop 0
buffer_load_dword v6,s[36:39],s68 offen offset:0 sc1 lds
s_waitcnt vmcnt(2)
s_barrier
ds_read_b32 v10,v7 offset:2048
s_mov_b32 s69,256
s_waitcnt lgkmcnt(0)
v_cmp_eq_u32 vcc,s60,v10
s_nop 0
s_cmp_eq_u32 vcc_lo,-1
s_cbranch_scc1 Lg4_go1_%=
s_sleep 4
s_add_u32 m0,s78,1024
s_nop 0
buffer_load_dword v6,s[36:39],s68 offen offset:0 sc1 lds
s_waitcnt vmcnt(2)
s_barrier
ds_read_b32 v10,v7 offset:3072
s_mov_b32 s69,1280
s_waitcnt lgkmcnt(0)
v_cmp_eq_u32 vcc,s60,v10
s_nop 0
s_cmp_eq_u32 vcc_lo,-1
s_cbranch_scc1 Lg4_go1_%=
s_sleep 4
s_add_u32 s79,s79,1
s_cmp_lt_u32 s79,20000
s_cbranch_scc1 Lg4_poll1_%=
Lg4_go1_%=:
s_mul_i32 s61,s49,7
s_lshr_b32 s61,s61,1
s_add_u32 m0,s63,28672
s_nop 0
buffer_load_dwordx4 v0,s[36:39],s61 offen offset:0 sc1 lds
buffer_load_dwordx4 v0,s[36:39],s61 offen offset:1024 sc1 lds
buffer_load_dwordx4 v0,s[36:39],s61 offen offset:2048 sc1 lds
buffer_load_dwordx4 v0,s[36:39],s61 offen offset:3072 sc1 lds
s_add_u32 m0,s64,28672
s_nop 0
buffer_load_dwordx4 v1,s[36:39],s61 offen offset:0 sc1 lds)"
R"(
buffer_load_dwordx4 v1,s[36:39],s61 offen offset:1024 sc1 lds
buffer_load_dwordx4 v1,s[36:39],s61 offen offset:2048 sc1 lds
v_add_u32 v60,s69,v7
s_waitcnt vmcnt(3)
s_barrier
ds_read_b32 v10,v60 offset:0
ds_read_b128 v[44:47],v2 offset:28672
ds_read_b128 v[48:51],v2 offset:29696
ds_read_b128 v[52:55],v2 offset:30720
s_lshr_b32 s70,s48,4
buffer_store_dword v5,v4,s[36:39],s70 offen offset:0 sc0 sc1
s_mov_b32 s48,s59
s_waitcnt lgkmcnt(3)
v_cmp_eq_u32 vcc,s60,v10
s_nop 0
s_cmp_eq_u32 vcc_lo,-1
s_cselect_b32 s67,s50,s59
s_mul_i32 s61,s67,7
s_lshr_b32 s61,s61,1
s_waitcnt lgkmcnt(2)
s_setpc_b64 s[92:93]
Lg4_gs1_%=:
s_mul_i32 s61,s49,7
s_lshr_b32 s61,s61,1
s_cmp_eq_u32 s54,1
s_cselect_b32 s68,s50,s49
s_mul_i32 s68,s68,5
s_lshr_b32 s68,s68,6
s_lshr_b32 s73,s48,0
s_add_u32 m0,s63,28672
s_nop 0
buffer_load_dwordx4 v0,s[36:39],s61 offen offset:0 sc1 lds
v_lshlrev_b32 v60,16,v68
v_and_b32 v61,0xffff0000,v68
v_lshlrev_b32 v62,16,v69
v_and_b32 v63,0xffff0000,v69
v_lshlrev_b32 v64,16,v70
buffer_load_dwordx4 v0,s[36:39],s61 offen offset:1024 sc1 lds
v_and_b32 v65,0xffff0000,v70
v_lshlrev_b32 v66,16,v71
v_and_b32 v67,0xffff0000,v71
v_add_f32 v12,v12,v60
v_add_f32 v13,v13,v61
buffer_load_dwordx4 v0,s[36:39],s61 offen offset:2048 sc1 lds
v_add_f32 v14,v14,v62
v_add_f32 v15,v15,v63
v_add_f32 v16,v16,v64
v_add_f32 v17,v17,v65
v_add_f32 v18,v18,v66
buffer_load_dwordx4 v0,s[36:39],s61 offen offset:3072 sc1 lds
v_add_f32 v19,v19,v67
v_cvt_pk_bf16_f32 v12,v12,v13
v_cvt_pk_bf16_f32 v13,v14,v15
v_cvt_pk_bf16_f32 v14,v16,v17
v_cvt_pk_bf16_f32 v15,v18,v19
s_add_u32 m0,s78,0
s_nop 0
buffer_load_dword v6,s[36:39],s68 offen offset:0 sc1 lds
buffer_store_dwordx4 v[12:15],v3,s[36:39],s73 offen offset:0 sc0 sc1
v_lshlrev_b32 v60,16,v72
v_and_b32 v61,0xffff0000,v72
v_lshlrev_b32 v62,16,v73
v_and_b32 v63,0xffff0000,v73
s_add_u32 m0,s64,28672
s_nop 0
buffer_load_dwordx4 v1,s[36:39],s61 offen offset:0 sc1 lds
v_lshlrev_b32 v64,16,v74
v_and_b32 v65,0xffff0000,v74
v_lshlrev_b32 v66,16,v75
v_and_b32 v67,0xffff0000,v75
v_add_f32 v20,v20,v60
buffer_load_dwordx4 v1,s[36:39],s61 offen offset:1024 sc1 lds
v_add_f32 v21,v21,v61
v_add_f32 v22,v22,v62
v_add_f32 v23,v23,v63
v_add_f32 v24,v24,v64
v_add_f32 v25,v25,v65
buffer_load_dwordx4 v1,s[36:39],s61 offen offset:2048 sc1 lds
v_add_f32 v26,v26,v66
v_add_f32 v27,v27,v67
v_cvt_pk_bf16_f32 v20,v20,v21
v_cvt_pk_bf16_f32 v21,v22,v23
v_cvt_pk_bf16_f32 v22,v24,v25
v_cvt_pk_bf16_f32 v23,v26,v27
buffer_store_dwordx4 v[20:23],v3,s[36:39],s73 offen offset:512 sc0 sc1
s_waitcnt vmcnt(5)
s_barrier
ds_read_b32 v10,v7 offset:0
ds_read_b128 v[44:47],v2 offset:28672
ds_read_b128 v[48:51],v2 offset:29696
ds_read_b128 v[52:55],v2 offset:30720
s_waitcnt lgkmcnt(3)
v_cmp_eq_u32 vcc,s60,v10
s_nop 0
s_cmp_eq_u32 vcc_hi,-1
s_cselect_b32 s67,s50,s59
s_mul_i32 s61,s67,7
s_lshr_b32 s61,s61,1
s_mov_b32 s65,s59
s_lshr_b32 s70,s59,4
buffer_store_dword v5,v4,s[36:39],s70 offen offset:0 sc0 sc1
s_waitcnt lgkmcnt(2)
s_setpc_b64 s[92:93]
Lg4_done1_%=:
s_waitcnt vmcnt(0)
s_barrier
s_lshr_b32 s70,s48,4
buffer_store_dword v5,v4,s[36:39],s70 offen offset:0 sc0 sc1
s_branch Lg4_done_%=
Lg4_done_%=:
s_waitcnt vmcnt(0) lgkmcnt(0)
s_barrier
s_endpgm
Lcode_g4_e:)"
  :: "s"(ws), "s"(wave), "s"(sStep), "s"(sX1), "s"(sX2), "s"(sU0), "s"(sWw), "s"(sBuf), "v"(vSrc), "v"(vSrc2), "v"(vA), "v"(vOut), "v"(vFlag), "v"(vPollOff), "v"(vSnapRd), "v"(vAuxS), "v"(vWd0), "v"(vWd1), "v"(vWr0), "v"(vWr1), "v"(vWr2), "s"(w4) : "v0", "v1", "v2", "v3", "v4", "v5", "v6", "v7", "v8", "v9", "v10", "v11", "v12", "v13", "v14", "v15", "v16", "v17", "v18", "v19", "v20", "v21", "v22", "v23", "v24", "v25", "v26", "v27", "v28", "v29", "v30", "v31", "v32", "v33", "v34", "v35", "v36", "v37", "v38", "v39", "v40", "v41", "v42", "v43", "v44", "v45", "v46", "v47", "v48", "v49", "v50", "v51", "v52", "v53", "v54", "v55", "v56", "v57", "v58", "v59", "v60", "v61", "v62", "v63", "v64", "v65", "v66", "v67", "v68", "v69", "v70", "v71", "v72", "v73", "v74", "v75", "v76", "v77", "v78", "v79", "v80", "v81", "v82", "v83", "v84", "v85", "v86", "v87", "v88", "v89", "v90", "v91", "v92", "v93", "v94", "v95", "v96", "v97", "v98", "v99", "v100", "v101", "v102", "v103", "v104", "v105", "v106", "v107", "v108", "v109", "v110", "v111", "v112", "v113", "v114", "v115", "v116", "v117", "v118", "v119", "v120", "v121", "v122", "v123", "v124", "v125", "v126", "v127", "v128", "v129", "v130", "v131", "v132", "v133", "v134", "v135", "v136", "v137", "v138", "v139", "v140", "v141", "v142", "v143", "v144", "v145", "v146", "v147", "v148", "v149", "v150", "v151", "v152", "v153", "v154", "v155", "v156", "v157", "v158", "v159", "v160", "v161", "v162", "v163", "v164", "v165", "v166", "v167", "v168", "v169", "v170", "v171", "v172", "v173", "v174", "v175", "v176", "v177", "v178", "v179", "v180", "v181", "v182", "v183", "v184", "v185", "v186", "v187", "v188", "v189", "v190", "v191", "v192", "v193", "v194", "v195", "v196", "v197", "v198", "v199", "v200", "v201", "v202", "v203", "v204", "v205", "v206", "v207", "v208", "v209", "v210", "v211", "v212", "v213", "v214", "v215", "v216", "v217", "v218", "v219", "v220", "v221", "v222", "v223", "v224", "v225", "v226", "v227", "v228", "v229", "v230", "v231", "v255", "a0", "a1", "a2", "a3", "a4", "a5", "a6", "a7", "a8", "a9", "a10", "a11", "a12", "a13", "a14", "a15", "a16", "a17", "a18", "a19", "a20", "a21", "a22", "a23", "a24", "a25", "a26", "a27", "a28", "a29", "a30", "a31", "a32", "a33", "a34", "a35", "a36", "a37", "a38", "a39", "a40", "a41", "a42", "a43", "a44", "a45", "a46", "a47", "a48", "a49", "a50", "a51", "a52", "a53", "a54", "a55", "a56", "a57", "a58", "a59", "a60", "a61", "a62", "a63", "a64", "a65", "a66", "a67", "a68", "a69", "a70", "a71", "a72", "a73", "a74", "a75", "a76", "a77", "a78", "a79", "a80", "a81", "a82", "a83", "a84", "a85", "a86", "a87", "a88", "a89", "a90", "a91", "a92", "a93", "a94", "a95", "a96", "a97", "a98", "a99", "a100", "a101", "a102", "a103", "a104", "a105", "a106", "a107", "a108", "a109", "a110", "a111", "a112", "a113", "a114", "a115", "a116", "a117", "a118", "a119", "a120", "a121", "a122", "a123", "a124", "a125", "a126", "a127", "a128", "a129", "a130", "a131", "a132", "a133", "a134", "a135", "a136", "a137", "a138", "a139", "a140", "a141", "a142", "a143", "a144", "a145", "a146", "a147", "a148", "a149", "a150", "a151", "a152", "a153", "a154", "a155", "a156", "a157", "a158", "a159", "a160", "a161", "a162", "a163", "a164", "a165", "a166", "a167", "a168", "a169", "a170", "a171", "a172", "a173", "a174", "a175", "a176", "a177", "a178", "a179", "a180", "a181", "a182", "a183", "a184", "a185", "a186", "a187", "a188", "a189", "a190", "a191", "a192", "a193", "a194", "a195", "a196", "a197", "a198", "a199", "a200", "a201", "a202", "a203", "a204", "a205", "a206", "a207", "a208", "a209", "a210", "a211", "a212", "a213", "a214", "a215", "a216", "a217", "a218", "a219", "a220", "a221", "a222", "a223", "a224", "a225", "a226", "a227", "a228", "a229", "a230", "a231", "a232", "a233", "a234", "a235", "a236", "a237", "a238", "a239", "a240", "a241", "a242", "a243", "a244", "a245", "a246", "a247", "a248", "a249", "a250", "a251", "a252", "a253", "a254", "a255", "s36", "s37", "s38", "s39", "s40", "s41", "s42", "s43", "s44", "s45", "s46", "s47", "s48", "s49", "s50", "s51", "s52", "s53", "s54", "s55", "s56", "s57", "s58", "s59", "s60", "s61", "s62", "s63", "s64", "s65", "s66", "s67", "s68", "s69", "s70", "s71", "s72", "s73", "s74", "s75", "s76", "s77", "s78", "s79", "s80", "s81", "s82", "s83", "s84", "s85", "s86", "s87", "s88", "s89", "s90", "s91", "s92", "s93", "s94", "s95", "s96", "s97", "s98", "s99", "vcc", "scc", "memory");
    __builtin_unreachable();
  } else {
   if (kind != 6) {
    COMMON(kind == 1 ? 8 : 16, kind == 1 ? OFF_FXP : OFF_FH, (kind == 1 ? OFF_FH : OFF_FX1) + slice * 128, AUX + 1536, 2048, 2048, 4);
    const int dl = kind == 2 ? ((wave & 2) ? -1 : 1) : 0, r0 = 16 + l15 - dl, r1 = 16 + l15 - 2 * dl;
    const unsigned sb = (kind == 1 ? OFF_XP : OFF_HS) - 32768 + wave * 4096 + lg * 256;
    const unsigned vSrc = sb + (r0 >> 4) * 32768 + (r0 & 15) * 16, vSrc2 = sb + 16384 + (r1 >> 4) * 32768 + (r1 & 15) * 16;
    const unsigned vOut = (kind == 1 ? OFF_HS : OFF_X1) + (slice * 4 + wave) * 1024 + vA;
    const unsigned vX = AUX + wave * 2048 + (kind == 1 ? l15 * 16 : vA), vX2 = AUX + wave * 2048 + l15 * 8;
    const unsigned vAuxS = kind == 1 ? OFF_S1 + ln * 4 : OFF_XP + (slice * 4 + wave) * 1024 + vA;
    const unsigned vAuxS2 = ln < 32 ? OFF_S1 - 1024 + 256 + ln * 4 : OOB;
    constexpr unsigned RED = AUX + 24576 + 1536;
    const unsigned vOut2 = wave != 2 ? OOB : kind == 1 ? (slice == 0 && ln < 16 ? OFF_S1 + 256 + ln * 8 : OOB) : ln < 32 ? OFF_ST + slice * 128 + ln * 4 : OOB;
    const unsigned vStW = RED + wave * 4 + (kind == 1 ? l15 * 32 + (lg == lq ? 16 : 0) : (l15 * 2 + (lg & 1)) * 16), vStR = RED + (kind == 1 ? l15 * 32 : (ln & 31) * 16);
    const unsigned sWw = 16 * wave * 2048 + slice * 256;
    const u16* w12 = kind == 1 ? w1 : w2;
    unsigned epOut;
    asm volatile(R"(
Lcode_g12_b:
s_cmp_eq_u32 %8,2
s_cbranch_scc1 Lg2_begin_%=
s_mov_b64 s[36:37],%1
s_mov_b32 s38,122727552
s_mov_b32 s39,0x00020000
s_mov_b32 s54,%2
s_mov_b32 s55,%3
s_mov_b32 s56,%4
s_mov_b32 s58,%5
s_mov_b32 s83,%6
s_mov_b32 s82,%7
s_mov_b32 s84,%8
v_mov_b32 v0,%9
v_mov_b32 v1,%10
v_mov_b32 v2,%11
v_mov_b32 v3,%12
v_mov_b32 v4,%13
v_mov_b32 v6,%14
v_mov_b32 v7,%15
v_mov_b32 v8,%16
v_mov_b32 v9,%17
v_mov_b32 v10,%18
v_mov_b32 v11,%19
v_mov_b32 v12,%20
v_mov_b32 v13,%21
v_mov_b32 v14,%22
v_mov_b32 v28,%23
v_mov_b32 v29,%24
v_mov_b32 v30,%25
v_mov_b32 v31,%26
v_mov_b32 v32,%27
s_mov_b64 s[40:41],%28
s_mov_b32 s42,2097152
s_mov_b32 s43,0x00020000
s_lshl_b32 s88,s54,11
s_mov_b32 s69,122727424
buffer_load_dword v5,off,s[36:39],s69 sc1
s_add_u32 m0,s88,0
s_add_u32 s61,s82,0
buffer_load_dwordx4 v28,s[40:43],s61 offen offset:0  lds
s_add_u32 m0,s88,1024
s_nop 0
buffer_load_dwordx4 v29,s[40:43],s61 offen offset:0  lds
s_add_u32 m0,s88,8064
s_nop 0
buffer_load_dwordx4 v28,s[40:43],s61 offen offset:128  lds
s_add_u32 m0,s88,9088
s_nop 0
buffer_load_dwordx4 v29,s[40:43],s61 offen offset:128  lds
s_add_u32 m0,s88,16384
s_add_u32 s61,s82,131072
buffer_load_dwordx4 v28,s[40:43],s61 offen offset:0  lds
s_add_u32 m0,s88,17408
s_nop 0
buffer_load_dwordx4 v29,s[40:43],s61 offen offset:0  lds
s_add_u32 m0,s88,24448
s_nop 0
buffer_load_dwordx4 v28,s[40:43],s61 offen offset:128  lds
s_add_u32 m0,s88,25472
s_nop 0
buffer_load_dwordx4 v29,s[40:43],s61 offen offset:128  lds
s_add_u32 m0,s88,32768
s_add_u32 s61,s82,262144
buffer_load_dwordx4 v28,s[40:43],s61 offen offset:0  lds
s_add_u32 m0,s88,33792
s_nop 0
buffer_load_dwordx4 v29,s[40:43],s61 offen offset:0  lds
s_add_u32 m0,s88,40832
s_nop 0
buffer_load_dwordx4 v28,s[40:43],s61 offen offset:128  lds
s_add_u32 m0,s88,41856
s_nop 0
buffer_load_dwordx4 v29,s[40:43],s61 offen offset:128  lds
s_add_u32 m0,s88,49152
s_add_u32 s61,s82,393216
buffer_load_dwordx4 v28,s[40:43],s61 offen offset:0  lds
s_add_u32 m0,s88,50176
s_nop 0
buffer_load_dwordx4 v29,s[40:43],s61 offen offset:0  lds
s_add_u32 m0,s88,57216
s_nop 0
buffer_load_dwordx4 v28,s[40:43],s61 offen offset:128  lds
s_add_u32 m0,s88,58240
s_nop 0
buffer_load_dwordx4 v29,s[40:43],s61 offen offset:128  lds
s_add_u32 m0,s88,65536
s_add_u32 s61,s82,524288
buffer_load_dwordx4 v28,s[40:43],s61 offen offset:0  lds
s_add_u32 m0,s88,66560
s_nop 0
buffer_load_dwordx4 v29,s[40:43],s61 offen offset:0  lds
s_add_u32 m0,s88,73600
s_nop 0
buffer_load_dwordx4 v28,s[40:43],s61 offen offset:128  lds
s_add_u32 m0,s88,74624
s_nop 0
buffer_load_dwordx4 v29,s[40:43],s61 offen offset:128  lds
s_add_u32 m0,s88,81920
s_add_u32 s61,s82,655360
buffer_load_dwordx4 v28,s[40:43],s61 offen offset:0  lds
s_add_u32 m0,s88,82944
s_nop 0
buffer_load_dwordx4 v29,s[40:43],s61 offen offset:0  lds
s_add_u32 m0,s88,89984
s_nop 0
buffer_load_dwordx4 v28,s[40:43],s61 offen offset:128  lds
s_add_u32 m0,s88,91008
s_nop 0
buffer_load_dwordx4 v29,s[40:43],s61 offen offset:128  lds
s_add_u32 m0,s88,98304
s_add_u32 s61,s82,786432
buffer_load_dwordx4 v28,s[40:43],s61 offen offset:0  lds
s_add_u32 m0,s88,99328
s_nop 0
buffer_load_dwordx4 v29,s[40:43],s61 offen offset:0  lds
s_add_u32 m0,s88,106368
s_nop 0
buffer_load_dwordx4 v28,s[40:43],s61 offen offset:128  lds
s_add_u32 m0,s88,107392
s_nop 0
buffer_load_dwordx4 v29,s[40:43],s61 offen offset:128  lds
v_add_u32 v36,49152,v30
v_add_u32 v37,49152,v31
v_add_u32 v38,98304,v30
v_add_u32 v39,98304,v31
v_mov_b32 v76,0x3f803f80
v_mov_b32 v77,0x3f803f80
v_mov_b32 v78,0x3f803f80
v_mov_b32 v79,0x3f803f80
s_waitcnt vmcnt(24)
s_barrier
s_add_u32 m0,s88,114688
s_add_u32 s61,s82,917504
buffer_load_dwordx4 v28,s[40:43],s61 offen offset:0  lds
s_add_u32 m0,s88,115712
ds_read_b64_tr_b16 a[0:1],v30 offset:0
buffer_load_dwordx4 v29,s[40:43],s61 offen offset:0  lds
s_add_u32 m0,s88,122752
ds_read_b64_tr_b16 a[2:3],v30 offset:512
buffer_load_dwordx4 v28,s[40:43],s61 offen offset:128  lds
s_add_u32 m0,s88,123776
ds_read_b64_tr_b16 a[4:5],v31 offset:0
buffer_load_dwordx4 v29,s[40:43],s61 offen offset:128  lds
ds_read_b64_tr_b16 a[6:7],v31 offset:512
ds_read_b64_tr_b16 a[8:9],v30 offset:4096
ds_read_b64_tr_b16 a[10:11],v30 offset:4608
ds_read_b64_tr_b16 a[12:13],v31 offset:4096
ds_read_b64_tr_b16 a[14:15],v31 offset:4608
s_waitcnt vmcnt(24)
s_barrier
s_add_u32 m0,s88,131072
s_add_u32 s61,s82,1048576
buffer_load_dwordx4 v28,s[40:43],s61 offen offset:0  lds
s_add_u32 m0,s88,132096
ds_read_b64_tr_b16 a[16:17],v30 offset:16384
buffer_load_dwordx4 v29,s[40:43],s61 offen offset:0  lds
s_add_u32 m0,s88,139136
ds_read_b64_tr_b16 a[18:19],v30 offset:16896
buffer_load_dwordx4 v28,s[40:43],s61 offen offset:128  lds
s_add_u32 m0,s88,140160
ds_read_b64_tr_b16 a[20:21],v31 offset:16384
buffer_load_dwordx4 v29,s[40:43],s61 offen offset:128  lds
ds_read_b64_tr_b16 a[22:23],v31 offset:16896
ds_read_b64_tr_b16 a[24:25],v30 offset:20480
ds_read_b64_tr_b16 a[26:27],v30 offset:20992
ds_read_b64_tr_b16 a[28:29],v31 offset:20480
ds_read_b64_tr_b16 a[30:31],v31 offset:20992
s_waitcnt lgkmcnt(8)
v_mfma_f32_16x16x32_bf16 v[20:23],a[0:3],v[76:79],0
v_mfma_f32_16x16x32_bf16 v[24:27],a[4:7],v[76:79],0
v_mfma_f32_16x16x32_bf16 v[20:23],a[8:11],v[76:79],v[20:23]
v_mfma_f32_16x16x32_bf16 v[24:27],a[12:15],v[76:79],v[24:27]
s_waitcnt vmcnt(24)
s_barrier
s_add_u32 m0,s88,0
s_add_u32 s61,s82,1179648
buffer_load_dwordx4 v28,s[40:43],s61 offen offset:0  lds
s_add_u32 m0,s88,1024
ds_read_b64_tr_b16 a[32:33],v30 offset:32768
buffer_load_dwordx4 v29,s[40:43],s61 offen offset:0  lds
s_add_u32 m0,s88,8064
ds_read_b64_tr_b16 a[34:35],v30 offset:33280
buffer_load_dwordx4 v28,s[40:43],s61 offen offset:128  lds
s_add_u32 m0,s88,9088
ds_read_b64_tr_b16 a[36:37],v31 offset:32768
buffer_load_dwordx4 v29,s[40:43],s61 offen offset:128  lds
ds_read_b64_tr_b16 a[38:39],v31 offset:33280
ds_read_b64_tr_b16 a[40:41],v30 offset:36864
ds_read_b64_tr_b16 a[42:43],v30 offset:37376
ds_read_b64_tr_b16 a[44:45],v31 offset:36864
ds_read_b64_tr_b16 a[46:47],v31 offset:37376
s_waitcnt lgkmcnt(8)
v_mfma_f32_16x16x32_bf16 v[20:23],a[16:19],v[76:79],v[20:23]
v_mfma_f32_16x16x32_bf16 v[24:27],a[20:23],v[76:79],v[24:27]
v_mfma_f32_16x16x32_bf16 v[20:23],a[24:27],v[76:79],v[20:23]
v_mfma_f32_16x16x32_bf16 v[24:27],a[28:31],v[76:79],v[24:27]
s_waitcnt vmcnt(24)
s_barrier
s_add_u32 m0,s88,16384)"
R"(
s_add_u32 s61,s82,1310720
buffer_load_dwordx4 v28,s[40:43],s61 offen offset:0  lds
s_add_u32 m0,s88,17408
ds_read_b64_tr_b16 a[48:49],v36 offset:0
buffer_load_dwordx4 v29,s[40:43],s61 offen offset:0  lds
s_add_u32 m0,s88,24448
ds_read_b64_tr_b16 a[50:51],v36 offset:512
buffer_load_dwordx4 v28,s[40:43],s61 offen offset:128  lds
s_add_u32 m0,s88,25472
ds_read_b64_tr_b16 a[52:53],v37 offset:0
buffer_load_dwordx4 v29,s[40:43],s61 offen offset:128  lds
ds_read_b64_tr_b16 a[54:55],v37 offset:512
ds_read_b64_tr_b16 a[56:57],v36 offset:4096
ds_read_b64_tr_b16 a[58:59],v36 offset:4608
ds_read_b64_tr_b16 a[60:61],v37 offset:4096
ds_read_b64_tr_b16 a[62:63],v37 offset:4608
s_waitcnt lgkmcnt(8)
v_mfma_f32_16x16x32_bf16 v[20:23],a[32:35],v[76:79],v[20:23]
v_mfma_f32_16x16x32_bf16 v[24:27],a[36:39],v[76:79],v[24:27]
v_mfma_f32_16x16x32_bf16 v[20:23],a[40:43],v[76:79],v[20:23]
v_mfma_f32_16x16x32_bf16 v[24:27],a[44:47],v[76:79],v[24:27]
s_waitcnt vmcnt(24)
s_barrier
s_add_u32 m0,s88,32768
s_add_u32 s61,s82,1441792
buffer_load_dwordx4 v28,s[40:43],s61 offen offset:0  lds
s_add_u32 m0,s88,33792
ds_read_b64_tr_b16 a[64:65],v36 offset:16384
buffer_load_dwordx4 v29,s[40:43],s61 offen offset:0  lds
s_add_u32 m0,s88,40832
ds_read_b64_tr_b16 a[66:67],v36 offset:16896
buffer_load_dwordx4 v28,s[40:43],s61 offen offset:128  lds
s_add_u32 m0,s88,41856
ds_read_b64_tr_b16 a[68:69],v37 offset:16384
buffer_load_dwordx4 v29,s[40:43],s61 offen offset:128  lds
ds_read_b64_tr_b16 a[70:71],v37 offset:16896
ds_read_b64_tr_b16 a[72:73],v36 offset:20480
ds_read_b64_tr_b16 a[74:75],v36 offset:20992
ds_read_b64_tr_b16 a[76:77],v37 offset:20480
ds_read_b64_tr_b16 a[78:79],v37 offset:20992
s_waitcnt lgkmcnt(8)
v_mfma_f32_16x16x32_bf16 v[20:23],a[48:51],v[76:79],v[20:23]
v_mfma_f32_16x16x32_bf16 v[24:27],a[52:55],v[76:79],v[24:27]
v_mfma_f32_16x16x32_bf16 v[20:23],a[56:59],v[76:79],v[20:23]
v_mfma_f32_16x16x32_bf16 v[24:27],a[60:63],v[76:79],v[24:27]
s_waitcnt vmcnt(24)
s_barrier
s_add_u32 m0,s88,49152
s_add_u32 s61,s82,1572864
buffer_load_dwordx4 v28,s[40:43],s61 offen offset:0  lds
s_add_u32 m0,s88,50176
ds_read_b64_tr_b16 a[80:81],v36 offset:32768
buffer_load_dwordx4 v29,s[40:43],s61 offen offset:0  lds
s_add_u32 m0,s88,57216
ds_read_b64_tr_b16 a[82:83],v36 offset:33280
buffer_load_dwordx4 v28,s[40:43],s61 offen offset:128  lds
s_add_u32 m0,s88,58240
ds_read_b64_tr_b16 a[84:85],v37 offset:32768
buffer_load_dwordx4 v29,s[40:43],s61 offen offset:128  lds
ds_read_b64_tr_b16 a[86:87],v37 offset:33280
ds_read_b64_tr_b16 a[88:89],v36 offset:36864
ds_read_b64_tr_b16 a[90:91],v36 offset:37376
ds_read_b64_tr_b16 a[92:93],v37 offset:36864
ds_read_b64_tr_b16 a[94:95],v37 offset:37376
s_waitcnt lgkmcnt(8)
v_mfma_f32_16x16x32_bf16 v[20:23],a[64:67],v[76:79],v[20:23]
v_mfma_f32_16x16x32_bf16 v[24:27],a[68:71],v[76:79],v[24:27]
v_mfma_f32_16x16x32_bf16 v[20:23],a[72:75],v[76:79],v[20:23]
v_mfma_f32_16x16x32_bf16 v[24:27],a[76:79],v[76:79],v[24:27]
s_waitcnt vmcnt(24)
s_barrier
s_add_u32 m0,s88,65536
s_add_u32 s61,s82,1703936
buffer_load_dwordx4 v28,s[40:43],s61 offen offset:0  lds
s_add_u32 m0,s88,66560
ds_read_b64_tr_b16 a[96:97],v38 offset:0
buffer_load_dwordx4 v29,s[40:43],s61 offen offset:0  lds
s_add_u32 m0,s88,73600
ds_read_b64_tr_b16 a[98:99],v38 offset:512
buffer_load_dwordx4 v28,s[40:43],s61 offen offset:128  lds
s_add_u32 m0,s88,74624
ds_read_b64_tr_b16 a[100:101],v39 offset:0
buffer_load_dwordx4 v29,s[40:43],s61 offen offset:128  lds
ds_read_b64_tr_b16 a[102:103],v39 offset:512
ds_read_b64_tr_b16 a[104:105],v38 offset:4096
ds_read_b64_tr_b16 a[106:107],v38 offset:4608
ds_read_b64_tr_b16 a[108:109],v39 offset:4096
ds_read_b64_tr_b16 a[110:111],v39 offset:4608
s_waitcnt lgkmcnt(8)
v_mfma_f32_16x16x32_bf16 v[20:23],a[80:83],v[76:79],v[20:23]
v_mfma_f32_16x16x32_bf16 v[24:27],a[84:87],v[76:79],v[24:27]
v_mfma_f32_16x16x32_bf16 v[20:23],a[88:91],v[76:79],v[20:23]
v_mfma_f32_16x16x32_bf16 v[24:27],a[92:95],v[76:79],v[24:27]
s_waitcnt vmcnt(24)
s_barrier
s_add_u32 m0,s88,81920
s_add_u32 s61,s82,1835008
buffer_load_dwordx4 v28,s[40:43],s61 offen offset:0  lds
s_add_u32 m0,s88,82944
ds_read_b64_tr_b16 a[112:113],v38 offset:16384
buffer_load_dwordx4 v29,s[40:43],s61 offen offset:0  lds
s_add_u32 m0,s88,89984
ds_read_b64_tr_b16 a[114:115],v38 offset:16896
buffer_load_dwordx4 v28,s[40:43],s61 offen offset:128  lds
s_add_u32 m0,s88,91008
ds_read_b64_tr_b16 a[116:117],v39 offset:16384
buffer_load_dwordx4 v29,s[40:43],s61 offen offset:128  lds
ds_read_b64_tr_b16 a[118:119],v39 offset:16896
ds_read_b64_tr_b16 a[120:121],v38 offset:20480
ds_read_b64_tr_b16 a[122:123],v38 offset:20992
ds_read_b64_tr_b16 a[124:125],v39 offset:20480
ds_read_b64_tr_b16 a[126:127],v39 offset:20992
s_waitcnt lgkmcnt(8)
v_mfma_f32_16x16x32_bf16 v[20:23],a[96:99],v[76:79],v[20:23]
v_mfma_f32_16x16x32_bf16 v[24:27],a[100:103],v[76:79],v[24:27]
v_mfma_f32_16x16x32_bf16 v[20:23],a[104:107],v[76:79],v[20:23]
v_mfma_f32_16x16x32_bf16 v[24:27],a[108:111],v[76:79],v[24:27]
s_waitcnt vmcnt(24)
s_barrier
s_add_u32 m0,s88,98304
s_add_u32 s61,s82,1966080
buffer_load_dwordx4 v28,s[40:43],s61 offen offset:0  lds
s_add_u32 m0,s88,99328
ds_read_b64_tr_b16 a[128:129],v38 offset:32768
buffer_load_dwordx4 v29,s[40:43],s61 offen offset:0  lds
s_add_u32 m0,s88,106368
ds_read_b64_tr_b16 a[130:131],v38 offset:33280
buffer_load_dwordx4 v28,s[40:43],s61 offen offset:128  lds
s_add_u32 m0,s88,107392
ds_read_b64_tr_b16 a[132:133],v39 offset:32768
buffer_load_dwordx4 v29,s[40:43],s61 offen offset:128  lds
ds_read_b64_tr_b16 a[134:135],v39 offset:33280
ds_read_b64_tr_b16 a[136:137],v38 offset:36864
ds_read_b64_tr_b16 a[138:139],v38 offset:37376
ds_read_b64_tr_b16 a[140:141],v39 offset:36864
ds_read_b64_tr_b16 a[142:143],v39 offset:37376
s_waitcnt lgkmcnt(8)
v_mfma_f32_16x16x32_bf16 v[20:23],a[112:115],v[76:79],v[20:23]
v_mfma_f32_16x16x32_bf16 v[24:27],a[116:119],v[76:79],v[24:27]
v_mfma_f32_16x16x32_bf16 v[20:23],a[120:123],v[76:79],v[20:23]
v_mfma_f32_16x16x32_bf16 v[24:27],a[124:127],v[76:79],v[24:27]
s_waitcnt vmcnt(24)
s_barrier
ds_read_b64_tr_b16 a[144:145],v30 offset:0
ds_read_b64_tr_b16 a[146:147],v30 offset:512
ds_read_b64_tr_b16 a[148:149],v31 offset:0
ds_read_b64_tr_b16 a[150:151],v31 offset:512
ds_read_b64_tr_b16 a[152:153],v30 offset:4096
ds_read_b64_tr_b16 a[154:155],v30 offset:4608
ds_read_b64_tr_b16 a[156:157],v31 offset:4096
ds_read_b64_tr_b16 a[158:159],v31 offset:4608
s_waitcnt lgkmcnt(8)
v_mfma_f32_16x16x32_bf16 v[20:23],a[128:131],v[76:79],v[20:23]
v_mfma_f32_16x16x32_bf16 v[24:27],a[132:135],v[76:79],v[24:27]
v_mfma_f32_16x16x32_bf16 v[20:23],a[136:139],v[76:79],v[20:23]
v_mfma_f32_16x16x32_bf16 v[24:27],a[140:143],v[76:79],v[24:27]
s_waitcnt vmcnt(20)
s_barrier
ds_read_b64_tr_b16 a[160:161],v30 offset:16384
ds_read_b64_tr_b16 a[162:163],v30 offset:16896
ds_read_b64_tr_b16 a[164:165],v31 offset:16384
ds_read_b64_tr_b16 a[166:167],v31 offset:16896
ds_read_b64_tr_b16 a[168:169],v30 offset:20480
ds_read_b64_tr_b16 a[170:171],v30 offset:20992
ds_read_b64_tr_b16 a[172:173],v31 offset:20480
ds_read_b64_tr_b16 a[174:175],v31 offset:20992
s_waitcnt lgkmcnt(8)
v_mfma_f32_16x16x32_bf16 v[20:23],a[144:147],v[76:79],v[20:23]
v_mfma_f32_16x16x32_bf16 v[24:27],a[148:151],v[76:79],v[24:27]
v_mfma_f32_16x16x32_bf16 v[20:23],a[152:155],v[76:79],v[20:23]
v_mfma_f32_16x16x32_bf16 v[24:27],a[156:159],v[76:79],v[24:27]
s_waitcnt vmcnt(16)
s_barrier
ds_read_b64_tr_b16 a[176:177],v30 offset:32768
ds_read_b64_tr_b16 a[178:179],v30 offset:33280
ds_read_b64_tr_b16 a[180:181],v31 offset:32768
ds_read_b64_tr_b16 a[182:183],v31 offset:33280
ds_read_b64_tr_b16 a[184:185],v30 offset:36864
ds_read_b64_tr_b16 a[186:187],v30 offset:37376
ds_read_b64_tr_b16 a[188:189],v31 offset:36864
ds_read_b64_tr_b16 a[190:191],v31 offset:37376
s_getpc_b64 s[46:47]
Lg1_cp_%=:
s_add_u32 s46,s46,Lg1_init_%=-Lg1_cp_%=
s_addc_u32 s47,s47,0
s_lshl_b32 s68,s54,13
v_lshl_add_u32 v60,v2,3,s68
v_min_u32 v61,Lg1_done_%=-Lg1_init_%=,v60
global_load_dword v64,v61,s[46:47]
v_add_u32 v60,32768,v60
v_min_u32 v62,Lg1_done_%=-Lg1_init_%=,v60
global_load_dword v65,v62,s[46:47]
s_waitcnt lgkmcnt(8)
v_mfma_f32_16x16x32_bf16 v[20:23],a[160:163],v[76:79],v[20:23]
v_mfma_f32_16x16x32_bf16 v[24:27],a[164:167],v[76:79],v[24:27]
v_mfma_f32_16x16x32_bf16 v[20:23],a[168:171],v[76:79],v[20:23]
v_mfma_f32_16x16x32_bf16 v[24:27],a[172:175],v[76:79],v[24:27]
s_waitcnt vmcnt(14)
s_barrier
ds_read_b64_tr_b16 a[192:193],v36 offset:0)"
R"(
ds_read_b64_tr_b16 a[194:195],v36 offset:512
ds_read_b64_tr_b16 a[196:197],v37 offset:0
ds_read_b64_tr_b16 a[198:199],v37 offset:512
ds_read_b64_tr_b16 a[200:201],v36 offset:4096
ds_read_b64_tr_b16 a[202:203],v36 offset:4608
ds_read_b64_tr_b16 a[204:205],v37 offset:4096
ds_read_b64_tr_b16 a[206:207],v37 offset:4608
s_waitcnt lgkmcnt(8)
v_mfma_f32_16x16x32_bf16 v[20:23],a[176:179],v[76:79],v[20:23]
v_mfma_f32_16x16x32_bf16 v[24:27],a[180:183],v[76:79],v[24:27]
v_mfma_f32_16x16x32_bf16 v[20:23],a[184:187],v[76:79],v[20:23]
v_mfma_f32_16x16x32_bf16 v[24:27],a[188:191],v[76:79],v[24:27]
s_waitcnt vmcnt(10)
s_barrier
ds_read_b64_tr_b16 a[208:209],v36 offset:16384
ds_read_b64_tr_b16 a[210:211],v36 offset:16896
ds_read_b64_tr_b16 a[212:213],v37 offset:16384
ds_read_b64_tr_b16 a[214:215],v37 offset:16896
ds_read_b64_tr_b16 a[216:217],v36 offset:20480
ds_read_b64_tr_b16 a[218:219],v36 offset:20992
ds_read_b64_tr_b16 a[220:221],v37 offset:20480
ds_read_b64_tr_b16 a[222:223],v37 offset:20992
s_waitcnt lgkmcnt(8)
v_mfma_f32_16x16x32_bf16 v[20:23],a[192:195],v[76:79],v[20:23]
v_mfma_f32_16x16x32_bf16 v[24:27],a[196:199],v[76:79],v[24:27]
v_mfma_f32_16x16x32_bf16 v[20:23],a[200:203],v[76:79],v[20:23]
v_mfma_f32_16x16x32_bf16 v[24:27],a[204:207],v[76:79],v[24:27]
s_waitcnt vmcnt(6)
s_barrier
ds_read_b64_tr_b16 a[224:225],v36 offset:32768
ds_read_b64_tr_b16 a[226:227],v36 offset:33280
ds_read_b64_tr_b16 a[228:229],v37 offset:32768
ds_read_b64_tr_b16 a[230:231],v37 offset:33280
ds_read_b64_tr_b16 a[232:233],v36 offset:36864
ds_read_b64_tr_b16 a[234:235],v36 offset:37376
ds_read_b64_tr_b16 a[236:237],v37 offset:36864
ds_read_b64_tr_b16 a[238:239],v37 offset:37376
s_waitcnt lgkmcnt(8)
v_mfma_f32_16x16x32_bf16 v[20:23],a[208:211],v[76:79],v[20:23]
v_mfma_f32_16x16x32_bf16 v[24:27],a[212:215],v[76:79],v[24:27]
v_mfma_f32_16x16x32_bf16 v[20:23],a[216:219],v[76:79],v[20:23]
v_mfma_f32_16x16x32_bf16 v[24:27],a[220:223],v[76:79],v[24:27]
s_waitcnt vmcnt(2)
s_barrier
ds_read_b64_tr_b16 a[240:241],v38 offset:0
ds_read_b64_tr_b16 a[242:243],v38 offset:512
ds_read_b64_tr_b16 a[244:245],v39 offset:0
ds_read_b64_tr_b16 a[246:247],v39 offset:512
ds_read_b64_tr_b16 a[248:249],v38 offset:4096
ds_read_b64_tr_b16 a[250:251],v38 offset:4608
ds_read_b64_tr_b16 a[252:253],v39 offset:4096
ds_read_b64_tr_b16 a[254:255],v39 offset:4608
s_waitcnt lgkmcnt(8)
v_mfma_f32_16x16x32_bf16 v[20:23],a[224:227],v[76:79],v[20:23]
v_mfma_f32_16x16x32_bf16 v[24:27],a[228:231],v[76:79],v[24:27]
v_mfma_f32_16x16x32_bf16 v[20:23],a[232:235],v[76:79],v[20:23]
v_mfma_f32_16x16x32_bf16 v[24:27],a[236:239],v[76:79],v[24:27]
s_waitcnt lgkmcnt(0)
v_mfma_f32_16x16x32_bf16 v[20:23],a[240:243],v[76:79],v[20:23]
v_mfma_f32_16x16x32_bf16 v[24:27],a[244:247],v[76:79],v[24:27]
v_mfma_f32_16x16x32_bf16 v[20:23],a[248:251],v[76:79],v[20:23]
v_mfma_f32_16x16x32_bf16 v[24:27],a[252:255],v[76:79],v[24:27]
s_waitcnt lgkmcnt(0)
s_barrier
v_readfirstlane_b32 s60,v5
s_nop 3
s_add_u32 s60,s60,1
s_cmp_eq_u32 s60,0
s_cselect_b32 s60,1,s60
Lg1_init_%=:
v_mov_b32 v5,s60
s_mov_b32 s59,536870912
s_add_u32 s57,s56,131072
s_mov_b32 s79,0
s_mov_b32 s75,0x3a800000
s_mov_b32 s76,0xc0135761
s_mov_b32 s77,0x3d372713
s_mov_b32 s44,s59
s_mov_b32 s45,s59
s_mov_b32 s46,s59
s_mov_b32 s47,s59
s_mov_b32 s48,s83
s_add_u32 s49,s48,s55
s_cmp_eq_u32 s49,s56
s_cselect_b32 s49,s57,s49
s_cmp_eq_u32 s49,s58
s_cselect_b32 s49,s59,s49
s_add_u32 s50,s49,s55
s_cmp_eq_u32 s50,s56
s_cselect_b32 s50,s57,s50
s_cmp_eq_u32 s50,s58
s_cselect_b32 s50,s59,s50
s_add_u32 s51,s50,s55
s_cmp_eq_u32 s51,s56
s_cselect_b32 s51,s57,s51
s_cmp_eq_u32 s51,s58
s_cselect_b32 s51,s59,s51
s_add_u32 s52,s51,s55
s_cmp_eq_u32 s52,s56
s_cselect_b32 s52,s57,s52
s_cmp_eq_u32 s52,s58
s_cselect_b32 s52,s59,s52
s_lshl_b32 s63,s54,12
s_add_u32 s63,s63,65536
s_add_u32 s64,s63,32768
s_lshl_b32 s88,s54,10
s_add_u32 s88,s88,131072
s_lshl_b32 s78,s54,11
s_add_u32 s78,s78,132608
v_mov_b32 v15,0x3727c5ac
v_mov_b32 v100,0x3f803f80
v_mov_b32 v101,0x3f803f80
v_mov_b32 v102,0x3f803f80
v_mov_b32 v103,0x3f803f80
s_mov_b32 s89,1048576
s_mov_b32 s82,0xaaaaaaaa
s_mov_b32 s83,0xaaaaaaaa
s_mov_b32 s84,0xcccccccc
s_mov_b32 s85,0xcccccccc
s_mov_b32 s98,0x00f0000f
s_mov_b32 s99,0xf0000f00
s_getpc_b64 s[90:91]
Lg1_pc_%=:
s_mov_b32 s68,Lg1_d1w0_%=-Lg1_pc_%=
s_cmp_eq_u32 s54,1
s_cselect_b32 s68,Lg1_d1w1_%=-Lg1_pc_%=,s68
s_cmp_eq_u32 s54,2
s_cselect_b32 s68,Lg1_d1w2_%=-Lg1_pc_%=,s68
s_cmp_eq_u32 s54,3
s_cselect_b32 s68,Lg1_d1w3_%=-Lg1_pc_%=,s68
s_add_u32 s96,s90,s68
s_addc_u32 s97,s91,0
s_mov_b32 s68,Lg1_d0w0_%=-Lg1_pc_%=
s_cmp_eq_u32 s54,1
s_cselect_b32 s68,Lg1_d0w1_%=-Lg1_pc_%=,s68
s_cmp_eq_u32 s54,2
s_cselect_b32 s68,Lg1_d0w2_%=-Lg1_pc_%=,s68
s_cmp_eq_u32 s54,3
s_cselect_b32 s68,Lg1_d0w3_%=-Lg1_pc_%=,s68
s_add_u32 s94,s90,s68
s_addc_u32 s95,s91,0
s_mov_b32 s68,Lg1_a1w0_%=-Lg1_pc_%=
s_cmp_eq_u32 s54,1
s_cselect_b32 s68,Lg1_a1w1_%=-Lg1_pc_%=,s68
s_cmp_eq_u32 s54,2
s_cselect_b32 s68,Lg1_a1w2_%=-Lg1_pc_%=,s68
s_cmp_eq_u32 s54,3
s_cselect_b32 s68,Lg1_a1w3_%=-Lg1_pc_%=,s68
s_add_u32 s92,s90,s68
s_addc_u32 s93,s91,0
s_mov_b32 s68,Lg1_a0w0_%=-Lg1_pc_%=
s_cmp_eq_u32 s54,1
s_cselect_b32 s68,Lg1_a0w1_%=-Lg1_pc_%=,s68
s_cmp_eq_u32 s54,2
s_cselect_b32 s68,Lg1_a0w2_%=-Lg1_pc_%=,s68
s_cmp_eq_u32 s54,3
s_cselect_b32 s68,Lg1_a0w3_%=-Lg1_pc_%=,s68
s_add_u32 s90,s90,s68
s_addc_u32 s91,s91,0
s_mov_b32 s66,s59
s_mov_b32 s65,0
s_mov_b32 s62,s63
s_branch Lg1_slow0_%=
Lg1_a0w0_%=:
v_mfma_f32_16x16x32_bf16 v[28:31],a[0:3],v[44:47],0
s_add_u32 m0,s62,16384
ds_read_b128 v[72:75],v2 offset:7168
s_lshr_b32 s73,s47,0
v_mfma_f32_16x16x32_bf16 v[32:35],a[4:7],v[44:47],0
buffer_load_dwordx4 v1,s[36:39],s66 offen offset:0 sc1 lds
v_mfma_f32_16x16x32_bf16 v[104:107],v[100:103],v[44:47],0
s_lshr_b32 s72,s66,6
s_lshr_b32 s71,s62,2
s_cmp_lt_u32 s66,4194304
v_mfma_f32_16x16x32_bf16 v[108:111],v[44:47],v[44:47],0
s_cselect_b32 s72,s59,s72
v_fma_f32 v36,-v96,v20,v36
v_fma_f32 v37,-v96,v21,v37
v_mfma_f32_16x16x32_bf16 v[28:31],a[8:11],v[48:51],v[28:31]
ds_read_b128 v[44:47],v2 offset:8192
v_fma_f32 v38,-v96,v22,v38
v_mfma_f32_16x16x32_bf16 v[32:35],a[12:15],v[48:51],v[32:35]
buffer_load_dwordx4 v1,s[36:39],s66 offen offset:1024 sc1 lds
v_mfma_f32_16x16x32_bf16 v[104:107],v[100:103],v[48:51],v[104:107]
v_fma_f32 v39,-v96,v23,v39
v_mul_f32 v76,v36,v36
v_mfma_f32_16x16x32_bf16 v[108:111],v[48:51],v[48:51],v[108:111]
v_mul_f32 v77,v37,v37
v_mul_f32 v78,v38,v38
v_mfma_f32_16x16x32_bf16 v[28:31],a[16:19],v[52:55],v[28:31]
ds_read_b128 v[48:51],v2 offset:9216
v_mul_f32 v79,v39,v39
v_mfma_f32_16x16x32_bf16 v[32:35],a[20:23],v[52:55],v[32:35]
buffer_load_dwordx4 v1,s[36:39],s66 offen offset:2048 sc1 lds
v_mfma_f32_16x16x32_bf16 v[104:107],v[100:103],v[52:55],v[104:107]
v_fma_f32 v76,v98,v76,v97
v_mul_f32 v76,v36,v76
v_mfma_f32_16x16x32_bf16 v[108:111],v[52:55],v[52:55],v[108:111]
v_exp_f32 v76,v76
v_mfma_f32_16x16x32_bf16 v[28:31],a[24:27],v[56:59],v[28:31])"
R"(
ds_read_b128 v[52:55],v2 offset:10240
v_fma_f32 v76,v76,v99,v99
v_mfma_f32_16x16x32_bf16 v[32:35],a[28:31],v[56:59],v[32:35]
buffer_load_dwordx4 v1,s[36:39],s66 offen offset:3072 sc1 lds
v_mfma_f32_16x16x32_bf16 v[104:107],v[100:103],v[56:59],v[104:107]
s_add_u32 m0,s71,s88
v_rcp_f32 v76,v76
v_mfma_f32_16x16x32_bf16 v[108:111],v[56:59],v[56:59],v[108:111]
s_waitcnt lgkmcnt(3)
buffer_load_dword v10,s[36:39],s72 offen offset:0 sc1 lds
v_mfma_f32_16x16x32_bf16 v[28:31],a[32:35],v[60:63],v[28:31]
s_xor_b32 s87,s64,65536
ds_read_b128 v[56:59],v2 offset:11264
v_fma_f32 v77,v98,v77,v97
v_mfma_f32_16x16x32_bf16 v[32:35],a[36:39],v[60:63],v[32:35]
s_cmp_ge_u32 s66,s59
v_mul_f32 v77,v37,v77
v_fma_f32 v78,v98,v78,v97
v_mfma_f32_16x16x32_bf16 v[28:31],a[40:43],v[64:67],v[28:31]
s_cselect_b32 s86,s49,s50
ds_read_b128 v[60:63],v2 offset:12288
v_mul_f32 v78,v38,v78
v_mfma_f32_16x16x32_bf16 v[32:35],a[44:47],v[64:67],v[32:35]
s_cselect_b32 s87,s87,s63
v_exp_f32 v77,v77
v_mfma_f32_16x16x32_bf16 v[28:31],a[48:51],v[68:71],v[28:31]
s_lshr_b32 s69,s50,5
ds_read_b128 v[64:67],v2 offset:13312
v_fma_f32 v77,v77,v99,v99
v_mfma_f32_16x16x32_bf16 v[32:35],a[52:55],v[68:71],v[32:35]
s_cmp_lt_u32 s50,4194304
v_exp_f32 v78,v78
v_mfma_f32_16x16x32_bf16 v[28:31],a[56:59],v[72:75],v[28:31]
s_cselect_b32 s68,0xffffcfff,-1
ds_read_b128 v[68:71],v2 offset:14336
v_fma_f32 v78,v78,v99,v99
v_mfma_f32_16x16x32_bf16 v[32:35],a[60:63],v[72:75],v[32:35]
s_waitcnt lgkmcnt(3)
v_rcp_f32 v77,v77
v_mfma_f32_16x16x32_bf16 v[28:31],a[64:67],v[44:47],v[28:31]
s_waitcnt vmcnt(11)
ds_read_b128 v[72:75],v2 offset:15360
v_fma_f32 v79,v98,v79,v97
v_mfma_f32_16x16x32_bf16 v[32:35],a[68:71],v[44:47],v[32:35]
s_barrier
v_mfma_f32_16x16x32_bf16 v[28:31],a[72:75],v[48:51],v[28:31]
s_and_b32 s68,s68,s69
ds_read_b128 v[44:47],v2 offset:16384
v_mul_f32 v79,v39,v79
v_mfma_f32_16x16x32_bf16 v[32:35],a[76:79],v[48:51],v[32:35]
s_cmp_lt_u32 s50,s89
v_exp_f32 v79,v79
v_mfma_f32_16x16x32_bf16 v[28:31],a[80:83],v[52:55],v[28:31]
s_cselect_b32 s68,s59,s68
ds_read_b128 v[48:51],v2 offset:17408
v_fma_f32 v79,v79,v99,v99
v_mfma_f32_16x16x32_bf16 v[32:35],a[84:87],v[52:55],v[32:35]
s_lshr_b32 s69,s51,5
v_rcp_f32 v78,v78
v_mfma_f32_16x16x32_bf16 v[28:31],a[88:91],v[56:59],v[28:31]
s_cmp_lt_u32 s51,4194304
ds_read_b128 v[52:55],v2 offset:18432
v_mul_f32 v36,v36,v76
v_mfma_f32_16x16x32_bf16 v[32:35],a[92:95],v[56:59],v[32:35]
s_waitcnt lgkmcnt(3)
v_rcp_f32 v79,v79
v_mfma_f32_16x16x32_bf16 v[28:31],a[96:99],v[60:63],v[28:31]
s_cselect_b32 s70,0xffffcfff,-1
ds_read_b128 v[56:59],v2 offset:19456
v_mul_f32 v37,v37,v77
v_mfma_f32_16x16x32_bf16 v[32:35],a[100:103],v[60:63],v[32:35]
s_and_b32 s70,s70,s69
v_mul_f32 v38,v38,v78
v_mul_f32 v39,v39,v79
v_mfma_f32_16x16x32_bf16 v[28:31],a[104:107],v[64:67],v[28:31]
s_cmp_lt_u32 s51,s89
ds_read_b128 v[60:63],v2 offset:20480
v_cvt_pk_bf16_f32 v84,v36,v37
v_mfma_f32_16x16x32_bf16 v[32:35],a[108:111],v[64:67],v[32:35]
s_cselect_b32 s70,s59,s70
v_cvt_pk_bf16_f32 v85,v38,v39
v_fma_f32 v40,-v96,v24,v40
v_mfma_f32_16x16x32_bf16 v[28:31],a[112:115],v[68:71],v[28:31]
s_mov_b32 s62,s87
ds_read_b128 v[64:67],v2 offset:21504
v_fma_f32 v41,-v96,v25,v41
v_mfma_f32_16x16x32_bf16 v[32:35],a[116:119],v[68:71],v[32:35]
s_cmp_lt_u32 s86,s89
v_fma_f32 v42,-v96,v26,v42
v_fma_f32 v43,-v96,v27,v43
v_mfma_f32_16x16x32_bf16 v[28:31],a[120:123],v[72:75],v[28:31]
ds_read_b128 v[68:71],v2 offset:22528
v_mul_f32 v80,v40,v40
v_mfma_f32_16x16x32_bf16 v[32:35],a[124:127],v[72:75],v[32:35]
s_waitcnt lgkmcnt(3)
v_mul_f32 v81,v41,v41
v_mul_f32 v82,v42,v42
v_mfma_f32_16x16x32_bf16 v[28:31],a[128:131],v[44:47],v[28:31]
ds_read_b128 v[72:75],v2 offset:23552
v_mul_f32 v83,v43,v43
v_mfma_f32_16x16x32_bf16 v[32:35],a[132:135],v[44:47],v[32:35]
v_fma_f32 v80,v98,v80,v97
v_mul_f32 v80,v40,v80
v_mfma_f32_16x16x32_bf16 v[104:107],v[100:103],v[44:47],v[104:107]
v_exp_f32 v80,v80
v_mfma_f32_16x16x32_bf16 v[108:111],v[44:47],v[44:47],v[108:111]
v_fma_f32 v80,v80,v99,v99
v_fma_f32 v81,v98,v81,v97
v_mfma_f32_16x16x32_bf16 v[28:31],a[136:139],v[48:51],v[28:31]
ds_read_b128 v[44:47],v2 offset:24576
v_mul_f32 v81,v41,v81
v_mfma_f32_16x16x32_bf16 v[32:35],a[140:143],v[48:51],v[32:35]
v_exp_f32 v81,v81
v_mfma_f32_16x16x32_bf16 v[104:107],v[100:103],v[48:51],v[104:107]
s_waitcnt vmcnt(10)
v_rcp_f32 v80,v80
v_mfma_f32_16x16x32_bf16 v[108:111],v[48:51],v[48:51],v[108:111]
s_barrier
v_mfma_f32_16x16x32_bf16 v[28:31],a[144:147],v[52:55],v[28:31]
ds_read_b128 v[48:51],v2 offset:25600
ds_read_b32 v16,v7 offset:0
v_mfma_f32_16x16x32_bf16 v[32:35],a[148:151],v[52:55],v[32:35]
v_fma_f32 v81,v81,v99,v99
v_fma_f32 v82,v98,v82,v97
v_mfma_f32_16x16x32_bf16 v[104:107],v[100:103],v[52:55],v[104:107]
v_rcp_f32 v81,v81
v_mfma_f32_16x16x32_bf16 v[108:111],v[52:55],v[52:55],v[108:111]
v_mul_f32 v82,v42,v82
v_fma_f32 v83,v98,v83,v97
v_mfma_f32_16x16x32_bf16 v[28:31],a[152:155],v[56:59],v[28:31]
s_waitcnt lgkmcnt(0)
ds_read_b128 v[52:55],v2 offset:26624
v_mul_f32 v83,v43,v83
v_mfma_f32_16x16x32_bf16 v[32:35],a[156:159],v[56:59],v[32:35]
v_cmp_eq_u32 vcc,s60,v16
v_exp_f32 v82,v82
v_mul_f32 v40,v40,v80
v_mfma_f32_16x16x32_bf16 v[104:107],v[100:103],v[56:59],v[104:107]
s_cselect_b32 vcc_lo,-1,vcc_lo
v_exp_f32 v83,v83
v_mfma_f32_16x16x32_bf16 v[108:111],v[56:59],v[56:59],v[108:111]
v_fma_f32 v82,v82,v99,v99
v_fma_f32 v83,v83,v99,v99
v_mfma_f32_16x16x32_bf16 v[28:31],a[160:163],v[60:63],v[28:31]
s_cmp_eq_u32 vcc_lo,-1
ds_read_b128 v[56:59],v2 offset:27648
v_mul_f32 v41,v41,v81
v_mfma_f32_16x16x32_bf16 v[32:35],a[164:167],v[60:63],v[32:35]
s_cselect_b32 s67,s86,s59
s_cmp_ge_u32 s67,s59
s_cselect_b32 s68,s68,s70
s_add_u32 m0,s78,256
v_mfma_f32_16x16x32_bf16 v[28:31],a[168:171],v[64:67],v[28:31]
buffer_load_dword v6,s[36:39],s68 offen offset:0 sc1 lds
v_cndmask_b32 v112,v108,v109,s[82:83]
v_cndmask_b32 v113,v110,v111,s[82:83]
v_cndmask_b32 v112,v112,v113,s[84:85]
v_cndmask_b32 v112,v104,v112,s[98:99]
v_mfma_f32_16x16x32_bf16 v[32:35],a[172:175],v[64:67],v[32:35]
s_mov_b32 m0,s62
ds_read_b128 v[60:63],v2 offset:28672
ds_write_b32 v13,v112 offset:0
v_mfma_f32_16x16x32_bf16 v[28:31],a[176:179],v[68:71],v[28:31]
buffer_load_dwordx4 v0,s[36:39],s67 offen offset:0 sc1 lds
v_mfma_f32_16x16x32_bf16 v[32:35],a[180:183],v[68:71],v[32:35]
s_xor_b32 s63,s63,65536
ds_read_b128 v[64:67],v2 offset:29696
v_cvt_pk_bf16_f32 v86,v40,v41
v_mfma_f32_16x16x32_bf16 v[28:31],a[184:187],v[72:75],v[28:31]
s_sub_u32 s69,s67,1048576
ds_read_b128 v[68:71],v2 offset:30720
s_cmp_lt_u32 s69,2097152
v_mfma_f32_16x16x32_bf16 v[32:35],a[188:191],v[72:75],v[32:35]
s_waitcnt lgkmcnt(4)
v_rcp_f32 v82,v82
v_mfma_f32_16x16x32_bf16 v[28:31],a[192:195],v[44:47],v[28:31]
s_waitcnt vmcnt(7) lgkmcnt(2)
ds_read_b128 v[72:75],v2 offset:31744
v_mul_f32 v42,v42,v82
v_mfma_f32_16x16x32_bf16 v[32:35],a[196:199],v[44:47],v[32:35]
s_barrier
v_mfma_f32_16x16x32_bf16 v[28:31],a[200:203],v[48:51],v[28:31]
buffer_load_dwordx4 v0,s[36:39],s67 offen offset:1024 sc1 lds
v_mfma_f32_16x16x32_bf16 v[32:35],a[204:207],v[48:51],v[32:35]
s_cselect_b32 s89,3145728,s89
ds_read_b128 v[44:47],v2 offset:32768
ds_read_b128 v[116:119],v14 offset:0
v_mfma_f32_16x16x32_bf16 v[28:31],a[208:211],v[52:55],v[28:31]
s_sub_u32 s69,s67,3145728
ds_read_b128 v[120:123],v14 offset:16
ds_read_b128 v[48:51],v2 offset:33792
v_mfma_f32_16x16x32_bf16 v[32:35],a[212:215],v[52:55],v[32:35]
s_cmp_lt_u32 s69,1048576
v_rcp_f32 v83,v83
v_mfma_f32_16x16x32_bf16 v[28:31],a[216:219],v[56:59],v[28:31]
buffer_load_dwordx4 v0,s[36:39],s67 offen offset:2048 sc1 lds
v_mfma_f32_16x16x32_bf16 v[32:35],a[220:223],v[56:59],v[32:35]
s_waitcnt lgkmcnt(4)
ds_read_b128 v[52:55],v2 offset:34816
v_mul_f32 v43,v43,v83)"
R"(
v_mfma_f32_16x16x32_bf16 v[28:31],a[224:227],v[60:63],v[28:31]
s_waitcnt lgkmcnt(2)
ds_read_b128 v[56:59],v2 offset:35840
v_cvt_pk_bf16_f32 v87,v42,v43
v_mfma_f32_16x16x32_bf16 v[32:35],a[228:231],v[60:63],v[32:35]
s_cselect_b32 s89,4194304,s89
v_add_f32 v116,v116,v117
v_add_f32 v118,v118,v119
v_mfma_f32_16x16x32_bf16 v[28:31],a[232:235],v[64:67],v[28:31]
buffer_load_dwordx4 v0,s[36:39],s67 offen offset:3072 sc1 lds
v_mfma_f32_16x16x32_bf16 v[32:35],a[236:239],v[64:67],v[32:35]
s_add_u32 s53,s52,s55
ds_read_b128 v[60:63],v2 offset:36864
v_add_f32 v116,v116,v118
v_mfma_f32_16x16x32_bf16 v[28:31],a[240:243],v[68:71],v[28:31]
s_cmp_eq_u32 s53,s56
ds_read_b128 v[64:67],v2 offset:37888
v_mul_f32 v92,s75,v116
v_mfma_f32_16x16x32_bf16 v[32:35],a[244:247],v[68:71],v[32:35]
s_cselect_b32 s53,s57,s53
v_add_f32 v120,v120,v121
v_add_f32 v122,v122,v123
v_add_f32 v120,v120,v122
v_mul_f32 v120,s75,v120
v_fma_f32 v120,-v92,v92,v120
v_max_f32 v120,0,v120
v_add_f32 v115,v15,v120
v_fma_f32 v120,v15,v115,v120
v_rsq_f32 v116,v120
v_mfma_f32_16x16x32_bf16 v[28:31],a[248:251],v[72:75],v[28:31]
s_waitcnt lgkmcnt(2)
s_cmp_eq_u32 s53,s58
s_cselect_b32 s53,s59,s53
ds_read_b128 v[68:71],v2 offset:38912
buffer_store_dwordx4 v[84:87],v3,s[36:39],s73 offen offset:0 sc0 sc1
v_mul_f32 v93,s76,v116
v_mul_f32 v94,v116,v116
v_mul_f32 v95,v120,v116
v_mul_f32 v94,s77,v94
v_mul_f32 v94,v94,v93
s_cmp_ge_u32 s66,s59
v_mfma_f32_16x16x32_bf16 v[32:35],a[252:255],v[72:75],v[32:35]
s_cbranch_scc1 Lg1_slow1_%=
Lg1_a1w0_%=:
v_mfma_f32_16x16x32_bf16 v[36:39],a[0:3],v[44:47],0
s_add_u32 m0,s62,16384
ds_read_b128 v[72:75],v2 offset:39936
s_lshr_b32 s73,s48,0
v_mfma_f32_16x16x32_bf16 v[40:43],a[4:7],v[44:47],0
buffer_load_dwordx4 v1,s[36:39],s67 offen offset:0 sc1 lds
v_mfma_f32_16x16x32_bf16 v[104:107],v[100:103],v[44:47],0
s_lshr_b32 s72,s67,6
s_lshr_b32 s71,s62,2
s_cmp_lt_u32 s67,4194304
v_mfma_f32_16x16x32_bf16 v[108:111],v[44:47],v[44:47],0
s_cselect_b32 s72,s59,s72
v_fma_f32 v28,-v92,v20,v28
v_fma_f32 v29,-v92,v21,v29
v_mfma_f32_16x16x32_bf16 v[36:39],a[8:11],v[48:51],v[36:39]
s_mov_b64 s[44:45],s[46:47]
ds_read_b128 v[44:47],v2 offset:40960
v_fma_f32 v30,-v92,v22,v30
v_mfma_f32_16x16x32_bf16 v[40:43],a[12:15],v[48:51],v[40:43]
buffer_load_dwordx4 v1,s[36:39],s67 offen offset:1024 sc1 lds
v_mfma_f32_16x16x32_bf16 v[104:107],v[100:103],v[48:51],v[104:107]
s_mov_b64 s[46:47],s[48:49]
v_fma_f32 v31,-v92,v23,v31
v_mul_f32 v76,v28,v28
v_mfma_f32_16x16x32_bf16 v[108:111],v[48:51],v[48:51],v[108:111]
s_mov_b64 s[48:49],s[50:51]
v_mul_f32 v77,v29,v29
v_mul_f32 v78,v30,v30
v_mfma_f32_16x16x32_bf16 v[36:39],a[16:19],v[52:55],v[36:39]
ds_read_b128 v[48:51],v2 offset:41984
v_mul_f32 v79,v31,v31
v_mfma_f32_16x16x32_bf16 v[40:43],a[20:23],v[52:55],v[40:43]
buffer_load_dwordx4 v1,s[36:39],s67 offen offset:2048 sc1 lds
v_mfma_f32_16x16x32_bf16 v[104:107],v[100:103],v[52:55],v[104:107]
v_fma_f32 v76,v94,v76,v93
v_mul_f32 v76,v28,v76
v_mfma_f32_16x16x32_bf16 v[108:111],v[52:55],v[52:55],v[108:111]
v_exp_f32 v76,v76
v_mfma_f32_16x16x32_bf16 v[36:39],a[24:27],v[56:59],v[36:39]
ds_read_b128 v[52:55],v2 offset:43008
v_fma_f32 v76,v76,v95,v95
v_mfma_f32_16x16x32_bf16 v[40:43],a[28:31],v[56:59],v[40:43]
buffer_load_dwordx4 v1,s[36:39],s67 offen offset:3072 sc1 lds
v_mfma_f32_16x16x32_bf16 v[104:107],v[100:103],v[56:59],v[104:107]
s_add_u32 m0,s71,s88
v_rcp_f32 v76,v76
v_mfma_f32_16x16x32_bf16 v[108:111],v[56:59],v[56:59],v[108:111]
s_waitcnt lgkmcnt(3)
buffer_load_dword v10,s[36:39],s72 offen offset:0 sc1 lds
v_mfma_f32_16x16x32_bf16 v[36:39],a[32:35],v[60:63],v[36:39]
s_xor_b32 s87,s63,65536
ds_read_b128 v[56:59],v2 offset:44032
v_fma_f32 v77,v94,v77,v93
v_mfma_f32_16x16x32_bf16 v[40:43],a[36:39],v[60:63],v[40:43]
s_cmp_ge_u32 s67,s59
v_mul_f32 v77,v29,v77
v_fma_f32 v78,v94,v78,v93
v_mfma_f32_16x16x32_bf16 v[36:39],a[40:43],v[64:67],v[36:39]
s_cselect_b32 s86,s50,s51
ds_read_b128 v[60:63],v2 offset:45056
v_mul_f32 v78,v30,v78
v_mfma_f32_16x16x32_bf16 v[40:43],a[44:47],v[64:67],v[40:43]
s_cselect_b32 s87,s87,s64
v_exp_f32 v77,v77
v_mfma_f32_16x16x32_bf16 v[36:39],a[48:51],v[68:71],v[36:39]
s_lshr_b32 s69,s51,5
ds_read_b128 v[64:67],v2 offset:46080
v_fma_f32 v77,v77,v95,v95
v_mfma_f32_16x16x32_bf16 v[40:43],a[52:55],v[68:71],v[40:43]
s_cmp_lt_u32 s51,4194304
v_exp_f32 v78,v78
v_mfma_f32_16x16x32_bf16 v[36:39],a[56:59],v[72:75],v[36:39]
s_cselect_b32 s68,0xffffcfff,-1
ds_read_b128 v[68:71],v2 offset:47104
v_fma_f32 v78,v78,v95,v95
v_mfma_f32_16x16x32_bf16 v[40:43],a[60:63],v[72:75],v[40:43]
s_waitcnt lgkmcnt(3)
v_rcp_f32 v77,v77
v_mfma_f32_16x16x32_bf16 v[36:39],a[64:67],v[44:47],v[36:39]
s_waitcnt vmcnt(11)
ds_read_b128 v[72:75],v2 offset:48128
v_fma_f32 v79,v94,v79,v93
v_mfma_f32_16x16x32_bf16 v[40:43],a[68:71],v[44:47],v[40:43]
s_barrier
v_mfma_f32_16x16x32_bf16 v[36:39],a[72:75],v[48:51],v[36:39]
s_and_b32 s68,s68,s69
ds_read_b128 v[44:47],v2 offset:49152
v_mul_f32 v79,v31,v79
v_mfma_f32_16x16x32_bf16 v[40:43],a[76:79],v[48:51],v[40:43]
s_cmp_lt_u32 s51,s89
v_exp_f32 v79,v79
v_mfma_f32_16x16x32_bf16 v[36:39],a[80:83],v[52:55],v[36:39]
s_cselect_b32 s68,s59,s68
ds_read_b128 v[48:51],v2 offset:50176
v_fma_f32 v79,v79,v95,v95
v_mfma_f32_16x16x32_bf16 v[40:43],a[84:87],v[52:55],v[40:43]
s_lshr_b32 s69,s52,5
v_rcp_f32 v78,v78
v_mfma_f32_16x16x32_bf16 v[36:39],a[88:91],v[56:59],v[36:39]
s_cmp_lt_u32 s52,4194304
ds_read_b128 v[52:55],v2 offset:51200
v_mul_f32 v28,v28,v76
v_mfma_f32_16x16x32_bf16 v[40:43],a[92:95],v[56:59],v[40:43]
s_waitcnt lgkmcnt(3)
v_rcp_f32 v79,v79
v_mfma_f32_16x16x32_bf16 v[36:39],a[96:99],v[60:63],v[36:39]
s_cselect_b32 s70,0xffffcfff,-1
ds_read_b128 v[56:59],v2 offset:52224
v_mul_f32 v29,v29,v77
v_mfma_f32_16x16x32_bf16 v[40:43],a[100:103],v[60:63],v[40:43]
s_and_b32 s70,s70,s69
v_mul_f32 v30,v30,v78
v_mul_f32 v31,v31,v79
v_mfma_f32_16x16x32_bf16 v[36:39],a[104:107],v[64:67],v[36:39]
s_cmp_lt_u32 s52,s89
ds_read_b128 v[60:63],v2 offset:53248
v_cvt_pk_bf16_f32 v84,v28,v29
v_mfma_f32_16x16x32_bf16 v[40:43],a[108:111],v[64:67],v[40:43]
s_cselect_b32 s70,s59,s70
v_cvt_pk_bf16_f32 v85,v30,v31
v_fma_f32 v32,-v92,v24,v32
v_mfma_f32_16x16x32_bf16 v[36:39],a[112:115],v[68:71],v[36:39]
s_mov_b32 s62,s87
ds_read_b128 v[64:67],v2 offset:54272
v_fma_f32 v33,-v92,v25,v33
v_mfma_f32_16x16x32_bf16 v[40:43],a[116:119],v[68:71],v[40:43]
s_cmp_lt_u32 s86,s89
v_fma_f32 v34,-v92,v26,v34
v_fma_f32 v35,-v92,v27,v35
v_mfma_f32_16x16x32_bf16 v[36:39],a[120:123],v[72:75],v[36:39]
s_mov_b64 s[50:51],s[52:53]
ds_read_b128 v[68:71],v2 offset:55296
v_mul_f32 v80,v32,v32
v_mfma_f32_16x16x32_bf16 v[40:43],a[124:127],v[72:75],v[40:43]
s_waitcnt lgkmcnt(3)
v_mul_f32 v81,v33,v33
v_mul_f32 v82,v34,v34
v_mfma_f32_16x16x32_bf16 v[36:39],a[128:131],v[44:47],v[36:39]
ds_read_b128 v[72:75],v2 offset:56320
v_mul_f32 v83,v35,v35
v_mfma_f32_16x16x32_bf16 v[40:43],a[132:135],v[44:47],v[40:43]
v_fma_f32 v80,v94,v80,v93
v_mul_f32 v80,v32,v80
v_mfma_f32_16x16x32_bf16 v[104:107],v[100:103],v[44:47],v[104:107]
v_exp_f32 v80,v80
v_mfma_f32_16x16x32_bf16 v[108:111],v[44:47],v[44:47],v[108:111]
v_fma_f32 v80,v80,v95,v95
v_fma_f32 v81,v94,v81,v93
v_mfma_f32_16x16x32_bf16 v[36:39],a[136:139],v[48:51],v[36:39]
ds_read_b128 v[44:47],v2 offset:57344
v_mul_f32 v81,v33,v81
v_mfma_f32_16x16x32_bf16 v[40:43],a[140:143],v[48:51],v[40:43]
v_exp_f32 v81,v81
v_mfma_f32_16x16x32_bf16 v[104:107],v[100:103],v[48:51],v[104:107]
s_waitcnt vmcnt(10)
v_rcp_f32 v80,v80)"
R"(
v_mfma_f32_16x16x32_bf16 v[108:111],v[48:51],v[48:51],v[108:111]
s_barrier
v_mfma_f32_16x16x32_bf16 v[36:39],a[144:147],v[52:55],v[36:39]
ds_read_b128 v[48:51],v2 offset:58368
ds_read_b32 v16,v7 offset:256
v_mfma_f32_16x16x32_bf16 v[40:43],a[148:151],v[52:55],v[40:43]
v_fma_f32 v81,v81,v95,v95
v_fma_f32 v82,v94,v82,v93
v_mfma_f32_16x16x32_bf16 v[104:107],v[100:103],v[52:55],v[104:107]
v_rcp_f32 v81,v81
v_mfma_f32_16x16x32_bf16 v[108:111],v[52:55],v[52:55],v[108:111]
v_mul_f32 v82,v34,v82
v_fma_f32 v83,v94,v83,v93
v_mfma_f32_16x16x32_bf16 v[36:39],a[152:155],v[56:59],v[36:39]
s_waitcnt lgkmcnt(0)
ds_read_b128 v[52:55],v2 offset:59392
v_mul_f32 v83,v35,v83
v_mfma_f32_16x16x32_bf16 v[40:43],a[156:159],v[56:59],v[40:43]
v_cmp_eq_u32 vcc,s60,v16
v_exp_f32 v82,v82
v_mul_f32 v32,v32,v80
v_mfma_f32_16x16x32_bf16 v[104:107],v[100:103],v[56:59],v[104:107]
s_cselect_b32 vcc_lo,-1,vcc_lo
v_exp_f32 v83,v83
v_mfma_f32_16x16x32_bf16 v[108:111],v[56:59],v[56:59],v[108:111]
v_fma_f32 v82,v82,v95,v95
v_fma_f32 v83,v83,v95,v95
v_mfma_f32_16x16x32_bf16 v[36:39],a[160:163],v[60:63],v[36:39]
s_cmp_eq_u32 vcc_lo,-1
ds_read_b128 v[56:59],v2 offset:60416
v_mul_f32 v33,v33,v81
v_mfma_f32_16x16x32_bf16 v[40:43],a[164:167],v[60:63],v[40:43]
s_cselect_b32 s66,s86,s59
s_cmp_ge_u32 s66,s59
s_cselect_b32 s68,s68,s70
s_add_u32 m0,s78,0
v_mfma_f32_16x16x32_bf16 v[36:39],a[168:171],v[64:67],v[36:39]
buffer_load_dword v6,s[36:39],s68 offen offset:0 sc1 lds
v_cndmask_b32 v112,v108,v109,s[82:83]
v_cndmask_b32 v113,v110,v111,s[82:83]
v_cndmask_b32 v112,v112,v113,s[84:85]
v_cndmask_b32 v112,v104,v112,s[98:99]
v_mfma_f32_16x16x32_bf16 v[40:43],a[172:175],v[64:67],v[40:43]
s_mov_b32 m0,s62
ds_read_b128 v[60:63],v2 offset:61440
ds_write_b32 v13,v112 offset:2048
v_mfma_f32_16x16x32_bf16 v[36:39],a[176:179],v[68:71],v[36:39]
buffer_load_dwordx4 v0,s[36:39],s66 offen offset:0 sc1 lds
v_mfma_f32_16x16x32_bf16 v[40:43],a[180:183],v[68:71],v[40:43]
s_xor_b32 s64,s64,65536
ds_read_b128 v[64:67],v2 offset:62464
v_cvt_pk_bf16_f32 v86,v32,v33
v_mfma_f32_16x16x32_bf16 v[36:39],a[184:187],v[72:75],v[36:39]
s_sub_u32 s69,s66,1048576
ds_read_b128 v[68:71],v2 offset:63488
s_cmp_lt_u32 s69,2097152
v_mfma_f32_16x16x32_bf16 v[40:43],a[188:191],v[72:75],v[40:43]
s_waitcnt lgkmcnt(4)
v_rcp_f32 v82,v82
v_mfma_f32_16x16x32_bf16 v[36:39],a[192:195],v[44:47],v[36:39]
s_waitcnt vmcnt(7) lgkmcnt(2)
ds_read_b128 v[72:75],v2 offset:64512
v_xor_b32 v2,65536,v2
v_mfma_f32_16x16x32_bf16 v[40:43],a[196:199],v[44:47],v[40:43]
s_barrier
v_mfma_f32_16x16x32_bf16 v[36:39],a[200:203],v[48:51],v[36:39]
buffer_load_dwordx4 v0,s[36:39],s66 offen offset:1024 sc1 lds
v_mfma_f32_16x16x32_bf16 v[40:43],a[204:207],v[48:51],v[40:43]
s_cselect_b32 s89,3145728,s89
ds_read_b128 v[44:47],v2 offset:0
ds_read_b128 v[116:119],v14 offset:2048
v_mfma_f32_16x16x32_bf16 v[36:39],a[208:211],v[52:55],v[36:39]
s_sub_u32 s69,s66,3145728
ds_read_b128 v[120:123],v14 offset:2064
ds_read_b128 v[48:51],v2 offset:1024
v_mfma_f32_16x16x32_bf16 v[40:43],a[212:215],v[52:55],v[40:43]
s_cmp_lt_u32 s69,1048576
v_rcp_f32 v83,v83
v_mfma_f32_16x16x32_bf16 v[36:39],a[216:219],v[56:59],v[36:39]
buffer_load_dwordx4 v0,s[36:39],s66 offen offset:2048 sc1 lds
v_mfma_f32_16x16x32_bf16 v[40:43],a[220:223],v[56:59],v[40:43]
s_waitcnt lgkmcnt(4)
ds_read_b128 v[52:55],v2 offset:2048
v_mul_f32 v34,v34,v82
v_mfma_f32_16x16x32_bf16 v[36:39],a[224:227],v[60:63],v[36:39]
s_waitcnt lgkmcnt(2)
ds_read_b128 v[56:59],v2 offset:3072
v_mul_f32 v35,v35,v83
v_mfma_f32_16x16x32_bf16 v[40:43],a[228:231],v[60:63],v[40:43]
s_cselect_b32 s89,4194304,s89
v_add_f32 v116,v116,v117
v_add_f32 v118,v118,v119
v_mfma_f32_16x16x32_bf16 v[36:39],a[232:235],v[64:67],v[36:39]
buffer_load_dwordx4 v0,s[36:39],s66 offen offset:3072 sc1 lds
v_mfma_f32_16x16x32_bf16 v[40:43],a[236:239],v[64:67],v[40:43]
s_add_u32 s74,s53,s55
ds_read_b128 v[60:63],v2 offset:4096
v_add_f32 v116,v116,v118
v_mfma_f32_16x16x32_bf16 v[36:39],a[240:243],v[68:71],v[36:39]
s_cmp_eq_u32 s74,s56
ds_read_b128 v[64:67],v2 offset:5120
v_mul_f32 v96,s75,v116
v_mfma_f32_16x16x32_bf16 v[40:43],a[244:247],v[68:71],v[40:43]
s_cselect_b32 s74,s57,s74
v_cvt_pk_bf16_f32 v87,v34,v35
v_add_f32 v120,v120,v121
v_add_f32 v122,v122,v123
v_add_f32 v120,v120,v122
v_mul_f32 v120,s75,v120
v_fma_f32 v120,-v96,v96,v120
v_max_f32 v120,0,v120
v_add_f32 v115,v15,v120
v_fma_f32 v120,v15,v115,v120
v_rsq_f32 v116,v120
v_mfma_f32_16x16x32_bf16 v[36:39],a[248:251],v[72:75],v[36:39]
s_waitcnt lgkmcnt(2)
s_cmp_eq_u32 s74,s58
s_cselect_b32 s74,s59,s74
ds_read_b128 v[68:71],v2 offset:6144
buffer_store_dwordx4 v[84:87],v3,s[36:39],s73 offen offset:0 sc0 sc1
v_xor_b32 v8,16384,v8
v_mul_f32 v97,s76,v116
v_mul_f32 v98,v116,v116
v_mul_f32 v99,v120,v116
v_mul_f32 v98,s77,v98
v_mul_f32 v98,v98,v97
s_mov_b32 s52,s74
s_cmp_lt_u32 s67,4194304
v_mfma_f32_16x16x32_bf16 v[40:43],a[252:255],v[72:75],v[40:43]
s_cbranch_scc1 Lg1_a0w0_%=
s_cmp_ge_u32 s48,4194304
s_cselect_b64 s[90:91],s[94:95],s[90:91]
s_cselect_b64 s[92:93],s[96:97],s[92:93]
s_cmp_lt_u32 s67,s59
s_cbranch_scc1 Lg1_d0w0_%=
s_branch Lg1_slow0_%=
Lg1_a0w1_%=:
v_mfma_f32_16x16x32_bf16 v[28:31],a[32:35],v[44:47],0
s_add_u32 m0,s62,16384
ds_read_b128 v[72:75],v2 offset:3072
s_lshr_b32 s73,s47,0
v_mfma_f32_16x16x32_bf16 v[32:35],a[36:39],v[44:47],0
s_lshr_b32 s72,s66,6
s_lshr_b32 s71,s62,2
s_cmp_lt_u32 s66,4194304
v_mfma_f32_16x16x32_bf16 v[104:107],v[100:103],v[44:47],0
buffer_load_dwordx4 v1,s[36:39],s66 offen offset:0 sc1 lds
v_mfma_f32_16x16x32_bf16 v[108:111],v[44:47],v[44:47],0
s_cselect_b32 s72,s59,s72
v_fma_f32 v36,-v96,v20,v36
v_fma_f32 v37,-v96,v21,v37
v_mfma_f32_16x16x32_bf16 v[28:31],a[40:43],v[48:51],v[28:31]
ds_read_b128 v[44:47],v2 offset:12288
v_fma_f32 v38,-v96,v22,v38
v_mfma_f32_16x16x32_bf16 v[32:35],a[44:47],v[48:51],v[32:35]
v_fma_f32 v39,-v96,v23,v39
v_mul_f32 v76,v36,v36
v_mfma_f32_16x16x32_bf16 v[104:107],v[100:103],v[48:51],v[104:107]
buffer_load_dwordx4 v1,s[36:39],s66 offen offset:1024 sc1 lds
v_mfma_f32_16x16x32_bf16 v[108:111],v[48:51],v[48:51],v[108:111]
v_mul_f32 v77,v37,v37
v_mul_f32 v78,v38,v38
v_mfma_f32_16x16x32_bf16 v[28:31],a[48:51],v[52:55],v[28:31]
ds_read_b128 v[48:51],v2 offset:13312
v_mul_f32 v79,v39,v39
v_mfma_f32_16x16x32_bf16 v[32:35],a[52:55],v[52:55],v[32:35]
v_fma_f32 v76,v98,v76,v97
v_mul_f32 v76,v36,v76
v_mfma_f32_16x16x32_bf16 v[104:107],v[100:103],v[52:55],v[104:107]
buffer_load_dwordx4 v1,s[36:39],s66 offen offset:2048 sc1 lds
v_mfma_f32_16x16x32_bf16 v[108:111],v[52:55],v[52:55],v[108:111]
v_exp_f32 v76,v76
v_mfma_f32_16x16x32_bf16 v[28:31],a[56:59],v[56:59],v[28:31]
ds_read_b128 v[52:55],v2 offset:14336
v_fma_f32 v76,v76,v99,v99
v_mfma_f32_16x16x32_bf16 v[32:35],a[60:63],v[56:59],v[32:35]
v_rcp_f32 v76,v76
v_mfma_f32_16x16x32_bf16 v[104:107],v[100:103],v[56:59],v[104:107]
buffer_load_dwordx4 v1,s[36:39],s66 offen offset:3072 sc1 lds
v_mfma_f32_16x16x32_bf16 v[108:111],v[56:59],v[56:59],v[108:111]
s_waitcnt lgkmcnt(3)
v_fma_f32 v77,v98,v77,v97
v_mul_f32 v77,v37,v77
v_mfma_f32_16x16x32_bf16 v[28:31],a[0:3],v[60:63],v[28:31]
s_add_u32 m0,s71,s88
ds_read_b128 v[56:59],v2 offset:15360
v_fma_f32 v78,v98,v78,v97
v_mfma_f32_16x16x32_bf16 v[32:35],a[4:7],v[60:63],v[32:35]
buffer_load_dword v10,s[36:39],s72 offen offset:0 sc1 lds
v_mfma_f32_16x16x32_bf16 v[28:31],a[8:11],v[64:67],v[28:31]
s_xor_b32 s87,s64,65536
ds_read_b128 v[60:63],v2 offset:8192
v_mul_f32 v78,v38,v78
v_mfma_f32_16x16x32_bf16 v[32:35],a[12:15],v[64:67],v[32:35]
s_cmp_ge_u32 s66,s59
v_exp_f32 v77,v77
v_mfma_f32_16x16x32_bf16 v[28:31],a[16:19],v[68:71],v[28:31]
s_cselect_b32 s86,s49,s50
ds_read_b128 v[64:67],v2 offset:9216
v_fma_f32 v77,v77,v99,v99)"
R"(
v_mfma_f32_16x16x32_bf16 v[32:35],a[20:23],v[68:71],v[32:35]
s_cselect_b32 s87,s87,s63
v_exp_f32 v78,v78
v_mfma_f32_16x16x32_bf16 v[28:31],a[24:27],v[72:75],v[28:31]
s_mov_b32 s62,s87
ds_read_b128 v[68:71],v2 offset:10240
v_fma_f32 v78,v78,v99,v99
v_mfma_f32_16x16x32_bf16 v[32:35],a[28:31],v[72:75],v[32:35]
s_waitcnt lgkmcnt(3)
v_rcp_f32 v77,v77
v_mfma_f32_16x16x32_bf16 v[28:31],a[96:99],v[44:47],v[28:31]
s_waitcnt vmcnt(10)
ds_read_b128 v[72:75],v2 offset:11264
v_fma_f32 v79,v98,v79,v97
v_mfma_f32_16x16x32_bf16 v[32:35],a[100:103],v[44:47],v[32:35]
s_barrier
v_mfma_f32_16x16x32_bf16 v[28:31],a[104:107],v[48:51],v[28:31]
s_cmp_lt_u32 s86,s89
ds_read_b128 v[44:47],v2 offset:20480
v_mul_f32 v79,v39,v79
v_mfma_f32_16x16x32_bf16 v[32:35],a[108:111],v[48:51],v[32:35]
s_mov_b32 m0,s62
v_exp_f32 v79,v79
v_mfma_f32_16x16x32_bf16 v[28:31],a[112:115],v[52:55],v[28:31]
ds_read_b128 v[48:51],v2 offset:21504
v_fma_f32 v79,v79,v99,v99
v_mfma_f32_16x16x32_bf16 v[32:35],a[116:119],v[52:55],v[32:35]
v_rcp_f32 v78,v78
v_mfma_f32_16x16x32_bf16 v[28:31],a[120:123],v[56:59],v[28:31]
ds_read_b128 v[52:55],v2 offset:22528
v_mul_f32 v36,v36,v76
v_mfma_f32_16x16x32_bf16 v[32:35],a[124:127],v[56:59],v[32:35]
s_waitcnt lgkmcnt(3)
v_rcp_f32 v79,v79
v_mfma_f32_16x16x32_bf16 v[28:31],a[64:67],v[60:63],v[28:31]
ds_read_b128 v[56:59],v2 offset:23552
v_mul_f32 v37,v37,v77
v_mfma_f32_16x16x32_bf16 v[32:35],a[68:71],v[60:63],v[32:35]
v_mul_f32 v38,v38,v78
v_mul_f32 v39,v39,v79
v_mfma_f32_16x16x32_bf16 v[28:31],a[72:75],v[64:67],v[28:31]
ds_read_b128 v[60:63],v2 offset:16384
v_cvt_pk_bf16_f32 v84,v36,v37
v_mfma_f32_16x16x32_bf16 v[32:35],a[76:79],v[64:67],v[32:35]
v_cvt_pk_bf16_f32 v85,v38,v39
v_fma_f32 v40,-v96,v24,v40
v_mfma_f32_16x16x32_bf16 v[28:31],a[80:83],v[68:71],v[28:31]
ds_read_b128 v[64:67],v2 offset:17408
v_fma_f32 v41,-v96,v25,v41
v_mfma_f32_16x16x32_bf16 v[32:35],a[84:87],v[68:71],v[32:35]
v_fma_f32 v42,-v96,v26,v42
v_fma_f32 v43,-v96,v27,v43
v_mfma_f32_16x16x32_bf16 v[28:31],a[88:91],v[72:75],v[28:31]
ds_read_b128 v[68:71],v2 offset:18432
v_mul_f32 v80,v40,v40
v_mfma_f32_16x16x32_bf16 v[32:35],a[92:95],v[72:75],v[32:35]
s_waitcnt lgkmcnt(3)
v_mul_f32 v81,v41,v41
v_mul_f32 v82,v42,v42
v_mfma_f32_16x16x32_bf16 v[28:31],a[160:163],v[44:47],v[28:31]
ds_read_b128 v[72:75],v2 offset:19456
v_mul_f32 v83,v43,v43
v_mfma_f32_16x16x32_bf16 v[32:35],a[164:167],v[44:47],v[32:35]
v_fma_f32 v80,v98,v80,v97
v_mul_f32 v80,v40,v80
v_mfma_f32_16x16x32_bf16 v[104:107],v[100:103],v[44:47],v[104:107]
v_exp_f32 v80,v80
v_mfma_f32_16x16x32_bf16 v[108:111],v[44:47],v[44:47],v[108:111]
v_fma_f32 v80,v80,v99,v99
v_fma_f32 v81,v98,v81,v97
v_mfma_f32_16x16x32_bf16 v[28:31],a[168:171],v[48:51],v[28:31]
ds_read_b128 v[44:47],v2 offset:28672
v_mul_f32 v81,v41,v81
v_mfma_f32_16x16x32_bf16 v[32:35],a[172:175],v[48:51],v[32:35]
v_exp_f32 v81,v81
v_mfma_f32_16x16x32_bf16 v[104:107],v[100:103],v[48:51],v[104:107]
s_nop 0
v_rcp_f32 v80,v80
v_mfma_f32_16x16x32_bf16 v[108:111],v[48:51],v[48:51],v[108:111]
s_barrier
v_mfma_f32_16x16x32_bf16 v[28:31],a[176:179],v[52:55],v[28:31]
ds_read_b128 v[48:51],v2 offset:29696
ds_read_b32 v16,v7 offset:0
v_mfma_f32_16x16x32_bf16 v[32:35],a[180:183],v[52:55],v[32:35]
v_fma_f32 v81,v81,v99,v99
v_fma_f32 v82,v98,v82,v97
v_mfma_f32_16x16x32_bf16 v[104:107],v[100:103],v[52:55],v[104:107]
v_rcp_f32 v81,v81
v_mfma_f32_16x16x32_bf16 v[108:111],v[52:55],v[52:55],v[108:111]
v_mul_f32 v82,v42,v82
v_fma_f32 v83,v98,v83,v97
v_mfma_f32_16x16x32_bf16 v[28:31],a[184:187],v[56:59],v[28:31]
s_waitcnt lgkmcnt(0)
ds_read_b128 v[52:55],v2 offset:30720
v_mul_f32 v83,v43,v83
v_mfma_f32_16x16x32_bf16 v[32:35],a[188:191],v[56:59],v[32:35]
v_cmp_eq_u32 vcc,s60,v16
v_exp_f32 v82,v82
v_mul_f32 v40,v40,v80
v_mfma_f32_16x16x32_bf16 v[104:107],v[100:103],v[56:59],v[104:107]
s_cselect_b32 vcc_lo,-1,vcc_lo
v_exp_f32 v83,v83
v_mfma_f32_16x16x32_bf16 v[108:111],v[56:59],v[56:59],v[108:111]
v_fma_f32 v82,v82,v99,v99
v_fma_f32 v83,v83,v99,v99
v_mfma_f32_16x16x32_bf16 v[28:31],a[128:131],v[60:63],v[28:31]
s_cmp_eq_u32 vcc_lo,-1
ds_read_b128 v[56:59],v2 offset:31744
s_cselect_b32 s67,s86,s59
v_mfma_f32_16x16x32_bf16 v[32:35],a[132:135],v[60:63],v[32:35]
buffer_load_dwordx4 v0,s[36:39],s67 offen offset:0 sc1 lds
v_mfma_f32_16x16x32_bf16 v[28:31],a[136:139],v[64:67],v[28:31]
s_xor_b32 s63,s63,65536
v_cndmask_b32 v112,v108,v109,s[82:83]
v_cndmask_b32 v113,v110,v111,s[82:83]
v_cndmask_b32 v112,v112,v113,s[84:85]
v_cndmask_b32 v112,v104,v112,s[98:99]
v_mfma_f32_16x16x32_bf16 v[32:35],a[140:143],v[64:67],v[32:35]
s_sub_u32 s69,s67,1048576
ds_read_b128 v[60:63],v2 offset:24576
ds_write_b32 v13,v112 offset:0
v_mfma_f32_16x16x32_bf16 v[28:31],a[144:147],v[68:71],v[28:31]
s_cmp_lt_u32 s69,2097152
ds_read_b128 v[64:67],v2 offset:25600
v_mul_f32 v41,v41,v81
v_mfma_f32_16x16x32_bf16 v[32:35],a[148:151],v[68:71],v[32:35]
buffer_load_dwordx4 v0,s[36:39],s67 offen offset:1024 sc1 lds
v_mfma_f32_16x16x32_bf16 v[28:31],a[152:155],v[72:75],v[28:31]
s_cselect_b32 s89,3145728,s89
ds_read_b128 v[68:71],v2 offset:26624
v_cvt_pk_bf16_f32 v86,v40,v41
v_mfma_f32_16x16x32_bf16 v[32:35],a[156:159],v[72:75],v[32:35]
s_waitcnt lgkmcnt(4)
v_rcp_f32 v82,v82
v_mfma_f32_16x16x32_bf16 v[28:31],a[224:227],v[44:47],v[28:31]
s_waitcnt vmcnt(7) lgkmcnt(2)
ds_read_b128 v[72:75],v2 offset:27648
v_mul_f32 v42,v42,v82
v_mfma_f32_16x16x32_bf16 v[32:35],a[228:231],v[44:47],v[32:35]
s_barrier
v_mfma_f32_16x16x32_bf16 v[28:31],a[232:235],v[48:51],v[28:31]
s_sub_u32 s69,s67,3145728
ds_read_b128 v[116:119],v14 offset:0
ds_read_b128 v[120:123],v14 offset:16
v_mfma_f32_16x16x32_bf16 v[32:35],a[236:239],v[48:51],v[32:35]
s_cmp_lt_u32 s69,1048576
ds_read_b128 v[44:47],v2 offset:36864
buffer_load_dwordx4 v0,s[36:39],s67 offen offset:2048 sc1 lds
v_mfma_f32_16x16x32_bf16 v[28:31],a[240:243],v[52:55],v[28:31]
s_cselect_b32 s89,4194304,s89
ds_read_b128 v[48:51],v2 offset:37888
s_add_u32 s53,s52,s55
v_mfma_f32_16x16x32_bf16 v[32:35],a[244:247],v[52:55],v[32:35]
s_cmp_eq_u32 s53,s56
v_rcp_f32 v83,v83
v_mfma_f32_16x16x32_bf16 v[28:31],a[248:251],v[56:59],v[28:31]
s_waitcnt lgkmcnt(2)
ds_read_b128 v[52:55],v2 offset:38912
v_mul_f32 v43,v43,v83
v_mfma_f32_16x16x32_bf16 v[32:35],a[252:255],v[56:59],v[32:35]
buffer_load_dwordx4 v0,s[36:39],s67 offen offset:3072 sc1 lds
v_mfma_f32_16x16x32_bf16 v[28:31],a[192:195],v[60:63],v[28:31]
s_cselect_b32 s53,s57,s53
ds_read_b128 v[56:59],v2 offset:39936
v_add_f32 v116,v116,v117
v_mfma_f32_16x16x32_bf16 v[32:35],a[196:199],v[60:63],v[32:35]
s_cmp_eq_u32 s53,s58
v_add_f32 v118,v118,v119
v_add_f32 v116,v116,v118
v_mfma_f32_16x16x32_bf16 v[28:31],a[200:203],v[64:67],v[28:31]
s_cselect_b32 s53,s59,s53
ds_read_b128 v[60:63],v2 offset:32768
v_cvt_pk_bf16_f32 v87,v42,v43
v_mfma_f32_16x16x32_bf16 v[32:35],a[204:207],v[64:67],v[32:35]
buffer_store_dwordx4 v[84:87],v3,s[36:39],s73 offen offset:0 sc0 sc1
v_mul_f32 v92,s75,v116
v_add_f32 v120,v120,v121
v_add_f32 v122,v122,v123
v_mfma_f32_16x16x32_bf16 v[28:31],a[208:211],v[68:71],v[28:31]
s_cmp_ge_u32 s66,s59
ds_read_b128 v[64:67],v2 offset:33792
v_add_f32 v120,v120,v122
v_mfma_f32_16x16x32_bf16 v[32:35],a[212:215],v[68:71],v[32:35]
v_mul_f32 v120,s75,v120
v_fma_f32 v120,-v92,v92,v120
v_max_f32 v120,0,v120
v_add_f32 v115,v15,v120
v_fma_f32 v120,v15,v115,v120
v_rsq_f32 v116,v120
v_mfma_f32_16x16x32_bf16 v[28:31],a[216:219],v[72:75],v[28:31]
s_waitcnt lgkmcnt(2)
ds_read_b128 v[68:71],v2 offset:34816
v_mul_f32 v93,s76,v116
v_mul_f32 v94,v116,v116
v_mul_f32 v95,v120,v116
v_mul_f32 v94,s77,v94
v_mul_f32 v94,v94,v93
v_mfma_f32_16x16x32_bf16 v[32:35],a[220:223],v[72:75],v[32:35]
s_cbranch_scc1 Lg1_slow1_%=
Lg1_a1w1_%=:)"
R"(
v_mfma_f32_16x16x32_bf16 v[36:39],a[32:35],v[44:47],0
s_add_u32 m0,s62,16384
ds_read_b128 v[72:75],v2 offset:35840
s_lshr_b32 s73,s48,0
v_mfma_f32_16x16x32_bf16 v[40:43],a[36:39],v[44:47],0
s_lshr_b32 s72,s67,6
s_lshr_b32 s71,s62,2
s_cmp_lt_u32 s67,4194304
v_mfma_f32_16x16x32_bf16 v[104:107],v[100:103],v[44:47],0
buffer_load_dwordx4 v1,s[36:39],s67 offen offset:0 sc1 lds
v_mfma_f32_16x16x32_bf16 v[108:111],v[44:47],v[44:47],0
s_cselect_b32 s72,s59,s72
v_fma_f32 v28,-v92,v20,v28
v_fma_f32 v29,-v92,v21,v29
v_mfma_f32_16x16x32_bf16 v[36:39],a[40:43],v[48:51],v[36:39]
s_mov_b64 s[44:45],s[46:47]
ds_read_b128 v[44:47],v2 offset:45056
v_fma_f32 v30,-v92,v22,v30
v_mfma_f32_16x16x32_bf16 v[40:43],a[44:47],v[48:51],v[40:43]
s_mov_b64 s[46:47],s[48:49]
v_fma_f32 v31,-v92,v23,v31
v_mul_f32 v76,v28,v28
v_mfma_f32_16x16x32_bf16 v[104:107],v[100:103],v[48:51],v[104:107]
buffer_load_dwordx4 v1,s[36:39],s67 offen offset:1024 sc1 lds
v_mfma_f32_16x16x32_bf16 v[108:111],v[48:51],v[48:51],v[108:111]
s_mov_b64 s[48:49],s[50:51]
v_mul_f32 v77,v29,v29
v_mul_f32 v78,v30,v30
v_mfma_f32_16x16x32_bf16 v[36:39],a[48:51],v[52:55],v[36:39]
ds_read_b128 v[48:51],v2 offset:46080
v_mul_f32 v79,v31,v31
v_mfma_f32_16x16x32_bf16 v[40:43],a[52:55],v[52:55],v[40:43]
v_fma_f32 v76,v94,v76,v93
v_mul_f32 v76,v28,v76
v_mfma_f32_16x16x32_bf16 v[104:107],v[100:103],v[52:55],v[104:107]
buffer_load_dwordx4 v1,s[36:39],s67 offen offset:2048 sc1 lds
v_mfma_f32_16x16x32_bf16 v[108:111],v[52:55],v[52:55],v[108:111]
v_exp_f32 v76,v76
v_mfma_f32_16x16x32_bf16 v[36:39],a[56:59],v[56:59],v[36:39]
ds_read_b128 v[52:55],v2 offset:47104
v_fma_f32 v76,v76,v95,v95
v_mfma_f32_16x16x32_bf16 v[40:43],a[60:63],v[56:59],v[40:43]
v_rcp_f32 v76,v76
v_mfma_f32_16x16x32_bf16 v[104:107],v[100:103],v[56:59],v[104:107]
buffer_load_dwordx4 v1,s[36:39],s67 offen offset:3072 sc1 lds
v_mfma_f32_16x16x32_bf16 v[108:111],v[56:59],v[56:59],v[108:111]
s_waitcnt lgkmcnt(3)
v_fma_f32 v77,v94,v77,v93
v_mul_f32 v77,v29,v77
v_mfma_f32_16x16x32_bf16 v[36:39],a[0:3],v[60:63],v[36:39]
s_add_u32 m0,s71,s88
ds_read_b128 v[56:59],v2 offset:48128
v_fma_f32 v78,v94,v78,v93
v_mfma_f32_16x16x32_bf16 v[40:43],a[4:7],v[60:63],v[40:43]
buffer_load_dword v10,s[36:39],s72 offen offset:0 sc1 lds
v_mfma_f32_16x16x32_bf16 v[36:39],a[8:11],v[64:67],v[36:39]
s_xor_b32 s87,s63,65536
ds_read_b128 v[60:63],v2 offset:40960
v_mul_f32 v78,v30,v78
v_mfma_f32_16x16x32_bf16 v[40:43],a[12:15],v[64:67],v[40:43]
s_cmp_ge_u32 s67,s59
v_exp_f32 v77,v77
v_mfma_f32_16x16x32_bf16 v[36:39],a[16:19],v[68:71],v[36:39]
s_cselect_b32 s86,s50,s51
ds_read_b128 v[64:67],v2 offset:41984
v_fma_f32 v77,v77,v95,v95
v_mfma_f32_16x16x32_bf16 v[40:43],a[20:23],v[68:71],v[40:43]
s_cselect_b32 s87,s87,s64
v_exp_f32 v78,v78
v_mfma_f32_16x16x32_bf16 v[36:39],a[24:27],v[72:75],v[36:39]
s_mov_b32 s62,s87
ds_read_b128 v[68:71],v2 offset:43008
v_fma_f32 v78,v78,v95,v95
v_mfma_f32_16x16x32_bf16 v[40:43],a[28:31],v[72:75],v[40:43]
s_waitcnt lgkmcnt(3)
v_rcp_f32 v77,v77
v_mfma_f32_16x16x32_bf16 v[36:39],a[96:99],v[44:47],v[36:39]
s_waitcnt vmcnt(10)
ds_read_b128 v[72:75],v2 offset:44032
v_fma_f32 v79,v94,v79,v93
v_mfma_f32_16x16x32_bf16 v[40:43],a[100:103],v[44:47],v[40:43]
s_barrier
v_mfma_f32_16x16x32_bf16 v[36:39],a[104:107],v[48:51],v[36:39]
s_cmp_lt_u32 s86,s89
ds_read_b128 v[44:47],v2 offset:53248
v_mul_f32 v79,v31,v79
v_mfma_f32_16x16x32_bf16 v[40:43],a[108:111],v[48:51],v[40:43]
s_mov_b32 m0,s62
v_exp_f32 v79,v79
v_mfma_f32_16x16x32_bf16 v[36:39],a[112:115],v[52:55],v[36:39]
s_mov_b64 s[50:51],s[52:53]
ds_read_b128 v[48:51],v2 offset:54272
v_fma_f32 v79,v79,v95,v95
v_mfma_f32_16x16x32_bf16 v[40:43],a[116:119],v[52:55],v[40:43]
v_rcp_f32 v78,v78
v_mfma_f32_16x16x32_bf16 v[36:39],a[120:123],v[56:59],v[36:39]
ds_read_b128 v[52:55],v2 offset:55296
v_mul_f32 v28,v28,v76
v_mfma_f32_16x16x32_bf16 v[40:43],a[124:127],v[56:59],v[40:43]
s_waitcnt lgkmcnt(3)
v_rcp_f32 v79,v79
v_mfma_f32_16x16x32_bf16 v[36:39],a[64:67],v[60:63],v[36:39]
ds_read_b128 v[56:59],v2 offset:56320
v_mul_f32 v29,v29,v77
v_mfma_f32_16x16x32_bf16 v[40:43],a[68:71],v[60:63],v[40:43]
v_mul_f32 v30,v30,v78
v_mul_f32 v31,v31,v79
v_mfma_f32_16x16x32_bf16 v[36:39],a[72:75],v[64:67],v[36:39]
ds_read_b128 v[60:63],v2 offset:49152
v_cvt_pk_bf16_f32 v84,v28,v29
v_mfma_f32_16x16x32_bf16 v[40:43],a[76:79],v[64:67],v[40:43]
v_cvt_pk_bf16_f32 v85,v30,v31
v_fma_f32 v32,-v92,v24,v32
v_mfma_f32_16x16x32_bf16 v[36:39],a[80:83],v[68:71],v[36:39]
ds_read_b128 v[64:67],v2 offset:50176
v_fma_f32 v33,-v92,v25,v33
v_mfma_f32_16x16x32_bf16 v[40:43],a[84:87],v[68:71],v[40:43]
v_fma_f32 v34,-v92,v26,v34
v_fma_f32 v35,-v92,v27,v35
v_mfma_f32_16x16x32_bf16 v[36:39],a[88:91],v[72:75],v[36:39]
ds_read_b128 v[68:71],v2 offset:51200
v_mul_f32 v80,v32,v32
v_mfma_f32_16x16x32_bf16 v[40:43],a[92:95],v[72:75],v[40:43]
s_waitcnt lgkmcnt(3)
v_mul_f32 v81,v33,v33
v_mul_f32 v82,v34,v34
v_mfma_f32_16x16x32_bf16 v[36:39],a[160:163],v[44:47],v[36:39]
ds_read_b128 v[72:75],v2 offset:52224
v_mul_f32 v83,v35,v35
v_mfma_f32_16x16x32_bf16 v[40:43],a[164:167],v[44:47],v[40:43]
v_fma_f32 v80,v94,v80,v93
v_mul_f32 v80,v32,v80
v_mfma_f32_16x16x32_bf16 v[104:107],v[100:103],v[44:47],v[104:107]
v_exp_f32 v80,v80
v_mfma_f32_16x16x32_bf16 v[108:111],v[44:47],v[44:47],v[108:111]
v_fma_f32 v80,v80,v95,v95
v_fma_f32 v81,v94,v81,v93
v_mfma_f32_16x16x32_bf16 v[36:39],a[168:171],v[48:51],v[36:39]
ds_read_b128 v[44:47],v2 offset:61440
v_mul_f32 v81,v33,v81
v_mfma_f32_16x16x32_bf16 v[40:43],a[172:175],v[48:51],v[40:43]
v_exp_f32 v81,v81
v_mfma_f32_16x16x32_bf16 v[104:107],v[100:103],v[48:51],v[104:107]
s_nop 0
v_rcp_f32 v80,v80
v_mfma_f32_16x16x32_bf16 v[108:111],v[48:51],v[48:51],v[108:111]
s_barrier
v_mfma_f32_16x16x32_bf16 v[36:39],a[176:179],v[52:55],v[36:39]
ds_read_b128 v[48:51],v2 offset:62464
ds_read_b32 v16,v7 offset:256
v_mfma_f32_16x16x32_bf16 v[40:43],a[180:183],v[52:55],v[40:43]
v_fma_f32 v81,v81,v95,v95
v_fma_f32 v82,v94,v82,v93
v_mfma_f32_16x16x32_bf16 v[104:107],v[100:103],v[52:55],v[104:107]
v_rcp_f32 v81,v81
v_mfma_f32_16x16x32_bf16 v[108:111],v[52:55],v[52:55],v[108:111]
v_mul_f32 v82,v34,v82
v_fma_f32 v83,v94,v83,v93
v_mfma_f32_16x16x32_bf16 v[36:39],a[184:187],v[56:59],v[36:39]
s_waitcnt lgkmcnt(0)
ds_read_b128 v[52:55],v2 offset:63488
v_mul_f32 v83,v35,v83
v_mfma_f32_16x16x32_bf16 v[40:43],a[188:191],v[56:59],v[40:43]
v_cmp_eq_u32 vcc,s60,v16
v_exp_f32 v82,v82
v_mul_f32 v32,v32,v80
v_mfma_f32_16x16x32_bf16 v[104:107],v[100:103],v[56:59],v[104:107]
s_cselect_b32 vcc_lo,-1,vcc_lo
v_exp_f32 v83,v83
v_mfma_f32_16x16x32_bf16 v[108:111],v[56:59],v[56:59],v[108:111]
v_fma_f32 v82,v82,v95,v95
v_fma_f32 v83,v83,v95,v95
v_mfma_f32_16x16x32_bf16 v[36:39],a[128:131],v[60:63],v[36:39]
s_cmp_eq_u32 vcc_lo,-1
ds_read_b128 v[56:59],v2 offset:64512
s_cselect_b32 s66,s86,s59
v_mfma_f32_16x16x32_bf16 v[40:43],a[132:135],v[60:63],v[40:43]
buffer_load_dwordx4 v0,s[36:39],s66 offen offset:0 sc1 lds
v_mfma_f32_16x16x32_bf16 v[36:39],a[136:139],v[64:67],v[36:39]
s_xor_b32 s64,s64,65536
v_cndmask_b32 v112,v108,v109,s[82:83]
v_cndmask_b32 v113,v110,v111,s[82:83]
v_cndmask_b32 v112,v112,v113,s[84:85]
v_cndmask_b32 v112,v104,v112,s[98:99]
v_mfma_f32_16x16x32_bf16 v[40:43],a[140:143],v[64:67],v[40:43]
s_sub_u32 s69,s66,1048576
ds_read_b128 v[60:63],v2 offset:57344
ds_write_b32 v13,v112 offset:2048
v_mfma_f32_16x16x32_bf16 v[36:39],a[144:147],v[68:71],v[36:39]
s_cmp_lt_u32 s69,2097152
ds_read_b128 v[64:67],v2 offset:58368
v_mul_f32 v33,v33,v81
v_mfma_f32_16x16x32_bf16 v[40:43],a[148:151],v[68:71],v[40:43]
buffer_load_dwordx4 v0,s[36:39],s66 offen offset:1024 sc1 lds
v_mfma_f32_16x16x32_bf16 v[36:39],a[152:155],v[72:75],v[36:39]
s_cselect_b32 s89,3145728,s89
ds_read_b128 v[68:71],v2 offset:59392
v_cvt_pk_bf16_f32 v86,v32,v33
v_mfma_f32_16x16x32_bf16 v[40:43],a[156:159],v[72:75],v[40:43]
s_waitcnt lgkmcnt(4))"
R"(
v_rcp_f32 v82,v82
v_mfma_f32_16x16x32_bf16 v[36:39],a[224:227],v[44:47],v[36:39]
s_waitcnt vmcnt(7) lgkmcnt(2)
ds_read_b128 v[72:75],v2 offset:60416
v_xor_b32 v2,65536,v2
v_mfma_f32_16x16x32_bf16 v[40:43],a[228:231],v[44:47],v[40:43]
s_barrier
v_mfma_f32_16x16x32_bf16 v[36:39],a[232:235],v[48:51],v[36:39]
s_sub_u32 s69,s66,3145728
ds_read_b128 v[116:119],v14 offset:2048
ds_read_b128 v[120:123],v14 offset:2064
v_mfma_f32_16x16x32_bf16 v[40:43],a[236:239],v[48:51],v[40:43]
s_cmp_lt_u32 s69,1048576
ds_read_b128 v[44:47],v2 offset:4096
buffer_load_dwordx4 v0,s[36:39],s66 offen offset:2048 sc1 lds
v_mfma_f32_16x16x32_bf16 v[36:39],a[240:243],v[52:55],v[36:39]
s_cselect_b32 s89,4194304,s89
ds_read_b128 v[48:51],v2 offset:5120
v_mul_f32 v34,v34,v82
v_mfma_f32_16x16x32_bf16 v[40:43],a[244:247],v[52:55],v[40:43]
s_add_u32 s74,s53,s55
v_rcp_f32 v83,v83
v_mfma_f32_16x16x32_bf16 v[36:39],a[248:251],v[56:59],v[36:39]
s_waitcnt lgkmcnt(2)
ds_read_b128 v[52:55],v2 offset:6144
v_mul_f32 v35,v35,v83
v_mfma_f32_16x16x32_bf16 v[40:43],a[252:255],v[56:59],v[40:43]
buffer_load_dwordx4 v0,s[36:39],s66 offen offset:3072 sc1 lds
v_mfma_f32_16x16x32_bf16 v[36:39],a[192:195],v[60:63],v[36:39]
s_cmp_eq_u32 s74,s56
ds_read_b128 v[56:59],v2 offset:7168
v_add_f32 v116,v116,v117
v_mfma_f32_16x16x32_bf16 v[40:43],a[196:199],v[60:63],v[40:43]
s_cselect_b32 s74,s57,s74
v_add_f32 v118,v118,v119
v_add_f32 v116,v116,v118
v_mfma_f32_16x16x32_bf16 v[36:39],a[200:203],v[64:67],v[36:39]
s_cmp_eq_u32 s74,s58
ds_read_b128 v[60:63],v2 offset:0
v_cvt_pk_bf16_f32 v87,v34,v35
v_mfma_f32_16x16x32_bf16 v[40:43],a[204:207],v[64:67],v[40:43]
buffer_store_dwordx4 v[84:87],v3,s[36:39],s73 offen offset:0 sc0 sc1
v_mul_f32 v96,s75,v116
v_add_f32 v120,v120,v121
v_add_f32 v122,v122,v123
v_mfma_f32_16x16x32_bf16 v[36:39],a[208:211],v[68:71],v[36:39]
s_cselect_b32 s74,s59,s74
ds_read_b128 v[64:67],v2 offset:1024
v_add_f32 v120,v120,v122
v_mfma_f32_16x16x32_bf16 v[40:43],a[212:215],v[68:71],v[40:43]
s_mov_b32 s52,s74
v_mul_f32 v120,s75,v120
v_fma_f32 v120,-v96,v96,v120
v_max_f32 v120,0,v120
v_add_f32 v115,v15,v120
v_fma_f32 v120,v15,v115,v120
v_rsq_f32 v116,v120
v_mfma_f32_16x16x32_bf16 v[36:39],a[216:219],v[72:75],v[36:39]
s_waitcnt lgkmcnt(2)
ds_read_b128 v[68:71],v2 offset:2048
v_xor_b32 v8,16384,v8
v_mul_f32 v97,s76,v116
v_mul_f32 v98,v116,v116
v_mul_f32 v99,v120,v116
v_mul_f32 v98,s77,v98
v_mul_f32 v98,v98,v97
s_cmp_lt_u32 s67,4194304
v_mfma_f32_16x16x32_bf16 v[40:43],a[220:223],v[72:75],v[40:43]
s_cbranch_scc1 Lg1_a0w1_%=
s_cmp_ge_u32 s48,4194304
s_cselect_b64 s[90:91],s[94:95],s[90:91]
s_cselect_b64 s[92:93],s[96:97],s[92:93]
s_cmp_lt_u32 s67,s59
s_cbranch_scc1 Lg1_d0w1_%=
s_branch Lg1_slow0_%=
Lg1_a0w2_%=:
v_mfma_f32_16x16x32_bf16 v[28:31],a[64:67],v[44:47],0
s_add_u32 m0,s62,16384
ds_read_b128 v[72:75],v2 offset:15360
s_lshr_b32 s73,s47,0
v_mfma_f32_16x16x32_bf16 v[32:35],a[68:71],v[44:47],0
s_lshr_b32 s65,s48,6
s_lshr_b32 s72,s66,6
s_lshr_b32 s71,s62,2
v_mfma_f32_16x16x32_bf16 v[104:107],v[100:103],v[44:47],0
s_cmp_lt_u32 s66,4194304
s_cselect_b32 s72,s59,s72
v_mfma_f32_16x16x32_bf16 v[108:111],v[44:47],v[44:47],0
buffer_load_dwordx4 v1,s[36:39],s66 offen offset:0 sc1 lds
v_mfma_f32_16x16x32_bf16 v[28:31],a[72:75],v[48:51],v[28:31]
ds_read_b128 v[44:47],v2 offset:0
v_fma_f32 v36,-v96,v20,v36
v_mfma_f32_16x16x32_bf16 v[32:35],a[76:79],v[48:51],v[32:35]
v_fma_f32 v37,-v96,v21,v37
v_fma_f32 v38,-v96,v22,v38
v_mfma_f32_16x16x32_bf16 v[104:107],v[100:103],v[48:51],v[104:107]
v_fma_f32 v39,-v96,v23,v39
v_mul_f32 v76,v36,v36
v_mfma_f32_16x16x32_bf16 v[108:111],v[48:51],v[48:51],v[108:111]
buffer_load_dwordx4 v1,s[36:39],s66 offen offset:1024 sc1 lds
v_mfma_f32_16x16x32_bf16 v[28:31],a[80:83],v[52:55],v[28:31]
ds_read_b128 v[48:51],v2 offset:1024
v_mul_f32 v77,v37,v37
v_mfma_f32_16x16x32_bf16 v[32:35],a[84:87],v[52:55],v[32:35]
v_mul_f32 v78,v38,v38
v_mul_f32 v79,v39,v39
v_mfma_f32_16x16x32_bf16 v[104:107],v[100:103],v[52:55],v[104:107]
v_fma_f32 v76,v98,v76,v97
v_mul_f32 v76,v36,v76
v_mfma_f32_16x16x32_bf16 v[108:111],v[52:55],v[52:55],v[108:111]
buffer_load_dwordx4 v1,s[36:39],s66 offen offset:2048 sc1 lds
v_mfma_f32_16x16x32_bf16 v[28:31],a[88:91],v[56:59],v[28:31]
ds_read_b128 v[52:55],v2 offset:2048
v_fma_f32 v77,v98,v77,v97
v_mfma_f32_16x16x32_bf16 v[32:35],a[92:95],v[56:59],v[32:35]
v_exp_f32 v76,v76
v_mfma_f32_16x16x32_bf16 v[104:107],v[100:103],v[56:59],v[104:107]
v_mul_f32 v77,v37,v77
v_fma_f32 v76,v76,v99,v99
v_mfma_f32_16x16x32_bf16 v[108:111],v[56:59],v[56:59],v[108:111]
s_waitcnt lgkmcnt(3)
buffer_load_dwordx4 v1,s[36:39],s66 offen offset:3072 sc1 lds
v_mfma_f32_16x16x32_bf16 v[28:31],a[96:99],v[60:63],v[28:31]
s_add_u32 m0,s71,s88
ds_read_b128 v[56:59],v2 offset:3072
v_fma_f32 v78,v98,v78,v97
v_mfma_f32_16x16x32_bf16 v[32:35],a[100:103],v[60:63],v[32:35]
buffer_load_dword v10,s[36:39],s72 offen offset:0 sc1 lds
v_mfma_f32_16x16x32_bf16 v[28:31],a[104:107],v[64:67],v[28:31]
s_xor_b32 s87,s64,65536
ds_read_b128 v[60:63],v2 offset:4096
v_fma_f32 v79,v98,v79,v97
v_mfma_f32_16x16x32_bf16 v[32:35],a[108:111],v[64:67],v[32:35]
s_cmp_ge_u32 s66,s59
v_exp_f32 v77,v77
v_mfma_f32_16x16x32_bf16 v[28:31],a[112:115],v[68:71],v[28:31]
s_cselect_b32 s86,s49,s50
ds_read_b128 v[64:67],v2 offset:5120
v_mul_f32 v78,v38,v78
v_mfma_f32_16x16x32_bf16 v[32:35],a[116:119],v[68:71],v[32:35]
s_cselect_b32 s87,s87,s63
v_exp_f32 v78,v78
v_mfma_f32_16x16x32_bf16 v[28:31],a[120:123],v[72:75],v[28:31]
s_mov_b32 s62,s87
ds_read_b128 v[68:71],v2 offset:6144
v_mul_f32 v79,v39,v79
v_mfma_f32_16x16x32_bf16 v[32:35],a[124:127],v[72:75],v[32:35]
s_waitcnt lgkmcnt(3)
v_exp_f32 v79,v79
v_mfma_f32_16x16x32_bf16 v[28:31],a[0:3],v[44:47],v[28:31]
s_waitcnt vmcnt(10)
ds_read_b128 v[72:75],v2 offset:7168
v_fma_f32 v77,v77,v99,v99
v_mfma_f32_16x16x32_bf16 v[32:35],a[4:7],v[44:47],v[32:35]
s_barrier
v_mfma_f32_16x16x32_bf16 v[28:31],a[8:11],v[48:51],v[28:31]
s_cmp_lt_u32 s86,s89
ds_read_b128 v[44:47],v2 offset:24576
v_fma_f32 v78,v78,v99,v99
v_mfma_f32_16x16x32_bf16 v[32:35],a[12:15],v[48:51],v[32:35]
s_mov_b32 m0,s62
v_rcp_f32 v76,v76
v_mfma_f32_16x16x32_bf16 v[28:31],a[16:19],v[52:55],v[28:31]
ds_read_b128 v[48:51],v2 offset:25600
v_fma_f32 v79,v79,v99,v99
v_mfma_f32_16x16x32_bf16 v[32:35],a[20:23],v[52:55],v[32:35]
v_rcp_f32 v77,v77
v_mfma_f32_16x16x32_bf16 v[28:31],a[24:27],v[56:59],v[28:31]
ds_read_b128 v[52:55],v2 offset:26624
v_mul_f32 v36,v36,v76
v_mfma_f32_16x16x32_bf16 v[32:35],a[28:31],v[56:59],v[32:35]
s_waitcnt lgkmcnt(3)
v_rcp_f32 v78,v78
v_mfma_f32_16x16x32_bf16 v[28:31],a[32:35],v[60:63],v[28:31]
ds_read_b128 v[56:59],v2 offset:27648
v_mul_f32 v37,v37,v77
v_mfma_f32_16x16x32_bf16 v[32:35],a[36:39],v[60:63],v[32:35]
v_rcp_f32 v79,v79
v_mfma_f32_16x16x32_bf16 v[28:31],a[40:43],v[64:67],v[28:31]
ds_read_b128 v[60:63],v2 offset:28672
v_mul_f32 v38,v38,v78
v_mfma_f32_16x16x32_bf16 v[32:35],a[44:47],v[64:67],v[32:35]
v_mul_f32 v39,v39,v79
v_cvt_pk_bf16_f32 v84,v36,v37
v_mfma_f32_16x16x32_bf16 v[28:31],a[48:51],v[68:71],v[28:31]
ds_read_b128 v[64:67],v2 offset:29696
v_cvt_pk_bf16_f32 v85,v38,v39
v_mfma_f32_16x16x32_bf16 v[32:35],a[52:55],v[68:71],v[32:35]
v_fma_f32 v40,-v96,v24,v40
v_fma_f32 v41,-v96,v25,v41
v_mfma_f32_16x16x32_bf16 v[28:31],a[56:59],v[72:75],v[28:31]
ds_read_b128 v[68:71],v2 offset:30720
v_fma_f32 v42,-v96,v26,v42
v_mfma_f32_16x16x32_bf16 v[32:35],a[60:63],v[72:75],v[32:35]
s_waitcnt lgkmcnt(3)
v_fma_f32 v43,-v96,v27,v43
v_mul_f32 v80,v40,v40
v_mfma_f32_16x16x32_bf16 v[28:31],a[192:195],v[44:47],v[28:31]
ds_read_b128 v[72:75],v2 offset:31744
v_mul_f32 v81,v41,v41)"
R"(
v_mfma_f32_16x16x32_bf16 v[32:35],a[196:199],v[44:47],v[32:35]
v_mul_f32 v82,v42,v42
v_mul_f32 v83,v43,v43
v_mfma_f32_16x16x32_bf16 v[104:107],v[100:103],v[44:47],v[104:107]
v_fma_f32 v80,v98,v80,v97
v_mul_f32 v80,v40,v80
v_mfma_f32_16x16x32_bf16 v[108:111],v[44:47],v[44:47],v[108:111]
v_exp_f32 v80,v80
v_mfma_f32_16x16x32_bf16 v[28:31],a[200:203],v[48:51],v[28:31]
ds_read_b128 v[44:47],v2 offset:16384
v_fma_f32 v80,v80,v99,v99
v_mfma_f32_16x16x32_bf16 v[32:35],a[204:207],v[48:51],v[32:35]
v_rcp_f32 v80,v80
v_mfma_f32_16x16x32_bf16 v[104:107],v[100:103],v[48:51],v[104:107]
s_nop 0
v_fma_f32 v81,v98,v81,v97
v_mul_f32 v81,v41,v81
v_mfma_f32_16x16x32_bf16 v[108:111],v[48:51],v[48:51],v[108:111]
s_barrier
v_mfma_f32_16x16x32_bf16 v[28:31],a[208:211],v[52:55],v[28:31]
ds_read_b128 v[48:51],v2 offset:17408
ds_read_b32 v16,v7 offset:0
v_mfma_f32_16x16x32_bf16 v[32:35],a[212:215],v[52:55],v[32:35]
v_exp_f32 v81,v81
v_mfma_f32_16x16x32_bf16 v[104:107],v[100:103],v[52:55],v[104:107]
v_fma_f32 v81,v81,v99,v99
v_fma_f32 v82,v98,v82,v97
v_mfma_f32_16x16x32_bf16 v[108:111],v[52:55],v[52:55],v[108:111]
v_rcp_f32 v81,v81
v_mfma_f32_16x16x32_bf16 v[28:31],a[216:219],v[56:59],v[28:31]
s_waitcnt lgkmcnt(0)
ds_read_b128 v[52:55],v2 offset:18432
v_mul_f32 v82,v42,v82
v_mfma_f32_16x16x32_bf16 v[32:35],a[220:223],v[56:59],v[32:35]
v_cmp_eq_u32 vcc,s60,v16
v_exp_f32 v82,v82
v_fma_f32 v83,v98,v83,v97
v_mfma_f32_16x16x32_bf16 v[104:107],v[100:103],v[56:59],v[104:107]
s_cselect_b32 vcc_lo,-1,vcc_lo
v_mul_f32 v83,v43,v83
v_fma_f32 v82,v82,v99,v99
v_mfma_f32_16x16x32_bf16 v[108:111],v[56:59],v[56:59],v[108:111]
v_exp_f32 v83,v83
v_mfma_f32_16x16x32_bf16 v[28:31],a[224:227],v[60:63],v[28:31]
s_cmp_eq_u32 vcc_lo,-1
ds_read_b128 v[56:59],v2 offset:19456
v_fma_f32 v83,v83,v99,v99
v_mfma_f32_16x16x32_bf16 v[32:35],a[228:231],v[60:63],v[32:35]
s_cselect_b32 s67,s86,s59
v_rcp_f32 v82,v82
v_mfma_f32_16x16x32_bf16 v[28:31],a[232:235],v[64:67],v[28:31]
buffer_load_dwordx4 v0,s[36:39],s67 offen offset:0 sc1 lds
v_cndmask_b32 v112,v108,v109,s[82:83]
v_cndmask_b32 v113,v110,v111,s[82:83]
v_cndmask_b32 v112,v112,v113,s[84:85]
v_cndmask_b32 v112,v104,v112,s[98:99]
v_mfma_f32_16x16x32_bf16 v[32:35],a[236:239],v[64:67],v[32:35]
s_xor_b32 s63,s63,65536
ds_read_b128 v[60:63],v2 offset:20480
ds_write_b32 v13,v112 offset:0
v_mfma_f32_16x16x32_bf16 v[28:31],a[240:243],v[68:71],v[28:31]
s_sub_u32 s69,s67,1048576
ds_read_b128 v[64:67],v2 offset:21504
v_mul_f32 v40,v40,v80
v_mfma_f32_16x16x32_bf16 v[32:35],a[244:247],v[68:71],v[32:35]
s_cmp_lt_u32 s69,2097152
v_rcp_f32 v83,v83
v_mfma_f32_16x16x32_bf16 v[28:31],a[248:251],v[72:75],v[28:31]
buffer_load_dwordx4 v0,s[36:39],s67 offen offset:1024 sc1 lds
v_mfma_f32_16x16x32_bf16 v[32:35],a[252:255],v[72:75],v[32:35]
s_waitcnt lgkmcnt(3)
ds_read_b128 v[68:71],v2 offset:22528
v_mul_f32 v41,v41,v81
v_mfma_f32_16x16x32_bf16 v[28:31],a[128:131],v[44:47],v[28:31]
s_waitcnt vmcnt(7) lgkmcnt(2)
ds_read_b128 v[72:75],v2 offset:23552
v_mul_f32 v42,v42,v82
v_mfma_f32_16x16x32_bf16 v[32:35],a[132:135],v[44:47],v[32:35]
s_barrier
v_mfma_f32_16x16x32_bf16 v[28:31],a[136:139],v[48:51],v[28:31]
s_cselect_b32 s89,3145728,s89
ds_read_b128 v[116:119],v14 offset:0
ds_read_b128 v[120:123],v14 offset:16
v_mfma_f32_16x16x32_bf16 v[32:35],a[140:143],v[48:51],v[32:35]
s_sub_u32 s69,s67,3145728
ds_read_b128 v[44:47],v2 offset:40960
v_mul_f32 v43,v43,v83
v_mfma_f32_16x16x32_bf16 v[28:31],a[144:147],v[52:55],v[28:31]
buffer_load_dwordx4 v0,s[36:39],s67 offen offset:2048 sc1 lds
v_mfma_f32_16x16x32_bf16 v[32:35],a[148:151],v[52:55],v[32:35]
s_cmp_lt_u32 s69,1048576
ds_read_b128 v[48:51],v2 offset:41984
v_cvt_pk_bf16_f32 v86,v40,v41
v_mfma_f32_16x16x32_bf16 v[28:31],a[152:155],v[56:59],v[28:31]
s_waitcnt lgkmcnt(2)
ds_read_b128 v[52:55],v2 offset:43008
v_cvt_pk_bf16_f32 v87,v42,v43
v_mfma_f32_16x16x32_bf16 v[32:35],a[156:159],v[56:59],v[32:35]
v_add_f32 v116,v116,v117
v_add_f32 v118,v118,v119
v_mfma_f32_16x16x32_bf16 v[28:31],a[160:163],v[60:63],v[28:31]
buffer_load_dwordx4 v0,s[36:39],s67 offen offset:3072 sc1 lds
v_mfma_f32_16x16x32_bf16 v[32:35],a[164:167],v[60:63],v[32:35]
s_cselect_b32 s89,4194304,s89
ds_read_b128 v[56:59],v2 offset:44032
v_add_f32 v116,v116,v118
v_mfma_f32_16x16x32_bf16 v[28:31],a[168:171],v[64:67],v[28:31]
s_add_u32 s53,s52,s55
ds_read_b128 v[60:63],v2 offset:45056
v_mul_f32 v92,s75,v116
v_mfma_f32_16x16x32_bf16 v[32:35],a[172:175],v[64:67],v[32:35]
s_cmp_eq_u32 s53,s56
v_add_f32 v120,v120,v121
v_add_f32 v122,v122,v123
v_mfma_f32_16x16x32_bf16 v[28:31],a[176:179],v[68:71],v[28:31]
buffer_store_dwordx4 v[84:87],v3,s[36:39],s73 offen offset:0 sc0 sc1
v_add_f32 v120,v120,v122
v_mul_f32 v120,s75,v120
v_fma_f32 v120,-v92,v92,v120
v_max_f32 v120,0,v120
v_add_f32 v115,v15,v120
v_fma_f32 v120,v15,v115,v120
v_rsq_f32 v115,v115
v_mfma_f32_16x16x32_bf16 v[32:35],a[180:183],v[68:71],v[32:35]
s_cselect_b32 s53,s57,s53
ds_read_b128 v[64:67],v2 offset:46080
v_rsq_f32 v116,v120
v_mul_f32_e64 v114,-v92,v115
v_mfma_f32_16x16x32_bf16 v[28:31],a[184:187],v[72:75],v[28:31]
s_waitcnt lgkmcnt(2)
s_cmp_eq_u32 s53,s58
s_cselect_b32 s53,s59,s53
ds_read_b128 v[68:71],v2 offset:47104
buffer_store_dwordx2 v[114:115],v12,s[36:39],s65 offen offset:0 sc0 sc1
v_mul_f32 v93,s76,v116
v_mul_f32 v94,v116,v116
v_mul_f32 v95,v120,v116
v_mul_f32 v94,s77,v94
v_mul_f32 v94,v94,v93
s_cmp_ge_u32 s66,s59
v_mfma_f32_16x16x32_bf16 v[32:35],a[188:191],v[72:75],v[32:35]
s_cbranch_scc1 Lg1_slow1_%=
Lg1_a1w2_%=:
v_mfma_f32_16x16x32_bf16 v[36:39],a[64:67],v[44:47],0
s_add_u32 m0,s62,16384
ds_read_b128 v[72:75],v2 offset:48128
s_lshr_b32 s73,s48,0
v_mfma_f32_16x16x32_bf16 v[40:43],a[68:71],v[44:47],0
s_lshr_b32 s65,s49,6
s_lshr_b32 s72,s67,6
s_lshr_b32 s71,s62,2
v_mfma_f32_16x16x32_bf16 v[104:107],v[100:103],v[44:47],0
s_cmp_lt_u32 s67,4194304
s_cselect_b32 s72,s59,s72
s_mov_b64 s[44:45],s[46:47]
v_mfma_f32_16x16x32_bf16 v[108:111],v[44:47],v[44:47],0
buffer_load_dwordx4 v1,s[36:39],s67 offen offset:0 sc1 lds
v_mfma_f32_16x16x32_bf16 v[36:39],a[72:75],v[48:51],v[36:39]
s_mov_b64 s[46:47],s[48:49]
ds_read_b128 v[44:47],v2 offset:32768
v_fma_f32 v28,-v92,v20,v28
v_mfma_f32_16x16x32_bf16 v[40:43],a[76:79],v[48:51],v[40:43]
s_mov_b64 s[48:49],s[50:51]
v_fma_f32 v29,-v92,v21,v29
v_fma_f32 v30,-v92,v22,v30
v_mfma_f32_16x16x32_bf16 v[104:107],v[100:103],v[48:51],v[104:107]
v_fma_f32 v31,-v92,v23,v31
v_mul_f32 v76,v28,v28
v_mfma_f32_16x16x32_bf16 v[108:111],v[48:51],v[48:51],v[108:111]
buffer_load_dwordx4 v1,s[36:39],s67 offen offset:1024 sc1 lds
v_mfma_f32_16x16x32_bf16 v[36:39],a[80:83],v[52:55],v[36:39]
ds_read_b128 v[48:51],v2 offset:33792
v_mul_f32 v77,v29,v29
v_mfma_f32_16x16x32_bf16 v[40:43],a[84:87],v[52:55],v[40:43]
v_mul_f32 v78,v30,v30
v_mul_f32 v79,v31,v31
v_mfma_f32_16x16x32_bf16 v[104:107],v[100:103],v[52:55],v[104:107]
v_fma_f32 v76,v94,v76,v93
v_mul_f32 v76,v28,v76
v_mfma_f32_16x16x32_bf16 v[108:111],v[52:55],v[52:55],v[108:111]
buffer_load_dwordx4 v1,s[36:39],s67 offen offset:2048 sc1 lds
v_mfma_f32_16x16x32_bf16 v[36:39],a[88:91],v[56:59],v[36:39]
ds_read_b128 v[52:55],v2 offset:34816
v_fma_f32 v77,v94,v77,v93
v_mfma_f32_16x16x32_bf16 v[40:43],a[92:95],v[56:59],v[40:43]
v_exp_f32 v76,v76
v_mfma_f32_16x16x32_bf16 v[104:107],v[100:103],v[56:59],v[104:107]
v_mul_f32 v77,v29,v77
v_fma_f32 v76,v76,v95,v95
v_mfma_f32_16x16x32_bf16 v[108:111],v[56:59],v[56:59],v[108:111]
s_waitcnt lgkmcnt(3)
buffer_load_dwordx4 v1,s[36:39],s67 offen offset:3072 sc1 lds
v_mfma_f32_16x16x32_bf16 v[36:39],a[96:99],v[60:63],v[36:39]
s_add_u32 m0,s71,s88
ds_read_b128 v[56:59],v2 offset:35840
v_fma_f32 v78,v94,v78,v93
v_mfma_f32_16x16x32_bf16 v[40:43],a[100:103],v[60:63],v[40:43]
buffer_load_dword v10,s[36:39],s72 offen offset:0 sc1 lds
v_mfma_f32_16x16x32_bf16 v[36:39],a[104:107],v[64:67],v[36:39]
s_xor_b32 s87,s63,65536)"
R"(
ds_read_b128 v[60:63],v2 offset:36864
v_fma_f32 v79,v94,v79,v93
v_mfma_f32_16x16x32_bf16 v[40:43],a[108:111],v[64:67],v[40:43]
s_cmp_ge_u32 s67,s59
v_exp_f32 v77,v77
v_mfma_f32_16x16x32_bf16 v[36:39],a[112:115],v[68:71],v[36:39]
s_cselect_b32 s86,s50,s51
ds_read_b128 v[64:67],v2 offset:37888
v_mul_f32 v78,v30,v78
v_mfma_f32_16x16x32_bf16 v[40:43],a[116:119],v[68:71],v[40:43]
s_cselect_b32 s87,s87,s64
v_exp_f32 v78,v78
v_mfma_f32_16x16x32_bf16 v[36:39],a[120:123],v[72:75],v[36:39]
s_mov_b32 s62,s87
ds_read_b128 v[68:71],v2 offset:38912
v_mul_f32 v79,v31,v79
v_mfma_f32_16x16x32_bf16 v[40:43],a[124:127],v[72:75],v[40:43]
s_waitcnt lgkmcnt(3)
v_exp_f32 v79,v79
v_mfma_f32_16x16x32_bf16 v[36:39],a[0:3],v[44:47],v[36:39]
s_waitcnt vmcnt(10)
ds_read_b128 v[72:75],v2 offset:39936
v_fma_f32 v77,v77,v95,v95
v_mfma_f32_16x16x32_bf16 v[40:43],a[4:7],v[44:47],v[40:43]
s_barrier
v_mfma_f32_16x16x32_bf16 v[36:39],a[8:11],v[48:51],v[36:39]
s_cmp_lt_u32 s86,s89
ds_read_b128 v[44:47],v2 offset:57344
v_fma_f32 v78,v78,v95,v95
v_mfma_f32_16x16x32_bf16 v[40:43],a[12:15],v[48:51],v[40:43]
s_mov_b32 m0,s62
v_rcp_f32 v76,v76
v_mfma_f32_16x16x32_bf16 v[36:39],a[16:19],v[52:55],v[36:39]
s_mov_b64 s[50:51],s[52:53]
ds_read_b128 v[48:51],v2 offset:58368
v_fma_f32 v79,v79,v95,v95
v_mfma_f32_16x16x32_bf16 v[40:43],a[20:23],v[52:55],v[40:43]
v_rcp_f32 v77,v77
v_mfma_f32_16x16x32_bf16 v[36:39],a[24:27],v[56:59],v[36:39]
ds_read_b128 v[52:55],v2 offset:59392
v_mul_f32 v28,v28,v76
v_mfma_f32_16x16x32_bf16 v[40:43],a[28:31],v[56:59],v[40:43]
s_waitcnt lgkmcnt(3)
v_rcp_f32 v78,v78
v_mfma_f32_16x16x32_bf16 v[36:39],a[32:35],v[60:63],v[36:39]
ds_read_b128 v[56:59],v2 offset:60416
v_mul_f32 v29,v29,v77
v_mfma_f32_16x16x32_bf16 v[40:43],a[36:39],v[60:63],v[40:43]
v_rcp_f32 v79,v79
v_mfma_f32_16x16x32_bf16 v[36:39],a[40:43],v[64:67],v[36:39]
ds_read_b128 v[60:63],v2 offset:61440
v_mul_f32 v30,v30,v78
v_mfma_f32_16x16x32_bf16 v[40:43],a[44:47],v[64:67],v[40:43]
v_mul_f32 v31,v31,v79
v_cvt_pk_bf16_f32 v84,v28,v29
v_mfma_f32_16x16x32_bf16 v[36:39],a[48:51],v[68:71],v[36:39]
ds_read_b128 v[64:67],v2 offset:62464
v_cvt_pk_bf16_f32 v85,v30,v31
v_mfma_f32_16x16x32_bf16 v[40:43],a[52:55],v[68:71],v[40:43]
v_fma_f32 v32,-v92,v24,v32
v_fma_f32 v33,-v92,v25,v33
v_mfma_f32_16x16x32_bf16 v[36:39],a[56:59],v[72:75],v[36:39]
ds_read_b128 v[68:71],v2 offset:63488
v_fma_f32 v34,-v92,v26,v34
v_mfma_f32_16x16x32_bf16 v[40:43],a[60:63],v[72:75],v[40:43]
s_waitcnt lgkmcnt(3)
v_fma_f32 v35,-v92,v27,v35
v_mul_f32 v80,v32,v32
v_mfma_f32_16x16x32_bf16 v[36:39],a[192:195],v[44:47],v[36:39]
ds_read_b128 v[72:75],v2 offset:64512
v_mul_f32 v81,v33,v33
v_mfma_f32_16x16x32_bf16 v[40:43],a[196:199],v[44:47],v[40:43]
v_mul_f32 v82,v34,v34
v_mul_f32 v83,v35,v35
v_mfma_f32_16x16x32_bf16 v[104:107],v[100:103],v[44:47],v[104:107]
v_fma_f32 v80,v94,v80,v93
v_mul_f32 v80,v32,v80
v_mfma_f32_16x16x32_bf16 v[108:111],v[44:47],v[44:47],v[108:111]
v_exp_f32 v80,v80
v_mfma_f32_16x16x32_bf16 v[36:39],a[200:203],v[48:51],v[36:39]
ds_read_b128 v[44:47],v2 offset:49152
v_fma_f32 v80,v80,v95,v95
v_mfma_f32_16x16x32_bf16 v[40:43],a[204:207],v[48:51],v[40:43]
v_rcp_f32 v80,v80
v_mfma_f32_16x16x32_bf16 v[104:107],v[100:103],v[48:51],v[104:107]
s_nop 0
v_fma_f32 v81,v94,v81,v93
v_mul_f32 v81,v33,v81
v_mfma_f32_16x16x32_bf16 v[108:111],v[48:51],v[48:51],v[108:111]
s_barrier
v_mfma_f32_16x16x32_bf16 v[36:39],a[208:211],v[52:55],v[36:39]
ds_read_b128 v[48:51],v2 offset:50176
ds_read_b32 v16,v7 offset:256
v_mfma_f32_16x16x32_bf16 v[40:43],a[212:215],v[52:55],v[40:43]
v_exp_f32 v81,v81
v_mfma_f32_16x16x32_bf16 v[104:107],v[100:103],v[52:55],v[104:107]
v_fma_f32 v81,v81,v95,v95
v_fma_f32 v82,v94,v82,v93
v_mfma_f32_16x16x32_bf16 v[108:111],v[52:55],v[52:55],v[108:111]
v_rcp_f32 v81,v81
v_mfma_f32_16x16x32_bf16 v[36:39],a[216:219],v[56:59],v[36:39]
s_waitcnt lgkmcnt(0)
ds_read_b128 v[52:55],v2 offset:51200
v_mul_f32 v82,v34,v82
v_mfma_f32_16x16x32_bf16 v[40:43],a[220:223],v[56:59],v[40:43]
v_cmp_eq_u32 vcc,s60,v16
v_exp_f32 v82,v82
v_fma_f32 v83,v94,v83,v93
v_mfma_f32_16x16x32_bf16 v[104:107],v[100:103],v[56:59],v[104:107]
s_cselect_b32 vcc_lo,-1,vcc_lo
v_mul_f32 v83,v35,v83
v_fma_f32 v82,v82,v95,v95
v_mfma_f32_16x16x32_bf16 v[108:111],v[56:59],v[56:59],v[108:111]
v_exp_f32 v83,v83
v_mfma_f32_16x16x32_bf16 v[36:39],a[224:227],v[60:63],v[36:39]
s_cmp_eq_u32 vcc_lo,-1
ds_read_b128 v[56:59],v2 offset:52224
v_fma_f32 v83,v83,v95,v95
v_mfma_f32_16x16x32_bf16 v[40:43],a[228:231],v[60:63],v[40:43]
s_cselect_b32 s66,s86,s59
v_rcp_f32 v82,v82
v_mfma_f32_16x16x32_bf16 v[36:39],a[232:235],v[64:67],v[36:39]
buffer_load_dwordx4 v0,s[36:39],s66 offen offset:0 sc1 lds
v_cndmask_b32 v112,v108,v109,s[82:83]
v_cndmask_b32 v113,v110,v111,s[82:83]
v_cndmask_b32 v112,v112,v113,s[84:85]
v_cndmask_b32 v112,v104,v112,s[98:99]
v_mfma_f32_16x16x32_bf16 v[40:43],a[236:239],v[64:67],v[40:43]
s_xor_b32 s64,s64,65536
ds_read_b128 v[60:63],v2 offset:53248
ds_write_b32 v13,v112 offset:2048
v_mfma_f32_16x16x32_bf16 v[36:39],a[240:243],v[68:71],v[36:39]
s_sub_u32 s69,s66,1048576
ds_read_b128 v[64:67],v2 offset:54272
v_mul_f32 v32,v32,v80
v_mfma_f32_16x16x32_bf16 v[40:43],a[244:247],v[68:71],v[40:43]
s_cmp_lt_u32 s69,2097152
v_rcp_f32 v83,v83
v_mfma_f32_16x16x32_bf16 v[36:39],a[248:251],v[72:75],v[36:39]
buffer_load_dwordx4 v0,s[36:39],s66 offen offset:1024 sc1 lds
v_mfma_f32_16x16x32_bf16 v[40:43],a[252:255],v[72:75],v[40:43]
s_waitcnt lgkmcnt(3)
ds_read_b128 v[68:71],v2 offset:55296
v_mul_f32 v33,v33,v81
v_mfma_f32_16x16x32_bf16 v[36:39],a[128:131],v[44:47],v[36:39]
s_waitcnt vmcnt(7) lgkmcnt(2)
ds_read_b128 v[72:75],v2 offset:56320
v_xor_b32 v2,65536,v2
v_mfma_f32_16x16x32_bf16 v[40:43],a[132:135],v[44:47],v[40:43]
s_barrier
v_mfma_f32_16x16x32_bf16 v[36:39],a[136:139],v[48:51],v[36:39]
s_cselect_b32 s89,3145728,s89
ds_read_b128 v[116:119],v14 offset:2048
ds_read_b128 v[120:123],v14 offset:2064
v_mfma_f32_16x16x32_bf16 v[40:43],a[140:143],v[48:51],v[40:43]
s_sub_u32 s69,s66,3145728
ds_read_b128 v[44:47],v2 offset:8192
v_mul_f32 v34,v34,v82
v_mfma_f32_16x16x32_bf16 v[36:39],a[144:147],v[52:55],v[36:39]
buffer_load_dwordx4 v0,s[36:39],s66 offen offset:2048 sc1 lds
v_mfma_f32_16x16x32_bf16 v[40:43],a[148:151],v[52:55],v[40:43]
s_cmp_lt_u32 s69,1048576
ds_read_b128 v[48:51],v2 offset:9216
v_mul_f32 v35,v35,v83
v_mfma_f32_16x16x32_bf16 v[36:39],a[152:155],v[56:59],v[36:39]
s_waitcnt lgkmcnt(2)
ds_read_b128 v[52:55],v2 offset:10240
v_cvt_pk_bf16_f32 v86,v32,v33
v_mfma_f32_16x16x32_bf16 v[40:43],a[156:159],v[56:59],v[40:43]
v_add_f32 v116,v116,v117
v_add_f32 v118,v118,v119
v_mfma_f32_16x16x32_bf16 v[36:39],a[160:163],v[60:63],v[36:39]
buffer_load_dwordx4 v0,s[36:39],s66 offen offset:3072 sc1 lds
v_mfma_f32_16x16x32_bf16 v[40:43],a[164:167],v[60:63],v[40:43]
s_cselect_b32 s89,4194304,s89
ds_read_b128 v[56:59],v2 offset:11264
v_add_f32 v116,v116,v118
v_mfma_f32_16x16x32_bf16 v[36:39],a[168:171],v[64:67],v[36:39]
s_add_u32 s74,s53,s55
ds_read_b128 v[60:63],v2 offset:12288
v_mul_f32 v96,s75,v116
v_mfma_f32_16x16x32_bf16 v[40:43],a[172:175],v[64:67],v[40:43]
s_cmp_eq_u32 s74,s56
v_cvt_pk_bf16_f32 v87,v34,v35
v_add_f32 v120,v120,v121
v_mfma_f32_16x16x32_bf16 v[36:39],a[176:179],v[68:71],v[36:39]
buffer_store_dwordx4 v[84:87],v3,s[36:39],s73 offen offset:0 sc0 sc1
v_add_f32 v122,v122,v123
v_add_f32 v120,v120,v122
v_mul_f32 v120,s75,v120
v_fma_f32 v120,-v96,v96,v120
v_max_f32 v120,0,v120
v_add_f32 v115,v15,v120
v_fma_f32 v120,v15,v115,v120
v_rsq_f32 v115,v115
v_mfma_f32_16x16x32_bf16 v[40:43],a[180:183],v[68:71],v[40:43]
s_cselect_b32 s74,s57,s74
ds_read_b128 v[64:67],v2 offset:13312
v_rsq_f32 v116,v120
v_mul_f32_e64 v114,-v96,v115)"
R"(
v_mfma_f32_16x16x32_bf16 v[36:39],a[184:187],v[72:75],v[36:39]
s_waitcnt lgkmcnt(2)
s_cmp_eq_u32 s74,s58
s_cselect_b32 s74,s59,s74
ds_read_b128 v[68:71],v2 offset:14336
buffer_store_dwordx2 v[114:115],v12,s[36:39],s65 offen offset:0 sc0 sc1
v_xor_b32 v8,16384,v8
v_mul_f32 v97,s76,v116
v_mul_f32 v98,v116,v116
v_mul_f32 v99,v120,v116
v_mul_f32 v98,s77,v98
v_mul_f32 v98,v98,v97
s_mov_b32 s52,s74
s_cmp_lt_u32 s67,4194304
v_mfma_f32_16x16x32_bf16 v[40:43],a[188:191],v[72:75],v[40:43]
s_cbranch_scc1 Lg1_a0w2_%=
s_cmp_ge_u32 s48,4194304
s_cselect_b64 s[90:91],s[94:95],s[90:91]
s_cselect_b64 s[92:93],s[96:97],s[92:93]
s_cmp_lt_u32 s67,s59
s_cbranch_scc1 Lg1_d0w2_%=
s_branch Lg1_slow0_%=
Lg1_a0w3_%=:
v_mfma_f32_16x16x32_bf16 v[28:31],a[96:99],v[44:47],0
s_add_u32 m0,s62,16384
ds_read_b128 v[72:75],v2 offset:11264
s_lshr_b32 s73,s47,0
v_mfma_f32_16x16x32_bf16 v[32:35],a[100:103],v[44:47],0
s_lshr_b32 s72,s66,6
s_lshr_b32 s71,s62,2
s_cmp_lt_u32 s66,4194304
v_mfma_f32_16x16x32_bf16 v[104:107],v[100:103],v[44:47],0
s_cselect_b32 s72,s59,s72
v_mfma_f32_16x16x32_bf16 v[108:111],v[44:47],v[44:47],0
v_fma_f32 v36,-v96,v20,v36
v_fma_f32 v37,-v96,v21,v37
v_mfma_f32_16x16x32_bf16 v[28:31],a[104:107],v[48:51],v[28:31]
buffer_load_dwordx4 v1,s[36:39],s66 offen offset:0 sc1 lds
v_mfma_f32_16x16x32_bf16 v[32:35],a[108:111],v[48:51],v[32:35]
ds_read_b128 v[44:47],v2 offset:4096
v_fma_f32 v38,-v96,v22,v38
v_mfma_f32_16x16x32_bf16 v[104:107],v[100:103],v[48:51],v[104:107]
v_fma_f32 v39,-v96,v23,v39
v_mul_f32 v76,v36,v36
v_mfma_f32_16x16x32_bf16 v[108:111],v[48:51],v[48:51],v[108:111]
v_mul_f32 v77,v37,v37
v_mul_f32 v78,v38,v38
v_mfma_f32_16x16x32_bf16 v[28:31],a[112:115],v[52:55],v[28:31]
buffer_load_dwordx4 v1,s[36:39],s66 offen offset:1024 sc1 lds
v_mfma_f32_16x16x32_bf16 v[32:35],a[116:119],v[52:55],v[32:35]
ds_read_b128 v[48:51],v2 offset:5120
v_mul_f32 v79,v39,v39
v_mfma_f32_16x16x32_bf16 v[104:107],v[100:103],v[52:55],v[104:107]
v_fma_f32 v76,v98,v76,v97
v_mul_f32 v76,v36,v76
v_mfma_f32_16x16x32_bf16 v[108:111],v[52:55],v[52:55],v[108:111]
v_exp_f32 v76,v76
v_mfma_f32_16x16x32_bf16 v[28:31],a[120:123],v[56:59],v[28:31]
buffer_load_dwordx4 v1,s[36:39],s66 offen offset:2048 sc1 lds
v_mfma_f32_16x16x32_bf16 v[32:35],a[124:127],v[56:59],v[32:35]
ds_read_b128 v[52:55],v2 offset:6144
v_fma_f32 v76,v76,v99,v99
v_mfma_f32_16x16x32_bf16 v[104:107],v[100:103],v[56:59],v[104:107]
v_rcp_f32 v76,v76
v_mfma_f32_16x16x32_bf16 v[108:111],v[56:59],v[56:59],v[108:111]
s_waitcnt lgkmcnt(3)
v_fma_f32 v77,v98,v77,v97
v_mul_f32 v77,v37,v77
v_mfma_f32_16x16x32_bf16 v[28:31],a[64:67],v[60:63],v[28:31]
buffer_load_dwordx4 v1,s[36:39],s66 offen offset:3072 sc1 lds
v_mfma_f32_16x16x32_bf16 v[32:35],a[68:71],v[60:63],v[32:35]
s_add_u32 m0,s71,s88
ds_read_b128 v[56:59],v2 offset:7168
v_fma_f32 v78,v98,v78,v97
v_mfma_f32_16x16x32_bf16 v[28:31],a[72:75],v[64:67],v[28:31]
buffer_load_dword v10,s[36:39],s72 offen offset:0 sc1 lds
v_mfma_f32_16x16x32_bf16 v[32:35],a[76:79],v[64:67],v[32:35]
s_xor_b32 s87,s64,65536
ds_read_b128 v[60:63],v2 offset:0
v_mul_f32 v78,v38,v78
v_mfma_f32_16x16x32_bf16 v[28:31],a[80:83],v[68:71],v[28:31]
s_cmp_ge_u32 s66,s59
ds_read_b128 v[64:67],v2 offset:1024
v_fma_f32 v79,v98,v79,v97
v_mfma_f32_16x16x32_bf16 v[32:35],a[84:87],v[68:71],v[32:35]
s_cselect_b32 s86,s49,s50
v_exp_f32 v77,v77
v_mfma_f32_16x16x32_bf16 v[28:31],a[88:91],v[72:75],v[28:31]
s_cselect_b32 s87,s87,s63
ds_read_b128 v[68:71],v2 offset:2048
v_mul_f32 v79,v39,v79
v_mfma_f32_16x16x32_bf16 v[32:35],a[92:95],v[72:75],v[32:35]
s_waitcnt lgkmcnt(3)
v_exp_f32 v78,v78
v_mfma_f32_16x16x32_bf16 v[28:31],a[32:35],v[44:47],v[28:31]
s_waitcnt vmcnt(11)
ds_read_b128 v[72:75],v2 offset:3072
v_fma_f32 v77,v77,v99,v99
v_mfma_f32_16x16x32_bf16 v[32:35],a[36:39],v[44:47],v[32:35]
s_barrier
v_mfma_f32_16x16x32_bf16 v[28:31],a[40:43],v[48:51],v[28:31]
s_mov_b32 s62,s87
ds_read_b128 v[44:47],v2 offset:28672
v_fma_f32 v78,v78,v99,v99
v_mfma_f32_16x16x32_bf16 v[32:35],a[44:47],v[48:51],v[32:35]
s_cmp_lt_u32 s86,s89
v_exp_f32 v79,v79
v_mfma_f32_16x16x32_bf16 v[28:31],a[48:51],v[52:55],v[28:31]
s_mov_b32 m0,s62
ds_read_b128 v[48:51],v2 offset:29696
v_fma_f32 v79,v79,v99,v99
v_mfma_f32_16x16x32_bf16 v[32:35],a[52:55],v[52:55],v[32:35]
v_rcp_f32 v77,v77
v_mfma_f32_16x16x32_bf16 v[28:31],a[56:59],v[56:59],v[28:31]
ds_read_b128 v[52:55],v2 offset:30720
v_mul_f32 v36,v36,v76
v_mfma_f32_16x16x32_bf16 v[32:35],a[60:63],v[56:59],v[32:35]
s_waitcnt lgkmcnt(3)
v_rcp_f32 v78,v78
v_mfma_f32_16x16x32_bf16 v[28:31],a[0:3],v[60:63],v[28:31]
ds_read_b128 v[56:59],v2 offset:31744
v_mul_f32 v37,v37,v77
v_mfma_f32_16x16x32_bf16 v[32:35],a[4:7],v[60:63],v[32:35]
v_rcp_f32 v79,v79
v_mfma_f32_16x16x32_bf16 v[28:31],a[8:11],v[64:67],v[28:31]
ds_read_b128 v[60:63],v2 offset:24576
v_mul_f32 v38,v38,v78
v_mfma_f32_16x16x32_bf16 v[32:35],a[12:15],v[64:67],v[32:35]
v_mul_f32 v39,v39,v79
v_cvt_pk_bf16_f32 v84,v36,v37
v_mfma_f32_16x16x32_bf16 v[28:31],a[16:19],v[68:71],v[28:31]
ds_read_b128 v[64:67],v2 offset:25600
v_cvt_pk_bf16_f32 v85,v38,v39
v_mfma_f32_16x16x32_bf16 v[32:35],a[20:23],v[68:71],v[32:35]
v_fma_f32 v40,-v96,v24,v40
v_fma_f32 v41,-v96,v25,v41
v_mfma_f32_16x16x32_bf16 v[28:31],a[24:27],v[72:75],v[28:31]
ds_read_b128 v[68:71],v2 offset:26624
v_fma_f32 v42,-v96,v26,v42
v_mfma_f32_16x16x32_bf16 v[32:35],a[28:31],v[72:75],v[32:35]
s_waitcnt lgkmcnt(3)
v_fma_f32 v43,-v96,v27,v43
v_mul_f32 v80,v40,v40
v_mfma_f32_16x16x32_bf16 v[28:31],a[224:227],v[44:47],v[28:31]
ds_read_b128 v[72:75],v2 offset:27648
v_mul_f32 v81,v41,v41
v_mfma_f32_16x16x32_bf16 v[32:35],a[228:231],v[44:47],v[32:35]
v_mul_f32 v82,v42,v42
v_mul_f32 v83,v43,v43
v_mfma_f32_16x16x32_bf16 v[104:107],v[100:103],v[44:47],v[104:107]
v_fma_f32 v80,v98,v80,v97
v_mul_f32 v80,v40,v80
v_mfma_f32_16x16x32_bf16 v[108:111],v[44:47],v[44:47],v[108:111]
v_exp_f32 v80,v80
v_mfma_f32_16x16x32_bf16 v[28:31],a[232:235],v[48:51],v[28:31]
ds_read_b128 v[44:47],v2 offset:20480
v_fma_f32 v80,v80,v99,v99
v_mfma_f32_16x16x32_bf16 v[32:35],a[236:239],v[48:51],v[32:35]
v_rcp_f32 v80,v80
v_mfma_f32_16x16x32_bf16 v[104:107],v[100:103],v[48:51],v[104:107]
s_nop 0
v_fma_f32 v81,v98,v81,v97
v_mul_f32 v81,v41,v81
v_mfma_f32_16x16x32_bf16 v[108:111],v[48:51],v[48:51],v[108:111]
s_barrier
v_mfma_f32_16x16x32_bf16 v[28:31],a[240:243],v[52:55],v[28:31]
ds_read_b128 v[48:51],v2 offset:21504
ds_read_b32 v16,v7 offset:0
v_mfma_f32_16x16x32_bf16 v[32:35],a[244:247],v[52:55],v[32:35]
v_exp_f32 v81,v81
v_mfma_f32_16x16x32_bf16 v[104:107],v[100:103],v[52:55],v[104:107]
v_fma_f32 v81,v81,v99,v99
v_fma_f32 v82,v98,v82,v97
v_mfma_f32_16x16x32_bf16 v[108:111],v[52:55],v[52:55],v[108:111]
v_rcp_f32 v81,v81
v_mfma_f32_16x16x32_bf16 v[28:31],a[248:251],v[56:59],v[28:31]
s_waitcnt lgkmcnt(0)
ds_read_b128 v[52:55],v2 offset:22528
v_mul_f32 v82,v42,v82
v_mfma_f32_16x16x32_bf16 v[32:35],a[252:255],v[56:59],v[32:35]
v_cmp_eq_u32 vcc,s60,v16
v_exp_f32 v82,v82
v_fma_f32 v83,v98,v83,v97
v_mfma_f32_16x16x32_bf16 v[104:107],v[100:103],v[56:59],v[104:107]
s_cselect_b32 vcc_lo,-1,vcc_lo
v_mul_f32 v83,v43,v83
v_fma_f32 v82,v82,v99,v99
v_mfma_f32_16x16x32_bf16 v[108:111],v[56:59],v[56:59],v[108:111]
v_exp_f32 v83,v83
v_mfma_f32_16x16x32_bf16 v[28:31],a[192:195],v[60:63],v[28:31]
s_cmp_eq_u32 vcc_lo,-1
ds_read_b128 v[56:59],v2 offset:23552
v_fma_f32 v83,v83,v99,v99
v_mfma_f32_16x16x32_bf16 v[32:35],a[196:199],v[60:63],v[32:35]
s_cselect_b32 s67,s86,s59
v_rcp_f32 v82,v82
v_mfma_f32_16x16x32_bf16 v[28:31],a[200:203],v[64:67],v[28:31]
s_lshr_b32 s70,s46,5
v_cndmask_b32 v112,v108,v109,s[82:83]
v_cndmask_b32 v113,v110,v111,s[82:83])"
R"(
v_cndmask_b32 v112,v112,v113,s[84:85]
v_cndmask_b32 v112,v104,v112,s[98:99]
v_mfma_f32_16x16x32_bf16 v[32:35],a[204:207],v[64:67],v[32:35]
s_xor_b32 s63,s63,65536
ds_read_b128 v[60:63],v2 offset:16384
ds_write_b32 v13,v112 offset:0
buffer_load_dwordx4 v0,s[36:39],s67 offen offset:0 sc1 lds
v_mfma_f32_16x16x32_bf16 v[28:31],a[208:211],v[68:71],v[28:31]
s_sub_u32 s69,s67,1048576
ds_read_b128 v[64:67],v2 offset:17408
v_mul_f32 v40,v40,v80
v_mfma_f32_16x16x32_bf16 v[32:35],a[212:215],v[68:71],v[32:35]
s_cmp_lt_u32 s69,2097152
v_rcp_f32 v83,v83
v_mfma_f32_16x16x32_bf16 v[28:31],a[216:219],v[72:75],v[28:31]
s_cselect_b32 s89,3145728,s89
ds_read_b128 v[68:71],v2 offset:18432
v_mul_f32 v41,v41,v81
v_mfma_f32_16x16x32_bf16 v[32:35],a[220:223],v[72:75],v[32:35]
s_waitcnt lgkmcnt(4)
buffer_load_dwordx4 v0,s[36:39],s67 offen offset:1024 sc1 lds
v_mfma_f32_16x16x32_bf16 v[28:31],a[160:163],v[44:47],v[28:31]
s_waitcnt vmcnt(7) lgkmcnt(2)
ds_read_b128 v[72:75],v2 offset:19456
v_mul_f32 v42,v42,v82
v_mfma_f32_16x16x32_bf16 v[32:35],a[164:167],v[44:47],v[32:35]
s_barrier
v_mfma_f32_16x16x32_bf16 v[28:31],a[168:171],v[48:51],v[28:31]
buffer_store_dword v5,v4,s[36:39],s70 offen offset:0 sc0 sc1
ds_read_b128 v[116:119],v14 offset:0
ds_read_b128 v[120:123],v14 offset:16
ds_read_b128 v[44:47],v2 offset:45056
v_mfma_f32_16x16x32_bf16 v[32:35],a[172:175],v[48:51],v[32:35]
s_sub_u32 s69,s67,3145728
v_mul_f32 v43,v43,v83
v_cvt_pk_bf16_f32 v86,v40,v41
v_mfma_f32_16x16x32_bf16 v[28:31],a[176:179],v[52:55],v[28:31]
s_cmp_lt_u32 s69,1048576
ds_read_b128 v[48:51],v2 offset:46080
v_cvt_pk_bf16_f32 v87,v42,v43
v_mfma_f32_16x16x32_bf16 v[32:35],a[180:183],v[52:55],v[32:35]
buffer_load_dwordx4 v0,s[36:39],s67 offen offset:2048 sc1 lds
v_mfma_f32_16x16x32_bf16 v[28:31],a[184:187],v[56:59],v[28:31]
s_waitcnt lgkmcnt(2)
ds_read_b128 v[52:55],v2 offset:47104
v_add_f32 v116,v116,v117
v_mfma_f32_16x16x32_bf16 v[32:35],a[188:191],v[56:59],v[32:35]
v_add_f32 v118,v118,v119
v_add_f32 v116,v116,v118
v_mfma_f32_16x16x32_bf16 v[28:31],a[128:131],v[60:63],v[28:31]
s_cselect_b32 s89,4194304,s89
ds_read_b128 v[56:59],v2 offset:48128
v_mul_f32 v92,s75,v116
v_mfma_f32_16x16x32_bf16 v[32:35],a[132:135],v[60:63],v[32:35]
buffer_load_dwordx4 v0,s[36:39],s67 offen offset:3072 sc1 lds
v_mfma_f32_16x16x32_bf16 v[28:31],a[136:139],v[64:67],v[28:31]
s_add_u32 s53,s52,s55
ds_read_b128 v[60:63],v2 offset:40960
v_add_f32 v120,v120,v121
v_mfma_f32_16x16x32_bf16 v[32:35],a[140:143],v[64:67],v[32:35]
s_cmp_eq_u32 s53,s56
v_add_f32 v122,v122,v123
v_add_f32 v120,v120,v122
v_mfma_f32_16x16x32_bf16 v[28:31],a[144:147],v[68:71],v[28:31]
s_cselect_b32 s53,s57,s53
ds_read_b128 v[64:67],v2 offset:41984
v_mul_f32 v120,s75,v120
v_mfma_f32_16x16x32_bf16 v[32:35],a[148:151],v[68:71],v[32:35]
buffer_store_dwordx4 v[84:87],v3,s[36:39],s73 offen offset:0 sc0 sc1
v_fma_f32 v120,-v92,v92,v120
v_max_f32 v120,0,v120
v_add_f32 v115,v15,v120
v_fma_f32 v120,v15,v115,v120
v_rsq_f32 v116,v120
v_mfma_f32_16x16x32_bf16 v[28:31],a[152:155],v[72:75],v[28:31]
s_waitcnt lgkmcnt(2)
s_cmp_eq_u32 s53,s58
s_cselect_b32 s53,s59,s53
ds_read_b128 v[68:71],v2 offset:43008
v_mul_f32 v93,s76,v116
v_mul_f32 v94,v116,v116
v_mul_f32 v95,v120,v116
v_mul_f32 v94,s77,v94
v_mul_f32 v94,v94,v93
s_cmp_ge_u32 s66,s59
v_mfma_f32_16x16x32_bf16 v[32:35],a[156:159],v[72:75],v[32:35]
s_cbranch_scc1 Lg1_slow1_%=
Lg1_a1w3_%=:
v_mfma_f32_16x16x32_bf16 v[36:39],a[96:99],v[44:47],0
s_add_u32 m0,s62,16384
ds_read_b128 v[72:75],v2 offset:44032
s_lshr_b32 s73,s48,0
v_mfma_f32_16x16x32_bf16 v[40:43],a[100:103],v[44:47],0
s_lshr_b32 s72,s67,6
s_lshr_b32 s71,s62,2
s_cmp_lt_u32 s67,4194304
v_mfma_f32_16x16x32_bf16 v[104:107],v[100:103],v[44:47],0
s_cselect_b32 s72,s59,s72
s_mov_b64 s[44:45],s[46:47]
v_mfma_f32_16x16x32_bf16 v[108:111],v[44:47],v[44:47],0
v_fma_f32 v28,-v92,v20,v28
v_fma_f32 v29,-v92,v21,v29
v_mfma_f32_16x16x32_bf16 v[36:39],a[104:107],v[48:51],v[36:39]
buffer_load_dwordx4 v1,s[36:39],s67 offen offset:0 sc1 lds
v_mfma_f32_16x16x32_bf16 v[40:43],a[108:111],v[48:51],v[40:43]
ds_read_b128 v[44:47],v2 offset:36864
v_fma_f32 v30,-v92,v22,v30
v_mfma_f32_16x16x32_bf16 v[104:107],v[100:103],v[48:51],v[104:107]
v_fma_f32 v31,-v92,v23,v31
v_mul_f32 v76,v28,v28
v_mfma_f32_16x16x32_bf16 v[108:111],v[48:51],v[48:51],v[108:111]
v_mul_f32 v77,v29,v29
v_mul_f32 v78,v30,v30
v_mul_f32 v79,v31,v31
v_mfma_f32_16x16x32_bf16 v[36:39],a[112:115],v[52:55],v[36:39]
buffer_load_dwordx4 v1,s[36:39],s67 offen offset:1024 sc1 lds
v_mfma_f32_16x16x32_bf16 v[40:43],a[116:119],v[52:55],v[40:43]
ds_read_b128 v[48:51],v2 offset:37888
v_fma_f32 v76,v94,v76,v93
v_mfma_f32_16x16x32_bf16 v[104:107],v[100:103],v[52:55],v[104:107]
v_mul_f32 v76,v28,v76
v_fma_f32 v77,v94,v77,v93
v_mfma_f32_16x16x32_bf16 v[108:111],v[52:55],v[52:55],v[108:111]
v_exp_f32 v76,v76
v_mfma_f32_16x16x32_bf16 v[36:39],a[120:123],v[56:59],v[36:39]
buffer_load_dwordx4 v1,s[36:39],s67 offen offset:2048 sc1 lds
ds_read_b128 v[52:55],v2 offset:38912
v_mfma_f32_16x16x32_bf16 v[40:43],a[124:127],v[56:59],v[40:43]
v_mul_f32 v77,v29,v77
v_fma_f32 v76,v76,v95,v95
v_mfma_f32_16x16x32_bf16 v[104:107],v[100:103],v[56:59],v[104:107]
v_exp_f32 v77,v77
v_mfma_f32_16x16x32_bf16 v[108:111],v[56:59],v[56:59],v[108:111]
s_waitcnt lgkmcnt(3)
v_rcp_f32 v76,v76
v_mfma_f32_16x16x32_bf16 v[36:39],a[64:67],v[60:63],v[36:39]
buffer_load_dwordx4 v1,s[36:39],s67 offen offset:3072 sc1 lds
v_mfma_f32_16x16x32_bf16 v[40:43],a[68:71],v[60:63],v[40:43]
s_add_u32 m0,s71,s88
ds_read_b128 v[56:59],v2 offset:39936
v_fma_f32 v77,v77,v95,v95
v_mfma_f32_16x16x32_bf16 v[36:39],a[72:75],v[64:67],v[36:39]
buffer_load_dword v10,s[36:39],s72 offen offset:0 sc1 lds
ds_read_b128 v[60:63],v2 offset:32768
v_mfma_f32_16x16x32_bf16 v[40:43],a[76:79],v[64:67],v[40:43]
s_xor_b32 s87,s63,65536
v_rcp_f32 v77,v77
v_mfma_f32_16x16x32_bf16 v[36:39],a[80:83],v[68:71],v[36:39]
s_cmp_ge_u32 s67,s59
ds_read_b128 v[64:67],v2 offset:33792
v_fma_f32 v78,v94,v78,v93
v_mfma_f32_16x16x32_bf16 v[40:43],a[84:87],v[68:71],v[40:43]
s_cselect_b32 s86,s50,s51
v_mul_f32 v78,v30,v78
v_fma_f32 v79,v94,v79,v93
v_mfma_f32_16x16x32_bf16 v[36:39],a[88:91],v[72:75],v[36:39]
s_cselect_b32 s87,s87,s64
ds_read_b128 v[68:71],v2 offset:34816
v_mul_f32 v79,v31,v79
v_mfma_f32_16x16x32_bf16 v[40:43],a[92:95],v[72:75],v[40:43]
s_waitcnt lgkmcnt(3)
v_exp_f32 v78,v78
v_mul_f32 v28,v28,v76
v_mfma_f32_16x16x32_bf16 v[36:39],a[32:35],v[44:47],v[36:39]
s_waitcnt vmcnt(11)
ds_read_b128 v[72:75],v2 offset:35840
v_fma_f32 v78,v78,v95,v95
v_mfma_f32_16x16x32_bf16 v[40:43],a[36:39],v[44:47],v[40:43]
s_barrier
v_mfma_f32_16x16x32_bf16 v[36:39],a[40:43],v[48:51],v[36:39]
s_mov_b32 s62,s87
ds_read_b128 v[44:47],v2 offset:61440
v_mul_f32 v29,v29,v77
v_mfma_f32_16x16x32_bf16 v[40:43],a[44:47],v[48:51],v[40:43]
s_cmp_lt_u32 s86,s89
v_exp_f32 v79,v79
v_mfma_f32_16x16x32_bf16 v[36:39],a[48:51],v[52:55],v[36:39]
s_mov_b32 m0,s62
ds_read_b128 v[48:51],v2 offset:62464
v_rcp_f32 v78,v78
v_mfma_f32_16x16x32_bf16 v[40:43],a[52:55],v[52:55],v[40:43]
v_fma_f32 v79,v79,v95,v95
v_mul_f32 v30,v30,v78
v_mfma_f32_16x16x32_bf16 v[36:39],a[56:59],v[56:59],v[36:39]
ds_read_b128 v[52:55],v2 offset:63488
v_cvt_pk_bf16_f32 v84,v28,v29
v_mfma_f32_16x16x32_bf16 v[40:43],a[60:63],v[56:59],v[40:43]
s_waitcnt lgkmcnt(3)
v_rcp_f32 v79,v79
v_mfma_f32_16x16x32_bf16 v[36:39],a[0:3],v[60:63],v[36:39]
ds_read_b128 v[56:59],v2 offset:64512
v_mul_f32 v31,v31,v79
v_mfma_f32_16x16x32_bf16 v[40:43],a[4:7],v[60:63],v[40:43]
v_cvt_pk_bf16_f32 v85,v30,v31
v_fma_f32 v32,-v92,v24,v32
v_mfma_f32_16x16x32_bf16 v[36:39],a[8:11],v[64:67],v[36:39]
ds_read_b128 v[60:63],v2 offset:57344
v_fma_f32 v33,-v92,v25,v33
v_fma_f32 v34,-v92,v26,v34
v_mfma_f32_16x16x32_bf16 v[40:43],a[12:15],v[64:67],v[40:43])"
R"(
v_fma_f32 v35,-v92,v27,v35
v_mul_f32 v80,v32,v32
v_mfma_f32_16x16x32_bf16 v[36:39],a[16:19],v[68:71],v[36:39]
ds_read_b128 v[64:67],v2 offset:58368
v_mul_f32 v81,v33,v33
v_mfma_f32_16x16x32_bf16 v[40:43],a[20:23],v[68:71],v[40:43]
v_mul_f32 v82,v34,v34
v_mul_f32 v83,v35,v35
v_mfma_f32_16x16x32_bf16 v[36:39],a[24:27],v[72:75],v[36:39]
ds_read_b128 v[68:71],v2 offset:59392
v_fma_f32 v80,v94,v80,v93
v_mfma_f32_16x16x32_bf16 v[40:43],a[28:31],v[72:75],v[40:43]
s_waitcnt lgkmcnt(3)
v_mul_f32 v80,v32,v80
v_exp_f32 v80,v80
v_mfma_f32_16x16x32_bf16 v[36:39],a[224:227],v[44:47],v[36:39]
ds_read_b128 v[72:75],v2 offset:60416
v_fma_f32 v80,v80,v95,v95
v_mfma_f32_16x16x32_bf16 v[40:43],a[228:231],v[44:47],v[40:43]
v_rcp_f32 v80,v80
v_mfma_f32_16x16x32_bf16 v[104:107],v[100:103],v[44:47],v[104:107]
v_fma_f32 v81,v94,v81,v93
v_mul_f32 v81,v33,v81
v_mfma_f32_16x16x32_bf16 v[108:111],v[44:47],v[44:47],v[108:111]
v_exp_f32 v81,v81
v_mfma_f32_16x16x32_bf16 v[36:39],a[232:235],v[48:51],v[36:39]
ds_read_b128 v[44:47],v2 offset:53248
v_fma_f32 v81,v81,v95,v95
v_fma_f32 v82,v94,v82,v93
v_mfma_f32_16x16x32_bf16 v[40:43],a[236:239],v[48:51],v[40:43]
v_rcp_f32 v81,v81
v_mfma_f32_16x16x32_bf16 v[104:107],v[100:103],v[48:51],v[104:107]
s_nop 0
v_mul_f32 v82,v34,v82
v_fma_f32 v83,v94,v83,v93
v_mfma_f32_16x16x32_bf16 v[108:111],v[48:51],v[48:51],v[108:111]
s_barrier
v_mfma_f32_16x16x32_bf16 v[36:39],a[240:243],v[52:55],v[36:39]
ds_read_b128 v[48:51],v2 offset:54272
ds_read_b32 v16,v7 offset:256
v_mfma_f32_16x16x32_bf16 v[40:43],a[244:247],v[52:55],v[40:43]
v_exp_f32 v82,v82
v_mfma_f32_16x16x32_bf16 v[104:107],v[100:103],v[52:55],v[104:107]
v_mul_f32 v83,v35,v83
v_exp_f32 v83,v83
v_mfma_f32_16x16x32_bf16 v[108:111],v[52:55],v[52:55],v[108:111]
v_fma_f32 v82,v82,v95,v95
v_fma_f32 v83,v83,v95,v95
v_mfma_f32_16x16x32_bf16 v[36:39],a[248:251],v[56:59],v[36:39]
s_waitcnt lgkmcnt(0)
ds_read_b128 v[52:55],v2 offset:55296
v_mul_f32 v32,v32,v80
v_mfma_f32_16x16x32_bf16 v[40:43],a[252:255],v[56:59],v[40:43]
v_cmp_eq_u32 vcc,s60,v16
v_rcp_f32 v82,v82
v_mul_f32 v33,v33,v81
v_mfma_f32_16x16x32_bf16 v[104:107],v[100:103],v[56:59],v[104:107]
s_cselect_b32 vcc_lo,-1,vcc_lo
v_rcp_f32 v83,v83
v_mfma_f32_16x16x32_bf16 v[108:111],v[56:59],v[56:59],v[108:111]
v_mul_f32 v34,v34,v82
v_mul_f32 v35,v35,v83
v_cvt_pk_bf16_f32 v86,v32,v33
v_mfma_f32_16x16x32_bf16 v[36:39],a[192:195],v[60:63],v[36:39]
s_cmp_eq_u32 vcc_lo,-1
ds_read_b128 v[56:59],v2 offset:56320
v_cvt_pk_bf16_f32 v87,v34,v35
v_mfma_f32_16x16x32_bf16 v[40:43],a[196:199],v[60:63],v[40:43]
s_cselect_b32 s66,s86,s59
v_cndmask_b32 v112,v108,v109,s[82:83]
v_cndmask_b32 v113,v110,v111,s[82:83]
v_mfma_f32_16x16x32_bf16 v[36:39],a[200:203],v[64:67],v[36:39]
s_lshr_b32 s70,s47,5
v_cndmask_b32 v112,v112,v113,s[84:85]
v_cndmask_b32 v112,v104,v112,s[98:99]
v_mfma_f32_16x16x32_bf16 v[40:43],a[204:207],v[64:67],v[40:43]
s_xor_b32 s64,s64,65536
ds_read_b128 v[60:63],v2 offset:49152
ds_write_b32 v13,v112 offset:2048
buffer_load_dwordx4 v0,s[36:39],s66 offen offset:0 sc1 lds
v_mfma_f32_16x16x32_bf16 v[36:39],a[208:211],v[68:71],v[36:39]
s_sub_u32 s69,s66,1048576
s_cmp_lt_u32 s69,2097152
ds_read_b128 v[64:67],v2 offset:50176
s_cselect_b32 s89,3145728,s89
v_mfma_f32_16x16x32_bf16 v[40:43],a[212:215],v[68:71],v[40:43]
s_sub_u32 s69,s66,3145728
s_cmp_lt_u32 s69,1048576
s_cselect_b32 s89,4194304,s89
v_mfma_f32_16x16x32_bf16 v[36:39],a[216:219],v[72:75],v[36:39]
s_add_u32 s74,s53,s55
ds_read_b128 v[68:71],v2 offset:51200
s_cmp_eq_u32 s74,s56
v_mfma_f32_16x16x32_bf16 v[40:43],a[220:223],v[72:75],v[40:43]
s_waitcnt lgkmcnt(4)
buffer_load_dwordx4 v0,s[36:39],s66 offen offset:1024 sc1 lds
v_mfma_f32_16x16x32_bf16 v[36:39],a[160:163],v[44:47],v[36:39]
s_waitcnt vmcnt(7) lgkmcnt(2)
ds_read_b128 v[72:75],v2 offset:52224
v_xor_b32 v2,65536,v2
v_mfma_f32_16x16x32_bf16 v[40:43],a[164:167],v[44:47],v[40:43]
s_barrier
v_mfma_f32_16x16x32_bf16 v[36:39],a[168:171],v[48:51],v[36:39]
buffer_store_dword v5,v4,s[36:39],s70 offen offset:0 sc0 sc1
ds_read_b128 v[116:119],v14 offset:2048
ds_read_b128 v[120:123],v14 offset:2064
ds_read_b128 v[44:47],v2 offset:12288
v_mfma_f32_16x16x32_bf16 v[40:43],a[172:175],v[48:51],v[40:43]
s_cselect_b32 s74,s57,s74
v_xor_b32 v8,16384,v8
s_cmp_eq_u32 s74,s58
v_mfma_f32_16x16x32_bf16 v[36:39],a[176:179],v[52:55],v[36:39]
s_cselect_b32 s74,s59,s74
ds_read_b128 v[48:51],v2 offset:13312
s_mov_b64 s[46:47],s[48:49]
v_mfma_f32_16x16x32_bf16 v[40:43],a[180:183],v[52:55],v[40:43]
buffer_load_dwordx4 v0,s[36:39],s66 offen offset:2048 sc1 lds
v_mfma_f32_16x16x32_bf16 v[36:39],a[184:187],v[56:59],v[36:39]
s_waitcnt lgkmcnt(2)
ds_read_b128 v[52:55],v2 offset:14336
v_add_f32 v116,v116,v117
v_mfma_f32_16x16x32_bf16 v[40:43],a[188:191],v[56:59],v[40:43]
v_add_f32 v118,v118,v119
v_add_f32 v116,v116,v118
v_mul_f32 v96,s75,v116
v_mfma_f32_16x16x32_bf16 v[36:39],a[128:131],v[60:63],v[36:39]
s_mov_b64 s[48:49],s[50:51]
ds_read_b128 v[56:59],v2 offset:15360
v_add_f32 v120,v120,v121
v_mfma_f32_16x16x32_bf16 v[40:43],a[132:135],v[60:63],v[40:43]
buffer_load_dwordx4 v0,s[36:39],s66 offen offset:3072 sc1 lds
v_mfma_f32_16x16x32_bf16 v[36:39],a[136:139],v[64:67],v[36:39]
s_mov_b64 s[50:51],s[52:53]
ds_read_b128 v[60:63],v2 offset:8192
v_add_f32 v122,v122,v123
v_mfma_f32_16x16x32_bf16 v[40:43],a[140:143],v[64:67],v[40:43]
s_mov_b32 s52,s74
v_add_f32 v120,v120,v122
v_mul_f32 v120,s75,v120
v_mfma_f32_16x16x32_bf16 v[36:39],a[144:147],v[68:71],v[36:39]
s_cmp_lt_u32 s67,4194304
ds_read_b128 v[64:67],v2 offset:9216
v_fma_f32 v120,-v96,v96,v120
v_mfma_f32_16x16x32_bf16 v[40:43],a[148:151],v[68:71],v[40:43]
buffer_store_dwordx4 v[84:87],v3,s[36:39],s73 offen offset:0 sc0 sc1
v_max_f32 v120,0,v120
v_add_f32 v115,v15,v120
v_fma_f32 v120,v15,v115,v120
v_rsq_f32 v116,v120
v_mfma_f32_16x16x32_bf16 v[36:39],a[152:155],v[72:75],v[36:39]
s_waitcnt lgkmcnt(2)
ds_read_b128 v[68:71],v2 offset:10240
v_mul_f32 v97,s76,v116
v_mul_f32 v98,v116,v116
v_mul_f32 v99,v120,v116
v_mul_f32 v98,s77,v98
v_mul_f32 v98,v98,v97
v_mfma_f32_16x16x32_bf16 v[40:43],a[156:159],v[72:75],v[40:43]
s_cbranch_scc1 Lg1_a0w3_%=
s_cmp_ge_u32 s48,4194304
s_cselect_b64 s[90:91],s[94:95],s[90:91]
s_cselect_b64 s[92:93],s[96:97],s[92:93]
s_cmp_lt_u32 s67,s59
s_cbranch_scc1 Lg1_d0w3_%=
s_branch Lg1_slow0_%=
Lg1_d0w0_%=:
v_mfma_f32_16x16x32_bf16 v[28:31],a[0:3],v[44:47],0
s_add_u32 m0,s62,16384
ds_read_b128 v[72:75],v2 offset:7168
s_lshr_b32 s73,s47,0
v_mfma_f32_16x16x32_bf16 v[32:35],a[4:7],v[44:47],0
buffer_load_dwordx4 v1,s[36:39],s66 offen offset:0 sc1 lds
v_mfma_f32_16x16x32_bf16 v[28:31],a[8:11],v[48:51],v[28:31]
s_lshr_b32 s72,s66,6
ds_read_b128 v[44:47],v2 offset:8192
s_lshr_b32 s71,s62,2
v_mfma_f32_16x16x32_bf16 v[32:35],a[12:15],v[48:51],v[32:35]
s_cmp_lt_u32 s66,4194304
v_fma_f32 v36,-v96,v20,v36
v_fma_f32 v37,-v96,v21,v37
v_mfma_f32_16x16x32_bf16 v[28:31],a[16:19],v[52:55],v[28:31]
s_cselect_b32 s72,s59,s72
ds_read_b128 v[48:51],v2 offset:9216
v_fma_f32 v38,-v96,v22,v38
v_mfma_f32_16x16x32_bf16 v[32:35],a[20:23],v[52:55],v[32:35]
buffer_load_dwordx4 v1,s[36:39],s66 offen offset:1024 sc1 lds
v_mfma_f32_16x16x32_bf16 v[28:31],a[24:27],v[56:59],v[28:31]
ds_read_b128 v[52:55],v2 offset:10240
v_fma_f32 v39,-v96,v23,v39
v_mfma_f32_16x16x32_bf16 v[32:35],a[28:31],v[56:59],v[32:35]
s_waitcnt lgkmcnt(3)
v_mul_f32 v76,v36,v36
v_mul_f32 v77,v37,v37
v_mfma_f32_16x16x32_bf16 v[28:31],a[32:35],v[60:63],v[28:31]
ds_read_b128 v[56:59],v2 offset:11264
v_mul_f32 v78,v38,v38
v_mfma_f32_16x16x32_bf16 v[32:35],a[36:39],v[60:63],v[32:35]
buffer_load_dwordx4 v1,s[36:39],s66 offen offset:2048 sc1 lds
v_mfma_f32_16x16x32_bf16 v[28:31],a[40:43],v[64:67],v[28:31]
ds_read_b128 v[60:63],v2 offset:12288)"
R"(
v_mul_f32 v79,v39,v39
v_mfma_f32_16x16x32_bf16 v[32:35],a[44:47],v[64:67],v[32:35]
v_fma_f32 v76,v98,v76,v97
v_mul_f32 v76,v36,v76
v_mfma_f32_16x16x32_bf16 v[28:31],a[48:51],v[68:71],v[28:31]
ds_read_b128 v[64:67],v2 offset:13312
v_fma_f32 v77,v98,v77,v97
v_mfma_f32_16x16x32_bf16 v[32:35],a[52:55],v[68:71],v[32:35]
buffer_load_dwordx4 v1,s[36:39],s66 offen offset:3072 sc1 lds
v_mfma_f32_16x16x32_bf16 v[28:31],a[56:59],v[72:75],v[28:31]
s_add_u32 m0,s71,s88
ds_read_b128 v[68:71],v2 offset:14336
v_mul_f32 v77,v37,v77
v_mfma_f32_16x16x32_bf16 v[32:35],a[60:63],v[72:75],v[32:35]
s_waitcnt lgkmcnt(3)
buffer_load_dword v10,s[36:39],s72 offen offset:0 sc1 lds
v_mfma_f32_16x16x32_bf16 v[28:31],a[64:67],v[44:47],v[28:31]
s_waitcnt vmcnt(11)
ds_read_b128 v[72:75],v2 offset:15360
v_fma_f32 v78,v98,v78,v97
v_mfma_f32_16x16x32_bf16 v[32:35],a[68:71],v[44:47],v[32:35]
s_barrier
v_mfma_f32_16x16x32_bf16 v[28:31],a[72:75],v[48:51],v[28:31]
s_xor_b32 s87,s64,65536
ds_read_b128 v[44:47],v2 offset:16384
v_fma_f32 v79,v98,v79,v97
v_mfma_f32_16x16x32_bf16 v[32:35],a[76:79],v[48:51],v[32:35]
s_cmp_ge_u32 s66,s59
v_exp_f32 v76,v76
v_mfma_f32_16x16x32_bf16 v[28:31],a[80:83],v[52:55],v[28:31]
s_cselect_b32 s86,s49,s50
ds_read_b128 v[48:51],v2 offset:17408
v_mul_f32 v78,v38,v78
v_mfma_f32_16x16x32_bf16 v[32:35],a[84:87],v[52:55],v[32:35]
s_cselect_b32 s87,s87,s63
v_exp_f32 v77,v77
v_mfma_f32_16x16x32_bf16 v[28:31],a[88:91],v[56:59],v[28:31]
s_lshr_b32 s69,s50,5
ds_read_b128 v[52:55],v2 offset:18432
v_mul_f32 v79,v39,v79
v_mfma_f32_16x16x32_bf16 v[32:35],a[92:95],v[56:59],v[32:35]
s_waitcnt lgkmcnt(3)
v_exp_f32 v78,v78
v_mfma_f32_16x16x32_bf16 v[28:31],a[96:99],v[60:63],v[28:31]
s_cmp_lt_u32 s50,4194304
ds_read_b128 v[56:59],v2 offset:19456
v_fma_f32 v76,v76,v99,v99
v_mfma_f32_16x16x32_bf16 v[32:35],a[100:103],v[60:63],v[32:35]
s_cselect_b32 s68,0xffffcfff,-1
v_exp_f32 v79,v79
v_mfma_f32_16x16x32_bf16 v[28:31],a[104:107],v[64:67],v[28:31]
s_and_b32 s68,s68,s69
ds_read_b128 v[60:63],v2 offset:20480
v_fma_f32 v77,v77,v99,v99
v_mfma_f32_16x16x32_bf16 v[32:35],a[108:111],v[64:67],v[32:35]
s_lshr_b32 s69,s51,5
v_rcp_f32 v76,v76
v_mfma_f32_16x16x32_bf16 v[28:31],a[112:115],v[68:71],v[28:31]
s_cmp_lt_u32 s51,4194304
ds_read_b128 v[64:67],v2 offset:21504
v_fma_f32 v78,v78,v99,v99
v_mfma_f32_16x16x32_bf16 v[32:35],a[116:119],v[68:71],v[32:35]
s_cselect_b32 s70,0xffffcfff,-1
v_rcp_f32 v77,v77
v_mfma_f32_16x16x32_bf16 v[28:31],a[120:123],v[72:75],v[28:31]
s_and_b32 s70,s70,s69
ds_read_b128 v[68:71],v2 offset:22528
v_fma_f32 v79,v79,v99,v99
v_mfma_f32_16x16x32_bf16 v[32:35],a[124:127],v[72:75],v[32:35]
s_waitcnt lgkmcnt(3)
v_rcp_f32 v78,v78
v_mfma_f32_16x16x32_bf16 v[28:31],a[128:131],v[44:47],v[28:31]
s_waitcnt vmcnt(10)
ds_read_b128 v[72:75],v2 offset:23552
v_mul_f32 v36,v36,v76
v_mfma_f32_16x16x32_bf16 v[32:35],a[132:135],v[44:47],v[32:35]
s_barrier
v_mfma_f32_16x16x32_bf16 v[28:31],a[136:139],v[48:51],v[28:31]
s_mov_b32 s62,s87
ds_read_b128 v[44:47],v2 offset:24576
ds_read_b32 v16,v7 offset:0
v_mfma_f32_16x16x32_bf16 v[32:35],a[140:143],v[48:51],v[32:35]
v_rcp_f32 v79,v79
v_mfma_f32_16x16x32_bf16 v[28:31],a[144:147],v[52:55],v[28:31]
ds_read_b128 v[48:51],v2 offset:25600
v_mul_f32 v37,v37,v77
v_mfma_f32_16x16x32_bf16 v[32:35],a[148:151],v[52:55],v[32:35]
v_mul_f32 v38,v38,v78
v_mul_f32 v39,v39,v79
v_mfma_f32_16x16x32_bf16 v[28:31],a[152:155],v[56:59],v[28:31]
s_waitcnt lgkmcnt(1)
ds_read_b128 v[52:55],v2 offset:26624
v_cvt_pk_bf16_f32 v84,v36,v37
v_mfma_f32_16x16x32_bf16 v[32:35],a[156:159],v[56:59],v[32:35]
v_cmp_eq_u32 vcc,s60,v16
v_cvt_pk_bf16_f32 v85,v38,v39
v_fma_f32 v40,-v96,v24,v40
v_fma_f32 v41,-v96,v25,v41
v_mfma_f32_16x16x32_bf16 v[28:31],a[160:163],v[60:63],v[28:31]
s_cmp_eq_u32 vcc_lo,-1
ds_read_b128 v[56:59],v2 offset:27648
v_fma_f32 v42,-v96,v26,v42
v_mfma_f32_16x16x32_bf16 v[32:35],a[164:167],v[60:63],v[32:35]
s_cselect_b32 s67,s86,s59
s_cmp_ge_u32 s67,s59
s_cselect_b32 s68,s68,s70
s_add_u32 m0,s78,256
v_mfma_f32_16x16x32_bf16 v[28:31],a[168:171],v[64:67],v[28:31]
buffer_load_dword v6,s[36:39],s68 offen offset:0 sc1 lds
v_mfma_f32_16x16x32_bf16 v[32:35],a[172:175],v[64:67],v[32:35]
s_mov_b32 m0,s62
ds_read_b128 v[60:63],v2 offset:28672
v_fma_f32 v43,-v96,v27,v43
v_mfma_f32_16x16x32_bf16 v[28:31],a[176:179],v[68:71],v[28:31]
buffer_load_dwordx4 v0,s[36:39],s67 offen offset:0 sc1 lds
v_mfma_f32_16x16x32_bf16 v[32:35],a[180:183],v[68:71],v[32:35]
s_xor_b32 s63,s63,65536
ds_read_b128 v[64:67],v2 offset:29696
v_mul_f32 v80,v40,v40
v_mfma_f32_16x16x32_bf16 v[28:31],a[184:187],v[72:75],v[28:31]
s_add_u32 s53,s52,s55
ds_read_b128 v[68:71],v2 offset:30720
v_mul_f32 v81,v41,v41
v_mfma_f32_16x16x32_bf16 v[32:35],a[188:191],v[72:75],v[32:35]
s_waitcnt lgkmcnt(3)
v_mul_f32 v82,v42,v42
v_mul_f32 v83,v43,v43
v_mfma_f32_16x16x32_bf16 v[28:31],a[192:195],v[44:47],v[28:31]
s_waitcnt vmcnt(7)
ds_read_b128 v[72:75],v2 offset:31744
v_fma_f32 v80,v98,v80,v97
v_mfma_f32_16x16x32_bf16 v[32:35],a[196:199],v[44:47],v[32:35]
s_barrier
v_mfma_f32_16x16x32_bf16 v[28:31],a[200:203],v[48:51],v[28:31]
buffer_load_dwordx4 v0,s[36:39],s67 offen offset:1024 sc1 lds
v_mfma_f32_16x16x32_bf16 v[32:35],a[204:207],v[48:51],v[32:35]
s_cmp_eq_u32 s53,s56
ds_read_b128 v[44:47],v2 offset:32768
ds_read_b128 v[92:95],v8 offset:0
v_mfma_f32_16x16x32_bf16 v[28:31],a[208:211],v[52:55],v[28:31]
s_cselect_b32 s53,s57,s53
ds_read_b128 v[48:51],v2 offset:33792
v_mul_f32 v80,v40,v80
v_mfma_f32_16x16x32_bf16 v[32:35],a[212:215],v[52:55],v[32:35]
s_cmp_eq_u32 s53,s58
v_fma_f32 v81,v98,v81,v97
v_fma_f32 v82,v98,v82,v97
v_fma_f32 v83,v98,v83,v97
v_mul_f32 v81,v41,v81
v_mul_f32 v82,v42,v82
v_mul_f32 v83,v43,v83
v_exp_f32 v80,v80
v_exp_f32 v81,v81
v_exp_f32 v82,v82
v_exp_f32 v83,v83
v_mfma_f32_16x16x32_bf16 v[28:31],a[216:219],v[56:59],v[28:31]
buffer_load_dwordx4 v0,s[36:39],s67 offen offset:2048 sc1 lds
v_fma_f32 v80,v80,v99,v99
v_fma_f32 v81,v81,v99,v99
v_fma_f32 v82,v82,v99,v99
v_fma_f32 v83,v83,v99,v99
v_rcp_f32 v80,v80
v_rcp_f32 v81,v81
v_rcp_f32 v82,v82
v_rcp_f32 v83,v83
v_mfma_f32_16x16x32_bf16 v[32:35],a[220:223],v[56:59],v[32:35]
s_waitcnt lgkmcnt(3)
ds_read_b128 v[52:55],v2 offset:34816
v_mul_f32 v40,v40,v80
v_mul_f32 v41,v41,v81
v_mul_f32 v42,v42,v82
v_mul_f32 v43,v43,v83
v_cvt_pk_bf16_f32 v86,v40,v41
v_cvt_pk_bf16_f32 v87,v42,v43
v_mfma_f32_16x16x32_bf16 v[28:31],a[224:227],v[60:63],v[28:31]
buffer_store_dwordx4 v[84:87],v3,s[36:39],s73 offen offset:0 sc0 sc1
ds_read_b128 v[56:59],v2 offset:35840
v_mfma_f32_16x16x32_bf16 v[32:35],a[228:231],v[60:63],v[32:35]
s_waitcnt lgkmcnt(3)
s_cselect_b32 s53,s59,s53
s_cmp_ge_u32 s66,s59
v_mfma_f32_16x16x32_bf16 v[28:31],a[232:235],v[64:67],v[28:31]
buffer_load_dwordx4 v0,s[36:39],s67 offen offset:3072 sc1 lds
v_mfma_f32_16x16x32_bf16 v[32:35],a[236:239],v[64:67],v[32:35]
ds_read_b128 v[60:63],v2 offset:36864
v_mfma_f32_16x16x32_bf16 v[28:31],a[240:243],v[68:71],v[28:31]
ds_read_b128 v[64:67],v2 offset:37888
v_mfma_f32_16x16x32_bf16 v[32:35],a[244:247],v[68:71],v[32:35]
v_mfma_f32_16x16x32_bf16 v[28:31],a[248:251],v[72:75],v[28:31]
s_waitcnt lgkmcnt(2)
ds_read_b128 v[68:71],v2 offset:38912
v_mfma_f32_16x16x32_bf16 v[32:35],a[252:255],v[72:75],v[32:35]
s_cbranch_scc1 Lg1_slow1_%=
Lg1_d1w0_%=:
v_mfma_f32_16x16x32_bf16 v[36:39],a[0:3],v[44:47],0
s_add_u32 m0,s62,16384
ds_read_b128 v[72:75],v2 offset:39936
s_lshr_b32 s73,s48,0
v_mfma_f32_16x16x32_bf16 v[40:43],a[4:7],v[44:47],0
buffer_load_dwordx4 v1,s[36:39],s67 offen offset:0 sc1 lds)"
R"(
v_mfma_f32_16x16x32_bf16 v[36:39],a[8:11],v[48:51],v[36:39]
s_lshr_b32 s72,s67,6
ds_read_b128 v[44:47],v2 offset:40960
s_lshr_b32 s71,s62,2
v_mfma_f32_16x16x32_bf16 v[40:43],a[12:15],v[48:51],v[40:43]
s_cmp_lt_u32 s67,4194304
v_fma_f32 v28,-v92,v20,v28
v_fma_f32 v29,-v92,v21,v29
v_mfma_f32_16x16x32_bf16 v[36:39],a[16:19],v[52:55],v[36:39]
s_cselect_b32 s72,s59,s72
ds_read_b128 v[48:51],v2 offset:41984
v_fma_f32 v30,-v92,v22,v30
v_mfma_f32_16x16x32_bf16 v[40:43],a[20:23],v[52:55],v[40:43]
buffer_load_dwordx4 v1,s[36:39],s67 offen offset:1024 sc1 lds
v_mfma_f32_16x16x32_bf16 v[36:39],a[24:27],v[56:59],v[36:39]
s_mov_b64 s[44:45],s[46:47]
ds_read_b128 v[52:55],v2 offset:43008
v_fma_f32 v31,-v92,v23,v31
v_mfma_f32_16x16x32_bf16 v[40:43],a[28:31],v[56:59],v[40:43]
s_waitcnt lgkmcnt(3)
v_mul_f32 v76,v28,v28
v_mul_f32 v77,v29,v29
v_mfma_f32_16x16x32_bf16 v[36:39],a[32:35],v[60:63],v[36:39]
s_mov_b64 s[46:47],s[48:49]
ds_read_b128 v[56:59],v2 offset:44032
v_mul_f32 v78,v30,v30
v_mfma_f32_16x16x32_bf16 v[40:43],a[36:39],v[60:63],v[40:43]
buffer_load_dwordx4 v1,s[36:39],s67 offen offset:2048 sc1 lds
v_mfma_f32_16x16x32_bf16 v[36:39],a[40:43],v[64:67],v[36:39]
s_mov_b64 s[48:49],s[50:51]
ds_read_b128 v[60:63],v2 offset:45056
v_mul_f32 v79,v31,v31
v_mfma_f32_16x16x32_bf16 v[40:43],a[44:47],v[64:67],v[40:43]
v_fma_f32 v76,v94,v76,v93
v_mul_f32 v76,v28,v76
v_mfma_f32_16x16x32_bf16 v[36:39],a[48:51],v[68:71],v[36:39]
ds_read_b128 v[64:67],v2 offset:46080
v_fma_f32 v77,v94,v77,v93
v_mfma_f32_16x16x32_bf16 v[40:43],a[52:55],v[68:71],v[40:43]
buffer_load_dwordx4 v1,s[36:39],s67 offen offset:3072 sc1 lds
v_mfma_f32_16x16x32_bf16 v[36:39],a[56:59],v[72:75],v[36:39]
s_add_u32 m0,s71,s88
ds_read_b128 v[68:71],v2 offset:47104
v_mul_f32 v77,v29,v77
v_mfma_f32_16x16x32_bf16 v[40:43],a[60:63],v[72:75],v[40:43]
s_waitcnt lgkmcnt(3)
buffer_load_dword v10,s[36:39],s72 offen offset:0 sc1 lds
v_mfma_f32_16x16x32_bf16 v[36:39],a[64:67],v[44:47],v[36:39]
s_waitcnt vmcnt(11)
ds_read_b128 v[72:75],v2 offset:48128
v_fma_f32 v78,v94,v78,v93
v_mfma_f32_16x16x32_bf16 v[40:43],a[68:71],v[44:47],v[40:43]
s_barrier
v_mfma_f32_16x16x32_bf16 v[36:39],a[72:75],v[48:51],v[36:39]
s_xor_b32 s87,s63,65536
ds_read_b128 v[44:47],v2 offset:49152
v_fma_f32 v79,v94,v79,v93
v_mfma_f32_16x16x32_bf16 v[40:43],a[76:79],v[48:51],v[40:43]
s_cmp_ge_u32 s67,s59
v_exp_f32 v76,v76
v_mfma_f32_16x16x32_bf16 v[36:39],a[80:83],v[52:55],v[36:39]
s_cselect_b32 s86,s50,s51
ds_read_b128 v[48:51],v2 offset:50176
v_mul_f32 v78,v30,v78
v_mfma_f32_16x16x32_bf16 v[40:43],a[84:87],v[52:55],v[40:43]
s_cselect_b32 s87,s87,s64
v_exp_f32 v77,v77
v_mfma_f32_16x16x32_bf16 v[36:39],a[88:91],v[56:59],v[36:39]
s_lshr_b32 s69,s51,5
ds_read_b128 v[52:55],v2 offset:51200
v_mul_f32 v79,v31,v79
v_mfma_f32_16x16x32_bf16 v[40:43],a[92:95],v[56:59],v[40:43]
s_waitcnt lgkmcnt(3)
v_exp_f32 v78,v78
v_mfma_f32_16x16x32_bf16 v[36:39],a[96:99],v[60:63],v[36:39]
s_cmp_lt_u32 s51,4194304
ds_read_b128 v[56:59],v2 offset:52224
v_fma_f32 v76,v76,v95,v95
v_mfma_f32_16x16x32_bf16 v[40:43],a[100:103],v[60:63],v[40:43]
s_cselect_b32 s68,0xffffcfff,-1
v_exp_f32 v79,v79
v_mfma_f32_16x16x32_bf16 v[36:39],a[104:107],v[64:67],v[36:39]
s_and_b32 s68,s68,s69
ds_read_b128 v[60:63],v2 offset:53248
v_fma_f32 v77,v77,v95,v95
v_mfma_f32_16x16x32_bf16 v[40:43],a[108:111],v[64:67],v[40:43]
s_lshr_b32 s69,s52,5
v_rcp_f32 v76,v76
v_mfma_f32_16x16x32_bf16 v[36:39],a[112:115],v[68:71],v[36:39]
s_cmp_lt_u32 s52,4194304
ds_read_b128 v[64:67],v2 offset:54272
v_fma_f32 v78,v78,v95,v95
v_mfma_f32_16x16x32_bf16 v[40:43],a[116:119],v[68:71],v[40:43]
s_cselect_b32 s70,0xffffcfff,-1
v_rcp_f32 v77,v77
v_mfma_f32_16x16x32_bf16 v[36:39],a[120:123],v[72:75],v[36:39]
s_and_b32 s70,s70,s69
ds_read_b128 v[68:71],v2 offset:55296
v_fma_f32 v79,v79,v95,v95
v_mfma_f32_16x16x32_bf16 v[40:43],a[124:127],v[72:75],v[40:43]
s_waitcnt lgkmcnt(3)
v_rcp_f32 v78,v78
v_mfma_f32_16x16x32_bf16 v[36:39],a[128:131],v[44:47],v[36:39]
s_waitcnt vmcnt(10)
ds_read_b128 v[72:75],v2 offset:56320
v_mul_f32 v28,v28,v76
v_mfma_f32_16x16x32_bf16 v[40:43],a[132:135],v[44:47],v[40:43]
s_barrier
v_mfma_f32_16x16x32_bf16 v[36:39],a[136:139],v[48:51],v[36:39]
s_mov_b32 s62,s87
ds_read_b128 v[44:47],v2 offset:57344
ds_read_b32 v16,v7 offset:256
v_mfma_f32_16x16x32_bf16 v[40:43],a[140:143],v[48:51],v[40:43]
s_mov_b64 s[50:51],s[52:53]
v_rcp_f32 v79,v79
v_mfma_f32_16x16x32_bf16 v[36:39],a[144:147],v[52:55],v[36:39]
ds_read_b128 v[48:51],v2 offset:58368
v_mul_f32 v29,v29,v77
v_mfma_f32_16x16x32_bf16 v[40:43],a[148:151],v[52:55],v[40:43]
v_mul_f32 v30,v30,v78
v_mul_f32 v31,v31,v79
v_mfma_f32_16x16x32_bf16 v[36:39],a[152:155],v[56:59],v[36:39]
s_waitcnt lgkmcnt(1)
ds_read_b128 v[52:55],v2 offset:59392
v_cvt_pk_bf16_f32 v84,v28,v29
v_mfma_f32_16x16x32_bf16 v[40:43],a[156:159],v[56:59],v[40:43]
v_cmp_eq_u32 vcc,s60,v16
v_cvt_pk_bf16_f32 v85,v30,v31
v_fma_f32 v32,-v92,v24,v32
v_fma_f32 v33,-v92,v25,v33
v_mfma_f32_16x16x32_bf16 v[36:39],a[160:163],v[60:63],v[36:39]
s_cmp_eq_u32 vcc_lo,-1
ds_read_b128 v[56:59],v2 offset:60416
v_fma_f32 v34,-v92,v26,v34
v_mfma_f32_16x16x32_bf16 v[40:43],a[164:167],v[60:63],v[40:43]
s_cselect_b32 s66,s86,s59
s_cmp_ge_u32 s66,s59
s_cselect_b32 s68,s68,s70
s_add_u32 m0,s78,0
v_mfma_f32_16x16x32_bf16 v[36:39],a[168:171],v[64:67],v[36:39]
buffer_load_dword v6,s[36:39],s68 offen offset:0 sc1 lds
v_mfma_f32_16x16x32_bf16 v[40:43],a[172:175],v[64:67],v[40:43]
s_mov_b32 m0,s62
ds_read_b128 v[60:63],v2 offset:61440
v_fma_f32 v35,-v92,v27,v35
v_mfma_f32_16x16x32_bf16 v[36:39],a[176:179],v[68:71],v[36:39]
buffer_load_dwordx4 v0,s[36:39],s66 offen offset:0 sc1 lds
v_mfma_f32_16x16x32_bf16 v[40:43],a[180:183],v[68:71],v[40:43]
s_xor_b32 s64,s64,65536
ds_read_b128 v[64:67],v2 offset:62464
v_mul_f32 v80,v32,v32
v_mfma_f32_16x16x32_bf16 v[36:39],a[184:187],v[72:75],v[36:39]
s_add_u32 s74,s53,s55
ds_read_b128 v[68:71],v2 offset:63488
v_mul_f32 v81,v33,v33
v_mfma_f32_16x16x32_bf16 v[40:43],a[188:191],v[72:75],v[40:43]
s_waitcnt lgkmcnt(3)
v_mul_f32 v82,v34,v34
v_mul_f32 v83,v35,v35
v_mfma_f32_16x16x32_bf16 v[36:39],a[192:195],v[44:47],v[36:39]
s_waitcnt vmcnt(7)
ds_read_b128 v[72:75],v2 offset:64512
v_xor_b32 v2,65536,v2
v_mfma_f32_16x16x32_bf16 v[40:43],a[196:199],v[44:47],v[40:43]
s_barrier
v_mfma_f32_16x16x32_bf16 v[36:39],a[200:203],v[48:51],v[36:39]
buffer_load_dwordx4 v0,s[36:39],s66 offen offset:1024 sc1 lds
v_mfma_f32_16x16x32_bf16 v[40:43],a[204:207],v[48:51],v[40:43]
s_cmp_eq_u32 s74,s56
ds_read_b128 v[44:47],v2 offset:0
ds_read_b128 v[96:99],v8 offset:8192
v_mfma_f32_16x16x32_bf16 v[36:39],a[208:211],v[52:55],v[36:39]
s_cselect_b32 s74,s57,s74
ds_read_b128 v[48:51],v2 offset:1024
v_fma_f32 v80,v94,v80,v93
v_mfma_f32_16x16x32_bf16 v[40:43],a[212:215],v[52:55],v[40:43]
s_cmp_eq_u32 s74,s58
v_fma_f32 v81,v94,v81,v93
v_fma_f32 v82,v94,v82,v93
v_fma_f32 v83,v94,v83,v93
v_mul_f32 v80,v32,v80
v_mul_f32 v81,v33,v81
v_mul_f32 v82,v34,v82
v_mul_f32 v83,v35,v83
v_exp_f32 v80,v80
v_exp_f32 v81,v81
v_exp_f32 v82,v82
v_exp_f32 v83,v83
v_mfma_f32_16x16x32_bf16 v[36:39],a[216:219],v[56:59],v[36:39]
buffer_load_dwordx4 v0,s[36:39],s66 offen offset:2048 sc1 lds
v_fma_f32 v80,v80,v95,v95
v_fma_f32 v81,v81,v95,v95
v_fma_f32 v82,v82,v95,v95
v_fma_f32 v83,v83,v95,v95
v_rcp_f32 v80,v80
v_rcp_f32 v81,v81
v_rcp_f32 v82,v82
v_rcp_f32 v83,v83
v_mfma_f32_16x16x32_bf16 v[40:43],a[220:223],v[56:59],v[40:43]
s_waitcnt lgkmcnt(3))"
R"(
ds_read_b128 v[52:55],v2 offset:2048
v_mul_f32 v32,v32,v80
v_mul_f32 v33,v33,v81
v_mul_f32 v34,v34,v82
v_mul_f32 v35,v35,v83
v_cvt_pk_bf16_f32 v86,v32,v33
v_cvt_pk_bf16_f32 v87,v34,v35
v_mfma_f32_16x16x32_bf16 v[36:39],a[224:227],v[60:63],v[36:39]
buffer_store_dwordx4 v[84:87],v3,s[36:39],s73 offen offset:0 sc0 sc1
ds_read_b128 v[56:59],v2 offset:3072
v_xor_b32 v8,16384,v8
v_mfma_f32_16x16x32_bf16 v[40:43],a[228:231],v[60:63],v[40:43]
s_waitcnt lgkmcnt(3)
s_cselect_b32 s74,s59,s74
s_mov_b32 s52,s74
v_mfma_f32_16x16x32_bf16 v[36:39],a[232:235],v[64:67],v[36:39]
buffer_load_dwordx4 v0,s[36:39],s66 offen offset:3072 sc1 lds
v_mfma_f32_16x16x32_bf16 v[40:43],a[236:239],v[64:67],v[40:43]
s_cmp_lt_u32 s67,s59
ds_read_b128 v[60:63],v2 offset:4096
v_mfma_f32_16x16x32_bf16 v[36:39],a[240:243],v[68:71],v[36:39]
ds_read_b128 v[64:67],v2 offset:5120
v_mfma_f32_16x16x32_bf16 v[40:43],a[244:247],v[68:71],v[40:43]
v_mfma_f32_16x16x32_bf16 v[36:39],a[248:251],v[72:75],v[36:39]
s_waitcnt lgkmcnt(2)
ds_read_b128 v[68:71],v2 offset:6144
v_mfma_f32_16x16x32_bf16 v[40:43],a[252:255],v[72:75],v[40:43]
s_cbranch_scc1 Lg1_d0w0_%=
s_branch Lg1_slow0_%=
Lg1_d0w1_%=:
v_mfma_f32_16x16x32_bf16 v[28:31],a[32:35],v[44:47],0
s_add_u32 m0,s62,16384
ds_read_b128 v[72:75],v2 offset:3072
s_lshr_b32 s73,s47,0
v_mfma_f32_16x16x32_bf16 v[32:35],a[36:39],v[44:47],0
s_lshr_b32 s72,s66,6
s_lshr_b32 s71,s62,2
s_cmp_lt_u32 s66,4194304
v_mfma_f32_16x16x32_bf16 v[28:31],a[40:43],v[48:51],v[28:31]
buffer_load_dwordx4 v1,s[36:39],s66 offen offset:0 sc1 lds
v_mfma_f32_16x16x32_bf16 v[32:35],a[44:47],v[48:51],v[32:35]
s_cselect_b32 s72,s59,s72
ds_read_b128 v[44:47],v2 offset:12288
v_fma_f32 v36,-v96,v20,v36
v_mfma_f32_16x16x32_bf16 v[28:31],a[48:51],v[52:55],v[28:31]
ds_read_b128 v[48:51],v2 offset:13312
v_fma_f32 v37,-v96,v21,v37
v_mfma_f32_16x16x32_bf16 v[32:35],a[52:55],v[52:55],v[32:35]
v_fma_f32 v38,-v96,v22,v38
v_fma_f32 v39,-v96,v23,v39
v_mfma_f32_16x16x32_bf16 v[28:31],a[56:59],v[56:59],v[28:31]
buffer_load_dwordx4 v1,s[36:39],s66 offen offset:1024 sc1 lds
v_mfma_f32_16x16x32_bf16 v[32:35],a[60:63],v[56:59],v[32:35]
s_waitcnt lgkmcnt(2)
ds_read_b128 v[52:55],v2 offset:14336
v_mul_f32 v76,v36,v36
v_mfma_f32_16x16x32_bf16 v[28:31],a[0:3],v[60:63],v[28:31]
ds_read_b128 v[56:59],v2 offset:15360
v_mul_f32 v77,v37,v37
v_mfma_f32_16x16x32_bf16 v[32:35],a[4:7],v[60:63],v[32:35]
v_mul_f32 v78,v38,v38
v_mul_f32 v79,v39,v39
v_mfma_f32_16x16x32_bf16 v[28:31],a[8:11],v[64:67],v[28:31]
buffer_load_dwordx4 v1,s[36:39],s66 offen offset:2048 sc1 lds
v_mfma_f32_16x16x32_bf16 v[32:35],a[12:15],v[64:67],v[32:35]
ds_read_b128 v[60:63],v2 offset:8192
v_fma_f32 v76,v98,v76,v97
v_mfma_f32_16x16x32_bf16 v[28:31],a[16:19],v[68:71],v[28:31]
ds_read_b128 v[64:67],v2 offset:9216
v_mul_f32 v76,v36,v76
v_mfma_f32_16x16x32_bf16 v[32:35],a[20:23],v[68:71],v[32:35]
v_exp_f32 v76,v76
v_mfma_f32_16x16x32_bf16 v[28:31],a[24:27],v[72:75],v[28:31]
buffer_load_dwordx4 v1,s[36:39],s66 offen offset:3072 sc1 lds
v_mfma_f32_16x16x32_bf16 v[32:35],a[28:31],v[72:75],v[32:35]
s_waitcnt lgkmcnt(2)
ds_read_b128 v[68:71],v2 offset:10240
s_add_u32 m0,s71,s88
v_mfma_f32_16x16x32_bf16 v[28:31],a[96:99],v[44:47],v[28:31]
s_xor_b32 s87,s64,65536
ds_read_b128 v[72:75],v2 offset:11264
buffer_load_dword v10,s[36:39],s72 offen offset:0 sc1 lds
s_waitcnt vmcnt(10)
v_mfma_f32_16x16x32_bf16 v[32:35],a[100:103],v[44:47],v[32:35]
s_barrier
v_mfma_f32_16x16x32_bf16 v[28:31],a[104:107],v[48:51],v[28:31]
s_cmp_ge_u32 s66,s59
ds_read_b128 v[44:47],v2 offset:20480
v_fma_f32 v76,v76,v99,v99
v_mfma_f32_16x16x32_bf16 v[32:35],a[108:111],v[48:51],v[32:35]
s_cselect_b32 s86,s49,s50
v_rcp_f32 v76,v76
v_mfma_f32_16x16x32_bf16 v[28:31],a[112:115],v[52:55],v[28:31]
s_cselect_b32 s87,s87,s63
ds_read_b128 v[48:51],v2 offset:21504
v_fma_f32 v77,v98,v77,v97
v_mfma_f32_16x16x32_bf16 v[32:35],a[116:119],v[52:55],v[32:35]
s_mov_b32 s62,s87
v_mul_f32 v77,v37,v77
v_fma_f32 v78,v98,v78,v97
v_mfma_f32_16x16x32_bf16 v[28:31],a[120:123],v[56:59],v[28:31]
s_mov_b32 m0,s62
ds_read_b128 v[52:55],v2 offset:22528
v_mul_f32 v78,v38,v78
v_mfma_f32_16x16x32_bf16 v[32:35],a[124:127],v[56:59],v[32:35]
s_waitcnt lgkmcnt(3)
v_exp_f32 v77,v77
v_mfma_f32_16x16x32_bf16 v[28:31],a[64:67],v[60:63],v[28:31]
ds_read_b128 v[56:59],v2 offset:23552
v_fma_f32 v77,v77,v99,v99
v_mfma_f32_16x16x32_bf16 v[32:35],a[68:71],v[60:63],v[32:35]
v_exp_f32 v78,v78
v_mfma_f32_16x16x32_bf16 v[28:31],a[72:75],v[64:67],v[28:31]
ds_read_b128 v[60:63],v2 offset:16384
v_fma_f32 v78,v78,v99,v99
v_mfma_f32_16x16x32_bf16 v[32:35],a[76:79],v[64:67],v[32:35]
v_rcp_f32 v77,v77
v_mfma_f32_16x16x32_bf16 v[28:31],a[80:83],v[68:71],v[28:31]
ds_read_b128 v[64:67],v2 offset:17408
v_fma_f32 v79,v98,v79,v97
v_mfma_f32_16x16x32_bf16 v[32:35],a[84:87],v[68:71],v[32:35]
v_rcp_f32 v78,v78
v_mfma_f32_16x16x32_bf16 v[28:31],a[88:91],v[72:75],v[28:31]
ds_read_b128 v[68:71],v2 offset:18432
v_mul_f32 v79,v39,v79
v_mfma_f32_16x16x32_bf16 v[32:35],a[92:95],v[72:75],v[32:35]
s_waitcnt lgkmcnt(3)
v_exp_f32 v79,v79
v_mfma_f32_16x16x32_bf16 v[28:31],a[160:163],v[44:47],v[28:31]
s_nop 0
ds_read_b128 v[72:75],v2 offset:19456
v_fma_f32 v79,v79,v99,v99
v_mfma_f32_16x16x32_bf16 v[32:35],a[164:167],v[44:47],v[32:35]
s_barrier
v_mfma_f32_16x16x32_bf16 v[28:31],a[168:171],v[48:51],v[28:31]
ds_read_b128 v[44:47],v2 offset:28672
ds_read_b32 v16,v7 offset:0
v_mfma_f32_16x16x32_bf16 v[32:35],a[172:175],v[48:51],v[32:35]
v_rcp_f32 v79,v79
v_mfma_f32_16x16x32_bf16 v[28:31],a[176:179],v[52:55],v[28:31]
ds_read_b128 v[48:51],v2 offset:29696
v_mul_f32 v36,v36,v76
v_mfma_f32_16x16x32_bf16 v[32:35],a[180:183],v[52:55],v[32:35]
v_mul_f32 v37,v37,v77
v_mul_f32 v38,v38,v78
v_mfma_f32_16x16x32_bf16 v[28:31],a[184:187],v[56:59],v[28:31]
s_waitcnt lgkmcnt(1)
ds_read_b128 v[52:55],v2 offset:30720
v_mul_f32 v39,v39,v79
v_mfma_f32_16x16x32_bf16 v[32:35],a[188:191],v[56:59],v[32:35]
v_cmp_eq_u32 vcc,s60,v16
v_cvt_pk_bf16_f32 v84,v36,v37
v_cvt_pk_bf16_f32 v85,v38,v39
v_fma_f32 v40,-v96,v24,v40
v_mfma_f32_16x16x32_bf16 v[28:31],a[128:131],v[60:63],v[28:31]
s_cmp_eq_u32 vcc_lo,-1
ds_read_b128 v[56:59],v2 offset:31744
s_cselect_b32 s67,s86,s59
v_mfma_f32_16x16x32_bf16 v[32:35],a[132:135],v[60:63],v[32:35]
buffer_load_dwordx4 v0,s[36:39],s67 offen offset:0 sc1 lds
v_mfma_f32_16x16x32_bf16 v[28:31],a[136:139],v[64:67],v[28:31]
s_xor_b32 s63,s63,65536
ds_read_b128 v[60:63],v2 offset:24576
v_fma_f32 v41,-v96,v25,v41
v_mfma_f32_16x16x32_bf16 v[32:35],a[140:143],v[64:67],v[32:35]
s_add_u32 s53,s52,s55
v_fma_f32 v42,-v96,v26,v42
v_fma_f32 v43,-v96,v27,v43
v_mfma_f32_16x16x32_bf16 v[28:31],a[144:147],v[68:71],v[28:31]
s_cmp_eq_u32 s53,s56
ds_read_b128 v[64:67],v2 offset:25600
v_mul_f32 v80,v40,v40
v_mfma_f32_16x16x32_bf16 v[32:35],a[148:151],v[68:71],v[32:35]
buffer_load_dwordx4 v0,s[36:39],s67 offen offset:1024 sc1 lds
v_mfma_f32_16x16x32_bf16 v[28:31],a[152:155],v[72:75],v[28:31]
s_cselect_b32 s53,s57,s53
ds_read_b128 v[68:71],v2 offset:26624
v_mul_f32 v81,v41,v41
v_mfma_f32_16x16x32_bf16 v[32:35],a[156:159],v[72:75],v[32:35]
s_waitcnt lgkmcnt(3)
v_mul_f32 v82,v42,v42
v_mul_f32 v83,v43,v43
v_mfma_f32_16x16x32_bf16 v[28:31],a[224:227],v[44:47],v[28:31]
s_waitcnt vmcnt(7)
ds_read_b128 v[72:75],v2 offset:27648
v_fma_f32 v80,v98,v80,v97
v_mfma_f32_16x16x32_bf16 v[32:35],a[228:231],v[44:47],v[32:35]
s_barrier
v_mfma_f32_16x16x32_bf16 v[28:31],a[232:235],v[48:51],v[28:31]
s_cmp_eq_u32 s53,s58
ds_read_b128 v[44:47],v2 offset:36864
ds_read_b128 v[92:95],v8 offset:0
v_mfma_f32_16x16x32_bf16 v[32:35],a[236:239],v[48:51],v[32:35]
buffer_load_dwordx4 v0,s[36:39],s67 offen offset:2048 sc1 lds
v_mfma_f32_16x16x32_bf16 v[28:31],a[240:243],v[52:55],v[28:31]
s_cselect_b32 s53,s59,s53
ds_read_b128 v[48:51],v2 offset:37888
v_mul_f32 v80,v40,v80
v_mfma_f32_16x16x32_bf16 v[32:35],a[244:247],v[52:55],v[32:35]
s_cmp_ge_u32 s66,s59)"
R"(
v_exp_f32 v80,v80
v_mfma_f32_16x16x32_bf16 v[28:31],a[248:251],v[56:59],v[28:31]
v_fma_f32 v81,v98,v81,v97
v_fma_f32 v82,v98,v82,v97
v_fma_f32 v83,v98,v83,v97
v_mul_f32 v81,v41,v81
v_mul_f32 v82,v42,v82
v_mul_f32 v83,v43,v83
v_exp_f32 v81,v81
v_exp_f32 v82,v82
v_exp_f32 v83,v83
v_mfma_f32_16x16x32_bf16 v[32:35],a[252:255],v[56:59],v[32:35]
s_waitcnt lgkmcnt(3)
ds_read_b128 v[52:55],v2 offset:38912
buffer_load_dwordx4 v0,s[36:39],s67 offen offset:3072 sc1 lds
v_fma_f32 v80,v80,v99,v99
v_fma_f32 v81,v81,v99,v99
v_fma_f32 v82,v82,v99,v99
v_fma_f32 v83,v83,v99,v99
v_rcp_f32 v80,v80
v_rcp_f32 v81,v81
v_rcp_f32 v82,v82
v_rcp_f32 v83,v83
v_mfma_f32_16x16x32_bf16 v[28:31],a[192:195],v[60:63],v[28:31]
s_waitcnt lgkmcnt(2)
v_mul_f32 v40,v40,v80
v_mul_f32 v41,v41,v81
v_mul_f32 v42,v42,v82
v_mul_f32 v43,v43,v83
v_cvt_pk_bf16_f32 v86,v40,v41
v_cvt_pk_bf16_f32 v87,v42,v43
v_mfma_f32_16x16x32_bf16 v[32:35],a[196:199],v[60:63],v[32:35]
ds_read_b128 v[56:59],v2 offset:39936
buffer_store_dwordx4 v[84:87],v3,s[36:39],s73 offen offset:0 sc0 sc1
v_mfma_f32_16x16x32_bf16 v[28:31],a[200:203],v[64:67],v[28:31]
ds_read_b128 v[60:63],v2 offset:32768
v_mfma_f32_16x16x32_bf16 v[32:35],a[204:207],v[64:67],v[32:35]
v_mfma_f32_16x16x32_bf16 v[28:31],a[208:211],v[68:71],v[28:31]
ds_read_b128 v[64:67],v2 offset:33792
v_mfma_f32_16x16x32_bf16 v[32:35],a[212:215],v[68:71],v[32:35]
v_mfma_f32_16x16x32_bf16 v[28:31],a[216:219],v[72:75],v[28:31]
s_waitcnt lgkmcnt(2)
ds_read_b128 v[68:71],v2 offset:34816
v_mfma_f32_16x16x32_bf16 v[32:35],a[220:223],v[72:75],v[32:35]
s_cbranch_scc1 Lg1_slow1_%=
Lg1_d1w1_%=:
v_mfma_f32_16x16x32_bf16 v[36:39],a[32:35],v[44:47],0
s_add_u32 m0,s62,16384
ds_read_b128 v[72:75],v2 offset:35840
s_lshr_b32 s73,s48,0
v_mfma_f32_16x16x32_bf16 v[40:43],a[36:39],v[44:47],0
s_lshr_b32 s72,s67,6
s_lshr_b32 s71,s62,2
s_cmp_lt_u32 s67,4194304
v_mfma_f32_16x16x32_bf16 v[36:39],a[40:43],v[48:51],v[36:39]
buffer_load_dwordx4 v1,s[36:39],s67 offen offset:0 sc1 lds
v_mfma_f32_16x16x32_bf16 v[40:43],a[44:47],v[48:51],v[40:43]
s_cselect_b32 s72,s59,s72
ds_read_b128 v[44:47],v2 offset:45056
v_fma_f32 v28,-v92,v20,v28
v_mfma_f32_16x16x32_bf16 v[36:39],a[48:51],v[52:55],v[36:39]
s_mov_b64 s[44:45],s[46:47]
ds_read_b128 v[48:51],v2 offset:46080
v_fma_f32 v29,-v92,v21,v29
v_mfma_f32_16x16x32_bf16 v[40:43],a[52:55],v[52:55],v[40:43]
s_mov_b64 s[46:47],s[48:49]
v_fma_f32 v30,-v92,v22,v30
v_fma_f32 v31,-v92,v23,v31
v_mfma_f32_16x16x32_bf16 v[36:39],a[56:59],v[56:59],v[36:39]
buffer_load_dwordx4 v1,s[36:39],s67 offen offset:1024 sc1 lds
v_mfma_f32_16x16x32_bf16 v[40:43],a[60:63],v[56:59],v[40:43]
s_waitcnt lgkmcnt(2)
ds_read_b128 v[52:55],v2 offset:47104
v_mul_f32 v76,v28,v28
v_mfma_f32_16x16x32_bf16 v[36:39],a[0:3],v[60:63],v[36:39]
s_mov_b64 s[48:49],s[50:51]
ds_read_b128 v[56:59],v2 offset:48128
v_mul_f32 v77,v29,v29
v_mfma_f32_16x16x32_bf16 v[40:43],a[4:7],v[60:63],v[40:43]
v_mul_f32 v78,v30,v30
v_mul_f32 v79,v31,v31
v_mfma_f32_16x16x32_bf16 v[36:39],a[8:11],v[64:67],v[36:39]
buffer_load_dwordx4 v1,s[36:39],s67 offen offset:2048 sc1 lds
v_mfma_f32_16x16x32_bf16 v[40:43],a[12:15],v[64:67],v[40:43]
ds_read_b128 v[60:63],v2 offset:40960
v_fma_f32 v76,v94,v76,v93
v_mfma_f32_16x16x32_bf16 v[36:39],a[16:19],v[68:71],v[36:39]
ds_read_b128 v[64:67],v2 offset:41984
v_mul_f32 v76,v28,v76
v_mfma_f32_16x16x32_bf16 v[40:43],a[20:23],v[68:71],v[40:43]
v_exp_f32 v76,v76
v_mfma_f32_16x16x32_bf16 v[36:39],a[24:27],v[72:75],v[36:39]
buffer_load_dwordx4 v1,s[36:39],s67 offen offset:3072 sc1 lds
v_mfma_f32_16x16x32_bf16 v[40:43],a[28:31],v[72:75],v[40:43]
s_waitcnt lgkmcnt(2)
ds_read_b128 v[68:71],v2 offset:43008
s_add_u32 m0,s71,s88
v_mfma_f32_16x16x32_bf16 v[36:39],a[96:99],v[44:47],v[36:39]
s_xor_b32 s87,s63,65536
ds_read_b128 v[72:75],v2 offset:44032
buffer_load_dword v10,s[36:39],s72 offen offset:0 sc1 lds
s_waitcnt vmcnt(10)
v_mfma_f32_16x16x32_bf16 v[40:43],a[100:103],v[44:47],v[40:43]
s_barrier
v_mfma_f32_16x16x32_bf16 v[36:39],a[104:107],v[48:51],v[36:39]
s_cmp_ge_u32 s67,s59
ds_read_b128 v[44:47],v2 offset:53248
v_fma_f32 v76,v76,v95,v95
v_mfma_f32_16x16x32_bf16 v[40:43],a[108:111],v[48:51],v[40:43]
s_cselect_b32 s86,s50,s51
v_rcp_f32 v76,v76
v_mfma_f32_16x16x32_bf16 v[36:39],a[112:115],v[52:55],v[36:39]
s_cselect_b32 s87,s87,s64
ds_read_b128 v[48:51],v2 offset:54272
v_fma_f32 v77,v94,v77,v93
v_mfma_f32_16x16x32_bf16 v[40:43],a[116:119],v[52:55],v[40:43]
s_mov_b32 s62,s87
v_mul_f32 v77,v29,v77
v_fma_f32 v78,v94,v78,v93
v_mfma_f32_16x16x32_bf16 v[36:39],a[120:123],v[56:59],v[36:39]
s_mov_b32 m0,s62
ds_read_b128 v[52:55],v2 offset:55296
v_mul_f32 v78,v30,v78
v_mfma_f32_16x16x32_bf16 v[40:43],a[124:127],v[56:59],v[40:43]
s_waitcnt lgkmcnt(3)
v_exp_f32 v77,v77
v_mfma_f32_16x16x32_bf16 v[36:39],a[64:67],v[60:63],v[36:39]
s_mov_b64 s[50:51],s[52:53]
ds_read_b128 v[56:59],v2 offset:56320
v_fma_f32 v77,v77,v95,v95
v_mfma_f32_16x16x32_bf16 v[40:43],a[68:71],v[60:63],v[40:43]
v_exp_f32 v78,v78
v_mfma_f32_16x16x32_bf16 v[36:39],a[72:75],v[64:67],v[36:39]
ds_read_b128 v[60:63],v2 offset:49152
v_fma_f32 v78,v78,v95,v95
v_mfma_f32_16x16x32_bf16 v[40:43],a[76:79],v[64:67],v[40:43]
v_rcp_f32 v77,v77
v_mfma_f32_16x16x32_bf16 v[36:39],a[80:83],v[68:71],v[36:39]
ds_read_b128 v[64:67],v2 offset:50176
v_fma_f32 v79,v94,v79,v93
v_mfma_f32_16x16x32_bf16 v[40:43],a[84:87],v[68:71],v[40:43]
v_rcp_f32 v78,v78
v_mfma_f32_16x16x32_bf16 v[36:39],a[88:91],v[72:75],v[36:39]
ds_read_b128 v[68:71],v2 offset:51200
v_mul_f32 v79,v31,v79
v_mfma_f32_16x16x32_bf16 v[40:43],a[92:95],v[72:75],v[40:43]
s_waitcnt lgkmcnt(3)
v_exp_f32 v79,v79
v_mfma_f32_16x16x32_bf16 v[36:39],a[160:163],v[44:47],v[36:39]
s_nop 0
ds_read_b128 v[72:75],v2 offset:52224
v_fma_f32 v79,v79,v95,v95
v_mfma_f32_16x16x32_bf16 v[40:43],a[164:167],v[44:47],v[40:43]
s_barrier
v_mfma_f32_16x16x32_bf16 v[36:39],a[168:171],v[48:51],v[36:39]
ds_read_b128 v[44:47],v2 offset:61440
ds_read_b32 v16,v7 offset:256
v_mfma_f32_16x16x32_bf16 v[40:43],a[172:175],v[48:51],v[40:43]
v_rcp_f32 v79,v79
v_mfma_f32_16x16x32_bf16 v[36:39],a[176:179],v[52:55],v[36:39]
ds_read_b128 v[48:51],v2 offset:62464
v_mul_f32 v28,v28,v76
v_mfma_f32_16x16x32_bf16 v[40:43],a[180:183],v[52:55],v[40:43]
v_mul_f32 v29,v29,v77
v_mul_f32 v30,v30,v78
v_mfma_f32_16x16x32_bf16 v[36:39],a[184:187],v[56:59],v[36:39]
s_waitcnt lgkmcnt(1)
ds_read_b128 v[52:55],v2 offset:63488
v_mul_f32 v31,v31,v79
v_mfma_f32_16x16x32_bf16 v[40:43],a[188:191],v[56:59],v[40:43]
v_cmp_eq_u32 vcc,s60,v16
v_cvt_pk_bf16_f32 v84,v28,v29
v_cvt_pk_bf16_f32 v85,v30,v31
v_fma_f32 v32,-v92,v24,v32
v_mfma_f32_16x16x32_bf16 v[36:39],a[128:131],v[60:63],v[36:39]
s_cmp_eq_u32 vcc_lo,-1
ds_read_b128 v[56:59],v2 offset:64512
s_cselect_b32 s66,s86,s59
v_mfma_f32_16x16x32_bf16 v[40:43],a[132:135],v[60:63],v[40:43]
buffer_load_dwordx4 v0,s[36:39],s66 offen offset:0 sc1 lds
v_mfma_f32_16x16x32_bf16 v[36:39],a[136:139],v[64:67],v[36:39]
s_xor_b32 s64,s64,65536
ds_read_b128 v[60:63],v2 offset:57344
v_fma_f32 v33,-v92,v25,v33
v_mfma_f32_16x16x32_bf16 v[40:43],a[140:143],v[64:67],v[40:43]
s_add_u32 s74,s53,s55
v_fma_f32 v34,-v92,v26,v34
v_fma_f32 v35,-v92,v27,v35
v_mfma_f32_16x16x32_bf16 v[36:39],a[144:147],v[68:71],v[36:39]
s_cmp_eq_u32 s74,s56
ds_read_b128 v[64:67],v2 offset:58368
v_mul_f32 v80,v32,v32
v_mfma_f32_16x16x32_bf16 v[40:43],a[148:151],v[68:71],v[40:43]
buffer_load_dwordx4 v0,s[36:39],s66 offen offset:1024 sc1 lds
v_mfma_f32_16x16x32_bf16 v[36:39],a[152:155],v[72:75],v[36:39]
s_cselect_b32 s74,s57,s74
ds_read_b128 v[68:71],v2 offset:59392
v_mul_f32 v81,v33,v33
v_mfma_f32_16x16x32_bf16 v[40:43],a[156:159],v[72:75],v[40:43]
s_waitcnt lgkmcnt(3))"
R"(
v_mul_f32 v82,v34,v34
v_mul_f32 v83,v35,v35
v_mfma_f32_16x16x32_bf16 v[36:39],a[224:227],v[44:47],v[36:39]
s_waitcnt vmcnt(7)
ds_read_b128 v[72:75],v2 offset:60416
v_xor_b32 v2,65536,v2
v_mfma_f32_16x16x32_bf16 v[40:43],a[228:231],v[44:47],v[40:43]
s_barrier
v_mfma_f32_16x16x32_bf16 v[36:39],a[232:235],v[48:51],v[36:39]
s_cmp_eq_u32 s74,s58
ds_read_b128 v[44:47],v2 offset:4096
ds_read_b128 v[96:99],v8 offset:8192
v_mfma_f32_16x16x32_bf16 v[40:43],a[236:239],v[48:51],v[40:43]
buffer_load_dwordx4 v0,s[36:39],s66 offen offset:2048 sc1 lds
v_mfma_f32_16x16x32_bf16 v[36:39],a[240:243],v[52:55],v[36:39]
s_cselect_b32 s74,s59,s74
ds_read_b128 v[48:51],v2 offset:5120
v_fma_f32 v80,v94,v80,v93
v_mfma_f32_16x16x32_bf16 v[40:43],a[244:247],v[52:55],v[40:43]
s_mov_b32 s52,s74
v_mul_f32 v80,v32,v80
v_fma_f32 v81,v94,v81,v93
v_mfma_f32_16x16x32_bf16 v[36:39],a[248:251],v[56:59],v[36:39]
s_cmp_lt_u32 s67,s59
v_fma_f32 v82,v94,v82,v93
v_fma_f32 v83,v94,v83,v93
v_mul_f32 v81,v33,v81
v_mul_f32 v82,v34,v82
v_mul_f32 v83,v35,v83
v_exp_f32 v80,v80
v_exp_f32 v81,v81
v_exp_f32 v82,v82
v_exp_f32 v83,v83
v_mfma_f32_16x16x32_bf16 v[40:43],a[252:255],v[56:59],v[40:43]
s_waitcnt lgkmcnt(3)
ds_read_b128 v[52:55],v2 offset:6144
buffer_load_dwordx4 v0,s[36:39],s66 offen offset:3072 sc1 lds
v_fma_f32 v80,v80,v95,v95
v_fma_f32 v81,v81,v95,v95
v_fma_f32 v82,v82,v95,v95
v_fma_f32 v83,v83,v95,v95
v_rcp_f32 v80,v80
v_rcp_f32 v81,v81
v_rcp_f32 v82,v82
v_rcp_f32 v83,v83
v_mfma_f32_16x16x32_bf16 v[36:39],a[192:195],v[60:63],v[36:39]
s_waitcnt lgkmcnt(2)
v_mul_f32 v32,v32,v80
v_mul_f32 v33,v33,v81
v_mul_f32 v34,v34,v82
v_mul_f32 v35,v35,v83
v_cvt_pk_bf16_f32 v86,v32,v33
v_cvt_pk_bf16_f32 v87,v34,v35
v_mfma_f32_16x16x32_bf16 v[40:43],a[196:199],v[60:63],v[40:43]
ds_read_b128 v[56:59],v2 offset:7168
buffer_store_dwordx4 v[84:87],v3,s[36:39],s73 offen offset:0 sc0 sc1
v_xor_b32 v8,16384,v8
v_mfma_f32_16x16x32_bf16 v[36:39],a[200:203],v[64:67],v[36:39]
ds_read_b128 v[60:63],v2 offset:0
v_mfma_f32_16x16x32_bf16 v[40:43],a[204:207],v[64:67],v[40:43]
v_mfma_f32_16x16x32_bf16 v[36:39],a[208:211],v[68:71],v[36:39]
ds_read_b128 v[64:67],v2 offset:1024
v_mfma_f32_16x16x32_bf16 v[40:43],a[212:215],v[68:71],v[40:43]
v_mfma_f32_16x16x32_bf16 v[36:39],a[216:219],v[72:75],v[36:39]
s_waitcnt lgkmcnt(2)
ds_read_b128 v[68:71],v2 offset:2048
v_mfma_f32_16x16x32_bf16 v[40:43],a[220:223],v[72:75],v[40:43]
s_cbranch_scc1 Lg1_d0w1_%=
s_branch Lg1_slow0_%=
Lg1_d0w2_%=:
v_mfma_f32_16x16x32_bf16 v[28:31],a[64:67],v[44:47],0
s_add_u32 m0,s62,16384
ds_read_b128 v[72:75],v2 offset:15360
s_lshr_b32 s73,s47,0
v_mfma_f32_16x16x32_bf16 v[32:35],a[68:71],v[44:47],0
s_lshr_b32 s72,s66,6
s_lshr_b32 s71,s62,2
s_cmp_lt_u32 s66,4194304
v_mfma_f32_16x16x32_bf16 v[28:31],a[72:75],v[48:51],v[28:31]
s_cselect_b32 s72,s59,s72
ds_read_b128 v[44:47],v2 offset:0
v_mfma_f32_16x16x32_bf16 v[32:35],a[76:79],v[48:51],v[32:35]
buffer_load_dwordx4 v1,s[36:39],s66 offen offset:0 sc1 lds
v_mfma_f32_16x16x32_bf16 v[28:31],a[80:83],v[52:55],v[28:31]
ds_read_b128 v[48:51],v2 offset:1024
v_fma_f32 v36,-v96,v20,v36
v_mfma_f32_16x16x32_bf16 v[32:35],a[84:87],v[52:55],v[32:35]
v_fma_f32 v37,-v96,v21,v37
v_fma_f32 v38,-v96,v22,v38
v_mfma_f32_16x16x32_bf16 v[28:31],a[88:91],v[56:59],v[28:31]
ds_read_b128 v[52:55],v2 offset:2048
v_fma_f32 v39,-v96,v23,v39
v_mfma_f32_16x16x32_bf16 v[32:35],a[92:95],v[56:59],v[32:35]
s_waitcnt lgkmcnt(3)
buffer_load_dwordx4 v1,s[36:39],s66 offen offset:1024 sc1 lds
v_mfma_f32_16x16x32_bf16 v[28:31],a[96:99],v[60:63],v[28:31]
ds_read_b128 v[56:59],v2 offset:3072
v_mul_f32 v76,v36,v36
v_mfma_f32_16x16x32_bf16 v[32:35],a[100:103],v[60:63],v[32:35]
v_mul_f32 v77,v37,v37
v_mul_f32 v78,v38,v38
v_mfma_f32_16x16x32_bf16 v[28:31],a[104:107],v[64:67],v[28:31]
ds_read_b128 v[60:63],v2 offset:4096
v_mul_f32 v79,v39,v39
v_mfma_f32_16x16x32_bf16 v[32:35],a[108:111],v[64:67],v[32:35]
buffer_load_dwordx4 v1,s[36:39],s66 offen offset:2048 sc1 lds
v_mfma_f32_16x16x32_bf16 v[28:31],a[112:115],v[68:71],v[28:31]
ds_read_b128 v[64:67],v2 offset:5120
v_fma_f32 v76,v98,v76,v97
v_mfma_f32_16x16x32_bf16 v[32:35],a[116:119],v[68:71],v[32:35]
v_mul_f32 v76,v36,v76
v_fma_f32 v77,v98,v77,v97
v_mfma_f32_16x16x32_bf16 v[28:31],a[120:123],v[72:75],v[28:31]
ds_read_b128 v[68:71],v2 offset:6144
v_mul_f32 v77,v37,v77
v_mfma_f32_16x16x32_bf16 v[32:35],a[124:127],v[72:75],v[32:35]
s_waitcnt lgkmcnt(3)
buffer_load_dwordx4 v1,s[36:39],s66 offen offset:3072 sc1 lds
s_add_u32 m0,s71,s88
v_mfma_f32_16x16x32_bf16 v[28:31],a[0:3],v[44:47],v[28:31]
s_xor_b32 s87,s64,65536
ds_read_b128 v[72:75],v2 offset:7168
buffer_load_dword v10,s[36:39],s72 offen offset:0 sc1 lds
s_waitcnt vmcnt(10)
v_mfma_f32_16x16x32_bf16 v[32:35],a[4:7],v[44:47],v[32:35]
s_barrier
v_mfma_f32_16x16x32_bf16 v[28:31],a[8:11],v[48:51],v[28:31]
s_cmp_ge_u32 s66,s59
ds_read_b128 v[44:47],v2 offset:24576
v_fma_f32 v78,v98,v78,v97
v_mfma_f32_16x16x32_bf16 v[32:35],a[12:15],v[48:51],v[32:35]
s_cselect_b32 s86,s49,s50
v_exp_f32 v76,v76
v_mfma_f32_16x16x32_bf16 v[28:31],a[16:19],v[52:55],v[28:31]
s_cselect_b32 s87,s87,s63
ds_read_b128 v[48:51],v2 offset:25600
v_mul_f32 v78,v38,v78
v_mfma_f32_16x16x32_bf16 v[32:35],a[20:23],v[52:55],v[32:35]
s_mov_b32 s62,s87
v_exp_f32 v77,v77
v_mfma_f32_16x16x32_bf16 v[28:31],a[24:27],v[56:59],v[28:31]
s_mov_b32 m0,s62
ds_read_b128 v[52:55],v2 offset:26624
v_fma_f32 v76,v76,v99,v99
v_mfma_f32_16x16x32_bf16 v[32:35],a[28:31],v[56:59],v[32:35]
s_waitcnt lgkmcnt(3)
v_exp_f32 v78,v78
v_mfma_f32_16x16x32_bf16 v[28:31],a[32:35],v[60:63],v[28:31]
ds_read_b128 v[56:59],v2 offset:27648
v_fma_f32 v77,v77,v99,v99
v_mfma_f32_16x16x32_bf16 v[32:35],a[36:39],v[60:63],v[32:35]
v_rcp_f32 v76,v76
v_mfma_f32_16x16x32_bf16 v[28:31],a[40:43],v[64:67],v[28:31]
ds_read_b128 v[60:63],v2 offset:28672
v_fma_f32 v78,v78,v99,v99
v_mfma_f32_16x16x32_bf16 v[32:35],a[44:47],v[64:67],v[32:35]
v_rcp_f32 v77,v77
v_mfma_f32_16x16x32_bf16 v[28:31],a[48:51],v[68:71],v[28:31]
ds_read_b128 v[64:67],v2 offset:29696
v_fma_f32 v79,v98,v79,v97
v_mfma_f32_16x16x32_bf16 v[32:35],a[52:55],v[68:71],v[32:35]
v_rcp_f32 v78,v78
v_mfma_f32_16x16x32_bf16 v[28:31],a[56:59],v[72:75],v[28:31]
ds_read_b128 v[68:71],v2 offset:30720
v_mul_f32 v79,v39,v79
v_mfma_f32_16x16x32_bf16 v[32:35],a[60:63],v[72:75],v[32:35]
s_waitcnt lgkmcnt(3)
v_exp_f32 v79,v79
v_mfma_f32_16x16x32_bf16 v[28:31],a[192:195],v[44:47],v[28:31]
s_nop 0
ds_read_b128 v[72:75],v2 offset:31744
v_fma_f32 v79,v79,v99,v99
v_mfma_f32_16x16x32_bf16 v[32:35],a[196:199],v[44:47],v[32:35]
s_barrier
v_mfma_f32_16x16x32_bf16 v[28:31],a[200:203],v[48:51],v[28:31]
ds_read_b128 v[44:47],v2 offset:16384
ds_read_b32 v16,v7 offset:0
v_mfma_f32_16x16x32_bf16 v[32:35],a[204:207],v[48:51],v[32:35]
v_rcp_f32 v79,v79
v_mfma_f32_16x16x32_bf16 v[28:31],a[208:211],v[52:55],v[28:31]
ds_read_b128 v[48:51],v2 offset:17408
v_mul_f32 v36,v36,v76
v_mfma_f32_16x16x32_bf16 v[32:35],a[212:215],v[52:55],v[32:35]
v_mul_f32 v37,v37,v77
v_mul_f32 v38,v38,v78
v_mfma_f32_16x16x32_bf16 v[28:31],a[216:219],v[56:59],v[28:31]
s_waitcnt lgkmcnt(1)
ds_read_b128 v[52:55],v2 offset:18432
v_mul_f32 v39,v39,v79
v_mfma_f32_16x16x32_bf16 v[32:35],a[220:223],v[56:59],v[32:35]
v_cmp_eq_u32 vcc,s60,v16
v_cvt_pk_bf16_f32 v84,v36,v37
v_cvt_pk_bf16_f32 v85,v38,v39
v_fma_f32 v40,-v96,v24,v40
v_mfma_f32_16x16x32_bf16 v[28:31],a[224:227],v[60:63],v[28:31]
s_cmp_eq_u32 vcc_lo,-1
ds_read_b128 v[56:59],v2 offset:19456
v_fma_f32 v41,-v96,v25,v41
v_mfma_f32_16x16x32_bf16 v[32:35],a[228:231],v[60:63],v[32:35]
s_cselect_b32 s67,s86,s59)"
R"(
v_fma_f32 v42,-v96,v26,v42
v_fma_f32 v43,-v96,v27,v43
v_mfma_f32_16x16x32_bf16 v[28:31],a[232:235],v[64:67],v[28:31]
buffer_load_dwordx4 v0,s[36:39],s67 offen offset:0 sc1 lds
v_mfma_f32_16x16x32_bf16 v[32:35],a[236:239],v[64:67],v[32:35]
s_xor_b32 s63,s63,65536
ds_read_b128 v[60:63],v2 offset:20480
v_mul_f32 v80,v40,v40
v_mfma_f32_16x16x32_bf16 v[28:31],a[240:243],v[68:71],v[28:31]
s_add_u32 s53,s52,s55
ds_read_b128 v[64:67],v2 offset:21504
v_mul_f32 v81,v41,v41
v_mfma_f32_16x16x32_bf16 v[32:35],a[244:247],v[68:71],v[32:35]
s_cmp_eq_u32 s53,s56
v_mul_f32 v82,v42,v42
v_mul_f32 v83,v43,v43
v_mfma_f32_16x16x32_bf16 v[28:31],a[248:251],v[72:75],v[28:31]
buffer_load_dwordx4 v0,s[36:39],s67 offen offset:1024 sc1 lds
v_mfma_f32_16x16x32_bf16 v[32:35],a[252:255],v[72:75],v[32:35]
s_waitcnt lgkmcnt(2)
ds_read_b128 v[68:71],v2 offset:22528
v_fma_f32 v80,v98,v80,v97
v_mfma_f32_16x16x32_bf16 v[28:31],a[128:131],v[44:47],v[28:31]
s_waitcnt vmcnt(7)
ds_read_b128 v[72:75],v2 offset:23552
v_mul_f32 v80,v40,v80
v_mfma_f32_16x16x32_bf16 v[32:35],a[132:135],v[44:47],v[32:35]
s_barrier
v_mfma_f32_16x16x32_bf16 v[28:31],a[136:139],v[48:51],v[28:31]
s_cselect_b32 s53,s57,s53
ds_read_b128 v[44:47],v2 offset:40960
ds_read_b128 v[92:95],v8 offset:0
v_mfma_f32_16x16x32_bf16 v[32:35],a[140:143],v[48:51],v[32:35]
s_cmp_eq_u32 s53,s58
v_exp_f32 v80,v80
v_mfma_f32_16x16x32_bf16 v[28:31],a[144:147],v[52:55],v[28:31]
buffer_load_dwordx4 v0,s[36:39],s67 offen offset:2048 sc1 lds
v_mfma_f32_16x16x32_bf16 v[32:35],a[148:151],v[52:55],v[32:35]
s_cselect_b32 s53,s59,s53
ds_read_b128 v[48:51],v2 offset:41984
v_fma_f32 v80,v80,v99,v99
v_mfma_f32_16x16x32_bf16 v[28:31],a[152:155],v[56:59],v[28:31]
s_cmp_ge_u32 s66,s59
ds_read_b128 v[52:55],v2 offset:43008
v_fma_f32 v81,v98,v81,v97
v_mfma_f32_16x16x32_bf16 v[32:35],a[156:159],v[56:59],v[32:35]
s_waitcnt lgkmcnt(4)
v_fma_f32 v82,v98,v82,v97
v_fma_f32 v83,v98,v83,v97
v_mul_f32 v81,v41,v81
v_mul_f32 v82,v42,v82
v_mul_f32 v83,v43,v83
v_exp_f32 v81,v81
v_exp_f32 v82,v82
v_exp_f32 v83,v83
v_mfma_f32_16x16x32_bf16 v[28:31],a[160:163],v[60:63],v[28:31]
s_waitcnt lgkmcnt(2)
buffer_load_dwordx4 v0,s[36:39],s67 offen offset:3072 sc1 lds
v_fma_f32 v81,v81,v99,v99
v_fma_f32 v82,v82,v99,v99
v_fma_f32 v83,v83,v99,v99
v_rcp_f32 v80,v80
v_rcp_f32 v81,v81
v_rcp_f32 v82,v82
v_rcp_f32 v83,v83
v_mfma_f32_16x16x32_bf16 v[32:35],a[164:167],v[60:63],v[32:35]
ds_read_b128 v[56:59],v2 offset:44032
v_mul_f32 v40,v40,v80
v_mul_f32 v41,v41,v81
v_mul_f32 v42,v42,v82
v_mul_f32 v43,v43,v83
v_cvt_pk_bf16_f32 v86,v40,v41
v_cvt_pk_bf16_f32 v87,v42,v43
v_mfma_f32_16x16x32_bf16 v[28:31],a[168:171],v[64:67],v[28:31]
buffer_store_dwordx4 v[84:87],v3,s[36:39],s73 offen offset:0 sc0 sc1
ds_read_b128 v[60:63],v2 offset:45056
v_mfma_f32_16x16x32_bf16 v[32:35],a[172:175],v[64:67],v[32:35]
v_mfma_f32_16x16x32_bf16 v[28:31],a[176:179],v[68:71],v[28:31]
ds_read_b128 v[64:67],v2 offset:46080
v_mfma_f32_16x16x32_bf16 v[32:35],a[180:183],v[68:71],v[32:35]
v_mfma_f32_16x16x32_bf16 v[28:31],a[184:187],v[72:75],v[28:31]
s_waitcnt lgkmcnt(2)
ds_read_b128 v[68:71],v2 offset:47104
v_mfma_f32_16x16x32_bf16 v[32:35],a[188:191],v[72:75],v[32:35]
s_cbranch_scc1 Lg1_slow1_%=
Lg1_d1w2_%=:
v_mfma_f32_16x16x32_bf16 v[36:39],a[64:67],v[44:47],0
s_add_u32 m0,s62,16384
ds_read_b128 v[72:75],v2 offset:48128
s_lshr_b32 s73,s48,0
v_mfma_f32_16x16x32_bf16 v[40:43],a[68:71],v[44:47],0
s_lshr_b32 s72,s67,6
s_lshr_b32 s71,s62,2
s_cmp_lt_u32 s67,4194304
v_mfma_f32_16x16x32_bf16 v[36:39],a[72:75],v[48:51],v[36:39]
s_cselect_b32 s72,s59,s72
ds_read_b128 v[44:47],v2 offset:32768
s_mov_b64 s[44:45],s[46:47]
v_mfma_f32_16x16x32_bf16 v[40:43],a[76:79],v[48:51],v[40:43]
buffer_load_dwordx4 v1,s[36:39],s67 offen offset:0 sc1 lds
v_mfma_f32_16x16x32_bf16 v[36:39],a[80:83],v[52:55],v[36:39]
s_mov_b64 s[46:47],s[48:49]
ds_read_b128 v[48:51],v2 offset:33792
v_fma_f32 v28,-v92,v20,v28
v_mfma_f32_16x16x32_bf16 v[40:43],a[84:87],v[52:55],v[40:43]
s_mov_b64 s[48:49],s[50:51]
v_fma_f32 v29,-v92,v21,v29
v_fma_f32 v30,-v92,v22,v30
v_mfma_f32_16x16x32_bf16 v[36:39],a[88:91],v[56:59],v[36:39]
ds_read_b128 v[52:55],v2 offset:34816
v_fma_f32 v31,-v92,v23,v31
v_mfma_f32_16x16x32_bf16 v[40:43],a[92:95],v[56:59],v[40:43]
s_waitcnt lgkmcnt(3)
buffer_load_dwordx4 v1,s[36:39],s67 offen offset:1024 sc1 lds
v_mfma_f32_16x16x32_bf16 v[36:39],a[96:99],v[60:63],v[36:39]
ds_read_b128 v[56:59],v2 offset:35840
v_mul_f32 v76,v28,v28
v_mfma_f32_16x16x32_bf16 v[40:43],a[100:103],v[60:63],v[40:43]
v_mul_f32 v77,v29,v29
v_mul_f32 v78,v30,v30
v_mfma_f32_16x16x32_bf16 v[36:39],a[104:107],v[64:67],v[36:39]
ds_read_b128 v[60:63],v2 offset:36864
v_mul_f32 v79,v31,v31
v_mfma_f32_16x16x32_bf16 v[40:43],a[108:111],v[64:67],v[40:43]
buffer_load_dwordx4 v1,s[36:39],s67 offen offset:2048 sc1 lds
v_mfma_f32_16x16x32_bf16 v[36:39],a[112:115],v[68:71],v[36:39]
ds_read_b128 v[64:67],v2 offset:37888
v_fma_f32 v76,v94,v76,v93
v_mfma_f32_16x16x32_bf16 v[40:43],a[116:119],v[68:71],v[40:43]
v_mul_f32 v76,v28,v76
v_fma_f32 v77,v94,v77,v93
v_mfma_f32_16x16x32_bf16 v[36:39],a[120:123],v[72:75],v[36:39]
ds_read_b128 v[68:71],v2 offset:38912
v_mul_f32 v77,v29,v77
v_mfma_f32_16x16x32_bf16 v[40:43],a[124:127],v[72:75],v[40:43]
s_waitcnt lgkmcnt(3)
buffer_load_dwordx4 v1,s[36:39],s67 offen offset:3072 sc1 lds
s_add_u32 m0,s71,s88
v_mfma_f32_16x16x32_bf16 v[36:39],a[0:3],v[44:47],v[36:39]
s_xor_b32 s87,s63,65536
ds_read_b128 v[72:75],v2 offset:39936
buffer_load_dword v10,s[36:39],s72 offen offset:0 sc1 lds
s_waitcnt vmcnt(10)
v_mfma_f32_16x16x32_bf16 v[40:43],a[4:7],v[44:47],v[40:43]
s_barrier
v_mfma_f32_16x16x32_bf16 v[36:39],a[8:11],v[48:51],v[36:39]
s_cmp_ge_u32 s67,s59
ds_read_b128 v[44:47],v2 offset:57344
v_fma_f32 v78,v94,v78,v93
v_mfma_f32_16x16x32_bf16 v[40:43],a[12:15],v[48:51],v[40:43]
s_cselect_b32 s86,s50,s51
v_exp_f32 v76,v76
v_mfma_f32_16x16x32_bf16 v[36:39],a[16:19],v[52:55],v[36:39]
s_cselect_b32 s87,s87,s64
ds_read_b128 v[48:51],v2 offset:58368
v_mul_f32 v78,v30,v78
v_mfma_f32_16x16x32_bf16 v[40:43],a[20:23],v[52:55],v[40:43]
s_mov_b32 s62,s87
v_exp_f32 v77,v77
v_mfma_f32_16x16x32_bf16 v[36:39],a[24:27],v[56:59],v[36:39]
s_mov_b32 m0,s62
ds_read_b128 v[52:55],v2 offset:59392
v_fma_f32 v76,v76,v95,v95
v_mfma_f32_16x16x32_bf16 v[40:43],a[28:31],v[56:59],v[40:43]
s_waitcnt lgkmcnt(3)
v_exp_f32 v78,v78
v_mfma_f32_16x16x32_bf16 v[36:39],a[32:35],v[60:63],v[36:39]
s_mov_b64 s[50:51],s[52:53]
ds_read_b128 v[56:59],v2 offset:60416
v_fma_f32 v77,v77,v95,v95
v_mfma_f32_16x16x32_bf16 v[40:43],a[36:39],v[60:63],v[40:43]
v_rcp_f32 v76,v76
v_mfma_f32_16x16x32_bf16 v[36:39],a[40:43],v[64:67],v[36:39]
ds_read_b128 v[60:63],v2 offset:61440
v_fma_f32 v78,v78,v95,v95
v_mfma_f32_16x16x32_bf16 v[40:43],a[44:47],v[64:67],v[40:43]
v_rcp_f32 v77,v77
v_mfma_f32_16x16x32_bf16 v[36:39],a[48:51],v[68:71],v[36:39]
ds_read_b128 v[64:67],v2 offset:62464
v_fma_f32 v79,v94,v79,v93
v_mfma_f32_16x16x32_bf16 v[40:43],a[52:55],v[68:71],v[40:43]
v_rcp_f32 v78,v78
v_mfma_f32_16x16x32_bf16 v[36:39],a[56:59],v[72:75],v[36:39]
ds_read_b128 v[68:71],v2 offset:63488
v_mul_f32 v79,v31,v79
v_mfma_f32_16x16x32_bf16 v[40:43],a[60:63],v[72:75],v[40:43]
s_waitcnt lgkmcnt(3)
v_exp_f32 v79,v79
v_mfma_f32_16x16x32_bf16 v[36:39],a[192:195],v[44:47],v[36:39]
s_nop 0
ds_read_b128 v[72:75],v2 offset:64512
v_fma_f32 v79,v79,v95,v95
v_mfma_f32_16x16x32_bf16 v[40:43],a[196:199],v[44:47],v[40:43]
s_barrier
v_mfma_f32_16x16x32_bf16 v[36:39],a[200:203],v[48:51],v[36:39]
ds_read_b128 v[44:47],v2 offset:49152
ds_read_b32 v16,v7 offset:256
v_mfma_f32_16x16x32_bf16 v[40:43],a[204:207],v[48:51],v[40:43]
v_rcp_f32 v79,v79
v_mfma_f32_16x16x32_bf16 v[36:39],a[208:211],v[52:55],v[36:39])"
R"(
ds_read_b128 v[48:51],v2 offset:50176
v_mul_f32 v28,v28,v76
v_mfma_f32_16x16x32_bf16 v[40:43],a[212:215],v[52:55],v[40:43]
v_mul_f32 v29,v29,v77
v_mul_f32 v30,v30,v78
v_mfma_f32_16x16x32_bf16 v[36:39],a[216:219],v[56:59],v[36:39]
s_waitcnt lgkmcnt(1)
ds_read_b128 v[52:55],v2 offset:51200
v_mul_f32 v31,v31,v79
v_mfma_f32_16x16x32_bf16 v[40:43],a[220:223],v[56:59],v[40:43]
v_cmp_eq_u32 vcc,s60,v16
v_cvt_pk_bf16_f32 v84,v28,v29
v_cvt_pk_bf16_f32 v85,v30,v31
v_fma_f32 v32,-v92,v24,v32
v_mfma_f32_16x16x32_bf16 v[36:39],a[224:227],v[60:63],v[36:39]
s_cmp_eq_u32 vcc_lo,-1
ds_read_b128 v[56:59],v2 offset:52224
v_fma_f32 v33,-v92,v25,v33
v_mfma_f32_16x16x32_bf16 v[40:43],a[228:231],v[60:63],v[40:43]
s_cselect_b32 s66,s86,s59
v_fma_f32 v34,-v92,v26,v34
v_fma_f32 v35,-v92,v27,v35
v_mfma_f32_16x16x32_bf16 v[36:39],a[232:235],v[64:67],v[36:39]
buffer_load_dwordx4 v0,s[36:39],s66 offen offset:0 sc1 lds
v_mfma_f32_16x16x32_bf16 v[40:43],a[236:239],v[64:67],v[40:43]
s_xor_b32 s64,s64,65536
ds_read_b128 v[60:63],v2 offset:53248
v_mul_f32 v80,v32,v32
v_mfma_f32_16x16x32_bf16 v[36:39],a[240:243],v[68:71],v[36:39]
s_add_u32 s74,s53,s55
ds_read_b128 v[64:67],v2 offset:54272
v_mul_f32 v81,v33,v33
v_mfma_f32_16x16x32_bf16 v[40:43],a[244:247],v[68:71],v[40:43]
s_cmp_eq_u32 s74,s56
v_mul_f32 v82,v34,v34
v_mul_f32 v83,v35,v35
v_mfma_f32_16x16x32_bf16 v[36:39],a[248:251],v[72:75],v[36:39]
buffer_load_dwordx4 v0,s[36:39],s66 offen offset:1024 sc1 lds
v_mfma_f32_16x16x32_bf16 v[40:43],a[252:255],v[72:75],v[40:43]
s_waitcnt lgkmcnt(2)
ds_read_b128 v[68:71],v2 offset:55296
v_fma_f32 v80,v94,v80,v93
v_mfma_f32_16x16x32_bf16 v[36:39],a[128:131],v[44:47],v[36:39]
s_waitcnt vmcnt(7)
ds_read_b128 v[72:75],v2 offset:56320
v_xor_b32 v2,65536,v2
v_mfma_f32_16x16x32_bf16 v[40:43],a[132:135],v[44:47],v[40:43]
s_barrier
v_mfma_f32_16x16x32_bf16 v[36:39],a[136:139],v[48:51],v[36:39]
s_cselect_b32 s74,s57,s74
ds_read_b128 v[44:47],v2 offset:8192
ds_read_b128 v[96:99],v8 offset:8192
v_mfma_f32_16x16x32_bf16 v[40:43],a[140:143],v[48:51],v[40:43]
s_cmp_eq_u32 s74,s58
v_mul_f32 v80,v32,v80
v_fma_f32 v81,v94,v81,v93
v_mfma_f32_16x16x32_bf16 v[36:39],a[144:147],v[52:55],v[36:39]
buffer_load_dwordx4 v0,s[36:39],s66 offen offset:2048 sc1 lds
v_mfma_f32_16x16x32_bf16 v[40:43],a[148:151],v[52:55],v[40:43]
s_cselect_b32 s74,s59,s74
ds_read_b128 v[48:51],v2 offset:9216
v_mul_f32 v81,v33,v81
v_mfma_f32_16x16x32_bf16 v[36:39],a[152:155],v[56:59],v[36:39]
s_mov_b32 s52,s74
ds_read_b128 v[52:55],v2 offset:10240
v_fma_f32 v82,v94,v82,v93
v_mfma_f32_16x16x32_bf16 v[40:43],a[156:159],v[56:59],v[40:43]
s_waitcnt lgkmcnt(4)
v_fma_f32 v83,v94,v83,v93
v_mul_f32 v82,v34,v82
v_mul_f32 v83,v35,v83
v_exp_f32 v80,v80
v_exp_f32 v81,v81
v_exp_f32 v82,v82
v_exp_f32 v83,v83
v_mfma_f32_16x16x32_bf16 v[36:39],a[160:163],v[60:63],v[36:39]
s_waitcnt lgkmcnt(2)
buffer_load_dwordx4 v0,s[36:39],s66 offen offset:3072 sc1 lds
v_fma_f32 v80,v80,v95,v95
v_fma_f32 v81,v81,v95,v95
v_fma_f32 v82,v82,v95,v95
v_fma_f32 v83,v83,v95,v95
v_rcp_f32 v80,v80
v_rcp_f32 v81,v81
v_rcp_f32 v82,v82
v_rcp_f32 v83,v83
v_mfma_f32_16x16x32_bf16 v[40:43],a[164:167],v[60:63],v[40:43]
s_cmp_lt_u32 s67,s59
ds_read_b128 v[56:59],v2 offset:11264
v_mul_f32 v32,v32,v80
v_mul_f32 v33,v33,v81
v_mul_f32 v34,v34,v82
v_mul_f32 v35,v35,v83
v_cvt_pk_bf16_f32 v86,v32,v33
v_cvt_pk_bf16_f32 v87,v34,v35
v_mfma_f32_16x16x32_bf16 v[36:39],a[168:171],v[64:67],v[36:39]
buffer_store_dwordx4 v[84:87],v3,s[36:39],s73 offen offset:0 sc0 sc1
ds_read_b128 v[60:63],v2 offset:12288
v_xor_b32 v8,16384,v8
v_mfma_f32_16x16x32_bf16 v[40:43],a[172:175],v[64:67],v[40:43]
v_mfma_f32_16x16x32_bf16 v[36:39],a[176:179],v[68:71],v[36:39]
ds_read_b128 v[64:67],v2 offset:13312
v_mfma_f32_16x16x32_bf16 v[40:43],a[180:183],v[68:71],v[40:43]
v_mfma_f32_16x16x32_bf16 v[36:39],a[184:187],v[72:75],v[36:39]
s_waitcnt lgkmcnt(2)
ds_read_b128 v[68:71],v2 offset:14336
v_mfma_f32_16x16x32_bf16 v[40:43],a[188:191],v[72:75],v[40:43]
s_cbranch_scc1 Lg1_d0w2_%=
s_branch Lg1_slow0_%=
Lg1_d0w3_%=:
v_mfma_f32_16x16x32_bf16 v[28:31],a[96:99],v[44:47],0
s_add_u32 m0,s62,16384
ds_read_b128 v[72:75],v2 offset:11264
s_lshr_b32 s73,s47,0
v_mfma_f32_16x16x32_bf16 v[32:35],a[100:103],v[44:47],0
s_lshr_b32 s72,s66,6
s_lshr_b32 s71,s62,2
s_cmp_lt_u32 s66,4194304
v_mfma_f32_16x16x32_bf16 v[28:31],a[104:107],v[48:51],v[28:31]
s_cselect_b32 s72,s59,s72
ds_read_b128 v[44:47],v2 offset:4096
v_mfma_f32_16x16x32_bf16 v[32:35],a[108:111],v[48:51],v[32:35]
v_fma_f32 v36,-v96,v20,v36
v_fma_f32 v37,-v96,v21,v37
v_mfma_f32_16x16x32_bf16 v[28:31],a[112:115],v[52:55],v[28:31]
buffer_load_dwordx4 v1,s[36:39],s66 offen offset:0 sc1 lds
v_mfma_f32_16x16x32_bf16 v[32:35],a[116:119],v[52:55],v[32:35]
ds_read_b128 v[48:51],v2 offset:5120
v_fma_f32 v38,-v96,v22,v38
v_mfma_f32_16x16x32_bf16 v[28:31],a[120:123],v[56:59],v[28:31]
ds_read_b128 v[52:55],v2 offset:6144
v_fma_f32 v39,-v96,v23,v39
v_mfma_f32_16x16x32_bf16 v[32:35],a[124:127],v[56:59],v[32:35]
s_waitcnt lgkmcnt(3)
v_mul_f32 v76,v36,v36
v_mul_f32 v77,v37,v37
v_mfma_f32_16x16x32_bf16 v[28:31],a[64:67],v[60:63],v[28:31]
buffer_load_dwordx4 v1,s[36:39],s66 offen offset:1024 sc1 lds
v_mfma_f32_16x16x32_bf16 v[32:35],a[68:71],v[60:63],v[32:35]
ds_read_b128 v[56:59],v2 offset:7168
v_mul_f32 v78,v38,v38
v_mfma_f32_16x16x32_bf16 v[28:31],a[72:75],v[64:67],v[28:31]
ds_read_b128 v[60:63],v2 offset:0
v_mul_f32 v79,v39,v39
v_mfma_f32_16x16x32_bf16 v[32:35],a[76:79],v[64:67],v[32:35]
v_fma_f32 v76,v98,v76,v97
v_mul_f32 v76,v36,v76
v_mfma_f32_16x16x32_bf16 v[28:31],a[80:83],v[68:71],v[28:31]
buffer_load_dwordx4 v1,s[36:39],s66 offen offset:2048 sc1 lds
v_mfma_f32_16x16x32_bf16 v[32:35],a[84:87],v[68:71],v[32:35]
ds_read_b128 v[64:67],v2 offset:1024
v_fma_f32 v77,v98,v77,v97
v_mfma_f32_16x16x32_bf16 v[28:31],a[88:91],v[72:75],v[28:31]
ds_read_b128 v[68:71],v2 offset:2048
v_mul_f32 v77,v37,v77
v_mfma_f32_16x16x32_bf16 v[32:35],a[92:95],v[72:75],v[32:35]
s_waitcnt lgkmcnt(3)
v_exp_f32 v76,v76
v_mfma_f32_16x16x32_bf16 v[28:31],a[32:35],v[44:47],v[28:31]
s_waitcnt vmcnt(9)
ds_read_b128 v[72:75],v2 offset:3072
v_fma_f32 v76,v76,v99,v99
v_mfma_f32_16x16x32_bf16 v[32:35],a[36:39],v[44:47],v[32:35]
s_barrier
v_mfma_f32_16x16x32_bf16 v[28:31],a[40:43],v[48:51],v[28:31]
buffer_load_dwordx4 v1,s[36:39],s66 offen offset:3072 sc1 lds
v_mfma_f32_16x16x32_bf16 v[32:35],a[44:47],v[48:51],v[32:35]
s_add_u32 m0,s71,s88
ds_read_b128 v[44:47],v2 offset:28672
v_fma_f32 v78,v98,v78,v97
v_mfma_f32_16x16x32_bf16 v[28:31],a[48:51],v[52:55],v[28:31]
buffer_load_dword v10,s[36:39],s72 offen offset:0 sc1 lds
v_mfma_f32_16x16x32_bf16 v[32:35],a[52:55],v[52:55],v[32:35]
s_xor_b32 s87,s64,65536
ds_read_b128 v[48:51],v2 offset:29696
v_fma_f32 v79,v98,v79,v97
v_mfma_f32_16x16x32_bf16 v[28:31],a[56:59],v[56:59],v[28:31]
s_cmp_ge_u32 s66,s59
ds_read_b128 v[52:55],v2 offset:30720
v_mul_f32 v78,v38,v78
v_mfma_f32_16x16x32_bf16 v[32:35],a[60:63],v[56:59],v[32:35]
s_waitcnt lgkmcnt(3)
v_exp_f32 v77,v77
v_mfma_f32_16x16x32_bf16 v[28:31],a[0:3],v[60:63],v[28:31]
s_cselect_b32 s86,s49,s50
ds_read_b128 v[56:59],v2 offset:31744
v_mul_f32 v79,v39,v79
v_mfma_f32_16x16x32_bf16 v[32:35],a[4:7],v[60:63],v[32:35]
s_cselect_b32 s87,s87,s63
v_exp_f32 v78,v78
v_mfma_f32_16x16x32_bf16 v[28:31],a[8:11],v[64:67],v[28:31]
s_mov_b32 s62,s87
ds_read_b128 v[60:63],v2 offset:24576
v_fma_f32 v77,v77,v99,v99
v_mfma_f32_16x16x32_bf16 v[32:35],a[12:15],v[64:67],v[32:35]
s_mov_b32 m0,s62
v_exp_f32 v79,v79
v_mfma_f32_16x16x32_bf16 v[28:31],a[16:19],v[68:71],v[28:31]
ds_read_b128 v[64:67],v2 offset:25600
v_fma_f32 v78,v78,v99,v99)"
R"(
v_mfma_f32_16x16x32_bf16 v[32:35],a[20:23],v[68:71],v[32:35]
v_rcp_f32 v76,v76
v_mfma_f32_16x16x32_bf16 v[28:31],a[24:27],v[72:75],v[28:31]
ds_read_b128 v[68:71],v2 offset:26624
v_fma_f32 v79,v79,v99,v99
v_mfma_f32_16x16x32_bf16 v[32:35],a[28:31],v[72:75],v[32:35]
s_waitcnt lgkmcnt(3)
v_rcp_f32 v77,v77
v_mfma_f32_16x16x32_bf16 v[28:31],a[224:227],v[44:47],v[28:31]
s_nop 0
ds_read_b128 v[72:75],v2 offset:27648
v_mul_f32 v36,v36,v76
v_mfma_f32_16x16x32_bf16 v[32:35],a[228:231],v[44:47],v[32:35]
s_barrier
v_mfma_f32_16x16x32_bf16 v[28:31],a[232:235],v[48:51],v[28:31]
ds_read_b128 v[44:47],v2 offset:20480
ds_read_b32 v16,v7 offset:0
v_mfma_f32_16x16x32_bf16 v[32:35],a[236:239],v[48:51],v[32:35]
v_rcp_f32 v78,v78
v_mfma_f32_16x16x32_bf16 v[28:31],a[240:243],v[52:55],v[28:31]
ds_read_b128 v[48:51],v2 offset:21504
v_mul_f32 v37,v37,v77
v_mfma_f32_16x16x32_bf16 v[32:35],a[244:247],v[52:55],v[32:35]
v_rcp_f32 v79,v79
v_mfma_f32_16x16x32_bf16 v[28:31],a[248:251],v[56:59],v[28:31]
s_waitcnt lgkmcnt(1)
ds_read_b128 v[52:55],v2 offset:22528
v_mul_f32 v38,v38,v78
v_mfma_f32_16x16x32_bf16 v[32:35],a[252:255],v[56:59],v[32:35]
v_cmp_eq_u32 vcc,s60,v16
v_mul_f32 v39,v39,v79
v_cvt_pk_bf16_f32 v84,v36,v37
v_cvt_pk_bf16_f32 v85,v38,v39
v_mfma_f32_16x16x32_bf16 v[28:31],a[192:195],v[60:63],v[28:31]
s_cmp_eq_u32 vcc_lo,-1
ds_read_b128 v[56:59],v2 offset:23552
v_fma_f32 v40,-v96,v24,v40
v_mfma_f32_16x16x32_bf16 v[32:35],a[196:199],v[60:63],v[32:35]
s_cselect_b32 s67,s86,s59
v_fma_f32 v41,-v96,v25,v41
v_fma_f32 v42,-v96,v26,v42
v_mfma_f32_16x16x32_bf16 v[28:31],a[200:203],v[64:67],v[28:31]
s_lshr_b32 s70,s46,5
ds_read_b128 v[60:63],v2 offset:16384
v_fma_f32 v43,-v96,v27,v43
v_mfma_f32_16x16x32_bf16 v[32:35],a[204:207],v[64:67],v[32:35]
buffer_load_dwordx4 v0,s[36:39],s67 offen offset:0 sc1 lds
v_mfma_f32_16x16x32_bf16 v[28:31],a[208:211],v[68:71],v[28:31]
s_xor_b32 s63,s63,65536
ds_read_b128 v[64:67],v2 offset:17408
v_mul_f32 v80,v40,v40
v_mfma_f32_16x16x32_bf16 v[32:35],a[212:215],v[68:71],v[32:35]
s_add_u32 s53,s52,s55
v_mul_f32 v81,v41,v41
v_mul_f32 v82,v42,v42
v_mfma_f32_16x16x32_bf16 v[28:31],a[216:219],v[72:75],v[28:31]
s_cmp_eq_u32 s53,s56
ds_read_b128 v[68:71],v2 offset:18432
v_mul_f32 v83,v43,v43
v_mfma_f32_16x16x32_bf16 v[32:35],a[220:223],v[72:75],v[32:35]
s_waitcnt lgkmcnt(3)
buffer_load_dwordx4 v0,s[36:39],s67 offen offset:1024 sc1 lds
v_mfma_f32_16x16x32_bf16 v[28:31],a[160:163],v[44:47],v[28:31]
s_waitcnt vmcnt(7)
ds_read_b128 v[72:75],v2 offset:19456
v_fma_f32 v80,v98,v80,v97
v_mfma_f32_16x16x32_bf16 v[32:35],a[164:167],v[44:47],v[32:35]
s_barrier
v_mfma_f32_16x16x32_bf16 v[28:31],a[168:171],v[48:51],v[28:31]
buffer_store_dword v5,v4,s[36:39],s70 offen offset:0 sc0 sc1
ds_read_b128 v[44:47],v2 offset:45056
ds_read_b128 v[92:95],v8 offset:0
v_mul_f32 v80,v40,v80
v_mfma_f32_16x16x32_bf16 v[32:35],a[172:175],v[48:51],v[32:35]
s_cselect_b32 s53,s57,s53
v_exp_f32 v80,v80
v_mfma_f32_16x16x32_bf16 v[28:31],a[176:179],v[52:55],v[28:31]
s_cmp_eq_u32 s53,s58
ds_read_b128 v[48:51],v2 offset:46080
v_fma_f32 v80,v80,v99,v99
v_mfma_f32_16x16x32_bf16 v[32:35],a[180:183],v[52:55],v[32:35]
buffer_load_dwordx4 v0,s[36:39],s67 offen offset:2048 sc1 lds
v_mfma_f32_16x16x32_bf16 v[28:31],a[184:187],v[56:59],v[28:31]
s_cselect_b32 s53,s59,s53
ds_read_b128 v[52:55],v2 offset:47104
v_fma_f32 v81,v98,v81,v97
v_mfma_f32_16x16x32_bf16 v[32:35],a[188:191],v[56:59],v[32:35]
s_waitcnt lgkmcnt(4)
v_rcp_f32 v80,v80
v_mfma_f32_16x16x32_bf16 v[28:31],a[128:131],v[60:63],v[28:31]
s_waitcnt lgkmcnt(2)
v_fma_f32 v82,v98,v82,v97
v_fma_f32 v83,v98,v83,v97
v_mul_f32 v81,v41,v81
v_mul_f32 v82,v42,v82
v_mul_f32 v83,v43,v83
v_exp_f32 v81,v81
v_exp_f32 v82,v82
v_exp_f32 v83,v83
v_mfma_f32_16x16x32_bf16 v[32:35],a[132:135],v[60:63],v[32:35]
s_cmp_ge_u32 s66,s59
ds_read_b128 v[56:59],v2 offset:48128
buffer_load_dwordx4 v0,s[36:39],s67 offen offset:3072 sc1 lds
v_fma_f32 v81,v81,v99,v99
v_fma_f32 v82,v82,v99,v99
v_fma_f32 v83,v83,v99,v99
v_rcp_f32 v81,v81
v_rcp_f32 v82,v82
v_rcp_f32 v83,v83
v_mfma_f32_16x16x32_bf16 v[28:31],a[136:139],v[64:67],v[28:31]
v_mul_f32 v40,v40,v80
v_mul_f32 v41,v41,v81
v_mul_f32 v42,v42,v82
v_mul_f32 v43,v43,v83
v_cvt_pk_bf16_f32 v86,v40,v41
v_cvt_pk_bf16_f32 v87,v42,v43
v_mfma_f32_16x16x32_bf16 v[32:35],a[140:143],v[64:67],v[32:35]
ds_read_b128 v[60:63],v2 offset:40960
buffer_store_dwordx4 v[84:87],v3,s[36:39],s73 offen offset:0 sc0 sc1
v_mfma_f32_16x16x32_bf16 v[28:31],a[144:147],v[68:71],v[28:31]
ds_read_b128 v[64:67],v2 offset:41984
v_mfma_f32_16x16x32_bf16 v[32:35],a[148:151],v[68:71],v[32:35]
v_mfma_f32_16x16x32_bf16 v[28:31],a[152:155],v[72:75],v[28:31]
s_waitcnt lgkmcnt(2)
ds_read_b128 v[68:71],v2 offset:43008
v_mfma_f32_16x16x32_bf16 v[32:35],a[156:159],v[72:75],v[32:35]
s_cbranch_scc1 Lg1_slow1_%=
Lg1_d1w3_%=:
v_mfma_f32_16x16x32_bf16 v[36:39],a[96:99],v[44:47],0
s_add_u32 m0,s62,16384
ds_read_b128 v[72:75],v2 offset:44032
s_lshr_b32 s73,s48,0
v_mfma_f32_16x16x32_bf16 v[40:43],a[100:103],v[44:47],0
s_lshr_b32 s72,s67,6
s_lshr_b32 s71,s62,2
s_cmp_lt_u32 s67,4194304
v_mfma_f32_16x16x32_bf16 v[36:39],a[104:107],v[48:51],v[36:39]
s_cselect_b32 s72,s59,s72
ds_read_b128 v[44:47],v2 offset:36864
s_mov_b64 s[44:45],s[46:47]
v_mfma_f32_16x16x32_bf16 v[40:43],a[108:111],v[48:51],v[40:43]
v_fma_f32 v28,-v92,v20,v28
v_fma_f32 v29,-v92,v21,v29
v_mfma_f32_16x16x32_bf16 v[36:39],a[112:115],v[52:55],v[36:39]
buffer_load_dwordx4 v1,s[36:39],s67 offen offset:0 sc1 lds
v_mfma_f32_16x16x32_bf16 v[40:43],a[116:119],v[52:55],v[40:43]
ds_read_b128 v[48:51],v2 offset:37888
v_fma_f32 v30,-v92,v22,v30
v_mfma_f32_16x16x32_bf16 v[36:39],a[120:123],v[56:59],v[36:39]
ds_read_b128 v[52:55],v2 offset:38912
v_fma_f32 v31,-v92,v23,v31
v_mfma_f32_16x16x32_bf16 v[40:43],a[124:127],v[56:59],v[40:43]
s_waitcnt lgkmcnt(3)
v_mul_f32 v76,v28,v28
v_mul_f32 v77,v29,v29
v_mfma_f32_16x16x32_bf16 v[36:39],a[64:67],v[60:63],v[36:39]
buffer_load_dwordx4 v1,s[36:39],s67 offen offset:1024 sc1 lds
v_mfma_f32_16x16x32_bf16 v[40:43],a[68:71],v[60:63],v[40:43]
ds_read_b128 v[56:59],v2 offset:39936
v_mul_f32 v78,v30,v30
v_mfma_f32_16x16x32_bf16 v[36:39],a[72:75],v[64:67],v[36:39]
ds_read_b128 v[60:63],v2 offset:32768
v_mul_f32 v79,v31,v31
v_mfma_f32_16x16x32_bf16 v[40:43],a[76:79],v[64:67],v[40:43]
v_fma_f32 v76,v94,v76,v93
v_mul_f32 v76,v28,v76
v_mfma_f32_16x16x32_bf16 v[36:39],a[80:83],v[68:71],v[36:39]
buffer_load_dwordx4 v1,s[36:39],s67 offen offset:2048 sc1 lds
v_mfma_f32_16x16x32_bf16 v[40:43],a[84:87],v[68:71],v[40:43]
ds_read_b128 v[64:67],v2 offset:33792
v_fma_f32 v77,v94,v77,v93
v_mfma_f32_16x16x32_bf16 v[36:39],a[88:91],v[72:75],v[36:39]
ds_read_b128 v[68:71],v2 offset:34816
v_mul_f32 v77,v29,v77
v_mfma_f32_16x16x32_bf16 v[40:43],a[92:95],v[72:75],v[40:43]
s_waitcnt lgkmcnt(3)
v_exp_f32 v76,v76
v_mfma_f32_16x16x32_bf16 v[36:39],a[32:35],v[44:47],v[36:39]
s_waitcnt vmcnt(9)
ds_read_b128 v[72:75],v2 offset:35840
v_fma_f32 v76,v76,v95,v95
v_mfma_f32_16x16x32_bf16 v[40:43],a[36:39],v[44:47],v[40:43]
s_barrier
v_mfma_f32_16x16x32_bf16 v[36:39],a[40:43],v[48:51],v[36:39]
buffer_load_dwordx4 v1,s[36:39],s67 offen offset:3072 sc1 lds
v_mfma_f32_16x16x32_bf16 v[40:43],a[44:47],v[48:51],v[40:43]
s_add_u32 m0,s71,s88
ds_read_b128 v[44:47],v2 offset:61440
v_fma_f32 v78,v94,v78,v93
v_mfma_f32_16x16x32_bf16 v[36:39],a[48:51],v[52:55],v[36:39]
buffer_load_dword v10,s[36:39],s72 offen offset:0 sc1 lds
v_mfma_f32_16x16x32_bf16 v[40:43],a[52:55],v[52:55],v[40:43]
s_xor_b32 s87,s63,65536
ds_read_b128 v[48:51],v2 offset:62464
v_fma_f32 v79,v94,v79,v93
v_mfma_f32_16x16x32_bf16 v[36:39],a[56:59],v[56:59],v[36:39]
s_cmp_ge_u32 s67,s59
ds_read_b128 v[52:55],v2 offset:63488
v_mul_f32 v78,v30,v78
v_mfma_f32_16x16x32_bf16 v[40:43],a[60:63],v[56:59],v[40:43])"
R"(
s_waitcnt lgkmcnt(3)
v_exp_f32 v77,v77
v_mfma_f32_16x16x32_bf16 v[36:39],a[0:3],v[60:63],v[36:39]
s_cselect_b32 s86,s50,s51
ds_read_b128 v[56:59],v2 offset:64512
v_mul_f32 v79,v31,v79
v_mfma_f32_16x16x32_bf16 v[40:43],a[4:7],v[60:63],v[40:43]
s_cselect_b32 s87,s87,s64
v_exp_f32 v78,v78
v_mfma_f32_16x16x32_bf16 v[36:39],a[8:11],v[64:67],v[36:39]
s_mov_b32 s62,s87
ds_read_b128 v[60:63],v2 offset:57344
v_fma_f32 v77,v77,v95,v95
v_mfma_f32_16x16x32_bf16 v[40:43],a[12:15],v[64:67],v[40:43]
s_mov_b32 m0,s62
v_exp_f32 v79,v79
v_mfma_f32_16x16x32_bf16 v[36:39],a[16:19],v[68:71],v[36:39]
ds_read_b128 v[64:67],v2 offset:58368
v_fma_f32 v78,v78,v95,v95
v_mfma_f32_16x16x32_bf16 v[40:43],a[20:23],v[68:71],v[40:43]
v_rcp_f32 v76,v76
v_mfma_f32_16x16x32_bf16 v[36:39],a[24:27],v[72:75],v[36:39]
ds_read_b128 v[68:71],v2 offset:59392
v_fma_f32 v79,v79,v95,v95
v_mfma_f32_16x16x32_bf16 v[40:43],a[28:31],v[72:75],v[40:43]
s_waitcnt lgkmcnt(3)
v_rcp_f32 v77,v77
v_mfma_f32_16x16x32_bf16 v[36:39],a[224:227],v[44:47],v[36:39]
s_nop 0
ds_read_b128 v[72:75],v2 offset:60416
v_mul_f32 v28,v28,v76
v_mfma_f32_16x16x32_bf16 v[40:43],a[228:231],v[44:47],v[40:43]
s_barrier
v_mfma_f32_16x16x32_bf16 v[36:39],a[232:235],v[48:51],v[36:39]
ds_read_b128 v[44:47],v2 offset:53248
ds_read_b32 v16,v7 offset:256
v_mfma_f32_16x16x32_bf16 v[40:43],a[236:239],v[48:51],v[40:43]
v_rcp_f32 v78,v78
v_mfma_f32_16x16x32_bf16 v[36:39],a[240:243],v[52:55],v[36:39]
ds_read_b128 v[48:51],v2 offset:54272
v_mul_f32 v29,v29,v77
v_mfma_f32_16x16x32_bf16 v[40:43],a[244:247],v[52:55],v[40:43]
v_rcp_f32 v79,v79
v_mfma_f32_16x16x32_bf16 v[36:39],a[248:251],v[56:59],v[36:39]
s_waitcnt lgkmcnt(1)
ds_read_b128 v[52:55],v2 offset:55296
v_mul_f32 v30,v30,v78
v_mfma_f32_16x16x32_bf16 v[40:43],a[252:255],v[56:59],v[40:43]
v_cmp_eq_u32 vcc,s60,v16
v_mul_f32 v31,v31,v79
v_cvt_pk_bf16_f32 v84,v28,v29
v_cvt_pk_bf16_f32 v85,v30,v31
v_mfma_f32_16x16x32_bf16 v[36:39],a[192:195],v[60:63],v[36:39]
s_cmp_eq_u32 vcc_lo,-1
ds_read_b128 v[56:59],v2 offset:56320
v_fma_f32 v32,-v92,v24,v32
v_mfma_f32_16x16x32_bf16 v[40:43],a[196:199],v[60:63],v[40:43]
s_cselect_b32 s66,s86,s59
v_fma_f32 v33,-v92,v25,v33
v_fma_f32 v34,-v92,v26,v34
v_mfma_f32_16x16x32_bf16 v[36:39],a[200:203],v[64:67],v[36:39]
s_lshr_b32 s70,s47,5
ds_read_b128 v[60:63],v2 offset:49152
v_fma_f32 v35,-v92,v27,v35
v_mfma_f32_16x16x32_bf16 v[40:43],a[204:207],v[64:67],v[40:43]
buffer_load_dwordx4 v0,s[36:39],s66 offen offset:0 sc1 lds
v_mfma_f32_16x16x32_bf16 v[36:39],a[208:211],v[68:71],v[36:39]
s_xor_b32 s64,s64,65536
ds_read_b128 v[64:67],v2 offset:50176
v_mul_f32 v80,v32,v32
v_mfma_f32_16x16x32_bf16 v[40:43],a[212:215],v[68:71],v[40:43]
s_add_u32 s74,s53,s55
v_mul_f32 v81,v33,v33
v_mul_f32 v82,v34,v34
v_mfma_f32_16x16x32_bf16 v[36:39],a[216:219],v[72:75],v[36:39]
s_cmp_eq_u32 s74,s56
ds_read_b128 v[68:71],v2 offset:51200
v_mul_f32 v83,v35,v35
v_mfma_f32_16x16x32_bf16 v[40:43],a[220:223],v[72:75],v[40:43]
s_waitcnt lgkmcnt(3)
buffer_load_dwordx4 v0,s[36:39],s66 offen offset:1024 sc1 lds
v_mfma_f32_16x16x32_bf16 v[36:39],a[160:163],v[44:47],v[36:39]
s_waitcnt vmcnt(7)
ds_read_b128 v[72:75],v2 offset:52224
v_xor_b32 v2,65536,v2
v_mfma_f32_16x16x32_bf16 v[40:43],a[164:167],v[44:47],v[40:43]
s_barrier
v_mfma_f32_16x16x32_bf16 v[36:39],a[168:171],v[48:51],v[36:39]
buffer_store_dword v5,v4,s[36:39],s70 offen offset:0 sc0 sc1
ds_read_b128 v[44:47],v2 offset:12288
ds_read_b128 v[96:99],v8 offset:8192
v_fma_f32 v80,v94,v80,v93
v_mfma_f32_16x16x32_bf16 v[40:43],a[172:175],v[48:51],v[40:43]
s_cselect_b32 s74,s57,s74
v_mul_f32 v80,v32,v80
v_fma_f32 v81,v94,v81,v93
v_mfma_f32_16x16x32_bf16 v[36:39],a[176:179],v[52:55],v[36:39]
s_cmp_eq_u32 s74,s58
ds_read_b128 v[48:51],v2 offset:13312
v_mul_f32 v81,v33,v81
v_mfma_f32_16x16x32_bf16 v[40:43],a[180:183],v[52:55],v[40:43]
buffer_load_dwordx4 v0,s[36:39],s66 offen offset:2048 sc1 lds
v_mfma_f32_16x16x32_bf16 v[36:39],a[184:187],v[56:59],v[36:39]
s_cselect_b32 s74,s59,s74
ds_read_b128 v[52:55],v2 offset:14336
v_fma_f32 v82,v94,v82,v93
v_mfma_f32_16x16x32_bf16 v[40:43],a[188:191],v[56:59],v[40:43]
s_waitcnt lgkmcnt(4)
v_exp_f32 v80,v80
v_mfma_f32_16x16x32_bf16 v[36:39],a[128:131],v[60:63],v[36:39]
s_waitcnt lgkmcnt(2)
v_fma_f32 v83,v94,v83,v93
v_mul_f32 v82,v34,v82
v_mul_f32 v83,v35,v83
v_exp_f32 v81,v81
v_exp_f32 v82,v82
v_exp_f32 v83,v83
v_mfma_f32_16x16x32_bf16 v[40:43],a[132:135],v[60:63],v[40:43]
s_mov_b64 s[46:47],s[48:49]
ds_read_b128 v[56:59],v2 offset:15360
buffer_load_dwordx4 v0,s[36:39],s66 offen offset:3072 sc1 lds
v_fma_f32 v80,v80,v95,v95
v_fma_f32 v81,v81,v95,v95
v_fma_f32 v82,v82,v95,v95
v_fma_f32 v83,v83,v95,v95
v_rcp_f32 v80,v80
v_rcp_f32 v81,v81
v_rcp_f32 v82,v82
v_rcp_f32 v83,v83
v_mfma_f32_16x16x32_bf16 v[36:39],a[136:139],v[64:67],v[36:39]
s_mov_b64 s[48:49],s[50:51]
v_mul_f32 v32,v32,v80
v_mul_f32 v33,v33,v81
v_mul_f32 v34,v34,v82
v_mul_f32 v35,v35,v83
v_cvt_pk_bf16_f32 v86,v32,v33
v_cvt_pk_bf16_f32 v87,v34,v35
v_mfma_f32_16x16x32_bf16 v[40:43],a[140:143],v[64:67],v[40:43]
s_mov_b64 s[50:51],s[52:53]
ds_read_b128 v[60:63],v2 offset:8192
buffer_store_dwordx4 v[84:87],v3,s[36:39],s73 offen offset:0 sc0 sc1
v_xor_b32 v8,16384,v8
s_mov_b32 s52,s74
s_cmp_lt_u32 s67,s59
v_mfma_f32_16x16x32_bf16 v[36:39],a[144:147],v[68:71],v[36:39]
ds_read_b128 v[64:67],v2 offset:9216
v_mfma_f32_16x16x32_bf16 v[40:43],a[148:151],v[68:71],v[40:43]
v_mfma_f32_16x16x32_bf16 v[36:39],a[152:155],v[72:75],v[36:39]
s_waitcnt lgkmcnt(2)
ds_read_b128 v[68:71],v2 offset:10240
v_mfma_f32_16x16x32_bf16 v[40:43],a[156:159],v[72:75],v[40:43]
s_cbranch_scc1 Lg1_d0w3_%=
s_branch Lg1_slow0_%=
Lg1_slow0_%=:
s_cmp_lt_u32 s66,s59
s_cbranch_scc1 Lg1_gs0_%=
s_cmp_eq_u32 s54,1
s_cselect_b32 s68,s49,s48
s_lshr_b32 s69,s68,5
s_cmp_lt_u32 s68,4194304
s_cselect_b32 s68,0xffffcfff,-1
s_and_b32 s68,s68,s69
s_add_u32 m0,s78,8192
s_nop 0
buffer_load_dword v6,s[36:39],s68 offen offset:0 sc1 lds
s_lshr_b32 s73,s47,0
v_fma_f32 v36,-v96,v20,v36
v_fma_f32 v37,-v96,v21,v37
v_fma_f32 v38,-v96,v22,v38
v_fma_f32 v39,-v96,v23,v39
v_mul_f32 v76,v36,v36
v_mul_f32 v77,v37,v37
v_mul_f32 v78,v38,v38
v_mul_f32 v79,v39,v39
v_fma_f32 v76,v98,v76,v97
v_fma_f32 v77,v98,v77,v97
v_fma_f32 v78,v98,v78,v97
v_fma_f32 v79,v98,v79,v97
v_mul_f32 v76,v36,v76
v_mul_f32 v77,v37,v77
v_mul_f32 v78,v38,v78
v_mul_f32 v79,v39,v79
v_exp_f32 v76,v76
v_exp_f32 v77,v77
v_exp_f32 v78,v78
v_exp_f32 v79,v79
v_fma_f32 v76,v76,v99,v99
v_fma_f32 v77,v77,v99,v99
v_fma_f32 v78,v78,v99,v99
v_fma_f32 v79,v79,v99,v99
v_rcp_f32 v76,v76
v_rcp_f32 v77,v77
v_rcp_f32 v78,v78
v_rcp_f32 v79,v79
v_mul_f32 v36,v36,v76
v_mul_f32 v37,v37,v77
v_mul_f32 v38,v38,v78
v_mul_f32 v39,v39,v79
v_cvt_pk_bf16_f32 v84,v36,v37
v_cvt_pk_bf16_f32 v85,v38,v39)"
R"(
s_add_u32 m0,s78,8448
s_nop 0
buffer_load_dword v6,s[36:39],s68 offen offset:0 sc1 lds
v_fma_f32 v40,-v96,v24,v40
v_fma_f32 v41,-v96,v25,v41
v_fma_f32 v42,-v96,v26,v42
v_fma_f32 v43,-v96,v27,v43
v_mul_f32 v80,v40,v40
v_mul_f32 v81,v41,v41
v_mul_f32 v82,v42,v42
v_mul_f32 v83,v43,v43
v_fma_f32 v80,v98,v80,v97
v_fma_f32 v81,v98,v81,v97
v_fma_f32 v82,v98,v82,v97
v_fma_f32 v83,v98,v83,v97
v_mul_f32 v80,v40,v80
v_mul_f32 v81,v41,v81
v_mul_f32 v82,v42,v82
v_mul_f32 v83,v43,v83
v_exp_f32 v80,v80
v_exp_f32 v81,v81
v_exp_f32 v82,v82
v_exp_f32 v83,v83
v_fma_f32 v80,v80,v99,v99
v_fma_f32 v81,v81,v99,v99
v_fma_f32 v82,v82,v99,v99
v_fma_f32 v83,v83,v99,v99
v_rcp_f32 v80,v80
v_rcp_f32 v81,v81
v_rcp_f32 v82,v82
v_rcp_f32 v83,v83
v_mul_f32 v40,v40,v80
v_mul_f32 v41,v41,v81
v_mul_f32 v42,v42,v82
v_mul_f32 v43,v43,v83
v_cvt_pk_bf16_f32 v86,v40,v41
v_cvt_pk_bf16_f32 v87,v42,v43
buffer_store_dwordx4 v[84:87],v3,s[36:39],s73 offen offset:0 sc0 sc1
s_cmp_ge_u32 s48,s59
s_cbranch_scc1 Lg1_done0_%=
s_add_u32 m0,s78,16384
s_nop 0
buffer_load_dword v6,s[36:39],s68 offen offset:0 sc1 lds
s_waitcnt vmcnt(3)
s_barrier
ds_read_b32 v16,v7 offset:8192
s_lshr_b32 s70,s46,5
buffer_store_dword v5,v4,s[36:39],s70 offen offset:0 sc0 sc1
s_mov_b32 s46,s59
s_mov_b32 s69,18432
s_waitcnt lgkmcnt(0)
v_cmp_eq_u32 vcc,s60,v16
s_nop 0
s_cmp_eq_u32 vcc_lo,-1
s_cbranch_scc1 Lg1_go0_%=
s_add_u32 m0,s78,16640
s_nop 0
buffer_load_dword v6,s[36:39],s68 offen offset:0 sc1 lds
s_waitcnt vmcnt(4)
s_barrier
ds_read_b32 v16,v7 offset:8448
s_mov_b32 s69,18688
s_waitcnt lgkmcnt(0)
v_cmp_eq_u32 vcc,s60,v16
s_nop 0
s_cmp_eq_u32 vcc_lo,-1
s_cbranch_scc1 Lg1_go0_%=
s_add_u32 m0,s78,8192
s_nop 0
buffer_load_dword v6,s[36:39],s68 offen offset:0 sc1 lds
s_waitcnt vmcnt(3)
s_barrier
ds_read_b32 v16,v7 offset:16384
s_lshr_b32 s70,s47,5
buffer_store_dword v5,v4,s[36:39],s70 offen offset:0 sc0 sc1
s_mov_b32 s47,s59
s_mov_b32 s69,10240
s_waitcnt lgkmcnt(0)
v_cmp_eq_u32 vcc,s60,v16
s_nop 0
s_cmp_eq_u32 vcc_lo,-1
s_cbranch_scc1 Lg1_go0_%=
s_add_u32 m0,s78,8448
s_nop 0
buffer_load_dword v6,s[36:39],s68 offen offset:0 sc1 lds
s_waitcnt vmcnt(3)
s_barrier
ds_read_b32 v16,v7 offset:16640
s_mov_b32 s69,10496
s_waitcnt lgkmcnt(0)
v_cmp_eq_u32 vcc,s60,v16
s_nop 0
s_cmp_eq_u32 vcc_lo,-1
s_cbranch_scc1 Lg1_go0_%=
Lg1_poll0_%=:
s_add_u32 m0,s78,16384
s_nop 0
buffer_load_dword v6,s[36:39],s68 offen offset:0 sc1 lds
s_waitcnt vmcnt(2)
s_barrier
ds_read_b32 v16,v7 offset:8192
s_mov_b32 s69,18432
s_waitcnt lgkmcnt(0)
v_cmp_eq_u32 vcc,s60,v16
s_nop 0
s_cmp_eq_u32 vcc_lo,-1
s_cbranch_scc1 Lg1_go0_%=
s_sleep 4
s_add_u32 m0,s78,16640
s_nop 0
buffer_load_dword v6,s[36:39],s68 offen offset:0 sc1 lds
s_waitcnt vmcnt(2)
s_barrier
ds_read_b32 v16,v7 offset:8448
s_mov_b32 s69,18688
s_waitcnt lgkmcnt(0)
v_cmp_eq_u32 vcc,s60,v16
s_nop 0
s_cmp_eq_u32 vcc_lo,-1
s_cbranch_scc1 Lg1_go0_%=
s_sleep 4
s_add_u32 m0,s78,8192
s_nop 0
buffer_load_dword v6,s[36:39],s68 offen offset:0 sc1 lds
s_waitcnt vmcnt(2)
s_barrier
ds_read_b32 v16,v7 offset:16384
s_mov_b32 s69,10240
s_waitcnt lgkmcnt(0)
v_cmp_eq_u32 vcc,s60,v16
s_nop 0
s_cmp_eq_u32 vcc_lo,-1
s_cbranch_scc1 Lg1_go0_%=
s_sleep 4
s_add_u32 m0,s78,8448
s_nop 0
buffer_load_dword v6,s[36:39],s68 offen offset:0 sc1 lds
s_waitcnt vmcnt(2)
s_barrier
ds_read_b32 v16,v7 offset:16640
s_mov_b32 s69,10496
s_waitcnt lgkmcnt(0)
v_cmp_eq_u32 vcc,s60,v16
s_nop 0
s_cmp_eq_u32 vcc_lo,-1
s_cbranch_scc1 Lg1_go0_%=
s_sleep 4
s_add_u32 s79,s79,1
s_cmp_lt_u32 s79,20000
s_cbranch_scc1 Lg1_poll0_%=
Lg1_go0_%=:
s_xor_b32 s62,s63,65536
s_mov_b32 m0,s62
s_nop 0
buffer_load_dwordx4 v0,s[36:39],s48 offen offset:0 sc1 lds
buffer_load_dwordx4 v0,s[36:39],s48 offen offset:1024 sc1 lds
buffer_load_dwordx4 v0,s[36:39],s48 offen offset:2048 sc1 lds
buffer_load_dwordx4 v0,s[36:39],s48 offen offset:3072 sc1 lds
s_add_u32 m0,s62,16384
s_nop 0
buffer_load_dwordx4 v1,s[36:39],s48 offen offset:0 sc1 lds
buffer_load_dwordx4 v1,s[36:39],s48 offen offset:1024 sc1 lds
buffer_load_dwordx4 v1,s[36:39],s48 offen offset:2048 sc1 lds
buffer_load_dwordx4 v1,s[36:39],s48 offen offset:3072 sc1 lds
s_lshr_b32 s72,s48,6
s_lshr_b32 s71,s62,2
s_cmp_lt_u32 s48,4194304
s_cselect_b32 s72,s59,s72
s_add_u32 m0,s71,s88
s_nop 0
buffer_load_dword v10,s[36:39],s72 offen offset:0 sc1 lds
s_cmp_eq_u32 vcc_hi,-1
s_cselect_b32 s66,s49,s59
s_xor_b32 s62,s64,65536
s_cmp_ge_u32 s66,s59
s_cselect_b32 s68,s49,s50
s_lshr_b32 s69,s68,5
s_cmp_lt_u32 s68,4194304
s_cselect_b32 s68,0xffffcfff,-1
s_and_b32 s68,s68,s69
s_add_u32 m0,s78,0
s_nop 0
buffer_load_dword v6,s[36:39],s68 offen offset:0 sc1 lds
s_mov_b32 m0,s62
s_nop 0
buffer_load_dwordx4 v0,s[36:39],s66 offen offset:0 sc1 lds
buffer_load_dwordx4 v0,s[36:39],s66 offen offset:1024 sc1 lds
buffer_load_dwordx4 v0,s[36:39],s66 offen offset:2048 sc1 lds
buffer_load_dwordx4 v0,s[36:39],s66 offen offset:3072 sc1 lds
s_waitcnt vmcnt(10)
s_barrier
s_lshl_b32 s70,s54,12
v_add_u32 v76,s70,v2
v_xor_b32 v77,4096,v76
ds_read_b128 v[44:47],v76 offset:0
ds_read_b128 v[48:51],v76 offset:1024
ds_read_b128 v[52:55],v76 offset:2048
ds_read_b128 v[56:59],v76 offset:3072
ds_read_b128 v[60:63],v77 offset:0
ds_read_b128 v[64:67],v77 offset:1024)"
R"(
ds_read_b128 v[68:71],v77 offset:2048
s_lshr_b32 s70,s46,5
buffer_store_dword v5,v4,s[36:39],s70 offen offset:0 sc0 sc1
s_mov_b32 s46,s59
s_lshr_b32 s70,s47,5
buffer_store_dword v5,v4,s[36:39],s70 offen offset:0 sc0 sc1
s_mov_b32 s47,s59
s_lshr_b32 s70,s59,5
buffer_store_dword v5,v4,s[36:39],s70 offen offset:0 sc0 sc1
s_waitcnt lgkmcnt(3)
s_setpc_b64 s[90:91]
Lg1_gs0_%=:
s_cmp_eq_u32 s54,1
s_cselect_b32 s68,s49,s48
s_lshr_b32 s69,s68,5
s_cmp_lt_u32 s68,4194304
s_cselect_b32 s68,0xffffcfff,-1
s_and_b32 s68,s68,s69
s_lshr_b32 s73,s47,0
s_add_u32 m0,s78,8192
s_nop 0
buffer_load_dword v6,s[36:39],s68 offen offset:0 sc1 lds
v_fma_f32 v36,-v96,v20,v36
v_fma_f32 v37,-v96,v21,v37
v_fma_f32 v38,-v96,v22,v38
v_fma_f32 v39,-v96,v23,v39
v_mul_f32 v76,v36,v36
v_mul_f32 v77,v37,v37
v_mul_f32 v78,v38,v38
v_mul_f32 v79,v39,v39
v_fma_f32 v76,v98,v76,v97
v_fma_f32 v77,v98,v77,v97
v_fma_f32 v78,v98,v78,v97
s_add_u32 m0,s62,16384
s_nop 0
buffer_load_dwordx4 v1,s[36:39],s66 offen offset:0 sc1 lds
v_fma_f32 v79,v98,v79,v97
v_mul_f32 v76,v36,v76
v_mul_f32 v77,v37,v77
v_mul_f32 v78,v38,v78
v_mul_f32 v79,v39,v79
v_exp_f32 v76,v76
v_exp_f32 v77,v77
v_exp_f32 v78,v78
v_exp_f32 v79,v79
v_fma_f32 v76,v76,v99,v99
v_fma_f32 v77,v77,v99,v99
buffer_load_dwordx4 v1,s[36:39],s66 offen offset:1024 sc1 lds
v_fma_f32 v78,v78,v99,v99
v_fma_f32 v79,v79,v99,v99
v_rcp_f32 v76,v76
v_rcp_f32 v77,v77
v_rcp_f32 v78,v78
v_rcp_f32 v79,v79
v_mul_f32 v36,v36,v76
v_mul_f32 v37,v37,v77
v_mul_f32 v38,v38,v78
v_mul_f32 v39,v39,v79
v_cvt_pk_bf16_f32 v84,v36,v37
buffer_load_dwordx4 v1,s[36:39],s66 offen offset:2048 sc1 lds
v_cvt_pk_bf16_f32 v85,v38,v39
v_fma_f32 v40,-v96,v24,v40
v_fma_f32 v41,-v96,v25,v41
v_fma_f32 v42,-v96,v26,v42
v_fma_f32 v43,-v96,v27,v43
v_mul_f32 v80,v40,v40
v_mul_f32 v81,v41,v41
v_mul_f32 v82,v42,v42
v_mul_f32 v83,v43,v43
v_fma_f32 v80,v98,v80,v97
v_fma_f32 v81,v98,v81,v97
buffer_load_dwordx4 v1,s[36:39],s66 offen offset:3072 sc1 lds
v_fma_f32 v82,v98,v82,v97
v_fma_f32 v83,v98,v83,v97
v_mul_f32 v80,v40,v80
v_mul_f32 v81,v41,v81
v_mul_f32 v82,v42,v82
v_mul_f32 v83,v43,v83
v_exp_f32 v80,v80
v_exp_f32 v81,v81
v_exp_f32 v82,v82
v_exp_f32 v83,v83
v_fma_f32 v80,v80,v99,v99
s_lshr_b32 s72,s66,6
s_lshr_b32 s71,s62,2
s_cmp_lt_u32 s66,4194304
s_cselect_b32 s72,s59,s72
s_add_u32 m0,s71,s88
s_nop 0
buffer_load_dword v10,s[36:39],s72 offen offset:0 sc1 lds
v_fma_f32 v81,v81,v99,v99
v_fma_f32 v82,v82,v99,v99
v_fma_f32 v83,v83,v99,v99
v_rcp_f32 v80,v80
v_rcp_f32 v81,v81
v_rcp_f32 v82,v82
v_rcp_f32 v83,v83
v_mul_f32 v40,v40,v80
v_mul_f32 v41,v41,v81
v_mul_f32 v42,v42,v82
v_mul_f32 v43,v43,v83
v_cvt_pk_bf16_f32 v86,v40,v41
v_cvt_pk_bf16_f32 v87,v42,v43
buffer_store_dwordx4 v[84:87],v3,s[36:39],s73 offen offset:0 sc0 sc1
s_waitcnt vmcnt(6)
s_barrier
ds_read_b32 v16,v7 offset:8192
s_lshl_b32 s70,s54,12
v_add_u32 v76,s70,v2
v_xor_b32 v77,4096,v76
ds_read_b128 v[44:47],v76 offset:0
ds_read_b128 v[48:51],v76 offset:1024
ds_read_b128 v[52:55],v76 offset:2048
ds_read_b128 v[56:59],v76 offset:3072
ds_read_b128 v[60:63],v77 offset:0
ds_read_b128 v[64:67],v77 offset:1024
ds_read_b128 v[68:71],v77 offset:2048
s_lshr_b32 s70,s46,5
buffer_store_dword v5,v4,s[36:39],s70 offen offset:0 sc0 sc1
s_waitcnt lgkmcnt(7)
v_cmp_eq_u32 vcc,s60,v16
s_nop 0
s_cmp_eq_u32 vcc_hi,-1
s_cselect_b32 s66,s49,s59
s_xor_b32 s62,s64,65536
s_cmp_ge_u32 s66,s59
s_cselect_b32 s68,s49,s50
s_lshr_b32 s69,s68,5
s_cmp_lt_u32 s68,4194304
s_cselect_b32 s68,0xffffcfff,-1
s_and_b32 s68,s68,s69
s_add_u32 m0,s78,0
s_nop 0
buffer_load_dword v6,s[36:39],s68 offen offset:0 sc1 lds
s_mov_b32 m0,s62
s_nop 0
buffer_load_dwordx4 v0,s[36:39],s66 offen offset:0 sc1 lds
buffer_load_dwordx4 v0,s[36:39],s66 offen offset:1024 sc1 lds
buffer_load_dwordx4 v0,s[36:39],s66 offen offset:2048 sc1 lds
buffer_load_dwordx4 v0,s[36:39],s66 offen offset:3072 sc1 lds
s_mov_b32 s46,s47
s_mov_b32 s47,s59
s_lshr_b32 s70,s59,5
buffer_store_dword v5,v4,s[36:39],s70 offen offset:0 sc0 sc1
s_waitcnt lgkmcnt(3)
s_setpc_b64 s[90:91]
Lg1_done0_%=:
s_waitcnt vmcnt(0)
s_barrier
s_lshr_b32 s70,s46,5
buffer_store_dword v5,v4,s[36:39],s70 offen offset:0 sc0 sc1
s_lshr_b32 s70,s47,5
buffer_store_dword v5,v4,s[36:39],s70 offen offset:0 sc0 sc1
s_branch Lg1_done_%=
Lg1_slow1_%=:
s_cmp_lt_u32 s67,s59
s_cbranch_scc1 Lg1_gs1_%=
s_cmp_eq_u32 s54,1
s_cselect_b32 s68,s50,s49
s_lshr_b32 s69,s68,5
s_cmp_lt_u32 s68,4194304
s_cselect_b32 s68,0xffffcfff,-1
s_and_b32 s68,s68,s69
s_add_u32 m0,s78,8192
s_nop 0
buffer_load_dword v6,s[36:39],s68 offen offset:0 sc1 lds
s_lshr_b32 s73,s48,0
v_fma_f32 v28,-v92,v20,v28
v_fma_f32 v29,-v92,v21,v29
v_fma_f32 v30,-v92,v22,v30
v_fma_f32 v31,-v92,v23,v31
v_mul_f32 v76,v28,v28
v_mul_f32 v77,v29,v29
v_mul_f32 v78,v30,v30
v_mul_f32 v79,v31,v31
v_fma_f32 v76,v94,v76,v93
v_fma_f32 v77,v94,v77,v93
v_fma_f32 v78,v94,v78,v93
v_fma_f32 v79,v94,v79,v93
v_mul_f32 v76,v28,v76
v_mul_f32 v77,v29,v77
v_mul_f32 v78,v30,v78
v_mul_f32 v79,v31,v79
v_exp_f32 v76,v76
v_exp_f32 v77,v77
v_exp_f32 v78,v78
v_exp_f32 v79,v79
v_fma_f32 v76,v76,v95,v95
v_fma_f32 v77,v77,v95,v95
v_fma_f32 v78,v78,v95,v95
v_fma_f32 v79,v79,v95,v95
v_rcp_f32 v76,v76
v_rcp_f32 v77,v77
v_rcp_f32 v78,v78
v_rcp_f32 v79,v79
v_mul_f32 v28,v28,v76
v_mul_f32 v29,v29,v77
v_mul_f32 v30,v30,v78
v_mul_f32 v31,v31,v79
v_cvt_pk_bf16_f32 v84,v28,v29)"
R"(
v_cvt_pk_bf16_f32 v85,v30,v31
s_add_u32 m0,s78,8448
s_nop 0
buffer_load_dword v6,s[36:39],s68 offen offset:0 sc1 lds
v_fma_f32 v32,-v92,v24,v32
v_fma_f32 v33,-v92,v25,v33
v_fma_f32 v34,-v92,v26,v34
v_fma_f32 v35,-v92,v27,v35
v_mul_f32 v80,v32,v32
v_mul_f32 v81,v33,v33
v_mul_f32 v82,v34,v34
v_mul_f32 v83,v35,v35
v_fma_f32 v80,v94,v80,v93
v_fma_f32 v81,v94,v81,v93
v_fma_f32 v82,v94,v82,v93
v_fma_f32 v83,v94,v83,v93
v_mul_f32 v80,v32,v80
v_mul_f32 v81,v33,v81
v_mul_f32 v82,v34,v82
v_mul_f32 v83,v35,v83
v_exp_f32 v80,v80
v_exp_f32 v81,v81
v_exp_f32 v82,v82
v_exp_f32 v83,v83
v_fma_f32 v80,v80,v95,v95
v_fma_f32 v81,v81,v95,v95
v_fma_f32 v82,v82,v95,v95
v_fma_f32 v83,v83,v95,v95
v_rcp_f32 v80,v80
v_rcp_f32 v81,v81
v_rcp_f32 v82,v82
v_rcp_f32 v83,v83
v_mul_f32 v32,v32,v80
v_mul_f32 v33,v33,v81
v_mul_f32 v34,v34,v82
v_mul_f32 v35,v35,v83
v_cvt_pk_bf16_f32 v86,v32,v33
v_cvt_pk_bf16_f32 v87,v34,v35
buffer_store_dwordx4 v[84:87],v3,s[36:39],s73 offen offset:0 sc0 sc1
s_cmp_ge_u32 s49,s59
s_cbranch_scc1 Lg1_done1_%=
s_add_u32 m0,s78,16384
s_nop 0
buffer_load_dword v6,s[36:39],s68 offen offset:0 sc1 lds
s_waitcnt vmcnt(3)
s_barrier
ds_read_b32 v16,v7 offset:8192
s_lshr_b32 s70,s47,5
buffer_store_dword v5,v4,s[36:39],s70 offen offset:0 sc0 sc1
s_mov_b32 s47,s59
s_mov_b32 s69,18432
s_waitcnt lgkmcnt(0)
v_cmp_eq_u32 vcc,s60,v16
s_nop 0
s_cmp_eq_u32 vcc_lo,-1
s_cbranch_scc1 Lg1_go1_%=
s_add_u32 m0,s78,16640
s_nop 0
buffer_load_dword v6,s[36:39],s68 offen offset:0 sc1 lds
s_waitcnt vmcnt(4)
s_barrier
ds_read_b32 v16,v7 offset:8448
s_mov_b32 s69,18688
s_waitcnt lgkmcnt(0)
v_cmp_eq_u32 vcc,s60,v16
s_nop 0
s_cmp_eq_u32 vcc_lo,-1
s_cbranch_scc1 Lg1_go1_%=
s_add_u32 m0,s78,8192
s_nop 0
buffer_load_dword v6,s[36:39],s68 offen offset:0 sc1 lds
s_waitcnt vmcnt(3)
s_barrier
ds_read_b32 v16,v7 offset:16384
s_lshr_b32 s70,s48,5
buffer_store_dword v5,v4,s[36:39],s70 offen offset:0 sc0 sc1
s_mov_b32 s48,s59
s_mov_b32 s69,10240
s_waitcnt lgkmcnt(0)
v_cmp_eq_u32 vcc,s60,v16
s_nop 0
s_cmp_eq_u32 vcc_lo,-1
s_cbranch_scc1 Lg1_go1_%=
s_add_u32 m0,s78,8448
s_nop 0
buffer_load_dword v6,s[36:39],s68 offen offset:0 sc1 lds
s_waitcnt vmcnt(3)
s_barrier
ds_read_b32 v16,v7 offset:16640
s_mov_b32 s69,10496
s_waitcnt lgkmcnt(0)
v_cmp_eq_u32 vcc,s60,v16
s_nop 0
s_cmp_eq_u32 vcc_lo,-1
s_cbranch_scc1 Lg1_go1_%=
Lg1_poll1_%=:
s_add_u32 m0,s78,16384
s_nop 0
buffer_load_dword v6,s[36:39],s68 offen offset:0 sc1 lds
s_waitcnt vmcnt(2)
s_barrier
ds_read_b32 v16,v7 offset:8192
s_mov_b32 s69,18432
s_waitcnt lgkmcnt(0)
v_cmp_eq_u32 vcc,s60,v16
s_nop 0
s_cmp_eq_u32 vcc_lo,-1
s_cbranch_scc1 Lg1_go1_%=
s_sleep 4
s_add_u32 m0,s78,16640
s_nop 0
buffer_load_dword v6,s[36:39],s68 offen offset:0 sc1 lds
s_waitcnt vmcnt(2)
s_barrier
ds_read_b32 v16,v7 offset:8448
s_mov_b32 s69,18688
s_waitcnt lgkmcnt(0)
v_cmp_eq_u32 vcc,s60,v16
s_nop 0
s_cmp_eq_u32 vcc_lo,-1
s_cbranch_scc1 Lg1_go1_%=
s_sleep 4
s_add_u32 m0,s78,8192
s_nop 0
buffer_load_dword v6,s[36:39],s68 offen offset:0 sc1 lds
s_waitcnt vmcnt(2)
s_barrier
ds_read_b32 v16,v7 offset:16384
s_mov_b32 s69,10240
s_waitcnt lgkmcnt(0)
v_cmp_eq_u32 vcc,s60,v16
s_nop 0
s_cmp_eq_u32 vcc_lo,-1
s_cbranch_scc1 Lg1_go1_%=
s_sleep 4
s_add_u32 m0,s78,8448
s_nop 0
buffer_load_dword v6,s[36:39],s68 offen offset:0 sc1 lds
s_waitcnt vmcnt(2)
s_barrier
ds_read_b32 v16,v7 offset:16640
s_mov_b32 s69,10496
s_waitcnt lgkmcnt(0)
v_cmp_eq_u32 vcc,s60,v16
s_nop 0
s_cmp_eq_u32 vcc_lo,-1
s_cbranch_scc1 Lg1_go1_%=
s_sleep 4
s_add_u32 s79,s79,1
s_cmp_lt_u32 s79,20000
s_cbranch_scc1 Lg1_poll1_%=
Lg1_go1_%=:
s_xor_b32 s62,s64,65536
s_mov_b32 m0,s62
s_nop 0
buffer_load_dwordx4 v0,s[36:39],s49 offen offset:0 sc1 lds
buffer_load_dwordx4 v0,s[36:39],s49 offen offset:1024 sc1 lds
buffer_load_dwordx4 v0,s[36:39],s49 offen offset:2048 sc1 lds
buffer_load_dwordx4 v0,s[36:39],s49 offen offset:3072 sc1 lds
s_add_u32 m0,s62,16384
s_nop 0
buffer_load_dwordx4 v1,s[36:39],s49 offen offset:0 sc1 lds
buffer_load_dwordx4 v1,s[36:39],s49 offen offset:1024 sc1 lds
buffer_load_dwordx4 v1,s[36:39],s49 offen offset:2048 sc1 lds
buffer_load_dwordx4 v1,s[36:39],s49 offen offset:3072 sc1 lds
s_lshr_b32 s72,s49,6
s_lshr_b32 s71,s62,2
s_cmp_lt_u32 s49,4194304
s_cselect_b32 s72,s59,s72
s_add_u32 m0,s71,s88
s_nop 0
buffer_load_dword v10,s[36:39],s72 offen offset:0 sc1 lds
s_cmp_eq_u32 vcc_hi,-1
s_cselect_b32 s67,s50,s59
s_xor_b32 s62,s63,65536
s_cmp_ge_u32 s67,s59
s_cselect_b32 s68,s50,s51
s_lshr_b32 s69,s68,5
s_cmp_lt_u32 s68,4194304
s_cselect_b32 s68,0xffffcfff,-1
s_and_b32 s68,s68,s69
s_add_u32 m0,s78,256
s_nop 0
buffer_load_dword v6,s[36:39],s68 offen offset:0 sc1 lds
s_mov_b32 m0,s62
s_nop 0
buffer_load_dwordx4 v0,s[36:39],s67 offen offset:0 sc1 lds
buffer_load_dwordx4 v0,s[36:39],s67 offen offset:1024 sc1 lds
buffer_load_dwordx4 v0,s[36:39],s67 offen offset:2048 sc1 lds
buffer_load_dwordx4 v0,s[36:39],s67 offen offset:3072 sc1 lds
s_waitcnt vmcnt(10)
s_barrier
s_lshl_b32 s70,s54,12
v_add_u32 v76,s70,v2
v_xor_b32 v77,4096,v76
ds_read_b128 v[44:47],v76 offset:32768
ds_read_b128 v[48:51],v76 offset:33792
ds_read_b128 v[52:55],v76 offset:34816
ds_read_b128 v[56:59],v76 offset:35840
ds_read_b128 v[60:63],v77 offset:32768)"
R"(
ds_read_b128 v[64:67],v77 offset:33792
ds_read_b128 v[68:71],v77 offset:34816
s_lshr_b32 s70,s47,5
buffer_store_dword v5,v4,s[36:39],s70 offen offset:0 sc0 sc1
s_mov_b32 s47,s59
s_lshr_b32 s70,s48,5
buffer_store_dword v5,v4,s[36:39],s70 offen offset:0 sc0 sc1
s_mov_b32 s48,s59
s_lshr_b32 s70,s59,5
buffer_store_dword v5,v4,s[36:39],s70 offen offset:0 sc0 sc1
s_waitcnt lgkmcnt(3)
s_setpc_b64 s[92:93]
Lg1_gs1_%=:
s_cmp_eq_u32 s54,1
s_cselect_b32 s68,s50,s49
s_lshr_b32 s69,s68,5
s_cmp_lt_u32 s68,4194304
s_cselect_b32 s68,0xffffcfff,-1
s_and_b32 s68,s68,s69
s_lshr_b32 s73,s48,0
s_add_u32 m0,s78,8192
s_nop 0
buffer_load_dword v6,s[36:39],s68 offen offset:0 sc1 lds
v_fma_f32 v28,-v92,v20,v28
v_fma_f32 v29,-v92,v21,v29
v_fma_f32 v30,-v92,v22,v30
v_fma_f32 v31,-v92,v23,v31
v_mul_f32 v76,v28,v28
v_mul_f32 v77,v29,v29
v_mul_f32 v78,v30,v30
v_mul_f32 v79,v31,v31
v_fma_f32 v76,v94,v76,v93
v_fma_f32 v77,v94,v77,v93
v_fma_f32 v78,v94,v78,v93
s_add_u32 m0,s62,16384
s_nop 0
buffer_load_dwordx4 v1,s[36:39],s67 offen offset:0 sc1 lds
v_fma_f32 v79,v94,v79,v93
v_mul_f32 v76,v28,v76
v_mul_f32 v77,v29,v77
v_mul_f32 v78,v30,v78
v_mul_f32 v79,v31,v79
v_exp_f32 v76,v76
v_exp_f32 v77,v77
v_exp_f32 v78,v78
v_exp_f32 v79,v79
v_fma_f32 v76,v76,v95,v95
v_fma_f32 v77,v77,v95,v95
buffer_load_dwordx4 v1,s[36:39],s67 offen offset:1024 sc1 lds
v_fma_f32 v78,v78,v95,v95
v_fma_f32 v79,v79,v95,v95
v_rcp_f32 v76,v76
v_rcp_f32 v77,v77
v_rcp_f32 v78,v78
v_rcp_f32 v79,v79
v_mul_f32 v28,v28,v76
v_mul_f32 v29,v29,v77
v_mul_f32 v30,v30,v78
v_mul_f32 v31,v31,v79
v_cvt_pk_bf16_f32 v84,v28,v29
buffer_load_dwordx4 v1,s[36:39],s67 offen offset:2048 sc1 lds
v_cvt_pk_bf16_f32 v85,v30,v31
v_fma_f32 v32,-v92,v24,v32
v_fma_f32 v33,-v92,v25,v33
v_fma_f32 v34,-v92,v26,v34
v_fma_f32 v35,-v92,v27,v35
v_mul_f32 v80,v32,v32
v_mul_f32 v81,v33,v33
v_mul_f32 v82,v34,v34
v_mul_f32 v83,v35,v35
v_fma_f32 v80,v94,v80,v93
v_fma_f32 v81,v94,v81,v93
buffer_load_dwordx4 v1,s[36:39],s67 offen offset:3072 sc1 lds
v_fma_f32 v82,v94,v82,v93
v_fma_f32 v83,v94,v83,v93
v_mul_f32 v80,v32,v80
v_mul_f32 v81,v33,v81
v_mul_f32 v82,v34,v82
v_mul_f32 v83,v35,v83
v_exp_f32 v80,v80
v_exp_f32 v81,v81
v_exp_f32 v82,v82
v_exp_f32 v83,v83
v_fma_f32 v80,v80,v95,v95
s_lshr_b32 s72,s67,6
s_lshr_b32 s71,s62,2
s_cmp_lt_u32 s67,4194304
s_cselect_b32 s72,s59,s72
s_add_u32 m0,s71,s88
s_nop 0
buffer_load_dword v10,s[36:39],s72 offen offset:0 sc1 lds
v_fma_f32 v81,v81,v95,v95
v_fma_f32 v82,v82,v95,v95
v_fma_f32 v83,v83,v95,v95
v_rcp_f32 v80,v80
v_rcp_f32 v81,v81
v_rcp_f32 v82,v82
v_rcp_f32 v83,v83
v_mul_f32 v32,v32,v80
v_mul_f32 v33,v33,v81
v_mul_f32 v34,v34,v82
v_mul_f32 v35,v35,v83
v_cvt_pk_bf16_f32 v86,v32,v33
v_cvt_pk_bf16_f32 v87,v34,v35
buffer_store_dwordx4 v[84:87],v3,s[36:39],s73 offen offset:0 sc0 sc1
s_waitcnt vmcnt(6)
s_barrier
ds_read_b32 v16,v7 offset:8192
s_lshl_b32 s70,s54,12
v_add_u32 v76,s70,v2
v_xor_b32 v77,4096,v76
ds_read_b128 v[44:47],v76 offset:32768
ds_read_b128 v[48:51],v76 offset:33792
ds_read_b128 v[52:55],v76 offset:34816
ds_read_b128 v[56:59],v76 offset:35840
ds_read_b128 v[60:63],v77 offset:32768
ds_read_b128 v[64:67],v77 offset:33792
ds_read_b128 v[68:71],v77 offset:34816
s_lshr_b32 s70,s47,5
buffer_store_dword v5,v4,s[36:39],s70 offen offset:0 sc0 sc1
s_waitcnt lgkmcnt(7)
v_cmp_eq_u32 vcc,s60,v16
s_nop 0
s_cmp_eq_u32 vcc_hi,-1
s_cselect_b32 s67,s50,s59
s_xor_b32 s62,s63,65536
s_cmp_ge_u32 s67,s59
s_cselect_b32 s68,s50,s51
s_lshr_b32 s69,s68,5
s_cmp_lt_u32 s68,4194304
s_cselect_b32 s68,0xffffcfff,-1
s_and_b32 s68,s68,s69
s_add_u32 m0,s78,256
s_nop 0
buffer_load_dword v6,s[36:39],s68 offen offset:0 sc1 lds
s_mov_b32 m0,s62
s_nop 0
buffer_load_dwordx4 v0,s[36:39],s67 offen offset:0 sc1 lds
buffer_load_dwordx4 v0,s[36:39],s67 offen offset:1024 sc1 lds
buffer_load_dwordx4 v0,s[36:39],s67 offen offset:2048 sc1 lds
buffer_load_dwordx4 v0,s[36:39],s67 offen offset:3072 sc1 lds
s_mov_b32 s47,s48
s_mov_b32 s48,s59
s_lshr_b32 s70,s59,5
buffer_store_dword v5,v4,s[36:39],s70 offen offset:0 sc0 sc1
s_waitcnt lgkmcnt(3)
s_setpc_b64 s[92:93]
Lg1_done1_%=:
s_waitcnt vmcnt(0)
s_barrier
s_lshr_b32 s70,s47,5
buffer_store_dword v5,v4,s[36:39],s70 offen offset:0 sc0 sc1
s_lshr_b32 s70,s48,5
buffer_store_dword v5,v4,s[36:39],s70 offen offset:0 sc0 sc1
s_branch Lg1_done_%=
Lg1_done_%=:
s_waitcnt vmcnt(0) lgkmcnt(0)
s_barrier
s_branch Lg2_end_%=
Lg2_begin_%=:
Lcode_g2_b:
s_mov_b64 s[36:37],%1
s_mov_b32 s38,122727552
s_mov_b32 s39,0x00020000
s_mov_b32 s54,%2
s_mov_b32 s55,%3
s_mov_b32 s56,%4
s_mov_b32 s58,%5
s_mov_b32 s83,%6
s_mov_b32 s82,%7
s_mov_b32 s84,%8
v_mov_b32 v0,%9
v_mov_b32 v1,%10
v_mov_b32 v2,%11
v_mov_b32 v3,%12
v_mov_b32 v4,%13
v_mov_b32 v6,%14
v_mov_b32 v7,%15
v_mov_b32 v8,%16
v_mov_b32 v9,%17
v_mov_b32 v10,%18
v_mov_b32 v11,%19
v_mov_b32 v12,%20
v_mov_b32 v13,%21
v_mov_b32 v14,%22
v_mov_b32 v16,%23
v_mov_b32 v17,%24
v_mov_b32 v18,%25
v_mov_b32 v19,%26
v_mov_b32 v20,%27
s_mov_b64 s[40:41],%28
s_mov_b32 s42,2097152
s_mov_b32 s43,0x00020000
s_lshl_b32 s88,s54,11
s_mov_b32 s69,122727424
buffer_load_dword v5,off,s[36:39],s69 sc1
s_add_u32 m0,s88,0
s_add_u32 s61,s82,0
buffer_load_dwordx4 v16,s[40:43],s61 offen offset:0  lds
s_add_u32 m0,s88,1024)"
R"(
s_nop 0
buffer_load_dwordx4 v17,s[40:43],s61 offen offset:0  lds
s_add_u32 m0,s88,8064
s_nop 0
buffer_load_dwordx4 v16,s[40:43],s61 offen offset:128  lds
s_add_u32 m0,s88,9088
s_nop 0
buffer_load_dwordx4 v17,s[40:43],s61 offen offset:128  lds
s_add_u32 m0,s88,16384
s_add_u32 s61,s82,131072
buffer_load_dwordx4 v16,s[40:43],s61 offen offset:0  lds
s_add_u32 m0,s88,17408
s_nop 0
buffer_load_dwordx4 v17,s[40:43],s61 offen offset:0  lds
s_add_u32 m0,s88,24448
s_nop 0
buffer_load_dwordx4 v16,s[40:43],s61 offen offset:128  lds
s_add_u32 m0,s88,25472
s_nop 0
buffer_load_dwordx4 v17,s[40:43],s61 offen offset:128  lds
s_add_u32 m0,s88,32768
s_add_u32 s61,s82,262144
buffer_load_dwordx4 v16,s[40:43],s61 offen offset:0  lds
s_add_u32 m0,s88,33792
s_nop 0
buffer_load_dwordx4 v17,s[40:43],s61 offen offset:0  lds
s_add_u32 m0,s88,40832
s_nop 0
buffer_load_dwordx4 v16,s[40:43],s61 offen offset:128  lds
s_add_u32 m0,s88,41856
s_nop 0
buffer_load_dwordx4 v17,s[40:43],s61 offen offset:128  lds
s_add_u32 m0,s88,49152
s_add_u32 s61,s82,393216
buffer_load_dwordx4 v16,s[40:43],s61 offen offset:0  lds
s_add_u32 m0,s88,50176
s_nop 0
buffer_load_dwordx4 v17,s[40:43],s61 offen offset:0  lds
s_add_u32 m0,s88,57216
s_nop 0
buffer_load_dwordx4 v16,s[40:43],s61 offen offset:128  lds
s_add_u32 m0,s88,58240
s_nop 0
buffer_load_dwordx4 v17,s[40:43],s61 offen offset:128  lds
s_add_u32 m0,s88,65536
s_add_u32 s61,s82,524288
buffer_load_dwordx4 v16,s[40:43],s61 offen offset:0  lds
s_add_u32 m0,s88,66560
s_nop 0
buffer_load_dwordx4 v17,s[40:43],s61 offen offset:0  lds
s_add_u32 m0,s88,73600
s_nop 0
buffer_load_dwordx4 v16,s[40:43],s61 offen offset:128  lds
s_add_u32 m0,s88,74624
s_nop 0
buffer_load_dwordx4 v17,s[40:43],s61 offen offset:128  lds
s_add_u32 m0,s88,81920
s_add_u32 s61,s82,655360
buffer_load_dwordx4 v16,s[40:43],s61 offen offset:0  lds
s_add_u32 m0,s88,82944
s_nop 0
buffer_load_dwordx4 v17,s[40:43],s61 offen offset:0  lds
s_add_u32 m0,s88,89984
s_nop 0
buffer_load_dwordx4 v16,s[40:43],s61 offen offset:128  lds
s_add_u32 m0,s88,91008
s_nop 0
buffer_load_dwordx4 v17,s[40:43],s61 offen offset:128  lds
s_add_u32 m0,s88,98304
s_add_u32 s61,s82,786432
buffer_load_dwordx4 v16,s[40:43],s61 offen offset:0  lds
s_add_u32 m0,s88,99328
s_nop 0
buffer_load_dwordx4 v17,s[40:43],s61 offen offset:0  lds
s_add_u32 m0,s88,106368
s_nop 0
buffer_load_dwordx4 v16,s[40:43],s61 offen offset:128  lds
s_add_u32 m0,s88,107392
s_nop 0
buffer_load_dwordx4 v17,s[40:43],s61 offen offset:128  lds
v_add_u32 v24,49152,v18
v_add_u32 v25,49152,v19
v_add_u32 v26,98304,v18
v_add_u32 v27,98304,v19
s_waitcnt vmcnt(24)
s_barrier
s_add_u32 m0,s88,114688
s_add_u32 s61,s82,917504
buffer_load_dwordx4 v16,s[40:43],s61 offen offset:0  lds
s_add_u32 m0,s88,115712
ds_read_b64_tr_b16 a[0:1],v18 offset:0
buffer_load_dwordx4 v17,s[40:43],s61 offen offset:0  lds
s_add_u32 m0,s88,122752
ds_read_b64_tr_b16 a[2:3],v18 offset:512
buffer_load_dwordx4 v16,s[40:43],s61 offen offset:128  lds
s_add_u32 m0,s88,123776
ds_read_b64_tr_b16 a[4:5],v19 offset:0
buffer_load_dwordx4 v17,s[40:43],s61 offen offset:128  lds
ds_read_b64_tr_b16 a[6:7],v19 offset:512
ds_read_b64_tr_b16 a[8:9],v18 offset:4096
ds_read_b64_tr_b16 a[10:11],v18 offset:4608
ds_read_b64_tr_b16 a[12:13],v19 offset:4096
ds_read_b64_tr_b16 a[14:15],v19 offset:4608
s_waitcnt vmcnt(24)
s_barrier
s_add_u32 m0,s88,131072
s_add_u32 s61,s82,1048576
buffer_load_dwordx4 v16,s[40:43],s61 offen offset:0  lds
s_add_u32 m0,s88,132096
ds_read_b64_tr_b16 a[16:17],v18 offset:16384
buffer_load_dwordx4 v17,s[40:43],s61 offen offset:0  lds
s_add_u32 m0,s88,139136
ds_read_b64_tr_b16 a[18:19],v18 offset:16896
buffer_load_dwordx4 v16,s[40:43],s61 offen offset:128  lds
s_add_u32 m0,s88,140160
ds_read_b64_tr_b16 a[20:21],v19 offset:16384
buffer_load_dwordx4 v17,s[40:43],s61 offen offset:128  lds
ds_read_b64_tr_b16 a[22:23],v19 offset:16896
ds_read_b64_tr_b16 a[24:25],v18 offset:20480
ds_read_b64_tr_b16 a[26:27],v18 offset:20992
ds_read_b64_tr_b16 a[28:29],v19 offset:20480
ds_read_b64_tr_b16 a[30:31],v19 offset:20992
s_waitcnt lgkmcnt(8)
s_waitcnt vmcnt(24)
s_barrier
s_add_u32 m0,s88,0
s_add_u32 s61,s82,1179648
buffer_load_dwordx4 v16,s[40:43],s61 offen offset:0  lds
s_add_u32 m0,s88,1024
ds_read_b64_tr_b16 a[32:33],v18 offset:32768
buffer_load_dwordx4 v17,s[40:43],s61 offen offset:0  lds
s_add_u32 m0,s88,8064
ds_read_b64_tr_b16 a[34:35],v18 offset:33280
buffer_load_dwordx4 v16,s[40:43],s61 offen offset:128  lds
s_add_u32 m0,s88,9088
ds_read_b64_tr_b16 a[36:37],v19 offset:32768
buffer_load_dwordx4 v17,s[40:43],s61 offen offset:128  lds
ds_read_b64_tr_b16 a[38:39],v19 offset:33280
ds_read_b64_tr_b16 a[40:41],v18 offset:36864
ds_read_b64_tr_b16 a[42:43],v18 offset:37376
ds_read_b64_tr_b16 a[44:45],v19 offset:36864
ds_read_b64_tr_b16 a[46:47],v19 offset:37376
s_waitcnt lgkmcnt(8)
s_waitcnt vmcnt(24)
s_barrier
s_add_u32 m0,s88,16384
s_add_u32 s61,s82,1310720
buffer_load_dwordx4 v16,s[40:43],s61 offen offset:0  lds
s_add_u32 m0,s88,17408
ds_read_b64_tr_b16 a[48:49],v24 offset:0
buffer_load_dwordx4 v17,s[40:43],s61 offen offset:0  lds
s_add_u32 m0,s88,24448
ds_read_b64_tr_b16 a[50:51],v24 offset:512
buffer_load_dwordx4 v16,s[40:43],s61 offen offset:128  lds
s_add_u32 m0,s88,25472
ds_read_b64_tr_b16 a[52:53],v25 offset:0
buffer_load_dwordx4 v17,s[40:43],s61 offen offset:128  lds
ds_read_b64_tr_b16 a[54:55],v25 offset:512
ds_read_b64_tr_b16 a[56:57],v24 offset:4096
ds_read_b64_tr_b16 a[58:59],v24 offset:4608
ds_read_b64_tr_b16 a[60:61],v25 offset:4096
ds_read_b64_tr_b16 a[62:63],v25 offset:4608
s_waitcnt lgkmcnt(8)
s_waitcnt vmcnt(24)
s_barrier
s_add_u32 m0,s88,32768
s_add_u32 s61,s82,1441792
buffer_load_dwordx4 v16,s[40:43],s61 offen offset:0  lds
s_add_u32 m0,s88,33792
ds_read_b64_tr_b16 a[64:65],v24 offset:16384
buffer_load_dwordx4 v17,s[40:43],s61 offen offset:0  lds
s_add_u32 m0,s88,40832
ds_read_b64_tr_b16 a[66:67],v24 offset:16896
buffer_load_dwordx4 v16,s[40:43],s61 offen offset:128  lds
s_add_u32 m0,s88,41856
ds_read_b64_tr_b16 a[68:69],v25 offset:16384
buffer_load_dwordx4 v17,s[40:43],s61 offen offset:128  lds
ds_read_b64_tr_b16 a[70:71],v25 offset:16896
ds_read_b64_tr_b16 a[72:73],v24 offset:20480
ds_read_b64_tr_b16 a[74:75],v24 offset:20992
ds_read_b64_tr_b16 a[76:77],v25 offset:20480
ds_read_b64_tr_b16 a[78:79],v25 offset:20992
s_waitcnt lgkmcnt(8)
s_waitcnt vmcnt(24)
s_barrier
s_add_u32 m0,s88,49152
s_add_u32 s61,s82,1572864
buffer_load_dwordx4 v16,s[40:43],s61 offen offset:0  lds
s_add_u32 m0,s88,50176
ds_read_b64_tr_b16 a[80:81],v24 offset:32768
buffer_load_dwordx4 v17,s[40:43],s61 offen offset:0  lds
s_add_u32 m0,s88,57216
ds_read_b64_tr_b16 a[82:83],v24 offset:33280
buffer_load_dwordx4 v16,s[40:43],s61 offen offset:128  lds
s_add_u32 m0,s88,58240
ds_read_b64_tr_b16 a[84:85],v25 offset:32768
buffer_load_dwordx4 v17,s[40:43],s61 offen offset:128  lds
ds_read_b64_tr_b16 a[86:87],v25 offset:33280
ds_read_b64_tr_b16 a[88:89],v24 offset:36864
ds_read_b64_tr_b16 a[90:91],v24 offset:37376)"
R"(
ds_read_b64_tr_b16 a[92:93],v25 offset:36864
ds_read_b64_tr_b16 a[94:95],v25 offset:37376
s_waitcnt lgkmcnt(8)
s_waitcnt vmcnt(24)
s_barrier
s_add_u32 m0,s88,65536
s_add_u32 s61,s82,1703936
buffer_load_dwordx4 v16,s[40:43],s61 offen offset:0  lds
s_add_u32 m0,s88,66560
ds_read_b64_tr_b16 a[96:97],v26 offset:0
buffer_load_dwordx4 v17,s[40:43],s61 offen offset:0  lds
s_add_u32 m0,s88,73600
ds_read_b64_tr_b16 a[98:99],v26 offset:512
buffer_load_dwordx4 v16,s[40:43],s61 offen offset:128  lds
s_add_u32 m0,s88,74624
ds_read_b64_tr_b16 a[100:101],v27 offset:0
buffer_load_dwordx4 v17,s[40:43],s61 offen offset:128  lds
ds_read_b64_tr_b16 a[102:103],v27 offset:512
ds_read_b64_tr_b16 a[104:105],v26 offset:4096
ds_read_b64_tr_b16 a[106:107],v26 offset:4608
ds_read_b64_tr_b16 a[108:109],v27 offset:4096
ds_read_b64_tr_b16 a[110:111],v27 offset:4608
s_waitcnt lgkmcnt(8)
s_waitcnt vmcnt(24)
s_barrier
s_add_u32 m0,s88,81920
s_add_u32 s61,s82,1835008
buffer_load_dwordx4 v16,s[40:43],s61 offen offset:0  lds
s_add_u32 m0,s88,82944
ds_read_b64_tr_b16 a[112:113],v26 offset:16384
buffer_load_dwordx4 v17,s[40:43],s61 offen offset:0  lds
s_add_u32 m0,s88,89984
ds_read_b64_tr_b16 a[114:115],v26 offset:16896
buffer_load_dwordx4 v16,s[40:43],s61 offen offset:128  lds
s_add_u32 m0,s88,91008
ds_read_b64_tr_b16 a[116:117],v27 offset:16384
buffer_load_dwordx4 v17,s[40:43],s61 offen offset:128  lds
ds_read_b64_tr_b16 a[118:119],v27 offset:16896
ds_read_b64_tr_b16 a[120:121],v26 offset:20480
ds_read_b64_tr_b16 a[122:123],v26 offset:20992
ds_read_b64_tr_b16 a[124:125],v27 offset:20480
ds_read_b64_tr_b16 a[126:127],v27 offset:20992
s_waitcnt lgkmcnt(8)
s_waitcnt vmcnt(24)
s_barrier
s_add_u32 m0,s88,98304
s_add_u32 s61,s82,1966080
buffer_load_dwordx4 v16,s[40:43],s61 offen offset:0  lds
s_add_u32 m0,s88,99328
ds_read_b64_tr_b16 a[128:129],v26 offset:32768
buffer_load_dwordx4 v17,s[40:43],s61 offen offset:0  lds
s_add_u32 m0,s88,106368
ds_read_b64_tr_b16 a[130:131],v26 offset:33280
buffer_load_dwordx4 v16,s[40:43],s61 offen offset:128  lds
s_add_u32 m0,s88,107392
ds_read_b64_tr_b16 a[132:133],v27 offset:32768
buffer_load_dwordx4 v17,s[40:43],s61 offen offset:128  lds
ds_read_b64_tr_b16 a[134:135],v27 offset:33280
ds_read_b64_tr_b16 a[136:137],v26 offset:36864
ds_read_b64_tr_b16 a[138:139],v26 offset:37376
ds_read_b64_tr_b16 a[140:141],v27 offset:36864
ds_read_b64_tr_b16 a[142:143],v27 offset:37376
s_waitcnt lgkmcnt(8)
s_waitcnt vmcnt(24)
s_barrier
ds_read_b64_tr_b16 a[144:145],v18 offset:0
ds_read_b64_tr_b16 a[146:147],v18 offset:512
ds_read_b64_tr_b16 a[148:149],v19 offset:0
ds_read_b64_tr_b16 a[150:151],v19 offset:512
ds_read_b64_tr_b16 a[152:153],v18 offset:4096
ds_read_b64_tr_b16 a[154:155],v18 offset:4608
ds_read_b64_tr_b16 a[156:157],v19 offset:4096
ds_read_b64_tr_b16 a[158:159],v19 offset:4608
s_waitcnt lgkmcnt(8)
s_waitcnt vmcnt(20)
s_barrier
ds_read_b64_tr_b16 a[160:161],v18 offset:16384
ds_read_b64_tr_b16 a[162:163],v18 offset:16896
ds_read_b64_tr_b16 a[164:165],v19 offset:16384
ds_read_b64_tr_b16 a[166:167],v19 offset:16896
ds_read_b64_tr_b16 a[168:169],v18 offset:20480
ds_read_b64_tr_b16 a[170:171],v18 offset:20992
ds_read_b64_tr_b16 a[172:173],v19 offset:20480
ds_read_b64_tr_b16 a[174:175],v19 offset:20992
s_waitcnt lgkmcnt(8)
s_waitcnt vmcnt(16)
s_barrier
ds_read_b64_tr_b16 a[176:177],v18 offset:32768
ds_read_b64_tr_b16 a[178:179],v18 offset:33280
ds_read_b64_tr_b16 a[180:181],v19 offset:32768
ds_read_b64_tr_b16 a[182:183],v19 offset:33280
ds_read_b64_tr_b16 a[184:185],v18 offset:36864
ds_read_b64_tr_b16 a[186:187],v18 offset:37376
ds_read_b64_tr_b16 a[188:189],v19 offset:36864
ds_read_b64_tr_b16 a[190:191],v19 offset:37376
s_getpc_b64 s[46:47]
Lg2_cp_%=:
s_add_u32 s46,s46,Lg2_init_%=-Lg2_cp_%=
s_addc_u32 s47,s47,0
s_lshl_b32 s68,s54,13
v_lshl_add_u32 v48,v2,3,s68
v_min_u32 v49,Lg2_done_%=-Lg2_init_%=,v48
global_load_dword v52,v49,s[46:47]
s_waitcnt lgkmcnt(8)
s_waitcnt vmcnt(13)
s_barrier
ds_read_b64_tr_b16 a[192:193],v24 offset:0
ds_read_b64_tr_b16 a[194:195],v24 offset:512
ds_read_b64_tr_b16 a[196:197],v25 offset:0
ds_read_b64_tr_b16 a[198:199],v25 offset:512
ds_read_b64_tr_b16 a[200:201],v24 offset:4096
ds_read_b64_tr_b16 a[202:203],v24 offset:4608
ds_read_b64_tr_b16 a[204:205],v25 offset:4096
ds_read_b64_tr_b16 a[206:207],v25 offset:4608
s_waitcnt lgkmcnt(8)
s_waitcnt vmcnt(9)
s_barrier
ds_read_b64_tr_b16 a[208:209],v24 offset:16384
ds_read_b64_tr_b16 a[210:211],v24 offset:16896
ds_read_b64_tr_b16 a[212:213],v25 offset:16384
ds_read_b64_tr_b16 a[214:215],v25 offset:16896
ds_read_b64_tr_b16 a[216:217],v24 offset:20480
ds_read_b64_tr_b16 a[218:219],v24 offset:20992
ds_read_b64_tr_b16 a[220:221],v25 offset:20480
ds_read_b64_tr_b16 a[222:223],v25 offset:20992
s_waitcnt lgkmcnt(8)
s_waitcnt vmcnt(5)
s_barrier
ds_read_b64_tr_b16 a[224:225],v24 offset:32768
ds_read_b64_tr_b16 a[226:227],v24 offset:33280
ds_read_b64_tr_b16 a[228:229],v25 offset:32768
ds_read_b64_tr_b16 a[230:231],v25 offset:33280
ds_read_b64_tr_b16 a[232:233],v24 offset:36864
ds_read_b64_tr_b16 a[234:235],v24 offset:37376
ds_read_b64_tr_b16 a[236:237],v25 offset:36864
ds_read_b64_tr_b16 a[238:239],v25 offset:37376
s_waitcnt lgkmcnt(8)
s_waitcnt vmcnt(1)
s_barrier
ds_read_b64_tr_b16 a[240:241],v26 offset:0
ds_read_b64_tr_b16 a[242:243],v26 offset:512
ds_read_b64_tr_b16 a[244:245],v27 offset:0
ds_read_b64_tr_b16 a[246:247],v27 offset:512
ds_read_b64_tr_b16 a[248:249],v26 offset:4096
ds_read_b64_tr_b16 a[250:251],v26 offset:4608
ds_read_b64_tr_b16 a[252:253],v27 offset:4096
ds_read_b64_tr_b16 a[254:255],v27 offset:4608
s_waitcnt lgkmcnt(8)
s_waitcnt lgkmcnt(0)
s_waitcnt lgkmcnt(0)
s_barrier
v_readfirstlane_b32 s60,v5
s_nop 3
s_add_u32 s60,s60,1
s_cmp_eq_u32 s60,0
s_cselect_b32 s60,1,s60
Lg2_init_%=:
v_mov_b32 v5,s60
s_mov_b32 s59,536870912
s_add_u32 s57,s56,131072
s_mov_b32 s79,0
s_mov_b32 s75,0x3a800000
s_mov_b32 s76,0xc0135761
s_mov_b32 s77,0x3d372713
s_mov_b32 s44,s59
s_mov_b32 s45,s59
s_mov_b32 s46,s59
s_mov_b32 s47,s59
s_mov_b32 s48,s83
s_add_u32 s49,s48,s55
s_cmp_eq_u32 s49,s56
s_cselect_b32 s49,s57,s49
s_cmp_eq_u32 s49,s58
s_cselect_b32 s49,s59,s49
s_add_u32 s50,s49,s55
s_cmp_eq_u32 s50,s56
s_cselect_b32 s50,s57,s50
s_cmp_eq_u32 s50,s58
s_cselect_b32 s50,s59,s50
s_add_u32 s51,s50,s55
s_cmp_eq_u32 s51,s56
s_cselect_b32 s51,s57,s51
s_cmp_eq_u32 s51,s58
s_cselect_b32 s51,s59,s51
s_add_u32 s52,s51,s55
s_cmp_eq_u32 s52,s56
s_cselect_b32 s52,s57,s52
s_cmp_eq_u32 s52,s58
s_cselect_b32 s52,s59,s52
s_lshl_b32 s63,s54,12
s_add_u32 s63,s63,65536
s_add_u32 s64,s63,32768
s_lshl_b32 s88,s54,10
s_add_u32 s88,s88,131072
s_lshl_b32 s78,s54,11
s_add_u32 s78,s78,132608
s_getpc_b64 s[90:91]
Lg2_pc_%=:
s_mov_b32 s68,Lg2_d1w0_%=-Lg2_pc_%=
s_cmp_eq_u32 s54,1)"
R"(
s_cselect_b32 s68,Lg2_d1w1_%=-Lg2_pc_%=,s68
s_cmp_eq_u32 s54,2
s_cselect_b32 s68,Lg2_d1w2_%=-Lg2_pc_%=,s68
s_cmp_eq_u32 s54,3
s_cselect_b32 s68,Lg2_d1w3_%=-Lg2_pc_%=,s68
s_add_u32 s92,s90,s68
s_addc_u32 s93,s91,0
s_mov_b32 s68,Lg2_d0w0_%=-Lg2_pc_%=
s_cmp_eq_u32 s54,1
s_cselect_b32 s68,Lg2_d0w1_%=-Lg2_pc_%=,s68
s_cmp_eq_u32 s54,2
s_cselect_b32 s68,Lg2_d0w2_%=-Lg2_pc_%=,s68
s_cmp_eq_u32 s54,3
s_cselect_b32 s68,Lg2_d0w3_%=-Lg2_pc_%=,s68
s_add_u32 s90,s90,s68
s_addc_u32 s91,s91,0
s_mov_b32 s66,s59
s_mov_b32 s65,0
s_mov_b32 s62,s63
s_branch Lg2_slow0_%=
Lg2_d0w0_%=:
v_mfma_f32_16x16x32_bf16 v[16:19],a[0:3],v[32:35],0
s_add_u32 m0,s62,16384
ds_read_b128 v[60:63],v2 offset:7168
v_lshlrev_b32 v64,16,v84
v_mfma_f32_16x16x32_bf16 v[20:23],a[4:7],v[32:35],0
buffer_load_dwordx4 v1,s[36:39],s66 offen offset:0 sc1 lds
v_mfma_f32_16x16x32_bf16 v[16:19],a[8:11],v[36:39],v[16:19]
s_lshr_b32 s73,s47,0
ds_read_b128 v[32:35],v2 offset:8192
v_and_b32 v65,0xffff0000,v84
v_mfma_f32_16x16x32_bf16 v[20:23],a[12:15],v[36:39],v[20:23]
s_xor_b32 s87,s64,65536
v_lshlrev_b32 v66,16,v85
v_and_b32 v67,0xffff0000,v85
v_mfma_f32_16x16x32_bf16 v[16:19],a[16:19],v[40:43],v[16:19]
s_cmp_ge_u32 s66,s59
ds_read_b128 v[36:39],v2 offset:9216
v_lshlrev_b32 v68,16,v86
v_mfma_f32_16x16x32_bf16 v[20:23],a[20:23],v[40:43],v[20:23]
buffer_load_dwordx4 v1,s[36:39],s66 offen offset:1024 sc1 lds
v_mfma_f32_16x16x32_bf16 v[16:19],a[24:27],v[44:47],v[16:19]
s_cselect_b32 s86,s49,s50
ds_read_b128 v[40:43],v2 offset:10240
v_and_b32 v69,0xffff0000,v86
v_mfma_f32_16x16x32_bf16 v[20:23],a[28:31],v[44:47],v[20:23]
s_waitcnt lgkmcnt(3)
v_lshlrev_b32 v70,16,v87
v_and_b32 v71,0xffff0000,v87
v_mfma_f32_16x16x32_bf16 v[16:19],a[32:35],v[48:51],v[16:19]
s_cselect_b32 s87,s87,s63
ds_read_b128 v[44:47],v2 offset:11264
v_fma_f32 v64,v64,v91,v90
v_mfma_f32_16x16x32_bf16 v[20:23],a[36:39],v[48:51],v[20:23]
buffer_load_dwordx4 v1,s[36:39],s66 offen offset:2048 sc1 lds
v_mfma_f32_16x16x32_bf16 v[16:19],a[40:43],v[52:55],v[16:19]
s_lshr_b32 s72,s66,6
ds_read_b128 v[48:51],v2 offset:12288
v_fma_f32 v65,v65,v91,v90
v_mfma_f32_16x16x32_bf16 v[20:23],a[44:47],v[52:55],v[20:23]
s_lshr_b32 s71,s62,2
v_fma_f32 v66,v66,v91,v90
v_fma_f32 v67,v67,v91,v90
v_mfma_f32_16x16x32_bf16 v[16:19],a[48:51],v[56:59],v[16:19]
s_mov_b32 s62,s87
ds_read_b128 v[52:55],v2 offset:13312
v_fma_f32 v68,v68,v91,v90
v_mfma_f32_16x16x32_bf16 v[20:23],a[52:55],v[56:59],v[20:23]
buffer_load_dwordx4 v1,s[36:39],s66 offen offset:3072 sc1 lds
v_mfma_f32_16x16x32_bf16 v[16:19],a[56:59],v[60:63],v[16:19]
s_add_u32 m0,s71,s88
ds_read_b128 v[56:59],v2 offset:14336
v_fma_f32 v69,v69,v91,v90
v_mfma_f32_16x16x32_bf16 v[20:23],a[60:63],v[60:63],v[20:23]
s_waitcnt lgkmcnt(3)
v_fma_f32 v70,v70,v91,v90
v_fma_f32 v71,v71,v91,v90
v_mfma_f32_16x16x32_bf16 v[16:19],a[64:67],v[32:35],v[16:19]
s_waitcnt vmcnt(10)
ds_read_b128 v[60:63],v2 offset:15360
v_add_f32 v24,v24,v64
v_mfma_f32_16x16x32_bf16 v[20:23],a[68:71],v[32:35],v[20:23]
s_barrier
v_mfma_f32_16x16x32_bf16 v[16:19],a[72:75],v[36:39],v[16:19]
s_lshr_b32 s68,s50,5
ds_read_b128 v[32:35],v2 offset:16384
v_add_f32 v25,v25,v65
v_mfma_f32_16x16x32_bf16 v[20:23],a[76:79],v[36:39],v[20:23]
buffer_load_dwordx4 v10,s[36:39],s66 offen offset:0 sc1 lds
v_mfma_f32_16x16x32_bf16 v[16:19],a[80:83],v[40:43],v[16:19]
buffer_load_dword v11,s[36:39],s72 offen offset:1024 sc1 lds
v_mfma_f32_16x16x32_bf16 v[20:23],a[84:87],v[40:43],v[20:23]
s_lshr_b32 s70,s51,5
ds_read_b128 v[36:39],v2 offset:17408
v_add_f32 v26,v26,v66
v_mfma_f32_16x16x32_bf16 v[16:19],a[88:91],v[44:47],v[16:19]
ds_read_b128 v[40:43],v2 offset:18432
v_add_f32 v27,v27,v67
v_mfma_f32_16x16x32_bf16 v[20:23],a[92:95],v[44:47],v[20:23]
s_waitcnt lgkmcnt(3)
v_add_f32 v28,v28,v68
v_add_f32 v29,v29,v69
v_add_f32 v30,v30,v70
v_add_f32 v31,v31,v71
v_cvt_pk_bf16_f32 v72,v24,v25
v_cvt_pk_bf16_f32 v73,v26,v27
v_cvt_pk_bf16_f32 v74,v28,v29
v_cvt_pk_bf16_f32 v75,v30,v31
v_mfma_f32_16x16x32_bf16 v[16:19],a[96:99],v[48:51],v[16:19]
buffer_store_dwordx4 v[72:75],v3,s[36:39],s73 offen offset:0 sc0 sc1
ds_read_b128 v[44:47],v2 offset:19456
v_add_f32 v64,v24,v25
v_mul_f32 v65,v24,v24
v_mfma_f32_16x16x32_bf16 v[20:23],a[100:103],v[48:51],v[20:23]
v_fma_f32 v65,v25,v25,v65
v_add_f32 v64,v64,v26
v_mfma_f32_16x16x32_bf16 v[16:19],a[104:107],v[52:55],v[16:19]
ds_read_b128 v[48:51],v2 offset:20480
v_fma_f32 v65,v26,v26,v65
v_mfma_f32_16x16x32_bf16 v[20:23],a[108:111],v[52:55],v[20:23]
v_add_f32 v64,v64,v27
v_fma_f32 v65,v27,v27,v65
v_mfma_f32_16x16x32_bf16 v[16:19],a[112:115],v[56:59],v[16:19]
ds_read_b128 v[52:55],v2 offset:21504
v_add_f32 v64,v64,v28
v_mfma_f32_16x16x32_bf16 v[20:23],a[116:119],v[56:59],v[20:23]
v_fma_f32 v65,v28,v28,v65
v_add_f32 v64,v64,v29
v_fma_f32 v65,v29,v29,v65
v_add_f32 v64,v64,v30
v_fma_f32 v65,v30,v30,v65
v_add_f32 v64,v64,v31
v_fma_f32 v65,v31,v31,v65
v_mfma_f32_16x16x32_bf16 v[16:19],a[120:123],v[60:63],v[16:19]
s_nop 0
v_permlane16_swap_b32 v64,v65
v_add_f32 v64,v64,v65
v_mov_b32 v65,v64
v_mfma_f32_16x16x32_bf16 v[20:23],a[124:127],v[60:63],v[20:23]
s_waitcnt lgkmcnt(2)
ds_read_b128 v[56:59],v2 offset:22528
v_permlane32_swap_b32 v64,v65
v_add_f32 v64,v64,v65
v_mfma_f32_16x16x32_bf16 v[16:19],a[128:131],v[32:35],v[16:19]
ds_write_b32 v13,v64 offset:0
v_mfma_f32_16x16x32_bf16 v[20:23],a[132:135],v[32:35],v[20:23]
ds_read_b128 v[60:63],v2 offset:23552
v_mfma_f32_16x16x32_bf16 v[16:19],a[136:139],v[36:39],v[16:19]
ds_read_b128 v[32:35],v2 offset:24576
v_mfma_f32_16x16x32_bf16 v[20:23],a[140:143],v[36:39],v[20:23]
v_mfma_f32_16x16x32_bf16 v[16:19],a[144:147],v[40:43],v[16:19]
s_waitcnt vmcnt(11) lgkmcnt(2)
ds_read_b128 v[36:39],v2 offset:25600
v_mfma_f32_16x16x32_bf16 v[20:23],a[148:151],v[40:43],v[20:23]
s_barrier
v_mfma_f32_16x16x32_bf16 v[16:19],a[152:155],v[44:47],v[16:19]
ds_read_b128 v[40:43],v2 offset:26624
ds_read_b32 v15,v7 offset:0
v_mfma_f32_16x16x32_bf16 v[20:23],a[156:159],v[44:47],v[20:23]
s_waitcnt lgkmcnt(4)
v_mfma_f32_16x16x32_bf16 v[16:19],a[160:163],v[48:51],v[16:19]
ds_read_b128 v[44:47],v2 offset:27648
v_mfma_f32_16x16x32_bf16 v[20:23],a[164:167],v[48:51],v[20:23]
v_mfma_f32_16x16x32_bf16 v[16:19],a[168:171],v[52:55],v[16:19]
s_waitcnt lgkmcnt(1)
ds_read_b128 v[48:51],v2 offset:28672
v_mfma_f32_16x16x32_bf16 v[20:23],a[172:175],v[52:55],v[20:23]
v_cmp_eq_u32 vcc,s60,v15
v_mfma_f32_16x16x32_bf16 v[16:19],a[176:179],v[56:59],v[16:19]
s_cmp_eq_u32 vcc_lo,-1
ds_read_b128 v[52:55],v2 offset:29696
s_cselect_b32 s67,s86,s59
v_mfma_f32_16x16x32_bf16 v[20:23],a[180:183],v[56:59],v[20:23]
s_cmp_ge_u32 s67,s59
s_cselect_b32 s68,s68,s70
s_add_u32 m0,s78,256
v_mfma_f32_16x16x32_bf16 v[16:19],a[184:187],v[60:63],v[16:19]
buffer_load_dword v6,s[36:39],s68 offen offset:0 sc1 lds
v_mfma_f32_16x16x32_bf16 v[20:23],a[188:191],v[60:63],v[20:23]
s_waitcnt lgkmcnt(2)
ds_read_b128 v[56:59],v2 offset:30720
s_xor_b32 s63,s63,65536
v_mfma_f32_16x16x32_bf16 v[16:19],a[192:195],v[32:35],v[16:19]
s_waitcnt vmcnt(8)
ds_read_b128 v[60:63],v2 offset:31744
s_mov_b32 m0,s62
v_mfma_f32_16x16x32_bf16 v[20:23],a[196:199],v[32:35],v[20:23]
s_barrier
v_mfma_f32_16x16x32_bf16 v[16:19],a[200:203],v[36:39],v[16:19]
buffer_load_dwordx4 v0,s[36:39],s67 offen offset:0 sc1 lds
v_mfma_f32_16x16x32_bf16 v[20:23],a[204:207],v[36:39],v[20:23]
s_add_u32 s53,s52,s55
ds_read_b128 v[32:35],v2 offset:32768
ds_read_b128 v[80:83],v8 offset:0
v_mfma_f32_16x16x32_bf16 v[16:19],a[208:211],v[40:43],v[16:19]
s_cmp_eq_u32 s53,s56
ds_read_b128 v[36:39],v2 offset:33792
ds_read_b64 v[88:89],v9 offset:1024
v_mfma_f32_16x16x32_bf16 v[20:23],a[212:215],v[40:43],v[20:23]
s_cselect_b32 s53,s57,s53)"
R"(
s_cmp_eq_u32 s53,s58
s_cselect_b32 s53,s59,s53
v_mfma_f32_16x16x32_bf16 v[16:19],a[216:219],v[44:47],v[16:19]
buffer_load_dwordx4 v0,s[36:39],s67 offen offset:1024 sc1 lds
v_mfma_f32_16x16x32_bf16 v[20:23],a[220:223],v[44:47],v[20:23]
s_waitcnt lgkmcnt(4)
ds_read_b128 v[40:43],v2 offset:34816
s_cmp_ge_u32 s66,s59
v_mfma_f32_16x16x32_bf16 v[16:19],a[224:227],v[48:51],v[16:19]
ds_read_b128 v[44:47],v2 offset:35840
v_mfma_f32_16x16x32_bf16 v[20:23],a[228:231],v[48:51],v[20:23]
v_mfma_f32_16x16x32_bf16 v[16:19],a[232:235],v[52:55],v[16:19]
s_waitcnt lgkmcnt(2)
buffer_load_dwordx4 v0,s[36:39],s67 offen offset:2048 sc1 lds
v_mfma_f32_16x16x32_bf16 v[20:23],a[236:239],v[52:55],v[20:23]
ds_read_b128 v[48:51],v2 offset:36864
v_mfma_f32_16x16x32_bf16 v[16:19],a[240:243],v[56:59],v[16:19]
ds_read_b128 v[52:55],v2 offset:37888
v_mfma_f32_16x16x32_bf16 v[20:23],a[244:247],v[56:59],v[20:23]
v_mfma_f32_16x16x32_bf16 v[16:19],a[248:251],v[60:63],v[16:19]
s_waitcnt lgkmcnt(2)
ds_read_b128 v[56:59],v2 offset:38912
buffer_load_dwordx4 v0,s[36:39],s67 offen offset:3072 sc1 lds
v_mfma_f32_16x16x32_bf16 v[20:23],a[252:255],v[60:63],v[20:23]
s_cbranch_scc1 Lg2_slow1_%=
Lg2_d1w0_%=:
v_mfma_f32_16x16x32_bf16 v[24:27],a[0:3],v[32:35],0
s_add_u32 m0,s62,16384
ds_read_b128 v[60:63],v2 offset:39936
v_lshlrev_b32 v64,16,v80
v_mfma_f32_16x16x32_bf16 v[28:31],a[4:7],v[32:35],0
buffer_load_dwordx4 v1,s[36:39],s67 offen offset:0 sc1 lds
v_mfma_f32_16x16x32_bf16 v[24:27],a[8:11],v[36:39],v[24:27]
s_lshr_b32 s73,s48,0
ds_read_b128 v[32:35],v2 offset:40960
v_and_b32 v65,0xffff0000,v80
v_mfma_f32_16x16x32_bf16 v[28:31],a[12:15],v[36:39],v[28:31]
s_xor_b32 s87,s63,65536
v_lshlrev_b32 v66,16,v81
v_and_b32 v67,0xffff0000,v81
v_mfma_f32_16x16x32_bf16 v[24:27],a[16:19],v[40:43],v[24:27]
s_cmp_ge_u32 s67,s59
ds_read_b128 v[36:39],v2 offset:41984
v_lshlrev_b32 v68,16,v82
v_mfma_f32_16x16x32_bf16 v[28:31],a[20:23],v[40:43],v[28:31]
buffer_load_dwordx4 v1,s[36:39],s67 offen offset:1024 sc1 lds
v_mfma_f32_16x16x32_bf16 v[24:27],a[24:27],v[44:47],v[24:27]
s_cselect_b32 s86,s50,s51
ds_read_b128 v[40:43],v2 offset:43008
v_and_b32 v69,0xffff0000,v82
v_mfma_f32_16x16x32_bf16 v[28:31],a[28:31],v[44:47],v[28:31]
s_waitcnt lgkmcnt(3)
v_lshlrev_b32 v70,16,v83
v_and_b32 v71,0xffff0000,v83
v_mfma_f32_16x16x32_bf16 v[24:27],a[32:35],v[48:51],v[24:27]
s_cselect_b32 s87,s87,s64
ds_read_b128 v[44:47],v2 offset:44032
v_fma_f32 v64,v64,v89,v88
v_mfma_f32_16x16x32_bf16 v[28:31],a[36:39],v[48:51],v[28:31]
buffer_load_dwordx4 v1,s[36:39],s67 offen offset:2048 sc1 lds
v_mfma_f32_16x16x32_bf16 v[24:27],a[40:43],v[52:55],v[24:27]
s_lshr_b32 s72,s67,6
ds_read_b128 v[48:51],v2 offset:45056
v_fma_f32 v65,v65,v89,v88
v_mfma_f32_16x16x32_bf16 v[28:31],a[44:47],v[52:55],v[28:31]
s_lshr_b32 s71,s62,2
v_fma_f32 v66,v66,v89,v88
v_fma_f32 v67,v67,v89,v88
v_mfma_f32_16x16x32_bf16 v[24:27],a[48:51],v[56:59],v[24:27]
s_mov_b32 s62,s87
ds_read_b128 v[52:55],v2 offset:46080
v_fma_f32 v68,v68,v89,v88
v_mfma_f32_16x16x32_bf16 v[28:31],a[52:55],v[56:59],v[28:31]
buffer_load_dwordx4 v1,s[36:39],s67 offen offset:3072 sc1 lds
v_mfma_f32_16x16x32_bf16 v[24:27],a[56:59],v[60:63],v[24:27]
s_add_u32 m0,s71,s88
ds_read_b128 v[56:59],v2 offset:47104
v_fma_f32 v69,v69,v89,v88
v_mfma_f32_16x16x32_bf16 v[28:31],a[60:63],v[60:63],v[28:31]
s_waitcnt lgkmcnt(3)
v_fma_f32 v70,v70,v89,v88
v_fma_f32 v71,v71,v89,v88
v_mfma_f32_16x16x32_bf16 v[24:27],a[64:67],v[32:35],v[24:27]
s_waitcnt vmcnt(10)
ds_read_b128 v[60:63],v2 offset:48128
v_add_f32 v16,v16,v64
v_mfma_f32_16x16x32_bf16 v[28:31],a[68:71],v[32:35],v[28:31]
s_barrier
v_mfma_f32_16x16x32_bf16 v[24:27],a[72:75],v[36:39],v[24:27]
s_lshr_b32 s68,s51,5
ds_read_b128 v[32:35],v2 offset:49152
v_add_f32 v17,v17,v65
v_mfma_f32_16x16x32_bf16 v[28:31],a[76:79],v[36:39],v[28:31]
buffer_load_dwordx4 v10,s[36:39],s67 offen offset:0 sc1 lds
v_mfma_f32_16x16x32_bf16 v[24:27],a[80:83],v[40:43],v[24:27]
buffer_load_dword v11,s[36:39],s72 offen offset:1024 sc1 lds
v_mfma_f32_16x16x32_bf16 v[28:31],a[84:87],v[40:43],v[28:31]
s_lshr_b32 s70,s52,5
ds_read_b128 v[36:39],v2 offset:50176
v_add_f32 v18,v18,v66
v_mfma_f32_16x16x32_bf16 v[24:27],a[88:91],v[44:47],v[24:27]
s_mov_b64 s[44:45],s[46:47]
ds_read_b128 v[40:43],v2 offset:51200
v_add_f32 v19,v19,v67
v_mfma_f32_16x16x32_bf16 v[28:31],a[92:95],v[44:47],v[28:31]
s_waitcnt lgkmcnt(3)
v_add_f32 v20,v20,v68
v_add_f32 v21,v21,v69
v_add_f32 v22,v22,v70
v_add_f32 v23,v23,v71
v_cvt_pk_bf16_f32 v72,v16,v17
v_cvt_pk_bf16_f32 v73,v18,v19
v_cvt_pk_bf16_f32 v74,v20,v21
v_cvt_pk_bf16_f32 v75,v22,v23
v_mfma_f32_16x16x32_bf16 v[24:27],a[96:99],v[48:51],v[24:27]
buffer_store_dwordx4 v[72:75],v3,s[36:39],s73 offen offset:0 sc0 sc1
ds_read_b128 v[44:47],v2 offset:52224
v_add_f32 v64,v16,v17
v_mul_f32 v65,v16,v16
v_mfma_f32_16x16x32_bf16 v[28:31],a[100:103],v[48:51],v[28:31]
s_mov_b64 s[46:47],s[48:49]
v_fma_f32 v65,v17,v17,v65
v_add_f32 v64,v64,v18
v_mfma_f32_16x16x32_bf16 v[24:27],a[104:107],v[52:55],v[24:27]
s_mov_b64 s[48:49],s[50:51]
ds_read_b128 v[48:51],v2 offset:53248
v_fma_f32 v65,v18,v18,v65
v_mfma_f32_16x16x32_bf16 v[28:31],a[108:111],v[52:55],v[28:31]
s_mov_b64 s[50:51],s[52:53]
v_add_f32 v64,v64,v19
v_fma_f32 v65,v19,v19,v65
v_mfma_f32_16x16x32_bf16 v[24:27],a[112:115],v[56:59],v[24:27]
ds_read_b128 v[52:55],v2 offset:54272
v_add_f32 v64,v64,v20
v_mfma_f32_16x16x32_bf16 v[28:31],a[116:119],v[56:59],v[28:31]
v_fma_f32 v65,v20,v20,v65
v_add_f32 v64,v64,v21
v_fma_f32 v65,v21,v21,v65
v_add_f32 v64,v64,v22
v_fma_f32 v65,v22,v22,v65
v_add_f32 v64,v64,v23
v_fma_f32 v65,v23,v23,v65
v_mfma_f32_16x16x32_bf16 v[24:27],a[120:123],v[60:63],v[24:27]
s_nop 0
v_permlane16_swap_b32 v64,v65
v_add_f32 v64,v64,v65
v_mov_b32 v65,v64
v_mfma_f32_16x16x32_bf16 v[28:31],a[124:127],v[60:63],v[28:31]
s_waitcnt lgkmcnt(2)
ds_read_b128 v[56:59],v2 offset:55296
v_permlane32_swap_b32 v64,v65
v_add_f32 v64,v64,v65
v_mfma_f32_16x16x32_bf16 v[24:27],a[128:131],v[32:35],v[24:27]
ds_write_b32 v13,v64 offset:2048
v_mfma_f32_16x16x32_bf16 v[28:31],a[132:135],v[32:35],v[28:31]
ds_read_b128 v[60:63],v2 offset:56320
v_mfma_f32_16x16x32_bf16 v[24:27],a[136:139],v[36:39],v[24:27]
ds_read_b128 v[32:35],v2 offset:57344
v_mfma_f32_16x16x32_bf16 v[28:31],a[140:143],v[36:39],v[28:31]
v_mfma_f32_16x16x32_bf16 v[24:27],a[144:147],v[40:43],v[24:27]
s_waitcnt vmcnt(11) lgkmcnt(2)
ds_read_b128 v[36:39],v2 offset:58368
v_mfma_f32_16x16x32_bf16 v[28:31],a[148:151],v[40:43],v[28:31]
s_barrier
v_mfma_f32_16x16x32_bf16 v[24:27],a[152:155],v[44:47],v[24:27]
ds_read_b128 v[40:43],v2 offset:59392
ds_read_b32 v15,v7 offset:256
v_mfma_f32_16x16x32_bf16 v[28:31],a[156:159],v[44:47],v[28:31]
s_waitcnt lgkmcnt(4)
v_mfma_f32_16x16x32_bf16 v[24:27],a[160:163],v[48:51],v[24:27]
ds_read_b128 v[44:47],v2 offset:60416
v_mfma_f32_16x16x32_bf16 v[28:31],a[164:167],v[48:51],v[28:31]
v_mfma_f32_16x16x32_bf16 v[24:27],a[168:171],v[52:55],v[24:27]
s_waitcnt lgkmcnt(1)
ds_read_b128 v[48:51],v2 offset:61440
v_mfma_f32_16x16x32_bf16 v[28:31],a[172:175],v[52:55],v[28:31]
v_cmp_eq_u32 vcc,s60,v15
v_mfma_f32_16x16x32_bf16 v[24:27],a[176:179],v[56:59],v[24:27]
s_cmp_eq_u32 vcc_lo,-1
ds_read_b128 v[52:55],v2 offset:62464
s_cselect_b32 s66,s86,s59
v_mfma_f32_16x16x32_bf16 v[28:31],a[180:183],v[56:59],v[28:31]
s_cmp_ge_u32 s66,s59
s_cselect_b32 s68,s68,s70
s_add_u32 m0,s78,0
v_mfma_f32_16x16x32_bf16 v[24:27],a[184:187],v[60:63],v[24:27]
buffer_load_dword v6,s[36:39],s68 offen offset:0 sc1 lds
v_mfma_f32_16x16x32_bf16 v[28:31],a[188:191],v[60:63],v[28:31]
s_waitcnt lgkmcnt(2)
ds_read_b128 v[56:59],v2 offset:63488
s_xor_b32 s64,s64,65536
v_mfma_f32_16x16x32_bf16 v[24:27],a[192:195],v[32:35],v[24:27]
s_waitcnt vmcnt(8)
ds_read_b128 v[60:63],v2 offset:64512
v_xor_b32 v2,65536,v2
v_mfma_f32_16x16x32_bf16 v[28:31],a[196:199],v[32:35],v[28:31]
s_barrier
s_mov_b32 m0,s62
v_mfma_f32_16x16x32_bf16 v[24:27],a[200:203],v[36:39],v[24:27]
buffer_load_dwordx4 v0,s[36:39],s66 offen offset:0 sc1 lds)"
R"(
v_mfma_f32_16x16x32_bf16 v[28:31],a[204:207],v[36:39],v[28:31]
s_add_u32 s74,s53,s55
ds_read_b128 v[32:35],v2 offset:0
ds_read_b128 v[84:87],v8 offset:8192
v_mfma_f32_16x16x32_bf16 v[24:27],a[208:211],v[40:43],v[24:27]
s_cmp_eq_u32 s74,s56
ds_read_b128 v[36:39],v2 offset:1024
ds_read_b64 v[90:91],v9 offset:9216
v_mfma_f32_16x16x32_bf16 v[28:31],a[212:215],v[40:43],v[28:31]
s_cselect_b32 s74,s57,s74
v_xor_b32 v8,16384,v8
v_xor_b32 v9,16384,v9
v_mfma_f32_16x16x32_bf16 v[24:27],a[216:219],v[44:47],v[24:27]
buffer_load_dwordx4 v0,s[36:39],s66 offen offset:1024 sc1 lds
v_mfma_f32_16x16x32_bf16 v[28:31],a[220:223],v[44:47],v[28:31]
s_waitcnt lgkmcnt(4)
ds_read_b128 v[40:43],v2 offset:2048
s_cmp_eq_u32 s74,s58
v_mfma_f32_16x16x32_bf16 v[24:27],a[224:227],v[48:51],v[24:27]
s_cselect_b32 s74,s59,s74
ds_read_b128 v[44:47],v2 offset:3072
s_mov_b32 s52,s74
v_mfma_f32_16x16x32_bf16 v[28:31],a[228:231],v[48:51],v[28:31]
s_cmp_lt_u32 s67,s59
v_mfma_f32_16x16x32_bf16 v[24:27],a[232:235],v[52:55],v[24:27]
s_waitcnt lgkmcnt(2)
buffer_load_dwordx4 v0,s[36:39],s66 offen offset:2048 sc1 lds
v_mfma_f32_16x16x32_bf16 v[28:31],a[236:239],v[52:55],v[28:31]
ds_read_b128 v[48:51],v2 offset:4096
v_mfma_f32_16x16x32_bf16 v[24:27],a[240:243],v[56:59],v[24:27]
ds_read_b128 v[52:55],v2 offset:5120
v_mfma_f32_16x16x32_bf16 v[28:31],a[244:247],v[56:59],v[28:31]
v_mfma_f32_16x16x32_bf16 v[24:27],a[248:251],v[60:63],v[24:27]
s_waitcnt lgkmcnt(2)
ds_read_b128 v[56:59],v2 offset:6144
buffer_load_dwordx4 v0,s[36:39],s66 offen offset:3072 sc1 lds
v_mfma_f32_16x16x32_bf16 v[28:31],a[252:255],v[60:63],v[28:31]
s_cbranch_scc1 Lg2_d0w0_%=
s_branch Lg2_slow0_%=
Lg2_d0w1_%=:
v_mfma_f32_16x16x32_bf16 v[16:19],a[0:3],v[32:35],0
s_add_u32 m0,s62,16384
ds_read_b128 v[60:63],v2 offset:7168
v_lshlrev_b32 v64,16,v84
v_mfma_f32_16x16x32_bf16 v[20:23],a[4:7],v[32:35],0
s_lshr_b32 s73,s47,0
v_and_b32 v65,0xffff0000,v84
v_lshlrev_b32 v66,16,v85
v_mfma_f32_16x16x32_bf16 v[16:19],a[8:11],v[36:39],v[16:19]
buffer_load_dwordx4 v1,s[36:39],s66 offen offset:0 sc1 lds
v_mfma_f32_16x16x32_bf16 v[20:23],a[12:15],v[36:39],v[20:23]
s_xor_b32 s87,s64,65536
ds_read_b128 v[32:35],v2 offset:8192
v_and_b32 v67,0xffff0000,v85
v_mfma_f32_16x16x32_bf16 v[16:19],a[16:19],v[40:43],v[16:19]
s_cmp_ge_u32 s66,s59
ds_read_b128 v[36:39],v2 offset:9216
v_lshlrev_b32 v68,16,v86
v_mfma_f32_16x16x32_bf16 v[20:23],a[20:23],v[40:43],v[20:23]
s_cselect_b32 s86,s49,s50
v_and_b32 v69,0xffff0000,v86
v_lshlrev_b32 v70,16,v87
v_mfma_f32_16x16x32_bf16 v[16:19],a[24:27],v[44:47],v[16:19]
buffer_load_dwordx4 v1,s[36:39],s66 offen offset:1024 sc1 lds
v_mfma_f32_16x16x32_bf16 v[20:23],a[28:31],v[44:47],v[20:23]
s_waitcnt lgkmcnt(2)
ds_read_b128 v[40:43],v2 offset:10240
v_and_b32 v71,0xffff0000,v87
v_mfma_f32_16x16x32_bf16 v[16:19],a[32:35],v[48:51],v[16:19]
s_cselect_b32 s87,s87,s63
ds_read_b128 v[44:47],v2 offset:11264
v_fma_f32 v64,v64,v91,v90
v_mfma_f32_16x16x32_bf16 v[20:23],a[36:39],v[48:51],v[20:23]
s_lshr_b32 s72,s66,6
v_fma_f32 v65,v65,v91,v90
v_fma_f32 v66,v66,v91,v90
v_mfma_f32_16x16x32_bf16 v[16:19],a[40:43],v[52:55],v[16:19]
buffer_load_dwordx4 v1,s[36:39],s66 offen offset:2048 sc1 lds
v_mfma_f32_16x16x32_bf16 v[20:23],a[44:47],v[52:55],v[20:23]
s_lshr_b32 s71,s62,2
ds_read_b128 v[48:51],v2 offset:12288
v_fma_f32 v67,v67,v91,v90
v_mfma_f32_16x16x32_bf16 v[16:19],a[48:51],v[56:59],v[16:19]
s_mov_b32 s62,s87
ds_read_b128 v[52:55],v2 offset:13312
v_fma_f32 v68,v68,v91,v90
v_mfma_f32_16x16x32_bf16 v[20:23],a[52:55],v[56:59],v[20:23]
v_fma_f32 v69,v69,v91,v90
v_fma_f32 v70,v70,v91,v90
v_mfma_f32_16x16x32_bf16 v[16:19],a[56:59],v[60:63],v[16:19]
buffer_load_dwordx4 v1,s[36:39],s66 offen offset:3072 sc1 lds
v_mfma_f32_16x16x32_bf16 v[20:23],a[60:63],v[60:63],v[20:23]
s_waitcnt lgkmcnt(2)
ds_read_b128 v[56:59],v2 offset:14336
v_fma_f32 v71,v71,v91,v90
v_mfma_f32_16x16x32_bf16 v[16:19],a[64:67],v[32:35],v[16:19]
s_waitcnt vmcnt(9)
ds_read_b128 v[60:63],v2 offset:15360
v_add_f32 v24,v24,v64
v_mfma_f32_16x16x32_bf16 v[20:23],a[68:71],v[32:35],v[20:23]
s_barrier
v_mfma_f32_16x16x32_bf16 v[16:19],a[72:75],v[36:39],v[16:19]
s_add_u32 m0,s71,s88
ds_read_b128 v[32:35],v2 offset:16384
v_add_f32 v25,v25,v65
v_mfma_f32_16x16x32_bf16 v[20:23],a[76:79],v[36:39],v[20:23]
v_add_f32 v26,v26,v66
v_add_f32 v27,v27,v67
v_mfma_f32_16x16x32_bf16 v[16:19],a[80:83],v[40:43],v[16:19]
buffer_load_dwordx4 v10,s[36:39],s66 offen offset:0 sc1 lds
v_mfma_f32_16x16x32_bf16 v[20:23],a[84:87],v[40:43],v[20:23]
ds_read_b128 v[36:39],v2 offset:17408
v_add_f32 v28,v28,v68
v_mfma_f32_16x16x32_bf16 v[16:19],a[88:91],v[44:47],v[16:19]
buffer_load_dword v11,s[36:39],s72 offen offset:1024 sc1 lds
v_mfma_f32_16x16x32_bf16 v[20:23],a[92:95],v[44:47],v[20:23]
s_waitcnt lgkmcnt(2)
ds_read_b128 v[40:43],v2 offset:18432
v_add_f32 v29,v29,v69
v_mfma_f32_16x16x32_bf16 v[16:19],a[96:99],v[48:51],v[16:19]
s_mov_b32 m0,s62
v_add_f32 v30,v30,v70
v_add_f32 v31,v31,v71
v_cvt_pk_bf16_f32 v72,v24,v25
v_cvt_pk_bf16_f32 v73,v26,v27
v_cvt_pk_bf16_f32 v74,v28,v29
v_cvt_pk_bf16_f32 v75,v30,v31
v_mfma_f32_16x16x32_bf16 v[20:23],a[100:103],v[48:51],v[20:23]
ds_read_b128 v[44:47],v2 offset:19456
buffer_store_dwordx4 v[72:75],v3,s[36:39],s73 offen offset:0 sc0 sc1
v_add_f32 v64,v24,v25
v_mul_f32 v65,v24,v24
v_fma_f32 v65,v25,v25,v65
v_mfma_f32_16x16x32_bf16 v[16:19],a[104:107],v[52:55],v[16:19]
ds_read_b128 v[48:51],v2 offset:20480
v_add_f32 v64,v64,v26
v_mfma_f32_16x16x32_bf16 v[20:23],a[108:111],v[52:55],v[20:23]
v_fma_f32 v65,v26,v26,v65
v_add_f32 v64,v64,v27
v_mfma_f32_16x16x32_bf16 v[16:19],a[112:115],v[56:59],v[16:19]
ds_read_b128 v[52:55],v2 offset:21504
v_fma_f32 v65,v27,v27,v65
v_mfma_f32_16x16x32_bf16 v[20:23],a[116:119],v[56:59],v[20:23]
v_add_f32 v64,v64,v28
v_fma_f32 v65,v28,v28,v65
v_add_f32 v64,v64,v29
v_fma_f32 v65,v29,v29,v65
v_add_f32 v64,v64,v30
v_fma_f32 v65,v30,v30,v65
v_add_f32 v64,v64,v31
v_fma_f32 v65,v31,v31,v65
v_mfma_f32_16x16x32_bf16 v[16:19],a[120:123],v[60:63],v[16:19]
s_nop 0
v_permlane16_swap_b32 v64,v65
v_add_f32 v64,v64,v65
v_mov_b32 v65,v64
v_mfma_f32_16x16x32_bf16 v[20:23],a[124:127],v[60:63],v[20:23]
s_waitcnt lgkmcnt(2)
ds_read_b128 v[56:59],v2 offset:22528
v_permlane32_swap_b32 v64,v65
v_add_f32 v64,v64,v65
v_mfma_f32_16x16x32_bf16 v[16:19],a[128:131],v[32:35],v[16:19]
ds_write_b32 v13,v64 offset:0
v_mfma_f32_16x16x32_bf16 v[20:23],a[132:135],v[32:35],v[20:23]
ds_read_b128 v[60:63],v2 offset:23552
v_mfma_f32_16x16x32_bf16 v[16:19],a[136:139],v[36:39],v[16:19]
ds_read_b128 v[32:35],v2 offset:24576
v_mfma_f32_16x16x32_bf16 v[20:23],a[140:143],v[36:39],v[20:23]
v_mfma_f32_16x16x32_bf16 v[16:19],a[144:147],v[40:43],v[16:19]
s_waitcnt vmcnt(11) lgkmcnt(2)
ds_read_b128 v[36:39],v2 offset:25600
v_mfma_f32_16x16x32_bf16 v[20:23],a[148:151],v[40:43],v[20:23]
s_barrier
v_mfma_f32_16x16x32_bf16 v[16:19],a[152:155],v[44:47],v[16:19]
ds_read_b128 v[40:43],v2 offset:26624
ds_read_b32 v15,v7 offset:0
v_mfma_f32_16x16x32_bf16 v[20:23],a[156:159],v[44:47],v[20:23]
s_waitcnt lgkmcnt(4)
v_mfma_f32_16x16x32_bf16 v[16:19],a[160:163],v[48:51],v[16:19]
ds_read_b128 v[44:47],v2 offset:27648
v_mfma_f32_16x16x32_bf16 v[20:23],a[164:167],v[48:51],v[20:23]
v_mfma_f32_16x16x32_bf16 v[16:19],a[168:171],v[52:55],v[16:19]
s_waitcnt lgkmcnt(1)
ds_read_b128 v[48:51],v2 offset:28672
v_mfma_f32_16x16x32_bf16 v[20:23],a[172:175],v[52:55],v[20:23]
v_cmp_eq_u32 vcc,s60,v15
v_mfma_f32_16x16x32_bf16 v[16:19],a[176:179],v[56:59],v[16:19]
s_cmp_eq_u32 vcc_lo,-1
ds_read_b128 v[52:55],v2 offset:29696
s_cselect_b32 s67,s86,s59
v_mfma_f32_16x16x32_bf16 v[20:23],a[180:183],v[56:59],v[20:23]
buffer_load_dwordx4 v0,s[36:39],s67 offen offset:0 sc1 lds
v_mfma_f32_16x16x32_bf16 v[16:19],a[184:187],v[60:63],v[16:19]
s_xor_b32 s63,s63,65536
ds_read_b128 v[56:59],v2 offset:30720
s_add_u32 s53,s52,s55
v_mfma_f32_16x16x32_bf16 v[20:23],a[188:191],v[60:63],v[20:23]
s_waitcnt lgkmcnt(3)
s_cmp_eq_u32 s53,s56
s_cselect_b32 s53,s57,s53)"
R"(
v_mfma_f32_16x16x32_bf16 v[16:19],a[192:195],v[32:35],v[16:19]
s_waitcnt vmcnt(8)
ds_read_b128 v[60:63],v2 offset:31744
s_cmp_eq_u32 s53,s58
v_mfma_f32_16x16x32_bf16 v[20:23],a[196:199],v[32:35],v[20:23]
s_barrier
v_mfma_f32_16x16x32_bf16 v[16:19],a[200:203],v[36:39],v[16:19]
s_cselect_b32 s53,s59,s53
ds_read_b128 v[32:35],v2 offset:32768
ds_read_b128 v[80:83],v8 offset:0
v_mfma_f32_16x16x32_bf16 v[20:23],a[204:207],v[36:39],v[20:23]
buffer_load_dwordx4 v0,s[36:39],s67 offen offset:1024 sc1 lds
v_mfma_f32_16x16x32_bf16 v[16:19],a[208:211],v[40:43],v[16:19]
s_cmp_ge_u32 s66,s59
ds_read_b128 v[36:39],v2 offset:33792
ds_read_b64 v[88:89],v9 offset:1024
v_mfma_f32_16x16x32_bf16 v[20:23],a[212:215],v[40:43],v[20:23]
v_mfma_f32_16x16x32_bf16 v[16:19],a[216:219],v[44:47],v[16:19]
ds_read_b128 v[40:43],v2 offset:34816
v_mfma_f32_16x16x32_bf16 v[20:23],a[220:223],v[44:47],v[20:23]
s_waitcnt lgkmcnt(5)
buffer_load_dwordx4 v0,s[36:39],s67 offen offset:2048 sc1 lds
v_mfma_f32_16x16x32_bf16 v[16:19],a[224:227],v[48:51],v[16:19]
ds_read_b128 v[44:47],v2 offset:35840
v_mfma_f32_16x16x32_bf16 v[20:23],a[228:231],v[48:51],v[20:23]
v_mfma_f32_16x16x32_bf16 v[16:19],a[232:235],v[52:55],v[16:19]
s_waitcnt lgkmcnt(2)
ds_read_b128 v[48:51],v2 offset:36864
v_mfma_f32_16x16x32_bf16 v[20:23],a[236:239],v[52:55],v[20:23]
buffer_load_dwordx4 v0,s[36:39],s67 offen offset:3072 sc1 lds
v_mfma_f32_16x16x32_bf16 v[16:19],a[240:243],v[56:59],v[16:19]
ds_read_b128 v[52:55],v2 offset:37888
v_mfma_f32_16x16x32_bf16 v[20:23],a[244:247],v[56:59],v[20:23]
v_mfma_f32_16x16x32_bf16 v[16:19],a[248:251],v[60:63],v[16:19]
s_waitcnt lgkmcnt(2)
ds_read_b128 v[56:59],v2 offset:38912
v_mfma_f32_16x16x32_bf16 v[20:23],a[252:255],v[60:63],v[20:23]
s_cbranch_scc1 Lg2_slow1_%=
Lg2_d1w1_%=:
v_mfma_f32_16x16x32_bf16 v[24:27],a[0:3],v[32:35],0
s_add_u32 m0,s62,16384
ds_read_b128 v[60:63],v2 offset:39936
v_lshlrev_b32 v64,16,v80
v_mfma_f32_16x16x32_bf16 v[28:31],a[4:7],v[32:35],0
s_lshr_b32 s73,s48,0
v_and_b32 v65,0xffff0000,v80
v_lshlrev_b32 v66,16,v81
v_mfma_f32_16x16x32_bf16 v[24:27],a[8:11],v[36:39],v[24:27]
buffer_load_dwordx4 v1,s[36:39],s67 offen offset:0 sc1 lds
v_mfma_f32_16x16x32_bf16 v[28:31],a[12:15],v[36:39],v[28:31]
s_xor_b32 s87,s63,65536
ds_read_b128 v[32:35],v2 offset:40960
v_and_b32 v67,0xffff0000,v81
v_mfma_f32_16x16x32_bf16 v[24:27],a[16:19],v[40:43],v[24:27]
s_cmp_ge_u32 s67,s59
ds_read_b128 v[36:39],v2 offset:41984
v_lshlrev_b32 v68,16,v82
v_mfma_f32_16x16x32_bf16 v[28:31],a[20:23],v[40:43],v[28:31]
s_cselect_b32 s86,s50,s51
v_and_b32 v69,0xffff0000,v82
v_lshlrev_b32 v70,16,v83
v_mfma_f32_16x16x32_bf16 v[24:27],a[24:27],v[44:47],v[24:27]
buffer_load_dwordx4 v1,s[36:39],s67 offen offset:1024 sc1 lds
v_mfma_f32_16x16x32_bf16 v[28:31],a[28:31],v[44:47],v[28:31]
s_waitcnt lgkmcnt(2)
ds_read_b128 v[40:43],v2 offset:43008
v_and_b32 v71,0xffff0000,v83
v_mfma_f32_16x16x32_bf16 v[24:27],a[32:35],v[48:51],v[24:27]
s_cselect_b32 s87,s87,s64
ds_read_b128 v[44:47],v2 offset:44032
v_fma_f32 v64,v64,v89,v88
v_mfma_f32_16x16x32_bf16 v[28:31],a[36:39],v[48:51],v[28:31]
s_lshr_b32 s72,s67,6
v_fma_f32 v65,v65,v89,v88
v_fma_f32 v66,v66,v89,v88
v_mfma_f32_16x16x32_bf16 v[24:27],a[40:43],v[52:55],v[24:27]
buffer_load_dwordx4 v1,s[36:39],s67 offen offset:2048 sc1 lds
v_mfma_f32_16x16x32_bf16 v[28:31],a[44:47],v[52:55],v[28:31]
s_lshr_b32 s71,s62,2
ds_read_b128 v[48:51],v2 offset:45056
v_fma_f32 v67,v67,v89,v88
v_mfma_f32_16x16x32_bf16 v[24:27],a[48:51],v[56:59],v[24:27]
s_mov_b32 s62,s87
ds_read_b128 v[52:55],v2 offset:46080
v_fma_f32 v68,v68,v89,v88
v_mfma_f32_16x16x32_bf16 v[28:31],a[52:55],v[56:59],v[28:31]
s_mov_b64 s[44:45],s[46:47]
v_fma_f32 v69,v69,v89,v88
v_fma_f32 v70,v70,v89,v88
v_mfma_f32_16x16x32_bf16 v[24:27],a[56:59],v[60:63],v[24:27]
buffer_load_dwordx4 v1,s[36:39],s67 offen offset:3072 sc1 lds
v_mfma_f32_16x16x32_bf16 v[28:31],a[60:63],v[60:63],v[28:31]
s_waitcnt lgkmcnt(2)
ds_read_b128 v[56:59],v2 offset:47104
v_fma_f32 v71,v71,v89,v88
v_mfma_f32_16x16x32_bf16 v[24:27],a[64:67],v[32:35],v[24:27]
s_waitcnt vmcnt(9)
ds_read_b128 v[60:63],v2 offset:48128
v_add_f32 v16,v16,v64
v_mfma_f32_16x16x32_bf16 v[28:31],a[68:71],v[32:35],v[28:31]
s_barrier
v_mfma_f32_16x16x32_bf16 v[24:27],a[72:75],v[36:39],v[24:27]
s_add_u32 m0,s71,s88
ds_read_b128 v[32:35],v2 offset:49152
v_add_f32 v17,v17,v65
v_mfma_f32_16x16x32_bf16 v[28:31],a[76:79],v[36:39],v[28:31]
s_mov_b64 s[46:47],s[48:49]
v_add_f32 v18,v18,v66
v_add_f32 v19,v19,v67
v_mfma_f32_16x16x32_bf16 v[24:27],a[80:83],v[40:43],v[24:27]
buffer_load_dwordx4 v10,s[36:39],s67 offen offset:0 sc1 lds
v_mfma_f32_16x16x32_bf16 v[28:31],a[84:87],v[40:43],v[28:31]
s_mov_b64 s[48:49],s[50:51]
ds_read_b128 v[36:39],v2 offset:50176
v_add_f32 v20,v20,v68
v_mfma_f32_16x16x32_bf16 v[24:27],a[88:91],v[44:47],v[24:27]
buffer_load_dword v11,s[36:39],s72 offen offset:1024 sc1 lds
v_mfma_f32_16x16x32_bf16 v[28:31],a[92:95],v[44:47],v[28:31]
s_waitcnt lgkmcnt(2)
ds_read_b128 v[40:43],v2 offset:51200
v_add_f32 v21,v21,v69
v_mfma_f32_16x16x32_bf16 v[24:27],a[96:99],v[48:51],v[24:27]
s_mov_b32 m0,s62
v_add_f32 v22,v22,v70
v_add_f32 v23,v23,v71
v_cvt_pk_bf16_f32 v72,v16,v17
v_cvt_pk_bf16_f32 v73,v18,v19
v_cvt_pk_bf16_f32 v74,v20,v21
v_cvt_pk_bf16_f32 v75,v22,v23
v_mfma_f32_16x16x32_bf16 v[28:31],a[100:103],v[48:51],v[28:31]
s_mov_b64 s[50:51],s[52:53]
ds_read_b128 v[44:47],v2 offset:52224
buffer_store_dwordx4 v[72:75],v3,s[36:39],s73 offen offset:0 sc0 sc1
v_add_f32 v64,v16,v17
v_mul_f32 v65,v16,v16
v_fma_f32 v65,v17,v17,v65
v_mfma_f32_16x16x32_bf16 v[24:27],a[104:107],v[52:55],v[24:27]
ds_read_b128 v[48:51],v2 offset:53248
v_add_f32 v64,v64,v18
v_mfma_f32_16x16x32_bf16 v[28:31],a[108:111],v[52:55],v[28:31]
v_fma_f32 v65,v18,v18,v65
v_add_f32 v64,v64,v19
v_mfma_f32_16x16x32_bf16 v[24:27],a[112:115],v[56:59],v[24:27]
ds_read_b128 v[52:55],v2 offset:54272
v_fma_f32 v65,v19,v19,v65
v_mfma_f32_16x16x32_bf16 v[28:31],a[116:119],v[56:59],v[28:31]
v_add_f32 v64,v64,v20
v_fma_f32 v65,v20,v20,v65
v_add_f32 v64,v64,v21
v_fma_f32 v65,v21,v21,v65
v_add_f32 v64,v64,v22
v_fma_f32 v65,v22,v22,v65
v_add_f32 v64,v64,v23
v_fma_f32 v65,v23,v23,v65
v_mfma_f32_16x16x32_bf16 v[24:27],a[120:123],v[60:63],v[24:27]
s_nop 0
v_permlane16_swap_b32 v64,v65
v_add_f32 v64,v64,v65
v_mov_b32 v65,v64
v_mfma_f32_16x16x32_bf16 v[28:31],a[124:127],v[60:63],v[28:31]
s_waitcnt lgkmcnt(2)
ds_read_b128 v[56:59],v2 offset:55296
v_permlane32_swap_b32 v64,v65
v_add_f32 v64,v64,v65
v_mfma_f32_16x16x32_bf16 v[24:27],a[128:131],v[32:35],v[24:27]
ds_write_b32 v13,v64 offset:2048
v_mfma_f32_16x16x32_bf16 v[28:31],a[132:135],v[32:35],v[28:31]
ds_read_b128 v[60:63],v2 offset:56320
v_mfma_f32_16x16x32_bf16 v[24:27],a[136:139],v[36:39],v[24:27]
ds_read_b128 v[32:35],v2 offset:57344
v_mfma_f32_16x16x32_bf16 v[28:31],a[140:143],v[36:39],v[28:31]
v_mfma_f32_16x16x32_bf16 v[24:27],a[144:147],v[40:43],v[24:27]
s_waitcnt vmcnt(11) lgkmcnt(2)
ds_read_b128 v[36:39],v2 offset:58368
v_mfma_f32_16x16x32_bf16 v[28:31],a[148:151],v[40:43],v[28:31]
s_barrier
v_mfma_f32_16x16x32_bf16 v[24:27],a[152:155],v[44:47],v[24:27]
ds_read_b128 v[40:43],v2 offset:59392
ds_read_b32 v15,v7 offset:256
v_mfma_f32_16x16x32_bf16 v[28:31],a[156:159],v[44:47],v[28:31]
s_waitcnt lgkmcnt(4)
v_mfma_f32_16x16x32_bf16 v[24:27],a[160:163],v[48:51],v[24:27]
ds_read_b128 v[44:47],v2 offset:60416
v_mfma_f32_16x16x32_bf16 v[28:31],a[164:167],v[48:51],v[28:31]
v_mfma_f32_16x16x32_bf16 v[24:27],a[168:171],v[52:55],v[24:27]
s_waitcnt lgkmcnt(1)
ds_read_b128 v[48:51],v2 offset:61440
v_mfma_f32_16x16x32_bf16 v[28:31],a[172:175],v[52:55],v[28:31]
v_cmp_eq_u32 vcc,s60,v15
v_mfma_f32_16x16x32_bf16 v[24:27],a[176:179],v[56:59],v[24:27]
s_cmp_eq_u32 vcc_lo,-1
ds_read_b128 v[52:55],v2 offset:62464
s_cselect_b32 s66,s86,s59
v_mfma_f32_16x16x32_bf16 v[28:31],a[180:183],v[56:59],v[28:31]
buffer_load_dwordx4 v0,s[36:39],s66 offen offset:0 sc1 lds
v_mfma_f32_16x16x32_bf16 v[24:27],a[184:187],v[60:63],v[24:27]
s_xor_b32 s64,s64,65536
ds_read_b128 v[56:59],v2 offset:63488
s_add_u32 s74,s53,s55
v_mfma_f32_16x16x32_bf16 v[28:31],a[188:191],v[60:63],v[28:31])"
R"(
s_waitcnt lgkmcnt(3)
s_cmp_eq_u32 s74,s56
s_cselect_b32 s74,s57,s74
v_mfma_f32_16x16x32_bf16 v[24:27],a[192:195],v[32:35],v[24:27]
s_waitcnt vmcnt(8)
ds_read_b128 v[60:63],v2 offset:64512
v_xor_b32 v2,65536,v2
v_mfma_f32_16x16x32_bf16 v[28:31],a[196:199],v[32:35],v[28:31]
s_barrier
v_mfma_f32_16x16x32_bf16 v[24:27],a[200:203],v[36:39],v[24:27]
s_cmp_eq_u32 s74,s58
ds_read_b128 v[32:35],v2 offset:0
ds_read_b128 v[84:87],v8 offset:8192
v_mfma_f32_16x16x32_bf16 v[28:31],a[204:207],v[36:39],v[28:31]
buffer_load_dwordx4 v0,s[36:39],s66 offen offset:1024 sc1 lds
v_mfma_f32_16x16x32_bf16 v[24:27],a[208:211],v[40:43],v[24:27]
s_cselect_b32 s74,s59,s74
ds_read_b128 v[36:39],v2 offset:1024
ds_read_b64 v[90:91],v9 offset:9216
v_mfma_f32_16x16x32_bf16 v[28:31],a[212:215],v[40:43],v[28:31]
s_mov_b32 s52,s74
v_xor_b32 v8,16384,v8
v_xor_b32 v9,16384,v9
v_mfma_f32_16x16x32_bf16 v[24:27],a[216:219],v[44:47],v[24:27]
s_cmp_lt_u32 s67,s59
ds_read_b128 v[40:43],v2 offset:2048
v_mfma_f32_16x16x32_bf16 v[28:31],a[220:223],v[44:47],v[28:31]
s_waitcnt lgkmcnt(5)
buffer_load_dwordx4 v0,s[36:39],s66 offen offset:2048 sc1 lds
v_mfma_f32_16x16x32_bf16 v[24:27],a[224:227],v[48:51],v[24:27]
ds_read_b128 v[44:47],v2 offset:3072
v_mfma_f32_16x16x32_bf16 v[28:31],a[228:231],v[48:51],v[28:31]
v_mfma_f32_16x16x32_bf16 v[24:27],a[232:235],v[52:55],v[24:27]
s_waitcnt lgkmcnt(2)
ds_read_b128 v[48:51],v2 offset:4096
v_mfma_f32_16x16x32_bf16 v[28:31],a[236:239],v[52:55],v[28:31]
buffer_load_dwordx4 v0,s[36:39],s66 offen offset:3072 sc1 lds
v_mfma_f32_16x16x32_bf16 v[24:27],a[240:243],v[56:59],v[24:27]
ds_read_b128 v[52:55],v2 offset:5120
v_mfma_f32_16x16x32_bf16 v[28:31],a[244:247],v[56:59],v[28:31]
v_mfma_f32_16x16x32_bf16 v[24:27],a[248:251],v[60:63],v[24:27]
s_waitcnt lgkmcnt(2)
ds_read_b128 v[56:59],v2 offset:6144
v_mfma_f32_16x16x32_bf16 v[28:31],a[252:255],v[60:63],v[28:31]
s_cbranch_scc1 Lg2_d0w1_%=
s_branch Lg2_slow0_%=
Lg2_d0w2_%=:
v_mfma_f32_16x16x32_bf16 v[16:19],a[0:3],v[32:35],0
s_add_u32 m0,s62,16384
ds_read_b128 v[60:63],v2 offset:7168
v_lshlrev_b32 v64,16,v84
v_mfma_f32_16x16x32_bf16 v[20:23],a[4:7],v[32:35],0
s_lshr_b32 s73,s47,0
v_and_b32 v65,0xffff0000,v84
v_lshlrev_b32 v66,16,v85
v_mfma_f32_16x16x32_bf16 v[16:19],a[8:11],v[36:39],v[16:19]
s_lshr_b32 s85,s47,5
ds_read_b128 v[32:35],v2 offset:8192
v_and_b32 v67,0xffff0000,v85
v_mfma_f32_16x16x32_bf16 v[20:23],a[12:15],v[36:39],v[20:23]
buffer_load_dwordx4 v1,s[36:39],s66 offen offset:0 sc1 lds
v_mfma_f32_16x16x32_bf16 v[16:19],a[16:19],v[40:43],v[16:19]
s_xor_b32 s87,s64,65536
ds_read_b128 v[36:39],v2 offset:9216
v_lshlrev_b32 v68,16,v86
v_mfma_f32_16x16x32_bf16 v[20:23],a[20:23],v[40:43],v[20:23]
s_cmp_ge_u32 s66,s59
v_and_b32 v69,0xffff0000,v86
v_lshlrev_b32 v70,16,v87
v_mfma_f32_16x16x32_bf16 v[16:19],a[24:27],v[44:47],v[16:19]
s_cselect_b32 s86,s49,s50
ds_read_b128 v[40:43],v2 offset:10240
v_and_b32 v71,0xffff0000,v87
v_mfma_f32_16x16x32_bf16 v[20:23],a[28:31],v[44:47],v[20:23]
s_waitcnt lgkmcnt(3)
buffer_load_dwordx4 v1,s[36:39],s66 offen offset:1024 sc1 lds
v_mfma_f32_16x16x32_bf16 v[16:19],a[32:35],v[48:51],v[16:19]
s_cselect_b32 s87,s87,s63
ds_read_b128 v[44:47],v2 offset:11264
v_fma_f32 v64,v64,v91,v90
v_mfma_f32_16x16x32_bf16 v[20:23],a[36:39],v[48:51],v[20:23]
s_lshr_b32 s72,s66,6
v_fma_f32 v65,v65,v91,v90
v_fma_f32 v66,v66,v91,v90
v_mfma_f32_16x16x32_bf16 v[16:19],a[40:43],v[52:55],v[16:19]
s_lshr_b32 s71,s62,2
ds_read_b128 v[48:51],v2 offset:12288
v_fma_f32 v67,v67,v91,v90
v_mfma_f32_16x16x32_bf16 v[20:23],a[44:47],v[52:55],v[20:23]
buffer_load_dwordx4 v1,s[36:39],s66 offen offset:2048 sc1 lds
v_mfma_f32_16x16x32_bf16 v[16:19],a[48:51],v[56:59],v[16:19]
s_mov_b32 s62,s87
ds_read_b128 v[52:55],v2 offset:13312
v_fma_f32 v68,v68,v91,v90
v_mfma_f32_16x16x32_bf16 v[20:23],a[52:55],v[56:59],v[20:23]
v_fma_f32 v69,v69,v91,v90
v_fma_f32 v70,v70,v91,v90
v_mfma_f32_16x16x32_bf16 v[16:19],a[56:59],v[60:63],v[16:19]
ds_read_b128 v[56:59],v2 offset:14336
v_fma_f32 v71,v71,v91,v90
v_mfma_f32_16x16x32_bf16 v[20:23],a[60:63],v[60:63],v[20:23]
s_waitcnt lgkmcnt(3)
buffer_load_dwordx4 v1,s[36:39],s66 offen offset:3072 sc1 lds
v_mfma_f32_16x16x32_bf16 v[16:19],a[64:67],v[32:35],v[16:19]
s_waitcnt vmcnt(10)
ds_read_b128 v[60:63],v2 offset:15360
v_add_f32 v24,v24,v64
v_mfma_f32_16x16x32_bf16 v[20:23],a[68:71],v[32:35],v[20:23]
s_barrier
v_mfma_f32_16x16x32_bf16 v[16:19],a[72:75],v[36:39],v[16:19]
s_add_u32 m0,s71,s88
ds_read_b128 v[32:35],v2 offset:16384
v_add_f32 v25,v25,v65
v_mfma_f32_16x16x32_bf16 v[20:23],a[76:79],v[36:39],v[20:23]
v_add_f32 v26,v26,v66
v_add_f32 v27,v27,v67
v_mfma_f32_16x16x32_bf16 v[16:19],a[80:83],v[40:43],v[16:19]
ds_read_b128 v[36:39],v2 offset:17408
v_add_f32 v28,v28,v68
v_mfma_f32_16x16x32_bf16 v[20:23],a[84:87],v[40:43],v[20:23]
buffer_load_dwordx4 v10,s[36:39],s66 offen offset:0 sc1 lds
v_mfma_f32_16x16x32_bf16 v[16:19],a[88:91],v[44:47],v[16:19]
buffer_load_dword v11,s[36:39],s72 offen offset:1024 sc1 lds
v_mfma_f32_16x16x32_bf16 v[20:23],a[92:95],v[44:47],v[20:23]
s_waitcnt lgkmcnt(2)
ds_read_b128 v[40:43],v2 offset:18432
v_add_f32 v29,v29,v69
v_mfma_f32_16x16x32_bf16 v[16:19],a[96:99],v[48:51],v[16:19]
s_mov_b32 m0,s62
ds_read_b128 v[44:47],v2 offset:19456
v_add_f32 v30,v30,v70
v_mfma_f32_16x16x32_bf16 v[20:23],a[100:103],v[48:51],v[20:23]
v_add_f32 v31,v31,v71
v_cvt_pk_bf16_f32 v72,v24,v25
v_cvt_pk_bf16_f32 v73,v26,v27
v_cvt_pk_bf16_f32 v74,v28,v29
v_cvt_pk_bf16_f32 v75,v30,v31
v_mfma_f32_16x16x32_bf16 v[16:19],a[104:107],v[52:55],v[16:19]
buffer_store_dwordx4 v[72:75],v3,s[36:39],s73 offen offset:0 sc0 sc1
ds_read_b128 v[48:51],v2 offset:20480
v_add_f32 v64,v24,v25
v_mul_f32 v65,v24,v24
v_mfma_f32_16x16x32_bf16 v[20:23],a[108:111],v[52:55],v[20:23]
v_fma_f32 v65,v25,v25,v65
v_add_f32 v64,v64,v26
v_mfma_f32_16x16x32_bf16 v[16:19],a[112:115],v[56:59],v[16:19]
ds_read_b128 v[52:55],v2 offset:21504
v_fma_f32 v65,v26,v26,v65
v_mfma_f32_16x16x32_bf16 v[20:23],a[116:119],v[56:59],v[20:23]
v_add_f32 v64,v64,v27
v_fma_f32 v65,v27,v27,v65
v_add_f32 v64,v64,v28
v_fma_f32 v65,v28,v28,v65
v_add_f32 v64,v64,v29
v_fma_f32 v65,v29,v29,v65
v_add_f32 v64,v64,v30
v_fma_f32 v65,v30,v30,v65
v_add_f32 v64,v64,v31
v_fma_f32 v65,v31,v31,v65
v_mfma_f32_16x16x32_bf16 v[16:19],a[120:123],v[60:63],v[16:19]
s_nop 0
v_permlane16_swap_b32 v64,v65
v_add_f32 v64,v64,v65
v_mov_b32 v65,v64
v_mfma_f32_16x16x32_bf16 v[20:23],a[124:127],v[60:63],v[20:23]
s_waitcnt lgkmcnt(2)
ds_read_b128 v[56:59],v2 offset:22528
v_permlane32_swap_b32 v64,v65
v_add_f32 v64,v64,v65
v_mfma_f32_16x16x32_bf16 v[16:19],a[128:131],v[32:35],v[16:19]
ds_write_b32 v13,v64 offset:0
v_mfma_f32_16x16x32_bf16 v[20:23],a[132:135],v[32:35],v[20:23]
ds_read_b128 v[60:63],v2 offset:23552
v_mfma_f32_16x16x32_bf16 v[16:19],a[136:139],v[36:39],v[16:19]
ds_read_b128 v[32:35],v2 offset:24576
v_mfma_f32_16x16x32_bf16 v[20:23],a[140:143],v[36:39],v[20:23]
v_mfma_f32_16x16x32_bf16 v[16:19],a[144:147],v[40:43],v[16:19]
s_waitcnt vmcnt(11) lgkmcnt(2)
ds_read_b128 v[36:39],v2 offset:25600
v_mfma_f32_16x16x32_bf16 v[20:23],a[148:151],v[40:43],v[20:23]
s_barrier
v_mfma_f32_16x16x32_bf16 v[16:19],a[152:155],v[44:47],v[16:19]
ds_read_b128 v[40:43],v2 offset:26624
ds_read_b32 v15,v7 offset:0
v_mfma_f32_16x16x32_bf16 v[20:23],a[156:159],v[44:47],v[20:23]
s_waitcnt lgkmcnt(4)
ds_read_b128 v[76:79],v14 offset:0
v_mfma_f32_16x16x32_bf16 v[16:19],a[160:163],v[48:51],v[16:19]
ds_read_b128 v[44:47],v2 offset:27648
v_mfma_f32_16x16x32_bf16 v[20:23],a[164:167],v[48:51],v[20:23]
v_mfma_f32_16x16x32_bf16 v[16:19],a[168:171],v[52:55],v[16:19]
s_waitcnt lgkmcnt(2)
ds_read_b128 v[48:51],v2 offset:28672
v_mfma_f32_16x16x32_bf16 v[20:23],a[172:175],v[52:55],v[20:23]
s_waitcnt lgkmcnt(2)
v_cmp_eq_u32 vcc,s60,v15
v_add_f32 v76,v76,v77
v_add_f32 v78,v78,v79
v_add_f32 v76,v76,v78
v_mfma_f32_16x16x32_bf16 v[16:19],a[176:179],v[56:59],v[16:19])"
R"(
buffer_store_dword v76,v12,s[36:39],s85 offen offset:0 sc0 sc1
ds_read_b128 v[52:55],v2 offset:29696
v_mfma_f32_16x16x32_bf16 v[20:23],a[180:183],v[56:59],v[20:23]
s_cmp_eq_u32 vcc_lo,-1
s_cselect_b32 s67,s86,s59
s_xor_b32 s63,s63,65536
v_mfma_f32_16x16x32_bf16 v[16:19],a[184:187],v[60:63],v[16:19]
buffer_load_dwordx4 v0,s[36:39],s67 offen offset:0 sc1 lds
v_mfma_f32_16x16x32_bf16 v[20:23],a[188:191],v[60:63],v[20:23]
s_waitcnt lgkmcnt(2)
ds_read_b128 v[56:59],v2 offset:30720
s_add_u32 s53,s52,s55
v_mfma_f32_16x16x32_bf16 v[16:19],a[192:195],v[32:35],v[16:19]
s_waitcnt vmcnt(9)
ds_read_b128 v[60:63],v2 offset:31744
s_cmp_eq_u32 s53,s56
v_mfma_f32_16x16x32_bf16 v[20:23],a[196:199],v[32:35],v[20:23]
s_barrier
v_mfma_f32_16x16x32_bf16 v[16:19],a[200:203],v[36:39],v[16:19]
s_cselect_b32 s53,s57,s53
ds_read_b128 v[32:35],v2 offset:32768
ds_read_b128 v[80:83],v8 offset:0
v_mfma_f32_16x16x32_bf16 v[20:23],a[204:207],v[36:39],v[20:23]
s_cmp_eq_u32 s53,s58
ds_read_b64 v[88:89],v9 offset:1024
s_cselect_b32 s53,s59,s53
v_mfma_f32_16x16x32_bf16 v[16:19],a[208:211],v[40:43],v[16:19]
buffer_load_dwordx4 v0,s[36:39],s67 offen offset:1024 sc1 lds
v_mfma_f32_16x16x32_bf16 v[20:23],a[212:215],v[40:43],v[20:23]
s_cmp_ge_u32 s66,s59
ds_read_b128 v[36:39],v2 offset:33792
v_mfma_f32_16x16x32_bf16 v[16:19],a[216:219],v[44:47],v[16:19]
ds_read_b128 v[40:43],v2 offset:34816
v_mfma_f32_16x16x32_bf16 v[20:23],a[220:223],v[44:47],v[20:23]
s_waitcnt lgkmcnt(5)
v_mfma_f32_16x16x32_bf16 v[16:19],a[224:227],v[48:51],v[16:19]
buffer_load_dwordx4 v0,s[36:39],s67 offen offset:2048 sc1 lds
v_mfma_f32_16x16x32_bf16 v[20:23],a[228:231],v[48:51],v[20:23]
s_waitcnt lgkmcnt(2)
ds_read_b128 v[44:47],v2 offset:35840
v_mfma_f32_16x16x32_bf16 v[16:19],a[232:235],v[52:55],v[16:19]
ds_read_b128 v[48:51],v2 offset:36864
v_mfma_f32_16x16x32_bf16 v[20:23],a[236:239],v[52:55],v[20:23]
v_mfma_f32_16x16x32_bf16 v[16:19],a[240:243],v[56:59],v[16:19]
buffer_load_dwordx4 v0,s[36:39],s67 offen offset:3072 sc1 lds
v_mfma_f32_16x16x32_bf16 v[20:23],a[244:247],v[56:59],v[20:23]
ds_read_b128 v[52:55],v2 offset:37888
v_mfma_f32_16x16x32_bf16 v[16:19],a[248:251],v[60:63],v[16:19]
s_waitcnt lgkmcnt(2)
ds_read_b128 v[56:59],v2 offset:38912
v_mfma_f32_16x16x32_bf16 v[20:23],a[252:255],v[60:63],v[20:23]
s_cbranch_scc1 Lg2_slow1_%=
Lg2_d1w2_%=:
v_mfma_f32_16x16x32_bf16 v[24:27],a[0:3],v[32:35],0
s_add_u32 m0,s62,16384
ds_read_b128 v[60:63],v2 offset:39936
v_lshlrev_b32 v64,16,v80
v_mfma_f32_16x16x32_bf16 v[28:31],a[4:7],v[32:35],0
s_lshr_b32 s73,s48,0
v_and_b32 v65,0xffff0000,v80
v_lshlrev_b32 v66,16,v81
v_mfma_f32_16x16x32_bf16 v[24:27],a[8:11],v[36:39],v[24:27]
s_lshr_b32 s85,s48,5
ds_read_b128 v[32:35],v2 offset:40960
v_and_b32 v67,0xffff0000,v81
v_mfma_f32_16x16x32_bf16 v[28:31],a[12:15],v[36:39],v[28:31]
buffer_load_dwordx4 v1,s[36:39],s67 offen offset:0 sc1 lds
v_mfma_f32_16x16x32_bf16 v[24:27],a[16:19],v[40:43],v[24:27]
s_xor_b32 s87,s63,65536
ds_read_b128 v[36:39],v2 offset:41984
v_lshlrev_b32 v68,16,v82
v_mfma_f32_16x16x32_bf16 v[28:31],a[20:23],v[40:43],v[28:31]
s_cmp_ge_u32 s67,s59
v_and_b32 v69,0xffff0000,v82
v_lshlrev_b32 v70,16,v83
v_mfma_f32_16x16x32_bf16 v[24:27],a[24:27],v[44:47],v[24:27]
s_cselect_b32 s86,s50,s51
ds_read_b128 v[40:43],v2 offset:43008
v_and_b32 v71,0xffff0000,v83
v_mfma_f32_16x16x32_bf16 v[28:31],a[28:31],v[44:47],v[28:31]
s_waitcnt lgkmcnt(3)
buffer_load_dwordx4 v1,s[36:39],s67 offen offset:1024 sc1 lds
v_mfma_f32_16x16x32_bf16 v[24:27],a[32:35],v[48:51],v[24:27]
s_cselect_b32 s87,s87,s64
ds_read_b128 v[44:47],v2 offset:44032
v_fma_f32 v64,v64,v89,v88
v_mfma_f32_16x16x32_bf16 v[28:31],a[36:39],v[48:51],v[28:31]
s_lshr_b32 s72,s67,6
v_fma_f32 v65,v65,v89,v88
v_fma_f32 v66,v66,v89,v88
v_mfma_f32_16x16x32_bf16 v[24:27],a[40:43],v[52:55],v[24:27]
s_lshr_b32 s71,s62,2
ds_read_b128 v[48:51],v2 offset:45056
v_fma_f32 v67,v67,v89,v88
v_mfma_f32_16x16x32_bf16 v[28:31],a[44:47],v[52:55],v[28:31]
buffer_load_dwordx4 v1,s[36:39],s67 offen offset:2048 sc1 lds
v_mfma_f32_16x16x32_bf16 v[24:27],a[48:51],v[56:59],v[24:27]
s_mov_b32 s62,s87
ds_read_b128 v[52:55],v2 offset:46080
v_fma_f32 v68,v68,v89,v88
v_mfma_f32_16x16x32_bf16 v[28:31],a[52:55],v[56:59],v[28:31]
s_mov_b64 s[44:45],s[46:47]
v_fma_f32 v69,v69,v89,v88
v_fma_f32 v70,v70,v89,v88
v_mfma_f32_16x16x32_bf16 v[24:27],a[56:59],v[60:63],v[24:27]
s_mov_b64 s[46:47],s[48:49]
ds_read_b128 v[56:59],v2 offset:47104
v_fma_f32 v71,v71,v89,v88
v_mfma_f32_16x16x32_bf16 v[28:31],a[60:63],v[60:63],v[28:31]
s_waitcnt lgkmcnt(3)
buffer_load_dwordx4 v1,s[36:39],s67 offen offset:3072 sc1 lds
v_mfma_f32_16x16x32_bf16 v[24:27],a[64:67],v[32:35],v[24:27]
s_waitcnt vmcnt(10)
ds_read_b128 v[60:63],v2 offset:48128
v_add_f32 v16,v16,v64
v_mfma_f32_16x16x32_bf16 v[28:31],a[68:71],v[32:35],v[28:31]
s_barrier
v_mfma_f32_16x16x32_bf16 v[24:27],a[72:75],v[36:39],v[24:27]
s_add_u32 m0,s71,s88
ds_read_b128 v[32:35],v2 offset:49152
v_add_f32 v17,v17,v65
v_mfma_f32_16x16x32_bf16 v[28:31],a[76:79],v[36:39],v[28:31]
s_mov_b64 s[48:49],s[50:51]
v_add_f32 v18,v18,v66
v_add_f32 v19,v19,v67
v_mfma_f32_16x16x32_bf16 v[24:27],a[80:83],v[40:43],v[24:27]
s_mov_b64 s[50:51],s[52:53]
ds_read_b128 v[36:39],v2 offset:50176
v_add_f32 v20,v20,v68
v_mfma_f32_16x16x32_bf16 v[28:31],a[84:87],v[40:43],v[28:31]
buffer_load_dwordx4 v10,s[36:39],s67 offen offset:0 sc1 lds
v_mfma_f32_16x16x32_bf16 v[24:27],a[88:91],v[44:47],v[24:27]
buffer_load_dword v11,s[36:39],s72 offen offset:1024 sc1 lds
v_mfma_f32_16x16x32_bf16 v[28:31],a[92:95],v[44:47],v[28:31]
s_waitcnt lgkmcnt(2)
ds_read_b128 v[40:43],v2 offset:51200
v_add_f32 v21,v21,v69
v_mfma_f32_16x16x32_bf16 v[24:27],a[96:99],v[48:51],v[24:27]
s_mov_b32 m0,s62
ds_read_b128 v[44:47],v2 offset:52224
v_add_f32 v22,v22,v70
v_mfma_f32_16x16x32_bf16 v[28:31],a[100:103],v[48:51],v[28:31]
v_add_f32 v23,v23,v71
v_cvt_pk_bf16_f32 v72,v16,v17
v_cvt_pk_bf16_f32 v73,v18,v19
v_cvt_pk_bf16_f32 v74,v20,v21
v_cvt_pk_bf16_f32 v75,v22,v23
v_mfma_f32_16x16x32_bf16 v[24:27],a[104:107],v[52:55],v[24:27]
buffer_store_dwordx4 v[72:75],v3,s[36:39],s73 offen offset:0 sc0 sc1
ds_read_b128 v[48:51],v2 offset:53248
v_add_f32 v64,v16,v17
v_mul_f32 v65,v16,v16
v_mfma_f32_16x16x32_bf16 v[28:31],a[108:111],v[52:55],v[28:31]
v_fma_f32 v65,v17,v17,v65
v_add_f32 v64,v64,v18
v_mfma_f32_16x16x32_bf16 v[24:27],a[112:115],v[56:59],v[24:27]
ds_read_b128 v[52:55],v2 offset:54272
v_fma_f32 v65,v18,v18,v65
v_mfma_f32_16x16x32_bf16 v[28:31],a[116:119],v[56:59],v[28:31]
v_add_f32 v64,v64,v19
v_fma_f32 v65,v19,v19,v65
v_add_f32 v64,v64,v20
v_fma_f32 v65,v20,v20,v65
v_add_f32 v64,v64,v21
v_fma_f32 v65,v21,v21,v65
v_add_f32 v64,v64,v22
v_fma_f32 v65,v22,v22,v65
v_add_f32 v64,v64,v23
v_fma_f32 v65,v23,v23,v65
v_mfma_f32_16x16x32_bf16 v[24:27],a[120:123],v[60:63],v[24:27]
s_nop 0
v_permlane16_swap_b32 v64,v65
v_add_f32 v64,v64,v65
v_mov_b32 v65,v64
v_mfma_f32_16x16x32_bf16 v[28:31],a[124:127],v[60:63],v[28:31]
s_waitcnt lgkmcnt(2)
ds_read_b128 v[56:59],v2 offset:55296
v_permlane32_swap_b32 v64,v65
v_add_f32 v64,v64,v65
v_mfma_f32_16x16x32_bf16 v[24:27],a[128:131],v[32:35],v[24:27]
ds_write_b32 v13,v64 offset:2048
v_mfma_f32_16x16x32_bf16 v[28:31],a[132:135],v[32:35],v[28:31]
ds_read_b128 v[60:63],v2 offset:56320
v_mfma_f32_16x16x32_bf16 v[24:27],a[136:139],v[36:39],v[24:27]
ds_read_b128 v[32:35],v2 offset:57344
v_mfma_f32_16x16x32_bf16 v[28:31],a[140:143],v[36:39],v[28:31]
v_mfma_f32_16x16x32_bf16 v[24:27],a[144:147],v[40:43],v[24:27]
s_waitcnt vmcnt(11) lgkmcnt(2)
ds_read_b128 v[36:39],v2 offset:58368
v_mfma_f32_16x16x32_bf16 v[28:31],a[148:151],v[40:43],v[28:31]
s_barrier
v_mfma_f32_16x16x32_bf16 v[24:27],a[152:155],v[44:47],v[24:27]
ds_read_b128 v[40:43],v2 offset:59392
ds_read_b32 v15,v7 offset:256
v_mfma_f32_16x16x32_bf16 v[28:31],a[156:159],v[44:47],v[28:31]
s_waitcnt lgkmcnt(4)
ds_read_b128 v[76:79],v14 offset:2048
v_mfma_f32_16x16x32_bf16 v[24:27],a[160:163],v[48:51],v[24:27]
ds_read_b128 v[44:47],v2 offset:60416
v_mfma_f32_16x16x32_bf16 v[28:31],a[164:167],v[48:51],v[28:31])"
R"(
v_mfma_f32_16x16x32_bf16 v[24:27],a[168:171],v[52:55],v[24:27]
s_waitcnt lgkmcnt(2)
ds_read_b128 v[48:51],v2 offset:61440
v_mfma_f32_16x16x32_bf16 v[28:31],a[172:175],v[52:55],v[28:31]
s_waitcnt lgkmcnt(2)
v_cmp_eq_u32 vcc,s60,v15
v_add_f32 v76,v76,v77
v_add_f32 v78,v78,v79
v_add_f32 v76,v76,v78
v_mfma_f32_16x16x32_bf16 v[24:27],a[176:179],v[56:59],v[24:27]
buffer_store_dword v76,v12,s[36:39],s85 offen offset:0 sc0 sc1
ds_read_b128 v[52:55],v2 offset:62464
v_mfma_f32_16x16x32_bf16 v[28:31],a[180:183],v[56:59],v[28:31]
s_cmp_eq_u32 vcc_lo,-1
s_cselect_b32 s66,s86,s59
s_xor_b32 s64,s64,65536
v_mfma_f32_16x16x32_bf16 v[24:27],a[184:187],v[60:63],v[24:27]
buffer_load_dwordx4 v0,s[36:39],s66 offen offset:0 sc1 lds
v_mfma_f32_16x16x32_bf16 v[28:31],a[188:191],v[60:63],v[28:31]
s_waitcnt lgkmcnt(2)
ds_read_b128 v[56:59],v2 offset:63488
s_add_u32 s74,s53,s55
v_mfma_f32_16x16x32_bf16 v[24:27],a[192:195],v[32:35],v[24:27]
s_waitcnt vmcnt(9)
ds_read_b128 v[60:63],v2 offset:64512
v_xor_b32 v2,65536,v2
v_mfma_f32_16x16x32_bf16 v[28:31],a[196:199],v[32:35],v[28:31]
s_barrier
v_mfma_f32_16x16x32_bf16 v[24:27],a[200:203],v[36:39],v[24:27]
s_cmp_eq_u32 s74,s56
ds_read_b128 v[32:35],v2 offset:0
ds_read_b128 v[84:87],v8 offset:8192
v_mfma_f32_16x16x32_bf16 v[28:31],a[204:207],v[36:39],v[28:31]
s_cselect_b32 s74,s57,s74
ds_read_b64 v[90:91],v9 offset:9216
v_xor_b32 v8,16384,v8
v_mfma_f32_16x16x32_bf16 v[24:27],a[208:211],v[40:43],v[24:27]
buffer_load_dwordx4 v0,s[36:39],s66 offen offset:1024 sc1 lds
v_mfma_f32_16x16x32_bf16 v[28:31],a[212:215],v[40:43],v[28:31]
s_cmp_eq_u32 s74,s58
ds_read_b128 v[36:39],v2 offset:1024
v_xor_b32 v9,16384,v9
v_mfma_f32_16x16x32_bf16 v[24:27],a[216:219],v[44:47],v[24:27]
s_cselect_b32 s74,s59,s74
ds_read_b128 v[40:43],v2 offset:2048
s_mov_b32 s52,s74
v_mfma_f32_16x16x32_bf16 v[28:31],a[220:223],v[44:47],v[28:31]
s_waitcnt lgkmcnt(5)
s_cmp_lt_u32 s67,s59
v_mfma_f32_16x16x32_bf16 v[24:27],a[224:227],v[48:51],v[24:27]
buffer_load_dwordx4 v0,s[36:39],s66 offen offset:2048 sc1 lds
v_mfma_f32_16x16x32_bf16 v[28:31],a[228:231],v[48:51],v[28:31]
s_waitcnt lgkmcnt(2)
ds_read_b128 v[44:47],v2 offset:3072
v_mfma_f32_16x16x32_bf16 v[24:27],a[232:235],v[52:55],v[24:27]
ds_read_b128 v[48:51],v2 offset:4096
v_mfma_f32_16x16x32_bf16 v[28:31],a[236:239],v[52:55],v[28:31]
v_mfma_f32_16x16x32_bf16 v[24:27],a[240:243],v[56:59],v[24:27]
buffer_load_dwordx4 v0,s[36:39],s66 offen offset:3072 sc1 lds
v_mfma_f32_16x16x32_bf16 v[28:31],a[244:247],v[56:59],v[28:31]
ds_read_b128 v[52:55],v2 offset:5120
v_mfma_f32_16x16x32_bf16 v[24:27],a[248:251],v[60:63],v[24:27]
s_waitcnt lgkmcnt(2)
ds_read_b128 v[56:59],v2 offset:6144
v_mfma_f32_16x16x32_bf16 v[28:31],a[252:255],v[60:63],v[28:31]
s_cbranch_scc1 Lg2_d0w2_%=
s_branch Lg2_slow0_%=
Lg2_d0w3_%=:
v_mfma_f32_16x16x32_bf16 v[16:19],a[0:3],v[32:35],0
s_add_u32 m0,s62,16384
ds_read_b128 v[60:63],v2 offset:7168
v_lshlrev_b32 v64,16,v84
v_mfma_f32_16x16x32_bf16 v[20:23],a[4:7],v[32:35],0
s_lshr_b32 s73,s47,0
v_and_b32 v65,0xffff0000,v84
v_lshlrev_b32 v66,16,v85
v_mfma_f32_16x16x32_bf16 v[16:19],a[8:11],v[36:39],v[16:19]
s_xor_b32 s87,s64,65536
ds_read_b128 v[32:35],v2 offset:8192
v_and_b32 v67,0xffff0000,v85
v_mfma_f32_16x16x32_bf16 v[20:23],a[12:15],v[36:39],v[20:23]
s_cmp_ge_u32 s66,s59
v_lshlrev_b32 v68,16,v86
v_and_b32 v69,0xffff0000,v86
v_mfma_f32_16x16x32_bf16 v[16:19],a[16:19],v[40:43],v[16:19]
buffer_load_dwordx4 v1,s[36:39],s66 offen offset:0 sc1 lds
v_mfma_f32_16x16x32_bf16 v[20:23],a[20:23],v[40:43],v[20:23]
s_cselect_b32 s86,s49,s50
ds_read_b128 v[36:39],v2 offset:9216
v_lshlrev_b32 v70,16,v87
v_mfma_f32_16x16x32_bf16 v[16:19],a[24:27],v[44:47],v[16:19]
s_cselect_b32 s87,s87,s63
ds_read_b128 v[40:43],v2 offset:10240
v_and_b32 v71,0xffff0000,v87
v_mfma_f32_16x16x32_bf16 v[20:23],a[28:31],v[44:47],v[20:23]
s_waitcnt lgkmcnt(3)
v_fma_f32 v64,v64,v91,v90
v_fma_f32 v65,v65,v91,v90
v_mfma_f32_16x16x32_bf16 v[16:19],a[32:35],v[48:51],v[16:19]
buffer_load_dwordx4 v1,s[36:39],s66 offen offset:1024 sc1 lds
v_mfma_f32_16x16x32_bf16 v[20:23],a[36:39],v[48:51],v[20:23]
s_lshr_b32 s72,s66,6
ds_read_b128 v[44:47],v2 offset:11264
v_fma_f32 v66,v66,v91,v90
v_mfma_f32_16x16x32_bf16 v[16:19],a[40:43],v[52:55],v[16:19]
s_lshr_b32 s71,s62,2
ds_read_b128 v[48:51],v2 offset:12288
v_fma_f32 v67,v67,v91,v90
v_mfma_f32_16x16x32_bf16 v[20:23],a[44:47],v[52:55],v[20:23]
s_mov_b32 s62,s87
v_fma_f32 v68,v68,v91,v90
v_fma_f32 v69,v69,v91,v90
v_mfma_f32_16x16x32_bf16 v[16:19],a[48:51],v[56:59],v[16:19]
buffer_load_dwordx4 v1,s[36:39],s66 offen offset:2048 sc1 lds
v_mfma_f32_16x16x32_bf16 v[20:23],a[52:55],v[56:59],v[20:23]
ds_read_b128 v[52:55],v2 offset:13312
v_fma_f32 v70,v70,v91,v90
v_mfma_f32_16x16x32_bf16 v[16:19],a[56:59],v[60:63],v[16:19]
ds_read_b128 v[56:59],v2 offset:14336
v_fma_f32 v71,v71,v91,v90
v_mfma_f32_16x16x32_bf16 v[20:23],a[60:63],v[60:63],v[20:23]
s_waitcnt lgkmcnt(3)
v_add_f32 v24,v24,v64
v_add_f32 v25,v25,v65
v_mfma_f32_16x16x32_bf16 v[16:19],a[64:67],v[32:35],v[16:19]
s_waitcnt vmcnt(9)
ds_read_b128 v[60:63],v2 offset:15360
v_add_f32 v26,v26,v66
v_mfma_f32_16x16x32_bf16 v[20:23],a[68:71],v[32:35],v[20:23]
s_barrier
v_mfma_f32_16x16x32_bf16 v[16:19],a[72:75],v[36:39],v[16:19]
buffer_load_dwordx4 v1,s[36:39],s66 offen offset:3072 sc1 lds
v_mfma_f32_16x16x32_bf16 v[20:23],a[76:79],v[36:39],v[20:23]
s_add_u32 m0,s71,s88
ds_read_b128 v[32:35],v2 offset:16384
v_add_f32 v27,v27,v67
v_mfma_f32_16x16x32_bf16 v[16:19],a[80:83],v[40:43],v[16:19]
s_lshr_b32 s70,s46,5
ds_read_b128 v[36:39],v2 offset:17408
v_add_f32 v28,v28,v68
v_mfma_f32_16x16x32_bf16 v[20:23],a[84:87],v[40:43],v[20:23]
v_add_f32 v29,v29,v69
v_add_f32 v30,v30,v70
v_mfma_f32_16x16x32_bf16 v[16:19],a[88:91],v[44:47],v[16:19]
buffer_load_dwordx4 v10,s[36:39],s66 offen offset:0 sc1 lds
v_mfma_f32_16x16x32_bf16 v[20:23],a[92:95],v[44:47],v[20:23]
s_waitcnt lgkmcnt(2)
ds_read_b128 v[40:43],v2 offset:18432
v_add_f32 v31,v31,v71
v_mfma_f32_16x16x32_bf16 v[16:19],a[96:99],v[48:51],v[16:19]
buffer_load_dword v11,s[36:39],s72 offen offset:1024 sc1 lds
v_mfma_f32_16x16x32_bf16 v[20:23],a[100:103],v[48:51],v[20:23]
s_mov_b32 m0,s62
ds_read_b128 v[44:47],v2 offset:19456
v_cvt_pk_bf16_f32 v72,v24,v25
v_mfma_f32_16x16x32_bf16 v[16:19],a[104:107],v[52:55],v[16:19]
v_cvt_pk_bf16_f32 v73,v26,v27
v_cvt_pk_bf16_f32 v74,v28,v29
v_cvt_pk_bf16_f32 v75,v30,v31
v_mfma_f32_16x16x32_bf16 v[20:23],a[108:111],v[52:55],v[20:23]
ds_read_b128 v[48:51],v2 offset:20480
buffer_store_dwordx4 v[72:75],v3,s[36:39],s73 offen offset:0 sc0 sc1
v_add_f32 v64,v24,v25
v_mul_f32 v65,v24,v24
v_fma_f32 v65,v25,v25,v65
v_mfma_f32_16x16x32_bf16 v[16:19],a[112:115],v[56:59],v[16:19]
ds_read_b128 v[52:55],v2 offset:21504
v_add_f32 v64,v64,v26
v_mfma_f32_16x16x32_bf16 v[20:23],a[116:119],v[56:59],v[20:23]
v_fma_f32 v65,v26,v26,v65
v_add_f32 v64,v64,v27
v_fma_f32 v65,v27,v27,v65
v_add_f32 v64,v64,v28
v_fma_f32 v65,v28,v28,v65
v_add_f32 v64,v64,v29
v_fma_f32 v65,v29,v29,v65
v_add_f32 v64,v64,v30
v_fma_f32 v65,v30,v30,v65
v_add_f32 v64,v64,v31
v_fma_f32 v65,v31,v31,v65
v_mfma_f32_16x16x32_bf16 v[16:19],a[120:123],v[60:63],v[16:19]
s_nop 0
v_permlane16_swap_b32 v64,v65
v_add_f32 v64,v64,v65
v_mov_b32 v65,v64
v_mfma_f32_16x16x32_bf16 v[20:23],a[124:127],v[60:63],v[20:23]
s_waitcnt lgkmcnt(2)
ds_read_b128 v[56:59],v2 offset:22528
v_permlane32_swap_b32 v64,v65
v_add_f32 v64,v64,v65
v_mfma_f32_16x16x32_bf16 v[16:19],a[128:131],v[32:35],v[16:19]
ds_write_b32 v13,v64 offset:0
v_mfma_f32_16x16x32_bf16 v[20:23],a[132:135],v[32:35],v[20:23]
ds_read_b128 v[60:63],v2 offset:23552
v_mfma_f32_16x16x32_bf16 v[16:19],a[136:139],v[36:39],v[16:19]
ds_read_b128 v[32:35],v2 offset:24576
v_mfma_f32_16x16x32_bf16 v[20:23],a[140:143],v[36:39],v[20:23]
v_mfma_f32_16x16x32_bf16 v[16:19],a[144:147],v[40:43],v[16:19]
s_waitcnt vmcnt(12) lgkmcnt(2)
ds_read_b128 v[36:39],v2 offset:25600)"
R"(
v_mfma_f32_16x16x32_bf16 v[20:23],a[148:151],v[40:43],v[20:23]
s_barrier
v_mfma_f32_16x16x32_bf16 v[16:19],a[152:155],v[44:47],v[16:19]
buffer_store_dword v5,v4,s[36:39],s70 offen offset:0 sc0 sc1
ds_read_b128 v[40:43],v2 offset:26624
ds_read_b32 v15,v7 offset:0
v_mfma_f32_16x16x32_bf16 v[20:23],a[156:159],v[44:47],v[20:23]
s_waitcnt lgkmcnt(4)
v_mfma_f32_16x16x32_bf16 v[16:19],a[160:163],v[48:51],v[16:19]
ds_read_b128 v[44:47],v2 offset:27648
v_mfma_f32_16x16x32_bf16 v[20:23],a[164:167],v[48:51],v[20:23]
v_mfma_f32_16x16x32_bf16 v[16:19],a[168:171],v[52:55],v[16:19]
s_waitcnt lgkmcnt(1)
ds_read_b128 v[48:51],v2 offset:28672
v_mfma_f32_16x16x32_bf16 v[20:23],a[172:175],v[52:55],v[20:23]
v_cmp_eq_u32 vcc,s60,v15
v_mfma_f32_16x16x32_bf16 v[16:19],a[176:179],v[56:59],v[16:19]
s_cmp_eq_u32 vcc_lo,-1
ds_read_b128 v[52:55],v2 offset:29696
s_cselect_b32 s67,s86,s59
v_mfma_f32_16x16x32_bf16 v[20:23],a[180:183],v[56:59],v[20:23]
s_xor_b32 s63,s63,65536
s_add_u32 s53,s52,s55
s_cmp_eq_u32 s53,s56
v_mfma_f32_16x16x32_bf16 v[16:19],a[184:187],v[60:63],v[16:19]
s_cselect_b32 s53,s57,s53
ds_read_b128 v[56:59],v2 offset:30720
s_cmp_eq_u32 s53,s58
v_mfma_f32_16x16x32_bf16 v[20:23],a[188:191],v[60:63],v[20:23]
s_waitcnt lgkmcnt(3)
buffer_load_dwordx4 v0,s[36:39],s67 offen offset:0 sc1 lds
v_mfma_f32_16x16x32_bf16 v[16:19],a[192:195],v[32:35],v[16:19]
s_waitcnt vmcnt(9)
ds_read_b128 v[60:63],v2 offset:31744
s_cselect_b32 s53,s59,s53
v_mfma_f32_16x16x32_bf16 v[20:23],a[196:199],v[32:35],v[20:23]
s_barrier
v_mfma_f32_16x16x32_bf16 v[16:19],a[200:203],v[36:39],v[16:19]
s_cmp_ge_u32 s66,s59
ds_read_b128 v[32:35],v2 offset:32768
ds_read_b128 v[80:83],v8 offset:0
v_mfma_f32_16x16x32_bf16 v[20:23],a[204:207],v[36:39],v[20:23]
ds_read_b64 v[88:89],v9 offset:1024
v_mfma_f32_16x16x32_bf16 v[16:19],a[208:211],v[40:43],v[16:19]
ds_read_b128 v[36:39],v2 offset:33792
v_mfma_f32_16x16x32_bf16 v[20:23],a[212:215],v[40:43],v[20:23]
buffer_load_dwordx4 v0,s[36:39],s67 offen offset:1024 sc1 lds
v_mfma_f32_16x16x32_bf16 v[16:19],a[216:219],v[44:47],v[16:19]
ds_read_b128 v[40:43],v2 offset:34816
v_mfma_f32_16x16x32_bf16 v[20:23],a[220:223],v[44:47],v[20:23]
s_waitcnt lgkmcnt(5)
v_mfma_f32_16x16x32_bf16 v[16:19],a[224:227],v[48:51],v[16:19]
ds_read_b128 v[44:47],v2 offset:35840
v_mfma_f32_16x16x32_bf16 v[20:23],a[228:231],v[48:51],v[20:23]
s_waitcnt lgkmcnt(3)
buffer_load_dwordx4 v0,s[36:39],s67 offen offset:2048 sc1 lds
v_mfma_f32_16x16x32_bf16 v[16:19],a[232:235],v[52:55],v[16:19]
ds_read_b128 v[48:51],v2 offset:36864
v_mfma_f32_16x16x32_bf16 v[20:23],a[236:239],v[52:55],v[20:23]
v_mfma_f32_16x16x32_bf16 v[16:19],a[240:243],v[56:59],v[16:19]
ds_read_b128 v[52:55],v2 offset:37888
v_mfma_f32_16x16x32_bf16 v[20:23],a[244:247],v[56:59],v[20:23]
buffer_load_dwordx4 v0,s[36:39],s67 offen offset:3072 sc1 lds
v_mfma_f32_16x16x32_bf16 v[16:19],a[248:251],v[60:63],v[16:19]
s_waitcnt lgkmcnt(2)
ds_read_b128 v[56:59],v2 offset:38912
v_mfma_f32_16x16x32_bf16 v[20:23],a[252:255],v[60:63],v[20:23]
s_cbranch_scc1 Lg2_slow1_%=
Lg2_d1w3_%=:
v_mfma_f32_16x16x32_bf16 v[24:27],a[0:3],v[32:35],0
s_add_u32 m0,s62,16384
ds_read_b128 v[60:63],v2 offset:39936
v_lshlrev_b32 v64,16,v80
v_mfma_f32_16x16x32_bf16 v[28:31],a[4:7],v[32:35],0
s_lshr_b32 s73,s48,0
v_and_b32 v65,0xffff0000,v80
v_lshlrev_b32 v66,16,v81
v_mfma_f32_16x16x32_bf16 v[24:27],a[8:11],v[36:39],v[24:27]
s_xor_b32 s87,s63,65536
ds_read_b128 v[32:35],v2 offset:40960
v_and_b32 v67,0xffff0000,v81
v_mfma_f32_16x16x32_bf16 v[28:31],a[12:15],v[36:39],v[28:31]
s_cmp_ge_u32 s67,s59
v_lshlrev_b32 v68,16,v82
v_and_b32 v69,0xffff0000,v82
v_mfma_f32_16x16x32_bf16 v[24:27],a[16:19],v[40:43],v[24:27]
buffer_load_dwordx4 v1,s[36:39],s67 offen offset:0 sc1 lds
v_mfma_f32_16x16x32_bf16 v[28:31],a[20:23],v[40:43],v[28:31]
s_cselect_b32 s86,s50,s51
ds_read_b128 v[36:39],v2 offset:41984
v_lshlrev_b32 v70,16,v83
v_mfma_f32_16x16x32_bf16 v[24:27],a[24:27],v[44:47],v[24:27]
s_cselect_b32 s87,s87,s64
ds_read_b128 v[40:43],v2 offset:43008
v_and_b32 v71,0xffff0000,v83
v_mfma_f32_16x16x32_bf16 v[28:31],a[28:31],v[44:47],v[28:31]
s_waitcnt lgkmcnt(3)
v_fma_f32 v64,v64,v89,v88
v_fma_f32 v65,v65,v89,v88
v_mfma_f32_16x16x32_bf16 v[24:27],a[32:35],v[48:51],v[24:27]
buffer_load_dwordx4 v1,s[36:39],s67 offen offset:1024 sc1 lds
v_mfma_f32_16x16x32_bf16 v[28:31],a[36:39],v[48:51],v[28:31]
s_lshr_b32 s72,s67,6
ds_read_b128 v[44:47],v2 offset:44032
v_fma_f32 v66,v66,v89,v88
v_mfma_f32_16x16x32_bf16 v[24:27],a[40:43],v[52:55],v[24:27]
s_lshr_b32 s71,s62,2
ds_read_b128 v[48:51],v2 offset:45056
v_fma_f32 v67,v67,v89,v88
v_mfma_f32_16x16x32_bf16 v[28:31],a[44:47],v[52:55],v[28:31]
s_mov_b32 s62,s87
v_fma_f32 v68,v68,v89,v88
v_fma_f32 v69,v69,v89,v88
v_mfma_f32_16x16x32_bf16 v[24:27],a[48:51],v[56:59],v[24:27]
buffer_load_dwordx4 v1,s[36:39],s67 offen offset:2048 sc1 lds
v_mfma_f32_16x16x32_bf16 v[28:31],a[52:55],v[56:59],v[28:31]
s_mov_b64 s[44:45],s[46:47]
ds_read_b128 v[52:55],v2 offset:46080
v_fma_f32 v70,v70,v89,v88
v_mfma_f32_16x16x32_bf16 v[24:27],a[56:59],v[60:63],v[24:27]
ds_read_b128 v[56:59],v2 offset:47104
v_fma_f32 v71,v71,v89,v88
v_mfma_f32_16x16x32_bf16 v[28:31],a[60:63],v[60:63],v[28:31]
s_waitcnt lgkmcnt(3)
v_add_f32 v16,v16,v64
v_add_f32 v17,v17,v65
v_mfma_f32_16x16x32_bf16 v[24:27],a[64:67],v[32:35],v[24:27]
s_waitcnt vmcnt(9)
ds_read_b128 v[60:63],v2 offset:48128
v_add_f32 v18,v18,v66
v_mfma_f32_16x16x32_bf16 v[28:31],a[68:71],v[32:35],v[28:31]
s_barrier
v_mfma_f32_16x16x32_bf16 v[24:27],a[72:75],v[36:39],v[24:27]
buffer_load_dwordx4 v1,s[36:39],s67 offen offset:3072 sc1 lds
v_mfma_f32_16x16x32_bf16 v[28:31],a[76:79],v[36:39],v[28:31]
s_add_u32 m0,s71,s88
ds_read_b128 v[32:35],v2 offset:49152
v_add_f32 v19,v19,v67
v_mfma_f32_16x16x32_bf16 v[24:27],a[80:83],v[40:43],v[24:27]
s_lshr_b32 s70,s47,5
ds_read_b128 v[36:39],v2 offset:50176
v_add_f32 v20,v20,v68
v_mfma_f32_16x16x32_bf16 v[28:31],a[84:87],v[40:43],v[28:31]
s_mov_b64 s[46:47],s[48:49]
v_add_f32 v21,v21,v69
v_add_f32 v22,v22,v70
v_mfma_f32_16x16x32_bf16 v[24:27],a[88:91],v[44:47],v[24:27]
buffer_load_dwordx4 v10,s[36:39],s67 offen offset:0 sc1 lds
v_mfma_f32_16x16x32_bf16 v[28:31],a[92:95],v[44:47],v[28:31]
s_waitcnt lgkmcnt(2)
ds_read_b128 v[40:43],v2 offset:51200
v_add_f32 v23,v23,v71
v_mfma_f32_16x16x32_bf16 v[24:27],a[96:99],v[48:51],v[24:27]
buffer_load_dword v11,s[36:39],s72 offen offset:1024 sc1 lds
v_mfma_f32_16x16x32_bf16 v[28:31],a[100:103],v[48:51],v[28:31]
s_mov_b32 m0,s62
ds_read_b128 v[44:47],v2 offset:52224
v_cvt_pk_bf16_f32 v72,v16,v17
v_mfma_f32_16x16x32_bf16 v[24:27],a[104:107],v[52:55],v[24:27]
s_mov_b64 s[48:49],s[50:51]
v_cvt_pk_bf16_f32 v73,v18,v19
v_cvt_pk_bf16_f32 v74,v20,v21
v_cvt_pk_bf16_f32 v75,v22,v23
v_mfma_f32_16x16x32_bf16 v[28:31],a[108:111],v[52:55],v[28:31]
s_mov_b64 s[50:51],s[52:53]
ds_read_b128 v[48:51],v2 offset:53248
buffer_store_dwordx4 v[72:75],v3,s[36:39],s73 offen offset:0 sc0 sc1
v_add_f32 v64,v16,v17
v_mul_f32 v65,v16,v16
v_fma_f32 v65,v17,v17,v65
v_mfma_f32_16x16x32_bf16 v[24:27],a[112:115],v[56:59],v[24:27]
ds_read_b128 v[52:55],v2 offset:54272
v_add_f32 v64,v64,v18
v_mfma_f32_16x16x32_bf16 v[28:31],a[116:119],v[56:59],v[28:31]
v_fma_f32 v65,v18,v18,v65
v_add_f32 v64,v64,v19
v_fma_f32 v65,v19,v19,v65
v_add_f32 v64,v64,v20
v_fma_f32 v65,v20,v20,v65
v_add_f32 v64,v64,v21
v_fma_f32 v65,v21,v21,v65
v_add_f32 v64,v64,v22
v_fma_f32 v65,v22,v22,v65
v_add_f32 v64,v64,v23
v_fma_f32 v65,v23,v23,v65
v_mfma_f32_16x16x32_bf16 v[24:27],a[120:123],v[60:63],v[24:27]
s_nop 0
v_permlane16_swap_b32 v64,v65
v_add_f32 v64,v64,v65
v_mov_b32 v65,v64
v_mfma_f32_16x16x32_bf16 v[28:31],a[124:127],v[60:63],v[28:31]
s_waitcnt lgkmcnt(2)
ds_read_b128 v[56:59],v2 offset:55296
v_permlane32_swap_b32 v64,v65
v_add_f32 v64,v64,v65
v_mfma_f32_16x16x32_bf16 v[24:27],a[128:131],v[32:35],v[24:27]
ds_write_b32 v13,v64 offset:2048
v_mfma_f32_16x16x32_bf16 v[28:31],a[132:135],v[32:35],v[28:31]
ds_read_b128 v[60:63],v2 offset:56320
v_mfma_f32_16x16x32_bf16 v[24:27],a[136:139],v[36:39],v[24:27])"
R"(
ds_read_b128 v[32:35],v2 offset:57344
v_mfma_f32_16x16x32_bf16 v[28:31],a[140:143],v[36:39],v[28:31]
v_mfma_f32_16x16x32_bf16 v[24:27],a[144:147],v[40:43],v[24:27]
s_waitcnt vmcnt(12) lgkmcnt(2)
ds_read_b128 v[36:39],v2 offset:58368
v_mfma_f32_16x16x32_bf16 v[28:31],a[148:151],v[40:43],v[28:31]
s_barrier
v_mfma_f32_16x16x32_bf16 v[24:27],a[152:155],v[44:47],v[24:27]
buffer_store_dword v5,v4,s[36:39],s70 offen offset:0 sc0 sc1
ds_read_b128 v[40:43],v2 offset:59392
ds_read_b32 v15,v7 offset:256
v_mfma_f32_16x16x32_bf16 v[28:31],a[156:159],v[44:47],v[28:31]
s_waitcnt lgkmcnt(4)
v_mfma_f32_16x16x32_bf16 v[24:27],a[160:163],v[48:51],v[24:27]
ds_read_b128 v[44:47],v2 offset:60416
v_mfma_f32_16x16x32_bf16 v[28:31],a[164:167],v[48:51],v[28:31]
v_mfma_f32_16x16x32_bf16 v[24:27],a[168:171],v[52:55],v[24:27]
s_waitcnt lgkmcnt(1)
ds_read_b128 v[48:51],v2 offset:61440
v_mfma_f32_16x16x32_bf16 v[28:31],a[172:175],v[52:55],v[28:31]
v_cmp_eq_u32 vcc,s60,v15
v_mfma_f32_16x16x32_bf16 v[24:27],a[176:179],v[56:59],v[24:27]
s_cmp_eq_u32 vcc_lo,-1
ds_read_b128 v[52:55],v2 offset:62464
s_cselect_b32 s66,s86,s59
v_mfma_f32_16x16x32_bf16 v[28:31],a[180:183],v[56:59],v[28:31]
s_xor_b32 s64,s64,65536
s_add_u32 s74,s53,s55
s_cmp_eq_u32 s74,s56
v_mfma_f32_16x16x32_bf16 v[24:27],a[184:187],v[60:63],v[24:27]
s_cselect_b32 s74,s57,s74
ds_read_b128 v[56:59],v2 offset:63488
s_cmp_eq_u32 s74,s58
v_mfma_f32_16x16x32_bf16 v[28:31],a[188:191],v[60:63],v[28:31]
s_waitcnt lgkmcnt(3)
buffer_load_dwordx4 v0,s[36:39],s66 offen offset:0 sc1 lds
v_mfma_f32_16x16x32_bf16 v[24:27],a[192:195],v[32:35],v[24:27]
s_waitcnt vmcnt(9)
ds_read_b128 v[60:63],v2 offset:64512
v_xor_b32 v2,65536,v2
v_mfma_f32_16x16x32_bf16 v[28:31],a[196:199],v[32:35],v[28:31]
s_barrier
v_mfma_f32_16x16x32_bf16 v[24:27],a[200:203],v[36:39],v[24:27]
s_cselect_b32 s74,s59,s74
ds_read_b128 v[32:35],v2 offset:0
ds_read_b128 v[84:87],v8 offset:8192
v_mfma_f32_16x16x32_bf16 v[28:31],a[204:207],v[36:39],v[28:31]
s_mov_b32 s52,s74
ds_read_b64 v[90:91],v9 offset:9216
v_xor_b32 v8,16384,v8
v_mfma_f32_16x16x32_bf16 v[24:27],a[208:211],v[40:43],v[24:27]
s_cmp_lt_u32 s67,s59
ds_read_b128 v[36:39],v2 offset:1024
v_xor_b32 v9,16384,v9
v_mfma_f32_16x16x32_bf16 v[28:31],a[212:215],v[40:43],v[28:31]
buffer_load_dwordx4 v0,s[36:39],s66 offen offset:1024 sc1 lds
v_mfma_f32_16x16x32_bf16 v[24:27],a[216:219],v[44:47],v[24:27]
ds_read_b128 v[40:43],v2 offset:2048
v_mfma_f32_16x16x32_bf16 v[28:31],a[220:223],v[44:47],v[28:31]
s_waitcnt lgkmcnt(5)
v_mfma_f32_16x16x32_bf16 v[24:27],a[224:227],v[48:51],v[24:27]
ds_read_b128 v[44:47],v2 offset:3072
v_mfma_f32_16x16x32_bf16 v[28:31],a[228:231],v[48:51],v[28:31]
s_waitcnt lgkmcnt(3)
buffer_load_dwordx4 v0,s[36:39],s66 offen offset:2048 sc1 lds
v_mfma_f32_16x16x32_bf16 v[24:27],a[232:235],v[52:55],v[24:27]
ds_read_b128 v[48:51],v2 offset:4096
v_mfma_f32_16x16x32_bf16 v[28:31],a[236:239],v[52:55],v[28:31]
v_mfma_f32_16x16x32_bf16 v[24:27],a[240:243],v[56:59],v[24:27]
ds_read_b128 v[52:55],v2 offset:5120
v_mfma_f32_16x16x32_bf16 v[28:31],a[244:247],v[56:59],v[28:31]
buffer_load_dwordx4 v0,s[36:39],s66 offen offset:3072 sc1 lds
v_mfma_f32_16x16x32_bf16 v[24:27],a[248:251],v[60:63],v[24:27]
s_waitcnt lgkmcnt(2)
ds_read_b128 v[56:59],v2 offset:6144
v_mfma_f32_16x16x32_bf16 v[28:31],a[252:255],v[60:63],v[28:31]
s_cbranch_scc1 Lg2_d0w3_%=
s_branch Lg2_slow0_%=
Lg2_slow0_%=:
s_cmp_lt_u32 s66,s59
s_cbranch_scc1 Lg2_gs0_%=
s_cmp_eq_u32 s54,1
s_cselect_b32 s68,s49,s48
s_lshr_b32 s68,s68,5
s_add_u32 m0,s78,8192
s_nop 0
buffer_load_dword v6,s[36:39],s68 offen offset:0 sc1 lds
s_lshr_b32 s73,s47,0
s_lshr_b32 s85,s47,5
v_lshlrev_b32 v64,16,v84
v_and_b32 v65,0xffff0000,v84
v_lshlrev_b32 v66,16,v85
v_and_b32 v67,0xffff0000,v85
v_lshlrev_b32 v68,16,v86
v_and_b32 v69,0xffff0000,v86
v_lshlrev_b32 v70,16,v87
v_and_b32 v71,0xffff0000,v87
v_fma_f32 v64,v64,v91,v90
v_fma_f32 v65,v65,v91,v90
v_fma_f32 v66,v66,v91,v90
v_fma_f32 v67,v67,v91,v90
v_fma_f32 v68,v68,v91,v90
v_fma_f32 v69,v69,v91,v90
v_fma_f32 v70,v70,v91,v90
v_fma_f32 v71,v71,v91,v90
v_add_f32 v24,v24,v64
v_add_f32 v25,v25,v65
v_add_f32 v26,v26,v66
v_add_f32 v27,v27,v67
v_add_f32 v28,v28,v68
v_add_f32 v29,v29,v69
v_add_f32 v30,v30,v70
v_add_f32 v31,v31,v71
v_cvt_pk_bf16_f32 v72,v24,v25
v_cvt_pk_bf16_f32 v73,v26,v27
v_cvt_pk_bf16_f32 v74,v28,v29
v_cvt_pk_bf16_f32 v75,v30,v31
s_add_u32 m0,s78,8448
s_nop 0
buffer_load_dword v6,s[36:39],s68 offen offset:0 sc1 lds
buffer_store_dwordx4 v[72:75],v3,s[36:39],s73 offen offset:0 sc0 sc1
v_add_f32 v64,v24,v25
v_mul_f32 v65,v24,v24
v_fma_f32 v65,v25,v25,v65
v_add_f32 v64,v64,v26
v_fma_f32 v65,v26,v26,v65
v_add_f32 v64,v64,v27
v_fma_f32 v65,v27,v27,v65
v_add_f32 v64,v64,v28
v_fma_f32 v65,v28,v28,v65
v_add_f32 v64,v64,v29
v_fma_f32 v65,v29,v29,v65
v_add_f32 v64,v64,v30
v_fma_f32 v65,v30,v30,v65
v_add_f32 v64,v64,v31
v_fma_f32 v65,v31,v31,v65
s_nop 1
v_permlane16_swap_b32 v64,v65
v_add_f32 v64,v64,v65
v_mov_b32 v65,v64
s_nop 1
v_permlane32_swap_b32 v64,v65
v_add_f32 v64,v64,v65
ds_write_b32 v13,v64 offset:0
s_waitcnt lgkmcnt(0)
s_barrier
ds_read_b128 v[76:79],v14 offset:0
s_waitcnt lgkmcnt(0)
v_add_f32 v76,v76,v77
v_add_f32 v78,v78,v79
v_add_f32 v76,v76,v78
buffer_store_dword v76,v12,s[36:39],s85 offen offset:0 sc0 sc1
s_cmp_ge_u32 s48,s59
s_cbranch_scc1 Lg2_done0_%=
s_add_u32 m0,s78,16384
s_nop 0
buffer_load_dword v6,s[36:39],s68 offen offset:0 sc1 lds
s_waitcnt vmcnt(4)
s_barrier
ds_read_b32 v15,v7 offset:8192
s_lshr_b32 s70,s46,5
buffer_store_dword v5,v4,s[36:39],s70 offen offset:0 sc0 sc1
s_mov_b32 s46,s59
s_mov_b32 s69,18432
s_waitcnt lgkmcnt(0)
v_cmp_eq_u32 vcc,s60,v15
s_nop 0
s_cmp_eq_u32 vcc_lo,-1
s_cbranch_scc1 Lg2_go0_%=
s_add_u32 m0,s78,16640
s_nop 0
buffer_load_dword v6,s[36:39],s68 offen offset:0 sc1 lds
s_waitcnt vmcnt(5)
s_barrier
ds_read_b32 v15,v7 offset:8448
s_mov_b32 s69,18688
s_waitcnt lgkmcnt(0)
v_cmp_eq_u32 vcc,s60,v15
s_nop 0
s_cmp_eq_u32 vcc_lo,-1
s_cbranch_scc1 Lg2_go0_%=
s_add_u32 m0,s78,8192
s_nop 0
buffer_load_dword v6,s[36:39],s68 offen offset:0 sc1 lds
s_waitcnt vmcnt(3)
s_barrier
ds_read_b32 v15,v7 offset:16384
s_lshr_b32 s70,s47,5
buffer_store_dword v5,v4,s[36:39],s70 offen offset:0 sc0 sc1
s_mov_b32 s47,s59
s_mov_b32 s69,10240
s_waitcnt lgkmcnt(0)
v_cmp_eq_u32 vcc,s60,v15
s_nop 0
s_cmp_eq_u32 vcc_lo,-1
s_cbranch_scc1 Lg2_go0_%=
s_add_u32 m0,s78,8448
s_nop 0
buffer_load_dword v6,s[36:39],s68 offen offset:0 sc1 lds
s_waitcnt vmcnt(3))"
R"(
s_barrier
ds_read_b32 v15,v7 offset:16640
s_mov_b32 s69,10496
s_waitcnt lgkmcnt(0)
v_cmp_eq_u32 vcc,s60,v15
s_nop 0
s_cmp_eq_u32 vcc_lo,-1
s_cbranch_scc1 Lg2_go0_%=
Lg2_poll0_%=:
s_add_u32 m0,s78,16384
s_nop 0
buffer_load_dword v6,s[36:39],s68 offen offset:0 sc1 lds
s_waitcnt vmcnt(2)
s_barrier
ds_read_b32 v15,v7 offset:8192
s_mov_b32 s69,18432
s_waitcnt lgkmcnt(0)
v_cmp_eq_u32 vcc,s60,v15
s_nop 0
s_cmp_eq_u32 vcc_lo,-1
s_cbranch_scc1 Lg2_go0_%=
s_sleep 4
s_add_u32 m0,s78,16640
s_nop 0
buffer_load_dword v6,s[36:39],s68 offen offset:0 sc1 lds
s_waitcnt vmcnt(2)
s_barrier
ds_read_b32 v15,v7 offset:8448
s_mov_b32 s69,18688
s_waitcnt lgkmcnt(0)
v_cmp_eq_u32 vcc,s60,v15
s_nop 0
s_cmp_eq_u32 vcc_lo,-1
s_cbranch_scc1 Lg2_go0_%=
s_sleep 4
s_add_u32 m0,s78,8192
s_nop 0
buffer_load_dword v6,s[36:39],s68 offen offset:0 sc1 lds
s_waitcnt vmcnt(2)
s_barrier
ds_read_b32 v15,v7 offset:16384
s_mov_b32 s69,10240
s_waitcnt lgkmcnt(0)
v_cmp_eq_u32 vcc,s60,v15
s_nop 0
s_cmp_eq_u32 vcc_lo,-1
s_cbranch_scc1 Lg2_go0_%=
s_sleep 4
s_add_u32 m0,s78,8448
s_nop 0
buffer_load_dword v6,s[36:39],s68 offen offset:0 sc1 lds
s_waitcnt vmcnt(2)
s_barrier
ds_read_b32 v15,v7 offset:16640
s_mov_b32 s69,10496
s_waitcnt lgkmcnt(0)
v_cmp_eq_u32 vcc,s60,v15
s_nop 0
s_cmp_eq_u32 vcc_lo,-1
s_cbranch_scc1 Lg2_go0_%=
s_sleep 4
s_add_u32 s79,s79,1
s_cmp_lt_u32 s79,20000
s_cbranch_scc1 Lg2_poll0_%=
Lg2_go0_%=:
s_xor_b32 s62,s63,65536
s_mov_b32 m0,s62
s_nop 0
buffer_load_dwordx4 v0,s[36:39],s48 offen offset:0 sc1 lds
buffer_load_dwordx4 v0,s[36:39],s48 offen offset:1024 sc1 lds
buffer_load_dwordx4 v0,s[36:39],s48 offen offset:2048 sc1 lds
buffer_load_dwordx4 v0,s[36:39],s48 offen offset:3072 sc1 lds
s_add_u32 m0,s62,16384
s_nop 0
buffer_load_dwordx4 v1,s[36:39],s48 offen offset:0 sc1 lds
buffer_load_dwordx4 v1,s[36:39],s48 offen offset:1024 sc1 lds
buffer_load_dwordx4 v1,s[36:39],s48 offen offset:2048 sc1 lds
buffer_load_dwordx4 v1,s[36:39],s48 offen offset:3072 sc1 lds
s_lshr_b32 s72,s48,6
s_lshr_b32 s71,s62,2
s_add_u32 m0,s71,s88
s_nop 0
buffer_load_dwordx4 v10,s[36:39],s48 offen offset:0 sc1 lds
buffer_load_dword v11,s[36:39],s72 offen offset:1024 sc1 lds
s_cmp_eq_u32 vcc_hi,-1
s_cselect_b32 s66,s49,s59
s_xor_b32 s62,s64,65536
s_cmp_ge_u32 s66,s59
s_cselect_b32 s68,s49,s50
s_lshr_b32 s68,s68,5
s_add_u32 m0,s78,0
s_nop 0
buffer_load_dword v6,s[36:39],s68 offen offset:0 sc1 lds
s_mov_b32 m0,s62
s_nop 0
buffer_load_dwordx4 v0,s[36:39],s66 offen offset:0 sc1 lds
buffer_load_dwordx4 v0,s[36:39],s66 offen offset:1024 sc1 lds
buffer_load_dwordx4 v0,s[36:39],s66 offen offset:2048 sc1 lds
buffer_load_dwordx4 v0,s[36:39],s66 offen offset:3072 sc1 lds
s_waitcnt vmcnt(11)
s_barrier
ds_read_b128 v[32:35],v2 offset:0
ds_read_b128 v[36:39],v2 offset:1024
ds_read_b128 v[40:43],v2 offset:2048
ds_read_b128 v[44:47],v2 offset:3072
ds_read_b128 v[48:51],v2 offset:4096
ds_read_b128 v[52:55],v2 offset:5120
ds_read_b128 v[56:59],v2 offset:6144
s_lshr_b32 s70,s46,5
buffer_store_dword v5,v4,s[36:39],s70 offen offset:0 sc0 sc1
s_mov_b32 s46,s59
s_lshr_b32 s70,s47,5
buffer_store_dword v5,v4,s[36:39],s70 offen offset:0 sc0 sc1
s_mov_b32 s47,s59
s_lshr_b32 s70,s59,5
buffer_store_dword v5,v4,s[36:39],s70 offen offset:0 sc0 sc1
s_waitcnt lgkmcnt(3)
s_setpc_b64 s[90:91]
Lg2_gs0_%=:
s_cmp_eq_u32 s54,1
s_cselect_b32 s68,s49,s48
s_lshr_b32 s68,s68,5
s_lshr_b32 s73,s47,0
s_lshr_b32 s85,s47,5
s_add_u32 m0,s78,8192
s_nop 0
buffer_load_dword v6,s[36:39],s68 offen offset:0 sc1 lds
v_lshlrev_b32 v64,16,v84
v_and_b32 v65,0xffff0000,v84
v_lshlrev_b32 v66,16,v85
v_and_b32 v67,0xffff0000,v85
v_lshlrev_b32 v68,16,v86
v_and_b32 v69,0xffff0000,v86
v_lshlrev_b32 v70,16,v87
v_and_b32 v71,0xffff0000,v87
s_add_u32 m0,s62,16384
s_nop 0
buffer_load_dwordx4 v1,s[36:39],s66 offen offset:0 sc1 lds
v_fma_f32 v64,v64,v91,v90
v_fma_f32 v65,v65,v91,v90
v_fma_f32 v66,v66,v91,v90
v_fma_f32 v67,v67,v91,v90
v_fma_f32 v68,v68,v91,v90
v_fma_f32 v69,v69,v91,v90
v_fma_f32 v70,v70,v91,v90
v_fma_f32 v71,v71,v91,v90
buffer_load_dwordx4 v1,s[36:39],s66 offen offset:1024 sc1 lds
v_add_f32 v24,v24,v64
v_add_f32 v25,v25,v65
v_add_f32 v26,v26,v66
v_add_f32 v27,v27,v67
v_add_f32 v28,v28,v68
v_add_f32 v29,v29,v69
v_add_f32 v30,v30,v70
v_add_f32 v31,v31,v71
buffer_load_dwordx4 v1,s[36:39],s66 offen offset:2048 sc1 lds
v_cvt_pk_bf16_f32 v72,v24,v25
v_cvt_pk_bf16_f32 v73,v26,v27
v_cvt_pk_bf16_f32 v74,v28,v29
v_cvt_pk_bf16_f32 v75,v30,v31
buffer_store_dwordx4 v[72:75],v3,s[36:39],s73 offen offset:0 sc0 sc1
v_add_f32 v64,v24,v25
v_mul_f32 v65,v24,v24
v_fma_f32 v65,v25,v25,v65
buffer_load_dwordx4 v1,s[36:39],s66 offen offset:3072 sc1 lds
v_add_f32 v64,v64,v26
v_fma_f32 v65,v26,v26,v65
v_add_f32 v64,v64,v27
v_fma_f32 v65,v27,v27,v65
v_add_f32 v64,v64,v28
v_fma_f32 v65,v28,v28,v65
v_add_f32 v64,v64,v29
v_fma_f32 v65,v29,v29,v65
s_lshr_b32 s72,s66,6
s_lshr_b32 s71,s62,2
s_add_u32 m0,s71,s88
s_nop 0
buffer_load_dwordx4 v10,s[36:39],s66 offen offset:0 sc1 lds
v_add_f32 v64,v64,v30
v_fma_f32 v65,v30,v30,v65
v_add_f32 v64,v64,v31
v_fma_f32 v65,v31,v31,v65
s_nop 1
v_permlane16_swap_b32 v64,v65
v_add_f32 v64,v64,v65
v_mov_b32 v65,v64
s_nop 1
v_permlane32_swap_b32 v64,v65
buffer_load_dword v11,s[36:39],s72 offen offset:1024 sc1 lds
v_add_f32 v64,v64,v65
ds_write_b32 v13,v64 offset:0
s_waitcnt lgkmcnt(0)
s_barrier
ds_read_b128 v[76:79],v14 offset:0
s_waitcnt lgkmcnt(0)
v_add_f32 v76,v76,v77
v_add_f32 v78,v78,v79
v_add_f32 v76,v76,v78
buffer_store_dword v76,v12,s[36:39],s85 offen offset:0 sc0 sc1
s_waitcnt vmcnt(8))"
R"(
s_barrier
ds_read_b32 v15,v7 offset:8192
ds_read_b128 v[32:35],v2 offset:0
ds_read_b128 v[36:39],v2 offset:1024
ds_read_b128 v[40:43],v2 offset:2048
ds_read_b128 v[44:47],v2 offset:3072
ds_read_b128 v[48:51],v2 offset:4096
ds_read_b128 v[52:55],v2 offset:5120
ds_read_b128 v[56:59],v2 offset:6144
s_lshr_b32 s70,s46,5
buffer_store_dword v5,v4,s[36:39],s70 offen offset:0 sc0 sc1
s_waitcnt lgkmcnt(7)
v_cmp_eq_u32 vcc,s60,v15
s_nop 0
s_cmp_eq_u32 vcc_hi,-1
s_cselect_b32 s66,s49,s59
s_xor_b32 s62,s64,65536
s_cmp_ge_u32 s66,s59
s_cselect_b32 s68,s49,s50
s_lshr_b32 s68,s68,5
s_add_u32 m0,s78,0
s_nop 0
buffer_load_dword v6,s[36:39],s68 offen offset:0 sc1 lds
s_mov_b32 m0,s62
s_nop 0
buffer_load_dwordx4 v0,s[36:39],s66 offen offset:0 sc1 lds
buffer_load_dwordx4 v0,s[36:39],s66 offen offset:1024 sc1 lds
buffer_load_dwordx4 v0,s[36:39],s66 offen offset:2048 sc1 lds
buffer_load_dwordx4 v0,s[36:39],s66 offen offset:3072 sc1 lds
s_mov_b32 s46,s47
s_mov_b32 s47,s59
s_lshr_b32 s70,s59,5
buffer_store_dword v5,v4,s[36:39],s70 offen offset:0 sc0 sc1
s_lshr_b32 s70,s59,5
buffer_store_dword v5,v4,s[36:39],s70 offen offset:0 sc0 sc1
s_waitcnt lgkmcnt(3)
s_setpc_b64 s[90:91]
Lg2_done0_%=:
s_waitcnt vmcnt(0)
s_barrier
s_lshr_b32 s70,s46,5
buffer_store_dword v5,v4,s[36:39],s70 offen offset:0 sc0 sc1
s_lshr_b32 s70,s47,5
buffer_store_dword v5,v4,s[36:39],s70 offen offset:0 sc0 sc1
s_branch Lg2_done_%=
Lg2_slow1_%=:
s_cmp_lt_u32 s67,s59
s_cbranch_scc1 Lg2_gs1_%=
s_cmp_eq_u32 s54,1
s_cselect_b32 s68,s50,s49
s_lshr_b32 s68,s68,5
s_add_u32 m0,s78,8192
s_nop 0
buffer_load_dword v6,s[36:39],s68 offen offset:0 sc1 lds
s_lshr_b32 s73,s48,0
s_lshr_b32 s85,s48,5
v_lshlrev_b32 v64,16,v80
v_and_b32 v65,0xffff0000,v80
v_lshlrev_b32 v66,16,v81
v_and_b32 v67,0xffff0000,v81
v_lshlrev_b32 v68,16,v82
v_and_b32 v69,0xffff0000,v82
v_lshlrev_b32 v70,16,v83
v_and_b32 v71,0xffff0000,v83
v_fma_f32 v64,v64,v89,v88
v_fma_f32 v65,v65,v89,v88
v_fma_f32 v66,v66,v89,v88
v_fma_f32 v67,v67,v89,v88
v_fma_f32 v68,v68,v89,v88
v_fma_f32 v69,v69,v89,v88
v_fma_f32 v70,v70,v89,v88
v_fma_f32 v71,v71,v89,v88
v_add_f32 v16,v16,v64
v_add_f32 v17,v17,v65
v_add_f32 v18,v18,v66
v_add_f32 v19,v19,v67
v_add_f32 v20,v20,v68
v_add_f32 v21,v21,v69
v_add_f32 v22,v22,v70
v_add_f32 v23,v23,v71
v_cvt_pk_bf16_f32 v72,v16,v17
v_cvt_pk_bf16_f32 v73,v18,v19
v_cvt_pk_bf16_f32 v74,v20,v21
v_cvt_pk_bf16_f32 v75,v22,v23
s_add_u32 m0,s78,8448
s_nop 0
buffer_load_dword v6,s[36:39],s68 offen offset:0 sc1 lds
buffer_store_dwordx4 v[72:75],v3,s[36:39],s73 offen offset:0 sc0 sc1
v_add_f32 v64,v16,v17
v_mul_f32 v65,v16,v16
v_fma_f32 v65,v17,v17,v65
v_add_f32 v64,v64,v18
v_fma_f32 v65,v18,v18,v65
v_add_f32 v64,v64,v19
v_fma_f32 v65,v19,v19,v65
v_add_f32 v64,v64,v20
v_fma_f32 v65,v20,v20,v65
v_add_f32 v64,v64,v21
v_fma_f32 v65,v21,v21,v65
v_add_f32 v64,v64,v22
v_fma_f32 v65,v22,v22,v65
v_add_f32 v64,v64,v23
v_fma_f32 v65,v23,v23,v65
s_nop 1
v_permlane16_swap_b32 v64,v65
v_add_f32 v64,v64,v65
v_mov_b32 v65,v64
s_nop 1
v_permlane32_swap_b32 v64,v65
v_add_f32 v64,v64,v65
ds_write_b32 v13,v64 offset:2048
s_waitcnt lgkmcnt(0)
s_barrier
ds_read_b128 v[76:79],v14 offset:2048
s_waitcnt lgkmcnt(0)
v_add_f32 v76,v76,v77
v_add_f32 v78,v78,v79
v_add_f32 v76,v76,v78
buffer_store_dword v76,v12,s[36:39],s85 offen offset:0 sc0 sc1
s_cmp_ge_u32 s49,s59
s_cbranch_scc1 Lg2_done1_%=
s_add_u32 m0,s78,16384
s_nop 0
buffer_load_dword v6,s[36:39],s68 offen offset:0 sc1 lds
s_waitcnt vmcnt(4)
s_barrier
ds_read_b32 v15,v7 offset:8192
s_lshr_b32 s70,s47,5
buffer_store_dword v5,v4,s[36:39],s70 offen offset:0 sc0 sc1
s_mov_b32 s47,s59
s_mov_b32 s69,18432
s_waitcnt lgkmcnt(0)
v_cmp_eq_u32 vcc,s60,v15
s_nop 0
s_cmp_eq_u32 vcc_lo,-1
s_cbranch_scc1 Lg2_go1_%=
s_add_u32 m0,s78,16640
s_nop 0
buffer_load_dword v6,s[36:39],s68 offen offset:0 sc1 lds
s_waitcnt vmcnt(5)
s_barrier
ds_read_b32 v15,v7 offset:8448
s_mov_b32 s69,18688
s_waitcnt lgkmcnt(0)
v_cmp_eq_u32 vcc,s60,v15
s_nop 0
s_cmp_eq_u32 vcc_lo,-1
s_cbranch_scc1 Lg2_go1_%=
s_add_u32 m0,s78,8192
s_nop 0
buffer_load_dword v6,s[36:39],s68 offen offset:0 sc1 lds
s_waitcnt vmcnt(3)
s_barrier
ds_read_b32 v15,v7 offset:16384
s_lshr_b32 s70,s48,5
buffer_store_dword v5,v4,s[36:39],s70 offen offset:0 sc0 sc1
s_mov_b32 s48,s59
s_mov_b32 s69,10240
s_waitcnt lgkmcnt(0)
v_cmp_eq_u32 vcc,s60,v15
s_nop 0
s_cmp_eq_u32 vcc_lo,-1
s_cbranch_scc1 Lg2_go1_%=
s_add_u32 m0,s78,8448
s_nop 0
buffer_load_dword v6,s[36:39],s68 offen offset:0 sc1 lds
s_waitcnt vmcnt(3)
s_barrier
ds_read_b32 v15,v7 offset:16640
s_mov_b32 s69,10496
s_waitcnt lgkmcnt(0)
v_cmp_eq_u32 vcc,s60,v15
s_nop 0
s_cmp_eq_u32 vcc_lo,-1
s_cbranch_scc1 Lg2_go1_%=
Lg2_poll1_%=:
s_add_u32 m0,s78,16384
s_nop 0
buffer_load_dword v6,s[36:39],s68 offen offset:0 sc1 lds
s_waitcnt vmcnt(2)
s_barrier
ds_read_b32 v15,v7 offset:8192
s_mov_b32 s69,18432
s_waitcnt lgkmcnt(0)
v_cmp_eq_u32 vcc,s60,v15
s_nop 0
s_cmp_eq_u32 vcc_lo,-1
s_cbranch_scc1 Lg2_go1_%=
s_sleep 4
s_add_u32 m0,s78,16640
s_nop 0
buffer_load_dword v6,s[36:39],s68 offen offset:0 sc1 lds
s_waitcnt vmcnt(2)
s_barrier
ds_read_b32 v15,v7 offset:8448
s_mov_b32 s69,18688
s_waitcnt lgkmcnt(0)
v_cmp_eq_u32 vcc,s60,v15
s_nop 0
s_cmp_eq_u32 vcc_lo,-1)"
R"(
s_cbranch_scc1 Lg2_go1_%=
s_sleep 4
s_add_u32 m0,s78,8192
s_nop 0
buffer_load_dword v6,s[36:39],s68 offen offset:0 sc1 lds
s_waitcnt vmcnt(2)
s_barrier
ds_read_b32 v15,v7 offset:16384
s_mov_b32 s69,10240
s_waitcnt lgkmcnt(0)
v_cmp_eq_u32 vcc,s60,v15
s_nop 0
s_cmp_eq_u32 vcc_lo,-1
s_cbranch_scc1 Lg2_go1_%=
s_sleep 4
s_add_u32 m0,s78,8448
s_nop 0
buffer_load_dword v6,s[36:39],s68 offen offset:0 sc1 lds
s_waitcnt vmcnt(2)
s_barrier
ds_read_b32 v15,v7 offset:16640
s_mov_b32 s69,10496
s_waitcnt lgkmcnt(0)
v_cmp_eq_u32 vcc,s60,v15
s_nop 0
s_cmp_eq_u32 vcc_lo,-1
s_cbranch_scc1 Lg2_go1_%=
s_sleep 4
s_add_u32 s79,s79,1
s_cmp_lt_u32 s79,20000
s_cbranch_scc1 Lg2_poll1_%=
Lg2_go1_%=:
s_xor_b32 s62,s64,65536
s_mov_b32 m0,s62
s_nop 0
buffer_load_dwordx4 v0,s[36:39],s49 offen offset:0 sc1 lds
buffer_load_dwordx4 v0,s[36:39],s49 offen offset:1024 sc1 lds
buffer_load_dwordx4 v0,s[36:39],s49 offen offset:2048 sc1 lds
buffer_load_dwordx4 v0,s[36:39],s49 offen offset:3072 sc1 lds
s_add_u32 m0,s62,16384
s_nop 0
buffer_load_dwordx4 v1,s[36:39],s49 offen offset:0 sc1 lds
buffer_load_dwordx4 v1,s[36:39],s49 offen offset:1024 sc1 lds
buffer_load_dwordx4 v1,s[36:39],s49 offen offset:2048 sc1 lds
buffer_load_dwordx4 v1,s[36:39],s49 offen offset:3072 sc1 lds
s_lshr_b32 s72,s49,6
s_lshr_b32 s71,s62,2
s_add_u32 m0,s71,s88
s_nop 0
buffer_load_dwordx4 v10,s[36:39],s49 offen offset:0 sc1 lds
buffer_load_dword v11,s[36:39],s72 offen offset:1024 sc1 lds
s_cmp_eq_u32 vcc_hi,-1
s_cselect_b32 s67,s50,s59
s_xor_b32 s62,s63,65536
s_cmp_ge_u32 s67,s59
s_cselect_b32 s68,s50,s51
s_lshr_b32 s68,s68,5
s_add_u32 m0,s78,256
s_nop 0
buffer_load_dword v6,s[36:39],s68 offen offset:0 sc1 lds
s_mov_b32 m0,s62
s_nop 0
buffer_load_dwordx4 v0,s[36:39],s67 offen offset:0 sc1 lds
buffer_load_dwordx4 v0,s[36:39],s67 offen offset:1024 sc1 lds
buffer_load_dwordx4 v0,s[36:39],s67 offen offset:2048 sc1 lds
buffer_load_dwordx4 v0,s[36:39],s67 offen offset:3072 sc1 lds
s_waitcnt vmcnt(11)
s_barrier
ds_read_b128 v[32:35],v2 offset:32768
ds_read_b128 v[36:39],v2 offset:33792
ds_read_b128 v[40:43],v2 offset:34816
ds_read_b128 v[44:47],v2 offset:35840
ds_read_b128 v[48:51],v2 offset:36864
ds_read_b128 v[52:55],v2 offset:37888
ds_read_b128 v[56:59],v2 offset:38912
s_lshr_b32 s70,s47,5
buffer_store_dword v5,v4,s[36:39],s70 offen offset:0 sc0 sc1
s_mov_b32 s47,s59
s_lshr_b32 s70,s48,5
buffer_store_dword v5,v4,s[36:39],s70 offen offset:0 sc0 sc1
s_mov_b32 s48,s59
s_lshr_b32 s70,s59,5
buffer_store_dword v5,v4,s[36:39],s70 offen offset:0 sc0 sc1
s_waitcnt lgkmcnt(3)
s_setpc_b64 s[92:93]
Lg2_gs1_%=:
s_cmp_eq_u32 s54,1
s_cselect_b32 s68,s50,s49
s_lshr_b32 s68,s68,5
s_lshr_b32 s73,s48,0
s_lshr_b32 s85,s48,5
s_add_u32 m0,s78,8192
s_nop 0
buffer_load_dword v6,s[36:39],s68 offen offset:0 sc1 lds
v_lshlrev_b32 v64,16,v80
v_and_b32 v65,0xffff0000,v80
v_lshlrev_b32 v66,16,v81
v_and_b32 v67,0xffff0000,v81
v_lshlrev_b32 v68,16,v82
v_and_b32 v69,0xffff0000,v82
v_lshlrev_b32 v70,16,v83
v_and_b32 v71,0xffff0000,v83
s_add_u32 m0,s62,16384
s_nop 0
buffer_load_dwordx4 v1,s[36:39],s67 offen offset:0 sc1 lds
v_fma_f32 v64,v64,v89,v88
v_fma_f32 v65,v65,v89,v88
v_fma_f32 v66,v66,v89,v88
v_fma_f32 v67,v67,v89,v88
v_fma_f32 v68,v68,v89,v88
v_fma_f32 v69,v69,v89,v88
v_fma_f32 v70,v70,v89,v88
v_fma_f32 v71,v71,v89,v88
buffer_load_dwordx4 v1,s[36:39],s67 offen offset:1024 sc1 lds
v_add_f32 v16,v16,v64
v_add_f32 v17,v17,v65
v_add_f32 v18,v18,v66
v_add_f32 v19,v19,v67
v_add_f32 v20,v20,v68
v_add_f32 v21,v21,v69
v_add_f32 v22,v22,v70
v_add_f32 v23,v23,v71
buffer_load_dwordx4 v1,s[36:39],s67 offen offset:2048 sc1 lds
v_cvt_pk_bf16_f32 v72,v16,v17
v_cvt_pk_bf16_f32 v73,v18,v19
v_cvt_pk_bf16_f32 v74,v20,v21
v_cvt_pk_bf16_f32 v75,v22,v23
buffer_store_dwordx4 v[72:75],v3,s[36:39],s73 offen offset:0 sc0 sc1
v_add_f32 v64,v16,v17
v_mul_f32 v65,v16,v16
v_fma_f32 v65,v17,v17,v65
buffer_load_dwordx4 v1,s[36:39],s67 offen offset:3072 sc1 lds
v_add_f32 v64,v64,v18
v_fma_f32 v65,v18,v18,v65
v_add_f32 v64,v64,v19
v_fma_f32 v65,v19,v19,v65
v_add_f32 v64,v64,v20
v_fma_f32 v65,v20,v20,v65
v_add_f32 v64,v64,v21
v_fma_f32 v65,v21,v21,v65
s_lshr_b32 s72,s67,6
s_lshr_b32 s71,s62,2
s_add_u32 m0,s71,s88
s_nop 0
buffer_load_dwordx4 v10,s[36:39],s67 offen offset:0 sc1 lds
v_add_f32 v64,v64,v22
v_fma_f32 v65,v22,v22,v65
v_add_f32 v64,v64,v23
v_fma_f32 v65,v23,v23,v65
s_nop 1
v_permlane16_swap_b32 v64,v65
v_add_f32 v64,v64,v65
v_mov_b32 v65,v64
s_nop 1
v_permlane32_swap_b32 v64,v65
buffer_load_dword v11,s[36:39],s72 offen offset:1024 sc1 lds
v_add_f32 v64,v64,v65
ds_write_b32 v13,v64 offset:2048
s_waitcnt lgkmcnt(0)
s_barrier
ds_read_b128 v[76:79],v14 offset:2048
s_waitcnt lgkmcnt(0)
v_add_f32 v76,v76,v77
v_add_f32 v78,v78,v79
v_add_f32 v76,v76,v78
buffer_store_dword v76,v12,s[36:39],s85 offen offset:0 sc0 sc1
s_waitcnt vmcnt(8)
s_barrier
ds_read_b32 v15,v7 offset:8192
ds_read_b128 v[32:35],v2 offset:32768
ds_read_b128 v[36:39],v2 offset:33792
ds_read_b128 v[40:43],v2 offset:34816
ds_read_b128 v[44:47],v2 offset:35840
ds_read_b128 v[48:51],v2 offset:36864
ds_read_b128 v[52:55],v2 offset:37888
ds_read_b128 v[56:59],v2 offset:38912
s_lshr_b32 s70,s47,5
buffer_store_dword v5,v4,s[36:39],s70 offen offset:0 sc0 sc1
s_waitcnt lgkmcnt(7)
v_cmp_eq_u32 vcc,s60,v15
s_nop 0
s_cmp_eq_u32 vcc_hi,-1
s_cselect_b32 s67,s50,s59
s_xor_b32 s62,s63,65536
s_cmp_ge_u32 s67,s59
s_cselect_b32 s68,s50,s51
s_lshr_b32 s68,s68,5
s_add_u32 m0,s78,256
s_nop 0
buffer_load_dword v6,s[36:39],s68 offen offset:0 sc1 lds
s_mov_b32 m0,s62
s_nop 0
buffer_load_dwordx4 v0,s[36:39],s67 offen offset:0 sc1 lds
buffer_load_dwordx4 v0,s[36:39],s67 offen offset:1024 sc1 lds
buffer_load_dwordx4 v0,s[36:39],s67 offen offset:2048 sc1 lds
buffer_load_dwordx4 v0,s[36:39],s67 offen offset:3072 sc1 lds
s_mov_b32 s47,s48
s_mov_b32 s48,s59
s_lshr_b32 s70,s59,5
buffer_store_dword v5,v4,s[36:39],s70 offen offset:0 sc0 sc1)"
R"(
s_lshr_b32 s70,s59,5
buffer_store_dword v5,v4,s[36:39],s70 offen offset:0 sc0 sc1
s_waitcnt lgkmcnt(3)
s_setpc_b64 s[92:93]
Lg2_done1_%=:
s_waitcnt vmcnt(0)
s_barrier
s_lshr_b32 s70,s47,5
buffer_store_dword v5,v4,s[36:39],s70 offen offset:0 sc0 sc1
s_lshr_b32 s70,s48,5
buffer_store_dword v5,v4,s[36:39],s70 offen offset:0 sc0 sc1
s_branch Lg2_done_%=
Lg2_done_%=:
s_waitcnt vmcnt(0) lgkmcnt(0)
s_barrier
Lg2_end_%=:
s_mov_b32 %0,s60
Lcode_g12_e:)"
  : "=&s"(epOut) : "s"(ws), "s"(wave), "s"(sStep), "s"(sX1), "s"(sX2), "s"(sU0), "s"(sWw), "s"(sBuf), "v"(vSrc), "v"(vSrc2), "v"(vA), "v"(vOut), "v"(vFlag), "v"(vPollOff), "v"(vSnapRd), "v"(vX), "v"(vX2), "v"(vAuxS), "v"(vAuxS2), "v"(vOut2), "v"(vStW), "v"(vStR), "v"(vWd0), "v"(vWd1), "v"(vWr0), "v"(vWr1), "v"(vWr2), "s"(w12) : "v0", "v1", "v2", "v3", "v4", "v5", "v6", "v7", "v8", "v9", "v10", "v11", "v12", "v13", "v14", "v15", "v16", "v17", "v18", "v19", "v20", "v21", "v22", "v23", "v24", "v25", "v26", "v27", "v28", "v29", "v30", "v31", "v32", "v33", "v34", "v35", "v36", "v37", "v38", "v39", "v40", "v41", "v42", "v43", "v44", "v45", "v46", "v47", "v48", "v49", "v50", "v51", "v52", "v53", "v54", "v55", "v56", "v57", "v58", "v59", "v60", "v61", "v62", "v63", "v64", "v65", "v66", "v67", "v68", "v69", "v70", "v71", "v72", "v73", "v74", "v75", "v76", "v77", "v78", "v79", "v80", "v81", "v82", "v83", "v84", "v85", "v86", "v87", "v88", "v89", "v90", "v91", "v92", "v93", "v94", "v95", "v96", "v97", "v98", "v99", "v100", "v101", "v102", "v103", "v104", "v105", "v106", "v107", "v108", "v109", "v110", "v111", "v112", "v113", "v114", "v115", "v116", "v117", "v118", "v119", "v120", "v121", "v122", "v123", "v124", "v125", "v126", "v127", "v128", "v129", "v130", "v131", "v132", "v133", "v134", "v135", "v136", "v137", "v138", "v139", "v140", "v141", "v142", "v143", "v144", "v145", "v146", "v147", "v148", "v149", "v150", "v151", "v152", "v153", "v154", "v155", "v156", "v157", "v158", "v159", "v160", "v161", "v162", "v163", "v164", "v165", "v166", "v167", "v168", "v169", "v170", "v171", "v172", "v173", "v174", "v175", "v176", "v177", "v178", "v179", "v180", "v181", "v182", "v183", "v184", "v185", "v186", "v187", "v188", "v189", "v190", "v191", "v192", "v193", "v194", "v195", "v196", "v197", "v198", "v199", "v200", "v201", "v202", "v203", "v204", "v205", "v206", "v207", "v208", "v209", "v210", "v211", "v212", "v213", "v214", "v215", "v216", "v217", "v218", "v219", "v220", "v221", "v222", "v223", "v224", "v225", "v226", "v227", "v228", "v229", "v230", "v231", "v255", "a0", "a1", "a2", "a3", "a4", "a5", "a6", "a7", "a8", "a9", "a10", "a11", "a12", "a13", "a14", "a15", "a16", "a17", "a18", "a19", "a20", "a21", "a22", "a23", "a24", "a25", "a26", "a27", "a28", "a29", "a30", "a31", "a32", "a33", "a34", "a35", "a36", "a37", "a38", "a39", "a40", "a41", "a42", "a43", "a44", "a45", "a46", "a47", "a48", "a49", "a50", "a51", "a52", "a53", "a54", "a55", "a56", "a57", "a58", "a59", "a60", "a61", "a62", "a63", "a64", "a65", "a66", "a67", "a68", "a69", "a70", "a71", "a72", "a73", "a74", "a75", "a76", "a77", "a78", "a79", "a80", "a81", "a82", "a83", "a84", "a85", "a86", "a87", "a88", "a89", "a90", "a91", "a92", "a93", "a94", "a95", "a96", "a97", "a98", "a99", "a100", "a101", "a102", "a103", "a104", "a105", "a106", "a107", "a108", "a109", "a110", "a111", "a112", "a113", "a114", "a115", "a116", "a117", "a118", "a119", "a120", "a121", "a122", "a123", "a124", "a125", "a126", "a127", "a128", "a129", "a130", "a131", "a132", "a133", "a134", "a135", "a136", "a137", "a138", "a139", "a140", "a141", "a142", "a143", "a144", "a145", "a146", "a147", "a148", "a149", "a150", "a151", "a152", "a153", "a154", "a155", "a156", "a157", "a158", "a159", "a160", "a161", "a162", "a163", "a164", "a165", "a166", "a167", "a168", "a169", "a170", "a171", "a172", "a173", "a174", "a175", "a176", "a177", "a178", "a179", "a180", "a181", "a182", "a183", "a184", "a185", "a186", "a187", "a188", "a189", "a190", "a191", "a192", "a193", "a194", "a195", "a196", "a197", "a198", "a199", "a200", "a201", "a202", "a203", "a204", "a205", "a206", "a207", "a208", "a209", "a210", "a211", "a212", "a213", "a214", "a215", "a216", "a217", "a218", "a219", "a220", "a221", "a222", "a223", "a224", "a225", "a226", "a227", "a228", "a229", "a230", "a231", "a232", "a233", "a234", "a235", "a236", "a237", "a238", "a239", "a240", "a241", "a242", "a243", "a244", "a245", "a246", "a247", "a248", "a249", "a250", "a251", "a252", "a253", "a254", "a255", "s36", "s37", "s38", "s39", "s40", "s41", "s42", "s43", "s44", "s45", "s46", "s47", "s48", "s49", "s50", "s51", "s52", "s53", "s54", "s55", "s56", "s57", "s58", "s59", "s60", "s61", "s62", "s63", "s64", "s65", "s66", "s67", "s68", "s69", "s70", "s71", "s72", "s73", "s74", "s75", "s76", "s77", "s78", "s79", "s80", "s81", "s82", "s83", "s84", "s85", "s86", "s87", "s88", "s89", "s90", "s91", "s92", "s93", "s94", "s95", "s96", "s97", "s98", "s99", "vcc", "scc", "memory");
    ep = epOut;
   }
    if (kind == 6)
    {
      LANE_ID(lr);
      wait_flags(ws + OFF_FXP + 7 * 16384 + wave * 1024, lr, 8, ep);
      const int r8 = lr & 7, seg = lr >> 3;
      const unsigned sEpR = __builtin_amdgcn_readfirstlane(ep), sK = slice, sStep = NRW, sK1 = RT1 + slice, sRq = OFF_FXP + LB * 2048 + wave * 128, sDbg = sDbg0 + (kind == 6 ? 0 : 1024);
      const unsigned vRF = (wave * 8 + (seg >> 2)) * 1024 + ((seg & 3) * 16 + r8) * 16;
      const unsigned vRwr = sLds + (seg == 0 ? r8 * 64 + wave * 4 : 512 + lr * 4), vRrd = sLds + r8 * 64;
      const unsigned vRP = sLds + ((lr >> 4) == ((lr & 15) >> 2) ? r8 * 64 + 16 + wave * 8 + (seg & 1) * 4 : 768 + lr * 4);
      const unsigned vRS = (wave == 0 && seg == 0) ? r8 * 16 : 0x80000000u, vFlag = lr == 0 ? 0u : 0x80000000u, vL4 = (lr & 15) * 128, vRL = wave * 4096 + lr * 16;
      asm volatile(R"(
Lcode_r_b:
s_mov_b64 s[36:37],%0
s_mov_b32 s38,122727552
s_mov_b32 s39,0x00020000
s_mov_b32 s54,%1
s_mov_b32 s50,%2
s_mov_b32 s92,%3
s_mov_b32 s48,%4
v_mov_b32 v0,%5
v_mov_b32 v1,%6
v_mov_b32 v2,%7
v_mov_b32 v3,%8
v_mov_b32 v4,%9
v_mov_b32 v5,%10
v_mov_b32 v6,%11
v_mov_b32 v11,%12
v_lshrrev_b32 v10,1,v4
v_mov_b32 v7,s48
v_mbcnt_lo_u32_b32 v9,-1,0
v_mbcnt_hi_u32_b32 v9,-1,v9
v_lshlrev_b32 v9,4,v9
s_mov_b32 s74,0
s_mov_b32 s96,0
s_mov_b32 s60,536870912
s_mov_b32 s46,0xaaaaaaaa
s_mov_b32 s47,0xaaaaaaaa
s_mov_b32 s56,0xcccccccc
s_mov_b32 s57,0xcccccccc
s_mov_b32 s76,0x3a800000
s_mov_b32 s77,0x3727c5ac
s_mov_b32 s78,0xc0135761
s_mov_b32 s79,0x3d372713
v_mov_b32 v192,0x3f803f80
v_mov_b32 v193,0x3f803f80
v_mov_b32 v194,0x3f803f80
v_mov_b32 v195,0x3f803f80
s_lshr_b32 s63,s54,1
s_and_b32 s64,s54,1
s_lshl_b32 s64,s64,7
s_lshl_b32 s87,s54,14
s_lshl_b32 s58,s63,15
s_add_u32 s58,s58,s64
s_add_u32 s58,s58,16908288
s_lshl_b32 s59,s63,9
s_add_u32 s59,s59,s64
s_add_u32 s59,s59,118556672
s_lshl_b32 s49,s54,9
s_add_u32 s49,s49,s92
s_lshl_b32 s91,s63,11
s_add_u32 s91,s91,121405440
s_lshl_b32 s69,s50,10
s_add_u32 s69,s69,s91
v_mov_b32 v20,0
v_mov_b32 v21,0
v_mov_b32 v22,0
v_mov_b32 v23,0
v_mov_b32 v24,0
v_mov_b32 v25,0
v_mov_b32 v26,0
v_mov_b32 v27,0
v_mov_b32 v16,0x3f80
v_mov_b32 v17,0x3f800000
v_cndmask_b32 v16,v16,v17,s[46:47]
s_mov_b32 s98,0x00300003
s_mov_b32 s99,0x30000300
v_cndmask_b32 v20,v20,v16,s[98:99]
s_lshl_b64 s[98:99],s[98:99],2
v_cndmask_b32 v21,v21,v16,s[98:99]
v_mov_b32 v26,v20
v_mov_b32 v27,v21
v_readfirstlane_b32 s52,v11
s_add_u32 s51,s87,8388608
s_mov_b32 s66,4096
s_nop 3
Lr_yp_%=:
s_add_u32 m0,s66,s52
s_cmp_ge_u32 s51,12582912
s_cbranch_scc1 Lr_ys1_%=
buffer_load_dwordx4 v11,s[36:39],s51 offen offset:0 sc1 lds
buffer_load_dwordx4 v11,s[36:39],s51 offen offset:1024 sc1 lds
buffer_load_dwordx4 v11,s[36:39],s51 offen offset:2048 sc1 lds
buffer_load_dwordx4 v11,s[36:39],s51 offen offset:3072 sc1 lds
s_lshl_b32 s63,s50,14
s_add_u32 s51,s51,s63
Lr_ys1_%=:
s_add_u32 s66,s66,16384
s_cmp_lt_u32 s66,151552
s_cbranch_scc1 Lr_yp_%=
s_mov_b32 s66,4096
v_add_u32 v18,4096,v11
Lr_bp9_%=:
buffer_load_dword v8,v6,s[36:39],s91 offen offset:0 sc1
s_waitcnt vmcnt(0)
v_cmp_eq_u32 vcc,s48,v8
s_nop 0
s_cmp_eq_u64 vcc,exec
s_cselect_b32 s63,1,s96
s_cmp_lg_u32 s63,0
s_cbranch_scc1 Lr_bq9_%=
s_add_u32 s74,s74,1
s_cmp_ge_u32 s74,20000
s_cselect_b32 s96,1,0
s_sleep 4
s_branch Lr_bp9_%=
Lr_bq9_%=:
s_add_u32 s63,s87,71303168
buffer_load_dwordx4 a[0:3],v11,s[36:39],s63 offen offset:0 sc1
buffer_load_dwordx4 a[4:7],v11,s[36:39],s63 offen offset:1024 sc1
buffer_load_dwordx4 a[8:11],v11,s[36:39],s63 offen offset:2048 sc1
buffer_load_dwordx4 a[12:15],v11,s[36:39],s63 offen offset:3072 sc1
s_add_u32 s63,s87,89128960
buffer_load_dwordx4 a[16:19],v11,s[36:39],s63 offen offset:0 sc1
buffer_load_dwordx4 a[20:23],v11,s[36:39],s63 offen offset:1024 sc1
buffer_load_dwordx4 a[24:27],v11,s[36:39],s63 offen offset:2048 sc1
buffer_load_dwordx4 a[28:31],v11,s[36:39],s63 offen offset:3072 sc1
s_add_u32 s63,s87,99614720
buffer_load_dwordx4 a[32:35],v11,s[36:39],s63 offen offset:0 sc1
buffer_load_dwordx4 a[36:39],v11,s[36:39],s63 offen offset:1024 sc1
buffer_load_dwordx4 a[40:43],v11,s[36:39],s63 offen offset:2048 sc1
buffer_load_dwordx4 a[44:47],v11,s[36:39],s63 offen offset:3072 sc1
s_add_u32 s63,s87,109051904
buffer_load_dwordx4 a[48:51],v11,s[36:39],s63 offen offset:0 sc1
buffer_load_dwordx4 a[52:55],v11,s[36:39],s63 offen offset:1024 sc1
buffer_load_dwordx4 a[56:59],v11,s[36:39],s63 offen offset:2048 sc1
buffer_load_dwordx4 a[60:63],v11,s[36:39],s63 offen offset:3072 sc1
buffer_load_dword v8,v6,s[36:39],s69 offen offset:0 sc1
s_waitcnt vmcnt(0)
Lr_it0_%=:
s_mov_b32 s88,s58
s_mov_b32 s89,s59
s_mov_b32 s90,s49
s_add_u32 s54,s54,s50
s_lshr_b32 s63,s54,1
s_and_b32 s64,s54,1
s_lshl_b32 s64,s64,7
s_lshl_b32 s87,s54,14
s_lshl_b32 s58,s63,15
s_add_u32 s58,s58,s64
s_add_u32 s58,s58,16908288
s_lshl_b32 s59,s63,9
s_add_u32 s59,s59,s64
s_add_u32 s59,s59,118556672
s_lshl_b32 s49,s54,9
s_add_u32 s49,s49,s92
s_lshl_b32 s91,s63,11
s_add_u32 s91,s91,121405440
s_lshl_b32 s69,s50,10
s_add_u32 s69,s69,s91
v_cmp_eq_u32 vcc,s48,v8
s_nop 0
s_cmp_eq_u64 vcc,exec
s_cselect_b32 s63,s54,256
s_cmp_lt_u32 s63,256
s_cbranch_scc1 Lr_ta0_%=
ds_read_b128 v[32:35],v18 offset:0
ds_read_b128 v[36:39],v18 offset:1024
ds_read_b128 v[40:43],v18 offset:2048
ds_read_b128 v[44:47],v18 offset:3072
v_mfma_f32_16x16x32_bf16 v[128:131],v[20:23],a[0:3],0
v_mfma_f32_16x16x32_bf16 v[132:135],v[24:27],a[0:3],0
v_mfma_f32_16x16x32_bf16 v[128:131],v[20:23],a[16:19],v[128:131]
v_mfma_f32_16x16x32_bf16 v[132:135],v[24:27],a[16:19],v[132:135]
v_mfma_f32_16x16x32_bf16 v[128:131],v[20:23],a[32:35],v[128:131]
v_mfma_f32_16x16x32_bf16 v[132:135],v[24:27],a[32:35],v[132:135]
v_mfma_f32_16x16x32_bf16 v[128:131],v[20:23],a[48:51],v[128:131]
v_mfma_f32_16x16x32_bf16 v[132:135],v[24:27],a[48:51],v[132:135]
v_mfma_f32_16x16x32_bf16 v[136:139],v[20:23],a[4:7],0
v_mfma_f32_16x16x32_bf16 v[140:143],v[24:27],a[4:7],0
v_mfma_f32_16x16x32_bf16 v[136:139],v[20:23],a[20:23],v[136:139]
v_mfma_f32_16x16x32_bf16 v[140:143],v[24:27],a[20:23],v[140:143]
v_mfma_f32_16x16x32_bf16 v[136:139],v[20:23],a[36:39],v[136:139]
s_nop 1
v_cvt_pk_bf16_f32 v208,v128,v129
v_cvt_pk_bf16_f32 v209,v130,v131
v_mfma_f32_16x16x32_bf16 v[140:143],v[24:27],a[36:39],v[140:143]
v_cvt_pk_bf16_f32 v210,v132,v133
v_cvt_pk_bf16_f32 v211,v134,v135
v_mfma_f32_16x16x32_bf16 v[136:139],v[20:23],a[52:55],v[136:139]
v_mfma_f32_16x16x32_bf16 v[140:143],v[24:27],a[52:55],v[140:143]
v_mfma_f32_16x16x32_bf16 v[144:147],v[20:23],a[8:11],0
v_mfma_f32_16x16x32_bf16 v[148:151],v[24:27],a[8:11],0
v_mfma_f32_16x16x32_bf16 v[144:147],v[20:23],a[24:27],v[144:147]
v_mfma_f32_16x16x32_bf16 v[148:151],v[24:27],a[24:27],v[148:151]
v_mfma_f32_16x16x32_bf16 v[144:147],v[20:23],a[40:43],v[144:147]
s_nop 1
v_cvt_pk_bf16_f32 v212,v136,v137
v_cvt_pk_bf16_f32 v213,v138,v139
v_mfma_f32_16x16x32_bf16 v[148:151],v[24:27],a[40:43],v[148:151]
v_cvt_pk_bf16_f32 v214,v140,v141
v_cvt_pk_bf16_f32 v215,v142,v143
v_mfma_f32_16x16x32_bf16 v[144:147],v[20:23],a[56:59],v[144:147]
v_mfma_f32_16x16x32_bf16 v[148:151],v[24:27],a[56:59],v[148:151]
v_mfma_f32_16x16x32_bf16 v[28:31],v[192:195],v[208:211],0
v_mfma_f32_16x16x32_bf16 v[204:207],v[208:211],v[208:211],0
v_mfma_f32_16x16x32_bf16 v[152:155],v[20:23],a[12:15],0
v_mfma_f32_16x16x32_bf16 v[156:159],v[24:27],a[12:15],0
v_mfma_f32_16x16x32_bf16 v[152:155],v[20:23],a[28:31],v[152:155]
v_mfma_f32_16x16x32_bf16 v[156:159],v[24:27],a[28:31],v[156:159]
v_mfma_f32_16x16x32_bf16 v[152:155],v[20:23],a[44:47],v[152:155]
v_cvt_pk_bf16_f32 v216,v144,v145)"
R"(
v_cvt_pk_bf16_f32 v217,v146,v147
v_mfma_f32_16x16x32_bf16 v[156:159],v[24:27],a[44:47],v[156:159]
v_cvt_pk_bf16_f32 v218,v148,v149
v_cvt_pk_bf16_f32 v219,v150,v151
v_mfma_f32_16x16x32_bf16 v[152:155],v[20:23],a[60:63],v[152:155]
v_mfma_f32_16x16x32_bf16 v[156:159],v[24:27],a[60:63],v[156:159]
v_mfma_f32_16x16x32_bf16 v[28:31],v[192:195],v[212:215],v[28:31]
v_mfma_f32_16x16x32_bf16 v[204:207],v[212:215],v[212:215],v[204:207]
v_mfma_f32_16x16x32_bf16 v[28:31],v[192:195],v[216:219],v[28:31]
v_mfma_f32_16x16x32_bf16 v[204:207],v[216:219],v[216:219],v[204:207]
s_waitcnt lgkmcnt(0)
v_mfma_f32_16x16x32_bf16 v[160:163],v[20:23],v[32:35],0
s_nop 0
v_cvt_pk_bf16_f32 v220,v152,v153
v_cvt_pk_bf16_f32 v221,v154,v155
v_mfma_f32_16x16x32_bf16 v[164:167],v[24:27],v[32:35],0
v_cvt_pk_bf16_f32 v222,v156,v157
v_cvt_pk_bf16_f32 v223,v158,v159
v_mfma_f32_16x16x32_bf16 v[168:171],v[20:23],v[36:39],0
s_nop 0
v_mfma_f32_16x16x32_bf16 v[28:31],v[192:195],v[220:223],v[28:31]
v_mfma_f32_16x16x32_bf16 v[204:207],v[220:223],v[220:223],v[204:207]
v_mfma_f32_16x16x32_bf16 v[172:175],v[24:27],v[36:39],0
v_mfma_f32_16x16x32_bf16 v[176:179],v[20:23],v[40:43],0
v_mfma_f32_16x16x32_bf16 v[180:183],v[24:27],v[40:43],0
s_nop 3
v_add_f32_dpp v224,v28,v28 row_ror:8 row_mask:0xf bank_mask:0xf
v_cndmask_b32 v225,v204,v205,s[46:47]
v_cndmask_b32 v226,v206,v207,s[46:47]
v_cndmask_b32 v225,v225,v226,s[56:57]
v_mfma_f32_16x16x32_bf16 v[184:187],v[20:23],v[44:47],0
ds_write_b32 v2,v224 offset:0
ds_write_b32 v0,v225 offset:0
v_mfma_f32_16x16x32_bf16 v[188:191],v[24:27],v[44:47],0
s_waitcnt lgkmcnt(0)
s_barrier
ds_read_b128 v[196:199],v3 offset:0
ds_read_b128 v[200:203],v3 offset:16
ds_read_b128 v[204:207],v3 offset:32
s_waitcnt lgkmcnt(0)
v_add_f32 v196,v196,v197
v_add_f32 v198,v198,v199
v_add_f32 v200,v200,v204
v_add_f32 v201,v201,v205
v_add_f32 v202,v202,v206
v_add_f32 v203,v203,v207
v_add_f32 v196,v196,v198
v_add_f32 v200,v200,v201
v_add_f32 v202,v202,v203
v_mul_f32 v196,s76,v196
v_add_f32 v197,v200,v202
v_mul_f32 v197,s76,v197
v_fma_f32 v197,-v196,v196,v197
v_max_f32 v197,0,v197
v_add_f32 v197,s77,v197
v_rsq_f32 v228,v197
s_nop 0
v_mul_f32_e64 v230,-v196,v228
v_mov_b32 v229,v228
v_mov_b32 v231,v230
v_pk_fma_f32 v[196:197],v[128:129],v[228:229],v[230:231]
v_pk_fma_f32 v[198:199],v[130:131],v[228:229],v[230:231]
v_pk_fma_f32 v[200:201],v[132:133],v[228:229],v[230:231]
v_pk_fma_f32 v[202:203],v[134:135],v[228:229],v[230:231]
v_pk_fma_f32 v[128:129],v[196:197],v[160:161],v[128:129]
v_pk_fma_f32 v[130:131],v[198:199],v[162:163],v[130:131]
v_pk_fma_f32 v[132:133],v[200:201],v[164:165],v[132:133]
v_pk_fma_f32 v[134:135],v[202:203],v[166:167],v[134:135]
v_pk_fma_f32 v[196:197],v[136:137],v[228:229],v[230:231]
v_pk_fma_f32 v[198:199],v[138:139],v[228:229],v[230:231]
v_pk_fma_f32 v[200:201],v[140:141],v[228:229],v[230:231]
v_pk_fma_f32 v[202:203],v[142:143],v[228:229],v[230:231]
v_pk_fma_f32 v[136:137],v[196:197],v[168:169],v[136:137]
v_pk_fma_f32 v[138:139],v[198:199],v[170:171],v[138:139]
v_pk_fma_f32 v[140:141],v[200:201],v[172:173],v[140:141]
v_pk_fma_f32 v[142:143],v[202:203],v[174:175],v[142:143]
v_pk_fma_f32 v[196:197],v[144:145],v[228:229],v[230:231]
v_pk_fma_f32 v[198:199],v[146:147],v[228:229],v[230:231]
v_pk_fma_f32 v[200:201],v[148:149],v[228:229],v[230:231]
v_pk_fma_f32 v[202:203],v[150:151],v[228:229],v[230:231]
v_pk_fma_f32 v[144:145],v[196:197],v[176:177],v[144:145]
v_pk_fma_f32 v[146:147],v[198:199],v[178:179],v[146:147]
v_pk_fma_f32 v[148:149],v[200:201],v[180:181],v[148:149]
v_pk_fma_f32 v[150:151],v[202:203],v[182:183],v[150:151]
v_pk_fma_f32 v[196:197],v[152:153],v[228:229],v[230:231]
v_pk_fma_f32 v[198:199],v[154:155],v[228:229],v[230:231]
v_pk_fma_f32 v[200:201],v[156:157],v[228:229],v[230:231]
v_pk_fma_f32 v[202:203],v[158:159],v[228:229],v[230:231]
v_pk_fma_f32 v[152:153],v[196:197],v[184:185],v[152:153]
v_pk_fma_f32 v[154:155],v[198:199],v[186:187],v[154:155]
v_pk_fma_f32 v[156:157],v[200:201],v[188:189],v[156:157]
v_pk_fma_f32 v[158:159],v[202:203],v[190:191],v[158:159]
v_cvt_pk_bf16_f32 v208,v128,v129
v_cvt_pk_bf16_f32 v209,v130,v131
v_cvt_pk_bf16_f32 v210,v132,v133
v_cvt_pk_bf16_f32 v211,v134,v135
v_cvt_pk_bf16_f32 v212,v136,v137
v_cvt_pk_bf16_f32 v213,v138,v139
v_cvt_pk_bf16_f32 v214,v140,v141
v_cvt_pk_bf16_f32 v215,v142,v143
v_mfma_f32_16x16x32_bf16 v[28:31],v[192:195],v[208:211],0
v_cvt_pk_bf16_f32 v216,v144,v145
v_cvt_pk_bf16_f32 v217,v146,v147
v_mfma_f32_16x16x32_bf16 v[204:207],v[208:211],v[208:211],0
v_cvt_pk_bf16_f32 v218,v148,v149
v_cvt_pk_bf16_f32 v219,v150,v151
v_mfma_f32_16x16x32_bf16 v[28:31],v[192:195],v[212:215],v[28:31]
v_cvt_pk_bf16_f32 v220,v152,v153
v_cvt_pk_bf16_f32 v221,v154,v155
v_mfma_f32_16x16x32_bf16 v[204:207],v[212:215],v[212:215],v[204:207]
v_cvt_pk_bf16_f32 v222,v156,v157
v_cvt_pk_bf16_f32 v223,v158,v159
v_mfma_f32_16x16x32_bf16 v[28:31],v[192:195],v[216:219],v[28:31]
v_mfma_f32_16x16x32_bf16 v[204:207],v[216:219],v[216:219],v[204:207]
v_mfma_f32_16x16x32_bf16 v[28:31],v[192:195],v[220:223],v[28:31]
v_mfma_f32_16x16x32_bf16 v[204:207],v[220:223],v[220:223],v[204:207]
s_waitcnt vmcnt(0)
buffer_store_dword v7,v5,s[36:39],s60 offen offset:0 sc0 sc1
s_mov_b32 s60,536870912
buffer_load_dword v8,v6,s[36:39],s91 offen offset:0 sc1
buffer_store_dwordx4 v[208:211],v1,s[36:39],s88 offen offset:0 sc0 sc1
buffer_store_dwordx4 v[212:215],v1,s[36:39],s88 offen offset:2048 sc0 sc1
s_add_u32 s88,s88,4096
v_add_f32_dpp v224,v28,v28 row_ror:8 row_mask:0xf bank_mask:0xf
v_cndmask_b32 v225,v204,v205,s[46:47]
v_cndmask_b32 v226,v206,v207,s[46:47]
v_cndmask_b32 v225,v225,v226,s[56:57]
s_and_b32 s63,s89,128
ds_write_b32 v2,v224 offset:1024
ds_write_b32 v0,v225 offset:1024
buffer_store_dwordx4 v[216:219],v1,s[36:39],s88 offen offset:0 sc0 sc1
buffer_store_dwordx4 v[220:223],v1,s[36:39],s88 offen offset:2048 sc0 sc1
s_waitcnt lgkmcnt(0)
s_barrier
ds_read_b128 v[196:199],v3 offset:1024
ds_read_b128 v[200:203],v3 offset:1040
ds_read_b128 v[204:207],v3 offset:1056
s_waitcnt lgkmcnt(0)
v_add_f32 v196,v196,v197
v_add_f32 v198,v198,v199
v_add_f32 v200,v200,v204
v_add_f32 v201,v201,v205
v_add_f32 v202,v202,v206
v_add_f32 v203,v203,v207
v_add_f32 v196,v196,v198
v_add_f32 v200,v200,v201
v_add_f32 v202,v202,v203
v_mul_f32 v196,s76,v196
v_add_f32 v197,v200,v202
v_mul_f32 v197,s76,v197
v_fma_f32 v197,-v196,v196,v197
v_max_f32 v197,0,v197
v_add_f32 v198,s77,v197
v_fma_f32 v199,s77,v198,v197
v_rsq_f32 v205,v198
v_rsq_f32 v198,v199
v_mov_b32 v200,v196
v_mul_f32_e64 v204,-v196,v205
v_mul_f32 v203,v199,v198
v_mul_f32 v201,s78,v198
v_mul_f32 v199,v198,v198
v_mul_f32 v202,s79,v199
v_mul_f32 v202,v202,v201
s_lshr_b32 s63,s63,1
s_sub_u32 s63,s89,s63
buffer_store_dwordx4 v[200:203],v4,s[36:39],s89 offen offset:0 sc0 sc1
buffer_store_dwordx2 v[204:205],v10,s[36:39],s63 offen offset:256 sc0 sc1
s_add_u32 m0,s66,s52
s_cmp_ge_u32 s51,12582912
s_cbranch_scc1 Lr_ys2_%=
buffer_load_dwordx4 v11,s[36:39],s51 offen offset:0 sc1 lds
buffer_load_dwordx4 v11,s[36:39],s51 offen offset:1024 sc1 lds
buffer_load_dwordx4 v11,s[36:39],s51 offen offset:2048 sc1 lds
buffer_load_dwordx4 v11,s[36:39],s51 offen offset:3072 sc1 lds
s_lshl_b32 s63,s50,14
s_add_u32 s51,s51,s63
Lr_ys2_%=:
s_add_u32 s66,s66,16384
s_cmp_eq_u32 s66,151552
s_cselect_b32 s66,4096,s66
v_add_u32 v18,s66,v11
s_mov_b32 s60,s90
s_waitcnt vmcnt(6)
v_cmp_eq_u32 vcc,s48,v8
s_nop 0
s_cmp_eq_u64 vcc,exec
s_cselect_b32 s63,s54,256
s_cmp_lt_u32 s63,256
s_cbranch_scc1 Lr_tc0_%=
buffer_load_dword v8,v6,s[36:39],s91 offen offset:0 sc1
s_waitcnt vmcnt(0)
buffer_store_dword v7,v5,s[36:39],s60 offen offset:0 sc0 sc1
s_mov_b32 s60,536870912
v_cmp_eq_u32 vcc,s48,v8
s_nop 0
s_cmp_eq_u64 vcc,exec
s_cselect_b32 s63,s54,256
s_cmp_lt_u32 s63,256
s_cbranch_scc1 Lr_bq0_%=
s_cmp_ge_u32 s54,256)"
R"(
s_cbranch_scc1 Lr_done_%=
Lr_bp0_%=:
buffer_load_dword v8,v6,s[36:39],s91 offen offset:0 sc1
s_waitcnt vmcnt(0)
v_cmp_eq_u32 vcc,s48,v8
s_nop 0
s_cmp_eq_u64 vcc,exec
s_cselect_b32 s63,1,s96
s_cmp_lg_u32 s63,0
s_cbranch_scc1 Lr_bq0_%=
s_add_u32 s74,s74,1
s_cmp_ge_u32 s74,20000
s_cselect_b32 s96,1,0
s_sleep 4
s_branch Lr_bp0_%=
Lr_bq0_%=:
s_add_u32 s63,s87,71303168
buffer_load_dwordx4 a[64:67],v11,s[36:39],s63 offen offset:0 sc1
buffer_load_dwordx4 a[68:71],v11,s[36:39],s63 offen offset:1024 sc1
buffer_load_dwordx4 a[72:75],v11,s[36:39],s63 offen offset:2048 sc1
buffer_load_dwordx4 a[76:79],v11,s[36:39],s63 offen offset:3072 sc1
s_add_u32 s63,s87,89128960
buffer_load_dwordx4 a[80:83],v11,s[36:39],s63 offen offset:0 sc1
buffer_load_dwordx4 a[84:87],v11,s[36:39],s63 offen offset:1024 sc1
buffer_load_dwordx4 a[88:91],v11,s[36:39],s63 offen offset:2048 sc1
buffer_load_dwordx4 a[92:95],v11,s[36:39],s63 offen offset:3072 sc1
s_add_u32 s63,s87,99614720
buffer_load_dwordx4 a[96:99],v11,s[36:39],s63 offen offset:0 sc1
buffer_load_dwordx4 a[100:103],v11,s[36:39],s63 offen offset:1024 sc1
buffer_load_dwordx4 a[104:107],v11,s[36:39],s63 offen offset:2048 sc1
buffer_load_dwordx4 a[108:111],v11,s[36:39],s63 offen offset:3072 sc1
s_add_u32 s63,s87,109051904
buffer_load_dwordx4 a[112:115],v11,s[36:39],s63 offen offset:0 sc1
buffer_load_dwordx4 a[116:119],v11,s[36:39],s63 offen offset:1024 sc1
buffer_load_dwordx4 a[120:123],v11,s[36:39],s63 offen offset:2048 sc1
buffer_load_dwordx4 a[124:127],v11,s[36:39],s63 offen offset:3072 sc1
buffer_load_dword v8,v6,s[36:39],s69 offen offset:0 sc1
s_waitcnt vmcnt(0)
s_branch Lr_it1_%=
Lr_ta0_%=:
ds_read_b128 v[32:35],v18 offset:0
ds_read_b128 v[36:39],v18 offset:1024
ds_read_b128 v[40:43],v18 offset:2048
ds_read_b128 v[44:47],v18 offset:3072
v_mfma_f32_16x16x32_bf16 v[128:131],v[20:23],a[0:3],0
s_add_u32 s63,s87,71303168
v_mfma_f32_16x16x32_bf16 v[132:135],v[24:27],a[0:3],0
buffer_load_dwordx4 a[64:67],v11,s[36:39],s63 offen offset:0 sc1
v_mfma_f32_16x16x32_bf16 v[128:131],v[20:23],a[16:19],v[128:131]
v_mfma_f32_16x16x32_bf16 v[132:135],v[24:27],a[16:19],v[132:135]
buffer_load_dwordx4 a[68:71],v11,s[36:39],s63 offen offset:1024 sc1
v_mfma_f32_16x16x32_bf16 v[128:131],v[20:23],a[32:35],v[128:131]
v_mfma_f32_16x16x32_bf16 v[132:135],v[24:27],a[32:35],v[132:135]
v_mfma_f32_16x16x32_bf16 v[128:131],v[20:23],a[48:51],v[128:131]
v_mfma_f32_16x16x32_bf16 v[132:135],v[24:27],a[48:51],v[132:135]
buffer_load_dwordx4 a[72:75],v11,s[36:39],s63 offen offset:2048 sc1
v_mfma_f32_16x16x32_bf16 v[136:139],v[20:23],a[4:7],0
v_mfma_f32_16x16x32_bf16 v[140:143],v[24:27],a[4:7],0
v_mfma_f32_16x16x32_bf16 v[136:139],v[20:23],a[20:23],v[136:139]
v_mfma_f32_16x16x32_bf16 v[140:143],v[24:27],a[20:23],v[140:143]
buffer_load_dwordx4 a[76:79],v11,s[36:39],s63 offen offset:3072 sc1
v_mfma_f32_16x16x32_bf16 v[136:139],v[20:23],a[36:39],v[136:139]
v_cvt_pk_bf16_f32 v208,v128,v129
v_cvt_pk_bf16_f32 v209,v130,v131
v_mfma_f32_16x16x32_bf16 v[140:143],v[24:27],a[36:39],v[140:143]
v_cvt_pk_bf16_f32 v210,v132,v133
v_cvt_pk_bf16_f32 v211,v134,v135
v_mfma_f32_16x16x32_bf16 v[136:139],v[20:23],a[52:55],v[136:139]
s_add_u32 s63,s87,89128960
v_mfma_f32_16x16x32_bf16 v[140:143],v[24:27],a[52:55],v[140:143]
buffer_load_dwordx4 a[80:83],v11,s[36:39],s63 offen offset:0 sc1
v_mfma_f32_16x16x32_bf16 v[144:147],v[20:23],a[8:11],0
v_mfma_f32_16x16x32_bf16 v[148:151],v[24:27],a[8:11],0
v_mfma_f32_16x16x32_bf16 v[144:147],v[20:23],a[24:27],v[144:147]
v_mfma_f32_16x16x32_bf16 v[148:151],v[24:27],a[24:27],v[148:151]
buffer_load_dwordx4 a[84:87],v11,s[36:39],s63 offen offset:1024 sc1
v_mfma_f32_16x16x32_bf16 v[144:147],v[20:23],a[40:43],v[144:147]
v_cvt_pk_bf16_f32 v212,v136,v137
v_cvt_pk_bf16_f32 v213,v138,v139
v_mfma_f32_16x16x32_bf16 v[148:151],v[24:27],a[40:43],v[148:151]
v_cvt_pk_bf16_f32 v214,v140,v141
v_cvt_pk_bf16_f32 v215,v142,v143
v_mfma_f32_16x16x32_bf16 v[144:147],v[20:23],a[56:59],v[144:147]
v_mfma_f32_16x16x32_bf16 v[148:151],v[24:27],a[56:59],v[148:151]
buffer_load_dwordx4 a[88:91],v11,s[36:39],s63 offen offset:2048 sc1
v_mfma_f32_16x16x32_bf16 v[28:31],v[192:195],v[208:211],0
v_mfma_f32_16x16x32_bf16 v[204:207],v[208:211],v[208:211],0
v_mfma_f32_16x16x32_bf16 v[152:155],v[20:23],a[12:15],0
v_mfma_f32_16x16x32_bf16 v[156:159],v[24:27],a[12:15],0
buffer_load_dwordx4 a[92:95],v11,s[36:39],s63 offen offset:3072 sc1
v_mfma_f32_16x16x32_bf16 v[152:155],v[20:23],a[28:31],v[152:155]
s_add_u32 s63,s87,99614720
v_mfma_f32_16x16x32_bf16 v[156:159],v[24:27],a[28:31],v[156:159]
v_mfma_f32_16x16x32_bf16 v[152:155],v[20:23],a[44:47],v[152:155]
v_cvt_pk_bf16_f32 v216,v144,v145
v_cvt_pk_bf16_f32 v217,v146,v147
v_mfma_f32_16x16x32_bf16 v[156:159],v[24:27],a[44:47],v[156:159]
v_cvt_pk_bf16_f32 v218,v148,v149
v_cvt_pk_bf16_f32 v219,v150,v151
v_mfma_f32_16x16x32_bf16 v[152:155],v[20:23],a[60:63],v[152:155]
buffer_load_dwordx4 a[96:99],v11,s[36:39],s63 offen offset:0 sc1
v_mfma_f32_16x16x32_bf16 v[156:159],v[24:27],a[60:63],v[156:159]
v_mfma_f32_16x16x32_bf16 v[28:31],v[192:195],v[212:215],v[28:31]
v_mfma_f32_16x16x32_bf16 v[204:207],v[212:215],v[212:215],v[204:207]
buffer_load_dwordx4 a[100:103],v11,s[36:39],s63 offen offset:1024 sc1
v_mfma_f32_16x16x32_bf16 v[28:31],v[192:195],v[216:219],v[28:31]
v_mfma_f32_16x16x32_bf16 v[204:207],v[216:219],v[216:219],v[204:207]
s_waitcnt lgkmcnt(0)
v_mfma_f32_16x16x32_bf16 v[160:163],v[20:23],v[32:35],0
v_cvt_pk_bf16_f32 v220,v152,v153
v_cvt_pk_bf16_f32 v221,v154,v155
v_mfma_f32_16x16x32_bf16 v[164:167],v[24:27],v[32:35],0
v_cvt_pk_bf16_f32 v222,v156,v157
v_cvt_pk_bf16_f32 v223,v158,v159
v_mfma_f32_16x16x32_bf16 v[168:171],v[20:23],v[36:39],0
buffer_load_dwordx4 a[104:107],v11,s[36:39],s63 offen offset:2048 sc1
v_mfma_f32_16x16x32_bf16 v[28:31],v[192:195],v[220:223],v[28:31]
buffer_load_dwordx4 a[108:111],v11,s[36:39],s63 offen offset:3072 sc1
v_mfma_f32_16x16x32_bf16 v[204:207],v[220:223],v[220:223],v[204:207]
s_add_u32 s63,s87,109051904
v_mfma_f32_16x16x32_bf16 v[172:175],v[24:27],v[36:39],0
v_mfma_f32_16x16x32_bf16 v[176:179],v[20:23],v[40:43],0
buffer_load_dwordx4 a[112:115],v11,s[36:39],s63 offen offset:0 sc1
v_mfma_f32_16x16x32_bf16 v[180:183],v[24:27],v[40:43],0
s_nop 0
v_add_f32_dpp v224,v28,v28 row_ror:8 row_mask:0xf bank_mask:0xf
s_nop 0
v_cndmask_b32 v225,v204,v205,s[46:47]
v_cndmask_b32 v226,v206,v207,s[46:47]
v_cndmask_b32 v225,v225,v226,s[56:57]
v_mfma_f32_16x16x32_bf16 v[184:187],v[20:23],v[44:47],0
ds_write_b32 v2,v224 offset:0
ds_write_b32 v0,v225 offset:0
v_mfma_f32_16x16x32_bf16 v[188:191],v[24:27],v[44:47],0
s_waitcnt lgkmcnt(0)
buffer_load_dwordx4 a[116:119],v11,s[36:39],s63 offen offset:1024 sc1
s_barrier
buffer_load_dwordx4 a[120:123],v11,s[36:39],s63 offen offset:2048 sc1
ds_read_b128 v[196:199],v3 offset:0
buffer_load_dwordx4 a[124:127],v11,s[36:39],s63 offen offset:3072 sc1
ds_read_b128 v[200:203],v3 offset:16
ds_read_b128 v[204:207],v3 offset:32
s_waitcnt lgkmcnt(0)
buffer_load_dword v8,v6,s[36:39],s69 offen offset:0 sc1
v_add_f32 v196,v196,v197
v_add_f32 v198,v198,v199
v_add_f32 v200,v200,v204
v_add_f32 v201,v201,v205
v_add_f32 v202,v202,v206
v_add_f32 v203,v203,v207
v_add_f32 v196,v196,v198
v_add_f32 v200,v200,v201
v_add_f32 v202,v202,v203
v_mul_f32 v196,s76,v196
v_add_f32 v197,v200,v202
v_mul_f32 v197,s76,v197
v_fma_f32 v197,-v196,v196,v197
v_max_f32 v197,0,v197
v_add_f32 v197,s77,v197
v_rsq_f32 v228,v197
s_nop 0
v_mul_f32_e64 v230,-v196,v228
v_mov_b32 v229,v228
v_mov_b32 v231,v230
v_pk_fma_f32 v[196:197],v[128:129],v[228:229],v[230:231]
v_pk_fma_f32 v[198:199],v[130:131],v[228:229],v[230:231]
v_pk_fma_f32 v[200:201],v[132:133],v[228:229],v[230:231]
v_pk_fma_f32 v[202:203],v[134:135],v[228:229],v[230:231]
v_pk_fma_f32 v[128:129],v[196:197],v[160:161],v[128:129]
v_pk_fma_f32 v[130:131],v[198:199],v[162:163],v[130:131]
v_pk_fma_f32 v[132:133],v[200:201],v[164:165],v[132:133]
v_pk_fma_f32 v[134:135],v[202:203],v[166:167],v[134:135]
v_pk_fma_f32 v[196:197],v[136:137],v[228:229],v[230:231]
v_pk_fma_f32 v[198:199],v[138:139],v[228:229],v[230:231]
v_pk_fma_f32 v[200:201],v[140:141],v[228:229],v[230:231]
v_pk_fma_f32 v[202:203],v[142:143],v[228:229],v[230:231]
v_pk_fma_f32 v[136:137],v[196:197],v[168:169],v[136:137]
v_pk_fma_f32 v[138:139],v[198:199],v[170:171],v[138:139]
v_pk_fma_f32 v[140:141],v[200:201],v[172:173],v[140:141]
v_pk_fma_f32 v[142:143],v[202:203],v[174:175],v[142:143]
v_pk_fma_f32 v[196:197],v[144:145],v[228:229],v[230:231]
v_pk_fma_f32 v[198:199],v[146:147],v[228:229],v[230:231]
v_pk_fma_f32 v[200:201],v[148:149],v[228:229],v[230:231]
v_pk_fma_f32 v[202:203],v[150:151],v[228:229],v[230:231]
v_pk_fma_f32 v[144:145],v[196:197],v[176:177],v[144:145]
v_pk_fma_f32 v[146:147],v[198:199],v[178:179],v[146:147]
v_pk_fma_f32 v[148:149],v[200:201],v[180:181],v[148:149]
v_pk_fma_f32 v[150:151],v[202:203],v[182:183],v[150:151]
v_pk_fma_f32 v[196:197],v[152:153],v[228:229],v[230:231]
v_pk_fma_f32 v[198:199],v[154:155],v[228:229],v[230:231]
v_pk_fma_f32 v[200:201],v[156:157],v[228:229],v[230:231]
v_pk_fma_f32 v[202:203],v[158:159],v[228:229],v[230:231]
v_pk_fma_f32 v[152:153],v[196:197],v[184:185],v[152:153]
v_pk_fma_f32 v[154:155],v[198:199],v[186:187],v[154:155]
v_pk_fma_f32 v[156:157],v[200:201],v[188:189],v[156:157]
v_pk_fma_f32 v[158:159],v[202:203],v[190:191],v[158:159]
v_cvt_pk_bf16_f32 v208,v128,v129
v_cvt_pk_bf16_f32 v209,v130,v131
v_cvt_pk_bf16_f32 v210,v132,v133
v_cvt_pk_bf16_f32 v211,v134,v135)"
R"(
v_cvt_pk_bf16_f32 v212,v136,v137
v_cvt_pk_bf16_f32 v213,v138,v139
v_cvt_pk_bf16_f32 v214,v140,v141
v_cvt_pk_bf16_f32 v215,v142,v143
v_mfma_f32_16x16x32_bf16 v[28:31],v[192:195],v[208:211],0
v_cvt_pk_bf16_f32 v216,v144,v145
v_cvt_pk_bf16_f32 v217,v146,v147
v_mfma_f32_16x16x32_bf16 v[204:207],v[208:211],v[208:211],0
v_cvt_pk_bf16_f32 v218,v148,v149
v_cvt_pk_bf16_f32 v219,v150,v151
v_mfma_f32_16x16x32_bf16 v[28:31],v[192:195],v[212:215],v[28:31]
v_cvt_pk_bf16_f32 v220,v152,v153
v_cvt_pk_bf16_f32 v221,v154,v155
v_mfma_f32_16x16x32_bf16 v[204:207],v[212:215],v[212:215],v[204:207]
v_cvt_pk_bf16_f32 v222,v156,v157
v_cvt_pk_bf16_f32 v223,v158,v159
v_mfma_f32_16x16x32_bf16 v[28:31],v[192:195],v[216:219],v[28:31]
v_mfma_f32_16x16x32_bf16 v[204:207],v[216:219],v[216:219],v[204:207]
v_mfma_f32_16x16x32_bf16 v[28:31],v[192:195],v[220:223],v[28:31]
v_mfma_f32_16x16x32_bf16 v[204:207],v[220:223],v[220:223],v[204:207]
s_waitcnt vmcnt(17)
buffer_store_dword v7,v5,s[36:39],s60 offen offset:0 sc0 sc1
s_mov_b32 s60,536870912
buffer_store_dwordx4 v[208:211],v1,s[36:39],s88 offen offset:0 sc0 sc1
buffer_store_dwordx4 v[212:215],v1,s[36:39],s88 offen offset:2048 sc0 sc1
s_add_u32 s88,s88,4096
s_nop 0
v_add_f32_dpp v224,v28,v28 row_ror:8 row_mask:0xf bank_mask:0xf
v_cndmask_b32 v225,v204,v205,s[46:47]
v_cndmask_b32 v226,v206,v207,s[46:47]
v_cndmask_b32 v225,v225,v226,s[56:57]
s_and_b32 s63,s89,128
ds_write_b32 v2,v224 offset:1024
ds_write_b32 v0,v225 offset:1024
buffer_store_dwordx4 v[216:219],v1,s[36:39],s88 offen offset:0 sc0 sc1
buffer_store_dwordx4 v[220:223],v1,s[36:39],s88 offen offset:2048 sc0 sc1
s_waitcnt lgkmcnt(0)
s_barrier
ds_read_b128 v[196:199],v3 offset:1024
ds_read_b128 v[200:203],v3 offset:1040
ds_read_b128 v[204:207],v3 offset:1056
s_waitcnt lgkmcnt(0)
v_add_f32 v196,v196,v197
v_add_f32 v198,v198,v199
v_add_f32 v200,v200,v204
v_add_f32 v201,v201,v205
v_add_f32 v202,v202,v206
v_add_f32 v203,v203,v207
v_add_f32 v196,v196,v198
v_add_f32 v200,v200,v201
v_add_f32 v202,v202,v203
v_mul_f32 v196,s76,v196
v_add_f32 v197,v200,v202
v_mul_f32 v197,s76,v197
v_fma_f32 v197,-v196,v196,v197
v_max_f32 v197,0,v197
v_add_f32 v198,s77,v197
v_fma_f32 v199,s77,v198,v197
v_rsq_f32 v205,v198
v_rsq_f32 v198,v199
v_mov_b32 v200,v196
v_mul_f32_e64 v204,-v196,v205
v_mul_f32 v203,v199,v198
v_mul_f32 v201,s78,v198
v_mul_f32 v199,v198,v198
v_mul_f32 v202,s79,v199
v_mul_f32 v202,v202,v201
s_lshr_b32 s63,s63,1
s_sub_u32 s63,s89,s63
buffer_store_dwordx4 v[200:203],v4,s[36:39],s89 offen offset:0 sc0 sc1
buffer_store_dwordx2 v[204:205],v10,s[36:39],s63 offen offset:256 sc0 sc1
s_add_u32 m0,s66,s52
s_cmp_ge_u32 s51,12582912
s_cbranch_scc1 Lr_ys3_%=
buffer_load_dwordx4 v11,s[36:39],s51 offen offset:0 sc1 lds
buffer_load_dwordx4 v11,s[36:39],s51 offen offset:1024 sc1 lds
buffer_load_dwordx4 v11,s[36:39],s51 offen offset:2048 sc1 lds
buffer_load_dwordx4 v11,s[36:39],s51 offen offset:3072 sc1 lds
s_lshl_b32 s63,s50,14
s_add_u32 s51,s51,s63
Lr_ys3_%=:
s_add_u32 s66,s66,16384
s_cmp_eq_u32 s66,151552
s_cselect_b32 s66,4096,s66
v_add_u32 v18,s66,v11
s_mov_b32 s60,s90
s_waitcnt vmcnt(7)
s_branch Lr_it1_%=
Lr_tc0_%=:
s_add_u32 s63,s87,71303168
buffer_load_dwordx4 a[64:67],v11,s[36:39],s63 offen offset:0 sc1
buffer_load_dwordx4 a[68:71],v11,s[36:39],s63 offen offset:1024 sc1
buffer_load_dwordx4 a[72:75],v11,s[36:39],s63 offen offset:2048 sc1
buffer_load_dwordx4 a[76:79],v11,s[36:39],s63 offen offset:3072 sc1
s_add_u32 s63,s87,89128960
buffer_load_dwordx4 a[80:83],v11,s[36:39],s63 offen offset:0 sc1
buffer_load_dwordx4 a[84:87],v11,s[36:39],s63 offen offset:1024 sc1
buffer_load_dwordx4 a[88:91],v11,s[36:39],s63 offen offset:2048 sc1
buffer_load_dwordx4 a[92:95],v11,s[36:39],s63 offen offset:3072 sc1
s_add_u32 s63,s87,99614720
buffer_load_dwordx4 a[96:99],v11,s[36:39],s63 offen offset:0 sc1
buffer_load_dwordx4 a[100:103],v11,s[36:39],s63 offen offset:1024 sc1
buffer_load_dwordx4 a[104:107],v11,s[36:39],s63 offen offset:2048 sc1
buffer_load_dwordx4 a[108:111],v11,s[36:39],s63 offen offset:3072 sc1
s_add_u32 s63,s87,109051904
buffer_load_dwordx4 a[112:115],v11,s[36:39],s63 offen offset:0 sc1
buffer_load_dwordx4 a[116:119],v11,s[36:39],s63 offen offset:1024 sc1
buffer_load_dwordx4 a[120:123],v11,s[36:39],s63 offen offset:2048 sc1
buffer_load_dwordx4 a[124:127],v11,s[36:39],s63 offen offset:3072 sc1
buffer_load_dword v8,v6,s[36:39],s69 offen offset:0 sc1
s_waitcnt vmcnt(17)
buffer_store_dword v7,v5,s[36:39],s60 offen offset:0 sc0 sc1
s_mov_b32 s60,536870912
s_waitcnt vmcnt(1)
s_branch Lr_it1_%=
Lr_it1_%=:
s_mov_b32 s88,s58
s_mov_b32 s89,s59
s_mov_b32 s90,s49
s_add_u32 s54,s54,s50
s_lshr_b32 s63,s54,1
s_and_b32 s64,s54,1
s_lshl_b32 s64,s64,7
s_lshl_b32 s87,s54,14
s_lshl_b32 s58,s63,15
s_add_u32 s58,s58,s64
s_add_u32 s58,s58,16908288
s_lshl_b32 s59,s63,9
s_add_u32 s59,s59,s64
s_add_u32 s59,s59,118556672
s_lshl_b32 s49,s54,9
s_add_u32 s49,s49,s92
s_lshl_b32 s91,s63,11
s_add_u32 s91,s91,121405440
s_lshl_b32 s69,s50,10
s_add_u32 s69,s69,s91
v_cmp_eq_u32 vcc,s48,v8
s_nop 0
s_cmp_eq_u64 vcc,exec
s_cselect_b32 s63,s54,256
s_cmp_lt_u32 s63,256
s_cbranch_scc1 Lr_ta1_%=
ds_read_b128 v[32:35],v18 offset:0
ds_read_b128 v[36:39],v18 offset:1024
ds_read_b128 v[40:43],v18 offset:2048
ds_read_b128 v[44:47],v18 offset:3072
v_mfma_f32_16x16x32_bf16 v[128:131],v[20:23],a[64:67],0
v_mfma_f32_16x16x32_bf16 v[132:135],v[24:27],a[64:67],0
v_mfma_f32_16x16x32_bf16 v[128:131],v[20:23],a[80:83],v[128:131]
v_mfma_f32_16x16x32_bf16 v[132:135],v[24:27],a[80:83],v[132:135]
v_mfma_f32_16x16x32_bf16 v[128:131],v[20:23],a[96:99],v[128:131]
v_mfma_f32_16x16x32_bf16 v[132:135],v[24:27],a[96:99],v[132:135]
v_mfma_f32_16x16x32_bf16 v[128:131],v[20:23],a[112:115],v[128:131]
v_mfma_f32_16x16x32_bf16 v[132:135],v[24:27],a[112:115],v[132:135]
v_mfma_f32_16x16x32_bf16 v[136:139],v[20:23],a[68:71],0
v_mfma_f32_16x16x32_bf16 v[140:143],v[24:27],a[68:71],0
v_mfma_f32_16x16x32_bf16 v[136:139],v[20:23],a[84:87],v[136:139]
v_mfma_f32_16x16x32_bf16 v[140:143],v[24:27],a[84:87],v[140:143]
v_mfma_f32_16x16x32_bf16 v[136:139],v[20:23],a[100:103],v[136:139]
s_nop 1
v_cvt_pk_bf16_f32 v208,v128,v129
v_cvt_pk_bf16_f32 v209,v130,v131
v_mfma_f32_16x16x32_bf16 v[140:143],v[24:27],a[100:103],v[140:143]
v_cvt_pk_bf16_f32 v210,v132,v133
v_cvt_pk_bf16_f32 v211,v134,v135
v_mfma_f32_16x16x32_bf16 v[136:139],v[20:23],a[116:119],v[136:139]
v_mfma_f32_16x16x32_bf16 v[140:143],v[24:27],a[116:119],v[140:143]
v_mfma_f32_16x16x32_bf16 v[144:147],v[20:23],a[72:75],0
v_mfma_f32_16x16x32_bf16 v[148:151],v[24:27],a[72:75],0
v_mfma_f32_16x16x32_bf16 v[144:147],v[20:23],a[88:91],v[144:147]
v_mfma_f32_16x16x32_bf16 v[148:151],v[24:27],a[88:91],v[148:151]
v_mfma_f32_16x16x32_bf16 v[144:147],v[20:23],a[104:107],v[144:147]
s_nop 1
v_cvt_pk_bf16_f32 v212,v136,v137
v_cvt_pk_bf16_f32 v213,v138,v139
v_mfma_f32_16x16x32_bf16 v[148:151],v[24:27],a[104:107],v[148:151]
v_cvt_pk_bf16_f32 v214,v140,v141
v_cvt_pk_bf16_f32 v215,v142,v143
v_mfma_f32_16x16x32_bf16 v[144:147],v[20:23],a[120:123],v[144:147]
v_mfma_f32_16x16x32_bf16 v[148:151],v[24:27],a[120:123],v[148:151]
v_mfma_f32_16x16x32_bf16 v[28:31],v[192:195],v[208:211],0
v_mfma_f32_16x16x32_bf16 v[204:207],v[208:211],v[208:211],0
v_mfma_f32_16x16x32_bf16 v[152:155],v[20:23],a[76:79],0
v_mfma_f32_16x16x32_bf16 v[156:159],v[24:27],a[76:79],0
v_mfma_f32_16x16x32_bf16 v[152:155],v[20:23],a[92:95],v[152:155]
v_mfma_f32_16x16x32_bf16 v[156:159],v[24:27],a[92:95],v[156:159]
v_mfma_f32_16x16x32_bf16 v[152:155],v[20:23],a[108:111],v[152:155]
v_cvt_pk_bf16_f32 v216,v144,v145
v_cvt_pk_bf16_f32 v217,v146,v147
v_mfma_f32_16x16x32_bf16 v[156:159],v[24:27],a[108:111],v[156:159]
v_cvt_pk_bf16_f32 v218,v148,v149
v_cvt_pk_bf16_f32 v219,v150,v151
v_mfma_f32_16x16x32_bf16 v[152:155],v[20:23],a[124:127],v[152:155]
v_mfma_f32_16x16x32_bf16 v[156:159],v[24:27],a[124:127],v[156:159]
v_mfma_f32_16x16x32_bf16 v[28:31],v[192:195],v[212:215],v[28:31]
v_mfma_f32_16x16x32_bf16 v[204:207],v[212:215],v[212:215],v[204:207]
v_mfma_f32_16x16x32_bf16 v[28:31],v[192:195],v[216:219],v[28:31]
v_mfma_f32_16x16x32_bf16 v[204:207],v[216:219],v[216:219],v[204:207]
s_waitcnt lgkmcnt(0)
v_mfma_f32_16x16x32_bf16 v[160:163],v[20:23],v[32:35],0)"
R"(
s_nop 0
v_cvt_pk_bf16_f32 v220,v152,v153
v_cvt_pk_bf16_f32 v221,v154,v155
v_mfma_f32_16x16x32_bf16 v[164:167],v[24:27],v[32:35],0
v_cvt_pk_bf16_f32 v222,v156,v157
v_cvt_pk_bf16_f32 v223,v158,v159
v_mfma_f32_16x16x32_bf16 v[168:171],v[20:23],v[36:39],0
s_nop 0
v_mfma_f32_16x16x32_bf16 v[28:31],v[192:195],v[220:223],v[28:31]
v_mfma_f32_16x16x32_bf16 v[204:207],v[220:223],v[220:223],v[204:207]
v_mfma_f32_16x16x32_bf16 v[172:175],v[24:27],v[36:39],0
v_mfma_f32_16x16x32_bf16 v[176:179],v[20:23],v[40:43],0
v_mfma_f32_16x16x32_bf16 v[180:183],v[24:27],v[40:43],0
s_nop 3
v_add_f32_dpp v224,v28,v28 row_ror:8 row_mask:0xf bank_mask:0xf
v_cndmask_b32 v225,v204,v205,s[46:47]
v_cndmask_b32 v226,v206,v207,s[46:47]
v_cndmask_b32 v225,v225,v226,s[56:57]
v_mfma_f32_16x16x32_bf16 v[184:187],v[20:23],v[44:47],0
ds_write_b32 v2,v224 offset:0
ds_write_b32 v0,v225 offset:0
v_mfma_f32_16x16x32_bf16 v[188:191],v[24:27],v[44:47],0
s_waitcnt lgkmcnt(0)
s_barrier
ds_read_b128 v[196:199],v3 offset:0
ds_read_b128 v[200:203],v3 offset:16
ds_read_b128 v[204:207],v3 offset:32
s_waitcnt lgkmcnt(0)
v_add_f32 v196,v196,v197
v_add_f32 v198,v198,v199
v_add_f32 v200,v200,v204
v_add_f32 v201,v201,v205
v_add_f32 v202,v202,v206
v_add_f32 v203,v203,v207
v_add_f32 v196,v196,v198
v_add_f32 v200,v200,v201
v_add_f32 v202,v202,v203
v_mul_f32 v196,s76,v196
v_add_f32 v197,v200,v202
v_mul_f32 v197,s76,v197
v_fma_f32 v197,-v196,v196,v197
v_max_f32 v197,0,v197
v_add_f32 v197,s77,v197
v_rsq_f32 v228,v197
s_nop 0
v_mul_f32_e64 v230,-v196,v228
v_mov_b32 v229,v228
v_mov_b32 v231,v230
v_pk_fma_f32 v[196:197],v[128:129],v[228:229],v[230:231]
v_pk_fma_f32 v[198:199],v[130:131],v[228:229],v[230:231]
v_pk_fma_f32 v[200:201],v[132:133],v[228:229],v[230:231]
v_pk_fma_f32 v[202:203],v[134:135],v[228:229],v[230:231]
v_pk_fma_f32 v[128:129],v[196:197],v[160:161],v[128:129]
v_pk_fma_f32 v[130:131],v[198:199],v[162:163],v[130:131]
v_pk_fma_f32 v[132:133],v[200:201],v[164:165],v[132:133]
v_pk_fma_f32 v[134:135],v[202:203],v[166:167],v[134:135]
v_pk_fma_f32 v[196:197],v[136:137],v[228:229],v[230:231]
v_pk_fma_f32 v[198:199],v[138:139],v[228:229],v[230:231]
v_pk_fma_f32 v[200:201],v[140:141],v[228:229],v[230:231]
v_pk_fma_f32 v[202:203],v[142:143],v[228:229],v[230:231]
v_pk_fma_f32 v[136:137],v[196:197],v[168:169],v[136:137]
v_pk_fma_f32 v[138:139],v[198:199],v[170:171],v[138:139]
v_pk_fma_f32 v[140:141],v[200:201],v[172:173],v[140:141]
v_pk_fma_f32 v[142:143],v[202:203],v[174:175],v[142:143]
v_pk_fma_f32 v[196:197],v[144:145],v[228:229],v[230:231]
v_pk_fma_f32 v[198:199],v[146:147],v[228:229],v[230:231]
v_pk_fma_f32 v[200:201],v[148:149],v[228:229],v[230:231]
v_pk_fma_f32 v[202:203],v[150:151],v[228:229],v[230:231]
v_pk_fma_f32 v[144:145],v[196:197],v[176:177],v[144:145]
v_pk_fma_f32 v[146:147],v[198:199],v[178:179],v[146:147]
v_pk_fma_f32 v[148:149],v[200:201],v[180:181],v[148:149]
v_pk_fma_f32 v[150:151],v[202:203],v[182:183],v[150:151]
v_pk_fma_f32 v[196:197],v[152:153],v[228:229],v[230:231]
v_pk_fma_f32 v[198:199],v[154:155],v[228:229],v[230:231]
v_pk_fma_f32 v[200:201],v[156:157],v[228:229],v[230:231]
v_pk_fma_f32 v[202:203],v[158:159],v[228:229],v[230:231]
v_pk_fma_f32 v[152:153],v[196:197],v[184:185],v[152:153]
v_pk_fma_f32 v[154:155],v[198:199],v[186:187],v[154:155]
v_pk_fma_f32 v[156:157],v[200:201],v[188:189],v[156:157]
v_pk_fma_f32 v[158:159],v[202:203],v[190:191],v[158:159]
v_cvt_pk_bf16_f32 v208,v128,v129
v_cvt_pk_bf16_f32 v209,v130,v131
v_cvt_pk_bf16_f32 v210,v132,v133
v_cvt_pk_bf16_f32 v211,v134,v135
v_cvt_pk_bf16_f32 v212,v136,v137
v_cvt_pk_bf16_f32 v213,v138,v139
v_cvt_pk_bf16_f32 v214,v140,v141
v_cvt_pk_bf16_f32 v215,v142,v143
v_mfma_f32_16x16x32_bf16 v[28:31],v[192:195],v[208:211],0
v_cvt_pk_bf16_f32 v216,v144,v145
v_cvt_pk_bf16_f32 v217,v146,v147
v_mfma_f32_16x16x32_bf16 v[204:207],v[208:211],v[208:211],0
v_cvt_pk_bf16_f32 v218,v148,v149
v_cvt_pk_bf16_f32 v219,v150,v151
v_mfma_f32_16x16x32_bf16 v[28:31],v[192:195],v[212:215],v[28:31]
v_cvt_pk_bf16_f32 v220,v152,v153
v_cvt_pk_bf16_f32 v221,v154,v155
v_mfma_f32_16x16x32_bf16 v[204:207],v[212:215],v[212:215],v[204:207]
v_cvt_pk_bf16_f32 v222,v156,v157
v_cvt_pk_bf16_f32 v223,v158,v159
v_mfma_f32_16x16x32_bf16 v[28:31],v[192:195],v[216:219],v[28:31]
v_mfma_f32_16x16x32_bf16 v[204:207],v[216:219],v[216:219],v[204:207]
v_mfma_f32_16x16x32_bf16 v[28:31],v[192:195],v[220:223],v[28:31]
v_mfma_f32_16x16x32_bf16 v[204:207],v[220:223],v[220:223],v[204:207]
s_waitcnt vmcnt(0)
buffer_store_dword v7,v5,s[36:39],s60 offen offset:0 sc0 sc1
s_mov_b32 s60,536870912
buffer_load_dword v8,v6,s[36:39],s91 offen offset:0 sc1
buffer_store_dwordx4 v[208:211],v1,s[36:39],s88 offen offset:0 sc0 sc1
buffer_store_dwordx4 v[212:215],v1,s[36:39],s88 offen offset:2048 sc0 sc1
s_add_u32 s88,s88,4096
v_add_f32_dpp v224,v28,v28 row_ror:8 row_mask:0xf bank_mask:0xf
v_cndmask_b32 v225,v204,v205,s[46:47]
v_cndmask_b32 v226,v206,v207,s[46:47]
v_cndmask_b32 v225,v225,v226,s[56:57]
s_and_b32 s63,s89,128
ds_write_b32 v2,v224 offset:1024
ds_write_b32 v0,v225 offset:1024
buffer_store_dwordx4 v[216:219],v1,s[36:39],s88 offen offset:0 sc0 sc1
buffer_store_dwordx4 v[220:223],v1,s[36:39],s88 offen offset:2048 sc0 sc1
s_waitcnt lgkmcnt(0)
s_barrier
ds_read_b128 v[196:199],v3 offset:1024
ds_read_b128 v[200:203],v3 offset:1040
ds_read_b128 v[204:207],v3 offset:1056
s_waitcnt lgkmcnt(0)
v_add_f32 v196,v196,v197
v_add_f32 v198,v198,v199
v_add_f32 v200,v200,v204
v_add_f32 v201,v201,v205
v_add_f32 v202,v202,v206
v_add_f32 v203,v203,v207
v_add_f32 v196,v196,v198
v_add_f32 v200,v200,v201
v_add_f32 v202,v202,v203
v_mul_f32 v196,s76,v196
v_add_f32 v197,v200,v202
v_mul_f32 v197,s76,v197
v_fma_f32 v197,-v196,v196,v197
v_max_f32 v197,0,v197
v_add_f32 v198,s77,v197
v_fma_f32 v199,s77,v198,v197
v_rsq_f32 v205,v198
v_rsq_f32 v198,v199
v_mov_b32 v200,v196
v_mul_f32_e64 v204,-v196,v205
v_mul_f32 v203,v199,v198
v_mul_f32 v201,s78,v198
v_mul_f32 v199,v198,v198
v_mul_f32 v202,s79,v199
v_mul_f32 v202,v202,v201
s_lshr_b32 s63,s63,1
s_sub_u32 s63,s89,s63
buffer_store_dwordx4 v[200:203],v4,s[36:39],s89 offen offset:0 sc0 sc1
buffer_store_dwordx2 v[204:205],v10,s[36:39],s63 offen offset:256 sc0 sc1
s_add_u32 m0,s66,s52
s_cmp_ge_u32 s51,12582912
s_cbranch_scc1 Lr_ys4_%=
buffer_load_dwordx4 v11,s[36:39],s51 offen offset:0 sc1 lds
buffer_load_dwordx4 v11,s[36:39],s51 offen offset:1024 sc1 lds
buffer_load_dwordx4 v11,s[36:39],s51 offen offset:2048 sc1 lds
buffer_load_dwordx4 v11,s[36:39],s51 offen offset:3072 sc1 lds
s_lshl_b32 s63,s50,14
s_add_u32 s51,s51,s63
Lr_ys4_%=:
s_add_u32 s66,s66,16384
s_cmp_eq_u32 s66,151552
s_cselect_b32 s66,4096,s66
v_add_u32 v18,s66,v11
s_mov_b32 s60,s90
s_waitcnt vmcnt(6)
v_cmp_eq_u32 vcc,s48,v8
s_nop 0
s_cmp_eq_u64 vcc,exec
s_cselect_b32 s63,s54,256
s_cmp_lt_u32 s63,256
s_cbranch_scc1 Lr_tc1_%=
buffer_load_dword v8,v6,s[36:39],s91 offen offset:0 sc1
s_waitcnt vmcnt(0)
buffer_store_dword v7,v5,s[36:39],s60 offen offset:0 sc0 sc1
s_mov_b32 s60,536870912
v_cmp_eq_u32 vcc,s48,v8
s_nop 0
s_cmp_eq_u64 vcc,exec
s_cselect_b32 s63,s54,256
s_cmp_lt_u32 s63,256
s_cbranch_scc1 Lr_bq1_%=
s_cmp_ge_u32 s54,256
s_cbranch_scc1 Lr_done_%=
Lr_bp1_%=:
buffer_load_dword v8,v6,s[36:39],s91 offen offset:0 sc1
s_waitcnt vmcnt(0)
v_cmp_eq_u32 vcc,s48,v8
s_nop 0
s_cmp_eq_u64 vcc,exec
s_cselect_b32 s63,1,s96
s_cmp_lg_u32 s63,0
s_cbranch_scc1 Lr_bq1_%=
s_add_u32 s74,s74,1
s_cmp_ge_u32 s74,20000)"
R"(
s_cselect_b32 s96,1,0
s_sleep 4
s_branch Lr_bp1_%=
Lr_bq1_%=:
s_add_u32 s63,s87,71303168
buffer_load_dwordx4 a[0:3],v11,s[36:39],s63 offen offset:0 sc1
buffer_load_dwordx4 a[4:7],v11,s[36:39],s63 offen offset:1024 sc1
buffer_load_dwordx4 a[8:11],v11,s[36:39],s63 offen offset:2048 sc1
buffer_load_dwordx4 a[12:15],v11,s[36:39],s63 offen offset:3072 sc1
s_add_u32 s63,s87,89128960
buffer_load_dwordx4 a[16:19],v11,s[36:39],s63 offen offset:0 sc1
buffer_load_dwordx4 a[20:23],v11,s[36:39],s63 offen offset:1024 sc1
buffer_load_dwordx4 a[24:27],v11,s[36:39],s63 offen offset:2048 sc1
buffer_load_dwordx4 a[28:31],v11,s[36:39],s63 offen offset:3072 sc1
s_add_u32 s63,s87,99614720
buffer_load_dwordx4 a[32:35],v11,s[36:39],s63 offen offset:0 sc1
buffer_load_dwordx4 a[36:39],v11,s[36:39],s63 offen offset:1024 sc1
buffer_load_dwordx4 a[40:43],v11,s[36:39],s63 offen offset:2048 sc1
buffer_load_dwordx4 a[44:47],v11,s[36:39],s63 offen offset:3072 sc1
s_add_u32 s63,s87,109051904
buffer_load_dwordx4 a[48:51],v11,s[36:39],s63 offen offset:0 sc1
buffer_load_dwordx4 a[52:55],v11,s[36:39],s63 offen offset:1024 sc1
buffer_load_dwordx4 a[56:59],v11,s[36:39],s63 offen offset:2048 sc1
buffer_load_dwordx4 a[60:63],v11,s[36:39],s63 offen offset:3072 sc1
buffer_load_dword v8,v6,s[36:39],s69 offen offset:0 sc1
s_waitcnt vmcnt(0)
s_branch Lr_it0_%=
Lr_ta1_%=:
ds_read_b128 v[32:35],v18 offset:0
ds_read_b128 v[36:39],v18 offset:1024
ds_read_b128 v[40:43],v18 offset:2048
ds_read_b128 v[44:47],v18 offset:3072
v_mfma_f32_16x16x32_bf16 v[128:131],v[20:23],a[64:67],0
s_add_u32 s63,s87,71303168
v_mfma_f32_16x16x32_bf16 v[132:135],v[24:27],a[64:67],0
buffer_load_dwordx4 a[0:3],v11,s[36:39],s63 offen offset:0 sc1
v_mfma_f32_16x16x32_bf16 v[128:131],v[20:23],a[80:83],v[128:131]
v_mfma_f32_16x16x32_bf16 v[132:135],v[24:27],a[80:83],v[132:135]
buffer_load_dwordx4 a[4:7],v11,s[36:39],s63 offen offset:1024 sc1
v_mfma_f32_16x16x32_bf16 v[128:131],v[20:23],a[96:99],v[128:131]
v_mfma_f32_16x16x32_bf16 v[132:135],v[24:27],a[96:99],v[132:135]
v_mfma_f32_16x16x32_bf16 v[128:131],v[20:23],a[112:115],v[128:131]
v_mfma_f32_16x16x32_bf16 v[132:135],v[24:27],a[112:115],v[132:135]
buffer_load_dwordx4 a[8:11],v11,s[36:39],s63 offen offset:2048 sc1
v_mfma_f32_16x16x32_bf16 v[136:139],v[20:23],a[68:71],0
v_mfma_f32_16x16x32_bf16 v[140:143],v[24:27],a[68:71],0
v_mfma_f32_16x16x32_bf16 v[136:139],v[20:23],a[84:87],v[136:139]
v_mfma_f32_16x16x32_bf16 v[140:143],v[24:27],a[84:87],v[140:143]
buffer_load_dwordx4 a[12:15],v11,s[36:39],s63 offen offset:3072 sc1
v_mfma_f32_16x16x32_bf16 v[136:139],v[20:23],a[100:103],v[136:139]
v_cvt_pk_bf16_f32 v208,v128,v129
v_cvt_pk_bf16_f32 v209,v130,v131
v_mfma_f32_16x16x32_bf16 v[140:143],v[24:27],a[100:103],v[140:143]
v_cvt_pk_bf16_f32 v210,v132,v133
v_cvt_pk_bf16_f32 v211,v134,v135
v_mfma_f32_16x16x32_bf16 v[136:139],v[20:23],a[116:119],v[136:139]
s_add_u32 s63,s87,89128960
v_mfma_f32_16x16x32_bf16 v[140:143],v[24:27],a[116:119],v[140:143]
buffer_load_dwordx4 a[16:19],v11,s[36:39],s63 offen offset:0 sc1
v_mfma_f32_16x16x32_bf16 v[144:147],v[20:23],a[72:75],0
v_mfma_f32_16x16x32_bf16 v[148:151],v[24:27],a[72:75],0
v_mfma_f32_16x16x32_bf16 v[144:147],v[20:23],a[88:91],v[144:147]
v_mfma_f32_16x16x32_bf16 v[148:151],v[24:27],a[88:91],v[148:151]
buffer_load_dwordx4 a[20:23],v11,s[36:39],s63 offen offset:1024 sc1
v_mfma_f32_16x16x32_bf16 v[144:147],v[20:23],a[104:107],v[144:147]
v_cvt_pk_bf16_f32 v212,v136,v137
v_cvt_pk_bf16_f32 v213,v138,v139
v_mfma_f32_16x16x32_bf16 v[148:151],v[24:27],a[104:107],v[148:151]
v_cvt_pk_bf16_f32 v214,v140,v141
v_cvt_pk_bf16_f32 v215,v142,v143
v_mfma_f32_16x16x32_bf16 v[144:147],v[20:23],a[120:123],v[144:147]
v_mfma_f32_16x16x32_bf16 v[148:151],v[24:27],a[120:123],v[148:151]
buffer_load_dwordx4 a[24:27],v11,s[36:39],s63 offen offset:2048 sc1
v_mfma_f32_16x16x32_bf16 v[28:31],v[192:195],v[208:211],0
v_mfma_f32_16x16x32_bf16 v[204:207],v[208:211],v[208:211],0
v_mfma_f32_16x16x32_bf16 v[152:155],v[20:23],a[76:79],0
v_mfma_f32_16x16x32_bf16 v[156:159],v[24:27],a[76:79],0
buffer_load_dwordx4 a[28:31],v11,s[36:39],s63 offen offset:3072 sc1
v_mfma_f32_16x16x32_bf16 v[152:155],v[20:23],a[92:95],v[152:155]
s_add_u32 s63,s87,99614720
v_mfma_f32_16x16x32_bf16 v[156:159],v[24:27],a[92:95],v[156:159]
v_mfma_f32_16x16x32_bf16 v[152:155],v[20:23],a[108:111],v[152:155]
v_cvt_pk_bf16_f32 v216,v144,v145
v_cvt_pk_bf16_f32 v217,v146,v147
v_mfma_f32_16x16x32_bf16 v[156:159],v[24:27],a[108:111],v[156:159]
v_cvt_pk_bf16_f32 v218,v148,v149
v_cvt_pk_bf16_f32 v219,v150,v151
v_mfma_f32_16x16x32_bf16 v[152:155],v[20:23],a[124:127],v[152:155]
buffer_load_dwordx4 a[32:35],v11,s[36:39],s63 offen offset:0 sc1
v_mfma_f32_16x16x32_bf16 v[156:159],v[24:27],a[124:127],v[156:159]
v_mfma_f32_16x16x32_bf16 v[28:31],v[192:195],v[212:215],v[28:31]
v_mfma_f32_16x16x32_bf16 v[204:207],v[212:215],v[212:215],v[204:207]
buffer_load_dwordx4 a[36:39],v11,s[36:39],s63 offen offset:1024 sc1
v_mfma_f32_16x16x32_bf16 v[28:31],v[192:195],v[216:219],v[28:31]
v_mfma_f32_16x16x32_bf16 v[204:207],v[216:219],v[216:219],v[204:207]
s_waitcnt lgkmcnt(0)
v_mfma_f32_16x16x32_bf16 v[160:163],v[20:23],v[32:35],0
v_cvt_pk_bf16_f32 v220,v152,v153
v_cvt_pk_bf16_f32 v221,v154,v155
v_mfma_f32_16x16x32_bf16 v[164:167],v[24:27],v[32:35],0
v_cvt_pk_bf16_f32 v222,v156,v157
v_cvt_pk_bf16_f32 v223,v158,v159
v_mfma_f32_16x16x32_bf16 v[168:171],v[20:23],v[36:39],0
buffer_load_dwordx4 a[40:43],v11,s[36:39],s63 offen offset:2048 sc1
v_mfma_f32_16x16x32_bf16 v[28:31],v[192:195],v[220:223],v[28:31]
buffer_load_dwordx4 a[44:47],v11,s[36:39],s63 offen offset:3072 sc1
v_mfma_f32_16x16x32_bf16 v[204:207],v[220:223],v[220:223],v[204:207]
s_add_u32 s63,s87,109051904
v_mfma_f32_16x16x32_bf16 v[172:175],v[24:27],v[36:39],0
v_mfma_f32_16x16x32_bf16 v[176:179],v[20:23],v[40:43],0
buffer_load_dwordx4 a[48:51],v11,s[36:39],s63 offen offset:0 sc1
v_mfma_f32_16x16x32_bf16 v[180:183],v[24:27],v[40:43],0
s_nop 0
v_add_f32_dpp v224,v28,v28 row_ror:8 row_mask:0xf bank_mask:0xf
s_nop 0
v_cndmask_b32 v225,v204,v205,s[46:47]
v_cndmask_b32 v226,v206,v207,s[46:47]
v_cndmask_b32 v225,v225,v226,s[56:57]
v_mfma_f32_16x16x32_bf16 v[184:187],v[20:23],v[44:47],0
ds_write_b32 v2,v224 offset:0
ds_write_b32 v0,v225 offset:0
v_mfma_f32_16x16x32_bf16 v[188:191],v[24:27],v[44:47],0
s_waitcnt lgkmcnt(0)
buffer_load_dwordx4 a[52:55],v11,s[36:39],s63 offen offset:1024 sc1
s_barrier
buffer_load_dwordx4 a[56:59],v11,s[36:39],s63 offen offset:2048 sc1
ds_read_b128 v[196:199],v3 offset:0
buffer_load_dwordx4 a[60:63],v11,s[36:39],s63 offen offset:3072 sc1
ds_read_b128 v[200:203],v3 offset:16
ds_read_b128 v[204:207],v3 offset:32
s_waitcnt lgkmcnt(0)
buffer_load_dword v8,v6,s[36:39],s69 offen offset:0 sc1
v_add_f32 v196,v196,v197
v_add_f32 v198,v198,v199
v_add_f32 v200,v200,v204
v_add_f32 v201,v201,v205
v_add_f32 v202,v202,v206
v_add_f32 v203,v203,v207
v_add_f32 v196,v196,v198
v_add_f32 v200,v200,v201
v_add_f32 v202,v202,v203
v_mul_f32 v196,s76,v196
v_add_f32 v197,v200,v202
v_mul_f32 v197,s76,v197
v_fma_f32 v197,-v196,v196,v197
v_max_f32 v197,0,v197
v_add_f32 v197,s77,v197
v_rsq_f32 v228,v197
s_nop 0
v_mul_f32_e64 v230,-v196,v228
v_mov_b32 v229,v228
v_mov_b32 v231,v230
v_pk_fma_f32 v[196:197],v[128:129],v[228:229],v[230:231]
v_pk_fma_f32 v[198:199],v[130:131],v[228:229],v[230:231]
v_pk_fma_f32 v[200:201],v[132:133],v[228:229],v[230:231]
v_pk_fma_f32 v[202:203],v[134:135],v[228:229],v[230:231]
v_pk_fma_f32 v[128:129],v[196:197],v[160:161],v[128:129]
v_pk_fma_f32 v[130:131],v[198:199],v[162:163],v[130:131]
v_pk_fma_f32 v[132:133],v[200:201],v[164:165],v[132:133]
v_pk_fma_f32 v[134:135],v[202:203],v[166:167],v[134:135]
v_pk_fma_f32 v[196:197],v[136:137],v[228:229],v[230:231]
v_pk_fma_f32 v[198:199],v[138:139],v[228:229],v[230:231]
v_pk_fma_f32 v[200:201],v[140:141],v[228:229],v[230:231]
v_pk_fma_f32 v[202:203],v[142:143],v[228:229],v[230:231]
v_pk_fma_f32 v[136:137],v[196:197],v[168:169],v[136:137]
v_pk_fma_f32 v[138:139],v[198:199],v[170:171],v[138:139]
v_pk_fma_f32 v[140:141],v[200:201],v[172:173],v[140:141]
v_pk_fma_f32 v[142:143],v[202:203],v[174:175],v[142:143]
v_pk_fma_f32 v[196:197],v[144:145],v[228:229],v[230:231]
v_pk_fma_f32 v[198:199],v[146:147],v[228:229],v[230:231]
v_pk_fma_f32 v[200:201],v[148:149],v[228:229],v[230:231]
v_pk_fma_f32 v[202:203],v[150:151],v[228:229],v[230:231]
v_pk_fma_f32 v[144:145],v[196:197],v[176:177],v[144:145]
v_pk_fma_f32 v[146:147],v[198:199],v[178:179],v[146:147]
v_pk_fma_f32 v[148:149],v[200:201],v[180:181],v[148:149]
v_pk_fma_f32 v[150:151],v[202:203],v[182:183],v[150:151]
v_pk_fma_f32 v[196:197],v[152:153],v[228:229],v[230:231]
v_pk_fma_f32 v[198:199],v[154:155],v[228:229],v[230:231]
v_pk_fma_f32 v[200:201],v[156:157],v[228:229],v[230:231]
v_pk_fma_f32 v[202:203],v[158:159],v[228:229],v[230:231]
v_pk_fma_f32 v[152:153],v[196:197],v[184:185],v[152:153]
v_pk_fma_f32 v[154:155],v[198:199],v[186:187],v[154:155]
v_pk_fma_f32 v[156:157],v[200:201],v[188:189],v[156:157]
v_pk_fma_f32 v[158:159],v[202:203],v[190:191],v[158:159]
v_cvt_pk_bf16_f32 v208,v128,v129
v_cvt_pk_bf16_f32 v209,v130,v131
v_cvt_pk_bf16_f32 v210,v132,v133
v_cvt_pk_bf16_f32 v211,v134,v135
v_cvt_pk_bf16_f32 v212,v136,v137
v_cvt_pk_bf16_f32 v213,v138,v139
v_cvt_pk_bf16_f32 v214,v140,v141
v_cvt_pk_bf16_f32 v215,v142,v143
v_mfma_f32_16x16x32_bf16 v[28:31],v[192:195],v[208:211],0
v_cvt_pk_bf16_f32 v216,v144,v145
v_cvt_pk_bf16_f32 v217,v146,v147
v_mfma_f32_16x16x32_bf16 v[204:207],v[208:211],v[208:211],0
v_cvt_pk_bf16_f32 v218,v148,v149
v_cvt_pk_bf16_f32 v219,v150,v151
v_mfma_f32_16x16x32_bf16 v[28:31],v[192:195],v[212:215],v[28:31]
v_cvt_pk_bf16_f32 v220,v152,v153)"
R"(
v_cvt_pk_bf16_f32 v221,v154,v155
v_mfma_f32_16x16x32_bf16 v[204:207],v[212:215],v[212:215],v[204:207]
v_cvt_pk_bf16_f32 v222,v156,v157
v_cvt_pk_bf16_f32 v223,v158,v159
v_mfma_f32_16x16x32_bf16 v[28:31],v[192:195],v[216:219],v[28:31]
v_mfma_f32_16x16x32_bf16 v[204:207],v[216:219],v[216:219],v[204:207]
v_mfma_f32_16x16x32_bf16 v[28:31],v[192:195],v[220:223],v[28:31]
v_mfma_f32_16x16x32_bf16 v[204:207],v[220:223],v[220:223],v[204:207]
s_waitcnt vmcnt(17)
buffer_store_dword v7,v5,s[36:39],s60 offen offset:0 sc0 sc1
s_mov_b32 s60,536870912
buffer_store_dwordx4 v[208:211],v1,s[36:39],s88 offen offset:0 sc0 sc1
buffer_store_dwordx4 v[212:215],v1,s[36:39],s88 offen offset:2048 sc0 sc1
s_add_u32 s88,s88,4096
s_nop 0
v_add_f32_dpp v224,v28,v28 row_ror:8 row_mask:0xf bank_mask:0xf
v_cndmask_b32 v225,v204,v205,s[46:47]
v_cndmask_b32 v226,v206,v207,s[46:47]
v_cndmask_b32 v225,v225,v226,s[56:57]
s_and_b32 s63,s89,128
ds_write_b32 v2,v224 offset:1024
ds_write_b32 v0,v225 offset:1024
buffer_store_dwordx4 v[216:219],v1,s[36:39],s88 offen offset:0 sc0 sc1
buffer_store_dwordx4 v[220:223],v1,s[36:39],s88 offen offset:2048 sc0 sc1
s_waitcnt lgkmcnt(0)
s_barrier
ds_read_b128 v[196:199],v3 offset:1024
ds_read_b128 v[200:203],v3 offset:1040
ds_read_b128 v[204:207],v3 offset:1056
s_waitcnt lgkmcnt(0)
v_add_f32 v196,v196,v197
v_add_f32 v198,v198,v199
v_add_f32 v200,v200,v204
v_add_f32 v201,v201,v205
v_add_f32 v202,v202,v206
v_add_f32 v203,v203,v207
v_add_f32 v196,v196,v198
v_add_f32 v200,v200,v201
v_add_f32 v202,v202,v203
v_mul_f32 v196,s76,v196
v_add_f32 v197,v200,v202
v_mul_f32 v197,s76,v197
v_fma_f32 v197,-v196,v196,v197
v_max_f32 v197,0,v197
v_add_f32 v198,s77,v197
v_fma_f32 v199,s77,v198,v197
v_rsq_f32 v205,v198
v_rsq_f32 v198,v199
v_mov_b32 v200,v196
v_mul_f32_e64 v204,-v196,v205
v_mul_f32 v203,v199,v198
v_mul_f32 v201,s78,v198
v_mul_f32 v199,v198,v198
v_mul_f32 v202,s79,v199
v_mul_f32 v202,v202,v201
s_lshr_b32 s63,s63,1
s_sub_u32 s63,s89,s63
buffer_store_dwordx4 v[200:203],v4,s[36:39],s89 offen offset:0 sc0 sc1
buffer_store_dwordx2 v[204:205],v10,s[36:39],s63 offen offset:256 sc0 sc1
s_add_u32 m0,s66,s52
s_cmp_ge_u32 s51,12582912
s_cbranch_scc1 Lr_ys5_%=
buffer_load_dwordx4 v11,s[36:39],s51 offen offset:0 sc1 lds
buffer_load_dwordx4 v11,s[36:39],s51 offen offset:1024 sc1 lds
buffer_load_dwordx4 v11,s[36:39],s51 offen offset:2048 sc1 lds
buffer_load_dwordx4 v11,s[36:39],s51 offen offset:3072 sc1 lds
s_lshl_b32 s63,s50,14
s_add_u32 s51,s51,s63
Lr_ys5_%=:
s_add_u32 s66,s66,16384
s_cmp_eq_u32 s66,151552
s_cselect_b32 s66,4096,s66
v_add_u32 v18,s66,v11
s_mov_b32 s60,s90
s_waitcnt vmcnt(7)
s_branch Lr_it0_%=
Lr_tc1_%=:
s_add_u32 s63,s87,71303168
buffer_load_dwordx4 a[0:3],v11,s[36:39],s63 offen offset:0 sc1
buffer_load_dwordx4 a[4:7],v11,s[36:39],s63 offen offset:1024 sc1
buffer_load_dwordx4 a[8:11],v11,s[36:39],s63 offen offset:2048 sc1
buffer_load_dwordx4 a[12:15],v11,s[36:39],s63 offen offset:3072 sc1
s_add_u32 s63,s87,89128960
buffer_load_dwordx4 a[16:19],v11,s[36:39],s63 offen offset:0 sc1
buffer_load_dwordx4 a[20:23],v11,s[36:39],s63 offen offset:1024 sc1
buffer_load_dwordx4 a[24:27],v11,s[36:39],s63 offen offset:2048 sc1
buffer_load_dwordx4 a[28:31],v11,s[36:39],s63 offen offset:3072 sc1
s_add_u32 s63,s87,99614720
buffer_load_dwordx4 a[32:35],v11,s[36:39],s63 offen offset:0 sc1
buffer_load_dwordx4 a[36:39],v11,s[36:39],s63 offen offset:1024 sc1
buffer_load_dwordx4 a[40:43],v11,s[36:39],s63 offen offset:2048 sc1
buffer_load_dwordx4 a[44:47],v11,s[36:39],s63 offen offset:3072 sc1
s_add_u32 s63,s87,109051904
buffer_load_dwordx4 a[48:51],v11,s[36:39],s63 offen offset:0 sc1
buffer_load_dwordx4 a[52:55],v11,s[36:39],s63 offen offset:1024 sc1
buffer_load_dwordx4 a[56:59],v11,s[36:39],s63 offen offset:2048 sc1
buffer_load_dwordx4 a[60:63],v11,s[36:39],s63 offen offset:3072 sc1
buffer_load_dword v8,v6,s[36:39],s69 offen offset:0 sc1
s_waitcnt vmcnt(17)
buffer_store_dword v7,v5,s[36:39],s60 offen offset:0 sc0 sc1
s_mov_b32 s60,536870912
s_waitcnt vmcnt(1)
s_branch Lr_it0_%=
Lr_done_%=:
s_waitcnt vmcnt(0) lgkmcnt(0)
s_barrier
Lcode_r_e:)"
  :: "s"(ws), "s"(sK), "s"(sStep), "s"(sRq), "s"(sEpR), "v"(vRP), "v"(vRF), "v"(vRwr), "v"(vRrd), "v"(vRS), "v"(vFlag), "v"(vL4), "v"(vRL) : "v0", "v1", "v2", "v3", "v4", "v5", "v6", "v7", "v8", "v9", "v10", "v11", "v12", "v13", "v14", "v15", "v16", "v17", "v18", "v19", "v20", "v21", "v22", "v23", "v24", "v25", "v26", "v27", "v28", "v29", "v30", "v31", "v32", "v33", "v34", "v35", "v36", "v37", "v38", "v39", "v40", "v41", "v42", "v43", "v44", "v45", "v46", "v47", "v48", "v49", "v50", "v51", "v52", "v53", "v54", "v55", "v56", "v57", "v58", "v59", "v60", "v61", "v62", "v63", "v64", "v65", "v66", "v67", "v68", "v69", "v70", "v71", "v72", "v73", "v74", "v75", "v76", "v77", "v78", "v79", "v80", "v81", "v82", "v83", "v84", "v85", "v86", "v87", "v88", "v89", "v90", "v91", "v92", "v93", "v94", "v95", "v96", "v97", "v98", "v99", "v100", "v101", "v102", "v103", "v104", "v105", "v106", "v107", "v108", "v109", "v110", "v111", "v112", "v113", "v114", "v115", "v116", "v117", "v118", "v119", "v120", "v121", "v122", "v123", "v124", "v125", "v126", "v127", "v128", "v129", "v130", "v131", "v132", "v133", "v134", "v135", "v136", "v137", "v138", "v139", "v140", "v141", "v142", "v143", "v144", "v145", "v146", "v147", "v148", "v149", "v150", "v151", "v152", "v153", "v154", "v155", "v156", "v157", "v158", "v159", "v160", "v161", "v162", "v163", "v164", "v165", "v166", "v167", "v168", "v169", "v170", "v171", "v172", "v173", "v174", "v175", "v176", "v177", "v178", "v179", "v180", "v181", "v182", "v183", "v184", "v185", "v186", "v187", "v188", "v189", "v190", "v191", "v192", "v193", "v194", "v195", "v196", "v197", "v198", "v199", "v200", "v201", "v202", "v203", "v204", "v205", "v206", "v207", "v208", "v209", "v210", "v211", "v212", "v213", "v214", "v215", "v216", "v217", "v218", "v219", "v220", "v221", "v222", "v223", "v224", "v225", "v226", "v227", "v228", "v229", "v230", "v231", "v255", "a0", "a1", "a2", "a3", "a4", "a5", "a6", "a7", "a8", "a9", "a10", "a11", "a12", "a13", "a14", "a15", "a16", "a17", "a18", "a19", "a20", "a21", "a22", "a23", "a24", "a25", "a26", "a27", "a28", "a29", "a30", "a31", "a32", "a33", "a34", "a35", "a36", "a37", "a38", "a39", "a40", "a41", "a42", "a43", "a44", "a45", "a46", "a47", "a48", "a49", "a50", "a51", "a52", "a53", "a54", "a55", "a56", "a57", "a58", "a59", "a60", "a61", "a62", "a63", "a64", "a65", "a66", "a67", "a68", "a69", "a70", "a71", "a72", "a73", "a74", "a75", "a76", "a77", "a78", "a79", "a80", "a81", "a82", "a83", "a84", "a85", "a86", "a87", "a88", "a89", "a90", "a91", "a92", "a93", "a94", "a95", "a96", "a97", "a98", "a99", "a100", "a101", "a102", "a103", "a104", "a105", "a106", "a107", "a108", "a109", "a110", "a111", "a112", "a113", "a114", "a115", "a116", "a117", "a118", "a119", "a120", "a121", "a122", "a123", "a124", "a125", "a126", "a127", "a128", "a129", "a130", "a131", "a132", "a133", "a134", "a135", "a136", "a137", "a138", "a139", "a140", "a141", "a142", "a143", "a144", "a145", "a146", "a147", "a148", "a149", "a150", "a151", "a152", "a153", "a154", "a155", "a156", "a157", "a158", "a159", "a160", "a161", "a162", "a163", "a164", "a165", "a166", "a167", "a168", "a169", "a170", "a171", "a172", "a173", "a174", "a175", "a176", "a177", "a178", "a179", "a180", "a181", "a182", "a183", "a184", "a185", "a186", "a187", "a188", "a189", "a190", "a191", "a192", "a193", "a194", "a195", "a196", "a197", "a198", "a199", "a200", "a201", "a202", "a203", "a204", "a205", "a206", "a207", "a208", "a209", "a210", "a211", "a212", "a213", "a214", "a215", "a216", "a217", "a218", "a219", "a220", "a221", "a222", "a223", "a224", "a225", "a226", "a227", "a228", "a229", "a230", "a231", "a232", "a233", "a234", "a235", "a236", "a237", "a238", "a239", "a240", "a241", "a242", "a243", "a244", "a245", "a246", "a247", "a248", "a249", "a250", "a251", "a252", "a253", "a254", "a255", "s36", "s37", "s38", "s39", "s40", "s41", "s42", "s43", "s44", "s45", "s46", "s47", "s48", "s49", "s50", "s51", "s52", "s53", "s54", "s55", "s56", "s57", "s58", "s59", "s60", "s61", "s62", "s63", "s64", "s65", "s66", "s67", "s68", "s69", "s70", "s71", "s72", "s73", "s74", "s75", "s76", "s77", "s78", "s79", "s80", "s81", "s82", "s83", "s84", "s85", "s86", "s87", "s88", "s89", "s90", "s91", "s92", "s93", "s94", "s95", "s96", "s97", "s98", "s99", "vcc", "scc", "memory");
    }
    LANE_ID(lane2);
    const int wk = kind == 6 ? slice : NRW + (kind - 1) * 32 + rep * 8 + slice;
    for (int t = wk; t < 256 - FINE; t += 82) row_last(ws, out, t, lane2, wave, ep);
    if (wk * 4 + wave < FINE * 16) chunk_last(ws, out, (256 - FINE) * 16 + wk * 4 + wave, lane2, ep);
    if (b == 0 && lane2 == 0 && wave == 0) __hip_atomic_store((gu32*)(ws + OFF_EP), ep, __ATOMIC_RELAXED, __HIP_MEMORY_SCOPE_AGENT);
    asm volatile("s_endpgm" ::: "memory");
    __builtin_unreachable();
  }
}

extern "C" void kernel_launch(void* const* d_in, const int* in_sizes, int n_in, void* d_out, int out_size,
                              void* d_ws, size_t ws_size, hipStream_t stream) {
  const u16* q = (const u16*)d_in[0];
  const u16* k = (const u16*)d_in[1];
  const u16* v = (const u16*)d_in[2];
  const u16* shift_k = (const u16*)d_in[3];
  const u16* ssm_k = (const u16*)d_in[4];
  const u16* ssm_D = (const u16*)d_in[5];
  const u16* w1 = (const u16*)d_in[7];
  const u16* w2 = (const u16*)d_in[8];
  const u16* w3 = (const u16*)d_in[9];
  const u16* w4 = (const u16*)d_in[10];
  flow_kernel<<<256, 256, 0, stream>>>(q, k, v, shift_k, ssm_k, ssm_D, w1, w2, w3, w4, (char*)d_ws, (u16*)d_out);
}
```

```cpp
#include <hip/hip_runtime.h>
#include <hip/hip_bf16.h>
#include <stdint.h>
#include <type_traits>

typedef short s16x8 __attribute__((ext_vector_type(8)));
typedef short s16x4 __attribute__((ext_vector_type(4)));
typedef float f32x4 __attribute__((ext_vector_type(4)));
typedef unsigned short u16;
typedef unsigned short u16x8 __attribute__((ext_vector_type(8)));
typedef unsigned short u16x4 __attribute__((ext_vector_type(4)));
typedef float f32x2 __attribute__((ext_vector_type(2)));
typedef __bf16 bf16x2 __attribute__((ext_vector_type(2)));
typedef unsigned u32x4v __attribute__((ext_vector_type(4)));
typedef __attribute__((address_space(1))) unsigned gu32;

constexpr int S_ = 2048, D_ = 1024, F_ = 3584;
#define PF 1

constexpr int KE = 1, RT1 = 0, LB = 66, FL_BYTES = 2141312, WS_USED = 122727552;
constexpr int OFF_KVR = 4194304, OFF_YF = 8388608, OFF_XP = 12582912, OFF_HS = 22085632, OFF_X1 = 31457280, OFF_G = 40894464, OFF_P0 = 71303168, OFF_DBG = 80740352, OFF_P1 = 89128960, OFF_QS = 98566144, OFF_P2 = 99614720, OFF_P3 = 109051904, OFF_S1 = 118489088, OFF_ST = 119013376, OFF_FL = 120586240, OFF_FXP = 120587264, OFF_FH = 120859648, OFF_FX1 = 121132032, OFF_FP = 121405440, OFF_FG = 121950720, OFF_FKV = 122629120, OFF_FY = 122661888, OFF_EP = 122727424;

#define WS_DIAG 0

__device__ __forceinline__ float bf2f(u16 v) { return __uint_as_float(((unsigned)v) << 16); }
__device__ __forceinline__ u16 f2bf(float f) { return __builtin_bit_cast(u16, (__bf16)f); }
__device__ __forceinline__ float rbf(float f) { return bf2f(f2bf(f)); }
__device__ __forceinline__ void st16(void* p, u16x8 v) {
  asm volatile("global_store_dwordx4 %0, %1, off sc0 sc1\n\ts_nop 1" :: "v"(p), "v"(v) : "memory");
}
__device__ __forceinline__ void bst16(u32x4v v, __amdgpu_buffer_rsrc_t rs, unsigned vo, unsigned so) {
  asm volatile("buffer_store_dwordx4 %0, %1, %2, %3 offen sc0 sc1\n\ts_nop 1" :: "v"(v), "v"(vo), "s"(rs), "s"(so) : "memory");
}
#define WAIT_VM(n) asm volatile("s_waitcnt vmcnt(%0)" ::"n"(n) : "memory")
#define WAIT_LGKM0() asm volatile("s_waitcnt lgkmcnt(0)" ::: "memory")

#define CODE_RANGE(B, E, p, n)                                                                                   \
  const char* p; unsigned n;                                                                                     \
  { unsigned lo_, hi_;                                                                                           \
    asm volatile("s_getpc_b64 s[98:99]\nLpc%=:\n\ts_add_u32 %0, s98, (" B "-Lpc%=)&4294967295\n\ts_addc_u32 %1, s99, (" B "-Lpc%=)>>32\n\t" \
                 "s_mov_b32 %2, " E "-" B : "=s"(lo_), "=s"(hi_), "=s"(n) :: "s98", "s99", "scc");               \
    p = (const char*)(((unsigned long long)hi_ << 32) | lo_); }
__device__ __forceinline__ unsigned touch(const char* p, unsigned n, unsigned off) {
  unsigned t;
  asm volatile("global_load_dword %0, %1, off" : "=v"(t) : "v"(p + (off < n ? off : 0u)) : "memory");
  return t;
}

__device__ __forceinline__ void touch_lds(const char* p, unsigned a) {
  asm volatile("s_mov_b32 m0, %1\n\ts_nop 0\n\tglobal_load_lds_dword %0, off" :: "v"(p), "s"(a) : "memory");
}
#define PFPOL ""
__device__ __forceinline__ void touch_pol(const char* p, unsigned a) {
  asm volatile("s_mov_b32 m0, %1\n\ts_nop 0\n\tglobal_load_lds_dword %0, off " PFPOL :: "v"(p), "s"(a) : "memory");
}
#define YSLEEP 4
#define KSLEEP 2
#define CTS(i)
#define CTS_INIT()
#define CTS_FLUSH()
__device__ __forceinline__ f32x2 up2(unsigned p) { return (f32x2){__uint_as_float(p << 16), __uint_as_float(p & 0xffff0000u)}; }
__device__ __forceinline__ unsigned pk2(f32x2 v) { return __builtin_bit_cast(unsigned, __builtin_convertvector(v, bf16x2)); }
__device__ __forceinline__ f32x2 rbf2(f32x2 v) { return up2(pk2(v)); }
#define BARRIER() asm volatile("s_waitcnt lgkmcnt(0)\n\ts_barrier" ::: "memory")
template <int N, class F> __device__ __forceinline__ void sfor(F&& f) {
  if constexpr (N > 0) { sfor<N - 1>(f); f(std::integral_constant<int, N - 1>{}); }
}
template <int NL>
__device__ __forceinline__ void poll_flags(const char* p, int n, int lane, unsigned ep) {
#pragma unroll 1
  for (int it = 0; it < 20000; ++it) {
    bool ok = true;
#pragma unroll
    for (int i = 0; i < NL; ++i) {
      const int f = lane + 64 * i;
      ok &= __hip_atomic_load((gu32*)(p + (f < n ? f : 0) * 128), __ATOMIC_RELAXED, __HIP_MEMORY_SCOPE_AGENT) == ep;
    }
    if (__all(ok)) break;
    __builtin_amdgcn_s_sleep(YSLEEP);
  }
  asm volatile("" ::: "memory");
}

constexpr int CV_HP = (2048 + 32) * 2;
constexpr int CV_UR = (2048 + 256) * 2;
constexpr int CV_CH = 2 * CV_HP + CV_UR;
#define T0 0
#define T0R 4
#define T1 0
#define LATEQ 4
constexpr int CV_STG = 8 * CV_CH;

struct KvRegs { u32x4v k0, k1, k2, k3, k4, k5, k6, v0, v1, v2, v3; };
struct SkRegs { u32x4v s0, s1, s2, s3; };
#define LD16(d, p, pol) asm volatile("global_load_dwordx4 %0, %1, off " pol : "=v"(d) : "v"(p) : "memory")
#define LDS16(d, vo, sp, o, pol) asm volatile("global_load_dwordx4 %0, %1, %2 offset:" #o " " pol : "=v"(d) : "v"(vo), "s"(sp) : "memory")
__device__ __forceinline__ void kv_load(KvRegs& R, const u16* __restrict__ kin, const u16* __restrict__ vin, int b, int tid) {
  const unsigned tr = (b >> 2) * 32 + 4 * (tid >> 5), oa = tr * 2048 + (b & 3) * 512 + 16 * (tid & 31) + 4096;
  LDS16(R.k3, oa, kin, -4096, "nt"); LDS16(R.k4, oa, kin, -2048, "nt"); LDS16(R.k5, oa, kin, 0, "nt"); LDS16(R.k6, oa, kin, 2048, "nt");
  LDS16(R.v0, oa, vin, -4096, "nt"); LDS16(R.v1, oa, vin, -2048, "nt"); LDS16(R.v2, oa, vin, 0, "nt"); LDS16(R.v3, oa, vin, 2048, "nt");
  const unsigned ob = tr ? oa - 6144 : oa;
  LDS16(R.k0, ob, kin, -4096, "nt"); LDS16(R.k1, ob, kin, -2048, "nt"); LDS16(R.k2, ob, kin, 0, "nt");
}
__device__ __forceinline__ void sk_load(SkRegs& R, const u16* __restrict__ shift_k, int b, int tid) {
  const unsigned o = (b & 3) * 2048 + 64 * (tid & 31);
  LDS16(R.s0, o, shift_k, 0, ""); LDS16(R.s1, o, shift_k, 16, ""); LDS16(R.s2, o, shift_k, 32, ""); LDS16(R.s3, o, shift_k, 48, "");
}
template <int N>
__device__ __forceinline__ void kv_wait(KvRegs& R, SkRegs& Q) {
  asm volatile("s_waitcnt vmcnt(%15)" : "+v"(R.k0), "+v"(R.k1), "+v"(R.k2), "+v"(R.k3), "+v"(R.k4), "+v"(R.k5), "+v"(R.k6), "+v"(R.v0), "+v"(R.v1), "+v"(R.v2),
               "+v"(R.v3), "+v"(Q.s0), "+v"(Q.s1), "+v"(Q.s2), "+v"(Q.s3) : "n"(N) : "memory");
}
__device__ __forceinline__ void kv_store(KvRegs& R, const SkRegs& Q, char* __restrict__ ws, char* T, int b, int tid) {
  const int t0 = (b >> 2) * 32, c0 = (b & 3) * 256, ch8 = tid & 31, rg = tid >> 5;
  if (t0 + 4 * rg < 3) R.k0 = R.k1 = R.k2 = (u32x4v){0u, 0u, 0u, 0u};
  typedef unsigned u32x2 __attribute__((ext_vector_type(2)));
  const u32x4v kr[7] = {R.k0, R.k1, R.k2, R.k3, R.k4, R.k5, R.k6}, vr[4] = {R.v0, R.v1, R.v2, R.v3}, sk[4] = {Q.s0, Q.s1, Q.s2, Q.s3};
#pragma unroll
  for (int j2 = 0; j2 < 4; ++j2) {
    const f32x2 sa = up2(sk[j2][0]), sb = up2(sk[j2][1]), sc = up2(sk[j2][2]), sd = up2(sk[j2][3]);
    const f32x2 s0 = {sa[0], sc[0]}, s1 = {sa[1], sc[1]}, s2 = {sb[0], sd[0]}, s3 = {sb[1], sd[1]};
    f32x2 kf[7], o[4];
#pragma unroll
    for (int r = 0; r < 7; ++r) kf[r] = up2(kr[r][j2]);
#pragma unroll
    for (int r = 0; r < 4; ++r)
      o[r] = __builtin_elementwise_fma(kf[r + 3], s0, __builtin_elementwise_fma(kf[r + 2], s1, __builtin_elementwise_fma(kf[r + 1], s2, kf[r] * s3))) * up2(vr[r][j2]);
#pragma unroll
    for (int e = 0; e < 2; ++e)
      *(u32x2*)(T + ch8 * 520 + (2 * j2 + e) * 64 + (28 - 4 * rg) * 2) = (u32x2){pk2((f32x2){o[3][e], o[2][e]}), pk2((f32x2){o[1][e], o[0][e]})};
  }
  BARRIER();
  u16* kvr = (u16*)(ws + OFF_KVR);
#pragma unroll
  for (int i = 0; i < 4; ++i) {
    const int ch = (tid >> 2) + 64 * i, cb = tid & 3;
    const char* p = T + ch * 64 + (ch >> 3) * 8 + cb * 16;
    const u16x4 lo = *(const u16x4*)p, hi = *(const u16x4*)(p + 8);
    st16(kvr + (size_t)(c0 + ch) * S_ + (2016 - t0) + cb * 8, __builtin_shufflevector(lo, hi, 0, 1, 2, 3, 4, 5, 6, 7));
  }
}

typedef unsigned u32x2v __attribute__((ext_vector_type(2)));
__device__ __forceinline__ void h_load(u32x4v (&hv)[8], unsigned& hx, unsigned& dD, const u16* __restrict__ ssm_k, const u16* __restrict__ ssm_D, int c0, int tid) {
  const __amdgpu_buffer_rsrc_t rs = __builtin_amdgcn_make_buffer_rsrc((void*)ssm_k, 0, c0 < 0 ? 0 : D_ * S_ * 2, 0x00020000);
  const unsigned cb = c0 < 0 ? 0 : c0, lane = tid & 63, wave = tid >> 6;
#pragma unroll
  for (int i = 0; i < 8; ++i) asm volatile("buffer_load_dwordx4 %0, %1, %2, %3 offen nt" : "=v"(hv[i]) : "v"(tid * 16), "s"(rs), "s"((cb + i) * 4096) : "memory");
  asm volatile("buffer_load_dword %0, %1, %2, %3 offen" : "=v"(hx) : "v"((lane & 7) * 4096 + ((wave + 1) & 3) * 1024), "s"(rs), "s"(cb * 4096) : "memory");
  asm volatile("global_load_dword %0, %1, %2" : "=v"(dD) : "v"(cb * 2 + wave * 4), "s"(ssm_D) : "memory");
}
template <int N>
__device__ __forceinline__ void h_wait(u32x4v (&hv)[8], unsigned& hx, unsigned& dD) {
  asm volatile("s_waitcnt vmcnt(%10)" : "+v"(hv[0]), "+v"(hv[1]), "+v"(hv[2]), "+v"(hv[3]), "+v"(hv[4]), "+v"(hv[5]), "+v"(hv[6]), "+v"(hv[7]), "+v"(hx), "+v"(dD) : "n"(N) : "memory");
}
__device__ __forceinline__ void h_spread(const u32x4v (&hv)[8], unsigned hx, char* lds, int tid) {
  if (tid < 32 && (tid & 3) != 3) *(u32x4v*)(lds + (tid >> 2) * CV_CH + ((tid >> 1) & 1) * CV_HP + (tid & 1) * 16) = (u32x4v){0u, 0u, 0u, 0u};
  *(u32x4v*)(lds + (tid >> 5) * CV_CH + 2 * CV_HP + 4096 + (tid & 31) * 16) = (u32x4v){0u, 0u, 0u, 0u};
#pragma unroll
  for (int i = 0; i < 8; ++i) {
    char* base = lds + i * CV_CH + 32 + tid * 16;
    unsigned hn = __builtin_amdgcn_update_dpp(0, (int)hv[i][0], 0x130, 0xf, 0xf, true);
    const unsigned last = __builtin_amdgcn_readlane(hx, i);
    if ((tid & 63) == 63) hn = last;
#define AB(a, b) __builtin_amdgcn_alignbyte(a, b, 2)
    if (tid == 0) *(u32x4v*)(lds + i * CV_CH + CV_HP + 16) = (u32x4v){0u, 0u, 0u, hv[i][0] << 16};
    *(u32x4v*)(base) = hv[i];
    *(u32x4v*)(base + CV_HP) = (u32x4v){AB(hv[i][1], hv[i][0]), AB(hv[i][2], hv[i][1]), AB(hv[i][3], hv[i][2]), AB(hn, hv[i][3])};
#undef AB
  }
}

__device__ __forceinline__ void pads(char* __restrict__ ws, int w, int lane, unsigned ep) {
  if (w < 132) {
    const int k = w < 128 ? w >> 5 : w - 128, u = k == 0 ? -1 : k == 1 ? 128 : k == 2 ? 131 : 260;
    st16(ws + (w < 128 ? OFF_HS + u * 32768 + (w & 31) * 1024 : OFF_FH + u * 1024) + lane * 16, __builtin_bit_cast(u16x8, (u32x4v){w >= 128 && !(lane & 7) ? ep : 0u, 0u, 0u, 0u}));
  }
}
__device__ __forceinline__ void prewrite(char* __restrict__ ws, unsigned off, unsigned str, int rep, int tid) {
#pragma unroll
  for (int i = 0; i < 2; ++i) st16(ws + off + (rep + 4 * (tid >> 3) + 132 * i) * str + (tid & 7) * 16, __builtin_bit_cast(u16x8, (u32x4v){0u, 0u, 0u, 0u}));
}

__device__ __forceinline__ void conv_part(char* __restrict__ ws, unsigned dD, char* lds, int g, int tid, int lane, int wave, unsigned ep, const char* wp, unsigned wstep) {
  const __amdgpu_buffer_rsrc_t rs = __builtin_amdgcn_make_buffer_rsrc(ws, 0, WS_USED, 0x00020000);
  const u16* kvr = (const u16*)(ws + OFF_KVR) + (size_t)(8 * g + 2 * wave) * S_;
  auto wait1 = [&](unsigned off) {
#pragma unroll 1
    for (int it = 0; it < 20000; ++it) {
      if (__all(__hip_atomic_load((gu32*)(ws + off), __ATOMIC_RELAXED, __HIP_MEMORY_SCOPE_AGENT) == ep)) break;
      __builtin_amdgcn_s_sleep(KSLEEP);
    }
    asm volatile("" ::: "memory");
  };
  const unsigned lb = (unsigned)(size_t)lds;
  auto u_fetch = [&](int part) {
#pragma unroll
    for (int j = 0; j < 2; ++j)
      asm volatile("s_mov_b32 m0, %1\n\ts_nop 0\n\tglobal_load_lds_dwordx4 %0, off sc1" :: "v"(kvr + j * S_ + part * 512 + lane * 8),
                   "s"(lb + (2 * wave + j) * CV_CH + 2 * CV_HP + part * 1024) : "memory");
  };
  const unsigned xo = (tid >> 4) * 32768 + g * 256 + (tid & 15) * 16;
  u32x4v qv[6];
  u32x2v qs[6];
  auto q_fetch = [&](int s0, int ns) {
#pragma unroll
    for (int i = 0; i < 6; ++i)
      if (i < ns) {
        asm volatile("buffer_load_dwordx4 %0, %1, %2, %3 offen sc1" : "=v"(qv[i]) : "v"(xo), "s"(rs), "s"(OFF_XP + (s0 + i) * 524288) : "memory");
        asm volatile("buffer_load_dwordx2 %0, %1, %2, %3 offen sc1" : "=v"(qs[i]) : "v"(tid * 8), "s"(rs), "s"(OFF_QS + (s0 + i) * 2048) : "memory");
      }
  };
  const int cg = g >> 5;
  const int l15 = lane & 15, lg = lane >> 4, cp = (l15 + 1) & 1;
  const unsigned ao = cp * CV_HP + 2 * (8 * lg + (l15 + 1 - cp)), bo = 2 * CV_HP + 2 * (2032 - 16 * l15 + 8 * lg) - 2 * 256 * 7;
  const unsigned own = lb + 2 * wave * CV_CH;
  char* stg = lds + CV_STG;
  s16x8 ad[2];
#pragma unroll
  for (int j = 0; j < 2; ++j) {
    const int e = 15 - l15 - 8 * lg;
    const unsigned d = (j ? dD >> 16 : dD & 0xffffu) << ((e & 1) * 16);
    ad[j] = __builtin_bit_cast(s16x8, (u32x4v){e >> 1 == 0 ? d : 0u, e >> 1 == 1 ? d : 0u, e >> 1 == 2 ? d : 0u, e >> 1 == 3 ? d : 0u});
  }
  f32x4 acc[2][6];
  unsigned f0, f1;
  bool mid;
  auto finish = [&](auto s0_c, auto ns_c) {
    constexpr int S0 = decltype(s0_c)::value, NS = decltype(ns_c)::value, A0 = S0 == 2 ? 0 : 4, Q0 = S0 == 6 ? 4 : 0;
#pragma unroll
    for (int s = 0; s < NS; ++s)
#pragma unroll
      for (int r = 0; r < 4; ++r) *(unsigned*)(stg + (Q0 + s) * 4352 + l15 * 272 + (4 * lg + r) * 16 + wave * 4) = pk2((f32x2){acc[0][A0 + s][r], acc[1][A0 + s][r]});
    if (S0 == 0) {
      asm volatile("s_waitcnt vmcnt(0)" : "+v"(qv[0]), "+v"(qv[1]), "+v"(qs[0]), "+v"(qs[1]), "+v"(f0) :: "memory");
      mid = __all(f0 == ep);
      if (mid) { u_fetch(2); u_fetch(1); }
    } else if (S0 == 2) asm volatile("" : "+v"(qv[0]), "+v"(qv[1]), "+v"(qv[2]), "+v"(qv[3]), "+v"(qv[4]), "+v"(qv[5]), "+v"(qs[0]), "+v"(qs[1]), "+v"(qs[2]), "+v"(qs[3]), "+v"(qs[4]), "+v"(qs[5]) :: "memory");
    BARRIER();
    u32x4v y4[NS];
#pragma unroll
    for (int s = 0; s < NS; ++s) {
      y4[s] = *(const u32x4v*)(stg + (Q0 + s) * 4352 + (tid >> 4) * 272 + (tid & 15) * 16);
      const f32x2 rd2 = {__uint_as_float(qs[Q0 + s][0]), __uint_as_float(qs[Q0 + s][0])}, nm2 = {__uint_as_float(qs[Q0 + s][1]), __uint_as_float(qs[Q0 + s][1])};
      u32x4v o;
#pragma unroll
      for (int e = 0; e < 4; ++e) {
        const f32x2 x = up2(qv[Q0 + s][e]);
        o[e] = pk2(__builtin_elementwise_fma(__builtin_elementwise_fma(x, rd2, nm2), up2(y4[s][e]), x));
      }
      bst16(o, rs, xo, OFF_XP + (S0 + s) * 524288);
    }
#pragma unroll
    for (int s = 0; s < NS; ++s) bst16(y4[s], rs, (tid >> 3) * 16384 + g * 128 + (tid & 7) * 16, OFF_YF + (S0 + s) * 524288);
    CTS(S0 ? 7 : 5);
  };
  auto touches = [&](int n) {
#pragma unroll
    for (int i = 0; i < 3; ++i)
      if (i < n) { touch_lds(wp, lb + 163584); wp += wstep; }
  };
  auto count = [&](int s0, int ns, auto n_c) {
    WAIT_VM(decltype(n_c)::value);
    BARRIER();
    if (tid < 4 * ns) __hip_atomic_fetch_add((gu32*)(ws + OFF_FXP + (s0 + (tid >> 2)) * 16384 + (tid & 3) * 1024 + (g >> 4) * 128), 1u, __ATOMIC_RELAXED, __HIP_MEMORY_SCOPE_AGENT);
  };
  auto pair = [&](unsigned bb) {
    u32x2v ax[2][2][2][2];
    s16x8 bx[2][2][2];
    auto rdA = [&](int q) {
#pragma unroll
      for (int j = 0; j < 2; ++j)
#pragma unroll
        for (int m = 0; m < 2; ++m) {
          asm volatile("ds_read2_b32 %0, %1 offset0:%2 offset1:%3" : "=v"(ax[q & 1][j][m][0]) : "v"(own + j * CV_CH + ao), "n"(128 * m + 16 * q), "n"(128 * m + 16 * q + 1));
          asm volatile("ds_read2_b32 %0, %1 offset0:%2 offset1:%3" : "=v"(ax[q & 1][j][m][1]) : "v"(own + j * CV_CH + ao), "n"(128 * m + 16 * q + 2), "n"(128 * m + 16 * q + 3));
          asm volatile("ds_read_b128 %0, %1 offset:%2" : "=v"(bx[q & 1][j][m]) : "v"(bb), "n"(j * CV_CH + 512 * (1 - m) + 64 * q));
        }
    };
    rdA(0);
    sfor<8>([&](auto q_c) {
      constexpr int q = decltype(q_c)::value, p = q & 1;
      if (q + 1 < 8) rdA(q + 1);
      asm volatile("s_waitcnt lgkmcnt(%0)" :: "n"(q + 1 < 8 ? 12 : 0) : "memory");
      __builtin_amdgcn_sched_barrier(0);
#pragma unroll
      for (int j = 0; j < 2; ++j) {
        const s16x8 a0f = __builtin_bit_cast(s16x8, __builtin_shufflevector(ax[p][j][0][0], ax[p][j][0][1], 0, 1, 2, 3)), a1f = __builtin_bit_cast(s16x8, __builtin_shufflevector(ax[p][j][1][0], ax[p][j][1][1], 0, 1, 2, 3));
        if (q == 0) {
          acc[j][4] = __builtin_amdgcn_mfma_f32_16x16x32_bf16(ad[j], bx[p][j][0], acc[j][4], 0, 0, 0);
          acc[j][5] = __builtin_amdgcn_mfma_f32_16x16x32_bf16(ad[j], bx[p][j][1], acc[j][5], 0, 0, 0);
        }
        acc[j][4] = __builtin_amdgcn_mfma_f32_16x16x32_bf16(a0f, bx[p][j][0], acc[j][4], 0, 0, 0);
        acc[j][5] = __builtin_amdgcn_mfma_f32_16x16x32_bf16(a0f, bx[p][j][1], acc[j][5], 0, 0, 0);
        acc[j][5] = __builtin_amdgcn_mfma_f32_16x16x32_bf16(a1f, bx[p][j][0], acc[j][5], 0, 0, 0);
      }
      __builtin_amdgcn_sched_barrier(0);
    });
  };
  wait1(OFF_FKV + ((lane & 15) * 4 + cg) * 128);
  CTS(3);
  u_fetch(3);
  wait1(OFF_FY + (lane & 31) * 128);
  q_fetch(0, 2);
  const unsigned fm = OFF_FKV + ((16 + (lane & 31)) * 4 + cg) * 128;
  asm volatile("global_load_dword %0, %1, %2 sc1" : "=v"(f0) : "v"(fm), "s"(ws) : "memory");
  CTS(4);
#pragma unroll
  for (int i = 0; i < 4; ++i) acc[i >> 1][4 + (i & 1)] = (f32x4){0.f, 0.f, 0.f, 0.f};
  pair(own + bo + 512 * 6);
  finish(std::integral_constant<int, 0>{}, std::integral_constant<int, 2>{});
  u32x2v a0[8][2], a1[8][2];
  s16x8 b0[6], b1[6];
  unsigned pa[4], pb = own + bo;
#pragma unroll
  for (int i = 0; i < 4; ++i) pa[i] = own + ao + 1024 * i;
  auto rd1 = [&](int rr, int i, u32x2v (&ah)[8][2], s16x8 (&bfr)[6]) {
    const int m = i < 18 ? i / 3 : 6 + (i - 18) / 2, e = i < 18 ? i % 3 : i & 1;
    if (e < 2) asm volatile("ds_read2_b32 %0, %1 offset0:%2 offset1:%3" : "=v"(ah[m][e & 1]) : "v"(pa[m >> 1]), "n"(128 * (m & 1) + 2 * e + 16 * rr), "n"(128 * (m & 1) + 2 * e + 16 * rr + 1));
    else asm volatile("ds_read_b128 %0, %1 offset:%2" : "=v"(bfr[m < 6 ? m : 0]) : "v"(pb), "n"(512 * (7 - m) + 64 * rr));
  };
  auto round = [&](f32x4 (&ac)[6], u32x2v (&ah)[8][2], s16x8 (&bfr)[6], u32x2v (&ahn)[8][2], s16x8 (&bfn)[6], int rr, unsigned step) {
    int k = 0;
#pragma unroll
    for (int s = 2; s <= 7; ++s)
#pragma unroll
      for (int m = 0; m <= s; ++m) {
        if (s - m > 5) continue;
        const s16x8 af = __builtin_bit_cast(s16x8, __builtin_shufflevector(ah[m][0], ah[m][1], 0, 1, 2, 3));
        ac[s - 2] = __builtin_amdgcn_mfma_f32_16x16x32_bf16(af, bfr[s - m > 5 ? 0 : s - m], ac[s - 2], 0, 0, 0);
        __builtin_amdgcn_sched_barrier(0);
        if (m == s) asm volatile("s_waitcnt lgkmcnt(12)" ::: "memory");
#pragma unroll
        for (int i = 0; i < 22; ++i)
          if (i * 4 / 3 == k) {
            rd1(rr, i, ahn, bfn);
            if (rr && (i == 4 || i == 10 || i == 16 || i == 21)) asm volatile("v_add_u32 %0, %1, %0" : "+v"(pa[i / 6]) : "s"(step));
            if (rr && i == 17) asm volatile("v_add_u32 %0, %1, %0" : "+v"(pb) : "s"(step));
          }
        ++k;
        __builtin_amdgcn_sched_barrier(0);
      }
  };
  if (!mid) { wait1(fm); u_fetch(2); u_fetch(1); WAIT_VM(0); }
  WAIT_VM(4);
#pragma unroll
  for (int i = 0; i < 22; ++i) rd1(0, i, a0, b0);
  WAIT_LGKM0();
  auto channel = [&](auto j_c, auto r0_c, auto r1_c) {
    constexpr int j = decltype(j_c)::value, r0 = decltype(r0_c)::value, r1 = decltype(r1_c)::value;
    __builtin_amdgcn_sched_barrier(0);
    if (r0 == 0) {
#pragma unroll
      for (int s = 2; s <= 7; ++s) acc[j][s - 2] = s < 6 ? __builtin_amdgcn_mfma_f32_16x16x32_bf16(ad[j], b0[s], (f32x4){0.f, 0.f, 0.f, 0.f}, 0, 0, 0) : (f32x4){0.f, 0.f, 0.f, 0.f};
    }
#pragma unroll 1
    for (int r = r0; r < r1; r += 2) {
      if (j == 0 && r == 2) {
        asm volatile("s_waitcnt vmcnt(0)\n\ts_barrier" ::: "memory");
        if (tid < 8) __hip_atomic_fetch_add((gu32*)(ws + OFF_FXP + (tid >> 2) * 16384 + (tid & 3) * 1024 + (g >> 4) * 128), 1u, __ATOMIC_RELAXED, __HIP_MEMORY_SCOPE_AGENT);
      }
      if (T0 && j == 0 && r == T0R) touches(T0);
      round(acc[j], a0, b0, a1, b1, 1, r < 6 ? 128 : j ? 0 : CV_CH - 384);
      round(acc[j], a1, b1, a0, b0, 0, 0);
    }
    WAIT_LGKM0();
    __builtin_amdgcn_sched_barrier(0);
  };
  typedef std::integral_constant<int, 0> I0;
  typedef std::integral_constant<int, 1> I1;
  channel(I0{}, I0{}, std::integral_constant<int, 8>{});
  CTS(13);
  channel(I1{}, I0{}, std::integral_constant<int, LATEQ - 2>{});
  const unsigned fa = OFF_FY + (32 + lane) * 128, fb = lane < 32 ? OFF_FY + (96 + lane) * 128 : OFF_FKV + ((48 + (lane & 15)) * 4 + cg) * 128;
  asm volatile("global_load_dword %0, %1, %2 sc1" : "=v"(f0) : "v"(fa), "s"(ws) : "memory");
  asm volatile("global_load_dword %0, %1, %2 sc1" : "=v"(f1) : "v"(fb), "s"(ws) : "memory");
  channel(I1{}, std::integral_constant<int, LATEQ - 2>{}, std::integral_constant<int, LATEQ>{});
  asm volatile("s_waitcnt vmcnt(0)" : "+v"(f0), "+v"(f1) :: "memory");
  if (!__all(f0 == ep && f1 == ep)) { wait1(fa); wait1(fb); }
  u_fetch(0);
  q_fetch(2, 6);
  touches(T1);
  CTS(15);
  channel(I1{}, std::integral_constant<int, LATEQ>{}, std::integral_constant<int, 8>{});
  WAIT_VM(T1);
  CTS(14);
  finish(std::integral_constant<int, 2>{}, std::integral_constant<int, 4>{});
  pair(own + bo);
  count(2, 4, I0{});
  CTS(11);
  finish(std::integral_constant<int, 6>{}, std::integral_constant<int, 2>{});
  touches(3 - T0 - T1);
  count(6, 2, std::integral_constant<int, 3 - T0 - T1>{});
}
__device__ __forceinline__ float sum2_seg(float s, float q) {
  s += __builtin_bit_cast(float, __builtin_amdgcn_update_dpp(0, __builtin_bit_cast(int, s), 0x128, 0xf, 0xf, true));
  q += __builtin_bit_cast(float, __builtin_amdgcn_update_dpp(0, __builtin_bit_cast(int, q), 0x128, 0xf, 0xf, true));
  asm volatile("s_nop 1\n\tv_permlane16_swap_b32 %0, %1" : "+v"(s), "+v"(q));
  s += q;
  q = s;
  asm volatile("s_nop 1\n\tv_permlane32_swap_b32 %0, %1" : "+v"(s), "+v"(q));
  return s + q;
}
#define LASTSLEEP 8
__device__ __forceinline__ void wait_flags(const char* p, int lane, int n, unsigned ep) {
  gu32* f = (gu32*)(p + (lane < n ? lane : 0) * 128);
#pragma unroll 1
  for (int it = 0; it < 10000; ++it) {
    if (__all(__hip_atomic_load(f, __ATOMIC_RELAXED, __HIP_MEMORY_SCOPE_AGENT) == ep)) break;
    __builtin_amdgcn_s_sleep(LASTSLEEP);
  }
  asm volatile("" ::: "memory");
}
struct QRegs { u32x4v a[4], b[4]; };
__device__ __forceinline__ void q_load(QRegs& R, const u16* __restrict__ q, int f, int lane, int wave) {
  const unsigned o = (f * 16 + (lane & 7)) * 2048 + wave * 512 + (lane >> 3) * 16, o2 = o + 16384;
  LDS16(R.a[0], o, q, 0, "nt"); LDS16(R.b[0], o2, q, 0, "nt"); LDS16(R.a[1], o, q, 128, "nt"); LDS16(R.b[1], o2, q, 128, "nt");
  LDS16(R.a[2], o, q, 256, "nt"); LDS16(R.b[2], o2, q, 256, "nt"); LDS16(R.a[3], o, q, 384, "nt"); LDS16(R.b[3], o2, q, 384, "nt");
}
template <int N>
__device__ __forceinline__ void q_wait(QRegs& R, unsigned& e) {
  asm volatile("s_waitcnt vmcnt(%9)" : "+v"(R.a[0]), "+v"(R.a[1]), "+v"(R.a[2]), "+v"(R.a[3]), "+v"(R.b[0]), "+v"(R.b[1]), "+v"(R.b[2]), "+v"(R.b[3]), "+v"(e) : "n"(N) : "memory");
}
__device__ __forceinline__ void row_stats(const u32x4v (&xq)[4], char* lds, int k, int lane, int wave, float& mean, float& var) {
  f32x2* red = (f32x2*)(lds + 16640);
  const int r8 = lane & 7, seg = lane >> 3;
  f32x2 s = {0.f, 0.f}, sq = {0.f, 0.f};
#pragma unroll
  for (int i = 0; i < 16; ++i) { const f32x2 x = up2(xq[i >> 2][i & 3]); s += x; sq = __builtin_elementwise_fma(x, x, sq); }
  const float a = sum2_seg(s[0] + s[1], sq[0] + sq[1]);
  if ((seg & 5) == 0) ((float*)red)[((k * 4 + wave) * 8 + r8) * 2 + (seg >> 1)] = a;
  BARRIER();
  const f32x2 t = (red[(k * 4) * 8 + r8] + red[(k * 4 + 1) * 8 + r8]) + (red[(k * 4 + 2) * 8 + r8] + red[(k * 4 + 3) * 8 + r8]);
  mean = t[0] * (1.0f / 1024.0f);
  var = fmaxf(t[1] * (1.0f / 1024.0f) - mean * mean, 0.f);
}
__device__ __forceinline__ void q_store(char* __restrict__ ws, int t, int lane, int wave, const u32x4v (&xq)[4], float mean, float var) {
  const __amdgpu_buffer_rsrc_t rs = __builtin_amdgcn_make_buffer_rsrc(ws, 0, WS_USED, 0x00020000);
  const int r8 = lane & 7, seg = lane >> 3;
  const unsigned fo = (t >> 1) * 32768 + (wave * 8 + (seg >> 2)) * 1024 + ((seg & 3) * 16 + (t & 1) * 8 + r8) * 16;
#pragma unroll
  for (int i = 0; i < 4; ++i) bst16(xq[i], rs, fo + i * 2048, OFF_XP);
  const float rd = __builtin_amdgcn_rsqf(var + 1e-5f);
  if (wave == 0 && seg == 0) __builtin_amdgcn_raw_buffer_store_b64((u32x2v){__float_as_uint(rd), __float_as_uint(-mean * rd)}, rs, t * 64 + r8 * 8, OFF_QS, 17);
}
__device__ __forceinline__ void row_last(char* __restrict__ ws, u16* __restrict__ out, int t, int lane, int wave, unsigned ep) {
  const __amdgpu_buffer_rsrc_t rs = __builtin_amdgcn_make_buffer_rsrc(ws, 0, WS_USED, 0x00020000);
  const int r8 = lane & 7, seg = lane >> 3;
  const unsigned fo = (4 * LB + t) * 16384 + wave * 4096 + lane * 16;
  wait_flags(ws + OFF_FP + (2 * LB + (t >> 1)) * 2048, lane, 16, ep);
  u32x4v p0[4], p1[4], p2[4], p3[4];
#pragma unroll
  for (int i = 0; i < 4; ++i) {
    p0[i] = __builtin_bit_cast(u32x4v, __builtin_amdgcn_raw_buffer_load_b128(rs, fo + i * 1024, OFF_P0, 16));
    p1[i] = __builtin_bit_cast(u32x4v, __builtin_amdgcn_raw_buffer_load_b128(rs, fo + i * 1024, OFF_P1, 16));
    p2[i] = __builtin_bit_cast(u32x4v, __builtin_amdgcn_raw_buffer_load_b128(rs, fo + i * 1024, OFF_P2, 16));
    p3[i] = __builtin_bit_cast(u32x4v, __builtin_amdgcn_raw_buffer_load_b128(rs, fo + i * 1024, OFF_P3, 16));
  }
#pragma unroll
  for (int i = 0; i < 4; ++i) {
    u32x4v o;
#pragma unroll
    for (int e = 0; e < 4; ++e) o[e] = pk2((up2(p0[i][e]) + up2(p1[i][e])) + (up2(p2[i][e]) + up2(p3[i][e])));
    st16(out + (size_t)(t * 8 + r8) * D_ + wave * 256 + i * 64 + seg * 8, __builtin_bit_cast(u16x8, o));
  }
}

#define FINE 20
__device__ __forceinline__ void chunk_last(char* __restrict__ ws, u16* __restrict__ out, int c, int lane, unsigned ep) {
  const __amdgpu_buffer_rsrc_t rs = __builtin_amdgcn_make_buffer_rsrc(ws, 0, WS_USED, 0x00020000);
  const unsigned fo = 4 * LB * 16384 + c * 1024 + lane * 16;
  gu32* f = (gu32*)(ws + OFF_FP + (2 * LB + (c >> 5)) * 2048 + ((lane & 3) * 4 + ((c >> 2) & 3)) * 128);
#pragma unroll 1
  for (int it = 0; it < 20000; ++it) {
    if (__all(__hip_atomic_load(f, __ATOMIC_RELAXED, __HIP_MEMORY_SCOPE_AGENT) == ep)) break;
    __builtin_amdgcn_s_sleep(4);
  }
  asm volatile("" ::: "memory");
  const u32x4v p0 = __builtin_bit_cast(u32x4v, __builtin_amdgcn_raw_buffer_load_b128(rs, fo, OFF_P0, 16)), p1 = __builtin_bit_cast(u32x4v, __builtin_amdgcn_raw_buffer_load_b128(rs, fo, OFF_P1, 16));
  const u32x4v p2 = __builtin_bit_cast(u32x4v, __builtin_amdgcn_raw_buffer_load_b128(rs, fo, OFF_P2, 16)), p3 = __builtin_bit_cast(u32x4v, __builtin_amdgcn_raw_buffer_load_b128(rs, fo, OFF_P3, 16));
  u32x4v o;
#pragma unroll
  for (int e = 0; e < 4; ++e) o[e] = pk2((up2(p0[e]) + up2(p1[e])) + (up2(p2[e]) + up2(p3[e])));
  st16(out + (size_t)((c >> 4) * 8 + (lane & 7)) * D_ + (c & 15) * 64 + (lane >> 3) * 8, __builtin_bit_cast(u16x8, o));
}

__device__ __forceinline__ int swzB(int r) { return ((((r >> 1) & 1) | (((r >> 3) & 1) << 1)) << 1); }
#define FLW 1
#define MAP3 1
#define MAP12 1
#define FLIP 0
#define KVL 0
#define NRW 18
#define PFROWS 110
__global__ __launch_bounds__(256, 1) void flow_kernel(const u16* __restrict__ q, const u16* __restrict__ kin, const u16* __restrict__ vin,
                                                      const u16* __restrict__ shift_k, const u16* __restrict__ ssm_k, const u16* __restrict__ ssm_D,
                                                      const u16* __restrict__ w1, const u16* __restrict__ w2,
                                                      const u16* __restrict__ w3, const u16* __restrict__ w4, char* __restrict__ ws,
                                                      u16* __restrict__ out) {
  __shared__ __attribute__((aligned(16))) char lds[163840];
  const int tid = threadIdx.x, lane = tid & 63, wave = __builtin_amdgcn_readfirstlane(tid >> 6);
  const int b = blockIdx.x, xcd = b & 7, idx = b >> 3;
  const unsigned sLds = (unsigned)(size_t)lds, sScr = sLds + 163584;
  CTS_INIT();
  CTS(0);
  asm volatile("s_getpc_b64 s[98:99]\n\ts_mov_b32 m0, %1\n\ts_nop 0\n\tglobal_load_lds_dword %0, s[98:99]" :: "v"(tid * 128), "s"(sScr) : "s98", "s99", "memory");
  asm volatile("" :: "s"(q), "s"(kin), "s"(vin), "s"(shift_k), "s"(ssm_k), "s"(ssm_D), "s"(w1), "s"(w2), "s"(w3), "s"(w4), "s"(ws), "s"(out));
  int kind, rep, slice, kh = 0;
  const int P = xcd >> 1, par = (xcd & 1) ^ FLIP, li = idx - (par ? 20 : 12);
  const int qq = (par ? 0 : 12) + idx - 8;
  auto code_touch = [&]() {
    unsigned long long cp;
    unsigned n;
    asm volatile("s_getpc_b64 %0\nLpd%=:\n"
                 "s_mov_b32 s98, Lcode_r_b-Lpd%=\ns_mov_b32 %1, Lcode_r_e-Lcode_r_b\n"
                 "s_cmp_eq_u32 %2, 1\ns_cselect_b32 s98, Lcode_g12_b-Lpd%=, s98\ns_cselect_b32 %1, Lcode_g2_b-Lcode_g12_b, %1\n"
                 "s_cmp_eq_u32 %2, 2\ns_cselect_b32 s98, Lcode_g2_b-Lpd%=, s98\ns_cselect_b32 %1, Lcode_g12_e-Lcode_g2_b, %1\n"
                 "s_cmp_eq_u32 %2, 3\ns_cselect_b32 s98, Lcode_g3_b-Lpd%=, s98\ns_cselect_b32 %1, Lcode_g3_e-Lcode_g3_b, %1\n"
                 "s_cmp_eq_u32 %2, 4\ns_cselect_b32 s98, Lcode_g4_b-Lpd%=, s98\ns_cselect_b32 %1, Lcode_g4_e-Lcode_g4_b, %1\n"
                 "s_cmp_eq_u32 %2, 5\ns_cselect_b32 s98, Lcode_g3o_b-Lpd%=, s98\ns_cselect_b32 %1, Lcode_g3o_e-Lcode_g3o_b, %1\n"
                 "s_ashr_i32 s99, s98, 31\ns_mov_b64 vcc, %0\ns_add_u32 s98, s98, vcc_lo\ns_addc_u32 s99, s99, vcc_hi\ns_mov_b64 %0, s[98:99]"
                 : "=&s"(cp), "=&s"(n) : "s"(kind) : "s98", "s99", "scc", "vcc");
    asm volatile("s_mov_b32 m0, %3\n\ts_nop 0\n\tglobal_load_lds_dword %0, %2\n\tglobal_load_lds_dword %1, %2"
                 :: "v"(min(tid * 128u, n - 4)), "v"(min(32768 + tid * 128u, n - 4)), "s"(cp), "s"(sScr) : "memory");
  };
  unsigned ep = 0;
#define EP_LOAD() asm volatile("global_load_dword %0, %1, %2 sc1" : "=v"(ep) : "v"(OFF_EP), "s"(ws) : "memory")
  auto epoch = [&]() { const unsigned e = __builtin_amdgcn_readfirstlane(ep) + 1u; return e ? e : 1u; };
  auto q_unit = [&](QRegs& QR, int f) {
    float ma, va, mb, vb_;
    row_stats(QR.a, lds + CV_STG, 0, lane, wave, ma, va);
    row_stats(QR.b, lds + CV_STG, 1, lane, wave, mb, vb_);
    q_store(ws, 2 * f, lane, wave, QR.a, ma, va);
    q_store(ws, 2 * f + 1, lane, wave, QR.b, mb, vb_);
  };
  auto publish = [&](unsigned off) {
    BARRIER();
    if (tid == 0) __hip_atomic_store((gu32*)(ws + off), ep, __ATOMIC_RELAXED, __HIP_MEMORY_SCOPE_AGENT);
  };
  if (idx < 8 && par == MAP12) {
    kind = 1; rep = P; slice = idx;
    code_touch();
    prewrite(ws, OFF_FH + slice * 128, 1024, rep, tid);
  } else {
    const int cv = (li + (par ? 20 : 0)) * 4 + P, n = idx < 8 ? 64 + P * 8 + idx : qq * 4 + P;
    QRegs QR;
    if (idx >= 8) {
      KvRegs KR;
      SkRegs SK;
      if (li >= 0) {
        const int tile = 64 + cv;
        u32x4v hv[8];
        unsigned hx, dD;
        h_load(hv, hx, dD, ssm_k, ssm_D, 8 * cv, tid);
        EP_LOAD();
        sk_load(SK, shift_k, tile, tid);
        kv_load(KR, kin, vin, tile, tid);
        {
          int l2 = li, p2 = par;
          asm volatile("" : "+s"(l2), "+s"(p2));
          const int nl = 8 - (P & 1), l = ((15 * P + 1) >> 1) + l2, tl = (l * 205) >> 10, kl = l - 5 * tl;
          kind = 4; rep = l2 >> 2; slice = l2 & 3; kh = P;
          if (p2 == 1) {
            kind = l2 < nl ? 3 : 6; rep = l2 < nl ? 3 + ((kl & 1) | (kl >> 2)) : 0;
            slice = l2 < nl ? 3 * tl + (kl >> 1) : ((9 * P) >> 1) + l2 - nl;
          }
        }
        touch_lds((const char*)((par ^ MAP12) ? w2 : w1) + (size_t)(par ? (li < 8 ? li * 256 + tid : 0) : (li < 16 ? li * 128 + (tid & 127) : 0)) * 1024 + 384, sScr);
        code_touch();
        h_wait<18>(hv, hx, dD);
        asm volatile("" : "+v"(ep));
        ep = epoch();
        CTS(12);
        h_spread(hv, hx, lds, tid);
        kv_wait<2>(KR, SK);
        kv_store(KR, SK, ws, lds + CV_STG, tile, tid);
        CTS(1);
        WAIT_VM(0);
        publish(OFF_FKV + tile * 128);
        CTS(2);
        const char* wp = (const char*)w4 + (par ? 0 : (size_t)(kh * 896 + li * 15) * 2048 + min(tid, 239) * 128);
        conv_part(ws, dD, lds, cv, tid, lane, wave, ep, wp, par ? 0 : 298 * 2048);
        CTS(8);
      } else {
        kv_load(KR, kin, vin, n, tid);
        sk_load(SK, shift_k, n, tid);
        EP_LOAD();
        {
          int q2 = qq;
          asm volatile("" : "+s"(q2));
          const int e = 15 * P + q2, t = (e * 205) >> 11, k = e - 10 * t, k3 = min((k * 11) >> 5, 2);
          kind = q2 < 15 ? 3 : 5; rep = q2 < 15 ? k - 3 * k3 : P; slice = q2 < 15 ? 3 * t + k3 : 18;
        }
        kv_wait<0>(KR, SK);
        asm volatile("" : "+v"(ep));
        ep = epoch();
        CTS(12);
        kv_store(KR, SK, ws, lds + CV_STG, n, tid);
        CTS(1);
        pads(ws, n * 4 + wave, lane, ep);
        if (n < 8)
          st16(ws + OFF_FXP + (lane >> 3) * 16384 + wave * 1024 + (2 * (n & 3) + (n >> 2)) * 128 + (lane & 7) * 16, __builtin_bit_cast(u16x8, (u32x4v){ep - 16u, 0u, 0u, 0u}));
        kv_load(KR, kin, vin, 192 + n, tid);
        WAIT_VM(11);
        publish(OFF_FKV + n * 128);
        CTS(2);
        q_load(QR, q, 32 + n, lane, wave);
        kv_wait<8>(KR, SK);
        kv_store(KR, SK, ws, lds, 192 + n, tid);
        q_wait<0>(QR, ep);
        publish(OFF_FKV + (192 + n) * 128);
        CTS(3);
      }
    } else {
      QRegs Q0;
      q_load(Q0, q, n - 64, lane, wave);
      EP_LOAD();
      kind = 2; rep = P; slice = idx;
      q_wait<0>(Q0, ep);
      ep = epoch();
      CTS(12);
      q_load(QR, q, 32 + n, lane, wave);
      q_unit(Q0, n - 64);
      CTS(1);
      q_wait<0>(QR, ep);
      publish(OFF_FY + (n - 64) * 128);
      CTS(2);
    }
    if (li < 0) {
      q_unit(QR, 32 + n);
      {
        const char* wp = (const char*)w2 + (size_t)(n < 64 ? 0 : (n - 64) * 512 + tid) * 128;
        touch_lds(wp, sScr);
        touch_lds(wp + (n < 64 ? 0 : 32768), sScr);
      }
      WAIT_VM(2);
      publish(OFF_FY + (32 + n) * 128);
      CTS(9);
      if (n >= 64) {
        prewrite(ws, OFF_FX1 + slice * 128, 1024, rep, tid);
        prewrite(ws, OFF_FP + slice * 128, 2048, rep, tid);
        prewrite(ws, OFF_FP + (8 + slice) * 128, 2048, rep, tid);
        prewrite(ws, OFF_FG + slice * 128, 2560, rep, tid);
        prewrite(ws, OFF_FG + (8 + slice) * 128, 2560, rep, tid);
        if (slice < 3) prewrite(ws, OFF_FG + (16 + slice) * 128, 2560, rep, tid);
        st16(ws + OFF_FXP + slice * 128 + (132 + rep + 4 * (tid >> 3)) * 1024 + (tid & 7) * 16, __builtin_bit_cast(u16x8, (u32x4v){0u, 0u, 0u, 0u}));
      }
      code_touch();
      if (n < 64) {
#pragma unroll
        for (int i = 0; i < 4; ++i) {
          const unsigned x = rep * 1024 + i * 256 + tid, row = kind == 3 ? (x * 21846) >> 16 : x >> 1;
          if (rep < (kind == 3 ? 3 : 2)) touch_lds((const char*)w3 + (size_t)row * 7168 + slice * 384 + (x - row * (kind == 3 ? 3 : 2)) * 128, sScr);
        }
      }
    }
    BARRIER();
    CTS(10);
  }
  CTS_FLUSH();
  const unsigned sDbg0 = OFF_DBG + (b * 4 + wave) * 8192;
#define LANE_ID(x) int x; asm volatile("v_mbcnt_lo_u32_b32 %0, -1, 0\n\tv_mbcnt_hi_u32_b32 %0, -1, %0" : "=v"(x))
#define OOB 0x80000000u
#define COMMON(NF, FIN, FOUT, SN0, WS, LDW, NREP)                                                                                    \
  LANE_ID(ln);                                                                                                                       \
  const int l15 = ln & 15, lg = ln >> 4, lq = l15 >> 2, lp = ln & 3, rr = 8 * lg + lq;                                               \
  const unsigned vA = ln * 16, vFlag = (ln < FLW && wave == 3) ? (FOUT) + ln * 4 : OOB;                                                        \
  const unsigned vPollOff = wave < 2 && ln < (NF) ? (FIN) + ((NF) == 16 && ln >= 8 ? (ln & 2 ? ln : ln - 16) : ln) * 128 : OOB, vSnapOff = wave == 0 ? vPollOff : OOB;                   \
  const unsigned vSnapRd = (SN0) + (ln >> 5) * (WS) + ((ln & 31) < (NF) ? (ln & 31) : 0) * 4;                                        \
  const unsigned vWd0 = (ln >> 3) * (LDW) + ((ln & 7) ^ swzB(ln >> 3)) * 16, vWd1 = ((ln >> 3) + 8) * (LDW) + ((ln & 7) ^ swzB(8 + (ln >> 3))) * 16; \
  const unsigned vWr0 = (kind == 4 ? wave * 4096 : (wave >> 1) * 8192) + rr * 128 + (((kind == 4 ? kh >> 1 : wave & 1) * 4 + lp) ^ swzB(rr)) * 16, vWr1 = vWr0 + 8;                  \
  const unsigned vWr2 = kind == 4 ? vWr0 ^ 64 : 16384 + rr * 128 + ((2 * wave + (lp >> 1)) ^ swzB(rr)) * 16 + (lp & 1) * 8;                                  \
                                            \
  const int res = kind == 3 ? rep + 3 * KE : rep;                     \
  const unsigned sStep = (NREP) << 15, sU0 = (rep < 3 ? rep : res) << 15, sX1 = ((NREP) == 4 ? 128 + rep : 128 + (res + 2) % 5) << 15; \
  const unsigned sX2 = ((NREP) == 4 ? 260 + rep : 260 + (res + 4) % 5) << 15, sDbg = sDbg0, sBuf = kind != 3 ? kind : (rep < 3 ? 3 : 5) << 15
  constexpr unsigned AUX = 131072;
  if (kind == 3 || kind == 5) {
    COMMON(8, OFF_FX1, OFF_FG + slice * 128, AUX + 1536, 2048, 7168, kind == 3 ? 5 : 4);
    const unsigned vSrc = OFF_X1 + wave * 4096 + vA, vOut = OFF_G + (slice * 6 + wave) * 1024 + vA;
    const unsigned vOut2 = OFF_G + (slice * 6 + 4 + (wave >> 1)) * 1024 + (((wave & 1) * 2 + (lg >> 1)) * 16 + l15) * 16 + (lg & 1) * 8;
    const unsigned vAuxS = OFF_ST + (ln & 7) * 128 + (wave * 8 + (ln >> 3)) * 4, vX = AUX + wave * 2048 + ln * 4;
    const unsigned vStW = (ln & 7) == 0 ? AUX + 1280 + (wave * 8 + (ln >> 3)) * 4 : AUX + 24576 + wave * 2048 + 1536 + ln * 4, vStR = AUX + 1280 + l15 * 8;
    const unsigned sWw = 16 * wave * 7168 + slice * 384;
    if (kind == 3) { asm volatile(R"(
Lcode_g3_b:
s_mov_b64 s[36:37],%0
s_mov_b32 s38,122727552
s_mov_b32 s39,0x00020000
s_mov_b32 s54,%1
s_mov_b32 s55,%2
s_mov_b32 s56,%3
s_mov_b32 s58,%4
s_mov_b32 s83,%5
s_mov_b32 s82,%6
s_mov_b32 s84,%7
v_mov_b32 v0,%8
v_mov_b32 v2,%9
v_mov_b32 v3,%10
v_mov_b32 v4,%11
v_mov_b32 v6,%12
v_mov_b32 v7,%13
v_mov_b32 v8,%14
v_mov_b32 v9,%15
v_mov_b32 v10,%16
v_mov_b32 v11,%17
v_mov_b32 v28,%18
v_mov_b32 v29,%19
v_mov_b32 v30,%20
v_mov_b32 v31,%21
v_mov_b32 v32,%22
s_mov_b64 s[40:41],%23
s_mov_b32 s42,7340032
s_mov_b32 s43,0x00020000
s_lshl_b32 s88,s54,11
s_mov_b32 s69,122727424
buffer_load_dword v5,off,s[36:39],s69 sc1
s_add_u32 m0,s88,0
s_add_u32 s61,s82,0
buffer_load_dwordx4 v28,s[40:43],s61 offen offset:0  lds
s_add_u32 m0,s88,1024
s_nop 0
buffer_load_dwordx4 v29,s[40:43],s61 offen offset:0  lds
s_add_u32 m0,s88,8064
s_nop 0
buffer_load_dwordx4 v28,s[40:43],s61 offen offset:128  lds
s_add_u32 m0,s88,9088
s_nop 0
buffer_load_dwordx4 v29,s[40:43],s61 offen offset:128  lds
s_add_u32 m0,s88,16128
s_nop 0
buffer_load_dwordx4 v28,s[40:43],s61 offen offset:256  lds
s_add_u32 m0,s88,17152
s_nop 0
buffer_load_dwordx4 v29,s[40:43],s61 offen offset:256  lds
s_add_u32 m0,s88,24576
s_add_u32 s61,s82,458752
buffer_load_dwordx4 v28,s[40:43],s61 offen offset:0  lds
s_add_u32 m0,s88,25600
s_nop 0
buffer_load_dwordx4 v29,s[40:43],s61 offen offset:0  lds
s_add_u32 m0,s88,32640
s_nop 0
buffer_load_dwordx4 v28,s[40:43],s61 offen offset:128  lds
s_add_u32 m0,s88,33664
s_nop 0
buffer_load_dwordx4 v29,s[40:43],s61 offen offset:128  lds
s_add_u32 m0,s88,40704
s_nop 0
buffer_load_dwordx4 v28,s[40:43],s61 offen offset:256  lds
s_add_u32 m0,s88,41728
s_nop 0
buffer_load_dwordx4 v29,s[40:43],s61 offen offset:256  lds
s_add_u32 m0,s88,49152
s_add_u32 s61,s82,917504
buffer_load_dwordx4 v28,s[40:43],s61 offen offset:0  lds
s_add_u32 m0,s88,50176
s_nop 0
buffer_load_dwordx4 v29,s[40:43],s61 offen offset:0  lds
s_add_u32 m0,s88,57216
s_nop 0
buffer_load_dwordx4 v28,s[40:43],s61 offen offset:128  lds
s_add_u32 m0,s88,58240
s_nop 0
buffer_load_dwordx4 v29,s[40:43],s61 offen offset:128  lds
s_add_u32 m0,s88,65280
s_nop 0
buffer_load_dwordx4 v28,s[40:43],s61 offen offset:256  lds
s_add_u32 m0,s88,66304
s_nop 0
buffer_load_dwordx4 v29,s[40:43],s61 offen offset:256  lds
s_add_u32 m0,s88,73728
s_add_u32 s61,s82,1376256
buffer_load_dwordx4 v28,s[40:43],s61 offen offset:0  lds
s_add_u32 m0,s88,74752
s_nop 0
buffer_load_dwordx4 v29,s[40:43],s61 offen offset:0  lds
s_add_u32 m0,s88,81792
s_nop 0
buffer_load_dwordx4 v28,s[40:43],s61 offen offset:128  lds
s_add_u32 m0,s88,82816
s_nop 0
buffer_load_dwordx4 v29,s[40:43],s61 offen offset:128  lds
s_add_u32 m0,s88,89856
s_nop 0
buffer_load_dwordx4 v28,s[40:43],s61 offen offset:256  lds
s_add_u32 m0,s88,90880
s_nop 0
buffer_load_dwordx4 v29,s[40:43],s61 offen offset:256  lds
v_add_u32 v40,49152,v30
v_add_u32 v41,49152,v31
v_add_u32 v42,49152,v32
v_add_u32 v43,98304,v30
v_add_u32 v44,98304,v31
v_add_u32 v45,98304,v32
v_mov_b32 v84,0x3f803f80
v_mov_b32 v85,0x3f803f80
v_mov_b32 v86,0x3f803f80
v_mov_b32 v87,0x3f803f80
s_waitcnt vmcnt(18)
s_barrier
s_add_u32 m0,s88,98304
s_add_u32 s61,s82,1835008
buffer_load_dwordx4 v28,s[40:43],s61 offen offset:0  lds
s_add_u32 m0,s88,99328
ds_read_b64_tr_b16 a[0:1],v30 offset:0
buffer_load_dwordx4 v29,s[40:43],s61 offen offset:0  lds
s_add_u32 m0,s88,106368
ds_read_b64_tr_b16 a[2:3],v30 offset:512
buffer_load_dwordx4 v28,s[40:43],s61 offen offset:128  lds
s_add_u32 m0,s88,107392
ds_read_b64_tr_b16 a[4:5],v31 offset:0
buffer_load_dwordx4 v29,s[40:43],s61 offen offset:128  lds
s_add_u32 m0,s88,114432
ds_read_b64_tr_b16 a[6:7],v31 offset:512
buffer_load_dwordx4 v28,s[40:43],s61 offen offset:256  lds
s_add_u32 m0,s88,115456
ds_read_b64_tr_b16 a[8:9],v32 offset:0
buffer_load_dwordx4 v29,s[40:43],s61 offen offset:256  lds
ds_read_b64_tr_b16 a[10:11],v32 offset:512
ds_read_b64_tr_b16 a[12:13],v30 offset:4096
ds_read_b64_tr_b16 a[14:15],v30 offset:4608
ds_read_b64_tr_b16 a[16:17],v31 offset:4096
ds_read_b64_tr_b16 a[18:19],v31 offset:4608
ds_read_b64_tr_b16 a[20:21],v32 offset:4096
ds_read_b64_tr_b16 a[22:23],v32 offset:4608
s_waitcnt vmcnt(18)
s_barrier
s_add_u32 m0,s88,122880
s_add_u32 s61,s82,2293760
buffer_load_dwordx4 v28,s[40:43],s61 offen offset:0  lds
s_add_u32 m0,s88,123904
ds_read_b64_tr_b16 a[24:25],v30 offset:24576
buffer_load_dwordx4 v29,s[40:43],s61 offen offset:0  lds
s_add_u32 m0,s88,130944
ds_read_b64_tr_b16 a[26:27],v30 offset:25088
buffer_load_dwordx4 v28,s[40:43],s61 offen offset:128  lds
s_add_u32 m0,s88,131968
ds_read_b64_tr_b16 a[28:29],v31 offset:24576
buffer_load_dwordx4 v29,s[40:43],s61 offen offset:128  lds
s_add_u32 m0,s88,139008
ds_read_b64_tr_b16 a[30:31],v31 offset:25088
buffer_load_dwordx4 v28,s[40:43],s61 offen offset:256  lds
s_add_u32 m0,s88,140032
ds_read_b64_tr_b16 a[32:33],v32 offset:24576
buffer_load_dwordx4 v29,s[40:43],s61 offen offset:256  lds
ds_read_b64_tr_b16 a[34:35],v32 offset:25088
ds_read_b64_tr_b16 a[36:37],v30 offset:28672
ds_read_b64_tr_b16 a[38:39],v30 offset:29184
ds_read_b64_tr_b16 a[40:41],v31 offset:28672
ds_read_b64_tr_b16 a[42:43],v31 offset:29184
ds_read_b64_tr_b16 a[44:45],v32 offset:28672
ds_read_b64_tr_b16 a[46:47],v32 offset:29184
s_waitcnt lgkmcnt(12)
v_mfma_f32_16x16x32_bf16 v[16:19],a[0:3],v[84:87],0
v_mfma_f32_16x16x32_bf16 v[20:23],a[4:7],v[84:87],0
v_mfma_f32_16x16x32_bf16 v[24:27],a[8:11],v[84:87],0
v_mfma_f32_16x16x32_bf16 v[16:19],a[12:15],v[84:87],v[16:19]
v_mfma_f32_16x16x32_bf16 v[20:23],a[16:19],v[84:87],v[20:23]
v_mfma_f32_16x16x32_bf16 v[24:27],a[20:23],v[84:87],v[24:27]
s_waitcnt vmcnt(18)
s_barrier
s_add_u32 m0,s88,0
s_add_u32 s61,s82,2752512
buffer_load_dwordx4 v28,s[40:43],s61 offen offset:0  lds
s_add_u32 m0,s88,1024
ds_read_b64_tr_b16 a[48:49],v40 offset:0
buffer_load_dwordx4 v29,s[40:43],s61 offen offset:0  lds
s_add_u32 m0,s88,8064
ds_read_b64_tr_b16 a[50:51],v40 offset:512
buffer_load_dwordx4 v28,s[40:43],s61 offen offset:128  lds
s_add_u32 m0,s88,9088
ds_read_b64_tr_b16 a[52:53],v41 offset:0
buffer_load_dwordx4 v29,s[40:43],s61 offen offset:128  lds
s_add_u32 m0,s88,16128
ds_read_b64_tr_b16 a[54:55],v41 offset:512
buffer_load_dwordx4 v28,s[40:43],s61 offen offset:256  lds
s_add_u32 m0,s88,17152
ds_read_b64_tr_b16 a[56:57],v42 offset:0
buffer_load_dwordx4 v29,s[40:43],s61 offen offset:256  lds
ds_read_b64_tr_b16 a[58:59],v42 offset:512
ds_read_b64_tr_b16 a[60:61],v40 offset:4096
ds_read_b64_tr_b16 a[62:63],v40 offset:4608
ds_read_b64_tr_b16 a[64:65],v41 offset:4096
ds_read_b64_tr_b16 a[66:67],v41 offset:4608)"
R"(
ds_read_b64_tr_b16 a[68:69],v42 offset:4096
ds_read_b64_tr_b16 a[70:71],v42 offset:4608
s_waitcnt lgkmcnt(12)
v_mfma_f32_16x16x32_bf16 v[16:19],a[24:27],v[84:87],v[16:19]
v_mfma_f32_16x16x32_bf16 v[20:23],a[28:31],v[84:87],v[20:23]
v_mfma_f32_16x16x32_bf16 v[24:27],a[32:35],v[84:87],v[24:27]
v_mfma_f32_16x16x32_bf16 v[16:19],a[36:39],v[84:87],v[16:19]
v_mfma_f32_16x16x32_bf16 v[20:23],a[40:43],v[84:87],v[20:23]
v_mfma_f32_16x16x32_bf16 v[24:27],a[44:47],v[84:87],v[24:27]
s_waitcnt vmcnt(18)
s_barrier
s_add_u32 m0,s88,24576
s_add_u32 s61,s82,3211264
buffer_load_dwordx4 v28,s[40:43],s61 offen offset:0  lds
s_add_u32 m0,s88,25600
ds_read_b64_tr_b16 a[72:73],v40 offset:24576
buffer_load_dwordx4 v29,s[40:43],s61 offen offset:0  lds
s_add_u32 m0,s88,32640
ds_read_b64_tr_b16 a[74:75],v40 offset:25088
buffer_load_dwordx4 v28,s[40:43],s61 offen offset:128  lds
s_add_u32 m0,s88,33664
ds_read_b64_tr_b16 a[76:77],v41 offset:24576
buffer_load_dwordx4 v29,s[40:43],s61 offen offset:128  lds
s_add_u32 m0,s88,40704
ds_read_b64_tr_b16 a[78:79],v41 offset:25088
buffer_load_dwordx4 v28,s[40:43],s61 offen offset:256  lds
s_add_u32 m0,s88,41728
ds_read_b64_tr_b16 a[80:81],v42 offset:24576
buffer_load_dwordx4 v29,s[40:43],s61 offen offset:256  lds
ds_read_b64_tr_b16 a[82:83],v42 offset:25088
ds_read_b64_tr_b16 a[84:85],v40 offset:28672
ds_read_b64_tr_b16 a[86:87],v40 offset:29184
ds_read_b64_tr_b16 a[88:89],v41 offset:28672
ds_read_b64_tr_b16 a[90:91],v41 offset:29184
ds_read_b64_tr_b16 a[92:93],v42 offset:28672
ds_read_b64_tr_b16 a[94:95],v42 offset:29184
s_waitcnt lgkmcnt(12)
v_mfma_f32_16x16x32_bf16 v[16:19],a[48:51],v[84:87],v[16:19]
v_mfma_f32_16x16x32_bf16 v[20:23],a[52:55],v[84:87],v[20:23]
v_mfma_f32_16x16x32_bf16 v[24:27],a[56:59],v[84:87],v[24:27]
v_mfma_f32_16x16x32_bf16 v[16:19],a[60:63],v[84:87],v[16:19]
v_mfma_f32_16x16x32_bf16 v[20:23],a[64:67],v[84:87],v[20:23]
v_mfma_f32_16x16x32_bf16 v[24:27],a[68:71],v[84:87],v[24:27]
s_waitcnt vmcnt(18)
s_barrier
s_add_u32 m0,s88,49152
s_add_u32 s61,s82,3670016
buffer_load_dwordx4 v28,s[40:43],s61 offen offset:0  lds
s_add_u32 m0,s88,50176
ds_read_b64_tr_b16 a[96:97],v43 offset:0
buffer_load_dwordx4 v29,s[40:43],s61 offen offset:0  lds
s_add_u32 m0,s88,57216
ds_read_b64_tr_b16 a[98:99],v43 offset:512
buffer_load_dwordx4 v28,s[40:43],s61 offen offset:128  lds
s_add_u32 m0,s88,58240
ds_read_b64_tr_b16 a[100:101],v44 offset:0
buffer_load_dwordx4 v29,s[40:43],s61 offen offset:128  lds
s_add_u32 m0,s88,65280
ds_read_b64_tr_b16 a[102:103],v44 offset:512
buffer_load_dwordx4 v28,s[40:43],s61 offen offset:256  lds
s_add_u32 m0,s88,66304
ds_read_b64_tr_b16 a[104:105],v45 offset:0
buffer_load_dwordx4 v29,s[40:43],s61 offen offset:256  lds
ds_read_b64_tr_b16 a[106:107],v45 offset:512
ds_read_b64_tr_b16 a[108:109],v43 offset:4096
ds_read_b64_tr_b16 a[110:111],v43 offset:4608
ds_read_b64_tr_b16 a[112:113],v44 offset:4096
ds_read_b64_tr_b16 a[114:115],v44 offset:4608
ds_read_b64_tr_b16 a[116:117],v45 offset:4096
ds_read_b64_tr_b16 a[118:119],v45 offset:4608
s_waitcnt lgkmcnt(12)
v_mfma_f32_16x16x32_bf16 v[16:19],a[72:75],v[84:87],v[16:19]
v_mfma_f32_16x16x32_bf16 v[20:23],a[76:79],v[84:87],v[20:23]
v_mfma_f32_16x16x32_bf16 v[24:27],a[80:83],v[84:87],v[24:27]
v_mfma_f32_16x16x32_bf16 v[16:19],a[84:87],v[84:87],v[16:19]
v_mfma_f32_16x16x32_bf16 v[20:23],a[88:91],v[84:87],v[20:23]
v_mfma_f32_16x16x32_bf16 v[24:27],a[92:95],v[84:87],v[24:27]
s_waitcnt vmcnt(18)
s_barrier
s_add_u32 m0,s88,73728
s_add_u32 s61,s82,4128768
buffer_load_dwordx4 v28,s[40:43],s61 offen offset:0  lds
s_add_u32 m0,s88,74752
ds_read_b64_tr_b16 a[120:121],v43 offset:24576
buffer_load_dwordx4 v29,s[40:43],s61 offen offset:0  lds
s_add_u32 m0,s88,81792
ds_read_b64_tr_b16 a[122:123],v43 offset:25088
buffer_load_dwordx4 v28,s[40:43],s61 offen offset:128  lds
s_add_u32 m0,s88,82816
ds_read_b64_tr_b16 a[124:125],v44 offset:24576
buffer_load_dwordx4 v29,s[40:43],s61 offen offset:128  lds
s_add_u32 m0,s88,89856
ds_read_b64_tr_b16 a[126:127],v44 offset:25088
buffer_load_dwordx4 v28,s[40:43],s61 offen offset:256  lds
s_add_u32 m0,s88,90880
ds_read_b64_tr_b16 a[128:129],v45 offset:24576
buffer_load_dwordx4 v29,s[40:43],s61 offen offset:256  lds
ds_read_b64_tr_b16 a[130:131],v45 offset:25088
ds_read_b64_tr_b16 a[132:133],v43 offset:28672
ds_read_b64_tr_b16 a[134:135],v43 offset:29184
ds_read_b64_tr_b16 a[136:137],v44 offset:28672
ds_read_b64_tr_b16 a[138:139],v44 offset:29184
ds_read_b64_tr_b16 a[140:141],v45 offset:28672
ds_read_b64_tr_b16 a[142:143],v45 offset:29184
s_waitcnt lgkmcnt(12)
v_mfma_f32_16x16x32_bf16 v[16:19],a[96:99],v[84:87],v[16:19]
v_mfma_f32_16x16x32_bf16 v[20:23],a[100:103],v[84:87],v[20:23]
v_mfma_f32_16x16x32_bf16 v[24:27],a[104:107],v[84:87],v[24:27]
v_mfma_f32_16x16x32_bf16 v[16:19],a[108:111],v[84:87],v[16:19]
v_mfma_f32_16x16x32_bf16 v[20:23],a[112:115],v[84:87],v[20:23]
v_mfma_f32_16x16x32_bf16 v[24:27],a[116:119],v[84:87],v[24:27]
s_waitcnt vmcnt(18)
s_barrier
s_add_u32 m0,s88,98304
s_add_u32 s61,s82,4587520
buffer_load_dwordx4 v28,s[40:43],s61 offen offset:0  lds
s_add_u32 m0,s88,99328
ds_read_b64_tr_b16 a[144:145],v30 offset:0
buffer_load_dwordx4 v29,s[40:43],s61 offen offset:0  lds
s_add_u32 m0,s88,106368
ds_read_b64_tr_b16 a[146:147],v30 offset:512
buffer_load_dwordx4 v28,s[40:43],s61 offen offset:128  lds
s_add_u32 m0,s88,107392
ds_read_b64_tr_b16 a[148:149],v31 offset:0
buffer_load_dwordx4 v29,s[40:43],s61 offen offset:128  lds
s_add_u32 m0,s88,114432
ds_read_b64_tr_b16 a[150:151],v31 offset:512
buffer_load_dwordx4 v28,s[40:43],s61 offen offset:256  lds
s_add_u32 m0,s88,115456
ds_read_b64_tr_b16 a[152:153],v32 offset:0
buffer_load_dwordx4 v29,s[40:43],s61 offen offset:256  lds
ds_read_b64_tr_b16 a[154:155],v32 offset:512
ds_read_b64_tr_b16 a[156:157],v30 offset:4096
ds_read_b64_tr_b16 a[158:159],v30 offset:4608
ds_read_b64_tr_b16 a[160:161],v31 offset:4096
ds_read_b64_tr_b16 a[162:163],v31 offset:4608
ds_read_b64_tr_b16 a[164:165],v32 offset:4096
ds_read_b64_tr_b16 a[166:167],v32 offset:4608
s_waitcnt lgkmcnt(12)
v_mfma_f32_16x16x32_bf16 v[16:19],a[120:123],v[84:87],v[16:19]
v_mfma_f32_16x16x32_bf16 v[20:23],a[124:127],v[84:87],v[20:23]
v_mfma_f32_16x16x32_bf16 v[24:27],a[128:131],v[84:87],v[24:27]
v_mfma_f32_16x16x32_bf16 v[16:19],a[132:135],v[84:87],v[16:19]
v_mfma_f32_16x16x32_bf16 v[20:23],a[136:139],v[84:87],v[20:23]
v_mfma_f32_16x16x32_bf16 v[24:27],a[140:143],v[84:87],v[24:27]
s_waitcnt vmcnt(18)
s_barrier
s_add_u32 m0,s88,122880
s_add_u32 s61,s82,5046272
buffer_load_dwordx4 v28,s[40:43],s61 offen offset:0  lds
s_add_u32 m0,s88,123904
ds_read_b64_tr_b16 a[168:169],v30 offset:24576
buffer_load_dwordx4 v29,s[40:43],s61 offen offset:0  lds
s_add_u32 m0,s88,130944
ds_read_b64_tr_b16 a[170:171],v30 offset:25088
buffer_load_dwordx4 v28,s[40:43],s61 offen offset:128  lds
s_add_u32 m0,s88,131968
ds_read_b64_tr_b16 a[172:173],v31 offset:24576
buffer_load_dwordx4 v29,s[40:43],s61 offen offset:128  lds
s_add_u32 m0,s88,139008
ds_read_b64_tr_b16 a[174:175],v31 offset:25088
buffer_load_dwordx4 v28,s[40:43],s61 offen offset:256  lds
s_add_u32 m0,s88,140032
ds_read_b64_tr_b16 a[176:177],v32 offset:24576
buffer_load_dwordx4 v29,s[40:43],s61 offen offset:256  lds
ds_read_b64_tr_b16 a[178:179],v32 offset:25088
ds_read_b64_tr_b16 a[180:181],v30 offset:28672
ds_read_b64_tr_b16 a[182:183],v30 offset:29184
ds_read_b64_tr_b16 a[184:185],v31 offset:28672
ds_read_b64_tr_b16 a[186:187],v31 offset:29184
ds_read_b64_tr_b16 a[188:189],v32 offset:28672
ds_read_b64_tr_b16 a[190:191],v32 offset:29184
s_waitcnt lgkmcnt(12)
v_mfma_f32_16x16x32_bf16 v[16:19],a[144:147],v[84:87],v[16:19]
v_mfma_f32_16x16x32_bf16 v[20:23],a[148:151],v[84:87],v[20:23]
v_mfma_f32_16x16x32_bf16 v[24:27],a[152:155],v[84:87],v[24:27]
v_mfma_f32_16x16x32_bf16 v[16:19],a[156:159],v[84:87],v[16:19]
v_mfma_f32_16x16x32_bf16 v[20:23],a[160:163],v[84:87],v[20:23]
v_mfma_f32_16x16x32_bf16 v[24:27],a[164:167],v[84:87],v[24:27]
s_waitcnt vmcnt(18)
s_barrier
s_add_u32 m0,s88,0
s_add_u32 s61,s82,5505024
buffer_load_dwordx4 v28,s[40:43],s61 offen offset:0  lds
s_add_u32 m0,s88,1024
ds_read_b64_tr_b16 a[192:193],v40 offset:0
buffer_load_dwordx4 v29,s[40:43],s61 offen offset:0  lds
s_add_u32 m0,s88,8064
ds_read_b64_tr_b16 a[194:195],v40 offset:512
buffer_load_dwordx4 v28,s[40:43],s61 offen offset:128  lds
s_add_u32 m0,s88,9088
ds_read_b64_tr_b16 a[196:197],v41 offset:0
buffer_load_dwordx4 v29,s[40:43],s61 offen offset:128  lds
s_add_u32 m0,s88,16128
ds_read_b64_tr_b16 a[198:199],v41 offset:512
buffer_load_dwordx4 v28,s[40:43],s61 offen offset:256  lds
s_add_u32 m0,s88,17152
ds_read_b64_tr_b16 a[200:201],v42 offset:0
buffer_load_dwordx4 v29,s[40:43],s61 offen offset:256  lds
ds_read_b64_tr_b16 a[202:203],v42 offset:512)"
R"(
ds_read_b64_tr_b16 a[204:205],v40 offset:4096
ds_read_b64_tr_b16 a[206:207],v40 offset:4608
ds_read_b64_tr_b16 a[208:209],v41 offset:4096
ds_read_b64_tr_b16 a[210:211],v41 offset:4608
ds_read_b64_tr_b16 a[212:213],v42 offset:4096
ds_read_b64_tr_b16 a[214:215],v42 offset:4608
s_waitcnt lgkmcnt(12)
v_mfma_f32_16x16x32_bf16 v[16:19],a[168:171],v[84:87],v[16:19]
v_mfma_f32_16x16x32_bf16 v[20:23],a[172:175],v[84:87],v[20:23]
v_mfma_f32_16x16x32_bf16 v[24:27],a[176:179],v[84:87],v[24:27]
v_mfma_f32_16x16x32_bf16 v[16:19],a[180:183],v[84:87],v[16:19]
v_mfma_f32_16x16x32_bf16 v[20:23],a[184:187],v[84:87],v[20:23]
v_mfma_f32_16x16x32_bf16 v[24:27],a[188:191],v[84:87],v[24:27]
s_waitcnt vmcnt(18)
s_barrier
s_add_u32 m0,s88,24576
s_add_u32 s61,s82,5963776
buffer_load_dwordx4 v28,s[40:43],s61 offen offset:0  lds
s_add_u32 m0,s88,25600
ds_read_b64_tr_b16 a[216:217],v40 offset:24576
buffer_load_dwordx4 v29,s[40:43],s61 offen offset:0  lds
s_add_u32 m0,s88,32640
ds_read_b64_tr_b16 a[218:219],v40 offset:25088
buffer_load_dwordx4 v28,s[40:43],s61 offen offset:128  lds
s_add_u32 m0,s88,33664
ds_read_b64_tr_b16 a[220:221],v41 offset:24576
buffer_load_dwordx4 v29,s[40:43],s61 offen offset:128  lds
s_add_u32 m0,s88,40704
ds_read_b64_tr_b16 a[222:223],v41 offset:25088
buffer_load_dwordx4 v28,s[40:43],s61 offen offset:256  lds
s_add_u32 m0,s88,41728
ds_read_b64_tr_b16 a[224:225],v42 offset:24576
buffer_load_dwordx4 v29,s[40:43],s61 offen offset:256  lds
ds_read_b64_tr_b16 a[226:227],v42 offset:25088
ds_read_b64_tr_b16 a[228:229],v40 offset:28672
ds_read_b64_tr_b16 a[230:231],v40 offset:29184
ds_read_b64_tr_b16 a[232:233],v41 offset:28672
ds_read_b64_tr_b16 a[234:235],v41 offset:29184
ds_read_b64_tr_b16 a[236:237],v42 offset:28672
ds_read_b64_tr_b16 a[238:239],v42 offset:29184
s_waitcnt lgkmcnt(12)
v_mfma_f32_16x16x32_bf16 v[16:19],a[192:195],v[84:87],v[16:19]
v_mfma_f32_16x16x32_bf16 v[20:23],a[196:199],v[84:87],v[20:23]
v_mfma_f32_16x16x32_bf16 v[24:27],a[200:203],v[84:87],v[24:27]
v_mfma_f32_16x16x32_bf16 v[16:19],a[204:207],v[84:87],v[16:19]
v_mfma_f32_16x16x32_bf16 v[20:23],a[208:211],v[84:87],v[20:23]
v_mfma_f32_16x16x32_bf16 v[24:27],a[212:215],v[84:87],v[24:27]
s_waitcnt vmcnt(18)
s_barrier
s_add_u32 m0,s88,49152
s_add_u32 s61,s82,6422528
buffer_load_dwordx4 v28,s[40:43],s61 offen offset:0  lds
s_add_u32 m0,s88,50176
ds_read_b64_tr_b16 a[240:241],v43 offset:0
buffer_load_dwordx4 v29,s[40:43],s61 offen offset:0  lds
s_add_u32 m0,s88,57216
ds_read_b64_tr_b16 a[242:243],v43 offset:512
buffer_load_dwordx4 v28,s[40:43],s61 offen offset:128  lds
s_add_u32 m0,s88,58240
ds_read_b64_tr_b16 a[244:245],v44 offset:0
buffer_load_dwordx4 v29,s[40:43],s61 offen offset:128  lds
s_add_u32 m0,s88,65280
ds_read_b64_tr_b16 a[246:247],v44 offset:512
buffer_load_dwordx4 v28,s[40:43],s61 offen offset:256  lds
s_add_u32 m0,s88,66304
ds_read_b64_tr_b16 a[248:249],v45 offset:0
buffer_load_dwordx4 v29,s[40:43],s61 offen offset:256  lds
ds_read_b64_tr_b16 a[250:251],v45 offset:512
ds_read_b64_tr_b16 a[252:253],v43 offset:4096
ds_read_b64_tr_b16 a[254:255],v43 offset:4608
ds_read_b64_tr_b16 v[128:129],v44 offset:4096
ds_read_b64_tr_b16 v[130:131],v44 offset:4608
ds_read_b64_tr_b16 v[132:133],v45 offset:4096
ds_read_b64_tr_b16 v[134:135],v45 offset:4608
s_waitcnt lgkmcnt(12)
v_mfma_f32_16x16x32_bf16 v[16:19],a[216:219],v[84:87],v[16:19]
v_mfma_f32_16x16x32_bf16 v[20:23],a[220:223],v[84:87],v[20:23]
v_mfma_f32_16x16x32_bf16 v[24:27],a[224:227],v[84:87],v[24:27]
v_mfma_f32_16x16x32_bf16 v[16:19],a[228:231],v[84:87],v[16:19]
v_mfma_f32_16x16x32_bf16 v[20:23],a[232:235],v[84:87],v[20:23]
v_mfma_f32_16x16x32_bf16 v[24:27],a[236:239],v[84:87],v[24:27]
s_waitcnt vmcnt(18)
s_barrier
s_add_u32 m0,s88,73728
s_add_u32 s61,s82,6881280
buffer_load_dwordx4 v28,s[40:43],s61 offen offset:0  lds
s_add_u32 m0,s88,74752
ds_read_b64_tr_b16 v[136:137],v43 offset:24576
buffer_load_dwordx4 v29,s[40:43],s61 offen offset:0  lds
s_add_u32 m0,s88,81792
ds_read_b64_tr_b16 v[138:139],v43 offset:25088
buffer_load_dwordx4 v28,s[40:43],s61 offen offset:128  lds
s_add_u32 m0,s88,82816
ds_read_b64_tr_b16 v[140:141],v44 offset:24576
buffer_load_dwordx4 v29,s[40:43],s61 offen offset:128  lds
s_add_u32 m0,s88,89856
ds_read_b64_tr_b16 v[142:143],v44 offset:25088
buffer_load_dwordx4 v28,s[40:43],s61 offen offset:256  lds
s_add_u32 m0,s88,90880
ds_read_b64_tr_b16 v[144:145],v45 offset:24576
buffer_load_dwordx4 v29,s[40:43],s61 offen offset:256  lds
ds_read_b64_tr_b16 v[146:147],v45 offset:25088
ds_read_b64_tr_b16 v[148:149],v43 offset:28672
ds_read_b64_tr_b16 v[150:151],v43 offset:29184
ds_read_b64_tr_b16 v[152:153],v44 offset:28672
ds_read_b64_tr_b16 v[154:155],v44 offset:29184
ds_read_b64_tr_b16 v[156:157],v45 offset:28672
ds_read_b64_tr_b16 v[158:159],v45 offset:29184
s_getpc_b64 s[46:47]
Lg3_cp_%=:
s_add_u32 s46,s46,Lg3_init_%=-Lg3_cp_%=
s_addc_u32 s47,s47,0
s_lshl_b32 s68,s54,13
v_lshl_add_u32 v68,v2,3,s68
v_min_u32 v69,Lg3_done_%=-Lg3_init_%=,v68
global_load_dword v72,v69,s[46:47]
s_waitcnt lgkmcnt(12)
v_mfma_f32_16x16x32_bf16 v[16:19],a[240:243],v[84:87],v[16:19]
v_mfma_f32_16x16x32_bf16 v[20:23],a[244:247],v[84:87],v[20:23]
v_mfma_f32_16x16x32_bf16 v[24:27],a[248:251],v[84:87],v[24:27]
v_mfma_f32_16x16x32_bf16 v[16:19],a[252:255],v[84:87],v[16:19]
v_mfma_f32_16x16x32_bf16 v[20:23],v[128:131],v[84:87],v[20:23]
v_mfma_f32_16x16x32_bf16 v[24:27],v[132:135],v[84:87],v[24:27]
s_waitcnt vmcnt(19)
s_barrier
ds_read_b64_tr_b16 v[160:161],v30 offset:0
ds_read_b64_tr_b16 v[162:163],v30 offset:512
ds_read_b64_tr_b16 v[164:165],v31 offset:0
ds_read_b64_tr_b16 v[166:167],v31 offset:512
ds_read_b64_tr_b16 v[168:169],v32 offset:0
ds_read_b64_tr_b16 v[170:171],v32 offset:512
ds_read_b64_tr_b16 v[172:173],v30 offset:4096
ds_read_b64_tr_b16 v[174:175],v30 offset:4608
ds_read_b64_tr_b16 v[176:177],v31 offset:4096
ds_read_b64_tr_b16 v[178:179],v31 offset:4608
ds_read_b64_tr_b16 v[180:181],v32 offset:4096
ds_read_b64_tr_b16 v[182:183],v32 offset:4608
s_waitcnt lgkmcnt(12)
v_mfma_f32_16x16x32_bf16 v[16:19],v[136:139],v[84:87],v[16:19]
v_mfma_f32_16x16x32_bf16 v[20:23],v[140:143],v[84:87],v[20:23]
v_mfma_f32_16x16x32_bf16 v[24:27],v[144:147],v[84:87],v[24:27]
v_mfma_f32_16x16x32_bf16 v[16:19],v[148:151],v[84:87],v[16:19]
v_mfma_f32_16x16x32_bf16 v[20:23],v[152:155],v[84:87],v[20:23]
v_mfma_f32_16x16x32_bf16 v[24:27],v[156:159],v[84:87],v[24:27]
s_waitcnt vmcnt(13)
s_barrier
ds_read_b64_tr_b16 v[184:185],v30 offset:24576
ds_read_b64_tr_b16 v[186:187],v30 offset:25088
ds_read_b64_tr_b16 v[188:189],v31 offset:24576
ds_read_b64_tr_b16 v[190:191],v31 offset:25088
ds_read_b64_tr_b16 v[192:193],v32 offset:24576
ds_read_b64_tr_b16 v[194:195],v32 offset:25088
ds_read_b64_tr_b16 v[196:197],v30 offset:28672
ds_read_b64_tr_b16 v[198:199],v30 offset:29184
ds_read_b64_tr_b16 v[200:201],v31 offset:28672
ds_read_b64_tr_b16 v[202:203],v31 offset:29184
ds_read_b64_tr_b16 v[204:205],v32 offset:28672
ds_read_b64_tr_b16 v[206:207],v32 offset:29184
s_waitcnt lgkmcnt(12)
v_mfma_f32_16x16x32_bf16 v[16:19],v[160:163],v[84:87],v[16:19]
v_mfma_f32_16x16x32_bf16 v[20:23],v[164:167],v[84:87],v[20:23]
v_mfma_f32_16x16x32_bf16 v[24:27],v[168:171],v[84:87],v[24:27]
v_mfma_f32_16x16x32_bf16 v[16:19],v[172:175],v[84:87],v[16:19]
v_mfma_f32_16x16x32_bf16 v[20:23],v[176:179],v[84:87],v[20:23]
v_mfma_f32_16x16x32_bf16 v[24:27],v[180:183],v[84:87],v[24:27]
s_waitcnt vmcnt(7)
s_barrier
ds_read_b64_tr_b16 v[208:209],v40 offset:0
ds_read_b64_tr_b16 v[210:211],v40 offset:512
ds_read_b64_tr_b16 v[212:213],v41 offset:0
ds_read_b64_tr_b16 v[214:215],v41 offset:512
ds_read_b64_tr_b16 v[216:217],v42 offset:0
ds_read_b64_tr_b16 v[218:219],v42 offset:512
ds_read_b64_tr_b16 v[220:221],v40 offset:4096
ds_read_b64_tr_b16 v[222:223],v40 offset:4608
ds_read_b64_tr_b16 v[224:225],v41 offset:4096
ds_read_b64_tr_b16 v[226:227],v41 offset:4608
ds_read_b64_tr_b16 v[228:229],v42 offset:4096
ds_read_b64_tr_b16 v[230:231],v42 offset:4608
s_waitcnt lgkmcnt(12)
v_mfma_f32_16x16x32_bf16 v[16:19],v[184:187],v[84:87],v[16:19]
v_mfma_f32_16x16x32_bf16 v[20:23],v[188:191],v[84:87],v[20:23]
v_mfma_f32_16x16x32_bf16 v[24:27],v[192:195],v[84:87],v[24:27]
v_mfma_f32_16x16x32_bf16 v[16:19],v[196:199],v[84:87],v[16:19]
v_mfma_f32_16x16x32_bf16 v[20:23],v[200:203],v[84:87],v[20:23]
v_mfma_f32_16x16x32_bf16 v[24:27],v[204:207],v[84:87],v[24:27]
s_waitcnt vmcnt(1)
s_barrier
ds_read_b64_tr_b16 v[232:233],v40 offset:24576
ds_read_b64_tr_b16 v[234:235],v40 offset:25088
ds_read_b64_tr_b16 v[236:237],v41 offset:24576
ds_read_b64_tr_b16 v[238:239],v41 offset:25088
ds_read_b64_tr_b16 v[240:241],v42 offset:24576
ds_read_b64_tr_b16 v[242:243],v42 offset:25088
ds_read_b64_tr_b16 v[244:245],v40 offset:28672
ds_read_b64_tr_b16 v[246:247],v40 offset:29184
ds_read_b64_tr_b16 v[248:249],v41 offset:28672
ds_read_b64_tr_b16 v[250:251],v41 offset:29184
ds_read_b64_tr_b16 v[252:253],v42 offset:28672
ds_read_b64_tr_b16 v[254:255],v42 offset:29184)"
R"(
s_waitcnt lgkmcnt(12)
v_mfma_f32_16x16x32_bf16 v[16:19],v[208:211],v[84:87],v[16:19]
v_mfma_f32_16x16x32_bf16 v[20:23],v[212:215],v[84:87],v[20:23]
v_mfma_f32_16x16x32_bf16 v[24:27],v[216:219],v[84:87],v[24:27]
v_mfma_f32_16x16x32_bf16 v[16:19],v[220:223],v[84:87],v[16:19]
v_mfma_f32_16x16x32_bf16 v[20:23],v[224:227],v[84:87],v[20:23]
v_mfma_f32_16x16x32_bf16 v[24:27],v[228:231],v[84:87],v[24:27]
s_waitcnt lgkmcnt(0)
v_mfma_f32_16x16x32_bf16 v[16:19],v[232:235],v[84:87],v[16:19]
v_mfma_f32_16x16x32_bf16 v[20:23],v[236:239],v[84:87],v[20:23]
v_mfma_f32_16x16x32_bf16 v[24:27],v[240:243],v[84:87],v[24:27]
v_mfma_f32_16x16x32_bf16 v[16:19],v[244:247],v[84:87],v[16:19]
v_mfma_f32_16x16x32_bf16 v[20:23],v[248:251],v[84:87],v[20:23]
v_mfma_f32_16x16x32_bf16 v[24:27],v[252:255],v[84:87],v[24:27]
s_waitcnt lgkmcnt(0)
s_barrier
v_readfirstlane_b32 s60,v5
s_nop 3
s_add_u32 s60,s60,1
s_cmp_eq_u32 s60,0
s_cselect_b32 s60,1,s60
Lg3_init_%=:
v_mov_b32 v5,s60
s_mov_b32 s59,536870912
s_add_u32 s57,s56,131072
s_mov_b32 s79,0
s_mov_b32 s75,0x3a800000
s_mov_b32 s76,0xc0135761
s_mov_b32 s77,0x3d372713
s_mov_b32 s44,s59
s_mov_b32 s45,s59
s_mov_b32 s46,s59
s_mov_b32 s47,s59
s_mov_b32 s48,s83
s_add_u32 s49,s48,s84
s_add_u32 s50,s49,s55
s_cmp_eq_u32 s50,s56
s_cselect_b32 s50,s57,s50
s_cmp_eq_u32 s50,s58
s_cselect_b32 s50,s59,s50
s_add_u32 s51,s50,s55
s_cmp_eq_u32 s51,s56
s_cselect_b32 s51,s57,s51
s_cmp_eq_u32 s51,s58
s_cselect_b32 s51,s59,s51
s_add_u32 s52,s51,s55
s_cmp_eq_u32 s52,s56
s_cselect_b32 s52,s57,s52
s_cmp_eq_u32 s52,s58
s_cselect_b32 s52,s59,s52
s_lshl_b32 s63,s54,12
s_add_u32 s63,s63,65536
s_add_u32 s64,s63,32768
s_lshl_b32 s88,s54,10
s_add_u32 s88,s88,131072
s_lshl_b32 s78,s54,11
s_add_u32 s78,s78,132608
v_add_u32 v1,16384,v0
v_mov_b32 v12,0x3727c5ac
s_getpc_b64 s[90:91]
Lg3_pc_%=:
s_mov_b32 s68,Lg3_d1w0_%=-Lg3_pc_%=
s_cmp_eq_u32 s54,1
s_cselect_b32 s68,Lg3_d1w1_%=-Lg3_pc_%=,s68
s_cmp_eq_u32 s54,2
s_cselect_b32 s68,Lg3_d1w2_%=-Lg3_pc_%=,s68
s_cmp_eq_u32 s54,3
s_cselect_b32 s68,Lg3_d1w3_%=-Lg3_pc_%=,s68
s_add_u32 s92,s90,s68
s_addc_u32 s93,s91,0
s_mov_b32 s68,Lg3_d0w0_%=-Lg3_pc_%=
s_cmp_eq_u32 s54,1
s_cselect_b32 s68,Lg3_d0w1_%=-Lg3_pc_%=,s68
s_cmp_eq_u32 s54,2
s_cselect_b32 s68,Lg3_d0w2_%=-Lg3_pc_%=,s68
s_cmp_eq_u32 s54,3
s_cselect_b32 s68,Lg3_d0w3_%=-Lg3_pc_%=,s68
s_add_u32 s90,s90,s68
s_addc_u32 s91,s91,0
s_mov_b32 s66,s59
s_mov_b32 s65,0
s_mov_b32 s62,s63
s_branch Lg3_slow0_%=
Lg3_d0w0_%=:
v_mfma_f32_16x16x32_bf16 v[28:31],a[0:3],v[52:55],0
s_mov_b32 m0,s62
ds_read_b128 v[72:75],v2 offset:5120
v_mul_f32 v116,s75,v112
v_mfma_f32_16x16x32_bf16 v[32:35],a[4:7],v[52:55],0
buffer_load_dwordx4 v0,s[36:39],s61 offen offset:0 sc1 lds
v_mfma_f32_16x16x32_bf16 v[36:39],a[8:11],v[52:55],0
s_mul_i32 s73,s47,7
v_fma_f32 v113,v113,s75,v12
v_fma_f32 v113,-v116,v116,v113
v_mfma_f32_16x16x32_bf16 v[28:31],a[12:15],v[56:59],v[28:31]
s_lshr_b32 s73,s73,1
ds_read_b128 v[76:79],v2 offset:6144
v_max_f32 v113,v113,v12
v_mfma_f32_16x16x32_bf16 v[32:35],a[16:19],v[56:59],v[32:35]
s_add_u32 s53,s52,s55
v_rsq_f32 v112,v113
v_mfma_f32_16x16x32_bf16 v[36:39],a[20:23],v[56:59],v[36:39]
s_waitcnt lgkmcnt(3)
buffer_load_dwordx4 v0,s[36:39],s61 offen offset:1024 sc1 lds
v_mfma_f32_16x16x32_bf16 v[28:31],a[24:27],v[60:63],v[28:31]
s_cmp_eq_u32 s53,s56
ds_read_b128 v[80:83],v2 offset:7168
v_mul_f32 v117,s76,v112
v_mfma_f32_16x16x32_bf16 v[32:35],a[28:31],v[60:63],v[32:35]
s_cselect_b32 s53,s57,s53
v_mul_f32 v118,v112,v112
v_mul_f32 v119,v113,v112
v_mfma_f32_16x16x32_bf16 v[36:39],a[32:35],v[60:63],v[36:39]
s_cmp_eq_u32 s53,s58
v_mul_f32 v118,s77,v118
v_mul_f32 v118,v118,v117
v_mfma_f32_16x16x32_bf16 v[28:31],a[36:39],v[64:67],v[28:31]
buffer_load_dwordx4 v0,s[36:39],s61 offen offset:2048 sc1 lds
v_mfma_f32_16x16x32_bf16 v[32:35],a[40:43],v[64:67],v[32:35]
s_cselect_b32 s53,s59,s53
ds_read_b128 v[52:55],v2 offset:8192
v_fma_f32 v40,-v116,v16,v40
v_mfma_f32_16x16x32_bf16 v[36:39],a[44:47],v[64:67],v[36:39]
s_waitcnt lgkmcnt(3)
v_fma_f32 v41,-v116,v17,v41
v_fma_f32 v42,-v116,v18,v42
v_mfma_f32_16x16x32_bf16 v[28:31],a[48:51],v[68:71],v[28:31]
s_cmp_ge_u32 s66,s59
ds_read_b128 v[56:59],v2 offset:9216
v_fma_f32 v43,-v116,v19,v43
v_mfma_f32_16x16x32_bf16 v[32:35],a[52:55],v[68:71],v[32:35]
buffer_load_dwordx4 v0,s[36:39],s61 offen offset:3072 sc1 lds
v_mfma_f32_16x16x32_bf16 v[36:39],a[56:59],v[68:71],v[36:39]
s_cselect_b32 s86,s49,s50
v_mul_f32 v84,v40,v40
v_mul_f32 v85,v41,v41
v_mfma_f32_16x16x32_bf16 v[28:31],a[60:63],v[72:75],v[28:31]
s_lshr_b32 s68,s86,5
ds_read_b128 v[60:63],v2 offset:10240
v_mul_f32 v86,v42,v42
v_mfma_f32_16x16x32_bf16 v[32:35],a[64:67],v[72:75],v[32:35]
s_add_u32 m0,s78,0
v_mul_f32 v87,v43,v43
v_fma_f32 v84,v118,v84,v117
v_mfma_f32_16x16x32_bf16 v[36:39],a[68:71],v[72:75],v[36:39]
s_waitcnt lgkmcnt(3)
v_mul_f32 v84,v40,v84
v_fma_f32 v85,v118,v85,v117
v_mfma_f32_16x16x32_bf16 v[28:31],a[72:75],v[76:79],v[28:31]
ds_read_b128 v[64:67],v2 offset:11264
v_mul_f32 v85,v41,v85
v_mfma_f32_16x16x32_bf16 v[32:35],a[76:79],v[76:79],v[32:35]
v_exp_f32 v84,v84
v_mfma_f32_16x16x32_bf16 v[36:39],a[80:83],v[76:79],v[36:39]
v_exp_f32 v85,v85
v_mfma_f32_16x16x32_bf16 v[28:31],a[84:87],v[80:83],v[28:31]
ds_read_b128 v[68:71],v2 offset:12288
v_fma_f32 v84,v84,v119,v119
v_mfma_f32_16x16x32_bf16 v[32:35],a[88:91],v[80:83],v[32:35]
v_rcp_f32 v84,v84
v_mfma_f32_16x16x32_bf16 v[36:39],a[92:95],v[80:83],v[36:39]
s_waitcnt lgkmcnt(3)
v_fma_f32 v85,v85,v119,v119
v_fma_f32 v86,v118,v86,v117
v_mfma_f32_16x16x32_bf16 v[28:31],a[96:99],v[52:55],v[28:31]
ds_read_b128 v[72:75],v2 offset:13312
v_mul_f32 v86,v42,v86
v_mfma_f32_16x16x32_bf16 v[32:35],a[100:103],v[52:55],v[32:35]
v_exp_f32 v86,v86
v_mfma_f32_16x16x32_bf16 v[36:39],a[104:107],v[52:55],v[36:39]
v_rcp_f32 v85,v85
v_mfma_f32_16x16x32_bf16 v[28:31],a[108:111],v[56:59],v[28:31]
ds_read_b128 v[76:79],v2 offset:14336
v_fma_f32 v86,v86,v119,v119
v_mfma_f32_16x16x32_bf16 v[32:35],a[112:115],v[56:59],v[32:35]
v_rcp_f32 v86,v86
v_mfma_f32_16x16x32_bf16 v[36:39],a[116:119],v[56:59],v[36:39]
s_waitcnt lgkmcnt(3)
v_fma_f32 v87,v118,v87,v117
v_mul_f32 v87,v43,v87
v_mfma_f32_16x16x32_bf16 v[28:31],a[120:123],v[60:63],v[28:31]
ds_read_b128 v[80:83],v2 offset:15360
v_mul_f32 v40,v40,v84
v_mfma_f32_16x16x32_bf16 v[32:35],a[124:127],v[60:63],v[32:35]
s_waitcnt vmcnt(6)
v_exp_f32 v87,v87
v_mfma_f32_16x16x32_bf16 v[36:39],a[128:131],v[60:63],v[36:39]
s_barrier
v_mfma_f32_16x16x32_bf16 v[28:31],a[132:135],v[64:67],v[28:31]
ds_read_b128 v[52:55],v2 offset:16384
v_fma_f32 v87,v87,v119,v119
v_mfma_f32_16x16x32_bf16 v[32:35],a[136:139],v[64:67],v[32:35]
v_rcp_f32 v87,v87
v_mfma_f32_16x16x32_bf16 v[36:39],a[140:143],v[64:67],v[36:39]
s_waitcnt lgkmcnt(3)
v_add_f32_dpp v104,v104,v104 quad_perm:[1,0,3,2] row_mask:0xf bank_mask:0xf
v_mul_f32 v41,v41,v85
v_mfma_f32_16x16x32_bf16 v[28:31],a[144:147],v[68:71],v[28:31]
ds_read_b128 v[56:59],v2 offset:17408
v_add_f32_dpp v104,v104,v104 quad_perm:[2,3,0,1] row_mask:0xf bank_mask:0xf)"
R"(
v_mfma_f32_16x16x32_bf16 v[32:35],a[148:151],v[68:71],v[32:35]
s_nop 0
v_add_f32_dpp v104,v104,v104 row_half_mirror row_mask:0xf bank_mask:0xf
v_mul_f32 v42,v42,v86
v_mfma_f32_16x16x32_bf16 v[36:39],a[152:155],v[68:71],v[36:39]
v_mul_f32 v43,v43,v87
v_cvt_pk_bf16_f32 v96,v40,v41
v_mfma_f32_16x16x32_bf16 v[28:31],a[156:159],v[72:75],v[28:31]
ds_read_b128 v[60:63],v2 offset:18432
v_cvt_pk_bf16_f32 v97,v42,v43
v_mfma_f32_16x16x32_bf16 v[32:35],a[160:163],v[72:75],v[32:35]
buffer_load_dword v6,s[36:39],s68 offen offset:0 sc1 lds
v_mfma_f32_16x16x32_bf16 v[36:39],a[164:167],v[72:75],v[36:39]
s_waitcnt lgkmcnt(3)
v_fma_f32 v44,-v116,v20,v44
v_fma_f32 v45,-v116,v21,v45
v_mfma_f32_16x16x32_bf16 v[28:31],a[168:171],v[76:79],v[28:31]
s_add_u32 m0,s62,16384
ds_read_b128 v[64:67],v2 offset:19456
v_fma_f32 v46,-v116,v22,v46
v_mfma_f32_16x16x32_bf16 v[32:35],a[172:175],v[76:79],v[32:35]
buffer_load_dwordx4 v1,s[36:39],s61 offen offset:0 sc1 lds
v_mfma_f32_16x16x32_bf16 v[36:39],a[176:179],v[76:79],v[36:39]
s_lshr_b32 s72,s61,5
v_fma_f32 v47,-v116,v23,v47
v_mul_f32 v88,v44,v44
v_mfma_f32_16x16x32_bf16 v[28:31],a[180:183],v[80:83],v[28:31]
s_mov_b32 s62,s63
ds_read_b128 v[68:71],v2 offset:20480
v_mul_f32 v89,v45,v45
v_mfma_f32_16x16x32_bf16 v[32:35],a[184:187],v[80:83],v[32:35]
ds_write_b32 v10,v104 offset:0
v_mfma_f32_16x16x32_bf16 v[36:39],a[188:191],v[80:83],v[36:39]
s_waitcnt lgkmcnt(4)
buffer_load_dwordx4 v1,s[36:39],s61 offen offset:1024 sc1 lds
v_mfma_f32_16x16x32_bf16 v[28:31],a[192:195],v[52:55],v[28:31]
ds_read_b128 v[72:75],v2 offset:21504
v_mul_f32 v90,v46,v46
v_mfma_f32_16x16x32_bf16 v[32:35],a[196:199],v[52:55],v[32:35]
v_mul_f32 v91,v47,v47
v_fma_f32 v88,v118,v88,v117
v_mfma_f32_16x16x32_bf16 v[36:39],a[200:203],v[52:55],v[36:39]
v_mul_f32 v88,v44,v88
v_fma_f32 v89,v118,v89,v117
v_mfma_f32_16x16x32_bf16 v[28:31],a[204:207],v[56:59],v[28:31]
buffer_load_dwordx4 v1,s[36:39],s61 offen offset:2048 sc1 lds
v_mfma_f32_16x16x32_bf16 v[32:35],a[208:211],v[56:59],v[32:35]
ds_read_b128 v[76:79],v2 offset:22528
v_mul_f32 v89,v45,v89
v_mfma_f32_16x16x32_bf16 v[36:39],a[212:215],v[56:59],v[36:39]
s_waitcnt lgkmcnt(4)
v_exp_f32 v88,v88
v_mfma_f32_16x16x32_bf16 v[28:31],a[216:219],v[60:63],v[28:31]
ds_read_b128 v[80:83],v2 offset:23552
v_fma_f32 v88,v88,v119,v119
v_mfma_f32_16x16x32_bf16 v[32:35],a[220:223],v[60:63],v[32:35]
buffer_load_dwordx4 v1,s[36:39],s61 offen offset:3072 sc1 lds
v_mfma_f32_16x16x32_bf16 v[36:39],a[224:227],v[60:63],v[36:39]
buffer_load_dword v108,v8,s[36:39],s72 offen offset:0 sc1
v_mfma_f32_16x16x32_bf16 v[28:31],a[228:231],v[64:67],v[28:31]
ds_read_b128 v[52:55],v2 offset:24576
v_fma_f32 v90,v118,v90,v117
v_mfma_f32_16x16x32_bf16 v[32:35],a[232:235],v[64:67],v[32:35]
v_exp_f32 v89,v89
v_mfma_f32_16x16x32_bf16 v[36:39],a[236:239],v[64:67],v[36:39]
s_waitcnt lgkmcnt(3)
v_rcp_f32 v88,v88
v_mfma_f32_16x16x32_bf16 v[28:31],a[240:243],v[68:71],v[28:31]
ds_read_b128 v[56:59],v2 offset:25600
v_mul_f32 v90,v46,v90
v_mfma_f32_16x16x32_bf16 v[32:35],a[244:247],v[68:71],v[32:35]
s_waitcnt vmcnt(10)
v_exp_f32 v90,v90
v_mfma_f32_16x16x32_bf16 v[36:39],a[248:251],v[68:71],v[36:39]
s_barrier
v_mfma_f32_16x16x32_bf16 v[28:31],a[252:255],v[72:75],v[28:31]
ds_read_b128 v[60:63],v2 offset:26624
v_fma_f32 v89,v89,v119,v119
v_mfma_f32_16x16x32_bf16 v[32:35],v[128:131],v[72:75],v[32:35]
v_rcp_f32 v89,v89
v_mfma_f32_16x16x32_bf16 v[36:39],v[132:135],v[72:75],v[36:39]
s_waitcnt lgkmcnt(3)
v_fma_f32 v90,v90,v119,v119
v_fma_f32 v91,v118,v91,v117
v_mfma_f32_16x16x32_bf16 v[28:31],v[136:139],v[76:79],v[28:31]
ds_read_b128 v[64:67],v2 offset:27648
v_mul_f32 v91,v47,v91
v_mfma_f32_16x16x32_bf16 v[32:35],v[140:143],v[76:79],v[32:35]
v_exp_f32 v91,v91
v_mfma_f32_16x16x32_bf16 v[36:39],v[144:147],v[76:79],v[36:39]
v_rcp_f32 v90,v90
v_mfma_f32_16x16x32_bf16 v[28:31],v[148:151],v[80:83],v[28:31]
ds_read_b128 v[68:71],v2 offset:28672
v_fma_f32 v91,v91,v119,v119
v_mfma_f32_16x16x32_bf16 v[32:35],v[152:155],v[80:83],v[32:35]
v_rcp_f32 v91,v91
v_mfma_f32_16x16x32_bf16 v[36:39],v[156:159],v[80:83],v[36:39]
s_waitcnt lgkmcnt(3)
v_mul_f32 v44,v44,v88
v_mul_f32 v45,v45,v89
v_mfma_f32_16x16x32_bf16 v[28:31],v[160:163],v[52:55],v[28:31]
ds_read_b128 v[72:75],v2 offset:29696
v_mul_f32 v46,v46,v90
v_mfma_f32_16x16x32_bf16 v[32:35],v[164:167],v[52:55],v[32:35]
v_mul_f32 v47,v47,v91
v_cvt_pk_bf16_f32 v98,v44,v45
v_mfma_f32_16x16x32_bf16 v[36:39],v[168:171],v[52:55],v[36:39]
v_cvt_pk_bf16_f32 v99,v46,v47
v_fma_f32 v48,-v116,v24,v48
v_mfma_f32_16x16x32_bf16 v[28:31],v[172:175],v[56:59],v[28:31]
ds_read_b128 v[76:79],v2 offset:30720
v_fma_f32 v49,-v116,v25,v49
v_mfma_f32_16x16x32_bf16 v[32:35],v[176:179],v[56:59],v[32:35]
v_fma_f32 v50,-v116,v26,v50
v_fma_f32 v51,-v116,v27,v51
v_mfma_f32_16x16x32_bf16 v[36:39],v[180:183],v[56:59],v[36:39]
s_waitcnt lgkmcnt(3)
buffer_store_dwordx4 v[96:99],v3,s[36:39],s73 offen offset:0 sc0 sc1
v_mul_f32 v92,v48,v48
v_mul_f32 v93,v49,v49
v_mul_f32 v94,v50,v50
v_mfma_f32_16x16x32_bf16 v[28:31],v[184:187],v[60:63],v[28:31]
ds_read_b128 v[80:83],v2 offset:31744
v_mul_f32 v95,v51,v51
v_mfma_f32_16x16x32_bf16 v[32:35],v[188:191],v[60:63],v[32:35]
s_waitcnt vmcnt(6)
v_fma_f32 v92,v118,v92,v117
v_mul_f32 v92,v48,v92
v_mfma_f32_16x16x32_bf16 v[36:39],v[192:195],v[60:63],v[36:39]
s_barrier
v_mfma_f32_16x16x32_bf16 v[28:31],v[196:199],v[64:67],v[28:31]
ds_read_b128 v[52:55],v2 offset:32768
ds_read_b32 v13,v7 offset:0
v_mfma_f32_16x16x32_bf16 v[32:35],v[200:203],v[64:67],v[32:35]
ds_read_b64 v[112:113],v11 offset:0
v_fma_f32 v93,v118,v93,v117
v_mfma_f32_16x16x32_bf16 v[36:39],v[204:207],v[64:67],v[36:39]
s_waitcnt lgkmcnt(5)
v_exp_f32 v92,v92
v_mfma_f32_16x16x32_bf16 v[28:31],v[208:211],v[68:71],v[28:31]
ds_read_b128 v[56:59],v2 offset:33792
v_mul_f32 v93,v49,v93
v_mfma_f32_16x16x32_bf16 v[32:35],v[212:215],v[68:71],v[32:35]
s_waitcnt lgkmcnt(2)
v_exp_f32 v93,v93
v_mfma_f32_16x16x32_bf16 v[36:39],v[216:219],v[68:71],v[36:39]
s_waitcnt lgkmcnt(1)
v_cmp_eq_u32 vcc,s60,v13
v_fma_f32 v92,v92,v119,v119
v_rcp_f32 v92,v92
v_mfma_f32_16x16x32_bf16 v[28:31],v[220:223],v[72:75],v[28:31]
s_cmp_eq_u32 vcc_lo,-1
ds_read_b128 v[60:63],v2 offset:34816
v_fma_f32 v93,v93,v119,v119
v_mfma_f32_16x16x32_bf16 v[32:35],v[224:227],v[72:75],v[32:35]
s_cselect_b32 s67,s86,s59
v_rcp_f32 v93,v93
v_mfma_f32_16x16x32_bf16 v[36:39],v[228:231],v[72:75],v[36:39]
v_fma_f32 v94,v118,v94,v117
v_mul_f32 v94,v50,v94
v_mfma_f32_16x16x32_bf16 v[28:31],v[232:235],v[76:79],v[28:31]
s_lshr_b32 s61,s67,0
ds_read_b128 v[64:67],v2 offset:35840
v_fma_f32 v95,v118,v95,v117
v_mfma_f32_16x16x32_bf16 v[32:35],v[236:239],v[76:79],v[32:35]
s_xor_b32 s63,s63,65536
v_mul_f32 v95,v51,v95
v_exp_f32 v94,v94
v_exp_f32 v95,v95
v_mfma_f32_16x16x32_bf16 v[36:39],v[240:243],v[76:79],v[36:39]
s_cmp_ge_u32 s66,s59
v_fma_f32 v94,v94,v119,v119
v_fma_f32 v95,v95,v119,v119
v_rcp_f32 v94,v94
v_rcp_f32 v95,v95
v_mfma_f32_16x16x32_bf16 v[28:31],v[244:247],v[80:83],v[28:31]
v_mul_f32 v48,v48,v92
v_mul_f32 v49,v49,v93
v_mul_f32 v50,v50,v94
v_mul_f32 v51,v51,v95
v_cvt_pk_bf16_f32 v100,v48,v49
v_cvt_pk_bf16_f32 v101,v50,v51
v_mfma_f32_16x16x32_bf16 v[32:35],v[248:251],v[80:83],v[32:35]
s_waitcnt lgkmcnt(2)
ds_read_b128 v[68:71],v2 offset:36864
buffer_store_dwordx2 v[100:101],v9,s[36:39],s73 offen offset:0 sc0 sc1
v_mfma_f32_16x16x32_bf16 v[36:39],v[252:255],v[80:83],v[36:39]
s_cbranch_scc1 Lg3_slow1_%=
Lg3_d1w0_%=:
v_mfma_f32_16x16x32_bf16 v[40:43],a[0:3],v[52:55],0
s_mov_b32 m0,s62
ds_read_b128 v[72:75],v2 offset:37888
v_mul_f32 v116,s75,v112
v_mfma_f32_16x16x32_bf16 v[44:47],a[4:7],v[52:55],0
buffer_load_dwordx4 v0,s[36:39],s61 offen offset:0 sc1 lds
v_mfma_f32_16x16x32_bf16 v[48:51],a[8:11],v[52:55],0
s_mul_i32 s73,s48,7
v_fma_f32 v113,v113,s75,v12
v_fma_f32 v113,-v116,v116,v113
v_mfma_f32_16x16x32_bf16 v[40:43],a[12:15],v[56:59],v[40:43])"
R"(
s_lshr_b32 s73,s73,1
ds_read_b128 v[76:79],v2 offset:38912
v_max_f32 v113,v113,v12
v_mfma_f32_16x16x32_bf16 v[44:47],a[16:19],v[56:59],v[44:47]
s_add_u32 s74,s53,s55
v_rsq_f32 v112,v113
v_mfma_f32_16x16x32_bf16 v[48:51],a[20:23],v[56:59],v[48:51]
s_waitcnt lgkmcnt(3)
buffer_load_dwordx4 v0,s[36:39],s61 offen offset:1024 sc1 lds
v_mfma_f32_16x16x32_bf16 v[40:43],a[24:27],v[60:63],v[40:43]
s_cmp_eq_u32 s74,s56
ds_read_b128 v[80:83],v2 offset:39936
v_mul_f32 v117,s76,v112
v_mfma_f32_16x16x32_bf16 v[44:47],a[28:31],v[60:63],v[44:47]
s_cselect_b32 s74,s57,s74
v_mul_f32 v118,v112,v112
v_mul_f32 v119,v113,v112
v_mfma_f32_16x16x32_bf16 v[48:51],a[32:35],v[60:63],v[48:51]
s_cmp_eq_u32 s74,s58
v_mul_f32 v118,s77,v118
v_mul_f32 v118,v118,v117
v_mfma_f32_16x16x32_bf16 v[40:43],a[36:39],v[64:67],v[40:43]
buffer_load_dwordx4 v0,s[36:39],s61 offen offset:2048 sc1 lds
v_mfma_f32_16x16x32_bf16 v[44:47],a[40:43],v[64:67],v[44:47]
s_cselect_b32 s74,s59,s74
ds_read_b128 v[52:55],v2 offset:40960
v_fma_f32 v28,-v116,v16,v28
v_mfma_f32_16x16x32_bf16 v[48:51],a[44:47],v[64:67],v[48:51]
s_waitcnt lgkmcnt(3)
v_fma_f32 v29,-v116,v17,v29
v_fma_f32 v30,-v116,v18,v30
v_mfma_f32_16x16x32_bf16 v[40:43],a[48:51],v[68:71],v[40:43]
s_cmp_ge_u32 s67,s59
ds_read_b128 v[56:59],v2 offset:41984
v_fma_f32 v31,-v116,v19,v31
v_mfma_f32_16x16x32_bf16 v[44:47],a[52:55],v[68:71],v[44:47]
buffer_load_dwordx4 v0,s[36:39],s61 offen offset:3072 sc1 lds
v_mfma_f32_16x16x32_bf16 v[48:51],a[56:59],v[68:71],v[48:51]
s_cselect_b32 s86,s50,s51
v_mul_f32 v84,v28,v28
v_mul_f32 v85,v29,v29
v_mfma_f32_16x16x32_bf16 v[40:43],a[60:63],v[72:75],v[40:43]
s_lshr_b32 s68,s86,5
ds_read_b128 v[60:63],v2 offset:43008
v_mul_f32 v86,v30,v30
v_mfma_f32_16x16x32_bf16 v[44:47],a[64:67],v[72:75],v[44:47]
s_add_u32 m0,s78,256
v_mul_f32 v87,v31,v31
v_fma_f32 v84,v118,v84,v117
v_mfma_f32_16x16x32_bf16 v[48:51],a[68:71],v[72:75],v[48:51]
s_waitcnt lgkmcnt(3)
v_mul_f32 v84,v28,v84
v_fma_f32 v85,v118,v85,v117
v_mfma_f32_16x16x32_bf16 v[40:43],a[72:75],v[76:79],v[40:43]
s_mov_b64 s[44:45],s[46:47]
ds_read_b128 v[64:67],v2 offset:44032
v_mul_f32 v85,v29,v85
v_mfma_f32_16x16x32_bf16 v[44:47],a[76:79],v[76:79],v[44:47]
s_mov_b64 s[46:47],s[48:49]
v_exp_f32 v84,v84
v_mfma_f32_16x16x32_bf16 v[48:51],a[80:83],v[76:79],v[48:51]
s_mov_b64 s[48:49],s[50:51]
v_exp_f32 v85,v85
v_mfma_f32_16x16x32_bf16 v[40:43],a[84:87],v[80:83],v[40:43]
s_mov_b64 s[50:51],s[52:53]
ds_read_b128 v[68:71],v2 offset:45056
v_fma_f32 v84,v84,v119,v119
v_mfma_f32_16x16x32_bf16 v[44:47],a[88:91],v[80:83],v[44:47]
s_mov_b32 s52,s74
v_rcp_f32 v84,v84
v_mfma_f32_16x16x32_bf16 v[48:51],a[92:95],v[80:83],v[48:51]
s_waitcnt lgkmcnt(3)
v_fma_f32 v85,v85,v119,v119
v_fma_f32 v86,v118,v86,v117
v_mfma_f32_16x16x32_bf16 v[40:43],a[96:99],v[52:55],v[40:43]
ds_read_b128 v[72:75],v2 offset:46080
v_mul_f32 v86,v30,v86
v_mfma_f32_16x16x32_bf16 v[44:47],a[100:103],v[52:55],v[44:47]
v_exp_f32 v86,v86
v_mfma_f32_16x16x32_bf16 v[48:51],a[104:107],v[52:55],v[48:51]
v_rcp_f32 v85,v85
v_mfma_f32_16x16x32_bf16 v[40:43],a[108:111],v[56:59],v[40:43]
ds_read_b128 v[76:79],v2 offset:47104
v_fma_f32 v86,v86,v119,v119
v_mfma_f32_16x16x32_bf16 v[44:47],a[112:115],v[56:59],v[44:47]
v_rcp_f32 v86,v86
v_mfma_f32_16x16x32_bf16 v[48:51],a[116:119],v[56:59],v[48:51]
s_waitcnt lgkmcnt(3)
v_fma_f32 v87,v118,v87,v117
v_mul_f32 v87,v31,v87
v_mfma_f32_16x16x32_bf16 v[40:43],a[120:123],v[60:63],v[40:43]
ds_read_b128 v[80:83],v2 offset:48128
v_mul_f32 v28,v28,v84
v_mfma_f32_16x16x32_bf16 v[44:47],a[124:127],v[60:63],v[44:47]
s_waitcnt vmcnt(6)
v_exp_f32 v87,v87
v_mfma_f32_16x16x32_bf16 v[48:51],a[128:131],v[60:63],v[48:51]
s_barrier
v_mfma_f32_16x16x32_bf16 v[40:43],a[132:135],v[64:67],v[40:43]
ds_read_b128 v[52:55],v2 offset:49152
v_fma_f32 v87,v87,v119,v119
v_mfma_f32_16x16x32_bf16 v[44:47],a[136:139],v[64:67],v[44:47]
v_rcp_f32 v87,v87
v_mfma_f32_16x16x32_bf16 v[48:51],a[140:143],v[64:67],v[48:51]
s_waitcnt lgkmcnt(3)
v_add_f32_dpp v108,v108,v108 quad_perm:[1,0,3,2] row_mask:0xf bank_mask:0xf
v_mul_f32 v29,v29,v85
v_mfma_f32_16x16x32_bf16 v[40:43],a[144:147],v[68:71],v[40:43]
ds_read_b128 v[56:59],v2 offset:50176
v_add_f32_dpp v108,v108,v108 quad_perm:[2,3,0,1] row_mask:0xf bank_mask:0xf
v_mfma_f32_16x16x32_bf16 v[44:47],a[148:151],v[68:71],v[44:47]
s_nop 0
v_add_f32_dpp v108,v108,v108 row_half_mirror row_mask:0xf bank_mask:0xf
v_mul_f32 v30,v30,v86
v_mfma_f32_16x16x32_bf16 v[48:51],a[152:155],v[68:71],v[48:51]
v_mul_f32 v31,v31,v87
v_cvt_pk_bf16_f32 v96,v28,v29
v_mfma_f32_16x16x32_bf16 v[40:43],a[156:159],v[72:75],v[40:43]
ds_read_b128 v[60:63],v2 offset:51200
v_cvt_pk_bf16_f32 v97,v30,v31
v_mfma_f32_16x16x32_bf16 v[44:47],a[160:163],v[72:75],v[44:47]
buffer_load_dword v6,s[36:39],s68 offen offset:0 sc1 lds
v_mfma_f32_16x16x32_bf16 v[48:51],a[164:167],v[72:75],v[48:51]
s_waitcnt lgkmcnt(3)
v_fma_f32 v32,-v116,v20,v32
v_fma_f32 v33,-v116,v21,v33
v_mfma_f32_16x16x32_bf16 v[40:43],a[168:171],v[76:79],v[40:43]
s_add_u32 m0,s62,16384
ds_read_b128 v[64:67],v2 offset:52224
v_fma_f32 v34,-v116,v22,v34
v_mfma_f32_16x16x32_bf16 v[44:47],a[172:175],v[76:79],v[44:47]
buffer_load_dwordx4 v1,s[36:39],s61 offen offset:0 sc1 lds
v_mfma_f32_16x16x32_bf16 v[48:51],a[176:179],v[76:79],v[48:51]
s_lshr_b32 s72,s61,5
v_fma_f32 v35,-v116,v23,v35
v_mul_f32 v88,v32,v32
v_mfma_f32_16x16x32_bf16 v[40:43],a[180:183],v[80:83],v[40:43]
s_mov_b32 s62,s64
ds_read_b128 v[68:71],v2 offset:53248
v_mul_f32 v89,v33,v33
v_mfma_f32_16x16x32_bf16 v[44:47],a[184:187],v[80:83],v[44:47]
ds_write_b32 v10,v108 offset:128
v_mfma_f32_16x16x32_bf16 v[48:51],a[188:191],v[80:83],v[48:51]
s_waitcnt lgkmcnt(4)
buffer_load_dwordx4 v1,s[36:39],s61 offen offset:1024 sc1 lds
v_mfma_f32_16x16x32_bf16 v[40:43],a[192:195],v[52:55],v[40:43]
ds_read_b128 v[72:75],v2 offset:54272
v_mul_f32 v90,v34,v34
v_mfma_f32_16x16x32_bf16 v[44:47],a[196:199],v[52:55],v[44:47]
v_mul_f32 v91,v35,v35
v_fma_f32 v88,v118,v88,v117
v_mfma_f32_16x16x32_bf16 v[48:51],a[200:203],v[52:55],v[48:51]
v_mul_f32 v88,v32,v88
v_fma_f32 v89,v118,v89,v117
v_mfma_f32_16x16x32_bf16 v[40:43],a[204:207],v[56:59],v[40:43]
buffer_load_dwordx4 v1,s[36:39],s61 offen offset:2048 sc1 lds
v_mfma_f32_16x16x32_bf16 v[44:47],a[208:211],v[56:59],v[44:47]
ds_read_b128 v[76:79],v2 offset:55296
v_mul_f32 v89,v33,v89
v_mfma_f32_16x16x32_bf16 v[48:51],a[212:215],v[56:59],v[48:51]
s_waitcnt lgkmcnt(4)
v_exp_f32 v88,v88
v_mfma_f32_16x16x32_bf16 v[40:43],a[216:219],v[60:63],v[40:43]
ds_read_b128 v[80:83],v2 offset:56320
v_fma_f32 v88,v88,v119,v119
v_mfma_f32_16x16x32_bf16 v[44:47],a[220:223],v[60:63],v[44:47]
buffer_load_dwordx4 v1,s[36:39],s61 offen offset:3072 sc1 lds
v_mfma_f32_16x16x32_bf16 v[48:51],a[224:227],v[60:63],v[48:51]
buffer_load_dword v104,v8,s[36:39],s72 offen offset:0 sc1
v_mfma_f32_16x16x32_bf16 v[40:43],a[228:231],v[64:67],v[40:43]
ds_read_b128 v[52:55],v2 offset:57344
v_fma_f32 v90,v118,v90,v117
v_mfma_f32_16x16x32_bf16 v[44:47],a[232:235],v[64:67],v[44:47]
v_exp_f32 v89,v89
v_mfma_f32_16x16x32_bf16 v[48:51],a[236:239],v[64:67],v[48:51]
s_waitcnt lgkmcnt(3)
v_rcp_f32 v88,v88
v_mfma_f32_16x16x32_bf16 v[40:43],a[240:243],v[68:71],v[40:43]
ds_read_b128 v[56:59],v2 offset:58368
v_mul_f32 v90,v34,v90
v_mfma_f32_16x16x32_bf16 v[44:47],a[244:247],v[68:71],v[44:47]
s_waitcnt vmcnt(10)
v_exp_f32 v90,v90
v_mfma_f32_16x16x32_bf16 v[48:51],a[248:251],v[68:71],v[48:51]
s_barrier
v_mfma_f32_16x16x32_bf16 v[40:43],a[252:255],v[72:75],v[40:43]
ds_read_b128 v[60:63],v2 offset:59392
v_fma_f32 v89,v89,v119,v119
v_mfma_f32_16x16x32_bf16 v[44:47],v[128:131],v[72:75],v[44:47]
v_rcp_f32 v89,v89
v_mfma_f32_16x16x32_bf16 v[48:51],v[132:135],v[72:75],v[48:51]
s_waitcnt lgkmcnt(3)
v_fma_f32 v90,v90,v119,v119
v_fma_f32 v91,v118,v91,v117
v_mfma_f32_16x16x32_bf16 v[40:43],v[136:139],v[76:79],v[40:43]
ds_read_b128 v[64:67],v2 offset:60416
v_mul_f32 v91,v35,v91
v_mfma_f32_16x16x32_bf16 v[44:47],v[140:143],v[76:79],v[44:47]
v_exp_f32 v91,v91
v_mfma_f32_16x16x32_bf16 v[48:51],v[144:147],v[76:79],v[48:51])"
R"(
v_rcp_f32 v90,v90
v_mfma_f32_16x16x32_bf16 v[40:43],v[148:151],v[80:83],v[40:43]
ds_read_b128 v[68:71],v2 offset:61440
v_fma_f32 v91,v91,v119,v119
v_mfma_f32_16x16x32_bf16 v[44:47],v[152:155],v[80:83],v[44:47]
v_rcp_f32 v91,v91
v_mfma_f32_16x16x32_bf16 v[48:51],v[156:159],v[80:83],v[48:51]
s_waitcnt lgkmcnt(3)
v_mul_f32 v32,v32,v88
v_mul_f32 v33,v33,v89
v_mfma_f32_16x16x32_bf16 v[40:43],v[160:163],v[52:55],v[40:43]
ds_read_b128 v[72:75],v2 offset:62464
v_mul_f32 v34,v34,v90
v_mfma_f32_16x16x32_bf16 v[44:47],v[164:167],v[52:55],v[44:47]
v_mul_f32 v35,v35,v91
v_cvt_pk_bf16_f32 v98,v32,v33
v_mfma_f32_16x16x32_bf16 v[48:51],v[168:171],v[52:55],v[48:51]
v_cvt_pk_bf16_f32 v99,v34,v35
v_fma_f32 v36,-v116,v24,v36
v_mfma_f32_16x16x32_bf16 v[40:43],v[172:175],v[56:59],v[40:43]
ds_read_b128 v[76:79],v2 offset:63488
v_fma_f32 v37,-v116,v25,v37
v_mfma_f32_16x16x32_bf16 v[44:47],v[176:179],v[56:59],v[44:47]
v_fma_f32 v38,-v116,v26,v38
v_fma_f32 v39,-v116,v27,v39
v_mfma_f32_16x16x32_bf16 v[48:51],v[180:183],v[56:59],v[48:51]
s_waitcnt lgkmcnt(3)
buffer_store_dwordx4 v[96:99],v3,s[36:39],s73 offen offset:0 sc0 sc1
v_mul_f32 v92,v36,v36
v_mul_f32 v93,v37,v37
v_mul_f32 v94,v38,v38
v_mfma_f32_16x16x32_bf16 v[40:43],v[184:187],v[60:63],v[40:43]
ds_read_b128 v[80:83],v2 offset:64512
v_xor_b32 v2,65536,v2
v_mfma_f32_16x16x32_bf16 v[44:47],v[188:191],v[60:63],v[44:47]
s_waitcnt vmcnt(6)
v_mul_f32 v95,v39,v39
v_fma_f32 v92,v118,v92,v117
v_mfma_f32_16x16x32_bf16 v[48:51],v[192:195],v[60:63],v[48:51]
s_barrier
v_mfma_f32_16x16x32_bf16 v[40:43],v[196:199],v[64:67],v[40:43]
ds_read_b128 v[52:55],v2 offset:0
ds_read_b32 v13,v7 offset:256
v_mfma_f32_16x16x32_bf16 v[44:47],v[200:203],v[64:67],v[44:47]
ds_read_b64 v[112:113],v11 offset:128
v_mul_f32 v92,v36,v92
v_mfma_f32_16x16x32_bf16 v[48:51],v[204:207],v[64:67],v[48:51]
s_waitcnt lgkmcnt(5)
v_exp_f32 v92,v92
v_mfma_f32_16x16x32_bf16 v[40:43],v[208:211],v[68:71],v[40:43]
ds_read_b128 v[56:59],v2 offset:1024
v_fma_f32 v92,v92,v119,v119
v_mfma_f32_16x16x32_bf16 v[44:47],v[212:215],v[68:71],v[44:47]
s_waitcnt lgkmcnt(2)
v_rcp_f32 v92,v92
v_mfma_f32_16x16x32_bf16 v[48:51],v[216:219],v[68:71],v[48:51]
s_waitcnt lgkmcnt(1)
v_cmp_eq_u32 vcc,s60,v13
v_fma_f32 v93,v118,v93,v117
v_mul_f32 v93,v37,v93
v_fma_f32 v94,v118,v94,v117
v_mfma_f32_16x16x32_bf16 v[40:43],v[220:223],v[72:75],v[40:43]
s_cmp_eq_u32 vcc_lo,-1
ds_read_b128 v[60:63],v2 offset:2048
v_mul_f32 v94,v38,v94
v_mfma_f32_16x16x32_bf16 v[44:47],v[224:227],v[72:75],v[44:47]
s_cselect_b32 s66,s86,s59
v_exp_f32 v93,v93
v_mfma_f32_16x16x32_bf16 v[48:51],v[228:231],v[72:75],v[48:51]
v_exp_f32 v94,v94
v_mfma_f32_16x16x32_bf16 v[40:43],v[232:235],v[76:79],v[40:43]
s_lshr_b32 s61,s66,0
ds_read_b128 v[64:67],v2 offset:3072
v_fma_f32 v93,v93,v119,v119
v_mfma_f32_16x16x32_bf16 v[44:47],v[236:239],v[76:79],v[44:47]
s_xor_b32 s64,s64,65536
v_fma_f32 v95,v118,v95,v117
v_mul_f32 v95,v39,v95
v_exp_f32 v95,v95
v_mfma_f32_16x16x32_bf16 v[48:51],v[240:243],v[76:79],v[48:51]
s_cmp_lt_u32 s67,s59
v_fma_f32 v94,v94,v119,v119
v_fma_f32 v95,v95,v119,v119
v_rcp_f32 v93,v93
v_rcp_f32 v94,v94
v_rcp_f32 v95,v95
v_mfma_f32_16x16x32_bf16 v[40:43],v[244:247],v[80:83],v[40:43]
v_mul_f32 v36,v36,v92
v_mul_f32 v37,v37,v93
v_mul_f32 v38,v38,v94
v_mul_f32 v39,v39,v95
v_cvt_pk_bf16_f32 v100,v36,v37
v_cvt_pk_bf16_f32 v101,v38,v39
v_mfma_f32_16x16x32_bf16 v[44:47],v[248:251],v[80:83],v[44:47]
s_waitcnt lgkmcnt(2)
ds_read_b128 v[68:71],v2 offset:4096
buffer_store_dwordx2 v[100:101],v9,s[36:39],s73 offen offset:0 sc0 sc1
v_mfma_f32_16x16x32_bf16 v[48:51],v[252:255],v[80:83],v[48:51]
s_cbranch_scc1 Lg3_d0w0_%=
s_branch Lg3_slow0_%=
Lg3_d0w1_%=:
v_mfma_f32_16x16x32_bf16 v[28:31],a[0:3],v[52:55],0
s_mov_b32 m0,s62
ds_read_b128 v[72:75],v2 offset:5120
v_mul_f32 v116,s75,v112
v_mfma_f32_16x16x32_bf16 v[32:35],a[4:7],v[52:55],0
s_mul_i32 s73,s47,7
v_fma_f32 v113,v113,s75,v12
v_fma_f32 v113,-v116,v116,v113
v_mfma_f32_16x16x32_bf16 v[36:39],a[8:11],v[52:55],0
buffer_load_dwordx4 v0,s[36:39],s61 offen offset:0 sc1 lds
v_mfma_f32_16x16x32_bf16 v[28:31],a[12:15],v[56:59],v[28:31]
s_lshr_b32 s73,s73,1
ds_read_b128 v[76:79],v2 offset:6144
v_max_f32 v113,v113,v12
v_mfma_f32_16x16x32_bf16 v[32:35],a[16:19],v[56:59],v[32:35]
s_add_u32 s53,s52,s55
v_rsq_f32 v112,v113
v_mfma_f32_16x16x32_bf16 v[36:39],a[20:23],v[56:59],v[36:39]
s_waitcnt lgkmcnt(3)
v_mul_f32 v117,s76,v112
v_mul_f32 v118,v112,v112
v_mfma_f32_16x16x32_bf16 v[28:31],a[24:27],v[60:63],v[28:31]
buffer_load_dwordx4 v0,s[36:39],s61 offen offset:1024 sc1 lds
v_mfma_f32_16x16x32_bf16 v[32:35],a[28:31],v[60:63],v[32:35]
s_cmp_eq_u32 s53,s56
ds_read_b128 v[80:83],v2 offset:7168
v_mul_f32 v119,v113,v112
v_mfma_f32_16x16x32_bf16 v[36:39],a[32:35],v[60:63],v[36:39]
s_cselect_b32 s53,s57,s53
v_mul_f32 v118,s77,v118
v_mul_f32 v118,v118,v117
v_mfma_f32_16x16x32_bf16 v[28:31],a[36:39],v[64:67],v[28:31]
s_cmp_eq_u32 s53,s58
ds_read_b128 v[52:55],v2 offset:8192
v_fma_f32 v40,-v116,v16,v40
v_mfma_f32_16x16x32_bf16 v[32:35],a[40:43],v[64:67],v[32:35]
buffer_load_dwordx4 v0,s[36:39],s61 offen offset:2048 sc1 lds
v_mfma_f32_16x16x32_bf16 v[36:39],a[44:47],v[64:67],v[36:39]
s_waitcnt lgkmcnt(3)
v_fma_f32 v41,-v116,v17,v41
v_fma_f32 v42,-v116,v18,v42
v_mfma_f32_16x16x32_bf16 v[28:31],a[48:51],v[68:71],v[28:31]
s_cselect_b32 s53,s59,s53
ds_read_b128 v[56:59],v2 offset:9216
v_fma_f32 v43,-v116,v19,v43
v_mfma_f32_16x16x32_bf16 v[32:35],a[52:55],v[68:71],v[32:35]
s_cmp_ge_u32 s66,s59
v_mul_f32 v84,v40,v40
v_mul_f32 v85,v41,v41
v_mfma_f32_16x16x32_bf16 v[36:39],a[56:59],v[68:71],v[36:39]
buffer_load_dwordx4 v0,s[36:39],s61 offen offset:3072 sc1 lds
v_mfma_f32_16x16x32_bf16 v[28:31],a[60:63],v[72:75],v[28:31]
s_cselect_b32 s86,s49,s50
ds_read_b128 v[60:63],v2 offset:10240
v_mul_f32 v86,v42,v42
v_mfma_f32_16x16x32_bf16 v[32:35],a[64:67],v[72:75],v[32:35]
s_add_u32 m0,s62,16384
v_mul_f32 v87,v43,v43
v_fma_f32 v84,v118,v84,v117
v_mfma_f32_16x16x32_bf16 v[36:39],a[68:71],v[72:75],v[36:39]
s_waitcnt lgkmcnt(3)
v_mul_f32 v84,v40,v84
v_fma_f32 v85,v118,v85,v117
v_mfma_f32_16x16x32_bf16 v[28:31],a[72:75],v[76:79],v[28:31]
s_lshr_b32 s72,s61,5
ds_read_b128 v[64:67],v2 offset:11264
v_mul_f32 v85,v41,v85
v_mfma_f32_16x16x32_bf16 v[32:35],a[76:79],v[76:79],v[32:35]
s_mov_b32 s62,s63
v_exp_f32 v84,v84
v_mfma_f32_16x16x32_bf16 v[36:39],a[80:83],v[76:79],v[36:39]
v_exp_f32 v85,v85
v_mfma_f32_16x16x32_bf16 v[28:31],a[84:87],v[80:83],v[28:31]
ds_read_b128 v[68:71],v2 offset:12288
v_fma_f32 v84,v84,v119,v119
v_mfma_f32_16x16x32_bf16 v[32:35],a[88:91],v[80:83],v[32:35]
v_rcp_f32 v84,v84
v_mfma_f32_16x16x32_bf16 v[36:39],a[92:95],v[80:83],v[36:39]
s_waitcnt lgkmcnt(3)
v_fma_f32 v85,v85,v119,v119
v_fma_f32 v86,v118,v86,v117
v_mfma_f32_16x16x32_bf16 v[28:31],a[96:99],v[52:55],v[28:31]
ds_read_b128 v[72:75],v2 offset:13312
v_mul_f32 v86,v42,v86
v_mfma_f32_16x16x32_bf16 v[32:35],a[100:103],v[52:55],v[32:35]
v_exp_f32 v86,v86
v_mfma_f32_16x16x32_bf16 v[36:39],a[104:107],v[52:55],v[36:39]
v_rcp_f32 v85,v85
v_mfma_f32_16x16x32_bf16 v[28:31],a[108:111],v[56:59],v[28:31]
ds_read_b128 v[76:79],v2 offset:14336
v_fma_f32 v86,v86,v119,v119
v_mfma_f32_16x16x32_bf16 v[32:35],a[112:115],v[56:59],v[32:35]
v_rcp_f32 v86,v86
v_mfma_f32_16x16x32_bf16 v[36:39],a[116:119],v[56:59],v[36:39]
s_waitcnt lgkmcnt(3)
v_fma_f32 v87,v118,v87,v117
v_mul_f32 v87,v43,v87
v_mfma_f32_16x16x32_bf16 v[28:31],a[120:123],v[60:63],v[28:31]
ds_read_b128 v[80:83],v2 offset:15360)"
R"(
v_mul_f32 v40,v40,v84
v_mfma_f32_16x16x32_bf16 v[32:35],a[124:127],v[60:63],v[32:35]
s_waitcnt vmcnt(6)
v_exp_f32 v87,v87
v_mfma_f32_16x16x32_bf16 v[36:39],a[128:131],v[60:63],v[36:39]
s_barrier
v_mfma_f32_16x16x32_bf16 v[28:31],a[132:135],v[64:67],v[28:31]
ds_read_b128 v[52:55],v2 offset:16384
v_fma_f32 v87,v87,v119,v119
v_mfma_f32_16x16x32_bf16 v[32:35],a[136:139],v[64:67],v[32:35]
v_rcp_f32 v87,v87
v_mfma_f32_16x16x32_bf16 v[36:39],a[140:143],v[64:67],v[36:39]
s_waitcnt lgkmcnt(3)
v_add_f32_dpp v104,v104,v104 quad_perm:[1,0,3,2] row_mask:0xf bank_mask:0xf
v_mul_f32 v41,v41,v85
v_mfma_f32_16x16x32_bf16 v[28:31],a[144:147],v[68:71],v[28:31]
ds_read_b128 v[56:59],v2 offset:17408
v_add_f32_dpp v104,v104,v104 quad_perm:[2,3,0,1] row_mask:0xf bank_mask:0xf
v_mfma_f32_16x16x32_bf16 v[32:35],a[148:151],v[68:71],v[32:35]
s_nop 0
v_add_f32_dpp v104,v104,v104 row_half_mirror row_mask:0xf bank_mask:0xf
v_mul_f32 v42,v42,v86
v_mfma_f32_16x16x32_bf16 v[36:39],a[152:155],v[68:71],v[36:39]
v_mul_f32 v43,v43,v87
v_cvt_pk_bf16_f32 v96,v40,v41
v_mfma_f32_16x16x32_bf16 v[28:31],a[156:159],v[72:75],v[28:31]
ds_read_b128 v[60:63],v2 offset:18432
v_cvt_pk_bf16_f32 v97,v42,v43
v_mfma_f32_16x16x32_bf16 v[32:35],a[160:163],v[72:75],v[32:35]
v_fma_f32 v44,-v116,v20,v44
v_fma_f32 v45,-v116,v21,v45
v_mfma_f32_16x16x32_bf16 v[36:39],a[164:167],v[72:75],v[36:39]
s_waitcnt lgkmcnt(3)
v_fma_f32 v46,-v116,v22,v46
v_fma_f32 v47,-v116,v23,v47
v_mfma_f32_16x16x32_bf16 v[28:31],a[168:171],v[76:79],v[28:31]
ds_read_b128 v[64:67],v2 offset:19456
v_mul_f32 v88,v44,v44
v_mfma_f32_16x16x32_bf16 v[32:35],a[172:175],v[76:79],v[32:35]
v_mul_f32 v89,v45,v45
v_mul_f32 v90,v46,v46
v_mfma_f32_16x16x32_bf16 v[36:39],a[176:179],v[76:79],v[36:39]
buffer_load_dwordx4 v1,s[36:39],s61 offen offset:0 sc1 lds
v_mfma_f32_16x16x32_bf16 v[28:31],a[180:183],v[80:83],v[28:31]
ds_read_b128 v[68:71],v2 offset:20480
v_mul_f32 v91,v47,v47
v_mfma_f32_16x16x32_bf16 v[32:35],a[184:187],v[80:83],v[32:35]
ds_write_b32 v10,v104 offset:0
v_mfma_f32_16x16x32_bf16 v[36:39],a[188:191],v[80:83],v[36:39]
s_waitcnt lgkmcnt(4)
v_fma_f32 v88,v118,v88,v117
v_mul_f32 v88,v44,v88
v_mfma_f32_16x16x32_bf16 v[28:31],a[192:195],v[52:55],v[28:31]
buffer_load_dwordx4 v1,s[36:39],s61 offen offset:1024 sc1 lds
v_mfma_f32_16x16x32_bf16 v[32:35],a[196:199],v[52:55],v[32:35]
ds_read_b128 v[72:75],v2 offset:21504
v_fma_f32 v89,v118,v89,v117
v_mfma_f32_16x16x32_bf16 v[36:39],a[200:203],v[52:55],v[36:39]
v_exp_f32 v88,v88
v_mfma_f32_16x16x32_bf16 v[28:31],a[204:207],v[56:59],v[28:31]
ds_read_b128 v[76:79],v2 offset:22528
v_mul_f32 v89,v45,v89
v_mfma_f32_16x16x32_bf16 v[32:35],a[208:211],v[56:59],v[32:35]
buffer_load_dwordx4 v1,s[36:39],s61 offen offset:2048 sc1 lds
v_mfma_f32_16x16x32_bf16 v[36:39],a[212:215],v[56:59],v[36:39]
s_waitcnt lgkmcnt(4)
v_exp_f32 v89,v89
v_mfma_f32_16x16x32_bf16 v[28:31],a[216:219],v[60:63],v[28:31]
ds_read_b128 v[80:83],v2 offset:23552
v_fma_f32 v88,v88,v119,v119
v_mfma_f32_16x16x32_bf16 v[32:35],a[220:223],v[60:63],v[32:35]
v_rcp_f32 v88,v88
v_mfma_f32_16x16x32_bf16 v[36:39],a[224:227],v[60:63],v[36:39]
buffer_load_dwordx4 v1,s[36:39],s61 offen offset:3072 sc1 lds
v_mfma_f32_16x16x32_bf16 v[28:31],a[228:231],v[64:67],v[28:31]
buffer_load_dword v108,v8,s[36:39],s72 offen offset:0 sc1
v_mfma_f32_16x16x32_bf16 v[32:35],a[232:235],v[64:67],v[32:35]
ds_read_b128 v[52:55],v2 offset:24576
v_fma_f32 v89,v89,v119,v119
v_mfma_f32_16x16x32_bf16 v[36:39],a[236:239],v[64:67],v[36:39]
s_waitcnt lgkmcnt(3)
v_rcp_f32 v89,v89
v_mfma_f32_16x16x32_bf16 v[28:31],a[240:243],v[68:71],v[28:31]
ds_read_b128 v[56:59],v2 offset:25600
v_fma_f32 v90,v118,v90,v117
v_mfma_f32_16x16x32_bf16 v[32:35],a[244:247],v[68:71],v[32:35]
s_waitcnt vmcnt(9)
v_mul_f32 v90,v46,v90
v_fma_f32 v91,v118,v91,v117
v_mfma_f32_16x16x32_bf16 v[36:39],a[248:251],v[68:71],v[36:39]
s_barrier
v_mfma_f32_16x16x32_bf16 v[28:31],a[252:255],v[72:75],v[28:31]
ds_read_b128 v[60:63],v2 offset:26624
v_mul_f32 v91,v47,v91
v_mfma_f32_16x16x32_bf16 v[32:35],v[128:131],v[72:75],v[32:35]
v_exp_f32 v90,v90
v_mfma_f32_16x16x32_bf16 v[36:39],v[132:135],v[72:75],v[36:39]
s_waitcnt lgkmcnt(3)
v_exp_f32 v91,v91
v_mfma_f32_16x16x32_bf16 v[28:31],v[136:139],v[76:79],v[28:31]
ds_read_b128 v[64:67],v2 offset:27648
v_fma_f32 v90,v90,v119,v119
v_mfma_f32_16x16x32_bf16 v[32:35],v[140:143],v[76:79],v[32:35]
v_rcp_f32 v90,v90
v_mfma_f32_16x16x32_bf16 v[36:39],v[144:147],v[76:79],v[36:39]
v_fma_f32 v91,v91,v119,v119
v_mul_f32 v44,v44,v88
v_mfma_f32_16x16x32_bf16 v[28:31],v[148:151],v[80:83],v[28:31]
ds_read_b128 v[68:71],v2 offset:28672
v_mul_f32 v45,v45,v89
v_mfma_f32_16x16x32_bf16 v[32:35],v[152:155],v[80:83],v[32:35]
v_rcp_f32 v91,v91
v_mfma_f32_16x16x32_bf16 v[36:39],v[156:159],v[80:83],v[36:39]
s_waitcnt lgkmcnt(3)
v_mul_f32 v46,v46,v90
v_mul_f32 v47,v47,v91
v_mfma_f32_16x16x32_bf16 v[28:31],v[160:163],v[52:55],v[28:31]
ds_read_b128 v[72:75],v2 offset:29696
v_cvt_pk_bf16_f32 v98,v44,v45
v_mfma_f32_16x16x32_bf16 v[32:35],v[164:167],v[52:55],v[32:35]
v_cvt_pk_bf16_f32 v99,v46,v47
v_fma_f32 v48,-v116,v24,v48
v_mfma_f32_16x16x32_bf16 v[36:39],v[168:171],v[52:55],v[36:39]
v_fma_f32 v49,-v116,v25,v49
v_fma_f32 v50,-v116,v26,v50
v_mfma_f32_16x16x32_bf16 v[28:31],v[172:175],v[56:59],v[28:31]
buffer_store_dwordx4 v[96:99],v3,s[36:39],s73 offen offset:0 sc0 sc1
ds_read_b128 v[76:79],v2 offset:30720
v_fma_f32 v51,-v116,v27,v51
v_mul_f32 v92,v48,v48
v_mfma_f32_16x16x32_bf16 v[32:35],v[176:179],v[56:59],v[32:35]
v_mul_f32 v93,v49,v49
v_mul_f32 v94,v50,v50
v_mfma_f32_16x16x32_bf16 v[36:39],v[180:183],v[56:59],v[36:39]
s_waitcnt lgkmcnt(3)
v_mul_f32 v95,v51,v51
v_fma_f32 v92,v118,v92,v117
v_mfma_f32_16x16x32_bf16 v[28:31],v[184:187],v[60:63],v[28:31]
ds_read_b128 v[80:83],v2 offset:31744
v_mul_f32 v92,v48,v92
v_mfma_f32_16x16x32_bf16 v[32:35],v[188:191],v[60:63],v[32:35]
s_waitcnt vmcnt(6)
v_exp_f32 v92,v92
v_mfma_f32_16x16x32_bf16 v[36:39],v[192:195],v[60:63],v[36:39]
s_barrier
v_mfma_f32_16x16x32_bf16 v[28:31],v[196:199],v[64:67],v[28:31]
ds_read_b128 v[52:55],v2 offset:32768
ds_read_b32 v13,v7 offset:0
v_mfma_f32_16x16x32_bf16 v[32:35],v[200:203],v[64:67],v[32:35]
ds_read_b64 v[112:113],v11 offset:0
v_fma_f32 v92,v92,v119,v119
v_mfma_f32_16x16x32_bf16 v[36:39],v[204:207],v[64:67],v[36:39]
s_waitcnt lgkmcnt(5)
v_rcp_f32 v92,v92
v_mfma_f32_16x16x32_bf16 v[28:31],v[208:211],v[68:71],v[28:31]
ds_read_b128 v[56:59],v2 offset:33792
v_fma_f32 v93,v118,v93,v117
v_mfma_f32_16x16x32_bf16 v[32:35],v[212:215],v[68:71],v[32:35]
s_waitcnt lgkmcnt(2)
v_mul_f32 v93,v49,v93
v_fma_f32 v94,v118,v94,v117
v_mfma_f32_16x16x32_bf16 v[36:39],v[216:219],v[68:71],v[36:39]
s_waitcnt lgkmcnt(1)
v_cmp_eq_u32 vcc,s60,v13
v_exp_f32 v93,v93
v_mul_f32 v94,v50,v94
v_mfma_f32_16x16x32_bf16 v[28:31],v[220:223],v[72:75],v[28:31]
s_cmp_eq_u32 vcc_lo,-1
ds_read_b128 v[60:63],v2 offset:34816
v_fma_f32 v93,v93,v119,v119
v_mfma_f32_16x16x32_bf16 v[32:35],v[224:227],v[72:75],v[32:35]
s_cselect_b32 s67,s86,s59
v_exp_f32 v94,v94
v_mfma_f32_16x16x32_bf16 v[36:39],v[228:231],v[72:75],v[36:39]
v_fma_f32 v95,v118,v95,v117
v_mul_f32 v95,v51,v95
v_exp_f32 v95,v95
v_mfma_f32_16x16x32_bf16 v[28:31],v[232:235],v[76:79],v[28:31]
s_lshr_b32 s61,s67,0
v_fma_f32 v94,v94,v119,v119
v_fma_f32 v95,v95,v119,v119
v_rcp_f32 v93,v93
v_rcp_f32 v94,v94
v_rcp_f32 v95,v95
v_mfma_f32_16x16x32_bf16 v[32:35],v[236:239],v[76:79],v[32:35]
s_xor_b32 s63,s63,65536
v_mul_f32 v48,v48,v92
v_mul_f32 v49,v49,v93
v_mul_f32 v50,v50,v94
v_mul_f32 v51,v51,v95
v_cvt_pk_bf16_f32 v100,v48,v49
v_cvt_pk_bf16_f32 v101,v50,v51
v_mfma_f32_16x16x32_bf16 v[36:39],v[240:243],v[76:79],v[36:39]
s_cmp_ge_u32 s66,s59
ds_read_b128 v[64:67],v2 offset:35840
buffer_store_dwordx2 v[100:101],v9,s[36:39],s73 offen offset:0 sc0 sc1
v_mfma_f32_16x16x32_bf16 v[28:31],v[244:247],v[80:83],v[28:31]
ds_read_b128 v[68:71],v2 offset:36864
v_mfma_f32_16x16x32_bf16 v[32:35],v[248:251],v[80:83],v[32:35]
s_waitcnt lgkmcnt(3))"
R"(
v_mfma_f32_16x16x32_bf16 v[36:39],v[252:255],v[80:83],v[36:39]
s_cbranch_scc1 Lg3_slow1_%=
Lg3_d1w1_%=:
v_mfma_f32_16x16x32_bf16 v[40:43],a[0:3],v[52:55],0
s_mov_b32 m0,s62
ds_read_b128 v[72:75],v2 offset:37888
v_mul_f32 v116,s75,v112
v_mfma_f32_16x16x32_bf16 v[44:47],a[4:7],v[52:55],0
s_mul_i32 s73,s48,7
v_fma_f32 v113,v113,s75,v12
v_fma_f32 v113,-v116,v116,v113
v_mfma_f32_16x16x32_bf16 v[48:51],a[8:11],v[52:55],0
buffer_load_dwordx4 v0,s[36:39],s61 offen offset:0 sc1 lds
v_mfma_f32_16x16x32_bf16 v[40:43],a[12:15],v[56:59],v[40:43]
s_lshr_b32 s73,s73,1
ds_read_b128 v[76:79],v2 offset:38912
v_max_f32 v113,v113,v12
v_mfma_f32_16x16x32_bf16 v[44:47],a[16:19],v[56:59],v[44:47]
s_add_u32 s74,s53,s55
v_rsq_f32 v112,v113
v_mfma_f32_16x16x32_bf16 v[48:51],a[20:23],v[56:59],v[48:51]
s_waitcnt lgkmcnt(3)
v_mul_f32 v117,s76,v112
v_mul_f32 v118,v112,v112
v_mfma_f32_16x16x32_bf16 v[40:43],a[24:27],v[60:63],v[40:43]
buffer_load_dwordx4 v0,s[36:39],s61 offen offset:1024 sc1 lds
v_mfma_f32_16x16x32_bf16 v[44:47],a[28:31],v[60:63],v[44:47]
s_cmp_eq_u32 s74,s56
ds_read_b128 v[80:83],v2 offset:39936
v_mul_f32 v119,v113,v112
v_mfma_f32_16x16x32_bf16 v[48:51],a[32:35],v[60:63],v[48:51]
s_cselect_b32 s74,s57,s74
v_mul_f32 v118,s77,v118
v_mul_f32 v118,v118,v117
v_mfma_f32_16x16x32_bf16 v[40:43],a[36:39],v[64:67],v[40:43]
s_cmp_eq_u32 s74,s58
ds_read_b128 v[52:55],v2 offset:40960
v_fma_f32 v28,-v116,v16,v28
v_mfma_f32_16x16x32_bf16 v[44:47],a[40:43],v[64:67],v[44:47]
buffer_load_dwordx4 v0,s[36:39],s61 offen offset:2048 sc1 lds
v_mfma_f32_16x16x32_bf16 v[48:51],a[44:47],v[64:67],v[48:51]
s_waitcnt lgkmcnt(3)
v_fma_f32 v29,-v116,v17,v29
v_fma_f32 v30,-v116,v18,v30
v_mfma_f32_16x16x32_bf16 v[40:43],a[48:51],v[68:71],v[40:43]
s_cselect_b32 s74,s59,s74
ds_read_b128 v[56:59],v2 offset:41984
v_fma_f32 v31,-v116,v19,v31
v_mfma_f32_16x16x32_bf16 v[44:47],a[52:55],v[68:71],v[44:47]
s_cmp_ge_u32 s67,s59
v_mul_f32 v84,v28,v28
v_mul_f32 v85,v29,v29
v_mfma_f32_16x16x32_bf16 v[48:51],a[56:59],v[68:71],v[48:51]
buffer_load_dwordx4 v0,s[36:39],s61 offen offset:3072 sc1 lds
v_mfma_f32_16x16x32_bf16 v[40:43],a[60:63],v[72:75],v[40:43]
s_cselect_b32 s86,s50,s51
ds_read_b128 v[60:63],v2 offset:43008
v_mul_f32 v86,v30,v30
v_mfma_f32_16x16x32_bf16 v[44:47],a[64:67],v[72:75],v[44:47]
s_add_u32 m0,s62,16384
v_mul_f32 v87,v31,v31
v_fma_f32 v84,v118,v84,v117
v_mfma_f32_16x16x32_bf16 v[48:51],a[68:71],v[72:75],v[48:51]
s_waitcnt lgkmcnt(3)
v_mul_f32 v84,v28,v84
v_fma_f32 v85,v118,v85,v117
v_mfma_f32_16x16x32_bf16 v[40:43],a[72:75],v[76:79],v[40:43]
s_lshr_b32 s72,s61,5
ds_read_b128 v[64:67],v2 offset:44032
v_mul_f32 v85,v29,v85
v_mfma_f32_16x16x32_bf16 v[44:47],a[76:79],v[76:79],v[44:47]
s_mov_b32 s62,s64
v_exp_f32 v84,v84
v_mfma_f32_16x16x32_bf16 v[48:51],a[80:83],v[76:79],v[48:51]
s_mov_b64 s[44:45],s[46:47]
v_exp_f32 v85,v85
v_mfma_f32_16x16x32_bf16 v[40:43],a[84:87],v[80:83],v[40:43]
s_mov_b64 s[46:47],s[48:49]
ds_read_b128 v[68:71],v2 offset:45056
v_fma_f32 v84,v84,v119,v119
v_mfma_f32_16x16x32_bf16 v[44:47],a[88:91],v[80:83],v[44:47]
s_mov_b64 s[48:49],s[50:51]
v_rcp_f32 v84,v84
v_mfma_f32_16x16x32_bf16 v[48:51],a[92:95],v[80:83],v[48:51]
s_waitcnt lgkmcnt(3)
v_fma_f32 v85,v85,v119,v119
v_fma_f32 v86,v118,v86,v117
v_mfma_f32_16x16x32_bf16 v[40:43],a[96:99],v[52:55],v[40:43]
s_mov_b64 s[50:51],s[52:53]
ds_read_b128 v[72:75],v2 offset:46080
v_mul_f32 v86,v30,v86
v_mfma_f32_16x16x32_bf16 v[44:47],a[100:103],v[52:55],v[44:47]
s_mov_b32 s52,s74
v_exp_f32 v86,v86
v_mfma_f32_16x16x32_bf16 v[48:51],a[104:107],v[52:55],v[48:51]
v_rcp_f32 v85,v85
v_mfma_f32_16x16x32_bf16 v[40:43],a[108:111],v[56:59],v[40:43]
ds_read_b128 v[76:79],v2 offset:47104
v_fma_f32 v86,v86,v119,v119
v_mfma_f32_16x16x32_bf16 v[44:47],a[112:115],v[56:59],v[44:47]
v_rcp_f32 v86,v86
v_mfma_f32_16x16x32_bf16 v[48:51],a[116:119],v[56:59],v[48:51]
s_waitcnt lgkmcnt(3)
v_fma_f32 v87,v118,v87,v117
v_mul_f32 v87,v31,v87
v_mfma_f32_16x16x32_bf16 v[40:43],a[120:123],v[60:63],v[40:43]
ds_read_b128 v[80:83],v2 offset:48128
v_mul_f32 v28,v28,v84
v_mfma_f32_16x16x32_bf16 v[44:47],a[124:127],v[60:63],v[44:47]
s_waitcnt vmcnt(6)
v_exp_f32 v87,v87
v_mfma_f32_16x16x32_bf16 v[48:51],a[128:131],v[60:63],v[48:51]
s_barrier
v_mfma_f32_16x16x32_bf16 v[40:43],a[132:135],v[64:67],v[40:43]
ds_read_b128 v[52:55],v2 offset:49152
v_fma_f32 v87,v87,v119,v119
v_mfma_f32_16x16x32_bf16 v[44:47],a[136:139],v[64:67],v[44:47]
v_rcp_f32 v87,v87
v_mfma_f32_16x16x32_bf16 v[48:51],a[140:143],v[64:67],v[48:51]
s_waitcnt lgkmcnt(3)
v_add_f32_dpp v108,v108,v108 quad_perm:[1,0,3,2] row_mask:0xf bank_mask:0xf
v_mul_f32 v29,v29,v85
v_mfma_f32_16x16x32_bf16 v[40:43],a[144:147],v[68:71],v[40:43]
ds_read_b128 v[56:59],v2 offset:50176
v_add_f32_dpp v108,v108,v108 quad_perm:[2,3,0,1] row_mask:0xf bank_mask:0xf
v_mfma_f32_16x16x32_bf16 v[44:47],a[148:151],v[68:71],v[44:47]
s_nop 0
v_add_f32_dpp v108,v108,v108 row_half_mirror row_mask:0xf bank_mask:0xf
v_mul_f32 v30,v30,v86
v_mfma_f32_16x16x32_bf16 v[48:51],a[152:155],v[68:71],v[48:51]
v_mul_f32 v31,v31,v87
v_cvt_pk_bf16_f32 v96,v28,v29
v_mfma_f32_16x16x32_bf16 v[40:43],a[156:159],v[72:75],v[40:43]
ds_read_b128 v[60:63],v2 offset:51200
v_cvt_pk_bf16_f32 v97,v30,v31
v_mfma_f32_16x16x32_bf16 v[44:47],a[160:163],v[72:75],v[44:47]
v_fma_f32 v32,-v116,v20,v32
v_fma_f32 v33,-v116,v21,v33
v_mfma_f32_16x16x32_bf16 v[48:51],a[164:167],v[72:75],v[48:51]
s_waitcnt lgkmcnt(3)
v_fma_f32 v34,-v116,v22,v34
v_fma_f32 v35,-v116,v23,v35
v_mfma_f32_16x16x32_bf16 v[40:43],a[168:171],v[76:79],v[40:43]
ds_read_b128 v[64:67],v2 offset:52224
v_mul_f32 v88,v32,v32
v_mfma_f32_16x16x32_bf16 v[44:47],a[172:175],v[76:79],v[44:47]
v_mul_f32 v89,v33,v33
v_mul_f32 v90,v34,v34
v_mfma_f32_16x16x32_bf16 v[48:51],a[176:179],v[76:79],v[48:51]
buffer_load_dwordx4 v1,s[36:39],s61 offen offset:0 sc1 lds
v_mfma_f32_16x16x32_bf16 v[40:43],a[180:183],v[80:83],v[40:43]
ds_read_b128 v[68:71],v2 offset:53248
v_mul_f32 v91,v35,v35
v_mfma_f32_16x16x32_bf16 v[44:47],a[184:187],v[80:83],v[44:47]
ds_write_b32 v10,v108 offset:128
v_mfma_f32_16x16x32_bf16 v[48:51],a[188:191],v[80:83],v[48:51]
s_waitcnt lgkmcnt(4)
v_fma_f32 v88,v118,v88,v117
v_mul_f32 v88,v32,v88
v_mfma_f32_16x16x32_bf16 v[40:43],a[192:195],v[52:55],v[40:43]
buffer_load_dwordx4 v1,s[36:39],s61 offen offset:1024 sc1 lds
v_mfma_f32_16x16x32_bf16 v[44:47],a[196:199],v[52:55],v[44:47]
ds_read_b128 v[72:75],v2 offset:54272
v_fma_f32 v89,v118,v89,v117
v_mfma_f32_16x16x32_bf16 v[48:51],a[200:203],v[52:55],v[48:51]
v_exp_f32 v88,v88
v_mfma_f32_16x16x32_bf16 v[40:43],a[204:207],v[56:59],v[40:43]
ds_read_b128 v[76:79],v2 offset:55296
v_mul_f32 v89,v33,v89
v_mfma_f32_16x16x32_bf16 v[44:47],a[208:211],v[56:59],v[44:47]
buffer_load_dwordx4 v1,s[36:39],s61 offen offset:2048 sc1 lds
v_mfma_f32_16x16x32_bf16 v[48:51],a[212:215],v[56:59],v[48:51]
s_waitcnt lgkmcnt(4)
v_exp_f32 v89,v89
v_mfma_f32_16x16x32_bf16 v[40:43],a[216:219],v[60:63],v[40:43]
ds_read_b128 v[80:83],v2 offset:56320
v_fma_f32 v88,v88,v119,v119
v_mfma_f32_16x16x32_bf16 v[44:47],a[220:223],v[60:63],v[44:47]
v_rcp_f32 v88,v88
v_mfma_f32_16x16x32_bf16 v[48:51],a[224:227],v[60:63],v[48:51]
buffer_load_dwordx4 v1,s[36:39],s61 offen offset:3072 sc1 lds
v_mfma_f32_16x16x32_bf16 v[40:43],a[228:231],v[64:67],v[40:43]
buffer_load_dword v104,v8,s[36:39],s72 offen offset:0 sc1
v_mfma_f32_16x16x32_bf16 v[44:47],a[232:235],v[64:67],v[44:47]
ds_read_b128 v[52:55],v2 offset:57344
v_fma_f32 v89,v89,v119,v119
v_mfma_f32_16x16x32_bf16 v[48:51],a[236:239],v[64:67],v[48:51]
s_waitcnt lgkmcnt(3)
v_rcp_f32 v89,v89
v_mfma_f32_16x16x32_bf16 v[40:43],a[240:243],v[68:71],v[40:43]
ds_read_b128 v[56:59],v2 offset:58368
v_fma_f32 v90,v118,v90,v117
v_mfma_f32_16x16x32_bf16 v[44:47],a[244:247],v[68:71],v[44:47]
s_waitcnt vmcnt(9)
v_mul_f32 v90,v34,v90
v_fma_f32 v91,v118,v91,v117
v_mfma_f32_16x16x32_bf16 v[48:51],a[248:251],v[68:71],v[48:51]
s_barrier
v_mfma_f32_16x16x32_bf16 v[40:43],a[252:255],v[72:75],v[40:43]
ds_read_b128 v[60:63],v2 offset:59392)"
R"(
v_mul_f32 v91,v35,v91
v_mfma_f32_16x16x32_bf16 v[44:47],v[128:131],v[72:75],v[44:47]
v_exp_f32 v90,v90
v_mfma_f32_16x16x32_bf16 v[48:51],v[132:135],v[72:75],v[48:51]
s_waitcnt lgkmcnt(3)
v_exp_f32 v91,v91
v_mfma_f32_16x16x32_bf16 v[40:43],v[136:139],v[76:79],v[40:43]
ds_read_b128 v[64:67],v2 offset:60416
v_fma_f32 v90,v90,v119,v119
v_mfma_f32_16x16x32_bf16 v[44:47],v[140:143],v[76:79],v[44:47]
v_rcp_f32 v90,v90
v_mfma_f32_16x16x32_bf16 v[48:51],v[144:147],v[76:79],v[48:51]
v_fma_f32 v91,v91,v119,v119
v_mul_f32 v32,v32,v88
v_mfma_f32_16x16x32_bf16 v[40:43],v[148:151],v[80:83],v[40:43]
ds_read_b128 v[68:71],v2 offset:61440
v_mul_f32 v33,v33,v89
v_mfma_f32_16x16x32_bf16 v[44:47],v[152:155],v[80:83],v[44:47]
v_rcp_f32 v91,v91
v_mfma_f32_16x16x32_bf16 v[48:51],v[156:159],v[80:83],v[48:51]
s_waitcnt lgkmcnt(3)
v_mul_f32 v34,v34,v90
v_mul_f32 v35,v35,v91
v_mfma_f32_16x16x32_bf16 v[40:43],v[160:163],v[52:55],v[40:43]
ds_read_b128 v[72:75],v2 offset:62464
v_cvt_pk_bf16_f32 v98,v32,v33
v_mfma_f32_16x16x32_bf16 v[44:47],v[164:167],v[52:55],v[44:47]
v_cvt_pk_bf16_f32 v99,v34,v35
v_fma_f32 v36,-v116,v24,v36
v_mfma_f32_16x16x32_bf16 v[48:51],v[168:171],v[52:55],v[48:51]
v_fma_f32 v37,-v116,v25,v37
v_fma_f32 v38,-v116,v26,v38
v_mfma_f32_16x16x32_bf16 v[40:43],v[172:175],v[56:59],v[40:43]
buffer_store_dwordx4 v[96:99],v3,s[36:39],s73 offen offset:0 sc0 sc1
ds_read_b128 v[76:79],v2 offset:63488
v_fma_f32 v39,-v116,v27,v39
v_mul_f32 v92,v36,v36
v_mfma_f32_16x16x32_bf16 v[44:47],v[176:179],v[56:59],v[44:47]
v_mul_f32 v93,v37,v37
v_mul_f32 v94,v38,v38
v_mfma_f32_16x16x32_bf16 v[48:51],v[180:183],v[56:59],v[48:51]
s_waitcnt lgkmcnt(3)
v_mul_f32 v95,v39,v39
v_fma_f32 v92,v118,v92,v117
v_mfma_f32_16x16x32_bf16 v[40:43],v[184:187],v[60:63],v[40:43]
ds_read_b128 v[80:83],v2 offset:64512
v_mul_f32 v92,v36,v92
v_mfma_f32_16x16x32_bf16 v[44:47],v[188:191],v[60:63],v[44:47]
s_waitcnt vmcnt(6)
v_xor_b32 v2,65536,v2
v_fma_f32 v93,v118,v93,v117
v_mfma_f32_16x16x32_bf16 v[48:51],v[192:195],v[60:63],v[48:51]
s_barrier
v_mfma_f32_16x16x32_bf16 v[40:43],v[196:199],v[64:67],v[40:43]
ds_read_b128 v[52:55],v2 offset:0
ds_read_b32 v13,v7 offset:256
v_mfma_f32_16x16x32_bf16 v[44:47],v[200:203],v[64:67],v[44:47]
ds_read_b64 v[112:113],v11 offset:128
v_mul_f32 v93,v37,v93
v_mfma_f32_16x16x32_bf16 v[48:51],v[204:207],v[64:67],v[48:51]
s_waitcnt lgkmcnt(5)
v_exp_f32 v92,v92
v_mfma_f32_16x16x32_bf16 v[40:43],v[208:211],v[68:71],v[40:43]
ds_read_b128 v[56:59],v2 offset:1024
v_fma_f32 v92,v92,v119,v119
v_mfma_f32_16x16x32_bf16 v[44:47],v[212:215],v[68:71],v[44:47]
s_waitcnt lgkmcnt(2)
v_exp_f32 v93,v93
v_mfma_f32_16x16x32_bf16 v[48:51],v[216:219],v[68:71],v[48:51]
s_waitcnt lgkmcnt(1)
v_cmp_eq_u32 vcc,s60,v13
v_rcp_f32 v92,v92
v_fma_f32 v93,v93,v119,v119
v_mfma_f32_16x16x32_bf16 v[40:43],v[220:223],v[72:75],v[40:43]
s_cmp_eq_u32 vcc_lo,-1
ds_read_b128 v[60:63],v2 offset:2048
v_fma_f32 v94,v118,v94,v117
v_mfma_f32_16x16x32_bf16 v[44:47],v[224:227],v[72:75],v[44:47]
s_cselect_b32 s66,s86,s59
v_rcp_f32 v93,v93
v_mfma_f32_16x16x32_bf16 v[48:51],v[228:231],v[72:75],v[48:51]
v_fma_f32 v95,v118,v95,v117
v_mul_f32 v94,v38,v94
v_mul_f32 v95,v39,v95
v_exp_f32 v94,v94
v_exp_f32 v95,v95
v_mfma_f32_16x16x32_bf16 v[40:43],v[232:235],v[76:79],v[40:43]
s_lshr_b32 s61,s66,0
v_fma_f32 v94,v94,v119,v119
v_fma_f32 v95,v95,v119,v119
v_rcp_f32 v94,v94
v_rcp_f32 v95,v95
v_mfma_f32_16x16x32_bf16 v[44:47],v[236:239],v[76:79],v[44:47]
s_xor_b32 s64,s64,65536
v_mul_f32 v36,v36,v92
v_mul_f32 v37,v37,v93
v_mul_f32 v38,v38,v94
v_mul_f32 v39,v39,v95
v_cvt_pk_bf16_f32 v100,v36,v37
v_cvt_pk_bf16_f32 v101,v38,v39
v_mfma_f32_16x16x32_bf16 v[48:51],v[240:243],v[76:79],v[48:51]
s_cmp_lt_u32 s67,s59
ds_read_b128 v[64:67],v2 offset:3072
buffer_store_dwordx2 v[100:101],v9,s[36:39],s73 offen offset:0 sc0 sc1
v_mfma_f32_16x16x32_bf16 v[40:43],v[244:247],v[80:83],v[40:43]
ds_read_b128 v[68:71],v2 offset:4096
v_mfma_f32_16x16x32_bf16 v[44:47],v[248:251],v[80:83],v[44:47]
s_waitcnt lgkmcnt(3)
v_mfma_f32_16x16x32_bf16 v[48:51],v[252:255],v[80:83],v[48:51]
s_cbranch_scc1 Lg3_d0w1_%=
s_branch Lg3_slow0_%=
Lg3_d0w2_%=:
v_mfma_f32_16x16x32_bf16 v[28:31],a[0:3],v[52:55],0
s_mov_b32 m0,s62
ds_read_b128 v[72:75],v2 offset:5120
v_mul_f32 v116,s75,v112
v_mfma_f32_16x16x32_bf16 v[32:35],a[4:7],v[52:55],0
s_mul_i32 s73,s47,7
v_fma_f32 v113,v113,s75,v12
v_fma_f32 v113,-v116,v116,v113
v_mfma_f32_16x16x32_bf16 v[36:39],a[8:11],v[52:55],0
s_lshr_b32 s73,s73,1
v_max_f32 v113,v113,v12
v_rsq_f32 v112,v113
v_mfma_f32_16x16x32_bf16 v[28:31],a[12:15],v[56:59],v[28:31]
buffer_load_dwordx4 v0,s[36:39],s61 offen offset:0 sc1 lds
v_mfma_f32_16x16x32_bf16 v[32:35],a[16:19],v[56:59],v[32:35]
s_add_u32 s53,s52,s55
ds_read_b128 v[76:79],v2 offset:6144
v_mul_f32 v117,s76,v112
v_mfma_f32_16x16x32_bf16 v[36:39],a[20:23],v[56:59],v[36:39]
s_waitcnt lgkmcnt(3)
v_mul_f32 v118,v112,v112
v_mul_f32 v119,v113,v112
v_mfma_f32_16x16x32_bf16 v[28:31],a[24:27],v[60:63],v[28:31]
s_cmp_eq_u32 s53,s56
ds_read_b128 v[80:83],v2 offset:7168
v_mul_f32 v118,s77,v118
v_mfma_f32_16x16x32_bf16 v[32:35],a[28:31],v[60:63],v[32:35]
buffer_load_dwordx4 v0,s[36:39],s61 offen offset:1024 sc1 lds
v_mfma_f32_16x16x32_bf16 v[36:39],a[32:35],v[60:63],v[36:39]
s_cselect_b32 s53,s57,s53
v_mul_f32 v118,v118,v117
v_fma_f32 v40,-v116,v16,v40
v_mfma_f32_16x16x32_bf16 v[28:31],a[36:39],v[64:67],v[28:31]
s_cmp_eq_u32 s53,s58
ds_read_b128 v[52:55],v2 offset:8192
v_fma_f32 v41,-v116,v17,v41
v_mfma_f32_16x16x32_bf16 v[32:35],a[40:43],v[64:67],v[32:35]
s_cselect_b32 s53,s59,s53
v_fma_f32 v42,-v116,v18,v42
v_fma_f32 v43,-v116,v19,v43
v_mfma_f32_16x16x32_bf16 v[36:39],a[44:47],v[64:67],v[36:39]
s_waitcnt lgkmcnt(3)
buffer_load_dwordx4 v0,s[36:39],s61 offen offset:2048 sc1 lds
v_mfma_f32_16x16x32_bf16 v[28:31],a[48:51],v[68:71],v[28:31]
s_cmp_ge_u32 s66,s59
ds_read_b128 v[56:59],v2 offset:9216
v_mul_f32 v84,v40,v40
v_mfma_f32_16x16x32_bf16 v[32:35],a[52:55],v[68:71],v[32:35]
s_cselect_b32 s86,s49,s50
v_mul_f32 v85,v41,v41
v_mul_f32 v86,v42,v42
v_mfma_f32_16x16x32_bf16 v[36:39],a[56:59],v[68:71],v[36:39]
v_mul_f32 v87,v43,v43
v_fma_f32 v84,v118,v84,v117
v_mul_f32 v84,v40,v84
v_mfma_f32_16x16x32_bf16 v[28:31],a[60:63],v[72:75],v[28:31]
buffer_load_dwordx4 v0,s[36:39],s61 offen offset:3072 sc1 lds
v_mfma_f32_16x16x32_bf16 v[32:35],a[64:67],v[72:75],v[32:35]
s_add_u32 m0,s62,16384
ds_read_b128 v[60:63],v2 offset:10240
v_fma_f32 v85,v118,v85,v117
v_mfma_f32_16x16x32_bf16 v[36:39],a[68:71],v[72:75],v[36:39]
s_waitcnt lgkmcnt(3)
v_exp_f32 v84,v84
v_mfma_f32_16x16x32_bf16 v[28:31],a[72:75],v[76:79],v[28:31]
s_lshr_b32 s72,s61,5
ds_read_b128 v[64:67],v2 offset:11264
v_mul_f32 v85,v41,v85
v_mfma_f32_16x16x32_bf16 v[32:35],a[76:79],v[76:79],v[32:35]
s_mov_b32 s62,s63
v_exp_f32 v85,v85
v_mfma_f32_16x16x32_bf16 v[36:39],a[80:83],v[76:79],v[36:39]
v_fma_f32 v84,v84,v119,v119
v_fma_f32 v85,v85,v119,v119
v_mfma_f32_16x16x32_bf16 v[28:31],a[84:87],v[80:83],v[28:31]
ds_read_b128 v[68:71],v2 offset:12288
v_fma_f32 v86,v118,v86,v117
v_mfma_f32_16x16x32_bf16 v[32:35],a[88:91],v[80:83],v[32:35]
v_rcp_f32 v84,v84
v_mfma_f32_16x16x32_bf16 v[36:39],a[92:95],v[80:83],v[36:39]
s_waitcnt lgkmcnt(3)
v_rcp_f32 v85,v85
v_mfma_f32_16x16x32_bf16 v[28:31],a[96:99],v[52:55],v[28:31]
ds_read_b128 v[72:75],v2 offset:13312
v_mul_f32 v86,v42,v86
v_mfma_f32_16x16x32_bf16 v[32:35],a[100:103],v[52:55],v[32:35]
v_exp_f32 v86,v86
v_mfma_f32_16x16x32_bf16 v[36:39],a[104:107],v[52:55],v[36:39])"
R"(
v_fma_f32 v86,v86,v119,v119
v_fma_f32 v87,v118,v87,v117
v_mfma_f32_16x16x32_bf16 v[28:31],a[108:111],v[56:59],v[28:31]
ds_read_b128 v[76:79],v2 offset:14336
v_rcp_f32 v86,v86
v_mfma_f32_16x16x32_bf16 v[32:35],a[112:115],v[56:59],v[32:35]
v_mul_f32 v87,v43,v87
v_mul_f32 v40,v40,v84
v_mfma_f32_16x16x32_bf16 v[36:39],a[116:119],v[56:59],v[36:39]
s_waitcnt lgkmcnt(3)
v_exp_f32 v87,v87
v_mfma_f32_16x16x32_bf16 v[28:31],a[120:123],v[60:63],v[28:31]
ds_read_b128 v[80:83],v2 offset:15360
v_fma_f32 v87,v87,v119,v119
v_mfma_f32_16x16x32_bf16 v[32:35],a[124:127],v[60:63],v[32:35]
s_waitcnt vmcnt(6)
v_rcp_f32 v87,v87
v_mfma_f32_16x16x32_bf16 v[36:39],a[128:131],v[60:63],v[36:39]
s_barrier
v_mfma_f32_16x16x32_bf16 v[28:31],a[132:135],v[64:67],v[28:31]
ds_read_b128 v[52:55],v2 offset:16384
v_add_f32_dpp v104,v104,v104 quad_perm:[1,0,3,2] row_mask:0xf bank_mask:0xf
v_mfma_f32_16x16x32_bf16 v[32:35],a[136:139],v[64:67],v[32:35]
s_nop 0
v_add_f32_dpp v104,v104,v104 quad_perm:[2,3,0,1] row_mask:0xf bank_mask:0xf
v_mul_f32 v41,v41,v85
v_mfma_f32_16x16x32_bf16 v[36:39],a[140:143],v[64:67],v[36:39]
s_waitcnt lgkmcnt(3)
v_add_f32_dpp v104,v104,v104 row_half_mirror row_mask:0xf bank_mask:0xf
v_mul_f32 v42,v42,v86
v_mfma_f32_16x16x32_bf16 v[28:31],a[144:147],v[68:71],v[28:31]
ds_read_b128 v[56:59],v2 offset:17408
v_mul_f32 v43,v43,v87
v_mfma_f32_16x16x32_bf16 v[32:35],a[148:151],v[68:71],v[32:35]
v_cvt_pk_bf16_f32 v96,v40,v41
v_cvt_pk_bf16_f32 v97,v42,v43
v_mfma_f32_16x16x32_bf16 v[36:39],a[152:155],v[68:71],v[36:39]
v_fma_f32 v44,-v116,v20,v44
v_fma_f32 v45,-v116,v21,v45
v_mfma_f32_16x16x32_bf16 v[28:31],a[156:159],v[72:75],v[28:31]
ds_read_b128 v[60:63],v2 offset:18432
v_fma_f32 v46,-v116,v22,v46
v_mfma_f32_16x16x32_bf16 v[32:35],a[160:163],v[72:75],v[32:35]
v_fma_f32 v47,-v116,v23,v47
v_mul_f32 v88,v44,v44
v_mul_f32 v89,v45,v45
v_mfma_f32_16x16x32_bf16 v[36:39],a[164:167],v[72:75],v[36:39]
s_waitcnt lgkmcnt(3)
v_mul_f32 v90,v46,v46
v_mul_f32 v91,v47,v47
v_mfma_f32_16x16x32_bf16 v[28:31],a[168:171],v[76:79],v[28:31]
ds_read_b128 v[64:67],v2 offset:19456
v_fma_f32 v88,v118,v88,v117
v_mfma_f32_16x16x32_bf16 v[32:35],a[172:175],v[76:79],v[32:35]
v_mul_f32 v88,v44,v88
v_fma_f32 v89,v118,v89,v117
v_mfma_f32_16x16x32_bf16 v[36:39],a[176:179],v[76:79],v[36:39]
v_exp_f32 v88,v88
v_mfma_f32_16x16x32_bf16 v[28:31],a[180:183],v[80:83],v[28:31]
buffer_load_dwordx4 v1,s[36:39],s61 offen offset:0 sc1 lds
v_mfma_f32_16x16x32_bf16 v[32:35],a[184:187],v[80:83],v[32:35]
ds_write_b32 v10,v104 offset:0
v_mfma_f32_16x16x32_bf16 v[36:39],a[188:191],v[80:83],v[36:39]
s_waitcnt lgkmcnt(3)
ds_read_b128 v[68:71],v2 offset:20480
v_mul_f32 v89,v45,v89
v_mfma_f32_16x16x32_bf16 v[28:31],a[192:195],v[52:55],v[28:31]
ds_read_b128 v[72:75],v2 offset:21504
v_fma_f32 v88,v88,v119,v119
v_mfma_f32_16x16x32_bf16 v[32:35],a[196:199],v[52:55],v[32:35]
buffer_load_dwordx4 v1,s[36:39],s61 offen offset:1024 sc1 lds
v_mfma_f32_16x16x32_bf16 v[36:39],a[200:203],v[52:55],v[36:39]
v_exp_f32 v89,v89
v_mfma_f32_16x16x32_bf16 v[28:31],a[204:207],v[56:59],v[28:31]
ds_read_b128 v[76:79],v2 offset:22528
v_fma_f32 v89,v89,v119,v119
v_mfma_f32_16x16x32_bf16 v[32:35],a[208:211],v[56:59],v[32:35]
v_rcp_f32 v88,v88
v_mfma_f32_16x16x32_bf16 v[36:39],a[212:215],v[56:59],v[36:39]
s_waitcnt lgkmcnt(4)
buffer_load_dwordx4 v1,s[36:39],s61 offen offset:2048 sc1 lds
v_fma_f32 v90,v118,v90,v117
v_mfma_f32_16x16x32_bf16 v[28:31],a[216:219],v[60:63],v[28:31]
ds_read_b128 v[80:83],v2 offset:23552
v_mul_f32 v90,v46,v90
v_mfma_f32_16x16x32_bf16 v[32:35],a[220:223],v[60:63],v[32:35]
v_exp_f32 v90,v90
v_mfma_f32_16x16x32_bf16 v[36:39],a[224:227],v[60:63],v[36:39]
v_rcp_f32 v89,v89
v_mfma_f32_16x16x32_bf16 v[28:31],a[228:231],v[64:67],v[28:31]
buffer_load_dwordx4 v1,s[36:39],s61 offen offset:3072 sc1 lds
v_mfma_f32_16x16x32_bf16 v[32:35],a[232:235],v[64:67],v[32:35]
buffer_load_dword v108,v8,s[36:39],s72 offen offset:0 sc1
v_mfma_f32_16x16x32_bf16 v[36:39],a[236:239],v[64:67],v[36:39]
s_waitcnt lgkmcnt(2)
ds_read_b128 v[52:55],v2 offset:24576
v_fma_f32 v90,v90,v119,v119
v_mfma_f32_16x16x32_bf16 v[28:31],a[240:243],v[68:71],v[28:31]
ds_read_b128 v[56:59],v2 offset:25600
v_fma_f32 v91,v118,v91,v117
v_mfma_f32_16x16x32_bf16 v[32:35],a[244:247],v[68:71],v[32:35]
s_waitcnt vmcnt(9)
v_rcp_f32 v90,v90
v_mfma_f32_16x16x32_bf16 v[36:39],a[248:251],v[68:71],v[36:39]
s_barrier
v_mfma_f32_16x16x32_bf16 v[28:31],a[252:255],v[72:75],v[28:31]
ds_read_b128 v[60:63],v2 offset:26624
v_mul_f32 v91,v47,v91
v_mfma_f32_16x16x32_bf16 v[32:35],v[128:131],v[72:75],v[32:35]
v_exp_f32 v91,v91
v_mfma_f32_16x16x32_bf16 v[36:39],v[132:135],v[72:75],v[36:39]
s_waitcnt lgkmcnt(3)
v_fma_f32 v91,v91,v119,v119
v_mul_f32 v44,v44,v88
v_mfma_f32_16x16x32_bf16 v[28:31],v[136:139],v[76:79],v[28:31]
ds_read_b128 v[64:67],v2 offset:27648
v_rcp_f32 v91,v91
v_mfma_f32_16x16x32_bf16 v[32:35],v[140:143],v[76:79],v[32:35]
v_mul_f32 v45,v45,v89
v_mul_f32 v46,v46,v90
v_mfma_f32_16x16x32_bf16 v[36:39],v[144:147],v[76:79],v[36:39]
v_mul_f32 v47,v47,v91
v_cvt_pk_bf16_f32 v98,v44,v45
v_mfma_f32_16x16x32_bf16 v[28:31],v[148:151],v[80:83],v[28:31]
ds_read_b128 v[68:71],v2 offset:28672
v_cvt_pk_bf16_f32 v99,v46,v47
v_mfma_f32_16x16x32_bf16 v[32:35],v[152:155],v[80:83],v[32:35]
v_fma_f32 v48,-v116,v24,v48
v_fma_f32 v49,-v116,v25,v49
v_mfma_f32_16x16x32_bf16 v[36:39],v[156:159],v[80:83],v[36:39]
s_waitcnt lgkmcnt(3)
v_fma_f32 v50,-v116,v26,v50
v_fma_f32 v51,-v116,v27,v51
v_mfma_f32_16x16x32_bf16 v[28:31],v[160:163],v[52:55],v[28:31]
ds_read_b128 v[72:75],v2 offset:29696
v_mul_f32 v92,v48,v48
v_mfma_f32_16x16x32_bf16 v[32:35],v[164:167],v[52:55],v[32:35]
v_mul_f32 v93,v49,v49
v_mul_f32 v94,v50,v50
v_mfma_f32_16x16x32_bf16 v[36:39],v[168:171],v[52:55],v[36:39]
buffer_store_dwordx4 v[96:99],v3,s[36:39],s73 offen offset:0 sc0 sc1
v_mul_f32 v95,v51,v51
v_fma_f32 v92,v118,v92,v117
v_mul_f32 v92,v48,v92
v_mfma_f32_16x16x32_bf16 v[28:31],v[172:175],v[56:59],v[28:31]
ds_read_b128 v[76:79],v2 offset:30720
v_fma_f32 v93,v118,v93,v117
v_mfma_f32_16x16x32_bf16 v[32:35],v[176:179],v[56:59],v[32:35]
v_exp_f32 v92,v92
v_mfma_f32_16x16x32_bf16 v[36:39],v[180:183],v[56:59],v[36:39]
s_waitcnt lgkmcnt(3)
v_mul_f32 v93,v49,v93
v_fma_f32 v92,v92,v119,v119
v_mfma_f32_16x16x32_bf16 v[28:31],v[184:187],v[60:63],v[28:31]
ds_read_b128 v[80:83],v2 offset:31744
v_fma_f32 v94,v118,v94,v117
v_mfma_f32_16x16x32_bf16 v[32:35],v[188:191],v[60:63],v[32:35]
s_waitcnt vmcnt(6)
v_exp_f32 v93,v93
v_mul_f32 v94,v50,v94
v_mfma_f32_16x16x32_bf16 v[36:39],v[192:195],v[60:63],v[36:39]
s_barrier
v_mfma_f32_16x16x32_bf16 v[28:31],v[196:199],v[64:67],v[28:31]
ds_read_b128 v[52:55],v2 offset:32768
ds_read_b32 v13,v7 offset:0
v_mfma_f32_16x16x32_bf16 v[32:35],v[200:203],v[64:67],v[32:35]
ds_read_b64 v[112:113],v11 offset:0
v_fma_f32 v95,v118,v95,v117
v_mfma_f32_16x16x32_bf16 v[36:39],v[204:207],v[64:67],v[36:39]
s_waitcnt lgkmcnt(5)
v_exp_f32 v94,v94
v_mfma_f32_16x16x32_bf16 v[28:31],v[208:211],v[68:71],v[28:31]
ds_read_b128 v[56:59],v2 offset:33792
v_mul_f32 v95,v51,v95
v_mfma_f32_16x16x32_bf16 v[32:35],v[212:215],v[68:71],v[32:35]
s_waitcnt lgkmcnt(2)
v_exp_f32 v95,v95
v_mfma_f32_16x16x32_bf16 v[36:39],v[216:219],v[68:71],v[36:39]
s_waitcnt lgkmcnt(1)
v_cmp_eq_u32 vcc,s60,v13
v_rcp_f32 v92,v92
v_fma_f32 v93,v93,v119,v119
v_mfma_f32_16x16x32_bf16 v[28:31],v[220:223],v[72:75],v[28:31]
s_cmp_eq_u32 vcc_lo,-1
ds_read_b128 v[60:63],v2 offset:34816
v_fma_f32 v94,v94,v119,v119
v_mfma_f32_16x16x32_bf16 v[32:35],v[224:227],v[72:75],v[32:35]
s_cselect_b32 s67,s86,s59
v_rcp_f32 v93,v93
v_mfma_f32_16x16x32_bf16 v[36:39],v[228:231],v[72:75],v[36:39]
v_fma_f32 v95,v95,v119,v119
v_rcp_f32 v94,v94
v_rcp_f32 v95,v95
v_mfma_f32_16x16x32_bf16 v[28:31],v[232:235],v[76:79],v[28:31]
s_lshr_b32 s61,s67,0
v_mul_f32 v48,v48,v92
v_mul_f32 v49,v49,v93
v_mul_f32 v50,v50,v94
v_mul_f32 v51,v51,v95
v_cvt_pk_bf16_f32 v100,v48,v49)"
R"(
v_cvt_pk_bf16_f32 v101,v50,v51
v_mfma_f32_16x16x32_bf16 v[32:35],v[236:239],v[76:79],v[32:35]
buffer_store_dwordx2 v[100:101],v9,s[36:39],s73 offen offset:0 sc0 sc1
ds_read_b128 v[64:67],v2 offset:35840
v_mfma_f32_16x16x32_bf16 v[36:39],v[240:243],v[76:79],v[36:39]
s_xor_b32 s63,s63,65536
s_cmp_ge_u32 s66,s59
v_mfma_f32_16x16x32_bf16 v[28:31],v[244:247],v[80:83],v[28:31]
ds_read_b128 v[68:71],v2 offset:36864
v_mfma_f32_16x16x32_bf16 v[32:35],v[248:251],v[80:83],v[32:35]
s_waitcnt lgkmcnt(3)
v_mfma_f32_16x16x32_bf16 v[36:39],v[252:255],v[80:83],v[36:39]
s_cbranch_scc1 Lg3_slow1_%=
Lg3_d1w2_%=:
v_mfma_f32_16x16x32_bf16 v[40:43],a[0:3],v[52:55],0
s_mov_b32 m0,s62
ds_read_b128 v[72:75],v2 offset:37888
v_mul_f32 v116,s75,v112
v_mfma_f32_16x16x32_bf16 v[44:47],a[4:7],v[52:55],0
s_mul_i32 s73,s48,7
v_fma_f32 v113,v113,s75,v12
v_fma_f32 v113,-v116,v116,v113
v_mfma_f32_16x16x32_bf16 v[48:51],a[8:11],v[52:55],0
s_lshr_b32 s73,s73,1
v_max_f32 v113,v113,v12
v_rsq_f32 v112,v113
v_mfma_f32_16x16x32_bf16 v[40:43],a[12:15],v[56:59],v[40:43]
buffer_load_dwordx4 v0,s[36:39],s61 offen offset:0 sc1 lds
v_mfma_f32_16x16x32_bf16 v[44:47],a[16:19],v[56:59],v[44:47]
s_add_u32 s74,s53,s55
ds_read_b128 v[76:79],v2 offset:38912
v_mul_f32 v117,s76,v112
v_mfma_f32_16x16x32_bf16 v[48:51],a[20:23],v[56:59],v[48:51]
s_waitcnt lgkmcnt(3)
v_mul_f32 v118,v112,v112
v_mul_f32 v119,v113,v112
v_mfma_f32_16x16x32_bf16 v[40:43],a[24:27],v[60:63],v[40:43]
s_cmp_eq_u32 s74,s56
ds_read_b128 v[80:83],v2 offset:39936
v_mul_f32 v118,s77,v118
v_mfma_f32_16x16x32_bf16 v[44:47],a[28:31],v[60:63],v[44:47]
buffer_load_dwordx4 v0,s[36:39],s61 offen offset:1024 sc1 lds
v_mfma_f32_16x16x32_bf16 v[48:51],a[32:35],v[60:63],v[48:51]
s_cselect_b32 s74,s57,s74
v_mul_f32 v118,v118,v117
v_fma_f32 v28,-v116,v16,v28
v_mfma_f32_16x16x32_bf16 v[40:43],a[36:39],v[64:67],v[40:43]
s_cmp_eq_u32 s74,s58
ds_read_b128 v[52:55],v2 offset:40960
v_fma_f32 v29,-v116,v17,v29
v_mfma_f32_16x16x32_bf16 v[44:47],a[40:43],v[64:67],v[44:47]
s_cselect_b32 s74,s59,s74
v_fma_f32 v30,-v116,v18,v30
v_fma_f32 v31,-v116,v19,v31
v_mfma_f32_16x16x32_bf16 v[48:51],a[44:47],v[64:67],v[48:51]
s_waitcnt lgkmcnt(3)
buffer_load_dwordx4 v0,s[36:39],s61 offen offset:2048 sc1 lds
v_mfma_f32_16x16x32_bf16 v[40:43],a[48:51],v[68:71],v[40:43]
s_cmp_ge_u32 s67,s59
ds_read_b128 v[56:59],v2 offset:41984
v_mul_f32 v84,v28,v28
v_mfma_f32_16x16x32_bf16 v[44:47],a[52:55],v[68:71],v[44:47]
s_cselect_b32 s86,s50,s51
v_mul_f32 v85,v29,v29
v_mul_f32 v86,v30,v30
v_mul_f32 v87,v31,v31
v_mfma_f32_16x16x32_bf16 v[48:51],a[56:59],v[68:71],v[48:51]
s_mov_b64 s[44:45],s[46:47]
v_fma_f32 v84,v118,v84,v117
v_mul_f32 v84,v28,v84
v_mfma_f32_16x16x32_bf16 v[40:43],a[60:63],v[72:75],v[40:43]
buffer_load_dwordx4 v0,s[36:39],s61 offen offset:3072 sc1 lds
v_mfma_f32_16x16x32_bf16 v[44:47],a[64:67],v[72:75],v[44:47]
s_add_u32 m0,s62,16384
ds_read_b128 v[60:63],v2 offset:43008
v_fma_f32 v85,v118,v85,v117
v_mfma_f32_16x16x32_bf16 v[48:51],a[68:71],v[72:75],v[48:51]
s_waitcnt lgkmcnt(3)
v_exp_f32 v84,v84
v_mfma_f32_16x16x32_bf16 v[40:43],a[72:75],v[76:79],v[40:43]
s_lshr_b32 s72,s61,5
ds_read_b128 v[64:67],v2 offset:44032
v_mul_f32 v85,v29,v85
v_mfma_f32_16x16x32_bf16 v[44:47],a[76:79],v[76:79],v[44:47]
s_mov_b32 s62,s64
v_exp_f32 v85,v85
v_mfma_f32_16x16x32_bf16 v[48:51],a[80:83],v[76:79],v[48:51]
s_mov_b64 s[46:47],s[48:49]
v_fma_f32 v84,v84,v119,v119
v_fma_f32 v85,v85,v119,v119
v_mfma_f32_16x16x32_bf16 v[40:43],a[84:87],v[80:83],v[40:43]
s_mov_b64 s[48:49],s[50:51]
ds_read_b128 v[68:71],v2 offset:45056
v_fma_f32 v86,v118,v86,v117
v_mfma_f32_16x16x32_bf16 v[44:47],a[88:91],v[80:83],v[44:47]
s_mov_b64 s[50:51],s[52:53]
v_rcp_f32 v84,v84
v_mfma_f32_16x16x32_bf16 v[48:51],a[92:95],v[80:83],v[48:51]
s_waitcnt lgkmcnt(3)
v_rcp_f32 v85,v85
v_mfma_f32_16x16x32_bf16 v[40:43],a[96:99],v[52:55],v[40:43]
s_mov_b32 s52,s74
ds_read_b128 v[72:75],v2 offset:46080
v_mul_f32 v86,v30,v86
v_fma_f32 v87,v118,v87,v117
v_mfma_f32_16x16x32_bf16 v[44:47],a[100:103],v[52:55],v[44:47]
v_exp_f32 v86,v86
v_mfma_f32_16x16x32_bf16 v[48:51],a[104:107],v[52:55],v[48:51]
v_mul_f32 v87,v31,v87
v_fma_f32 v86,v86,v119,v119
v_mfma_f32_16x16x32_bf16 v[40:43],a[108:111],v[56:59],v[40:43]
ds_read_b128 v[76:79],v2 offset:47104
v_mul_f32 v28,v28,v84
v_mfma_f32_16x16x32_bf16 v[44:47],a[112:115],v[56:59],v[44:47]
v_exp_f32 v87,v87
v_mfma_f32_16x16x32_bf16 v[48:51],a[116:119],v[56:59],v[48:51]
s_waitcnt lgkmcnt(3)
v_rcp_f32 v86,v86
v_mfma_f32_16x16x32_bf16 v[40:43],a[120:123],v[60:63],v[40:43]
ds_read_b128 v[80:83],v2 offset:48128
v_fma_f32 v87,v87,v119,v119
v_mfma_f32_16x16x32_bf16 v[44:47],a[124:127],v[60:63],v[44:47]
s_waitcnt vmcnt(6)
v_rcp_f32 v87,v87
v_mfma_f32_16x16x32_bf16 v[48:51],a[128:131],v[60:63],v[48:51]
s_barrier
v_mfma_f32_16x16x32_bf16 v[40:43],a[132:135],v[64:67],v[40:43]
ds_read_b128 v[52:55],v2 offset:49152
v_add_f32_dpp v108,v108,v108 quad_perm:[1,0,3,2] row_mask:0xf bank_mask:0xf
v_mfma_f32_16x16x32_bf16 v[44:47],a[136:139],v[64:67],v[44:47]
s_nop 0
v_add_f32_dpp v108,v108,v108 quad_perm:[2,3,0,1] row_mask:0xf bank_mask:0xf
v_mul_f32 v29,v29,v85
v_mfma_f32_16x16x32_bf16 v[48:51],a[140:143],v[64:67],v[48:51]
s_waitcnt lgkmcnt(3)
v_add_f32_dpp v108,v108,v108 row_half_mirror row_mask:0xf bank_mask:0xf
v_mul_f32 v30,v30,v86
v_mul_f32 v31,v31,v87
v_mfma_f32_16x16x32_bf16 v[40:43],a[144:147],v[68:71],v[40:43]
ds_read_b128 v[56:59],v2 offset:50176
v_cvt_pk_bf16_f32 v96,v28,v29
v_mfma_f32_16x16x32_bf16 v[44:47],a[148:151],v[68:71],v[44:47]
v_cvt_pk_bf16_f32 v97,v30,v31
v_fma_f32 v32,-v116,v20,v32
v_mfma_f32_16x16x32_bf16 v[48:51],a[152:155],v[68:71],v[48:51]
v_fma_f32 v33,-v116,v21,v33
v_fma_f32 v34,-v116,v22,v34
v_mfma_f32_16x16x32_bf16 v[40:43],a[156:159],v[72:75],v[40:43]
ds_read_b128 v[60:63],v2 offset:51200
v_fma_f32 v35,-v116,v23,v35
v_mfma_f32_16x16x32_bf16 v[44:47],a[160:163],v[72:75],v[44:47]
v_mul_f32 v88,v32,v32
v_mul_f32 v89,v33,v33
v_mfma_f32_16x16x32_bf16 v[48:51],a[164:167],v[72:75],v[48:51]
s_waitcnt lgkmcnt(3)
v_mul_f32 v90,v34,v34
v_mul_f32 v91,v35,v35
v_mfma_f32_16x16x32_bf16 v[40:43],a[168:171],v[76:79],v[40:43]
ds_read_b128 v[64:67],v2 offset:52224
v_fma_f32 v88,v118,v88,v117
v_mfma_f32_16x16x32_bf16 v[44:47],a[172:175],v[76:79],v[44:47]
v_mul_f32 v88,v32,v88
v_fma_f32 v89,v118,v89,v117
v_mfma_f32_16x16x32_bf16 v[48:51],a[176:179],v[76:79],v[48:51]
v_exp_f32 v88,v88
v_mfma_f32_16x16x32_bf16 v[40:43],a[180:183],v[80:83],v[40:43]
buffer_load_dwordx4 v1,s[36:39],s61 offen offset:0 sc1 lds
v_mfma_f32_16x16x32_bf16 v[44:47],a[184:187],v[80:83],v[44:47]
ds_write_b32 v10,v108 offset:128
v_mfma_f32_16x16x32_bf16 v[48:51],a[188:191],v[80:83],v[48:51]
s_waitcnt lgkmcnt(3)
ds_read_b128 v[68:71],v2 offset:53248
v_mul_f32 v89,v33,v89
v_fma_f32 v88,v88,v119,v119
v_mfma_f32_16x16x32_bf16 v[40:43],a[192:195],v[52:55],v[40:43]
ds_read_b128 v[72:75],v2 offset:54272
v_fma_f32 v90,v118,v90,v117
v_mfma_f32_16x16x32_bf16 v[44:47],a[196:199],v[52:55],v[44:47]
buffer_load_dwordx4 v1,s[36:39],s61 offen offset:1024 sc1 lds
v_mfma_f32_16x16x32_bf16 v[48:51],a[200:203],v[52:55],v[48:51]
v_exp_f32 v89,v89
v_mfma_f32_16x16x32_bf16 v[40:43],a[204:207],v[56:59],v[40:43]
ds_read_b128 v[76:79],v2 offset:55296
v_mul_f32 v90,v34,v90
v_mfma_f32_16x16x32_bf16 v[44:47],a[208:211],v[56:59],v[44:47]
v_exp_f32 v90,v90
v_mfma_f32_16x16x32_bf16 v[48:51],a[212:215],v[56:59],v[48:51]
s_waitcnt lgkmcnt(4)
buffer_load_dwordx4 v1,s[36:39],s61 offen offset:2048 sc1 lds
v_mfma_f32_16x16x32_bf16 v[40:43],a[216:219],v[60:63],v[40:43]
ds_read_b128 v[80:83],v2 offset:56320
v_fma_f32 v89,v89,v119,v119
v_mfma_f32_16x16x32_bf16 v[44:47],a[220:223],v[60:63],v[44:47]
v_rcp_f32 v88,v88
v_mfma_f32_16x16x32_bf16 v[48:51],a[224:227],v[60:63],v[48:51]
v_rcp_f32 v89,v89
v_mfma_f32_16x16x32_bf16 v[40:43],a[228:231],v[64:67],v[40:43]
buffer_load_dwordx4 v1,s[36:39],s61 offen offset:3072 sc1 lds
v_mfma_f32_16x16x32_bf16 v[44:47],a[232:235],v[64:67],v[44:47]
buffer_load_dword v104,v8,s[36:39],s72 offen offset:0 sc1)"
R"(
ds_read_b128 v[52:55],v2 offset:57344
v_mfma_f32_16x16x32_bf16 v[48:51],a[236:239],v[64:67],v[48:51]
s_waitcnt lgkmcnt(3)
v_fma_f32 v90,v90,v119,v119
v_fma_f32 v91,v118,v91,v117
v_mfma_f32_16x16x32_bf16 v[40:43],a[240:243],v[68:71],v[40:43]
ds_read_b128 v[56:59],v2 offset:58368
v_mul_f32 v91,v35,v91
v_mfma_f32_16x16x32_bf16 v[44:47],a[244:247],v[68:71],v[44:47]
s_waitcnt vmcnt(9)
v_exp_f32 v91,v91
v_mfma_f32_16x16x32_bf16 v[48:51],a[248:251],v[68:71],v[48:51]
s_barrier
v_mfma_f32_16x16x32_bf16 v[40:43],a[252:255],v[72:75],v[40:43]
ds_read_b128 v[60:63],v2 offset:59392
v_fma_f32 v91,v91,v119,v119
v_mfma_f32_16x16x32_bf16 v[44:47],v[128:131],v[72:75],v[44:47]
v_rcp_f32 v90,v90
v_mfma_f32_16x16x32_bf16 v[48:51],v[132:135],v[72:75],v[48:51]
s_waitcnt lgkmcnt(3)
v_rcp_f32 v91,v91
v_mfma_f32_16x16x32_bf16 v[40:43],v[136:139],v[76:79],v[40:43]
ds_read_b128 v[64:67],v2 offset:60416
v_mul_f32 v32,v32,v88
v_mfma_f32_16x16x32_bf16 v[44:47],v[140:143],v[76:79],v[44:47]
v_mul_f32 v33,v33,v89
v_mul_f32 v34,v34,v90
v_mfma_f32_16x16x32_bf16 v[48:51],v[144:147],v[76:79],v[48:51]
v_mul_f32 v35,v35,v91
v_cvt_pk_bf16_f32 v98,v32,v33
v_mfma_f32_16x16x32_bf16 v[40:43],v[148:151],v[80:83],v[40:43]
ds_read_b128 v[68:71],v2 offset:61440
v_cvt_pk_bf16_f32 v99,v34,v35
v_fma_f32 v36,-v116,v24,v36
v_mfma_f32_16x16x32_bf16 v[44:47],v[152:155],v[80:83],v[44:47]
v_fma_f32 v37,-v116,v25,v37
v_fma_f32 v38,-v116,v26,v38
v_mfma_f32_16x16x32_bf16 v[48:51],v[156:159],v[80:83],v[48:51]
s_waitcnt lgkmcnt(3)
v_fma_f32 v39,-v116,v27,v39
v_mul_f32 v92,v36,v36
v_mfma_f32_16x16x32_bf16 v[40:43],v[160:163],v[52:55],v[40:43]
ds_read_b128 v[72:75],v2 offset:62464
v_mul_f32 v93,v37,v37
v_mfma_f32_16x16x32_bf16 v[44:47],v[164:167],v[52:55],v[44:47]
v_mul_f32 v94,v38,v38
v_mul_f32 v95,v39,v39
v_mfma_f32_16x16x32_bf16 v[48:51],v[168:171],v[52:55],v[48:51]
buffer_store_dwordx4 v[96:99],v3,s[36:39],s73 offen offset:0 sc0 sc1
v_fma_f32 v92,v118,v92,v117
v_mul_f32 v92,v36,v92
v_fma_f32 v93,v118,v93,v117
v_mfma_f32_16x16x32_bf16 v[40:43],v[172:175],v[56:59],v[40:43]
ds_read_b128 v[76:79],v2 offset:63488
v_mul_f32 v93,v37,v93
v_mfma_f32_16x16x32_bf16 v[44:47],v[176:179],v[56:59],v[44:47]
v_exp_f32 v92,v92
v_mfma_f32_16x16x32_bf16 v[48:51],v[180:183],v[56:59],v[48:51]
s_waitcnt lgkmcnt(3)
v_exp_f32 v93,v93
v_mfma_f32_16x16x32_bf16 v[40:43],v[184:187],v[60:63],v[40:43]
ds_read_b128 v[80:83],v2 offset:64512
v_fma_f32 v92,v92,v119,v119
v_mfma_f32_16x16x32_bf16 v[44:47],v[188:191],v[60:63],v[44:47]
s_waitcnt vmcnt(6)
v_xor_b32 v2,65536,v2
v_fma_f32 v93,v93,v119,v119
v_mfma_f32_16x16x32_bf16 v[48:51],v[192:195],v[60:63],v[48:51]
s_barrier
v_mfma_f32_16x16x32_bf16 v[40:43],v[196:199],v[64:67],v[40:43]
ds_read_b128 v[52:55],v2 offset:0
ds_read_b32 v13,v7 offset:256
v_mfma_f32_16x16x32_bf16 v[44:47],v[200:203],v[64:67],v[44:47]
ds_read_b64 v[112:113],v11 offset:128
v_fma_f32 v94,v118,v94,v117
v_mfma_f32_16x16x32_bf16 v[48:51],v[204:207],v[64:67],v[48:51]
s_waitcnt lgkmcnt(5)
v_rcp_f32 v92,v92
v_mfma_f32_16x16x32_bf16 v[40:43],v[208:211],v[68:71],v[40:43]
ds_read_b128 v[56:59],v2 offset:1024
v_mul_f32 v94,v38,v94
v_mfma_f32_16x16x32_bf16 v[44:47],v[212:215],v[68:71],v[44:47]
s_waitcnt lgkmcnt(2)
v_exp_f32 v94,v94
v_mfma_f32_16x16x32_bf16 v[48:51],v[216:219],v[68:71],v[48:51]
s_waitcnt lgkmcnt(1)
v_cmp_eq_u32 vcc,s60,v13
v_rcp_f32 v93,v93
v_fma_f32 v94,v94,v119,v119
v_mfma_f32_16x16x32_bf16 v[40:43],v[220:223],v[72:75],v[40:43]
s_cmp_eq_u32 vcc_lo,-1
ds_read_b128 v[60:63],v2 offset:2048
v_fma_f32 v95,v118,v95,v117
v_mfma_f32_16x16x32_bf16 v[44:47],v[224:227],v[72:75],v[44:47]
s_cselect_b32 s66,s86,s59
v_mul_f32 v95,v39,v95
v_exp_f32 v95,v95
v_mfma_f32_16x16x32_bf16 v[48:51],v[228:231],v[72:75],v[48:51]
v_fma_f32 v95,v95,v119,v119
v_rcp_f32 v94,v94
v_rcp_f32 v95,v95
v_mfma_f32_16x16x32_bf16 v[40:43],v[232:235],v[76:79],v[40:43]
s_lshr_b32 s61,s66,0
v_mul_f32 v36,v36,v92
v_mul_f32 v37,v37,v93
v_mul_f32 v38,v38,v94
v_mul_f32 v39,v39,v95
v_cvt_pk_bf16_f32 v100,v36,v37
v_cvt_pk_bf16_f32 v101,v38,v39
v_mfma_f32_16x16x32_bf16 v[44:47],v[236:239],v[76:79],v[44:47]
buffer_store_dwordx2 v[100:101],v9,s[36:39],s73 offen offset:0 sc0 sc1
ds_read_b128 v[64:67],v2 offset:3072
v_mfma_f32_16x16x32_bf16 v[48:51],v[240:243],v[76:79],v[48:51]
s_xor_b32 s64,s64,65536
s_cmp_lt_u32 s67,s59
v_mfma_f32_16x16x32_bf16 v[40:43],v[244:247],v[80:83],v[40:43]
ds_read_b128 v[68:71],v2 offset:4096
v_mfma_f32_16x16x32_bf16 v[44:47],v[248:251],v[80:83],v[44:47]
s_waitcnt lgkmcnt(3)
v_mfma_f32_16x16x32_bf16 v[48:51],v[252:255],v[80:83],v[48:51]
s_cbranch_scc1 Lg3_d0w2_%=
s_branch Lg3_slow0_%=
Lg3_d0w3_%=:
v_mfma_f32_16x16x32_bf16 v[28:31],a[0:3],v[52:55],0
s_mov_b32 m0,s62
ds_read_b128 v[72:75],v2 offset:5120
v_mul_f32 v116,s75,v112
v_mfma_f32_16x16x32_bf16 v[32:35],a[4:7],v[52:55],0
s_mul_i32 s73,s47,7
v_fma_f32 v113,v113,s75,v12
v_fma_f32 v113,-v116,v116,v113
v_mfma_f32_16x16x32_bf16 v[36:39],a[8:11],v[52:55],0
s_lshr_b32 s73,s73,1
v_max_f32 v113,v113,v12
v_rsq_f32 v112,v113
v_mfma_f32_16x16x32_bf16 v[28:31],a[12:15],v[56:59],v[28:31]
s_add_u32 s53,s52,s55
ds_read_b128 v[76:79],v2 offset:6144
v_mul_f32 v117,s76,v112
v_mfma_f32_16x16x32_bf16 v[32:35],a[16:19],v[56:59],v[32:35]
buffer_load_dwordx4 v0,s[36:39],s61 offen offset:0 sc1 lds
v_mfma_f32_16x16x32_bf16 v[36:39],a[20:23],v[56:59],v[36:39]
s_waitcnt lgkmcnt(3)
v_mul_f32 v118,v112,v112
v_mul_f32 v119,v113,v112
v_mfma_f32_16x16x32_bf16 v[28:31],a[24:27],v[60:63],v[28:31]
s_cmp_eq_u32 s53,s56
ds_read_b128 v[80:83],v2 offset:7168
v_mul_f32 v118,s77,v118
v_mfma_f32_16x16x32_bf16 v[32:35],a[28:31],v[60:63],v[32:35]
s_cselect_b32 s53,s57,s53
v_mul_f32 v118,v118,v117
v_fma_f32 v40,-v116,v16,v40
v_mfma_f32_16x16x32_bf16 v[36:39],a[32:35],v[60:63],v[36:39]
buffer_load_dwordx4 v0,s[36:39],s61 offen offset:1024 sc1 lds
v_mfma_f32_16x16x32_bf16 v[28:31],a[36:39],v[64:67],v[28:31]
s_cmp_eq_u32 s53,s58
ds_read_b128 v[52:55],v2 offset:8192
v_fma_f32 v41,-v116,v17,v41
v_mfma_f32_16x16x32_bf16 v[32:35],a[40:43],v[64:67],v[32:35]
s_cselect_b32 s53,s59,s53
v_fma_f32 v42,-v116,v18,v42
v_fma_f32 v43,-v116,v19,v43
v_mfma_f32_16x16x32_bf16 v[36:39],a[44:47],v[64:67],v[36:39]
s_waitcnt lgkmcnt(3)
v_mul_f32 v84,v40,v40
v_mul_f32 v85,v41,v41
v_mfma_f32_16x16x32_bf16 v[28:31],a[48:51],v[68:71],v[28:31]
buffer_load_dwordx4 v0,s[36:39],s61 offen offset:2048 sc1 lds
v_mfma_f32_16x16x32_bf16 v[32:35],a[52:55],v[68:71],v[32:35]
s_cmp_ge_u32 s66,s59
ds_read_b128 v[56:59],v2 offset:9216
v_mul_f32 v86,v42,v42
v_mfma_f32_16x16x32_bf16 v[36:39],a[56:59],v[68:71],v[36:39]
s_cselect_b32 s86,s49,s50
v_mul_f32 v87,v43,v43
v_fma_f32 v84,v118,v84,v117
v_mfma_f32_16x16x32_bf16 v[28:31],a[60:63],v[72:75],v[28:31]
s_mul_i32 s70,s46,5
ds_read_b128 v[60:63],v2 offset:10240
v_mul_f32 v84,v40,v84
v_mfma_f32_16x16x32_bf16 v[32:35],a[64:67],v[72:75],v[32:35]
buffer_load_dwordx4 v0,s[36:39],s61 offen offset:3072 sc1 lds
v_mfma_f32_16x16x32_bf16 v[36:39],a[68:71],v[72:75],v[36:39]
s_waitcnt lgkmcnt(3)
v_exp_f32 v84,v84
v_mfma_f32_16x16x32_bf16 v[28:31],a[72:75],v[76:79],v[28:31]
s_add_u32 m0,s62,16384
ds_read_b128 v[64:67],v2 offset:11264
v_fma_f32 v84,v84,v119,v119
v_mfma_f32_16x16x32_bf16 v[32:35],a[76:79],v[76:79],v[32:35]
s_lshr_b32 s70,s70,6
v_rcp_f32 v84,v84
v_mfma_f32_16x16x32_bf16 v[36:39],a[80:83],v[76:79],v[36:39]
s_lshr_b32 s72,s61,5
v_fma_f32 v85,v118,v85,v117
v_mul_f32 v85,v41,v85
v_mfma_f32_16x16x32_bf16 v[28:31],a[84:87],v[80:83],v[28:31]
s_mov_b32 s62,s63
ds_read_b128 v[68:71],v2 offset:12288)"
R"(
v_fma_f32 v86,v118,v86,v117
v_mfma_f32_16x16x32_bf16 v[32:35],a[88:91],v[80:83],v[32:35]
v_exp_f32 v85,v85
v_mfma_f32_16x16x32_bf16 v[36:39],a[92:95],v[80:83],v[36:39]
s_waitcnt lgkmcnt(3)
v_mul_f32 v86,v42,v86
v_fma_f32 v85,v85,v119,v119
v_mfma_f32_16x16x32_bf16 v[28:31],a[96:99],v[52:55],v[28:31]
ds_read_b128 v[72:75],v2 offset:13312
v_fma_f32 v87,v118,v87,v117
v_mfma_f32_16x16x32_bf16 v[32:35],a[100:103],v[52:55],v[32:35]
v_exp_f32 v86,v86
v_mfma_f32_16x16x32_bf16 v[36:39],a[104:107],v[52:55],v[36:39]
v_rcp_f32 v85,v85
v_mfma_f32_16x16x32_bf16 v[28:31],a[108:111],v[56:59],v[28:31]
ds_read_b128 v[76:79],v2 offset:14336
v_mul_f32 v87,v43,v87
v_mfma_f32_16x16x32_bf16 v[32:35],a[112:115],v[56:59],v[32:35]
v_exp_f32 v87,v87
v_mfma_f32_16x16x32_bf16 v[36:39],a[116:119],v[56:59],v[36:39]
s_waitcnt lgkmcnt(3)
v_fma_f32 v86,v86,v119,v119
v_fma_f32 v87,v87,v119,v119
v_mfma_f32_16x16x32_bf16 v[28:31],a[120:123],v[60:63],v[28:31]
ds_read_b128 v[80:83],v2 offset:15360
v_mul_f32 v40,v40,v84
v_mfma_f32_16x16x32_bf16 v[32:35],a[124:127],v[60:63],v[32:35]
s_waitcnt vmcnt(7)
v_rcp_f32 v86,v86
v_mfma_f32_16x16x32_bf16 v[36:39],a[128:131],v[60:63],v[36:39]
s_barrier
v_mfma_f32_16x16x32_bf16 v[28:31],a[132:135],v[64:67],v[28:31]
ds_read_b128 v[52:55],v2 offset:16384
v_add_f32_dpp v104,v104,v104 quad_perm:[1,0,3,2] row_mask:0xf bank_mask:0xf
v_mfma_f32_16x16x32_bf16 v[32:35],a[136:139],v[64:67],v[32:35]
v_rcp_f32 v87,v87
v_mfma_f32_16x16x32_bf16 v[36:39],a[140:143],v[64:67],v[36:39]
s_waitcnt lgkmcnt(3)
v_add_f32_dpp v104,v104,v104 quad_perm:[2,3,0,1] row_mask:0xf bank_mask:0xf
v_mul_f32 v41,v41,v85
v_mfma_f32_16x16x32_bf16 v[28:31],a[144:147],v[68:71],v[28:31]
ds_read_b128 v[56:59],v2 offset:17408
v_add_f32_dpp v104,v104,v104 row_half_mirror row_mask:0xf bank_mask:0xf
v_mfma_f32_16x16x32_bf16 v[32:35],a[148:151],v[68:71],v[32:35]
v_mul_f32 v42,v42,v86
v_mul_f32 v43,v43,v87
v_mfma_f32_16x16x32_bf16 v[36:39],a[152:155],v[68:71],v[36:39]
v_cvt_pk_bf16_f32 v96,v40,v41
v_cvt_pk_bf16_f32 v97,v42,v43
v_mfma_f32_16x16x32_bf16 v[28:31],a[156:159],v[72:75],v[28:31]
ds_read_b128 v[60:63],v2 offset:18432
v_fma_f32 v44,-v116,v20,v44
v_mfma_f32_16x16x32_bf16 v[32:35],a[160:163],v[72:75],v[32:35]
v_fma_f32 v45,-v116,v21,v45
v_fma_f32 v46,-v116,v22,v46
v_mfma_f32_16x16x32_bf16 v[36:39],a[164:167],v[72:75],v[36:39]
s_waitcnt lgkmcnt(3)
v_fma_f32 v47,-v116,v23,v47
v_mul_f32 v88,v44,v44
v_mfma_f32_16x16x32_bf16 v[28:31],a[168:171],v[76:79],v[28:31]
ds_read_b128 v[64:67],v2 offset:19456
v_mul_f32 v89,v45,v45
v_mfma_f32_16x16x32_bf16 v[32:35],a[172:175],v[76:79],v[32:35]
v_mul_f32 v90,v46,v46
v_mul_f32 v91,v47,v47
v_mfma_f32_16x16x32_bf16 v[36:39],a[176:179],v[76:79],v[36:39]
v_fma_f32 v88,v118,v88,v117
v_mul_f32 v88,v44,v88
v_mfma_f32_16x16x32_bf16 v[28:31],a[180:183],v[80:83],v[28:31]
ds_read_b128 v[68:71],v2 offset:20480
v_fma_f32 v89,v118,v89,v117
v_mfma_f32_16x16x32_bf16 v[32:35],a[184:187],v[80:83],v[32:35]
buffer_load_dwordx4 v1,s[36:39],s61 offen offset:0 sc1 lds
v_mfma_f32_16x16x32_bf16 v[36:39],a[188:191],v[80:83],v[36:39]
s_waitcnt lgkmcnt(3)
ds_write_b32 v10,v104 offset:0
v_mfma_f32_16x16x32_bf16 v[28:31],a[192:195],v[52:55],v[28:31]
ds_read_b128 v[72:75],v2 offset:21504
v_mul_f32 v89,v45,v89
v_mfma_f32_16x16x32_bf16 v[32:35],a[196:199],v[52:55],v[32:35]
v_exp_f32 v88,v88
v_mfma_f32_16x16x32_bf16 v[36:39],a[200:203],v[52:55],v[36:39]
buffer_load_dwordx4 v1,s[36:39],s61 offen offset:1024 sc1 lds
v_mfma_f32_16x16x32_bf16 v[28:31],a[204:207],v[56:59],v[28:31]
ds_read_b128 v[76:79],v2 offset:22528
v_fma_f32 v88,v88,v119,v119
v_mfma_f32_16x16x32_bf16 v[32:35],a[208:211],v[56:59],v[32:35]
v_exp_f32 v89,v89
v_mfma_f32_16x16x32_bf16 v[36:39],a[212:215],v[56:59],v[36:39]
s_waitcnt lgkmcnt(4)
v_rcp_f32 v88,v88
v_mfma_f32_16x16x32_bf16 v[28:31],a[216:219],v[60:63],v[28:31]
buffer_load_dwordx4 v1,s[36:39],s61 offen offset:2048 sc1 lds
v_mfma_f32_16x16x32_bf16 v[32:35],a[220:223],v[60:63],v[32:35]
ds_read_b128 v[80:83],v2 offset:23552
v_fma_f32 v89,v89,v119,v119
v_mfma_f32_16x16x32_bf16 v[36:39],a[224:227],v[60:63],v[36:39]
v_rcp_f32 v89,v89
v_mfma_f32_16x16x32_bf16 v[28:31],a[228:231],v[64:67],v[28:31]
ds_read_b128 v[52:55],v2 offset:24576
v_fma_f32 v90,v118,v90,v117
v_mfma_f32_16x16x32_bf16 v[32:35],a[232:235],v[64:67],v[32:35]
buffer_load_dwordx4 v1,s[36:39],s61 offen offset:3072 sc1 lds
v_mfma_f32_16x16x32_bf16 v[36:39],a[236:239],v[64:67],v[36:39]
s_waitcnt lgkmcnt(3)
buffer_load_dword v108,v8,s[36:39],s72 offen offset:0 sc1
v_mfma_f32_16x16x32_bf16 v[28:31],a[240:243],v[68:71],v[28:31]
ds_read_b128 v[56:59],v2 offset:25600
v_mul_f32 v90,v46,v90
v_mfma_f32_16x16x32_bf16 v[32:35],a[244:247],v[68:71],v[32:35]
s_waitcnt vmcnt(9)
v_exp_f32 v90,v90
v_mfma_f32_16x16x32_bf16 v[36:39],a[248:251],v[68:71],v[36:39]
s_barrier
v_mfma_f32_16x16x32_bf16 v[28:31],a[252:255],v[72:75],v[28:31]
buffer_store_dword v5,v4,s[36:39],s70 offen offset:0 sc0 sc1
ds_read_b128 v[60:63],v2 offset:26624
v_fma_f32 v90,v90,v119,v119
v_fma_f32 v91,v118,v91,v117
v_mfma_f32_16x16x32_bf16 v[32:35],v[128:131],v[72:75],v[32:35]
v_rcp_f32 v90,v90
v_mfma_f32_16x16x32_bf16 v[36:39],v[132:135],v[72:75],v[36:39]
s_waitcnt lgkmcnt(3)
v_mul_f32 v91,v47,v91
v_mul_f32 v44,v44,v88
v_mfma_f32_16x16x32_bf16 v[28:31],v[136:139],v[76:79],v[28:31]
ds_read_b128 v[64:67],v2 offset:27648
v_mul_f32 v45,v45,v89
v_mfma_f32_16x16x32_bf16 v[32:35],v[140:143],v[76:79],v[32:35]
v_exp_f32 v91,v91
v_mfma_f32_16x16x32_bf16 v[36:39],v[144:147],v[76:79],v[36:39]
v_fma_f32 v91,v91,v119,v119
v_mul_f32 v46,v46,v90
v_mfma_f32_16x16x32_bf16 v[28:31],v[148:151],v[80:83],v[28:31]
ds_read_b128 v[68:71],v2 offset:28672
v_cvt_pk_bf16_f32 v98,v44,v45
v_mfma_f32_16x16x32_bf16 v[32:35],v[152:155],v[80:83],v[32:35]
v_rcp_f32 v91,v91
v_mfma_f32_16x16x32_bf16 v[36:39],v[156:159],v[80:83],v[36:39]
s_waitcnt lgkmcnt(3)
v_mul_f32 v47,v47,v91
v_cvt_pk_bf16_f32 v99,v46,v47
v_mfma_f32_16x16x32_bf16 v[28:31],v[160:163],v[52:55],v[28:31]
ds_read_b128 v[72:75],v2 offset:29696
v_fma_f32 v48,-v116,v24,v48
v_mfma_f32_16x16x32_bf16 v[32:35],v[164:167],v[52:55],v[32:35]
v_fma_f32 v49,-v116,v25,v49
v_fma_f32 v50,-v116,v26,v50
v_mfma_f32_16x16x32_bf16 v[36:39],v[168:171],v[52:55],v[36:39]
v_fma_f32 v51,-v116,v27,v51
v_mul_f32 v92,v48,v48
v_mfma_f32_16x16x32_bf16 v[28:31],v[172:175],v[56:59],v[28:31]
ds_read_b128 v[76:79],v2 offset:30720
v_mul_f32 v93,v49,v49
v_mfma_f32_16x16x32_bf16 v[32:35],v[176:179],v[56:59],v[32:35]
buffer_store_dwordx4 v[96:99],v3,s[36:39],s73 offen offset:0 sc0 sc1
v_mul_f32 v94,v50,v50
v_mul_f32 v95,v51,v51
v_fma_f32 v92,v118,v92,v117
v_mfma_f32_16x16x32_bf16 v[36:39],v[180:183],v[56:59],v[36:39]
s_waitcnt lgkmcnt(3)
v_mul_f32 v92,v48,v92
v_fma_f32 v93,v118,v93,v117
v_mfma_f32_16x16x32_bf16 v[28:31],v[184:187],v[60:63],v[28:31]
ds_read_b128 v[80:83],v2 offset:31744
v_mul_f32 v93,v49,v93
v_mfma_f32_16x16x32_bf16 v[32:35],v[188:191],v[60:63],v[32:35]
s_waitcnt vmcnt(7)
v_exp_f32 v92,v92
v_mfma_f32_16x16x32_bf16 v[36:39],v[192:195],v[60:63],v[36:39]
s_barrier
v_mfma_f32_16x16x32_bf16 v[28:31],v[196:199],v[64:67],v[28:31]
ds_read_b128 v[52:55],v2 offset:32768
ds_read_b32 v13,v7 offset:0
v_mfma_f32_16x16x32_bf16 v[32:35],v[200:203],v[64:67],v[32:35]
ds_read_b64 v[112:113],v11 offset:0
v_fma_f32 v92,v92,v119,v119
v_mfma_f32_16x16x32_bf16 v[36:39],v[204:207],v[64:67],v[36:39]
s_waitcnt lgkmcnt(5)
v_exp_f32 v93,v93
v_mfma_f32_16x16x32_bf16 v[28:31],v[208:211],v[68:71],v[28:31]
ds_read_b128 v[56:59],v2 offset:33792
v_fma_f32 v93,v93,v119,v119
v_mfma_f32_16x16x32_bf16 v[32:35],v[212:215],v[68:71],v[32:35]
s_waitcnt lgkmcnt(2)
v_rcp_f32 v92,v92
v_mfma_f32_16x16x32_bf16 v[36:39],v[216:219],v[68:71],v[36:39]
s_waitcnt lgkmcnt(1)
v_cmp_eq_u32 vcc,s60,v13
v_rcp_f32 v93,v93
v_fma_f32 v94,v118,v94,v117
v_mfma_f32_16x16x32_bf16 v[28:31],v[220:223],v[72:75],v[28:31]
s_cmp_eq_u32 vcc_lo,-1
ds_read_b128 v[60:63],v2 offset:34816
v_mul_f32 v94,v50,v94
v_mfma_f32_16x16x32_bf16 v[32:35],v[224:227],v[72:75],v[32:35]
s_cselect_b32 s67,s86,s59
v_exp_f32 v94,v94
v_mfma_f32_16x16x32_bf16 v[36:39],v[228:231],v[72:75],v[36:39]
v_fma_f32 v94,v94,v119,v119)"
R"(
v_fma_f32 v95,v118,v95,v117
v_mfma_f32_16x16x32_bf16 v[28:31],v[232:235],v[76:79],v[28:31]
s_lshr_b32 s61,s67,0
v_mul_f32 v95,v51,v95
v_exp_f32 v95,v95
v_mfma_f32_16x16x32_bf16 v[32:35],v[236:239],v[76:79],v[32:35]
s_xor_b32 s63,s63,65536
v_fma_f32 v95,v95,v119,v119
v_rcp_f32 v94,v94
v_rcp_f32 v95,v95
v_mfma_f32_16x16x32_bf16 v[36:39],v[240:243],v[76:79],v[36:39]
s_cmp_ge_u32 s66,s59
ds_read_b128 v[64:67],v2 offset:35840
v_mul_f32 v48,v48,v92
v_mul_f32 v49,v49,v93
v_mul_f32 v50,v50,v94
v_mul_f32 v51,v51,v95
v_cvt_pk_bf16_f32 v100,v48,v49
v_cvt_pk_bf16_f32 v101,v50,v51
v_mfma_f32_16x16x32_bf16 v[28:31],v[244:247],v[80:83],v[28:31]
buffer_store_dwordx2 v[100:101],v9,s[36:39],s73 offen offset:0 sc0 sc1
ds_read_b128 v[68:71],v2 offset:36864
v_mfma_f32_16x16x32_bf16 v[32:35],v[248:251],v[80:83],v[32:35]
s_waitcnt lgkmcnt(3)
v_mfma_f32_16x16x32_bf16 v[36:39],v[252:255],v[80:83],v[36:39]
s_cbranch_scc1 Lg3_slow1_%=
Lg3_d1w3_%=:
v_mfma_f32_16x16x32_bf16 v[40:43],a[0:3],v[52:55],0
s_mov_b32 m0,s62
ds_read_b128 v[72:75],v2 offset:37888
v_mul_f32 v116,s75,v112
v_mfma_f32_16x16x32_bf16 v[44:47],a[4:7],v[52:55],0
s_mul_i32 s73,s48,7
v_fma_f32 v113,v113,s75,v12
v_fma_f32 v113,-v116,v116,v113
v_mfma_f32_16x16x32_bf16 v[48:51],a[8:11],v[52:55],0
s_lshr_b32 s73,s73,1
v_max_f32 v113,v113,v12
v_rsq_f32 v112,v113
v_mfma_f32_16x16x32_bf16 v[40:43],a[12:15],v[56:59],v[40:43]
s_add_u32 s74,s53,s55
ds_read_b128 v[76:79],v2 offset:38912
v_mul_f32 v117,s76,v112
v_mfma_f32_16x16x32_bf16 v[44:47],a[16:19],v[56:59],v[44:47]
buffer_load_dwordx4 v0,s[36:39],s61 offen offset:0 sc1 lds
v_mfma_f32_16x16x32_bf16 v[48:51],a[20:23],v[56:59],v[48:51]
s_waitcnt lgkmcnt(3)
v_mul_f32 v118,v112,v112
v_mul_f32 v119,v113,v112
v_mfma_f32_16x16x32_bf16 v[40:43],a[24:27],v[60:63],v[40:43]
s_cmp_eq_u32 s74,s56
ds_read_b128 v[80:83],v2 offset:39936
v_mul_f32 v118,s77,v118
v_mfma_f32_16x16x32_bf16 v[44:47],a[28:31],v[60:63],v[44:47]
s_cselect_b32 s74,s57,s74
v_mul_f32 v118,v118,v117
v_fma_f32 v28,-v116,v16,v28
v_mfma_f32_16x16x32_bf16 v[48:51],a[32:35],v[60:63],v[48:51]
buffer_load_dwordx4 v0,s[36:39],s61 offen offset:1024 sc1 lds
v_mfma_f32_16x16x32_bf16 v[40:43],a[36:39],v[64:67],v[40:43]
s_cmp_eq_u32 s74,s58
ds_read_b128 v[52:55],v2 offset:40960
v_fma_f32 v29,-v116,v17,v29
v_mfma_f32_16x16x32_bf16 v[44:47],a[40:43],v[64:67],v[44:47]
s_cselect_b32 s74,s59,s74
v_fma_f32 v30,-v116,v18,v30
v_fma_f32 v31,-v116,v19,v31
v_mfma_f32_16x16x32_bf16 v[48:51],a[44:47],v[64:67],v[48:51]
s_waitcnt lgkmcnt(3)
v_mul_f32 v84,v28,v28
v_mul_f32 v85,v29,v29
v_mfma_f32_16x16x32_bf16 v[40:43],a[48:51],v[68:71],v[40:43]
buffer_load_dwordx4 v0,s[36:39],s61 offen offset:2048 sc1 lds
v_mfma_f32_16x16x32_bf16 v[44:47],a[52:55],v[68:71],v[44:47]
s_cmp_ge_u32 s67,s59
ds_read_b128 v[56:59],v2 offset:41984
v_mul_f32 v86,v30,v30
v_mfma_f32_16x16x32_bf16 v[48:51],a[56:59],v[68:71],v[48:51]
s_cselect_b32 s86,s50,s51
v_mul_f32 v87,v31,v31
v_fma_f32 v84,v118,v84,v117
v_mfma_f32_16x16x32_bf16 v[40:43],a[60:63],v[72:75],v[40:43]
s_mul_i32 s70,s47,5
ds_read_b128 v[60:63],v2 offset:43008
v_mul_f32 v84,v28,v84
v_mfma_f32_16x16x32_bf16 v[44:47],a[64:67],v[72:75],v[44:47]
buffer_load_dwordx4 v0,s[36:39],s61 offen offset:3072 sc1 lds
v_mfma_f32_16x16x32_bf16 v[48:51],a[68:71],v[72:75],v[48:51]
s_waitcnt lgkmcnt(3)
v_exp_f32 v84,v84
v_mfma_f32_16x16x32_bf16 v[40:43],a[72:75],v[76:79],v[40:43]
s_add_u32 m0,s62,16384
ds_read_b128 v[64:67],v2 offset:44032
v_fma_f32 v84,v84,v119,v119
v_mfma_f32_16x16x32_bf16 v[44:47],a[76:79],v[76:79],v[44:47]
s_lshr_b32 s70,s70,6
v_rcp_f32 v84,v84
v_mfma_f32_16x16x32_bf16 v[48:51],a[80:83],v[76:79],v[48:51]
s_lshr_b32 s72,s61,5
v_fma_f32 v85,v118,v85,v117
v_mul_f32 v85,v29,v85
v_mfma_f32_16x16x32_bf16 v[40:43],a[84:87],v[80:83],v[40:43]
s_mov_b32 s62,s64
ds_read_b128 v[68:71],v2 offset:45056
v_fma_f32 v86,v118,v86,v117
v_mfma_f32_16x16x32_bf16 v[44:47],a[88:91],v[80:83],v[44:47]
s_mov_b64 s[44:45],s[46:47]
v_exp_f32 v85,v85
v_mfma_f32_16x16x32_bf16 v[48:51],a[92:95],v[80:83],v[48:51]
s_waitcnt lgkmcnt(3)
v_mul_f32 v86,v30,v86
v_fma_f32 v85,v85,v119,v119
v_mfma_f32_16x16x32_bf16 v[40:43],a[96:99],v[52:55],v[40:43]
s_mov_b64 s[46:47],s[48:49]
ds_read_b128 v[72:75],v2 offset:46080
v_exp_f32 v86,v86
v_mfma_f32_16x16x32_bf16 v[44:47],a[100:103],v[52:55],v[44:47]
s_mov_b64 s[48:49],s[50:51]
v_rcp_f32 v85,v85
v_mfma_f32_16x16x32_bf16 v[48:51],a[104:107],v[52:55],v[48:51]
s_mov_b64 s[50:51],s[52:53]
v_fma_f32 v86,v86,v119,v119
v_fma_f32 v87,v118,v87,v117
v_mfma_f32_16x16x32_bf16 v[40:43],a[108:111],v[56:59],v[40:43]
s_mov_b32 s52,s74
ds_read_b128 v[76:79],v2 offset:47104
v_mul_f32 v87,v31,v87
v_mfma_f32_16x16x32_bf16 v[44:47],a[112:115],v[56:59],v[44:47]
v_exp_f32 v87,v87
v_mfma_f32_16x16x32_bf16 v[48:51],a[116:119],v[56:59],v[48:51]
s_waitcnt lgkmcnt(3)
v_rcp_f32 v86,v86
v_mfma_f32_16x16x32_bf16 v[40:43],a[120:123],v[60:63],v[40:43]
ds_read_b128 v[80:83],v2 offset:48128
v_fma_f32 v87,v87,v119,v119
v_mfma_f32_16x16x32_bf16 v[44:47],a[124:127],v[60:63],v[44:47]
s_waitcnt vmcnt(7)
v_rcp_f32 v87,v87
v_mfma_f32_16x16x32_bf16 v[48:51],a[128:131],v[60:63],v[48:51]
s_barrier
v_mfma_f32_16x16x32_bf16 v[40:43],a[132:135],v[64:67],v[40:43]
ds_read_b128 v[52:55],v2 offset:49152
v_add_f32_dpp v108,v108,v108 quad_perm:[1,0,3,2] row_mask:0xf bank_mask:0xf
v_mfma_f32_16x16x32_bf16 v[44:47],a[136:139],v[64:67],v[44:47]
s_nop 0
v_add_f32_dpp v108,v108,v108 quad_perm:[2,3,0,1] row_mask:0xf bank_mask:0xf
v_mul_f32 v28,v28,v84
v_mfma_f32_16x16x32_bf16 v[48:51],a[140:143],v[64:67],v[48:51]
s_waitcnt lgkmcnt(3)
v_add_f32_dpp v108,v108,v108 row_half_mirror row_mask:0xf bank_mask:0xf
v_mul_f32 v29,v29,v85
v_mfma_f32_16x16x32_bf16 v[40:43],a[144:147],v[68:71],v[40:43]
ds_read_b128 v[56:59],v2 offset:50176
v_mul_f32 v30,v30,v86
v_mfma_f32_16x16x32_bf16 v[44:47],a[148:151],v[68:71],v[44:47]
v_mul_f32 v31,v31,v87
v_cvt_pk_bf16_f32 v96,v28,v29
v_mfma_f32_16x16x32_bf16 v[48:51],a[152:155],v[68:71],v[48:51]
v_cvt_pk_bf16_f32 v97,v30,v31
v_fma_f32 v32,-v116,v20,v32
v_mfma_f32_16x16x32_bf16 v[40:43],a[156:159],v[72:75],v[40:43]
ds_read_b128 v[60:63],v2 offset:51200
v_fma_f32 v33,-v116,v21,v33
v_mfma_f32_16x16x32_bf16 v[44:47],a[160:163],v[72:75],v[44:47]
v_fma_f32 v34,-v116,v22,v34
v_fma_f32 v35,-v116,v23,v35
v_mfma_f32_16x16x32_bf16 v[48:51],a[164:167],v[72:75],v[48:51]
s_waitcnt lgkmcnt(3)
v_mul_f32 v88,v32,v32
v_mul_f32 v89,v33,v33
v_mfma_f32_16x16x32_bf16 v[40:43],a[168:171],v[76:79],v[40:43]
ds_read_b128 v[64:67],v2 offset:52224
v_mul_f32 v90,v34,v34
v_mfma_f32_16x16x32_bf16 v[44:47],a[172:175],v[76:79],v[44:47]
v_mul_f32 v91,v35,v35
v_fma_f32 v88,v118,v88,v117
v_mfma_f32_16x16x32_bf16 v[48:51],a[176:179],v[76:79],v[48:51]
v_mul_f32 v88,v32,v88
v_fma_f32 v89,v118,v89,v117
v_mfma_f32_16x16x32_bf16 v[40:43],a[180:183],v[80:83],v[40:43]
ds_read_b128 v[68:71],v2 offset:53248
v_mul_f32 v89,v33,v89
v_mfma_f32_16x16x32_bf16 v[44:47],a[184:187],v[80:83],v[44:47]
buffer_load_dwordx4 v1,s[36:39],s61 offen offset:0 sc1 lds
v_mfma_f32_16x16x32_bf16 v[48:51],a[188:191],v[80:83],v[48:51]
s_waitcnt lgkmcnt(3)
ds_write_b32 v10,v108 offset:128
v_fma_f32 v90,v118,v90,v117
v_mfma_f32_16x16x32_bf16 v[40:43],a[192:195],v[52:55],v[40:43]
ds_read_b128 v[72:75],v2 offset:54272
v_fma_f32 v91,v118,v91,v117
v_mfma_f32_16x16x32_bf16 v[44:47],a[196:199],v[52:55],v[44:47]
v_exp_f32 v88,v88
v_mfma_f32_16x16x32_bf16 v[48:51],a[200:203],v[52:55],v[48:51]
buffer_load_dwordx4 v1,s[36:39],s61 offen offset:1024 sc1 lds
v_mfma_f32_16x16x32_bf16 v[40:43],a[204:207],v[56:59],v[40:43]
ds_read_b128 v[76:79],v2 offset:55296
v_mul_f32 v90,v34,v90
v_mfma_f32_16x16x32_bf16 v[44:47],a[208:211],v[56:59],v[44:47]
v_exp_f32 v89,v89)"
R"(
v_mfma_f32_16x16x32_bf16 v[48:51],a[212:215],v[56:59],v[48:51]
s_waitcnt lgkmcnt(4)
v_exp_f32 v90,v90
v_mfma_f32_16x16x32_bf16 v[40:43],a[216:219],v[60:63],v[40:43]
buffer_load_dwordx4 v1,s[36:39],s61 offen offset:2048 sc1 lds
v_mfma_f32_16x16x32_bf16 v[44:47],a[220:223],v[60:63],v[44:47]
ds_read_b128 v[80:83],v2 offset:56320
v_mul_f32 v91,v35,v91
v_mfma_f32_16x16x32_bf16 v[48:51],a[224:227],v[60:63],v[48:51]
v_exp_f32 v91,v91
v_mfma_f32_16x16x32_bf16 v[40:43],a[228:231],v[64:67],v[40:43]
ds_read_b128 v[52:55],v2 offset:57344
v_fma_f32 v88,v88,v119,v119
v_mfma_f32_16x16x32_bf16 v[44:47],a[232:235],v[64:67],v[44:47]
buffer_load_dwordx4 v1,s[36:39],s61 offen offset:3072 sc1 lds
v_mfma_f32_16x16x32_bf16 v[48:51],a[236:239],v[64:67],v[48:51]
s_waitcnt lgkmcnt(3)
buffer_load_dword v104,v8,s[36:39],s72 offen offset:0 sc1
v_mfma_f32_16x16x32_bf16 v[40:43],a[240:243],v[68:71],v[40:43]
ds_read_b128 v[56:59],v2 offset:58368
v_fma_f32 v89,v89,v119,v119
v_mfma_f32_16x16x32_bf16 v[44:47],a[244:247],v[68:71],v[44:47]
s_waitcnt vmcnt(9)
v_rcp_f32 v88,v88
v_mfma_f32_16x16x32_bf16 v[48:51],a[248:251],v[68:71],v[48:51]
s_barrier
v_mfma_f32_16x16x32_bf16 v[40:43],a[252:255],v[72:75],v[40:43]
buffer_store_dword v5,v4,s[36:39],s70 offen offset:0 sc0 sc1
ds_read_b128 v[60:63],v2 offset:59392
v_rcp_f32 v89,v89
v_mfma_f32_16x16x32_bf16 v[44:47],v[128:131],v[72:75],v[44:47]
v_fma_f32 v90,v90,v119,v119
v_fma_f32 v91,v91,v119,v119
v_mfma_f32_16x16x32_bf16 v[48:51],v[132:135],v[72:75],v[48:51]
s_waitcnt lgkmcnt(3)
v_rcp_f32 v90,v90
v_mfma_f32_16x16x32_bf16 v[40:43],v[136:139],v[76:79],v[40:43]
ds_read_b128 v[64:67],v2 offset:60416
v_mul_f32 v32,v32,v88
v_mfma_f32_16x16x32_bf16 v[44:47],v[140:143],v[76:79],v[44:47]
v_rcp_f32 v91,v91
v_mfma_f32_16x16x32_bf16 v[48:51],v[144:147],v[76:79],v[48:51]
v_mul_f32 v33,v33,v89
v_mul_f32 v34,v34,v90
v_mfma_f32_16x16x32_bf16 v[40:43],v[148:151],v[80:83],v[40:43]
ds_read_b128 v[68:71],v2 offset:61440
v_mul_f32 v35,v35,v91
v_mfma_f32_16x16x32_bf16 v[44:47],v[152:155],v[80:83],v[44:47]
v_cvt_pk_bf16_f32 v98,v32,v33
v_cvt_pk_bf16_f32 v99,v34,v35
v_fma_f32 v36,-v116,v24,v36
v_mfma_f32_16x16x32_bf16 v[48:51],v[156:159],v[80:83],v[48:51]
s_waitcnt lgkmcnt(3)
v_fma_f32 v37,-v116,v25,v37
v_fma_f32 v38,-v116,v26,v38
v_mfma_f32_16x16x32_bf16 v[40:43],v[160:163],v[52:55],v[40:43]
ds_read_b128 v[72:75],v2 offset:62464
v_fma_f32 v39,-v116,v27,v39
v_mfma_f32_16x16x32_bf16 v[44:47],v[164:167],v[52:55],v[44:47]
v_mul_f32 v92,v36,v36
v_mul_f32 v93,v37,v37
v_mfma_f32_16x16x32_bf16 v[48:51],v[168:171],v[52:55],v[48:51]
v_mul_f32 v94,v38,v38
v_mul_f32 v95,v39,v39
v_mfma_f32_16x16x32_bf16 v[40:43],v[172:175],v[56:59],v[40:43]
ds_read_b128 v[76:79],v2 offset:63488
v_fma_f32 v92,v118,v92,v117
v_mfma_f32_16x16x32_bf16 v[44:47],v[176:179],v[56:59],v[44:47]
buffer_store_dwordx4 v[96:99],v3,s[36:39],s73 offen offset:0 sc0 sc1
v_mul_f32 v92,v36,v92
v_exp_f32 v92,v92
v_mfma_f32_16x16x32_bf16 v[48:51],v[180:183],v[56:59],v[48:51]
s_waitcnt lgkmcnt(3)
v_fma_f32 v92,v92,v119,v119
v_fma_f32 v93,v118,v93,v117
v_mfma_f32_16x16x32_bf16 v[40:43],v[184:187],v[60:63],v[40:43]
ds_read_b128 v[80:83],v2 offset:64512
v_mul_f32 v93,v37,v93
v_mfma_f32_16x16x32_bf16 v[44:47],v[188:191],v[60:63],v[44:47]
s_waitcnt vmcnt(7)
v_xor_b32 v2,65536,v2
v_fma_f32 v94,v118,v94,v117
v_mfma_f32_16x16x32_bf16 v[48:51],v[192:195],v[60:63],v[48:51]
s_barrier
v_mfma_f32_16x16x32_bf16 v[40:43],v[196:199],v[64:67],v[40:43]
ds_read_b128 v[52:55],v2 offset:0
ds_read_b32 v13,v7 offset:256
v_mfma_f32_16x16x32_bf16 v[44:47],v[200:203],v[64:67],v[44:47]
ds_read_b64 v[112:113],v11 offset:128
v_fma_f32 v95,v118,v95,v117
v_mfma_f32_16x16x32_bf16 v[48:51],v[204:207],v[64:67],v[48:51]
s_waitcnt lgkmcnt(5)
v_exp_f32 v93,v93
v_mfma_f32_16x16x32_bf16 v[40:43],v[208:211],v[68:71],v[40:43]
ds_read_b128 v[56:59],v2 offset:1024
v_mul_f32 v94,v38,v94
v_mfma_f32_16x16x32_bf16 v[44:47],v[212:215],v[68:71],v[44:47]
s_waitcnt lgkmcnt(2)
v_exp_f32 v94,v94
v_mfma_f32_16x16x32_bf16 v[48:51],v[216:219],v[68:71],v[48:51]
s_waitcnt lgkmcnt(1)
v_cmp_eq_u32 vcc,s60,v13
v_rcp_f32 v92,v92
v_mul_f32 v95,v39,v95
v_mfma_f32_16x16x32_bf16 v[40:43],v[220:223],v[72:75],v[40:43]
s_cmp_eq_u32 vcc_lo,-1
ds_read_b128 v[60:63],v2 offset:2048
v_fma_f32 v93,v93,v119,v119
v_mfma_f32_16x16x32_bf16 v[44:47],v[224:227],v[72:75],v[44:47]
s_cselect_b32 s66,s86,s59
v_exp_f32 v95,v95
v_mfma_f32_16x16x32_bf16 v[48:51],v[228:231],v[72:75],v[48:51]
v_rcp_f32 v93,v93
v_mfma_f32_16x16x32_bf16 v[40:43],v[232:235],v[76:79],v[40:43]
s_lshr_b32 s61,s66,0
ds_read_b128 v[64:67],v2 offset:3072
v_fma_f32 v94,v94,v119,v119
v_mfma_f32_16x16x32_bf16 v[44:47],v[236:239],v[76:79],v[44:47]
s_xor_b32 s64,s64,65536
v_fma_f32 v95,v95,v119,v119
v_rcp_f32 v94,v94
v_rcp_f32 v95,v95
v_mfma_f32_16x16x32_bf16 v[48:51],v[240:243],v[76:79],v[48:51]
s_cmp_lt_u32 s67,s59
v_mul_f32 v36,v36,v92
v_mul_f32 v37,v37,v93
v_mul_f32 v38,v38,v94
v_mul_f32 v39,v39,v95
v_cvt_pk_bf16_f32 v100,v36,v37
v_cvt_pk_bf16_f32 v101,v38,v39
v_mfma_f32_16x16x32_bf16 v[40:43],v[244:247],v[80:83],v[40:43]
buffer_store_dwordx2 v[100:101],v9,s[36:39],s73 offen offset:0 sc0 sc1
ds_read_b128 v[68:71],v2 offset:4096
v_mfma_f32_16x16x32_bf16 v[44:47],v[248:251],v[80:83],v[44:47]
s_waitcnt lgkmcnt(3)
v_mfma_f32_16x16x32_bf16 v[48:51],v[252:255],v[80:83],v[48:51]
s_cbranch_scc1 Lg3_d0w3_%=
s_branch Lg3_slow0_%=
Lg3_slow0_%=:
s_cmp_lt_u32 s66,s59
s_cbranch_scc1 Lg3_gs0_%=
Lg3_slowb0_%=:
s_cmp_eq_u32 s54,1
s_cselect_b32 s68,s49,s48
s_lshr_b32 s68,s68,5
s_add_u32 m0,s78,8192
s_nop 0
buffer_load_dword v6,s[36:39],s68 offen offset:0 sc1 lds
s_mul_i32 s73,s47,7
s_lshr_b32 s73,s73,1
v_mul_f32 v116,s75,v112
v_fma_f32 v113,v113,s75,v12
v_fma_f32 v113,-v116,v116,v113
v_max_f32 v113,v113,v12
v_rsq_f32 v112,v113
s_nop 0
v_mul_f32 v117,s76,v112
v_mul_f32 v118,v112,v112
v_mul_f32 v119,v113,v112
v_mul_f32 v118,s77,v118
v_mul_f32 v118,v118,v117
v_fma_f32 v40,-v116,v16,v40
v_fma_f32 v41,-v116,v17,v41
v_fma_f32 v42,-v116,v18,v42
v_fma_f32 v43,-v116,v19,v43
v_mul_f32 v84,v40,v40
v_mul_f32 v85,v41,v41
v_mul_f32 v86,v42,v42
v_mul_f32 v87,v43,v43
v_fma_f32 v84,v118,v84,v117
v_fma_f32 v85,v118,v85,v117
v_fma_f32 v86,v118,v86,v117
v_fma_f32 v87,v118,v87,v117
v_mul_f32 v84,v40,v84
v_mul_f32 v85,v41,v85
v_mul_f32 v86,v42,v86
v_mul_f32 v87,v43,v87
v_exp_f32 v84,v84
v_exp_f32 v85,v85
v_exp_f32 v86,v86
v_exp_f32 v87,v87
v_fma_f32 v84,v84,v119,v119
v_fma_f32 v85,v85,v119,v119
v_fma_f32 v86,v86,v119,v119
v_fma_f32 v87,v87,v119,v119
v_rcp_f32 v84,v84
v_rcp_f32 v85,v85
v_rcp_f32 v86,v86
v_rcp_f32 v87,v87
v_mul_f32 v40,v40,v84
v_mul_f32 v41,v41,v85
v_mul_f32 v42,v42,v86
v_mul_f32 v43,v43,v87
v_cvt_pk_bf16_f32 v96,v40,v41
v_cvt_pk_bf16_f32 v97,v42,v43
v_fma_f32 v44,-v116,v20,v44
v_fma_f32 v45,-v116,v21,v45
v_fma_f32 v46,-v116,v22,v46
v_fma_f32 v47,-v116,v23,v47
v_mul_f32 v88,v44,v44)"
R"(
v_mul_f32 v89,v45,v45
v_mul_f32 v90,v46,v46
v_mul_f32 v91,v47,v47
v_fma_f32 v88,v118,v88,v117
v_fma_f32 v89,v118,v89,v117
v_fma_f32 v90,v118,v90,v117
v_fma_f32 v91,v118,v91,v117
v_mul_f32 v88,v44,v88
s_add_u32 m0,s78,8448
s_nop 0
buffer_load_dword v6,s[36:39],s68 offen offset:0 sc1 lds
v_mul_f32 v89,v45,v89
v_mul_f32 v90,v46,v90
v_mul_f32 v91,v47,v91
v_exp_f32 v88,v88
v_exp_f32 v89,v89
v_exp_f32 v90,v90
v_exp_f32 v91,v91
v_fma_f32 v88,v88,v119,v119
v_fma_f32 v89,v89,v119,v119
v_fma_f32 v90,v90,v119,v119
v_fma_f32 v91,v91,v119,v119
v_rcp_f32 v88,v88
v_rcp_f32 v89,v89
v_rcp_f32 v90,v90
v_rcp_f32 v91,v91
v_mul_f32 v44,v44,v88
v_mul_f32 v45,v45,v89
v_mul_f32 v46,v46,v90
v_mul_f32 v47,v47,v91
v_cvt_pk_bf16_f32 v98,v44,v45
v_cvt_pk_bf16_f32 v99,v46,v47
buffer_store_dwordx4 v[96:99],v3,s[36:39],s73 offen offset:0 sc0 sc1
v_fma_f32 v48,-v116,v24,v48
v_fma_f32 v49,-v116,v25,v49
v_fma_f32 v50,-v116,v26,v50
v_fma_f32 v51,-v116,v27,v51
v_mul_f32 v92,v48,v48
v_mul_f32 v93,v49,v49
v_mul_f32 v94,v50,v50
v_mul_f32 v95,v51,v51
v_fma_f32 v92,v118,v92,v117
v_fma_f32 v93,v118,v93,v117
v_fma_f32 v94,v118,v94,v117
v_fma_f32 v95,v118,v95,v117
v_mul_f32 v92,v48,v92
v_mul_f32 v93,v49,v93
v_mul_f32 v94,v50,v94
v_mul_f32 v95,v51,v95
v_exp_f32 v92,v92
v_exp_f32 v93,v93
v_exp_f32 v94,v94
v_exp_f32 v95,v95
v_fma_f32 v92,v92,v119,v119
v_fma_f32 v93,v93,v119,v119
v_fma_f32 v94,v94,v119,v119
v_fma_f32 v95,v95,v119,v119
v_rcp_f32 v92,v92
v_rcp_f32 v93,v93
v_rcp_f32 v94,v94
v_rcp_f32 v95,v95
v_mul_f32 v48,v48,v92
v_mul_f32 v49,v49,v93
v_mul_f32 v50,v50,v94
v_mul_f32 v51,v51,v95
v_cvt_pk_bf16_f32 v100,v48,v49
v_cvt_pk_bf16_f32 v101,v50,v51
buffer_store_dwordx2 v[100:101],v9,s[36:39],s73 offen offset:0 sc0 sc1
s_cmp_ge_u32 s48,s59
s_cbranch_scc1 Lg3_done0_%=
s_add_u32 m0,s78,16384
s_nop 0
buffer_load_dword v6,s[36:39],s68 offen offset:0 sc1 lds
s_waitcnt vmcnt(4)
s_barrier
ds_read_b32 v13,v7 offset:8192
s_mul_i32 s70,s46,5
s_lshr_b32 s70,s70,6
buffer_store_dword v5,v4,s[36:39],s70 offen offset:0 sc0 sc1
s_mov_b32 s46,s59
s_mov_b32 s69,18432
s_waitcnt lgkmcnt(0)
v_cmp_eq_u32 vcc,s60,v13
s_nop 0
s_cmp_eq_u32 vcc_lo,-1
s_cbranch_scc1 Lg3_go0_%=
s_add_u32 m0,s78,16640
s_nop 0
buffer_load_dword v6,s[36:39],s68 offen offset:0 sc1 lds
s_waitcnt vmcnt(5)
s_barrier
ds_read_b32 v13,v7 offset:8448
s_mov_b32 s69,18688
s_waitcnt lgkmcnt(0)
v_cmp_eq_u32 vcc,s60,v13
s_nop 0
s_cmp_eq_u32 vcc_lo,-1
s_cbranch_scc1 Lg3_go0_%=
s_add_u32 m0,s78,8192
s_nop 0
buffer_load_dword v6,s[36:39],s68 offen offset:0 sc1 lds
s_waitcnt vmcnt(3)
s_barrier
ds_read_b32 v13,v7 offset:16384
s_mul_i32 s70,s47,5
s_lshr_b32 s70,s70,6
buffer_store_dword v5,v4,s[36:39],s70 offen offset:0 sc0 sc1
s_mov_b32 s47,s59
s_mov_b32 s69,10240
s_waitcnt lgkmcnt(0)
v_cmp_eq_u32 vcc,s60,v13
s_nop 0
s_cmp_eq_u32 vcc_lo,-1
s_cbranch_scc1 Lg3_go0_%=
s_add_u32 m0,s78,8448
s_nop 0
buffer_load_dword v6,s[36:39],s68 offen offset:0 sc1 lds
s_waitcnt vmcnt(3)
s_barrier
ds_read_b32 v13,v7 offset:16640
s_mov_b32 s69,10496
s_waitcnt lgkmcnt(0)
v_cmp_eq_u32 vcc,s60,v13
s_nop 0
s_cmp_eq_u32 vcc_lo,-1
s_cbranch_scc1 Lg3_go0_%=
Lg3_poll0_%=:
s_add_u32 m0,s78,16384
s_nop 0
buffer_load_dword v6,s[36:39],s68 offen offset:0 sc1 lds
s_waitcnt vmcnt(2)
s_barrier
ds_read_b32 v13,v7 offset:8192
s_mov_b32 s69,18432
s_waitcnt lgkmcnt(0)
v_cmp_eq_u32 vcc,s60,v13
s_nop 0
s_cmp_eq_u32 vcc_lo,-1
s_cbranch_scc1 Lg3_go0_%=
s_sleep 4
s_add_u32 m0,s78,16640
s_nop 0
buffer_load_dword v6,s[36:39],s68 offen offset:0 sc1 lds
s_waitcnt vmcnt(2)
s_barrier
ds_read_b32 v13,v7 offset:8448
s_mov_b32 s69,18688
s_waitcnt lgkmcnt(0)
v_cmp_eq_u32 vcc,s60,v13
s_nop 0
s_cmp_eq_u32 vcc_lo,-1
s_cbranch_scc1 Lg3_go0_%=
s_sleep 4
s_add_u32 m0,s78,8192
s_nop 0
buffer_load_dword v6,s[36:39],s68 offen offset:0 sc1 lds
s_waitcnt vmcnt(2)
s_barrier
ds_read_b32 v13,v7 offset:16384
s_mov_b32 s69,10240
s_waitcnt lgkmcnt(0)
v_cmp_eq_u32 vcc,s60,v13
s_nop 0
s_cmp_eq_u32 vcc_lo,-1
s_cbranch_scc1 Lg3_go0_%=
s_sleep 4
s_add_u32 m0,s78,8448
s_nop 0
buffer_load_dword v6,s[36:39],s68 offen offset:0 sc1 lds
s_waitcnt vmcnt(2)
s_barrier
ds_read_b32 v13,v7 offset:16640
s_mov_b32 s69,10496
s_waitcnt lgkmcnt(0)
v_cmp_eq_u32 vcc,s60,v13
s_nop 0
s_cmp_eq_u32 vcc_lo,-1
s_cbranch_scc1 Lg3_go0_%=
s_sleep 4
s_add_u32 s79,s79,1
s_cmp_lt_u32 s79,20000
s_cbranch_scc1 Lg3_poll0_%=
Lg3_go0_%=:
s_xor_b32 s62,s63,65536
s_lshr_b32 s61,s48,0
s_mov_b32 m0,s62
s_nop 0
buffer_load_dwordx4 v0,s[36:39],s61 offen offset:0 sc1 lds
buffer_load_dwordx4 v0,s[36:39],s61 offen offset:1024 sc1 lds
buffer_load_dwordx4 v0,s[36:39],s61 offen offset:2048 sc1 lds
buffer_load_dwordx4 v0,s[36:39],s61 offen offset:3072 sc1 lds
s_add_u32 m0,s62,16384
s_nop 0
buffer_load_dwordx4 v1,s[36:39],s61 offen offset:0 sc1 lds
buffer_load_dwordx4 v1,s[36:39],s61 offen offset:1024 sc1 lds
buffer_load_dwordx4 v1,s[36:39],s61 offen offset:2048 sc1 lds
buffer_load_dwordx4 v1,s[36:39],s61 offen offset:3072 sc1 lds
s_lshr_b32 s72,s61,5
buffer_load_dword v104,v8,s[36:39],s72 offen offset:0 sc1
v_add_u32 v84,s69,v7)"
R"(
s_waitcnt vmcnt(5)
s_barrier
ds_read_b32 v13,v84 offset:0
ds_read_b128 v[52:55],v2 offset:0
ds_read_b128 v[56:59],v2 offset:1024
ds_read_b128 v[60:63],v2 offset:2048
ds_read_b128 v[64:67],v2 offset:3072
ds_read_b128 v[68:71],v2 offset:4096
s_mul_i32 s70,s46,5
s_lshr_b32 s70,s70,6
buffer_store_dword v5,v4,s[36:39],s70 offen offset:0 sc0 sc1
s_mov_b32 s46,s59
s_mul_i32 s70,s47,5
s_lshr_b32 s70,s70,6
buffer_store_dword v5,v4,s[36:39],s70 offen offset:0 sc0 sc1
s_mov_b32 s47,s59
s_mul_i32 s70,s59,5
s_lshr_b32 s70,s70,6
buffer_store_dword v5,v4,s[36:39],s70 offen offset:0 sc0 sc1
s_waitcnt lgkmcnt(5)
v_cmp_eq_u32 vcc,s60,v13
s_nop 0
s_cmp_eq_u32 vcc_lo,-1
s_cselect_b32 s66,s49,s59
s_lshr_b32 s61,s66,0
s_xor_b32 s62,s64,65536
s_waitcnt lgkmcnt(3)
s_setpc_b64 s[90:91]
Lg3_gs0_%=:
s_xor_b32 s62,s63,65536
s_lshr_b32 s61,s48,0
s_cmp_eq_u32 s54,1
s_cselect_b32 s68,s49,s48
s_lshr_b32 s68,s68,5
s_mul_i32 s73,s47,7
s_lshr_b32 s73,s73,1
s_mov_b32 m0,s62
s_nop 0
buffer_load_dwordx4 v0,s[36:39],s61 offen offset:0 sc1 lds
v_mul_f32 v116,s75,v112
v_fma_f32 v113,v113,s75,v12
v_fma_f32 v113,-v116,v116,v113
v_max_f32 v113,v113,v12
v_rsq_f32 v112,v113
s_nop 0
v_mul_f32 v117,s76,v112
v_mul_f32 v118,v112,v112
v_mul_f32 v119,v113,v112
v_mul_f32 v118,s77,v118
v_mul_f32 v118,v118,v117
v_fma_f32 v40,-v116,v16,v40
buffer_load_dwordx4 v0,s[36:39],s61 offen offset:1024 sc1 lds
v_fma_f32 v41,-v116,v17,v41
v_fma_f32 v42,-v116,v18,v42
v_fma_f32 v43,-v116,v19,v43
v_mul_f32 v84,v40,v40
v_mul_f32 v85,v41,v41
v_mul_f32 v86,v42,v42
v_mul_f32 v87,v43,v43
v_fma_f32 v84,v118,v84,v117
v_fma_f32 v85,v118,v85,v117
v_fma_f32 v86,v118,v86,v117
v_fma_f32 v87,v118,v87,v117
buffer_load_dwordx4 v0,s[36:39],s61 offen offset:2048 sc1 lds
v_mul_f32 v84,v40,v84
v_mul_f32 v85,v41,v85
v_mul_f32 v86,v42,v86
v_mul_f32 v87,v43,v87
v_exp_f32 v84,v84
v_exp_f32 v85,v85
v_exp_f32 v86,v86
v_exp_f32 v87,v87
v_fma_f32 v84,v84,v119,v119
v_fma_f32 v85,v85,v119,v119
v_fma_f32 v86,v86,v119,v119
buffer_load_dwordx4 v0,s[36:39],s61 offen offset:3072 sc1 lds
v_fma_f32 v87,v87,v119,v119
v_rcp_f32 v84,v84
v_rcp_f32 v85,v85
v_rcp_f32 v86,v86
v_rcp_f32 v87,v87
v_mul_f32 v40,v40,v84
v_mul_f32 v41,v41,v85
v_mul_f32 v42,v42,v86
v_mul_f32 v43,v43,v87
v_cvt_pk_bf16_f32 v96,v40,v41
v_cvt_pk_bf16_f32 v97,v42,v43
s_add_u32 m0,s78,8192
s_nop 0
buffer_load_dword v6,s[36:39],s68 offen offset:0 sc1 lds
v_fma_f32 v44,-v116,v20,v44
v_fma_f32 v45,-v116,v21,v45
v_fma_f32 v46,-v116,v22,v46
v_fma_f32 v47,-v116,v23,v47
v_mul_f32 v88,v44,v44
v_mul_f32 v89,v45,v45
v_mul_f32 v90,v46,v46
v_mul_f32 v91,v47,v47
v_fma_f32 v88,v118,v88,v117
v_fma_f32 v89,v118,v89,v117
v_fma_f32 v90,v118,v90,v117
s_add_u32 m0,s62,16384
s_nop 0
buffer_load_dwordx4 v1,s[36:39],s61 offen offset:0 sc1 lds
v_fma_f32 v91,v118,v91,v117
v_mul_f32 v88,v44,v88
v_mul_f32 v89,v45,v89
v_mul_f32 v90,v46,v90
v_mul_f32 v91,v47,v91
v_exp_f32 v88,v88
v_exp_f32 v89,v89
v_exp_f32 v90,v90
v_exp_f32 v91,v91
v_fma_f32 v88,v88,v119,v119
v_fma_f32 v89,v89,v119,v119
buffer_load_dwordx4 v1,s[36:39],s61 offen offset:1024 sc1 lds
v_fma_f32 v90,v90,v119,v119
v_fma_f32 v91,v91,v119,v119
v_rcp_f32 v88,v88
v_rcp_f32 v89,v89
v_rcp_f32 v90,v90
v_rcp_f32 v91,v91
v_mul_f32 v44,v44,v88
v_mul_f32 v45,v45,v89
v_mul_f32 v46,v46,v90
v_mul_f32 v47,v47,v91
v_cvt_pk_bf16_f32 v98,v44,v45
buffer_load_dwordx4 v1,s[36:39],s61 offen offset:2048 sc1 lds
v_cvt_pk_bf16_f32 v99,v46,v47
buffer_store_dwordx4 v[96:99],v3,s[36:39],s73 offen offset:0 sc0 sc1
v_fma_f32 v48,-v116,v24,v48
v_fma_f32 v49,-v116,v25,v49
v_fma_f32 v50,-v116,v26,v50
v_fma_f32 v51,-v116,v27,v51
v_mul_f32 v92,v48,v48
v_mul_f32 v93,v49,v49
v_mul_f32 v94,v50,v50
v_mul_f32 v95,v51,v51
v_fma_f32 v92,v118,v92,v117
buffer_load_dwordx4 v1,s[36:39],s61 offen offset:3072 sc1 lds
v_fma_f32 v93,v118,v93,v117
v_fma_f32 v94,v118,v94,v117
v_fma_f32 v95,v118,v95,v117
v_mul_f32 v92,v48,v92
v_mul_f32 v93,v49,v93
v_mul_f32 v94,v50,v94
v_mul_f32 v95,v51,v95
v_exp_f32 v92,v92
v_exp_f32 v93,v93
v_exp_f32 v94,v94
v_exp_f32 v95,v95
s_lshr_b32 s72,s61,5
buffer_load_dword v104,v8,s[36:39],s72 offen offset:0 sc1
v_fma_f32 v92,v92,v119,v119
v_fma_f32 v93,v93,v119,v119
v_fma_f32 v94,v94,v119,v119
v_fma_f32 v95,v95,v119,v119
v_rcp_f32 v92,v92
v_rcp_f32 v93,v93
v_rcp_f32 v94,v94
v_rcp_f32 v95,v95
v_mul_f32 v48,v48,v92
v_mul_f32 v49,v49,v93
v_mul_f32 v50,v50,v94
v_mul_f32 v51,v51,v95
v_cvt_pk_bf16_f32 v100,v48,v49
v_cvt_pk_bf16_f32 v101,v50,v51
buffer_store_dwordx2 v[100:101],v9,s[36:39],s73 offen offset:0 sc0 sc1
s_waitcnt vmcnt(7)
s_barrier
ds_read_b32 v13,v7 offset:8192
ds_read_b128 v[52:55],v2 offset:0
ds_read_b128 v[56:59],v2 offset:1024
ds_read_b128 v[60:63],v2 offset:2048
ds_read_b128 v[64:67],v2 offset:3072
ds_read_b128 v[68:71],v2 offset:4096
s_mul_i32 s70,s46,5
s_lshr_b32 s70,s70,6
buffer_store_dword v5,v4,s[36:39],s70 offen offset:0 sc0 sc1
s_waitcnt lgkmcnt(5)
v_cmp_eq_u32 vcc,s60,v13
s_nop 0
s_cmp_eq_u32 vcc_hi,-1
s_cselect_b32 s66,s49,s59
s_lshr_b32 s61,s66,0
s_xor_b32 s62,s64,65536
s_mov_b32 s46,s47
s_mov_b32 s47,s59
s_mul_i32 s70,s59,5
s_lshr_b32 s70,s70,6
buffer_store_dword v5,v4,s[36:39],s70 offen offset:0 sc0 sc1
s_waitcnt lgkmcnt(3)
s_setpc_b64 s[90:91]
Lg3_done0_%=:
s_waitcnt vmcnt(0)
s_barrier
s_mul_i32 s70,s46,5
s_lshr_b32 s70,s70,6
buffer_store_dword v5,v4,s[36:39],s70 offen offset:0 sc0 sc1
s_mul_i32 s70,s47,5)"
R"(
s_lshr_b32 s70,s70,6
buffer_store_dword v5,v4,s[36:39],s70 offen offset:0 sc0 sc1
s_branch Lg3_done_%=
Lg3_slow1_%=:
s_cmp_lt_u32 s67,s59
s_cbranch_scc1 Lg3_gs1_%=
Lg3_slowb1_%=:
s_cmp_eq_u32 s54,1
s_cselect_b32 s68,s50,s49
s_lshr_b32 s68,s68,5
s_add_u32 m0,s78,8192
s_nop 0
buffer_load_dword v6,s[36:39],s68 offen offset:0 sc1 lds
s_mul_i32 s73,s48,7
s_lshr_b32 s73,s73,1
v_mul_f32 v116,s75,v112
v_fma_f32 v113,v113,s75,v12
v_fma_f32 v113,-v116,v116,v113
v_max_f32 v113,v113,v12
v_rsq_f32 v112,v113
s_nop 0
v_mul_f32 v117,s76,v112
v_mul_f32 v118,v112,v112
v_mul_f32 v119,v113,v112
v_mul_f32 v118,s77,v118
v_mul_f32 v118,v118,v117
v_fma_f32 v28,-v116,v16,v28
v_fma_f32 v29,-v116,v17,v29
v_fma_f32 v30,-v116,v18,v30
v_fma_f32 v31,-v116,v19,v31
v_mul_f32 v84,v28,v28
v_mul_f32 v85,v29,v29
v_mul_f32 v86,v30,v30
v_mul_f32 v87,v31,v31
v_fma_f32 v84,v118,v84,v117
v_fma_f32 v85,v118,v85,v117
v_fma_f32 v86,v118,v86,v117
v_fma_f32 v87,v118,v87,v117
v_mul_f32 v84,v28,v84
v_mul_f32 v85,v29,v85
v_mul_f32 v86,v30,v86
v_mul_f32 v87,v31,v87
v_exp_f32 v84,v84
v_exp_f32 v85,v85
v_exp_f32 v86,v86
v_exp_f32 v87,v87
v_fma_f32 v84,v84,v119,v119
v_fma_f32 v85,v85,v119,v119
v_fma_f32 v86,v86,v119,v119
v_fma_f32 v87,v87,v119,v119
v_rcp_f32 v84,v84
v_rcp_f32 v85,v85
v_rcp_f32 v86,v86
v_rcp_f32 v87,v87
v_mul_f32 v28,v28,v84
v_mul_f32 v29,v29,v85
v_mul_f32 v30,v30,v86
v_mul_f32 v31,v31,v87
v_cvt_pk_bf16_f32 v96,v28,v29
v_cvt_pk_bf16_f32 v97,v30,v31
v_fma_f32 v32,-v116,v20,v32
v_fma_f32 v33,-v116,v21,v33
v_fma_f32 v34,-v116,v22,v34
v_fma_f32 v35,-v116,v23,v35
v_mul_f32 v88,v32,v32
v_mul_f32 v89,v33,v33
v_mul_f32 v90,v34,v34
v_mul_f32 v91,v35,v35
v_fma_f32 v88,v118,v88,v117
v_fma_f32 v89,v118,v89,v117
v_fma_f32 v90,v118,v90,v117
v_fma_f32 v91,v118,v91,v117
v_mul_f32 v88,v32,v88
s_add_u32 m0,s78,8448
s_nop 0
buffer_load_dword v6,s[36:39],s68 offen offset:0 sc1 lds
v_mul_f32 v89,v33,v89
v_mul_f32 v90,v34,v90
v_mul_f32 v91,v35,v91
v_exp_f32 v88,v88
v_exp_f32 v89,v89
v_exp_f32 v90,v90
v_exp_f32 v91,v91
v_fma_f32 v88,v88,v119,v119
v_fma_f32 v89,v89,v119,v119
v_fma_f32 v90,v90,v119,v119
v_fma_f32 v91,v91,v119,v119
v_rcp_f32 v88,v88
v_rcp_f32 v89,v89
v_rcp_f32 v90,v90
v_rcp_f32 v91,v91
v_mul_f32 v32,v32,v88
v_mul_f32 v33,v33,v89
v_mul_f32 v34,v34,v90
v_mul_f32 v35,v35,v91
v_cvt_pk_bf16_f32 v98,v32,v33
v_cvt_pk_bf16_f32 v99,v34,v35
buffer_store_dwordx4 v[96:99],v3,s[36:39],s73 offen offset:0 sc0 sc1
v_fma_f32 v36,-v116,v24,v36
v_fma_f32 v37,-v116,v25,v37
v_fma_f32 v38,-v116,v26,v38
v_fma_f32 v39,-v116,v27,v39
v_mul_f32 v92,v36,v36
v_mul_f32 v93,v37,v37
v_mul_f32 v94,v38,v38
v_mul_f32 v95,v39,v39
v_fma_f32 v92,v118,v92,v117
v_fma_f32 v93,v118,v93,v117
v_fma_f32 v94,v118,v94,v117
v_fma_f32 v95,v118,v95,v117
v_mul_f32 v92,v36,v92
v_mul_f32 v93,v37,v93
v_mul_f32 v94,v38,v94
v_mul_f32 v95,v39,v95
v_exp_f32 v92,v92
v_exp_f32 v93,v93
v_exp_f32 v94,v94
v_exp_f32 v95,v95
v_fma_f32 v92,v92,v119,v119
v_fma_f32 v93,v93,v119,v119
v_fma_f32 v94,v94,v119,v119
v_fma_f32 v95,v95,v119,v119
v_rcp_f32 v92,v92
v_rcp_f32 v93,v93
v_rcp_f32 v94,v94
v_rcp_f32 v95,v95
v_mul_f32 v36,v36,v92
v_mul_f32 v37,v37,v93
v_mul_f32 v38,v38,v94
v_mul_f32 v39,v39,v95
v_cvt_pk_bf16_f32 v100,v36,v37
v_cvt_pk_bf16_f32 v101,v38,v39
buffer_store_dwordx2 v[100:101],v9,s[36:39],s73 offen offset:0 sc0 sc1
s_cmp_ge_u32 s49,s59
s_cbranch_scc1 Lg3_done1_%=
s_add_u32 m0,s78,16384
s_nop 0
buffer_load_dword v6,s[36:39],s68 offen offset:0 sc1 lds
s_waitcnt vmcnt(4)
s_barrier
ds_read_b32 v13,v7 offset:8192
s_mul_i32 s70,s47,5
s_lshr_b32 s70,s70,6
buffer_store_dword v5,v4,s[36:39],s70 offen offset:0 sc0 sc1
s_mov_b32 s47,s59
s_mov_b32 s69,18432
s_waitcnt lgkmcnt(0)
v_cmp_eq_u32 vcc,s60,v13
s_nop 0
s_cmp_eq_u32 vcc_lo,-1
s_cbranch_scc1 Lg3_go1_%=
s_add_u32 m0,s78,16640
s_nop 0
buffer_load_dword v6,s[36:39],s68 offen offset:0 sc1 lds
s_waitcnt vmcnt(5)
s_barrier
ds_read_b32 v13,v7 offset:8448
s_mov_b32 s69,18688
s_waitcnt lgkmcnt(0)
v_cmp_eq_u32 vcc,s60,v13
s_nop 0
s_cmp_eq_u32 vcc_lo,-1
s_cbranch_scc1 Lg3_go1_%=
s_add_u32 m0,s78,8192
s_nop 0
buffer_load_dword v6,s[36:39],s68 offen offset:0 sc1 lds
s_waitcnt vmcnt(3)
s_barrier
ds_read_b32 v13,v7 offset:16384
s_mul_i32 s70,s48,5
s_lshr_b32 s70,s70,6
buffer_store_dword v5,v4,s[36:39],s70 offen offset:0 sc0 sc1
s_mov_b32 s48,s59
s_mov_b32 s69,10240
s_waitcnt lgkmcnt(0)
v_cmp_eq_u32 vcc,s60,v13
s_nop 0
s_cmp_eq_u32 vcc_lo,-1
s_cbranch_scc1 Lg3_go1_%=
s_add_u32 m0,s78,8448
s_nop 0
buffer_load_dword v6,s[36:39],s68 offen offset:0 sc1 lds
s_waitcnt vmcnt(3)
s_barrier
ds_read_b32 v13,v7 offset:16640
s_mov_b32 s69,10496
s_waitcnt lgkmcnt(0)
v_cmp_eq_u32 vcc,s60,v13
s_nop 0
s_cmp_eq_u32 vcc_lo,-1
s_cbranch_scc1 Lg3_go1_%=
Lg3_poll1_%=:
s_add_u32 m0,s78,16384
s_nop 0
buffer_load_dword v6,s[36:39],s68 offen offset:0 sc1 lds
s_waitcnt vmcnt(2)
s_barrier
ds_read_b32 v13,v7 offset:8192
s_mov_b32 s69,18432
s_waitcnt lgkmcnt(0))"
R"(
v_cmp_eq_u32 vcc,s60,v13
s_nop 0
s_cmp_eq_u32 vcc_lo,-1
s_cbranch_scc1 Lg3_go1_%=
s_sleep 4
s_add_u32 m0,s78,16640
s_nop 0
buffer_load_dword v6,s[36:39],s68 offen offset:0 sc1 lds
s_waitcnt vmcnt(2)
s_barrier
ds_read_b32 v13,v7 offset:8448
s_mov_b32 s69,18688
s_waitcnt lgkmcnt(0)
v_cmp_eq_u32 vcc,s60,v13
s_nop 0
s_cmp_eq_u32 vcc_lo,-1
s_cbranch_scc1 Lg3_go1_%=
s_sleep 4
s_add_u32 m0,s78,8192
s_nop 0
buffer_load_dword v6,s[36:39],s68 offen offset:0 sc1 lds
s_waitcnt vmcnt(2)
s_barrier
ds_read_b32 v13,v7 offset:16384
s_mov_b32 s69,10240
s_waitcnt lgkmcnt(0)
v_cmp_eq_u32 vcc,s60,v13
s_nop 0
s_cmp_eq_u32 vcc_lo,-1
s_cbranch_scc1 Lg3_go1_%=
s_sleep 4
s_add_u32 m0,s78,8448
s_nop 0
buffer_load_dword v6,s[36:39],s68 offen offset:0 sc1 lds
s_waitcnt vmcnt(2)
s_barrier
ds_read_b32 v13,v7 offset:16640
s_mov_b32 s69,10496
s_waitcnt lgkmcnt(0)
v_cmp_eq_u32 vcc,s60,v13
s_nop 0
s_cmp_eq_u32 vcc_lo,-1
s_cbranch_scc1 Lg3_go1_%=
s_sleep 4
s_add_u32 s79,s79,1
s_cmp_lt_u32 s79,20000
s_cbranch_scc1 Lg3_poll1_%=
Lg3_go1_%=:
s_xor_b32 s62,s64,65536
s_lshr_b32 s61,s49,0
s_mov_b32 m0,s62
s_nop 0
buffer_load_dwordx4 v0,s[36:39],s61 offen offset:0 sc1 lds
buffer_load_dwordx4 v0,s[36:39],s61 offen offset:1024 sc1 lds
buffer_load_dwordx4 v0,s[36:39],s61 offen offset:2048 sc1 lds
buffer_load_dwordx4 v0,s[36:39],s61 offen offset:3072 sc1 lds
s_add_u32 m0,s62,16384
s_nop 0
buffer_load_dwordx4 v1,s[36:39],s61 offen offset:0 sc1 lds
buffer_load_dwordx4 v1,s[36:39],s61 offen offset:1024 sc1 lds
buffer_load_dwordx4 v1,s[36:39],s61 offen offset:2048 sc1 lds
buffer_load_dwordx4 v1,s[36:39],s61 offen offset:3072 sc1 lds
s_lshr_b32 s72,s61,5
buffer_load_dword v108,v8,s[36:39],s72 offen offset:0 sc1
v_add_u32 v84,s69,v7
s_waitcnt vmcnt(5)
s_barrier
ds_read_b32 v13,v84 offset:0
ds_read_b128 v[52:55],v2 offset:32768
ds_read_b128 v[56:59],v2 offset:33792
ds_read_b128 v[60:63],v2 offset:34816
ds_read_b128 v[64:67],v2 offset:35840
ds_read_b128 v[68:71],v2 offset:36864
s_mul_i32 s70,s47,5
s_lshr_b32 s70,s70,6
buffer_store_dword v5,v4,s[36:39],s70 offen offset:0 sc0 sc1
s_mov_b32 s47,s59
s_mul_i32 s70,s48,5
s_lshr_b32 s70,s70,6
buffer_store_dword v5,v4,s[36:39],s70 offen offset:0 sc0 sc1
s_mov_b32 s48,s59
s_mul_i32 s70,s59,5
s_lshr_b32 s70,s70,6
buffer_store_dword v5,v4,s[36:39],s70 offen offset:0 sc0 sc1
s_waitcnt lgkmcnt(5)
v_cmp_eq_u32 vcc,s60,v13
s_nop 0
s_cmp_eq_u32 vcc_lo,-1
s_cselect_b32 s67,s50,s59
s_lshr_b32 s61,s67,0
s_xor_b32 s62,s63,65536
s_waitcnt lgkmcnt(3)
s_setpc_b64 s[92:93]
Lg3_gs1_%=:
s_xor_b32 s62,s64,65536
s_lshr_b32 s61,s49,0
s_cmp_eq_u32 s54,1
s_cselect_b32 s68,s50,s49
s_lshr_b32 s68,s68,5
s_mul_i32 s73,s48,7
s_lshr_b32 s73,s73,1
s_mov_b32 m0,s62
s_nop 0
buffer_load_dwordx4 v0,s[36:39],s61 offen offset:0 sc1 lds
v_mul_f32 v116,s75,v112
v_fma_f32 v113,v113,s75,v12
v_fma_f32 v113,-v116,v116,v113
v_max_f32 v113,v113,v12
v_rsq_f32 v112,v113
s_nop 0
v_mul_f32 v117,s76,v112
v_mul_f32 v118,v112,v112
v_mul_f32 v119,v113,v112
v_mul_f32 v118,s77,v118
v_mul_f32 v118,v118,v117
v_fma_f32 v28,-v116,v16,v28
buffer_load_dwordx4 v0,s[36:39],s61 offen offset:1024 sc1 lds
v_fma_f32 v29,-v116,v17,v29
v_fma_f32 v30,-v116,v18,v30
v_fma_f32 v31,-v116,v19,v31
v_mul_f32 v84,v28,v28
v_mul_f32 v85,v29,v29
v_mul_f32 v86,v30,v30
v_mul_f32 v87,v31,v31
v_fma_f32 v84,v118,v84,v117
v_fma_f32 v85,v118,v85,v117
v_fma_f32 v86,v118,v86,v117
v_fma_f32 v87,v118,v87,v117
buffer_load_dwordx4 v0,s[36:39],s61 offen offset:2048 sc1 lds
v_mul_f32 v84,v28,v84
v_mul_f32 v85,v29,v85
v_mul_f32 v86,v30,v86
v_mul_f32 v87,v31,v87
v_exp_f32 v84,v84
v_exp_f32 v85,v85
v_exp_f32 v86,v86
v_exp_f32 v87,v87
v_fma_f32 v84,v84,v119,v119
v_fma_f32 v85,v85,v119,v119
v_fma_f32 v86,v86,v119,v119
buffer_load_dwordx4 v0,s[36:39],s61 offen offset:3072 sc1 lds
v_fma_f32 v87,v87,v119,v119
v_rcp_f32 v84,v84
v_rcp_f32 v85,v85
v_rcp_f32 v86,v86
v_rcp_f32 v87,v87
v_mul_f32 v28,v28,v84
v_mul_f32 v29,v29,v85
v_mul_f32 v30,v30,v86
v_mul_f32 v31,v31,v87
v_cvt_pk_bf16_f32 v96,v28,v29
v_cvt_pk_bf16_f32 v97,v30,v31
s_add_u32 m0,s78,8192
s_nop 0
buffer_load_dword v6,s[36:39],s68 offen offset:0 sc1 lds
v_fma_f32 v32,-v116,v20,v32
v_fma_f32 v33,-v116,v21,v33
v_fma_f32 v34,-v116,v22,v34
v_fma_f32 v35,-v116,v23,v35
v_mul_f32 v88,v32,v32
v_mul_f32 v89,v33,v33
v_mul_f32 v90,v34,v34
v_mul_f32 v91,v35,v35
v_fma_f32 v88,v118,v88,v117
v_fma_f32 v89,v118,v89,v117
v_fma_f32 v90,v118,v90,v117
s_add_u32 m0,s62,16384
s_nop 0
buffer_load_dwordx4 v1,s[36:39],s61 offen offset:0 sc1 lds
v_fma_f32 v91,v118,v91,v117
v_mul_f32 v88,v32,v88
v_mul_f32 v89,v33,v89
v_mul_f32 v90,v34,v90
v_mul_f32 v91,v35,v91
v_exp_f32 v88,v88
v_exp_f32 v89,v89
v_exp_f32 v90,v90
v_exp_f32 v91,v91
v_fma_f32 v88,v88,v119,v119
v_fma_f32 v89,v89,v119,v119
buffer_load_dwordx4 v1,s[36:39],s61 offen offset:1024 sc1 lds
v_fma_f32 v90,v90,v119,v119
v_fma_f32 v91,v91,v119,v119
v_rcp_f32 v88,v88
v_rcp_f32 v89,v89
v_rcp_f32 v90,v90
v_rcp_f32 v91,v91
v_mul_f32 v32,v32,v88
v_mul_f32 v33,v33,v89
v_mul_f32 v34,v34,v90
v_mul_f32 v35,v35,v91
v_cvt_pk_bf16_f32 v98,v32,v33
buffer_load_dwordx4 v1,s[36:39],s61 offen offset:2048 sc1 lds
v_cvt_pk_bf16_f32 v99,v34,v35
buffer_store_dwordx4 v[96:99],v3,s[36:39],s73 offen offset:0 sc0 sc1
v_fma_f32 v36,-v116,v24,v36
v_fma_f32 v37,-v116,v25,v37
v_fma_f32 v38,-v116,v26,v38
v_fma_f32 v39,-v116,v27,v39
v_mul_f32 v92,v36,v36)"
R"(
v_mul_f32 v93,v37,v37
v_mul_f32 v94,v38,v38
v_mul_f32 v95,v39,v39
v_fma_f32 v92,v118,v92,v117
buffer_load_dwordx4 v1,s[36:39],s61 offen offset:3072 sc1 lds
v_fma_f32 v93,v118,v93,v117
v_fma_f32 v94,v118,v94,v117
v_fma_f32 v95,v118,v95,v117
v_mul_f32 v92,v36,v92
v_mul_f32 v93,v37,v93
v_mul_f32 v94,v38,v94
v_mul_f32 v95,v39,v95
v_exp_f32 v92,v92
v_exp_f32 v93,v93
v_exp_f32 v94,v94
v_exp_f32 v95,v95
s_lshr_b32 s72,s61,5
buffer_load_dword v108,v8,s[36:39],s72 offen offset:0 sc1
v_fma_f32 v92,v92,v119,v119
v_fma_f32 v93,v93,v119,v119
v_fma_f32 v94,v94,v119,v119
v_fma_f32 v95,v95,v119,v119
v_rcp_f32 v92,v92
v_rcp_f32 v93,v93
v_rcp_f32 v94,v94
v_rcp_f32 v95,v95
v_mul_f32 v36,v36,v92
v_mul_f32 v37,v37,v93
v_mul_f32 v38,v38,v94
v_mul_f32 v39,v39,v95
v_cvt_pk_bf16_f32 v100,v36,v37
v_cvt_pk_bf16_f32 v101,v38,v39
buffer_store_dwordx2 v[100:101],v9,s[36:39],s73 offen offset:0 sc0 sc1
s_waitcnt vmcnt(7)
s_barrier
ds_read_b32 v13,v7 offset:8192
ds_read_b128 v[52:55],v2 offset:32768
ds_read_b128 v[56:59],v2 offset:33792
ds_read_b128 v[60:63],v2 offset:34816
ds_read_b128 v[64:67],v2 offset:35840
ds_read_b128 v[68:71],v2 offset:36864
s_mul_i32 s70,s47,5
s_lshr_b32 s70,s70,6
buffer_store_dword v5,v4,s[36:39],s70 offen offset:0 sc0 sc1
s_waitcnt lgkmcnt(5)
v_cmp_eq_u32 vcc,s60,v13
s_nop 0
s_cmp_eq_u32 vcc_hi,-1
s_cselect_b32 s67,s50,s59
s_lshr_b32 s61,s67,0
s_xor_b32 s62,s63,65536
s_mov_b32 s47,s48
s_mov_b32 s48,s59
s_mul_i32 s70,s59,5
s_lshr_b32 s70,s70,6
buffer_store_dword v5,v4,s[36:39],s70 offen offset:0 sc0 sc1
s_waitcnt lgkmcnt(3)
s_setpc_b64 s[92:93]
Lg3_done1_%=:
s_waitcnt vmcnt(0)
s_barrier
s_mul_i32 s70,s47,5
s_lshr_b32 s70,s70,6
buffer_store_dword v5,v4,s[36:39],s70 offen offset:0 sc0 sc1
s_mul_i32 s70,s48,5
s_lshr_b32 s70,s70,6
buffer_store_dword v5,v4,s[36:39],s70 offen offset:0 sc0 sc1
s_branch Lg3_done_%=
Lg3_done_%=:
s_waitcnt vmcnt(0) lgkmcnt(0)
s_barrier
s_endpgm
Lcode_g3_e:)"
  :: "s"(ws), "s"(wave), "s"(sStep), "s"(sX1), "s"(sX2), "s"(sU0), "s"(sWw), "s"(sBuf), "v"(vSrc), "v"(vA), "v"(vOut), "v"(vFlag), "v"(vPollOff), "v"(vSnapRd), "v"(vAuxS), "v"(vOut2), "v"(vStW), "v"(vStR), "v"(vWd0), "v"(vWd1), "v"(vWr0), "v"(vWr1), "v"(vWr2), "s"(w3) : "v0", "v1", "v2", "v3", "v4", "v5", "v6", "v7", "v8", "v9", "v10", "v11", "v12", "v13", "v14", "v15", "v16", "v17", "v18", "v19", "v20", "v21", "v22", "v23", "v24", "v25", "v26", "v27", "v28", "v29", "v30", "v31", "v32", "v33", "v34", "v35", "v36", "v37", "v38", "v39", "v40", "v41", "v42", "v43", "v44", "v45", "v46", "v47", "v48", "v49", "v50", "v51", "v52", "v53", "v54", "v55", "v56", "v57", "v58", "v59", "v60", "v61", "v62", "v63", "v64", "v65", "v66", "v67", "v68", "v69", "v70", "v71", "v72", "v73", "v74", "v75", "v76", "v77", "v78", "v79", "v80", "v81", "v82", "v83", "v84", "v85", "v86", "v87", "v88", "v89", "v90", "v91", "v92", "v93", "v94", "v95", "v96", "v97", "v98", "v99", "v100", "v101", "v102", "v103", "v104", "v105", "v106", "v107", "v108", "v109", "v110", "v111", "v112", "v113", "v114", "v115", "v116", "v117", "v118", "v119", "v120", "v121", "v122", "v123", "v124", "v125", "v126", "v127", "v128", "v129", "v130", "v131", "v132", "v133", "v134", "v135", "v136", "v137", "v138", "v139", "v140", "v141", "v142", "v143", "v144", "v145", "v146", "v147", "v148", "v149", "v150", "v151", "v152", "v153", "v154", "v155", "v156", "v157", "v158", "v159", "v160", "v161", "v162", "v163", "v164", "v165", "v166", "v167", "v168", "v169", "v170", "v171", "v172", "v173", "v174", "v175", "v176", "v177", "v178", "v179", "v180", "v181", "v182", "v183", "v184", "v185", "v186", "v187", "v188", "v189", "v190", "v191", "v192", "v193", "v194", "v195", "v196", "v197", "v198", "v199", "v200", "v201", "v202", "v203", "v204", "v205", "v206", "v207", "v208", "v209", "v210", "v211", "v212", "v213", "v214", "v215", "v216", "v217", "v218", "v219", "v220", "v221", "v222", "v223", "v224", "v225", "v226", "v227", "v228", "v229", "v230", "v231", "v255", "a0", "a1", "a2", "a3", "a4", "a5", "a6", "a7", "a8", "a9", "a10", "a11", "a12", "a13", "a14", "a15", "a16", "a17", "a18", "a19", "a20", "a21", "a22", "a23", "a24", "a25", "a26", "a27", "a28", "a29", "a30", "a31", "a32", "a33", "a34", "a35", "a36", "a37", "a38", "a39", "a40", "a41", "a42", "a43", "a44", "a45", "a46", "a47", "a48", "a49", "a50", "a51", "a52", "a53", "a54", "a55", "a56", "a57", "a58", "a59", "a60", "a61", "a62", "a63", "a64", "a65", "a66", "a67", "a68", "a69", "a70", "a71", "a72", "a73", "a74", "a75", "a76", "a77", "a78", "a79", "a80", "a81", "a82", "a83", "a84", "a85", "a86", "a87", "a88", "a89", "a90", "a91", "a92", "a93", "a94", "a95", "a96", "a97", "a98", "a99", "a100", "a101", "a102", "a103", "a104", "a105", "a106", "a107", "a108", "a109", "a110", "a111", "a112", "a113", "a114", "a115", "a116", "a117", "a118", "a119", "a120", "a121", "a122", "a123", "a124", "a125", "a126", "a127", "a128", "a129", "a130", "a131", "a132", "a133", "a134", "a135", "a136", "a137", "a138", "a139", "a140", "a141", "a142", "a143", "a144", "a145", "a146", "a147", "a148", "a149", "a150", "a151", "a152", "a153", "a154", "a155", "a156", "a157", "a158", "a159", "a160", "a161", "a162", "a163", "a164", "a165", "a166", "a167", "a168", "a169", "a170", "a171", "a172", "a173", "a174", "a175", "a176", "a177", "a178", "a179", "a180", "a181", "a182", "a183", "a184", "a185", "a186", "a187", "a188", "a189", "a190", "a191", "a192", "a193", "a194", "a195", "a196", "a197", "a198", "a199", "a200", "a201", "a202", "a203", "a204", "a205", "a206", "a207", "a208", "a209", "a210", "a211", "a212", "a213", "a214", "a215", "a216", "a217", "a218", "a219", "a220", "a221", "a222", "a223", "a224", "a225", "a226", "a227", "a228", "a229", "a230", "a231", "a232", "a233", "a234", "a235", "a236", "a237", "a238", "a239", "a240", "a241", "a242", "a243", "a244", "a245", "a246", "a247", "a248", "a249", "a250", "a251", "a252", "a253", "a254", "a255", "s36", "s37", "s38", "s39", "s40", "s41", "s42", "s43", "s44", "s45", "s46", "s47", "s48", "s49", "s50", "s51", "s52", "s53", "s54", "s55", "s56", "s57", "s58", "s59", "s60", "s61", "s62", "s63", "s64", "s65", "s66", "s67", "s68", "s69", "s70", "s71", "s72", "s73", "s74", "s75", "s76", "s77", "s78", "s79", "s80", "s81", "s82", "s83", "s84", "s85", "s86", "s87", "s88", "s89", "s90", "s91", "s92", "s93", "s94", "s95", "s96", "s97", "s98", "s99", "vcc", "scc", "memory"); } else { asm volatile(R"(
Lcode_g3o_b:
s_mov_b64 s[36:37],%0
s_mov_b32 s38,122727552
s_mov_b32 s39,0x00020000
s_mov_b32 s54,%1
s_mov_b32 s55,%2
s_mov_b32 s56,%3
s_mov_b32 s58,%4
s_mov_b32 s83,%5
s_mov_b32 s82,%6
s_mov_b32 s84,%7
v_mov_b32 v0,%8
v_mov_b32 v2,%9
v_mov_b32 v3,%10
v_mov_b32 v4,%11
v_mov_b32 v6,%12
v_mov_b32 v7,%13
v_mov_b32 v8,%14
v_mov_b32 v9,%15
v_mov_b32 v10,%16
v_mov_b32 v11,%17
v_mov_b32 v24,%18
v_mov_b32 v25,%19
v_mov_b32 v26,%20
v_mov_b32 v27,%21
v_mov_b32 v28,%22
s_mov_b64 s[40:41],%23
s_mov_b32 s42,7340032
s_mov_b32 s43,0x00020000
s_lshl_b32 s88,s54,11
s_mov_b32 s69,122727424
buffer_load_dword v5,off,s[36:39],s69 sc1
s_add_u32 m0,s88,0
s_add_u32 s61,s82,0
buffer_load_dwordx4 v24,s[40:43],s61 offen offset:0  lds
s_add_u32 m0,s88,1024
s_nop 0
buffer_load_dwordx4 v25,s[40:43],s61 offen offset:0  lds
s_add_u32 m0,s88,8064
s_nop 0
buffer_load_dwordx4 v24,s[40:43],s61 offen offset:128  lds
s_add_u32 m0,s88,9088
s_nop 0
buffer_load_dwordx4 v25,s[40:43],s61 offen offset:128  lds
s_add_u32 m0,s88,16384
s_add_u32 s61,s82,458752
buffer_load_dwordx4 v24,s[40:43],s61 offen offset:0  lds
s_add_u32 m0,s88,17408
s_nop 0
buffer_load_dwordx4 v25,s[40:43],s61 offen offset:0  lds
s_add_u32 m0,s88,24448
s_nop 0
buffer_load_dwordx4 v24,s[40:43],s61 offen offset:128  lds
s_add_u32 m0,s88,25472
s_nop 0
buffer_load_dwordx4 v25,s[40:43],s61 offen offset:128  lds
s_add_u32 m0,s88,32768
s_add_u32 s61,s82,917504
buffer_load_dwordx4 v24,s[40:43],s61 offen offset:0  lds
s_add_u32 m0,s88,33792
s_nop 0
buffer_load_dwordx4 v25,s[40:43],s61 offen offset:0  lds
s_add_u32 m0,s88,40832
s_nop 0
buffer_load_dwordx4 v24,s[40:43],s61 offen offset:128  lds
s_add_u32 m0,s88,41856
s_nop 0
buffer_load_dwordx4 v25,s[40:43],s61 offen offset:128  lds
s_add_u32 m0,s88,49152
s_add_u32 s61,s82,1376256
buffer_load_dwordx4 v24,s[40:43],s61 offen offset:0  lds
s_add_u32 m0,s88,50176
s_nop 0
buffer_load_dwordx4 v25,s[40:43],s61 offen offset:0  lds
s_add_u32 m0,s88,57216
s_nop 0
buffer_load_dwordx4 v24,s[40:43],s61 offen offset:128  lds
s_add_u32 m0,s88,58240
s_nop 0
buffer_load_dwordx4 v25,s[40:43],s61 offen offset:128  lds
s_add_u32 m0,s88,65536
s_add_u32 s61,s82,1835008
buffer_load_dwordx4 v24,s[40:43],s61 offen offset:0  lds
s_add_u32 m0,s88,66560
s_nop 0
buffer_load_dwordx4 v25,s[40:43],s61 offen offset:0  lds
s_add_u32 m0,s88,73600
s_nop 0
buffer_load_dwordx4 v24,s[40:43],s61 offen offset:128  lds
s_add_u32 m0,s88,74624
s_nop 0
buffer_load_dwordx4 v25,s[40:43],s61 offen offset:128  lds
s_add_u32 m0,s88,81920
s_add_u32 s61,s82,2293760
buffer_load_dwordx4 v24,s[40:43],s61 offen offset:0  lds
s_add_u32 m0,s88,82944
s_nop 0
buffer_load_dwordx4 v25,s[40:43],s61 offen offset:0  lds
s_add_u32 m0,s88,89984
s_nop 0
buffer_load_dwordx4 v24,s[40:43],s61 offen offset:128  lds
s_add_u32 m0,s88,91008
s_nop 0
buffer_load_dwordx4 v25,s[40:43],s61 offen offset:128  lds
s_add_u32 m0,s88,98304
s_add_u32 s61,s82,2752512
buffer_load_dwordx4 v24,s[40:43],s61 offen offset:0  lds
s_add_u32 m0,s88,99328
s_nop 0
buffer_load_dwordx4 v25,s[40:43],s61 offen offset:0  lds
s_add_u32 m0,s88,106368
s_nop 0
buffer_load_dwordx4 v24,s[40:43],s61 offen offset:128  lds
s_add_u32 m0,s88,107392
s_nop 0
buffer_load_dwordx4 v25,s[40:43],s61 offen offset:128  lds
v_add_u32 v32,49152,v26
v_add_u32 v33,49152,v27
v_add_u32 v34,98304,v26
v_add_u32 v35,98304,v27
v_mov_b32 v72,0x3f803f80
v_mov_b32 v73,0x3f803f80
v_mov_b32 v74,0x3f803f80
v_mov_b32 v75,0x3f803f80
s_waitcnt vmcnt(24)
s_barrier
s_add_u32 m0,s88,114688
s_add_u32 s61,s82,3211264
buffer_load_dwordx4 v24,s[40:43],s61 offen offset:0  lds
s_add_u32 m0,s88,115712
ds_read_b64_tr_b16 a[0:1],v26 offset:0
buffer_load_dwordx4 v25,s[40:43],s61 offen offset:0  lds
s_add_u32 m0,s88,122752
ds_read_b64_tr_b16 a[2:3],v26 offset:512
buffer_load_dwordx4 v24,s[40:43],s61 offen offset:128  lds
s_add_u32 m0,s88,123776
ds_read_b64_tr_b16 a[4:5],v27 offset:0
buffer_load_dwordx4 v25,s[40:43],s61 offen offset:128  lds
ds_read_b64_tr_b16 a[6:7],v27 offset:512
ds_read_b64_tr_b16 a[8:9],v26 offset:4096
ds_read_b64_tr_b16 a[10:11],v26 offset:4608
ds_read_b64_tr_b16 a[12:13],v27 offset:4096
ds_read_b64_tr_b16 a[14:15],v27 offset:4608
s_waitcnt vmcnt(24)
s_barrier
s_add_u32 m0,s88,131072
s_add_u32 s61,s82,3670016
buffer_load_dwordx4 v24,s[40:43],s61 offen offset:0  lds
s_add_u32 m0,s88,132096
ds_read_b64_tr_b16 a[16:17],v26 offset:16384
buffer_load_dwordx4 v25,s[40:43],s61 offen offset:0  lds
s_add_u32 m0,s88,139136
ds_read_b64_tr_b16 a[18:19],v26 offset:16896
buffer_load_dwordx4 v24,s[40:43],s61 offen offset:128  lds
s_add_u32 m0,s88,140160
ds_read_b64_tr_b16 a[20:21],v27 offset:16384
buffer_load_dwordx4 v25,s[40:43],s61 offen offset:128  lds
ds_read_b64_tr_b16 a[22:23],v27 offset:16896
ds_read_b64_tr_b16 a[24:25],v26 offset:20480
ds_read_b64_tr_b16 a[26:27],v26 offset:20992
ds_read_b64_tr_b16 a[28:29],v27 offset:20480
ds_read_b64_tr_b16 a[30:31],v27 offset:20992
s_waitcnt lgkmcnt(8)
v_mfma_f32_16x16x32_bf16 v[16:19],a[0:3],v[72:75],0
v_mfma_f32_16x16x32_bf16 v[20:23],a[4:7],v[72:75],0
v_mfma_f32_16x16x32_bf16 v[16:19],a[8:11],v[72:75],v[16:19]
v_mfma_f32_16x16x32_bf16 v[20:23],a[12:15],v[72:75],v[20:23]
s_waitcnt vmcnt(24)
s_barrier
s_add_u32 m0,s88,0
s_add_u32 s61,s82,4128768
buffer_load_dwordx4 v24,s[40:43],s61 offen offset:0  lds
s_add_u32 m0,s88,1024
ds_read_b64_tr_b16 a[32:33],v26 offset:32768
buffer_load_dwordx4 v25,s[40:43],s61 offen offset:0  lds
s_add_u32 m0,s88,8064
ds_read_b64_tr_b16 a[34:35],v26 offset:33280
buffer_load_dwordx4 v24,s[40:43],s61 offen offset:128  lds
s_add_u32 m0,s88,9088
ds_read_b64_tr_b16 a[36:37],v27 offset:32768
buffer_load_dwordx4 v25,s[40:43],s61 offen offset:128  lds
ds_read_b64_tr_b16 a[38:39],v27 offset:33280
ds_read_b64_tr_b16 a[40:41],v26 offset:36864
ds_read_b64_tr_b16 a[42:43],v26 offset:37376
ds_read_b64_tr_b16 a[44:45],v27 offset:36864
ds_read_b64_tr_b16 a[46:47],v27 offset:37376
s_waitcnt lgkmcnt(8)
v_mfma_f32_16x16x32_bf16 v[16:19],a[16:19],v[72:75],v[16:19]
v_mfma_f32_16x16x32_bf16 v[20:23],a[20:23],v[72:75],v[20:23]
v_mfma_f32_16x16x32_bf16 v[16:19],a[24:27],v[72:75],v[16:19]
v_mfma_f32_16x16x32_bf16 v[20:23],a[28:31],v[72:75],v[20:23]
s_waitcnt vmcnt(24)
s_barrier
s_add_u32 m0,s88,16384
s_add_u32 s61,s82,4587520
buffer_load_dwordx4 v24,s[40:43],s61 offen offset:0  lds
s_add_u32 m0,s88,17408
ds_read_b64_tr_b16 a[48:49],v32 offset:0
buffer_load_dwordx4 v25,s[40:43],s61 offen offset:0  lds
s_add_u32 m0,s88,24448)"
R"(
ds_read_b64_tr_b16 a[50:51],v32 offset:512
buffer_load_dwordx4 v24,s[40:43],s61 offen offset:128  lds
s_add_u32 m0,s88,25472
ds_read_b64_tr_b16 a[52:53],v33 offset:0
buffer_load_dwordx4 v25,s[40:43],s61 offen offset:128  lds
ds_read_b64_tr_b16 a[54:55],v33 offset:512
ds_read_b64_tr_b16 a[56:57],v32 offset:4096
ds_read_b64_tr_b16 a[58:59],v32 offset:4608
ds_read_b64_tr_b16 a[60:61],v33 offset:4096
ds_read_b64_tr_b16 a[62:63],v33 offset:4608
s_waitcnt lgkmcnt(8)
v_mfma_f32_16x16x32_bf16 v[16:19],a[32:35],v[72:75],v[16:19]
v_mfma_f32_16x16x32_bf16 v[20:23],a[36:39],v[72:75],v[20:23]
v_mfma_f32_16x16x32_bf16 v[16:19],a[40:43],v[72:75],v[16:19]
v_mfma_f32_16x16x32_bf16 v[20:23],a[44:47],v[72:75],v[20:23]
s_waitcnt vmcnt(24)
s_barrier
s_add_u32 m0,s88,32768
s_add_u32 s61,s82,5046272
buffer_load_dwordx4 v24,s[40:43],s61 offen offset:0  lds
s_add_u32 m0,s88,33792
ds_read_b64_tr_b16 a[64:65],v32 offset:16384
buffer_load_dwordx4 v25,s[40:43],s61 offen offset:0  lds
s_add_u32 m0,s88,40832
ds_read_b64_tr_b16 a[66:67],v32 offset:16896
buffer_load_dwordx4 v24,s[40:43],s61 offen offset:128  lds
s_add_u32 m0,s88,41856
ds_read_b64_tr_b16 a[68:69],v33 offset:16384
buffer_load_dwordx4 v25,s[40:43],s61 offen offset:128  lds
ds_read_b64_tr_b16 a[70:71],v33 offset:16896
ds_read_b64_tr_b16 a[72:73],v32 offset:20480
ds_read_b64_tr_b16 a[74:75],v32 offset:20992
ds_read_b64_tr_b16 a[76:77],v33 offset:20480
ds_read_b64_tr_b16 a[78:79],v33 offset:20992
s_waitcnt lgkmcnt(8)
v_mfma_f32_16x16x32_bf16 v[16:19],a[48:51],v[72:75],v[16:19]
v_mfma_f32_16x16x32_bf16 v[20:23],a[52:55],v[72:75],v[20:23]
v_mfma_f32_16x16x32_bf16 v[16:19],a[56:59],v[72:75],v[16:19]
v_mfma_f32_16x16x32_bf16 v[20:23],a[60:63],v[72:75],v[20:23]
s_waitcnt vmcnt(24)
s_barrier
s_add_u32 m0,s88,49152
s_add_u32 s61,s82,5505024
buffer_load_dwordx4 v24,s[40:43],s61 offen offset:0  lds
s_add_u32 m0,s88,50176
ds_read_b64_tr_b16 a[80:81],v32 offset:32768
buffer_load_dwordx4 v25,s[40:43],s61 offen offset:0  lds
s_add_u32 m0,s88,57216
ds_read_b64_tr_b16 a[82:83],v32 offset:33280
buffer_load_dwordx4 v24,s[40:43],s61 offen offset:128  lds
s_add_u32 m0,s88,58240
ds_read_b64_tr_b16 a[84:85],v33 offset:32768
buffer_load_dwordx4 v25,s[40:43],s61 offen offset:128  lds
ds_read_b64_tr_b16 a[86:87],v33 offset:33280
ds_read_b64_tr_b16 a[88:89],v32 offset:36864
ds_read_b64_tr_b16 a[90:91],v32 offset:37376
ds_read_b64_tr_b16 a[92:93],v33 offset:36864
ds_read_b64_tr_b16 a[94:95],v33 offset:37376
s_waitcnt lgkmcnt(8)
v_mfma_f32_16x16x32_bf16 v[16:19],a[64:67],v[72:75],v[16:19]
v_mfma_f32_16x16x32_bf16 v[20:23],a[68:71],v[72:75],v[20:23]
v_mfma_f32_16x16x32_bf16 v[16:19],a[72:75],v[72:75],v[16:19]
v_mfma_f32_16x16x32_bf16 v[20:23],a[76:79],v[72:75],v[20:23]
s_waitcnt vmcnt(24)
s_barrier
s_add_u32 m0,s88,65536
s_add_u32 s61,s82,5963776
buffer_load_dwordx4 v24,s[40:43],s61 offen offset:0  lds
s_add_u32 m0,s88,66560
ds_read_b64_tr_b16 a[96:97],v34 offset:0
buffer_load_dwordx4 v25,s[40:43],s61 offen offset:0  lds
s_add_u32 m0,s88,73600
ds_read_b64_tr_b16 a[98:99],v34 offset:512
buffer_load_dwordx4 v24,s[40:43],s61 offen offset:128  lds
s_add_u32 m0,s88,74624
ds_read_b64_tr_b16 a[100:101],v35 offset:0
buffer_load_dwordx4 v25,s[40:43],s61 offen offset:128  lds
ds_read_b64_tr_b16 a[102:103],v35 offset:512
ds_read_b64_tr_b16 a[104:105],v34 offset:4096
ds_read_b64_tr_b16 a[106:107],v34 offset:4608
ds_read_b64_tr_b16 a[108:109],v35 offset:4096
ds_read_b64_tr_b16 a[110:111],v35 offset:4608
s_waitcnt lgkmcnt(8)
v_mfma_f32_16x16x32_bf16 v[16:19],a[80:83],v[72:75],v[16:19]
v_mfma_f32_16x16x32_bf16 v[20:23],a[84:87],v[72:75],v[20:23]
v_mfma_f32_16x16x32_bf16 v[16:19],a[88:91],v[72:75],v[16:19]
v_mfma_f32_16x16x32_bf16 v[20:23],a[92:95],v[72:75],v[20:23]
s_waitcnt vmcnt(24)
s_barrier
s_add_u32 m0,s88,81920
s_add_u32 s61,s82,6422528
buffer_load_dwordx4 v24,s[40:43],s61 offen offset:0  lds
s_add_u32 m0,s88,82944
ds_read_b64_tr_b16 a[112:113],v34 offset:16384
buffer_load_dwordx4 v25,s[40:43],s61 offen offset:0  lds
s_add_u32 m0,s88,89984
ds_read_b64_tr_b16 a[114:115],v34 offset:16896
buffer_load_dwordx4 v24,s[40:43],s61 offen offset:128  lds
s_add_u32 m0,s88,91008
ds_read_b64_tr_b16 a[116:117],v35 offset:16384
buffer_load_dwordx4 v25,s[40:43],s61 offen offset:128  lds
ds_read_b64_tr_b16 a[118:119],v35 offset:16896
ds_read_b64_tr_b16 a[120:121],v34 offset:20480
ds_read_b64_tr_b16 a[122:123],v34 offset:20992
ds_read_b64_tr_b16 a[124:125],v35 offset:20480
ds_read_b64_tr_b16 a[126:127],v35 offset:20992
s_waitcnt lgkmcnt(8)
v_mfma_f32_16x16x32_bf16 v[16:19],a[96:99],v[72:75],v[16:19]
v_mfma_f32_16x16x32_bf16 v[20:23],a[100:103],v[72:75],v[20:23]
v_mfma_f32_16x16x32_bf16 v[16:19],a[104:107],v[72:75],v[16:19]
v_mfma_f32_16x16x32_bf16 v[20:23],a[108:111],v[72:75],v[20:23]
s_waitcnt vmcnt(24)
s_barrier
s_add_u32 m0,s88,98304
s_add_u32 s61,s82,6881280
buffer_load_dwordx4 v24,s[40:43],s61 offen offset:0  lds
s_add_u32 m0,s88,99328
ds_read_b64_tr_b16 a[128:129],v34 offset:32768
buffer_load_dwordx4 v25,s[40:43],s61 offen offset:0  lds
s_add_u32 m0,s88,106368
ds_read_b64_tr_b16 a[130:131],v34 offset:33280
buffer_load_dwordx4 v24,s[40:43],s61 offen offset:128  lds
s_add_u32 m0,s88,107392
ds_read_b64_tr_b16 a[132:133],v35 offset:32768
buffer_load_dwordx4 v25,s[40:43],s61 offen offset:128  lds
ds_read_b64_tr_b16 a[134:135],v35 offset:33280
ds_read_b64_tr_b16 a[136:137],v34 offset:36864
ds_read_b64_tr_b16 a[138:139],v34 offset:37376
ds_read_b64_tr_b16 a[140:141],v35 offset:36864
ds_read_b64_tr_b16 a[142:143],v35 offset:37376
s_waitcnt lgkmcnt(8)
v_mfma_f32_16x16x32_bf16 v[16:19],a[112:115],v[72:75],v[16:19]
v_mfma_f32_16x16x32_bf16 v[20:23],a[116:119],v[72:75],v[20:23]
v_mfma_f32_16x16x32_bf16 v[16:19],a[120:123],v[72:75],v[16:19]
v_mfma_f32_16x16x32_bf16 v[20:23],a[124:127],v[72:75],v[20:23]
s_waitcnt vmcnt(24)
s_barrier
ds_read_b64_tr_b16 a[144:145],v26 offset:0
ds_read_b64_tr_b16 a[146:147],v26 offset:512
ds_read_b64_tr_b16 a[148:149],v27 offset:0
ds_read_b64_tr_b16 a[150:151],v27 offset:512
ds_read_b64_tr_b16 a[152:153],v26 offset:4096
ds_read_b64_tr_b16 a[154:155],v26 offset:4608
ds_read_b64_tr_b16 a[156:157],v27 offset:4096
ds_read_b64_tr_b16 a[158:159],v27 offset:4608
s_waitcnt lgkmcnt(8)
v_mfma_f32_16x16x32_bf16 v[16:19],a[128:131],v[72:75],v[16:19]
v_mfma_f32_16x16x32_bf16 v[20:23],a[132:135],v[72:75],v[20:23]
v_mfma_f32_16x16x32_bf16 v[16:19],a[136:139],v[72:75],v[16:19]
v_mfma_f32_16x16x32_bf16 v[20:23],a[140:143],v[72:75],v[20:23]
s_waitcnt vmcnt(20)
s_barrier
ds_read_b64_tr_b16 a[160:161],v26 offset:16384
ds_read_b64_tr_b16 a[162:163],v26 offset:16896
ds_read_b64_tr_b16 a[164:165],v27 offset:16384
ds_read_b64_tr_b16 a[166:167],v27 offset:16896
ds_read_b64_tr_b16 a[168:169],v26 offset:20480
ds_read_b64_tr_b16 a[170:171],v26 offset:20992
ds_read_b64_tr_b16 a[172:173],v27 offset:20480
ds_read_b64_tr_b16 a[174:175],v27 offset:20992
s_waitcnt lgkmcnt(8)
v_mfma_f32_16x16x32_bf16 v[16:19],a[144:147],v[72:75],v[16:19]
v_mfma_f32_16x16x32_bf16 v[20:23],a[148:151],v[72:75],v[20:23]
v_mfma_f32_16x16x32_bf16 v[16:19],a[152:155],v[72:75],v[16:19]
v_mfma_f32_16x16x32_bf16 v[20:23],a[156:159],v[72:75],v[20:23]
s_waitcnt vmcnt(16)
s_barrier
ds_read_b64_tr_b16 a[176:177],v26 offset:32768
ds_read_b64_tr_b16 a[178:179],v26 offset:33280
ds_read_b64_tr_b16 a[180:181],v27 offset:32768
ds_read_b64_tr_b16 a[182:183],v27 offset:33280
ds_read_b64_tr_b16 a[184:185],v26 offset:36864
ds_read_b64_tr_b16 a[186:187],v26 offset:37376
ds_read_b64_tr_b16 a[188:189],v27 offset:36864
ds_read_b64_tr_b16 a[190:191],v27 offset:37376
s_getpc_b64 s[46:47]
Lg3o_cp_%=:
s_add_u32 s46,s46,Lg3o_init_%=-Lg3o_cp_%=
s_addc_u32 s47,s47,0
s_lshl_b32 s68,s54,13
v_lshl_add_u32 v56,v2,3,s68
v_min_u32 v57,Lg3o_done_%=-Lg3o_init_%=,v56
global_load_dword v60,v57,s[46:47]
s_waitcnt lgkmcnt(8)
v_mfma_f32_16x16x32_bf16 v[16:19],a[160:163],v[72:75],v[16:19]
v_mfma_f32_16x16x32_bf16 v[20:23],a[164:167],v[72:75],v[20:23]
v_mfma_f32_16x16x32_bf16 v[16:19],a[168:171],v[72:75],v[16:19]
v_mfma_f32_16x16x32_bf16 v[20:23],a[172:175],v[72:75],v[20:23]
s_waitcnt vmcnt(13)
s_barrier
ds_read_b64_tr_b16 a[192:193],v32 offset:0
ds_read_b64_tr_b16 a[194:195],v32 offset:512
ds_read_b64_tr_b16 a[196:197],v33 offset:0
ds_read_b64_tr_b16 a[198:199],v33 offset:512
ds_read_b64_tr_b16 a[200:201],v32 offset:4096
ds_read_b64_tr_b16 a[202:203],v32 offset:4608
ds_read_b64_tr_b16 a[204:205],v33 offset:4096
ds_read_b64_tr_b16 a[206:207],v33 offset:4608
s_waitcnt lgkmcnt(8)
v_mfma_f32_16x16x32_bf16 v[16:19],a[176:179],v[72:75],v[16:19])"
R"(
v_mfma_f32_16x16x32_bf16 v[20:23],a[180:183],v[72:75],v[20:23]
v_mfma_f32_16x16x32_bf16 v[16:19],a[184:187],v[72:75],v[16:19]
v_mfma_f32_16x16x32_bf16 v[20:23],a[188:191],v[72:75],v[20:23]
s_waitcnt vmcnt(9)
s_barrier
ds_read_b64_tr_b16 a[208:209],v32 offset:16384
ds_read_b64_tr_b16 a[210:211],v32 offset:16896
ds_read_b64_tr_b16 a[212:213],v33 offset:16384
ds_read_b64_tr_b16 a[214:215],v33 offset:16896
ds_read_b64_tr_b16 a[216:217],v32 offset:20480
ds_read_b64_tr_b16 a[218:219],v32 offset:20992
ds_read_b64_tr_b16 a[220:221],v33 offset:20480
ds_read_b64_tr_b16 a[222:223],v33 offset:20992
s_waitcnt lgkmcnt(8)
v_mfma_f32_16x16x32_bf16 v[16:19],a[192:195],v[72:75],v[16:19]
v_mfma_f32_16x16x32_bf16 v[20:23],a[196:199],v[72:75],v[20:23]
v_mfma_f32_16x16x32_bf16 v[16:19],a[200:203],v[72:75],v[16:19]
v_mfma_f32_16x16x32_bf16 v[20:23],a[204:207],v[72:75],v[20:23]
s_waitcnt vmcnt(5)
s_barrier
ds_read_b64_tr_b16 a[224:225],v32 offset:32768
ds_read_b64_tr_b16 a[226:227],v32 offset:33280
ds_read_b64_tr_b16 a[228:229],v33 offset:32768
ds_read_b64_tr_b16 a[230:231],v33 offset:33280
ds_read_b64_tr_b16 a[232:233],v32 offset:36864
ds_read_b64_tr_b16 a[234:235],v32 offset:37376
ds_read_b64_tr_b16 a[236:237],v33 offset:36864
ds_read_b64_tr_b16 a[238:239],v33 offset:37376
s_waitcnt lgkmcnt(8)
v_mfma_f32_16x16x32_bf16 v[16:19],a[208:211],v[72:75],v[16:19]
v_mfma_f32_16x16x32_bf16 v[20:23],a[212:215],v[72:75],v[20:23]
v_mfma_f32_16x16x32_bf16 v[16:19],a[216:219],v[72:75],v[16:19]
v_mfma_f32_16x16x32_bf16 v[20:23],a[220:223],v[72:75],v[20:23]
s_waitcnt vmcnt(1)
s_barrier
ds_read_b64_tr_b16 a[240:241],v34 offset:0
ds_read_b64_tr_b16 a[242:243],v34 offset:512
ds_read_b64_tr_b16 a[244:245],v35 offset:0
ds_read_b64_tr_b16 a[246:247],v35 offset:512
ds_read_b64_tr_b16 a[248:249],v34 offset:4096
ds_read_b64_tr_b16 a[250:251],v34 offset:4608
ds_read_b64_tr_b16 a[252:253],v35 offset:4096
ds_read_b64_tr_b16 a[254:255],v35 offset:4608
s_waitcnt lgkmcnt(8)
v_mfma_f32_16x16x32_bf16 v[16:19],a[224:227],v[72:75],v[16:19]
v_mfma_f32_16x16x32_bf16 v[20:23],a[228:231],v[72:75],v[20:23]
v_mfma_f32_16x16x32_bf16 v[16:19],a[232:235],v[72:75],v[16:19]
v_mfma_f32_16x16x32_bf16 v[20:23],a[236:239],v[72:75],v[20:23]
s_waitcnt lgkmcnt(0)
v_mfma_f32_16x16x32_bf16 v[16:19],a[240:243],v[72:75],v[16:19]
v_mfma_f32_16x16x32_bf16 v[20:23],a[244:247],v[72:75],v[20:23]
v_mfma_f32_16x16x32_bf16 v[16:19],a[248:251],v[72:75],v[16:19]
v_mfma_f32_16x16x32_bf16 v[20:23],a[252:255],v[72:75],v[20:23]
s_waitcnt lgkmcnt(0)
s_barrier
v_readfirstlane_b32 s60,v5
s_nop 3
s_add_u32 s60,s60,1
s_cmp_eq_u32 s60,0
s_cselect_b32 s60,1,s60
Lg3o_init_%=:
v_mov_b32 v5,s60
s_mov_b32 s59,536870912
s_add_u32 s57,s56,131072
s_mov_b32 s79,0
s_mov_b32 s75,0x3a800000
s_mov_b32 s76,0xc0135761
s_mov_b32 s77,0x3d372713
s_mov_b32 s44,s59
s_mov_b32 s45,s59
s_mov_b32 s46,s59
s_mov_b32 s47,s59
s_mov_b32 s48,s83
s_add_u32 s49,s48,s55
s_cmp_eq_u32 s49,s56
s_cselect_b32 s49,s57,s49
s_cmp_eq_u32 s49,s58
s_cselect_b32 s49,s59,s49
s_add_u32 s50,s49,s55
s_cmp_eq_u32 s50,s56
s_cselect_b32 s50,s57,s50
s_cmp_eq_u32 s50,s58
s_cselect_b32 s50,s59,s50
s_add_u32 s51,s50,s55
s_cmp_eq_u32 s51,s56
s_cselect_b32 s51,s57,s51
s_cmp_eq_u32 s51,s58
s_cselect_b32 s51,s59,s51
s_add_u32 s52,s51,s55
s_cmp_eq_u32 s52,s56
s_cselect_b32 s52,s57,s52
s_cmp_eq_u32 s52,s58
s_cselect_b32 s52,s59,s52
s_lshl_b32 s63,s54,12
s_add_u32 s63,s63,65536
s_add_u32 s64,s63,32768
s_lshl_b32 s88,s54,10
s_add_u32 s88,s88,131072
s_lshl_b32 s78,s54,11
s_add_u32 s78,s78,132608
v_add_u32 v1,16384,v0
v_mov_b32 v12,0x3727c5ac
s_getpc_b64 s[90:91]
Lg3o_pc_%=:
s_mov_b32 s68,Lg3o_d1w0_%=-Lg3o_pc_%=
s_cmp_eq_u32 s54,1
s_cselect_b32 s68,Lg3o_d1w1_%=-Lg3o_pc_%=,s68
s_cmp_eq_u32 s54,2
s_cselect_b32 s68,Lg3o_d1w2_%=-Lg3o_pc_%=,s68
s_cmp_eq_u32 s54,3
s_cselect_b32 s68,Lg3o_d1w3_%=-Lg3o_pc_%=,s68
s_add_u32 s92,s90,s68
s_addc_u32 s93,s91,0
s_mov_b32 s68,Lg3o_d0w0_%=-Lg3o_pc_%=
s_cmp_eq_u32 s54,1
s_cselect_b32 s68,Lg3o_d0w1_%=-Lg3o_pc_%=,s68
s_cmp_eq_u32 s54,2
s_cselect_b32 s68,Lg3o_d0w2_%=-Lg3o_pc_%=,s68
s_cmp_eq_u32 s54,3
s_cselect_b32 s68,Lg3o_d0w3_%=-Lg3o_pc_%=,s68
s_add_u32 s90,s90,s68
s_addc_u32 s91,s91,0
s_mov_b32 s66,s59
s_mov_b32 s65,0
s_mov_b32 s62,s63
s_branch Lg3o_slow0_%=
Lg3o_d0w0_%=:
v_mfma_f32_16x16x32_bf16 v[24:27],a[0:3],v[40:43],0
s_add_u32 m0,s62,16384
ds_read_b128 v[68:71],v2 offset:7168
v_mul_f32 v96,s75,v92
v_mfma_f32_16x16x32_bf16 v[28:31],a[4:7],v[40:43],0
buffer_load_dwordx4 v1,s[36:39],s66 offen offset:0 sc1 lds
v_mfma_f32_16x16x32_bf16 v[24:27],a[8:11],v[44:47],v[24:27]
s_mul_i32 s73,s47,7
ds_read_b128 v[40:43],v2 offset:8192
v_fma_f32 v93,v93,s75,v12
v_mfma_f32_16x16x32_bf16 v[28:31],a[12:15],v[44:47],v[28:31]
s_lshr_b32 s73,s73,1
v_fma_f32 v93,-v96,v96,v93
v_max_f32 v93,v93,v12
v_mfma_f32_16x16x32_bf16 v[24:27],a[16:19],v[48:51],v[24:27]
s_lshr_b32 s72,s66,5
ds_read_b128 v[44:47],v2 offset:9216
v_fma_f32 v32,-v96,v16,v32
v_mfma_f32_16x16x32_bf16 v[28:31],a[20:23],v[48:51],v[28:31]
buffer_load_dwordx4 v1,s[36:39],s66 offen offset:1024 sc1 lds
v_mfma_f32_16x16x32_bf16 v[24:27],a[24:27],v[52:55],v[24:27]
s_lshr_b32 s71,s62,2
ds_read_b128 v[48:51],v2 offset:10240
v_fma_f32 v33,-v96,v17,v33
v_mfma_f32_16x16x32_bf16 v[28:31],a[28:31],v[52:55],v[28:31]
s_waitcnt lgkmcnt(3)
v_rsq_f32 v92,v93
v_mfma_f32_16x16x32_bf16 v[24:27],a[32:35],v[56:59],v[24:27]
ds_read_b128 v[52:55],v2 offset:11264
v_mul_f32 v97,s76,v92
v_mfma_f32_16x16x32_bf16 v[28:31],a[36:39],v[56:59],v[28:31]
buffer_load_dwordx4 v1,s[36:39],s66 offen offset:2048 sc1 lds
v_mfma_f32_16x16x32_bf16 v[24:27],a[40:43],v[60:63],v[24:27]
ds_read_b128 v[56:59],v2 offset:12288
v_mul_f32 v98,v92,v92
v_mfma_f32_16x16x32_bf16 v[28:31],a[44:47],v[60:63],v[28:31]
v_mul_f32 v99,v93,v92
v_mul_f32 v98,s77,v98
v_mfma_f32_16x16x32_bf16 v[24:27],a[48:51],v[64:67],v[24:27]
ds_read_b128 v[60:63],v2 offset:13312
v_mul_f32 v98,v98,v97
v_mfma_f32_16x16x32_bf16 v[28:31],a[52:55],v[64:67],v[28:31]
buffer_load_dwordx4 v1,s[36:39],s66 offen offset:3072 sc1 lds
v_mfma_f32_16x16x32_bf16 v[24:27],a[56:59],v[68:71],v[24:27]
s_add_u32 m0,s71,s88
ds_read_b128 v[64:67],v2 offset:14336
v_fma_f32 v34,-v96,v18,v34
v_mfma_f32_16x16x32_bf16 v[28:31],a[60:63],v[68:71],v[28:31]
s_waitcnt lgkmcnt(3)
buffer_load_dword v9,s[36:39],s72 offen offset:0 sc1 lds
v_mfma_f32_16x16x32_bf16 v[24:27],a[64:67],v[40:43],v[24:27]
s_waitcnt vmcnt(11)
ds_read_b128 v[68:71],v2 offset:15360
v_fma_f32 v35,-v96,v19,v35
v_mfma_f32_16x16x32_bf16 v[28:31],a[68:71],v[40:43],v[28:31]
s_barrier
v_mfma_f32_16x16x32_bf16 v[24:27],a[72:75],v[44:47],v[24:27]
s_xor_b32 s87,s64,65536
ds_read_b128 v[40:43],v2 offset:16384
ds_read_b32 v88,v8 offset:0
v_mfma_f32_16x16x32_bf16 v[28:31],a[76:79],v[44:47],v[28:31]
s_cmp_ge_u32 s66,s59
v_mul_f32 v72,v32,v32
v_mul_f32 v73,v33,v33
v_mfma_f32_16x16x32_bf16 v[24:27],a[80:83],v[48:51],v[24:27]
s_cselect_b32 s86,s49,s50
ds_read_b128 v[44:47],v2 offset:17408
v_mul_f32 v74,v34,v34
v_mfma_f32_16x16x32_bf16 v[28:31],a[84:87],v[48:51],v[28:31]
s_cselect_b32 s87,s87,s63
v_mul_f32 v75,v35,v35
v_fma_f32 v72,v98,v72,v97
v_mfma_f32_16x16x32_bf16 v[24:27],a[88:91],v[52:55],v[24:27])"
R"(
s_lshr_b32 s68,s50,5
ds_read_b128 v[48:51],v2 offset:18432
v_mul_f32 v72,v32,v72
v_mfma_f32_16x16x32_bf16 v[28:31],a[92:95],v[52:55],v[28:31]
s_waitcnt lgkmcnt(4)
v_exp_f32 v72,v72
v_mfma_f32_16x16x32_bf16 v[24:27],a[96:99],v[56:59],v[24:27]
s_waitcnt vmcnt(10) lgkmcnt(2)
ds_read_b128 v[52:55],v2 offset:19456
v_fma_f32 v72,v72,v99,v99
v_mfma_f32_16x16x32_bf16 v[28:31],a[100:103],v[56:59],v[28:31]
s_barrier
v_mfma_f32_16x16x32_bf16 v[24:27],a[104:107],v[60:63],v[24:27]
s_lshr_b32 s70,s51,5
ds_read_b128 v[56:59],v2 offset:20480
ds_read_b32 v13,v7 offset:0
v_mfma_f32_16x16x32_bf16 v[28:31],a[108:111],v[60:63],v[28:31]
s_mov_b32 s62,s87
v_rcp_f32 v72,v72
v_mfma_f32_16x16x32_bf16 v[24:27],a[112:115],v[64:67],v[24:27]
ds_read_b128 v[60:63],v2 offset:21504
v_add_f32_dpp v88,v88,v88 quad_perm:[1,0,3,2] row_mask:0xf bank_mask:0xf
v_mfma_f32_16x16x32_bf16 v[28:31],a[116:119],v[64:67],v[28:31]
s_nop 0
v_add_f32_dpp v88,v88,v88 quad_perm:[2,3,0,1] row_mask:0xf bank_mask:0xf
v_fma_f32 v73,v98,v73,v97
v_mfma_f32_16x16x32_bf16 v[24:27],a[120:123],v[68:71],v[24:27]
s_waitcnt lgkmcnt(1)
ds_read_b128 v[64:67],v2 offset:22528
v_mul_f32 v73,v33,v73
v_mfma_f32_16x16x32_bf16 v[28:31],a[124:127],v[68:71],v[28:31]
v_cmp_eq_u32 vcc,s60,v13
v_exp_f32 v73,v73
v_add_f32_dpp v88,v88,v88 row_half_mirror row_mask:0xf bank_mask:0xf
v_mfma_f32_16x16x32_bf16 v[24:27],a[128:131],v[40:43],v[24:27]
s_cmp_eq_u32 vcc_lo,-1
ds_write_b32 v10,v88 offset:0
v_mfma_f32_16x16x32_bf16 v[28:31],a[132:135],v[40:43],v[28:31]
s_cselect_b32 s67,s86,s59
s_cmp_ge_u32 s67,s59
s_cselect_b32 s68,s68,s70
ds_read_b128 v[68:71],v2 offset:23552
s_add_u32 m0,s78,256
v_mfma_f32_16x16x32_bf16 v[24:27],a[136:139],v[44:47],v[24:27]
buffer_load_dword v6,s[36:39],s68 offen offset:0 sc1 lds
v_mfma_f32_16x16x32_bf16 v[28:31],a[140:143],v[44:47],v[28:31]
s_mov_b32 m0,s62
ds_read_b128 v[40:43],v2 offset:24576
v_fma_f32 v73,v73,v99,v99
v_mfma_f32_16x16x32_bf16 v[24:27],a[144:147],v[48:51],v[24:27]
buffer_load_dwordx4 v0,s[36:39],s67 offen offset:0 sc1 lds
v_mfma_f32_16x16x32_bf16 v[28:31],a[148:151],v[48:51],v[28:31]
s_xor_b32 s63,s63,65536
ds_read_b128 v[44:47],v2 offset:25600
v_fma_f32 v74,v98,v74,v97
v_mfma_f32_16x16x32_bf16 v[24:27],a[152:155],v[52:55],v[24:27]
s_add_u32 s53,s52,s55
ds_read_b128 v[48:51],v2 offset:26624
v_mul_f32 v74,v34,v74
v_mfma_f32_16x16x32_bf16 v[28:31],a[156:159],v[52:55],v[28:31]
s_waitcnt lgkmcnt(3)
v_exp_f32 v74,v74
v_mfma_f32_16x16x32_bf16 v[24:27],a[160:163],v[56:59],v[24:27]
buffer_load_dwordx4 v0,s[36:39],s67 offen offset:1024 sc1 lds
v_mfma_f32_16x16x32_bf16 v[28:31],a[164:167],v[56:59],v[28:31]
s_cmp_eq_u32 s53,s56
ds_read_b128 v[52:55],v2 offset:27648
v_fma_f32 v74,v74,v99,v99
v_mfma_f32_16x16x32_bf16 v[24:27],a[168:171],v[60:63],v[24:27]
s_cselect_b32 s53,s57,s53
ds_read_b128 v[56:59],v2 offset:28672
v_fma_f32 v75,v98,v75,v97
v_mfma_f32_16x16x32_bf16 v[28:31],a[172:175],v[60:63],v[28:31]
s_cmp_eq_u32 s53,s58
v_rcp_f32 v73,v73
v_mfma_f32_16x16x32_bf16 v[24:27],a[176:179],v[64:67],v[24:27]
buffer_load_dwordx4 v0,s[36:39],s67 offen offset:2048 sc1 lds
v_mfma_f32_16x16x32_bf16 v[28:31],a[180:183],v[64:67],v[28:31]
s_cselect_b32 s53,s59,s53
ds_read_b128 v[60:63],v2 offset:29696
v_mul_f32 v75,v35,v75
v_mfma_f32_16x16x32_bf16 v[24:27],a[184:187],v[68:71],v[24:27]
s_cmp_ge_u32 s66,s59
ds_read_b128 v[64:67],v2 offset:30720
v_mul_f32 v32,v32,v72
v_mfma_f32_16x16x32_bf16 v[28:31],a[188:191],v[68:71],v[28:31]
s_waitcnt lgkmcnt(3)
v_exp_f32 v75,v75
v_mfma_f32_16x16x32_bf16 v[24:27],a[192:195],v[40:43],v[24:27]
s_waitcnt vmcnt(9)
ds_read_b128 v[68:71],v2 offset:31744
v_fma_f32 v75,v75,v99,v99
v_mfma_f32_16x16x32_bf16 v[28:31],a[196:199],v[40:43],v[28:31]
s_barrier
v_fma_f32 v36,-v96,v20,v36
v_fma_f32 v37,-v96,v21,v37
v_fma_f32 v38,-v96,v22,v38
v_fma_f32 v39,-v96,v23,v39
v_mul_f32 v76,v36,v36
v_mul_f32 v77,v37,v37
v_mul_f32 v78,v38,v38
v_mul_f32 v79,v39,v39
v_fma_f32 v76,v98,v76,v97
v_fma_f32 v77,v98,v77,v97
v_fma_f32 v78,v98,v78,v97
v_fma_f32 v79,v98,v79,v97
v_mul_f32 v76,v36,v76
v_mul_f32 v77,v37,v77
v_mul_f32 v78,v38,v78
v_mul_f32 v79,v39,v79
v_exp_f32 v76,v76
v_exp_f32 v77,v77
v_exp_f32 v78,v78
v_exp_f32 v79,v79
v_mfma_f32_16x16x32_bf16 v[24:27],a[200:203],v[44:47],v[24:27]
buffer_load_dwordx4 v0,s[36:39],s67 offen offset:3072 sc1 lds
v_rcp_f32 v74,v74
v_rcp_f32 v75,v75
v_fma_f32 v76,v76,v99,v99
v_fma_f32 v77,v77,v99,v99
v_fma_f32 v78,v78,v99,v99
v_fma_f32 v79,v79,v99,v99
v_rcp_f32 v76,v76
v_rcp_f32 v77,v77
v_rcp_f32 v78,v78
v_rcp_f32 v79,v79
v_mfma_f32_16x16x32_bf16 v[28:31],a[204:207],v[44:47],v[28:31]
ds_read_b128 v[40:43],v2 offset:32768
v_mul_f32 v33,v33,v73
v_mul_f32 v34,v34,v74
v_mul_f32 v35,v35,v75
v_cvt_pk_bf16_f32 v80,v32,v33
v_cvt_pk_bf16_f32 v81,v34,v35
v_mul_f32 v36,v36,v76
v_mul_f32 v37,v37,v77
v_mul_f32 v38,v38,v78
v_mul_f32 v39,v39,v79
v_cvt_pk_bf16_f32 v82,v36,v37
v_cvt_pk_bf16_f32 v83,v38,v39
v_mfma_f32_16x16x32_bf16 v[24:27],a[208:211],v[48:51],v[24:27]
buffer_store_dwordx4 v[80:83],v3,s[36:39],s73 offen offset:0 sc0 sc1
ds_read_b128 v[44:47],v2 offset:33792
ds_read_b64 v[92:93],v11 offset:0
v_mfma_f32_16x16x32_bf16 v[28:31],a[212:215],v[48:51],v[28:31]
v_mfma_f32_16x16x32_bf16 v[24:27],a[216:219],v[52:55],v[24:27]
ds_read_b128 v[48:51],v2 offset:34816
v_mfma_f32_16x16x32_bf16 v[28:31],a[220:223],v[52:55],v[28:31]
s_waitcnt lgkmcnt(4)
v_mfma_f32_16x16x32_bf16 v[24:27],a[224:227],v[56:59],v[24:27]
s_waitcnt lgkmcnt(1)
ds_read_b128 v[52:55],v2 offset:35840
v_mfma_f32_16x16x32_bf16 v[28:31],a[228:231],v[56:59],v[28:31]
v_mfma_f32_16x16x32_bf16 v[24:27],a[232:235],v[60:63],v[24:27]
ds_read_b128 v[56:59],v2 offset:36864
v_mfma_f32_16x16x32_bf16 v[28:31],a[236:239],v[60:63],v[28:31]
v_mfma_f32_16x16x32_bf16 v[24:27],a[240:243],v[64:67],v[24:27]
ds_read_b128 v[60:63],v2 offset:37888
v_mfma_f32_16x16x32_bf16 v[28:31],a[244:247],v[64:67],v[28:31]
v_mfma_f32_16x16x32_bf16 v[24:27],a[248:251],v[68:71],v[24:27]
s_waitcnt lgkmcnt(2)
ds_read_b128 v[64:67],v2 offset:38912
v_mfma_f32_16x16x32_bf16 v[28:31],a[252:255],v[68:71],v[28:31]
s_cbranch_scc1 Lg3o_slow1_%=
Lg3o_d1w0_%=:
v_mfma_f32_16x16x32_bf16 v[32:35],a[0:3],v[40:43],0
s_add_u32 m0,s62,16384
ds_read_b128 v[68:71],v2 offset:39936
v_mul_f32 v96,s75,v92
v_mfma_f32_16x16x32_bf16 v[36:39],a[4:7],v[40:43],0
buffer_load_dwordx4 v1,s[36:39],s67 offen offset:0 sc1 lds
v_mfma_f32_16x16x32_bf16 v[32:35],a[8:11],v[44:47],v[32:35]
s_mul_i32 s73,s48,7
ds_read_b128 v[40:43],v2 offset:40960
v_fma_f32 v93,v93,s75,v12
v_mfma_f32_16x16x32_bf16 v[36:39],a[12:15],v[44:47],v[36:39]
s_lshr_b32 s73,s73,1
v_fma_f32 v93,-v96,v96,v93
v_max_f32 v93,v93,v12
v_mfma_f32_16x16x32_bf16 v[32:35],a[16:19],v[48:51],v[32:35]
s_lshr_b32 s72,s67,5
ds_read_b128 v[44:47],v2 offset:41984
v_fma_f32 v24,-v96,v16,v24
v_mfma_f32_16x16x32_bf16 v[36:39],a[20:23],v[48:51],v[36:39]
buffer_load_dwordx4 v1,s[36:39],s67 offen offset:1024 sc1 lds
v_mfma_f32_16x16x32_bf16 v[32:35],a[24:27],v[52:55],v[32:35]
s_lshr_b32 s71,s62,2
ds_read_b128 v[48:51],v2 offset:43008
v_fma_f32 v25,-v96,v17,v25
v_mfma_f32_16x16x32_bf16 v[36:39],a[28:31],v[52:55],v[36:39]
s_waitcnt lgkmcnt(3)
v_rsq_f32 v92,v93
v_mfma_f32_16x16x32_bf16 v[32:35],a[32:35],v[56:59],v[32:35]
s_mov_b64 s[44:45],s[46:47]
ds_read_b128 v[52:55],v2 offset:44032
v_mul_f32 v97,s76,v92
v_mfma_f32_16x16x32_bf16 v[36:39],a[36:39],v[56:59],v[36:39]
buffer_load_dwordx4 v1,s[36:39],s67 offen offset:2048 sc1 lds
v_mfma_f32_16x16x32_bf16 v[32:35],a[40:43],v[60:63],v[32:35]
s_mov_b64 s[46:47],s[48:49]
ds_read_b128 v[56:59],v2 offset:45056)"
R"(
v_mul_f32 v98,v92,v92
v_mfma_f32_16x16x32_bf16 v[36:39],a[44:47],v[60:63],v[36:39]
s_mov_b64 s[48:49],s[50:51]
v_mul_f32 v99,v93,v92
v_mul_f32 v98,s77,v98
v_mfma_f32_16x16x32_bf16 v[32:35],a[48:51],v[64:67],v[32:35]
ds_read_b128 v[60:63],v2 offset:46080
v_mul_f32 v98,v98,v97
v_mfma_f32_16x16x32_bf16 v[36:39],a[52:55],v[64:67],v[36:39]
buffer_load_dwordx4 v1,s[36:39],s67 offen offset:3072 sc1 lds
v_mfma_f32_16x16x32_bf16 v[32:35],a[56:59],v[68:71],v[32:35]
s_add_u32 m0,s71,s88
ds_read_b128 v[64:67],v2 offset:47104
v_fma_f32 v26,-v96,v18,v26
v_mfma_f32_16x16x32_bf16 v[36:39],a[60:63],v[68:71],v[36:39]
s_waitcnt lgkmcnt(3)
buffer_load_dword v9,s[36:39],s72 offen offset:0 sc1 lds
v_mfma_f32_16x16x32_bf16 v[32:35],a[64:67],v[40:43],v[32:35]
s_waitcnt vmcnt(11)
ds_read_b128 v[68:71],v2 offset:48128
v_fma_f32 v27,-v96,v19,v27
v_mfma_f32_16x16x32_bf16 v[36:39],a[68:71],v[40:43],v[36:39]
s_barrier
v_mfma_f32_16x16x32_bf16 v[32:35],a[72:75],v[44:47],v[32:35]
s_xor_b32 s87,s63,65536
ds_read_b128 v[40:43],v2 offset:49152
ds_read_b32 v88,v8 offset:8192
v_mfma_f32_16x16x32_bf16 v[36:39],a[76:79],v[44:47],v[36:39]
s_cmp_ge_u32 s67,s59
v_mul_f32 v72,v24,v24
v_mul_f32 v73,v25,v25
v_mfma_f32_16x16x32_bf16 v[32:35],a[80:83],v[48:51],v[32:35]
s_cselect_b32 s86,s50,s51
ds_read_b128 v[44:47],v2 offset:50176
v_mul_f32 v74,v26,v26
v_mfma_f32_16x16x32_bf16 v[36:39],a[84:87],v[48:51],v[36:39]
s_cselect_b32 s87,s87,s64
v_mul_f32 v75,v27,v27
v_fma_f32 v72,v98,v72,v97
v_mfma_f32_16x16x32_bf16 v[32:35],a[88:91],v[52:55],v[32:35]
s_lshr_b32 s68,s51,5
ds_read_b128 v[48:51],v2 offset:51200
v_mul_f32 v72,v24,v72
v_mfma_f32_16x16x32_bf16 v[36:39],a[92:95],v[52:55],v[36:39]
s_waitcnt lgkmcnt(4)
v_exp_f32 v72,v72
v_mfma_f32_16x16x32_bf16 v[32:35],a[96:99],v[56:59],v[32:35]
s_waitcnt vmcnt(10) lgkmcnt(2)
ds_read_b128 v[52:55],v2 offset:52224
v_fma_f32 v72,v72,v99,v99
v_mfma_f32_16x16x32_bf16 v[36:39],a[100:103],v[56:59],v[36:39]
s_barrier
v_mfma_f32_16x16x32_bf16 v[32:35],a[104:107],v[60:63],v[32:35]
s_lshr_b32 s70,s52,5
ds_read_b128 v[56:59],v2 offset:53248
ds_read_b32 v13,v7 offset:256
v_mfma_f32_16x16x32_bf16 v[36:39],a[108:111],v[60:63],v[36:39]
s_mov_b32 s62,s87
v_rcp_f32 v72,v72
v_mfma_f32_16x16x32_bf16 v[32:35],a[112:115],v[64:67],v[32:35]
s_mov_b64 s[50:51],s[52:53]
ds_read_b128 v[60:63],v2 offset:54272
v_add_f32_dpp v88,v88,v88 quad_perm:[1,0,3,2] row_mask:0xf bank_mask:0xf
v_mfma_f32_16x16x32_bf16 v[36:39],a[116:119],v[64:67],v[36:39]
s_nop 0
v_add_f32_dpp v88,v88,v88 quad_perm:[2,3,0,1] row_mask:0xf bank_mask:0xf
v_fma_f32 v73,v98,v73,v97
v_mfma_f32_16x16x32_bf16 v[32:35],a[120:123],v[68:71],v[32:35]
s_waitcnt lgkmcnt(1)
ds_read_b128 v[64:67],v2 offset:55296
v_mul_f32 v73,v25,v73
v_mfma_f32_16x16x32_bf16 v[36:39],a[124:127],v[68:71],v[36:39]
v_cmp_eq_u32 vcc,s60,v13
v_exp_f32 v73,v73
v_add_f32_dpp v88,v88,v88 row_half_mirror row_mask:0xf bank_mask:0xf
v_mfma_f32_16x16x32_bf16 v[32:35],a[128:131],v[40:43],v[32:35]
s_cmp_eq_u32 vcc_lo,-1
ds_write_b32 v10,v88 offset:128
v_mfma_f32_16x16x32_bf16 v[36:39],a[132:135],v[40:43],v[36:39]
s_cselect_b32 s66,s86,s59
s_cmp_ge_u32 s66,s59
s_cselect_b32 s68,s68,s70
ds_read_b128 v[68:71],v2 offset:56320
s_add_u32 m0,s78,0
v_mfma_f32_16x16x32_bf16 v[32:35],a[136:139],v[44:47],v[32:35]
buffer_load_dword v6,s[36:39],s68 offen offset:0 sc1 lds
v_mfma_f32_16x16x32_bf16 v[36:39],a[140:143],v[44:47],v[36:39]
s_mov_b32 m0,s62
ds_read_b128 v[40:43],v2 offset:57344
v_fma_f32 v73,v73,v99,v99
v_mfma_f32_16x16x32_bf16 v[32:35],a[144:147],v[48:51],v[32:35]
buffer_load_dwordx4 v0,s[36:39],s66 offen offset:0 sc1 lds
v_mfma_f32_16x16x32_bf16 v[36:39],a[148:151],v[48:51],v[36:39]
s_xor_b32 s64,s64,65536
ds_read_b128 v[44:47],v2 offset:58368
v_fma_f32 v74,v98,v74,v97
v_mfma_f32_16x16x32_bf16 v[32:35],a[152:155],v[52:55],v[32:35]
s_add_u32 s74,s53,s55
ds_read_b128 v[48:51],v2 offset:59392
v_mul_f32 v74,v26,v74
v_mfma_f32_16x16x32_bf16 v[36:39],a[156:159],v[52:55],v[36:39]
s_waitcnt lgkmcnt(3)
v_exp_f32 v74,v74
v_mfma_f32_16x16x32_bf16 v[32:35],a[160:163],v[56:59],v[32:35]
buffer_load_dwordx4 v0,s[36:39],s66 offen offset:1024 sc1 lds
v_mfma_f32_16x16x32_bf16 v[36:39],a[164:167],v[56:59],v[36:39]
s_cmp_eq_u32 s74,s56
ds_read_b128 v[52:55],v2 offset:60416
v_fma_f32 v74,v74,v99,v99
v_mfma_f32_16x16x32_bf16 v[32:35],a[168:171],v[60:63],v[32:35]
s_cselect_b32 s74,s57,s74
ds_read_b128 v[56:59],v2 offset:61440
v_fma_f32 v75,v98,v75,v97
v_mfma_f32_16x16x32_bf16 v[36:39],a[172:175],v[60:63],v[36:39]
s_cmp_eq_u32 s74,s58
v_rcp_f32 v73,v73
v_mfma_f32_16x16x32_bf16 v[32:35],a[176:179],v[64:67],v[32:35]
buffer_load_dwordx4 v0,s[36:39],s66 offen offset:2048 sc1 lds
v_mfma_f32_16x16x32_bf16 v[36:39],a[180:183],v[64:67],v[36:39]
s_cselect_b32 s74,s59,s74
ds_read_b128 v[60:63],v2 offset:62464
v_mul_f32 v75,v27,v75
v_mfma_f32_16x16x32_bf16 v[32:35],a[184:187],v[68:71],v[32:35]
s_mov_b32 s52,s74
ds_read_b128 v[64:67],v2 offset:63488
v_mul_f32 v24,v24,v72
v_mfma_f32_16x16x32_bf16 v[36:39],a[188:191],v[68:71],v[36:39]
s_waitcnt lgkmcnt(3)
v_exp_f32 v75,v75
v_mfma_f32_16x16x32_bf16 v[32:35],a[192:195],v[40:43],v[32:35]
s_waitcnt vmcnt(9)
ds_read_b128 v[68:71],v2 offset:64512
v_xor_b32 v2,65536,v2
v_mfma_f32_16x16x32_bf16 v[36:39],a[196:199],v[40:43],v[36:39]
s_barrier
v_fma_f32 v28,-v96,v20,v28
v_fma_f32 v29,-v96,v21,v29
v_fma_f32 v30,-v96,v22,v30
v_fma_f32 v31,-v96,v23,v31
v_mul_f32 v76,v28,v28
v_mul_f32 v77,v29,v29
v_mul_f32 v78,v30,v30
v_mul_f32 v79,v31,v31
v_fma_f32 v76,v98,v76,v97
v_fma_f32 v77,v98,v77,v97
v_fma_f32 v78,v98,v78,v97
v_fma_f32 v79,v98,v79,v97
v_mul_f32 v76,v28,v76
v_mul_f32 v77,v29,v77
v_mul_f32 v78,v30,v78
v_mul_f32 v79,v31,v79
v_exp_f32 v76,v76
v_exp_f32 v77,v77
v_exp_f32 v78,v78
v_exp_f32 v79,v79
v_mfma_f32_16x16x32_bf16 v[32:35],a[200:203],v[44:47],v[32:35]
buffer_load_dwordx4 v0,s[36:39],s66 offen offset:3072 sc1 lds
v_fma_f32 v75,v75,v99,v99
v_rcp_f32 v74,v74
v_rcp_f32 v75,v75
v_fma_f32 v76,v76,v99,v99
v_fma_f32 v77,v77,v99,v99
v_fma_f32 v78,v78,v99,v99
v_fma_f32 v79,v79,v99,v99
v_rcp_f32 v76,v76
v_rcp_f32 v77,v77
v_rcp_f32 v78,v78
v_rcp_f32 v79,v79
v_mfma_f32_16x16x32_bf16 v[36:39],a[204:207],v[44:47],v[36:39]
s_cmp_lt_u32 s67,s59
ds_read_b128 v[40:43],v2 offset:0
v_mul_f32 v25,v25,v73
v_mul_f32 v26,v26,v74
v_mul_f32 v27,v27,v75
v_cvt_pk_bf16_f32 v80,v24,v25
v_cvt_pk_bf16_f32 v81,v26,v27
v_mul_f32 v28,v28,v76
v_mul_f32 v29,v29,v77
v_mul_f32 v30,v30,v78
v_mul_f32 v31,v31,v79
v_cvt_pk_bf16_f32 v82,v28,v29
v_cvt_pk_bf16_f32 v83,v30,v31
v_mfma_f32_16x16x32_bf16 v[32:35],a[208:211],v[48:51],v[32:35]
buffer_store_dwordx4 v[80:83],v3,s[36:39],s73 offen offset:0 sc0 sc1
ds_read_b128 v[44:47],v2 offset:1024
ds_read_b64 v[92:93],v11 offset:128
v_xor_b32 v8,16384,v8
v_mfma_f32_16x16x32_bf16 v[36:39],a[212:215],v[48:51],v[36:39]
v_mfma_f32_16x16x32_bf16 v[32:35],a[216:219],v[52:55],v[32:35]
ds_read_b128 v[48:51],v2 offset:2048
v_mfma_f32_16x16x32_bf16 v[36:39],a[220:223],v[52:55],v[36:39]
s_waitcnt lgkmcnt(4)
v_mfma_f32_16x16x32_bf16 v[32:35],a[224:227],v[56:59],v[32:35]
s_waitcnt lgkmcnt(1)
ds_read_b128 v[52:55],v2 offset:3072
v_mfma_f32_16x16x32_bf16 v[36:39],a[228:231],v[56:59],v[36:39]
v_mfma_f32_16x16x32_bf16 v[32:35],a[232:235],v[60:63],v[32:35]
ds_read_b128 v[56:59],v2 offset:4096
v_mfma_f32_16x16x32_bf16 v[36:39],a[236:239],v[60:63],v[36:39]
v_mfma_f32_16x16x32_bf16 v[32:35],a[240:243],v[64:67],v[32:35])"
R"(
ds_read_b128 v[60:63],v2 offset:5120
v_mfma_f32_16x16x32_bf16 v[36:39],a[244:247],v[64:67],v[36:39]
v_mfma_f32_16x16x32_bf16 v[32:35],a[248:251],v[68:71],v[32:35]
s_waitcnt lgkmcnt(2)
ds_read_b128 v[64:67],v2 offset:6144
v_mfma_f32_16x16x32_bf16 v[36:39],a[252:255],v[68:71],v[36:39]
s_cbranch_scc1 Lg3o_d0w0_%=
s_branch Lg3o_slow0_%=
Lg3o_d0w1_%=:
v_mfma_f32_16x16x32_bf16 v[24:27],a[0:3],v[40:43],0
s_add_u32 m0,s62,16384
ds_read_b128 v[68:71],v2 offset:7168
v_mul_f32 v96,s75,v92
v_mfma_f32_16x16x32_bf16 v[28:31],a[4:7],v[40:43],0
s_mul_i32 s73,s47,7
v_fma_f32 v93,v93,s75,v12
v_fma_f32 v93,-v96,v96,v93
v_mfma_f32_16x16x32_bf16 v[24:27],a[8:11],v[44:47],v[24:27]
buffer_load_dwordx4 v1,s[36:39],s66 offen offset:0 sc1 lds
v_mfma_f32_16x16x32_bf16 v[28:31],a[12:15],v[44:47],v[28:31]
s_lshr_b32 s73,s73,1
ds_read_b128 v[40:43],v2 offset:8192
v_max_f32 v93,v93,v12
v_mfma_f32_16x16x32_bf16 v[24:27],a[16:19],v[48:51],v[24:27]
s_lshr_b32 s72,s66,5
ds_read_b128 v[44:47],v2 offset:9216
v_fma_f32 v32,-v96,v16,v32
v_mfma_f32_16x16x32_bf16 v[28:31],a[20:23],v[48:51],v[28:31]
s_lshr_b32 s71,s62,2
v_rsq_f32 v92,v93
v_mfma_f32_16x16x32_bf16 v[24:27],a[24:27],v[52:55],v[24:27]
buffer_load_dwordx4 v1,s[36:39],s66 offen offset:1024 sc1 lds
v_mfma_f32_16x16x32_bf16 v[28:31],a[28:31],v[52:55],v[28:31]
s_waitcnt lgkmcnt(2)
ds_read_b128 v[48:51],v2 offset:10240
v_mul_f32 v97,s76,v92
v_mfma_f32_16x16x32_bf16 v[24:27],a[32:35],v[56:59],v[24:27]
ds_read_b128 v[52:55],v2 offset:11264
v_mul_f32 v98,v92,v92
v_mfma_f32_16x16x32_bf16 v[28:31],a[36:39],v[56:59],v[28:31]
v_mul_f32 v99,v93,v92
v_mul_f32 v98,s77,v98
v_mfma_f32_16x16x32_bf16 v[24:27],a[40:43],v[60:63],v[24:27]
buffer_load_dwordx4 v1,s[36:39],s66 offen offset:2048 sc1 lds
v_mfma_f32_16x16x32_bf16 v[28:31],a[44:47],v[60:63],v[28:31]
ds_read_b128 v[56:59],v2 offset:12288
v_mul_f32 v98,v98,v97
v_mfma_f32_16x16x32_bf16 v[24:27],a[48:51],v[64:67],v[24:27]
ds_read_b128 v[60:63],v2 offset:13312
v_fma_f32 v33,-v96,v17,v33
v_mfma_f32_16x16x32_bf16 v[28:31],a[52:55],v[64:67],v[28:31]
v_fma_f32 v34,-v96,v18,v34
v_fma_f32 v35,-v96,v19,v35
v_mfma_f32_16x16x32_bf16 v[24:27],a[56:59],v[68:71],v[24:27]
buffer_load_dwordx4 v1,s[36:39],s66 offen offset:3072 sc1 lds
v_mfma_f32_16x16x32_bf16 v[28:31],a[60:63],v[68:71],v[28:31]
s_waitcnt lgkmcnt(2)
ds_read_b128 v[64:67],v2 offset:14336
s_add_u32 m0,s71,s88
v_mfma_f32_16x16x32_bf16 v[24:27],a[64:67],v[40:43],v[24:27]
s_xor_b32 s87,s64,65536
ds_read_b128 v[68:71],v2 offset:15360
buffer_load_dword v9,s[36:39],s72 offen offset:0 sc1 lds
s_waitcnt vmcnt(10)
v_mfma_f32_16x16x32_bf16 v[28:31],a[68:71],v[40:43],v[28:31]
s_barrier
v_mfma_f32_16x16x32_bf16 v[24:27],a[72:75],v[44:47],v[24:27]
s_cmp_ge_u32 s66,s59
ds_read_b128 v[40:43],v2 offset:16384
ds_read_b32 v88,v8 offset:0
v_mfma_f32_16x16x32_bf16 v[28:31],a[76:79],v[44:47],v[28:31]
s_cselect_b32 s86,s49,s50
v_mul_f32 v72,v32,v32
v_mul_f32 v73,v33,v33
v_mfma_f32_16x16x32_bf16 v[24:27],a[80:83],v[48:51],v[24:27]
s_cselect_b32 s87,s87,s63
ds_read_b128 v[44:47],v2 offset:17408
v_mul_f32 v74,v34,v34
v_mfma_f32_16x16x32_bf16 v[28:31],a[84:87],v[48:51],v[28:31]
s_mov_b32 s62,s87
v_mul_f32 v75,v35,v35
v_fma_f32 v72,v98,v72,v97
v_mfma_f32_16x16x32_bf16 v[24:27],a[88:91],v[52:55],v[24:27]
s_mov_b32 m0,s62
ds_read_b128 v[48:51],v2 offset:18432
v_mul_f32 v72,v32,v72
v_mfma_f32_16x16x32_bf16 v[28:31],a[92:95],v[52:55],v[28:31]
s_waitcnt lgkmcnt(4)
v_exp_f32 v72,v72
v_mfma_f32_16x16x32_bf16 v[24:27],a[96:99],v[56:59],v[24:27]
s_nop 0
s_waitcnt lgkmcnt(2)
ds_read_b128 v[52:55],v2 offset:19456
v_fma_f32 v72,v72,v99,v99
v_mfma_f32_16x16x32_bf16 v[28:31],a[100:103],v[56:59],v[28:31]
s_barrier
v_mfma_f32_16x16x32_bf16 v[24:27],a[104:107],v[60:63],v[24:27]
ds_read_b128 v[56:59],v2 offset:20480
ds_read_b32 v13,v7 offset:0
v_mfma_f32_16x16x32_bf16 v[28:31],a[108:111],v[60:63],v[28:31]
v_rcp_f32 v72,v72
v_mfma_f32_16x16x32_bf16 v[24:27],a[112:115],v[64:67],v[24:27]
ds_read_b128 v[60:63],v2 offset:21504
v_add_f32_dpp v88,v88,v88 quad_perm:[1,0,3,2] row_mask:0xf bank_mask:0xf
v_mfma_f32_16x16x32_bf16 v[28:31],a[116:119],v[64:67],v[28:31]
s_nop 0
v_add_f32_dpp v88,v88,v88 quad_perm:[2,3,0,1] row_mask:0xf bank_mask:0xf
v_fma_f32 v73,v98,v73,v97
v_mfma_f32_16x16x32_bf16 v[24:27],a[120:123],v[68:71],v[24:27]
s_waitcnt lgkmcnt(1)
ds_read_b128 v[64:67],v2 offset:22528
v_mul_f32 v73,v33,v73
v_mfma_f32_16x16x32_bf16 v[28:31],a[124:127],v[68:71],v[28:31]
v_cmp_eq_u32 vcc,s60,v13
v_exp_f32 v73,v73
v_add_f32_dpp v88,v88,v88 row_half_mirror row_mask:0xf bank_mask:0xf
v_mfma_f32_16x16x32_bf16 v[24:27],a[128:131],v[40:43],v[24:27]
s_cmp_eq_u32 vcc_lo,-1
ds_write_b32 v10,v88 offset:0
s_cselect_b32 s67,s86,s59
v_mfma_f32_16x16x32_bf16 v[28:31],a[132:135],v[40:43],v[28:31]
s_xor_b32 s63,s63,65536
ds_read_b128 v[68:71],v2 offset:23552
buffer_load_dwordx4 v0,s[36:39],s67 offen offset:0 sc1 lds
v_mfma_f32_16x16x32_bf16 v[24:27],a[136:139],v[44:47],v[24:27]
s_add_u32 s53,s52,s55
ds_read_b128 v[40:43],v2 offset:24576
v_fma_f32 v73,v73,v99,v99
v_mfma_f32_16x16x32_bf16 v[28:31],a[140:143],v[44:47],v[28:31]
s_cmp_eq_u32 s53,s56
v_rcp_f32 v73,v73
v_mfma_f32_16x16x32_bf16 v[24:27],a[144:147],v[48:51],v[24:27]
s_cselect_b32 s53,s57,s53
ds_read_b128 v[44:47],v2 offset:25600
v_fma_f32 v74,v98,v74,v97
v_mfma_f32_16x16x32_bf16 v[28:31],a[148:151],v[48:51],v[28:31]
buffer_load_dwordx4 v0,s[36:39],s67 offen offset:1024 sc1 lds
v_mfma_f32_16x16x32_bf16 v[24:27],a[152:155],v[52:55],v[24:27]
s_cmp_eq_u32 s53,s58
ds_read_b128 v[48:51],v2 offset:26624
v_mul_f32 v74,v34,v74
v_mfma_f32_16x16x32_bf16 v[28:31],a[156:159],v[52:55],v[28:31]
s_waitcnt lgkmcnt(3)
v_exp_f32 v74,v74
v_mfma_f32_16x16x32_bf16 v[24:27],a[160:163],v[56:59],v[24:27]
s_cselect_b32 s53,s59,s53
ds_read_b128 v[52:55],v2 offset:27648
v_fma_f32 v74,v74,v99,v99
v_mfma_f32_16x16x32_bf16 v[28:31],a[164:167],v[56:59],v[28:31]
buffer_load_dwordx4 v0,s[36:39],s67 offen offset:2048 sc1 lds
v_mfma_f32_16x16x32_bf16 v[24:27],a[168:171],v[60:63],v[24:27]
s_cmp_ge_u32 s66,s59
ds_read_b128 v[56:59],v2 offset:28672
v_fma_f32 v75,v98,v75,v97
v_mfma_f32_16x16x32_bf16 v[28:31],a[172:175],v[60:63],v[28:31]
v_rcp_f32 v74,v74
v_mfma_f32_16x16x32_bf16 v[24:27],a[176:179],v[64:67],v[24:27]
ds_read_b128 v[60:63],v2 offset:29696
v_mul_f32 v75,v35,v75
v_mfma_f32_16x16x32_bf16 v[28:31],a[180:183],v[64:67],v[28:31]
buffer_load_dwordx4 v0,s[36:39],s67 offen offset:3072 sc1 lds
v_mfma_f32_16x16x32_bf16 v[24:27],a[184:187],v[68:71],v[24:27]
ds_read_b128 v[64:67],v2 offset:30720
v_mul_f32 v32,v32,v72
v_mfma_f32_16x16x32_bf16 v[28:31],a[188:191],v[68:71],v[28:31]
s_waitcnt lgkmcnt(3)
v_exp_f32 v75,v75
v_mfma_f32_16x16x32_bf16 v[24:27],a[192:195],v[40:43],v[24:27]
s_waitcnt vmcnt(9)
ds_read_b128 v[68:71],v2 offset:31744
v_fma_f32 v75,v75,v99,v99
v_mfma_f32_16x16x32_bf16 v[28:31],a[196:199],v[40:43],v[28:31]
s_barrier
v_mfma_f32_16x16x32_bf16 v[24:27],a[200:203],v[44:47],v[24:27]
v_fma_f32 v36,-v96,v20,v36
v_fma_f32 v37,-v96,v21,v37
v_fma_f32 v38,-v96,v22,v38
v_fma_f32 v39,-v96,v23,v39
v_mul_f32 v76,v36,v36
v_mul_f32 v77,v37,v37
v_mul_f32 v78,v38,v38
v_mul_f32 v79,v39,v39
v_fma_f32 v76,v98,v76,v97
v_fma_f32 v77,v98,v77,v97
v_fma_f32 v78,v98,v78,v97
v_fma_f32 v79,v98,v79,v97
v_mul_f32 v76,v36,v76
v_mul_f32 v77,v37,v77
v_mul_f32 v78,v38,v78
v_mul_f32 v79,v39,v79
v_exp_f32 v76,v76
v_exp_f32 v77,v77
v_exp_f32 v78,v78
v_exp_f32 v79,v79
v_mfma_f32_16x16x32_bf16 v[28:31],a[204:207],v[44:47],v[28:31]
ds_read_b128 v[40:43],v2 offset:32768
v_rcp_f32 v75,v75
v_fma_f32 v76,v76,v99,v99
v_fma_f32 v77,v77,v99,v99
v_fma_f32 v78,v78,v99,v99)"
R"(
v_fma_f32 v79,v79,v99,v99
v_rcp_f32 v76,v76
v_rcp_f32 v77,v77
v_rcp_f32 v78,v78
v_rcp_f32 v79,v79
v_mfma_f32_16x16x32_bf16 v[24:27],a[208:211],v[48:51],v[24:27]
v_mul_f32 v33,v33,v73
v_mul_f32 v34,v34,v74
v_mul_f32 v35,v35,v75
v_cvt_pk_bf16_f32 v80,v32,v33
v_cvt_pk_bf16_f32 v81,v34,v35
v_mul_f32 v36,v36,v76
v_mul_f32 v37,v37,v77
v_mul_f32 v38,v38,v78
v_mul_f32 v39,v39,v79
v_cvt_pk_bf16_f32 v82,v36,v37
v_cvt_pk_bf16_f32 v83,v38,v39
v_mfma_f32_16x16x32_bf16 v[28:31],a[212:215],v[48:51],v[28:31]
ds_read_b128 v[44:47],v2 offset:33792
buffer_store_dwordx4 v[80:83],v3,s[36:39],s73 offen offset:0 sc0 sc1
ds_read_b64 v[92:93],v11 offset:0
v_mfma_f32_16x16x32_bf16 v[24:27],a[216:219],v[52:55],v[24:27]
ds_read_b128 v[48:51],v2 offset:34816
v_mfma_f32_16x16x32_bf16 v[28:31],a[220:223],v[52:55],v[28:31]
s_waitcnt lgkmcnt(4)
v_mfma_f32_16x16x32_bf16 v[24:27],a[224:227],v[56:59],v[24:27]
ds_read_b128 v[52:55],v2 offset:35840
v_mfma_f32_16x16x32_bf16 v[28:31],a[228:231],v[56:59],v[28:31]
s_waitcnt lgkmcnt(2)
v_mfma_f32_16x16x32_bf16 v[24:27],a[232:235],v[60:63],v[24:27]
ds_read_b128 v[56:59],v2 offset:36864
v_mfma_f32_16x16x32_bf16 v[28:31],a[236:239],v[60:63],v[28:31]
v_mfma_f32_16x16x32_bf16 v[24:27],a[240:243],v[64:67],v[24:27]
ds_read_b128 v[60:63],v2 offset:37888
v_mfma_f32_16x16x32_bf16 v[28:31],a[244:247],v[64:67],v[28:31]
v_mfma_f32_16x16x32_bf16 v[24:27],a[248:251],v[68:71],v[24:27]
s_waitcnt lgkmcnt(2)
ds_read_b128 v[64:67],v2 offset:38912
v_mfma_f32_16x16x32_bf16 v[28:31],a[252:255],v[68:71],v[28:31]
s_cbranch_scc1 Lg3o_slow1_%=
Lg3o_d1w1_%=:
v_mfma_f32_16x16x32_bf16 v[32:35],a[0:3],v[40:43],0
s_add_u32 m0,s62,16384
ds_read_b128 v[68:71],v2 offset:39936
v_mul_f32 v96,s75,v92
v_mfma_f32_16x16x32_bf16 v[36:39],a[4:7],v[40:43],0
s_mul_i32 s73,s48,7
v_fma_f32 v93,v93,s75,v12
v_fma_f32 v93,-v96,v96,v93
v_mfma_f32_16x16x32_bf16 v[32:35],a[8:11],v[44:47],v[32:35]
buffer_load_dwordx4 v1,s[36:39],s67 offen offset:0 sc1 lds
v_mfma_f32_16x16x32_bf16 v[36:39],a[12:15],v[44:47],v[36:39]
s_lshr_b32 s73,s73,1
ds_read_b128 v[40:43],v2 offset:40960
v_max_f32 v93,v93,v12
v_mfma_f32_16x16x32_bf16 v[32:35],a[16:19],v[48:51],v[32:35]
s_lshr_b32 s72,s67,5
ds_read_b128 v[44:47],v2 offset:41984
v_fma_f32 v24,-v96,v16,v24
v_mfma_f32_16x16x32_bf16 v[36:39],a[20:23],v[48:51],v[36:39]
s_lshr_b32 s71,s62,2
v_rsq_f32 v92,v93
v_mfma_f32_16x16x32_bf16 v[32:35],a[24:27],v[52:55],v[32:35]
buffer_load_dwordx4 v1,s[36:39],s67 offen offset:1024 sc1 lds
v_mfma_f32_16x16x32_bf16 v[36:39],a[28:31],v[52:55],v[36:39]
s_waitcnt lgkmcnt(2)
ds_read_b128 v[48:51],v2 offset:43008
v_mul_f32 v97,s76,v92
v_mfma_f32_16x16x32_bf16 v[32:35],a[32:35],v[56:59],v[32:35]
s_mov_b64 s[44:45],s[46:47]
ds_read_b128 v[52:55],v2 offset:44032
v_mul_f32 v98,v92,v92
v_mfma_f32_16x16x32_bf16 v[36:39],a[36:39],v[56:59],v[36:39]
s_mov_b64 s[46:47],s[48:49]
v_mul_f32 v99,v93,v92
v_mul_f32 v98,s77,v98
v_mfma_f32_16x16x32_bf16 v[32:35],a[40:43],v[60:63],v[32:35]
buffer_load_dwordx4 v1,s[36:39],s67 offen offset:2048 sc1 lds
v_mfma_f32_16x16x32_bf16 v[36:39],a[44:47],v[60:63],v[36:39]
s_mov_b64 s[48:49],s[50:51]
ds_read_b128 v[56:59],v2 offset:45056
v_mul_f32 v98,v98,v97
v_mfma_f32_16x16x32_bf16 v[32:35],a[48:51],v[64:67],v[32:35]
ds_read_b128 v[60:63],v2 offset:46080
v_fma_f32 v25,-v96,v17,v25
v_mfma_f32_16x16x32_bf16 v[36:39],a[52:55],v[64:67],v[36:39]
v_fma_f32 v26,-v96,v18,v26
v_fma_f32 v27,-v96,v19,v27
v_mfma_f32_16x16x32_bf16 v[32:35],a[56:59],v[68:71],v[32:35]
buffer_load_dwordx4 v1,s[36:39],s67 offen offset:3072 sc1 lds
v_mfma_f32_16x16x32_bf16 v[36:39],a[60:63],v[68:71],v[36:39]
s_waitcnt lgkmcnt(2)
ds_read_b128 v[64:67],v2 offset:47104
s_add_u32 m0,s71,s88
v_mfma_f32_16x16x32_bf16 v[32:35],a[64:67],v[40:43],v[32:35]
s_xor_b32 s87,s63,65536
ds_read_b128 v[68:71],v2 offset:48128
buffer_load_dword v9,s[36:39],s72 offen offset:0 sc1 lds
s_waitcnt vmcnt(10)
v_mfma_f32_16x16x32_bf16 v[36:39],a[68:71],v[40:43],v[36:39]
s_barrier
v_mfma_f32_16x16x32_bf16 v[32:35],a[72:75],v[44:47],v[32:35]
s_cmp_ge_u32 s67,s59
ds_read_b128 v[40:43],v2 offset:49152
ds_read_b32 v88,v8 offset:8192
v_mfma_f32_16x16x32_bf16 v[36:39],a[76:79],v[44:47],v[36:39]
s_cselect_b32 s86,s50,s51
v_mul_f32 v72,v24,v24
v_mul_f32 v73,v25,v25
v_mfma_f32_16x16x32_bf16 v[32:35],a[80:83],v[48:51],v[32:35]
s_cselect_b32 s87,s87,s64
ds_read_b128 v[44:47],v2 offset:50176
v_mul_f32 v74,v26,v26
v_mfma_f32_16x16x32_bf16 v[36:39],a[84:87],v[48:51],v[36:39]
s_mov_b32 s62,s87
v_mul_f32 v75,v27,v27
v_fma_f32 v72,v98,v72,v97
v_mfma_f32_16x16x32_bf16 v[32:35],a[88:91],v[52:55],v[32:35]
s_mov_b32 m0,s62
ds_read_b128 v[48:51],v2 offset:51200
v_mul_f32 v72,v24,v72
v_mfma_f32_16x16x32_bf16 v[36:39],a[92:95],v[52:55],v[36:39]
s_waitcnt lgkmcnt(4)
v_exp_f32 v72,v72
v_mfma_f32_16x16x32_bf16 v[32:35],a[96:99],v[56:59],v[32:35]
s_nop 0
s_waitcnt lgkmcnt(2)
ds_read_b128 v[52:55],v2 offset:52224
v_fma_f32 v72,v72,v99,v99
v_mfma_f32_16x16x32_bf16 v[36:39],a[100:103],v[56:59],v[36:39]
s_barrier
v_mfma_f32_16x16x32_bf16 v[32:35],a[104:107],v[60:63],v[32:35]
s_mov_b64 s[50:51],s[52:53]
ds_read_b128 v[56:59],v2 offset:53248
ds_read_b32 v13,v7 offset:256
v_mfma_f32_16x16x32_bf16 v[36:39],a[108:111],v[60:63],v[36:39]
v_rcp_f32 v72,v72
v_mfma_f32_16x16x32_bf16 v[32:35],a[112:115],v[64:67],v[32:35]
ds_read_b128 v[60:63],v2 offset:54272
v_add_f32_dpp v88,v88,v88 quad_perm:[1,0,3,2] row_mask:0xf bank_mask:0xf
v_mfma_f32_16x16x32_bf16 v[36:39],a[116:119],v[64:67],v[36:39]
s_nop 0
v_add_f32_dpp v88,v88,v88 quad_perm:[2,3,0,1] row_mask:0xf bank_mask:0xf
v_fma_f32 v73,v98,v73,v97
v_mfma_f32_16x16x32_bf16 v[32:35],a[120:123],v[68:71],v[32:35]
s_waitcnt lgkmcnt(1)
ds_read_b128 v[64:67],v2 offset:55296
v_mul_f32 v73,v25,v73
v_mfma_f32_16x16x32_bf16 v[36:39],a[124:127],v[68:71],v[36:39]
v_cmp_eq_u32 vcc,s60,v13
v_exp_f32 v73,v73
v_add_f32_dpp v88,v88,v88 row_half_mirror row_mask:0xf bank_mask:0xf
v_mfma_f32_16x16x32_bf16 v[32:35],a[128:131],v[40:43],v[32:35]
s_cmp_eq_u32 vcc_lo,-1
ds_write_b32 v10,v88 offset:128
s_cselect_b32 s66,s86,s59
v_mfma_f32_16x16x32_bf16 v[36:39],a[132:135],v[40:43],v[36:39]
s_xor_b32 s64,s64,65536
ds_read_b128 v[68:71],v2 offset:56320
buffer_load_dwordx4 v0,s[36:39],s66 offen offset:0 sc1 lds
v_mfma_f32_16x16x32_bf16 v[32:35],a[136:139],v[44:47],v[32:35]
s_add_u32 s74,s53,s55
ds_read_b128 v[40:43],v2 offset:57344
v_fma_f32 v73,v73,v99,v99
v_mfma_f32_16x16x32_bf16 v[36:39],a[140:143],v[44:47],v[36:39]
s_cmp_eq_u32 s74,s56
v_rcp_f32 v73,v73
v_mfma_f32_16x16x32_bf16 v[32:35],a[144:147],v[48:51],v[32:35]
s_cselect_b32 s74,s57,s74
ds_read_b128 v[44:47],v2 offset:58368
v_fma_f32 v74,v98,v74,v97
v_mfma_f32_16x16x32_bf16 v[36:39],a[148:151],v[48:51],v[36:39]
buffer_load_dwordx4 v0,s[36:39],s66 offen offset:1024 sc1 lds
v_mfma_f32_16x16x32_bf16 v[32:35],a[152:155],v[52:55],v[32:35]
s_cmp_eq_u32 s74,s58
ds_read_b128 v[48:51],v2 offset:59392
v_mul_f32 v74,v26,v74
v_mfma_f32_16x16x32_bf16 v[36:39],a[156:159],v[52:55],v[36:39]
s_waitcnt lgkmcnt(3)
v_exp_f32 v74,v74
v_mfma_f32_16x16x32_bf16 v[32:35],a[160:163],v[56:59],v[32:35]
s_cselect_b32 s74,s59,s74
ds_read_b128 v[52:55],v2 offset:60416
v_fma_f32 v74,v74,v99,v99
v_mfma_f32_16x16x32_bf16 v[36:39],a[164:167],v[56:59],v[36:39]
buffer_load_dwordx4 v0,s[36:39],s66 offen offset:2048 sc1 lds
v_mfma_f32_16x16x32_bf16 v[32:35],a[168:171],v[60:63],v[32:35]
s_mov_b32 s52,s74
ds_read_b128 v[56:59],v2 offset:61440
v_fma_f32 v75,v98,v75,v97
v_mfma_f32_16x16x32_bf16 v[36:39],a[172:175],v[60:63],v[36:39]
s_cmp_lt_u32 s67,s59
v_rcp_f32 v74,v74
v_mfma_f32_16x16x32_bf16 v[32:35],a[176:179],v[64:67],v[32:35]
ds_read_b128 v[60:63],v2 offset:62464
v_mul_f32 v75,v27,v75
v_mfma_f32_16x16x32_bf16 v[36:39],a[180:183],v[64:67],v[36:39]
buffer_load_dwordx4 v0,s[36:39],s66 offen offset:3072 sc1 lds
v_mfma_f32_16x16x32_bf16 v[32:35],a[184:187],v[68:71],v[32:35]
ds_read_b128 v[64:67],v2 offset:63488)"
R"(
v_mul_f32 v24,v24,v72
v_mfma_f32_16x16x32_bf16 v[36:39],a[188:191],v[68:71],v[36:39]
s_waitcnt lgkmcnt(3)
v_exp_f32 v75,v75
v_mfma_f32_16x16x32_bf16 v[32:35],a[192:195],v[40:43],v[32:35]
s_waitcnt vmcnt(9)
ds_read_b128 v[68:71],v2 offset:64512
v_xor_b32 v2,65536,v2
v_mfma_f32_16x16x32_bf16 v[36:39],a[196:199],v[40:43],v[36:39]
s_barrier
v_mfma_f32_16x16x32_bf16 v[32:35],a[200:203],v[44:47],v[32:35]
v_fma_f32 v28,-v96,v20,v28
v_fma_f32 v29,-v96,v21,v29
v_fma_f32 v30,-v96,v22,v30
v_fma_f32 v31,-v96,v23,v31
v_mul_f32 v76,v28,v28
v_mul_f32 v77,v29,v29
v_mul_f32 v78,v30,v30
v_mul_f32 v79,v31,v31
v_fma_f32 v76,v98,v76,v97
v_fma_f32 v77,v98,v77,v97
v_fma_f32 v78,v98,v78,v97
v_fma_f32 v79,v98,v79,v97
v_mul_f32 v76,v28,v76
v_mul_f32 v77,v29,v77
v_mul_f32 v78,v30,v78
v_mul_f32 v79,v31,v79
v_exp_f32 v76,v76
v_exp_f32 v77,v77
v_exp_f32 v78,v78
v_exp_f32 v79,v79
v_mfma_f32_16x16x32_bf16 v[36:39],a[204:207],v[44:47],v[36:39]
ds_read_b128 v[40:43],v2 offset:0
v_fma_f32 v75,v75,v99,v99
v_rcp_f32 v75,v75
v_fma_f32 v76,v76,v99,v99
v_fma_f32 v77,v77,v99,v99
v_fma_f32 v78,v78,v99,v99
v_fma_f32 v79,v79,v99,v99
v_rcp_f32 v76,v76
v_rcp_f32 v77,v77
v_rcp_f32 v78,v78
v_rcp_f32 v79,v79
v_mfma_f32_16x16x32_bf16 v[32:35],a[208:211],v[48:51],v[32:35]
v_mul_f32 v25,v25,v73
v_mul_f32 v26,v26,v74
v_mul_f32 v27,v27,v75
v_cvt_pk_bf16_f32 v80,v24,v25
v_cvt_pk_bf16_f32 v81,v26,v27
v_mul_f32 v28,v28,v76
v_mul_f32 v29,v29,v77
v_mul_f32 v30,v30,v78
v_mul_f32 v31,v31,v79
v_cvt_pk_bf16_f32 v82,v28,v29
v_cvt_pk_bf16_f32 v83,v30,v31
v_mfma_f32_16x16x32_bf16 v[36:39],a[212:215],v[48:51],v[36:39]
ds_read_b128 v[44:47],v2 offset:1024
buffer_store_dwordx4 v[80:83],v3,s[36:39],s73 offen offset:0 sc0 sc1
ds_read_b64 v[92:93],v11 offset:128
v_xor_b32 v8,16384,v8
v_mfma_f32_16x16x32_bf16 v[32:35],a[216:219],v[52:55],v[32:35]
ds_read_b128 v[48:51],v2 offset:2048
v_mfma_f32_16x16x32_bf16 v[36:39],a[220:223],v[52:55],v[36:39]
s_waitcnt lgkmcnt(4)
v_mfma_f32_16x16x32_bf16 v[32:35],a[224:227],v[56:59],v[32:35]
ds_read_b128 v[52:55],v2 offset:3072
v_mfma_f32_16x16x32_bf16 v[36:39],a[228:231],v[56:59],v[36:39]
s_waitcnt lgkmcnt(2)
v_mfma_f32_16x16x32_bf16 v[32:35],a[232:235],v[60:63],v[32:35]
ds_read_b128 v[56:59],v2 offset:4096
v_mfma_f32_16x16x32_bf16 v[36:39],a[236:239],v[60:63],v[36:39]
v_mfma_f32_16x16x32_bf16 v[32:35],a[240:243],v[64:67],v[32:35]
ds_read_b128 v[60:63],v2 offset:5120
v_mfma_f32_16x16x32_bf16 v[36:39],a[244:247],v[64:67],v[36:39]
v_mfma_f32_16x16x32_bf16 v[32:35],a[248:251],v[68:71],v[32:35]
s_waitcnt lgkmcnt(2)
ds_read_b128 v[64:67],v2 offset:6144
v_mfma_f32_16x16x32_bf16 v[36:39],a[252:255],v[68:71],v[36:39]
s_cbranch_scc1 Lg3o_d0w1_%=
s_branch Lg3o_slow0_%=
Lg3o_d0w2_%=:
v_mfma_f32_16x16x32_bf16 v[24:27],a[0:3],v[40:43],0
s_add_u32 m0,s62,16384
ds_read_b128 v[68:71],v2 offset:7168
v_mul_f32 v96,s75,v92
v_mfma_f32_16x16x32_bf16 v[28:31],a[4:7],v[40:43],0
s_mul_i32 s73,s47,7
v_fma_f32 v93,v93,s75,v12
v_fma_f32 v93,-v96,v96,v93
v_mfma_f32_16x16x32_bf16 v[24:27],a[8:11],v[44:47],v[24:27]
s_lshr_b32 s73,s73,1
ds_read_b128 v[40:43],v2 offset:8192
v_max_f32 v93,v93,v12
v_mfma_f32_16x16x32_bf16 v[28:31],a[12:15],v[44:47],v[28:31]
buffer_load_dwordx4 v1,s[36:39],s66 offen offset:0 sc1 lds
v_mfma_f32_16x16x32_bf16 v[24:27],a[16:19],v[48:51],v[24:27]
s_lshr_b32 s72,s66,5
ds_read_b128 v[44:47],v2 offset:9216
v_fma_f32 v32,-v96,v16,v32
v_mfma_f32_16x16x32_bf16 v[28:31],a[20:23],v[48:51],v[28:31]
s_lshr_b32 s71,s62,2
v_rsq_f32 v92,v93
v_mfma_f32_16x16x32_bf16 v[24:27],a[24:27],v[52:55],v[24:27]
ds_read_b128 v[48:51],v2 offset:10240
v_mul_f32 v97,s76,v92
v_mfma_f32_16x16x32_bf16 v[28:31],a[28:31],v[52:55],v[28:31]
s_waitcnt lgkmcnt(3)
buffer_load_dwordx4 v1,s[36:39],s66 offen offset:1024 sc1 lds
v_mfma_f32_16x16x32_bf16 v[24:27],a[32:35],v[56:59],v[24:27]
ds_read_b128 v[52:55],v2 offset:11264
v_mul_f32 v98,v92,v92
v_mfma_f32_16x16x32_bf16 v[28:31],a[36:39],v[56:59],v[28:31]
v_mul_f32 v99,v93,v92
v_mul_f32 v98,s77,v98
v_mfma_f32_16x16x32_bf16 v[24:27],a[40:43],v[60:63],v[24:27]
ds_read_b128 v[56:59],v2 offset:12288
v_mul_f32 v98,v98,v97
v_mfma_f32_16x16x32_bf16 v[28:31],a[44:47],v[60:63],v[28:31]
buffer_load_dwordx4 v1,s[36:39],s66 offen offset:2048 sc1 lds
v_mfma_f32_16x16x32_bf16 v[24:27],a[48:51],v[64:67],v[24:27]
ds_read_b128 v[60:63],v2 offset:13312
v_fma_f32 v33,-v96,v17,v33
v_mfma_f32_16x16x32_bf16 v[28:31],a[52:55],v[64:67],v[28:31]
v_fma_f32 v34,-v96,v18,v34
v_fma_f32 v35,-v96,v19,v35
v_mfma_f32_16x16x32_bf16 v[24:27],a[56:59],v[68:71],v[24:27]
ds_read_b128 v[64:67],v2 offset:14336
v_mul_f32 v72,v32,v32
v_mfma_f32_16x16x32_bf16 v[28:31],a[60:63],v[68:71],v[28:31]
s_waitcnt lgkmcnt(3)
buffer_load_dwordx4 v1,s[36:39],s66 offen offset:3072 sc1 lds
s_add_u32 m0,s71,s88
v_mfma_f32_16x16x32_bf16 v[24:27],a[64:67],v[40:43],v[24:27]
s_xor_b32 s87,s64,65536
ds_read_b128 v[68:71],v2 offset:15360
buffer_load_dword v9,s[36:39],s72 offen offset:0 sc1 lds
s_waitcnt vmcnt(10)
v_mfma_f32_16x16x32_bf16 v[28:31],a[68:71],v[40:43],v[28:31]
s_barrier
v_mfma_f32_16x16x32_bf16 v[24:27],a[72:75],v[44:47],v[24:27]
s_cmp_ge_u32 s66,s59
ds_read_b128 v[40:43],v2 offset:16384
ds_read_b32 v88,v8 offset:0
v_mfma_f32_16x16x32_bf16 v[28:31],a[76:79],v[44:47],v[28:31]
s_cselect_b32 s86,s49,s50
v_mul_f32 v73,v33,v33
v_mul_f32 v74,v34,v34
v_mfma_f32_16x16x32_bf16 v[24:27],a[80:83],v[48:51],v[24:27]
s_cselect_b32 s87,s87,s63
ds_read_b128 v[44:47],v2 offset:17408
v_mul_f32 v75,v35,v35
v_mfma_f32_16x16x32_bf16 v[28:31],a[84:87],v[48:51],v[28:31]
s_mov_b32 s62,s87
v_fma_f32 v72,v98,v72,v97
v_mul_f32 v72,v32,v72
v_mfma_f32_16x16x32_bf16 v[24:27],a[88:91],v[52:55],v[24:27]
s_mov_b32 m0,s62
ds_read_b128 v[48:51],v2 offset:18432
v_fma_f32 v73,v98,v73,v97
v_mfma_f32_16x16x32_bf16 v[28:31],a[92:95],v[52:55],v[28:31]
s_waitcnt lgkmcnt(4)
v_exp_f32 v72,v72
v_mfma_f32_16x16x32_bf16 v[24:27],a[96:99],v[56:59],v[24:27]
s_nop 0
s_waitcnt lgkmcnt(2)
ds_read_b128 v[52:55],v2 offset:19456
v_mul_f32 v73,v33,v73
v_mfma_f32_16x16x32_bf16 v[28:31],a[100:103],v[56:59],v[28:31]
s_barrier
v_mfma_f32_16x16x32_bf16 v[24:27],a[104:107],v[60:63],v[24:27]
ds_read_b128 v[56:59],v2 offset:20480
ds_read_b32 v13,v7 offset:0
v_mfma_f32_16x16x32_bf16 v[28:31],a[108:111],v[60:63],v[28:31]
v_exp_f32 v73,v73
v_mfma_f32_16x16x32_bf16 v[24:27],a[112:115],v[64:67],v[24:27]
ds_read_b128 v[60:63],v2 offset:21504
v_fma_f32 v72,v72,v99,v99
v_mfma_f32_16x16x32_bf16 v[28:31],a[116:119],v[64:67],v[28:31]
v_rcp_f32 v72,v72
v_mfma_f32_16x16x32_bf16 v[24:27],a[120:123],v[68:71],v[24:27]
s_waitcnt lgkmcnt(1)
ds_read_b128 v[64:67],v2 offset:22528
v_fma_f32 v73,v73,v99,v99
v_mfma_f32_16x16x32_bf16 v[28:31],a[124:127],v[68:71],v[28:31]
v_cmp_eq_u32 vcc,s60,v13
v_rcp_f32 v73,v73
v_add_f32_dpp v88,v88,v88 quad_perm:[1,0,3,2] row_mask:0xf bank_mask:0xf
v_mfma_f32_16x16x32_bf16 v[24:27],a[128:131],v[40:43],v[24:27]
s_cmp_eq_u32 vcc_lo,-1
ds_read_b128 v[68:71],v2 offset:23552
v_add_f32_dpp v88,v88,v88 quad_perm:[2,3,0,1] row_mask:0xf bank_mask:0xf
v_mfma_f32_16x16x32_bf16 v[28:31],a[132:135],v[40:43],v[28:31]
s_cselect_b32 s67,s86,s59
v_add_f32_dpp v88,v88,v88 row_half_mirror row_mask:0xf bank_mask:0xf
v_fma_f32 v74,v98,v74,v97
v_mfma_f32_16x16x32_bf16 v[24:27],a[136:139],v[44:47],v[24:27]
buffer_load_dwordx4 v0,s[36:39],s67 offen offset:0 sc1 lds
v_mfma_f32_16x16x32_bf16 v[28:31],a[140:143],v[44:47],v[28:31]
s_xor_b32 s63,s63,65536
ds_read_b128 v[40:43],v2 offset:24576)"
R"(
v_mul_f32 v74,v34,v74
v_mfma_f32_16x16x32_bf16 v[24:27],a[144:147],v[48:51],v[24:27]
s_add_u32 s53,s52,s55
ds_write_b32 v10,v88 offset:0
v_mfma_f32_16x16x32_bf16 v[28:31],a[148:151],v[48:51],v[28:31]
s_cmp_eq_u32 s53,s56
ds_read_b128 v[44:47],v2 offset:25600
v_fma_f32 v75,v98,v75,v97
v_mfma_f32_16x16x32_bf16 v[24:27],a[152:155],v[52:55],v[24:27]
buffer_load_dwordx4 v0,s[36:39],s67 offen offset:1024 sc1 lds
v_mfma_f32_16x16x32_bf16 v[28:31],a[156:159],v[52:55],v[28:31]
s_waitcnt lgkmcnt(3)
ds_read_b128 v[48:51],v2 offset:26624
v_mul_f32 v75,v35,v75
v_mfma_f32_16x16x32_bf16 v[24:27],a[160:163],v[56:59],v[24:27]
s_cselect_b32 s53,s57,s53
ds_read_b128 v[52:55],v2 offset:27648
v_mul_f32 v32,v32,v72
v_mfma_f32_16x16x32_bf16 v[28:31],a[164:167],v[56:59],v[28:31]
s_cmp_eq_u32 s53,s58
v_exp_f32 v74,v74
v_mfma_f32_16x16x32_bf16 v[24:27],a[168:171],v[60:63],v[24:27]
buffer_load_dwordx4 v0,s[36:39],s67 offen offset:2048 sc1 lds
v_mfma_f32_16x16x32_bf16 v[28:31],a[172:175],v[60:63],v[28:31]
s_cselect_b32 s53,s59,s53
ds_read_b128 v[56:59],v2 offset:28672
v_fma_f32 v74,v74,v99,v99
v_mfma_f32_16x16x32_bf16 v[24:27],a[176:179],v[64:67],v[24:27]
s_cmp_ge_u32 s66,s59
ds_read_b128 v[60:63],v2 offset:29696
v_mul_f32 v33,v33,v73
v_mfma_f32_16x16x32_bf16 v[28:31],a[180:183],v[64:67],v[28:31]
v_exp_f32 v75,v75
v_mfma_f32_16x16x32_bf16 v[24:27],a[184:187],v[68:71],v[24:27]
buffer_load_dwordx4 v0,s[36:39],s67 offen offset:3072 sc1 lds
v_mfma_f32_16x16x32_bf16 v[28:31],a[188:191],v[68:71],v[28:31]
s_waitcnt lgkmcnt(2)
ds_read_b128 v[64:67],v2 offset:30720
v_fma_f32 v75,v75,v99,v99
v_mfma_f32_16x16x32_bf16 v[24:27],a[192:195],v[40:43],v[24:27]
s_waitcnt vmcnt(9)
ds_read_b128 v[68:71],v2 offset:31744
v_cvt_pk_bf16_f32 v80,v32,v33
v_mfma_f32_16x16x32_bf16 v[28:31],a[196:199],v[40:43],v[28:31]
s_barrier
v_mfma_f32_16x16x32_bf16 v[24:27],a[200:203],v[44:47],v[24:27]
ds_read_b128 v[40:43],v2 offset:32768
ds_read_b64 v[92:93],v11 offset:0
v_mfma_f32_16x16x32_bf16 v[28:31],a[204:207],v[44:47],v[28:31]
v_fma_f32 v36,-v96,v20,v36
v_fma_f32 v37,-v96,v21,v37
v_fma_f32 v38,-v96,v22,v38
v_fma_f32 v39,-v96,v23,v39
v_mul_f32 v76,v36,v36
v_mul_f32 v77,v37,v37
v_mul_f32 v78,v38,v38
v_mul_f32 v79,v39,v39
v_fma_f32 v76,v98,v76,v97
v_fma_f32 v77,v98,v77,v97
v_fma_f32 v78,v98,v78,v97
v_fma_f32 v79,v98,v79,v97
v_mul_f32 v76,v36,v76
v_mul_f32 v77,v37,v77
v_mul_f32 v78,v38,v78
v_mul_f32 v79,v39,v79
v_exp_f32 v76,v76
v_exp_f32 v77,v77
v_exp_f32 v78,v78
v_exp_f32 v79,v79
v_mfma_f32_16x16x32_bf16 v[24:27],a[208:211],v[48:51],v[24:27]
v_rcp_f32 v74,v74
v_rcp_f32 v75,v75
v_fma_f32 v76,v76,v99,v99
v_fma_f32 v77,v77,v99,v99
v_fma_f32 v78,v78,v99,v99
v_fma_f32 v79,v79,v99,v99
v_rcp_f32 v76,v76
v_rcp_f32 v77,v77
v_rcp_f32 v78,v78
v_rcp_f32 v79,v79
v_mfma_f32_16x16x32_bf16 v[28:31],a[212:215],v[48:51],v[28:31]
ds_read_b128 v[44:47],v2 offset:33792
v_mul_f32 v34,v34,v74
v_mul_f32 v35,v35,v75
v_cvt_pk_bf16_f32 v81,v34,v35
v_mul_f32 v36,v36,v76
v_mul_f32 v37,v37,v77
v_mul_f32 v38,v38,v78
v_mul_f32 v39,v39,v79
v_cvt_pk_bf16_f32 v82,v36,v37
v_cvt_pk_bf16_f32 v83,v38,v39
v_mfma_f32_16x16x32_bf16 v[24:27],a[216:219],v[52:55],v[24:27]
s_waitcnt lgkmcnt(1)
buffer_store_dwordx4 v[80:83],v3,s[36:39],s73 offen offset:0 sc0 sc1
ds_read_b128 v[48:51],v2 offset:34816
v_mfma_f32_16x16x32_bf16 v[28:31],a[220:223],v[52:55],v[28:31]
v_mfma_f32_16x16x32_bf16 v[24:27],a[224:227],v[56:59],v[24:27]
ds_read_b128 v[52:55],v2 offset:35840
v_mfma_f32_16x16x32_bf16 v[28:31],a[228:231],v[56:59],v[28:31]
v_mfma_f32_16x16x32_bf16 v[24:27],a[232:235],v[60:63],v[24:27]
ds_read_b128 v[56:59],v2 offset:36864
v_mfma_f32_16x16x32_bf16 v[28:31],a[236:239],v[60:63],v[28:31]
v_mfma_f32_16x16x32_bf16 v[24:27],a[240:243],v[64:67],v[24:27]
ds_read_b128 v[60:63],v2 offset:37888
v_mfma_f32_16x16x32_bf16 v[28:31],a[244:247],v[64:67],v[28:31]
v_mfma_f32_16x16x32_bf16 v[24:27],a[248:251],v[68:71],v[24:27]
s_waitcnt lgkmcnt(2)
ds_read_b128 v[64:67],v2 offset:38912
v_mfma_f32_16x16x32_bf16 v[28:31],a[252:255],v[68:71],v[28:31]
s_cbranch_scc1 Lg3o_slow1_%=
Lg3o_d1w2_%=:
v_mfma_f32_16x16x32_bf16 v[32:35],a[0:3],v[40:43],0
s_add_u32 m0,s62,16384
ds_read_b128 v[68:71],v2 offset:39936
v_mul_f32 v96,s75,v92
v_mfma_f32_16x16x32_bf16 v[36:39],a[4:7],v[40:43],0
s_mul_i32 s73,s48,7
v_fma_f32 v93,v93,s75,v12
v_fma_f32 v93,-v96,v96,v93
v_mfma_f32_16x16x32_bf16 v[32:35],a[8:11],v[44:47],v[32:35]
s_lshr_b32 s73,s73,1
ds_read_b128 v[40:43],v2 offset:40960
v_max_f32 v93,v93,v12
v_mfma_f32_16x16x32_bf16 v[36:39],a[12:15],v[44:47],v[36:39]
buffer_load_dwordx4 v1,s[36:39],s67 offen offset:0 sc1 lds
v_mfma_f32_16x16x32_bf16 v[32:35],a[16:19],v[48:51],v[32:35]
s_lshr_b32 s72,s67,5
ds_read_b128 v[44:47],v2 offset:41984
v_fma_f32 v24,-v96,v16,v24
v_mfma_f32_16x16x32_bf16 v[36:39],a[20:23],v[48:51],v[36:39]
s_lshr_b32 s71,s62,2
v_rsq_f32 v92,v93
v_mfma_f32_16x16x32_bf16 v[32:35],a[24:27],v[52:55],v[32:35]
s_mov_b64 s[44:45],s[46:47]
ds_read_b128 v[48:51],v2 offset:43008
v_mul_f32 v97,s76,v92
v_mfma_f32_16x16x32_bf16 v[36:39],a[28:31],v[52:55],v[36:39]
s_waitcnt lgkmcnt(3)
buffer_load_dwordx4 v1,s[36:39],s67 offen offset:1024 sc1 lds
v_mfma_f32_16x16x32_bf16 v[32:35],a[32:35],v[56:59],v[32:35]
s_mov_b64 s[46:47],s[48:49]
ds_read_b128 v[52:55],v2 offset:44032
v_mul_f32 v98,v92,v92
v_mfma_f32_16x16x32_bf16 v[36:39],a[36:39],v[56:59],v[36:39]
s_mov_b64 s[48:49],s[50:51]
v_mul_f32 v99,v93,v92
v_mul_f32 v98,s77,v98
v_mfma_f32_16x16x32_bf16 v[32:35],a[40:43],v[60:63],v[32:35]
ds_read_b128 v[56:59],v2 offset:45056
v_mul_f32 v98,v98,v97
v_mfma_f32_16x16x32_bf16 v[36:39],a[44:47],v[60:63],v[36:39]
buffer_load_dwordx4 v1,s[36:39],s67 offen offset:2048 sc1 lds
v_mfma_f32_16x16x32_bf16 v[32:35],a[48:51],v[64:67],v[32:35]
ds_read_b128 v[60:63],v2 offset:46080
v_fma_f32 v25,-v96,v17,v25
v_mfma_f32_16x16x32_bf16 v[36:39],a[52:55],v[64:67],v[36:39]
v_fma_f32 v26,-v96,v18,v26
v_fma_f32 v27,-v96,v19,v27
v_mfma_f32_16x16x32_bf16 v[32:35],a[56:59],v[68:71],v[32:35]
ds_read_b128 v[64:67],v2 offset:47104
v_mul_f32 v72,v24,v24
v_mfma_f32_16x16x32_bf16 v[36:39],a[60:63],v[68:71],v[36:39]
s_waitcnt lgkmcnt(3)
buffer_load_dwordx4 v1,s[36:39],s67 offen offset:3072 sc1 lds
s_add_u32 m0,s71,s88
v_mfma_f32_16x16x32_bf16 v[32:35],a[64:67],v[40:43],v[32:35]
s_xor_b32 s87,s63,65536
ds_read_b128 v[68:71],v2 offset:48128
buffer_load_dword v9,s[36:39],s72 offen offset:0 sc1 lds
s_waitcnt vmcnt(10)
v_mfma_f32_16x16x32_bf16 v[36:39],a[68:71],v[40:43],v[36:39]
s_barrier
v_mfma_f32_16x16x32_bf16 v[32:35],a[72:75],v[44:47],v[32:35]
s_cmp_ge_u32 s67,s59
ds_read_b128 v[40:43],v2 offset:49152
ds_read_b32 v88,v8 offset:8192
v_mfma_f32_16x16x32_bf16 v[36:39],a[76:79],v[44:47],v[36:39]
s_cselect_b32 s86,s50,s51
v_mul_f32 v73,v25,v25
v_mul_f32 v74,v26,v26
v_mfma_f32_16x16x32_bf16 v[32:35],a[80:83],v[48:51],v[32:35]
s_cselect_b32 s87,s87,s64
ds_read_b128 v[44:47],v2 offset:50176
v_mul_f32 v75,v27,v27
v_mfma_f32_16x16x32_bf16 v[36:39],a[84:87],v[48:51],v[36:39]
s_mov_b32 s62,s87
v_fma_f32 v72,v98,v72,v97
v_mul_f32 v72,v24,v72
v_mfma_f32_16x16x32_bf16 v[32:35],a[88:91],v[52:55],v[32:35]
s_mov_b32 m0,s62
ds_read_b128 v[48:51],v2 offset:51200
v_fma_f32 v73,v98,v73,v97
v_mfma_f32_16x16x32_bf16 v[36:39],a[92:95],v[52:55],v[36:39]
s_waitcnt lgkmcnt(4)
v_exp_f32 v72,v72
v_mfma_f32_16x16x32_bf16 v[32:35],a[96:99],v[56:59],v[32:35]
s_nop 0
s_waitcnt lgkmcnt(2)
ds_read_b128 v[52:55],v2 offset:52224
v_mul_f32 v73,v25,v73)"
R"(
v_mfma_f32_16x16x32_bf16 v[36:39],a[100:103],v[56:59],v[36:39]
s_barrier
v_mfma_f32_16x16x32_bf16 v[32:35],a[104:107],v[60:63],v[32:35]
s_mov_b64 s[50:51],s[52:53]
ds_read_b128 v[56:59],v2 offset:53248
ds_read_b32 v13,v7 offset:256
v_mfma_f32_16x16x32_bf16 v[36:39],a[108:111],v[60:63],v[36:39]
v_exp_f32 v73,v73
v_mfma_f32_16x16x32_bf16 v[32:35],a[112:115],v[64:67],v[32:35]
ds_read_b128 v[60:63],v2 offset:54272
v_fma_f32 v72,v72,v99,v99
v_mfma_f32_16x16x32_bf16 v[36:39],a[116:119],v[64:67],v[36:39]
v_rcp_f32 v72,v72
v_mfma_f32_16x16x32_bf16 v[32:35],a[120:123],v[68:71],v[32:35]
s_waitcnt lgkmcnt(1)
ds_read_b128 v[64:67],v2 offset:55296
v_fma_f32 v73,v73,v99,v99
v_mfma_f32_16x16x32_bf16 v[36:39],a[124:127],v[68:71],v[36:39]
v_cmp_eq_u32 vcc,s60,v13
v_rcp_f32 v73,v73
v_add_f32_dpp v88,v88,v88 quad_perm:[1,0,3,2] row_mask:0xf bank_mask:0xf
v_mfma_f32_16x16x32_bf16 v[32:35],a[128:131],v[40:43],v[32:35]
s_cmp_eq_u32 vcc_lo,-1
ds_read_b128 v[68:71],v2 offset:56320
v_add_f32_dpp v88,v88,v88 quad_perm:[2,3,0,1] row_mask:0xf bank_mask:0xf
v_mfma_f32_16x16x32_bf16 v[36:39],a[132:135],v[40:43],v[36:39]
s_cselect_b32 s66,s86,s59
v_add_f32_dpp v88,v88,v88 row_half_mirror row_mask:0xf bank_mask:0xf
v_fma_f32 v74,v98,v74,v97
v_mfma_f32_16x16x32_bf16 v[32:35],a[136:139],v[44:47],v[32:35]
buffer_load_dwordx4 v0,s[36:39],s66 offen offset:0 sc1 lds
v_mfma_f32_16x16x32_bf16 v[36:39],a[140:143],v[44:47],v[36:39]
s_xor_b32 s64,s64,65536
ds_read_b128 v[40:43],v2 offset:57344
v_mul_f32 v74,v26,v74
v_mfma_f32_16x16x32_bf16 v[32:35],a[144:147],v[48:51],v[32:35]
s_add_u32 s74,s53,s55
ds_write_b32 v10,v88 offset:128
v_mfma_f32_16x16x32_bf16 v[36:39],a[148:151],v[48:51],v[36:39]
s_cmp_eq_u32 s74,s56
ds_read_b128 v[44:47],v2 offset:58368
v_fma_f32 v75,v98,v75,v97
v_mfma_f32_16x16x32_bf16 v[32:35],a[152:155],v[52:55],v[32:35]
buffer_load_dwordx4 v0,s[36:39],s66 offen offset:1024 sc1 lds
v_mfma_f32_16x16x32_bf16 v[36:39],a[156:159],v[52:55],v[36:39]
s_waitcnt lgkmcnt(3)
ds_read_b128 v[48:51],v2 offset:59392
v_mul_f32 v75,v27,v75
v_mfma_f32_16x16x32_bf16 v[32:35],a[160:163],v[56:59],v[32:35]
s_cselect_b32 s74,s57,s74
ds_read_b128 v[52:55],v2 offset:60416
v_mul_f32 v24,v24,v72
v_mfma_f32_16x16x32_bf16 v[36:39],a[164:167],v[56:59],v[36:39]
s_cmp_eq_u32 s74,s58
v_exp_f32 v74,v74
v_mfma_f32_16x16x32_bf16 v[32:35],a[168:171],v[60:63],v[32:35]
buffer_load_dwordx4 v0,s[36:39],s66 offen offset:2048 sc1 lds
v_mfma_f32_16x16x32_bf16 v[36:39],a[172:175],v[60:63],v[36:39]
s_cselect_b32 s74,s59,s74
ds_read_b128 v[56:59],v2 offset:61440
v_fma_f32 v74,v74,v99,v99
v_mfma_f32_16x16x32_bf16 v[32:35],a[176:179],v[64:67],v[32:35]
s_mov_b32 s52,s74
ds_read_b128 v[60:63],v2 offset:62464
v_mul_f32 v25,v25,v73
v_mfma_f32_16x16x32_bf16 v[36:39],a[180:183],v[64:67],v[36:39]
s_cmp_lt_u32 s67,s59
v_exp_f32 v75,v75
v_mfma_f32_16x16x32_bf16 v[32:35],a[184:187],v[68:71],v[32:35]
buffer_load_dwordx4 v0,s[36:39],s66 offen offset:3072 sc1 lds
v_mfma_f32_16x16x32_bf16 v[36:39],a[188:191],v[68:71],v[36:39]
s_waitcnt lgkmcnt(2)
ds_read_b128 v[64:67],v2 offset:63488
v_fma_f32 v75,v75,v99,v99
v_mfma_f32_16x16x32_bf16 v[32:35],a[192:195],v[40:43],v[32:35]
s_waitcnt vmcnt(9)
ds_read_b128 v[68:71],v2 offset:64512
v_xor_b32 v2,65536,v2
v_mfma_f32_16x16x32_bf16 v[36:39],a[196:199],v[40:43],v[36:39]
s_barrier
v_mfma_f32_16x16x32_bf16 v[32:35],a[200:203],v[44:47],v[32:35]
ds_read_b128 v[40:43],v2 offset:0
ds_read_b64 v[92:93],v11 offset:128
v_mfma_f32_16x16x32_bf16 v[36:39],a[204:207],v[44:47],v[36:39]
v_fma_f32 v28,-v96,v20,v28
v_fma_f32 v29,-v96,v21,v29
v_fma_f32 v30,-v96,v22,v30
v_fma_f32 v31,-v96,v23,v31
v_mul_f32 v76,v28,v28
v_mul_f32 v77,v29,v29
v_mul_f32 v78,v30,v30
v_mul_f32 v79,v31,v31
v_fma_f32 v76,v98,v76,v97
v_fma_f32 v77,v98,v77,v97
v_fma_f32 v78,v98,v78,v97
v_fma_f32 v79,v98,v79,v97
v_mul_f32 v76,v28,v76
v_mul_f32 v77,v29,v77
v_mul_f32 v78,v30,v78
v_mul_f32 v79,v31,v79
v_exp_f32 v76,v76
v_exp_f32 v77,v77
v_exp_f32 v78,v78
v_exp_f32 v79,v79
v_mfma_f32_16x16x32_bf16 v[32:35],a[208:211],v[48:51],v[32:35]
v_rcp_f32 v74,v74
v_rcp_f32 v75,v75
v_fma_f32 v76,v76,v99,v99
v_fma_f32 v77,v77,v99,v99
v_fma_f32 v78,v78,v99,v99
v_fma_f32 v79,v79,v99,v99
v_rcp_f32 v76,v76
v_rcp_f32 v77,v77
v_rcp_f32 v78,v78
v_rcp_f32 v79,v79
v_mfma_f32_16x16x32_bf16 v[36:39],a[212:215],v[48:51],v[36:39]
ds_read_b128 v[44:47],v2 offset:1024
v_mul_f32 v26,v26,v74
v_mul_f32 v27,v27,v75
v_cvt_pk_bf16_f32 v80,v24,v25
v_cvt_pk_bf16_f32 v81,v26,v27
v_mul_f32 v28,v28,v76
v_mul_f32 v29,v29,v77
v_mul_f32 v30,v30,v78
v_mul_f32 v31,v31,v79
v_cvt_pk_bf16_f32 v82,v28,v29
v_cvt_pk_bf16_f32 v83,v30,v31
v_mfma_f32_16x16x32_bf16 v[32:35],a[216:219],v[52:55],v[32:35]
s_waitcnt lgkmcnt(1)
buffer_store_dwordx4 v[80:83],v3,s[36:39],s73 offen offset:0 sc0 sc1
ds_read_b128 v[48:51],v2 offset:2048
v_xor_b32 v8,16384,v8
v_mfma_f32_16x16x32_bf16 v[36:39],a[220:223],v[52:55],v[36:39]
v_mfma_f32_16x16x32_bf16 v[32:35],a[224:227],v[56:59],v[32:35]
ds_read_b128 v[52:55],v2 offset:3072
v_mfma_f32_16x16x32_bf16 v[36:39],a[228:231],v[56:59],v[36:39]
v_mfma_f32_16x16x32_bf16 v[32:35],a[232:235],v[60:63],v[32:35]
ds_read_b128 v[56:59],v2 offset:4096
v_mfma_f32_16x16x32_bf16 v[36:39],a[236:239],v[60:63],v[36:39]
v_mfma_f32_16x16x32_bf16 v[32:35],a[240:243],v[64:67],v[32:35]
ds_read_b128 v[60:63],v2 offset:5120
v_mfma_f32_16x16x32_bf16 v[36:39],a[244:247],v[64:67],v[36:39]
v_mfma_f32_16x16x32_bf16 v[32:35],a[248:251],v[68:71],v[32:35]
s_waitcnt lgkmcnt(2)
ds_read_b128 v[64:67],v2 offset:6144
v_mfma_f32_16x16x32_bf16 v[36:39],a[252:255],v[68:71],v[36:39]
s_cbranch_scc1 Lg3o_d0w2_%=
s_branch Lg3o_slow0_%=
Lg3o_d0w3_%=:
v_mfma_f32_16x16x32_bf16 v[24:27],a[0:3],v[40:43],0
s_add_u32 m0,s62,16384
ds_read_b128 v[68:71],v2 offset:7168
v_mul_f32 v96,s75,v92
v_mfma_f32_16x16x32_bf16 v[28:31],a[4:7],v[40:43],0
s_mul_i32 s73,s47,7
v_fma_f32 v93,v93,s75,v12
v_fma_f32 v93,-v96,v96,v93
v_mfma_f32_16x16x32_bf16 v[24:27],a[8:11],v[44:47],v[24:27]
s_lshr_b32 s73,s73,1
ds_read_b128 v[40:43],v2 offset:8192
v_max_f32 v93,v93,v12
v_mfma_f32_16x16x32_bf16 v[28:31],a[12:15],v[44:47],v[28:31]
s_lshr_b32 s72,s66,5
v_rsq_f32 v92,v93
v_mfma_f32_16x16x32_bf16 v[24:27],a[16:19],v[48:51],v[24:27]
buffer_load_dwordx4 v1,s[36:39],s66 offen offset:0 sc1 lds
v_mfma_f32_16x16x32_bf16 v[28:31],a[20:23],v[48:51],v[28:31]
s_lshr_b32 s71,s62,2
ds_read_b128 v[44:47],v2 offset:9216
v_mul_f32 v97,s76,v92
v_mfma_f32_16x16x32_bf16 v[24:27],a[24:27],v[52:55],v[24:27]
s_mul_i32 s70,s46,5
ds_read_b128 v[48:51],v2 offset:10240
v_mul_f32 v98,v92,v92
v_mfma_f32_16x16x32_bf16 v[28:31],a[28:31],v[52:55],v[28:31]
s_waitcnt lgkmcnt(3)
v_mul_f32 v99,v93,v92
v_mul_f32 v98,s77,v98
v_mfma_f32_16x16x32_bf16 v[24:27],a[32:35],v[56:59],v[24:27]
buffer_load_dwordx4 v1,s[36:39],s66 offen offset:1024 sc1 lds
v_mfma_f32_16x16x32_bf16 v[28:31],a[36:39],v[56:59],v[28:31]
ds_read_b128 v[52:55],v2 offset:11264
v_mul_f32 v98,v98,v97
v_mfma_f32_16x16x32_bf16 v[24:27],a[40:43],v[60:63],v[24:27]
ds_read_b128 v[56:59],v2 offset:12288
v_fma_f32 v32,-v96,v16,v32
v_mfma_f32_16x16x32_bf16 v[28:31],a[44:47],v[60:63],v[28:31]
v_fma_f32 v33,-v96,v17,v33
v_fma_f32 v34,-v96,v18,v34
v_mfma_f32_16x16x32_bf16 v[24:27],a[48:51],v[64:67],v[24:27]
buffer_load_dwordx4 v1,s[36:39],s66 offen offset:2048 sc1 lds
v_mfma_f32_16x16x32_bf16 v[28:31],a[52:55],v[64:67],v[28:31]
ds_read_b128 v[60:63],v2 offset:13312
v_fma_f32 v35,-v96,v19,v35
v_mfma_f32_16x16x32_bf16 v[24:27],a[56:59],v[68:71],v[24:27]
ds_read_b128 v[64:67],v2 offset:14336
v_mul_f32 v72,v32,v32
v_mfma_f32_16x16x32_bf16 v[28:31],a[60:63],v[68:71],v[28:31]
s_waitcnt lgkmcnt(3)
v_mul_f32 v73,v33,v33)"
R"(
v_mul_f32 v74,v34,v34
v_mfma_f32_16x16x32_bf16 v[24:27],a[64:67],v[40:43],v[24:27]
s_waitcnt vmcnt(9)
ds_read_b128 v[68:71],v2 offset:15360
v_mul_f32 v75,v35,v35
v_mfma_f32_16x16x32_bf16 v[28:31],a[68:71],v[40:43],v[28:31]
s_barrier
v_mfma_f32_16x16x32_bf16 v[24:27],a[72:75],v[44:47],v[24:27]
buffer_load_dwordx4 v1,s[36:39],s66 offen offset:3072 sc1 lds
v_mfma_f32_16x16x32_bf16 v[28:31],a[76:79],v[44:47],v[28:31]
s_add_u32 m0,s71,s88
ds_read_b128 v[40:43],v2 offset:16384
ds_read_b32 v88,v8 offset:0
v_mfma_f32_16x16x32_bf16 v[24:27],a[80:83],v[48:51],v[24:27]
buffer_load_dword v9,s[36:39],s72 offen offset:0 sc1 lds
v_mfma_f32_16x16x32_bf16 v[28:31],a[84:87],v[48:51],v[28:31]
s_xor_b32 s87,s64,65536
ds_read_b128 v[44:47],v2 offset:17408
v_fma_f32 v72,v98,v72,v97
v_mfma_f32_16x16x32_bf16 v[24:27],a[88:91],v[52:55],v[24:27]
s_cmp_ge_u32 s66,s59
ds_read_b128 v[48:51],v2 offset:18432
v_mul_f32 v72,v32,v72
v_mfma_f32_16x16x32_bf16 v[28:31],a[92:95],v[52:55],v[28:31]
s_waitcnt lgkmcnt(4)
v_exp_f32 v72,v72
v_mfma_f32_16x16x32_bf16 v[24:27],a[96:99],v[56:59],v[24:27]
s_nop 0
ds_read_b128 v[52:55],v2 offset:19456
v_fma_f32 v72,v72,v99,v99
v_mfma_f32_16x16x32_bf16 v[28:31],a[100:103],v[56:59],v[28:31]
s_barrier
v_mfma_f32_16x16x32_bf16 v[24:27],a[104:107],v[60:63],v[24:27]
s_waitcnt lgkmcnt(3)
ds_read_b128 v[56:59],v2 offset:20480
ds_read_b32 v13,v7 offset:0
v_mfma_f32_16x16x32_bf16 v[28:31],a[108:111],v[60:63],v[28:31]
s_cselect_b32 s86,s49,s50
v_rcp_f32 v72,v72
v_mfma_f32_16x16x32_bf16 v[24:27],a[112:115],v[64:67],v[24:27]
s_cselect_b32 s87,s87,s63
ds_read_b128 v[60:63],v2 offset:21504
v_add_f32_dpp v88,v88,v88 quad_perm:[1,0,3,2] row_mask:0xf bank_mask:0xf
v_mfma_f32_16x16x32_bf16 v[28:31],a[116:119],v[64:67],v[28:31]
s_mov_b32 s62,s87
v_add_f32_dpp v88,v88,v88 quad_perm:[2,3,0,1] row_mask:0xf bank_mask:0xf
v_fma_f32 v73,v98,v73,v97
v_mfma_f32_16x16x32_bf16 v[24:27],a[120:123],v[68:71],v[24:27]
s_waitcnt lgkmcnt(1)
ds_read_b128 v[64:67],v2 offset:22528
v_mul_f32 v73,v33,v73
v_mfma_f32_16x16x32_bf16 v[28:31],a[124:127],v[68:71],v[28:31]
v_cmp_eq_u32 vcc,s60,v13
v_exp_f32 v73,v73
v_add_f32_dpp v88,v88,v88 row_half_mirror row_mask:0xf bank_mask:0xf
v_mfma_f32_16x16x32_bf16 v[24:27],a[128:131],v[40:43],v[24:27]
s_cmp_eq_u32 vcc_lo,-1
ds_write_b32 v10,v88 offset:0
v_mfma_f32_16x16x32_bf16 v[28:31],a[132:135],v[40:43],v[28:31]
s_cselect_b32 s67,s86,s59
ds_read_b128 v[68:71],v2 offset:23552
v_fma_f32 v73,v73,v99,v99
v_mfma_f32_16x16x32_bf16 v[24:27],a[136:139],v[44:47],v[24:27]
s_mov_b32 m0,s62
ds_read_b128 v[40:43],v2 offset:24576
v_fma_f32 v74,v98,v74,v97
v_mfma_f32_16x16x32_bf16 v[28:31],a[140:143],v[44:47],v[28:31]
buffer_load_dwordx4 v0,s[36:39],s67 offen offset:0 sc1 lds
v_mfma_f32_16x16x32_bf16 v[24:27],a[144:147],v[48:51],v[24:27]
s_xor_b32 s63,s63,65536
ds_read_b128 v[44:47],v2 offset:25600
v_mul_f32 v74,v34,v74
v_mfma_f32_16x16x32_bf16 v[28:31],a[148:151],v[48:51],v[28:31]
s_add_u32 s53,s52,s55
v_exp_f32 v74,v74
v_mfma_f32_16x16x32_bf16 v[24:27],a[152:155],v[52:55],v[24:27]
s_cmp_eq_u32 s53,s56
ds_read_b128 v[48:51],v2 offset:26624
v_fma_f32 v74,v74,v99,v99
v_mfma_f32_16x16x32_bf16 v[28:31],a[156:159],v[52:55],v[28:31]
s_waitcnt lgkmcnt(3)
buffer_load_dwordx4 v0,s[36:39],s67 offen offset:1024 sc1 lds
v_mfma_f32_16x16x32_bf16 v[24:27],a[160:163],v[56:59],v[24:27]
s_cselect_b32 s53,s57,s53
ds_read_b128 v[52:55],v2 offset:27648
v_fma_f32 v75,v98,v75,v97
v_mfma_f32_16x16x32_bf16 v[28:31],a[164:167],v[56:59],v[28:31]
s_cmp_eq_u32 s53,s58
v_rcp_f32 v73,v73
v_mfma_f32_16x16x32_bf16 v[24:27],a[168:171],v[60:63],v[24:27]
s_cselect_b32 s53,s59,s53
ds_read_b128 v[56:59],v2 offset:28672
v_mul_f32 v75,v35,v75
v_mfma_f32_16x16x32_bf16 v[28:31],a[172:175],v[60:63],v[28:31]
buffer_load_dwordx4 v0,s[36:39],s67 offen offset:2048 sc1 lds
v_mfma_f32_16x16x32_bf16 v[24:27],a[176:179],v[64:67],v[24:27]
s_lshr_b32 s70,s70,6
ds_read_b128 v[60:63],v2 offset:29696
v_mul_f32 v32,v32,v72
v_mfma_f32_16x16x32_bf16 v[28:31],a[180:183],v[64:67],v[28:31]
s_cmp_ge_u32 s66,s59
v_exp_f32 v75,v75
v_mfma_f32_16x16x32_bf16 v[24:27],a[184:187],v[68:71],v[24:27]
ds_read_b128 v[64:67],v2 offset:30720
v_fma_f32 v75,v75,v99,v99
v_mfma_f32_16x16x32_bf16 v[28:31],a[188:191],v[68:71],v[28:31]
s_waitcnt lgkmcnt(3)
buffer_load_dwordx4 v0,s[36:39],s67 offen offset:3072 sc1 lds
v_mfma_f32_16x16x32_bf16 v[24:27],a[192:195],v[40:43],v[24:27]
s_waitcnt vmcnt(9)
ds_read_b128 v[68:71],v2 offset:31744
v_mul_f32 v33,v33,v73
v_mfma_f32_16x16x32_bf16 v[28:31],a[196:199],v[40:43],v[28:31]
s_barrier
v_mfma_f32_16x16x32_bf16 v[24:27],a[200:203],v[44:47],v[24:27]
buffer_store_dword v5,v4,s[36:39],s70 offen offset:0 sc0 sc1
ds_read_b128 v[40:43],v2 offset:32768
ds_read_b64 v[92:93],v11 offset:0
v_cvt_pk_bf16_f32 v80,v32,v33
v_mfma_f32_16x16x32_bf16 v[28:31],a[204:207],v[44:47],v[28:31]
v_rcp_f32 v74,v74
v_mfma_f32_16x16x32_bf16 v[24:27],a[208:211],v[48:51],v[24:27]
v_fma_f32 v36,-v96,v20,v36
v_fma_f32 v37,-v96,v21,v37
v_fma_f32 v38,-v96,v22,v38
v_fma_f32 v39,-v96,v23,v39
v_mul_f32 v76,v36,v36
v_mul_f32 v77,v37,v37
v_mul_f32 v78,v38,v38
v_mul_f32 v79,v39,v39
v_fma_f32 v76,v98,v76,v97
v_fma_f32 v77,v98,v77,v97
v_fma_f32 v78,v98,v78,v97
v_fma_f32 v79,v98,v79,v97
v_mul_f32 v76,v36,v76
v_mul_f32 v77,v37,v77
v_mul_f32 v78,v38,v78
v_mul_f32 v79,v39,v79
v_exp_f32 v76,v76
v_exp_f32 v77,v77
v_exp_f32 v78,v78
v_exp_f32 v79,v79
v_mfma_f32_16x16x32_bf16 v[28:31],a[212:215],v[48:51],v[28:31]
ds_read_b128 v[44:47],v2 offset:33792
v_rcp_f32 v75,v75
v_fma_f32 v76,v76,v99,v99
v_fma_f32 v77,v77,v99,v99
v_fma_f32 v78,v78,v99,v99
v_fma_f32 v79,v79,v99,v99
v_rcp_f32 v76,v76
v_rcp_f32 v77,v77
v_rcp_f32 v78,v78
v_rcp_f32 v79,v79
v_mfma_f32_16x16x32_bf16 v[24:27],a[216:219],v[52:55],v[24:27]
s_waitcnt lgkmcnt(1)
v_mul_f32 v34,v34,v74
v_mul_f32 v35,v35,v75
v_cvt_pk_bf16_f32 v81,v34,v35
v_mul_f32 v36,v36,v76
v_mul_f32 v37,v37,v77
v_mul_f32 v38,v38,v78
v_mul_f32 v39,v39,v79
v_cvt_pk_bf16_f32 v82,v36,v37
v_cvt_pk_bf16_f32 v83,v38,v39
v_mfma_f32_16x16x32_bf16 v[28:31],a[220:223],v[52:55],v[28:31]
ds_read_b128 v[48:51],v2 offset:34816
buffer_store_dwordx4 v[80:83],v3,s[36:39],s73 offen offset:0 sc0 sc1
v_mfma_f32_16x16x32_bf16 v[24:27],a[224:227],v[56:59],v[24:27]
ds_read_b128 v[52:55],v2 offset:35840
v_mfma_f32_16x16x32_bf16 v[28:31],a[228:231],v[56:59],v[28:31]
v_mfma_f32_16x16x32_bf16 v[24:27],a[232:235],v[60:63],v[24:27]
ds_read_b128 v[56:59],v2 offset:36864
v_mfma_f32_16x16x32_bf16 v[28:31],a[236:239],v[60:63],v[28:31]
v_mfma_f32_16x16x32_bf16 v[24:27],a[240:243],v[64:67],v[24:27]
ds_read_b128 v[60:63],v2 offset:37888
v_mfma_f32_16x16x32_bf16 v[28:31],a[244:247],v[64:67],v[28:31]
v_mfma_f32_16x16x32_bf16 v[24:27],a[248:251],v[68:71],v[24:27]
s_waitcnt lgkmcnt(2)
ds_read_b128 v[64:67],v2 offset:38912
v_mfma_f32_16x16x32_bf16 v[28:31],a[252:255],v[68:71],v[28:31]
s_cbranch_scc1 Lg3o_slow1_%=
Lg3o_d1w3_%=:
v_mfma_f32_16x16x32_bf16 v[32:35],a[0:3],v[40:43],0
s_add_u32 m0,s62,16384
ds_read_b128 v[68:71],v2 offset:39936
v_mul_f32 v96,s75,v92
v_mfma_f32_16x16x32_bf16 v[36:39],a[4:7],v[40:43],0
s_mul_i32 s73,s48,7
v_fma_f32 v93,v93,s75,v12
v_fma_f32 v93,-v96,v96,v93
v_mfma_f32_16x16x32_bf16 v[32:35],a[8:11],v[44:47],v[32:35]
s_lshr_b32 s73,s73,1
ds_read_b128 v[40:43],v2 offset:40960
v_max_f32 v93,v93,v12
v_mfma_f32_16x16x32_bf16 v[36:39],a[12:15],v[44:47],v[36:39]
s_lshr_b32 s72,s67,5
v_rsq_f32 v92,v93
v_mfma_f32_16x16x32_bf16 v[32:35],a[16:19],v[48:51],v[32:35]
buffer_load_dwordx4 v1,s[36:39],s67 offen offset:0 sc1 lds
v_mfma_f32_16x16x32_bf16 v[36:39],a[20:23],v[48:51],v[36:39])"
R"(
s_lshr_b32 s71,s62,2
ds_read_b128 v[44:47],v2 offset:41984
v_mul_f32 v97,s76,v92
v_mfma_f32_16x16x32_bf16 v[32:35],a[24:27],v[52:55],v[32:35]
s_mul_i32 s70,s47,5
ds_read_b128 v[48:51],v2 offset:43008
v_mul_f32 v98,v92,v92
v_mfma_f32_16x16x32_bf16 v[36:39],a[28:31],v[52:55],v[36:39]
s_waitcnt lgkmcnt(3)
v_mul_f32 v99,v93,v92
v_mul_f32 v98,s77,v98
v_mfma_f32_16x16x32_bf16 v[32:35],a[32:35],v[56:59],v[32:35]
buffer_load_dwordx4 v1,s[36:39],s67 offen offset:1024 sc1 lds
v_mfma_f32_16x16x32_bf16 v[36:39],a[36:39],v[56:59],v[36:39]
s_mov_b64 s[44:45],s[46:47]
ds_read_b128 v[52:55],v2 offset:44032
v_mul_f32 v98,v98,v97
v_mfma_f32_16x16x32_bf16 v[32:35],a[40:43],v[60:63],v[32:35]
s_mov_b64 s[46:47],s[48:49]
ds_read_b128 v[56:59],v2 offset:45056
v_fma_f32 v24,-v96,v16,v24
v_mfma_f32_16x16x32_bf16 v[36:39],a[44:47],v[60:63],v[36:39]
s_mov_b64 s[48:49],s[50:51]
v_fma_f32 v25,-v96,v17,v25
v_fma_f32 v26,-v96,v18,v26
v_mfma_f32_16x16x32_bf16 v[32:35],a[48:51],v[64:67],v[32:35]
buffer_load_dwordx4 v1,s[36:39],s67 offen offset:2048 sc1 lds
v_mfma_f32_16x16x32_bf16 v[36:39],a[52:55],v[64:67],v[36:39]
ds_read_b128 v[60:63],v2 offset:46080
v_fma_f32 v27,-v96,v19,v27
v_mfma_f32_16x16x32_bf16 v[32:35],a[56:59],v[68:71],v[32:35]
ds_read_b128 v[64:67],v2 offset:47104
v_mul_f32 v72,v24,v24
v_mfma_f32_16x16x32_bf16 v[36:39],a[60:63],v[68:71],v[36:39]
s_waitcnt lgkmcnt(3)
v_mul_f32 v73,v25,v25
v_mul_f32 v74,v26,v26
v_mfma_f32_16x16x32_bf16 v[32:35],a[64:67],v[40:43],v[32:35]
s_waitcnt vmcnt(9)
ds_read_b128 v[68:71],v2 offset:48128
v_mul_f32 v75,v27,v27
v_mfma_f32_16x16x32_bf16 v[36:39],a[68:71],v[40:43],v[36:39]
s_barrier
v_mfma_f32_16x16x32_bf16 v[32:35],a[72:75],v[44:47],v[32:35]
buffer_load_dwordx4 v1,s[36:39],s67 offen offset:3072 sc1 lds
v_mfma_f32_16x16x32_bf16 v[36:39],a[76:79],v[44:47],v[36:39]
s_add_u32 m0,s71,s88
ds_read_b128 v[40:43],v2 offset:49152
ds_read_b32 v88,v8 offset:8192
v_mfma_f32_16x16x32_bf16 v[32:35],a[80:83],v[48:51],v[32:35]
buffer_load_dword v9,s[36:39],s72 offen offset:0 sc1 lds
v_mfma_f32_16x16x32_bf16 v[36:39],a[84:87],v[48:51],v[36:39]
s_xor_b32 s87,s63,65536
ds_read_b128 v[44:47],v2 offset:50176
v_fma_f32 v72,v98,v72,v97
v_mfma_f32_16x16x32_bf16 v[32:35],a[88:91],v[52:55],v[32:35]
s_cmp_ge_u32 s67,s59
ds_read_b128 v[48:51],v2 offset:51200
v_mul_f32 v72,v24,v72
v_mfma_f32_16x16x32_bf16 v[36:39],a[92:95],v[52:55],v[36:39]
s_waitcnt lgkmcnt(4)
v_exp_f32 v72,v72
v_mfma_f32_16x16x32_bf16 v[32:35],a[96:99],v[56:59],v[32:35]
s_nop 0
ds_read_b128 v[52:55],v2 offset:52224
v_fma_f32 v72,v72,v99,v99
v_mfma_f32_16x16x32_bf16 v[36:39],a[100:103],v[56:59],v[36:39]
s_barrier
v_mfma_f32_16x16x32_bf16 v[32:35],a[104:107],v[60:63],v[32:35]
s_waitcnt lgkmcnt(3)
ds_read_b128 v[56:59],v2 offset:53248
ds_read_b32 v13,v7 offset:256
v_mfma_f32_16x16x32_bf16 v[36:39],a[108:111],v[60:63],v[36:39]
s_cselect_b32 s86,s50,s51
v_rcp_f32 v72,v72
v_mfma_f32_16x16x32_bf16 v[32:35],a[112:115],v[64:67],v[32:35]
s_cselect_b32 s87,s87,s64
ds_read_b128 v[60:63],v2 offset:54272
v_add_f32_dpp v88,v88,v88 quad_perm:[1,0,3,2] row_mask:0xf bank_mask:0xf
v_mfma_f32_16x16x32_bf16 v[36:39],a[116:119],v[64:67],v[36:39]
s_mov_b32 s62,s87
v_add_f32_dpp v88,v88,v88 quad_perm:[2,3,0,1] row_mask:0xf bank_mask:0xf
v_fma_f32 v73,v98,v73,v97
v_mfma_f32_16x16x32_bf16 v[32:35],a[120:123],v[68:71],v[32:35]
s_waitcnt lgkmcnt(1)
ds_read_b128 v[64:67],v2 offset:55296
v_mul_f32 v73,v25,v73
v_mfma_f32_16x16x32_bf16 v[36:39],a[124:127],v[68:71],v[36:39]
v_cmp_eq_u32 vcc,s60,v13
v_exp_f32 v73,v73
v_add_f32_dpp v88,v88,v88 row_half_mirror row_mask:0xf bank_mask:0xf
v_mfma_f32_16x16x32_bf16 v[32:35],a[128:131],v[40:43],v[32:35]
s_cmp_eq_u32 vcc_lo,-1
ds_write_b32 v10,v88 offset:128
v_mfma_f32_16x16x32_bf16 v[36:39],a[132:135],v[40:43],v[36:39]
s_cselect_b32 s66,s86,s59
ds_read_b128 v[68:71],v2 offset:56320
v_fma_f32 v73,v73,v99,v99
v_mfma_f32_16x16x32_bf16 v[32:35],a[136:139],v[44:47],v[32:35]
s_mov_b32 m0,s62
ds_read_b128 v[40:43],v2 offset:57344
v_fma_f32 v74,v98,v74,v97
v_mfma_f32_16x16x32_bf16 v[36:39],a[140:143],v[44:47],v[36:39]
buffer_load_dwordx4 v0,s[36:39],s66 offen offset:0 sc1 lds
v_mfma_f32_16x16x32_bf16 v[32:35],a[144:147],v[48:51],v[32:35]
s_xor_b32 s64,s64,65536
ds_read_b128 v[44:47],v2 offset:58368
v_mul_f32 v74,v26,v74
v_mfma_f32_16x16x32_bf16 v[36:39],a[148:151],v[48:51],v[36:39]
s_add_u32 s74,s53,s55
v_exp_f32 v74,v74
v_mfma_f32_16x16x32_bf16 v[32:35],a[152:155],v[52:55],v[32:35]
s_cmp_eq_u32 s74,s56
ds_read_b128 v[48:51],v2 offset:59392
v_fma_f32 v74,v74,v99,v99
v_mfma_f32_16x16x32_bf16 v[36:39],a[156:159],v[52:55],v[36:39]
s_waitcnt lgkmcnt(3)
buffer_load_dwordx4 v0,s[36:39],s66 offen offset:1024 sc1 lds
v_mfma_f32_16x16x32_bf16 v[32:35],a[160:163],v[56:59],v[32:35]
s_cselect_b32 s74,s57,s74
ds_read_b128 v[52:55],v2 offset:60416
v_fma_f32 v75,v98,v75,v97
v_mfma_f32_16x16x32_bf16 v[36:39],a[164:167],v[56:59],v[36:39]
s_cmp_eq_u32 s74,s58
v_rcp_f32 v73,v73
v_mfma_f32_16x16x32_bf16 v[32:35],a[168:171],v[60:63],v[32:35]
s_cselect_b32 s74,s59,s74
ds_read_b128 v[56:59],v2 offset:61440
v_mul_f32 v75,v27,v75
v_mfma_f32_16x16x32_bf16 v[36:39],a[172:175],v[60:63],v[36:39]
buffer_load_dwordx4 v0,s[36:39],s66 offen offset:2048 sc1 lds
v_mfma_f32_16x16x32_bf16 v[32:35],a[176:179],v[64:67],v[32:35]
s_lshr_b32 s70,s70,6
ds_read_b128 v[60:63],v2 offset:62464
v_mul_f32 v24,v24,v72
v_mfma_f32_16x16x32_bf16 v[36:39],a[180:183],v[64:67],v[36:39]
s_mov_b64 s[50:51],s[52:53]
v_exp_f32 v75,v75
v_mfma_f32_16x16x32_bf16 v[32:35],a[184:187],v[68:71],v[32:35]
s_mov_b32 s52,s74
ds_read_b128 v[64:67],v2 offset:63488
v_fma_f32 v75,v75,v99,v99
v_mfma_f32_16x16x32_bf16 v[36:39],a[188:191],v[68:71],v[36:39]
s_waitcnt lgkmcnt(3)
buffer_load_dwordx4 v0,s[36:39],s66 offen offset:3072 sc1 lds
v_mfma_f32_16x16x32_bf16 v[32:35],a[192:195],v[40:43],v[32:35]
s_waitcnt vmcnt(9)
ds_read_b128 v[68:71],v2 offset:64512
v_xor_b32 v2,65536,v2
v_mfma_f32_16x16x32_bf16 v[36:39],a[196:199],v[40:43],v[36:39]
s_barrier
v_mfma_f32_16x16x32_bf16 v[32:35],a[200:203],v[44:47],v[32:35]
buffer_store_dword v5,v4,s[36:39],s70 offen offset:0 sc0 sc1
ds_read_b128 v[40:43],v2 offset:0
ds_read_b64 v[92:93],v11 offset:128
v_mul_f32 v25,v25,v73
v_mfma_f32_16x16x32_bf16 v[36:39],a[204:207],v[44:47],v[36:39]
s_cmp_lt_u32 s67,s59
v_rcp_f32 v74,v74
v_mfma_f32_16x16x32_bf16 v[32:35],a[208:211],v[48:51],v[32:35]
v_fma_f32 v28,-v96,v20,v28
v_fma_f32 v29,-v96,v21,v29
v_fma_f32 v30,-v96,v22,v30
v_fma_f32 v31,-v96,v23,v31
v_mul_f32 v76,v28,v28
v_mul_f32 v77,v29,v29
v_mul_f32 v78,v30,v30
v_mul_f32 v79,v31,v31
v_fma_f32 v76,v98,v76,v97
v_fma_f32 v77,v98,v77,v97
v_fma_f32 v78,v98,v78,v97
v_fma_f32 v79,v98,v79,v97
v_mul_f32 v76,v28,v76
v_mul_f32 v77,v29,v77
v_mul_f32 v78,v30,v78
v_mul_f32 v79,v31,v79
v_exp_f32 v76,v76
v_exp_f32 v77,v77
v_exp_f32 v78,v78
v_exp_f32 v79,v79
v_mfma_f32_16x16x32_bf16 v[36:39],a[212:215],v[48:51],v[36:39]
ds_read_b128 v[44:47],v2 offset:1024
v_rcp_f32 v75,v75
v_fma_f32 v76,v76,v99,v99
v_fma_f32 v77,v77,v99,v99
v_fma_f32 v78,v78,v99,v99
v_fma_f32 v79,v79,v99,v99
v_rcp_f32 v76,v76
v_rcp_f32 v77,v77
v_rcp_f32 v78,v78
v_rcp_f32 v79,v79
v_mfma_f32_16x16x32_bf16 v[32:35],a[216:219],v[52:55],v[32:35]
s_waitcnt lgkmcnt(1)
v_mul_f32 v26,v26,v74
v_mul_f32 v27,v27,v75
v_cvt_pk_bf16_f32 v80,v24,v25
v_cvt_pk_bf16_f32 v81,v26,v27
v_mul_f32 v28,v28,v76
v_mul_f32 v29,v29,v77
v_mul_f32 v30,v30,v78)"
R"(
v_mul_f32 v31,v31,v79
v_cvt_pk_bf16_f32 v82,v28,v29
v_cvt_pk_bf16_f32 v83,v30,v31
v_mfma_f32_16x16x32_bf16 v[36:39],a[220:223],v[52:55],v[36:39]
ds_read_b128 v[48:51],v2 offset:2048
buffer_store_dwordx4 v[80:83],v3,s[36:39],s73 offen offset:0 sc0 sc1
v_xor_b32 v8,16384,v8
v_mfma_f32_16x16x32_bf16 v[32:35],a[224:227],v[56:59],v[32:35]
ds_read_b128 v[52:55],v2 offset:3072
v_mfma_f32_16x16x32_bf16 v[36:39],a[228:231],v[56:59],v[36:39]
v_mfma_f32_16x16x32_bf16 v[32:35],a[232:235],v[60:63],v[32:35]
ds_read_b128 v[56:59],v2 offset:4096
v_mfma_f32_16x16x32_bf16 v[36:39],a[236:239],v[60:63],v[36:39]
v_mfma_f32_16x16x32_bf16 v[32:35],a[240:243],v[64:67],v[32:35]
ds_read_b128 v[60:63],v2 offset:5120
v_mfma_f32_16x16x32_bf16 v[36:39],a[244:247],v[64:67],v[36:39]
v_mfma_f32_16x16x32_bf16 v[32:35],a[248:251],v[68:71],v[32:35]
s_waitcnt lgkmcnt(2)
ds_read_b128 v[64:67],v2 offset:6144
v_mfma_f32_16x16x32_bf16 v[36:39],a[252:255],v[68:71],v[36:39]
s_cbranch_scc1 Lg3o_d0w3_%=
s_branch Lg3o_slow0_%=
Lg3o_slow0_%=:
s_cmp_lt_u32 s66,s59
s_cbranch_scc1 Lg3o_gs0_%=
Lg3o_slowb0_%=:
s_cmp_eq_u32 s54,1
s_cselect_b32 s68,s49,s48
s_lshr_b32 s68,s68,5
s_add_u32 m0,s78,8192
s_nop 0
buffer_load_dword v6,s[36:39],s68 offen offset:0 sc1 lds
s_mul_i32 s73,s47,7
s_lshr_b32 s73,s73,1
v_mul_f32 v96,s75,v92
v_fma_f32 v93,v93,s75,v12
v_fma_f32 v93,-v96,v96,v93
v_max_f32 v93,v93,v12
v_rsq_f32 v92,v93
s_nop 0
v_mul_f32 v97,s76,v92
v_mul_f32 v98,v92,v92
v_mul_f32 v99,v93,v92
v_mul_f32 v98,s77,v98
v_mul_f32 v98,v98,v97
v_fma_f32 v32,-v96,v16,v32
v_fma_f32 v33,-v96,v17,v33
v_fma_f32 v34,-v96,v18,v34
v_fma_f32 v35,-v96,v19,v35
v_mul_f32 v72,v32,v32
v_mul_f32 v73,v33,v33
v_mul_f32 v74,v34,v34
v_mul_f32 v75,v35,v35
v_fma_f32 v72,v98,v72,v97
v_fma_f32 v73,v98,v73,v97
v_fma_f32 v74,v98,v74,v97
v_fma_f32 v75,v98,v75,v97
v_mul_f32 v72,v32,v72
v_mul_f32 v73,v33,v73
v_mul_f32 v74,v34,v74
v_mul_f32 v75,v35,v75
v_exp_f32 v72,v72
v_exp_f32 v73,v73
v_exp_f32 v74,v74
v_exp_f32 v75,v75
v_fma_f32 v72,v72,v99,v99
v_fma_f32 v73,v73,v99,v99
v_fma_f32 v74,v74,v99,v99
v_fma_f32 v75,v75,v99,v99
v_rcp_f32 v72,v72
v_rcp_f32 v73,v73
v_rcp_f32 v74,v74
v_rcp_f32 v75,v75
v_mul_f32 v32,v32,v72
s_add_u32 m0,s78,8448
s_nop 0
buffer_load_dword v6,s[36:39],s68 offen offset:0 sc1 lds
v_mul_f32 v33,v33,v73
v_mul_f32 v34,v34,v74
v_mul_f32 v35,v35,v75
v_cvt_pk_bf16_f32 v80,v32,v33
v_cvt_pk_bf16_f32 v81,v34,v35
v_fma_f32 v36,-v96,v20,v36
v_fma_f32 v37,-v96,v21,v37
v_fma_f32 v38,-v96,v22,v38
v_fma_f32 v39,-v96,v23,v39
v_mul_f32 v76,v36,v36
v_mul_f32 v77,v37,v37
v_mul_f32 v78,v38,v38
v_mul_f32 v79,v39,v39
v_fma_f32 v76,v98,v76,v97
v_fma_f32 v77,v98,v77,v97
v_fma_f32 v78,v98,v78,v97
v_fma_f32 v79,v98,v79,v97
v_mul_f32 v76,v36,v76
v_mul_f32 v77,v37,v77
v_mul_f32 v78,v38,v78
v_mul_f32 v79,v39,v79
v_exp_f32 v76,v76
v_exp_f32 v77,v77
v_exp_f32 v78,v78
v_exp_f32 v79,v79
v_fma_f32 v76,v76,v99,v99
v_fma_f32 v77,v77,v99,v99
v_fma_f32 v78,v78,v99,v99
v_fma_f32 v79,v79,v99,v99
v_rcp_f32 v76,v76
v_rcp_f32 v77,v77
v_rcp_f32 v78,v78
v_rcp_f32 v79,v79
v_mul_f32 v36,v36,v76
v_mul_f32 v37,v37,v77
v_mul_f32 v38,v38,v78
v_mul_f32 v39,v39,v79
v_cvt_pk_bf16_f32 v82,v36,v37
v_cvt_pk_bf16_f32 v83,v38,v39
buffer_store_dwordx4 v[80:83],v3,s[36:39],s73 offen offset:0 sc0 sc1
s_cmp_ge_u32 s48,s59
s_cbranch_scc1 Lg3o_done0_%=
s_add_u32 m0,s78,16384
s_nop 0
buffer_load_dword v6,s[36:39],s68 offen offset:0 sc1 lds
s_waitcnt vmcnt(3)
s_barrier
ds_read_b32 v13,v7 offset:8192
s_mul_i32 s70,s46,5
s_lshr_b32 s70,s70,6
buffer_store_dword v5,v4,s[36:39],s70 offen offset:0 sc0 sc1
s_mov_b32 s46,s59
s_mov_b32 s69,18432
s_waitcnt lgkmcnt(0)
v_cmp_eq_u32 vcc,s60,v13
s_nop 0
s_cmp_eq_u32 vcc_lo,-1
s_cbranch_scc1 Lg3o_go0_%=
s_add_u32 m0,s78,16640
s_nop 0
buffer_load_dword v6,s[36:39],s68 offen offset:0 sc1 lds
s_waitcnt vmcnt(4)
s_barrier
ds_read_b32 v13,v7 offset:8448
s_mov_b32 s69,18688
s_waitcnt lgkmcnt(0)
v_cmp_eq_u32 vcc,s60,v13
s_nop 0
s_cmp_eq_u32 vcc_lo,-1
s_cbranch_scc1 Lg3o_go0_%=
s_add_u32 m0,s78,8192
s_nop 0
buffer_load_dword v6,s[36:39],s68 offen offset:0 sc1 lds
s_waitcnt vmcnt(3)
s_barrier
ds_read_b32 v13,v7 offset:16384
s_mul_i32 s70,s47,5
s_lshr_b32 s70,s70,6
buffer_store_dword v5,v4,s[36:39],s70 offen offset:0 sc0 sc1
s_mov_b32 s47,s59
s_mov_b32 s69,10240
s_waitcnt lgkmcnt(0)
v_cmp_eq_u32 vcc,s60,v13
s_nop 0
s_cmp_eq_u32 vcc_lo,-1
s_cbranch_scc1 Lg3o_go0_%=
s_add_u32 m0,s78,8448
s_nop 0
buffer_load_dword v6,s[36:39],s68 offen offset:0 sc1 lds
s_waitcnt vmcnt(3)
s_barrier
ds_read_b32 v13,v7 offset:16640
s_mov_b32 s69,10496
s_waitcnt lgkmcnt(0)
v_cmp_eq_u32 vcc,s60,v13
s_nop 0
s_cmp_eq_u32 vcc_lo,-1
s_cbranch_scc1 Lg3o_go0_%=
Lg3o_poll0_%=:
s_add_u32 m0,s78,16384
s_nop 0
buffer_load_dword v6,s[36:39],s68 offen offset:0 sc1 lds
s_waitcnt vmcnt(2)
s_barrier
ds_read_b32 v13,v7 offset:8192
s_mov_b32 s69,18432
s_waitcnt lgkmcnt(0)
v_cmp_eq_u32 vcc,s60,v13
s_nop 0
s_cmp_eq_u32 vcc_lo,-1
s_cbranch_scc1 Lg3o_go0_%=
s_sleep 4
s_add_u32 m0,s78,16640
s_nop 0
buffer_load_dword v6,s[36:39],s68 offen offset:0 sc1 lds
s_waitcnt vmcnt(2)
s_barrier
ds_read_b32 v13,v7 offset:8448
s_mov_b32 s69,18688
s_waitcnt lgkmcnt(0)
v_cmp_eq_u32 vcc,s60,v13
s_nop 0
s_cmp_eq_u32 vcc_lo,-1)"
R"(
s_cbranch_scc1 Lg3o_go0_%=
s_sleep 4
s_add_u32 m0,s78,8192
s_nop 0
buffer_load_dword v6,s[36:39],s68 offen offset:0 sc1 lds
s_waitcnt vmcnt(2)
s_barrier
ds_read_b32 v13,v7 offset:16384
s_mov_b32 s69,10240
s_waitcnt lgkmcnt(0)
v_cmp_eq_u32 vcc,s60,v13
s_nop 0
s_cmp_eq_u32 vcc_lo,-1
s_cbranch_scc1 Lg3o_go0_%=
s_sleep 4
s_add_u32 m0,s78,8448
s_nop 0
buffer_load_dword v6,s[36:39],s68 offen offset:0 sc1 lds
s_waitcnt vmcnt(2)
s_barrier
ds_read_b32 v13,v7 offset:16640
s_mov_b32 s69,10496
s_waitcnt lgkmcnt(0)
v_cmp_eq_u32 vcc,s60,v13
s_nop 0
s_cmp_eq_u32 vcc_lo,-1
s_cbranch_scc1 Lg3o_go0_%=
s_sleep 4
s_add_u32 s79,s79,1
s_cmp_lt_u32 s79,20000
s_cbranch_scc1 Lg3o_poll0_%=
Lg3o_go0_%=:
s_xor_b32 s62,s63,65536
s_mov_b32 m0,s62
s_nop 0
buffer_load_dwordx4 v0,s[36:39],s48 offen offset:0 sc1 lds
buffer_load_dwordx4 v0,s[36:39],s48 offen offset:1024 sc1 lds
buffer_load_dwordx4 v0,s[36:39],s48 offen offset:2048 sc1 lds
buffer_load_dwordx4 v0,s[36:39],s48 offen offset:3072 sc1 lds
s_add_u32 m0,s62,16384
s_nop 0
buffer_load_dwordx4 v1,s[36:39],s48 offen offset:0 sc1 lds
buffer_load_dwordx4 v1,s[36:39],s48 offen offset:1024 sc1 lds
buffer_load_dwordx4 v1,s[36:39],s48 offen offset:2048 sc1 lds
buffer_load_dwordx4 v1,s[36:39],s48 offen offset:3072 sc1 lds
s_lshr_b32 s72,s48,5
s_lshr_b32 s71,s62,2
s_add_u32 m0,s71,s88
s_nop 0
buffer_load_dword v9,s[36:39],s72 offen offset:0 sc1 lds
s_cmp_eq_u32 vcc_hi,-1
s_cselect_b32 s66,s49,s59
s_xor_b32 s62,s64,65536
s_cmp_ge_u32 s66,s59
s_cselect_b32 s68,s49,s50
s_lshr_b32 s68,s68,5
s_add_u32 m0,s78,0
s_nop 0
buffer_load_dword v6,s[36:39],s68 offen offset:0 sc1 lds
s_mov_b32 m0,s62
s_nop 0
buffer_load_dwordx4 v0,s[36:39],s66 offen offset:0 sc1 lds
buffer_load_dwordx4 v0,s[36:39],s66 offen offset:1024 sc1 lds
buffer_load_dwordx4 v0,s[36:39],s66 offen offset:2048 sc1 lds
buffer_load_dwordx4 v0,s[36:39],s66 offen offset:3072 sc1 lds
s_waitcnt vmcnt(10)
s_barrier
ds_read_b128 v[40:43],v2 offset:0
ds_read_b128 v[44:47],v2 offset:1024
ds_read_b128 v[48:51],v2 offset:2048
ds_read_b128 v[52:55],v2 offset:3072
ds_read_b128 v[56:59],v2 offset:4096
ds_read_b128 v[60:63],v2 offset:5120
ds_read_b128 v[64:67],v2 offset:6144
s_mul_i32 s70,s46,5
s_lshr_b32 s70,s70,6
buffer_store_dword v5,v4,s[36:39],s70 offen offset:0 sc0 sc1
s_mov_b32 s46,s59
s_mul_i32 s70,s47,5
s_lshr_b32 s70,s70,6
buffer_store_dword v5,v4,s[36:39],s70 offen offset:0 sc0 sc1
s_mov_b32 s47,s59
s_mul_i32 s70,s59,5
s_lshr_b32 s70,s70,6
buffer_store_dword v5,v4,s[36:39],s70 offen offset:0 sc0 sc1
s_waitcnt lgkmcnt(3)
s_setpc_b64 s[90:91]
Lg3o_gs0_%=:
s_cmp_eq_u32 s54,1
s_cselect_b32 s68,s49,s48
s_lshr_b32 s68,s68,5
s_mul_i32 s73,s47,7
s_lshr_b32 s73,s73,1
s_add_u32 m0,s78,8192
s_nop 0
buffer_load_dword v6,s[36:39],s68 offen offset:0 sc1 lds
v_mul_f32 v96,s75,v92
v_fma_f32 v93,v93,s75,v12
v_fma_f32 v93,-v96,v96,v93
v_max_f32 v93,v93,v12
v_rsq_f32 v92,v93
s_nop 0
v_mul_f32 v97,s76,v92
v_mul_f32 v98,v92,v92
v_mul_f32 v99,v93,v92
v_mul_f32 v98,s77,v98
v_mul_f32 v98,v98,v97
v_fma_f32 v32,-v96,v16,v32
v_fma_f32 v33,-v96,v17,v33
v_fma_f32 v34,-v96,v18,v34
s_add_u32 m0,s62,16384
s_nop 0
buffer_load_dwordx4 v1,s[36:39],s66 offen offset:0 sc1 lds
v_fma_f32 v35,-v96,v19,v35
v_mul_f32 v72,v32,v32
v_mul_f32 v73,v33,v33
v_mul_f32 v74,v34,v34
v_mul_f32 v75,v35,v35
v_fma_f32 v72,v98,v72,v97
v_fma_f32 v73,v98,v73,v97
v_fma_f32 v74,v98,v74,v97
v_fma_f32 v75,v98,v75,v97
v_mul_f32 v72,v32,v72
v_mul_f32 v73,v33,v73
v_mul_f32 v74,v34,v74
v_mul_f32 v75,v35,v75
buffer_load_dwordx4 v1,s[36:39],s66 offen offset:1024 sc1 lds
v_exp_f32 v72,v72
v_exp_f32 v73,v73
v_exp_f32 v74,v74
v_exp_f32 v75,v75
v_fma_f32 v72,v72,v99,v99
v_fma_f32 v73,v73,v99,v99
v_fma_f32 v74,v74,v99,v99
v_fma_f32 v75,v75,v99,v99
v_rcp_f32 v72,v72
v_rcp_f32 v73,v73
v_rcp_f32 v74,v74
v_rcp_f32 v75,v75
v_mul_f32 v32,v32,v72
buffer_load_dwordx4 v1,s[36:39],s66 offen offset:2048 sc1 lds
v_mul_f32 v33,v33,v73
v_mul_f32 v34,v34,v74
v_mul_f32 v35,v35,v75
v_cvt_pk_bf16_f32 v80,v32,v33
v_cvt_pk_bf16_f32 v81,v34,v35
v_fma_f32 v36,-v96,v20,v36
v_fma_f32 v37,-v96,v21,v37
v_fma_f32 v38,-v96,v22,v38
v_fma_f32 v39,-v96,v23,v39
v_mul_f32 v76,v36,v36
v_mul_f32 v77,v37,v37
v_mul_f32 v78,v38,v38
v_mul_f32 v79,v39,v39
buffer_load_dwordx4 v1,s[36:39],s66 offen offset:3072 sc1 lds
v_fma_f32 v76,v98,v76,v97
v_fma_f32 v77,v98,v77,v97
v_fma_f32 v78,v98,v78,v97
v_fma_f32 v79,v98,v79,v97
v_mul_f32 v76,v36,v76
v_mul_f32 v77,v37,v77
v_mul_f32 v78,v38,v78
v_mul_f32 v79,v39,v79
v_exp_f32 v76,v76
v_exp_f32 v77,v77
v_exp_f32 v78,v78
v_exp_f32 v79,v79
v_fma_f32 v76,v76,v99,v99
s_lshr_b32 s72,s66,5
s_lshr_b32 s71,s62,2
s_add_u32 m0,s71,s88
s_nop 0
buffer_load_dword v9,s[36:39],s72 offen offset:0 sc1 lds
v_fma_f32 v77,v77,v99,v99
v_fma_f32 v78,v78,v99,v99
v_fma_f32 v79,v79,v99,v99
v_rcp_f32 v76,v76
v_rcp_f32 v77,v77
v_rcp_f32 v78,v78
v_rcp_f32 v79,v79
v_mul_f32 v36,v36,v76
v_mul_f32 v37,v37,v77
v_mul_f32 v38,v38,v78
v_mul_f32 v39,v39,v79
v_cvt_pk_bf16_f32 v82,v36,v37
v_cvt_pk_bf16_f32 v83,v38,v39
buffer_store_dwordx4 v[80:83],v3,s[36:39],s73 offen offset:0 sc0 sc1
s_waitcnt vmcnt(6)
s_barrier
ds_read_b32 v13,v7 offset:8192
ds_read_b128 v[40:43],v2 offset:0
ds_read_b128 v[44:47],v2 offset:1024
ds_read_b128 v[48:51],v2 offset:2048
ds_read_b128 v[52:55],v2 offset:3072
ds_read_b128 v[56:59],v2 offset:4096
ds_read_b128 v[60:63],v2 offset:5120
ds_read_b128 v[64:67],v2 offset:6144
s_mul_i32 s70,s46,5
s_lshr_b32 s70,s70,6
buffer_store_dword v5,v4,s[36:39],s70 offen offset:0 sc0 sc1)"
R"(
s_waitcnt lgkmcnt(7)
v_cmp_eq_u32 vcc,s60,v13
s_nop 0
s_cmp_eq_u32 vcc_hi,-1
s_cselect_b32 s66,s49,s59
s_xor_b32 s62,s64,65536
s_cmp_ge_u32 s66,s59
s_cselect_b32 s68,s49,s50
s_lshr_b32 s68,s68,5
s_add_u32 m0,s78,0
s_nop 0
buffer_load_dword v6,s[36:39],s68 offen offset:0 sc1 lds
s_mov_b32 m0,s62
s_nop 0
buffer_load_dwordx4 v0,s[36:39],s66 offen offset:0 sc1 lds
buffer_load_dwordx4 v0,s[36:39],s66 offen offset:1024 sc1 lds
buffer_load_dwordx4 v0,s[36:39],s66 offen offset:2048 sc1 lds
buffer_load_dwordx4 v0,s[36:39],s66 offen offset:3072 sc1 lds
s_mov_b32 s46,s47
s_mov_b32 s47,s59
s_mul_i32 s70,s59,5
s_lshr_b32 s70,s70,6
buffer_store_dword v5,v4,s[36:39],s70 offen offset:0 sc0 sc1
s_waitcnt lgkmcnt(3)
s_setpc_b64 s[90:91]
Lg3o_done0_%=:
s_waitcnt vmcnt(0)
s_barrier
s_mul_i32 s70,s46,5
s_lshr_b32 s70,s70,6
buffer_store_dword v5,v4,s[36:39],s70 offen offset:0 sc0 sc1
s_mul_i32 s70,s47,5
s_lshr_b32 s70,s70,6
buffer_store_dword v5,v4,s[36:39],s70 offen offset:0 sc0 sc1
s_branch Lg3o_done_%=
Lg3o_slow1_%=:
s_cmp_lt_u32 s67,s59
s_cbranch_scc1 Lg3o_gs1_%=
Lg3o_slowb1_%=:
s_cmp_eq_u32 s54,1
s_cselect_b32 s68,s50,s49
s_lshr_b32 s68,s68,5
s_add_u32 m0,s78,8192
s_nop 0
buffer_load_dword v6,s[36:39],s68 offen offset:0 sc1 lds
s_mul_i32 s73,s48,7
s_lshr_b32 s73,s73,1
v_mul_f32 v96,s75,v92
v_fma_f32 v93,v93,s75,v12
v_fma_f32 v93,-v96,v96,v93
v_max_f32 v93,v93,v12
v_rsq_f32 v92,v93
s_nop 0
v_mul_f32 v97,s76,v92
v_mul_f32 v98,v92,v92
v_mul_f32 v99,v93,v92
v_mul_f32 v98,s77,v98
v_mul_f32 v98,v98,v97
v_fma_f32 v24,-v96,v16,v24
v_fma_f32 v25,-v96,v17,v25
v_fma_f32 v26,-v96,v18,v26
v_fma_f32 v27,-v96,v19,v27
v_mul_f32 v72,v24,v24
v_mul_f32 v73,v25,v25
v_mul_f32 v74,v26,v26
v_mul_f32 v75,v27,v27
v_fma_f32 v72,v98,v72,v97
v_fma_f32 v73,v98,v73,v97
v_fma_f32 v74,v98,v74,v97
v_fma_f32 v75,v98,v75,v97
v_mul_f32 v72,v24,v72
v_mul_f32 v73,v25,v73
v_mul_f32 v74,v26,v74
v_mul_f32 v75,v27,v75
v_exp_f32 v72,v72
v_exp_f32 v73,v73
v_exp_f32 v74,v74
v_exp_f32 v75,v75
v_fma_f32 v72,v72,v99,v99
v_fma_f32 v73,v73,v99,v99
v_fma_f32 v74,v74,v99,v99
v_fma_f32 v75,v75,v99,v99
v_rcp_f32 v72,v72
v_rcp_f32 v73,v73
v_rcp_f32 v74,v74
v_rcp_f32 v75,v75
v_mul_f32 v24,v24,v72
s_add_u32 m0,s78,8448
s_nop 0
buffer_load_dword v6,s[36:39],s68 offen offset:0 sc1 lds
v_mul_f32 v25,v25,v73
v_mul_f32 v26,v26,v74
v_mul_f32 v27,v27,v75
v_cvt_pk_bf16_f32 v80,v24,v25
v_cvt_pk_bf16_f32 v81,v26,v27
v_fma_f32 v28,-v96,v20,v28
v_fma_f32 v29,-v96,v21,v29
v_fma_f32 v30,-v96,v22,v30
v_fma_f32 v31,-v96,v23,v31
v_mul_f32 v76,v28,v28
v_mul_f32 v77,v29,v29
v_mul_f32 v78,v30,v30
v_mul_f32 v79,v31,v31
v_fma_f32 v76,v98,v76,v97
v_fma_f32 v77,v98,v77,v97
v_fma_f32 v78,v98,v78,v97
v_fma_f32 v79,v98,v79,v97
v_mul_f32 v76,v28,v76
v_mul_f32 v77,v29,v77
v_mul_f32 v78,v30,v78
v_mul_f32 v79,v31,v79
v_exp_f32 v76,v76
v_exp_f32 v77,v77
v_exp_f32 v78,v78
v_exp_f32 v79,v79
v_fma_f32 v76,v76,v99,v99
v_fma_f32 v77,v77,v99,v99
v_fma_f32 v78,v78,v99,v99
v_fma_f32 v79,v79,v99,v99
v_rcp_f32 v76,v76
v_rcp_f32 v77,v77
v_rcp_f32 v78,v78
v_rcp_f32 v79,v79
v_mul_f32 v28,v28,v76
v_mul_f32 v29,v29,v77
v_mul_f32 v30,v30,v78
v_mul_f32 v31,v31,v79
v_cvt_pk_bf16_f32 v82,v28,v29
v_cvt_pk_bf16_f32 v83,v30,v31
buffer_store_dwordx4 v[80:83],v3,s[36:39],s73 offen offset:0 sc0 sc1
s_cmp_ge_u32 s49,s59
s_cbranch_scc1 Lg3o_done1_%=
s_add_u32 m0,s78,16384
s_nop 0
buffer_load_dword v6,s[36:39],s68 offen offset:0 sc1 lds
s_waitcnt vmcnt(3)
s_barrier
ds_read_b32 v13,v7 offset:8192
s_mul_i32 s70,s47,5
s_lshr_b32 s70,s70,6
buffer_store_dword v5,v4,s[36:39],s70 offen offset:0 sc0 sc1
s_mov_b32 s47,s59
s_mov_b32 s69,18432
s_waitcnt lgkmcnt(0)
v_cmp_eq_u32 vcc,s60,v13
s_nop 0
s_cmp_eq_u32 vcc_lo,-1
s_cbranch_scc1 Lg3o_go1_%=
s_add_u32 m0,s78,16640
s_nop 0
buffer_load_dword v6,s[36:39],s68 offen offset:0 sc1 lds
s_waitcnt vmcnt(4)
s_barrier
ds_read_b32 v13,v7 offset:8448
s_mov_b32 s69,18688
s_waitcnt lgkmcnt(0)
v_cmp_eq_u32 vcc,s60,v13
s_nop 0
s_cmp_eq_u32 vcc_lo,-1
s_cbranch_scc1 Lg3o_go1_%=
s_add_u32 m0,s78,8192
s_nop 0
buffer_load_dword v6,s[36:39],s68 offen offset:0 sc1 lds
s_waitcnt vmcnt(3)
s_barrier
ds_read_b32 v13,v7 offset:16384
s_mul_i32 s70,s48,5
s_lshr_b32 s70,s70,6
buffer_store_dword v5,v4,s[36:39],s70 offen offset:0 sc0 sc1
s_mov_b32 s48,s59
s_mov_b32 s69,10240
s_waitcnt lgkmcnt(0)
v_cmp_eq_u32 vcc,s60,v13
s_nop 0
s_cmp_eq_u32 vcc_lo,-1
s_cbranch_scc1 Lg3o_go1_%=
s_add_u32 m0,s78,8448
s_nop 0
buffer_load_dword v6,s[36:39],s68 offen offset:0 sc1 lds
s_waitcnt vmcnt(3)
s_barrier
ds_read_b32 v13,v7 offset:16640
s_mov_b32 s69,10496
s_waitcnt lgkmcnt(0)
v_cmp_eq_u32 vcc,s60,v13
s_nop 0
s_cmp_eq_u32 vcc_lo,-1
s_cbranch_scc1 Lg3o_go1_%=
Lg3o_poll1_%=:
s_add_u32 m0,s78,16384
s_nop 0
buffer_load_dword v6,s[36:39],s68 offen offset:0 sc1 lds
s_waitcnt vmcnt(2)
s_barrier
ds_read_b32 v13,v7 offset:8192
s_mov_b32 s69,18432
s_waitcnt lgkmcnt(0)
v_cmp_eq_u32 vcc,s60,v13
s_nop 0
s_cmp_eq_u32 vcc_lo,-1)"
R"(
s_cbranch_scc1 Lg3o_go1_%=
s_sleep 4
s_add_u32 m0,s78,16640
s_nop 0
buffer_load_dword v6,s[36:39],s68 offen offset:0 sc1 lds
s_waitcnt vmcnt(2)
s_barrier
ds_read_b32 v13,v7 offset:8448
s_mov_b32 s69,18688
s_waitcnt lgkmcnt(0)
v_cmp_eq_u32 vcc,s60,v13
s_nop 0
s_cmp_eq_u32 vcc_lo,-1
s_cbranch_scc1 Lg3o_go1_%=
s_sleep 4
s_add_u32 m0,s78,8192
s_nop 0
buffer_load_dword v6,s[36:39],s68 offen offset:0 sc1 lds
s_waitcnt vmcnt(2)
s_barrier
ds_read_b32 v13,v7 offset:16384
s_mov_b32 s69,10240
s_waitcnt lgkmcnt(0)
v_cmp_eq_u32 vcc,s60,v13
s_nop 0
s_cmp_eq_u32 vcc_lo,-1
s_cbranch_scc1 Lg3o_go1_%=
s_sleep 4
s_add_u32 m0,s78,8448
s_nop 0
buffer_load_dword v6,s[36:39],s68 offen offset:0 sc1 lds
s_waitcnt vmcnt(2)
s_barrier
ds_read_b32 v13,v7 offset:16640
s_mov_b32 s69,10496
s_waitcnt lgkmcnt(0)
v_cmp_eq_u32 vcc,s60,v13
s_nop 0
s_cmp_eq_u32 vcc_lo,-1
s_cbranch_scc1 Lg3o_go1_%=
s_sleep 4
s_add_u32 s79,s79,1
s_cmp_lt_u32 s79,20000
s_cbranch_scc1 Lg3o_poll1_%=
Lg3o_go1_%=:
s_xor_b32 s62,s64,65536
s_mov_b32 m0,s62
s_nop 0
buffer_load_dwordx4 v0,s[36:39],s49 offen offset:0 sc1 lds
buffer_load_dwordx4 v0,s[36:39],s49 offen offset:1024 sc1 lds
buffer_load_dwordx4 v0,s[36:39],s49 offen offset:2048 sc1 lds
buffer_load_dwordx4 v0,s[36:39],s49 offen offset:3072 sc1 lds
s_add_u32 m0,s62,16384
s_nop 0
buffer_load_dwordx4 v1,s[36:39],s49 offen offset:0 sc1 lds
buffer_load_dwordx4 v1,s[36:39],s49 offen offset:1024 sc1 lds
buffer_load_dwordx4 v1,s[36:39],s49 offen offset:2048 sc1 lds
buffer_load_dwordx4 v1,s[36:39],s49 offen offset:3072 sc1 lds
s_lshr_b32 s72,s49,5
s_lshr_b32 s71,s62,2
s_add_u32 m0,s71,s88
s_nop 0
buffer_load_dword v9,s[36:39],s72 offen offset:0 sc1 lds
s_cmp_eq_u32 vcc_hi,-1
s_cselect_b32 s67,s50,s59
s_xor_b32 s62,s63,65536
s_cmp_ge_u32 s67,s59
s_cselect_b32 s68,s50,s51
s_lshr_b32 s68,s68,5
s_add_u32 m0,s78,256
s_nop 0
buffer_load_dword v6,s[36:39],s68 offen offset:0 sc1 lds
s_mov_b32 m0,s62
s_nop 0
buffer_load_dwordx4 v0,s[36:39],s67 offen offset:0 sc1 lds
buffer_load_dwordx4 v0,s[36:39],s67 offen offset:1024 sc1 lds
buffer_load_dwordx4 v0,s[36:39],s67 offen offset:2048 sc1 lds
buffer_load_dwordx4 v0,s[36:39],s67 offen offset:3072 sc1 lds
s_waitcnt vmcnt(10)
s_barrier
ds_read_b128 v[40:43],v2 offset:32768
ds_read_b128 v[44:47],v2 offset:33792
ds_read_b128 v[48:51],v2 offset:34816
ds_read_b128 v[52:55],v2 offset:35840
ds_read_b128 v[56:59],v2 offset:36864
ds_read_b128 v[60:63],v2 offset:37888
ds_read_b128 v[64:67],v2 offset:38912
s_mul_i32 s70,s47,5
s_lshr_b32 s70,s70,6
buffer_store_dword v5,v4,s[36:39],s70 offen offset:0 sc0 sc1
s_mov_b32 s47,s59
s_mul_i32 s70,s48,5
s_lshr_b32 s70,s70,6
buffer_store_dword v5,v4,s[36:39],s70 offen offset:0 sc0 sc1
s_mov_b32 s48,s59
s_mul_i32 s70,s59,5
s_lshr_b32 s70,s70,6
buffer_store_dword v5,v4,s[36:39],s70 offen offset:0 sc0 sc1
s_waitcnt lgkmcnt(3)
s_setpc_b64 s[92:93]
Lg3o_gs1_%=:
s_cmp_eq_u32 s54,1
s_cselect_b32 s68,s50,s49
s_lshr_b32 s68,s68,5
s_mul_i32 s73,s48,7
s_lshr_b32 s73,s73,1
s_add_u32 m0,s78,8192
s_nop 0
buffer_load_dword v6,s[36:39],s68 offen offset:0 sc1 lds
v_mul_f32 v96,s75,v92
v_fma_f32 v93,v93,s75,v12
v_fma_f32 v93,-v96,v96,v93
v_max_f32 v93,v93,v12
v_rsq_f32 v92,v93
s_nop 0
v_mul_f32 v97,s76,v92
v_mul_f32 v98,v92,v92
v_mul_f32 v99,v93,v92
v_mul_f32 v98,s77,v98
v_mul_f32 v98,v98,v97
v_fma_f32 v24,-v96,v16,v24
v_fma_f32 v25,-v96,v17,v25
v_fma_f32 v26,-v96,v18,v26
s_add_u32 m0,s62,16384
s_nop 0
buffer_load_dwordx4 v1,s[36:39],s67 offen offset:0 sc1 lds
v_fma_f32 v27,-v96,v19,v27
v_mul_f32 v72,v24,v24
v_mul_f32 v73,v25,v25
v_mul_f32 v74,v26,v26
v_mul_f32 v75,v27,v27
v_fma_f32 v72,v98,v72,v97
v_fma_f32 v73,v98,v73,v97
v_fma_f32 v74,v98,v74,v97
v_fma_f32 v75,v98,v75,v97
v_mul_f32 v72,v24,v72
v_mul_f32 v73,v25,v73
v_mul_f32 v74,v26,v74
v_mul_f32 v75,v27,v75
buffer_load_dwordx4 v1,s[36:39],s67 offen offset:1024 sc1 lds
v_exp_f32 v72,v72
v_exp_f32 v73,v73
v_exp_f32 v74,v74
v_exp_f32 v75,v75
v_fma_f32 v72,v72,v99,v99
v_fma_f32 v73,v73,v99,v99
v_fma_f32 v74,v74,v99,v99
v_fma_f32 v75,v75,v99,v99
v_rcp_f32 v72,v72
v_rcp_f32 v73,v73
v_rcp_f32 v74,v74
v_rcp_f32 v75,v75
v_mul_f32 v24,v24,v72
buffer_load_dwordx4 v1,s[36:39],s67 offen offset:2048 sc1 lds
v_mul_f32 v25,v25,v73
v_mul_f32 v26,v26,v74
v_mul_f32 v27,v27,v75
v_cvt_pk_bf16_f32 v80,v24,v25
v_cvt_pk_bf16_f32 v81,v26,v27
v_fma_f32 v28,-v96,v20,v28
v_fma_f32 v29,-v96,v21,v29
v_fma_f32 v30,-v96,v22,v30
v_fma_f32 v31,-v96,v23,v31
v_mul_f32 v76,v28,v28
v_mul_f32 v77,v29,v29
v_mul_f32 v78,v30,v30
v_mul_f32 v79,v31,v31
buffer_load_dwordx4 v1,s[36:39],s67 offen offset:3072 sc1 lds
v_fma_f32 v76,v98,v76,v97
v_fma_f32 v77,v98,v77,v97
v_fma_f32 v78,v98,v78,v97
v_fma_f32 v79,v98,v79,v97
v_mul_f32 v76,v28,v76
v_mul_f32 v77,v29,v77
v_mul_f32 v78,v30,v78
v_mul_f32 v79,v31,v79
v_exp_f32 v76,v76
v_exp_f32 v77,v77
v_exp_f32 v78,v78
v_exp_f32 v79,v79
v_fma_f32 v76,v76,v99,v99
s_lshr_b32 s72,s67,5
s_lshr_b32 s71,s62,2
s_add_u32 m0,s71,s88
s_nop 0
buffer_load_dword v9,s[36:39],s72 offen offset:0 sc1 lds
v_fma_f32 v77,v77,v99,v99
v_fma_f32 v78,v78,v99,v99
v_fma_f32 v79,v79,v99,v99
v_rcp_f32 v76,v76
v_rcp_f32 v77,v77
v_rcp_f32 v78,v78
v_rcp_f32 v79,v79
v_mul_f32 v28,v28,v76
v_mul_f32 v29,v29,v77
v_mul_f32 v30,v30,v78
v_mul_f32 v31,v31,v79
v_cvt_pk_bf16_f32 v82,v28,v29
v_cvt_pk_bf16_f32 v83,v30,v31
buffer_store_dwordx4 v[80:83],v3,s[36:39],s73 offen offset:0 sc0 sc1)"
R"(
s_waitcnt vmcnt(6)
s_barrier
ds_read_b32 v13,v7 offset:8192
ds_read_b128 v[40:43],v2 offset:32768
ds_read_b128 v[44:47],v2 offset:33792
ds_read_b128 v[48:51],v2 offset:34816
ds_read_b128 v[52:55],v2 offset:35840
ds_read_b128 v[56:59],v2 offset:36864
ds_read_b128 v[60:63],v2 offset:37888
ds_read_b128 v[64:67],v2 offset:38912
s_mul_i32 s70,s47,5
s_lshr_b32 s70,s70,6
buffer_store_dword v5,v4,s[36:39],s70 offen offset:0 sc0 sc1
s_waitcnt lgkmcnt(7)
v_cmp_eq_u32 vcc,s60,v13
s_nop 0
s_cmp_eq_u32 vcc_hi,-1
s_cselect_b32 s67,s50,s59
s_xor_b32 s62,s63,65536
s_cmp_ge_u32 s67,s59
s_cselect_b32 s68,s50,s51
s_lshr_b32 s68,s68,5
s_add_u32 m0,s78,256
s_nop 0
buffer_load_dword v6,s[36:39],s68 offen offset:0 sc1 lds
s_mov_b32 m0,s62
s_nop 0
buffer_load_dwordx4 v0,s[36:39],s67 offen offset:0 sc1 lds
buffer_load_dwordx4 v0,s[36:39],s67 offen offset:1024 sc1 lds
buffer_load_dwordx4 v0,s[36:39],s67 offen offset:2048 sc1 lds
buffer_load_dwordx4 v0,s[36:39],s67 offen offset:3072 sc1 lds
s_mov_b32 s47,s48
s_mov_b32 s48,s59
s_mul_i32 s70,s59,5
s_lshr_b32 s70,s70,6
buffer_store_dword v5,v4,s[36:39],s70 offen offset:0 sc0 sc1
s_waitcnt lgkmcnt(3)
s_setpc_b64 s[92:93]
Lg3o_done1_%=:
s_waitcnt vmcnt(0)
s_barrier
s_mul_i32 s70,s47,5
s_lshr_b32 s70,s70,6
buffer_store_dword v5,v4,s[36:39],s70 offen offset:0 sc0 sc1
s_mul_i32 s70,s48,5
s_lshr_b32 s70,s70,6
buffer_store_dword v5,v4,s[36:39],s70 offen offset:0 sc0 sc1
s_branch Lg3o_done_%=
Lg3o_done_%=:
s_waitcnt vmcnt(0) lgkmcnt(0)
s_barrier
s_endpgm
Lcode_g3o_e:)"
  :: "s"(ws), "s"(wave), "s"(sStep), "s"(sX1), "s"(sX2), "s"(sU0), "s"(sWw), "s"(sBuf), "v"(vSrc), "v"(vA), "v"(vOut), "v"(vFlag), "v"(vPollOff), "v"(vSnapRd), "v"(vX), "v"(vAuxS), "v"(vStW), "v"(vStR), "v"(vWd0), "v"(vWd1), "v"(vWr0), "v"(vWr1), "v"(vWr2), "s"(w3) : "v0", "v1", "v2", "v3", "v4", "v5", "v6", "v7", "v8", "v9", "v10", "v11", "v12", "v13", "v14", "v15", "v16", "v17", "v18", "v19", "v20", "v21", "v22", "v23", "v24", "v25", "v26", "v27", "v28", "v29", "v30", "v31", "v32", "v33", "v34", "v35", "v36", "v37", "v38", "v39", "v40", "v41", "v42", "v43", "v44", "v45", "v46", "v47", "v48", "v49", "v50", "v51", "v52", "v53", "v54", "v55", "v56", "v57", "v58", "v59", "v60", "v61", "v62", "v63", "v64", "v65", "v66", "v67", "v68", "v69", "v70", "v71", "v72", "v73", "v74", "v75", "v76", "v77", "v78", "v79", "v80", "v81", "v82", "v83", "v84", "v85", "v86", "v87", "v88", "v89", "v90", "v91", "v92", "v93", "v94", "v95", "v96", "v97", "v98", "v99", "v100", "v101", "v102", "v103", "v104", "v105", "v106", "v107", "v108", "v109", "v110", "v111", "v112", "v113", "v114", "v115", "v116", "v117", "v118", "v119", "v120", "v121", "v122", "v123", "v124", "v125", "v126", "v127", "v128", "v129", "v130", "v131", "v132", "v133", "v134", "v135", "v136", "v137", "v138", "v139", "v140", "v141", "v142", "v143", "v144", "v145", "v146", "v147", "v148", "v149", "v150", "v151", "v152", "v153", "v154", "v155", "v156", "v157", "v158", "v159", "v160", "v161", "v162", "v163", "v164", "v165", "v166", "v167", "v168", "v169", "v170", "v171", "v172", "v173", "v174", "v175", "v176", "v177", "v178", "v179", "v180", "v181", "v182", "v183", "v184", "v185", "v186", "v187", "v188", "v189", "v190", "v191", "v192", "v193", "v194", "v195", "v196", "v197", "v198", "v199", "v200", "v201", "v202", "v203", "v204", "v205", "v206", "v207", "v208", "v209", "v210", "v211", "v212", "v213", "v214", "v215", "v216", "v217", "v218", "v219", "v220", "v221", "v222", "v223", "v224", "v225", "v226", "v227", "v228", "v229", "v230", "v231", "v255", "a0", "a1", "a2", "a3", "a4", "a5", "a6", "a7", "a8", "a9", "a10", "a11", "a12", "a13", "a14", "a15", "a16", "a17", "a18", "a19", "a20", "a21", "a22", "a23", "a24", "a25", "a26", "a27", "a28", "a29", "a30", "a31", "a32", "a33", "a34", "a35", "a36", "a37", "a38", "a39", "a40", "a41", "a42", "a43", "a44", "a45", "a46", "a47", "a48", "a49", "a50", "a51", "a52", "a53", "a54", "a55", "a56", "a57", "a58", "a59", "a60", "a61", "a62", "a63", "a64", "a65", "a66", "a67", "a68", "a69", "a70", "a71", "a72", "a73", "a74", "a75", "a76", "a77", "a78", "a79", "a80", "a81", "a82", "a83", "a84", "a85", "a86", "a87", "a88", "a89", "a90", "a91", "a92", "a93", "a94", "a95", "a96", "a97", "a98", "a99", "a100", "a101", "a102", "a103", "a104", "a105", "a106", "a107", "a108", "a109", "a110", "a111", "a112", "a113", "a114", "a115", "a116", "a117", "a118", "a119", "a120", "a121", "a122", "a123", "a124", "a125", "a126", "a127", "a128", "a129", "a130", "a131", "a132", "a133", "a134", "a135", "a136", "a137", "a138", "a139", "a140", "a141", "a142", "a143", "a144", "a145", "a146", "a147", "a148", "a149", "a150", "a151", "a152", "a153", "a154", "a155", "a156", "a157", "a158", "a159", "a160", "a161", "a162", "a163", "a164", "a165", "a166", "a167", "a168", "a169", "a170", "a171", "a172", "a173", "a174", "a175", "a176", "a177", "a178", "a179", "a180", "a181", "a182", "a183", "a184", "a185", "a186", "a187", "a188", "a189", "a190", "a191", "a192", "a193", "a194", "a195", "a196", "a197", "a198", "a199", "a200", "a201", "a202", "a203", "a204", "a205", "a206", "a207", "a208", "a209", "a210", "a211", "a212", "a213", "a214", "a215", "a216", "a217", "a218", "a219", "a220", "a221", "a222", "a223", "a224", "a225", "a226", "a227", "a228", "a229", "a230", "a231", "a232", "a233", "a234", "a235", "a236", "a237", "a238", "a239", "a240", "a241", "a242", "a243", "a244", "a245", "a246", "a247", "a248", "a249", "a250", "a251", "a252", "a253", "a254", "a255", "s36", "s37", "s38", "s39", "s40", "s41", "s42", "s43", "s44", "s45", "s46", "s47", "s48", "s49", "s50", "s51", "s52", "s53", "s54", "s55", "s56", "s57", "s58", "s59", "s60", "s61", "s62", "s63", "s64", "s65", "s66", "s67", "s68", "s69", "s70", "s71", "s72", "s73", "s74", "s75", "s76", "s77", "s78", "s79", "s80", "s81", "s82", "s83", "s84", "s85", "s86", "s87", "s88", "s89", "s90", "s91", "s92", "s93", "s94", "s95", "s96", "s97", "s98", "s99", "vcc", "scc", "memory"); }
    __builtin_unreachable();
  } else if (kind == 4) {
    COMMON(kh == 1 ? 6 : 5, OFF_FG + ((14 * kh) / 3) * 128, OFF_FP + (kh * 4 + slice) * 128, 65536, 256, 2048, 5);
    const unsigned vSrc = OFF_G + kh * 28672 + wave * 4096 + vA, vSrc2 = OFF_G + kh * 28672 + 16384 + wave * 3072 + vA;
    const unsigned vOut = (kh == 0 ? OFF_P0 : kh == 1 ? OFF_P1 : kh == 2 ? OFF_P2 : OFF_P3) + (l15 >> 3) * 16384 + (slice * 8 + wave * 2 + (kh >> 1)) * 512 + lg * 128 + (l15 & 7) * 16;
    const unsigned vOut2 = vOut ^ 512;
    const unsigned vAuxS = ((kh & 1) ? OFF_XP : OFF_X1) + (slice * 8 + wave * 2 + 1 - (kh >> 1)) * 1024 + vA;
    const unsigned sWw = __builtin_amdgcn_readfirstlane(kh * 896 * 2048 + slice * 512 + wave * 128);
    asm volatile(R"(
Lcode_g4_b:
s_mov_b64 s[36:37],%0
s_mov_b32 s38,122727552
s_mov_b32 s39,0x00020000
s_mov_b32 s54,%1
s_mov_b32 s55,%2
s_mov_b32 s56,%3
s_mov_b32 s58,%4
s_mov_b32 s83,%5
s_mov_b32 s82,%6
s_mov_b32 s84,%7
v_mov_b32 v0,%8
v_mov_b32 v1,%9
v_mov_b32 v2,%10
v_mov_b32 v3,%11
v_mov_b32 v4,%12
v_mov_b32 v5,%13
v_mov_b32 v7,%14
v_mov_b32 v8,%15
v_mov_b32 v9,%16
v_mov_b32 v12,%17
v_mov_b32 v13,%18
v_mov_b32 v14,%19
v_mov_b32 v15,%20
v_mov_b32 v16,%21
s_mov_b64 s[40:41],%22
s_mov_b32 s42,7340032
s_mov_b32 s43,0x00020000
s_lshl_b32 s88,s54,12
s_mul_i32 s68,s54,9216
s_add_u32 s68,s68,122880
v_add_u32 v10,s68,v2
s_mov_b32 s69,122727424
buffer_load_dword v6,off,s[36:39],s69 sc1
s_add_u32 m0,s88,0
s_add_u32 s61,s82,0
buffer_load_dwordx4 v12,s[40:43],s61 offen offset:0  lds
s_add_u32 m0,s88,1024
s_nop 0
buffer_load_dwordx4 v13,s[40:43],s61 offen offset:0  lds
s_add_u32 m0,s88,2048
s_add_u32 s61,s82,32768
buffer_load_dwordx4 v12,s[40:43],s61 offen offset:0  lds
s_add_u32 m0,s88,3072
s_nop 0
buffer_load_dwordx4 v13,s[40:43],s61 offen offset:0  lds
s_add_u32 m0,s88,16384
s_add_u32 s61,s82,65536
buffer_load_dwordx4 v12,s[40:43],s61 offen offset:0  lds
s_add_u32 m0,s88,17408
s_nop 0
buffer_load_dwordx4 v13,s[40:43],s61 offen offset:0  lds
s_add_u32 m0,s88,18432
s_add_u32 s61,s82,98304
buffer_load_dwordx4 v12,s[40:43],s61 offen offset:0  lds
s_add_u32 m0,s88,19456
s_nop 0
buffer_load_dwordx4 v13,s[40:43],s61 offen offset:0  lds
s_add_u32 m0,s88,32768
s_add_u32 s61,s82,131072
buffer_load_dwordx4 v12,s[40:43],s61 offen offset:0  lds
s_add_u32 m0,s88,33792
s_nop 0
buffer_load_dwordx4 v13,s[40:43],s61 offen offset:0  lds
s_add_u32 m0,s88,34816
s_add_u32 s61,s82,163840
buffer_load_dwordx4 v12,s[40:43],s61 offen offset:0  lds
s_add_u32 m0,s88,35840
s_nop 0
buffer_load_dwordx4 v13,s[40:43],s61 offen offset:0  lds
s_add_u32 m0,s88,49152
s_add_u32 s61,s82,196608
buffer_load_dwordx4 v12,s[40:43],s61 offen offset:0  lds
s_add_u32 m0,s88,50176
s_nop 0
buffer_load_dwordx4 v13,s[40:43],s61 offen offset:0  lds
s_add_u32 m0,s88,51200
s_add_u32 s61,s82,229376
buffer_load_dwordx4 v12,s[40:43],s61 offen offset:0  lds
s_add_u32 m0,s88,52224
s_nop 0
buffer_load_dwordx4 v13,s[40:43],s61 offen offset:0  lds
s_add_u32 m0,s88,65536
s_add_u32 s61,s82,262144
buffer_load_dwordx4 v12,s[40:43],s61 offen offset:0  lds
s_add_u32 m0,s88,66560
s_nop 0
buffer_load_dwordx4 v13,s[40:43],s61 offen offset:0  lds
s_add_u32 m0,s88,67584
s_add_u32 s61,s82,294912
buffer_load_dwordx4 v12,s[40:43],s61 offen offset:0  lds
s_add_u32 m0,s88,68608
s_nop 0
buffer_load_dwordx4 v13,s[40:43],s61 offen offset:0  lds
v_add_u32 v28,49152,v14
v_add_u32 v30,49152,v16
v_add_u32 v32,98304,v14
v_add_u32 v34,98304,v16
s_waitcnt vmcnt(16)
s_add_u32 m0,s88,81920
s_add_u32 s61,s82,327680
buffer_load_dwordx4 v12,s[40:43],s61 offen offset:0  lds
s_add_u32 m0,s88,82944
ds_read_b64_tr_b16 a[0:1],v14 offset:0
buffer_load_dwordx4 v13,s[40:43],s61 offen offset:0  lds
s_add_u32 m0,s88,83968
s_add_u32 s61,s82,360448
buffer_load_dwordx4 v12,s[40:43],s61 offen offset:0  lds
s_add_u32 m0,s88,84992
ds_read_b64_tr_b16 a[2:3],v14 offset:512
buffer_load_dwordx4 v13,s[40:43],s61 offen offset:0  lds
ds_read_b64_tr_b16 a[4:5],v14 offset:8
ds_read_b64_tr_b16 a[6:7],v14 offset:520
ds_read_b64_tr_b16 a[8:9],v16 offset:0
ds_read_b64_tr_b16 a[10:11],v16 offset:512
ds_read_b64_tr_b16 a[12:13],v16 offset:8
ds_read_b64_tr_b16 a[14:15],v16 offset:520
s_waitcnt vmcnt(16)
s_add_u32 m0,s88,98304
s_add_u32 s61,s82,393216
buffer_load_dwordx4 v12,s[40:43],s61 offen offset:0  lds
s_add_u32 m0,s88,99328
ds_read_b64_tr_b16 a[16:17],v14 offset:16384
buffer_load_dwordx4 v13,s[40:43],s61 offen offset:0  lds
s_add_u32 m0,s88,100352
s_add_u32 s61,s82,425984
buffer_load_dwordx4 v12,s[40:43],s61 offen offset:0  lds
s_add_u32 m0,s88,101376
ds_read_b64_tr_b16 a[18:19],v14 offset:16896
buffer_load_dwordx4 v13,s[40:43],s61 offen offset:0  lds
ds_read_b64_tr_b16 a[20:21],v14 offset:16392
ds_read_b64_tr_b16 a[22:23],v14 offset:16904
ds_read_b64_tr_b16 a[24:25],v16 offset:16384
ds_read_b64_tr_b16 a[26:27],v16 offset:16896
ds_read_b64_tr_b16 a[28:29],v16 offset:16392
ds_read_b64_tr_b16 a[30:31],v16 offset:16904
s_waitcnt lgkmcnt(8)
s_waitcnt vmcnt(16)
s_add_u32 m0,s88,0
s_add_u32 s61,s82,458752
buffer_load_dwordx4 v12,s[40:43],s61 offen offset:0  lds
s_add_u32 m0,s88,1024
ds_read_b64_tr_b16 a[32:33],v14 offset:32768
buffer_load_dwordx4 v13,s[40:43],s61 offen offset:0  lds
s_add_u32 m0,s88,2048
s_add_u32 s61,s82,491520
buffer_load_dwordx4 v12,s[40:43],s61 offen offset:0  lds
s_add_u32 m0,s88,3072
ds_read_b64_tr_b16 a[34:35],v14 offset:33280
buffer_load_dwordx4 v13,s[40:43],s61 offen offset:0  lds
ds_read_b64_tr_b16 a[36:37],v14 offset:32776
ds_read_b64_tr_b16 a[38:39],v14 offset:33288
ds_read_b64_tr_b16 a[40:41],v16 offset:32768
ds_read_b64_tr_b16 a[42:43],v16 offset:33280
ds_read_b64_tr_b16 a[44:45],v16 offset:32776
ds_read_b64_tr_b16 a[46:47],v16 offset:33288
s_waitcnt lgkmcnt(8)
s_waitcnt vmcnt(16)
s_add_u32 m0,s88,16384
s_add_u32 s61,s82,524288
buffer_load_dwordx4 v12,s[40:43],s61 offen offset:0  lds
s_add_u32 m0,s88,17408
ds_read_b64_tr_b16 a[48:49],v28 offset:0
buffer_load_dwordx4 v13,s[40:43],s61 offen offset:0  lds
s_add_u32 m0,s88,18432
s_add_u32 s61,s82,557056
buffer_load_dwordx4 v12,s[40:43],s61 offen offset:0  lds
s_add_u32 m0,s88,19456
ds_read_b64_tr_b16 a[50:51],v28 offset:512
buffer_load_dwordx4 v13,s[40:43],s61 offen offset:0  lds
ds_read_b64_tr_b16 a[52:53],v28 offset:8
ds_read_b64_tr_b16 a[54:55],v28 offset:520
ds_read_b64_tr_b16 a[56:57],v30 offset:0
ds_read_b64_tr_b16 a[58:59],v30 offset:512
ds_read_b64_tr_b16 a[60:61],v30 offset:8
ds_read_b64_tr_b16 a[62:63],v30 offset:520
s_waitcnt lgkmcnt(8)
s_waitcnt vmcnt(16)
s_add_u32 m0,s88,32768
s_add_u32 s61,s82,589824
buffer_load_dwordx4 v12,s[40:43],s61 offen offset:0  lds
s_add_u32 m0,s88,33792
ds_read_b64_tr_b16 a[64:65],v28 offset:16384
buffer_load_dwordx4 v13,s[40:43],s61 offen offset:0  lds
s_add_u32 m0,s88,34816
s_add_u32 s61,s82,622592
buffer_load_dwordx4 v12,s[40:43],s61 offen offset:0  lds
s_add_u32 m0,s88,35840
ds_read_b64_tr_b16 a[66:67],v28 offset:16896
buffer_load_dwordx4 v13,s[40:43],s61 offen offset:0  lds
ds_read_b64_tr_b16 a[68:69],v28 offset:16392
ds_read_b64_tr_b16 a[70:71],v28 offset:16904
ds_read_b64_tr_b16 a[72:73],v30 offset:16384
ds_read_b64_tr_b16 a[74:75],v30 offset:16896
ds_read_b64_tr_b16 a[76:77],v30 offset:16392
ds_read_b64_tr_b16 a[78:79],v30 offset:16904
s_waitcnt lgkmcnt(8)
s_waitcnt vmcnt(16)
s_add_u32 m0,s88,49152
s_add_u32 s61,s82,655360)"
R"(
buffer_load_dwordx4 v12,s[40:43],s61 offen offset:0  lds
s_add_u32 m0,s88,50176
ds_read_b64_tr_b16 a[80:81],v28 offset:32768
buffer_load_dwordx4 v13,s[40:43],s61 offen offset:0  lds
s_add_u32 m0,s88,51200
s_add_u32 s61,s82,688128
buffer_load_dwordx4 v12,s[40:43],s61 offen offset:0  lds
s_add_u32 m0,s88,52224
ds_read_b64_tr_b16 a[82:83],v28 offset:33280
buffer_load_dwordx4 v13,s[40:43],s61 offen offset:0  lds
ds_read_b64_tr_b16 a[84:85],v28 offset:32776
ds_read_b64_tr_b16 a[86:87],v28 offset:33288
ds_read_b64_tr_b16 a[88:89],v30 offset:32768
ds_read_b64_tr_b16 a[90:91],v30 offset:33280
ds_read_b64_tr_b16 a[92:93],v30 offset:32776
ds_read_b64_tr_b16 a[94:95],v30 offset:33288
s_waitcnt lgkmcnt(8)
s_waitcnt vmcnt(16)
s_add_u32 m0,s88,65536
s_add_u32 s61,s82,720896
buffer_load_dwordx4 v12,s[40:43],s61 offen offset:0  lds
s_add_u32 m0,s88,66560
ds_read_b64_tr_b16 a[96:97],v32 offset:0
buffer_load_dwordx4 v13,s[40:43],s61 offen offset:0  lds
s_add_u32 m0,s88,67584
s_add_u32 s61,s82,753664
buffer_load_dwordx4 v12,s[40:43],s61 offen offset:0  lds
s_add_u32 m0,s88,68608
ds_read_b64_tr_b16 a[98:99],v32 offset:512
buffer_load_dwordx4 v13,s[40:43],s61 offen offset:0  lds
ds_read_b64_tr_b16 a[100:101],v32 offset:8
ds_read_b64_tr_b16 a[102:103],v32 offset:520
ds_read_b64_tr_b16 a[104:105],v34 offset:0
ds_read_b64_tr_b16 a[106:107],v34 offset:512
ds_read_b64_tr_b16 a[108:109],v34 offset:8
ds_read_b64_tr_b16 a[110:111],v34 offset:520
s_waitcnt lgkmcnt(8)
s_waitcnt vmcnt(16)
s_add_u32 m0,s88,81920
s_add_u32 s61,s82,786432
buffer_load_dwordx4 v12,s[40:43],s61 offen offset:0  lds
s_add_u32 m0,s88,82944
ds_read_b64_tr_b16 a[112:113],v14 offset:0
buffer_load_dwordx4 v13,s[40:43],s61 offen offset:0  lds
s_add_u32 m0,s88,83968
s_add_u32 s61,s82,819200
buffer_load_dwordx4 v12,s[40:43],s61 offen offset:0  lds
s_add_u32 m0,s88,84992
ds_read_b64_tr_b16 a[114:115],v14 offset:512
buffer_load_dwordx4 v13,s[40:43],s61 offen offset:0  lds
ds_read_b64_tr_b16 a[116:117],v14 offset:8
ds_read_b64_tr_b16 a[118:119],v14 offset:520
ds_read_b64_tr_b16 a[120:121],v16 offset:0
ds_read_b64_tr_b16 a[122:123],v16 offset:512
ds_read_b64_tr_b16 a[124:125],v16 offset:8
ds_read_b64_tr_b16 a[126:127],v16 offset:520
s_waitcnt lgkmcnt(8)
s_waitcnt vmcnt(16)
s_add_u32 m0,s88,98304
s_add_u32 s61,s82,851968
buffer_load_dwordx4 v12,s[40:43],s61 offen offset:0  lds
s_add_u32 m0,s88,99328
ds_read_b64_tr_b16 a[128:129],v14 offset:16384
buffer_load_dwordx4 v13,s[40:43],s61 offen offset:0  lds
s_add_u32 m0,s88,100352
s_add_u32 s61,s82,884736
buffer_load_dwordx4 v12,s[40:43],s61 offen offset:0  lds
s_add_u32 m0,s88,101376
ds_read_b64_tr_b16 a[130:131],v14 offset:16896
buffer_load_dwordx4 v13,s[40:43],s61 offen offset:0  lds
ds_read_b64_tr_b16 a[132:133],v14 offset:16392
ds_read_b64_tr_b16 a[134:135],v14 offset:16904
ds_read_b64_tr_b16 a[136:137],v16 offset:16384
ds_read_b64_tr_b16 a[138:139],v16 offset:16896
ds_read_b64_tr_b16 a[140:141],v16 offset:16392
ds_read_b64_tr_b16 a[142:143],v16 offset:16904
s_waitcnt lgkmcnt(8)
s_waitcnt vmcnt(16)
s_add_u32 m0,s88,0
s_add_u32 s61,s82,917504
buffer_load_dwordx4 v12,s[40:43],s61 offen offset:0  lds
s_add_u32 m0,s88,1024
ds_read_b64_tr_b16 a[144:145],v14 offset:32768
buffer_load_dwordx4 v13,s[40:43],s61 offen offset:0  lds
s_add_u32 m0,s88,2048
s_add_u32 s61,s82,950272
buffer_load_dwordx4 v12,s[40:43],s61 offen offset:0  lds
s_add_u32 m0,s88,3072
ds_read_b64_tr_b16 a[146:147],v14 offset:33280
buffer_load_dwordx4 v13,s[40:43],s61 offen offset:0  lds
ds_read_b64_tr_b16 a[148:149],v14 offset:32776
ds_read_b64_tr_b16 a[150:151],v14 offset:33288
ds_read_b64_tr_b16 a[152:153],v16 offset:32768
ds_read_b64_tr_b16 a[154:155],v16 offset:33280
ds_read_b64_tr_b16 a[156:157],v16 offset:32776
ds_read_b64_tr_b16 a[158:159],v16 offset:33288
s_waitcnt lgkmcnt(8)
s_waitcnt vmcnt(16)
s_add_u32 m0,s88,16384
s_add_u32 s61,s82,983040
buffer_load_dwordx4 v12,s[40:43],s61 offen offset:0  lds
s_add_u32 m0,s88,17408
ds_read_b64_tr_b16 a[160:161],v28 offset:0
buffer_load_dwordx4 v13,s[40:43],s61 offen offset:0  lds
s_add_u32 m0,s88,18432
s_add_u32 s61,s82,1015808
buffer_load_dwordx4 v12,s[40:43],s61 offen offset:0  lds
s_add_u32 m0,s88,19456
ds_read_b64_tr_b16 a[162:163],v28 offset:512
buffer_load_dwordx4 v13,s[40:43],s61 offen offset:0  lds
ds_read_b64_tr_b16 a[164:165],v28 offset:8
ds_read_b64_tr_b16 a[166:167],v28 offset:520
ds_read_b64_tr_b16 a[168:169],v30 offset:0
ds_read_b64_tr_b16 a[170:171],v30 offset:512
ds_read_b64_tr_b16 a[172:173],v30 offset:8
ds_read_b64_tr_b16 a[174:175],v30 offset:520
s_waitcnt lgkmcnt(8)
s_waitcnt vmcnt(16)
s_add_u32 m0,s88,32768
s_add_u32 s61,s82,1048576
buffer_load_dwordx4 v12,s[40:43],s61 offen offset:0  lds
s_add_u32 m0,s88,33792
ds_read_b64_tr_b16 a[176:177],v28 offset:16384
buffer_load_dwordx4 v13,s[40:43],s61 offen offset:0  lds
s_add_u32 m0,s88,34816
s_add_u32 s61,s82,1081344
buffer_load_dwordx4 v12,s[40:43],s61 offen offset:0  lds
s_add_u32 m0,s88,35840
ds_read_b64_tr_b16 a[178:179],v28 offset:16896
buffer_load_dwordx4 v13,s[40:43],s61 offen offset:0  lds
ds_read_b64_tr_b16 a[180:181],v28 offset:16392
ds_read_b64_tr_b16 a[182:183],v28 offset:16904
ds_read_b64_tr_b16 a[184:185],v30 offset:16384
ds_read_b64_tr_b16 a[186:187],v30 offset:16896
ds_read_b64_tr_b16 a[188:189],v30 offset:16392
ds_read_b64_tr_b16 a[190:191],v30 offset:16904
s_waitcnt lgkmcnt(8)
s_waitcnt vmcnt(16)
s_add_u32 m0,s88,49152
s_add_u32 s61,s82,1114112
buffer_load_dwordx4 v12,s[40:43],s61 offen offset:0  lds
s_add_u32 m0,s88,50176
ds_read_b64_tr_b16 a[192:193],v28 offset:32768
buffer_load_dwordx4 v13,s[40:43],s61 offen offset:0  lds
s_add_u32 m0,s88,51200
s_add_u32 s61,s82,1146880
buffer_load_dwordx4 v12,s[40:43],s61 offen offset:0  lds
s_add_u32 m0,s88,52224
ds_read_b64_tr_b16 a[194:195],v28 offset:33280
buffer_load_dwordx4 v13,s[40:43],s61 offen offset:0  lds
ds_read_b64_tr_b16 a[196:197],v28 offset:32776
ds_read_b64_tr_b16 a[198:199],v28 offset:33288
ds_read_b64_tr_b16 a[200:201],v30 offset:32768
ds_read_b64_tr_b16 a[202:203],v30 offset:33280
ds_read_b64_tr_b16 a[204:205],v30 offset:32776
ds_read_b64_tr_b16 a[206:207],v30 offset:33288
s_waitcnt lgkmcnt(8)
s_waitcnt vmcnt(16)
s_add_u32 m0,s88,65536
s_add_u32 s61,s82,1179648
buffer_load_dwordx4 v12,s[40:43],s61 offen offset:0  lds
s_add_u32 m0,s88,66560
ds_read_b64_tr_b16 a[208:209],v32 offset:0
buffer_load_dwordx4 v13,s[40:43],s61 offen offset:0  lds
s_add_u32 m0,s88,67584
s_add_u32 s61,s82,1212416
buffer_load_dwordx4 v12,s[40:43],s61 offen offset:0  lds
s_add_u32 m0,s88,68608
ds_read_b64_tr_b16 a[210:211],v32 offset:512
buffer_load_dwordx4 v13,s[40:43],s61 offen offset:0  lds
ds_read_b64_tr_b16 a[212:213],v32 offset:8
ds_read_b64_tr_b16 a[214:215],v32 offset:520
ds_read_b64_tr_b16 a[216:217],v34 offset:0
ds_read_b64_tr_b16 a[218:219],v34 offset:512
ds_read_b64_tr_b16 a[220:221],v34 offset:8
ds_read_b64_tr_b16 a[222:223],v34 offset:520
s_waitcnt lgkmcnt(8)
s_waitcnt vmcnt(16)
s_add_u32 m0,s88,81920
s_add_u32 s61,s82,1245184
buffer_load_dwordx4 v12,s[40:43],s61 offen offset:0  lds
s_add_u32 m0,s88,82944
ds_read_b64_tr_b16 a[224:225],v14 offset:0
buffer_load_dwordx4 v13,s[40:43],s61 offen offset:0  lds
s_add_u32 m0,s88,83968
s_add_u32 s61,s82,1277952
buffer_load_dwordx4 v12,s[40:43],s61 offen offset:0  lds
s_add_u32 m0,s88,84992
ds_read_b64_tr_b16 a[226:227],v14 offset:512
buffer_load_dwordx4 v13,s[40:43],s61 offen offset:0  lds
ds_read_b64_tr_b16 a[228:229],v14 offset:8
ds_read_b64_tr_b16 a[230:231],v14 offset:520
ds_read_b64_tr_b16 a[232:233],v16 offset:0
ds_read_b64_tr_b16 a[234:235],v16 offset:512
ds_read_b64_tr_b16 a[236:237],v16 offset:8
ds_read_b64_tr_b16 a[238:239],v16 offset:520
s_waitcnt lgkmcnt(8)
s_waitcnt vmcnt(16)
s_add_u32 m0,s88,98304
s_add_u32 s61,s82,1310720)"
R"(
buffer_load_dwordx4 v12,s[40:43],s61 offen offset:0  lds
s_add_u32 m0,s88,99328
ds_read_b64_tr_b16 a[240:241],v14 offset:16384
buffer_load_dwordx4 v13,s[40:43],s61 offen offset:0  lds
s_add_u32 m0,s88,100352
s_add_u32 s61,s82,1343488
buffer_load_dwordx4 v12,s[40:43],s61 offen offset:0  lds
s_add_u32 m0,s88,101376
ds_read_b64_tr_b16 a[242:243],v14 offset:16896
buffer_load_dwordx4 v13,s[40:43],s61 offen offset:0  lds
ds_read_b64_tr_b16 a[244:245],v14 offset:16392
ds_read_b64_tr_b16 a[246:247],v14 offset:16904
ds_read_b64_tr_b16 a[248:249],v16 offset:16384
ds_read_b64_tr_b16 a[250:251],v16 offset:16896
ds_read_b64_tr_b16 a[252:253],v16 offset:16392
ds_read_b64_tr_b16 a[254:255],v16 offset:16904
s_waitcnt lgkmcnt(8)
s_waitcnt vmcnt(16)
s_add_u32 m0,s88,0
s_add_u32 s61,s82,1376256
buffer_load_dwordx4 v12,s[40:43],s61 offen offset:0  lds
s_add_u32 m0,s88,1024
ds_read_b64_tr_b16 v[100:101],v14 offset:32768
buffer_load_dwordx4 v13,s[40:43],s61 offen offset:0  lds
s_add_u32 m0,s88,2048
s_add_u32 s61,s82,1409024
buffer_load_dwordx4 v12,s[40:43],s61 offen offset:0  lds
s_add_u32 m0,s88,3072
ds_read_b64_tr_b16 v[102:103],v14 offset:33280
buffer_load_dwordx4 v13,s[40:43],s61 offen offset:0  lds
ds_read_b64_tr_b16 v[104:105],v14 offset:32776
ds_read_b64_tr_b16 v[106:107],v14 offset:33288
ds_read_b64_tr_b16 v[108:109],v16 offset:32768
ds_read_b64_tr_b16 v[110:111],v16 offset:33280
ds_read_b64_tr_b16 v[112:113],v16 offset:32776
ds_read_b64_tr_b16 v[114:115],v16 offset:33288
s_waitcnt lgkmcnt(8)
s_waitcnt vmcnt(16)
s_add_u32 m0,s88,16384
s_add_u32 s61,s82,1441792
buffer_load_dwordx4 v12,s[40:43],s61 offen offset:0  lds
s_add_u32 m0,s88,17408
ds_read_b64_tr_b16 v[116:117],v28 offset:0
buffer_load_dwordx4 v13,s[40:43],s61 offen offset:0  lds
s_add_u32 m0,s88,18432
s_add_u32 s61,s82,1474560
buffer_load_dwordx4 v12,s[40:43],s61 offen offset:0  lds
s_add_u32 m0,s88,19456
ds_read_b64_tr_b16 v[118:119],v28 offset:512
buffer_load_dwordx4 v13,s[40:43],s61 offen offset:0  lds
ds_read_b64_tr_b16 v[120:121],v28 offset:8
ds_read_b64_tr_b16 v[122:123],v28 offset:520
ds_read_b64_tr_b16 v[124:125],v30 offset:0
ds_read_b64_tr_b16 v[126:127],v30 offset:512
ds_read_b64_tr_b16 v[128:129],v30 offset:8
ds_read_b64_tr_b16 v[130:131],v30 offset:520
s_waitcnt lgkmcnt(8)
s_waitcnt vmcnt(16)
s_add_u32 m0,s88,32768
s_add_u32 s61,s82,1507328
buffer_load_dwordx4 v12,s[40:43],s61 offen offset:0  lds
s_add_u32 m0,s88,33792
ds_read_b64_tr_b16 v[132:133],v28 offset:16384
buffer_load_dwordx4 v13,s[40:43],s61 offen offset:0  lds
s_add_u32 m0,s88,34816
s_add_u32 s61,s82,1540096
buffer_load_dwordx4 v12,s[40:43],s61 offen offset:0  lds
s_add_u32 m0,s88,35840
ds_read_b64_tr_b16 v[134:135],v28 offset:16896
buffer_load_dwordx4 v13,s[40:43],s61 offen offset:0  lds
ds_read_b64_tr_b16 v[136:137],v28 offset:16392
ds_read_b64_tr_b16 v[138:139],v28 offset:16904
ds_read_b64_tr_b16 v[140:141],v30 offset:16384
ds_read_b64_tr_b16 v[142:143],v30 offset:16896
ds_read_b64_tr_b16 v[144:145],v30 offset:16392
ds_read_b64_tr_b16 v[146:147],v30 offset:16904
s_waitcnt lgkmcnt(8)
s_waitcnt vmcnt(16)
s_add_u32 m0,s88,49152
s_add_u32 s61,s82,1572864
buffer_load_dwordx4 v12,s[40:43],s61 offen offset:0  lds
s_add_u32 m0,s88,50176
ds_read_b64_tr_b16 v[148:149],v28 offset:32768
buffer_load_dwordx4 v13,s[40:43],s61 offen offset:0  lds
s_add_u32 m0,s88,51200
s_add_u32 s61,s82,1605632
buffer_load_dwordx4 v12,s[40:43],s61 offen offset:0  lds
s_add_u32 m0,s88,52224
ds_read_b64_tr_b16 v[150:151],v28 offset:33280
buffer_load_dwordx4 v13,s[40:43],s61 offen offset:0  lds
ds_read_b64_tr_b16 v[152:153],v28 offset:32776
ds_read_b64_tr_b16 v[154:155],v28 offset:33288
ds_read_b64_tr_b16 v[156:157],v30 offset:32768
ds_read_b64_tr_b16 v[158:159],v30 offset:33280
ds_read_b64_tr_b16 v[52:53],v30 offset:32776
ds_read_b64_tr_b16 v[54:55],v30 offset:33288
s_waitcnt lgkmcnt(8)
s_waitcnt vmcnt(16)
s_add_u32 m0,s88,65536
s_add_u32 s61,s82,1638400
buffer_load_dwordx4 v12,s[40:43],s61 offen offset:0  lds
s_add_u32 m0,s88,66560
ds_read_b64_tr_b16 v[160:161],v32 offset:0
buffer_load_dwordx4 v13,s[40:43],s61 offen offset:0  lds
s_add_u32 m0,s88,67584
s_add_u32 s61,s82,1671168
buffer_load_dwordx4 v12,s[40:43],s61 offen offset:0  lds
s_add_u32 m0,s88,68608
ds_read_b64_tr_b16 v[162:163],v32 offset:512
buffer_load_dwordx4 v13,s[40:43],s61 offen offset:0  lds
ds_read_b64_tr_b16 v[164:165],v32 offset:8
ds_read_b64_tr_b16 v[166:167],v32 offset:520
ds_read_b64_tr_b16 v[168:169],v34 offset:0
ds_read_b64_tr_b16 v[170:171],v34 offset:512
ds_read_b64_tr_b16 v[44:45],v34 offset:8
ds_read_b64_tr_b16 v[46:47],v34 offset:520
s_waitcnt lgkmcnt(8)
ds_write_b128 v10,v[52:55] offset:0
s_waitcnt vmcnt(16)
s_add_u32 m0,s88,81920
s_add_u32 s61,s82,1703936
buffer_load_dwordx4 v12,s[40:43],s61 offen offset:0  lds
s_add_u32 m0,s88,82944
ds_read_b64_tr_b16 v[172:173],v14 offset:0
buffer_load_dwordx4 v13,s[40:43],s61 offen offset:0  lds
s_add_u32 m0,s88,83968
s_add_u32 s61,s82,1736704
buffer_load_dwordx4 v12,s[40:43],s61 offen offset:0  lds
s_add_u32 m0,s88,84992
ds_read_b64_tr_b16 v[174:175],v14 offset:512
buffer_load_dwordx4 v13,s[40:43],s61 offen offset:0  lds
ds_read_b64_tr_b16 v[176:177],v14 offset:8
ds_read_b64_tr_b16 v[178:179],v14 offset:520
ds_read_b64_tr_b16 v[180:181],v16 offset:0
ds_read_b64_tr_b16 v[182:183],v16 offset:512
ds_read_b64_tr_b16 v[52:53],v16 offset:8
ds_read_b64_tr_b16 v[54:55],v16 offset:520
s_waitcnt lgkmcnt(8)
ds_write_b128 v10,v[44:47] offset:1024
s_waitcnt vmcnt(16)
s_add_u32 m0,s88,98304
s_add_u32 s61,s82,1769472
buffer_load_dwordx4 v12,s[40:43],s61 offen offset:0  lds
s_add_u32 m0,s88,99328
ds_read_b64_tr_b16 v[184:185],v14 offset:16384
buffer_load_dwordx4 v13,s[40:43],s61 offen offset:0  lds
s_add_u32 m0,s88,100352
s_add_u32 s61,s82,1802240
buffer_load_dwordx4 v12,s[40:43],s61 offen offset:0  lds
s_add_u32 m0,s88,101376
ds_read_b64_tr_b16 v[186:187],v14 offset:16896
buffer_load_dwordx4 v13,s[40:43],s61 offen offset:0  lds
ds_read_b64_tr_b16 v[188:189],v14 offset:16392
ds_read_b64_tr_b16 v[190:191],v14 offset:16904
ds_read_b64_tr_b16 v[192:193],v16 offset:16384
ds_read_b64_tr_b16 v[194:195],v16 offset:16896
ds_read_b64_tr_b16 v[44:45],v16 offset:16392
ds_read_b64_tr_b16 v[46:47],v16 offset:16904
s_waitcnt lgkmcnt(8)
ds_write_b128 v10,v[52:55] offset:2048
s_waitcnt vmcnt(16)
ds_read_b64_tr_b16 v[196:197],v14 offset:32768
ds_read_b64_tr_b16 v[198:199],v14 offset:33280
ds_read_b64_tr_b16 v[200:201],v14 offset:32776
ds_read_b64_tr_b16 v[202:203],v14 offset:33288
ds_read_b64_tr_b16 v[204:205],v16 offset:32768
ds_read_b64_tr_b16 v[206:207],v16 offset:33280
ds_read_b64_tr_b16 v[52:53],v16 offset:32776
ds_read_b64_tr_b16 v[54:55],v16 offset:33288
s_getpc_b64 s[46:47]
Lg4_cp_%=:
s_add_u32 s46,s46,Lg4_init_%=-Lg4_cp_%=
s_addc_u32 s47,s47,0
s_lshl_b32 s68,s54,13
v_lshl_add_u32 v76,v2,3,s68
v_min_u32 v77,Lg4_done_%=-Lg4_init_%=,v76
global_load_dword v80,v77,s[46:47]
s_waitcnt lgkmcnt(8)
ds_write_b128 v10,v[44:47] offset:3072
s_waitcnt vmcnt(13)
ds_read_b64_tr_b16 v[208:209],v28 offset:0
ds_read_b64_tr_b16 v[210:211],v28 offset:512
ds_read_b64_tr_b16 v[212:213],v28 offset:8
ds_read_b64_tr_b16 v[214:215],v28 offset:520
ds_read_b64_tr_b16 v[216:217],v30 offset:0
ds_read_b64_tr_b16 v[218:219],v30 offset:512
ds_read_b64_tr_b16 v[44:45],v30 offset:8
ds_read_b64_tr_b16 v[46:47],v30 offset:520
s_waitcnt lgkmcnt(8)
ds_write_b128 v10,v[52:55] offset:4096
s_waitcnt vmcnt(9)
ds_read_b64_tr_b16 v[220:221],v28 offset:16384
ds_read_b64_tr_b16 v[222:223],v28 offset:16896
ds_read_b64_tr_b16 v[224:225],v28 offset:16392
ds_read_b64_tr_b16 v[226:227],v28 offset:16904
ds_read_b64_tr_b16 v[228:229],v30 offset:16384
ds_read_b64_tr_b16 v[230:231],v30 offset:16896
ds_read_b64_tr_b16 v[52:53],v30 offset:16392
ds_read_b64_tr_b16 v[54:55],v30 offset:16904
s_waitcnt lgkmcnt(8))"
R"(
ds_write_b128 v10,v[44:47] offset:5120
s_waitcnt vmcnt(5)
ds_read_b64_tr_b16 v[232:233],v28 offset:32768
ds_read_b64_tr_b16 v[234:235],v28 offset:33280
ds_read_b64_tr_b16 v[236:237],v28 offset:32776
ds_read_b64_tr_b16 v[238:239],v28 offset:33288
ds_read_b64_tr_b16 v[240:241],v30 offset:32768
ds_read_b64_tr_b16 v[242:243],v30 offset:33280
ds_read_b64_tr_b16 v[44:45],v30 offset:32776
ds_read_b64_tr_b16 v[46:47],v30 offset:33288
s_waitcnt lgkmcnt(8)
ds_write_b128 v10,v[52:55] offset:6144
s_waitcnt vmcnt(1)
ds_read_b64_tr_b16 v[244:245],v32 offset:0
ds_read_b64_tr_b16 v[246:247],v32 offset:512
ds_read_b64_tr_b16 v[248:249],v32 offset:8
ds_read_b64_tr_b16 v[250:251],v32 offset:520
ds_read_b64_tr_b16 v[252:253],v34 offset:0
ds_read_b64_tr_b16 v[254:255],v34 offset:512
ds_read_b64_tr_b16 v[52:53],v34 offset:8
ds_read_b64_tr_b16 v[54:55],v34 offset:520
s_waitcnt lgkmcnt(8)
ds_write_b128 v10,v[44:47] offset:7168
s_waitcnt lgkmcnt(0)
ds_write_b128 v10,v[52:55] offset:8192
s_waitcnt lgkmcnt(0)
s_barrier
v_readfirstlane_b32 s60,v6
s_nop 3
s_add_u32 s60,s60,1
s_cmp_eq_u32 s60,0
s_cselect_b32 s60,1,s60
Lg4_init_%=:
v_mov_b32 v6,s60
s_mov_b32 s59,536870912
s_add_u32 s57,s56,131072
s_mov_b32 s79,0
s_mov_b32 s75,0x3a800000
s_mov_b32 s76,0xc0135761
s_mov_b32 s77,0x3d372713
s_mov_b32 s44,s59
s_mov_b32 s45,s59
s_mov_b32 s46,s59
s_mov_b32 s47,s59
s_mov_b32 s48,s83
s_add_u32 s49,s48,s55
s_cmp_eq_u32 s49,s56
s_cselect_b32 s49,s57,s49
s_cmp_eq_u32 s49,s58
s_cselect_b32 s49,s59,s49
s_add_u32 s50,s49,s55
s_cmp_eq_u32 s50,s56
s_cselect_b32 s50,s57,s50
s_cmp_eq_u32 s50,s58
s_cselect_b32 s50,s59,s50
s_add_u32 s51,s50,s55
s_cmp_eq_u32 s51,s56
s_cselect_b32 s51,s57,s51
s_cmp_eq_u32 s51,s58
s_cselect_b32 s51,s59,s51
s_add_u32 s52,s51,s55
s_cmp_eq_u32 s52,s56
s_cselect_b32 s52,s57,s52
s_cmp_eq_u32 s52,s58
s_cselect_b32 s52,s59,s52
s_lshl_b32 s63,s54,12
s_mul_i32 s64,s54,3072
s_add_u32 s64,s64,16384
s_lshl_b32 s78,s54,8
s_add_u32 s78,s78,65536
s_getpc_b64 s[90:91]
Lg4_pc_%=:
s_mov_b32 s68,Lg4_d1w0_%=-Lg4_pc_%=
s_cmp_eq_u32 s54,1
s_cselect_b32 s68,Lg4_d1w1_%=-Lg4_pc_%=,s68
s_cmp_eq_u32 s54,2
s_cselect_b32 s68,Lg4_d1w2_%=-Lg4_pc_%=,s68
s_cmp_eq_u32 s54,3
s_cselect_b32 s68,Lg4_d1w3_%=-Lg4_pc_%=,s68
s_add_u32 s92,s90,s68
s_addc_u32 s93,s91,0
s_mov_b32 s68,Lg4_d0w0_%=-Lg4_pc_%=
s_cmp_eq_u32 s54,1
s_cselect_b32 s68,Lg4_d0w1_%=-Lg4_pc_%=,s68
s_cmp_eq_u32 s54,2
s_cselect_b32 s68,Lg4_d0w2_%=-Lg4_pc_%=,s68
s_cmp_eq_u32 s54,3
s_cselect_b32 s68,Lg4_d0w3_%=-Lg4_pc_%=,s68
s_add_u32 s90,s90,s68
s_addc_u32 s91,s91,0
s_mov_b32 s66,s59
s_mov_b32 s65,0
s_mov_b32 s62,s63
s_branch Lg4_slowb0_%=
Lg4_d0w0_%=:
v_mfma_f32_16x16x32_bf16 v[12:15],a[0:3],v[44:47],0
s_max_u32 s73,s47,s65
ds_read_b128 v[56:59],v2 offset:3072
v_lshlrev_b32 v60,16,v68
v_mfma_f32_16x16x32_bf16 v[16:19],a[4:7],v[44:47],0
s_mov_b32 s65,0
v_and_b32 v61,0xffff0000,v68
v_lshlrev_b32 v62,16,v69
v_mfma_f32_16x16x32_bf16 v[20:23],a[8:11],v[44:47],0
s_add_u32 s53,s52,s55
v_and_b32 v63,0xffff0000,v69
v_lshlrev_b32 v64,16,v70
v_mfma_f32_16x16x32_bf16 v[24:27],a[12:15],v[44:47],0
s_waitcnt lgkmcnt(2)
v_and_b32 v65,0xffff0000,v70
v_lshlrev_b32 v66,16,v71
v_mfma_f32_16x16x32_bf16 v[12:15],a[16:19],v[48:51],v[12:15]
s_cmp_eq_u32 s53,s56
ds_read_b128 v[44:47],v2 offset:4096
v_and_b32 v67,0xffff0000,v71
v_mfma_f32_16x16x32_bf16 v[16:19],a[20:23],v[48:51],v[16:19]
s_cselect_b32 s53,s57,s53
v_cvt_pk_bf16_f32 v28,v28,v29
v_cvt_pk_bf16_f32 v29,v30,v31
v_mfma_f32_16x16x32_bf16 v[20:23],a[24:27],v[48:51],v[20:23]
s_cmp_eq_u32 s53,s58
v_cvt_pk_bf16_f32 v30,v32,v33
v_cvt_pk_bf16_f32 v31,v34,v35
v_mfma_f32_16x16x32_bf16 v[24:27],a[28:31],v[48:51],v[24:27]
s_waitcnt lgkmcnt(2)
v_add_f32 v36,v36,v60
v_add_f32 v37,v37,v61
v_mfma_f32_16x16x32_bf16 v[12:15],a[32:35],v[52:55],v[12:15]
buffer_store_dwordx4 v[28:31],v3,s[36:39],s73 offen offset:0 sc0 sc1
ds_read_b128 v[48:51],v2 offset:5120
v_add_f32 v38,v38,v62
v_add_f32 v39,v39,v63
v_mfma_f32_16x16x32_bf16 v[16:19],a[36:39],v[52:55],v[16:19]
s_cselect_b32 s53,s59,s53
v_add_f32 v40,v40,v64
v_add_f32 v41,v41,v65
v_mfma_f32_16x16x32_bf16 v[20:23],a[40:43],v[52:55],v[20:23]
v_add_f32 v42,v42,v66
v_add_f32 v43,v43,v67
v_mfma_f32_16x16x32_bf16 v[24:27],a[44:47],v[52:55],v[24:27]
s_waitcnt lgkmcnt(2)
v_cvt_pk_bf16_f32 v36,v36,v37
v_cvt_pk_bf16_f32 v37,v38,v39
v_mfma_f32_16x16x32_bf16 v[12:15],a[48:51],v[56:59],v[12:15]
ds_read_b128 v[52:55],v2 offset:6144
v_cvt_pk_bf16_f32 v38,v40,v41
v_mfma_f32_16x16x32_bf16 v[16:19],a[52:55],v[56:59],v[16:19]
v_cvt_pk_bf16_f32 v39,v42,v43
v_mfma_f32_16x16x32_bf16 v[20:23],a[56:59],v[56:59],v[20:23]
s_waitcnt vmcnt(1) lgkmcnt(2)
v_mfma_f32_16x16x32_bf16 v[24:27],a[60:63],v[56:59],v[24:27]
s_barrier
v_mfma_f32_16x16x32_bf16 v[12:15],a[64:67],v[44:47],v[12:15]
ds_read_b32 v11,v8 offset:4096
ds_read_b128 v[56:59],v2 offset:7168
v_mfma_f32_16x16x32_bf16 v[16:19],a[68:71],v[44:47],v[16:19]
v_mfma_f32_16x16x32_bf16 v[20:23],a[72:75],v[44:47],v[20:23]
v_mfma_f32_16x16x32_bf16 v[24:27],a[76:79],v[44:47],v[24:27]
s_waitcnt lgkmcnt(3)
v_mfma_f32_16x16x32_bf16 v[12:15],a[80:83],v[48:51],v[12:15]
s_waitcnt lgkmcnt(1)
buffer_store_dwordx4 v[36:39],v4,s[36:39],s73 offen offset:0 sc0 sc1
v_cmp_eq_u32 vcc,s60,v11
v_mfma_f32_16x16x32_bf16 v[16:19],a[84:87],v[48:51],v[16:19]
s_cmp_eq_u32 vcc_lo,-1
ds_read_b128 v[44:47],v2 offset:8192
s_cselect_b32 s66,s49,s59
v_mfma_f32_16x16x32_bf16 v[20:23],a[88:91],v[48:51],v[20:23]
s_mul_i32 s61,s66,7
s_lshr_b32 s61,s61,1
s_add_u32 m0,s63,28672
v_mfma_f32_16x16x32_bf16 v[24:27],a[92:95],v[48:51],v[24:27]
s_cmp_ge_u32 s66,s59
s_cselect_b32 s68,s49,s59
v_mfma_f32_16x16x32_bf16 v[12:15],a[96:99],v[52:55],v[12:15]
buffer_load_dwordx4 v0,s[36:39],s61 offen offset:0 sc1 lds
v_mfma_f32_16x16x32_bf16 v[16:19],a[100:103],v[52:55],v[16:19]
s_mul_i32 s68,s68,5
ds_read_b128 v[48:51],v2 offset:9216
s_lshr_b32 s68,s68,6
v_mfma_f32_16x16x32_bf16 v[20:23],a[104:107],v[52:55],v[20:23]
v_mfma_f32_16x16x32_bf16 v[24:27],a[108:111],v[52:55],v[24:27]
s_waitcnt lgkmcnt(2)
v_mfma_f32_16x16x32_bf16 v[12:15],a[112:115],v[56:59],v[12:15]
buffer_load_dwordx4 v0,s[36:39],s61 offen offset:1024 sc1 lds
v_mfma_f32_16x16x32_bf16 v[16:19],a[116:119],v[56:59],v[16:19]
ds_read_b128 v[52:55],v2 offset:10240
v_mfma_f32_16x16x32_bf16 v[20:23],a[120:123],v[56:59],v[20:23]
v_mfma_f32_16x16x32_bf16 v[24:27],a[124:127],v[56:59],v[24:27]
s_waitcnt lgkmcnt(2)
v_mfma_f32_16x16x32_bf16 v[12:15],a[128:131],v[44:47],v[12:15]
buffer_load_dwordx4 v0,s[36:39],s61 offen offset:2048 sc1 lds
v_mfma_f32_16x16x32_bf16 v[16:19],a[132:135],v[44:47],v[16:19]
ds_read_b128 v[56:59],v2 offset:11264
v_mfma_f32_16x16x32_bf16 v[20:23],a[136:139],v[44:47],v[20:23]
v_mfma_f32_16x16x32_bf16 v[24:27],a[140:143],v[44:47],v[24:27]
s_waitcnt lgkmcnt(2)
v_mfma_f32_16x16x32_bf16 v[12:15],a[144:147],v[48:51],v[12:15]
buffer_load_dwordx4 v0,s[36:39],s61 offen offset:3072 sc1 lds
v_mfma_f32_16x16x32_bf16 v[16:19],a[148:151],v[48:51],v[16:19])"
R"(
s_add_u32 m0,s78,6144
ds_read_b128 v[44:47],v2 offset:12288
v_mfma_f32_16x16x32_bf16 v[20:23],a[152:155],v[48:51],v[20:23]
v_mfma_f32_16x16x32_bf16 v[24:27],a[156:159],v[48:51],v[24:27]
s_waitcnt lgkmcnt(2)
v_mfma_f32_16x16x32_bf16 v[12:15],a[160:163],v[52:55],v[12:15]
buffer_load_dwordx4 v[68:71],v9,s[36:39],s48 offen offset:0 sc1
v_mfma_f32_16x16x32_bf16 v[16:19],a[164:167],v[52:55],v[16:19]
ds_read_b128 v[48:51],v2 offset:13312
v_mfma_f32_16x16x32_bf16 v[20:23],a[168:171],v[52:55],v[20:23]
v_mfma_f32_16x16x32_bf16 v[24:27],a[172:175],v[52:55],v[24:27]
s_waitcnt lgkmcnt(2)
v_mfma_f32_16x16x32_bf16 v[12:15],a[176:179],v[56:59],v[12:15]
ds_read_b128 v[52:55],v2 offset:14336
v_mfma_f32_16x16x32_bf16 v[16:19],a[180:183],v[56:59],v[16:19]
buffer_load_dword v7,s[36:39],s68 offen offset:0 sc1 lds
v_mfma_f32_16x16x32_bf16 v[20:23],a[184:187],v[56:59],v[20:23]
s_add_u32 m0,s64,28672
s_mul_i32 s68,s50,5
s_lshr_b32 s68,s68,6
v_mfma_f32_16x16x32_bf16 v[24:27],a[188:191],v[56:59],v[24:27]
s_waitcnt lgkmcnt(2)
v_mfma_f32_16x16x32_bf16 v[12:15],a[192:195],v[44:47],v[12:15]
ds_read_b128 v[56:59],v2 offset:15360
v_mfma_f32_16x16x32_bf16 v[16:19],a[196:199],v[44:47],v[16:19]
v_mfma_f32_16x16x32_bf16 v[20:23],a[200:203],v[44:47],v[20:23]
s_waitcnt vmcnt(9) lgkmcnt(2)
v_mfma_f32_16x16x32_bf16 v[24:27],a[204:207],v[44:47],v[24:27]
s_barrier
v_mfma_f32_16x16x32_bf16 v[12:15],a[208:211],v[48:51],v[12:15]
ds_read_b128 v[44:47],v2 offset:16384
v_mfma_f32_16x16x32_bf16 v[16:19],a[212:215],v[48:51],v[16:19]
buffer_load_dwordx4 v1,s[36:39],s61 offen offset:0 sc1 lds
v_mfma_f32_16x16x32_bf16 v[20:23],a[216:219],v[48:51],v[20:23]
v_mfma_f32_16x16x32_bf16 v[24:27],a[220:223],v[48:51],v[24:27]
s_waitcnt lgkmcnt(2)
v_mfma_f32_16x16x32_bf16 v[12:15],a[224:227],v[52:55],v[12:15]
ds_read_b128 v[48:51],v2 offset:17408
v_mfma_f32_16x16x32_bf16 v[16:19],a[228:231],v[52:55],v[16:19]
v_mfma_f32_16x16x32_bf16 v[20:23],a[232:235],v[52:55],v[20:23]
v_mfma_f32_16x16x32_bf16 v[24:27],a[236:239],v[52:55],v[24:27]
s_waitcnt lgkmcnt(2)
buffer_load_dwordx4 v1,s[36:39],s61 offen offset:1024 sc1 lds
v_mfma_f32_16x16x32_bf16 v[12:15],a[240:243],v[56:59],v[12:15]
ds_read_b128 v[52:55],v2 offset:18432
v_mfma_f32_16x16x32_bf16 v[16:19],a[244:247],v[56:59],v[16:19]
v_mfma_f32_16x16x32_bf16 v[20:23],a[248:251],v[56:59],v[20:23]
v_mfma_f32_16x16x32_bf16 v[24:27],a[252:255],v[56:59],v[24:27]
s_waitcnt lgkmcnt(2)
v_mfma_f32_16x16x32_bf16 v[12:15],v[100:103],v[44:47],v[12:15]
ds_read_b128 v[56:59],v2 offset:19456
v_mfma_f32_16x16x32_bf16 v[16:19],v[104:107],v[44:47],v[16:19]
buffer_load_dwordx4 v1,s[36:39],s61 offen offset:2048 sc1 lds
v_mfma_f32_16x16x32_bf16 v[20:23],v[108:111],v[44:47],v[20:23]
s_add_u32 m0,s78,5120
ds_read_b128 v[88:91],v10 offset:0
s_cmp_ge_u32 s66,s59
v_mfma_f32_16x16x32_bf16 v[24:27],v[112:115],v[44:47],v[24:27]
s_waitcnt lgkmcnt(3)
v_mfma_f32_16x16x32_bf16 v[12:15],v[116:119],v[48:51],v[12:15]
ds_read_b128 v[44:47],v2 offset:20480
v_mfma_f32_16x16x32_bf16 v[16:19],v[120:123],v[48:51],v[16:19]
ds_read_b128 v[76:79],v10 offset:1024
v_mfma_f32_16x16x32_bf16 v[20:23],v[124:127],v[48:51],v[20:23]
v_mfma_f32_16x16x32_bf16 v[24:27],v[128:131],v[48:51],v[24:27]
s_waitcnt lgkmcnt(4)
v_mfma_f32_16x16x32_bf16 v[12:15],v[132:135],v[52:55],v[12:15]
buffer_load_dword v7,s[36:39],s68 offen offset:0 sc1 lds
v_mfma_f32_16x16x32_bf16 v[16:19],v[136:139],v[52:55],v[16:19]
ds_read_b128 v[48:51],v2 offset:21504
ds_read_b128 v[80:83],v10 offset:2048
v_mfma_f32_16x16x32_bf16 v[20:23],v[140:143],v[52:55],v[20:23]
v_mfma_f32_16x16x32_bf16 v[24:27],v[144:147],v[52:55],v[24:27]
s_waitcnt lgkmcnt(4)
v_mfma_f32_16x16x32_bf16 v[12:15],v[148:151],v[56:59],v[12:15]
ds_read_b128 v[52:55],v2 offset:22528
v_mfma_f32_16x16x32_bf16 v[16:19],v[152:155],v[56:59],v[16:19]
ds_read_b128 v[84:87],v10 offset:3072
v_mfma_f32_16x16x32_bf16 v[20:23],v[156:159],v[56:59],v[20:23]
v_mfma_f32_16x16x32_bf16 v[24:27],v[88:91],v[56:59],v[24:27]
s_waitcnt lgkmcnt(4)
v_mfma_f32_16x16x32_bf16 v[12:15],v[160:163],v[44:47],v[12:15]
ds_read_b128 v[56:59],v2 offset:23552
v_mfma_f32_16x16x32_bf16 v[16:19],v[164:167],v[44:47],v[16:19]
ds_read_b128 v[88:91],v10 offset:4096
v_mfma_f32_16x16x32_bf16 v[20:23],v[168:171],v[44:47],v[20:23]
v_mfma_f32_16x16x32_bf16 v[24:27],v[76:79],v[44:47],v[24:27]
s_waitcnt lgkmcnt(4)
v_mfma_f32_16x16x32_bf16 v[12:15],v[172:175],v[48:51],v[12:15]
ds_read_b128 v[44:47],v2 offset:24576
v_mfma_f32_16x16x32_bf16 v[16:19],v[176:179],v[48:51],v[16:19]
ds_read_b128 v[76:79],v10 offset:5120
v_mfma_f32_16x16x32_bf16 v[20:23],v[180:183],v[48:51],v[20:23]
s_waitcnt vmcnt(10) lgkmcnt(4)
v_mfma_f32_16x16x32_bf16 v[24:27],v[80:83],v[48:51],v[24:27]
s_barrier
v_mfma_f32_16x16x32_bf16 v[12:15],v[184:187],v[52:55],v[12:15]
ds_read_b128 v[48:51],v2 offset:25600
v_mfma_f32_16x16x32_bf16 v[16:19],v[188:191],v[52:55],v[16:19]
ds_read_b128 v[80:83],v10 offset:6144
v_mfma_f32_16x16x32_bf16 v[20:23],v[192:195],v[52:55],v[20:23]
v_mfma_f32_16x16x32_bf16 v[24:27],v[84:87],v[52:55],v[24:27]
s_waitcnt lgkmcnt(4)
v_mfma_f32_16x16x32_bf16 v[12:15],v[196:199],v[56:59],v[12:15]
ds_read_b128 v[52:55],v2 offset:26624
v_mfma_f32_16x16x32_bf16 v[16:19],v[200:203],v[56:59],v[16:19]
ds_read_b128 v[84:87],v10 offset:7168
v_mfma_f32_16x16x32_bf16 v[20:23],v[204:207],v[56:59],v[20:23]
v_mfma_f32_16x16x32_bf16 v[24:27],v[88:91],v[56:59],v[24:27]
s_waitcnt lgkmcnt(4)
v_mfma_f32_16x16x32_bf16 v[12:15],v[208:211],v[44:47],v[12:15]
ds_read_b128 v[56:59],v2 offset:27648
v_mfma_f32_16x16x32_bf16 v[16:19],v[212:215],v[44:47],v[16:19]
ds_read_b128 v[88:91],v10 offset:8192
v_mfma_f32_16x16x32_bf16 v[20:23],v[216:219],v[44:47],v[20:23]
s_waitcnt vmcnt(4) lgkmcnt(4)
v_mfma_f32_16x16x32_bf16 v[24:27],v[76:79],v[44:47],v[24:27]
s_barrier
v_mfma_f32_16x16x32_bf16 v[12:15],v[220:223],v[48:51],v[12:15]
ds_read_b128 v[44:47],v2 offset:28672
v_mfma_f32_16x16x32_bf16 v[16:19],v[224:227],v[48:51],v[16:19]
v_mfma_f32_16x16x32_bf16 v[20:23],v[228:231],v[48:51],v[20:23]
v_mfma_f32_16x16x32_bf16 v[24:27],v[80:83],v[48:51],v[24:27]
s_waitcnt lgkmcnt(3)
v_mfma_f32_16x16x32_bf16 v[12:15],v[232:235],v[52:55],v[12:15]
ds_read_b128 v[48:51],v2 offset:29696
v_mfma_f32_16x16x32_bf16 v[16:19],v[236:239],v[52:55],v[16:19]
v_mfma_f32_16x16x32_bf16 v[20:23],v[240:243],v[52:55],v[20:23]
v_mfma_f32_16x16x32_bf16 v[24:27],v[84:87],v[52:55],v[24:27]
s_waitcnt lgkmcnt(2)
v_mfma_f32_16x16x32_bf16 v[12:15],v[244:247],v[56:59],v[12:15]
ds_read_b128 v[52:55],v2 offset:30720
v_mfma_f32_16x16x32_bf16 v[16:19],v[248:251],v[56:59],v[16:19]
v_mfma_f32_16x16x32_bf16 v[20:23],v[252:255],v[56:59],v[20:23]
s_waitcnt lgkmcnt(2)
v_mfma_f32_16x16x32_bf16 v[24:27],v[88:91],v[56:59],v[24:27]
s_cbranch_scc1 Lg4_slow1_%=
Lg4_d1w0_%=:
v_mfma_f32_16x16x32_bf16 v[28:31],a[0:3],v[44:47],0
s_max_u32 s73,s48,s65
ds_read_b128 v[56:59],v2 offset:31744
v_lshlrev_b32 v60,16,v68
v_mfma_f32_16x16x32_bf16 v[32:35],a[4:7],v[44:47],0
s_mov_b32 s65,0
v_and_b32 v61,0xffff0000,v68
v_lshlrev_b32 v62,16,v69
v_mfma_f32_16x16x32_bf16 v[36:39],a[8:11],v[44:47],0
s_add_u32 s74,s53,s55
v_and_b32 v63,0xffff0000,v69
v_lshlrev_b32 v64,16,v70
v_mfma_f32_16x16x32_bf16 v[40:43],a[12:15],v[44:47],0
s_waitcnt lgkmcnt(2)
v_and_b32 v65,0xffff0000,v70
v_lshlrev_b32 v66,16,v71
v_mfma_f32_16x16x32_bf16 v[28:31],a[16:19],v[48:51],v[28:31]
s_cmp_eq_u32 s74,s56
ds_read_b128 v[44:47],v2 offset:32768
v_and_b32 v67,0xffff0000,v71
v_mfma_f32_16x16x32_bf16 v[32:35],a[20:23],v[48:51],v[32:35]
s_cselect_b32 s74,s57,s74
v_cvt_pk_bf16_f32 v12,v12,v13
v_cvt_pk_bf16_f32 v13,v14,v15
v_mfma_f32_16x16x32_bf16 v[36:39],a[24:27],v[48:51],v[36:39]
s_cmp_eq_u32 s74,s58
v_cvt_pk_bf16_f32 v14,v16,v17
v_cvt_pk_bf16_f32 v15,v18,v19
v_mfma_f32_16x16x32_bf16 v[40:43],a[28:31],v[48:51],v[40:43]
s_waitcnt lgkmcnt(2)
v_add_f32 v20,v20,v60
v_add_f32 v21,v21,v61
v_mfma_f32_16x16x32_bf16 v[28:31],a[32:35],v[52:55],v[28:31]
buffer_store_dwordx4 v[12:15],v3,s[36:39],s73 offen offset:0 sc0 sc1
ds_read_b128 v[48:51],v2 offset:33792
v_add_f32 v22,v22,v62
v_add_f32 v23,v23,v63
v_mfma_f32_16x16x32_bf16 v[32:35],a[36:39],v[52:55],v[32:35]
s_cselect_b32 s74,s59,s74
v_add_f32 v24,v24,v64
v_add_f32 v25,v25,v65
v_mfma_f32_16x16x32_bf16 v[36:39],a[40:43],v[52:55],v[36:39]
s_mov_b64 s[44:45],s[46:47]
v_add_f32 v26,v26,v66
v_add_f32 v27,v27,v67
v_mfma_f32_16x16x32_bf16 v[40:43],a[44:47],v[52:55],v[40:43]
s_waitcnt lgkmcnt(2)
v_cvt_pk_bf16_f32 v20,v20,v21
v_cvt_pk_bf16_f32 v21,v22,v23
v_mfma_f32_16x16x32_bf16 v[28:31],a[48:51],v[56:59],v[28:31]
s_mov_b64 s[46:47],s[48:49]
ds_read_b128 v[52:55],v2 offset:34816
v_cvt_pk_bf16_f32 v22,v24,v25
v_mfma_f32_16x16x32_bf16 v[32:35],a[52:55],v[56:59],v[32:35]
v_cvt_pk_bf16_f32 v23,v26,v27
v_mfma_f32_16x16x32_bf16 v[36:39],a[56:59],v[56:59],v[36:39]
s_waitcnt vmcnt(1) lgkmcnt(2)
v_mfma_f32_16x16x32_bf16 v[40:43],a[60:63],v[56:59],v[40:43]
s_barrier
v_mfma_f32_16x16x32_bf16 v[28:31],a[64:67],v[44:47],v[28:31]
ds_read_b32 v11,v8 offset:5120
ds_read_b128 v[56:59],v2 offset:35840)"
R"(
v_mfma_f32_16x16x32_bf16 v[32:35],a[68:71],v[44:47],v[32:35]
v_mfma_f32_16x16x32_bf16 v[36:39],a[72:75],v[44:47],v[36:39]
v_mfma_f32_16x16x32_bf16 v[40:43],a[76:79],v[44:47],v[40:43]
s_waitcnt lgkmcnt(3)
v_mfma_f32_16x16x32_bf16 v[28:31],a[80:83],v[48:51],v[28:31]
s_waitcnt lgkmcnt(1)
buffer_store_dwordx4 v[20:23],v4,s[36:39],s73 offen offset:0 sc0 sc1
v_cmp_eq_u32 vcc,s60,v11
v_mfma_f32_16x16x32_bf16 v[32:35],a[84:87],v[48:51],v[32:35]
s_cmp_eq_u32 vcc_lo,-1
ds_read_b128 v[44:47],v2 offset:36864
s_cselect_b32 s67,s50,s59
v_mfma_f32_16x16x32_bf16 v[36:39],a[88:91],v[48:51],v[36:39]
s_mul_i32 s61,s67,7
s_lshr_b32 s61,s61,1
s_add_u32 m0,s63,0
v_mfma_f32_16x16x32_bf16 v[40:43],a[92:95],v[48:51],v[40:43]
s_cmp_ge_u32 s67,s59
s_cselect_b32 s68,s50,s59
v_mfma_f32_16x16x32_bf16 v[28:31],a[96:99],v[52:55],v[28:31]
buffer_load_dwordx4 v0,s[36:39],s61 offen offset:0 sc1 lds
v_mfma_f32_16x16x32_bf16 v[32:35],a[100:103],v[52:55],v[32:35]
s_mul_i32 s68,s68,5
ds_read_b128 v[48:51],v2 offset:37888
s_lshr_b32 s68,s68,6
v_mfma_f32_16x16x32_bf16 v[36:39],a[104:107],v[52:55],v[36:39]
v_mfma_f32_16x16x32_bf16 v[40:43],a[108:111],v[52:55],v[40:43]
s_waitcnt lgkmcnt(2)
v_mfma_f32_16x16x32_bf16 v[28:31],a[112:115],v[56:59],v[28:31]
buffer_load_dwordx4 v0,s[36:39],s61 offen offset:1024 sc1 lds
v_mfma_f32_16x16x32_bf16 v[32:35],a[116:119],v[56:59],v[32:35]
ds_read_b128 v[52:55],v2 offset:38912
v_mfma_f32_16x16x32_bf16 v[36:39],a[120:123],v[56:59],v[36:39]
v_mfma_f32_16x16x32_bf16 v[40:43],a[124:127],v[56:59],v[40:43]
s_waitcnt lgkmcnt(2)
v_mfma_f32_16x16x32_bf16 v[28:31],a[128:131],v[44:47],v[28:31]
buffer_load_dwordx4 v0,s[36:39],s61 offen offset:2048 sc1 lds
v_mfma_f32_16x16x32_bf16 v[32:35],a[132:135],v[44:47],v[32:35]
ds_read_b128 v[56:59],v2 offset:39936
v_mfma_f32_16x16x32_bf16 v[36:39],a[136:139],v[44:47],v[36:39]
v_mfma_f32_16x16x32_bf16 v[40:43],a[140:143],v[44:47],v[40:43]
s_waitcnt lgkmcnt(2)
v_mfma_f32_16x16x32_bf16 v[28:31],a[144:147],v[48:51],v[28:31]
buffer_load_dwordx4 v0,s[36:39],s61 offen offset:3072 sc1 lds
v_mfma_f32_16x16x32_bf16 v[32:35],a[148:151],v[48:51],v[32:35]
s_add_u32 m0,s78,6144
ds_read_b128 v[44:47],v2 offset:40960
v_mfma_f32_16x16x32_bf16 v[36:39],a[152:155],v[48:51],v[36:39]
v_mfma_f32_16x16x32_bf16 v[40:43],a[156:159],v[48:51],v[40:43]
s_waitcnt lgkmcnt(2)
v_mfma_f32_16x16x32_bf16 v[28:31],a[160:163],v[52:55],v[28:31]
buffer_load_dwordx4 v[68:71],v9,s[36:39],s49 offen offset:0 sc1
v_mfma_f32_16x16x32_bf16 v[32:35],a[164:167],v[52:55],v[32:35]
s_mov_b64 s[48:49],s[50:51]
ds_read_b128 v[48:51],v2 offset:41984
v_mfma_f32_16x16x32_bf16 v[36:39],a[168:171],v[52:55],v[36:39]
v_mfma_f32_16x16x32_bf16 v[40:43],a[172:175],v[52:55],v[40:43]
s_waitcnt lgkmcnt(2)
v_mfma_f32_16x16x32_bf16 v[28:31],a[176:179],v[56:59],v[28:31]
ds_read_b128 v[52:55],v2 offset:43008
v_mfma_f32_16x16x32_bf16 v[32:35],a[180:183],v[56:59],v[32:35]
buffer_load_dword v7,s[36:39],s68 offen offset:0 sc1 lds
v_mfma_f32_16x16x32_bf16 v[36:39],a[184:187],v[56:59],v[36:39]
s_add_u32 m0,s64,0
s_mul_i32 s68,s51,5
s_lshr_b32 s68,s68,6
v_mfma_f32_16x16x32_bf16 v[40:43],a[188:191],v[56:59],v[40:43]
s_waitcnt lgkmcnt(2)
s_mov_b64 s[50:51],s[52:53]
s_mov_b32 s52,s74
v_mfma_f32_16x16x32_bf16 v[28:31],a[192:195],v[44:47],v[28:31]
ds_read_b128 v[56:59],v2 offset:44032
v_mfma_f32_16x16x32_bf16 v[32:35],a[196:199],v[44:47],v[32:35]
v_mfma_f32_16x16x32_bf16 v[36:39],a[200:203],v[44:47],v[36:39]
s_waitcnt vmcnt(9) lgkmcnt(2)
v_mfma_f32_16x16x32_bf16 v[40:43],a[204:207],v[44:47],v[40:43]
s_barrier
v_mfma_f32_16x16x32_bf16 v[28:31],a[208:211],v[48:51],v[28:31]
ds_read_b128 v[44:47],v2 offset:45056
v_mfma_f32_16x16x32_bf16 v[32:35],a[212:215],v[48:51],v[32:35]
buffer_load_dwordx4 v1,s[36:39],s61 offen offset:0 sc1 lds
v_mfma_f32_16x16x32_bf16 v[36:39],a[216:219],v[48:51],v[36:39]
v_mfma_f32_16x16x32_bf16 v[40:43],a[220:223],v[48:51],v[40:43]
s_waitcnt lgkmcnt(2)
v_mfma_f32_16x16x32_bf16 v[28:31],a[224:227],v[52:55],v[28:31]
ds_read_b128 v[48:51],v2 offset:46080
v_mfma_f32_16x16x32_bf16 v[32:35],a[228:231],v[52:55],v[32:35]
v_mfma_f32_16x16x32_bf16 v[36:39],a[232:235],v[52:55],v[36:39]
v_mfma_f32_16x16x32_bf16 v[40:43],a[236:239],v[52:55],v[40:43]
s_waitcnt lgkmcnt(2)
buffer_load_dwordx4 v1,s[36:39],s61 offen offset:1024 sc1 lds
v_mfma_f32_16x16x32_bf16 v[28:31],a[240:243],v[56:59],v[28:31]
ds_read_b128 v[52:55],v2 offset:47104
v_mfma_f32_16x16x32_bf16 v[32:35],a[244:247],v[56:59],v[32:35]
v_mfma_f32_16x16x32_bf16 v[36:39],a[248:251],v[56:59],v[36:39]
v_mfma_f32_16x16x32_bf16 v[40:43],a[252:255],v[56:59],v[40:43]
s_waitcnt lgkmcnt(2)
v_mfma_f32_16x16x32_bf16 v[28:31],v[100:103],v[44:47],v[28:31]
ds_read_b128 v[56:59],v2 offset:48128
v_mfma_f32_16x16x32_bf16 v[32:35],v[104:107],v[44:47],v[32:35]
buffer_load_dwordx4 v1,s[36:39],s61 offen offset:2048 sc1 lds
v_mfma_f32_16x16x32_bf16 v[36:39],v[108:111],v[44:47],v[36:39]
s_add_u32 m0,s78,4096
ds_read_b128 v[88:91],v10 offset:0
s_cmp_lt_u32 s67,s59
v_mfma_f32_16x16x32_bf16 v[40:43],v[112:115],v[44:47],v[40:43]
s_waitcnt lgkmcnt(3)
v_mfma_f32_16x16x32_bf16 v[28:31],v[116:119],v[48:51],v[28:31]
ds_read_b128 v[44:47],v2 offset:49152
v_mfma_f32_16x16x32_bf16 v[32:35],v[120:123],v[48:51],v[32:35]
ds_read_b128 v[76:79],v10 offset:1024
v_mfma_f32_16x16x32_bf16 v[36:39],v[124:127],v[48:51],v[36:39]
v_mfma_f32_16x16x32_bf16 v[40:43],v[128:131],v[48:51],v[40:43]
s_waitcnt lgkmcnt(4)
v_mfma_f32_16x16x32_bf16 v[28:31],v[132:135],v[52:55],v[28:31]
buffer_load_dword v7,s[36:39],s68 offen offset:0 sc1 lds
v_mfma_f32_16x16x32_bf16 v[32:35],v[136:139],v[52:55],v[32:35]
ds_read_b128 v[48:51],v2 offset:50176
ds_read_b128 v[80:83],v10 offset:2048
v_mfma_f32_16x16x32_bf16 v[36:39],v[140:143],v[52:55],v[36:39]
v_mfma_f32_16x16x32_bf16 v[40:43],v[144:147],v[52:55],v[40:43]
s_waitcnt lgkmcnt(4)
v_mfma_f32_16x16x32_bf16 v[28:31],v[148:151],v[56:59],v[28:31]
ds_read_b128 v[52:55],v2 offset:51200
v_mfma_f32_16x16x32_bf16 v[32:35],v[152:155],v[56:59],v[32:35]
ds_read_b128 v[84:87],v10 offset:3072
v_mfma_f32_16x16x32_bf16 v[36:39],v[156:159],v[56:59],v[36:39]
v_mfma_f32_16x16x32_bf16 v[40:43],v[88:91],v[56:59],v[40:43]
s_waitcnt lgkmcnt(4)
v_mfma_f32_16x16x32_bf16 v[28:31],v[160:163],v[44:47],v[28:31]
ds_read_b128 v[56:59],v2 offset:52224
v_mfma_f32_16x16x32_bf16 v[32:35],v[164:167],v[44:47],v[32:35]
ds_read_b128 v[88:91],v10 offset:4096
v_mfma_f32_16x16x32_bf16 v[36:39],v[168:171],v[44:47],v[36:39]
v_mfma_f32_16x16x32_bf16 v[40:43],v[76:79],v[44:47],v[40:43]
s_waitcnt lgkmcnt(4)
v_mfma_f32_16x16x32_bf16 v[28:31],v[172:175],v[48:51],v[28:31]
ds_read_b128 v[44:47],v2 offset:53248
v_mfma_f32_16x16x32_bf16 v[32:35],v[176:179],v[48:51],v[32:35]
ds_read_b128 v[76:79],v10 offset:5120
v_mfma_f32_16x16x32_bf16 v[36:39],v[180:183],v[48:51],v[36:39]
s_waitcnt vmcnt(10) lgkmcnt(4)
v_mfma_f32_16x16x32_bf16 v[40:43],v[80:83],v[48:51],v[40:43]
s_barrier
v_mfma_f32_16x16x32_bf16 v[28:31],v[184:187],v[52:55],v[28:31]
ds_read_b128 v[48:51],v2 offset:54272
v_mfma_f32_16x16x32_bf16 v[32:35],v[188:191],v[52:55],v[32:35]
ds_read_b128 v[80:83],v10 offset:6144
v_mfma_f32_16x16x32_bf16 v[36:39],v[192:195],v[52:55],v[36:39]
v_mfma_f32_16x16x32_bf16 v[40:43],v[84:87],v[52:55],v[40:43]
s_waitcnt lgkmcnt(4)
v_mfma_f32_16x16x32_bf16 v[28:31],v[196:199],v[56:59],v[28:31]
ds_read_b128 v[52:55],v2 offset:55296
v_mfma_f32_16x16x32_bf16 v[32:35],v[200:203],v[56:59],v[32:35]
ds_read_b128 v[84:87],v10 offset:7168
v_mfma_f32_16x16x32_bf16 v[36:39],v[204:207],v[56:59],v[36:39]
v_mfma_f32_16x16x32_bf16 v[40:43],v[88:91],v[56:59],v[40:43]
s_waitcnt lgkmcnt(4)
v_mfma_f32_16x16x32_bf16 v[28:31],v[208:211],v[44:47],v[28:31]
ds_read_b128 v[56:59],v2 offset:56320
v_mfma_f32_16x16x32_bf16 v[32:35],v[212:215],v[44:47],v[32:35]
ds_read_b128 v[88:91],v10 offset:8192
v_mfma_f32_16x16x32_bf16 v[36:39],v[216:219],v[44:47],v[36:39]
s_waitcnt vmcnt(4) lgkmcnt(4)
v_mfma_f32_16x16x32_bf16 v[40:43],v[76:79],v[44:47],v[40:43]
s_barrier
v_mfma_f32_16x16x32_bf16 v[28:31],v[220:223],v[48:51],v[28:31]
ds_read_b128 v[44:47],v2 offset:0
v_mfma_f32_16x16x32_bf16 v[32:35],v[224:227],v[48:51],v[32:35]
v_mfma_f32_16x16x32_bf16 v[36:39],v[228:231],v[48:51],v[36:39]
v_mfma_f32_16x16x32_bf16 v[40:43],v[80:83],v[48:51],v[40:43]
s_waitcnt lgkmcnt(3)
v_mfma_f32_16x16x32_bf16 v[28:31],v[232:235],v[52:55],v[28:31]
ds_read_b128 v[48:51],v2 offset:1024
v_mfma_f32_16x16x32_bf16 v[32:35],v[236:239],v[52:55],v[32:35]
v_mfma_f32_16x16x32_bf16 v[36:39],v[240:243],v[52:55],v[36:39]
v_mfma_f32_16x16x32_bf16 v[40:43],v[84:87],v[52:55],v[40:43]
s_waitcnt lgkmcnt(2)
v_mfma_f32_16x16x32_bf16 v[28:31],v[244:247],v[56:59],v[28:31]
ds_read_b128 v[52:55],v2 offset:2048
v_mfma_f32_16x16x32_bf16 v[32:35],v[248:251],v[56:59],v[32:35]
v_mfma_f32_16x16x32_bf16 v[36:39],v[252:255],v[56:59],v[36:39]
s_waitcnt lgkmcnt(2)
v_mfma_f32_16x16x32_bf16 v[40:43],v[88:91],v[56:59],v[40:43]
s_cbranch_scc1 Lg4_d0w0_%=
s_branch Lg4_slow0_%=
Lg4_d0w1_%=:
v_mfma_f32_16x16x32_bf16 v[12:15],a[0:3],v[44:47],0
s_max_u32 s73,s47,s65
ds_read_b128 v[56:59],v2 offset:3072
v_lshlrev_b32 v60,16,v68
v_mfma_f32_16x16x32_bf16 v[16:19],a[4:7],v[44:47],0
s_mov_b32 s65,0
v_and_b32 v61,0xffff0000,v68
v_lshlrev_b32 v62,16,v69
v_mfma_f32_16x16x32_bf16 v[20:23],a[8:11],v[44:47],0
s_add_u32 s53,s52,s55
v_and_b32 v63,0xffff0000,v69
v_lshlrev_b32 v64,16,v70
v_mfma_f32_16x16x32_bf16 v[24:27],a[12:15],v[44:47],0)"
R"(
s_waitcnt lgkmcnt(2)
v_and_b32 v65,0xffff0000,v70
v_lshlrev_b32 v66,16,v71
v_mfma_f32_16x16x32_bf16 v[12:15],a[16:19],v[48:51],v[12:15]
s_cmp_eq_u32 s53,s56
ds_read_b128 v[44:47],v2 offset:4096
v_and_b32 v67,0xffff0000,v71
v_mfma_f32_16x16x32_bf16 v[16:19],a[20:23],v[48:51],v[16:19]
s_cselect_b32 s53,s57,s53
v_cvt_pk_bf16_f32 v28,v28,v29
v_cvt_pk_bf16_f32 v29,v30,v31
v_mfma_f32_16x16x32_bf16 v[20:23],a[24:27],v[48:51],v[20:23]
s_cmp_eq_u32 s53,s58
v_cvt_pk_bf16_f32 v30,v32,v33
v_cvt_pk_bf16_f32 v31,v34,v35
v_mfma_f32_16x16x32_bf16 v[24:27],a[28:31],v[48:51],v[24:27]
s_waitcnt lgkmcnt(2)
v_add_f32 v36,v36,v60
v_add_f32 v37,v37,v61
v_mfma_f32_16x16x32_bf16 v[12:15],a[32:35],v[52:55],v[12:15]
s_cselect_b32 s53,s59,s53
ds_read_b128 v[48:51],v2 offset:5120
v_add_f32 v38,v38,v62
v_mfma_f32_16x16x32_bf16 v[16:19],a[36:39],v[52:55],v[16:19]
buffer_store_dwordx4 v[28:31],v3,s[36:39],s73 offen offset:0 sc0 sc1
v_add_f32 v39,v39,v63
v_add_f32 v40,v40,v64
v_add_f32 v41,v41,v65
v_mfma_f32_16x16x32_bf16 v[20:23],a[40:43],v[52:55],v[20:23]
v_add_f32 v42,v42,v66
v_add_f32 v43,v43,v67
v_mfma_f32_16x16x32_bf16 v[24:27],a[44:47],v[52:55],v[24:27]
s_waitcnt lgkmcnt(2)
v_cvt_pk_bf16_f32 v36,v36,v37
v_cvt_pk_bf16_f32 v37,v38,v39
v_mfma_f32_16x16x32_bf16 v[12:15],a[48:51],v[56:59],v[12:15]
ds_read_b128 v[52:55],v2 offset:6144
v_cvt_pk_bf16_f32 v38,v40,v41
v_mfma_f32_16x16x32_bf16 v[16:19],a[52:55],v[56:59],v[16:19]
v_cvt_pk_bf16_f32 v39,v42,v43
v_mfma_f32_16x16x32_bf16 v[20:23],a[56:59],v[56:59],v[20:23]
s_waitcnt lgkmcnt(2)
s_nop 0
v_mfma_f32_16x16x32_bf16 v[24:27],a[60:63],v[56:59],v[24:27]
s_barrier
v_mfma_f32_16x16x32_bf16 v[12:15],a[64:67],v[44:47],v[12:15]
ds_read_b128 v[56:59],v2 offset:7168
ds_read_b32 v11,v8 offset:4096
v_mfma_f32_16x16x32_bf16 v[16:19],a[68:71],v[44:47],v[16:19]
v_mfma_f32_16x16x32_bf16 v[20:23],a[72:75],v[44:47],v[20:23]
v_mfma_f32_16x16x32_bf16 v[24:27],a[76:79],v[44:47],v[24:27]
s_waitcnt lgkmcnt(3)
v_mfma_f32_16x16x32_bf16 v[12:15],a[80:83],v[48:51],v[12:15]
s_waitcnt lgkmcnt(0)
ds_read_b128 v[44:47],v2 offset:8192
v_mfma_f32_16x16x32_bf16 v[16:19],a[84:87],v[48:51],v[16:19]
buffer_store_dwordx4 v[36:39],v4,s[36:39],s73 offen offset:0 sc0 sc1
v_cmp_eq_u32 vcc,s60,v11
v_mfma_f32_16x16x32_bf16 v[20:23],a[88:91],v[48:51],v[20:23]
s_cmp_eq_u32 vcc_lo,-1
s_cselect_b32 s66,s49,s59
s_mul_i32 s61,s66,7
v_mfma_f32_16x16x32_bf16 v[24:27],a[92:95],v[48:51],v[24:27]
s_lshr_b32 s61,s61,1
s_add_u32 m0,s63,28672
v_mfma_f32_16x16x32_bf16 v[12:15],a[96:99],v[52:55],v[12:15]
ds_read_b128 v[48:51],v2 offset:9216
v_mfma_f32_16x16x32_bf16 v[16:19],a[100:103],v[52:55],v[16:19]
buffer_load_dwordx4 v0,s[36:39],s61 offen offset:0 sc1 lds
v_mfma_f32_16x16x32_bf16 v[20:23],a[104:107],v[52:55],v[20:23]
v_mfma_f32_16x16x32_bf16 v[24:27],a[108:111],v[52:55],v[24:27]
v_mfma_f32_16x16x32_bf16 v[12:15],a[112:115],v[56:59],v[12:15]
ds_read_b128 v[52:55],v2 offset:10240
v_mfma_f32_16x16x32_bf16 v[16:19],a[116:119],v[56:59],v[16:19]
buffer_load_dwordx4 v0,s[36:39],s61 offen offset:1024 sc1 lds
v_mfma_f32_16x16x32_bf16 v[20:23],a[120:123],v[56:59],v[20:23]
v_mfma_f32_16x16x32_bf16 v[24:27],a[124:127],v[56:59],v[24:27]
s_waitcnt lgkmcnt(2)
v_mfma_f32_16x16x32_bf16 v[12:15],a[128:131],v[44:47],v[12:15]
ds_read_b128 v[56:59],v2 offset:11264
v_mfma_f32_16x16x32_bf16 v[16:19],a[132:135],v[44:47],v[16:19]
buffer_load_dwordx4 v0,s[36:39],s61 offen offset:2048 sc1 lds
v_mfma_f32_16x16x32_bf16 v[20:23],a[136:139],v[44:47],v[20:23]
v_mfma_f32_16x16x32_bf16 v[24:27],a[140:143],v[44:47],v[24:27]
s_waitcnt lgkmcnt(2)
v_mfma_f32_16x16x32_bf16 v[12:15],a[144:147],v[48:51],v[12:15]
ds_read_b128 v[44:47],v2 offset:12288
v_mfma_f32_16x16x32_bf16 v[16:19],a[148:151],v[48:51],v[16:19]
buffer_load_dwordx4 v0,s[36:39],s61 offen offset:3072 sc1 lds
v_mfma_f32_16x16x32_bf16 v[20:23],a[152:155],v[48:51],v[20:23]
s_add_u32 m0,s64,28672
s_cmp_ge_u32 s66,s59
v_mfma_f32_16x16x32_bf16 v[24:27],a[156:159],v[48:51],v[24:27]
s_waitcnt lgkmcnt(2)
v_mfma_f32_16x16x32_bf16 v[12:15],a[160:163],v[52:55],v[12:15]
ds_read_b128 v[48:51],v2 offset:13312
v_mfma_f32_16x16x32_bf16 v[16:19],a[164:167],v[52:55],v[16:19]
buffer_load_dwordx4 v[68:71],v9,s[36:39],s48 offen offset:0 sc1
v_mfma_f32_16x16x32_bf16 v[20:23],a[168:171],v[52:55],v[20:23]
v_mfma_f32_16x16x32_bf16 v[24:27],a[172:175],v[52:55],v[24:27]
s_waitcnt lgkmcnt(2)
v_mfma_f32_16x16x32_bf16 v[12:15],a[176:179],v[56:59],v[12:15]
ds_read_b128 v[52:55],v2 offset:14336
v_mfma_f32_16x16x32_bf16 v[16:19],a[180:183],v[56:59],v[16:19]
v_mfma_f32_16x16x32_bf16 v[20:23],a[184:187],v[56:59],v[20:23]
v_mfma_f32_16x16x32_bf16 v[24:27],a[188:191],v[56:59],v[24:27]
s_waitcnt lgkmcnt(2)
v_mfma_f32_16x16x32_bf16 v[12:15],a[192:195],v[44:47],v[12:15]
ds_read_b128 v[56:59],v2 offset:15360
v_mfma_f32_16x16x32_bf16 v[16:19],a[196:199],v[44:47],v[16:19]
v_mfma_f32_16x16x32_bf16 v[20:23],a[200:203],v[44:47],v[20:23]
s_waitcnt vmcnt(7) lgkmcnt(2)
v_mfma_f32_16x16x32_bf16 v[24:27],a[204:207],v[44:47],v[24:27]
s_barrier
v_mfma_f32_16x16x32_bf16 v[12:15],a[208:211],v[48:51],v[12:15]
ds_read_b128 v[44:47],v2 offset:16384
v_mfma_f32_16x16x32_bf16 v[16:19],a[212:215],v[48:51],v[16:19]
v_mfma_f32_16x16x32_bf16 v[20:23],a[216:219],v[48:51],v[20:23]
buffer_load_dwordx4 v1,s[36:39],s61 offen offset:0 sc1 lds
v_mfma_f32_16x16x32_bf16 v[24:27],a[220:223],v[48:51],v[24:27]
s_waitcnt lgkmcnt(2)
v_mfma_f32_16x16x32_bf16 v[12:15],a[224:227],v[52:55],v[12:15]
ds_read_b128 v[48:51],v2 offset:17408
v_mfma_f32_16x16x32_bf16 v[16:19],a[228:231],v[52:55],v[16:19]
v_mfma_f32_16x16x32_bf16 v[20:23],a[232:235],v[52:55],v[20:23]
v_mfma_f32_16x16x32_bf16 v[24:27],a[236:239],v[52:55],v[24:27]
s_waitcnt lgkmcnt(2)
v_mfma_f32_16x16x32_bf16 v[12:15],a[240:243],v[56:59],v[12:15]
buffer_load_dwordx4 v1,s[36:39],s61 offen offset:1024 sc1 lds
v_mfma_f32_16x16x32_bf16 v[16:19],a[244:247],v[56:59],v[16:19]
ds_read_b128 v[52:55],v2 offset:18432
v_mfma_f32_16x16x32_bf16 v[20:23],a[248:251],v[56:59],v[20:23]
v_mfma_f32_16x16x32_bf16 v[24:27],a[252:255],v[56:59],v[24:27]
s_waitcnt lgkmcnt(2)
v_mfma_f32_16x16x32_bf16 v[12:15],v[100:103],v[44:47],v[12:15]
ds_read_b128 v[56:59],v2 offset:19456
v_mfma_f32_16x16x32_bf16 v[16:19],v[104:107],v[44:47],v[16:19]
ds_read_b128 v[88:91],v10 offset:0
v_mfma_f32_16x16x32_bf16 v[20:23],v[108:111],v[44:47],v[20:23]
buffer_load_dwordx4 v1,s[36:39],s61 offen offset:2048 sc1 lds
v_mfma_f32_16x16x32_bf16 v[24:27],v[112:115],v[44:47],v[24:27]
s_waitcnt lgkmcnt(3)
v_mfma_f32_16x16x32_bf16 v[12:15],v[116:119],v[48:51],v[12:15]
ds_read_b128 v[44:47],v2 offset:20480
v_mfma_f32_16x16x32_bf16 v[16:19],v[120:123],v[48:51],v[16:19]
ds_read_b128 v[76:79],v10 offset:1024
v_mfma_f32_16x16x32_bf16 v[20:23],v[124:127],v[48:51],v[20:23]
v_mfma_f32_16x16x32_bf16 v[24:27],v[128:131],v[48:51],v[24:27]
s_waitcnt lgkmcnt(4)
v_mfma_f32_16x16x32_bf16 v[12:15],v[132:135],v[52:55],v[12:15]
ds_read_b128 v[48:51],v2 offset:21504
v_mfma_f32_16x16x32_bf16 v[16:19],v[136:139],v[52:55],v[16:19]
ds_read_b128 v[80:83],v10 offset:2048
v_mfma_f32_16x16x32_bf16 v[20:23],v[140:143],v[52:55],v[20:23]
v_mfma_f32_16x16x32_bf16 v[24:27],v[144:147],v[52:55],v[24:27]
s_waitcnt lgkmcnt(4)
v_mfma_f32_16x16x32_bf16 v[12:15],v[148:151],v[56:59],v[12:15]
ds_read_b128 v[52:55],v2 offset:22528
v_mfma_f32_16x16x32_bf16 v[16:19],v[152:155],v[56:59],v[16:19]
ds_read_b128 v[84:87],v10 offset:3072
v_mfma_f32_16x16x32_bf16 v[20:23],v[156:159],v[56:59],v[20:23]
v_mfma_f32_16x16x32_bf16 v[24:27],v[88:91],v[56:59],v[24:27]
s_waitcnt lgkmcnt(4)
v_mfma_f32_16x16x32_bf16 v[12:15],v[160:163],v[44:47],v[12:15]
ds_read_b128 v[56:59],v2 offset:23552
v_mfma_f32_16x16x32_bf16 v[16:19],v[164:167],v[44:47],v[16:19]
ds_read_b128 v[88:91],v10 offset:4096
v_mfma_f32_16x16x32_bf16 v[20:23],v[168:171],v[44:47],v[20:23]
v_mfma_f32_16x16x32_bf16 v[24:27],v[76:79],v[44:47],v[24:27]
s_waitcnt lgkmcnt(4)
v_mfma_f32_16x16x32_bf16 v[12:15],v[172:175],v[48:51],v[12:15]
ds_read_b128 v[44:47],v2 offset:24576
v_mfma_f32_16x16x32_bf16 v[16:19],v[176:179],v[48:51],v[16:19]
ds_read_b128 v[76:79],v10 offset:5120
v_mfma_f32_16x16x32_bf16 v[20:23],v[180:183],v[48:51],v[20:23]
s_waitcnt vmcnt(8) lgkmcnt(4)
v_mfma_f32_16x16x32_bf16 v[24:27],v[80:83],v[48:51],v[24:27]
s_barrier
v_mfma_f32_16x16x32_bf16 v[12:15],v[184:187],v[52:55],v[12:15]
ds_read_b128 v[48:51],v2 offset:25600
v_mfma_f32_16x16x32_bf16 v[16:19],v[188:191],v[52:55],v[16:19]
ds_read_b128 v[80:83],v10 offset:6144
v_mfma_f32_16x16x32_bf16 v[20:23],v[192:195],v[52:55],v[20:23]
v_mfma_f32_16x16x32_bf16 v[24:27],v[84:87],v[52:55],v[24:27]
s_waitcnt lgkmcnt(4)
v_mfma_f32_16x16x32_bf16 v[12:15],v[196:199],v[56:59],v[12:15]
ds_read_b128 v[52:55],v2 offset:26624
v_mfma_f32_16x16x32_bf16 v[16:19],v[200:203],v[56:59],v[16:19]
ds_read_b128 v[84:87],v10 offset:7168
v_mfma_f32_16x16x32_bf16 v[20:23],v[204:207],v[56:59],v[20:23]
v_mfma_f32_16x16x32_bf16 v[24:27],v[88:91],v[56:59],v[24:27]
s_waitcnt lgkmcnt(4)
v_mfma_f32_16x16x32_bf16 v[12:15],v[208:211],v[44:47],v[12:15]
ds_read_b128 v[56:59],v2 offset:27648
v_mfma_f32_16x16x32_bf16 v[16:19],v[212:215],v[44:47],v[16:19]
ds_read_b128 v[88:91],v10 offset:8192
v_mfma_f32_16x16x32_bf16 v[20:23],v[216:219],v[44:47],v[20:23]
s_waitcnt vmcnt(3) lgkmcnt(4)
v_mfma_f32_16x16x32_bf16 v[24:27],v[76:79],v[44:47],v[24:27]
s_barrier)"
R"(
v_mfma_f32_16x16x32_bf16 v[12:15],v[220:223],v[48:51],v[12:15]
ds_read_b128 v[44:47],v2 offset:28672
v_mfma_f32_16x16x32_bf16 v[16:19],v[224:227],v[48:51],v[16:19]
v_mfma_f32_16x16x32_bf16 v[20:23],v[228:231],v[48:51],v[20:23]
v_mfma_f32_16x16x32_bf16 v[24:27],v[80:83],v[48:51],v[24:27]
s_waitcnt lgkmcnt(3)
v_mfma_f32_16x16x32_bf16 v[12:15],v[232:235],v[52:55],v[12:15]
ds_read_b128 v[48:51],v2 offset:29696
v_mfma_f32_16x16x32_bf16 v[16:19],v[236:239],v[52:55],v[16:19]
v_mfma_f32_16x16x32_bf16 v[20:23],v[240:243],v[52:55],v[20:23]
v_mfma_f32_16x16x32_bf16 v[24:27],v[84:87],v[52:55],v[24:27]
s_waitcnt lgkmcnt(2)
v_mfma_f32_16x16x32_bf16 v[12:15],v[244:247],v[56:59],v[12:15]
ds_read_b128 v[52:55],v2 offset:30720
v_mfma_f32_16x16x32_bf16 v[16:19],v[248:251],v[56:59],v[16:19]
v_mfma_f32_16x16x32_bf16 v[20:23],v[252:255],v[56:59],v[20:23]
s_waitcnt lgkmcnt(2)
v_mfma_f32_16x16x32_bf16 v[24:27],v[88:91],v[56:59],v[24:27]
s_cbranch_scc1 Lg4_slow1_%=
Lg4_d1w1_%=:
v_mfma_f32_16x16x32_bf16 v[28:31],a[0:3],v[44:47],0
s_max_u32 s73,s48,s65
ds_read_b128 v[56:59],v2 offset:31744
v_lshlrev_b32 v60,16,v68
v_mfma_f32_16x16x32_bf16 v[32:35],a[4:7],v[44:47],0
s_mov_b32 s65,0
v_and_b32 v61,0xffff0000,v68
v_lshlrev_b32 v62,16,v69
v_mfma_f32_16x16x32_bf16 v[36:39],a[8:11],v[44:47],0
s_add_u32 s74,s53,s55
v_and_b32 v63,0xffff0000,v69
v_lshlrev_b32 v64,16,v70
v_mfma_f32_16x16x32_bf16 v[40:43],a[12:15],v[44:47],0
s_waitcnt lgkmcnt(2)
v_and_b32 v65,0xffff0000,v70
v_lshlrev_b32 v66,16,v71
v_mfma_f32_16x16x32_bf16 v[28:31],a[16:19],v[48:51],v[28:31]
s_cmp_eq_u32 s74,s56
ds_read_b128 v[44:47],v2 offset:32768
v_and_b32 v67,0xffff0000,v71
v_mfma_f32_16x16x32_bf16 v[32:35],a[20:23],v[48:51],v[32:35]
s_cselect_b32 s74,s57,s74
v_cvt_pk_bf16_f32 v12,v12,v13
v_cvt_pk_bf16_f32 v13,v14,v15
v_mfma_f32_16x16x32_bf16 v[36:39],a[24:27],v[48:51],v[36:39]
s_cmp_eq_u32 s74,s58
v_cvt_pk_bf16_f32 v14,v16,v17
v_cvt_pk_bf16_f32 v15,v18,v19
v_mfma_f32_16x16x32_bf16 v[40:43],a[28:31],v[48:51],v[40:43]
s_waitcnt lgkmcnt(2)
v_add_f32 v20,v20,v60
v_add_f32 v21,v21,v61
v_mfma_f32_16x16x32_bf16 v[28:31],a[32:35],v[52:55],v[28:31]
s_cselect_b32 s74,s59,s74
ds_read_b128 v[48:51],v2 offset:33792
v_add_f32 v22,v22,v62
v_mfma_f32_16x16x32_bf16 v[32:35],a[36:39],v[52:55],v[32:35]
buffer_store_dwordx4 v[12:15],v3,s[36:39],s73 offen offset:0 sc0 sc1
v_add_f32 v23,v23,v63
v_add_f32 v24,v24,v64
v_add_f32 v25,v25,v65
v_mfma_f32_16x16x32_bf16 v[36:39],a[40:43],v[52:55],v[36:39]
s_mov_b64 s[44:45],s[46:47]
v_add_f32 v26,v26,v66
v_add_f32 v27,v27,v67
v_mfma_f32_16x16x32_bf16 v[40:43],a[44:47],v[52:55],v[40:43]
s_waitcnt lgkmcnt(2)
v_cvt_pk_bf16_f32 v20,v20,v21
v_cvt_pk_bf16_f32 v21,v22,v23
v_mfma_f32_16x16x32_bf16 v[28:31],a[48:51],v[56:59],v[28:31]
s_mov_b64 s[46:47],s[48:49]
ds_read_b128 v[52:55],v2 offset:34816
v_cvt_pk_bf16_f32 v22,v24,v25
v_mfma_f32_16x16x32_bf16 v[32:35],a[52:55],v[56:59],v[32:35]
v_cvt_pk_bf16_f32 v23,v26,v27
v_mfma_f32_16x16x32_bf16 v[36:39],a[56:59],v[56:59],v[36:39]
s_waitcnt lgkmcnt(2)
s_nop 0
v_mfma_f32_16x16x32_bf16 v[40:43],a[60:63],v[56:59],v[40:43]
s_barrier
v_mfma_f32_16x16x32_bf16 v[28:31],a[64:67],v[44:47],v[28:31]
ds_read_b128 v[56:59],v2 offset:35840
ds_read_b32 v11,v8 offset:5120
v_mfma_f32_16x16x32_bf16 v[32:35],a[68:71],v[44:47],v[32:35]
v_mfma_f32_16x16x32_bf16 v[36:39],a[72:75],v[44:47],v[36:39]
v_mfma_f32_16x16x32_bf16 v[40:43],a[76:79],v[44:47],v[40:43]
s_waitcnt lgkmcnt(3)
v_mfma_f32_16x16x32_bf16 v[28:31],a[80:83],v[48:51],v[28:31]
s_waitcnt lgkmcnt(0)
ds_read_b128 v[44:47],v2 offset:36864
v_mfma_f32_16x16x32_bf16 v[32:35],a[84:87],v[48:51],v[32:35]
buffer_store_dwordx4 v[20:23],v4,s[36:39],s73 offen offset:0 sc0 sc1
v_cmp_eq_u32 vcc,s60,v11
v_mfma_f32_16x16x32_bf16 v[36:39],a[88:91],v[48:51],v[36:39]
s_cmp_eq_u32 vcc_lo,-1
s_cselect_b32 s67,s50,s59
s_mul_i32 s61,s67,7
v_mfma_f32_16x16x32_bf16 v[40:43],a[92:95],v[48:51],v[40:43]
s_lshr_b32 s61,s61,1
s_add_u32 m0,s63,0
v_mfma_f32_16x16x32_bf16 v[28:31],a[96:99],v[52:55],v[28:31]
ds_read_b128 v[48:51],v2 offset:37888
v_mfma_f32_16x16x32_bf16 v[32:35],a[100:103],v[52:55],v[32:35]
buffer_load_dwordx4 v0,s[36:39],s61 offen offset:0 sc1 lds
v_mfma_f32_16x16x32_bf16 v[36:39],a[104:107],v[52:55],v[36:39]
v_mfma_f32_16x16x32_bf16 v[40:43],a[108:111],v[52:55],v[40:43]
v_mfma_f32_16x16x32_bf16 v[28:31],a[112:115],v[56:59],v[28:31]
ds_read_b128 v[52:55],v2 offset:38912
v_mfma_f32_16x16x32_bf16 v[32:35],a[116:119],v[56:59],v[32:35]
buffer_load_dwordx4 v0,s[36:39],s61 offen offset:1024 sc1 lds
v_mfma_f32_16x16x32_bf16 v[36:39],a[120:123],v[56:59],v[36:39]
v_mfma_f32_16x16x32_bf16 v[40:43],a[124:127],v[56:59],v[40:43]
s_waitcnt lgkmcnt(2)
v_mfma_f32_16x16x32_bf16 v[28:31],a[128:131],v[44:47],v[28:31]
ds_read_b128 v[56:59],v2 offset:39936
v_mfma_f32_16x16x32_bf16 v[32:35],a[132:135],v[44:47],v[32:35]
buffer_load_dwordx4 v0,s[36:39],s61 offen offset:2048 sc1 lds
v_mfma_f32_16x16x32_bf16 v[36:39],a[136:139],v[44:47],v[36:39]
v_mfma_f32_16x16x32_bf16 v[40:43],a[140:143],v[44:47],v[40:43]
s_waitcnt lgkmcnt(2)
v_mfma_f32_16x16x32_bf16 v[28:31],a[144:147],v[48:51],v[28:31]
ds_read_b128 v[44:47],v2 offset:40960
v_mfma_f32_16x16x32_bf16 v[32:35],a[148:151],v[48:51],v[32:35]
buffer_load_dwordx4 v0,s[36:39],s61 offen offset:3072 sc1 lds
v_mfma_f32_16x16x32_bf16 v[36:39],a[152:155],v[48:51],v[36:39]
s_add_u32 m0,s64,0
s_cmp_lt_u32 s67,s59
v_mfma_f32_16x16x32_bf16 v[40:43],a[156:159],v[48:51],v[40:43]
s_waitcnt lgkmcnt(2)
v_mfma_f32_16x16x32_bf16 v[28:31],a[160:163],v[52:55],v[28:31]
ds_read_b128 v[48:51],v2 offset:41984
v_mfma_f32_16x16x32_bf16 v[32:35],a[164:167],v[52:55],v[32:35]
buffer_load_dwordx4 v[68:71],v9,s[36:39],s49 offen offset:0 sc1
v_mfma_f32_16x16x32_bf16 v[36:39],a[168:171],v[52:55],v[36:39]
s_mov_b64 s[48:49],s[50:51]
s_mov_b64 s[50:51],s[52:53]
s_mov_b32 s52,s74
v_mfma_f32_16x16x32_bf16 v[40:43],a[172:175],v[52:55],v[40:43]
s_waitcnt lgkmcnt(2)
v_mfma_f32_16x16x32_bf16 v[28:31],a[176:179],v[56:59],v[28:31]
ds_read_b128 v[52:55],v2 offset:43008
v_mfma_f32_16x16x32_bf16 v[32:35],a[180:183],v[56:59],v[32:35]
v_mfma_f32_16x16x32_bf16 v[36:39],a[184:187],v[56:59],v[36:39]
v_mfma_f32_16x16x32_bf16 v[40:43],a[188:191],v[56:59],v[40:43]
s_waitcnt lgkmcnt(2)
v_mfma_f32_16x16x32_bf16 v[28:31],a[192:195],v[44:47],v[28:31]
ds_read_b128 v[56:59],v2 offset:44032
v_mfma_f32_16x16x32_bf16 v[32:35],a[196:199],v[44:47],v[32:35]
v_mfma_f32_16x16x32_bf16 v[36:39],a[200:203],v[44:47],v[36:39]
s_waitcnt vmcnt(7) lgkmcnt(2)
v_mfma_f32_16x16x32_bf16 v[40:43],a[204:207],v[44:47],v[40:43]
s_barrier
v_mfma_f32_16x16x32_bf16 v[28:31],a[208:211],v[48:51],v[28:31]
ds_read_b128 v[44:47],v2 offset:45056
v_mfma_f32_16x16x32_bf16 v[32:35],a[212:215],v[48:51],v[32:35]
v_mfma_f32_16x16x32_bf16 v[36:39],a[216:219],v[48:51],v[36:39]
buffer_load_dwordx4 v1,s[36:39],s61 offen offset:0 sc1 lds
v_mfma_f32_16x16x32_bf16 v[40:43],a[220:223],v[48:51],v[40:43]
s_waitcnt lgkmcnt(2)
v_mfma_f32_16x16x32_bf16 v[28:31],a[224:227],v[52:55],v[28:31]
ds_read_b128 v[48:51],v2 offset:46080
v_mfma_f32_16x16x32_bf16 v[32:35],a[228:231],v[52:55],v[32:35]
v_mfma_f32_16x16x32_bf16 v[36:39],a[232:235],v[52:55],v[36:39]
v_mfma_f32_16x16x32_bf16 v[40:43],a[236:239],v[52:55],v[40:43]
s_waitcnt lgkmcnt(2)
v_mfma_f32_16x16x32_bf16 v[28:31],a[240:243],v[56:59],v[28:31]
buffer_load_dwordx4 v1,s[36:39],s61 offen offset:1024 sc1 lds
v_mfma_f32_16x16x32_bf16 v[32:35],a[244:247],v[56:59],v[32:35]
ds_read_b128 v[52:55],v2 offset:47104
v_mfma_f32_16x16x32_bf16 v[36:39],a[248:251],v[56:59],v[36:39]
v_mfma_f32_16x16x32_bf16 v[40:43],a[252:255],v[56:59],v[40:43]
s_waitcnt lgkmcnt(2)
v_mfma_f32_16x16x32_bf16 v[28:31],v[100:103],v[44:47],v[28:31]
ds_read_b128 v[56:59],v2 offset:48128
v_mfma_f32_16x16x32_bf16 v[32:35],v[104:107],v[44:47],v[32:35]
ds_read_b128 v[88:91],v10 offset:0
v_mfma_f32_16x16x32_bf16 v[36:39],v[108:111],v[44:47],v[36:39]
buffer_load_dwordx4 v1,s[36:39],s61 offen offset:2048 sc1 lds
v_mfma_f32_16x16x32_bf16 v[40:43],v[112:115],v[44:47],v[40:43]
s_waitcnt lgkmcnt(3)
v_mfma_f32_16x16x32_bf16 v[28:31],v[116:119],v[48:51],v[28:31]
ds_read_b128 v[44:47],v2 offset:49152
v_mfma_f32_16x16x32_bf16 v[32:35],v[120:123],v[48:51],v[32:35]
ds_read_b128 v[76:79],v10 offset:1024
v_mfma_f32_16x16x32_bf16 v[36:39],v[124:127],v[48:51],v[36:39]
v_mfma_f32_16x16x32_bf16 v[40:43],v[128:131],v[48:51],v[40:43]
s_waitcnt lgkmcnt(4)
v_mfma_f32_16x16x32_bf16 v[28:31],v[132:135],v[52:55],v[28:31]
ds_read_b128 v[48:51],v2 offset:50176
v_mfma_f32_16x16x32_bf16 v[32:35],v[136:139],v[52:55],v[32:35]
ds_read_b128 v[80:83],v10 offset:2048
v_mfma_f32_16x16x32_bf16 v[36:39],v[140:143],v[52:55],v[36:39]
v_mfma_f32_16x16x32_bf16 v[40:43],v[144:147],v[52:55],v[40:43]
s_waitcnt lgkmcnt(4)
v_mfma_f32_16x16x32_bf16 v[28:31],v[148:151],v[56:59],v[28:31]
ds_read_b128 v[52:55],v2 offset:51200
v_mfma_f32_16x16x32_bf16 v[32:35],v[152:155],v[56:59],v[32:35]
ds_read_b128 v[84:87],v10 offset:3072
v_mfma_f32_16x16x32_bf16 v[36:39],v[156:159],v[56:59],v[36:39]
v_mfma_f32_16x16x32_bf16 v[40:43],v[88:91],v[56:59],v[40:43])"
R"(
s_waitcnt lgkmcnt(4)
v_mfma_f32_16x16x32_bf16 v[28:31],v[160:163],v[44:47],v[28:31]
ds_read_b128 v[56:59],v2 offset:52224
v_mfma_f32_16x16x32_bf16 v[32:35],v[164:167],v[44:47],v[32:35]
ds_read_b128 v[88:91],v10 offset:4096
v_mfma_f32_16x16x32_bf16 v[36:39],v[168:171],v[44:47],v[36:39]
v_mfma_f32_16x16x32_bf16 v[40:43],v[76:79],v[44:47],v[40:43]
s_waitcnt lgkmcnt(4)
v_mfma_f32_16x16x32_bf16 v[28:31],v[172:175],v[48:51],v[28:31]
ds_read_b128 v[44:47],v2 offset:53248
v_mfma_f32_16x16x32_bf16 v[32:35],v[176:179],v[48:51],v[32:35]
ds_read_b128 v[76:79],v10 offset:5120
v_mfma_f32_16x16x32_bf16 v[36:39],v[180:183],v[48:51],v[36:39]
s_waitcnt vmcnt(8) lgkmcnt(4)
v_mfma_f32_16x16x32_bf16 v[40:43],v[80:83],v[48:51],v[40:43]
s_barrier
v_mfma_f32_16x16x32_bf16 v[28:31],v[184:187],v[52:55],v[28:31]
ds_read_b128 v[48:51],v2 offset:54272
v_mfma_f32_16x16x32_bf16 v[32:35],v[188:191],v[52:55],v[32:35]
ds_read_b128 v[80:83],v10 offset:6144
v_mfma_f32_16x16x32_bf16 v[36:39],v[192:195],v[52:55],v[36:39]
v_mfma_f32_16x16x32_bf16 v[40:43],v[84:87],v[52:55],v[40:43]
s_waitcnt lgkmcnt(4)
v_mfma_f32_16x16x32_bf16 v[28:31],v[196:199],v[56:59],v[28:31]
ds_read_b128 v[52:55],v2 offset:55296
v_mfma_f32_16x16x32_bf16 v[32:35],v[200:203],v[56:59],v[32:35]
ds_read_b128 v[84:87],v10 offset:7168
v_mfma_f32_16x16x32_bf16 v[36:39],v[204:207],v[56:59],v[36:39]
v_mfma_f32_16x16x32_bf16 v[40:43],v[88:91],v[56:59],v[40:43]
s_waitcnt lgkmcnt(4)
v_mfma_f32_16x16x32_bf16 v[28:31],v[208:211],v[44:47],v[28:31]
ds_read_b128 v[56:59],v2 offset:56320
v_mfma_f32_16x16x32_bf16 v[32:35],v[212:215],v[44:47],v[32:35]
ds_read_b128 v[88:91],v10 offset:8192
v_mfma_f32_16x16x32_bf16 v[36:39],v[216:219],v[44:47],v[36:39]
s_waitcnt vmcnt(3) lgkmcnt(4)
v_mfma_f32_16x16x32_bf16 v[40:43],v[76:79],v[44:47],v[40:43]
s_barrier
v_mfma_f32_16x16x32_bf16 v[28:31],v[220:223],v[48:51],v[28:31]
ds_read_b128 v[44:47],v2 offset:0
v_mfma_f32_16x16x32_bf16 v[32:35],v[224:227],v[48:51],v[32:35]
v_mfma_f32_16x16x32_bf16 v[36:39],v[228:231],v[48:51],v[36:39]
v_mfma_f32_16x16x32_bf16 v[40:43],v[80:83],v[48:51],v[40:43]
s_waitcnt lgkmcnt(3)
v_mfma_f32_16x16x32_bf16 v[28:31],v[232:235],v[52:55],v[28:31]
ds_read_b128 v[48:51],v2 offset:1024
v_mfma_f32_16x16x32_bf16 v[32:35],v[236:239],v[52:55],v[32:35]
v_mfma_f32_16x16x32_bf16 v[36:39],v[240:243],v[52:55],v[36:39]
v_mfma_f32_16x16x32_bf16 v[40:43],v[84:87],v[52:55],v[40:43]
s_waitcnt lgkmcnt(2)
v_mfma_f32_16x16x32_bf16 v[28:31],v[244:247],v[56:59],v[28:31]
ds_read_b128 v[52:55],v2 offset:2048
v_mfma_f32_16x16x32_bf16 v[32:35],v[248:251],v[56:59],v[32:35]
v_mfma_f32_16x16x32_bf16 v[36:39],v[252:255],v[56:59],v[36:39]
s_waitcnt lgkmcnt(2)
v_mfma_f32_16x16x32_bf16 v[40:43],v[88:91],v[56:59],v[40:43]
s_cbranch_scc1 Lg4_d0w1_%=
s_branch Lg4_slow0_%=
Lg4_d0w2_%=:
v_mfma_f32_16x16x32_bf16 v[12:15],a[0:3],v[44:47],0
s_max_u32 s73,s47,s65
ds_read_b128 v[56:59],v2 offset:3072
v_lshlrev_b32 v60,16,v68
v_mfma_f32_16x16x32_bf16 v[16:19],a[4:7],v[44:47],0
s_mov_b32 s65,0
v_and_b32 v61,0xffff0000,v68
v_lshlrev_b32 v62,16,v69
v_mfma_f32_16x16x32_bf16 v[20:23],a[8:11],v[44:47],0
s_add_u32 s53,s52,s55
v_and_b32 v63,0xffff0000,v69
v_lshlrev_b32 v64,16,v70
v_mfma_f32_16x16x32_bf16 v[24:27],a[12:15],v[44:47],0
s_waitcnt lgkmcnt(2)
v_and_b32 v65,0xffff0000,v70
v_lshlrev_b32 v66,16,v71
v_mfma_f32_16x16x32_bf16 v[12:15],a[16:19],v[48:51],v[12:15]
s_cmp_eq_u32 s53,s56
ds_read_b128 v[44:47],v2 offset:4096
v_and_b32 v67,0xffff0000,v71
v_mfma_f32_16x16x32_bf16 v[16:19],a[20:23],v[48:51],v[16:19]
s_cselect_b32 s53,s57,s53
v_cvt_pk_bf16_f32 v28,v28,v29
v_cvt_pk_bf16_f32 v29,v30,v31
v_mfma_f32_16x16x32_bf16 v[20:23],a[24:27],v[48:51],v[20:23]
s_cmp_eq_u32 s53,s58
v_cvt_pk_bf16_f32 v30,v32,v33
v_cvt_pk_bf16_f32 v31,v34,v35
v_mfma_f32_16x16x32_bf16 v[24:27],a[28:31],v[48:51],v[24:27]
s_waitcnt lgkmcnt(2)
v_add_f32 v36,v36,v60
v_add_f32 v37,v37,v61
v_mfma_f32_16x16x32_bf16 v[12:15],a[32:35],v[52:55],v[12:15]
s_cselect_b32 s53,s59,s53
ds_read_b128 v[48:51],v2 offset:5120
v_add_f32 v38,v38,v62
v_mfma_f32_16x16x32_bf16 v[16:19],a[36:39],v[52:55],v[16:19]
v_add_f32 v39,v39,v63
v_add_f32 v40,v40,v64
v_mfma_f32_16x16x32_bf16 v[20:23],a[40:43],v[52:55],v[20:23]
buffer_store_dwordx4 v[28:31],v3,s[36:39],s73 offen offset:0 sc0 sc1
v_add_f32 v41,v41,v65
v_add_f32 v42,v42,v66
v_add_f32 v43,v43,v67
v_mfma_f32_16x16x32_bf16 v[24:27],a[44:47],v[52:55],v[24:27]
s_waitcnt lgkmcnt(2)
v_cvt_pk_bf16_f32 v36,v36,v37
v_cvt_pk_bf16_f32 v37,v38,v39
v_mfma_f32_16x16x32_bf16 v[12:15],a[48:51],v[56:59],v[12:15]
ds_read_b128 v[52:55],v2 offset:6144
v_cvt_pk_bf16_f32 v38,v40,v41
v_mfma_f32_16x16x32_bf16 v[16:19],a[52:55],v[56:59],v[16:19]
v_cvt_pk_bf16_f32 v39,v42,v43
v_mfma_f32_16x16x32_bf16 v[20:23],a[56:59],v[56:59],v[20:23]
s_waitcnt lgkmcnt(2)
s_nop 0
v_mfma_f32_16x16x32_bf16 v[24:27],a[60:63],v[56:59],v[24:27]
s_barrier
v_mfma_f32_16x16x32_bf16 v[12:15],a[64:67],v[44:47],v[12:15]
ds_read_b128 v[56:59],v2 offset:7168
ds_read_b32 v11,v8 offset:4096
v_mfma_f32_16x16x32_bf16 v[16:19],a[68:71],v[44:47],v[16:19]
v_mfma_f32_16x16x32_bf16 v[20:23],a[72:75],v[44:47],v[20:23]
v_mfma_f32_16x16x32_bf16 v[24:27],a[76:79],v[44:47],v[24:27]
s_waitcnt lgkmcnt(3)
v_mfma_f32_16x16x32_bf16 v[12:15],a[80:83],v[48:51],v[12:15]
s_waitcnt lgkmcnt(0)
ds_read_b128 v[44:47],v2 offset:8192
v_mfma_f32_16x16x32_bf16 v[16:19],a[84:87],v[48:51],v[16:19]
v_mfma_f32_16x16x32_bf16 v[20:23],a[88:91],v[48:51],v[20:23]
buffer_store_dwordx4 v[36:39],v4,s[36:39],s73 offen offset:0 sc0 sc1
v_cmp_eq_u32 vcc,s60,v11
v_mfma_f32_16x16x32_bf16 v[24:27],a[92:95],v[48:51],v[24:27]
s_cmp_eq_u32 vcc_lo,-1
s_cselect_b32 s66,s49,s59
v_mfma_f32_16x16x32_bf16 v[12:15],a[96:99],v[52:55],v[12:15]
s_mul_i32 s61,s66,7
ds_read_b128 v[48:51],v2 offset:9216
s_lshr_b32 s61,s61,1
v_mfma_f32_16x16x32_bf16 v[16:19],a[100:103],v[52:55],v[16:19]
s_add_u32 m0,s63,28672
v_mfma_f32_16x16x32_bf16 v[20:23],a[104:107],v[52:55],v[20:23]
buffer_load_dwordx4 v0,s[36:39],s61 offen offset:0 sc1 lds
v_mfma_f32_16x16x32_bf16 v[24:27],a[108:111],v[52:55],v[24:27]
v_mfma_f32_16x16x32_bf16 v[12:15],a[112:115],v[56:59],v[12:15]
ds_read_b128 v[52:55],v2 offset:10240
v_mfma_f32_16x16x32_bf16 v[16:19],a[116:119],v[56:59],v[16:19]
v_mfma_f32_16x16x32_bf16 v[20:23],a[120:123],v[56:59],v[20:23]
buffer_load_dwordx4 v0,s[36:39],s61 offen offset:1024 sc1 lds
v_mfma_f32_16x16x32_bf16 v[24:27],a[124:127],v[56:59],v[24:27]
s_waitcnt lgkmcnt(2)
v_mfma_f32_16x16x32_bf16 v[12:15],a[128:131],v[44:47],v[12:15]
ds_read_b128 v[56:59],v2 offset:11264
v_mfma_f32_16x16x32_bf16 v[16:19],a[132:135],v[44:47],v[16:19]
v_mfma_f32_16x16x32_bf16 v[20:23],a[136:139],v[44:47],v[20:23]
buffer_load_dwordx4 v0,s[36:39],s61 offen offset:2048 sc1 lds
v_mfma_f32_16x16x32_bf16 v[24:27],a[140:143],v[44:47],v[24:27]
s_waitcnt lgkmcnt(2)
v_mfma_f32_16x16x32_bf16 v[12:15],a[144:147],v[48:51],v[12:15]
ds_read_b128 v[44:47],v2 offset:12288
v_mfma_f32_16x16x32_bf16 v[16:19],a[148:151],v[48:51],v[16:19]
v_mfma_f32_16x16x32_bf16 v[20:23],a[152:155],v[48:51],v[20:23]
buffer_load_dwordx4 v0,s[36:39],s61 offen offset:3072 sc1 lds
v_mfma_f32_16x16x32_bf16 v[24:27],a[156:159],v[48:51],v[24:27]
s_waitcnt lgkmcnt(2)
s_add_u32 m0,s64,28672
s_cmp_ge_u32 s66,s59
v_mfma_f32_16x16x32_bf16 v[12:15],a[160:163],v[52:55],v[12:15]
ds_read_b128 v[48:51],v2 offset:13312
v_mfma_f32_16x16x32_bf16 v[16:19],a[164:167],v[52:55],v[16:19]
v_mfma_f32_16x16x32_bf16 v[20:23],a[168:171],v[52:55],v[20:23]
buffer_load_dwordx4 v[68:71],v9,s[36:39],s48 offen offset:0 sc1
v_mfma_f32_16x16x32_bf16 v[24:27],a[172:175],v[52:55],v[24:27]
s_waitcnt lgkmcnt(2)
v_mfma_f32_16x16x32_bf16 v[12:15],a[176:179],v[56:59],v[12:15]
ds_read_b128 v[52:55],v2 offset:14336
v_mfma_f32_16x16x32_bf16 v[16:19],a[180:183],v[56:59],v[16:19]
v_mfma_f32_16x16x32_bf16 v[20:23],a[184:187],v[56:59],v[20:23]
v_mfma_f32_16x16x32_bf16 v[24:27],a[188:191],v[56:59],v[24:27]
s_waitcnt lgkmcnt(2)
v_mfma_f32_16x16x32_bf16 v[12:15],a[192:195],v[44:47],v[12:15]
ds_read_b128 v[56:59],v2 offset:15360
v_mfma_f32_16x16x32_bf16 v[16:19],a[196:199],v[44:47],v[16:19]
v_mfma_f32_16x16x32_bf16 v[20:23],a[200:203],v[44:47],v[20:23]
s_waitcnt vmcnt(7) lgkmcnt(2)
v_mfma_f32_16x16x32_bf16 v[24:27],a[204:207],v[44:47],v[24:27]
s_barrier
v_mfma_f32_16x16x32_bf16 v[12:15],a[208:211],v[48:51],v[12:15]
ds_read_b128 v[44:47],v2 offset:16384
v_mfma_f32_16x16x32_bf16 v[16:19],a[212:215],v[48:51],v[16:19]
v_mfma_f32_16x16x32_bf16 v[20:23],a[216:219],v[48:51],v[20:23]
v_mfma_f32_16x16x32_bf16 v[24:27],a[220:223],v[48:51],v[24:27]
s_waitcnt lgkmcnt(2)
buffer_load_dwordx4 v1,s[36:39],s61 offen offset:0 sc1 lds
v_mfma_f32_16x16x32_bf16 v[12:15],a[224:227],v[52:55],v[12:15]
ds_read_b128 v[48:51],v2 offset:17408
v_mfma_f32_16x16x32_bf16 v[16:19],a[228:231],v[52:55],v[16:19]
v_mfma_f32_16x16x32_bf16 v[20:23],a[232:235],v[52:55],v[20:23]
v_mfma_f32_16x16x32_bf16 v[24:27],a[236:239],v[52:55],v[24:27]
s_waitcnt lgkmcnt(2)
v_mfma_f32_16x16x32_bf16 v[12:15],a[240:243],v[56:59],v[12:15])"
R"(
ds_read_b128 v[52:55],v2 offset:18432
v_mfma_f32_16x16x32_bf16 v[16:19],a[244:247],v[56:59],v[16:19]
buffer_load_dwordx4 v1,s[36:39],s61 offen offset:1024 sc1 lds
v_mfma_f32_16x16x32_bf16 v[20:23],a[248:251],v[56:59],v[20:23]
v_mfma_f32_16x16x32_bf16 v[24:27],a[252:255],v[56:59],v[24:27]
s_waitcnt lgkmcnt(2)
v_mfma_f32_16x16x32_bf16 v[12:15],v[100:103],v[44:47],v[12:15]
ds_read_b128 v[56:59],v2 offset:19456
v_mfma_f32_16x16x32_bf16 v[16:19],v[104:107],v[44:47],v[16:19]
ds_read_b128 v[88:91],v10 offset:0
v_mfma_f32_16x16x32_bf16 v[20:23],v[108:111],v[44:47],v[20:23]
v_mfma_f32_16x16x32_bf16 v[24:27],v[112:115],v[44:47],v[24:27]
s_waitcnt lgkmcnt(3)
buffer_load_dwordx4 v1,s[36:39],s61 offen offset:2048 sc1 lds
v_mfma_f32_16x16x32_bf16 v[12:15],v[116:119],v[48:51],v[12:15]
ds_read_b128 v[44:47],v2 offset:20480
v_mfma_f32_16x16x32_bf16 v[16:19],v[120:123],v[48:51],v[16:19]
ds_read_b128 v[76:79],v10 offset:1024
v_mfma_f32_16x16x32_bf16 v[20:23],v[124:127],v[48:51],v[20:23]
v_mfma_f32_16x16x32_bf16 v[24:27],v[128:131],v[48:51],v[24:27]
s_waitcnt lgkmcnt(4)
v_mfma_f32_16x16x32_bf16 v[12:15],v[132:135],v[52:55],v[12:15]
ds_read_b128 v[48:51],v2 offset:21504
v_mfma_f32_16x16x32_bf16 v[16:19],v[136:139],v[52:55],v[16:19]
ds_read_b128 v[80:83],v10 offset:2048
v_mfma_f32_16x16x32_bf16 v[20:23],v[140:143],v[52:55],v[20:23]
v_mfma_f32_16x16x32_bf16 v[24:27],v[144:147],v[52:55],v[24:27]
s_waitcnt lgkmcnt(4)
v_mfma_f32_16x16x32_bf16 v[12:15],v[148:151],v[56:59],v[12:15]
ds_read_b128 v[52:55],v2 offset:22528
v_mfma_f32_16x16x32_bf16 v[16:19],v[152:155],v[56:59],v[16:19]
ds_read_b128 v[84:87],v10 offset:3072
v_mfma_f32_16x16x32_bf16 v[20:23],v[156:159],v[56:59],v[20:23]
v_mfma_f32_16x16x32_bf16 v[24:27],v[88:91],v[56:59],v[24:27]
s_waitcnt lgkmcnt(4)
v_mfma_f32_16x16x32_bf16 v[12:15],v[160:163],v[44:47],v[12:15]
ds_read_b128 v[56:59],v2 offset:23552
v_mfma_f32_16x16x32_bf16 v[16:19],v[164:167],v[44:47],v[16:19]
ds_read_b128 v[88:91],v10 offset:4096
v_mfma_f32_16x16x32_bf16 v[20:23],v[168:171],v[44:47],v[20:23]
v_mfma_f32_16x16x32_bf16 v[24:27],v[76:79],v[44:47],v[24:27]
s_waitcnt lgkmcnt(4)
v_mfma_f32_16x16x32_bf16 v[12:15],v[172:175],v[48:51],v[12:15]
ds_read_b128 v[44:47],v2 offset:24576
v_mfma_f32_16x16x32_bf16 v[16:19],v[176:179],v[48:51],v[16:19]
ds_read_b128 v[76:79],v10 offset:5120
v_mfma_f32_16x16x32_bf16 v[20:23],v[180:183],v[48:51],v[20:23]
s_waitcnt vmcnt(8) lgkmcnt(4)
v_mfma_f32_16x16x32_bf16 v[24:27],v[80:83],v[48:51],v[24:27]
s_barrier
v_mfma_f32_16x16x32_bf16 v[12:15],v[184:187],v[52:55],v[12:15]
ds_read_b128 v[48:51],v2 offset:25600
v_mfma_f32_16x16x32_bf16 v[16:19],v[188:191],v[52:55],v[16:19]
ds_read_b128 v[80:83],v10 offset:6144
v_mfma_f32_16x16x32_bf16 v[20:23],v[192:195],v[52:55],v[20:23]
v_mfma_f32_16x16x32_bf16 v[24:27],v[84:87],v[52:55],v[24:27]
s_waitcnt lgkmcnt(4)
v_mfma_f32_16x16x32_bf16 v[12:15],v[196:199],v[56:59],v[12:15]
ds_read_b128 v[52:55],v2 offset:26624
v_mfma_f32_16x16x32_bf16 v[16:19],v[200:203],v[56:59],v[16:19]
ds_read_b128 v[84:87],v10 offset:7168
v_mfma_f32_16x16x32_bf16 v[20:23],v[204:207],v[56:59],v[20:23]
v_mfma_f32_16x16x32_bf16 v[24:27],v[88:91],v[56:59],v[24:27]
s_waitcnt lgkmcnt(4)
v_mfma_f32_16x16x32_bf16 v[12:15],v[208:211],v[44:47],v[12:15]
ds_read_b128 v[56:59],v2 offset:27648
v_mfma_f32_16x16x32_bf16 v[16:19],v[212:215],v[44:47],v[16:19]
ds_read_b128 v[88:91],v10 offset:8192
v_mfma_f32_16x16x32_bf16 v[20:23],v[216:219],v[44:47],v[20:23]
s_waitcnt vmcnt(3) lgkmcnt(4)
v_mfma_f32_16x16x32_bf16 v[24:27],v[76:79],v[44:47],v[24:27]
s_barrier
v_mfma_f32_16x16x32_bf16 v[12:15],v[220:223],v[48:51],v[12:15]
ds_read_b128 v[44:47],v2 offset:28672
v_mfma_f32_16x16x32_bf16 v[16:19],v[224:227],v[48:51],v[16:19]
v_mfma_f32_16x16x32_bf16 v[20:23],v[228:231],v[48:51],v[20:23]
v_mfma_f32_16x16x32_bf16 v[24:27],v[80:83],v[48:51],v[24:27]
s_waitcnt lgkmcnt(3)
v_mfma_f32_16x16x32_bf16 v[12:15],v[232:235],v[52:55],v[12:15]
ds_read_b128 v[48:51],v2 offset:29696
v_mfma_f32_16x16x32_bf16 v[16:19],v[236:239],v[52:55],v[16:19]
v_mfma_f32_16x16x32_bf16 v[20:23],v[240:243],v[52:55],v[20:23]
v_mfma_f32_16x16x32_bf16 v[24:27],v[84:87],v[52:55],v[24:27]
s_waitcnt lgkmcnt(2)
v_mfma_f32_16x16x32_bf16 v[12:15],v[244:247],v[56:59],v[12:15]
ds_read_b128 v[52:55],v2 offset:30720
v_mfma_f32_16x16x32_bf16 v[16:19],v[248:251],v[56:59],v[16:19]
v_mfma_f32_16x16x32_bf16 v[20:23],v[252:255],v[56:59],v[20:23]
s_waitcnt lgkmcnt(2)
v_mfma_f32_16x16x32_bf16 v[24:27],v[88:91],v[56:59],v[24:27]
s_cbranch_scc1 Lg4_slow1_%=
Lg4_d1w2_%=:
v_mfma_f32_16x16x32_bf16 v[28:31],a[0:3],v[44:47],0
s_max_u32 s73,s48,s65
ds_read_b128 v[56:59],v2 offset:31744
v_lshlrev_b32 v60,16,v68
v_mfma_f32_16x16x32_bf16 v[32:35],a[4:7],v[44:47],0
s_mov_b32 s65,0
v_and_b32 v61,0xffff0000,v68
v_lshlrev_b32 v62,16,v69
v_mfma_f32_16x16x32_bf16 v[36:39],a[8:11],v[44:47],0
s_add_u32 s74,s53,s55
v_and_b32 v63,0xffff0000,v69
v_lshlrev_b32 v64,16,v70
v_mfma_f32_16x16x32_bf16 v[40:43],a[12:15],v[44:47],0
s_waitcnt lgkmcnt(2)
v_and_b32 v65,0xffff0000,v70
v_lshlrev_b32 v66,16,v71
v_mfma_f32_16x16x32_bf16 v[28:31],a[16:19],v[48:51],v[28:31]
s_cmp_eq_u32 s74,s56
ds_read_b128 v[44:47],v2 offset:32768
v_and_b32 v67,0xffff0000,v71
v_mfma_f32_16x16x32_bf16 v[32:35],a[20:23],v[48:51],v[32:35]
s_cselect_b32 s74,s57,s74
v_cvt_pk_bf16_f32 v12,v12,v13
v_cvt_pk_bf16_f32 v13,v14,v15
v_mfma_f32_16x16x32_bf16 v[36:39],a[24:27],v[48:51],v[36:39]
s_cmp_eq_u32 s74,s58
v_cvt_pk_bf16_f32 v14,v16,v17
v_cvt_pk_bf16_f32 v15,v18,v19
v_mfma_f32_16x16x32_bf16 v[40:43],a[28:31],v[48:51],v[40:43]
s_waitcnt lgkmcnt(2)
v_add_f32 v20,v20,v60
v_add_f32 v21,v21,v61
v_mfma_f32_16x16x32_bf16 v[28:31],a[32:35],v[52:55],v[28:31]
s_cselect_b32 s74,s59,s74
ds_read_b128 v[48:51],v2 offset:33792
v_add_f32 v22,v22,v62
v_mfma_f32_16x16x32_bf16 v[32:35],a[36:39],v[52:55],v[32:35]
s_mov_b64 s[44:45],s[46:47]
v_add_f32 v23,v23,v63
v_add_f32 v24,v24,v64
v_mfma_f32_16x16x32_bf16 v[36:39],a[40:43],v[52:55],v[36:39]
buffer_store_dwordx4 v[12:15],v3,s[36:39],s73 offen offset:0 sc0 sc1
v_add_f32 v25,v25,v65
v_add_f32 v26,v26,v66
v_add_f32 v27,v27,v67
v_mfma_f32_16x16x32_bf16 v[40:43],a[44:47],v[52:55],v[40:43]
s_waitcnt lgkmcnt(2)
v_cvt_pk_bf16_f32 v20,v20,v21
v_cvt_pk_bf16_f32 v21,v22,v23
v_mfma_f32_16x16x32_bf16 v[28:31],a[48:51],v[56:59],v[28:31]
s_mov_b64 s[46:47],s[48:49]
ds_read_b128 v[52:55],v2 offset:34816
v_cvt_pk_bf16_f32 v22,v24,v25
v_mfma_f32_16x16x32_bf16 v[32:35],a[52:55],v[56:59],v[32:35]
v_cvt_pk_bf16_f32 v23,v26,v27
v_mfma_f32_16x16x32_bf16 v[36:39],a[56:59],v[56:59],v[36:39]
s_waitcnt lgkmcnt(2)
s_nop 0
v_mfma_f32_16x16x32_bf16 v[40:43],a[60:63],v[56:59],v[40:43]
s_barrier
v_mfma_f32_16x16x32_bf16 v[28:31],a[64:67],v[44:47],v[28:31]
ds_read_b128 v[56:59],v2 offset:35840
ds_read_b32 v11,v8 offset:5120
v_mfma_f32_16x16x32_bf16 v[32:35],a[68:71],v[44:47],v[32:35]
v_mfma_f32_16x16x32_bf16 v[36:39],a[72:75],v[44:47],v[36:39]
v_mfma_f32_16x16x32_bf16 v[40:43],a[76:79],v[44:47],v[40:43]
s_waitcnt lgkmcnt(3)
v_mfma_f32_16x16x32_bf16 v[28:31],a[80:83],v[48:51],v[28:31]
s_waitcnt lgkmcnt(0)
ds_read_b128 v[44:47],v2 offset:36864
v_mfma_f32_16x16x32_bf16 v[32:35],a[84:87],v[48:51],v[32:35]
v_mfma_f32_16x16x32_bf16 v[36:39],a[88:91],v[48:51],v[36:39]
buffer_store_dwordx4 v[20:23],v4,s[36:39],s73 offen offset:0 sc0 sc1
v_cmp_eq_u32 vcc,s60,v11
v_mfma_f32_16x16x32_bf16 v[40:43],a[92:95],v[48:51],v[40:43]
s_cmp_eq_u32 vcc_lo,-1
s_cselect_b32 s67,s50,s59
v_mfma_f32_16x16x32_bf16 v[28:31],a[96:99],v[52:55],v[28:31]
s_mul_i32 s61,s67,7
ds_read_b128 v[48:51],v2 offset:37888
s_lshr_b32 s61,s61,1
v_mfma_f32_16x16x32_bf16 v[32:35],a[100:103],v[52:55],v[32:35]
s_add_u32 m0,s63,0
v_mfma_f32_16x16x32_bf16 v[36:39],a[104:107],v[52:55],v[36:39]
buffer_load_dwordx4 v0,s[36:39],s61 offen offset:0 sc1 lds
v_mfma_f32_16x16x32_bf16 v[40:43],a[108:111],v[52:55],v[40:43]
v_mfma_f32_16x16x32_bf16 v[28:31],a[112:115],v[56:59],v[28:31]
ds_read_b128 v[52:55],v2 offset:38912
v_mfma_f32_16x16x32_bf16 v[32:35],a[116:119],v[56:59],v[32:35]
v_mfma_f32_16x16x32_bf16 v[36:39],a[120:123],v[56:59],v[36:39]
buffer_load_dwordx4 v0,s[36:39],s61 offen offset:1024 sc1 lds
v_mfma_f32_16x16x32_bf16 v[40:43],a[124:127],v[56:59],v[40:43]
s_waitcnt lgkmcnt(2)
v_mfma_f32_16x16x32_bf16 v[28:31],a[128:131],v[44:47],v[28:31]
ds_read_b128 v[56:59],v2 offset:39936
v_mfma_f32_16x16x32_bf16 v[32:35],a[132:135],v[44:47],v[32:35]
v_mfma_f32_16x16x32_bf16 v[36:39],a[136:139],v[44:47],v[36:39]
buffer_load_dwordx4 v0,s[36:39],s61 offen offset:2048 sc1 lds
v_mfma_f32_16x16x32_bf16 v[40:43],a[140:143],v[44:47],v[40:43]
s_waitcnt lgkmcnt(2)
v_mfma_f32_16x16x32_bf16 v[28:31],a[144:147],v[48:51],v[28:31]
ds_read_b128 v[44:47],v2 offset:40960
v_mfma_f32_16x16x32_bf16 v[32:35],a[148:151],v[48:51],v[32:35]
v_mfma_f32_16x16x32_bf16 v[36:39],a[152:155],v[48:51],v[36:39]
buffer_load_dwordx4 v0,s[36:39],s61 offen offset:3072 sc1 lds
v_mfma_f32_16x16x32_bf16 v[40:43],a[156:159],v[48:51],v[40:43]
s_waitcnt lgkmcnt(2)
s_add_u32 m0,s64,0)"
R"(
s_cmp_lt_u32 s67,s59
v_mfma_f32_16x16x32_bf16 v[28:31],a[160:163],v[52:55],v[28:31]
ds_read_b128 v[48:51],v2 offset:41984
v_mfma_f32_16x16x32_bf16 v[32:35],a[164:167],v[52:55],v[32:35]
v_mfma_f32_16x16x32_bf16 v[36:39],a[168:171],v[52:55],v[36:39]
buffer_load_dwordx4 v[68:71],v9,s[36:39],s49 offen offset:0 sc1
v_mfma_f32_16x16x32_bf16 v[40:43],a[172:175],v[52:55],v[40:43]
s_waitcnt lgkmcnt(2)
s_mov_b64 s[48:49],s[50:51]
s_mov_b64 s[50:51],s[52:53]
v_mfma_f32_16x16x32_bf16 v[28:31],a[176:179],v[56:59],v[28:31]
s_mov_b32 s52,s74
ds_read_b128 v[52:55],v2 offset:43008
v_mfma_f32_16x16x32_bf16 v[32:35],a[180:183],v[56:59],v[32:35]
v_mfma_f32_16x16x32_bf16 v[36:39],a[184:187],v[56:59],v[36:39]
v_mfma_f32_16x16x32_bf16 v[40:43],a[188:191],v[56:59],v[40:43]
s_waitcnt lgkmcnt(2)
v_mfma_f32_16x16x32_bf16 v[28:31],a[192:195],v[44:47],v[28:31]
ds_read_b128 v[56:59],v2 offset:44032
v_mfma_f32_16x16x32_bf16 v[32:35],a[196:199],v[44:47],v[32:35]
v_mfma_f32_16x16x32_bf16 v[36:39],a[200:203],v[44:47],v[36:39]
s_waitcnt vmcnt(7) lgkmcnt(2)
v_mfma_f32_16x16x32_bf16 v[40:43],a[204:207],v[44:47],v[40:43]
s_barrier
v_mfma_f32_16x16x32_bf16 v[28:31],a[208:211],v[48:51],v[28:31]
ds_read_b128 v[44:47],v2 offset:45056
v_mfma_f32_16x16x32_bf16 v[32:35],a[212:215],v[48:51],v[32:35]
v_mfma_f32_16x16x32_bf16 v[36:39],a[216:219],v[48:51],v[36:39]
v_mfma_f32_16x16x32_bf16 v[40:43],a[220:223],v[48:51],v[40:43]
s_waitcnt lgkmcnt(2)
buffer_load_dwordx4 v1,s[36:39],s61 offen offset:0 sc1 lds
v_mfma_f32_16x16x32_bf16 v[28:31],a[224:227],v[52:55],v[28:31]
ds_read_b128 v[48:51],v2 offset:46080
v_mfma_f32_16x16x32_bf16 v[32:35],a[228:231],v[52:55],v[32:35]
v_mfma_f32_16x16x32_bf16 v[36:39],a[232:235],v[52:55],v[36:39]
v_mfma_f32_16x16x32_bf16 v[40:43],a[236:239],v[52:55],v[40:43]
s_waitcnt lgkmcnt(2)
v_mfma_f32_16x16x32_bf16 v[28:31],a[240:243],v[56:59],v[28:31]
ds_read_b128 v[52:55],v2 offset:47104
v_mfma_f32_16x16x32_bf16 v[32:35],a[244:247],v[56:59],v[32:35]
buffer_load_dwordx4 v1,s[36:39],s61 offen offset:1024 sc1 lds
v_mfma_f32_16x16x32_bf16 v[36:39],a[248:251],v[56:59],v[36:39]
v_mfma_f32_16x16x32_bf16 v[40:43],a[252:255],v[56:59],v[40:43]
s_waitcnt lgkmcnt(2)
v_mfma_f32_16x16x32_bf16 v[28:31],v[100:103],v[44:47],v[28:31]
ds_read_b128 v[56:59],v2 offset:48128
v_mfma_f32_16x16x32_bf16 v[32:35],v[104:107],v[44:47],v[32:35]
ds_read_b128 v[88:91],v10 offset:0
v_mfma_f32_16x16x32_bf16 v[36:39],v[108:111],v[44:47],v[36:39]
v_mfma_f32_16x16x32_bf16 v[40:43],v[112:115],v[44:47],v[40:43]
s_waitcnt lgkmcnt(3)
buffer_load_dwordx4 v1,s[36:39],s61 offen offset:2048 sc1 lds
v_mfma_f32_16x16x32_bf16 v[28:31],v[116:119],v[48:51],v[28:31]
ds_read_b128 v[44:47],v2 offset:49152
v_mfma_f32_16x16x32_bf16 v[32:35],v[120:123],v[48:51],v[32:35]
ds_read_b128 v[76:79],v10 offset:1024
v_mfma_f32_16x16x32_bf16 v[36:39],v[124:127],v[48:51],v[36:39]
v_mfma_f32_16x16x32_bf16 v[40:43],v[128:131],v[48:51],v[40:43]
s_waitcnt lgkmcnt(4)
v_mfma_f32_16x16x32_bf16 v[28:31],v[132:135],v[52:55],v[28:31]
ds_read_b128 v[48:51],v2 offset:50176
v_mfma_f32_16x16x32_bf16 v[32:35],v[136:139],v[52:55],v[32:35]
ds_read_b128 v[80:83],v10 offset:2048
v_mfma_f32_16x16x32_bf16 v[36:39],v[140:143],v[52:55],v[36:39]
v_mfma_f32_16x16x32_bf16 v[40:43],v[144:147],v[52:55],v[40:43]
s_waitcnt lgkmcnt(4)
v_mfma_f32_16x16x32_bf16 v[28:31],v[148:151],v[56:59],v[28:31]
ds_read_b128 v[52:55],v2 offset:51200
v_mfma_f32_16x16x32_bf16 v[32:35],v[152:155],v[56:59],v[32:35]
ds_read_b128 v[84:87],v10 offset:3072
v_mfma_f32_16x16x32_bf16 v[36:39],v[156:159],v[56:59],v[36:39]
v_mfma_f32_16x16x32_bf16 v[40:43],v[88:91],v[56:59],v[40:43]
s_waitcnt lgkmcnt(4)
v_mfma_f32_16x16x32_bf16 v[28:31],v[160:163],v[44:47],v[28:31]
ds_read_b128 v[56:59],v2 offset:52224
v_mfma_f32_16x16x32_bf16 v[32:35],v[164:167],v[44:47],v[32:35]
ds_read_b128 v[88:91],v10 offset:4096
v_mfma_f32_16x16x32_bf16 v[36:39],v[168:171],v[44:47],v[36:39]
v_mfma_f32_16x16x32_bf16 v[40:43],v[76:79],v[44:47],v[40:43]
s_waitcnt lgkmcnt(4)
v_mfma_f32_16x16x32_bf16 v[28:31],v[172:175],v[48:51],v[28:31]
ds_read_b128 v[44:47],v2 offset:53248
v_mfma_f32_16x16x32_bf16 v[32:35],v[176:179],v[48:51],v[32:35]
ds_read_b128 v[76:79],v10 offset:5120
v_mfma_f32_16x16x32_bf16 v[36:39],v[180:183],v[48:51],v[36:39]
s_waitcnt vmcnt(8) lgkmcnt(4)
v_mfma_f32_16x16x32_bf16 v[40:43],v[80:83],v[48:51],v[40:43]
s_barrier
v_mfma_f32_16x16x32_bf16 v[28:31],v[184:187],v[52:55],v[28:31]
ds_read_b128 v[48:51],v2 offset:54272
v_mfma_f32_16x16x32_bf16 v[32:35],v[188:191],v[52:55],v[32:35]
ds_read_b128 v[80:83],v10 offset:6144
v_mfma_f32_16x16x32_bf16 v[36:39],v[192:195],v[52:55],v[36:39]
v_mfma_f32_16x16x32_bf16 v[40:43],v[84:87],v[52:55],v[40:43]
s_waitcnt lgkmcnt(4)
v_mfma_f32_16x16x32_bf16 v[28:31],v[196:199],v[56:59],v[28:31]
ds_read_b128 v[52:55],v2 offset:55296
v_mfma_f32_16x16x32_bf16 v[32:35],v[200:203],v[56:59],v[32:35]
ds_read_b128 v[84:87],v10 offset:7168
v_mfma_f32_16x16x32_bf16 v[36:39],v[204:207],v[56:59],v[36:39]
v_mfma_f32_16x16x32_bf16 v[40:43],v[88:91],v[56:59],v[40:43]
s_waitcnt lgkmcnt(4)
v_mfma_f32_16x16x32_bf16 v[28:31],v[208:211],v[44:47],v[28:31]
ds_read_b128 v[56:59],v2 offset:56320
v_mfma_f32_16x16x32_bf16 v[32:35],v[212:215],v[44:47],v[32:35]
ds_read_b128 v[88:91],v10 offset:8192
v_mfma_f32_16x16x32_bf16 v[36:39],v[216:219],v[44:47],v[36:39]
s_waitcnt vmcnt(3) lgkmcnt(4)
v_mfma_f32_16x16x32_bf16 v[40:43],v[76:79],v[44:47],v[40:43]
s_barrier
v_mfma_f32_16x16x32_bf16 v[28:31],v[220:223],v[48:51],v[28:31]
ds_read_b128 v[44:47],v2 offset:0
v_mfma_f32_16x16x32_bf16 v[32:35],v[224:227],v[48:51],v[32:35]
v_mfma_f32_16x16x32_bf16 v[36:39],v[228:231],v[48:51],v[36:39]
v_mfma_f32_16x16x32_bf16 v[40:43],v[80:83],v[48:51],v[40:43]
s_waitcnt lgkmcnt(3)
v_mfma_f32_16x16x32_bf16 v[28:31],v[232:235],v[52:55],v[28:31]
ds_read_b128 v[48:51],v2 offset:1024
v_mfma_f32_16x16x32_bf16 v[32:35],v[236:239],v[52:55],v[32:35]
v_mfma_f32_16x16x32_bf16 v[36:39],v[240:243],v[52:55],v[36:39]
v_mfma_f32_16x16x32_bf16 v[40:43],v[84:87],v[52:55],v[40:43]
s_waitcnt lgkmcnt(2)
v_mfma_f32_16x16x32_bf16 v[28:31],v[244:247],v[56:59],v[28:31]
ds_read_b128 v[52:55],v2 offset:2048
v_mfma_f32_16x16x32_bf16 v[32:35],v[248:251],v[56:59],v[32:35]
v_mfma_f32_16x16x32_bf16 v[36:39],v[252:255],v[56:59],v[36:39]
s_waitcnt lgkmcnt(2)
v_mfma_f32_16x16x32_bf16 v[40:43],v[88:91],v[56:59],v[40:43]
s_cbranch_scc1 Lg4_d0w2_%=
s_branch Lg4_slow0_%=
Lg4_d0w3_%=:
v_mfma_f32_16x16x32_bf16 v[12:15],a[0:3],v[44:47],0
s_max_u32 s73,s47,s65
ds_read_b128 v[56:59],v2 offset:3072
v_lshlrev_b32 v60,16,v68
v_mfma_f32_16x16x32_bf16 v[16:19],a[4:7],v[44:47],0
s_mov_b32 s65,0
v_and_b32 v61,0xffff0000,v68
v_lshlrev_b32 v62,16,v69
v_mfma_f32_16x16x32_bf16 v[20:23],a[8:11],v[44:47],0
s_add_u32 s53,s52,s55
v_and_b32 v63,0xffff0000,v69
v_lshlrev_b32 v64,16,v70
v_mfma_f32_16x16x32_bf16 v[24:27],a[12:15],v[44:47],0
s_waitcnt lgkmcnt(2)
v_and_b32 v65,0xffff0000,v70
v_lshlrev_b32 v66,16,v71
v_mfma_f32_16x16x32_bf16 v[12:15],a[16:19],v[48:51],v[12:15]
s_cmp_eq_u32 s53,s56
ds_read_b128 v[44:47],v2 offset:4096
v_and_b32 v67,0xffff0000,v71
v_mfma_f32_16x16x32_bf16 v[16:19],a[20:23],v[48:51],v[16:19]
s_cselect_b32 s53,s57,s53
v_cvt_pk_bf16_f32 v28,v28,v29
v_cvt_pk_bf16_f32 v29,v30,v31
v_mfma_f32_16x16x32_bf16 v[20:23],a[24:27],v[48:51],v[20:23]
s_cmp_eq_u32 s53,s58
v_cvt_pk_bf16_f32 v30,v32,v33
v_cvt_pk_bf16_f32 v31,v34,v35
v_mfma_f32_16x16x32_bf16 v[24:27],a[28:31],v[48:51],v[24:27]
s_waitcnt lgkmcnt(2)
v_add_f32 v36,v36,v60
v_add_f32 v37,v37,v61
v_mfma_f32_16x16x32_bf16 v[12:15],a[32:35],v[52:55],v[12:15]
s_cselect_b32 s53,s59,s53
ds_read_b128 v[48:51],v2 offset:5120
v_add_f32 v38,v38,v62
v_mfma_f32_16x16x32_bf16 v[16:19],a[36:39],v[52:55],v[16:19]
v_add_f32 v39,v39,v63
v_add_f32 v40,v40,v64
v_mfma_f32_16x16x32_bf16 v[20:23],a[40:43],v[52:55],v[20:23]
v_add_f32 v41,v41,v65
v_add_f32 v42,v42,v66
v_mfma_f32_16x16x32_bf16 v[24:27],a[44:47],v[52:55],v[24:27]
s_waitcnt lgkmcnt(2)
buffer_store_dwordx4 v[28:31],v3,s[36:39],s73 offen offset:0 sc0 sc1
v_add_f32 v43,v43,v67
v_cvt_pk_bf16_f32 v36,v36,v37
v_cvt_pk_bf16_f32 v37,v38,v39
v_mfma_f32_16x16x32_bf16 v[12:15],a[48:51],v[56:59],v[12:15]
ds_read_b128 v[52:55],v2 offset:6144
v_cvt_pk_bf16_f32 v38,v40,v41
v_mfma_f32_16x16x32_bf16 v[16:19],a[52:55],v[56:59],v[16:19]
v_cvt_pk_bf16_f32 v39,v42,v43
v_mfma_f32_16x16x32_bf16 v[20:23],a[56:59],v[56:59],v[20:23]
s_waitcnt lgkmcnt(2)
s_nop 0
v_mfma_f32_16x16x32_bf16 v[24:27],a[60:63],v[56:59],v[24:27]
s_barrier
v_mfma_f32_16x16x32_bf16 v[12:15],a[64:67],v[44:47],v[12:15]
ds_read_b128 v[56:59],v2 offset:7168
ds_read_b32 v11,v8 offset:4096
v_mfma_f32_16x16x32_bf16 v[16:19],a[68:71],v[44:47],v[16:19]
v_mfma_f32_16x16x32_bf16 v[20:23],a[72:75],v[44:47],v[20:23]
v_mfma_f32_16x16x32_bf16 v[24:27],a[76:79],v[44:47],v[24:27]
s_waitcnt lgkmcnt(3)
v_mfma_f32_16x16x32_bf16 v[12:15],a[80:83],v[48:51],v[12:15]
s_waitcnt lgkmcnt(0)
ds_read_b128 v[44:47],v2 offset:8192
v_mfma_f32_16x16x32_bf16 v[16:19],a[84:87],v[48:51],v[16:19])"
R"(
v_mfma_f32_16x16x32_bf16 v[20:23],a[88:91],v[48:51],v[20:23]
v_mfma_f32_16x16x32_bf16 v[24:27],a[92:95],v[48:51],v[24:27]
buffer_store_dwordx4 v[36:39],v4,s[36:39],s73 offen offset:0 sc0 sc1
v_cmp_eq_u32 vcc,s60,v11
v_mfma_f32_16x16x32_bf16 v[12:15],a[96:99],v[52:55],v[12:15]
s_cmp_eq_u32 vcc_lo,-1
ds_read_b128 v[48:51],v2 offset:9216
s_cselect_b32 s66,s49,s59
v_mfma_f32_16x16x32_bf16 v[16:19],a[100:103],v[52:55],v[16:19]
s_mul_i32 s61,s66,7
s_lshr_b32 s61,s61,1
s_add_u32 m0,s63,28672
v_mfma_f32_16x16x32_bf16 v[20:23],a[104:107],v[52:55],v[20:23]
v_mfma_f32_16x16x32_bf16 v[24:27],a[108:111],v[52:55],v[24:27]
buffer_load_dwordx4 v0,s[36:39],s61 offen offset:0 sc1 lds
v_mfma_f32_16x16x32_bf16 v[12:15],a[112:115],v[56:59],v[12:15]
ds_read_b128 v[52:55],v2 offset:10240
v_mfma_f32_16x16x32_bf16 v[16:19],a[116:119],v[56:59],v[16:19]
v_mfma_f32_16x16x32_bf16 v[20:23],a[120:123],v[56:59],v[20:23]
v_mfma_f32_16x16x32_bf16 v[24:27],a[124:127],v[56:59],v[24:27]
s_waitcnt lgkmcnt(2)
buffer_load_dwordx4 v0,s[36:39],s61 offen offset:1024 sc1 lds
v_mfma_f32_16x16x32_bf16 v[12:15],a[128:131],v[44:47],v[12:15]
ds_read_b128 v[56:59],v2 offset:11264
v_mfma_f32_16x16x32_bf16 v[16:19],a[132:135],v[44:47],v[16:19]
v_mfma_f32_16x16x32_bf16 v[20:23],a[136:139],v[44:47],v[20:23]
v_mfma_f32_16x16x32_bf16 v[24:27],a[140:143],v[44:47],v[24:27]
s_waitcnt lgkmcnt(2)
buffer_load_dwordx4 v0,s[36:39],s61 offen offset:2048 sc1 lds
v_mfma_f32_16x16x32_bf16 v[12:15],a[144:147],v[48:51],v[12:15]
ds_read_b128 v[44:47],v2 offset:12288
v_mfma_f32_16x16x32_bf16 v[16:19],a[148:151],v[48:51],v[16:19]
v_mfma_f32_16x16x32_bf16 v[20:23],a[152:155],v[48:51],v[20:23]
v_mfma_f32_16x16x32_bf16 v[24:27],a[156:159],v[48:51],v[24:27]
s_waitcnt lgkmcnt(2)
buffer_load_dwordx4 v0,s[36:39],s61 offen offset:3072 sc1 lds
v_mfma_f32_16x16x32_bf16 v[12:15],a[160:163],v[52:55],v[12:15]
s_add_u32 m0,s64,28672
ds_read_b128 v[48:51],v2 offset:13312
s_lshr_b32 s70,s47,4
v_mfma_f32_16x16x32_bf16 v[16:19],a[164:167],v[52:55],v[16:19]
s_cmp_ge_u32 s66,s59
v_mfma_f32_16x16x32_bf16 v[20:23],a[168:171],v[52:55],v[20:23]
v_mfma_f32_16x16x32_bf16 v[24:27],a[172:175],v[52:55],v[24:27]
s_waitcnt lgkmcnt(2)
buffer_load_dwordx4 v[68:71],v9,s[36:39],s48 offen offset:0 sc1
v_mfma_f32_16x16x32_bf16 v[12:15],a[176:179],v[56:59],v[12:15]
ds_read_b128 v[52:55],v2 offset:14336
v_mfma_f32_16x16x32_bf16 v[16:19],a[180:183],v[56:59],v[16:19]
v_mfma_f32_16x16x32_bf16 v[20:23],a[184:187],v[56:59],v[20:23]
v_mfma_f32_16x16x32_bf16 v[24:27],a[188:191],v[56:59],v[24:27]
s_waitcnt lgkmcnt(2)
v_mfma_f32_16x16x32_bf16 v[12:15],a[192:195],v[44:47],v[12:15]
ds_read_b128 v[56:59],v2 offset:15360
v_mfma_f32_16x16x32_bf16 v[16:19],a[196:199],v[44:47],v[16:19]
v_mfma_f32_16x16x32_bf16 v[20:23],a[200:203],v[44:47],v[20:23]
s_waitcnt vmcnt(8) lgkmcnt(2)
v_mfma_f32_16x16x32_bf16 v[24:27],a[204:207],v[44:47],v[24:27]
s_barrier
v_mfma_f32_16x16x32_bf16 v[12:15],a[208:211],v[48:51],v[12:15]
ds_read_b128 v[44:47],v2 offset:16384
v_mfma_f32_16x16x32_bf16 v[16:19],a[212:215],v[48:51],v[16:19]
v_mfma_f32_16x16x32_bf16 v[20:23],a[216:219],v[48:51],v[20:23]
v_mfma_f32_16x16x32_bf16 v[24:27],a[220:223],v[48:51],v[24:27]
s_waitcnt lgkmcnt(2)
v_mfma_f32_16x16x32_bf16 v[12:15],a[224:227],v[52:55],v[12:15]
buffer_load_dwordx4 v1,s[36:39],s61 offen offset:0 sc1 lds
v_mfma_f32_16x16x32_bf16 v[16:19],a[228:231],v[52:55],v[16:19]
ds_read_b128 v[48:51],v2 offset:17408
v_mfma_f32_16x16x32_bf16 v[20:23],a[232:235],v[52:55],v[20:23]
v_mfma_f32_16x16x32_bf16 v[24:27],a[236:239],v[52:55],v[24:27]
s_waitcnt lgkmcnt(2)
v_mfma_f32_16x16x32_bf16 v[12:15],a[240:243],v[56:59],v[12:15]
ds_read_b128 v[52:55],v2 offset:18432
v_mfma_f32_16x16x32_bf16 v[16:19],a[244:247],v[56:59],v[16:19]
v_mfma_f32_16x16x32_bf16 v[20:23],a[248:251],v[56:59],v[20:23]
buffer_load_dwordx4 v1,s[36:39],s61 offen offset:1024 sc1 lds
v_mfma_f32_16x16x32_bf16 v[24:27],a[252:255],v[56:59],v[24:27]
s_waitcnt lgkmcnt(2)
v_mfma_f32_16x16x32_bf16 v[12:15],v[100:103],v[44:47],v[12:15]
ds_read_b128 v[56:59],v2 offset:19456
v_mfma_f32_16x16x32_bf16 v[16:19],v[104:107],v[44:47],v[16:19]
ds_read_b128 v[88:91],v10 offset:0
v_mfma_f32_16x16x32_bf16 v[20:23],v[108:111],v[44:47],v[20:23]
v_mfma_f32_16x16x32_bf16 v[24:27],v[112:115],v[44:47],v[24:27]
s_waitcnt lgkmcnt(3)
v_mfma_f32_16x16x32_bf16 v[12:15],v[116:119],v[48:51],v[12:15]
buffer_load_dwordx4 v1,s[36:39],s61 offen offset:2048 sc1 lds
v_mfma_f32_16x16x32_bf16 v[16:19],v[120:123],v[48:51],v[16:19]
ds_read_b128 v[44:47],v2 offset:20480
ds_read_b128 v[76:79],v10 offset:1024
v_mfma_f32_16x16x32_bf16 v[20:23],v[124:127],v[48:51],v[20:23]
v_mfma_f32_16x16x32_bf16 v[24:27],v[128:131],v[48:51],v[24:27]
s_waitcnt lgkmcnt(4)
v_mfma_f32_16x16x32_bf16 v[12:15],v[132:135],v[52:55],v[12:15]
ds_read_b128 v[48:51],v2 offset:21504
v_mfma_f32_16x16x32_bf16 v[16:19],v[136:139],v[52:55],v[16:19]
ds_read_b128 v[80:83],v10 offset:2048
v_mfma_f32_16x16x32_bf16 v[20:23],v[140:143],v[52:55],v[20:23]
v_mfma_f32_16x16x32_bf16 v[24:27],v[144:147],v[52:55],v[24:27]
s_waitcnt lgkmcnt(4)
v_mfma_f32_16x16x32_bf16 v[12:15],v[148:151],v[56:59],v[12:15]
ds_read_b128 v[52:55],v2 offset:22528
v_mfma_f32_16x16x32_bf16 v[16:19],v[152:155],v[56:59],v[16:19]
ds_read_b128 v[84:87],v10 offset:3072
v_mfma_f32_16x16x32_bf16 v[20:23],v[156:159],v[56:59],v[20:23]
v_mfma_f32_16x16x32_bf16 v[24:27],v[88:91],v[56:59],v[24:27]
s_waitcnt lgkmcnt(4)
v_mfma_f32_16x16x32_bf16 v[12:15],v[160:163],v[44:47],v[12:15]
ds_read_b128 v[56:59],v2 offset:23552
v_mfma_f32_16x16x32_bf16 v[16:19],v[164:167],v[44:47],v[16:19]
ds_read_b128 v[88:91],v10 offset:4096
v_mfma_f32_16x16x32_bf16 v[20:23],v[168:171],v[44:47],v[20:23]
v_mfma_f32_16x16x32_bf16 v[24:27],v[76:79],v[44:47],v[24:27]
s_waitcnt lgkmcnt(4)
v_mfma_f32_16x16x32_bf16 v[12:15],v[172:175],v[48:51],v[12:15]
ds_read_b128 v[44:47],v2 offset:24576
v_mfma_f32_16x16x32_bf16 v[16:19],v[176:179],v[48:51],v[16:19]
ds_read_b128 v[76:79],v10 offset:5120
v_mfma_f32_16x16x32_bf16 v[20:23],v[180:183],v[48:51],v[20:23]
s_waitcnt vmcnt(8) lgkmcnt(4)
v_mfma_f32_16x16x32_bf16 v[24:27],v[80:83],v[48:51],v[24:27]
s_barrier
v_mfma_f32_16x16x32_bf16 v[12:15],v[184:187],v[52:55],v[12:15]
buffer_store_dword v6,v5,s[36:39],s70 offen offset:0 sc0 sc1
ds_read_b128 v[48:51],v2 offset:25600
v_mfma_f32_16x16x32_bf16 v[16:19],v[188:191],v[52:55],v[16:19]
ds_read_b128 v[80:83],v10 offset:6144
v_mfma_f32_16x16x32_bf16 v[20:23],v[192:195],v[52:55],v[20:23]
v_mfma_f32_16x16x32_bf16 v[24:27],v[84:87],v[52:55],v[24:27]
s_waitcnt lgkmcnt(4)
v_mfma_f32_16x16x32_bf16 v[12:15],v[196:199],v[56:59],v[12:15]
ds_read_b128 v[52:55],v2 offset:26624
v_mfma_f32_16x16x32_bf16 v[16:19],v[200:203],v[56:59],v[16:19]
ds_read_b128 v[84:87],v10 offset:7168
v_mfma_f32_16x16x32_bf16 v[20:23],v[204:207],v[56:59],v[20:23]
v_mfma_f32_16x16x32_bf16 v[24:27],v[88:91],v[56:59],v[24:27]
s_waitcnt lgkmcnt(4)
v_mfma_f32_16x16x32_bf16 v[12:15],v[208:211],v[44:47],v[12:15]
ds_read_b128 v[56:59],v2 offset:27648
v_mfma_f32_16x16x32_bf16 v[16:19],v[212:215],v[44:47],v[16:19]
ds_read_b128 v[88:91],v10 offset:8192
v_mfma_f32_16x16x32_bf16 v[20:23],v[216:219],v[44:47],v[20:23]
s_waitcnt vmcnt(4) lgkmcnt(4)
v_mfma_f32_16x16x32_bf16 v[24:27],v[76:79],v[44:47],v[24:27]
s_barrier
v_mfma_f32_16x16x32_bf16 v[12:15],v[220:223],v[48:51],v[12:15]
ds_read_b128 v[44:47],v2 offset:28672
v_mfma_f32_16x16x32_bf16 v[16:19],v[224:227],v[48:51],v[16:19]
v_mfma_f32_16x16x32_bf16 v[20:23],v[228:231],v[48:51],v[20:23]
v_mfma_f32_16x16x32_bf16 v[24:27],v[80:83],v[48:51],v[24:27]
s_waitcnt lgkmcnt(3)
v_mfma_f32_16x16x32_bf16 v[12:15],v[232:235],v[52:55],v[12:15]
ds_read_b128 v[48:51],v2 offset:29696
v_mfma_f32_16x16x32_bf16 v[16:19],v[236:239],v[52:55],v[16:19]
v_mfma_f32_16x16x32_bf16 v[20:23],v[240:243],v[52:55],v[20:23]
v_mfma_f32_16x16x32_bf16 v[24:27],v[84:87],v[52:55],v[24:27]
s_waitcnt lgkmcnt(2)
v_mfma_f32_16x16x32_bf16 v[12:15],v[244:247],v[56:59],v[12:15]
ds_read_b128 v[52:55],v2 offset:30720
v_mfma_f32_16x16x32_bf16 v[16:19],v[248:251],v[56:59],v[16:19]
v_mfma_f32_16x16x32_bf16 v[20:23],v[252:255],v[56:59],v[20:23]
s_waitcnt lgkmcnt(2)
v_mfma_f32_16x16x32_bf16 v[24:27],v[88:91],v[56:59],v[24:27]
s_cbranch_scc1 Lg4_slow1_%=
Lg4_d1w3_%=:
v_mfma_f32_16x16x32_bf16 v[28:31],a[0:3],v[44:47],0
s_max_u32 s73,s48,s65
ds_read_b128 v[56:59],v2 offset:31744
v_lshlrev_b32 v60,16,v68
v_mfma_f32_16x16x32_bf16 v[32:35],a[4:7],v[44:47],0
s_mov_b32 s65,0
v_and_b32 v61,0xffff0000,v68
v_lshlrev_b32 v62,16,v69
v_mfma_f32_16x16x32_bf16 v[36:39],a[8:11],v[44:47],0
s_add_u32 s74,s53,s55
v_and_b32 v63,0xffff0000,v69
v_lshlrev_b32 v64,16,v70
v_mfma_f32_16x16x32_bf16 v[40:43],a[12:15],v[44:47],0
s_waitcnt lgkmcnt(2)
v_and_b32 v65,0xffff0000,v70
v_lshlrev_b32 v66,16,v71
v_mfma_f32_16x16x32_bf16 v[28:31],a[16:19],v[48:51],v[28:31]
s_cmp_eq_u32 s74,s56
ds_read_b128 v[44:47],v2 offset:32768
v_and_b32 v67,0xffff0000,v71
v_mfma_f32_16x16x32_bf16 v[32:35],a[20:23],v[48:51],v[32:35]
s_cselect_b32 s74,s57,s74
v_cvt_pk_bf16_f32 v12,v12,v13
v_cvt_pk_bf16_f32 v13,v14,v15
v_mfma_f32_16x16x32_bf16 v[36:39],a[24:27],v[48:51],v[36:39]
s_cmp_eq_u32 s74,s58
v_cvt_pk_bf16_f32 v14,v16,v17
v_cvt_pk_bf16_f32 v15,v18,v19
v_mfma_f32_16x16x32_bf16 v[40:43],a[28:31],v[48:51],v[40:43]
s_waitcnt lgkmcnt(2)
v_add_f32 v20,v20,v60
v_add_f32 v21,v21,v61
v_mfma_f32_16x16x32_bf16 v[28:31],a[32:35],v[52:55],v[28:31]
s_cselect_b32 s74,s59,s74)"
R"(
ds_read_b128 v[48:51],v2 offset:33792
v_add_f32 v22,v22,v62
v_mfma_f32_16x16x32_bf16 v[32:35],a[36:39],v[52:55],v[32:35]
s_mov_b64 s[44:45],s[46:47]
v_add_f32 v23,v23,v63
v_add_f32 v24,v24,v64
v_mfma_f32_16x16x32_bf16 v[36:39],a[40:43],v[52:55],v[36:39]
s_mov_b64 s[46:47],s[48:49]
v_add_f32 v25,v25,v65
v_add_f32 v26,v26,v66
v_mfma_f32_16x16x32_bf16 v[40:43],a[44:47],v[52:55],v[40:43]
s_waitcnt lgkmcnt(2)
buffer_store_dwordx4 v[12:15],v3,s[36:39],s73 offen offset:0 sc0 sc1
v_add_f32 v27,v27,v67
v_cvt_pk_bf16_f32 v20,v20,v21
v_cvt_pk_bf16_f32 v21,v22,v23
v_mfma_f32_16x16x32_bf16 v[28:31],a[48:51],v[56:59],v[28:31]
ds_read_b128 v[52:55],v2 offset:34816
v_cvt_pk_bf16_f32 v22,v24,v25
v_mfma_f32_16x16x32_bf16 v[32:35],a[52:55],v[56:59],v[32:35]
v_cvt_pk_bf16_f32 v23,v26,v27
v_mfma_f32_16x16x32_bf16 v[36:39],a[56:59],v[56:59],v[36:39]
s_waitcnt lgkmcnt(2)
s_nop 0
v_mfma_f32_16x16x32_bf16 v[40:43],a[60:63],v[56:59],v[40:43]
s_barrier
v_mfma_f32_16x16x32_bf16 v[28:31],a[64:67],v[44:47],v[28:31]
ds_read_b128 v[56:59],v2 offset:35840
ds_read_b32 v11,v8 offset:5120
v_mfma_f32_16x16x32_bf16 v[32:35],a[68:71],v[44:47],v[32:35]
v_mfma_f32_16x16x32_bf16 v[36:39],a[72:75],v[44:47],v[36:39]
v_mfma_f32_16x16x32_bf16 v[40:43],a[76:79],v[44:47],v[40:43]
s_waitcnt lgkmcnt(3)
v_mfma_f32_16x16x32_bf16 v[28:31],a[80:83],v[48:51],v[28:31]
s_waitcnt lgkmcnt(0)
ds_read_b128 v[44:47],v2 offset:36864
v_mfma_f32_16x16x32_bf16 v[32:35],a[84:87],v[48:51],v[32:35]
v_mfma_f32_16x16x32_bf16 v[36:39],a[88:91],v[48:51],v[36:39]
v_mfma_f32_16x16x32_bf16 v[40:43],a[92:95],v[48:51],v[40:43]
buffer_store_dwordx4 v[20:23],v4,s[36:39],s73 offen offset:0 sc0 sc1
v_cmp_eq_u32 vcc,s60,v11
v_mfma_f32_16x16x32_bf16 v[28:31],a[96:99],v[52:55],v[28:31]
s_cmp_eq_u32 vcc_lo,-1
ds_read_b128 v[48:51],v2 offset:37888
s_cselect_b32 s67,s50,s59
v_mfma_f32_16x16x32_bf16 v[32:35],a[100:103],v[52:55],v[32:35]
s_mul_i32 s61,s67,7
s_lshr_b32 s61,s61,1
s_add_u32 m0,s63,0
v_mfma_f32_16x16x32_bf16 v[36:39],a[104:107],v[52:55],v[36:39]
v_mfma_f32_16x16x32_bf16 v[40:43],a[108:111],v[52:55],v[40:43]
buffer_load_dwordx4 v0,s[36:39],s61 offen offset:0 sc1 lds
v_mfma_f32_16x16x32_bf16 v[28:31],a[112:115],v[56:59],v[28:31]
ds_read_b128 v[52:55],v2 offset:38912
v_mfma_f32_16x16x32_bf16 v[32:35],a[116:119],v[56:59],v[32:35]
v_mfma_f32_16x16x32_bf16 v[36:39],a[120:123],v[56:59],v[36:39]
v_mfma_f32_16x16x32_bf16 v[40:43],a[124:127],v[56:59],v[40:43]
s_waitcnt lgkmcnt(2)
buffer_load_dwordx4 v0,s[36:39],s61 offen offset:1024 sc1 lds
v_mfma_f32_16x16x32_bf16 v[28:31],a[128:131],v[44:47],v[28:31]
ds_read_b128 v[56:59],v2 offset:39936
v_mfma_f32_16x16x32_bf16 v[32:35],a[132:135],v[44:47],v[32:35]
v_mfma_f32_16x16x32_bf16 v[36:39],a[136:139],v[44:47],v[36:39]
v_mfma_f32_16x16x32_bf16 v[40:43],a[140:143],v[44:47],v[40:43]
s_waitcnt lgkmcnt(2)
buffer_load_dwordx4 v0,s[36:39],s61 offen offset:2048 sc1 lds
v_mfma_f32_16x16x32_bf16 v[28:31],a[144:147],v[48:51],v[28:31]
ds_read_b128 v[44:47],v2 offset:40960
v_mfma_f32_16x16x32_bf16 v[32:35],a[148:151],v[48:51],v[32:35]
v_mfma_f32_16x16x32_bf16 v[36:39],a[152:155],v[48:51],v[36:39]
v_mfma_f32_16x16x32_bf16 v[40:43],a[156:159],v[48:51],v[40:43]
s_waitcnt lgkmcnt(2)
buffer_load_dwordx4 v0,s[36:39],s61 offen offset:3072 sc1 lds
v_mfma_f32_16x16x32_bf16 v[28:31],a[160:163],v[52:55],v[28:31]
s_add_u32 m0,s64,0
ds_read_b128 v[48:51],v2 offset:41984
s_lshr_b32 s70,s48,4
v_mfma_f32_16x16x32_bf16 v[32:35],a[164:167],v[52:55],v[32:35]
s_cmp_lt_u32 s67,s59
v_mfma_f32_16x16x32_bf16 v[36:39],a[168:171],v[52:55],v[36:39]
v_mfma_f32_16x16x32_bf16 v[40:43],a[172:175],v[52:55],v[40:43]
s_waitcnt lgkmcnt(2)
buffer_load_dwordx4 v[68:71],v9,s[36:39],s49 offen offset:0 sc1
v_mfma_f32_16x16x32_bf16 v[28:31],a[176:179],v[56:59],v[28:31]
s_mov_b64 s[48:49],s[50:51]
ds_read_b128 v[52:55],v2 offset:43008
s_mov_b64 s[50:51],s[52:53]
v_mfma_f32_16x16x32_bf16 v[32:35],a[180:183],v[56:59],v[32:35]
s_mov_b32 s52,s74
v_mfma_f32_16x16x32_bf16 v[36:39],a[184:187],v[56:59],v[36:39]
v_mfma_f32_16x16x32_bf16 v[40:43],a[188:191],v[56:59],v[40:43]
s_waitcnt lgkmcnt(2)
v_mfma_f32_16x16x32_bf16 v[28:31],a[192:195],v[44:47],v[28:31]
ds_read_b128 v[56:59],v2 offset:44032
v_mfma_f32_16x16x32_bf16 v[32:35],a[196:199],v[44:47],v[32:35]
v_mfma_f32_16x16x32_bf16 v[36:39],a[200:203],v[44:47],v[36:39]
s_waitcnt vmcnt(8) lgkmcnt(2)
v_mfma_f32_16x16x32_bf16 v[40:43],a[204:207],v[44:47],v[40:43]
s_barrier
v_mfma_f32_16x16x32_bf16 v[28:31],a[208:211],v[48:51],v[28:31]
ds_read_b128 v[44:47],v2 offset:45056
v_mfma_f32_16x16x32_bf16 v[32:35],a[212:215],v[48:51],v[32:35]
v_mfma_f32_16x16x32_bf16 v[36:39],a[216:219],v[48:51],v[36:39]
v_mfma_f32_16x16x32_bf16 v[40:43],a[220:223],v[48:51],v[40:43]
s_waitcnt lgkmcnt(2)
v_mfma_f32_16x16x32_bf16 v[28:31],a[224:227],v[52:55],v[28:31]
buffer_load_dwordx4 v1,s[36:39],s61 offen offset:0 sc1 lds
v_mfma_f32_16x16x32_bf16 v[32:35],a[228:231],v[52:55],v[32:35]
ds_read_b128 v[48:51],v2 offset:46080
v_mfma_f32_16x16x32_bf16 v[36:39],a[232:235],v[52:55],v[36:39]
v_mfma_f32_16x16x32_bf16 v[40:43],a[236:239],v[52:55],v[40:43]
s_waitcnt lgkmcnt(2)
v_mfma_f32_16x16x32_bf16 v[28:31],a[240:243],v[56:59],v[28:31]
ds_read_b128 v[52:55],v2 offset:47104
v_mfma_f32_16x16x32_bf16 v[32:35],a[244:247],v[56:59],v[32:35]
v_mfma_f32_16x16x32_bf16 v[36:39],a[248:251],v[56:59],v[36:39]
buffer_load_dwordx4 v1,s[36:39],s61 offen offset:1024 sc1 lds
v_mfma_f32_16x16x32_bf16 v[40:43],a[252:255],v[56:59],v[40:43]
s_waitcnt lgkmcnt(2)
v_mfma_f32_16x16x32_bf16 v[28:31],v[100:103],v[44:47],v[28:31]
ds_read_b128 v[56:59],v2 offset:48128
v_mfma_f32_16x16x32_bf16 v[32:35],v[104:107],v[44:47],v[32:35]
ds_read_b128 v[88:91],v10 offset:0
v_mfma_f32_16x16x32_bf16 v[36:39],v[108:111],v[44:47],v[36:39]
v_mfma_f32_16x16x32_bf16 v[40:43],v[112:115],v[44:47],v[40:43]
s_waitcnt lgkmcnt(3)
v_mfma_f32_16x16x32_bf16 v[28:31],v[116:119],v[48:51],v[28:31]
buffer_load_dwordx4 v1,s[36:39],s61 offen offset:2048 sc1 lds
v_mfma_f32_16x16x32_bf16 v[32:35],v[120:123],v[48:51],v[32:35]
ds_read_b128 v[44:47],v2 offset:49152
ds_read_b128 v[76:79],v10 offset:1024
v_mfma_f32_16x16x32_bf16 v[36:39],v[124:127],v[48:51],v[36:39]
v_mfma_f32_16x16x32_bf16 v[40:43],v[128:131],v[48:51],v[40:43]
s_waitcnt lgkmcnt(4)
v_mfma_f32_16x16x32_bf16 v[28:31],v[132:135],v[52:55],v[28:31]
ds_read_b128 v[48:51],v2 offset:50176
v_mfma_f32_16x16x32_bf16 v[32:35],v[136:139],v[52:55],v[32:35]
ds_read_b128 v[80:83],v10 offset:2048
v_mfma_f32_16x16x32_bf16 v[36:39],v[140:143],v[52:55],v[36:39]
v_mfma_f32_16x16x32_bf16 v[40:43],v[144:147],v[52:55],v[40:43]
s_waitcnt lgkmcnt(4)
v_mfma_f32_16x16x32_bf16 v[28:31],v[148:151],v[56:59],v[28:31]
ds_read_b128 v[52:55],v2 offset:51200
v_mfma_f32_16x16x32_bf16 v[32:35],v[152:155],v[56:59],v[32:35]
ds_read_b128 v[84:87],v10 offset:3072
v_mfma_f32_16x16x32_bf16 v[36:39],v[156:159],v[56:59],v[36:39]
v_mfma_f32_16x16x32_bf16 v[40:43],v[88:91],v[56:59],v[40:43]
s_waitcnt lgkmcnt(4)
v_mfma_f32_16x16x32_bf16 v[28:31],v[160:163],v[44:47],v[28:31]
ds_read_b128 v[56:59],v2 offset:52224
v_mfma_f32_16x16x32_bf16 v[32:35],v[164:167],v[44:47],v[32:35]
ds_read_b128 v[88:91],v10 offset:4096
v_mfma_f32_16x16x32_bf16 v[36:39],v[168:171],v[44:47],v[36:39]
v_mfma_f32_16x16x32_bf16 v[40:43],v[76:79],v[44:47],v[40:43]
s_waitcnt lgkmcnt(4)
v_mfma_f32_16x16x32_bf16 v[28:31],v[172:175],v[48:51],v[28:31]
ds_read_b128 v[44:47],v2 offset:53248
v_mfma_f32_16x16x32_bf16 v[32:35],v[176:179],v[48:51],v[32:35]
ds_read_b128 v[76:79],v10 offset:5120
v_mfma_f32_16x16x32_bf16 v[36:39],v[180:183],v[48:51],v[36:39]
s_waitcnt vmcnt(8) lgkmcnt(4)
v_mfma_f32_16x16x32_bf16 v[40:43],v[80:83],v[48:51],v[40:43]
s_barrier
v_mfma_f32_16x16x32_bf16 v[28:31],v[184:187],v[52:55],v[28:31]
buffer_store_dword v6,v5,s[36:39],s70 offen offset:0 sc0 sc1
ds_read_b128 v[48:51],v2 offset:54272
v_mfma_f32_16x16x32_bf16 v[32:35],v[188:191],v[52:55],v[32:35]
ds_read_b128 v[80:83],v10 offset:6144
v_mfma_f32_16x16x32_bf16 v[36:39],v[192:195],v[52:55],v[36:39]
v_mfma_f32_16x16x32_bf16 v[40:43],v[84:87],v[52:55],v[40:43]
s_waitcnt lgkmcnt(4)
v_mfma_f32_16x16x32_bf16 v[28:31],v[196:199],v[56:59],v[28:31]
ds_read_b128 v[52:55],v2 offset:55296
v_mfma_f32_16x16x32_bf16 v[32:35],v[200:203],v[56:59],v[32:35]
ds_read_b128 v[84:87],v10 offset:7168
v_mfma_f32_16x16x32_bf16 v[36:39],v[204:207],v[56:59],v[36:39]
v_mfma_f32_16x16x32_bf16 v[40:43],v[88:91],v[56:59],v[40:43]
s_waitcnt lgkmcnt(4)
v_mfma_f32_16x16x32_bf16 v[28:31],v[208:211],v[44:47],v[28:31]
ds_read_b128 v[56:59],v2 offset:56320
v_mfma_f32_16x16x32_bf16 v[32:35],v[212:215],v[44:47],v[32:35]
ds_read_b128 v[88:91],v10 offset:8192
v_mfma_f32_16x16x32_bf16 v[36:39],v[216:219],v[44:47],v[36:39]
s_waitcnt vmcnt(4) lgkmcnt(4)
v_mfma_f32_16x16x32_bf16 v[40:43],v[76:79],v[44:47],v[40:43]
s_barrier
v_mfma_f32_16x16x32_bf16 v[28:31],v[220:223],v[48:51],v[28:31]
ds_read_b128 v[44:47],v2 offset:0
v_mfma_f32_16x16x32_bf16 v[32:35],v[224:227],v[48:51],v[32:35]
v_mfma_f32_16x16x32_bf16 v[36:39],v[228:231],v[48:51],v[36:39]
v_mfma_f32_16x16x32_bf16 v[40:43],v[80:83],v[48:51],v[40:43]
s_waitcnt lgkmcnt(3)
v_mfma_f32_16x16x32_bf16 v[28:31],v[232:235],v[52:55],v[28:31]
ds_read_b128 v[48:51],v2 offset:1024
v_mfma_f32_16x16x32_bf16 v[32:35],v[236:239],v[52:55],v[32:35]
v_mfma_f32_16x16x32_bf16 v[36:39],v[240:243],v[52:55],v[36:39]
v_mfma_f32_16x16x32_bf16 v[40:43],v[84:87],v[52:55],v[40:43]
s_waitcnt lgkmcnt(2)
v_mfma_f32_16x16x32_bf16 v[28:31],v[244:247],v[56:59],v[28:31]
ds_read_b128 v[52:55],v2 offset:2048)"
R"(
v_mfma_f32_16x16x32_bf16 v[32:35],v[248:251],v[56:59],v[32:35]
v_mfma_f32_16x16x32_bf16 v[36:39],v[252:255],v[56:59],v[36:39]
s_waitcnt lgkmcnt(2)
v_mfma_f32_16x16x32_bf16 v[40:43],v[88:91],v[56:59],v[40:43]
s_cbranch_scc1 Lg4_d0w3_%=
s_branch Lg4_slow0_%=
Lg4_slow0_%=:
ds_read_b32 v11,v8 offset:6144
s_waitcnt lgkmcnt(0)
v_cmp_eq_u32 vcc,s60,v11
s_nop 0
s_cmp_eq_u32 vcc_lo,-1
s_cbranch_scc1 Lg4_gs0_%=
Lg4_slowb0_%=:
s_cmp_eq_u32 s54,1
s_cselect_b32 s68,s49,s48
s_mul_i32 s68,s68,5
s_lshr_b32 s68,s68,6
s_add_u32 m0,s78,0
s_nop 0
buffer_load_dword v7,s[36:39],s68 offen offset:0 sc1 lds
s_lshr_b32 s73,s47,0
v_lshlrev_b32 v60,16,v68
v_and_b32 v61,0xffff0000,v68
v_lshlrev_b32 v62,16,v69
v_and_b32 v63,0xffff0000,v69
v_lshlrev_b32 v64,16,v70
v_and_b32 v65,0xffff0000,v70
v_lshlrev_b32 v66,16,v71
v_and_b32 v67,0xffff0000,v71
v_cvt_pk_bf16_f32 v28,v28,v29
v_cvt_pk_bf16_f32 v29,v30,v31
v_cvt_pk_bf16_f32 v30,v32,v33
v_cvt_pk_bf16_f32 v31,v34,v35
s_add_u32 m0,s78,1024
s_nop 0
buffer_load_dword v7,s[36:39],s68 offen offset:0 sc1 lds
buffer_store_dwordx4 v[28:31],v3,s[36:39],s73 offen offset:0 sc0 sc1
v_add_f32 v36,v36,v60
v_add_f32 v37,v37,v61
v_add_f32 v38,v38,v62
v_add_f32 v39,v39,v63
v_add_f32 v40,v40,v64
v_add_f32 v41,v41,v65
v_add_f32 v42,v42,v66
v_add_f32 v43,v43,v67
v_cvt_pk_bf16_f32 v36,v36,v37
v_cvt_pk_bf16_f32 v37,v38,v39
v_cvt_pk_bf16_f32 v38,v40,v41
v_cvt_pk_bf16_f32 v39,v42,v43
buffer_store_dwordx4 v[36:39],v4,s[36:39],s73 offen offset:0 sc0 sc1
s_cmp_ge_u32 s48,s59
s_cbranch_scc1 Lg4_done0_%=
s_add_u32 m0,s78,2048
s_nop 0
buffer_load_dword v7,s[36:39],s68 offen offset:0 sc1 lds
s_waitcnt vmcnt(4)
s_barrier
ds_read_b32 v11,v8 offset:0
s_mov_b32 s69,2304
s_waitcnt lgkmcnt(0)
v_cmp_eq_u32 vcc,s60,v11
s_nop 0
s_cmp_eq_u32 vcc_lo,-1
s_cbranch_scc1 Lg4_go0_%=
s_add_u32 m0,s78,3072
s_nop 0
buffer_load_dword v7,s[36:39],s68 offen offset:0 sc1 lds
s_waitcnt vmcnt(4)
s_barrier
ds_read_b32 v11,v8 offset:1024
s_mov_b32 s69,3328
s_waitcnt lgkmcnt(0)
v_cmp_eq_u32 vcc,s60,v11
s_nop 0
s_cmp_eq_u32 vcc_lo,-1
s_cbranch_scc1 Lg4_go0_%=
s_add_u32 m0,s78,0
s_nop 0
buffer_load_dword v7,s[36:39],s68 offen offset:0 sc1 lds
s_waitcnt vmcnt(2)
s_barrier
ds_read_b32 v11,v8 offset:2048
s_lshr_b32 s70,s47,4
buffer_store_dword v6,v5,s[36:39],s70 offen offset:0 sc0 sc1
s_mov_b32 s47,s59
s_mov_b32 s69,256
s_waitcnt lgkmcnt(0)
v_cmp_eq_u32 vcc,s60,v11
s_nop 0
s_cmp_eq_u32 vcc_lo,-1
s_cbranch_scc1 Lg4_go0_%=
s_add_u32 m0,s78,1024
s_nop 0
buffer_load_dword v7,s[36:39],s68 offen offset:0 sc1 lds
s_waitcnt vmcnt(3)
s_barrier
ds_read_b32 v11,v8 offset:3072
s_mov_b32 s69,1280
s_waitcnt lgkmcnt(0)
v_cmp_eq_u32 vcc,s60,v11
s_nop 0
s_cmp_eq_u32 vcc_lo,-1
s_cbranch_scc1 Lg4_go0_%=
Lg4_poll0_%=:
s_add_u32 m0,s78,2048
s_nop 0
buffer_load_dword v7,s[36:39],s68 offen offset:0 sc1 lds
s_waitcnt vmcnt(2)
s_barrier
ds_read_b32 v11,v8 offset:0
s_mov_b32 s69,2304
s_waitcnt lgkmcnt(0)
v_cmp_eq_u32 vcc,s60,v11
s_nop 0
s_cmp_eq_u32 vcc_lo,-1
s_cbranch_scc1 Lg4_go0_%=
s_sleep 4
s_add_u32 m0,s78,3072
s_nop 0
buffer_load_dword v7,s[36:39],s68 offen offset:0 sc1 lds
s_waitcnt vmcnt(2)
s_barrier
ds_read_b32 v11,v8 offset:1024
s_mov_b32 s69,3328
s_waitcnt lgkmcnt(0)
v_cmp_eq_u32 vcc,s60,v11
s_nop 0
s_cmp_eq_u32 vcc_lo,-1
s_cbranch_scc1 Lg4_go0_%=
s_sleep 4
s_add_u32 m0,s78,0
s_nop 0
buffer_load_dword v7,s[36:39],s68 offen offset:0 sc1 lds
s_waitcnt vmcnt(2)
s_barrier
ds_read_b32 v11,v8 offset:2048
s_mov_b32 s69,256
s_waitcnt lgkmcnt(0)
v_cmp_eq_u32 vcc,s60,v11
s_nop 0
s_cmp_eq_u32 vcc_lo,-1
s_cbranch_scc1 Lg4_go0_%=
s_sleep 4
s_add_u32 m0,s78,1024
s_nop 0
buffer_load_dword v7,s[36:39],s68 offen offset:0 sc1 lds
s_waitcnt vmcnt(2)
s_barrier
ds_read_b32 v11,v8 offset:3072
s_mov_b32 s69,1280
s_waitcnt lgkmcnt(0)
v_cmp_eq_u32 vcc,s60,v11
s_nop 0
s_cmp_eq_u32 vcc_lo,-1
s_cbranch_scc1 Lg4_go0_%=
s_sleep 4
s_add_u32 s79,s79,1
s_cmp_lt_u32 s79,20000
s_cbranch_scc1 Lg4_poll0_%=
Lg4_go0_%=:
s_mul_i32 s61,s48,7
s_lshr_b32 s61,s61,1
s_add_u32 m0,s63,0
s_nop 0
buffer_load_dwordx4 v0,s[36:39],s61 offen offset:0 sc1 lds
buffer_load_dwordx4 v0,s[36:39],s61 offen offset:1024 sc1 lds
buffer_load_dwordx4 v0,s[36:39],s61 offen offset:2048 sc1 lds
buffer_load_dwordx4 v0,s[36:39],s61 offen offset:3072 sc1 lds
s_add_u32 m0,s64,0
s_nop 0
buffer_load_dwordx4 v1,s[36:39],s61 offen offset:0 sc1 lds
buffer_load_dwordx4 v1,s[36:39],s61 offen offset:1024 sc1 lds
buffer_load_dwordx4 v1,s[36:39],s61 offen offset:2048 sc1 lds
s_mul_i32 s68,s49,5
s_lshr_b32 s68,s68,6
s_add_u32 m0,s78,4096
s_nop 0
buffer_load_dword v7,s[36:39],s68 offen offset:0 sc1 lds
s_waitcnt vmcnt(4)
s_barrier
ds_read_b128 v[44:47],v2 offset:0
ds_read_b128 v[48:51],v2 offset:1024
ds_read_b128 v[52:55],v2 offset:2048
s_lshr_b32 s70,s47,4
buffer_store_dword v6,v5,s[36:39],s70 offen offset:0 sc0 sc1
s_mov_b32 s47,s59
s_waitcnt lgkmcnt(2)
s_setpc_b64 s[90:91]
Lg4_gs0_%=:
s_mul_i32 s61,s48,7
s_lshr_b32 s61,s61,1
s_cmp_eq_u32 s54,1
s_cselect_b32 s68,s49,s48
s_mul_i32 s68,s68,5
s_lshr_b32 s68,s68,6
s_lshr_b32 s73,s47,0
s_add_u32 m0,s63,0
s_nop 0
buffer_load_dwordx4 v0,s[36:39],s61 offen offset:0 sc1 lds)"
R"(
v_lshlrev_b32 v60,16,v68
v_and_b32 v61,0xffff0000,v68
v_lshlrev_b32 v62,16,v69
v_and_b32 v63,0xffff0000,v69
buffer_load_dwordx4 v0,s[36:39],s61 offen offset:1024 sc1 lds
v_lshlrev_b32 v64,16,v70
v_and_b32 v65,0xffff0000,v70
v_lshlrev_b32 v66,16,v71
v_and_b32 v67,0xffff0000,v71
buffer_load_dwordx4 v0,s[36:39],s61 offen offset:2048 sc1 lds
v_cvt_pk_bf16_f32 v28,v28,v29
v_cvt_pk_bf16_f32 v29,v30,v31
v_cvt_pk_bf16_f32 v30,v32,v33
v_cvt_pk_bf16_f32 v31,v34,v35
buffer_load_dwordx4 v0,s[36:39],s61 offen offset:3072 sc1 lds
buffer_store_dwordx4 v[28:31],v3,s[36:39],s73 offen offset:0 sc0 sc1
v_add_f32 v36,v36,v60
v_add_f32 v37,v37,v61
v_add_f32 v38,v38,v62
s_add_u32 m0,s64,0
s_nop 0
buffer_load_dwordx4 v1,s[36:39],s61 offen offset:0 sc1 lds
v_add_f32 v39,v39,v63
v_add_f32 v40,v40,v64
v_add_f32 v41,v41,v65
v_add_f32 v42,v42,v66
buffer_load_dwordx4 v1,s[36:39],s61 offen offset:1024 sc1 lds
v_add_f32 v43,v43,v67
v_cvt_pk_bf16_f32 v36,v36,v37
v_cvt_pk_bf16_f32 v37,v38,v39
v_cvt_pk_bf16_f32 v38,v40,v41
buffer_load_dwordx4 v1,s[36:39],s61 offen offset:2048 sc1 lds
v_cvt_pk_bf16_f32 v39,v42,v43
buffer_store_dwordx4 v[36:39],v4,s[36:39],s73 offen offset:0 sc0 sc1
s_mul_i32 s68,s49,5
s_lshr_b32 s68,s68,6
s_add_u32 m0,s78,4096
s_nop 0
buffer_load_dword v7,s[36:39],s68 offen offset:0 sc1 lds
s_waitcnt vmcnt(6)
s_barrier
ds_read_b128 v[44:47],v2 offset:0
ds_read_b128 v[48:51],v2 offset:1024
ds_read_b128 v[52:55],v2 offset:2048
s_mov_b32 s65,s59
s_lshr_b32 s70,s59,4
buffer_store_dword v6,v5,s[36:39],s70 offen offset:0 sc0 sc1
s_waitcnt lgkmcnt(2)
s_setpc_b64 s[90:91]
Lg4_done0_%=:
s_waitcnt vmcnt(0)
s_barrier
s_lshr_b32 s70,s47,4
buffer_store_dword v6,v5,s[36:39],s70 offen offset:0 sc0 sc1
s_branch Lg4_done_%=
Lg4_slow1_%=:
ds_read_b32 v11,v8 offset:6144
s_waitcnt lgkmcnt(0)
v_cmp_eq_u32 vcc,s60,v11
s_nop 0
s_cmp_eq_u32 vcc_lo,-1
s_cbranch_scc1 Lg4_gs1_%=
Lg4_slowb1_%=:
s_cmp_eq_u32 s54,1
s_cselect_b32 s68,s50,s49
s_mul_i32 s68,s68,5
s_lshr_b32 s68,s68,6
s_add_u32 m0,s78,0
s_nop 0
buffer_load_dword v7,s[36:39],s68 offen offset:0 sc1 lds
s_lshr_b32 s73,s48,0
v_lshlrev_b32 v60,16,v68
v_and_b32 v61,0xffff0000,v68
v_lshlrev_b32 v62,16,v69
v_and_b32 v63,0xffff0000,v69
v_lshlrev_b32 v64,16,v70
v_and_b32 v65,0xffff0000,v70
v_lshlrev_b32 v66,16,v71
v_and_b32 v67,0xffff0000,v71
v_cvt_pk_bf16_f32 v12,v12,v13
v_cvt_pk_bf16_f32 v13,v14,v15
v_cvt_pk_bf16_f32 v14,v16,v17
v_cvt_pk_bf16_f32 v15,v18,v19
s_add_u32 m0,s78,1024
s_nop 0
buffer_load_dword v7,s[36:39],s68 offen offset:0 sc1 lds
buffer_store_dwordx4 v[12:15],v3,s[36:39],s73 offen offset:0 sc0 sc1
v_add_f32 v20,v20,v60
v_add_f32 v21,v21,v61
v_add_f32 v22,v22,v62
v_add_f32 v23,v23,v63
v_add_f32 v24,v24,v64
v_add_f32 v25,v25,v65
v_add_f32 v26,v26,v66
v_add_f32 v27,v27,v67
v_cvt_pk_bf16_f32 v20,v20,v21
v_cvt_pk_bf16_f32 v21,v22,v23
v_cvt_pk_bf16_f32 v22,v24,v25
v_cvt_pk_bf16_f32 v23,v26,v27
buffer_store_dwordx4 v[20:23],v4,s[36:39],s73 offen offset:0 sc0 sc1
s_cmp_ge_u32 s49,s59
s_cbranch_scc1 Lg4_done1_%=
s_add_u32 m0,s78,2048
s_nop 0
buffer_load_dword v7,s[36:39],s68 offen offset:0 sc1 lds
s_waitcnt vmcnt(4)
s_barrier
ds_read_b32 v11,v8 offset:0
s_mov_b32 s69,2304
s_waitcnt lgkmcnt(0)
v_cmp_eq_u32 vcc,s60,v11
s_nop 0
s_cmp_eq_u32 vcc_lo,-1
s_cbranch_scc1 Lg4_go1_%=
s_add_u32 m0,s78,3072
s_nop 0
buffer_load_dword v7,s[36:39],s68 offen offset:0 sc1 lds
s_waitcnt vmcnt(4)
s_barrier
ds_read_b32 v11,v8 offset:1024
s_mov_b32 s69,3328
s_waitcnt lgkmcnt(0)
v_cmp_eq_u32 vcc,s60,v11
s_nop 0
s_cmp_eq_u32 vcc_lo,-1
s_cbranch_scc1 Lg4_go1_%=
s_add_u32 m0,s78,0
s_nop 0
buffer_load_dword v7,s[36:39],s68 offen offset:0 sc1 lds
s_waitcnt vmcnt(2)
s_barrier
ds_read_b32 v11,v8 offset:2048
s_lshr_b32 s70,s48,4
buffer_store_dword v6,v5,s[36:39],s70 offen offset:0 sc0 sc1
s_mov_b32 s48,s59
s_mov_b32 s69,256
s_waitcnt lgkmcnt(0)
v_cmp_eq_u32 vcc,s60,v11
s_nop 0
s_cmp_eq_u32 vcc_lo,-1
s_cbranch_scc1 Lg4_go1_%=
s_add_u32 m0,s78,1024
s_nop 0
buffer_load_dword v7,s[36:39],s68 offen offset:0 sc1 lds
s_waitcnt vmcnt(3)
s_barrier
ds_read_b32 v11,v8 offset:3072
s_mov_b32 s69,1280
s_waitcnt lgkmcnt(0)
v_cmp_eq_u32 vcc,s60,v11
s_nop 0
s_cmp_eq_u32 vcc_lo,-1
s_cbranch_scc1 Lg4_go1_%=
Lg4_poll1_%=:
s_add_u32 m0,s78,2048
s_nop 0
buffer_load_dword v7,s[36:39],s68 offen offset:0 sc1 lds
s_waitcnt vmcnt(2)
s_barrier
ds_read_b32 v11,v8 offset:0
s_mov_b32 s69,2304
s_waitcnt lgkmcnt(0)
v_cmp_eq_u32 vcc,s60,v11
s_nop 0
s_cmp_eq_u32 vcc_lo,-1
s_cbranch_scc1 Lg4_go1_%=
s_sleep 4
s_add_u32 m0,s78,3072
s_nop 0
buffer_load_dword v7,s[36:39],s68 offen offset:0 sc1 lds
s_waitcnt vmcnt(2)
s_barrier
ds_read_b32 v11,v8 offset:1024
s_mov_b32 s69,3328
s_waitcnt lgkmcnt(0)
v_cmp_eq_u32 vcc,s60,v11
s_nop 0
s_cmp_eq_u32 vcc_lo,-1
s_cbranch_scc1 Lg4_go1_%=
s_sleep 4
s_add_u32 m0,s78,0
s_nop 0
buffer_load_dword v7,s[36:39],s68 offen offset:0 sc1 lds
s_waitcnt vmcnt(2)
s_barrier
ds_read_b32 v11,v8 offset:2048
s_mov_b32 s69,256
s_waitcnt lgkmcnt(0)
v_cmp_eq_u32 vcc,s60,v11
s_nop 0
s_cmp_eq_u32 vcc_lo,-1
s_cbranch_scc1 Lg4_go1_%=
s_sleep 4
s_add_u32 m0,s78,1024
s_nop 0
buffer_load_dword v7,s[36:39],s68 offen offset:0 sc1 lds
s_waitcnt vmcnt(2)
s_barrier
ds_read_b32 v11,v8 offset:3072
s_mov_b32 s69,1280)"
R"(
s_waitcnt lgkmcnt(0)
v_cmp_eq_u32 vcc,s60,v11
s_nop 0
s_cmp_eq_u32 vcc_lo,-1
s_cbranch_scc1 Lg4_go1_%=
s_sleep 4
s_add_u32 s79,s79,1
s_cmp_lt_u32 s79,20000
s_cbranch_scc1 Lg4_poll1_%=
Lg4_go1_%=:
s_mul_i32 s61,s49,7
s_lshr_b32 s61,s61,1
s_add_u32 m0,s63,28672
s_nop 0
buffer_load_dwordx4 v0,s[36:39],s61 offen offset:0 sc1 lds
buffer_load_dwordx4 v0,s[36:39],s61 offen offset:1024 sc1 lds
buffer_load_dwordx4 v0,s[36:39],s61 offen offset:2048 sc1 lds
buffer_load_dwordx4 v0,s[36:39],s61 offen offset:3072 sc1 lds
s_add_u32 m0,s64,28672
s_nop 0
buffer_load_dwordx4 v1,s[36:39],s61 offen offset:0 sc1 lds
buffer_load_dwordx4 v1,s[36:39],s61 offen offset:1024 sc1 lds
buffer_load_dwordx4 v1,s[36:39],s61 offen offset:2048 sc1 lds
s_mul_i32 s68,s50,5
s_lshr_b32 s68,s68,6
s_add_u32 m0,s78,5120
s_nop 0
buffer_load_dword v7,s[36:39],s68 offen offset:0 sc1 lds
s_waitcnt vmcnt(4)
s_barrier
ds_read_b128 v[44:47],v2 offset:28672
ds_read_b128 v[48:51],v2 offset:29696
ds_read_b128 v[52:55],v2 offset:30720
s_lshr_b32 s70,s48,4
buffer_store_dword v6,v5,s[36:39],s70 offen offset:0 sc0 sc1
s_mov_b32 s48,s59
s_waitcnt lgkmcnt(2)
s_setpc_b64 s[92:93]
Lg4_gs1_%=:
s_mul_i32 s61,s49,7
s_lshr_b32 s61,s61,1
s_cmp_eq_u32 s54,1
s_cselect_b32 s68,s50,s49
s_mul_i32 s68,s68,5
s_lshr_b32 s68,s68,6
s_lshr_b32 s73,s48,0
s_add_u32 m0,s63,28672
s_nop 0
buffer_load_dwordx4 v0,s[36:39],s61 offen offset:0 sc1 lds
v_lshlrev_b32 v60,16,v68
v_and_b32 v61,0xffff0000,v68
v_lshlrev_b32 v62,16,v69
v_and_b32 v63,0xffff0000,v69
buffer_load_dwordx4 v0,s[36:39],s61 offen offset:1024 sc1 lds
v_lshlrev_b32 v64,16,v70
v_and_b32 v65,0xffff0000,v70
v_lshlrev_b32 v66,16,v71
v_and_b32 v67,0xffff0000,v71
buffer_load_dwordx4 v0,s[36:39],s61 offen offset:2048 sc1 lds
v_cvt_pk_bf16_f32 v12,v12,v13
v_cvt_pk_bf16_f32 v13,v14,v15
v_cvt_pk_bf16_f32 v14,v16,v17
v_cvt_pk_bf16_f32 v15,v18,v19
buffer_load_dwordx4 v0,s[36:39],s61 offen offset:3072 sc1 lds
buffer_store_dwordx4 v[12:15],v3,s[36:39],s73 offen offset:0 sc0 sc1
v_add_f32 v20,v20,v60
v_add_f32 v21,v21,v61
v_add_f32 v22,v22,v62
s_add_u32 m0,s64,28672
s_nop 0
buffer_load_dwordx4 v1,s[36:39],s61 offen offset:0 sc1 lds
v_add_f32 v23,v23,v63
v_add_f32 v24,v24,v64
v_add_f32 v25,v25,v65
v_add_f32 v26,v26,v66
buffer_load_dwordx4 v1,s[36:39],s61 offen offset:1024 sc1 lds
v_add_f32 v27,v27,v67
v_cvt_pk_bf16_f32 v20,v20,v21
v_cvt_pk_bf16_f32 v21,v22,v23
v_cvt_pk_bf16_f32 v22,v24,v25
buffer_load_dwordx4 v1,s[36:39],s61 offen offset:2048 sc1 lds
v_cvt_pk_bf16_f32 v23,v26,v27
buffer_store_dwordx4 v[20:23],v4,s[36:39],s73 offen offset:0 sc0 sc1
s_mul_i32 s68,s50,5
s_lshr_b32 s68,s68,6
s_add_u32 m0,s78,5120
s_nop 0
buffer_load_dword v7,s[36:39],s68 offen offset:0 sc1 lds
s_waitcnt vmcnt(6)
s_barrier
ds_read_b128 v[44:47],v2 offset:28672
ds_read_b128 v[48:51],v2 offset:29696
ds_read_b128 v[52:55],v2 offset:30720
s_mov_b32 s65,s59
s_lshr_b32 s70,s59,4
buffer_store_dword v6,v5,s[36:39],s70 offen offset:0 sc0 sc1
s_waitcnt lgkmcnt(2)
s_setpc_b64 s[92:93]
Lg4_done1_%=:
s_waitcnt vmcnt(0)
s_barrier
s_lshr_b32 s70,s48,4
buffer_store_dword v6,v5,s[36:39],s70 offen offset:0 sc0 sc1
s_branch Lg4_done_%=
Lg4_done_%=:
s_waitcnt vmcnt(0) lgkmcnt(0)
s_barrier
s_endpgm
Lcode_g4_e:)"
  :: "s"(ws), "s"(wave), "s"(sStep), "s"(sX1), "s"(sX2), "s"(sU0), "s"(sWw), "s"(sBuf), "v"(vSrc), "v"(vSrc2), "v"(vA), "v"(vOut), "v"(vOut2), "v"(vFlag), "v"(vPollOff), "v"(vSnapRd), "v"(vAuxS), "v"(vWd0), "v"(vWd1), "v"(vWr0), "v"(vWr1), "v"(vWr2), "s"(w4) : "v0", "v1", "v2", "v3", "v4", "v5", "v6", "v7", "v8", "v9", "v10", "v11", "v12", "v13", "v14", "v15", "v16", "v17", "v18", "v19", "v20", "v21", "v22", "v23", "v24", "v25", "v26", "v27", "v28", "v29", "v30", "v31", "v32", "v33", "v34", "v35", "v36", "v37", "v38", "v39", "v40", "v41", "v42", "v43", "v44", "v45", "v46", "v47", "v48", "v49", "v50", "v51", "v52", "v53", "v54", "v55", "v56", "v57", "v58", "v59", "v60", "v61", "v62", "v63", "v64", "v65", "v66", "v67", "v68", "v69", "v70", "v71", "v72", "v73", "v74", "v75", "v76", "v77", "v78", "v79", "v80", "v81", "v82", "v83", "v84", "v85", "v86", "v87", "v88", "v89", "v90", "v91", "v92", "v93", "v94", "v95", "v96", "v97", "v98", "v99", "v100", "v101", "v102", "v103", "v104", "v105", "v106", "v107", "v108", "v109", "v110", "v111", "v112", "v113", "v114", "v115", "v116", "v117", "v118", "v119", "v120", "v121", "v122", "v123", "v124", "v125", "v126", "v127", "v128", "v129", "v130", "v131", "v132", "v133", "v134", "v135", "v136", "v137", "v138", "v139", "v140", "v141", "v142", "v143", "v144", "v145", "v146", "v147", "v148", "v149", "v150", "v151", "v152", "v153", "v154", "v155", "v156", "v157", "v158", "v159", "v160", "v161", "v162", "v163", "v164", "v165", "v166", "v167", "v168", "v169", "v170", "v171", "v172", "v173", "v174", "v175", "v176", "v177", "v178", "v179", "v180", "v181", "v182", "v183", "v184", "v185", "v186", "v187", "v188", "v189", "v190", "v191", "v192", "v193", "v194", "v195", "v196", "v197", "v198", "v199", "v200", "v201", "v202", "v203", "v204", "v205", "v206", "v207", "v208", "v209", "v210", "v211", "v212", "v213", "v214", "v215", "v216", "v217", "v218", "v219", "v220", "v221", "v222", "v223", "v224", "v225", "v226", "v227", "v228", "v229", "v230", "v231", "v255", "a0", "a1", "a2", "a3", "a4", "a5", "a6", "a7", "a8", "a9", "a10", "a11", "a12", "a13", "a14", "a15", "a16", "a17", "a18", "a19", "a20", "a21", "a22", "a23", "a24", "a25", "a26", "a27", "a28", "a29", "a30", "a31", "a32", "a33", "a34", "a35", "a36", "a37", "a38", "a39", "a40", "a41", "a42", "a43", "a44", "a45", "a46", "a47", "a48", "a49", "a50", "a51", "a52", "a53", "a54", "a55", "a56", "a57", "a58", "a59", "a60", "a61", "a62", "a63", "a64", "a65", "a66", "a67", "a68", "a69", "a70", "a71", "a72", "a73", "a74", "a75", "a76", "a77", "a78", "a79", "a80", "a81", "a82", "a83", "a84", "a85", "a86", "a87", "a88", "a89", "a90", "a91", "a92", "a93", "a94", "a95", "a96", "a97", "a98", "a99", "a100", "a101", "a102", "a103", "a104", "a105", "a106", "a107", "a108", "a109", "a110", "a111", "a112", "a113", "a114", "a115", "a116", "a117", "a118", "a119", "a120", "a121", "a122", "a123", "a124", "a125", "a126", "a127", "a128", "a129", "a130", "a131", "a132", "a133", "a134", "a135", "a136", "a137", "a138", "a139", "a140", "a141", "a142", "a143", "a144", "a145", "a146", "a147", "a148", "a149", "a150", "a151", "a152", "a153", "a154", "a155", "a156", "a157", "a158", "a159", "a160", "a161", "a162", "a163", "a164", "a165", "a166", "a167", "a168", "a169", "a170", "a171", "a172", "a173", "a174", "a175", "a176", "a177", "a178", "a179", "a180", "a181", "a182", "a183", "a184", "a185", "a186", "a187", "a188", "a189", "a190", "a191", "a192", "a193", "a194", "a195", "a196", "a197", "a198", "a199", "a200", "a201", "a202", "a203", "a204", "a205", "a206", "a207", "a208", "a209", "a210", "a211", "a212", "a213", "a214", "a215", "a216", "a217", "a218", "a219", "a220", "a221", "a222", "a223", "a224", "a225", "a226", "a227", "a228", "a229", "a230", "a231", "a232", "a233", "a234", "a235", "a236", "a237", "a238", "a239", "a240", "a241", "a242", "a243", "a244", "a245", "a246", "a247", "a248", "a249", "a250", "a251", "a252", "a253", "a254", "a255", "s36", "s37", "s38", "s39", "s40", "s41", "s42", "s43", "s44", "s45", "s46", "s47", "s48", "s49", "s50", "s51", "s52", "s53", "s54", "s55", "s56", "s57", "s58", "s59", "s60", "s61", "s62", "s63", "s64", "s65", "s66", "s67", "s68", "s69", "s70", "s71", "s72", "s73", "s74", "s75", "s76", "s77", "s78", "s79", "s80", "s81", "s82", "s83", "s84", "s85", "s86", "s87", "s88", "s89", "s90", "s91", "s92", "s93", "s94", "s95", "s96", "s97", "s98", "s99", "vcc", "scc", "memory");
    __builtin_unreachable();
  } else {
   if (kind != 6) {
    COMMON(kind == 1 ? 8 : 16, kind == 1 ? OFF_FXP : OFF_FH, (kind == 1 ? OFF_FH : OFF_FX1) + slice * 128, AUX + 1536, 2048, 2048, 4);
    const int dl = kind == 2 ? ((wave & 2) ? -1 : 1) : 0, r0 = 16 + l15 - dl, r1 = 16 + l15 - 2 * dl;
    const unsigned sb = (kind == 1 ? OFF_XP : OFF_HS) - 32768 + wave * 4096 + lg * 256;
    const unsigned vSrc = sb + (r0 >> 4) * 32768 + (r0 & 15) * 16, vSrc2 = sb + 16384 + (r1 >> 4) * 32768 + (r1 & 15) * 16;
    const unsigned vOut = (kind == 1 ? OFF_HS : OFF_X1) + (slice * 4 + wave) * 1024 + vA;
    const unsigned vX = AUX + wave * 2048 + (kind == 1 ? l15 * 16 : vA), vX2 = AUX + wave * 2048 + l15 * 8;
    const unsigned vAuxS = kind == 1 ? OFF_S1 + ln * 4 : OFF_XP + (slice * 4 + wave) * 1024 + vA;
    const unsigned vAuxS2 = ln < 32 ? OFF_S1 - 1024 + 256 + ln * 4 : OOB;
    constexpr unsigned RED = AUX + 24576 + 1536;
    const unsigned vOut2 = wave != 2 ? OOB : kind == 1 ? (slice == 0 && ln < 16 ? OFF_S1 + 256 + ln * 8 : OOB) : ln < 32 ? OFF_ST + slice * 128 + ln * 4 : OOB;
    const unsigned vStW = RED + wave * 4 + (kind == 1 ? l15 * 32 + (lg == lq ? 16 : 0) : (l15 * 2 + (lg & 1)) * 16), vStR = RED + (kind == 1 ? l15 * 32 : (ln & 31) * 16);
    const unsigned sWw = 16 * wave * 2048 + slice * 256;
    const u16* w12 = kind == 1 ? w1 : w2;
    unsigned epOut;
    asm volatile(R"(
Lcode_g12_b:
s_cmp_eq_u32 %8,2
s_cbranch_scc1 Lg2_begin_%=
s_mov_b64 s[36:37],%1
s_mov_b32 s38,122727552
s_mov_b32 s39,0x00020000
s_mov_b32 s54,%2
s_mov_b32 s55,%3
s_mov_b32 s56,%4
s_mov_b32 s58,%5
s_mov_b32 s83,%6
s_mov_b32 s82,%7
s_mov_b32 s84,%8
v_mov_b32 v0,%9
v_mov_b32 v1,%10
v_mov_b32 v2,%11
v_mov_b32 v3,%12
v_mov_b32 v4,%13
v_mov_b32 v6,%14
v_mov_b32 v7,%15
v_mov_b32 v8,%16
v_mov_b32 v9,%17
v_mov_b32 v10,%18
v_mov_b32 v11,%19
v_mov_b32 v12,%20
v_mov_b32 v13,%21
v_mov_b32 v14,%22
v_mov_b32 v28,%23
v_mov_b32 v29,%24
v_mov_b32 v30,%25
v_mov_b32 v31,%26
v_mov_b32 v32,%27
s_mov_b64 s[40:41],%28
s_mov_b32 s42,2097152
s_mov_b32 s43,0x00020000
s_lshl_b32 s88,s54,11
s_mov_b32 s69,122727424
buffer_load_dword v5,off,s[36:39],s69 sc1
s_add_u32 m0,s88,0
s_add_u32 s61,s82,0
buffer_load_dwordx4 v28,s[40:43],s61 offen offset:0  lds
s_add_u32 m0,s88,1024
s_nop 0
buffer_load_dwordx4 v29,s[40:43],s61 offen offset:0  lds
s_add_u32 m0,s88,8064
s_nop 0
buffer_load_dwordx4 v28,s[40:43],s61 offen offset:128  lds
s_add_u32 m0,s88,9088
s_nop 0
buffer_load_dwordx4 v29,s[40:43],s61 offen offset:128  lds
s_add_u32 m0,s88,16384
s_add_u32 s61,s82,131072
buffer_load_dwordx4 v28,s[40:43],s61 offen offset:0  lds
s_add_u32 m0,s88,17408
s_nop 0
buffer_load_dwordx4 v29,s[40:43],s61 offen offset:0  lds
s_add_u32 m0,s88,24448
s_nop 0
buffer_load_dwordx4 v28,s[40:43],s61 offen offset:128  lds
s_add_u32 m0,s88,25472
s_nop 0
buffer_load_dwordx4 v29,s[40:43],s61 offen offset:128  lds
s_add_u32 m0,s88,32768
s_add_u32 s61,s82,262144
buffer_load_dwordx4 v28,s[40:43],s61 offen offset:0  lds
s_add_u32 m0,s88,33792
s_nop 0
buffer_load_dwordx4 v29,s[40:43],s61 offen offset:0  lds
s_add_u32 m0,s88,40832
s_nop 0
buffer_load_dwordx4 v28,s[40:43],s61 offen offset:128  lds
s_add_u32 m0,s88,41856
s_nop 0
buffer_load_dwordx4 v29,s[40:43],s61 offen offset:128  lds
s_add_u32 m0,s88,49152
s_add_u32 s61,s82,393216
buffer_load_dwordx4 v28,s[40:43],s61 offen offset:0  lds
s_add_u32 m0,s88,50176
s_nop 0
buffer_load_dwordx4 v29,s[40:43],s61 offen offset:0  lds
s_add_u32 m0,s88,57216
s_nop 0
buffer_load_dwordx4 v28,s[40:43],s61 offen offset:128  lds
s_add_u32 m0,s88,58240
s_nop 0
buffer_load_dwordx4 v29,s[40:43],s61 offen offset:128  lds
s_add_u32 m0,s88,65536
s_add_u32 s61,s82,524288
buffer_load_dwordx4 v28,s[40:43],s61 offen offset:0  lds
s_add_u32 m0,s88,66560
s_nop 0
buffer_load_dwordx4 v29,s[40:43],s61 offen offset:0  lds
s_add_u32 m0,s88,73600
s_nop 0
buffer_load_dwordx4 v28,s[40:43],s61 offen offset:128  lds
s_add_u32 m0,s88,74624
s_nop 0
buffer_load_dwordx4 v29,s[40:43],s61 offen offset:128  lds
s_add_u32 m0,s88,81920
s_add_u32 s61,s82,655360
buffer_load_dwordx4 v28,s[40:43],s61 offen offset:0  lds
s_add_u32 m0,s88,82944
s_nop 0
buffer_load_dwordx4 v29,s[40:43],s61 offen offset:0  lds
s_add_u32 m0,s88,89984
s_nop 0
buffer_load_dwordx4 v28,s[40:43],s61 offen offset:128  lds
s_add_u32 m0,s88,91008
s_nop 0
buffer_load_dwordx4 v29,s[40:43],s61 offen offset:128  lds
s_add_u32 m0,s88,98304
s_add_u32 s61,s82,786432
buffer_load_dwordx4 v28,s[40:43],s61 offen offset:0  lds
s_add_u32 m0,s88,99328
s_nop 0
buffer_load_dwordx4 v29,s[40:43],s61 offen offset:0  lds
s_add_u32 m0,s88,106368
s_nop 0
buffer_load_dwordx4 v28,s[40:43],s61 offen offset:128  lds
s_add_u32 m0,s88,107392
s_nop 0
buffer_load_dwordx4 v29,s[40:43],s61 offen offset:128  lds
v_add_u32 v36,49152,v30
v_add_u32 v37,49152,v31
v_add_u32 v38,98304,v30
v_add_u32 v39,98304,v31
v_mov_b32 v76,0x3f803f80
v_mov_b32 v77,0x3f803f80
v_mov_b32 v78,0x3f803f80
v_mov_b32 v79,0x3f803f80
s_waitcnt vmcnt(24)
s_barrier
s_add_u32 m0,s88,114688
s_add_u32 s61,s82,917504
buffer_load_dwordx4 v28,s[40:43],s61 offen offset:0  lds
s_add_u32 m0,s88,115712
ds_read_b64_tr_b16 a[0:1],v30 offset:0
buffer_load_dwordx4 v29,s[40:43],s61 offen offset:0  lds
s_add_u32 m0,s88,122752
ds_read_b64_tr_b16 a[2:3],v30 offset:512
buffer_load_dwordx4 v28,s[40:43],s61 offen offset:128  lds
s_add_u32 m0,s88,123776
ds_read_b64_tr_b16 a[4:5],v31 offset:0
buffer_load_dwordx4 v29,s[40:43],s61 offen offset:128  lds
ds_read_b64_tr_b16 a[6:7],v31 offset:512
ds_read_b64_tr_b16 a[8:9],v30 offset:4096
ds_read_b64_tr_b16 a[10:11],v30 offset:4608
ds_read_b64_tr_b16 a[12:13],v31 offset:4096
ds_read_b64_tr_b16 a[14:15],v31 offset:4608
s_waitcnt vmcnt(24)
s_barrier
s_add_u32 m0,s88,131072
s_add_u32 s61,s82,1048576
buffer_load_dwordx4 v28,s[40:43],s61 offen offset:0  lds
s_add_u32 m0,s88,132096
ds_read_b64_tr_b16 a[16:17],v30 offset:16384
buffer_load_dwordx4 v29,s[40:43],s61 offen offset:0  lds
s_add_u32 m0,s88,139136
ds_read_b64_tr_b16 a[18:19],v30 offset:16896
buffer_load_dwordx4 v28,s[40:43],s61 offen offset:128  lds
s_add_u32 m0,s88,140160
ds_read_b64_tr_b16 a[20:21],v31 offset:16384
buffer_load_dwordx4 v29,s[40:43],s61 offen offset:128  lds
ds_read_b64_tr_b16 a[22:23],v31 offset:16896
ds_read_b64_tr_b16 a[24:25],v30 offset:20480
ds_read_b64_tr_b16 a[26:27],v30 offset:20992
ds_read_b64_tr_b16 a[28:29],v31 offset:20480
ds_read_b64_tr_b16 a[30:31],v31 offset:20992
s_waitcnt lgkmcnt(8)
v_mfma_f32_16x16x32_bf16 v[20:23],a[0:3],v[76:79],0
v_mfma_f32_16x16x32_bf16 v[24:27],a[4:7],v[76:79],0
v_mfma_f32_16x16x32_bf16 v[20:23],a[8:11],v[76:79],v[20:23]
v_mfma_f32_16x16x32_bf16 v[24:27],a[12:15],v[76:79],v[24:27]
s_waitcnt vmcnt(24)
s_barrier
s_add_u32 m0,s88,0
s_add_u32 s61,s82,1179648
buffer_load_dwordx4 v28,s[40:43],s61 offen offset:0  lds
s_add_u32 m0,s88,1024
ds_read_b64_tr_b16 a[32:33],v30 offset:32768
buffer_load_dwordx4 v29,s[40:43],s61 offen offset:0  lds
s_add_u32 m0,s88,8064
ds_read_b64_tr_b16 a[34:35],v30 offset:33280
buffer_load_dwordx4 v28,s[40:43],s61 offen offset:128  lds
s_add_u32 m0,s88,9088
ds_read_b64_tr_b16 a[36:37],v31 offset:32768
buffer_load_dwordx4 v29,s[40:43],s61 offen offset:128  lds
ds_read_b64_tr_b16 a[38:39],v31 offset:33280
ds_read_b64_tr_b16 a[40:41],v30 offset:36864
ds_read_b64_tr_b16 a[42:43],v30 offset:37376
ds_read_b64_tr_b16 a[44:45],v31 offset:36864
ds_read_b64_tr_b16 a[46:47],v31 offset:37376
s_waitcnt lgkmcnt(8)
v_mfma_f32_16x16x32_bf16 v[20:23],a[16:19],v[76:79],v[20:23]
v_mfma_f32_16x16x32_bf16 v[24:27],a[20:23],v[76:79],v[24:27]
v_mfma_f32_16x16x32_bf16 v[20:23],a[24:27],v[76:79],v[20:23]
v_mfma_f32_16x16x32_bf16 v[24:27],a[28:31],v[76:79],v[24:27]
s_waitcnt vmcnt(24)
s_barrier
s_add_u32 m0,s88,16384)"
R"(
s_add_u32 s61,s82,1310720
buffer_load_dwordx4 v28,s[40:43],s61 offen offset:0  lds
s_add_u32 m0,s88,17408
ds_read_b64_tr_b16 a[48:49],v36 offset:0
buffer_load_dwordx4 v29,s[40:43],s61 offen offset:0  lds
s_add_u32 m0,s88,24448
ds_read_b64_tr_b16 a[50:51],v36 offset:512
buffer_load_dwordx4 v28,s[40:43],s61 offen offset:128  lds
s_add_u32 m0,s88,25472
ds_read_b64_tr_b16 a[52:53],v37 offset:0
buffer_load_dwordx4 v29,s[40:43],s61 offen offset:128  lds
ds_read_b64_tr_b16 a[54:55],v37 offset:512
ds_read_b64_tr_b16 a[56:57],v36 offset:4096
ds_read_b64_tr_b16 a[58:59],v36 offset:4608
ds_read_b64_tr_b16 a[60:61],v37 offset:4096
ds_read_b64_tr_b16 a[62:63],v37 offset:4608
s_waitcnt lgkmcnt(8)
v_mfma_f32_16x16x32_bf16 v[20:23],a[32:35],v[76:79],v[20:23]
v_mfma_f32_16x16x32_bf16 v[24:27],a[36:39],v[76:79],v[24:27]
v_mfma_f32_16x16x32_bf16 v[20:23],a[40:43],v[76:79],v[20:23]
v_mfma_f32_16x16x32_bf16 v[24:27],a[44:47],v[76:79],v[24:27]
s_waitcnt vmcnt(24)
s_barrier
s_add_u32 m0,s88,32768
s_add_u32 s61,s82,1441792
buffer_load_dwordx4 v28,s[40:43],s61 offen offset:0  lds
s_add_u32 m0,s88,33792
ds_read_b64_tr_b16 a[64:65],v36 offset:16384
buffer_load_dwordx4 v29,s[40:43],s61 offen offset:0  lds
s_add_u32 m0,s88,40832
ds_read_b64_tr_b16 a[66:67],v36 offset:16896
buffer_load_dwordx4 v28,s[40:43],s61 offen offset:128  lds
s_add_u32 m0,s88,41856
ds_read_b64_tr_b16 a[68:69],v37 offset:16384
buffer_load_dwordx4 v29,s[40:43],s61 offen offset:128  lds
ds_read_b64_tr_b16 a[70:71],v37 offset:16896
ds_read_b64_tr_b16 a[72:73],v36 offset:20480
ds_read_b64_tr_b16 a[74:75],v36 offset:20992
ds_read_b64_tr_b16 a[76:77],v37 offset:20480
ds_read_b64_tr_b16 a[78:79],v37 offset:20992
s_waitcnt lgkmcnt(8)
v_mfma_f32_16x16x32_bf16 v[20:23],a[48:51],v[76:79],v[20:23]
v_mfma_f32_16x16x32_bf16 v[24:27],a[52:55],v[76:79],v[24:27]
v_mfma_f32_16x16x32_bf16 v[20:23],a[56:59],v[76:79],v[20:23]
v_mfma_f32_16x16x32_bf16 v[24:27],a[60:63],v[76:79],v[24:27]
s_waitcnt vmcnt(24)
s_barrier
s_add_u32 m0,s88,49152
s_add_u32 s61,s82,1572864
buffer_load_dwordx4 v28,s[40:43],s61 offen offset:0  lds
s_add_u32 m0,s88,50176
ds_read_b64_tr_b16 a[80:81],v36 offset:32768
buffer_load_dwordx4 v29,s[40:43],s61 offen offset:0  lds
s_add_u32 m0,s88,57216
ds_read_b64_tr_b16 a[82:83],v36 offset:33280
buffer_load_dwordx4 v28,s[40:43],s61 offen offset:128  lds
s_add_u32 m0,s88,58240
ds_read_b64_tr_b16 a[84:85],v37 offset:32768
buffer_load_dwordx4 v29,s[40:43],s61 offen offset:128  lds
ds_read_b64_tr_b16 a[86:87],v37 offset:33280
ds_read_b64_tr_b16 a[88:89],v36 offset:36864
ds_read_b64_tr_b16 a[90:91],v36 offset:37376
ds_read_b64_tr_b16 a[92:93],v37 offset:36864
ds_read_b64_tr_b16 a[94:95],v37 offset:37376
s_waitcnt lgkmcnt(8)
v_mfma_f32_16x16x32_bf16 v[20:23],a[64:67],v[76:79],v[20:23]
v_mfma_f32_16x16x32_bf16 v[24:27],a[68:71],v[76:79],v[24:27]
v_mfma_f32_16x16x32_bf16 v[20:23],a[72:75],v[76:79],v[20:23]
v_mfma_f32_16x16x32_bf16 v[24:27],a[76:79],v[76:79],v[24:27]
s_waitcnt vmcnt(24)
s_barrier
s_add_u32 m0,s88,65536
s_add_u32 s61,s82,1703936
buffer_load_dwordx4 v28,s[40:43],s61 offen offset:0  lds
s_add_u32 m0,s88,66560
ds_read_b64_tr_b16 a[96:97],v38 offset:0
buffer_load_dwordx4 v29,s[40:43],s61 offen offset:0  lds
s_add_u32 m0,s88,73600
ds_read_b64_tr_b16 a[98:99],v38 offset:512
buffer_load_dwordx4 v28,s[40:43],s61 offen offset:128  lds
s_add_u32 m0,s88,74624
ds_read_b64_tr_b16 a[100:101],v39 offset:0
buffer_load_dwordx4 v29,s[40:43],s61 offen offset:128  lds
ds_read_b64_tr_b16 a[102:103],v39 offset:512
ds_read_b64_tr_b16 a[104:105],v38 offset:4096
ds_read_b64_tr_b16 a[106:107],v38 offset:4608
ds_read_b64_tr_b16 a[108:109],v39 offset:4096
ds_read_b64_tr_b16 a[110:111],v39 offset:4608
s_waitcnt lgkmcnt(8)
v_mfma_f32_16x16x32_bf16 v[20:23],a[80:83],v[76:79],v[20:23]
v_mfma_f32_16x16x32_bf16 v[24:27],a[84:87],v[76:79],v[24:27]
v_mfma_f32_16x16x32_bf16 v[20:23],a[88:91],v[76:79],v[20:23]
v_mfma_f32_16x16x32_bf16 v[24:27],a[92:95],v[76:79],v[24:27]
s_waitcnt vmcnt(24)
s_barrier
s_add_u32 m0,s88,81920
s_add_u32 s61,s82,1835008
buffer_load_dwordx4 v28,s[40:43],s61 offen offset:0  lds
s_add_u32 m0,s88,82944
ds_read_b64_tr_b16 a[112:113],v38 offset:16384
buffer_load_dwordx4 v29,s[40:43],s61 offen offset:0  lds
s_add_u32 m0,s88,89984
ds_read_b64_tr_b16 a[114:115],v38 offset:16896
buffer_load_dwordx4 v28,s[40:43],s61 offen offset:128  lds
s_add_u32 m0,s88,91008
ds_read_b64_tr_b16 a[116:117],v39 offset:16384
buffer_load_dwordx4 v29,s[40:43],s61 offen offset:128  lds
ds_read_b64_tr_b16 a[118:119],v39 offset:16896
ds_read_b64_tr_b16 a[120:121],v38 offset:20480
ds_read_b64_tr_b16 a[122:123],v38 offset:20992
ds_read_b64_tr_b16 a[124:125],v39 offset:20480
ds_read_b64_tr_b16 a[126:127],v39 offset:20992
s_waitcnt lgkmcnt(8)
v_mfma_f32_16x16x32_bf16 v[20:23],a[96:99],v[76:79],v[20:23]
v_mfma_f32_16x16x32_bf16 v[24:27],a[100:103],v[76:79],v[24:27]
v_mfma_f32_16x16x32_bf16 v[20:23],a[104:107],v[76:79],v[20:23]
v_mfma_f32_16x16x32_bf16 v[24:27],a[108:111],v[76:79],v[24:27]
s_waitcnt vmcnt(24)
s_barrier
s_add_u32 m0,s88,98304
s_add_u32 s61,s82,1966080
buffer_load_dwordx4 v28,s[40:43],s61 offen offset:0  lds
s_add_u32 m0,s88,99328
ds_read_b64_tr_b16 a[128:129],v38 offset:32768
buffer_load_dwordx4 v29,s[40:43],s61 offen offset:0  lds
s_add_u32 m0,s88,106368
ds_read_b64_tr_b16 a[130:131],v38 offset:33280
buffer_load_dwordx4 v28,s[40:43],s61 offen offset:128  lds
s_add_u32 m0,s88,107392
ds_read_b64_tr_b16 a[132:133],v39 offset:32768
buffer_load_dwordx4 v29,s[40:43],s61 offen offset:128  lds
ds_read_b64_tr_b16 a[134:135],v39 offset:33280
ds_read_b64_tr_b16 a[136:137],v38 offset:36864
ds_read_b64_tr_b16 a[138:139],v38 offset:37376
ds_read_b64_tr_b16 a[140:141],v39 offset:36864
ds_read_b64_tr_b16 a[142:143],v39 offset:37376
s_waitcnt lgkmcnt(8)
v_mfma_f32_16x16x32_bf16 v[20:23],a[112:115],v[76:79],v[20:23]
v_mfma_f32_16x16x32_bf16 v[24:27],a[116:119],v[76:79],v[24:27]
v_mfma_f32_16x16x32_bf16 v[20:23],a[120:123],v[76:79],v[20:23]
v_mfma_f32_16x16x32_bf16 v[24:27],a[124:127],v[76:79],v[24:27]
s_waitcnt vmcnt(24)
s_barrier
ds_read_b64_tr_b16 a[144:145],v30 offset:0
ds_read_b64_tr_b16 a[146:147],v30 offset:512
ds_read_b64_tr_b16 a[148:149],v31 offset:0
ds_read_b64_tr_b16 a[150:151],v31 offset:512
ds_read_b64_tr_b16 a[152:153],v30 offset:4096
ds_read_b64_tr_b16 a[154:155],v30 offset:4608
ds_read_b64_tr_b16 a[156:157],v31 offset:4096
ds_read_b64_tr_b16 a[158:159],v31 offset:4608
s_waitcnt lgkmcnt(8)
v_mfma_f32_16x16x32_bf16 v[20:23],a[128:131],v[76:79],v[20:23]
v_mfma_f32_16x16x32_bf16 v[24:27],a[132:135],v[76:79],v[24:27]
v_mfma_f32_16x16x32_bf16 v[20:23],a[136:139],v[76:79],v[20:23]
v_mfma_f32_16x16x32_bf16 v[24:27],a[140:143],v[76:79],v[24:27]
s_waitcnt vmcnt(20)
s_barrier
ds_read_b64_tr_b16 a[160:161],v30 offset:16384
ds_read_b64_tr_b16 a[162:163],v30 offset:16896
ds_read_b64_tr_b16 a[164:165],v31 offset:16384
ds_read_b64_tr_b16 a[166:167],v31 offset:16896
ds_read_b64_tr_b16 a[168:169],v30 offset:20480
ds_read_b64_tr_b16 a[170:171],v30 offset:20992
ds_read_b64_tr_b16 a[172:173],v31 offset:20480
ds_read_b64_tr_b16 a[174:175],v31 offset:20992
s_waitcnt lgkmcnt(8)
v_mfma_f32_16x16x32_bf16 v[20:23],a[144:147],v[76:79],v[20:23]
v_mfma_f32_16x16x32_bf16 v[24:27],a[148:151],v[76:79],v[24:27]
v_mfma_f32_16x16x32_bf16 v[20:23],a[152:155],v[76:79],v[20:23]
v_mfma_f32_16x16x32_bf16 v[24:27],a[156:159],v[76:79],v[24:27]
s_waitcnt vmcnt(16)
s_barrier
ds_read_b64_tr_b16 a[176:177],v30 offset:32768
ds_read_b64_tr_b16 a[178:179],v30 offset:33280
ds_read_b64_tr_b16 a[180:181],v31 offset:32768
ds_read_b64_tr_b16 a[182:183],v31 offset:33280
ds_read_b64_tr_b16 a[184:185],v30 offset:36864
ds_read_b64_tr_b16 a[186:187],v30 offset:37376
ds_read_b64_tr_b16 a[188:189],v31 offset:36864
ds_read_b64_tr_b16 a[190:191],v31 offset:37376
s_getpc_b64 s[46:47]
Lg1_cp_%=:
s_add_u32 s46,s46,Lg1_init_%=-Lg1_cp_%=
s_addc_u32 s47,s47,0
s_lshl_b32 s68,s54,13
v_lshl_add_u32 v60,v2,3,s68
v_min_u32 v61,Lg1_done_%=-Lg1_init_%=,v60
global_load_dword v64,v61,s[46:47]
v_add_u32 v60,32768,v60
v_min_u32 v62,Lg1_done_%=-Lg1_init_%=,v60
global_load_dword v65,v62,s[46:47]
s_waitcnt lgkmcnt(8)
v_mfma_f32_16x16x32_bf16 v[20:23],a[160:163],v[76:79],v[20:23]
v_mfma_f32_16x16x32_bf16 v[24:27],a[164:167],v[76:79],v[24:27]
v_mfma_f32_16x16x32_bf16 v[20:23],a[168:171],v[76:79],v[20:23]
v_mfma_f32_16x16x32_bf16 v[24:27],a[172:175],v[76:79],v[24:27]
s_waitcnt vmcnt(14)
s_barrier
ds_read_b64_tr_b16 a[192:193],v36 offset:0)"
R"(
ds_read_b64_tr_b16 a[194:195],v36 offset:512
ds_read_b64_tr_b16 a[196:197],v37 offset:0
ds_read_b64_tr_b16 a[198:199],v37 offset:512
ds_read_b64_tr_b16 a[200:201],v36 offset:4096
ds_read_b64_tr_b16 a[202:203],v36 offset:4608
ds_read_b64_tr_b16 a[204:205],v37 offset:4096
ds_read_b64_tr_b16 a[206:207],v37 offset:4608
s_waitcnt lgkmcnt(8)
v_mfma_f32_16x16x32_bf16 v[20:23],a[176:179],v[76:79],v[20:23]
v_mfma_f32_16x16x32_bf16 v[24:27],a[180:183],v[76:79],v[24:27]
v_mfma_f32_16x16x32_bf16 v[20:23],a[184:187],v[76:79],v[20:23]
v_mfma_f32_16x16x32_bf16 v[24:27],a[188:191],v[76:79],v[24:27]
s_waitcnt vmcnt(10)
s_barrier
ds_read_b64_tr_b16 a[208:209],v36 offset:16384
ds_read_b64_tr_b16 a[210:211],v36 offset:16896
ds_read_b64_tr_b16 a[212:213],v37 offset:16384
ds_read_b64_tr_b16 a[214:215],v37 offset:16896
ds_read_b64_tr_b16 a[216:217],v36 offset:20480
ds_read_b64_tr_b16 a[218:219],v36 offset:20992
ds_read_b64_tr_b16 a[220:221],v37 offset:20480
ds_read_b64_tr_b16 a[222:223],v37 offset:20992
s_waitcnt lgkmcnt(8)
v_mfma_f32_16x16x32_bf16 v[20:23],a[192:195],v[76:79],v[20:23]
v_mfma_f32_16x16x32_bf16 v[24:27],a[196:199],v[76:79],v[24:27]
v_mfma_f32_16x16x32_bf16 v[20:23],a[200:203],v[76:79],v[20:23]
v_mfma_f32_16x16x32_bf16 v[24:27],a[204:207],v[76:79],v[24:27]
s_waitcnt vmcnt(6)
s_barrier
ds_read_b64_tr_b16 a[224:225],v36 offset:32768
ds_read_b64_tr_b16 a[226:227],v36 offset:33280
ds_read_b64_tr_b16 a[228:229],v37 offset:32768
ds_read_b64_tr_b16 a[230:231],v37 offset:33280
ds_read_b64_tr_b16 a[232:233],v36 offset:36864
ds_read_b64_tr_b16 a[234:235],v36 offset:37376
ds_read_b64_tr_b16 a[236:237],v37 offset:36864
ds_read_b64_tr_b16 a[238:239],v37 offset:37376
s_waitcnt lgkmcnt(8)
v_mfma_f32_16x16x32_bf16 v[20:23],a[208:211],v[76:79],v[20:23]
v_mfma_f32_16x16x32_bf16 v[24:27],a[212:215],v[76:79],v[24:27]
v_mfma_f32_16x16x32_bf16 v[20:23],a[216:219],v[76:79],v[20:23]
v_mfma_f32_16x16x32_bf16 v[24:27],a[220:223],v[76:79],v[24:27]
s_waitcnt vmcnt(2)
s_barrier
ds_read_b64_tr_b16 a[240:241],v38 offset:0
ds_read_b64_tr_b16 a[242:243],v38 offset:512
ds_read_b64_tr_b16 a[244:245],v39 offset:0
ds_read_b64_tr_b16 a[246:247],v39 offset:512
ds_read_b64_tr_b16 a[248:249],v38 offset:4096
ds_read_b64_tr_b16 a[250:251],v38 offset:4608
ds_read_b64_tr_b16 a[252:253],v39 offset:4096
ds_read_b64_tr_b16 a[254:255],v39 offset:4608
s_waitcnt lgkmcnt(8)
v_mfma_f32_16x16x32_bf16 v[20:23],a[224:227],v[76:79],v[20:23]
v_mfma_f32_16x16x32_bf16 v[24:27],a[228:231],v[76:79],v[24:27]
v_mfma_f32_16x16x32_bf16 v[20:23],a[232:235],v[76:79],v[20:23]
v_mfma_f32_16x16x32_bf16 v[24:27],a[236:239],v[76:79],v[24:27]
s_waitcnt lgkmcnt(0)
v_mfma_f32_16x16x32_bf16 v[20:23],a[240:243],v[76:79],v[20:23]
v_mfma_f32_16x16x32_bf16 v[24:27],a[244:247],v[76:79],v[24:27]
v_mfma_f32_16x16x32_bf16 v[20:23],a[248:251],v[76:79],v[20:23]
v_mfma_f32_16x16x32_bf16 v[24:27],a[252:255],v[76:79],v[24:27]
s_waitcnt lgkmcnt(0)
s_barrier
v_readfirstlane_b32 s60,v5
s_nop 3
s_add_u32 s60,s60,1
s_cmp_eq_u32 s60,0
s_cselect_b32 s60,1,s60
Lg1_init_%=:
v_mov_b32 v5,s60
s_mov_b32 s59,536870912
s_add_u32 s57,s56,131072
s_mov_b32 s79,0
s_mov_b32 s75,0x3a800000
s_mov_b32 s76,0xc0135761
s_mov_b32 s77,0x3d372713
s_mov_b32 s44,s59
s_mov_b32 s45,s59
s_mov_b32 s46,s59
s_mov_b32 s47,s59
s_mov_b32 s48,s83
s_add_u32 s49,s48,s55
s_cmp_eq_u32 s49,s56
s_cselect_b32 s49,s57,s49
s_cmp_eq_u32 s49,s58
s_cselect_b32 s49,s59,s49
s_add_u32 s50,s49,s55
s_cmp_eq_u32 s50,s56
s_cselect_b32 s50,s57,s50
s_cmp_eq_u32 s50,s58
s_cselect_b32 s50,s59,s50
s_add_u32 s51,s50,s55
s_cmp_eq_u32 s51,s56
s_cselect_b32 s51,s57,s51
s_cmp_eq_u32 s51,s58
s_cselect_b32 s51,s59,s51
s_add_u32 s52,s51,s55
s_cmp_eq_u32 s52,s56
s_cselect_b32 s52,s57,s52
s_cmp_eq_u32 s52,s58
s_cselect_b32 s52,s59,s52
s_lshl_b32 s63,s54,12
s_add_u32 s63,s63,65536
s_add_u32 s64,s63,32768
s_lshl_b32 s88,s54,10
s_add_u32 s88,s88,131072
s_lshl_b32 s78,s54,11
s_add_u32 s78,s78,132608
v_mov_b32 v15,0x3727c5ac
v_mov_b32 v100,0x3f803f80
v_mov_b32 v101,0x3f803f80
v_mov_b32 v102,0x3f803f80
v_mov_b32 v103,0x3f803f80
s_mov_b32 s89,1048576
s_mov_b32 s82,0xaaaaaaaa
s_mov_b32 s83,0xaaaaaaaa
s_mov_b32 s84,0xcccccccc
s_mov_b32 s85,0xcccccccc
s_mov_b32 s98,0x00f0000f
s_mov_b32 s99,0xf0000f00
s_getpc_b64 s[90:91]
Lg1_pc_%=:
s_mov_b32 s68,Lg1_d1w0_%=-Lg1_pc_%=
s_cmp_eq_u32 s54,1
s_cselect_b32 s68,Lg1_d1w1_%=-Lg1_pc_%=,s68
s_cmp_eq_u32 s54,2
s_cselect_b32 s68,Lg1_d1w2_%=-Lg1_pc_%=,s68
s_cmp_eq_u32 s54,3
s_cselect_b32 s68,Lg1_d1w3_%=-Lg1_pc_%=,s68
s_add_u32 s96,s90,s68
s_addc_u32 s97,s91,0
s_mov_b32 s68,Lg1_d0w0_%=-Lg1_pc_%=
s_cmp_eq_u32 s54,1
s_cselect_b32 s68,Lg1_d0w1_%=-Lg1_pc_%=,s68
s_cmp_eq_u32 s54,2
s_cselect_b32 s68,Lg1_d0w2_%=-Lg1_pc_%=,s68
s_cmp_eq_u32 s54,3
s_cselect_b32 s68,Lg1_d0w3_%=-Lg1_pc_%=,s68
s_add_u32 s94,s90,s68
s_addc_u32 s95,s91,0
s_mov_b32 s68,Lg1_a1w0_%=-Lg1_pc_%=
s_cmp_eq_u32 s54,1
s_cselect_b32 s68,Lg1_a1w1_%=-Lg1_pc_%=,s68
s_cmp_eq_u32 s54,2
s_cselect_b32 s68,Lg1_a1w2_%=-Lg1_pc_%=,s68
s_cmp_eq_u32 s54,3
s_cselect_b32 s68,Lg1_a1w3_%=-Lg1_pc_%=,s68
s_add_u32 s92,s90,s68
s_addc_u32 s93,s91,0
s_mov_b32 s68,Lg1_a0w0_%=-Lg1_pc_%=
s_cmp_eq_u32 s54,1
s_cselect_b32 s68,Lg1_a0w1_%=-Lg1_pc_%=,s68
s_cmp_eq_u32 s54,2
s_cselect_b32 s68,Lg1_a0w2_%=-Lg1_pc_%=,s68
s_cmp_eq_u32 s54,3
s_cselect_b32 s68,Lg1_a0w3_%=-Lg1_pc_%=,s68
s_add_u32 s90,s90,s68
s_addc_u32 s91,s91,0
s_mov_b32 s66,s59
s_mov_b32 s65,0
s_mov_b32 s62,s63
s_branch Lg1_slow0_%=
Lg1_a0w0_%=:
v_mfma_f32_16x16x32_bf16 v[28:31],a[0:3],v[44:47],0
s_add_u32 m0,s62,16384
ds_read_b128 v[72:75],v2 offset:7168
s_sub_u32 s69,s66,1048576
v_mfma_f32_16x16x32_bf16 v[32:35],a[4:7],v[44:47],0
buffer_load_dwordx4 v1,s[36:39],s66 offen offset:0 sc1 lds
v_mfma_f32_16x16x32_bf16 v[104:107],v[100:103],v[44:47],0
s_cmp_lt_u32 s69,2097152
s_cselect_b32 s89,3145728,s89
s_sub_u32 s69,s66,3145728
v_mfma_f32_16x16x32_bf16 v[108:111],v[44:47],v[44:47],0
s_cmp_lt_u32 s69,1048576
v_fma_f32 v36,-v96,v20,v36
v_fma_f32 v37,-v96,v21,v37
v_mfma_f32_16x16x32_bf16 v[28:31],a[8:11],v[48:51],v[28:31]
s_cselect_b32 s89,4194304,s89
ds_read_b128 v[44:47],v2 offset:8192
v_fma_f32 v38,-v96,v22,v38
v_mfma_f32_16x16x32_bf16 v[32:35],a[12:15],v[48:51],v[32:35]
buffer_load_dwordx4 v1,s[36:39],s66 offen offset:1024 sc1 lds
v_mfma_f32_16x16x32_bf16 v[104:107],v[100:103],v[48:51],v[104:107]
s_lshr_b32 s73,s47,0
v_fma_f32 v39,-v96,v23,v39
v_mul_f32 v76,v36,v36
v_mfma_f32_16x16x32_bf16 v[108:111],v[48:51],v[48:51],v[108:111]
s_lshr_b32 s72,s66,6
v_mul_f32 v77,v37,v37
v_mul_f32 v78,v38,v38
v_mfma_f32_16x16x32_bf16 v[28:31],a[16:19],v[52:55],v[28:31]
s_lshr_b32 s71,s62,2
ds_read_b128 v[48:51],v2 offset:9216
v_mul_f32 v79,v39,v39
v_mfma_f32_16x16x32_bf16 v[32:35],a[20:23],v[52:55],v[32:35]
buffer_load_dwordx4 v1,s[36:39],s66 offen offset:2048 sc1 lds
v_mfma_f32_16x16x32_bf16 v[104:107],v[100:103],v[52:55],v[104:107]
s_cmp_lt_u32 s66,4194304)"
R"(
v_fma_f32 v76,v98,v76,v97
v_mul_f32 v76,v36,v76
v_mfma_f32_16x16x32_bf16 v[108:111],v[52:55],v[52:55],v[108:111]
s_cselect_b32 s72,s59,s72
v_exp_f32 v76,v76
v_mfma_f32_16x16x32_bf16 v[28:31],a[24:27],v[56:59],v[28:31]
ds_read_b128 v[52:55],v2 offset:10240
v_fma_f32 v76,v76,v99,v99
v_mfma_f32_16x16x32_bf16 v[32:35],a[28:31],v[56:59],v[32:35]
buffer_load_dwordx4 v1,s[36:39],s66 offen offset:3072 sc1 lds
v_mfma_f32_16x16x32_bf16 v[104:107],v[100:103],v[56:59],v[104:107]
s_add_u32 m0,s71,s88
v_rcp_f32 v76,v76
v_mfma_f32_16x16x32_bf16 v[108:111],v[56:59],v[56:59],v[108:111]
s_waitcnt lgkmcnt(3)
buffer_load_dword v10,s[36:39],s72 offen offset:0 sc1 lds
v_mfma_f32_16x16x32_bf16 v[28:31],a[32:35],v[60:63],v[28:31]
s_xor_b32 s87,s64,65536
ds_read_b128 v[56:59],v2 offset:11264
v_fma_f32 v77,v98,v77,v97
v_mfma_f32_16x16x32_bf16 v[32:35],a[36:39],v[60:63],v[32:35]
s_cmp_ge_u32 s66,s59
v_mul_f32 v77,v37,v77
v_fma_f32 v78,v98,v78,v97
v_mfma_f32_16x16x32_bf16 v[28:31],a[40:43],v[64:67],v[28:31]
s_cselect_b32 s86,s49,s50
ds_read_b128 v[60:63],v2 offset:12288
v_mul_f32 v78,v38,v78
v_mfma_f32_16x16x32_bf16 v[32:35],a[44:47],v[64:67],v[32:35]
s_cselect_b32 s87,s87,s63
v_exp_f32 v77,v77
v_mfma_f32_16x16x32_bf16 v[28:31],a[48:51],v[68:71],v[28:31]
s_mov_b32 s62,s87
ds_read_b128 v[64:67],v2 offset:13312
v_fma_f32 v77,v77,v99,v99
v_mfma_f32_16x16x32_bf16 v[32:35],a[52:55],v[68:71],v[32:35]
s_cmp_lt_u32 s86,s89
v_exp_f32 v78,v78
v_mfma_f32_16x16x32_bf16 v[28:31],a[56:59],v[72:75],v[28:31]
ds_read_b128 v[68:71],v2 offset:14336
v_fma_f32 v78,v78,v99,v99
v_mfma_f32_16x16x32_bf16 v[32:35],a[60:63],v[72:75],v[32:35]
s_waitcnt lgkmcnt(3)
v_rcp_f32 v77,v77
v_mfma_f32_16x16x32_bf16 v[28:31],a[64:67],v[44:47],v[28:31]
s_waitcnt vmcnt(10)
ds_read_b128 v[72:75],v2 offset:15360
v_fma_f32 v79,v98,v79,v97
v_mfma_f32_16x16x32_bf16 v[32:35],a[68:71],v[44:47],v[32:35]
s_barrier
v_mfma_f32_16x16x32_bf16 v[28:31],a[72:75],v[48:51],v[28:31]
ds_read_b128 v[44:47],v2 offset:16384
ds_read_b32 v16,v7 offset:0
v_mfma_f32_16x16x32_bf16 v[32:35],a[76:79],v[48:51],v[32:35]
v_rcp_f32 v78,v78
v_mfma_f32_16x16x32_bf16 v[28:31],a[80:83],v[52:55],v[28:31]
ds_read_b128 v[48:51],v2 offset:17408
v_mul_f32 v79,v39,v79
v_mfma_f32_16x16x32_bf16 v[32:35],a[84:87],v[52:55],v[32:35]
v_exp_f32 v79,v79
v_mfma_f32_16x16x32_bf16 v[28:31],a[88:91],v[56:59],v[28:31]
s_waitcnt lgkmcnt(1)
ds_read_b128 v[52:55],v2 offset:18432
v_fma_f32 v79,v79,v99,v99
v_mfma_f32_16x16x32_bf16 v[32:35],a[92:95],v[56:59],v[32:35]
v_cmp_eq_u32 vcc,s60,v16
v_rcp_f32 v79,v79
v_mul_f32 v36,v36,v76
v_mfma_f32_16x16x32_bf16 v[28:31],a[96:99],v[60:63],v[28:31]
s_cselect_b32 vcc_lo,-1,vcc_lo
ds_read_b128 v[56:59],v2 offset:19456
v_mul_f32 v37,v37,v77
v_mfma_f32_16x16x32_bf16 v[32:35],a[100:103],v[60:63],v[32:35]
s_cmp_eq_u32 vcc_lo,-1
v_mul_f32 v38,v38,v78
v_mul_f32 v39,v39,v79
v_mfma_f32_16x16x32_bf16 v[28:31],a[104:107],v[64:67],v[28:31]
s_cselect_b32 s67,s86,s59
ds_read_b128 v[60:63],v2 offset:20480
v_cvt_pk_bf16_f32 v84,v36,v37
v_mfma_f32_16x16x32_bf16 v[32:35],a[108:111],v[64:67],v[32:35]
s_lshr_b32 s69,s50,5
v_cvt_pk_bf16_f32 v85,v38,v39
v_fma_f32 v40,-v96,v24,v40
v_mfma_f32_16x16x32_bf16 v[28:31],a[112:115],v[68:71],v[28:31]
s_cmp_lt_u32 s50,4194304
ds_read_b128 v[64:67],v2 offset:21504
v_fma_f32 v41,-v96,v25,v41
v_mfma_f32_16x16x32_bf16 v[32:35],a[116:119],v[68:71],v[32:35]
s_cselect_b32 s68,0xffffcfff,-1
v_fma_f32 v42,-v96,v26,v42
v_fma_f32 v43,-v96,v27,v43
v_mfma_f32_16x16x32_bf16 v[28:31],a[120:123],v[72:75],v[28:31]
s_and_b32 s68,s68,s69
ds_read_b128 v[68:71],v2 offset:22528
v_mul_f32 v80,v40,v40
v_mfma_f32_16x16x32_bf16 v[32:35],a[124:127],v[72:75],v[32:35]
s_waitcnt lgkmcnt(3)
v_mul_f32 v81,v41,v41
v_mul_f32 v82,v42,v42
v_mfma_f32_16x16x32_bf16 v[28:31],a[128:131],v[44:47],v[28:31]
s_cmp_lt_u32 s50,s89
ds_read_b128 v[72:75],v2 offset:23552
v_mul_f32 v83,v43,v43
v_mfma_f32_16x16x32_bf16 v[32:35],a[132:135],v[44:47],v[32:35]
s_cselect_b32 s68,s59,s68
v_fma_f32 v80,v98,v80,v97
v_mul_f32 v80,v40,v80
v_mfma_f32_16x16x32_bf16 v[104:107],v[100:103],v[44:47],v[104:107]
s_lshr_b32 s69,s51,5
v_exp_f32 v80,v80
v_mfma_f32_16x16x32_bf16 v[108:111],v[44:47],v[44:47],v[108:111]
s_cmp_lt_u32 s51,4194304
v_fma_f32 v80,v80,v99,v99
v_fma_f32 v81,v98,v81,v97
v_mfma_f32_16x16x32_bf16 v[28:31],a[136:139],v[48:51],v[28:31]
s_cselect_b32 s70,0xffffcfff,-1
ds_read_b128 v[44:47],v2 offset:24576
v_mul_f32 v81,v41,v81
v_mfma_f32_16x16x32_bf16 v[32:35],a[140:143],v[48:51],v[32:35]
s_and_b32 s70,s70,s69
v_exp_f32 v81,v81
v_mfma_f32_16x16x32_bf16 v[104:107],v[100:103],v[48:51],v[104:107]
s_cmp_lt_u32 s51,s89
v_rcp_f32 v80,v80
v_mfma_f32_16x16x32_bf16 v[108:111],v[48:51],v[48:51],v[108:111]
s_cselect_b32 s70,s59,s70
v_fma_f32 v81,v81,v99,v99
v_fma_f32 v82,v98,v82,v97
v_mfma_f32_16x16x32_bf16 v[28:31],a[144:147],v[52:55],v[28:31]
s_add_u32 s53,s52,s55
ds_read_b128 v[48:51],v2 offset:25600
v_mul_f32 v82,v42,v82
v_mfma_f32_16x16x32_bf16 v[32:35],a[148:151],v[52:55],v[32:35]
s_cmp_eq_u32 s53,s56
v_exp_f32 v82,v82
v_mfma_f32_16x16x32_bf16 v[104:107],v[100:103],v[52:55],v[104:107]
s_cselect_b32 s53,s57,s53
v_rcp_f32 v81,v81
v_mfma_f32_16x16x32_bf16 v[108:111],v[52:55],v[52:55],v[108:111]
s_cmp_eq_u32 s53,s58
v_fma_f32 v82,v82,v99,v99
v_fma_f32 v83,v98,v83,v97
v_mfma_f32_16x16x32_bf16 v[28:31],a[152:155],v[56:59],v[28:31]
s_cselect_b32 s53,s59,s53
ds_read_b128 v[52:55],v2 offset:26624
v_mul_f32 v83,v43,v83
v_mfma_f32_16x16x32_bf16 v[32:35],a[156:159],v[56:59],v[32:35]
s_xor_b32 s63,s63,65536
v_exp_f32 v83,v83
v_mfma_f32_16x16x32_bf16 v[104:107],v[100:103],v[56:59],v[104:107]
s_cmp_ge_u32 s67,s59
v_rcp_f32 v82,v82
v_mfma_f32_16x16x32_bf16 v[108:111],v[56:59],v[56:59],v[108:111]
s_waitcnt lgkmcnt(3)
v_fma_f32 v83,v83,v99,v99
v_mul_f32 v40,v40,v80
v_mfma_f32_16x16x32_bf16 v[28:31],a[160:163],v[60:63],v[28:31]
s_cselect_b32 s68,s68,s70
ds_read_b128 v[56:59],v2 offset:27648
v_mul_f32 v41,v41,v81
v_mfma_f32_16x16x32_bf16 v[32:35],a[164:167],v[60:63],v[32:35]
s_add_u32 m0,s78,256
v_rcp_f32 v83,v83
v_mfma_f32_16x16x32_bf16 v[28:31],a[168:171],v[64:67],v[28:31]
buffer_load_dword v6,s[36:39],s68 offen offset:0 sc1 lds
v_cndmask_b32 v112,v108,v109,s[82:83]
v_cndmask_b32 v113,v110,v111,s[82:83]
v_cndmask_b32 v112,v112,v113,s[84:85]
v_cndmask_b32 v112,v104,v112,s[98:99]
v_mfma_f32_16x16x32_bf16 v[32:35],a[172:175],v[64:67],v[32:35]
s_mov_b32 m0,s62
ds_read_b128 v[60:63],v2 offset:28672
ds_write_b32 v13,v112 offset:0
v_mfma_f32_16x16x32_bf16 v[28:31],a[176:179],v[68:71],v[28:31]
buffer_load_dwordx4 v0,s[36:39],s67 offen offset:0 sc1 lds
v_mfma_f32_16x16x32_bf16 v[32:35],a[180:183],v[68:71],v[32:35]
s_cmp_ge_u32 s66,s59
ds_read_b128 v[64:67],v2 offset:29696
v_mul_f32 v42,v42,v82
v_mfma_f32_16x16x32_bf16 v[28:31],a[184:187],v[72:75],v[28:31]
ds_read_b128 v[68:71],v2 offset:30720
v_mul_f32 v43,v43,v83
v_mfma_f32_16x16x32_bf16 v[32:35],a[188:191],v[72:75],v[32:35]
s_waitcnt lgkmcnt(4)
v_cvt_pk_bf16_f32 v86,v40,v41
v_cvt_pk_bf16_f32 v87,v42,v43
v_mfma_f32_16x16x32_bf16 v[28:31],a[192:195],v[44:47],v[28:31]
s_waitcnt vmcnt(7) lgkmcnt(2)
ds_read_b128 v[72:75],v2 offset:31744
v_mfma_f32_16x16x32_bf16 v[32:35],a[196:199],v[44:47],v[32:35]
s_barrier
v_mfma_f32_16x16x32_bf16 v[28:31],a[200:203],v[48:51],v[28:31]
buffer_load_dwordx4 v0,s[36:39],s67 offen offset:1024 sc1 lds
v_mfma_f32_16x16x32_bf16 v[32:35],a[204:207],v[48:51],v[32:35]
ds_read_b128 v[44:47],v2 offset:32768
ds_read_b128 v[116:119],v14 offset:0
v_mfma_f32_16x16x32_bf16 v[28:31],a[208:211],v[52:55],v[28:31]
ds_read_b128 v[120:123],v14 offset:16
ds_read_b128 v[48:51],v2 offset:33792)"
R"(
v_mfma_f32_16x16x32_bf16 v[32:35],a[212:215],v[52:55],v[32:35]
v_mfma_f32_16x16x32_bf16 v[28:31],a[216:219],v[56:59],v[28:31]
buffer_load_dwordx4 v0,s[36:39],s67 offen offset:2048 sc1 lds
v_mfma_f32_16x16x32_bf16 v[32:35],a[220:223],v[56:59],v[32:35]
s_waitcnt lgkmcnt(4)
ds_read_b128 v[52:55],v2 offset:34816
v_mfma_f32_16x16x32_bf16 v[28:31],a[224:227],v[60:63],v[28:31]
s_waitcnt lgkmcnt(2)
ds_read_b128 v[56:59],v2 offset:35840
v_add_f32 v116,v116,v117
v_mfma_f32_16x16x32_bf16 v[32:35],a[228:231],v[60:63],v[32:35]
v_add_f32 v118,v118,v119
v_add_f32 v116,v116,v118
v_mfma_f32_16x16x32_bf16 v[28:31],a[232:235],v[64:67],v[28:31]
buffer_load_dwordx4 v0,s[36:39],s67 offen offset:3072 sc1 lds
v_mfma_f32_16x16x32_bf16 v[32:35],a[236:239],v[64:67],v[32:35]
ds_read_b128 v[60:63],v2 offset:36864
v_mul_f32 v92,s75,v116
v_mfma_f32_16x16x32_bf16 v[28:31],a[240:243],v[68:71],v[28:31]
ds_read_b128 v[64:67],v2 offset:37888
v_add_f32 v120,v120,v121
v_mfma_f32_16x16x32_bf16 v[32:35],a[244:247],v[68:71],v[32:35]
v_add_f32 v122,v122,v123
v_add_f32 v120,v120,v122
v_mul_f32 v120,s75,v120
v_fma_f32 v120,-v92,v92,v120
v_max_f32 v120,0,v120
v_add_f32 v115,v15,v120
v_fma_f32 v120,v15,v115,v120
v_rsq_f32 v116,v120
v_mfma_f32_16x16x32_bf16 v[28:31],a[248:251],v[72:75],v[28:31]
s_waitcnt lgkmcnt(2)
ds_read_b128 v[68:71],v2 offset:38912
buffer_store_dwordx4 v[84:87],v3,s[36:39],s73 offen offset:0 sc0 sc1
v_mul_f32 v93,s76,v116
v_mul_f32 v94,v116,v116
v_mul_f32 v95,v120,v116
v_mul_f32 v94,s77,v94
v_mul_f32 v94,v94,v93
v_mfma_f32_16x16x32_bf16 v[32:35],a[252:255],v[72:75],v[32:35]
s_cbranch_scc1 Lg1_slow1_%=
Lg1_a1w0_%=:
v_mfma_f32_16x16x32_bf16 v[36:39],a[0:3],v[44:47],0
s_add_u32 m0,s62,16384
ds_read_b128 v[72:75],v2 offset:39936
s_sub_u32 s69,s67,1048576
v_mfma_f32_16x16x32_bf16 v[40:43],a[4:7],v[44:47],0
buffer_load_dwordx4 v1,s[36:39],s67 offen offset:0 sc1 lds
v_mfma_f32_16x16x32_bf16 v[104:107],v[100:103],v[44:47],0
s_cmp_lt_u32 s69,2097152
s_cselect_b32 s89,3145728,s89
s_sub_u32 s69,s67,3145728
v_mfma_f32_16x16x32_bf16 v[108:111],v[44:47],v[44:47],0
s_cmp_lt_u32 s69,1048576
v_fma_f32 v28,-v92,v20,v28
v_fma_f32 v29,-v92,v21,v29
v_mfma_f32_16x16x32_bf16 v[36:39],a[8:11],v[48:51],v[36:39]
s_cselect_b32 s89,4194304,s89
ds_read_b128 v[44:47],v2 offset:40960
v_fma_f32 v30,-v92,v22,v30
v_mfma_f32_16x16x32_bf16 v[40:43],a[12:15],v[48:51],v[40:43]
buffer_load_dwordx4 v1,s[36:39],s67 offen offset:1024 sc1 lds
v_mfma_f32_16x16x32_bf16 v[104:107],v[100:103],v[48:51],v[104:107]
s_lshr_b32 s73,s48,0
v_fma_f32 v31,-v92,v23,v31
v_mul_f32 v76,v28,v28
v_mfma_f32_16x16x32_bf16 v[108:111],v[48:51],v[48:51],v[108:111]
s_lshr_b32 s72,s67,6
v_mul_f32 v77,v29,v29
v_mul_f32 v78,v30,v30
v_mfma_f32_16x16x32_bf16 v[36:39],a[16:19],v[52:55],v[36:39]
s_lshr_b32 s71,s62,2
ds_read_b128 v[48:51],v2 offset:41984
v_mul_f32 v79,v31,v31
v_mfma_f32_16x16x32_bf16 v[40:43],a[20:23],v[52:55],v[40:43]
buffer_load_dwordx4 v1,s[36:39],s67 offen offset:2048 sc1 lds
v_mfma_f32_16x16x32_bf16 v[104:107],v[100:103],v[52:55],v[104:107]
s_cmp_lt_u32 s67,4194304
v_fma_f32 v76,v94,v76,v93
v_mul_f32 v76,v28,v76
v_mfma_f32_16x16x32_bf16 v[108:111],v[52:55],v[52:55],v[108:111]
s_cselect_b32 s72,s59,s72
v_exp_f32 v76,v76
v_mfma_f32_16x16x32_bf16 v[36:39],a[24:27],v[56:59],v[36:39]
s_mov_b64 s[44:45],s[46:47]
ds_read_b128 v[52:55],v2 offset:43008
v_fma_f32 v76,v76,v95,v95
v_mfma_f32_16x16x32_bf16 v[40:43],a[28:31],v[56:59],v[40:43]
buffer_load_dwordx4 v1,s[36:39],s67 offen offset:3072 sc1 lds
v_mfma_f32_16x16x32_bf16 v[104:107],v[100:103],v[56:59],v[104:107]
s_add_u32 m0,s71,s88
v_rcp_f32 v76,v76
v_mfma_f32_16x16x32_bf16 v[108:111],v[56:59],v[56:59],v[108:111]
s_waitcnt lgkmcnt(3)
buffer_load_dword v10,s[36:39],s72 offen offset:0 sc1 lds
v_mfma_f32_16x16x32_bf16 v[36:39],a[32:35],v[60:63],v[36:39]
s_xor_b32 s87,s63,65536
ds_read_b128 v[56:59],v2 offset:44032
v_fma_f32 v77,v94,v77,v93
v_mfma_f32_16x16x32_bf16 v[40:43],a[36:39],v[60:63],v[40:43]
s_cmp_ge_u32 s67,s59
v_mul_f32 v77,v29,v77
v_fma_f32 v78,v94,v78,v93
v_mfma_f32_16x16x32_bf16 v[36:39],a[40:43],v[64:67],v[36:39]
s_cselect_b32 s86,s50,s51
ds_read_b128 v[60:63],v2 offset:45056
v_mul_f32 v78,v30,v78
v_mfma_f32_16x16x32_bf16 v[40:43],a[44:47],v[64:67],v[40:43]
s_cselect_b32 s87,s87,s64
v_exp_f32 v77,v77
v_mfma_f32_16x16x32_bf16 v[36:39],a[48:51],v[68:71],v[36:39]
s_mov_b32 s62,s87
ds_read_b128 v[64:67],v2 offset:46080
v_fma_f32 v77,v77,v95,v95
v_mfma_f32_16x16x32_bf16 v[40:43],a[52:55],v[68:71],v[40:43]
s_cmp_lt_u32 s86,s89
v_exp_f32 v78,v78
v_mfma_f32_16x16x32_bf16 v[36:39],a[56:59],v[72:75],v[36:39]
s_mov_b64 s[46:47],s[48:49]
ds_read_b128 v[68:71],v2 offset:47104
v_fma_f32 v78,v78,v95,v95
v_mfma_f32_16x16x32_bf16 v[40:43],a[60:63],v[72:75],v[40:43]
s_waitcnt lgkmcnt(3)
v_rcp_f32 v77,v77
v_mfma_f32_16x16x32_bf16 v[36:39],a[64:67],v[44:47],v[36:39]
s_waitcnt vmcnt(10)
ds_read_b128 v[72:75],v2 offset:48128
v_fma_f32 v79,v94,v79,v93
v_mfma_f32_16x16x32_bf16 v[40:43],a[68:71],v[44:47],v[40:43]
s_barrier
v_mfma_f32_16x16x32_bf16 v[36:39],a[72:75],v[48:51],v[36:39]
s_mov_b64 s[48:49],s[50:51]
ds_read_b128 v[44:47],v2 offset:49152
ds_read_b32 v16,v7 offset:256
v_mfma_f32_16x16x32_bf16 v[40:43],a[76:79],v[48:51],v[40:43]
v_rcp_f32 v78,v78
v_mfma_f32_16x16x32_bf16 v[36:39],a[80:83],v[52:55],v[36:39]
ds_read_b128 v[48:51],v2 offset:50176
v_mul_f32 v79,v31,v79
v_mfma_f32_16x16x32_bf16 v[40:43],a[84:87],v[52:55],v[40:43]
v_exp_f32 v79,v79
v_mfma_f32_16x16x32_bf16 v[36:39],a[88:91],v[56:59],v[36:39]
s_waitcnt lgkmcnt(1)
ds_read_b128 v[52:55],v2 offset:51200
v_fma_f32 v79,v79,v95,v95
v_mfma_f32_16x16x32_bf16 v[40:43],a[92:95],v[56:59],v[40:43]
v_cmp_eq_u32 vcc,s60,v16
v_rcp_f32 v79,v79
v_mul_f32 v28,v28,v76
v_mfma_f32_16x16x32_bf16 v[36:39],a[96:99],v[60:63],v[36:39]
s_cselect_b32 vcc_lo,-1,vcc_lo
ds_read_b128 v[56:59],v2 offset:52224
v_mul_f32 v29,v29,v77
v_mfma_f32_16x16x32_bf16 v[40:43],a[100:103],v[60:63],v[40:43]
s_cmp_eq_u32 vcc_lo,-1
v_mul_f32 v30,v30,v78
v_mul_f32 v31,v31,v79
v_mfma_f32_16x16x32_bf16 v[36:39],a[104:107],v[64:67],v[36:39]
s_cselect_b32 s66,s86,s59
ds_read_b128 v[60:63],v2 offset:53248
v_cvt_pk_bf16_f32 v84,v28,v29
v_mfma_f32_16x16x32_bf16 v[40:43],a[108:111],v[64:67],v[40:43]
s_lshr_b32 s69,s51,5
v_cvt_pk_bf16_f32 v85,v30,v31
v_fma_f32 v32,-v92,v24,v32
v_mfma_f32_16x16x32_bf16 v[36:39],a[112:115],v[68:71],v[36:39]
s_cmp_lt_u32 s51,4194304
ds_read_b128 v[64:67],v2 offset:54272
v_fma_f32 v33,-v92,v25,v33
v_mfma_f32_16x16x32_bf16 v[40:43],a[116:119],v[68:71],v[40:43]
s_cselect_b32 s68,0xffffcfff,-1
v_fma_f32 v34,-v92,v26,v34
v_fma_f32 v35,-v92,v27,v35
v_mfma_f32_16x16x32_bf16 v[36:39],a[120:123],v[72:75],v[36:39]
s_and_b32 s68,s68,s69
ds_read_b128 v[68:71],v2 offset:55296
v_mul_f32 v80,v32,v32
v_mfma_f32_16x16x32_bf16 v[40:43],a[124:127],v[72:75],v[40:43]
s_waitcnt lgkmcnt(3)
v_mul_f32 v81,v33,v33
v_mul_f32 v82,v34,v34
v_mfma_f32_16x16x32_bf16 v[36:39],a[128:131],v[44:47],v[36:39]
s_cmp_lt_u32 s51,s89
ds_read_b128 v[72:75],v2 offset:56320
v_mul_f32 v83,v35,v35
v_mfma_f32_16x16x32_bf16 v[40:43],a[132:135],v[44:47],v[40:43]
s_cselect_b32 s68,s59,s68
v_fma_f32 v80,v94,v80,v93
v_mul_f32 v80,v32,v80
v_mfma_f32_16x16x32_bf16 v[104:107],v[100:103],v[44:47],v[104:107]
s_lshr_b32 s69,s52,5
v_exp_f32 v80,v80
v_mfma_f32_16x16x32_bf16 v[108:111],v[44:47],v[44:47],v[108:111]
s_cmp_lt_u32 s52,4194304
v_fma_f32 v80,v80,v95,v95
v_fma_f32 v81,v94,v81,v93
v_mfma_f32_16x16x32_bf16 v[36:39],a[136:139],v[48:51],v[36:39]
s_cselect_b32 s70,0xffffcfff,-1
ds_read_b128 v[44:47],v2 offset:57344
v_mul_f32 v81,v33,v81)"
R"(
v_mfma_f32_16x16x32_bf16 v[40:43],a[140:143],v[48:51],v[40:43]
s_and_b32 s70,s70,s69
v_exp_f32 v81,v81
v_mfma_f32_16x16x32_bf16 v[104:107],v[100:103],v[48:51],v[104:107]
s_cmp_lt_u32 s52,s89
v_rcp_f32 v80,v80
v_mfma_f32_16x16x32_bf16 v[108:111],v[48:51],v[48:51],v[108:111]
s_cselect_b32 s70,s59,s70
v_fma_f32 v81,v81,v95,v95
v_fma_f32 v82,v94,v82,v93
v_mfma_f32_16x16x32_bf16 v[36:39],a[144:147],v[52:55],v[36:39]
s_add_u32 s74,s53,s55
ds_read_b128 v[48:51],v2 offset:58368
v_mul_f32 v82,v34,v82
v_mfma_f32_16x16x32_bf16 v[40:43],a[148:151],v[52:55],v[40:43]
s_cmp_eq_u32 s74,s56
v_exp_f32 v82,v82
v_mfma_f32_16x16x32_bf16 v[104:107],v[100:103],v[52:55],v[104:107]
s_cselect_b32 s74,s57,s74
v_rcp_f32 v81,v81
v_mfma_f32_16x16x32_bf16 v[108:111],v[52:55],v[52:55],v[108:111]
s_cmp_eq_u32 s74,s58
v_fma_f32 v82,v82,v95,v95
v_fma_f32 v83,v94,v83,v93
v_mfma_f32_16x16x32_bf16 v[36:39],a[152:155],v[56:59],v[36:39]
s_cselect_b32 s74,s59,s74
ds_read_b128 v[52:55],v2 offset:59392
v_mul_f32 v83,v35,v83
v_mfma_f32_16x16x32_bf16 v[40:43],a[156:159],v[56:59],v[40:43]
s_xor_b32 s64,s64,65536
v_exp_f32 v83,v83
v_mfma_f32_16x16x32_bf16 v[104:107],v[100:103],v[56:59],v[104:107]
s_cmp_ge_u32 s66,s59
v_rcp_f32 v82,v82
v_mfma_f32_16x16x32_bf16 v[108:111],v[56:59],v[56:59],v[108:111]
s_waitcnt lgkmcnt(3)
v_fma_f32 v83,v83,v95,v95
v_mul_f32 v32,v32,v80
v_mfma_f32_16x16x32_bf16 v[36:39],a[160:163],v[60:63],v[36:39]
s_cselect_b32 s68,s68,s70
ds_read_b128 v[56:59],v2 offset:60416
v_mul_f32 v33,v33,v81
v_mfma_f32_16x16x32_bf16 v[40:43],a[164:167],v[60:63],v[40:43]
s_add_u32 m0,s78,0
v_rcp_f32 v83,v83
v_mfma_f32_16x16x32_bf16 v[36:39],a[168:171],v[64:67],v[36:39]
buffer_load_dword v6,s[36:39],s68 offen offset:0 sc1 lds
v_cndmask_b32 v112,v108,v109,s[82:83]
v_cndmask_b32 v113,v110,v111,s[82:83]
v_cndmask_b32 v112,v112,v113,s[84:85]
v_cndmask_b32 v112,v104,v112,s[98:99]
v_mfma_f32_16x16x32_bf16 v[40:43],a[172:175],v[64:67],v[40:43]
s_mov_b32 m0,s62
ds_read_b128 v[60:63],v2 offset:61440
ds_write_b32 v13,v112 offset:2048
v_mfma_f32_16x16x32_bf16 v[36:39],a[176:179],v[68:71],v[36:39]
buffer_load_dwordx4 v0,s[36:39],s66 offen offset:0 sc1 lds
v_mfma_f32_16x16x32_bf16 v[40:43],a[180:183],v[68:71],v[40:43]
s_mov_b64 s[50:51],s[52:53]
ds_read_b128 v[64:67],v2 offset:62464
v_mul_f32 v34,v34,v82
v_mfma_f32_16x16x32_bf16 v[36:39],a[184:187],v[72:75],v[36:39]
s_mov_b32 s52,s74
ds_read_b128 v[68:71],v2 offset:63488
v_mul_f32 v35,v35,v83
v_mfma_f32_16x16x32_bf16 v[40:43],a[188:191],v[72:75],v[40:43]
s_waitcnt lgkmcnt(4)
v_cvt_pk_bf16_f32 v86,v32,v33
v_cvt_pk_bf16_f32 v87,v34,v35
v_mfma_f32_16x16x32_bf16 v[36:39],a[192:195],v[44:47],v[36:39]
s_waitcnt vmcnt(7) lgkmcnt(2)
ds_read_b128 v[72:75],v2 offset:64512
v_xor_b32 v2,65536,v2
v_mfma_f32_16x16x32_bf16 v[40:43],a[196:199],v[44:47],v[40:43]
s_barrier
v_mfma_f32_16x16x32_bf16 v[36:39],a[200:203],v[48:51],v[36:39]
buffer_load_dwordx4 v0,s[36:39],s66 offen offset:1024 sc1 lds
v_mfma_f32_16x16x32_bf16 v[40:43],a[204:207],v[48:51],v[40:43]
s_cmp_lt_u32 s67,4194304
ds_read_b128 v[44:47],v2 offset:0
ds_read_b128 v[116:119],v14 offset:2048
v_mfma_f32_16x16x32_bf16 v[36:39],a[208:211],v[52:55],v[36:39]
ds_read_b128 v[120:123],v14 offset:2064
ds_read_b128 v[48:51],v2 offset:1024
v_mfma_f32_16x16x32_bf16 v[40:43],a[212:215],v[52:55],v[40:43]
v_xor_b32 v8,16384,v8
v_mfma_f32_16x16x32_bf16 v[36:39],a[216:219],v[56:59],v[36:39]
buffer_load_dwordx4 v0,s[36:39],s66 offen offset:2048 sc1 lds
v_mfma_f32_16x16x32_bf16 v[40:43],a[220:223],v[56:59],v[40:43]
s_waitcnt lgkmcnt(4)
ds_read_b128 v[52:55],v2 offset:2048
v_mfma_f32_16x16x32_bf16 v[36:39],a[224:227],v[60:63],v[36:39]
s_waitcnt lgkmcnt(2)
ds_read_b128 v[56:59],v2 offset:3072
v_add_f32 v116,v116,v117
v_mfma_f32_16x16x32_bf16 v[40:43],a[228:231],v[60:63],v[40:43]
v_add_f32 v118,v118,v119
v_add_f32 v116,v116,v118
v_mfma_f32_16x16x32_bf16 v[36:39],a[232:235],v[64:67],v[36:39]
buffer_load_dwordx4 v0,s[36:39],s66 offen offset:3072 sc1 lds
v_mfma_f32_16x16x32_bf16 v[40:43],a[236:239],v[64:67],v[40:43]
ds_read_b128 v[60:63],v2 offset:4096
v_mul_f32 v96,s75,v116
v_mfma_f32_16x16x32_bf16 v[36:39],a[240:243],v[68:71],v[36:39]
ds_read_b128 v[64:67],v2 offset:5120
v_add_f32 v120,v120,v121
v_mfma_f32_16x16x32_bf16 v[40:43],a[244:247],v[68:71],v[40:43]
v_add_f32 v122,v122,v123
v_add_f32 v120,v120,v122
v_mul_f32 v120,s75,v120
v_fma_f32 v120,-v96,v96,v120
v_max_f32 v120,0,v120
v_add_f32 v115,v15,v120
v_fma_f32 v120,v15,v115,v120
v_rsq_f32 v116,v120
v_mfma_f32_16x16x32_bf16 v[36:39],a[248:251],v[72:75],v[36:39]
s_waitcnt lgkmcnt(2)
ds_read_b128 v[68:71],v2 offset:6144
buffer_store_dwordx4 v[84:87],v3,s[36:39],s73 offen offset:0 sc0 sc1
v_mul_f32 v97,s76,v116
v_mul_f32 v98,v116,v116
v_mul_f32 v99,v120,v116
v_mul_f32 v98,s77,v98
v_mul_f32 v98,v98,v97
v_mfma_f32_16x16x32_bf16 v[40:43],a[252:255],v[72:75],v[40:43]
s_cbranch_scc1 Lg1_a0w0_%=
s_cmp_ge_u32 s48,4194304
s_cselect_b64 s[90:91],s[94:95],s[90:91]
s_cselect_b64 s[92:93],s[96:97],s[92:93]
s_cmp_lt_u32 s67,s59
s_cbranch_scc1 Lg1_d0w0_%=
s_branch Lg1_slow0_%=
Lg1_a0w1_%=:
v_mfma_f32_16x16x32_bf16 v[28:31],a[32:35],v[44:47],0
s_add_u32 m0,s62,16384
ds_read_b128 v[72:75],v2 offset:3072
s_sub_u32 s69,s66,1048576
v_mfma_f32_16x16x32_bf16 v[32:35],a[36:39],v[44:47],0
s_cmp_lt_u32 s69,2097152
s_cselect_b32 s89,3145728,s89
s_sub_u32 s69,s66,3145728
v_mfma_f32_16x16x32_bf16 v[104:107],v[100:103],v[44:47],0
buffer_load_dwordx4 v1,s[36:39],s66 offen offset:0 sc1 lds
v_mfma_f32_16x16x32_bf16 v[108:111],v[44:47],v[44:47],0
s_cmp_lt_u32 s69,1048576
v_fma_f32 v36,-v96,v20,v36
v_fma_f32 v37,-v96,v21,v37
v_mfma_f32_16x16x32_bf16 v[28:31],a[40:43],v[48:51],v[28:31]
s_cselect_b32 s89,4194304,s89
ds_read_b128 v[44:47],v2 offset:12288
v_fma_f32 v38,-v96,v22,v38
v_mfma_f32_16x16x32_bf16 v[32:35],a[44:47],v[48:51],v[32:35]
s_lshr_b32 s73,s47,0
v_fma_f32 v39,-v96,v23,v39
v_mul_f32 v76,v36,v36
v_mfma_f32_16x16x32_bf16 v[104:107],v[100:103],v[48:51],v[104:107]
buffer_load_dwordx4 v1,s[36:39],s66 offen offset:1024 sc1 lds
v_mfma_f32_16x16x32_bf16 v[108:111],v[48:51],v[48:51],v[108:111]
s_lshr_b32 s72,s66,6
v_mul_f32 v77,v37,v37
v_mul_f32 v78,v38,v38
v_mfma_f32_16x16x32_bf16 v[28:31],a[48:51],v[52:55],v[28:31]
s_lshr_b32 s71,s62,2
ds_read_b128 v[48:51],v2 offset:13312
v_mul_f32 v79,v39,v39
v_mfma_f32_16x16x32_bf16 v[32:35],a[52:55],v[52:55],v[32:35]
s_cmp_lt_u32 s66,4194304
v_fma_f32 v76,v98,v76,v97
v_mul_f32 v76,v36,v76
v_mfma_f32_16x16x32_bf16 v[104:107],v[100:103],v[52:55],v[104:107]
buffer_load_dwordx4 v1,s[36:39],s66 offen offset:2048 sc1 lds
v_mfma_f32_16x16x32_bf16 v[108:111],v[52:55],v[52:55],v[108:111]
s_cselect_b32 s72,s59,s72
v_exp_f32 v76,v76
v_mfma_f32_16x16x32_bf16 v[28:31],a[56:59],v[56:59],v[28:31]
ds_read_b128 v[52:55],v2 offset:14336
v_fma_f32 v76,v76,v99,v99
v_mfma_f32_16x16x32_bf16 v[32:35],a[60:63],v[56:59],v[32:35]
v_rcp_f32 v76,v76
v_mfma_f32_16x16x32_bf16 v[104:107],v[100:103],v[56:59],v[104:107]
buffer_load_dwordx4 v1,s[36:39],s66 offen offset:3072 sc1 lds
v_mfma_f32_16x16x32_bf16 v[108:111],v[56:59],v[56:59],v[108:111]
s_waitcnt lgkmcnt(3)
v_fma_f32 v77,v98,v77,v97
v_mul_f32 v77,v37,v77
v_mfma_f32_16x16x32_bf16 v[28:31],a[0:3],v[60:63],v[28:31]
s_add_u32 m0,s71,s88
ds_read_b128 v[56:59],v2 offset:15360
v_fma_f32 v78,v98,v78,v97
v_mfma_f32_16x16x32_bf16 v[32:35],a[4:7],v[60:63],v[32:35]
buffer_load_dword v10,s[36:39],s72 offen offset:0 sc1 lds
v_mfma_f32_16x16x32_bf16 v[28:31],a[8:11],v[64:67],v[28:31]
s_xor_b32 s87,s64,65536
ds_read_b128 v[60:63],v2 offset:8192
v_mul_f32 v78,v38,v78
v_mfma_f32_16x16x32_bf16 v[32:35],a[12:15],v[64:67],v[32:35]
s_cmp_ge_u32 s66,s59
v_exp_f32 v77,v77
v_mfma_f32_16x16x32_bf16 v[28:31],a[16:19],v[68:71],v[28:31]
s_cselect_b32 s86,s49,s50)"
R"(
ds_read_b128 v[64:67],v2 offset:9216
v_fma_f32 v77,v77,v99,v99
v_mfma_f32_16x16x32_bf16 v[32:35],a[20:23],v[68:71],v[32:35]
s_cselect_b32 s87,s87,s63
v_exp_f32 v78,v78
v_mfma_f32_16x16x32_bf16 v[28:31],a[24:27],v[72:75],v[28:31]
s_mov_b32 s62,s87
ds_read_b128 v[68:71],v2 offset:10240
v_fma_f32 v78,v78,v99,v99
v_mfma_f32_16x16x32_bf16 v[32:35],a[28:31],v[72:75],v[32:35]
s_waitcnt lgkmcnt(3)
v_rcp_f32 v77,v77
v_mfma_f32_16x16x32_bf16 v[28:31],a[96:99],v[44:47],v[28:31]
s_waitcnt vmcnt(10)
ds_read_b128 v[72:75],v2 offset:11264
v_fma_f32 v79,v98,v79,v97
v_mfma_f32_16x16x32_bf16 v[32:35],a[100:103],v[44:47],v[32:35]
s_barrier
v_mfma_f32_16x16x32_bf16 v[28:31],a[104:107],v[48:51],v[28:31]
s_cmp_lt_u32 s86,s89
ds_read_b128 v[44:47],v2 offset:20480
ds_read_b32 v16,v7 offset:0
v_mfma_f32_16x16x32_bf16 v[32:35],a[108:111],v[48:51],v[32:35]
s_mov_b32 m0,s62
v_rcp_f32 v78,v78
v_mfma_f32_16x16x32_bf16 v[28:31],a[112:115],v[52:55],v[28:31]
ds_read_b128 v[48:51],v2 offset:21504
v_mul_f32 v79,v39,v79
v_mfma_f32_16x16x32_bf16 v[32:35],a[116:119],v[52:55],v[32:35]
v_exp_f32 v79,v79
v_mfma_f32_16x16x32_bf16 v[28:31],a[120:123],v[56:59],v[28:31]
s_waitcnt lgkmcnt(1)
ds_read_b128 v[52:55],v2 offset:22528
v_fma_f32 v79,v79,v99,v99
v_mfma_f32_16x16x32_bf16 v[32:35],a[124:127],v[56:59],v[32:35]
v_cmp_eq_u32 vcc,s60,v16
v_rcp_f32 v79,v79
v_mul_f32 v36,v36,v76
v_mfma_f32_16x16x32_bf16 v[28:31],a[64:67],v[60:63],v[28:31]
s_cselect_b32 vcc_lo,-1,vcc_lo
ds_read_b128 v[56:59],v2 offset:23552
v_mul_f32 v37,v37,v77
v_mfma_f32_16x16x32_bf16 v[32:35],a[68:71],v[60:63],v[32:35]
s_cmp_eq_u32 vcc_lo,-1
v_mul_f32 v38,v38,v78
v_mul_f32 v39,v39,v79
v_mfma_f32_16x16x32_bf16 v[28:31],a[72:75],v[64:67],v[28:31]
s_cselect_b32 s67,s86,s59
ds_read_b128 v[60:63],v2 offset:16384
v_cvt_pk_bf16_f32 v84,v36,v37
v_mfma_f32_16x16x32_bf16 v[32:35],a[76:79],v[64:67],v[32:35]
s_add_u32 s53,s52,s55
v_cvt_pk_bf16_f32 v85,v38,v39
v_fma_f32 v40,-v96,v24,v40
v_mfma_f32_16x16x32_bf16 v[28:31],a[80:83],v[68:71],v[28:31]
s_cmp_eq_u32 s53,s56
ds_read_b128 v[64:67],v2 offset:17408
v_fma_f32 v41,-v96,v25,v41
v_mfma_f32_16x16x32_bf16 v[32:35],a[84:87],v[68:71],v[32:35]
s_cselect_b32 s53,s57,s53
v_fma_f32 v42,-v96,v26,v42
v_fma_f32 v43,-v96,v27,v43
v_mfma_f32_16x16x32_bf16 v[28:31],a[88:91],v[72:75],v[28:31]
s_cmp_eq_u32 s53,s58
ds_read_b128 v[68:71],v2 offset:18432
v_mul_f32 v80,v40,v40
v_mfma_f32_16x16x32_bf16 v[32:35],a[92:95],v[72:75],v[32:35]
s_waitcnt lgkmcnt(3)
v_mul_f32 v81,v41,v41
v_mul_f32 v82,v42,v42
v_mfma_f32_16x16x32_bf16 v[28:31],a[160:163],v[44:47],v[28:31]
s_cselect_b32 s53,s59,s53
ds_read_b128 v[72:75],v2 offset:19456
v_mul_f32 v83,v43,v43
v_mfma_f32_16x16x32_bf16 v[32:35],a[164:167],v[44:47],v[32:35]
s_xor_b32 s63,s63,65536
v_fma_f32 v80,v98,v80,v97
v_mul_f32 v80,v40,v80
v_mfma_f32_16x16x32_bf16 v[104:107],v[100:103],v[44:47],v[104:107]
s_cmp_ge_u32 s66,s59
v_exp_f32 v80,v80
v_mfma_f32_16x16x32_bf16 v[108:111],v[44:47],v[44:47],v[108:111]
v_fma_f32 v80,v80,v99,v99
v_fma_f32 v81,v98,v81,v97
v_mfma_f32_16x16x32_bf16 v[28:31],a[168:171],v[48:51],v[28:31]
ds_read_b128 v[44:47],v2 offset:28672
v_mul_f32 v81,v41,v81
v_mfma_f32_16x16x32_bf16 v[32:35],a[172:175],v[48:51],v[32:35]
v_exp_f32 v81,v81
v_mfma_f32_16x16x32_bf16 v[104:107],v[100:103],v[48:51],v[104:107]
v_rcp_f32 v80,v80
v_mfma_f32_16x16x32_bf16 v[108:111],v[48:51],v[48:51],v[108:111]
v_fma_f32 v81,v81,v99,v99
v_fma_f32 v82,v98,v82,v97
v_mfma_f32_16x16x32_bf16 v[28:31],a[176:179],v[52:55],v[28:31]
ds_read_b128 v[48:51],v2 offset:29696
v_mul_f32 v82,v42,v82
v_mfma_f32_16x16x32_bf16 v[32:35],a[180:183],v[52:55],v[32:35]
v_exp_f32 v82,v82
v_mfma_f32_16x16x32_bf16 v[104:107],v[100:103],v[52:55],v[104:107]
v_rcp_f32 v81,v81
v_mfma_f32_16x16x32_bf16 v[108:111],v[52:55],v[52:55],v[108:111]
v_fma_f32 v82,v82,v99,v99
v_fma_f32 v83,v98,v83,v97
v_mfma_f32_16x16x32_bf16 v[28:31],a[184:187],v[56:59],v[28:31]
ds_read_b128 v[52:55],v2 offset:30720
v_mul_f32 v83,v43,v83
v_mfma_f32_16x16x32_bf16 v[32:35],a[188:191],v[56:59],v[32:35]
v_exp_f32 v83,v83
v_mfma_f32_16x16x32_bf16 v[104:107],v[100:103],v[56:59],v[104:107]
v_rcp_f32 v82,v82
v_mfma_f32_16x16x32_bf16 v[108:111],v[56:59],v[56:59],v[108:111]
s_waitcnt lgkmcnt(3)
v_fma_f32 v83,v83,v99,v99
v_mul_f32 v40,v40,v80
v_mfma_f32_16x16x32_bf16 v[28:31],a[128:131],v[60:63],v[28:31]
ds_read_b128 v[56:59],v2 offset:31744
v_mul_f32 v41,v41,v81
v_mfma_f32_16x16x32_bf16 v[32:35],a[132:135],v[60:63],v[32:35]
buffer_load_dwordx4 v0,s[36:39],s67 offen offset:0 sc1 lds
v_mfma_f32_16x16x32_bf16 v[28:31],a[136:139],v[64:67],v[28:31]
v_cndmask_b32 v112,v108,v109,s[82:83]
v_cndmask_b32 v113,v110,v111,s[82:83]
v_cndmask_b32 v112,v112,v113,s[84:85]
v_cndmask_b32 v112,v104,v112,s[98:99]
v_mfma_f32_16x16x32_bf16 v[32:35],a[140:143],v[64:67],v[32:35]
ds_read_b128 v[60:63],v2 offset:24576
ds_write_b32 v13,v112 offset:0
v_mfma_f32_16x16x32_bf16 v[28:31],a[144:147],v[68:71],v[28:31]
ds_read_b128 v[64:67],v2 offset:25600
v_mul_f32 v42,v42,v82
v_mfma_f32_16x16x32_bf16 v[32:35],a[148:151],v[68:71],v[32:35]
buffer_load_dwordx4 v0,s[36:39],s67 offen offset:1024 sc1 lds
v_mfma_f32_16x16x32_bf16 v[28:31],a[152:155],v[72:75],v[28:31]
ds_read_b128 v[68:71],v2 offset:26624
v_cvt_pk_bf16_f32 v86,v40,v41
v_mfma_f32_16x16x32_bf16 v[32:35],a[156:159],v[72:75],v[32:35]
s_waitcnt lgkmcnt(4)
v_rcp_f32 v83,v83
v_mfma_f32_16x16x32_bf16 v[28:31],a[224:227],v[44:47],v[28:31]
s_waitcnt vmcnt(7) lgkmcnt(2)
ds_read_b128 v[72:75],v2 offset:27648
v_mul_f32 v43,v43,v83
v_mfma_f32_16x16x32_bf16 v[32:35],a[228:231],v[44:47],v[32:35]
s_barrier
v_mfma_f32_16x16x32_bf16 v[28:31],a[232:235],v[48:51],v[28:31]
ds_read_b128 v[116:119],v14 offset:0
ds_read_b128 v[120:123],v14 offset:16
v_mfma_f32_16x16x32_bf16 v[32:35],a[236:239],v[48:51],v[32:35]
ds_read_b128 v[44:47],v2 offset:36864
buffer_load_dwordx4 v0,s[36:39],s67 offen offset:2048 sc1 lds
v_mfma_f32_16x16x32_bf16 v[28:31],a[240:243],v[52:55],v[28:31]
ds_read_b128 v[48:51],v2 offset:37888
v_cvt_pk_bf16_f32 v87,v42,v43
v_mfma_f32_16x16x32_bf16 v[32:35],a[244:247],v[52:55],v[32:35]
v_mfma_f32_16x16x32_bf16 v[28:31],a[248:251],v[56:59],v[28:31]
s_waitcnt lgkmcnt(2)
ds_read_b128 v[52:55],v2 offset:38912
v_add_f32 v116,v116,v117
v_mfma_f32_16x16x32_bf16 v[32:35],a[252:255],v[56:59],v[32:35]
buffer_load_dwordx4 v0,s[36:39],s67 offen offset:3072 sc1 lds
v_mfma_f32_16x16x32_bf16 v[28:31],a[192:195],v[60:63],v[28:31]
ds_read_b128 v[56:59],v2 offset:39936
v_add_f32 v118,v118,v119
v_mfma_f32_16x16x32_bf16 v[32:35],a[196:199],v[60:63],v[32:35]
v_add_f32 v116,v116,v118
v_mul_f32 v92,s75,v116
v_mfma_f32_16x16x32_bf16 v[28:31],a[200:203],v[64:67],v[28:31]
ds_read_b128 v[60:63],v2 offset:32768
v_add_f32 v120,v120,v121
v_mfma_f32_16x16x32_bf16 v[32:35],a[204:207],v[64:67],v[32:35]
buffer_store_dwordx4 v[84:87],v3,s[36:39],s73 offen offset:0 sc0 sc1
v_add_f32 v122,v122,v123
v_add_f32 v120,v120,v122
v_mul_f32 v120,s75,v120
v_mfma_f32_16x16x32_bf16 v[28:31],a[208:211],v[68:71],v[28:31]
ds_read_b128 v[64:67],v2 offset:33792
v_fma_f32 v120,-v92,v92,v120
v_mfma_f32_16x16x32_bf16 v[32:35],a[212:215],v[68:71],v[32:35]
v_max_f32 v120,0,v120
v_add_f32 v115,v15,v120
v_fma_f32 v120,v15,v115,v120
v_rsq_f32 v116,v120
v_mfma_f32_16x16x32_bf16 v[28:31],a[216:219],v[72:75],v[28:31]
s_waitcnt lgkmcnt(2)
ds_read_b128 v[68:71],v2 offset:34816
v_mul_f32 v93,s76,v116
v_mul_f32 v94,v116,v116
v_mul_f32 v95,v120,v116
v_mul_f32 v94,s77,v94
v_mul_f32 v94,v94,v93
v_mfma_f32_16x16x32_bf16 v[32:35],a[220:223],v[72:75],v[32:35]
s_cbranch_scc1 Lg1_slow1_%=
Lg1_a1w1_%=:
v_mfma_f32_16x16x32_bf16 v[36:39],a[32:35],v[44:47],0
s_add_u32 m0,s62,16384
ds_read_b128 v[72:75],v2 offset:35840
s_sub_u32 s69,s67,1048576
v_mfma_f32_16x16x32_bf16 v[40:43],a[36:39],v[44:47],0
s_cmp_lt_u32 s69,2097152)"
R"(
s_cselect_b32 s89,3145728,s89
s_sub_u32 s69,s67,3145728
v_mfma_f32_16x16x32_bf16 v[104:107],v[100:103],v[44:47],0
buffer_load_dwordx4 v1,s[36:39],s67 offen offset:0 sc1 lds
v_mfma_f32_16x16x32_bf16 v[108:111],v[44:47],v[44:47],0
s_cmp_lt_u32 s69,1048576
v_fma_f32 v28,-v92,v20,v28
v_fma_f32 v29,-v92,v21,v29
v_mfma_f32_16x16x32_bf16 v[36:39],a[40:43],v[48:51],v[36:39]
s_cselect_b32 s89,4194304,s89
ds_read_b128 v[44:47],v2 offset:45056
v_fma_f32 v30,-v92,v22,v30
v_mfma_f32_16x16x32_bf16 v[40:43],a[44:47],v[48:51],v[40:43]
s_lshr_b32 s73,s48,0
v_fma_f32 v31,-v92,v23,v31
v_mul_f32 v76,v28,v28
v_mfma_f32_16x16x32_bf16 v[104:107],v[100:103],v[48:51],v[104:107]
buffer_load_dwordx4 v1,s[36:39],s67 offen offset:1024 sc1 lds
v_mfma_f32_16x16x32_bf16 v[108:111],v[48:51],v[48:51],v[108:111]
s_lshr_b32 s72,s67,6
v_mul_f32 v77,v29,v29
v_mul_f32 v78,v30,v30
v_mfma_f32_16x16x32_bf16 v[36:39],a[48:51],v[52:55],v[36:39]
s_lshr_b32 s71,s62,2
ds_read_b128 v[48:51],v2 offset:46080
v_mul_f32 v79,v31,v31
v_mfma_f32_16x16x32_bf16 v[40:43],a[52:55],v[52:55],v[40:43]
s_cmp_lt_u32 s67,4194304
v_fma_f32 v76,v94,v76,v93
v_mul_f32 v76,v28,v76
v_mfma_f32_16x16x32_bf16 v[104:107],v[100:103],v[52:55],v[104:107]
buffer_load_dwordx4 v1,s[36:39],s67 offen offset:2048 sc1 lds
v_mfma_f32_16x16x32_bf16 v[108:111],v[52:55],v[52:55],v[108:111]
s_cselect_b32 s72,s59,s72
v_exp_f32 v76,v76
v_mfma_f32_16x16x32_bf16 v[36:39],a[56:59],v[56:59],v[36:39]
s_mov_b64 s[44:45],s[46:47]
ds_read_b128 v[52:55],v2 offset:47104
v_fma_f32 v76,v76,v95,v95
v_mfma_f32_16x16x32_bf16 v[40:43],a[60:63],v[56:59],v[40:43]
s_mov_b64 s[46:47],s[48:49]
v_rcp_f32 v76,v76
v_mfma_f32_16x16x32_bf16 v[104:107],v[100:103],v[56:59],v[104:107]
buffer_load_dwordx4 v1,s[36:39],s67 offen offset:3072 sc1 lds
v_mfma_f32_16x16x32_bf16 v[108:111],v[56:59],v[56:59],v[108:111]
s_waitcnt lgkmcnt(3)
v_fma_f32 v77,v94,v77,v93
v_mul_f32 v77,v29,v77
v_mfma_f32_16x16x32_bf16 v[36:39],a[0:3],v[60:63],v[36:39]
s_add_u32 m0,s71,s88
ds_read_b128 v[56:59],v2 offset:48128
v_fma_f32 v78,v94,v78,v93
v_mfma_f32_16x16x32_bf16 v[40:43],a[4:7],v[60:63],v[40:43]
buffer_load_dword v10,s[36:39],s72 offen offset:0 sc1 lds
v_mfma_f32_16x16x32_bf16 v[36:39],a[8:11],v[64:67],v[36:39]
s_xor_b32 s87,s63,65536
ds_read_b128 v[60:63],v2 offset:40960
v_mul_f32 v78,v30,v78
v_mfma_f32_16x16x32_bf16 v[40:43],a[12:15],v[64:67],v[40:43]
s_cmp_ge_u32 s67,s59
v_exp_f32 v77,v77
v_mfma_f32_16x16x32_bf16 v[36:39],a[16:19],v[68:71],v[36:39]
s_cselect_b32 s86,s50,s51
ds_read_b128 v[64:67],v2 offset:41984
v_fma_f32 v77,v77,v95,v95
v_mfma_f32_16x16x32_bf16 v[40:43],a[20:23],v[68:71],v[40:43]
s_cselect_b32 s87,s87,s64
v_exp_f32 v78,v78
v_mfma_f32_16x16x32_bf16 v[36:39],a[24:27],v[72:75],v[36:39]
s_mov_b32 s62,s87
ds_read_b128 v[68:71],v2 offset:43008
v_fma_f32 v78,v78,v95,v95
v_mfma_f32_16x16x32_bf16 v[40:43],a[28:31],v[72:75],v[40:43]
s_waitcnt lgkmcnt(3)
v_rcp_f32 v77,v77
v_mfma_f32_16x16x32_bf16 v[36:39],a[96:99],v[44:47],v[36:39]
s_waitcnt vmcnt(10)
ds_read_b128 v[72:75],v2 offset:44032
v_fma_f32 v79,v94,v79,v93
v_mfma_f32_16x16x32_bf16 v[40:43],a[100:103],v[44:47],v[40:43]
s_barrier
v_mfma_f32_16x16x32_bf16 v[36:39],a[104:107],v[48:51],v[36:39]
s_cmp_lt_u32 s86,s89
ds_read_b128 v[44:47],v2 offset:53248
ds_read_b32 v16,v7 offset:256
v_mfma_f32_16x16x32_bf16 v[40:43],a[108:111],v[48:51],v[40:43]
s_mov_b32 m0,s62
v_rcp_f32 v78,v78
v_mfma_f32_16x16x32_bf16 v[36:39],a[112:115],v[52:55],v[36:39]
s_mov_b64 s[48:49],s[50:51]
ds_read_b128 v[48:51],v2 offset:54272
v_mul_f32 v79,v31,v79
v_mfma_f32_16x16x32_bf16 v[40:43],a[116:119],v[52:55],v[40:43]
s_mov_b64 s[50:51],s[52:53]
v_exp_f32 v79,v79
v_mfma_f32_16x16x32_bf16 v[36:39],a[120:123],v[56:59],v[36:39]
s_waitcnt lgkmcnt(1)
ds_read_b128 v[52:55],v2 offset:55296
v_fma_f32 v79,v79,v95,v95
v_mfma_f32_16x16x32_bf16 v[40:43],a[124:127],v[56:59],v[40:43]
v_cmp_eq_u32 vcc,s60,v16
v_rcp_f32 v79,v79
v_mul_f32 v28,v28,v76
v_mfma_f32_16x16x32_bf16 v[36:39],a[64:67],v[60:63],v[36:39]
s_cselect_b32 vcc_lo,-1,vcc_lo
ds_read_b128 v[56:59],v2 offset:56320
v_mul_f32 v29,v29,v77
v_mfma_f32_16x16x32_bf16 v[40:43],a[68:71],v[60:63],v[40:43]
s_cmp_eq_u32 vcc_lo,-1
v_mul_f32 v30,v30,v78
v_mul_f32 v31,v31,v79
v_mfma_f32_16x16x32_bf16 v[36:39],a[72:75],v[64:67],v[36:39]
s_cselect_b32 s66,s86,s59
ds_read_b128 v[60:63],v2 offset:49152
v_cvt_pk_bf16_f32 v84,v28,v29
v_mfma_f32_16x16x32_bf16 v[40:43],a[76:79],v[64:67],v[40:43]
s_add_u32 s74,s53,s55
v_cvt_pk_bf16_f32 v85,v30,v31
v_fma_f32 v32,-v92,v24,v32
v_mfma_f32_16x16x32_bf16 v[36:39],a[80:83],v[68:71],v[36:39]
s_cmp_eq_u32 s74,s56
ds_read_b128 v[64:67],v2 offset:50176
v_fma_f32 v33,-v92,v25,v33
v_mfma_f32_16x16x32_bf16 v[40:43],a[84:87],v[68:71],v[40:43]
s_cselect_b32 s74,s57,s74
v_fma_f32 v34,-v92,v26,v34
v_fma_f32 v35,-v92,v27,v35
v_mfma_f32_16x16x32_bf16 v[36:39],a[88:91],v[72:75],v[36:39]
s_cmp_eq_u32 s74,s58
ds_read_b128 v[68:71],v2 offset:51200
v_mul_f32 v80,v32,v32
v_mfma_f32_16x16x32_bf16 v[40:43],a[92:95],v[72:75],v[40:43]
s_waitcnt lgkmcnt(3)
v_mul_f32 v81,v33,v33
v_mul_f32 v82,v34,v34
v_mfma_f32_16x16x32_bf16 v[36:39],a[160:163],v[44:47],v[36:39]
s_cselect_b32 s74,s59,s74
ds_read_b128 v[72:75],v2 offset:52224
v_mul_f32 v83,v35,v35
v_mfma_f32_16x16x32_bf16 v[40:43],a[164:167],v[44:47],v[40:43]
s_xor_b32 s64,s64,65536
v_fma_f32 v80,v94,v80,v93
v_mul_f32 v80,v32,v80
v_mfma_f32_16x16x32_bf16 v[104:107],v[100:103],v[44:47],v[104:107]
s_mov_b32 s52,s74
v_exp_f32 v80,v80
v_mfma_f32_16x16x32_bf16 v[108:111],v[44:47],v[44:47],v[108:111]
s_cmp_lt_u32 s67,4194304
v_fma_f32 v80,v80,v95,v95
v_fma_f32 v81,v94,v81,v93
v_mfma_f32_16x16x32_bf16 v[36:39],a[168:171],v[48:51],v[36:39]
ds_read_b128 v[44:47],v2 offset:61440
v_mul_f32 v81,v33,v81
v_mfma_f32_16x16x32_bf16 v[40:43],a[172:175],v[48:51],v[40:43]
v_exp_f32 v81,v81
v_mfma_f32_16x16x32_bf16 v[104:107],v[100:103],v[48:51],v[104:107]
v_rcp_f32 v80,v80
v_mfma_f32_16x16x32_bf16 v[108:111],v[48:51],v[48:51],v[108:111]
v_fma_f32 v81,v81,v95,v95
v_fma_f32 v82,v94,v82,v93
v_mfma_f32_16x16x32_bf16 v[36:39],a[176:179],v[52:55],v[36:39]
ds_read_b128 v[48:51],v2 offset:62464
v_mul_f32 v82,v34,v82
v_mfma_f32_16x16x32_bf16 v[40:43],a[180:183],v[52:55],v[40:43]
v_exp_f32 v82,v82
v_mfma_f32_16x16x32_bf16 v[104:107],v[100:103],v[52:55],v[104:107]
v_rcp_f32 v81,v81
v_mfma_f32_16x16x32_bf16 v[108:111],v[52:55],v[52:55],v[108:111]
v_fma_f32 v82,v82,v95,v95
v_fma_f32 v83,v94,v83,v93
v_mfma_f32_16x16x32_bf16 v[36:39],a[184:187],v[56:59],v[36:39]
ds_read_b128 v[52:55],v2 offset:63488
v_mul_f32 v83,v35,v83
v_mfma_f32_16x16x32_bf16 v[40:43],a[188:191],v[56:59],v[40:43]
v_exp_f32 v83,v83
v_mfma_f32_16x16x32_bf16 v[104:107],v[100:103],v[56:59],v[104:107]
v_rcp_f32 v82,v82
v_mfma_f32_16x16x32_bf16 v[108:111],v[56:59],v[56:59],v[108:111]
s_waitcnt lgkmcnt(3)
v_fma_f32 v83,v83,v95,v95
v_mul_f32 v32,v32,v80
v_mfma_f32_16x16x32_bf16 v[36:39],a[128:131],v[60:63],v[36:39]
ds_read_b128 v[56:59],v2 offset:64512
v_mul_f32 v33,v33,v81
v_mfma_f32_16x16x32_bf16 v[40:43],a[132:135],v[60:63],v[40:43]
buffer_load_dwordx4 v0,s[36:39],s66 offen offset:0 sc1 lds
v_mfma_f32_16x16x32_bf16 v[36:39],a[136:139],v[64:67],v[36:39]
v_cndmask_b32 v112,v108,v109,s[82:83]
v_cndmask_b32 v113,v110,v111,s[82:83]
v_cndmask_b32 v112,v112,v113,s[84:85]
v_cndmask_b32 v112,v104,v112,s[98:99]
v_mfma_f32_16x16x32_bf16 v[40:43],a[140:143],v[64:67],v[40:43]
ds_read_b128 v[60:63],v2 offset:57344
ds_write_b32 v13,v112 offset:2048
v_mfma_f32_16x16x32_bf16 v[36:39],a[144:147],v[68:71],v[36:39]
ds_read_b128 v[64:67],v2 offset:58368
v_mul_f32 v34,v34,v82
v_mfma_f32_16x16x32_bf16 v[40:43],a[148:151],v[68:71],v[40:43]
buffer_load_dwordx4 v0,s[36:39],s66 offen offset:1024 sc1 lds
v_mfma_f32_16x16x32_bf16 v[36:39],a[152:155],v[72:75],v[36:39])"
R"(
ds_read_b128 v[68:71],v2 offset:59392
v_cvt_pk_bf16_f32 v86,v32,v33
v_mfma_f32_16x16x32_bf16 v[40:43],a[156:159],v[72:75],v[40:43]
s_waitcnt lgkmcnt(4)
v_rcp_f32 v83,v83
v_mfma_f32_16x16x32_bf16 v[36:39],a[224:227],v[44:47],v[36:39]
s_waitcnt vmcnt(7) lgkmcnt(2)
ds_read_b128 v[72:75],v2 offset:60416
v_xor_b32 v2,65536,v2
v_mfma_f32_16x16x32_bf16 v[40:43],a[228:231],v[44:47],v[40:43]
s_barrier
v_mfma_f32_16x16x32_bf16 v[36:39],a[232:235],v[48:51],v[36:39]
ds_read_b128 v[116:119],v14 offset:2048
ds_read_b128 v[120:123],v14 offset:2064
v_mfma_f32_16x16x32_bf16 v[40:43],a[236:239],v[48:51],v[40:43]
ds_read_b128 v[44:47],v2 offset:4096
buffer_load_dwordx4 v0,s[36:39],s66 offen offset:2048 sc1 lds
v_mfma_f32_16x16x32_bf16 v[36:39],a[240:243],v[52:55],v[36:39]
ds_read_b128 v[48:51],v2 offset:5120
v_mul_f32 v35,v35,v83
v_mfma_f32_16x16x32_bf16 v[40:43],a[244:247],v[52:55],v[40:43]
v_cvt_pk_bf16_f32 v87,v34,v35
v_xor_b32 v8,16384,v8
v_mfma_f32_16x16x32_bf16 v[36:39],a[248:251],v[56:59],v[36:39]
s_waitcnt lgkmcnt(2)
ds_read_b128 v[52:55],v2 offset:6144
v_add_f32 v116,v116,v117
v_mfma_f32_16x16x32_bf16 v[40:43],a[252:255],v[56:59],v[40:43]
buffer_load_dwordx4 v0,s[36:39],s66 offen offset:3072 sc1 lds
v_mfma_f32_16x16x32_bf16 v[36:39],a[192:195],v[60:63],v[36:39]
ds_read_b128 v[56:59],v2 offset:7168
v_add_f32 v118,v118,v119
v_mfma_f32_16x16x32_bf16 v[40:43],a[196:199],v[60:63],v[40:43]
v_add_f32 v116,v116,v118
v_mul_f32 v96,s75,v116
v_mfma_f32_16x16x32_bf16 v[36:39],a[200:203],v[64:67],v[36:39]
ds_read_b128 v[60:63],v2 offset:0
v_add_f32 v120,v120,v121
v_mfma_f32_16x16x32_bf16 v[40:43],a[204:207],v[64:67],v[40:43]
buffer_store_dwordx4 v[84:87],v3,s[36:39],s73 offen offset:0 sc0 sc1
v_add_f32 v122,v122,v123
v_add_f32 v120,v120,v122
v_mul_f32 v120,s75,v120
v_mfma_f32_16x16x32_bf16 v[36:39],a[208:211],v[68:71],v[36:39]
ds_read_b128 v[64:67],v2 offset:1024
v_fma_f32 v120,-v96,v96,v120
v_mfma_f32_16x16x32_bf16 v[40:43],a[212:215],v[68:71],v[40:43]
v_max_f32 v120,0,v120
v_add_f32 v115,v15,v120
v_fma_f32 v120,v15,v115,v120
v_rsq_f32 v116,v120
v_mfma_f32_16x16x32_bf16 v[36:39],a[216:219],v[72:75],v[36:39]
s_waitcnt lgkmcnt(2)
ds_read_b128 v[68:71],v2 offset:2048
v_mul_f32 v97,s76,v116
v_mul_f32 v98,v116,v116
v_mul_f32 v99,v120,v116
v_mul_f32 v98,s77,v98
v_mul_f32 v98,v98,v97
v_mfma_f32_16x16x32_bf16 v[40:43],a[220:223],v[72:75],v[40:43]
s_cbranch_scc1 Lg1_a0w1_%=
s_cmp_ge_u32 s48,4194304
s_cselect_b64 s[90:91],s[94:95],s[90:91]
s_cselect_b64 s[92:93],s[96:97],s[92:93]
s_cmp_lt_u32 s67,s59
s_cbranch_scc1 Lg1_d0w1_%=
s_branch Lg1_slow0_%=
Lg1_a0w2_%=:
v_mfma_f32_16x16x32_bf16 v[28:31],a[64:67],v[44:47],0
s_sub_u32 s69,s66,1048576
ds_read_b128 v[72:75],v2 offset:15360
s_cmp_lt_u32 s69,2097152
v_mfma_f32_16x16x32_bf16 v[32:35],a[68:71],v[44:47],0
s_cselect_b32 s89,3145728,s89
s_sub_u32 s69,s66,3145728
s_cmp_lt_u32 s69,1048576
v_mfma_f32_16x16x32_bf16 v[104:107],v[100:103],v[44:47],0
s_cselect_b32 s89,4194304,s89
s_add_u32 m0,s62,16384
s_lshr_b32 s73,s47,0
v_mfma_f32_16x16x32_bf16 v[108:111],v[44:47],v[44:47],0
buffer_load_dwordx4 v1,s[36:39],s66 offen offset:0 sc1 lds
v_mfma_f32_16x16x32_bf16 v[28:31],a[72:75],v[48:51],v[28:31]
s_lshr_b32 s65,s48,6
ds_read_b128 v[44:47],v2 offset:0
v_fma_f32 v36,-v96,v20,v36
v_mfma_f32_16x16x32_bf16 v[32:35],a[76:79],v[48:51],v[32:35]
s_lshr_b32 s72,s66,6
v_fma_f32 v37,-v96,v21,v37
v_fma_f32 v38,-v96,v22,v38
v_mfma_f32_16x16x32_bf16 v[104:107],v[100:103],v[48:51],v[104:107]
s_lshr_b32 s71,s62,2
v_fma_f32 v39,-v96,v23,v39
v_mul_f32 v76,v36,v36
v_mfma_f32_16x16x32_bf16 v[108:111],v[48:51],v[48:51],v[108:111]
buffer_load_dwordx4 v1,s[36:39],s66 offen offset:1024 sc1 lds
v_mfma_f32_16x16x32_bf16 v[28:31],a[80:83],v[52:55],v[28:31]
s_cmp_lt_u32 s66,4194304
ds_read_b128 v[48:51],v2 offset:1024
v_mul_f32 v77,v37,v37
v_mfma_f32_16x16x32_bf16 v[32:35],a[84:87],v[52:55],v[32:35]
s_cselect_b32 s72,s59,s72
v_mul_f32 v78,v38,v38
v_mul_f32 v79,v39,v39
v_mfma_f32_16x16x32_bf16 v[104:107],v[100:103],v[52:55],v[104:107]
v_fma_f32 v76,v98,v76,v97
v_mul_f32 v76,v36,v76
v_mfma_f32_16x16x32_bf16 v[108:111],v[52:55],v[52:55],v[108:111]
buffer_load_dwordx4 v1,s[36:39],s66 offen offset:2048 sc1 lds
v_mfma_f32_16x16x32_bf16 v[28:31],a[88:91],v[56:59],v[28:31]
ds_read_b128 v[52:55],v2 offset:2048
v_fma_f32 v77,v98,v77,v97
v_mfma_f32_16x16x32_bf16 v[32:35],a[92:95],v[56:59],v[32:35]
v_exp_f32 v76,v76
v_mfma_f32_16x16x32_bf16 v[104:107],v[100:103],v[56:59],v[104:107]
v_mul_f32 v77,v37,v77
v_fma_f32 v76,v76,v99,v99
v_mfma_f32_16x16x32_bf16 v[108:111],v[56:59],v[56:59],v[108:111]
s_waitcnt lgkmcnt(3)
buffer_load_dwordx4 v1,s[36:39],s66 offen offset:3072 sc1 lds
v_mfma_f32_16x16x32_bf16 v[28:31],a[96:99],v[60:63],v[28:31]
s_add_u32 m0,s71,s88
ds_read_b128 v[56:59],v2 offset:3072
v_fma_f32 v78,v98,v78,v97
v_mfma_f32_16x16x32_bf16 v[32:35],a[100:103],v[60:63],v[32:35]
buffer_load_dword v10,s[36:39],s72 offen offset:0 sc1 lds
v_mfma_f32_16x16x32_bf16 v[28:31],a[104:107],v[64:67],v[28:31]
s_xor_b32 s87,s64,65536
ds_read_b128 v[60:63],v2 offset:4096
v_fma_f32 v79,v98,v79,v97
v_mfma_f32_16x16x32_bf16 v[32:35],a[108:111],v[64:67],v[32:35]
s_cmp_ge_u32 s66,s59
v_exp_f32 v77,v77
v_mfma_f32_16x16x32_bf16 v[28:31],a[112:115],v[68:71],v[28:31]
s_cselect_b32 s86,s49,s50
ds_read_b128 v[64:67],v2 offset:5120
v_mul_f32 v78,v38,v78
v_mfma_f32_16x16x32_bf16 v[32:35],a[116:119],v[68:71],v[32:35]
s_cselect_b32 s87,s87,s63
v_exp_f32 v78,v78
v_mfma_f32_16x16x32_bf16 v[28:31],a[120:123],v[72:75],v[28:31]
s_mov_b32 s62,s87
ds_read_b128 v[68:71],v2 offset:6144
v_mul_f32 v79,v39,v79
v_mfma_f32_16x16x32_bf16 v[32:35],a[124:127],v[72:75],v[32:35]
s_waitcnt lgkmcnt(3)
v_exp_f32 v79,v79
v_mfma_f32_16x16x32_bf16 v[28:31],a[0:3],v[44:47],v[28:31]
s_waitcnt vmcnt(10)
ds_read_b128 v[72:75],v2 offset:7168
v_fma_f32 v77,v77,v99,v99
v_mfma_f32_16x16x32_bf16 v[32:35],a[4:7],v[44:47],v[32:35]
s_barrier
v_mfma_f32_16x16x32_bf16 v[28:31],a[8:11],v[48:51],v[28:31]
s_cmp_lt_u32 s86,s89
ds_read_b128 v[44:47],v2 offset:24576
ds_read_b32 v16,v7 offset:0
v_mfma_f32_16x16x32_bf16 v[32:35],a[12:15],v[48:51],v[32:35]
s_mov_b32 m0,s62
v_rcp_f32 v76,v76
v_mfma_f32_16x16x32_bf16 v[28:31],a[16:19],v[52:55],v[28:31]
ds_read_b128 v[48:51],v2 offset:25600
v_fma_f32 v78,v78,v99,v99
v_mfma_f32_16x16x32_bf16 v[32:35],a[20:23],v[52:55],v[32:35]
v_rcp_f32 v77,v77
v_mfma_f32_16x16x32_bf16 v[28:31],a[24:27],v[56:59],v[28:31]
s_waitcnt lgkmcnt(1)
ds_read_b128 v[52:55],v2 offset:26624
v_fma_f32 v79,v79,v99,v99
v_mfma_f32_16x16x32_bf16 v[32:35],a[28:31],v[56:59],v[32:35]
v_cmp_eq_u32 vcc,s60,v16
v_rcp_f32 v78,v78
v_mul_f32 v36,v36,v76
v_mfma_f32_16x16x32_bf16 v[28:31],a[32:35],v[60:63],v[28:31]
s_cselect_b32 vcc_lo,-1,vcc_lo
ds_read_b128 v[56:59],v2 offset:27648
v_mul_f32 v37,v37,v77
v_mfma_f32_16x16x32_bf16 v[32:35],a[36:39],v[60:63],v[32:35]
s_cmp_eq_u32 vcc_lo,-1
v_rcp_f32 v79,v79
v_mfma_f32_16x16x32_bf16 v[28:31],a[40:43],v[64:67],v[28:31]
s_cselect_b32 s67,s86,s59
ds_read_b128 v[60:63],v2 offset:28672
v_mul_f32 v38,v38,v78
v_mfma_f32_16x16x32_bf16 v[32:35],a[44:47],v[64:67],v[32:35]
s_add_u32 s53,s52,s55
v_mul_f32 v39,v39,v79
v_cvt_pk_bf16_f32 v84,v36,v37
v_mfma_f32_16x16x32_bf16 v[28:31],a[48:51],v[68:71],v[28:31]
s_cmp_eq_u32 s53,s56
ds_read_b128 v[64:67],v2 offset:29696
v_cvt_pk_bf16_f32 v85,v38,v39
v_mfma_f32_16x16x32_bf16 v[32:35],a[52:55],v[68:71],v[32:35]
s_cselect_b32 s53,s57,s53
v_fma_f32 v40,-v96,v24,v40
v_fma_f32 v41,-v96,v25,v41
v_mfma_f32_16x16x32_bf16 v[28:31],a[56:59],v[72:75],v[28:31]
s_cmp_eq_u32 s53,s58
ds_read_b128 v[68:71],v2 offset:30720
v_fma_f32 v42,-v96,v26,v42)"
R"(
v_mfma_f32_16x16x32_bf16 v[32:35],a[60:63],v[72:75],v[32:35]
s_waitcnt lgkmcnt(3)
v_fma_f32 v43,-v96,v27,v43
v_mul_f32 v80,v40,v40
v_mfma_f32_16x16x32_bf16 v[28:31],a[192:195],v[44:47],v[28:31]
s_cselect_b32 s53,s59,s53
ds_read_b128 v[72:75],v2 offset:31744
v_mul_f32 v81,v41,v41
v_mfma_f32_16x16x32_bf16 v[32:35],a[196:199],v[44:47],v[32:35]
s_xor_b32 s63,s63,65536
v_mul_f32 v82,v42,v42
v_mul_f32 v83,v43,v43
v_mfma_f32_16x16x32_bf16 v[104:107],v[100:103],v[44:47],v[104:107]
s_cmp_ge_u32 s66,s59
v_fma_f32 v80,v98,v80,v97
v_mul_f32 v80,v40,v80
v_mfma_f32_16x16x32_bf16 v[108:111],v[44:47],v[44:47],v[108:111]
v_exp_f32 v80,v80
v_mfma_f32_16x16x32_bf16 v[28:31],a[200:203],v[48:51],v[28:31]
ds_read_b128 v[44:47],v2 offset:16384
v_fma_f32 v80,v80,v99,v99
v_mfma_f32_16x16x32_bf16 v[32:35],a[204:207],v[48:51],v[32:35]
v_rcp_f32 v80,v80
v_mfma_f32_16x16x32_bf16 v[104:107],v[100:103],v[48:51],v[104:107]
v_fma_f32 v81,v98,v81,v97
v_mul_f32 v81,v41,v81
v_mfma_f32_16x16x32_bf16 v[108:111],v[48:51],v[48:51],v[108:111]
v_exp_f32 v81,v81
v_mfma_f32_16x16x32_bf16 v[28:31],a[208:211],v[52:55],v[28:31]
ds_read_b128 v[48:51],v2 offset:17408
v_fma_f32 v81,v81,v99,v99
v_mfma_f32_16x16x32_bf16 v[32:35],a[212:215],v[52:55],v[32:35]
v_rcp_f32 v81,v81
v_mfma_f32_16x16x32_bf16 v[104:107],v[100:103],v[52:55],v[104:107]
v_fma_f32 v82,v98,v82,v97
v_mul_f32 v82,v42,v82
v_mfma_f32_16x16x32_bf16 v[108:111],v[52:55],v[52:55],v[108:111]
v_exp_f32 v82,v82
v_mfma_f32_16x16x32_bf16 v[28:31],a[216:219],v[56:59],v[28:31]
ds_read_b128 v[52:55],v2 offset:18432
v_fma_f32 v82,v82,v99,v99
v_mfma_f32_16x16x32_bf16 v[32:35],a[220:223],v[56:59],v[32:35]
v_rcp_f32 v82,v82
v_mfma_f32_16x16x32_bf16 v[104:107],v[100:103],v[56:59],v[104:107]
v_fma_f32 v83,v98,v83,v97
v_mul_f32 v83,v43,v83
v_mfma_f32_16x16x32_bf16 v[108:111],v[56:59],v[56:59],v[108:111]
s_waitcnt lgkmcnt(3)
v_exp_f32 v83,v83
v_mfma_f32_16x16x32_bf16 v[28:31],a[224:227],v[60:63],v[28:31]
ds_read_b128 v[56:59],v2 offset:19456
v_fma_f32 v83,v83,v99,v99
v_mfma_f32_16x16x32_bf16 v[32:35],a[228:231],v[60:63],v[32:35]
v_rcp_f32 v83,v83
v_mfma_f32_16x16x32_bf16 v[28:31],a[232:235],v[64:67],v[28:31]
buffer_load_dwordx4 v0,s[36:39],s67 offen offset:0 sc1 lds
v_cndmask_b32 v112,v108,v109,s[82:83]
v_cndmask_b32 v113,v110,v111,s[82:83]
v_cndmask_b32 v112,v112,v113,s[84:85]
v_cndmask_b32 v112,v104,v112,s[98:99]
v_mfma_f32_16x16x32_bf16 v[32:35],a[236:239],v[64:67],v[32:35]
ds_read_b128 v[60:63],v2 offset:20480
ds_write_b32 v13,v112 offset:0
v_mfma_f32_16x16x32_bf16 v[28:31],a[240:243],v[68:71],v[28:31]
ds_read_b128 v[64:67],v2 offset:21504
v_mul_f32 v40,v40,v80
v_mfma_f32_16x16x32_bf16 v[32:35],a[244:247],v[68:71],v[32:35]
v_mul_f32 v41,v41,v81
v_mul_f32 v42,v42,v82
v_mfma_f32_16x16x32_bf16 v[28:31],a[248:251],v[72:75],v[28:31]
buffer_load_dwordx4 v0,s[36:39],s67 offen offset:1024 sc1 lds
v_mfma_f32_16x16x32_bf16 v[32:35],a[252:255],v[72:75],v[32:35]
s_waitcnt lgkmcnt(3)
ds_read_b128 v[68:71],v2 offset:22528
v_mul_f32 v43,v43,v83
v_mfma_f32_16x16x32_bf16 v[28:31],a[128:131],v[44:47],v[28:31]
s_waitcnt vmcnt(7) lgkmcnt(2)
ds_read_b128 v[72:75],v2 offset:23552
v_cvt_pk_bf16_f32 v86,v40,v41
v_mfma_f32_16x16x32_bf16 v[32:35],a[132:135],v[44:47],v[32:35]
s_barrier
v_mfma_f32_16x16x32_bf16 v[28:31],a[136:139],v[48:51],v[28:31]
ds_read_b128 v[116:119],v14 offset:0
ds_read_b128 v[120:123],v14 offset:16
v_mfma_f32_16x16x32_bf16 v[32:35],a[140:143],v[48:51],v[32:35]
ds_read_b128 v[44:47],v2 offset:40960
v_cvt_pk_bf16_f32 v87,v42,v43
v_mfma_f32_16x16x32_bf16 v[28:31],a[144:147],v[52:55],v[28:31]
buffer_load_dwordx4 v0,s[36:39],s67 offen offset:2048 sc1 lds
v_mfma_f32_16x16x32_bf16 v[32:35],a[148:151],v[52:55],v[32:35]
ds_read_b128 v[48:51],v2 offset:41984
v_mfma_f32_16x16x32_bf16 v[28:31],a[152:155],v[56:59],v[28:31]
s_waitcnt lgkmcnt(2)
ds_read_b128 v[52:55],v2 offset:43008
v_add_f32 v116,v116,v117
v_mfma_f32_16x16x32_bf16 v[32:35],a[156:159],v[56:59],v[32:35]
v_add_f32 v118,v118,v119
v_add_f32 v116,v116,v118
v_mfma_f32_16x16x32_bf16 v[28:31],a[160:163],v[60:63],v[28:31]
buffer_load_dwordx4 v0,s[36:39],s67 offen offset:3072 sc1 lds
v_mfma_f32_16x16x32_bf16 v[32:35],a[164:167],v[60:63],v[32:35]
ds_read_b128 v[56:59],v2 offset:44032
v_mul_f32 v92,s75,v116
v_mfma_f32_16x16x32_bf16 v[28:31],a[168:171],v[64:67],v[28:31]
ds_read_b128 v[60:63],v2 offset:45056
v_add_f32 v120,v120,v121
v_mfma_f32_16x16x32_bf16 v[32:35],a[172:175],v[64:67],v[32:35]
v_add_f32 v122,v122,v123
v_add_f32 v120,v120,v122
v_mfma_f32_16x16x32_bf16 v[28:31],a[176:179],v[68:71],v[28:31]
buffer_store_dwordx4 v[84:87],v3,s[36:39],s73 offen offset:0 sc0 sc1
v_mul_f32 v120,s75,v120
v_fma_f32 v120,-v92,v92,v120
v_max_f32 v120,0,v120
v_add_f32 v115,v15,v120
v_fma_f32 v120,v15,v115,v120
v_rsq_f32 v115,v115
v_mfma_f32_16x16x32_bf16 v[32:35],a[180:183],v[68:71],v[32:35]
ds_read_b128 v[64:67],v2 offset:46080
v_rsq_f32 v116,v120
v_mul_f32_e64 v114,-v92,v115
v_mfma_f32_16x16x32_bf16 v[28:31],a[184:187],v[72:75],v[28:31]
s_waitcnt lgkmcnt(2)
ds_read_b128 v[68:71],v2 offset:47104
buffer_store_dwordx2 v[114:115],v12,s[36:39],s65 offen offset:0 sc0 sc1
v_mul_f32 v93,s76,v116
v_mul_f32 v94,v116,v116
v_mul_f32 v95,v120,v116
v_mul_f32 v94,s77,v94
v_mul_f32 v94,v94,v93
v_mfma_f32_16x16x32_bf16 v[32:35],a[188:191],v[72:75],v[32:35]
s_cbranch_scc1 Lg1_slow1_%=
Lg1_a1w2_%=:
v_mfma_f32_16x16x32_bf16 v[36:39],a[64:67],v[44:47],0
s_sub_u32 s69,s67,1048576
ds_read_b128 v[72:75],v2 offset:48128
s_cmp_lt_u32 s69,2097152
v_mfma_f32_16x16x32_bf16 v[40:43],a[68:71],v[44:47],0
s_cselect_b32 s89,3145728,s89
s_sub_u32 s69,s67,3145728
s_cmp_lt_u32 s69,1048576
v_mfma_f32_16x16x32_bf16 v[104:107],v[100:103],v[44:47],0
s_cselect_b32 s89,4194304,s89
s_add_u32 m0,s62,16384
s_lshr_b32 s73,s48,0
v_mfma_f32_16x16x32_bf16 v[108:111],v[44:47],v[44:47],0
buffer_load_dwordx4 v1,s[36:39],s67 offen offset:0 sc1 lds
v_mfma_f32_16x16x32_bf16 v[36:39],a[72:75],v[48:51],v[36:39]
s_lshr_b32 s65,s49,6
ds_read_b128 v[44:47],v2 offset:32768
v_fma_f32 v28,-v92,v20,v28
v_mfma_f32_16x16x32_bf16 v[40:43],a[76:79],v[48:51],v[40:43]
s_lshr_b32 s72,s67,6
v_fma_f32 v29,-v92,v21,v29
v_fma_f32 v30,-v92,v22,v30
v_mfma_f32_16x16x32_bf16 v[104:107],v[100:103],v[48:51],v[104:107]
s_lshr_b32 s71,s62,2
v_fma_f32 v31,-v92,v23,v31
v_mul_f32 v76,v28,v28
v_mfma_f32_16x16x32_bf16 v[108:111],v[48:51],v[48:51],v[108:111]
buffer_load_dwordx4 v1,s[36:39],s67 offen offset:1024 sc1 lds
v_mfma_f32_16x16x32_bf16 v[36:39],a[80:83],v[52:55],v[36:39]
s_cmp_lt_u32 s67,4194304
ds_read_b128 v[48:51],v2 offset:33792
v_mul_f32 v77,v29,v29
v_mfma_f32_16x16x32_bf16 v[40:43],a[84:87],v[52:55],v[40:43]
s_cselect_b32 s72,s59,s72
v_mul_f32 v78,v30,v30
v_mul_f32 v79,v31,v31
v_mfma_f32_16x16x32_bf16 v[104:107],v[100:103],v[52:55],v[104:107]
s_mov_b64 s[44:45],s[46:47]
v_fma_f32 v76,v94,v76,v93
v_mul_f32 v76,v28,v76
v_mfma_f32_16x16x32_bf16 v[108:111],v[52:55],v[52:55],v[108:111]
buffer_load_dwordx4 v1,s[36:39],s67 offen offset:2048 sc1 lds
v_mfma_f32_16x16x32_bf16 v[36:39],a[88:91],v[56:59],v[36:39]
s_mov_b64 s[46:47],s[48:49]
ds_read_b128 v[52:55],v2 offset:34816
v_fma_f32 v77,v94,v77,v93
v_mfma_f32_16x16x32_bf16 v[40:43],a[92:95],v[56:59],v[40:43]
s_mov_b64 s[48:49],s[50:51]
v_exp_f32 v76,v76
v_mfma_f32_16x16x32_bf16 v[104:107],v[100:103],v[56:59],v[104:107]
v_mul_f32 v77,v29,v77
v_fma_f32 v76,v76,v95,v95
v_mfma_f32_16x16x32_bf16 v[108:111],v[56:59],v[56:59],v[108:111]
s_waitcnt lgkmcnt(3)
buffer_load_dwordx4 v1,s[36:39],s67 offen offset:3072 sc1 lds
v_mfma_f32_16x16x32_bf16 v[36:39],a[96:99],v[60:63],v[36:39]
s_add_u32 m0,s71,s88
ds_read_b128 v[56:59],v2 offset:35840
v_fma_f32 v78,v94,v78,v93
v_mfma_f32_16x16x32_bf16 v[40:43],a[100:103],v[60:63],v[40:43]
buffer_load_dword v10,s[36:39],s72 offen offset:0 sc1 lds
v_mfma_f32_16x16x32_bf16 v[36:39],a[104:107],v[64:67],v[36:39]
s_xor_b32 s87,s63,65536
ds_read_b128 v[60:63],v2 offset:36864
v_fma_f32 v79,v94,v79,v93
v_mfma_f32_16x16x32_bf16 v[40:43],a[108:111],v[64:67],v[40:43]
s_cmp_ge_u32 s67,s59)"
R"(
v_exp_f32 v77,v77
v_mfma_f32_16x16x32_bf16 v[36:39],a[112:115],v[68:71],v[36:39]
s_cselect_b32 s86,s50,s51
ds_read_b128 v[64:67],v2 offset:37888
v_mul_f32 v78,v30,v78
v_mfma_f32_16x16x32_bf16 v[40:43],a[116:119],v[68:71],v[40:43]
s_cselect_b32 s87,s87,s64
v_exp_f32 v78,v78
v_mfma_f32_16x16x32_bf16 v[36:39],a[120:123],v[72:75],v[36:39]
s_mov_b32 s62,s87
ds_read_b128 v[68:71],v2 offset:38912
v_mul_f32 v79,v31,v79
v_mfma_f32_16x16x32_bf16 v[40:43],a[124:127],v[72:75],v[40:43]
s_waitcnt lgkmcnt(3)
v_exp_f32 v79,v79
v_mfma_f32_16x16x32_bf16 v[36:39],a[0:3],v[44:47],v[36:39]
s_waitcnt vmcnt(10)
ds_read_b128 v[72:75],v2 offset:39936
v_fma_f32 v77,v77,v95,v95
v_mfma_f32_16x16x32_bf16 v[40:43],a[4:7],v[44:47],v[40:43]
s_barrier
v_mfma_f32_16x16x32_bf16 v[36:39],a[8:11],v[48:51],v[36:39]
s_cmp_lt_u32 s86,s89
ds_read_b128 v[44:47],v2 offset:57344
ds_read_b32 v16,v7 offset:256
v_mfma_f32_16x16x32_bf16 v[40:43],a[12:15],v[48:51],v[40:43]
s_mov_b32 m0,s62
v_rcp_f32 v76,v76
v_mfma_f32_16x16x32_bf16 v[36:39],a[16:19],v[52:55],v[36:39]
s_mov_b64 s[50:51],s[52:53]
ds_read_b128 v[48:51],v2 offset:58368
v_fma_f32 v78,v78,v95,v95
v_mfma_f32_16x16x32_bf16 v[40:43],a[20:23],v[52:55],v[40:43]
v_rcp_f32 v77,v77
v_mfma_f32_16x16x32_bf16 v[36:39],a[24:27],v[56:59],v[36:39]
s_waitcnt lgkmcnt(1)
ds_read_b128 v[52:55],v2 offset:59392
v_fma_f32 v79,v79,v95,v95
v_mfma_f32_16x16x32_bf16 v[40:43],a[28:31],v[56:59],v[40:43]
v_cmp_eq_u32 vcc,s60,v16
v_rcp_f32 v78,v78
v_mul_f32 v28,v28,v76
v_mfma_f32_16x16x32_bf16 v[36:39],a[32:35],v[60:63],v[36:39]
s_cselect_b32 vcc_lo,-1,vcc_lo
ds_read_b128 v[56:59],v2 offset:60416
v_mul_f32 v29,v29,v77
v_mfma_f32_16x16x32_bf16 v[40:43],a[36:39],v[60:63],v[40:43]
s_cmp_eq_u32 vcc_lo,-1
v_rcp_f32 v79,v79
v_mfma_f32_16x16x32_bf16 v[36:39],a[40:43],v[64:67],v[36:39]
s_cselect_b32 s66,s86,s59
ds_read_b128 v[60:63],v2 offset:61440
v_mul_f32 v30,v30,v78
v_mfma_f32_16x16x32_bf16 v[40:43],a[44:47],v[64:67],v[40:43]
s_add_u32 s74,s53,s55
v_mul_f32 v31,v31,v79
v_cvt_pk_bf16_f32 v84,v28,v29
v_mfma_f32_16x16x32_bf16 v[36:39],a[48:51],v[68:71],v[36:39]
s_cmp_eq_u32 s74,s56
ds_read_b128 v[64:67],v2 offset:62464
v_cvt_pk_bf16_f32 v85,v30,v31
v_mfma_f32_16x16x32_bf16 v[40:43],a[52:55],v[68:71],v[40:43]
s_cselect_b32 s74,s57,s74
v_fma_f32 v32,-v92,v24,v32
v_fma_f32 v33,-v92,v25,v33
v_mfma_f32_16x16x32_bf16 v[36:39],a[56:59],v[72:75],v[36:39]
s_cmp_eq_u32 s74,s58
ds_read_b128 v[68:71],v2 offset:63488
v_fma_f32 v34,-v92,v26,v34
v_mfma_f32_16x16x32_bf16 v[40:43],a[60:63],v[72:75],v[40:43]
s_waitcnt lgkmcnt(3)
v_fma_f32 v35,-v92,v27,v35
v_mul_f32 v80,v32,v32
v_mfma_f32_16x16x32_bf16 v[36:39],a[192:195],v[44:47],v[36:39]
s_cselect_b32 s74,s59,s74
ds_read_b128 v[72:75],v2 offset:64512
v_mul_f32 v81,v33,v33
v_mfma_f32_16x16x32_bf16 v[40:43],a[196:199],v[44:47],v[40:43]
s_xor_b32 s64,s64,65536
v_mul_f32 v82,v34,v34
v_mul_f32 v83,v35,v35
v_mfma_f32_16x16x32_bf16 v[104:107],v[100:103],v[44:47],v[104:107]
s_mov_b32 s52,s74
v_fma_f32 v80,v94,v80,v93
v_mul_f32 v80,v32,v80
v_mfma_f32_16x16x32_bf16 v[108:111],v[44:47],v[44:47],v[108:111]
s_cmp_lt_u32 s67,4194304
v_exp_f32 v80,v80
v_mfma_f32_16x16x32_bf16 v[36:39],a[200:203],v[48:51],v[36:39]
ds_read_b128 v[44:47],v2 offset:49152
v_fma_f32 v80,v80,v95,v95
v_mfma_f32_16x16x32_bf16 v[40:43],a[204:207],v[48:51],v[40:43]
v_rcp_f32 v80,v80
v_mfma_f32_16x16x32_bf16 v[104:107],v[100:103],v[48:51],v[104:107]
v_fma_f32 v81,v94,v81,v93
v_mul_f32 v81,v33,v81
v_mfma_f32_16x16x32_bf16 v[108:111],v[48:51],v[48:51],v[108:111]
v_exp_f32 v81,v81
v_mfma_f32_16x16x32_bf16 v[36:39],a[208:211],v[52:55],v[36:39]
ds_read_b128 v[48:51],v2 offset:50176
v_fma_f32 v81,v81,v95,v95
v_mfma_f32_16x16x32_bf16 v[40:43],a[212:215],v[52:55],v[40:43]
v_rcp_f32 v81,v81
v_mfma_f32_16x16x32_bf16 v[104:107],v[100:103],v[52:55],v[104:107]
v_fma_f32 v82,v94,v82,v93
v_mul_f32 v82,v34,v82
v_mfma_f32_16x16x32_bf16 v[108:111],v[52:55],v[52:55],v[108:111]
v_exp_f32 v82,v82
v_mfma_f32_16x16x32_bf16 v[36:39],a[216:219],v[56:59],v[36:39]
ds_read_b128 v[52:55],v2 offset:51200
v_fma_f32 v82,v82,v95,v95
v_mfma_f32_16x16x32_bf16 v[40:43],a[220:223],v[56:59],v[40:43]
v_rcp_f32 v82,v82
v_mfma_f32_16x16x32_bf16 v[104:107],v[100:103],v[56:59],v[104:107]
v_fma_f32 v83,v94,v83,v93
v_mul_f32 v83,v35,v83
v_mfma_f32_16x16x32_bf16 v[108:111],v[56:59],v[56:59],v[108:111]
s_waitcnt lgkmcnt(3)
v_exp_f32 v83,v83
v_mfma_f32_16x16x32_bf16 v[36:39],a[224:227],v[60:63],v[36:39]
ds_read_b128 v[56:59],v2 offset:52224
v_fma_f32 v83,v83,v95,v95
v_mfma_f32_16x16x32_bf16 v[40:43],a[228:231],v[60:63],v[40:43]
v_rcp_f32 v83,v83
v_mfma_f32_16x16x32_bf16 v[36:39],a[232:235],v[64:67],v[36:39]
buffer_load_dwordx4 v0,s[36:39],s66 offen offset:0 sc1 lds
v_cndmask_b32 v112,v108,v109,s[82:83]
v_cndmask_b32 v113,v110,v111,s[82:83]
v_cndmask_b32 v112,v112,v113,s[84:85]
v_cndmask_b32 v112,v104,v112,s[98:99]
v_mfma_f32_16x16x32_bf16 v[40:43],a[236:239],v[64:67],v[40:43]
ds_read_b128 v[60:63],v2 offset:53248
ds_write_b32 v13,v112 offset:2048
v_mfma_f32_16x16x32_bf16 v[36:39],a[240:243],v[68:71],v[36:39]
ds_read_b128 v[64:67],v2 offset:54272
v_mul_f32 v32,v32,v80
v_mfma_f32_16x16x32_bf16 v[40:43],a[244:247],v[68:71],v[40:43]
v_mul_f32 v33,v33,v81
v_mul_f32 v34,v34,v82
v_mfma_f32_16x16x32_bf16 v[36:39],a[248:251],v[72:75],v[36:39]
buffer_load_dwordx4 v0,s[36:39],s66 offen offset:1024 sc1 lds
v_mfma_f32_16x16x32_bf16 v[40:43],a[252:255],v[72:75],v[40:43]
s_waitcnt lgkmcnt(3)
ds_read_b128 v[68:71],v2 offset:55296
v_mul_f32 v35,v35,v83
v_mfma_f32_16x16x32_bf16 v[36:39],a[128:131],v[44:47],v[36:39]
s_waitcnt vmcnt(7) lgkmcnt(2)
ds_read_b128 v[72:75],v2 offset:56320
v_xor_b32 v2,65536,v2
v_mfma_f32_16x16x32_bf16 v[40:43],a[132:135],v[44:47],v[40:43]
s_barrier
v_mfma_f32_16x16x32_bf16 v[36:39],a[136:139],v[48:51],v[36:39]
ds_read_b128 v[116:119],v14 offset:2048
ds_read_b128 v[120:123],v14 offset:2064
v_mfma_f32_16x16x32_bf16 v[40:43],a[140:143],v[48:51],v[40:43]
ds_read_b128 v[44:47],v2 offset:8192
v_cvt_pk_bf16_f32 v86,v32,v33
v_mfma_f32_16x16x32_bf16 v[36:39],a[144:147],v[52:55],v[36:39]
buffer_load_dwordx4 v0,s[36:39],s66 offen offset:2048 sc1 lds
v_mfma_f32_16x16x32_bf16 v[40:43],a[148:151],v[52:55],v[40:43]
ds_read_b128 v[48:51],v2 offset:9216
v_cvt_pk_bf16_f32 v87,v34,v35
v_mfma_f32_16x16x32_bf16 v[36:39],a[152:155],v[56:59],v[36:39]
s_waitcnt lgkmcnt(2)
ds_read_b128 v[52:55],v2 offset:10240
v_xor_b32 v8,16384,v8
v_mfma_f32_16x16x32_bf16 v[40:43],a[156:159],v[56:59],v[40:43]
v_add_f32 v116,v116,v117
v_add_f32 v118,v118,v119
v_mfma_f32_16x16x32_bf16 v[36:39],a[160:163],v[60:63],v[36:39]
buffer_load_dwordx4 v0,s[36:39],s66 offen offset:3072 sc1 lds
v_mfma_f32_16x16x32_bf16 v[40:43],a[164:167],v[60:63],v[40:43]
ds_read_b128 v[56:59],v2 offset:11264
v_add_f32 v116,v116,v118
v_mfma_f32_16x16x32_bf16 v[36:39],a[168:171],v[64:67],v[36:39]
ds_read_b128 v[60:63],v2 offset:12288
v_mul_f32 v96,s75,v116
v_mfma_f32_16x16x32_bf16 v[40:43],a[172:175],v[64:67],v[40:43]
v_add_f32 v120,v120,v121
v_add_f32 v122,v122,v123
v_mfma_f32_16x16x32_bf16 v[36:39],a[176:179],v[68:71],v[36:39]
buffer_store_dwordx4 v[84:87],v3,s[36:39],s73 offen offset:0 sc0 sc1
v_add_f32 v120,v120,v122
v_mul_f32 v120,s75,v120
v_fma_f32 v120,-v96,v96,v120
v_max_f32 v120,0,v120
v_add_f32 v115,v15,v120
v_fma_f32 v120,v15,v115,v120
v_rsq_f32 v115,v115
v_mfma_f32_16x16x32_bf16 v[40:43],a[180:183],v[68:71],v[40:43]
ds_read_b128 v[64:67],v2 offset:13312
v_rsq_f32 v116,v120
v_mul_f32_e64 v114,-v96,v115
v_mfma_f32_16x16x32_bf16 v[36:39],a[184:187],v[72:75],v[36:39]
s_waitcnt lgkmcnt(2)
ds_read_b128 v[68:71],v2 offset:14336
buffer_store_dwordx2 v[114:115],v12,s[36:39],s65 offen offset:0 sc0 sc1
v_mul_f32 v97,s76,v116
v_mul_f32 v98,v116,v116
v_mul_f32 v99,v120,v116)"
R"(
v_mul_f32 v98,s77,v98
v_mul_f32 v98,v98,v97
v_mfma_f32_16x16x32_bf16 v[40:43],a[188:191],v[72:75],v[40:43]
s_cbranch_scc1 Lg1_a0w2_%=
s_cmp_ge_u32 s48,4194304
s_cselect_b64 s[90:91],s[94:95],s[90:91]
s_cselect_b64 s[92:93],s[96:97],s[92:93]
s_cmp_lt_u32 s67,s59
s_cbranch_scc1 Lg1_d0w2_%=
s_branch Lg1_slow0_%=
Lg1_a0w3_%=:
v_mfma_f32_16x16x32_bf16 v[28:31],a[96:99],v[44:47],0
s_sub_u32 s69,s66,1048576
ds_read_b128 v[72:75],v2 offset:11264
s_cmp_lt_u32 s69,2097152
v_mfma_f32_16x16x32_bf16 v[32:35],a[100:103],v[44:47],0
s_cselect_b32 s89,3145728,s89
s_sub_u32 s69,s66,3145728
s_cmp_lt_u32 s69,1048576
v_mfma_f32_16x16x32_bf16 v[104:107],v[100:103],v[44:47],0
s_cselect_b32 s89,4194304,s89
s_add_u32 m0,s62,16384
s_lshr_b32 s73,s47,0
v_mfma_f32_16x16x32_bf16 v[108:111],v[44:47],v[44:47],0
s_lshr_b32 s72,s66,6
v_fma_f32 v36,-v96,v20,v36
v_fma_f32 v37,-v96,v21,v37
v_mfma_f32_16x16x32_bf16 v[28:31],a[104:107],v[48:51],v[28:31]
buffer_load_dwordx4 v1,s[36:39],s66 offen offset:0 sc1 lds
v_mfma_f32_16x16x32_bf16 v[32:35],a[108:111],v[48:51],v[32:35]
s_lshr_b32 s71,s62,2
ds_read_b128 v[44:47],v2 offset:4096
v_fma_f32 v38,-v96,v22,v38
v_mfma_f32_16x16x32_bf16 v[104:107],v[100:103],v[48:51],v[104:107]
s_cmp_lt_u32 s66,4194304
v_fma_f32 v39,-v96,v23,v39
v_mul_f32 v76,v36,v36
v_mfma_f32_16x16x32_bf16 v[108:111],v[48:51],v[48:51],v[108:111]
s_cselect_b32 s72,s59,s72
v_mul_f32 v77,v37,v37
v_mul_f32 v78,v38,v38
v_mfma_f32_16x16x32_bf16 v[28:31],a[112:115],v[52:55],v[28:31]
buffer_load_dwordx4 v1,s[36:39],s66 offen offset:1024 sc1 lds
v_mfma_f32_16x16x32_bf16 v[32:35],a[116:119],v[52:55],v[32:35]
ds_read_b128 v[48:51],v2 offset:5120
v_mul_f32 v79,v39,v39
v_mfma_f32_16x16x32_bf16 v[104:107],v[100:103],v[52:55],v[104:107]
v_fma_f32 v76,v98,v76,v97
v_mul_f32 v76,v36,v76
v_mfma_f32_16x16x32_bf16 v[108:111],v[52:55],v[52:55],v[108:111]
v_exp_f32 v76,v76
v_mfma_f32_16x16x32_bf16 v[28:31],a[120:123],v[56:59],v[28:31]
buffer_load_dwordx4 v1,s[36:39],s66 offen offset:2048 sc1 lds
v_mfma_f32_16x16x32_bf16 v[32:35],a[124:127],v[56:59],v[32:35]
ds_read_b128 v[52:55],v2 offset:6144
v_fma_f32 v76,v76,v99,v99
v_mfma_f32_16x16x32_bf16 v[104:107],v[100:103],v[56:59],v[104:107]
v_rcp_f32 v76,v76
v_mfma_f32_16x16x32_bf16 v[108:111],v[56:59],v[56:59],v[108:111]
s_waitcnt lgkmcnt(3)
v_fma_f32 v77,v98,v77,v97
v_mul_f32 v77,v37,v77
v_mfma_f32_16x16x32_bf16 v[28:31],a[64:67],v[60:63],v[28:31]
buffer_load_dwordx4 v1,s[36:39],s66 offen offset:3072 sc1 lds
v_mfma_f32_16x16x32_bf16 v[32:35],a[68:71],v[60:63],v[32:35]
s_add_u32 m0,s71,s88
ds_read_b128 v[56:59],v2 offset:7168
v_fma_f32 v78,v98,v78,v97
v_mfma_f32_16x16x32_bf16 v[28:31],a[72:75],v[64:67],v[28:31]
buffer_load_dword v10,s[36:39],s72 offen offset:0 sc1 lds
v_mfma_f32_16x16x32_bf16 v[32:35],a[76:79],v[64:67],v[32:35]
s_xor_b32 s87,s64,65536
ds_read_b128 v[60:63],v2 offset:0
v_mul_f32 v78,v38,v78
v_mfma_f32_16x16x32_bf16 v[28:31],a[80:83],v[68:71],v[28:31]
s_cmp_ge_u32 s66,s59
ds_read_b128 v[64:67],v2 offset:1024
v_fma_f32 v79,v98,v79,v97
v_mfma_f32_16x16x32_bf16 v[32:35],a[84:87],v[68:71],v[32:35]
s_cselect_b32 s86,s49,s50
v_exp_f32 v77,v77
v_mfma_f32_16x16x32_bf16 v[28:31],a[88:91],v[72:75],v[28:31]
s_cselect_b32 s87,s87,s63
ds_read_b128 v[68:71],v2 offset:2048
v_mul_f32 v79,v39,v79
v_mfma_f32_16x16x32_bf16 v[32:35],a[92:95],v[72:75],v[32:35]
s_waitcnt lgkmcnt(3)
v_exp_f32 v78,v78
v_mfma_f32_16x16x32_bf16 v[28:31],a[32:35],v[44:47],v[28:31]
s_waitcnt vmcnt(11)
ds_read_b128 v[72:75],v2 offset:3072
v_fma_f32 v77,v77,v99,v99
v_mfma_f32_16x16x32_bf16 v[32:35],a[36:39],v[44:47],v[32:35]
s_barrier
v_mfma_f32_16x16x32_bf16 v[28:31],a[40:43],v[48:51],v[28:31]
s_mov_b32 s62,s87
ds_read_b128 v[44:47],v2 offset:28672
ds_read_b32 v16,v7 offset:0
v_mfma_f32_16x16x32_bf16 v[32:35],a[44:47],v[48:51],v[32:35]
s_cmp_lt_u32 s86,s89
v_exp_f32 v79,v79
v_mfma_f32_16x16x32_bf16 v[28:31],a[48:51],v[52:55],v[28:31]
s_mov_b32 m0,s62
ds_read_b128 v[48:51],v2 offset:29696
v_fma_f32 v78,v78,v99,v99
v_mfma_f32_16x16x32_bf16 v[32:35],a[52:55],v[52:55],v[32:35]
v_rcp_f32 v77,v77
v_mfma_f32_16x16x32_bf16 v[28:31],a[56:59],v[56:59],v[28:31]
s_waitcnt lgkmcnt(1)
ds_read_b128 v[52:55],v2 offset:30720
v_fma_f32 v79,v79,v99,v99
v_mfma_f32_16x16x32_bf16 v[32:35],a[60:63],v[56:59],v[32:35]
v_cmp_eq_u32 vcc,s60,v16
v_rcp_f32 v78,v78
v_mul_f32 v36,v36,v76
v_mfma_f32_16x16x32_bf16 v[28:31],a[0:3],v[60:63],v[28:31]
s_cselect_b32 vcc_lo,-1,vcc_lo
ds_read_b128 v[56:59],v2 offset:31744
v_mul_f32 v37,v37,v77
v_mfma_f32_16x16x32_bf16 v[32:35],a[4:7],v[60:63],v[32:35]
s_cmp_eq_u32 vcc_lo,-1
v_rcp_f32 v79,v79
v_mfma_f32_16x16x32_bf16 v[28:31],a[8:11],v[64:67],v[28:31]
s_cselect_b32 s67,s86,s59
ds_read_b128 v[60:63],v2 offset:24576
v_mul_f32 v38,v38,v78
v_mfma_f32_16x16x32_bf16 v[32:35],a[12:15],v[64:67],v[32:35]
s_add_u32 s53,s52,s55
v_mul_f32 v39,v39,v79
v_cvt_pk_bf16_f32 v84,v36,v37
v_mfma_f32_16x16x32_bf16 v[28:31],a[16:19],v[68:71],v[28:31]
s_cmp_eq_u32 s53,s56
ds_read_b128 v[64:67],v2 offset:25600
v_cvt_pk_bf16_f32 v85,v38,v39
v_mfma_f32_16x16x32_bf16 v[32:35],a[20:23],v[68:71],v[32:35]
s_cselect_b32 s53,s57,s53
v_fma_f32 v40,-v96,v24,v40
v_fma_f32 v41,-v96,v25,v41
v_mfma_f32_16x16x32_bf16 v[28:31],a[24:27],v[72:75],v[28:31]
s_cmp_eq_u32 s53,s58
ds_read_b128 v[68:71],v2 offset:26624
v_fma_f32 v42,-v96,v26,v42
v_mfma_f32_16x16x32_bf16 v[32:35],a[28:31],v[72:75],v[32:35]
s_waitcnt lgkmcnt(3)
v_fma_f32 v43,-v96,v27,v43
v_mul_f32 v80,v40,v40
v_mfma_f32_16x16x32_bf16 v[28:31],a[224:227],v[44:47],v[28:31]
s_cselect_b32 s53,s59,s53
ds_read_b128 v[72:75],v2 offset:27648
v_mul_f32 v81,v41,v41
v_mfma_f32_16x16x32_bf16 v[32:35],a[228:231],v[44:47],v[32:35]
s_xor_b32 s63,s63,65536
v_mul_f32 v82,v42,v42
v_mul_f32 v83,v43,v43
v_mfma_f32_16x16x32_bf16 v[104:107],v[100:103],v[44:47],v[104:107]
s_lshr_b32 s70,s46,5
v_fma_f32 v80,v98,v80,v97
v_mul_f32 v80,v40,v80
v_mfma_f32_16x16x32_bf16 v[108:111],v[44:47],v[44:47],v[108:111]
s_cmp_ge_u32 s66,s59
v_exp_f32 v80,v80
v_mfma_f32_16x16x32_bf16 v[28:31],a[232:235],v[48:51],v[28:31]
ds_read_b128 v[44:47],v2 offset:20480
v_fma_f32 v80,v80,v99,v99
v_mfma_f32_16x16x32_bf16 v[32:35],a[236:239],v[48:51],v[32:35]
v_rcp_f32 v80,v80
v_mfma_f32_16x16x32_bf16 v[104:107],v[100:103],v[48:51],v[104:107]
v_fma_f32 v81,v98,v81,v97
v_mul_f32 v81,v41,v81
v_mfma_f32_16x16x32_bf16 v[108:111],v[48:51],v[48:51],v[108:111]
v_exp_f32 v81,v81
v_mfma_f32_16x16x32_bf16 v[28:31],a[240:243],v[52:55],v[28:31]
ds_read_b128 v[48:51],v2 offset:21504
v_fma_f32 v81,v81,v99,v99
v_mfma_f32_16x16x32_bf16 v[32:35],a[244:247],v[52:55],v[32:35]
v_rcp_f32 v81,v81
v_mfma_f32_16x16x32_bf16 v[104:107],v[100:103],v[52:55],v[104:107]
v_fma_f32 v82,v98,v82,v97
v_mul_f32 v82,v42,v82
v_mfma_f32_16x16x32_bf16 v[108:111],v[52:55],v[52:55],v[108:111]
v_exp_f32 v82,v82
v_mfma_f32_16x16x32_bf16 v[28:31],a[248:251],v[56:59],v[28:31]
ds_read_b128 v[52:55],v2 offset:22528
v_fma_f32 v82,v82,v99,v99
v_mfma_f32_16x16x32_bf16 v[32:35],a[252:255],v[56:59],v[32:35]
v_rcp_f32 v82,v82
v_mfma_f32_16x16x32_bf16 v[104:107],v[100:103],v[56:59],v[104:107]
v_fma_f32 v83,v98,v83,v97
v_mul_f32 v83,v43,v83
v_mfma_f32_16x16x32_bf16 v[108:111],v[56:59],v[56:59],v[108:111]
s_waitcnt lgkmcnt(3)
v_exp_f32 v83,v83
v_mfma_f32_16x16x32_bf16 v[28:31],a[192:195],v[60:63],v[28:31]
ds_read_b128 v[56:59],v2 offset:23552
v_fma_f32 v83,v83,v99,v99
v_mfma_f32_16x16x32_bf16 v[32:35],a[196:199],v[60:63],v[32:35]
v_rcp_f32 v83,v83
v_mfma_f32_16x16x32_bf16 v[28:31],a[200:203],v[64:67],v[28:31]
v_cndmask_b32 v112,v108,v109,s[82:83]
v_cndmask_b32 v113,v110,v111,s[82:83])"
R"(
v_cndmask_b32 v112,v112,v113,s[84:85]
v_cndmask_b32 v112,v104,v112,s[98:99]
v_mfma_f32_16x16x32_bf16 v[32:35],a[204:207],v[64:67],v[32:35]
ds_read_b128 v[60:63],v2 offset:16384
ds_write_b32 v13,v112 offset:0
buffer_load_dwordx4 v0,s[36:39],s67 offen offset:0 sc1 lds
v_mfma_f32_16x16x32_bf16 v[28:31],a[208:211],v[68:71],v[28:31]
ds_read_b128 v[64:67],v2 offset:17408
v_mul_f32 v40,v40,v80
v_mfma_f32_16x16x32_bf16 v[32:35],a[212:215],v[68:71],v[32:35]
v_mul_f32 v41,v41,v81
v_mul_f32 v42,v42,v82
v_mfma_f32_16x16x32_bf16 v[28:31],a[216:219],v[72:75],v[28:31]
ds_read_b128 v[68:71],v2 offset:18432
v_mul_f32 v43,v43,v83
v_mfma_f32_16x16x32_bf16 v[32:35],a[220:223],v[72:75],v[32:35]
s_waitcnt lgkmcnt(4)
buffer_load_dwordx4 v0,s[36:39],s67 offen offset:1024 sc1 lds
v_mfma_f32_16x16x32_bf16 v[28:31],a[160:163],v[44:47],v[28:31]
s_waitcnt vmcnt(7) lgkmcnt(2)
ds_read_b128 v[72:75],v2 offset:19456
v_cvt_pk_bf16_f32 v86,v40,v41
v_mfma_f32_16x16x32_bf16 v[32:35],a[164:167],v[44:47],v[32:35]
s_barrier
v_mfma_f32_16x16x32_bf16 v[28:31],a[168:171],v[48:51],v[28:31]
buffer_store_dword v5,v4,s[36:39],s70 offen offset:0 sc0 sc1
ds_read_b128 v[116:119],v14 offset:0
ds_read_b128 v[120:123],v14 offset:16
ds_read_b128 v[44:47],v2 offset:45056
v_mfma_f32_16x16x32_bf16 v[32:35],a[172:175],v[48:51],v[32:35]
v_cvt_pk_bf16_f32 v87,v42,v43
v_mfma_f32_16x16x32_bf16 v[28:31],a[176:179],v[52:55],v[28:31]
ds_read_b128 v[48:51],v2 offset:46080
v_mfma_f32_16x16x32_bf16 v[32:35],a[180:183],v[52:55],v[32:35]
buffer_load_dwordx4 v0,s[36:39],s67 offen offset:2048 sc1 lds
v_mfma_f32_16x16x32_bf16 v[28:31],a[184:187],v[56:59],v[28:31]
s_waitcnt lgkmcnt(2)
ds_read_b128 v[52:55],v2 offset:47104
v_add_f32 v116,v116,v117
v_mfma_f32_16x16x32_bf16 v[32:35],a[188:191],v[56:59],v[32:35]
v_add_f32 v118,v118,v119
v_add_f32 v116,v116,v118
v_mfma_f32_16x16x32_bf16 v[28:31],a[128:131],v[60:63],v[28:31]
ds_read_b128 v[56:59],v2 offset:48128
v_mul_f32 v92,s75,v116
v_mfma_f32_16x16x32_bf16 v[32:35],a[132:135],v[60:63],v[32:35]
buffer_load_dwordx4 v0,s[36:39],s67 offen offset:3072 sc1 lds
v_mfma_f32_16x16x32_bf16 v[28:31],a[136:139],v[64:67],v[28:31]
ds_read_b128 v[60:63],v2 offset:40960
v_add_f32 v120,v120,v121
v_mfma_f32_16x16x32_bf16 v[32:35],a[140:143],v[64:67],v[32:35]
v_add_f32 v122,v122,v123
v_add_f32 v120,v120,v122
v_mfma_f32_16x16x32_bf16 v[28:31],a[144:147],v[68:71],v[28:31]
ds_read_b128 v[64:67],v2 offset:41984
v_mul_f32 v120,s75,v120
v_mfma_f32_16x16x32_bf16 v[32:35],a[148:151],v[68:71],v[32:35]
buffer_store_dwordx4 v[84:87],v3,s[36:39],s73 offen offset:0 sc0 sc1
v_fma_f32 v120,-v92,v92,v120
v_max_f32 v120,0,v120
v_add_f32 v115,v15,v120
v_fma_f32 v120,v15,v115,v120
v_rsq_f32 v116,v120
v_mfma_f32_16x16x32_bf16 v[28:31],a[152:155],v[72:75],v[28:31]
s_waitcnt lgkmcnt(2)
ds_read_b128 v[68:71],v2 offset:43008
v_mul_f32 v93,s76,v116
v_mul_f32 v94,v116,v116
v_mul_f32 v95,v120,v116
v_mul_f32 v94,s77,v94
v_mul_f32 v94,v94,v93
v_mfma_f32_16x16x32_bf16 v[32:35],a[156:159],v[72:75],v[32:35]
s_cbranch_scc1 Lg1_slow1_%=
Lg1_a1w3_%=:
v_mfma_f32_16x16x32_bf16 v[36:39],a[96:99],v[44:47],0
s_sub_u32 s69,s67,1048576
ds_read_b128 v[72:75],v2 offset:44032
s_cmp_lt_u32 s69,2097152
v_mfma_f32_16x16x32_bf16 v[40:43],a[100:103],v[44:47],0
s_cselect_b32 s89,3145728,s89
s_sub_u32 s69,s67,3145728
s_cmp_lt_u32 s69,1048576
v_mfma_f32_16x16x32_bf16 v[104:107],v[100:103],v[44:47],0
s_cselect_b32 s89,4194304,s89
s_add_u32 m0,s62,16384
s_lshr_b32 s73,s48,0
v_mfma_f32_16x16x32_bf16 v[108:111],v[44:47],v[44:47],0
s_lshr_b32 s72,s67,6
v_fma_f32 v28,-v92,v20,v28
v_fma_f32 v29,-v92,v21,v29
v_mfma_f32_16x16x32_bf16 v[36:39],a[104:107],v[48:51],v[36:39]
buffer_load_dwordx4 v1,s[36:39],s67 offen offset:0 sc1 lds
v_mfma_f32_16x16x32_bf16 v[40:43],a[108:111],v[48:51],v[40:43]
s_lshr_b32 s71,s62,2
ds_read_b128 v[44:47],v2 offset:36864
v_fma_f32 v30,-v92,v22,v30
v_mfma_f32_16x16x32_bf16 v[104:107],v[100:103],v[48:51],v[104:107]
s_cmp_lt_u32 s67,4194304
v_fma_f32 v31,-v92,v23,v31
v_mul_f32 v76,v28,v28
v_mfma_f32_16x16x32_bf16 v[108:111],v[48:51],v[48:51],v[108:111]
s_cselect_b32 s72,s59,s72
v_mul_f32 v77,v29,v29
v_mul_f32 v78,v30,v30
v_mfma_f32_16x16x32_bf16 v[36:39],a[112:115],v[52:55],v[36:39]
buffer_load_dwordx4 v1,s[36:39],s67 offen offset:1024 sc1 lds
v_mfma_f32_16x16x32_bf16 v[40:43],a[116:119],v[52:55],v[40:43]
s_mov_b64 s[44:45],s[46:47]
ds_read_b128 v[48:51],v2 offset:37888
v_mul_f32 v79,v31,v31
v_mfma_f32_16x16x32_bf16 v[104:107],v[100:103],v[52:55],v[104:107]
v_fma_f32 v76,v94,v76,v93
v_mul_f32 v76,v28,v76
v_mfma_f32_16x16x32_bf16 v[108:111],v[52:55],v[52:55],v[108:111]
v_exp_f32 v76,v76
v_mfma_f32_16x16x32_bf16 v[36:39],a[120:123],v[56:59],v[36:39]
buffer_load_dwordx4 v1,s[36:39],s67 offen offset:2048 sc1 lds
v_mfma_f32_16x16x32_bf16 v[40:43],a[124:127],v[56:59],v[40:43]
ds_read_b128 v[52:55],v2 offset:38912
v_fma_f32 v76,v76,v95,v95
v_mfma_f32_16x16x32_bf16 v[104:107],v[100:103],v[56:59],v[104:107]
v_rcp_f32 v76,v76
v_mfma_f32_16x16x32_bf16 v[108:111],v[56:59],v[56:59],v[108:111]
s_waitcnt lgkmcnt(3)
v_fma_f32 v77,v94,v77,v93
v_mul_f32 v77,v29,v77
v_mfma_f32_16x16x32_bf16 v[36:39],a[64:67],v[60:63],v[36:39]
buffer_load_dwordx4 v1,s[36:39],s67 offen offset:3072 sc1 lds
v_mfma_f32_16x16x32_bf16 v[40:43],a[68:71],v[60:63],v[40:43]
s_add_u32 m0,s71,s88
ds_read_b128 v[56:59],v2 offset:39936
v_fma_f32 v78,v94,v78,v93
v_mfma_f32_16x16x32_bf16 v[36:39],a[72:75],v[64:67],v[36:39]
buffer_load_dword v10,s[36:39],s72 offen offset:0 sc1 lds
v_mfma_f32_16x16x32_bf16 v[40:43],a[76:79],v[64:67],v[40:43]
s_xor_b32 s87,s63,65536
ds_read_b128 v[60:63],v2 offset:32768
v_mul_f32 v78,v30,v78
v_mfma_f32_16x16x32_bf16 v[36:39],a[80:83],v[68:71],v[36:39]
s_cmp_ge_u32 s67,s59
ds_read_b128 v[64:67],v2 offset:33792
v_fma_f32 v79,v94,v79,v93
v_mfma_f32_16x16x32_bf16 v[40:43],a[84:87],v[68:71],v[40:43]
s_cselect_b32 s86,s50,s51
v_exp_f32 v77,v77
v_mfma_f32_16x16x32_bf16 v[36:39],a[88:91],v[72:75],v[36:39]
s_cselect_b32 s87,s87,s64
ds_read_b128 v[68:71],v2 offset:34816
v_mul_f32 v79,v31,v79
v_mfma_f32_16x16x32_bf16 v[40:43],a[92:95],v[72:75],v[40:43]
s_waitcnt lgkmcnt(3)
v_exp_f32 v78,v78
v_mfma_f32_16x16x32_bf16 v[36:39],a[32:35],v[44:47],v[36:39]
s_waitcnt vmcnt(11)
ds_read_b128 v[72:75],v2 offset:35840
v_fma_f32 v77,v77,v95,v95
v_mfma_f32_16x16x32_bf16 v[40:43],a[36:39],v[44:47],v[40:43]
s_barrier
v_mfma_f32_16x16x32_bf16 v[36:39],a[40:43],v[48:51],v[36:39]
s_mov_b32 s62,s87
ds_read_b128 v[44:47],v2 offset:61440
ds_read_b32 v16,v7 offset:256
v_mfma_f32_16x16x32_bf16 v[40:43],a[44:47],v[48:51],v[40:43]
s_cmp_lt_u32 s86,s89
v_exp_f32 v79,v79
v_mfma_f32_16x16x32_bf16 v[36:39],a[48:51],v[52:55],v[36:39]
s_mov_b32 m0,s62
ds_read_b128 v[48:51],v2 offset:62464
v_fma_f32 v78,v78,v95,v95
v_mfma_f32_16x16x32_bf16 v[40:43],a[52:55],v[52:55],v[40:43]
v_rcp_f32 v77,v77
v_mfma_f32_16x16x32_bf16 v[36:39],a[56:59],v[56:59],v[36:39]
s_waitcnt lgkmcnt(1)
ds_read_b128 v[52:55],v2 offset:63488
v_fma_f32 v79,v79,v95,v95
v_mfma_f32_16x16x32_bf16 v[40:43],a[60:63],v[56:59],v[40:43]
v_cmp_eq_u32 vcc,s60,v16
v_rcp_f32 v78,v78
v_mul_f32 v28,v28,v76
v_mfma_f32_16x16x32_bf16 v[36:39],a[0:3],v[60:63],v[36:39]
s_cselect_b32 vcc_lo,-1,vcc_lo
ds_read_b128 v[56:59],v2 offset:64512
v_mul_f32 v29,v29,v77
v_mfma_f32_16x16x32_bf16 v[40:43],a[4:7],v[60:63],v[40:43]
s_cmp_eq_u32 vcc_lo,-1
v_rcp_f32 v79,v79
v_mfma_f32_16x16x32_bf16 v[36:39],a[8:11],v[64:67],v[36:39]
s_cselect_b32 s66,s86,s59
ds_read_b128 v[60:63],v2 offset:57344
v_mul_f32 v30,v30,v78
v_mfma_f32_16x16x32_bf16 v[40:43],a[12:15],v[64:67],v[40:43]
s_add_u32 s74,s53,s55
v_mul_f32 v31,v31,v79
v_cvt_pk_bf16_f32 v84,v28,v29
v_mfma_f32_16x16x32_bf16 v[36:39],a[16:19],v[68:71],v[36:39]
s_cmp_eq_u32 s74,s56
ds_read_b128 v[64:67],v2 offset:58368
v_cvt_pk_bf16_f32 v85,v30,v31
v_mfma_f32_16x16x32_bf16 v[40:43],a[20:23],v[68:71],v[40:43]
s_cselect_b32 s74,s57,s74)"
R"(
v_fma_f32 v32,-v92,v24,v32
v_fma_f32 v33,-v92,v25,v33
v_mfma_f32_16x16x32_bf16 v[36:39],a[24:27],v[72:75],v[36:39]
s_cmp_eq_u32 s74,s58
ds_read_b128 v[68:71],v2 offset:59392
v_fma_f32 v34,-v92,v26,v34
v_mfma_f32_16x16x32_bf16 v[40:43],a[28:31],v[72:75],v[40:43]
s_waitcnt lgkmcnt(3)
v_fma_f32 v35,-v92,v27,v35
v_mul_f32 v80,v32,v32
v_mfma_f32_16x16x32_bf16 v[36:39],a[224:227],v[44:47],v[36:39]
s_cselect_b32 s74,s59,s74
ds_read_b128 v[72:75],v2 offset:60416
v_mul_f32 v81,v33,v33
v_mfma_f32_16x16x32_bf16 v[40:43],a[228:231],v[44:47],v[40:43]
s_xor_b32 s64,s64,65536
v_mul_f32 v82,v34,v34
v_mul_f32 v83,v35,v35
v_mfma_f32_16x16x32_bf16 v[104:107],v[100:103],v[44:47],v[104:107]
s_lshr_b32 s70,s47,5
v_fma_f32 v80,v94,v80,v93
v_mul_f32 v80,v32,v80
v_mfma_f32_16x16x32_bf16 v[108:111],v[44:47],v[44:47],v[108:111]
s_mov_b64 s[46:47],s[48:49]
v_exp_f32 v80,v80
v_mfma_f32_16x16x32_bf16 v[36:39],a[232:235],v[48:51],v[36:39]
s_mov_b64 s[48:49],s[50:51]
ds_read_b128 v[44:47],v2 offset:53248
v_fma_f32 v80,v80,v95,v95
v_mfma_f32_16x16x32_bf16 v[40:43],a[236:239],v[48:51],v[40:43]
s_mov_b64 s[50:51],s[52:53]
v_rcp_f32 v80,v80
v_mfma_f32_16x16x32_bf16 v[104:107],v[100:103],v[48:51],v[104:107]
s_mov_b32 s52,s74
v_fma_f32 v81,v94,v81,v93
v_mul_f32 v81,v33,v81
v_mfma_f32_16x16x32_bf16 v[108:111],v[48:51],v[48:51],v[108:111]
s_cmp_lt_u32 s67,4194304
v_exp_f32 v81,v81
v_mfma_f32_16x16x32_bf16 v[36:39],a[240:243],v[52:55],v[36:39]
ds_read_b128 v[48:51],v2 offset:54272
v_fma_f32 v81,v81,v95,v95
v_mfma_f32_16x16x32_bf16 v[40:43],a[244:247],v[52:55],v[40:43]
v_rcp_f32 v81,v81
v_mfma_f32_16x16x32_bf16 v[104:107],v[100:103],v[52:55],v[104:107]
v_fma_f32 v82,v94,v82,v93
v_mul_f32 v82,v34,v82
v_mfma_f32_16x16x32_bf16 v[108:111],v[52:55],v[52:55],v[108:111]
v_exp_f32 v82,v82
v_mfma_f32_16x16x32_bf16 v[36:39],a[248:251],v[56:59],v[36:39]
ds_read_b128 v[52:55],v2 offset:55296
v_fma_f32 v82,v82,v95,v95
v_mfma_f32_16x16x32_bf16 v[40:43],a[252:255],v[56:59],v[40:43]
v_rcp_f32 v82,v82
v_mfma_f32_16x16x32_bf16 v[104:107],v[100:103],v[56:59],v[104:107]
v_fma_f32 v83,v94,v83,v93
v_mul_f32 v83,v35,v83
v_mfma_f32_16x16x32_bf16 v[108:111],v[56:59],v[56:59],v[108:111]
s_waitcnt lgkmcnt(3)
v_exp_f32 v83,v83
v_mfma_f32_16x16x32_bf16 v[36:39],a[192:195],v[60:63],v[36:39]
ds_read_b128 v[56:59],v2 offset:56320
v_fma_f32 v83,v83,v95,v95
v_mfma_f32_16x16x32_bf16 v[40:43],a[196:199],v[60:63],v[40:43]
v_rcp_f32 v83,v83
v_mfma_f32_16x16x32_bf16 v[36:39],a[200:203],v[64:67],v[36:39]
v_cndmask_b32 v112,v108,v109,s[82:83]
v_cndmask_b32 v113,v110,v111,s[82:83]
v_cndmask_b32 v112,v112,v113,s[84:85]
v_cndmask_b32 v112,v104,v112,s[98:99]
v_mfma_f32_16x16x32_bf16 v[40:43],a[204:207],v[64:67],v[40:43]
ds_read_b128 v[60:63],v2 offset:49152
ds_write_b32 v13,v112 offset:2048
buffer_load_dwordx4 v0,s[36:39],s66 offen offset:0 sc1 lds
v_mfma_f32_16x16x32_bf16 v[36:39],a[208:211],v[68:71],v[36:39]
ds_read_b128 v[64:67],v2 offset:50176
v_mul_f32 v32,v32,v80
v_mfma_f32_16x16x32_bf16 v[40:43],a[212:215],v[68:71],v[40:43]
v_mul_f32 v33,v33,v81
v_mul_f32 v34,v34,v82
v_mfma_f32_16x16x32_bf16 v[36:39],a[216:219],v[72:75],v[36:39]
ds_read_b128 v[68:71],v2 offset:51200
v_mul_f32 v35,v35,v83
v_mfma_f32_16x16x32_bf16 v[40:43],a[220:223],v[72:75],v[40:43]
s_waitcnt lgkmcnt(4)
buffer_load_dwordx4 v0,s[36:39],s66 offen offset:1024 sc1 lds
v_mfma_f32_16x16x32_bf16 v[36:39],a[160:163],v[44:47],v[36:39]
s_waitcnt vmcnt(7) lgkmcnt(2)
ds_read_b128 v[72:75],v2 offset:52224
v_xor_b32 v2,65536,v2
v_mfma_f32_16x16x32_bf16 v[40:43],a[164:167],v[44:47],v[40:43]
s_barrier
v_mfma_f32_16x16x32_bf16 v[36:39],a[168:171],v[48:51],v[36:39]
buffer_store_dword v5,v4,s[36:39],s70 offen offset:0 sc0 sc1
ds_read_b128 v[116:119],v14 offset:2048
ds_read_b128 v[120:123],v14 offset:2064
ds_read_b128 v[44:47],v2 offset:12288
v_mfma_f32_16x16x32_bf16 v[40:43],a[172:175],v[48:51],v[40:43]
v_cvt_pk_bf16_f32 v86,v32,v33
v_cvt_pk_bf16_f32 v87,v34,v35
v_mfma_f32_16x16x32_bf16 v[36:39],a[176:179],v[52:55],v[36:39]
ds_read_b128 v[48:51],v2 offset:13312
v_xor_b32 v8,16384,v8
v_mfma_f32_16x16x32_bf16 v[40:43],a[180:183],v[52:55],v[40:43]
buffer_load_dwordx4 v0,s[36:39],s66 offen offset:2048 sc1 lds
v_mfma_f32_16x16x32_bf16 v[36:39],a[184:187],v[56:59],v[36:39]
s_waitcnt lgkmcnt(2)
ds_read_b128 v[52:55],v2 offset:14336
v_add_f32 v116,v116,v117
v_mfma_f32_16x16x32_bf16 v[40:43],a[188:191],v[56:59],v[40:43]
v_add_f32 v118,v118,v119
v_add_f32 v116,v116,v118
v_mfma_f32_16x16x32_bf16 v[36:39],a[128:131],v[60:63],v[36:39]
ds_read_b128 v[56:59],v2 offset:15360
v_mul_f32 v96,s75,v116
v_mfma_f32_16x16x32_bf16 v[40:43],a[132:135],v[60:63],v[40:43]
buffer_load_dwordx4 v0,s[36:39],s66 offen offset:3072 sc1 lds
v_mfma_f32_16x16x32_bf16 v[36:39],a[136:139],v[64:67],v[36:39]
ds_read_b128 v[60:63],v2 offset:8192
v_add_f32 v120,v120,v121
v_mfma_f32_16x16x32_bf16 v[40:43],a[140:143],v[64:67],v[40:43]
v_add_f32 v122,v122,v123
v_add_f32 v120,v120,v122
v_mfma_f32_16x16x32_bf16 v[36:39],a[144:147],v[68:71],v[36:39]
ds_read_b128 v[64:67],v2 offset:9216
v_mul_f32 v120,s75,v120
v_mfma_f32_16x16x32_bf16 v[40:43],a[148:151],v[68:71],v[40:43]
buffer_store_dwordx4 v[84:87],v3,s[36:39],s73 offen offset:0 sc0 sc1
v_fma_f32 v120,-v96,v96,v120
v_max_f32 v120,0,v120
v_add_f32 v115,v15,v120
v_fma_f32 v120,v15,v115,v120
v_rsq_f32 v116,v120
v_mfma_f32_16x16x32_bf16 v[36:39],a[152:155],v[72:75],v[36:39]
s_waitcnt lgkmcnt(2)
ds_read_b128 v[68:71],v2 offset:10240
v_mul_f32 v97,s76,v116
v_mul_f32 v98,v116,v116
v_mul_f32 v99,v120,v116
v_mul_f32 v98,s77,v98
v_mul_f32 v98,v98,v97
v_mfma_f32_16x16x32_bf16 v[40:43],a[156:159],v[72:75],v[40:43]
s_cbranch_scc1 Lg1_a0w3_%=
s_cmp_ge_u32 s48,4194304
s_cselect_b64 s[90:91],s[94:95],s[90:91]
s_cselect_b64 s[92:93],s[96:97],s[92:93]
s_cmp_lt_u32 s67,s59
s_cbranch_scc1 Lg1_d0w3_%=
s_branch Lg1_slow0_%=
Lg1_d0w0_%=:
v_mfma_f32_16x16x32_bf16 v[28:31],a[0:3],v[44:47],0
s_add_u32 m0,s62,16384
ds_read_b128 v[72:75],v2 offset:7168
s_lshr_b32 s73,s47,0
v_mfma_f32_16x16x32_bf16 v[32:35],a[4:7],v[44:47],0
buffer_load_dwordx4 v1,s[36:39],s66 offen offset:0 sc1 lds
v_mfma_f32_16x16x32_bf16 v[28:31],a[8:11],v[48:51],v[28:31]
s_lshr_b32 s72,s66,6
ds_read_b128 v[44:47],v2 offset:8192
s_lshr_b32 s71,s62,2
v_mfma_f32_16x16x32_bf16 v[32:35],a[12:15],v[48:51],v[32:35]
s_cmp_lt_u32 s66,4194304
v_fma_f32 v36,-v96,v20,v36
v_fma_f32 v37,-v96,v21,v37
v_mfma_f32_16x16x32_bf16 v[28:31],a[16:19],v[52:55],v[28:31]
s_cselect_b32 s72,s59,s72
ds_read_b128 v[48:51],v2 offset:9216
v_fma_f32 v38,-v96,v22,v38
v_mfma_f32_16x16x32_bf16 v[32:35],a[20:23],v[52:55],v[32:35]
buffer_load_dwordx4 v1,s[36:39],s66 offen offset:1024 sc1 lds
v_mfma_f32_16x16x32_bf16 v[28:31],a[24:27],v[56:59],v[28:31]
ds_read_b128 v[52:55],v2 offset:10240
v_fma_f32 v39,-v96,v23,v39
v_mfma_f32_16x16x32_bf16 v[32:35],a[28:31],v[56:59],v[32:35]
s_waitcnt lgkmcnt(3)
v_mul_f32 v76,v36,v36
v_mul_f32 v77,v37,v37
v_mfma_f32_16x16x32_bf16 v[28:31],a[32:35],v[60:63],v[28:31]
ds_read_b128 v[56:59],v2 offset:11264
v_mul_f32 v78,v38,v38
v_mfma_f32_16x16x32_bf16 v[32:35],a[36:39],v[60:63],v[32:35]
buffer_load_dwordx4 v1,s[36:39],s66 offen offset:2048 sc1 lds
v_mfma_f32_16x16x32_bf16 v[28:31],a[40:43],v[64:67],v[28:31]
ds_read_b128 v[60:63],v2 offset:12288
v_mul_f32 v79,v39,v39
v_mfma_f32_16x16x32_bf16 v[32:35],a[44:47],v[64:67],v[32:35]
v_fma_f32 v76,v98,v76,v97
v_mul_f32 v76,v36,v76
v_mfma_f32_16x16x32_bf16 v[28:31],a[48:51],v[68:71],v[28:31]
ds_read_b128 v[64:67],v2 offset:13312
v_fma_f32 v77,v98,v77,v97
v_mfma_f32_16x16x32_bf16 v[32:35],a[52:55],v[68:71],v[32:35]
buffer_load_dwordx4 v1,s[36:39],s66 offen offset:3072 sc1 lds
v_mfma_f32_16x16x32_bf16 v[28:31],a[56:59],v[72:75],v[28:31]
s_add_u32 m0,s71,s88
ds_read_b128 v[68:71],v2 offset:14336
v_mul_f32 v77,v37,v77
v_mfma_f32_16x16x32_bf16 v[32:35],a[60:63],v[72:75],v[32:35]
s_waitcnt lgkmcnt(3)
buffer_load_dword v10,s[36:39],s72 offen offset:0 sc1 lds)"
R"(
v_mfma_f32_16x16x32_bf16 v[28:31],a[64:67],v[44:47],v[28:31]
s_waitcnt vmcnt(11)
ds_read_b128 v[72:75],v2 offset:15360
v_fma_f32 v78,v98,v78,v97
v_mfma_f32_16x16x32_bf16 v[32:35],a[68:71],v[44:47],v[32:35]
s_barrier
v_mfma_f32_16x16x32_bf16 v[28:31],a[72:75],v[48:51],v[28:31]
s_xor_b32 s87,s64,65536
ds_read_b128 v[44:47],v2 offset:16384
v_fma_f32 v79,v98,v79,v97
v_mfma_f32_16x16x32_bf16 v[32:35],a[76:79],v[48:51],v[32:35]
s_cmp_ge_u32 s66,s59
v_exp_f32 v76,v76
v_mfma_f32_16x16x32_bf16 v[28:31],a[80:83],v[52:55],v[28:31]
s_cselect_b32 s86,s49,s50
ds_read_b128 v[48:51],v2 offset:17408
v_mul_f32 v78,v38,v78
v_mfma_f32_16x16x32_bf16 v[32:35],a[84:87],v[52:55],v[32:35]
s_cselect_b32 s87,s87,s63
v_exp_f32 v77,v77
v_mfma_f32_16x16x32_bf16 v[28:31],a[88:91],v[56:59],v[28:31]
s_lshr_b32 s69,s50,5
ds_read_b128 v[52:55],v2 offset:18432
v_mul_f32 v79,v39,v79
v_mfma_f32_16x16x32_bf16 v[32:35],a[92:95],v[56:59],v[32:35]
s_waitcnt lgkmcnt(3)
v_exp_f32 v78,v78
v_mfma_f32_16x16x32_bf16 v[28:31],a[96:99],v[60:63],v[28:31]
s_cmp_lt_u32 s50,4194304
ds_read_b128 v[56:59],v2 offset:19456
v_fma_f32 v76,v76,v99,v99
v_mfma_f32_16x16x32_bf16 v[32:35],a[100:103],v[60:63],v[32:35]
s_cselect_b32 s68,0xffffcfff,-1
v_exp_f32 v79,v79
v_mfma_f32_16x16x32_bf16 v[28:31],a[104:107],v[64:67],v[28:31]
s_and_b32 s68,s68,s69
ds_read_b128 v[60:63],v2 offset:20480
v_fma_f32 v77,v77,v99,v99
v_mfma_f32_16x16x32_bf16 v[32:35],a[108:111],v[64:67],v[32:35]
s_lshr_b32 s69,s51,5
v_rcp_f32 v76,v76
v_mfma_f32_16x16x32_bf16 v[28:31],a[112:115],v[68:71],v[28:31]
s_cmp_lt_u32 s51,4194304
ds_read_b128 v[64:67],v2 offset:21504
v_fma_f32 v78,v78,v99,v99
v_mfma_f32_16x16x32_bf16 v[32:35],a[116:119],v[68:71],v[32:35]
s_cselect_b32 s70,0xffffcfff,-1
v_rcp_f32 v77,v77
v_mfma_f32_16x16x32_bf16 v[28:31],a[120:123],v[72:75],v[28:31]
s_and_b32 s70,s70,s69
ds_read_b128 v[68:71],v2 offset:22528
v_fma_f32 v79,v79,v99,v99
v_mfma_f32_16x16x32_bf16 v[32:35],a[124:127],v[72:75],v[32:35]
s_waitcnt lgkmcnt(3)
v_rcp_f32 v78,v78
v_mfma_f32_16x16x32_bf16 v[28:31],a[128:131],v[44:47],v[28:31]
s_waitcnt vmcnt(10)
ds_read_b128 v[72:75],v2 offset:23552
v_mul_f32 v36,v36,v76
v_mfma_f32_16x16x32_bf16 v[32:35],a[132:135],v[44:47],v[32:35]
s_barrier
v_mfma_f32_16x16x32_bf16 v[28:31],a[136:139],v[48:51],v[28:31]
s_mov_b32 s62,s87
ds_read_b128 v[44:47],v2 offset:24576
ds_read_b32 v16,v7 offset:0
v_mfma_f32_16x16x32_bf16 v[32:35],a[140:143],v[48:51],v[32:35]
v_rcp_f32 v79,v79
v_mfma_f32_16x16x32_bf16 v[28:31],a[144:147],v[52:55],v[28:31]
ds_read_b128 v[48:51],v2 offset:25600
v_mul_f32 v37,v37,v77
v_mfma_f32_16x16x32_bf16 v[32:35],a[148:151],v[52:55],v[32:35]
v_mul_f32 v38,v38,v78
v_mul_f32 v39,v39,v79
v_mfma_f32_16x16x32_bf16 v[28:31],a[152:155],v[56:59],v[28:31]
s_waitcnt lgkmcnt(1)
ds_read_b128 v[52:55],v2 offset:26624
v_cvt_pk_bf16_f32 v84,v36,v37
v_mfma_f32_16x16x32_bf16 v[32:35],a[156:159],v[56:59],v[32:35]
v_cmp_eq_u32 vcc,s60,v16
v_cvt_pk_bf16_f32 v85,v38,v39
v_fma_f32 v40,-v96,v24,v40
v_fma_f32 v41,-v96,v25,v41
v_mfma_f32_16x16x32_bf16 v[28:31],a[160:163],v[60:63],v[28:31]
s_cmp_eq_u32 vcc_lo,-1
ds_read_b128 v[56:59],v2 offset:27648
v_fma_f32 v42,-v96,v26,v42
v_mfma_f32_16x16x32_bf16 v[32:35],a[164:167],v[60:63],v[32:35]
s_cselect_b32 s67,s86,s59
s_cmp_ge_u32 s67,s59
s_cselect_b32 s68,s68,s70
s_add_u32 m0,s78,256
v_mfma_f32_16x16x32_bf16 v[28:31],a[168:171],v[64:67],v[28:31]
buffer_load_dword v6,s[36:39],s68 offen offset:0 sc1 lds
v_mfma_f32_16x16x32_bf16 v[32:35],a[172:175],v[64:67],v[32:35]
s_mov_b32 m0,s62
ds_read_b128 v[60:63],v2 offset:28672
v_fma_f32 v43,-v96,v27,v43
v_mfma_f32_16x16x32_bf16 v[28:31],a[176:179],v[68:71],v[28:31]
buffer_load_dwordx4 v0,s[36:39],s67 offen offset:0 sc1 lds
v_mfma_f32_16x16x32_bf16 v[32:35],a[180:183],v[68:71],v[32:35]
s_xor_b32 s63,s63,65536
ds_read_b128 v[64:67],v2 offset:29696
v_mul_f32 v80,v40,v40
v_mfma_f32_16x16x32_bf16 v[28:31],a[184:187],v[72:75],v[28:31]
s_add_u32 s53,s52,s55
ds_read_b128 v[68:71],v2 offset:30720
v_mul_f32 v81,v41,v41
v_mfma_f32_16x16x32_bf16 v[32:35],a[188:191],v[72:75],v[32:35]
s_waitcnt lgkmcnt(3)
v_mul_f32 v82,v42,v42
v_mul_f32 v83,v43,v43
v_mfma_f32_16x16x32_bf16 v[28:31],a[192:195],v[44:47],v[28:31]
s_waitcnt vmcnt(7)
ds_read_b128 v[72:75],v2 offset:31744
v_fma_f32 v80,v98,v80,v97
v_mfma_f32_16x16x32_bf16 v[32:35],a[196:199],v[44:47],v[32:35]
s_barrier
v_mfma_f32_16x16x32_bf16 v[28:31],a[200:203],v[48:51],v[28:31]
buffer_load_dwordx4 v0,s[36:39],s67 offen offset:1024 sc1 lds
v_mfma_f32_16x16x32_bf16 v[32:35],a[204:207],v[48:51],v[32:35]
s_cmp_eq_u32 s53,s56
ds_read_b128 v[44:47],v2 offset:32768
ds_read_b128 v[92:95],v8 offset:0
v_mfma_f32_16x16x32_bf16 v[28:31],a[208:211],v[52:55],v[28:31]
s_cselect_b32 s53,s57,s53
ds_read_b128 v[48:51],v2 offset:33792
v_mul_f32 v80,v40,v80
v_mfma_f32_16x16x32_bf16 v[32:35],a[212:215],v[52:55],v[32:35]
s_cmp_eq_u32 s53,s58
v_fma_f32 v81,v98,v81,v97
v_fma_f32 v82,v98,v82,v97
v_fma_f32 v83,v98,v83,v97
v_mul_f32 v81,v41,v81
v_mul_f32 v82,v42,v82
v_mul_f32 v83,v43,v83
v_exp_f32 v80,v80
v_exp_f32 v81,v81
v_exp_f32 v82,v82
v_exp_f32 v83,v83
v_mfma_f32_16x16x32_bf16 v[28:31],a[216:219],v[56:59],v[28:31]
buffer_load_dwordx4 v0,s[36:39],s67 offen offset:2048 sc1 lds
v_fma_f32 v80,v80,v99,v99
v_fma_f32 v81,v81,v99,v99
v_fma_f32 v82,v82,v99,v99
v_fma_f32 v83,v83,v99,v99
v_rcp_f32 v80,v80
v_rcp_f32 v81,v81
v_rcp_f32 v82,v82
v_rcp_f32 v83,v83
v_mfma_f32_16x16x32_bf16 v[32:35],a[220:223],v[56:59],v[32:35]
s_waitcnt lgkmcnt(3)
ds_read_b128 v[52:55],v2 offset:34816
v_mul_f32 v40,v40,v80
v_mul_f32 v41,v41,v81
v_mul_f32 v42,v42,v82
v_mul_f32 v43,v43,v83
v_cvt_pk_bf16_f32 v86,v40,v41
v_cvt_pk_bf16_f32 v87,v42,v43
v_mfma_f32_16x16x32_bf16 v[28:31],a[224:227],v[60:63],v[28:31]
buffer_store_dwordx4 v[84:87],v3,s[36:39],s73 offen offset:0 sc0 sc1
ds_read_b128 v[56:59],v2 offset:35840
v_mfma_f32_16x16x32_bf16 v[32:35],a[228:231],v[60:63],v[32:35]
s_waitcnt lgkmcnt(3)
s_cselect_b32 s53,s59,s53
s_cmp_ge_u32 s66,s59
v_mfma_f32_16x16x32_bf16 v[28:31],a[232:235],v[64:67],v[28:31]
buffer_load_dwordx4 v0,s[36:39],s67 offen offset:3072 sc1 lds
v_mfma_f32_16x16x32_bf16 v[32:35],a[236:239],v[64:67],v[32:35]
ds_read_b128 v[60:63],v2 offset:36864
v_mfma_f32_16x16x32_bf16 v[28:31],a[240:243],v[68:71],v[28:31]
ds_read_b128 v[64:67],v2 offset:37888
v_mfma_f32_16x16x32_bf16 v[32:35],a[244:247],v[68:71],v[32:35]
v_mfma_f32_16x16x32_bf16 v[28:31],a[248:251],v[72:75],v[28:31]
s_waitcnt lgkmcnt(2)
ds_read_b128 v[68:71],v2 offset:38912
v_mfma_f32_16x16x32_bf16 v[32:35],a[252:255],v[72:75],v[32:35]
s_cbranch_scc1 Lg1_slow1_%=
Lg1_d1w0_%=:
v_mfma_f32_16x16x32_bf16 v[36:39],a[0:3],v[44:47],0
s_add_u32 m0,s62,16384
ds_read_b128 v[72:75],v2 offset:39936
s_lshr_b32 s73,s48,0
v_mfma_f32_16x16x32_bf16 v[40:43],a[4:7],v[44:47],0
buffer_load_dwordx4 v1,s[36:39],s67 offen offset:0 sc1 lds
v_mfma_f32_16x16x32_bf16 v[36:39],a[8:11],v[48:51],v[36:39]
s_lshr_b32 s72,s67,6
ds_read_b128 v[44:47],v2 offset:40960
s_lshr_b32 s71,s62,2
v_mfma_f32_16x16x32_bf16 v[40:43],a[12:15],v[48:51],v[40:43]
s_cmp_lt_u32 s67,4194304
v_fma_f32 v28,-v92,v20,v28
v_fma_f32 v29,-v92,v21,v29
v_mfma_f32_16x16x32_bf16 v[36:39],a[16:19],v[52:55],v[36:39]
s_cselect_b32 s72,s59,s72
ds_read_b128 v[48:51],v2 offset:41984
v_fma_f32 v30,-v92,v22,v30
v_mfma_f32_16x16x32_bf16 v[40:43],a[20:23],v[52:55],v[40:43]
buffer_load_dwordx4 v1,s[36:39],s67 offen offset:1024 sc1 lds
v_mfma_f32_16x16x32_bf16 v[36:39],a[24:27],v[56:59],v[36:39]
s_mov_b64 s[44:45],s[46:47])"
R"(
ds_read_b128 v[52:55],v2 offset:43008
v_fma_f32 v31,-v92,v23,v31
v_mfma_f32_16x16x32_bf16 v[40:43],a[28:31],v[56:59],v[40:43]
s_waitcnt lgkmcnt(3)
v_mul_f32 v76,v28,v28
v_mul_f32 v77,v29,v29
v_mfma_f32_16x16x32_bf16 v[36:39],a[32:35],v[60:63],v[36:39]
s_mov_b64 s[46:47],s[48:49]
ds_read_b128 v[56:59],v2 offset:44032
v_mul_f32 v78,v30,v30
v_mfma_f32_16x16x32_bf16 v[40:43],a[36:39],v[60:63],v[40:43]
buffer_load_dwordx4 v1,s[36:39],s67 offen offset:2048 sc1 lds
v_mfma_f32_16x16x32_bf16 v[36:39],a[40:43],v[64:67],v[36:39]
s_mov_b64 s[48:49],s[50:51]
ds_read_b128 v[60:63],v2 offset:45056
v_mul_f32 v79,v31,v31
v_mfma_f32_16x16x32_bf16 v[40:43],a[44:47],v[64:67],v[40:43]
v_fma_f32 v76,v94,v76,v93
v_mul_f32 v76,v28,v76
v_mfma_f32_16x16x32_bf16 v[36:39],a[48:51],v[68:71],v[36:39]
ds_read_b128 v[64:67],v2 offset:46080
v_fma_f32 v77,v94,v77,v93
v_mfma_f32_16x16x32_bf16 v[40:43],a[52:55],v[68:71],v[40:43]
buffer_load_dwordx4 v1,s[36:39],s67 offen offset:3072 sc1 lds
v_mfma_f32_16x16x32_bf16 v[36:39],a[56:59],v[72:75],v[36:39]
s_add_u32 m0,s71,s88
ds_read_b128 v[68:71],v2 offset:47104
v_mul_f32 v77,v29,v77
v_mfma_f32_16x16x32_bf16 v[40:43],a[60:63],v[72:75],v[40:43]
s_waitcnt lgkmcnt(3)
buffer_load_dword v10,s[36:39],s72 offen offset:0 sc1 lds
v_mfma_f32_16x16x32_bf16 v[36:39],a[64:67],v[44:47],v[36:39]
s_waitcnt vmcnt(11)
ds_read_b128 v[72:75],v2 offset:48128
v_fma_f32 v78,v94,v78,v93
v_mfma_f32_16x16x32_bf16 v[40:43],a[68:71],v[44:47],v[40:43]
s_barrier
v_mfma_f32_16x16x32_bf16 v[36:39],a[72:75],v[48:51],v[36:39]
s_xor_b32 s87,s63,65536
ds_read_b128 v[44:47],v2 offset:49152
v_fma_f32 v79,v94,v79,v93
v_mfma_f32_16x16x32_bf16 v[40:43],a[76:79],v[48:51],v[40:43]
s_cmp_ge_u32 s67,s59
v_exp_f32 v76,v76
v_mfma_f32_16x16x32_bf16 v[36:39],a[80:83],v[52:55],v[36:39]
s_cselect_b32 s86,s50,s51
ds_read_b128 v[48:51],v2 offset:50176
v_mul_f32 v78,v30,v78
v_mfma_f32_16x16x32_bf16 v[40:43],a[84:87],v[52:55],v[40:43]
s_cselect_b32 s87,s87,s64
v_exp_f32 v77,v77
v_mfma_f32_16x16x32_bf16 v[36:39],a[88:91],v[56:59],v[36:39]
s_lshr_b32 s69,s51,5
ds_read_b128 v[52:55],v2 offset:51200
v_mul_f32 v79,v31,v79
v_mfma_f32_16x16x32_bf16 v[40:43],a[92:95],v[56:59],v[40:43]
s_waitcnt lgkmcnt(3)
v_exp_f32 v78,v78
v_mfma_f32_16x16x32_bf16 v[36:39],a[96:99],v[60:63],v[36:39]
s_cmp_lt_u32 s51,4194304
ds_read_b128 v[56:59],v2 offset:52224
v_fma_f32 v76,v76,v95,v95
v_mfma_f32_16x16x32_bf16 v[40:43],a[100:103],v[60:63],v[40:43]
s_cselect_b32 s68,0xffffcfff,-1
v_exp_f32 v79,v79
v_mfma_f32_16x16x32_bf16 v[36:39],a[104:107],v[64:67],v[36:39]
s_and_b32 s68,s68,s69
ds_read_b128 v[60:63],v2 offset:53248
v_fma_f32 v77,v77,v95,v95
v_mfma_f32_16x16x32_bf16 v[40:43],a[108:111],v[64:67],v[40:43]
s_lshr_b32 s69,s52,5
v_rcp_f32 v76,v76
v_mfma_f32_16x16x32_bf16 v[36:39],a[112:115],v[68:71],v[36:39]
s_cmp_lt_u32 s52,4194304
ds_read_b128 v[64:67],v2 offset:54272
v_fma_f32 v78,v78,v95,v95
v_mfma_f32_16x16x32_bf16 v[40:43],a[116:119],v[68:71],v[40:43]
s_cselect_b32 s70,0xffffcfff,-1
v_rcp_f32 v77,v77
v_mfma_f32_16x16x32_bf16 v[36:39],a[120:123],v[72:75],v[36:39]
s_and_b32 s70,s70,s69
ds_read_b128 v[68:71],v2 offset:55296
v_fma_f32 v79,v79,v95,v95
v_mfma_f32_16x16x32_bf16 v[40:43],a[124:127],v[72:75],v[40:43]
s_waitcnt lgkmcnt(3)
v_rcp_f32 v78,v78
v_mfma_f32_16x16x32_bf16 v[36:39],a[128:131],v[44:47],v[36:39]
s_waitcnt vmcnt(10)
ds_read_b128 v[72:75],v2 offset:56320
v_mul_f32 v28,v28,v76
v_mfma_f32_16x16x32_bf16 v[40:43],a[132:135],v[44:47],v[40:43]
s_barrier
v_mfma_f32_16x16x32_bf16 v[36:39],a[136:139],v[48:51],v[36:39]
s_mov_b32 s62,s87
ds_read_b128 v[44:47],v2 offset:57344
ds_read_b32 v16,v7 offset:256
v_mfma_f32_16x16x32_bf16 v[40:43],a[140:143],v[48:51],v[40:43]
s_mov_b64 s[50:51],s[52:53]
v_rcp_f32 v79,v79
v_mfma_f32_16x16x32_bf16 v[36:39],a[144:147],v[52:55],v[36:39]
ds_read_b128 v[48:51],v2 offset:58368
v_mul_f32 v29,v29,v77
v_mfma_f32_16x16x32_bf16 v[40:43],a[148:151],v[52:55],v[40:43]
v_mul_f32 v30,v30,v78
v_mul_f32 v31,v31,v79
v_mfma_f32_16x16x32_bf16 v[36:39],a[152:155],v[56:59],v[36:39]
s_waitcnt lgkmcnt(1)
ds_read_b128 v[52:55],v2 offset:59392
v_cvt_pk_bf16_f32 v84,v28,v29
v_mfma_f32_16x16x32_bf16 v[40:43],a[156:159],v[56:59],v[40:43]
v_cmp_eq_u32 vcc,s60,v16
v_cvt_pk_bf16_f32 v85,v30,v31
v_fma_f32 v32,-v92,v24,v32
v_fma_f32 v33,-v92,v25,v33
v_mfma_f32_16x16x32_bf16 v[36:39],a[160:163],v[60:63],v[36:39]
s_cmp_eq_u32 vcc_lo,-1
ds_read_b128 v[56:59],v2 offset:60416
v_fma_f32 v34,-v92,v26,v34
v_mfma_f32_16x16x32_bf16 v[40:43],a[164:167],v[60:63],v[40:43]
s_cselect_b32 s66,s86,s59
s_cmp_ge_u32 s66,s59
s_cselect_b32 s68,s68,s70
s_add_u32 m0,s78,0
v_mfma_f32_16x16x32_bf16 v[36:39],a[168:171],v[64:67],v[36:39]
buffer_load_dword v6,s[36:39],s68 offen offset:0 sc1 lds
v_mfma_f32_16x16x32_bf16 v[40:43],a[172:175],v[64:67],v[40:43]
s_mov_b32 m0,s62
ds_read_b128 v[60:63],v2 offset:61440
v_fma_f32 v35,-v92,v27,v35
v_mfma_f32_16x16x32_bf16 v[36:39],a[176:179],v[68:71],v[36:39]
buffer_load_dwordx4 v0,s[36:39],s66 offen offset:0 sc1 lds
v_mfma_f32_16x16x32_bf16 v[40:43],a[180:183],v[68:71],v[40:43]
s_xor_b32 s64,s64,65536
ds_read_b128 v[64:67],v2 offset:62464
v_mul_f32 v80,v32,v32
v_mfma_f32_16x16x32_bf16 v[36:39],a[184:187],v[72:75],v[36:39]
s_add_u32 s74,s53,s55
ds_read_b128 v[68:71],v2 offset:63488
v_mul_f32 v81,v33,v33
v_mfma_f32_16x16x32_bf16 v[40:43],a[188:191],v[72:75],v[40:43]
s_waitcnt lgkmcnt(3)
v_mul_f32 v82,v34,v34
v_mul_f32 v83,v35,v35
v_mfma_f32_16x16x32_bf16 v[36:39],a[192:195],v[44:47],v[36:39]
s_waitcnt vmcnt(7)
ds_read_b128 v[72:75],v2 offset:64512
v_xor_b32 v2,65536,v2
v_mfma_f32_16x16x32_bf16 v[40:43],a[196:199],v[44:47],v[40:43]
s_barrier
v_mfma_f32_16x16x32_bf16 v[36:39],a[200:203],v[48:51],v[36:39]
buffer_load_dwordx4 v0,s[36:39],s66 offen offset:1024 sc1 lds
v_mfma_f32_16x16x32_bf16 v[40:43],a[204:207],v[48:51],v[40:43]
s_cmp_eq_u32 s74,s56
ds_read_b128 v[44:47],v2 offset:0
ds_read_b128 v[96:99],v8 offset:8192
v_mfma_f32_16x16x32_bf16 v[36:39],a[208:211],v[52:55],v[36:39]
s_cselect_b32 s74,s57,s74
ds_read_b128 v[48:51],v2 offset:1024
v_fma_f32 v80,v94,v80,v93
v_mfma_f32_16x16x32_bf16 v[40:43],a[212:215],v[52:55],v[40:43]
s_cmp_eq_u32 s74,s58
v_fma_f32 v81,v94,v81,v93
v_fma_f32 v82,v94,v82,v93
v_fma_f32 v83,v94,v83,v93
v_mul_f32 v80,v32,v80
v_mul_f32 v81,v33,v81
v_mul_f32 v82,v34,v82
v_mul_f32 v83,v35,v83
v_exp_f32 v80,v80
v_exp_f32 v81,v81
v_exp_f32 v82,v82
v_exp_f32 v83,v83
v_mfma_f32_16x16x32_bf16 v[36:39],a[216:219],v[56:59],v[36:39]
buffer_load_dwordx4 v0,s[36:39],s66 offen offset:2048 sc1 lds
v_fma_f32 v80,v80,v95,v95
v_fma_f32 v81,v81,v95,v95
v_fma_f32 v82,v82,v95,v95
v_fma_f32 v83,v83,v95,v95
v_rcp_f32 v80,v80
v_rcp_f32 v81,v81
v_rcp_f32 v82,v82
v_rcp_f32 v83,v83
v_mfma_f32_16x16x32_bf16 v[40:43],a[220:223],v[56:59],v[40:43]
s_waitcnt lgkmcnt(3)
ds_read_b128 v[52:55],v2 offset:2048
v_mul_f32 v32,v32,v80
v_mul_f32 v33,v33,v81
v_mul_f32 v34,v34,v82
v_mul_f32 v35,v35,v83
v_cvt_pk_bf16_f32 v86,v32,v33
v_cvt_pk_bf16_f32 v87,v34,v35
v_mfma_f32_16x16x32_bf16 v[36:39],a[224:227],v[60:63],v[36:39]
buffer_store_dwordx4 v[84:87],v3,s[36:39],s73 offen offset:0 sc0 sc1
ds_read_b128 v[56:59],v2 offset:3072
v_xor_b32 v8,16384,v8
v_mfma_f32_16x16x32_bf16 v[40:43],a[228:231],v[60:63],v[40:43]
s_waitcnt lgkmcnt(3)
s_cselect_b32 s74,s59,s74
s_mov_b32 s52,s74
v_mfma_f32_16x16x32_bf16 v[36:39],a[232:235],v[64:67],v[36:39])"
R"(
buffer_load_dwordx4 v0,s[36:39],s66 offen offset:3072 sc1 lds
v_mfma_f32_16x16x32_bf16 v[40:43],a[236:239],v[64:67],v[40:43]
s_cmp_lt_u32 s67,s59
ds_read_b128 v[60:63],v2 offset:4096
v_mfma_f32_16x16x32_bf16 v[36:39],a[240:243],v[68:71],v[36:39]
ds_read_b128 v[64:67],v2 offset:5120
v_mfma_f32_16x16x32_bf16 v[40:43],a[244:247],v[68:71],v[40:43]
v_mfma_f32_16x16x32_bf16 v[36:39],a[248:251],v[72:75],v[36:39]
s_waitcnt lgkmcnt(2)
ds_read_b128 v[68:71],v2 offset:6144
v_mfma_f32_16x16x32_bf16 v[40:43],a[252:255],v[72:75],v[40:43]
s_cbranch_scc1 Lg1_d0w0_%=
s_branch Lg1_slow0_%=
Lg1_d0w1_%=:
v_mfma_f32_16x16x32_bf16 v[28:31],a[32:35],v[44:47],0
s_add_u32 m0,s62,16384
ds_read_b128 v[72:75],v2 offset:3072
s_lshr_b32 s73,s47,0
v_mfma_f32_16x16x32_bf16 v[32:35],a[36:39],v[44:47],0
s_lshr_b32 s72,s66,6
s_lshr_b32 s71,s62,2
s_cmp_lt_u32 s66,4194304
v_mfma_f32_16x16x32_bf16 v[28:31],a[40:43],v[48:51],v[28:31]
buffer_load_dwordx4 v1,s[36:39],s66 offen offset:0 sc1 lds
v_mfma_f32_16x16x32_bf16 v[32:35],a[44:47],v[48:51],v[32:35]
s_cselect_b32 s72,s59,s72
ds_read_b128 v[44:47],v2 offset:12288
v_fma_f32 v36,-v96,v20,v36
v_mfma_f32_16x16x32_bf16 v[28:31],a[48:51],v[52:55],v[28:31]
ds_read_b128 v[48:51],v2 offset:13312
v_fma_f32 v37,-v96,v21,v37
v_mfma_f32_16x16x32_bf16 v[32:35],a[52:55],v[52:55],v[32:35]
v_fma_f32 v38,-v96,v22,v38
v_fma_f32 v39,-v96,v23,v39
v_mfma_f32_16x16x32_bf16 v[28:31],a[56:59],v[56:59],v[28:31]
buffer_load_dwordx4 v1,s[36:39],s66 offen offset:1024 sc1 lds
v_mfma_f32_16x16x32_bf16 v[32:35],a[60:63],v[56:59],v[32:35]
s_waitcnt lgkmcnt(2)
ds_read_b128 v[52:55],v2 offset:14336
v_mul_f32 v76,v36,v36
v_mfma_f32_16x16x32_bf16 v[28:31],a[0:3],v[60:63],v[28:31]
ds_read_b128 v[56:59],v2 offset:15360
v_mul_f32 v77,v37,v37
v_mfma_f32_16x16x32_bf16 v[32:35],a[4:7],v[60:63],v[32:35]
v_mul_f32 v78,v38,v38
v_mul_f32 v79,v39,v39
v_mfma_f32_16x16x32_bf16 v[28:31],a[8:11],v[64:67],v[28:31]
buffer_load_dwordx4 v1,s[36:39],s66 offen offset:2048 sc1 lds
v_mfma_f32_16x16x32_bf16 v[32:35],a[12:15],v[64:67],v[32:35]
ds_read_b128 v[60:63],v2 offset:8192
v_fma_f32 v76,v98,v76,v97
v_mfma_f32_16x16x32_bf16 v[28:31],a[16:19],v[68:71],v[28:31]
ds_read_b128 v[64:67],v2 offset:9216
v_mul_f32 v76,v36,v76
v_mfma_f32_16x16x32_bf16 v[32:35],a[20:23],v[68:71],v[32:35]
v_exp_f32 v76,v76
v_mfma_f32_16x16x32_bf16 v[28:31],a[24:27],v[72:75],v[28:31]
buffer_load_dwordx4 v1,s[36:39],s66 offen offset:3072 sc1 lds
v_mfma_f32_16x16x32_bf16 v[32:35],a[28:31],v[72:75],v[32:35]
s_waitcnt lgkmcnt(2)
ds_read_b128 v[68:71],v2 offset:10240
s_add_u32 m0,s71,s88
v_mfma_f32_16x16x32_bf16 v[28:31],a[96:99],v[44:47],v[28:31]
s_xor_b32 s87,s64,65536
ds_read_b128 v[72:75],v2 offset:11264
buffer_load_dword v10,s[36:39],s72 offen offset:0 sc1 lds
s_waitcnt vmcnt(10)
v_mfma_f32_16x16x32_bf16 v[32:35],a[100:103],v[44:47],v[32:35]
s_barrier
v_mfma_f32_16x16x32_bf16 v[28:31],a[104:107],v[48:51],v[28:31]
s_cmp_ge_u32 s66,s59
ds_read_b128 v[44:47],v2 offset:20480
v_fma_f32 v76,v76,v99,v99
v_mfma_f32_16x16x32_bf16 v[32:35],a[108:111],v[48:51],v[32:35]
s_cselect_b32 s86,s49,s50
v_rcp_f32 v76,v76
v_mfma_f32_16x16x32_bf16 v[28:31],a[112:115],v[52:55],v[28:31]
s_cselect_b32 s87,s87,s63
ds_read_b128 v[48:51],v2 offset:21504
v_fma_f32 v77,v98,v77,v97
v_mfma_f32_16x16x32_bf16 v[32:35],a[116:119],v[52:55],v[32:35]
s_mov_b32 s62,s87
v_mul_f32 v77,v37,v77
v_fma_f32 v78,v98,v78,v97
v_mfma_f32_16x16x32_bf16 v[28:31],a[120:123],v[56:59],v[28:31]
s_mov_b32 m0,s62
ds_read_b128 v[52:55],v2 offset:22528
v_mul_f32 v78,v38,v78
v_mfma_f32_16x16x32_bf16 v[32:35],a[124:127],v[56:59],v[32:35]
s_waitcnt lgkmcnt(3)
v_exp_f32 v77,v77
v_mfma_f32_16x16x32_bf16 v[28:31],a[64:67],v[60:63],v[28:31]
ds_read_b128 v[56:59],v2 offset:23552
v_fma_f32 v77,v77,v99,v99
v_mfma_f32_16x16x32_bf16 v[32:35],a[68:71],v[60:63],v[32:35]
v_exp_f32 v78,v78
v_mfma_f32_16x16x32_bf16 v[28:31],a[72:75],v[64:67],v[28:31]
ds_read_b128 v[60:63],v2 offset:16384
v_fma_f32 v78,v78,v99,v99
v_mfma_f32_16x16x32_bf16 v[32:35],a[76:79],v[64:67],v[32:35]
v_rcp_f32 v77,v77
v_mfma_f32_16x16x32_bf16 v[28:31],a[80:83],v[68:71],v[28:31]
ds_read_b128 v[64:67],v2 offset:17408
v_fma_f32 v79,v98,v79,v97
v_mfma_f32_16x16x32_bf16 v[32:35],a[84:87],v[68:71],v[32:35]
v_rcp_f32 v78,v78
v_mfma_f32_16x16x32_bf16 v[28:31],a[88:91],v[72:75],v[28:31]
ds_read_b128 v[68:71],v2 offset:18432
v_mul_f32 v79,v39,v79
v_mfma_f32_16x16x32_bf16 v[32:35],a[92:95],v[72:75],v[32:35]
s_waitcnt lgkmcnt(3)
v_exp_f32 v79,v79
v_mfma_f32_16x16x32_bf16 v[28:31],a[160:163],v[44:47],v[28:31]
s_nop 0
ds_read_b128 v[72:75],v2 offset:19456
v_fma_f32 v79,v79,v99,v99
v_mfma_f32_16x16x32_bf16 v[32:35],a[164:167],v[44:47],v[32:35]
s_barrier
v_mfma_f32_16x16x32_bf16 v[28:31],a[168:171],v[48:51],v[28:31]
ds_read_b128 v[44:47],v2 offset:28672
ds_read_b32 v16,v7 offset:0
v_mfma_f32_16x16x32_bf16 v[32:35],a[172:175],v[48:51],v[32:35]
v_rcp_f32 v79,v79
v_mfma_f32_16x16x32_bf16 v[28:31],a[176:179],v[52:55],v[28:31]
ds_read_b128 v[48:51],v2 offset:29696
v_mul_f32 v36,v36,v76
v_mfma_f32_16x16x32_bf16 v[32:35],a[180:183],v[52:55],v[32:35]
v_mul_f32 v37,v37,v77
v_mul_f32 v38,v38,v78
v_mfma_f32_16x16x32_bf16 v[28:31],a[184:187],v[56:59],v[28:31]
s_waitcnt lgkmcnt(1)
ds_read_b128 v[52:55],v2 offset:30720
v_mul_f32 v39,v39,v79
v_mfma_f32_16x16x32_bf16 v[32:35],a[188:191],v[56:59],v[32:35]
v_cmp_eq_u32 vcc,s60,v16
v_cvt_pk_bf16_f32 v84,v36,v37
v_cvt_pk_bf16_f32 v85,v38,v39
v_fma_f32 v40,-v96,v24,v40
v_mfma_f32_16x16x32_bf16 v[28:31],a[128:131],v[60:63],v[28:31]
s_cmp_eq_u32 vcc_lo,-1
ds_read_b128 v[56:59],v2 offset:31744
s_cselect_b32 s67,s86,s59
v_mfma_f32_16x16x32_bf16 v[32:35],a[132:135],v[60:63],v[32:35]
buffer_load_dwordx4 v0,s[36:39],s67 offen offset:0 sc1 lds
v_mfma_f32_16x16x32_bf16 v[28:31],a[136:139],v[64:67],v[28:31]
s_xor_b32 s63,s63,65536
ds_read_b128 v[60:63],v2 offset:24576
v_fma_f32 v41,-v96,v25,v41
v_mfma_f32_16x16x32_bf16 v[32:35],a[140:143],v[64:67],v[32:35]
s_add_u32 s53,s52,s55
v_fma_f32 v42,-v96,v26,v42
v_fma_f32 v43,-v96,v27,v43
v_mfma_f32_16x16x32_bf16 v[28:31],a[144:147],v[68:71],v[28:31]
s_cmp_eq_u32 s53,s56
ds_read_b128 v[64:67],v2 offset:25600
v_mul_f32 v80,v40,v40
v_mfma_f32_16x16x32_bf16 v[32:35],a[148:151],v[68:71],v[32:35]
buffer_load_dwordx4 v0,s[36:39],s67 offen offset:1024 sc1 lds
v_mfma_f32_16x16x32_bf16 v[28:31],a[152:155],v[72:75],v[28:31]
s_cselect_b32 s53,s57,s53
ds_read_b128 v[68:71],v2 offset:26624
v_mul_f32 v81,v41,v41
v_mfma_f32_16x16x32_bf16 v[32:35],a[156:159],v[72:75],v[32:35]
s_waitcnt lgkmcnt(3)
v_mul_f32 v82,v42,v42
v_mul_f32 v83,v43,v43
v_mfma_f32_16x16x32_bf16 v[28:31],a[224:227],v[44:47],v[28:31]
s_waitcnt vmcnt(7)
ds_read_b128 v[72:75],v2 offset:27648
v_fma_f32 v80,v98,v80,v97
v_mfma_f32_16x16x32_bf16 v[32:35],a[228:231],v[44:47],v[32:35]
s_barrier
v_mfma_f32_16x16x32_bf16 v[28:31],a[232:235],v[48:51],v[28:31]
s_cmp_eq_u32 s53,s58
ds_read_b128 v[44:47],v2 offset:36864
ds_read_b128 v[92:95],v8 offset:0
v_mfma_f32_16x16x32_bf16 v[32:35],a[236:239],v[48:51],v[32:35]
buffer_load_dwordx4 v0,s[36:39],s67 offen offset:2048 sc1 lds
v_mfma_f32_16x16x32_bf16 v[28:31],a[240:243],v[52:55],v[28:31]
s_cselect_b32 s53,s59,s53
ds_read_b128 v[48:51],v2 offset:37888
v_mul_f32 v80,v40,v80
v_mfma_f32_16x16x32_bf16 v[32:35],a[244:247],v[52:55],v[32:35]
s_cmp_ge_u32 s66,s59
v_exp_f32 v80,v80
v_mfma_f32_16x16x32_bf16 v[28:31],a[248:251],v[56:59],v[28:31]
v_fma_f32 v81,v98,v81,v97
v_fma_f32 v82,v98,v82,v97
v_fma_f32 v83,v98,v83,v97
v_mul_f32 v81,v41,v81
v_mul_f32 v82,v42,v82
v_mul_f32 v83,v43,v83
v_exp_f32 v81,v81
v_exp_f32 v82,v82
v_exp_f32 v83,v83
v_mfma_f32_16x16x32_bf16 v[32:35],a[252:255],v[56:59],v[32:35]
s_waitcnt lgkmcnt(3)
ds_read_b128 v[52:55],v2 offset:38912
buffer_load_dwordx4 v0,s[36:39],s67 offen offset:3072 sc1 lds
v_fma_f32 v80,v80,v99,v99)"
R"(
v_fma_f32 v81,v81,v99,v99
v_fma_f32 v82,v82,v99,v99
v_fma_f32 v83,v83,v99,v99
v_rcp_f32 v80,v80
v_rcp_f32 v81,v81
v_rcp_f32 v82,v82
v_rcp_f32 v83,v83
v_mfma_f32_16x16x32_bf16 v[28:31],a[192:195],v[60:63],v[28:31]
s_waitcnt lgkmcnt(2)
v_mul_f32 v40,v40,v80
v_mul_f32 v41,v41,v81
v_mul_f32 v42,v42,v82
v_mul_f32 v43,v43,v83
v_cvt_pk_bf16_f32 v86,v40,v41
v_cvt_pk_bf16_f32 v87,v42,v43
v_mfma_f32_16x16x32_bf16 v[32:35],a[196:199],v[60:63],v[32:35]
ds_read_b128 v[56:59],v2 offset:39936
buffer_store_dwordx4 v[84:87],v3,s[36:39],s73 offen offset:0 sc0 sc1
v_mfma_f32_16x16x32_bf16 v[28:31],a[200:203],v[64:67],v[28:31]
ds_read_b128 v[60:63],v2 offset:32768
v_mfma_f32_16x16x32_bf16 v[32:35],a[204:207],v[64:67],v[32:35]
v_mfma_f32_16x16x32_bf16 v[28:31],a[208:211],v[68:71],v[28:31]
ds_read_b128 v[64:67],v2 offset:33792
v_mfma_f32_16x16x32_bf16 v[32:35],a[212:215],v[68:71],v[32:35]
v_mfma_f32_16x16x32_bf16 v[28:31],a[216:219],v[72:75],v[28:31]
s_waitcnt lgkmcnt(2)
ds_read_b128 v[68:71],v2 offset:34816
v_mfma_f32_16x16x32_bf16 v[32:35],a[220:223],v[72:75],v[32:35]
s_cbranch_scc1 Lg1_slow1_%=
Lg1_d1w1_%=:
v_mfma_f32_16x16x32_bf16 v[36:39],a[32:35],v[44:47],0
s_add_u32 m0,s62,16384
ds_read_b128 v[72:75],v2 offset:35840
s_lshr_b32 s73,s48,0
v_mfma_f32_16x16x32_bf16 v[40:43],a[36:39],v[44:47],0
s_lshr_b32 s72,s67,6
s_lshr_b32 s71,s62,2
s_cmp_lt_u32 s67,4194304
v_mfma_f32_16x16x32_bf16 v[36:39],a[40:43],v[48:51],v[36:39]
buffer_load_dwordx4 v1,s[36:39],s67 offen offset:0 sc1 lds
v_mfma_f32_16x16x32_bf16 v[40:43],a[44:47],v[48:51],v[40:43]
s_cselect_b32 s72,s59,s72
ds_read_b128 v[44:47],v2 offset:45056
v_fma_f32 v28,-v92,v20,v28
v_mfma_f32_16x16x32_bf16 v[36:39],a[48:51],v[52:55],v[36:39]
s_mov_b64 s[44:45],s[46:47]
ds_read_b128 v[48:51],v2 offset:46080
v_fma_f32 v29,-v92,v21,v29
v_mfma_f32_16x16x32_bf16 v[40:43],a[52:55],v[52:55],v[40:43]
s_mov_b64 s[46:47],s[48:49]
v_fma_f32 v30,-v92,v22,v30
v_fma_f32 v31,-v92,v23,v31
v_mfma_f32_16x16x32_bf16 v[36:39],a[56:59],v[56:59],v[36:39]
buffer_load_dwordx4 v1,s[36:39],s67 offen offset:1024 sc1 lds
v_mfma_f32_16x16x32_bf16 v[40:43],a[60:63],v[56:59],v[40:43]
s_waitcnt lgkmcnt(2)
ds_read_b128 v[52:55],v2 offset:47104
v_mul_f32 v76,v28,v28
v_mfma_f32_16x16x32_bf16 v[36:39],a[0:3],v[60:63],v[36:39]
s_mov_b64 s[48:49],s[50:51]
ds_read_b128 v[56:59],v2 offset:48128
v_mul_f32 v77,v29,v29
v_mfma_f32_16x16x32_bf16 v[40:43],a[4:7],v[60:63],v[40:43]
v_mul_f32 v78,v30,v30
v_mul_f32 v79,v31,v31
v_mfma_f32_16x16x32_bf16 v[36:39],a[8:11],v[64:67],v[36:39]
buffer_load_dwordx4 v1,s[36:39],s67 offen offset:2048 sc1 lds
v_mfma_f32_16x16x32_bf16 v[40:43],a[12:15],v[64:67],v[40:43]
ds_read_b128 v[60:63],v2 offset:40960
v_fma_f32 v76,v94,v76,v93
v_mfma_f32_16x16x32_bf16 v[36:39],a[16:19],v[68:71],v[36:39]
ds_read_b128 v[64:67],v2 offset:41984
v_mul_f32 v76,v28,v76
v_mfma_f32_16x16x32_bf16 v[40:43],a[20:23],v[68:71],v[40:43]
v_exp_f32 v76,v76
v_mfma_f32_16x16x32_bf16 v[36:39],a[24:27],v[72:75],v[36:39]
buffer_load_dwordx4 v1,s[36:39],s67 offen offset:3072 sc1 lds
v_mfma_f32_16x16x32_bf16 v[40:43],a[28:31],v[72:75],v[40:43]
s_waitcnt lgkmcnt(2)
ds_read_b128 v[68:71],v2 offset:43008
s_add_u32 m0,s71,s88
v_mfma_f32_16x16x32_bf16 v[36:39],a[96:99],v[44:47],v[36:39]
s_xor_b32 s87,s63,65536
ds_read_b128 v[72:75],v2 offset:44032
buffer_load_dword v10,s[36:39],s72 offen offset:0 sc1 lds
s_waitcnt vmcnt(10)
v_mfma_f32_16x16x32_bf16 v[40:43],a[100:103],v[44:47],v[40:43]
s_barrier
v_mfma_f32_16x16x32_bf16 v[36:39],a[104:107],v[48:51],v[36:39]
s_cmp_ge_u32 s67,s59
ds_read_b128 v[44:47],v2 offset:53248
v_fma_f32 v76,v76,v95,v95
v_mfma_f32_16x16x32_bf16 v[40:43],a[108:111],v[48:51],v[40:43]
s_cselect_b32 s86,s50,s51
v_rcp_f32 v76,v76
v_mfma_f32_16x16x32_bf16 v[36:39],a[112:115],v[52:55],v[36:39]
s_cselect_b32 s87,s87,s64
ds_read_b128 v[48:51],v2 offset:54272
v_fma_f32 v77,v94,v77,v93
v_mfma_f32_16x16x32_bf16 v[40:43],a[116:119],v[52:55],v[40:43]
s_mov_b32 s62,s87
v_mul_f32 v77,v29,v77
v_fma_f32 v78,v94,v78,v93
v_mfma_f32_16x16x32_bf16 v[36:39],a[120:123],v[56:59],v[36:39]
s_mov_b32 m0,s62
ds_read_b128 v[52:55],v2 offset:55296
v_mul_f32 v78,v30,v78
v_mfma_f32_16x16x32_bf16 v[40:43],a[124:127],v[56:59],v[40:43]
s_waitcnt lgkmcnt(3)
v_exp_f32 v77,v77
v_mfma_f32_16x16x32_bf16 v[36:39],a[64:67],v[60:63],v[36:39]
s_mov_b64 s[50:51],s[52:53]
ds_read_b128 v[56:59],v2 offset:56320
v_fma_f32 v77,v77,v95,v95
v_mfma_f32_16x16x32_bf16 v[40:43],a[68:71],v[60:63],v[40:43]
v_exp_f32 v78,v78
v_mfma_f32_16x16x32_bf16 v[36:39],a[72:75],v[64:67],v[36:39]
ds_read_b128 v[60:63],v2 offset:49152
v_fma_f32 v78,v78,v95,v95
v_mfma_f32_16x16x32_bf16 v[40:43],a[76:79],v[64:67],v[40:43]
v_rcp_f32 v77,v77
v_mfma_f32_16x16x32_bf16 v[36:39],a[80:83],v[68:71],v[36:39]
ds_read_b128 v[64:67],v2 offset:50176
v_fma_f32 v79,v94,v79,v93
v_mfma_f32_16x16x32_bf16 v[40:43],a[84:87],v[68:71],v[40:43]
v_rcp_f32 v78,v78
v_mfma_f32_16x16x32_bf16 v[36:39],a[88:91],v[72:75],v[36:39]
ds_read_b128 v[68:71],v2 offset:51200
v_mul_f32 v79,v31,v79
v_mfma_f32_16x16x32_bf16 v[40:43],a[92:95],v[72:75],v[40:43]
s_waitcnt lgkmcnt(3)
v_exp_f32 v79,v79
v_mfma_f32_16x16x32_bf16 v[36:39],a[160:163],v[44:47],v[36:39]
s_nop 0
ds_read_b128 v[72:75],v2 offset:52224
v_fma_f32 v79,v79,v95,v95
v_mfma_f32_16x16x32_bf16 v[40:43],a[164:167],v[44:47],v[40:43]
s_barrier
v_mfma_f32_16x16x32_bf16 v[36:39],a[168:171],v[48:51],v[36:39]
ds_read_b128 v[44:47],v2 offset:61440
ds_read_b32 v16,v7 offset:256
v_mfma_f32_16x16x32_bf16 v[40:43],a[172:175],v[48:51],v[40:43]
v_rcp_f32 v79,v79
v_mfma_f32_16x16x32_bf16 v[36:39],a[176:179],v[52:55],v[36:39]
ds_read_b128 v[48:51],v2 offset:62464
v_mul_f32 v28,v28,v76
v_mfma_f32_16x16x32_bf16 v[40:43],a[180:183],v[52:55],v[40:43]
v_mul_f32 v29,v29,v77
v_mul_f32 v30,v30,v78
v_mfma_f32_16x16x32_bf16 v[36:39],a[184:187],v[56:59],v[36:39]
s_waitcnt lgkmcnt(1)
ds_read_b128 v[52:55],v2 offset:63488
v_mul_f32 v31,v31,v79
v_mfma_f32_16x16x32_bf16 v[40:43],a[188:191],v[56:59],v[40:43]
v_cmp_eq_u32 vcc,s60,v16
v_cvt_pk_bf16_f32 v84,v28,v29
v_cvt_pk_bf16_f32 v85,v30,v31
v_fma_f32 v32,-v92,v24,v32
v_mfma_f32_16x16x32_bf16 v[36:39],a[128:131],v[60:63],v[36:39]
s_cmp_eq_u32 vcc_lo,-1
ds_read_b128 v[56:59],v2 offset:64512
s_cselect_b32 s66,s86,s59
v_mfma_f32_16x16x32_bf16 v[40:43],a[132:135],v[60:63],v[40:43]
buffer_load_dwordx4 v0,s[36:39],s66 offen offset:0 sc1 lds
v_mfma_f32_16x16x32_bf16 v[36:39],a[136:139],v[64:67],v[36:39]
s_xor_b32 s64,s64,65536
ds_read_b128 v[60:63],v2 offset:57344
v_fma_f32 v33,-v92,v25,v33
v_mfma_f32_16x16x32_bf16 v[40:43],a[140:143],v[64:67],v[40:43]
s_add_u32 s74,s53,s55
v_fma_f32 v34,-v92,v26,v34
v_fma_f32 v35,-v92,v27,v35
v_mfma_f32_16x16x32_bf16 v[36:39],a[144:147],v[68:71],v[36:39]
s_cmp_eq_u32 s74,s56
ds_read_b128 v[64:67],v2 offset:58368
v_mul_f32 v80,v32,v32
v_mfma_f32_16x16x32_bf16 v[40:43],a[148:151],v[68:71],v[40:43]
buffer_load_dwordx4 v0,s[36:39],s66 offen offset:1024 sc1 lds
v_mfma_f32_16x16x32_bf16 v[36:39],a[152:155],v[72:75],v[36:39]
s_cselect_b32 s74,s57,s74
ds_read_b128 v[68:71],v2 offset:59392
v_mul_f32 v81,v33,v33
v_mfma_f32_16x16x32_bf16 v[40:43],a[156:159],v[72:75],v[40:43]
s_waitcnt lgkmcnt(3)
v_mul_f32 v82,v34,v34
v_mul_f32 v83,v35,v35
v_mfma_f32_16x16x32_bf16 v[36:39],a[224:227],v[44:47],v[36:39]
s_waitcnt vmcnt(7)
ds_read_b128 v[72:75],v2 offset:60416
v_xor_b32 v2,65536,v2
v_mfma_f32_16x16x32_bf16 v[40:43],a[228:231],v[44:47],v[40:43]
s_barrier
v_mfma_f32_16x16x32_bf16 v[36:39],a[232:235],v[48:51],v[36:39]
s_cmp_eq_u32 s74,s58
ds_read_b128 v[44:47],v2 offset:4096
ds_read_b128 v[96:99],v8 offset:8192
v_mfma_f32_16x16x32_bf16 v[40:43],a[236:239],v[48:51],v[40:43]
buffer_load_dwordx4 v0,s[36:39],s66 offen offset:2048 sc1 lds
v_mfma_f32_16x16x32_bf16 v[36:39],a[240:243],v[52:55],v[36:39]
s_cselect_b32 s74,s59,s74)"
R"(
ds_read_b128 v[48:51],v2 offset:5120
v_fma_f32 v80,v94,v80,v93
v_mfma_f32_16x16x32_bf16 v[40:43],a[244:247],v[52:55],v[40:43]
s_mov_b32 s52,s74
v_mul_f32 v80,v32,v80
v_fma_f32 v81,v94,v81,v93
v_mfma_f32_16x16x32_bf16 v[36:39],a[248:251],v[56:59],v[36:39]
s_cmp_lt_u32 s67,s59
v_fma_f32 v82,v94,v82,v93
v_fma_f32 v83,v94,v83,v93
v_mul_f32 v81,v33,v81
v_mul_f32 v82,v34,v82
v_mul_f32 v83,v35,v83
v_exp_f32 v80,v80
v_exp_f32 v81,v81
v_exp_f32 v82,v82
v_exp_f32 v83,v83
v_mfma_f32_16x16x32_bf16 v[40:43],a[252:255],v[56:59],v[40:43]
s_waitcnt lgkmcnt(3)
ds_read_b128 v[52:55],v2 offset:6144
buffer_load_dwordx4 v0,s[36:39],s66 offen offset:3072 sc1 lds
v_fma_f32 v80,v80,v95,v95
v_fma_f32 v81,v81,v95,v95
v_fma_f32 v82,v82,v95,v95
v_fma_f32 v83,v83,v95,v95
v_rcp_f32 v80,v80
v_rcp_f32 v81,v81
v_rcp_f32 v82,v82
v_rcp_f32 v83,v83
v_mfma_f32_16x16x32_bf16 v[36:39],a[192:195],v[60:63],v[36:39]
s_waitcnt lgkmcnt(2)
v_mul_f32 v32,v32,v80
v_mul_f32 v33,v33,v81
v_mul_f32 v34,v34,v82
v_mul_f32 v35,v35,v83
v_cvt_pk_bf16_f32 v86,v32,v33
v_cvt_pk_bf16_f32 v87,v34,v35
v_mfma_f32_16x16x32_bf16 v[40:43],a[196:199],v[60:63],v[40:43]
ds_read_b128 v[56:59],v2 offset:7168
buffer_store_dwordx4 v[84:87],v3,s[36:39],s73 offen offset:0 sc0 sc1
v_xor_b32 v8,16384,v8
v_mfma_f32_16x16x32_bf16 v[36:39],a[200:203],v[64:67],v[36:39]
ds_read_b128 v[60:63],v2 offset:0
v_mfma_f32_16x16x32_bf16 v[40:43],a[204:207],v[64:67],v[40:43]
v_mfma_f32_16x16x32_bf16 v[36:39],a[208:211],v[68:71],v[36:39]
ds_read_b128 v[64:67],v2 offset:1024
v_mfma_f32_16x16x32_bf16 v[40:43],a[212:215],v[68:71],v[40:43]
v_mfma_f32_16x16x32_bf16 v[36:39],a[216:219],v[72:75],v[36:39]
s_waitcnt lgkmcnt(2)
ds_read_b128 v[68:71],v2 offset:2048
v_mfma_f32_16x16x32_bf16 v[40:43],a[220:223],v[72:75],v[40:43]
s_cbranch_scc1 Lg1_d0w1_%=
s_branch Lg1_slow0_%=
Lg1_d0w2_%=:
v_mfma_f32_16x16x32_bf16 v[28:31],a[64:67],v[44:47],0
s_add_u32 m0,s62,16384
ds_read_b128 v[72:75],v2 offset:15360
s_lshr_b32 s73,s47,0
v_mfma_f32_16x16x32_bf16 v[32:35],a[68:71],v[44:47],0
s_lshr_b32 s72,s66,6
s_lshr_b32 s71,s62,2
s_cmp_lt_u32 s66,4194304
v_mfma_f32_16x16x32_bf16 v[28:31],a[72:75],v[48:51],v[28:31]
s_cselect_b32 s72,s59,s72
ds_read_b128 v[44:47],v2 offset:0
v_mfma_f32_16x16x32_bf16 v[32:35],a[76:79],v[48:51],v[32:35]
buffer_load_dwordx4 v1,s[36:39],s66 offen offset:0 sc1 lds
v_mfma_f32_16x16x32_bf16 v[28:31],a[80:83],v[52:55],v[28:31]
ds_read_b128 v[48:51],v2 offset:1024
v_fma_f32 v36,-v96,v20,v36
v_mfma_f32_16x16x32_bf16 v[32:35],a[84:87],v[52:55],v[32:35]
v_fma_f32 v37,-v96,v21,v37
v_fma_f32 v38,-v96,v22,v38
v_mfma_f32_16x16x32_bf16 v[28:31],a[88:91],v[56:59],v[28:31]
ds_read_b128 v[52:55],v2 offset:2048
v_fma_f32 v39,-v96,v23,v39
v_mfma_f32_16x16x32_bf16 v[32:35],a[92:95],v[56:59],v[32:35]
s_waitcnt lgkmcnt(3)
buffer_load_dwordx4 v1,s[36:39],s66 offen offset:1024 sc1 lds
v_mfma_f32_16x16x32_bf16 v[28:31],a[96:99],v[60:63],v[28:31]
ds_read_b128 v[56:59],v2 offset:3072
v_mul_f32 v76,v36,v36
v_mfma_f32_16x16x32_bf16 v[32:35],a[100:103],v[60:63],v[32:35]
v_mul_f32 v77,v37,v37
v_mul_f32 v78,v38,v38
v_mfma_f32_16x16x32_bf16 v[28:31],a[104:107],v[64:67],v[28:31]
ds_read_b128 v[60:63],v2 offset:4096
v_mul_f32 v79,v39,v39
v_mfma_f32_16x16x32_bf16 v[32:35],a[108:111],v[64:67],v[32:35]
buffer_load_dwordx4 v1,s[36:39],s66 offen offset:2048 sc1 lds
v_mfma_f32_16x16x32_bf16 v[28:31],a[112:115],v[68:71],v[28:31]
ds_read_b128 v[64:67],v2 offset:5120
v_fma_f32 v76,v98,v76,v97
v_mfma_f32_16x16x32_bf16 v[32:35],a[116:119],v[68:71],v[32:35]
v_mul_f32 v76,v36,v76
v_fma_f32 v77,v98,v77,v97
v_mfma_f32_16x16x32_bf16 v[28:31],a[120:123],v[72:75],v[28:31]
ds_read_b128 v[68:71],v2 offset:6144
v_mul_f32 v77,v37,v77
v_mfma_f32_16x16x32_bf16 v[32:35],a[124:127],v[72:75],v[32:35]
s_waitcnt lgkmcnt(3)
buffer_load_dwordx4 v1,s[36:39],s66 offen offset:3072 sc1 lds
s_add_u32 m0,s71,s88
v_mfma_f32_16x16x32_bf16 v[28:31],a[0:3],v[44:47],v[28:31]
s_xor_b32 s87,s64,65536
ds_read_b128 v[72:75],v2 offset:7168
buffer_load_dword v10,s[36:39],s72 offen offset:0 sc1 lds
s_waitcnt vmcnt(10)
v_mfma_f32_16x16x32_bf16 v[32:35],a[4:7],v[44:47],v[32:35]
s_barrier
v_mfma_f32_16x16x32_bf16 v[28:31],a[8:11],v[48:51],v[28:31]
s_cmp_ge_u32 s66,s59
ds_read_b128 v[44:47],v2 offset:24576
v_fma_f32 v78,v98,v78,v97
v_mfma_f32_16x16x32_bf16 v[32:35],a[12:15],v[48:51],v[32:35]
s_cselect_b32 s86,s49,s50
v_exp_f32 v76,v76
v_mfma_f32_16x16x32_bf16 v[28:31],a[16:19],v[52:55],v[28:31]
s_cselect_b32 s87,s87,s63
ds_read_b128 v[48:51],v2 offset:25600
v_mul_f32 v78,v38,v78
v_mfma_f32_16x16x32_bf16 v[32:35],a[20:23],v[52:55],v[32:35]
s_mov_b32 s62,s87
v_exp_f32 v77,v77
v_mfma_f32_16x16x32_bf16 v[28:31],a[24:27],v[56:59],v[28:31]
s_mov_b32 m0,s62
ds_read_b128 v[52:55],v2 offset:26624
v_fma_f32 v76,v76,v99,v99
v_mfma_f32_16x16x32_bf16 v[32:35],a[28:31],v[56:59],v[32:35]
s_waitcnt lgkmcnt(3)
v_exp_f32 v78,v78
v_mfma_f32_16x16x32_bf16 v[28:31],a[32:35],v[60:63],v[28:31]
ds_read_b128 v[56:59],v2 offset:27648
v_fma_f32 v77,v77,v99,v99
v_mfma_f32_16x16x32_bf16 v[32:35],a[36:39],v[60:63],v[32:35]
v_rcp_f32 v76,v76
v_mfma_f32_16x16x32_bf16 v[28:31],a[40:43],v[64:67],v[28:31]
ds_read_b128 v[60:63],v2 offset:28672
v_fma_f32 v78,v78,v99,v99
v_mfma_f32_16x16x32_bf16 v[32:35],a[44:47],v[64:67],v[32:35]
v_rcp_f32 v77,v77
v_mfma_f32_16x16x32_bf16 v[28:31],a[48:51],v[68:71],v[28:31]
ds_read_b128 v[64:67],v2 offset:29696
v_fma_f32 v79,v98,v79,v97
v_mfma_f32_16x16x32_bf16 v[32:35],a[52:55],v[68:71],v[32:35]
v_rcp_f32 v78,v78
v_mfma_f32_16x16x32_bf16 v[28:31],a[56:59],v[72:75],v[28:31]
ds_read_b128 v[68:71],v2 offset:30720
v_mul_f32 v79,v39,v79
v_mfma_f32_16x16x32_bf16 v[32:35],a[60:63],v[72:75],v[32:35]
s_waitcnt lgkmcnt(3)
v_exp_f32 v79,v79
v_mfma_f32_16x16x32_bf16 v[28:31],a[192:195],v[44:47],v[28:31]
s_nop 0
ds_read_b128 v[72:75],v2 offset:31744
v_fma_f32 v79,v79,v99,v99
v_mfma_f32_16x16x32_bf16 v[32:35],a[196:199],v[44:47],v[32:35]
s_barrier
v_mfma_f32_16x16x32_bf16 v[28:31],a[200:203],v[48:51],v[28:31]
ds_read_b128 v[44:47],v2 offset:16384
ds_read_b32 v16,v7 offset:0
v_mfma_f32_16x16x32_bf16 v[32:35],a[204:207],v[48:51],v[32:35]
v_rcp_f32 v79,v79
v_mfma_f32_16x16x32_bf16 v[28:31],a[208:211],v[52:55],v[28:31]
ds_read_b128 v[48:51],v2 offset:17408
v_mul_f32 v36,v36,v76
v_mfma_f32_16x16x32_bf16 v[32:35],a[212:215],v[52:55],v[32:35]
v_mul_f32 v37,v37,v77
v_mul_f32 v38,v38,v78
v_mfma_f32_16x16x32_bf16 v[28:31],a[216:219],v[56:59],v[28:31]
s_waitcnt lgkmcnt(1)
ds_read_b128 v[52:55],v2 offset:18432
v_mul_f32 v39,v39,v79
v_mfma_f32_16x16x32_bf16 v[32:35],a[220:223],v[56:59],v[32:35]
v_cmp_eq_u32 vcc,s60,v16
v_cvt_pk_bf16_f32 v84,v36,v37
v_cvt_pk_bf16_f32 v85,v38,v39
v_fma_f32 v40,-v96,v24,v40
v_mfma_f32_16x16x32_bf16 v[28:31],a[224:227],v[60:63],v[28:31]
s_cmp_eq_u32 vcc_lo,-1
ds_read_b128 v[56:59],v2 offset:19456
v_fma_f32 v41,-v96,v25,v41
v_mfma_f32_16x16x32_bf16 v[32:35],a[228:231],v[60:63],v[32:35]
s_cselect_b32 s67,s86,s59
v_fma_f32 v42,-v96,v26,v42
v_fma_f32 v43,-v96,v27,v43
v_mfma_f32_16x16x32_bf16 v[28:31],a[232:235],v[64:67],v[28:31]
buffer_load_dwordx4 v0,s[36:39],s67 offen offset:0 sc1 lds
v_mfma_f32_16x16x32_bf16 v[32:35],a[236:239],v[64:67],v[32:35]
s_xor_b32 s63,s63,65536
ds_read_b128 v[60:63],v2 offset:20480
v_mul_f32 v80,v40,v40
v_mfma_f32_16x16x32_bf16 v[28:31],a[240:243],v[68:71],v[28:31]
s_add_u32 s53,s52,s55
ds_read_b128 v[64:67],v2 offset:21504
v_mul_f32 v81,v41,v41
v_mfma_f32_16x16x32_bf16 v[32:35],a[244:247],v[68:71],v[32:35]
s_cmp_eq_u32 s53,s56
v_mul_f32 v82,v42,v42
v_mul_f32 v83,v43,v43)"
R"(
v_mfma_f32_16x16x32_bf16 v[28:31],a[248:251],v[72:75],v[28:31]
buffer_load_dwordx4 v0,s[36:39],s67 offen offset:1024 sc1 lds
v_mfma_f32_16x16x32_bf16 v[32:35],a[252:255],v[72:75],v[32:35]
s_waitcnt lgkmcnt(2)
ds_read_b128 v[68:71],v2 offset:22528
v_fma_f32 v80,v98,v80,v97
v_mfma_f32_16x16x32_bf16 v[28:31],a[128:131],v[44:47],v[28:31]
s_waitcnt vmcnt(7)
ds_read_b128 v[72:75],v2 offset:23552
v_mul_f32 v80,v40,v80
v_mfma_f32_16x16x32_bf16 v[32:35],a[132:135],v[44:47],v[32:35]
s_barrier
v_mfma_f32_16x16x32_bf16 v[28:31],a[136:139],v[48:51],v[28:31]
s_cselect_b32 s53,s57,s53
ds_read_b128 v[44:47],v2 offset:40960
ds_read_b128 v[92:95],v8 offset:0
v_mfma_f32_16x16x32_bf16 v[32:35],a[140:143],v[48:51],v[32:35]
s_cmp_eq_u32 s53,s58
v_exp_f32 v80,v80
v_mfma_f32_16x16x32_bf16 v[28:31],a[144:147],v[52:55],v[28:31]
buffer_load_dwordx4 v0,s[36:39],s67 offen offset:2048 sc1 lds
v_mfma_f32_16x16x32_bf16 v[32:35],a[148:151],v[52:55],v[32:35]
s_cselect_b32 s53,s59,s53
ds_read_b128 v[48:51],v2 offset:41984
v_fma_f32 v80,v80,v99,v99
v_mfma_f32_16x16x32_bf16 v[28:31],a[152:155],v[56:59],v[28:31]
s_cmp_ge_u32 s66,s59
ds_read_b128 v[52:55],v2 offset:43008
v_fma_f32 v81,v98,v81,v97
v_mfma_f32_16x16x32_bf16 v[32:35],a[156:159],v[56:59],v[32:35]
s_waitcnt lgkmcnt(4)
v_fma_f32 v82,v98,v82,v97
v_fma_f32 v83,v98,v83,v97
v_mul_f32 v81,v41,v81
v_mul_f32 v82,v42,v82
v_mul_f32 v83,v43,v83
v_exp_f32 v81,v81
v_exp_f32 v82,v82
v_exp_f32 v83,v83
v_mfma_f32_16x16x32_bf16 v[28:31],a[160:163],v[60:63],v[28:31]
s_waitcnt lgkmcnt(2)
buffer_load_dwordx4 v0,s[36:39],s67 offen offset:3072 sc1 lds
v_fma_f32 v81,v81,v99,v99
v_fma_f32 v82,v82,v99,v99
v_fma_f32 v83,v83,v99,v99
v_rcp_f32 v80,v80
v_rcp_f32 v81,v81
v_rcp_f32 v82,v82
v_rcp_f32 v83,v83
v_mfma_f32_16x16x32_bf16 v[32:35],a[164:167],v[60:63],v[32:35]
ds_read_b128 v[56:59],v2 offset:44032
v_mul_f32 v40,v40,v80
v_mul_f32 v41,v41,v81
v_mul_f32 v42,v42,v82
v_mul_f32 v43,v43,v83
v_cvt_pk_bf16_f32 v86,v40,v41
v_cvt_pk_bf16_f32 v87,v42,v43
v_mfma_f32_16x16x32_bf16 v[28:31],a[168:171],v[64:67],v[28:31]
buffer_store_dwordx4 v[84:87],v3,s[36:39],s73 offen offset:0 sc0 sc1
ds_read_b128 v[60:63],v2 offset:45056
v_mfma_f32_16x16x32_bf16 v[32:35],a[172:175],v[64:67],v[32:35]
v_mfma_f32_16x16x32_bf16 v[28:31],a[176:179],v[68:71],v[28:31]
ds_read_b128 v[64:67],v2 offset:46080
v_mfma_f32_16x16x32_bf16 v[32:35],a[180:183],v[68:71],v[32:35]
v_mfma_f32_16x16x32_bf16 v[28:31],a[184:187],v[72:75],v[28:31]
s_waitcnt lgkmcnt(2)
ds_read_b128 v[68:71],v2 offset:47104
v_mfma_f32_16x16x32_bf16 v[32:35],a[188:191],v[72:75],v[32:35]
s_cbranch_scc1 Lg1_slow1_%=
Lg1_d1w2_%=:
v_mfma_f32_16x16x32_bf16 v[36:39],a[64:67],v[44:47],0
s_add_u32 m0,s62,16384
ds_read_b128 v[72:75],v2 offset:48128
s_lshr_b32 s73,s48,0
v_mfma_f32_16x16x32_bf16 v[40:43],a[68:71],v[44:47],0
s_lshr_b32 s72,s67,6
s_lshr_b32 s71,s62,2
s_cmp_lt_u32 s67,4194304
v_mfma_f32_16x16x32_bf16 v[36:39],a[72:75],v[48:51],v[36:39]
s_cselect_b32 s72,s59,s72
ds_read_b128 v[44:47],v2 offset:32768
s_mov_b64 s[44:45],s[46:47]
v_mfma_f32_16x16x32_bf16 v[40:43],a[76:79],v[48:51],v[40:43]
buffer_load_dwordx4 v1,s[36:39],s67 offen offset:0 sc1 lds
v_mfma_f32_16x16x32_bf16 v[36:39],a[80:83],v[52:55],v[36:39]
s_mov_b64 s[46:47],s[48:49]
ds_read_b128 v[48:51],v2 offset:33792
v_fma_f32 v28,-v92,v20,v28
v_mfma_f32_16x16x32_bf16 v[40:43],a[84:87],v[52:55],v[40:43]
s_mov_b64 s[48:49],s[50:51]
v_fma_f32 v29,-v92,v21,v29
v_fma_f32 v30,-v92,v22,v30
v_mfma_f32_16x16x32_bf16 v[36:39],a[88:91],v[56:59],v[36:39]
ds_read_b128 v[52:55],v2 offset:34816
v_fma_f32 v31,-v92,v23,v31
v_mfma_f32_16x16x32_bf16 v[40:43],a[92:95],v[56:59],v[40:43]
s_waitcnt lgkmcnt(3)
buffer_load_dwordx4 v1,s[36:39],s67 offen offset:1024 sc1 lds
v_mfma_f32_16x16x32_bf16 v[36:39],a[96:99],v[60:63],v[36:39]
ds_read_b128 v[56:59],v2 offset:35840
v_mul_f32 v76,v28,v28
v_mfma_f32_16x16x32_bf16 v[40:43],a[100:103],v[60:63],v[40:43]
v_mul_f32 v77,v29,v29
v_mul_f32 v78,v30,v30
v_mfma_f32_16x16x32_bf16 v[36:39],a[104:107],v[64:67],v[36:39]
ds_read_b128 v[60:63],v2 offset:36864
v_mul_f32 v79,v31,v31
v_mfma_f32_16x16x32_bf16 v[40:43],a[108:111],v[64:67],v[40:43]
buffer_load_dwordx4 v1,s[36:39],s67 offen offset:2048 sc1 lds
v_mfma_f32_16x16x32_bf16 v[36:39],a[112:115],v[68:71],v[36:39]
ds_read_b128 v[64:67],v2 offset:37888
v_fma_f32 v76,v94,v76,v93
v_mfma_f32_16x16x32_bf16 v[40:43],a[116:119],v[68:71],v[40:43]
v_mul_f32 v76,v28,v76
v_fma_f32 v77,v94,v77,v93
v_mfma_f32_16x16x32_bf16 v[36:39],a[120:123],v[72:75],v[36:39]
ds_read_b128 v[68:71],v2 offset:38912
v_mul_f32 v77,v29,v77
v_mfma_f32_16x16x32_bf16 v[40:43],a[124:127],v[72:75],v[40:43]
s_waitcnt lgkmcnt(3)
buffer_load_dwordx4 v1,s[36:39],s67 offen offset:3072 sc1 lds
s_add_u32 m0,s71,s88
v_mfma_f32_16x16x32_bf16 v[36:39],a[0:3],v[44:47],v[36:39]
s_xor_b32 s87,s63,65536
ds_read_b128 v[72:75],v2 offset:39936
buffer_load_dword v10,s[36:39],s72 offen offset:0 sc1 lds
s_waitcnt vmcnt(10)
v_mfma_f32_16x16x32_bf16 v[40:43],a[4:7],v[44:47],v[40:43]
s_barrier
v_mfma_f32_16x16x32_bf16 v[36:39],a[8:11],v[48:51],v[36:39]
s_cmp_ge_u32 s67,s59
ds_read_b128 v[44:47],v2 offset:57344
v_fma_f32 v78,v94,v78,v93
v_mfma_f32_16x16x32_bf16 v[40:43],a[12:15],v[48:51],v[40:43]
s_cselect_b32 s86,s50,s51
v_exp_f32 v76,v76
v_mfma_f32_16x16x32_bf16 v[36:39],a[16:19],v[52:55],v[36:39]
s_cselect_b32 s87,s87,s64
ds_read_b128 v[48:51],v2 offset:58368
v_mul_f32 v78,v30,v78
v_mfma_f32_16x16x32_bf16 v[40:43],a[20:23],v[52:55],v[40:43]
s_mov_b32 s62,s87
v_exp_f32 v77,v77
v_mfma_f32_16x16x32_bf16 v[36:39],a[24:27],v[56:59],v[36:39]
s_mov_b32 m0,s62
ds_read_b128 v[52:55],v2 offset:59392
v_fma_f32 v76,v76,v95,v95
v_mfma_f32_16x16x32_bf16 v[40:43],a[28:31],v[56:59],v[40:43]
s_waitcnt lgkmcnt(3)
v_exp_f32 v78,v78
v_mfma_f32_16x16x32_bf16 v[36:39],a[32:35],v[60:63],v[36:39]
s_mov_b64 s[50:51],s[52:53]
ds_read_b128 v[56:59],v2 offset:60416
v_fma_f32 v77,v77,v95,v95
v_mfma_f32_16x16x32_bf16 v[40:43],a[36:39],v[60:63],v[40:43]
v_rcp_f32 v76,v76
v_mfma_f32_16x16x32_bf16 v[36:39],a[40:43],v[64:67],v[36:39]
ds_read_b128 v[60:63],v2 offset:61440
v_fma_f32 v78,v78,v95,v95
v_mfma_f32_16x16x32_bf16 v[40:43],a[44:47],v[64:67],v[40:43]
v_rcp_f32 v77,v77
v_mfma_f32_16x16x32_bf16 v[36:39],a[48:51],v[68:71],v[36:39]
ds_read_b128 v[64:67],v2 offset:62464
v_fma_f32 v79,v94,v79,v93
v_mfma_f32_16x16x32_bf16 v[40:43],a[52:55],v[68:71],v[40:43]
v_rcp_f32 v78,v78
v_mfma_f32_16x16x32_bf16 v[36:39],a[56:59],v[72:75],v[36:39]
ds_read_b128 v[68:71],v2 offset:63488
v_mul_f32 v79,v31,v79
v_mfma_f32_16x16x32_bf16 v[40:43],a[60:63],v[72:75],v[40:43]
s_waitcnt lgkmcnt(3)
v_exp_f32 v79,v79
v_mfma_f32_16x16x32_bf16 v[36:39],a[192:195],v[44:47],v[36:39]
s_nop 0
ds_read_b128 v[72:75],v2 offset:64512
v_fma_f32 v79,v79,v95,v95
v_mfma_f32_16x16x32_bf16 v[40:43],a[196:199],v[44:47],v[40:43]
s_barrier
v_mfma_f32_16x16x32_bf16 v[36:39],a[200:203],v[48:51],v[36:39]
ds_read_b128 v[44:47],v2 offset:49152
ds_read_b32 v16,v7 offset:256
v_mfma_f32_16x16x32_bf16 v[40:43],a[204:207],v[48:51],v[40:43]
v_rcp_f32 v79,v79
v_mfma_f32_16x16x32_bf16 v[36:39],a[208:211],v[52:55],v[36:39]
ds_read_b128 v[48:51],v2 offset:50176
v_mul_f32 v28,v28,v76
v_mfma_f32_16x16x32_bf16 v[40:43],a[212:215],v[52:55],v[40:43]
v_mul_f32 v29,v29,v77
v_mul_f32 v30,v30,v78
v_mfma_f32_16x16x32_bf16 v[36:39],a[216:219],v[56:59],v[36:39]
s_waitcnt lgkmcnt(1)
ds_read_b128 v[52:55],v2 offset:51200
v_mul_f32 v31,v31,v79
v_mfma_f32_16x16x32_bf16 v[40:43],a[220:223],v[56:59],v[40:43]
v_cmp_eq_u32 vcc,s60,v16
v_cvt_pk_bf16_f32 v84,v28,v29
v_cvt_pk_bf16_f32 v85,v30,v31
v_fma_f32 v32,-v92,v24,v32
v_mfma_f32_16x16x32_bf16 v[36:39],a[224:227],v[60:63],v[36:39]
s_cmp_eq_u32 vcc_lo,-1)"
R"(
ds_read_b128 v[56:59],v2 offset:52224
v_fma_f32 v33,-v92,v25,v33
v_mfma_f32_16x16x32_bf16 v[40:43],a[228:231],v[60:63],v[40:43]
s_cselect_b32 s66,s86,s59
v_fma_f32 v34,-v92,v26,v34
v_fma_f32 v35,-v92,v27,v35
v_mfma_f32_16x16x32_bf16 v[36:39],a[232:235],v[64:67],v[36:39]
buffer_load_dwordx4 v0,s[36:39],s66 offen offset:0 sc1 lds
v_mfma_f32_16x16x32_bf16 v[40:43],a[236:239],v[64:67],v[40:43]
s_xor_b32 s64,s64,65536
ds_read_b128 v[60:63],v2 offset:53248
v_mul_f32 v80,v32,v32
v_mfma_f32_16x16x32_bf16 v[36:39],a[240:243],v[68:71],v[36:39]
s_add_u32 s74,s53,s55
ds_read_b128 v[64:67],v2 offset:54272
v_mul_f32 v81,v33,v33
v_mfma_f32_16x16x32_bf16 v[40:43],a[244:247],v[68:71],v[40:43]
s_cmp_eq_u32 s74,s56
v_mul_f32 v82,v34,v34
v_mul_f32 v83,v35,v35
v_mfma_f32_16x16x32_bf16 v[36:39],a[248:251],v[72:75],v[36:39]
buffer_load_dwordx4 v0,s[36:39],s66 offen offset:1024 sc1 lds
v_mfma_f32_16x16x32_bf16 v[40:43],a[252:255],v[72:75],v[40:43]
s_waitcnt lgkmcnt(2)
ds_read_b128 v[68:71],v2 offset:55296
v_fma_f32 v80,v94,v80,v93
v_mfma_f32_16x16x32_bf16 v[36:39],a[128:131],v[44:47],v[36:39]
s_waitcnt vmcnt(7)
ds_read_b128 v[72:75],v2 offset:56320
v_xor_b32 v2,65536,v2
v_mfma_f32_16x16x32_bf16 v[40:43],a[132:135],v[44:47],v[40:43]
s_barrier
v_mfma_f32_16x16x32_bf16 v[36:39],a[136:139],v[48:51],v[36:39]
s_cselect_b32 s74,s57,s74
ds_read_b128 v[44:47],v2 offset:8192
ds_read_b128 v[96:99],v8 offset:8192
v_mfma_f32_16x16x32_bf16 v[40:43],a[140:143],v[48:51],v[40:43]
s_cmp_eq_u32 s74,s58
v_mul_f32 v80,v32,v80
v_fma_f32 v81,v94,v81,v93
v_mfma_f32_16x16x32_bf16 v[36:39],a[144:147],v[52:55],v[36:39]
buffer_load_dwordx4 v0,s[36:39],s66 offen offset:2048 sc1 lds
v_mfma_f32_16x16x32_bf16 v[40:43],a[148:151],v[52:55],v[40:43]
s_cselect_b32 s74,s59,s74
ds_read_b128 v[48:51],v2 offset:9216
v_mul_f32 v81,v33,v81
v_mfma_f32_16x16x32_bf16 v[36:39],a[152:155],v[56:59],v[36:39]
s_mov_b32 s52,s74
ds_read_b128 v[52:55],v2 offset:10240
v_fma_f32 v82,v94,v82,v93
v_mfma_f32_16x16x32_bf16 v[40:43],a[156:159],v[56:59],v[40:43]
s_waitcnt lgkmcnt(4)
v_fma_f32 v83,v94,v83,v93
v_mul_f32 v82,v34,v82
v_mul_f32 v83,v35,v83
v_exp_f32 v80,v80
v_exp_f32 v81,v81
v_exp_f32 v82,v82
v_exp_f32 v83,v83
v_mfma_f32_16x16x32_bf16 v[36:39],a[160:163],v[60:63],v[36:39]
s_waitcnt lgkmcnt(2)
buffer_load_dwordx4 v0,s[36:39],s66 offen offset:3072 sc1 lds
v_fma_f32 v80,v80,v95,v95
v_fma_f32 v81,v81,v95,v95
v_fma_f32 v82,v82,v95,v95
v_fma_f32 v83,v83,v95,v95
v_rcp_f32 v80,v80
v_rcp_f32 v81,v81
v_rcp_f32 v82,v82
v_rcp_f32 v83,v83
v_mfma_f32_16x16x32_bf16 v[40:43],a[164:167],v[60:63],v[40:43]
s_cmp_lt_u32 s67,s59
ds_read_b128 v[56:59],v2 offset:11264
v_mul_f32 v32,v32,v80
v_mul_f32 v33,v33,v81
v_mul_f32 v34,v34,v82
v_mul_f32 v35,v35,v83
v_cvt_pk_bf16_f32 v86,v32,v33
v_cvt_pk_bf16_f32 v87,v34,v35
v_mfma_f32_16x16x32_bf16 v[36:39],a[168:171],v[64:67],v[36:39]
buffer_store_dwordx4 v[84:87],v3,s[36:39],s73 offen offset:0 sc0 sc1
ds_read_b128 v[60:63],v2 offset:12288
v_xor_b32 v8,16384,v8
v_mfma_f32_16x16x32_bf16 v[40:43],a[172:175],v[64:67],v[40:43]
v_mfma_f32_16x16x32_bf16 v[36:39],a[176:179],v[68:71],v[36:39]
ds_read_b128 v[64:67],v2 offset:13312
v_mfma_f32_16x16x32_bf16 v[40:43],a[180:183],v[68:71],v[40:43]
v_mfma_f32_16x16x32_bf16 v[36:39],a[184:187],v[72:75],v[36:39]
s_waitcnt lgkmcnt(2)
ds_read_b128 v[68:71],v2 offset:14336
v_mfma_f32_16x16x32_bf16 v[40:43],a[188:191],v[72:75],v[40:43]
s_cbranch_scc1 Lg1_d0w2_%=
s_branch Lg1_slow0_%=
Lg1_d0w3_%=:
v_mfma_f32_16x16x32_bf16 v[28:31],a[96:99],v[44:47],0
s_add_u32 m0,s62,16384
ds_read_b128 v[72:75],v2 offset:11264
s_lshr_b32 s73,s47,0
v_mfma_f32_16x16x32_bf16 v[32:35],a[100:103],v[44:47],0
s_lshr_b32 s72,s66,6
s_lshr_b32 s71,s62,2
s_cmp_lt_u32 s66,4194304
v_mfma_f32_16x16x32_bf16 v[28:31],a[104:107],v[48:51],v[28:31]
s_cselect_b32 s72,s59,s72
ds_read_b128 v[44:47],v2 offset:4096
v_mfma_f32_16x16x32_bf16 v[32:35],a[108:111],v[48:51],v[32:35]
v_fma_f32 v36,-v96,v20,v36
v_fma_f32 v37,-v96,v21,v37
v_mfma_f32_16x16x32_bf16 v[28:31],a[112:115],v[52:55],v[28:31]
buffer_load_dwordx4 v1,s[36:39],s66 offen offset:0 sc1 lds
v_mfma_f32_16x16x32_bf16 v[32:35],a[116:119],v[52:55],v[32:35]
ds_read_b128 v[48:51],v2 offset:5120
v_fma_f32 v38,-v96,v22,v38
v_mfma_f32_16x16x32_bf16 v[28:31],a[120:123],v[56:59],v[28:31]
ds_read_b128 v[52:55],v2 offset:6144
v_fma_f32 v39,-v96,v23,v39
v_mfma_f32_16x16x32_bf16 v[32:35],a[124:127],v[56:59],v[32:35]
s_waitcnt lgkmcnt(3)
v_mul_f32 v76,v36,v36
v_mul_f32 v77,v37,v37
v_mfma_f32_16x16x32_bf16 v[28:31],a[64:67],v[60:63],v[28:31]
buffer_load_dwordx4 v1,s[36:39],s66 offen offset:1024 sc1 lds
v_mfma_f32_16x16x32_bf16 v[32:35],a[68:71],v[60:63],v[32:35]
ds_read_b128 v[56:59],v2 offset:7168
v_mul_f32 v78,v38,v38
v_mfma_f32_16x16x32_bf16 v[28:31],a[72:75],v[64:67],v[28:31]
ds_read_b128 v[60:63],v2 offset:0
v_mul_f32 v79,v39,v39
v_mfma_f32_16x16x32_bf16 v[32:35],a[76:79],v[64:67],v[32:35]
v_fma_f32 v76,v98,v76,v97
v_mul_f32 v76,v36,v76
v_mfma_f32_16x16x32_bf16 v[28:31],a[80:83],v[68:71],v[28:31]
buffer_load_dwordx4 v1,s[36:39],s66 offen offset:2048 sc1 lds
v_mfma_f32_16x16x32_bf16 v[32:35],a[84:87],v[68:71],v[32:35]
ds_read_b128 v[64:67],v2 offset:1024
v_fma_f32 v77,v98,v77,v97
v_mfma_f32_16x16x32_bf16 v[28:31],a[88:91],v[72:75],v[28:31]
ds_read_b128 v[68:71],v2 offset:2048
v_mul_f32 v77,v37,v77
v_mfma_f32_16x16x32_bf16 v[32:35],a[92:95],v[72:75],v[32:35]
s_waitcnt lgkmcnt(3)
v_exp_f32 v76,v76
v_mfma_f32_16x16x32_bf16 v[28:31],a[32:35],v[44:47],v[28:31]
s_waitcnt vmcnt(9)
ds_read_b128 v[72:75],v2 offset:3072
v_fma_f32 v76,v76,v99,v99
v_mfma_f32_16x16x32_bf16 v[32:35],a[36:39],v[44:47],v[32:35]
s_barrier
v_mfma_f32_16x16x32_bf16 v[28:31],a[40:43],v[48:51],v[28:31]
buffer_load_dwordx4 v1,s[36:39],s66 offen offset:3072 sc1 lds
v_mfma_f32_16x16x32_bf16 v[32:35],a[44:47],v[48:51],v[32:35]
s_add_u32 m0,s71,s88
ds_read_b128 v[44:47],v2 offset:28672
v_fma_f32 v78,v98,v78,v97
v_mfma_f32_16x16x32_bf16 v[28:31],a[48:51],v[52:55],v[28:31]
buffer_load_dword v10,s[36:39],s72 offen offset:0 sc1 lds
v_mfma_f32_16x16x32_bf16 v[32:35],a[52:55],v[52:55],v[32:35]
s_xor_b32 s87,s64,65536
ds_read_b128 v[48:51],v2 offset:29696
v_fma_f32 v79,v98,v79,v97
v_mfma_f32_16x16x32_bf16 v[28:31],a[56:59],v[56:59],v[28:31]
s_cmp_ge_u32 s66,s59
ds_read_b128 v[52:55],v2 offset:30720
v_mul_f32 v78,v38,v78
v_mfma_f32_16x16x32_bf16 v[32:35],a[60:63],v[56:59],v[32:35]
s_waitcnt lgkmcnt(3)
v_exp_f32 v77,v77
v_mfma_f32_16x16x32_bf16 v[28:31],a[0:3],v[60:63],v[28:31]
s_cselect_b32 s86,s49,s50
ds_read_b128 v[56:59],v2 offset:31744
v_mul_f32 v79,v39,v79
v_mfma_f32_16x16x32_bf16 v[32:35],a[4:7],v[60:63],v[32:35]
s_cselect_b32 s87,s87,s63
v_exp_f32 v78,v78
v_mfma_f32_16x16x32_bf16 v[28:31],a[8:11],v[64:67],v[28:31]
s_mov_b32 s62,s87
ds_read_b128 v[60:63],v2 offset:24576
v_fma_f32 v77,v77,v99,v99
v_mfma_f32_16x16x32_bf16 v[32:35],a[12:15],v[64:67],v[32:35]
s_mov_b32 m0,s62
v_exp_f32 v79,v79
v_mfma_f32_16x16x32_bf16 v[28:31],a[16:19],v[68:71],v[28:31]
ds_read_b128 v[64:67],v2 offset:25600
v_fma_f32 v78,v78,v99,v99
v_mfma_f32_16x16x32_bf16 v[32:35],a[20:23],v[68:71],v[32:35]
v_rcp_f32 v76,v76
v_mfma_f32_16x16x32_bf16 v[28:31],a[24:27],v[72:75],v[28:31]
ds_read_b128 v[68:71],v2 offset:26624
v_fma_f32 v79,v79,v99,v99
v_mfma_f32_16x16x32_bf16 v[32:35],a[28:31],v[72:75],v[32:35]
s_waitcnt lgkmcnt(3)
v_rcp_f32 v77,v77
v_mfma_f32_16x16x32_bf16 v[28:31],a[224:227],v[44:47],v[28:31]
s_nop 0
ds_read_b128 v[72:75],v2 offset:27648
v_mul_f32 v36,v36,v76
v_mfma_f32_16x16x32_bf16 v[32:35],a[228:231],v[44:47],v[32:35]
s_barrier
v_mfma_f32_16x16x32_bf16 v[28:31],a[232:235],v[48:51],v[28:31]
ds_read_b128 v[44:47],v2 offset:20480)"
R"(
ds_read_b32 v16,v7 offset:0
v_mfma_f32_16x16x32_bf16 v[32:35],a[236:239],v[48:51],v[32:35]
v_rcp_f32 v78,v78
v_mfma_f32_16x16x32_bf16 v[28:31],a[240:243],v[52:55],v[28:31]
ds_read_b128 v[48:51],v2 offset:21504
v_mul_f32 v37,v37,v77
v_mfma_f32_16x16x32_bf16 v[32:35],a[244:247],v[52:55],v[32:35]
v_rcp_f32 v79,v79
v_mfma_f32_16x16x32_bf16 v[28:31],a[248:251],v[56:59],v[28:31]
s_waitcnt lgkmcnt(1)
ds_read_b128 v[52:55],v2 offset:22528
v_mul_f32 v38,v38,v78
v_mfma_f32_16x16x32_bf16 v[32:35],a[252:255],v[56:59],v[32:35]
v_cmp_eq_u32 vcc,s60,v16
v_mul_f32 v39,v39,v79
v_cvt_pk_bf16_f32 v84,v36,v37
v_cvt_pk_bf16_f32 v85,v38,v39
v_mfma_f32_16x16x32_bf16 v[28:31],a[192:195],v[60:63],v[28:31]
s_cmp_eq_u32 vcc_lo,-1
ds_read_b128 v[56:59],v2 offset:23552
v_fma_f32 v40,-v96,v24,v40
v_mfma_f32_16x16x32_bf16 v[32:35],a[196:199],v[60:63],v[32:35]
s_cselect_b32 s67,s86,s59
v_fma_f32 v41,-v96,v25,v41
v_fma_f32 v42,-v96,v26,v42
v_mfma_f32_16x16x32_bf16 v[28:31],a[200:203],v[64:67],v[28:31]
s_lshr_b32 s70,s46,5
ds_read_b128 v[60:63],v2 offset:16384
v_fma_f32 v43,-v96,v27,v43
v_mfma_f32_16x16x32_bf16 v[32:35],a[204:207],v[64:67],v[32:35]
buffer_load_dwordx4 v0,s[36:39],s67 offen offset:0 sc1 lds
v_mfma_f32_16x16x32_bf16 v[28:31],a[208:211],v[68:71],v[28:31]
s_xor_b32 s63,s63,65536
ds_read_b128 v[64:67],v2 offset:17408
v_mul_f32 v80,v40,v40
v_mfma_f32_16x16x32_bf16 v[32:35],a[212:215],v[68:71],v[32:35]
s_add_u32 s53,s52,s55
v_mul_f32 v81,v41,v41
v_mul_f32 v82,v42,v42
v_mfma_f32_16x16x32_bf16 v[28:31],a[216:219],v[72:75],v[28:31]
s_cmp_eq_u32 s53,s56
ds_read_b128 v[68:71],v2 offset:18432
v_mul_f32 v83,v43,v43
v_mfma_f32_16x16x32_bf16 v[32:35],a[220:223],v[72:75],v[32:35]
s_waitcnt lgkmcnt(3)
buffer_load_dwordx4 v0,s[36:39],s67 offen offset:1024 sc1 lds
v_mfma_f32_16x16x32_bf16 v[28:31],a[160:163],v[44:47],v[28:31]
s_waitcnt vmcnt(7)
ds_read_b128 v[72:75],v2 offset:19456
v_fma_f32 v80,v98,v80,v97
v_mfma_f32_16x16x32_bf16 v[32:35],a[164:167],v[44:47],v[32:35]
s_barrier
v_mfma_f32_16x16x32_bf16 v[28:31],a[168:171],v[48:51],v[28:31]
buffer_store_dword v5,v4,s[36:39],s70 offen offset:0 sc0 sc1
ds_read_b128 v[44:47],v2 offset:45056
ds_read_b128 v[92:95],v8 offset:0
v_mul_f32 v80,v40,v80
v_mfma_f32_16x16x32_bf16 v[32:35],a[172:175],v[48:51],v[32:35]
s_cselect_b32 s53,s57,s53
v_exp_f32 v80,v80
v_mfma_f32_16x16x32_bf16 v[28:31],a[176:179],v[52:55],v[28:31]
s_cmp_eq_u32 s53,s58
ds_read_b128 v[48:51],v2 offset:46080
v_fma_f32 v80,v80,v99,v99
v_mfma_f32_16x16x32_bf16 v[32:35],a[180:183],v[52:55],v[32:35]
buffer_load_dwordx4 v0,s[36:39],s67 offen offset:2048 sc1 lds
v_mfma_f32_16x16x32_bf16 v[28:31],a[184:187],v[56:59],v[28:31]
s_cselect_b32 s53,s59,s53
ds_read_b128 v[52:55],v2 offset:47104
v_fma_f32 v81,v98,v81,v97
v_mfma_f32_16x16x32_bf16 v[32:35],a[188:191],v[56:59],v[32:35]
s_waitcnt lgkmcnt(4)
v_rcp_f32 v80,v80
v_mfma_f32_16x16x32_bf16 v[28:31],a[128:131],v[60:63],v[28:31]
s_waitcnt lgkmcnt(2)
v_fma_f32 v82,v98,v82,v97
v_fma_f32 v83,v98,v83,v97
v_mul_f32 v81,v41,v81
v_mul_f32 v82,v42,v82
v_mul_f32 v83,v43,v83
v_exp_f32 v81,v81
v_exp_f32 v82,v82
v_exp_f32 v83,v83
v_mfma_f32_16x16x32_bf16 v[32:35],a[132:135],v[60:63],v[32:35]
s_cmp_ge_u32 s66,s59
ds_read_b128 v[56:59],v2 offset:48128
buffer_load_dwordx4 v0,s[36:39],s67 offen offset:3072 sc1 lds
v_fma_f32 v81,v81,v99,v99
v_fma_f32 v82,v82,v99,v99
v_fma_f32 v83,v83,v99,v99
v_rcp_f32 v81,v81
v_rcp_f32 v82,v82
v_rcp_f32 v83,v83
v_mfma_f32_16x16x32_bf16 v[28:31],a[136:139],v[64:67],v[28:31]
v_mul_f32 v40,v40,v80
v_mul_f32 v41,v41,v81
v_mul_f32 v42,v42,v82
v_mul_f32 v43,v43,v83
v_cvt_pk_bf16_f32 v86,v40,v41
v_cvt_pk_bf16_f32 v87,v42,v43
v_mfma_f32_16x16x32_bf16 v[32:35],a[140:143],v[64:67],v[32:35]
ds_read_b128 v[60:63],v2 offset:40960
buffer_store_dwordx4 v[84:87],v3,s[36:39],s73 offen offset:0 sc0 sc1
v_mfma_f32_16x16x32_bf16 v[28:31],a[144:147],v[68:71],v[28:31]
ds_read_b128 v[64:67],v2 offset:41984
v_mfma_f32_16x16x32_bf16 v[32:35],a[148:151],v[68:71],v[32:35]
v_mfma_f32_16x16x32_bf16 v[28:31],a[152:155],v[72:75],v[28:31]
s_waitcnt lgkmcnt(2)
ds_read_b128 v[68:71],v2 offset:43008
v_mfma_f32_16x16x32_bf16 v[32:35],a[156:159],v[72:75],v[32:35]
s_cbranch_scc1 Lg1_slow1_%=
Lg1_d1w3_%=:
v_mfma_f32_16x16x32_bf16 v[36:39],a[96:99],v[44:47],0
s_add_u32 m0,s62,16384
ds_read_b128 v[72:75],v2 offset:44032
s_lshr_b32 s73,s48,0
v_mfma_f32_16x16x32_bf16 v[40:43],a[100:103],v[44:47],0
s_lshr_b32 s72,s67,6
s_lshr_b32 s71,s62,2
s_cmp_lt_u32 s67,4194304
v_mfma_f32_16x16x32_bf16 v[36:39],a[104:107],v[48:51],v[36:39]
s_cselect_b32 s72,s59,s72
ds_read_b128 v[44:47],v2 offset:36864
s_mov_b64 s[44:45],s[46:47]
v_mfma_f32_16x16x32_bf16 v[40:43],a[108:111],v[48:51],v[40:43]
v_fma_f32 v28,-v92,v20,v28
v_fma_f32 v29,-v92,v21,v29
v_mfma_f32_16x16x32_bf16 v[36:39],a[112:115],v[52:55],v[36:39]
buffer_load_dwordx4 v1,s[36:39],s67 offen offset:0 sc1 lds
v_mfma_f32_16x16x32_bf16 v[40:43],a[116:119],v[52:55],v[40:43]
ds_read_b128 v[48:51],v2 offset:37888
v_fma_f32 v30,-v92,v22,v30
v_mfma_f32_16x16x32_bf16 v[36:39],a[120:123],v[56:59],v[36:39]
ds_read_b128 v[52:55],v2 offset:38912
v_fma_f32 v31,-v92,v23,v31
v_mfma_f32_16x16x32_bf16 v[40:43],a[124:127],v[56:59],v[40:43]
s_waitcnt lgkmcnt(3)
v_mul_f32 v76,v28,v28
v_mul_f32 v77,v29,v29
v_mfma_f32_16x16x32_bf16 v[36:39],a[64:67],v[60:63],v[36:39]
buffer_load_dwordx4 v1,s[36:39],s67 offen offset:1024 sc1 lds
v_mfma_f32_16x16x32_bf16 v[40:43],a[68:71],v[60:63],v[40:43]
ds_read_b128 v[56:59],v2 offset:39936
v_mul_f32 v78,v30,v30
v_mfma_f32_16x16x32_bf16 v[36:39],a[72:75],v[64:67],v[36:39]
ds_read_b128 v[60:63],v2 offset:32768
v_mul_f32 v79,v31,v31
v_mfma_f32_16x16x32_bf16 v[40:43],a[76:79],v[64:67],v[40:43]
v_fma_f32 v76,v94,v76,v93
v_mul_f32 v76,v28,v76
v_mfma_f32_16x16x32_bf16 v[36:39],a[80:83],v[68:71],v[36:39]
buffer_load_dwordx4 v1,s[36:39],s67 offen offset:2048 sc1 lds
v_mfma_f32_16x16x32_bf16 v[40:43],a[84:87],v[68:71],v[40:43]
ds_read_b128 v[64:67],v2 offset:33792
v_fma_f32 v77,v94,v77,v93
v_mfma_f32_16x16x32_bf16 v[36:39],a[88:91],v[72:75],v[36:39]
ds_read_b128 v[68:71],v2 offset:34816
v_mul_f32 v77,v29,v77
v_mfma_f32_16x16x32_bf16 v[40:43],a[92:95],v[72:75],v[40:43]
s_waitcnt lgkmcnt(3)
v_exp_f32 v76,v76
v_mfma_f32_16x16x32_bf16 v[36:39],a[32:35],v[44:47],v[36:39]
s_waitcnt vmcnt(9)
ds_read_b128 v[72:75],v2 offset:35840
v_fma_f32 v76,v76,v95,v95
v_mfma_f32_16x16x32_bf16 v[40:43],a[36:39],v[44:47],v[40:43]
s_barrier
v_mfma_f32_16x16x32_bf16 v[36:39],a[40:43],v[48:51],v[36:39]
buffer_load_dwordx4 v1,s[36:39],s67 offen offset:3072 sc1 lds
v_mfma_f32_16x16x32_bf16 v[40:43],a[44:47],v[48:51],v[40:43]
s_add_u32 m0,s71,s88
ds_read_b128 v[44:47],v2 offset:61440
v_fma_f32 v78,v94,v78,v93
v_mfma_f32_16x16x32_bf16 v[36:39],a[48:51],v[52:55],v[36:39]
buffer_load_dword v10,s[36:39],s72 offen offset:0 sc1 lds
v_mfma_f32_16x16x32_bf16 v[40:43],a[52:55],v[52:55],v[40:43]
s_xor_b32 s87,s63,65536
ds_read_b128 v[48:51],v2 offset:62464
v_fma_f32 v79,v94,v79,v93
v_mfma_f32_16x16x32_bf16 v[36:39],a[56:59],v[56:59],v[36:39]
s_cmp_ge_u32 s67,s59
ds_read_b128 v[52:55],v2 offset:63488
v_mul_f32 v78,v30,v78
v_mfma_f32_16x16x32_bf16 v[40:43],a[60:63],v[56:59],v[40:43]
s_waitcnt lgkmcnt(3)
v_exp_f32 v77,v77
v_mfma_f32_16x16x32_bf16 v[36:39],a[0:3],v[60:63],v[36:39]
s_cselect_b32 s86,s50,s51
ds_read_b128 v[56:59],v2 offset:64512
v_mul_f32 v79,v31,v79
v_mfma_f32_16x16x32_bf16 v[40:43],a[4:7],v[60:63],v[40:43]
s_cselect_b32 s87,s87,s64
v_exp_f32 v78,v78
v_mfma_f32_16x16x32_bf16 v[36:39],a[8:11],v[64:67],v[36:39]
s_mov_b32 s62,s87
ds_read_b128 v[60:63],v2 offset:57344
v_fma_f32 v77,v77,v95,v95
v_mfma_f32_16x16x32_bf16 v[40:43],a[12:15],v[64:67],v[40:43]
s_mov_b32 m0,s62
v_exp_f32 v79,v79)"
R"(
v_mfma_f32_16x16x32_bf16 v[36:39],a[16:19],v[68:71],v[36:39]
ds_read_b128 v[64:67],v2 offset:58368
v_fma_f32 v78,v78,v95,v95
v_mfma_f32_16x16x32_bf16 v[40:43],a[20:23],v[68:71],v[40:43]
v_rcp_f32 v76,v76
v_mfma_f32_16x16x32_bf16 v[36:39],a[24:27],v[72:75],v[36:39]
ds_read_b128 v[68:71],v2 offset:59392
v_fma_f32 v79,v79,v95,v95
v_mfma_f32_16x16x32_bf16 v[40:43],a[28:31],v[72:75],v[40:43]
s_waitcnt lgkmcnt(3)
v_rcp_f32 v77,v77
v_mfma_f32_16x16x32_bf16 v[36:39],a[224:227],v[44:47],v[36:39]
s_nop 0
ds_read_b128 v[72:75],v2 offset:60416
v_mul_f32 v28,v28,v76
v_mfma_f32_16x16x32_bf16 v[40:43],a[228:231],v[44:47],v[40:43]
s_barrier
v_mfma_f32_16x16x32_bf16 v[36:39],a[232:235],v[48:51],v[36:39]
ds_read_b128 v[44:47],v2 offset:53248
ds_read_b32 v16,v7 offset:256
v_mfma_f32_16x16x32_bf16 v[40:43],a[236:239],v[48:51],v[40:43]
v_rcp_f32 v78,v78
v_mfma_f32_16x16x32_bf16 v[36:39],a[240:243],v[52:55],v[36:39]
ds_read_b128 v[48:51],v2 offset:54272
v_mul_f32 v29,v29,v77
v_mfma_f32_16x16x32_bf16 v[40:43],a[244:247],v[52:55],v[40:43]
v_rcp_f32 v79,v79
v_mfma_f32_16x16x32_bf16 v[36:39],a[248:251],v[56:59],v[36:39]
s_waitcnt lgkmcnt(1)
ds_read_b128 v[52:55],v2 offset:55296
v_mul_f32 v30,v30,v78
v_mfma_f32_16x16x32_bf16 v[40:43],a[252:255],v[56:59],v[40:43]
v_cmp_eq_u32 vcc,s60,v16
v_mul_f32 v31,v31,v79
v_cvt_pk_bf16_f32 v84,v28,v29
v_cvt_pk_bf16_f32 v85,v30,v31
v_mfma_f32_16x16x32_bf16 v[36:39],a[192:195],v[60:63],v[36:39]
s_cmp_eq_u32 vcc_lo,-1
ds_read_b128 v[56:59],v2 offset:56320
v_fma_f32 v32,-v92,v24,v32
v_mfma_f32_16x16x32_bf16 v[40:43],a[196:199],v[60:63],v[40:43]
s_cselect_b32 s66,s86,s59
v_fma_f32 v33,-v92,v25,v33
v_fma_f32 v34,-v92,v26,v34
v_mfma_f32_16x16x32_bf16 v[36:39],a[200:203],v[64:67],v[36:39]
s_lshr_b32 s70,s47,5
ds_read_b128 v[60:63],v2 offset:49152
v_fma_f32 v35,-v92,v27,v35
v_mfma_f32_16x16x32_bf16 v[40:43],a[204:207],v[64:67],v[40:43]
buffer_load_dwordx4 v0,s[36:39],s66 offen offset:0 sc1 lds
v_mfma_f32_16x16x32_bf16 v[36:39],a[208:211],v[68:71],v[36:39]
s_xor_b32 s64,s64,65536
ds_read_b128 v[64:67],v2 offset:50176
v_mul_f32 v80,v32,v32
v_mfma_f32_16x16x32_bf16 v[40:43],a[212:215],v[68:71],v[40:43]
s_add_u32 s74,s53,s55
v_mul_f32 v81,v33,v33
v_mul_f32 v82,v34,v34
v_mfma_f32_16x16x32_bf16 v[36:39],a[216:219],v[72:75],v[36:39]
s_cmp_eq_u32 s74,s56
ds_read_b128 v[68:71],v2 offset:51200
v_mul_f32 v83,v35,v35
v_mfma_f32_16x16x32_bf16 v[40:43],a[220:223],v[72:75],v[40:43]
s_waitcnt lgkmcnt(3)
buffer_load_dwordx4 v0,s[36:39],s66 offen offset:1024 sc1 lds
v_mfma_f32_16x16x32_bf16 v[36:39],a[160:163],v[44:47],v[36:39]
s_waitcnt vmcnt(7)
ds_read_b128 v[72:75],v2 offset:52224
v_xor_b32 v2,65536,v2
v_mfma_f32_16x16x32_bf16 v[40:43],a[164:167],v[44:47],v[40:43]
s_barrier
v_mfma_f32_16x16x32_bf16 v[36:39],a[168:171],v[48:51],v[36:39]
buffer_store_dword v5,v4,s[36:39],s70 offen offset:0 sc0 sc1
ds_read_b128 v[44:47],v2 offset:12288
ds_read_b128 v[96:99],v8 offset:8192
v_fma_f32 v80,v94,v80,v93
v_mfma_f32_16x16x32_bf16 v[40:43],a[172:175],v[48:51],v[40:43]
s_cselect_b32 s74,s57,s74
v_mul_f32 v80,v32,v80
v_fma_f32 v81,v94,v81,v93
v_mfma_f32_16x16x32_bf16 v[36:39],a[176:179],v[52:55],v[36:39]
s_cmp_eq_u32 s74,s58
ds_read_b128 v[48:51],v2 offset:13312
v_mul_f32 v81,v33,v81
v_mfma_f32_16x16x32_bf16 v[40:43],a[180:183],v[52:55],v[40:43]
buffer_load_dwordx4 v0,s[36:39],s66 offen offset:2048 sc1 lds
v_mfma_f32_16x16x32_bf16 v[36:39],a[184:187],v[56:59],v[36:39]
s_cselect_b32 s74,s59,s74
ds_read_b128 v[52:55],v2 offset:14336
v_fma_f32 v82,v94,v82,v93
v_mfma_f32_16x16x32_bf16 v[40:43],a[188:191],v[56:59],v[40:43]
s_waitcnt lgkmcnt(4)
v_exp_f32 v80,v80
v_mfma_f32_16x16x32_bf16 v[36:39],a[128:131],v[60:63],v[36:39]
s_waitcnt lgkmcnt(2)
v_fma_f32 v83,v94,v83,v93
v_mul_f32 v82,v34,v82
v_mul_f32 v83,v35,v83
v_exp_f32 v81,v81
v_exp_f32 v82,v82
v_exp_f32 v83,v83
v_mfma_f32_16x16x32_bf16 v[40:43],a[132:135],v[60:63],v[40:43]
s_mov_b64 s[46:47],s[48:49]
ds_read_b128 v[56:59],v2 offset:15360
buffer_load_dwordx4 v0,s[36:39],s66 offen offset:3072 sc1 lds
v_fma_f32 v80,v80,v95,v95
v_fma_f32 v81,v81,v95,v95
v_fma_f32 v82,v82,v95,v95
v_fma_f32 v83,v83,v95,v95
v_rcp_f32 v80,v80
v_rcp_f32 v81,v81
v_rcp_f32 v82,v82
v_rcp_f32 v83,v83
v_mfma_f32_16x16x32_bf16 v[36:39],a[136:139],v[64:67],v[36:39]
s_mov_b64 s[48:49],s[50:51]
v_mul_f32 v32,v32,v80
v_mul_f32 v33,v33,v81
v_mul_f32 v34,v34,v82
v_mul_f32 v35,v35,v83
v_cvt_pk_bf16_f32 v86,v32,v33
v_cvt_pk_bf16_f32 v87,v34,v35
v_mfma_f32_16x16x32_bf16 v[40:43],a[140:143],v[64:67],v[40:43]
s_mov_b64 s[50:51],s[52:53]
ds_read_b128 v[60:63],v2 offset:8192
buffer_store_dwordx4 v[84:87],v3,s[36:39],s73 offen offset:0 sc0 sc1
v_xor_b32 v8,16384,v8
s_mov_b32 s52,s74
s_cmp_lt_u32 s67,s59
v_mfma_f32_16x16x32_bf16 v[36:39],a[144:147],v[68:71],v[36:39]
ds_read_b128 v[64:67],v2 offset:9216
v_mfma_f32_16x16x32_bf16 v[40:43],a[148:151],v[68:71],v[40:43]
v_mfma_f32_16x16x32_bf16 v[36:39],a[152:155],v[72:75],v[36:39]
s_waitcnt lgkmcnt(2)
ds_read_b128 v[68:71],v2 offset:10240
v_mfma_f32_16x16x32_bf16 v[40:43],a[156:159],v[72:75],v[40:43]
s_cbranch_scc1 Lg1_d0w3_%=
s_branch Lg1_slow0_%=
Lg1_slow0_%=:
s_cmp_lt_u32 s66,s59
s_cbranch_scc1 Lg1_gs0_%=
Lg1_slowb0_%=:
s_cmp_eq_u32 s54,1
s_cselect_b32 s68,s49,s48
s_lshr_b32 s69,s68,5
s_cmp_lt_u32 s68,4194304
s_cselect_b32 s68,0xffffcfff,-1
s_and_b32 s68,s68,s69
s_add_u32 m0,s78,8192
s_nop 0
buffer_load_dword v6,s[36:39],s68 offen offset:0 sc1 lds
s_lshr_b32 s73,s47,0
v_fma_f32 v36,-v96,v20,v36
v_fma_f32 v37,-v96,v21,v37
v_fma_f32 v38,-v96,v22,v38
v_fma_f32 v39,-v96,v23,v39
v_mul_f32 v76,v36,v36
v_mul_f32 v77,v37,v37
v_mul_f32 v78,v38,v38
v_mul_f32 v79,v39,v39
v_fma_f32 v76,v98,v76,v97
v_fma_f32 v77,v98,v77,v97
v_fma_f32 v78,v98,v78,v97
v_fma_f32 v79,v98,v79,v97
v_mul_f32 v76,v36,v76
v_mul_f32 v77,v37,v77
v_mul_f32 v78,v38,v78
v_mul_f32 v79,v39,v79
v_exp_f32 v76,v76
v_exp_f32 v77,v77
v_exp_f32 v78,v78
v_exp_f32 v79,v79
v_fma_f32 v76,v76,v99,v99
v_fma_f32 v77,v77,v99,v99
v_fma_f32 v78,v78,v99,v99
v_fma_f32 v79,v79,v99,v99
v_rcp_f32 v76,v76
v_rcp_f32 v77,v77
v_rcp_f32 v78,v78
v_rcp_f32 v79,v79
v_mul_f32 v36,v36,v76
v_mul_f32 v37,v37,v77
v_mul_f32 v38,v38,v78
v_mul_f32 v39,v39,v79
v_cvt_pk_bf16_f32 v84,v36,v37
v_cvt_pk_bf16_f32 v85,v38,v39
s_add_u32 m0,s78,8448
s_nop 0
buffer_load_dword v6,s[36:39],s68 offen offset:0 sc1 lds
v_fma_f32 v40,-v96,v24,v40
v_fma_f32 v41,-v96,v25,v41
v_fma_f32 v42,-v96,v26,v42
v_fma_f32 v43,-v96,v27,v43
v_mul_f32 v80,v40,v40
v_mul_f32 v81,v41,v41
v_mul_f32 v82,v42,v42
v_mul_f32 v83,v43,v43
v_fma_f32 v80,v98,v80,v97
v_fma_f32 v81,v98,v81,v97
v_fma_f32 v82,v98,v82,v97
v_fma_f32 v83,v98,v83,v97)"
R"(
v_mul_f32 v80,v40,v80
v_mul_f32 v81,v41,v81
v_mul_f32 v82,v42,v82
v_mul_f32 v83,v43,v83
v_exp_f32 v80,v80
v_exp_f32 v81,v81
v_exp_f32 v82,v82
v_exp_f32 v83,v83
v_fma_f32 v80,v80,v99,v99
v_fma_f32 v81,v81,v99,v99
v_fma_f32 v82,v82,v99,v99
v_fma_f32 v83,v83,v99,v99
v_rcp_f32 v80,v80
v_rcp_f32 v81,v81
v_rcp_f32 v82,v82
v_rcp_f32 v83,v83
v_mul_f32 v40,v40,v80
v_mul_f32 v41,v41,v81
v_mul_f32 v42,v42,v82
v_mul_f32 v43,v43,v83
v_cvt_pk_bf16_f32 v86,v40,v41
v_cvt_pk_bf16_f32 v87,v42,v43
buffer_store_dwordx4 v[84:87],v3,s[36:39],s73 offen offset:0 sc0 sc1
s_cmp_ge_u32 s48,s59
s_cbranch_scc1 Lg1_done0_%=
s_add_u32 m0,s78,16384
s_nop 0
buffer_load_dword v6,s[36:39],s68 offen offset:0 sc1 lds
s_waitcnt vmcnt(3)
s_barrier
ds_read_b32 v16,v7 offset:8192
s_lshr_b32 s70,s46,5
buffer_store_dword v5,v4,s[36:39],s70 offen offset:0 sc0 sc1
s_mov_b32 s46,s59
s_mov_b32 s69,18432
s_waitcnt lgkmcnt(0)
v_cmp_eq_u32 vcc,s60,v16
s_nop 0
s_cmp_eq_u32 vcc_lo,-1
s_cbranch_scc1 Lg1_go0_%=
s_add_u32 m0,s78,16640
s_nop 0
buffer_load_dword v6,s[36:39],s68 offen offset:0 sc1 lds
s_waitcnt vmcnt(4)
s_barrier
ds_read_b32 v16,v7 offset:8448
s_mov_b32 s69,18688
s_waitcnt lgkmcnt(0)
v_cmp_eq_u32 vcc,s60,v16
s_nop 0
s_cmp_eq_u32 vcc_lo,-1
s_cbranch_scc1 Lg1_go0_%=
s_add_u32 m0,s78,8192
s_nop 0
buffer_load_dword v6,s[36:39],s68 offen offset:0 sc1 lds
s_waitcnt vmcnt(3)
s_barrier
ds_read_b32 v16,v7 offset:16384
s_lshr_b32 s70,s47,5
buffer_store_dword v5,v4,s[36:39],s70 offen offset:0 sc0 sc1
s_mov_b32 s47,s59
s_mov_b32 s69,10240
s_waitcnt lgkmcnt(0)
v_cmp_eq_u32 vcc,s60,v16
s_nop 0
s_cmp_eq_u32 vcc_lo,-1
s_cbranch_scc1 Lg1_go0_%=
s_add_u32 m0,s78,8448
s_nop 0
buffer_load_dword v6,s[36:39],s68 offen offset:0 sc1 lds
s_waitcnt vmcnt(3)
s_barrier
ds_read_b32 v16,v7 offset:16640
s_mov_b32 s69,10496
s_waitcnt lgkmcnt(0)
v_cmp_eq_u32 vcc,s60,v16
s_nop 0
s_cmp_eq_u32 vcc_lo,-1
s_cbranch_scc1 Lg1_go0_%=
Lg1_poll0_%=:
s_add_u32 m0,s78,16384
s_nop 0
buffer_load_dword v6,s[36:39],s68 offen offset:0 sc1 lds
s_waitcnt vmcnt(2)
s_barrier
ds_read_b32 v16,v7 offset:8192
s_mov_b32 s69,18432
s_waitcnt lgkmcnt(0)
v_cmp_eq_u32 vcc,s60,v16
s_nop 0
s_cmp_eq_u32 vcc_lo,-1
s_cbranch_scc1 Lg1_go0_%=
s_sleep 4
s_add_u32 m0,s78,16640
s_nop 0
buffer_load_dword v6,s[36:39],s68 offen offset:0 sc1 lds
s_waitcnt vmcnt(2)
s_barrier
ds_read_b32 v16,v7 offset:8448
s_mov_b32 s69,18688
s_waitcnt lgkmcnt(0)
v_cmp_eq_u32 vcc,s60,v16
s_nop 0
s_cmp_eq_u32 vcc_lo,-1
s_cbranch_scc1 Lg1_go0_%=
s_sleep 4
s_add_u32 m0,s78,8192
s_nop 0
buffer_load_dword v6,s[36:39],s68 offen offset:0 sc1 lds
s_waitcnt vmcnt(2)
s_barrier
ds_read_b32 v16,v7 offset:16384
s_mov_b32 s69,10240
s_waitcnt lgkmcnt(0)
v_cmp_eq_u32 vcc,s60,v16
s_nop 0
s_cmp_eq_u32 vcc_lo,-1
s_cbranch_scc1 Lg1_go0_%=
s_sleep 4
s_add_u32 m0,s78,8448
s_nop 0
buffer_load_dword v6,s[36:39],s68 offen offset:0 sc1 lds
s_waitcnt vmcnt(2)
s_barrier
ds_read_b32 v16,v7 offset:16640
s_mov_b32 s69,10496
s_waitcnt lgkmcnt(0)
v_cmp_eq_u32 vcc,s60,v16
s_nop 0
s_cmp_eq_u32 vcc_lo,-1
s_cbranch_scc1 Lg1_go0_%=
s_sleep 4
s_add_u32 s79,s79,1
s_cmp_lt_u32 s79,20000
s_cbranch_scc1 Lg1_poll0_%=
Lg1_go0_%=:
s_xor_b32 s62,s63,65536
s_mov_b32 m0,s62
s_nop 0
buffer_load_dwordx4 v0,s[36:39],s48 offen offset:0 sc1 lds
buffer_load_dwordx4 v0,s[36:39],s48 offen offset:1024 sc1 lds
buffer_load_dwordx4 v0,s[36:39],s48 offen offset:2048 sc1 lds
buffer_load_dwordx4 v0,s[36:39],s48 offen offset:3072 sc1 lds
s_add_u32 m0,s62,16384
s_nop 0
buffer_load_dwordx4 v1,s[36:39],s48 offen offset:0 sc1 lds
buffer_load_dwordx4 v1,s[36:39],s48 offen offset:1024 sc1 lds
buffer_load_dwordx4 v1,s[36:39],s48 offen offset:2048 sc1 lds
buffer_load_dwordx4 v1,s[36:39],s48 offen offset:3072 sc1 lds
s_lshr_b32 s72,s48,6
s_lshr_b32 s71,s62,2
s_cmp_lt_u32 s48,4194304
s_cselect_b32 s72,s59,s72
s_add_u32 m0,s71,s88
s_nop 0
buffer_load_dword v10,s[36:39],s72 offen offset:0 sc1 lds
s_cmp_eq_u32 vcc_hi,-1
s_cselect_b32 s66,s49,s59
s_xor_b32 s62,s64,65536
s_cmp_ge_u32 s66,s59
s_cselect_b32 s68,s49,s50
s_lshr_b32 s69,s68,5
s_cmp_lt_u32 s68,4194304
s_cselect_b32 s68,0xffffcfff,-1
s_and_b32 s68,s68,s69
s_add_u32 m0,s78,0
s_nop 0
buffer_load_dword v6,s[36:39],s68 offen offset:0 sc1 lds
s_mov_b32 m0,s62
s_nop 0
buffer_load_dwordx4 v0,s[36:39],s66 offen offset:0 sc1 lds
buffer_load_dwordx4 v0,s[36:39],s66 offen offset:1024 sc1 lds
buffer_load_dwordx4 v0,s[36:39],s66 offen offset:2048 sc1 lds
buffer_load_dwordx4 v0,s[36:39],s66 offen offset:3072 sc1 lds
s_waitcnt vmcnt(10)
s_barrier
s_lshl_b32 s70,s54,12
v_add_u32 v76,s70,v2
v_xor_b32 v77,4096,v76
ds_read_b128 v[44:47],v76 offset:0
ds_read_b128 v[48:51],v76 offset:1024
ds_read_b128 v[52:55],v76 offset:2048
ds_read_b128 v[56:59],v76 offset:3072
ds_read_b128 v[60:63],v77 offset:0
ds_read_b128 v[64:67],v77 offset:1024
ds_read_b128 v[68:71],v77 offset:2048
s_lshr_b32 s70,s46,5
buffer_store_dword v5,v4,s[36:39],s70 offen offset:0 sc0 sc1
s_mov_b32 s46,s59
s_lshr_b32 s70,s47,5
buffer_store_dword v5,v4,s[36:39],s70 offen offset:0 sc0 sc1
s_mov_b32 s47,s59
s_lshr_b32 s70,s59,5
buffer_store_dword v5,v4,s[36:39],s70 offen offset:0 sc0 sc1
s_waitcnt lgkmcnt(3)
s_setpc_b64 s[90:91]
Lg1_gs0_%=:
s_cmp_eq_u32 s54,1
s_cselect_b32 s68,s49,s48
s_lshr_b32 s69,s68,5)"
R"(
s_cmp_lt_u32 s68,4194304
s_cselect_b32 s68,0xffffcfff,-1
s_and_b32 s68,s68,s69
s_lshr_b32 s73,s47,0
s_add_u32 m0,s78,8192
s_nop 0
buffer_load_dword v6,s[36:39],s68 offen offset:0 sc1 lds
v_fma_f32 v36,-v96,v20,v36
v_fma_f32 v37,-v96,v21,v37
v_fma_f32 v38,-v96,v22,v38
v_fma_f32 v39,-v96,v23,v39
v_mul_f32 v76,v36,v36
v_mul_f32 v77,v37,v37
v_mul_f32 v78,v38,v38
v_mul_f32 v79,v39,v39
v_fma_f32 v76,v98,v76,v97
v_fma_f32 v77,v98,v77,v97
v_fma_f32 v78,v98,v78,v97
s_add_u32 m0,s62,16384
s_nop 0
buffer_load_dwordx4 v1,s[36:39],s66 offen offset:0 sc1 lds
v_fma_f32 v79,v98,v79,v97
v_mul_f32 v76,v36,v76
v_mul_f32 v77,v37,v77
v_mul_f32 v78,v38,v78
v_mul_f32 v79,v39,v79
v_exp_f32 v76,v76
v_exp_f32 v77,v77
v_exp_f32 v78,v78
v_exp_f32 v79,v79
v_fma_f32 v76,v76,v99,v99
v_fma_f32 v77,v77,v99,v99
buffer_load_dwordx4 v1,s[36:39],s66 offen offset:1024 sc1 lds
v_fma_f32 v78,v78,v99,v99
v_fma_f32 v79,v79,v99,v99
v_rcp_f32 v76,v76
v_rcp_f32 v77,v77
v_rcp_f32 v78,v78
v_rcp_f32 v79,v79
v_mul_f32 v36,v36,v76
v_mul_f32 v37,v37,v77
v_mul_f32 v38,v38,v78
v_mul_f32 v39,v39,v79
v_cvt_pk_bf16_f32 v84,v36,v37
buffer_load_dwordx4 v1,s[36:39],s66 offen offset:2048 sc1 lds
v_cvt_pk_bf16_f32 v85,v38,v39
v_fma_f32 v40,-v96,v24,v40
v_fma_f32 v41,-v96,v25,v41
v_fma_f32 v42,-v96,v26,v42
v_fma_f32 v43,-v96,v27,v43
v_mul_f32 v80,v40,v40
v_mul_f32 v81,v41,v41
v_mul_f32 v82,v42,v42
v_mul_f32 v83,v43,v43
v_fma_f32 v80,v98,v80,v97
v_fma_f32 v81,v98,v81,v97
buffer_load_dwordx4 v1,s[36:39],s66 offen offset:3072 sc1 lds
v_fma_f32 v82,v98,v82,v97
v_fma_f32 v83,v98,v83,v97
v_mul_f32 v80,v40,v80
v_mul_f32 v81,v41,v81
v_mul_f32 v82,v42,v82
v_mul_f32 v83,v43,v83
v_exp_f32 v80,v80
v_exp_f32 v81,v81
v_exp_f32 v82,v82
v_exp_f32 v83,v83
v_fma_f32 v80,v80,v99,v99
s_lshr_b32 s72,s66,6
s_lshr_b32 s71,s62,2
s_cmp_lt_u32 s66,4194304
s_cselect_b32 s72,s59,s72
s_add_u32 m0,s71,s88
s_nop 0
buffer_load_dword v10,s[36:39],s72 offen offset:0 sc1 lds
v_fma_f32 v81,v81,v99,v99
v_fma_f32 v82,v82,v99,v99
v_fma_f32 v83,v83,v99,v99
v_rcp_f32 v80,v80
v_rcp_f32 v81,v81
v_rcp_f32 v82,v82
v_rcp_f32 v83,v83
v_mul_f32 v40,v40,v80
v_mul_f32 v41,v41,v81
v_mul_f32 v42,v42,v82
v_mul_f32 v43,v43,v83
v_cvt_pk_bf16_f32 v86,v40,v41
v_cvt_pk_bf16_f32 v87,v42,v43
buffer_store_dwordx4 v[84:87],v3,s[36:39],s73 offen offset:0 sc0 sc1
s_waitcnt vmcnt(6)
s_barrier
ds_read_b32 v16,v7 offset:8192
s_lshl_b32 s70,s54,12
v_add_u32 v76,s70,v2
v_xor_b32 v77,4096,v76
ds_read_b128 v[44:47],v76 offset:0
ds_read_b128 v[48:51],v76 offset:1024
ds_read_b128 v[52:55],v76 offset:2048
ds_read_b128 v[56:59],v76 offset:3072
ds_read_b128 v[60:63],v77 offset:0
ds_read_b128 v[64:67],v77 offset:1024
ds_read_b128 v[68:71],v77 offset:2048
s_lshr_b32 s70,s46,5
buffer_store_dword v5,v4,s[36:39],s70 offen offset:0 sc0 sc1
s_waitcnt lgkmcnt(7)
v_cmp_eq_u32 vcc,s60,v16
s_nop 0
s_cmp_eq_u32 vcc_hi,-1
s_cselect_b32 s66,s49,s59
s_xor_b32 s62,s64,65536
s_cmp_ge_u32 s66,s59
s_cselect_b32 s68,s49,s50
s_lshr_b32 s69,s68,5
s_cmp_lt_u32 s68,4194304
s_cselect_b32 s68,0xffffcfff,-1
s_and_b32 s68,s68,s69
s_add_u32 m0,s78,0
s_nop 0
buffer_load_dword v6,s[36:39],s68 offen offset:0 sc1 lds
s_mov_b32 m0,s62
s_nop 0
buffer_load_dwordx4 v0,s[36:39],s66 offen offset:0 sc1 lds
buffer_load_dwordx4 v0,s[36:39],s66 offen offset:1024 sc1 lds
buffer_load_dwordx4 v0,s[36:39],s66 offen offset:2048 sc1 lds
buffer_load_dwordx4 v0,s[36:39],s66 offen offset:3072 sc1 lds
s_mov_b32 s46,s47
s_mov_b32 s47,s59
s_lshr_b32 s70,s59,5
buffer_store_dword v5,v4,s[36:39],s70 offen offset:0 sc0 sc1
s_waitcnt lgkmcnt(3)
s_setpc_b64 s[90:91]
Lg1_done0_%=:
s_waitcnt vmcnt(0)
s_barrier
s_lshr_b32 s70,s46,5
buffer_store_dword v5,v4,s[36:39],s70 offen offset:0 sc0 sc1
s_lshr_b32 s70,s47,5
buffer_store_dword v5,v4,s[36:39],s70 offen offset:0 sc0 sc1
s_branch Lg1_done_%=
Lg1_slow1_%=:
s_cmp_lt_u32 s67,s59
s_cbranch_scc1 Lg1_gs1_%=
Lg1_slowb1_%=:
s_cmp_eq_u32 s54,1
s_cselect_b32 s68,s50,s49
s_lshr_b32 s69,s68,5
s_cmp_lt_u32 s68,4194304
s_cselect_b32 s68,0xffffcfff,-1
s_and_b32 s68,s68,s69
s_add_u32 m0,s78,8192
s_nop 0
buffer_load_dword v6,s[36:39],s68 offen offset:0 sc1 lds
s_lshr_b32 s73,s48,0
v_fma_f32 v28,-v92,v20,v28
v_fma_f32 v29,-v92,v21,v29
v_fma_f32 v30,-v92,v22,v30
v_fma_f32 v31,-v92,v23,v31
v_mul_f32 v76,v28,v28
v_mul_f32 v77,v29,v29
v_mul_f32 v78,v30,v30
v_mul_f32 v79,v31,v31
v_fma_f32 v76,v94,v76,v93
v_fma_f32 v77,v94,v77,v93
v_fma_f32 v78,v94,v78,v93
v_fma_f32 v79,v94,v79,v93
v_mul_f32 v76,v28,v76
v_mul_f32 v77,v29,v77
v_mul_f32 v78,v30,v78
v_mul_f32 v79,v31,v79
v_exp_f32 v76,v76
v_exp_f32 v77,v77
v_exp_f32 v78,v78
v_exp_f32 v79,v79
v_fma_f32 v76,v76,v95,v95
v_fma_f32 v77,v77,v95,v95
v_fma_f32 v78,v78,v95,v95
v_fma_f32 v79,v79,v95,v95
v_rcp_f32 v76,v76
v_rcp_f32 v77,v77
v_rcp_f32 v78,v78
v_rcp_f32 v79,v79
v_mul_f32 v28,v28,v76
v_mul_f32 v29,v29,v77
v_mul_f32 v30,v30,v78
v_mul_f32 v31,v31,v79
v_cvt_pk_bf16_f32 v84,v28,v29
v_cvt_pk_bf16_f32 v85,v30,v31
s_add_u32 m0,s78,8448
s_nop 0
buffer_load_dword v6,s[36:39],s68 offen offset:0 sc1 lds
v_fma_f32 v32,-v92,v24,v32
v_fma_f32 v33,-v92,v25,v33
v_fma_f32 v34,-v92,v26,v34
v_fma_f32 v35,-v92,v27,v35
v_mul_f32 v80,v32,v32
v_mul_f32 v81,v33,v33
v_mul_f32 v82,v34,v34
v_mul_f32 v83,v35,v35
v_fma_f32 v80,v94,v80,v93
v_fma_f32 v81,v94,v81,v93)"
R"(
v_fma_f32 v82,v94,v82,v93
v_fma_f32 v83,v94,v83,v93
v_mul_f32 v80,v32,v80
v_mul_f32 v81,v33,v81
v_mul_f32 v82,v34,v82
v_mul_f32 v83,v35,v83
v_exp_f32 v80,v80
v_exp_f32 v81,v81
v_exp_f32 v82,v82
v_exp_f32 v83,v83
v_fma_f32 v80,v80,v95,v95
v_fma_f32 v81,v81,v95,v95
v_fma_f32 v82,v82,v95,v95
v_fma_f32 v83,v83,v95,v95
v_rcp_f32 v80,v80
v_rcp_f32 v81,v81
v_rcp_f32 v82,v82
v_rcp_f32 v83,v83
v_mul_f32 v32,v32,v80
v_mul_f32 v33,v33,v81
v_mul_f32 v34,v34,v82
v_mul_f32 v35,v35,v83
v_cvt_pk_bf16_f32 v86,v32,v33
v_cvt_pk_bf16_f32 v87,v34,v35
buffer_store_dwordx4 v[84:87],v3,s[36:39],s73 offen offset:0 sc0 sc1
s_cmp_ge_u32 s49,s59
s_cbranch_scc1 Lg1_done1_%=
s_add_u32 m0,s78,16384
s_nop 0
buffer_load_dword v6,s[36:39],s68 offen offset:0 sc1 lds
s_waitcnt vmcnt(3)
s_barrier
ds_read_b32 v16,v7 offset:8192
s_lshr_b32 s70,s47,5
buffer_store_dword v5,v4,s[36:39],s70 offen offset:0 sc0 sc1
s_mov_b32 s47,s59
s_mov_b32 s69,18432
s_waitcnt lgkmcnt(0)
v_cmp_eq_u32 vcc,s60,v16
s_nop 0
s_cmp_eq_u32 vcc_lo,-1
s_cbranch_scc1 Lg1_go1_%=
s_add_u32 m0,s78,16640
s_nop 0
buffer_load_dword v6,s[36:39],s68 offen offset:0 sc1 lds
s_waitcnt vmcnt(4)
s_barrier
ds_read_b32 v16,v7 offset:8448
s_mov_b32 s69,18688
s_waitcnt lgkmcnt(0)
v_cmp_eq_u32 vcc,s60,v16
s_nop 0
s_cmp_eq_u32 vcc_lo,-1
s_cbranch_scc1 Lg1_go1_%=
s_add_u32 m0,s78,8192
s_nop 0
buffer_load_dword v6,s[36:39],s68 offen offset:0 sc1 lds
s_waitcnt vmcnt(3)
s_barrier
ds_read_b32 v16,v7 offset:16384
s_lshr_b32 s70,s48,5
buffer_store_dword v5,v4,s[36:39],s70 offen offset:0 sc0 sc1
s_mov_b32 s48,s59
s_mov_b32 s69,10240
s_waitcnt lgkmcnt(0)
v_cmp_eq_u32 vcc,s60,v16
s_nop 0
s_cmp_eq_u32 vcc_lo,-1
s_cbranch_scc1 Lg1_go1_%=
s_add_u32 m0,s78,8448
s_nop 0
buffer_load_dword v6,s[36:39],s68 offen offset:0 sc1 lds
s_waitcnt vmcnt(3)
s_barrier
ds_read_b32 v16,v7 offset:16640
s_mov_b32 s69,10496
s_waitcnt lgkmcnt(0)
v_cmp_eq_u32 vcc,s60,v16
s_nop 0
s_cmp_eq_u32 vcc_lo,-1
s_cbranch_scc1 Lg1_go1_%=
Lg1_poll1_%=:
s_add_u32 m0,s78,16384
s_nop 0
buffer_load_dword v6,s[36:39],s68 offen offset:0 sc1 lds
s_waitcnt vmcnt(2)
s_barrier
ds_read_b32 v16,v7 offset:8192
s_mov_b32 s69,18432
s_waitcnt lgkmcnt(0)
v_cmp_eq_u32 vcc,s60,v16
s_nop 0
s_cmp_eq_u32 vcc_lo,-1
s_cbranch_scc1 Lg1_go1_%=
s_sleep 4
s_add_u32 m0,s78,16640
s_nop 0
buffer_load_dword v6,s[36:39],s68 offen offset:0 sc1 lds
s_waitcnt vmcnt(2)
s_barrier
ds_read_b32 v16,v7 offset:8448
s_mov_b32 s69,18688
s_waitcnt lgkmcnt(0)
v_cmp_eq_u32 vcc,s60,v16
s_nop 0
s_cmp_eq_u32 vcc_lo,-1
s_cbranch_scc1 Lg1_go1_%=
s_sleep 4
s_add_u32 m0,s78,8192
s_nop 0
buffer_load_dword v6,s[36:39],s68 offen offset:0 sc1 lds
s_waitcnt vmcnt(2)
s_barrier
ds_read_b32 v16,v7 offset:16384
s_mov_b32 s69,10240
s_waitcnt lgkmcnt(0)
v_cmp_eq_u32 vcc,s60,v16
s_nop 0
s_cmp_eq_u32 vcc_lo,-1
s_cbranch_scc1 Lg1_go1_%=
s_sleep 4
s_add_u32 m0,s78,8448
s_nop 0
buffer_load_dword v6,s[36:39],s68 offen offset:0 sc1 lds
s_waitcnt vmcnt(2)
s_barrier
ds_read_b32 v16,v7 offset:16640
s_mov_b32 s69,10496
s_waitcnt lgkmcnt(0)
v_cmp_eq_u32 vcc,s60,v16
s_nop 0
s_cmp_eq_u32 vcc_lo,-1
s_cbranch_scc1 Lg1_go1_%=
s_sleep 4
s_add_u32 s79,s79,1
s_cmp_lt_u32 s79,20000
s_cbranch_scc1 Lg1_poll1_%=
Lg1_go1_%=:
s_xor_b32 s62,s64,65536
s_mov_b32 m0,s62
s_nop 0
buffer_load_dwordx4 v0,s[36:39],s49 offen offset:0 sc1 lds
buffer_load_dwordx4 v0,s[36:39],s49 offen offset:1024 sc1 lds
buffer_load_dwordx4 v0,s[36:39],s49 offen offset:2048 sc1 lds
buffer_load_dwordx4 v0,s[36:39],s49 offen offset:3072 sc1 lds
s_add_u32 m0,s62,16384
s_nop 0
buffer_load_dwordx4 v1,s[36:39],s49 offen offset:0 sc1 lds
buffer_load_dwordx4 v1,s[36:39],s49 offen offset:1024 sc1 lds
buffer_load_dwordx4 v1,s[36:39],s49 offen offset:2048 sc1 lds
buffer_load_dwordx4 v1,s[36:39],s49 offen offset:3072 sc1 lds
s_lshr_b32 s72,s49,6
s_lshr_b32 s71,s62,2
s_cmp_lt_u32 s49,4194304
s_cselect_b32 s72,s59,s72
s_add_u32 m0,s71,s88
s_nop 0
buffer_load_dword v10,s[36:39],s72 offen offset:0 sc1 lds
s_cmp_eq_u32 vcc_hi,-1
s_cselect_b32 s67,s50,s59
s_xor_b32 s62,s63,65536
s_cmp_ge_u32 s67,s59
s_cselect_b32 s68,s50,s51
s_lshr_b32 s69,s68,5
s_cmp_lt_u32 s68,4194304
s_cselect_b32 s68,0xffffcfff,-1
s_and_b32 s68,s68,s69
s_add_u32 m0,s78,256
s_nop 0
buffer_load_dword v6,s[36:39],s68 offen offset:0 sc1 lds
s_mov_b32 m0,s62
s_nop 0
buffer_load_dwordx4 v0,s[36:39],s67 offen offset:0 sc1 lds
buffer_load_dwordx4 v0,s[36:39],s67 offen offset:1024 sc1 lds
buffer_load_dwordx4 v0,s[36:39],s67 offen offset:2048 sc1 lds
buffer_load_dwordx4 v0,s[36:39],s67 offen offset:3072 sc1 lds
s_waitcnt vmcnt(10)
s_barrier
s_lshl_b32 s70,s54,12
v_add_u32 v76,s70,v2
v_xor_b32 v77,4096,v76
ds_read_b128 v[44:47],v76 offset:32768
ds_read_b128 v[48:51],v76 offset:33792
ds_read_b128 v[52:55],v76 offset:34816
ds_read_b128 v[56:59],v76 offset:35840
ds_read_b128 v[60:63],v77 offset:32768
ds_read_b128 v[64:67],v77 offset:33792
ds_read_b128 v[68:71],v77 offset:34816
s_lshr_b32 s70,s47,5
buffer_store_dword v5,v4,s[36:39],s70 offen offset:0 sc0 sc1
s_mov_b32 s47,s59
s_lshr_b32 s70,s48,5
buffer_store_dword v5,v4,s[36:39],s70 offen offset:0 sc0 sc1
s_mov_b32 s48,s59
s_lshr_b32 s70,s59,5
buffer_store_dword v5,v4,s[36:39],s70 offen offset:0 sc0 sc1
s_waitcnt lgkmcnt(3)
s_setpc_b64 s[92:93]
Lg1_gs1_%=:
s_cmp_eq_u32 s54,1)"
R"(
s_cselect_b32 s68,s50,s49
s_lshr_b32 s69,s68,5
s_cmp_lt_u32 s68,4194304
s_cselect_b32 s68,0xffffcfff,-1
s_and_b32 s68,s68,s69
s_lshr_b32 s73,s48,0
s_add_u32 m0,s78,8192
s_nop 0
buffer_load_dword v6,s[36:39],s68 offen offset:0 sc1 lds
v_fma_f32 v28,-v92,v20,v28
v_fma_f32 v29,-v92,v21,v29
v_fma_f32 v30,-v92,v22,v30
v_fma_f32 v31,-v92,v23,v31
v_mul_f32 v76,v28,v28
v_mul_f32 v77,v29,v29
v_mul_f32 v78,v30,v30
v_mul_f32 v79,v31,v31
v_fma_f32 v76,v94,v76,v93
v_fma_f32 v77,v94,v77,v93
v_fma_f32 v78,v94,v78,v93
s_add_u32 m0,s62,16384
s_nop 0
buffer_load_dwordx4 v1,s[36:39],s67 offen offset:0 sc1 lds
v_fma_f32 v79,v94,v79,v93
v_mul_f32 v76,v28,v76
v_mul_f32 v77,v29,v77
v_mul_f32 v78,v30,v78
v_mul_f32 v79,v31,v79
v_exp_f32 v76,v76
v_exp_f32 v77,v77
v_exp_f32 v78,v78
v_exp_f32 v79,v79
v_fma_f32 v76,v76,v95,v95
v_fma_f32 v77,v77,v95,v95
buffer_load_dwordx4 v1,s[36:39],s67 offen offset:1024 sc1 lds
v_fma_f32 v78,v78,v95,v95
v_fma_f32 v79,v79,v95,v95
v_rcp_f32 v76,v76
v_rcp_f32 v77,v77
v_rcp_f32 v78,v78
v_rcp_f32 v79,v79
v_mul_f32 v28,v28,v76
v_mul_f32 v29,v29,v77
v_mul_f32 v30,v30,v78
v_mul_f32 v31,v31,v79
v_cvt_pk_bf16_f32 v84,v28,v29
buffer_load_dwordx4 v1,s[36:39],s67 offen offset:2048 sc1 lds
v_cvt_pk_bf16_f32 v85,v30,v31
v_fma_f32 v32,-v92,v24,v32
v_fma_f32 v33,-v92,v25,v33
v_fma_f32 v34,-v92,v26,v34
v_fma_f32 v35,-v92,v27,v35
v_mul_f32 v80,v32,v32
v_mul_f32 v81,v33,v33
v_mul_f32 v82,v34,v34
v_mul_f32 v83,v35,v35
v_fma_f32 v80,v94,v80,v93
v_fma_f32 v81,v94,v81,v93
buffer_load_dwordx4 v1,s[36:39],s67 offen offset:3072 sc1 lds
v_fma_f32 v82,v94,v82,v93
v_fma_f32 v83,v94,v83,v93
v_mul_f32 v80,v32,v80
v_mul_f32 v81,v33,v81
v_mul_f32 v82,v34,v82
v_mul_f32 v83,v35,v83
v_exp_f32 v80,v80
v_exp_f32 v81,v81
v_exp_f32 v82,v82
v_exp_f32 v83,v83
v_fma_f32 v80,v80,v95,v95
s_lshr_b32 s72,s67,6
s_lshr_b32 s71,s62,2
s_cmp_lt_u32 s67,4194304
s_cselect_b32 s72,s59,s72
s_add_u32 m0,s71,s88
s_nop 0
buffer_load_dword v10,s[36:39],s72 offen offset:0 sc1 lds
v_fma_f32 v81,v81,v95,v95
v_fma_f32 v82,v82,v95,v95
v_fma_f32 v83,v83,v95,v95
v_rcp_f32 v80,v80
v_rcp_f32 v81,v81
v_rcp_f32 v82,v82
v_rcp_f32 v83,v83
v_mul_f32 v32,v32,v80
v_mul_f32 v33,v33,v81
v_mul_f32 v34,v34,v82
v_mul_f32 v35,v35,v83
v_cvt_pk_bf16_f32 v86,v32,v33
v_cvt_pk_bf16_f32 v87,v34,v35
buffer_store_dwordx4 v[84:87],v3,s[36:39],s73 offen offset:0 sc0 sc1
s_waitcnt vmcnt(6)
s_barrier
ds_read_b32 v16,v7 offset:8192
s_lshl_b32 s70,s54,12
v_add_u32 v76,s70,v2
v_xor_b32 v77,4096,v76
ds_read_b128 v[44:47],v76 offset:32768
ds_read_b128 v[48:51],v76 offset:33792
ds_read_b128 v[52:55],v76 offset:34816
ds_read_b128 v[56:59],v76 offset:35840
ds_read_b128 v[60:63],v77 offset:32768
ds_read_b128 v[64:67],v77 offset:33792
ds_read_b128 v[68:71],v77 offset:34816
s_lshr_b32 s70,s47,5
buffer_store_dword v5,v4,s[36:39],s70 offen offset:0 sc0 sc1
s_waitcnt lgkmcnt(7)
v_cmp_eq_u32 vcc,s60,v16
s_nop 0
s_cmp_eq_u32 vcc_hi,-1
s_cselect_b32 s67,s50,s59
s_xor_b32 s62,s63,65536
s_cmp_ge_u32 s67,s59
s_cselect_b32 s68,s50,s51
s_lshr_b32 s69,s68,5
s_cmp_lt_u32 s68,4194304
s_cselect_b32 s68,0xffffcfff,-1
s_and_b32 s68,s68,s69
s_add_u32 m0,s78,256
s_nop 0
buffer_load_dword v6,s[36:39],s68 offen offset:0 sc1 lds
s_mov_b32 m0,s62
s_nop 0
buffer_load_dwordx4 v0,s[36:39],s67 offen offset:0 sc1 lds
buffer_load_dwordx4 v0,s[36:39],s67 offen offset:1024 sc1 lds
buffer_load_dwordx4 v0,s[36:39],s67 offen offset:2048 sc1 lds
buffer_load_dwordx4 v0,s[36:39],s67 offen offset:3072 sc1 lds
s_mov_b32 s47,s48
s_mov_b32 s48,s59
s_lshr_b32 s70,s59,5
buffer_store_dword v5,v4,s[36:39],s70 offen offset:0 sc0 sc1
s_waitcnt lgkmcnt(3)
s_setpc_b64 s[92:93]
Lg1_done1_%=:
s_waitcnt vmcnt(0)
s_barrier
s_lshr_b32 s70,s47,5
buffer_store_dword v5,v4,s[36:39],s70 offen offset:0 sc0 sc1
s_lshr_b32 s70,s48,5
buffer_store_dword v5,v4,s[36:39],s70 offen offset:0 sc0 sc1
s_branch Lg1_done_%=
Lg1_done_%=:
s_waitcnt vmcnt(0) lgkmcnt(0)
s_barrier
s_branch Lg2_end_%=
Lg2_begin_%=:
Lcode_g2_b:
s_mov_b64 s[36:37],%1
s_mov_b32 s38,122727552
s_mov_b32 s39,0x00020000
s_mov_b32 s54,%2
s_mov_b32 s55,%3
s_mov_b32 s56,%4
s_mov_b32 s58,%5
s_mov_b32 s83,%6
s_mov_b32 s82,%7
s_mov_b32 s84,%8
v_mov_b32 v0,%9
v_mov_b32 v1,%10
v_mov_b32 v2,%11
v_mov_b32 v3,%12
v_mov_b32 v4,%13
v_mov_b32 v6,%14
v_mov_b32 v7,%15
v_mov_b32 v8,%16
v_mov_b32 v9,%17
v_mov_b32 v10,%18
v_mov_b32 v11,%19
v_mov_b32 v12,%20
v_mov_b32 v13,%21
v_mov_b32 v14,%22
v_mov_b32 v16,%23
v_mov_b32 v17,%24
v_mov_b32 v18,%25
v_mov_b32 v19,%26
v_mov_b32 v20,%27
s_mov_b64 s[40:41],%28
s_mov_b32 s42,2097152
s_mov_b32 s43,0x00020000
s_lshl_b32 s88,s54,11
s_mov_b32 s69,122727424
buffer_load_dword v5,off,s[36:39],s69 sc1
s_add_u32 m0,s88,0
s_add_u32 s61,s82,0
buffer_load_dwordx4 v16,s[40:43],s61 offen offset:0  lds
s_add_u32 m0,s88,1024
s_nop 0
buffer_load_dwordx4 v17,s[40:43],s61 offen offset:0  lds
s_add_u32 m0,s88,8064
s_nop 0
buffer_load_dwordx4 v16,s[40:43],s61 offen offset:128  lds
s_add_u32 m0,s88,9088
s_nop 0
buffer_load_dwordx4 v17,s[40:43],s61 offen offset:128  lds
s_add_u32 m0,s88,16384
s_add_u32 s61,s82,131072
buffer_load_dwordx4 v16,s[40:43],s61 offen offset:0  lds
s_add_u32 m0,s88,17408
s_nop 0
buffer_load_dwordx4 v17,s[40:43],s61 offen offset:0  lds)"
R"(
s_add_u32 m0,s88,24448
s_nop 0
buffer_load_dwordx4 v16,s[40:43],s61 offen offset:128  lds
s_add_u32 m0,s88,25472
s_nop 0
buffer_load_dwordx4 v17,s[40:43],s61 offen offset:128  lds
s_add_u32 m0,s88,32768
s_add_u32 s61,s82,262144
buffer_load_dwordx4 v16,s[40:43],s61 offen offset:0  lds
s_add_u32 m0,s88,33792
s_nop 0
buffer_load_dwordx4 v17,s[40:43],s61 offen offset:0  lds
s_add_u32 m0,s88,40832
s_nop 0
buffer_load_dwordx4 v16,s[40:43],s61 offen offset:128  lds
s_add_u32 m0,s88,41856
s_nop 0
buffer_load_dwordx4 v17,s[40:43],s61 offen offset:128  lds
s_add_u32 m0,s88,49152
s_add_u32 s61,s82,393216
buffer_load_dwordx4 v16,s[40:43],s61 offen offset:0  lds
s_add_u32 m0,s88,50176
s_nop 0
buffer_load_dwordx4 v17,s[40:43],s61 offen offset:0  lds
s_add_u32 m0,s88,57216
s_nop 0
buffer_load_dwordx4 v16,s[40:43],s61 offen offset:128  lds
s_add_u32 m0,s88,58240
s_nop 0
buffer_load_dwordx4 v17,s[40:43],s61 offen offset:128  lds
s_add_u32 m0,s88,65536
s_add_u32 s61,s82,524288
buffer_load_dwordx4 v16,s[40:43],s61 offen offset:0  lds
s_add_u32 m0,s88,66560
s_nop 0
buffer_load_dwordx4 v17,s[40:43],s61 offen offset:0  lds
s_add_u32 m0,s88,73600
s_nop 0
buffer_load_dwordx4 v16,s[40:43],s61 offen offset:128  lds
s_add_u32 m0,s88,74624
s_nop 0
buffer_load_dwordx4 v17,s[40:43],s61 offen offset:128  lds
s_add_u32 m0,s88,81920
s_add_u32 s61,s82,655360
buffer_load_dwordx4 v16,s[40:43],s61 offen offset:0  lds
s_add_u32 m0,s88,82944
s_nop 0
buffer_load_dwordx4 v17,s[40:43],s61 offen offset:0  lds
s_add_u32 m0,s88,89984
s_nop 0
buffer_load_dwordx4 v16,s[40:43],s61 offen offset:128  lds
s_add_u32 m0,s88,91008
s_nop 0
buffer_load_dwordx4 v17,s[40:43],s61 offen offset:128  lds
s_add_u32 m0,s88,98304
s_add_u32 s61,s82,786432
buffer_load_dwordx4 v16,s[40:43],s61 offen offset:0  lds
s_add_u32 m0,s88,99328
s_nop 0
buffer_load_dwordx4 v17,s[40:43],s61 offen offset:0  lds
s_add_u32 m0,s88,106368
s_nop 0
buffer_load_dwordx4 v16,s[40:43],s61 offen offset:128  lds
s_add_u32 m0,s88,107392
s_nop 0
buffer_load_dwordx4 v17,s[40:43],s61 offen offset:128  lds
v_add_u32 v24,49152,v18
v_add_u32 v25,49152,v19
v_add_u32 v26,98304,v18
v_add_u32 v27,98304,v19
s_waitcnt vmcnt(24)
s_barrier
s_add_u32 m0,s88,114688
s_add_u32 s61,s82,917504
buffer_load_dwordx4 v16,s[40:43],s61 offen offset:0  lds
s_add_u32 m0,s88,115712
ds_read_b64_tr_b16 a[0:1],v18 offset:0
buffer_load_dwordx4 v17,s[40:43],s61 offen offset:0  lds
s_add_u32 m0,s88,122752
ds_read_b64_tr_b16 a[2:3],v18 offset:512
buffer_load_dwordx4 v16,s[40:43],s61 offen offset:128  lds
s_add_u32 m0,s88,123776
ds_read_b64_tr_b16 a[4:5],v19 offset:0
buffer_load_dwordx4 v17,s[40:43],s61 offen offset:128  lds
ds_read_b64_tr_b16 a[6:7],v19 offset:512
ds_read_b64_tr_b16 a[8:9],v18 offset:4096
ds_read_b64_tr_b16 a[10:11],v18 offset:4608
ds_read_b64_tr_b16 a[12:13],v19 offset:4096
ds_read_b64_tr_b16 a[14:15],v19 offset:4608
s_waitcnt vmcnt(24)
s_barrier
s_add_u32 m0,s88,131072
s_add_u32 s61,s82,1048576
buffer_load_dwordx4 v16,s[40:43],s61 offen offset:0  lds
s_add_u32 m0,s88,132096
ds_read_b64_tr_b16 a[16:17],v18 offset:16384
buffer_load_dwordx4 v17,s[40:43],s61 offen offset:0  lds
s_add_u32 m0,s88,139136
ds_read_b64_tr_b16 a[18:19],v18 offset:16896
buffer_load_dwordx4 v16,s[40:43],s61 offen offset:128  lds
s_add_u32 m0,s88,140160
ds_read_b64_tr_b16 a[20:21],v19 offset:16384
buffer_load_dwordx4 v17,s[40:43],s61 offen offset:128  lds
ds_read_b64_tr_b16 a[22:23],v19 offset:16896
ds_read_b64_tr_b16 a[24:25],v18 offset:20480
ds_read_b64_tr_b16 a[26:27],v18 offset:20992
ds_read_b64_tr_b16 a[28:29],v19 offset:20480
ds_read_b64_tr_b16 a[30:31],v19 offset:20992
s_waitcnt lgkmcnt(8)
s_waitcnt vmcnt(24)
s_barrier
s_add_u32 m0,s88,0
s_add_u32 s61,s82,1179648
buffer_load_dwordx4 v16,s[40:43],s61 offen offset:0  lds
s_add_u32 m0,s88,1024
ds_read_b64_tr_b16 a[32:33],v18 offset:32768
buffer_load_dwordx4 v17,s[40:43],s61 offen offset:0  lds
s_add_u32 m0,s88,8064
ds_read_b64_tr_b16 a[34:35],v18 offset:33280
buffer_load_dwordx4 v16,s[40:43],s61 offen offset:128  lds
s_add_u32 m0,s88,9088
ds_read_b64_tr_b16 a[36:37],v19 offset:32768
buffer_load_dwordx4 v17,s[40:43],s61 offen offset:128  lds
ds_read_b64_tr_b16 a[38:39],v19 offset:33280
ds_read_b64_tr_b16 a[40:41],v18 offset:36864
ds_read_b64_tr_b16 a[42:43],v18 offset:37376
ds_read_b64_tr_b16 a[44:45],v19 offset:36864
ds_read_b64_tr_b16 a[46:47],v19 offset:37376
s_waitcnt lgkmcnt(8)
s_waitcnt vmcnt(24)
s_barrier
s_add_u32 m0,s88,16384
s_add_u32 s61,s82,1310720
buffer_load_dwordx4 v16,s[40:43],s61 offen offset:0  lds
s_add_u32 m0,s88,17408
ds_read_b64_tr_b16 a[48:49],v24 offset:0
buffer_load_dwordx4 v17,s[40:43],s61 offen offset:0  lds
s_add_u32 m0,s88,24448
ds_read_b64_tr_b16 a[50:51],v24 offset:512
buffer_load_dwordx4 v16,s[40:43],s61 offen offset:128  lds
s_add_u32 m0,s88,25472
ds_read_b64_tr_b16 a[52:53],v25 offset:0
buffer_load_dwordx4 v17,s[40:43],s61 offen offset:128  lds
ds_read_b64_tr_b16 a[54:55],v25 offset:512
ds_read_b64_tr_b16 a[56:57],v24 offset:4096
ds_read_b64_tr_b16 a[58:59],v24 offset:4608
ds_read_b64_tr_b16 a[60:61],v25 offset:4096
ds_read_b64_tr_b16 a[62:63],v25 offset:4608
s_waitcnt lgkmcnt(8)
s_waitcnt vmcnt(24)
s_barrier
s_add_u32 m0,s88,32768
s_add_u32 s61,s82,1441792
buffer_load_dwordx4 v16,s[40:43],s61 offen offset:0  lds
s_add_u32 m0,s88,33792
ds_read_b64_tr_b16 a[64:65],v24 offset:16384
buffer_load_dwordx4 v17,s[40:43],s61 offen offset:0  lds
s_add_u32 m0,s88,40832
ds_read_b64_tr_b16 a[66:67],v24 offset:16896
buffer_load_dwordx4 v16,s[40:43],s61 offen offset:128  lds
s_add_u32 m0,s88,41856
ds_read_b64_tr_b16 a[68:69],v25 offset:16384
buffer_load_dwordx4 v17,s[40:43],s61 offen offset:128  lds
ds_read_b64_tr_b16 a[70:71],v25 offset:16896
ds_read_b64_tr_b16 a[72:73],v24 offset:20480
ds_read_b64_tr_b16 a[74:75],v24 offset:20992
ds_read_b64_tr_b16 a[76:77],v25 offset:20480
ds_read_b64_tr_b16 a[78:79],v25 offset:20992
s_waitcnt lgkmcnt(8)
s_waitcnt vmcnt(24)
s_barrier
s_add_u32 m0,s88,49152
s_add_u32 s61,s82,1572864
buffer_load_dwordx4 v16,s[40:43],s61 offen offset:0  lds
s_add_u32 m0,s88,50176
ds_read_b64_tr_b16 a[80:81],v24 offset:32768
buffer_load_dwordx4 v17,s[40:43],s61 offen offset:0  lds
s_add_u32 m0,s88,57216
ds_read_b64_tr_b16 a[82:83],v24 offset:33280
buffer_load_dwordx4 v16,s[40:43],s61 offen offset:128  lds
s_add_u32 m0,s88,58240
ds_read_b64_tr_b16 a[84:85],v25 offset:32768
buffer_load_dwordx4 v17,s[40:43],s61 offen offset:128  lds
ds_read_b64_tr_b16 a[86:87],v25 offset:33280
ds_read_b64_tr_b16 a[88:89],v24 offset:36864
ds_read_b64_tr_b16 a[90:91],v24 offset:37376
ds_read_b64_tr_b16 a[92:93],v25 offset:36864
ds_read_b64_tr_b16 a[94:95],v25 offset:37376
s_waitcnt lgkmcnt(8)
s_waitcnt vmcnt(24)
s_barrier
s_add_u32 m0,s88,65536
s_add_u32 s61,s82,1703936
buffer_load_dwordx4 v16,s[40:43],s61 offen offset:0  lds
s_add_u32 m0,s88,66560
ds_read_b64_tr_b16 a[96:97],v26 offset:0
buffer_load_dwordx4 v17,s[40:43],s61 offen offset:0  lds
s_add_u32 m0,s88,73600
ds_read_b64_tr_b16 a[98:99],v26 offset:512
buffer_load_dwordx4 v16,s[40:43],s61 offen offset:128  lds)"
R"(
s_add_u32 m0,s88,74624
ds_read_b64_tr_b16 a[100:101],v27 offset:0
buffer_load_dwordx4 v17,s[40:43],s61 offen offset:128  lds
ds_read_b64_tr_b16 a[102:103],v27 offset:512
ds_read_b64_tr_b16 a[104:105],v26 offset:4096
ds_read_b64_tr_b16 a[106:107],v26 offset:4608
ds_read_b64_tr_b16 a[108:109],v27 offset:4096
ds_read_b64_tr_b16 a[110:111],v27 offset:4608
s_waitcnt lgkmcnt(8)
s_waitcnt vmcnt(24)
s_barrier
s_add_u32 m0,s88,81920
s_add_u32 s61,s82,1835008
buffer_load_dwordx4 v16,s[40:43],s61 offen offset:0  lds
s_add_u32 m0,s88,82944
ds_read_b64_tr_b16 a[112:113],v26 offset:16384
buffer_load_dwordx4 v17,s[40:43],s61 offen offset:0  lds
s_add_u32 m0,s88,89984
ds_read_b64_tr_b16 a[114:115],v26 offset:16896
buffer_load_dwordx4 v16,s[40:43],s61 offen offset:128  lds
s_add_u32 m0,s88,91008
ds_read_b64_tr_b16 a[116:117],v27 offset:16384
buffer_load_dwordx4 v17,s[40:43],s61 offen offset:128  lds
ds_read_b64_tr_b16 a[118:119],v27 offset:16896
ds_read_b64_tr_b16 a[120:121],v26 offset:20480
ds_read_b64_tr_b16 a[122:123],v26 offset:20992
ds_read_b64_tr_b16 a[124:125],v27 offset:20480
ds_read_b64_tr_b16 a[126:127],v27 offset:20992
s_waitcnt lgkmcnt(8)
s_waitcnt vmcnt(24)
s_barrier
s_add_u32 m0,s88,98304
s_add_u32 s61,s82,1966080
buffer_load_dwordx4 v16,s[40:43],s61 offen offset:0  lds
s_add_u32 m0,s88,99328
ds_read_b64_tr_b16 a[128:129],v26 offset:32768
buffer_load_dwordx4 v17,s[40:43],s61 offen offset:0  lds
s_add_u32 m0,s88,106368
ds_read_b64_tr_b16 a[130:131],v26 offset:33280
buffer_load_dwordx4 v16,s[40:43],s61 offen offset:128  lds
s_add_u32 m0,s88,107392
ds_read_b64_tr_b16 a[132:133],v27 offset:32768
buffer_load_dwordx4 v17,s[40:43],s61 offen offset:128  lds
ds_read_b64_tr_b16 a[134:135],v27 offset:33280
ds_read_b64_tr_b16 a[136:137],v26 offset:36864
ds_read_b64_tr_b16 a[138:139],v26 offset:37376
ds_read_b64_tr_b16 a[140:141],v27 offset:36864
ds_read_b64_tr_b16 a[142:143],v27 offset:37376
s_waitcnt lgkmcnt(8)
s_waitcnt vmcnt(24)
s_barrier
ds_read_b64_tr_b16 a[144:145],v18 offset:0
ds_read_b64_tr_b16 a[146:147],v18 offset:512
ds_read_b64_tr_b16 a[148:149],v19 offset:0
ds_read_b64_tr_b16 a[150:151],v19 offset:512
ds_read_b64_tr_b16 a[152:153],v18 offset:4096
ds_read_b64_tr_b16 a[154:155],v18 offset:4608
ds_read_b64_tr_b16 a[156:157],v19 offset:4096
ds_read_b64_tr_b16 a[158:159],v19 offset:4608
s_waitcnt lgkmcnt(8)
s_waitcnt vmcnt(20)
s_barrier
ds_read_b64_tr_b16 a[160:161],v18 offset:16384
ds_read_b64_tr_b16 a[162:163],v18 offset:16896
ds_read_b64_tr_b16 a[164:165],v19 offset:16384
ds_read_b64_tr_b16 a[166:167],v19 offset:16896
ds_read_b64_tr_b16 a[168:169],v18 offset:20480
ds_read_b64_tr_b16 a[170:171],v18 offset:20992
ds_read_b64_tr_b16 a[172:173],v19 offset:20480
ds_read_b64_tr_b16 a[174:175],v19 offset:20992
s_waitcnt lgkmcnt(8)
s_waitcnt vmcnt(16)
s_barrier
ds_read_b64_tr_b16 a[176:177],v18 offset:32768
ds_read_b64_tr_b16 a[178:179],v18 offset:33280
ds_read_b64_tr_b16 a[180:181],v19 offset:32768
ds_read_b64_tr_b16 a[182:183],v19 offset:33280
ds_read_b64_tr_b16 a[184:185],v18 offset:36864
ds_read_b64_tr_b16 a[186:187],v18 offset:37376
ds_read_b64_tr_b16 a[188:189],v19 offset:36864
ds_read_b64_tr_b16 a[190:191],v19 offset:37376
s_getpc_b64 s[46:47]
Lg2_cp_%=:
s_add_u32 s46,s46,Lg2_init_%=-Lg2_cp_%=
s_addc_u32 s47,s47,0
s_lshl_b32 s68,s54,13
v_lshl_add_u32 v48,v2,3,s68
v_min_u32 v49,Lg2_done_%=-Lg2_init_%=,v48
global_load_dword v52,v49,s[46:47]
s_waitcnt lgkmcnt(8)
s_waitcnt vmcnt(13)
s_barrier
ds_read_b64_tr_b16 a[192:193],v24 offset:0
ds_read_b64_tr_b16 a[194:195],v24 offset:512
ds_read_b64_tr_b16 a[196:197],v25 offset:0
ds_read_b64_tr_b16 a[198:199],v25 offset:512
ds_read_b64_tr_b16 a[200:201],v24 offset:4096
ds_read_b64_tr_b16 a[202:203],v24 offset:4608
ds_read_b64_tr_b16 a[204:205],v25 offset:4096
ds_read_b64_tr_b16 a[206:207],v25 offset:4608
s_waitcnt lgkmcnt(8)
s_waitcnt vmcnt(9)
s_barrier
ds_read_b64_tr_b16 a[208:209],v24 offset:16384
ds_read_b64_tr_b16 a[210:211],v24 offset:16896
ds_read_b64_tr_b16 a[212:213],v25 offset:16384
ds_read_b64_tr_b16 a[214:215],v25 offset:16896
ds_read_b64_tr_b16 a[216:217],v24 offset:20480
ds_read_b64_tr_b16 a[218:219],v24 offset:20992
ds_read_b64_tr_b16 a[220:221],v25 offset:20480
ds_read_b64_tr_b16 a[222:223],v25 offset:20992
s_waitcnt lgkmcnt(8)
s_waitcnt vmcnt(5)
s_barrier
ds_read_b64_tr_b16 a[224:225],v24 offset:32768
ds_read_b64_tr_b16 a[226:227],v24 offset:33280
ds_read_b64_tr_b16 a[228:229],v25 offset:32768
ds_read_b64_tr_b16 a[230:231],v25 offset:33280
ds_read_b64_tr_b16 a[232:233],v24 offset:36864
ds_read_b64_tr_b16 a[234:235],v24 offset:37376
ds_read_b64_tr_b16 a[236:237],v25 offset:36864
ds_read_b64_tr_b16 a[238:239],v25 offset:37376
s_waitcnt lgkmcnt(8)
s_waitcnt vmcnt(1)
s_barrier
ds_read_b64_tr_b16 a[240:241],v26 offset:0
ds_read_b64_tr_b16 a[242:243],v26 offset:512
ds_read_b64_tr_b16 a[244:245],v27 offset:0
ds_read_b64_tr_b16 a[246:247],v27 offset:512
ds_read_b64_tr_b16 a[248:249],v26 offset:4096
ds_read_b64_tr_b16 a[250:251],v26 offset:4608
ds_read_b64_tr_b16 a[252:253],v27 offset:4096
ds_read_b64_tr_b16 a[254:255],v27 offset:4608
s_waitcnt lgkmcnt(8)
s_waitcnt lgkmcnt(0)
s_waitcnt lgkmcnt(0)
s_barrier
v_readfirstlane_b32 s60,v5
s_nop 3
s_add_u32 s60,s60,1
s_cmp_eq_u32 s60,0
s_cselect_b32 s60,1,s60
Lg2_init_%=:
v_mov_b32 v5,s60
s_mov_b32 s59,536870912
s_add_u32 s57,s56,131072
s_mov_b32 s79,0
s_mov_b32 s75,0x3a800000
s_mov_b32 s76,0xc0135761
s_mov_b32 s77,0x3d372713
s_mov_b32 s44,s59
s_mov_b32 s45,s59
s_mov_b32 s46,s59
s_mov_b32 s47,s59
s_mov_b32 s48,s83
s_add_u32 s49,s48,s55
s_cmp_eq_u32 s49,s56
s_cselect_b32 s49,s57,s49
s_cmp_eq_u32 s49,s58
s_cselect_b32 s49,s59,s49
s_add_u32 s50,s49,s55
s_cmp_eq_u32 s50,s56
s_cselect_b32 s50,s57,s50
s_cmp_eq_u32 s50,s58
s_cselect_b32 s50,s59,s50
s_add_u32 s51,s50,s55
s_cmp_eq_u32 s51,s56
s_cselect_b32 s51,s57,s51
s_cmp_eq_u32 s51,s58
s_cselect_b32 s51,s59,s51
s_add_u32 s52,s51,s55
s_cmp_eq_u32 s52,s56
s_cselect_b32 s52,s57,s52
s_cmp_eq_u32 s52,s58
s_cselect_b32 s52,s59,s52
s_lshl_b32 s63,s54,12
s_add_u32 s63,s63,65536
s_add_u32 s64,s63,32768
s_lshl_b32 s88,s54,10
s_add_u32 s88,s88,131072
s_lshl_b32 s78,s54,11
s_add_u32 s78,s78,132608
s_getpc_b64 s[90:91]
Lg2_pc_%=:
s_mov_b32 s68,Lg2_d1w0_%=-Lg2_pc_%=
s_cmp_eq_u32 s54,1
s_cselect_b32 s68,Lg2_d1w1_%=-Lg2_pc_%=,s68
s_cmp_eq_u32 s54,2
s_cselect_b32 s68,Lg2_d1w2_%=-Lg2_pc_%=,s68
s_cmp_eq_u32 s54,3
s_cselect_b32 s68,Lg2_d1w3_%=-Lg2_pc_%=,s68
s_add_u32 s92,s90,s68
s_addc_u32 s93,s91,0
s_mov_b32 s68,Lg2_d0w0_%=-Lg2_pc_%=
s_cmp_eq_u32 s54,1
s_cselect_b32 s68,Lg2_d0w1_%=-Lg2_pc_%=,s68
s_cmp_eq_u32 s54,2
s_cselect_b32 s68,Lg2_d0w2_%=-Lg2_pc_%=,s68
s_cmp_eq_u32 s54,3
s_cselect_b32 s68,Lg2_d0w3_%=-Lg2_pc_%=,s68)"
R"(
s_add_u32 s90,s90,s68
s_addc_u32 s91,s91,0
s_mov_b32 s66,s59
s_mov_b32 s65,0
s_mov_b32 s62,s63
s_branch Lg2_slow0_%=
Lg2_d0w0_%=:
v_mfma_f32_16x16x32_bf16 v[16:19],a[0:3],v[32:35],0
s_add_u32 m0,s62,16384
ds_read_b128 v[60:63],v2 offset:7168
v_lshlrev_b32 v64,16,v84
v_mfma_f32_16x16x32_bf16 v[20:23],a[4:7],v[32:35],0
buffer_load_dwordx4 v1,s[36:39],s66 offen offset:0 sc1 lds
v_mfma_f32_16x16x32_bf16 v[16:19],a[8:11],v[36:39],v[16:19]
s_lshr_b32 s73,s47,0
ds_read_b128 v[32:35],v2 offset:8192
v_and_b32 v65,0xffff0000,v84
v_mfma_f32_16x16x32_bf16 v[20:23],a[12:15],v[36:39],v[20:23]
s_xor_b32 s87,s64,65536
v_lshlrev_b32 v66,16,v85
v_and_b32 v67,0xffff0000,v85
v_mfma_f32_16x16x32_bf16 v[16:19],a[16:19],v[40:43],v[16:19]
s_cmp_ge_u32 s66,s59
ds_read_b128 v[36:39],v2 offset:9216
v_lshlrev_b32 v68,16,v86
v_mfma_f32_16x16x32_bf16 v[20:23],a[20:23],v[40:43],v[20:23]
buffer_load_dwordx4 v1,s[36:39],s66 offen offset:1024 sc1 lds
v_mfma_f32_16x16x32_bf16 v[16:19],a[24:27],v[44:47],v[16:19]
s_cselect_b32 s86,s49,s50
ds_read_b128 v[40:43],v2 offset:10240
v_and_b32 v69,0xffff0000,v86
v_mfma_f32_16x16x32_bf16 v[20:23],a[28:31],v[44:47],v[20:23]
s_waitcnt lgkmcnt(3)
v_lshlrev_b32 v70,16,v87
v_and_b32 v71,0xffff0000,v87
v_mfma_f32_16x16x32_bf16 v[16:19],a[32:35],v[48:51],v[16:19]
s_cselect_b32 s87,s87,s63
ds_read_b128 v[44:47],v2 offset:11264
v_fma_f32 v64,v64,v91,v90
v_mfma_f32_16x16x32_bf16 v[20:23],a[36:39],v[48:51],v[20:23]
buffer_load_dwordx4 v1,s[36:39],s66 offen offset:2048 sc1 lds
v_mfma_f32_16x16x32_bf16 v[16:19],a[40:43],v[52:55],v[16:19]
s_lshr_b32 s72,s66,6
ds_read_b128 v[48:51],v2 offset:12288
v_fma_f32 v65,v65,v91,v90
v_mfma_f32_16x16x32_bf16 v[20:23],a[44:47],v[52:55],v[20:23]
s_lshr_b32 s71,s62,2
v_fma_f32 v66,v66,v91,v90
v_fma_f32 v67,v67,v91,v90
v_mfma_f32_16x16x32_bf16 v[16:19],a[48:51],v[56:59],v[16:19]
s_mov_b32 s62,s87
ds_read_b128 v[52:55],v2 offset:13312
v_fma_f32 v68,v68,v91,v90
v_mfma_f32_16x16x32_bf16 v[20:23],a[52:55],v[56:59],v[20:23]
buffer_load_dwordx4 v1,s[36:39],s66 offen offset:3072 sc1 lds
v_mfma_f32_16x16x32_bf16 v[16:19],a[56:59],v[60:63],v[16:19]
s_add_u32 m0,s71,s88
ds_read_b128 v[56:59],v2 offset:14336
v_fma_f32 v69,v69,v91,v90
v_mfma_f32_16x16x32_bf16 v[20:23],a[60:63],v[60:63],v[20:23]
s_waitcnt lgkmcnt(3)
v_fma_f32 v70,v70,v91,v90
v_fma_f32 v71,v71,v91,v90
v_mfma_f32_16x16x32_bf16 v[16:19],a[64:67],v[32:35],v[16:19]
s_waitcnt vmcnt(10)
ds_read_b128 v[60:63],v2 offset:15360
v_add_f32 v24,v24,v64
v_mfma_f32_16x16x32_bf16 v[20:23],a[68:71],v[32:35],v[20:23]
s_barrier
v_mfma_f32_16x16x32_bf16 v[16:19],a[72:75],v[36:39],v[16:19]
s_lshr_b32 s68,s50,5
ds_read_b128 v[32:35],v2 offset:16384
v_add_f32 v25,v25,v65
v_mfma_f32_16x16x32_bf16 v[20:23],a[76:79],v[36:39],v[20:23]
buffer_load_dwordx4 v10,s[36:39],s66 offen offset:0 sc1 lds
v_mfma_f32_16x16x32_bf16 v[16:19],a[80:83],v[40:43],v[16:19]
buffer_load_dword v11,s[36:39],s72 offen offset:1024 sc1 lds
v_mfma_f32_16x16x32_bf16 v[20:23],a[84:87],v[40:43],v[20:23]
s_lshr_b32 s70,s51,5
ds_read_b128 v[36:39],v2 offset:17408
v_add_f32 v26,v26,v66
v_mfma_f32_16x16x32_bf16 v[16:19],a[88:91],v[44:47],v[16:19]
ds_read_b128 v[40:43],v2 offset:18432
v_add_f32 v27,v27,v67
v_mfma_f32_16x16x32_bf16 v[20:23],a[92:95],v[44:47],v[20:23]
s_waitcnt lgkmcnt(3)
v_add_f32 v28,v28,v68
v_add_f32 v29,v29,v69
v_add_f32 v30,v30,v70
v_add_f32 v31,v31,v71
v_cvt_pk_bf16_f32 v72,v24,v25
v_cvt_pk_bf16_f32 v73,v26,v27
v_cvt_pk_bf16_f32 v74,v28,v29
v_cvt_pk_bf16_f32 v75,v30,v31
v_mfma_f32_16x16x32_bf16 v[16:19],a[96:99],v[48:51],v[16:19]
buffer_store_dwordx4 v[72:75],v3,s[36:39],s73 offen offset:0 sc0 sc1
ds_read_b128 v[44:47],v2 offset:19456
v_add_f32 v64,v24,v25
v_mul_f32 v65,v24,v24
v_mfma_f32_16x16x32_bf16 v[20:23],a[100:103],v[48:51],v[20:23]
v_fma_f32 v65,v25,v25,v65
v_add_f32 v64,v64,v26
v_mfma_f32_16x16x32_bf16 v[16:19],a[104:107],v[52:55],v[16:19]
ds_read_b128 v[48:51],v2 offset:20480
v_fma_f32 v65,v26,v26,v65
v_mfma_f32_16x16x32_bf16 v[20:23],a[108:111],v[52:55],v[20:23]
v_add_f32 v64,v64,v27
v_fma_f32 v65,v27,v27,v65
v_mfma_f32_16x16x32_bf16 v[16:19],a[112:115],v[56:59],v[16:19]
ds_read_b128 v[52:55],v2 offset:21504
v_add_f32 v64,v64,v28
v_mfma_f32_16x16x32_bf16 v[20:23],a[116:119],v[56:59],v[20:23]
v_fma_f32 v65,v28,v28,v65
v_add_f32 v64,v64,v29
v_fma_f32 v65,v29,v29,v65
v_add_f32 v64,v64,v30
v_fma_f32 v65,v30,v30,v65
v_add_f32 v64,v64,v31
v_fma_f32 v65,v31,v31,v65
v_mfma_f32_16x16x32_bf16 v[16:19],a[120:123],v[60:63],v[16:19]
s_nop 0
v_permlane16_swap_b32 v64,v65
v_add_f32 v64,v64,v65
v_mov_b32 v65,v64
v_mfma_f32_16x16x32_bf16 v[20:23],a[124:127],v[60:63],v[20:23]
s_waitcnt lgkmcnt(2)
ds_read_b128 v[56:59],v2 offset:22528
v_permlane32_swap_b32 v64,v65
v_add_f32 v64,v64,v65
v_mfma_f32_16x16x32_bf16 v[16:19],a[128:131],v[32:35],v[16:19]
ds_write_b32 v13,v64 offset:0
v_mfma_f32_16x16x32_bf16 v[20:23],a[132:135],v[32:35],v[20:23]
ds_read_b128 v[60:63],v2 offset:23552
v_mfma_f32_16x16x32_bf16 v[16:19],a[136:139],v[36:39],v[16:19]
ds_read_b128 v[32:35],v2 offset:24576
v_mfma_f32_16x16x32_bf16 v[20:23],a[140:143],v[36:39],v[20:23]
v_mfma_f32_16x16x32_bf16 v[16:19],a[144:147],v[40:43],v[16:19]
s_waitcnt vmcnt(11) lgkmcnt(2)
ds_read_b128 v[36:39],v2 offset:25600
v_mfma_f32_16x16x32_bf16 v[20:23],a[148:151],v[40:43],v[20:23]
s_barrier
v_mfma_f32_16x16x32_bf16 v[16:19],a[152:155],v[44:47],v[16:19]
ds_read_b128 v[40:43],v2 offset:26624
ds_read_b32 v15,v7 offset:0
v_mfma_f32_16x16x32_bf16 v[20:23],a[156:159],v[44:47],v[20:23]
s_waitcnt lgkmcnt(4)
v_mfma_f32_16x16x32_bf16 v[16:19],a[160:163],v[48:51],v[16:19]
ds_read_b128 v[44:47],v2 offset:27648
v_mfma_f32_16x16x32_bf16 v[20:23],a[164:167],v[48:51],v[20:23]
v_mfma_f32_16x16x32_bf16 v[16:19],a[168:171],v[52:55],v[16:19]
s_waitcnt lgkmcnt(1)
ds_read_b128 v[48:51],v2 offset:28672
v_mfma_f32_16x16x32_bf16 v[20:23],a[172:175],v[52:55],v[20:23]
v_cmp_eq_u32 vcc,s60,v15
v_mfma_f32_16x16x32_bf16 v[16:19],a[176:179],v[56:59],v[16:19]
s_cmp_eq_u32 vcc_lo,-1
ds_read_b128 v[52:55],v2 offset:29696
s_cselect_b32 s67,s86,s59
v_mfma_f32_16x16x32_bf16 v[20:23],a[180:183],v[56:59],v[20:23]
s_cmp_ge_u32 s67,s59
s_cselect_b32 s68,s68,s70
s_add_u32 m0,s78,256
v_mfma_f32_16x16x32_bf16 v[16:19],a[184:187],v[60:63],v[16:19]
buffer_load_dword v6,s[36:39],s68 offen offset:0 sc1 lds
v_mfma_f32_16x16x32_bf16 v[20:23],a[188:191],v[60:63],v[20:23]
s_waitcnt lgkmcnt(2)
ds_read_b128 v[56:59],v2 offset:30720
s_xor_b32 s63,s63,65536
v_mfma_f32_16x16x32_bf16 v[16:19],a[192:195],v[32:35],v[16:19]
s_waitcnt vmcnt(8)
ds_read_b128 v[60:63],v2 offset:31744
s_mov_b32 m0,s62
v_mfma_f32_16x16x32_bf16 v[20:23],a[196:199],v[32:35],v[20:23]
s_barrier
v_mfma_f32_16x16x32_bf16 v[16:19],a[200:203],v[36:39],v[16:19]
buffer_load_dwordx4 v0,s[36:39],s67 offen offset:0 sc1 lds
v_mfma_f32_16x16x32_bf16 v[20:23],a[204:207],v[36:39],v[20:23]
s_add_u32 s53,s52,s55
ds_read_b128 v[32:35],v2 offset:32768
ds_read_b128 v[80:83],v8 offset:0
v_mfma_f32_16x16x32_bf16 v[16:19],a[208:211],v[40:43],v[16:19]
s_cmp_eq_u32 s53,s56
ds_read_b128 v[36:39],v2 offset:33792
ds_read_b64 v[88:89],v9 offset:1024
v_mfma_f32_16x16x32_bf16 v[20:23],a[212:215],v[40:43],v[20:23]
s_cselect_b32 s53,s57,s53
s_cmp_eq_u32 s53,s58
s_cselect_b32 s53,s59,s53
v_mfma_f32_16x16x32_bf16 v[16:19],a[216:219],v[44:47],v[16:19]
buffer_load_dwordx4 v0,s[36:39],s67 offen offset:1024 sc1 lds
v_mfma_f32_16x16x32_bf16 v[20:23],a[220:223],v[44:47],v[20:23]
s_waitcnt lgkmcnt(4)
ds_read_b128 v[40:43],v2 offset:34816
s_cmp_ge_u32 s66,s59
v_mfma_f32_16x16x32_bf16 v[16:19],a[224:227],v[48:51],v[16:19]
ds_read_b128 v[44:47],v2 offset:35840
v_mfma_f32_16x16x32_bf16 v[20:23],a[228:231],v[48:51],v[20:23]
v_mfma_f32_16x16x32_bf16 v[16:19],a[232:235],v[52:55],v[16:19]
s_waitcnt lgkmcnt(2)
buffer_load_dwordx4 v0,s[36:39],s67 offen offset:2048 sc1 lds)"
R"(
v_mfma_f32_16x16x32_bf16 v[20:23],a[236:239],v[52:55],v[20:23]
ds_read_b128 v[48:51],v2 offset:36864
v_mfma_f32_16x16x32_bf16 v[16:19],a[240:243],v[56:59],v[16:19]
ds_read_b128 v[52:55],v2 offset:37888
v_mfma_f32_16x16x32_bf16 v[20:23],a[244:247],v[56:59],v[20:23]
v_mfma_f32_16x16x32_bf16 v[16:19],a[248:251],v[60:63],v[16:19]
s_waitcnt lgkmcnt(2)
ds_read_b128 v[56:59],v2 offset:38912
buffer_load_dwordx4 v0,s[36:39],s67 offen offset:3072 sc1 lds
v_mfma_f32_16x16x32_bf16 v[20:23],a[252:255],v[60:63],v[20:23]
s_cbranch_scc1 Lg2_slow1_%=
Lg2_d1w0_%=:
v_mfma_f32_16x16x32_bf16 v[24:27],a[0:3],v[32:35],0
s_add_u32 m0,s62,16384
ds_read_b128 v[60:63],v2 offset:39936
v_lshlrev_b32 v64,16,v80
v_mfma_f32_16x16x32_bf16 v[28:31],a[4:7],v[32:35],0
buffer_load_dwordx4 v1,s[36:39],s67 offen offset:0 sc1 lds
v_mfma_f32_16x16x32_bf16 v[24:27],a[8:11],v[36:39],v[24:27]
s_lshr_b32 s73,s48,0
ds_read_b128 v[32:35],v2 offset:40960
v_and_b32 v65,0xffff0000,v80
v_mfma_f32_16x16x32_bf16 v[28:31],a[12:15],v[36:39],v[28:31]
s_xor_b32 s87,s63,65536
v_lshlrev_b32 v66,16,v81
v_and_b32 v67,0xffff0000,v81
v_mfma_f32_16x16x32_bf16 v[24:27],a[16:19],v[40:43],v[24:27]
s_cmp_ge_u32 s67,s59
ds_read_b128 v[36:39],v2 offset:41984
v_lshlrev_b32 v68,16,v82
v_mfma_f32_16x16x32_bf16 v[28:31],a[20:23],v[40:43],v[28:31]
buffer_load_dwordx4 v1,s[36:39],s67 offen offset:1024 sc1 lds
v_mfma_f32_16x16x32_bf16 v[24:27],a[24:27],v[44:47],v[24:27]
s_cselect_b32 s86,s50,s51
ds_read_b128 v[40:43],v2 offset:43008
v_and_b32 v69,0xffff0000,v82
v_mfma_f32_16x16x32_bf16 v[28:31],a[28:31],v[44:47],v[28:31]
s_waitcnt lgkmcnt(3)
v_lshlrev_b32 v70,16,v83
v_and_b32 v71,0xffff0000,v83
v_mfma_f32_16x16x32_bf16 v[24:27],a[32:35],v[48:51],v[24:27]
s_cselect_b32 s87,s87,s64
ds_read_b128 v[44:47],v2 offset:44032
v_fma_f32 v64,v64,v89,v88
v_mfma_f32_16x16x32_bf16 v[28:31],a[36:39],v[48:51],v[28:31]
buffer_load_dwordx4 v1,s[36:39],s67 offen offset:2048 sc1 lds
v_mfma_f32_16x16x32_bf16 v[24:27],a[40:43],v[52:55],v[24:27]
s_lshr_b32 s72,s67,6
ds_read_b128 v[48:51],v2 offset:45056
v_fma_f32 v65,v65,v89,v88
v_mfma_f32_16x16x32_bf16 v[28:31],a[44:47],v[52:55],v[28:31]
s_lshr_b32 s71,s62,2
v_fma_f32 v66,v66,v89,v88
v_fma_f32 v67,v67,v89,v88
v_mfma_f32_16x16x32_bf16 v[24:27],a[48:51],v[56:59],v[24:27]
s_mov_b32 s62,s87
ds_read_b128 v[52:55],v2 offset:46080
v_fma_f32 v68,v68,v89,v88
v_mfma_f32_16x16x32_bf16 v[28:31],a[52:55],v[56:59],v[28:31]
buffer_load_dwordx4 v1,s[36:39],s67 offen offset:3072 sc1 lds
v_mfma_f32_16x16x32_bf16 v[24:27],a[56:59],v[60:63],v[24:27]
s_add_u32 m0,s71,s88
ds_read_b128 v[56:59],v2 offset:47104
v_fma_f32 v69,v69,v89,v88
v_mfma_f32_16x16x32_bf16 v[28:31],a[60:63],v[60:63],v[28:31]
s_waitcnt lgkmcnt(3)
v_fma_f32 v70,v70,v89,v88
v_fma_f32 v71,v71,v89,v88
v_mfma_f32_16x16x32_bf16 v[24:27],a[64:67],v[32:35],v[24:27]
s_waitcnt vmcnt(10)
ds_read_b128 v[60:63],v2 offset:48128
v_add_f32 v16,v16,v64
v_mfma_f32_16x16x32_bf16 v[28:31],a[68:71],v[32:35],v[28:31]
s_barrier
v_mfma_f32_16x16x32_bf16 v[24:27],a[72:75],v[36:39],v[24:27]
s_lshr_b32 s68,s51,5
ds_read_b128 v[32:35],v2 offset:49152
v_add_f32 v17,v17,v65
v_mfma_f32_16x16x32_bf16 v[28:31],a[76:79],v[36:39],v[28:31]
buffer_load_dwordx4 v10,s[36:39],s67 offen offset:0 sc1 lds
v_mfma_f32_16x16x32_bf16 v[24:27],a[80:83],v[40:43],v[24:27]
buffer_load_dword v11,s[36:39],s72 offen offset:1024 sc1 lds
v_mfma_f32_16x16x32_bf16 v[28:31],a[84:87],v[40:43],v[28:31]
s_lshr_b32 s70,s52,5
ds_read_b128 v[36:39],v2 offset:50176
v_add_f32 v18,v18,v66
v_mfma_f32_16x16x32_bf16 v[24:27],a[88:91],v[44:47],v[24:27]
s_mov_b64 s[44:45],s[46:47]
ds_read_b128 v[40:43],v2 offset:51200
v_add_f32 v19,v19,v67
v_mfma_f32_16x16x32_bf16 v[28:31],a[92:95],v[44:47],v[28:31]
s_waitcnt lgkmcnt(3)
v_add_f32 v20,v20,v68
v_add_f32 v21,v21,v69
v_add_f32 v22,v22,v70
v_add_f32 v23,v23,v71
v_cvt_pk_bf16_f32 v72,v16,v17
v_cvt_pk_bf16_f32 v73,v18,v19
v_cvt_pk_bf16_f32 v74,v20,v21
v_cvt_pk_bf16_f32 v75,v22,v23
v_mfma_f32_16x16x32_bf16 v[24:27],a[96:99],v[48:51],v[24:27]
buffer_store_dwordx4 v[72:75],v3,s[36:39],s73 offen offset:0 sc0 sc1
ds_read_b128 v[44:47],v2 offset:52224
v_add_f32 v64,v16,v17
v_mul_f32 v65,v16,v16
v_mfma_f32_16x16x32_bf16 v[28:31],a[100:103],v[48:51],v[28:31]
s_mov_b64 s[46:47],s[48:49]
v_fma_f32 v65,v17,v17,v65
v_add_f32 v64,v64,v18
v_mfma_f32_16x16x32_bf16 v[24:27],a[104:107],v[52:55],v[24:27]
s_mov_b64 s[48:49],s[50:51]
ds_read_b128 v[48:51],v2 offset:53248
v_fma_f32 v65,v18,v18,v65
v_mfma_f32_16x16x32_bf16 v[28:31],a[108:111],v[52:55],v[28:31]
s_mov_b64 s[50:51],s[52:53]
v_add_f32 v64,v64,v19
v_fma_f32 v65,v19,v19,v65
v_mfma_f32_16x16x32_bf16 v[24:27],a[112:115],v[56:59],v[24:27]
ds_read_b128 v[52:55],v2 offset:54272
v_add_f32 v64,v64,v20
v_mfma_f32_16x16x32_bf16 v[28:31],a[116:119],v[56:59],v[28:31]
v_fma_f32 v65,v20,v20,v65
v_add_f32 v64,v64,v21
v_fma_f32 v65,v21,v21,v65
v_add_f32 v64,v64,v22
v_fma_f32 v65,v22,v22,v65
v_add_f32 v64,v64,v23
v_fma_f32 v65,v23,v23,v65
v_mfma_f32_16x16x32_bf16 v[24:27],a[120:123],v[60:63],v[24:27]
s_nop 0
v_permlane16_swap_b32 v64,v65
v_add_f32 v64,v64,v65
v_mov_b32 v65,v64
v_mfma_f32_16x16x32_bf16 v[28:31],a[124:127],v[60:63],v[28:31]
s_waitcnt lgkmcnt(2)
ds_read_b128 v[56:59],v2 offset:55296
v_permlane32_swap_b32 v64,v65
v_add_f32 v64,v64,v65
v_mfma_f32_16x16x32_bf16 v[24:27],a[128:131],v[32:35],v[24:27]
ds_write_b32 v13,v64 offset:2048
v_mfma_f32_16x16x32_bf16 v[28:31],a[132:135],v[32:35],v[28:31]
ds_read_b128 v[60:63],v2 offset:56320
v_mfma_f32_16x16x32_bf16 v[24:27],a[136:139],v[36:39],v[24:27]
ds_read_b128 v[32:35],v2 offset:57344
v_mfma_f32_16x16x32_bf16 v[28:31],a[140:143],v[36:39],v[28:31]
v_mfma_f32_16x16x32_bf16 v[24:27],a[144:147],v[40:43],v[24:27]
s_waitcnt vmcnt(11) lgkmcnt(2)
ds_read_b128 v[36:39],v2 offset:58368
v_mfma_f32_16x16x32_bf16 v[28:31],a[148:151],v[40:43],v[28:31]
s_barrier
v_mfma_f32_16x16x32_bf16 v[24:27],a[152:155],v[44:47],v[24:27]
ds_read_b128 v[40:43],v2 offset:59392
ds_read_b32 v15,v7 offset:256
v_mfma_f32_16x16x32_bf16 v[28:31],a[156:159],v[44:47],v[28:31]
s_waitcnt lgkmcnt(4)
v_mfma_f32_16x16x32_bf16 v[24:27],a[160:163],v[48:51],v[24:27]
ds_read_b128 v[44:47],v2 offset:60416
v_mfma_f32_16x16x32_bf16 v[28:31],a[164:167],v[48:51],v[28:31]
v_mfma_f32_16x16x32_bf16 v[24:27],a[168:171],v[52:55],v[24:27]
s_waitcnt lgkmcnt(1)
ds_read_b128 v[48:51],v2 offset:61440
v_mfma_f32_16x16x32_bf16 v[28:31],a[172:175],v[52:55],v[28:31]
v_cmp_eq_u32 vcc,s60,v15
v_mfma_f32_16x16x32_bf16 v[24:27],a[176:179],v[56:59],v[24:27]
s_cmp_eq_u32 vcc_lo,-1
ds_read_b128 v[52:55],v2 offset:62464
s_cselect_b32 s66,s86,s59
v_mfma_f32_16x16x32_bf16 v[28:31],a[180:183],v[56:59],v[28:31]
s_cmp_ge_u32 s66,s59
s_cselect_b32 s68,s68,s70
s_add_u32 m0,s78,0
v_mfma_f32_16x16x32_bf16 v[24:27],a[184:187],v[60:63],v[24:27]
buffer_load_dword v6,s[36:39],s68 offen offset:0 sc1 lds
v_mfma_f32_16x16x32_bf16 v[28:31],a[188:191],v[60:63],v[28:31]
s_waitcnt lgkmcnt(2)
ds_read_b128 v[56:59],v2 offset:63488
s_xor_b32 s64,s64,65536
v_mfma_f32_16x16x32_bf16 v[24:27],a[192:195],v[32:35],v[24:27]
s_waitcnt vmcnt(8)
ds_read_b128 v[60:63],v2 offset:64512
v_xor_b32 v2,65536,v2
v_mfma_f32_16x16x32_bf16 v[28:31],a[196:199],v[32:35],v[28:31]
s_barrier
s_mov_b32 m0,s62
v_mfma_f32_16x16x32_bf16 v[24:27],a[200:203],v[36:39],v[24:27]
buffer_load_dwordx4 v0,s[36:39],s66 offen offset:0 sc1 lds
v_mfma_f32_16x16x32_bf16 v[28:31],a[204:207],v[36:39],v[28:31]
s_add_u32 s74,s53,s55
ds_read_b128 v[32:35],v2 offset:0
ds_read_b128 v[84:87],v8 offset:8192
v_mfma_f32_16x16x32_bf16 v[24:27],a[208:211],v[40:43],v[24:27]
s_cmp_eq_u32 s74,s56
ds_read_b128 v[36:39],v2 offset:1024
ds_read_b64 v[90:91],v9 offset:9216
v_mfma_f32_16x16x32_bf16 v[28:31],a[212:215],v[40:43],v[28:31]
s_cselect_b32 s74,s57,s74
v_xor_b32 v8,16384,v8
v_xor_b32 v9,16384,v9
v_mfma_f32_16x16x32_bf16 v[24:27],a[216:219],v[44:47],v[24:27]
buffer_load_dwordx4 v0,s[36:39],s66 offen offset:1024 sc1 lds)"
R"(
v_mfma_f32_16x16x32_bf16 v[28:31],a[220:223],v[44:47],v[28:31]
s_waitcnt lgkmcnt(4)
ds_read_b128 v[40:43],v2 offset:2048
s_cmp_eq_u32 s74,s58
v_mfma_f32_16x16x32_bf16 v[24:27],a[224:227],v[48:51],v[24:27]
s_cselect_b32 s74,s59,s74
ds_read_b128 v[44:47],v2 offset:3072
s_mov_b32 s52,s74
v_mfma_f32_16x16x32_bf16 v[28:31],a[228:231],v[48:51],v[28:31]
s_cmp_lt_u32 s67,s59
v_mfma_f32_16x16x32_bf16 v[24:27],a[232:235],v[52:55],v[24:27]
s_waitcnt lgkmcnt(2)
buffer_load_dwordx4 v0,s[36:39],s66 offen offset:2048 sc1 lds
v_mfma_f32_16x16x32_bf16 v[28:31],a[236:239],v[52:55],v[28:31]
ds_read_b128 v[48:51],v2 offset:4096
v_mfma_f32_16x16x32_bf16 v[24:27],a[240:243],v[56:59],v[24:27]
ds_read_b128 v[52:55],v2 offset:5120
v_mfma_f32_16x16x32_bf16 v[28:31],a[244:247],v[56:59],v[28:31]
v_mfma_f32_16x16x32_bf16 v[24:27],a[248:251],v[60:63],v[24:27]
s_waitcnt lgkmcnt(2)
ds_read_b128 v[56:59],v2 offset:6144
buffer_load_dwordx4 v0,s[36:39],s66 offen offset:3072 sc1 lds
v_mfma_f32_16x16x32_bf16 v[28:31],a[252:255],v[60:63],v[28:31]
s_cbranch_scc1 Lg2_d0w0_%=
s_branch Lg2_slow0_%=
Lg2_d0w1_%=:
v_mfma_f32_16x16x32_bf16 v[16:19],a[0:3],v[32:35],0
s_add_u32 m0,s62,16384
ds_read_b128 v[60:63],v2 offset:7168
v_lshlrev_b32 v64,16,v84
v_mfma_f32_16x16x32_bf16 v[20:23],a[4:7],v[32:35],0
s_lshr_b32 s73,s47,0
v_and_b32 v65,0xffff0000,v84
v_lshlrev_b32 v66,16,v85
v_mfma_f32_16x16x32_bf16 v[16:19],a[8:11],v[36:39],v[16:19]
buffer_load_dwordx4 v1,s[36:39],s66 offen offset:0 sc1 lds
v_mfma_f32_16x16x32_bf16 v[20:23],a[12:15],v[36:39],v[20:23]
s_xor_b32 s87,s64,65536
ds_read_b128 v[32:35],v2 offset:8192
v_and_b32 v67,0xffff0000,v85
v_mfma_f32_16x16x32_bf16 v[16:19],a[16:19],v[40:43],v[16:19]
s_cmp_ge_u32 s66,s59
ds_read_b128 v[36:39],v2 offset:9216
v_lshlrev_b32 v68,16,v86
v_mfma_f32_16x16x32_bf16 v[20:23],a[20:23],v[40:43],v[20:23]
s_cselect_b32 s86,s49,s50
v_and_b32 v69,0xffff0000,v86
v_lshlrev_b32 v70,16,v87
v_mfma_f32_16x16x32_bf16 v[16:19],a[24:27],v[44:47],v[16:19]
buffer_load_dwordx4 v1,s[36:39],s66 offen offset:1024 sc1 lds
v_mfma_f32_16x16x32_bf16 v[20:23],a[28:31],v[44:47],v[20:23]
s_waitcnt lgkmcnt(2)
ds_read_b128 v[40:43],v2 offset:10240
v_and_b32 v71,0xffff0000,v87
v_mfma_f32_16x16x32_bf16 v[16:19],a[32:35],v[48:51],v[16:19]
s_cselect_b32 s87,s87,s63
ds_read_b128 v[44:47],v2 offset:11264
v_fma_f32 v64,v64,v91,v90
v_mfma_f32_16x16x32_bf16 v[20:23],a[36:39],v[48:51],v[20:23]
s_lshr_b32 s72,s66,6
v_fma_f32 v65,v65,v91,v90
v_fma_f32 v66,v66,v91,v90
v_mfma_f32_16x16x32_bf16 v[16:19],a[40:43],v[52:55],v[16:19]
buffer_load_dwordx4 v1,s[36:39],s66 offen offset:2048 sc1 lds
v_mfma_f32_16x16x32_bf16 v[20:23],a[44:47],v[52:55],v[20:23]
s_lshr_b32 s71,s62,2
ds_read_b128 v[48:51],v2 offset:12288
v_fma_f32 v67,v67,v91,v90
v_mfma_f32_16x16x32_bf16 v[16:19],a[48:51],v[56:59],v[16:19]
s_mov_b32 s62,s87
ds_read_b128 v[52:55],v2 offset:13312
v_fma_f32 v68,v68,v91,v90
v_mfma_f32_16x16x32_bf16 v[20:23],a[52:55],v[56:59],v[20:23]
v_fma_f32 v69,v69,v91,v90
v_fma_f32 v70,v70,v91,v90
v_mfma_f32_16x16x32_bf16 v[16:19],a[56:59],v[60:63],v[16:19]
buffer_load_dwordx4 v1,s[36:39],s66 offen offset:3072 sc1 lds
v_mfma_f32_16x16x32_bf16 v[20:23],a[60:63],v[60:63],v[20:23]
s_waitcnt lgkmcnt(2)
ds_read_b128 v[56:59],v2 offset:14336
v_fma_f32 v71,v71,v91,v90
v_mfma_f32_16x16x32_bf16 v[16:19],a[64:67],v[32:35],v[16:19]
s_waitcnt vmcnt(9)
ds_read_b128 v[60:63],v2 offset:15360
v_add_f32 v24,v24,v64
v_mfma_f32_16x16x32_bf16 v[20:23],a[68:71],v[32:35],v[20:23]
s_barrier
v_mfma_f32_16x16x32_bf16 v[16:19],a[72:75],v[36:39],v[16:19]
s_add_u32 m0,s71,s88
ds_read_b128 v[32:35],v2 offset:16384
v_add_f32 v25,v25,v65
v_mfma_f32_16x16x32_bf16 v[20:23],a[76:79],v[36:39],v[20:23]
v_add_f32 v26,v26,v66
v_add_f32 v27,v27,v67
v_mfma_f32_16x16x32_bf16 v[16:19],a[80:83],v[40:43],v[16:19]
buffer_load_dwordx4 v10,s[36:39],s66 offen offset:0 sc1 lds
v_mfma_f32_16x16x32_bf16 v[20:23],a[84:87],v[40:43],v[20:23]
ds_read_b128 v[36:39],v2 offset:17408
v_add_f32 v28,v28,v68
v_mfma_f32_16x16x32_bf16 v[16:19],a[88:91],v[44:47],v[16:19]
buffer_load_dword v11,s[36:39],s72 offen offset:1024 sc1 lds
v_mfma_f32_16x16x32_bf16 v[20:23],a[92:95],v[44:47],v[20:23]
s_waitcnt lgkmcnt(2)
ds_read_b128 v[40:43],v2 offset:18432
v_add_f32 v29,v29,v69
v_mfma_f32_16x16x32_bf16 v[16:19],a[96:99],v[48:51],v[16:19]
s_mov_b32 m0,s62
v_add_f32 v30,v30,v70
v_add_f32 v31,v31,v71
v_cvt_pk_bf16_f32 v72,v24,v25
v_cvt_pk_bf16_f32 v73,v26,v27
v_cvt_pk_bf16_f32 v74,v28,v29
v_cvt_pk_bf16_f32 v75,v30,v31
v_mfma_f32_16x16x32_bf16 v[20:23],a[100:103],v[48:51],v[20:23]
ds_read_b128 v[44:47],v2 offset:19456
buffer_store_dwordx4 v[72:75],v3,s[36:39],s73 offen offset:0 sc0 sc1
v_add_f32 v64,v24,v25
v_mul_f32 v65,v24,v24
v_fma_f32 v65,v25,v25,v65
v_mfma_f32_16x16x32_bf16 v[16:19],a[104:107],v[52:55],v[16:19]
ds_read_b128 v[48:51],v2 offset:20480
v_add_f32 v64,v64,v26
v_mfma_f32_16x16x32_bf16 v[20:23],a[108:111],v[52:55],v[20:23]
v_fma_f32 v65,v26,v26,v65
v_add_f32 v64,v64,v27
v_mfma_f32_16x16x32_bf16 v[16:19],a[112:115],v[56:59],v[16:19]
ds_read_b128 v[52:55],v2 offset:21504
v_fma_f32 v65,v27,v27,v65
v_mfma_f32_16x16x32_bf16 v[20:23],a[116:119],v[56:59],v[20:23]
v_add_f32 v64,v64,v28
v_fma_f32 v65,v28,v28,v65
v_add_f32 v64,v64,v29
v_fma_f32 v65,v29,v29,v65
v_add_f32 v64,v64,v30
v_fma_f32 v65,v30,v30,v65
v_add_f32 v64,v64,v31
v_fma_f32 v65,v31,v31,v65
v_mfma_f32_16x16x32_bf16 v[16:19],a[120:123],v[60:63],v[16:19]
s_nop 0
v_permlane16_swap_b32 v64,v65
v_add_f32 v64,v64,v65
v_mov_b32 v65,v64
v_mfma_f32_16x16x32_bf16 v[20:23],a[124:127],v[60:63],v[20:23]
s_waitcnt lgkmcnt(2)
ds_read_b128 v[56:59],v2 offset:22528
v_permlane32_swap_b32 v64,v65
v_add_f32 v64,v64,v65
v_mfma_f32_16x16x32_bf16 v[16:19],a[128:131],v[32:35],v[16:19]
ds_write_b32 v13,v64 offset:0
v_mfma_f32_16x16x32_bf16 v[20:23],a[132:135],v[32:35],v[20:23]
ds_read_b128 v[60:63],v2 offset:23552
v_mfma_f32_16x16x32_bf16 v[16:19],a[136:139],v[36:39],v[16:19]
ds_read_b128 v[32:35],v2 offset:24576
v_mfma_f32_16x16x32_bf16 v[20:23],a[140:143],v[36:39],v[20:23]
v_mfma_f32_16x16x32_bf16 v[16:19],a[144:147],v[40:43],v[16:19]
s_waitcnt vmcnt(11) lgkmcnt(2)
ds_read_b128 v[36:39],v2 offset:25600
v_mfma_f32_16x16x32_bf16 v[20:23],a[148:151],v[40:43],v[20:23]
s_barrier
v_mfma_f32_16x16x32_bf16 v[16:19],a[152:155],v[44:47],v[16:19]
ds_read_b128 v[40:43],v2 offset:26624
ds_read_b32 v15,v7 offset:0
v_mfma_f32_16x16x32_bf16 v[20:23],a[156:159],v[44:47],v[20:23]
s_waitcnt lgkmcnt(4)
v_mfma_f32_16x16x32_bf16 v[16:19],a[160:163],v[48:51],v[16:19]
ds_read_b128 v[44:47],v2 offset:27648
v_mfma_f32_16x16x32_bf16 v[20:23],a[164:167],v[48:51],v[20:23]
v_mfma_f32_16x16x32_bf16 v[16:19],a[168:171],v[52:55],v[16:19]
s_waitcnt lgkmcnt(1)
ds_read_b128 v[48:51],v2 offset:28672
v_mfma_f32_16x16x32_bf16 v[20:23],a[172:175],v[52:55],v[20:23]
v_cmp_eq_u32 vcc,s60,v15
v_mfma_f32_16x16x32_bf16 v[16:19],a[176:179],v[56:59],v[16:19]
s_cmp_eq_u32 vcc_lo,-1
ds_read_b128 v[52:55],v2 offset:29696
s_cselect_b32 s67,s86,s59
v_mfma_f32_16x16x32_bf16 v[20:23],a[180:183],v[56:59],v[20:23]
buffer_load_dwordx4 v0,s[36:39],s67 offen offset:0 sc1 lds
v_mfma_f32_16x16x32_bf16 v[16:19],a[184:187],v[60:63],v[16:19]
s_xor_b32 s63,s63,65536
ds_read_b128 v[56:59],v2 offset:30720
s_add_u32 s53,s52,s55
v_mfma_f32_16x16x32_bf16 v[20:23],a[188:191],v[60:63],v[20:23]
s_waitcnt lgkmcnt(3)
s_cmp_eq_u32 s53,s56
s_cselect_b32 s53,s57,s53
v_mfma_f32_16x16x32_bf16 v[16:19],a[192:195],v[32:35],v[16:19]
s_waitcnt vmcnt(8)
ds_read_b128 v[60:63],v2 offset:31744
s_cmp_eq_u32 s53,s58
v_mfma_f32_16x16x32_bf16 v[20:23],a[196:199],v[32:35],v[20:23]
s_barrier
v_mfma_f32_16x16x32_bf16 v[16:19],a[200:203],v[36:39],v[16:19]
s_cselect_b32 s53,s59,s53
ds_read_b128 v[32:35],v2 offset:32768
ds_read_b128 v[80:83],v8 offset:0
v_mfma_f32_16x16x32_bf16 v[20:23],a[204:207],v[36:39],v[20:23]
buffer_load_dwordx4 v0,s[36:39],s67 offen offset:1024 sc1 lds
v_mfma_f32_16x16x32_bf16 v[16:19],a[208:211],v[40:43],v[16:19]
s_cmp_ge_u32 s66,s59)"
R"(
ds_read_b128 v[36:39],v2 offset:33792
ds_read_b64 v[88:89],v9 offset:1024
v_mfma_f32_16x16x32_bf16 v[20:23],a[212:215],v[40:43],v[20:23]
v_mfma_f32_16x16x32_bf16 v[16:19],a[216:219],v[44:47],v[16:19]
ds_read_b128 v[40:43],v2 offset:34816
v_mfma_f32_16x16x32_bf16 v[20:23],a[220:223],v[44:47],v[20:23]
s_waitcnt lgkmcnt(5)
buffer_load_dwordx4 v0,s[36:39],s67 offen offset:2048 sc1 lds
v_mfma_f32_16x16x32_bf16 v[16:19],a[224:227],v[48:51],v[16:19]
ds_read_b128 v[44:47],v2 offset:35840
v_mfma_f32_16x16x32_bf16 v[20:23],a[228:231],v[48:51],v[20:23]
v_mfma_f32_16x16x32_bf16 v[16:19],a[232:235],v[52:55],v[16:19]
s_waitcnt lgkmcnt(2)
ds_read_b128 v[48:51],v2 offset:36864
v_mfma_f32_16x16x32_bf16 v[20:23],a[236:239],v[52:55],v[20:23]
buffer_load_dwordx4 v0,s[36:39],s67 offen offset:3072 sc1 lds
v_mfma_f32_16x16x32_bf16 v[16:19],a[240:243],v[56:59],v[16:19]
ds_read_b128 v[52:55],v2 offset:37888
v_mfma_f32_16x16x32_bf16 v[20:23],a[244:247],v[56:59],v[20:23]
v_mfma_f32_16x16x32_bf16 v[16:19],a[248:251],v[60:63],v[16:19]
s_waitcnt lgkmcnt(2)
ds_read_b128 v[56:59],v2 offset:38912
v_mfma_f32_16x16x32_bf16 v[20:23],a[252:255],v[60:63],v[20:23]
s_cbranch_scc1 Lg2_slow1_%=
Lg2_d1w1_%=:
v_mfma_f32_16x16x32_bf16 v[24:27],a[0:3],v[32:35],0
s_add_u32 m0,s62,16384
ds_read_b128 v[60:63],v2 offset:39936
v_lshlrev_b32 v64,16,v80
v_mfma_f32_16x16x32_bf16 v[28:31],a[4:7],v[32:35],0
s_lshr_b32 s73,s48,0
v_and_b32 v65,0xffff0000,v80
v_lshlrev_b32 v66,16,v81
v_mfma_f32_16x16x32_bf16 v[24:27],a[8:11],v[36:39],v[24:27]
buffer_load_dwordx4 v1,s[36:39],s67 offen offset:0 sc1 lds
v_mfma_f32_16x16x32_bf16 v[28:31],a[12:15],v[36:39],v[28:31]
s_xor_b32 s87,s63,65536
ds_read_b128 v[32:35],v2 offset:40960
v_and_b32 v67,0xffff0000,v81
v_mfma_f32_16x16x32_bf16 v[24:27],a[16:19],v[40:43],v[24:27]
s_cmp_ge_u32 s67,s59
ds_read_b128 v[36:39],v2 offset:41984
v_lshlrev_b32 v68,16,v82
v_mfma_f32_16x16x32_bf16 v[28:31],a[20:23],v[40:43],v[28:31]
s_cselect_b32 s86,s50,s51
v_and_b32 v69,0xffff0000,v82
v_lshlrev_b32 v70,16,v83
v_mfma_f32_16x16x32_bf16 v[24:27],a[24:27],v[44:47],v[24:27]
buffer_load_dwordx4 v1,s[36:39],s67 offen offset:1024 sc1 lds
v_mfma_f32_16x16x32_bf16 v[28:31],a[28:31],v[44:47],v[28:31]
s_waitcnt lgkmcnt(2)
ds_read_b128 v[40:43],v2 offset:43008
v_and_b32 v71,0xffff0000,v83
v_mfma_f32_16x16x32_bf16 v[24:27],a[32:35],v[48:51],v[24:27]
s_cselect_b32 s87,s87,s64
ds_read_b128 v[44:47],v2 offset:44032
v_fma_f32 v64,v64,v89,v88
v_mfma_f32_16x16x32_bf16 v[28:31],a[36:39],v[48:51],v[28:31]
s_lshr_b32 s72,s67,6
v_fma_f32 v65,v65,v89,v88
v_fma_f32 v66,v66,v89,v88
v_mfma_f32_16x16x32_bf16 v[24:27],a[40:43],v[52:55],v[24:27]
buffer_load_dwordx4 v1,s[36:39],s67 offen offset:2048 sc1 lds
v_mfma_f32_16x16x32_bf16 v[28:31],a[44:47],v[52:55],v[28:31]
s_lshr_b32 s71,s62,2
ds_read_b128 v[48:51],v2 offset:45056
v_fma_f32 v67,v67,v89,v88
v_mfma_f32_16x16x32_bf16 v[24:27],a[48:51],v[56:59],v[24:27]
s_mov_b32 s62,s87
ds_read_b128 v[52:55],v2 offset:46080
v_fma_f32 v68,v68,v89,v88
v_mfma_f32_16x16x32_bf16 v[28:31],a[52:55],v[56:59],v[28:31]
s_mov_b64 s[44:45],s[46:47]
v_fma_f32 v69,v69,v89,v88
v_fma_f32 v70,v70,v89,v88
v_mfma_f32_16x16x32_bf16 v[24:27],a[56:59],v[60:63],v[24:27]
buffer_load_dwordx4 v1,s[36:39],s67 offen offset:3072 sc1 lds
v_mfma_f32_16x16x32_bf16 v[28:31],a[60:63],v[60:63],v[28:31]
s_waitcnt lgkmcnt(2)
ds_read_b128 v[56:59],v2 offset:47104
v_fma_f32 v71,v71,v89,v88
v_mfma_f32_16x16x32_bf16 v[24:27],a[64:67],v[32:35],v[24:27]
s_waitcnt vmcnt(9)
ds_read_b128 v[60:63],v2 offset:48128
v_add_f32 v16,v16,v64
v_mfma_f32_16x16x32_bf16 v[28:31],a[68:71],v[32:35],v[28:31]
s_barrier
v_mfma_f32_16x16x32_bf16 v[24:27],a[72:75],v[36:39],v[24:27]
s_add_u32 m0,s71,s88
ds_read_b128 v[32:35],v2 offset:49152
v_add_f32 v17,v17,v65
v_mfma_f32_16x16x32_bf16 v[28:31],a[76:79],v[36:39],v[28:31]
s_mov_b64 s[46:47],s[48:49]
v_add_f32 v18,v18,v66
v_add_f32 v19,v19,v67
v_mfma_f32_16x16x32_bf16 v[24:27],a[80:83],v[40:43],v[24:27]
buffer_load_dwordx4 v10,s[36:39],s67 offen offset:0 sc1 lds
v_mfma_f32_16x16x32_bf16 v[28:31],a[84:87],v[40:43],v[28:31]
s_mov_b64 s[48:49],s[50:51]
ds_read_b128 v[36:39],v2 offset:50176
v_add_f32 v20,v20,v68
v_mfma_f32_16x16x32_bf16 v[24:27],a[88:91],v[44:47],v[24:27]
buffer_load_dword v11,s[36:39],s72 offen offset:1024 sc1 lds
v_mfma_f32_16x16x32_bf16 v[28:31],a[92:95],v[44:47],v[28:31]
s_waitcnt lgkmcnt(2)
ds_read_b128 v[40:43],v2 offset:51200
v_add_f32 v21,v21,v69
v_mfma_f32_16x16x32_bf16 v[24:27],a[96:99],v[48:51],v[24:27]
s_mov_b32 m0,s62
v_add_f32 v22,v22,v70
v_add_f32 v23,v23,v71
v_cvt_pk_bf16_f32 v72,v16,v17
v_cvt_pk_bf16_f32 v73,v18,v19
v_cvt_pk_bf16_f32 v74,v20,v21
v_cvt_pk_bf16_f32 v75,v22,v23
v_mfma_f32_16x16x32_bf16 v[28:31],a[100:103],v[48:51],v[28:31]
s_mov_b64 s[50:51],s[52:53]
ds_read_b128 v[44:47],v2 offset:52224
buffer_store_dwordx4 v[72:75],v3,s[36:39],s73 offen offset:0 sc0 sc1
v_add_f32 v64,v16,v17
v_mul_f32 v65,v16,v16
v_fma_f32 v65,v17,v17,v65
v_mfma_f32_16x16x32_bf16 v[24:27],a[104:107],v[52:55],v[24:27]
ds_read_b128 v[48:51],v2 offset:53248
v_add_f32 v64,v64,v18
v_mfma_f32_16x16x32_bf16 v[28:31],a[108:111],v[52:55],v[28:31]
v_fma_f32 v65,v18,v18,v65
v_add_f32 v64,v64,v19
v_mfma_f32_16x16x32_bf16 v[24:27],a[112:115],v[56:59],v[24:27]
ds_read_b128 v[52:55],v2 offset:54272
v_fma_f32 v65,v19,v19,v65
v_mfma_f32_16x16x32_bf16 v[28:31],a[116:119],v[56:59],v[28:31]
v_add_f32 v64,v64,v20
v_fma_f32 v65,v20,v20,v65
v_add_f32 v64,v64,v21
v_fma_f32 v65,v21,v21,v65
v_add_f32 v64,v64,v22
v_fma_f32 v65,v22,v22,v65
v_add_f32 v64,v64,v23
v_fma_f32 v65,v23,v23,v65
v_mfma_f32_16x16x32_bf16 v[24:27],a[120:123],v[60:63],v[24:27]
s_nop 0
v_permlane16_swap_b32 v64,v65
v_add_f32 v64,v64,v65
v_mov_b32 v65,v64
v_mfma_f32_16x16x32_bf16 v[28:31],a[124:127],v[60:63],v[28:31]
s_waitcnt lgkmcnt(2)
ds_read_b128 v[56:59],v2 offset:55296
v_permlane32_swap_b32 v64,v65
v_add_f32 v64,v64,v65
v_mfma_f32_16x16x32_bf16 v[24:27],a[128:131],v[32:35],v[24:27]
ds_write_b32 v13,v64 offset:2048
v_mfma_f32_16x16x32_bf16 v[28:31],a[132:135],v[32:35],v[28:31]
ds_read_b128 v[60:63],v2 offset:56320
v_mfma_f32_16x16x32_bf16 v[24:27],a[136:139],v[36:39],v[24:27]
ds_read_b128 v[32:35],v2 offset:57344
v_mfma_f32_16x16x32_bf16 v[28:31],a[140:143],v[36:39],v[28:31]
v_mfma_f32_16x16x32_bf16 v[24:27],a[144:147],v[40:43],v[24:27]
s_waitcnt vmcnt(11) lgkmcnt(2)
ds_read_b128 v[36:39],v2 offset:58368
v_mfma_f32_16x16x32_bf16 v[28:31],a[148:151],v[40:43],v[28:31]
s_barrier
v_mfma_f32_16x16x32_bf16 v[24:27],a[152:155],v[44:47],v[24:27]
ds_read_b128 v[40:43],v2 offset:59392
ds_read_b32 v15,v7 offset:256
v_mfma_f32_16x16x32_bf16 v[28:31],a[156:159],v[44:47],v[28:31]
s_waitcnt lgkmcnt(4)
v_mfma_f32_16x16x32_bf16 v[24:27],a[160:163],v[48:51],v[24:27]
ds_read_b128 v[44:47],v2 offset:60416
v_mfma_f32_16x16x32_bf16 v[28:31],a[164:167],v[48:51],v[28:31]
v_mfma_f32_16x16x32_bf16 v[24:27],a[168:171],v[52:55],v[24:27]
s_waitcnt lgkmcnt(1)
ds_read_b128 v[48:51],v2 offset:61440
v_mfma_f32_16x16x32_bf16 v[28:31],a[172:175],v[52:55],v[28:31]
v_cmp_eq_u32 vcc,s60,v15
v_mfma_f32_16x16x32_bf16 v[24:27],a[176:179],v[56:59],v[24:27]
s_cmp_eq_u32 vcc_lo,-1
ds_read_b128 v[52:55],v2 offset:62464
s_cselect_b32 s66,s86,s59
v_mfma_f32_16x16x32_bf16 v[28:31],a[180:183],v[56:59],v[28:31]
buffer_load_dwordx4 v0,s[36:39],s66 offen offset:0 sc1 lds
v_mfma_f32_16x16x32_bf16 v[24:27],a[184:187],v[60:63],v[24:27]
s_xor_b32 s64,s64,65536
ds_read_b128 v[56:59],v2 offset:63488
s_add_u32 s74,s53,s55
v_mfma_f32_16x16x32_bf16 v[28:31],a[188:191],v[60:63],v[28:31]
s_waitcnt lgkmcnt(3)
s_cmp_eq_u32 s74,s56
s_cselect_b32 s74,s57,s74
v_mfma_f32_16x16x32_bf16 v[24:27],a[192:195],v[32:35],v[24:27]
s_waitcnt vmcnt(8)
ds_read_b128 v[60:63],v2 offset:64512
v_xor_b32 v2,65536,v2
v_mfma_f32_16x16x32_bf16 v[28:31],a[196:199],v[32:35],v[28:31]
s_barrier
v_mfma_f32_16x16x32_bf16 v[24:27],a[200:203],v[36:39],v[24:27]
s_cmp_eq_u32 s74,s58
ds_read_b128 v[32:35],v2 offset:0
ds_read_b128 v[84:87],v8 offset:8192
v_mfma_f32_16x16x32_bf16 v[28:31],a[204:207],v[36:39],v[28:31])"
R"(
buffer_load_dwordx4 v0,s[36:39],s66 offen offset:1024 sc1 lds
v_mfma_f32_16x16x32_bf16 v[24:27],a[208:211],v[40:43],v[24:27]
s_cselect_b32 s74,s59,s74
ds_read_b128 v[36:39],v2 offset:1024
ds_read_b64 v[90:91],v9 offset:9216
v_mfma_f32_16x16x32_bf16 v[28:31],a[212:215],v[40:43],v[28:31]
s_mov_b32 s52,s74
v_xor_b32 v8,16384,v8
v_xor_b32 v9,16384,v9
v_mfma_f32_16x16x32_bf16 v[24:27],a[216:219],v[44:47],v[24:27]
s_cmp_lt_u32 s67,s59
ds_read_b128 v[40:43],v2 offset:2048
v_mfma_f32_16x16x32_bf16 v[28:31],a[220:223],v[44:47],v[28:31]
s_waitcnt lgkmcnt(5)
buffer_load_dwordx4 v0,s[36:39],s66 offen offset:2048 sc1 lds
v_mfma_f32_16x16x32_bf16 v[24:27],a[224:227],v[48:51],v[24:27]
ds_read_b128 v[44:47],v2 offset:3072
v_mfma_f32_16x16x32_bf16 v[28:31],a[228:231],v[48:51],v[28:31]
v_mfma_f32_16x16x32_bf16 v[24:27],a[232:235],v[52:55],v[24:27]
s_waitcnt lgkmcnt(2)
ds_read_b128 v[48:51],v2 offset:4096
v_mfma_f32_16x16x32_bf16 v[28:31],a[236:239],v[52:55],v[28:31]
buffer_load_dwordx4 v0,s[36:39],s66 offen offset:3072 sc1 lds
v_mfma_f32_16x16x32_bf16 v[24:27],a[240:243],v[56:59],v[24:27]
ds_read_b128 v[52:55],v2 offset:5120
v_mfma_f32_16x16x32_bf16 v[28:31],a[244:247],v[56:59],v[28:31]
v_mfma_f32_16x16x32_bf16 v[24:27],a[248:251],v[60:63],v[24:27]
s_waitcnt lgkmcnt(2)
ds_read_b128 v[56:59],v2 offset:6144
v_mfma_f32_16x16x32_bf16 v[28:31],a[252:255],v[60:63],v[28:31]
s_cbranch_scc1 Lg2_d0w1_%=
s_branch Lg2_slow0_%=
Lg2_d0w2_%=:
v_mfma_f32_16x16x32_bf16 v[16:19],a[0:3],v[32:35],0
s_add_u32 m0,s62,16384
ds_read_b128 v[60:63],v2 offset:7168
v_lshlrev_b32 v64,16,v84
v_mfma_f32_16x16x32_bf16 v[20:23],a[4:7],v[32:35],0
s_lshr_b32 s73,s47,0
v_and_b32 v65,0xffff0000,v84
v_lshlrev_b32 v66,16,v85
v_mfma_f32_16x16x32_bf16 v[16:19],a[8:11],v[36:39],v[16:19]
s_lshr_b32 s85,s47,5
ds_read_b128 v[32:35],v2 offset:8192
v_and_b32 v67,0xffff0000,v85
v_mfma_f32_16x16x32_bf16 v[20:23],a[12:15],v[36:39],v[20:23]
buffer_load_dwordx4 v1,s[36:39],s66 offen offset:0 sc1 lds
v_mfma_f32_16x16x32_bf16 v[16:19],a[16:19],v[40:43],v[16:19]
s_xor_b32 s87,s64,65536
ds_read_b128 v[36:39],v2 offset:9216
v_lshlrev_b32 v68,16,v86
v_mfma_f32_16x16x32_bf16 v[20:23],a[20:23],v[40:43],v[20:23]
s_cmp_ge_u32 s66,s59
v_and_b32 v69,0xffff0000,v86
v_lshlrev_b32 v70,16,v87
v_mfma_f32_16x16x32_bf16 v[16:19],a[24:27],v[44:47],v[16:19]
s_cselect_b32 s86,s49,s50
ds_read_b128 v[40:43],v2 offset:10240
v_and_b32 v71,0xffff0000,v87
v_mfma_f32_16x16x32_bf16 v[20:23],a[28:31],v[44:47],v[20:23]
s_waitcnt lgkmcnt(3)
buffer_load_dwordx4 v1,s[36:39],s66 offen offset:1024 sc1 lds
v_mfma_f32_16x16x32_bf16 v[16:19],a[32:35],v[48:51],v[16:19]
s_cselect_b32 s87,s87,s63
ds_read_b128 v[44:47],v2 offset:11264
v_fma_f32 v64,v64,v91,v90
v_mfma_f32_16x16x32_bf16 v[20:23],a[36:39],v[48:51],v[20:23]
s_lshr_b32 s72,s66,6
v_fma_f32 v65,v65,v91,v90
v_fma_f32 v66,v66,v91,v90
v_mfma_f32_16x16x32_bf16 v[16:19],a[40:43],v[52:55],v[16:19]
s_lshr_b32 s71,s62,2
ds_read_b128 v[48:51],v2 offset:12288
v_fma_f32 v67,v67,v91,v90
v_mfma_f32_16x16x32_bf16 v[20:23],a[44:47],v[52:55],v[20:23]
buffer_load_dwordx4 v1,s[36:39],s66 offen offset:2048 sc1 lds
v_mfma_f32_16x16x32_bf16 v[16:19],a[48:51],v[56:59],v[16:19]
s_mov_b32 s62,s87
ds_read_b128 v[52:55],v2 offset:13312
v_fma_f32 v68,v68,v91,v90
v_mfma_f32_16x16x32_bf16 v[20:23],a[52:55],v[56:59],v[20:23]
v_fma_f32 v69,v69,v91,v90
v_fma_f32 v70,v70,v91,v90
v_mfma_f32_16x16x32_bf16 v[16:19],a[56:59],v[60:63],v[16:19]
ds_read_b128 v[56:59],v2 offset:14336
v_fma_f32 v71,v71,v91,v90
v_mfma_f32_16x16x32_bf16 v[20:23],a[60:63],v[60:63],v[20:23]
s_waitcnt lgkmcnt(3)
buffer_load_dwordx4 v1,s[36:39],s66 offen offset:3072 sc1 lds
v_mfma_f32_16x16x32_bf16 v[16:19],a[64:67],v[32:35],v[16:19]
s_waitcnt vmcnt(10)
ds_read_b128 v[60:63],v2 offset:15360
v_add_f32 v24,v24,v64
v_mfma_f32_16x16x32_bf16 v[20:23],a[68:71],v[32:35],v[20:23]
s_barrier
v_mfma_f32_16x16x32_bf16 v[16:19],a[72:75],v[36:39],v[16:19]
s_add_u32 m0,s71,s88
ds_read_b128 v[32:35],v2 offset:16384
v_add_f32 v25,v25,v65
v_mfma_f32_16x16x32_bf16 v[20:23],a[76:79],v[36:39],v[20:23]
v_add_f32 v26,v26,v66
v_add_f32 v27,v27,v67
v_mfma_f32_16x16x32_bf16 v[16:19],a[80:83],v[40:43],v[16:19]
ds_read_b128 v[36:39],v2 offset:17408
v_add_f32 v28,v28,v68
v_mfma_f32_16x16x32_bf16 v[20:23],a[84:87],v[40:43],v[20:23]
buffer_load_dwordx4 v10,s[36:39],s66 offen offset:0 sc1 lds
v_mfma_f32_16x16x32_bf16 v[16:19],a[88:91],v[44:47],v[16:19]
buffer_load_dword v11,s[36:39],s72 offen offset:1024 sc1 lds
v_mfma_f32_16x16x32_bf16 v[20:23],a[92:95],v[44:47],v[20:23]
s_waitcnt lgkmcnt(2)
ds_read_b128 v[40:43],v2 offset:18432
v_add_f32 v29,v29,v69
v_mfma_f32_16x16x32_bf16 v[16:19],a[96:99],v[48:51],v[16:19]
s_mov_b32 m0,s62
ds_read_b128 v[44:47],v2 offset:19456
v_add_f32 v30,v30,v70
v_mfma_f32_16x16x32_bf16 v[20:23],a[100:103],v[48:51],v[20:23]
v_add_f32 v31,v31,v71
v_cvt_pk_bf16_f32 v72,v24,v25
v_cvt_pk_bf16_f32 v73,v26,v27
v_cvt_pk_bf16_f32 v74,v28,v29
v_cvt_pk_bf16_f32 v75,v30,v31
v_mfma_f32_16x16x32_bf16 v[16:19],a[104:107],v[52:55],v[16:19]
buffer_store_dwordx4 v[72:75],v3,s[36:39],s73 offen offset:0 sc0 sc1
ds_read_b128 v[48:51],v2 offset:20480
v_add_f32 v64,v24,v25
v_mul_f32 v65,v24,v24
v_mfma_f32_16x16x32_bf16 v[20:23],a[108:111],v[52:55],v[20:23]
v_fma_f32 v65,v25,v25,v65
v_add_f32 v64,v64,v26
v_mfma_f32_16x16x32_bf16 v[16:19],a[112:115],v[56:59],v[16:19]
ds_read_b128 v[52:55],v2 offset:21504
v_fma_f32 v65,v26,v26,v65
v_mfma_f32_16x16x32_bf16 v[20:23],a[116:119],v[56:59],v[20:23]
v_add_f32 v64,v64,v27
v_fma_f32 v65,v27,v27,v65
v_add_f32 v64,v64,v28
v_fma_f32 v65,v28,v28,v65
v_add_f32 v64,v64,v29
v_fma_f32 v65,v29,v29,v65
v_add_f32 v64,v64,v30
v_fma_f32 v65,v30,v30,v65
v_add_f32 v64,v64,v31
v_fma_f32 v65,v31,v31,v65
v_mfma_f32_16x16x32_bf16 v[16:19],a[120:123],v[60:63],v[16:19]
s_nop 0
v_permlane16_swap_b32 v64,v65
v_add_f32 v64,v64,v65
v_mov_b32 v65,v64
v_mfma_f32_16x16x32_bf16 v[20:23],a[124:127],v[60:63],v[20:23]
s_waitcnt lgkmcnt(2)
ds_read_b128 v[56:59],v2 offset:22528
v_permlane32_swap_b32 v64,v65
v_add_f32 v64,v64,v65
v_mfma_f32_16x16x32_bf16 v[16:19],a[128:131],v[32:35],v[16:19]
ds_write_b32 v13,v64 offset:0
v_mfma_f32_16x16x32_bf16 v[20:23],a[132:135],v[32:35],v[20:23]
ds_read_b128 v[60:63],v2 offset:23552
v_mfma_f32_16x16x32_bf16 v[16:19],a[136:139],v[36:39],v[16:19]
ds_read_b128 v[32:35],v2 offset:24576
v_mfma_f32_16x16x32_bf16 v[20:23],a[140:143],v[36:39],v[20:23]
v_mfma_f32_16x16x32_bf16 v[16:19],a[144:147],v[40:43],v[16:19]
s_waitcnt vmcnt(11) lgkmcnt(2)
ds_read_b128 v[36:39],v2 offset:25600
v_mfma_f32_16x16x32_bf16 v[20:23],a[148:151],v[40:43],v[20:23]
s_barrier
v_mfma_f32_16x16x32_bf16 v[16:19],a[152:155],v[44:47],v[16:19]
ds_read_b128 v[40:43],v2 offset:26624
ds_read_b32 v15,v7 offset:0
v_mfma_f32_16x16x32_bf16 v[20:23],a[156:159],v[44:47],v[20:23]
s_waitcnt lgkmcnt(4)
ds_read_b128 v[76:79],v14 offset:0
v_mfma_f32_16x16x32_bf16 v[16:19],a[160:163],v[48:51],v[16:19]
ds_read_b128 v[44:47],v2 offset:27648
v_mfma_f32_16x16x32_bf16 v[20:23],a[164:167],v[48:51],v[20:23]
v_mfma_f32_16x16x32_bf16 v[16:19],a[168:171],v[52:55],v[16:19]
s_waitcnt lgkmcnt(2)
ds_read_b128 v[48:51],v2 offset:28672
v_mfma_f32_16x16x32_bf16 v[20:23],a[172:175],v[52:55],v[20:23]
s_waitcnt lgkmcnt(2)
v_cmp_eq_u32 vcc,s60,v15
v_add_f32 v76,v76,v77
v_add_f32 v78,v78,v79
v_add_f32 v76,v76,v78
v_mfma_f32_16x16x32_bf16 v[16:19],a[176:179],v[56:59],v[16:19]
buffer_store_dword v76,v12,s[36:39],s85 offen offset:0 sc0 sc1
ds_read_b128 v[52:55],v2 offset:29696
v_mfma_f32_16x16x32_bf16 v[20:23],a[180:183],v[56:59],v[20:23]
s_cmp_eq_u32 vcc_lo,-1
s_cselect_b32 s67,s86,s59
s_xor_b32 s63,s63,65536
v_mfma_f32_16x16x32_bf16 v[16:19],a[184:187],v[60:63],v[16:19]
buffer_load_dwordx4 v0,s[36:39],s67 offen offset:0 sc1 lds
v_mfma_f32_16x16x32_bf16 v[20:23],a[188:191],v[60:63],v[20:23]
s_waitcnt lgkmcnt(2)
ds_read_b128 v[56:59],v2 offset:30720
s_add_u32 s53,s52,s55
v_mfma_f32_16x16x32_bf16 v[16:19],a[192:195],v[32:35],v[16:19]
s_waitcnt vmcnt(9))"
R"(
ds_read_b128 v[60:63],v2 offset:31744
s_cmp_eq_u32 s53,s56
v_mfma_f32_16x16x32_bf16 v[20:23],a[196:199],v[32:35],v[20:23]
s_barrier
v_mfma_f32_16x16x32_bf16 v[16:19],a[200:203],v[36:39],v[16:19]
s_cselect_b32 s53,s57,s53
ds_read_b128 v[32:35],v2 offset:32768
ds_read_b128 v[80:83],v8 offset:0
v_mfma_f32_16x16x32_bf16 v[20:23],a[204:207],v[36:39],v[20:23]
s_cmp_eq_u32 s53,s58
ds_read_b64 v[88:89],v9 offset:1024
s_cselect_b32 s53,s59,s53
v_mfma_f32_16x16x32_bf16 v[16:19],a[208:211],v[40:43],v[16:19]
buffer_load_dwordx4 v0,s[36:39],s67 offen offset:1024 sc1 lds
v_mfma_f32_16x16x32_bf16 v[20:23],a[212:215],v[40:43],v[20:23]
s_cmp_ge_u32 s66,s59
ds_read_b128 v[36:39],v2 offset:33792
v_mfma_f32_16x16x32_bf16 v[16:19],a[216:219],v[44:47],v[16:19]
ds_read_b128 v[40:43],v2 offset:34816
v_mfma_f32_16x16x32_bf16 v[20:23],a[220:223],v[44:47],v[20:23]
s_waitcnt lgkmcnt(5)
v_mfma_f32_16x16x32_bf16 v[16:19],a[224:227],v[48:51],v[16:19]
buffer_load_dwordx4 v0,s[36:39],s67 offen offset:2048 sc1 lds
v_mfma_f32_16x16x32_bf16 v[20:23],a[228:231],v[48:51],v[20:23]
s_waitcnt lgkmcnt(2)
ds_read_b128 v[44:47],v2 offset:35840
v_mfma_f32_16x16x32_bf16 v[16:19],a[232:235],v[52:55],v[16:19]
ds_read_b128 v[48:51],v2 offset:36864
v_mfma_f32_16x16x32_bf16 v[20:23],a[236:239],v[52:55],v[20:23]
v_mfma_f32_16x16x32_bf16 v[16:19],a[240:243],v[56:59],v[16:19]
buffer_load_dwordx4 v0,s[36:39],s67 offen offset:3072 sc1 lds
v_mfma_f32_16x16x32_bf16 v[20:23],a[244:247],v[56:59],v[20:23]
ds_read_b128 v[52:55],v2 offset:37888
v_mfma_f32_16x16x32_bf16 v[16:19],a[248:251],v[60:63],v[16:19]
s_waitcnt lgkmcnt(2)
ds_read_b128 v[56:59],v2 offset:38912
v_mfma_f32_16x16x32_bf16 v[20:23],a[252:255],v[60:63],v[20:23]
s_cbranch_scc1 Lg2_slow1_%=
Lg2_d1w2_%=:
v_mfma_f32_16x16x32_bf16 v[24:27],a[0:3],v[32:35],0
s_add_u32 m0,s62,16384
ds_read_b128 v[60:63],v2 offset:39936
v_lshlrev_b32 v64,16,v80
v_mfma_f32_16x16x32_bf16 v[28:31],a[4:7],v[32:35],0
s_lshr_b32 s73,s48,0
v_and_b32 v65,0xffff0000,v80
v_lshlrev_b32 v66,16,v81
v_mfma_f32_16x16x32_bf16 v[24:27],a[8:11],v[36:39],v[24:27]
s_lshr_b32 s85,s48,5
ds_read_b128 v[32:35],v2 offset:40960
v_and_b32 v67,0xffff0000,v81
v_mfma_f32_16x16x32_bf16 v[28:31],a[12:15],v[36:39],v[28:31]
buffer_load_dwordx4 v1,s[36:39],s67 offen offset:0 sc1 lds
v_mfma_f32_16x16x32_bf16 v[24:27],a[16:19],v[40:43],v[24:27]
s_xor_b32 s87,s63,65536
ds_read_b128 v[36:39],v2 offset:41984
v_lshlrev_b32 v68,16,v82
v_mfma_f32_16x16x32_bf16 v[28:31],a[20:23],v[40:43],v[28:31]
s_cmp_ge_u32 s67,s59
v_and_b32 v69,0xffff0000,v82
v_lshlrev_b32 v70,16,v83
v_mfma_f32_16x16x32_bf16 v[24:27],a[24:27],v[44:47],v[24:27]
s_cselect_b32 s86,s50,s51
ds_read_b128 v[40:43],v2 offset:43008
v_and_b32 v71,0xffff0000,v83
v_mfma_f32_16x16x32_bf16 v[28:31],a[28:31],v[44:47],v[28:31]
s_waitcnt lgkmcnt(3)
buffer_load_dwordx4 v1,s[36:39],s67 offen offset:1024 sc1 lds
v_mfma_f32_16x16x32_bf16 v[24:27],a[32:35],v[48:51],v[24:27]
s_cselect_b32 s87,s87,s64
ds_read_b128 v[44:47],v2 offset:44032
v_fma_f32 v64,v64,v89,v88
v_mfma_f32_16x16x32_bf16 v[28:31],a[36:39],v[48:51],v[28:31]
s_lshr_b32 s72,s67,6
v_fma_f32 v65,v65,v89,v88
v_fma_f32 v66,v66,v89,v88
v_mfma_f32_16x16x32_bf16 v[24:27],a[40:43],v[52:55],v[24:27]
s_lshr_b32 s71,s62,2
ds_read_b128 v[48:51],v2 offset:45056
v_fma_f32 v67,v67,v89,v88
v_mfma_f32_16x16x32_bf16 v[28:31],a[44:47],v[52:55],v[28:31]
buffer_load_dwordx4 v1,s[36:39],s67 offen offset:2048 sc1 lds
v_mfma_f32_16x16x32_bf16 v[24:27],a[48:51],v[56:59],v[24:27]
s_mov_b32 s62,s87
ds_read_b128 v[52:55],v2 offset:46080
v_fma_f32 v68,v68,v89,v88
v_mfma_f32_16x16x32_bf16 v[28:31],a[52:55],v[56:59],v[28:31]
s_mov_b64 s[44:45],s[46:47]
v_fma_f32 v69,v69,v89,v88
v_fma_f32 v70,v70,v89,v88
v_mfma_f32_16x16x32_bf16 v[24:27],a[56:59],v[60:63],v[24:27]
s_mov_b64 s[46:47],s[48:49]
ds_read_b128 v[56:59],v2 offset:47104
v_fma_f32 v71,v71,v89,v88
v_mfma_f32_16x16x32_bf16 v[28:31],a[60:63],v[60:63],v[28:31]
s_waitcnt lgkmcnt(3)
buffer_load_dwordx4 v1,s[36:39],s67 offen offset:3072 sc1 lds
v_mfma_f32_16x16x32_bf16 v[24:27],a[64:67],v[32:35],v[24:27]
s_waitcnt vmcnt(10)
ds_read_b128 v[60:63],v2 offset:48128
v_add_f32 v16,v16,v64
v_mfma_f32_16x16x32_bf16 v[28:31],a[68:71],v[32:35],v[28:31]
s_barrier
v_mfma_f32_16x16x32_bf16 v[24:27],a[72:75],v[36:39],v[24:27]
s_add_u32 m0,s71,s88
ds_read_b128 v[32:35],v2 offset:49152
v_add_f32 v17,v17,v65
v_mfma_f32_16x16x32_bf16 v[28:31],a[76:79],v[36:39],v[28:31]
s_mov_b64 s[48:49],s[50:51]
v_add_f32 v18,v18,v66
v_add_f32 v19,v19,v67
v_mfma_f32_16x16x32_bf16 v[24:27],a[80:83],v[40:43],v[24:27]
s_mov_b64 s[50:51],s[52:53]
ds_read_b128 v[36:39],v2 offset:50176
v_add_f32 v20,v20,v68
v_mfma_f32_16x16x32_bf16 v[28:31],a[84:87],v[40:43],v[28:31]
buffer_load_dwordx4 v10,s[36:39],s67 offen offset:0 sc1 lds
v_mfma_f32_16x16x32_bf16 v[24:27],a[88:91],v[44:47],v[24:27]
buffer_load_dword v11,s[36:39],s72 offen offset:1024 sc1 lds
v_mfma_f32_16x16x32_bf16 v[28:31],a[92:95],v[44:47],v[28:31]
s_waitcnt lgkmcnt(2)
ds_read_b128 v[40:43],v2 offset:51200
v_add_f32 v21,v21,v69
v_mfma_f32_16x16x32_bf16 v[24:27],a[96:99],v[48:51],v[24:27]
s_mov_b32 m0,s62
ds_read_b128 v[44:47],v2 offset:52224
v_add_f32 v22,v22,v70
v_mfma_f32_16x16x32_bf16 v[28:31],a[100:103],v[48:51],v[28:31]
v_add_f32 v23,v23,v71
v_cvt_pk_bf16_f32 v72,v16,v17
v_cvt_pk_bf16_f32 v73,v18,v19
v_cvt_pk_bf16_f32 v74,v20,v21
v_cvt_pk_bf16_f32 v75,v22,v23
v_mfma_f32_16x16x32_bf16 v[24:27],a[104:107],v[52:55],v[24:27]
buffer_store_dwordx4 v[72:75],v3,s[36:39],s73 offen offset:0 sc0 sc1
ds_read_b128 v[48:51],v2 offset:53248
v_add_f32 v64,v16,v17
v_mul_f32 v65,v16,v16
v_mfma_f32_16x16x32_bf16 v[28:31],a[108:111],v[52:55],v[28:31]
v_fma_f32 v65,v17,v17,v65
v_add_f32 v64,v64,v18
v_mfma_f32_16x16x32_bf16 v[24:27],a[112:115],v[56:59],v[24:27]
ds_read_b128 v[52:55],v2 offset:54272
v_fma_f32 v65,v18,v18,v65
v_mfma_f32_16x16x32_bf16 v[28:31],a[116:119],v[56:59],v[28:31]
v_add_f32 v64,v64,v19
v_fma_f32 v65,v19,v19,v65
v_add_f32 v64,v64,v20
v_fma_f32 v65,v20,v20,v65
v_add_f32 v64,v64,v21
v_fma_f32 v65,v21,v21,v65
v_add_f32 v64,v64,v22
v_fma_f32 v65,v22,v22,v65
v_add_f32 v64,v64,v23
v_fma_f32 v65,v23,v23,v65
v_mfma_f32_16x16x32_bf16 v[24:27],a[120:123],v[60:63],v[24:27]
s_nop 0
v_permlane16_swap_b32 v64,v65
v_add_f32 v64,v64,v65
v_mov_b32 v65,v64
v_mfma_f32_16x16x32_bf16 v[28:31],a[124:127],v[60:63],v[28:31]
s_waitcnt lgkmcnt(2)
ds_read_b128 v[56:59],v2 offset:55296
v_permlane32_swap_b32 v64,v65
v_add_f32 v64,v64,v65
v_mfma_f32_16x16x32_bf16 v[24:27],a[128:131],v[32:35],v[24:27]
ds_write_b32 v13,v64 offset:2048
v_mfma_f32_16x16x32_bf16 v[28:31],a[132:135],v[32:35],v[28:31]
ds_read_b128 v[60:63],v2 offset:56320
v_mfma_f32_16x16x32_bf16 v[24:27],a[136:139],v[36:39],v[24:27]
ds_read_b128 v[32:35],v2 offset:57344
v_mfma_f32_16x16x32_bf16 v[28:31],a[140:143],v[36:39],v[28:31]
v_mfma_f32_16x16x32_bf16 v[24:27],a[144:147],v[40:43],v[24:27]
s_waitcnt vmcnt(11) lgkmcnt(2)
ds_read_b128 v[36:39],v2 offset:58368
v_mfma_f32_16x16x32_bf16 v[28:31],a[148:151],v[40:43],v[28:31]
s_barrier
v_mfma_f32_16x16x32_bf16 v[24:27],a[152:155],v[44:47],v[24:27]
ds_read_b128 v[40:43],v2 offset:59392
ds_read_b32 v15,v7 offset:256
v_mfma_f32_16x16x32_bf16 v[28:31],a[156:159],v[44:47],v[28:31]
s_waitcnt lgkmcnt(4)
ds_read_b128 v[76:79],v14 offset:2048
v_mfma_f32_16x16x32_bf16 v[24:27],a[160:163],v[48:51],v[24:27]
ds_read_b128 v[44:47],v2 offset:60416
v_mfma_f32_16x16x32_bf16 v[28:31],a[164:167],v[48:51],v[28:31]
v_mfma_f32_16x16x32_bf16 v[24:27],a[168:171],v[52:55],v[24:27]
s_waitcnt lgkmcnt(2)
ds_read_b128 v[48:51],v2 offset:61440
v_mfma_f32_16x16x32_bf16 v[28:31],a[172:175],v[52:55],v[28:31]
s_waitcnt lgkmcnt(2)
v_cmp_eq_u32 vcc,s60,v15
v_add_f32 v76,v76,v77
v_add_f32 v78,v78,v79
v_add_f32 v76,v76,v78
v_mfma_f32_16x16x32_bf16 v[24:27],a[176:179],v[56:59],v[24:27]
buffer_store_dword v76,v12,s[36:39],s85 offen offset:0 sc0 sc1
ds_read_b128 v[52:55],v2 offset:62464
v_mfma_f32_16x16x32_bf16 v[28:31],a[180:183],v[56:59],v[28:31]
s_cmp_eq_u32 vcc_lo,-1)"
R"(
s_cselect_b32 s66,s86,s59
s_xor_b32 s64,s64,65536
v_mfma_f32_16x16x32_bf16 v[24:27],a[184:187],v[60:63],v[24:27]
buffer_load_dwordx4 v0,s[36:39],s66 offen offset:0 sc1 lds
v_mfma_f32_16x16x32_bf16 v[28:31],a[188:191],v[60:63],v[28:31]
s_waitcnt lgkmcnt(2)
ds_read_b128 v[56:59],v2 offset:63488
s_add_u32 s74,s53,s55
v_mfma_f32_16x16x32_bf16 v[24:27],a[192:195],v[32:35],v[24:27]
s_waitcnt vmcnt(9)
ds_read_b128 v[60:63],v2 offset:64512
v_xor_b32 v2,65536,v2
v_mfma_f32_16x16x32_bf16 v[28:31],a[196:199],v[32:35],v[28:31]
s_barrier
v_mfma_f32_16x16x32_bf16 v[24:27],a[200:203],v[36:39],v[24:27]
s_cmp_eq_u32 s74,s56
ds_read_b128 v[32:35],v2 offset:0
ds_read_b128 v[84:87],v8 offset:8192
v_mfma_f32_16x16x32_bf16 v[28:31],a[204:207],v[36:39],v[28:31]
s_cselect_b32 s74,s57,s74
ds_read_b64 v[90:91],v9 offset:9216
v_xor_b32 v8,16384,v8
v_mfma_f32_16x16x32_bf16 v[24:27],a[208:211],v[40:43],v[24:27]
buffer_load_dwordx4 v0,s[36:39],s66 offen offset:1024 sc1 lds
v_mfma_f32_16x16x32_bf16 v[28:31],a[212:215],v[40:43],v[28:31]
s_cmp_eq_u32 s74,s58
ds_read_b128 v[36:39],v2 offset:1024
v_xor_b32 v9,16384,v9
v_mfma_f32_16x16x32_bf16 v[24:27],a[216:219],v[44:47],v[24:27]
s_cselect_b32 s74,s59,s74
ds_read_b128 v[40:43],v2 offset:2048
s_mov_b32 s52,s74
v_mfma_f32_16x16x32_bf16 v[28:31],a[220:223],v[44:47],v[28:31]
s_waitcnt lgkmcnt(5)
s_cmp_lt_u32 s67,s59
v_mfma_f32_16x16x32_bf16 v[24:27],a[224:227],v[48:51],v[24:27]
buffer_load_dwordx4 v0,s[36:39],s66 offen offset:2048 sc1 lds
v_mfma_f32_16x16x32_bf16 v[28:31],a[228:231],v[48:51],v[28:31]
s_waitcnt lgkmcnt(2)
ds_read_b128 v[44:47],v2 offset:3072
v_mfma_f32_16x16x32_bf16 v[24:27],a[232:235],v[52:55],v[24:27]
ds_read_b128 v[48:51],v2 offset:4096
v_mfma_f32_16x16x32_bf16 v[28:31],a[236:239],v[52:55],v[28:31]
v_mfma_f32_16x16x32_bf16 v[24:27],a[240:243],v[56:59],v[24:27]
buffer_load_dwordx4 v0,s[36:39],s66 offen offset:3072 sc1 lds
v_mfma_f32_16x16x32_bf16 v[28:31],a[244:247],v[56:59],v[28:31]
ds_read_b128 v[52:55],v2 offset:5120
v_mfma_f32_16x16x32_bf16 v[24:27],a[248:251],v[60:63],v[24:27]
s_waitcnt lgkmcnt(2)
ds_read_b128 v[56:59],v2 offset:6144
v_mfma_f32_16x16x32_bf16 v[28:31],a[252:255],v[60:63],v[28:31]
s_cbranch_scc1 Lg2_d0w2_%=
s_branch Lg2_slow0_%=
Lg2_d0w3_%=:
v_mfma_f32_16x16x32_bf16 v[16:19],a[0:3],v[32:35],0
s_add_u32 m0,s62,16384
ds_read_b128 v[60:63],v2 offset:7168
v_lshlrev_b32 v64,16,v84
v_mfma_f32_16x16x32_bf16 v[20:23],a[4:7],v[32:35],0
s_lshr_b32 s73,s47,0
v_and_b32 v65,0xffff0000,v84
v_lshlrev_b32 v66,16,v85
v_mfma_f32_16x16x32_bf16 v[16:19],a[8:11],v[36:39],v[16:19]
s_xor_b32 s87,s64,65536
ds_read_b128 v[32:35],v2 offset:8192
v_and_b32 v67,0xffff0000,v85
v_mfma_f32_16x16x32_bf16 v[20:23],a[12:15],v[36:39],v[20:23]
s_cmp_ge_u32 s66,s59
v_lshlrev_b32 v68,16,v86
v_and_b32 v69,0xffff0000,v86
v_mfma_f32_16x16x32_bf16 v[16:19],a[16:19],v[40:43],v[16:19]
buffer_load_dwordx4 v1,s[36:39],s66 offen offset:0 sc1 lds
v_mfma_f32_16x16x32_bf16 v[20:23],a[20:23],v[40:43],v[20:23]
s_cselect_b32 s86,s49,s50
ds_read_b128 v[36:39],v2 offset:9216
v_lshlrev_b32 v70,16,v87
v_mfma_f32_16x16x32_bf16 v[16:19],a[24:27],v[44:47],v[16:19]
s_cselect_b32 s87,s87,s63
ds_read_b128 v[40:43],v2 offset:10240
v_and_b32 v71,0xffff0000,v87
v_mfma_f32_16x16x32_bf16 v[20:23],a[28:31],v[44:47],v[20:23]
s_waitcnt lgkmcnt(3)
v_fma_f32 v64,v64,v91,v90
v_fma_f32 v65,v65,v91,v90
v_mfma_f32_16x16x32_bf16 v[16:19],a[32:35],v[48:51],v[16:19]
buffer_load_dwordx4 v1,s[36:39],s66 offen offset:1024 sc1 lds
v_mfma_f32_16x16x32_bf16 v[20:23],a[36:39],v[48:51],v[20:23]
s_lshr_b32 s72,s66,6
ds_read_b128 v[44:47],v2 offset:11264
v_fma_f32 v66,v66,v91,v90
v_mfma_f32_16x16x32_bf16 v[16:19],a[40:43],v[52:55],v[16:19]
s_lshr_b32 s71,s62,2
ds_read_b128 v[48:51],v2 offset:12288
v_fma_f32 v67,v67,v91,v90
v_mfma_f32_16x16x32_bf16 v[20:23],a[44:47],v[52:55],v[20:23]
s_mov_b32 s62,s87
v_fma_f32 v68,v68,v91,v90
v_fma_f32 v69,v69,v91,v90
v_mfma_f32_16x16x32_bf16 v[16:19],a[48:51],v[56:59],v[16:19]
buffer_load_dwordx4 v1,s[36:39],s66 offen offset:2048 sc1 lds
v_mfma_f32_16x16x32_bf16 v[20:23],a[52:55],v[56:59],v[20:23]
ds_read_b128 v[52:55],v2 offset:13312
v_fma_f32 v70,v70,v91,v90
v_mfma_f32_16x16x32_bf16 v[16:19],a[56:59],v[60:63],v[16:19]
ds_read_b128 v[56:59],v2 offset:14336
v_fma_f32 v71,v71,v91,v90
v_mfma_f32_16x16x32_bf16 v[20:23],a[60:63],v[60:63],v[20:23]
s_waitcnt lgkmcnt(3)
v_add_f32 v24,v24,v64
v_add_f32 v25,v25,v65
v_mfma_f32_16x16x32_bf16 v[16:19],a[64:67],v[32:35],v[16:19]
s_waitcnt vmcnt(9)
ds_read_b128 v[60:63],v2 offset:15360
v_add_f32 v26,v26,v66
v_mfma_f32_16x16x32_bf16 v[20:23],a[68:71],v[32:35],v[20:23]
s_barrier
v_mfma_f32_16x16x32_bf16 v[16:19],a[72:75],v[36:39],v[16:19]
buffer_load_dwordx4 v1,s[36:39],s66 offen offset:3072 sc1 lds
v_mfma_f32_16x16x32_bf16 v[20:23],a[76:79],v[36:39],v[20:23]
s_add_u32 m0,s71,s88
ds_read_b128 v[32:35],v2 offset:16384
v_add_f32 v27,v27,v67
v_mfma_f32_16x16x32_bf16 v[16:19],a[80:83],v[40:43],v[16:19]
s_lshr_b32 s70,s46,5
ds_read_b128 v[36:39],v2 offset:17408
v_add_f32 v28,v28,v68
v_mfma_f32_16x16x32_bf16 v[20:23],a[84:87],v[40:43],v[20:23]
v_add_f32 v29,v29,v69
v_add_f32 v30,v30,v70
v_mfma_f32_16x16x32_bf16 v[16:19],a[88:91],v[44:47],v[16:19]
buffer_load_dwordx4 v10,s[36:39],s66 offen offset:0 sc1 lds
v_mfma_f32_16x16x32_bf16 v[20:23],a[92:95],v[44:47],v[20:23]
s_waitcnt lgkmcnt(2)
ds_read_b128 v[40:43],v2 offset:18432
v_add_f32 v31,v31,v71
v_mfma_f32_16x16x32_bf16 v[16:19],a[96:99],v[48:51],v[16:19]
buffer_load_dword v11,s[36:39],s72 offen offset:1024 sc1 lds
v_mfma_f32_16x16x32_bf16 v[20:23],a[100:103],v[48:51],v[20:23]
s_mov_b32 m0,s62
ds_read_b128 v[44:47],v2 offset:19456
v_cvt_pk_bf16_f32 v72,v24,v25
v_mfma_f32_16x16x32_bf16 v[16:19],a[104:107],v[52:55],v[16:19]
v_cvt_pk_bf16_f32 v73,v26,v27
v_cvt_pk_bf16_f32 v74,v28,v29
v_cvt_pk_bf16_f32 v75,v30,v31
v_mfma_f32_16x16x32_bf16 v[20:23],a[108:111],v[52:55],v[20:23]
ds_read_b128 v[48:51],v2 offset:20480
buffer_store_dwordx4 v[72:75],v3,s[36:39],s73 offen offset:0 sc0 sc1
v_add_f32 v64,v24,v25
v_mul_f32 v65,v24,v24
v_fma_f32 v65,v25,v25,v65
v_mfma_f32_16x16x32_bf16 v[16:19],a[112:115],v[56:59],v[16:19]
ds_read_b128 v[52:55],v2 offset:21504
v_add_f32 v64,v64,v26
v_mfma_f32_16x16x32_bf16 v[20:23],a[116:119],v[56:59],v[20:23]
v_fma_f32 v65,v26,v26,v65
v_add_f32 v64,v64,v27
v_fma_f32 v65,v27,v27,v65
v_add_f32 v64,v64,v28
v_fma_f32 v65,v28,v28,v65
v_add_f32 v64,v64,v29
v_fma_f32 v65,v29,v29,v65
v_add_f32 v64,v64,v30
v_fma_f32 v65,v30,v30,v65
v_add_f32 v64,v64,v31
v_fma_f32 v65,v31,v31,v65
v_mfma_f32_16x16x32_bf16 v[16:19],a[120:123],v[60:63],v[16:19]
s_nop 0
v_permlane16_swap_b32 v64,v65
v_add_f32 v64,v64,v65
v_mov_b32 v65,v64
v_mfma_f32_16x16x32_bf16 v[20:23],a[124:127],v[60:63],v[20:23]
s_waitcnt lgkmcnt(2)
ds_read_b128 v[56:59],v2 offset:22528
v_permlane32_swap_b32 v64,v65
v_add_f32 v64,v64,v65
v_mfma_f32_16x16x32_bf16 v[16:19],a[128:131],v[32:35],v[16:19]
ds_write_b32 v13,v64 offset:0
v_mfma_f32_16x16x32_bf16 v[20:23],a[132:135],v[32:35],v[20:23]
ds_read_b128 v[60:63],v2 offset:23552
v_mfma_f32_16x16x32_bf16 v[16:19],a[136:139],v[36:39],v[16:19]
ds_read_b128 v[32:35],v2 offset:24576
v_mfma_f32_16x16x32_bf16 v[20:23],a[140:143],v[36:39],v[20:23]
v_mfma_f32_16x16x32_bf16 v[16:19],a[144:147],v[40:43],v[16:19]
s_waitcnt vmcnt(12) lgkmcnt(2)
ds_read_b128 v[36:39],v2 offset:25600
v_mfma_f32_16x16x32_bf16 v[20:23],a[148:151],v[40:43],v[20:23]
s_barrier
v_mfma_f32_16x16x32_bf16 v[16:19],a[152:155],v[44:47],v[16:19]
buffer_store_dword v5,v4,s[36:39],s70 offen offset:0 sc0 sc1
ds_read_b128 v[40:43],v2 offset:26624
ds_read_b32 v15,v7 offset:0
v_mfma_f32_16x16x32_bf16 v[20:23],a[156:159],v[44:47],v[20:23]
s_waitcnt lgkmcnt(4)
v_mfma_f32_16x16x32_bf16 v[16:19],a[160:163],v[48:51],v[16:19]
ds_read_b128 v[44:47],v2 offset:27648
v_mfma_f32_16x16x32_bf16 v[20:23],a[164:167],v[48:51],v[20:23]
v_mfma_f32_16x16x32_bf16 v[16:19],a[168:171],v[52:55],v[16:19]
s_waitcnt lgkmcnt(1)
ds_read_b128 v[48:51],v2 offset:28672)"
R"(
v_mfma_f32_16x16x32_bf16 v[20:23],a[172:175],v[52:55],v[20:23]
v_cmp_eq_u32 vcc,s60,v15
v_mfma_f32_16x16x32_bf16 v[16:19],a[176:179],v[56:59],v[16:19]
s_cmp_eq_u32 vcc_lo,-1
ds_read_b128 v[52:55],v2 offset:29696
s_cselect_b32 s67,s86,s59
v_mfma_f32_16x16x32_bf16 v[20:23],a[180:183],v[56:59],v[20:23]
s_xor_b32 s63,s63,65536
s_add_u32 s53,s52,s55
s_cmp_eq_u32 s53,s56
v_mfma_f32_16x16x32_bf16 v[16:19],a[184:187],v[60:63],v[16:19]
s_cselect_b32 s53,s57,s53
ds_read_b128 v[56:59],v2 offset:30720
s_cmp_eq_u32 s53,s58
v_mfma_f32_16x16x32_bf16 v[20:23],a[188:191],v[60:63],v[20:23]
s_waitcnt lgkmcnt(3)
buffer_load_dwordx4 v0,s[36:39],s67 offen offset:0 sc1 lds
v_mfma_f32_16x16x32_bf16 v[16:19],a[192:195],v[32:35],v[16:19]
s_waitcnt vmcnt(9)
ds_read_b128 v[60:63],v2 offset:31744
s_cselect_b32 s53,s59,s53
v_mfma_f32_16x16x32_bf16 v[20:23],a[196:199],v[32:35],v[20:23]
s_barrier
v_mfma_f32_16x16x32_bf16 v[16:19],a[200:203],v[36:39],v[16:19]
s_cmp_ge_u32 s66,s59
ds_read_b128 v[32:35],v2 offset:32768
ds_read_b128 v[80:83],v8 offset:0
v_mfma_f32_16x16x32_bf16 v[20:23],a[204:207],v[36:39],v[20:23]
ds_read_b64 v[88:89],v9 offset:1024
v_mfma_f32_16x16x32_bf16 v[16:19],a[208:211],v[40:43],v[16:19]
ds_read_b128 v[36:39],v2 offset:33792
v_mfma_f32_16x16x32_bf16 v[20:23],a[212:215],v[40:43],v[20:23]
buffer_load_dwordx4 v0,s[36:39],s67 offen offset:1024 sc1 lds
v_mfma_f32_16x16x32_bf16 v[16:19],a[216:219],v[44:47],v[16:19]
ds_read_b128 v[40:43],v2 offset:34816
v_mfma_f32_16x16x32_bf16 v[20:23],a[220:223],v[44:47],v[20:23]
s_waitcnt lgkmcnt(5)
v_mfma_f32_16x16x32_bf16 v[16:19],a[224:227],v[48:51],v[16:19]
ds_read_b128 v[44:47],v2 offset:35840
v_mfma_f32_16x16x32_bf16 v[20:23],a[228:231],v[48:51],v[20:23]
s_waitcnt lgkmcnt(3)
buffer_load_dwordx4 v0,s[36:39],s67 offen offset:2048 sc1 lds
v_mfma_f32_16x16x32_bf16 v[16:19],a[232:235],v[52:55],v[16:19]
ds_read_b128 v[48:51],v2 offset:36864
v_mfma_f32_16x16x32_bf16 v[20:23],a[236:239],v[52:55],v[20:23]
v_mfma_f32_16x16x32_bf16 v[16:19],a[240:243],v[56:59],v[16:19]
ds_read_b128 v[52:55],v2 offset:37888
v_mfma_f32_16x16x32_bf16 v[20:23],a[244:247],v[56:59],v[20:23]
buffer_load_dwordx4 v0,s[36:39],s67 offen offset:3072 sc1 lds
v_mfma_f32_16x16x32_bf16 v[16:19],a[248:251],v[60:63],v[16:19]
s_waitcnt lgkmcnt(2)
ds_read_b128 v[56:59],v2 offset:38912
v_mfma_f32_16x16x32_bf16 v[20:23],a[252:255],v[60:63],v[20:23]
s_cbranch_scc1 Lg2_slow1_%=
Lg2_d1w3_%=:
v_mfma_f32_16x16x32_bf16 v[24:27],a[0:3],v[32:35],0
s_add_u32 m0,s62,16384
ds_read_b128 v[60:63],v2 offset:39936
v_lshlrev_b32 v64,16,v80
v_mfma_f32_16x16x32_bf16 v[28:31],a[4:7],v[32:35],0
s_lshr_b32 s73,s48,0
v_and_b32 v65,0xffff0000,v80
v_lshlrev_b32 v66,16,v81
v_mfma_f32_16x16x32_bf16 v[24:27],a[8:11],v[36:39],v[24:27]
s_xor_b32 s87,s63,65536
ds_read_b128 v[32:35],v2 offset:40960
v_and_b32 v67,0xffff0000,v81
v_mfma_f32_16x16x32_bf16 v[28:31],a[12:15],v[36:39],v[28:31]
s_cmp_ge_u32 s67,s59
v_lshlrev_b32 v68,16,v82
v_and_b32 v69,0xffff0000,v82
v_mfma_f32_16x16x32_bf16 v[24:27],a[16:19],v[40:43],v[24:27]
buffer_load_dwordx4 v1,s[36:39],s67 offen offset:0 sc1 lds
v_mfma_f32_16x16x32_bf16 v[28:31],a[20:23],v[40:43],v[28:31]
s_cselect_b32 s86,s50,s51
ds_read_b128 v[36:39],v2 offset:41984
v_lshlrev_b32 v70,16,v83
v_mfma_f32_16x16x32_bf16 v[24:27],a[24:27],v[44:47],v[24:27]
s_cselect_b32 s87,s87,s64
ds_read_b128 v[40:43],v2 offset:43008
v_and_b32 v71,0xffff0000,v83
v_mfma_f32_16x16x32_bf16 v[28:31],a[28:31],v[44:47],v[28:31]
s_waitcnt lgkmcnt(3)
v_fma_f32 v64,v64,v89,v88
v_fma_f32 v65,v65,v89,v88
v_mfma_f32_16x16x32_bf16 v[24:27],a[32:35],v[48:51],v[24:27]
buffer_load_dwordx4 v1,s[36:39],s67 offen offset:1024 sc1 lds
v_mfma_f32_16x16x32_bf16 v[28:31],a[36:39],v[48:51],v[28:31]
s_lshr_b32 s72,s67,6
ds_read_b128 v[44:47],v2 offset:44032
v_fma_f32 v66,v66,v89,v88
v_mfma_f32_16x16x32_bf16 v[24:27],a[40:43],v[52:55],v[24:27]
s_lshr_b32 s71,s62,2
ds_read_b128 v[48:51],v2 offset:45056
v_fma_f32 v67,v67,v89,v88
v_mfma_f32_16x16x32_bf16 v[28:31],a[44:47],v[52:55],v[28:31]
s_mov_b32 s62,s87
v_fma_f32 v68,v68,v89,v88
v_fma_f32 v69,v69,v89,v88
v_mfma_f32_16x16x32_bf16 v[24:27],a[48:51],v[56:59],v[24:27]
buffer_load_dwordx4 v1,s[36:39],s67 offen offset:2048 sc1 lds
v_mfma_f32_16x16x32_bf16 v[28:31],a[52:55],v[56:59],v[28:31]
s_mov_b64 s[44:45],s[46:47]
ds_read_b128 v[52:55],v2 offset:46080
v_fma_f32 v70,v70,v89,v88
v_mfma_f32_16x16x32_bf16 v[24:27],a[56:59],v[60:63],v[24:27]
ds_read_b128 v[56:59],v2 offset:47104
v_fma_f32 v71,v71,v89,v88
v_mfma_f32_16x16x32_bf16 v[28:31],a[60:63],v[60:63],v[28:31]
s_waitcnt lgkmcnt(3)
v_add_f32 v16,v16,v64
v_add_f32 v17,v17,v65
v_mfma_f32_16x16x32_bf16 v[24:27],a[64:67],v[32:35],v[24:27]
s_waitcnt vmcnt(9)
ds_read_b128 v[60:63],v2 offset:48128
v_add_f32 v18,v18,v66
v_mfma_f32_16x16x32_bf16 v[28:31],a[68:71],v[32:35],v[28:31]
s_barrier
v_mfma_f32_16x16x32_bf16 v[24:27],a[72:75],v[36:39],v[24:27]
buffer_load_dwordx4 v1,s[36:39],s67 offen offset:3072 sc1 lds
v_mfma_f32_16x16x32_bf16 v[28:31],a[76:79],v[36:39],v[28:31]
s_add_u32 m0,s71,s88
ds_read_b128 v[32:35],v2 offset:49152
v_add_f32 v19,v19,v67
v_mfma_f32_16x16x32_bf16 v[24:27],a[80:83],v[40:43],v[24:27]
s_lshr_b32 s70,s47,5
ds_read_b128 v[36:39],v2 offset:50176
v_add_f32 v20,v20,v68
v_mfma_f32_16x16x32_bf16 v[28:31],a[84:87],v[40:43],v[28:31]
s_mov_b64 s[46:47],s[48:49]
v_add_f32 v21,v21,v69
v_add_f32 v22,v22,v70
v_mfma_f32_16x16x32_bf16 v[24:27],a[88:91],v[44:47],v[24:27]
buffer_load_dwordx4 v10,s[36:39],s67 offen offset:0 sc1 lds
v_mfma_f32_16x16x32_bf16 v[28:31],a[92:95],v[44:47],v[28:31]
s_waitcnt lgkmcnt(2)
ds_read_b128 v[40:43],v2 offset:51200
v_add_f32 v23,v23,v71
v_mfma_f32_16x16x32_bf16 v[24:27],a[96:99],v[48:51],v[24:27]
buffer_load_dword v11,s[36:39],s72 offen offset:1024 sc1 lds
v_mfma_f32_16x16x32_bf16 v[28:31],a[100:103],v[48:51],v[28:31]
s_mov_b32 m0,s62
ds_read_b128 v[44:47],v2 offset:52224
v_cvt_pk_bf16_f32 v72,v16,v17
v_mfma_f32_16x16x32_bf16 v[24:27],a[104:107],v[52:55],v[24:27]
s_mov_b64 s[48:49],s[50:51]
v_cvt_pk_bf16_f32 v73,v18,v19
v_cvt_pk_bf16_f32 v74,v20,v21
v_cvt_pk_bf16_f32 v75,v22,v23
v_mfma_f32_16x16x32_bf16 v[28:31],a[108:111],v[52:55],v[28:31]
s_mov_b64 s[50:51],s[52:53]
ds_read_b128 v[48:51],v2 offset:53248
buffer_store_dwordx4 v[72:75],v3,s[36:39],s73 offen offset:0 sc0 sc1
v_add_f32 v64,v16,v17
v_mul_f32 v65,v16,v16
v_fma_f32 v65,v17,v17,v65
v_mfma_f32_16x16x32_bf16 v[24:27],a[112:115],v[56:59],v[24:27]
ds_read_b128 v[52:55],v2 offset:54272
v_add_f32 v64,v64,v18
v_mfma_f32_16x16x32_bf16 v[28:31],a[116:119],v[56:59],v[28:31]
v_fma_f32 v65,v18,v18,v65
v_add_f32 v64,v64,v19
v_fma_f32 v65,v19,v19,v65
v_add_f32 v64,v64,v20
v_fma_f32 v65,v20,v20,v65
v_add_f32 v64,v64,v21
v_fma_f32 v65,v21,v21,v65
v_add_f32 v64,v64,v22
v_fma_f32 v65,v22,v22,v65
v_add_f32 v64,v64,v23
v_fma_f32 v65,v23,v23,v65
v_mfma_f32_16x16x32_bf16 v[24:27],a[120:123],v[60:63],v[24:27]
s_nop 0
v_permlane16_swap_b32 v64,v65
v_add_f32 v64,v64,v65
v_mov_b32 v65,v64
v_mfma_f32_16x16x32_bf16 v[28:31],a[124:127],v[60:63],v[28:31]
s_waitcnt lgkmcnt(2)
ds_read_b128 v[56:59],v2 offset:55296
v_permlane32_swap_b32 v64,v65
v_add_f32 v64,v64,v65
v_mfma_f32_16x16x32_bf16 v[24:27],a[128:131],v[32:35],v[24:27]
ds_write_b32 v13,v64 offset:2048
v_mfma_f32_16x16x32_bf16 v[28:31],a[132:135],v[32:35],v[28:31]
ds_read_b128 v[60:63],v2 offset:56320
v_mfma_f32_16x16x32_bf16 v[24:27],a[136:139],v[36:39],v[24:27]
ds_read_b128 v[32:35],v2 offset:57344
v_mfma_f32_16x16x32_bf16 v[28:31],a[140:143],v[36:39],v[28:31]
v_mfma_f32_16x16x32_bf16 v[24:27],a[144:147],v[40:43],v[24:27]
s_waitcnt vmcnt(12) lgkmcnt(2)
ds_read_b128 v[36:39],v2 offset:58368
v_mfma_f32_16x16x32_bf16 v[28:31],a[148:151],v[40:43],v[28:31]
s_barrier
v_mfma_f32_16x16x32_bf16 v[24:27],a[152:155],v[44:47],v[24:27]
buffer_store_dword v5,v4,s[36:39],s70 offen offset:0 sc0 sc1
ds_read_b128 v[40:43],v2 offset:59392
ds_read_b32 v15,v7 offset:256
v_mfma_f32_16x16x32_bf16 v[28:31],a[156:159],v[44:47],v[28:31]
s_waitcnt lgkmcnt(4)
v_mfma_f32_16x16x32_bf16 v[24:27],a[160:163],v[48:51],v[24:27])"
R"(
ds_read_b128 v[44:47],v2 offset:60416
v_mfma_f32_16x16x32_bf16 v[28:31],a[164:167],v[48:51],v[28:31]
v_mfma_f32_16x16x32_bf16 v[24:27],a[168:171],v[52:55],v[24:27]
s_waitcnt lgkmcnt(1)
ds_read_b128 v[48:51],v2 offset:61440
v_mfma_f32_16x16x32_bf16 v[28:31],a[172:175],v[52:55],v[28:31]
v_cmp_eq_u32 vcc,s60,v15
v_mfma_f32_16x16x32_bf16 v[24:27],a[176:179],v[56:59],v[24:27]
s_cmp_eq_u32 vcc_lo,-1
ds_read_b128 v[52:55],v2 offset:62464
s_cselect_b32 s66,s86,s59
v_mfma_f32_16x16x32_bf16 v[28:31],a[180:183],v[56:59],v[28:31]
s_xor_b32 s64,s64,65536
s_add_u32 s74,s53,s55
s_cmp_eq_u32 s74,s56
v_mfma_f32_16x16x32_bf16 v[24:27],a[184:187],v[60:63],v[24:27]
s_cselect_b32 s74,s57,s74
ds_read_b128 v[56:59],v2 offset:63488
s_cmp_eq_u32 s74,s58
v_mfma_f32_16x16x32_bf16 v[28:31],a[188:191],v[60:63],v[28:31]
s_waitcnt lgkmcnt(3)
buffer_load_dwordx4 v0,s[36:39],s66 offen offset:0 sc1 lds
v_mfma_f32_16x16x32_bf16 v[24:27],a[192:195],v[32:35],v[24:27]
s_waitcnt vmcnt(9)
ds_read_b128 v[60:63],v2 offset:64512
v_xor_b32 v2,65536,v2
v_mfma_f32_16x16x32_bf16 v[28:31],a[196:199],v[32:35],v[28:31]
s_barrier
v_mfma_f32_16x16x32_bf16 v[24:27],a[200:203],v[36:39],v[24:27]
s_cselect_b32 s74,s59,s74
ds_read_b128 v[32:35],v2 offset:0
ds_read_b128 v[84:87],v8 offset:8192
v_mfma_f32_16x16x32_bf16 v[28:31],a[204:207],v[36:39],v[28:31]
s_mov_b32 s52,s74
ds_read_b64 v[90:91],v9 offset:9216
v_xor_b32 v8,16384,v8
v_mfma_f32_16x16x32_bf16 v[24:27],a[208:211],v[40:43],v[24:27]
s_cmp_lt_u32 s67,s59
ds_read_b128 v[36:39],v2 offset:1024
v_xor_b32 v9,16384,v9
v_mfma_f32_16x16x32_bf16 v[28:31],a[212:215],v[40:43],v[28:31]
buffer_load_dwordx4 v0,s[36:39],s66 offen offset:1024 sc1 lds
v_mfma_f32_16x16x32_bf16 v[24:27],a[216:219],v[44:47],v[24:27]
ds_read_b128 v[40:43],v2 offset:2048
v_mfma_f32_16x16x32_bf16 v[28:31],a[220:223],v[44:47],v[28:31]
s_waitcnt lgkmcnt(5)
v_mfma_f32_16x16x32_bf16 v[24:27],a[224:227],v[48:51],v[24:27]
ds_read_b128 v[44:47],v2 offset:3072
v_mfma_f32_16x16x32_bf16 v[28:31],a[228:231],v[48:51],v[28:31]
s_waitcnt lgkmcnt(3)
buffer_load_dwordx4 v0,s[36:39],s66 offen offset:2048 sc1 lds
v_mfma_f32_16x16x32_bf16 v[24:27],a[232:235],v[52:55],v[24:27]
ds_read_b128 v[48:51],v2 offset:4096
v_mfma_f32_16x16x32_bf16 v[28:31],a[236:239],v[52:55],v[28:31]
v_mfma_f32_16x16x32_bf16 v[24:27],a[240:243],v[56:59],v[24:27]
ds_read_b128 v[52:55],v2 offset:5120
v_mfma_f32_16x16x32_bf16 v[28:31],a[244:247],v[56:59],v[28:31]
buffer_load_dwordx4 v0,s[36:39],s66 offen offset:3072 sc1 lds
v_mfma_f32_16x16x32_bf16 v[24:27],a[248:251],v[60:63],v[24:27]
s_waitcnt lgkmcnt(2)
ds_read_b128 v[56:59],v2 offset:6144
v_mfma_f32_16x16x32_bf16 v[28:31],a[252:255],v[60:63],v[28:31]
s_cbranch_scc1 Lg2_d0w3_%=
s_branch Lg2_slow0_%=
Lg2_slow0_%=:
s_cmp_lt_u32 s66,s59
s_cbranch_scc1 Lg2_gs0_%=
Lg2_slowb0_%=:
s_cmp_eq_u32 s54,1
s_cselect_b32 s68,s49,s48
s_lshr_b32 s68,s68,5
s_add_u32 m0,s78,8192
s_nop 0
buffer_load_dword v6,s[36:39],s68 offen offset:0 sc1 lds
s_lshr_b32 s73,s47,0
s_lshr_b32 s85,s47,5
v_lshlrev_b32 v64,16,v84
v_and_b32 v65,0xffff0000,v84
v_lshlrev_b32 v66,16,v85
v_and_b32 v67,0xffff0000,v85
v_lshlrev_b32 v68,16,v86
v_and_b32 v69,0xffff0000,v86
v_lshlrev_b32 v70,16,v87
v_and_b32 v71,0xffff0000,v87
v_fma_f32 v64,v64,v91,v90
v_fma_f32 v65,v65,v91,v90
v_fma_f32 v66,v66,v91,v90
v_fma_f32 v67,v67,v91,v90
v_fma_f32 v68,v68,v91,v90
v_fma_f32 v69,v69,v91,v90
v_fma_f32 v70,v70,v91,v90
v_fma_f32 v71,v71,v91,v90
v_add_f32 v24,v24,v64
v_add_f32 v25,v25,v65
v_add_f32 v26,v26,v66
v_add_f32 v27,v27,v67
v_add_f32 v28,v28,v68
v_add_f32 v29,v29,v69
v_add_f32 v30,v30,v70
v_add_f32 v31,v31,v71
v_cvt_pk_bf16_f32 v72,v24,v25
v_cvt_pk_bf16_f32 v73,v26,v27
v_cvt_pk_bf16_f32 v74,v28,v29
v_cvt_pk_bf16_f32 v75,v30,v31
s_add_u32 m0,s78,8448
s_nop 0
buffer_load_dword v6,s[36:39],s68 offen offset:0 sc1 lds
buffer_store_dwordx4 v[72:75],v3,s[36:39],s73 offen offset:0 sc0 sc1
v_add_f32 v64,v24,v25
v_mul_f32 v65,v24,v24
v_fma_f32 v65,v25,v25,v65
v_add_f32 v64,v64,v26
v_fma_f32 v65,v26,v26,v65
v_add_f32 v64,v64,v27
v_fma_f32 v65,v27,v27,v65
v_add_f32 v64,v64,v28
v_fma_f32 v65,v28,v28,v65
v_add_f32 v64,v64,v29
v_fma_f32 v65,v29,v29,v65
v_add_f32 v64,v64,v30
v_fma_f32 v65,v30,v30,v65
v_add_f32 v64,v64,v31
v_fma_f32 v65,v31,v31,v65
s_nop 1
v_permlane16_swap_b32 v64,v65
v_add_f32 v64,v64,v65
v_mov_b32 v65,v64
s_nop 1
v_permlane32_swap_b32 v64,v65
v_add_f32 v64,v64,v65
ds_write_b32 v13,v64 offset:0
s_waitcnt lgkmcnt(0)
s_barrier
ds_read_b128 v[76:79],v14 offset:0
s_waitcnt lgkmcnt(0)
v_add_f32 v76,v76,v77
v_add_f32 v78,v78,v79
v_add_f32 v76,v76,v78
buffer_store_dword v76,v12,s[36:39],s85 offen offset:0 sc0 sc1
s_cmp_ge_u32 s48,s59
s_cbranch_scc1 Lg2_done0_%=
s_add_u32 m0,s78,16384
s_nop 0
buffer_load_dword v6,s[36:39],s68 offen offset:0 sc1 lds
s_waitcnt vmcnt(4)
s_barrier
ds_read_b32 v15,v7 offset:8192
s_lshr_b32 s70,s46,5
buffer_store_dword v5,v4,s[36:39],s70 offen offset:0 sc0 sc1
s_mov_b32 s46,s59
s_mov_b32 s69,18432
s_waitcnt lgkmcnt(0)
v_cmp_eq_u32 vcc,s60,v15
s_nop 0
s_cmp_eq_u32 vcc_lo,-1
s_cbranch_scc1 Lg2_go0_%=
s_add_u32 m0,s78,16640
s_nop 0
buffer_load_dword v6,s[36:39],s68 offen offset:0 sc1 lds
s_waitcnt vmcnt(5)
s_barrier
ds_read_b32 v15,v7 offset:8448
s_mov_b32 s69,18688
s_waitcnt lgkmcnt(0)
v_cmp_eq_u32 vcc,s60,v15
s_nop 0
s_cmp_eq_u32 vcc_lo,-1
s_cbranch_scc1 Lg2_go0_%=
s_add_u32 m0,s78,8192
s_nop 0
buffer_load_dword v6,s[36:39],s68 offen offset:0 sc1 lds
s_waitcnt vmcnt(3)
s_barrier
ds_read_b32 v15,v7 offset:16384
s_lshr_b32 s70,s47,5
buffer_store_dword v5,v4,s[36:39],s70 offen offset:0 sc0 sc1
s_mov_b32 s47,s59
s_mov_b32 s69,10240
s_waitcnt lgkmcnt(0)
v_cmp_eq_u32 vcc,s60,v15
s_nop 0
s_cmp_eq_u32 vcc_lo,-1
s_cbranch_scc1 Lg2_go0_%=
s_add_u32 m0,s78,8448
s_nop 0
buffer_load_dword v6,s[36:39],s68 offen offset:0 sc1 lds
s_waitcnt vmcnt(3)
s_barrier
ds_read_b32 v15,v7 offset:16640
s_mov_b32 s69,10496
s_waitcnt lgkmcnt(0)
v_cmp_eq_u32 vcc,s60,v15
s_nop 0
s_cmp_eq_u32 vcc_lo,-1
s_cbranch_scc1 Lg2_go0_%=
Lg2_poll0_%=:
s_add_u32 m0,s78,16384
s_nop 0
buffer_load_dword v6,s[36:39],s68 offen offset:0 sc1 lds
s_waitcnt vmcnt(2))"
R"(
s_barrier
ds_read_b32 v15,v7 offset:8192
s_mov_b32 s69,18432
s_waitcnt lgkmcnt(0)
v_cmp_eq_u32 vcc,s60,v15
s_nop 0
s_cmp_eq_u32 vcc_lo,-1
s_cbranch_scc1 Lg2_go0_%=
s_sleep 4
s_add_u32 m0,s78,16640
s_nop 0
buffer_load_dword v6,s[36:39],s68 offen offset:0 sc1 lds
s_waitcnt vmcnt(2)
s_barrier
ds_read_b32 v15,v7 offset:8448
s_mov_b32 s69,18688
s_waitcnt lgkmcnt(0)
v_cmp_eq_u32 vcc,s60,v15
s_nop 0
s_cmp_eq_u32 vcc_lo,-1
s_cbranch_scc1 Lg2_go0_%=
s_sleep 4
s_add_u32 m0,s78,8192
s_nop 0
buffer_load_dword v6,s[36:39],s68 offen offset:0 sc1 lds
s_waitcnt vmcnt(2)
s_barrier
ds_read_b32 v15,v7 offset:16384
s_mov_b32 s69,10240
s_waitcnt lgkmcnt(0)
v_cmp_eq_u32 vcc,s60,v15
s_nop 0
s_cmp_eq_u32 vcc_lo,-1
s_cbranch_scc1 Lg2_go0_%=
s_sleep 4
s_add_u32 m0,s78,8448
s_nop 0
buffer_load_dword v6,s[36:39],s68 offen offset:0 sc1 lds
s_waitcnt vmcnt(2)
s_barrier
ds_read_b32 v15,v7 offset:16640
s_mov_b32 s69,10496
s_waitcnt lgkmcnt(0)
v_cmp_eq_u32 vcc,s60,v15
s_nop 0
s_cmp_eq_u32 vcc_lo,-1
s_cbranch_scc1 Lg2_go0_%=
s_sleep 4
s_add_u32 s79,s79,1
s_cmp_lt_u32 s79,20000
s_cbranch_scc1 Lg2_poll0_%=
Lg2_go0_%=:
s_xor_b32 s62,s63,65536
s_mov_b32 m0,s62
s_nop 0
buffer_load_dwordx4 v0,s[36:39],s48 offen offset:0 sc1 lds
buffer_load_dwordx4 v0,s[36:39],s48 offen offset:1024 sc1 lds
buffer_load_dwordx4 v0,s[36:39],s48 offen offset:2048 sc1 lds
buffer_load_dwordx4 v0,s[36:39],s48 offen offset:3072 sc1 lds
s_add_u32 m0,s62,16384
s_nop 0
buffer_load_dwordx4 v1,s[36:39],s48 offen offset:0 sc1 lds
buffer_load_dwordx4 v1,s[36:39],s48 offen offset:1024 sc1 lds
buffer_load_dwordx4 v1,s[36:39],s48 offen offset:2048 sc1 lds
buffer_load_dwordx4 v1,s[36:39],s48 offen offset:3072 sc1 lds
s_lshr_b32 s72,s48,6
s_lshr_b32 s71,s62,2
s_add_u32 m0,s71,s88
s_nop 0
buffer_load_dwordx4 v10,s[36:39],s48 offen offset:0 sc1 lds
buffer_load_dword v11,s[36:39],s72 offen offset:1024 sc1 lds
s_cmp_eq_u32 vcc_hi,-1
s_cselect_b32 s66,s49,s59
s_xor_b32 s62,s64,65536
s_cmp_ge_u32 s66,s59
s_cselect_b32 s68,s49,s50
s_lshr_b32 s68,s68,5
s_add_u32 m0,s78,0
s_nop 0
buffer_load_dword v6,s[36:39],s68 offen offset:0 sc1 lds
s_mov_b32 m0,s62
s_nop 0
buffer_load_dwordx4 v0,s[36:39],s66 offen offset:0 sc1 lds
buffer_load_dwordx4 v0,s[36:39],s66 offen offset:1024 sc1 lds
buffer_load_dwordx4 v0,s[36:39],s66 offen offset:2048 sc1 lds
buffer_load_dwordx4 v0,s[36:39],s66 offen offset:3072 sc1 lds
s_waitcnt vmcnt(11)
s_barrier
ds_read_b128 v[32:35],v2 offset:0
ds_read_b128 v[36:39],v2 offset:1024
ds_read_b128 v[40:43],v2 offset:2048
ds_read_b128 v[44:47],v2 offset:3072
ds_read_b128 v[48:51],v2 offset:4096
ds_read_b128 v[52:55],v2 offset:5120
ds_read_b128 v[56:59],v2 offset:6144
s_lshr_b32 s70,s46,5
buffer_store_dword v5,v4,s[36:39],s70 offen offset:0 sc0 sc1
s_mov_b32 s46,s59
s_lshr_b32 s70,s47,5
buffer_store_dword v5,v4,s[36:39],s70 offen offset:0 sc0 sc1
s_mov_b32 s47,s59
s_lshr_b32 s70,s59,5
buffer_store_dword v5,v4,s[36:39],s70 offen offset:0 sc0 sc1
s_waitcnt lgkmcnt(3)
s_setpc_b64 s[90:91]
Lg2_gs0_%=:
s_cmp_eq_u32 s54,1
s_cselect_b32 s68,s49,s48
s_lshr_b32 s68,s68,5
s_lshr_b32 s73,s47,0
s_lshr_b32 s85,s47,5
s_add_u32 m0,s78,8192
s_nop 0
buffer_load_dword v6,s[36:39],s68 offen offset:0 sc1 lds
v_lshlrev_b32 v64,16,v84
v_and_b32 v65,0xffff0000,v84
v_lshlrev_b32 v66,16,v85
v_and_b32 v67,0xffff0000,v85
v_lshlrev_b32 v68,16,v86
v_and_b32 v69,0xffff0000,v86
v_lshlrev_b32 v70,16,v87
v_and_b32 v71,0xffff0000,v87
s_add_u32 m0,s62,16384
s_nop 0
buffer_load_dwordx4 v1,s[36:39],s66 offen offset:0 sc1 lds
v_fma_f32 v64,v64,v91,v90
v_fma_f32 v65,v65,v91,v90
v_fma_f32 v66,v66,v91,v90
v_fma_f32 v67,v67,v91,v90
v_fma_f32 v68,v68,v91,v90
v_fma_f32 v69,v69,v91,v90
v_fma_f32 v70,v70,v91,v90
v_fma_f32 v71,v71,v91,v90
buffer_load_dwordx4 v1,s[36:39],s66 offen offset:1024 sc1 lds
v_add_f32 v24,v24,v64
v_add_f32 v25,v25,v65
v_add_f32 v26,v26,v66
v_add_f32 v27,v27,v67
v_add_f32 v28,v28,v68
v_add_f32 v29,v29,v69
v_add_f32 v30,v30,v70
v_add_f32 v31,v31,v71
buffer_load_dwordx4 v1,s[36:39],s66 offen offset:2048 sc1 lds
v_cvt_pk_bf16_f32 v72,v24,v25
v_cvt_pk_bf16_f32 v73,v26,v27
v_cvt_pk_bf16_f32 v74,v28,v29
v_cvt_pk_bf16_f32 v75,v30,v31
buffer_store_dwordx4 v[72:75],v3,s[36:39],s73 offen offset:0 sc0 sc1
v_add_f32 v64,v24,v25
v_mul_f32 v65,v24,v24
v_fma_f32 v65,v25,v25,v65
buffer_load_dwordx4 v1,s[36:39],s66 offen offset:3072 sc1 lds
v_add_f32 v64,v64,v26
v_fma_f32 v65,v26,v26,v65
v_add_f32 v64,v64,v27
v_fma_f32 v65,v27,v27,v65
v_add_f32 v64,v64,v28
v_fma_f32 v65,v28,v28,v65
v_add_f32 v64,v64,v29
v_fma_f32 v65,v29,v29,v65
s_lshr_b32 s72,s66,6
s_lshr_b32 s71,s62,2
s_add_u32 m0,s71,s88
s_nop 0
buffer_load_dwordx4 v10,s[36:39],s66 offen offset:0 sc1 lds
v_add_f32 v64,v64,v30
v_fma_f32 v65,v30,v30,v65
v_add_f32 v64,v64,v31
v_fma_f32 v65,v31,v31,v65
s_nop 1
v_permlane16_swap_b32 v64,v65
v_add_f32 v64,v64,v65
v_mov_b32 v65,v64
s_nop 1
v_permlane32_swap_b32 v64,v65
buffer_load_dword v11,s[36:39],s72 offen offset:1024 sc1 lds
v_add_f32 v64,v64,v65
ds_write_b32 v13,v64 offset:0
s_waitcnt lgkmcnt(0)
s_barrier
ds_read_b128 v[76:79],v14 offset:0
s_waitcnt lgkmcnt(0)
v_add_f32 v76,v76,v77
v_add_f32 v78,v78,v79
v_add_f32 v76,v76,v78
buffer_store_dword v76,v12,s[36:39],s85 offen offset:0 sc0 sc1
s_waitcnt vmcnt(8)
s_barrier
ds_read_b32 v15,v7 offset:8192
ds_read_b128 v[32:35],v2 offset:0
ds_read_b128 v[36:39],v2 offset:1024
ds_read_b128 v[40:43],v2 offset:2048
ds_read_b128 v[44:47],v2 offset:3072
ds_read_b128 v[48:51],v2 offset:4096
ds_read_b128 v[52:55],v2 offset:5120
ds_read_b128 v[56:59],v2 offset:6144
s_lshr_b32 s70,s46,5
buffer_store_dword v5,v4,s[36:39],s70 offen offset:0 sc0 sc1
s_waitcnt lgkmcnt(7)
v_cmp_eq_u32 vcc,s60,v15)"
R"(
s_nop 0
s_cmp_eq_u32 vcc_hi,-1
s_cselect_b32 s66,s49,s59
s_xor_b32 s62,s64,65536
s_cmp_ge_u32 s66,s59
s_cselect_b32 s68,s49,s50
s_lshr_b32 s68,s68,5
s_add_u32 m0,s78,0
s_nop 0
buffer_load_dword v6,s[36:39],s68 offen offset:0 sc1 lds
s_mov_b32 m0,s62
s_nop 0
buffer_load_dwordx4 v0,s[36:39],s66 offen offset:0 sc1 lds
buffer_load_dwordx4 v0,s[36:39],s66 offen offset:1024 sc1 lds
buffer_load_dwordx4 v0,s[36:39],s66 offen offset:2048 sc1 lds
buffer_load_dwordx4 v0,s[36:39],s66 offen offset:3072 sc1 lds
s_mov_b32 s46,s47
s_mov_b32 s47,s59
s_lshr_b32 s70,s59,5
buffer_store_dword v5,v4,s[36:39],s70 offen offset:0 sc0 sc1
s_lshr_b32 s70,s59,5
buffer_store_dword v5,v4,s[36:39],s70 offen offset:0 sc0 sc1
s_waitcnt lgkmcnt(3)
s_setpc_b64 s[90:91]
Lg2_done0_%=:
s_waitcnt vmcnt(0)
s_barrier
s_lshr_b32 s70,s46,5
buffer_store_dword v5,v4,s[36:39],s70 offen offset:0 sc0 sc1
s_lshr_b32 s70,s47,5
buffer_store_dword v5,v4,s[36:39],s70 offen offset:0 sc0 sc1
s_branch Lg2_done_%=
Lg2_slow1_%=:
s_cmp_lt_u32 s67,s59
s_cbranch_scc1 Lg2_gs1_%=
Lg2_slowb1_%=:
s_cmp_eq_u32 s54,1
s_cselect_b32 s68,s50,s49
s_lshr_b32 s68,s68,5
s_add_u32 m0,s78,8192
s_nop 0
buffer_load_dword v6,s[36:39],s68 offen offset:0 sc1 lds
s_lshr_b32 s73,s48,0
s_lshr_b32 s85,s48,5
v_lshlrev_b32 v64,16,v80
v_and_b32 v65,0xffff0000,v80
v_lshlrev_b32 v66,16,v81
v_and_b32 v67,0xffff0000,v81
v_lshlrev_b32 v68,16,v82
v_and_b32 v69,0xffff0000,v82
v_lshlrev_b32 v70,16,v83
v_and_b32 v71,0xffff0000,v83
v_fma_f32 v64,v64,v89,v88
v_fma_f32 v65,v65,v89,v88
v_fma_f32 v66,v66,v89,v88
v_fma_f32 v67,v67,v89,v88
v_fma_f32 v68,v68,v89,v88
v_fma_f32 v69,v69,v89,v88
v_fma_f32 v70,v70,v89,v88
v_fma_f32 v71,v71,v89,v88
v_add_f32 v16,v16,v64
v_add_f32 v17,v17,v65
v_add_f32 v18,v18,v66
v_add_f32 v19,v19,v67
v_add_f32 v20,v20,v68
v_add_f32 v21,v21,v69
v_add_f32 v22,v22,v70
v_add_f32 v23,v23,v71
v_cvt_pk_bf16_f32 v72,v16,v17
v_cvt_pk_bf16_f32 v73,v18,v19
v_cvt_pk_bf16_f32 v74,v20,v21
v_cvt_pk_bf16_f32 v75,v22,v23
s_add_u32 m0,s78,8448
s_nop 0
buffer_load_dword v6,s[36:39],s68 offen offset:0 sc1 lds
buffer_store_dwordx4 v[72:75],v3,s[36:39],s73 offen offset:0 sc0 sc1
v_add_f32 v64,v16,v17
v_mul_f32 v65,v16,v16
v_fma_f32 v65,v17,v17,v65
v_add_f32 v64,v64,v18
v_fma_f32 v65,v18,v18,v65
v_add_f32 v64,v64,v19
v_fma_f32 v65,v19,v19,v65
v_add_f32 v64,v64,v20
v_fma_f32 v65,v20,v20,v65
v_add_f32 v64,v64,v21
v_fma_f32 v65,v21,v21,v65
v_add_f32 v64,v64,v22
v_fma_f32 v65,v22,v22,v65
v_add_f32 v64,v64,v23
v_fma_f32 v65,v23,v23,v65
s_nop 1
v_permlane16_swap_b32 v64,v65
v_add_f32 v64,v64,v65
v_mov_b32 v65,v64
s_nop 1
v_permlane32_swap_b32 v64,v65
v_add_f32 v64,v64,v65
ds_write_b32 v13,v64 offset:2048
s_waitcnt lgkmcnt(0)
s_barrier
ds_read_b128 v[76:79],v14 offset:2048
s_waitcnt lgkmcnt(0)
v_add_f32 v76,v76,v77
v_add_f32 v78,v78,v79
v_add_f32 v76,v76,v78
buffer_store_dword v76,v12,s[36:39],s85 offen offset:0 sc0 sc1
s_cmp_ge_u32 s49,s59
s_cbranch_scc1 Lg2_done1_%=
s_add_u32 m0,s78,16384
s_nop 0
buffer_load_dword v6,s[36:39],s68 offen offset:0 sc1 lds
s_waitcnt vmcnt(4)
s_barrier
ds_read_b32 v15,v7 offset:8192
s_lshr_b32 s70,s47,5
buffer_store_dword v5,v4,s[36:39],s70 offen offset:0 sc0 sc1
s_mov_b32 s47,s59
s_mov_b32 s69,18432
s_waitcnt lgkmcnt(0)
v_cmp_eq_u32 vcc,s60,v15
s_nop 0
s_cmp_eq_u32 vcc_lo,-1
s_cbranch_scc1 Lg2_go1_%=
s_add_u32 m0,s78,16640
s_nop 0
buffer_load_dword v6,s[36:39],s68 offen offset:0 sc1 lds
s_waitcnt vmcnt(5)
s_barrier
ds_read_b32 v15,v7 offset:8448
s_mov_b32 s69,18688
s_waitcnt lgkmcnt(0)
v_cmp_eq_u32 vcc,s60,v15
s_nop 0
s_cmp_eq_u32 vcc_lo,-1
s_cbranch_scc1 Lg2_go1_%=
s_add_u32 m0,s78,8192
s_nop 0
buffer_load_dword v6,s[36:39],s68 offen offset:0 sc1 lds
s_waitcnt vmcnt(3)
s_barrier
ds_read_b32 v15,v7 offset:16384
s_lshr_b32 s70,s48,5
buffer_store_dword v5,v4,s[36:39],s70 offen offset:0 sc0 sc1
s_mov_b32 s48,s59
s_mov_b32 s69,10240
s_waitcnt lgkmcnt(0)
v_cmp_eq_u32 vcc,s60,v15
s_nop 0
s_cmp_eq_u32 vcc_lo,-1
s_cbranch_scc1 Lg2_go1_%=
s_add_u32 m0,s78,8448
s_nop 0
buffer_load_dword v6,s[36:39],s68 offen offset:0 sc1 lds
s_waitcnt vmcnt(3)
s_barrier
ds_read_b32 v15,v7 offset:16640
s_mov_b32 s69,10496
s_waitcnt lgkmcnt(0)
v_cmp_eq_u32 vcc,s60,v15
s_nop 0
s_cmp_eq_u32 vcc_lo,-1
s_cbranch_scc1 Lg2_go1_%=
Lg2_poll1_%=:
s_add_u32 m0,s78,16384
s_nop 0
buffer_load_dword v6,s[36:39],s68 offen offset:0 sc1 lds
s_waitcnt vmcnt(2)
s_barrier
ds_read_b32 v15,v7 offset:8192
s_mov_b32 s69,18432
s_waitcnt lgkmcnt(0)
v_cmp_eq_u32 vcc,s60,v15
s_nop 0
s_cmp_eq_u32 vcc_lo,-1
s_cbranch_scc1 Lg2_go1_%=
s_sleep 4
s_add_u32 m0,s78,16640
s_nop 0
buffer_load_dword v6,s[36:39],s68 offen offset:0 sc1 lds
s_waitcnt vmcnt(2)
s_barrier
ds_read_b32 v15,v7 offset:8448
s_mov_b32 s69,18688
s_waitcnt lgkmcnt(0)
v_cmp_eq_u32 vcc,s60,v15
s_nop 0
s_cmp_eq_u32 vcc_lo,-1
s_cbranch_scc1 Lg2_go1_%=
s_sleep 4
s_add_u32 m0,s78,8192
s_nop 0
buffer_load_dword v6,s[36:39],s68 offen offset:0 sc1 lds
s_waitcnt vmcnt(2)
s_barrier
ds_read_b32 v15,v7 offset:16384
s_mov_b32 s69,10240
s_waitcnt lgkmcnt(0)
v_cmp_eq_u32 vcc,s60,v15
s_nop 0)"
R"(
s_cmp_eq_u32 vcc_lo,-1
s_cbranch_scc1 Lg2_go1_%=
s_sleep 4
s_add_u32 m0,s78,8448
s_nop 0
buffer_load_dword v6,s[36:39],s68 offen offset:0 sc1 lds
s_waitcnt vmcnt(2)
s_barrier
ds_read_b32 v15,v7 offset:16640
s_mov_b32 s69,10496
s_waitcnt lgkmcnt(0)
v_cmp_eq_u32 vcc,s60,v15
s_nop 0
s_cmp_eq_u32 vcc_lo,-1
s_cbranch_scc1 Lg2_go1_%=
s_sleep 4
s_add_u32 s79,s79,1
s_cmp_lt_u32 s79,20000
s_cbranch_scc1 Lg2_poll1_%=
Lg2_go1_%=:
s_xor_b32 s62,s64,65536
s_mov_b32 m0,s62
s_nop 0
buffer_load_dwordx4 v0,s[36:39],s49 offen offset:0 sc1 lds
buffer_load_dwordx4 v0,s[36:39],s49 offen offset:1024 sc1 lds
buffer_load_dwordx4 v0,s[36:39],s49 offen offset:2048 sc1 lds
buffer_load_dwordx4 v0,s[36:39],s49 offen offset:3072 sc1 lds
s_add_u32 m0,s62,16384
s_nop 0
buffer_load_dwordx4 v1,s[36:39],s49 offen offset:0 sc1 lds
buffer_load_dwordx4 v1,s[36:39],s49 offen offset:1024 sc1 lds
buffer_load_dwordx4 v1,s[36:39],s49 offen offset:2048 sc1 lds
buffer_load_dwordx4 v1,s[36:39],s49 offen offset:3072 sc1 lds
s_lshr_b32 s72,s49,6
s_lshr_b32 s71,s62,2
s_add_u32 m0,s71,s88
s_nop 0
buffer_load_dwordx4 v10,s[36:39],s49 offen offset:0 sc1 lds
buffer_load_dword v11,s[36:39],s72 offen offset:1024 sc1 lds
s_cmp_eq_u32 vcc_hi,-1
s_cselect_b32 s67,s50,s59
s_xor_b32 s62,s63,65536
s_cmp_ge_u32 s67,s59
s_cselect_b32 s68,s50,s51
s_lshr_b32 s68,s68,5
s_add_u32 m0,s78,256
s_nop 0
buffer_load_dword v6,s[36:39],s68 offen offset:0 sc1 lds
s_mov_b32 m0,s62
s_nop 0
buffer_load_dwordx4 v0,s[36:39],s67 offen offset:0 sc1 lds
buffer_load_dwordx4 v0,s[36:39],s67 offen offset:1024 sc1 lds
buffer_load_dwordx4 v0,s[36:39],s67 offen offset:2048 sc1 lds
buffer_load_dwordx4 v0,s[36:39],s67 offen offset:3072 sc1 lds
s_waitcnt vmcnt(11)
s_barrier
ds_read_b128 v[32:35],v2 offset:32768
ds_read_b128 v[36:39],v2 offset:33792
ds_read_b128 v[40:43],v2 offset:34816
ds_read_b128 v[44:47],v2 offset:35840
ds_read_b128 v[48:51],v2 offset:36864
ds_read_b128 v[52:55],v2 offset:37888
ds_read_b128 v[56:59],v2 offset:38912
s_lshr_b32 s70,s47,5
buffer_store_dword v5,v4,s[36:39],s70 offen offset:0 sc0 sc1
s_mov_b32 s47,s59
s_lshr_b32 s70,s48,5
buffer_store_dword v5,v4,s[36:39],s70 offen offset:0 sc0 sc1
s_mov_b32 s48,s59
s_lshr_b32 s70,s59,5
buffer_store_dword v5,v4,s[36:39],s70 offen offset:0 sc0 sc1
s_waitcnt lgkmcnt(3)
s_setpc_b64 s[92:93]
Lg2_gs1_%=:
s_cmp_eq_u32 s54,1
s_cselect_b32 s68,s50,s49
s_lshr_b32 s68,s68,5
s_lshr_b32 s73,s48,0
s_lshr_b32 s85,s48,5
s_add_u32 m0,s78,8192
s_nop 0
buffer_load_dword v6,s[36:39],s68 offen offset:0 sc1 lds
v_lshlrev_b32 v64,16,v80
v_and_b32 v65,0xffff0000,v80
v_lshlrev_b32 v66,16,v81
v_and_b32 v67,0xffff0000,v81
v_lshlrev_b32 v68,16,v82
v_and_b32 v69,0xffff0000,v82
v_lshlrev_b32 v70,16,v83
v_and_b32 v71,0xffff0000,v83
s_add_u32 m0,s62,16384
s_nop 0
buffer_load_dwordx4 v1,s[36:39],s67 offen offset:0 sc1 lds
v_fma_f32 v64,v64,v89,v88
v_fma_f32 v65,v65,v89,v88
v_fma_f32 v66,v66,v89,v88
v_fma_f32 v67,v67,v89,v88
v_fma_f32 v68,v68,v89,v88
v_fma_f32 v69,v69,v89,v88
v_fma_f32 v70,v70,v89,v88
v_fma_f32 v71,v71,v89,v88
buffer_load_dwordx4 v1,s[36:39],s67 offen offset:1024 sc1 lds
v_add_f32 v16,v16,v64
v_add_f32 v17,v17,v65
v_add_f32 v18,v18,v66
v_add_f32 v19,v19,v67
v_add_f32 v20,v20,v68
v_add_f32 v21,v21,v69
v_add_f32 v22,v22,v70
v_add_f32 v23,v23,v71
buffer_load_dwordx4 v1,s[36:39],s67 offen offset:2048 sc1 lds
v_cvt_pk_bf16_f32 v72,v16,v17
v_cvt_pk_bf16_f32 v73,v18,v19
v_cvt_pk_bf16_f32 v74,v20,v21
v_cvt_pk_bf16_f32 v75,v22,v23
buffer_store_dwordx4 v[72:75],v3,s[36:39],s73 offen offset:0 sc0 sc1
v_add_f32 v64,v16,v17
v_mul_f32 v65,v16,v16
v_fma_f32 v65,v17,v17,v65
buffer_load_dwordx4 v1,s[36:39],s67 offen offset:3072 sc1 lds
v_add_f32 v64,v64,v18
v_fma_f32 v65,v18,v18,v65
v_add_f32 v64,v64,v19
v_fma_f32 v65,v19,v19,v65
v_add_f32 v64,v64,v20
v_fma_f32 v65,v20,v20,v65
v_add_f32 v64,v64,v21
v_fma_f32 v65,v21,v21,v65
s_lshr_b32 s72,s67,6
s_lshr_b32 s71,s62,2
s_add_u32 m0,s71,s88
s_nop 0
buffer_load_dwordx4 v10,s[36:39],s67 offen offset:0 sc1 lds
v_add_f32 v64,v64,v22
v_fma_f32 v65,v22,v22,v65
v_add_f32 v64,v64,v23
v_fma_f32 v65,v23,v23,v65
s_nop 1
v_permlane16_swap_b32 v64,v65
v_add_f32 v64,v64,v65
v_mov_b32 v65,v64
s_nop 1
v_permlane32_swap_b32 v64,v65
buffer_load_dword v11,s[36:39],s72 offen offset:1024 sc1 lds
v_add_f32 v64,v64,v65
ds_write_b32 v13,v64 offset:2048
s_waitcnt lgkmcnt(0)
s_barrier
ds_read_b128 v[76:79],v14 offset:2048
s_waitcnt lgkmcnt(0)
v_add_f32 v76,v76,v77
v_add_f32 v78,v78,v79
v_add_f32 v76,v76,v78
buffer_store_dword v76,v12,s[36:39],s85 offen offset:0 sc0 sc1
s_waitcnt vmcnt(8)
s_barrier
ds_read_b32 v15,v7 offset:8192
ds_read_b128 v[32:35],v2 offset:32768
ds_read_b128 v[36:39],v2 offset:33792
ds_read_b128 v[40:43],v2 offset:34816
ds_read_b128 v[44:47],v2 offset:35840
ds_read_b128 v[48:51],v2 offset:36864
ds_read_b128 v[52:55],v2 offset:37888
ds_read_b128 v[56:59],v2 offset:38912
s_lshr_b32 s70,s47,5
buffer_store_dword v5,v4,s[36:39],s70 offen offset:0 sc0 sc1
s_waitcnt lgkmcnt(7)
v_cmp_eq_u32 vcc,s60,v15
s_nop 0
s_cmp_eq_u32 vcc_hi,-1
s_cselect_b32 s67,s50,s59
s_xor_b32 s62,s63,65536
s_cmp_ge_u32 s67,s59
s_cselect_b32 s68,s50,s51
s_lshr_b32 s68,s68,5
s_add_u32 m0,s78,256
s_nop 0
buffer_load_dword v6,s[36:39],s68 offen offset:0 sc1 lds
s_mov_b32 m0,s62
s_nop 0
buffer_load_dwordx4 v0,s[36:39],s67 offen offset:0 sc1 lds
buffer_load_dwordx4 v0,s[36:39],s67 offen offset:1024 sc1 lds
buffer_load_dwordx4 v0,s[36:39],s67 offen offset:2048 sc1 lds
buffer_load_dwordx4 v0,s[36:39],s67 offen offset:3072 sc1 lds
s_mov_b32 s47,s48
s_mov_b32 s48,s59
s_lshr_b32 s70,s59,5
buffer_store_dword v5,v4,s[36:39],s70 offen offset:0 sc0 sc1
s_lshr_b32 s70,s59,5
buffer_store_dword v5,v4,s[36:39],s70 offen offset:0 sc0 sc1
s_waitcnt lgkmcnt(3)
s_setpc_b64 s[92:93]
Lg2_done1_%=:
s_waitcnt vmcnt(0)
s_barrier
s_lshr_b32 s70,s47,5
buffer_store_dword v5,v4,s[36:39],s70 offen offset:0 sc0 sc1
s_lshr_b32 s70,s48,5
buffer_store_dword v5,v4,s[36:39],s70 offen offset:0 sc0 sc1
s_branch Lg2_done_%=)"
R"(
Lg2_done_%=:
s_waitcnt vmcnt(0) lgkmcnt(0)
s_barrier
Lg2_end_%=:
s_mov_b32 %0,s60
Lcode_g12_e:)"
  : "=&s"(epOut) : "s"(ws), "s"(wave), "s"(sStep), "s"(sX1), "s"(sX2), "s"(sU0), "s"(sWw), "s"(sBuf), "v"(vSrc), "v"(vSrc2), "v"(vA), "v"(vOut), "v"(vFlag), "v"(vPollOff), "v"(vSnapRd), "v"(vX), "v"(vX2), "v"(vAuxS), "v"(vAuxS2), "v"(vOut2), "v"(vStW), "v"(vStR), "v"(vWd0), "v"(vWd1), "v"(vWr0), "v"(vWr1), "v"(vWr2), "s"(w12) : "v0", "v1", "v2", "v3", "v4", "v5", "v6", "v7", "v8", "v9", "v10", "v11", "v12", "v13", "v14", "v15", "v16", "v17", "v18", "v19", "v20", "v21", "v22", "v23", "v24", "v25", "v26", "v27", "v28", "v29", "v30", "v31", "v32", "v33", "v34", "v35", "v36", "v37", "v38", "v39", "v40", "v41", "v42", "v43", "v44", "v45", "v46", "v47", "v48", "v49", "v50", "v51", "v52", "v53", "v54", "v55", "v56", "v57", "v58", "v59", "v60", "v61", "v62", "v63", "v64", "v65", "v66", "v67", "v68", "v69", "v70", "v71", "v72", "v73", "v74", "v75", "v76", "v77", "v78", "v79", "v80", "v81", "v82", "v83", "v84", "v85", "v86", "v87", "v88", "v89", "v90", "v91", "v92", "v93", "v94", "v95", "v96", "v97", "v98", "v99", "v100", "v101", "v102", "v103", "v104", "v105", "v106", "v107", "v108", "v109", "v110", "v111", "v112", "v113", "v114", "v115", "v116", "v117", "v118", "v119", "v120", "v121", "v122", "v123", "v124", "v125", "v126", "v127", "v128", "v129", "v130", "v131", "v132", "v133", "v134", "v135", "v136", "v137", "v138", "v139", "v140", "v141", "v142", "v143", "v144", "v145", "v146", "v147", "v148", "v149", "v150", "v151", "v152", "v153", "v154", "v155", "v156", "v157", "v158", "v159", "v160", "v161", "v162", "v163", "v164", "v165", "v166", "v167", "v168", "v169", "v170", "v171", "v172", "v173", "v174", "v175", "v176", "v177", "v178", "v179", "v180", "v181", "v182", "v183", "v184", "v185", "v186", "v187", "v188", "v189", "v190", "v191", "v192", "v193", "v194", "v195", "v196", "v197", "v198", "v199", "v200", "v201", "v202", "v203", "v204", "v205", "v206", "v207", "v208", "v209", "v210", "v211", "v212", "v213", "v214", "v215", "v216", "v217", "v218", "v219", "v220", "v221", "v222", "v223", "v224", "v225", "v226", "v227", "v228", "v229", "v230", "v231", "v255", "a0", "a1", "a2", "a3", "a4", "a5", "a6", "a7", "a8", "a9", "a10", "a11", "a12", "a13", "a14", "a15", "a16", "a17", "a18", "a19", "a20", "a21", "a22", "a23", "a24", "a25", "a26", "a27", "a28", "a29", "a30", "a31", "a32", "a33", "a34", "a35", "a36", "a37", "a38", "a39", "a40", "a41", "a42", "a43", "a44", "a45", "a46", "a47", "a48", "a49", "a50", "a51", "a52", "a53", "a54", "a55", "a56", "a57", "a58", "a59", "a60", "a61", "a62", "a63", "a64", "a65", "a66", "a67", "a68", "a69", "a70", "a71", "a72", "a73", "a74", "a75", "a76", "a77", "a78", "a79", "a80", "a81", "a82", "a83", "a84", "a85", "a86", "a87", "a88", "a89", "a90", "a91", "a92", "a93", "a94", "a95", "a96", "a97", "a98", "a99", "a100", "a101", "a102", "a103", "a104", "a105", "a106", "a107", "a108", "a109", "a110", "a111", "a112", "a113", "a114", "a115", "a116", "a117", "a118", "a119", "a120", "a121", "a122", "a123", "a124", "a125", "a126", "a127", "a128", "a129", "a130", "a131", "a132", "a133", "a134", "a135", "a136", "a137", "a138", "a139", "a140", "a141", "a142", "a143", "a144", "a145", "a146", "a147", "a148", "a149", "a150", "a151", "a152", "a153", "a154", "a155", "a156", "a157", "a158", "a159", "a160", "a161", "a162", "a163", "a164", "a165", "a166", "a167", "a168", "a169", "a170", "a171", "a172", "a173", "a174", "a175", "a176", "a177", "a178", "a179", "a180", "a181", "a182", "a183", "a184", "a185", "a186", "a187", "a188", "a189", "a190", "a191", "a192", "a193", "a194", "a195", "a196", "a197", "a198", "a199", "a200", "a201", "a202", "a203", "a204", "a205", "a206", "a207", "a208", "a209", "a210", "a211", "a212", "a213", "a214", "a215", "a216", "a217", "a218", "a219", "a220", "a221", "a222", "a223", "a224", "a225", "a226", "a227", "a228", "a229", "a230", "a231", "a232", "a233", "a234", "a235", "a236", "a237", "a238", "a239", "a240", "a241", "a242", "a243", "a244", "a245", "a246", "a247", "a248", "a249", "a250", "a251", "a252", "a253", "a254", "a255", "s36", "s37", "s38", "s39", "s40", "s41", "s42", "s43", "s44", "s45", "s46", "s47", "s48", "s49", "s50", "s51", "s52", "s53", "s54", "s55", "s56", "s57", "s58", "s59", "s60", "s61", "s62", "s63", "s64", "s65", "s66", "s67", "s68", "s69", "s70", "s71", "s72", "s73", "s74", "s75", "s76", "s77", "s78", "s79", "s80", "s81", "s82", "s83", "s84", "s85", "s86", "s87", "s88", "s89", "s90", "s91", "s92", "s93", "s94", "s95", "s96", "s97", "s98", "s99", "vcc", "scc", "memory");
    ep = epOut;
   }
    if (kind == 6)
    {
      LANE_ID(lr);
      wait_flags(ws + OFF_FXP + 7 * 16384 + wave * 1024, lr, 8, ep);
      const int r8 = lr & 7, seg = lr >> 3;
      const unsigned sEpR = __builtin_amdgcn_readfirstlane(ep), sK = slice, sStep = NRW, sK1 = RT1 + slice, sRq = OFF_FXP + LB * 2048 + wave * 128, sDbg = sDbg0 + (kind == 6 ? 0 : 1024);
      const unsigned vRF = (wave * 8 + (seg >> 2)) * 1024 + ((seg & 3) * 16 + r8) * 16;
      const unsigned vRwr = sLds + (seg == 0 ? r8 * 64 + wave * 4 : 512 + lr * 4), vRrd = sLds + r8 * 64;
      const unsigned vRP = sLds + ((lr >> 4) == ((lr & 15) >> 2) ? r8 * 64 + 16 + wave * 8 + (seg & 1) * 4 : 768 + lr * 4);
      const unsigned vRS = (wave == 0 && seg == 0) ? r8 * 16 : 0x80000000u, vFlag = lr == 0 ? 0u : 0x80000000u, vL4 = (lr & 15) * 128, vRL = wave * 4096 + lr * 16;
      asm volatile(R"(
Lcode_r_b:
s_mov_b64 s[36:37],%0
s_mov_b32 s38,122727552
s_mov_b32 s39,0x00020000
s_mov_b32 s54,%1
s_mov_b32 s50,%2
s_mov_b32 s92,%3
s_mov_b32 s48,%4
v_mov_b32 v0,%5
v_mov_b32 v1,%6
v_mov_b32 v2,%7
v_mov_b32 v3,%8
v_mov_b32 v4,%9
v_mov_b32 v5,%10
v_mov_b32 v6,%11
v_mov_b32 v11,%12
v_lshrrev_b32 v10,1,v4
v_mov_b32 v7,s48
v_mbcnt_lo_u32_b32 v9,-1,0
v_mbcnt_hi_u32_b32 v9,-1,v9
v_lshlrev_b32 v9,4,v9
s_mov_b32 s74,0
s_mov_b32 s96,0
s_mov_b32 s60,536870912
s_mov_b32 s46,0xaaaaaaaa
s_mov_b32 s47,0xaaaaaaaa
s_mov_b32 s56,0xcccccccc
s_mov_b32 s57,0xcccccccc
s_mov_b32 s76,0x3a800000
s_mov_b32 s77,0x3727c5ac
s_mov_b32 s78,0xc0135761
s_mov_b32 s79,0x3d372713
v_mov_b32 v192,0x3f803f80
v_mov_b32 v193,0x3f803f80
v_mov_b32 v194,0x3f803f80
v_mov_b32 v195,0x3f803f80
s_lshr_b32 s63,s54,1
s_and_b32 s64,s54,1
s_lshl_b32 s64,s64,7
s_lshl_b32 s87,s54,14
s_lshl_b32 s58,s63,15
s_add_u32 s58,s58,s64
s_add_u32 s58,s58,16908288
s_lshl_b32 s59,s63,9
s_add_u32 s59,s59,s64
s_add_u32 s59,s59,118556672
s_lshl_b32 s49,s54,9
s_add_u32 s49,s49,s92
s_lshl_b32 s91,s63,11
s_add_u32 s91,s91,121405440
s_lshl_b32 s69,s50,10
s_add_u32 s69,s69,s91
v_mov_b32 v20,0
v_mov_b32 v21,0
v_mov_b32 v22,0
v_mov_b32 v23,0
v_mov_b32 v24,0
v_mov_b32 v25,0
v_mov_b32 v26,0
v_mov_b32 v27,0
v_mov_b32 v16,0x3f80
v_mov_b32 v17,0x3f800000
v_cndmask_b32 v16,v16,v17,s[46:47]
s_mov_b32 s98,0x00300003
s_mov_b32 s99,0x30000300
v_cndmask_b32 v20,v20,v16,s[98:99]
s_lshl_b64 s[98:99],s[98:99],2
v_cndmask_b32 v21,v21,v16,s[98:99]
v_mov_b32 v26,v20
v_mov_b32 v27,v21
v_readfirstlane_b32 s52,v11
s_add_u32 s51,s87,8388608
s_mov_b32 s66,4096
s_nop 3
Lr_yp_%=:
s_add_u32 m0,s66,s52
s_cmp_ge_u32 s51,12582912
s_cbranch_scc1 Lr_ys1_%=
buffer_load_dwordx4 v11,s[36:39],s51 offen offset:0 sc1 lds
buffer_load_dwordx4 v11,s[36:39],s51 offen offset:1024 sc1 lds
buffer_load_dwordx4 v11,s[36:39],s51 offen offset:2048 sc1 lds
buffer_load_dwordx4 v11,s[36:39],s51 offen offset:3072 sc1 lds
s_lshl_b32 s63,s50,14
s_add_u32 s51,s51,s63
Lr_ys1_%=:
s_add_u32 s66,s66,16384
s_cmp_lt_u32 s66,151552
s_cbranch_scc1 Lr_yp_%=
s_mov_b32 s66,4096
v_add_u32 v18,4096,v11
Lr_bp9_%=:
buffer_load_dword v8,v6,s[36:39],s91 offen offset:0 sc1
s_waitcnt vmcnt(0)
v_cmp_eq_u32 vcc,s48,v8
s_nop 0
s_cmp_eq_u64 vcc,exec
s_cselect_b32 s63,1,s96
s_cmp_lg_u32 s63,0
s_cbranch_scc1 Lr_bq9_%=
s_add_u32 s74,s74,1
s_cmp_ge_u32 s74,20000
s_cselect_b32 s96,1,0
s_sleep 4
s_branch Lr_bp9_%=
Lr_bq9_%=:
s_add_u32 s63,s87,71303168
buffer_load_dwordx4 a[0:3],v11,s[36:39],s63 offen offset:0 sc1
buffer_load_dwordx4 a[4:7],v11,s[36:39],s63 offen offset:1024 sc1
buffer_load_dwordx4 a[8:11],v11,s[36:39],s63 offen offset:2048 sc1
buffer_load_dwordx4 a[12:15],v11,s[36:39],s63 offen offset:3072 sc1
s_add_u32 s63,s87,89128960
buffer_load_dwordx4 a[16:19],v11,s[36:39],s63 offen offset:0 sc1
buffer_load_dwordx4 a[20:23],v11,s[36:39],s63 offen offset:1024 sc1
buffer_load_dwordx4 a[24:27],v11,s[36:39],s63 offen offset:2048 sc1
buffer_load_dwordx4 a[28:31],v11,s[36:39],s63 offen offset:3072 sc1
s_add_u32 s63,s87,99614720
buffer_load_dwordx4 a[32:35],v11,s[36:39],s63 offen offset:0 sc1
buffer_load_dwordx4 a[36:39],v11,s[36:39],s63 offen offset:1024 sc1
buffer_load_dwordx4 a[40:43],v11,s[36:39],s63 offen offset:2048 sc1
buffer_load_dwordx4 a[44:47],v11,s[36:39],s63 offen offset:3072 sc1
s_add_u32 s63,s87,109051904
buffer_load_dwordx4 a[48:51],v11,s[36:39],s63 offen offset:0 sc1
buffer_load_dwordx4 a[52:55],v11,s[36:39],s63 offen offset:1024 sc1
buffer_load_dwordx4 a[56:59],v11,s[36:39],s63 offen offset:2048 sc1
buffer_load_dwordx4 a[60:63],v11,s[36:39],s63 offen offset:3072 sc1
buffer_load_dword v8,v6,s[36:39],s69 offen offset:0 sc1
s_waitcnt vmcnt(0)
Lr_it0_%=:
s_mov_b32 s88,s58
s_mov_b32 s89,s59
s_mov_b32 s90,s49
s_add_u32 s54,s54,s50
s_lshr_b32 s63,s54,1
s_and_b32 s64,s54,1
s_lshl_b32 s64,s64,7
s_lshl_b32 s87,s54,14
s_lshl_b32 s58,s63,15
s_add_u32 s58,s58,s64
s_add_u32 s58,s58,16908288
s_lshl_b32 s59,s63,9
s_add_u32 s59,s59,s64
s_add_u32 s59,s59,118556672
s_lshl_b32 s49,s54,9
s_add_u32 s49,s49,s92
s_lshl_b32 s91,s63,11
s_add_u32 s91,s91,121405440
s_lshl_b32 s69,s50,10
s_add_u32 s69,s69,s91
v_cmp_eq_u32 vcc,s48,v8
s_nop 0
s_cmp_eq_u64 vcc,exec
s_cselect_b32 s63,s54,256
s_cmp_lt_u32 s63,256
s_cbranch_scc1 Lr_ta0_%=
ds_read_b128 v[32:35],v18 offset:0
ds_read_b128 v[36:39],v18 offset:1024
ds_read_b128 v[40:43],v18 offset:2048
ds_read_b128 v[44:47],v18 offset:3072
v_mfma_f32_16x16x32_bf16 v[128:131],v[20:23],a[0:3],0
v_mfma_f32_16x16x32_bf16 v[132:135],v[24:27],a[0:3],0
v_mfma_f32_16x16x32_bf16 v[128:131],v[20:23],a[16:19],v[128:131]
v_mfma_f32_16x16x32_bf16 v[132:135],v[24:27],a[16:19],v[132:135]
v_mfma_f32_16x16x32_bf16 v[128:131],v[20:23],a[32:35],v[128:131]
v_mfma_f32_16x16x32_bf16 v[132:135],v[24:27],a[32:35],v[132:135]
v_mfma_f32_16x16x32_bf16 v[128:131],v[20:23],a[48:51],v[128:131]
v_mfma_f32_16x16x32_bf16 v[132:135],v[24:27],a[48:51],v[132:135]
v_mfma_f32_16x16x32_bf16 v[136:139],v[20:23],a[4:7],0
v_mfma_f32_16x16x32_bf16 v[140:143],v[24:27],a[4:7],0
v_mfma_f32_16x16x32_bf16 v[136:139],v[20:23],a[20:23],v[136:139]
v_mfma_f32_16x16x32_bf16 v[140:143],v[24:27],a[20:23],v[140:143]
v_mfma_f32_16x16x32_bf16 v[136:139],v[20:23],a[36:39],v[136:139]
s_nop 1
v_cvt_pk_bf16_f32 v208,v128,v129
v_cvt_pk_bf16_f32 v209,v130,v131
v_mfma_f32_16x16x32_bf16 v[140:143],v[24:27],a[36:39],v[140:143]
v_cvt_pk_bf16_f32 v210,v132,v133
v_cvt_pk_bf16_f32 v211,v134,v135
v_mfma_f32_16x16x32_bf16 v[136:139],v[20:23],a[52:55],v[136:139]
v_mfma_f32_16x16x32_bf16 v[140:143],v[24:27],a[52:55],v[140:143]
v_mfma_f32_16x16x32_bf16 v[144:147],v[20:23],a[8:11],0
v_mfma_f32_16x16x32_bf16 v[148:151],v[24:27],a[8:11],0
v_mfma_f32_16x16x32_bf16 v[144:147],v[20:23],a[24:27],v[144:147]
v_mfma_f32_16x16x32_bf16 v[148:151],v[24:27],a[24:27],v[148:151]
v_mfma_f32_16x16x32_bf16 v[144:147],v[20:23],a[40:43],v[144:147]
s_nop 1
v_cvt_pk_bf16_f32 v212,v136,v137
v_cvt_pk_bf16_f32 v213,v138,v139
v_mfma_f32_16x16x32_bf16 v[148:151],v[24:27],a[40:43],v[148:151]
v_cvt_pk_bf16_f32 v214,v140,v141
v_cvt_pk_bf16_f32 v215,v142,v143
v_mfma_f32_16x16x32_bf16 v[144:147],v[20:23],a[56:59],v[144:147]
v_mfma_f32_16x16x32_bf16 v[148:151],v[24:27],a[56:59],v[148:151]
v_mfma_f32_16x16x32_bf16 v[28:31],v[192:195],v[208:211],0
v_mfma_f32_16x16x32_bf16 v[204:207],v[208:211],v[208:211],0
v_mfma_f32_16x16x32_bf16 v[152:155],v[20:23],a[12:15],0
v_mfma_f32_16x16x32_bf16 v[156:159],v[24:27],a[12:15],0
v_mfma_f32_16x16x32_bf16 v[152:155],v[20:23],a[28:31],v[152:155]
v_mfma_f32_16x16x32_bf16 v[156:159],v[24:27],a[28:31],v[156:159]
v_mfma_f32_16x16x32_bf16 v[152:155],v[20:23],a[44:47],v[152:155]
v_cvt_pk_bf16_f32 v216,v144,v145)"
R"(
v_cvt_pk_bf16_f32 v217,v146,v147
v_mfma_f32_16x16x32_bf16 v[156:159],v[24:27],a[44:47],v[156:159]
v_cvt_pk_bf16_f32 v218,v148,v149
v_cvt_pk_bf16_f32 v219,v150,v151
v_mfma_f32_16x16x32_bf16 v[152:155],v[20:23],a[60:63],v[152:155]
v_mfma_f32_16x16x32_bf16 v[156:159],v[24:27],a[60:63],v[156:159]
v_mfma_f32_16x16x32_bf16 v[28:31],v[192:195],v[212:215],v[28:31]
v_mfma_f32_16x16x32_bf16 v[204:207],v[212:215],v[212:215],v[204:207]
v_mfma_f32_16x16x32_bf16 v[28:31],v[192:195],v[216:219],v[28:31]
v_mfma_f32_16x16x32_bf16 v[204:207],v[216:219],v[216:219],v[204:207]
s_waitcnt lgkmcnt(0)
v_mfma_f32_16x16x32_bf16 v[160:163],v[20:23],v[32:35],0
s_nop 0
v_cvt_pk_bf16_f32 v220,v152,v153
v_cvt_pk_bf16_f32 v221,v154,v155
v_mfma_f32_16x16x32_bf16 v[164:167],v[24:27],v[32:35],0
v_cvt_pk_bf16_f32 v222,v156,v157
v_cvt_pk_bf16_f32 v223,v158,v159
v_mfma_f32_16x16x32_bf16 v[168:171],v[20:23],v[36:39],0
s_nop 0
v_mfma_f32_16x16x32_bf16 v[28:31],v[192:195],v[220:223],v[28:31]
v_mfma_f32_16x16x32_bf16 v[204:207],v[220:223],v[220:223],v[204:207]
v_mfma_f32_16x16x32_bf16 v[172:175],v[24:27],v[36:39],0
v_mfma_f32_16x16x32_bf16 v[176:179],v[20:23],v[40:43],0
v_mfma_f32_16x16x32_bf16 v[180:183],v[24:27],v[40:43],0
s_nop 3
v_add_f32_dpp v224,v28,v28 row_ror:8 row_mask:0xf bank_mask:0xf
v_cndmask_b32 v225,v204,v205,s[46:47]
v_cndmask_b32 v226,v206,v207,s[46:47]
v_cndmask_b32 v225,v225,v226,s[56:57]
v_mfma_f32_16x16x32_bf16 v[184:187],v[20:23],v[44:47],0
ds_write_b32 v2,v224 offset:0
ds_write_b32 v0,v225 offset:0
v_mfma_f32_16x16x32_bf16 v[188:191],v[24:27],v[44:47],0
s_waitcnt lgkmcnt(0)
s_barrier
ds_read_b128 v[196:199],v3 offset:0
ds_read_b128 v[200:203],v3 offset:16
ds_read_b128 v[204:207],v3 offset:32
s_waitcnt lgkmcnt(0)
v_add_f32 v196,v196,v197
v_add_f32 v198,v198,v199
v_add_f32 v200,v200,v204
v_add_f32 v201,v201,v205
v_add_f32 v202,v202,v206
v_add_f32 v203,v203,v207
v_add_f32 v196,v196,v198
v_add_f32 v200,v200,v201
v_add_f32 v202,v202,v203
v_mul_f32 v196,s76,v196
v_add_f32 v197,v200,v202
v_mul_f32 v197,s76,v197
v_fma_f32 v197,-v196,v196,v197
v_max_f32 v197,0,v197
v_add_f32 v197,s77,v197
v_rsq_f32 v228,v197
s_nop 0
v_mul_f32_e64 v230,-v196,v228
v_mov_b32 v229,v228
v_mov_b32 v231,v230
v_pk_fma_f32 v[196:197],v[128:129],v[228:229],v[230:231]
v_pk_fma_f32 v[198:199],v[130:131],v[228:229],v[230:231]
v_pk_fma_f32 v[200:201],v[132:133],v[228:229],v[230:231]
v_pk_fma_f32 v[202:203],v[134:135],v[228:229],v[230:231]
v_pk_fma_f32 v[128:129],v[196:197],v[160:161],v[128:129]
v_pk_fma_f32 v[130:131],v[198:199],v[162:163],v[130:131]
v_pk_fma_f32 v[132:133],v[200:201],v[164:165],v[132:133]
v_pk_fma_f32 v[134:135],v[202:203],v[166:167],v[134:135]
v_pk_fma_f32 v[196:197],v[136:137],v[228:229],v[230:231]
v_pk_fma_f32 v[198:199],v[138:139],v[228:229],v[230:231]
v_pk_fma_f32 v[200:201],v[140:141],v[228:229],v[230:231]
v_pk_fma_f32 v[202:203],v[142:143],v[228:229],v[230:231]
v_pk_fma_f32 v[136:137],v[196:197],v[168:169],v[136:137]
v_pk_fma_f32 v[138:139],v[198:199],v[170:171],v[138:139]
v_pk_fma_f32 v[140:141],v[200:201],v[172:173],v[140:141]
v_pk_fma_f32 v[142:143],v[202:203],v[174:175],v[142:143]
v_pk_fma_f32 v[196:197],v[144:145],v[228:229],v[230:231]
v_pk_fma_f32 v[198:199],v[146:147],v[228:229],v[230:231]
v_pk_fma_f32 v[200:201],v[148:149],v[228:229],v[230:231]
v_pk_fma_f32 v[202:203],v[150:151],v[228:229],v[230:231]
v_pk_fma_f32 v[144:145],v[196:197],v[176:177],v[144:145]
v_pk_fma_f32 v[146:147],v[198:199],v[178:179],v[146:147]
v_pk_fma_f32 v[148:149],v[200:201],v[180:181],v[148:149]
v_pk_fma_f32 v[150:151],v[202:203],v[182:183],v[150:151]
v_pk_fma_f32 v[196:197],v[152:153],v[228:229],v[230:231]
v_pk_fma_f32 v[198:199],v[154:155],v[228:229],v[230:231]
v_pk_fma_f32 v[200:201],v[156:157],v[228:229],v[230:231]
v_pk_fma_f32 v[202:203],v[158:159],v[228:229],v[230:231]
v_pk_fma_f32 v[152:153],v[196:197],v[184:185],v[152:153]
v_pk_fma_f32 v[154:155],v[198:199],v[186:187],v[154:155]
v_pk_fma_f32 v[156:157],v[200:201],v[188:189],v[156:157]
v_pk_fma_f32 v[158:159],v[202:203],v[190:191],v[158:159]
v_cvt_pk_bf16_f32 v208,v128,v129
v_cvt_pk_bf16_f32 v209,v130,v131
v_cvt_pk_bf16_f32 v210,v132,v133
v_cvt_pk_bf16_f32 v211,v134,v135
v_cvt_pk_bf16_f32 v212,v136,v137
v_cvt_pk_bf16_f32 v213,v138,v139
v_cvt_pk_bf16_f32 v214,v140,v141
v_cvt_pk_bf16_f32 v215,v142,v143
v_mfma_f32_16x16x32_bf16 v[28:31],v[192:195],v[208:211],0
v_cvt_pk_bf16_f32 v216,v144,v145
v_cvt_pk_bf16_f32 v217,v146,v147
v_mfma_f32_16x16x32_bf16 v[204:207],v[208:211],v[208:211],0
v_cvt_pk_bf16_f32 v218,v148,v149
v_cvt_pk_bf16_f32 v219,v150,v151
v_mfma_f32_16x16x32_bf16 v[28:31],v[192:195],v[212:215],v[28:31]
v_cvt_pk_bf16_f32 v220,v152,v153
v_cvt_pk_bf16_f32 v221,v154,v155
v_mfma_f32_16x16x32_bf16 v[204:207],v[212:215],v[212:215],v[204:207]
v_cvt_pk_bf16_f32 v222,v156,v157
v_cvt_pk_bf16_f32 v223,v158,v159
v_mfma_f32_16x16x32_bf16 v[28:31],v[192:195],v[216:219],v[28:31]
v_mfma_f32_16x16x32_bf16 v[204:207],v[216:219],v[216:219],v[204:207]
v_mfma_f32_16x16x32_bf16 v[28:31],v[192:195],v[220:223],v[28:31]
v_mfma_f32_16x16x32_bf16 v[204:207],v[220:223],v[220:223],v[204:207]
s_waitcnt vmcnt(0)
buffer_store_dword v7,v5,s[36:39],s60 offen offset:0 sc0 sc1
s_mov_b32 s60,536870912
buffer_load_dword v8,v6,s[36:39],s91 offen offset:0 sc1
buffer_store_dwordx4 v[208:211],v1,s[36:39],s88 offen offset:0 sc0 sc1
buffer_store_dwordx4 v[212:215],v1,s[36:39],s88 offen offset:2048 sc0 sc1
s_add_u32 s88,s88,4096
v_add_f32_dpp v224,v28,v28 row_ror:8 row_mask:0xf bank_mask:0xf
v_cndmask_b32 v225,v204,v205,s[46:47]
v_cndmask_b32 v226,v206,v207,s[46:47]
v_cndmask_b32 v225,v225,v226,s[56:57]
s_and_b32 s63,s89,128
ds_write_b32 v2,v224 offset:1024
ds_write_b32 v0,v225 offset:1024
buffer_store_dwordx4 v[216:219],v1,s[36:39],s88 offen offset:0 sc0 sc1
buffer_store_dwordx4 v[220:223],v1,s[36:39],s88 offen offset:2048 sc0 sc1
s_waitcnt lgkmcnt(0)
s_barrier
ds_read_b128 v[196:199],v3 offset:1024
ds_read_b128 v[200:203],v3 offset:1040
ds_read_b128 v[204:207],v3 offset:1056
s_waitcnt lgkmcnt(0)
v_add_f32 v196,v196,v197
v_add_f32 v198,v198,v199
v_add_f32 v200,v200,v204
v_add_f32 v201,v201,v205
v_add_f32 v202,v202,v206
v_add_f32 v203,v203,v207
v_add_f32 v196,v196,v198
v_add_f32 v200,v200,v201
v_add_f32 v202,v202,v203
v_mul_f32 v196,s76,v196
v_add_f32 v197,v200,v202
v_mul_f32 v197,s76,v197
v_fma_f32 v197,-v196,v196,v197
v_max_f32 v197,0,v197
v_add_f32 v198,s77,v197
v_fma_f32 v199,s77,v198,v197
v_rsq_f32 v205,v198
v_rsq_f32 v198,v199
v_mov_b32 v200,v196
v_mul_f32_e64 v204,-v196,v205
v_mul_f32 v203,v199,v198
v_mul_f32 v201,s78,v198
v_mul_f32 v199,v198,v198
v_mul_f32 v202,s79,v199
v_mul_f32 v202,v202,v201
s_lshr_b32 s63,s63,1
s_sub_u32 s63,s89,s63
buffer_store_dwordx4 v[200:203],v4,s[36:39],s89 offen offset:0 sc0 sc1
buffer_store_dwordx2 v[204:205],v10,s[36:39],s63 offen offset:256 sc0 sc1
s_add_u32 m0,s66,s52
s_cmp_ge_u32 s51,12582912
s_cbranch_scc1 Lr_ys2_%=
buffer_load_dwordx4 v11,s[36:39],s51 offen offset:0 sc1 lds
buffer_load_dwordx4 v11,s[36:39],s51 offen offset:1024 sc1 lds
buffer_load_dwordx4 v11,s[36:39],s51 offen offset:2048 sc1 lds
buffer_load_dwordx4 v11,s[36:39],s51 offen offset:3072 sc1 lds
s_lshl_b32 s63,s50,14
s_add_u32 s51,s51,s63
Lr_ys2_%=:
s_add_u32 s66,s66,16384
s_cmp_eq_u32 s66,151552
s_cselect_b32 s66,4096,s66
v_add_u32 v18,s66,v11
s_mov_b32 s60,s90
s_waitcnt vmcnt(6)
v_cmp_eq_u32 vcc,s48,v8
s_nop 0
s_cmp_eq_u64 vcc,exec
s_cselect_b32 s63,s54,256
s_cmp_lt_u32 s63,256
s_cbranch_scc1 Lr_tc0_%=
buffer_load_dword v8,v6,s[36:39],s91 offen offset:0 sc1
s_waitcnt vmcnt(0)
buffer_store_dword v7,v5,s[36:39],s60 offen offset:0 sc0 sc1
s_mov_b32 s60,536870912
v_cmp_eq_u32 vcc,s48,v8
s_nop 0
s_cmp_eq_u64 vcc,exec
s_cselect_b32 s63,s54,256
s_cmp_lt_u32 s63,256
s_cbranch_scc1 Lr_bq0_%=
s_cmp_ge_u32 s54,256)"
R"(
s_cbranch_scc1 Lr_done_%=
Lr_bp0_%=:
buffer_load_dword v8,v6,s[36:39],s91 offen offset:0 sc1
s_waitcnt vmcnt(0)
v_cmp_eq_u32 vcc,s48,v8
s_nop 0
s_cmp_eq_u64 vcc,exec
s_cselect_b32 s63,1,s96
s_cmp_lg_u32 s63,0
s_cbranch_scc1 Lr_bq0_%=
s_add_u32 s74,s74,1
s_cmp_ge_u32 s74,20000
s_cselect_b32 s96,1,0
s_sleep 4
s_branch Lr_bp0_%=
Lr_bq0_%=:
s_add_u32 s63,s87,71303168
buffer_load_dwordx4 a[64:67],v11,s[36:39],s63 offen offset:0 sc1
buffer_load_dwordx4 a[68:71],v11,s[36:39],s63 offen offset:1024 sc1
buffer_load_dwordx4 a[72:75],v11,s[36:39],s63 offen offset:2048 sc1
buffer_load_dwordx4 a[76:79],v11,s[36:39],s63 offen offset:3072 sc1
s_add_u32 s63,s87,89128960
buffer_load_dwordx4 a[80:83],v11,s[36:39],s63 offen offset:0 sc1
buffer_load_dwordx4 a[84:87],v11,s[36:39],s63 offen offset:1024 sc1
buffer_load_dwordx4 a[88:91],v11,s[36:39],s63 offen offset:2048 sc1
buffer_load_dwordx4 a[92:95],v11,s[36:39],s63 offen offset:3072 sc1
s_add_u32 s63,s87,99614720
buffer_load_dwordx4 a[96:99],v11,s[36:39],s63 offen offset:0 sc1
buffer_load_dwordx4 a[100:103],v11,s[36:39],s63 offen offset:1024 sc1
buffer_load_dwordx4 a[104:107],v11,s[36:39],s63 offen offset:2048 sc1
buffer_load_dwordx4 a[108:111],v11,s[36:39],s63 offen offset:3072 sc1
s_add_u32 s63,s87,109051904
buffer_load_dwordx4 a[112:115],v11,s[36:39],s63 offen offset:0 sc1
buffer_load_dwordx4 a[116:119],v11,s[36:39],s63 offen offset:1024 sc1
buffer_load_dwordx4 a[120:123],v11,s[36:39],s63 offen offset:2048 sc1
buffer_load_dwordx4 a[124:127],v11,s[36:39],s63 offen offset:3072 sc1
buffer_load_dword v8,v6,s[36:39],s69 offen offset:0 sc1
s_waitcnt vmcnt(0)
s_branch Lr_it1_%=
Lr_ta0_%=:
ds_read_b128 v[32:35],v18 offset:0
ds_read_b128 v[36:39],v18 offset:1024
ds_read_b128 v[40:43],v18 offset:2048
ds_read_b128 v[44:47],v18 offset:3072
v_mfma_f32_16x16x32_bf16 v[128:131],v[20:23],a[0:3],0
s_add_u32 s63,s87,71303168
v_mfma_f32_16x16x32_bf16 v[132:135],v[24:27],a[0:3],0
buffer_load_dwordx4 a[64:67],v11,s[36:39],s63 offen offset:0 sc1
v_mfma_f32_16x16x32_bf16 v[128:131],v[20:23],a[16:19],v[128:131]
v_mfma_f32_16x16x32_bf16 v[132:135],v[24:27],a[16:19],v[132:135]
buffer_load_dwordx4 a[68:71],v11,s[36:39],s63 offen offset:1024 sc1
v_mfma_f32_16x16x32_bf16 v[128:131],v[20:23],a[32:35],v[128:131]
v_mfma_f32_16x16x32_bf16 v[132:135],v[24:27],a[32:35],v[132:135]
v_mfma_f32_16x16x32_bf16 v[128:131],v[20:23],a[48:51],v[128:131]
v_mfma_f32_16x16x32_bf16 v[132:135],v[24:27],a[48:51],v[132:135]
buffer_load_dwordx4 a[72:75],v11,s[36:39],s63 offen offset:2048 sc1
v_mfma_f32_16x16x32_bf16 v[136:139],v[20:23],a[4:7],0
v_mfma_f32_16x16x32_bf16 v[140:143],v[24:27],a[4:7],0
v_mfma_f32_16x16x32_bf16 v[136:139],v[20:23],a[20:23],v[136:139]
v_mfma_f32_16x16x32_bf16 v[140:143],v[24:27],a[20:23],v[140:143]
buffer_load_dwordx4 a[76:79],v11,s[36:39],s63 offen offset:3072 sc1
v_mfma_f32_16x16x32_bf16 v[136:139],v[20:23],a[36:39],v[136:139]
v_cvt_pk_bf16_f32 v208,v128,v129
v_cvt_pk_bf16_f32 v209,v130,v131
v_mfma_f32_16x16x32_bf16 v[140:143],v[24:27],a[36:39],v[140:143]
v_cvt_pk_bf16_f32 v210,v132,v133
v_cvt_pk_bf16_f32 v211,v134,v135
v_mfma_f32_16x16x32_bf16 v[136:139],v[20:23],a[52:55],v[136:139]
s_add_u32 s63,s87,89128960
v_mfma_f32_16x16x32_bf16 v[140:143],v[24:27],a[52:55],v[140:143]
buffer_load_dwordx4 a[80:83],v11,s[36:39],s63 offen offset:0 sc1
v_mfma_f32_16x16x32_bf16 v[144:147],v[20:23],a[8:11],0
v_mfma_f32_16x16x32_bf16 v[148:151],v[24:27],a[8:11],0
v_mfma_f32_16x16x32_bf16 v[144:147],v[20:23],a[24:27],v[144:147]
v_mfma_f32_16x16x32_bf16 v[148:151],v[24:27],a[24:27],v[148:151]
buffer_load_dwordx4 a[84:87],v11,s[36:39],s63 offen offset:1024 sc1
v_mfma_f32_16x16x32_bf16 v[144:147],v[20:23],a[40:43],v[144:147]
v_cvt_pk_bf16_f32 v212,v136,v137
v_cvt_pk_bf16_f32 v213,v138,v139
v_mfma_f32_16x16x32_bf16 v[148:151],v[24:27],a[40:43],v[148:151]
v_cvt_pk_bf16_f32 v214,v140,v141
v_cvt_pk_bf16_f32 v215,v142,v143
v_mfma_f32_16x16x32_bf16 v[144:147],v[20:23],a[56:59],v[144:147]
v_mfma_f32_16x16x32_bf16 v[148:151],v[24:27],a[56:59],v[148:151]
buffer_load_dwordx4 a[88:91],v11,s[36:39],s63 offen offset:2048 sc1
v_mfma_f32_16x16x32_bf16 v[28:31],v[192:195],v[208:211],0
v_mfma_f32_16x16x32_bf16 v[204:207],v[208:211],v[208:211],0
v_mfma_f32_16x16x32_bf16 v[152:155],v[20:23],a[12:15],0
v_mfma_f32_16x16x32_bf16 v[156:159],v[24:27],a[12:15],0
buffer_load_dwordx4 a[92:95],v11,s[36:39],s63 offen offset:3072 sc1
v_mfma_f32_16x16x32_bf16 v[152:155],v[20:23],a[28:31],v[152:155]
s_add_u32 s63,s87,99614720
v_mfma_f32_16x16x32_bf16 v[156:159],v[24:27],a[28:31],v[156:159]
v_mfma_f32_16x16x32_bf16 v[152:155],v[20:23],a[44:47],v[152:155]
v_cvt_pk_bf16_f32 v216,v144,v145
v_cvt_pk_bf16_f32 v217,v146,v147
v_mfma_f32_16x16x32_bf16 v[156:159],v[24:27],a[44:47],v[156:159]
v_cvt_pk_bf16_f32 v218,v148,v149
v_cvt_pk_bf16_f32 v219,v150,v151
v_mfma_f32_16x16x32_bf16 v[152:155],v[20:23],a[60:63],v[152:155]
buffer_load_dwordx4 a[96:99],v11,s[36:39],s63 offen offset:0 sc1
v_mfma_f32_16x16x32_bf16 v[156:159],v[24:27],a[60:63],v[156:159]
v_mfma_f32_16x16x32_bf16 v[28:31],v[192:195],v[212:215],v[28:31]
v_mfma_f32_16x16x32_bf16 v[204:207],v[212:215],v[212:215],v[204:207]
buffer_load_dwordx4 a[100:103],v11,s[36:39],s63 offen offset:1024 sc1
v_mfma_f32_16x16x32_bf16 v[28:31],v[192:195],v[216:219],v[28:31]
v_mfma_f32_16x16x32_bf16 v[204:207],v[216:219],v[216:219],v[204:207]
s_waitcnt lgkmcnt(0)
v_mfma_f32_16x16x32_bf16 v[160:163],v[20:23],v[32:35],0
v_cvt_pk_bf16_f32 v220,v152,v153
v_cvt_pk_bf16_f32 v221,v154,v155
v_mfma_f32_16x16x32_bf16 v[164:167],v[24:27],v[32:35],0
v_cvt_pk_bf16_f32 v222,v156,v157
v_cvt_pk_bf16_f32 v223,v158,v159
v_mfma_f32_16x16x32_bf16 v[168:171],v[20:23],v[36:39],0
buffer_load_dwordx4 a[104:107],v11,s[36:39],s63 offen offset:2048 sc1
v_mfma_f32_16x16x32_bf16 v[28:31],v[192:195],v[220:223],v[28:31]
buffer_load_dwordx4 a[108:111],v11,s[36:39],s63 offen offset:3072 sc1
v_mfma_f32_16x16x32_bf16 v[204:207],v[220:223],v[220:223],v[204:207]
s_add_u32 s63,s87,109051904
v_mfma_f32_16x16x32_bf16 v[172:175],v[24:27],v[36:39],0
v_mfma_f32_16x16x32_bf16 v[176:179],v[20:23],v[40:43],0
buffer_load_dwordx4 a[112:115],v11,s[36:39],s63 offen offset:0 sc1
v_mfma_f32_16x16x32_bf16 v[180:183],v[24:27],v[40:43],0
s_nop 0
v_add_f32_dpp v224,v28,v28 row_ror:8 row_mask:0xf bank_mask:0xf
s_nop 0
v_cndmask_b32 v225,v204,v205,s[46:47]
v_cndmask_b32 v226,v206,v207,s[46:47]
v_cndmask_b32 v225,v225,v226,s[56:57]
v_mfma_f32_16x16x32_bf16 v[184:187],v[20:23],v[44:47],0
ds_write_b32 v2,v224 offset:0
ds_write_b32 v0,v225 offset:0
v_mfma_f32_16x16x32_bf16 v[188:191],v[24:27],v[44:47],0
s_waitcnt lgkmcnt(0)
buffer_load_dwordx4 a[116:119],v11,s[36:39],s63 offen offset:1024 sc1
s_barrier
buffer_load_dwordx4 a[120:123],v11,s[36:39],s63 offen offset:2048 sc1
ds_read_b128 v[196:199],v3 offset:0
buffer_load_dwordx4 a[124:127],v11,s[36:39],s63 offen offset:3072 sc1
ds_read_b128 v[200:203],v3 offset:16
ds_read_b128 v[204:207],v3 offset:32
s_waitcnt lgkmcnt(0)
buffer_load_dword v8,v6,s[36:39],s69 offen offset:0 sc1
v_add_f32 v196,v196,v197
v_add_f32 v198,v198,v199
v_add_f32 v200,v200,v204
v_add_f32 v201,v201,v205
v_add_f32 v202,v202,v206
v_add_f32 v203,v203,v207
v_add_f32 v196,v196,v198
v_add_f32 v200,v200,v201
v_add_f32 v202,v202,v203
v_mul_f32 v196,s76,v196
v_add_f32 v197,v200,v202
v_mul_f32 v197,s76,v197
v_fma_f32 v197,-v196,v196,v197
v_max_f32 v197,0,v197
v_add_f32 v197,s77,v197
v_rsq_f32 v228,v197
s_nop 0
v_mul_f32_e64 v230,-v196,v228
v_mov_b32 v229,v228
v_mov_b32 v231,v230
v_pk_fma_f32 v[196:197],v[128:129],v[228:229],v[230:231]
v_pk_fma_f32 v[198:199],v[130:131],v[228:229],v[230:231]
v_pk_fma_f32 v[200:201],v[132:133],v[228:229],v[230:231]
v_pk_fma_f32 v[202:203],v[134:135],v[228:229],v[230:231]
v_pk_fma_f32 v[128:129],v[196:197],v[160:161],v[128:129]
v_pk_fma_f32 v[130:131],v[198:199],v[162:163],v[130:131]
v_pk_fma_f32 v[132:133],v[200:201],v[164:165],v[132:133]
v_pk_fma_f32 v[134:135],v[202:203],v[166:167],v[134:135]
v_pk_fma_f32 v[196:197],v[136:137],v[228:229],v[230:231]
v_pk_fma_f32 v[198:199],v[138:139],v[228:229],v[230:231]
v_pk_fma_f32 v[200:201],v[140:141],v[228:229],v[230:231]
v_pk_fma_f32 v[202:203],v[142:143],v[228:229],v[230:231]
v_pk_fma_f32 v[136:137],v[196:197],v[168:169],v[136:137]
v_pk_fma_f32 v[138:139],v[198:199],v[170:171],v[138:139]
v_pk_fma_f32 v[140:141],v[200:201],v[172:173],v[140:141]
v_pk_fma_f32 v[142:143],v[202:203],v[174:175],v[142:143]
v_pk_fma_f32 v[196:197],v[144:145],v[228:229],v[230:231]
v_pk_fma_f32 v[198:199],v[146:147],v[228:229],v[230:231]
v_pk_fma_f32 v[200:201],v[148:149],v[228:229],v[230:231]
v_pk_fma_f32 v[202:203],v[150:151],v[228:229],v[230:231]
v_pk_fma_f32 v[144:145],v[196:197],v[176:177],v[144:145]
v_pk_fma_f32 v[146:147],v[198:199],v[178:179],v[146:147]
v_pk_fma_f32 v[148:149],v[200:201],v[180:181],v[148:149]
v_pk_fma_f32 v[150:151],v[202:203],v[182:183],v[150:151]
v_pk_fma_f32 v[196:197],v[152:153],v[228:229],v[230:231]
v_pk_fma_f32 v[198:199],v[154:155],v[228:229],v[230:231]
v_pk_fma_f32 v[200:201],v[156:157],v[228:229],v[230:231]
v_pk_fma_f32 v[202:203],v[158:159],v[228:229],v[230:231]
v_pk_fma_f32 v[152:153],v[196:197],v[184:185],v[152:153]
v_pk_fma_f32 v[154:155],v[198:199],v[186:187],v[154:155]
v_pk_fma_f32 v[156:157],v[200:201],v[188:189],v[156:157]
v_pk_fma_f32 v[158:159],v[202:203],v[190:191],v[158:159]
v_cvt_pk_bf16_f32 v208,v128,v129
v_cvt_pk_bf16_f32 v209,v130,v131
v_cvt_pk_bf16_f32 v210,v132,v133
v_cvt_pk_bf16_f32 v211,v134,v135)"
R"(
v_cvt_pk_bf16_f32 v212,v136,v137
v_cvt_pk_bf16_f32 v213,v138,v139
v_cvt_pk_bf16_f32 v214,v140,v141
v_cvt_pk_bf16_f32 v215,v142,v143
v_mfma_f32_16x16x32_bf16 v[28:31],v[192:195],v[208:211],0
v_cvt_pk_bf16_f32 v216,v144,v145
v_cvt_pk_bf16_f32 v217,v146,v147
v_mfma_f32_16x16x32_bf16 v[204:207],v[208:211],v[208:211],0
v_cvt_pk_bf16_f32 v218,v148,v149
v_cvt_pk_bf16_f32 v219,v150,v151
v_mfma_f32_16x16x32_bf16 v[28:31],v[192:195],v[212:215],v[28:31]
v_cvt_pk_bf16_f32 v220,v152,v153
v_cvt_pk_bf16_f32 v221,v154,v155
v_mfma_f32_16x16x32_bf16 v[204:207],v[212:215],v[212:215],v[204:207]
v_cvt_pk_bf16_f32 v222,v156,v157
v_cvt_pk_bf16_f32 v223,v158,v159
v_mfma_f32_16x16x32_bf16 v[28:31],v[192:195],v[216:219],v[28:31]
v_mfma_f32_16x16x32_bf16 v[204:207],v[216:219],v[216:219],v[204:207]
v_mfma_f32_16x16x32_bf16 v[28:31],v[192:195],v[220:223],v[28:31]
v_mfma_f32_16x16x32_bf16 v[204:207],v[220:223],v[220:223],v[204:207]
s_waitcnt vmcnt(17)
buffer_store_dword v7,v5,s[36:39],s60 offen offset:0 sc0 sc1
s_mov_b32 s60,536870912
buffer_store_dwordx4 v[208:211],v1,s[36:39],s88 offen offset:0 sc0 sc1
buffer_store_dwordx4 v[212:215],v1,s[36:39],s88 offen offset:2048 sc0 sc1
s_add_u32 s88,s88,4096
s_nop 0
v_add_f32_dpp v224,v28,v28 row_ror:8 row_mask:0xf bank_mask:0xf
v_cndmask_b32 v225,v204,v205,s[46:47]
v_cndmask_b32 v226,v206,v207,s[46:47]
v_cndmask_b32 v225,v225,v226,s[56:57]
s_and_b32 s63,s89,128
ds_write_b32 v2,v224 offset:1024
ds_write_b32 v0,v225 offset:1024
buffer_store_dwordx4 v[216:219],v1,s[36:39],s88 offen offset:0 sc0 sc1
buffer_store_dwordx4 v[220:223],v1,s[36:39],s88 offen offset:2048 sc0 sc1
s_waitcnt lgkmcnt(0)
s_barrier
ds_read_b128 v[196:199],v3 offset:1024
ds_read_b128 v[200:203],v3 offset:1040
ds_read_b128 v[204:207],v3 offset:1056
s_waitcnt lgkmcnt(0)
v_add_f32 v196,v196,v197
v_add_f32 v198,v198,v199
v_add_f32 v200,v200,v204
v_add_f32 v201,v201,v205
v_add_f32 v202,v202,v206
v_add_f32 v203,v203,v207
v_add_f32 v196,v196,v198
v_add_f32 v200,v200,v201
v_add_f32 v202,v202,v203
v_mul_f32 v196,s76,v196
v_add_f32 v197,v200,v202
v_mul_f32 v197,s76,v197
v_fma_f32 v197,-v196,v196,v197
v_max_f32 v197,0,v197
v_add_f32 v198,s77,v197
v_fma_f32 v199,s77,v198,v197
v_rsq_f32 v205,v198
v_rsq_f32 v198,v199
v_mov_b32 v200,v196
v_mul_f32_e64 v204,-v196,v205
v_mul_f32 v203,v199,v198
v_mul_f32 v201,s78,v198
v_mul_f32 v199,v198,v198
v_mul_f32 v202,s79,v199
v_mul_f32 v202,v202,v201
s_lshr_b32 s63,s63,1
s_sub_u32 s63,s89,s63
buffer_store_dwordx4 v[200:203],v4,s[36:39],s89 offen offset:0 sc0 sc1
buffer_store_dwordx2 v[204:205],v10,s[36:39],s63 offen offset:256 sc0 sc1
s_add_u32 m0,s66,s52
s_cmp_ge_u32 s51,12582912
s_cbranch_scc1 Lr_ys3_%=
buffer_load_dwordx4 v11,s[36:39],s51 offen offset:0 sc1 lds
buffer_load_dwordx4 v11,s[36:39],s51 offen offset:1024 sc1 lds
buffer_load_dwordx4 v11,s[36:39],s51 offen offset:2048 sc1 lds
buffer_load_dwordx4 v11,s[36:39],s51 offen offset:3072 sc1 lds
s_lshl_b32 s63,s50,14
s_add_u32 s51,s51,s63
Lr_ys3_%=:
s_add_u32 s66,s66,16384
s_cmp_eq_u32 s66,151552
s_cselect_b32 s66,4096,s66
v_add_u32 v18,s66,v11
s_mov_b32 s60,s90
s_waitcnt vmcnt(7)
s_branch Lr_it1_%=
Lr_tc0_%=:
s_add_u32 s63,s87,71303168
buffer_load_dwordx4 a[64:67],v11,s[36:39],s63 offen offset:0 sc1
buffer_load_dwordx4 a[68:71],v11,s[36:39],s63 offen offset:1024 sc1
buffer_load_dwordx4 a[72:75],v11,s[36:39],s63 offen offset:2048 sc1
buffer_load_dwordx4 a[76:79],v11,s[36:39],s63 offen offset:3072 sc1
s_add_u32 s63,s87,89128960
buffer_load_dwordx4 a[80:83],v11,s[36:39],s63 offen offset:0 sc1
buffer_load_dwordx4 a[84:87],v11,s[36:39],s63 offen offset:1024 sc1
buffer_load_dwordx4 a[88:91],v11,s[36:39],s63 offen offset:2048 sc1
buffer_load_dwordx4 a[92:95],v11,s[36:39],s63 offen offset:3072 sc1
s_add_u32 s63,s87,99614720
buffer_load_dwordx4 a[96:99],v11,s[36:39],s63 offen offset:0 sc1
buffer_load_dwordx4 a[100:103],v11,s[36:39],s63 offen offset:1024 sc1
buffer_load_dwordx4 a[104:107],v11,s[36:39],s63 offen offset:2048 sc1
buffer_load_dwordx4 a[108:111],v11,s[36:39],s63 offen offset:3072 sc1
s_add_u32 s63,s87,109051904
buffer_load_dwordx4 a[112:115],v11,s[36:39],s63 offen offset:0 sc1
buffer_load_dwordx4 a[116:119],v11,s[36:39],s63 offen offset:1024 sc1
buffer_load_dwordx4 a[120:123],v11,s[36:39],s63 offen offset:2048 sc1
buffer_load_dwordx4 a[124:127],v11,s[36:39],s63 offen offset:3072 sc1
buffer_load_dword v8,v6,s[36:39],s69 offen offset:0 sc1
s_waitcnt vmcnt(17)
buffer_store_dword v7,v5,s[36:39],s60 offen offset:0 sc0 sc1
s_mov_b32 s60,536870912
s_waitcnt vmcnt(1)
s_branch Lr_it1_%=
Lr_it1_%=:
s_mov_b32 s88,s58
s_mov_b32 s89,s59
s_mov_b32 s90,s49
s_add_u32 s54,s54,s50
s_lshr_b32 s63,s54,1
s_and_b32 s64,s54,1
s_lshl_b32 s64,s64,7
s_lshl_b32 s87,s54,14
s_lshl_b32 s58,s63,15
s_add_u32 s58,s58,s64
s_add_u32 s58,s58,16908288
s_lshl_b32 s59,s63,9
s_add_u32 s59,s59,s64
s_add_u32 s59,s59,118556672
s_lshl_b32 s49,s54,9
s_add_u32 s49,s49,s92
s_lshl_b32 s91,s63,11
s_add_u32 s91,s91,121405440
s_lshl_b32 s69,s50,10
s_add_u32 s69,s69,s91
v_cmp_eq_u32 vcc,s48,v8
s_nop 0
s_cmp_eq_u64 vcc,exec
s_cselect_b32 s63,s54,256
s_cmp_lt_u32 s63,256
s_cbranch_scc1 Lr_ta1_%=
ds_read_b128 v[32:35],v18 offset:0
ds_read_b128 v[36:39],v18 offset:1024
ds_read_b128 v[40:43],v18 offset:2048
ds_read_b128 v[44:47],v18 offset:3072
v_mfma_f32_16x16x32_bf16 v[128:131],v[20:23],a[64:67],0
v_mfma_f32_16x16x32_bf16 v[132:135],v[24:27],a[64:67],0
v_mfma_f32_16x16x32_bf16 v[128:131],v[20:23],a[80:83],v[128:131]
v_mfma_f32_16x16x32_bf16 v[132:135],v[24:27],a[80:83],v[132:135]
v_mfma_f32_16x16x32_bf16 v[128:131],v[20:23],a[96:99],v[128:131]
v_mfma_f32_16x16x32_bf16 v[132:135],v[24:27],a[96:99],v[132:135]
v_mfma_f32_16x16x32_bf16 v[128:131],v[20:23],a[112:115],v[128:131]
v_mfma_f32_16x16x32_bf16 v[132:135],v[24:27],a[112:115],v[132:135]
v_mfma_f32_16x16x32_bf16 v[136:139],v[20:23],a[68:71],0
v_mfma_f32_16x16x32_bf16 v[140:143],v[24:27],a[68:71],0
v_mfma_f32_16x16x32_bf16 v[136:139],v[20:23],a[84:87],v[136:139]
v_mfma_f32_16x16x32_bf16 v[140:143],v[24:27],a[84:87],v[140:143]
v_mfma_f32_16x16x32_bf16 v[136:139],v[20:23],a[100:103],v[136:139]
s_nop 1
v_cvt_pk_bf16_f32 v208,v128,v129
v_cvt_pk_bf16_f32 v209,v130,v131
v_mfma_f32_16x16x32_bf16 v[140:143],v[24:27],a[100:103],v[140:143]
v_cvt_pk_bf16_f32 v210,v132,v133
v_cvt_pk_bf16_f32 v211,v134,v135
v_mfma_f32_16x16x32_bf16 v[136:139],v[20:23],a[116:119],v[136:139]
v_mfma_f32_16x16x32_bf16 v[140:143],v[24:27],a[116:119],v[140:143]
v_mfma_f32_16x16x32_bf16 v[144:147],v[20:23],a[72:75],0
v_mfma_f32_16x16x32_bf16 v[148:151],v[24:27],a[72:75],0
v_mfma_f32_16x16x32_bf16 v[144:147],v[20:23],a[88:91],v[144:147]
v_mfma_f32_16x16x32_bf16 v[148:151],v[24:27],a[88:91],v[148:151]
v_mfma_f32_16x16x32_bf16 v[144:147],v[20:23],a[104:107],v[144:147]
s_nop 1
v_cvt_pk_bf16_f32 v212,v136,v137
v_cvt_pk_bf16_f32 v213,v138,v139
v_mfma_f32_16x16x32_bf16 v[148:151],v[24:27],a[104:107],v[148:151]
v_cvt_pk_bf16_f32 v214,v140,v141
v_cvt_pk_bf16_f32 v215,v142,v143
v_mfma_f32_16x16x32_bf16 v[144:147],v[20:23],a[120:123],v[144:147]
v_mfma_f32_16x16x32_bf16 v[148:151],v[24:27],a[120:123],v[148:151]
v_mfma_f32_16x16x32_bf16 v[28:31],v[192:195],v[208:211],0
v_mfma_f32_16x16x32_bf16 v[204:207],v[208:211],v[208:211],0
v_mfma_f32_16x16x32_bf16 v[152:155],v[20:23],a[76:79],0
v_mfma_f32_16x16x32_bf16 v[156:159],v[24:27],a[76:79],0
v_mfma_f32_16x16x32_bf16 v[152:155],v[20:23],a[92:95],v[152:155]
v_mfma_f32_16x16x32_bf16 v[156:159],v[24:27],a[92:95],v[156:159]
v_mfma_f32_16x16x32_bf16 v[152:155],v[20:23],a[108:111],v[152:155]
v_cvt_pk_bf16_f32 v216,v144,v145
v_cvt_pk_bf16_f32 v217,v146,v147
v_mfma_f32_16x16x32_bf16 v[156:159],v[24:27],a[108:111],v[156:159]
v_cvt_pk_bf16_f32 v218,v148,v149
v_cvt_pk_bf16_f32 v219,v150,v151
v_mfma_f32_16x16x32_bf16 v[152:155],v[20:23],a[124:127],v[152:155]
v_mfma_f32_16x16x32_bf16 v[156:159],v[24:27],a[124:127],v[156:159]
v_mfma_f32_16x16x32_bf16 v[28:31],v[192:195],v[212:215],v[28:31]
v_mfma_f32_16x16x32_bf16 v[204:207],v[212:215],v[212:215],v[204:207]
v_mfma_f32_16x16x32_bf16 v[28:31],v[192:195],v[216:219],v[28:31]
v_mfma_f32_16x16x32_bf16 v[204:207],v[216:219],v[216:219],v[204:207]
s_waitcnt lgkmcnt(0)
v_mfma_f32_16x16x32_bf16 v[160:163],v[20:23],v[32:35],0)"
R"(
s_nop 0
v_cvt_pk_bf16_f32 v220,v152,v153
v_cvt_pk_bf16_f32 v221,v154,v155
v_mfma_f32_16x16x32_bf16 v[164:167],v[24:27],v[32:35],0
v_cvt_pk_bf16_f32 v222,v156,v157
v_cvt_pk_bf16_f32 v223,v158,v159
v_mfma_f32_16x16x32_bf16 v[168:171],v[20:23],v[36:39],0
s_nop 0
v_mfma_f32_16x16x32_bf16 v[28:31],v[192:195],v[220:223],v[28:31]
v_mfma_f32_16x16x32_bf16 v[204:207],v[220:223],v[220:223],v[204:207]
v_mfma_f32_16x16x32_bf16 v[172:175],v[24:27],v[36:39],0
v_mfma_f32_16x16x32_bf16 v[176:179],v[20:23],v[40:43],0
v_mfma_f32_16x16x32_bf16 v[180:183],v[24:27],v[40:43],0
s_nop 3
v_add_f32_dpp v224,v28,v28 row_ror:8 row_mask:0xf bank_mask:0xf
v_cndmask_b32 v225,v204,v205,s[46:47]
v_cndmask_b32 v226,v206,v207,s[46:47]
v_cndmask_b32 v225,v225,v226,s[56:57]
v_mfma_f32_16x16x32_bf16 v[184:187],v[20:23],v[44:47],0
ds_write_b32 v2,v224 offset:0
ds_write_b32 v0,v225 offset:0
v_mfma_f32_16x16x32_bf16 v[188:191],v[24:27],v[44:47],0
s_waitcnt lgkmcnt(0)
s_barrier
ds_read_b128 v[196:199],v3 offset:0
ds_read_b128 v[200:203],v3 offset:16
ds_read_b128 v[204:207],v3 offset:32
s_waitcnt lgkmcnt(0)
v_add_f32 v196,v196,v197
v_add_f32 v198,v198,v199
v_add_f32 v200,v200,v204
v_add_f32 v201,v201,v205
v_add_f32 v202,v202,v206
v_add_f32 v203,v203,v207
v_add_f32 v196,v196,v198
v_add_f32 v200,v200,v201
v_add_f32 v202,v202,v203
v_mul_f32 v196,s76,v196
v_add_f32 v197,v200,v202
v_mul_f32 v197,s76,v197
v_fma_f32 v197,-v196,v196,v197
v_max_f32 v197,0,v197
v_add_f32 v197,s77,v197
v_rsq_f32 v228,v197
s_nop 0
v_mul_f32_e64 v230,-v196,v228
v_mov_b32 v229,v228
v_mov_b32 v231,v230
v_pk_fma_f32 v[196:197],v[128:129],v[228:229],v[230:231]
v_pk_fma_f32 v[198:199],v[130:131],v[228:229],v[230:231]
v_pk_fma_f32 v[200:201],v[132:133],v[228:229],v[230:231]
v_pk_fma_f32 v[202:203],v[134:135],v[228:229],v[230:231]
v_pk_fma_f32 v[128:129],v[196:197],v[160:161],v[128:129]
v_pk_fma_f32 v[130:131],v[198:199],v[162:163],v[130:131]
v_pk_fma_f32 v[132:133],v[200:201],v[164:165],v[132:133]
v_pk_fma_f32 v[134:135],v[202:203],v[166:167],v[134:135]
v_pk_fma_f32 v[196:197],v[136:137],v[228:229],v[230:231]
v_pk_fma_f32 v[198:199],v[138:139],v[228:229],v[230:231]
v_pk_fma_f32 v[200:201],v[140:141],v[228:229],v[230:231]
v_pk_fma_f32 v[202:203],v[142:143],v[228:229],v[230:231]
v_pk_fma_f32 v[136:137],v[196:197],v[168:169],v[136:137]
v_pk_fma_f32 v[138:139],v[198:199],v[170:171],v[138:139]
v_pk_fma_f32 v[140:141],v[200:201],v[172:173],v[140:141]
v_pk_fma_f32 v[142:143],v[202:203],v[174:175],v[142:143]
v_pk_fma_f32 v[196:197],v[144:145],v[228:229],v[230:231]
v_pk_fma_f32 v[198:199],v[146:147],v[228:229],v[230:231]
v_pk_fma_f32 v[200:201],v[148:149],v[228:229],v[230:231]
v_pk_fma_f32 v[202:203],v[150:151],v[228:229],v[230:231]
v_pk_fma_f32 v[144:145],v[196:197],v[176:177],v[144:145]
v_pk_fma_f32 v[146:147],v[198:199],v[178:179],v[146:147]
v_pk_fma_f32 v[148:149],v[200:201],v[180:181],v[148:149]
v_pk_fma_f32 v[150:151],v[202:203],v[182:183],v[150:151]
v_pk_fma_f32 v[196:197],v[152:153],v[228:229],v[230:231]
v_pk_fma_f32 v[198:199],v[154:155],v[228:229],v[230:231]
v_pk_fma_f32 v[200:201],v[156:157],v[228:229],v[230:231]
v_pk_fma_f32 v[202:203],v[158:159],v[228:229],v[230:231]
v_pk_fma_f32 v[152:153],v[196:197],v[184:185],v[152:153]
v_pk_fma_f32 v[154:155],v[198:199],v[186:187],v[154:155]
v_pk_fma_f32 v[156:157],v[200:201],v[188:189],v[156:157]
v_pk_fma_f32 v[158:159],v[202:203],v[190:191],v[158:159]
v_cvt_pk_bf16_f32 v208,v128,v129
v_cvt_pk_bf16_f32 v209,v130,v131
v_cvt_pk_bf16_f32 v210,v132,v133
v_cvt_pk_bf16_f32 v211,v134,v135
v_cvt_pk_bf16_f32 v212,v136,v137
v_cvt_pk_bf16_f32 v213,v138,v139
v_cvt_pk_bf16_f32 v214,v140,v141
v_cvt_pk_bf16_f32 v215,v142,v143
v_mfma_f32_16x16x32_bf16 v[28:31],v[192:195],v[208:211],0
v_cvt_pk_bf16_f32 v216,v144,v145
v_cvt_pk_bf16_f32 v217,v146,v147
v_mfma_f32_16x16x32_bf16 v[204:207],v[208:211],v[208:211],0
v_cvt_pk_bf16_f32 v218,v148,v149
v_cvt_pk_bf16_f32 v219,v150,v151
v_mfma_f32_16x16x32_bf16 v[28:31],v[192:195],v[212:215],v[28:31]
v_cvt_pk_bf16_f32 v220,v152,v153
v_cvt_pk_bf16_f32 v221,v154,v155
v_mfma_f32_16x16x32_bf16 v[204:207],v[212:215],v[212:215],v[204:207]
v_cvt_pk_bf16_f32 v222,v156,v157
v_cvt_pk_bf16_f32 v223,v158,v159
v_mfma_f32_16x16x32_bf16 v[28:31],v[192:195],v[216:219],v[28:31]
v_mfma_f32_16x16x32_bf16 v[204:207],v[216:219],v[216:219],v[204:207]
v_mfma_f32_16x16x32_bf16 v[28:31],v[192:195],v[220:223],v[28:31]
v_mfma_f32_16x16x32_bf16 v[204:207],v[220:223],v[220:223],v[204:207]
s_waitcnt vmcnt(0)
buffer_store_dword v7,v5,s[36:39],s60 offen offset:0 sc0 sc1
s_mov_b32 s60,536870912
buffer_load_dword v8,v6,s[36:39],s91 offen offset:0 sc1
buffer_store_dwordx4 v[208:211],v1,s[36:39],s88 offen offset:0 sc0 sc1
buffer_store_dwordx4 v[212:215],v1,s[36:39],s88 offen offset:2048 sc0 sc1
s_add_u32 s88,s88,4096
v_add_f32_dpp v224,v28,v28 row_ror:8 row_mask:0xf bank_mask:0xf
v_cndmask_b32 v225,v204,v205,s[46:47]
v_cndmask_b32 v226,v206,v207,s[46:47]
v_cndmask_b32 v225,v225,v226,s[56:57]
s_and_b32 s63,s89,128
ds_write_b32 v2,v224 offset:1024
ds_write_b32 v0,v225 offset:1024
buffer_store_dwordx4 v[216:219],v1,s[36:39],s88 offen offset:0 sc0 sc1
buffer_store_dwordx4 v[220:223],v1,s[36:39],s88 offen offset:2048 sc0 sc1
s_waitcnt lgkmcnt(0)
s_barrier
ds_read_b128 v[196:199],v3 offset:1024
ds_read_b128 v[200:203],v3 offset:1040
ds_read_b128 v[204:207],v3 offset:1056
s_waitcnt lgkmcnt(0)
v_add_f32 v196,v196,v197
v_add_f32 v198,v198,v199
v_add_f32 v200,v200,v204
v_add_f32 v201,v201,v205
v_add_f32 v202,v202,v206
v_add_f32 v203,v203,v207
v_add_f32 v196,v196,v198
v_add_f32 v200,v200,v201
v_add_f32 v202,v202,v203
v_mul_f32 v196,s76,v196
v_add_f32 v197,v200,v202
v_mul_f32 v197,s76,v197
v_fma_f32 v197,-v196,v196,v197
v_max_f32 v197,0,v197
v_add_f32 v198,s77,v197
v_fma_f32 v199,s77,v198,v197
v_rsq_f32 v205,v198
v_rsq_f32 v198,v199
v_mov_b32 v200,v196
v_mul_f32_e64 v204,-v196,v205
v_mul_f32 v203,v199,v198
v_mul_f32 v201,s78,v198
v_mul_f32 v199,v198,v198
v_mul_f32 v202,s79,v199
v_mul_f32 v202,v202,v201
s_lshr_b32 s63,s63,1
s_sub_u32 s63,s89,s63
buffer_store_dwordx4 v[200:203],v4,s[36:39],s89 offen offset:0 sc0 sc1
buffer_store_dwordx2 v[204:205],v10,s[36:39],s63 offen offset:256 sc0 sc1
s_add_u32 m0,s66,s52
s_cmp_ge_u32 s51,12582912
s_cbranch_scc1 Lr_ys4_%=
buffer_load_dwordx4 v11,s[36:39],s51 offen offset:0 sc1 lds
buffer_load_dwordx4 v11,s[36:39],s51 offen offset:1024 sc1 lds
buffer_load_dwordx4 v11,s[36:39],s51 offen offset:2048 sc1 lds
buffer_load_dwordx4 v11,s[36:39],s51 offen offset:3072 sc1 lds
s_lshl_b32 s63,s50,14
s_add_u32 s51,s51,s63
Lr_ys4_%=:
s_add_u32 s66,s66,16384
s_cmp_eq_u32 s66,151552
s_cselect_b32 s66,4096,s66
v_add_u32 v18,s66,v11
s_mov_b32 s60,s90
s_waitcnt vmcnt(6)
v_cmp_eq_u32 vcc,s48,v8
s_nop 0
s_cmp_eq_u64 vcc,exec
s_cselect_b32 s63,s54,256
s_cmp_lt_u32 s63,256
s_cbranch_scc1 Lr_tc1_%=
buffer_load_dword v8,v6,s[36:39],s91 offen offset:0 sc1
s_waitcnt vmcnt(0)
buffer_store_dword v7,v5,s[36:39],s60 offen offset:0 sc0 sc1
s_mov_b32 s60,536870912
v_cmp_eq_u32 vcc,s48,v8
s_nop 0
s_cmp_eq_u64 vcc,exec
s_cselect_b32 s63,s54,256
s_cmp_lt_u32 s63,256
s_cbranch_scc1 Lr_bq1_%=
s_cmp_ge_u32 s54,256
s_cbranch_scc1 Lr_done_%=
Lr_bp1_%=:
buffer_load_dword v8,v6,s[36:39],s91 offen offset:0 sc1
s_waitcnt vmcnt(0)
v_cmp_eq_u32 vcc,s48,v8
s_nop 0
s_cmp_eq_u64 vcc,exec
s_cselect_b32 s63,1,s96
s_cmp_lg_u32 s63,0
s_cbranch_scc1 Lr_bq1_%=
s_add_u32 s74,s74,1
s_cmp_ge_u32 s74,20000)"
R"(
s_cselect_b32 s96,1,0
s_sleep 4
s_branch Lr_bp1_%=
Lr_bq1_%=:
s_add_u32 s63,s87,71303168
buffer_load_dwordx4 a[0:3],v11,s[36:39],s63 offen offset:0 sc1
buffer_load_dwordx4 a[4:7],v11,s[36:39],s63 offen offset:1024 sc1
buffer_load_dwordx4 a[8:11],v11,s[36:39],s63 offen offset:2048 sc1
buffer_load_dwordx4 a[12:15],v11,s[36:39],s63 offen offset:3072 sc1
s_add_u32 s63,s87,89128960
buffer_load_dwordx4 a[16:19],v11,s[36:39],s63 offen offset:0 sc1
buffer_load_dwordx4 a[20:23],v11,s[36:39],s63 offen offset:1024 sc1
buffer_load_dwordx4 a[24:27],v11,s[36:39],s63 offen offset:2048 sc1
buffer_load_dwordx4 a[28:31],v11,s[36:39],s63 offen offset:3072 sc1
s_add_u32 s63,s87,99614720
buffer_load_dwordx4 a[32:35],v11,s[36:39],s63 offen offset:0 sc1
buffer_load_dwordx4 a[36:39],v11,s[36:39],s63 offen offset:1024 sc1
buffer_load_dwordx4 a[40:43],v11,s[36:39],s63 offen offset:2048 sc1
buffer_load_dwordx4 a[44:47],v11,s[36:39],s63 offen offset:3072 sc1
s_add_u32 s63,s87,109051904
buffer_load_dwordx4 a[48:51],v11,s[36:39],s63 offen offset:0 sc1
buffer_load_dwordx4 a[52:55],v11,s[36:39],s63 offen offset:1024 sc1
buffer_load_dwordx4 a[56:59],v11,s[36:39],s63 offen offset:2048 sc1
buffer_load_dwordx4 a[60:63],v11,s[36:39],s63 offen offset:3072 sc1
buffer_load_dword v8,v6,s[36:39],s69 offen offset:0 sc1
s_waitcnt vmcnt(0)
s_branch Lr_it0_%=
Lr_ta1_%=:
ds_read_b128 v[32:35],v18 offset:0
ds_read_b128 v[36:39],v18 offset:1024
ds_read_b128 v[40:43],v18 offset:2048
ds_read_b128 v[44:47],v18 offset:3072
v_mfma_f32_16x16x32_bf16 v[128:131],v[20:23],a[64:67],0
s_add_u32 s63,s87,71303168
v_mfma_f32_16x16x32_bf16 v[132:135],v[24:27],a[64:67],0
buffer_load_dwordx4 a[0:3],v11,s[36:39],s63 offen offset:0 sc1
v_mfma_f32_16x16x32_bf16 v[128:131],v[20:23],a[80:83],v[128:131]
v_mfma_f32_16x16x32_bf16 v[132:135],v[24:27],a[80:83],v[132:135]
buffer_load_dwordx4 a[4:7],v11,s[36:39],s63 offen offset:1024 sc1
v_mfma_f32_16x16x32_bf16 v[128:131],v[20:23],a[96:99],v[128:131]
v_mfma_f32_16x16x32_bf16 v[132:135],v[24:27],a[96:99],v[132:135]
v_mfma_f32_16x16x32_bf16 v[128:131],v[20:23],a[112:115],v[128:131]
v_mfma_f32_16x16x32_bf16 v[132:135],v[24:27],a[112:115],v[132:135]
buffer_load_dwordx4 a[8:11],v11,s[36:39],s63 offen offset:2048 sc1
v_mfma_f32_16x16x32_bf16 v[136:139],v[20:23],a[68:71],0
v_mfma_f32_16x16x32_bf16 v[140:143],v[24:27],a[68:71],0
v_mfma_f32_16x16x32_bf16 v[136:139],v[20:23],a[84:87],v[136:139]
v_mfma_f32_16x16x32_bf16 v[140:143],v[24:27],a[84:87],v[140:143]
buffer_load_dwordx4 a[12:15],v11,s[36:39],s63 offen offset:3072 sc1
v_mfma_f32_16x16x32_bf16 v[136:139],v[20:23],a[100:103],v[136:139]
v_cvt_pk_bf16_f32 v208,v128,v129
v_cvt_pk_bf16_f32 v209,v130,v131
v_mfma_f32_16x16x32_bf16 v[140:143],v[24:27],a[100:103],v[140:143]
v_cvt_pk_bf16_f32 v210,v132,v133
v_cvt_pk_bf16_f32 v211,v134,v135
v_mfma_f32_16x16x32_bf16 v[136:139],v[20:23],a[116:119],v[136:139]
s_add_u32 s63,s87,89128960
v_mfma_f32_16x16x32_bf16 v[140:143],v[24:27],a[116:119],v[140:143]
buffer_load_dwordx4 a[16:19],v11,s[36:39],s63 offen offset:0 sc1
v_mfma_f32_16x16x32_bf16 v[144:147],v[20:23],a[72:75],0
v_mfma_f32_16x16x32_bf16 v[148:151],v[24:27],a[72:75],0
v_mfma_f32_16x16x32_bf16 v[144:147],v[20:23],a[88:91],v[144:147]
v_mfma_f32_16x16x32_bf16 v[148:151],v[24:27],a[88:91],v[148:151]
buffer_load_dwordx4 a[20:23],v11,s[36:39],s63 offen offset:1024 sc1
v_mfma_f32_16x16x32_bf16 v[144:147],v[20:23],a[104:107],v[144:147]
v_cvt_pk_bf16_f32 v212,v136,v137
v_cvt_pk_bf16_f32 v213,v138,v139
v_mfma_f32_16x16x32_bf16 v[148:151],v[24:27],a[104:107],v[148:151]
v_cvt_pk_bf16_f32 v214,v140,v141
v_cvt_pk_bf16_f32 v215,v142,v143
v_mfma_f32_16x16x32_bf16 v[144:147],v[20:23],a[120:123],v[144:147]
v_mfma_f32_16x16x32_bf16 v[148:151],v[24:27],a[120:123],v[148:151]
buffer_load_dwordx4 a[24:27],v11,s[36:39],s63 offen offset:2048 sc1
v_mfma_f32_16x16x32_bf16 v[28:31],v[192:195],v[208:211],0
v_mfma_f32_16x16x32_bf16 v[204:207],v[208:211],v[208:211],0
v_mfma_f32_16x16x32_bf16 v[152:155],v[20:23],a[76:79],0
v_mfma_f32_16x16x32_bf16 v[156:159],v[24:27],a[76:79],0
buffer_load_dwordx4 a[28:31],v11,s[36:39],s63 offen offset:3072 sc1
v_mfma_f32_16x16x32_bf16 v[152:155],v[20:23],a[92:95],v[152:155]
s_add_u32 s63,s87,99614720
v_mfma_f32_16x16x32_bf16 v[156:159],v[24:27],a[92:95],v[156:159]
v_mfma_f32_16x16x32_bf16 v[152:155],v[20:23],a[108:111],v[152:155]
v_cvt_pk_bf16_f32 v216,v144,v145
v_cvt_pk_bf16_f32 v217,v146,v147
v_mfma_f32_16x16x32_bf16 v[156:159],v[24:27],a[108:111],v[156:159]
v_cvt_pk_bf16_f32 v218,v148,v149
v_cvt_pk_bf16_f32 v219,v150,v151
v_mfma_f32_16x16x32_bf16 v[152:155],v[20:23],a[124:127],v[152:155]
buffer_load_dwordx4 a[32:35],v11,s[36:39],s63 offen offset:0 sc1
v_mfma_f32_16x16x32_bf16 v[156:159],v[24:27],a[124:127],v[156:159]
v_mfma_f32_16x16x32_bf16 v[28:31],v[192:195],v[212:215],v[28:31]
v_mfma_f32_16x16x32_bf16 v[204:207],v[212:215],v[212:215],v[204:207]
buffer_load_dwordx4 a[36:39],v11,s[36:39],s63 offen offset:1024 sc1
v_mfma_f32_16x16x32_bf16 v[28:31],v[192:195],v[216:219],v[28:31]
v_mfma_f32_16x16x32_bf16 v[204:207],v[216:219],v[216:219],v[204:207]
s_waitcnt lgkmcnt(0)
v_mfma_f32_16x16x32_bf16 v[160:163],v[20:23],v[32:35],0
v_cvt_pk_bf16_f32 v220,v152,v153
v_cvt_pk_bf16_f32 v221,v154,v155
v_mfma_f32_16x16x32_bf16 v[164:167],v[24:27],v[32:35],0
v_cvt_pk_bf16_f32 v222,v156,v157
v_cvt_pk_bf16_f32 v223,v158,v159
v_mfma_f32_16x16x32_bf16 v[168:171],v[20:23],v[36:39],0
buffer_load_dwordx4 a[40:43],v11,s[36:39],s63 offen offset:2048 sc1
v_mfma_f32_16x16x32_bf16 v[28:31],v[192:195],v[220:223],v[28:31]
buffer_load_dwordx4 a[44:47],v11,s[36:39],s63 offen offset:3072 sc1
v_mfma_f32_16x16x32_bf16 v[204:207],v[220:223],v[220:223],v[204:207]
s_add_u32 s63,s87,109051904
v_mfma_f32_16x16x32_bf16 v[172:175],v[24:27],v[36:39],0
v_mfma_f32_16x16x32_bf16 v[176:179],v[20:23],v[40:43],0
buffer_load_dwordx4 a[48:51],v11,s[36:39],s63 offen offset:0 sc1
v_mfma_f32_16x16x32_bf16 v[180:183],v[24:27],v[40:43],0
s_nop 0
v_add_f32_dpp v224,v28,v28 row_ror:8 row_mask:0xf bank_mask:0xf
s_nop 0
v_cndmask_b32 v225,v204,v205,s[46:47]
v_cndmask_b32 v226,v206,v207,s[46:47]
v_cndmask_b32 v225,v225,v226,s[56:57]
v_mfma_f32_16x16x32_bf16 v[184:187],v[20:23],v[44:47],0
ds_write_b32 v2,v224 offset:0
ds_write_b32 v0,v225 offset:0
v_mfma_f32_16x16x32_bf16 v[188:191],v[24:27],v[44:47],0
s_waitcnt lgkmcnt(0)
buffer_load_dwordx4 a[52:55],v11,s[36:39],s63 offen offset:1024 sc1
s_barrier
buffer_load_dwordx4 a[56:59],v11,s[36:39],s63 offen offset:2048 sc1
ds_read_b128 v[196:199],v3 offset:0
buffer_load_dwordx4 a[60:63],v11,s[36:39],s63 offen offset:3072 sc1
ds_read_b128 v[200:203],v3 offset:16
ds_read_b128 v[204:207],v3 offset:32
s_waitcnt lgkmcnt(0)
buffer_load_dword v8,v6,s[36:39],s69 offen offset:0 sc1
v_add_f32 v196,v196,v197
v_add_f32 v198,v198,v199
v_add_f32 v200,v200,v204
v_add_f32 v201,v201,v205
v_add_f32 v202,v202,v206
v_add_f32 v203,v203,v207
v_add_f32 v196,v196,v198
v_add_f32 v200,v200,v201
v_add_f32 v202,v202,v203
v_mul_f32 v196,s76,v196
v_add_f32 v197,v200,v202
v_mul_f32 v197,s76,v197
v_fma_f32 v197,-v196,v196,v197
v_max_f32 v197,0,v197
v_add_f32 v197,s77,v197
v_rsq_f32 v228,v197
s_nop 0
v_mul_f32_e64 v230,-v196,v228
v_mov_b32 v229,v228
v_mov_b32 v231,v230
v_pk_fma_f32 v[196:197],v[128:129],v[228:229],v[230:231]
v_pk_fma_f32 v[198:199],v[130:131],v[228:229],v[230:231]
v_pk_fma_f32 v[200:201],v[132:133],v[228:229],v[230:231]
v_pk_fma_f32 v[202:203],v[134:135],v[228:229],v[230:231]
v_pk_fma_f32 v[128:129],v[196:197],v[160:161],v[128:129]
v_pk_fma_f32 v[130:131],v[198:199],v[162:163],v[130:131]
v_pk_fma_f32 v[132:133],v[200:201],v[164:165],v[132:133]
v_pk_fma_f32 v[134:135],v[202:203],v[166:167],v[134:135]
v_pk_fma_f32 v[196:197],v[136:137],v[228:229],v[230:231]
v_pk_fma_f32 v[198:199],v[138:139],v[228:229],v[230:231]
v_pk_fma_f32 v[200:201],v[140:141],v[228:229],v[230:231]
v_pk_fma_f32 v[202:203],v[142:143],v[228:229],v[230:231]
v_pk_fma_f32 v[136:137],v[196:197],v[168:169],v[136:137]
v_pk_fma_f32 v[138:139],v[198:199],v[170:171],v[138:139]
v_pk_fma_f32 v[140:141],v[200:201],v[172:173],v[140:141]
v_pk_fma_f32 v[142:143],v[202:203],v[174:175],v[142:143]
v_pk_fma_f32 v[196:197],v[144:145],v[228:229],v[230:231]
v_pk_fma_f32 v[198:199],v[146:147],v[228:229],v[230:231]
v_pk_fma_f32 v[200:201],v[148:149],v[228:229],v[230:231]
v_pk_fma_f32 v[202:203],v[150:151],v[228:229],v[230:231]
v_pk_fma_f32 v[144:145],v[196:197],v[176:177],v[144:145]
v_pk_fma_f32 v[146:147],v[198:199],v[178:179],v[146:147]
v_pk_fma_f32 v[148:149],v[200:201],v[180:181],v[148:149]
v_pk_fma_f32 v[150:151],v[202:203],v[182:183],v[150:151]
v_pk_fma_f32 v[196:197],v[152:153],v[228:229],v[230:231]
v_pk_fma_f32 v[198:199],v[154:155],v[228:229],v[230:231]
v_pk_fma_f32 v[200:201],v[156:157],v[228:229],v[230:231]
v_pk_fma_f32 v[202:203],v[158:159],v[228:229],v[230:231]
v_pk_fma_f32 v[152:153],v[196:197],v[184:185],v[152:153]
v_pk_fma_f32 v[154:155],v[198:199],v[186:187],v[154:155]
v_pk_fma_f32 v[156:157],v[200:201],v[188:189],v[156:157]
v_pk_fma_f32 v[158:159],v[202:203],v[190:191],v[158:159]
v_cvt_pk_bf16_f32 v208,v128,v129
v_cvt_pk_bf16_f32 v209,v130,v131
v_cvt_pk_bf16_f32 v210,v132,v133
v_cvt_pk_bf16_f32 v211,v134,v135
v_cvt_pk_bf16_f32 v212,v136,v137
v_cvt_pk_bf16_f32 v213,v138,v139
v_cvt_pk_bf16_f32 v214,v140,v141
v_cvt_pk_bf16_f32 v215,v142,v143
v_mfma_f32_16x16x32_bf16 v[28:31],v[192:195],v[208:211],0
v_cvt_pk_bf16_f32 v216,v144,v145
v_cvt_pk_bf16_f32 v217,v146,v147
v_mfma_f32_16x16x32_bf16 v[204:207],v[208:211],v[208:211],0
v_cvt_pk_bf16_f32 v218,v148,v149
v_cvt_pk_bf16_f32 v219,v150,v151
v_mfma_f32_16x16x32_bf16 v[28:31],v[192:195],v[212:215],v[28:31]
v_cvt_pk_bf16_f32 v220,v152,v153)"
R"(
v_cvt_pk_bf16_f32 v221,v154,v155
v_mfma_f32_16x16x32_bf16 v[204:207],v[212:215],v[212:215],v[204:207]
v_cvt_pk_bf16_f32 v222,v156,v157
v_cvt_pk_bf16_f32 v223,v158,v159
v_mfma_f32_16x16x32_bf16 v[28:31],v[192:195],v[216:219],v[28:31]
v_mfma_f32_16x16x32_bf16 v[204:207],v[216:219],v[216:219],v[204:207]
v_mfma_f32_16x16x32_bf16 v[28:31],v[192:195],v[220:223],v[28:31]
v_mfma_f32_16x16x32_bf16 v[204:207],v[220:223],v[220:223],v[204:207]
s_waitcnt vmcnt(17)
buffer_store_dword v7,v5,s[36:39],s60 offen offset:0 sc0 sc1
s_mov_b32 s60,536870912
buffer_store_dwordx4 v[208:211],v1,s[36:39],s88 offen offset:0 sc0 sc1
buffer_store_dwordx4 v[212:215],v1,s[36:39],s88 offen offset:2048 sc0 sc1
s_add_u32 s88,s88,4096
s_nop 0
v_add_f32_dpp v224,v28,v28 row_ror:8 row_mask:0xf bank_mask:0xf
v_cndmask_b32 v225,v204,v205,s[46:47]
v_cndmask_b32 v226,v206,v207,s[46:47]
v_cndmask_b32 v225,v225,v226,s[56:57]
s_and_b32 s63,s89,128
ds_write_b32 v2,v224 offset:1024
ds_write_b32 v0,v225 offset:1024
buffer_store_dwordx4 v[216:219],v1,s[36:39],s88 offen offset:0 sc0 sc1
buffer_store_dwordx4 v[220:223],v1,s[36:39],s88 offen offset:2048 sc0 sc1
s_waitcnt lgkmcnt(0)
s_barrier
ds_read_b128 v[196:199],v3 offset:1024
ds_read_b128 v[200:203],v3 offset:1040
ds_read_b128 v[204:207],v3 offset:1056
s_waitcnt lgkmcnt(0)
v_add_f32 v196,v196,v197
v_add_f32 v198,v198,v199
v_add_f32 v200,v200,v204
v_add_f32 v201,v201,v205
v_add_f32 v202,v202,v206
v_add_f32 v203,v203,v207
v_add_f32 v196,v196,v198
v_add_f32 v200,v200,v201
v_add_f32 v202,v202,v203
v_mul_f32 v196,s76,v196
v_add_f32 v197,v200,v202
v_mul_f32 v197,s76,v197
v_fma_f32 v197,-v196,v196,v197
v_max_f32 v197,0,v197
v_add_f32 v198,s77,v197
v_fma_f32 v199,s77,v198,v197
v_rsq_f32 v205,v198
v_rsq_f32 v198,v199
v_mov_b32 v200,v196
v_mul_f32_e64 v204,-v196,v205
v_mul_f32 v203,v199,v198
v_mul_f32 v201,s78,v198
v_mul_f32 v199,v198,v198
v_mul_f32 v202,s79,v199
v_mul_f32 v202,v202,v201
s_lshr_b32 s63,s63,1
s_sub_u32 s63,s89,s63
buffer_store_dwordx4 v[200:203],v4,s[36:39],s89 offen offset:0 sc0 sc1
buffer_store_dwordx2 v[204:205],v10,s[36:39],s63 offen offset:256 sc0 sc1
s_add_u32 m0,s66,s52
s_cmp_ge_u32 s51,12582912
s_cbranch_scc1 Lr_ys5_%=
buffer_load_dwordx4 v11,s[36:39],s51 offen offset:0 sc1 lds
buffer_load_dwordx4 v11,s[36:39],s51 offen offset:1024 sc1 lds
buffer_load_dwordx4 v11,s[36:39],s51 offen offset:2048 sc1 lds
buffer_load_dwordx4 v11,s[36:39],s51 offen offset:3072 sc1 lds
s_lshl_b32 s63,s50,14
s_add_u32 s51,s51,s63
Lr_ys5_%=:
s_add_u32 s66,s66,16384
s_cmp_eq_u32 s66,151552
s_cselect_b32 s66,4096,s66
v_add_u32 v18,s66,v11
s_mov_b32 s60,s90
s_waitcnt vmcnt(7)
s_branch Lr_it0_%=
Lr_tc1_%=:
s_add_u32 s63,s87,71303168
buffer_load_dwordx4 a[0:3],v11,s[36:39],s63 offen offset:0 sc1
buffer_load_dwordx4 a[4:7],v11,s[36:39],s63 offen offset:1024 sc1
buffer_load_dwordx4 a[8:11],v11,s[36:39],s63 offen offset:2048 sc1
buffer_load_dwordx4 a[12:15],v11,s[36:39],s63 offen offset:3072 sc1
s_add_u32 s63,s87,89128960
buffer_load_dwordx4 a[16:19],v11,s[36:39],s63 offen offset:0 sc1
buffer_load_dwordx4 a[20:23],v11,s[36:39],s63 offen offset:1024 sc1
buffer_load_dwordx4 a[24:27],v11,s[36:39],s63 offen offset:2048 sc1
buffer_load_dwordx4 a[28:31],v11,s[36:39],s63 offen offset:3072 sc1
s_add_u32 s63,s87,99614720
buffer_load_dwordx4 a[32:35],v11,s[36:39],s63 offen offset:0 sc1
buffer_load_dwordx4 a[36:39],v11,s[36:39],s63 offen offset:1024 sc1
buffer_load_dwordx4 a[40:43],v11,s[36:39],s63 offen offset:2048 sc1
buffer_load_dwordx4 a[44:47],v11,s[36:39],s63 offen offset:3072 sc1
s_add_u32 s63,s87,109051904
buffer_load_dwordx4 a[48:51],v11,s[36:39],s63 offen offset:0 sc1
buffer_load_dwordx4 a[52:55],v11,s[36:39],s63 offen offset:1024 sc1
buffer_load_dwordx4 a[56:59],v11,s[36:39],s63 offen offset:2048 sc1
buffer_load_dwordx4 a[60:63],v11,s[36:39],s63 offen offset:3072 sc1
buffer_load_dword v8,v6,s[36:39],s69 offen offset:0 sc1
s_waitcnt vmcnt(17)
buffer_store_dword v7,v5,s[36:39],s60 offen offset:0 sc0 sc1
s_mov_b32 s60,536870912
s_waitcnt vmcnt(1)
s_branch Lr_it0_%=
Lr_done_%=:
s_waitcnt vmcnt(0) lgkmcnt(0)
s_barrier
Lcode_r_e:)"
  :: "s"(ws), "s"(sK), "s"(sStep), "s"(sRq), "s"(sEpR), "v"(vRP), "v"(vRF), "v"(vRwr), "v"(vRrd), "v"(vRS), "v"(vFlag), "v"(vL4), "v"(vRL) : "v0", "v1", "v2", "v3", "v4", "v5", "v6", "v7", "v8", "v9", "v10", "v11", "v12", "v13", "v14", "v15", "v16", "v17", "v18", "v19", "v20", "v21", "v22", "v23", "v24", "v25", "v26", "v27", "v28", "v29", "v30", "v31", "v32", "v33", "v34", "v35", "v36", "v37", "v38", "v39", "v40", "v41", "v42", "v43", "v44", "v45", "v46", "v47", "v48", "v49", "v50", "v51", "v52", "v53", "v54", "v55", "v56", "v57", "v58", "v59", "v60", "v61", "v62", "v63", "v64", "v65", "v66", "v67", "v68", "v69", "v70", "v71", "v72", "v73", "v74", "v75", "v76", "v77", "v78", "v79", "v80", "v81", "v82", "v83", "v84", "v85", "v86", "v87", "v88", "v89", "v90", "v91", "v92", "v93", "v94", "v95", "v96", "v97", "v98", "v99", "v100", "v101", "v102", "v103", "v104", "v105", "v106", "v107", "v108", "v109", "v110", "v111", "v112", "v113", "v114", "v115", "v116", "v117", "v118", "v119", "v120", "v121", "v122", "v123", "v124", "v125", "v126", "v127", "v128", "v129", "v130", "v131", "v132", "v133", "v134", "v135", "v136", "v137", "v138", "v139", "v140", "v141", "v142", "v143", "v144", "v145", "v146", "v147", "v148", "v149", "v150", "v151", "v152", "v153", "v154", "v155", "v156", "v157", "v158", "v159", "v160", "v161", "v162", "v163", "v164", "v165", "v166", "v167", "v168", "v169", "v170", "v171", "v172", "v173", "v174", "v175", "v176", "v177", "v178", "v179", "v180", "v181", "v182", "v183", "v184", "v185", "v186", "v187", "v188", "v189", "v190", "v191", "v192", "v193", "v194", "v195", "v196", "v197", "v198", "v199", "v200", "v201", "v202", "v203", "v204", "v205", "v206", "v207", "v208", "v209", "v210", "v211", "v212", "v213", "v214", "v215", "v216", "v217", "v218", "v219", "v220", "v221", "v222", "v223", "v224", "v225", "v226", "v227", "v228", "v229", "v230", "v231", "v255", "a0", "a1", "a2", "a3", "a4", "a5", "a6", "a7", "a8", "a9", "a10", "a11", "a12", "a13", "a14", "a15", "a16", "a17", "a18", "a19", "a20", "a21", "a22", "a23", "a24", "a25", "a26", "a27", "a28", "a29", "a30", "a31", "a32", "a33", "a34", "a35", "a36", "a37", "a38", "a39", "a40", "a41", "a42", "a43", "a44", "a45", "a46", "a47", "a48", "a49", "a50", "a51", "a52", "a53", "a54", "a55", "a56", "a57", "a58", "a59", "a60", "a61", "a62", "a63", "a64", "a65", "a66", "a67", "a68", "a69", "a70", "a71", "a72", "a73", "a74", "a75", "a76", "a77", "a78", "a79", "a80", "a81", "a82", "a83", "a84", "a85", "a86", "a87", "a88", "a89", "a90", "a91", "a92", "a93", "a94", "a95", "a96", "a97", "a98", "a99", "a100", "a101", "a102", "a103", "a104", "a105", "a106", "a107", "a108", "a109", "a110", "a111", "a112", "a113", "a114", "a115", "a116", "a117", "a118", "a119", "a120", "a121", "a122", "a123", "a124", "a125", "a126", "a127", "a128", "a129", "a130", "a131", "a132", "a133", "a134", "a135", "a136", "a137", "a138", "a139", "a140", "a141", "a142", "a143", "a144", "a145", "a146", "a147", "a148", "a149", "a150", "a151", "a152", "a153", "a154", "a155", "a156", "a157", "a158", "a159", "a160", "a161", "a162", "a163", "a164", "a165", "a166", "a167", "a168", "a169", "a170", "a171", "a172", "a173", "a174", "a175", "a176", "a177", "a178", "a179", "a180", "a181", "a182", "a183", "a184", "a185", "a186", "a187", "a188", "a189", "a190", "a191", "a192", "a193", "a194", "a195", "a196", "a197", "a198", "a199", "a200", "a201", "a202", "a203", "a204", "a205", "a206", "a207", "a208", "a209", "a210", "a211", "a212", "a213", "a214", "a215", "a216", "a217", "a218", "a219", "a220", "a221", "a222", "a223", "a224", "a225", "a226", "a227", "a228", "a229", "a230", "a231", "a232", "a233", "a234", "a235", "a236", "a237", "a238", "a239", "a240", "a241", "a242", "a243", "a244", "a245", "a246", "a247", "a248", "a249", "a250", "a251", "a252", "a253", "a254", "a255", "s36", "s37", "s38", "s39", "s40", "s41", "s42", "s43", "s44", "s45", "s46", "s47", "s48", "s49", "s50", "s51", "s52", "s53", "s54", "s55", "s56", "s57", "s58", "s59", "s60", "s61", "s62", "s63", "s64", "s65", "s66", "s67", "s68", "s69", "s70", "s71", "s72", "s73", "s74", "s75", "s76", "s77", "s78", "s79", "s80", "s81", "s82", "s83", "s84", "s85", "s86", "s87", "s88", "s89", "s90", "s91", "s92", "s93", "s94", "s95", "s96", "s97", "s98", "s99", "vcc", "scc", "memory");
    }
    LANE_ID(lane2);
    const int wk = kind == 6 ? slice : NRW + (kind - 1) * 32 + rep * 8 + slice;
    for (int t = wk; t < 256 - FINE; t += 82) row_last(ws, out, t, lane2, wave, ep);
    if (wk * 4 + wave < FINE * 16) chunk_last(ws, out, (256 - FINE) * 16 + wk * 4 + wave, lane2, ep);
    if (b == 0 && lane2 == 0 && wave == 0) __hip_atomic_store((gu32*)(ws + OFF_EP), ep, __ATOMIC_RELAXED, __HIP_MEMORY_SCOPE_AGENT);
    asm volatile("s_endpgm" ::: "memory");
    __builtin_unreachable();
  }
}

extern "C" void kernel_launch(void* const* d_in, const int* in_sizes, int n_in, void* d_out, int out_size,
                              void* d_ws, size_t ws_size, hipStream_t stream) {
  const u16* q = (const u16*)d_in[0];
  const u16* k = (const u16*)d_in[1];
  const u16* v = (const u16*)d_in[2];
  const u16* shift_k = (const u16*)d_in[3];
  const u16* ssm_k = (const u16*)d_in[4];
  const u16* ssm_D = (const u16*)d_in[5];
  const u16* w1 = (const u16*)d_in[7];
  const u16* w2 = (const u16*)d_in[8];
  const u16* w3 = (const u16*)d_in[9];
  const u16* w4 = (const u16*)d_in[10];
  flow_kernel<<<256, 256, 0, stream>>>(q, k, v, shift_k, ssm_k, ssm_D, w1, w2, w3, w4, (char*)d_ws, (u16*)d_out);
}
```

```cpp
#include <hip/hip_runtime.h>
#include <hip/hip_bf16.h>
#include <stdint.h>
#include <type_traits>

typedef short s16x8 __attribute__((ext_vector_type(8)));
typedef short s16x4 __attribute__((ext_vector_type(4)));
typedef float f32x4 __attribute__((ext_vector_type(4)));
typedef unsigned short u16;
typedef unsigned short u16x8 __attribute__((ext_vector_type(8)));
typedef unsigned short u16x4 __attribute__((ext_vector_type(4)));
typedef float f32x2 __attribute__((ext_vector_type(2)));
typedef __bf16 bf16x2 __attribute__((ext_vector_type(2)));
typedef unsigned u32x4v __attribute__((ext_vector_type(4)));
typedef __attribute__((address_space(1))) unsigned gu32;

constexpr int S_ = 2048, D_ = 1024, F_ = 3584;

constexpr int PL1 = 264, KE = 1, RT1 = 0, LB = 66, FL_BYTES = 2141312, WS_USED = 122727552;
constexpr int OFF_KVR = 4194304, OFF_YF = 8388608, OFF_XP = 12582912, OFF_HS = 22085632, OFF_X1 = 31457280, OFF_G = 40894464, OFF_P0 = 71303168, OFF_DBG = 80740352, OFF_P1 = 89128960, OFF_QS = 98566144, OFF_P2 = 99614720, OFF_P3 = 109051904, OFF_S1 = 118489088, OFF_ST = 119013376, OFF_FL = 120586240, OFF_FXP = 120587264, OFF_FH = 120859648, OFF_FX1 = 121132032, OFF_FP = 121405440, OFF_FG = 121950720, OFF_FKV = 122629120, OFF_FY = 122661888, OFF_EP = 122727424;


__device__ __forceinline__ float bf2f(u16 v) { return __uint_as_float(((unsigned)v) << 16); }
__device__ __forceinline__ u16 f2bf(float f) { return __builtin_bit_cast(u16, (__bf16)f); }
__device__ __forceinline__ float rbf(float f) { return bf2f(f2bf(f)); }
__device__ __forceinline__ void st16(void* p, u16x8 v) {
  asm volatile("global_store_dwordx4 %0, %1, off sc0 sc1\n\ts_nop 1" :: "v"(p), "v"(v) : "memory");
}
__device__ __forceinline__ void bst16(u32x4v v, __amdgpu_buffer_rsrc_t rs, unsigned vo, unsigned so) {
  asm volatile("buffer_store_dwordx4 %0, %1, %2, %3 offen sc0 sc1\n\ts_nop 1" :: "v"(v), "v"(vo), "s"(rs), "s"(so) : "memory");
}
#define WAIT_VM(n) asm volatile("s_waitcnt vmcnt(%0)" ::"n"(n) : "memory")
#define WAIT_LGKM0() asm volatile("s_waitcnt lgkmcnt(0)" ::: "memory")

__device__ __forceinline__ void touch_lds(const char* p, unsigned a) {
  asm volatile("s_mov_b32 m0, %1\n\ts_nop 0\n\tglobal_load_lds_dword %0, off" :: "v"(p), "s"(a) : "memory");
}
#define YSLEEP 4
#define KSLEEP 2
__device__ __forceinline__ f32x2 up2(unsigned p) { return (f32x2){__uint_as_float(p << 16), __uint_as_float(p & 0xffff0000u)}; }
__device__ __forceinline__ unsigned pk2(f32x2 v) { return __builtin_bit_cast(unsigned, __builtin_convertvector(v, bf16x2)); }
__device__ __forceinline__ f32x2 rbf2(f32x2 v) { return up2(pk2(v)); }
#define BARRIER() asm volatile("s_waitcnt lgkmcnt(0)\n\ts_barrier" ::: "memory")
template <int N, class F> __device__ __forceinline__ void sfor(F&& f) {
  if constexpr (N > 0) { sfor<N - 1>(f); f(std::integral_constant<int, N - 1>{}); }
}
template <int NL>
__device__ __forceinline__ void poll_flags(const char* p, int n, int lane, unsigned ep) {
#pragma unroll 1
  for (int it = 0; it < 20000; ++it) {
    bool ok = true;
#pragma unroll
    for (int i = 0; i < NL; ++i) {
      const int f = lane + 64 * i;
      ok &= __hip_atomic_load((gu32*)(p + (f < n ? f : 0) * 128), __ATOMIC_RELAXED, __HIP_MEMORY_SCOPE_AGENT) == ep;
    }
    if (__all(ok)) break;
    __builtin_amdgcn_s_sleep(YSLEEP);
  }
  asm volatile("" ::: "memory");
}

constexpr int CV_HP = (2048 + 32) * 2;
constexpr int CV_UR = (2048 + 256) * 2;
constexpr int CV_CH = 2 * CV_HP + CV_UR;
#define T0 0
#define T0R 4
#define T1 0
#define LATEQ 4
constexpr int CV_STG = 8 * CV_CH;

struct KvRegs { u32x4v k0, k1, k2, k3, k4, k5, k6, v0, v1, v2, v3; };
struct SkRegs { u32x4v s0, s1, s2, s3; };
#define LD16(d, p, pol) asm volatile("global_load_dwordx4 %0, %1, off " pol : "=v"(d) : "v"(p) : "memory")
#define LDS16(d, vo, sp, o, pol) asm volatile("global_load_dwordx4 %0, %1, %2 offset:" #o " " pol : "=v"(d) : "v"(vo), "s"(sp) : "memory")
__device__ __forceinline__ void kv_load(KvRegs& R, const u16* __restrict__ kin, const u16* __restrict__ vin, int b, int tid) {
  const unsigned tr = (b >> 2) * 32 + 4 * (tid >> 5), oa = tr * 2048 + (b & 3) * 512 + 16 * (tid & 31) + 4096;
  LDS16(R.k3, oa, kin, -4096, "nt"); LDS16(R.k4, oa, kin, -2048, "nt"); LDS16(R.k5, oa, kin, 0, "nt"); LDS16(R.k6, oa, kin, 2048, "nt");
  LDS16(R.v0, oa, vin, -4096, "nt"); LDS16(R.v1, oa, vin, -2048, "nt"); LDS16(R.v2, oa, vin, 0, "nt"); LDS16(R.v3, oa, vin, 2048, "nt");
  const unsigned ob = tr ? oa - 6144 : oa;
  LDS16(R.k0, ob, kin, -4096, "nt"); LDS16(R.k1, ob, kin, -2048, "nt"); LDS16(R.k2, ob, kin, 0, "nt");
}
__device__ __forceinline__ void sk_load(SkRegs& R, const u16* __restrict__ shift_k, int b, int tid) {
  const unsigned o = (b & 3) * 2048 + 64 * (tid & 31);
  LDS16(R.s0, o, shift_k, 0, ""); LDS16(R.s1, o, shift_k, 16, ""); LDS16(R.s2, o, shift_k, 32, ""); LDS16(R.s3, o, shift_k, 48, "");
}
template <int N>
__device__ __forceinline__ void kv_wait(KvRegs& R, SkRegs& Q) {
  asm volatile("s_waitcnt vmcnt(%15)" : "+v"(R.k0), "+v"(R.k1), "+v"(R.k2), "+v"(R.k3), "+v"(R.k4), "+v"(R.k5), "+v"(R.k6), "+v"(R.v0), "+v"(R.v1), "+v"(R.v2),
               "+v"(R.v3), "+v"(Q.s0), "+v"(Q.s1), "+v"(Q.s2), "+v"(Q.s3) : "n"(N) : "memory");
}
__device__ __forceinline__ void kv_store(KvRegs& R, const SkRegs& Q, char* __restrict__ ws, char* T, int b, int tid) {
  const int t0 = (b >> 2) * 32, c0 = (b & 3) * 256, ch8 = tid & 31, rg = tid >> 5;
  if (t0 + 4 * rg < 3) R.k0 = R.k1 = R.k2 = (u32x4v){0u, 0u, 0u, 0u};
  typedef unsigned u32x2 __attribute__((ext_vector_type(2)));
  const u32x4v kr[7] = {R.k0, R.k1, R.k2, R.k3, R.k4, R.k5, R.k6}, vr[4] = {R.v0, R.v1, R.v2, R.v3}, sk[4] = {Q.s0, Q.s1, Q.s2, Q.s3};
#pragma unroll
  for (int j2 = 0; j2 < 4; ++j2) {
    const f32x2 sa = up2(sk[j2][0]), sb = up2(sk[j2][1]), sc = up2(sk[j2][2]), sd = up2(sk[j2][3]);
    const f32x2 s0 = {sa[0], sc[0]}, s1 = {sa[1], sc[1]}, s2 = {sb[0], sd[0]}, s3 = {sb[1], sd[1]};
    f32x2 kf[7], o[4];
#pragma unroll
    for (int r = 0; r < 7; ++r) kf[r] = up2(kr[r][j2]);
#pragma unroll
    for (int r = 0; r < 4; ++r)
      o[r] = __builtin_elementwise_fma(kf[r + 3], s0, __builtin_elementwise_fma(kf[r + 2], s1, __builtin_elementwise_fma(kf[r + 1], s2, kf[r] * s3))) * up2(vr[r][j2]);
#pragma unroll
    for (int e = 0; e < 2; ++e)
      *(u32x2*)(T + ch8 * 520 + (2 * j2 + e) * 64 + (28 - 4 * rg) * 2) = (u32x2){pk2((f32x2){o[3][e], o[2][e]}), pk2((f32x2){o[1][e], o[0][e]})};
  }
  BARRIER();
  u16* kvr = (u16*)(ws + OFF_KVR);
#pragma unroll
  for (int i = 0; i < 4; ++i) {
    const int ch = (tid >> 2) + 64 * i, cb = tid & 3;
    const char* p = T + ch * 64 + (ch >> 3) * 8 + cb * 16;
    const u16x4 lo = *(const u16x4*)p, hi = *(const u16x4*)(p + 8);
    st16(kvr + (size_t)(c0 + ch) * S_ + (2016 - t0) + cb * 8, __builtin_shufflevector(lo, hi, 0, 1, 2, 3, 4, 5, 6, 7));
  }
}

typedef unsigned u32x2v __attribute__((ext_vector_type(2)));
__device__ __forceinline__ void h_load(u32x4v (&hv)[8], unsigned& hx, unsigned& dD, const u16* __restrict__ ssm_k, const u16* __restrict__ ssm_D, int c0, int tid) {
  const __amdgpu_buffer_rsrc_t rs = __builtin_amdgcn_make_buffer_rsrc((void*)ssm_k, 0, c0 < 0 ? 0 : D_ * S_ * 2, 0x00020000);
  const unsigned cb = c0 < 0 ? 0 : c0, lane = tid & 63, wave = tid >> 6;
#pragma unroll
  for (int i = 0; i < 8; ++i) asm volatile("buffer_load_dwordx4 %0, %1, %2, %3 offen nt" : "=v"(hv[i]) : "v"(tid * 16), "s"(rs), "s"((cb + i) * 4096) : "memory");
  asm volatile("buffer_load_dword %0, %1, %2, %3 offen" : "=v"(hx) : "v"((lane & 7) * 4096 + ((wave + 1) & 3) * 1024), "s"(rs), "s"(cb * 4096) : "memory");
  asm volatile("global_load_dword %0, %1, %2" : "=v"(dD) : "v"(cb * 2 + wave * 4), "s"(ssm_D) : "memory");
}
template <int N>
__device__ __forceinline__ void h_wait(u32x4v (&hv)[8], unsigned& hx, unsigned& dD) {
  asm volatile("s_waitcnt vmcnt(%10)" : "+v"(hv[0]), "+v"(hv[1]), "+v"(hv[2]), "+v"(hv[3]), "+v"(hv[4]), "+v"(hv[5]), "+v"(hv[6]), "+v"(hv[7]), "+v"(hx), "+v"(dD) : "n"(N) : "memory");
}
__device__ __forceinline__ void h_spread(const u32x4v (&hv)[8], unsigned hx, char* lds, int tid) {
  if (tid < 32 && (tid & 3) != 3) *(u32x4v*)(lds + (tid >> 2) * CV_CH + ((tid >> 1) & 1) * CV_HP + (tid & 1) * 16) = (u32x4v){0u, 0u, 0u, 0u};
  *(u32x4v*)(lds + (tid >> 5) * CV_CH + 2 * CV_HP + 4096 + (tid & 31) * 16) = (u32x4v){0u, 0u, 0u, 0u};
#pragma unroll
  for (int i = 0; i < 8; ++i) {
    char* base = lds + i * CV_CH + 32 + tid * 16;
    unsigned hn = __builtin_amdgcn_update_dpp(0, (int)hv[i][0], 0x130, 0xf, 0xf, true);
    const unsigned last = __builtin_amdgcn_readlane(hx, i);
    if ((tid & 63) == 63) hn = last;
#define AB(a, b) __builtin_amdgcn_alignbyte(a, b, 2)
    if (tid == 0) *(u32x4v*)(lds + i * CV_CH + CV_HP + 16) = (u32x4v){0u, 0u, 0u, hv[i][0] << 16};
    *(u32x4v*)(base) = hv[i];
    *(u32x4v*)(base + CV_HP) = (u32x4v){AB(hv[i][1], hv[i][0]), AB(hv[i][2], hv[i][1]), AB(hv[i][3], hv[i][2]), AB(hn, hv[i][3])};
#undef AB
  }
}

__device__ __forceinline__ void pads(char* __restrict__ ws, int w, int lane, unsigned ep) {
  if (w < 132) {
    const int k = w < 128 ? w >> 5 : w - 128, u = k == 0 ? -1 : k == 1 ? 128 : k == 2 ? 131 : 260;
    st16(ws + (w < 128 ? OFF_HS + u * 32768 + (w & 31) * 1024 : OFF_FH + u * 1024) + lane * 16, __builtin_bit_cast(u16x8, (u32x4v){w >= 128 && !(lane & 7) ? ep : 0u, 0u, 0u, 0u}));
  }
}
__device__ __forceinline__ void prewrite(char* __restrict__ ws, unsigned off, unsigned str, int rep, int tid) {
#pragma unroll
  for (int i = 0; i < 2; ++i) st16(ws + off + (rep + 4 * (tid >> 3) + 132 * i) * str + (tid & 7) * 16, __builtin_bit_cast(u16x8, (u32x4v){0u, 0u, 0u, 0u}));
}

__device__ __forceinline__ void conv_part(char* __restrict__ ws, unsigned dD, char* lds, int g, int tid, int lane, int wave, unsigned ep, const char* wp, unsigned wstep) {
  const __amdgpu_buffer_rsrc_t rs = __builtin_amdgcn_make_buffer_rsrc(ws, 0, WS_USED, 0x00020000);
  const u16* kvr = (const u16*)(ws + OFF_KVR) + (size_t)(8 * g + 2 * wave) * S_;
  auto wait1 = [&](unsigned off) {
#pragma unroll 1
    for (int it = 0; it < 20000; ++it) {
      if (__all(__hip_atomic_load((gu32*)(ws + off), __ATOMIC_RELAXED, __HIP_MEMORY_SCOPE_AGENT) == ep)) break;
      __builtin_amdgcn_s_sleep(KSLEEP);
    }
    asm volatile("" ::: "memory");
  };
  const unsigned lb = (unsigned)(size_t)lds;
  auto u_fetch = [&](int part) {
#pragma unroll
    for (int j = 0; j < 2; ++j)
      asm volatile("s_mov_b32 m0, %1\n\ts_nop 0\n\tglobal_load_lds_dwordx4 %0, off sc1" :: "v"(kvr + j * S_ + part * 512 + lane * 8),
                   "s"(lb + (2 * wave + j) * CV_CH + 2 * CV_HP + part * 1024) : "memory");
  };
  const unsigned xo = (tid >> 4) * 32768 + g * 256 + (tid & 15) * 16;
  u32x4v qv[6];
  u32x2v qs[6];
  auto q_fetch = [&](int s0, int ns) {
#pragma unroll
    for (int i = 0; i < 6; ++i)
      if (i < ns) {
        asm volatile("buffer_load_dwordx4 %0, %1, %2, %3 offen sc1" : "=v"(qv[i]) : "v"(xo), "s"(rs), "s"(OFF_XP + (s0 + i) * 524288) : "memory");
        asm volatile("buffer_load_dwordx2 %0, %1, %2, %3 offen sc1" : "=v"(qs[i]) : "v"(tid * 8), "s"(rs), "s"(OFF_QS + (s0 + i) * 2048) : "memory");
      }
  };
  const int cg = g >> 5;
  const int l15 = lane & 15, lg = lane >> 4, cp = (l15 + 1) & 1;
  const unsigned ao = cp * CV_HP + 2 * (8 * lg + (l15 + 1 - cp)), bo = 2 * CV_HP + 2 * (2032 - 16 * l15 + 8 * lg) - 2 * 256 * 7;
  const unsigned own = lb + 2 * wave * CV_CH;
  char* stg = lds + CV_STG;
  s16x8 ad[2];
#pragma unroll
  for (int j = 0; j < 2; ++j) {
    const int e = 15 - l15 - 8 * lg;
    const unsigned d = (j ? dD >> 16 : dD & 0xffffu) << ((e & 1) * 16);
    ad[j] = __builtin_bit_cast(s16x8, (u32x4v){e >> 1 == 0 ? d : 0u, e >> 1 == 1 ? d : 0u, e >> 1 == 2 ? d : 0u, e >> 1 == 3 ? d : 0u});
  }
  f32x4 acc[2][6];
  unsigned f0, f1;
  bool mid;
  auto finish = [&](auto s0_c, auto ns_c) {
    constexpr int S0 = decltype(s0_c)::value, NS = decltype(ns_c)::value, A0 = S0 == 2 ? 0 : 4, Q0 = S0 == 6 ? 4 : 0;
#pragma unroll
    for (int s = 0; s < NS; ++s)
#pragma unroll
      for (int r = 0; r < 4; ++r) *(unsigned*)(stg + (Q0 + s) * 4352 + l15 * 272 + (4 * lg + r) * 16 + wave * 4) = pk2((f32x2){acc[0][A0 + s][r], acc[1][A0 + s][r]});
    if (S0 == 0) {
      asm volatile("s_waitcnt vmcnt(0)" : "+v"(qv[0]), "+v"(qv[1]), "+v"(qs[0]), "+v"(qs[1]), "+v"(f0) :: "memory");
      mid = __all(f0 == ep);
      if (mid) { u_fetch(2); u_fetch(1); }
    } else if (S0 == 2) asm volatile("" : "+v"(qv[0]), "+v"(qv[1]), "+v"(qv[2]), "+v"(qv[3]), "+v"(qv[4]), "+v"(qv[5]), "+v"(qs[0]), "+v"(qs[1]), "+v"(qs[2]), "+v"(qs[3]), "+v"(qs[4]), "+v"(qs[5]) :: "memory");
    BARRIER();
    u32x4v y4[NS];
#pragma unroll
    for (int s = 0; s < NS; ++s) {
      y4[s] = *(const u32x4v*)(stg + (Q0 + s) * 4352 + (tid >> 4) * 272 + (tid & 15) * 16);
      const f32x2 rd2 = {__uint_as_float(qs[Q0 + s][0]), __uint_as_float(qs[Q0 + s][0])}, nm2 = {__uint_as_float(qs[Q0 + s][1]), __uint_as_float(qs[Q0 + s][1])};
      u32x4v o;
#pragma unroll
      for (int e = 0; e < 4; ++e) {
        const f32x2 x = up2(qv[Q0 + s][e]);
        o[e] = pk2(__builtin_elementwise_fma(__builtin_elementwise_fma(x, rd2, nm2), up2(y4[s][e]), x));
      }
      bst16(o, rs, xo, OFF_XP + (S0 + s) * 524288);
    }
#pragma unroll
    for (int s = 0; s < NS; ++s) bst16(y4[s], rs, (tid >> 3) * 16384 + g * 128 + (tid & 7) * 16, OFF_YF + (S0 + s) * 524288);
  };
  auto touches = [&](int n) {
#pragma unroll
    for (int i = 0; i < 3; ++i)
      if (i < n) { touch_lds(wp, lb + 163584); wp += wstep; }
  };
  auto count = [&](int s0, int ns, auto n_c) {
    WAIT_VM(decltype(n_c)::value);
    BARRIER();
    if (tid < 4 * ns) __hip_atomic_fetch_add((gu32*)(ws + OFF_FXP + (s0 + (tid >> 2)) * 16384 + (tid & 3) * 1024 + (g >> 4) * 128), 1u, __ATOMIC_RELAXED, __HIP_MEMORY_SCOPE_AGENT);
  };
  auto pair = [&](unsigned bb) {
    u32x2v ax[2][2][2][2];
    s16x8 bx[2][2][2];
    auto rdA = [&](int q) {
#pragma unroll
      for (int j = 0; j < 2; ++j)
#pragma unroll
        for (int m = 0; m < 2; ++m) {
          asm volatile("ds_read2_b32 %0, %1 offset0:%2 offset1:%3" : "=v"(ax[q & 1][j][m][0]) : "v"(own + j * CV_CH + ao), "n"(128 * m + 16 * q), "n"(128 * m + 16 * q + 1));
          asm volatile("ds_read2_b32 %0, %1 offset0:%2 offset1:%3" : "=v"(ax[q & 1][j][m][1]) : "v"(own + j * CV_CH + ao), "n"(128 * m + 16 * q + 2), "n"(128 * m + 16 * q + 3));
          asm volatile("ds_read_b128 %0, %1 offset:%2" : "=v"(bx[q & 1][j][m]) : "v"(bb), "n"(j * CV_CH + 512 * (1 - m) + 64 * q));
        }
    };
    rdA(0);
    sfor<8>([&](auto q_c) {
      constexpr int q = decltype(q_c)::value, p = q & 1;
      if (q + 1 < 8) rdA(q + 1);
      asm volatile("s_waitcnt lgkmcnt(%0)" :: "n"(q + 1 < 8 ? 12 : 0) : "memory");
      __builtin_amdgcn_sched_barrier(0);
#pragma unroll
      for (int j = 0; j < 2; ++j) {
        const s16x8 a0f = __builtin_bit_cast(s16x8, __builtin_shufflevector(ax[p][j][0][0], ax[p][j][0][1], 0, 1, 2, 3)), a1f = __builtin_bit_cast(s16x8, __builtin_shufflevector(ax[p][j][1][0], ax[p][j][1][1], 0, 1, 2, 3));
        if (q == 0) {
          acc[j][4] = __builtin_amdgcn_mfma_f32_16x16x32_bf16(ad[j], bx[p][j][0], acc[j][4], 0, 0, 0);
          acc[j][5] = __builtin_amdgcn_mfma_f32_16x16x32_bf16(ad[j], bx[p][j][1], acc[j][5], 0, 0, 0);
        }
        acc[j][4] = __builtin_amdgcn_mfma_f32_16x16x32_bf16(a0f, bx[p][j][0], acc[j][4], 0, 0, 0);
        acc[j][5] = __builtin_amdgcn_mfma_f32_16x16x32_bf16(a0f, bx[p][j][1], acc[j][5], 0, 0, 0);
        acc[j][5] = __builtin_amdgcn_mfma_f32_16x16x32_bf16(a1f, bx[p][j][0], acc[j][5], 0, 0, 0);
      }
      __builtin_amdgcn_sched_barrier(0);
    });
  };
  wait1(OFF_FKV + ((lane & 15) * 4 + cg) * 128);
  u_fetch(3);
  wait1(OFF_FY + (lane & 31) * 128);
  q_fetch(0, 2);
  const unsigned fm = OFF_FKV + ((16 + (lane & 31)) * 4 + cg) * 128;
  asm volatile("global_load_dword %0, %1, %2 sc1" : "=v"(f0) : "v"(fm), "s"(ws) : "memory");
#pragma unroll
  for (int i = 0; i < 4; ++i) acc[i >> 1][4 + (i & 1)] = (f32x4){0.f, 0.f, 0.f, 0.f};
  pair(own + bo + 512 * 6);
  finish(std::integral_constant<int, 0>{}, std::integral_constant<int, 2>{});
  u32x2v a0[8][2], a1[8][2];
  s16x8 b0[6], b1[6];
  unsigned pa[4], pb = own + bo;
#pragma unroll
  for (int i = 0; i < 4; ++i) pa[i] = own + ao + 1024 * i;
  auto rd1 = [&](int rr, int i, u32x2v (&ah)[8][2], s16x8 (&bfr)[6]) {
    const int m = i < 18 ? i / 3 : 6 + (i - 18) / 2, e = i < 18 ? i % 3 : i & 1;
    if (e < 2) asm volatile("ds_read2_b32 %0, %1 offset0:%2 offset1:%3" : "=v"(ah[m][e & 1]) : "v"(pa[m >> 1]), "n"(128 * (m & 1) + 2 * e + 16 * rr), "n"(128 * (m & 1) + 2 * e + 16 * rr + 1));
    else asm volatile("ds_read_b128 %0, %1 offset:%2" : "=v"(bfr[m < 6 ? m : 0]) : "v"(pb), "n"(512 * (7 - m) + 64 * rr));
  };
  auto round = [&](f32x4 (&ac)[6], u32x2v (&ah)[8][2], s16x8 (&bfr)[6], u32x2v (&ahn)[8][2], s16x8 (&bfn)[6], int rr, unsigned step) {
    int k = 0;
#pragma unroll
    for (int s = 2; s <= 7; ++s)
#pragma unroll
      for (int m = 0; m <= s; ++m) {
        if (s - m > 5) continue;
        const s16x8 af = __builtin_bit_cast(s16x8, __builtin_shufflevector(ah[m][0], ah[m][1], 0, 1, 2, 3));
        ac[s - 2] = __builtin_amdgcn_mfma_f32_16x16x32_bf16(af, bfr[s - m > 5 ? 0 : s - m], ac[s - 2], 0, 0, 0);
        __builtin_amdgcn_sched_barrier(0);
        if (m == s) asm volatile("s_waitcnt lgkmcnt(12)" ::: "memory");
#pragma unroll
        for (int i = 0; i < 22; ++i)
          if (i * 4 / 3 == k) {
            rd1(rr, i, ahn, bfn);
            if (rr && (i == 4 || i == 10 || i == 16 || i == 21)) asm volatile("v_add_u32 %0, %1, %0" : "+v"(pa[i / 6]) : "s"(step));
            if (rr && i == 17) asm volatile("v_add_u32 %0, %1, %0" : "+v"(pb) : "s"(step));
          }
        ++k;
        __builtin_amdgcn_sched_barrier(0);
      }
  };
  if (!mid) { wait1(fm); u_fetch(2); u_fetch(1); WAIT_VM(0); }
  WAIT_VM(4);
#pragma unroll
  for (int i = 0; i < 22; ++i) rd1(0, i, a0, b0);
  WAIT_LGKM0();
  auto channel = [&](auto j_c, auto r0_c, auto r1_c) {
    constexpr int j = decltype(j_c)::value, r0 = decltype(r0_c)::value, r1 = decltype(r1_c)::value;
    __builtin_amdgcn_sched_barrier(0);
    if (r0 == 0) {
#pragma unroll
      for (int s = 2; s <= 7; ++s) acc[j][s - 2] = s < 6 ? __builtin_amdgcn_mfma_f32_16x16x32_bf16(ad[j], b0[s], (f32x4){0.f, 0.f, 0.f, 0.f}, 0, 0, 0) : (f32x4){0.f, 0.f, 0.f, 0.f};
    }
#pragma unroll 1
    for (int r = r0; r < r1; r += 2) {
      if (j == 0 && r == 2) {
        asm volatile("s_waitcnt vmcnt(0)\n\ts_barrier" ::: "memory");
        if (tid < 8) __hip_atomic_fetch_add((gu32*)(ws + OFF_FXP + (tid >> 2) * 16384 + (tid & 3) * 1024 + (g >> 4) * 128), 1u, __ATOMIC_RELAXED, __HIP_MEMORY_SCOPE_AGENT);
      }
      if (T0 && j == 0 && r == T0R) touches(T0);
      round(acc[j], a0, b0, a1, b1, 1, r < 6 ? 128 : j ? 0 : CV_CH - 384);
      round(acc[j], a1, b1, a0, b0, 0, 0);
    }
    WAIT_LGKM0();
    __builtin_amdgcn_sched_barrier(0);
  };
  typedef std::integral_constant<int, 0> I0;
  typedef std::integral_constant<int, 1> I1;
  channel(I0{}, I0{}, std::integral_constant<int, 8>{});
  channel(I1{}, I0{}, std::integral_constant<int, LATEQ - 2>{});
  const unsigned fa = OFF_FY + (32 + lane) * 128, fb = lane < 32 ? OFF_FY + (96 + lane) * 128 : OFF_FKV + ((48 + (lane & 15)) * 4 + cg) * 128;
  asm volatile("global_load_dword %0, %1, %2 sc1" : "=v"(f0) : "v"(fa), "s"(ws) : "memory");
  asm volatile("global_load_dword %0, %1, %2 sc1" : "=v"(f1) : "v"(fb), "s"(ws) : "memory");
  channel(I1{}, std::integral_constant<int, LATEQ - 2>{}, std::integral_constant<int, LATEQ>{});
  asm volatile("s_waitcnt vmcnt(0)" : "+v"(f0), "+v"(f1) :: "memory");
  if (!__all(f0 == ep && f1 == ep)) { wait1(fa); wait1(fb); }
  u_fetch(0);
  q_fetch(2, 6);
  touches(T1);
  channel(I1{}, std::integral_constant<int, LATEQ>{}, std::integral_constant<int, 8>{});
  WAIT_VM(T1);
  finish(std::integral_constant<int, 2>{}, std::integral_constant<int, 4>{});
  pair(own + bo);
  count(2, 4, I0{});
  finish(std::integral_constant<int, 6>{}, std::integral_constant<int, 2>{});
  touches(3 - T0 - T1);
  count(6, 2, std::integral_constant<int, 3 - T0 - T1>{});
}
__device__ __forceinline__ float sum2_seg(float s, float q) {
  s += __builtin_bit_cast(float, __builtin_amdgcn_update_dpp(0, __builtin_bit_cast(int, s), 0x128, 0xf, 0xf, true));
  q += __builtin_bit_cast(float, __builtin_amdgcn_update_dpp(0, __builtin_bit_cast(int, q), 0x128, 0xf, 0xf, true));
  asm volatile("s_nop 1\n\tv_permlane16_swap_b32 %0, %1" : "+v"(s), "+v"(q));
  s += q;
  q = s;
  asm volatile("s_nop 1\n\tv_permlane32_swap_b32 %0, %1" : "+v"(s), "+v"(q));
  return s + q;
}
#define LASTSLEEP 8
__device__ __forceinline__ void wait_flags(const char* p, int lane, int n, unsigned ep) {
  gu32* f = (gu32*)(p + (lane < n ? lane : 0) * 128);
#pragma unroll 1
  for (int it = 0; it < 10000; ++it) {
    if (__all(__hip_atomic_load(f, __ATOMIC_RELAXED, __HIP_MEMORY_SCOPE_AGENT) == ep)) break;
    __builtin_amdgcn_s_sleep(LASTSLEEP);
  }
  asm volatile("" ::: "memory");
}
struct QRegs { u32x4v a[4], b[4]; };
__device__ __forceinline__ void q_load(QRegs& R, const u16* __restrict__ q, int f, int lane, int wave) {
  const unsigned o = (f * 16 + (lane & 7)) * 2048 + wave * 512 + (lane >> 3) * 16, o2 = o + 16384;
  LDS16(R.a[0], o, q, 0, "nt"); LDS16(R.b[0], o2, q, 0, "nt"); LDS16(R.a[1], o, q, 128, "nt"); LDS16(R.b[1], o2, q, 128, "nt");
  LDS16(R.a[2], o, q, 256, "nt"); LDS16(R.b[2], o2, q, 256, "nt"); LDS16(R.a[3], o, q, 384, "nt"); LDS16(R.b[3], o2, q, 384, "nt");
}
template <int N>
__device__ __forceinline__ void q_wait(QRegs& R, unsigned& e) {
  asm volatile("s_waitcnt vmcnt(%9)" : "+v"(R.a[0]), "+v"(R.a[1]), "+v"(R.a[2]), "+v"(R.a[3]), "+v"(R.b[0]), "+v"(R.b[1]), "+v"(R.b[2]), "+v"(R.b[3]), "+v"(e) : "n"(N) : "memory");
}
__device__ __forceinline__ void row_stats(const u32x4v (&xq)[4], char* lds, int k, int lane, int wave, float& mean, float& var) {
  f32x2* red = (f32x2*)(lds + 16640);
  const int r8 = lane & 7, seg = lane >> 3;
  f32x2 s = {0.f, 0.f}, sq = {0.f, 0.f};
#pragma unroll
  for (int i = 0; i < 16; ++i) { const f32x2 x = up2(xq[i >> 2][i & 3]); s += x; sq = __builtin_elementwise_fma(x, x, sq); }
  const float a = sum2_seg(s[0] + s[1], sq[0] + sq[1]);
  if ((seg & 5) == 0) ((float*)red)[((k * 4 + wave) * 8 + r8) * 2 + (seg >> 1)] = a;
  BARRIER();
  const f32x2 t = (red[(k * 4) * 8 + r8] + red[(k * 4 + 1) * 8 + r8]) + (red[(k * 4 + 2) * 8 + r8] + red[(k * 4 + 3) * 8 + r8]);
  mean = t[0] * (1.0f / 1024.0f);
  var = fmaxf(t[1] * (1.0f / 1024.0f) - mean * mean, 0.f);
}
__device__ __forceinline__ void q_store(char* __restrict__ ws, int t, int lane, int wave, const u32x4v (&xq)[4], float mean, float var) {
  const __amdgpu_buffer_rsrc_t rs = __builtin_amdgcn_make_buffer_rsrc(ws, 0, WS_USED, 0x00020000);
  const int r8 = lane & 7, seg = lane >> 3;
  const unsigned fo = (t >> 1) * 32768 + (wave * 8 + (seg >> 2)) * 1024 + ((seg & 3) * 16 + (t & 1) * 8 + r8) * 16;
#pragma unroll
  for (int i = 0; i < 4; ++i) bst16(xq[i], rs, fo + i * 2048, OFF_XP);
  const float rd = __builtin_amdgcn_rsqf(var + 1e-5f);
  if (wave == 0 && seg == 0) __builtin_amdgcn_raw_buffer_store_b64((u32x2v){__float_as_uint(rd), __float_as_uint(-mean * rd)}, rs, t * 64 + r8 * 8, OFF_QS, 17);
}
__device__ __forceinline__ void row_last(char* __restrict__ ws, u16* __restrict__ out, int t, int lane, int wave, unsigned ep) {
  const __amdgpu_buffer_rsrc_t rs = __builtin_amdgcn_make_buffer_rsrc(ws, 0, WS_USED, 0x00020000);
  const int r8 = lane & 7, seg = lane >> 3;
  const unsigned fo = (PL1 + t) * 16384 + wave * 4096 + lane * 16;
  wait_flags(ws + OFF_FP + (2 * LB + (t >> 1)) * 2048, lane, 16, ep);
  u32x4v p0[4], p1[4], p2[4], p3[4];
#pragma unroll
  for (int i = 0; i < 4; ++i) {
    p0[i] = __builtin_bit_cast(u32x4v, __builtin_amdgcn_raw_buffer_load_b128(rs, fo + i * 1024, OFF_P0, 16));
    p1[i] = __builtin_bit_cast(u32x4v, __builtin_amdgcn_raw_buffer_load_b128(rs, fo + i * 1024, OFF_P1, 16));
    p2[i] = __builtin_bit_cast(u32x4v, __builtin_amdgcn_raw_buffer_load_b128(rs, fo + i * 1024, OFF_P2, 16));
    p3[i] = __builtin_bit_cast(u32x4v, __builtin_amdgcn_raw_buffer_load_b128(rs, fo + i * 1024, OFF_P3, 16));
  }
#pragma unroll
  for (int i = 0; i < 4; ++i) {
    u32x4v o;
#pragma unroll
    for (int e = 0; e < 4; ++e) o[e] = pk2((up2(p0[i][e]) + up2(p1[i][e])) + (up2(p2[i][e]) + up2(p3[i][e])));
    st16(out + (size_t)(t * 8 + r8) * D_ + wave * 256 + i * 64 + seg * 8, __builtin_bit_cast(u16x8, o));
  }
}

#define FINE 20
__device__ __forceinline__ void chunk_last(char* __restrict__ ws, u16* __restrict__ out, int c, int lane, unsigned ep) {
  const __amdgpu_buffer_rsrc_t rs = __builtin_amdgcn_make_buffer_rsrc(ws, 0, WS_USED, 0x00020000);
  const unsigned fo = PL1 * 16384 + c * 1024 + lane * 16;
  gu32* f = (gu32*)(ws + OFF_FP + (2 * LB + (c >> 5)) * 2048 + ((lane & 3) * 4 + ((c >> 2) & 3)) * 128);
#pragma unroll 1
  for (int it = 0; it < 20000; ++it) {
    if (__all(__hip_atomic_load(f, __ATOMIC_RELAXED, __HIP_MEMORY_SCOPE_AGENT) == ep)) break;
    __builtin_amdgcn_s_sleep(4);
  }
  asm volatile("" ::: "memory");
  const u32x4v p0 = __builtin_bit_cast(u32x4v, __builtin_amdgcn_raw_buffer_load_b128(rs, fo, OFF_P0, 16)), p1 = __builtin_bit_cast(u32x4v, __builtin_amdgcn_raw_buffer_load_b128(rs, fo, OFF_P1, 16));
  const u32x4v p2 = __builtin_bit_cast(u32x4v, __builtin_amdgcn_raw_buffer_load_b128(rs, fo, OFF_P2, 16)), p3 = __builtin_bit_cast(u32x4v, __builtin_amdgcn_raw_buffer_load_b128(rs, fo, OFF_P3, 16));
  u32x4v o;
#pragma unroll
  for (int e = 0; e < 4; ++e) o[e] = pk2((up2(p0[e]) + up2(p1[e])) + (up2(p2[e]) + up2(p3[e])));
  st16(out + (size_t)((c >> 4) * 8 + (lane & 7)) * D_ + (c & 15) * 64 + (lane >> 3) * 8, __builtin_bit_cast(u16x8, o));
}

__device__ __forceinline__ int swzB(int r) { return ((((r >> 1) & 1) | (((r >> 3) & 1) << 1)) << 1); }
#define FLW 1
#define MAP3 1
#define MAP12 1
#define FLIP 0
#define KVL 0
#define RE 0
#define NRW 18
#define PFROWS 110
__global__ __launch_bounds__(256, 1) void flow_kernel(const u16* __restrict__ q, const u16* __restrict__ kin, const u16* __restrict__ vin,
                                                      const u16* __restrict__ shift_k, const u16* __restrict__ ssm_k, const u16* __restrict__ ssm_D,
                                                      const u16* __restrict__ w1, const u16* __restrict__ w2,
                                                      const u16* __restrict__ w3, const u16* __restrict__ w4, char* __restrict__ ws,
                                                      u16* __restrict__ out) {
  __shared__ __attribute__((aligned(16))) char lds[163840];
  const int tid = threadIdx.x, lane = tid & 63, wave = __builtin_amdgcn_readfirstlane(tid >> 6);
  const int b = blockIdx.x, xcd = b & 7, idx = b >> 3;
  const unsigned sLds = (unsigned)(size_t)lds, sScr = sLds + 163584;
  asm volatile("s_getpc_b64 s[98:99]\n\ts_mov_b32 m0, %1\n\ts_nop 0\n\tglobal_load_lds_dword %0, s[98:99]" :: "v"(tid * 128), "s"(sScr) : "s98", "s99", "memory");
  asm volatile("" :: "s"(q), "s"(kin), "s"(vin), "s"(shift_k), "s"(ssm_k), "s"(ssm_D), "s"(w1), "s"(w2), "s"(w3), "s"(w4), "s"(ws), "s"(out));
  int kind, rep, slice, kh = 0;
  const int P = xcd >> 1, par = (xcd & 1) ^ FLIP, li = idx - (par ? 20 + RE : 12 - RE);
  const int qq = (par ? 0 : 12 + RE) + idx - 8;
  auto code_touch = [&]() {
    unsigned long long cp;
    unsigned n;
    asm volatile("s_getpc_b64 %0\nLpd%=:\n"
                 "s_mov_b32 s98, Lcode_r_b-Lpd%=\ns_mov_b32 %1, Lcode_r_e-Lcode_r_b\n"
                 "s_cmp_eq_u32 %2, 1\ns_cselect_b32 s98, Lcode_g12_b-Lpd%=, s98\ns_cselect_b32 %1, Lcode_g2_b-Lcode_g12_b, %1\n"
                 "s_cmp_eq_u32 %2, 2\ns_cselect_b32 s98, Lcode_g2_b-Lpd%=, s98\ns_cselect_b32 %1, Lcode_g12_e-Lcode_g2_b, %1\n"
                 "s_cmp_eq_u32 %2, 3\ns_cselect_b32 s98, Lcode_g3_b-Lpd%=, s98\ns_cselect_b32 %1, Lcode_g3_e-Lcode_g3_b, %1\n"
                 "s_cmp_eq_u32 %2, 4\ns_cselect_b32 s98, Lcode_g4_b-Lpd%=, s98\ns_cselect_b32 %1, Lcode_g4_e-Lcode_g4_b, %1\n"
                 "s_cmp_eq_u32 %2, 5\ns_cselect_b32 s98, Lcode_g3o_b-Lpd%=, s98\ns_cselect_b32 %1, Lcode_g3o_e-Lcode_g3o_b, %1\n"
                 "s_ashr_i32 s99, s98, 31\ns_mov_b64 vcc, %0\ns_add_u32 s98, s98, vcc_lo\ns_addc_u32 s99, s99, vcc_hi\ns_mov_b64 %0, s[98:99]"
                 : "=&s"(cp), "=&s"(n) : "s"(kind) : "s98", "s99", "scc", "vcc");
    asm volatile("s_mov_b32 m0, %3\n\ts_nop 0\n\tglobal_load_lds_dword %0, %2\n\tglobal_load_lds_dword %1, %2"
                 :: "v"(min(tid * 128u, n - 4)), "v"(min(32768 + tid * 128u, n - 4)), "s"(cp), "s"(sScr) : "memory");
  };
  unsigned ep = 0;
#define EP_LOAD() asm volatile("global_load_dword %0, %1, %2 sc1" : "=v"(ep) : "v"(OFF_EP), "s"(ws) : "memory")
  auto epoch = [&]() { const unsigned e = __builtin_amdgcn_readfirstlane(ep) + 1u; return e ? e : 1u; };
  auto q_unit = [&](QRegs& QR, int f) {
    float ma, va, mb, vb_;
    row_stats(QR.a, lds + CV_STG, 0, lane, wave, ma, va);
    row_stats(QR.b, lds + CV_STG, 1, lane, wave, mb, vb_);
    q_store(ws, 2 * f, lane, wave, QR.a, ma, va);
    q_store(ws, 2 * f + 1, lane, wave, QR.b, mb, vb_);
  };
  auto publish = [&](unsigned off) {
    BARRIER();
    if (tid == 0) __hip_atomic_store((gu32*)(ws + off), ep, __ATOMIC_RELAXED, __HIP_MEMORY_SCOPE_AGENT);
  };
  if (idx < 8 && par == MAP12) {
    kind = 1; rep = P; slice = idx;
    code_touch();
    prewrite(ws, OFF_FH + slice * 128, 1024, rep, tid);
  } else {
    const int cv = (li + (par ? 20 + RE : 0)) * 4 + P, n = idx < 8 ? 64 + P * 8 + idx : qq * 4 + P;
    QRegs QR;
    if (idx >= 8) {
      KvRegs KR;
      SkRegs SK;
      if (li >= 0) {
        const int tile = 64 + cv;
        u32x4v hv[8];
        unsigned hx, dD;
        h_load(hv, hx, dD, ssm_k, ssm_D, 8 * cv, tid);
        EP_LOAD();
        sk_load(SK, shift_k, tile, tid);
        kv_load(KR, kin, vin, tile, tid);
        {
          int l2 = li, p2 = par;
          asm volatile("" : "+s"(l2), "+s"(p2));
          const int nl = 8 - (P & 1), l = ((15 * P + 1) >> 1) + l2, tl = (l * 205) >> 10, kl = l - 5 * tl;
          kind = l2 < 20 ? 4 : 6; rep = l2 < 20 ? l2 >> 2 : 0; slice = l2 < 20 ? l2 & 3 : ((9 * P) >> 1) + l2 - 20; kh = P;
          if (p2 == 1) {
            kind = l2 < nl ? 3 : 6; rep = l2 < nl ? 3 + ((kl & 1) | (kl >> 2)) : 0;
            slice = l2 < nl ? 3 * tl + (kl >> 1) : ((9 * P) >> 1) + RE + l2 - nl;
          }
        }
        touch_lds((const char*)((par ^ MAP12) ? w2 : w1) + (size_t)(par ? (li < 8 ? li * 256 + tid : 0) : (li < 16 ? li * 128 + (tid & 127) : 0)) * 1024 + 384, sScr);
        code_touch();
        h_wait<18>(hv, hx, dD);
        asm volatile("" : "+v"(ep));
        ep = epoch();
        h_spread(hv, hx, lds, tid);
        kv_wait<2>(KR, SK);
        kv_store(KR, SK, ws, lds + CV_STG, tile, tid);
        WAIT_VM(0);
        publish(OFF_FKV + tile * 128);
        const char* wp = (const char*)w4 + (kind != 4 ? 0 : (size_t)(kh * 896 + li * 15) * 2048 + min(tid, 239) * 128);
        conv_part(ws, dD, lds, cv, tid, lane, wave, ep, wp, kind != 4 ? 0 : 298 * 2048);
      } else {
        kv_load(KR, kin, vin, n, tid);
        sk_load(SK, shift_k, n, tid);
        EP_LOAD();
        {
          int q2 = qq;
          asm volatile("" : "+s"(q2));
          const int e = 15 * P + q2, t = (e * 205) >> 11, k = e - 10 * t, k3 = min((k * 11) >> 5, 2);
          kind = q2 < 15 ? 3 : 5; rep = q2 < 15 ? k - 3 * k3 : P; slice = q2 < 15 ? 3 * t + k3 : 18;
        }
        kv_wait<0>(KR, SK);
        asm volatile("" : "+v"(ep));
        ep = epoch();
        kv_store(KR, SK, ws, lds + CV_STG, n, tid);
        pads(ws, n * 4 + wave, lane, ep);
        if (n < 8)
          st16(ws + OFF_FXP + (lane >> 3) * 16384 + wave * 1024 + (2 * (n & 3) + (n >> 2)) * 128 + (lane & 7) * 16, __builtin_bit_cast(u16x8, (u32x4v){ep - 16u, 0u, 0u, 0u}));
        kv_load(KR, kin, vin, 192 + n, tid);
        WAIT_VM(11);
        publish(OFF_FKV + n * 128);
        q_load(QR, q, 32 + n, lane, wave);
        kv_wait<8>(KR, SK);
        kv_store(KR, SK, ws, lds, 192 + n, tid);
        q_wait<0>(QR, ep);
        publish(OFF_FKV + (192 + n) * 128);
      }
    } else {
      QRegs Q0;
      q_load(Q0, q, n - 64, lane, wave);
      EP_LOAD();
      kind = 2; rep = P; slice = idx;
      q_wait<0>(Q0, ep);
      ep = epoch();
      q_load(QR, q, 32 + n, lane, wave);
      q_unit(Q0, n - 64);
      q_wait<0>(QR, ep);
      publish(OFF_FY + (n - 64) * 128);
    }
    if (li < 0) {
      q_unit(QR, 32 + n);
      {
        const char* wp = (const char*)w2 + (size_t)(n < 64 ? 0 : (n - 64) * 512 + tid) * 128;
        touch_lds(wp, sScr);
        touch_lds(wp + (n < 64 ? 0 : 32768), sScr);
      }
      WAIT_VM(2);
      publish(OFF_FY + (32 + n) * 128);
      if (n >= 64) {
        prewrite(ws, OFF_FX1 + slice * 128, 1024, rep, tid);
        prewrite(ws, OFF_FP + slice * 128, 2048, rep, tid);
        prewrite(ws, OFF_FP + (8 + slice) * 128, 2048, rep, tid);
        prewrite(ws, OFF_FG + slice * 128, 2560, rep, tid);
        prewrite(ws, OFF_FG + (8 + slice) * 128, 2560, rep, tid);
        if (slice < 3) prewrite(ws, OFF_FG + (16 + slice) * 128, 2560, rep, tid);
        st16(ws + OFF_FXP + slice * 128 + (132 + rep + 4 * (tid >> 3)) * 1024 + (tid & 7) * 16, __builtin_bit_cast(u16x8, (u32x4v){0u, 0u, 0u, 0u}));
      }
      code_touch();
      if (n < 64) {
#pragma unroll
        for (int i = 0; i < 4; ++i) {
          const unsigned x = rep * 1024 + i * 256 + tid, row = kind == 3 ? (x * 21846) >> 16 : x >> 1;
          if (rep < (kind == 3 ? 3 : 2)) touch_lds((const char*)w3 + (size_t)row * 7168 + slice * 384 + (x - row * (kind == 3 ? 3 : 2)) * 128, sScr);
        }
      }
    }
    BARRIER();
  }
  const unsigned sDbg0 = OFF_DBG + (b * 4 + wave) * 8192;
#define LANE_ID(x) int x; asm volatile("v_mbcnt_lo_u32_b32 %0, -1, 0\n\tv_mbcnt_hi_u32_b32 %0, -1, %0" : "=v"(x))
#define OOB 0x80000000u
#define COMMON(NF, FIN, FOUT, SN0, WS, LDW, NREP)                                                                                    \
  LANE_ID(ln);                                                                                                                       \
  const int l15 = ln & 15, lg = ln >> 4, lq = l15 >> 2, lp = ln & 3, rr = 8 * lg + lq;                                               \
  const unsigned vA = ln * 16, vFlag = (ln < FLW && wave == 3) ? (FOUT) + ln * 4 : OOB;                                                        \
  const unsigned vPollOff = wave < 2 && ln < (NF) ? (FIN) + ((NF) == 16 && ln >= 8 ? (ln & 2 ? ln : ln - 16) : ln) * 128 : OOB, vSnapOff = wave == 0 ? vPollOff : OOB;                   \
  const unsigned vSnapRd = (SN0) + (ln >> 5) * (WS) + ((ln & 31) < (NF) ? (ln & 31) : 0) * 4;                                        \
  const unsigned vWd0 = (ln >> 3) * (LDW) + ((ln & 7) ^ swzB(ln >> 3)) * 16, vWd1 = ((ln >> 3) + 8) * (LDW) + ((ln & 7) ^ swzB(8 + (ln >> 3))) * 16; \
  const unsigned vWr0 = (kind == 4 ? wave * 4096 : (wave >> 1) * 8192) + rr * 128 + (((kind == 4 ? kh >> 1 : wave & 1) * 4 + lp) ^ swzB(rr)) * 16, vWr1 = vWr0 + 8;                  \
  const unsigned vWr2 = kind == 4 ? vWr0 ^ 64 : 16384 + rr * 128 + ((2 * wave + (lp >> 1)) ^ swzB(rr)) * 16 + (lp & 1) * 8;                                  \
                                            \
  const int res = kind == 3 ? rep + 3 * KE : rep;                     \
  const unsigned sStep = (NREP) << 15, sU0 = (rep < 3 ? rep : res) << 15, sX1 = ((NREP) == 4 ? 128 + rep : 128 + (res + 2) % 5) << 15; \
  const unsigned sX2 = ((NREP) == 4 ? 260 + rep : 260 + (res + 4) % 5) << 15, sDbg = sDbg0, sBuf = kind != 3 ? kind : (rep < 3 ? 3 : 5) << 15
  constexpr unsigned AUX = 131072;
  if (kind == 3 || kind == 5) {
    COMMON(8, OFF_FX1, OFF_FG + slice * 128, AUX + 1536, 2048, 7168, kind == 3 ? 5 : 4);
    const unsigned vSrc = OFF_X1 + wave * 4096 + vA, vOut = OFF_G + (slice * 6 + wave) * 1024 + vA;
    const unsigned vOut2 = OFF_G + (slice * 6 + 4 + (wave >> 1)) * 1024 + (((wave & 1) * 2 + (lg >> 1)) * 16 + l15) * 16 + (lg & 1) * 8;
    const unsigned vAuxS = OFF_ST + (ln & 7) * 128 + (wave * 8 + (ln >> 3)) * 4, vX = AUX + wave * 2048 + ln * 4;
    const unsigned vStW = (ln & 7) == 0 ? AUX + 1280 + (wave * 8 + (ln >> 3)) * 4 : AUX + 24576 + wave * 2048 + 1536 + ln * 4, vStR = AUX + 1280 + l15 * 8;
    const unsigned sWw = 16 * wave * 7168 + slice * 384;
    if (kind == 3) { asm volatile(R"(
Lcode_g3_b:
s_mov_b64 s[36:37],%0
s_mov_b32 s38,122727552
s_mov_b32 s39,0x00020000
s_mov_b32 s54,%1
s_mov_b32 s55,%2
s_mov_b32 s56,%3
s_mov_b32 s58,%4
s_mov_b32 s83,%5
s_mov_b32 s82,%6
s_mov_b32 s84,%7
v_mov_b32 v0,%8
v_mov_b32 v2,%9
v_mov_b32 v3,%10
v_mov_b32 v4,%11
v_mov_b32 v6,%12
v_mov_b32 v7,%13
v_mov_b32 v8,%14
v_mov_b32 v9,%15
v_mov_b32 v10,%16
v_mov_b32 v11,%17
v_mov_b32 v28,%18
v_mov_b32 v29,%19
v_mov_b32 v30,%20
v_mov_b32 v31,%21
v_mov_b32 v32,%22
s_mov_b64 s[40:41],%23
s_mov_b32 s42,7340032
s_mov_b32 s43,0x00020000
s_lshl_b32 s88,s54,11
s_mov_b32 s69,122727424
buffer_load_dword v5,off,s[36:39],s69 sc1
s_add_u32 m0,s88,0
s_add_u32 s61,s82,0
buffer_load_dwordx4 v28,s[40:43],s61 offen offset:0  lds
s_add_u32 m0,s88,1024
s_nop 0
buffer_load_dwordx4 v29,s[40:43],s61 offen offset:0  lds
s_add_u32 m0,s88,8064
s_nop 0
buffer_load_dwordx4 v28,s[40:43],s61 offen offset:128  lds
s_add_u32 m0,s88,9088
s_nop 0
buffer_load_dwordx4 v29,s[40:43],s61 offen offset:128  lds
s_add_u32 m0,s88,16128
s_nop 0
buffer_load_dwordx4 v28,s[40:43],s61 offen offset:256  lds
s_add_u32 m0,s88,17152
s_nop 0
buffer_load_dwordx4 v29,s[40:43],s61 offen offset:256  lds
s_add_u32 m0,s88,24576
s_add_u32 s61,s82,458752
buffer_load_dwordx4 v28,s[40:43],s61 offen offset:0  lds
s_add_u32 m0,s88,25600
s_nop 0
buffer_load_dwordx4 v29,s[40:43],s61 offen offset:0  lds
s_add_u32 m0,s88,32640
s_nop 0
buffer_load_dwordx4 v28,s[40:43],s61 offen offset:128  lds
s_add_u32 m0,s88,33664
s_nop 0
buffer_load_dwordx4 v29,s[40:43],s61 offen offset:128  lds
s_add_u32 m0,s88,40704
s_nop 0
buffer_load_dwordx4 v28,s[40:43],s61 offen offset:256  lds
s_add_u32 m0,s88,41728
s_nop 0
buffer_load_dwordx4 v29,s[40:43],s61 offen offset:256  lds
s_add_u32 m0,s88,49152
s_add_u32 s61,s82,917504
buffer_load_dwordx4 v28,s[40:43],s61 offen offset:0  lds
s_add_u32 m0,s88,50176
s_nop 0
buffer_load_dwordx4 v29,s[40:43],s61 offen offset:0  lds
s_add_u32 m0,s88,57216
s_nop 0
buffer_load_dwordx4 v28,s[40:43],s61 offen offset:128  lds
s_add_u32 m0,s88,58240
s_nop 0
buffer_load_dwordx4 v29,s[40:43],s61 offen offset:128  lds
s_add_u32 m0,s88,65280
s_nop 0
buffer_load_dwordx4 v28,s[40:43],s61 offen offset:256  lds
s_add_u32 m0,s88,66304
s_nop 0
buffer_load_dwordx4 v29,s[40:43],s61 offen offset:256  lds
s_add_u32 m0,s88,73728
s_add_u32 s61,s82,1376256
buffer_load_dwordx4 v28,s[40:43],s61 offen offset:0  lds
s_add_u32 m0,s88,74752
s_nop 0
buffer_load_dwordx4 v29,s[40:43],s61 offen offset:0  lds
s_add_u32 m0,s88,81792
s_nop 0
buffer_load_dwordx4 v28,s[40:43],s61 offen offset:128  lds
s_add_u32 m0,s88,82816
s_nop 0
buffer_load_dwordx4 v29,s[40:43],s61 offen offset:128  lds
s_add_u32 m0,s88,89856
s_nop 0
buffer_load_dwordx4 v28,s[40:43],s61 offen offset:256  lds
s_add_u32 m0,s88,90880
s_nop 0
buffer_load_dwordx4 v29,s[40:43],s61 offen offset:256  lds
v_add_u32 v40,49152,v30
v_add_u32 v41,49152,v31
v_add_u32 v42,49152,v32
v_add_u32 v43,98304,v30
v_add_u32 v44,98304,v31
v_add_u32 v45,98304,v32
v_mov_b32 v84,0x3f803f80
v_mov_b32 v85,0x3f803f80
v_mov_b32 v86,0x3f803f80
v_mov_b32 v87,0x3f803f80
s_waitcnt vmcnt(18)
s_barrier
s_add_u32 m0,s88,98304
s_add_u32 s61,s82,1835008
buffer_load_dwordx4 v28,s[40:43],s61 offen offset:0  lds
s_add_u32 m0,s88,99328
ds_read_b64_tr_b16 a[0:1],v30 offset:0
buffer_load_dwordx4 v29,s[40:43],s61 offen offset:0  lds
s_add_u32 m0,s88,106368
ds_read_b64_tr_b16 a[2:3],v30 offset:512
buffer_load_dwordx4 v28,s[40:43],s61 offen offset:128  lds
s_add_u32 m0,s88,107392
ds_read_b64_tr_b16 a[4:5],v31 offset:0
buffer_load_dwordx4 v29,s[40:43],s61 offen offset:128  lds
s_add_u32 m0,s88,114432
ds_read_b64_tr_b16 a[6:7],v31 offset:512
buffer_load_dwordx4 v28,s[40:43],s61 offen offset:256  lds
s_add_u32 m0,s88,115456
ds_read_b64_tr_b16 a[8:9],v32 offset:0
buffer_load_dwordx4 v29,s[40:43],s61 offen offset:256  lds
ds_read_b64_tr_b16 a[10:11],v32 offset:512
ds_read_b64_tr_b16 a[12:13],v30 offset:4096
ds_read_b64_tr_b16 a[14:15],v30 offset:4608
ds_read_b64_tr_b16 a[16:17],v31 offset:4096
ds_read_b64_tr_b16 a[18:19],v31 offset:4608
ds_read_b64_tr_b16 a[20:21],v32 offset:4096
ds_read_b64_tr_b16 a[22:23],v32 offset:4608
s_waitcnt vmcnt(18)
s_barrier
s_add_u32 m0,s88,122880
s_add_u32 s61,s82,2293760
buffer_load_dwordx4 v28,s[40:43],s61 offen offset:0  lds
s_add_u32 m0,s88,123904
ds_read_b64_tr_b16 a[24:25],v30 offset:24576
buffer_load_dwordx4 v29,s[40:43],s61 offen offset:0  lds
s_add_u32 m0,s88,130944
ds_read_b64_tr_b16 a[26:27],v30 offset:25088
buffer_load_dwordx4 v28,s[40:43],s61 offen offset:128  lds
s_add_u32 m0,s88,131968
ds_read_b64_tr_b16 a[28:29],v31 offset:24576
buffer_load_dwordx4 v29,s[40:43],s61 offen offset:128  lds
s_add_u32 m0,s88,139008
ds_read_b64_tr_b16 a[30:31],v31 offset:25088
buffer_load_dwordx4 v28,s[40:43],s61 offen offset:256  lds
s_add_u32 m0,s88,140032
ds_read_b64_tr_b16 a[32:33],v32 offset:24576
buffer_load_dwordx4 v29,s[40:43],s61 offen offset:256  lds
ds_read_b64_tr_b16 a[34:35],v32 offset:25088
ds_read_b64_tr_b16 a[36:37],v30 offset:28672
ds_read_b64_tr_b16 a[38:39],v30 offset:29184
ds_read_b64_tr_b16 a[40:41],v31 offset:28672
ds_read_b64_tr_b16 a[42:43],v31 offset:29184
ds_read_b64_tr_b16 a[44:45],v32 offset:28672
ds_read_b64_tr_b16 a[46:47],v32 offset:29184
s_waitcnt lgkmcnt(12)
v_mfma_f32_16x16x32_bf16 v[16:19],a[0:3],v[84:87],0
v_mfma_f32_16x16x32_bf16 v[20:23],a[4:7],v[84:87],0
v_mfma_f32_16x16x32_bf16 v[24:27],a[8:11],v[84:87],0
v_mfma_f32_16x16x32_bf16 v[16:19],a[12:15],v[84:87],v[16:19]
v_mfma_f32_16x16x32_bf16 v[20:23],a[16:19],v[84:87],v[20:23]
v_mfma_f32_16x16x32_bf16 v[24:27],a[20:23],v[84:87],v[24:27]
s_waitcnt vmcnt(18)
s_barrier
s_add_u32 m0,s88,0
s_add_u32 s61,s82,2752512
buffer_load_dwordx4 v28,s[40:43],s61 offen offset:0  lds
s_add_u32 m0,s88,1024
ds_read_b64_tr_b16 a[48:49],v40 offset:0
buffer_load_dwordx4 v29,s[40:43],s61 offen offset:0  lds
s_add_u32 m0,s88,8064
ds_read_b64_tr_b16 a[50:51],v40 offset:512
buffer_load_dwordx4 v28,s[40:43],s61 offen offset:128  lds
s_add_u32 m0,s88,9088
ds_read_b64_tr_b16 a[52:53],v41 offset:0
buffer_load_dwordx4 v29,s[40:43],s61 offen offset:128  lds
s_add_u32 m0,s88,16128
ds_read_b64_tr_b16 a[54:55],v41 offset:512
buffer_load_dwordx4 v28,s[40:43],s61 offen offset:256  lds
s_add_u32 m0,s88,17152
ds_read_b64_tr_b16 a[56:57],v42 offset:0
buffer_load_dwordx4 v29,s[40:43],s61 offen offset:256  lds
ds_read_b64_tr_b16 a[58:59],v42 offset:512
ds_read_b64_tr_b16 a[60:61],v40 offset:4096
ds_read_b64_tr_b16 a[62:63],v40 offset:4608
ds_read_b64_tr_b16 a[64:65],v41 offset:4096
ds_read_b64_tr_b16 a[66:67],v41 offset:4608)"
R"(
ds_read_b64_tr_b16 a[68:69],v42 offset:4096
ds_read_b64_tr_b16 a[70:71],v42 offset:4608
s_waitcnt lgkmcnt(12)
v_mfma_f32_16x16x32_bf16 v[16:19],a[24:27],v[84:87],v[16:19]
v_mfma_f32_16x16x32_bf16 v[20:23],a[28:31],v[84:87],v[20:23]
v_mfma_f32_16x16x32_bf16 v[24:27],a[32:35],v[84:87],v[24:27]
v_mfma_f32_16x16x32_bf16 v[16:19],a[36:39],v[84:87],v[16:19]
v_mfma_f32_16x16x32_bf16 v[20:23],a[40:43],v[84:87],v[20:23]
v_mfma_f32_16x16x32_bf16 v[24:27],a[44:47],v[84:87],v[24:27]
s_waitcnt vmcnt(18)
s_barrier
s_add_u32 m0,s88,24576
s_add_u32 s61,s82,3211264
buffer_load_dwordx4 v28,s[40:43],s61 offen offset:0  lds
s_add_u32 m0,s88,25600
ds_read_b64_tr_b16 a[72:73],v40 offset:24576
buffer_load_dwordx4 v29,s[40:43],s61 offen offset:0  lds
s_add_u32 m0,s88,32640
ds_read_b64_tr_b16 a[74:75],v40 offset:25088
buffer_load_dwordx4 v28,s[40:43],s61 offen offset:128  lds
s_add_u32 m0,s88,33664
ds_read_b64_tr_b16 a[76:77],v41 offset:24576
buffer_load_dwordx4 v29,s[40:43],s61 offen offset:128  lds
s_add_u32 m0,s88,40704
ds_read_b64_tr_b16 a[78:79],v41 offset:25088
buffer_load_dwordx4 v28,s[40:43],s61 offen offset:256  lds
s_add_u32 m0,s88,41728
ds_read_b64_tr_b16 a[80:81],v42 offset:24576
buffer_load_dwordx4 v29,s[40:43],s61 offen offset:256  lds
ds_read_b64_tr_b16 a[82:83],v42 offset:25088
ds_read_b64_tr_b16 a[84:85],v40 offset:28672
ds_read_b64_tr_b16 a[86:87],v40 offset:29184
ds_read_b64_tr_b16 a[88:89],v41 offset:28672
ds_read_b64_tr_b16 a[90:91],v41 offset:29184
ds_read_b64_tr_b16 a[92:93],v42 offset:28672
ds_read_b64_tr_b16 a[94:95],v42 offset:29184
s_waitcnt lgkmcnt(12)
v_mfma_f32_16x16x32_bf16 v[16:19],a[48:51],v[84:87],v[16:19]
v_mfma_f32_16x16x32_bf16 v[20:23],a[52:55],v[84:87],v[20:23]
v_mfma_f32_16x16x32_bf16 v[24:27],a[56:59],v[84:87],v[24:27]
v_mfma_f32_16x16x32_bf16 v[16:19],a[60:63],v[84:87],v[16:19]
v_mfma_f32_16x16x32_bf16 v[20:23],a[64:67],v[84:87],v[20:23]
v_mfma_f32_16x16x32_bf16 v[24:27],a[68:71],v[84:87],v[24:27]
s_waitcnt vmcnt(18)
s_barrier
s_add_u32 m0,s88,49152
s_add_u32 s61,s82,3670016
buffer_load_dwordx4 v28,s[40:43],s61 offen offset:0  lds
s_add_u32 m0,s88,50176
ds_read_b64_tr_b16 a[96:97],v43 offset:0
buffer_load_dwordx4 v29,s[40:43],s61 offen offset:0  lds
s_add_u32 m0,s88,57216
ds_read_b64_tr_b16 a[98:99],v43 offset:512
buffer_load_dwordx4 v28,s[40:43],s61 offen offset:128  lds
s_add_u32 m0,s88,58240
ds_read_b64_tr_b16 a[100:101],v44 offset:0
buffer_load_dwordx4 v29,s[40:43],s61 offen offset:128  lds
s_add_u32 m0,s88,65280
ds_read_b64_tr_b16 a[102:103],v44 offset:512
buffer_load_dwordx4 v28,s[40:43],s61 offen offset:256  lds
s_add_u32 m0,s88,66304
ds_read_b64_tr_b16 a[104:105],v45 offset:0
buffer_load_dwordx4 v29,s[40:43],s61 offen offset:256  lds
ds_read_b64_tr_b16 a[106:107],v45 offset:512
ds_read_b64_tr_b16 a[108:109],v43 offset:4096
ds_read_b64_tr_b16 a[110:111],v43 offset:4608
ds_read_b64_tr_b16 a[112:113],v44 offset:4096
ds_read_b64_tr_b16 a[114:115],v44 offset:4608
ds_read_b64_tr_b16 a[116:117],v45 offset:4096
ds_read_b64_tr_b16 a[118:119],v45 offset:4608
s_waitcnt lgkmcnt(12)
v_mfma_f32_16x16x32_bf16 v[16:19],a[72:75],v[84:87],v[16:19]
v_mfma_f32_16x16x32_bf16 v[20:23],a[76:79],v[84:87],v[20:23]
v_mfma_f32_16x16x32_bf16 v[24:27],a[80:83],v[84:87],v[24:27]
v_mfma_f32_16x16x32_bf16 v[16:19],a[84:87],v[84:87],v[16:19]
v_mfma_f32_16x16x32_bf16 v[20:23],a[88:91],v[84:87],v[20:23]
v_mfma_f32_16x16x32_bf16 v[24:27],a[92:95],v[84:87],v[24:27]
s_waitcnt vmcnt(18)
s_barrier
s_add_u32 m0,s88,73728
s_add_u32 s61,s82,4128768
buffer_load_dwordx4 v28,s[40:43],s61 offen offset:0  lds
s_add_u32 m0,s88,74752
ds_read_b64_tr_b16 a[120:121],v43 offset:24576
buffer_load_dwordx4 v29,s[40:43],s61 offen offset:0  lds
s_add_u32 m0,s88,81792
ds_read_b64_tr_b16 a[122:123],v43 offset:25088
buffer_load_dwordx4 v28,s[40:43],s61 offen offset:128  lds
s_add_u32 m0,s88,82816
ds_read_b64_tr_b16 a[124:125],v44 offset:24576
buffer_load_dwordx4 v29,s[40:43],s61 offen offset:128  lds
s_add_u32 m0,s88,89856
ds_read_b64_tr_b16 a[126:127],v44 offset:25088
buffer_load_dwordx4 v28,s[40:43],s61 offen offset:256  lds
s_add_u32 m0,s88,90880
ds_read_b64_tr_b16 a[128:129],v45 offset:24576
buffer_load_dwordx4 v29,s[40:43],s61 offen offset:256  lds
ds_read_b64_tr_b16 a[130:131],v45 offset:25088
ds_read_b64_tr_b16 a[132:133],v43 offset:28672
ds_read_b64_tr_b16 a[134:135],v43 offset:29184
ds_read_b64_tr_b16 a[136:137],v44 offset:28672
ds_read_b64_tr_b16 a[138:139],v44 offset:29184
ds_read_b64_tr_b16 a[140:141],v45 offset:28672
ds_read_b64_tr_b16 a[142:143],v45 offset:29184
s_waitcnt lgkmcnt(12)
v_mfma_f32_16x16x32_bf16 v[16:19],a[96:99],v[84:87],v[16:19]
v_mfma_f32_16x16x32_bf16 v[20:23],a[100:103],v[84:87],v[20:23]
v_mfma_f32_16x16x32_bf16 v[24:27],a[104:107],v[84:87],v[24:27]
v_mfma_f32_16x16x32_bf16 v[16:19],a[108:111],v[84:87],v[16:19]
v_mfma_f32_16x16x32_bf16 v[20:23],a[112:115],v[84:87],v[20:23]
v_mfma_f32_16x16x32_bf16 v[24:27],a[116:119],v[84:87],v[24:27]
s_waitcnt vmcnt(18)
s_barrier
s_add_u32 m0,s88,98304
s_add_u32 s61,s82,4587520
buffer_load_dwordx4 v28,s[40:43],s61 offen offset:0  lds
s_add_u32 m0,s88,99328
ds_read_b64_tr_b16 a[144:145],v30 offset:0
buffer_load_dwordx4 v29,s[40:43],s61 offen offset:0  lds
s_add_u32 m0,s88,106368
ds_read_b64_tr_b16 a[146:147],v30 offset:512
buffer_load_dwordx4 v28,s[40:43],s61 offen offset:128  lds
s_add_u32 m0,s88,107392
ds_read_b64_tr_b16 a[148:149],v31 offset:0
buffer_load_dwordx4 v29,s[40:43],s61 offen offset:128  lds
s_add_u32 m0,s88,114432
ds_read_b64_tr_b16 a[150:151],v31 offset:512
buffer_load_dwordx4 v28,s[40:43],s61 offen offset:256  lds
s_add_u32 m0,s88,115456
ds_read_b64_tr_b16 a[152:153],v32 offset:0
buffer_load_dwordx4 v29,s[40:43],s61 offen offset:256  lds
ds_read_b64_tr_b16 a[154:155],v32 offset:512
ds_read_b64_tr_b16 a[156:157],v30 offset:4096
ds_read_b64_tr_b16 a[158:159],v30 offset:4608
ds_read_b64_tr_b16 a[160:161],v31 offset:4096
ds_read_b64_tr_b16 a[162:163],v31 offset:4608
ds_read_b64_tr_b16 a[164:165],v32 offset:4096
ds_read_b64_tr_b16 a[166:167],v32 offset:4608
s_waitcnt lgkmcnt(12)
v_mfma_f32_16x16x32_bf16 v[16:19],a[120:123],v[84:87],v[16:19]
v_mfma_f32_16x16x32_bf16 v[20:23],a[124:127],v[84:87],v[20:23]
v_mfma_f32_16x16x32_bf16 v[24:27],a[128:131],v[84:87],v[24:27]
v_mfma_f32_16x16x32_bf16 v[16:19],a[132:135],v[84:87],v[16:19]
v_mfma_f32_16x16x32_bf16 v[20:23],a[136:139],v[84:87],v[20:23]
v_mfma_f32_16x16x32_bf16 v[24:27],a[140:143],v[84:87],v[24:27]
s_waitcnt vmcnt(18)
s_barrier
s_add_u32 m0,s88,122880
s_add_u32 s61,s82,5046272
buffer_load_dwordx4 v28,s[40:43],s61 offen offset:0  lds
s_add_u32 m0,s88,123904
ds_read_b64_tr_b16 a[168:169],v30 offset:24576
buffer_load_dwordx4 v29,s[40:43],s61 offen offset:0  lds
s_add_u32 m0,s88,130944
ds_read_b64_tr_b16 a[170:171],v30 offset:25088
buffer_load_dwordx4 v28,s[40:43],s61 offen offset:128  lds
s_add_u32 m0,s88,131968
ds_read_b64_tr_b16 a[172:173],v31 offset:24576
buffer_load_dwordx4 v29,s[40:43],s61 offen offset:128  lds
s_add_u32 m0,s88,139008
ds_read_b64_tr_b16 a[174:175],v31 offset:25088
buffer_load_dwordx4 v28,s[40:43],s61 offen offset:256  lds
s_add_u32 m0,s88,140032
ds_read_b64_tr_b16 a[176:177],v32 offset:24576
buffer_load_dwordx4 v29,s[40:43],s61 offen offset:256  lds
ds_read_b64_tr_b16 a[178:179],v32 offset:25088
ds_read_b64_tr_b16 a[180:181],v30 offset:28672
ds_read_b64_tr_b16 a[182:183],v30 offset:29184
ds_read_b64_tr_b16 a[184:185],v31 offset:28672
ds_read_b64_tr_b16 a[186:187],v31 offset:29184
ds_read_b64_tr_b16 a[188:189],v32 offset:28672
ds_read_b64_tr_b16 a[190:191],v32 offset:29184
s_waitcnt lgkmcnt(12)
v_mfma_f32_16x16x32_bf16 v[16:19],a[144:147],v[84:87],v[16:19]
v_mfma_f32_16x16x32_bf16 v[20:23],a[148:151],v[84:87],v[20:23]
v_mfma_f32_16x16x32_bf16 v[24:27],a[152:155],v[84:87],v[24:27]
v_mfma_f32_16x16x32_bf16 v[16:19],a[156:159],v[84:87],v[16:19]
v_mfma_f32_16x16x32_bf16 v[20:23],a[160:163],v[84:87],v[20:23]
v_mfma_f32_16x16x32_bf16 v[24:27],a[164:167],v[84:87],v[24:27]
s_waitcnt vmcnt(18)
s_barrier
s_add_u32 m0,s88,0
s_add_u32 s61,s82,5505024
buffer_load_dwordx4 v28,s[40:43],s61 offen offset:0  lds
s_add_u32 m0,s88,1024
ds_read_b64_tr_b16 a[192:193],v40 offset:0
buffer_load_dwordx4 v29,s[40:43],s61 offen offset:0  lds
s_add_u32 m0,s88,8064
ds_read_b64_tr_b16 a[194:195],v40 offset:512
buffer_load_dwordx4 v28,s[40:43],s61 offen offset:128  lds
s_add_u32 m0,s88,9088
ds_read_b64_tr_b16 a[196:197],v41 offset:0
buffer_load_dwordx4 v29,s[40:43],s61 offen offset:128  lds
s_add_u32 m0,s88,16128
ds_read_b64_tr_b16 a[198:199],v41 offset:512
buffer_load_dwordx4 v28,s[40:43],s61 offen offset:256  lds
s_add_u32 m0,s88,17152
ds_read_b64_tr_b16 a[200:201],v42 offset:0
buffer_load_dwordx4 v29,s[40:43],s61 offen offset:256  lds
ds_read_b64_tr_b16 a[202:203],v42 offset:512)"
R"(
ds_read_b64_tr_b16 a[204:205],v40 offset:4096
ds_read_b64_tr_b16 a[206:207],v40 offset:4608
ds_read_b64_tr_b16 a[208:209],v41 offset:4096
ds_read_b64_tr_b16 a[210:211],v41 offset:4608
ds_read_b64_tr_b16 a[212:213],v42 offset:4096
ds_read_b64_tr_b16 a[214:215],v42 offset:4608
s_waitcnt lgkmcnt(12)
v_mfma_f32_16x16x32_bf16 v[16:19],a[168:171],v[84:87],v[16:19]
v_mfma_f32_16x16x32_bf16 v[20:23],a[172:175],v[84:87],v[20:23]
v_mfma_f32_16x16x32_bf16 v[24:27],a[176:179],v[84:87],v[24:27]
v_mfma_f32_16x16x32_bf16 v[16:19],a[180:183],v[84:87],v[16:19]
v_mfma_f32_16x16x32_bf16 v[20:23],a[184:187],v[84:87],v[20:23]
v_mfma_f32_16x16x32_bf16 v[24:27],a[188:191],v[84:87],v[24:27]
s_waitcnt vmcnt(18)
s_barrier
s_add_u32 m0,s88,24576
s_add_u32 s61,s82,5963776
buffer_load_dwordx4 v28,s[40:43],s61 offen offset:0  lds
s_add_u32 m0,s88,25600
ds_read_b64_tr_b16 a[216:217],v40 offset:24576
buffer_load_dwordx4 v29,s[40:43],s61 offen offset:0  lds
s_add_u32 m0,s88,32640
ds_read_b64_tr_b16 a[218:219],v40 offset:25088
buffer_load_dwordx4 v28,s[40:43],s61 offen offset:128  lds
s_add_u32 m0,s88,33664
ds_read_b64_tr_b16 a[220:221],v41 offset:24576
buffer_load_dwordx4 v29,s[40:43],s61 offen offset:128  lds
s_add_u32 m0,s88,40704
ds_read_b64_tr_b16 a[222:223],v41 offset:25088
buffer_load_dwordx4 v28,s[40:43],s61 offen offset:256  lds
s_add_u32 m0,s88,41728
ds_read_b64_tr_b16 a[224:225],v42 offset:24576
buffer_load_dwordx4 v29,s[40:43],s61 offen offset:256  lds
ds_read_b64_tr_b16 a[226:227],v42 offset:25088
ds_read_b64_tr_b16 a[228:229],v40 offset:28672
ds_read_b64_tr_b16 a[230:231],v40 offset:29184
ds_read_b64_tr_b16 a[232:233],v41 offset:28672
ds_read_b64_tr_b16 a[234:235],v41 offset:29184
ds_read_b64_tr_b16 a[236:237],v42 offset:28672
ds_read_b64_tr_b16 a[238:239],v42 offset:29184
s_waitcnt lgkmcnt(12)
v_mfma_f32_16x16x32_bf16 v[16:19],a[192:195],v[84:87],v[16:19]
v_mfma_f32_16x16x32_bf16 v[20:23],a[196:199],v[84:87],v[20:23]
v_mfma_f32_16x16x32_bf16 v[24:27],a[200:203],v[84:87],v[24:27]
v_mfma_f32_16x16x32_bf16 v[16:19],a[204:207],v[84:87],v[16:19]
v_mfma_f32_16x16x32_bf16 v[20:23],a[208:211],v[84:87],v[20:23]
v_mfma_f32_16x16x32_bf16 v[24:27],a[212:215],v[84:87],v[24:27]
s_waitcnt vmcnt(18)
s_barrier
s_add_u32 m0,s88,49152
s_add_u32 s61,s82,6422528
buffer_load_dwordx4 v28,s[40:43],s61 offen offset:0  lds
s_add_u32 m0,s88,50176
ds_read_b64_tr_b16 a[240:241],v43 offset:0
buffer_load_dwordx4 v29,s[40:43],s61 offen offset:0  lds
s_add_u32 m0,s88,57216
ds_read_b64_tr_b16 a[242:243],v43 offset:512
buffer_load_dwordx4 v28,s[40:43],s61 offen offset:128  lds
s_add_u32 m0,s88,58240
ds_read_b64_tr_b16 a[244:245],v44 offset:0
buffer_load_dwordx4 v29,s[40:43],s61 offen offset:128  lds
s_add_u32 m0,s88,65280
ds_read_b64_tr_b16 a[246:247],v44 offset:512
buffer_load_dwordx4 v28,s[40:43],s61 offen offset:256  lds
s_add_u32 m0,s88,66304
ds_read_b64_tr_b16 a[248:249],v45 offset:0
buffer_load_dwordx4 v29,s[40:43],s61 offen offset:256  lds
ds_read_b64_tr_b16 a[250:251],v45 offset:512
ds_read_b64_tr_b16 a[252:253],v43 offset:4096
ds_read_b64_tr_b16 a[254:255],v43 offset:4608
ds_read_b64_tr_b16 v[128:129],v44 offset:4096
ds_read_b64_tr_b16 v[130:131],v44 offset:4608
ds_read_b64_tr_b16 v[132:133],v45 offset:4096
ds_read_b64_tr_b16 v[134:135],v45 offset:4608
s_waitcnt lgkmcnt(12)
v_mfma_f32_16x16x32_bf16 v[16:19],a[216:219],v[84:87],v[16:19]
v_mfma_f32_16x16x32_bf16 v[20:23],a[220:223],v[84:87],v[20:23]
v_mfma_f32_16x16x32_bf16 v[24:27],a[224:227],v[84:87],v[24:27]
v_mfma_f32_16x16x32_bf16 v[16:19],a[228:231],v[84:87],v[16:19]
v_mfma_f32_16x16x32_bf16 v[20:23],a[232:235],v[84:87],v[20:23]
v_mfma_f32_16x16x32_bf16 v[24:27],a[236:239],v[84:87],v[24:27]
s_waitcnt vmcnt(18)
s_barrier
s_add_u32 m0,s88,73728
s_add_u32 s61,s82,6881280
buffer_load_dwordx4 v28,s[40:43],s61 offen offset:0  lds
s_add_u32 m0,s88,74752
ds_read_b64_tr_b16 v[136:137],v43 offset:24576
buffer_load_dwordx4 v29,s[40:43],s61 offen offset:0  lds
s_add_u32 m0,s88,81792
ds_read_b64_tr_b16 v[138:139],v43 offset:25088
buffer_load_dwordx4 v28,s[40:43],s61 offen offset:128  lds
s_add_u32 m0,s88,82816
ds_read_b64_tr_b16 v[140:141],v44 offset:24576
buffer_load_dwordx4 v29,s[40:43],s61 offen offset:128  lds
s_add_u32 m0,s88,89856
ds_read_b64_tr_b16 v[142:143],v44 offset:25088
buffer_load_dwordx4 v28,s[40:43],s61 offen offset:256  lds
s_add_u32 m0,s88,90880
ds_read_b64_tr_b16 v[144:145],v45 offset:24576
buffer_load_dwordx4 v29,s[40:43],s61 offen offset:256  lds
ds_read_b64_tr_b16 v[146:147],v45 offset:25088
ds_read_b64_tr_b16 v[148:149],v43 offset:28672
ds_read_b64_tr_b16 v[150:151],v43 offset:29184
ds_read_b64_tr_b16 v[152:153],v44 offset:28672
ds_read_b64_tr_b16 v[154:155],v44 offset:29184
ds_read_b64_tr_b16 v[156:157],v45 offset:28672
ds_read_b64_tr_b16 v[158:159],v45 offset:29184
s_getpc_b64 s[46:47]
Lg3_cp_%=:
s_add_u32 s46,s46,Lg3_init_%=-Lg3_cp_%=
s_addc_u32 s47,s47,0
s_lshl_b32 s68,s54,13
v_lshl_add_u32 v68,v2,3,s68
v_min_u32 v69,Lg3_done_%=-Lg3_init_%=,v68
global_load_dword v72,v69,s[46:47]
s_waitcnt lgkmcnt(12)
v_mfma_f32_16x16x32_bf16 v[16:19],a[240:243],v[84:87],v[16:19]
v_mfma_f32_16x16x32_bf16 v[20:23],a[244:247],v[84:87],v[20:23]
v_mfma_f32_16x16x32_bf16 v[24:27],a[248:251],v[84:87],v[24:27]
v_mfma_f32_16x16x32_bf16 v[16:19],a[252:255],v[84:87],v[16:19]
v_mfma_f32_16x16x32_bf16 v[20:23],v[128:131],v[84:87],v[20:23]
v_mfma_f32_16x16x32_bf16 v[24:27],v[132:135],v[84:87],v[24:27]
s_waitcnt vmcnt(19)
s_barrier
ds_read_b64_tr_b16 v[160:161],v30 offset:0
ds_read_b64_tr_b16 v[162:163],v30 offset:512
ds_read_b64_tr_b16 v[164:165],v31 offset:0
ds_read_b64_tr_b16 v[166:167],v31 offset:512
ds_read_b64_tr_b16 v[168:169],v32 offset:0
ds_read_b64_tr_b16 v[170:171],v32 offset:512
ds_read_b64_tr_b16 v[172:173],v30 offset:4096
ds_read_b64_tr_b16 v[174:175],v30 offset:4608
ds_read_b64_tr_b16 v[176:177],v31 offset:4096
ds_read_b64_tr_b16 v[178:179],v31 offset:4608
ds_read_b64_tr_b16 v[180:181],v32 offset:4096
ds_read_b64_tr_b16 v[182:183],v32 offset:4608
s_waitcnt lgkmcnt(12)
v_mfma_f32_16x16x32_bf16 v[16:19],v[136:139],v[84:87],v[16:19]
v_mfma_f32_16x16x32_bf16 v[20:23],v[140:143],v[84:87],v[20:23]
v_mfma_f32_16x16x32_bf16 v[24:27],v[144:147],v[84:87],v[24:27]
v_mfma_f32_16x16x32_bf16 v[16:19],v[148:151],v[84:87],v[16:19]
v_mfma_f32_16x16x32_bf16 v[20:23],v[152:155],v[84:87],v[20:23]
v_mfma_f32_16x16x32_bf16 v[24:27],v[156:159],v[84:87],v[24:27]
s_waitcnt vmcnt(13)
s_barrier
ds_read_b64_tr_b16 v[184:185],v30 offset:24576
ds_read_b64_tr_b16 v[186:187],v30 offset:25088
ds_read_b64_tr_b16 v[188:189],v31 offset:24576
ds_read_b64_tr_b16 v[190:191],v31 offset:25088
ds_read_b64_tr_b16 v[192:193],v32 offset:24576
ds_read_b64_tr_b16 v[194:195],v32 offset:25088
ds_read_b64_tr_b16 v[196:197],v30 offset:28672
ds_read_b64_tr_b16 v[198:199],v30 offset:29184
ds_read_b64_tr_b16 v[200:201],v31 offset:28672
ds_read_b64_tr_b16 v[202:203],v31 offset:29184
ds_read_b64_tr_b16 v[204:205],v32 offset:28672
ds_read_b64_tr_b16 v[206:207],v32 offset:29184
s_waitcnt lgkmcnt(12)
v_mfma_f32_16x16x32_bf16 v[16:19],v[160:163],v[84:87],v[16:19]
v_mfma_f32_16x16x32_bf16 v[20:23],v[164:167],v[84:87],v[20:23]
v_mfma_f32_16x16x32_bf16 v[24:27],v[168:171],v[84:87],v[24:27]
v_mfma_f32_16x16x32_bf16 v[16:19],v[172:175],v[84:87],v[16:19]
v_mfma_f32_16x16x32_bf16 v[20:23],v[176:179],v[84:87],v[20:23]
v_mfma_f32_16x16x32_bf16 v[24:27],v[180:183],v[84:87],v[24:27]
s_waitcnt vmcnt(7)
s_barrier
ds_read_b64_tr_b16 v[208:209],v40 offset:0
ds_read_b64_tr_b16 v[210:211],v40 offset:512
ds_read_b64_tr_b16 v[212:213],v41 offset:0
ds_read_b64_tr_b16 v[214:215],v41 offset:512
ds_read_b64_tr_b16 v[216:217],v42 offset:0
ds_read_b64_tr_b16 v[218:219],v42 offset:512
ds_read_b64_tr_b16 v[220:221],v40 offset:4096
ds_read_b64_tr_b16 v[222:223],v40 offset:4608
ds_read_b64_tr_b16 v[224:225],v41 offset:4096
ds_read_b64_tr_b16 v[226:227],v41 offset:4608
ds_read_b64_tr_b16 v[228:229],v42 offset:4096
ds_read_b64_tr_b16 v[230:231],v42 offset:4608
s_waitcnt lgkmcnt(12)
v_mfma_f32_16x16x32_bf16 v[16:19],v[184:187],v[84:87],v[16:19]
v_mfma_f32_16x16x32_bf16 v[20:23],v[188:191],v[84:87],v[20:23]
v_mfma_f32_16x16x32_bf16 v[24:27],v[192:195],v[84:87],v[24:27]
v_mfma_f32_16x16x32_bf16 v[16:19],v[196:199],v[84:87],v[16:19]
v_mfma_f32_16x16x32_bf16 v[20:23],v[200:203],v[84:87],v[20:23]
v_mfma_f32_16x16x32_bf16 v[24:27],v[204:207],v[84:87],v[24:27]
s_waitcnt vmcnt(1)
s_barrier
ds_read_b64_tr_b16 v[232:233],v40 offset:24576
ds_read_b64_tr_b16 v[234:235],v40 offset:25088
ds_read_b64_tr_b16 v[236:237],v41 offset:24576
ds_read_b64_tr_b16 v[238:239],v41 offset:25088
ds_read_b64_tr_b16 v[240:241],v42 offset:24576
ds_read_b64_tr_b16 v[242:243],v42 offset:25088
ds_read_b64_tr_b16 v[244:245],v40 offset:28672
ds_read_b64_tr_b16 v[246:247],v40 offset:29184
ds_read_b64_tr_b16 v[248:249],v41 offset:28672
ds_read_b64_tr_b16 v[250:251],v41 offset:29184
ds_read_b64_tr_b16 v[252:253],v42 offset:28672
ds_read_b64_tr_b16 v[254:255],v42 offset:29184)"
R"(
s_waitcnt lgkmcnt(12)
v_mfma_f32_16x16x32_bf16 v[16:19],v[208:211],v[84:87],v[16:19]
v_mfma_f32_16x16x32_bf16 v[20:23],v[212:215],v[84:87],v[20:23]
v_mfma_f32_16x16x32_bf16 v[24:27],v[216:219],v[84:87],v[24:27]
v_mfma_f32_16x16x32_bf16 v[16:19],v[220:223],v[84:87],v[16:19]
v_mfma_f32_16x16x32_bf16 v[20:23],v[224:227],v[84:87],v[20:23]
v_mfma_f32_16x16x32_bf16 v[24:27],v[228:231],v[84:87],v[24:27]
s_waitcnt lgkmcnt(0)
v_mfma_f32_16x16x32_bf16 v[16:19],v[232:235],v[84:87],v[16:19]
v_mfma_f32_16x16x32_bf16 v[20:23],v[236:239],v[84:87],v[20:23]
v_mfma_f32_16x16x32_bf16 v[24:27],v[240:243],v[84:87],v[24:27]
v_mfma_f32_16x16x32_bf16 v[16:19],v[244:247],v[84:87],v[16:19]
v_mfma_f32_16x16x32_bf16 v[20:23],v[248:251],v[84:87],v[20:23]
v_mfma_f32_16x16x32_bf16 v[24:27],v[252:255],v[84:87],v[24:27]
s_waitcnt lgkmcnt(0)
s_barrier
v_readfirstlane_b32 s60,v5
s_nop 3
s_add_u32 s60,s60,1
s_cmp_eq_u32 s60,0
s_cselect_b32 s60,1,s60
Lg3_init_%=:
v_mov_b32 v5,s60
s_mov_b32 s59,536870912
s_add_u32 s57,s56,131072
s_mov_b32 s79,0
s_mov_b32 s75,0x3a800000
s_mov_b32 s76,0xc0135761
s_mov_b32 s77,0x3d372713
s_mov_b32 s44,s59
s_mov_b32 s45,s59
s_mov_b32 s46,s59
s_mov_b32 s47,s59
s_mov_b32 s48,s83
s_add_u32 s49,s48,s84
s_add_u32 s50,s49,s55
s_cmp_eq_u32 s50,s56
s_cselect_b32 s50,s57,s50
s_cmp_eq_u32 s50,s58
s_cselect_b32 s50,s59,s50
s_add_u32 s51,s50,s55
s_cmp_eq_u32 s51,s56
s_cselect_b32 s51,s57,s51
s_cmp_eq_u32 s51,s58
s_cselect_b32 s51,s59,s51
s_add_u32 s52,s51,s55
s_cmp_eq_u32 s52,s56
s_cselect_b32 s52,s57,s52
s_cmp_eq_u32 s52,s58
s_cselect_b32 s52,s59,s52
s_lshl_b32 s63,s54,12
s_add_u32 s63,s63,65536
s_add_u32 s64,s63,32768
s_lshl_b32 s88,s54,10
s_add_u32 s88,s88,131072
s_lshl_b32 s78,s54,11
s_add_u32 s78,s78,132608
v_add_u32 v1,16384,v0
v_mov_b32 v12,0x3727c5ac
s_getpc_b64 s[90:91]
Lg3_pc_%=:
s_mov_b32 s68,Lg3_d1w0_%=-Lg3_pc_%=
s_cmp_eq_u32 s54,1
s_cselect_b32 s68,Lg3_d1w1_%=-Lg3_pc_%=,s68
s_cmp_eq_u32 s54,2
s_cselect_b32 s68,Lg3_d1w2_%=-Lg3_pc_%=,s68
s_cmp_eq_u32 s54,3
s_cselect_b32 s68,Lg3_d1w3_%=-Lg3_pc_%=,s68
s_add_u32 s92,s90,s68
s_addc_u32 s93,s91,0
s_mov_b32 s68,Lg3_d0w0_%=-Lg3_pc_%=
s_cmp_eq_u32 s54,1
s_cselect_b32 s68,Lg3_d0w1_%=-Lg3_pc_%=,s68
s_cmp_eq_u32 s54,2
s_cselect_b32 s68,Lg3_d0w2_%=-Lg3_pc_%=,s68
s_cmp_eq_u32 s54,3
s_cselect_b32 s68,Lg3_d0w3_%=-Lg3_pc_%=,s68
s_add_u32 s90,s90,s68
s_addc_u32 s91,s91,0
s_mov_b32 s66,s59
s_mov_b32 s65,0
s_mov_b32 s62,s63
s_branch Lg3_slow0_%=
Lg3_d0w0_%=:
v_mfma_f32_16x16x32_bf16 v[28:31],a[0:3],v[52:55],0
s_mov_b32 m0,s62
ds_read_b128 v[72:75],v2 offset:5120
v_mul_f32 v116,s75,v112
v_mfma_f32_16x16x32_bf16 v[32:35],a[4:7],v[52:55],0
buffer_load_dwordx4 v0,s[36:39],s61 offen offset:0 sc1 lds
v_mfma_f32_16x16x32_bf16 v[36:39],a[8:11],v[52:55],0
s_mul_i32 s73,s47,7
v_fma_f32 v113,v113,s75,v12
v_fma_f32 v113,-v116,v116,v113
v_mfma_f32_16x16x32_bf16 v[28:31],a[12:15],v[56:59],v[28:31]
s_lshr_b32 s73,s73,1
ds_read_b128 v[76:79],v2 offset:6144
v_max_f32 v113,v113,v12
v_mfma_f32_16x16x32_bf16 v[32:35],a[16:19],v[56:59],v[32:35]
s_add_u32 s53,s52,s55
v_rsq_f32 v112,v113
v_mfma_f32_16x16x32_bf16 v[36:39],a[20:23],v[56:59],v[36:39]
s_waitcnt lgkmcnt(3)
buffer_load_dwordx4 v0,s[36:39],s61 offen offset:1024 sc1 lds
v_mfma_f32_16x16x32_bf16 v[28:31],a[24:27],v[60:63],v[28:31]
s_cmp_eq_u32 s53,s56
ds_read_b128 v[80:83],v2 offset:7168
v_mul_f32 v117,s76,v112
v_mfma_f32_16x16x32_bf16 v[32:35],a[28:31],v[60:63],v[32:35]
s_cselect_b32 s53,s57,s53
v_mul_f32 v118,v112,v112
v_mul_f32 v119,v113,v112
v_mfma_f32_16x16x32_bf16 v[36:39],a[32:35],v[60:63],v[36:39]
s_cmp_eq_u32 s53,s58
v_mul_f32 v118,s77,v118
v_mul_f32 v118,v118,v117
v_mfma_f32_16x16x32_bf16 v[28:31],a[36:39],v[64:67],v[28:31]
buffer_load_dwordx4 v0,s[36:39],s61 offen offset:2048 sc1 lds
v_mfma_f32_16x16x32_bf16 v[32:35],a[40:43],v[64:67],v[32:35]
s_cselect_b32 s53,s59,s53
ds_read_b128 v[52:55],v2 offset:8192
v_fma_f32 v40,-v116,v16,v40
v_mfma_f32_16x16x32_bf16 v[36:39],a[44:47],v[64:67],v[36:39]
s_waitcnt lgkmcnt(3)
v_fma_f32 v41,-v116,v17,v41
v_fma_f32 v42,-v116,v18,v42
v_mfma_f32_16x16x32_bf16 v[28:31],a[48:51],v[68:71],v[28:31]
s_cmp_ge_u32 s66,s59
ds_read_b128 v[56:59],v2 offset:9216
v_fma_f32 v43,-v116,v19,v43
v_mfma_f32_16x16x32_bf16 v[32:35],a[52:55],v[68:71],v[32:35]
buffer_load_dwordx4 v0,s[36:39],s61 offen offset:3072 sc1 lds
v_mfma_f32_16x16x32_bf16 v[36:39],a[56:59],v[68:71],v[36:39]
s_cselect_b32 s86,s49,s50
v_mul_f32 v84,v40,v40
v_mul_f32 v85,v41,v41
v_mfma_f32_16x16x32_bf16 v[28:31],a[60:63],v[72:75],v[28:31]
s_lshr_b32 s68,s86,5
ds_read_b128 v[60:63],v2 offset:10240
v_mul_f32 v86,v42,v42
v_mfma_f32_16x16x32_bf16 v[32:35],a[64:67],v[72:75],v[32:35]
s_add_u32 m0,s78,0
v_mul_f32 v87,v43,v43
v_fma_f32 v84,v118,v84,v117
v_mfma_f32_16x16x32_bf16 v[36:39],a[68:71],v[72:75],v[36:39]
s_waitcnt lgkmcnt(3)
v_mul_f32 v84,v40,v84
v_fma_f32 v85,v118,v85,v117
v_mfma_f32_16x16x32_bf16 v[28:31],a[72:75],v[76:79],v[28:31]
ds_read_b128 v[64:67],v2 offset:11264
v_mul_f32 v85,v41,v85
v_mfma_f32_16x16x32_bf16 v[32:35],a[76:79],v[76:79],v[32:35]
v_exp_f32 v84,v84
v_mfma_f32_16x16x32_bf16 v[36:39],a[80:83],v[76:79],v[36:39]
v_exp_f32 v85,v85
v_mfma_f32_16x16x32_bf16 v[28:31],a[84:87],v[80:83],v[28:31]
ds_read_b128 v[68:71],v2 offset:12288
v_fma_f32 v84,v84,v119,v119
v_mfma_f32_16x16x32_bf16 v[32:35],a[88:91],v[80:83],v[32:35]
v_rcp_f32 v84,v84
v_mfma_f32_16x16x32_bf16 v[36:39],a[92:95],v[80:83],v[36:39]
s_waitcnt lgkmcnt(3)
v_fma_f32 v85,v85,v119,v119
v_fma_f32 v86,v118,v86,v117
v_mfma_f32_16x16x32_bf16 v[28:31],a[96:99],v[52:55],v[28:31]
ds_read_b128 v[72:75],v2 offset:13312
v_mul_f32 v86,v42,v86
v_mfma_f32_16x16x32_bf16 v[32:35],a[100:103],v[52:55],v[32:35]
v_exp_f32 v86,v86
v_mfma_f32_16x16x32_bf16 v[36:39],a[104:107],v[52:55],v[36:39]
v_rcp_f32 v85,v85
v_mfma_f32_16x16x32_bf16 v[28:31],a[108:111],v[56:59],v[28:31]
ds_read_b128 v[76:79],v2 offset:14336
v_fma_f32 v86,v86,v119,v119
v_mfma_f32_16x16x32_bf16 v[32:35],a[112:115],v[56:59],v[32:35]
v_rcp_f32 v86,v86
v_mfma_f32_16x16x32_bf16 v[36:39],a[116:119],v[56:59],v[36:39]
s_waitcnt lgkmcnt(3)
v_fma_f32 v87,v118,v87,v117
v_mul_f32 v87,v43,v87
v_mfma_f32_16x16x32_bf16 v[28:31],a[120:123],v[60:63],v[28:31]
ds_read_b128 v[80:83],v2 offset:15360
v_mul_f32 v40,v40,v84
v_mfma_f32_16x16x32_bf16 v[32:35],a[124:127],v[60:63],v[32:35]
s_waitcnt vmcnt(6)
v_exp_f32 v87,v87
v_mfma_f32_16x16x32_bf16 v[36:39],a[128:131],v[60:63],v[36:39]
s_barrier
v_mfma_f32_16x16x32_bf16 v[28:31],a[132:135],v[64:67],v[28:31]
ds_read_b128 v[52:55],v2 offset:16384
v_fma_f32 v87,v87,v119,v119
v_mfma_f32_16x16x32_bf16 v[32:35],a[136:139],v[64:67],v[32:35]
v_rcp_f32 v87,v87
v_mfma_f32_16x16x32_bf16 v[36:39],a[140:143],v[64:67],v[36:39]
s_waitcnt lgkmcnt(3)
v_add_f32_dpp v104,v104,v104 quad_perm:[1,0,3,2] row_mask:0xf bank_mask:0xf
v_mul_f32 v41,v41,v85
v_mfma_f32_16x16x32_bf16 v[28:31],a[144:147],v[68:71],v[28:31]
ds_read_b128 v[56:59],v2 offset:17408
v_add_f32_dpp v104,v104,v104 quad_perm:[2,3,0,1] row_mask:0xf bank_mask:0xf)"
R"(
v_mfma_f32_16x16x32_bf16 v[32:35],a[148:151],v[68:71],v[32:35]
s_nop 0
v_add_f32_dpp v104,v104,v104 row_half_mirror row_mask:0xf bank_mask:0xf
v_mul_f32 v42,v42,v86
v_mfma_f32_16x16x32_bf16 v[36:39],a[152:155],v[68:71],v[36:39]
v_mul_f32 v43,v43,v87
v_cvt_pk_bf16_f32 v96,v40,v41
v_mfma_f32_16x16x32_bf16 v[28:31],a[156:159],v[72:75],v[28:31]
ds_read_b128 v[60:63],v2 offset:18432
v_cvt_pk_bf16_f32 v97,v42,v43
v_mfma_f32_16x16x32_bf16 v[32:35],a[160:163],v[72:75],v[32:35]
buffer_load_dword v6,s[36:39],s68 offen offset:0 sc1 lds
v_mfma_f32_16x16x32_bf16 v[36:39],a[164:167],v[72:75],v[36:39]
s_waitcnt lgkmcnt(3)
v_fma_f32 v44,-v116,v20,v44
v_fma_f32 v45,-v116,v21,v45
v_mfma_f32_16x16x32_bf16 v[28:31],a[168:171],v[76:79],v[28:31]
s_add_u32 m0,s62,16384
ds_read_b128 v[64:67],v2 offset:19456
v_fma_f32 v46,-v116,v22,v46
v_mfma_f32_16x16x32_bf16 v[32:35],a[172:175],v[76:79],v[32:35]
buffer_load_dwordx4 v1,s[36:39],s61 offen offset:0 sc1 lds
v_mfma_f32_16x16x32_bf16 v[36:39],a[176:179],v[76:79],v[36:39]
s_lshr_b32 s72,s61,5
v_fma_f32 v47,-v116,v23,v47
v_mul_f32 v88,v44,v44
v_mfma_f32_16x16x32_bf16 v[28:31],a[180:183],v[80:83],v[28:31]
s_mov_b32 s62,s63
ds_read_b128 v[68:71],v2 offset:20480
v_mul_f32 v89,v45,v45
v_mfma_f32_16x16x32_bf16 v[32:35],a[184:187],v[80:83],v[32:35]
ds_write_b32 v10,v104 offset:0
v_mfma_f32_16x16x32_bf16 v[36:39],a[188:191],v[80:83],v[36:39]
s_waitcnt lgkmcnt(4)
buffer_load_dwordx4 v1,s[36:39],s61 offen offset:1024 sc1 lds
v_mfma_f32_16x16x32_bf16 v[28:31],a[192:195],v[52:55],v[28:31]
ds_read_b128 v[72:75],v2 offset:21504
v_mul_f32 v90,v46,v46
v_mfma_f32_16x16x32_bf16 v[32:35],a[196:199],v[52:55],v[32:35]
v_mul_f32 v91,v47,v47
v_fma_f32 v88,v118,v88,v117
v_mfma_f32_16x16x32_bf16 v[36:39],a[200:203],v[52:55],v[36:39]
v_mul_f32 v88,v44,v88
v_fma_f32 v89,v118,v89,v117
v_mfma_f32_16x16x32_bf16 v[28:31],a[204:207],v[56:59],v[28:31]
buffer_load_dwordx4 v1,s[36:39],s61 offen offset:2048 sc1 lds
v_mfma_f32_16x16x32_bf16 v[32:35],a[208:211],v[56:59],v[32:35]
ds_read_b128 v[76:79],v2 offset:22528
v_mul_f32 v89,v45,v89
v_mfma_f32_16x16x32_bf16 v[36:39],a[212:215],v[56:59],v[36:39]
s_waitcnt lgkmcnt(4)
v_exp_f32 v88,v88
v_mfma_f32_16x16x32_bf16 v[28:31],a[216:219],v[60:63],v[28:31]
ds_read_b128 v[80:83],v2 offset:23552
v_fma_f32 v88,v88,v119,v119
v_mfma_f32_16x16x32_bf16 v[32:35],a[220:223],v[60:63],v[32:35]
buffer_load_dwordx4 v1,s[36:39],s61 offen offset:3072 sc1 lds
v_mfma_f32_16x16x32_bf16 v[36:39],a[224:227],v[60:63],v[36:39]
buffer_load_dword v108,v8,s[36:39],s72 offen offset:0 sc1
v_mfma_f32_16x16x32_bf16 v[28:31],a[228:231],v[64:67],v[28:31]
ds_read_b128 v[52:55],v2 offset:24576
v_fma_f32 v90,v118,v90,v117
v_mfma_f32_16x16x32_bf16 v[32:35],a[232:235],v[64:67],v[32:35]
v_exp_f32 v89,v89
v_mfma_f32_16x16x32_bf16 v[36:39],a[236:239],v[64:67],v[36:39]
s_waitcnt lgkmcnt(3)
v_rcp_f32 v88,v88
v_mfma_f32_16x16x32_bf16 v[28:31],a[240:243],v[68:71],v[28:31]
ds_read_b128 v[56:59],v2 offset:25600
v_mul_f32 v90,v46,v90
v_mfma_f32_16x16x32_bf16 v[32:35],a[244:247],v[68:71],v[32:35]
s_waitcnt vmcnt(10)
v_exp_f32 v90,v90
v_mfma_f32_16x16x32_bf16 v[36:39],a[248:251],v[68:71],v[36:39]
s_barrier
v_mfma_f32_16x16x32_bf16 v[28:31],a[252:255],v[72:75],v[28:31]
ds_read_b128 v[60:63],v2 offset:26624
v_fma_f32 v89,v89,v119,v119
v_mfma_f32_16x16x32_bf16 v[32:35],v[128:131],v[72:75],v[32:35]
v_rcp_f32 v89,v89
v_mfma_f32_16x16x32_bf16 v[36:39],v[132:135],v[72:75],v[36:39]
s_waitcnt lgkmcnt(3)
v_fma_f32 v90,v90,v119,v119
v_fma_f32 v91,v118,v91,v117
v_mfma_f32_16x16x32_bf16 v[28:31],v[136:139],v[76:79],v[28:31]
ds_read_b128 v[64:67],v2 offset:27648
v_mul_f32 v91,v47,v91
v_mfma_f32_16x16x32_bf16 v[32:35],v[140:143],v[76:79],v[32:35]
v_exp_f32 v91,v91
v_mfma_f32_16x16x32_bf16 v[36:39],v[144:147],v[76:79],v[36:39]
v_rcp_f32 v90,v90
v_mfma_f32_16x16x32_bf16 v[28:31],v[148:151],v[80:83],v[28:31]
ds_read_b128 v[68:71],v2 offset:28672
v_fma_f32 v91,v91,v119,v119
v_mfma_f32_16x16x32_bf16 v[32:35],v[152:155],v[80:83],v[32:35]
v_rcp_f32 v91,v91
v_mfma_f32_16x16x32_bf16 v[36:39],v[156:159],v[80:83],v[36:39]
s_waitcnt lgkmcnt(3)
v_mul_f32 v44,v44,v88
v_mul_f32 v45,v45,v89
v_mfma_f32_16x16x32_bf16 v[28:31],v[160:163],v[52:55],v[28:31]
ds_read_b128 v[72:75],v2 offset:29696
v_mul_f32 v46,v46,v90
v_mfma_f32_16x16x32_bf16 v[32:35],v[164:167],v[52:55],v[32:35]
v_mul_f32 v47,v47,v91
v_cvt_pk_bf16_f32 v98,v44,v45
v_mfma_f32_16x16x32_bf16 v[36:39],v[168:171],v[52:55],v[36:39]
v_cvt_pk_bf16_f32 v99,v46,v47
v_fma_f32 v48,-v116,v24,v48
v_mfma_f32_16x16x32_bf16 v[28:31],v[172:175],v[56:59],v[28:31]
ds_read_b128 v[76:79],v2 offset:30720
v_fma_f32 v49,-v116,v25,v49
v_mfma_f32_16x16x32_bf16 v[32:35],v[176:179],v[56:59],v[32:35]
v_fma_f32 v50,-v116,v26,v50
v_fma_f32 v51,-v116,v27,v51
v_mfma_f32_16x16x32_bf16 v[36:39],v[180:183],v[56:59],v[36:39]
s_waitcnt lgkmcnt(3)
buffer_store_dwordx4 v[96:99],v3,s[36:39],s73 offen offset:0 sc0 sc1
v_mul_f32 v92,v48,v48
v_mul_f32 v93,v49,v49
v_mul_f32 v94,v50,v50
v_mfma_f32_16x16x32_bf16 v[28:31],v[184:187],v[60:63],v[28:31]
ds_read_b128 v[80:83],v2 offset:31744
v_mul_f32 v95,v51,v51
v_mfma_f32_16x16x32_bf16 v[32:35],v[188:191],v[60:63],v[32:35]
s_waitcnt vmcnt(6)
v_fma_f32 v92,v118,v92,v117
v_mul_f32 v92,v48,v92
v_mfma_f32_16x16x32_bf16 v[36:39],v[192:195],v[60:63],v[36:39]
s_barrier
v_mfma_f32_16x16x32_bf16 v[28:31],v[196:199],v[64:67],v[28:31]
ds_read_b128 v[52:55],v2 offset:32768
ds_read_b32 v13,v7 offset:0
v_mfma_f32_16x16x32_bf16 v[32:35],v[200:203],v[64:67],v[32:35]
ds_read_b64 v[112:113],v11 offset:0
v_fma_f32 v93,v118,v93,v117
v_mfma_f32_16x16x32_bf16 v[36:39],v[204:207],v[64:67],v[36:39]
s_waitcnt lgkmcnt(5)
v_exp_f32 v92,v92
v_mfma_f32_16x16x32_bf16 v[28:31],v[208:211],v[68:71],v[28:31]
ds_read_b128 v[56:59],v2 offset:33792
v_mul_f32 v93,v49,v93
v_mfma_f32_16x16x32_bf16 v[32:35],v[212:215],v[68:71],v[32:35]
s_waitcnt lgkmcnt(2)
v_exp_f32 v93,v93
v_mfma_f32_16x16x32_bf16 v[36:39],v[216:219],v[68:71],v[36:39]
s_waitcnt lgkmcnt(1)
v_cmp_eq_u32 vcc,s60,v13
v_fma_f32 v92,v92,v119,v119
v_rcp_f32 v92,v92
v_mfma_f32_16x16x32_bf16 v[28:31],v[220:223],v[72:75],v[28:31]
s_cmp_eq_u32 vcc_lo,-1
ds_read_b128 v[60:63],v2 offset:34816
v_fma_f32 v93,v93,v119,v119
v_mfma_f32_16x16x32_bf16 v[32:35],v[224:227],v[72:75],v[32:35]
s_cselect_b32 s67,s86,s59
v_rcp_f32 v93,v93
v_mfma_f32_16x16x32_bf16 v[36:39],v[228:231],v[72:75],v[36:39]
v_fma_f32 v94,v118,v94,v117
v_mul_f32 v94,v50,v94
v_mfma_f32_16x16x32_bf16 v[28:31],v[232:235],v[76:79],v[28:31]
s_lshr_b32 s61,s67,0
ds_read_b128 v[64:67],v2 offset:35840
v_fma_f32 v95,v118,v95,v117
v_mfma_f32_16x16x32_bf16 v[32:35],v[236:239],v[76:79],v[32:35]
s_xor_b32 s63,s63,65536
v_mul_f32 v95,v51,v95
v_exp_f32 v94,v94
v_exp_f32 v95,v95
v_mfma_f32_16x16x32_bf16 v[36:39],v[240:243],v[76:79],v[36:39]
s_cmp_ge_u32 s66,s59
v_fma_f32 v94,v94,v119,v119
v_fma_f32 v95,v95,v119,v119
v_rcp_f32 v94,v94
v_rcp_f32 v95,v95
v_mfma_f32_16x16x32_bf16 v[28:31],v[244:247],v[80:83],v[28:31]
v_mul_f32 v48,v48,v92
v_mul_f32 v49,v49,v93
v_mul_f32 v50,v50,v94
v_mul_f32 v51,v51,v95
v_cvt_pk_bf16_f32 v100,v48,v49
v_cvt_pk_bf16_f32 v101,v50,v51
v_mfma_f32_16x16x32_bf16 v[32:35],v[248:251],v[80:83],v[32:35]
s_waitcnt lgkmcnt(2)
ds_read_b128 v[68:71],v2 offset:36864
buffer_store_dwordx2 v[100:101],v9,s[36:39],s73 offen offset:0 sc0 sc1
v_mfma_f32_16x16x32_bf16 v[36:39],v[252:255],v[80:83],v[36:39]
s_cbranch_scc1 Lg3_slow1_%=
Lg3_d1w0_%=:
v_mfma_f32_16x16x32_bf16 v[40:43],a[0:3],v[52:55],0
s_mov_b32 m0,s62
ds_read_b128 v[72:75],v2 offset:37888
v_mul_f32 v116,s75,v112
v_mfma_f32_16x16x32_bf16 v[44:47],a[4:7],v[52:55],0
buffer_load_dwordx4 v0,s[36:39],s61 offen offset:0 sc1 lds
v_mfma_f32_16x16x32_bf16 v[48:51],a[8:11],v[52:55],0
s_mul_i32 s73,s48,7
v_fma_f32 v113,v113,s75,v12
v_fma_f32 v113,-v116,v116,v113
v_mfma_f32_16x16x32_bf16 v[40:43],a[12:15],v[56:59],v[40:43])"
R"(
s_lshr_b32 s73,s73,1
ds_read_b128 v[76:79],v2 offset:38912
v_max_f32 v113,v113,v12
v_mfma_f32_16x16x32_bf16 v[44:47],a[16:19],v[56:59],v[44:47]
s_add_u32 s74,s53,s55
v_rsq_f32 v112,v113
v_mfma_f32_16x16x32_bf16 v[48:51],a[20:23],v[56:59],v[48:51]
s_waitcnt lgkmcnt(3)
buffer_load_dwordx4 v0,s[36:39],s61 offen offset:1024 sc1 lds
v_mfma_f32_16x16x32_bf16 v[40:43],a[24:27],v[60:63],v[40:43]
s_cmp_eq_u32 s74,s56
ds_read_b128 v[80:83],v2 offset:39936
v_mul_f32 v117,s76,v112
v_mfma_f32_16x16x32_bf16 v[44:47],a[28:31],v[60:63],v[44:47]
s_cselect_b32 s74,s57,s74
v_mul_f32 v118,v112,v112
v_mul_f32 v119,v113,v112
v_mfma_f32_16x16x32_bf16 v[48:51],a[32:35],v[60:63],v[48:51]
s_cmp_eq_u32 s74,s58
v_mul_f32 v118,s77,v118
v_mul_f32 v118,v118,v117
v_mfma_f32_16x16x32_bf16 v[40:43],a[36:39],v[64:67],v[40:43]
buffer_load_dwordx4 v0,s[36:39],s61 offen offset:2048 sc1 lds
v_mfma_f32_16x16x32_bf16 v[44:47],a[40:43],v[64:67],v[44:47]
s_cselect_b32 s74,s59,s74
ds_read_b128 v[52:55],v2 offset:40960
v_fma_f32 v28,-v116,v16,v28
v_mfma_f32_16x16x32_bf16 v[48:51],a[44:47],v[64:67],v[48:51]
s_waitcnt lgkmcnt(3)
v_fma_f32 v29,-v116,v17,v29
v_fma_f32 v30,-v116,v18,v30
v_mfma_f32_16x16x32_bf16 v[40:43],a[48:51],v[68:71],v[40:43]
s_cmp_ge_u32 s67,s59
ds_read_b128 v[56:59],v2 offset:41984
v_fma_f32 v31,-v116,v19,v31
v_mfma_f32_16x16x32_bf16 v[44:47],a[52:55],v[68:71],v[44:47]
buffer_load_dwordx4 v0,s[36:39],s61 offen offset:3072 sc1 lds
v_mfma_f32_16x16x32_bf16 v[48:51],a[56:59],v[68:71],v[48:51]
s_cselect_b32 s86,s50,s51
v_mul_f32 v84,v28,v28
v_mul_f32 v85,v29,v29
v_mfma_f32_16x16x32_bf16 v[40:43],a[60:63],v[72:75],v[40:43]
s_lshr_b32 s68,s86,5
ds_read_b128 v[60:63],v2 offset:43008
v_mul_f32 v86,v30,v30
v_mfma_f32_16x16x32_bf16 v[44:47],a[64:67],v[72:75],v[44:47]
s_add_u32 m0,s78,256
v_mul_f32 v87,v31,v31
v_fma_f32 v84,v118,v84,v117
v_mfma_f32_16x16x32_bf16 v[48:51],a[68:71],v[72:75],v[48:51]
s_waitcnt lgkmcnt(3)
v_mul_f32 v84,v28,v84
v_fma_f32 v85,v118,v85,v117
v_mfma_f32_16x16x32_bf16 v[40:43],a[72:75],v[76:79],v[40:43]
s_mov_b64 s[44:45],s[46:47]
ds_read_b128 v[64:67],v2 offset:44032
v_mul_f32 v85,v29,v85
v_mfma_f32_16x16x32_bf16 v[44:47],a[76:79],v[76:79],v[44:47]
s_mov_b64 s[46:47],s[48:49]
v_exp_f32 v84,v84
v_mfma_f32_16x16x32_bf16 v[48:51],a[80:83],v[76:79],v[48:51]
s_mov_b64 s[48:49],s[50:51]
v_exp_f32 v85,v85
v_mfma_f32_16x16x32_bf16 v[40:43],a[84:87],v[80:83],v[40:43]
s_mov_b64 s[50:51],s[52:53]
ds_read_b128 v[68:71],v2 offset:45056
v_fma_f32 v84,v84,v119,v119
v_mfma_f32_16x16x32_bf16 v[44:47],a[88:91],v[80:83],v[44:47]
s_mov_b32 s52,s74
v_rcp_f32 v84,v84
v_mfma_f32_16x16x32_bf16 v[48:51],a[92:95],v[80:83],v[48:51]
s_waitcnt lgkmcnt(3)
v_fma_f32 v85,v85,v119,v119
v_fma_f32 v86,v118,v86,v117
v_mfma_f32_16x16x32_bf16 v[40:43],a[96:99],v[52:55],v[40:43]
ds_read_b128 v[72:75],v2 offset:46080
v_mul_f32 v86,v30,v86
v_mfma_f32_16x16x32_bf16 v[44:47],a[100:103],v[52:55],v[44:47]
v_exp_f32 v86,v86
v_mfma_f32_16x16x32_bf16 v[48:51],a[104:107],v[52:55],v[48:51]
v_rcp_f32 v85,v85
v_mfma_f32_16x16x32_bf16 v[40:43],a[108:111],v[56:59],v[40:43]
ds_read_b128 v[76:79],v2 offset:47104
v_fma_f32 v86,v86,v119,v119
v_mfma_f32_16x16x32_bf16 v[44:47],a[112:115],v[56:59],v[44:47]
v_rcp_f32 v86,v86
v_mfma_f32_16x16x32_bf16 v[48:51],a[116:119],v[56:59],v[48:51]
s_waitcnt lgkmcnt(3)
v_fma_f32 v87,v118,v87,v117
v_mul_f32 v87,v31,v87
v_mfma_f32_16x16x32_bf16 v[40:43],a[120:123],v[60:63],v[40:43]
ds_read_b128 v[80:83],v2 offset:48128
v_mul_f32 v28,v28,v84
v_mfma_f32_16x16x32_bf16 v[44:47],a[124:127],v[60:63],v[44:47]
s_waitcnt vmcnt(6)
v_exp_f32 v87,v87
v_mfma_f32_16x16x32_bf16 v[48:51],a[128:131],v[60:63],v[48:51]
s_barrier
v_mfma_f32_16x16x32_bf16 v[40:43],a[132:135],v[64:67],v[40:43]
ds_read_b128 v[52:55],v2 offset:49152
v_fma_f32 v87,v87,v119,v119
v_mfma_f32_16x16x32_bf16 v[44:47],a[136:139],v[64:67],v[44:47]
v_rcp_f32 v87,v87
v_mfma_f32_16x16x32_bf16 v[48:51],a[140:143],v[64:67],v[48:51]
s_waitcnt lgkmcnt(3)
v_add_f32_dpp v108,v108,v108 quad_perm:[1,0,3,2] row_mask:0xf bank_mask:0xf
v_mul_f32 v29,v29,v85
v_mfma_f32_16x16x32_bf16 v[40:43],a[144:147],v[68:71],v[40:43]
ds_read_b128 v[56:59],v2 offset:50176
v_add_f32_dpp v108,v108,v108 quad_perm:[2,3,0,1] row_mask:0xf bank_mask:0xf
v_mfma_f32_16x16x32_bf16 v[44:47],a[148:151],v[68:71],v[44:47]
s_nop 0
v_add_f32_dpp v108,v108,v108 row_half_mirror row_mask:0xf bank_mask:0xf
v_mul_f32 v30,v30,v86
v_mfma_f32_16x16x32_bf16 v[48:51],a[152:155],v[68:71],v[48:51]
v_mul_f32 v31,v31,v87
v_cvt_pk_bf16_f32 v96,v28,v29
v_mfma_f32_16x16x32_bf16 v[40:43],a[156:159],v[72:75],v[40:43]
ds_read_b128 v[60:63],v2 offset:51200
v_cvt_pk_bf16_f32 v97,v30,v31
v_mfma_f32_16x16x32_bf16 v[44:47],a[160:163],v[72:75],v[44:47]
buffer_load_dword v6,s[36:39],s68 offen offset:0 sc1 lds
v_mfma_f32_16x16x32_bf16 v[48:51],a[164:167],v[72:75],v[48:51]
s_waitcnt lgkmcnt(3)
v_fma_f32 v32,-v116,v20,v32
v_fma_f32 v33,-v116,v21,v33
v_mfma_f32_16x16x32_bf16 v[40:43],a[168:171],v[76:79],v[40:43]
s_add_u32 m0,s62,16384
ds_read_b128 v[64:67],v2 offset:52224
v_fma_f32 v34,-v116,v22,v34
v_mfma_f32_16x16x32_bf16 v[44:47],a[172:175],v[76:79],v[44:47]
buffer_load_dwordx4 v1,s[36:39],s61 offen offset:0 sc1 lds
v_mfma_f32_16x16x32_bf16 v[48:51],a[176:179],v[76:79],v[48:51]
s_lshr_b32 s72,s61,5
v_fma_f32 v35,-v116,v23,v35
v_mul_f32 v88,v32,v32
v_mfma_f32_16x16x32_bf16 v[40:43],a[180:183],v[80:83],v[40:43]
s_mov_b32 s62,s64
ds_read_b128 v[68:71],v2 offset:53248
v_mul_f32 v89,v33,v33
v_mfma_f32_16x16x32_bf16 v[44:47],a[184:187],v[80:83],v[44:47]
ds_write_b32 v10,v108 offset:128
v_mfma_f32_16x16x32_bf16 v[48:51],a[188:191],v[80:83],v[48:51]
s_waitcnt lgkmcnt(4)
buffer_load_dwordx4 v1,s[36:39],s61 offen offset:1024 sc1 lds
v_mfma_f32_16x16x32_bf16 v[40:43],a[192:195],v[52:55],v[40:43]
ds_read_b128 v[72:75],v2 offset:54272
v_mul_f32 v90,v34,v34
v_mfma_f32_16x16x32_bf16 v[44:47],a[196:199],v[52:55],v[44:47]
v_mul_f32 v91,v35,v35
v_fma_f32 v88,v118,v88,v117
v_mfma_f32_16x16x32_bf16 v[48:51],a[200:203],v[52:55],v[48:51]
v_mul_f32 v88,v32,v88
v_fma_f32 v89,v118,v89,v117
v_mfma_f32_16x16x32_bf16 v[40:43],a[204:207],v[56:59],v[40:43]
buffer_load_dwordx4 v1,s[36:39],s61 offen offset:2048 sc1 lds
v_mfma_f32_16x16x32_bf16 v[44:47],a[208:211],v[56:59],v[44:47]
ds_read_b128 v[76:79],v2 offset:55296
v_mul_f32 v89,v33,v89
v_mfma_f32_16x16x32_bf16 v[48:51],a[212:215],v[56:59],v[48:51]
s_waitcnt lgkmcnt(4)
v_exp_f32 v88,v88
v_mfma_f32_16x16x32_bf16 v[40:43],a[216:219],v[60:63],v[40:43]
ds_read_b128 v[80:83],v2 offset:56320
v_fma_f32 v88,v88,v119,v119
v_mfma_f32_16x16x32_bf16 v[44:47],a[220:223],v[60:63],v[44:47]
buffer_load_dwordx4 v1,s[36:39],s61 offen offset:3072 sc1 lds
v_mfma_f32_16x16x32_bf16 v[48:51],a[224:227],v[60:63],v[48:51]
buffer_load_dword v104,v8,s[36:39],s72 offen offset:0 sc1
v_mfma_f32_16x16x32_bf16 v[40:43],a[228:231],v[64:67],v[40:43]
ds_read_b128 v[52:55],v2 offset:57344
v_fma_f32 v90,v118,v90,v117
v_mfma_f32_16x16x32_bf16 v[44:47],a[232:235],v[64:67],v[44:47]
v_exp_f32 v89,v89
v_mfma_f32_16x16x32_bf16 v[48:51],a[236:239],v[64:67],v[48:51]
s_waitcnt lgkmcnt(3)
v_rcp_f32 v88,v88
v_mfma_f32_16x16x32_bf16 v[40:43],a[240:243],v[68:71],v[40:43]
ds_read_b128 v[56:59],v2 offset:58368
v_mul_f32 v90,v34,v90
v_mfma_f32_16x16x32_bf16 v[44:47],a[244:247],v[68:71],v[44:47]
s_waitcnt vmcnt(10)
v_exp_f32 v90,v90
v_mfma_f32_16x16x32_bf16 v[48:51],a[248:251],v[68:71],v[48:51]
s_barrier
v_mfma_f32_16x16x32_bf16 v[40:43],a[252:255],v[72:75],v[40:43]
ds_read_b128 v[60:63],v2 offset:59392
v_fma_f32 v89,v89,v119,v119
v_mfma_f32_16x16x32_bf16 v[44:47],v[128:131],v[72:75],v[44:47]
v_rcp_f32 v89,v89
v_mfma_f32_16x16x32_bf16 v[48:51],v[132:135],v[72:75],v[48:51]
s_waitcnt lgkmcnt(3)
v_fma_f32 v90,v90,v119,v119
v_fma_f32 v91,v118,v91,v117
v_mfma_f32_16x16x32_bf16 v[40:43],v[136:139],v[76:79],v[40:43]
ds_read_b128 v[64:67],v2 offset:60416
v_mul_f32 v91,v35,v91
v_mfma_f32_16x16x32_bf16 v[44:47],v[140:143],v[76:79],v[44:47]
v_exp_f32 v91,v91
v_mfma_f32_16x16x32_bf16 v[48:51],v[144:147],v[76:79],v[48:51])"
R"(
v_rcp_f32 v90,v90
v_mfma_f32_16x16x32_bf16 v[40:43],v[148:151],v[80:83],v[40:43]
ds_read_b128 v[68:71],v2 offset:61440
v_fma_f32 v91,v91,v119,v119
v_mfma_f32_16x16x32_bf16 v[44:47],v[152:155],v[80:83],v[44:47]
v_rcp_f32 v91,v91
v_mfma_f32_16x16x32_bf16 v[48:51],v[156:159],v[80:83],v[48:51]
s_waitcnt lgkmcnt(3)
v_mul_f32 v32,v32,v88
v_mul_f32 v33,v33,v89
v_mfma_f32_16x16x32_bf16 v[40:43],v[160:163],v[52:55],v[40:43]
ds_read_b128 v[72:75],v2 offset:62464
v_mul_f32 v34,v34,v90
v_mfma_f32_16x16x32_bf16 v[44:47],v[164:167],v[52:55],v[44:47]
v_mul_f32 v35,v35,v91
v_cvt_pk_bf16_f32 v98,v32,v33
v_mfma_f32_16x16x32_bf16 v[48:51],v[168:171],v[52:55],v[48:51]
v_cvt_pk_bf16_f32 v99,v34,v35
v_fma_f32 v36,-v116,v24,v36
v_mfma_f32_16x16x32_bf16 v[40:43],v[172:175],v[56:59],v[40:43]
ds_read_b128 v[76:79],v2 offset:63488
v_fma_f32 v37,-v116,v25,v37
v_mfma_f32_16x16x32_bf16 v[44:47],v[176:179],v[56:59],v[44:47]
v_fma_f32 v38,-v116,v26,v38
v_fma_f32 v39,-v116,v27,v39
v_mfma_f32_16x16x32_bf16 v[48:51],v[180:183],v[56:59],v[48:51]
s_waitcnt lgkmcnt(3)
buffer_store_dwordx4 v[96:99],v3,s[36:39],s73 offen offset:0 sc0 sc1
v_mul_f32 v92,v36,v36
v_mul_f32 v93,v37,v37
v_mul_f32 v94,v38,v38
v_mfma_f32_16x16x32_bf16 v[40:43],v[184:187],v[60:63],v[40:43]
ds_read_b128 v[80:83],v2 offset:64512
v_xor_b32 v2,65536,v2
v_mfma_f32_16x16x32_bf16 v[44:47],v[188:191],v[60:63],v[44:47]
s_waitcnt vmcnt(6)
v_mul_f32 v95,v39,v39
v_fma_f32 v92,v118,v92,v117
v_mfma_f32_16x16x32_bf16 v[48:51],v[192:195],v[60:63],v[48:51]
s_barrier
v_mfma_f32_16x16x32_bf16 v[40:43],v[196:199],v[64:67],v[40:43]
ds_read_b128 v[52:55],v2 offset:0
ds_read_b32 v13,v7 offset:256
v_mfma_f32_16x16x32_bf16 v[44:47],v[200:203],v[64:67],v[44:47]
ds_read_b64 v[112:113],v11 offset:128
v_mul_f32 v92,v36,v92
v_mfma_f32_16x16x32_bf16 v[48:51],v[204:207],v[64:67],v[48:51]
s_waitcnt lgkmcnt(5)
v_exp_f32 v92,v92
v_mfma_f32_16x16x32_bf16 v[40:43],v[208:211],v[68:71],v[40:43]
ds_read_b128 v[56:59],v2 offset:1024
v_fma_f32 v92,v92,v119,v119
v_mfma_f32_16x16x32_bf16 v[44:47],v[212:215],v[68:71],v[44:47]
s_waitcnt lgkmcnt(2)
v_rcp_f32 v92,v92
v_mfma_f32_16x16x32_bf16 v[48:51],v[216:219],v[68:71],v[48:51]
s_waitcnt lgkmcnt(1)
v_cmp_eq_u32 vcc,s60,v13
v_fma_f32 v93,v118,v93,v117
v_mul_f32 v93,v37,v93
v_fma_f32 v94,v118,v94,v117
v_mfma_f32_16x16x32_bf16 v[40:43],v[220:223],v[72:75],v[40:43]
s_cmp_eq_u32 vcc_lo,-1
ds_read_b128 v[60:63],v2 offset:2048
v_mul_f32 v94,v38,v94
v_mfma_f32_16x16x32_bf16 v[44:47],v[224:227],v[72:75],v[44:47]
s_cselect_b32 s66,s86,s59
v_exp_f32 v93,v93
v_mfma_f32_16x16x32_bf16 v[48:51],v[228:231],v[72:75],v[48:51]
v_exp_f32 v94,v94
v_mfma_f32_16x16x32_bf16 v[40:43],v[232:235],v[76:79],v[40:43]
s_lshr_b32 s61,s66,0
ds_read_b128 v[64:67],v2 offset:3072
v_fma_f32 v93,v93,v119,v119
v_mfma_f32_16x16x32_bf16 v[44:47],v[236:239],v[76:79],v[44:47]
s_xor_b32 s64,s64,65536
v_fma_f32 v95,v118,v95,v117
v_mul_f32 v95,v39,v95
v_exp_f32 v95,v95
v_mfma_f32_16x16x32_bf16 v[48:51],v[240:243],v[76:79],v[48:51]
s_cmp_lt_u32 s67,s59
v_fma_f32 v94,v94,v119,v119
v_fma_f32 v95,v95,v119,v119
v_rcp_f32 v93,v93
v_rcp_f32 v94,v94
v_rcp_f32 v95,v95
v_mfma_f32_16x16x32_bf16 v[40:43],v[244:247],v[80:83],v[40:43]
v_mul_f32 v36,v36,v92
v_mul_f32 v37,v37,v93
v_mul_f32 v38,v38,v94
v_mul_f32 v39,v39,v95
v_cvt_pk_bf16_f32 v100,v36,v37
v_cvt_pk_bf16_f32 v101,v38,v39
v_mfma_f32_16x16x32_bf16 v[44:47],v[248:251],v[80:83],v[44:47]
s_waitcnt lgkmcnt(2)
ds_read_b128 v[68:71],v2 offset:4096
buffer_store_dwordx2 v[100:101],v9,s[36:39],s73 offen offset:0 sc0 sc1
v_mfma_f32_16x16x32_bf16 v[48:51],v[252:255],v[80:83],v[48:51]
s_cbranch_scc1 Lg3_d0w0_%=
s_branch Lg3_slow0_%=
Lg3_d0w1_%=:
v_mfma_f32_16x16x32_bf16 v[28:31],a[0:3],v[52:55],0
s_mov_b32 m0,s62
ds_read_b128 v[72:75],v2 offset:5120
v_mul_f32 v116,s75,v112
v_mfma_f32_16x16x32_bf16 v[32:35],a[4:7],v[52:55],0
s_mul_i32 s73,s47,7
v_fma_f32 v113,v113,s75,v12
v_fma_f32 v113,-v116,v116,v113
v_mfma_f32_16x16x32_bf16 v[36:39],a[8:11],v[52:55],0
buffer_load_dwordx4 v0,s[36:39],s61 offen offset:0 sc1 lds
v_mfma_f32_16x16x32_bf16 v[28:31],a[12:15],v[56:59],v[28:31]
s_lshr_b32 s73,s73,1
ds_read_b128 v[76:79],v2 offset:6144
v_max_f32 v113,v113,v12
v_mfma_f32_16x16x32_bf16 v[32:35],a[16:19],v[56:59],v[32:35]
s_add_u32 s53,s52,s55
v_rsq_f32 v112,v113
v_mfma_f32_16x16x32_bf16 v[36:39],a[20:23],v[56:59],v[36:39]
s_waitcnt lgkmcnt(3)
v_mul_f32 v117,s76,v112
v_mul_f32 v118,v112,v112
v_mfma_f32_16x16x32_bf16 v[28:31],a[24:27],v[60:63],v[28:31]
buffer_load_dwordx4 v0,s[36:39],s61 offen offset:1024 sc1 lds
v_mfma_f32_16x16x32_bf16 v[32:35],a[28:31],v[60:63],v[32:35]
s_cmp_eq_u32 s53,s56
ds_read_b128 v[80:83],v2 offset:7168
v_mul_f32 v119,v113,v112
v_mfma_f32_16x16x32_bf16 v[36:39],a[32:35],v[60:63],v[36:39]
s_cselect_b32 s53,s57,s53
v_mul_f32 v118,s77,v118
v_mul_f32 v118,v118,v117
v_mfma_f32_16x16x32_bf16 v[28:31],a[36:39],v[64:67],v[28:31]
s_cmp_eq_u32 s53,s58
ds_read_b128 v[52:55],v2 offset:8192
v_fma_f32 v40,-v116,v16,v40
v_mfma_f32_16x16x32_bf16 v[32:35],a[40:43],v[64:67],v[32:35]
buffer_load_dwordx4 v0,s[36:39],s61 offen offset:2048 sc1 lds
v_mfma_f32_16x16x32_bf16 v[36:39],a[44:47],v[64:67],v[36:39]
s_waitcnt lgkmcnt(3)
v_fma_f32 v41,-v116,v17,v41
v_fma_f32 v42,-v116,v18,v42
v_mfma_f32_16x16x32_bf16 v[28:31],a[48:51],v[68:71],v[28:31]
s_cselect_b32 s53,s59,s53
ds_read_b128 v[56:59],v2 offset:9216
v_fma_f32 v43,-v116,v19,v43
v_mfma_f32_16x16x32_bf16 v[32:35],a[52:55],v[68:71],v[32:35]
s_cmp_ge_u32 s66,s59
v_mul_f32 v84,v40,v40
v_mul_f32 v85,v41,v41
v_mfma_f32_16x16x32_bf16 v[36:39],a[56:59],v[68:71],v[36:39]
buffer_load_dwordx4 v0,s[36:39],s61 offen offset:3072 sc1 lds
v_mfma_f32_16x16x32_bf16 v[28:31],a[60:63],v[72:75],v[28:31]
s_cselect_b32 s86,s49,s50
ds_read_b128 v[60:63],v2 offset:10240
v_mul_f32 v86,v42,v42
v_mfma_f32_16x16x32_bf16 v[32:35],a[64:67],v[72:75],v[32:35]
s_add_u32 m0,s62,16384
v_mul_f32 v87,v43,v43
v_fma_f32 v84,v118,v84,v117
v_mfma_f32_16x16x32_bf16 v[36:39],a[68:71],v[72:75],v[36:39]
s_waitcnt lgkmcnt(3)
v_mul_f32 v84,v40,v84
v_fma_f32 v85,v118,v85,v117
v_mfma_f32_16x16x32_bf16 v[28:31],a[72:75],v[76:79],v[28:31]
s_lshr_b32 s72,s61,5
ds_read_b128 v[64:67],v2 offset:11264
v_mul_f32 v85,v41,v85
v_mfma_f32_16x16x32_bf16 v[32:35],a[76:79],v[76:79],v[32:35]
s_mov_b32 s62,s63
v_exp_f32 v84,v84
v_mfma_f32_16x16x32_bf16 v[36:39],a[80:83],v[76:79],v[36:39]
v_exp_f32 v85,v85
v_mfma_f32_16x16x32_bf16 v[28:31],a[84:87],v[80:83],v[28:31]
ds_read_b128 v[68:71],v2 offset:12288
v_fma_f32 v84,v84,v119,v119
v_mfma_f32_16x16x32_bf16 v[32:35],a[88:91],v[80:83],v[32:35]
v_rcp_f32 v84,v84
v_mfma_f32_16x16x32_bf16 v[36:39],a[92:95],v[80:83],v[36:39]
s_waitcnt lgkmcnt(3)
v_fma_f32 v85,v85,v119,v119
v_fma_f32 v86,v118,v86,v117
v_mfma_f32_16x16x32_bf16 v[28:31],a[96:99],v[52:55],v[28:31]
ds_read_b128 v[72:75],v2 offset:13312
v_mul_f32 v86,v42,v86
v_mfma_f32_16x16x32_bf16 v[32:35],a[100:103],v[52:55],v[32:35]
v_exp_f32 v86,v86
v_mfma_f32_16x16x32_bf16 v[36:39],a[104:107],v[52:55],v[36:39]
v_rcp_f32 v85,v85
v_mfma_f32_16x16x32_bf16 v[28:31],a[108:111],v[56:59],v[28:31]
ds_read_b128 v[76:79],v2 offset:14336
v_fma_f32 v86,v86,v119,v119
v_mfma_f32_16x16x32_bf16 v[32:35],a[112:115],v[56:59],v[32:35]
v_rcp_f32 v86,v86
v_mfma_f32_16x16x32_bf16 v[36:39],a[116:119],v[56:59],v[36:39]
s_waitcnt lgkmcnt(3)
v_fma_f32 v87,v118,v87,v117
v_mul_f32 v87,v43,v87
v_mfma_f32_16x16x32_bf16 v[28:31],a[120:123],v[60:63],v[28:31]
ds_read_b128 v[80:83],v2 offset:15360)"
R"(
v_mul_f32 v40,v40,v84
v_mfma_f32_16x16x32_bf16 v[32:35],a[124:127],v[60:63],v[32:35]
s_waitcnt vmcnt(6)
v_exp_f32 v87,v87
v_mfma_f32_16x16x32_bf16 v[36:39],a[128:131],v[60:63],v[36:39]
s_barrier
v_mfma_f32_16x16x32_bf16 v[28:31],a[132:135],v[64:67],v[28:31]
ds_read_b128 v[52:55],v2 offset:16384
v_fma_f32 v87,v87,v119,v119
v_mfma_f32_16x16x32_bf16 v[32:35],a[136:139],v[64:67],v[32:35]
v_rcp_f32 v87,v87
v_mfma_f32_16x16x32_bf16 v[36:39],a[140:143],v[64:67],v[36:39]
s_waitcnt lgkmcnt(3)
v_add_f32_dpp v104,v104,v104 quad_perm:[1,0,3,2] row_mask:0xf bank_mask:0xf
v_mul_f32 v41,v41,v85
v_mfma_f32_16x16x32_bf16 v[28:31],a[144:147],v[68:71],v[28:31]
ds_read_b128 v[56:59],v2 offset:17408
v_add_f32_dpp v104,v104,v104 quad_perm:[2,3,0,1] row_mask:0xf bank_mask:0xf
v_mfma_f32_16x16x32_bf16 v[32:35],a[148:151],v[68:71],v[32:35]
s_nop 0
v_add_f32_dpp v104,v104,v104 row_half_mirror row_mask:0xf bank_mask:0xf
v_mul_f32 v42,v42,v86
v_mfma_f32_16x16x32_bf16 v[36:39],a[152:155],v[68:71],v[36:39]
v_mul_f32 v43,v43,v87
v_cvt_pk_bf16_f32 v96,v40,v41
v_mfma_f32_16x16x32_bf16 v[28:31],a[156:159],v[72:75],v[28:31]
ds_read_b128 v[60:63],v2 offset:18432
v_cvt_pk_bf16_f32 v97,v42,v43
v_mfma_f32_16x16x32_bf16 v[32:35],a[160:163],v[72:75],v[32:35]
v_fma_f32 v44,-v116,v20,v44
v_fma_f32 v45,-v116,v21,v45
v_mfma_f32_16x16x32_bf16 v[36:39],a[164:167],v[72:75],v[36:39]
s_waitcnt lgkmcnt(3)
v_fma_f32 v46,-v116,v22,v46
v_fma_f32 v47,-v116,v23,v47
v_mfma_f32_16x16x32_bf16 v[28:31],a[168:171],v[76:79],v[28:31]
ds_read_b128 v[64:67],v2 offset:19456
v_mul_f32 v88,v44,v44
v_mfma_f32_16x16x32_bf16 v[32:35],a[172:175],v[76:79],v[32:35]
v_mul_f32 v89,v45,v45
v_mul_f32 v90,v46,v46
v_mfma_f32_16x16x32_bf16 v[36:39],a[176:179],v[76:79],v[36:39]
buffer_load_dwordx4 v1,s[36:39],s61 offen offset:0 sc1 lds
v_mfma_f32_16x16x32_bf16 v[28:31],a[180:183],v[80:83],v[28:31]
ds_read_b128 v[68:71],v2 offset:20480
v_mul_f32 v91,v47,v47
v_mfma_f32_16x16x32_bf16 v[32:35],a[184:187],v[80:83],v[32:35]
ds_write_b32 v10,v104 offset:0
v_mfma_f32_16x16x32_bf16 v[36:39],a[188:191],v[80:83],v[36:39]
s_waitcnt lgkmcnt(4)
v_fma_f32 v88,v118,v88,v117
v_mul_f32 v88,v44,v88
v_mfma_f32_16x16x32_bf16 v[28:31],a[192:195],v[52:55],v[28:31]
buffer_load_dwordx4 v1,s[36:39],s61 offen offset:1024 sc1 lds
v_mfma_f32_16x16x32_bf16 v[32:35],a[196:199],v[52:55],v[32:35]
ds_read_b128 v[72:75],v2 offset:21504
v_fma_f32 v89,v118,v89,v117
v_mfma_f32_16x16x32_bf16 v[36:39],a[200:203],v[52:55],v[36:39]
v_exp_f32 v88,v88
v_mfma_f32_16x16x32_bf16 v[28:31],a[204:207],v[56:59],v[28:31]
ds_read_b128 v[76:79],v2 offset:22528
v_mul_f32 v89,v45,v89
v_mfma_f32_16x16x32_bf16 v[32:35],a[208:211],v[56:59],v[32:35]
buffer_load_dwordx4 v1,s[36:39],s61 offen offset:2048 sc1 lds
v_mfma_f32_16x16x32_bf16 v[36:39],a[212:215],v[56:59],v[36:39]
s_waitcnt lgkmcnt(4)
v_exp_f32 v89,v89
v_mfma_f32_16x16x32_bf16 v[28:31],a[216:219],v[60:63],v[28:31]
ds_read_b128 v[80:83],v2 offset:23552
v_fma_f32 v88,v88,v119,v119
v_mfma_f32_16x16x32_bf16 v[32:35],a[220:223],v[60:63],v[32:35]
v_rcp_f32 v88,v88
v_mfma_f32_16x16x32_bf16 v[36:39],a[224:227],v[60:63],v[36:39]
buffer_load_dwordx4 v1,s[36:39],s61 offen offset:3072 sc1 lds
v_mfma_f32_16x16x32_bf16 v[28:31],a[228:231],v[64:67],v[28:31]
buffer_load_dword v108,v8,s[36:39],s72 offen offset:0 sc1
v_mfma_f32_16x16x32_bf16 v[32:35],a[232:235],v[64:67],v[32:35]
ds_read_b128 v[52:55],v2 offset:24576
v_fma_f32 v89,v89,v119,v119
v_mfma_f32_16x16x32_bf16 v[36:39],a[236:239],v[64:67],v[36:39]
s_waitcnt lgkmcnt(3)
v_rcp_f32 v89,v89
v_mfma_f32_16x16x32_bf16 v[28:31],a[240:243],v[68:71],v[28:31]
ds_read_b128 v[56:59],v2 offset:25600
v_fma_f32 v90,v118,v90,v117
v_mfma_f32_16x16x32_bf16 v[32:35],a[244:247],v[68:71],v[32:35]
s_waitcnt vmcnt(9)
v_mul_f32 v90,v46,v90
v_fma_f32 v91,v118,v91,v117
v_mfma_f32_16x16x32_bf16 v[36:39],a[248:251],v[68:71],v[36:39]
s_barrier
v_mfma_f32_16x16x32_bf16 v[28:31],a[252:255],v[72:75],v[28:31]
ds_read_b128 v[60:63],v2 offset:26624
v_mul_f32 v91,v47,v91
v_mfma_f32_16x16x32_bf16 v[32:35],v[128:131],v[72:75],v[32:35]
v_exp_f32 v90,v90
v_mfma_f32_16x16x32_bf16 v[36:39],v[132:135],v[72:75],v[36:39]
s_waitcnt lgkmcnt(3)
v_exp_f32 v91,v91
v_mfma_f32_16x16x32_bf16 v[28:31],v[136:139],v[76:79],v[28:31]
ds_read_b128 v[64:67],v2 offset:27648
v_fma_f32 v90,v90,v119,v119
v_mfma_f32_16x16x32_bf16 v[32:35],v[140:143],v[76:79],v[32:35]
v_rcp_f32 v90,v90
v_mfma_f32_16x16x32_bf16 v[36:39],v[144:147],v[76:79],v[36:39]
v_fma_f32 v91,v91,v119,v119
v_mul_f32 v44,v44,v88
v_mfma_f32_16x16x32_bf16 v[28:31],v[148:151],v[80:83],v[28:31]
ds_read_b128 v[68:71],v2 offset:28672
v_mul_f32 v45,v45,v89
v_mfma_f32_16x16x32_bf16 v[32:35],v[152:155],v[80:83],v[32:35]
v_rcp_f32 v91,v91
v_mfma_f32_16x16x32_bf16 v[36:39],v[156:159],v[80:83],v[36:39]
s_waitcnt lgkmcnt(3)
v_mul_f32 v46,v46,v90
v_mul_f32 v47,v47,v91
v_mfma_f32_16x16x32_bf16 v[28:31],v[160:163],v[52:55],v[28:31]
ds_read_b128 v[72:75],v2 offset:29696
v_cvt_pk_bf16_f32 v98,v44,v45
v_mfma_f32_16x16x32_bf16 v[32:35],v[164:167],v[52:55],v[32:35]
v_cvt_pk_bf16_f32 v99,v46,v47
v_fma_f32 v48,-v116,v24,v48
v_mfma_f32_16x16x32_bf16 v[36:39],v[168:171],v[52:55],v[36:39]
v_fma_f32 v49,-v116,v25,v49
v_fma_f32 v50,-v116,v26,v50
v_mfma_f32_16x16x32_bf16 v[28:31],v[172:175],v[56:59],v[28:31]
buffer_store_dwordx4 v[96:99],v3,s[36:39],s73 offen offset:0 sc0 sc1
ds_read_b128 v[76:79],v2 offset:30720
v_fma_f32 v51,-v116,v27,v51
v_mul_f32 v92,v48,v48
v_mfma_f32_16x16x32_bf16 v[32:35],v[176:179],v[56:59],v[32:35]
v_mul_f32 v93,v49,v49
v_mul_f32 v94,v50,v50
v_mfma_f32_16x16x32_bf16 v[36:39],v[180:183],v[56:59],v[36:39]
s_waitcnt lgkmcnt(3)
v_mul_f32 v95,v51,v51
v_fma_f32 v92,v118,v92,v117
v_mfma_f32_16x16x32_bf16 v[28:31],v[184:187],v[60:63],v[28:31]
ds_read_b128 v[80:83],v2 offset:31744
v_mul_f32 v92,v48,v92
v_mfma_f32_16x16x32_bf16 v[32:35],v[188:191],v[60:63],v[32:35]
s_waitcnt vmcnt(6)
v_exp_f32 v92,v92
v_mfma_f32_16x16x32_bf16 v[36:39],v[192:195],v[60:63],v[36:39]
s_barrier
v_mfma_f32_16x16x32_bf16 v[28:31],v[196:199],v[64:67],v[28:31]
ds_read_b128 v[52:55],v2 offset:32768
ds_read_b32 v13,v7 offset:0
v_mfma_f32_16x16x32_bf16 v[32:35],v[200:203],v[64:67],v[32:35]
ds_read_b64 v[112:113],v11 offset:0
v_fma_f32 v92,v92,v119,v119
v_mfma_f32_16x16x32_bf16 v[36:39],v[204:207],v[64:67],v[36:39]
s_waitcnt lgkmcnt(5)
v_rcp_f32 v92,v92
v_mfma_f32_16x16x32_bf16 v[28:31],v[208:211],v[68:71],v[28:31]
ds_read_b128 v[56:59],v2 offset:33792
v_fma_f32 v93,v118,v93,v117
v_mfma_f32_16x16x32_bf16 v[32:35],v[212:215],v[68:71],v[32:35]
s_waitcnt lgkmcnt(2)
v_mul_f32 v93,v49,v93
v_fma_f32 v94,v118,v94,v117
v_mfma_f32_16x16x32_bf16 v[36:39],v[216:219],v[68:71],v[36:39]
s_waitcnt lgkmcnt(1)
v_cmp_eq_u32 vcc,s60,v13
v_exp_f32 v93,v93
v_mul_f32 v94,v50,v94
v_mfma_f32_16x16x32_bf16 v[28:31],v[220:223],v[72:75],v[28:31]
s_cmp_eq_u32 vcc_lo,-1
ds_read_b128 v[60:63],v2 offset:34816
v_fma_f32 v93,v93,v119,v119
v_mfma_f32_16x16x32_bf16 v[32:35],v[224:227],v[72:75],v[32:35]
s_cselect_b32 s67,s86,s59
v_exp_f32 v94,v94
v_mfma_f32_16x16x32_bf16 v[36:39],v[228:231],v[72:75],v[36:39]
v_fma_f32 v95,v118,v95,v117
v_mul_f32 v95,v51,v95
v_exp_f32 v95,v95
v_mfma_f32_16x16x32_bf16 v[28:31],v[232:235],v[76:79],v[28:31]
s_lshr_b32 s61,s67,0
v_fma_f32 v94,v94,v119,v119
v_fma_f32 v95,v95,v119,v119
v_rcp_f32 v93,v93
v_rcp_f32 v94,v94
v_rcp_f32 v95,v95
v_mfma_f32_16x16x32_bf16 v[32:35],v[236:239],v[76:79],v[32:35]
s_xor_b32 s63,s63,65536
v_mul_f32 v48,v48,v92
v_mul_f32 v49,v49,v93
v_mul_f32 v50,v50,v94
v_mul_f32 v51,v51,v95
v_cvt_pk_bf16_f32 v100,v48,v49
v_cvt_pk_bf16_f32 v101,v50,v51
v_mfma_f32_16x16x32_bf16 v[36:39],v[240:243],v[76:79],v[36:39]
s_cmp_ge_u32 s66,s59
ds_read_b128 v[64:67],v2 offset:35840
buffer_store_dwordx2 v[100:101],v9,s[36:39],s73 offen offset:0 sc0 sc1
v_mfma_f32_16x16x32_bf16 v[28:31],v[244:247],v[80:83],v[28:31]
ds_read_b128 v[68:71],v2 offset:36864
v_mfma_f32_16x16x32_bf16 v[32:35],v[248:251],v[80:83],v[32:35]
s_waitcnt lgkmcnt(3))"
R"(
v_mfma_f32_16x16x32_bf16 v[36:39],v[252:255],v[80:83],v[36:39]
s_cbranch_scc1 Lg3_slow1_%=
Lg3_d1w1_%=:
v_mfma_f32_16x16x32_bf16 v[40:43],a[0:3],v[52:55],0
s_mov_b32 m0,s62
ds_read_b128 v[72:75],v2 offset:37888
v_mul_f32 v116,s75,v112
v_mfma_f32_16x16x32_bf16 v[44:47],a[4:7],v[52:55],0
s_mul_i32 s73,s48,7
v_fma_f32 v113,v113,s75,v12
v_fma_f32 v113,-v116,v116,v113
v_mfma_f32_16x16x32_bf16 v[48:51],a[8:11],v[52:55],0
buffer_load_dwordx4 v0,s[36:39],s61 offen offset:0 sc1 lds
v_mfma_f32_16x16x32_bf16 v[40:43],a[12:15],v[56:59],v[40:43]
s_lshr_b32 s73,s73,1
ds_read_b128 v[76:79],v2 offset:38912
v_max_f32 v113,v113,v12
v_mfma_f32_16x16x32_bf16 v[44:47],a[16:19],v[56:59],v[44:47]
s_add_u32 s74,s53,s55
v_rsq_f32 v112,v113
v_mfma_f32_16x16x32_bf16 v[48:51],a[20:23],v[56:59],v[48:51]
s_waitcnt lgkmcnt(3)
v_mul_f32 v117,s76,v112
v_mul_f32 v118,v112,v112
v_mfma_f32_16x16x32_bf16 v[40:43],a[24:27],v[60:63],v[40:43]
buffer_load_dwordx4 v0,s[36:39],s61 offen offset:1024 sc1 lds
v_mfma_f32_16x16x32_bf16 v[44:47],a[28:31],v[60:63],v[44:47]
s_cmp_eq_u32 s74,s56
ds_read_b128 v[80:83],v2 offset:39936
v_mul_f32 v119,v113,v112
v_mfma_f32_16x16x32_bf16 v[48:51],a[32:35],v[60:63],v[48:51]
s_cselect_b32 s74,s57,s74
v_mul_f32 v118,s77,v118
v_mul_f32 v118,v118,v117
v_mfma_f32_16x16x32_bf16 v[40:43],a[36:39],v[64:67],v[40:43]
s_cmp_eq_u32 s74,s58
ds_read_b128 v[52:55],v2 offset:40960
v_fma_f32 v28,-v116,v16,v28
v_mfma_f32_16x16x32_bf16 v[44:47],a[40:43],v[64:67],v[44:47]
buffer_load_dwordx4 v0,s[36:39],s61 offen offset:2048 sc1 lds
v_mfma_f32_16x16x32_bf16 v[48:51],a[44:47],v[64:67],v[48:51]
s_waitcnt lgkmcnt(3)
v_fma_f32 v29,-v116,v17,v29
v_fma_f32 v30,-v116,v18,v30
v_mfma_f32_16x16x32_bf16 v[40:43],a[48:51],v[68:71],v[40:43]
s_cselect_b32 s74,s59,s74
ds_read_b128 v[56:59],v2 offset:41984
v_fma_f32 v31,-v116,v19,v31
v_mfma_f32_16x16x32_bf16 v[44:47],a[52:55],v[68:71],v[44:47]
s_cmp_ge_u32 s67,s59
v_mul_f32 v84,v28,v28
v_mul_f32 v85,v29,v29
v_mfma_f32_16x16x32_bf16 v[48:51],a[56:59],v[68:71],v[48:51]
buffer_load_dwordx4 v0,s[36:39],s61 offen offset:3072 sc1 lds
v_mfma_f32_16x16x32_bf16 v[40:43],a[60:63],v[72:75],v[40:43]
s_cselect_b32 s86,s50,s51
ds_read_b128 v[60:63],v2 offset:43008
v_mul_f32 v86,v30,v30
v_mfma_f32_16x16x32_bf16 v[44:47],a[64:67],v[72:75],v[44:47]
s_add_u32 m0,s62,16384
v_mul_f32 v87,v31,v31
v_fma_f32 v84,v118,v84,v117
v_mfma_f32_16x16x32_bf16 v[48:51],a[68:71],v[72:75],v[48:51]
s_waitcnt lgkmcnt(3)
v_mul_f32 v84,v28,v84
v_fma_f32 v85,v118,v85,v117
v_mfma_f32_16x16x32_bf16 v[40:43],a[72:75],v[76:79],v[40:43]
s_lshr_b32 s72,s61,5
ds_read_b128 v[64:67],v2 offset:44032
v_mul_f32 v85,v29,v85
v_mfma_f32_16x16x32_bf16 v[44:47],a[76:79],v[76:79],v[44:47]
s_mov_b32 s62,s64
v_exp_f32 v84,v84
v_mfma_f32_16x16x32_bf16 v[48:51],a[80:83],v[76:79],v[48:51]
s_mov_b64 s[44:45],s[46:47]
v_exp_f32 v85,v85
v_mfma_f32_16x16x32_bf16 v[40:43],a[84:87],v[80:83],v[40:43]
s_mov_b64 s[46:47],s[48:49]
ds_read_b128 v[68:71],v2 offset:45056
v_fma_f32 v84,v84,v119,v119
v_mfma_f32_16x16x32_bf16 v[44:47],a[88:91],v[80:83],v[44:47]
s_mov_b64 s[48:49],s[50:51]
v_rcp_f32 v84,v84
v_mfma_f32_16x16x32_bf16 v[48:51],a[92:95],v[80:83],v[48:51]
s_waitcnt lgkmcnt(3)
v_fma_f32 v85,v85,v119,v119
v_fma_f32 v86,v118,v86,v117
v_mfma_f32_16x16x32_bf16 v[40:43],a[96:99],v[52:55],v[40:43]
s_mov_b64 s[50:51],s[52:53]
ds_read_b128 v[72:75],v2 offset:46080
v_mul_f32 v86,v30,v86
v_mfma_f32_16x16x32_bf16 v[44:47],a[100:103],v[52:55],v[44:47]
s_mov_b32 s52,s74
v_exp_f32 v86,v86
v_mfma_f32_16x16x32_bf16 v[48:51],a[104:107],v[52:55],v[48:51]
v_rcp_f32 v85,v85
v_mfma_f32_16x16x32_bf16 v[40:43],a[108:111],v[56:59],v[40:43]
ds_read_b128 v[76:79],v2 offset:47104
v_fma_f32 v86,v86,v119,v119
v_mfma_f32_16x16x32_bf16 v[44:47],a[112:115],v[56:59],v[44:47]
v_rcp_f32 v86,v86
v_mfma_f32_16x16x32_bf16 v[48:51],a[116:119],v[56:59],v[48:51]
s_waitcnt lgkmcnt(3)
v_fma_f32 v87,v118,v87,v117
v_mul_f32 v87,v31,v87
v_mfma_f32_16x16x32_bf16 v[40:43],a[120:123],v[60:63],v[40:43]
ds_read_b128 v[80:83],v2 offset:48128
v_mul_f32 v28,v28,v84
v_mfma_f32_16x16x32_bf16 v[44:47],a[124:127],v[60:63],v[44:47]
s_waitcnt vmcnt(6)
v_exp_f32 v87,v87
v_mfma_f32_16x16x32_bf16 v[48:51],a[128:131],v[60:63],v[48:51]
s_barrier
v_mfma_f32_16x16x32_bf16 v[40:43],a[132:135],v[64:67],v[40:43]
ds_read_b128 v[52:55],v2 offset:49152
v_fma_f32 v87,v87,v119,v119
v_mfma_f32_16x16x32_bf16 v[44:47],a[136:139],v[64:67],v[44:47]
v_rcp_f32 v87,v87
v_mfma_f32_16x16x32_bf16 v[48:51],a[140:143],v[64:67],v[48:51]
s_waitcnt lgkmcnt(3)
v_add_f32_dpp v108,v108,v108 quad_perm:[1,0,3,2] row_mask:0xf bank_mask:0xf
v_mul_f32 v29,v29,v85
v_mfma_f32_16x16x32_bf16 v[40:43],a[144:147],v[68:71],v[40:43]
ds_read_b128 v[56:59],v2 offset:50176
v_add_f32_dpp v108,v108,v108 quad_perm:[2,3,0,1] row_mask:0xf bank_mask:0xf
v_mfma_f32_16x16x32_bf16 v[44:47],a[148:151],v[68:71],v[44:47]
s_nop 0
v_add_f32_dpp v108,v108,v108 row_half_mirror row_mask:0xf bank_mask:0xf
v_mul_f32 v30,v30,v86
v_mfma_f32_16x16x32_bf16 v[48:51],a[152:155],v[68:71],v[48:51]
v_mul_f32 v31,v31,v87
v_cvt_pk_bf16_f32 v96,v28,v29
v_mfma_f32_16x16x32_bf16 v[40:43],a[156:159],v[72:75],v[40:43]
ds_read_b128 v[60:63],v2 offset:51200
v_cvt_pk_bf16_f32 v97,v30,v31
v_mfma_f32_16x16x32_bf16 v[44:47],a[160:163],v[72:75],v[44:47]
v_fma_f32 v32,-v116,v20,v32
v_fma_f32 v33,-v116,v21,v33
v_mfma_f32_16x16x32_bf16 v[48:51],a[164:167],v[72:75],v[48:51]
s_waitcnt lgkmcnt(3)
v_fma_f32 v34,-v116,v22,v34
v_fma_f32 v35,-v116,v23,v35
v_mfma_f32_16x16x32_bf16 v[40:43],a[168:171],v[76:79],v[40:43]
ds_read_b128 v[64:67],v2 offset:52224
v_mul_f32 v88,v32,v32
v_mfma_f32_16x16x32_bf16 v[44:47],a[172:175],v[76:79],v[44:47]
v_mul_f32 v89,v33,v33
v_mul_f32 v90,v34,v34
v_mfma_f32_16x16x32_bf16 v[48:51],a[176:179],v[76:79],v[48:51]
buffer_load_dwordx4 v1,s[36:39],s61 offen offset:0 sc1 lds
v_mfma_f32_16x16x32_bf16 v[40:43],a[180:183],v[80:83],v[40:43]
ds_read_b128 v[68:71],v2 offset:53248
v_mul_f32 v91,v35,v35
v_mfma_f32_16x16x32_bf16 v[44:47],a[184:187],v[80:83],v[44:47]
ds_write_b32 v10,v108 offset:128
v_mfma_f32_16x16x32_bf16 v[48:51],a[188:191],v[80:83],v[48:51]
s_waitcnt lgkmcnt(4)
v_fma_f32 v88,v118,v88,v117
v_mul_f32 v88,v32,v88
v_mfma_f32_16x16x32_bf16 v[40:43],a[192:195],v[52:55],v[40:43]
buffer_load_dwordx4 v1,s[36:39],s61 offen offset:1024 sc1 lds
v_mfma_f32_16x16x32_bf16 v[44:47],a[196:199],v[52:55],v[44:47]
ds_read_b128 v[72:75],v2 offset:54272
v_fma_f32 v89,v118,v89,v117
v_mfma_f32_16x16x32_bf16 v[48:51],a[200:203],v[52:55],v[48:51]
v_exp_f32 v88,v88
v_mfma_f32_16x16x32_bf16 v[40:43],a[204:207],v[56:59],v[40:43]
ds_read_b128 v[76:79],v2 offset:55296
v_mul_f32 v89,v33,v89
v_mfma_f32_16x16x32_bf16 v[44:47],a[208:211],v[56:59],v[44:47]
buffer_load_dwordx4 v1,s[36:39],s61 offen offset:2048 sc1 lds
v_mfma_f32_16x16x32_bf16 v[48:51],a[212:215],v[56:59],v[48:51]
s_waitcnt lgkmcnt(4)
v_exp_f32 v89,v89
v_mfma_f32_16x16x32_bf16 v[40:43],a[216:219],v[60:63],v[40:43]
ds_read_b128 v[80:83],v2 offset:56320
v_fma_f32 v88,v88,v119,v119
v_mfma_f32_16x16x32_bf16 v[44:47],a[220:223],v[60:63],v[44:47]
v_rcp_f32 v88,v88
v_mfma_f32_16x16x32_bf16 v[48:51],a[224:227],v[60:63],v[48:51]
buffer_load_dwordx4 v1,s[36:39],s61 offen offset:3072 sc1 lds
v_mfma_f32_16x16x32_bf16 v[40:43],a[228:231],v[64:67],v[40:43]
buffer_load_dword v104,v8,s[36:39],s72 offen offset:0 sc1
v_mfma_f32_16x16x32_bf16 v[44:47],a[232:235],v[64:67],v[44:47]
ds_read_b128 v[52:55],v2 offset:57344
v_fma_f32 v89,v89,v119,v119
v_mfma_f32_16x16x32_bf16 v[48:51],a[236:239],v[64:67],v[48:51]
s_waitcnt lgkmcnt(3)
v_rcp_f32 v89,v89
v_mfma_f32_16x16x32_bf16 v[40:43],a[240:243],v[68:71],v[40:43]
ds_read_b128 v[56:59],v2 offset:58368
v_fma_f32 v90,v118,v90,v117
v_mfma_f32_16x16x32_bf16 v[44:47],a[244:247],v[68:71],v[44:47]
s_waitcnt vmcnt(9)
v_mul_f32 v90,v34,v90
v_fma_f32 v91,v118,v91,v117
v_mfma_f32_16x16x32_bf16 v[48:51],a[248:251],v[68:71],v[48:51]
s_barrier
v_mfma_f32_16x16x32_bf16 v[40:43],a[252:255],v[72:75],v[40:43]
ds_read_b128 v[60:63],v2 offset:59392)"
R"(
v_mul_f32 v91,v35,v91
v_mfma_f32_16x16x32_bf16 v[44:47],v[128:131],v[72:75],v[44:47]
v_exp_f32 v90,v90
v_mfma_f32_16x16x32_bf16 v[48:51],v[132:135],v[72:75],v[48:51]
s_waitcnt lgkmcnt(3)
v_exp_f32 v91,v91
v_mfma_f32_16x16x32_bf16 v[40:43],v[136:139],v[76:79],v[40:43]
ds_read_b128 v[64:67],v2 offset:60416
v_fma_f32 v90,v90,v119,v119
v_mfma_f32_16x16x32_bf16 v[44:47],v[140:143],v[76:79],v[44:47]
v_rcp_f32 v90,v90
v_mfma_f32_16x16x32_bf16 v[48:51],v[144:147],v[76:79],v[48:51]
v_fma_f32 v91,v91,v119,v119
v_mul_f32 v32,v32,v88
v_mfma_f32_16x16x32_bf16 v[40:43],v[148:151],v[80:83],v[40:43]
ds_read_b128 v[68:71],v2 offset:61440
v_mul_f32 v33,v33,v89
v_mfma_f32_16x16x32_bf16 v[44:47],v[152:155],v[80:83],v[44:47]
v_rcp_f32 v91,v91
v_mfma_f32_16x16x32_bf16 v[48:51],v[156:159],v[80:83],v[48:51]
s_waitcnt lgkmcnt(3)
v_mul_f32 v34,v34,v90
v_mul_f32 v35,v35,v91
v_mfma_f32_16x16x32_bf16 v[40:43],v[160:163],v[52:55],v[40:43]
ds_read_b128 v[72:75],v2 offset:62464
v_cvt_pk_bf16_f32 v98,v32,v33
v_mfma_f32_16x16x32_bf16 v[44:47],v[164:167],v[52:55],v[44:47]
v_cvt_pk_bf16_f32 v99,v34,v35
v_fma_f32 v36,-v116,v24,v36
v_mfma_f32_16x16x32_bf16 v[48:51],v[168:171],v[52:55],v[48:51]
v_fma_f32 v37,-v116,v25,v37
v_fma_f32 v38,-v116,v26,v38
v_mfma_f32_16x16x32_bf16 v[40:43],v[172:175],v[56:59],v[40:43]
buffer_store_dwordx4 v[96:99],v3,s[36:39],s73 offen offset:0 sc0 sc1
ds_read_b128 v[76:79],v2 offset:63488
v_fma_f32 v39,-v116,v27,v39
v_mul_f32 v92,v36,v36
v_mfma_f32_16x16x32_bf16 v[44:47],v[176:179],v[56:59],v[44:47]
v_mul_f32 v93,v37,v37
v_mul_f32 v94,v38,v38
v_mfma_f32_16x16x32_bf16 v[48:51],v[180:183],v[56:59],v[48:51]
s_waitcnt lgkmcnt(3)
v_mul_f32 v95,v39,v39
v_fma_f32 v92,v118,v92,v117
v_mfma_f32_16x16x32_bf16 v[40:43],v[184:187],v[60:63],v[40:43]
ds_read_b128 v[80:83],v2 offset:64512
v_mul_f32 v92,v36,v92
v_mfma_f32_16x16x32_bf16 v[44:47],v[188:191],v[60:63],v[44:47]
s_waitcnt vmcnt(6)
v_xor_b32 v2,65536,v2
v_fma_f32 v93,v118,v93,v117
v_mfma_f32_16x16x32_bf16 v[48:51],v[192:195],v[60:63],v[48:51]
s_barrier
v_mfma_f32_16x16x32_bf16 v[40:43],v[196:199],v[64:67],v[40:43]
ds_read_b128 v[52:55],v2 offset:0
ds_read_b32 v13,v7 offset:256
v_mfma_f32_16x16x32_bf16 v[44:47],v[200:203],v[64:67],v[44:47]
ds_read_b64 v[112:113],v11 offset:128
v_mul_f32 v93,v37,v93
v_mfma_f32_16x16x32_bf16 v[48:51],v[204:207],v[64:67],v[48:51]
s_waitcnt lgkmcnt(5)
v_exp_f32 v92,v92
v_mfma_f32_16x16x32_bf16 v[40:43],v[208:211],v[68:71],v[40:43]
ds_read_b128 v[56:59],v2 offset:1024
v_fma_f32 v92,v92,v119,v119
v_mfma_f32_16x16x32_bf16 v[44:47],v[212:215],v[68:71],v[44:47]
s_waitcnt lgkmcnt(2)
v_exp_f32 v93,v93
v_mfma_f32_16x16x32_bf16 v[48:51],v[216:219],v[68:71],v[48:51]
s_waitcnt lgkmcnt(1)
v_cmp_eq_u32 vcc,s60,v13
v_rcp_f32 v92,v92
v_fma_f32 v93,v93,v119,v119
v_mfma_f32_16x16x32_bf16 v[40:43],v[220:223],v[72:75],v[40:43]
s_cmp_eq_u32 vcc_lo,-1
ds_read_b128 v[60:63],v2 offset:2048
v_fma_f32 v94,v118,v94,v117
v_mfma_f32_16x16x32_bf16 v[44:47],v[224:227],v[72:75],v[44:47]
s_cselect_b32 s66,s86,s59
v_rcp_f32 v93,v93
v_mfma_f32_16x16x32_bf16 v[48:51],v[228:231],v[72:75],v[48:51]
v_fma_f32 v95,v118,v95,v117
v_mul_f32 v94,v38,v94
v_mul_f32 v95,v39,v95
v_exp_f32 v94,v94
v_exp_f32 v95,v95
v_mfma_f32_16x16x32_bf16 v[40:43],v[232:235],v[76:79],v[40:43]
s_lshr_b32 s61,s66,0
v_fma_f32 v94,v94,v119,v119
v_fma_f32 v95,v95,v119,v119
v_rcp_f32 v94,v94
v_rcp_f32 v95,v95
v_mfma_f32_16x16x32_bf16 v[44:47],v[236:239],v[76:79],v[44:47]
s_xor_b32 s64,s64,65536
v_mul_f32 v36,v36,v92
v_mul_f32 v37,v37,v93
v_mul_f32 v38,v38,v94
v_mul_f32 v39,v39,v95
v_cvt_pk_bf16_f32 v100,v36,v37
v_cvt_pk_bf16_f32 v101,v38,v39
v_mfma_f32_16x16x32_bf16 v[48:51],v[240:243],v[76:79],v[48:51]
s_cmp_lt_u32 s67,s59
ds_read_b128 v[64:67],v2 offset:3072
buffer_store_dwordx2 v[100:101],v9,s[36:39],s73 offen offset:0 sc0 sc1
v_mfma_f32_16x16x32_bf16 v[40:43],v[244:247],v[80:83],v[40:43]
ds_read_b128 v[68:71],v2 offset:4096
v_mfma_f32_16x16x32_bf16 v[44:47],v[248:251],v[80:83],v[44:47]
s_waitcnt lgkmcnt(3)
v_mfma_f32_16x16x32_bf16 v[48:51],v[252:255],v[80:83],v[48:51]
s_cbranch_scc1 Lg3_d0w1_%=
s_branch Lg3_slow0_%=
Lg3_d0w2_%=:
v_mfma_f32_16x16x32_bf16 v[28:31],a[0:3],v[52:55],0
s_mov_b32 m0,s62
ds_read_b128 v[72:75],v2 offset:5120
v_mul_f32 v116,s75,v112
v_mfma_f32_16x16x32_bf16 v[32:35],a[4:7],v[52:55],0
s_mul_i32 s73,s47,7
v_fma_f32 v113,v113,s75,v12
v_fma_f32 v113,-v116,v116,v113
v_mfma_f32_16x16x32_bf16 v[36:39],a[8:11],v[52:55],0
s_lshr_b32 s73,s73,1
v_max_f32 v113,v113,v12
v_rsq_f32 v112,v113
v_mfma_f32_16x16x32_bf16 v[28:31],a[12:15],v[56:59],v[28:31]
buffer_load_dwordx4 v0,s[36:39],s61 offen offset:0 sc1 lds
v_mfma_f32_16x16x32_bf16 v[32:35],a[16:19],v[56:59],v[32:35]
s_add_u32 s53,s52,s55
ds_read_b128 v[76:79],v2 offset:6144
v_mul_f32 v117,s76,v112
v_mfma_f32_16x16x32_bf16 v[36:39],a[20:23],v[56:59],v[36:39]
s_waitcnt lgkmcnt(3)
v_mul_f32 v118,v112,v112
v_mul_f32 v119,v113,v112
v_mfma_f32_16x16x32_bf16 v[28:31],a[24:27],v[60:63],v[28:31]
s_cmp_eq_u32 s53,s56
ds_read_b128 v[80:83],v2 offset:7168
v_mul_f32 v118,s77,v118
v_mfma_f32_16x16x32_bf16 v[32:35],a[28:31],v[60:63],v[32:35]
buffer_load_dwordx4 v0,s[36:39],s61 offen offset:1024 sc1 lds
v_mfma_f32_16x16x32_bf16 v[36:39],a[32:35],v[60:63],v[36:39]
s_cselect_b32 s53,s57,s53
v_mul_f32 v118,v118,v117
v_fma_f32 v40,-v116,v16,v40
v_mfma_f32_16x16x32_bf16 v[28:31],a[36:39],v[64:67],v[28:31]
s_cmp_eq_u32 s53,s58
ds_read_b128 v[52:55],v2 offset:8192
v_fma_f32 v41,-v116,v17,v41
v_mfma_f32_16x16x32_bf16 v[32:35],a[40:43],v[64:67],v[32:35]
s_cselect_b32 s53,s59,s53
v_fma_f32 v42,-v116,v18,v42
v_fma_f32 v43,-v116,v19,v43
v_mfma_f32_16x16x32_bf16 v[36:39],a[44:47],v[64:67],v[36:39]
s_waitcnt lgkmcnt(3)
buffer_load_dwordx4 v0,s[36:39],s61 offen offset:2048 sc1 lds
v_mfma_f32_16x16x32_bf16 v[28:31],a[48:51],v[68:71],v[28:31]
s_cmp_ge_u32 s66,s59
ds_read_b128 v[56:59],v2 offset:9216
v_mul_f32 v84,v40,v40
v_mfma_f32_16x16x32_bf16 v[32:35],a[52:55],v[68:71],v[32:35]
s_cselect_b32 s86,s49,s50
v_mul_f32 v85,v41,v41
v_mul_f32 v86,v42,v42
v_mfma_f32_16x16x32_bf16 v[36:39],a[56:59],v[68:71],v[36:39]
v_mul_f32 v87,v43,v43
v_fma_f32 v84,v118,v84,v117
v_mul_f32 v84,v40,v84
v_mfma_f32_16x16x32_bf16 v[28:31],a[60:63],v[72:75],v[28:31]
buffer_load_dwordx4 v0,s[36:39],s61 offen offset:3072 sc1 lds
v_mfma_f32_16x16x32_bf16 v[32:35],a[64:67],v[72:75],v[32:35]
s_add_u32 m0,s62,16384
ds_read_b128 v[60:63],v2 offset:10240
v_fma_f32 v85,v118,v85,v117
v_mfma_f32_16x16x32_bf16 v[36:39],a[68:71],v[72:75],v[36:39]
s_waitcnt lgkmcnt(3)
v_exp_f32 v84,v84
v_mfma_f32_16x16x32_bf16 v[28:31],a[72:75],v[76:79],v[28:31]
s_lshr_b32 s72,s61,5
ds_read_b128 v[64:67],v2 offset:11264
v_mul_f32 v85,v41,v85
v_mfma_f32_16x16x32_bf16 v[32:35],a[76:79],v[76:79],v[32:35]
s_mov_b32 s62,s63
v_exp_f32 v85,v85
v_mfma_f32_16x16x32_bf16 v[36:39],a[80:83],v[76:79],v[36:39]
v_fma_f32 v84,v84,v119,v119
v_fma_f32 v85,v85,v119,v119
v_mfma_f32_16x16x32_bf16 v[28:31],a[84:87],v[80:83],v[28:31]
ds_read_b128 v[68:71],v2 offset:12288
v_fma_f32 v86,v118,v86,v117
v_mfma_f32_16x16x32_bf16 v[32:35],a[88:91],v[80:83],v[32:35]
v_rcp_f32 v84,v84
v_mfma_f32_16x16x32_bf16 v[36:39],a[92:95],v[80:83],v[36:39]
s_waitcnt lgkmcnt(3)
v_rcp_f32 v85,v85
v_mfma_f32_16x16x32_bf16 v[28:31],a[96:99],v[52:55],v[28:31]
ds_read_b128 v[72:75],v2 offset:13312
v_mul_f32 v86,v42,v86
v_mfma_f32_16x16x32_bf16 v[32:35],a[100:103],v[52:55],v[32:35]
v_exp_f32 v86,v86
v_mfma_f32_16x16x32_bf16 v[36:39],a[104:107],v[52:55],v[36:39])"
R"(
v_fma_f32 v86,v86,v119,v119
v_fma_f32 v87,v118,v87,v117
v_mfma_f32_16x16x32_bf16 v[28:31],a[108:111],v[56:59],v[28:31]
ds_read_b128 v[76:79],v2 offset:14336
v_rcp_f32 v86,v86
v_mfma_f32_16x16x32_bf16 v[32:35],a[112:115],v[56:59],v[32:35]
v_mul_f32 v87,v43,v87
v_mul_f32 v40,v40,v84
v_mfma_f32_16x16x32_bf16 v[36:39],a[116:119],v[56:59],v[36:39]
s_waitcnt lgkmcnt(3)
v_exp_f32 v87,v87
v_mfma_f32_16x16x32_bf16 v[28:31],a[120:123],v[60:63],v[28:31]
ds_read_b128 v[80:83],v2 offset:15360
v_fma_f32 v87,v87,v119,v119
v_mfma_f32_16x16x32_bf16 v[32:35],a[124:127],v[60:63],v[32:35]
s_waitcnt vmcnt(6)
v_rcp_f32 v87,v87
v_mfma_f32_16x16x32_bf16 v[36:39],a[128:131],v[60:63],v[36:39]
s_barrier
v_mfma_f32_16x16x32_bf16 v[28:31],a[132:135],v[64:67],v[28:31]
ds_read_b128 v[52:55],v2 offset:16384
v_add_f32_dpp v104,v104,v104 quad_perm:[1,0,3,2] row_mask:0xf bank_mask:0xf
v_mfma_f32_16x16x32_bf16 v[32:35],a[136:139],v[64:67],v[32:35]
s_nop 0
v_add_f32_dpp v104,v104,v104 quad_perm:[2,3,0,1] row_mask:0xf bank_mask:0xf
v_mul_f32 v41,v41,v85
v_mfma_f32_16x16x32_bf16 v[36:39],a[140:143],v[64:67],v[36:39]
s_waitcnt lgkmcnt(3)
v_add_f32_dpp v104,v104,v104 row_half_mirror row_mask:0xf bank_mask:0xf
v_mul_f32 v42,v42,v86
v_mfma_f32_16x16x32_bf16 v[28:31],a[144:147],v[68:71],v[28:31]
ds_read_b128 v[56:59],v2 offset:17408
v_mul_f32 v43,v43,v87
v_mfma_f32_16x16x32_bf16 v[32:35],a[148:151],v[68:71],v[32:35]
v_cvt_pk_bf16_f32 v96,v40,v41
v_cvt_pk_bf16_f32 v97,v42,v43
v_mfma_f32_16x16x32_bf16 v[36:39],a[152:155],v[68:71],v[36:39]
v_fma_f32 v44,-v116,v20,v44
v_fma_f32 v45,-v116,v21,v45
v_mfma_f32_16x16x32_bf16 v[28:31],a[156:159],v[72:75],v[28:31]
ds_read_b128 v[60:63],v2 offset:18432
v_fma_f32 v46,-v116,v22,v46
v_mfma_f32_16x16x32_bf16 v[32:35],a[160:163],v[72:75],v[32:35]
v_fma_f32 v47,-v116,v23,v47
v_mul_f32 v88,v44,v44
v_mul_f32 v89,v45,v45
v_mfma_f32_16x16x32_bf16 v[36:39],a[164:167],v[72:75],v[36:39]
s_waitcnt lgkmcnt(3)
v_mul_f32 v90,v46,v46
v_mul_f32 v91,v47,v47
v_mfma_f32_16x16x32_bf16 v[28:31],a[168:171],v[76:79],v[28:31]
ds_read_b128 v[64:67],v2 offset:19456
v_fma_f32 v88,v118,v88,v117
v_mfma_f32_16x16x32_bf16 v[32:35],a[172:175],v[76:79],v[32:35]
v_mul_f32 v88,v44,v88
v_fma_f32 v89,v118,v89,v117
v_mfma_f32_16x16x32_bf16 v[36:39],a[176:179],v[76:79],v[36:39]
v_exp_f32 v88,v88
v_mfma_f32_16x16x32_bf16 v[28:31],a[180:183],v[80:83],v[28:31]
buffer_load_dwordx4 v1,s[36:39],s61 offen offset:0 sc1 lds
v_mfma_f32_16x16x32_bf16 v[32:35],a[184:187],v[80:83],v[32:35]
ds_write_b32 v10,v104 offset:0
v_mfma_f32_16x16x32_bf16 v[36:39],a[188:191],v[80:83],v[36:39]
s_waitcnt lgkmcnt(3)
ds_read_b128 v[68:71],v2 offset:20480
v_mul_f32 v89,v45,v89
v_mfma_f32_16x16x32_bf16 v[28:31],a[192:195],v[52:55],v[28:31]
ds_read_b128 v[72:75],v2 offset:21504
v_fma_f32 v88,v88,v119,v119
v_mfma_f32_16x16x32_bf16 v[32:35],a[196:199],v[52:55],v[32:35]
buffer_load_dwordx4 v1,s[36:39],s61 offen offset:1024 sc1 lds
v_mfma_f32_16x16x32_bf16 v[36:39],a[200:203],v[52:55],v[36:39]
v_exp_f32 v89,v89
v_mfma_f32_16x16x32_bf16 v[28:31],a[204:207],v[56:59],v[28:31]
ds_read_b128 v[76:79],v2 offset:22528
v_fma_f32 v89,v89,v119,v119
v_mfma_f32_16x16x32_bf16 v[32:35],a[208:211],v[56:59],v[32:35]
v_rcp_f32 v88,v88
v_mfma_f32_16x16x32_bf16 v[36:39],a[212:215],v[56:59],v[36:39]
s_waitcnt lgkmcnt(4)
buffer_load_dwordx4 v1,s[36:39],s61 offen offset:2048 sc1 lds
v_fma_f32 v90,v118,v90,v117
v_mfma_f32_16x16x32_bf16 v[28:31],a[216:219],v[60:63],v[28:31]
ds_read_b128 v[80:83],v2 offset:23552
v_mul_f32 v90,v46,v90
v_mfma_f32_16x16x32_bf16 v[32:35],a[220:223],v[60:63],v[32:35]
v_exp_f32 v90,v90
v_mfma_f32_16x16x32_bf16 v[36:39],a[224:227],v[60:63],v[36:39]
v_rcp_f32 v89,v89
v_mfma_f32_16x16x32_bf16 v[28:31],a[228:231],v[64:67],v[28:31]
buffer_load_dwordx4 v1,s[36:39],s61 offen offset:3072 sc1 lds
v_mfma_f32_16x16x32_bf16 v[32:35],a[232:235],v[64:67],v[32:35]
buffer_load_dword v108,v8,s[36:39],s72 offen offset:0 sc1
v_mfma_f32_16x16x32_bf16 v[36:39],a[236:239],v[64:67],v[36:39]
s_waitcnt lgkmcnt(2)
ds_read_b128 v[52:55],v2 offset:24576
v_fma_f32 v90,v90,v119,v119
v_mfma_f32_16x16x32_bf16 v[28:31],a[240:243],v[68:71],v[28:31]
ds_read_b128 v[56:59],v2 offset:25600
v_fma_f32 v91,v118,v91,v117
v_mfma_f32_16x16x32_bf16 v[32:35],a[244:247],v[68:71],v[32:35]
s_waitcnt vmcnt(9)
v_rcp_f32 v90,v90
v_mfma_f32_16x16x32_bf16 v[36:39],a[248:251],v[68:71],v[36:39]
s_barrier
v_mfma_f32_16x16x32_bf16 v[28:31],a[252:255],v[72:75],v[28:31]
ds_read_b128 v[60:63],v2 offset:26624
v_mul_f32 v91,v47,v91
v_mfma_f32_16x16x32_bf16 v[32:35],v[128:131],v[72:75],v[32:35]
v_exp_f32 v91,v91
v_mfma_f32_16x16x32_bf16 v[36:39],v[132:135],v[72:75],v[36:39]
s_waitcnt lgkmcnt(3)
v_fma_f32 v91,v91,v119,v119
v_mul_f32 v44,v44,v88
v_mfma_f32_16x16x32_bf16 v[28:31],v[136:139],v[76:79],v[28:31]
ds_read_b128 v[64:67],v2 offset:27648
v_rcp_f32 v91,v91
v_mfma_f32_16x16x32_bf16 v[32:35],v[140:143],v[76:79],v[32:35]
v_mul_f32 v45,v45,v89
v_mul_f32 v46,v46,v90
v_mfma_f32_16x16x32_bf16 v[36:39],v[144:147],v[76:79],v[36:39]
v_mul_f32 v47,v47,v91
v_cvt_pk_bf16_f32 v98,v44,v45
v_mfma_f32_16x16x32_bf16 v[28:31],v[148:151],v[80:83],v[28:31]
ds_read_b128 v[68:71],v2 offset:28672
v_cvt_pk_bf16_f32 v99,v46,v47
v_mfma_f32_16x16x32_bf16 v[32:35],v[152:155],v[80:83],v[32:35]
v_fma_f32 v48,-v116,v24,v48
v_fma_f32 v49,-v116,v25,v49
v_mfma_f32_16x16x32_bf16 v[36:39],v[156:159],v[80:83],v[36:39]
s_waitcnt lgkmcnt(3)
v_fma_f32 v50,-v116,v26,v50
v_fma_f32 v51,-v116,v27,v51
v_mfma_f32_16x16x32_bf16 v[28:31],v[160:163],v[52:55],v[28:31]
ds_read_b128 v[72:75],v2 offset:29696
v_mul_f32 v92,v48,v48
v_mfma_f32_16x16x32_bf16 v[32:35],v[164:167],v[52:55],v[32:35]
v_mul_f32 v93,v49,v49
v_mul_f32 v94,v50,v50
v_mfma_f32_16x16x32_bf16 v[36:39],v[168:171],v[52:55],v[36:39]
buffer_store_dwordx4 v[96:99],v3,s[36:39],s73 offen offset:0 sc0 sc1
v_mul_f32 v95,v51,v51
v_fma_f32 v92,v118,v92,v117
v_mul_f32 v92,v48,v92
v_mfma_f32_16x16x32_bf16 v[28:31],v[172:175],v[56:59],v[28:31]
ds_read_b128 v[76:79],v2 offset:30720
v_fma_f32 v93,v118,v93,v117
v_mfma_f32_16x16x32_bf16 v[32:35],v[176:179],v[56:59],v[32:35]
v_exp_f32 v92,v92
v_mfma_f32_16x16x32_bf16 v[36:39],v[180:183],v[56:59],v[36:39]
s_waitcnt lgkmcnt(3)
v_mul_f32 v93,v49,v93
v_fma_f32 v92,v92,v119,v119
v_mfma_f32_16x16x32_bf16 v[28:31],v[184:187],v[60:63],v[28:31]
ds_read_b128 v[80:83],v2 offset:31744
v_fma_f32 v94,v118,v94,v117
v_mfma_f32_16x16x32_bf16 v[32:35],v[188:191],v[60:63],v[32:35]
s_waitcnt vmcnt(6)
v_exp_f32 v93,v93
v_mul_f32 v94,v50,v94
v_mfma_f32_16x16x32_bf16 v[36:39],v[192:195],v[60:63],v[36:39]
s_barrier
v_mfma_f32_16x16x32_bf16 v[28:31],v[196:199],v[64:67],v[28:31]
ds_read_b128 v[52:55],v2 offset:32768
ds_read_b32 v13,v7 offset:0
v_mfma_f32_16x16x32_bf16 v[32:35],v[200:203],v[64:67],v[32:35]
ds_read_b64 v[112:113],v11 offset:0
v_fma_f32 v95,v118,v95,v117
v_mfma_f32_16x16x32_bf16 v[36:39],v[204:207],v[64:67],v[36:39]
s_waitcnt lgkmcnt(5)
v_exp_f32 v94,v94
v_mfma_f32_16x16x32_bf16 v[28:31],v[208:211],v[68:71],v[28:31]
ds_read_b128 v[56:59],v2 offset:33792
v_mul_f32 v95,v51,v95
v_mfma_f32_16x16x32_bf16 v[32:35],v[212:215],v[68:71],v[32:35]
s_waitcnt lgkmcnt(2)
v_exp_f32 v95,v95
v_mfma_f32_16x16x32_bf16 v[36:39],v[216:219],v[68:71],v[36:39]
s_waitcnt lgkmcnt(1)
v_cmp_eq_u32 vcc,s60,v13
v_rcp_f32 v92,v92
v_fma_f32 v93,v93,v119,v119
v_mfma_f32_16x16x32_bf16 v[28:31],v[220:223],v[72:75],v[28:31]
s_cmp_eq_u32 vcc_lo,-1
ds_read_b128 v[60:63],v2 offset:34816
v_fma_f32 v94,v94,v119,v119
v_mfma_f32_16x16x32_bf16 v[32:35],v[224:227],v[72:75],v[32:35]
s_cselect_b32 s67,s86,s59
v_rcp_f32 v93,v93
v_mfma_f32_16x16x32_bf16 v[36:39],v[228:231],v[72:75],v[36:39]
v_fma_f32 v95,v95,v119,v119
v_rcp_f32 v94,v94
v_rcp_f32 v95,v95
v_mfma_f32_16x16x32_bf16 v[28:31],v[232:235],v[76:79],v[28:31]
s_lshr_b32 s61,s67,0
v_mul_f32 v48,v48,v92
v_mul_f32 v49,v49,v93
v_mul_f32 v50,v50,v94
v_mul_f32 v51,v51,v95
v_cvt_pk_bf16_f32 v100,v48,v49)"
R"(
v_cvt_pk_bf16_f32 v101,v50,v51
v_mfma_f32_16x16x32_bf16 v[32:35],v[236:239],v[76:79],v[32:35]
buffer_store_dwordx2 v[100:101],v9,s[36:39],s73 offen offset:0 sc0 sc1
ds_read_b128 v[64:67],v2 offset:35840
v_mfma_f32_16x16x32_bf16 v[36:39],v[240:243],v[76:79],v[36:39]
s_xor_b32 s63,s63,65536
s_cmp_ge_u32 s66,s59
v_mfma_f32_16x16x32_bf16 v[28:31],v[244:247],v[80:83],v[28:31]
ds_read_b128 v[68:71],v2 offset:36864
v_mfma_f32_16x16x32_bf16 v[32:35],v[248:251],v[80:83],v[32:35]
s_waitcnt lgkmcnt(3)
v_mfma_f32_16x16x32_bf16 v[36:39],v[252:255],v[80:83],v[36:39]
s_cbranch_scc1 Lg3_slow1_%=
Lg3_d1w2_%=:
v_mfma_f32_16x16x32_bf16 v[40:43],a[0:3],v[52:55],0
s_mov_b32 m0,s62
ds_read_b128 v[72:75],v2 offset:37888
v_mul_f32 v116,s75,v112
v_mfma_f32_16x16x32_bf16 v[44:47],a[4:7],v[52:55],0
s_mul_i32 s73,s48,7
v_fma_f32 v113,v113,s75,v12
v_fma_f32 v113,-v116,v116,v113
v_mfma_f32_16x16x32_bf16 v[48:51],a[8:11],v[52:55],0
s_lshr_b32 s73,s73,1
v_max_f32 v113,v113,v12
v_rsq_f32 v112,v113
v_mfma_f32_16x16x32_bf16 v[40:43],a[12:15],v[56:59],v[40:43]
buffer_load_dwordx4 v0,s[36:39],s61 offen offset:0 sc1 lds
v_mfma_f32_16x16x32_bf16 v[44:47],a[16:19],v[56:59],v[44:47]
s_add_u32 s74,s53,s55
ds_read_b128 v[76:79],v2 offset:38912
v_mul_f32 v117,s76,v112
v_mfma_f32_16x16x32_bf16 v[48:51],a[20:23],v[56:59],v[48:51]
s_waitcnt lgkmcnt(3)
v_mul_f32 v118,v112,v112
v_mul_f32 v119,v113,v112
v_mfma_f32_16x16x32_bf16 v[40:43],a[24:27],v[60:63],v[40:43]
s_cmp_eq_u32 s74,s56
ds_read_b128 v[80:83],v2 offset:39936
v_mul_f32 v118,s77,v118
v_mfma_f32_16x16x32_bf16 v[44:47],a[28:31],v[60:63],v[44:47]
buffer_load_dwordx4 v0,s[36:39],s61 offen offset:1024 sc1 lds
v_mfma_f32_16x16x32_bf16 v[48:51],a[32:35],v[60:63],v[48:51]
s_cselect_b32 s74,s57,s74
v_mul_f32 v118,v118,v117
v_fma_f32 v28,-v116,v16,v28
v_mfma_f32_16x16x32_bf16 v[40:43],a[36:39],v[64:67],v[40:43]
s_cmp_eq_u32 s74,s58
ds_read_b128 v[52:55],v2 offset:40960
v_fma_f32 v29,-v116,v17,v29
v_mfma_f32_16x16x32_bf16 v[44:47],a[40:43],v[64:67],v[44:47]
s_cselect_b32 s74,s59,s74
v_fma_f32 v30,-v116,v18,v30
v_fma_f32 v31,-v116,v19,v31
v_mfma_f32_16x16x32_bf16 v[48:51],a[44:47],v[64:67],v[48:51]
s_waitcnt lgkmcnt(3)
buffer_load_dwordx4 v0,s[36:39],s61 offen offset:2048 sc1 lds
v_mfma_f32_16x16x32_bf16 v[40:43],a[48:51],v[68:71],v[40:43]
s_cmp_ge_u32 s67,s59
ds_read_b128 v[56:59],v2 offset:41984
v_mul_f32 v84,v28,v28
v_mfma_f32_16x16x32_bf16 v[44:47],a[52:55],v[68:71],v[44:47]
s_cselect_b32 s86,s50,s51
v_mul_f32 v85,v29,v29
v_mul_f32 v86,v30,v30
v_mul_f32 v87,v31,v31
v_mfma_f32_16x16x32_bf16 v[48:51],a[56:59],v[68:71],v[48:51]
s_mov_b64 s[44:45],s[46:47]
v_fma_f32 v84,v118,v84,v117
v_mul_f32 v84,v28,v84
v_mfma_f32_16x16x32_bf16 v[40:43],a[60:63],v[72:75],v[40:43]
buffer_load_dwordx4 v0,s[36:39],s61 offen offset:3072 sc1 lds
v_mfma_f32_16x16x32_bf16 v[44:47],a[64:67],v[72:75],v[44:47]
s_add_u32 m0,s62,16384
ds_read_b128 v[60:63],v2 offset:43008
v_fma_f32 v85,v118,v85,v117
v_mfma_f32_16x16x32_bf16 v[48:51],a[68:71],v[72:75],v[48:51]
s_waitcnt lgkmcnt(3)
v_exp_f32 v84,v84
v_mfma_f32_16x16x32_bf16 v[40:43],a[72:75],v[76:79],v[40:43]
s_lshr_b32 s72,s61,5
ds_read_b128 v[64:67],v2 offset:44032
v_mul_f32 v85,v29,v85
v_mfma_f32_16x16x32_bf16 v[44:47],a[76:79],v[76:79],v[44:47]
s_mov_b32 s62,s64
v_exp_f32 v85,v85
v_mfma_f32_16x16x32_bf16 v[48:51],a[80:83],v[76:79],v[48:51]
s_mov_b64 s[46:47],s[48:49]
v_fma_f32 v84,v84,v119,v119
v_fma_f32 v85,v85,v119,v119
v_mfma_f32_16x16x32_bf16 v[40:43],a[84:87],v[80:83],v[40:43]
s_mov_b64 s[48:49],s[50:51]
ds_read_b128 v[68:71],v2 offset:45056
v_fma_f32 v86,v118,v86,v117
v_mfma_f32_16x16x32_bf16 v[44:47],a[88:91],v[80:83],v[44:47]
s_mov_b64 s[50:51],s[52:53]
v_rcp_f32 v84,v84
v_mfma_f32_16x16x32_bf16 v[48:51],a[92:95],v[80:83],v[48:51]
s_waitcnt lgkmcnt(3)
v_rcp_f32 v85,v85
v_mfma_f32_16x16x32_bf16 v[40:43],a[96:99],v[52:55],v[40:43]
s_mov_b32 s52,s74
ds_read_b128 v[72:75],v2 offset:46080
v_mul_f32 v86,v30,v86
v_fma_f32 v87,v118,v87,v117
v_mfma_f32_16x16x32_bf16 v[44:47],a[100:103],v[52:55],v[44:47]
v_exp_f32 v86,v86
v_mfma_f32_16x16x32_bf16 v[48:51],a[104:107],v[52:55],v[48:51]
v_mul_f32 v87,v31,v87
v_fma_f32 v86,v86,v119,v119
v_mfma_f32_16x16x32_bf16 v[40:43],a[108:111],v[56:59],v[40:43]
ds_read_b128 v[76:79],v2 offset:47104
v_mul_f32 v28,v28,v84
v_mfma_f32_16x16x32_bf16 v[44:47],a[112:115],v[56:59],v[44:47]
v_exp_f32 v87,v87
v_mfma_f32_16x16x32_bf16 v[48:51],a[116:119],v[56:59],v[48:51]
s_waitcnt lgkmcnt(3)
v_rcp_f32 v86,v86
v_mfma_f32_16x16x32_bf16 v[40:43],a[120:123],v[60:63],v[40:43]
ds_read_b128 v[80:83],v2 offset:48128
v_fma_f32 v87,v87,v119,v119
v_mfma_f32_16x16x32_bf16 v[44:47],a[124:127],v[60:63],v[44:47]
s_waitcnt vmcnt(6)
v_rcp_f32 v87,v87
v_mfma_f32_16x16x32_bf16 v[48:51],a[128:131],v[60:63],v[48:51]
s_barrier
v_mfma_f32_16x16x32_bf16 v[40:43],a[132:135],v[64:67],v[40:43]
ds_read_b128 v[52:55],v2 offset:49152
v_add_f32_dpp v108,v108,v108 quad_perm:[1,0,3,2] row_mask:0xf bank_mask:0xf
v_mfma_f32_16x16x32_bf16 v[44:47],a[136:139],v[64:67],v[44:47]
s_nop 0
v_add_f32_dpp v108,v108,v108 quad_perm:[2,3,0,1] row_mask:0xf bank_mask:0xf
v_mul_f32 v29,v29,v85
v_mfma_f32_16x16x32_bf16 v[48:51],a[140:143],v[64:67],v[48:51]
s_waitcnt lgkmcnt(3)
v_add_f32_dpp v108,v108,v108 row_half_mirror row_mask:0xf bank_mask:0xf
v_mul_f32 v30,v30,v86
v_mul_f32 v31,v31,v87
v_mfma_f32_16x16x32_bf16 v[40:43],a[144:147],v[68:71],v[40:43]
ds_read_b128 v[56:59],v2 offset:50176
v_cvt_pk_bf16_f32 v96,v28,v29
v_mfma_f32_16x16x32_bf16 v[44:47],a[148:151],v[68:71],v[44:47]
v_cvt_pk_bf16_f32 v97,v30,v31
v_fma_f32 v32,-v116,v20,v32
v_mfma_f32_16x16x32_bf16 v[48:51],a[152:155],v[68:71],v[48:51]
v_fma_f32 v33,-v116,v21,v33
v_fma_f32 v34,-v116,v22,v34
v_mfma_f32_16x16x32_bf16 v[40:43],a[156:159],v[72:75],v[40:43]
ds_read_b128 v[60:63],v2 offset:51200
v_fma_f32 v35,-v116,v23,v35
v_mfma_f32_16x16x32_bf16 v[44:47],a[160:163],v[72:75],v[44:47]
v_mul_f32 v88,v32,v32
v_mul_f32 v89,v33,v33
v_mfma_f32_16x16x32_bf16 v[48:51],a[164:167],v[72:75],v[48:51]
s_waitcnt lgkmcnt(3)
v_mul_f32 v90,v34,v34
v_mul_f32 v91,v35,v35
v_mfma_f32_16x16x32_bf16 v[40:43],a[168:171],v[76:79],v[40:43]
ds_read_b128 v[64:67],v2 offset:52224
v_fma_f32 v88,v118,v88,v117
v_mfma_f32_16x16x32_bf16 v[44:47],a[172:175],v[76:79],v[44:47]
v_mul_f32 v88,v32,v88
v_fma_f32 v89,v118,v89,v117
v_mfma_f32_16x16x32_bf16 v[48:51],a[176:179],v[76:79],v[48:51]
v_exp_f32 v88,v88
v_mfma_f32_16x16x32_bf16 v[40:43],a[180:183],v[80:83],v[40:43]
buffer_load_dwordx4 v1,s[36:39],s61 offen offset:0 sc1 lds
v_mfma_f32_16x16x32_bf16 v[44:47],a[184:187],v[80:83],v[44:47]
ds_write_b32 v10,v108 offset:128
v_mfma_f32_16x16x32_bf16 v[48:51],a[188:191],v[80:83],v[48:51]
s_waitcnt lgkmcnt(3)
ds_read_b128 v[68:71],v2 offset:53248
v_mul_f32 v89,v33,v89
v_fma_f32 v88,v88,v119,v119
v_mfma_f32_16x16x32_bf16 v[40:43],a[192:195],v[52:55],v[40:43]
ds_read_b128 v[72:75],v2 offset:54272
v_fma_f32 v90,v118,v90,v117
v_mfma_f32_16x16x32_bf16 v[44:47],a[196:199],v[52:55],v[44:47]
buffer_load_dwordx4 v1,s[36:39],s61 offen offset:1024 sc1 lds
v_mfma_f32_16x16x32_bf16 v[48:51],a[200:203],v[52:55],v[48:51]
v_exp_f32 v89,v89
v_mfma_f32_16x16x32_bf16 v[40:43],a[204:207],v[56:59],v[40:43]
ds_read_b128 v[76:79],v2 offset:55296
v_mul_f32 v90,v34,v90
v_mfma_f32_16x16x32_bf16 v[44:47],a[208:211],v[56:59],v[44:47]
v_exp_f32 v90,v90
v_mfma_f32_16x16x32_bf16 v[48:51],a[212:215],v[56:59],v[48:51]
s_waitcnt lgkmcnt(4)
buffer_load_dwordx4 v1,s[36:39],s61 offen offset:2048 sc1 lds
v_mfma_f32_16x16x32_bf16 v[40:43],a[216:219],v[60:63],v[40:43]
ds_read_b128 v[80:83],v2 offset:56320
v_fma_f32 v89,v89,v119,v119
v_mfma_f32_16x16x32_bf16 v[44:47],a[220:223],v[60:63],v[44:47]
v_rcp_f32 v88,v88
v_mfma_f32_16x16x32_bf16 v[48:51],a[224:227],v[60:63],v[48:51]
v_rcp_f32 v89,v89
v_mfma_f32_16x16x32_bf16 v[40:43],a[228:231],v[64:67],v[40:43]
buffer_load_dwordx4 v1,s[36:39],s61 offen offset:3072 sc1 lds
v_mfma_f32_16x16x32_bf16 v[44:47],a[232:235],v[64:67],v[44:47]
buffer_load_dword v104,v8,s[36:39],s72 offen offset:0 sc1)"
R"(
ds_read_b128 v[52:55],v2 offset:57344
v_mfma_f32_16x16x32_bf16 v[48:51],a[236:239],v[64:67],v[48:51]
s_waitcnt lgkmcnt(3)
v_fma_f32 v90,v90,v119,v119
v_fma_f32 v91,v118,v91,v117
v_mfma_f32_16x16x32_bf16 v[40:43],a[240:243],v[68:71],v[40:43]
ds_read_b128 v[56:59],v2 offset:58368
v_mul_f32 v91,v35,v91
v_mfma_f32_16x16x32_bf16 v[44:47],a[244:247],v[68:71],v[44:47]
s_waitcnt vmcnt(9)
v_exp_f32 v91,v91
v_mfma_f32_16x16x32_bf16 v[48:51],a[248:251],v[68:71],v[48:51]
s_barrier
v_mfma_f32_16x16x32_bf16 v[40:43],a[252:255],v[72:75],v[40:43]
ds_read_b128 v[60:63],v2 offset:59392
v_fma_f32 v91,v91,v119,v119
v_mfma_f32_16x16x32_bf16 v[44:47],v[128:131],v[72:75],v[44:47]
v_rcp_f32 v90,v90
v_mfma_f32_16x16x32_bf16 v[48:51],v[132:135],v[72:75],v[48:51]
s_waitcnt lgkmcnt(3)
v_rcp_f32 v91,v91
v_mfma_f32_16x16x32_bf16 v[40:43],v[136:139],v[76:79],v[40:43]
ds_read_b128 v[64:67],v2 offset:60416
v_mul_f32 v32,v32,v88
v_mfma_f32_16x16x32_bf16 v[44:47],v[140:143],v[76:79],v[44:47]
v_mul_f32 v33,v33,v89
v_mul_f32 v34,v34,v90
v_mfma_f32_16x16x32_bf16 v[48:51],v[144:147],v[76:79],v[48:51]
v_mul_f32 v35,v35,v91
v_cvt_pk_bf16_f32 v98,v32,v33
v_mfma_f32_16x16x32_bf16 v[40:43],v[148:151],v[80:83],v[40:43]
ds_read_b128 v[68:71],v2 offset:61440
v_cvt_pk_bf16_f32 v99,v34,v35
v_fma_f32 v36,-v116,v24,v36
v_mfma_f32_16x16x32_bf16 v[44:47],v[152:155],v[80:83],v[44:47]
v_fma_f32 v37,-v116,v25,v37
v_fma_f32 v38,-v116,v26,v38
v_mfma_f32_16x16x32_bf16 v[48:51],v[156:159],v[80:83],v[48:51]
s_waitcnt lgkmcnt(3)
v_fma_f32 v39,-v116,v27,v39
v_mul_f32 v92,v36,v36
v_mfma_f32_16x16x32_bf16 v[40:43],v[160:163],v[52:55],v[40:43]
ds_read_b128 v[72:75],v2 offset:62464
v_mul_f32 v93,v37,v37
v_mfma_f32_16x16x32_bf16 v[44:47],v[164:167],v[52:55],v[44:47]
v_mul_f32 v94,v38,v38
v_mul_f32 v95,v39,v39
v_mfma_f32_16x16x32_bf16 v[48:51],v[168:171],v[52:55],v[48:51]
buffer_store_dwordx4 v[96:99],v3,s[36:39],s73 offen offset:0 sc0 sc1
v_fma_f32 v92,v118,v92,v117
v_mul_f32 v92,v36,v92
v_fma_f32 v93,v118,v93,v117
v_mfma_f32_16x16x32_bf16 v[40:43],v[172:175],v[56:59],v[40:43]
ds_read_b128 v[76:79],v2 offset:63488
v_mul_f32 v93,v37,v93
v_mfma_f32_16x16x32_bf16 v[44:47],v[176:179],v[56:59],v[44:47]
v_exp_f32 v92,v92
v_mfma_f32_16x16x32_bf16 v[48:51],v[180:183],v[56:59],v[48:51]
s_waitcnt lgkmcnt(3)
v_exp_f32 v93,v93
v_mfma_f32_16x16x32_bf16 v[40:43],v[184:187],v[60:63],v[40:43]
ds_read_b128 v[80:83],v2 offset:64512
v_fma_f32 v92,v92,v119,v119
v_mfma_f32_16x16x32_bf16 v[44:47],v[188:191],v[60:63],v[44:47]
s_waitcnt vmcnt(6)
v_xor_b32 v2,65536,v2
v_fma_f32 v93,v93,v119,v119
v_mfma_f32_16x16x32_bf16 v[48:51],v[192:195],v[60:63],v[48:51]
s_barrier
v_mfma_f32_16x16x32_bf16 v[40:43],v[196:199],v[64:67],v[40:43]
ds_read_b128 v[52:55],v2 offset:0
ds_read_b32 v13,v7 offset:256
v_mfma_f32_16x16x32_bf16 v[44:47],v[200:203],v[64:67],v[44:47]
ds_read_b64 v[112:113],v11 offset:128
v_fma_f32 v94,v118,v94,v117
v_mfma_f32_16x16x32_bf16 v[48:51],v[204:207],v[64:67],v[48:51]
s_waitcnt lgkmcnt(5)
v_rcp_f32 v92,v92
v_mfma_f32_16x16x32_bf16 v[40:43],v[208:211],v[68:71],v[40:43]
ds_read_b128 v[56:59],v2 offset:1024
v_mul_f32 v94,v38,v94
v_mfma_f32_16x16x32_bf16 v[44:47],v[212:215],v[68:71],v[44:47]
s_waitcnt lgkmcnt(2)
v_exp_f32 v94,v94
v_mfma_f32_16x16x32_bf16 v[48:51],v[216:219],v[68:71],v[48:51]
s_waitcnt lgkmcnt(1)
v_cmp_eq_u32 vcc,s60,v13
v_rcp_f32 v93,v93
v_fma_f32 v94,v94,v119,v119
v_mfma_f32_16x16x32_bf16 v[40:43],v[220:223],v[72:75],v[40:43]
s_cmp_eq_u32 vcc_lo,-1
ds_read_b128 v[60:63],v2 offset:2048
v_fma_f32 v95,v118,v95,v117
v_mfma_f32_16x16x32_bf16 v[44:47],v[224:227],v[72:75],v[44:47]
s_cselect_b32 s66,s86,s59
v_mul_f32 v95,v39,v95
v_exp_f32 v95,v95
v_mfma_f32_16x16x32_bf16 v[48:51],v[228:231],v[72:75],v[48:51]
v_fma_f32 v95,v95,v119,v119
v_rcp_f32 v94,v94
v_rcp_f32 v95,v95
v_mfma_f32_16x16x32_bf16 v[40:43],v[232:235],v[76:79],v[40:43]
s_lshr_b32 s61,s66,0
v_mul_f32 v36,v36,v92
v_mul_f32 v37,v37,v93
v_mul_f32 v38,v38,v94
v_mul_f32 v39,v39,v95
v_cvt_pk_bf16_f32 v100,v36,v37
v_cvt_pk_bf16_f32 v101,v38,v39
v_mfma_f32_16x16x32_bf16 v[44:47],v[236:239],v[76:79],v[44:47]
buffer_store_dwordx2 v[100:101],v9,s[36:39],s73 offen offset:0 sc0 sc1
ds_read_b128 v[64:67],v2 offset:3072
v_mfma_f32_16x16x32_bf16 v[48:51],v[240:243],v[76:79],v[48:51]
s_xor_b32 s64,s64,65536
s_cmp_lt_u32 s67,s59
v_mfma_f32_16x16x32_bf16 v[40:43],v[244:247],v[80:83],v[40:43]
ds_read_b128 v[68:71],v2 offset:4096
v_mfma_f32_16x16x32_bf16 v[44:47],v[248:251],v[80:83],v[44:47]
s_waitcnt lgkmcnt(3)
v_mfma_f32_16x16x32_bf16 v[48:51],v[252:255],v[80:83],v[48:51]
s_cbranch_scc1 Lg3_d0w2_%=
s_branch Lg3_slow0_%=
Lg3_d0w3_%=:
v_mfma_f32_16x16x32_bf16 v[28:31],a[0:3],v[52:55],0
s_mov_b32 m0,s62
ds_read_b128 v[72:75],v2 offset:5120
v_mul_f32 v116,s75,v112
v_mfma_f32_16x16x32_bf16 v[32:35],a[4:7],v[52:55],0
s_mul_i32 s73,s47,7
v_fma_f32 v113,v113,s75,v12
v_fma_f32 v113,-v116,v116,v113
v_mfma_f32_16x16x32_bf16 v[36:39],a[8:11],v[52:55],0
s_lshr_b32 s73,s73,1
v_max_f32 v113,v113,v12
v_rsq_f32 v112,v113
v_mfma_f32_16x16x32_bf16 v[28:31],a[12:15],v[56:59],v[28:31]
s_add_u32 s53,s52,s55
ds_read_b128 v[76:79],v2 offset:6144
v_mul_f32 v117,s76,v112
v_mfma_f32_16x16x32_bf16 v[32:35],a[16:19],v[56:59],v[32:35]
buffer_load_dwordx4 v0,s[36:39],s61 offen offset:0 sc1 lds
v_mfma_f32_16x16x32_bf16 v[36:39],a[20:23],v[56:59],v[36:39]
s_waitcnt lgkmcnt(3)
v_mul_f32 v118,v112,v112
v_mul_f32 v119,v113,v112
v_mfma_f32_16x16x32_bf16 v[28:31],a[24:27],v[60:63],v[28:31]
s_cmp_eq_u32 s53,s56
ds_read_b128 v[80:83],v2 offset:7168
v_mul_f32 v118,s77,v118
v_mfma_f32_16x16x32_bf16 v[32:35],a[28:31],v[60:63],v[32:35]
s_cselect_b32 s53,s57,s53
v_mul_f32 v118,v118,v117
v_fma_f32 v40,-v116,v16,v40
v_mfma_f32_16x16x32_bf16 v[36:39],a[32:35],v[60:63],v[36:39]
buffer_load_dwordx4 v0,s[36:39],s61 offen offset:1024 sc1 lds
v_mfma_f32_16x16x32_bf16 v[28:31],a[36:39],v[64:67],v[28:31]
s_cmp_eq_u32 s53,s58
ds_read_b128 v[52:55],v2 offset:8192
v_fma_f32 v41,-v116,v17,v41
v_mfma_f32_16x16x32_bf16 v[32:35],a[40:43],v[64:67],v[32:35]
s_cselect_b32 s53,s59,s53
v_fma_f32 v42,-v116,v18,v42
v_fma_f32 v43,-v116,v19,v43
v_mfma_f32_16x16x32_bf16 v[36:39],a[44:47],v[64:67],v[36:39]
s_waitcnt lgkmcnt(3)
v_mul_f32 v84,v40,v40
v_mul_f32 v85,v41,v41
v_mfma_f32_16x16x32_bf16 v[28:31],a[48:51],v[68:71],v[28:31]
buffer_load_dwordx4 v0,s[36:39],s61 offen offset:2048 sc1 lds
v_mfma_f32_16x16x32_bf16 v[32:35],a[52:55],v[68:71],v[32:35]
s_cmp_ge_u32 s66,s59
ds_read_b128 v[56:59],v2 offset:9216
v_mul_f32 v86,v42,v42
v_mfma_f32_16x16x32_bf16 v[36:39],a[56:59],v[68:71],v[36:39]
s_cselect_b32 s86,s49,s50
v_mul_f32 v87,v43,v43
v_fma_f32 v84,v118,v84,v117
v_mfma_f32_16x16x32_bf16 v[28:31],a[60:63],v[72:75],v[28:31]
s_mul_i32 s70,s46,5
ds_read_b128 v[60:63],v2 offset:10240
v_mul_f32 v84,v40,v84
v_mfma_f32_16x16x32_bf16 v[32:35],a[64:67],v[72:75],v[32:35]
buffer_load_dwordx4 v0,s[36:39],s61 offen offset:3072 sc1 lds
v_mfma_f32_16x16x32_bf16 v[36:39],a[68:71],v[72:75],v[36:39]
s_waitcnt lgkmcnt(3)
v_exp_f32 v84,v84
v_mfma_f32_16x16x32_bf16 v[28:31],a[72:75],v[76:79],v[28:31]
s_add_u32 m0,s62,16384
ds_read_b128 v[64:67],v2 offset:11264
v_fma_f32 v84,v84,v119,v119
v_mfma_f32_16x16x32_bf16 v[32:35],a[76:79],v[76:79],v[32:35]
s_lshr_b32 s70,s70,6
v_rcp_f32 v84,v84
v_mfma_f32_16x16x32_bf16 v[36:39],a[80:83],v[76:79],v[36:39]
s_lshr_b32 s72,s61,5
v_fma_f32 v85,v118,v85,v117
v_mul_f32 v85,v41,v85
v_mfma_f32_16x16x32_bf16 v[28:31],a[84:87],v[80:83],v[28:31]
s_mov_b32 s62,s63
ds_read_b128 v[68:71],v2 offset:12288)"
R"(
v_fma_f32 v86,v118,v86,v117
v_mfma_f32_16x16x32_bf16 v[32:35],a[88:91],v[80:83],v[32:35]
v_exp_f32 v85,v85
v_mfma_f32_16x16x32_bf16 v[36:39],a[92:95],v[80:83],v[36:39]
s_waitcnt lgkmcnt(3)
v_mul_f32 v86,v42,v86
v_fma_f32 v85,v85,v119,v119
v_mfma_f32_16x16x32_bf16 v[28:31],a[96:99],v[52:55],v[28:31]
ds_read_b128 v[72:75],v2 offset:13312
v_fma_f32 v87,v118,v87,v117
v_mfma_f32_16x16x32_bf16 v[32:35],a[100:103],v[52:55],v[32:35]
v_exp_f32 v86,v86
v_mfma_f32_16x16x32_bf16 v[36:39],a[104:107],v[52:55],v[36:39]
v_rcp_f32 v85,v85
v_mfma_f32_16x16x32_bf16 v[28:31],a[108:111],v[56:59],v[28:31]
ds_read_b128 v[76:79],v2 offset:14336
v_mul_f32 v87,v43,v87
v_mfma_f32_16x16x32_bf16 v[32:35],a[112:115],v[56:59],v[32:35]
v_exp_f32 v87,v87
v_mfma_f32_16x16x32_bf16 v[36:39],a[116:119],v[56:59],v[36:39]
s_waitcnt lgkmcnt(3)
v_fma_f32 v86,v86,v119,v119
v_fma_f32 v87,v87,v119,v119
v_mfma_f32_16x16x32_bf16 v[28:31],a[120:123],v[60:63],v[28:31]
ds_read_b128 v[80:83],v2 offset:15360
v_mul_f32 v40,v40,v84
v_mfma_f32_16x16x32_bf16 v[32:35],a[124:127],v[60:63],v[32:35]
s_waitcnt vmcnt(7)
v_rcp_f32 v86,v86
v_mfma_f32_16x16x32_bf16 v[36:39],a[128:131],v[60:63],v[36:39]
s_barrier
v_mfma_f32_16x16x32_bf16 v[28:31],a[132:135],v[64:67],v[28:31]
ds_read_b128 v[52:55],v2 offset:16384
v_add_f32_dpp v104,v104,v104 quad_perm:[1,0,3,2] row_mask:0xf bank_mask:0xf
v_mfma_f32_16x16x32_bf16 v[32:35],a[136:139],v[64:67],v[32:35]
v_rcp_f32 v87,v87
v_mfma_f32_16x16x32_bf16 v[36:39],a[140:143],v[64:67],v[36:39]
s_waitcnt lgkmcnt(3)
v_add_f32_dpp v104,v104,v104 quad_perm:[2,3,0,1] row_mask:0xf bank_mask:0xf
v_mul_f32 v41,v41,v85
v_mfma_f32_16x16x32_bf16 v[28:31],a[144:147],v[68:71],v[28:31]
ds_read_b128 v[56:59],v2 offset:17408
v_add_f32_dpp v104,v104,v104 row_half_mirror row_mask:0xf bank_mask:0xf
v_mfma_f32_16x16x32_bf16 v[32:35],a[148:151],v[68:71],v[32:35]
v_mul_f32 v42,v42,v86
v_mul_f32 v43,v43,v87
v_mfma_f32_16x16x32_bf16 v[36:39],a[152:155],v[68:71],v[36:39]
v_cvt_pk_bf16_f32 v96,v40,v41
v_cvt_pk_bf16_f32 v97,v42,v43
v_mfma_f32_16x16x32_bf16 v[28:31],a[156:159],v[72:75],v[28:31]
ds_read_b128 v[60:63],v2 offset:18432
v_fma_f32 v44,-v116,v20,v44
v_mfma_f32_16x16x32_bf16 v[32:35],a[160:163],v[72:75],v[32:35]
v_fma_f32 v45,-v116,v21,v45
v_fma_f32 v46,-v116,v22,v46
v_mfma_f32_16x16x32_bf16 v[36:39],a[164:167],v[72:75],v[36:39]
s_waitcnt lgkmcnt(3)
v_fma_f32 v47,-v116,v23,v47
v_mul_f32 v88,v44,v44
v_mfma_f32_16x16x32_bf16 v[28:31],a[168:171],v[76:79],v[28:31]
ds_read_b128 v[64:67],v2 offset:19456
v_mul_f32 v89,v45,v45
v_mfma_f32_16x16x32_bf16 v[32:35],a[172:175],v[76:79],v[32:35]
v_mul_f32 v90,v46,v46
v_mul_f32 v91,v47,v47
v_mfma_f32_16x16x32_bf16 v[36:39],a[176:179],v[76:79],v[36:39]
v_fma_f32 v88,v118,v88,v117
v_mul_f32 v88,v44,v88
v_mfma_f32_16x16x32_bf16 v[28:31],a[180:183],v[80:83],v[28:31]
ds_read_b128 v[68:71],v2 offset:20480
v_fma_f32 v89,v118,v89,v117
v_mfma_f32_16x16x32_bf16 v[32:35],a[184:187],v[80:83],v[32:35]
buffer_load_dwordx4 v1,s[36:39],s61 offen offset:0 sc1 lds
v_mfma_f32_16x16x32_bf16 v[36:39],a[188:191],v[80:83],v[36:39]
s_waitcnt lgkmcnt(3)
ds_write_b32 v10,v104 offset:0
v_mfma_f32_16x16x32_bf16 v[28:31],a[192:195],v[52:55],v[28:31]
ds_read_b128 v[72:75],v2 offset:21504
v_mul_f32 v89,v45,v89
v_mfma_f32_16x16x32_bf16 v[32:35],a[196:199],v[52:55],v[32:35]
v_exp_f32 v88,v88
v_mfma_f32_16x16x32_bf16 v[36:39],a[200:203],v[52:55],v[36:39]
buffer_load_dwordx4 v1,s[36:39],s61 offen offset:1024 sc1 lds
v_mfma_f32_16x16x32_bf16 v[28:31],a[204:207],v[56:59],v[28:31]
ds_read_b128 v[76:79],v2 offset:22528
v_fma_f32 v88,v88,v119,v119
v_mfma_f32_16x16x32_bf16 v[32:35],a[208:211],v[56:59],v[32:35]
v_exp_f32 v89,v89
v_mfma_f32_16x16x32_bf16 v[36:39],a[212:215],v[56:59],v[36:39]
s_waitcnt lgkmcnt(4)
v_rcp_f32 v88,v88
v_mfma_f32_16x16x32_bf16 v[28:31],a[216:219],v[60:63],v[28:31]
buffer_load_dwordx4 v1,s[36:39],s61 offen offset:2048 sc1 lds
v_mfma_f32_16x16x32_bf16 v[32:35],a[220:223],v[60:63],v[32:35]
ds_read_b128 v[80:83],v2 offset:23552
v_fma_f32 v89,v89,v119,v119
v_mfma_f32_16x16x32_bf16 v[36:39],a[224:227],v[60:63],v[36:39]
v_rcp_f32 v89,v89
v_mfma_f32_16x16x32_bf16 v[28:31],a[228:231],v[64:67],v[28:31]
ds_read_b128 v[52:55],v2 offset:24576
v_fma_f32 v90,v118,v90,v117
v_mfma_f32_16x16x32_bf16 v[32:35],a[232:235],v[64:67],v[32:35]
buffer_load_dwordx4 v1,s[36:39],s61 offen offset:3072 sc1 lds
v_mfma_f32_16x16x32_bf16 v[36:39],a[236:239],v[64:67],v[36:39]
s_waitcnt lgkmcnt(3)
buffer_load_dword v108,v8,s[36:39],s72 offen offset:0 sc1
v_mfma_f32_16x16x32_bf16 v[28:31],a[240:243],v[68:71],v[28:31]
ds_read_b128 v[56:59],v2 offset:25600
v_mul_f32 v90,v46,v90
v_mfma_f32_16x16x32_bf16 v[32:35],a[244:247],v[68:71],v[32:35]
s_waitcnt vmcnt(9)
v_exp_f32 v90,v90
v_mfma_f32_16x16x32_bf16 v[36:39],a[248:251],v[68:71],v[36:39]
s_barrier
v_mfma_f32_16x16x32_bf16 v[28:31],a[252:255],v[72:75],v[28:31]
buffer_store_dword v5,v4,s[36:39],s70 offen offset:0 sc0 sc1
ds_read_b128 v[60:63],v2 offset:26624
v_fma_f32 v90,v90,v119,v119
v_fma_f32 v91,v118,v91,v117
v_mfma_f32_16x16x32_bf16 v[32:35],v[128:131],v[72:75],v[32:35]
v_rcp_f32 v90,v90
v_mfma_f32_16x16x32_bf16 v[36:39],v[132:135],v[72:75],v[36:39]
s_waitcnt lgkmcnt(3)
v_mul_f32 v91,v47,v91
v_mul_f32 v44,v44,v88
v_mfma_f32_16x16x32_bf16 v[28:31],v[136:139],v[76:79],v[28:31]
ds_read_b128 v[64:67],v2 offset:27648
v_mul_f32 v45,v45,v89
v_mfma_f32_16x16x32_bf16 v[32:35],v[140:143],v[76:79],v[32:35]
v_exp_f32 v91,v91
v_mfma_f32_16x16x32_bf16 v[36:39],v[144:147],v[76:79],v[36:39]
v_fma_f32 v91,v91,v119,v119
v_mul_f32 v46,v46,v90
v_mfma_f32_16x16x32_bf16 v[28:31],v[148:151],v[80:83],v[28:31]
ds_read_b128 v[68:71],v2 offset:28672
v_cvt_pk_bf16_f32 v98,v44,v45
v_mfma_f32_16x16x32_bf16 v[32:35],v[152:155],v[80:83],v[32:35]
v_rcp_f32 v91,v91
v_mfma_f32_16x16x32_bf16 v[36:39],v[156:159],v[80:83],v[36:39]
s_waitcnt lgkmcnt(3)
v_mul_f32 v47,v47,v91
v_cvt_pk_bf16_f32 v99,v46,v47
v_mfma_f32_16x16x32_bf16 v[28:31],v[160:163],v[52:55],v[28:31]
ds_read_b128 v[72:75],v2 offset:29696
v_fma_f32 v48,-v116,v24,v48
v_mfma_f32_16x16x32_bf16 v[32:35],v[164:167],v[52:55],v[32:35]
v_fma_f32 v49,-v116,v25,v49
v_fma_f32 v50,-v116,v26,v50
v_mfma_f32_16x16x32_bf16 v[36:39],v[168:171],v[52:55],v[36:39]
v_fma_f32 v51,-v116,v27,v51
v_mul_f32 v92,v48,v48
v_mfma_f32_16x16x32_bf16 v[28:31],v[172:175],v[56:59],v[28:31]
ds_read_b128 v[76:79],v2 offset:30720
v_mul_f32 v93,v49,v49
v_mfma_f32_16x16x32_bf16 v[32:35],v[176:179],v[56:59],v[32:35]
buffer_store_dwordx4 v[96:99],v3,s[36:39],s73 offen offset:0 sc0 sc1
v_mul_f32 v94,v50,v50
v_mul_f32 v95,v51,v51
v_fma_f32 v92,v118,v92,v117
v_mfma_f32_16x16x32_bf16 v[36:39],v[180:183],v[56:59],v[36:39]
s_waitcnt lgkmcnt(3)
v_mul_f32 v92,v48,v92
v_fma_f32 v93,v118,v93,v117
v_mfma_f32_16x16x32_bf16 v[28:31],v[184:187],v[60:63],v[28:31]
ds_read_b128 v[80:83],v2 offset:31744
v_mul_f32 v93,v49,v93
v_mfma_f32_16x16x32_bf16 v[32:35],v[188:191],v[60:63],v[32:35]
s_waitcnt vmcnt(7)
v_exp_f32 v92,v92
v_mfma_f32_16x16x32_bf16 v[36:39],v[192:195],v[60:63],v[36:39]
s_barrier
v_mfma_f32_16x16x32_bf16 v[28:31],v[196:199],v[64:67],v[28:31]
ds_read_b128 v[52:55],v2 offset:32768
ds_read_b32 v13,v7 offset:0
v_mfma_f32_16x16x32_bf16 v[32:35],v[200:203],v[64:67],v[32:35]
ds_read_b64 v[112:113],v11 offset:0
v_fma_f32 v92,v92,v119,v119
v_mfma_f32_16x16x32_bf16 v[36:39],v[204:207],v[64:67],v[36:39]
s_waitcnt lgkmcnt(5)
v_exp_f32 v93,v93
v_mfma_f32_16x16x32_bf16 v[28:31],v[208:211],v[68:71],v[28:31]
ds_read_b128 v[56:59],v2 offset:33792
v_fma_f32 v93,v93,v119,v119
v_mfma_f32_16x16x32_bf16 v[32:35],v[212:215],v[68:71],v[32:35]
s_waitcnt lgkmcnt(2)
v_rcp_f32 v92,v92
v_mfma_f32_16x16x32_bf16 v[36:39],v[216:219],v[68:71],v[36:39]
s_waitcnt lgkmcnt(1)
v_cmp_eq_u32 vcc,s60,v13
v_rcp_f32 v93,v93
v_fma_f32 v94,v118,v94,v117
v_mfma_f32_16x16x32_bf16 v[28:31],v[220:223],v[72:75],v[28:31]
s_cmp_eq_u32 vcc_lo,-1
ds_read_b128 v[60:63],v2 offset:34816
v_mul_f32 v94,v50,v94
v_mfma_f32_16x16x32_bf16 v[32:35],v[224:227],v[72:75],v[32:35]
s_cselect_b32 s67,s86,s59
v_exp_f32 v94,v94
v_mfma_f32_16x16x32_bf16 v[36:39],v[228:231],v[72:75],v[36:39]
v_fma_f32 v94,v94,v119,v119)"
R"(
v_fma_f32 v95,v118,v95,v117
v_mfma_f32_16x16x32_bf16 v[28:31],v[232:235],v[76:79],v[28:31]
s_lshr_b32 s61,s67,0
v_mul_f32 v95,v51,v95
v_exp_f32 v95,v95
v_mfma_f32_16x16x32_bf16 v[32:35],v[236:239],v[76:79],v[32:35]
s_xor_b32 s63,s63,65536
v_fma_f32 v95,v95,v119,v119
v_rcp_f32 v94,v94
v_rcp_f32 v95,v95
v_mfma_f32_16x16x32_bf16 v[36:39],v[240:243],v[76:79],v[36:39]
s_cmp_ge_u32 s66,s59
ds_read_b128 v[64:67],v2 offset:35840
v_mul_f32 v48,v48,v92
v_mul_f32 v49,v49,v93
v_mul_f32 v50,v50,v94
v_mul_f32 v51,v51,v95
v_cvt_pk_bf16_f32 v100,v48,v49
v_cvt_pk_bf16_f32 v101,v50,v51
v_mfma_f32_16x16x32_bf16 v[28:31],v[244:247],v[80:83],v[28:31]
buffer_store_dwordx2 v[100:101],v9,s[36:39],s73 offen offset:0 sc0 sc1
ds_read_b128 v[68:71],v2 offset:36864
v_mfma_f32_16x16x32_bf16 v[32:35],v[248:251],v[80:83],v[32:35]
s_waitcnt lgkmcnt(3)
v_mfma_f32_16x16x32_bf16 v[36:39],v[252:255],v[80:83],v[36:39]
s_cbranch_scc1 Lg3_slow1_%=
Lg3_d1w3_%=:
v_mfma_f32_16x16x32_bf16 v[40:43],a[0:3],v[52:55],0
s_mov_b32 m0,s62
ds_read_b128 v[72:75],v2 offset:37888
v_mul_f32 v116,s75,v112
v_mfma_f32_16x16x32_bf16 v[44:47],a[4:7],v[52:55],0
s_mul_i32 s73,s48,7
v_fma_f32 v113,v113,s75,v12
v_fma_f32 v113,-v116,v116,v113
v_mfma_f32_16x16x32_bf16 v[48:51],a[8:11],v[52:55],0
s_lshr_b32 s73,s73,1
v_max_f32 v113,v113,v12
v_rsq_f32 v112,v113
v_mfma_f32_16x16x32_bf16 v[40:43],a[12:15],v[56:59],v[40:43]
s_add_u32 s74,s53,s55
ds_read_b128 v[76:79],v2 offset:38912
v_mul_f32 v117,s76,v112
v_mfma_f32_16x16x32_bf16 v[44:47],a[16:19],v[56:59],v[44:47]
buffer_load_dwordx4 v0,s[36:39],s61 offen offset:0 sc1 lds
v_mfma_f32_16x16x32_bf16 v[48:51],a[20:23],v[56:59],v[48:51]
s_waitcnt lgkmcnt(3)
v_mul_f32 v118,v112,v112
v_mul_f32 v119,v113,v112
v_mfma_f32_16x16x32_bf16 v[40:43],a[24:27],v[60:63],v[40:43]
s_cmp_eq_u32 s74,s56
ds_read_b128 v[80:83],v2 offset:39936
v_mul_f32 v118,s77,v118
v_mfma_f32_16x16x32_bf16 v[44:47],a[28:31],v[60:63],v[44:47]
s_cselect_b32 s74,s57,s74
v_mul_f32 v118,v118,v117
v_fma_f32 v28,-v116,v16,v28
v_mfma_f32_16x16x32_bf16 v[48:51],a[32:35],v[60:63],v[48:51]
buffer_load_dwordx4 v0,s[36:39],s61 offen offset:1024 sc1 lds
v_mfma_f32_16x16x32_bf16 v[40:43],a[36:39],v[64:67],v[40:43]
s_cmp_eq_u32 s74,s58
ds_read_b128 v[52:55],v2 offset:40960
v_fma_f32 v29,-v116,v17,v29
v_mfma_f32_16x16x32_bf16 v[44:47],a[40:43],v[64:67],v[44:47]
s_cselect_b32 s74,s59,s74
v_fma_f32 v30,-v116,v18,v30
v_fma_f32 v31,-v116,v19,v31
v_mfma_f32_16x16x32_bf16 v[48:51],a[44:47],v[64:67],v[48:51]
s_waitcnt lgkmcnt(3)
v_mul_f32 v84,v28,v28
v_mul_f32 v85,v29,v29
v_mfma_f32_16x16x32_bf16 v[40:43],a[48:51],v[68:71],v[40:43]
buffer_load_dwordx4 v0,s[36:39],s61 offen offset:2048 sc1 lds
v_mfma_f32_16x16x32_bf16 v[44:47],a[52:55],v[68:71],v[44:47]
s_cmp_ge_u32 s67,s59
ds_read_b128 v[56:59],v2 offset:41984
v_mul_f32 v86,v30,v30
v_mfma_f32_16x16x32_bf16 v[48:51],a[56:59],v[68:71],v[48:51]
s_cselect_b32 s86,s50,s51
v_mul_f32 v87,v31,v31
v_fma_f32 v84,v118,v84,v117
v_mfma_f32_16x16x32_bf16 v[40:43],a[60:63],v[72:75],v[40:43]
s_mul_i32 s70,s47,5
ds_read_b128 v[60:63],v2 offset:43008
v_mul_f32 v84,v28,v84
v_mfma_f32_16x16x32_bf16 v[44:47],a[64:67],v[72:75],v[44:47]
buffer_load_dwordx4 v0,s[36:39],s61 offen offset:3072 sc1 lds
v_mfma_f32_16x16x32_bf16 v[48:51],a[68:71],v[72:75],v[48:51]
s_waitcnt lgkmcnt(3)
v_exp_f32 v84,v84
v_mfma_f32_16x16x32_bf16 v[40:43],a[72:75],v[76:79],v[40:43]
s_add_u32 m0,s62,16384
ds_read_b128 v[64:67],v2 offset:44032
v_fma_f32 v84,v84,v119,v119
v_mfma_f32_16x16x32_bf16 v[44:47],a[76:79],v[76:79],v[44:47]
s_lshr_b32 s70,s70,6
v_rcp_f32 v84,v84
v_mfma_f32_16x16x32_bf16 v[48:51],a[80:83],v[76:79],v[48:51]
s_lshr_b32 s72,s61,5
v_fma_f32 v85,v118,v85,v117
v_mul_f32 v85,v29,v85
v_mfma_f32_16x16x32_bf16 v[40:43],a[84:87],v[80:83],v[40:43]
s_mov_b32 s62,s64
ds_read_b128 v[68:71],v2 offset:45056
v_fma_f32 v86,v118,v86,v117
v_mfma_f32_16x16x32_bf16 v[44:47],a[88:91],v[80:83],v[44:47]
s_mov_b64 s[44:45],s[46:47]
v_exp_f32 v85,v85
v_mfma_f32_16x16x32_bf16 v[48:51],a[92:95],v[80:83],v[48:51]
s_waitcnt lgkmcnt(3)
v_mul_f32 v86,v30,v86
v_fma_f32 v85,v85,v119,v119
v_mfma_f32_16x16x32_bf16 v[40:43],a[96:99],v[52:55],v[40:43]
s_mov_b64 s[46:47],s[48:49]
ds_read_b128 v[72:75],v2 offset:46080
v_exp_f32 v86,v86
v_mfma_f32_16x16x32_bf16 v[44:47],a[100:103],v[52:55],v[44:47]
s_mov_b64 s[48:49],s[50:51]
v_rcp_f32 v85,v85
v_mfma_f32_16x16x32_bf16 v[48:51],a[104:107],v[52:55],v[48:51]
s_mov_b64 s[50:51],s[52:53]
v_fma_f32 v86,v86,v119,v119
v_fma_f32 v87,v118,v87,v117
v_mfma_f32_16x16x32_bf16 v[40:43],a[108:111],v[56:59],v[40:43]
s_mov_b32 s52,s74
ds_read_b128 v[76:79],v2 offset:47104
v_mul_f32 v87,v31,v87
v_mfma_f32_16x16x32_bf16 v[44:47],a[112:115],v[56:59],v[44:47]
v_exp_f32 v87,v87
v_mfma_f32_16x16x32_bf16 v[48:51],a[116:119],v[56:59],v[48:51]
s_waitcnt lgkmcnt(3)
v_rcp_f32 v86,v86
v_mfma_f32_16x16x32_bf16 v[40:43],a[120:123],v[60:63],v[40:43]
ds_read_b128 v[80:83],v2 offset:48128
v_fma_f32 v87,v87,v119,v119
v_mfma_f32_16x16x32_bf16 v[44:47],a[124:127],v[60:63],v[44:47]
s_waitcnt vmcnt(7)
v_rcp_f32 v87,v87
v_mfma_f32_16x16x32_bf16 v[48:51],a[128:131],v[60:63],v[48:51]
s_barrier
v_mfma_f32_16x16x32_bf16 v[40:43],a[132:135],v[64:67],v[40:43]
ds_read_b128 v[52:55],v2 offset:49152
v_add_f32_dpp v108,v108,v108 quad_perm:[1,0,3,2] row_mask:0xf bank_mask:0xf
v_mfma_f32_16x16x32_bf16 v[44:47],a[136:139],v[64:67],v[44:47]
s_nop 0
v_add_f32_dpp v108,v108,v108 quad_perm:[2,3,0,1] row_mask:0xf bank_mask:0xf
v_mul_f32 v28,v28,v84
v_mfma_f32_16x16x32_bf16 v[48:51],a[140:143],v[64:67],v[48:51]
s_waitcnt lgkmcnt(3)
v_add_f32_dpp v108,v108,v108 row_half_mirror row_mask:0xf bank_mask:0xf
v_mul_f32 v29,v29,v85
v_mfma_f32_16x16x32_bf16 v[40:43],a[144:147],v[68:71],v[40:43]
ds_read_b128 v[56:59],v2 offset:50176
v_mul_f32 v30,v30,v86
v_mfma_f32_16x16x32_bf16 v[44:47],a[148:151],v[68:71],v[44:47]
v_mul_f32 v31,v31,v87
v_cvt_pk_bf16_f32 v96,v28,v29
v_mfma_f32_16x16x32_bf16 v[48:51],a[152:155],v[68:71],v[48:51]
v_cvt_pk_bf16_f32 v97,v30,v31
v_fma_f32 v32,-v116,v20,v32
v_mfma_f32_16x16x32_bf16 v[40:43],a[156:159],v[72:75],v[40:43]
ds_read_b128 v[60:63],v2 offset:51200
v_fma_f32 v33,-v116,v21,v33
v_mfma_f32_16x16x32_bf16 v[44:47],a[160:163],v[72:75],v[44:47]
v_fma_f32 v34,-v116,v22,v34
v_fma_f32 v35,-v116,v23,v35
v_mfma_f32_16x16x32_bf16 v[48:51],a[164:167],v[72:75],v[48:51]
s_waitcnt lgkmcnt(3)
v_mul_f32 v88,v32,v32
v_mul_f32 v89,v33,v33
v_mfma_f32_16x16x32_bf16 v[40:43],a[168:171],v[76:79],v[40:43]
ds_read_b128 v[64:67],v2 offset:52224
v_mul_f32 v90,v34,v34
v_mfma_f32_16x16x32_bf16 v[44:47],a[172:175],v[76:79],v[44:47]
v_mul_f32 v91,v35,v35
v_fma_f32 v88,v118,v88,v117
v_mfma_f32_16x16x32_bf16 v[48:51],a[176:179],v[76:79],v[48:51]
v_mul_f32 v88,v32,v88
v_fma_f32 v89,v118,v89,v117
v_mfma_f32_16x16x32_bf16 v[40:43],a[180:183],v[80:83],v[40:43]
ds_read_b128 v[68:71],v2 offset:53248
v_mul_f32 v89,v33,v89
v_mfma_f32_16x16x32_bf16 v[44:47],a[184:187],v[80:83],v[44:47]
buffer_load_dwordx4 v1,s[36:39],s61 offen offset:0 sc1 lds
v_mfma_f32_16x16x32_bf16 v[48:51],a[188:191],v[80:83],v[48:51]
s_waitcnt lgkmcnt(3)
ds_write_b32 v10,v108 offset:128
v_fma_f32 v90,v118,v90,v117
v_mfma_f32_16x16x32_bf16 v[40:43],a[192:195],v[52:55],v[40:43]
ds_read_b128 v[72:75],v2 offset:54272
v_fma_f32 v91,v118,v91,v117
v_mfma_f32_16x16x32_bf16 v[44:47],a[196:199],v[52:55],v[44:47]
v_exp_f32 v88,v88
v_mfma_f32_16x16x32_bf16 v[48:51],a[200:203],v[52:55],v[48:51]
buffer_load_dwordx4 v1,s[36:39],s61 offen offset:1024 sc1 lds
v_mfma_f32_16x16x32_bf16 v[40:43],a[204:207],v[56:59],v[40:43]
ds_read_b128 v[76:79],v2 offset:55296
v_mul_f32 v90,v34,v90
v_mfma_f32_16x16x32_bf16 v[44:47],a[208:211],v[56:59],v[44:47]
v_exp_f32 v89,v89)"
R"(
v_mfma_f32_16x16x32_bf16 v[48:51],a[212:215],v[56:59],v[48:51]
s_waitcnt lgkmcnt(4)
v_exp_f32 v90,v90
v_mfma_f32_16x16x32_bf16 v[40:43],a[216:219],v[60:63],v[40:43]
buffer_load_dwordx4 v1,s[36:39],s61 offen offset:2048 sc1 lds
v_mfma_f32_16x16x32_bf16 v[44:47],a[220:223],v[60:63],v[44:47]
ds_read_b128 v[80:83],v2 offset:56320
v_mul_f32 v91,v35,v91
v_mfma_f32_16x16x32_bf16 v[48:51],a[224:227],v[60:63],v[48:51]
v_exp_f32 v91,v91
v_mfma_f32_16x16x32_bf16 v[40:43],a[228:231],v[64:67],v[40:43]
ds_read_b128 v[52:55],v2 offset:57344
v_fma_f32 v88,v88,v119,v119
v_mfma_f32_16x16x32_bf16 v[44:47],a[232:235],v[64:67],v[44:47]
buffer_load_dwordx4 v1,s[36:39],s61 offen offset:3072 sc1 lds
v_mfma_f32_16x16x32_bf16 v[48:51],a[236:239],v[64:67],v[48:51]
s_waitcnt lgkmcnt(3)
buffer_load_dword v104,v8,s[36:39],s72 offen offset:0 sc1
v_mfma_f32_16x16x32_bf16 v[40:43],a[240:243],v[68:71],v[40:43]
ds_read_b128 v[56:59],v2 offset:58368
v_fma_f32 v89,v89,v119,v119
v_mfma_f32_16x16x32_bf16 v[44:47],a[244:247],v[68:71],v[44:47]
s_waitcnt vmcnt(9)
v_rcp_f32 v88,v88
v_mfma_f32_16x16x32_bf16 v[48:51],a[248:251],v[68:71],v[48:51]
s_barrier
v_mfma_f32_16x16x32_bf16 v[40:43],a[252:255],v[72:75],v[40:43]
buffer_store_dword v5,v4,s[36:39],s70 offen offset:0 sc0 sc1
ds_read_b128 v[60:63],v2 offset:59392
v_rcp_f32 v89,v89
v_mfma_f32_16x16x32_bf16 v[44:47],v[128:131],v[72:75],v[44:47]
v_fma_f32 v90,v90,v119,v119
v_fma_f32 v91,v91,v119,v119
v_mfma_f32_16x16x32_bf16 v[48:51],v[132:135],v[72:75],v[48:51]
s_waitcnt lgkmcnt(3)
v_rcp_f32 v90,v90
v_mfma_f32_16x16x32_bf16 v[40:43],v[136:139],v[76:79],v[40:43]
ds_read_b128 v[64:67],v2 offset:60416
v_mul_f32 v32,v32,v88
v_mfma_f32_16x16x32_bf16 v[44:47],v[140:143],v[76:79],v[44:47]
v_rcp_f32 v91,v91
v_mfma_f32_16x16x32_bf16 v[48:51],v[144:147],v[76:79],v[48:51]
v_mul_f32 v33,v33,v89
v_mul_f32 v34,v34,v90
v_mfma_f32_16x16x32_bf16 v[40:43],v[148:151],v[80:83],v[40:43]
ds_read_b128 v[68:71],v2 offset:61440
v_mul_f32 v35,v35,v91
v_mfma_f32_16x16x32_bf16 v[44:47],v[152:155],v[80:83],v[44:47]
v_cvt_pk_bf16_f32 v98,v32,v33
v_cvt_pk_bf16_f32 v99,v34,v35
v_fma_f32 v36,-v116,v24,v36
v_mfma_f32_16x16x32_bf16 v[48:51],v[156:159],v[80:83],v[48:51]
s_waitcnt lgkmcnt(3)
v_fma_f32 v37,-v116,v25,v37
v_fma_f32 v38,-v116,v26,v38
v_mfma_f32_16x16x32_bf16 v[40:43],v[160:163],v[52:55],v[40:43]
ds_read_b128 v[72:75],v2 offset:62464
v_fma_f32 v39,-v116,v27,v39
v_mfma_f32_16x16x32_bf16 v[44:47],v[164:167],v[52:55],v[44:47]
v_mul_f32 v92,v36,v36
v_mul_f32 v93,v37,v37
v_mfma_f32_16x16x32_bf16 v[48:51],v[168:171],v[52:55],v[48:51]
v_mul_f32 v94,v38,v38
v_mul_f32 v95,v39,v39
v_mfma_f32_16x16x32_bf16 v[40:43],v[172:175],v[56:59],v[40:43]
ds_read_b128 v[76:79],v2 offset:63488
v_fma_f32 v92,v118,v92,v117
v_mfma_f32_16x16x32_bf16 v[44:47],v[176:179],v[56:59],v[44:47]
buffer_store_dwordx4 v[96:99],v3,s[36:39],s73 offen offset:0 sc0 sc1
v_mul_f32 v92,v36,v92
v_exp_f32 v92,v92
v_mfma_f32_16x16x32_bf16 v[48:51],v[180:183],v[56:59],v[48:51]
s_waitcnt lgkmcnt(3)
v_fma_f32 v92,v92,v119,v119
v_fma_f32 v93,v118,v93,v117
v_mfma_f32_16x16x32_bf16 v[40:43],v[184:187],v[60:63],v[40:43]
ds_read_b128 v[80:83],v2 offset:64512
v_mul_f32 v93,v37,v93
v_mfma_f32_16x16x32_bf16 v[44:47],v[188:191],v[60:63],v[44:47]
s_waitcnt vmcnt(7)
v_xor_b32 v2,65536,v2
v_fma_f32 v94,v118,v94,v117
v_mfma_f32_16x16x32_bf16 v[48:51],v[192:195],v[60:63],v[48:51]
s_barrier
v_mfma_f32_16x16x32_bf16 v[40:43],v[196:199],v[64:67],v[40:43]
ds_read_b128 v[52:55],v2 offset:0
ds_read_b32 v13,v7 offset:256
v_mfma_f32_16x16x32_bf16 v[44:47],v[200:203],v[64:67],v[44:47]
ds_read_b64 v[112:113],v11 offset:128
v_fma_f32 v95,v118,v95,v117
v_mfma_f32_16x16x32_bf16 v[48:51],v[204:207],v[64:67],v[48:51]
s_waitcnt lgkmcnt(5)
v_exp_f32 v93,v93
v_mfma_f32_16x16x32_bf16 v[40:43],v[208:211],v[68:71],v[40:43]
ds_read_b128 v[56:59],v2 offset:1024
v_mul_f32 v94,v38,v94
v_mfma_f32_16x16x32_bf16 v[44:47],v[212:215],v[68:71],v[44:47]
s_waitcnt lgkmcnt(2)
v_exp_f32 v94,v94
v_mfma_f32_16x16x32_bf16 v[48:51],v[216:219],v[68:71],v[48:51]
s_waitcnt lgkmcnt(1)
v_cmp_eq_u32 vcc,s60,v13
v_rcp_f32 v92,v92
v_mul_f32 v95,v39,v95
v_mfma_f32_16x16x32_bf16 v[40:43],v[220:223],v[72:75],v[40:43]
s_cmp_eq_u32 vcc_lo,-1
ds_read_b128 v[60:63],v2 offset:2048
v_fma_f32 v93,v93,v119,v119
v_mfma_f32_16x16x32_bf16 v[44:47],v[224:227],v[72:75],v[44:47]
s_cselect_b32 s66,s86,s59
v_exp_f32 v95,v95
v_mfma_f32_16x16x32_bf16 v[48:51],v[228:231],v[72:75],v[48:51]
v_rcp_f32 v93,v93
v_mfma_f32_16x16x32_bf16 v[40:43],v[232:235],v[76:79],v[40:43]
s_lshr_b32 s61,s66,0
ds_read_b128 v[64:67],v2 offset:3072
v_fma_f32 v94,v94,v119,v119
v_mfma_f32_16x16x32_bf16 v[44:47],v[236:239],v[76:79],v[44:47]
s_xor_b32 s64,s64,65536
v_fma_f32 v95,v95,v119,v119
v_rcp_f32 v94,v94
v_rcp_f32 v95,v95
v_mfma_f32_16x16x32_bf16 v[48:51],v[240:243],v[76:79],v[48:51]
s_cmp_lt_u32 s67,s59
v_mul_f32 v36,v36,v92
v_mul_f32 v37,v37,v93
v_mul_f32 v38,v38,v94
v_mul_f32 v39,v39,v95
v_cvt_pk_bf16_f32 v100,v36,v37
v_cvt_pk_bf16_f32 v101,v38,v39
v_mfma_f32_16x16x32_bf16 v[40:43],v[244:247],v[80:83],v[40:43]
buffer_store_dwordx2 v[100:101],v9,s[36:39],s73 offen offset:0 sc0 sc1
ds_read_b128 v[68:71],v2 offset:4096
v_mfma_f32_16x16x32_bf16 v[44:47],v[248:251],v[80:83],v[44:47]
s_waitcnt lgkmcnt(3)
v_mfma_f32_16x16x32_bf16 v[48:51],v[252:255],v[80:83],v[48:51]
s_cbranch_scc1 Lg3_d0w3_%=
s_branch Lg3_slow0_%=
Lg3_slow0_%=:
s_cmp_lt_u32 s66,s59
s_cbranch_scc1 Lg3_gs0_%=
Lg3_slowb0_%=:
s_cmp_eq_u32 s54,1
s_cselect_b32 s68,s49,s48
s_lshr_b32 s68,s68,5
s_add_u32 m0,s78,8192
s_nop 0
buffer_load_dword v6,s[36:39],s68 offen offset:0 sc1 lds
s_mul_i32 s73,s47,7
s_lshr_b32 s73,s73,1
v_mul_f32 v116,s75,v112
v_fma_f32 v113,v113,s75,v12
v_fma_f32 v113,-v116,v116,v113
v_max_f32 v113,v113,v12
v_rsq_f32 v112,v113
s_nop 0
v_mul_f32 v117,s76,v112
v_mul_f32 v118,v112,v112
v_mul_f32 v119,v113,v112
v_mul_f32 v118,s77,v118
v_mul_f32 v118,v118,v117
v_fma_f32 v40,-v116,v16,v40
v_fma_f32 v41,-v116,v17,v41
v_fma_f32 v42,-v116,v18,v42
v_fma_f32 v43,-v116,v19,v43
v_mul_f32 v84,v40,v40
v_mul_f32 v85,v41,v41
v_mul_f32 v86,v42,v42
v_mul_f32 v87,v43,v43
v_fma_f32 v84,v118,v84,v117
v_fma_f32 v85,v118,v85,v117
v_fma_f32 v86,v118,v86,v117
v_fma_f32 v87,v118,v87,v117
v_mul_f32 v84,v40,v84
v_mul_f32 v85,v41,v85
v_mul_f32 v86,v42,v86
v_mul_f32 v87,v43,v87
v_exp_f32 v84,v84
v_exp_f32 v85,v85
v_exp_f32 v86,v86
v_exp_f32 v87,v87
v_fma_f32 v84,v84,v119,v119
v_fma_f32 v85,v85,v119,v119
v_fma_f32 v86,v86,v119,v119
v_fma_f32 v87,v87,v119,v119
v_rcp_f32 v84,v84
v_rcp_f32 v85,v85
v_rcp_f32 v86,v86
v_rcp_f32 v87,v87
v_mul_f32 v40,v40,v84
v_mul_f32 v41,v41,v85
v_mul_f32 v42,v42,v86
v_mul_f32 v43,v43,v87
v_cvt_pk_bf16_f32 v96,v40,v41
v_cvt_pk_bf16_f32 v97,v42,v43
v_fma_f32 v44,-v116,v20,v44
v_fma_f32 v45,-v116,v21,v45
v_fma_f32 v46,-v116,v22,v46
v_fma_f32 v47,-v116,v23,v47
v_mul_f32 v88,v44,v44)"
R"(
v_mul_f32 v89,v45,v45
v_mul_f32 v90,v46,v46
v_mul_f32 v91,v47,v47
v_fma_f32 v88,v118,v88,v117
v_fma_f32 v89,v118,v89,v117
v_fma_f32 v90,v118,v90,v117
v_fma_f32 v91,v118,v91,v117
v_mul_f32 v88,v44,v88
s_add_u32 m0,s78,8448
s_nop 0
buffer_load_dword v6,s[36:39],s68 offen offset:0 sc1 lds
v_mul_f32 v89,v45,v89
v_mul_f32 v90,v46,v90
v_mul_f32 v91,v47,v91
v_exp_f32 v88,v88
v_exp_f32 v89,v89
v_exp_f32 v90,v90
v_exp_f32 v91,v91
v_fma_f32 v88,v88,v119,v119
v_fma_f32 v89,v89,v119,v119
v_fma_f32 v90,v90,v119,v119
v_fma_f32 v91,v91,v119,v119
v_rcp_f32 v88,v88
v_rcp_f32 v89,v89
v_rcp_f32 v90,v90
v_rcp_f32 v91,v91
v_mul_f32 v44,v44,v88
v_mul_f32 v45,v45,v89
v_mul_f32 v46,v46,v90
v_mul_f32 v47,v47,v91
v_cvt_pk_bf16_f32 v98,v44,v45
v_cvt_pk_bf16_f32 v99,v46,v47
buffer_store_dwordx4 v[96:99],v3,s[36:39],s73 offen offset:0 sc0 sc1
v_fma_f32 v48,-v116,v24,v48
v_fma_f32 v49,-v116,v25,v49
v_fma_f32 v50,-v116,v26,v50
v_fma_f32 v51,-v116,v27,v51
v_mul_f32 v92,v48,v48
v_mul_f32 v93,v49,v49
v_mul_f32 v94,v50,v50
v_mul_f32 v95,v51,v51
v_fma_f32 v92,v118,v92,v117
v_fma_f32 v93,v118,v93,v117
v_fma_f32 v94,v118,v94,v117
v_fma_f32 v95,v118,v95,v117
v_mul_f32 v92,v48,v92
v_mul_f32 v93,v49,v93
v_mul_f32 v94,v50,v94
v_mul_f32 v95,v51,v95
v_exp_f32 v92,v92
v_exp_f32 v93,v93
v_exp_f32 v94,v94
v_exp_f32 v95,v95
v_fma_f32 v92,v92,v119,v119
v_fma_f32 v93,v93,v119,v119
v_fma_f32 v94,v94,v119,v119
v_fma_f32 v95,v95,v119,v119
v_rcp_f32 v92,v92
v_rcp_f32 v93,v93
v_rcp_f32 v94,v94
v_rcp_f32 v95,v95
v_mul_f32 v48,v48,v92
v_mul_f32 v49,v49,v93
v_mul_f32 v50,v50,v94
v_mul_f32 v51,v51,v95
v_cvt_pk_bf16_f32 v100,v48,v49
v_cvt_pk_bf16_f32 v101,v50,v51
buffer_store_dwordx2 v[100:101],v9,s[36:39],s73 offen offset:0 sc0 sc1
s_cmp_ge_u32 s48,s59
s_cbranch_scc1 Lg3_done0_%=
s_add_u32 m0,s78,16384
s_nop 0
buffer_load_dword v6,s[36:39],s68 offen offset:0 sc1 lds
s_waitcnt vmcnt(4)
s_barrier
ds_read_b32 v13,v7 offset:8192
s_mul_i32 s70,s46,5
s_lshr_b32 s70,s70,6
buffer_store_dword v5,v4,s[36:39],s70 offen offset:0 sc0 sc1
s_mov_b32 s46,s59
s_mov_b32 s69,18432
s_waitcnt lgkmcnt(0)
v_cmp_eq_u32 vcc,s60,v13
s_nop 0
s_cmp_eq_u32 vcc_lo,-1
s_cbranch_scc1 Lg3_go0_%=
s_add_u32 m0,s78,16640
s_nop 0
buffer_load_dword v6,s[36:39],s68 offen offset:0 sc1 lds
s_waitcnt vmcnt(5)
s_barrier
ds_read_b32 v13,v7 offset:8448
s_mov_b32 s69,18688
s_waitcnt lgkmcnt(0)
v_cmp_eq_u32 vcc,s60,v13
s_nop 0
s_cmp_eq_u32 vcc_lo,-1
s_cbranch_scc1 Lg3_go0_%=
s_add_u32 m0,s78,8192
s_nop 0
buffer_load_dword v6,s[36:39],s68 offen offset:0 sc1 lds
s_waitcnt vmcnt(3)
s_barrier
ds_read_b32 v13,v7 offset:16384
s_mul_i32 s70,s47,5
s_lshr_b32 s70,s70,6
buffer_store_dword v5,v4,s[36:39],s70 offen offset:0 sc0 sc1
s_mov_b32 s47,s59
s_mov_b32 s69,10240
s_waitcnt lgkmcnt(0)
v_cmp_eq_u32 vcc,s60,v13
s_nop 0
s_cmp_eq_u32 vcc_lo,-1
s_cbranch_scc1 Lg3_go0_%=
s_add_u32 m0,s78,8448
s_nop 0
buffer_load_dword v6,s[36:39],s68 offen offset:0 sc1 lds
s_waitcnt vmcnt(3)
s_barrier
ds_read_b32 v13,v7 offset:16640
s_mov_b32 s69,10496
s_waitcnt lgkmcnt(0)
v_cmp_eq_u32 vcc,s60,v13
s_nop 0
s_cmp_eq_u32 vcc_lo,-1
s_cbranch_scc1 Lg3_go0_%=
Lg3_poll0_%=:
s_add_u32 m0,s78,16384
s_nop 0
buffer_load_dword v6,s[36:39],s68 offen offset:0 sc1 lds
s_waitcnt vmcnt(2)
s_barrier
ds_read_b32 v13,v7 offset:8192
s_mov_b32 s69,18432
s_waitcnt lgkmcnt(0)
v_cmp_eq_u32 vcc,s60,v13
s_nop 0
s_cmp_eq_u32 vcc_lo,-1
s_cbranch_scc1 Lg3_go0_%=
s_sleep 4
s_add_u32 m0,s78,16640
s_nop 0
buffer_load_dword v6,s[36:39],s68 offen offset:0 sc1 lds
s_waitcnt vmcnt(2)
s_barrier
ds_read_b32 v13,v7 offset:8448
s_mov_b32 s69,18688
s_waitcnt lgkmcnt(0)
v_cmp_eq_u32 vcc,s60,v13
s_nop 0
s_cmp_eq_u32 vcc_lo,-1
s_cbranch_scc1 Lg3_go0_%=
s_sleep 4
s_add_u32 m0,s78,8192
s_nop 0
buffer_load_dword v6,s[36:39],s68 offen offset:0 sc1 lds
s_waitcnt vmcnt(2)
s_barrier
ds_read_b32 v13,v7 offset:16384
s_mov_b32 s69,10240
s_waitcnt lgkmcnt(0)
v_cmp_eq_u32 vcc,s60,v13
s_nop 0
s_cmp_eq_u32 vcc_lo,-1
s_cbranch_scc1 Lg3_go0_%=
s_sleep 4
s_add_u32 m0,s78,8448
s_nop 0
buffer_load_dword v6,s[36:39],s68 offen offset:0 sc1 lds
s_waitcnt vmcnt(2)
s_barrier
ds_read_b32 v13,v7 offset:16640
s_mov_b32 s69,10496
s_waitcnt lgkmcnt(0)
v_cmp_eq_u32 vcc,s60,v13
s_nop 0
s_cmp_eq_u32 vcc_lo,-1
s_cbranch_scc1 Lg3_go0_%=
s_sleep 4
s_add_u32 s79,s79,1
s_cmp_lt_u32 s79,20000
s_cbranch_scc1 Lg3_poll0_%=
Lg3_go0_%=:
s_xor_b32 s62,s63,65536
s_lshr_b32 s61,s48,0
s_mov_b32 m0,s62
s_nop 0
buffer_load_dwordx4 v0,s[36:39],s61 offen offset:0 sc1 lds
buffer_load_dwordx4 v0,s[36:39],s61 offen offset:1024 sc1 lds
buffer_load_dwordx4 v0,s[36:39],s61 offen offset:2048 sc1 lds
buffer_load_dwordx4 v0,s[36:39],s61 offen offset:3072 sc1 lds
s_add_u32 m0,s62,16384
s_nop 0
buffer_load_dwordx4 v1,s[36:39],s61 offen offset:0 sc1 lds
buffer_load_dwordx4 v1,s[36:39],s61 offen offset:1024 sc1 lds
buffer_load_dwordx4 v1,s[36:39],s61 offen offset:2048 sc1 lds
buffer_load_dwordx4 v1,s[36:39],s61 offen offset:3072 sc1 lds
s_lshr_b32 s72,s61,5
buffer_load_dword v104,v8,s[36:39],s72 offen offset:0 sc1
v_add_u32 v84,s69,v7)"
R"(
s_waitcnt vmcnt(5)
s_barrier
ds_read_b32 v13,v84 offset:0
ds_read_b128 v[52:55],v2 offset:0
ds_read_b128 v[56:59],v2 offset:1024
ds_read_b128 v[60:63],v2 offset:2048
ds_read_b128 v[64:67],v2 offset:3072
ds_read_b128 v[68:71],v2 offset:4096
s_mul_i32 s70,s46,5
s_lshr_b32 s70,s70,6
buffer_store_dword v5,v4,s[36:39],s70 offen offset:0 sc0 sc1
s_mov_b32 s46,s59
s_mul_i32 s70,s47,5
s_lshr_b32 s70,s70,6
buffer_store_dword v5,v4,s[36:39],s70 offen offset:0 sc0 sc1
s_mov_b32 s47,s59
s_mul_i32 s70,s59,5
s_lshr_b32 s70,s70,6
buffer_store_dword v5,v4,s[36:39],s70 offen offset:0 sc0 sc1
s_waitcnt lgkmcnt(5)
v_cmp_eq_u32 vcc,s60,v13
s_nop 0
s_cmp_eq_u32 vcc_lo,-1
s_cselect_b32 s66,s49,s59
s_lshr_b32 s61,s66,0
s_xor_b32 s62,s64,65536
s_waitcnt lgkmcnt(3)
s_setpc_b64 s[90:91]
Lg3_gs0_%=:
s_xor_b32 s62,s63,65536
s_lshr_b32 s61,s48,0
s_cmp_eq_u32 s54,1
s_cselect_b32 s68,s49,s48
s_lshr_b32 s68,s68,5
s_mul_i32 s73,s47,7
s_lshr_b32 s73,s73,1
s_mov_b32 m0,s62
s_nop 0
buffer_load_dwordx4 v0,s[36:39],s61 offen offset:0 sc1 lds
v_mul_f32 v116,s75,v112
v_fma_f32 v113,v113,s75,v12
v_fma_f32 v113,-v116,v116,v113
v_max_f32 v113,v113,v12
v_rsq_f32 v112,v113
s_nop 0
v_mul_f32 v117,s76,v112
v_mul_f32 v118,v112,v112
v_mul_f32 v119,v113,v112
v_mul_f32 v118,s77,v118
v_mul_f32 v118,v118,v117
v_fma_f32 v40,-v116,v16,v40
buffer_load_dwordx4 v0,s[36:39],s61 offen offset:1024 sc1 lds
v_fma_f32 v41,-v116,v17,v41
v_fma_f32 v42,-v116,v18,v42
v_fma_f32 v43,-v116,v19,v43
v_mul_f32 v84,v40,v40
v_mul_f32 v85,v41,v41
v_mul_f32 v86,v42,v42
v_mul_f32 v87,v43,v43
v_fma_f32 v84,v118,v84,v117
v_fma_f32 v85,v118,v85,v117
v_fma_f32 v86,v118,v86,v117
v_fma_f32 v87,v118,v87,v117
buffer_load_dwordx4 v0,s[36:39],s61 offen offset:2048 sc1 lds
v_mul_f32 v84,v40,v84
v_mul_f32 v85,v41,v85
v_mul_f32 v86,v42,v86
v_mul_f32 v87,v43,v87
v_exp_f32 v84,v84
v_exp_f32 v85,v85
v_exp_f32 v86,v86
v_exp_f32 v87,v87
v_fma_f32 v84,v84,v119,v119
v_fma_f32 v85,v85,v119,v119
v_fma_f32 v86,v86,v119,v119
buffer_load_dwordx4 v0,s[36:39],s61 offen offset:3072 sc1 lds
v_fma_f32 v87,v87,v119,v119
v_rcp_f32 v84,v84
v_rcp_f32 v85,v85
v_rcp_f32 v86,v86
v_rcp_f32 v87,v87
v_mul_f32 v40,v40,v84
v_mul_f32 v41,v41,v85
v_mul_f32 v42,v42,v86
v_mul_f32 v43,v43,v87
v_cvt_pk_bf16_f32 v96,v40,v41
v_cvt_pk_bf16_f32 v97,v42,v43
s_add_u32 m0,s78,8192
s_nop 0
buffer_load_dword v6,s[36:39],s68 offen offset:0 sc1 lds
v_fma_f32 v44,-v116,v20,v44
v_fma_f32 v45,-v116,v21,v45
v_fma_f32 v46,-v116,v22,v46
v_fma_f32 v47,-v116,v23,v47
v_mul_f32 v88,v44,v44
v_mul_f32 v89,v45,v45
v_mul_f32 v90,v46,v46
v_mul_f32 v91,v47,v47
v_fma_f32 v88,v118,v88,v117
v_fma_f32 v89,v118,v89,v117
v_fma_f32 v90,v118,v90,v117
s_add_u32 m0,s62,16384
s_nop 0
buffer_load_dwordx4 v1,s[36:39],s61 offen offset:0 sc1 lds
v_fma_f32 v91,v118,v91,v117
v_mul_f32 v88,v44,v88
v_mul_f32 v89,v45,v89
v_mul_f32 v90,v46,v90
v_mul_f32 v91,v47,v91
v_exp_f32 v88,v88
v_exp_f32 v89,v89
v_exp_f32 v90,v90
v_exp_f32 v91,v91
v_fma_f32 v88,v88,v119,v119
v_fma_f32 v89,v89,v119,v119
buffer_load_dwordx4 v1,s[36:39],s61 offen offset:1024 sc1 lds
v_fma_f32 v90,v90,v119,v119
v_fma_f32 v91,v91,v119,v119
v_rcp_f32 v88,v88
v_rcp_f32 v89,v89
v_rcp_f32 v90,v90
v_rcp_f32 v91,v91
v_mul_f32 v44,v44,v88
v_mul_f32 v45,v45,v89
v_mul_f32 v46,v46,v90
v_mul_f32 v47,v47,v91
v_cvt_pk_bf16_f32 v98,v44,v45
buffer_load_dwordx4 v1,s[36:39],s61 offen offset:2048 sc1 lds
v_cvt_pk_bf16_f32 v99,v46,v47
buffer_store_dwordx4 v[96:99],v3,s[36:39],s73 offen offset:0 sc0 sc1
v_fma_f32 v48,-v116,v24,v48
v_fma_f32 v49,-v116,v25,v49
v_fma_f32 v50,-v116,v26,v50
v_fma_f32 v51,-v116,v27,v51
v_mul_f32 v92,v48,v48
v_mul_f32 v93,v49,v49
v_mul_f32 v94,v50,v50
v_mul_f32 v95,v51,v51
v_fma_f32 v92,v118,v92,v117
buffer_load_dwordx4 v1,s[36:39],s61 offen offset:3072 sc1 lds
v_fma_f32 v93,v118,v93,v117
v_fma_f32 v94,v118,v94,v117
v_fma_f32 v95,v118,v95,v117
v_mul_f32 v92,v48,v92
v_mul_f32 v93,v49,v93
v_mul_f32 v94,v50,v94
v_mul_f32 v95,v51,v95
v_exp_f32 v92,v92
v_exp_f32 v93,v93
v_exp_f32 v94,v94
v_exp_f32 v95,v95
s_lshr_b32 s72,s61,5
buffer_load_dword v104,v8,s[36:39],s72 offen offset:0 sc1
v_fma_f32 v92,v92,v119,v119
v_fma_f32 v93,v93,v119,v119
v_fma_f32 v94,v94,v119,v119
v_fma_f32 v95,v95,v119,v119
v_rcp_f32 v92,v92
v_rcp_f32 v93,v93
v_rcp_f32 v94,v94
v_rcp_f32 v95,v95
v_mul_f32 v48,v48,v92
v_mul_f32 v49,v49,v93
v_mul_f32 v50,v50,v94
v_mul_f32 v51,v51,v95
v_cvt_pk_bf16_f32 v100,v48,v49
v_cvt_pk_bf16_f32 v101,v50,v51
buffer_store_dwordx2 v[100:101],v9,s[36:39],s73 offen offset:0 sc0 sc1
s_waitcnt vmcnt(7)
s_barrier
ds_read_b32 v13,v7 offset:8192
ds_read_b128 v[52:55],v2 offset:0
ds_read_b128 v[56:59],v2 offset:1024
ds_read_b128 v[60:63],v2 offset:2048
ds_read_b128 v[64:67],v2 offset:3072
ds_read_b128 v[68:71],v2 offset:4096
s_mul_i32 s70,s46,5
s_lshr_b32 s70,s70,6
buffer_store_dword v5,v4,s[36:39],s70 offen offset:0 sc0 sc1
s_waitcnt lgkmcnt(5)
v_cmp_eq_u32 vcc,s60,v13
s_nop 0
s_cmp_eq_u32 vcc_hi,-1
s_cselect_b32 s66,s49,s59
s_lshr_b32 s61,s66,0
s_xor_b32 s62,s64,65536
s_mov_b32 s46,s47
s_mov_b32 s47,s59
s_mul_i32 s70,s59,5
s_lshr_b32 s70,s70,6
buffer_store_dword v5,v4,s[36:39],s70 offen offset:0 sc0 sc1
s_waitcnt lgkmcnt(3)
s_setpc_b64 s[90:91]
Lg3_done0_%=:
s_waitcnt vmcnt(0)
s_barrier
s_mul_i32 s70,s46,5
s_lshr_b32 s70,s70,6
buffer_store_dword v5,v4,s[36:39],s70 offen offset:0 sc0 sc1
s_mul_i32 s70,s47,5)"
R"(
s_lshr_b32 s70,s70,6
buffer_store_dword v5,v4,s[36:39],s70 offen offset:0 sc0 sc1
s_branch Lg3_done_%=
Lg3_slow1_%=:
s_cmp_lt_u32 s67,s59
s_cbranch_scc1 Lg3_gs1_%=
Lg3_slowb1_%=:
s_cmp_eq_u32 s54,1
s_cselect_b32 s68,s50,s49
s_lshr_b32 s68,s68,5
s_add_u32 m0,s78,8192
s_nop 0
buffer_load_dword v6,s[36:39],s68 offen offset:0 sc1 lds
s_mul_i32 s73,s48,7
s_lshr_b32 s73,s73,1
v_mul_f32 v116,s75,v112
v_fma_f32 v113,v113,s75,v12
v_fma_f32 v113,-v116,v116,v113
v_max_f32 v113,v113,v12
v_rsq_f32 v112,v113
s_nop 0
v_mul_f32 v117,s76,v112
v_mul_f32 v118,v112,v112
v_mul_f32 v119,v113,v112
v_mul_f32 v118,s77,v118
v_mul_f32 v118,v118,v117
v_fma_f32 v28,-v116,v16,v28
v_fma_f32 v29,-v116,v17,v29
v_fma_f32 v30,-v116,v18,v30
v_fma_f32 v31,-v116,v19,v31
v_mul_f32 v84,v28,v28
v_mul_f32 v85,v29,v29
v_mul_f32 v86,v30,v30
v_mul_f32 v87,v31,v31
v_fma_f32 v84,v118,v84,v117
v_fma_f32 v85,v118,v85,v117
v_fma_f32 v86,v118,v86,v117
v_fma_f32 v87,v118,v87,v117
v_mul_f32 v84,v28,v84
v_mul_f32 v85,v29,v85
v_mul_f32 v86,v30,v86
v_mul_f32 v87,v31,v87
v_exp_f32 v84,v84
v_exp_f32 v85,v85
v_exp_f32 v86,v86
v_exp_f32 v87,v87
v_fma_f32 v84,v84,v119,v119
v_fma_f32 v85,v85,v119,v119
v_fma_f32 v86,v86,v119,v119
v_fma_f32 v87,v87,v119,v119
v_rcp_f32 v84,v84
v_rcp_f32 v85,v85
v_rcp_f32 v86,v86
v_rcp_f32 v87,v87
v_mul_f32 v28,v28,v84
v_mul_f32 v29,v29,v85
v_mul_f32 v30,v30,v86
v_mul_f32 v31,v31,v87
v_cvt_pk_bf16_f32 v96,v28,v29
v_cvt_pk_bf16_f32 v97,v30,v31
v_fma_f32 v32,-v116,v20,v32
v_fma_f32 v33,-v116,v21,v33
v_fma_f32 v34,-v116,v22,v34
v_fma_f32 v35,-v116,v23,v35
v_mul_f32 v88,v32,v32
v_mul_f32 v89,v33,v33
v_mul_f32 v90,v34,v34
v_mul_f32 v91,v35,v35
v_fma_f32 v88,v118,v88,v117
v_fma_f32 v89,v118,v89,v117
v_fma_f32 v90,v118,v90,v117
v_fma_f32 v91,v118,v91,v117
v_mul_f32 v88,v32,v88
s_add_u32 m0,s78,8448
s_nop 0
buffer_load_dword v6,s[36:39],s68 offen offset:0 sc1 lds
v_mul_f32 v89,v33,v89
v_mul_f32 v90,v34,v90
v_mul_f32 v91,v35,v91
v_exp_f32 v88,v88
v_exp_f32 v89,v89
v_exp_f32 v90,v90
v_exp_f32 v91,v91
v_fma_f32 v88,v88,v119,v119
v_fma_f32 v89,v89,v119,v119
v_fma_f32 v90,v90,v119,v119
v_fma_f32 v91,v91,v119,v119
v_rcp_f32 v88,v88
v_rcp_f32 v89,v89
v_rcp_f32 v90,v90
v_rcp_f32 v91,v91
v_mul_f32 v32,v32,v88
v_mul_f32 v33,v33,v89
v_mul_f32 v34,v34,v90
v_mul_f32 v35,v35,v91
v_cvt_pk_bf16_f32 v98,v32,v33
v_cvt_pk_bf16_f32 v99,v34,v35
buffer_store_dwordx4 v[96:99],v3,s[36:39],s73 offen offset:0 sc0 sc1
v_fma_f32 v36,-v116,v24,v36
v_fma_f32 v37,-v116,v25,v37
v_fma_f32 v38,-v116,v26,v38
v_fma_f32 v39,-v116,v27,v39
v_mul_f32 v92,v36,v36
v_mul_f32 v93,v37,v37
v_mul_f32 v94,v38,v38
v_mul_f32 v95,v39,v39
v_fma_f32 v92,v118,v92,v117
v_fma_f32 v93,v118,v93,v117
v_fma_f32 v94,v118,v94,v117
v_fma_f32 v95,v118,v95,v117
v_mul_f32 v92,v36,v92
v_mul_f32 v93,v37,v93
v_mul_f32 v94,v38,v94
v_mul_f32 v95,v39,v95
v_exp_f32 v92,v92
v_exp_f32 v93,v93
v_exp_f32 v94,v94
v_exp_f32 v95,v95
v_fma_f32 v92,v92,v119,v119
v_fma_f32 v93,v93,v119,v119
v_fma_f32 v94,v94,v119,v119
v_fma_f32 v95,v95,v119,v119
v_rcp_f32 v92,v92
v_rcp_f32 v93,v93
v_rcp_f32 v94,v94
v_rcp_f32 v95,v95
v_mul_f32 v36,v36,v92
v_mul_f32 v37,v37,v93
v_mul_f32 v38,v38,v94
v_mul_f32 v39,v39,v95
v_cvt_pk_bf16_f32 v100,v36,v37
v_cvt_pk_bf16_f32 v101,v38,v39
buffer_store_dwordx2 v[100:101],v9,s[36:39],s73 offen offset:0 sc0 sc1
s_cmp_ge_u32 s49,s59
s_cbranch_scc1 Lg3_done1_%=
s_add_u32 m0,s78,16384
s_nop 0
buffer_load_dword v6,s[36:39],s68 offen offset:0 sc1 lds
s_waitcnt vmcnt(4)
s_barrier
ds_read_b32 v13,v7 offset:8192
s_mul_i32 s70,s47,5
s_lshr_b32 s70,s70,6
buffer_store_dword v5,v4,s[36:39],s70 offen offset:0 sc0 sc1
s_mov_b32 s47,s59
s_mov_b32 s69,18432
s_waitcnt lgkmcnt(0)
v_cmp_eq_u32 vcc,s60,v13
s_nop 0
s_cmp_eq_u32 vcc_lo,-1
s_cbranch_scc1 Lg3_go1_%=
s_add_u32 m0,s78,16640
s_nop 0
buffer_load_dword v6,s[36:39],s68 offen offset:0 sc1 lds
s_waitcnt vmcnt(5)
s_barrier
ds_read_b32 v13,v7 offset:8448
s_mov_b32 s69,18688
s_waitcnt lgkmcnt(0)
v_cmp_eq_u32 vcc,s60,v13
s_nop 0
s_cmp_eq_u32 vcc_lo,-1
s_cbranch_scc1 Lg3_go1_%=
s_add_u32 m0,s78,8192
s_nop 0
buffer_load_dword v6,s[36:39],s68 offen offset:0 sc1 lds
s_waitcnt vmcnt(3)
s_barrier
ds_read_b32 v13,v7 offset:16384
s_mul_i32 s70,s48,5
s_lshr_b32 s70,s70,6
buffer_store_dword v5,v4,s[36:39],s70 offen offset:0 sc0 sc1
s_mov_b32 s48,s59
s_mov_b32 s69,10240
s_waitcnt lgkmcnt(0)
v_cmp_eq_u32 vcc,s60,v13
s_nop 0
s_cmp_eq_u32 vcc_lo,-1
s_cbranch_scc1 Lg3_go1_%=
s_add_u32 m0,s78,8448
s_nop 0
buffer_load_dword v6,s[36:39],s68 offen offset:0 sc1 lds
s_waitcnt vmcnt(3)
s_barrier
ds_read_b32 v13,v7 offset:16640
s_mov_b32 s69,10496
s_waitcnt lgkmcnt(0)
v_cmp_eq_u32 vcc,s60,v13
s_nop 0
s_cmp_eq_u32 vcc_lo,-1
s_cbranch_scc1 Lg3_go1_%=
Lg3_poll1_%=:
s_add_u32 m0,s78,16384
s_nop 0
buffer_load_dword v6,s[36:39],s68 offen offset:0 sc1 lds
s_waitcnt vmcnt(2)
s_barrier
ds_read_b32 v13,v7 offset:8192
s_mov_b32 s69,18432
s_waitcnt lgkmcnt(0))"
R"(
v_cmp_eq_u32 vcc,s60,v13
s_nop 0
s_cmp_eq_u32 vcc_lo,-1
s_cbranch_scc1 Lg3_go1_%=
s_sleep 4
s_add_u32 m0,s78,16640
s_nop 0
buffer_load_dword v6,s[36:39],s68 offen offset:0 sc1 lds
s_waitcnt vmcnt(2)
s_barrier
ds_read_b32 v13,v7 offset:8448
s_mov_b32 s69,18688
s_waitcnt lgkmcnt(0)
v_cmp_eq_u32 vcc,s60,v13
s_nop 0
s_cmp_eq_u32 vcc_lo,-1
s_cbranch_scc1 Lg3_go1_%=
s_sleep 4
s_add_u32 m0,s78,8192
s_nop 0
buffer_load_dword v6,s[36:39],s68 offen offset:0 sc1 lds
s_waitcnt vmcnt(2)
s_barrier
ds_read_b32 v13,v7 offset:16384
s_mov_b32 s69,10240
s_waitcnt lgkmcnt(0)
v_cmp_eq_u32 vcc,s60,v13
s_nop 0
s_cmp_eq_u32 vcc_lo,-1
s_cbranch_scc1 Lg3_go1_%=
s_sleep 4
s_add_u32 m0,s78,8448
s_nop 0
buffer_load_dword v6,s[36:39],s68 offen offset:0 sc1 lds
s_waitcnt vmcnt(2)
s_barrier
ds_read_b32 v13,v7 offset:16640
s_mov_b32 s69,10496
s_waitcnt lgkmcnt(0)
v_cmp_eq_u32 vcc,s60,v13
s_nop 0
s_cmp_eq_u32 vcc_lo,-1
s_cbranch_scc1 Lg3_go1_%=
s_sleep 4
s_add_u32 s79,s79,1
s_cmp_lt_u32 s79,20000
s_cbranch_scc1 Lg3_poll1_%=
Lg3_go1_%=:
s_xor_b32 s62,s64,65536
s_lshr_b32 s61,s49,0
s_mov_b32 m0,s62
s_nop 0
buffer_load_dwordx4 v0,s[36:39],s61 offen offset:0 sc1 lds
buffer_load_dwordx4 v0,s[36:39],s61 offen offset:1024 sc1 lds
buffer_load_dwordx4 v0,s[36:39],s61 offen offset:2048 sc1 lds
buffer_load_dwordx4 v0,s[36:39],s61 offen offset:3072 sc1 lds
s_add_u32 m0,s62,16384
s_nop 0
buffer_load_dwordx4 v1,s[36:39],s61 offen offset:0 sc1 lds
buffer_load_dwordx4 v1,s[36:39],s61 offen offset:1024 sc1 lds
buffer_load_dwordx4 v1,s[36:39],s61 offen offset:2048 sc1 lds
buffer_load_dwordx4 v1,s[36:39],s61 offen offset:3072 sc1 lds
s_lshr_b32 s72,s61,5
buffer_load_dword v108,v8,s[36:39],s72 offen offset:0 sc1
v_add_u32 v84,s69,v7
s_waitcnt vmcnt(5)
s_barrier
ds_read_b32 v13,v84 offset:0
ds_read_b128 v[52:55],v2 offset:32768
ds_read_b128 v[56:59],v2 offset:33792
ds_read_b128 v[60:63],v2 offset:34816
ds_read_b128 v[64:67],v2 offset:35840
ds_read_b128 v[68:71],v2 offset:36864
s_mul_i32 s70,s47,5
s_lshr_b32 s70,s70,6
buffer_store_dword v5,v4,s[36:39],s70 offen offset:0 sc0 sc1
s_mov_b32 s47,s59
s_mul_i32 s70,s48,5
s_lshr_b32 s70,s70,6
buffer_store_dword v5,v4,s[36:39],s70 offen offset:0 sc0 sc1
s_mov_b32 s48,s59
s_mul_i32 s70,s59,5
s_lshr_b32 s70,s70,6
buffer_store_dword v5,v4,s[36:39],s70 offen offset:0 sc0 sc1
s_waitcnt lgkmcnt(5)
v_cmp_eq_u32 vcc,s60,v13
s_nop 0
s_cmp_eq_u32 vcc_lo,-1
s_cselect_b32 s67,s50,s59
s_lshr_b32 s61,s67,0
s_xor_b32 s62,s63,65536
s_waitcnt lgkmcnt(3)
s_setpc_b64 s[92:93]
Lg3_gs1_%=:
s_xor_b32 s62,s64,65536
s_lshr_b32 s61,s49,0
s_cmp_eq_u32 s54,1
s_cselect_b32 s68,s50,s49
s_lshr_b32 s68,s68,5
s_mul_i32 s73,s48,7
s_lshr_b32 s73,s73,1
s_mov_b32 m0,s62
s_nop 0
buffer_load_dwordx4 v0,s[36:39],s61 offen offset:0 sc1 lds
v_mul_f32 v116,s75,v112
v_fma_f32 v113,v113,s75,v12
v_fma_f32 v113,-v116,v116,v113
v_max_f32 v113,v113,v12
v_rsq_f32 v112,v113
s_nop 0
v_mul_f32 v117,s76,v112
v_mul_f32 v118,v112,v112
v_mul_f32 v119,v113,v112
v_mul_f32 v118,s77,v118
v_mul_f32 v118,v118,v117
v_fma_f32 v28,-v116,v16,v28
buffer_load_dwordx4 v0,s[36:39],s61 offen offset:1024 sc1 lds
v_fma_f32 v29,-v116,v17,v29
v_fma_f32 v30,-v116,v18,v30
v_fma_f32 v31,-v116,v19,v31
v_mul_f32 v84,v28,v28
v_mul_f32 v85,v29,v29
v_mul_f32 v86,v30,v30
v_mul_f32 v87,v31,v31
v_fma_f32 v84,v118,v84,v117
v_fma_f32 v85,v118,v85,v117
v_fma_f32 v86,v118,v86,v117
v_fma_f32 v87,v118,v87,v117
buffer_load_dwordx4 v0,s[36:39],s61 offen offset:2048 sc1 lds
v_mul_f32 v84,v28,v84
v_mul_f32 v85,v29,v85
v_mul_f32 v86,v30,v86
v_mul_f32 v87,v31,v87
v_exp_f32 v84,v84
v_exp_f32 v85,v85
v_exp_f32 v86,v86
v_exp_f32 v87,v87
v_fma_f32 v84,v84,v119,v119
v_fma_f32 v85,v85,v119,v119
v_fma_f32 v86,v86,v119,v119
buffer_load_dwordx4 v0,s[36:39],s61 offen offset:3072 sc1 lds
v_fma_f32 v87,v87,v119,v119
v_rcp_f32 v84,v84
v_rcp_f32 v85,v85
v_rcp_f32 v86,v86
v_rcp_f32 v87,v87
v_mul_f32 v28,v28,v84
v_mul_f32 v29,v29,v85
v_mul_f32 v30,v30,v86
v_mul_f32 v31,v31,v87
v_cvt_pk_bf16_f32 v96,v28,v29
v_cvt_pk_bf16_f32 v97,v30,v31
s_add_u32 m0,s78,8192
s_nop 0
buffer_load_dword v6,s[36:39],s68 offen offset:0 sc1 lds
v_fma_f32 v32,-v116,v20,v32
v_fma_f32 v33,-v116,v21,v33
v_fma_f32 v34,-v116,v22,v34
v_fma_f32 v35,-v116,v23,v35
v_mul_f32 v88,v32,v32
v_mul_f32 v89,v33,v33
v_mul_f32 v90,v34,v34
v_mul_f32 v91,v35,v35
v_fma_f32 v88,v118,v88,v117
v_fma_f32 v89,v118,v89,v117
v_fma_f32 v90,v118,v90,v117
s_add_u32 m0,s62,16384
s_nop 0
buffer_load_dwordx4 v1,s[36:39],s61 offen offset:0 sc1 lds
v_fma_f32 v91,v118,v91,v117
v_mul_f32 v88,v32,v88
v_mul_f32 v89,v33,v89
v_mul_f32 v90,v34,v90
v_mul_f32 v91,v35,v91
v_exp_f32 v88,v88
v_exp_f32 v89,v89
v_exp_f32 v90,v90
v_exp_f32 v91,v91
v_fma_f32 v88,v88,v119,v119
v_fma_f32 v89,v89,v119,v119
buffer_load_dwordx4 v1,s[36:39],s61 offen offset:1024 sc1 lds
v_fma_f32 v90,v90,v119,v119
v_fma_f32 v91,v91,v119,v119
v_rcp_f32 v88,v88
v_rcp_f32 v89,v89
v_rcp_f32 v90,v90
v_rcp_f32 v91,v91
v_mul_f32 v32,v32,v88
v_mul_f32 v33,v33,v89
v_mul_f32 v34,v34,v90
v_mul_f32 v35,v35,v91
v_cvt_pk_bf16_f32 v98,v32,v33
buffer_load_dwordx4 v1,s[36:39],s61 offen offset:2048 sc1 lds
v_cvt_pk_bf16_f32 v99,v34,v35
buffer_store_dwordx4 v[96:99],v3,s[36:39],s73 offen offset:0 sc0 sc1
v_fma_f32 v36,-v116,v24,v36
v_fma_f32 v37,-v116,v25,v37
v_fma_f32 v38,-v116,v26,v38
v_fma_f32 v39,-v116,v27,v39
v_mul_f32 v92,v36,v36)"
R"(
v_mul_f32 v93,v37,v37
v_mul_f32 v94,v38,v38
v_mul_f32 v95,v39,v39
v_fma_f32 v92,v118,v92,v117
buffer_load_dwordx4 v1,s[36:39],s61 offen offset:3072 sc1 lds
v_fma_f32 v93,v118,v93,v117
v_fma_f32 v94,v118,v94,v117
v_fma_f32 v95,v118,v95,v117
v_mul_f32 v92,v36,v92
v_mul_f32 v93,v37,v93
v_mul_f32 v94,v38,v94
v_mul_f32 v95,v39,v95
v_exp_f32 v92,v92
v_exp_f32 v93,v93
v_exp_f32 v94,v94
v_exp_f32 v95,v95
s_lshr_b32 s72,s61,5
buffer_load_dword v108,v8,s[36:39],s72 offen offset:0 sc1
v_fma_f32 v92,v92,v119,v119
v_fma_f32 v93,v93,v119,v119
v_fma_f32 v94,v94,v119,v119
v_fma_f32 v95,v95,v119,v119
v_rcp_f32 v92,v92
v_rcp_f32 v93,v93
v_rcp_f32 v94,v94
v_rcp_f32 v95,v95
v_mul_f32 v36,v36,v92
v_mul_f32 v37,v37,v93
v_mul_f32 v38,v38,v94
v_mul_f32 v39,v39,v95
v_cvt_pk_bf16_f32 v100,v36,v37
v_cvt_pk_bf16_f32 v101,v38,v39
buffer_store_dwordx2 v[100:101],v9,s[36:39],s73 offen offset:0 sc0 sc1
s_waitcnt vmcnt(7)
s_barrier
ds_read_b32 v13,v7 offset:8192
ds_read_b128 v[52:55],v2 offset:32768
ds_read_b128 v[56:59],v2 offset:33792
ds_read_b128 v[60:63],v2 offset:34816
ds_read_b128 v[64:67],v2 offset:35840
ds_read_b128 v[68:71],v2 offset:36864
s_mul_i32 s70,s47,5
s_lshr_b32 s70,s70,6
buffer_store_dword v5,v4,s[36:39],s70 offen offset:0 sc0 sc1
s_waitcnt lgkmcnt(5)
v_cmp_eq_u32 vcc,s60,v13
s_nop 0
s_cmp_eq_u32 vcc_hi,-1
s_cselect_b32 s67,s50,s59
s_lshr_b32 s61,s67,0
s_xor_b32 s62,s63,65536
s_mov_b32 s47,s48
s_mov_b32 s48,s59
s_mul_i32 s70,s59,5
s_lshr_b32 s70,s70,6
buffer_store_dword v5,v4,s[36:39],s70 offen offset:0 sc0 sc1
s_waitcnt lgkmcnt(3)
s_setpc_b64 s[92:93]
Lg3_done1_%=:
s_waitcnt vmcnt(0)
s_barrier
s_mul_i32 s70,s47,5
s_lshr_b32 s70,s70,6
buffer_store_dword v5,v4,s[36:39],s70 offen offset:0 sc0 sc1
s_mul_i32 s70,s48,5
s_lshr_b32 s70,s70,6
buffer_store_dword v5,v4,s[36:39],s70 offen offset:0 sc0 sc1
s_branch Lg3_done_%=
Lg3_done_%=:
s_waitcnt vmcnt(0) lgkmcnt(0)
s_barrier
s_endpgm
Lcode_g3_e:)"
  :: "s"(ws), "s"(wave), "s"(sStep), "s"(sX1), "s"(sX2), "s"(sU0), "s"(sWw), "s"(sBuf), "v"(vSrc), "v"(vA), "v"(vOut), "v"(vFlag), "v"(vPollOff), "v"(vSnapRd), "v"(vAuxS), "v"(vOut2), "v"(vStW), "v"(vStR), "v"(vWd0), "v"(vWd1), "v"(vWr0), "v"(vWr1), "v"(vWr2), "s"(w3) : "v0", "v1", "v2", "v3", "v4", "v5", "v6", "v7", "v8", "v9", "v10", "v11", "v12", "v13", "v14", "v15", "v16", "v17", "v18", "v19", "v20", "v21", "v22", "v23", "v24", "v25", "v26", "v27", "v28", "v29", "v30", "v31", "v32", "v33", "v34", "v35", "v36", "v37", "v38", "v39", "v40", "v41", "v42", "v43", "v44", "v45", "v46", "v47", "v48", "v49", "v50", "v51", "v52", "v53", "v54", "v55", "v56", "v57", "v58", "v59", "v60", "v61", "v62", "v63", "v64", "v65", "v66", "v67", "v68", "v69", "v70", "v71", "v72", "v73", "v74", "v75", "v76", "v77", "v78", "v79", "v80", "v81", "v82", "v83", "v84", "v85", "v86", "v87", "v88", "v89", "v90", "v91", "v92", "v93", "v94", "v95", "v96", "v97", "v98", "v99", "v100", "v101", "v102", "v103", "v104", "v105", "v106", "v107", "v108", "v109", "v110", "v111", "v112", "v113", "v114", "v115", "v116", "v117", "v118", "v119", "v120", "v121", "v122", "v123", "v124", "v125", "v126", "v127", "v128", "v129", "v130", "v131", "v132", "v133", "v134", "v135", "v136", "v137", "v138", "v139", "v140", "v141", "v142", "v143", "v144", "v145", "v146", "v147", "v148", "v149", "v150", "v151", "v152", "v153", "v154", "v155", "v156", "v157", "v158", "v159", "v160", "v161", "v162", "v163", "v164", "v165", "v166", "v167", "v168", "v169", "v170", "v171", "v172", "v173", "v174", "v175", "v176", "v177", "v178", "v179", "v180", "v181", "v182", "v183", "v184", "v185", "v186", "v187", "v188", "v189", "v190", "v191", "v192", "v193", "v194", "v195", "v196", "v197", "v198", "v199", "v200", "v201", "v202", "v203", "v204", "v205", "v206", "v207", "v208", "v209", "v210", "v211", "v212", "v213", "v214", "v215", "v216", "v217", "v218", "v219", "v220", "v221", "v222", "v223", "v224", "v225", "v226", "v227", "v228", "v229", "v230", "v231", "v255", "a0", "a1", "a2", "a3", "a4", "a5", "a6", "a7", "a8", "a9", "a10", "a11", "a12", "a13", "a14", "a15", "a16", "a17", "a18", "a19", "a20", "a21", "a22", "a23", "a24", "a25", "a26", "a27", "a28", "a29", "a30", "a31", "a32", "a33", "a34", "a35", "a36", "a37", "a38", "a39", "a40", "a41", "a42", "a43", "a44", "a45", "a46", "a47", "a48", "a49", "a50", "a51", "a52", "a53", "a54", "a55", "a56", "a57", "a58", "a59", "a60", "a61", "a62", "a63", "a64", "a65", "a66", "a67", "a68", "a69", "a70", "a71", "a72", "a73", "a74", "a75", "a76", "a77", "a78", "a79", "a80", "a81", "a82", "a83", "a84", "a85", "a86", "a87", "a88", "a89", "a90", "a91", "a92", "a93", "a94", "a95", "a96", "a97", "a98", "a99", "a100", "a101", "a102", "a103", "a104", "a105", "a106", "a107", "a108", "a109", "a110", "a111", "a112", "a113", "a114", "a115", "a116", "a117", "a118", "a119", "a120", "a121", "a122", "a123", "a124", "a125", "a126", "a127", "a128", "a129", "a130", "a131", "a132", "a133", "a134", "a135", "a136", "a137", "a138", "a139", "a140", "a141", "a142", "a143", "a144", "a145", "a146", "a147", "a148", "a149", "a150", "a151", "a152", "a153", "a154", "a155", "a156", "a157", "a158", "a159", "a160", "a161", "a162", "a163", "a164", "a165", "a166", "a167", "a168", "a169", "a170", "a171", "a172", "a173", "a174", "a175", "a176", "a177", "a178", "a179", "a180", "a181", "a182", "a183", "a184", "a185", "a186", "a187", "a188", "a189", "a190", "a191", "a192", "a193", "a194", "a195", "a196", "a197", "a198", "a199", "a200", "a201", "a202", "a203", "a204", "a205", "a206", "a207", "a208", "a209", "a210", "a211", "a212", "a213", "a214", "a215", "a216", "a217", "a218", "a219", "a220", "a221", "a222", "a223", "a224", "a225", "a226", "a227", "a228", "a229", "a230", "a231", "a232", "a233", "a234", "a235", "a236", "a237", "a238", "a239", "a240", "a241", "a242", "a243", "a244", "a245", "a246", "a247", "a248", "a249", "a250", "a251", "a252", "a253", "a254", "a255", "s36", "s37", "s38", "s39", "s40", "s41", "s42", "s43", "s44", "s45", "s46", "s47", "s48", "s49", "s50", "s51", "s52", "s53", "s54", "s55", "s56", "s57", "s58", "s59", "s60", "s61", "s62", "s63", "s64", "s65", "s66", "s67", "s68", "s69", "s70", "s71", "s72", "s73", "s74", "s75", "s76", "s77", "s78", "s79", "s80", "s81", "s82", "s83", "s84", "s85", "s86", "s87", "s88", "s89", "s90", "s91", "s92", "s93", "s94", "s95", "s96", "s97", "s98", "s99", "vcc", "scc", "memory"); } else { asm volatile(R"(
Lcode_g3o_b:
s_mov_b64 s[36:37],%0
s_mov_b32 s38,122727552
s_mov_b32 s39,0x00020000
s_mov_b32 s54,%1
s_mov_b32 s55,%2
s_mov_b32 s56,%3
s_mov_b32 s58,%4
s_mov_b32 s83,%5
s_mov_b32 s82,%6
s_mov_b32 s84,%7
v_mov_b32 v0,%8
v_mov_b32 v2,%9
v_mov_b32 v3,%10
v_mov_b32 v4,%11
v_mov_b32 v6,%12
v_mov_b32 v7,%13
v_mov_b32 v8,%14
v_mov_b32 v9,%15
v_mov_b32 v10,%16
v_mov_b32 v11,%17
v_mov_b32 v24,%18
v_mov_b32 v25,%19
v_mov_b32 v26,%20
v_mov_b32 v27,%21
v_mov_b32 v28,%22
s_mov_b64 s[40:41],%23
s_mov_b32 s42,7340032
s_mov_b32 s43,0x00020000
s_lshl_b32 s88,s54,11
s_mov_b32 s69,122727424
buffer_load_dword v5,off,s[36:39],s69 sc1
s_add_u32 m0,s88,0
s_add_u32 s61,s82,0
buffer_load_dwordx4 v24,s[40:43],s61 offen offset:0  lds
s_add_u32 m0,s88,1024
s_nop 0
buffer_load_dwordx4 v25,s[40:43],s61 offen offset:0  lds
s_add_u32 m0,s88,8064
s_nop 0
buffer_load_dwordx4 v24,s[40:43],s61 offen offset:128  lds
s_add_u32 m0,s88,9088
s_nop 0
buffer_load_dwordx4 v25,s[40:43],s61 offen offset:128  lds
s_add_u32 m0,s88,16384
s_add_u32 s61,s82,458752
buffer_load_dwordx4 v24,s[40:43],s61 offen offset:0  lds
s_add_u32 m0,s88,17408
s_nop 0
buffer_load_dwordx4 v25,s[40:43],s61 offen offset:0  lds
s_add_u32 m0,s88,24448
s_nop 0
buffer_load_dwordx4 v24,s[40:43],s61 offen offset:128  lds
s_add_u32 m0,s88,25472
s_nop 0
buffer_load_dwordx4 v25,s[40:43],s61 offen offset:128  lds
s_add_u32 m0,s88,32768
s_add_u32 s61,s82,917504
buffer_load_dwordx4 v24,s[40:43],s61 offen offset:0  lds
s_add_u32 m0,s88,33792
s_nop 0
buffer_load_dwordx4 v25,s[40:43],s61 offen offset:0  lds
s_add_u32 m0,s88,40832
s_nop 0
buffer_load_dwordx4 v24,s[40:43],s61 offen offset:128  lds
s_add_u32 m0,s88,41856
s_nop 0
buffer_load_dwordx4 v25,s[40:43],s61 offen offset:128  lds
s_add_u32 m0,s88,49152
s_add_u32 s61,s82,1376256
buffer_load_dwordx4 v24,s[40:43],s61 offen offset:0  lds
s_add_u32 m0,s88,50176
s_nop 0
buffer_load_dwordx4 v25,s[40:43],s61 offen offset:0  lds
s_add_u32 m0,s88,57216
s_nop 0
buffer_load_dwordx4 v24,s[40:43],s61 offen offset:128  lds
s_add_u32 m0,s88,58240
s_nop 0
buffer_load_dwordx4 v25,s[40:43],s61 offen offset:128  lds
s_add_u32 m0,s88,65536
s_add_u32 s61,s82,1835008
buffer_load_dwordx4 v24,s[40:43],s61 offen offset:0  lds
s_add_u32 m0,s88,66560
s_nop 0
buffer_load_dwordx4 v25,s[40:43],s61 offen offset:0  lds
s_add_u32 m0,s88,73600
s_nop 0
buffer_load_dwordx4 v24,s[40:43],s61 offen offset:128  lds
s_add_u32 m0,s88,74624
s_nop 0
buffer_load_dwordx4 v25,s[40:43],s61 offen offset:128  lds
s_add_u32 m0,s88,81920
s_add_u32 s61,s82,2293760
buffer_load_dwordx4 v24,s[40:43],s61 offen offset:0  lds
s_add_u32 m0,s88,82944
s_nop 0
buffer_load_dwordx4 v25,s[40:43],s61 offen offset:0  lds
s_add_u32 m0,s88,89984
s_nop 0
buffer_load_dwordx4 v24,s[40:43],s61 offen offset:128  lds
s_add_u32 m0,s88,91008
s_nop 0
buffer_load_dwordx4 v25,s[40:43],s61 offen offset:128  lds
s_add_u32 m0,s88,98304
s_add_u32 s61,s82,2752512
buffer_load_dwordx4 v24,s[40:43],s61 offen offset:0  lds
s_add_u32 m0,s88,99328
s_nop 0
buffer_load_dwordx4 v25,s[40:43],s61 offen offset:0  lds
s_add_u32 m0,s88,106368
s_nop 0
buffer_load_dwordx4 v24,s[40:43],s61 offen offset:128  lds
s_add_u32 m0,s88,107392
s_nop 0
buffer_load_dwordx4 v25,s[40:43],s61 offen offset:128  lds
v_add_u32 v32,49152,v26
v_add_u32 v33,49152,v27
v_add_u32 v34,98304,v26
v_add_u32 v35,98304,v27
v_mov_b32 v72,0x3f803f80
v_mov_b32 v73,0x3f803f80
v_mov_b32 v74,0x3f803f80
v_mov_b32 v75,0x3f803f80
s_waitcnt vmcnt(24)
s_barrier
s_add_u32 m0,s88,114688
s_add_u32 s61,s82,3211264
buffer_load_dwordx4 v24,s[40:43],s61 offen offset:0  lds
s_add_u32 m0,s88,115712
ds_read_b64_tr_b16 a[0:1],v26 offset:0
buffer_load_dwordx4 v25,s[40:43],s61 offen offset:0  lds
s_add_u32 m0,s88,122752
ds_read_b64_tr_b16 a[2:3],v26 offset:512
buffer_load_dwordx4 v24,s[40:43],s61 offen offset:128  lds
s_add_u32 m0,s88,123776
ds_read_b64_tr_b16 a[4:5],v27 offset:0
buffer_load_dwordx4 v25,s[40:43],s61 offen offset:128  lds
ds_read_b64_tr_b16 a[6:7],v27 offset:512
ds_read_b64_tr_b16 a[8:9],v26 offset:4096
ds_read_b64_tr_b16 a[10:11],v26 offset:4608
ds_read_b64_tr_b16 a[12:13],v27 offset:4096
ds_read_b64_tr_b16 a[14:15],v27 offset:4608
s_waitcnt vmcnt(24)
s_barrier
s_add_u32 m0,s88,131072
s_add_u32 s61,s82,3670016
buffer_load_dwordx4 v24,s[40:43],s61 offen offset:0  lds
s_add_u32 m0,s88,132096
ds_read_b64_tr_b16 a[16:17],v26 offset:16384
buffer_load_dwordx4 v25,s[40:43],s61 offen offset:0  lds
s_add_u32 m0,s88,139136
ds_read_b64_tr_b16 a[18:19],v26 offset:16896
buffer_load_dwordx4 v24,s[40:43],s61 offen offset:128  lds
s_add_u32 m0,s88,140160
ds_read_b64_tr_b16 a[20:21],v27 offset:16384
buffer_load_dwordx4 v25,s[40:43],s61 offen offset:128  lds
ds_read_b64_tr_b16 a[22:23],v27 offset:16896
ds_read_b64_tr_b16 a[24:25],v26 offset:20480
ds_read_b64_tr_b16 a[26:27],v26 offset:20992
ds_read_b64_tr_b16 a[28:29],v27 offset:20480
ds_read_b64_tr_b16 a[30:31],v27 offset:20992
s_waitcnt lgkmcnt(8)
v_mfma_f32_16x16x32_bf16 v[16:19],a[0:3],v[72:75],0
v_mfma_f32_16x16x32_bf16 v[20:23],a[4:7],v[72:75],0
v_mfma_f32_16x16x32_bf16 v[16:19],a[8:11],v[72:75],v[16:19]
v_mfma_f32_16x16x32_bf16 v[20:23],a[12:15],v[72:75],v[20:23]
s_waitcnt vmcnt(24)
s_barrier
s_add_u32 m0,s88,0
s_add_u32 s61,s82,4128768
buffer_load_dwordx4 v24,s[40:43],s61 offen offset:0  lds
s_add_u32 m0,s88,1024
ds_read_b64_tr_b16 a[32:33],v26 offset:32768
buffer_load_dwordx4 v25,s[40:43],s61 offen offset:0  lds
s_add_u32 m0,s88,8064
ds_read_b64_tr_b16 a[34:35],v26 offset:33280
buffer_load_dwordx4 v24,s[40:43],s61 offen offset:128  lds
s_add_u32 m0,s88,9088
ds_read_b64_tr_b16 a[36:37],v27 offset:32768
buffer_load_dwordx4 v25,s[40:43],s61 offen offset:128  lds
ds_read_b64_tr_b16 a[38:39],v27 offset:33280
ds_read_b64_tr_b16 a[40:41],v26 offset:36864
ds_read_b64_tr_b16 a[42:43],v26 offset:37376
ds_read_b64_tr_b16 a[44:45],v27 offset:36864
ds_read_b64_tr_b16 a[46:47],v27 offset:37376
s_waitcnt lgkmcnt(8)
v_mfma_f32_16x16x32_bf16 v[16:19],a[16:19],v[72:75],v[16:19]
v_mfma_f32_16x16x32_bf16 v[20:23],a[20:23],v[72:75],v[20:23]
v_mfma_f32_16x16x32_bf16 v[16:19],a[24:27],v[72:75],v[16:19]
v_mfma_f32_16x16x32_bf16 v[20:23],a[28:31],v[72:75],v[20:23]
s_waitcnt vmcnt(24)
s_barrier
s_add_u32 m0,s88,16384
s_add_u32 s61,s82,4587520
buffer_load_dwordx4 v24,s[40:43],s61 offen offset:0  lds
s_add_u32 m0,s88,17408
ds_read_b64_tr_b16 a[48:49],v32 offset:0
buffer_load_dwordx4 v25,s[40:43],s61 offen offset:0  lds
s_add_u32 m0,s88,24448)"
R"(
ds_read_b64_tr_b16 a[50:51],v32 offset:512
buffer_load_dwordx4 v24,s[40:43],s61 offen offset:128  lds
s_add_u32 m0,s88,25472
ds_read_b64_tr_b16 a[52:53],v33 offset:0
buffer_load_dwordx4 v25,s[40:43],s61 offen offset:128  lds
ds_read_b64_tr_b16 a[54:55],v33 offset:512
ds_read_b64_tr_b16 a[56:57],v32 offset:4096
ds_read_b64_tr_b16 a[58:59],v32 offset:4608
ds_read_b64_tr_b16 a[60:61],v33 offset:4096
ds_read_b64_tr_b16 a[62:63],v33 offset:4608
s_waitcnt lgkmcnt(8)
v_mfma_f32_16x16x32_bf16 v[16:19],a[32:35],v[72:75],v[16:19]
v_mfma_f32_16x16x32_bf16 v[20:23],a[36:39],v[72:75],v[20:23]
v_mfma_f32_16x16x32_bf16 v[16:19],a[40:43],v[72:75],v[16:19]
v_mfma_f32_16x16x32_bf16 v[20:23],a[44:47],v[72:75],v[20:23]
s_waitcnt vmcnt(24)
s_barrier
s_add_u32 m0,s88,32768
s_add_u32 s61,s82,5046272
buffer_load_dwordx4 v24,s[40:43],s61 offen offset:0  lds
s_add_u32 m0,s88,33792
ds_read_b64_tr_b16 a[64:65],v32 offset:16384
buffer_load_dwordx4 v25,s[40:43],s61 offen offset:0  lds
s_add_u32 m0,s88,40832
ds_read_b64_tr_b16 a[66:67],v32 offset:16896
buffer_load_dwordx4 v24,s[40:43],s61 offen offset:128  lds
s_add_u32 m0,s88,41856
ds_read_b64_tr_b16 a[68:69],v33 offset:16384
buffer_load_dwordx4 v25,s[40:43],s61 offen offset:128  lds
ds_read_b64_tr_b16 a[70:71],v33 offset:16896
ds_read_b64_tr_b16 a[72:73],v32 offset:20480
ds_read_b64_tr_b16 a[74:75],v32 offset:20992
ds_read_b64_tr_b16 a[76:77],v33 offset:20480
ds_read_b64_tr_b16 a[78:79],v33 offset:20992
s_waitcnt lgkmcnt(8)
v_mfma_f32_16x16x32_bf16 v[16:19],a[48:51],v[72:75],v[16:19]
v_mfma_f32_16x16x32_bf16 v[20:23],a[52:55],v[72:75],v[20:23]
v_mfma_f32_16x16x32_bf16 v[16:19],a[56:59],v[72:75],v[16:19]
v_mfma_f32_16x16x32_bf16 v[20:23],a[60:63],v[72:75],v[20:23]
s_waitcnt vmcnt(24)
s_barrier
s_add_u32 m0,s88,49152
s_add_u32 s61,s82,5505024
buffer_load_dwordx4 v24,s[40:43],s61 offen offset:0  lds
s_add_u32 m0,s88,50176
ds_read_b64_tr_b16 a[80:81],v32 offset:32768
buffer_load_dwordx4 v25,s[40:43],s61 offen offset:0  lds
s_add_u32 m0,s88,57216
ds_read_b64_tr_b16 a[82:83],v32 offset:33280
buffer_load_dwordx4 v24,s[40:43],s61 offen offset:128  lds
s_add_u32 m0,s88,58240
ds_read_b64_tr_b16 a[84:85],v33 offset:32768
buffer_load_dwordx4 v25,s[40:43],s61 offen offset:128  lds
ds_read_b64_tr_b16 a[86:87],v33 offset:33280
ds_read_b64_tr_b16 a[88:89],v32 offset:36864
ds_read_b64_tr_b16 a[90:91],v32 offset:37376
ds_read_b64_tr_b16 a[92:93],v33 offset:36864
ds_read_b64_tr_b16 a[94:95],v33 offset:37376
s_waitcnt lgkmcnt(8)
v_mfma_f32_16x16x32_bf16 v[16:19],a[64:67],v[72:75],v[16:19]
v_mfma_f32_16x16x32_bf16 v[20:23],a[68:71],v[72:75],v[20:23]
v_mfma_f32_16x16x32_bf16 v[16:19],a[72:75],v[72:75],v[16:19]
v_mfma_f32_16x16x32_bf16 v[20:23],a[76:79],v[72:75],v[20:23]
s_waitcnt vmcnt(24)
s_barrier
s_add_u32 m0,s88,65536
s_add_u32 s61,s82,5963776
buffer_load_dwordx4 v24,s[40:43],s61 offen offset:0  lds
s_add_u32 m0,s88,66560
ds_read_b64_tr_b16 a[96:97],v34 offset:0
buffer_load_dwordx4 v25,s[40:43],s61 offen offset:0  lds
s_add_u32 m0,s88,73600
ds_read_b64_tr_b16 a[98:99],v34 offset:512
buffer_load_dwordx4 v24,s[40:43],s61 offen offset:128  lds
s_add_u32 m0,s88,74624
ds_read_b64_tr_b16 a[100:101],v35 offset:0
buffer_load_dwordx4 v25,s[40:43],s61 offen offset:128  lds
ds_read_b64_tr_b16 a[102:103],v35 offset:512
ds_read_b64_tr_b16 a[104:105],v34 offset:4096
ds_read_b64_tr_b16 a[106:107],v34 offset:4608
ds_read_b64_tr_b16 a[108:109],v35 offset:4096
ds_read_b64_tr_b16 a[110:111],v35 offset:4608
s_waitcnt lgkmcnt(8)
v_mfma_f32_16x16x32_bf16 v[16:19],a[80:83],v[72:75],v[16:19]
v_mfma_f32_16x16x32_bf16 v[20:23],a[84:87],v[72:75],v[20:23]
v_mfma_f32_16x16x32_bf16 v[16:19],a[88:91],v[72:75],v[16:19]
v_mfma_f32_16x16x32_bf16 v[20:23],a[92:95],v[72:75],v[20:23]
s_waitcnt vmcnt(24)
s_barrier
s_add_u32 m0,s88,81920
s_add_u32 s61,s82,6422528
buffer_load_dwordx4 v24,s[40:43],s61 offen offset:0  lds
s_add_u32 m0,s88,82944
ds_read_b64_tr_b16 a[112:113],v34 offset:16384
buffer_load_dwordx4 v25,s[40:43],s61 offen offset:0  lds
s_add_u32 m0,s88,89984
ds_read_b64_tr_b16 a[114:115],v34 offset:16896
buffer_load_dwordx4 v24,s[40:43],s61 offen offset:128  lds
s_add_u32 m0,s88,91008
ds_read_b64_tr_b16 a[116:117],v35 offset:16384
buffer_load_dwordx4 v25,s[40:43],s61 offen offset:128  lds
ds_read_b64_tr_b16 a[118:119],v35 offset:16896
ds_read_b64_tr_b16 a[120:121],v34 offset:20480
ds_read_b64_tr_b16 a[122:123],v34 offset:20992
ds_read_b64_tr_b16 a[124:125],v35 offset:20480
ds_read_b64_tr_b16 a[126:127],v35 offset:20992
s_waitcnt lgkmcnt(8)
v_mfma_f32_16x16x32_bf16 v[16:19],a[96:99],v[72:75],v[16:19]
v_mfma_f32_16x16x32_bf16 v[20:23],a[100:103],v[72:75],v[20:23]
v_mfma_f32_16x16x32_bf16 v[16:19],a[104:107],v[72:75],v[16:19]
v_mfma_f32_16x16x32_bf16 v[20:23],a[108:111],v[72:75],v[20:23]
s_waitcnt vmcnt(24)
s_barrier
s_add_u32 m0,s88,98304
s_add_u32 s61,s82,6881280
buffer_load_dwordx4 v24,s[40:43],s61 offen offset:0  lds
s_add_u32 m0,s88,99328
ds_read_b64_tr_b16 a[128:129],v34 offset:32768
buffer_load_dwordx4 v25,s[40:43],s61 offen offset:0  lds
s_add_u32 m0,s88,106368
ds_read_b64_tr_b16 a[130:131],v34 offset:33280
buffer_load_dwordx4 v24,s[40:43],s61 offen offset:128  lds
s_add_u32 m0,s88,107392
ds_read_b64_tr_b16 a[132:133],v35 offset:32768
buffer_load_dwordx4 v25,s[40:43],s61 offen offset:128  lds
ds_read_b64_tr_b16 a[134:135],v35 offset:33280
ds_read_b64_tr_b16 a[136:137],v34 offset:36864
ds_read_b64_tr_b16 a[138:139],v34 offset:37376
ds_read_b64_tr_b16 a[140:141],v35 offset:36864
ds_read_b64_tr_b16 a[142:143],v35 offset:37376
s_waitcnt lgkmcnt(8)
v_mfma_f32_16x16x32_bf16 v[16:19],a[112:115],v[72:75],v[16:19]
v_mfma_f32_16x16x32_bf16 v[20:23],a[116:119],v[72:75],v[20:23]
v_mfma_f32_16x16x32_bf16 v[16:19],a[120:123],v[72:75],v[16:19]
v_mfma_f32_16x16x32_bf16 v[20:23],a[124:127],v[72:75],v[20:23]
s_waitcnt vmcnt(24)
s_barrier
ds_read_b64_tr_b16 a[144:145],v26 offset:0
ds_read_b64_tr_b16 a[146:147],v26 offset:512
ds_read_b64_tr_b16 a[148:149],v27 offset:0
ds_read_b64_tr_b16 a[150:151],v27 offset:512
ds_read_b64_tr_b16 a[152:153],v26 offset:4096
ds_read_b64_tr_b16 a[154:155],v26 offset:4608
ds_read_b64_tr_b16 a[156:157],v27 offset:4096
ds_read_b64_tr_b16 a[158:159],v27 offset:4608
s_waitcnt lgkmcnt(8)
v_mfma_f32_16x16x32_bf16 v[16:19],a[128:131],v[72:75],v[16:19]
v_mfma_f32_16x16x32_bf16 v[20:23],a[132:135],v[72:75],v[20:23]
v_mfma_f32_16x16x32_bf16 v[16:19],a[136:139],v[72:75],v[16:19]
v_mfma_f32_16x16x32_bf16 v[20:23],a[140:143],v[72:75],v[20:23]
s_waitcnt vmcnt(20)
s_barrier
ds_read_b64_tr_b16 a[160:161],v26 offset:16384
ds_read_b64_tr_b16 a[162:163],v26 offset:16896
ds_read_b64_tr_b16 a[164:165],v27 offset:16384
ds_read_b64_tr_b16 a[166:167],v27 offset:16896
ds_read_b64_tr_b16 a[168:169],v26 offset:20480
ds_read_b64_tr_b16 a[170:171],v26 offset:20992
ds_read_b64_tr_b16 a[172:173],v27 offset:20480
ds_read_b64_tr_b16 a[174:175],v27 offset:20992
s_waitcnt lgkmcnt(8)
v_mfma_f32_16x16x32_bf16 v[16:19],a[144:147],v[72:75],v[16:19]
v_mfma_f32_16x16x32_bf16 v[20:23],a[148:151],v[72:75],v[20:23]
v_mfma_f32_16x16x32_bf16 v[16:19],a[152:155],v[72:75],v[16:19]
v_mfma_f32_16x16x32_bf16 v[20:23],a[156:159],v[72:75],v[20:23]
s_waitcnt vmcnt(16)
s_barrier
ds_read_b64_tr_b16 a[176:177],v26 offset:32768
ds_read_b64_tr_b16 a[178:179],v26 offset:33280
ds_read_b64_tr_b16 a[180:181],v27 offset:32768
ds_read_b64_tr_b16 a[182:183],v27 offset:33280
ds_read_b64_tr_b16 a[184:185],v26 offset:36864
ds_read_b64_tr_b16 a[186:187],v26 offset:37376
ds_read_b64_tr_b16 a[188:189],v27 offset:36864
ds_read_b64_tr_b16 a[190:191],v27 offset:37376
s_getpc_b64 s[46:47]
Lg3o_cp_%=:
s_add_u32 s46,s46,Lg3o_init_%=-Lg3o_cp_%=
s_addc_u32 s47,s47,0
s_lshl_b32 s68,s54,13
v_lshl_add_u32 v56,v2,3,s68
v_min_u32 v57,Lg3o_done_%=-Lg3o_init_%=,v56
global_load_dword v60,v57,s[46:47]
s_waitcnt lgkmcnt(8)
v_mfma_f32_16x16x32_bf16 v[16:19],a[160:163],v[72:75],v[16:19]
v_mfma_f32_16x16x32_bf16 v[20:23],a[164:167],v[72:75],v[20:23]
v_mfma_f32_16x16x32_bf16 v[16:19],a[168:171],v[72:75],v[16:19]
v_mfma_f32_16x16x32_bf16 v[20:23],a[172:175],v[72:75],v[20:23]
s_waitcnt vmcnt(13)
s_barrier
ds_read_b64_tr_b16 a[192:193],v32 offset:0
ds_read_b64_tr_b16 a[194:195],v32 offset:512
ds_read_b64_tr_b16 a[196:197],v33 offset:0
ds_read_b64_tr_b16 a[198:199],v33 offset:512
ds_read_b64_tr_b16 a[200:201],v32 offset:4096
ds_read_b64_tr_b16 a[202:203],v32 offset:4608
ds_read_b64_tr_b16 a[204:205],v33 offset:4096
ds_read_b64_tr_b16 a[206:207],v33 offset:4608
s_waitcnt lgkmcnt(8)
v_mfma_f32_16x16x32_bf16 v[16:19],a[176:179],v[72:75],v[16:19])"
R"(
v_mfma_f32_16x16x32_bf16 v[20:23],a[180:183],v[72:75],v[20:23]
v_mfma_f32_16x16x32_bf16 v[16:19],a[184:187],v[72:75],v[16:19]
v_mfma_f32_16x16x32_bf16 v[20:23],a[188:191],v[72:75],v[20:23]
s_waitcnt vmcnt(9)
s_barrier
ds_read_b64_tr_b16 a[208:209],v32 offset:16384
ds_read_b64_tr_b16 a[210:211],v32 offset:16896
ds_read_b64_tr_b16 a[212:213],v33 offset:16384
ds_read_b64_tr_b16 a[214:215],v33 offset:16896
ds_read_b64_tr_b16 a[216:217],v32 offset:20480
ds_read_b64_tr_b16 a[218:219],v32 offset:20992
ds_read_b64_tr_b16 a[220:221],v33 offset:20480
ds_read_b64_tr_b16 a[222:223],v33 offset:20992
s_waitcnt lgkmcnt(8)
v_mfma_f32_16x16x32_bf16 v[16:19],a[192:195],v[72:75],v[16:19]
v_mfma_f32_16x16x32_bf16 v[20:23],a[196:199],v[72:75],v[20:23]
v_mfma_f32_16x16x32_bf16 v[16:19],a[200:203],v[72:75],v[16:19]
v_mfma_f32_16x16x32_bf16 v[20:23],a[204:207],v[72:75],v[20:23]
s_waitcnt vmcnt(5)
s_barrier
ds_read_b64_tr_b16 a[224:225],v32 offset:32768
ds_read_b64_tr_b16 a[226:227],v32 offset:33280
ds_read_b64_tr_b16 a[228:229],v33 offset:32768
ds_read_b64_tr_b16 a[230:231],v33 offset:33280
ds_read_b64_tr_b16 a[232:233],v32 offset:36864
ds_read_b64_tr_b16 a[234:235],v32 offset:37376
ds_read_b64_tr_b16 a[236:237],v33 offset:36864
ds_read_b64_tr_b16 a[238:239],v33 offset:37376
s_waitcnt lgkmcnt(8)
v_mfma_f32_16x16x32_bf16 v[16:19],a[208:211],v[72:75],v[16:19]
v_mfma_f32_16x16x32_bf16 v[20:23],a[212:215],v[72:75],v[20:23]
v_mfma_f32_16x16x32_bf16 v[16:19],a[216:219],v[72:75],v[16:19]
v_mfma_f32_16x16x32_bf16 v[20:23],a[220:223],v[72:75],v[20:23]
s_waitcnt vmcnt(1)
s_barrier
ds_read_b64_tr_b16 a[240:241],v34 offset:0
ds_read_b64_tr_b16 a[242:243],v34 offset:512
ds_read_b64_tr_b16 a[244:245],v35 offset:0
ds_read_b64_tr_b16 a[246:247],v35 offset:512
ds_read_b64_tr_b16 a[248:249],v34 offset:4096
ds_read_b64_tr_b16 a[250:251],v34 offset:4608
ds_read_b64_tr_b16 a[252:253],v35 offset:4096
ds_read_b64_tr_b16 a[254:255],v35 offset:4608
s_waitcnt lgkmcnt(8)
v_mfma_f32_16x16x32_bf16 v[16:19],a[224:227],v[72:75],v[16:19]
v_mfma_f32_16x16x32_bf16 v[20:23],a[228:231],v[72:75],v[20:23]
v_mfma_f32_16x16x32_bf16 v[16:19],a[232:235],v[72:75],v[16:19]
v_mfma_f32_16x16x32_bf16 v[20:23],a[236:239],v[72:75],v[20:23]
s_waitcnt lgkmcnt(0)
v_mfma_f32_16x16x32_bf16 v[16:19],a[240:243],v[72:75],v[16:19]
v_mfma_f32_16x16x32_bf16 v[20:23],a[244:247],v[72:75],v[20:23]
v_mfma_f32_16x16x32_bf16 v[16:19],a[248:251],v[72:75],v[16:19]
v_mfma_f32_16x16x32_bf16 v[20:23],a[252:255],v[72:75],v[20:23]
s_waitcnt lgkmcnt(0)
s_barrier
v_readfirstlane_b32 s60,v5
s_nop 3
s_add_u32 s60,s60,1
s_cmp_eq_u32 s60,0
s_cselect_b32 s60,1,s60
Lg3o_init_%=:
v_mov_b32 v5,s60
s_mov_b32 s59,536870912
s_add_u32 s57,s56,131072
s_mov_b32 s79,0
s_mov_b32 s75,0x3a800000
s_mov_b32 s76,0xc0135761
s_mov_b32 s77,0x3d372713
s_mov_b32 s44,s59
s_mov_b32 s45,s59
s_mov_b32 s46,s59
s_mov_b32 s47,s59
s_mov_b32 s48,s83
s_add_u32 s49,s48,s55
s_cmp_eq_u32 s49,s56
s_cselect_b32 s49,s57,s49
s_cmp_eq_u32 s49,s58
s_cselect_b32 s49,s59,s49
s_add_u32 s50,s49,s55
s_cmp_eq_u32 s50,s56
s_cselect_b32 s50,s57,s50
s_cmp_eq_u32 s50,s58
s_cselect_b32 s50,s59,s50
s_add_u32 s51,s50,s55
s_cmp_eq_u32 s51,s56
s_cselect_b32 s51,s57,s51
s_cmp_eq_u32 s51,s58
s_cselect_b32 s51,s59,s51
s_add_u32 s52,s51,s55
s_cmp_eq_u32 s52,s56
s_cselect_b32 s52,s57,s52
s_cmp_eq_u32 s52,s58
s_cselect_b32 s52,s59,s52
s_lshl_b32 s63,s54,12
s_add_u32 s63,s63,65536
s_add_u32 s64,s63,32768
s_lshl_b32 s88,s54,10
s_add_u32 s88,s88,131072
s_lshl_b32 s78,s54,11
s_add_u32 s78,s78,132608
v_add_u32 v1,16384,v0
v_mov_b32 v12,0x3727c5ac
s_getpc_b64 s[90:91]
Lg3o_pc_%=:
s_mov_b32 s68,Lg3o_d1w0_%=-Lg3o_pc_%=
s_cmp_eq_u32 s54,1
s_cselect_b32 s68,Lg3o_d1w1_%=-Lg3o_pc_%=,s68
s_cmp_eq_u32 s54,2
s_cselect_b32 s68,Lg3o_d1w2_%=-Lg3o_pc_%=,s68
s_cmp_eq_u32 s54,3
s_cselect_b32 s68,Lg3o_d1w3_%=-Lg3o_pc_%=,s68
s_add_u32 s92,s90,s68
s_addc_u32 s93,s91,0
s_mov_b32 s68,Lg3o_d0w0_%=-Lg3o_pc_%=
s_cmp_eq_u32 s54,1
s_cselect_b32 s68,Lg3o_d0w1_%=-Lg3o_pc_%=,s68
s_cmp_eq_u32 s54,2
s_cselect_b32 s68,Lg3o_d0w2_%=-Lg3o_pc_%=,s68
s_cmp_eq_u32 s54,3
s_cselect_b32 s68,Lg3o_d0w3_%=-Lg3o_pc_%=,s68
s_add_u32 s90,s90,s68
s_addc_u32 s91,s91,0
s_mov_b32 s66,s59
s_mov_b32 s65,0
s_mov_b32 s62,s63
s_branch Lg3o_slow0_%=
Lg3o_d0w0_%=:
v_mfma_f32_16x16x32_bf16 v[24:27],a[0:3],v[40:43],0
s_add_u32 m0,s62,16384
ds_read_b128 v[68:71],v2 offset:7168
v_mul_f32 v96,s75,v92
v_mfma_f32_16x16x32_bf16 v[28:31],a[4:7],v[40:43],0
buffer_load_dwordx4 v1,s[36:39],s66 offen offset:0 sc1 lds
v_mfma_f32_16x16x32_bf16 v[24:27],a[8:11],v[44:47],v[24:27]
s_mul_i32 s73,s47,7
ds_read_b128 v[40:43],v2 offset:8192
v_fma_f32 v93,v93,s75,v12
v_mfma_f32_16x16x32_bf16 v[28:31],a[12:15],v[44:47],v[28:31]
s_lshr_b32 s73,s73,1
v_fma_f32 v93,-v96,v96,v93
v_max_f32 v93,v93,v12
v_mfma_f32_16x16x32_bf16 v[24:27],a[16:19],v[48:51],v[24:27]
s_lshr_b32 s72,s66,5
ds_read_b128 v[44:47],v2 offset:9216
v_fma_f32 v32,-v96,v16,v32
v_mfma_f32_16x16x32_bf16 v[28:31],a[20:23],v[48:51],v[28:31]
buffer_load_dwordx4 v1,s[36:39],s66 offen offset:1024 sc1 lds
v_mfma_f32_16x16x32_bf16 v[24:27],a[24:27],v[52:55],v[24:27]
s_lshr_b32 s71,s62,2
ds_read_b128 v[48:51],v2 offset:10240
v_fma_f32 v33,-v96,v17,v33
v_mfma_f32_16x16x32_bf16 v[28:31],a[28:31],v[52:55],v[28:31]
s_waitcnt lgkmcnt(3)
v_rsq_f32 v92,v93
v_mfma_f32_16x16x32_bf16 v[24:27],a[32:35],v[56:59],v[24:27]
ds_read_b128 v[52:55],v2 offset:11264
v_mul_f32 v97,s76,v92
v_mfma_f32_16x16x32_bf16 v[28:31],a[36:39],v[56:59],v[28:31]
buffer_load_dwordx4 v1,s[36:39],s66 offen offset:2048 sc1 lds
v_mfma_f32_16x16x32_bf16 v[24:27],a[40:43],v[60:63],v[24:27]
ds_read_b128 v[56:59],v2 offset:12288
v_mul_f32 v98,v92,v92
v_mfma_f32_16x16x32_bf16 v[28:31],a[44:47],v[60:63],v[28:31]
v_mul_f32 v99,v93,v92
v_mul_f32 v98,s77,v98
v_mfma_f32_16x16x32_bf16 v[24:27],a[48:51],v[64:67],v[24:27]
ds_read_b128 v[60:63],v2 offset:13312
v_mul_f32 v98,v98,v97
v_mfma_f32_16x16x32_bf16 v[28:31],a[52:55],v[64:67],v[28:31]
buffer_load_dwordx4 v1,s[36:39],s66 offen offset:3072 sc1 lds
v_mfma_f32_16x16x32_bf16 v[24:27],a[56:59],v[68:71],v[24:27]
s_add_u32 m0,s71,s88
ds_read_b128 v[64:67],v2 offset:14336
v_fma_f32 v34,-v96,v18,v34
v_mfma_f32_16x16x32_bf16 v[28:31],a[60:63],v[68:71],v[28:31]
s_waitcnt lgkmcnt(3)
buffer_load_dword v9,s[36:39],s72 offen offset:0 sc1 lds
v_mfma_f32_16x16x32_bf16 v[24:27],a[64:67],v[40:43],v[24:27]
s_waitcnt vmcnt(11)
ds_read_b128 v[68:71],v2 offset:15360
v_fma_f32 v35,-v96,v19,v35
v_mfma_f32_16x16x32_bf16 v[28:31],a[68:71],v[40:43],v[28:31]
s_barrier
v_mfma_f32_16x16x32_bf16 v[24:27],a[72:75],v[44:47],v[24:27]
s_xor_b32 s87,s64,65536
ds_read_b128 v[40:43],v2 offset:16384
ds_read_b32 v88,v8 offset:0
v_mfma_f32_16x16x32_bf16 v[28:31],a[76:79],v[44:47],v[28:31]
s_cmp_ge_u32 s66,s59
v_mul_f32 v72,v32,v32
v_mul_f32 v73,v33,v33
v_mfma_f32_16x16x32_bf16 v[24:27],a[80:83],v[48:51],v[24:27]
s_cselect_b32 s86,s49,s50
ds_read_b128 v[44:47],v2 offset:17408
v_mul_f32 v74,v34,v34
v_mfma_f32_16x16x32_bf16 v[28:31],a[84:87],v[48:51],v[28:31]
s_cselect_b32 s87,s87,s63
v_mul_f32 v75,v35,v35
v_fma_f32 v72,v98,v72,v97
v_mfma_f32_16x16x32_bf16 v[24:27],a[88:91],v[52:55],v[24:27])"
R"(
s_lshr_b32 s68,s50,5
ds_read_b128 v[48:51],v2 offset:18432
v_mul_f32 v72,v32,v72
v_mfma_f32_16x16x32_bf16 v[28:31],a[92:95],v[52:55],v[28:31]
s_waitcnt lgkmcnt(4)
v_exp_f32 v72,v72
v_mfma_f32_16x16x32_bf16 v[24:27],a[96:99],v[56:59],v[24:27]
s_waitcnt vmcnt(10) lgkmcnt(2)
ds_read_b128 v[52:55],v2 offset:19456
v_fma_f32 v72,v72,v99,v99
v_mfma_f32_16x16x32_bf16 v[28:31],a[100:103],v[56:59],v[28:31]
s_barrier
v_mfma_f32_16x16x32_bf16 v[24:27],a[104:107],v[60:63],v[24:27]
s_lshr_b32 s70,s51,5
ds_read_b128 v[56:59],v2 offset:20480
ds_read_b32 v13,v7 offset:0
v_mfma_f32_16x16x32_bf16 v[28:31],a[108:111],v[60:63],v[28:31]
s_mov_b32 s62,s87
v_rcp_f32 v72,v72
v_mfma_f32_16x16x32_bf16 v[24:27],a[112:115],v[64:67],v[24:27]
ds_read_b128 v[60:63],v2 offset:21504
v_add_f32_dpp v88,v88,v88 quad_perm:[1,0,3,2] row_mask:0xf bank_mask:0xf
v_mfma_f32_16x16x32_bf16 v[28:31],a[116:119],v[64:67],v[28:31]
s_nop 0
v_add_f32_dpp v88,v88,v88 quad_perm:[2,3,0,1] row_mask:0xf bank_mask:0xf
v_fma_f32 v73,v98,v73,v97
v_mfma_f32_16x16x32_bf16 v[24:27],a[120:123],v[68:71],v[24:27]
s_waitcnt lgkmcnt(1)
ds_read_b128 v[64:67],v2 offset:22528
v_mul_f32 v73,v33,v73
v_mfma_f32_16x16x32_bf16 v[28:31],a[124:127],v[68:71],v[28:31]
v_cmp_eq_u32 vcc,s60,v13
v_exp_f32 v73,v73
v_add_f32_dpp v88,v88,v88 row_half_mirror row_mask:0xf bank_mask:0xf
v_mfma_f32_16x16x32_bf16 v[24:27],a[128:131],v[40:43],v[24:27]
s_cmp_eq_u32 vcc_lo,-1
ds_write_b32 v10,v88 offset:0
v_mfma_f32_16x16x32_bf16 v[28:31],a[132:135],v[40:43],v[28:31]
s_cselect_b32 s67,s86,s59
s_cmp_ge_u32 s67,s59
s_cselect_b32 s68,s68,s70
ds_read_b128 v[68:71],v2 offset:23552
s_add_u32 m0,s78,256
v_mfma_f32_16x16x32_bf16 v[24:27],a[136:139],v[44:47],v[24:27]
buffer_load_dword v6,s[36:39],s68 offen offset:0 sc1 lds
v_mfma_f32_16x16x32_bf16 v[28:31],a[140:143],v[44:47],v[28:31]
s_mov_b32 m0,s62
ds_read_b128 v[40:43],v2 offset:24576
v_fma_f32 v73,v73,v99,v99
v_mfma_f32_16x16x32_bf16 v[24:27],a[144:147],v[48:51],v[24:27]
buffer_load_dwordx4 v0,s[36:39],s67 offen offset:0 sc1 lds
v_mfma_f32_16x16x32_bf16 v[28:31],a[148:151],v[48:51],v[28:31]
s_xor_b32 s63,s63,65536
ds_read_b128 v[44:47],v2 offset:25600
v_fma_f32 v74,v98,v74,v97
v_mfma_f32_16x16x32_bf16 v[24:27],a[152:155],v[52:55],v[24:27]
s_add_u32 s53,s52,s55
ds_read_b128 v[48:51],v2 offset:26624
v_mul_f32 v74,v34,v74
v_mfma_f32_16x16x32_bf16 v[28:31],a[156:159],v[52:55],v[28:31]
s_waitcnt lgkmcnt(3)
v_exp_f32 v74,v74
v_mfma_f32_16x16x32_bf16 v[24:27],a[160:163],v[56:59],v[24:27]
buffer_load_dwordx4 v0,s[36:39],s67 offen offset:1024 sc1 lds
v_mfma_f32_16x16x32_bf16 v[28:31],a[164:167],v[56:59],v[28:31]
s_cmp_eq_u32 s53,s56
ds_read_b128 v[52:55],v2 offset:27648
v_fma_f32 v74,v74,v99,v99
v_mfma_f32_16x16x32_bf16 v[24:27],a[168:171],v[60:63],v[24:27]
s_cselect_b32 s53,s57,s53
ds_read_b128 v[56:59],v2 offset:28672
v_fma_f32 v75,v98,v75,v97
v_mfma_f32_16x16x32_bf16 v[28:31],a[172:175],v[60:63],v[28:31]
s_cmp_eq_u32 s53,s58
v_rcp_f32 v73,v73
v_mfma_f32_16x16x32_bf16 v[24:27],a[176:179],v[64:67],v[24:27]
buffer_load_dwordx4 v0,s[36:39],s67 offen offset:2048 sc1 lds
v_mfma_f32_16x16x32_bf16 v[28:31],a[180:183],v[64:67],v[28:31]
s_cselect_b32 s53,s59,s53
ds_read_b128 v[60:63],v2 offset:29696
v_mul_f32 v75,v35,v75
v_mfma_f32_16x16x32_bf16 v[24:27],a[184:187],v[68:71],v[24:27]
s_cmp_ge_u32 s66,s59
ds_read_b128 v[64:67],v2 offset:30720
v_mul_f32 v32,v32,v72
v_mfma_f32_16x16x32_bf16 v[28:31],a[188:191],v[68:71],v[28:31]
s_waitcnt lgkmcnt(3)
v_exp_f32 v75,v75
v_mfma_f32_16x16x32_bf16 v[24:27],a[192:195],v[40:43],v[24:27]
s_waitcnt vmcnt(9)
ds_read_b128 v[68:71],v2 offset:31744
v_fma_f32 v75,v75,v99,v99
v_mfma_f32_16x16x32_bf16 v[28:31],a[196:199],v[40:43],v[28:31]
s_barrier
v_fma_f32 v36,-v96,v20,v36
v_fma_f32 v37,-v96,v21,v37
v_fma_f32 v38,-v96,v22,v38
v_fma_f32 v39,-v96,v23,v39
v_mul_f32 v76,v36,v36
v_mul_f32 v77,v37,v37
v_mul_f32 v78,v38,v38
v_mul_f32 v79,v39,v39
v_fma_f32 v76,v98,v76,v97
v_fma_f32 v77,v98,v77,v97
v_fma_f32 v78,v98,v78,v97
v_fma_f32 v79,v98,v79,v97
v_mul_f32 v76,v36,v76
v_mul_f32 v77,v37,v77
v_mul_f32 v78,v38,v78
v_mul_f32 v79,v39,v79
v_exp_f32 v76,v76
v_exp_f32 v77,v77
v_exp_f32 v78,v78
v_exp_f32 v79,v79
v_mfma_f32_16x16x32_bf16 v[24:27],a[200:203],v[44:47],v[24:27]
buffer_load_dwordx4 v0,s[36:39],s67 offen offset:3072 sc1 lds
v_rcp_f32 v74,v74
v_rcp_f32 v75,v75
v_fma_f32 v76,v76,v99,v99
v_fma_f32 v77,v77,v99,v99
v_fma_f32 v78,v78,v99,v99
v_fma_f32 v79,v79,v99,v99
v_rcp_f32 v76,v76
v_rcp_f32 v77,v77
v_rcp_f32 v78,v78
v_rcp_f32 v79,v79
v_mfma_f32_16x16x32_bf16 v[28:31],a[204:207],v[44:47],v[28:31]
ds_read_b128 v[40:43],v2 offset:32768
v_mul_f32 v33,v33,v73
v_mul_f32 v34,v34,v74
v_mul_f32 v35,v35,v75
v_cvt_pk_bf16_f32 v80,v32,v33
v_cvt_pk_bf16_f32 v81,v34,v35
v_mul_f32 v36,v36,v76
v_mul_f32 v37,v37,v77
v_mul_f32 v38,v38,v78
v_mul_f32 v39,v39,v79
v_cvt_pk_bf16_f32 v82,v36,v37
v_cvt_pk_bf16_f32 v83,v38,v39
v_mfma_f32_16x16x32_bf16 v[24:27],a[208:211],v[48:51],v[24:27]
buffer_store_dwordx4 v[80:83],v3,s[36:39],s73 offen offset:0 sc0 sc1
ds_read_b128 v[44:47],v2 offset:33792
ds_read_b64 v[92:93],v11 offset:0
v_mfma_f32_16x16x32_bf16 v[28:31],a[212:215],v[48:51],v[28:31]
v_mfma_f32_16x16x32_bf16 v[24:27],a[216:219],v[52:55],v[24:27]
ds_read_b128 v[48:51],v2 offset:34816
v_mfma_f32_16x16x32_bf16 v[28:31],a[220:223],v[52:55],v[28:31]
s_waitcnt lgkmcnt(4)
v_mfma_f32_16x16x32_bf16 v[24:27],a[224:227],v[56:59],v[24:27]
s_waitcnt lgkmcnt(1)
ds_read_b128 v[52:55],v2 offset:35840
v_mfma_f32_16x16x32_bf16 v[28:31],a[228:231],v[56:59],v[28:31]
v_mfma_f32_16x16x32_bf16 v[24:27],a[232:235],v[60:63],v[24:27]
ds_read_b128 v[56:59],v2 offset:36864
v_mfma_f32_16x16x32_bf16 v[28:31],a[236:239],v[60:63],v[28:31]
v_mfma_f32_16x16x32_bf16 v[24:27],a[240:243],v[64:67],v[24:27]
ds_read_b128 v[60:63],v2 offset:37888
v_mfma_f32_16x16x32_bf16 v[28:31],a[244:247],v[64:67],v[28:31]
v_mfma_f32_16x16x32_bf16 v[24:27],a[248:251],v[68:71],v[24:27]
s_waitcnt lgkmcnt(2)
ds_read_b128 v[64:67],v2 offset:38912
v_mfma_f32_16x16x32_bf16 v[28:31],a[252:255],v[68:71],v[28:31]
s_cbranch_scc1 Lg3o_slow1_%=
Lg3o_d1w0_%=:
v_mfma_f32_16x16x32_bf16 v[32:35],a[0:3],v[40:43],0
s_add_u32 m0,s62,16384
ds_read_b128 v[68:71],v2 offset:39936
v_mul_f32 v96,s75,v92
v_mfma_f32_16x16x32_bf16 v[36:39],a[4:7],v[40:43],0
buffer_load_dwordx4 v1,s[36:39],s67 offen offset:0 sc1 lds
v_mfma_f32_16x16x32_bf16 v[32:35],a[8:11],v[44:47],v[32:35]
s_mul_i32 s73,s48,7
ds_read_b128 v[40:43],v2 offset:40960
v_fma_f32 v93,v93,s75,v12
v_mfma_f32_16x16x32_bf16 v[36:39],a[12:15],v[44:47],v[36:39]
s_lshr_b32 s73,s73,1
v_fma_f32 v93,-v96,v96,v93
v_max_f32 v93,v93,v12
v_mfma_f32_16x16x32_bf16 v[32:35],a[16:19],v[48:51],v[32:35]
s_lshr_b32 s72,s67,5
ds_read_b128 v[44:47],v2 offset:41984
v_fma_f32 v24,-v96,v16,v24
v_mfma_f32_16x16x32_bf16 v[36:39],a[20:23],v[48:51],v[36:39]
buffer_load_dwordx4 v1,s[36:39],s67 offen offset:1024 sc1 lds
v_mfma_f32_16x16x32_bf16 v[32:35],a[24:27],v[52:55],v[32:35]
s_lshr_b32 s71,s62,2
ds_read_b128 v[48:51],v2 offset:43008
v_fma_f32 v25,-v96,v17,v25
v_mfma_f32_16x16x32_bf16 v[36:39],a[28:31],v[52:55],v[36:39]
s_waitcnt lgkmcnt(3)
v_rsq_f32 v92,v93
v_mfma_f32_16x16x32_bf16 v[32:35],a[32:35],v[56:59],v[32:35]
s_mov_b64 s[44:45],s[46:47]
ds_read_b128 v[52:55],v2 offset:44032
v_mul_f32 v97,s76,v92
v_mfma_f32_16x16x32_bf16 v[36:39],a[36:39],v[56:59],v[36:39]
buffer_load_dwordx4 v1,s[36:39],s67 offen offset:2048 sc1 lds
v_mfma_f32_16x16x32_bf16 v[32:35],a[40:43],v[60:63],v[32:35]
s_mov_b64 s[46:47],s[48:49]
ds_read_b128 v[56:59],v2 offset:45056)"
R"(
v_mul_f32 v98,v92,v92
v_mfma_f32_16x16x32_bf16 v[36:39],a[44:47],v[60:63],v[36:39]
s_mov_b64 s[48:49],s[50:51]
v_mul_f32 v99,v93,v92
v_mul_f32 v98,s77,v98
v_mfma_f32_16x16x32_bf16 v[32:35],a[48:51],v[64:67],v[32:35]
ds_read_b128 v[60:63],v2 offset:46080
v_mul_f32 v98,v98,v97
v_mfma_f32_16x16x32_bf16 v[36:39],a[52:55],v[64:67],v[36:39]
buffer_load_dwordx4 v1,s[36:39],s67 offen offset:3072 sc1 lds
v_mfma_f32_16x16x32_bf16 v[32:35],a[56:59],v[68:71],v[32:35]
s_add_u32 m0,s71,s88
ds_read_b128 v[64:67],v2 offset:47104
v_fma_f32 v26,-v96,v18,v26
v_mfma_f32_16x16x32_bf16 v[36:39],a[60:63],v[68:71],v[36:39]
s_waitcnt lgkmcnt(3)
buffer_load_dword v9,s[36:39],s72 offen offset:0 sc1 lds
v_mfma_f32_16x16x32_bf16 v[32:35],a[64:67],v[40:43],v[32:35]
s_waitcnt vmcnt(11)
ds_read_b128 v[68:71],v2 offset:48128
v_fma_f32 v27,-v96,v19,v27
v_mfma_f32_16x16x32_bf16 v[36:39],a[68:71],v[40:43],v[36:39]
s_barrier
v_mfma_f32_16x16x32_bf16 v[32:35],a[72:75],v[44:47],v[32:35]
s_xor_b32 s87,s63,65536
ds_read_b128 v[40:43],v2 offset:49152
ds_read_b32 v88,v8 offset:8192
v_mfma_f32_16x16x32_bf16 v[36:39],a[76:79],v[44:47],v[36:39]
s_cmp_ge_u32 s67,s59
v_mul_f32 v72,v24,v24
v_mul_f32 v73,v25,v25
v_mfma_f32_16x16x32_bf16 v[32:35],a[80:83],v[48:51],v[32:35]
s_cselect_b32 s86,s50,s51
ds_read_b128 v[44:47],v2 offset:50176
v_mul_f32 v74,v26,v26
v_mfma_f32_16x16x32_bf16 v[36:39],a[84:87],v[48:51],v[36:39]
s_cselect_b32 s87,s87,s64
v_mul_f32 v75,v27,v27
v_fma_f32 v72,v98,v72,v97
v_mfma_f32_16x16x32_bf16 v[32:35],a[88:91],v[52:55],v[32:35]
s_lshr_b32 s68,s51,5
ds_read_b128 v[48:51],v2 offset:51200
v_mul_f32 v72,v24,v72
v_mfma_f32_16x16x32_bf16 v[36:39],a[92:95],v[52:55],v[36:39]
s_waitcnt lgkmcnt(4)
v_exp_f32 v72,v72
v_mfma_f32_16x16x32_bf16 v[32:35],a[96:99],v[56:59],v[32:35]
s_waitcnt vmcnt(10) lgkmcnt(2)
ds_read_b128 v[52:55],v2 offset:52224
v_fma_f32 v72,v72,v99,v99
v_mfma_f32_16x16x32_bf16 v[36:39],a[100:103],v[56:59],v[36:39]
s_barrier
v_mfma_f32_16x16x32_bf16 v[32:35],a[104:107],v[60:63],v[32:35]
s_lshr_b32 s70,s52,5
ds_read_b128 v[56:59],v2 offset:53248
ds_read_b32 v13,v7 offset:256
v_mfma_f32_16x16x32_bf16 v[36:39],a[108:111],v[60:63],v[36:39]
s_mov_b32 s62,s87
v_rcp_f32 v72,v72
v_mfma_f32_16x16x32_bf16 v[32:35],a[112:115],v[64:67],v[32:35]
s_mov_b64 s[50:51],s[52:53]
ds_read_b128 v[60:63],v2 offset:54272
v_add_f32_dpp v88,v88,v88 quad_perm:[1,0,3,2] row_mask:0xf bank_mask:0xf
v_mfma_f32_16x16x32_bf16 v[36:39],a[116:119],v[64:67],v[36:39]
s_nop 0
v_add_f32_dpp v88,v88,v88 quad_perm:[2,3,0,1] row_mask:0xf bank_mask:0xf
v_fma_f32 v73,v98,v73,v97
v_mfma_f32_16x16x32_bf16 v[32:35],a[120:123],v[68:71],v[32:35]
s_waitcnt lgkmcnt(1)
ds_read_b128 v[64:67],v2 offset:55296
v_mul_f32 v73,v25,v73
v_mfma_f32_16x16x32_bf16 v[36:39],a[124:127],v[68:71],v[36:39]
v_cmp_eq_u32 vcc,s60,v13
v_exp_f32 v73,v73
v_add_f32_dpp v88,v88,v88 row_half_mirror row_mask:0xf bank_mask:0xf
v_mfma_f32_16x16x32_bf16 v[32:35],a[128:131],v[40:43],v[32:35]
s_cmp_eq_u32 vcc_lo,-1
ds_write_b32 v10,v88 offset:128
v_mfma_f32_16x16x32_bf16 v[36:39],a[132:135],v[40:43],v[36:39]
s_cselect_b32 s66,s86,s59
s_cmp_ge_u32 s66,s59
s_cselect_b32 s68,s68,s70
ds_read_b128 v[68:71],v2 offset:56320
s_add_u32 m0,s78,0
v_mfma_f32_16x16x32_bf16 v[32:35],a[136:139],v[44:47],v[32:35]
buffer_load_dword v6,s[36:39],s68 offen offset:0 sc1 lds
v_mfma_f32_16x16x32_bf16 v[36:39],a[140:143],v[44:47],v[36:39]
s_mov_b32 m0,s62
ds_read_b128 v[40:43],v2 offset:57344
v_fma_f32 v73,v73,v99,v99
v_mfma_f32_16x16x32_bf16 v[32:35],a[144:147],v[48:51],v[32:35]
buffer_load_dwordx4 v0,s[36:39],s66 offen offset:0 sc1 lds
v_mfma_f32_16x16x32_bf16 v[36:39],a[148:151],v[48:51],v[36:39]
s_xor_b32 s64,s64,65536
ds_read_b128 v[44:47],v2 offset:58368
v_fma_f32 v74,v98,v74,v97
v_mfma_f32_16x16x32_bf16 v[32:35],a[152:155],v[52:55],v[32:35]
s_add_u32 s74,s53,s55
ds_read_b128 v[48:51],v2 offset:59392
v_mul_f32 v74,v26,v74
v_mfma_f32_16x16x32_bf16 v[36:39],a[156:159],v[52:55],v[36:39]
s_waitcnt lgkmcnt(3)
v_exp_f32 v74,v74
v_mfma_f32_16x16x32_bf16 v[32:35],a[160:163],v[56:59],v[32:35]
buffer_load_dwordx4 v0,s[36:39],s66 offen offset:1024 sc1 lds
v_mfma_f32_16x16x32_bf16 v[36:39],a[164:167],v[56:59],v[36:39]
s_cmp_eq_u32 s74,s56
ds_read_b128 v[52:55],v2 offset:60416
v_fma_f32 v74,v74,v99,v99
v_mfma_f32_16x16x32_bf16 v[32:35],a[168:171],v[60:63],v[32:35]
s_cselect_b32 s74,s57,s74
ds_read_b128 v[56:59],v2 offset:61440
v_fma_f32 v75,v98,v75,v97
v_mfma_f32_16x16x32_bf16 v[36:39],a[172:175],v[60:63],v[36:39]
s_cmp_eq_u32 s74,s58
v_rcp_f32 v73,v73
v_mfma_f32_16x16x32_bf16 v[32:35],a[176:179],v[64:67],v[32:35]
buffer_load_dwordx4 v0,s[36:39],s66 offen offset:2048 sc1 lds
v_mfma_f32_16x16x32_bf16 v[36:39],a[180:183],v[64:67],v[36:39]
s_cselect_b32 s74,s59,s74
ds_read_b128 v[60:63],v2 offset:62464
v_mul_f32 v75,v27,v75
v_mfma_f32_16x16x32_bf16 v[32:35],a[184:187],v[68:71],v[32:35]
s_mov_b32 s52,s74
ds_read_b128 v[64:67],v2 offset:63488
v_mul_f32 v24,v24,v72
v_mfma_f32_16x16x32_bf16 v[36:39],a[188:191],v[68:71],v[36:39]
s_waitcnt lgkmcnt(3)
v_exp_f32 v75,v75
v_mfma_f32_16x16x32_bf16 v[32:35],a[192:195],v[40:43],v[32:35]
s_waitcnt vmcnt(9)
ds_read_b128 v[68:71],v2 offset:64512
v_xor_b32 v2,65536,v2
v_mfma_f32_16x16x32_bf16 v[36:39],a[196:199],v[40:43],v[36:39]
s_barrier
v_fma_f32 v28,-v96,v20,v28
v_fma_f32 v29,-v96,v21,v29
v_fma_f32 v30,-v96,v22,v30
v_fma_f32 v31,-v96,v23,v31
v_mul_f32 v76,v28,v28
v_mul_f32 v77,v29,v29
v_mul_f32 v78,v30,v30
v_mul_f32 v79,v31,v31
v_fma_f32 v76,v98,v76,v97
v_fma_f32 v77,v98,v77,v97
v_fma_f32 v78,v98,v78,v97
v_fma_f32 v79,v98,v79,v97
v_mul_f32 v76,v28,v76
v_mul_f32 v77,v29,v77
v_mul_f32 v78,v30,v78
v_mul_f32 v79,v31,v79
v_exp_f32 v76,v76
v_exp_f32 v77,v77
v_exp_f32 v78,v78
v_exp_f32 v79,v79
v_mfma_f32_16x16x32_bf16 v[32:35],a[200:203],v[44:47],v[32:35]
buffer_load_dwordx4 v0,s[36:39],s66 offen offset:3072 sc1 lds
v_fma_f32 v75,v75,v99,v99
v_rcp_f32 v74,v74
v_rcp_f32 v75,v75
v_fma_f32 v76,v76,v99,v99
v_fma_f32 v77,v77,v99,v99
v_fma_f32 v78,v78,v99,v99
v_fma_f32 v79,v79,v99,v99
v_rcp_f32 v76,v76
v_rcp_f32 v77,v77
v_rcp_f32 v78,v78
v_rcp_f32 v79,v79
v_mfma_f32_16x16x32_bf16 v[36:39],a[204:207],v[44:47],v[36:39]
s_cmp_lt_u32 s67,s59
ds_read_b128 v[40:43],v2 offset:0
v_mul_f32 v25,v25,v73
v_mul_f32 v26,v26,v74
v_mul_f32 v27,v27,v75
v_cvt_pk_bf16_f32 v80,v24,v25
v_cvt_pk_bf16_f32 v81,v26,v27
v_mul_f32 v28,v28,v76
v_mul_f32 v29,v29,v77
v_mul_f32 v30,v30,v78
v_mul_f32 v31,v31,v79
v_cvt_pk_bf16_f32 v82,v28,v29
v_cvt_pk_bf16_f32 v83,v30,v31
v_mfma_f32_16x16x32_bf16 v[32:35],a[208:211],v[48:51],v[32:35]
buffer_store_dwordx4 v[80:83],v3,s[36:39],s73 offen offset:0 sc0 sc1
ds_read_b128 v[44:47],v2 offset:1024
ds_read_b64 v[92:93],v11 offset:128
v_xor_b32 v8,16384,v8
v_mfma_f32_16x16x32_bf16 v[36:39],a[212:215],v[48:51],v[36:39]
v_mfma_f32_16x16x32_bf16 v[32:35],a[216:219],v[52:55],v[32:35]
ds_read_b128 v[48:51],v2 offset:2048
v_mfma_f32_16x16x32_bf16 v[36:39],a[220:223],v[52:55],v[36:39]
s_waitcnt lgkmcnt(4)
v_mfma_f32_16x16x32_bf16 v[32:35],a[224:227],v[56:59],v[32:35]
s_waitcnt lgkmcnt(1)
ds_read_b128 v[52:55],v2 offset:3072
v_mfma_f32_16x16x32_bf16 v[36:39],a[228:231],v[56:59],v[36:39]
v_mfma_f32_16x16x32_bf16 v[32:35],a[232:235],v[60:63],v[32:35]
ds_read_b128 v[56:59],v2 offset:4096
v_mfma_f32_16x16x32_bf16 v[36:39],a[236:239],v[60:63],v[36:39]
v_mfma_f32_16x16x32_bf16 v[32:35],a[240:243],v[64:67],v[32:35])"
R"(
ds_read_b128 v[60:63],v2 offset:5120
v_mfma_f32_16x16x32_bf16 v[36:39],a[244:247],v[64:67],v[36:39]
v_mfma_f32_16x16x32_bf16 v[32:35],a[248:251],v[68:71],v[32:35]
s_waitcnt lgkmcnt(2)
ds_read_b128 v[64:67],v2 offset:6144
v_mfma_f32_16x16x32_bf16 v[36:39],a[252:255],v[68:71],v[36:39]
s_cbranch_scc1 Lg3o_d0w0_%=
s_branch Lg3o_slow0_%=
Lg3o_d0w1_%=:
v_mfma_f32_16x16x32_bf16 v[24:27],a[0:3],v[40:43],0
s_add_u32 m0,s62,16384
ds_read_b128 v[68:71],v2 offset:7168
v_mul_f32 v96,s75,v92
v_mfma_f32_16x16x32_bf16 v[28:31],a[4:7],v[40:43],0
s_mul_i32 s73,s47,7
v_fma_f32 v93,v93,s75,v12
v_fma_f32 v93,-v96,v96,v93
v_mfma_f32_16x16x32_bf16 v[24:27],a[8:11],v[44:47],v[24:27]
buffer_load_dwordx4 v1,s[36:39],s66 offen offset:0 sc1 lds
v_mfma_f32_16x16x32_bf16 v[28:31],a[12:15],v[44:47],v[28:31]
s_lshr_b32 s73,s73,1
ds_read_b128 v[40:43],v2 offset:8192
v_max_f32 v93,v93,v12
v_mfma_f32_16x16x32_bf16 v[24:27],a[16:19],v[48:51],v[24:27]
s_lshr_b32 s72,s66,5
ds_read_b128 v[44:47],v2 offset:9216
v_fma_f32 v32,-v96,v16,v32
v_mfma_f32_16x16x32_bf16 v[28:31],a[20:23],v[48:51],v[28:31]
s_lshr_b32 s71,s62,2
v_rsq_f32 v92,v93
v_mfma_f32_16x16x32_bf16 v[24:27],a[24:27],v[52:55],v[24:27]
buffer_load_dwordx4 v1,s[36:39],s66 offen offset:1024 sc1 lds
v_mfma_f32_16x16x32_bf16 v[28:31],a[28:31],v[52:55],v[28:31]
s_waitcnt lgkmcnt(2)
ds_read_b128 v[48:51],v2 offset:10240
v_mul_f32 v97,s76,v92
v_mfma_f32_16x16x32_bf16 v[24:27],a[32:35],v[56:59],v[24:27]
ds_read_b128 v[52:55],v2 offset:11264
v_mul_f32 v98,v92,v92
v_mfma_f32_16x16x32_bf16 v[28:31],a[36:39],v[56:59],v[28:31]
v_mul_f32 v99,v93,v92
v_mul_f32 v98,s77,v98
v_mfma_f32_16x16x32_bf16 v[24:27],a[40:43],v[60:63],v[24:27]
buffer_load_dwordx4 v1,s[36:39],s66 offen offset:2048 sc1 lds
v_mfma_f32_16x16x32_bf16 v[28:31],a[44:47],v[60:63],v[28:31]
ds_read_b128 v[56:59],v2 offset:12288
v_mul_f32 v98,v98,v97
v_mfma_f32_16x16x32_bf16 v[24:27],a[48:51],v[64:67],v[24:27]
ds_read_b128 v[60:63],v2 offset:13312
v_fma_f32 v33,-v96,v17,v33
v_mfma_f32_16x16x32_bf16 v[28:31],a[52:55],v[64:67],v[28:31]
v_fma_f32 v34,-v96,v18,v34
v_fma_f32 v35,-v96,v19,v35
v_mfma_f32_16x16x32_bf16 v[24:27],a[56:59],v[68:71],v[24:27]
buffer_load_dwordx4 v1,s[36:39],s66 offen offset:3072 sc1 lds
v_mfma_f32_16x16x32_bf16 v[28:31],a[60:63],v[68:71],v[28:31]
s_waitcnt lgkmcnt(2)
ds_read_b128 v[64:67],v2 offset:14336
s_add_u32 m0,s71,s88
v_mfma_f32_16x16x32_bf16 v[24:27],a[64:67],v[40:43],v[24:27]
s_xor_b32 s87,s64,65536
ds_read_b128 v[68:71],v2 offset:15360
buffer_load_dword v9,s[36:39],s72 offen offset:0 sc1 lds
s_waitcnt vmcnt(10)
v_mfma_f32_16x16x32_bf16 v[28:31],a[68:71],v[40:43],v[28:31]
s_barrier
v_mfma_f32_16x16x32_bf16 v[24:27],a[72:75],v[44:47],v[24:27]
s_cmp_ge_u32 s66,s59
ds_read_b128 v[40:43],v2 offset:16384
ds_read_b32 v88,v8 offset:0
v_mfma_f32_16x16x32_bf16 v[28:31],a[76:79],v[44:47],v[28:31]
s_cselect_b32 s86,s49,s50
v_mul_f32 v72,v32,v32
v_mul_f32 v73,v33,v33
v_mfma_f32_16x16x32_bf16 v[24:27],a[80:83],v[48:51],v[24:27]
s_cselect_b32 s87,s87,s63
ds_read_b128 v[44:47],v2 offset:17408
v_mul_f32 v74,v34,v34
v_mfma_f32_16x16x32_bf16 v[28:31],a[84:87],v[48:51],v[28:31]
s_mov_b32 s62,s87
v_mul_f32 v75,v35,v35
v_fma_f32 v72,v98,v72,v97
v_mfma_f32_16x16x32_bf16 v[24:27],a[88:91],v[52:55],v[24:27]
s_mov_b32 m0,s62
ds_read_b128 v[48:51],v2 offset:18432
v_mul_f32 v72,v32,v72
v_mfma_f32_16x16x32_bf16 v[28:31],a[92:95],v[52:55],v[28:31]
s_waitcnt lgkmcnt(4)
v_exp_f32 v72,v72
v_mfma_f32_16x16x32_bf16 v[24:27],a[96:99],v[56:59],v[24:27]
s_nop 0
s_waitcnt lgkmcnt(2)
ds_read_b128 v[52:55],v2 offset:19456
v_fma_f32 v72,v72,v99,v99
v_mfma_f32_16x16x32_bf16 v[28:31],a[100:103],v[56:59],v[28:31]
s_barrier
v_mfma_f32_16x16x32_bf16 v[24:27],a[104:107],v[60:63],v[24:27]
ds_read_b128 v[56:59],v2 offset:20480
ds_read_b32 v13,v7 offset:0
v_mfma_f32_16x16x32_bf16 v[28:31],a[108:111],v[60:63],v[28:31]
v_rcp_f32 v72,v72
v_mfma_f32_16x16x32_bf16 v[24:27],a[112:115],v[64:67],v[24:27]
ds_read_b128 v[60:63],v2 offset:21504
v_add_f32_dpp v88,v88,v88 quad_perm:[1,0,3,2] row_mask:0xf bank_mask:0xf
v_mfma_f32_16x16x32_bf16 v[28:31],a[116:119],v[64:67],v[28:31]
s_nop 0
v_add_f32_dpp v88,v88,v88 quad_perm:[2,3,0,1] row_mask:0xf bank_mask:0xf
v_fma_f32 v73,v98,v73,v97
v_mfma_f32_16x16x32_bf16 v[24:27],a[120:123],v[68:71],v[24:27]
s_waitcnt lgkmcnt(1)
ds_read_b128 v[64:67],v2 offset:22528
v_mul_f32 v73,v33,v73
v_mfma_f32_16x16x32_bf16 v[28:31],a[124:127],v[68:71],v[28:31]
v_cmp_eq_u32 vcc,s60,v13
v_exp_f32 v73,v73
v_add_f32_dpp v88,v88,v88 row_half_mirror row_mask:0xf bank_mask:0xf
v_mfma_f32_16x16x32_bf16 v[24:27],a[128:131],v[40:43],v[24:27]
s_cmp_eq_u32 vcc_lo,-1
ds_write_b32 v10,v88 offset:0
s_cselect_b32 s67,s86,s59
v_mfma_f32_16x16x32_bf16 v[28:31],a[132:135],v[40:43],v[28:31]
s_xor_b32 s63,s63,65536
ds_read_b128 v[68:71],v2 offset:23552
buffer_load_dwordx4 v0,s[36:39],s67 offen offset:0 sc1 lds
v_mfma_f32_16x16x32_bf16 v[24:27],a[136:139],v[44:47],v[24:27]
s_add_u32 s53,s52,s55
ds_read_b128 v[40:43],v2 offset:24576
v_fma_f32 v73,v73,v99,v99
v_mfma_f32_16x16x32_bf16 v[28:31],a[140:143],v[44:47],v[28:31]
s_cmp_eq_u32 s53,s56
v_rcp_f32 v73,v73
v_mfma_f32_16x16x32_bf16 v[24:27],a[144:147],v[48:51],v[24:27]
s_cselect_b32 s53,s57,s53
ds_read_b128 v[44:47],v2 offset:25600
v_fma_f32 v74,v98,v74,v97
v_mfma_f32_16x16x32_bf16 v[28:31],a[148:151],v[48:51],v[28:31]
buffer_load_dwordx4 v0,s[36:39],s67 offen offset:1024 sc1 lds
v_mfma_f32_16x16x32_bf16 v[24:27],a[152:155],v[52:55],v[24:27]
s_cmp_eq_u32 s53,s58
ds_read_b128 v[48:51],v2 offset:26624
v_mul_f32 v74,v34,v74
v_mfma_f32_16x16x32_bf16 v[28:31],a[156:159],v[52:55],v[28:31]
s_waitcnt lgkmcnt(3)
v_exp_f32 v74,v74
v_mfma_f32_16x16x32_bf16 v[24:27],a[160:163],v[56:59],v[24:27]
s_cselect_b32 s53,s59,s53
ds_read_b128 v[52:55],v2 offset:27648
v_fma_f32 v74,v74,v99,v99
v_mfma_f32_16x16x32_bf16 v[28:31],a[164:167],v[56:59],v[28:31]
buffer_load_dwordx4 v0,s[36:39],s67 offen offset:2048 sc1 lds
v_mfma_f32_16x16x32_bf16 v[24:27],a[168:171],v[60:63],v[24:27]
s_cmp_ge_u32 s66,s59
ds_read_b128 v[56:59],v2 offset:28672
v_fma_f32 v75,v98,v75,v97
v_mfma_f32_16x16x32_bf16 v[28:31],a[172:175],v[60:63],v[28:31]
v_rcp_f32 v74,v74
v_mfma_f32_16x16x32_bf16 v[24:27],a[176:179],v[64:67],v[24:27]
ds_read_b128 v[60:63],v2 offset:29696
v_mul_f32 v75,v35,v75
v_mfma_f32_16x16x32_bf16 v[28:31],a[180:183],v[64:67],v[28:31]
buffer_load_dwordx4 v0,s[36:39],s67 offen offset:3072 sc1 lds
v_mfma_f32_16x16x32_bf16 v[24:27],a[184:187],v[68:71],v[24:27]
ds_read_b128 v[64:67],v2 offset:30720
v_mul_f32 v32,v32,v72
v_mfma_f32_16x16x32_bf16 v[28:31],a[188:191],v[68:71],v[28:31]
s_waitcnt lgkmcnt(3)
v_exp_f32 v75,v75
v_mfma_f32_16x16x32_bf16 v[24:27],a[192:195],v[40:43],v[24:27]
s_waitcnt vmcnt(9)
ds_read_b128 v[68:71],v2 offset:31744
v_fma_f32 v75,v75,v99,v99
v_mfma_f32_16x16x32_bf16 v[28:31],a[196:199],v[40:43],v[28:31]
s_barrier
v_mfma_f32_16x16x32_bf16 v[24:27],a[200:203],v[44:47],v[24:27]
v_fma_f32 v36,-v96,v20,v36
v_fma_f32 v37,-v96,v21,v37
v_fma_f32 v38,-v96,v22,v38
v_fma_f32 v39,-v96,v23,v39
v_mul_f32 v76,v36,v36
v_mul_f32 v77,v37,v37
v_mul_f32 v78,v38,v38
v_mul_f32 v79,v39,v39
v_fma_f32 v76,v98,v76,v97
v_fma_f32 v77,v98,v77,v97
v_fma_f32 v78,v98,v78,v97
v_fma_f32 v79,v98,v79,v97
v_mul_f32 v76,v36,v76
v_mul_f32 v77,v37,v77
v_mul_f32 v78,v38,v78
v_mul_f32 v79,v39,v79
v_exp_f32 v76,v76
v_exp_f32 v77,v77
v_exp_f32 v78,v78
v_exp_f32 v79,v79
v_mfma_f32_16x16x32_bf16 v[28:31],a[204:207],v[44:47],v[28:31]
ds_read_b128 v[40:43],v2 offset:32768
v_rcp_f32 v75,v75
v_fma_f32 v76,v76,v99,v99
v_fma_f32 v77,v77,v99,v99
v_fma_f32 v78,v78,v99,v99)"
R"(
v_fma_f32 v79,v79,v99,v99
v_rcp_f32 v76,v76
v_rcp_f32 v77,v77
v_rcp_f32 v78,v78
v_rcp_f32 v79,v79
v_mfma_f32_16x16x32_bf16 v[24:27],a[208:211],v[48:51],v[24:27]
v_mul_f32 v33,v33,v73
v_mul_f32 v34,v34,v74
v_mul_f32 v35,v35,v75
v_cvt_pk_bf16_f32 v80,v32,v33
v_cvt_pk_bf16_f32 v81,v34,v35
v_mul_f32 v36,v36,v76
v_mul_f32 v37,v37,v77
v_mul_f32 v38,v38,v78
v_mul_f32 v39,v39,v79
v_cvt_pk_bf16_f32 v82,v36,v37
v_cvt_pk_bf16_f32 v83,v38,v39
v_mfma_f32_16x16x32_bf16 v[28:31],a[212:215],v[48:51],v[28:31]
ds_read_b128 v[44:47],v2 offset:33792
buffer_store_dwordx4 v[80:83],v3,s[36:39],s73 offen offset:0 sc0 sc1
ds_read_b64 v[92:93],v11 offset:0
v_mfma_f32_16x16x32_bf16 v[24:27],a[216:219],v[52:55],v[24:27]
ds_read_b128 v[48:51],v2 offset:34816
v_mfma_f32_16x16x32_bf16 v[28:31],a[220:223],v[52:55],v[28:31]
s_waitcnt lgkmcnt(4)
v_mfma_f32_16x16x32_bf16 v[24:27],a[224:227],v[56:59],v[24:27]
ds_read_b128 v[52:55],v2 offset:35840
v_mfma_f32_16x16x32_bf16 v[28:31],a[228:231],v[56:59],v[28:31]
s_waitcnt lgkmcnt(2)
v_mfma_f32_16x16x32_bf16 v[24:27],a[232:235],v[60:63],v[24:27]
ds_read_b128 v[56:59],v2 offset:36864
v_mfma_f32_16x16x32_bf16 v[28:31],a[236:239],v[60:63],v[28:31]
v_mfma_f32_16x16x32_bf16 v[24:27],a[240:243],v[64:67],v[24:27]
ds_read_b128 v[60:63],v2 offset:37888
v_mfma_f32_16x16x32_bf16 v[28:31],a[244:247],v[64:67],v[28:31]
v_mfma_f32_16x16x32_bf16 v[24:27],a[248:251],v[68:71],v[24:27]
s_waitcnt lgkmcnt(2)
ds_read_b128 v[64:67],v2 offset:38912
v_mfma_f32_16x16x32_bf16 v[28:31],a[252:255],v[68:71],v[28:31]
s_cbranch_scc1 Lg3o_slow1_%=
Lg3o_d1w1_%=:
v_mfma_f32_16x16x32_bf16 v[32:35],a[0:3],v[40:43],0
s_add_u32 m0,s62,16384
ds_read_b128 v[68:71],v2 offset:39936
v_mul_f32 v96,s75,v92
v_mfma_f32_16x16x32_bf16 v[36:39],a[4:7],v[40:43],0
s_mul_i32 s73,s48,7
v_fma_f32 v93,v93,s75,v12
v_fma_f32 v93,-v96,v96,v93
v_mfma_f32_16x16x32_bf16 v[32:35],a[8:11],v[44:47],v[32:35]
buffer_load_dwordx4 v1,s[36:39],s67 offen offset:0 sc1 lds
v_mfma_f32_16x16x32_bf16 v[36:39],a[12:15],v[44:47],v[36:39]
s_lshr_b32 s73,s73,1
ds_read_b128 v[40:43],v2 offset:40960
v_max_f32 v93,v93,v12
v_mfma_f32_16x16x32_bf16 v[32:35],a[16:19],v[48:51],v[32:35]
s_lshr_b32 s72,s67,5
ds_read_b128 v[44:47],v2 offset:41984
v_fma_f32 v24,-v96,v16,v24
v_mfma_f32_16x16x32_bf16 v[36:39],a[20:23],v[48:51],v[36:39]
s_lshr_b32 s71,s62,2
v_rsq_f32 v92,v93
v_mfma_f32_16x16x32_bf16 v[32:35],a[24:27],v[52:55],v[32:35]
buffer_load_dwordx4 v1,s[36:39],s67 offen offset:1024 sc1 lds
v_mfma_f32_16x16x32_bf16 v[36:39],a[28:31],v[52:55],v[36:39]
s_waitcnt lgkmcnt(2)
ds_read_b128 v[48:51],v2 offset:43008
v_mul_f32 v97,s76,v92
v_mfma_f32_16x16x32_bf16 v[32:35],a[32:35],v[56:59],v[32:35]
s_mov_b64 s[44:45],s[46:47]
ds_read_b128 v[52:55],v2 offset:44032
v_mul_f32 v98,v92,v92
v_mfma_f32_16x16x32_bf16 v[36:39],a[36:39],v[56:59],v[36:39]
s_mov_b64 s[46:47],s[48:49]
v_mul_f32 v99,v93,v92
v_mul_f32 v98,s77,v98
v_mfma_f32_16x16x32_bf16 v[32:35],a[40:43],v[60:63],v[32:35]
buffer_load_dwordx4 v1,s[36:39],s67 offen offset:2048 sc1 lds
v_mfma_f32_16x16x32_bf16 v[36:39],a[44:47],v[60:63],v[36:39]
s_mov_b64 s[48:49],s[50:51]
ds_read_b128 v[56:59],v2 offset:45056
v_mul_f32 v98,v98,v97
v_mfma_f32_16x16x32_bf16 v[32:35],a[48:51],v[64:67],v[32:35]
ds_read_b128 v[60:63],v2 offset:46080
v_fma_f32 v25,-v96,v17,v25
v_mfma_f32_16x16x32_bf16 v[36:39],a[52:55],v[64:67],v[36:39]
v_fma_f32 v26,-v96,v18,v26
v_fma_f32 v27,-v96,v19,v27
v_mfma_f32_16x16x32_bf16 v[32:35],a[56:59],v[68:71],v[32:35]
buffer_load_dwordx4 v1,s[36:39],s67 offen offset:3072 sc1 lds
v_mfma_f32_16x16x32_bf16 v[36:39],a[60:63],v[68:71],v[36:39]
s_waitcnt lgkmcnt(2)
ds_read_b128 v[64:67],v2 offset:47104
s_add_u32 m0,s71,s88
v_mfma_f32_16x16x32_bf16 v[32:35],a[64:67],v[40:43],v[32:35]
s_xor_b32 s87,s63,65536
ds_read_b128 v[68:71],v2 offset:48128
buffer_load_dword v9,s[36:39],s72 offen offset:0 sc1 lds
s_waitcnt vmcnt(10)
v_mfma_f32_16x16x32_bf16 v[36:39],a[68:71],v[40:43],v[36:39]
s_barrier
v_mfma_f32_16x16x32_bf16 v[32:35],a[72:75],v[44:47],v[32:35]
s_cmp_ge_u32 s67,s59
ds_read_b128 v[40:43],v2 offset:49152
ds_read_b32 v88,v8 offset:8192
v_mfma_f32_16x16x32_bf16 v[36:39],a[76:79],v[44:47],v[36:39]
s_cselect_b32 s86,s50,s51
v_mul_f32 v72,v24,v24
v_mul_f32 v73,v25,v25
v_mfma_f32_16x16x32_bf16 v[32:35],a[80:83],v[48:51],v[32:35]
s_cselect_b32 s87,s87,s64
ds_read_b128 v[44:47],v2 offset:50176
v_mul_f32 v74,v26,v26
v_mfma_f32_16x16x32_bf16 v[36:39],a[84:87],v[48:51],v[36:39]
s_mov_b32 s62,s87
v_mul_f32 v75,v27,v27
v_fma_f32 v72,v98,v72,v97
v_mfma_f32_16x16x32_bf16 v[32:35],a[88:91],v[52:55],v[32:35]
s_mov_b32 m0,s62
ds_read_b128 v[48:51],v2 offset:51200
v_mul_f32 v72,v24,v72
v_mfma_f32_16x16x32_bf16 v[36:39],a[92:95],v[52:55],v[36:39]
s_waitcnt lgkmcnt(4)
v_exp_f32 v72,v72
v_mfma_f32_16x16x32_bf16 v[32:35],a[96:99],v[56:59],v[32:35]
s_nop 0
s_waitcnt lgkmcnt(2)
ds_read_b128 v[52:55],v2 offset:52224
v_fma_f32 v72,v72,v99,v99
v_mfma_f32_16x16x32_bf16 v[36:39],a[100:103],v[56:59],v[36:39]
s_barrier
v_mfma_f32_16x16x32_bf16 v[32:35],a[104:107],v[60:63],v[32:35]
s_mov_b64 s[50:51],s[52:53]
ds_read_b128 v[56:59],v2 offset:53248
ds_read_b32 v13,v7 offset:256
v_mfma_f32_16x16x32_bf16 v[36:39],a[108:111],v[60:63],v[36:39]
v_rcp_f32 v72,v72
v_mfma_f32_16x16x32_bf16 v[32:35],a[112:115],v[64:67],v[32:35]
ds_read_b128 v[60:63],v2 offset:54272
v_add_f32_dpp v88,v88,v88 quad_perm:[1,0,3,2] row_mask:0xf bank_mask:0xf
v_mfma_f32_16x16x32_bf16 v[36:39],a[116:119],v[64:67],v[36:39]
s_nop 0
v_add_f32_dpp v88,v88,v88 quad_perm:[2,3,0,1] row_mask:0xf bank_mask:0xf
v_fma_f32 v73,v98,v73,v97
v_mfma_f32_16x16x32_bf16 v[32:35],a[120:123],v[68:71],v[32:35]
s_waitcnt lgkmcnt(1)
ds_read_b128 v[64:67],v2 offset:55296
v_mul_f32 v73,v25,v73
v_mfma_f32_16x16x32_bf16 v[36:39],a[124:127],v[68:71],v[36:39]
v_cmp_eq_u32 vcc,s60,v13
v_exp_f32 v73,v73
v_add_f32_dpp v88,v88,v88 row_half_mirror row_mask:0xf bank_mask:0xf
v_mfma_f32_16x16x32_bf16 v[32:35],a[128:131],v[40:43],v[32:35]
s_cmp_eq_u32 vcc_lo,-1
ds_write_b32 v10,v88 offset:128
s_cselect_b32 s66,s86,s59
v_mfma_f32_16x16x32_bf16 v[36:39],a[132:135],v[40:43],v[36:39]
s_xor_b32 s64,s64,65536
ds_read_b128 v[68:71],v2 offset:56320
buffer_load_dwordx4 v0,s[36:39],s66 offen offset:0 sc1 lds
v_mfma_f32_16x16x32_bf16 v[32:35],a[136:139],v[44:47],v[32:35]
s_add_u32 s74,s53,s55
ds_read_b128 v[40:43],v2 offset:57344
v_fma_f32 v73,v73,v99,v99
v_mfma_f32_16x16x32_bf16 v[36:39],a[140:143],v[44:47],v[36:39]
s_cmp_eq_u32 s74,s56
v_rcp_f32 v73,v73
v_mfma_f32_16x16x32_bf16 v[32:35],a[144:147],v[48:51],v[32:35]
s_cselect_b32 s74,s57,s74
ds_read_b128 v[44:47],v2 offset:58368
v_fma_f32 v74,v98,v74,v97
v_mfma_f32_16x16x32_bf16 v[36:39],a[148:151],v[48:51],v[36:39]
buffer_load_dwordx4 v0,s[36:39],s66 offen offset:1024 sc1 lds
v_mfma_f32_16x16x32_bf16 v[32:35],a[152:155],v[52:55],v[32:35]
s_cmp_eq_u32 s74,s58
ds_read_b128 v[48:51],v2 offset:59392
v_mul_f32 v74,v26,v74
v_mfma_f32_16x16x32_bf16 v[36:39],a[156:159],v[52:55],v[36:39]
s_waitcnt lgkmcnt(3)
v_exp_f32 v74,v74
v_mfma_f32_16x16x32_bf16 v[32:35],a[160:163],v[56:59],v[32:35]
s_cselect_b32 s74,s59,s74
ds_read_b128 v[52:55],v2 offset:60416
v_fma_f32 v74,v74,v99,v99
v_mfma_f32_16x16x32_bf16 v[36:39],a[164:167],v[56:59],v[36:39]
buffer_load_dwordx4 v0,s[36:39],s66 offen offset:2048 sc1 lds
v_mfma_f32_16x16x32_bf16 v[32:35],a[168:171],v[60:63],v[32:35]
s_mov_b32 s52,s74
ds_read_b128 v[56:59],v2 offset:61440
v_fma_f32 v75,v98,v75,v97
v_mfma_f32_16x16x32_bf16 v[36:39],a[172:175],v[60:63],v[36:39]
s_cmp_lt_u32 s67,s59
v_rcp_f32 v74,v74
v_mfma_f32_16x16x32_bf16 v[32:35],a[176:179],v[64:67],v[32:35]
ds_read_b128 v[60:63],v2 offset:62464
v_mul_f32 v75,v27,v75
v_mfma_f32_16x16x32_bf16 v[36:39],a[180:183],v[64:67],v[36:39]
buffer_load_dwordx4 v0,s[36:39],s66 offen offset:3072 sc1 lds
v_mfma_f32_16x16x32_bf16 v[32:35],a[184:187],v[68:71],v[32:35]
ds_read_b128 v[64:67],v2 offset:63488)"
R"(
v_mul_f32 v24,v24,v72
v_mfma_f32_16x16x32_bf16 v[36:39],a[188:191],v[68:71],v[36:39]
s_waitcnt lgkmcnt(3)
v_exp_f32 v75,v75
v_mfma_f32_16x16x32_bf16 v[32:35],a[192:195],v[40:43],v[32:35]
s_waitcnt vmcnt(9)
ds_read_b128 v[68:71],v2 offset:64512
v_xor_b32 v2,65536,v2
v_mfma_f32_16x16x32_bf16 v[36:39],a[196:199],v[40:43],v[36:39]
s_barrier
v_mfma_f32_16x16x32_bf16 v[32:35],a[200:203],v[44:47],v[32:35]
v_fma_f32 v28,-v96,v20,v28
v_fma_f32 v29,-v96,v21,v29
v_fma_f32 v30,-v96,v22,v30
v_fma_f32 v31,-v96,v23,v31
v_mul_f32 v76,v28,v28
v_mul_f32 v77,v29,v29
v_mul_f32 v78,v30,v30
v_mul_f32 v79,v31,v31
v_fma_f32 v76,v98,v76,v97
v_fma_f32 v77,v98,v77,v97
v_fma_f32 v78,v98,v78,v97
v_fma_f32 v79,v98,v79,v97
v_mul_f32 v76,v28,v76
v_mul_f32 v77,v29,v77
v_mul_f32 v78,v30,v78
v_mul_f32 v79,v31,v79
v_exp_f32 v76,v76
v_exp_f32 v77,v77
v_exp_f32 v78,v78
v_exp_f32 v79,v79
v_mfma_f32_16x16x32_bf16 v[36:39],a[204:207],v[44:47],v[36:39]
ds_read_b128 v[40:43],v2 offset:0
v_fma_f32 v75,v75,v99,v99
v_rcp_f32 v75,v75
v_fma_f32 v76,v76,v99,v99
v_fma_f32 v77,v77,v99,v99
v_fma_f32 v78,v78,v99,v99
v_fma_f32 v79,v79,v99,v99
v_rcp_f32 v76,v76
v_rcp_f32 v77,v77
v_rcp_f32 v78,v78
v_rcp_f32 v79,v79
v_mfma_f32_16x16x32_bf16 v[32:35],a[208:211],v[48:51],v[32:35]
v_mul_f32 v25,v25,v73
v_mul_f32 v26,v26,v74
v_mul_f32 v27,v27,v75
v_cvt_pk_bf16_f32 v80,v24,v25
v_cvt_pk_bf16_f32 v81,v26,v27
v_mul_f32 v28,v28,v76
v_mul_f32 v29,v29,v77
v_mul_f32 v30,v30,v78
v_mul_f32 v31,v31,v79
v_cvt_pk_bf16_f32 v82,v28,v29
v_cvt_pk_bf16_f32 v83,v30,v31
v_mfma_f32_16x16x32_bf16 v[36:39],a[212:215],v[48:51],v[36:39]
ds_read_b128 v[44:47],v2 offset:1024
buffer_store_dwordx4 v[80:83],v3,s[36:39],s73 offen offset:0 sc0 sc1
ds_read_b64 v[92:93],v11 offset:128
v_xor_b32 v8,16384,v8
v_mfma_f32_16x16x32_bf16 v[32:35],a[216:219],v[52:55],v[32:35]
ds_read_b128 v[48:51],v2 offset:2048
v_mfma_f32_16x16x32_bf16 v[36:39],a[220:223],v[52:55],v[36:39]
s_waitcnt lgkmcnt(4)
v_mfma_f32_16x16x32_bf16 v[32:35],a[224:227],v[56:59],v[32:35]
ds_read_b128 v[52:55],v2 offset:3072
v_mfma_f32_16x16x32_bf16 v[36:39],a[228:231],v[56:59],v[36:39]
s_waitcnt lgkmcnt(2)
v_mfma_f32_16x16x32_bf16 v[32:35],a[232:235],v[60:63],v[32:35]
ds_read_b128 v[56:59],v2 offset:4096
v_mfma_f32_16x16x32_bf16 v[36:39],a[236:239],v[60:63],v[36:39]
v_mfma_f32_16x16x32_bf16 v[32:35],a[240:243],v[64:67],v[32:35]
ds_read_b128 v[60:63],v2 offset:5120
v_mfma_f32_16x16x32_bf16 v[36:39],a[244:247],v[64:67],v[36:39]
v_mfma_f32_16x16x32_bf16 v[32:35],a[248:251],v[68:71],v[32:35]
s_waitcnt lgkmcnt(2)
ds_read_b128 v[64:67],v2 offset:6144
v_mfma_f32_16x16x32_bf16 v[36:39],a[252:255],v[68:71],v[36:39]
s_cbranch_scc1 Lg3o_d0w1_%=
s_branch Lg3o_slow0_%=
Lg3o_d0w2_%=:
v_mfma_f32_16x16x32_bf16 v[24:27],a[0:3],v[40:43],0
s_add_u32 m0,s62,16384
ds_read_b128 v[68:71],v2 offset:7168
v_mul_f32 v96,s75,v92
v_mfma_f32_16x16x32_bf16 v[28:31],a[4:7],v[40:43],0
s_mul_i32 s73,s47,7
v_fma_f32 v93,v93,s75,v12
v_fma_f32 v93,-v96,v96,v93
v_mfma_f32_16x16x32_bf16 v[24:27],a[8:11],v[44:47],v[24:27]
s_lshr_b32 s73,s73,1
ds_read_b128 v[40:43],v2 offset:8192
v_max_f32 v93,v93,v12
v_mfma_f32_16x16x32_bf16 v[28:31],a[12:15],v[44:47],v[28:31]
buffer_load_dwordx4 v1,s[36:39],s66 offen offset:0 sc1 lds
v_mfma_f32_16x16x32_bf16 v[24:27],a[16:19],v[48:51],v[24:27]
s_lshr_b32 s72,s66,5
ds_read_b128 v[44:47],v2 offset:9216
v_fma_f32 v32,-v96,v16,v32
v_mfma_f32_16x16x32_bf16 v[28:31],a[20:23],v[48:51],v[28:31]
s_lshr_b32 s71,s62,2
v_rsq_f32 v92,v93
v_mfma_f32_16x16x32_bf16 v[24:27],a[24:27],v[52:55],v[24:27]
ds_read_b128 v[48:51],v2 offset:10240
v_mul_f32 v97,s76,v92
v_mfma_f32_16x16x32_bf16 v[28:31],a[28:31],v[52:55],v[28:31]
s_waitcnt lgkmcnt(3)
buffer_load_dwordx4 v1,s[36:39],s66 offen offset:1024 sc1 lds
v_mfma_f32_16x16x32_bf16 v[24:27],a[32:35],v[56:59],v[24:27]
ds_read_b128 v[52:55],v2 offset:11264
v_mul_f32 v98,v92,v92
v_mfma_f32_16x16x32_bf16 v[28:31],a[36:39],v[56:59],v[28:31]
v_mul_f32 v99,v93,v92
v_mul_f32 v98,s77,v98
v_mfma_f32_16x16x32_bf16 v[24:27],a[40:43],v[60:63],v[24:27]
ds_read_b128 v[56:59],v2 offset:12288
v_mul_f32 v98,v98,v97
v_mfma_f32_16x16x32_bf16 v[28:31],a[44:47],v[60:63],v[28:31]
buffer_load_dwordx4 v1,s[36:39],s66 offen offset:2048 sc1 lds
v_mfma_f32_16x16x32_bf16 v[24:27],a[48:51],v[64:67],v[24:27]
ds_read_b128 v[60:63],v2 offset:13312
v_fma_f32 v33,-v96,v17,v33
v_mfma_f32_16x16x32_bf16 v[28:31],a[52:55],v[64:67],v[28:31]
v_fma_f32 v34,-v96,v18,v34
v_fma_f32 v35,-v96,v19,v35
v_mfma_f32_16x16x32_bf16 v[24:27],a[56:59],v[68:71],v[24:27]
ds_read_b128 v[64:67],v2 offset:14336
v_mul_f32 v72,v32,v32
v_mfma_f32_16x16x32_bf16 v[28:31],a[60:63],v[68:71],v[28:31]
s_waitcnt lgkmcnt(3)
buffer_load_dwordx4 v1,s[36:39],s66 offen offset:3072 sc1 lds
s_add_u32 m0,s71,s88
v_mfma_f32_16x16x32_bf16 v[24:27],a[64:67],v[40:43],v[24:27]
s_xor_b32 s87,s64,65536
ds_read_b128 v[68:71],v2 offset:15360
buffer_load_dword v9,s[36:39],s72 offen offset:0 sc1 lds
s_waitcnt vmcnt(10)
v_mfma_f32_16x16x32_bf16 v[28:31],a[68:71],v[40:43],v[28:31]
s_barrier
v_mfma_f32_16x16x32_bf16 v[24:27],a[72:75],v[44:47],v[24:27]
s_cmp_ge_u32 s66,s59
ds_read_b128 v[40:43],v2 offset:16384
ds_read_b32 v88,v8 offset:0
v_mfma_f32_16x16x32_bf16 v[28:31],a[76:79],v[44:47],v[28:31]
s_cselect_b32 s86,s49,s50
v_mul_f32 v73,v33,v33
v_mul_f32 v74,v34,v34
v_mfma_f32_16x16x32_bf16 v[24:27],a[80:83],v[48:51],v[24:27]
s_cselect_b32 s87,s87,s63
ds_read_b128 v[44:47],v2 offset:17408
v_mul_f32 v75,v35,v35
v_mfma_f32_16x16x32_bf16 v[28:31],a[84:87],v[48:51],v[28:31]
s_mov_b32 s62,s87
v_fma_f32 v72,v98,v72,v97
v_mul_f32 v72,v32,v72
v_mfma_f32_16x16x32_bf16 v[24:27],a[88:91],v[52:55],v[24:27]
s_mov_b32 m0,s62
ds_read_b128 v[48:51],v2 offset:18432
v_fma_f32 v73,v98,v73,v97
v_mfma_f32_16x16x32_bf16 v[28:31],a[92:95],v[52:55],v[28:31]
s_waitcnt lgkmcnt(4)
v_exp_f32 v72,v72
v_mfma_f32_16x16x32_bf16 v[24:27],a[96:99],v[56:59],v[24:27]
s_nop 0
s_waitcnt lgkmcnt(2)
ds_read_b128 v[52:55],v2 offset:19456
v_mul_f32 v73,v33,v73
v_mfma_f32_16x16x32_bf16 v[28:31],a[100:103],v[56:59],v[28:31]
s_barrier
v_mfma_f32_16x16x32_bf16 v[24:27],a[104:107],v[60:63],v[24:27]
ds_read_b128 v[56:59],v2 offset:20480
ds_read_b32 v13,v7 offset:0
v_mfma_f32_16x16x32_bf16 v[28:31],a[108:111],v[60:63],v[28:31]
v_exp_f32 v73,v73
v_mfma_f32_16x16x32_bf16 v[24:27],a[112:115],v[64:67],v[24:27]
ds_read_b128 v[60:63],v2 offset:21504
v_fma_f32 v72,v72,v99,v99
v_mfma_f32_16x16x32_bf16 v[28:31],a[116:119],v[64:67],v[28:31]
v_rcp_f32 v72,v72
v_mfma_f32_16x16x32_bf16 v[24:27],a[120:123],v[68:71],v[24:27]
s_waitcnt lgkmcnt(1)
ds_read_b128 v[64:67],v2 offset:22528
v_fma_f32 v73,v73,v99,v99
v_mfma_f32_16x16x32_bf16 v[28:31],a[124:127],v[68:71],v[28:31]
v_cmp_eq_u32 vcc,s60,v13
v_rcp_f32 v73,v73
v_add_f32_dpp v88,v88,v88 quad_perm:[1,0,3,2] row_mask:0xf bank_mask:0xf
v_mfma_f32_16x16x32_bf16 v[24:27],a[128:131],v[40:43],v[24:27]
s_cmp_eq_u32 vcc_lo,-1
ds_read_b128 v[68:71],v2 offset:23552
v_add_f32_dpp v88,v88,v88 quad_perm:[2,3,0,1] row_mask:0xf bank_mask:0xf
v_mfma_f32_16x16x32_bf16 v[28:31],a[132:135],v[40:43],v[28:31]
s_cselect_b32 s67,s86,s59
v_add_f32_dpp v88,v88,v88 row_half_mirror row_mask:0xf bank_mask:0xf
v_fma_f32 v74,v98,v74,v97
v_mfma_f32_16x16x32_bf16 v[24:27],a[136:139],v[44:47],v[24:27]
buffer_load_dwordx4 v0,s[36:39],s67 offen offset:0 sc1 lds
v_mfma_f32_16x16x32_bf16 v[28:31],a[140:143],v[44:47],v[28:31]
s_xor_b32 s63,s63,65536
ds_read_b128 v[40:43],v2 offset:24576)"
R"(
v_mul_f32 v74,v34,v74
v_mfma_f32_16x16x32_bf16 v[24:27],a[144:147],v[48:51],v[24:27]
s_add_u32 s53,s52,s55
ds_write_b32 v10,v88 offset:0
v_mfma_f32_16x16x32_bf16 v[28:31],a[148:151],v[48:51],v[28:31]
s_cmp_eq_u32 s53,s56
ds_read_b128 v[44:47],v2 offset:25600
v_fma_f32 v75,v98,v75,v97
v_mfma_f32_16x16x32_bf16 v[24:27],a[152:155],v[52:55],v[24:27]
buffer_load_dwordx4 v0,s[36:39],s67 offen offset:1024 sc1 lds
v_mfma_f32_16x16x32_bf16 v[28:31],a[156:159],v[52:55],v[28:31]
s_waitcnt lgkmcnt(3)
ds_read_b128 v[48:51],v2 offset:26624
v_mul_f32 v75,v35,v75
v_mfma_f32_16x16x32_bf16 v[24:27],a[160:163],v[56:59],v[24:27]
s_cselect_b32 s53,s57,s53
ds_read_b128 v[52:55],v2 offset:27648
v_mul_f32 v32,v32,v72
v_mfma_f32_16x16x32_bf16 v[28:31],a[164:167],v[56:59],v[28:31]
s_cmp_eq_u32 s53,s58
v_exp_f32 v74,v74
v_mfma_f32_16x16x32_bf16 v[24:27],a[168:171],v[60:63],v[24:27]
buffer_load_dwordx4 v0,s[36:39],s67 offen offset:2048 sc1 lds
v_mfma_f32_16x16x32_bf16 v[28:31],a[172:175],v[60:63],v[28:31]
s_cselect_b32 s53,s59,s53
ds_read_b128 v[56:59],v2 offset:28672
v_fma_f32 v74,v74,v99,v99
v_mfma_f32_16x16x32_bf16 v[24:27],a[176:179],v[64:67],v[24:27]
s_cmp_ge_u32 s66,s59
ds_read_b128 v[60:63],v2 offset:29696
v_mul_f32 v33,v33,v73
v_mfma_f32_16x16x32_bf16 v[28:31],a[180:183],v[64:67],v[28:31]
v_exp_f32 v75,v75
v_mfma_f32_16x16x32_bf16 v[24:27],a[184:187],v[68:71],v[24:27]
buffer_load_dwordx4 v0,s[36:39],s67 offen offset:3072 sc1 lds
v_mfma_f32_16x16x32_bf16 v[28:31],a[188:191],v[68:71],v[28:31]
s_waitcnt lgkmcnt(2)
ds_read_b128 v[64:67],v2 offset:30720
v_fma_f32 v75,v75,v99,v99
v_mfma_f32_16x16x32_bf16 v[24:27],a[192:195],v[40:43],v[24:27]
s_waitcnt vmcnt(9)
ds_read_b128 v[68:71],v2 offset:31744
v_cvt_pk_bf16_f32 v80,v32,v33
v_mfma_f32_16x16x32_bf16 v[28:31],a[196:199],v[40:43],v[28:31]
s_barrier
v_mfma_f32_16x16x32_bf16 v[24:27],a[200:203],v[44:47],v[24:27]
ds_read_b128 v[40:43],v2 offset:32768
ds_read_b64 v[92:93],v11 offset:0
v_mfma_f32_16x16x32_bf16 v[28:31],a[204:207],v[44:47],v[28:31]
v_fma_f32 v36,-v96,v20,v36
v_fma_f32 v37,-v96,v21,v37
v_fma_f32 v38,-v96,v22,v38
v_fma_f32 v39,-v96,v23,v39
v_mul_f32 v76,v36,v36
v_mul_f32 v77,v37,v37
v_mul_f32 v78,v38,v38
v_mul_f32 v79,v39,v39
v_fma_f32 v76,v98,v76,v97
v_fma_f32 v77,v98,v77,v97
v_fma_f32 v78,v98,v78,v97
v_fma_f32 v79,v98,v79,v97
v_mul_f32 v76,v36,v76
v_mul_f32 v77,v37,v77
v_mul_f32 v78,v38,v78
v_mul_f32 v79,v39,v79
v_exp_f32 v76,v76
v_exp_f32 v77,v77
v_exp_f32 v78,v78
v_exp_f32 v79,v79
v_mfma_f32_16x16x32_bf16 v[24:27],a[208:211],v[48:51],v[24:27]
v_rcp_f32 v74,v74
v_rcp_f32 v75,v75
v_fma_f32 v76,v76,v99,v99
v_fma_f32 v77,v77,v99,v99
v_fma_f32 v78,v78,v99,v99
v_fma_f32 v79,v79,v99,v99
v_rcp_f32 v76,v76
v_rcp_f32 v77,v77
v_rcp_f32 v78,v78
v_rcp_f32 v79,v79
v_mfma_f32_16x16x32_bf16 v[28:31],a[212:215],v[48:51],v[28:31]
ds_read_b128 v[44:47],v2 offset:33792
v_mul_f32 v34,v34,v74
v_mul_f32 v35,v35,v75
v_cvt_pk_bf16_f32 v81,v34,v35
v_mul_f32 v36,v36,v76
v_mul_f32 v37,v37,v77
v_mul_f32 v38,v38,v78
v_mul_f32 v39,v39,v79
v_cvt_pk_bf16_f32 v82,v36,v37
v_cvt_pk_bf16_f32 v83,v38,v39
v_mfma_f32_16x16x32_bf16 v[24:27],a[216:219],v[52:55],v[24:27]
s_waitcnt lgkmcnt(1)
buffer_store_dwordx4 v[80:83],v3,s[36:39],s73 offen offset:0 sc0 sc1
ds_read_b128 v[48:51],v2 offset:34816
v_mfma_f32_16x16x32_bf16 v[28:31],a[220:223],v[52:55],v[28:31]
v_mfma_f32_16x16x32_bf16 v[24:27],a[224:227],v[56:59],v[24:27]
ds_read_b128 v[52:55],v2 offset:35840
v_mfma_f32_16x16x32_bf16 v[28:31],a[228:231],v[56:59],v[28:31]
v_mfma_f32_16x16x32_bf16 v[24:27],a[232:235],v[60:63],v[24:27]
ds_read_b128 v[56:59],v2 offset:36864
v_mfma_f32_16x16x32_bf16 v[28:31],a[236:239],v[60:63],v[28:31]
v_mfma_f32_16x16x32_bf16 v[24:27],a[240:243],v[64:67],v[24:27]
ds_read_b128 v[60:63],v2 offset:37888
v_mfma_f32_16x16x32_bf16 v[28:31],a[244:247],v[64:67],v[28:31]
v_mfma_f32_16x16x32_bf16 v[24:27],a[248:251],v[68:71],v[24:27]
s_waitcnt lgkmcnt(2)
ds_read_b128 v[64:67],v2 offset:38912
v_mfma_f32_16x16x32_bf16 v[28:31],a[252:255],v[68:71],v[28:31]
s_cbranch_scc1 Lg3o_slow1_%=
Lg3o_d1w2_%=:
v_mfma_f32_16x16x32_bf16 v[32:35],a[0:3],v[40:43],0
s_add_u32 m0,s62,16384
ds_read_b128 v[68:71],v2 offset:39936
v_mul_f32 v96,s75,v92
v_mfma_f32_16x16x32_bf16 v[36:39],a[4:7],v[40:43],0
s_mul_i32 s73,s48,7
v_fma_f32 v93,v93,s75,v12
v_fma_f32 v93,-v96,v96,v93
v_mfma_f32_16x16x32_bf16 v[32:35],a[8:11],v[44:47],v[32:35]
s_lshr_b32 s73,s73,1
ds_read_b128 v[40:43],v2 offset:40960
v_max_f32 v93,v93,v12
v_mfma_f32_16x16x32_bf16 v[36:39],a[12:15],v[44:47],v[36:39]
buffer_load_dwordx4 v1,s[36:39],s67 offen offset:0 sc1 lds
v_mfma_f32_16x16x32_bf16 v[32:35],a[16:19],v[48:51],v[32:35]
s_lshr_b32 s72,s67,5
ds_read_b128 v[44:47],v2 offset:41984
v_fma_f32 v24,-v96,v16,v24
v_mfma_f32_16x16x32_bf16 v[36:39],a[20:23],v[48:51],v[36:39]
s_lshr_b32 s71,s62,2
v_rsq_f32 v92,v93
v_mfma_f32_16x16x32_bf16 v[32:35],a[24:27],v[52:55],v[32:35]
s_mov_b64 s[44:45],s[46:47]
ds_read_b128 v[48:51],v2 offset:43008
v_mul_f32 v97,s76,v92
v_mfma_f32_16x16x32_bf16 v[36:39],a[28:31],v[52:55],v[36:39]
s_waitcnt lgkmcnt(3)
buffer_load_dwordx4 v1,s[36:39],s67 offen offset:1024 sc1 lds
v_mfma_f32_16x16x32_bf16 v[32:35],a[32:35],v[56:59],v[32:35]
s_mov_b64 s[46:47],s[48:49]
ds_read_b128 v[52:55],v2 offset:44032
v_mul_f32 v98,v92,v92
v_mfma_f32_16x16x32_bf16 v[36:39],a[36:39],v[56:59],v[36:39]
s_mov_b64 s[48:49],s[50:51]
v_mul_f32 v99,v93,v92
v_mul_f32 v98,s77,v98
v_mfma_f32_16x16x32_bf16 v[32:35],a[40:43],v[60:63],v[32:35]
ds_read_b128 v[56:59],v2 offset:45056
v_mul_f32 v98,v98,v97
v_mfma_f32_16x16x32_bf16 v[36:39],a[44:47],v[60:63],v[36:39]
buffer_load_dwordx4 v1,s[36:39],s67 offen offset:2048 sc1 lds
v_mfma_f32_16x16x32_bf16 v[32:35],a[48:51],v[64:67],v[32:35]
ds_read_b128 v[60:63],v2 offset:46080
v_fma_f32 v25,-v96,v17,v25
v_mfma_f32_16x16x32_bf16 v[36:39],a[52:55],v[64:67],v[36:39]
v_fma_f32 v26,-v96,v18,v26
v_fma_f32 v27,-v96,v19,v27
v_mfma_f32_16x16x32_bf16 v[32:35],a[56:59],v[68:71],v[32:35]
ds_read_b128 v[64:67],v2 offset:47104
v_mul_f32 v72,v24,v24
v_mfma_f32_16x16x32_bf16 v[36:39],a[60:63],v[68:71],v[36:39]
s_waitcnt lgkmcnt(3)
buffer_load_dwordx4 v1,s[36:39],s67 offen offset:3072 sc1 lds
s_add_u32 m0,s71,s88
v_mfma_f32_16x16x32_bf16 v[32:35],a[64:67],v[40:43],v[32:35]
s_xor_b32 s87,s63,65536
ds_read_b128 v[68:71],v2 offset:48128
buffer_load_dword v9,s[36:39],s72 offen offset:0 sc1 lds
s_waitcnt vmcnt(10)
v_mfma_f32_16x16x32_bf16 v[36:39],a[68:71],v[40:43],v[36:39]
s_barrier
v_mfma_f32_16x16x32_bf16 v[32:35],a[72:75],v[44:47],v[32:35]
s_cmp_ge_u32 s67,s59
ds_read_b128 v[40:43],v2 offset:49152
ds_read_b32 v88,v8 offset:8192
v_mfma_f32_16x16x32_bf16 v[36:39],a[76:79],v[44:47],v[36:39]
s_cselect_b32 s86,s50,s51
v_mul_f32 v73,v25,v25
v_mul_f32 v74,v26,v26
v_mfma_f32_16x16x32_bf16 v[32:35],a[80:83],v[48:51],v[32:35]
s_cselect_b32 s87,s87,s64
ds_read_b128 v[44:47],v2 offset:50176
v_mul_f32 v75,v27,v27
v_mfma_f32_16x16x32_bf16 v[36:39],a[84:87],v[48:51],v[36:39]
s_mov_b32 s62,s87
v_fma_f32 v72,v98,v72,v97
v_mul_f32 v72,v24,v72
v_mfma_f32_16x16x32_bf16 v[32:35],a[88:91],v[52:55],v[32:35]
s_mov_b32 m0,s62
ds_read_b128 v[48:51],v2 offset:51200
v_fma_f32 v73,v98,v73,v97
v_mfma_f32_16x16x32_bf16 v[36:39],a[92:95],v[52:55],v[36:39]
s_waitcnt lgkmcnt(4)
v_exp_f32 v72,v72
v_mfma_f32_16x16x32_bf16 v[32:35],a[96:99],v[56:59],v[32:35]
s_nop 0
s_waitcnt lgkmcnt(2)
ds_read_b128 v[52:55],v2 offset:52224
v_mul_f32 v73,v25,v73)"
R"(
v_mfma_f32_16x16x32_bf16 v[36:39],a[100:103],v[56:59],v[36:39]
s_barrier
v_mfma_f32_16x16x32_bf16 v[32:35],a[104:107],v[60:63],v[32:35]
s_mov_b64 s[50:51],s[52:53]
ds_read_b128 v[56:59],v2 offset:53248
ds_read_b32 v13,v7 offset:256
v_mfma_f32_16x16x32_bf16 v[36:39],a[108:111],v[60:63],v[36:39]
v_exp_f32 v73,v73
v_mfma_f32_16x16x32_bf16 v[32:35],a[112:115],v[64:67],v[32:35]
ds_read_b128 v[60:63],v2 offset:54272
v_fma_f32 v72,v72,v99,v99
v_mfma_f32_16x16x32_bf16 v[36:39],a[116:119],v[64:67],v[36:39]
v_rcp_f32 v72,v72
v_mfma_f32_16x16x32_bf16 v[32:35],a[120:123],v[68:71],v[32:35]
s_waitcnt lgkmcnt(1)
ds_read_b128 v[64:67],v2 offset:55296
v_fma_f32 v73,v73,v99,v99
v_mfma_f32_16x16x32_bf16 v[36:39],a[124:127],v[68:71],v[36:39]
v_cmp_eq_u32 vcc,s60,v13
v_rcp_f32 v73,v73
v_add_f32_dpp v88,v88,v88 quad_perm:[1,0,3,2] row_mask:0xf bank_mask:0xf
v_mfma_f32_16x16x32_bf16 v[32:35],a[128:131],v[40:43],v[32:35]
s_cmp_eq_u32 vcc_lo,-1
ds_read_b128 v[68:71],v2 offset:56320
v_add_f32_dpp v88,v88,v88 quad_perm:[2,3,0,1] row_mask:0xf bank_mask:0xf
v_mfma_f32_16x16x32_bf16 v[36:39],a[132:135],v[40:43],v[36:39]
s_cselect_b32 s66,s86,s59
v_add_f32_dpp v88,v88,v88 row_half_mirror row_mask:0xf bank_mask:0xf
v_fma_f32 v74,v98,v74,v97
v_mfma_f32_16x16x32_bf16 v[32:35],a[136:139],v[44:47],v[32:35]
buffer_load_dwordx4 v0,s[36:39],s66 offen offset:0 sc1 lds
v_mfma_f32_16x16x32_bf16 v[36:39],a[140:143],v[44:47],v[36:39]
s_xor_b32 s64,s64,65536
ds_read_b128 v[40:43],v2 offset:57344
v_mul_f32 v74,v26,v74
v_mfma_f32_16x16x32_bf16 v[32:35],a[144:147],v[48:51],v[32:35]
s_add_u32 s74,s53,s55
ds_write_b32 v10,v88 offset:128
v_mfma_f32_16x16x32_bf16 v[36:39],a[148:151],v[48:51],v[36:39]
s_cmp_eq_u32 s74,s56
ds_read_b128 v[44:47],v2 offset:58368
v_fma_f32 v75,v98,v75,v97
v_mfma_f32_16x16x32_bf16 v[32:35],a[152:155],v[52:55],v[32:35]
buffer_load_dwordx4 v0,s[36:39],s66 offen offset:1024 sc1 lds
v_mfma_f32_16x16x32_bf16 v[36:39],a[156:159],v[52:55],v[36:39]
s_waitcnt lgkmcnt(3)
ds_read_b128 v[48:51],v2 offset:59392
v_mul_f32 v75,v27,v75
v_mfma_f32_16x16x32_bf16 v[32:35],a[160:163],v[56:59],v[32:35]
s_cselect_b32 s74,s57,s74
ds_read_b128 v[52:55],v2 offset:60416
v_mul_f32 v24,v24,v72
v_mfma_f32_16x16x32_bf16 v[36:39],a[164:167],v[56:59],v[36:39]
s_cmp_eq_u32 s74,s58
v_exp_f32 v74,v74
v_mfma_f32_16x16x32_bf16 v[32:35],a[168:171],v[60:63],v[32:35]
buffer_load_dwordx4 v0,s[36:39],s66 offen offset:2048 sc1 lds
v_mfma_f32_16x16x32_bf16 v[36:39],a[172:175],v[60:63],v[36:39]
s_cselect_b32 s74,s59,s74
ds_read_b128 v[56:59],v2 offset:61440
v_fma_f32 v74,v74,v99,v99
v_mfma_f32_16x16x32_bf16 v[32:35],a[176:179],v[64:67],v[32:35]
s_mov_b32 s52,s74
ds_read_b128 v[60:63],v2 offset:62464
v_mul_f32 v25,v25,v73
v_mfma_f32_16x16x32_bf16 v[36:39],a[180:183],v[64:67],v[36:39]
s_cmp_lt_u32 s67,s59
v_exp_f32 v75,v75
v_mfma_f32_16x16x32_bf16 v[32:35],a[184:187],v[68:71],v[32:35]
buffer_load_dwordx4 v0,s[36:39],s66 offen offset:3072 sc1 lds
v_mfma_f32_16x16x32_bf16 v[36:39],a[188:191],v[68:71],v[36:39]
s_waitcnt lgkmcnt(2)
ds_read_b128 v[64:67],v2 offset:63488
v_fma_f32 v75,v75,v99,v99
v_mfma_f32_16x16x32_bf16 v[32:35],a[192:195],v[40:43],v[32:35]
s_waitcnt vmcnt(9)
ds_read_b128 v[68:71],v2 offset:64512
v_xor_b32 v2,65536,v2
v_mfma_f32_16x16x32_bf16 v[36:39],a[196:199],v[40:43],v[36:39]
s_barrier
v_mfma_f32_16x16x32_bf16 v[32:35],a[200:203],v[44:47],v[32:35]
ds_read_b128 v[40:43],v2 offset:0
ds_read_b64 v[92:93],v11 offset:128
v_mfma_f32_16x16x32_bf16 v[36:39],a[204:207],v[44:47],v[36:39]
v_fma_f32 v28,-v96,v20,v28
v_fma_f32 v29,-v96,v21,v29
v_fma_f32 v30,-v96,v22,v30
v_fma_f32 v31,-v96,v23,v31
v_mul_f32 v76,v28,v28
v_mul_f32 v77,v29,v29
v_mul_f32 v78,v30,v30
v_mul_f32 v79,v31,v31
v_fma_f32 v76,v98,v76,v97
v_fma_f32 v77,v98,v77,v97
v_fma_f32 v78,v98,v78,v97
v_fma_f32 v79,v98,v79,v97
v_mul_f32 v76,v28,v76
v_mul_f32 v77,v29,v77
v_mul_f32 v78,v30,v78
v_mul_f32 v79,v31,v79
v_exp_f32 v76,v76
v_exp_f32 v77,v77
v_exp_f32 v78,v78
v_exp_f32 v79,v79
v_mfma_f32_16x16x32_bf16 v[32:35],a[208:211],v[48:51],v[32:35]
v_rcp_f32 v74,v74
v_rcp_f32 v75,v75
v_fma_f32 v76,v76,v99,v99
v_fma_f32 v77,v77,v99,v99
v_fma_f32 v78,v78,v99,v99
v_fma_f32 v79,v79,v99,v99
v_rcp_f32 v76,v76
v_rcp_f32 v77,v77
v_rcp_f32 v78,v78
v_rcp_f32 v79,v79
v_mfma_f32_16x16x32_bf16 v[36:39],a[212:215],v[48:51],v[36:39]
ds_read_b128 v[44:47],v2 offset:1024
v_mul_f32 v26,v26,v74
v_mul_f32 v27,v27,v75
v_cvt_pk_bf16_f32 v80,v24,v25
v_cvt_pk_bf16_f32 v81,v26,v27
v_mul_f32 v28,v28,v76
v_mul_f32 v29,v29,v77
v_mul_f32 v30,v30,v78
v_mul_f32 v31,v31,v79
v_cvt_pk_bf16_f32 v82,v28,v29
v_cvt_pk_bf16_f32 v83,v30,v31
v_mfma_f32_16x16x32_bf16 v[32:35],a[216:219],v[52:55],v[32:35]
s_waitcnt lgkmcnt(1)
buffer_store_dwordx4 v[80:83],v3,s[36:39],s73 offen offset:0 sc0 sc1
ds_read_b128 v[48:51],v2 offset:2048
v_xor_b32 v8,16384,v8
v_mfma_f32_16x16x32_bf16 v[36:39],a[220:223],v[52:55],v[36:39]
v_mfma_f32_16x16x32_bf16 v[32:35],a[224:227],v[56:59],v[32:35]
ds_read_b128 v[52:55],v2 offset:3072
v_mfma_f32_16x16x32_bf16 v[36:39],a[228:231],v[56:59],v[36:39]
v_mfma_f32_16x16x32_bf16 v[32:35],a[232:235],v[60:63],v[32:35]
ds_read_b128 v[56:59],v2 offset:4096
v_mfma_f32_16x16x32_bf16 v[36:39],a[236:239],v[60:63],v[36:39]
v_mfma_f32_16x16x32_bf16 v[32:35],a[240:243],v[64:67],v[32:35]
ds_read_b128 v[60:63],v2 offset:5120
v_mfma_f32_16x16x32_bf16 v[36:39],a[244:247],v[64:67],v[36:39]
v_mfma_f32_16x16x32_bf16 v[32:35],a[248:251],v[68:71],v[32:35]
s_waitcnt lgkmcnt(2)
ds_read_b128 v[64:67],v2 offset:6144
v_mfma_f32_16x16x32_bf16 v[36:39],a[252:255],v[68:71],v[36:39]
s_cbranch_scc1 Lg3o_d0w2_%=
s_branch Lg3o_slow0_%=
Lg3o_d0w3_%=:
v_mfma_f32_16x16x32_bf16 v[24:27],a[0:3],v[40:43],0
s_add_u32 m0,s62,16384
ds_read_b128 v[68:71],v2 offset:7168
v_mul_f32 v96,s75,v92
v_mfma_f32_16x16x32_bf16 v[28:31],a[4:7],v[40:43],0
s_mul_i32 s73,s47,7
v_fma_f32 v93,v93,s75,v12
v_fma_f32 v93,-v96,v96,v93
v_mfma_f32_16x16x32_bf16 v[24:27],a[8:11],v[44:47],v[24:27]
s_lshr_b32 s73,s73,1
ds_read_b128 v[40:43],v2 offset:8192
v_max_f32 v93,v93,v12
v_mfma_f32_16x16x32_bf16 v[28:31],a[12:15],v[44:47],v[28:31]
s_lshr_b32 s72,s66,5
v_rsq_f32 v92,v93
v_mfma_f32_16x16x32_bf16 v[24:27],a[16:19],v[48:51],v[24:27]
buffer_load_dwordx4 v1,s[36:39],s66 offen offset:0 sc1 lds
v_mfma_f32_16x16x32_bf16 v[28:31],a[20:23],v[48:51],v[28:31]
s_lshr_b32 s71,s62,2
ds_read_b128 v[44:47],v2 offset:9216
v_mul_f32 v97,s76,v92
v_mfma_f32_16x16x32_bf16 v[24:27],a[24:27],v[52:55],v[24:27]
s_mul_i32 s70,s46,5
ds_read_b128 v[48:51],v2 offset:10240
v_mul_f32 v98,v92,v92
v_mfma_f32_16x16x32_bf16 v[28:31],a[28:31],v[52:55],v[28:31]
s_waitcnt lgkmcnt(3)
v_mul_f32 v99,v93,v92
v_mul_f32 v98,s77,v98
v_mfma_f32_16x16x32_bf16 v[24:27],a[32:35],v[56:59],v[24:27]
buffer_load_dwordx4 v1,s[36:39],s66 offen offset:1024 sc1 lds
v_mfma_f32_16x16x32_bf16 v[28:31],a[36:39],v[56:59],v[28:31]
ds_read_b128 v[52:55],v2 offset:11264
v_mul_f32 v98,v98,v97
v_mfma_f32_16x16x32_bf16 v[24:27],a[40:43],v[60:63],v[24:27]
ds_read_b128 v[56:59],v2 offset:12288
v_fma_f32 v32,-v96,v16,v32
v_mfma_f32_16x16x32_bf16 v[28:31],a[44:47],v[60:63],v[28:31]
v_fma_f32 v33,-v96,v17,v33
v_fma_f32 v34,-v96,v18,v34
v_mfma_f32_16x16x32_bf16 v[24:27],a[48:51],v[64:67],v[24:27]
buffer_load_dwordx4 v1,s[36:39],s66 offen offset:2048 sc1 lds
v_mfma_f32_16x16x32_bf16 v[28:31],a[52:55],v[64:67],v[28:31]
ds_read_b128 v[60:63],v2 offset:13312
v_fma_f32 v35,-v96,v19,v35
v_mfma_f32_16x16x32_bf16 v[24:27],a[56:59],v[68:71],v[24:27]
ds_read_b128 v[64:67],v2 offset:14336
v_mul_f32 v72,v32,v32
v_mfma_f32_16x16x32_bf16 v[28:31],a[60:63],v[68:71],v[28:31]
s_waitcnt lgkmcnt(3)
v_mul_f32 v73,v33,v33)"
R"(
v_mul_f32 v74,v34,v34
v_mfma_f32_16x16x32_bf16 v[24:27],a[64:67],v[40:43],v[24:27]
s_waitcnt vmcnt(9)
ds_read_b128 v[68:71],v2 offset:15360
v_mul_f32 v75,v35,v35
v_mfma_f32_16x16x32_bf16 v[28:31],a[68:71],v[40:43],v[28:31]
s_barrier
v_mfma_f32_16x16x32_bf16 v[24:27],a[72:75],v[44:47],v[24:27]
buffer_load_dwordx4 v1,s[36:39],s66 offen offset:3072 sc1 lds
v_mfma_f32_16x16x32_bf16 v[28:31],a[76:79],v[44:47],v[28:31]
s_add_u32 m0,s71,s88
ds_read_b128 v[40:43],v2 offset:16384
ds_read_b32 v88,v8 offset:0
v_mfma_f32_16x16x32_bf16 v[24:27],a[80:83],v[48:51],v[24:27]
buffer_load_dword v9,s[36:39],s72 offen offset:0 sc1 lds
v_mfma_f32_16x16x32_bf16 v[28:31],a[84:87],v[48:51],v[28:31]
s_xor_b32 s87,s64,65536
ds_read_b128 v[44:47],v2 offset:17408
v_fma_f32 v72,v98,v72,v97
v_mfma_f32_16x16x32_bf16 v[24:27],a[88:91],v[52:55],v[24:27]
s_cmp_ge_u32 s66,s59
ds_read_b128 v[48:51],v2 offset:18432
v_mul_f32 v72,v32,v72
v_mfma_f32_16x16x32_bf16 v[28:31],a[92:95],v[52:55],v[28:31]
s_waitcnt lgkmcnt(4)
v_exp_f32 v72,v72
v_mfma_f32_16x16x32_bf16 v[24:27],a[96:99],v[56:59],v[24:27]
s_nop 0
ds_read_b128 v[52:55],v2 offset:19456
v_fma_f32 v72,v72,v99,v99
v_mfma_f32_16x16x32_bf16 v[28:31],a[100:103],v[56:59],v[28:31]
s_barrier
v_mfma_f32_16x16x32_bf16 v[24:27],a[104:107],v[60:63],v[24:27]
s_waitcnt lgkmcnt(3)
ds_read_b128 v[56:59],v2 offset:20480
ds_read_b32 v13,v7 offset:0
v_mfma_f32_16x16x32_bf16 v[28:31],a[108:111],v[60:63],v[28:31]
s_cselect_b32 s86,s49,s50
v_rcp_f32 v72,v72
v_mfma_f32_16x16x32_bf16 v[24:27],a[112:115],v[64:67],v[24:27]
s_cselect_b32 s87,s87,s63
ds_read_b128 v[60:63],v2 offset:21504
v_add_f32_dpp v88,v88,v88 quad_perm:[1,0,3,2] row_mask:0xf bank_mask:0xf
v_mfma_f32_16x16x32_bf16 v[28:31],a[116:119],v[64:67],v[28:31]
s_mov_b32 s62,s87
v_add_f32_dpp v88,v88,v88 quad_perm:[2,3,0,1] row_mask:0xf bank_mask:0xf
v_fma_f32 v73,v98,v73,v97
v_mfma_f32_16x16x32_bf16 v[24:27],a[120:123],v[68:71],v[24:27]
s_waitcnt lgkmcnt(1)
ds_read_b128 v[64:67],v2 offset:22528
v_mul_f32 v73,v33,v73
v_mfma_f32_16x16x32_bf16 v[28:31],a[124:127],v[68:71],v[28:31]
v_cmp_eq_u32 vcc,s60,v13
v_exp_f32 v73,v73
v_add_f32_dpp v88,v88,v88 row_half_mirror row_mask:0xf bank_mask:0xf
v_mfma_f32_16x16x32_bf16 v[24:27],a[128:131],v[40:43],v[24:27]
s_cmp_eq_u32 vcc_lo,-1
ds_write_b32 v10,v88 offset:0
v_mfma_f32_16x16x32_bf16 v[28:31],a[132:135],v[40:43],v[28:31]
s_cselect_b32 s67,s86,s59
ds_read_b128 v[68:71],v2 offset:23552
v_fma_f32 v73,v73,v99,v99
v_mfma_f32_16x16x32_bf16 v[24:27],a[136:139],v[44:47],v[24:27]
s_mov_b32 m0,s62
ds_read_b128 v[40:43],v2 offset:24576
v_fma_f32 v74,v98,v74,v97
v_mfma_f32_16x16x32_bf16 v[28:31],a[140:143],v[44:47],v[28:31]
buffer_load_dwordx4 v0,s[36:39],s67 offen offset:0 sc1 lds
v_mfma_f32_16x16x32_bf16 v[24:27],a[144:147],v[48:51],v[24:27]
s_xor_b32 s63,s63,65536
ds_read_b128 v[44:47],v2 offset:25600
v_mul_f32 v74,v34,v74
v_mfma_f32_16x16x32_bf16 v[28:31],a[148:151],v[48:51],v[28:31]
s_add_u32 s53,s52,s55
v_exp_f32 v74,v74
v_mfma_f32_16x16x32_bf16 v[24:27],a[152:155],v[52:55],v[24:27]
s_cmp_eq_u32 s53,s56
ds_read_b128 v[48:51],v2 offset:26624
v_fma_f32 v74,v74,v99,v99
v_mfma_f32_16x16x32_bf16 v[28:31],a[156:159],v[52:55],v[28:31]
s_waitcnt lgkmcnt(3)
buffer_load_dwordx4 v0,s[36:39],s67 offen offset:1024 sc1 lds
v_mfma_f32_16x16x32_bf16 v[24:27],a[160:163],v[56:59],v[24:27]
s_cselect_b32 s53,s57,s53
ds_read_b128 v[52:55],v2 offset:27648
v_fma_f32 v75,v98,v75,v97
v_mfma_f32_16x16x32_bf16 v[28:31],a[164:167],v[56:59],v[28:31]
s_cmp_eq_u32 s53,s58
v_rcp_f32 v73,v73
v_mfma_f32_16x16x32_bf16 v[24:27],a[168:171],v[60:63],v[24:27]
s_cselect_b32 s53,s59,s53
ds_read_b128 v[56:59],v2 offset:28672
v_mul_f32 v75,v35,v75
v_mfma_f32_16x16x32_bf16 v[28:31],a[172:175],v[60:63],v[28:31]
buffer_load_dwordx4 v0,s[36:39],s67 offen offset:2048 sc1 lds
v_mfma_f32_16x16x32_bf16 v[24:27],a[176:179],v[64:67],v[24:27]
s_lshr_b32 s70,s70,6
ds_read_b128 v[60:63],v2 offset:29696
v_mul_f32 v32,v32,v72
v_mfma_f32_16x16x32_bf16 v[28:31],a[180:183],v[64:67],v[28:31]
s_cmp_ge_u32 s66,s59
v_exp_f32 v75,v75
v_mfma_f32_16x16x32_bf16 v[24:27],a[184:187],v[68:71],v[24:27]
ds_read_b128 v[64:67],v2 offset:30720
v_fma_f32 v75,v75,v99,v99
v_mfma_f32_16x16x32_bf16 v[28:31],a[188:191],v[68:71],v[28:31]
s_waitcnt lgkmcnt(3)
buffer_load_dwordx4 v0,s[36:39],s67 offen offset:3072 sc1 lds
v_mfma_f32_16x16x32_bf16 v[24:27],a[192:195],v[40:43],v[24:27]
s_waitcnt vmcnt(9)
ds_read_b128 v[68:71],v2 offset:31744
v_mul_f32 v33,v33,v73
v_mfma_f32_16x16x32_bf16 v[28:31],a[196:199],v[40:43],v[28:31]
s_barrier
v_mfma_f32_16x16x32_bf16 v[24:27],a[200:203],v[44:47],v[24:27]
buffer_store_dword v5,v4,s[36:39],s70 offen offset:0 sc0 sc1
ds_read_b128 v[40:43],v2 offset:32768
ds_read_b64 v[92:93],v11 offset:0
v_cvt_pk_bf16_f32 v80,v32,v33
v_mfma_f32_16x16x32_bf16 v[28:31],a[204:207],v[44:47],v[28:31]
v_rcp_f32 v74,v74
v_mfma_f32_16x16x32_bf16 v[24:27],a[208:211],v[48:51],v[24:27]
v_fma_f32 v36,-v96,v20,v36
v_fma_f32 v37,-v96,v21,v37
v_fma_f32 v38,-v96,v22,v38
v_fma_f32 v39,-v96,v23,v39
v_mul_f32 v76,v36,v36
v_mul_f32 v77,v37,v37
v_mul_f32 v78,v38,v38
v_mul_f32 v79,v39,v39
v_fma_f32 v76,v98,v76,v97
v_fma_f32 v77,v98,v77,v97
v_fma_f32 v78,v98,v78,v97
v_fma_f32 v79,v98,v79,v97
v_mul_f32 v76,v36,v76
v_mul_f32 v77,v37,v77
v_mul_f32 v78,v38,v78
v_mul_f32 v79,v39,v79
v_exp_f32 v76,v76
v_exp_f32 v77,v77
v_exp_f32 v78,v78
v_exp_f32 v79,v79
v_mfma_f32_16x16x32_bf16 v[28:31],a[212:215],v[48:51],v[28:31]
ds_read_b128 v[44:47],v2 offset:33792
v_rcp_f32 v75,v75
v_fma_f32 v76,v76,v99,v99
v_fma_f32 v77,v77,v99,v99
v_fma_f32 v78,v78,v99,v99
v_fma_f32 v79,v79,v99,v99
v_rcp_f32 v76,v76
v_rcp_f32 v77,v77
v_rcp_f32 v78,v78
v_rcp_f32 v79,v79
v_mfma_f32_16x16x32_bf16 v[24:27],a[216:219],v[52:55],v[24:27]
s_waitcnt lgkmcnt(1)
v_mul_f32 v34,v34,v74
v_mul_f32 v35,v35,v75
v_cvt_pk_bf16_f32 v81,v34,v35
v_mul_f32 v36,v36,v76
v_mul_f32 v37,v37,v77
v_mul_f32 v38,v38,v78
v_mul_f32 v39,v39,v79
v_cvt_pk_bf16_f32 v82,v36,v37
v_cvt_pk_bf16_f32 v83,v38,v39
v_mfma_f32_16x16x32_bf16 v[28:31],a[220:223],v[52:55],v[28:31]
ds_read_b128 v[48:51],v2 offset:34816
buffer_store_dwordx4 v[80:83],v3,s[36:39],s73 offen offset:0 sc0 sc1
v_mfma_f32_16x16x32_bf16 v[24:27],a[224:227],v[56:59],v[24:27]
ds_read_b128 v[52:55],v2 offset:35840
v_mfma_f32_16x16x32_bf16 v[28:31],a[228:231],v[56:59],v[28:31]
v_mfma_f32_16x16x32_bf16 v[24:27],a[232:235],v[60:63],v[24:27]
ds_read_b128 v[56:59],v2 offset:36864
v_mfma_f32_16x16x32_bf16 v[28:31],a[236:239],v[60:63],v[28:31]
v_mfma_f32_16x16x32_bf16 v[24:27],a[240:243],v[64:67],v[24:27]
ds_read_b128 v[60:63],v2 offset:37888
v_mfma_f32_16x16x32_bf16 v[28:31],a[244:247],v[64:67],v[28:31]
v_mfma_f32_16x16x32_bf16 v[24:27],a[248:251],v[68:71],v[24:27]
s_waitcnt lgkmcnt(2)
ds_read_b128 v[64:67],v2 offset:38912
v_mfma_f32_16x16x32_bf16 v[28:31],a[252:255],v[68:71],v[28:31]
s_cbranch_scc1 Lg3o_slow1_%=
Lg3o_d1w3_%=:
v_mfma_f32_16x16x32_bf16 v[32:35],a[0:3],v[40:43],0
s_add_u32 m0,s62,16384
ds_read_b128 v[68:71],v2 offset:39936
v_mul_f32 v96,s75,v92
v_mfma_f32_16x16x32_bf16 v[36:39],a[4:7],v[40:43],0
s_mul_i32 s73,s48,7
v_fma_f32 v93,v93,s75,v12
v_fma_f32 v93,-v96,v96,v93
v_mfma_f32_16x16x32_bf16 v[32:35],a[8:11],v[44:47],v[32:35]
s_lshr_b32 s73,s73,1
ds_read_b128 v[40:43],v2 offset:40960
v_max_f32 v93,v93,v12
v_mfma_f32_16x16x32_bf16 v[36:39],a[12:15],v[44:47],v[36:39]
s_lshr_b32 s72,s67,5
v_rsq_f32 v92,v93
v_mfma_f32_16x16x32_bf16 v[32:35],a[16:19],v[48:51],v[32:35]
buffer_load_dwordx4 v1,s[36:39],s67 offen offset:0 sc1 lds
v_mfma_f32_16x16x32_bf16 v[36:39],a[20:23],v[48:51],v[36:39])"
R"(
s_lshr_b32 s71,s62,2
ds_read_b128 v[44:47],v2 offset:41984
v_mul_f32 v97,s76,v92
v_mfma_f32_16x16x32_bf16 v[32:35],a[24:27],v[52:55],v[32:35]
s_mul_i32 s70,s47,5
ds_read_b128 v[48:51],v2 offset:43008
v_mul_f32 v98,v92,v92
v_mfma_f32_16x16x32_bf16 v[36:39],a[28:31],v[52:55],v[36:39]
s_waitcnt lgkmcnt(3)
v_mul_f32 v99,v93,v92
v_mul_f32 v98,s77,v98
v_mfma_f32_16x16x32_bf16 v[32:35],a[32:35],v[56:59],v[32:35]
buffer_load_dwordx4 v1,s[36:39],s67 offen offset:1024 sc1 lds
v_mfma_f32_16x16x32_bf16 v[36:39],a[36:39],v[56:59],v[36:39]
s_mov_b64 s[44:45],s[46:47]
ds_read_b128 v[52:55],v2 offset:44032
v_mul_f32 v98,v98,v97
v_mfma_f32_16x16x32_bf16 v[32:35],a[40:43],v[60:63],v[32:35]
s_mov_b64 s[46:47],s[48:49]
ds_read_b128 v[56:59],v2 offset:45056
v_fma_f32 v24,-v96,v16,v24
v_mfma_f32_16x16x32_bf16 v[36:39],a[44:47],v[60:63],v[36:39]
s_mov_b64 s[48:49],s[50:51]
v_fma_f32 v25,-v96,v17,v25
v_fma_f32 v26,-v96,v18,v26
v_mfma_f32_16x16x32_bf16 v[32:35],a[48:51],v[64:67],v[32:35]
buffer_load_dwordx4 v1,s[36:39],s67 offen offset:2048 sc1 lds
v_mfma_f32_16x16x32_bf16 v[36:39],a[52:55],v[64:67],v[36:39]
ds_read_b128 v[60:63],v2 offset:46080
v_fma_f32 v27,-v96,v19,v27
v_mfma_f32_16x16x32_bf16 v[32:35],a[56:59],v[68:71],v[32:35]
ds_read_b128 v[64:67],v2 offset:47104
v_mul_f32 v72,v24,v24
v_mfma_f32_16x16x32_bf16 v[36:39],a[60:63],v[68:71],v[36:39]
s_waitcnt lgkmcnt(3)
v_mul_f32 v73,v25,v25
v_mul_f32 v74,v26,v26
v_mfma_f32_16x16x32_bf16 v[32:35],a[64:67],v[40:43],v[32:35]
s_waitcnt vmcnt(9)
ds_read_b128 v[68:71],v2 offset:48128
v_mul_f32 v75,v27,v27
v_mfma_f32_16x16x32_bf16 v[36:39],a[68:71],v[40:43],v[36:39]
s_barrier
v_mfma_f32_16x16x32_bf16 v[32:35],a[72:75],v[44:47],v[32:35]
buffer_load_dwordx4 v1,s[36:39],s67 offen offset:3072 sc1 lds
v_mfma_f32_16x16x32_bf16 v[36:39],a[76:79],v[44:47],v[36:39]
s_add_u32 m0,s71,s88
ds_read_b128 v[40:43],v2 offset:49152
ds_read_b32 v88,v8 offset:8192
v_mfma_f32_16x16x32_bf16 v[32:35],a[80:83],v[48:51],v[32:35]
buffer_load_dword v9,s[36:39],s72 offen offset:0 sc1 lds
v_mfma_f32_16x16x32_bf16 v[36:39],a[84:87],v[48:51],v[36:39]
s_xor_b32 s87,s63,65536
ds_read_b128 v[44:47],v2 offset:50176
v_fma_f32 v72,v98,v72,v97
v_mfma_f32_16x16x32_bf16 v[32:35],a[88:91],v[52:55],v[32:35]
s_cmp_ge_u32 s67,s59
ds_read_b128 v[48:51],v2 offset:51200
v_mul_f32 v72,v24,v72
v_mfma_f32_16x16x32_bf16 v[36:39],a[92:95],v[52:55],v[36:39]
s_waitcnt lgkmcnt(4)
v_exp_f32 v72,v72
v_mfma_f32_16x16x32_bf16 v[32:35],a[96:99],v[56:59],v[32:35]
s_nop 0
ds_read_b128 v[52:55],v2 offset:52224
v_fma_f32 v72,v72,v99,v99
v_mfma_f32_16x16x32_bf16 v[36:39],a[100:103],v[56:59],v[36:39]
s_barrier
v_mfma_f32_16x16x32_bf16 v[32:35],a[104:107],v[60:63],v[32:35]
s_waitcnt lgkmcnt(3)
ds_read_b128 v[56:59],v2 offset:53248
ds_read_b32 v13,v7 offset:256
v_mfma_f32_16x16x32_bf16 v[36:39],a[108:111],v[60:63],v[36:39]
s_cselect_b32 s86,s50,s51
v_rcp_f32 v72,v72
v_mfma_f32_16x16x32_bf16 v[32:35],a[112:115],v[64:67],v[32:35]
s_cselect_b32 s87,s87,s64
ds_read_b128 v[60:63],v2 offset:54272
v_add_f32_dpp v88,v88,v88 quad_perm:[1,0,3,2] row_mask:0xf bank_mask:0xf
v_mfma_f32_16x16x32_bf16 v[36:39],a[116:119],v[64:67],v[36:39]
s_mov_b32 s62,s87
v_add_f32_dpp v88,v88,v88 quad_perm:[2,3,0,1] row_mask:0xf bank_mask:0xf
v_fma_f32 v73,v98,v73,v97
v_mfma_f32_16x16x32_bf16 v[32:35],a[120:123],v[68:71],v[32:35]
s_waitcnt lgkmcnt(1)
ds_read_b128 v[64:67],v2 offset:55296
v_mul_f32 v73,v25,v73
v_mfma_f32_16x16x32_bf16 v[36:39],a[124:127],v[68:71],v[36:39]
v_cmp_eq_u32 vcc,s60,v13
v_exp_f32 v73,v73
v_add_f32_dpp v88,v88,v88 row_half_mirror row_mask:0xf bank_mask:0xf
v_mfma_f32_16x16x32_bf16 v[32:35],a[128:131],v[40:43],v[32:35]
s_cmp_eq_u32 vcc_lo,-1
ds_write_b32 v10,v88 offset:128
v_mfma_f32_16x16x32_bf16 v[36:39],a[132:135],v[40:43],v[36:39]
s_cselect_b32 s66,s86,s59
ds_read_b128 v[68:71],v2 offset:56320
v_fma_f32 v73,v73,v99,v99
v_mfma_f32_16x16x32_bf16 v[32:35],a[136:139],v[44:47],v[32:35]
s_mov_b32 m0,s62
ds_read_b128 v[40:43],v2 offset:57344
v_fma_f32 v74,v98,v74,v97
v_mfma_f32_16x16x32_bf16 v[36:39],a[140:143],v[44:47],v[36:39]
buffer_load_dwordx4 v0,s[36:39],s66 offen offset:0 sc1 lds
v_mfma_f32_16x16x32_bf16 v[32:35],a[144:147],v[48:51],v[32:35]
s_xor_b32 s64,s64,65536
ds_read_b128 v[44:47],v2 offset:58368
v_mul_f32 v74,v26,v74
v_mfma_f32_16x16x32_bf16 v[36:39],a[148:151],v[48:51],v[36:39]
s_add_u32 s74,s53,s55
v_exp_f32 v74,v74
v_mfma_f32_16x16x32_bf16 v[32:35],a[152:155],v[52:55],v[32:35]
s_cmp_eq_u32 s74,s56
ds_read_b128 v[48:51],v2 offset:59392
v_fma_f32 v74,v74,v99,v99
v_mfma_f32_16x16x32_bf16 v[36:39],a[156:159],v[52:55],v[36:39]
s_waitcnt lgkmcnt(3)
buffer_load_dwordx4 v0,s[36:39],s66 offen offset:1024 sc1 lds
v_mfma_f32_16x16x32_bf16 v[32:35],a[160:163],v[56:59],v[32:35]
s_cselect_b32 s74,s57,s74
ds_read_b128 v[52:55],v2 offset:60416
v_fma_f32 v75,v98,v75,v97
v_mfma_f32_16x16x32_bf16 v[36:39],a[164:167],v[56:59],v[36:39]
s_cmp_eq_u32 s74,s58
v_rcp_f32 v73,v73
v_mfma_f32_16x16x32_bf16 v[32:35],a[168:171],v[60:63],v[32:35]
s_cselect_b32 s74,s59,s74
ds_read_b128 v[56:59],v2 offset:61440
v_mul_f32 v75,v27,v75
v_mfma_f32_16x16x32_bf16 v[36:39],a[172:175],v[60:63],v[36:39]
buffer_load_dwordx4 v0,s[36:39],s66 offen offset:2048 sc1 lds
v_mfma_f32_16x16x32_bf16 v[32:35],a[176:179],v[64:67],v[32:35]
s_lshr_b32 s70,s70,6
ds_read_b128 v[60:63],v2 offset:62464
v_mul_f32 v24,v24,v72
v_mfma_f32_16x16x32_bf16 v[36:39],a[180:183],v[64:67],v[36:39]
s_mov_b64 s[50:51],s[52:53]
v_exp_f32 v75,v75
v_mfma_f32_16x16x32_bf16 v[32:35],a[184:187],v[68:71],v[32:35]
s_mov_b32 s52,s74
ds_read_b128 v[64:67],v2 offset:63488
v_fma_f32 v75,v75,v99,v99
v_mfma_f32_16x16x32_bf16 v[36:39],a[188:191],v[68:71],v[36:39]
s_waitcnt lgkmcnt(3)
buffer_load_dwordx4 v0,s[36:39],s66 offen offset:3072 sc1 lds
v_mfma_f32_16x16x32_bf16 v[32:35],a[192:195],v[40:43],v[32:35]
s_waitcnt vmcnt(9)
ds_read_b128 v[68:71],v2 offset:64512
v_xor_b32 v2,65536,v2
v_mfma_f32_16x16x32_bf16 v[36:39],a[196:199],v[40:43],v[36:39]
s_barrier
v_mfma_f32_16x16x32_bf16 v[32:35],a[200:203],v[44:47],v[32:35]
buffer_store_dword v5,v4,s[36:39],s70 offen offset:0 sc0 sc1
ds_read_b128 v[40:43],v2 offset:0
ds_read_b64 v[92:93],v11 offset:128
v_mul_f32 v25,v25,v73
v_mfma_f32_16x16x32_bf16 v[36:39],a[204:207],v[44:47],v[36:39]
s_cmp_lt_u32 s67,s59
v_rcp_f32 v74,v74
v_mfma_f32_16x16x32_bf16 v[32:35],a[208:211],v[48:51],v[32:35]
v_fma_f32 v28,-v96,v20,v28
v_fma_f32 v29,-v96,v21,v29
v_fma_f32 v30,-v96,v22,v30
v_fma_f32 v31,-v96,v23,v31
v_mul_f32 v76,v28,v28
v_mul_f32 v77,v29,v29
v_mul_f32 v78,v30,v30
v_mul_f32 v79,v31,v31
v_fma_f32 v76,v98,v76,v97
v_fma_f32 v77,v98,v77,v97
v_fma_f32 v78,v98,v78,v97
v_fma_f32 v79,v98,v79,v97
v_mul_f32 v76,v28,v76
v_mul_f32 v77,v29,v77
v_mul_f32 v78,v30,v78
v_mul_f32 v79,v31,v79
v_exp_f32 v76,v76
v_exp_f32 v77,v77
v_exp_f32 v78,v78
v_exp_f32 v79,v79
v_mfma_f32_16x16x32_bf16 v[36:39],a[212:215],v[48:51],v[36:39]
ds_read_b128 v[44:47],v2 offset:1024
v_rcp_f32 v75,v75
v_fma_f32 v76,v76,v99,v99
v_fma_f32 v77,v77,v99,v99
v_fma_f32 v78,v78,v99,v99
v_fma_f32 v79,v79,v99,v99
v_rcp_f32 v76,v76
v_rcp_f32 v77,v77
v_rcp_f32 v78,v78
v_rcp_f32 v79,v79
v_mfma_f32_16x16x32_bf16 v[32:35],a[216:219],v[52:55],v[32:35]
s_waitcnt lgkmcnt(1)
v_mul_f32 v26,v26,v74
v_mul_f32 v27,v27,v75
v_cvt_pk_bf16_f32 v80,v24,v25
v_cvt_pk_bf16_f32 v81,v26,v27
v_mul_f32 v28,v28,v76
v_mul_f32 v29,v29,v77
v_mul_f32 v30,v30,v78)"
R"(
v_mul_f32 v31,v31,v79
v_cvt_pk_bf16_f32 v82,v28,v29
v_cvt_pk_bf16_f32 v83,v30,v31
v_mfma_f32_16x16x32_bf16 v[36:39],a[220:223],v[52:55],v[36:39]
ds_read_b128 v[48:51],v2 offset:2048
buffer_store_dwordx4 v[80:83],v3,s[36:39],s73 offen offset:0 sc0 sc1
v_xor_b32 v8,16384,v8
v_mfma_f32_16x16x32_bf16 v[32:35],a[224:227],v[56:59],v[32:35]
ds_read_b128 v[52:55],v2 offset:3072
v_mfma_f32_16x16x32_bf16 v[36:39],a[228:231],v[56:59],v[36:39]
v_mfma_f32_16x16x32_bf16 v[32:35],a[232:235],v[60:63],v[32:35]
ds_read_b128 v[56:59],v2 offset:4096
v_mfma_f32_16x16x32_bf16 v[36:39],a[236:239],v[60:63],v[36:39]
v_mfma_f32_16x16x32_bf16 v[32:35],a[240:243],v[64:67],v[32:35]
ds_read_b128 v[60:63],v2 offset:5120
v_mfma_f32_16x16x32_bf16 v[36:39],a[244:247],v[64:67],v[36:39]
v_mfma_f32_16x16x32_bf16 v[32:35],a[248:251],v[68:71],v[32:35]
s_waitcnt lgkmcnt(2)
ds_read_b128 v[64:67],v2 offset:6144
v_mfma_f32_16x16x32_bf16 v[36:39],a[252:255],v[68:71],v[36:39]
s_cbranch_scc1 Lg3o_d0w3_%=
s_branch Lg3o_slow0_%=
Lg3o_slow0_%=:
s_cmp_lt_u32 s66,s59
s_cbranch_scc1 Lg3o_gs0_%=
Lg3o_slowb0_%=:
s_cmp_eq_u32 s54,1
s_cselect_b32 s68,s49,s48
s_lshr_b32 s68,s68,5
s_add_u32 m0,s78,8192
s_nop 0
buffer_load_dword v6,s[36:39],s68 offen offset:0 sc1 lds
s_mul_i32 s73,s47,7
s_lshr_b32 s73,s73,1
v_mul_f32 v96,s75,v92
v_fma_f32 v93,v93,s75,v12
v_fma_f32 v93,-v96,v96,v93
v_max_f32 v93,v93,v12
v_rsq_f32 v92,v93
s_nop 0
v_mul_f32 v97,s76,v92
v_mul_f32 v98,v92,v92
v_mul_f32 v99,v93,v92
v_mul_f32 v98,s77,v98
v_mul_f32 v98,v98,v97
v_fma_f32 v32,-v96,v16,v32
v_fma_f32 v33,-v96,v17,v33
v_fma_f32 v34,-v96,v18,v34
v_fma_f32 v35,-v96,v19,v35
v_mul_f32 v72,v32,v32
v_mul_f32 v73,v33,v33
v_mul_f32 v74,v34,v34
v_mul_f32 v75,v35,v35
v_fma_f32 v72,v98,v72,v97
v_fma_f32 v73,v98,v73,v97
v_fma_f32 v74,v98,v74,v97
v_fma_f32 v75,v98,v75,v97
v_mul_f32 v72,v32,v72
v_mul_f32 v73,v33,v73
v_mul_f32 v74,v34,v74
v_mul_f32 v75,v35,v75
v_exp_f32 v72,v72
v_exp_f32 v73,v73
v_exp_f32 v74,v74
v_exp_f32 v75,v75
v_fma_f32 v72,v72,v99,v99
v_fma_f32 v73,v73,v99,v99
v_fma_f32 v74,v74,v99,v99
v_fma_f32 v75,v75,v99,v99
v_rcp_f32 v72,v72
v_rcp_f32 v73,v73
v_rcp_f32 v74,v74
v_rcp_f32 v75,v75
v_mul_f32 v32,v32,v72
s_add_u32 m0,s78,8448
s_nop 0
buffer_load_dword v6,s[36:39],s68 offen offset:0 sc1 lds
v_mul_f32 v33,v33,v73
v_mul_f32 v34,v34,v74
v_mul_f32 v35,v35,v75
v_cvt_pk_bf16_f32 v80,v32,v33
v_cvt_pk_bf16_f32 v81,v34,v35
v_fma_f32 v36,-v96,v20,v36
v_fma_f32 v37,-v96,v21,v37
v_fma_f32 v38,-v96,v22,v38
v_fma_f32 v39,-v96,v23,v39
v_mul_f32 v76,v36,v36
v_mul_f32 v77,v37,v37
v_mul_f32 v78,v38,v38
v_mul_f32 v79,v39,v39
v_fma_f32 v76,v98,v76,v97
v_fma_f32 v77,v98,v77,v97
v_fma_f32 v78,v98,v78,v97
v_fma_f32 v79,v98,v79,v97
v_mul_f32 v76,v36,v76
v_mul_f32 v77,v37,v77
v_mul_f32 v78,v38,v78
v_mul_f32 v79,v39,v79
v_exp_f32 v76,v76
v_exp_f32 v77,v77
v_exp_f32 v78,v78
v_exp_f32 v79,v79
v_fma_f32 v76,v76,v99,v99
v_fma_f32 v77,v77,v99,v99
v_fma_f32 v78,v78,v99,v99
v_fma_f32 v79,v79,v99,v99
v_rcp_f32 v76,v76
v_rcp_f32 v77,v77
v_rcp_f32 v78,v78
v_rcp_f32 v79,v79
v_mul_f32 v36,v36,v76
v_mul_f32 v37,v37,v77
v_mul_f32 v38,v38,v78
v_mul_f32 v39,v39,v79
v_cvt_pk_bf16_f32 v82,v36,v37
v_cvt_pk_bf16_f32 v83,v38,v39
buffer_store_dwordx4 v[80:83],v3,s[36:39],s73 offen offset:0 sc0 sc1
s_cmp_ge_u32 s48,s59
s_cbranch_scc1 Lg3o_done0_%=
s_add_u32 m0,s78,16384
s_nop 0
buffer_load_dword v6,s[36:39],s68 offen offset:0 sc1 lds
s_waitcnt vmcnt(3)
s_barrier
ds_read_b32 v13,v7 offset:8192
s_mul_i32 s70,s46,5
s_lshr_b32 s70,s70,6
buffer_store_dword v5,v4,s[36:39],s70 offen offset:0 sc0 sc1
s_mov_b32 s46,s59
s_mov_b32 s69,18432
s_waitcnt lgkmcnt(0)
v_cmp_eq_u32 vcc,s60,v13
s_nop 0
s_cmp_eq_u32 vcc_lo,-1
s_cbranch_scc1 Lg3o_go0_%=
s_add_u32 m0,s78,16640
s_nop 0
buffer_load_dword v6,s[36:39],s68 offen offset:0 sc1 lds
s_waitcnt vmcnt(4)
s_barrier
ds_read_b32 v13,v7 offset:8448
s_mov_b32 s69,18688
s_waitcnt lgkmcnt(0)
v_cmp_eq_u32 vcc,s60,v13
s_nop 0
s_cmp_eq_u32 vcc_lo,-1
s_cbranch_scc1 Lg3o_go0_%=
s_add_u32 m0,s78,8192
s_nop 0
buffer_load_dword v6,s[36:39],s68 offen offset:0 sc1 lds
s_waitcnt vmcnt(3)
s_barrier
ds_read_b32 v13,v7 offset:16384
s_mul_i32 s70,s47,5
s_lshr_b32 s70,s70,6
buffer_store_dword v5,v4,s[36:39],s70 offen offset:0 sc0 sc1
s_mov_b32 s47,s59
s_mov_b32 s69,10240
s_waitcnt lgkmcnt(0)
v_cmp_eq_u32 vcc,s60,v13
s_nop 0
s_cmp_eq_u32 vcc_lo,-1
s_cbranch_scc1 Lg3o_go0_%=
s_add_u32 m0,s78,8448
s_nop 0
buffer_load_dword v6,s[36:39],s68 offen offset:0 sc1 lds
s_waitcnt vmcnt(3)
s_barrier
ds_read_b32 v13,v7 offset:16640
s_mov_b32 s69,10496
s_waitcnt lgkmcnt(0)
v_cmp_eq_u32 vcc,s60,v13
s_nop 0
s_cmp_eq_u32 vcc_lo,-1
s_cbranch_scc1 Lg3o_go0_%=
Lg3o_poll0_%=:
s_add_u32 m0,s78,16384
s_nop 0
buffer_load_dword v6,s[36:39],s68 offen offset:0 sc1 lds
s_waitcnt vmcnt(2)
s_barrier
ds_read_b32 v13,v7 offset:8192
s_mov_b32 s69,18432
s_waitcnt lgkmcnt(0)
v_cmp_eq_u32 vcc,s60,v13
s_nop 0
s_cmp_eq_u32 vcc_lo,-1
s_cbranch_scc1 Lg3o_go0_%=
s_sleep 4
s_add_u32 m0,s78,16640
s_nop 0
buffer_load_dword v6,s[36:39],s68 offen offset:0 sc1 lds
s_waitcnt vmcnt(2)
s_barrier
ds_read_b32 v13,v7 offset:8448
s_mov_b32 s69,18688
s_waitcnt lgkmcnt(0)
v_cmp_eq_u32 vcc,s60,v13
s_nop 0
s_cmp_eq_u32 vcc_lo,-1)"
R"(
s_cbranch_scc1 Lg3o_go0_%=
s_sleep 4
s_add_u32 m0,s78,8192
s_nop 0
buffer_load_dword v6,s[36:39],s68 offen offset:0 sc1 lds
s_waitcnt vmcnt(2)
s_barrier
ds_read_b32 v13,v7 offset:16384
s_mov_b32 s69,10240
s_waitcnt lgkmcnt(0)
v_cmp_eq_u32 vcc,s60,v13
s_nop 0
s_cmp_eq_u32 vcc_lo,-1
s_cbranch_scc1 Lg3o_go0_%=
s_sleep 4
s_add_u32 m0,s78,8448
s_nop 0
buffer_load_dword v6,s[36:39],s68 offen offset:0 sc1 lds
s_waitcnt vmcnt(2)
s_barrier
ds_read_b32 v13,v7 offset:16640
s_mov_b32 s69,10496
s_waitcnt lgkmcnt(0)
v_cmp_eq_u32 vcc,s60,v13
s_nop 0
s_cmp_eq_u32 vcc_lo,-1
s_cbranch_scc1 Lg3o_go0_%=
s_sleep 4
s_add_u32 s79,s79,1
s_cmp_lt_u32 s79,20000
s_cbranch_scc1 Lg3o_poll0_%=
Lg3o_go0_%=:
s_xor_b32 s62,s63,65536
s_mov_b32 m0,s62
s_nop 0
buffer_load_dwordx4 v0,s[36:39],s48 offen offset:0 sc1 lds
buffer_load_dwordx4 v0,s[36:39],s48 offen offset:1024 sc1 lds
buffer_load_dwordx4 v0,s[36:39],s48 offen offset:2048 sc1 lds
buffer_load_dwordx4 v0,s[36:39],s48 offen offset:3072 sc1 lds
s_add_u32 m0,s62,16384
s_nop 0
buffer_load_dwordx4 v1,s[36:39],s48 offen offset:0 sc1 lds
buffer_load_dwordx4 v1,s[36:39],s48 offen offset:1024 sc1 lds
buffer_load_dwordx4 v1,s[36:39],s48 offen offset:2048 sc1 lds
buffer_load_dwordx4 v1,s[36:39],s48 offen offset:3072 sc1 lds
s_lshr_b32 s72,s48,5
s_lshr_b32 s71,s62,2
s_add_u32 m0,s71,s88
s_nop 0
buffer_load_dword v9,s[36:39],s72 offen offset:0 sc1 lds
s_cmp_eq_u32 vcc_hi,-1
s_cselect_b32 s66,s49,s59
s_xor_b32 s62,s64,65536
s_cmp_ge_u32 s66,s59
s_cselect_b32 s68,s49,s50
s_lshr_b32 s68,s68,5
s_add_u32 m0,s78,0
s_nop 0
buffer_load_dword v6,s[36:39],s68 offen offset:0 sc1 lds
s_mov_b32 m0,s62
s_nop 0
buffer_load_dwordx4 v0,s[36:39],s66 offen offset:0 sc1 lds
buffer_load_dwordx4 v0,s[36:39],s66 offen offset:1024 sc1 lds
buffer_load_dwordx4 v0,s[36:39],s66 offen offset:2048 sc1 lds
buffer_load_dwordx4 v0,s[36:39],s66 offen offset:3072 sc1 lds
s_waitcnt vmcnt(10)
s_barrier
ds_read_b128 v[40:43],v2 offset:0
ds_read_b128 v[44:47],v2 offset:1024
ds_read_b128 v[48:51],v2 offset:2048
ds_read_b128 v[52:55],v2 offset:3072
ds_read_b128 v[56:59],v2 offset:4096
ds_read_b128 v[60:63],v2 offset:5120
ds_read_b128 v[64:67],v2 offset:6144
s_mul_i32 s70,s46,5
s_lshr_b32 s70,s70,6
buffer_store_dword v5,v4,s[36:39],s70 offen offset:0 sc0 sc1
s_mov_b32 s46,s59
s_mul_i32 s70,s47,5
s_lshr_b32 s70,s70,6
buffer_store_dword v5,v4,s[36:39],s70 offen offset:0 sc0 sc1
s_mov_b32 s47,s59
s_mul_i32 s70,s59,5
s_lshr_b32 s70,s70,6
buffer_store_dword v5,v4,s[36:39],s70 offen offset:0 sc0 sc1
s_waitcnt lgkmcnt(3)
s_setpc_b64 s[90:91]
Lg3o_gs0_%=:
s_cmp_eq_u32 s54,1
s_cselect_b32 s68,s49,s48
s_lshr_b32 s68,s68,5
s_mul_i32 s73,s47,7
s_lshr_b32 s73,s73,1
s_add_u32 m0,s78,8192
s_nop 0
buffer_load_dword v6,s[36:39],s68 offen offset:0 sc1 lds
v_mul_f32 v96,s75,v92
v_fma_f32 v93,v93,s75,v12
v_fma_f32 v93,-v96,v96,v93
v_max_f32 v93,v93,v12
v_rsq_f32 v92,v93
s_nop 0
v_mul_f32 v97,s76,v92
v_mul_f32 v98,v92,v92
v_mul_f32 v99,v93,v92
v_mul_f32 v98,s77,v98
v_mul_f32 v98,v98,v97
v_fma_f32 v32,-v96,v16,v32
v_fma_f32 v33,-v96,v17,v33
v_fma_f32 v34,-v96,v18,v34
s_add_u32 m0,s62,16384
s_nop 0
buffer_load_dwordx4 v1,s[36:39],s66 offen offset:0 sc1 lds
v_fma_f32 v35,-v96,v19,v35
v_mul_f32 v72,v32,v32
v_mul_f32 v73,v33,v33
v_mul_f32 v74,v34,v34
v_mul_f32 v75,v35,v35
v_fma_f32 v72,v98,v72,v97
v_fma_f32 v73,v98,v73,v97
v_fma_f32 v74,v98,v74,v97
v_fma_f32 v75,v98,v75,v97
v_mul_f32 v72,v32,v72
v_mul_f32 v73,v33,v73
v_mul_f32 v74,v34,v74
v_mul_f32 v75,v35,v75
buffer_load_dwordx4 v1,s[36:39],s66 offen offset:1024 sc1 lds
v_exp_f32 v72,v72
v_exp_f32 v73,v73
v_exp_f32 v74,v74
v_exp_f32 v75,v75
v_fma_f32 v72,v72,v99,v99
v_fma_f32 v73,v73,v99,v99
v_fma_f32 v74,v74,v99,v99
v_fma_f32 v75,v75,v99,v99
v_rcp_f32 v72,v72
v_rcp_f32 v73,v73
v_rcp_f32 v74,v74
v_rcp_f32 v75,v75
v_mul_f32 v32,v32,v72
buffer_load_dwordx4 v1,s[36:39],s66 offen offset:2048 sc1 lds
v_mul_f32 v33,v33,v73
v_mul_f32 v34,v34,v74
v_mul_f32 v35,v35,v75
v_cvt_pk_bf16_f32 v80,v32,v33
v_cvt_pk_bf16_f32 v81,v34,v35
v_fma_f32 v36,-v96,v20,v36
v_fma_f32 v37,-v96,v21,v37
v_fma_f32 v38,-v96,v22,v38
v_fma_f32 v39,-v96,v23,v39
v_mul_f32 v76,v36,v36
v_mul_f32 v77,v37,v37
v_mul_f32 v78,v38,v38
v_mul_f32 v79,v39,v39
buffer_load_dwordx4 v1,s[36:39],s66 offen offset:3072 sc1 lds
v_fma_f32 v76,v98,v76,v97
v_fma_f32 v77,v98,v77,v97
v_fma_f32 v78,v98,v78,v97
v_fma_f32 v79,v98,v79,v97
v_mul_f32 v76,v36,v76
v_mul_f32 v77,v37,v77
v_mul_f32 v78,v38,v78
v_mul_f32 v79,v39,v79
v_exp_f32 v76,v76
v_exp_f32 v77,v77
v_exp_f32 v78,v78
v_exp_f32 v79,v79
v_fma_f32 v76,v76,v99,v99
s_lshr_b32 s72,s66,5
s_lshr_b32 s71,s62,2
s_add_u32 m0,s71,s88
s_nop 0
buffer_load_dword v9,s[36:39],s72 offen offset:0 sc1 lds
v_fma_f32 v77,v77,v99,v99
v_fma_f32 v78,v78,v99,v99
v_fma_f32 v79,v79,v99,v99
v_rcp_f32 v76,v76
v_rcp_f32 v77,v77
v_rcp_f32 v78,v78
v_rcp_f32 v79,v79
v_mul_f32 v36,v36,v76
v_mul_f32 v37,v37,v77
v_mul_f32 v38,v38,v78
v_mul_f32 v39,v39,v79
v_cvt_pk_bf16_f32 v82,v36,v37
v_cvt_pk_bf16_f32 v83,v38,v39
buffer_store_dwordx4 v[80:83],v3,s[36:39],s73 offen offset:0 sc0 sc1
s_waitcnt vmcnt(6)
s_barrier
ds_read_b32 v13,v7 offset:8192
ds_read_b128 v[40:43],v2 offset:0
ds_read_b128 v[44:47],v2 offset:1024
ds_read_b128 v[48:51],v2 offset:2048
ds_read_b128 v[52:55],v2 offset:3072
ds_read_b128 v[56:59],v2 offset:4096
ds_read_b128 v[60:63],v2 offset:5120
ds_read_b128 v[64:67],v2 offset:6144
s_mul_i32 s70,s46,5
s_lshr_b32 s70,s70,6
buffer_store_dword v5,v4,s[36:39],s70 offen offset:0 sc0 sc1)"
R"(
s_waitcnt lgkmcnt(7)
v_cmp_eq_u32 vcc,s60,v13
s_nop 0
s_cmp_eq_u32 vcc_hi,-1
s_cselect_b32 s66,s49,s59
s_xor_b32 s62,s64,65536
s_cmp_ge_u32 s66,s59
s_cselect_b32 s68,s49,s50
s_lshr_b32 s68,s68,5
s_add_u32 m0,s78,0
s_nop 0
buffer_load_dword v6,s[36:39],s68 offen offset:0 sc1 lds
s_mov_b32 m0,s62
s_nop 0
buffer_load_dwordx4 v0,s[36:39],s66 offen offset:0 sc1 lds
buffer_load_dwordx4 v0,s[36:39],s66 offen offset:1024 sc1 lds
buffer_load_dwordx4 v0,s[36:39],s66 offen offset:2048 sc1 lds
buffer_load_dwordx4 v0,s[36:39],s66 offen offset:3072 sc1 lds
s_mov_b32 s46,s47
s_mov_b32 s47,s59
s_mul_i32 s70,s59,5
s_lshr_b32 s70,s70,6
buffer_store_dword v5,v4,s[36:39],s70 offen offset:0 sc0 sc1
s_waitcnt lgkmcnt(3)
s_setpc_b64 s[90:91]
Lg3o_done0_%=:
s_waitcnt vmcnt(0)
s_barrier
s_mul_i32 s70,s46,5
s_lshr_b32 s70,s70,6
buffer_store_dword v5,v4,s[36:39],s70 offen offset:0 sc0 sc1
s_mul_i32 s70,s47,5
s_lshr_b32 s70,s70,6
buffer_store_dword v5,v4,s[36:39],s70 offen offset:0 sc0 sc1
s_branch Lg3o_done_%=
Lg3o_slow1_%=:
s_cmp_lt_u32 s67,s59
s_cbranch_scc1 Lg3o_gs1_%=
Lg3o_slowb1_%=:
s_cmp_eq_u32 s54,1
s_cselect_b32 s68,s50,s49
s_lshr_b32 s68,s68,5
s_add_u32 m0,s78,8192
s_nop 0
buffer_load_dword v6,s[36:39],s68 offen offset:0 sc1 lds
s_mul_i32 s73,s48,7
s_lshr_b32 s73,s73,1
v_mul_f32 v96,s75,v92
v_fma_f32 v93,v93,s75,v12
v_fma_f32 v93,-v96,v96,v93
v_max_f32 v93,v93,v12
v_rsq_f32 v92,v93
s_nop 0
v_mul_f32 v97,s76,v92
v_mul_f32 v98,v92,v92
v_mul_f32 v99,v93,v92
v_mul_f32 v98,s77,v98
v_mul_f32 v98,v98,v97
v_fma_f32 v24,-v96,v16,v24
v_fma_f32 v25,-v96,v17,v25
v_fma_f32 v26,-v96,v18,v26
v_fma_f32 v27,-v96,v19,v27
v_mul_f32 v72,v24,v24
v_mul_f32 v73,v25,v25
v_mul_f32 v74,v26,v26
v_mul_f32 v75,v27,v27
v_fma_f32 v72,v98,v72,v97
v_fma_f32 v73,v98,v73,v97
v_fma_f32 v74,v98,v74,v97
v_fma_f32 v75,v98,v75,v97
v_mul_f32 v72,v24,v72
v_mul_f32 v73,v25,v73
v_mul_f32 v74,v26,v74
v_mul_f32 v75,v27,v75
v_exp_f32 v72,v72
v_exp_f32 v73,v73
v_exp_f32 v74,v74
v_exp_f32 v75,v75
v_fma_f32 v72,v72,v99,v99
v_fma_f32 v73,v73,v99,v99
v_fma_f32 v74,v74,v99,v99
v_fma_f32 v75,v75,v99,v99
v_rcp_f32 v72,v72
v_rcp_f32 v73,v73
v_rcp_f32 v74,v74
v_rcp_f32 v75,v75
v_mul_f32 v24,v24,v72
s_add_u32 m0,s78,8448
s_nop 0
buffer_load_dword v6,s[36:39],s68 offen offset:0 sc1 lds
v_mul_f32 v25,v25,v73
v_mul_f32 v26,v26,v74
v_mul_f32 v27,v27,v75
v_cvt_pk_bf16_f32 v80,v24,v25
v_cvt_pk_bf16_f32 v81,v26,v27
v_fma_f32 v28,-v96,v20,v28
v_fma_f32 v29,-v96,v21,v29
v_fma_f32 v30,-v96,v22,v30
v_fma_f32 v31,-v96,v23,v31
v_mul_f32 v76,v28,v28
v_mul_f32 v77,v29,v29
v_mul_f32 v78,v30,v30
v_mul_f32 v79,v31,v31
v_fma_f32 v76,v98,v76,v97
v_fma_f32 v77,v98,v77,v97
v_fma_f32 v78,v98,v78,v97
v_fma_f32 v79,v98,v79,v97
v_mul_f32 v76,v28,v76
v_mul_f32 v77,v29,v77
v_mul_f32 v78,v30,v78
v_mul_f32 v79,v31,v79
v_exp_f32 v76,v76
v_exp_f32 v77,v77
v_exp_f32 v78,v78
v_exp_f32 v79,v79
v_fma_f32 v76,v76,v99,v99
v_fma_f32 v77,v77,v99,v99
v_fma_f32 v78,v78,v99,v99
v_fma_f32 v79,v79,v99,v99
v_rcp_f32 v76,v76
v_rcp_f32 v77,v77
v_rcp_f32 v78,v78
v_rcp_f32 v79,v79
v_mul_f32 v28,v28,v76
v_mul_f32 v29,v29,v77
v_mul_f32 v30,v30,v78
v_mul_f32 v31,v31,v79
v_cvt_pk_bf16_f32 v82,v28,v29
v_cvt_pk_bf16_f32 v83,v30,v31
buffer_store_dwordx4 v[80:83],v3,s[36:39],s73 offen offset:0 sc0 sc1
s_cmp_ge_u32 s49,s59
s_cbranch_scc1 Lg3o_done1_%=
s_add_u32 m0,s78,16384
s_nop 0
buffer_load_dword v6,s[36:39],s68 offen offset:0 sc1 lds
s_waitcnt vmcnt(3)
s_barrier
ds_read_b32 v13,v7 offset:8192
s_mul_i32 s70,s47,5
s_lshr_b32 s70,s70,6
buffer_store_dword v5,v4,s[36:39],s70 offen offset:0 sc0 sc1
s_mov_b32 s47,s59
s_mov_b32 s69,18432
s_waitcnt lgkmcnt(0)
v_cmp_eq_u32 vcc,s60,v13
s_nop 0
s_cmp_eq_u32 vcc_lo,-1
s_cbranch_scc1 Lg3o_go1_%=
s_add_u32 m0,s78,16640
s_nop 0
buffer_load_dword v6,s[36:39],s68 offen offset:0 sc1 lds
s_waitcnt vmcnt(4)
s_barrier
ds_read_b32 v13,v7 offset:8448
s_mov_b32 s69,18688
s_waitcnt lgkmcnt(0)
v_cmp_eq_u32 vcc,s60,v13
s_nop 0
s_cmp_eq_u32 vcc_lo,-1
s_cbranch_scc1 Lg3o_go1_%=
s_add_u32 m0,s78,8192
s_nop 0
buffer_load_dword v6,s[36:39],s68 offen offset:0 sc1 lds
s_waitcnt vmcnt(3)
s_barrier
ds_read_b32 v13,v7 offset:16384
s_mul_i32 s70,s48,5
s_lshr_b32 s70,s70,6
buffer_store_dword v5,v4,s[36:39],s70 offen offset:0 sc0 sc1
s_mov_b32 s48,s59
s_mov_b32 s69,10240
s_waitcnt lgkmcnt(0)
v_cmp_eq_u32 vcc,s60,v13
s_nop 0
s_cmp_eq_u32 vcc_lo,-1
s_cbranch_scc1 Lg3o_go1_%=
s_add_u32 m0,s78,8448
s_nop 0
buffer_load_dword v6,s[36:39],s68 offen offset:0 sc1 lds
s_waitcnt vmcnt(3)
s_barrier
ds_read_b32 v13,v7 offset:16640
s_mov_b32 s69,10496
s_waitcnt lgkmcnt(0)
v_cmp_eq_u32 vcc,s60,v13
s_nop 0
s_cmp_eq_u32 vcc_lo,-1
s_cbranch_scc1 Lg3o_go1_%=
Lg3o_poll1_%=:
s_add_u32 m0,s78,16384
s_nop 0
buffer_load_dword v6,s[36:39],s68 offen offset:0 sc1 lds
s_waitcnt vmcnt(2)
s_barrier
ds_read_b32 v13,v7 offset:8192
s_mov_b32 s69,18432
s_waitcnt lgkmcnt(0)
v_cmp_eq_u32 vcc,s60,v13
s_nop 0
s_cmp_eq_u32 vcc_lo,-1)"
R"(
s_cbranch_scc1 Lg3o_go1_%=
s_sleep 4
s_add_u32 m0,s78,16640
s_nop 0
buffer_load_dword v6,s[36:39],s68 offen offset:0 sc1 lds
s_waitcnt vmcnt(2)
s_barrier
ds_read_b32 v13,v7 offset:8448
s_mov_b32 s69,18688
s_waitcnt lgkmcnt(0)
v_cmp_eq_u32 vcc,s60,v13
s_nop 0
s_cmp_eq_u32 vcc_lo,-1
s_cbranch_scc1 Lg3o_go1_%=
s_sleep 4
s_add_u32 m0,s78,8192
s_nop 0
buffer_load_dword v6,s[36:39],s68 offen offset:0 sc1 lds
s_waitcnt vmcnt(2)
s_barrier
ds_read_b32 v13,v7 offset:16384
s_mov_b32 s69,10240
s_waitcnt lgkmcnt(0)
v_cmp_eq_u32 vcc,s60,v13
s_nop 0
s_cmp_eq_u32 vcc_lo,-1
s_cbranch_scc1 Lg3o_go1_%=
s_sleep 4
s_add_u32 m0,s78,8448
s_nop 0
buffer_load_dword v6,s[36:39],s68 offen offset:0 sc1 lds
s_waitcnt vmcnt(2)
s_barrier
ds_read_b32 v13,v7 offset:16640
s_mov_b32 s69,10496
s_waitcnt lgkmcnt(0)
v_cmp_eq_u32 vcc,s60,v13
s_nop 0
s_cmp_eq_u32 vcc_lo,-1
s_cbranch_scc1 Lg3o_go1_%=
s_sleep 4
s_add_u32 s79,s79,1
s_cmp_lt_u32 s79,20000
s_cbranch_scc1 Lg3o_poll1_%=
Lg3o_go1_%=:
s_xor_b32 s62,s64,65536
s_mov_b32 m0,s62
s_nop 0
buffer_load_dwordx4 v0,s[36:39],s49 offen offset:0 sc1 lds
buffer_load_dwordx4 v0,s[36:39],s49 offen offset:1024 sc1 lds
buffer_load_dwordx4 v0,s[36:39],s49 offen offset:2048 sc1 lds
buffer_load_dwordx4 v0,s[36:39],s49 offen offset:3072 sc1 lds
s_add_u32 m0,s62,16384
s_nop 0
buffer_load_dwordx4 v1,s[36:39],s49 offen offset:0 sc1 lds
buffer_load_dwordx4 v1,s[36:39],s49 offen offset:1024 sc1 lds
buffer_load_dwordx4 v1,s[36:39],s49 offen offset:2048 sc1 lds
buffer_load_dwordx4 v1,s[36:39],s49 offen offset:3072 sc1 lds
s_lshr_b32 s72,s49,5
s_lshr_b32 s71,s62,2
s_add_u32 m0,s71,s88
s_nop 0
buffer_load_dword v9,s[36:39],s72 offen offset:0 sc1 lds
s_cmp_eq_u32 vcc_hi,-1
s_cselect_b32 s67,s50,s59
s_xor_b32 s62,s63,65536
s_cmp_ge_u32 s67,s59
s_cselect_b32 s68,s50,s51
s_lshr_b32 s68,s68,5
s_add_u32 m0,s78,256
s_nop 0
buffer_load_dword v6,s[36:39],s68 offen offset:0 sc1 lds
s_mov_b32 m0,s62
s_nop 0
buffer_load_dwordx4 v0,s[36:39],s67 offen offset:0 sc1 lds
buffer_load_dwordx4 v0,s[36:39],s67 offen offset:1024 sc1 lds
buffer_load_dwordx4 v0,s[36:39],s67 offen offset:2048 sc1 lds
buffer_load_dwordx4 v0,s[36:39],s67 offen offset:3072 sc1 lds
s_waitcnt vmcnt(10)
s_barrier
ds_read_b128 v[40:43],v2 offset:32768
ds_read_b128 v[44:47],v2 offset:33792
ds_read_b128 v[48:51],v2 offset:34816
ds_read_b128 v[52:55],v2 offset:35840
ds_read_b128 v[56:59],v2 offset:36864
ds_read_b128 v[60:63],v2 offset:37888
ds_read_b128 v[64:67],v2 offset:38912
s_mul_i32 s70,s47,5
s_lshr_b32 s70,s70,6
buffer_store_dword v5,v4,s[36:39],s70 offen offset:0 sc0 sc1
s_mov_b32 s47,s59
s_mul_i32 s70,s48,5
s_lshr_b32 s70,s70,6
buffer_store_dword v5,v4,s[36:39],s70 offen offset:0 sc0 sc1
s_mov_b32 s48,s59
s_mul_i32 s70,s59,5
s_lshr_b32 s70,s70,6
buffer_store_dword v5,v4,s[36:39],s70 offen offset:0 sc0 sc1
s_waitcnt lgkmcnt(3)
s_setpc_b64 s[92:93]
Lg3o_gs1_%=:
s_cmp_eq_u32 s54,1
s_cselect_b32 s68,s50,s49
s_lshr_b32 s68,s68,5
s_mul_i32 s73,s48,7
s_lshr_b32 s73,s73,1
s_add_u32 m0,s78,8192
s_nop 0
buffer_load_dword v6,s[36:39],s68 offen offset:0 sc1 lds
v_mul_f32 v96,s75,v92
v_fma_f32 v93,v93,s75,v12
v_fma_f32 v93,-v96,v96,v93
v_max_f32 v93,v93,v12
v_rsq_f32 v92,v93
s_nop 0
v_mul_f32 v97,s76,v92
v_mul_f32 v98,v92,v92
v_mul_f32 v99,v93,v92
v_mul_f32 v98,s77,v98
v_mul_f32 v98,v98,v97
v_fma_f32 v24,-v96,v16,v24
v_fma_f32 v25,-v96,v17,v25
v_fma_f32 v26,-v96,v18,v26
s_add_u32 m0,s62,16384
s_nop 0
buffer_load_dwordx4 v1,s[36:39],s67 offen offset:0 sc1 lds
v_fma_f32 v27,-v96,v19,v27
v_mul_f32 v72,v24,v24
v_mul_f32 v73,v25,v25
v_mul_f32 v74,v26,v26
v_mul_f32 v75,v27,v27
v_fma_f32 v72,v98,v72,v97
v_fma_f32 v73,v98,v73,v97
v_fma_f32 v74,v98,v74,v97
v_fma_f32 v75,v98,v75,v97
v_mul_f32 v72,v24,v72
v_mul_f32 v73,v25,v73
v_mul_f32 v74,v26,v74
v_mul_f32 v75,v27,v75
buffer_load_dwordx4 v1,s[36:39],s67 offen offset:1024 sc1 lds
v_exp_f32 v72,v72
v_exp_f32 v73,v73
v_exp_f32 v74,v74
v_exp_f32 v75,v75
v_fma_f32 v72,v72,v99,v99
v_fma_f32 v73,v73,v99,v99
v_fma_f32 v74,v74,v99,v99
v_fma_f32 v75,v75,v99,v99
v_rcp_f32 v72,v72
v_rcp_f32 v73,v73
v_rcp_f32 v74,v74
v_rcp_f32 v75,v75
v_mul_f32 v24,v24,v72
buffer_load_dwordx4 v1,s[36:39],s67 offen offset:2048 sc1 lds
v_mul_f32 v25,v25,v73
v_mul_f32 v26,v26,v74
v_mul_f32 v27,v27,v75
v_cvt_pk_bf16_f32 v80,v24,v25
v_cvt_pk_bf16_f32 v81,v26,v27
v_fma_f32 v28,-v96,v20,v28
v_fma_f32 v29,-v96,v21,v29
v_fma_f32 v30,-v96,v22,v30
v_fma_f32 v31,-v96,v23,v31
v_mul_f32 v76,v28,v28
v_mul_f32 v77,v29,v29
v_mul_f32 v78,v30,v30
v_mul_f32 v79,v31,v31
buffer_load_dwordx4 v1,s[36:39],s67 offen offset:3072 sc1 lds
v_fma_f32 v76,v98,v76,v97
v_fma_f32 v77,v98,v77,v97
v_fma_f32 v78,v98,v78,v97
v_fma_f32 v79,v98,v79,v97
v_mul_f32 v76,v28,v76
v_mul_f32 v77,v29,v77
v_mul_f32 v78,v30,v78
v_mul_f32 v79,v31,v79
v_exp_f32 v76,v76
v_exp_f32 v77,v77
v_exp_f32 v78,v78
v_exp_f32 v79,v79
v_fma_f32 v76,v76,v99,v99
s_lshr_b32 s72,s67,5
s_lshr_b32 s71,s62,2
s_add_u32 m0,s71,s88
s_nop 0
buffer_load_dword v9,s[36:39],s72 offen offset:0 sc1 lds
v_fma_f32 v77,v77,v99,v99
v_fma_f32 v78,v78,v99,v99
v_fma_f32 v79,v79,v99,v99
v_rcp_f32 v76,v76
v_rcp_f32 v77,v77
v_rcp_f32 v78,v78
v_rcp_f32 v79,v79
v_mul_f32 v28,v28,v76
v_mul_f32 v29,v29,v77
v_mul_f32 v30,v30,v78
v_mul_f32 v31,v31,v79
v_cvt_pk_bf16_f32 v82,v28,v29
v_cvt_pk_bf16_f32 v83,v30,v31
buffer_store_dwordx4 v[80:83],v3,s[36:39],s73 offen offset:0 sc0 sc1)"
R"(
s_waitcnt vmcnt(6)
s_barrier
ds_read_b32 v13,v7 offset:8192
ds_read_b128 v[40:43],v2 offset:32768
ds_read_b128 v[44:47],v2 offset:33792
ds_read_b128 v[48:51],v2 offset:34816
ds_read_b128 v[52:55],v2 offset:35840
ds_read_b128 v[56:59],v2 offset:36864
ds_read_b128 v[60:63],v2 offset:37888
ds_read_b128 v[64:67],v2 offset:38912
s_mul_i32 s70,s47,5
s_lshr_b32 s70,s70,6
buffer_store_dword v5,v4,s[36:39],s70 offen offset:0 sc0 sc1
s_waitcnt lgkmcnt(7)
v_cmp_eq_u32 vcc,s60,v13
s_nop 0
s_cmp_eq_u32 vcc_hi,-1
s_cselect_b32 s67,s50,s59
s_xor_b32 s62,s63,65536
s_cmp_ge_u32 s67,s59
s_cselect_b32 s68,s50,s51
s_lshr_b32 s68,s68,5
s_add_u32 m0,s78,256
s_nop 0
buffer_load_dword v6,s[36:39],s68 offen offset:0 sc1 lds
s_mov_b32 m0,s62
s_nop 0
buffer_load_dwordx4 v0,s[36:39],s67 offen offset:0 sc1 lds
buffer_load_dwordx4 v0,s[36:39],s67 offen offset:1024 sc1 lds
buffer_load_dwordx4 v0,s[36:39],s67 offen offset:2048 sc1 lds
buffer_load_dwordx4 v0,s[36:39],s67 offen offset:3072 sc1 lds
s_mov_b32 s47,s48
s_mov_b32 s48,s59
s_mul_i32 s70,s59,5
s_lshr_b32 s70,s70,6
buffer_store_dword v5,v4,s[36:39],s70 offen offset:0 sc0 sc1
s_waitcnt lgkmcnt(3)
s_setpc_b64 s[92:93]
Lg3o_done1_%=:
s_waitcnt vmcnt(0)
s_barrier
s_mul_i32 s70,s47,5
s_lshr_b32 s70,s70,6
buffer_store_dword v5,v4,s[36:39],s70 offen offset:0 sc0 sc1
s_mul_i32 s70,s48,5
s_lshr_b32 s70,s70,6
buffer_store_dword v5,v4,s[36:39],s70 offen offset:0 sc0 sc1
s_branch Lg3o_done_%=
Lg3o_done_%=:
s_waitcnt vmcnt(0) lgkmcnt(0)
s_barrier
s_endpgm
Lcode_g3o_e:)"
  :: "s"(ws), "s"(wave), "s"(sStep), "s"(sX1), "s"(sX2), "s"(sU0), "s"(sWw), "s"(sBuf), "v"(vSrc), "v"(vA), "v"(vOut), "v"(vFlag), "v"(vPollOff), "v"(vSnapRd), "v"(vX), "v"(vAuxS), "v"(vStW), "v"(vStR), "v"(vWd0), "v"(vWd1), "v"(vWr0), "v"(vWr1), "v"(vWr2), "s"(w3) : "v0", "v1", "v2", "v3", "v4", "v5", "v6", "v7", "v8", "v9", "v10", "v11", "v12", "v13", "v14", "v15", "v16", "v17", "v18", "v19", "v20", "v21", "v22", "v23", "v24", "v25", "v26", "v27", "v28", "v29", "v30", "v31", "v32", "v33", "v34", "v35", "v36", "v37", "v38", "v39", "v40", "v41", "v42", "v43", "v44", "v45", "v46", "v47", "v48", "v49", "v50", "v51", "v52", "v53", "v54", "v55", "v56", "v57", "v58", "v59", "v60", "v61", "v62", "v63", "v64", "v65", "v66", "v67", "v68", "v69", "v70", "v71", "v72", "v73", "v74", "v75", "v76", "v77", "v78", "v79", "v80", "v81", "v82", "v83", "v84", "v85", "v86", "v87", "v88", "v89", "v90", "v91", "v92", "v93", "v94", "v95", "v96", "v97", "v98", "v99", "v100", "v101", "v102", "v103", "v104", "v105", "v106", "v107", "v108", "v109", "v110", "v111", "v112", "v113", "v114", "v115", "v116", "v117", "v118", "v119", "v120", "v121", "v122", "v123", "v124", "v125", "v126", "v127", "v128", "v129", "v130", "v131", "v132", "v133", "v134", "v135", "v136", "v137", "v138", "v139", "v140", "v141", "v142", "v143", "v144", "v145", "v146", "v147", "v148", "v149", "v150", "v151", "v152", "v153", "v154", "v155", "v156", "v157", "v158", "v159", "v160", "v161", "v162", "v163", "v164", "v165", "v166", "v167", "v168", "v169", "v170", "v171", "v172", "v173", "v174", "v175", "v176", "v177", "v178", "v179", "v180", "v181", "v182", "v183", "v184", "v185", "v186", "v187", "v188", "v189", "v190", "v191", "v192", "v193", "v194", "v195", "v196", "v197", "v198", "v199", "v200", "v201", "v202", "v203", "v204", "v205", "v206", "v207", "v208", "v209", "v210", "v211", "v212", "v213", "v214", "v215", "v216", "v217", "v218", "v219", "v220", "v221", "v222", "v223", "v224", "v225", "v226", "v227", "v228", "v229", "v230", "v231", "v255", "a0", "a1", "a2", "a3", "a4", "a5", "a6", "a7", "a8", "a9", "a10", "a11", "a12", "a13", "a14", "a15", "a16", "a17", "a18", "a19", "a20", "a21", "a22", "a23", "a24", "a25", "a26", "a27", "a28", "a29", "a30", "a31", "a32", "a33", "a34", "a35", "a36", "a37", "a38", "a39", "a40", "a41", "a42", "a43", "a44", "a45", "a46", "a47", "a48", "a49", "a50", "a51", "a52", "a53", "a54", "a55", "a56", "a57", "a58", "a59", "a60", "a61", "a62", "a63", "a64", "a65", "a66", "a67", "a68", "a69", "a70", "a71", "a72", "a73", "a74", "a75", "a76", "a77", "a78", "a79", "a80", "a81", "a82", "a83", "a84", "a85", "a86", "a87", "a88", "a89", "a90", "a91", "a92", "a93", "a94", "a95", "a96", "a97", "a98", "a99", "a100", "a101", "a102", "a103", "a104", "a105", "a106", "a107", "a108", "a109", "a110", "a111", "a112", "a113", "a114", "a115", "a116", "a117", "a118", "a119", "a120", "a121", "a122", "a123", "a124", "a125", "a126", "a127", "a128", "a129", "a130", "a131", "a132", "a133", "a134", "a135", "a136", "a137", "a138", "a139", "a140", "a141", "a142", "a143", "a144", "a145", "a146", "a147", "a148", "a149", "a150", "a151", "a152", "a153", "a154", "a155", "a156", "a157", "a158", "a159", "a160", "a161", "a162", "a163", "a164", "a165", "a166", "a167", "a168", "a169", "a170", "a171", "a172", "a173", "a174", "a175", "a176", "a177", "a178", "a179", "a180", "a181", "a182", "a183", "a184", "a185", "a186", "a187", "a188", "a189", "a190", "a191", "a192", "a193", "a194", "a195", "a196", "a197", "a198", "a199", "a200", "a201", "a202", "a203", "a204", "a205", "a206", "a207", "a208", "a209", "a210", "a211", "a212", "a213", "a214", "a215", "a216", "a217", "a218", "a219", "a220", "a221", "a222", "a223", "a224", "a225", "a226", "a227", "a228", "a229", "a230", "a231", "a232", "a233", "a234", "a235", "a236", "a237", "a238", "a239", "a240", "a241", "a242", "a243", "a244", "a245", "a246", "a247", "a248", "a249", "a250", "a251", "a252", "a253", "a254", "a255", "s36", "s37", "s38", "s39", "s40", "s41", "s42", "s43", "s44", "s45", "s46", "s47", "s48", "s49", "s50", "s51", "s52", "s53", "s54", "s55", "s56", "s57", "s58", "s59", "s60", "s61", "s62", "s63", "s64", "s65", "s66", "s67", "s68", "s69", "s70", "s71", "s72", "s73", "s74", "s75", "s76", "s77", "s78", "s79", "s80", "s81", "s82", "s83", "s84", "s85", "s86", "s87", "s88", "s89", "s90", "s91", "s92", "s93", "s94", "s95", "s96", "s97", "s98", "s99", "vcc", "scc", "memory"); }
    __builtin_unreachable();
  } else if (kind == 4) {
    COMMON(kh == 1 ? 6 : 5, OFF_FG + ((14 * kh) / 3) * 128, OFF_FP + (kh * 4 + slice) * 128, 65536, 256, 2048, 5);
    const unsigned vSrc = OFF_G + kh * 28672 + wave * 4096 + vA, vSrc2 = OFF_G + kh * 28672 + 16384 + wave * 3072 + vA;
    const unsigned vOut = (kh == 0 ? OFF_P0 : kh == 1 ? OFF_P1 : kh == 2 ? OFF_P2 : OFF_P3) + (l15 >> 3) * 16384 + (slice * 8 + wave * 2 + (kh >> 1)) * 512 + lg * 128 + (l15 & 7) * 16;
    const unsigned vOut2 = vOut ^ 512;
    const unsigned vAuxS = ((kh & 1) ? OFF_XP : OFF_X1) + (slice * 8 + wave * 2 + 1 - (kh >> 1)) * 1024 + vA;
    const unsigned sWw = __builtin_amdgcn_readfirstlane(kh * 896 * 2048 + slice * 512 + wave * 128);
    asm volatile(R"(
Lcode_g4_b:
s_mov_b64 s[36:37],%0
s_mov_b32 s38,122727552
s_mov_b32 s39,0x00020000
s_mov_b32 s54,%1
s_mov_b32 s55,%2
s_mov_b32 s56,%3
s_mov_b32 s58,%4
s_mov_b32 s83,%5
s_mov_b32 s82,%6
s_mov_b32 s84,%7
v_mov_b32 v0,%8
v_mov_b32 v1,%9
v_mov_b32 v2,%10
v_mov_b32 v3,%11
v_mov_b32 v4,%12
v_mov_b32 v5,%13
v_mov_b32 v7,%14
v_mov_b32 v8,%15
v_mov_b32 v9,%16
v_mov_b32 v12,%17
v_mov_b32 v13,%18
v_mov_b32 v14,%19
v_mov_b32 v15,%20
v_mov_b32 v16,%21
s_mov_b64 s[40:41],%22
s_mov_b32 s42,7340032
s_mov_b32 s43,0x00020000
s_lshl_b32 s88,s54,12
s_mul_i32 s68,s54,9216
s_add_u32 s68,s68,122880
v_add_u32 v10,s68,v2
s_mov_b32 s69,122727424
buffer_load_dword v6,off,s[36:39],s69 sc1
s_add_u32 m0,s88,0
s_add_u32 s61,s82,0
buffer_load_dwordx4 v12,s[40:43],s61 offen offset:0  lds
s_add_u32 m0,s88,1024
s_nop 0
buffer_load_dwordx4 v13,s[40:43],s61 offen offset:0  lds
s_add_u32 m0,s88,2048
s_add_u32 s61,s82,32768
buffer_load_dwordx4 v12,s[40:43],s61 offen offset:0  lds
s_add_u32 m0,s88,3072
s_nop 0
buffer_load_dwordx4 v13,s[40:43],s61 offen offset:0  lds
s_add_u32 m0,s88,16384
s_add_u32 s61,s82,65536
buffer_load_dwordx4 v12,s[40:43],s61 offen offset:0  lds
s_add_u32 m0,s88,17408
s_nop 0
buffer_load_dwordx4 v13,s[40:43],s61 offen offset:0  lds
s_add_u32 m0,s88,18432
s_add_u32 s61,s82,98304
buffer_load_dwordx4 v12,s[40:43],s61 offen offset:0  lds
s_add_u32 m0,s88,19456
s_nop 0
buffer_load_dwordx4 v13,s[40:43],s61 offen offset:0  lds
s_add_u32 m0,s88,32768
s_add_u32 s61,s82,131072
buffer_load_dwordx4 v12,s[40:43],s61 offen offset:0  lds
s_add_u32 m0,s88,33792
s_nop 0
buffer_load_dwordx4 v13,s[40:43],s61 offen offset:0  lds
s_add_u32 m0,s88,34816
s_add_u32 s61,s82,163840
buffer_load_dwordx4 v12,s[40:43],s61 offen offset:0  lds
s_add_u32 m0,s88,35840
s_nop 0
buffer_load_dwordx4 v13,s[40:43],s61 offen offset:0  lds
s_add_u32 m0,s88,49152
s_add_u32 s61,s82,196608
buffer_load_dwordx4 v12,s[40:43],s61 offen offset:0  lds
s_add_u32 m0,s88,50176
s_nop 0
buffer_load_dwordx4 v13,s[40:43],s61 offen offset:0  lds
s_add_u32 m0,s88,51200
s_add_u32 s61,s82,229376
buffer_load_dwordx4 v12,s[40:43],s61 offen offset:0  lds
s_add_u32 m0,s88,52224
s_nop 0
buffer_load_dwordx4 v13,s[40:43],s61 offen offset:0  lds
s_add_u32 m0,s88,65536
s_add_u32 s61,s82,262144
buffer_load_dwordx4 v12,s[40:43],s61 offen offset:0  lds
s_add_u32 m0,s88,66560
s_nop 0
buffer_load_dwordx4 v13,s[40:43],s61 offen offset:0  lds
s_add_u32 m0,s88,67584
s_add_u32 s61,s82,294912
buffer_load_dwordx4 v12,s[40:43],s61 offen offset:0  lds
s_add_u32 m0,s88,68608
s_nop 0
buffer_load_dwordx4 v13,s[40:43],s61 offen offset:0  lds
v_add_u32 v28,49152,v14
v_add_u32 v30,49152,v16
v_add_u32 v32,98304,v14
v_add_u32 v34,98304,v16
s_waitcnt vmcnt(16)
s_add_u32 m0,s88,81920
s_add_u32 s61,s82,327680
buffer_load_dwordx4 v12,s[40:43],s61 offen offset:0  lds
s_add_u32 m0,s88,82944
ds_read_b64_tr_b16 a[0:1],v14 offset:0
buffer_load_dwordx4 v13,s[40:43],s61 offen offset:0  lds
s_add_u32 m0,s88,83968
s_add_u32 s61,s82,360448
buffer_load_dwordx4 v12,s[40:43],s61 offen offset:0  lds
s_add_u32 m0,s88,84992
ds_read_b64_tr_b16 a[2:3],v14 offset:512
buffer_load_dwordx4 v13,s[40:43],s61 offen offset:0  lds
ds_read_b64_tr_b16 a[4:5],v14 offset:8
ds_read_b64_tr_b16 a[6:7],v14 offset:520
ds_read_b64_tr_b16 a[8:9],v16 offset:0
ds_read_b64_tr_b16 a[10:11],v16 offset:512
ds_read_b64_tr_b16 a[12:13],v16 offset:8
ds_read_b64_tr_b16 a[14:15],v16 offset:520
s_waitcnt vmcnt(16)
s_add_u32 m0,s88,98304
s_add_u32 s61,s82,393216
buffer_load_dwordx4 v12,s[40:43],s61 offen offset:0  lds
s_add_u32 m0,s88,99328
ds_read_b64_tr_b16 a[16:17],v14 offset:16384
buffer_load_dwordx4 v13,s[40:43],s61 offen offset:0  lds
s_add_u32 m0,s88,100352
s_add_u32 s61,s82,425984
buffer_load_dwordx4 v12,s[40:43],s61 offen offset:0  lds
s_add_u32 m0,s88,101376
ds_read_b64_tr_b16 a[18:19],v14 offset:16896
buffer_load_dwordx4 v13,s[40:43],s61 offen offset:0  lds
ds_read_b64_tr_b16 a[20:21],v14 offset:16392
ds_read_b64_tr_b16 a[22:23],v14 offset:16904
ds_read_b64_tr_b16 a[24:25],v16 offset:16384
ds_read_b64_tr_b16 a[26:27],v16 offset:16896
ds_read_b64_tr_b16 a[28:29],v16 offset:16392
ds_read_b64_tr_b16 a[30:31],v16 offset:16904
s_waitcnt lgkmcnt(8)
s_waitcnt vmcnt(16)
s_add_u32 m0,s88,0
s_add_u32 s61,s82,458752
buffer_load_dwordx4 v12,s[40:43],s61 offen offset:0  lds
s_add_u32 m0,s88,1024
ds_read_b64_tr_b16 a[32:33],v14 offset:32768
buffer_load_dwordx4 v13,s[40:43],s61 offen offset:0  lds
s_add_u32 m0,s88,2048
s_add_u32 s61,s82,491520
buffer_load_dwordx4 v12,s[40:43],s61 offen offset:0  lds
s_add_u32 m0,s88,3072
ds_read_b64_tr_b16 a[34:35],v14 offset:33280
buffer_load_dwordx4 v13,s[40:43],s61 offen offset:0  lds
ds_read_b64_tr_b16 a[36:37],v14 offset:32776
ds_read_b64_tr_b16 a[38:39],v14 offset:33288
ds_read_b64_tr_b16 a[40:41],v16 offset:32768
ds_read_b64_tr_b16 a[42:43],v16 offset:33280
ds_read_b64_tr_b16 a[44:45],v16 offset:32776
ds_read_b64_tr_b16 a[46:47],v16 offset:33288
s_waitcnt lgkmcnt(8)
s_waitcnt vmcnt(16)
s_add_u32 m0,s88,16384
s_add_u32 s61,s82,524288
buffer_load_dwordx4 v12,s[40:43],s61 offen offset:0  lds
s_add_u32 m0,s88,17408
ds_read_b64_tr_b16 a[48:49],v28 offset:0
buffer_load_dwordx4 v13,s[40:43],s61 offen offset:0  lds
s_add_u32 m0,s88,18432
s_add_u32 s61,s82,557056
buffer_load_dwordx4 v12,s[40:43],s61 offen offset:0  lds
s_add_u32 m0,s88,19456
ds_read_b64_tr_b16 a[50:51],v28 offset:512
buffer_load_dwordx4 v13,s[40:43],s61 offen offset:0  lds
ds_read_b64_tr_b16 a[52:53],v28 offset:8
ds_read_b64_tr_b16 a[54:55],v28 offset:520
ds_read_b64_tr_b16 a[56:57],v30 offset:0
ds_read_b64_tr_b16 a[58:59],v30 offset:512
ds_read_b64_tr_b16 a[60:61],v30 offset:8
ds_read_b64_tr_b16 a[62:63],v30 offset:520
s_waitcnt lgkmcnt(8)
s_waitcnt vmcnt(16)
s_add_u32 m0,s88,32768
s_add_u32 s61,s82,589824
buffer_load_dwordx4 v12,s[40:43],s61 offen offset:0  lds
s_add_u32 m0,s88,33792
ds_read_b64_tr_b16 a[64:65],v28 offset:16384
buffer_load_dwordx4 v13,s[40:43],s61 offen offset:0  lds
s_add_u32 m0,s88,34816
s_add_u32 s61,s82,622592
buffer_load_dwordx4 v12,s[40:43],s61 offen offset:0  lds
s_add_u32 m0,s88,35840
ds_read_b64_tr_b16 a[66:67],v28 offset:16896
buffer_load_dwordx4 v13,s[40:43],s61 offen offset:0  lds
ds_read_b64_tr_b16 a[68:69],v28 offset:16392
ds_read_b64_tr_b16 a[70:71],v28 offset:16904
ds_read_b64_tr_b16 a[72:73],v30 offset:16384
ds_read_b64_tr_b16 a[74:75],v30 offset:16896
ds_read_b64_tr_b16 a[76:77],v30 offset:16392
ds_read_b64_tr_b16 a[78:79],v30 offset:16904
s_waitcnt lgkmcnt(8)
s_waitcnt vmcnt(16)
s_add_u32 m0,s88,49152
s_add_u32 s61,s82,655360)"
R"(
buffer_load_dwordx4 v12,s[40:43],s61 offen offset:0  lds
s_add_u32 m0,s88,50176
ds_read_b64_tr_b16 a[80:81],v28 offset:32768
buffer_load_dwordx4 v13,s[40:43],s61 offen offset:0  lds
s_add_u32 m0,s88,51200
s_add_u32 s61,s82,688128
buffer_load_dwordx4 v12,s[40:43],s61 offen offset:0  lds
s_add_u32 m0,s88,52224
ds_read_b64_tr_b16 a[82:83],v28 offset:33280
buffer_load_dwordx4 v13,s[40:43],s61 offen offset:0  lds
ds_read_b64_tr_b16 a[84:85],v28 offset:32776
ds_read_b64_tr_b16 a[86:87],v28 offset:33288
ds_read_b64_tr_b16 a[88:89],v30 offset:32768
ds_read_b64_tr_b16 a[90:91],v30 offset:33280
ds_read_b64_tr_b16 a[92:93],v30 offset:32776
ds_read_b64_tr_b16 a[94:95],v30 offset:33288
s_waitcnt lgkmcnt(8)
s_waitcnt vmcnt(16)
s_add_u32 m0,s88,65536
s_add_u32 s61,s82,720896
buffer_load_dwordx4 v12,s[40:43],s61 offen offset:0  lds
s_add_u32 m0,s88,66560
ds_read_b64_tr_b16 a[96:97],v32 offset:0
buffer_load_dwordx4 v13,s[40:43],s61 offen offset:0  lds
s_add_u32 m0,s88,67584
s_add_u32 s61,s82,753664
buffer_load_dwordx4 v12,s[40:43],s61 offen offset:0  lds
s_add_u32 m0,s88,68608
ds_read_b64_tr_b16 a[98:99],v32 offset:512
buffer_load_dwordx4 v13,s[40:43],s61 offen offset:0  lds
ds_read_b64_tr_b16 a[100:101],v32 offset:8
ds_read_b64_tr_b16 a[102:103],v32 offset:520
ds_read_b64_tr_b16 a[104:105],v34 offset:0
ds_read_b64_tr_b16 a[106:107],v34 offset:512
ds_read_b64_tr_b16 a[108:109],v34 offset:8
ds_read_b64_tr_b16 a[110:111],v34 offset:520
s_waitcnt lgkmcnt(8)
s_waitcnt vmcnt(16)
s_add_u32 m0,s88,81920
s_add_u32 s61,s82,786432
buffer_load_dwordx4 v12,s[40:43],s61 offen offset:0  lds
s_add_u32 m0,s88,82944
ds_read_b64_tr_b16 a[112:113],v14 offset:0
buffer_load_dwordx4 v13,s[40:43],s61 offen offset:0  lds
s_add_u32 m0,s88,83968
s_add_u32 s61,s82,819200
buffer_load_dwordx4 v12,s[40:43],s61 offen offset:0  lds
s_add_u32 m0,s88,84992
ds_read_b64_tr_b16 a[114:115],v14 offset:512
buffer_load_dwordx4 v13,s[40:43],s61 offen offset:0  lds
ds_read_b64_tr_b16 a[116:117],v14 offset:8
ds_read_b64_tr_b16 a[118:119],v14 offset:520
ds_read_b64_tr_b16 a[120:121],v16 offset:0
ds_read_b64_tr_b16 a[122:123],v16 offset:512
ds_read_b64_tr_b16 a[124:125],v16 offset:8
ds_read_b64_tr_b16 a[126:127],v16 offset:520
s_waitcnt lgkmcnt(8)
s_waitcnt vmcnt(16)
s_add_u32 m0,s88,98304
s_add_u32 s61,s82,851968
buffer_load_dwordx4 v12,s[40:43],s61 offen offset:0  lds
s_add_u32 m0,s88,99328
ds_read_b64_tr_b16 a[128:129],v14 offset:16384
buffer_load_dwordx4 v13,s[40:43],s61 offen offset:0  lds
s_add_u32 m0,s88,100352
s_add_u32 s61,s82,884736
buffer_load_dwordx4 v12,s[40:43],s61 offen offset:0  lds
s_add_u32 m0,s88,101376
ds_read_b64_tr_b16 a[130:131],v14 offset:16896
buffer_load_dwordx4 v13,s[40:43],s61 offen offset:0  lds
ds_read_b64_tr_b16 a[132:133],v14 offset:16392
ds_read_b64_tr_b16 a[134:135],v14 offset:16904
ds_read_b64_tr_b16 a[136:137],v16 offset:16384
ds_read_b64_tr_b16 a[138:139],v16 offset:16896
ds_read_b64_tr_b16 a[140:141],v16 offset:16392
ds_read_b64_tr_b16 a[142:143],v16 offset:16904
s_waitcnt lgkmcnt(8)
s_waitcnt vmcnt(16)
s_add_u32 m0,s88,0
s_add_u32 s61,s82,917504
buffer_load_dwordx4 v12,s[40:43],s61 offen offset:0  lds
s_add_u32 m0,s88,1024
ds_read_b64_tr_b16 a[144:145],v14 offset:32768
buffer_load_dwordx4 v13,s[40:43],s61 offen offset:0  lds
s_add_u32 m0,s88,2048
s_add_u32 s61,s82,950272
buffer_load_dwordx4 v12,s[40:43],s61 offen offset:0  lds
s_add_u32 m0,s88,3072
ds_read_b64_tr_b16 a[146:147],v14 offset:33280
buffer_load_dwordx4 v13,s[40:43],s61 offen offset:0  lds
ds_read_b64_tr_b16 a[148:149],v14 offset:32776
ds_read_b64_tr_b16 a[150:151],v14 offset:33288
ds_read_b64_tr_b16 a[152:153],v16 offset:32768
ds_read_b64_tr_b16 a[154:155],v16 offset:33280
ds_read_b64_tr_b16 a[156:157],v16 offset:32776
ds_read_b64_tr_b16 a[158:159],v16 offset:33288
s_waitcnt lgkmcnt(8)
s_waitcnt vmcnt(16)
s_add_u32 m0,s88,16384
s_add_u32 s61,s82,983040
buffer_load_dwordx4 v12,s[40:43],s61 offen offset:0  lds
s_add_u32 m0,s88,17408
ds_read_b64_tr_b16 a[160:161],v28 offset:0
buffer_load_dwordx4 v13,s[40:43],s61 offen offset:0  lds
s_add_u32 m0,s88,18432
s_add_u32 s61,s82,1015808
buffer_load_dwordx4 v12,s[40:43],s61 offen offset:0  lds
s_add_u32 m0,s88,19456
ds_read_b64_tr_b16 a[162:163],v28 offset:512
buffer_load_dwordx4 v13,s[40:43],s61 offen offset:0  lds
ds_read_b64_tr_b16 a[164:165],v28 offset:8
ds_read_b64_tr_b16 a[166:167],v28 offset:520
ds_read_b64_tr_b16 a[168:169],v30 offset:0
ds_read_b64_tr_b16 a[170:171],v30 offset:512
ds_read_b64_tr_b16 a[172:173],v30 offset:8
ds_read_b64_tr_b16 a[174:175],v30 offset:520
s_waitcnt lgkmcnt(8)
s_waitcnt vmcnt(16)
s_add_u32 m0,s88,32768
s_add_u32 s61,s82,1048576
buffer_load_dwordx4 v12,s[40:43],s61 offen offset:0  lds
s_add_u32 m0,s88,33792
ds_read_b64_tr_b16 a[176:177],v28 offset:16384
buffer_load_dwordx4 v13,s[40:43],s61 offen offset:0  lds
s_add_u32 m0,s88,34816
s_add_u32 s61,s82,1081344
buffer_load_dwordx4 v12,s[40:43],s61 offen offset:0  lds
s_add_u32 m0,s88,35840
ds_read_b64_tr_b16 a[178:179],v28 offset:16896
buffer_load_dwordx4 v13,s[40:43],s61 offen offset:0  lds
ds_read_b64_tr_b16 a[180:181],v28 offset:16392
ds_read_b64_tr_b16 a[182:183],v28 offset:16904
ds_read_b64_tr_b16 a[184:185],v30 offset:16384
ds_read_b64_tr_b16 a[186:187],v30 offset:16896
ds_read_b64_tr_b16 a[188:189],v30 offset:16392
ds_read_b64_tr_b16 a[190:191],v30 offset:16904
s_waitcnt lgkmcnt(8)
s_waitcnt vmcnt(16)
s_add_u32 m0,s88,49152
s_add_u32 s61,s82,1114112
buffer_load_dwordx4 v12,s[40:43],s61 offen offset:0  lds
s_add_u32 m0,s88,50176
ds_read_b64_tr_b16 a[192:193],v28 offset:32768
buffer_load_dwordx4 v13,s[40:43],s61 offen offset:0  lds
s_add_u32 m0,s88,51200
s_add_u32 s61,s82,1146880
buffer_load_dwordx4 v12,s[40:43],s61 offen offset:0  lds
s_add_u32 m0,s88,52224
ds_read_b64_tr_b16 a[194:195],v28 offset:33280
buffer_load_dwordx4 v13,s[40:43],s61 offen offset:0  lds
ds_read_b64_tr_b16 a[196:197],v28 offset:32776
ds_read_b64_tr_b16 a[198:199],v28 offset:33288
ds_read_b64_tr_b16 a[200:201],v30 offset:32768
ds_read_b64_tr_b16 a[202:203],v30 offset:33280
ds_read_b64_tr_b16 a[204:205],v30 offset:32776
ds_read_b64_tr_b16 a[206:207],v30 offset:33288
s_waitcnt lgkmcnt(8)
s_waitcnt vmcnt(16)
s_add_u32 m0,s88,65536
s_add_u32 s61,s82,1179648
buffer_load_dwordx4 v12,s[40:43],s61 offen offset:0  lds
s_add_u32 m0,s88,66560
ds_read_b64_tr_b16 a[208:209],v32 offset:0
buffer_load_dwordx4 v13,s[40:43],s61 offen offset:0  lds
s_add_u32 m0,s88,67584
s_add_u32 s61,s82,1212416
buffer_load_dwordx4 v12,s[40:43],s61 offen offset:0  lds
s_add_u32 m0,s88,68608
ds_read_b64_tr_b16 a[210:211],v32 offset:512
buffer_load_dwordx4 v13,s[40:43],s61 offen offset:0  lds
ds_read_b64_tr_b16 a[212:213],v32 offset:8
ds_read_b64_tr_b16 a[214:215],v32 offset:520
ds_read_b64_tr_b16 a[216:217],v34 offset:0
ds_read_b64_tr_b16 a[218:219],v34 offset:512
ds_read_b64_tr_b16 a[220:221],v34 offset:8
ds_read_b64_tr_b16 a[222:223],v34 offset:520
s_waitcnt lgkmcnt(8)
s_waitcnt vmcnt(16)
s_add_u32 m0,s88,81920
s_add_u32 s61,s82,1245184
buffer_load_dwordx4 v12,s[40:43],s61 offen offset:0  lds
s_add_u32 m0,s88,82944
ds_read_b64_tr_b16 a[224:225],v14 offset:0
buffer_load_dwordx4 v13,s[40:43],s61 offen offset:0  lds
s_add_u32 m0,s88,83968
s_add_u32 s61,s82,1277952
buffer_load_dwordx4 v12,s[40:43],s61 offen offset:0  lds
s_add_u32 m0,s88,84992
ds_read_b64_tr_b16 a[226:227],v14 offset:512
buffer_load_dwordx4 v13,s[40:43],s61 offen offset:0  lds
ds_read_b64_tr_b16 a[228:229],v14 offset:8
ds_read_b64_tr_b16 a[230:231],v14 offset:520
ds_read_b64_tr_b16 a[232:233],v16 offset:0
ds_read_b64_tr_b16 a[234:235],v16 offset:512
ds_read_b64_tr_b16 a[236:237],v16 offset:8
ds_read_b64_tr_b16 a[238:239],v16 offset:520
s_waitcnt lgkmcnt(8)
s_waitcnt vmcnt(16)
s_add_u32 m0,s88,98304
s_add_u32 s61,s82,1310720)"
R"(
buffer_load_dwordx4 v12,s[40:43],s61 offen offset:0  lds
s_add_u32 m0,s88,99328
ds_read_b64_tr_b16 a[240:241],v14 offset:16384
buffer_load_dwordx4 v13,s[40:43],s61 offen offset:0  lds
s_add_u32 m0,s88,100352
s_add_u32 s61,s82,1343488
buffer_load_dwordx4 v12,s[40:43],s61 offen offset:0  lds
s_add_u32 m0,s88,101376
ds_read_b64_tr_b16 a[242:243],v14 offset:16896
buffer_load_dwordx4 v13,s[40:43],s61 offen offset:0  lds
ds_read_b64_tr_b16 a[244:245],v14 offset:16392
ds_read_b64_tr_b16 a[246:247],v14 offset:16904
ds_read_b64_tr_b16 a[248:249],v16 offset:16384
ds_read_b64_tr_b16 a[250:251],v16 offset:16896
ds_read_b64_tr_b16 a[252:253],v16 offset:16392
ds_read_b64_tr_b16 a[254:255],v16 offset:16904
s_waitcnt lgkmcnt(8)
s_waitcnt vmcnt(16)
s_add_u32 m0,s88,0
s_add_u32 s61,s82,1376256
buffer_load_dwordx4 v12,s[40:43],s61 offen offset:0  lds
s_add_u32 m0,s88,1024
ds_read_b64_tr_b16 v[100:101],v14 offset:32768
buffer_load_dwordx4 v13,s[40:43],s61 offen offset:0  lds
s_add_u32 m0,s88,2048
s_add_u32 s61,s82,1409024
buffer_load_dwordx4 v12,s[40:43],s61 offen offset:0  lds
s_add_u32 m0,s88,3072
ds_read_b64_tr_b16 v[102:103],v14 offset:33280
buffer_load_dwordx4 v13,s[40:43],s61 offen offset:0  lds
ds_read_b64_tr_b16 v[104:105],v14 offset:32776
ds_read_b64_tr_b16 v[106:107],v14 offset:33288
ds_read_b64_tr_b16 v[108:109],v16 offset:32768
ds_read_b64_tr_b16 v[110:111],v16 offset:33280
ds_read_b64_tr_b16 v[112:113],v16 offset:32776
ds_read_b64_tr_b16 v[114:115],v16 offset:33288
s_waitcnt lgkmcnt(8)
s_waitcnt vmcnt(16)
s_add_u32 m0,s88,16384
s_add_u32 s61,s82,1441792
buffer_load_dwordx4 v12,s[40:43],s61 offen offset:0  lds
s_add_u32 m0,s88,17408
ds_read_b64_tr_b16 v[116:117],v28 offset:0
buffer_load_dwordx4 v13,s[40:43],s61 offen offset:0  lds
s_add_u32 m0,s88,18432
s_add_u32 s61,s82,1474560
buffer_load_dwordx4 v12,s[40:43],s61 offen offset:0  lds
s_add_u32 m0,s88,19456
ds_read_b64_tr_b16 v[118:119],v28 offset:512
buffer_load_dwordx4 v13,s[40:43],s61 offen offset:0  lds
ds_read_b64_tr_b16 v[120:121],v28 offset:8
ds_read_b64_tr_b16 v[122:123],v28 offset:520
ds_read_b64_tr_b16 v[124:125],v30 offset:0
ds_read_b64_tr_b16 v[126:127],v30 offset:512
ds_read_b64_tr_b16 v[128:129],v30 offset:8
ds_read_b64_tr_b16 v[130:131],v30 offset:520
s_waitcnt lgkmcnt(8)
s_waitcnt vmcnt(16)
s_add_u32 m0,s88,32768
s_add_u32 s61,s82,1507328
buffer_load_dwordx4 v12,s[40:43],s61 offen offset:0  lds
s_add_u32 m0,s88,33792
ds_read_b64_tr_b16 v[132:133],v28 offset:16384
buffer_load_dwordx4 v13,s[40:43],s61 offen offset:0  lds
s_add_u32 m0,s88,34816
s_add_u32 s61,s82,1540096
buffer_load_dwordx4 v12,s[40:43],s61 offen offset:0  lds
s_add_u32 m0,s88,35840
ds_read_b64_tr_b16 v[134:135],v28 offset:16896
buffer_load_dwordx4 v13,s[40:43],s61 offen offset:0  lds
ds_read_b64_tr_b16 v[136:137],v28 offset:16392
ds_read_b64_tr_b16 v[138:139],v28 offset:16904
ds_read_b64_tr_b16 v[140:141],v30 offset:16384
ds_read_b64_tr_b16 v[142:143],v30 offset:16896
ds_read_b64_tr_b16 v[144:145],v30 offset:16392
ds_read_b64_tr_b16 v[146:147],v30 offset:16904
s_waitcnt lgkmcnt(8)
s_waitcnt vmcnt(16)
s_add_u32 m0,s88,49152
s_add_u32 s61,s82,1572864
buffer_load_dwordx4 v12,s[40:43],s61 offen offset:0  lds
s_add_u32 m0,s88,50176
ds_read_b64_tr_b16 v[148:149],v28 offset:32768
buffer_load_dwordx4 v13,s[40:43],s61 offen offset:0  lds
s_add_u32 m0,s88,51200
s_add_u32 s61,s82,1605632
buffer_load_dwordx4 v12,s[40:43],s61 offen offset:0  lds
s_add_u32 m0,s88,52224
ds_read_b64_tr_b16 v[150:151],v28 offset:33280
buffer_load_dwordx4 v13,s[40:43],s61 offen offset:0  lds
ds_read_b64_tr_b16 v[152:153],v28 offset:32776
ds_read_b64_tr_b16 v[154:155],v28 offset:33288
ds_read_b64_tr_b16 v[156:157],v30 offset:32768
ds_read_b64_tr_b16 v[158:159],v30 offset:33280
ds_read_b64_tr_b16 v[52:53],v30 offset:32776
ds_read_b64_tr_b16 v[54:55],v30 offset:33288
s_waitcnt lgkmcnt(8)
s_waitcnt vmcnt(16)
s_add_u32 m0,s88,65536
s_add_u32 s61,s82,1638400
buffer_load_dwordx4 v12,s[40:43],s61 offen offset:0  lds
s_add_u32 m0,s88,66560
ds_read_b64_tr_b16 v[160:161],v32 offset:0
buffer_load_dwordx4 v13,s[40:43],s61 offen offset:0  lds
s_add_u32 m0,s88,67584
s_add_u32 s61,s82,1671168
buffer_load_dwordx4 v12,s[40:43],s61 offen offset:0  lds
s_add_u32 m0,s88,68608
ds_read_b64_tr_b16 v[162:163],v32 offset:512
buffer_load_dwordx4 v13,s[40:43],s61 offen offset:0  lds
ds_read_b64_tr_b16 v[164:165],v32 offset:8
ds_read_b64_tr_b16 v[166:167],v32 offset:520
ds_read_b64_tr_b16 v[168:169],v34 offset:0
ds_read_b64_tr_b16 v[170:171],v34 offset:512
ds_read_b64_tr_b16 v[44:45],v34 offset:8
ds_read_b64_tr_b16 v[46:47],v34 offset:520
s_waitcnt lgkmcnt(8)
ds_write_b128 v10,v[52:55] offset:0
s_waitcnt vmcnt(16)
s_add_u32 m0,s88,81920
s_add_u32 s61,s82,1703936
buffer_load_dwordx4 v12,s[40:43],s61 offen offset:0  lds
s_add_u32 m0,s88,82944
ds_read_b64_tr_b16 v[172:173],v14 offset:0
buffer_load_dwordx4 v13,s[40:43],s61 offen offset:0  lds
s_add_u32 m0,s88,83968
s_add_u32 s61,s82,1736704
buffer_load_dwordx4 v12,s[40:43],s61 offen offset:0  lds
s_add_u32 m0,s88,84992
ds_read_b64_tr_b16 v[174:175],v14 offset:512
buffer_load_dwordx4 v13,s[40:43],s61 offen offset:0  lds
ds_read_b64_tr_b16 v[176:177],v14 offset:8
ds_read_b64_tr_b16 v[178:179],v14 offset:520
ds_read_b64_tr_b16 v[180:181],v16 offset:0
ds_read_b64_tr_b16 v[182:183],v16 offset:512
ds_read_b64_tr_b16 v[52:53],v16 offset:8
ds_read_b64_tr_b16 v[54:55],v16 offset:520
s_waitcnt lgkmcnt(8)
ds_write_b128 v10,v[44:47] offset:1024
s_waitcnt vmcnt(16)
s_add_u32 m0,s88,98304
s_add_u32 s61,s82,1769472
buffer_load_dwordx4 v12,s[40:43],s61 offen offset:0  lds
s_add_u32 m0,s88,99328
ds_read_b64_tr_b16 v[184:185],v14 offset:16384
buffer_load_dwordx4 v13,s[40:43],s61 offen offset:0  lds
s_add_u32 m0,s88,100352
s_add_u32 s61,s82,1802240
buffer_load_dwordx4 v12,s[40:43],s61 offen offset:0  lds
s_add_u32 m0,s88,101376
ds_read_b64_tr_b16 v[186:187],v14 offset:16896
buffer_load_dwordx4 v13,s[40:43],s61 offen offset:0  lds
ds_read_b64_tr_b16 v[188:189],v14 offset:16392
ds_read_b64_tr_b16 v[190:191],v14 offset:16904
ds_read_b64_tr_b16 v[192:193],v16 offset:16384
ds_read_b64_tr_b16 v[194:195],v16 offset:16896
ds_read_b64_tr_b16 v[44:45],v16 offset:16392
ds_read_b64_tr_b16 v[46:47],v16 offset:16904
s_waitcnt lgkmcnt(8)
ds_write_b128 v10,v[52:55] offset:2048
s_waitcnt vmcnt(16)
ds_read_b64_tr_b16 v[196:197],v14 offset:32768
ds_read_b64_tr_b16 v[198:199],v14 offset:33280
ds_read_b64_tr_b16 v[200:201],v14 offset:32776
ds_read_b64_tr_b16 v[202:203],v14 offset:33288
ds_read_b64_tr_b16 v[204:205],v16 offset:32768
ds_read_b64_tr_b16 v[206:207],v16 offset:33280
ds_read_b64_tr_b16 v[52:53],v16 offset:32776
ds_read_b64_tr_b16 v[54:55],v16 offset:33288
s_getpc_b64 s[46:47]
Lg4_cp_%=:
s_add_u32 s46,s46,Lg4_init_%=-Lg4_cp_%=
s_addc_u32 s47,s47,0
s_lshl_b32 s68,s54,13
v_lshl_add_u32 v76,v2,3,s68
v_min_u32 v77,Lg4_done_%=-Lg4_init_%=,v76
global_load_dword v80,v77,s[46:47]
s_waitcnt lgkmcnt(8)
ds_write_b128 v10,v[44:47] offset:3072
s_waitcnt vmcnt(13)
ds_read_b64_tr_b16 v[208:209],v28 offset:0
ds_read_b64_tr_b16 v[210:211],v28 offset:512
ds_read_b64_tr_b16 v[212:213],v28 offset:8
ds_read_b64_tr_b16 v[214:215],v28 offset:520
ds_read_b64_tr_b16 v[216:217],v30 offset:0
ds_read_b64_tr_b16 v[218:219],v30 offset:512
ds_read_b64_tr_b16 v[44:45],v30 offset:8
ds_read_b64_tr_b16 v[46:47],v30 offset:520
s_waitcnt lgkmcnt(8)
ds_write_b128 v10,v[52:55] offset:4096
s_waitcnt vmcnt(9)
ds_read_b64_tr_b16 v[220:221],v28 offset:16384
ds_read_b64_tr_b16 v[222:223],v28 offset:16896
ds_read_b64_tr_b16 v[224:225],v28 offset:16392
ds_read_b64_tr_b16 v[226:227],v28 offset:16904
ds_read_b64_tr_b16 v[228:229],v30 offset:16384
ds_read_b64_tr_b16 v[230:231],v30 offset:16896
ds_read_b64_tr_b16 v[52:53],v30 offset:16392
ds_read_b64_tr_b16 v[54:55],v30 offset:16904
s_waitcnt lgkmcnt(8))"
R"(
ds_write_b128 v10,v[44:47] offset:5120
s_waitcnt vmcnt(5)
ds_read_b64_tr_b16 v[232:233],v28 offset:32768
ds_read_b64_tr_b16 v[234:235],v28 offset:33280
ds_read_b64_tr_b16 v[236:237],v28 offset:32776
ds_read_b64_tr_b16 v[238:239],v28 offset:33288
ds_read_b64_tr_b16 v[240:241],v30 offset:32768
ds_read_b64_tr_b16 v[242:243],v30 offset:33280
ds_read_b64_tr_b16 v[44:45],v30 offset:32776
ds_read_b64_tr_b16 v[46:47],v30 offset:33288
s_waitcnt lgkmcnt(8)
ds_write_b128 v10,v[52:55] offset:6144
s_waitcnt vmcnt(1)
ds_read_b64_tr_b16 v[244:245],v32 offset:0
ds_read_b64_tr_b16 v[246:247],v32 offset:512
ds_read_b64_tr_b16 v[248:249],v32 offset:8
ds_read_b64_tr_b16 v[250:251],v32 offset:520
ds_read_b64_tr_b16 v[252:253],v34 offset:0
ds_read_b64_tr_b16 v[254:255],v34 offset:512
ds_read_b64_tr_b16 v[52:53],v34 offset:8
ds_read_b64_tr_b16 v[54:55],v34 offset:520
s_waitcnt lgkmcnt(8)
ds_write_b128 v10,v[44:47] offset:7168
s_waitcnt lgkmcnt(0)
ds_write_b128 v10,v[52:55] offset:8192
s_waitcnt lgkmcnt(0)
s_barrier
v_readfirstlane_b32 s60,v6
s_nop 3
s_add_u32 s60,s60,1
s_cmp_eq_u32 s60,0
s_cselect_b32 s60,1,s60
Lg4_init_%=:
v_mov_b32 v6,s60
s_mov_b32 s59,536870912
s_add_u32 s57,s56,131072
s_mov_b32 s79,0
s_mov_b32 s75,0x3a800000
s_mov_b32 s76,0xc0135761
s_mov_b32 s77,0x3d372713
s_mov_b32 s44,s59
s_mov_b32 s45,s59
s_mov_b32 s46,s59
s_mov_b32 s47,s59
s_mov_b32 s48,s83
s_add_u32 s49,s48,s55
s_cmp_eq_u32 s49,s56
s_cselect_b32 s49,s57,s49
s_cmp_eq_u32 s49,s58
s_cselect_b32 s49,s59,s49
s_add_u32 s50,s49,s55
s_cmp_eq_u32 s50,s56
s_cselect_b32 s50,s57,s50
s_cmp_eq_u32 s50,s58
s_cselect_b32 s50,s59,s50
s_add_u32 s51,s50,s55
s_cmp_eq_u32 s51,s56
s_cselect_b32 s51,s57,s51
s_cmp_eq_u32 s51,s58
s_cselect_b32 s51,s59,s51
s_add_u32 s52,s51,s55
s_cmp_eq_u32 s52,s56
s_cselect_b32 s52,s57,s52
s_cmp_eq_u32 s52,s58
s_cselect_b32 s52,s59,s52
s_lshl_b32 s63,s54,12
s_mul_i32 s64,s54,3072
s_add_u32 s64,s64,16384
s_lshl_b32 s78,s54,8
s_add_u32 s78,s78,65536
s_getpc_b64 s[90:91]
Lg4_pc_%=:
s_mov_b32 s68,Lg4_d1w0_%=-Lg4_pc_%=
s_cmp_eq_u32 s54,1
s_cselect_b32 s68,Lg4_d1w1_%=-Lg4_pc_%=,s68
s_cmp_eq_u32 s54,2
s_cselect_b32 s68,Lg4_d1w2_%=-Lg4_pc_%=,s68
s_cmp_eq_u32 s54,3
s_cselect_b32 s68,Lg4_d1w3_%=-Lg4_pc_%=,s68
s_add_u32 s92,s90,s68
s_addc_u32 s93,s91,0
s_mov_b32 s68,Lg4_d0w0_%=-Lg4_pc_%=
s_cmp_eq_u32 s54,1
s_cselect_b32 s68,Lg4_d0w1_%=-Lg4_pc_%=,s68
s_cmp_eq_u32 s54,2
s_cselect_b32 s68,Lg4_d0w2_%=-Lg4_pc_%=,s68
s_cmp_eq_u32 s54,3
s_cselect_b32 s68,Lg4_d0w3_%=-Lg4_pc_%=,s68
s_add_u32 s90,s90,s68
s_addc_u32 s91,s91,0
s_mov_b32 s66,s59
s_mov_b32 s65,0
s_mov_b32 s62,s63
s_branch Lg4_slowb0_%=
Lg4_d0w0_%=:
v_mfma_f32_16x16x32_bf16 v[12:15],a[0:3],v[44:47],0
s_mov_b32 s73,s47
ds_read_b128 v[56:59],v2 offset:3072
v_lshlrev_b32 v60,16,v68
v_mfma_f32_16x16x32_bf16 v[16:19],a[4:7],v[44:47],0
s_max_u32 s73,s73,s65
v_and_b32 v61,0xffff0000,v68
v_lshlrev_b32 v62,16,v69
v_mfma_f32_16x16x32_bf16 v[20:23],a[8:11],v[44:47],0
s_mov_b32 s65,0
v_and_b32 v63,0xffff0000,v69
v_lshlrev_b32 v64,16,v70
v_mfma_f32_16x16x32_bf16 v[24:27],a[12:15],v[44:47],0
s_waitcnt lgkmcnt(2)
v_and_b32 v65,0xffff0000,v70
v_lshlrev_b32 v66,16,v71
v_mfma_f32_16x16x32_bf16 v[12:15],a[16:19],v[48:51],v[12:15]
s_mul_i32 s61,s49,7
ds_read_b128 v[44:47],v2 offset:4096
v_and_b32 v67,0xffff0000,v71
v_mfma_f32_16x16x32_bf16 v[16:19],a[20:23],v[48:51],v[16:19]
s_lshr_b32 s61,s61,1
v_cvt_pk_bf16_f32 v28,v28,v29
v_cvt_pk_bf16_f32 v29,v30,v31
v_mfma_f32_16x16x32_bf16 v[20:23],a[24:27],v[48:51],v[20:23]
v_cvt_pk_bf16_f32 v30,v32,v33
v_cvt_pk_bf16_f32 v31,v34,v35
v_mfma_f32_16x16x32_bf16 v[24:27],a[28:31],v[48:51],v[24:27]
s_waitcnt lgkmcnt(2)
v_add_f32 v36,v36,v60
v_add_f32 v37,v37,v61
v_mfma_f32_16x16x32_bf16 v[12:15],a[32:35],v[52:55],v[12:15]
buffer_store_dwordx4 v[28:31],v3,s[36:39],s73 offen offset:0 sc0 sc1
ds_read_b128 v[48:51],v2 offset:5120
v_add_f32 v38,v38,v62
v_add_f32 v39,v39,v63
v_mfma_f32_16x16x32_bf16 v[16:19],a[36:39],v[52:55],v[16:19]
v_add_f32 v40,v40,v64
v_add_f32 v41,v41,v65
v_mfma_f32_16x16x32_bf16 v[20:23],a[40:43],v[52:55],v[20:23]
v_add_f32 v42,v42,v66
v_add_f32 v43,v43,v67
v_mfma_f32_16x16x32_bf16 v[24:27],a[44:47],v[52:55],v[24:27]
s_waitcnt lgkmcnt(2)
v_cvt_pk_bf16_f32 v36,v36,v37
v_cvt_pk_bf16_f32 v37,v38,v39
v_mfma_f32_16x16x32_bf16 v[12:15],a[48:51],v[56:59],v[12:15]
ds_read_b128 v[52:55],v2 offset:6144
v_cvt_pk_bf16_f32 v38,v40,v41
v_mfma_f32_16x16x32_bf16 v[16:19],a[52:55],v[56:59],v[16:19]
v_cvt_pk_bf16_f32 v39,v42,v43
v_mfma_f32_16x16x32_bf16 v[20:23],a[56:59],v[56:59],v[20:23]
s_waitcnt vmcnt(1) lgkmcnt(2)
v_mfma_f32_16x16x32_bf16 v[24:27],a[60:63],v[56:59],v[24:27]
s_barrier
v_mfma_f32_16x16x32_bf16 v[12:15],a[64:67],v[44:47],v[12:15]
ds_read_b32 v11,v8 offset:4096
ds_read_b128 v[56:59],v2 offset:7168
v_mfma_f32_16x16x32_bf16 v[16:19],a[68:71],v[44:47],v[16:19]
v_mfma_f32_16x16x32_bf16 v[20:23],a[72:75],v[44:47],v[20:23]
v_mfma_f32_16x16x32_bf16 v[24:27],a[76:79],v[44:47],v[24:27]
s_waitcnt lgkmcnt(3)
v_mfma_f32_16x16x32_bf16 v[12:15],a[80:83],v[48:51],v[12:15]
s_waitcnt lgkmcnt(1)
buffer_store_dwordx4 v[36:39],v4,s[36:39],s73 offen offset:0 sc0 sc1
v_cmp_eq_u32 vcc,s60,v11
v_mfma_f32_16x16x32_bf16 v[16:19],a[84:87],v[48:51],v[16:19]
s_cmp_eq_u32 vcc_lo,-1
ds_read_b128 v[44:47],v2 offset:8192
s_cselect_b32 s66,s49,s59
v_mfma_f32_16x16x32_bf16 v[20:23],a[88:91],v[48:51],v[20:23]
s_cselect_b32 s61,s61,s59
s_add_u32 m0,s63,28672
s_cmp_ge_u32 s66,s59
v_mfma_f32_16x16x32_bf16 v[24:27],a[92:95],v[48:51],v[24:27]
s_cselect_b32 s68,s49,s59
s_mul_i32 s68,s68,5
v_mfma_f32_16x16x32_bf16 v[12:15],a[96:99],v[52:55],v[12:15]
buffer_load_dwordx4 v0,s[36:39],s61 offen offset:0 sc1 lds
v_mfma_f32_16x16x32_bf16 v[16:19],a[100:103],v[52:55],v[16:19]
s_lshr_b32 s68,s68,6
ds_read_b128 v[48:51],v2 offset:9216
v_mfma_f32_16x16x32_bf16 v[20:23],a[104:107],v[52:55],v[20:23]
v_mfma_f32_16x16x32_bf16 v[24:27],a[108:111],v[52:55],v[24:27]
s_waitcnt lgkmcnt(2)
v_mfma_f32_16x16x32_bf16 v[12:15],a[112:115],v[56:59],v[12:15]
buffer_load_dwordx4 v0,s[36:39],s61 offen offset:1024 sc1 lds
v_mfma_f32_16x16x32_bf16 v[16:19],a[116:119],v[56:59],v[16:19]
ds_read_b128 v[52:55],v2 offset:10240
v_mfma_f32_16x16x32_bf16 v[20:23],a[120:123],v[56:59],v[20:23]
v_mfma_f32_16x16x32_bf16 v[24:27],a[124:127],v[56:59],v[24:27]
s_waitcnt lgkmcnt(2)
v_mfma_f32_16x16x32_bf16 v[12:15],a[128:131],v[44:47],v[12:15]
buffer_load_dwordx4 v0,s[36:39],s61 offen offset:2048 sc1 lds
v_mfma_f32_16x16x32_bf16 v[16:19],a[132:135],v[44:47],v[16:19]
ds_read_b128 v[56:59],v2 offset:11264
v_mfma_f32_16x16x32_bf16 v[20:23],a[136:139],v[44:47],v[20:23]
v_mfma_f32_16x16x32_bf16 v[24:27],a[140:143],v[44:47],v[24:27]
s_waitcnt lgkmcnt(2)
v_mfma_f32_16x16x32_bf16 v[12:15],a[144:147],v[48:51],v[12:15]
buffer_load_dwordx4 v0,s[36:39],s61 offen offset:3072 sc1 lds
v_mfma_f32_16x16x32_bf16 v[16:19],a[148:151],v[48:51],v[16:19]
s_add_u32 m0,s78,6144
ds_read_b128 v[44:47],v2 offset:12288
s_add_u32 s53,s52,s55)"
R"(
v_mfma_f32_16x16x32_bf16 v[20:23],a[152:155],v[48:51],v[20:23]
s_cmp_eq_u32 s53,s56
s_cselect_b32 s53,s57,s53
s_cmp_eq_u32 s53,s58
v_mfma_f32_16x16x32_bf16 v[24:27],a[156:159],v[48:51],v[24:27]
s_waitcnt lgkmcnt(2)
s_cselect_b32 s53,s59,s53
v_mfma_f32_16x16x32_bf16 v[12:15],a[160:163],v[52:55],v[12:15]
buffer_load_dwordx4 v[68:71],v9,s[36:39],s48 offen offset:0 sc1
v_mfma_f32_16x16x32_bf16 v[16:19],a[164:167],v[52:55],v[16:19]
ds_read_b128 v[48:51],v2 offset:13312
v_mfma_f32_16x16x32_bf16 v[20:23],a[168:171],v[52:55],v[20:23]
v_mfma_f32_16x16x32_bf16 v[24:27],a[172:175],v[52:55],v[24:27]
s_waitcnt lgkmcnt(2)
v_mfma_f32_16x16x32_bf16 v[12:15],a[176:179],v[56:59],v[12:15]
ds_read_b128 v[52:55],v2 offset:14336
v_mfma_f32_16x16x32_bf16 v[16:19],a[180:183],v[56:59],v[16:19]
buffer_load_dword v7,s[36:39],s68 offen offset:0 sc1 lds
v_mfma_f32_16x16x32_bf16 v[20:23],a[184:187],v[56:59],v[20:23]
s_add_u32 m0,s64,28672
s_mul_i32 s68,s50,5
s_lshr_b32 s68,s68,6
v_mfma_f32_16x16x32_bf16 v[24:27],a[188:191],v[56:59],v[24:27]
s_waitcnt lgkmcnt(2)
v_mfma_f32_16x16x32_bf16 v[12:15],a[192:195],v[44:47],v[12:15]
ds_read_b128 v[56:59],v2 offset:15360
v_mfma_f32_16x16x32_bf16 v[16:19],a[196:199],v[44:47],v[16:19]
v_mfma_f32_16x16x32_bf16 v[20:23],a[200:203],v[44:47],v[20:23]
s_waitcnt vmcnt(9) lgkmcnt(2)
v_mfma_f32_16x16x32_bf16 v[24:27],a[204:207],v[44:47],v[24:27]
s_barrier
v_mfma_f32_16x16x32_bf16 v[12:15],a[208:211],v[48:51],v[12:15]
ds_read_b128 v[44:47],v2 offset:16384
v_mfma_f32_16x16x32_bf16 v[16:19],a[212:215],v[48:51],v[16:19]
buffer_load_dwordx4 v1,s[36:39],s61 offen offset:0 sc1 lds
v_mfma_f32_16x16x32_bf16 v[20:23],a[216:219],v[48:51],v[20:23]
v_mfma_f32_16x16x32_bf16 v[24:27],a[220:223],v[48:51],v[24:27]
s_waitcnt lgkmcnt(2)
v_mfma_f32_16x16x32_bf16 v[12:15],a[224:227],v[52:55],v[12:15]
ds_read_b128 v[48:51],v2 offset:17408
v_mfma_f32_16x16x32_bf16 v[16:19],a[228:231],v[52:55],v[16:19]
v_mfma_f32_16x16x32_bf16 v[20:23],a[232:235],v[52:55],v[20:23]
v_mfma_f32_16x16x32_bf16 v[24:27],a[236:239],v[52:55],v[24:27]
s_waitcnt lgkmcnt(2)
buffer_load_dwordx4 v1,s[36:39],s61 offen offset:1024 sc1 lds
v_mfma_f32_16x16x32_bf16 v[12:15],a[240:243],v[56:59],v[12:15]
ds_read_b128 v[52:55],v2 offset:18432
v_mfma_f32_16x16x32_bf16 v[16:19],a[244:247],v[56:59],v[16:19]
v_mfma_f32_16x16x32_bf16 v[20:23],a[248:251],v[56:59],v[20:23]
v_mfma_f32_16x16x32_bf16 v[24:27],a[252:255],v[56:59],v[24:27]
s_waitcnt lgkmcnt(2)
v_mfma_f32_16x16x32_bf16 v[12:15],v[100:103],v[44:47],v[12:15]
ds_read_b128 v[56:59],v2 offset:19456
v_mfma_f32_16x16x32_bf16 v[16:19],v[104:107],v[44:47],v[16:19]
buffer_load_dwordx4 v1,s[36:39],s61 offen offset:2048 sc1 lds
v_mfma_f32_16x16x32_bf16 v[20:23],v[108:111],v[44:47],v[20:23]
s_add_u32 m0,s78,5120
ds_read_b128 v[88:91],v10 offset:0
s_cmp_ge_u32 s66,s59
v_mfma_f32_16x16x32_bf16 v[24:27],v[112:115],v[44:47],v[24:27]
s_waitcnt lgkmcnt(3)
v_mfma_f32_16x16x32_bf16 v[12:15],v[116:119],v[48:51],v[12:15]
ds_read_b128 v[44:47],v2 offset:20480
v_mfma_f32_16x16x32_bf16 v[16:19],v[120:123],v[48:51],v[16:19]
ds_read_b128 v[76:79],v10 offset:1024
v_mfma_f32_16x16x32_bf16 v[20:23],v[124:127],v[48:51],v[20:23]
v_mfma_f32_16x16x32_bf16 v[24:27],v[128:131],v[48:51],v[24:27]
s_waitcnt lgkmcnt(4)
v_mfma_f32_16x16x32_bf16 v[12:15],v[132:135],v[52:55],v[12:15]
buffer_load_dword v7,s[36:39],s68 offen offset:0 sc1 lds
v_mfma_f32_16x16x32_bf16 v[16:19],v[136:139],v[52:55],v[16:19]
ds_read_b128 v[48:51],v2 offset:21504
ds_read_b128 v[80:83],v10 offset:2048
v_mfma_f32_16x16x32_bf16 v[20:23],v[140:143],v[52:55],v[20:23]
v_mfma_f32_16x16x32_bf16 v[24:27],v[144:147],v[52:55],v[24:27]
s_waitcnt lgkmcnt(4)
v_mfma_f32_16x16x32_bf16 v[12:15],v[148:151],v[56:59],v[12:15]
ds_read_b128 v[52:55],v2 offset:22528
v_mfma_f32_16x16x32_bf16 v[16:19],v[152:155],v[56:59],v[16:19]
ds_read_b128 v[84:87],v10 offset:3072
v_mfma_f32_16x16x32_bf16 v[20:23],v[156:159],v[56:59],v[20:23]
v_mfma_f32_16x16x32_bf16 v[24:27],v[88:91],v[56:59],v[24:27]
s_waitcnt lgkmcnt(4)
v_mfma_f32_16x16x32_bf16 v[12:15],v[160:163],v[44:47],v[12:15]
ds_read_b128 v[56:59],v2 offset:23552
v_mfma_f32_16x16x32_bf16 v[16:19],v[164:167],v[44:47],v[16:19]
ds_read_b128 v[88:91],v10 offset:4096
v_mfma_f32_16x16x32_bf16 v[20:23],v[168:171],v[44:47],v[20:23]
v_mfma_f32_16x16x32_bf16 v[24:27],v[76:79],v[44:47],v[24:27]
s_waitcnt lgkmcnt(4)
v_mfma_f32_16x16x32_bf16 v[12:15],v[172:175],v[48:51],v[12:15]
ds_read_b128 v[44:47],v2 offset:24576
v_mfma_f32_16x16x32_bf16 v[16:19],v[176:179],v[48:51],v[16:19]
ds_read_b128 v[76:79],v10 offset:5120
v_mfma_f32_16x16x32_bf16 v[20:23],v[180:183],v[48:51],v[20:23]
s_waitcnt vmcnt(10) lgkmcnt(4)
v_mfma_f32_16x16x32_bf16 v[24:27],v[80:83],v[48:51],v[24:27]
s_barrier
v_mfma_f32_16x16x32_bf16 v[12:15],v[184:187],v[52:55],v[12:15]
ds_read_b128 v[48:51],v2 offset:25600
v_mfma_f32_16x16x32_bf16 v[16:19],v[188:191],v[52:55],v[16:19]
ds_read_b128 v[80:83],v10 offset:6144
v_mfma_f32_16x16x32_bf16 v[20:23],v[192:195],v[52:55],v[20:23]
v_mfma_f32_16x16x32_bf16 v[24:27],v[84:87],v[52:55],v[24:27]
s_waitcnt lgkmcnt(4)
v_mfma_f32_16x16x32_bf16 v[12:15],v[196:199],v[56:59],v[12:15]
ds_read_b128 v[52:55],v2 offset:26624
v_mfma_f32_16x16x32_bf16 v[16:19],v[200:203],v[56:59],v[16:19]
ds_read_b128 v[84:87],v10 offset:7168
v_mfma_f32_16x16x32_bf16 v[20:23],v[204:207],v[56:59],v[20:23]
v_mfma_f32_16x16x32_bf16 v[24:27],v[88:91],v[56:59],v[24:27]
s_waitcnt lgkmcnt(4)
v_mfma_f32_16x16x32_bf16 v[12:15],v[208:211],v[44:47],v[12:15]
ds_read_b128 v[56:59],v2 offset:27648
v_mfma_f32_16x16x32_bf16 v[16:19],v[212:215],v[44:47],v[16:19]
ds_read_b128 v[88:91],v10 offset:8192
v_mfma_f32_16x16x32_bf16 v[20:23],v[216:219],v[44:47],v[20:23]
s_waitcnt vmcnt(4) lgkmcnt(4)
v_mfma_f32_16x16x32_bf16 v[24:27],v[76:79],v[44:47],v[24:27]
s_barrier
v_mfma_f32_16x16x32_bf16 v[12:15],v[220:223],v[48:51],v[12:15]
ds_read_b128 v[44:47],v2 offset:28672
v_mfma_f32_16x16x32_bf16 v[16:19],v[224:227],v[48:51],v[16:19]
v_mfma_f32_16x16x32_bf16 v[20:23],v[228:231],v[48:51],v[20:23]
v_mfma_f32_16x16x32_bf16 v[24:27],v[80:83],v[48:51],v[24:27]
s_waitcnt lgkmcnt(3)
v_mfma_f32_16x16x32_bf16 v[12:15],v[232:235],v[52:55],v[12:15]
ds_read_b128 v[48:51],v2 offset:29696
v_mfma_f32_16x16x32_bf16 v[16:19],v[236:239],v[52:55],v[16:19]
v_mfma_f32_16x16x32_bf16 v[20:23],v[240:243],v[52:55],v[20:23]
v_mfma_f32_16x16x32_bf16 v[24:27],v[84:87],v[52:55],v[24:27]
s_waitcnt lgkmcnt(2)
v_mfma_f32_16x16x32_bf16 v[12:15],v[244:247],v[56:59],v[12:15]
ds_read_b128 v[52:55],v2 offset:30720
v_mfma_f32_16x16x32_bf16 v[16:19],v[248:251],v[56:59],v[16:19]
v_mfma_f32_16x16x32_bf16 v[20:23],v[252:255],v[56:59],v[20:23]
s_waitcnt lgkmcnt(2)
v_mfma_f32_16x16x32_bf16 v[24:27],v[88:91],v[56:59],v[24:27]
s_cbranch_scc1 Lg4_slow1_%=
Lg4_d1w0_%=:
v_mfma_f32_16x16x32_bf16 v[28:31],a[0:3],v[44:47],0
s_mov_b32 s73,s48
ds_read_b128 v[56:59],v2 offset:31744
v_lshlrev_b32 v60,16,v68
v_mfma_f32_16x16x32_bf16 v[32:35],a[4:7],v[44:47],0
s_max_u32 s73,s73,s65
v_and_b32 v61,0xffff0000,v68
v_lshlrev_b32 v62,16,v69
v_mfma_f32_16x16x32_bf16 v[36:39],a[8:11],v[44:47],0
s_mov_b32 s65,0
v_and_b32 v63,0xffff0000,v69
v_lshlrev_b32 v64,16,v70
v_mfma_f32_16x16x32_bf16 v[40:43],a[12:15],v[44:47],0
s_waitcnt lgkmcnt(2)
v_and_b32 v65,0xffff0000,v70
v_lshlrev_b32 v66,16,v71
v_mfma_f32_16x16x32_bf16 v[28:31],a[16:19],v[48:51],v[28:31]
s_mul_i32 s61,s50,7
ds_read_b128 v[44:47],v2 offset:32768
v_and_b32 v67,0xffff0000,v71
v_mfma_f32_16x16x32_bf16 v[32:35],a[20:23],v[48:51],v[32:35]
s_lshr_b32 s61,s61,1
v_cvt_pk_bf16_f32 v12,v12,v13
v_cvt_pk_bf16_f32 v13,v14,v15
v_mfma_f32_16x16x32_bf16 v[36:39],a[24:27],v[48:51],v[36:39]
s_mov_b64 s[44:45],s[46:47]
v_cvt_pk_bf16_f32 v14,v16,v17
v_cvt_pk_bf16_f32 v15,v18,v19
v_mfma_f32_16x16x32_bf16 v[40:43],a[28:31],v[48:51],v[40:43]
s_waitcnt lgkmcnt(2)
v_add_f32 v20,v20,v60
v_add_f32 v21,v21,v61
v_mfma_f32_16x16x32_bf16 v[28:31],a[32:35],v[52:55],v[28:31]
buffer_store_dwordx4 v[12:15],v3,s[36:39],s73 offen offset:0 sc0 sc1
ds_read_b128 v[48:51],v2 offset:33792
v_add_f32 v22,v22,v62
v_add_f32 v23,v23,v63
v_mfma_f32_16x16x32_bf16 v[32:35],a[36:39],v[52:55],v[32:35]
s_mov_b64 s[46:47],s[48:49]
v_add_f32 v24,v24,v64
v_add_f32 v25,v25,v65
v_mfma_f32_16x16x32_bf16 v[36:39],a[40:43],v[52:55],v[36:39]
v_add_f32 v26,v26,v66
v_add_f32 v27,v27,v67
v_mfma_f32_16x16x32_bf16 v[40:43],a[44:47],v[52:55],v[40:43]
s_waitcnt lgkmcnt(2)
v_cvt_pk_bf16_f32 v20,v20,v21
v_cvt_pk_bf16_f32 v21,v22,v23
v_mfma_f32_16x16x32_bf16 v[28:31],a[48:51],v[56:59],v[28:31]
ds_read_b128 v[52:55],v2 offset:34816
v_cvt_pk_bf16_f32 v22,v24,v25
v_mfma_f32_16x16x32_bf16 v[32:35],a[52:55],v[56:59],v[32:35]
v_cvt_pk_bf16_f32 v23,v26,v27
v_mfma_f32_16x16x32_bf16 v[36:39],a[56:59],v[56:59],v[36:39]
s_waitcnt vmcnt(1) lgkmcnt(2)
v_mfma_f32_16x16x32_bf16 v[40:43],a[60:63],v[56:59],v[40:43]
s_barrier
v_mfma_f32_16x16x32_bf16 v[28:31],a[64:67],v[44:47],v[28:31]
ds_read_b32 v11,v8 offset:5120
ds_read_b128 v[56:59],v2 offset:35840)"
R"(
v_mfma_f32_16x16x32_bf16 v[32:35],a[68:71],v[44:47],v[32:35]
v_mfma_f32_16x16x32_bf16 v[36:39],a[72:75],v[44:47],v[36:39]
v_mfma_f32_16x16x32_bf16 v[40:43],a[76:79],v[44:47],v[40:43]
s_waitcnt lgkmcnt(3)
v_mfma_f32_16x16x32_bf16 v[28:31],a[80:83],v[48:51],v[28:31]
s_waitcnt lgkmcnt(1)
buffer_store_dwordx4 v[20:23],v4,s[36:39],s73 offen offset:0 sc0 sc1
v_cmp_eq_u32 vcc,s60,v11
v_mfma_f32_16x16x32_bf16 v[32:35],a[84:87],v[48:51],v[32:35]
s_cmp_eq_u32 vcc_lo,-1
ds_read_b128 v[44:47],v2 offset:36864
s_cselect_b32 s67,s50,s59
v_mfma_f32_16x16x32_bf16 v[36:39],a[88:91],v[48:51],v[36:39]
s_cselect_b32 s61,s61,s59
s_add_u32 m0,s63,0
s_cmp_ge_u32 s67,s59
v_mfma_f32_16x16x32_bf16 v[40:43],a[92:95],v[48:51],v[40:43]
s_cselect_b32 s68,s50,s59
s_mul_i32 s68,s68,5
v_mfma_f32_16x16x32_bf16 v[28:31],a[96:99],v[52:55],v[28:31]
buffer_load_dwordx4 v0,s[36:39],s61 offen offset:0 sc1 lds
v_mfma_f32_16x16x32_bf16 v[32:35],a[100:103],v[52:55],v[32:35]
s_lshr_b32 s68,s68,6
ds_read_b128 v[48:51],v2 offset:37888
v_mfma_f32_16x16x32_bf16 v[36:39],a[104:107],v[52:55],v[36:39]
v_mfma_f32_16x16x32_bf16 v[40:43],a[108:111],v[52:55],v[40:43]
s_waitcnt lgkmcnt(2)
v_mfma_f32_16x16x32_bf16 v[28:31],a[112:115],v[56:59],v[28:31]
buffer_load_dwordx4 v0,s[36:39],s61 offen offset:1024 sc1 lds
v_mfma_f32_16x16x32_bf16 v[32:35],a[116:119],v[56:59],v[32:35]
ds_read_b128 v[52:55],v2 offset:38912
v_mfma_f32_16x16x32_bf16 v[36:39],a[120:123],v[56:59],v[36:39]
v_mfma_f32_16x16x32_bf16 v[40:43],a[124:127],v[56:59],v[40:43]
s_waitcnt lgkmcnt(2)
v_mfma_f32_16x16x32_bf16 v[28:31],a[128:131],v[44:47],v[28:31]
buffer_load_dwordx4 v0,s[36:39],s61 offen offset:2048 sc1 lds
v_mfma_f32_16x16x32_bf16 v[32:35],a[132:135],v[44:47],v[32:35]
ds_read_b128 v[56:59],v2 offset:39936
v_mfma_f32_16x16x32_bf16 v[36:39],a[136:139],v[44:47],v[36:39]
v_mfma_f32_16x16x32_bf16 v[40:43],a[140:143],v[44:47],v[40:43]
s_waitcnt lgkmcnt(2)
v_mfma_f32_16x16x32_bf16 v[28:31],a[144:147],v[48:51],v[28:31]
buffer_load_dwordx4 v0,s[36:39],s61 offen offset:3072 sc1 lds
v_mfma_f32_16x16x32_bf16 v[32:35],a[148:151],v[48:51],v[32:35]
s_add_u32 m0,s78,6144
ds_read_b128 v[44:47],v2 offset:40960
s_add_u32 s74,s53,s55
v_mfma_f32_16x16x32_bf16 v[36:39],a[152:155],v[48:51],v[36:39]
s_cmp_eq_u32 s74,s56
s_cselect_b32 s74,s57,s74
s_cmp_eq_u32 s74,s58
v_mfma_f32_16x16x32_bf16 v[40:43],a[156:159],v[48:51],v[40:43]
s_waitcnt lgkmcnt(2)
s_cselect_b32 s74,s59,s74
v_mfma_f32_16x16x32_bf16 v[28:31],a[160:163],v[52:55],v[28:31]
buffer_load_dwordx4 v[68:71],v9,s[36:39],s49 offen offset:0 sc1
v_mfma_f32_16x16x32_bf16 v[32:35],a[164:167],v[52:55],v[32:35]
s_mov_b64 s[48:49],s[50:51]
ds_read_b128 v[48:51],v2 offset:41984
v_mfma_f32_16x16x32_bf16 v[36:39],a[168:171],v[52:55],v[36:39]
v_mfma_f32_16x16x32_bf16 v[40:43],a[172:175],v[52:55],v[40:43]
s_waitcnt lgkmcnt(2)
v_mfma_f32_16x16x32_bf16 v[28:31],a[176:179],v[56:59],v[28:31]
ds_read_b128 v[52:55],v2 offset:43008
v_mfma_f32_16x16x32_bf16 v[32:35],a[180:183],v[56:59],v[32:35]
buffer_load_dword v7,s[36:39],s68 offen offset:0 sc1 lds
v_mfma_f32_16x16x32_bf16 v[36:39],a[184:187],v[56:59],v[36:39]
s_add_u32 m0,s64,0
s_mul_i32 s68,s51,5
s_lshr_b32 s68,s68,6
v_mfma_f32_16x16x32_bf16 v[40:43],a[188:191],v[56:59],v[40:43]
s_waitcnt lgkmcnt(2)
s_mov_b64 s[50:51],s[52:53]
s_mov_b32 s52,s74
v_mfma_f32_16x16x32_bf16 v[28:31],a[192:195],v[44:47],v[28:31]
ds_read_b128 v[56:59],v2 offset:44032
v_mfma_f32_16x16x32_bf16 v[32:35],a[196:199],v[44:47],v[32:35]
v_mfma_f32_16x16x32_bf16 v[36:39],a[200:203],v[44:47],v[36:39]
s_waitcnt vmcnt(9) lgkmcnt(2)
v_mfma_f32_16x16x32_bf16 v[40:43],a[204:207],v[44:47],v[40:43]
s_barrier
v_mfma_f32_16x16x32_bf16 v[28:31],a[208:211],v[48:51],v[28:31]
ds_read_b128 v[44:47],v2 offset:45056
v_mfma_f32_16x16x32_bf16 v[32:35],a[212:215],v[48:51],v[32:35]
buffer_load_dwordx4 v1,s[36:39],s61 offen offset:0 sc1 lds
v_mfma_f32_16x16x32_bf16 v[36:39],a[216:219],v[48:51],v[36:39]
v_mfma_f32_16x16x32_bf16 v[40:43],a[220:223],v[48:51],v[40:43]
s_waitcnt lgkmcnt(2)
v_mfma_f32_16x16x32_bf16 v[28:31],a[224:227],v[52:55],v[28:31]
ds_read_b128 v[48:51],v2 offset:46080
v_mfma_f32_16x16x32_bf16 v[32:35],a[228:231],v[52:55],v[32:35]
v_mfma_f32_16x16x32_bf16 v[36:39],a[232:235],v[52:55],v[36:39]
v_mfma_f32_16x16x32_bf16 v[40:43],a[236:239],v[52:55],v[40:43]
s_waitcnt lgkmcnt(2)
buffer_load_dwordx4 v1,s[36:39],s61 offen offset:1024 sc1 lds
v_mfma_f32_16x16x32_bf16 v[28:31],a[240:243],v[56:59],v[28:31]
ds_read_b128 v[52:55],v2 offset:47104
v_mfma_f32_16x16x32_bf16 v[32:35],a[244:247],v[56:59],v[32:35]
v_mfma_f32_16x16x32_bf16 v[36:39],a[248:251],v[56:59],v[36:39]
v_mfma_f32_16x16x32_bf16 v[40:43],a[252:255],v[56:59],v[40:43]
s_waitcnt lgkmcnt(2)
v_mfma_f32_16x16x32_bf16 v[28:31],v[100:103],v[44:47],v[28:31]
ds_read_b128 v[56:59],v2 offset:48128
v_mfma_f32_16x16x32_bf16 v[32:35],v[104:107],v[44:47],v[32:35]
buffer_load_dwordx4 v1,s[36:39],s61 offen offset:2048 sc1 lds
v_mfma_f32_16x16x32_bf16 v[36:39],v[108:111],v[44:47],v[36:39]
s_add_u32 m0,s78,4096
ds_read_b128 v[88:91],v10 offset:0
s_cmp_lt_u32 s67,s59
v_mfma_f32_16x16x32_bf16 v[40:43],v[112:115],v[44:47],v[40:43]
s_waitcnt lgkmcnt(3)
v_mfma_f32_16x16x32_bf16 v[28:31],v[116:119],v[48:51],v[28:31]
ds_read_b128 v[44:47],v2 offset:49152
v_mfma_f32_16x16x32_bf16 v[32:35],v[120:123],v[48:51],v[32:35]
ds_read_b128 v[76:79],v10 offset:1024
v_mfma_f32_16x16x32_bf16 v[36:39],v[124:127],v[48:51],v[36:39]
v_mfma_f32_16x16x32_bf16 v[40:43],v[128:131],v[48:51],v[40:43]
s_waitcnt lgkmcnt(4)
v_mfma_f32_16x16x32_bf16 v[28:31],v[132:135],v[52:55],v[28:31]
buffer_load_dword v7,s[36:39],s68 offen offset:0 sc1 lds
v_mfma_f32_16x16x32_bf16 v[32:35],v[136:139],v[52:55],v[32:35]
ds_read_b128 v[48:51],v2 offset:50176
ds_read_b128 v[80:83],v10 offset:2048
v_mfma_f32_16x16x32_bf16 v[36:39],v[140:143],v[52:55],v[36:39]
v_mfma_f32_16x16x32_bf16 v[40:43],v[144:147],v[52:55],v[40:43]
s_waitcnt lgkmcnt(4)
v_mfma_f32_16x16x32_bf16 v[28:31],v[148:151],v[56:59],v[28:31]
ds_read_b128 v[52:55],v2 offset:51200
v_mfma_f32_16x16x32_bf16 v[32:35],v[152:155],v[56:59],v[32:35]
ds_read_b128 v[84:87],v10 offset:3072
v_mfma_f32_16x16x32_bf16 v[36:39],v[156:159],v[56:59],v[36:39]
v_mfma_f32_16x16x32_bf16 v[40:43],v[88:91],v[56:59],v[40:43]
s_waitcnt lgkmcnt(4)
v_mfma_f32_16x16x32_bf16 v[28:31],v[160:163],v[44:47],v[28:31]
ds_read_b128 v[56:59],v2 offset:52224
v_mfma_f32_16x16x32_bf16 v[32:35],v[164:167],v[44:47],v[32:35]
ds_read_b128 v[88:91],v10 offset:4096
v_mfma_f32_16x16x32_bf16 v[36:39],v[168:171],v[44:47],v[36:39]
v_mfma_f32_16x16x32_bf16 v[40:43],v[76:79],v[44:47],v[40:43]
s_waitcnt lgkmcnt(4)
v_mfma_f32_16x16x32_bf16 v[28:31],v[172:175],v[48:51],v[28:31]
ds_read_b128 v[44:47],v2 offset:53248
v_mfma_f32_16x16x32_bf16 v[32:35],v[176:179],v[48:51],v[32:35]
ds_read_b128 v[76:79],v10 offset:5120
v_mfma_f32_16x16x32_bf16 v[36:39],v[180:183],v[48:51],v[36:39]
s_waitcnt vmcnt(10) lgkmcnt(4)
v_mfma_f32_16x16x32_bf16 v[40:43],v[80:83],v[48:51],v[40:43]
s_barrier
v_mfma_f32_16x16x32_bf16 v[28:31],v[184:187],v[52:55],v[28:31]
ds_read_b128 v[48:51],v2 offset:54272
v_mfma_f32_16x16x32_bf16 v[32:35],v[188:191],v[52:55],v[32:35]
ds_read_b128 v[80:83],v10 offset:6144
v_mfma_f32_16x16x32_bf16 v[36:39],v[192:195],v[52:55],v[36:39]
v_mfma_f32_16x16x32_bf16 v[40:43],v[84:87],v[52:55],v[40:43]
s_waitcnt lgkmcnt(4)
v_mfma_f32_16x16x32_bf16 v[28:31],v[196:199],v[56:59],v[28:31]
ds_read_b128 v[52:55],v2 offset:55296
v_mfma_f32_16x16x32_bf16 v[32:35],v[200:203],v[56:59],v[32:35]
ds_read_b128 v[84:87],v10 offset:7168
v_mfma_f32_16x16x32_bf16 v[36:39],v[204:207],v[56:59],v[36:39]
v_mfma_f32_16x16x32_bf16 v[40:43],v[88:91],v[56:59],v[40:43]
s_waitcnt lgkmcnt(4)
v_mfma_f32_16x16x32_bf16 v[28:31],v[208:211],v[44:47],v[28:31]
ds_read_b128 v[56:59],v2 offset:56320
v_mfma_f32_16x16x32_bf16 v[32:35],v[212:215],v[44:47],v[32:35]
ds_read_b128 v[88:91],v10 offset:8192
v_mfma_f32_16x16x32_bf16 v[36:39],v[216:219],v[44:47],v[36:39]
s_waitcnt vmcnt(4) lgkmcnt(4)
v_mfma_f32_16x16x32_bf16 v[40:43],v[76:79],v[44:47],v[40:43]
s_barrier
v_mfma_f32_16x16x32_bf16 v[28:31],v[220:223],v[48:51],v[28:31]
ds_read_b128 v[44:47],v2 offset:0
v_mfma_f32_16x16x32_bf16 v[32:35],v[224:227],v[48:51],v[32:35]
v_mfma_f32_16x16x32_bf16 v[36:39],v[228:231],v[48:51],v[36:39]
v_mfma_f32_16x16x32_bf16 v[40:43],v[80:83],v[48:51],v[40:43]
s_waitcnt lgkmcnt(3)
v_mfma_f32_16x16x32_bf16 v[28:31],v[232:235],v[52:55],v[28:31]
ds_read_b128 v[48:51],v2 offset:1024
v_mfma_f32_16x16x32_bf16 v[32:35],v[236:239],v[52:55],v[32:35]
v_mfma_f32_16x16x32_bf16 v[36:39],v[240:243],v[52:55],v[36:39]
v_mfma_f32_16x16x32_bf16 v[40:43],v[84:87],v[52:55],v[40:43]
s_waitcnt lgkmcnt(2)
v_mfma_f32_16x16x32_bf16 v[28:31],v[244:247],v[56:59],v[28:31]
ds_read_b128 v[52:55],v2 offset:2048
v_mfma_f32_16x16x32_bf16 v[32:35],v[248:251],v[56:59],v[32:35]
v_mfma_f32_16x16x32_bf16 v[36:39],v[252:255],v[56:59],v[36:39]
s_waitcnt lgkmcnt(2)
v_mfma_f32_16x16x32_bf16 v[40:43],v[88:91],v[56:59],v[40:43]
s_cbranch_scc1 Lg4_d0w0_%=
s_branch Lg4_slow0_%=
Lg4_d0w1_%=:
v_mfma_f32_16x16x32_bf16 v[12:15],a[0:3],v[44:47],0
s_mov_b32 s73,s47
ds_read_b128 v[56:59],v2 offset:3072
v_lshlrev_b32 v60,16,v68
v_mfma_f32_16x16x32_bf16 v[16:19],a[4:7],v[44:47],0
s_max_u32 s73,s73,s65
v_and_b32 v61,0xffff0000,v68
v_lshlrev_b32 v62,16,v69
v_mfma_f32_16x16x32_bf16 v[20:23],a[8:11],v[44:47],0)"
R"(
s_mov_b32 s65,0
v_and_b32 v63,0xffff0000,v69
v_lshlrev_b32 v64,16,v70
v_mfma_f32_16x16x32_bf16 v[24:27],a[12:15],v[44:47],0
s_waitcnt lgkmcnt(2)
v_and_b32 v65,0xffff0000,v70
v_lshlrev_b32 v66,16,v71
v_mfma_f32_16x16x32_bf16 v[12:15],a[16:19],v[48:51],v[12:15]
s_mul_i32 s61,s49,7
ds_read_b128 v[44:47],v2 offset:4096
v_and_b32 v67,0xffff0000,v71
v_mfma_f32_16x16x32_bf16 v[16:19],a[20:23],v[48:51],v[16:19]
s_lshr_b32 s61,s61,1
v_cvt_pk_bf16_f32 v28,v28,v29
v_cvt_pk_bf16_f32 v29,v30,v31
v_mfma_f32_16x16x32_bf16 v[20:23],a[24:27],v[48:51],v[20:23]
v_cvt_pk_bf16_f32 v30,v32,v33
v_cvt_pk_bf16_f32 v31,v34,v35
v_mfma_f32_16x16x32_bf16 v[24:27],a[28:31],v[48:51],v[24:27]
s_waitcnt lgkmcnt(2)
v_add_f32 v36,v36,v60
v_add_f32 v37,v37,v61
v_mfma_f32_16x16x32_bf16 v[12:15],a[32:35],v[52:55],v[12:15]
ds_read_b128 v[48:51],v2 offset:5120
v_add_f32 v38,v38,v62
v_mfma_f32_16x16x32_bf16 v[16:19],a[36:39],v[52:55],v[16:19]
buffer_store_dwordx4 v[28:31],v3,s[36:39],s73 offen offset:0 sc0 sc1
v_add_f32 v39,v39,v63
v_add_f32 v40,v40,v64
v_add_f32 v41,v41,v65
v_mfma_f32_16x16x32_bf16 v[20:23],a[40:43],v[52:55],v[20:23]
v_add_f32 v42,v42,v66
v_add_f32 v43,v43,v67
v_mfma_f32_16x16x32_bf16 v[24:27],a[44:47],v[52:55],v[24:27]
s_waitcnt lgkmcnt(2)
v_cvt_pk_bf16_f32 v36,v36,v37
v_cvt_pk_bf16_f32 v37,v38,v39
v_mfma_f32_16x16x32_bf16 v[12:15],a[48:51],v[56:59],v[12:15]
ds_read_b128 v[52:55],v2 offset:6144
v_cvt_pk_bf16_f32 v38,v40,v41
v_mfma_f32_16x16x32_bf16 v[16:19],a[52:55],v[56:59],v[16:19]
v_cvt_pk_bf16_f32 v39,v42,v43
v_mfma_f32_16x16x32_bf16 v[20:23],a[56:59],v[56:59],v[20:23]
s_waitcnt lgkmcnt(2)
s_nop 0
v_mfma_f32_16x16x32_bf16 v[24:27],a[60:63],v[56:59],v[24:27]
s_barrier
v_mfma_f32_16x16x32_bf16 v[12:15],a[64:67],v[44:47],v[12:15]
ds_read_b128 v[56:59],v2 offset:7168
ds_read_b32 v11,v8 offset:4096
v_mfma_f32_16x16x32_bf16 v[16:19],a[68:71],v[44:47],v[16:19]
v_mfma_f32_16x16x32_bf16 v[20:23],a[72:75],v[44:47],v[20:23]
v_mfma_f32_16x16x32_bf16 v[24:27],a[76:79],v[44:47],v[24:27]
s_waitcnt lgkmcnt(3)
v_mfma_f32_16x16x32_bf16 v[12:15],a[80:83],v[48:51],v[12:15]
s_waitcnt lgkmcnt(0)
ds_read_b128 v[44:47],v2 offset:8192
v_mfma_f32_16x16x32_bf16 v[16:19],a[84:87],v[48:51],v[16:19]
buffer_store_dwordx4 v[36:39],v4,s[36:39],s73 offen offset:0 sc0 sc1
v_cmp_eq_u32 vcc,s60,v11
v_mfma_f32_16x16x32_bf16 v[20:23],a[88:91],v[48:51],v[20:23]
s_cmp_eq_u32 vcc_lo,-1
s_cselect_b32 s66,s49,s59
s_cselect_b32 s61,s61,s59
v_mfma_f32_16x16x32_bf16 v[24:27],a[92:95],v[48:51],v[24:27]
s_add_u32 m0,s63,28672
s_add_u32 s53,s52,s55
v_mfma_f32_16x16x32_bf16 v[12:15],a[96:99],v[52:55],v[12:15]
s_cmp_eq_u32 s53,s56
ds_read_b128 v[48:51],v2 offset:9216
s_cselect_b32 s53,s57,s53
v_mfma_f32_16x16x32_bf16 v[16:19],a[100:103],v[52:55],v[16:19]
buffer_load_dwordx4 v0,s[36:39],s61 offen offset:0 sc1 lds
v_mfma_f32_16x16x32_bf16 v[20:23],a[104:107],v[52:55],v[20:23]
s_cmp_eq_u32 s53,s58
s_cselect_b32 s53,s59,s53
v_mfma_f32_16x16x32_bf16 v[24:27],a[108:111],v[52:55],v[24:27]
v_mfma_f32_16x16x32_bf16 v[12:15],a[112:115],v[56:59],v[12:15]
ds_read_b128 v[52:55],v2 offset:10240
v_mfma_f32_16x16x32_bf16 v[16:19],a[116:119],v[56:59],v[16:19]
buffer_load_dwordx4 v0,s[36:39],s61 offen offset:1024 sc1 lds
v_mfma_f32_16x16x32_bf16 v[20:23],a[120:123],v[56:59],v[20:23]
v_mfma_f32_16x16x32_bf16 v[24:27],a[124:127],v[56:59],v[24:27]
s_waitcnt lgkmcnt(2)
v_mfma_f32_16x16x32_bf16 v[12:15],a[128:131],v[44:47],v[12:15]
ds_read_b128 v[56:59],v2 offset:11264
v_mfma_f32_16x16x32_bf16 v[16:19],a[132:135],v[44:47],v[16:19]
buffer_load_dwordx4 v0,s[36:39],s61 offen offset:2048 sc1 lds
v_mfma_f32_16x16x32_bf16 v[20:23],a[136:139],v[44:47],v[20:23]
v_mfma_f32_16x16x32_bf16 v[24:27],a[140:143],v[44:47],v[24:27]
s_waitcnt lgkmcnt(2)
v_mfma_f32_16x16x32_bf16 v[12:15],a[144:147],v[48:51],v[12:15]
ds_read_b128 v[44:47],v2 offset:12288
v_mfma_f32_16x16x32_bf16 v[16:19],a[148:151],v[48:51],v[16:19]
buffer_load_dwordx4 v0,s[36:39],s61 offen offset:3072 sc1 lds
v_mfma_f32_16x16x32_bf16 v[20:23],a[152:155],v[48:51],v[20:23]
s_add_u32 m0,s64,28672
s_cmp_ge_u32 s66,s59
v_mfma_f32_16x16x32_bf16 v[24:27],a[156:159],v[48:51],v[24:27]
s_waitcnt lgkmcnt(2)
v_mfma_f32_16x16x32_bf16 v[12:15],a[160:163],v[52:55],v[12:15]
ds_read_b128 v[48:51],v2 offset:13312
v_mfma_f32_16x16x32_bf16 v[16:19],a[164:167],v[52:55],v[16:19]
buffer_load_dwordx4 v[68:71],v9,s[36:39],s48 offen offset:0 sc1
v_mfma_f32_16x16x32_bf16 v[20:23],a[168:171],v[52:55],v[20:23]
v_mfma_f32_16x16x32_bf16 v[24:27],a[172:175],v[52:55],v[24:27]
s_waitcnt lgkmcnt(2)
v_mfma_f32_16x16x32_bf16 v[12:15],a[176:179],v[56:59],v[12:15]
ds_read_b128 v[52:55],v2 offset:14336
v_mfma_f32_16x16x32_bf16 v[16:19],a[180:183],v[56:59],v[16:19]
v_mfma_f32_16x16x32_bf16 v[20:23],a[184:187],v[56:59],v[20:23]
v_mfma_f32_16x16x32_bf16 v[24:27],a[188:191],v[56:59],v[24:27]
s_waitcnt lgkmcnt(2)
v_mfma_f32_16x16x32_bf16 v[12:15],a[192:195],v[44:47],v[12:15]
ds_read_b128 v[56:59],v2 offset:15360
v_mfma_f32_16x16x32_bf16 v[16:19],a[196:199],v[44:47],v[16:19]
v_mfma_f32_16x16x32_bf16 v[20:23],a[200:203],v[44:47],v[20:23]
s_waitcnt vmcnt(7) lgkmcnt(2)
v_mfma_f32_16x16x32_bf16 v[24:27],a[204:207],v[44:47],v[24:27]
s_barrier
v_mfma_f32_16x16x32_bf16 v[12:15],a[208:211],v[48:51],v[12:15]
ds_read_b128 v[44:47],v2 offset:16384
v_mfma_f32_16x16x32_bf16 v[16:19],a[212:215],v[48:51],v[16:19]
v_mfma_f32_16x16x32_bf16 v[20:23],a[216:219],v[48:51],v[20:23]
buffer_load_dwordx4 v1,s[36:39],s61 offen offset:0 sc1 lds
v_mfma_f32_16x16x32_bf16 v[24:27],a[220:223],v[48:51],v[24:27]
s_waitcnt lgkmcnt(2)
v_mfma_f32_16x16x32_bf16 v[12:15],a[224:227],v[52:55],v[12:15]
ds_read_b128 v[48:51],v2 offset:17408
v_mfma_f32_16x16x32_bf16 v[16:19],a[228:231],v[52:55],v[16:19]
v_mfma_f32_16x16x32_bf16 v[20:23],a[232:235],v[52:55],v[20:23]
v_mfma_f32_16x16x32_bf16 v[24:27],a[236:239],v[52:55],v[24:27]
s_waitcnt lgkmcnt(2)
v_mfma_f32_16x16x32_bf16 v[12:15],a[240:243],v[56:59],v[12:15]
buffer_load_dwordx4 v1,s[36:39],s61 offen offset:1024 sc1 lds
v_mfma_f32_16x16x32_bf16 v[16:19],a[244:247],v[56:59],v[16:19]
ds_read_b128 v[52:55],v2 offset:18432
v_mfma_f32_16x16x32_bf16 v[20:23],a[248:251],v[56:59],v[20:23]
v_mfma_f32_16x16x32_bf16 v[24:27],a[252:255],v[56:59],v[24:27]
s_waitcnt lgkmcnt(2)
v_mfma_f32_16x16x32_bf16 v[12:15],v[100:103],v[44:47],v[12:15]
ds_read_b128 v[56:59],v2 offset:19456
v_mfma_f32_16x16x32_bf16 v[16:19],v[104:107],v[44:47],v[16:19]
ds_read_b128 v[88:91],v10 offset:0
v_mfma_f32_16x16x32_bf16 v[20:23],v[108:111],v[44:47],v[20:23]
buffer_load_dwordx4 v1,s[36:39],s61 offen offset:2048 sc1 lds
v_mfma_f32_16x16x32_bf16 v[24:27],v[112:115],v[44:47],v[24:27]
s_waitcnt lgkmcnt(3)
v_mfma_f32_16x16x32_bf16 v[12:15],v[116:119],v[48:51],v[12:15]
ds_read_b128 v[44:47],v2 offset:20480
v_mfma_f32_16x16x32_bf16 v[16:19],v[120:123],v[48:51],v[16:19]
ds_read_b128 v[76:79],v10 offset:1024
v_mfma_f32_16x16x32_bf16 v[20:23],v[124:127],v[48:51],v[20:23]
v_mfma_f32_16x16x32_bf16 v[24:27],v[128:131],v[48:51],v[24:27]
s_waitcnt lgkmcnt(4)
v_mfma_f32_16x16x32_bf16 v[12:15],v[132:135],v[52:55],v[12:15]
ds_read_b128 v[48:51],v2 offset:21504
v_mfma_f32_16x16x32_bf16 v[16:19],v[136:139],v[52:55],v[16:19]
ds_read_b128 v[80:83],v10 offset:2048
v_mfma_f32_16x16x32_bf16 v[20:23],v[140:143],v[52:55],v[20:23]
v_mfma_f32_16x16x32_bf16 v[24:27],v[144:147],v[52:55],v[24:27]
s_waitcnt lgkmcnt(4)
v_mfma_f32_16x16x32_bf16 v[12:15],v[148:151],v[56:59],v[12:15]
ds_read_b128 v[52:55],v2 offset:22528
v_mfma_f32_16x16x32_bf16 v[16:19],v[152:155],v[56:59],v[16:19]
ds_read_b128 v[84:87],v10 offset:3072
v_mfma_f32_16x16x32_bf16 v[20:23],v[156:159],v[56:59],v[20:23]
v_mfma_f32_16x16x32_bf16 v[24:27],v[88:91],v[56:59],v[24:27]
s_waitcnt lgkmcnt(4)
v_mfma_f32_16x16x32_bf16 v[12:15],v[160:163],v[44:47],v[12:15]
ds_read_b128 v[56:59],v2 offset:23552
v_mfma_f32_16x16x32_bf16 v[16:19],v[164:167],v[44:47],v[16:19]
ds_read_b128 v[88:91],v10 offset:4096
v_mfma_f32_16x16x32_bf16 v[20:23],v[168:171],v[44:47],v[20:23]
v_mfma_f32_16x16x32_bf16 v[24:27],v[76:79],v[44:47],v[24:27]
s_waitcnt lgkmcnt(4)
v_mfma_f32_16x16x32_bf16 v[12:15],v[172:175],v[48:51],v[12:15]
ds_read_b128 v[44:47],v2 offset:24576
v_mfma_f32_16x16x32_bf16 v[16:19],v[176:179],v[48:51],v[16:19]
ds_read_b128 v[76:79],v10 offset:5120
v_mfma_f32_16x16x32_bf16 v[20:23],v[180:183],v[48:51],v[20:23]
s_waitcnt vmcnt(8) lgkmcnt(4)
v_mfma_f32_16x16x32_bf16 v[24:27],v[80:83],v[48:51],v[24:27]
s_barrier
v_mfma_f32_16x16x32_bf16 v[12:15],v[184:187],v[52:55],v[12:15]
ds_read_b128 v[48:51],v2 offset:25600
v_mfma_f32_16x16x32_bf16 v[16:19],v[188:191],v[52:55],v[16:19]
ds_read_b128 v[80:83],v10 offset:6144
v_mfma_f32_16x16x32_bf16 v[20:23],v[192:195],v[52:55],v[20:23]
v_mfma_f32_16x16x32_bf16 v[24:27],v[84:87],v[52:55],v[24:27]
s_waitcnt lgkmcnt(4)
v_mfma_f32_16x16x32_bf16 v[12:15],v[196:199],v[56:59],v[12:15]
ds_read_b128 v[52:55],v2 offset:26624
v_mfma_f32_16x16x32_bf16 v[16:19],v[200:203],v[56:59],v[16:19]
ds_read_b128 v[84:87],v10 offset:7168
v_mfma_f32_16x16x32_bf16 v[20:23],v[204:207],v[56:59],v[20:23]
v_mfma_f32_16x16x32_bf16 v[24:27],v[88:91],v[56:59],v[24:27]
s_waitcnt lgkmcnt(4)
v_mfma_f32_16x16x32_bf16 v[12:15],v[208:211],v[44:47],v[12:15]
ds_read_b128 v[56:59],v2 offset:27648)"
R"(
v_mfma_f32_16x16x32_bf16 v[16:19],v[212:215],v[44:47],v[16:19]
ds_read_b128 v[88:91],v10 offset:8192
v_mfma_f32_16x16x32_bf16 v[20:23],v[216:219],v[44:47],v[20:23]
s_waitcnt vmcnt(3) lgkmcnt(4)
v_mfma_f32_16x16x32_bf16 v[24:27],v[76:79],v[44:47],v[24:27]
s_barrier
v_mfma_f32_16x16x32_bf16 v[12:15],v[220:223],v[48:51],v[12:15]
ds_read_b128 v[44:47],v2 offset:28672
v_mfma_f32_16x16x32_bf16 v[16:19],v[224:227],v[48:51],v[16:19]
v_mfma_f32_16x16x32_bf16 v[20:23],v[228:231],v[48:51],v[20:23]
v_mfma_f32_16x16x32_bf16 v[24:27],v[80:83],v[48:51],v[24:27]
s_waitcnt lgkmcnt(3)
v_mfma_f32_16x16x32_bf16 v[12:15],v[232:235],v[52:55],v[12:15]
ds_read_b128 v[48:51],v2 offset:29696
v_mfma_f32_16x16x32_bf16 v[16:19],v[236:239],v[52:55],v[16:19]
v_mfma_f32_16x16x32_bf16 v[20:23],v[240:243],v[52:55],v[20:23]
v_mfma_f32_16x16x32_bf16 v[24:27],v[84:87],v[52:55],v[24:27]
s_waitcnt lgkmcnt(2)
v_mfma_f32_16x16x32_bf16 v[12:15],v[244:247],v[56:59],v[12:15]
ds_read_b128 v[52:55],v2 offset:30720
v_mfma_f32_16x16x32_bf16 v[16:19],v[248:251],v[56:59],v[16:19]
v_mfma_f32_16x16x32_bf16 v[20:23],v[252:255],v[56:59],v[20:23]
s_waitcnt lgkmcnt(2)
v_mfma_f32_16x16x32_bf16 v[24:27],v[88:91],v[56:59],v[24:27]
s_cbranch_scc1 Lg4_slow1_%=
Lg4_d1w1_%=:
v_mfma_f32_16x16x32_bf16 v[28:31],a[0:3],v[44:47],0
s_mov_b32 s73,s48
ds_read_b128 v[56:59],v2 offset:31744
v_lshlrev_b32 v60,16,v68
v_mfma_f32_16x16x32_bf16 v[32:35],a[4:7],v[44:47],0
s_max_u32 s73,s73,s65
v_and_b32 v61,0xffff0000,v68
v_lshlrev_b32 v62,16,v69
v_mfma_f32_16x16x32_bf16 v[36:39],a[8:11],v[44:47],0
s_mov_b32 s65,0
v_and_b32 v63,0xffff0000,v69
v_lshlrev_b32 v64,16,v70
v_mfma_f32_16x16x32_bf16 v[40:43],a[12:15],v[44:47],0
s_waitcnt lgkmcnt(2)
v_and_b32 v65,0xffff0000,v70
v_lshlrev_b32 v66,16,v71
v_mfma_f32_16x16x32_bf16 v[28:31],a[16:19],v[48:51],v[28:31]
s_mul_i32 s61,s50,7
ds_read_b128 v[44:47],v2 offset:32768
v_and_b32 v67,0xffff0000,v71
v_mfma_f32_16x16x32_bf16 v[32:35],a[20:23],v[48:51],v[32:35]
s_lshr_b32 s61,s61,1
v_cvt_pk_bf16_f32 v12,v12,v13
v_cvt_pk_bf16_f32 v13,v14,v15
v_mfma_f32_16x16x32_bf16 v[36:39],a[24:27],v[48:51],v[36:39]
s_mov_b64 s[44:45],s[46:47]
v_cvt_pk_bf16_f32 v14,v16,v17
v_cvt_pk_bf16_f32 v15,v18,v19
v_mfma_f32_16x16x32_bf16 v[40:43],a[28:31],v[48:51],v[40:43]
s_waitcnt lgkmcnt(2)
v_add_f32 v20,v20,v60
v_add_f32 v21,v21,v61
v_mfma_f32_16x16x32_bf16 v[28:31],a[32:35],v[52:55],v[28:31]
s_mov_b64 s[46:47],s[48:49]
ds_read_b128 v[48:51],v2 offset:33792
v_add_f32 v22,v22,v62
v_mfma_f32_16x16x32_bf16 v[32:35],a[36:39],v[52:55],v[32:35]
buffer_store_dwordx4 v[12:15],v3,s[36:39],s73 offen offset:0 sc0 sc1
v_add_f32 v23,v23,v63
v_add_f32 v24,v24,v64
v_add_f32 v25,v25,v65
v_mfma_f32_16x16x32_bf16 v[36:39],a[40:43],v[52:55],v[36:39]
v_add_f32 v26,v26,v66
v_add_f32 v27,v27,v67
v_mfma_f32_16x16x32_bf16 v[40:43],a[44:47],v[52:55],v[40:43]
s_waitcnt lgkmcnt(2)
v_cvt_pk_bf16_f32 v20,v20,v21
v_cvt_pk_bf16_f32 v21,v22,v23
v_mfma_f32_16x16x32_bf16 v[28:31],a[48:51],v[56:59],v[28:31]
ds_read_b128 v[52:55],v2 offset:34816
v_cvt_pk_bf16_f32 v22,v24,v25
v_mfma_f32_16x16x32_bf16 v[32:35],a[52:55],v[56:59],v[32:35]
v_cvt_pk_bf16_f32 v23,v26,v27
v_mfma_f32_16x16x32_bf16 v[36:39],a[56:59],v[56:59],v[36:39]
s_waitcnt lgkmcnt(2)
s_nop 0
v_mfma_f32_16x16x32_bf16 v[40:43],a[60:63],v[56:59],v[40:43]
s_barrier
v_mfma_f32_16x16x32_bf16 v[28:31],a[64:67],v[44:47],v[28:31]
ds_read_b128 v[56:59],v2 offset:35840
ds_read_b32 v11,v8 offset:5120
v_mfma_f32_16x16x32_bf16 v[32:35],a[68:71],v[44:47],v[32:35]
v_mfma_f32_16x16x32_bf16 v[36:39],a[72:75],v[44:47],v[36:39]
v_mfma_f32_16x16x32_bf16 v[40:43],a[76:79],v[44:47],v[40:43]
s_waitcnt lgkmcnt(3)
v_mfma_f32_16x16x32_bf16 v[28:31],a[80:83],v[48:51],v[28:31]
s_waitcnt lgkmcnt(0)
ds_read_b128 v[44:47],v2 offset:36864
v_mfma_f32_16x16x32_bf16 v[32:35],a[84:87],v[48:51],v[32:35]
buffer_store_dwordx4 v[20:23],v4,s[36:39],s73 offen offset:0 sc0 sc1
v_cmp_eq_u32 vcc,s60,v11
v_mfma_f32_16x16x32_bf16 v[36:39],a[88:91],v[48:51],v[36:39]
s_cmp_eq_u32 vcc_lo,-1
s_cselect_b32 s67,s50,s59
s_cselect_b32 s61,s61,s59
v_mfma_f32_16x16x32_bf16 v[40:43],a[92:95],v[48:51],v[40:43]
s_add_u32 m0,s63,0
s_add_u32 s74,s53,s55
v_mfma_f32_16x16x32_bf16 v[28:31],a[96:99],v[52:55],v[28:31]
s_cmp_eq_u32 s74,s56
ds_read_b128 v[48:51],v2 offset:37888
s_cselect_b32 s74,s57,s74
v_mfma_f32_16x16x32_bf16 v[32:35],a[100:103],v[52:55],v[32:35]
buffer_load_dwordx4 v0,s[36:39],s61 offen offset:0 sc1 lds
v_mfma_f32_16x16x32_bf16 v[36:39],a[104:107],v[52:55],v[36:39]
s_cmp_eq_u32 s74,s58
s_cselect_b32 s74,s59,s74
v_mfma_f32_16x16x32_bf16 v[40:43],a[108:111],v[52:55],v[40:43]
v_mfma_f32_16x16x32_bf16 v[28:31],a[112:115],v[56:59],v[28:31]
ds_read_b128 v[52:55],v2 offset:38912
v_mfma_f32_16x16x32_bf16 v[32:35],a[116:119],v[56:59],v[32:35]
buffer_load_dwordx4 v0,s[36:39],s61 offen offset:1024 sc1 lds
v_mfma_f32_16x16x32_bf16 v[36:39],a[120:123],v[56:59],v[36:39]
v_mfma_f32_16x16x32_bf16 v[40:43],a[124:127],v[56:59],v[40:43]
s_waitcnt lgkmcnt(2)
v_mfma_f32_16x16x32_bf16 v[28:31],a[128:131],v[44:47],v[28:31]
ds_read_b128 v[56:59],v2 offset:39936
v_mfma_f32_16x16x32_bf16 v[32:35],a[132:135],v[44:47],v[32:35]
buffer_load_dwordx4 v0,s[36:39],s61 offen offset:2048 sc1 lds
v_mfma_f32_16x16x32_bf16 v[36:39],a[136:139],v[44:47],v[36:39]
v_mfma_f32_16x16x32_bf16 v[40:43],a[140:143],v[44:47],v[40:43]
s_waitcnt lgkmcnt(2)
v_mfma_f32_16x16x32_bf16 v[28:31],a[144:147],v[48:51],v[28:31]
ds_read_b128 v[44:47],v2 offset:40960
v_mfma_f32_16x16x32_bf16 v[32:35],a[148:151],v[48:51],v[32:35]
buffer_load_dwordx4 v0,s[36:39],s61 offen offset:3072 sc1 lds
v_mfma_f32_16x16x32_bf16 v[36:39],a[152:155],v[48:51],v[36:39]
s_add_u32 m0,s64,0
s_cmp_lt_u32 s67,s59
v_mfma_f32_16x16x32_bf16 v[40:43],a[156:159],v[48:51],v[40:43]
s_waitcnt lgkmcnt(2)
v_mfma_f32_16x16x32_bf16 v[28:31],a[160:163],v[52:55],v[28:31]
ds_read_b128 v[48:51],v2 offset:41984
v_mfma_f32_16x16x32_bf16 v[32:35],a[164:167],v[52:55],v[32:35]
buffer_load_dwordx4 v[68:71],v9,s[36:39],s49 offen offset:0 sc1
v_mfma_f32_16x16x32_bf16 v[36:39],a[168:171],v[52:55],v[36:39]
s_mov_b64 s[48:49],s[50:51]
s_mov_b64 s[50:51],s[52:53]
s_mov_b32 s52,s74
v_mfma_f32_16x16x32_bf16 v[40:43],a[172:175],v[52:55],v[40:43]
s_waitcnt lgkmcnt(2)
v_mfma_f32_16x16x32_bf16 v[28:31],a[176:179],v[56:59],v[28:31]
ds_read_b128 v[52:55],v2 offset:43008
v_mfma_f32_16x16x32_bf16 v[32:35],a[180:183],v[56:59],v[32:35]
v_mfma_f32_16x16x32_bf16 v[36:39],a[184:187],v[56:59],v[36:39]
v_mfma_f32_16x16x32_bf16 v[40:43],a[188:191],v[56:59],v[40:43]
s_waitcnt lgkmcnt(2)
v_mfma_f32_16x16x32_bf16 v[28:31],a[192:195],v[44:47],v[28:31]
ds_read_b128 v[56:59],v2 offset:44032
v_mfma_f32_16x16x32_bf16 v[32:35],a[196:199],v[44:47],v[32:35]
v_mfma_f32_16x16x32_bf16 v[36:39],a[200:203],v[44:47],v[36:39]
s_waitcnt vmcnt(7) lgkmcnt(2)
v_mfma_f32_16x16x32_bf16 v[40:43],a[204:207],v[44:47],v[40:43]
s_barrier
v_mfma_f32_16x16x32_bf16 v[28:31],a[208:211],v[48:51],v[28:31]
ds_read_b128 v[44:47],v2 offset:45056
v_mfma_f32_16x16x32_bf16 v[32:35],a[212:215],v[48:51],v[32:35]
v_mfma_f32_16x16x32_bf16 v[36:39],a[216:219],v[48:51],v[36:39]
buffer_load_dwordx4 v1,s[36:39],s61 offen offset:0 sc1 lds
v_mfma_f32_16x16x32_bf16 v[40:43],a[220:223],v[48:51],v[40:43]
s_waitcnt lgkmcnt(2)
v_mfma_f32_16x16x32_bf16 v[28:31],a[224:227],v[52:55],v[28:31]
ds_read_b128 v[48:51],v2 offset:46080
v_mfma_f32_16x16x32_bf16 v[32:35],a[228:231],v[52:55],v[32:35]
v_mfma_f32_16x16x32_bf16 v[36:39],a[232:235],v[52:55],v[36:39]
v_mfma_f32_16x16x32_bf16 v[40:43],a[236:239],v[52:55],v[40:43]
s_waitcnt lgkmcnt(2)
v_mfma_f32_16x16x32_bf16 v[28:31],a[240:243],v[56:59],v[28:31]
buffer_load_dwordx4 v1,s[36:39],s61 offen offset:1024 sc1 lds
v_mfma_f32_16x16x32_bf16 v[32:35],a[244:247],v[56:59],v[32:35]
ds_read_b128 v[52:55],v2 offset:47104
v_mfma_f32_16x16x32_bf16 v[36:39],a[248:251],v[56:59],v[36:39]
v_mfma_f32_16x16x32_bf16 v[40:43],a[252:255],v[56:59],v[40:43]
s_waitcnt lgkmcnt(2)
v_mfma_f32_16x16x32_bf16 v[28:31],v[100:103],v[44:47],v[28:31]
ds_read_b128 v[56:59],v2 offset:48128
v_mfma_f32_16x16x32_bf16 v[32:35],v[104:107],v[44:47],v[32:35]
ds_read_b128 v[88:91],v10 offset:0
v_mfma_f32_16x16x32_bf16 v[36:39],v[108:111],v[44:47],v[36:39]
buffer_load_dwordx4 v1,s[36:39],s61 offen offset:2048 sc1 lds
v_mfma_f32_16x16x32_bf16 v[40:43],v[112:115],v[44:47],v[40:43]
s_waitcnt lgkmcnt(3)
v_mfma_f32_16x16x32_bf16 v[28:31],v[116:119],v[48:51],v[28:31]
ds_read_b128 v[44:47],v2 offset:49152
v_mfma_f32_16x16x32_bf16 v[32:35],v[120:123],v[48:51],v[32:35]
ds_read_b128 v[76:79],v10 offset:1024
v_mfma_f32_16x16x32_bf16 v[36:39],v[124:127],v[48:51],v[36:39]
v_mfma_f32_16x16x32_bf16 v[40:43],v[128:131],v[48:51],v[40:43]
s_waitcnt lgkmcnt(4)
v_mfma_f32_16x16x32_bf16 v[28:31],v[132:135],v[52:55],v[28:31]
ds_read_b128 v[48:51],v2 offset:50176
v_mfma_f32_16x16x32_bf16 v[32:35],v[136:139],v[52:55],v[32:35]
ds_read_b128 v[80:83],v10 offset:2048
v_mfma_f32_16x16x32_bf16 v[36:39],v[140:143],v[52:55],v[36:39])"
R"(
v_mfma_f32_16x16x32_bf16 v[40:43],v[144:147],v[52:55],v[40:43]
s_waitcnt lgkmcnt(4)
v_mfma_f32_16x16x32_bf16 v[28:31],v[148:151],v[56:59],v[28:31]
ds_read_b128 v[52:55],v2 offset:51200
v_mfma_f32_16x16x32_bf16 v[32:35],v[152:155],v[56:59],v[32:35]
ds_read_b128 v[84:87],v10 offset:3072
v_mfma_f32_16x16x32_bf16 v[36:39],v[156:159],v[56:59],v[36:39]
v_mfma_f32_16x16x32_bf16 v[40:43],v[88:91],v[56:59],v[40:43]
s_waitcnt lgkmcnt(4)
v_mfma_f32_16x16x32_bf16 v[28:31],v[160:163],v[44:47],v[28:31]
ds_read_b128 v[56:59],v2 offset:52224
v_mfma_f32_16x16x32_bf16 v[32:35],v[164:167],v[44:47],v[32:35]
ds_read_b128 v[88:91],v10 offset:4096
v_mfma_f32_16x16x32_bf16 v[36:39],v[168:171],v[44:47],v[36:39]
v_mfma_f32_16x16x32_bf16 v[40:43],v[76:79],v[44:47],v[40:43]
s_waitcnt lgkmcnt(4)
v_mfma_f32_16x16x32_bf16 v[28:31],v[172:175],v[48:51],v[28:31]
ds_read_b128 v[44:47],v2 offset:53248
v_mfma_f32_16x16x32_bf16 v[32:35],v[176:179],v[48:51],v[32:35]
ds_read_b128 v[76:79],v10 offset:5120
v_mfma_f32_16x16x32_bf16 v[36:39],v[180:183],v[48:51],v[36:39]
s_waitcnt vmcnt(8) lgkmcnt(4)
v_mfma_f32_16x16x32_bf16 v[40:43],v[80:83],v[48:51],v[40:43]
s_barrier
v_mfma_f32_16x16x32_bf16 v[28:31],v[184:187],v[52:55],v[28:31]
ds_read_b128 v[48:51],v2 offset:54272
v_mfma_f32_16x16x32_bf16 v[32:35],v[188:191],v[52:55],v[32:35]
ds_read_b128 v[80:83],v10 offset:6144
v_mfma_f32_16x16x32_bf16 v[36:39],v[192:195],v[52:55],v[36:39]
v_mfma_f32_16x16x32_bf16 v[40:43],v[84:87],v[52:55],v[40:43]
s_waitcnt lgkmcnt(4)
v_mfma_f32_16x16x32_bf16 v[28:31],v[196:199],v[56:59],v[28:31]
ds_read_b128 v[52:55],v2 offset:55296
v_mfma_f32_16x16x32_bf16 v[32:35],v[200:203],v[56:59],v[32:35]
ds_read_b128 v[84:87],v10 offset:7168
v_mfma_f32_16x16x32_bf16 v[36:39],v[204:207],v[56:59],v[36:39]
v_mfma_f32_16x16x32_bf16 v[40:43],v[88:91],v[56:59],v[40:43]
s_waitcnt lgkmcnt(4)
v_mfma_f32_16x16x32_bf16 v[28:31],v[208:211],v[44:47],v[28:31]
ds_read_b128 v[56:59],v2 offset:56320
v_mfma_f32_16x16x32_bf16 v[32:35],v[212:215],v[44:47],v[32:35]
ds_read_b128 v[88:91],v10 offset:8192
v_mfma_f32_16x16x32_bf16 v[36:39],v[216:219],v[44:47],v[36:39]
s_waitcnt vmcnt(3) lgkmcnt(4)
v_mfma_f32_16x16x32_bf16 v[40:43],v[76:79],v[44:47],v[40:43]
s_barrier
v_mfma_f32_16x16x32_bf16 v[28:31],v[220:223],v[48:51],v[28:31]
ds_read_b128 v[44:47],v2 offset:0
v_mfma_f32_16x16x32_bf16 v[32:35],v[224:227],v[48:51],v[32:35]
v_mfma_f32_16x16x32_bf16 v[36:39],v[228:231],v[48:51],v[36:39]
v_mfma_f32_16x16x32_bf16 v[40:43],v[80:83],v[48:51],v[40:43]
s_waitcnt lgkmcnt(3)
v_mfma_f32_16x16x32_bf16 v[28:31],v[232:235],v[52:55],v[28:31]
ds_read_b128 v[48:51],v2 offset:1024
v_mfma_f32_16x16x32_bf16 v[32:35],v[236:239],v[52:55],v[32:35]
v_mfma_f32_16x16x32_bf16 v[36:39],v[240:243],v[52:55],v[36:39]
v_mfma_f32_16x16x32_bf16 v[40:43],v[84:87],v[52:55],v[40:43]
s_waitcnt lgkmcnt(2)
v_mfma_f32_16x16x32_bf16 v[28:31],v[244:247],v[56:59],v[28:31]
ds_read_b128 v[52:55],v2 offset:2048
v_mfma_f32_16x16x32_bf16 v[32:35],v[248:251],v[56:59],v[32:35]
v_mfma_f32_16x16x32_bf16 v[36:39],v[252:255],v[56:59],v[36:39]
s_waitcnt lgkmcnt(2)
v_mfma_f32_16x16x32_bf16 v[40:43],v[88:91],v[56:59],v[40:43]
s_cbranch_scc1 Lg4_d0w1_%=
s_branch Lg4_slow0_%=
Lg4_d0w2_%=:
v_mfma_f32_16x16x32_bf16 v[12:15],a[0:3],v[44:47],0
s_mov_b32 s73,s47
ds_read_b128 v[56:59],v2 offset:3072
v_lshlrev_b32 v60,16,v68
v_mfma_f32_16x16x32_bf16 v[16:19],a[4:7],v[44:47],0
s_max_u32 s73,s73,s65
v_and_b32 v61,0xffff0000,v68
v_lshlrev_b32 v62,16,v69
v_mfma_f32_16x16x32_bf16 v[20:23],a[8:11],v[44:47],0
s_mov_b32 s65,0
v_and_b32 v63,0xffff0000,v69
v_lshlrev_b32 v64,16,v70
v_mfma_f32_16x16x32_bf16 v[24:27],a[12:15],v[44:47],0
s_waitcnt lgkmcnt(2)
v_and_b32 v65,0xffff0000,v70
v_lshlrev_b32 v66,16,v71
v_mfma_f32_16x16x32_bf16 v[12:15],a[16:19],v[48:51],v[12:15]
s_mul_i32 s61,s49,7
ds_read_b128 v[44:47],v2 offset:4096
v_and_b32 v67,0xffff0000,v71
v_mfma_f32_16x16x32_bf16 v[16:19],a[20:23],v[48:51],v[16:19]
s_lshr_b32 s61,s61,1
v_cvt_pk_bf16_f32 v28,v28,v29
v_cvt_pk_bf16_f32 v29,v30,v31
v_mfma_f32_16x16x32_bf16 v[20:23],a[24:27],v[48:51],v[20:23]
v_cvt_pk_bf16_f32 v30,v32,v33
v_cvt_pk_bf16_f32 v31,v34,v35
v_mfma_f32_16x16x32_bf16 v[24:27],a[28:31],v[48:51],v[24:27]
s_waitcnt lgkmcnt(2)
v_add_f32 v36,v36,v60
v_add_f32 v37,v37,v61
v_mfma_f32_16x16x32_bf16 v[12:15],a[32:35],v[52:55],v[12:15]
ds_read_b128 v[48:51],v2 offset:5120
v_add_f32 v38,v38,v62
v_mfma_f32_16x16x32_bf16 v[16:19],a[36:39],v[52:55],v[16:19]
v_add_f32 v39,v39,v63
v_add_f32 v40,v40,v64
v_mfma_f32_16x16x32_bf16 v[20:23],a[40:43],v[52:55],v[20:23]
buffer_store_dwordx4 v[28:31],v3,s[36:39],s73 offen offset:0 sc0 sc1
v_add_f32 v41,v41,v65
v_add_f32 v42,v42,v66
v_add_f32 v43,v43,v67
v_mfma_f32_16x16x32_bf16 v[24:27],a[44:47],v[52:55],v[24:27]
s_waitcnt lgkmcnt(2)
v_cvt_pk_bf16_f32 v36,v36,v37
v_cvt_pk_bf16_f32 v37,v38,v39
v_mfma_f32_16x16x32_bf16 v[12:15],a[48:51],v[56:59],v[12:15]
ds_read_b128 v[52:55],v2 offset:6144
v_cvt_pk_bf16_f32 v38,v40,v41
v_mfma_f32_16x16x32_bf16 v[16:19],a[52:55],v[56:59],v[16:19]
v_cvt_pk_bf16_f32 v39,v42,v43
v_mfma_f32_16x16x32_bf16 v[20:23],a[56:59],v[56:59],v[20:23]
s_waitcnt lgkmcnt(2)
s_nop 0
v_mfma_f32_16x16x32_bf16 v[24:27],a[60:63],v[56:59],v[24:27]
s_barrier
v_mfma_f32_16x16x32_bf16 v[12:15],a[64:67],v[44:47],v[12:15]
ds_read_b128 v[56:59],v2 offset:7168
ds_read_b32 v11,v8 offset:4096
v_mfma_f32_16x16x32_bf16 v[16:19],a[68:71],v[44:47],v[16:19]
v_mfma_f32_16x16x32_bf16 v[20:23],a[72:75],v[44:47],v[20:23]
v_mfma_f32_16x16x32_bf16 v[24:27],a[76:79],v[44:47],v[24:27]
s_waitcnt lgkmcnt(3)
v_mfma_f32_16x16x32_bf16 v[12:15],a[80:83],v[48:51],v[12:15]
s_waitcnt lgkmcnt(0)
ds_read_b128 v[44:47],v2 offset:8192
v_mfma_f32_16x16x32_bf16 v[16:19],a[84:87],v[48:51],v[16:19]
v_mfma_f32_16x16x32_bf16 v[20:23],a[88:91],v[48:51],v[20:23]
buffer_store_dwordx4 v[36:39],v4,s[36:39],s73 offen offset:0 sc0 sc1
v_cmp_eq_u32 vcc,s60,v11
v_mfma_f32_16x16x32_bf16 v[24:27],a[92:95],v[48:51],v[24:27]
s_cmp_eq_u32 vcc_lo,-1
s_cselect_b32 s66,s49,s59
v_mfma_f32_16x16x32_bf16 v[12:15],a[96:99],v[52:55],v[12:15]
s_cselect_b32 s61,s61,s59
ds_read_b128 v[48:51],v2 offset:9216
s_add_u32 m0,s63,28672
v_mfma_f32_16x16x32_bf16 v[16:19],a[100:103],v[52:55],v[16:19]
s_add_u32 s53,s52,s55
s_cmp_eq_u32 s53,s56
s_cselect_b32 s53,s57,s53
v_mfma_f32_16x16x32_bf16 v[20:23],a[104:107],v[52:55],v[20:23]
buffer_load_dwordx4 v0,s[36:39],s61 offen offset:0 sc1 lds
v_mfma_f32_16x16x32_bf16 v[24:27],a[108:111],v[52:55],v[24:27]
s_cmp_eq_u32 s53,s58
s_cselect_b32 s53,s59,s53
v_mfma_f32_16x16x32_bf16 v[12:15],a[112:115],v[56:59],v[12:15]
ds_read_b128 v[52:55],v2 offset:10240
v_mfma_f32_16x16x32_bf16 v[16:19],a[116:119],v[56:59],v[16:19]
v_mfma_f32_16x16x32_bf16 v[20:23],a[120:123],v[56:59],v[20:23]
buffer_load_dwordx4 v0,s[36:39],s61 offen offset:1024 sc1 lds
v_mfma_f32_16x16x32_bf16 v[24:27],a[124:127],v[56:59],v[24:27]
s_waitcnt lgkmcnt(2)
v_mfma_f32_16x16x32_bf16 v[12:15],a[128:131],v[44:47],v[12:15]
ds_read_b128 v[56:59],v2 offset:11264
v_mfma_f32_16x16x32_bf16 v[16:19],a[132:135],v[44:47],v[16:19]
v_mfma_f32_16x16x32_bf16 v[20:23],a[136:139],v[44:47],v[20:23]
buffer_load_dwordx4 v0,s[36:39],s61 offen offset:2048 sc1 lds
v_mfma_f32_16x16x32_bf16 v[24:27],a[140:143],v[44:47],v[24:27]
s_waitcnt lgkmcnt(2)
v_mfma_f32_16x16x32_bf16 v[12:15],a[144:147],v[48:51],v[12:15]
ds_read_b128 v[44:47],v2 offset:12288
v_mfma_f32_16x16x32_bf16 v[16:19],a[148:151],v[48:51],v[16:19]
v_mfma_f32_16x16x32_bf16 v[20:23],a[152:155],v[48:51],v[20:23]
buffer_load_dwordx4 v0,s[36:39],s61 offen offset:3072 sc1 lds
v_mfma_f32_16x16x32_bf16 v[24:27],a[156:159],v[48:51],v[24:27]
s_waitcnt lgkmcnt(2)
s_add_u32 m0,s64,28672
s_cmp_ge_u32 s66,s59
v_mfma_f32_16x16x32_bf16 v[12:15],a[160:163],v[52:55],v[12:15]
ds_read_b128 v[48:51],v2 offset:13312
v_mfma_f32_16x16x32_bf16 v[16:19],a[164:167],v[52:55],v[16:19]
v_mfma_f32_16x16x32_bf16 v[20:23],a[168:171],v[52:55],v[20:23]
buffer_load_dwordx4 v[68:71],v9,s[36:39],s48 offen offset:0 sc1
v_mfma_f32_16x16x32_bf16 v[24:27],a[172:175],v[52:55],v[24:27]
s_waitcnt lgkmcnt(2)
v_mfma_f32_16x16x32_bf16 v[12:15],a[176:179],v[56:59],v[12:15]
ds_read_b128 v[52:55],v2 offset:14336
v_mfma_f32_16x16x32_bf16 v[16:19],a[180:183],v[56:59],v[16:19]
v_mfma_f32_16x16x32_bf16 v[20:23],a[184:187],v[56:59],v[20:23]
v_mfma_f32_16x16x32_bf16 v[24:27],a[188:191],v[56:59],v[24:27]
s_waitcnt lgkmcnt(2)
v_mfma_f32_16x16x32_bf16 v[12:15],a[192:195],v[44:47],v[12:15]
ds_read_b128 v[56:59],v2 offset:15360
v_mfma_f32_16x16x32_bf16 v[16:19],a[196:199],v[44:47],v[16:19]
v_mfma_f32_16x16x32_bf16 v[20:23],a[200:203],v[44:47],v[20:23]
s_waitcnt vmcnt(7) lgkmcnt(2)
v_mfma_f32_16x16x32_bf16 v[24:27],a[204:207],v[44:47],v[24:27]
s_barrier
v_mfma_f32_16x16x32_bf16 v[12:15],a[208:211],v[48:51],v[12:15]
ds_read_b128 v[44:47],v2 offset:16384
v_mfma_f32_16x16x32_bf16 v[16:19],a[212:215],v[48:51],v[16:19]
v_mfma_f32_16x16x32_bf16 v[20:23],a[216:219],v[48:51],v[20:23])"
R"(
v_mfma_f32_16x16x32_bf16 v[24:27],a[220:223],v[48:51],v[24:27]
s_waitcnt lgkmcnt(2)
buffer_load_dwordx4 v1,s[36:39],s61 offen offset:0 sc1 lds
v_mfma_f32_16x16x32_bf16 v[12:15],a[224:227],v[52:55],v[12:15]
ds_read_b128 v[48:51],v2 offset:17408
v_mfma_f32_16x16x32_bf16 v[16:19],a[228:231],v[52:55],v[16:19]
v_mfma_f32_16x16x32_bf16 v[20:23],a[232:235],v[52:55],v[20:23]
v_mfma_f32_16x16x32_bf16 v[24:27],a[236:239],v[52:55],v[24:27]
s_waitcnt lgkmcnt(2)
v_mfma_f32_16x16x32_bf16 v[12:15],a[240:243],v[56:59],v[12:15]
ds_read_b128 v[52:55],v2 offset:18432
v_mfma_f32_16x16x32_bf16 v[16:19],a[244:247],v[56:59],v[16:19]
buffer_load_dwordx4 v1,s[36:39],s61 offen offset:1024 sc1 lds
v_mfma_f32_16x16x32_bf16 v[20:23],a[248:251],v[56:59],v[20:23]
v_mfma_f32_16x16x32_bf16 v[24:27],a[252:255],v[56:59],v[24:27]
s_waitcnt lgkmcnt(2)
v_mfma_f32_16x16x32_bf16 v[12:15],v[100:103],v[44:47],v[12:15]
ds_read_b128 v[56:59],v2 offset:19456
v_mfma_f32_16x16x32_bf16 v[16:19],v[104:107],v[44:47],v[16:19]
ds_read_b128 v[88:91],v10 offset:0
v_mfma_f32_16x16x32_bf16 v[20:23],v[108:111],v[44:47],v[20:23]
v_mfma_f32_16x16x32_bf16 v[24:27],v[112:115],v[44:47],v[24:27]
s_waitcnt lgkmcnt(3)
buffer_load_dwordx4 v1,s[36:39],s61 offen offset:2048 sc1 lds
v_mfma_f32_16x16x32_bf16 v[12:15],v[116:119],v[48:51],v[12:15]
ds_read_b128 v[44:47],v2 offset:20480
v_mfma_f32_16x16x32_bf16 v[16:19],v[120:123],v[48:51],v[16:19]
ds_read_b128 v[76:79],v10 offset:1024
v_mfma_f32_16x16x32_bf16 v[20:23],v[124:127],v[48:51],v[20:23]
v_mfma_f32_16x16x32_bf16 v[24:27],v[128:131],v[48:51],v[24:27]
s_waitcnt lgkmcnt(4)
v_mfma_f32_16x16x32_bf16 v[12:15],v[132:135],v[52:55],v[12:15]
ds_read_b128 v[48:51],v2 offset:21504
v_mfma_f32_16x16x32_bf16 v[16:19],v[136:139],v[52:55],v[16:19]
ds_read_b128 v[80:83],v10 offset:2048
v_mfma_f32_16x16x32_bf16 v[20:23],v[140:143],v[52:55],v[20:23]
v_mfma_f32_16x16x32_bf16 v[24:27],v[144:147],v[52:55],v[24:27]
s_waitcnt lgkmcnt(4)
v_mfma_f32_16x16x32_bf16 v[12:15],v[148:151],v[56:59],v[12:15]
ds_read_b128 v[52:55],v2 offset:22528
v_mfma_f32_16x16x32_bf16 v[16:19],v[152:155],v[56:59],v[16:19]
ds_read_b128 v[84:87],v10 offset:3072
v_mfma_f32_16x16x32_bf16 v[20:23],v[156:159],v[56:59],v[20:23]
v_mfma_f32_16x16x32_bf16 v[24:27],v[88:91],v[56:59],v[24:27]
s_waitcnt lgkmcnt(4)
v_mfma_f32_16x16x32_bf16 v[12:15],v[160:163],v[44:47],v[12:15]
ds_read_b128 v[56:59],v2 offset:23552
v_mfma_f32_16x16x32_bf16 v[16:19],v[164:167],v[44:47],v[16:19]
ds_read_b128 v[88:91],v10 offset:4096
v_mfma_f32_16x16x32_bf16 v[20:23],v[168:171],v[44:47],v[20:23]
v_mfma_f32_16x16x32_bf16 v[24:27],v[76:79],v[44:47],v[24:27]
s_waitcnt lgkmcnt(4)
v_mfma_f32_16x16x32_bf16 v[12:15],v[172:175],v[48:51],v[12:15]
ds_read_b128 v[44:47],v2 offset:24576
v_mfma_f32_16x16x32_bf16 v[16:19],v[176:179],v[48:51],v[16:19]
ds_read_b128 v[76:79],v10 offset:5120
v_mfma_f32_16x16x32_bf16 v[20:23],v[180:183],v[48:51],v[20:23]
s_waitcnt vmcnt(8) lgkmcnt(4)
v_mfma_f32_16x16x32_bf16 v[24:27],v[80:83],v[48:51],v[24:27]
s_barrier
v_mfma_f32_16x16x32_bf16 v[12:15],v[184:187],v[52:55],v[12:15]
ds_read_b128 v[48:51],v2 offset:25600
v_mfma_f32_16x16x32_bf16 v[16:19],v[188:191],v[52:55],v[16:19]
ds_read_b128 v[80:83],v10 offset:6144
v_mfma_f32_16x16x32_bf16 v[20:23],v[192:195],v[52:55],v[20:23]
v_mfma_f32_16x16x32_bf16 v[24:27],v[84:87],v[52:55],v[24:27]
s_waitcnt lgkmcnt(4)
v_mfma_f32_16x16x32_bf16 v[12:15],v[196:199],v[56:59],v[12:15]
ds_read_b128 v[52:55],v2 offset:26624
v_mfma_f32_16x16x32_bf16 v[16:19],v[200:203],v[56:59],v[16:19]
ds_read_b128 v[84:87],v10 offset:7168
v_mfma_f32_16x16x32_bf16 v[20:23],v[204:207],v[56:59],v[20:23]
v_mfma_f32_16x16x32_bf16 v[24:27],v[88:91],v[56:59],v[24:27]
s_waitcnt lgkmcnt(4)
v_mfma_f32_16x16x32_bf16 v[12:15],v[208:211],v[44:47],v[12:15]
ds_read_b128 v[56:59],v2 offset:27648
v_mfma_f32_16x16x32_bf16 v[16:19],v[212:215],v[44:47],v[16:19]
ds_read_b128 v[88:91],v10 offset:8192
v_mfma_f32_16x16x32_bf16 v[20:23],v[216:219],v[44:47],v[20:23]
s_waitcnt vmcnt(3) lgkmcnt(4)
v_mfma_f32_16x16x32_bf16 v[24:27],v[76:79],v[44:47],v[24:27]
s_barrier
v_mfma_f32_16x16x32_bf16 v[12:15],v[220:223],v[48:51],v[12:15]
ds_read_b128 v[44:47],v2 offset:28672
v_mfma_f32_16x16x32_bf16 v[16:19],v[224:227],v[48:51],v[16:19]
v_mfma_f32_16x16x32_bf16 v[20:23],v[228:231],v[48:51],v[20:23]
v_mfma_f32_16x16x32_bf16 v[24:27],v[80:83],v[48:51],v[24:27]
s_waitcnt lgkmcnt(3)
v_mfma_f32_16x16x32_bf16 v[12:15],v[232:235],v[52:55],v[12:15]
ds_read_b128 v[48:51],v2 offset:29696
v_mfma_f32_16x16x32_bf16 v[16:19],v[236:239],v[52:55],v[16:19]
v_mfma_f32_16x16x32_bf16 v[20:23],v[240:243],v[52:55],v[20:23]
v_mfma_f32_16x16x32_bf16 v[24:27],v[84:87],v[52:55],v[24:27]
s_waitcnt lgkmcnt(2)
v_mfma_f32_16x16x32_bf16 v[12:15],v[244:247],v[56:59],v[12:15]
ds_read_b128 v[52:55],v2 offset:30720
v_mfma_f32_16x16x32_bf16 v[16:19],v[248:251],v[56:59],v[16:19]
v_mfma_f32_16x16x32_bf16 v[20:23],v[252:255],v[56:59],v[20:23]
s_waitcnt lgkmcnt(2)
v_mfma_f32_16x16x32_bf16 v[24:27],v[88:91],v[56:59],v[24:27]
s_cbranch_scc1 Lg4_slow1_%=
Lg4_d1w2_%=:
v_mfma_f32_16x16x32_bf16 v[28:31],a[0:3],v[44:47],0
s_mov_b32 s73,s48
ds_read_b128 v[56:59],v2 offset:31744
v_lshlrev_b32 v60,16,v68
v_mfma_f32_16x16x32_bf16 v[32:35],a[4:7],v[44:47],0
s_max_u32 s73,s73,s65
v_and_b32 v61,0xffff0000,v68
v_lshlrev_b32 v62,16,v69
v_mfma_f32_16x16x32_bf16 v[36:39],a[8:11],v[44:47],0
s_mov_b32 s65,0
v_and_b32 v63,0xffff0000,v69
v_lshlrev_b32 v64,16,v70
v_mfma_f32_16x16x32_bf16 v[40:43],a[12:15],v[44:47],0
s_waitcnt lgkmcnt(2)
v_and_b32 v65,0xffff0000,v70
v_lshlrev_b32 v66,16,v71
v_mfma_f32_16x16x32_bf16 v[28:31],a[16:19],v[48:51],v[28:31]
s_mul_i32 s61,s50,7
ds_read_b128 v[44:47],v2 offset:32768
v_and_b32 v67,0xffff0000,v71
v_mfma_f32_16x16x32_bf16 v[32:35],a[20:23],v[48:51],v[32:35]
s_lshr_b32 s61,s61,1
v_cvt_pk_bf16_f32 v12,v12,v13
v_cvt_pk_bf16_f32 v13,v14,v15
v_mfma_f32_16x16x32_bf16 v[36:39],a[24:27],v[48:51],v[36:39]
s_mov_b64 s[44:45],s[46:47]
v_cvt_pk_bf16_f32 v14,v16,v17
v_cvt_pk_bf16_f32 v15,v18,v19
v_mfma_f32_16x16x32_bf16 v[40:43],a[28:31],v[48:51],v[40:43]
s_waitcnt lgkmcnt(2)
v_add_f32 v20,v20,v60
v_add_f32 v21,v21,v61
v_mfma_f32_16x16x32_bf16 v[28:31],a[32:35],v[52:55],v[28:31]
s_mov_b64 s[46:47],s[48:49]
ds_read_b128 v[48:51],v2 offset:33792
v_add_f32 v22,v22,v62
v_mfma_f32_16x16x32_bf16 v[32:35],a[36:39],v[52:55],v[32:35]
v_add_f32 v23,v23,v63
v_add_f32 v24,v24,v64
v_mfma_f32_16x16x32_bf16 v[36:39],a[40:43],v[52:55],v[36:39]
buffer_store_dwordx4 v[12:15],v3,s[36:39],s73 offen offset:0 sc0 sc1
v_add_f32 v25,v25,v65
v_add_f32 v26,v26,v66
v_add_f32 v27,v27,v67
v_mfma_f32_16x16x32_bf16 v[40:43],a[44:47],v[52:55],v[40:43]
s_waitcnt lgkmcnt(2)
v_cvt_pk_bf16_f32 v20,v20,v21
v_cvt_pk_bf16_f32 v21,v22,v23
v_mfma_f32_16x16x32_bf16 v[28:31],a[48:51],v[56:59],v[28:31]
ds_read_b128 v[52:55],v2 offset:34816
v_cvt_pk_bf16_f32 v22,v24,v25
v_mfma_f32_16x16x32_bf16 v[32:35],a[52:55],v[56:59],v[32:35]
v_cvt_pk_bf16_f32 v23,v26,v27
v_mfma_f32_16x16x32_bf16 v[36:39],a[56:59],v[56:59],v[36:39]
s_waitcnt lgkmcnt(2)
s_nop 0
v_mfma_f32_16x16x32_bf16 v[40:43],a[60:63],v[56:59],v[40:43]
s_barrier
v_mfma_f32_16x16x32_bf16 v[28:31],a[64:67],v[44:47],v[28:31]
ds_read_b128 v[56:59],v2 offset:35840
ds_read_b32 v11,v8 offset:5120
v_mfma_f32_16x16x32_bf16 v[32:35],a[68:71],v[44:47],v[32:35]
v_mfma_f32_16x16x32_bf16 v[36:39],a[72:75],v[44:47],v[36:39]
v_mfma_f32_16x16x32_bf16 v[40:43],a[76:79],v[44:47],v[40:43]
s_waitcnt lgkmcnt(3)
v_mfma_f32_16x16x32_bf16 v[28:31],a[80:83],v[48:51],v[28:31]
s_waitcnt lgkmcnt(0)
ds_read_b128 v[44:47],v2 offset:36864
v_mfma_f32_16x16x32_bf16 v[32:35],a[84:87],v[48:51],v[32:35]
v_mfma_f32_16x16x32_bf16 v[36:39],a[88:91],v[48:51],v[36:39]
buffer_store_dwordx4 v[20:23],v4,s[36:39],s73 offen offset:0 sc0 sc1
v_cmp_eq_u32 vcc,s60,v11
v_mfma_f32_16x16x32_bf16 v[40:43],a[92:95],v[48:51],v[40:43]
s_cmp_eq_u32 vcc_lo,-1
s_cselect_b32 s67,s50,s59
v_mfma_f32_16x16x32_bf16 v[28:31],a[96:99],v[52:55],v[28:31]
s_cselect_b32 s61,s61,s59
ds_read_b128 v[48:51],v2 offset:37888
s_add_u32 m0,s63,0
v_mfma_f32_16x16x32_bf16 v[32:35],a[100:103],v[52:55],v[32:35]
s_add_u32 s74,s53,s55
s_cmp_eq_u32 s74,s56
s_cselect_b32 s74,s57,s74
v_mfma_f32_16x16x32_bf16 v[36:39],a[104:107],v[52:55],v[36:39]
buffer_load_dwordx4 v0,s[36:39],s61 offen offset:0 sc1 lds
v_mfma_f32_16x16x32_bf16 v[40:43],a[108:111],v[52:55],v[40:43]
s_cmp_eq_u32 s74,s58
s_cselect_b32 s74,s59,s74
v_mfma_f32_16x16x32_bf16 v[28:31],a[112:115],v[56:59],v[28:31]
ds_read_b128 v[52:55],v2 offset:38912
v_mfma_f32_16x16x32_bf16 v[32:35],a[116:119],v[56:59],v[32:35]
v_mfma_f32_16x16x32_bf16 v[36:39],a[120:123],v[56:59],v[36:39]
buffer_load_dwordx4 v0,s[36:39],s61 offen offset:1024 sc1 lds
v_mfma_f32_16x16x32_bf16 v[40:43],a[124:127],v[56:59],v[40:43]
s_waitcnt lgkmcnt(2)
v_mfma_f32_16x16x32_bf16 v[28:31],a[128:131],v[44:47],v[28:31]
ds_read_b128 v[56:59],v2 offset:39936
v_mfma_f32_16x16x32_bf16 v[32:35],a[132:135],v[44:47],v[32:35])"
R"(
v_mfma_f32_16x16x32_bf16 v[36:39],a[136:139],v[44:47],v[36:39]
buffer_load_dwordx4 v0,s[36:39],s61 offen offset:2048 sc1 lds
v_mfma_f32_16x16x32_bf16 v[40:43],a[140:143],v[44:47],v[40:43]
s_waitcnt lgkmcnt(2)
v_mfma_f32_16x16x32_bf16 v[28:31],a[144:147],v[48:51],v[28:31]
ds_read_b128 v[44:47],v2 offset:40960
v_mfma_f32_16x16x32_bf16 v[32:35],a[148:151],v[48:51],v[32:35]
v_mfma_f32_16x16x32_bf16 v[36:39],a[152:155],v[48:51],v[36:39]
buffer_load_dwordx4 v0,s[36:39],s61 offen offset:3072 sc1 lds
v_mfma_f32_16x16x32_bf16 v[40:43],a[156:159],v[48:51],v[40:43]
s_waitcnt lgkmcnt(2)
s_add_u32 m0,s64,0
s_cmp_lt_u32 s67,s59
v_mfma_f32_16x16x32_bf16 v[28:31],a[160:163],v[52:55],v[28:31]
ds_read_b128 v[48:51],v2 offset:41984
v_mfma_f32_16x16x32_bf16 v[32:35],a[164:167],v[52:55],v[32:35]
v_mfma_f32_16x16x32_bf16 v[36:39],a[168:171],v[52:55],v[36:39]
buffer_load_dwordx4 v[68:71],v9,s[36:39],s49 offen offset:0 sc1
v_mfma_f32_16x16x32_bf16 v[40:43],a[172:175],v[52:55],v[40:43]
s_waitcnt lgkmcnt(2)
s_mov_b64 s[48:49],s[50:51]
s_mov_b64 s[50:51],s[52:53]
v_mfma_f32_16x16x32_bf16 v[28:31],a[176:179],v[56:59],v[28:31]
s_mov_b32 s52,s74
ds_read_b128 v[52:55],v2 offset:43008
v_mfma_f32_16x16x32_bf16 v[32:35],a[180:183],v[56:59],v[32:35]
v_mfma_f32_16x16x32_bf16 v[36:39],a[184:187],v[56:59],v[36:39]
v_mfma_f32_16x16x32_bf16 v[40:43],a[188:191],v[56:59],v[40:43]
s_waitcnt lgkmcnt(2)
v_mfma_f32_16x16x32_bf16 v[28:31],a[192:195],v[44:47],v[28:31]
ds_read_b128 v[56:59],v2 offset:44032
v_mfma_f32_16x16x32_bf16 v[32:35],a[196:199],v[44:47],v[32:35]
v_mfma_f32_16x16x32_bf16 v[36:39],a[200:203],v[44:47],v[36:39]
s_waitcnt vmcnt(7) lgkmcnt(2)
v_mfma_f32_16x16x32_bf16 v[40:43],a[204:207],v[44:47],v[40:43]
s_barrier
v_mfma_f32_16x16x32_bf16 v[28:31],a[208:211],v[48:51],v[28:31]
ds_read_b128 v[44:47],v2 offset:45056
v_mfma_f32_16x16x32_bf16 v[32:35],a[212:215],v[48:51],v[32:35]
v_mfma_f32_16x16x32_bf16 v[36:39],a[216:219],v[48:51],v[36:39]
v_mfma_f32_16x16x32_bf16 v[40:43],a[220:223],v[48:51],v[40:43]
s_waitcnt lgkmcnt(2)
buffer_load_dwordx4 v1,s[36:39],s61 offen offset:0 sc1 lds
v_mfma_f32_16x16x32_bf16 v[28:31],a[224:227],v[52:55],v[28:31]
ds_read_b128 v[48:51],v2 offset:46080
v_mfma_f32_16x16x32_bf16 v[32:35],a[228:231],v[52:55],v[32:35]
v_mfma_f32_16x16x32_bf16 v[36:39],a[232:235],v[52:55],v[36:39]
v_mfma_f32_16x16x32_bf16 v[40:43],a[236:239],v[52:55],v[40:43]
s_waitcnt lgkmcnt(2)
v_mfma_f32_16x16x32_bf16 v[28:31],a[240:243],v[56:59],v[28:31]
ds_read_b128 v[52:55],v2 offset:47104
v_mfma_f32_16x16x32_bf16 v[32:35],a[244:247],v[56:59],v[32:35]
buffer_load_dwordx4 v1,s[36:39],s61 offen offset:1024 sc1 lds
v_mfma_f32_16x16x32_bf16 v[36:39],a[248:251],v[56:59],v[36:39]
v_mfma_f32_16x16x32_bf16 v[40:43],a[252:255],v[56:59],v[40:43]
s_waitcnt lgkmcnt(2)
v_mfma_f32_16x16x32_bf16 v[28:31],v[100:103],v[44:47],v[28:31]
ds_read_b128 v[56:59],v2 offset:48128
v_mfma_f32_16x16x32_bf16 v[32:35],v[104:107],v[44:47],v[32:35]
ds_read_b128 v[88:91],v10 offset:0
v_mfma_f32_16x16x32_bf16 v[36:39],v[108:111],v[44:47],v[36:39]
v_mfma_f32_16x16x32_bf16 v[40:43],v[112:115],v[44:47],v[40:43]
s_waitcnt lgkmcnt(3)
buffer_load_dwordx4 v1,s[36:39],s61 offen offset:2048 sc1 lds
v_mfma_f32_16x16x32_bf16 v[28:31],v[116:119],v[48:51],v[28:31]
ds_read_b128 v[44:47],v2 offset:49152
v_mfma_f32_16x16x32_bf16 v[32:35],v[120:123],v[48:51],v[32:35]
ds_read_b128 v[76:79],v10 offset:1024
v_mfma_f32_16x16x32_bf16 v[36:39],v[124:127],v[48:51],v[36:39]
v_mfma_f32_16x16x32_bf16 v[40:43],v[128:131],v[48:51],v[40:43]
s_waitcnt lgkmcnt(4)
v_mfma_f32_16x16x32_bf16 v[28:31],v[132:135],v[52:55],v[28:31]
ds_read_b128 v[48:51],v2 offset:50176
v_mfma_f32_16x16x32_bf16 v[32:35],v[136:139],v[52:55],v[32:35]
ds_read_b128 v[80:83],v10 offset:2048
v_mfma_f32_16x16x32_bf16 v[36:39],v[140:143],v[52:55],v[36:39]
v_mfma_f32_16x16x32_bf16 v[40:43],v[144:147],v[52:55],v[40:43]
s_waitcnt lgkmcnt(4)
v_mfma_f32_16x16x32_bf16 v[28:31],v[148:151],v[56:59],v[28:31]
ds_read_b128 v[52:55],v2 offset:51200
v_mfma_f32_16x16x32_bf16 v[32:35],v[152:155],v[56:59],v[32:35]
ds_read_b128 v[84:87],v10 offset:3072
v_mfma_f32_16x16x32_bf16 v[36:39],v[156:159],v[56:59],v[36:39]
v_mfma_f32_16x16x32_bf16 v[40:43],v[88:91],v[56:59],v[40:43]
s_waitcnt lgkmcnt(4)
v_mfma_f32_16x16x32_bf16 v[28:31],v[160:163],v[44:47],v[28:31]
ds_read_b128 v[56:59],v2 offset:52224
v_mfma_f32_16x16x32_bf16 v[32:35],v[164:167],v[44:47],v[32:35]
ds_read_b128 v[88:91],v10 offset:4096
v_mfma_f32_16x16x32_bf16 v[36:39],v[168:171],v[44:47],v[36:39]
v_mfma_f32_16x16x32_bf16 v[40:43],v[76:79],v[44:47],v[40:43]
s_waitcnt lgkmcnt(4)
v_mfma_f32_16x16x32_bf16 v[28:31],v[172:175],v[48:51],v[28:31]
ds_read_b128 v[44:47],v2 offset:53248
v_mfma_f32_16x16x32_bf16 v[32:35],v[176:179],v[48:51],v[32:35]
ds_read_b128 v[76:79],v10 offset:5120
v_mfma_f32_16x16x32_bf16 v[36:39],v[180:183],v[48:51],v[36:39]
s_waitcnt vmcnt(8) lgkmcnt(4)
v_mfma_f32_16x16x32_bf16 v[40:43],v[80:83],v[48:51],v[40:43]
s_barrier
v_mfma_f32_16x16x32_bf16 v[28:31],v[184:187],v[52:55],v[28:31]
ds_read_b128 v[48:51],v2 offset:54272
v_mfma_f32_16x16x32_bf16 v[32:35],v[188:191],v[52:55],v[32:35]
ds_read_b128 v[80:83],v10 offset:6144
v_mfma_f32_16x16x32_bf16 v[36:39],v[192:195],v[52:55],v[36:39]
v_mfma_f32_16x16x32_bf16 v[40:43],v[84:87],v[52:55],v[40:43]
s_waitcnt lgkmcnt(4)
v_mfma_f32_16x16x32_bf16 v[28:31],v[196:199],v[56:59],v[28:31]
ds_read_b128 v[52:55],v2 offset:55296
v_mfma_f32_16x16x32_bf16 v[32:35],v[200:203],v[56:59],v[32:35]
ds_read_b128 v[84:87],v10 offset:7168
v_mfma_f32_16x16x32_bf16 v[36:39],v[204:207],v[56:59],v[36:39]
v_mfma_f32_16x16x32_bf16 v[40:43],v[88:91],v[56:59],v[40:43]
s_waitcnt lgkmcnt(4)
v_mfma_f32_16x16x32_bf16 v[28:31],v[208:211],v[44:47],v[28:31]
ds_read_b128 v[56:59],v2 offset:56320
v_mfma_f32_16x16x32_bf16 v[32:35],v[212:215],v[44:47],v[32:35]
ds_read_b128 v[88:91],v10 offset:8192
v_mfma_f32_16x16x32_bf16 v[36:39],v[216:219],v[44:47],v[36:39]
s_waitcnt vmcnt(3) lgkmcnt(4)
v_mfma_f32_16x16x32_bf16 v[40:43],v[76:79],v[44:47],v[40:43]
s_barrier
v_mfma_f32_16x16x32_bf16 v[28:31],v[220:223],v[48:51],v[28:31]
ds_read_b128 v[44:47],v2 offset:0
v_mfma_f32_16x16x32_bf16 v[32:35],v[224:227],v[48:51],v[32:35]
v_mfma_f32_16x16x32_bf16 v[36:39],v[228:231],v[48:51],v[36:39]
v_mfma_f32_16x16x32_bf16 v[40:43],v[80:83],v[48:51],v[40:43]
s_waitcnt lgkmcnt(3)
v_mfma_f32_16x16x32_bf16 v[28:31],v[232:235],v[52:55],v[28:31]
ds_read_b128 v[48:51],v2 offset:1024
v_mfma_f32_16x16x32_bf16 v[32:35],v[236:239],v[52:55],v[32:35]
v_mfma_f32_16x16x32_bf16 v[36:39],v[240:243],v[52:55],v[36:39]
v_mfma_f32_16x16x32_bf16 v[40:43],v[84:87],v[52:55],v[40:43]
s_waitcnt lgkmcnt(2)
v_mfma_f32_16x16x32_bf16 v[28:31],v[244:247],v[56:59],v[28:31]
ds_read_b128 v[52:55],v2 offset:2048
v_mfma_f32_16x16x32_bf16 v[32:35],v[248:251],v[56:59],v[32:35]
v_mfma_f32_16x16x32_bf16 v[36:39],v[252:255],v[56:59],v[36:39]
s_waitcnt lgkmcnt(2)
v_mfma_f32_16x16x32_bf16 v[40:43],v[88:91],v[56:59],v[40:43]
s_cbranch_scc1 Lg4_d0w2_%=
s_branch Lg4_slow0_%=
Lg4_d0w3_%=:
v_mfma_f32_16x16x32_bf16 v[12:15],a[0:3],v[44:47],0
s_mov_b32 s73,s47
ds_read_b128 v[56:59],v2 offset:3072
v_lshlrev_b32 v60,16,v68
v_mfma_f32_16x16x32_bf16 v[16:19],a[4:7],v[44:47],0
s_max_u32 s73,s73,s65
v_and_b32 v61,0xffff0000,v68
v_lshlrev_b32 v62,16,v69
v_mfma_f32_16x16x32_bf16 v[20:23],a[8:11],v[44:47],0
s_mov_b32 s65,0
v_and_b32 v63,0xffff0000,v69
v_lshlrev_b32 v64,16,v70
v_mfma_f32_16x16x32_bf16 v[24:27],a[12:15],v[44:47],0
s_waitcnt lgkmcnt(2)
v_and_b32 v65,0xffff0000,v70
v_lshlrev_b32 v66,16,v71
v_mfma_f32_16x16x32_bf16 v[12:15],a[16:19],v[48:51],v[12:15]
s_mul_i32 s61,s49,7
ds_read_b128 v[44:47],v2 offset:4096
v_and_b32 v67,0xffff0000,v71
v_mfma_f32_16x16x32_bf16 v[16:19],a[20:23],v[48:51],v[16:19]
s_lshr_b32 s61,s61,1
v_cvt_pk_bf16_f32 v28,v28,v29
v_cvt_pk_bf16_f32 v29,v30,v31
v_mfma_f32_16x16x32_bf16 v[20:23],a[24:27],v[48:51],v[20:23]
v_cvt_pk_bf16_f32 v30,v32,v33
v_cvt_pk_bf16_f32 v31,v34,v35
v_mfma_f32_16x16x32_bf16 v[24:27],a[28:31],v[48:51],v[24:27]
s_waitcnt lgkmcnt(2)
v_add_f32 v36,v36,v60
v_add_f32 v37,v37,v61
v_mfma_f32_16x16x32_bf16 v[12:15],a[32:35],v[52:55],v[12:15]
ds_read_b128 v[48:51],v2 offset:5120
v_add_f32 v38,v38,v62
v_mfma_f32_16x16x32_bf16 v[16:19],a[36:39],v[52:55],v[16:19]
v_add_f32 v39,v39,v63
v_add_f32 v40,v40,v64
v_mfma_f32_16x16x32_bf16 v[20:23],a[40:43],v[52:55],v[20:23]
v_add_f32 v41,v41,v65
v_add_f32 v42,v42,v66
v_mfma_f32_16x16x32_bf16 v[24:27],a[44:47],v[52:55],v[24:27]
s_waitcnt lgkmcnt(2)
buffer_store_dwordx4 v[28:31],v3,s[36:39],s73 offen offset:0 sc0 sc1
v_add_f32 v43,v43,v67
v_cvt_pk_bf16_f32 v36,v36,v37
v_cvt_pk_bf16_f32 v37,v38,v39
v_mfma_f32_16x16x32_bf16 v[12:15],a[48:51],v[56:59],v[12:15]
ds_read_b128 v[52:55],v2 offset:6144
v_cvt_pk_bf16_f32 v38,v40,v41
v_mfma_f32_16x16x32_bf16 v[16:19],a[52:55],v[56:59],v[16:19]
v_cvt_pk_bf16_f32 v39,v42,v43
v_mfma_f32_16x16x32_bf16 v[20:23],a[56:59],v[56:59],v[20:23]
s_waitcnt lgkmcnt(2)
s_nop 0
v_mfma_f32_16x16x32_bf16 v[24:27],a[60:63],v[56:59],v[24:27]
s_barrier
v_mfma_f32_16x16x32_bf16 v[12:15],a[64:67],v[44:47],v[12:15])"
R"(
ds_read_b128 v[56:59],v2 offset:7168
ds_read_b32 v11,v8 offset:4096
v_mfma_f32_16x16x32_bf16 v[16:19],a[68:71],v[44:47],v[16:19]
v_mfma_f32_16x16x32_bf16 v[20:23],a[72:75],v[44:47],v[20:23]
v_mfma_f32_16x16x32_bf16 v[24:27],a[76:79],v[44:47],v[24:27]
s_waitcnt lgkmcnt(3)
v_mfma_f32_16x16x32_bf16 v[12:15],a[80:83],v[48:51],v[12:15]
s_waitcnt lgkmcnt(0)
ds_read_b128 v[44:47],v2 offset:8192
v_mfma_f32_16x16x32_bf16 v[16:19],a[84:87],v[48:51],v[16:19]
v_mfma_f32_16x16x32_bf16 v[20:23],a[88:91],v[48:51],v[20:23]
v_mfma_f32_16x16x32_bf16 v[24:27],a[92:95],v[48:51],v[24:27]
buffer_store_dwordx4 v[36:39],v4,s[36:39],s73 offen offset:0 sc0 sc1
v_cmp_eq_u32 vcc,s60,v11
v_mfma_f32_16x16x32_bf16 v[12:15],a[96:99],v[52:55],v[12:15]
s_cmp_eq_u32 vcc_lo,-1
ds_read_b128 v[48:51],v2 offset:9216
s_cselect_b32 s66,s49,s59
v_mfma_f32_16x16x32_bf16 v[16:19],a[100:103],v[52:55],v[16:19]
s_cselect_b32 s61,s61,s59
s_add_u32 m0,s63,28672
s_add_u32 s53,s52,s55
v_mfma_f32_16x16x32_bf16 v[20:23],a[104:107],v[52:55],v[20:23]
s_cmp_eq_u32 s53,s56
s_cselect_b32 s53,s57,s53
s_cmp_eq_u32 s53,s58
v_mfma_f32_16x16x32_bf16 v[24:27],a[108:111],v[52:55],v[24:27]
buffer_load_dwordx4 v0,s[36:39],s61 offen offset:0 sc1 lds
v_mfma_f32_16x16x32_bf16 v[12:15],a[112:115],v[56:59],v[12:15]
s_cselect_b32 s53,s59,s53
ds_read_b128 v[52:55],v2 offset:10240
v_mfma_f32_16x16x32_bf16 v[16:19],a[116:119],v[56:59],v[16:19]
v_mfma_f32_16x16x32_bf16 v[20:23],a[120:123],v[56:59],v[20:23]
v_mfma_f32_16x16x32_bf16 v[24:27],a[124:127],v[56:59],v[24:27]
s_waitcnt lgkmcnt(2)
buffer_load_dwordx4 v0,s[36:39],s61 offen offset:1024 sc1 lds
v_mfma_f32_16x16x32_bf16 v[12:15],a[128:131],v[44:47],v[12:15]
ds_read_b128 v[56:59],v2 offset:11264
v_mfma_f32_16x16x32_bf16 v[16:19],a[132:135],v[44:47],v[16:19]
v_mfma_f32_16x16x32_bf16 v[20:23],a[136:139],v[44:47],v[20:23]
v_mfma_f32_16x16x32_bf16 v[24:27],a[140:143],v[44:47],v[24:27]
s_waitcnt lgkmcnt(2)
buffer_load_dwordx4 v0,s[36:39],s61 offen offset:2048 sc1 lds
v_mfma_f32_16x16x32_bf16 v[12:15],a[144:147],v[48:51],v[12:15]
ds_read_b128 v[44:47],v2 offset:12288
v_mfma_f32_16x16x32_bf16 v[16:19],a[148:151],v[48:51],v[16:19]
v_mfma_f32_16x16x32_bf16 v[20:23],a[152:155],v[48:51],v[20:23]
v_mfma_f32_16x16x32_bf16 v[24:27],a[156:159],v[48:51],v[24:27]
s_waitcnt lgkmcnt(2)
buffer_load_dwordx4 v0,s[36:39],s61 offen offset:3072 sc1 lds
v_mfma_f32_16x16x32_bf16 v[12:15],a[160:163],v[52:55],v[12:15]
s_add_u32 m0,s64,28672
ds_read_b128 v[48:51],v2 offset:13312
s_lshr_b32 s70,s47,4
v_mfma_f32_16x16x32_bf16 v[16:19],a[164:167],v[52:55],v[16:19]
s_cmp_ge_u32 s66,s59
v_mfma_f32_16x16x32_bf16 v[20:23],a[168:171],v[52:55],v[20:23]
v_mfma_f32_16x16x32_bf16 v[24:27],a[172:175],v[52:55],v[24:27]
s_waitcnt lgkmcnt(2)
buffer_load_dwordx4 v[68:71],v9,s[36:39],s48 offen offset:0 sc1
v_mfma_f32_16x16x32_bf16 v[12:15],a[176:179],v[56:59],v[12:15]
ds_read_b128 v[52:55],v2 offset:14336
v_mfma_f32_16x16x32_bf16 v[16:19],a[180:183],v[56:59],v[16:19]
v_mfma_f32_16x16x32_bf16 v[20:23],a[184:187],v[56:59],v[20:23]
v_mfma_f32_16x16x32_bf16 v[24:27],a[188:191],v[56:59],v[24:27]
s_waitcnt lgkmcnt(2)
v_mfma_f32_16x16x32_bf16 v[12:15],a[192:195],v[44:47],v[12:15]
ds_read_b128 v[56:59],v2 offset:15360
v_mfma_f32_16x16x32_bf16 v[16:19],a[196:199],v[44:47],v[16:19]
v_mfma_f32_16x16x32_bf16 v[20:23],a[200:203],v[44:47],v[20:23]
s_waitcnt vmcnt(8) lgkmcnt(2)
v_mfma_f32_16x16x32_bf16 v[24:27],a[204:207],v[44:47],v[24:27]
s_barrier
v_mfma_f32_16x16x32_bf16 v[12:15],a[208:211],v[48:51],v[12:15]
ds_read_b128 v[44:47],v2 offset:16384
v_mfma_f32_16x16x32_bf16 v[16:19],a[212:215],v[48:51],v[16:19]
v_mfma_f32_16x16x32_bf16 v[20:23],a[216:219],v[48:51],v[20:23]
v_mfma_f32_16x16x32_bf16 v[24:27],a[220:223],v[48:51],v[24:27]
s_waitcnt lgkmcnt(2)
v_mfma_f32_16x16x32_bf16 v[12:15],a[224:227],v[52:55],v[12:15]
buffer_load_dwordx4 v1,s[36:39],s61 offen offset:0 sc1 lds
v_mfma_f32_16x16x32_bf16 v[16:19],a[228:231],v[52:55],v[16:19]
ds_read_b128 v[48:51],v2 offset:17408
v_mfma_f32_16x16x32_bf16 v[20:23],a[232:235],v[52:55],v[20:23]
v_mfma_f32_16x16x32_bf16 v[24:27],a[236:239],v[52:55],v[24:27]
s_waitcnt lgkmcnt(2)
v_mfma_f32_16x16x32_bf16 v[12:15],a[240:243],v[56:59],v[12:15]
ds_read_b128 v[52:55],v2 offset:18432
v_mfma_f32_16x16x32_bf16 v[16:19],a[244:247],v[56:59],v[16:19]
v_mfma_f32_16x16x32_bf16 v[20:23],a[248:251],v[56:59],v[20:23]
buffer_load_dwordx4 v1,s[36:39],s61 offen offset:1024 sc1 lds
v_mfma_f32_16x16x32_bf16 v[24:27],a[252:255],v[56:59],v[24:27]
s_waitcnt lgkmcnt(2)
v_mfma_f32_16x16x32_bf16 v[12:15],v[100:103],v[44:47],v[12:15]
ds_read_b128 v[56:59],v2 offset:19456
v_mfma_f32_16x16x32_bf16 v[16:19],v[104:107],v[44:47],v[16:19]
ds_read_b128 v[88:91],v10 offset:0
v_mfma_f32_16x16x32_bf16 v[20:23],v[108:111],v[44:47],v[20:23]
v_mfma_f32_16x16x32_bf16 v[24:27],v[112:115],v[44:47],v[24:27]
s_waitcnt lgkmcnt(3)
v_mfma_f32_16x16x32_bf16 v[12:15],v[116:119],v[48:51],v[12:15]
buffer_load_dwordx4 v1,s[36:39],s61 offen offset:2048 sc1 lds
v_mfma_f32_16x16x32_bf16 v[16:19],v[120:123],v[48:51],v[16:19]
ds_read_b128 v[44:47],v2 offset:20480
ds_read_b128 v[76:79],v10 offset:1024
v_mfma_f32_16x16x32_bf16 v[20:23],v[124:127],v[48:51],v[20:23]
v_mfma_f32_16x16x32_bf16 v[24:27],v[128:131],v[48:51],v[24:27]
s_waitcnt lgkmcnt(4)
v_mfma_f32_16x16x32_bf16 v[12:15],v[132:135],v[52:55],v[12:15]
ds_read_b128 v[48:51],v2 offset:21504
v_mfma_f32_16x16x32_bf16 v[16:19],v[136:139],v[52:55],v[16:19]
ds_read_b128 v[80:83],v10 offset:2048
v_mfma_f32_16x16x32_bf16 v[20:23],v[140:143],v[52:55],v[20:23]
v_mfma_f32_16x16x32_bf16 v[24:27],v[144:147],v[52:55],v[24:27]
s_waitcnt lgkmcnt(4)
v_mfma_f32_16x16x32_bf16 v[12:15],v[148:151],v[56:59],v[12:15]
ds_read_b128 v[52:55],v2 offset:22528
v_mfma_f32_16x16x32_bf16 v[16:19],v[152:155],v[56:59],v[16:19]
ds_read_b128 v[84:87],v10 offset:3072
v_mfma_f32_16x16x32_bf16 v[20:23],v[156:159],v[56:59],v[20:23]
v_mfma_f32_16x16x32_bf16 v[24:27],v[88:91],v[56:59],v[24:27]
s_waitcnt lgkmcnt(4)
v_mfma_f32_16x16x32_bf16 v[12:15],v[160:163],v[44:47],v[12:15]
ds_read_b128 v[56:59],v2 offset:23552
v_mfma_f32_16x16x32_bf16 v[16:19],v[164:167],v[44:47],v[16:19]
ds_read_b128 v[88:91],v10 offset:4096
v_mfma_f32_16x16x32_bf16 v[20:23],v[168:171],v[44:47],v[20:23]
v_mfma_f32_16x16x32_bf16 v[24:27],v[76:79],v[44:47],v[24:27]
s_waitcnt lgkmcnt(4)
v_mfma_f32_16x16x32_bf16 v[12:15],v[172:175],v[48:51],v[12:15]
ds_read_b128 v[44:47],v2 offset:24576
v_mfma_f32_16x16x32_bf16 v[16:19],v[176:179],v[48:51],v[16:19]
ds_read_b128 v[76:79],v10 offset:5120
v_mfma_f32_16x16x32_bf16 v[20:23],v[180:183],v[48:51],v[20:23]
s_waitcnt vmcnt(8) lgkmcnt(4)
v_mfma_f32_16x16x32_bf16 v[24:27],v[80:83],v[48:51],v[24:27]
s_barrier
v_mfma_f32_16x16x32_bf16 v[12:15],v[184:187],v[52:55],v[12:15]
buffer_store_dword v6,v5,s[36:39],s70 offen offset:0 sc0 sc1
ds_read_b128 v[48:51],v2 offset:25600
v_mfma_f32_16x16x32_bf16 v[16:19],v[188:191],v[52:55],v[16:19]
ds_read_b128 v[80:83],v10 offset:6144
v_mfma_f32_16x16x32_bf16 v[20:23],v[192:195],v[52:55],v[20:23]
v_mfma_f32_16x16x32_bf16 v[24:27],v[84:87],v[52:55],v[24:27]
s_waitcnt lgkmcnt(4)
v_mfma_f32_16x16x32_bf16 v[12:15],v[196:199],v[56:59],v[12:15]
ds_read_b128 v[52:55],v2 offset:26624
v_mfma_f32_16x16x32_bf16 v[16:19],v[200:203],v[56:59],v[16:19]
ds_read_b128 v[84:87],v10 offset:7168
v_mfma_f32_16x16x32_bf16 v[20:23],v[204:207],v[56:59],v[20:23]
v_mfma_f32_16x16x32_bf16 v[24:27],v[88:91],v[56:59],v[24:27]
s_waitcnt lgkmcnt(4)
v_mfma_f32_16x16x32_bf16 v[12:15],v[208:211],v[44:47],v[12:15]
ds_read_b128 v[56:59],v2 offset:27648
v_mfma_f32_16x16x32_bf16 v[16:19],v[212:215],v[44:47],v[16:19]
ds_read_b128 v[88:91],v10 offset:8192
v_mfma_f32_16x16x32_bf16 v[20:23],v[216:219],v[44:47],v[20:23]
s_waitcnt vmcnt(4) lgkmcnt(4)
v_mfma_f32_16x16x32_bf16 v[24:27],v[76:79],v[44:47],v[24:27]
s_barrier
v_mfma_f32_16x16x32_bf16 v[12:15],v[220:223],v[48:51],v[12:15]
ds_read_b128 v[44:47],v2 offset:28672
v_mfma_f32_16x16x32_bf16 v[16:19],v[224:227],v[48:51],v[16:19]
v_mfma_f32_16x16x32_bf16 v[20:23],v[228:231],v[48:51],v[20:23]
v_mfma_f32_16x16x32_bf16 v[24:27],v[80:83],v[48:51],v[24:27]
s_waitcnt lgkmcnt(3)
v_mfma_f32_16x16x32_bf16 v[12:15],v[232:235],v[52:55],v[12:15]
ds_read_b128 v[48:51],v2 offset:29696
v_mfma_f32_16x16x32_bf16 v[16:19],v[236:239],v[52:55],v[16:19]
v_mfma_f32_16x16x32_bf16 v[20:23],v[240:243],v[52:55],v[20:23]
v_mfma_f32_16x16x32_bf16 v[24:27],v[84:87],v[52:55],v[24:27]
s_waitcnt lgkmcnt(2)
v_mfma_f32_16x16x32_bf16 v[12:15],v[244:247],v[56:59],v[12:15]
ds_read_b128 v[52:55],v2 offset:30720
v_mfma_f32_16x16x32_bf16 v[16:19],v[248:251],v[56:59],v[16:19]
v_mfma_f32_16x16x32_bf16 v[20:23],v[252:255],v[56:59],v[20:23]
s_waitcnt lgkmcnt(2)
v_mfma_f32_16x16x32_bf16 v[24:27],v[88:91],v[56:59],v[24:27]
s_cbranch_scc1 Lg4_slow1_%=
Lg4_d1w3_%=:
v_mfma_f32_16x16x32_bf16 v[28:31],a[0:3],v[44:47],0
s_mov_b32 s73,s48
ds_read_b128 v[56:59],v2 offset:31744
v_lshlrev_b32 v60,16,v68
v_mfma_f32_16x16x32_bf16 v[32:35],a[4:7],v[44:47],0
s_max_u32 s73,s73,s65
v_and_b32 v61,0xffff0000,v68
v_lshlrev_b32 v62,16,v69
v_mfma_f32_16x16x32_bf16 v[36:39],a[8:11],v[44:47],0
s_mov_b32 s65,0
v_and_b32 v63,0xffff0000,v69
v_lshlrev_b32 v64,16,v70
v_mfma_f32_16x16x32_bf16 v[40:43],a[12:15],v[44:47],0
s_waitcnt lgkmcnt(2)
v_and_b32 v65,0xffff0000,v70
v_lshlrev_b32 v66,16,v71
v_mfma_f32_16x16x32_bf16 v[28:31],a[16:19],v[48:51],v[28:31]
s_mul_i32 s61,s50,7
ds_read_b128 v[44:47],v2 offset:32768
v_and_b32 v67,0xffff0000,v71)"
R"(
v_mfma_f32_16x16x32_bf16 v[32:35],a[20:23],v[48:51],v[32:35]
s_lshr_b32 s61,s61,1
v_cvt_pk_bf16_f32 v12,v12,v13
v_cvt_pk_bf16_f32 v13,v14,v15
v_mfma_f32_16x16x32_bf16 v[36:39],a[24:27],v[48:51],v[36:39]
s_mov_b64 s[44:45],s[46:47]
v_cvt_pk_bf16_f32 v14,v16,v17
v_cvt_pk_bf16_f32 v15,v18,v19
v_mfma_f32_16x16x32_bf16 v[40:43],a[28:31],v[48:51],v[40:43]
s_waitcnt lgkmcnt(2)
v_add_f32 v20,v20,v60
v_add_f32 v21,v21,v61
v_mfma_f32_16x16x32_bf16 v[28:31],a[32:35],v[52:55],v[28:31]
s_mov_b64 s[46:47],s[48:49]
ds_read_b128 v[48:51],v2 offset:33792
v_add_f32 v22,v22,v62
v_mfma_f32_16x16x32_bf16 v[32:35],a[36:39],v[52:55],v[32:35]
v_add_f32 v23,v23,v63
v_add_f32 v24,v24,v64
v_mfma_f32_16x16x32_bf16 v[36:39],a[40:43],v[52:55],v[36:39]
v_add_f32 v25,v25,v65
v_add_f32 v26,v26,v66
v_mfma_f32_16x16x32_bf16 v[40:43],a[44:47],v[52:55],v[40:43]
s_waitcnt lgkmcnt(2)
buffer_store_dwordx4 v[12:15],v3,s[36:39],s73 offen offset:0 sc0 sc1
v_add_f32 v27,v27,v67
v_cvt_pk_bf16_f32 v20,v20,v21
v_cvt_pk_bf16_f32 v21,v22,v23
v_mfma_f32_16x16x32_bf16 v[28:31],a[48:51],v[56:59],v[28:31]
ds_read_b128 v[52:55],v2 offset:34816
v_cvt_pk_bf16_f32 v22,v24,v25
v_mfma_f32_16x16x32_bf16 v[32:35],a[52:55],v[56:59],v[32:35]
v_cvt_pk_bf16_f32 v23,v26,v27
v_mfma_f32_16x16x32_bf16 v[36:39],a[56:59],v[56:59],v[36:39]
s_waitcnt lgkmcnt(2)
s_nop 0
v_mfma_f32_16x16x32_bf16 v[40:43],a[60:63],v[56:59],v[40:43]
s_barrier
v_mfma_f32_16x16x32_bf16 v[28:31],a[64:67],v[44:47],v[28:31]
ds_read_b128 v[56:59],v2 offset:35840
ds_read_b32 v11,v8 offset:5120
v_mfma_f32_16x16x32_bf16 v[32:35],a[68:71],v[44:47],v[32:35]
v_mfma_f32_16x16x32_bf16 v[36:39],a[72:75],v[44:47],v[36:39]
v_mfma_f32_16x16x32_bf16 v[40:43],a[76:79],v[44:47],v[40:43]
s_waitcnt lgkmcnt(3)
v_mfma_f32_16x16x32_bf16 v[28:31],a[80:83],v[48:51],v[28:31]
s_waitcnt lgkmcnt(0)
ds_read_b128 v[44:47],v2 offset:36864
v_mfma_f32_16x16x32_bf16 v[32:35],a[84:87],v[48:51],v[32:35]
v_mfma_f32_16x16x32_bf16 v[36:39],a[88:91],v[48:51],v[36:39]
v_mfma_f32_16x16x32_bf16 v[40:43],a[92:95],v[48:51],v[40:43]
buffer_store_dwordx4 v[20:23],v4,s[36:39],s73 offen offset:0 sc0 sc1
v_cmp_eq_u32 vcc,s60,v11
v_mfma_f32_16x16x32_bf16 v[28:31],a[96:99],v[52:55],v[28:31]
s_cmp_eq_u32 vcc_lo,-1
ds_read_b128 v[48:51],v2 offset:37888
s_cselect_b32 s67,s50,s59
v_mfma_f32_16x16x32_bf16 v[32:35],a[100:103],v[52:55],v[32:35]
s_cselect_b32 s61,s61,s59
s_add_u32 m0,s63,0
s_add_u32 s74,s53,s55
v_mfma_f32_16x16x32_bf16 v[36:39],a[104:107],v[52:55],v[36:39]
s_cmp_eq_u32 s74,s56
s_cselect_b32 s74,s57,s74
s_cmp_eq_u32 s74,s58
v_mfma_f32_16x16x32_bf16 v[40:43],a[108:111],v[52:55],v[40:43]
buffer_load_dwordx4 v0,s[36:39],s61 offen offset:0 sc1 lds
v_mfma_f32_16x16x32_bf16 v[28:31],a[112:115],v[56:59],v[28:31]
s_cselect_b32 s74,s59,s74
ds_read_b128 v[52:55],v2 offset:38912
v_mfma_f32_16x16x32_bf16 v[32:35],a[116:119],v[56:59],v[32:35]
v_mfma_f32_16x16x32_bf16 v[36:39],a[120:123],v[56:59],v[36:39]
v_mfma_f32_16x16x32_bf16 v[40:43],a[124:127],v[56:59],v[40:43]
s_waitcnt lgkmcnt(2)
buffer_load_dwordx4 v0,s[36:39],s61 offen offset:1024 sc1 lds
v_mfma_f32_16x16x32_bf16 v[28:31],a[128:131],v[44:47],v[28:31]
ds_read_b128 v[56:59],v2 offset:39936
v_mfma_f32_16x16x32_bf16 v[32:35],a[132:135],v[44:47],v[32:35]
v_mfma_f32_16x16x32_bf16 v[36:39],a[136:139],v[44:47],v[36:39]
v_mfma_f32_16x16x32_bf16 v[40:43],a[140:143],v[44:47],v[40:43]
s_waitcnt lgkmcnt(2)
buffer_load_dwordx4 v0,s[36:39],s61 offen offset:2048 sc1 lds
v_mfma_f32_16x16x32_bf16 v[28:31],a[144:147],v[48:51],v[28:31]
ds_read_b128 v[44:47],v2 offset:40960
v_mfma_f32_16x16x32_bf16 v[32:35],a[148:151],v[48:51],v[32:35]
v_mfma_f32_16x16x32_bf16 v[36:39],a[152:155],v[48:51],v[36:39]
v_mfma_f32_16x16x32_bf16 v[40:43],a[156:159],v[48:51],v[40:43]
s_waitcnt lgkmcnt(2)
buffer_load_dwordx4 v0,s[36:39],s61 offen offset:3072 sc1 lds
v_mfma_f32_16x16x32_bf16 v[28:31],a[160:163],v[52:55],v[28:31]
s_add_u32 m0,s64,0
ds_read_b128 v[48:51],v2 offset:41984
s_lshr_b32 s70,s48,4
v_mfma_f32_16x16x32_bf16 v[32:35],a[164:167],v[52:55],v[32:35]
s_cmp_lt_u32 s67,s59
v_mfma_f32_16x16x32_bf16 v[36:39],a[168:171],v[52:55],v[36:39]
v_mfma_f32_16x16x32_bf16 v[40:43],a[172:175],v[52:55],v[40:43]
s_waitcnt lgkmcnt(2)
buffer_load_dwordx4 v[68:71],v9,s[36:39],s49 offen offset:0 sc1
v_mfma_f32_16x16x32_bf16 v[28:31],a[176:179],v[56:59],v[28:31]
s_mov_b64 s[48:49],s[50:51]
ds_read_b128 v[52:55],v2 offset:43008
s_mov_b64 s[50:51],s[52:53]
v_mfma_f32_16x16x32_bf16 v[32:35],a[180:183],v[56:59],v[32:35]
s_mov_b32 s52,s74
v_mfma_f32_16x16x32_bf16 v[36:39],a[184:187],v[56:59],v[36:39]
v_mfma_f32_16x16x32_bf16 v[40:43],a[188:191],v[56:59],v[40:43]
s_waitcnt lgkmcnt(2)
v_mfma_f32_16x16x32_bf16 v[28:31],a[192:195],v[44:47],v[28:31]
ds_read_b128 v[56:59],v2 offset:44032
v_mfma_f32_16x16x32_bf16 v[32:35],a[196:199],v[44:47],v[32:35]
v_mfma_f32_16x16x32_bf16 v[36:39],a[200:203],v[44:47],v[36:39]
s_waitcnt vmcnt(8) lgkmcnt(2)
v_mfma_f32_16x16x32_bf16 v[40:43],a[204:207],v[44:47],v[40:43]
s_barrier
v_mfma_f32_16x16x32_bf16 v[28:31],a[208:211],v[48:51],v[28:31]
ds_read_b128 v[44:47],v2 offset:45056
v_mfma_f32_16x16x32_bf16 v[32:35],a[212:215],v[48:51],v[32:35]
v_mfma_f32_16x16x32_bf16 v[36:39],a[216:219],v[48:51],v[36:39]
v_mfma_f32_16x16x32_bf16 v[40:43],a[220:223],v[48:51],v[40:43]
s_waitcnt lgkmcnt(2)
v_mfma_f32_16x16x32_bf16 v[28:31],a[224:227],v[52:55],v[28:31]
buffer_load_dwordx4 v1,s[36:39],s61 offen offset:0 sc1 lds
v_mfma_f32_16x16x32_bf16 v[32:35],a[228:231],v[52:55],v[32:35]
ds_read_b128 v[48:51],v2 offset:46080
v_mfma_f32_16x16x32_bf16 v[36:39],a[232:235],v[52:55],v[36:39]
v_mfma_f32_16x16x32_bf16 v[40:43],a[236:239],v[52:55],v[40:43]
s_waitcnt lgkmcnt(2)
v_mfma_f32_16x16x32_bf16 v[28:31],a[240:243],v[56:59],v[28:31]
ds_read_b128 v[52:55],v2 offset:47104
v_mfma_f32_16x16x32_bf16 v[32:35],a[244:247],v[56:59],v[32:35]
v_mfma_f32_16x16x32_bf16 v[36:39],a[248:251],v[56:59],v[36:39]
buffer_load_dwordx4 v1,s[36:39],s61 offen offset:1024 sc1 lds
v_mfma_f32_16x16x32_bf16 v[40:43],a[252:255],v[56:59],v[40:43]
s_waitcnt lgkmcnt(2)
v_mfma_f32_16x16x32_bf16 v[28:31],v[100:103],v[44:47],v[28:31]
ds_read_b128 v[56:59],v2 offset:48128
v_mfma_f32_16x16x32_bf16 v[32:35],v[104:107],v[44:47],v[32:35]
ds_read_b128 v[88:91],v10 offset:0
v_mfma_f32_16x16x32_bf16 v[36:39],v[108:111],v[44:47],v[36:39]
v_mfma_f32_16x16x32_bf16 v[40:43],v[112:115],v[44:47],v[40:43]
s_waitcnt lgkmcnt(3)
v_mfma_f32_16x16x32_bf16 v[28:31],v[116:119],v[48:51],v[28:31]
buffer_load_dwordx4 v1,s[36:39],s61 offen offset:2048 sc1 lds
v_mfma_f32_16x16x32_bf16 v[32:35],v[120:123],v[48:51],v[32:35]
ds_read_b128 v[44:47],v2 offset:49152
ds_read_b128 v[76:79],v10 offset:1024
v_mfma_f32_16x16x32_bf16 v[36:39],v[124:127],v[48:51],v[36:39]
v_mfma_f32_16x16x32_bf16 v[40:43],v[128:131],v[48:51],v[40:43]
s_waitcnt lgkmcnt(4)
v_mfma_f32_16x16x32_bf16 v[28:31],v[132:135],v[52:55],v[28:31]
ds_read_b128 v[48:51],v2 offset:50176
v_mfma_f32_16x16x32_bf16 v[32:35],v[136:139],v[52:55],v[32:35]
ds_read_b128 v[80:83],v10 offset:2048
v_mfma_f32_16x16x32_bf16 v[36:39],v[140:143],v[52:55],v[36:39]
v_mfma_f32_16x16x32_bf16 v[40:43],v[144:147],v[52:55],v[40:43]
s_waitcnt lgkmcnt(4)
v_mfma_f32_16x16x32_bf16 v[28:31],v[148:151],v[56:59],v[28:31]
ds_read_b128 v[52:55],v2 offset:51200
v_mfma_f32_16x16x32_bf16 v[32:35],v[152:155],v[56:59],v[32:35]
ds_read_b128 v[84:87],v10 offset:3072
v_mfma_f32_16x16x32_bf16 v[36:39],v[156:159],v[56:59],v[36:39]
v_mfma_f32_16x16x32_bf16 v[40:43],v[88:91],v[56:59],v[40:43]
s_waitcnt lgkmcnt(4)
v_mfma_f32_16x16x32_bf16 v[28:31],v[160:163],v[44:47],v[28:31]
ds_read_b128 v[56:59],v2 offset:52224
v_mfma_f32_16x16x32_bf16 v[32:35],v[164:167],v[44:47],v[32:35]
ds_read_b128 v[88:91],v10 offset:4096
v_mfma_f32_16x16x32_bf16 v[36:39],v[168:171],v[44:47],v[36:39]
v_mfma_f32_16x16x32_bf16 v[40:43],v[76:79],v[44:47],v[40:43]
s_waitcnt lgkmcnt(4)
v_mfma_f32_16x16x32_bf16 v[28:31],v[172:175],v[48:51],v[28:31]
ds_read_b128 v[44:47],v2 offset:53248
v_mfma_f32_16x16x32_bf16 v[32:35],v[176:179],v[48:51],v[32:35]
ds_read_b128 v[76:79],v10 offset:5120
v_mfma_f32_16x16x32_bf16 v[36:39],v[180:183],v[48:51],v[36:39]
s_waitcnt vmcnt(8) lgkmcnt(4)
v_mfma_f32_16x16x32_bf16 v[40:43],v[80:83],v[48:51],v[40:43]
s_barrier
v_mfma_f32_16x16x32_bf16 v[28:31],v[184:187],v[52:55],v[28:31]
buffer_store_dword v6,v5,s[36:39],s70 offen offset:0 sc0 sc1
ds_read_b128 v[48:51],v2 offset:54272
v_mfma_f32_16x16x32_bf16 v[32:35],v[188:191],v[52:55],v[32:35]
ds_read_b128 v[80:83],v10 offset:6144
v_mfma_f32_16x16x32_bf16 v[36:39],v[192:195],v[52:55],v[36:39]
v_mfma_f32_16x16x32_bf16 v[40:43],v[84:87],v[52:55],v[40:43]
s_waitcnt lgkmcnt(4)
v_mfma_f32_16x16x32_bf16 v[28:31],v[196:199],v[56:59],v[28:31]
ds_read_b128 v[52:55],v2 offset:55296
v_mfma_f32_16x16x32_bf16 v[32:35],v[200:203],v[56:59],v[32:35]
ds_read_b128 v[84:87],v10 offset:7168
v_mfma_f32_16x16x32_bf16 v[36:39],v[204:207],v[56:59],v[36:39]
v_mfma_f32_16x16x32_bf16 v[40:43],v[88:91],v[56:59],v[40:43]
s_waitcnt lgkmcnt(4)
v_mfma_f32_16x16x32_bf16 v[28:31],v[208:211],v[44:47],v[28:31]
ds_read_b128 v[56:59],v2 offset:56320
v_mfma_f32_16x16x32_bf16 v[32:35],v[212:215],v[44:47],v[32:35]
ds_read_b128 v[88:91],v10 offset:8192
v_mfma_f32_16x16x32_bf16 v[36:39],v[216:219],v[44:47],v[36:39]
s_waitcnt vmcnt(4) lgkmcnt(4))"
R"(
v_mfma_f32_16x16x32_bf16 v[40:43],v[76:79],v[44:47],v[40:43]
s_barrier
v_mfma_f32_16x16x32_bf16 v[28:31],v[220:223],v[48:51],v[28:31]
ds_read_b128 v[44:47],v2 offset:0
v_mfma_f32_16x16x32_bf16 v[32:35],v[224:227],v[48:51],v[32:35]
v_mfma_f32_16x16x32_bf16 v[36:39],v[228:231],v[48:51],v[36:39]
v_mfma_f32_16x16x32_bf16 v[40:43],v[80:83],v[48:51],v[40:43]
s_waitcnt lgkmcnt(3)
v_mfma_f32_16x16x32_bf16 v[28:31],v[232:235],v[52:55],v[28:31]
ds_read_b128 v[48:51],v2 offset:1024
v_mfma_f32_16x16x32_bf16 v[32:35],v[236:239],v[52:55],v[32:35]
v_mfma_f32_16x16x32_bf16 v[36:39],v[240:243],v[52:55],v[36:39]
v_mfma_f32_16x16x32_bf16 v[40:43],v[84:87],v[52:55],v[40:43]
s_waitcnt lgkmcnt(2)
v_mfma_f32_16x16x32_bf16 v[28:31],v[244:247],v[56:59],v[28:31]
ds_read_b128 v[52:55],v2 offset:2048
v_mfma_f32_16x16x32_bf16 v[32:35],v[248:251],v[56:59],v[32:35]
v_mfma_f32_16x16x32_bf16 v[36:39],v[252:255],v[56:59],v[36:39]
s_waitcnt lgkmcnt(2)
v_mfma_f32_16x16x32_bf16 v[40:43],v[88:91],v[56:59],v[40:43]
s_cbranch_scc1 Lg4_d0w3_%=
s_branch Lg4_slow0_%=
Lg4_slow0_%=:
ds_read_b32 v11,v8 offset:6144
s_waitcnt lgkmcnt(0)
v_cmp_eq_u32 vcc,s60,v11
s_nop 0
s_cmp_eq_u32 vcc_lo,-1
s_cbranch_scc1 Lg4_gs0_%=
Lg4_slowb0_%=:
s_cmp_eq_u32 s54,1
s_cselect_b32 s68,s49,s48
s_mul_i32 s68,s68,5
s_lshr_b32 s68,s68,6
s_add_u32 m0,s78,0
s_nop 0
buffer_load_dword v7,s[36:39],s68 offen offset:0 sc1 lds
s_mov_b32 s73,s47
v_lshlrev_b32 v60,16,v68
v_and_b32 v61,0xffff0000,v68
v_lshlrev_b32 v62,16,v69
v_and_b32 v63,0xffff0000,v69
v_lshlrev_b32 v64,16,v70
v_and_b32 v65,0xffff0000,v70
v_lshlrev_b32 v66,16,v71
v_and_b32 v67,0xffff0000,v71
v_cvt_pk_bf16_f32 v28,v28,v29
v_cvt_pk_bf16_f32 v29,v30,v31
v_cvt_pk_bf16_f32 v30,v32,v33
v_cvt_pk_bf16_f32 v31,v34,v35
s_add_u32 m0,s78,1024
s_nop 0
buffer_load_dword v7,s[36:39],s68 offen offset:0 sc1 lds
buffer_store_dwordx4 v[28:31],v3,s[36:39],s73 offen offset:0 sc0 sc1
v_add_f32 v36,v36,v60
v_add_f32 v37,v37,v61
v_add_f32 v38,v38,v62
v_add_f32 v39,v39,v63
v_add_f32 v40,v40,v64
v_add_f32 v41,v41,v65
v_add_f32 v42,v42,v66
v_add_f32 v43,v43,v67
v_cvt_pk_bf16_f32 v36,v36,v37
v_cvt_pk_bf16_f32 v37,v38,v39
v_cvt_pk_bf16_f32 v38,v40,v41
v_cvt_pk_bf16_f32 v39,v42,v43
buffer_store_dwordx4 v[36:39],v4,s[36:39],s73 offen offset:0 sc0 sc1
s_cmp_ge_u32 s48,s59
s_cbranch_scc1 Lg4_done0_%=
s_add_u32 m0,s78,2048
s_nop 0
buffer_load_dword v7,s[36:39],s68 offen offset:0 sc1 lds
s_waitcnt vmcnt(4)
s_barrier
ds_read_b32 v11,v8 offset:0
s_mov_b32 s69,2304
s_waitcnt lgkmcnt(0)
v_cmp_eq_u32 vcc,s60,v11
s_nop 0
s_cmp_eq_u32 vcc_lo,-1
s_cbranch_scc1 Lg4_go0_%=
s_add_u32 m0,s78,3072
s_nop 0
buffer_load_dword v7,s[36:39],s68 offen offset:0 sc1 lds
s_waitcnt vmcnt(4)
s_barrier
ds_read_b32 v11,v8 offset:1024
s_mov_b32 s69,3328
s_waitcnt lgkmcnt(0)
v_cmp_eq_u32 vcc,s60,v11
s_nop 0
s_cmp_eq_u32 vcc_lo,-1
s_cbranch_scc1 Lg4_go0_%=
s_add_u32 m0,s78,0
s_nop 0
buffer_load_dword v7,s[36:39],s68 offen offset:0 sc1 lds
s_waitcnt vmcnt(2)
s_barrier
ds_read_b32 v11,v8 offset:2048
s_lshr_b32 s70,s47,4
buffer_store_dword v6,v5,s[36:39],s70 offen offset:0 sc0 sc1
s_mov_b32 s47,s59
s_mov_b32 s69,256
s_waitcnt lgkmcnt(0)
v_cmp_eq_u32 vcc,s60,v11
s_nop 0
s_cmp_eq_u32 vcc_lo,-1
s_cbranch_scc1 Lg4_go0_%=
s_add_u32 m0,s78,1024
s_nop 0
buffer_load_dword v7,s[36:39],s68 offen offset:0 sc1 lds
s_waitcnt vmcnt(3)
s_barrier
ds_read_b32 v11,v8 offset:3072
s_mov_b32 s69,1280
s_waitcnt lgkmcnt(0)
v_cmp_eq_u32 vcc,s60,v11
s_nop 0
s_cmp_eq_u32 vcc_lo,-1
s_cbranch_scc1 Lg4_go0_%=
Lg4_poll0_%=:
s_add_u32 m0,s78,2048
s_nop 0
buffer_load_dword v7,s[36:39],s68 offen offset:0 sc1 lds
s_waitcnt vmcnt(2)
s_barrier
ds_read_b32 v11,v8 offset:0
s_mov_b32 s69,2304
s_waitcnt lgkmcnt(0)
v_cmp_eq_u32 vcc,s60,v11
s_nop 0
s_cmp_eq_u32 vcc_lo,-1
s_cbranch_scc1 Lg4_go0_%=
s_sleep 4
s_add_u32 m0,s78,3072
s_nop 0
buffer_load_dword v7,s[36:39],s68 offen offset:0 sc1 lds
s_waitcnt vmcnt(2)
s_barrier
ds_read_b32 v11,v8 offset:1024
s_mov_b32 s69,3328
s_waitcnt lgkmcnt(0)
v_cmp_eq_u32 vcc,s60,v11
s_nop 0
s_cmp_eq_u32 vcc_lo,-1
s_cbranch_scc1 Lg4_go0_%=
s_sleep 4
s_add_u32 m0,s78,0
s_nop 0
buffer_load_dword v7,s[36:39],s68 offen offset:0 sc1 lds
s_waitcnt vmcnt(2)
s_barrier
ds_read_b32 v11,v8 offset:2048
s_mov_b32 s69,256
s_waitcnt lgkmcnt(0)
v_cmp_eq_u32 vcc,s60,v11
s_nop 0
s_cmp_eq_u32 vcc_lo,-1
s_cbranch_scc1 Lg4_go0_%=
s_sleep 4
s_add_u32 m0,s78,1024
s_nop 0
buffer_load_dword v7,s[36:39],s68 offen offset:0 sc1 lds
s_waitcnt vmcnt(2)
s_barrier
ds_read_b32 v11,v8 offset:3072
s_mov_b32 s69,1280
s_waitcnt lgkmcnt(0)
v_cmp_eq_u32 vcc,s60,v11
s_nop 0
s_cmp_eq_u32 vcc_lo,-1
s_cbranch_scc1 Lg4_go0_%=
s_sleep 4
s_add_u32 s79,s79,1
s_cmp_lt_u32 s79,20000
s_cbranch_scc1 Lg4_poll0_%=
Lg4_go0_%=:
s_mul_i32 s61,s48,7
s_lshr_b32 s61,s61,1
s_add_u32 m0,s63,0
s_nop 0
buffer_load_dwordx4 v0,s[36:39],s61 offen offset:0 sc1 lds
buffer_load_dwordx4 v0,s[36:39],s61 offen offset:1024 sc1 lds
buffer_load_dwordx4 v0,s[36:39],s61 offen offset:2048 sc1 lds
buffer_load_dwordx4 v0,s[36:39],s61 offen offset:3072 sc1 lds
s_add_u32 m0,s64,0
s_nop 0
buffer_load_dwordx4 v1,s[36:39],s61 offen offset:0 sc1 lds
buffer_load_dwordx4 v1,s[36:39],s61 offen offset:1024 sc1 lds
buffer_load_dwordx4 v1,s[36:39],s61 offen offset:2048 sc1 lds
s_mul_i32 s68,s49,5
s_lshr_b32 s68,s68,6
s_add_u32 m0,s78,4096
s_nop 0
buffer_load_dword v7,s[36:39],s68 offen offset:0 sc1 lds
s_waitcnt vmcnt(4)
s_barrier
ds_read_b128 v[44:47],v2 offset:0
ds_read_b128 v[48:51],v2 offset:1024
ds_read_b128 v[52:55],v2 offset:2048)"
R"(
s_lshr_b32 s70,s47,4
buffer_store_dword v6,v5,s[36:39],s70 offen offset:0 sc0 sc1
s_mov_b32 s47,s59
s_waitcnt lgkmcnt(2)
s_setpc_b64 s[90:91]
Lg4_gs0_%=:
s_mul_i32 s61,s48,7
s_lshr_b32 s61,s61,1
s_cmp_eq_u32 s54,1
s_cselect_b32 s68,s49,s48
s_mul_i32 s68,s68,5
s_lshr_b32 s68,s68,6
s_mov_b32 s73,s47
s_add_u32 m0,s63,0
s_nop 0
buffer_load_dwordx4 v0,s[36:39],s61 offen offset:0 sc1 lds
v_lshlrev_b32 v60,16,v68
v_and_b32 v61,0xffff0000,v68
v_lshlrev_b32 v62,16,v69
v_and_b32 v63,0xffff0000,v69
buffer_load_dwordx4 v0,s[36:39],s61 offen offset:1024 sc1 lds
v_lshlrev_b32 v64,16,v70
v_and_b32 v65,0xffff0000,v70
v_lshlrev_b32 v66,16,v71
v_and_b32 v67,0xffff0000,v71
buffer_load_dwordx4 v0,s[36:39],s61 offen offset:2048 sc1 lds
v_cvt_pk_bf16_f32 v28,v28,v29
v_cvt_pk_bf16_f32 v29,v30,v31
v_cvt_pk_bf16_f32 v30,v32,v33
v_cvt_pk_bf16_f32 v31,v34,v35
buffer_load_dwordx4 v0,s[36:39],s61 offen offset:3072 sc1 lds
buffer_store_dwordx4 v[28:31],v3,s[36:39],s73 offen offset:0 sc0 sc1
v_add_f32 v36,v36,v60
v_add_f32 v37,v37,v61
v_add_f32 v38,v38,v62
s_add_u32 m0,s64,0
s_nop 0
buffer_load_dwordx4 v1,s[36:39],s61 offen offset:0 sc1 lds
v_add_f32 v39,v39,v63
v_add_f32 v40,v40,v64
v_add_f32 v41,v41,v65
v_add_f32 v42,v42,v66
buffer_load_dwordx4 v1,s[36:39],s61 offen offset:1024 sc1 lds
v_add_f32 v43,v43,v67
v_cvt_pk_bf16_f32 v36,v36,v37
v_cvt_pk_bf16_f32 v37,v38,v39
v_cvt_pk_bf16_f32 v38,v40,v41
buffer_load_dwordx4 v1,s[36:39],s61 offen offset:2048 sc1 lds
v_cvt_pk_bf16_f32 v39,v42,v43
buffer_store_dwordx4 v[36:39],v4,s[36:39],s73 offen offset:0 sc0 sc1
s_mul_i32 s68,s49,5
s_lshr_b32 s68,s68,6
s_add_u32 m0,s78,4096
s_nop 0
buffer_load_dword v7,s[36:39],s68 offen offset:0 sc1 lds
s_waitcnt vmcnt(6)
s_barrier
ds_read_b128 v[44:47],v2 offset:0
ds_read_b128 v[48:51],v2 offset:1024
ds_read_b128 v[52:55],v2 offset:2048
s_mov_b32 s65,s59
s_lshr_b32 s70,s59,4
buffer_store_dword v6,v5,s[36:39],s70 offen offset:0 sc0 sc1
s_waitcnt lgkmcnt(2)
s_setpc_b64 s[90:91]
Lg4_done0_%=:
s_waitcnt vmcnt(0)
s_barrier
s_lshr_b32 s70,s47,4
buffer_store_dword v6,v5,s[36:39],s70 offen offset:0 sc0 sc1
s_branch Lg4_done_%=
Lg4_slow1_%=:
ds_read_b32 v11,v8 offset:6144
s_waitcnt lgkmcnt(0)
v_cmp_eq_u32 vcc,s60,v11
s_nop 0
s_cmp_eq_u32 vcc_lo,-1
s_cbranch_scc1 Lg4_gs1_%=
Lg4_slowb1_%=:
s_cmp_eq_u32 s54,1
s_cselect_b32 s68,s50,s49
s_mul_i32 s68,s68,5
s_lshr_b32 s68,s68,6
s_add_u32 m0,s78,0
s_nop 0
buffer_load_dword v7,s[36:39],s68 offen offset:0 sc1 lds
s_mov_b32 s73,s48
v_lshlrev_b32 v60,16,v68
v_and_b32 v61,0xffff0000,v68
v_lshlrev_b32 v62,16,v69
v_and_b32 v63,0xffff0000,v69
v_lshlrev_b32 v64,16,v70
v_and_b32 v65,0xffff0000,v70
v_lshlrev_b32 v66,16,v71
v_and_b32 v67,0xffff0000,v71
v_cvt_pk_bf16_f32 v12,v12,v13
v_cvt_pk_bf16_f32 v13,v14,v15
v_cvt_pk_bf16_f32 v14,v16,v17
v_cvt_pk_bf16_f32 v15,v18,v19
s_add_u32 m0,s78,1024
s_nop 0
buffer_load_dword v7,s[36:39],s68 offen offset:0 sc1 lds
buffer_store_dwordx4 v[12:15],v3,s[36:39],s73 offen offset:0 sc0 sc1
v_add_f32 v20,v20,v60
v_add_f32 v21,v21,v61
v_add_f32 v22,v22,v62
v_add_f32 v23,v23,v63
v_add_f32 v24,v24,v64
v_add_f32 v25,v25,v65
v_add_f32 v26,v26,v66
v_add_f32 v27,v27,v67
v_cvt_pk_bf16_f32 v20,v20,v21
v_cvt_pk_bf16_f32 v21,v22,v23
v_cvt_pk_bf16_f32 v22,v24,v25
v_cvt_pk_bf16_f32 v23,v26,v27
buffer_store_dwordx4 v[20:23],v4,s[36:39],s73 offen offset:0 sc0 sc1
s_cmp_ge_u32 s49,s59
s_cbranch_scc1 Lg4_done1_%=
s_add_u32 m0,s78,2048
s_nop 0
buffer_load_dword v7,s[36:39],s68 offen offset:0 sc1 lds
s_waitcnt vmcnt(4)
s_barrier
ds_read_b32 v11,v8 offset:0
s_mov_b32 s69,2304
s_waitcnt lgkmcnt(0)
v_cmp_eq_u32 vcc,s60,v11
s_nop 0
s_cmp_eq_u32 vcc_lo,-1
s_cbranch_scc1 Lg4_go1_%=
s_add_u32 m0,s78,3072
s_nop 0
buffer_load_dword v7,s[36:39],s68 offen offset:0 sc1 lds
s_waitcnt vmcnt(4)
s_barrier
ds_read_b32 v11,v8 offset:1024
s_mov_b32 s69,3328
s_waitcnt lgkmcnt(0)
v_cmp_eq_u32 vcc,s60,v11
s_nop 0
s_cmp_eq_u32 vcc_lo,-1
s_cbranch_scc1 Lg4_go1_%=
s_add_u32 m0,s78,0
s_nop 0
buffer_load_dword v7,s[36:39],s68 offen offset:0 sc1 lds
s_waitcnt vmcnt(2)
s_barrier
ds_read_b32 v11,v8 offset:2048
s_lshr_b32 s70,s48,4
buffer_store_dword v6,v5,s[36:39],s70 offen offset:0 sc0 sc1
s_mov_b32 s48,s59
s_mov_b32 s69,256
s_waitcnt lgkmcnt(0)
v_cmp_eq_u32 vcc,s60,v11
s_nop 0
s_cmp_eq_u32 vcc_lo,-1
s_cbranch_scc1 Lg4_go1_%=
s_add_u32 m0,s78,1024
s_nop 0
buffer_load_dword v7,s[36:39],s68 offen offset:0 sc1 lds
s_waitcnt vmcnt(3)
s_barrier
ds_read_b32 v11,v8 offset:3072
s_mov_b32 s69,1280
s_waitcnt lgkmcnt(0)
v_cmp_eq_u32 vcc,s60,v11
s_nop 0
s_cmp_eq_u32 vcc_lo,-1
s_cbranch_scc1 Lg4_go1_%=
Lg4_poll1_%=:
s_add_u32 m0,s78,2048
s_nop 0
buffer_load_dword v7,s[36:39],s68 offen offset:0 sc1 lds
s_waitcnt vmcnt(2)
s_barrier
ds_read_b32 v11,v8 offset:0
s_mov_b32 s69,2304
s_waitcnt lgkmcnt(0)
v_cmp_eq_u32 vcc,s60,v11
s_nop 0
s_cmp_eq_u32 vcc_lo,-1
s_cbranch_scc1 Lg4_go1_%=
s_sleep 4
s_add_u32 m0,s78,3072
s_nop 0
buffer_load_dword v7,s[36:39],s68 offen offset:0 sc1 lds
s_waitcnt vmcnt(2)
s_barrier
ds_read_b32 v11,v8 offset:1024
s_mov_b32 s69,3328
s_waitcnt lgkmcnt(0)
v_cmp_eq_u32 vcc,s60,v11
s_nop 0
s_cmp_eq_u32 vcc_lo,-1
s_cbranch_scc1 Lg4_go1_%=
s_sleep 4
s_add_u32 m0,s78,0
s_nop 0
buffer_load_dword v7,s[36:39],s68 offen offset:0 sc1 lds
s_waitcnt vmcnt(2))"
R"(
s_barrier
ds_read_b32 v11,v8 offset:2048
s_mov_b32 s69,256
s_waitcnt lgkmcnt(0)
v_cmp_eq_u32 vcc,s60,v11
s_nop 0
s_cmp_eq_u32 vcc_lo,-1
s_cbranch_scc1 Lg4_go1_%=
s_sleep 4
s_add_u32 m0,s78,1024
s_nop 0
buffer_load_dword v7,s[36:39],s68 offen offset:0 sc1 lds
s_waitcnt vmcnt(2)
s_barrier
ds_read_b32 v11,v8 offset:3072
s_mov_b32 s69,1280
s_waitcnt lgkmcnt(0)
v_cmp_eq_u32 vcc,s60,v11
s_nop 0
s_cmp_eq_u32 vcc_lo,-1
s_cbranch_scc1 Lg4_go1_%=
s_sleep 4
s_add_u32 s79,s79,1
s_cmp_lt_u32 s79,20000
s_cbranch_scc1 Lg4_poll1_%=
Lg4_go1_%=:
s_mul_i32 s61,s49,7
s_lshr_b32 s61,s61,1
s_add_u32 m0,s63,28672
s_nop 0
buffer_load_dwordx4 v0,s[36:39],s61 offen offset:0 sc1 lds
buffer_load_dwordx4 v0,s[36:39],s61 offen offset:1024 sc1 lds
buffer_load_dwordx4 v0,s[36:39],s61 offen offset:2048 sc1 lds
buffer_load_dwordx4 v0,s[36:39],s61 offen offset:3072 sc1 lds
s_add_u32 m0,s64,28672
s_nop 0
buffer_load_dwordx4 v1,s[36:39],s61 offen offset:0 sc1 lds
buffer_load_dwordx4 v1,s[36:39],s61 offen offset:1024 sc1 lds
buffer_load_dwordx4 v1,s[36:39],s61 offen offset:2048 sc1 lds
s_mul_i32 s68,s50,5
s_lshr_b32 s68,s68,6
s_add_u32 m0,s78,5120
s_nop 0
buffer_load_dword v7,s[36:39],s68 offen offset:0 sc1 lds
s_waitcnt vmcnt(4)
s_barrier
ds_read_b128 v[44:47],v2 offset:28672
ds_read_b128 v[48:51],v2 offset:29696
ds_read_b128 v[52:55],v2 offset:30720
s_lshr_b32 s70,s48,4
buffer_store_dword v6,v5,s[36:39],s70 offen offset:0 sc0 sc1
s_mov_b32 s48,s59
s_waitcnt lgkmcnt(2)
s_setpc_b64 s[92:93]
Lg4_gs1_%=:
s_mul_i32 s61,s49,7
s_lshr_b32 s61,s61,1
s_cmp_eq_u32 s54,1
s_cselect_b32 s68,s50,s49
s_mul_i32 s68,s68,5
s_lshr_b32 s68,s68,6
s_mov_b32 s73,s48
s_add_u32 m0,s63,28672
s_nop 0
buffer_load_dwordx4 v0,s[36:39],s61 offen offset:0 sc1 lds
v_lshlrev_b32 v60,16,v68
v_and_b32 v61,0xffff0000,v68
v_lshlrev_b32 v62,16,v69
v_and_b32 v63,0xffff0000,v69
buffer_load_dwordx4 v0,s[36:39],s61 offen offset:1024 sc1 lds
v_lshlrev_b32 v64,16,v70
v_and_b32 v65,0xffff0000,v70
v_lshlrev_b32 v66,16,v71
v_and_b32 v67,0xffff0000,v71
buffer_load_dwordx4 v0,s[36:39],s61 offen offset:2048 sc1 lds
v_cvt_pk_bf16_f32 v12,v12,v13
v_cvt_pk_bf16_f32 v13,v14,v15
v_cvt_pk_bf16_f32 v14,v16,v17
v_cvt_pk_bf16_f32 v15,v18,v19
buffer_load_dwordx4 v0,s[36:39],s61 offen offset:3072 sc1 lds
buffer_store_dwordx4 v[12:15],v3,s[36:39],s73 offen offset:0 sc0 sc1
v_add_f32 v20,v20,v60
v_add_f32 v21,v21,v61
v_add_f32 v22,v22,v62
s_add_u32 m0,s64,28672
s_nop 0
buffer_load_dwordx4 v1,s[36:39],s61 offen offset:0 sc1 lds
v_add_f32 v23,v23,v63
v_add_f32 v24,v24,v64
v_add_f32 v25,v25,v65
v_add_f32 v26,v26,v66
buffer_load_dwordx4 v1,s[36:39],s61 offen offset:1024 sc1 lds
v_add_f32 v27,v27,v67
v_cvt_pk_bf16_f32 v20,v20,v21
v_cvt_pk_bf16_f32 v21,v22,v23
v_cvt_pk_bf16_f32 v22,v24,v25
buffer_load_dwordx4 v1,s[36:39],s61 offen offset:2048 sc1 lds
v_cvt_pk_bf16_f32 v23,v26,v27
buffer_store_dwordx4 v[20:23],v4,s[36:39],s73 offen offset:0 sc0 sc1
s_mul_i32 s68,s50,5
s_lshr_b32 s68,s68,6
s_add_u32 m0,s78,5120
s_nop 0
buffer_load_dword v7,s[36:39],s68 offen offset:0 sc1 lds
s_waitcnt vmcnt(6)
s_barrier
ds_read_b128 v[44:47],v2 offset:28672
ds_read_b128 v[48:51],v2 offset:29696
ds_read_b128 v[52:55],v2 offset:30720
s_mov_b32 s65,s59
s_lshr_b32 s70,s59,4
buffer_store_dword v6,v5,s[36:39],s70 offen offset:0 sc0 sc1
s_waitcnt lgkmcnt(2)
s_setpc_b64 s[92:93]
Lg4_done1_%=:
s_waitcnt vmcnt(0)
s_barrier
s_lshr_b32 s70,s48,4
buffer_store_dword v6,v5,s[36:39],s70 offen offset:0 sc0 sc1
s_branch Lg4_done_%=
Lg4_done_%=:
s_waitcnt vmcnt(0) lgkmcnt(0)
s_barrier
s_endpgm
Lcode_g4_e:)"
  :: "s"(ws), "s"(wave), "s"(sStep), "s"(sX1), "s"(sX2), "s"(sU0), "s"(sWw), "s"(sBuf), "v"(vSrc), "v"(vSrc2), "v"(vA), "v"(vOut), "v"(vOut2), "v"(vFlag), "v"(vPollOff), "v"(vSnapRd), "v"(vAuxS), "v"(vWd0), "v"(vWd1), "v"(vWr0), "v"(vWr1), "v"(vWr2), "s"(w4) : "v0", "v1", "v2", "v3", "v4", "v5", "v6", "v7", "v8", "v9", "v10", "v11", "v12", "v13", "v14", "v15", "v16", "v17", "v18", "v19", "v20", "v21", "v22", "v23", "v24", "v25", "v26", "v27", "v28", "v29", "v30", "v31", "v32", "v33", "v34", "v35", "v36", "v37", "v38", "v39", "v40", "v41", "v42", "v43", "v44", "v45", "v46", "v47", "v48", "v49", "v50", "v51", "v52", "v53", "v54", "v55", "v56", "v57", "v58", "v59", "v60", "v61", "v62", "v63", "v64", "v65", "v66", "v67", "v68", "v69", "v70", "v71", "v72", "v73", "v74", "v75", "v76", "v77", "v78", "v79", "v80", "v81", "v82", "v83", "v84", "v85", "v86", "v87", "v88", "v89", "v90", "v91", "v92", "v93", "v94", "v95", "v96", "v97", "v98", "v99", "v100", "v101", "v102", "v103", "v104", "v105", "v106", "v107", "v108", "v109", "v110", "v111", "v112", "v113", "v114", "v115", "v116", "v117", "v118", "v119", "v120", "v121", "v122", "v123", "v124", "v125", "v126", "v127", "v128", "v129", "v130", "v131", "v132", "v133", "v134", "v135", "v136", "v137", "v138", "v139", "v140", "v141", "v142", "v143", "v144", "v145", "v146", "v147", "v148", "v149", "v150", "v151", "v152", "v153", "v154", "v155", "v156", "v157", "v158", "v159", "v160", "v161", "v162", "v163", "v164", "v165", "v166", "v167", "v168", "v169", "v170", "v171", "v172", "v173", "v174", "v175", "v176", "v177", "v178", "v179", "v180", "v181", "v182", "v183", "v184", "v185", "v186", "v187", "v188", "v189", "v190", "v191", "v192", "v193", "v194", "v195", "v196", "v197", "v198", "v199", "v200", "v201", "v202", "v203", "v204", "v205", "v206", "v207", "v208", "v209", "v210", "v211", "v212", "v213", "v214", "v215", "v216", "v217", "v218", "v219", "v220", "v221", "v222", "v223", "v224", "v225", "v226", "v227", "v228", "v229", "v230", "v231", "v255", "a0", "a1", "a2", "a3", "a4", "a5", "a6", "a7", "a8", "a9", "a10", "a11", "a12", "a13", "a14", "a15", "a16", "a17", "a18", "a19", "a20", "a21", "a22", "a23", "a24", "a25", "a26", "a27", "a28", "a29", "a30", "a31", "a32", "a33", "a34", "a35", "a36", "a37", "a38", "a39", "a40", "a41", "a42", "a43", "a44", "a45", "a46", "a47", "a48", "a49", "a50", "a51", "a52", "a53", "a54", "a55", "a56", "a57", "a58", "a59", "a60", "a61", "a62", "a63", "a64", "a65", "a66", "a67", "a68", "a69", "a70", "a71", "a72", "a73", "a74", "a75", "a76", "a77", "a78", "a79", "a80", "a81", "a82", "a83", "a84", "a85", "a86", "a87", "a88", "a89", "a90", "a91", "a92", "a93", "a94", "a95", "a96", "a97", "a98", "a99", "a100", "a101", "a102", "a103", "a104", "a105", "a106", "a107", "a108", "a109", "a110", "a111", "a112", "a113", "a114", "a115", "a116", "a117", "a118", "a119", "a120", "a121", "a122", "a123", "a124", "a125", "a126", "a127", "a128", "a129", "a130", "a131", "a132", "a133", "a134", "a135", "a136", "a137", "a138", "a139", "a140", "a141", "a142", "a143", "a144", "a145", "a146", "a147", "a148", "a149", "a150", "a151", "a152", "a153", "a154", "a155", "a156", "a157", "a158", "a159", "a160", "a161", "a162", "a163", "a164", "a165", "a166", "a167", "a168", "a169", "a170", "a171", "a172", "a173", "a174", "a175", "a176", "a177", "a178", "a179", "a180", "a181", "a182", "a183", "a184", "a185", "a186", "a187", "a188", "a189", "a190", "a191", "a192", "a193", "a194", "a195", "a196", "a197", "a198", "a199", "a200", "a201", "a202", "a203", "a204", "a205", "a206", "a207", "a208", "a209", "a210", "a211", "a212", "a213", "a214", "a215", "a216", "a217", "a218", "a219", "a220", "a221", "a222", "a223", "a224", "a225", "a226", "a227", "a228", "a229", "a230", "a231", "a232", "a233", "a234", "a235", "a236", "a237", "a238", "a239", "a240", "a241", "a242", "a243", "a244", "a245", "a246", "a247", "a248", "a249", "a250", "a251", "a252", "a253", "a254", "a255", "s36", "s37", "s38", "s39", "s40", "s41", "s42", "s43", "s44", "s45", "s46", "s47", "s48", "s49", "s50", "s51", "s52", "s53", "s54", "s55", "s56", "s57", "s58", "s59", "s60", "s61", "s62", "s63", "s64", "s65", "s66", "s67", "s68", "s69", "s70", "s71", "s72", "s73", "s74", "s75", "s76", "s77", "s78", "s79", "s80", "s81", "s82", "s83", "s84", "s85", "s86", "s87", "s88", "s89", "s90", "s91", "s92", "s93", "s94", "s95", "s96", "s97", "s98", "s99", "vcc", "scc", "memory");
    __builtin_unreachable();
  } else {
   if (kind != 6) {
    COMMON(kind == 1 ? 8 : 16, kind == 1 ? OFF_FXP : OFF_FH, (kind == 1 ? OFF_FH : OFF_FX1) + slice * 128, AUX + 1536, 2048, 2048, 4);
    const int dl = kind == 2 ? ((wave & 2) ? -1 : 1) : 0, r0 = 16 + l15 - dl, r1 = 16 + l15 - 2 * dl;
    const unsigned sb = (kind == 1 ? OFF_XP : OFF_HS) - 32768 + wave * 4096 + lg * 256;
    const unsigned vSrc = sb + (r0 >> 4) * 32768 + (r0 & 15) * 16, vSrc2 = sb + 16384 + (r1 >> 4) * 32768 + (r1 & 15) * 16;
    const unsigned vOut = (kind == 1 ? OFF_HS : OFF_X1) + (slice * 4 + wave) * 1024 + vA;
    const unsigned vX = AUX + wave * 2048 + (kind == 1 ? l15 * 16 : vA), vX2 = AUX + wave * 2048 + l15 * 8;
    const unsigned vAuxS = kind == 1 ? OFF_S1 + ln * 4 : OFF_XP + (slice * 4 + wave) * 1024 + vA;
    const unsigned vAuxS2 = ln < 32 ? OFF_S1 - 1024 + 256 + ln * 4 : OOB;
    constexpr unsigned RED = AUX + 24576 + 1536;
    const unsigned vOut2 = wave != 2 ? OOB : kind == 1 ? (slice == 0 && ln < 16 ? OFF_S1 + 256 + ln * 8 : OOB) : ln < 32 ? OFF_ST + slice * 128 + ln * 4 : OOB;
    const unsigned vStW = RED + wave * 4 + (kind == 1 ? l15 * 32 + (lg == lq ? 16 : 0) : (l15 * 2 + (lg & 1)) * 16), vStR = RED + (kind == 1 ? l15 * 32 : (ln & 31) * 16);
    const unsigned sWw = 16 * wave * 2048 + slice * 256;
    const u16* w12 = kind == 1 ? w1 : w2;
    unsigned epOut;
    asm volatile(R"(
Lcode_g12_b:
s_cmp_eq_u32 %8,2
s_cbranch_scc1 Lg2_begin_%=
s_mov_b64 s[36:37],%1
s_mov_b32 s38,122727552
s_mov_b32 s39,0x00020000
s_mov_b32 s54,%2
s_mov_b32 s55,%3
s_mov_b32 s56,%4
s_mov_b32 s58,%5
s_mov_b32 s83,%6
s_mov_b32 s82,%7
s_mov_b32 s84,%8
v_mov_b32 v0,%9
v_mov_b32 v1,%10
v_mov_b32 v2,%11
v_mov_b32 v3,%12
v_mov_b32 v4,%13
v_mov_b32 v6,%14
v_mov_b32 v7,%15
v_mov_b32 v8,%16
v_mov_b32 v9,%17
v_mov_b32 v10,%18
v_mov_b32 v11,%19
v_mov_b32 v12,%20
v_mov_b32 v13,%21
v_mov_b32 v14,%22
v_mov_b32 v28,%23
v_mov_b32 v29,%24
v_mov_b32 v30,%25
v_mov_b32 v31,%26
v_mov_b32 v32,%27
s_mov_b64 s[40:41],%28
s_mov_b32 s42,2097152
s_mov_b32 s43,0x00020000
s_lshl_b32 s88,s54,11
s_mov_b32 s69,122727424
buffer_load_dword v5,off,s[36:39],s69 sc1
s_add_u32 m0,s88,0
s_add_u32 s61,s82,0
buffer_load_dwordx4 v28,s[40:43],s61 offen offset:0  lds
s_add_u32 m0,s88,1024
s_nop 0
buffer_load_dwordx4 v29,s[40:43],s61 offen offset:0  lds
s_add_u32 m0,s88,8064
s_nop 0
buffer_load_dwordx4 v28,s[40:43],s61 offen offset:128  lds
s_add_u32 m0,s88,9088
s_nop 0
buffer_load_dwordx4 v29,s[40:43],s61 offen offset:128  lds
s_add_u32 m0,s88,16384
s_add_u32 s61,s82,131072
buffer_load_dwordx4 v28,s[40:43],s61 offen offset:0  lds
s_add_u32 m0,s88,17408
s_nop 0
buffer_load_dwordx4 v29,s[40:43],s61 offen offset:0  lds
s_add_u32 m0,s88,24448
s_nop 0
buffer_load_dwordx4 v28,s[40:43],s61 offen offset:128  lds
s_add_u32 m0,s88,25472
s_nop 0
buffer_load_dwordx4 v29,s[40:43],s61 offen offset:128  lds
s_add_u32 m0,s88,32768
s_add_u32 s61,s82,262144
buffer_load_dwordx4 v28,s[40:43],s61 offen offset:0  lds
s_add_u32 m0,s88,33792
s_nop 0
buffer_load_dwordx4 v29,s[40:43],s61 offen offset:0  lds
s_add_u32 m0,s88,40832
s_nop 0
buffer_load_dwordx4 v28,s[40:43],s61 offen offset:128  lds
s_add_u32 m0,s88,41856
s_nop 0
buffer_load_dwordx4 v29,s[40:43],s61 offen offset:128  lds
s_add_u32 m0,s88,49152
s_add_u32 s61,s82,393216
buffer_load_dwordx4 v28,s[40:43],s61 offen offset:0  lds
s_add_u32 m0,s88,50176
s_nop 0
buffer_load_dwordx4 v29,s[40:43],s61 offen offset:0  lds
s_add_u32 m0,s88,57216
s_nop 0
buffer_load_dwordx4 v28,s[40:43],s61 offen offset:128  lds
s_add_u32 m0,s88,58240
s_nop 0
buffer_load_dwordx4 v29,s[40:43],s61 offen offset:128  lds
s_add_u32 m0,s88,65536
s_add_u32 s61,s82,524288
buffer_load_dwordx4 v28,s[40:43],s61 offen offset:0  lds
s_add_u32 m0,s88,66560
s_nop 0
buffer_load_dwordx4 v29,s[40:43],s61 offen offset:0  lds
s_add_u32 m0,s88,73600
s_nop 0
buffer_load_dwordx4 v28,s[40:43],s61 offen offset:128  lds
s_add_u32 m0,s88,74624
s_nop 0
buffer_load_dwordx4 v29,s[40:43],s61 offen offset:128  lds
s_add_u32 m0,s88,81920
s_add_u32 s61,s82,655360
buffer_load_dwordx4 v28,s[40:43],s61 offen offset:0  lds
s_add_u32 m0,s88,82944
s_nop 0
buffer_load_dwordx4 v29,s[40:43],s61 offen offset:0  lds
s_add_u32 m0,s88,89984
s_nop 0
buffer_load_dwordx4 v28,s[40:43],s61 offen offset:128  lds
s_add_u32 m0,s88,91008
s_nop 0
buffer_load_dwordx4 v29,s[40:43],s61 offen offset:128  lds
s_add_u32 m0,s88,98304
s_add_u32 s61,s82,786432
buffer_load_dwordx4 v28,s[40:43],s61 offen offset:0  lds
s_add_u32 m0,s88,99328
s_nop 0
buffer_load_dwordx4 v29,s[40:43],s61 offen offset:0  lds
s_add_u32 m0,s88,106368
s_nop 0
buffer_load_dwordx4 v28,s[40:43],s61 offen offset:128  lds
s_add_u32 m0,s88,107392
s_nop 0
buffer_load_dwordx4 v29,s[40:43],s61 offen offset:128  lds
v_add_u32 v36,49152,v30
v_add_u32 v37,49152,v31
v_add_u32 v38,98304,v30
v_add_u32 v39,98304,v31
v_mov_b32 v76,0x3f803f80
v_mov_b32 v77,0x3f803f80
v_mov_b32 v78,0x3f803f80
v_mov_b32 v79,0x3f803f80
s_waitcnt vmcnt(24)
s_barrier
s_add_u32 m0,s88,114688
s_add_u32 s61,s82,917504
buffer_load_dwordx4 v28,s[40:43],s61 offen offset:0  lds
s_add_u32 m0,s88,115712
ds_read_b64_tr_b16 a[0:1],v30 offset:0
buffer_load_dwordx4 v29,s[40:43],s61 offen offset:0  lds
s_add_u32 m0,s88,122752
ds_read_b64_tr_b16 a[2:3],v30 offset:512
buffer_load_dwordx4 v28,s[40:43],s61 offen offset:128  lds
s_add_u32 m0,s88,123776
ds_read_b64_tr_b16 a[4:5],v31 offset:0
buffer_load_dwordx4 v29,s[40:43],s61 offen offset:128  lds
ds_read_b64_tr_b16 a[6:7],v31 offset:512
ds_read_b64_tr_b16 a[8:9],v30 offset:4096
ds_read_b64_tr_b16 a[10:11],v30 offset:4608
ds_read_b64_tr_b16 a[12:13],v31 offset:4096
ds_read_b64_tr_b16 a[14:15],v31 offset:4608
s_waitcnt vmcnt(24)
s_barrier
s_add_u32 m0,s88,131072
s_add_u32 s61,s82,1048576
buffer_load_dwordx4 v28,s[40:43],s61 offen offset:0  lds
s_add_u32 m0,s88,132096
ds_read_b64_tr_b16 a[16:17],v30 offset:16384
buffer_load_dwordx4 v29,s[40:43],s61 offen offset:0  lds
s_add_u32 m0,s88,139136
ds_read_b64_tr_b16 a[18:19],v30 offset:16896
buffer_load_dwordx4 v28,s[40:43],s61 offen offset:128  lds
s_add_u32 m0,s88,140160
ds_read_b64_tr_b16 a[20:21],v31 offset:16384
buffer_load_dwordx4 v29,s[40:43],s61 offen offset:128  lds
ds_read_b64_tr_b16 a[22:23],v31 offset:16896
ds_read_b64_tr_b16 a[24:25],v30 offset:20480
ds_read_b64_tr_b16 a[26:27],v30 offset:20992
ds_read_b64_tr_b16 a[28:29],v31 offset:20480
ds_read_b64_tr_b16 a[30:31],v31 offset:20992
s_waitcnt lgkmcnt(8)
v_mfma_f32_16x16x32_bf16 v[20:23],a[0:3],v[76:79],0
v_mfma_f32_16x16x32_bf16 v[24:27],a[4:7],v[76:79],0
v_mfma_f32_16x16x32_bf16 v[20:23],a[8:11],v[76:79],v[20:23]
v_mfma_f32_16x16x32_bf16 v[24:27],a[12:15],v[76:79],v[24:27]
s_waitcnt vmcnt(24)
s_barrier
s_add_u32 m0,s88,0
s_add_u32 s61,s82,1179648
buffer_load_dwordx4 v28,s[40:43],s61 offen offset:0  lds
s_add_u32 m0,s88,1024
ds_read_b64_tr_b16 a[32:33],v30 offset:32768
buffer_load_dwordx4 v29,s[40:43],s61 offen offset:0  lds
s_add_u32 m0,s88,8064
ds_read_b64_tr_b16 a[34:35],v30 offset:33280
buffer_load_dwordx4 v28,s[40:43],s61 offen offset:128  lds
s_add_u32 m0,s88,9088
ds_read_b64_tr_b16 a[36:37],v31 offset:32768
buffer_load_dwordx4 v29,s[40:43],s61 offen offset:128  lds
ds_read_b64_tr_b16 a[38:39],v31 offset:33280
ds_read_b64_tr_b16 a[40:41],v30 offset:36864
ds_read_b64_tr_b16 a[42:43],v30 offset:37376
ds_read_b64_tr_b16 a[44:45],v31 offset:36864
ds_read_b64_tr_b16 a[46:47],v31 offset:37376
s_waitcnt lgkmcnt(8)
v_mfma_f32_16x16x32_bf16 v[20:23],a[16:19],v[76:79],v[20:23]
v_mfma_f32_16x16x32_bf16 v[24:27],a[20:23],v[76:79],v[24:27]
v_mfma_f32_16x16x32_bf16 v[20:23],a[24:27],v[76:79],v[20:23]
v_mfma_f32_16x16x32_bf16 v[24:27],a[28:31],v[76:79],v[24:27]
s_waitcnt vmcnt(24)
s_barrier
s_add_u32 m0,s88,16384)"
R"(
s_add_u32 s61,s82,1310720
buffer_load_dwordx4 v28,s[40:43],s61 offen offset:0  lds
s_add_u32 m0,s88,17408
ds_read_b64_tr_b16 a[48:49],v36 offset:0
buffer_load_dwordx4 v29,s[40:43],s61 offen offset:0  lds
s_add_u32 m0,s88,24448
ds_read_b64_tr_b16 a[50:51],v36 offset:512
buffer_load_dwordx4 v28,s[40:43],s61 offen offset:128  lds
s_add_u32 m0,s88,25472
ds_read_b64_tr_b16 a[52:53],v37 offset:0
buffer_load_dwordx4 v29,s[40:43],s61 offen offset:128  lds
ds_read_b64_tr_b16 a[54:55],v37 offset:512
ds_read_b64_tr_b16 a[56:57],v36 offset:4096
ds_read_b64_tr_b16 a[58:59],v36 offset:4608
ds_read_b64_tr_b16 a[60:61],v37 offset:4096
ds_read_b64_tr_b16 a[62:63],v37 offset:4608
s_waitcnt lgkmcnt(8)
v_mfma_f32_16x16x32_bf16 v[20:23],a[32:35],v[76:79],v[20:23]
v_mfma_f32_16x16x32_bf16 v[24:27],a[36:39],v[76:79],v[24:27]
v_mfma_f32_16x16x32_bf16 v[20:23],a[40:43],v[76:79],v[20:23]
v_mfma_f32_16x16x32_bf16 v[24:27],a[44:47],v[76:79],v[24:27]
s_waitcnt vmcnt(24)
s_barrier
s_add_u32 m0,s88,32768
s_add_u32 s61,s82,1441792
buffer_load_dwordx4 v28,s[40:43],s61 offen offset:0  lds
s_add_u32 m0,s88,33792
ds_read_b64_tr_b16 a[64:65],v36 offset:16384
buffer_load_dwordx4 v29,s[40:43],s61 offen offset:0  lds
s_add_u32 m0,s88,40832
ds_read_b64_tr_b16 a[66:67],v36 offset:16896
buffer_load_dwordx4 v28,s[40:43],s61 offen offset:128  lds
s_add_u32 m0,s88,41856
ds_read_b64_tr_b16 a[68:69],v37 offset:16384
buffer_load_dwordx4 v29,s[40:43],s61 offen offset:128  lds
ds_read_b64_tr_b16 a[70:71],v37 offset:16896
ds_read_b64_tr_b16 a[72:73],v36 offset:20480
ds_read_b64_tr_b16 a[74:75],v36 offset:20992
ds_read_b64_tr_b16 a[76:77],v37 offset:20480
ds_read_b64_tr_b16 a[78:79],v37 offset:20992
s_waitcnt lgkmcnt(8)
v_mfma_f32_16x16x32_bf16 v[20:23],a[48:51],v[76:79],v[20:23]
v_mfma_f32_16x16x32_bf16 v[24:27],a[52:55],v[76:79],v[24:27]
v_mfma_f32_16x16x32_bf16 v[20:23],a[56:59],v[76:79],v[20:23]
v_mfma_f32_16x16x32_bf16 v[24:27],a[60:63],v[76:79],v[24:27]
s_waitcnt vmcnt(24)
s_barrier
s_add_u32 m0,s88,49152
s_add_u32 s61,s82,1572864
buffer_load_dwordx4 v28,s[40:43],s61 offen offset:0  lds
s_add_u32 m0,s88,50176
ds_read_b64_tr_b16 a[80:81],v36 offset:32768
buffer_load_dwordx4 v29,s[40:43],s61 offen offset:0  lds
s_add_u32 m0,s88,57216
ds_read_b64_tr_b16 a[82:83],v36 offset:33280
buffer_load_dwordx4 v28,s[40:43],s61 offen offset:128  lds
s_add_u32 m0,s88,58240
ds_read_b64_tr_b16 a[84:85],v37 offset:32768
buffer_load_dwordx4 v29,s[40:43],s61 offen offset:128  lds
ds_read_b64_tr_b16 a[86:87],v37 offset:33280
ds_read_b64_tr_b16 a[88:89],v36 offset:36864
ds_read_b64_tr_b16 a[90:91],v36 offset:37376
ds_read_b64_tr_b16 a[92:93],v37 offset:36864
ds_read_b64_tr_b16 a[94:95],v37 offset:37376
s_waitcnt lgkmcnt(8)
v_mfma_f32_16x16x32_bf16 v[20:23],a[64:67],v[76:79],v[20:23]
v_mfma_f32_16x16x32_bf16 v[24:27],a[68:71],v[76:79],v[24:27]
v_mfma_f32_16x16x32_bf16 v[20:23],a[72:75],v[76:79],v[20:23]
v_mfma_f32_16x16x32_bf16 v[24:27],a[76:79],v[76:79],v[24:27]
s_waitcnt vmcnt(24)
s_barrier
s_add_u32 m0,s88,65536
s_add_u32 s61,s82,1703936
buffer_load_dwordx4 v28,s[40:43],s61 offen offset:0  lds
s_add_u32 m0,s88,66560
ds_read_b64_tr_b16 a[96:97],v38 offset:0
buffer_load_dwordx4 v29,s[40:43],s61 offen offset:0  lds
s_add_u32 m0,s88,73600
ds_read_b64_tr_b16 a[98:99],v38 offset:512
buffer_load_dwordx4 v28,s[40:43],s61 offen offset:128  lds
s_add_u32 m0,s88,74624
ds_read_b64_tr_b16 a[100:101],v39 offset:0
buffer_load_dwordx4 v29,s[40:43],s61 offen offset:128  lds
ds_read_b64_tr_b16 a[102:103],v39 offset:512
ds_read_b64_tr_b16 a[104:105],v38 offset:4096
ds_read_b64_tr_b16 a[106:107],v38 offset:4608
ds_read_b64_tr_b16 a[108:109],v39 offset:4096
ds_read_b64_tr_b16 a[110:111],v39 offset:4608
s_waitcnt lgkmcnt(8)
v_mfma_f32_16x16x32_bf16 v[20:23],a[80:83],v[76:79],v[20:23]
v_mfma_f32_16x16x32_bf16 v[24:27],a[84:87],v[76:79],v[24:27]
v_mfma_f32_16x16x32_bf16 v[20:23],a[88:91],v[76:79],v[20:23]
v_mfma_f32_16x16x32_bf16 v[24:27],a[92:95],v[76:79],v[24:27]
s_waitcnt vmcnt(24)
s_barrier
s_add_u32 m0,s88,81920
s_add_u32 s61,s82,1835008
buffer_load_dwordx4 v28,s[40:43],s61 offen offset:0  lds
s_add_u32 m0,s88,82944
ds_read_b64_tr_b16 a[112:113],v38 offset:16384
buffer_load_dwordx4 v29,s[40:43],s61 offen offset:0  lds
s_add_u32 m0,s88,89984
ds_read_b64_tr_b16 a[114:115],v38 offset:16896
buffer_load_dwordx4 v28,s[40:43],s61 offen offset:128  lds
s_add_u32 m0,s88,91008
ds_read_b64_tr_b16 a[116:117],v39 offset:16384
buffer_load_dwordx4 v29,s[40:43],s61 offen offset:128  lds
ds_read_b64_tr_b16 a[118:119],v39 offset:16896
ds_read_b64_tr_b16 a[120:121],v38 offset:20480
ds_read_b64_tr_b16 a[122:123],v38 offset:20992
ds_read_b64_tr_b16 a[124:125],v39 offset:20480
ds_read_b64_tr_b16 a[126:127],v39 offset:20992
s_waitcnt lgkmcnt(8)
v_mfma_f32_16x16x32_bf16 v[20:23],a[96:99],v[76:79],v[20:23]
v_mfma_f32_16x16x32_bf16 v[24:27],a[100:103],v[76:79],v[24:27]
v_mfma_f32_16x16x32_bf16 v[20:23],a[104:107],v[76:79],v[20:23]
v_mfma_f32_16x16x32_bf16 v[24:27],a[108:111],v[76:79],v[24:27]
s_waitcnt vmcnt(24)
s_barrier
s_add_u32 m0,s88,98304
s_add_u32 s61,s82,1966080
buffer_load_dwordx4 v28,s[40:43],s61 offen offset:0  lds
s_add_u32 m0,s88,99328
ds_read_b64_tr_b16 a[128:129],v38 offset:32768
buffer_load_dwordx4 v29,s[40:43],s61 offen offset:0  lds
s_add_u32 m0,s88,106368
ds_read_b64_tr_b16 a[130:131],v38 offset:33280
buffer_load_dwordx4 v28,s[40:43],s61 offen offset:128  lds
s_add_u32 m0,s88,107392
ds_read_b64_tr_b16 a[132:133],v39 offset:32768
buffer_load_dwordx4 v29,s[40:43],s61 offen offset:128  lds
ds_read_b64_tr_b16 a[134:135],v39 offset:33280
ds_read_b64_tr_b16 a[136:137],v38 offset:36864
ds_read_b64_tr_b16 a[138:139],v38 offset:37376
ds_read_b64_tr_b16 a[140:141],v39 offset:36864
ds_read_b64_tr_b16 a[142:143],v39 offset:37376
s_waitcnt lgkmcnt(8)
v_mfma_f32_16x16x32_bf16 v[20:23],a[112:115],v[76:79],v[20:23]
v_mfma_f32_16x16x32_bf16 v[24:27],a[116:119],v[76:79],v[24:27]
v_mfma_f32_16x16x32_bf16 v[20:23],a[120:123],v[76:79],v[20:23]
v_mfma_f32_16x16x32_bf16 v[24:27],a[124:127],v[76:79],v[24:27]
s_waitcnt vmcnt(24)
s_barrier
ds_read_b64_tr_b16 a[144:145],v30 offset:0
ds_read_b64_tr_b16 a[146:147],v30 offset:512
ds_read_b64_tr_b16 a[148:149],v31 offset:0
ds_read_b64_tr_b16 a[150:151],v31 offset:512
ds_read_b64_tr_b16 a[152:153],v30 offset:4096
ds_read_b64_tr_b16 a[154:155],v30 offset:4608
ds_read_b64_tr_b16 a[156:157],v31 offset:4096
ds_read_b64_tr_b16 a[158:159],v31 offset:4608
s_waitcnt lgkmcnt(8)
v_mfma_f32_16x16x32_bf16 v[20:23],a[128:131],v[76:79],v[20:23]
v_mfma_f32_16x16x32_bf16 v[24:27],a[132:135],v[76:79],v[24:27]
v_mfma_f32_16x16x32_bf16 v[20:23],a[136:139],v[76:79],v[20:23]
v_mfma_f32_16x16x32_bf16 v[24:27],a[140:143],v[76:79],v[24:27]
s_waitcnt vmcnt(20)
s_barrier
ds_read_b64_tr_b16 a[160:161],v30 offset:16384
ds_read_b64_tr_b16 a[162:163],v30 offset:16896
ds_read_b64_tr_b16 a[164:165],v31 offset:16384
ds_read_b64_tr_b16 a[166:167],v31 offset:16896
ds_read_b64_tr_b16 a[168:169],v30 offset:20480
ds_read_b64_tr_b16 a[170:171],v30 offset:20992
ds_read_b64_tr_b16 a[172:173],v31 offset:20480
ds_read_b64_tr_b16 a[174:175],v31 offset:20992
s_waitcnt lgkmcnt(8)
v_mfma_f32_16x16x32_bf16 v[20:23],a[144:147],v[76:79],v[20:23]
v_mfma_f32_16x16x32_bf16 v[24:27],a[148:151],v[76:79],v[24:27]
v_mfma_f32_16x16x32_bf16 v[20:23],a[152:155],v[76:79],v[20:23]
v_mfma_f32_16x16x32_bf16 v[24:27],a[156:159],v[76:79],v[24:27]
s_waitcnt vmcnt(16)
s_barrier
ds_read_b64_tr_b16 a[176:177],v30 offset:32768
ds_read_b64_tr_b16 a[178:179],v30 offset:33280
ds_read_b64_tr_b16 a[180:181],v31 offset:32768
ds_read_b64_tr_b16 a[182:183],v31 offset:33280
ds_read_b64_tr_b16 a[184:185],v30 offset:36864
ds_read_b64_tr_b16 a[186:187],v30 offset:37376
ds_read_b64_tr_b16 a[188:189],v31 offset:36864
ds_read_b64_tr_b16 a[190:191],v31 offset:37376
s_getpc_b64 s[46:47]
Lg1_cp_%=:
s_add_u32 s46,s46,Lg1_init_%=-Lg1_cp_%=
s_addc_u32 s47,s47,0
s_lshl_b32 s68,s54,13
v_lshl_add_u32 v60,v2,3,s68
v_min_u32 v61,Lg1_done_%=-Lg1_init_%=,v60
global_load_dword v64,v61,s[46:47]
v_add_u32 v60,32768,v60
v_min_u32 v62,Lg1_done_%=-Lg1_init_%=,v60
global_load_dword v65,v62,s[46:47]
s_waitcnt lgkmcnt(8)
v_mfma_f32_16x16x32_bf16 v[20:23],a[160:163],v[76:79],v[20:23]
v_mfma_f32_16x16x32_bf16 v[24:27],a[164:167],v[76:79],v[24:27]
v_mfma_f32_16x16x32_bf16 v[20:23],a[168:171],v[76:79],v[20:23]
v_mfma_f32_16x16x32_bf16 v[24:27],a[172:175],v[76:79],v[24:27]
s_waitcnt vmcnt(14)
s_barrier
ds_read_b64_tr_b16 a[192:193],v36 offset:0)"
R"(
ds_read_b64_tr_b16 a[194:195],v36 offset:512
ds_read_b64_tr_b16 a[196:197],v37 offset:0
ds_read_b64_tr_b16 a[198:199],v37 offset:512
ds_read_b64_tr_b16 a[200:201],v36 offset:4096
ds_read_b64_tr_b16 a[202:203],v36 offset:4608
ds_read_b64_tr_b16 a[204:205],v37 offset:4096
ds_read_b64_tr_b16 a[206:207],v37 offset:4608
s_waitcnt lgkmcnt(8)
v_mfma_f32_16x16x32_bf16 v[20:23],a[176:179],v[76:79],v[20:23]
v_mfma_f32_16x16x32_bf16 v[24:27],a[180:183],v[76:79],v[24:27]
v_mfma_f32_16x16x32_bf16 v[20:23],a[184:187],v[76:79],v[20:23]
v_mfma_f32_16x16x32_bf16 v[24:27],a[188:191],v[76:79],v[24:27]
s_waitcnt vmcnt(10)
s_barrier
ds_read_b64_tr_b16 a[208:209],v36 offset:16384
ds_read_b64_tr_b16 a[210:211],v36 offset:16896
ds_read_b64_tr_b16 a[212:213],v37 offset:16384
ds_read_b64_tr_b16 a[214:215],v37 offset:16896
ds_read_b64_tr_b16 a[216:217],v36 offset:20480
ds_read_b64_tr_b16 a[218:219],v36 offset:20992
ds_read_b64_tr_b16 a[220:221],v37 offset:20480
ds_read_b64_tr_b16 a[222:223],v37 offset:20992
s_waitcnt lgkmcnt(8)
v_mfma_f32_16x16x32_bf16 v[20:23],a[192:195],v[76:79],v[20:23]
v_mfma_f32_16x16x32_bf16 v[24:27],a[196:199],v[76:79],v[24:27]
v_mfma_f32_16x16x32_bf16 v[20:23],a[200:203],v[76:79],v[20:23]
v_mfma_f32_16x16x32_bf16 v[24:27],a[204:207],v[76:79],v[24:27]
s_waitcnt vmcnt(6)
s_barrier
ds_read_b64_tr_b16 a[224:225],v36 offset:32768
ds_read_b64_tr_b16 a[226:227],v36 offset:33280
ds_read_b64_tr_b16 a[228:229],v37 offset:32768
ds_read_b64_tr_b16 a[230:231],v37 offset:33280
ds_read_b64_tr_b16 a[232:233],v36 offset:36864
ds_read_b64_tr_b16 a[234:235],v36 offset:37376
ds_read_b64_tr_b16 a[236:237],v37 offset:36864
ds_read_b64_tr_b16 a[238:239],v37 offset:37376
s_waitcnt lgkmcnt(8)
v_mfma_f32_16x16x32_bf16 v[20:23],a[208:211],v[76:79],v[20:23]
v_mfma_f32_16x16x32_bf16 v[24:27],a[212:215],v[76:79],v[24:27]
v_mfma_f32_16x16x32_bf16 v[20:23],a[216:219],v[76:79],v[20:23]
v_mfma_f32_16x16x32_bf16 v[24:27],a[220:223],v[76:79],v[24:27]
s_waitcnt vmcnt(2)
s_barrier
ds_read_b64_tr_b16 a[240:241],v38 offset:0
ds_read_b64_tr_b16 a[242:243],v38 offset:512
ds_read_b64_tr_b16 a[244:245],v39 offset:0
ds_read_b64_tr_b16 a[246:247],v39 offset:512
ds_read_b64_tr_b16 a[248:249],v38 offset:4096
ds_read_b64_tr_b16 a[250:251],v38 offset:4608
ds_read_b64_tr_b16 a[252:253],v39 offset:4096
ds_read_b64_tr_b16 a[254:255],v39 offset:4608
s_waitcnt lgkmcnt(8)
v_mfma_f32_16x16x32_bf16 v[20:23],a[224:227],v[76:79],v[20:23]
v_mfma_f32_16x16x32_bf16 v[24:27],a[228:231],v[76:79],v[24:27]
v_mfma_f32_16x16x32_bf16 v[20:23],a[232:235],v[76:79],v[20:23]
v_mfma_f32_16x16x32_bf16 v[24:27],a[236:239],v[76:79],v[24:27]
s_waitcnt lgkmcnt(0)
v_mfma_f32_16x16x32_bf16 v[20:23],a[240:243],v[76:79],v[20:23]
v_mfma_f32_16x16x32_bf16 v[24:27],a[244:247],v[76:79],v[24:27]
v_mfma_f32_16x16x32_bf16 v[20:23],a[248:251],v[76:79],v[20:23]
v_mfma_f32_16x16x32_bf16 v[24:27],a[252:255],v[76:79],v[24:27]
s_waitcnt lgkmcnt(0)
s_barrier
v_readfirstlane_b32 s60,v5
s_nop 3
s_add_u32 s60,s60,1
s_cmp_eq_u32 s60,0
s_cselect_b32 s60,1,s60
Lg1_init_%=:
v_mov_b32 v5,s60
s_mov_b32 s59,536870912
s_add_u32 s57,s56,131072
s_mov_b32 s79,0
s_mov_b32 s75,0x3a800000
s_mov_b32 s76,0xc0135761
s_mov_b32 s77,0x3d372713
s_mov_b32 s44,s59
s_mov_b32 s45,s59
s_mov_b32 s46,s59
s_mov_b32 s47,s59
s_mov_b32 s48,s83
s_add_u32 s49,s48,s55
s_cmp_eq_u32 s49,s56
s_cselect_b32 s49,s57,s49
s_cmp_eq_u32 s49,s58
s_cselect_b32 s49,s59,s49
s_add_u32 s50,s49,s55
s_cmp_eq_u32 s50,s56
s_cselect_b32 s50,s57,s50
s_cmp_eq_u32 s50,s58
s_cselect_b32 s50,s59,s50
s_add_u32 s51,s50,s55
s_cmp_eq_u32 s51,s56
s_cselect_b32 s51,s57,s51
s_cmp_eq_u32 s51,s58
s_cselect_b32 s51,s59,s51
s_add_u32 s52,s51,s55
s_cmp_eq_u32 s52,s56
s_cselect_b32 s52,s57,s52
s_cmp_eq_u32 s52,s58
s_cselect_b32 s52,s59,s52
s_lshl_b32 s63,s54,12
s_add_u32 s63,s63,65536
s_add_u32 s64,s63,32768
s_lshl_b32 s88,s54,10
s_add_u32 s88,s88,131072
s_lshl_b32 s78,s54,11
s_add_u32 s78,s78,132608
v_mov_b32 v15,0x3727c5ac
v_mov_b32 v100,0x3f803f80
v_mov_b32 v101,0x3f803f80
v_mov_b32 v102,0x3f803f80
v_mov_b32 v103,0x3f803f80
s_mov_b32 s89,1048576
s_mov_b32 s82,0xaaaaaaaa
s_mov_b32 s83,0xaaaaaaaa
s_mov_b32 s84,0xcccccccc
s_mov_b32 s85,0xcccccccc
s_mov_b32 s98,0x00f0000f
s_mov_b32 s99,0xf0000f00
s_getpc_b64 s[90:91]
Lg1_pc_%=:
s_mov_b32 s68,Lg1_d1w0_%=-Lg1_pc_%=
s_cmp_eq_u32 s54,1
s_cselect_b32 s68,Lg1_d1w1_%=-Lg1_pc_%=,s68
s_cmp_eq_u32 s54,2
s_cselect_b32 s68,Lg1_d1w2_%=-Lg1_pc_%=,s68
s_cmp_eq_u32 s54,3
s_cselect_b32 s68,Lg1_d1w3_%=-Lg1_pc_%=,s68
s_add_u32 s96,s90,s68
s_addc_u32 s97,s91,0
s_mov_b32 s68,Lg1_d0w0_%=-Lg1_pc_%=
s_cmp_eq_u32 s54,1
s_cselect_b32 s68,Lg1_d0w1_%=-Lg1_pc_%=,s68
s_cmp_eq_u32 s54,2
s_cselect_b32 s68,Lg1_d0w2_%=-Lg1_pc_%=,s68
s_cmp_eq_u32 s54,3
s_cselect_b32 s68,Lg1_d0w3_%=-Lg1_pc_%=,s68
s_add_u32 s94,s90,s68
s_addc_u32 s95,s91,0
s_mov_b32 s68,Lg1_a1w0_%=-Lg1_pc_%=
s_cmp_eq_u32 s54,1
s_cselect_b32 s68,Lg1_a1w1_%=-Lg1_pc_%=,s68
s_cmp_eq_u32 s54,2
s_cselect_b32 s68,Lg1_a1w2_%=-Lg1_pc_%=,s68
s_cmp_eq_u32 s54,3
s_cselect_b32 s68,Lg1_a1w3_%=-Lg1_pc_%=,s68
s_add_u32 s92,s90,s68
s_addc_u32 s93,s91,0
s_mov_b32 s68,Lg1_a0w0_%=-Lg1_pc_%=
s_cmp_eq_u32 s54,1
s_cselect_b32 s68,Lg1_a0w1_%=-Lg1_pc_%=,s68
s_cmp_eq_u32 s54,2
s_cselect_b32 s68,Lg1_a0w2_%=-Lg1_pc_%=,s68
s_cmp_eq_u32 s54,3
s_cselect_b32 s68,Lg1_a0w3_%=-Lg1_pc_%=,s68
s_add_u32 s90,s90,s68
s_addc_u32 s91,s91,0
s_mov_b32 s66,s59
s_mov_b32 s65,0
s_mov_b32 s62,s63
s_branch Lg1_slow0_%=
Lg1_a0w0_%=:
v_mfma_f32_16x16x32_bf16 v[28:31],a[0:3],v[44:47],0
s_add_u32 m0,s62,16384
ds_read_b128 v[72:75],v2 offset:7168
s_sub_u32 s69,s66,1048576
v_mfma_f32_16x16x32_bf16 v[32:35],a[4:7],v[44:47],0
buffer_load_dwordx4 v1,s[36:39],s66 offen offset:0 sc1 lds
v_mfma_f32_16x16x32_bf16 v[104:107],v[100:103],v[44:47],0
s_cmp_lt_u32 s69,2097152
s_cselect_b32 s89,3145728,s89
s_sub_u32 s69,s66,3145728
v_mfma_f32_16x16x32_bf16 v[108:111],v[44:47],v[44:47],0
s_cmp_lt_u32 s69,1048576
v_fma_f32 v36,-v96,v20,v36
v_fma_f32 v37,-v96,v21,v37
v_mfma_f32_16x16x32_bf16 v[28:31],a[8:11],v[48:51],v[28:31]
s_cselect_b32 s89,4194304,s89
ds_read_b128 v[44:47],v2 offset:8192
v_fma_f32 v38,-v96,v22,v38
v_mfma_f32_16x16x32_bf16 v[32:35],a[12:15],v[48:51],v[32:35]
buffer_load_dwordx4 v1,s[36:39],s66 offen offset:1024 sc1 lds
v_mfma_f32_16x16x32_bf16 v[104:107],v[100:103],v[48:51],v[104:107]
s_lshr_b32 s73,s47,0
v_fma_f32 v39,-v96,v23,v39
v_mul_f32 v76,v36,v36
v_mfma_f32_16x16x32_bf16 v[108:111],v[48:51],v[48:51],v[108:111]
s_lshr_b32 s72,s66,6
v_mul_f32 v77,v37,v37
v_mul_f32 v78,v38,v38
v_mfma_f32_16x16x32_bf16 v[28:31],a[16:19],v[52:55],v[28:31]
s_lshr_b32 s71,s62,2
ds_read_b128 v[48:51],v2 offset:9216
v_mul_f32 v79,v39,v39
v_mfma_f32_16x16x32_bf16 v[32:35],a[20:23],v[52:55],v[32:35]
buffer_load_dwordx4 v1,s[36:39],s66 offen offset:2048 sc1 lds
v_mfma_f32_16x16x32_bf16 v[104:107],v[100:103],v[52:55],v[104:107]
s_cmp_lt_u32 s66,4194304)"
R"(
v_fma_f32 v76,v98,v76,v97
v_mul_f32 v76,v36,v76
v_mfma_f32_16x16x32_bf16 v[108:111],v[52:55],v[52:55],v[108:111]
s_cselect_b32 s72,s59,s72
v_exp_f32 v76,v76
v_mfma_f32_16x16x32_bf16 v[28:31],a[24:27],v[56:59],v[28:31]
ds_read_b128 v[52:55],v2 offset:10240
v_fma_f32 v76,v76,v99,v99
v_mfma_f32_16x16x32_bf16 v[32:35],a[28:31],v[56:59],v[32:35]
buffer_load_dwordx4 v1,s[36:39],s66 offen offset:3072 sc1 lds
v_mfma_f32_16x16x32_bf16 v[104:107],v[100:103],v[56:59],v[104:107]
s_add_u32 m0,s71,s88
v_rcp_f32 v76,v76
v_mfma_f32_16x16x32_bf16 v[108:111],v[56:59],v[56:59],v[108:111]
s_waitcnt lgkmcnt(3)
buffer_load_dword v10,s[36:39],s72 offen offset:0 sc1 lds
v_mfma_f32_16x16x32_bf16 v[28:31],a[32:35],v[60:63],v[28:31]
s_xor_b32 s87,s64,65536
ds_read_b128 v[56:59],v2 offset:11264
v_fma_f32 v77,v98,v77,v97
v_mfma_f32_16x16x32_bf16 v[32:35],a[36:39],v[60:63],v[32:35]
s_cmp_ge_u32 s66,s59
v_mul_f32 v77,v37,v77
v_fma_f32 v78,v98,v78,v97
v_mfma_f32_16x16x32_bf16 v[28:31],a[40:43],v[64:67],v[28:31]
s_cselect_b32 s86,s49,s50
ds_read_b128 v[60:63],v2 offset:12288
v_mul_f32 v78,v38,v78
v_mfma_f32_16x16x32_bf16 v[32:35],a[44:47],v[64:67],v[32:35]
s_cselect_b32 s87,s87,s63
v_exp_f32 v77,v77
v_mfma_f32_16x16x32_bf16 v[28:31],a[48:51],v[68:71],v[28:31]
s_mov_b32 s62,s87
ds_read_b128 v[64:67],v2 offset:13312
v_fma_f32 v77,v77,v99,v99
v_mfma_f32_16x16x32_bf16 v[32:35],a[52:55],v[68:71],v[32:35]
s_cmp_lt_u32 s86,s89
v_exp_f32 v78,v78
v_mfma_f32_16x16x32_bf16 v[28:31],a[56:59],v[72:75],v[28:31]
ds_read_b128 v[68:71],v2 offset:14336
v_fma_f32 v78,v78,v99,v99
v_mfma_f32_16x16x32_bf16 v[32:35],a[60:63],v[72:75],v[32:35]
s_waitcnt lgkmcnt(3)
v_rcp_f32 v77,v77
v_mfma_f32_16x16x32_bf16 v[28:31],a[64:67],v[44:47],v[28:31]
s_waitcnt vmcnt(10)
ds_read_b128 v[72:75],v2 offset:15360
v_fma_f32 v79,v98,v79,v97
v_mfma_f32_16x16x32_bf16 v[32:35],a[68:71],v[44:47],v[32:35]
s_barrier
v_mfma_f32_16x16x32_bf16 v[28:31],a[72:75],v[48:51],v[28:31]
ds_read_b128 v[44:47],v2 offset:16384
ds_read_b32 v16,v7 offset:0
v_mfma_f32_16x16x32_bf16 v[32:35],a[76:79],v[48:51],v[32:35]
v_rcp_f32 v78,v78
v_mfma_f32_16x16x32_bf16 v[28:31],a[80:83],v[52:55],v[28:31]
ds_read_b128 v[48:51],v2 offset:17408
v_mul_f32 v79,v39,v79
v_mfma_f32_16x16x32_bf16 v[32:35],a[84:87],v[52:55],v[32:35]
v_exp_f32 v79,v79
v_mfma_f32_16x16x32_bf16 v[28:31],a[88:91],v[56:59],v[28:31]
s_waitcnt lgkmcnt(1)
ds_read_b128 v[52:55],v2 offset:18432
v_fma_f32 v79,v79,v99,v99
v_mfma_f32_16x16x32_bf16 v[32:35],a[92:95],v[56:59],v[32:35]
v_cmp_eq_u32 vcc,s60,v16
v_rcp_f32 v79,v79
v_mul_f32 v36,v36,v76
v_mfma_f32_16x16x32_bf16 v[28:31],a[96:99],v[60:63],v[28:31]
s_cselect_b32 vcc_lo,-1,vcc_lo
ds_read_b128 v[56:59],v2 offset:19456
v_mul_f32 v37,v37,v77
v_mfma_f32_16x16x32_bf16 v[32:35],a[100:103],v[60:63],v[32:35]
s_cmp_eq_u32 vcc_lo,-1
v_mul_f32 v38,v38,v78
v_mul_f32 v39,v39,v79
v_mfma_f32_16x16x32_bf16 v[28:31],a[104:107],v[64:67],v[28:31]
s_cselect_b32 s67,s86,s59
ds_read_b128 v[60:63],v2 offset:20480
v_cvt_pk_bf16_f32 v84,v36,v37
v_mfma_f32_16x16x32_bf16 v[32:35],a[108:111],v[64:67],v[32:35]
s_lshr_b32 s69,s50,5
v_cvt_pk_bf16_f32 v85,v38,v39
v_fma_f32 v40,-v96,v24,v40
v_mfma_f32_16x16x32_bf16 v[28:31],a[112:115],v[68:71],v[28:31]
s_cmp_lt_u32 s50,4194304
ds_read_b128 v[64:67],v2 offset:21504
v_fma_f32 v41,-v96,v25,v41
v_mfma_f32_16x16x32_bf16 v[32:35],a[116:119],v[68:71],v[32:35]
s_cselect_b32 s68,0xffffcfff,-1
v_fma_f32 v42,-v96,v26,v42
v_fma_f32 v43,-v96,v27,v43
v_mfma_f32_16x16x32_bf16 v[28:31],a[120:123],v[72:75],v[28:31]
s_and_b32 s68,s68,s69
ds_read_b128 v[68:71],v2 offset:22528
v_mul_f32 v80,v40,v40
v_mfma_f32_16x16x32_bf16 v[32:35],a[124:127],v[72:75],v[32:35]
s_waitcnt lgkmcnt(3)
v_mul_f32 v81,v41,v41
v_mul_f32 v82,v42,v42
v_mfma_f32_16x16x32_bf16 v[28:31],a[128:131],v[44:47],v[28:31]
s_cmp_lt_u32 s50,s89
ds_read_b128 v[72:75],v2 offset:23552
v_mul_f32 v83,v43,v43
v_mfma_f32_16x16x32_bf16 v[32:35],a[132:135],v[44:47],v[32:35]
s_cselect_b32 s68,s59,s68
v_fma_f32 v80,v98,v80,v97
v_mul_f32 v80,v40,v80
v_mfma_f32_16x16x32_bf16 v[104:107],v[100:103],v[44:47],v[104:107]
s_lshr_b32 s69,s51,5
v_exp_f32 v80,v80
v_mfma_f32_16x16x32_bf16 v[108:111],v[44:47],v[44:47],v[108:111]
s_cmp_lt_u32 s51,4194304
v_fma_f32 v80,v80,v99,v99
v_fma_f32 v81,v98,v81,v97
v_mfma_f32_16x16x32_bf16 v[28:31],a[136:139],v[48:51],v[28:31]
s_cselect_b32 s70,0xffffcfff,-1
ds_read_b128 v[44:47],v2 offset:24576
v_mul_f32 v81,v41,v81
v_mfma_f32_16x16x32_bf16 v[32:35],a[140:143],v[48:51],v[32:35]
s_and_b32 s70,s70,s69
v_exp_f32 v81,v81
v_mfma_f32_16x16x32_bf16 v[104:107],v[100:103],v[48:51],v[104:107]
s_cmp_lt_u32 s51,s89
v_rcp_f32 v80,v80
v_mfma_f32_16x16x32_bf16 v[108:111],v[48:51],v[48:51],v[108:111]
s_cselect_b32 s70,s59,s70
v_fma_f32 v81,v81,v99,v99
v_fma_f32 v82,v98,v82,v97
v_mfma_f32_16x16x32_bf16 v[28:31],a[144:147],v[52:55],v[28:31]
s_add_u32 s53,s52,s55
ds_read_b128 v[48:51],v2 offset:25600
v_mul_f32 v82,v42,v82
v_mfma_f32_16x16x32_bf16 v[32:35],a[148:151],v[52:55],v[32:35]
s_cmp_eq_u32 s53,s56
v_exp_f32 v82,v82
v_mfma_f32_16x16x32_bf16 v[104:107],v[100:103],v[52:55],v[104:107]
s_cselect_b32 s53,s57,s53
v_rcp_f32 v81,v81
v_mfma_f32_16x16x32_bf16 v[108:111],v[52:55],v[52:55],v[108:111]
s_cmp_eq_u32 s53,s58
v_fma_f32 v82,v82,v99,v99
v_fma_f32 v83,v98,v83,v97
v_mfma_f32_16x16x32_bf16 v[28:31],a[152:155],v[56:59],v[28:31]
s_cselect_b32 s53,s59,s53
ds_read_b128 v[52:55],v2 offset:26624
v_mul_f32 v83,v43,v83
v_mfma_f32_16x16x32_bf16 v[32:35],a[156:159],v[56:59],v[32:35]
s_xor_b32 s63,s63,65536
v_exp_f32 v83,v83
v_mfma_f32_16x16x32_bf16 v[104:107],v[100:103],v[56:59],v[104:107]
s_cmp_ge_u32 s67,s59
v_rcp_f32 v82,v82
v_mfma_f32_16x16x32_bf16 v[108:111],v[56:59],v[56:59],v[108:111]
s_waitcnt lgkmcnt(3)
v_fma_f32 v83,v83,v99,v99
v_mul_f32 v40,v40,v80
v_mfma_f32_16x16x32_bf16 v[28:31],a[160:163],v[60:63],v[28:31]
s_cselect_b32 s68,s68,s70
ds_read_b128 v[56:59],v2 offset:27648
v_mul_f32 v41,v41,v81
v_mfma_f32_16x16x32_bf16 v[32:35],a[164:167],v[60:63],v[32:35]
s_add_u32 m0,s78,256
v_rcp_f32 v83,v83
v_mfma_f32_16x16x32_bf16 v[28:31],a[168:171],v[64:67],v[28:31]
buffer_load_dword v6,s[36:39],s68 offen offset:0 sc1 lds
v_cndmask_b32 v112,v108,v109,s[82:83]
v_cndmask_b32 v113,v110,v111,s[82:83]
v_cndmask_b32 v112,v112,v113,s[84:85]
v_cndmask_b32 v112,v104,v112,s[98:99]
v_mfma_f32_16x16x32_bf16 v[32:35],a[172:175],v[64:67],v[32:35]
s_mov_b32 m0,s62
ds_read_b128 v[60:63],v2 offset:28672
ds_write_b32 v13,v112 offset:0
v_mfma_f32_16x16x32_bf16 v[28:31],a[176:179],v[68:71],v[28:31]
buffer_load_dwordx4 v0,s[36:39],s67 offen offset:0 sc1 lds
v_mfma_f32_16x16x32_bf16 v[32:35],a[180:183],v[68:71],v[32:35]
s_cmp_ge_u32 s66,s59
ds_read_b128 v[64:67],v2 offset:29696
v_mul_f32 v42,v42,v82
v_mfma_f32_16x16x32_bf16 v[28:31],a[184:187],v[72:75],v[28:31]
ds_read_b128 v[68:71],v2 offset:30720
v_mul_f32 v43,v43,v83
v_mfma_f32_16x16x32_bf16 v[32:35],a[188:191],v[72:75],v[32:35]
s_waitcnt lgkmcnt(4)
v_cvt_pk_bf16_f32 v86,v40,v41
v_cvt_pk_bf16_f32 v87,v42,v43
v_mfma_f32_16x16x32_bf16 v[28:31],a[192:195],v[44:47],v[28:31]
s_waitcnt vmcnt(7) lgkmcnt(2)
ds_read_b128 v[72:75],v2 offset:31744
v_mfma_f32_16x16x32_bf16 v[32:35],a[196:199],v[44:47],v[32:35]
s_barrier
v_mfma_f32_16x16x32_bf16 v[28:31],a[200:203],v[48:51],v[28:31]
buffer_load_dwordx4 v0,s[36:39],s67 offen offset:1024 sc1 lds
v_mfma_f32_16x16x32_bf16 v[32:35],a[204:207],v[48:51],v[32:35]
ds_read_b128 v[44:47],v2 offset:32768
ds_read_b128 v[116:119],v14 offset:0
v_mfma_f32_16x16x32_bf16 v[28:31],a[208:211],v[52:55],v[28:31]
ds_read_b128 v[120:123],v14 offset:16
ds_read_b128 v[48:51],v2 offset:33792)"
R"(
v_mfma_f32_16x16x32_bf16 v[32:35],a[212:215],v[52:55],v[32:35]
v_mfma_f32_16x16x32_bf16 v[28:31],a[216:219],v[56:59],v[28:31]
buffer_load_dwordx4 v0,s[36:39],s67 offen offset:2048 sc1 lds
v_mfma_f32_16x16x32_bf16 v[32:35],a[220:223],v[56:59],v[32:35]
s_waitcnt lgkmcnt(4)
ds_read_b128 v[52:55],v2 offset:34816
v_mfma_f32_16x16x32_bf16 v[28:31],a[224:227],v[60:63],v[28:31]
s_waitcnt lgkmcnt(2)
ds_read_b128 v[56:59],v2 offset:35840
v_add_f32 v116,v116,v117
v_mfma_f32_16x16x32_bf16 v[32:35],a[228:231],v[60:63],v[32:35]
v_add_f32 v118,v118,v119
v_add_f32 v116,v116,v118
v_mfma_f32_16x16x32_bf16 v[28:31],a[232:235],v[64:67],v[28:31]
buffer_load_dwordx4 v0,s[36:39],s67 offen offset:3072 sc1 lds
v_mfma_f32_16x16x32_bf16 v[32:35],a[236:239],v[64:67],v[32:35]
ds_read_b128 v[60:63],v2 offset:36864
v_mul_f32 v92,s75,v116
v_mfma_f32_16x16x32_bf16 v[28:31],a[240:243],v[68:71],v[28:31]
ds_read_b128 v[64:67],v2 offset:37888
v_add_f32 v120,v120,v121
v_mfma_f32_16x16x32_bf16 v[32:35],a[244:247],v[68:71],v[32:35]
v_add_f32 v122,v122,v123
v_add_f32 v120,v120,v122
v_mul_f32 v120,s75,v120
v_fma_f32 v120,-v92,v92,v120
v_max_f32 v120,0,v120
v_add_f32 v115,v15,v120
v_fma_f32 v120,v15,v115,v120
v_rsq_f32 v116,v120
v_mfma_f32_16x16x32_bf16 v[28:31],a[248:251],v[72:75],v[28:31]
s_waitcnt lgkmcnt(2)
ds_read_b128 v[68:71],v2 offset:38912
buffer_store_dwordx4 v[84:87],v3,s[36:39],s73 offen offset:0 sc0 sc1
v_mul_f32 v93,s76,v116
v_mul_f32 v94,v116,v116
v_mul_f32 v95,v120,v116
v_mul_f32 v94,s77,v94
v_mul_f32 v94,v94,v93
v_mfma_f32_16x16x32_bf16 v[32:35],a[252:255],v[72:75],v[32:35]
s_cbranch_scc1 Lg1_slow1_%=
Lg1_a1w0_%=:
v_mfma_f32_16x16x32_bf16 v[36:39],a[0:3],v[44:47],0
s_add_u32 m0,s62,16384
ds_read_b128 v[72:75],v2 offset:39936
s_sub_u32 s69,s67,1048576
v_mfma_f32_16x16x32_bf16 v[40:43],a[4:7],v[44:47],0
buffer_load_dwordx4 v1,s[36:39],s67 offen offset:0 sc1 lds
v_mfma_f32_16x16x32_bf16 v[104:107],v[100:103],v[44:47],0
s_cmp_lt_u32 s69,2097152
s_cselect_b32 s89,3145728,s89
s_sub_u32 s69,s67,3145728
v_mfma_f32_16x16x32_bf16 v[108:111],v[44:47],v[44:47],0
s_cmp_lt_u32 s69,1048576
v_fma_f32 v28,-v92,v20,v28
v_fma_f32 v29,-v92,v21,v29
v_mfma_f32_16x16x32_bf16 v[36:39],a[8:11],v[48:51],v[36:39]
s_cselect_b32 s89,4194304,s89
ds_read_b128 v[44:47],v2 offset:40960
v_fma_f32 v30,-v92,v22,v30
v_mfma_f32_16x16x32_bf16 v[40:43],a[12:15],v[48:51],v[40:43]
buffer_load_dwordx4 v1,s[36:39],s67 offen offset:1024 sc1 lds
v_mfma_f32_16x16x32_bf16 v[104:107],v[100:103],v[48:51],v[104:107]
s_lshr_b32 s73,s48,0
v_fma_f32 v31,-v92,v23,v31
v_mul_f32 v76,v28,v28
v_mfma_f32_16x16x32_bf16 v[108:111],v[48:51],v[48:51],v[108:111]
s_lshr_b32 s72,s67,6
v_mul_f32 v77,v29,v29
v_mul_f32 v78,v30,v30
v_mfma_f32_16x16x32_bf16 v[36:39],a[16:19],v[52:55],v[36:39]
s_lshr_b32 s71,s62,2
ds_read_b128 v[48:51],v2 offset:41984
v_mul_f32 v79,v31,v31
v_mfma_f32_16x16x32_bf16 v[40:43],a[20:23],v[52:55],v[40:43]
buffer_load_dwordx4 v1,s[36:39],s67 offen offset:2048 sc1 lds
v_mfma_f32_16x16x32_bf16 v[104:107],v[100:103],v[52:55],v[104:107]
s_cmp_lt_u32 s67,4194304
v_fma_f32 v76,v94,v76,v93
v_mul_f32 v76,v28,v76
v_mfma_f32_16x16x32_bf16 v[108:111],v[52:55],v[52:55],v[108:111]
s_cselect_b32 s72,s59,s72
v_exp_f32 v76,v76
v_mfma_f32_16x16x32_bf16 v[36:39],a[24:27],v[56:59],v[36:39]
s_mov_b64 s[44:45],s[46:47]
ds_read_b128 v[52:55],v2 offset:43008
v_fma_f32 v76,v76,v95,v95
v_mfma_f32_16x16x32_bf16 v[40:43],a[28:31],v[56:59],v[40:43]
buffer_load_dwordx4 v1,s[36:39],s67 offen offset:3072 sc1 lds
v_mfma_f32_16x16x32_bf16 v[104:107],v[100:103],v[56:59],v[104:107]
s_add_u32 m0,s71,s88
v_rcp_f32 v76,v76
v_mfma_f32_16x16x32_bf16 v[108:111],v[56:59],v[56:59],v[108:111]
s_waitcnt lgkmcnt(3)
buffer_load_dword v10,s[36:39],s72 offen offset:0 sc1 lds
v_mfma_f32_16x16x32_bf16 v[36:39],a[32:35],v[60:63],v[36:39]
s_xor_b32 s87,s63,65536
ds_read_b128 v[56:59],v2 offset:44032
v_fma_f32 v77,v94,v77,v93
v_mfma_f32_16x16x32_bf16 v[40:43],a[36:39],v[60:63],v[40:43]
s_cmp_ge_u32 s67,s59
v_mul_f32 v77,v29,v77
v_fma_f32 v78,v94,v78,v93
v_mfma_f32_16x16x32_bf16 v[36:39],a[40:43],v[64:67],v[36:39]
s_cselect_b32 s86,s50,s51
ds_read_b128 v[60:63],v2 offset:45056
v_mul_f32 v78,v30,v78
v_mfma_f32_16x16x32_bf16 v[40:43],a[44:47],v[64:67],v[40:43]
s_cselect_b32 s87,s87,s64
v_exp_f32 v77,v77
v_mfma_f32_16x16x32_bf16 v[36:39],a[48:51],v[68:71],v[36:39]
s_mov_b32 s62,s87
ds_read_b128 v[64:67],v2 offset:46080
v_fma_f32 v77,v77,v95,v95
v_mfma_f32_16x16x32_bf16 v[40:43],a[52:55],v[68:71],v[40:43]
s_cmp_lt_u32 s86,s89
v_exp_f32 v78,v78
v_mfma_f32_16x16x32_bf16 v[36:39],a[56:59],v[72:75],v[36:39]
s_mov_b64 s[46:47],s[48:49]
ds_read_b128 v[68:71],v2 offset:47104
v_fma_f32 v78,v78,v95,v95
v_mfma_f32_16x16x32_bf16 v[40:43],a[60:63],v[72:75],v[40:43]
s_waitcnt lgkmcnt(3)
v_rcp_f32 v77,v77
v_mfma_f32_16x16x32_bf16 v[36:39],a[64:67],v[44:47],v[36:39]
s_waitcnt vmcnt(10)
ds_read_b128 v[72:75],v2 offset:48128
v_fma_f32 v79,v94,v79,v93
v_mfma_f32_16x16x32_bf16 v[40:43],a[68:71],v[44:47],v[40:43]
s_barrier
v_mfma_f32_16x16x32_bf16 v[36:39],a[72:75],v[48:51],v[36:39]
s_mov_b64 s[48:49],s[50:51]
ds_read_b128 v[44:47],v2 offset:49152
ds_read_b32 v16,v7 offset:256
v_mfma_f32_16x16x32_bf16 v[40:43],a[76:79],v[48:51],v[40:43]
v_rcp_f32 v78,v78
v_mfma_f32_16x16x32_bf16 v[36:39],a[80:83],v[52:55],v[36:39]
ds_read_b128 v[48:51],v2 offset:50176
v_mul_f32 v79,v31,v79
v_mfma_f32_16x16x32_bf16 v[40:43],a[84:87],v[52:55],v[40:43]
v_exp_f32 v79,v79
v_mfma_f32_16x16x32_bf16 v[36:39],a[88:91],v[56:59],v[36:39]
s_waitcnt lgkmcnt(1)
ds_read_b128 v[52:55],v2 offset:51200
v_fma_f32 v79,v79,v95,v95
v_mfma_f32_16x16x32_bf16 v[40:43],a[92:95],v[56:59],v[40:43]
v_cmp_eq_u32 vcc,s60,v16
v_rcp_f32 v79,v79
v_mul_f32 v28,v28,v76
v_mfma_f32_16x16x32_bf16 v[36:39],a[96:99],v[60:63],v[36:39]
s_cselect_b32 vcc_lo,-1,vcc_lo
ds_read_b128 v[56:59],v2 offset:52224
v_mul_f32 v29,v29,v77
v_mfma_f32_16x16x32_bf16 v[40:43],a[100:103],v[60:63],v[40:43]
s_cmp_eq_u32 vcc_lo,-1
v_mul_f32 v30,v30,v78
v_mul_f32 v31,v31,v79
v_mfma_f32_16x16x32_bf16 v[36:39],a[104:107],v[64:67],v[36:39]
s_cselect_b32 s66,s86,s59
ds_read_b128 v[60:63],v2 offset:53248
v_cvt_pk_bf16_f32 v84,v28,v29
v_mfma_f32_16x16x32_bf16 v[40:43],a[108:111],v[64:67],v[40:43]
s_lshr_b32 s69,s51,5
v_cvt_pk_bf16_f32 v85,v30,v31
v_fma_f32 v32,-v92,v24,v32
v_mfma_f32_16x16x32_bf16 v[36:39],a[112:115],v[68:71],v[36:39]
s_cmp_lt_u32 s51,4194304
ds_read_b128 v[64:67],v2 offset:54272
v_fma_f32 v33,-v92,v25,v33
v_mfma_f32_16x16x32_bf16 v[40:43],a[116:119],v[68:71],v[40:43]
s_cselect_b32 s68,0xffffcfff,-1
v_fma_f32 v34,-v92,v26,v34
v_fma_f32 v35,-v92,v27,v35
v_mfma_f32_16x16x32_bf16 v[36:39],a[120:123],v[72:75],v[36:39]
s_and_b32 s68,s68,s69
ds_read_b128 v[68:71],v2 offset:55296
v_mul_f32 v80,v32,v32
v_mfma_f32_16x16x32_bf16 v[40:43],a[124:127],v[72:75],v[40:43]
s_waitcnt lgkmcnt(3)
v_mul_f32 v81,v33,v33
v_mul_f32 v82,v34,v34
v_mfma_f32_16x16x32_bf16 v[36:39],a[128:131],v[44:47],v[36:39]
s_cmp_lt_u32 s51,s89
ds_read_b128 v[72:75],v2 offset:56320
v_mul_f32 v83,v35,v35
v_mfma_f32_16x16x32_bf16 v[40:43],a[132:135],v[44:47],v[40:43]
s_cselect_b32 s68,s59,s68
v_fma_f32 v80,v94,v80,v93
v_mul_f32 v80,v32,v80
v_mfma_f32_16x16x32_bf16 v[104:107],v[100:103],v[44:47],v[104:107]
s_lshr_b32 s69,s52,5
v_exp_f32 v80,v80
v_mfma_f32_16x16x32_bf16 v[108:111],v[44:47],v[44:47],v[108:111]
s_cmp_lt_u32 s52,4194304
v_fma_f32 v80,v80,v95,v95
v_fma_f32 v81,v94,v81,v93
v_mfma_f32_16x16x32_bf16 v[36:39],a[136:139],v[48:51],v[36:39]
s_cselect_b32 s70,0xffffcfff,-1
ds_read_b128 v[44:47],v2 offset:57344
v_mul_f32 v81,v33,v81)"
R"(
v_mfma_f32_16x16x32_bf16 v[40:43],a[140:143],v[48:51],v[40:43]
s_and_b32 s70,s70,s69
v_exp_f32 v81,v81
v_mfma_f32_16x16x32_bf16 v[104:107],v[100:103],v[48:51],v[104:107]
s_cmp_lt_u32 s52,s89
v_rcp_f32 v80,v80
v_mfma_f32_16x16x32_bf16 v[108:111],v[48:51],v[48:51],v[108:111]
s_cselect_b32 s70,s59,s70
v_fma_f32 v81,v81,v95,v95
v_fma_f32 v82,v94,v82,v93
v_mfma_f32_16x16x32_bf16 v[36:39],a[144:147],v[52:55],v[36:39]
s_add_u32 s74,s53,s55
ds_read_b128 v[48:51],v2 offset:58368
v_mul_f32 v82,v34,v82
v_mfma_f32_16x16x32_bf16 v[40:43],a[148:151],v[52:55],v[40:43]
s_cmp_eq_u32 s74,s56
v_exp_f32 v82,v82
v_mfma_f32_16x16x32_bf16 v[104:107],v[100:103],v[52:55],v[104:107]
s_cselect_b32 s74,s57,s74
v_rcp_f32 v81,v81
v_mfma_f32_16x16x32_bf16 v[108:111],v[52:55],v[52:55],v[108:111]
s_cmp_eq_u32 s74,s58
v_fma_f32 v82,v82,v95,v95
v_fma_f32 v83,v94,v83,v93
v_mfma_f32_16x16x32_bf16 v[36:39],a[152:155],v[56:59],v[36:39]
s_cselect_b32 s74,s59,s74
ds_read_b128 v[52:55],v2 offset:59392
v_mul_f32 v83,v35,v83
v_mfma_f32_16x16x32_bf16 v[40:43],a[156:159],v[56:59],v[40:43]
s_xor_b32 s64,s64,65536
v_exp_f32 v83,v83
v_mfma_f32_16x16x32_bf16 v[104:107],v[100:103],v[56:59],v[104:107]
s_cmp_ge_u32 s66,s59
v_rcp_f32 v82,v82
v_mfma_f32_16x16x32_bf16 v[108:111],v[56:59],v[56:59],v[108:111]
s_waitcnt lgkmcnt(3)
v_fma_f32 v83,v83,v95,v95
v_mul_f32 v32,v32,v80
v_mfma_f32_16x16x32_bf16 v[36:39],a[160:163],v[60:63],v[36:39]
s_cselect_b32 s68,s68,s70
ds_read_b128 v[56:59],v2 offset:60416
v_mul_f32 v33,v33,v81
v_mfma_f32_16x16x32_bf16 v[40:43],a[164:167],v[60:63],v[40:43]
s_add_u32 m0,s78,0
v_rcp_f32 v83,v83
v_mfma_f32_16x16x32_bf16 v[36:39],a[168:171],v[64:67],v[36:39]
buffer_load_dword v6,s[36:39],s68 offen offset:0 sc1 lds
v_cndmask_b32 v112,v108,v109,s[82:83]
v_cndmask_b32 v113,v110,v111,s[82:83]
v_cndmask_b32 v112,v112,v113,s[84:85]
v_cndmask_b32 v112,v104,v112,s[98:99]
v_mfma_f32_16x16x32_bf16 v[40:43],a[172:175],v[64:67],v[40:43]
s_mov_b32 m0,s62
ds_read_b128 v[60:63],v2 offset:61440
ds_write_b32 v13,v112 offset:2048
v_mfma_f32_16x16x32_bf16 v[36:39],a[176:179],v[68:71],v[36:39]
buffer_load_dwordx4 v0,s[36:39],s66 offen offset:0 sc1 lds
v_mfma_f32_16x16x32_bf16 v[40:43],a[180:183],v[68:71],v[40:43]
s_mov_b64 s[50:51],s[52:53]
ds_read_b128 v[64:67],v2 offset:62464
v_mul_f32 v34,v34,v82
v_mfma_f32_16x16x32_bf16 v[36:39],a[184:187],v[72:75],v[36:39]
s_mov_b32 s52,s74
ds_read_b128 v[68:71],v2 offset:63488
v_mul_f32 v35,v35,v83
v_mfma_f32_16x16x32_bf16 v[40:43],a[188:191],v[72:75],v[40:43]
s_waitcnt lgkmcnt(4)
v_cvt_pk_bf16_f32 v86,v32,v33
v_cvt_pk_bf16_f32 v87,v34,v35
v_mfma_f32_16x16x32_bf16 v[36:39],a[192:195],v[44:47],v[36:39]
s_waitcnt vmcnt(7) lgkmcnt(2)
ds_read_b128 v[72:75],v2 offset:64512
v_xor_b32 v2,65536,v2
v_mfma_f32_16x16x32_bf16 v[40:43],a[196:199],v[44:47],v[40:43]
s_barrier
v_mfma_f32_16x16x32_bf16 v[36:39],a[200:203],v[48:51],v[36:39]
buffer_load_dwordx4 v0,s[36:39],s66 offen offset:1024 sc1 lds
v_mfma_f32_16x16x32_bf16 v[40:43],a[204:207],v[48:51],v[40:43]
s_cmp_lt_u32 s67,4194304
ds_read_b128 v[44:47],v2 offset:0
ds_read_b128 v[116:119],v14 offset:2048
v_mfma_f32_16x16x32_bf16 v[36:39],a[208:211],v[52:55],v[36:39]
ds_read_b128 v[120:123],v14 offset:2064
ds_read_b128 v[48:51],v2 offset:1024
v_mfma_f32_16x16x32_bf16 v[40:43],a[212:215],v[52:55],v[40:43]
v_xor_b32 v8,16384,v8
v_mfma_f32_16x16x32_bf16 v[36:39],a[216:219],v[56:59],v[36:39]
buffer_load_dwordx4 v0,s[36:39],s66 offen offset:2048 sc1 lds
v_mfma_f32_16x16x32_bf16 v[40:43],a[220:223],v[56:59],v[40:43]
s_waitcnt lgkmcnt(4)
ds_read_b128 v[52:55],v2 offset:2048
v_mfma_f32_16x16x32_bf16 v[36:39],a[224:227],v[60:63],v[36:39]
s_waitcnt lgkmcnt(2)
ds_read_b128 v[56:59],v2 offset:3072
v_add_f32 v116,v116,v117
v_mfma_f32_16x16x32_bf16 v[40:43],a[228:231],v[60:63],v[40:43]
v_add_f32 v118,v118,v119
v_add_f32 v116,v116,v118
v_mfma_f32_16x16x32_bf16 v[36:39],a[232:235],v[64:67],v[36:39]
buffer_load_dwordx4 v0,s[36:39],s66 offen offset:3072 sc1 lds
v_mfma_f32_16x16x32_bf16 v[40:43],a[236:239],v[64:67],v[40:43]
ds_read_b128 v[60:63],v2 offset:4096
v_mul_f32 v96,s75,v116
v_mfma_f32_16x16x32_bf16 v[36:39],a[240:243],v[68:71],v[36:39]
ds_read_b128 v[64:67],v2 offset:5120
v_add_f32 v120,v120,v121
v_mfma_f32_16x16x32_bf16 v[40:43],a[244:247],v[68:71],v[40:43]
v_add_f32 v122,v122,v123
v_add_f32 v120,v120,v122
v_mul_f32 v120,s75,v120
v_fma_f32 v120,-v96,v96,v120
v_max_f32 v120,0,v120
v_add_f32 v115,v15,v120
v_fma_f32 v120,v15,v115,v120
v_rsq_f32 v116,v120
v_mfma_f32_16x16x32_bf16 v[36:39],a[248:251],v[72:75],v[36:39]
s_waitcnt lgkmcnt(2)
ds_read_b128 v[68:71],v2 offset:6144
buffer_store_dwordx4 v[84:87],v3,s[36:39],s73 offen offset:0 sc0 sc1
v_mul_f32 v97,s76,v116
v_mul_f32 v98,v116,v116
v_mul_f32 v99,v120,v116
v_mul_f32 v98,s77,v98
v_mul_f32 v98,v98,v97
v_mfma_f32_16x16x32_bf16 v[40:43],a[252:255],v[72:75],v[40:43]
s_cbranch_scc1 Lg1_a0w0_%=
s_cmp_ge_u32 s48,4194304
s_cselect_b64 s[90:91],s[94:95],s[90:91]
s_cselect_b64 s[92:93],s[96:97],s[92:93]
s_cmp_lt_u32 s67,s59
s_cbranch_scc1 Lg1_d0w0_%=
s_branch Lg1_slow0_%=
Lg1_a0w1_%=:
v_mfma_f32_16x16x32_bf16 v[28:31],a[32:35],v[44:47],0
s_add_u32 m0,s62,16384
ds_read_b128 v[72:75],v2 offset:3072
s_sub_u32 s69,s66,1048576
v_mfma_f32_16x16x32_bf16 v[32:35],a[36:39],v[44:47],0
s_cmp_lt_u32 s69,2097152
s_cselect_b32 s89,3145728,s89
s_sub_u32 s69,s66,3145728
v_mfma_f32_16x16x32_bf16 v[104:107],v[100:103],v[44:47],0
buffer_load_dwordx4 v1,s[36:39],s66 offen offset:0 sc1 lds
v_mfma_f32_16x16x32_bf16 v[108:111],v[44:47],v[44:47],0
s_cmp_lt_u32 s69,1048576
v_fma_f32 v36,-v96,v20,v36
v_fma_f32 v37,-v96,v21,v37
v_mfma_f32_16x16x32_bf16 v[28:31],a[40:43],v[48:51],v[28:31]
s_cselect_b32 s89,4194304,s89
ds_read_b128 v[44:47],v2 offset:12288
v_fma_f32 v38,-v96,v22,v38
v_mfma_f32_16x16x32_bf16 v[32:35],a[44:47],v[48:51],v[32:35]
s_lshr_b32 s73,s47,0
v_fma_f32 v39,-v96,v23,v39
v_mul_f32 v76,v36,v36
v_mfma_f32_16x16x32_bf16 v[104:107],v[100:103],v[48:51],v[104:107]
buffer_load_dwordx4 v1,s[36:39],s66 offen offset:1024 sc1 lds
v_mfma_f32_16x16x32_bf16 v[108:111],v[48:51],v[48:51],v[108:111]
s_lshr_b32 s72,s66,6
v_mul_f32 v77,v37,v37
v_mul_f32 v78,v38,v38
v_mfma_f32_16x16x32_bf16 v[28:31],a[48:51],v[52:55],v[28:31]
s_lshr_b32 s71,s62,2
ds_read_b128 v[48:51],v2 offset:13312
v_mul_f32 v79,v39,v39
v_mfma_f32_16x16x32_bf16 v[32:35],a[52:55],v[52:55],v[32:35]
s_cmp_lt_u32 s66,4194304
v_fma_f32 v76,v98,v76,v97
v_mul_f32 v76,v36,v76
v_mfma_f32_16x16x32_bf16 v[104:107],v[100:103],v[52:55],v[104:107]
buffer_load_dwordx4 v1,s[36:39],s66 offen offset:2048 sc1 lds
v_mfma_f32_16x16x32_bf16 v[108:111],v[52:55],v[52:55],v[108:111]
s_cselect_b32 s72,s59,s72
v_exp_f32 v76,v76
v_mfma_f32_16x16x32_bf16 v[28:31],a[56:59],v[56:59],v[28:31]
ds_read_b128 v[52:55],v2 offset:14336
v_fma_f32 v76,v76,v99,v99
v_mfma_f32_16x16x32_bf16 v[32:35],a[60:63],v[56:59],v[32:35]
v_rcp_f32 v76,v76
v_mfma_f32_16x16x32_bf16 v[104:107],v[100:103],v[56:59],v[104:107]
buffer_load_dwordx4 v1,s[36:39],s66 offen offset:3072 sc1 lds
v_mfma_f32_16x16x32_bf16 v[108:111],v[56:59],v[56:59],v[108:111]
s_waitcnt lgkmcnt(3)
v_fma_f32 v77,v98,v77,v97
v_mul_f32 v77,v37,v77
v_mfma_f32_16x16x32_bf16 v[28:31],a[0:3],v[60:63],v[28:31]
s_add_u32 m0,s71,s88
ds_read_b128 v[56:59],v2 offset:15360
v_fma_f32 v78,v98,v78,v97
v_mfma_f32_16x16x32_bf16 v[32:35],a[4:7],v[60:63],v[32:35]
buffer_load_dword v10,s[36:39],s72 offen offset:0 sc1 lds
v_mfma_f32_16x16x32_bf16 v[28:31],a[8:11],v[64:67],v[28:31]
s_xor_b32 s87,s64,65536
ds_read_b128 v[60:63],v2 offset:8192
v_mul_f32 v78,v38,v78
v_mfma_f32_16x16x32_bf16 v[32:35],a[12:15],v[64:67],v[32:35]
s_cmp_ge_u32 s66,s59
v_exp_f32 v77,v77
v_mfma_f32_16x16x32_bf16 v[28:31],a[16:19],v[68:71],v[28:31]
s_cselect_b32 s86,s49,s50)"
R"(
ds_read_b128 v[64:67],v2 offset:9216
v_fma_f32 v77,v77,v99,v99
v_mfma_f32_16x16x32_bf16 v[32:35],a[20:23],v[68:71],v[32:35]
s_cselect_b32 s87,s87,s63
v_exp_f32 v78,v78
v_mfma_f32_16x16x32_bf16 v[28:31],a[24:27],v[72:75],v[28:31]
s_mov_b32 s62,s87
ds_read_b128 v[68:71],v2 offset:10240
v_fma_f32 v78,v78,v99,v99
v_mfma_f32_16x16x32_bf16 v[32:35],a[28:31],v[72:75],v[32:35]
s_waitcnt lgkmcnt(3)
v_rcp_f32 v77,v77
v_mfma_f32_16x16x32_bf16 v[28:31],a[96:99],v[44:47],v[28:31]
s_waitcnt vmcnt(10)
ds_read_b128 v[72:75],v2 offset:11264
v_fma_f32 v79,v98,v79,v97
v_mfma_f32_16x16x32_bf16 v[32:35],a[100:103],v[44:47],v[32:35]
s_barrier
v_mfma_f32_16x16x32_bf16 v[28:31],a[104:107],v[48:51],v[28:31]
s_cmp_lt_u32 s86,s89
ds_read_b128 v[44:47],v2 offset:20480
ds_read_b32 v16,v7 offset:0
v_mfma_f32_16x16x32_bf16 v[32:35],a[108:111],v[48:51],v[32:35]
s_mov_b32 m0,s62
v_rcp_f32 v78,v78
v_mfma_f32_16x16x32_bf16 v[28:31],a[112:115],v[52:55],v[28:31]
ds_read_b128 v[48:51],v2 offset:21504
v_mul_f32 v79,v39,v79
v_mfma_f32_16x16x32_bf16 v[32:35],a[116:119],v[52:55],v[32:35]
v_exp_f32 v79,v79
v_mfma_f32_16x16x32_bf16 v[28:31],a[120:123],v[56:59],v[28:31]
s_waitcnt lgkmcnt(1)
ds_read_b128 v[52:55],v2 offset:22528
v_fma_f32 v79,v79,v99,v99
v_mfma_f32_16x16x32_bf16 v[32:35],a[124:127],v[56:59],v[32:35]
v_cmp_eq_u32 vcc,s60,v16
v_rcp_f32 v79,v79
v_mul_f32 v36,v36,v76
v_mfma_f32_16x16x32_bf16 v[28:31],a[64:67],v[60:63],v[28:31]
s_cselect_b32 vcc_lo,-1,vcc_lo
ds_read_b128 v[56:59],v2 offset:23552
v_mul_f32 v37,v37,v77
v_mfma_f32_16x16x32_bf16 v[32:35],a[68:71],v[60:63],v[32:35]
s_cmp_eq_u32 vcc_lo,-1
v_mul_f32 v38,v38,v78
v_mul_f32 v39,v39,v79
v_mfma_f32_16x16x32_bf16 v[28:31],a[72:75],v[64:67],v[28:31]
s_cselect_b32 s67,s86,s59
ds_read_b128 v[60:63],v2 offset:16384
v_cvt_pk_bf16_f32 v84,v36,v37
v_mfma_f32_16x16x32_bf16 v[32:35],a[76:79],v[64:67],v[32:35]
s_add_u32 s53,s52,s55
v_cvt_pk_bf16_f32 v85,v38,v39
v_fma_f32 v40,-v96,v24,v40
v_mfma_f32_16x16x32_bf16 v[28:31],a[80:83],v[68:71],v[28:31]
s_cmp_eq_u32 s53,s56
ds_read_b128 v[64:67],v2 offset:17408
v_fma_f32 v41,-v96,v25,v41
v_mfma_f32_16x16x32_bf16 v[32:35],a[84:87],v[68:71],v[32:35]
s_cselect_b32 s53,s57,s53
v_fma_f32 v42,-v96,v26,v42
v_fma_f32 v43,-v96,v27,v43
v_mfma_f32_16x16x32_bf16 v[28:31],a[88:91],v[72:75],v[28:31]
s_cmp_eq_u32 s53,s58
ds_read_b128 v[68:71],v2 offset:18432
v_mul_f32 v80,v40,v40
v_mfma_f32_16x16x32_bf16 v[32:35],a[92:95],v[72:75],v[32:35]
s_waitcnt lgkmcnt(3)
v_mul_f32 v81,v41,v41
v_mul_f32 v82,v42,v42
v_mfma_f32_16x16x32_bf16 v[28:31],a[160:163],v[44:47],v[28:31]
s_cselect_b32 s53,s59,s53
ds_read_b128 v[72:75],v2 offset:19456
v_mul_f32 v83,v43,v43
v_mfma_f32_16x16x32_bf16 v[32:35],a[164:167],v[44:47],v[32:35]
s_xor_b32 s63,s63,65536
v_fma_f32 v80,v98,v80,v97
v_mul_f32 v80,v40,v80
v_mfma_f32_16x16x32_bf16 v[104:107],v[100:103],v[44:47],v[104:107]
s_cmp_ge_u32 s66,s59
v_exp_f32 v80,v80
v_mfma_f32_16x16x32_bf16 v[108:111],v[44:47],v[44:47],v[108:111]
v_fma_f32 v80,v80,v99,v99
v_fma_f32 v81,v98,v81,v97
v_mfma_f32_16x16x32_bf16 v[28:31],a[168:171],v[48:51],v[28:31]
ds_read_b128 v[44:47],v2 offset:28672
v_mul_f32 v81,v41,v81
v_mfma_f32_16x16x32_bf16 v[32:35],a[172:175],v[48:51],v[32:35]
v_exp_f32 v81,v81
v_mfma_f32_16x16x32_bf16 v[104:107],v[100:103],v[48:51],v[104:107]
v_rcp_f32 v80,v80
v_mfma_f32_16x16x32_bf16 v[108:111],v[48:51],v[48:51],v[108:111]
v_fma_f32 v81,v81,v99,v99
v_fma_f32 v82,v98,v82,v97
v_mfma_f32_16x16x32_bf16 v[28:31],a[176:179],v[52:55],v[28:31]
ds_read_b128 v[48:51],v2 offset:29696
v_mul_f32 v82,v42,v82
v_mfma_f32_16x16x32_bf16 v[32:35],a[180:183],v[52:55],v[32:35]
v_exp_f32 v82,v82
v_mfma_f32_16x16x32_bf16 v[104:107],v[100:103],v[52:55],v[104:107]
v_rcp_f32 v81,v81
v_mfma_f32_16x16x32_bf16 v[108:111],v[52:55],v[52:55],v[108:111]
v_fma_f32 v82,v82,v99,v99
v_fma_f32 v83,v98,v83,v97
v_mfma_f32_16x16x32_bf16 v[28:31],a[184:187],v[56:59],v[28:31]
ds_read_b128 v[52:55],v2 offset:30720
v_mul_f32 v83,v43,v83
v_mfma_f32_16x16x32_bf16 v[32:35],a[188:191],v[56:59],v[32:35]
v_exp_f32 v83,v83
v_mfma_f32_16x16x32_bf16 v[104:107],v[100:103],v[56:59],v[104:107]
v_rcp_f32 v82,v82
v_mfma_f32_16x16x32_bf16 v[108:111],v[56:59],v[56:59],v[108:111]
s_waitcnt lgkmcnt(3)
v_fma_f32 v83,v83,v99,v99
v_mul_f32 v40,v40,v80
v_mfma_f32_16x16x32_bf16 v[28:31],a[128:131],v[60:63],v[28:31]
ds_read_b128 v[56:59],v2 offset:31744
v_mul_f32 v41,v41,v81
v_mfma_f32_16x16x32_bf16 v[32:35],a[132:135],v[60:63],v[32:35]
buffer_load_dwordx4 v0,s[36:39],s67 offen offset:0 sc1 lds
v_mfma_f32_16x16x32_bf16 v[28:31],a[136:139],v[64:67],v[28:31]
v_cndmask_b32 v112,v108,v109,s[82:83]
v_cndmask_b32 v113,v110,v111,s[82:83]
v_cndmask_b32 v112,v112,v113,s[84:85]
v_cndmask_b32 v112,v104,v112,s[98:99]
v_mfma_f32_16x16x32_bf16 v[32:35],a[140:143],v[64:67],v[32:35]
ds_read_b128 v[60:63],v2 offset:24576
ds_write_b32 v13,v112 offset:0
v_mfma_f32_16x16x32_bf16 v[28:31],a[144:147],v[68:71],v[28:31]
ds_read_b128 v[64:67],v2 offset:25600
v_mul_f32 v42,v42,v82
v_mfma_f32_16x16x32_bf16 v[32:35],a[148:151],v[68:71],v[32:35]
buffer_load_dwordx4 v0,s[36:39],s67 offen offset:1024 sc1 lds
v_mfma_f32_16x16x32_bf16 v[28:31],a[152:155],v[72:75],v[28:31]
ds_read_b128 v[68:71],v2 offset:26624
v_cvt_pk_bf16_f32 v86,v40,v41
v_mfma_f32_16x16x32_bf16 v[32:35],a[156:159],v[72:75],v[32:35]
s_waitcnt lgkmcnt(4)
v_rcp_f32 v83,v83
v_mfma_f32_16x16x32_bf16 v[28:31],a[224:227],v[44:47],v[28:31]
s_waitcnt vmcnt(7) lgkmcnt(2)
ds_read_b128 v[72:75],v2 offset:27648
v_mul_f32 v43,v43,v83
v_mfma_f32_16x16x32_bf16 v[32:35],a[228:231],v[44:47],v[32:35]
s_barrier
v_mfma_f32_16x16x32_bf16 v[28:31],a[232:235],v[48:51],v[28:31]
ds_read_b128 v[116:119],v14 offset:0
ds_read_b128 v[120:123],v14 offset:16
v_mfma_f32_16x16x32_bf16 v[32:35],a[236:239],v[48:51],v[32:35]
ds_read_b128 v[44:47],v2 offset:36864
buffer_load_dwordx4 v0,s[36:39],s67 offen offset:2048 sc1 lds
v_mfma_f32_16x16x32_bf16 v[28:31],a[240:243],v[52:55],v[28:31]
ds_read_b128 v[48:51],v2 offset:37888
v_cvt_pk_bf16_f32 v87,v42,v43
v_mfma_f32_16x16x32_bf16 v[32:35],a[244:247],v[52:55],v[32:35]
v_mfma_f32_16x16x32_bf16 v[28:31],a[248:251],v[56:59],v[28:31]
s_waitcnt lgkmcnt(2)
ds_read_b128 v[52:55],v2 offset:38912
v_add_f32 v116,v116,v117
v_mfma_f32_16x16x32_bf16 v[32:35],a[252:255],v[56:59],v[32:35]
buffer_load_dwordx4 v0,s[36:39],s67 offen offset:3072 sc1 lds
v_mfma_f32_16x16x32_bf16 v[28:31],a[192:195],v[60:63],v[28:31]
ds_read_b128 v[56:59],v2 offset:39936
v_add_f32 v118,v118,v119
v_mfma_f32_16x16x32_bf16 v[32:35],a[196:199],v[60:63],v[32:35]
v_add_f32 v116,v116,v118
v_mul_f32 v92,s75,v116
v_mfma_f32_16x16x32_bf16 v[28:31],a[200:203],v[64:67],v[28:31]
ds_read_b128 v[60:63],v2 offset:32768
v_add_f32 v120,v120,v121
v_mfma_f32_16x16x32_bf16 v[32:35],a[204:207],v[64:67],v[32:35]
buffer_store_dwordx4 v[84:87],v3,s[36:39],s73 offen offset:0 sc0 sc1
v_add_f32 v122,v122,v123
v_add_f32 v120,v120,v122
v_mul_f32 v120,s75,v120
v_mfma_f32_16x16x32_bf16 v[28:31],a[208:211],v[68:71],v[28:31]
ds_read_b128 v[64:67],v2 offset:33792
v_fma_f32 v120,-v92,v92,v120
v_mfma_f32_16x16x32_bf16 v[32:35],a[212:215],v[68:71],v[32:35]
v_max_f32 v120,0,v120
v_add_f32 v115,v15,v120
v_fma_f32 v120,v15,v115,v120
v_rsq_f32 v116,v120
v_mfma_f32_16x16x32_bf16 v[28:31],a[216:219],v[72:75],v[28:31]
s_waitcnt lgkmcnt(2)
ds_read_b128 v[68:71],v2 offset:34816
v_mul_f32 v93,s76,v116
v_mul_f32 v94,v116,v116
v_mul_f32 v95,v120,v116
v_mul_f32 v94,s77,v94
v_mul_f32 v94,v94,v93
v_mfma_f32_16x16x32_bf16 v[32:35],a[220:223],v[72:75],v[32:35]
s_cbranch_scc1 Lg1_slow1_%=
Lg1_a1w1_%=:
v_mfma_f32_16x16x32_bf16 v[36:39],a[32:35],v[44:47],0
s_add_u32 m0,s62,16384
ds_read_b128 v[72:75],v2 offset:35840
s_sub_u32 s69,s67,1048576
v_mfma_f32_16x16x32_bf16 v[40:43],a[36:39],v[44:47],0
s_cmp_lt_u32 s69,2097152)"
R"(
s_cselect_b32 s89,3145728,s89
s_sub_u32 s69,s67,3145728
v_mfma_f32_16x16x32_bf16 v[104:107],v[100:103],v[44:47],0
buffer_load_dwordx4 v1,s[36:39],s67 offen offset:0 sc1 lds
v_mfma_f32_16x16x32_bf16 v[108:111],v[44:47],v[44:47],0
s_cmp_lt_u32 s69,1048576
v_fma_f32 v28,-v92,v20,v28
v_fma_f32 v29,-v92,v21,v29
v_mfma_f32_16x16x32_bf16 v[36:39],a[40:43],v[48:51],v[36:39]
s_cselect_b32 s89,4194304,s89
ds_read_b128 v[44:47],v2 offset:45056
v_fma_f32 v30,-v92,v22,v30
v_mfma_f32_16x16x32_bf16 v[40:43],a[44:47],v[48:51],v[40:43]
s_lshr_b32 s73,s48,0
v_fma_f32 v31,-v92,v23,v31
v_mul_f32 v76,v28,v28
v_mfma_f32_16x16x32_bf16 v[104:107],v[100:103],v[48:51],v[104:107]
buffer_load_dwordx4 v1,s[36:39],s67 offen offset:1024 sc1 lds
v_mfma_f32_16x16x32_bf16 v[108:111],v[48:51],v[48:51],v[108:111]
s_lshr_b32 s72,s67,6
v_mul_f32 v77,v29,v29
v_mul_f32 v78,v30,v30
v_mfma_f32_16x16x32_bf16 v[36:39],a[48:51],v[52:55],v[36:39]
s_lshr_b32 s71,s62,2
ds_read_b128 v[48:51],v2 offset:46080
v_mul_f32 v79,v31,v31
v_mfma_f32_16x16x32_bf16 v[40:43],a[52:55],v[52:55],v[40:43]
s_cmp_lt_u32 s67,4194304
v_fma_f32 v76,v94,v76,v93
v_mul_f32 v76,v28,v76
v_mfma_f32_16x16x32_bf16 v[104:107],v[100:103],v[52:55],v[104:107]
buffer_load_dwordx4 v1,s[36:39],s67 offen offset:2048 sc1 lds
v_mfma_f32_16x16x32_bf16 v[108:111],v[52:55],v[52:55],v[108:111]
s_cselect_b32 s72,s59,s72
v_exp_f32 v76,v76
v_mfma_f32_16x16x32_bf16 v[36:39],a[56:59],v[56:59],v[36:39]
s_mov_b64 s[44:45],s[46:47]
ds_read_b128 v[52:55],v2 offset:47104
v_fma_f32 v76,v76,v95,v95
v_mfma_f32_16x16x32_bf16 v[40:43],a[60:63],v[56:59],v[40:43]
s_mov_b64 s[46:47],s[48:49]
v_rcp_f32 v76,v76
v_mfma_f32_16x16x32_bf16 v[104:107],v[100:103],v[56:59],v[104:107]
buffer_load_dwordx4 v1,s[36:39],s67 offen offset:3072 sc1 lds
v_mfma_f32_16x16x32_bf16 v[108:111],v[56:59],v[56:59],v[108:111]
s_waitcnt lgkmcnt(3)
v_fma_f32 v77,v94,v77,v93
v_mul_f32 v77,v29,v77
v_mfma_f32_16x16x32_bf16 v[36:39],a[0:3],v[60:63],v[36:39]
s_add_u32 m0,s71,s88
ds_read_b128 v[56:59],v2 offset:48128
v_fma_f32 v78,v94,v78,v93
v_mfma_f32_16x16x32_bf16 v[40:43],a[4:7],v[60:63],v[40:43]
buffer_load_dword v10,s[36:39],s72 offen offset:0 sc1 lds
v_mfma_f32_16x16x32_bf16 v[36:39],a[8:11],v[64:67],v[36:39]
s_xor_b32 s87,s63,65536
ds_read_b128 v[60:63],v2 offset:40960
v_mul_f32 v78,v30,v78
v_mfma_f32_16x16x32_bf16 v[40:43],a[12:15],v[64:67],v[40:43]
s_cmp_ge_u32 s67,s59
v_exp_f32 v77,v77
v_mfma_f32_16x16x32_bf16 v[36:39],a[16:19],v[68:71],v[36:39]
s_cselect_b32 s86,s50,s51
ds_read_b128 v[64:67],v2 offset:41984
v_fma_f32 v77,v77,v95,v95
v_mfma_f32_16x16x32_bf16 v[40:43],a[20:23],v[68:71],v[40:43]
s_cselect_b32 s87,s87,s64
v_exp_f32 v78,v78
v_mfma_f32_16x16x32_bf16 v[36:39],a[24:27],v[72:75],v[36:39]
s_mov_b32 s62,s87
ds_read_b128 v[68:71],v2 offset:43008
v_fma_f32 v78,v78,v95,v95
v_mfma_f32_16x16x32_bf16 v[40:43],a[28:31],v[72:75],v[40:43]
s_waitcnt lgkmcnt(3)
v_rcp_f32 v77,v77
v_mfma_f32_16x16x32_bf16 v[36:39],a[96:99],v[44:47],v[36:39]
s_waitcnt vmcnt(10)
ds_read_b128 v[72:75],v2 offset:44032
v_fma_f32 v79,v94,v79,v93
v_mfma_f32_16x16x32_bf16 v[40:43],a[100:103],v[44:47],v[40:43]
s_barrier
v_mfma_f32_16x16x32_bf16 v[36:39],a[104:107],v[48:51],v[36:39]
s_cmp_lt_u32 s86,s89
ds_read_b128 v[44:47],v2 offset:53248
ds_read_b32 v16,v7 offset:256
v_mfma_f32_16x16x32_bf16 v[40:43],a[108:111],v[48:51],v[40:43]
s_mov_b32 m0,s62
v_rcp_f32 v78,v78
v_mfma_f32_16x16x32_bf16 v[36:39],a[112:115],v[52:55],v[36:39]
s_mov_b64 s[48:49],s[50:51]
ds_read_b128 v[48:51],v2 offset:54272
v_mul_f32 v79,v31,v79
v_mfma_f32_16x16x32_bf16 v[40:43],a[116:119],v[52:55],v[40:43]
s_mov_b64 s[50:51],s[52:53]
v_exp_f32 v79,v79
v_mfma_f32_16x16x32_bf16 v[36:39],a[120:123],v[56:59],v[36:39]
s_waitcnt lgkmcnt(1)
ds_read_b128 v[52:55],v2 offset:55296
v_fma_f32 v79,v79,v95,v95
v_mfma_f32_16x16x32_bf16 v[40:43],a[124:127],v[56:59],v[40:43]
v_cmp_eq_u32 vcc,s60,v16
v_rcp_f32 v79,v79
v_mul_f32 v28,v28,v76
v_mfma_f32_16x16x32_bf16 v[36:39],a[64:67],v[60:63],v[36:39]
s_cselect_b32 vcc_lo,-1,vcc_lo
ds_read_b128 v[56:59],v2 offset:56320
v_mul_f32 v29,v29,v77
v_mfma_f32_16x16x32_bf16 v[40:43],a[68:71],v[60:63],v[40:43]
s_cmp_eq_u32 vcc_lo,-1
v_mul_f32 v30,v30,v78
v_mul_f32 v31,v31,v79
v_mfma_f32_16x16x32_bf16 v[36:39],a[72:75],v[64:67],v[36:39]
s_cselect_b32 s66,s86,s59
ds_read_b128 v[60:63],v2 offset:49152
v_cvt_pk_bf16_f32 v84,v28,v29
v_mfma_f32_16x16x32_bf16 v[40:43],a[76:79],v[64:67],v[40:43]
s_add_u32 s74,s53,s55
v_cvt_pk_bf16_f32 v85,v30,v31
v_fma_f32 v32,-v92,v24,v32
v_mfma_f32_16x16x32_bf16 v[36:39],a[80:83],v[68:71],v[36:39]
s_cmp_eq_u32 s74,s56
ds_read_b128 v[64:67],v2 offset:50176
v_fma_f32 v33,-v92,v25,v33
v_mfma_f32_16x16x32_bf16 v[40:43],a[84:87],v[68:71],v[40:43]
s_cselect_b32 s74,s57,s74
v_fma_f32 v34,-v92,v26,v34
v_fma_f32 v35,-v92,v27,v35
v_mfma_f32_16x16x32_bf16 v[36:39],a[88:91],v[72:75],v[36:39]
s_cmp_eq_u32 s74,s58
ds_read_b128 v[68:71],v2 offset:51200
v_mul_f32 v80,v32,v32
v_mfma_f32_16x16x32_bf16 v[40:43],a[92:95],v[72:75],v[40:43]
s_waitcnt lgkmcnt(3)
v_mul_f32 v81,v33,v33
v_mul_f32 v82,v34,v34
v_mfma_f32_16x16x32_bf16 v[36:39],a[160:163],v[44:47],v[36:39]
s_cselect_b32 s74,s59,s74
ds_read_b128 v[72:75],v2 offset:52224
v_mul_f32 v83,v35,v35
v_mfma_f32_16x16x32_bf16 v[40:43],a[164:167],v[44:47],v[40:43]
s_xor_b32 s64,s64,65536
v_fma_f32 v80,v94,v80,v93
v_mul_f32 v80,v32,v80
v_mfma_f32_16x16x32_bf16 v[104:107],v[100:103],v[44:47],v[104:107]
s_mov_b32 s52,s74
v_exp_f32 v80,v80
v_mfma_f32_16x16x32_bf16 v[108:111],v[44:47],v[44:47],v[108:111]
s_cmp_lt_u32 s67,4194304
v_fma_f32 v80,v80,v95,v95
v_fma_f32 v81,v94,v81,v93
v_mfma_f32_16x16x32_bf16 v[36:39],a[168:171],v[48:51],v[36:39]
ds_read_b128 v[44:47],v2 offset:61440
v_mul_f32 v81,v33,v81
v_mfma_f32_16x16x32_bf16 v[40:43],a[172:175],v[48:51],v[40:43]
v_exp_f32 v81,v81
v_mfma_f32_16x16x32_bf16 v[104:107],v[100:103],v[48:51],v[104:107]
v_rcp_f32 v80,v80
v_mfma_f32_16x16x32_bf16 v[108:111],v[48:51],v[48:51],v[108:111]
v_fma_f32 v81,v81,v95,v95
v_fma_f32 v82,v94,v82,v93
v_mfma_f32_16x16x32_bf16 v[36:39],a[176:179],v[52:55],v[36:39]
ds_read_b128 v[48:51],v2 offset:62464
v_mul_f32 v82,v34,v82
v_mfma_f32_16x16x32_bf16 v[40:43],a[180:183],v[52:55],v[40:43]
v_exp_f32 v82,v82
v_mfma_f32_16x16x32_bf16 v[104:107],v[100:103],v[52:55],v[104:107]
v_rcp_f32 v81,v81
v_mfma_f32_16x16x32_bf16 v[108:111],v[52:55],v[52:55],v[108:111]
v_fma_f32 v82,v82,v95,v95
v_fma_f32 v83,v94,v83,v93
v_mfma_f32_16x16x32_bf16 v[36:39],a[184:187],v[56:59],v[36:39]
ds_read_b128 v[52:55],v2 offset:63488
v_mul_f32 v83,v35,v83
v_mfma_f32_16x16x32_bf16 v[40:43],a[188:191],v[56:59],v[40:43]
v_exp_f32 v83,v83
v_mfma_f32_16x16x32_bf16 v[104:107],v[100:103],v[56:59],v[104:107]
v_rcp_f32 v82,v82
v_mfma_f32_16x16x32_bf16 v[108:111],v[56:59],v[56:59],v[108:111]
s_waitcnt lgkmcnt(3)
v_fma_f32 v83,v83,v95,v95
v_mul_f32 v32,v32,v80
v_mfma_f32_16x16x32_bf16 v[36:39],a[128:131],v[60:63],v[36:39]
ds_read_b128 v[56:59],v2 offset:64512
v_mul_f32 v33,v33,v81
v_mfma_f32_16x16x32_bf16 v[40:43],a[132:135],v[60:63],v[40:43]
buffer_load_dwordx4 v0,s[36:39],s66 offen offset:0 sc1 lds
v_mfma_f32_16x16x32_bf16 v[36:39],a[136:139],v[64:67],v[36:39]
v_cndmask_b32 v112,v108,v109,s[82:83]
v_cndmask_b32 v113,v110,v111,s[82:83]
v_cndmask_b32 v112,v112,v113,s[84:85]
v_cndmask_b32 v112,v104,v112,s[98:99]
v_mfma_f32_16x16x32_bf16 v[40:43],a[140:143],v[64:67],v[40:43]
ds_read_b128 v[60:63],v2 offset:57344
ds_write_b32 v13,v112 offset:2048
v_mfma_f32_16x16x32_bf16 v[36:39],a[144:147],v[68:71],v[36:39]
ds_read_b128 v[64:67],v2 offset:58368
v_mul_f32 v34,v34,v82
v_mfma_f32_16x16x32_bf16 v[40:43],a[148:151],v[68:71],v[40:43]
buffer_load_dwordx4 v0,s[36:39],s66 offen offset:1024 sc1 lds
v_mfma_f32_16x16x32_bf16 v[36:39],a[152:155],v[72:75],v[36:39])"
R"(
ds_read_b128 v[68:71],v2 offset:59392
v_cvt_pk_bf16_f32 v86,v32,v33
v_mfma_f32_16x16x32_bf16 v[40:43],a[156:159],v[72:75],v[40:43]
s_waitcnt lgkmcnt(4)
v_rcp_f32 v83,v83
v_mfma_f32_16x16x32_bf16 v[36:39],a[224:227],v[44:47],v[36:39]
s_waitcnt vmcnt(7) lgkmcnt(2)
ds_read_b128 v[72:75],v2 offset:60416
v_xor_b32 v2,65536,v2
v_mfma_f32_16x16x32_bf16 v[40:43],a[228:231],v[44:47],v[40:43]
s_barrier
v_mfma_f32_16x16x32_bf16 v[36:39],a[232:235],v[48:51],v[36:39]
ds_read_b128 v[116:119],v14 offset:2048
ds_read_b128 v[120:123],v14 offset:2064
v_mfma_f32_16x16x32_bf16 v[40:43],a[236:239],v[48:51],v[40:43]
ds_read_b128 v[44:47],v2 offset:4096
buffer_load_dwordx4 v0,s[36:39],s66 offen offset:2048 sc1 lds
v_mfma_f32_16x16x32_bf16 v[36:39],a[240:243],v[52:55],v[36:39]
ds_read_b128 v[48:51],v2 offset:5120
v_mul_f32 v35,v35,v83
v_mfma_f32_16x16x32_bf16 v[40:43],a[244:247],v[52:55],v[40:43]
v_cvt_pk_bf16_f32 v87,v34,v35
v_xor_b32 v8,16384,v8
v_mfma_f32_16x16x32_bf16 v[36:39],a[248:251],v[56:59],v[36:39]
s_waitcnt lgkmcnt(2)
ds_read_b128 v[52:55],v2 offset:6144
v_add_f32 v116,v116,v117
v_mfma_f32_16x16x32_bf16 v[40:43],a[252:255],v[56:59],v[40:43]
buffer_load_dwordx4 v0,s[36:39],s66 offen offset:3072 sc1 lds
v_mfma_f32_16x16x32_bf16 v[36:39],a[192:195],v[60:63],v[36:39]
ds_read_b128 v[56:59],v2 offset:7168
v_add_f32 v118,v118,v119
v_mfma_f32_16x16x32_bf16 v[40:43],a[196:199],v[60:63],v[40:43]
v_add_f32 v116,v116,v118
v_mul_f32 v96,s75,v116
v_mfma_f32_16x16x32_bf16 v[36:39],a[200:203],v[64:67],v[36:39]
ds_read_b128 v[60:63],v2 offset:0
v_add_f32 v120,v120,v121
v_mfma_f32_16x16x32_bf16 v[40:43],a[204:207],v[64:67],v[40:43]
buffer_store_dwordx4 v[84:87],v3,s[36:39],s73 offen offset:0 sc0 sc1
v_add_f32 v122,v122,v123
v_add_f32 v120,v120,v122
v_mul_f32 v120,s75,v120
v_mfma_f32_16x16x32_bf16 v[36:39],a[208:211],v[68:71],v[36:39]
ds_read_b128 v[64:67],v2 offset:1024
v_fma_f32 v120,-v96,v96,v120
v_mfma_f32_16x16x32_bf16 v[40:43],a[212:215],v[68:71],v[40:43]
v_max_f32 v120,0,v120
v_add_f32 v115,v15,v120
v_fma_f32 v120,v15,v115,v120
v_rsq_f32 v116,v120
v_mfma_f32_16x16x32_bf16 v[36:39],a[216:219],v[72:75],v[36:39]
s_waitcnt lgkmcnt(2)
ds_read_b128 v[68:71],v2 offset:2048
v_mul_f32 v97,s76,v116
v_mul_f32 v98,v116,v116
v_mul_f32 v99,v120,v116
v_mul_f32 v98,s77,v98
v_mul_f32 v98,v98,v97
v_mfma_f32_16x16x32_bf16 v[40:43],a[220:223],v[72:75],v[40:43]
s_cbranch_scc1 Lg1_a0w1_%=
s_cmp_ge_u32 s48,4194304
s_cselect_b64 s[90:91],s[94:95],s[90:91]
s_cselect_b64 s[92:93],s[96:97],s[92:93]
s_cmp_lt_u32 s67,s59
s_cbranch_scc1 Lg1_d0w1_%=
s_branch Lg1_slow0_%=
Lg1_a0w2_%=:
v_mfma_f32_16x16x32_bf16 v[28:31],a[64:67],v[44:47],0
s_sub_u32 s69,s66,1048576
ds_read_b128 v[72:75],v2 offset:15360
s_cmp_lt_u32 s69,2097152
v_mfma_f32_16x16x32_bf16 v[32:35],a[68:71],v[44:47],0
s_cselect_b32 s89,3145728,s89
s_sub_u32 s69,s66,3145728
s_cmp_lt_u32 s69,1048576
v_mfma_f32_16x16x32_bf16 v[104:107],v[100:103],v[44:47],0
s_cselect_b32 s89,4194304,s89
s_add_u32 m0,s62,16384
s_lshr_b32 s73,s47,0
v_mfma_f32_16x16x32_bf16 v[108:111],v[44:47],v[44:47],0
buffer_load_dwordx4 v1,s[36:39],s66 offen offset:0 sc1 lds
v_mfma_f32_16x16x32_bf16 v[28:31],a[72:75],v[48:51],v[28:31]
s_lshr_b32 s65,s48,6
ds_read_b128 v[44:47],v2 offset:0
v_fma_f32 v36,-v96,v20,v36
v_mfma_f32_16x16x32_bf16 v[32:35],a[76:79],v[48:51],v[32:35]
s_lshr_b32 s72,s66,6
v_fma_f32 v37,-v96,v21,v37
v_fma_f32 v38,-v96,v22,v38
v_mfma_f32_16x16x32_bf16 v[104:107],v[100:103],v[48:51],v[104:107]
s_lshr_b32 s71,s62,2
v_fma_f32 v39,-v96,v23,v39
v_mul_f32 v76,v36,v36
v_mfma_f32_16x16x32_bf16 v[108:111],v[48:51],v[48:51],v[108:111]
buffer_load_dwordx4 v1,s[36:39],s66 offen offset:1024 sc1 lds
v_mfma_f32_16x16x32_bf16 v[28:31],a[80:83],v[52:55],v[28:31]
s_cmp_lt_u32 s66,4194304
ds_read_b128 v[48:51],v2 offset:1024
v_mul_f32 v77,v37,v37
v_mfma_f32_16x16x32_bf16 v[32:35],a[84:87],v[52:55],v[32:35]
s_cselect_b32 s72,s59,s72
v_mul_f32 v78,v38,v38
v_mul_f32 v79,v39,v39
v_mfma_f32_16x16x32_bf16 v[104:107],v[100:103],v[52:55],v[104:107]
v_fma_f32 v76,v98,v76,v97
v_mul_f32 v76,v36,v76
v_mfma_f32_16x16x32_bf16 v[108:111],v[52:55],v[52:55],v[108:111]
buffer_load_dwordx4 v1,s[36:39],s66 offen offset:2048 sc1 lds
v_mfma_f32_16x16x32_bf16 v[28:31],a[88:91],v[56:59],v[28:31]
ds_read_b128 v[52:55],v2 offset:2048
v_fma_f32 v77,v98,v77,v97
v_mfma_f32_16x16x32_bf16 v[32:35],a[92:95],v[56:59],v[32:35]
v_exp_f32 v76,v76
v_mfma_f32_16x16x32_bf16 v[104:107],v[100:103],v[56:59],v[104:107]
v_mul_f32 v77,v37,v77
v_fma_f32 v76,v76,v99,v99
v_mfma_f32_16x16x32_bf16 v[108:111],v[56:59],v[56:59],v[108:111]
s_waitcnt lgkmcnt(3)
buffer_load_dwordx4 v1,s[36:39],s66 offen offset:3072 sc1 lds
v_mfma_f32_16x16x32_bf16 v[28:31],a[96:99],v[60:63],v[28:31]
s_add_u32 m0,s71,s88
ds_read_b128 v[56:59],v2 offset:3072
v_fma_f32 v78,v98,v78,v97
v_mfma_f32_16x16x32_bf16 v[32:35],a[100:103],v[60:63],v[32:35]
buffer_load_dword v10,s[36:39],s72 offen offset:0 sc1 lds
v_mfma_f32_16x16x32_bf16 v[28:31],a[104:107],v[64:67],v[28:31]
s_xor_b32 s87,s64,65536
ds_read_b128 v[60:63],v2 offset:4096
v_fma_f32 v79,v98,v79,v97
v_mfma_f32_16x16x32_bf16 v[32:35],a[108:111],v[64:67],v[32:35]
s_cmp_ge_u32 s66,s59
v_exp_f32 v77,v77
v_mfma_f32_16x16x32_bf16 v[28:31],a[112:115],v[68:71],v[28:31]
s_cselect_b32 s86,s49,s50
ds_read_b128 v[64:67],v2 offset:5120
v_mul_f32 v78,v38,v78
v_mfma_f32_16x16x32_bf16 v[32:35],a[116:119],v[68:71],v[32:35]
s_cselect_b32 s87,s87,s63
v_exp_f32 v78,v78
v_mfma_f32_16x16x32_bf16 v[28:31],a[120:123],v[72:75],v[28:31]
s_mov_b32 s62,s87
ds_read_b128 v[68:71],v2 offset:6144
v_mul_f32 v79,v39,v79
v_mfma_f32_16x16x32_bf16 v[32:35],a[124:127],v[72:75],v[32:35]
s_waitcnt lgkmcnt(3)
v_exp_f32 v79,v79
v_mfma_f32_16x16x32_bf16 v[28:31],a[0:3],v[44:47],v[28:31]
s_waitcnt vmcnt(10)
ds_read_b128 v[72:75],v2 offset:7168
v_fma_f32 v77,v77,v99,v99
v_mfma_f32_16x16x32_bf16 v[32:35],a[4:7],v[44:47],v[32:35]
s_barrier
v_mfma_f32_16x16x32_bf16 v[28:31],a[8:11],v[48:51],v[28:31]
s_cmp_lt_u32 s86,s89
ds_read_b128 v[44:47],v2 offset:24576
ds_read_b32 v16,v7 offset:0
v_mfma_f32_16x16x32_bf16 v[32:35],a[12:15],v[48:51],v[32:35]
s_mov_b32 m0,s62
v_rcp_f32 v76,v76
v_mfma_f32_16x16x32_bf16 v[28:31],a[16:19],v[52:55],v[28:31]
ds_read_b128 v[48:51],v2 offset:25600
v_fma_f32 v78,v78,v99,v99
v_mfma_f32_16x16x32_bf16 v[32:35],a[20:23],v[52:55],v[32:35]
v_rcp_f32 v77,v77
v_mfma_f32_16x16x32_bf16 v[28:31],a[24:27],v[56:59],v[28:31]
s_waitcnt lgkmcnt(1)
ds_read_b128 v[52:55],v2 offset:26624
v_fma_f32 v79,v79,v99,v99
v_mfma_f32_16x16x32_bf16 v[32:35],a[28:31],v[56:59],v[32:35]
v_cmp_eq_u32 vcc,s60,v16
v_rcp_f32 v78,v78
v_mul_f32 v36,v36,v76
v_mfma_f32_16x16x32_bf16 v[28:31],a[32:35],v[60:63],v[28:31]
s_cselect_b32 vcc_lo,-1,vcc_lo
ds_read_b128 v[56:59],v2 offset:27648
v_mul_f32 v37,v37,v77
v_mfma_f32_16x16x32_bf16 v[32:35],a[36:39],v[60:63],v[32:35]
s_cmp_eq_u32 vcc_lo,-1
v_rcp_f32 v79,v79
v_mfma_f32_16x16x32_bf16 v[28:31],a[40:43],v[64:67],v[28:31]
s_cselect_b32 s67,s86,s59
ds_read_b128 v[60:63],v2 offset:28672
v_mul_f32 v38,v38,v78
v_mfma_f32_16x16x32_bf16 v[32:35],a[44:47],v[64:67],v[32:35]
s_add_u32 s53,s52,s55
v_mul_f32 v39,v39,v79
v_cvt_pk_bf16_f32 v84,v36,v37
v_mfma_f32_16x16x32_bf16 v[28:31],a[48:51],v[68:71],v[28:31]
s_cmp_eq_u32 s53,s56
ds_read_b128 v[64:67],v2 offset:29696
v_cvt_pk_bf16_f32 v85,v38,v39
v_mfma_f32_16x16x32_bf16 v[32:35],a[52:55],v[68:71],v[32:35]
s_cselect_b32 s53,s57,s53
v_fma_f32 v40,-v96,v24,v40
v_fma_f32 v41,-v96,v25,v41
v_mfma_f32_16x16x32_bf16 v[28:31],a[56:59],v[72:75],v[28:31]
s_cmp_eq_u32 s53,s58
ds_read_b128 v[68:71],v2 offset:30720
v_fma_f32 v42,-v96,v26,v42)"
R"(
v_mfma_f32_16x16x32_bf16 v[32:35],a[60:63],v[72:75],v[32:35]
s_waitcnt lgkmcnt(3)
v_fma_f32 v43,-v96,v27,v43
v_mul_f32 v80,v40,v40
v_mfma_f32_16x16x32_bf16 v[28:31],a[192:195],v[44:47],v[28:31]
s_cselect_b32 s53,s59,s53
ds_read_b128 v[72:75],v2 offset:31744
v_mul_f32 v81,v41,v41
v_mfma_f32_16x16x32_bf16 v[32:35],a[196:199],v[44:47],v[32:35]
s_xor_b32 s63,s63,65536
v_mul_f32 v82,v42,v42
v_mul_f32 v83,v43,v43
v_mfma_f32_16x16x32_bf16 v[104:107],v[100:103],v[44:47],v[104:107]
s_cmp_ge_u32 s66,s59
v_fma_f32 v80,v98,v80,v97
v_mul_f32 v80,v40,v80
v_mfma_f32_16x16x32_bf16 v[108:111],v[44:47],v[44:47],v[108:111]
v_exp_f32 v80,v80
v_mfma_f32_16x16x32_bf16 v[28:31],a[200:203],v[48:51],v[28:31]
ds_read_b128 v[44:47],v2 offset:16384
v_fma_f32 v80,v80,v99,v99
v_mfma_f32_16x16x32_bf16 v[32:35],a[204:207],v[48:51],v[32:35]
v_rcp_f32 v80,v80
v_mfma_f32_16x16x32_bf16 v[104:107],v[100:103],v[48:51],v[104:107]
v_fma_f32 v81,v98,v81,v97
v_mul_f32 v81,v41,v81
v_mfma_f32_16x16x32_bf16 v[108:111],v[48:51],v[48:51],v[108:111]
v_exp_f32 v81,v81
v_mfma_f32_16x16x32_bf16 v[28:31],a[208:211],v[52:55],v[28:31]
ds_read_b128 v[48:51],v2 offset:17408
v_fma_f32 v81,v81,v99,v99
v_mfma_f32_16x16x32_bf16 v[32:35],a[212:215],v[52:55],v[32:35]
v_rcp_f32 v81,v81
v_mfma_f32_16x16x32_bf16 v[104:107],v[100:103],v[52:55],v[104:107]
v_fma_f32 v82,v98,v82,v97
v_mul_f32 v82,v42,v82
v_mfma_f32_16x16x32_bf16 v[108:111],v[52:55],v[52:55],v[108:111]
v_exp_f32 v82,v82
v_mfma_f32_16x16x32_bf16 v[28:31],a[216:219],v[56:59],v[28:31]
ds_read_b128 v[52:55],v2 offset:18432
v_fma_f32 v82,v82,v99,v99
v_mfma_f32_16x16x32_bf16 v[32:35],a[220:223],v[56:59],v[32:35]
v_rcp_f32 v82,v82
v_mfma_f32_16x16x32_bf16 v[104:107],v[100:103],v[56:59],v[104:107]
v_fma_f32 v83,v98,v83,v97
v_mul_f32 v83,v43,v83
v_mfma_f32_16x16x32_bf16 v[108:111],v[56:59],v[56:59],v[108:111]
s_waitcnt lgkmcnt(3)
v_exp_f32 v83,v83
v_mfma_f32_16x16x32_bf16 v[28:31],a[224:227],v[60:63],v[28:31]
ds_read_b128 v[56:59],v2 offset:19456
v_fma_f32 v83,v83,v99,v99
v_mfma_f32_16x16x32_bf16 v[32:35],a[228:231],v[60:63],v[32:35]
v_rcp_f32 v83,v83
v_mfma_f32_16x16x32_bf16 v[28:31],a[232:235],v[64:67],v[28:31]
buffer_load_dwordx4 v0,s[36:39],s67 offen offset:0 sc1 lds
v_cndmask_b32 v112,v108,v109,s[82:83]
v_cndmask_b32 v113,v110,v111,s[82:83]
v_cndmask_b32 v112,v112,v113,s[84:85]
v_cndmask_b32 v112,v104,v112,s[98:99]
v_mfma_f32_16x16x32_bf16 v[32:35],a[236:239],v[64:67],v[32:35]
ds_read_b128 v[60:63],v2 offset:20480
ds_write_b32 v13,v112 offset:0
v_mfma_f32_16x16x32_bf16 v[28:31],a[240:243],v[68:71],v[28:31]
ds_read_b128 v[64:67],v2 offset:21504
v_mul_f32 v40,v40,v80
v_mfma_f32_16x16x32_bf16 v[32:35],a[244:247],v[68:71],v[32:35]
v_mul_f32 v41,v41,v81
v_mul_f32 v42,v42,v82
v_mfma_f32_16x16x32_bf16 v[28:31],a[248:251],v[72:75],v[28:31]
buffer_load_dwordx4 v0,s[36:39],s67 offen offset:1024 sc1 lds
v_mfma_f32_16x16x32_bf16 v[32:35],a[252:255],v[72:75],v[32:35]
s_waitcnt lgkmcnt(3)
ds_read_b128 v[68:71],v2 offset:22528
v_mul_f32 v43,v43,v83
v_mfma_f32_16x16x32_bf16 v[28:31],a[128:131],v[44:47],v[28:31]
s_waitcnt vmcnt(7) lgkmcnt(2)
ds_read_b128 v[72:75],v2 offset:23552
v_cvt_pk_bf16_f32 v86,v40,v41
v_mfma_f32_16x16x32_bf16 v[32:35],a[132:135],v[44:47],v[32:35]
s_barrier
v_mfma_f32_16x16x32_bf16 v[28:31],a[136:139],v[48:51],v[28:31]
ds_read_b128 v[116:119],v14 offset:0
ds_read_b128 v[120:123],v14 offset:16
v_mfma_f32_16x16x32_bf16 v[32:35],a[140:143],v[48:51],v[32:35]
ds_read_b128 v[44:47],v2 offset:40960
v_cvt_pk_bf16_f32 v87,v42,v43
v_mfma_f32_16x16x32_bf16 v[28:31],a[144:147],v[52:55],v[28:31]
buffer_load_dwordx4 v0,s[36:39],s67 offen offset:2048 sc1 lds
v_mfma_f32_16x16x32_bf16 v[32:35],a[148:151],v[52:55],v[32:35]
ds_read_b128 v[48:51],v2 offset:41984
v_mfma_f32_16x16x32_bf16 v[28:31],a[152:155],v[56:59],v[28:31]
s_waitcnt lgkmcnt(2)
ds_read_b128 v[52:55],v2 offset:43008
v_add_f32 v116,v116,v117
v_mfma_f32_16x16x32_bf16 v[32:35],a[156:159],v[56:59],v[32:35]
v_add_f32 v118,v118,v119
v_add_f32 v116,v116,v118
v_mfma_f32_16x16x32_bf16 v[28:31],a[160:163],v[60:63],v[28:31]
buffer_load_dwordx4 v0,s[36:39],s67 offen offset:3072 sc1 lds
v_mfma_f32_16x16x32_bf16 v[32:35],a[164:167],v[60:63],v[32:35]
ds_read_b128 v[56:59],v2 offset:44032
v_mul_f32 v92,s75,v116
v_mfma_f32_16x16x32_bf16 v[28:31],a[168:171],v[64:67],v[28:31]
ds_read_b128 v[60:63],v2 offset:45056
v_add_f32 v120,v120,v121
v_mfma_f32_16x16x32_bf16 v[32:35],a[172:175],v[64:67],v[32:35]
v_add_f32 v122,v122,v123
v_add_f32 v120,v120,v122
v_mfma_f32_16x16x32_bf16 v[28:31],a[176:179],v[68:71],v[28:31]
buffer_store_dwordx4 v[84:87],v3,s[36:39],s73 offen offset:0 sc0 sc1
v_mul_f32 v120,s75,v120
v_fma_f32 v120,-v92,v92,v120
v_max_f32 v120,0,v120
v_add_f32 v115,v15,v120
v_fma_f32 v120,v15,v115,v120
v_rsq_f32 v115,v115
v_mfma_f32_16x16x32_bf16 v[32:35],a[180:183],v[68:71],v[32:35]
ds_read_b128 v[64:67],v2 offset:46080
v_rsq_f32 v116,v120
v_mul_f32_e64 v114,-v92,v115
v_mfma_f32_16x16x32_bf16 v[28:31],a[184:187],v[72:75],v[28:31]
s_waitcnt lgkmcnt(2)
ds_read_b128 v[68:71],v2 offset:47104
buffer_store_dwordx2 v[114:115],v12,s[36:39],s65 offen offset:0 sc0 sc1
v_mul_f32 v93,s76,v116
v_mul_f32 v94,v116,v116
v_mul_f32 v95,v120,v116
v_mul_f32 v94,s77,v94
v_mul_f32 v94,v94,v93
v_mfma_f32_16x16x32_bf16 v[32:35],a[188:191],v[72:75],v[32:35]
s_cbranch_scc1 Lg1_slow1_%=
Lg1_a1w2_%=:
v_mfma_f32_16x16x32_bf16 v[36:39],a[64:67],v[44:47],0
s_sub_u32 s69,s67,1048576
ds_read_b128 v[72:75],v2 offset:48128
s_cmp_lt_u32 s69,2097152
v_mfma_f32_16x16x32_bf16 v[40:43],a[68:71],v[44:47],0
s_cselect_b32 s89,3145728,s89
s_sub_u32 s69,s67,3145728
s_cmp_lt_u32 s69,1048576
v_mfma_f32_16x16x32_bf16 v[104:107],v[100:103],v[44:47],0
s_cselect_b32 s89,4194304,s89
s_add_u32 m0,s62,16384
s_lshr_b32 s73,s48,0
v_mfma_f32_16x16x32_bf16 v[108:111],v[44:47],v[44:47],0
buffer_load_dwordx4 v1,s[36:39],s67 offen offset:0 sc1 lds
v_mfma_f32_16x16x32_bf16 v[36:39],a[72:75],v[48:51],v[36:39]
s_lshr_b32 s65,s49,6
ds_read_b128 v[44:47],v2 offset:32768
v_fma_f32 v28,-v92,v20,v28
v_mfma_f32_16x16x32_bf16 v[40:43],a[76:79],v[48:51],v[40:43]
s_lshr_b32 s72,s67,6
v_fma_f32 v29,-v92,v21,v29
v_fma_f32 v30,-v92,v22,v30
v_mfma_f32_16x16x32_bf16 v[104:107],v[100:103],v[48:51],v[104:107]
s_lshr_b32 s71,s62,2
v_fma_f32 v31,-v92,v23,v31
v_mul_f32 v76,v28,v28
v_mfma_f32_16x16x32_bf16 v[108:111],v[48:51],v[48:51],v[108:111]
buffer_load_dwordx4 v1,s[36:39],s67 offen offset:1024 sc1 lds
v_mfma_f32_16x16x32_bf16 v[36:39],a[80:83],v[52:55],v[36:39]
s_cmp_lt_u32 s67,4194304
ds_read_b128 v[48:51],v2 offset:33792
v_mul_f32 v77,v29,v29
v_mfma_f32_16x16x32_bf16 v[40:43],a[84:87],v[52:55],v[40:43]
s_cselect_b32 s72,s59,s72
v_mul_f32 v78,v30,v30
v_mul_f32 v79,v31,v31
v_mfma_f32_16x16x32_bf16 v[104:107],v[100:103],v[52:55],v[104:107]
s_mov_b64 s[44:45],s[46:47]
v_fma_f32 v76,v94,v76,v93
v_mul_f32 v76,v28,v76
v_mfma_f32_16x16x32_bf16 v[108:111],v[52:55],v[52:55],v[108:111]
buffer_load_dwordx4 v1,s[36:39],s67 offen offset:2048 sc1 lds
v_mfma_f32_16x16x32_bf16 v[36:39],a[88:91],v[56:59],v[36:39]
s_mov_b64 s[46:47],s[48:49]
ds_read_b128 v[52:55],v2 offset:34816
v_fma_f32 v77,v94,v77,v93
v_mfma_f32_16x16x32_bf16 v[40:43],a[92:95],v[56:59],v[40:43]
s_mov_b64 s[48:49],s[50:51]
v_exp_f32 v76,v76
v_mfma_f32_16x16x32_bf16 v[104:107],v[100:103],v[56:59],v[104:107]
v_mul_f32 v77,v29,v77
v_fma_f32 v76,v76,v95,v95
v_mfma_f32_16x16x32_bf16 v[108:111],v[56:59],v[56:59],v[108:111]
s_waitcnt lgkmcnt(3)
buffer_load_dwordx4 v1,s[36:39],s67 offen offset:3072 sc1 lds
v_mfma_f32_16x16x32_bf16 v[36:39],a[96:99],v[60:63],v[36:39]
s_add_u32 m0,s71,s88
ds_read_b128 v[56:59],v2 offset:35840
v_fma_f32 v78,v94,v78,v93
v_mfma_f32_16x16x32_bf16 v[40:43],a[100:103],v[60:63],v[40:43]
buffer_load_dword v10,s[36:39],s72 offen offset:0 sc1 lds
v_mfma_f32_16x16x32_bf16 v[36:39],a[104:107],v[64:67],v[36:39]
s_xor_b32 s87,s63,65536
ds_read_b128 v[60:63],v2 offset:36864
v_fma_f32 v79,v94,v79,v93
v_mfma_f32_16x16x32_bf16 v[40:43],a[108:111],v[64:67],v[40:43]
s_cmp_ge_u32 s67,s59)"
R"(
v_exp_f32 v77,v77
v_mfma_f32_16x16x32_bf16 v[36:39],a[112:115],v[68:71],v[36:39]
s_cselect_b32 s86,s50,s51
ds_read_b128 v[64:67],v2 offset:37888
v_mul_f32 v78,v30,v78
v_mfma_f32_16x16x32_bf16 v[40:43],a[116:119],v[68:71],v[40:43]
s_cselect_b32 s87,s87,s64
v_exp_f32 v78,v78
v_mfma_f32_16x16x32_bf16 v[36:39],a[120:123],v[72:75],v[36:39]
s_mov_b32 s62,s87
ds_read_b128 v[68:71],v2 offset:38912
v_mul_f32 v79,v31,v79
v_mfma_f32_16x16x32_bf16 v[40:43],a[124:127],v[72:75],v[40:43]
s_waitcnt lgkmcnt(3)
v_exp_f32 v79,v79
v_mfma_f32_16x16x32_bf16 v[36:39],a[0:3],v[44:47],v[36:39]
s_waitcnt vmcnt(10)
ds_read_b128 v[72:75],v2 offset:39936
v_fma_f32 v77,v77,v95,v95
v_mfma_f32_16x16x32_bf16 v[40:43],a[4:7],v[44:47],v[40:43]
s_barrier
v_mfma_f32_16x16x32_bf16 v[36:39],a[8:11],v[48:51],v[36:39]
s_cmp_lt_u32 s86,s89
ds_read_b128 v[44:47],v2 offset:57344
ds_read_b32 v16,v7 offset:256
v_mfma_f32_16x16x32_bf16 v[40:43],a[12:15],v[48:51],v[40:43]
s_mov_b32 m0,s62
v_rcp_f32 v76,v76
v_mfma_f32_16x16x32_bf16 v[36:39],a[16:19],v[52:55],v[36:39]
s_mov_b64 s[50:51],s[52:53]
ds_read_b128 v[48:51],v2 offset:58368
v_fma_f32 v78,v78,v95,v95
v_mfma_f32_16x16x32_bf16 v[40:43],a[20:23],v[52:55],v[40:43]
v_rcp_f32 v77,v77
v_mfma_f32_16x16x32_bf16 v[36:39],a[24:27],v[56:59],v[36:39]
s_waitcnt lgkmcnt(1)
ds_read_b128 v[52:55],v2 offset:59392
v_fma_f32 v79,v79,v95,v95
v_mfma_f32_16x16x32_bf16 v[40:43],a[28:31],v[56:59],v[40:43]
v_cmp_eq_u32 vcc,s60,v16
v_rcp_f32 v78,v78
v_mul_f32 v28,v28,v76
v_mfma_f32_16x16x32_bf16 v[36:39],a[32:35],v[60:63],v[36:39]
s_cselect_b32 vcc_lo,-1,vcc_lo
ds_read_b128 v[56:59],v2 offset:60416
v_mul_f32 v29,v29,v77
v_mfma_f32_16x16x32_bf16 v[40:43],a[36:39],v[60:63],v[40:43]
s_cmp_eq_u32 vcc_lo,-1
v_rcp_f32 v79,v79
v_mfma_f32_16x16x32_bf16 v[36:39],a[40:43],v[64:67],v[36:39]
s_cselect_b32 s66,s86,s59
ds_read_b128 v[60:63],v2 offset:61440
v_mul_f32 v30,v30,v78
v_mfma_f32_16x16x32_bf16 v[40:43],a[44:47],v[64:67],v[40:43]
s_add_u32 s74,s53,s55
v_mul_f32 v31,v31,v79
v_cvt_pk_bf16_f32 v84,v28,v29
v_mfma_f32_16x16x32_bf16 v[36:39],a[48:51],v[68:71],v[36:39]
s_cmp_eq_u32 s74,s56
ds_read_b128 v[64:67],v2 offset:62464
v_cvt_pk_bf16_f32 v85,v30,v31
v_mfma_f32_16x16x32_bf16 v[40:43],a[52:55],v[68:71],v[40:43]
s_cselect_b32 s74,s57,s74
v_fma_f32 v32,-v92,v24,v32
v_fma_f32 v33,-v92,v25,v33
v_mfma_f32_16x16x32_bf16 v[36:39],a[56:59],v[72:75],v[36:39]
s_cmp_eq_u32 s74,s58
ds_read_b128 v[68:71],v2 offset:63488
v_fma_f32 v34,-v92,v26,v34
v_mfma_f32_16x16x32_bf16 v[40:43],a[60:63],v[72:75],v[40:43]
s_waitcnt lgkmcnt(3)
v_fma_f32 v35,-v92,v27,v35
v_mul_f32 v80,v32,v32
v_mfma_f32_16x16x32_bf16 v[36:39],a[192:195],v[44:47],v[36:39]
s_cselect_b32 s74,s59,s74
ds_read_b128 v[72:75],v2 offset:64512
v_mul_f32 v81,v33,v33
v_mfma_f32_16x16x32_bf16 v[40:43],a[196:199],v[44:47],v[40:43]
s_xor_b32 s64,s64,65536
v_mul_f32 v82,v34,v34
v_mul_f32 v83,v35,v35
v_mfma_f32_16x16x32_bf16 v[104:107],v[100:103],v[44:47],v[104:107]
s_mov_b32 s52,s74
v_fma_f32 v80,v94,v80,v93
v_mul_f32 v80,v32,v80
v_mfma_f32_16x16x32_bf16 v[108:111],v[44:47],v[44:47],v[108:111]
s_cmp_lt_u32 s67,4194304
v_exp_f32 v80,v80
v_mfma_f32_16x16x32_bf16 v[36:39],a[200:203],v[48:51],v[36:39]
ds_read_b128 v[44:47],v2 offset:49152
v_fma_f32 v80,v80,v95,v95
v_mfma_f32_16x16x32_bf16 v[40:43],a[204:207],v[48:51],v[40:43]
v_rcp_f32 v80,v80
v_mfma_f32_16x16x32_bf16 v[104:107],v[100:103],v[48:51],v[104:107]
v_fma_f32 v81,v94,v81,v93
v_mul_f32 v81,v33,v81
v_mfma_f32_16x16x32_bf16 v[108:111],v[48:51],v[48:51],v[108:111]
v_exp_f32 v81,v81
v_mfma_f32_16x16x32_bf16 v[36:39],a[208:211],v[52:55],v[36:39]
ds_read_b128 v[48:51],v2 offset:50176
v_fma_f32 v81,v81,v95,v95
v_mfma_f32_16x16x32_bf16 v[40:43],a[212:215],v[52:55],v[40:43]
v_rcp_f32 v81,v81
v_mfma_f32_16x16x32_bf16 v[104:107],v[100:103],v[52:55],v[104:107]
v_fma_f32 v82,v94,v82,v93
v_mul_f32 v82,v34,v82
v_mfma_f32_16x16x32_bf16 v[108:111],v[52:55],v[52:55],v[108:111]
v_exp_f32 v82,v82
v_mfma_f32_16x16x32_bf16 v[36:39],a[216:219],v[56:59],v[36:39]
ds_read_b128 v[52:55],v2 offset:51200
v_fma_f32 v82,v82,v95,v95
v_mfma_f32_16x16x32_bf16 v[40:43],a[220:223],v[56:59],v[40:43]
v_rcp_f32 v82,v82
v_mfma_f32_16x16x32_bf16 v[104:107],v[100:103],v[56:59],v[104:107]
v_fma_f32 v83,v94,v83,v93
v_mul_f32 v83,v35,v83
v_mfma_f32_16x16x32_bf16 v[108:111],v[56:59],v[56:59],v[108:111]
s_waitcnt lgkmcnt(3)
v_exp_f32 v83,v83
v_mfma_f32_16x16x32_bf16 v[36:39],a[224:227],v[60:63],v[36:39]
ds_read_b128 v[56:59],v2 offset:52224
v_fma_f32 v83,v83,v95,v95
v_mfma_f32_16x16x32_bf16 v[40:43],a[228:231],v[60:63],v[40:43]
v_rcp_f32 v83,v83
v_mfma_f32_16x16x32_bf16 v[36:39],a[232:235],v[64:67],v[36:39]
buffer_load_dwordx4 v0,s[36:39],s66 offen offset:0 sc1 lds
v_cndmask_b32 v112,v108,v109,s[82:83]
v_cndmask_b32 v113,v110,v111,s[82:83]
v_cndmask_b32 v112,v112,v113,s[84:85]
v_cndmask_b32 v112,v104,v112,s[98:99]
v_mfma_f32_16x16x32_bf16 v[40:43],a[236:239],v[64:67],v[40:43]
ds_read_b128 v[60:63],v2 offset:53248
ds_write_b32 v13,v112 offset:2048
v_mfma_f32_16x16x32_bf16 v[36:39],a[240:243],v[68:71],v[36:39]
ds_read_b128 v[64:67],v2 offset:54272
v_mul_f32 v32,v32,v80
v_mfma_f32_16x16x32_bf16 v[40:43],a[244:247],v[68:71],v[40:43]
v_mul_f32 v33,v33,v81
v_mul_f32 v34,v34,v82
v_mfma_f32_16x16x32_bf16 v[36:39],a[248:251],v[72:75],v[36:39]
buffer_load_dwordx4 v0,s[36:39],s66 offen offset:1024 sc1 lds
v_mfma_f32_16x16x32_bf16 v[40:43],a[252:255],v[72:75],v[40:43]
s_waitcnt lgkmcnt(3)
ds_read_b128 v[68:71],v2 offset:55296
v_mul_f32 v35,v35,v83
v_mfma_f32_16x16x32_bf16 v[36:39],a[128:131],v[44:47],v[36:39]
s_waitcnt vmcnt(7) lgkmcnt(2)
ds_read_b128 v[72:75],v2 offset:56320
v_xor_b32 v2,65536,v2
v_mfma_f32_16x16x32_bf16 v[40:43],a[132:135],v[44:47],v[40:43]
s_barrier
v_mfma_f32_16x16x32_bf16 v[36:39],a[136:139],v[48:51],v[36:39]
ds_read_b128 v[116:119],v14 offset:2048
ds_read_b128 v[120:123],v14 offset:2064
v_mfma_f32_16x16x32_bf16 v[40:43],a[140:143],v[48:51],v[40:43]
ds_read_b128 v[44:47],v2 offset:8192
v_cvt_pk_bf16_f32 v86,v32,v33
v_mfma_f32_16x16x32_bf16 v[36:39],a[144:147],v[52:55],v[36:39]
buffer_load_dwordx4 v0,s[36:39],s66 offen offset:2048 sc1 lds
v_mfma_f32_16x16x32_bf16 v[40:43],a[148:151],v[52:55],v[40:43]
ds_read_b128 v[48:51],v2 offset:9216
v_cvt_pk_bf16_f32 v87,v34,v35
v_mfma_f32_16x16x32_bf16 v[36:39],a[152:155],v[56:59],v[36:39]
s_waitcnt lgkmcnt(2)
ds_read_b128 v[52:55],v2 offset:10240
v_xor_b32 v8,16384,v8
v_mfma_f32_16x16x32_bf16 v[40:43],a[156:159],v[56:59],v[40:43]
v_add_f32 v116,v116,v117
v_add_f32 v118,v118,v119
v_mfma_f32_16x16x32_bf16 v[36:39],a[160:163],v[60:63],v[36:39]
buffer_load_dwordx4 v0,s[36:39],s66 offen offset:3072 sc1 lds
v_mfma_f32_16x16x32_bf16 v[40:43],a[164:167],v[60:63],v[40:43]
ds_read_b128 v[56:59],v2 offset:11264
v_add_f32 v116,v116,v118
v_mfma_f32_16x16x32_bf16 v[36:39],a[168:171],v[64:67],v[36:39]
ds_read_b128 v[60:63],v2 offset:12288
v_mul_f32 v96,s75,v116
v_mfma_f32_16x16x32_bf16 v[40:43],a[172:175],v[64:67],v[40:43]
v_add_f32 v120,v120,v121
v_add_f32 v122,v122,v123
v_mfma_f32_16x16x32_bf16 v[36:39],a[176:179],v[68:71],v[36:39]
buffer_store_dwordx4 v[84:87],v3,s[36:39],s73 offen offset:0 sc0 sc1
v_add_f32 v120,v120,v122
v_mul_f32 v120,s75,v120
v_fma_f32 v120,-v96,v96,v120
v_max_f32 v120,0,v120
v_add_f32 v115,v15,v120
v_fma_f32 v120,v15,v115,v120
v_rsq_f32 v115,v115
v_mfma_f32_16x16x32_bf16 v[40:43],a[180:183],v[68:71],v[40:43]
ds_read_b128 v[64:67],v2 offset:13312
v_rsq_f32 v116,v120
v_mul_f32_e64 v114,-v96,v115
v_mfma_f32_16x16x32_bf16 v[36:39],a[184:187],v[72:75],v[36:39]
s_waitcnt lgkmcnt(2)
ds_read_b128 v[68:71],v2 offset:14336
buffer_store_dwordx2 v[114:115],v12,s[36:39],s65 offen offset:0 sc0 sc1
v_mul_f32 v97,s76,v116
v_mul_f32 v98,v116,v116
v_mul_f32 v99,v120,v116)"
R"(
v_mul_f32 v98,s77,v98
v_mul_f32 v98,v98,v97
v_mfma_f32_16x16x32_bf16 v[40:43],a[188:191],v[72:75],v[40:43]
s_cbranch_scc1 Lg1_a0w2_%=
s_cmp_ge_u32 s48,4194304
s_cselect_b64 s[90:91],s[94:95],s[90:91]
s_cselect_b64 s[92:93],s[96:97],s[92:93]
s_cmp_lt_u32 s67,s59
s_cbranch_scc1 Lg1_d0w2_%=
s_branch Lg1_slow0_%=
Lg1_a0w3_%=:
v_mfma_f32_16x16x32_bf16 v[28:31],a[96:99],v[44:47],0
s_sub_u32 s69,s66,1048576
ds_read_b128 v[72:75],v2 offset:11264
s_cmp_lt_u32 s69,2097152
v_mfma_f32_16x16x32_bf16 v[32:35],a[100:103],v[44:47],0
s_cselect_b32 s89,3145728,s89
s_sub_u32 s69,s66,3145728
s_cmp_lt_u32 s69,1048576
v_mfma_f32_16x16x32_bf16 v[104:107],v[100:103],v[44:47],0
s_cselect_b32 s89,4194304,s89
s_add_u32 m0,s62,16384
s_lshr_b32 s73,s47,0
v_mfma_f32_16x16x32_bf16 v[108:111],v[44:47],v[44:47],0
s_lshr_b32 s72,s66,6
v_fma_f32 v36,-v96,v20,v36
v_fma_f32 v37,-v96,v21,v37
v_mfma_f32_16x16x32_bf16 v[28:31],a[104:107],v[48:51],v[28:31]
buffer_load_dwordx4 v1,s[36:39],s66 offen offset:0 sc1 lds
v_mfma_f32_16x16x32_bf16 v[32:35],a[108:111],v[48:51],v[32:35]
s_lshr_b32 s71,s62,2
ds_read_b128 v[44:47],v2 offset:4096
v_fma_f32 v38,-v96,v22,v38
v_mfma_f32_16x16x32_bf16 v[104:107],v[100:103],v[48:51],v[104:107]
s_cmp_lt_u32 s66,4194304
v_fma_f32 v39,-v96,v23,v39
v_mul_f32 v76,v36,v36
v_mfma_f32_16x16x32_bf16 v[108:111],v[48:51],v[48:51],v[108:111]
s_cselect_b32 s72,s59,s72
v_mul_f32 v77,v37,v37
v_mul_f32 v78,v38,v38
v_mfma_f32_16x16x32_bf16 v[28:31],a[112:115],v[52:55],v[28:31]
buffer_load_dwordx4 v1,s[36:39],s66 offen offset:1024 sc1 lds
v_mfma_f32_16x16x32_bf16 v[32:35],a[116:119],v[52:55],v[32:35]
ds_read_b128 v[48:51],v2 offset:5120
v_mul_f32 v79,v39,v39
v_mfma_f32_16x16x32_bf16 v[104:107],v[100:103],v[52:55],v[104:107]
v_fma_f32 v76,v98,v76,v97
v_mul_f32 v76,v36,v76
v_mfma_f32_16x16x32_bf16 v[108:111],v[52:55],v[52:55],v[108:111]
v_exp_f32 v76,v76
v_mfma_f32_16x16x32_bf16 v[28:31],a[120:123],v[56:59],v[28:31]
buffer_load_dwordx4 v1,s[36:39],s66 offen offset:2048 sc1 lds
v_mfma_f32_16x16x32_bf16 v[32:35],a[124:127],v[56:59],v[32:35]
ds_read_b128 v[52:55],v2 offset:6144
v_fma_f32 v76,v76,v99,v99
v_mfma_f32_16x16x32_bf16 v[104:107],v[100:103],v[56:59],v[104:107]
v_rcp_f32 v76,v76
v_mfma_f32_16x16x32_bf16 v[108:111],v[56:59],v[56:59],v[108:111]
s_waitcnt lgkmcnt(3)
v_fma_f32 v77,v98,v77,v97
v_mul_f32 v77,v37,v77
v_mfma_f32_16x16x32_bf16 v[28:31],a[64:67],v[60:63],v[28:31]
buffer_load_dwordx4 v1,s[36:39],s66 offen offset:3072 sc1 lds
v_mfma_f32_16x16x32_bf16 v[32:35],a[68:71],v[60:63],v[32:35]
s_add_u32 m0,s71,s88
ds_read_b128 v[56:59],v2 offset:7168
v_fma_f32 v78,v98,v78,v97
v_mfma_f32_16x16x32_bf16 v[28:31],a[72:75],v[64:67],v[28:31]
buffer_load_dword v10,s[36:39],s72 offen offset:0 sc1 lds
v_mfma_f32_16x16x32_bf16 v[32:35],a[76:79],v[64:67],v[32:35]
s_xor_b32 s87,s64,65536
ds_read_b128 v[60:63],v2 offset:0
v_mul_f32 v78,v38,v78
v_mfma_f32_16x16x32_bf16 v[28:31],a[80:83],v[68:71],v[28:31]
s_cmp_ge_u32 s66,s59
ds_read_b128 v[64:67],v2 offset:1024
v_fma_f32 v79,v98,v79,v97
v_mfma_f32_16x16x32_bf16 v[32:35],a[84:87],v[68:71],v[32:35]
s_cselect_b32 s86,s49,s50
v_exp_f32 v77,v77
v_mfma_f32_16x16x32_bf16 v[28:31],a[88:91],v[72:75],v[28:31]
s_cselect_b32 s87,s87,s63
ds_read_b128 v[68:71],v2 offset:2048
v_mul_f32 v79,v39,v79
v_mfma_f32_16x16x32_bf16 v[32:35],a[92:95],v[72:75],v[32:35]
s_waitcnt lgkmcnt(3)
v_exp_f32 v78,v78
v_mfma_f32_16x16x32_bf16 v[28:31],a[32:35],v[44:47],v[28:31]
s_waitcnt vmcnt(11)
ds_read_b128 v[72:75],v2 offset:3072
v_fma_f32 v77,v77,v99,v99
v_mfma_f32_16x16x32_bf16 v[32:35],a[36:39],v[44:47],v[32:35]
s_barrier
v_mfma_f32_16x16x32_bf16 v[28:31],a[40:43],v[48:51],v[28:31]
s_mov_b32 s62,s87
ds_read_b128 v[44:47],v2 offset:28672
ds_read_b32 v16,v7 offset:0
v_mfma_f32_16x16x32_bf16 v[32:35],a[44:47],v[48:51],v[32:35]
s_cmp_lt_u32 s86,s89
v_exp_f32 v79,v79
v_mfma_f32_16x16x32_bf16 v[28:31],a[48:51],v[52:55],v[28:31]
s_mov_b32 m0,s62
ds_read_b128 v[48:51],v2 offset:29696
v_fma_f32 v78,v78,v99,v99
v_mfma_f32_16x16x32_bf16 v[32:35],a[52:55],v[52:55],v[32:35]
v_rcp_f32 v77,v77
v_mfma_f32_16x16x32_bf16 v[28:31],a[56:59],v[56:59],v[28:31]
s_waitcnt lgkmcnt(1)
ds_read_b128 v[52:55],v2 offset:30720
v_fma_f32 v79,v79,v99,v99
v_mfma_f32_16x16x32_bf16 v[32:35],a[60:63],v[56:59],v[32:35]
v_cmp_eq_u32 vcc,s60,v16
v_rcp_f32 v78,v78
v_mul_f32 v36,v36,v76
v_mfma_f32_16x16x32_bf16 v[28:31],a[0:3],v[60:63],v[28:31]
s_cselect_b32 vcc_lo,-1,vcc_lo
ds_read_b128 v[56:59],v2 offset:31744
v_mul_f32 v37,v37,v77
v_mfma_f32_16x16x32_bf16 v[32:35],a[4:7],v[60:63],v[32:35]
s_cmp_eq_u32 vcc_lo,-1
v_rcp_f32 v79,v79
v_mfma_f32_16x16x32_bf16 v[28:31],a[8:11],v[64:67],v[28:31]
s_cselect_b32 s67,s86,s59
ds_read_b128 v[60:63],v2 offset:24576
v_mul_f32 v38,v38,v78
v_mfma_f32_16x16x32_bf16 v[32:35],a[12:15],v[64:67],v[32:35]
s_add_u32 s53,s52,s55
v_mul_f32 v39,v39,v79
v_cvt_pk_bf16_f32 v84,v36,v37
v_mfma_f32_16x16x32_bf16 v[28:31],a[16:19],v[68:71],v[28:31]
s_cmp_eq_u32 s53,s56
ds_read_b128 v[64:67],v2 offset:25600
v_cvt_pk_bf16_f32 v85,v38,v39
v_mfma_f32_16x16x32_bf16 v[32:35],a[20:23],v[68:71],v[32:35]
s_cselect_b32 s53,s57,s53
v_fma_f32 v40,-v96,v24,v40
v_fma_f32 v41,-v96,v25,v41
v_mfma_f32_16x16x32_bf16 v[28:31],a[24:27],v[72:75],v[28:31]
s_cmp_eq_u32 s53,s58
ds_read_b128 v[68:71],v2 offset:26624
v_fma_f32 v42,-v96,v26,v42
v_mfma_f32_16x16x32_bf16 v[32:35],a[28:31],v[72:75],v[32:35]
s_waitcnt lgkmcnt(3)
v_fma_f32 v43,-v96,v27,v43
v_mul_f32 v80,v40,v40
v_mfma_f32_16x16x32_bf16 v[28:31],a[224:227],v[44:47],v[28:31]
s_cselect_b32 s53,s59,s53
ds_read_b128 v[72:75],v2 offset:27648
v_mul_f32 v81,v41,v41
v_mfma_f32_16x16x32_bf16 v[32:35],a[228:231],v[44:47],v[32:35]
s_xor_b32 s63,s63,65536
v_mul_f32 v82,v42,v42
v_mul_f32 v83,v43,v43
v_mfma_f32_16x16x32_bf16 v[104:107],v[100:103],v[44:47],v[104:107]
s_lshr_b32 s70,s46,5
v_fma_f32 v80,v98,v80,v97
v_mul_f32 v80,v40,v80
v_mfma_f32_16x16x32_bf16 v[108:111],v[44:47],v[44:47],v[108:111]
s_cmp_ge_u32 s66,s59
v_exp_f32 v80,v80
v_mfma_f32_16x16x32_bf16 v[28:31],a[232:235],v[48:51],v[28:31]
ds_read_b128 v[44:47],v2 offset:20480
v_fma_f32 v80,v80,v99,v99
v_mfma_f32_16x16x32_bf16 v[32:35],a[236:239],v[48:51],v[32:35]
v_rcp_f32 v80,v80
v_mfma_f32_16x16x32_bf16 v[104:107],v[100:103],v[48:51],v[104:107]
v_fma_f32 v81,v98,v81,v97
v_mul_f32 v81,v41,v81
v_mfma_f32_16x16x32_bf16 v[108:111],v[48:51],v[48:51],v[108:111]
v_exp_f32 v81,v81
v_mfma_f32_16x16x32_bf16 v[28:31],a[240:243],v[52:55],v[28:31]
ds_read_b128 v[48:51],v2 offset:21504
v_fma_f32 v81,v81,v99,v99
v_mfma_f32_16x16x32_bf16 v[32:35],a[244:247],v[52:55],v[32:35]
v_rcp_f32 v81,v81
v_mfma_f32_16x16x32_bf16 v[104:107],v[100:103],v[52:55],v[104:107]
v_fma_f32 v82,v98,v82,v97
v_mul_f32 v82,v42,v82
v_mfma_f32_16x16x32_bf16 v[108:111],v[52:55],v[52:55],v[108:111]
v_exp_f32 v82,v82
v_mfma_f32_16x16x32_bf16 v[28:31],a[248:251],v[56:59],v[28:31]
ds_read_b128 v[52:55],v2 offset:22528
v_fma_f32 v82,v82,v99,v99
v_mfma_f32_16x16x32_bf16 v[32:35],a[252:255],v[56:59],v[32:35]
v_rcp_f32 v82,v82
v_mfma_f32_16x16x32_bf16 v[104:107],v[100:103],v[56:59],v[104:107]
v_fma_f32 v83,v98,v83,v97
v_mul_f32 v83,v43,v83
v_mfma_f32_16x16x32_bf16 v[108:111],v[56:59],v[56:59],v[108:111]
s_waitcnt lgkmcnt(3)
v_exp_f32 v83,v83
v_mfma_f32_16x16x32_bf16 v[28:31],a[192:195],v[60:63],v[28:31]
ds_read_b128 v[56:59],v2 offset:23552
v_fma_f32 v83,v83,v99,v99
v_mfma_f32_16x16x32_bf16 v[32:35],a[196:199],v[60:63],v[32:35]
v_rcp_f32 v83,v83
v_mfma_f32_16x16x32_bf16 v[28:31],a[200:203],v[64:67],v[28:31]
v_cndmask_b32 v112,v108,v109,s[82:83]
v_cndmask_b32 v113,v110,v111,s[82:83])"
R"(
v_cndmask_b32 v112,v112,v113,s[84:85]
v_cndmask_b32 v112,v104,v112,s[98:99]
v_mfma_f32_16x16x32_bf16 v[32:35],a[204:207],v[64:67],v[32:35]
ds_read_b128 v[60:63],v2 offset:16384
ds_write_b32 v13,v112 offset:0
buffer_load_dwordx4 v0,s[36:39],s67 offen offset:0 sc1 lds
v_mfma_f32_16x16x32_bf16 v[28:31],a[208:211],v[68:71],v[28:31]
ds_read_b128 v[64:67],v2 offset:17408
v_mul_f32 v40,v40,v80
v_mfma_f32_16x16x32_bf16 v[32:35],a[212:215],v[68:71],v[32:35]
v_mul_f32 v41,v41,v81
v_mul_f32 v42,v42,v82
v_mfma_f32_16x16x32_bf16 v[28:31],a[216:219],v[72:75],v[28:31]
ds_read_b128 v[68:71],v2 offset:18432
v_mul_f32 v43,v43,v83
v_mfma_f32_16x16x32_bf16 v[32:35],a[220:223],v[72:75],v[32:35]
s_waitcnt lgkmcnt(4)
buffer_load_dwordx4 v0,s[36:39],s67 offen offset:1024 sc1 lds
v_mfma_f32_16x16x32_bf16 v[28:31],a[160:163],v[44:47],v[28:31]
s_waitcnt vmcnt(7) lgkmcnt(2)
ds_read_b128 v[72:75],v2 offset:19456
v_cvt_pk_bf16_f32 v86,v40,v41
v_mfma_f32_16x16x32_bf16 v[32:35],a[164:167],v[44:47],v[32:35]
s_barrier
v_mfma_f32_16x16x32_bf16 v[28:31],a[168:171],v[48:51],v[28:31]
buffer_store_dword v5,v4,s[36:39],s70 offen offset:0 sc0 sc1
ds_read_b128 v[116:119],v14 offset:0
ds_read_b128 v[120:123],v14 offset:16
ds_read_b128 v[44:47],v2 offset:45056
v_mfma_f32_16x16x32_bf16 v[32:35],a[172:175],v[48:51],v[32:35]
v_cvt_pk_bf16_f32 v87,v42,v43
v_mfma_f32_16x16x32_bf16 v[28:31],a[176:179],v[52:55],v[28:31]
ds_read_b128 v[48:51],v2 offset:46080
v_mfma_f32_16x16x32_bf16 v[32:35],a[180:183],v[52:55],v[32:35]
buffer_load_dwordx4 v0,s[36:39],s67 offen offset:2048 sc1 lds
v_mfma_f32_16x16x32_bf16 v[28:31],a[184:187],v[56:59],v[28:31]
s_waitcnt lgkmcnt(2)
ds_read_b128 v[52:55],v2 offset:47104
v_add_f32 v116,v116,v117
v_mfma_f32_16x16x32_bf16 v[32:35],a[188:191],v[56:59],v[32:35]
v_add_f32 v118,v118,v119
v_add_f32 v116,v116,v118
v_mfma_f32_16x16x32_bf16 v[28:31],a[128:131],v[60:63],v[28:31]
ds_read_b128 v[56:59],v2 offset:48128
v_mul_f32 v92,s75,v116
v_mfma_f32_16x16x32_bf16 v[32:35],a[132:135],v[60:63],v[32:35]
buffer_load_dwordx4 v0,s[36:39],s67 offen offset:3072 sc1 lds
v_mfma_f32_16x16x32_bf16 v[28:31],a[136:139],v[64:67],v[28:31]
ds_read_b128 v[60:63],v2 offset:40960
v_add_f32 v120,v120,v121
v_mfma_f32_16x16x32_bf16 v[32:35],a[140:143],v[64:67],v[32:35]
v_add_f32 v122,v122,v123
v_add_f32 v120,v120,v122
v_mfma_f32_16x16x32_bf16 v[28:31],a[144:147],v[68:71],v[28:31]
ds_read_b128 v[64:67],v2 offset:41984
v_mul_f32 v120,s75,v120
v_mfma_f32_16x16x32_bf16 v[32:35],a[148:151],v[68:71],v[32:35]
buffer_store_dwordx4 v[84:87],v3,s[36:39],s73 offen offset:0 sc0 sc1
v_fma_f32 v120,-v92,v92,v120
v_max_f32 v120,0,v120
v_add_f32 v115,v15,v120
v_fma_f32 v120,v15,v115,v120
v_rsq_f32 v116,v120
v_mfma_f32_16x16x32_bf16 v[28:31],a[152:155],v[72:75],v[28:31]
s_waitcnt lgkmcnt(2)
ds_read_b128 v[68:71],v2 offset:43008
v_mul_f32 v93,s76,v116
v_mul_f32 v94,v116,v116
v_mul_f32 v95,v120,v116
v_mul_f32 v94,s77,v94
v_mul_f32 v94,v94,v93
v_mfma_f32_16x16x32_bf16 v[32:35],a[156:159],v[72:75],v[32:35]
s_cbranch_scc1 Lg1_slow1_%=
Lg1_a1w3_%=:
v_mfma_f32_16x16x32_bf16 v[36:39],a[96:99],v[44:47],0
s_sub_u32 s69,s67,1048576
ds_read_b128 v[72:75],v2 offset:44032
s_cmp_lt_u32 s69,2097152
v_mfma_f32_16x16x32_bf16 v[40:43],a[100:103],v[44:47],0
s_cselect_b32 s89,3145728,s89
s_sub_u32 s69,s67,3145728
s_cmp_lt_u32 s69,1048576
v_mfma_f32_16x16x32_bf16 v[104:107],v[100:103],v[44:47],0
s_cselect_b32 s89,4194304,s89
s_add_u32 m0,s62,16384
s_lshr_b32 s73,s48,0
v_mfma_f32_16x16x32_bf16 v[108:111],v[44:47],v[44:47],0
s_lshr_b32 s72,s67,6
v_fma_f32 v28,-v92,v20,v28
v_fma_f32 v29,-v92,v21,v29
v_mfma_f32_16x16x32_bf16 v[36:39],a[104:107],v[48:51],v[36:39]
buffer_load_dwordx4 v1,s[36:39],s67 offen offset:0 sc1 lds
v_mfma_f32_16x16x32_bf16 v[40:43],a[108:111],v[48:51],v[40:43]
s_lshr_b32 s71,s62,2
ds_read_b128 v[44:47],v2 offset:36864
v_fma_f32 v30,-v92,v22,v30
v_mfma_f32_16x16x32_bf16 v[104:107],v[100:103],v[48:51],v[104:107]
s_cmp_lt_u32 s67,4194304
v_fma_f32 v31,-v92,v23,v31
v_mul_f32 v76,v28,v28
v_mfma_f32_16x16x32_bf16 v[108:111],v[48:51],v[48:51],v[108:111]
s_cselect_b32 s72,s59,s72
v_mul_f32 v77,v29,v29
v_mul_f32 v78,v30,v30
v_mfma_f32_16x16x32_bf16 v[36:39],a[112:115],v[52:55],v[36:39]
buffer_load_dwordx4 v1,s[36:39],s67 offen offset:1024 sc1 lds
v_mfma_f32_16x16x32_bf16 v[40:43],a[116:119],v[52:55],v[40:43]
s_mov_b64 s[44:45],s[46:47]
ds_read_b128 v[48:51],v2 offset:37888
v_mul_f32 v79,v31,v31
v_mfma_f32_16x16x32_bf16 v[104:107],v[100:103],v[52:55],v[104:107]
v_fma_f32 v76,v94,v76,v93
v_mul_f32 v76,v28,v76
v_mfma_f32_16x16x32_bf16 v[108:111],v[52:55],v[52:55],v[108:111]
v_exp_f32 v76,v76
v_mfma_f32_16x16x32_bf16 v[36:39],a[120:123],v[56:59],v[36:39]
buffer_load_dwordx4 v1,s[36:39],s67 offen offset:2048 sc1 lds
v_mfma_f32_16x16x32_bf16 v[40:43],a[124:127],v[56:59],v[40:43]
ds_read_b128 v[52:55],v2 offset:38912
v_fma_f32 v76,v76,v95,v95
v_mfma_f32_16x16x32_bf16 v[104:107],v[100:103],v[56:59],v[104:107]
v_rcp_f32 v76,v76
v_mfma_f32_16x16x32_bf16 v[108:111],v[56:59],v[56:59],v[108:111]
s_waitcnt lgkmcnt(3)
v_fma_f32 v77,v94,v77,v93
v_mul_f32 v77,v29,v77
v_mfma_f32_16x16x32_bf16 v[36:39],a[64:67],v[60:63],v[36:39]
buffer_load_dwordx4 v1,s[36:39],s67 offen offset:3072 sc1 lds
v_mfma_f32_16x16x32_bf16 v[40:43],a[68:71],v[60:63],v[40:43]
s_add_u32 m0,s71,s88
ds_read_b128 v[56:59],v2 offset:39936
v_fma_f32 v78,v94,v78,v93
v_mfma_f32_16x16x32_bf16 v[36:39],a[72:75],v[64:67],v[36:39]
buffer_load_dword v10,s[36:39],s72 offen offset:0 sc1 lds
v_mfma_f32_16x16x32_bf16 v[40:43],a[76:79],v[64:67],v[40:43]
s_xor_b32 s87,s63,65536
ds_read_b128 v[60:63],v2 offset:32768
v_mul_f32 v78,v30,v78
v_mfma_f32_16x16x32_bf16 v[36:39],a[80:83],v[68:71],v[36:39]
s_cmp_ge_u32 s67,s59
ds_read_b128 v[64:67],v2 offset:33792
v_fma_f32 v79,v94,v79,v93
v_mfma_f32_16x16x32_bf16 v[40:43],a[84:87],v[68:71],v[40:43]
s_cselect_b32 s86,s50,s51
v_exp_f32 v77,v77
v_mfma_f32_16x16x32_bf16 v[36:39],a[88:91],v[72:75],v[36:39]
s_cselect_b32 s87,s87,s64
ds_read_b128 v[68:71],v2 offset:34816
v_mul_f32 v79,v31,v79
v_mfma_f32_16x16x32_bf16 v[40:43],a[92:95],v[72:75],v[40:43]
s_waitcnt lgkmcnt(3)
v_exp_f32 v78,v78
v_mfma_f32_16x16x32_bf16 v[36:39],a[32:35],v[44:47],v[36:39]
s_waitcnt vmcnt(11)
ds_read_b128 v[72:75],v2 offset:35840
v_fma_f32 v77,v77,v95,v95
v_mfma_f32_16x16x32_bf16 v[40:43],a[36:39],v[44:47],v[40:43]
s_barrier
v_mfma_f32_16x16x32_bf16 v[36:39],a[40:43],v[48:51],v[36:39]
s_mov_b32 s62,s87
ds_read_b128 v[44:47],v2 offset:61440
ds_read_b32 v16,v7 offset:256
v_mfma_f32_16x16x32_bf16 v[40:43],a[44:47],v[48:51],v[40:43]
s_cmp_lt_u32 s86,s89
v_exp_f32 v79,v79
v_mfma_f32_16x16x32_bf16 v[36:39],a[48:51],v[52:55],v[36:39]
s_mov_b32 m0,s62
ds_read_b128 v[48:51],v2 offset:62464
v_fma_f32 v78,v78,v95,v95
v_mfma_f32_16x16x32_bf16 v[40:43],a[52:55],v[52:55],v[40:43]
v_rcp_f32 v77,v77
v_mfma_f32_16x16x32_bf16 v[36:39],a[56:59],v[56:59],v[36:39]
s_waitcnt lgkmcnt(1)
ds_read_b128 v[52:55],v2 offset:63488
v_fma_f32 v79,v79,v95,v95
v_mfma_f32_16x16x32_bf16 v[40:43],a[60:63],v[56:59],v[40:43]
v_cmp_eq_u32 vcc,s60,v16
v_rcp_f32 v78,v78
v_mul_f32 v28,v28,v76
v_mfma_f32_16x16x32_bf16 v[36:39],a[0:3],v[60:63],v[36:39]
s_cselect_b32 vcc_lo,-1,vcc_lo
ds_read_b128 v[56:59],v2 offset:64512
v_mul_f32 v29,v29,v77
v_mfma_f32_16x16x32_bf16 v[40:43],a[4:7],v[60:63],v[40:43]
s_cmp_eq_u32 vcc_lo,-1
v_rcp_f32 v79,v79
v_mfma_f32_16x16x32_bf16 v[36:39],a[8:11],v[64:67],v[36:39]
s_cselect_b32 s66,s86,s59
ds_read_b128 v[60:63],v2 offset:57344
v_mul_f32 v30,v30,v78
v_mfma_f32_16x16x32_bf16 v[40:43],a[12:15],v[64:67],v[40:43]
s_add_u32 s74,s53,s55
v_mul_f32 v31,v31,v79
v_cvt_pk_bf16_f32 v84,v28,v29
v_mfma_f32_16x16x32_bf16 v[36:39],a[16:19],v[68:71],v[36:39]
s_cmp_eq_u32 s74,s56
ds_read_b128 v[64:67],v2 offset:58368
v_cvt_pk_bf16_f32 v85,v30,v31
v_mfma_f32_16x16x32_bf16 v[40:43],a[20:23],v[68:71],v[40:43]
s_cselect_b32 s74,s57,s74)"
R"(
v_fma_f32 v32,-v92,v24,v32
v_fma_f32 v33,-v92,v25,v33
v_mfma_f32_16x16x32_bf16 v[36:39],a[24:27],v[72:75],v[36:39]
s_cmp_eq_u32 s74,s58
ds_read_b128 v[68:71],v2 offset:59392
v_fma_f32 v34,-v92,v26,v34
v_mfma_f32_16x16x32_bf16 v[40:43],a[28:31],v[72:75],v[40:43]
s_waitcnt lgkmcnt(3)
v_fma_f32 v35,-v92,v27,v35
v_mul_f32 v80,v32,v32
v_mfma_f32_16x16x32_bf16 v[36:39],a[224:227],v[44:47],v[36:39]
s_cselect_b32 s74,s59,s74
ds_read_b128 v[72:75],v2 offset:60416
v_mul_f32 v81,v33,v33
v_mfma_f32_16x16x32_bf16 v[40:43],a[228:231],v[44:47],v[40:43]
s_xor_b32 s64,s64,65536
v_mul_f32 v82,v34,v34
v_mul_f32 v83,v35,v35
v_mfma_f32_16x16x32_bf16 v[104:107],v[100:103],v[44:47],v[104:107]
s_lshr_b32 s70,s47,5
v_fma_f32 v80,v94,v80,v93
v_mul_f32 v80,v32,v80
v_mfma_f32_16x16x32_bf16 v[108:111],v[44:47],v[44:47],v[108:111]
s_mov_b64 s[46:47],s[48:49]
v_exp_f32 v80,v80
v_mfma_f32_16x16x32_bf16 v[36:39],a[232:235],v[48:51],v[36:39]
s_mov_b64 s[48:49],s[50:51]
ds_read_b128 v[44:47],v2 offset:53248
v_fma_f32 v80,v80,v95,v95
v_mfma_f32_16x16x32_bf16 v[40:43],a[236:239],v[48:51],v[40:43]
s_mov_b64 s[50:51],s[52:53]
v_rcp_f32 v80,v80
v_mfma_f32_16x16x32_bf16 v[104:107],v[100:103],v[48:51],v[104:107]
s_mov_b32 s52,s74
v_fma_f32 v81,v94,v81,v93
v_mul_f32 v81,v33,v81
v_mfma_f32_16x16x32_bf16 v[108:111],v[48:51],v[48:51],v[108:111]
s_cmp_lt_u32 s67,4194304
v_exp_f32 v81,v81
v_mfma_f32_16x16x32_bf16 v[36:39],a[240:243],v[52:55],v[36:39]
ds_read_b128 v[48:51],v2 offset:54272
v_fma_f32 v81,v81,v95,v95
v_mfma_f32_16x16x32_bf16 v[40:43],a[244:247],v[52:55],v[40:43]
v_rcp_f32 v81,v81
v_mfma_f32_16x16x32_bf16 v[104:107],v[100:103],v[52:55],v[104:107]
v_fma_f32 v82,v94,v82,v93
v_mul_f32 v82,v34,v82
v_mfma_f32_16x16x32_bf16 v[108:111],v[52:55],v[52:55],v[108:111]
v_exp_f32 v82,v82
v_mfma_f32_16x16x32_bf16 v[36:39],a[248:251],v[56:59],v[36:39]
ds_read_b128 v[52:55],v2 offset:55296
v_fma_f32 v82,v82,v95,v95
v_mfma_f32_16x16x32_bf16 v[40:43],a[252:255],v[56:59],v[40:43]
v_rcp_f32 v82,v82
v_mfma_f32_16x16x32_bf16 v[104:107],v[100:103],v[56:59],v[104:107]
v_fma_f32 v83,v94,v83,v93
v_mul_f32 v83,v35,v83
v_mfma_f32_16x16x32_bf16 v[108:111],v[56:59],v[56:59],v[108:111]
s_waitcnt lgkmcnt(3)
v_exp_f32 v83,v83
v_mfma_f32_16x16x32_bf16 v[36:39],a[192:195],v[60:63],v[36:39]
ds_read_b128 v[56:59],v2 offset:56320
v_fma_f32 v83,v83,v95,v95
v_mfma_f32_16x16x32_bf16 v[40:43],a[196:199],v[60:63],v[40:43]
v_rcp_f32 v83,v83
v_mfma_f32_16x16x32_bf16 v[36:39],a[200:203],v[64:67],v[36:39]
v_cndmask_b32 v112,v108,v109,s[82:83]
v_cndmask_b32 v113,v110,v111,s[82:83]
v_cndmask_b32 v112,v112,v113,s[84:85]
v_cndmask_b32 v112,v104,v112,s[98:99]
v_mfma_f32_16x16x32_bf16 v[40:43],a[204:207],v[64:67],v[40:43]
ds_read_b128 v[60:63],v2 offset:49152
ds_write_b32 v13,v112 offset:2048
buffer_load_dwordx4 v0,s[36:39],s66 offen offset:0 sc1 lds
v_mfma_f32_16x16x32_bf16 v[36:39],a[208:211],v[68:71],v[36:39]
ds_read_b128 v[64:67],v2 offset:50176
v_mul_f32 v32,v32,v80
v_mfma_f32_16x16x32_bf16 v[40:43],a[212:215],v[68:71],v[40:43]
v_mul_f32 v33,v33,v81
v_mul_f32 v34,v34,v82
v_mfma_f32_16x16x32_bf16 v[36:39],a[216:219],v[72:75],v[36:39]
ds_read_b128 v[68:71],v2 offset:51200
v_mul_f32 v35,v35,v83
v_mfma_f32_16x16x32_bf16 v[40:43],a[220:223],v[72:75],v[40:43]
s_waitcnt lgkmcnt(4)
buffer_load_dwordx4 v0,s[36:39],s66 offen offset:1024 sc1 lds
v_mfma_f32_16x16x32_bf16 v[36:39],a[160:163],v[44:47],v[36:39]
s_waitcnt vmcnt(7) lgkmcnt(2)
ds_read_b128 v[72:75],v2 offset:52224
v_xor_b32 v2,65536,v2
v_mfma_f32_16x16x32_bf16 v[40:43],a[164:167],v[44:47],v[40:43]
s_barrier
v_mfma_f32_16x16x32_bf16 v[36:39],a[168:171],v[48:51],v[36:39]
buffer_store_dword v5,v4,s[36:39],s70 offen offset:0 sc0 sc1
ds_read_b128 v[116:119],v14 offset:2048
ds_read_b128 v[120:123],v14 offset:2064
ds_read_b128 v[44:47],v2 offset:12288
v_mfma_f32_16x16x32_bf16 v[40:43],a[172:175],v[48:51],v[40:43]
v_cvt_pk_bf16_f32 v86,v32,v33
v_cvt_pk_bf16_f32 v87,v34,v35
v_mfma_f32_16x16x32_bf16 v[36:39],a[176:179],v[52:55],v[36:39]
ds_read_b128 v[48:51],v2 offset:13312
v_xor_b32 v8,16384,v8
v_mfma_f32_16x16x32_bf16 v[40:43],a[180:183],v[52:55],v[40:43]
buffer_load_dwordx4 v0,s[36:39],s66 offen offset:2048 sc1 lds
v_mfma_f32_16x16x32_bf16 v[36:39],a[184:187],v[56:59],v[36:39]
s_waitcnt lgkmcnt(2)
ds_read_b128 v[52:55],v2 offset:14336
v_add_f32 v116,v116,v117
v_mfma_f32_16x16x32_bf16 v[40:43],a[188:191],v[56:59],v[40:43]
v_add_f32 v118,v118,v119
v_add_f32 v116,v116,v118
v_mfma_f32_16x16x32_bf16 v[36:39],a[128:131],v[60:63],v[36:39]
ds_read_b128 v[56:59],v2 offset:15360
v_mul_f32 v96,s75,v116
v_mfma_f32_16x16x32_bf16 v[40:43],a[132:135],v[60:63],v[40:43]
buffer_load_dwordx4 v0,s[36:39],s66 offen offset:3072 sc1 lds
v_mfma_f32_16x16x32_bf16 v[36:39],a[136:139],v[64:67],v[36:39]
ds_read_b128 v[60:63],v2 offset:8192
v_add_f32 v120,v120,v121
v_mfma_f32_16x16x32_bf16 v[40:43],a[140:143],v[64:67],v[40:43]
v_add_f32 v122,v122,v123
v_add_f32 v120,v120,v122
v_mfma_f32_16x16x32_bf16 v[36:39],a[144:147],v[68:71],v[36:39]
ds_read_b128 v[64:67],v2 offset:9216
v_mul_f32 v120,s75,v120
v_mfma_f32_16x16x32_bf16 v[40:43],a[148:151],v[68:71],v[40:43]
buffer_store_dwordx4 v[84:87],v3,s[36:39],s73 offen offset:0 sc0 sc1
v_fma_f32 v120,-v96,v96,v120
v_max_f32 v120,0,v120
v_add_f32 v115,v15,v120
v_fma_f32 v120,v15,v115,v120
v_rsq_f32 v116,v120
v_mfma_f32_16x16x32_bf16 v[36:39],a[152:155],v[72:75],v[36:39]
s_waitcnt lgkmcnt(2)
ds_read_b128 v[68:71],v2 offset:10240
v_mul_f32 v97,s76,v116
v_mul_f32 v98,v116,v116
v_mul_f32 v99,v120,v116
v_mul_f32 v98,s77,v98
v_mul_f32 v98,v98,v97
v_mfma_f32_16x16x32_bf16 v[40:43],a[156:159],v[72:75],v[40:43]
s_cbranch_scc1 Lg1_a0w3_%=
s_cmp_ge_u32 s48,4194304
s_cselect_b64 s[90:91],s[94:95],s[90:91]
s_cselect_b64 s[92:93],s[96:97],s[92:93]
s_cmp_lt_u32 s67,s59
s_cbranch_scc1 Lg1_d0w3_%=
s_branch Lg1_slow0_%=
Lg1_d0w0_%=:
v_mfma_f32_16x16x32_bf16 v[28:31],a[0:3],v[44:47],0
s_add_u32 m0,s62,16384
ds_read_b128 v[72:75],v2 offset:7168
s_lshr_b32 s73,s47,0
v_mfma_f32_16x16x32_bf16 v[32:35],a[4:7],v[44:47],0
buffer_load_dwordx4 v1,s[36:39],s66 offen offset:0 sc1 lds
v_mfma_f32_16x16x32_bf16 v[28:31],a[8:11],v[48:51],v[28:31]
s_lshr_b32 s72,s66,6
ds_read_b128 v[44:47],v2 offset:8192
s_lshr_b32 s71,s62,2
v_mfma_f32_16x16x32_bf16 v[32:35],a[12:15],v[48:51],v[32:35]
s_cmp_lt_u32 s66,4194304
v_fma_f32 v36,-v96,v20,v36
v_fma_f32 v37,-v96,v21,v37
v_mfma_f32_16x16x32_bf16 v[28:31],a[16:19],v[52:55],v[28:31]
s_cselect_b32 s72,s59,s72
ds_read_b128 v[48:51],v2 offset:9216
v_fma_f32 v38,-v96,v22,v38
v_mfma_f32_16x16x32_bf16 v[32:35],a[20:23],v[52:55],v[32:35]
buffer_load_dwordx4 v1,s[36:39],s66 offen offset:1024 sc1 lds
v_mfma_f32_16x16x32_bf16 v[28:31],a[24:27],v[56:59],v[28:31]
ds_read_b128 v[52:55],v2 offset:10240
v_fma_f32 v39,-v96,v23,v39
v_mfma_f32_16x16x32_bf16 v[32:35],a[28:31],v[56:59],v[32:35]
s_waitcnt lgkmcnt(3)
v_mul_f32 v76,v36,v36
v_mul_f32 v77,v37,v37
v_mfma_f32_16x16x32_bf16 v[28:31],a[32:35],v[60:63],v[28:31]
ds_read_b128 v[56:59],v2 offset:11264
v_mul_f32 v78,v38,v38
v_mfma_f32_16x16x32_bf16 v[32:35],a[36:39],v[60:63],v[32:35]
buffer_load_dwordx4 v1,s[36:39],s66 offen offset:2048 sc1 lds
v_mfma_f32_16x16x32_bf16 v[28:31],a[40:43],v[64:67],v[28:31]
ds_read_b128 v[60:63],v2 offset:12288
v_mul_f32 v79,v39,v39
v_mfma_f32_16x16x32_bf16 v[32:35],a[44:47],v[64:67],v[32:35]
v_fma_f32 v76,v98,v76,v97
v_mul_f32 v76,v36,v76
v_mfma_f32_16x16x32_bf16 v[28:31],a[48:51],v[68:71],v[28:31]
ds_read_b128 v[64:67],v2 offset:13312
v_fma_f32 v77,v98,v77,v97
v_mfma_f32_16x16x32_bf16 v[32:35],a[52:55],v[68:71],v[32:35]
buffer_load_dwordx4 v1,s[36:39],s66 offen offset:3072 sc1 lds
v_mfma_f32_16x16x32_bf16 v[28:31],a[56:59],v[72:75],v[28:31]
s_add_u32 m0,s71,s88
ds_read_b128 v[68:71],v2 offset:14336
v_mul_f32 v77,v37,v77
v_mfma_f32_16x16x32_bf16 v[32:35],a[60:63],v[72:75],v[32:35]
s_waitcnt lgkmcnt(3)
buffer_load_dword v10,s[36:39],s72 offen offset:0 sc1 lds)"
R"(
v_mfma_f32_16x16x32_bf16 v[28:31],a[64:67],v[44:47],v[28:31]
s_waitcnt vmcnt(11)
ds_read_b128 v[72:75],v2 offset:15360
v_fma_f32 v78,v98,v78,v97
v_mfma_f32_16x16x32_bf16 v[32:35],a[68:71],v[44:47],v[32:35]
s_barrier
v_mfma_f32_16x16x32_bf16 v[28:31],a[72:75],v[48:51],v[28:31]
s_xor_b32 s87,s64,65536
ds_read_b128 v[44:47],v2 offset:16384
v_fma_f32 v79,v98,v79,v97
v_mfma_f32_16x16x32_bf16 v[32:35],a[76:79],v[48:51],v[32:35]
s_cmp_ge_u32 s66,s59
v_exp_f32 v76,v76
v_mfma_f32_16x16x32_bf16 v[28:31],a[80:83],v[52:55],v[28:31]
s_cselect_b32 s86,s49,s50
ds_read_b128 v[48:51],v2 offset:17408
v_mul_f32 v78,v38,v78
v_mfma_f32_16x16x32_bf16 v[32:35],a[84:87],v[52:55],v[32:35]
s_cselect_b32 s87,s87,s63
v_exp_f32 v77,v77
v_mfma_f32_16x16x32_bf16 v[28:31],a[88:91],v[56:59],v[28:31]
s_lshr_b32 s69,s50,5
ds_read_b128 v[52:55],v2 offset:18432
v_mul_f32 v79,v39,v79
v_mfma_f32_16x16x32_bf16 v[32:35],a[92:95],v[56:59],v[32:35]
s_waitcnt lgkmcnt(3)
v_exp_f32 v78,v78
v_mfma_f32_16x16x32_bf16 v[28:31],a[96:99],v[60:63],v[28:31]
s_cmp_lt_u32 s50,4194304
ds_read_b128 v[56:59],v2 offset:19456
v_fma_f32 v76,v76,v99,v99
v_mfma_f32_16x16x32_bf16 v[32:35],a[100:103],v[60:63],v[32:35]
s_cselect_b32 s68,0xffffcfff,-1
v_exp_f32 v79,v79
v_mfma_f32_16x16x32_bf16 v[28:31],a[104:107],v[64:67],v[28:31]
s_and_b32 s68,s68,s69
ds_read_b128 v[60:63],v2 offset:20480
v_fma_f32 v77,v77,v99,v99
v_mfma_f32_16x16x32_bf16 v[32:35],a[108:111],v[64:67],v[32:35]
s_lshr_b32 s69,s51,5
v_rcp_f32 v76,v76
v_mfma_f32_16x16x32_bf16 v[28:31],a[112:115],v[68:71],v[28:31]
s_cmp_lt_u32 s51,4194304
ds_read_b128 v[64:67],v2 offset:21504
v_fma_f32 v78,v78,v99,v99
v_mfma_f32_16x16x32_bf16 v[32:35],a[116:119],v[68:71],v[32:35]
s_cselect_b32 s70,0xffffcfff,-1
v_rcp_f32 v77,v77
v_mfma_f32_16x16x32_bf16 v[28:31],a[120:123],v[72:75],v[28:31]
s_and_b32 s70,s70,s69
ds_read_b128 v[68:71],v2 offset:22528
v_fma_f32 v79,v79,v99,v99
v_mfma_f32_16x16x32_bf16 v[32:35],a[124:127],v[72:75],v[32:35]
s_waitcnt lgkmcnt(3)
v_rcp_f32 v78,v78
v_mfma_f32_16x16x32_bf16 v[28:31],a[128:131],v[44:47],v[28:31]
s_waitcnt vmcnt(10)
ds_read_b128 v[72:75],v2 offset:23552
v_mul_f32 v36,v36,v76
v_mfma_f32_16x16x32_bf16 v[32:35],a[132:135],v[44:47],v[32:35]
s_barrier
v_mfma_f32_16x16x32_bf16 v[28:31],a[136:139],v[48:51],v[28:31]
s_mov_b32 s62,s87
ds_read_b128 v[44:47],v2 offset:24576
ds_read_b32 v16,v7 offset:0
v_mfma_f32_16x16x32_bf16 v[32:35],a[140:143],v[48:51],v[32:35]
v_rcp_f32 v79,v79
v_mfma_f32_16x16x32_bf16 v[28:31],a[144:147],v[52:55],v[28:31]
ds_read_b128 v[48:51],v2 offset:25600
v_mul_f32 v37,v37,v77
v_mfma_f32_16x16x32_bf16 v[32:35],a[148:151],v[52:55],v[32:35]
v_mul_f32 v38,v38,v78
v_mul_f32 v39,v39,v79
v_mfma_f32_16x16x32_bf16 v[28:31],a[152:155],v[56:59],v[28:31]
s_waitcnt lgkmcnt(1)
ds_read_b128 v[52:55],v2 offset:26624
v_cvt_pk_bf16_f32 v84,v36,v37
v_mfma_f32_16x16x32_bf16 v[32:35],a[156:159],v[56:59],v[32:35]
v_cmp_eq_u32 vcc,s60,v16
v_cvt_pk_bf16_f32 v85,v38,v39
v_fma_f32 v40,-v96,v24,v40
v_fma_f32 v41,-v96,v25,v41
v_mfma_f32_16x16x32_bf16 v[28:31],a[160:163],v[60:63],v[28:31]
s_cmp_eq_u32 vcc_lo,-1
ds_read_b128 v[56:59],v2 offset:27648
v_fma_f32 v42,-v96,v26,v42
v_mfma_f32_16x16x32_bf16 v[32:35],a[164:167],v[60:63],v[32:35]
s_cselect_b32 s67,s86,s59
s_cmp_ge_u32 s67,s59
s_cselect_b32 s68,s68,s70
s_add_u32 m0,s78,256
v_mfma_f32_16x16x32_bf16 v[28:31],a[168:171],v[64:67],v[28:31]
buffer_load_dword v6,s[36:39],s68 offen offset:0 sc1 lds
v_mfma_f32_16x16x32_bf16 v[32:35],a[172:175],v[64:67],v[32:35]
s_mov_b32 m0,s62
ds_read_b128 v[60:63],v2 offset:28672
v_fma_f32 v43,-v96,v27,v43
v_mfma_f32_16x16x32_bf16 v[28:31],a[176:179],v[68:71],v[28:31]
buffer_load_dwordx4 v0,s[36:39],s67 offen offset:0 sc1 lds
v_mfma_f32_16x16x32_bf16 v[32:35],a[180:183],v[68:71],v[32:35]
s_xor_b32 s63,s63,65536
ds_read_b128 v[64:67],v2 offset:29696
v_mul_f32 v80,v40,v40
v_mfma_f32_16x16x32_bf16 v[28:31],a[184:187],v[72:75],v[28:31]
s_add_u32 s53,s52,s55
ds_read_b128 v[68:71],v2 offset:30720
v_mul_f32 v81,v41,v41
v_mfma_f32_16x16x32_bf16 v[32:35],a[188:191],v[72:75],v[32:35]
s_waitcnt lgkmcnt(3)
v_mul_f32 v82,v42,v42
v_mul_f32 v83,v43,v43
v_mfma_f32_16x16x32_bf16 v[28:31],a[192:195],v[44:47],v[28:31]
s_waitcnt vmcnt(7)
ds_read_b128 v[72:75],v2 offset:31744
v_fma_f32 v80,v98,v80,v97
v_mfma_f32_16x16x32_bf16 v[32:35],a[196:199],v[44:47],v[32:35]
s_barrier
v_mfma_f32_16x16x32_bf16 v[28:31],a[200:203],v[48:51],v[28:31]
buffer_load_dwordx4 v0,s[36:39],s67 offen offset:1024 sc1 lds
v_mfma_f32_16x16x32_bf16 v[32:35],a[204:207],v[48:51],v[32:35]
s_cmp_eq_u32 s53,s56
ds_read_b128 v[44:47],v2 offset:32768
ds_read_b128 v[92:95],v8 offset:0
v_mfma_f32_16x16x32_bf16 v[28:31],a[208:211],v[52:55],v[28:31]
s_cselect_b32 s53,s57,s53
ds_read_b128 v[48:51],v2 offset:33792
v_mul_f32 v80,v40,v80
v_mfma_f32_16x16x32_bf16 v[32:35],a[212:215],v[52:55],v[32:35]
s_cmp_eq_u32 s53,s58
v_fma_f32 v81,v98,v81,v97
v_fma_f32 v82,v98,v82,v97
v_fma_f32 v83,v98,v83,v97
v_mul_f32 v81,v41,v81
v_mul_f32 v82,v42,v82
v_mul_f32 v83,v43,v83
v_exp_f32 v80,v80
v_exp_f32 v81,v81
v_exp_f32 v82,v82
v_exp_f32 v83,v83
v_mfma_f32_16x16x32_bf16 v[28:31],a[216:219],v[56:59],v[28:31]
buffer_load_dwordx4 v0,s[36:39],s67 offen offset:2048 sc1 lds
v_fma_f32 v80,v80,v99,v99
v_fma_f32 v81,v81,v99,v99
v_fma_f32 v82,v82,v99,v99
v_fma_f32 v83,v83,v99,v99
v_rcp_f32 v80,v80
v_rcp_f32 v81,v81
v_rcp_f32 v82,v82
v_rcp_f32 v83,v83
v_mfma_f32_16x16x32_bf16 v[32:35],a[220:223],v[56:59],v[32:35]
s_waitcnt lgkmcnt(3)
ds_read_b128 v[52:55],v2 offset:34816
v_mul_f32 v40,v40,v80
v_mul_f32 v41,v41,v81
v_mul_f32 v42,v42,v82
v_mul_f32 v43,v43,v83
v_cvt_pk_bf16_f32 v86,v40,v41
v_cvt_pk_bf16_f32 v87,v42,v43
v_mfma_f32_16x16x32_bf16 v[28:31],a[224:227],v[60:63],v[28:31]
buffer_store_dwordx4 v[84:87],v3,s[36:39],s73 offen offset:0 sc0 sc1
ds_read_b128 v[56:59],v2 offset:35840
v_mfma_f32_16x16x32_bf16 v[32:35],a[228:231],v[60:63],v[32:35]
s_waitcnt lgkmcnt(3)
s_cselect_b32 s53,s59,s53
s_cmp_ge_u32 s66,s59
v_mfma_f32_16x16x32_bf16 v[28:31],a[232:235],v[64:67],v[28:31]
buffer_load_dwordx4 v0,s[36:39],s67 offen offset:3072 sc1 lds
v_mfma_f32_16x16x32_bf16 v[32:35],a[236:239],v[64:67],v[32:35]
ds_read_b128 v[60:63],v2 offset:36864
v_mfma_f32_16x16x32_bf16 v[28:31],a[240:243],v[68:71],v[28:31]
ds_read_b128 v[64:67],v2 offset:37888
v_mfma_f32_16x16x32_bf16 v[32:35],a[244:247],v[68:71],v[32:35]
v_mfma_f32_16x16x32_bf16 v[28:31],a[248:251],v[72:75],v[28:31]
s_waitcnt lgkmcnt(2)
ds_read_b128 v[68:71],v2 offset:38912
v_mfma_f32_16x16x32_bf16 v[32:35],a[252:255],v[72:75],v[32:35]
s_cbranch_scc1 Lg1_slow1_%=
Lg1_d1w0_%=:
v_mfma_f32_16x16x32_bf16 v[36:39],a[0:3],v[44:47],0
s_add_u32 m0,s62,16384
ds_read_b128 v[72:75],v2 offset:39936
s_lshr_b32 s73,s48,0
v_mfma_f32_16x16x32_bf16 v[40:43],a[4:7],v[44:47],0
buffer_load_dwordx4 v1,s[36:39],s67 offen offset:0 sc1 lds
v_mfma_f32_16x16x32_bf16 v[36:39],a[8:11],v[48:51],v[36:39]
s_lshr_b32 s72,s67,6
ds_read_b128 v[44:47],v2 offset:40960
s_lshr_b32 s71,s62,2
v_mfma_f32_16x16x32_bf16 v[40:43],a[12:15],v[48:51],v[40:43]
s_cmp_lt_u32 s67,4194304
v_fma_f32 v28,-v92,v20,v28
v_fma_f32 v29,-v92,v21,v29
v_mfma_f32_16x16x32_bf16 v[36:39],a[16:19],v[52:55],v[36:39]
s_cselect_b32 s72,s59,s72
ds_read_b128 v[48:51],v2 offset:41984
v_fma_f32 v30,-v92,v22,v30
v_mfma_f32_16x16x32_bf16 v[40:43],a[20:23],v[52:55],v[40:43]
buffer_load_dwordx4 v1,s[36:39],s67 offen offset:1024 sc1 lds
v_mfma_f32_16x16x32_bf16 v[36:39],a[24:27],v[56:59],v[36:39]
s_mov_b64 s[44:45],s[46:47])"
R"(
ds_read_b128 v[52:55],v2 offset:43008
v_fma_f32 v31,-v92,v23,v31
v_mfma_f32_16x16x32_bf16 v[40:43],a[28:31],v[56:59],v[40:43]
s_waitcnt lgkmcnt(3)
v_mul_f32 v76,v28,v28
v_mul_f32 v77,v29,v29
v_mfma_f32_16x16x32_bf16 v[36:39],a[32:35],v[60:63],v[36:39]
s_mov_b64 s[46:47],s[48:49]
ds_read_b128 v[56:59],v2 offset:44032
v_mul_f32 v78,v30,v30
v_mfma_f32_16x16x32_bf16 v[40:43],a[36:39],v[60:63],v[40:43]
buffer_load_dwordx4 v1,s[36:39],s67 offen offset:2048 sc1 lds
v_mfma_f32_16x16x32_bf16 v[36:39],a[40:43],v[64:67],v[36:39]
s_mov_b64 s[48:49],s[50:51]
ds_read_b128 v[60:63],v2 offset:45056
v_mul_f32 v79,v31,v31
v_mfma_f32_16x16x32_bf16 v[40:43],a[44:47],v[64:67],v[40:43]
v_fma_f32 v76,v94,v76,v93
v_mul_f32 v76,v28,v76
v_mfma_f32_16x16x32_bf16 v[36:39],a[48:51],v[68:71],v[36:39]
ds_read_b128 v[64:67],v2 offset:46080
v_fma_f32 v77,v94,v77,v93
v_mfma_f32_16x16x32_bf16 v[40:43],a[52:55],v[68:71],v[40:43]
buffer_load_dwordx4 v1,s[36:39],s67 offen offset:3072 sc1 lds
v_mfma_f32_16x16x32_bf16 v[36:39],a[56:59],v[72:75],v[36:39]
s_add_u32 m0,s71,s88
ds_read_b128 v[68:71],v2 offset:47104
v_mul_f32 v77,v29,v77
v_mfma_f32_16x16x32_bf16 v[40:43],a[60:63],v[72:75],v[40:43]
s_waitcnt lgkmcnt(3)
buffer_load_dword v10,s[36:39],s72 offen offset:0 sc1 lds
v_mfma_f32_16x16x32_bf16 v[36:39],a[64:67],v[44:47],v[36:39]
s_waitcnt vmcnt(11)
ds_read_b128 v[72:75],v2 offset:48128
v_fma_f32 v78,v94,v78,v93
v_mfma_f32_16x16x32_bf16 v[40:43],a[68:71],v[44:47],v[40:43]
s_barrier
v_mfma_f32_16x16x32_bf16 v[36:39],a[72:75],v[48:51],v[36:39]
s_xor_b32 s87,s63,65536
ds_read_b128 v[44:47],v2 offset:49152
v_fma_f32 v79,v94,v79,v93
v_mfma_f32_16x16x32_bf16 v[40:43],a[76:79],v[48:51],v[40:43]
s_cmp_ge_u32 s67,s59
v_exp_f32 v76,v76
v_mfma_f32_16x16x32_bf16 v[36:39],a[80:83],v[52:55],v[36:39]
s_cselect_b32 s86,s50,s51
ds_read_b128 v[48:51],v2 offset:50176
v_mul_f32 v78,v30,v78
v_mfma_f32_16x16x32_bf16 v[40:43],a[84:87],v[52:55],v[40:43]
s_cselect_b32 s87,s87,s64
v_exp_f32 v77,v77
v_mfma_f32_16x16x32_bf16 v[36:39],a[88:91],v[56:59],v[36:39]
s_lshr_b32 s69,s51,5
ds_read_b128 v[52:55],v2 offset:51200
v_mul_f32 v79,v31,v79
v_mfma_f32_16x16x32_bf16 v[40:43],a[92:95],v[56:59],v[40:43]
s_waitcnt lgkmcnt(3)
v_exp_f32 v78,v78
v_mfma_f32_16x16x32_bf16 v[36:39],a[96:99],v[60:63],v[36:39]
s_cmp_lt_u32 s51,4194304
ds_read_b128 v[56:59],v2 offset:52224
v_fma_f32 v76,v76,v95,v95
v_mfma_f32_16x16x32_bf16 v[40:43],a[100:103],v[60:63],v[40:43]
s_cselect_b32 s68,0xffffcfff,-1
v_exp_f32 v79,v79
v_mfma_f32_16x16x32_bf16 v[36:39],a[104:107],v[64:67],v[36:39]
s_and_b32 s68,s68,s69
ds_read_b128 v[60:63],v2 offset:53248
v_fma_f32 v77,v77,v95,v95
v_mfma_f32_16x16x32_bf16 v[40:43],a[108:111],v[64:67],v[40:43]
s_lshr_b32 s69,s52,5
v_rcp_f32 v76,v76
v_mfma_f32_16x16x32_bf16 v[36:39],a[112:115],v[68:71],v[36:39]
s_cmp_lt_u32 s52,4194304
ds_read_b128 v[64:67],v2 offset:54272
v_fma_f32 v78,v78,v95,v95
v_mfma_f32_16x16x32_bf16 v[40:43],a[116:119],v[68:71],v[40:43]
s_cselect_b32 s70,0xffffcfff,-1
v_rcp_f32 v77,v77
v_mfma_f32_16x16x32_bf16 v[36:39],a[120:123],v[72:75],v[36:39]
s_and_b32 s70,s70,s69
ds_read_b128 v[68:71],v2 offset:55296
v_fma_f32 v79,v79,v95,v95
v_mfma_f32_16x16x32_bf16 v[40:43],a[124:127],v[72:75],v[40:43]
s_waitcnt lgkmcnt(3)
v_rcp_f32 v78,v78
v_mfma_f32_16x16x32_bf16 v[36:39],a[128:131],v[44:47],v[36:39]
s_waitcnt vmcnt(10)
ds_read_b128 v[72:75],v2 offset:56320
v_mul_f32 v28,v28,v76
v_mfma_f32_16x16x32_bf16 v[40:43],a[132:135],v[44:47],v[40:43]
s_barrier
v_mfma_f32_16x16x32_bf16 v[36:39],a[136:139],v[48:51],v[36:39]
s_mov_b32 s62,s87
ds_read_b128 v[44:47],v2 offset:57344
ds_read_b32 v16,v7 offset:256
v_mfma_f32_16x16x32_bf16 v[40:43],a[140:143],v[48:51],v[40:43]
s_mov_b64 s[50:51],s[52:53]
v_rcp_f32 v79,v79
v_mfma_f32_16x16x32_bf16 v[36:39],a[144:147],v[52:55],v[36:39]
ds_read_b128 v[48:51],v2 offset:58368
v_mul_f32 v29,v29,v77
v_mfma_f32_16x16x32_bf16 v[40:43],a[148:151],v[52:55],v[40:43]
v_mul_f32 v30,v30,v78
v_mul_f32 v31,v31,v79
v_mfma_f32_16x16x32_bf16 v[36:39],a[152:155],v[56:59],v[36:39]
s_waitcnt lgkmcnt(1)
ds_read_b128 v[52:55],v2 offset:59392
v_cvt_pk_bf16_f32 v84,v28,v29
v_mfma_f32_16x16x32_bf16 v[40:43],a[156:159],v[56:59],v[40:43]
v_cmp_eq_u32 vcc,s60,v16
v_cvt_pk_bf16_f32 v85,v30,v31
v_fma_f32 v32,-v92,v24,v32
v_fma_f32 v33,-v92,v25,v33
v_mfma_f32_16x16x32_bf16 v[36:39],a[160:163],v[60:63],v[36:39]
s_cmp_eq_u32 vcc_lo,-1
ds_read_b128 v[56:59],v2 offset:60416
v_fma_f32 v34,-v92,v26,v34
v_mfma_f32_16x16x32_bf16 v[40:43],a[164:167],v[60:63],v[40:43]
s_cselect_b32 s66,s86,s59
s_cmp_ge_u32 s66,s59
s_cselect_b32 s68,s68,s70
s_add_u32 m0,s78,0
v_mfma_f32_16x16x32_bf16 v[36:39],a[168:171],v[64:67],v[36:39]
buffer_load_dword v6,s[36:39],s68 offen offset:0 sc1 lds
v_mfma_f32_16x16x32_bf16 v[40:43],a[172:175],v[64:67],v[40:43]
s_mov_b32 m0,s62
ds_read_b128 v[60:63],v2 offset:61440
v_fma_f32 v35,-v92,v27,v35
v_mfma_f32_16x16x32_bf16 v[36:39],a[176:179],v[68:71],v[36:39]
buffer_load_dwordx4 v0,s[36:39],s66 offen offset:0 sc1 lds
v_mfma_f32_16x16x32_bf16 v[40:43],a[180:183],v[68:71],v[40:43]
s_xor_b32 s64,s64,65536
ds_read_b128 v[64:67],v2 offset:62464
v_mul_f32 v80,v32,v32
v_mfma_f32_16x16x32_bf16 v[36:39],a[184:187],v[72:75],v[36:39]
s_add_u32 s74,s53,s55
ds_read_b128 v[68:71],v2 offset:63488
v_mul_f32 v81,v33,v33
v_mfma_f32_16x16x32_bf16 v[40:43],a[188:191],v[72:75],v[40:43]
s_waitcnt lgkmcnt(3)
v_mul_f32 v82,v34,v34
v_mul_f32 v83,v35,v35
v_mfma_f32_16x16x32_bf16 v[36:39],a[192:195],v[44:47],v[36:39]
s_waitcnt vmcnt(7)
ds_read_b128 v[72:75],v2 offset:64512
v_xor_b32 v2,65536,v2
v_mfma_f32_16x16x32_bf16 v[40:43],a[196:199],v[44:47],v[40:43]
s_barrier
v_mfma_f32_16x16x32_bf16 v[36:39],a[200:203],v[48:51],v[36:39]
buffer_load_dwordx4 v0,s[36:39],s66 offen offset:1024 sc1 lds
v_mfma_f32_16x16x32_bf16 v[40:43],a[204:207],v[48:51],v[40:43]
s_cmp_eq_u32 s74,s56
ds_read_b128 v[44:47],v2 offset:0
ds_read_b128 v[96:99],v8 offset:8192
v_mfma_f32_16x16x32_bf16 v[36:39],a[208:211],v[52:55],v[36:39]
s_cselect_b32 s74,s57,s74
ds_read_b128 v[48:51],v2 offset:1024
v_fma_f32 v80,v94,v80,v93
v_mfma_f32_16x16x32_bf16 v[40:43],a[212:215],v[52:55],v[40:43]
s_cmp_eq_u32 s74,s58
v_fma_f32 v81,v94,v81,v93
v_fma_f32 v82,v94,v82,v93
v_fma_f32 v83,v94,v83,v93
v_mul_f32 v80,v32,v80
v_mul_f32 v81,v33,v81
v_mul_f32 v82,v34,v82
v_mul_f32 v83,v35,v83
v_exp_f32 v80,v80
v_exp_f32 v81,v81
v_exp_f32 v82,v82
v_exp_f32 v83,v83
v_mfma_f32_16x16x32_bf16 v[36:39],a[216:219],v[56:59],v[36:39]
buffer_load_dwordx4 v0,s[36:39],s66 offen offset:2048 sc1 lds
v_fma_f32 v80,v80,v95,v95
v_fma_f32 v81,v81,v95,v95
v_fma_f32 v82,v82,v95,v95
v_fma_f32 v83,v83,v95,v95
v_rcp_f32 v80,v80
v_rcp_f32 v81,v81
v_rcp_f32 v82,v82
v_rcp_f32 v83,v83
v_mfma_f32_16x16x32_bf16 v[40:43],a[220:223],v[56:59],v[40:43]
s_waitcnt lgkmcnt(3)
ds_read_b128 v[52:55],v2 offset:2048
v_mul_f32 v32,v32,v80
v_mul_f32 v33,v33,v81
v_mul_f32 v34,v34,v82
v_mul_f32 v35,v35,v83
v_cvt_pk_bf16_f32 v86,v32,v33
v_cvt_pk_bf16_f32 v87,v34,v35
v_mfma_f32_16x16x32_bf16 v[36:39],a[224:227],v[60:63],v[36:39]
buffer_store_dwordx4 v[84:87],v3,s[36:39],s73 offen offset:0 sc0 sc1
ds_read_b128 v[56:59],v2 offset:3072
v_xor_b32 v8,16384,v8
v_mfma_f32_16x16x32_bf16 v[40:43],a[228:231],v[60:63],v[40:43]
s_waitcnt lgkmcnt(3)
s_cselect_b32 s74,s59,s74
s_mov_b32 s52,s74
v_mfma_f32_16x16x32_bf16 v[36:39],a[232:235],v[64:67],v[36:39])"
R"(
buffer_load_dwordx4 v0,s[36:39],s66 offen offset:3072 sc1 lds
v_mfma_f32_16x16x32_bf16 v[40:43],a[236:239],v[64:67],v[40:43]
s_cmp_lt_u32 s67,s59
ds_read_b128 v[60:63],v2 offset:4096
v_mfma_f32_16x16x32_bf16 v[36:39],a[240:243],v[68:71],v[36:39]
ds_read_b128 v[64:67],v2 offset:5120
v_mfma_f32_16x16x32_bf16 v[40:43],a[244:247],v[68:71],v[40:43]
v_mfma_f32_16x16x32_bf16 v[36:39],a[248:251],v[72:75],v[36:39]
s_waitcnt lgkmcnt(2)
ds_read_b128 v[68:71],v2 offset:6144
v_mfma_f32_16x16x32_bf16 v[40:43],a[252:255],v[72:75],v[40:43]
s_cbranch_scc1 Lg1_d0w0_%=
s_branch Lg1_slow0_%=
Lg1_d0w1_%=:
v_mfma_f32_16x16x32_bf16 v[28:31],a[32:35],v[44:47],0
s_add_u32 m0,s62,16384
ds_read_b128 v[72:75],v2 offset:3072
s_lshr_b32 s73,s47,0
v_mfma_f32_16x16x32_bf16 v[32:35],a[36:39],v[44:47],0
s_lshr_b32 s72,s66,6
s_lshr_b32 s71,s62,2
s_cmp_lt_u32 s66,4194304
v_mfma_f32_16x16x32_bf16 v[28:31],a[40:43],v[48:51],v[28:31]
buffer_load_dwordx4 v1,s[36:39],s66 offen offset:0 sc1 lds
v_mfma_f32_16x16x32_bf16 v[32:35],a[44:47],v[48:51],v[32:35]
s_cselect_b32 s72,s59,s72
ds_read_b128 v[44:47],v2 offset:12288
v_fma_f32 v36,-v96,v20,v36
v_mfma_f32_16x16x32_bf16 v[28:31],a[48:51],v[52:55],v[28:31]
ds_read_b128 v[48:51],v2 offset:13312
v_fma_f32 v37,-v96,v21,v37
v_mfma_f32_16x16x32_bf16 v[32:35],a[52:55],v[52:55],v[32:35]
v_fma_f32 v38,-v96,v22,v38
v_fma_f32 v39,-v96,v23,v39
v_mfma_f32_16x16x32_bf16 v[28:31],a[56:59],v[56:59],v[28:31]
buffer_load_dwordx4 v1,s[36:39],s66 offen offset:1024 sc1 lds
v_mfma_f32_16x16x32_bf16 v[32:35],a[60:63],v[56:59],v[32:35]
s_waitcnt lgkmcnt(2)
ds_read_b128 v[52:55],v2 offset:14336
v_mul_f32 v76,v36,v36
v_mfma_f32_16x16x32_bf16 v[28:31],a[0:3],v[60:63],v[28:31]
ds_read_b128 v[56:59],v2 offset:15360
v_mul_f32 v77,v37,v37
v_mfma_f32_16x16x32_bf16 v[32:35],a[4:7],v[60:63],v[32:35]
v_mul_f32 v78,v38,v38
v_mul_f32 v79,v39,v39
v_mfma_f32_16x16x32_bf16 v[28:31],a[8:11],v[64:67],v[28:31]
buffer_load_dwordx4 v1,s[36:39],s66 offen offset:2048 sc1 lds
v_mfma_f32_16x16x32_bf16 v[32:35],a[12:15],v[64:67],v[32:35]
ds_read_b128 v[60:63],v2 offset:8192
v_fma_f32 v76,v98,v76,v97
v_mfma_f32_16x16x32_bf16 v[28:31],a[16:19],v[68:71],v[28:31]
ds_read_b128 v[64:67],v2 offset:9216
v_mul_f32 v76,v36,v76
v_mfma_f32_16x16x32_bf16 v[32:35],a[20:23],v[68:71],v[32:35]
v_exp_f32 v76,v76
v_mfma_f32_16x16x32_bf16 v[28:31],a[24:27],v[72:75],v[28:31]
buffer_load_dwordx4 v1,s[36:39],s66 offen offset:3072 sc1 lds
v_mfma_f32_16x16x32_bf16 v[32:35],a[28:31],v[72:75],v[32:35]
s_waitcnt lgkmcnt(2)
ds_read_b128 v[68:71],v2 offset:10240
s_add_u32 m0,s71,s88
v_mfma_f32_16x16x32_bf16 v[28:31],a[96:99],v[44:47],v[28:31]
s_xor_b32 s87,s64,65536
ds_read_b128 v[72:75],v2 offset:11264
buffer_load_dword v10,s[36:39],s72 offen offset:0 sc1 lds
s_waitcnt vmcnt(10)
v_mfma_f32_16x16x32_bf16 v[32:35],a[100:103],v[44:47],v[32:35]
s_barrier
v_mfma_f32_16x16x32_bf16 v[28:31],a[104:107],v[48:51],v[28:31]
s_cmp_ge_u32 s66,s59
ds_read_b128 v[44:47],v2 offset:20480
v_fma_f32 v76,v76,v99,v99
v_mfma_f32_16x16x32_bf16 v[32:35],a[108:111],v[48:51],v[32:35]
s_cselect_b32 s86,s49,s50
v_rcp_f32 v76,v76
v_mfma_f32_16x16x32_bf16 v[28:31],a[112:115],v[52:55],v[28:31]
s_cselect_b32 s87,s87,s63
ds_read_b128 v[48:51],v2 offset:21504
v_fma_f32 v77,v98,v77,v97
v_mfma_f32_16x16x32_bf16 v[32:35],a[116:119],v[52:55],v[32:35]
s_mov_b32 s62,s87
v_mul_f32 v77,v37,v77
v_fma_f32 v78,v98,v78,v97
v_mfma_f32_16x16x32_bf16 v[28:31],a[120:123],v[56:59],v[28:31]
s_mov_b32 m0,s62
ds_read_b128 v[52:55],v2 offset:22528
v_mul_f32 v78,v38,v78
v_mfma_f32_16x16x32_bf16 v[32:35],a[124:127],v[56:59],v[32:35]
s_waitcnt lgkmcnt(3)
v_exp_f32 v77,v77
v_mfma_f32_16x16x32_bf16 v[28:31],a[64:67],v[60:63],v[28:31]
ds_read_b128 v[56:59],v2 offset:23552
v_fma_f32 v77,v77,v99,v99
v_mfma_f32_16x16x32_bf16 v[32:35],a[68:71],v[60:63],v[32:35]
v_exp_f32 v78,v78
v_mfma_f32_16x16x32_bf16 v[28:31],a[72:75],v[64:67],v[28:31]
ds_read_b128 v[60:63],v2 offset:16384
v_fma_f32 v78,v78,v99,v99
v_mfma_f32_16x16x32_bf16 v[32:35],a[76:79],v[64:67],v[32:35]
v_rcp_f32 v77,v77
v_mfma_f32_16x16x32_bf16 v[28:31],a[80:83],v[68:71],v[28:31]
ds_read_b128 v[64:67],v2 offset:17408
v_fma_f32 v79,v98,v79,v97
v_mfma_f32_16x16x32_bf16 v[32:35],a[84:87],v[68:71],v[32:35]
v_rcp_f32 v78,v78
v_mfma_f32_16x16x32_bf16 v[28:31],a[88:91],v[72:75],v[28:31]
ds_read_b128 v[68:71],v2 offset:18432
v_mul_f32 v79,v39,v79
v_mfma_f32_16x16x32_bf16 v[32:35],a[92:95],v[72:75],v[32:35]
s_waitcnt lgkmcnt(3)
v_exp_f32 v79,v79
v_mfma_f32_16x16x32_bf16 v[28:31],a[160:163],v[44:47],v[28:31]
s_nop 0
ds_read_b128 v[72:75],v2 offset:19456
v_fma_f32 v79,v79,v99,v99
v_mfma_f32_16x16x32_bf16 v[32:35],a[164:167],v[44:47],v[32:35]
s_barrier
v_mfma_f32_16x16x32_bf16 v[28:31],a[168:171],v[48:51],v[28:31]
ds_read_b128 v[44:47],v2 offset:28672
ds_read_b32 v16,v7 offset:0
v_mfma_f32_16x16x32_bf16 v[32:35],a[172:175],v[48:51],v[32:35]
v_rcp_f32 v79,v79
v_mfma_f32_16x16x32_bf16 v[28:31],a[176:179],v[52:55],v[28:31]
ds_read_b128 v[48:51],v2 offset:29696
v_mul_f32 v36,v36,v76
v_mfma_f32_16x16x32_bf16 v[32:35],a[180:183],v[52:55],v[32:35]
v_mul_f32 v37,v37,v77
v_mul_f32 v38,v38,v78
v_mfma_f32_16x16x32_bf16 v[28:31],a[184:187],v[56:59],v[28:31]
s_waitcnt lgkmcnt(1)
ds_read_b128 v[52:55],v2 offset:30720
v_mul_f32 v39,v39,v79
v_mfma_f32_16x16x32_bf16 v[32:35],a[188:191],v[56:59],v[32:35]
v_cmp_eq_u32 vcc,s60,v16
v_cvt_pk_bf16_f32 v84,v36,v37
v_cvt_pk_bf16_f32 v85,v38,v39
v_fma_f32 v40,-v96,v24,v40
v_mfma_f32_16x16x32_bf16 v[28:31],a[128:131],v[60:63],v[28:31]
s_cmp_eq_u32 vcc_lo,-1
ds_read_b128 v[56:59],v2 offset:31744
s_cselect_b32 s67,s86,s59
v_mfma_f32_16x16x32_bf16 v[32:35],a[132:135],v[60:63],v[32:35]
buffer_load_dwordx4 v0,s[36:39],s67 offen offset:0 sc1 lds
v_mfma_f32_16x16x32_bf16 v[28:31],a[136:139],v[64:67],v[28:31]
s_xor_b32 s63,s63,65536
ds_read_b128 v[60:63],v2 offset:24576
v_fma_f32 v41,-v96,v25,v41
v_mfma_f32_16x16x32_bf16 v[32:35],a[140:143],v[64:67],v[32:35]
s_add_u32 s53,s52,s55
v_fma_f32 v42,-v96,v26,v42
v_fma_f32 v43,-v96,v27,v43
v_mfma_f32_16x16x32_bf16 v[28:31],a[144:147],v[68:71],v[28:31]
s_cmp_eq_u32 s53,s56
ds_read_b128 v[64:67],v2 offset:25600
v_mul_f32 v80,v40,v40
v_mfma_f32_16x16x32_bf16 v[32:35],a[148:151],v[68:71],v[32:35]
buffer_load_dwordx4 v0,s[36:39],s67 offen offset:1024 sc1 lds
v_mfma_f32_16x16x32_bf16 v[28:31],a[152:155],v[72:75],v[28:31]
s_cselect_b32 s53,s57,s53
ds_read_b128 v[68:71],v2 offset:26624
v_mul_f32 v81,v41,v41
v_mfma_f32_16x16x32_bf16 v[32:35],a[156:159],v[72:75],v[32:35]
s_waitcnt lgkmcnt(3)
v_mul_f32 v82,v42,v42
v_mul_f32 v83,v43,v43
v_mfma_f32_16x16x32_bf16 v[28:31],a[224:227],v[44:47],v[28:31]
s_waitcnt vmcnt(7)
ds_read_b128 v[72:75],v2 offset:27648
v_fma_f32 v80,v98,v80,v97
v_mfma_f32_16x16x32_bf16 v[32:35],a[228:231],v[44:47],v[32:35]
s_barrier
v_mfma_f32_16x16x32_bf16 v[28:31],a[232:235],v[48:51],v[28:31]
s_cmp_eq_u32 s53,s58
ds_read_b128 v[44:47],v2 offset:36864
ds_read_b128 v[92:95],v8 offset:0
v_mfma_f32_16x16x32_bf16 v[32:35],a[236:239],v[48:51],v[32:35]
buffer_load_dwordx4 v0,s[36:39],s67 offen offset:2048 sc1 lds
v_mfma_f32_16x16x32_bf16 v[28:31],a[240:243],v[52:55],v[28:31]
s_cselect_b32 s53,s59,s53
ds_read_b128 v[48:51],v2 offset:37888
v_mul_f32 v80,v40,v80
v_mfma_f32_16x16x32_bf16 v[32:35],a[244:247],v[52:55],v[32:35]
s_cmp_ge_u32 s66,s59
v_exp_f32 v80,v80
v_mfma_f32_16x16x32_bf16 v[28:31],a[248:251],v[56:59],v[28:31]
v_fma_f32 v81,v98,v81,v97
v_fma_f32 v82,v98,v82,v97
v_fma_f32 v83,v98,v83,v97
v_mul_f32 v81,v41,v81
v_mul_f32 v82,v42,v82
v_mul_f32 v83,v43,v83
v_exp_f32 v81,v81
v_exp_f32 v82,v82
v_exp_f32 v83,v83
v_mfma_f32_16x16x32_bf16 v[32:35],a[252:255],v[56:59],v[32:35]
s_waitcnt lgkmcnt(3)
ds_read_b128 v[52:55],v2 offset:38912
buffer_load_dwordx4 v0,s[36:39],s67 offen offset:3072 sc1 lds
v_fma_f32 v80,v80,v99,v99)"
R"(
v_fma_f32 v81,v81,v99,v99
v_fma_f32 v82,v82,v99,v99
v_fma_f32 v83,v83,v99,v99
v_rcp_f32 v80,v80
v_rcp_f32 v81,v81
v_rcp_f32 v82,v82
v_rcp_f32 v83,v83
v_mfma_f32_16x16x32_bf16 v[28:31],a[192:195],v[60:63],v[28:31]
s_waitcnt lgkmcnt(2)
v_mul_f32 v40,v40,v80
v_mul_f32 v41,v41,v81
v_mul_f32 v42,v42,v82
v_mul_f32 v43,v43,v83
v_cvt_pk_bf16_f32 v86,v40,v41
v_cvt_pk_bf16_f32 v87,v42,v43
v_mfma_f32_16x16x32_bf16 v[32:35],a[196:199],v[60:63],v[32:35]
ds_read_b128 v[56:59],v2 offset:39936
buffer_store_dwordx4 v[84:87],v3,s[36:39],s73 offen offset:0 sc0 sc1
v_mfma_f32_16x16x32_bf16 v[28:31],a[200:203],v[64:67],v[28:31]
ds_read_b128 v[60:63],v2 offset:32768
v_mfma_f32_16x16x32_bf16 v[32:35],a[204:207],v[64:67],v[32:35]
v_mfma_f32_16x16x32_bf16 v[28:31],a[208:211],v[68:71],v[28:31]
ds_read_b128 v[64:67],v2 offset:33792
v_mfma_f32_16x16x32_bf16 v[32:35],a[212:215],v[68:71],v[32:35]
v_mfma_f32_16x16x32_bf16 v[28:31],a[216:219],v[72:75],v[28:31]
s_waitcnt lgkmcnt(2)
ds_read_b128 v[68:71],v2 offset:34816
v_mfma_f32_16x16x32_bf16 v[32:35],a[220:223],v[72:75],v[32:35]
s_cbranch_scc1 Lg1_slow1_%=
Lg1_d1w1_%=:
v_mfma_f32_16x16x32_bf16 v[36:39],a[32:35],v[44:47],0
s_add_u32 m0,s62,16384
ds_read_b128 v[72:75],v2 offset:35840
s_lshr_b32 s73,s48,0
v_mfma_f32_16x16x32_bf16 v[40:43],a[36:39],v[44:47],0
s_lshr_b32 s72,s67,6
s_lshr_b32 s71,s62,2
s_cmp_lt_u32 s67,4194304
v_mfma_f32_16x16x32_bf16 v[36:39],a[40:43],v[48:51],v[36:39]
buffer_load_dwordx4 v1,s[36:39],s67 offen offset:0 sc1 lds
v_mfma_f32_16x16x32_bf16 v[40:43],a[44:47],v[48:51],v[40:43]
s_cselect_b32 s72,s59,s72
ds_read_b128 v[44:47],v2 offset:45056
v_fma_f32 v28,-v92,v20,v28
v_mfma_f32_16x16x32_bf16 v[36:39],a[48:51],v[52:55],v[36:39]
s_mov_b64 s[44:45],s[46:47]
ds_read_b128 v[48:51],v2 offset:46080
v_fma_f32 v29,-v92,v21,v29
v_mfma_f32_16x16x32_bf16 v[40:43],a[52:55],v[52:55],v[40:43]
s_mov_b64 s[46:47],s[48:49]
v_fma_f32 v30,-v92,v22,v30
v_fma_f32 v31,-v92,v23,v31
v_mfma_f32_16x16x32_bf16 v[36:39],a[56:59],v[56:59],v[36:39]
buffer_load_dwordx4 v1,s[36:39],s67 offen offset:1024 sc1 lds
v_mfma_f32_16x16x32_bf16 v[40:43],a[60:63],v[56:59],v[40:43]
s_waitcnt lgkmcnt(2)
ds_read_b128 v[52:55],v2 offset:47104
v_mul_f32 v76,v28,v28
v_mfma_f32_16x16x32_bf16 v[36:39],a[0:3],v[60:63],v[36:39]
s_mov_b64 s[48:49],s[50:51]
ds_read_b128 v[56:59],v2 offset:48128
v_mul_f32 v77,v29,v29
v_mfma_f32_16x16x32_bf16 v[40:43],a[4:7],v[60:63],v[40:43]
v_mul_f32 v78,v30,v30
v_mul_f32 v79,v31,v31
v_mfma_f32_16x16x32_bf16 v[36:39],a[8:11],v[64:67],v[36:39]
buffer_load_dwordx4 v1,s[36:39],s67 offen offset:2048 sc1 lds
v_mfma_f32_16x16x32_bf16 v[40:43],a[12:15],v[64:67],v[40:43]
ds_read_b128 v[60:63],v2 offset:40960
v_fma_f32 v76,v94,v76,v93
v_mfma_f32_16x16x32_bf16 v[36:39],a[16:19],v[68:71],v[36:39]
ds_read_b128 v[64:67],v2 offset:41984
v_mul_f32 v76,v28,v76
v_mfma_f32_16x16x32_bf16 v[40:43],a[20:23],v[68:71],v[40:43]
v_exp_f32 v76,v76
v_mfma_f32_16x16x32_bf16 v[36:39],a[24:27],v[72:75],v[36:39]
buffer_load_dwordx4 v1,s[36:39],s67 offen offset:3072 sc1 lds
v_mfma_f32_16x16x32_bf16 v[40:43],a[28:31],v[72:75],v[40:43]
s_waitcnt lgkmcnt(2)
ds_read_b128 v[68:71],v2 offset:43008
s_add_u32 m0,s71,s88
v_mfma_f32_16x16x32_bf16 v[36:39],a[96:99],v[44:47],v[36:39]
s_xor_b32 s87,s63,65536
ds_read_b128 v[72:75],v2 offset:44032
buffer_load_dword v10,s[36:39],s72 offen offset:0 sc1 lds
s_waitcnt vmcnt(10)
v_mfma_f32_16x16x32_bf16 v[40:43],a[100:103],v[44:47],v[40:43]
s_barrier
v_mfma_f32_16x16x32_bf16 v[36:39],a[104:107],v[48:51],v[36:39]
s_cmp_ge_u32 s67,s59
ds_read_b128 v[44:47],v2 offset:53248
v_fma_f32 v76,v76,v95,v95
v_mfma_f32_16x16x32_bf16 v[40:43],a[108:111],v[48:51],v[40:43]
s_cselect_b32 s86,s50,s51
v_rcp_f32 v76,v76
v_mfma_f32_16x16x32_bf16 v[36:39],a[112:115],v[52:55],v[36:39]
s_cselect_b32 s87,s87,s64
ds_read_b128 v[48:51],v2 offset:54272
v_fma_f32 v77,v94,v77,v93
v_mfma_f32_16x16x32_bf16 v[40:43],a[116:119],v[52:55],v[40:43]
s_mov_b32 s62,s87
v_mul_f32 v77,v29,v77
v_fma_f32 v78,v94,v78,v93
v_mfma_f32_16x16x32_bf16 v[36:39],a[120:123],v[56:59],v[36:39]
s_mov_b32 m0,s62
ds_read_b128 v[52:55],v2 offset:55296
v_mul_f32 v78,v30,v78
v_mfma_f32_16x16x32_bf16 v[40:43],a[124:127],v[56:59],v[40:43]
s_waitcnt lgkmcnt(3)
v_exp_f32 v77,v77
v_mfma_f32_16x16x32_bf16 v[36:39],a[64:67],v[60:63],v[36:39]
s_mov_b64 s[50:51],s[52:53]
ds_read_b128 v[56:59],v2 offset:56320
v_fma_f32 v77,v77,v95,v95
v_mfma_f32_16x16x32_bf16 v[40:43],a[68:71],v[60:63],v[40:43]
v_exp_f32 v78,v78
v_mfma_f32_16x16x32_bf16 v[36:39],a[72:75],v[64:67],v[36:39]
ds_read_b128 v[60:63],v2 offset:49152
v_fma_f32 v78,v78,v95,v95
v_mfma_f32_16x16x32_bf16 v[40:43],a[76:79],v[64:67],v[40:43]
v_rcp_f32 v77,v77
v_mfma_f32_16x16x32_bf16 v[36:39],a[80:83],v[68:71],v[36:39]
ds_read_b128 v[64:67],v2 offset:50176
v_fma_f32 v79,v94,v79,v93
v_mfma_f32_16x16x32_bf16 v[40:43],a[84:87],v[68:71],v[40:43]
v_rcp_f32 v78,v78
v_mfma_f32_16x16x32_bf16 v[36:39],a[88:91],v[72:75],v[36:39]
ds_read_b128 v[68:71],v2 offset:51200
v_mul_f32 v79,v31,v79
v_mfma_f32_16x16x32_bf16 v[40:43],a[92:95],v[72:75],v[40:43]
s_waitcnt lgkmcnt(3)
v_exp_f32 v79,v79
v_mfma_f32_16x16x32_bf16 v[36:39],a[160:163],v[44:47],v[36:39]
s_nop 0
ds_read_b128 v[72:75],v2 offset:52224
v_fma_f32 v79,v79,v95,v95
v_mfma_f32_16x16x32_bf16 v[40:43],a[164:167],v[44:47],v[40:43]
s_barrier
v_mfma_f32_16x16x32_bf16 v[36:39],a[168:171],v[48:51],v[36:39]
ds_read_b128 v[44:47],v2 offset:61440
ds_read_b32 v16,v7 offset:256
v_mfma_f32_16x16x32_bf16 v[40:43],a[172:175],v[48:51],v[40:43]
v_rcp_f32 v79,v79
v_mfma_f32_16x16x32_bf16 v[36:39],a[176:179],v[52:55],v[36:39]
ds_read_b128 v[48:51],v2 offset:62464
v_mul_f32 v28,v28,v76
v_mfma_f32_16x16x32_bf16 v[40:43],a[180:183],v[52:55],v[40:43]
v_mul_f32 v29,v29,v77
v_mul_f32 v30,v30,v78
v_mfma_f32_16x16x32_bf16 v[36:39],a[184:187],v[56:59],v[36:39]
s_waitcnt lgkmcnt(1)
ds_read_b128 v[52:55],v2 offset:63488
v_mul_f32 v31,v31,v79
v_mfma_f32_16x16x32_bf16 v[40:43],a[188:191],v[56:59],v[40:43]
v_cmp_eq_u32 vcc,s60,v16
v_cvt_pk_bf16_f32 v84,v28,v29
v_cvt_pk_bf16_f32 v85,v30,v31
v_fma_f32 v32,-v92,v24,v32
v_mfma_f32_16x16x32_bf16 v[36:39],a[128:131],v[60:63],v[36:39]
s_cmp_eq_u32 vcc_lo,-1
ds_read_b128 v[56:59],v2 offset:64512
s_cselect_b32 s66,s86,s59
v_mfma_f32_16x16x32_bf16 v[40:43],a[132:135],v[60:63],v[40:43]
buffer_load_dwordx4 v0,s[36:39],s66 offen offset:0 sc1 lds
v_mfma_f32_16x16x32_bf16 v[36:39],a[136:139],v[64:67],v[36:39]
s_xor_b32 s64,s64,65536
ds_read_b128 v[60:63],v2 offset:57344
v_fma_f32 v33,-v92,v25,v33
v_mfma_f32_16x16x32_bf16 v[40:43],a[140:143],v[64:67],v[40:43]
s_add_u32 s74,s53,s55
v_fma_f32 v34,-v92,v26,v34
v_fma_f32 v35,-v92,v27,v35
v_mfma_f32_16x16x32_bf16 v[36:39],a[144:147],v[68:71],v[36:39]
s_cmp_eq_u32 s74,s56
ds_read_b128 v[64:67],v2 offset:58368
v_mul_f32 v80,v32,v32
v_mfma_f32_16x16x32_bf16 v[40:43],a[148:151],v[68:71],v[40:43]
buffer_load_dwordx4 v0,s[36:39],s66 offen offset:1024 sc1 lds
v_mfma_f32_16x16x32_bf16 v[36:39],a[152:155],v[72:75],v[36:39]
s_cselect_b32 s74,s57,s74
ds_read_b128 v[68:71],v2 offset:59392
v_mul_f32 v81,v33,v33
v_mfma_f32_16x16x32_bf16 v[40:43],a[156:159],v[72:75],v[40:43]
s_waitcnt lgkmcnt(3)
v_mul_f32 v82,v34,v34
v_mul_f32 v83,v35,v35
v_mfma_f32_16x16x32_bf16 v[36:39],a[224:227],v[44:47],v[36:39]
s_waitcnt vmcnt(7)
ds_read_b128 v[72:75],v2 offset:60416
v_xor_b32 v2,65536,v2
v_mfma_f32_16x16x32_bf16 v[40:43],a[228:231],v[44:47],v[40:43]
s_barrier
v_mfma_f32_16x16x32_bf16 v[36:39],a[232:235],v[48:51],v[36:39]
s_cmp_eq_u32 s74,s58
ds_read_b128 v[44:47],v2 offset:4096
ds_read_b128 v[96:99],v8 offset:8192
v_mfma_f32_16x16x32_bf16 v[40:43],a[236:239],v[48:51],v[40:43]
buffer_load_dwordx4 v0,s[36:39],s66 offen offset:2048 sc1 lds
v_mfma_f32_16x16x32_bf16 v[36:39],a[240:243],v[52:55],v[36:39]
s_cselect_b32 s74,s59,s74)"
R"(
ds_read_b128 v[48:51],v2 offset:5120
v_fma_f32 v80,v94,v80,v93
v_mfma_f32_16x16x32_bf16 v[40:43],a[244:247],v[52:55],v[40:43]
s_mov_b32 s52,s74
v_mul_f32 v80,v32,v80
v_fma_f32 v81,v94,v81,v93
v_mfma_f32_16x16x32_bf16 v[36:39],a[248:251],v[56:59],v[36:39]
s_cmp_lt_u32 s67,s59
v_fma_f32 v82,v94,v82,v93
v_fma_f32 v83,v94,v83,v93
v_mul_f32 v81,v33,v81
v_mul_f32 v82,v34,v82
v_mul_f32 v83,v35,v83
v_exp_f32 v80,v80
v_exp_f32 v81,v81
v_exp_f32 v82,v82
v_exp_f32 v83,v83
v_mfma_f32_16x16x32_bf16 v[40:43],a[252:255],v[56:59],v[40:43]
s_waitcnt lgkmcnt(3)
ds_read_b128 v[52:55],v2 offset:6144
buffer_load_dwordx4 v0,s[36:39],s66 offen offset:3072 sc1 lds
v_fma_f32 v80,v80,v95,v95
v_fma_f32 v81,v81,v95,v95
v_fma_f32 v82,v82,v95,v95
v_fma_f32 v83,v83,v95,v95
v_rcp_f32 v80,v80
v_rcp_f32 v81,v81
v_rcp_f32 v82,v82
v_rcp_f32 v83,v83
v_mfma_f32_16x16x32_bf16 v[36:39],a[192:195],v[60:63],v[36:39]
s_waitcnt lgkmcnt(2)
v_mul_f32 v32,v32,v80
v_mul_f32 v33,v33,v81
v_mul_f32 v34,v34,v82
v_mul_f32 v35,v35,v83
v_cvt_pk_bf16_f32 v86,v32,v33
v_cvt_pk_bf16_f32 v87,v34,v35
v_mfma_f32_16x16x32_bf16 v[40:43],a[196:199],v[60:63],v[40:43]
ds_read_b128 v[56:59],v2 offset:7168
buffer_store_dwordx4 v[84:87],v3,s[36:39],s73 offen offset:0 sc0 sc1
v_xor_b32 v8,16384,v8
v_mfma_f32_16x16x32_bf16 v[36:39],a[200:203],v[64:67],v[36:39]
ds_read_b128 v[60:63],v2 offset:0
v_mfma_f32_16x16x32_bf16 v[40:43],a[204:207],v[64:67],v[40:43]
v_mfma_f32_16x16x32_bf16 v[36:39],a[208:211],v[68:71],v[36:39]
ds_read_b128 v[64:67],v2 offset:1024
v_mfma_f32_16x16x32_bf16 v[40:43],a[212:215],v[68:71],v[40:43]
v_mfma_f32_16x16x32_bf16 v[36:39],a[216:219],v[72:75],v[36:39]
s_waitcnt lgkmcnt(2)
ds_read_b128 v[68:71],v2 offset:2048
v_mfma_f32_16x16x32_bf16 v[40:43],a[220:223],v[72:75],v[40:43]
s_cbranch_scc1 Lg1_d0w1_%=
s_branch Lg1_slow0_%=
Lg1_d0w2_%=:
v_mfma_f32_16x16x32_bf16 v[28:31],a[64:67],v[44:47],0
s_add_u32 m0,s62,16384
ds_read_b128 v[72:75],v2 offset:15360
s_lshr_b32 s73,s47,0
v_mfma_f32_16x16x32_bf16 v[32:35],a[68:71],v[44:47],0
s_lshr_b32 s72,s66,6
s_lshr_b32 s71,s62,2
s_cmp_lt_u32 s66,4194304
v_mfma_f32_16x16x32_bf16 v[28:31],a[72:75],v[48:51],v[28:31]
s_cselect_b32 s72,s59,s72
ds_read_b128 v[44:47],v2 offset:0
v_mfma_f32_16x16x32_bf16 v[32:35],a[76:79],v[48:51],v[32:35]
buffer_load_dwordx4 v1,s[36:39],s66 offen offset:0 sc1 lds
v_mfma_f32_16x16x32_bf16 v[28:31],a[80:83],v[52:55],v[28:31]
ds_read_b128 v[48:51],v2 offset:1024
v_fma_f32 v36,-v96,v20,v36
v_mfma_f32_16x16x32_bf16 v[32:35],a[84:87],v[52:55],v[32:35]
v_fma_f32 v37,-v96,v21,v37
v_fma_f32 v38,-v96,v22,v38
v_mfma_f32_16x16x32_bf16 v[28:31],a[88:91],v[56:59],v[28:31]
ds_read_b128 v[52:55],v2 offset:2048
v_fma_f32 v39,-v96,v23,v39
v_mfma_f32_16x16x32_bf16 v[32:35],a[92:95],v[56:59],v[32:35]
s_waitcnt lgkmcnt(3)
buffer_load_dwordx4 v1,s[36:39],s66 offen offset:1024 sc1 lds
v_mfma_f32_16x16x32_bf16 v[28:31],a[96:99],v[60:63],v[28:31]
ds_read_b128 v[56:59],v2 offset:3072
v_mul_f32 v76,v36,v36
v_mfma_f32_16x16x32_bf16 v[32:35],a[100:103],v[60:63],v[32:35]
v_mul_f32 v77,v37,v37
v_mul_f32 v78,v38,v38
v_mfma_f32_16x16x32_bf16 v[28:31],a[104:107],v[64:67],v[28:31]
ds_read_b128 v[60:63],v2 offset:4096
v_mul_f32 v79,v39,v39
v_mfma_f32_16x16x32_bf16 v[32:35],a[108:111],v[64:67],v[32:35]
buffer_load_dwordx4 v1,s[36:39],s66 offen offset:2048 sc1 lds
v_mfma_f32_16x16x32_bf16 v[28:31],a[112:115],v[68:71],v[28:31]
ds_read_b128 v[64:67],v2 offset:5120
v_fma_f32 v76,v98,v76,v97
v_mfma_f32_16x16x32_bf16 v[32:35],a[116:119],v[68:71],v[32:35]
v_mul_f32 v76,v36,v76
v_fma_f32 v77,v98,v77,v97
v_mfma_f32_16x16x32_bf16 v[28:31],a[120:123],v[72:75],v[28:31]
ds_read_b128 v[68:71],v2 offset:6144
v_mul_f32 v77,v37,v77
v_mfma_f32_16x16x32_bf16 v[32:35],a[124:127],v[72:75],v[32:35]
s_waitcnt lgkmcnt(3)
buffer_load_dwordx4 v1,s[36:39],s66 offen offset:3072 sc1 lds
s_add_u32 m0,s71,s88
v_mfma_f32_16x16x32_bf16 v[28:31],a[0:3],v[44:47],v[28:31]
s_xor_b32 s87,s64,65536
ds_read_b128 v[72:75],v2 offset:7168
buffer_load_dword v10,s[36:39],s72 offen offset:0 sc1 lds
s_waitcnt vmcnt(10)
v_mfma_f32_16x16x32_bf16 v[32:35],a[4:7],v[44:47],v[32:35]
s_barrier
v_mfma_f32_16x16x32_bf16 v[28:31],a[8:11],v[48:51],v[28:31]
s_cmp_ge_u32 s66,s59
ds_read_b128 v[44:47],v2 offset:24576
v_fma_f32 v78,v98,v78,v97
v_mfma_f32_16x16x32_bf16 v[32:35],a[12:15],v[48:51],v[32:35]
s_cselect_b32 s86,s49,s50
v_exp_f32 v76,v76
v_mfma_f32_16x16x32_bf16 v[28:31],a[16:19],v[52:55],v[28:31]
s_cselect_b32 s87,s87,s63
ds_read_b128 v[48:51],v2 offset:25600
v_mul_f32 v78,v38,v78
v_mfma_f32_16x16x32_bf16 v[32:35],a[20:23],v[52:55],v[32:35]
s_mov_b32 s62,s87
v_exp_f32 v77,v77
v_mfma_f32_16x16x32_bf16 v[28:31],a[24:27],v[56:59],v[28:31]
s_mov_b32 m0,s62
ds_read_b128 v[52:55],v2 offset:26624
v_fma_f32 v76,v76,v99,v99
v_mfma_f32_16x16x32_bf16 v[32:35],a[28:31],v[56:59],v[32:35]
s_waitcnt lgkmcnt(3)
v_exp_f32 v78,v78
v_mfma_f32_16x16x32_bf16 v[28:31],a[32:35],v[60:63],v[28:31]
ds_read_b128 v[56:59],v2 offset:27648
v_fma_f32 v77,v77,v99,v99
v_mfma_f32_16x16x32_bf16 v[32:35],a[36:39],v[60:63],v[32:35]
v_rcp_f32 v76,v76
v_mfma_f32_16x16x32_bf16 v[28:31],a[40:43],v[64:67],v[28:31]
ds_read_b128 v[60:63],v2 offset:28672
v_fma_f32 v78,v78,v99,v99
v_mfma_f32_16x16x32_bf16 v[32:35],a[44:47],v[64:67],v[32:35]
v_rcp_f32 v77,v77
v_mfma_f32_16x16x32_bf16 v[28:31],a[48:51],v[68:71],v[28:31]
ds_read_b128 v[64:67],v2 offset:29696
v_fma_f32 v79,v98,v79,v97
v_mfma_f32_16x16x32_bf16 v[32:35],a[52:55],v[68:71],v[32:35]
v_rcp_f32 v78,v78
v_mfma_f32_16x16x32_bf16 v[28:31],a[56:59],v[72:75],v[28:31]
ds_read_b128 v[68:71],v2 offset:30720
v_mul_f32 v79,v39,v79
v_mfma_f32_16x16x32_bf16 v[32:35],a[60:63],v[72:75],v[32:35]
s_waitcnt lgkmcnt(3)
v_exp_f32 v79,v79
v_mfma_f32_16x16x32_bf16 v[28:31],a[192:195],v[44:47],v[28:31]
s_nop 0
ds_read_b128 v[72:75],v2 offset:31744
v_fma_f32 v79,v79,v99,v99
v_mfma_f32_16x16x32_bf16 v[32:35],a[196:199],v[44:47],v[32:35]
s_barrier
v_mfma_f32_16x16x32_bf16 v[28:31],a[200:203],v[48:51],v[28:31]
ds_read_b128 v[44:47],v2 offset:16384
ds_read_b32 v16,v7 offset:0
v_mfma_f32_16x16x32_bf16 v[32:35],a[204:207],v[48:51],v[32:35]
v_rcp_f32 v79,v79
v_mfma_f32_16x16x32_bf16 v[28:31],a[208:211],v[52:55],v[28:31]
ds_read_b128 v[48:51],v2 offset:17408
v_mul_f32 v36,v36,v76
v_mfma_f32_16x16x32_bf16 v[32:35],a[212:215],v[52:55],v[32:35]
v_mul_f32 v37,v37,v77
v_mul_f32 v38,v38,v78
v_mfma_f32_16x16x32_bf16 v[28:31],a[216:219],v[56:59],v[28:31]
s_waitcnt lgkmcnt(1)
ds_read_b128 v[52:55],v2 offset:18432
v_mul_f32 v39,v39,v79
v_mfma_f32_16x16x32_bf16 v[32:35],a[220:223],v[56:59],v[32:35]
v_cmp_eq_u32 vcc,s60,v16
v_cvt_pk_bf16_f32 v84,v36,v37
v_cvt_pk_bf16_f32 v85,v38,v39
v_fma_f32 v40,-v96,v24,v40
v_mfma_f32_16x16x32_bf16 v[28:31],a[224:227],v[60:63],v[28:31]
s_cmp_eq_u32 vcc_lo,-1
ds_read_b128 v[56:59],v2 offset:19456
v_fma_f32 v41,-v96,v25,v41
v_mfma_f32_16x16x32_bf16 v[32:35],a[228:231],v[60:63],v[32:35]
s_cselect_b32 s67,s86,s59
v_fma_f32 v42,-v96,v26,v42
v_fma_f32 v43,-v96,v27,v43
v_mfma_f32_16x16x32_bf16 v[28:31],a[232:235],v[64:67],v[28:31]
buffer_load_dwordx4 v0,s[36:39],s67 offen offset:0 sc1 lds
v_mfma_f32_16x16x32_bf16 v[32:35],a[236:239],v[64:67],v[32:35]
s_xor_b32 s63,s63,65536
ds_read_b128 v[60:63],v2 offset:20480
v_mul_f32 v80,v40,v40
v_mfma_f32_16x16x32_bf16 v[28:31],a[240:243],v[68:71],v[28:31]
s_add_u32 s53,s52,s55
ds_read_b128 v[64:67],v2 offset:21504
v_mul_f32 v81,v41,v41
v_mfma_f32_16x16x32_bf16 v[32:35],a[244:247],v[68:71],v[32:35]
s_cmp_eq_u32 s53,s56
v_mul_f32 v82,v42,v42
v_mul_f32 v83,v43,v43)"
R"(
v_mfma_f32_16x16x32_bf16 v[28:31],a[248:251],v[72:75],v[28:31]
buffer_load_dwordx4 v0,s[36:39],s67 offen offset:1024 sc1 lds
v_mfma_f32_16x16x32_bf16 v[32:35],a[252:255],v[72:75],v[32:35]
s_waitcnt lgkmcnt(2)
ds_read_b128 v[68:71],v2 offset:22528
v_fma_f32 v80,v98,v80,v97
v_mfma_f32_16x16x32_bf16 v[28:31],a[128:131],v[44:47],v[28:31]
s_waitcnt vmcnt(7)
ds_read_b128 v[72:75],v2 offset:23552
v_mul_f32 v80,v40,v80
v_mfma_f32_16x16x32_bf16 v[32:35],a[132:135],v[44:47],v[32:35]
s_barrier
v_mfma_f32_16x16x32_bf16 v[28:31],a[136:139],v[48:51],v[28:31]
s_cselect_b32 s53,s57,s53
ds_read_b128 v[44:47],v2 offset:40960
ds_read_b128 v[92:95],v8 offset:0
v_mfma_f32_16x16x32_bf16 v[32:35],a[140:143],v[48:51],v[32:35]
s_cmp_eq_u32 s53,s58
v_exp_f32 v80,v80
v_mfma_f32_16x16x32_bf16 v[28:31],a[144:147],v[52:55],v[28:31]
buffer_load_dwordx4 v0,s[36:39],s67 offen offset:2048 sc1 lds
v_mfma_f32_16x16x32_bf16 v[32:35],a[148:151],v[52:55],v[32:35]
s_cselect_b32 s53,s59,s53
ds_read_b128 v[48:51],v2 offset:41984
v_fma_f32 v80,v80,v99,v99
v_mfma_f32_16x16x32_bf16 v[28:31],a[152:155],v[56:59],v[28:31]
s_cmp_ge_u32 s66,s59
ds_read_b128 v[52:55],v2 offset:43008
v_fma_f32 v81,v98,v81,v97
v_mfma_f32_16x16x32_bf16 v[32:35],a[156:159],v[56:59],v[32:35]
s_waitcnt lgkmcnt(4)
v_fma_f32 v82,v98,v82,v97
v_fma_f32 v83,v98,v83,v97
v_mul_f32 v81,v41,v81
v_mul_f32 v82,v42,v82
v_mul_f32 v83,v43,v83
v_exp_f32 v81,v81
v_exp_f32 v82,v82
v_exp_f32 v83,v83
v_mfma_f32_16x16x32_bf16 v[28:31],a[160:163],v[60:63],v[28:31]
s_waitcnt lgkmcnt(2)
buffer_load_dwordx4 v0,s[36:39],s67 offen offset:3072 sc1 lds
v_fma_f32 v81,v81,v99,v99
v_fma_f32 v82,v82,v99,v99
v_fma_f32 v83,v83,v99,v99
v_rcp_f32 v80,v80
v_rcp_f32 v81,v81
v_rcp_f32 v82,v82
v_rcp_f32 v83,v83
v_mfma_f32_16x16x32_bf16 v[32:35],a[164:167],v[60:63],v[32:35]
ds_read_b128 v[56:59],v2 offset:44032
v_mul_f32 v40,v40,v80
v_mul_f32 v41,v41,v81
v_mul_f32 v42,v42,v82
v_mul_f32 v43,v43,v83
v_cvt_pk_bf16_f32 v86,v40,v41
v_cvt_pk_bf16_f32 v87,v42,v43
v_mfma_f32_16x16x32_bf16 v[28:31],a[168:171],v[64:67],v[28:31]
buffer_store_dwordx4 v[84:87],v3,s[36:39],s73 offen offset:0 sc0 sc1
ds_read_b128 v[60:63],v2 offset:45056
v_mfma_f32_16x16x32_bf16 v[32:35],a[172:175],v[64:67],v[32:35]
v_mfma_f32_16x16x32_bf16 v[28:31],a[176:179],v[68:71],v[28:31]
ds_read_b128 v[64:67],v2 offset:46080
v_mfma_f32_16x16x32_bf16 v[32:35],a[180:183],v[68:71],v[32:35]
v_mfma_f32_16x16x32_bf16 v[28:31],a[184:187],v[72:75],v[28:31]
s_waitcnt lgkmcnt(2)
ds_read_b128 v[68:71],v2 offset:47104
v_mfma_f32_16x16x32_bf16 v[32:35],a[188:191],v[72:75],v[32:35]
s_cbranch_scc1 Lg1_slow1_%=
Lg1_d1w2_%=:
v_mfma_f32_16x16x32_bf16 v[36:39],a[64:67],v[44:47],0
s_add_u32 m0,s62,16384
ds_read_b128 v[72:75],v2 offset:48128
s_lshr_b32 s73,s48,0
v_mfma_f32_16x16x32_bf16 v[40:43],a[68:71],v[44:47],0
s_lshr_b32 s72,s67,6
s_lshr_b32 s71,s62,2
s_cmp_lt_u32 s67,4194304
v_mfma_f32_16x16x32_bf16 v[36:39],a[72:75],v[48:51],v[36:39]
s_cselect_b32 s72,s59,s72
ds_read_b128 v[44:47],v2 offset:32768
s_mov_b64 s[44:45],s[46:47]
v_mfma_f32_16x16x32_bf16 v[40:43],a[76:79],v[48:51],v[40:43]
buffer_load_dwordx4 v1,s[36:39],s67 offen offset:0 sc1 lds
v_mfma_f32_16x16x32_bf16 v[36:39],a[80:83],v[52:55],v[36:39]
s_mov_b64 s[46:47],s[48:49]
ds_read_b128 v[48:51],v2 offset:33792
v_fma_f32 v28,-v92,v20,v28
v_mfma_f32_16x16x32_bf16 v[40:43],a[84:87],v[52:55],v[40:43]
s_mov_b64 s[48:49],s[50:51]
v_fma_f32 v29,-v92,v21,v29
v_fma_f32 v30,-v92,v22,v30
v_mfma_f32_16x16x32_bf16 v[36:39],a[88:91],v[56:59],v[36:39]
ds_read_b128 v[52:55],v2 offset:34816
v_fma_f32 v31,-v92,v23,v31
v_mfma_f32_16x16x32_bf16 v[40:43],a[92:95],v[56:59],v[40:43]
s_waitcnt lgkmcnt(3)
buffer_load_dwordx4 v1,s[36:39],s67 offen offset:1024 sc1 lds
v_mfma_f32_16x16x32_bf16 v[36:39],a[96:99],v[60:63],v[36:39]
ds_read_b128 v[56:59],v2 offset:35840
v_mul_f32 v76,v28,v28
v_mfma_f32_16x16x32_bf16 v[40:43],a[100:103],v[60:63],v[40:43]
v_mul_f32 v77,v29,v29
v_mul_f32 v78,v30,v30
v_mfma_f32_16x16x32_bf16 v[36:39],a[104:107],v[64:67],v[36:39]
ds_read_b128 v[60:63],v2 offset:36864
v_mul_f32 v79,v31,v31
v_mfma_f32_16x16x32_bf16 v[40:43],a[108:111],v[64:67],v[40:43]
buffer_load_dwordx4 v1,s[36:39],s67 offen offset:2048 sc1 lds
v_mfma_f32_16x16x32_bf16 v[36:39],a[112:115],v[68:71],v[36:39]
ds_read_b128 v[64:67],v2 offset:37888
v_fma_f32 v76,v94,v76,v93
v_mfma_f32_16x16x32_bf16 v[40:43],a[116:119],v[68:71],v[40:43]
v_mul_f32 v76,v28,v76
v_fma_f32 v77,v94,v77,v93
v_mfma_f32_16x16x32_bf16 v[36:39],a[120:123],v[72:75],v[36:39]
ds_read_b128 v[68:71],v2 offset:38912
v_mul_f32 v77,v29,v77
v_mfma_f32_16x16x32_bf16 v[40:43],a[124:127],v[72:75],v[40:43]
s_waitcnt lgkmcnt(3)
buffer_load_dwordx4 v1,s[36:39],s67 offen offset:3072 sc1 lds
s_add_u32 m0,s71,s88
v_mfma_f32_16x16x32_bf16 v[36:39],a[0:3],v[44:47],v[36:39]
s_xor_b32 s87,s63,65536
ds_read_b128 v[72:75],v2 offset:39936
buffer_load_dword v10,s[36:39],s72 offen offset:0 sc1 lds
s_waitcnt vmcnt(10)
v_mfma_f32_16x16x32_bf16 v[40:43],a[4:7],v[44:47],v[40:43]
s_barrier
v_mfma_f32_16x16x32_bf16 v[36:39],a[8:11],v[48:51],v[36:39]
s_cmp_ge_u32 s67,s59
ds_read_b128 v[44:47],v2 offset:57344
v_fma_f32 v78,v94,v78,v93
v_mfma_f32_16x16x32_bf16 v[40:43],a[12:15],v[48:51],v[40:43]
s_cselect_b32 s86,s50,s51
v_exp_f32 v76,v76
v_mfma_f32_16x16x32_bf16 v[36:39],a[16:19],v[52:55],v[36:39]
s_cselect_b32 s87,s87,s64
ds_read_b128 v[48:51],v2 offset:58368
v_mul_f32 v78,v30,v78
v_mfma_f32_16x16x32_bf16 v[40:43],a[20:23],v[52:55],v[40:43]
s_mov_b32 s62,s87
v_exp_f32 v77,v77
v_mfma_f32_16x16x32_bf16 v[36:39],a[24:27],v[56:59],v[36:39]
s_mov_b32 m0,s62
ds_read_b128 v[52:55],v2 offset:59392
v_fma_f32 v76,v76,v95,v95
v_mfma_f32_16x16x32_bf16 v[40:43],a[28:31],v[56:59],v[40:43]
s_waitcnt lgkmcnt(3)
v_exp_f32 v78,v78
v_mfma_f32_16x16x32_bf16 v[36:39],a[32:35],v[60:63],v[36:39]
s_mov_b64 s[50:51],s[52:53]
ds_read_b128 v[56:59],v2 offset:60416
v_fma_f32 v77,v77,v95,v95
v_mfma_f32_16x16x32_bf16 v[40:43],a[36:39],v[60:63],v[40:43]
v_rcp_f32 v76,v76
v_mfma_f32_16x16x32_bf16 v[36:39],a[40:43],v[64:67],v[36:39]
ds_read_b128 v[60:63],v2 offset:61440
v_fma_f32 v78,v78,v95,v95
v_mfma_f32_16x16x32_bf16 v[40:43],a[44:47],v[64:67],v[40:43]
v_rcp_f32 v77,v77
v_mfma_f32_16x16x32_bf16 v[36:39],a[48:51],v[68:71],v[36:39]
ds_read_b128 v[64:67],v2 offset:62464
v_fma_f32 v79,v94,v79,v93
v_mfma_f32_16x16x32_bf16 v[40:43],a[52:55],v[68:71],v[40:43]
v_rcp_f32 v78,v78
v_mfma_f32_16x16x32_bf16 v[36:39],a[56:59],v[72:75],v[36:39]
ds_read_b128 v[68:71],v2 offset:63488
v_mul_f32 v79,v31,v79
v_mfma_f32_16x16x32_bf16 v[40:43],a[60:63],v[72:75],v[40:43]
s_waitcnt lgkmcnt(3)
v_exp_f32 v79,v79
v_mfma_f32_16x16x32_bf16 v[36:39],a[192:195],v[44:47],v[36:39]
s_nop 0
ds_read_b128 v[72:75],v2 offset:64512
v_fma_f32 v79,v79,v95,v95
v_mfma_f32_16x16x32_bf16 v[40:43],a[196:199],v[44:47],v[40:43]
s_barrier
v_mfma_f32_16x16x32_bf16 v[36:39],a[200:203],v[48:51],v[36:39]
ds_read_b128 v[44:47],v2 offset:49152
ds_read_b32 v16,v7 offset:256
v_mfma_f32_16x16x32_bf16 v[40:43],a[204:207],v[48:51],v[40:43]
v_rcp_f32 v79,v79
v_mfma_f32_16x16x32_bf16 v[36:39],a[208:211],v[52:55],v[36:39]
ds_read_b128 v[48:51],v2 offset:50176
v_mul_f32 v28,v28,v76
v_mfma_f32_16x16x32_bf16 v[40:43],a[212:215],v[52:55],v[40:43]
v_mul_f32 v29,v29,v77
v_mul_f32 v30,v30,v78
v_mfma_f32_16x16x32_bf16 v[36:39],a[216:219],v[56:59],v[36:39]
s_waitcnt lgkmcnt(1)
ds_read_b128 v[52:55],v2 offset:51200
v_mul_f32 v31,v31,v79
v_mfma_f32_16x16x32_bf16 v[40:43],a[220:223],v[56:59],v[40:43]
v_cmp_eq_u32 vcc,s60,v16
v_cvt_pk_bf16_f32 v84,v28,v29
v_cvt_pk_bf16_f32 v85,v30,v31
v_fma_f32 v32,-v92,v24,v32
v_mfma_f32_16x16x32_bf16 v[36:39],a[224:227],v[60:63],v[36:39]
s_cmp_eq_u32 vcc_lo,-1)"
R"(
ds_read_b128 v[56:59],v2 offset:52224
v_fma_f32 v33,-v92,v25,v33
v_mfma_f32_16x16x32_bf16 v[40:43],a[228:231],v[60:63],v[40:43]
s_cselect_b32 s66,s86,s59
v_fma_f32 v34,-v92,v26,v34
v_fma_f32 v35,-v92,v27,v35
v_mfma_f32_16x16x32_bf16 v[36:39],a[232:235],v[64:67],v[36:39]
buffer_load_dwordx4 v0,s[36:39],s66 offen offset:0 sc1 lds
v_mfma_f32_16x16x32_bf16 v[40:43],a[236:239],v[64:67],v[40:43]
s_xor_b32 s64,s64,65536
ds_read_b128 v[60:63],v2 offset:53248
v_mul_f32 v80,v32,v32
v_mfma_f32_16x16x32_bf16 v[36:39],a[240:243],v[68:71],v[36:39]
s_add_u32 s74,s53,s55
ds_read_b128 v[64:67],v2 offset:54272
v_mul_f32 v81,v33,v33
v_mfma_f32_16x16x32_bf16 v[40:43],a[244:247],v[68:71],v[40:43]
s_cmp_eq_u32 s74,s56
v_mul_f32 v82,v34,v34
v_mul_f32 v83,v35,v35
v_mfma_f32_16x16x32_bf16 v[36:39],a[248:251],v[72:75],v[36:39]
buffer_load_dwordx4 v0,s[36:39],s66 offen offset:1024 sc1 lds
v_mfma_f32_16x16x32_bf16 v[40:43],a[252:255],v[72:75],v[40:43]
s_waitcnt lgkmcnt(2)
ds_read_b128 v[68:71],v2 offset:55296
v_fma_f32 v80,v94,v80,v93
v_mfma_f32_16x16x32_bf16 v[36:39],a[128:131],v[44:47],v[36:39]
s_waitcnt vmcnt(7)
ds_read_b128 v[72:75],v2 offset:56320
v_xor_b32 v2,65536,v2
v_mfma_f32_16x16x32_bf16 v[40:43],a[132:135],v[44:47],v[40:43]
s_barrier
v_mfma_f32_16x16x32_bf16 v[36:39],a[136:139],v[48:51],v[36:39]
s_cselect_b32 s74,s57,s74
ds_read_b128 v[44:47],v2 offset:8192
ds_read_b128 v[96:99],v8 offset:8192
v_mfma_f32_16x16x32_bf16 v[40:43],a[140:143],v[48:51],v[40:43]
s_cmp_eq_u32 s74,s58
v_mul_f32 v80,v32,v80
v_fma_f32 v81,v94,v81,v93
v_mfma_f32_16x16x32_bf16 v[36:39],a[144:147],v[52:55],v[36:39]
buffer_load_dwordx4 v0,s[36:39],s66 offen offset:2048 sc1 lds
v_mfma_f32_16x16x32_bf16 v[40:43],a[148:151],v[52:55],v[40:43]
s_cselect_b32 s74,s59,s74
ds_read_b128 v[48:51],v2 offset:9216
v_mul_f32 v81,v33,v81
v_mfma_f32_16x16x32_bf16 v[36:39],a[152:155],v[56:59],v[36:39]
s_mov_b32 s52,s74
ds_read_b128 v[52:55],v2 offset:10240
v_fma_f32 v82,v94,v82,v93
v_mfma_f32_16x16x32_bf16 v[40:43],a[156:159],v[56:59],v[40:43]
s_waitcnt lgkmcnt(4)
v_fma_f32 v83,v94,v83,v93
v_mul_f32 v82,v34,v82
v_mul_f32 v83,v35,v83
v_exp_f32 v80,v80
v_exp_f32 v81,v81
v_exp_f32 v82,v82
v_exp_f32 v83,v83
v_mfma_f32_16x16x32_bf16 v[36:39],a[160:163],v[60:63],v[36:39]
s_waitcnt lgkmcnt(2)
buffer_load_dwordx4 v0,s[36:39],s66 offen offset:3072 sc1 lds
v_fma_f32 v80,v80,v95,v95
v_fma_f32 v81,v81,v95,v95
v_fma_f32 v82,v82,v95,v95
v_fma_f32 v83,v83,v95,v95
v_rcp_f32 v80,v80
v_rcp_f32 v81,v81
v_rcp_f32 v82,v82
v_rcp_f32 v83,v83
v_mfma_f32_16x16x32_bf16 v[40:43],a[164:167],v[60:63],v[40:43]
s_cmp_lt_u32 s67,s59
ds_read_b128 v[56:59],v2 offset:11264
v_mul_f32 v32,v32,v80
v_mul_f32 v33,v33,v81
v_mul_f32 v34,v34,v82
v_mul_f32 v35,v35,v83
v_cvt_pk_bf16_f32 v86,v32,v33
v_cvt_pk_bf16_f32 v87,v34,v35
v_mfma_f32_16x16x32_bf16 v[36:39],a[168:171],v[64:67],v[36:39]
buffer_store_dwordx4 v[84:87],v3,s[36:39],s73 offen offset:0 sc0 sc1
ds_read_b128 v[60:63],v2 offset:12288
v_xor_b32 v8,16384,v8
v_mfma_f32_16x16x32_bf16 v[40:43],a[172:175],v[64:67],v[40:43]
v_mfma_f32_16x16x32_bf16 v[36:39],a[176:179],v[68:71],v[36:39]
ds_read_b128 v[64:67],v2 offset:13312
v_mfma_f32_16x16x32_bf16 v[40:43],a[180:183],v[68:71],v[40:43]
v_mfma_f32_16x16x32_bf16 v[36:39],a[184:187],v[72:75],v[36:39]
s_waitcnt lgkmcnt(2)
ds_read_b128 v[68:71],v2 offset:14336
v_mfma_f32_16x16x32_bf16 v[40:43],a[188:191],v[72:75],v[40:43]
s_cbranch_scc1 Lg1_d0w2_%=
s_branch Lg1_slow0_%=
Lg1_d0w3_%=:
v_mfma_f32_16x16x32_bf16 v[28:31],a[96:99],v[44:47],0
s_add_u32 m0,s62,16384
ds_read_b128 v[72:75],v2 offset:11264
s_lshr_b32 s73,s47,0
v_mfma_f32_16x16x32_bf16 v[32:35],a[100:103],v[44:47],0
s_lshr_b32 s72,s66,6
s_lshr_b32 s71,s62,2
s_cmp_lt_u32 s66,4194304
v_mfma_f32_16x16x32_bf16 v[28:31],a[104:107],v[48:51],v[28:31]
s_cselect_b32 s72,s59,s72
ds_read_b128 v[44:47],v2 offset:4096
v_mfma_f32_16x16x32_bf16 v[32:35],a[108:111],v[48:51],v[32:35]
v_fma_f32 v36,-v96,v20,v36
v_fma_f32 v37,-v96,v21,v37
v_mfma_f32_16x16x32_bf16 v[28:31],a[112:115],v[52:55],v[28:31]
buffer_load_dwordx4 v1,s[36:39],s66 offen offset:0 sc1 lds
v_mfma_f32_16x16x32_bf16 v[32:35],a[116:119],v[52:55],v[32:35]
ds_read_b128 v[48:51],v2 offset:5120
v_fma_f32 v38,-v96,v22,v38
v_mfma_f32_16x16x32_bf16 v[28:31],a[120:123],v[56:59],v[28:31]
ds_read_b128 v[52:55],v2 offset:6144
v_fma_f32 v39,-v96,v23,v39
v_mfma_f32_16x16x32_bf16 v[32:35],a[124:127],v[56:59],v[32:35]
s_waitcnt lgkmcnt(3)
v_mul_f32 v76,v36,v36
v_mul_f32 v77,v37,v37
v_mfma_f32_16x16x32_bf16 v[28:31],a[64:67],v[60:63],v[28:31]
buffer_load_dwordx4 v1,s[36:39],s66 offen offset:1024 sc1 lds
v_mfma_f32_16x16x32_bf16 v[32:35],a[68:71],v[60:63],v[32:35]
ds_read_b128 v[56:59],v2 offset:7168
v_mul_f32 v78,v38,v38
v_mfma_f32_16x16x32_bf16 v[28:31],a[72:75],v[64:67],v[28:31]
ds_read_b128 v[60:63],v2 offset:0
v_mul_f32 v79,v39,v39
v_mfma_f32_16x16x32_bf16 v[32:35],a[76:79],v[64:67],v[32:35]
v_fma_f32 v76,v98,v76,v97
v_mul_f32 v76,v36,v76
v_mfma_f32_16x16x32_bf16 v[28:31],a[80:83],v[68:71],v[28:31]
buffer_load_dwordx4 v1,s[36:39],s66 offen offset:2048 sc1 lds
v_mfma_f32_16x16x32_bf16 v[32:35],a[84:87],v[68:71],v[32:35]
ds_read_b128 v[64:67],v2 offset:1024
v_fma_f32 v77,v98,v77,v97
v_mfma_f32_16x16x32_bf16 v[28:31],a[88:91],v[72:75],v[28:31]
ds_read_b128 v[68:71],v2 offset:2048
v_mul_f32 v77,v37,v77
v_mfma_f32_16x16x32_bf16 v[32:35],a[92:95],v[72:75],v[32:35]
s_waitcnt lgkmcnt(3)
v_exp_f32 v76,v76
v_mfma_f32_16x16x32_bf16 v[28:31],a[32:35],v[44:47],v[28:31]
s_waitcnt vmcnt(9)
ds_read_b128 v[72:75],v2 offset:3072
v_fma_f32 v76,v76,v99,v99
v_mfma_f32_16x16x32_bf16 v[32:35],a[36:39],v[44:47],v[32:35]
s_barrier
v_mfma_f32_16x16x32_bf16 v[28:31],a[40:43],v[48:51],v[28:31]
buffer_load_dwordx4 v1,s[36:39],s66 offen offset:3072 sc1 lds
v_mfma_f32_16x16x32_bf16 v[32:35],a[44:47],v[48:51],v[32:35]
s_add_u32 m0,s71,s88
ds_read_b128 v[44:47],v2 offset:28672
v_fma_f32 v78,v98,v78,v97
v_mfma_f32_16x16x32_bf16 v[28:31],a[48:51],v[52:55],v[28:31]
buffer_load_dword v10,s[36:39],s72 offen offset:0 sc1 lds
v_mfma_f32_16x16x32_bf16 v[32:35],a[52:55],v[52:55],v[32:35]
s_xor_b32 s87,s64,65536
ds_read_b128 v[48:51],v2 offset:29696
v_fma_f32 v79,v98,v79,v97
v_mfma_f32_16x16x32_bf16 v[28:31],a[56:59],v[56:59],v[28:31]
s_cmp_ge_u32 s66,s59
ds_read_b128 v[52:55],v2 offset:30720
v_mul_f32 v78,v38,v78
v_mfma_f32_16x16x32_bf16 v[32:35],a[60:63],v[56:59],v[32:35]
s_waitcnt lgkmcnt(3)
v_exp_f32 v77,v77
v_mfma_f32_16x16x32_bf16 v[28:31],a[0:3],v[60:63],v[28:31]
s_cselect_b32 s86,s49,s50
ds_read_b128 v[56:59],v2 offset:31744
v_mul_f32 v79,v39,v79
v_mfma_f32_16x16x32_bf16 v[32:35],a[4:7],v[60:63],v[32:35]
s_cselect_b32 s87,s87,s63
v_exp_f32 v78,v78
v_mfma_f32_16x16x32_bf16 v[28:31],a[8:11],v[64:67],v[28:31]
s_mov_b32 s62,s87
ds_read_b128 v[60:63],v2 offset:24576
v_fma_f32 v77,v77,v99,v99
v_mfma_f32_16x16x32_bf16 v[32:35],a[12:15],v[64:67],v[32:35]
s_mov_b32 m0,s62
v_exp_f32 v79,v79
v_mfma_f32_16x16x32_bf16 v[28:31],a[16:19],v[68:71],v[28:31]
ds_read_b128 v[64:67],v2 offset:25600
v_fma_f32 v78,v78,v99,v99
v_mfma_f32_16x16x32_bf16 v[32:35],a[20:23],v[68:71],v[32:35]
v_rcp_f32 v76,v76
v_mfma_f32_16x16x32_bf16 v[28:31],a[24:27],v[72:75],v[28:31]
ds_read_b128 v[68:71],v2 offset:26624
v_fma_f32 v79,v79,v99,v99
v_mfma_f32_16x16x32_bf16 v[32:35],a[28:31],v[72:75],v[32:35]
s_waitcnt lgkmcnt(3)
v_rcp_f32 v77,v77
v_mfma_f32_16x16x32_bf16 v[28:31],a[224:227],v[44:47],v[28:31]
s_nop 0
ds_read_b128 v[72:75],v2 offset:27648
v_mul_f32 v36,v36,v76
v_mfma_f32_16x16x32_bf16 v[32:35],a[228:231],v[44:47],v[32:35]
s_barrier
v_mfma_f32_16x16x32_bf16 v[28:31],a[232:235],v[48:51],v[28:31]
ds_read_b128 v[44:47],v2 offset:20480)"
R"(
ds_read_b32 v16,v7 offset:0
v_mfma_f32_16x16x32_bf16 v[32:35],a[236:239],v[48:51],v[32:35]
v_rcp_f32 v78,v78
v_mfma_f32_16x16x32_bf16 v[28:31],a[240:243],v[52:55],v[28:31]
ds_read_b128 v[48:51],v2 offset:21504
v_mul_f32 v37,v37,v77
v_mfma_f32_16x16x32_bf16 v[32:35],a[244:247],v[52:55],v[32:35]
v_rcp_f32 v79,v79
v_mfma_f32_16x16x32_bf16 v[28:31],a[248:251],v[56:59],v[28:31]
s_waitcnt lgkmcnt(1)
ds_read_b128 v[52:55],v2 offset:22528
v_mul_f32 v38,v38,v78
v_mfma_f32_16x16x32_bf16 v[32:35],a[252:255],v[56:59],v[32:35]
v_cmp_eq_u32 vcc,s60,v16
v_mul_f32 v39,v39,v79
v_cvt_pk_bf16_f32 v84,v36,v37
v_cvt_pk_bf16_f32 v85,v38,v39
v_mfma_f32_16x16x32_bf16 v[28:31],a[192:195],v[60:63],v[28:31]
s_cmp_eq_u32 vcc_lo,-1
ds_read_b128 v[56:59],v2 offset:23552
v_fma_f32 v40,-v96,v24,v40
v_mfma_f32_16x16x32_bf16 v[32:35],a[196:199],v[60:63],v[32:35]
s_cselect_b32 s67,s86,s59
v_fma_f32 v41,-v96,v25,v41
v_fma_f32 v42,-v96,v26,v42
v_mfma_f32_16x16x32_bf16 v[28:31],a[200:203],v[64:67],v[28:31]
s_lshr_b32 s70,s46,5
ds_read_b128 v[60:63],v2 offset:16384
v_fma_f32 v43,-v96,v27,v43
v_mfma_f32_16x16x32_bf16 v[32:35],a[204:207],v[64:67],v[32:35]
buffer_load_dwordx4 v0,s[36:39],s67 offen offset:0 sc1 lds
v_mfma_f32_16x16x32_bf16 v[28:31],a[208:211],v[68:71],v[28:31]
s_xor_b32 s63,s63,65536
ds_read_b128 v[64:67],v2 offset:17408
v_mul_f32 v80,v40,v40
v_mfma_f32_16x16x32_bf16 v[32:35],a[212:215],v[68:71],v[32:35]
s_add_u32 s53,s52,s55
v_mul_f32 v81,v41,v41
v_mul_f32 v82,v42,v42
v_mfma_f32_16x16x32_bf16 v[28:31],a[216:219],v[72:75],v[28:31]
s_cmp_eq_u32 s53,s56
ds_read_b128 v[68:71],v2 offset:18432
v_mul_f32 v83,v43,v43
v_mfma_f32_16x16x32_bf16 v[32:35],a[220:223],v[72:75],v[32:35]
s_waitcnt lgkmcnt(3)
buffer_load_dwordx4 v0,s[36:39],s67 offen offset:1024 sc1 lds
v_mfma_f32_16x16x32_bf16 v[28:31],a[160:163],v[44:47],v[28:31]
s_waitcnt vmcnt(7)
ds_read_b128 v[72:75],v2 offset:19456
v_fma_f32 v80,v98,v80,v97
v_mfma_f32_16x16x32_bf16 v[32:35],a[164:167],v[44:47],v[32:35]
s_barrier
v_mfma_f32_16x16x32_bf16 v[28:31],a[168:171],v[48:51],v[28:31]
buffer_store_dword v5,v4,s[36:39],s70 offen offset:0 sc0 sc1
ds_read_b128 v[44:47],v2 offset:45056
ds_read_b128 v[92:95],v8 offset:0
v_mul_f32 v80,v40,v80
v_mfma_f32_16x16x32_bf16 v[32:35],a[172:175],v[48:51],v[32:35]
s_cselect_b32 s53,s57,s53
v_exp_f32 v80,v80
v_mfma_f32_16x16x32_bf16 v[28:31],a[176:179],v[52:55],v[28:31]
s_cmp_eq_u32 s53,s58
ds_read_b128 v[48:51],v2 offset:46080
v_fma_f32 v80,v80,v99,v99
v_mfma_f32_16x16x32_bf16 v[32:35],a[180:183],v[52:55],v[32:35]
buffer_load_dwordx4 v0,s[36:39],s67 offen offset:2048 sc1 lds
v_mfma_f32_16x16x32_bf16 v[28:31],a[184:187],v[56:59],v[28:31]
s_cselect_b32 s53,s59,s53
ds_read_b128 v[52:55],v2 offset:47104
v_fma_f32 v81,v98,v81,v97
v_mfma_f32_16x16x32_bf16 v[32:35],a[188:191],v[56:59],v[32:35]
s_waitcnt lgkmcnt(4)
v_rcp_f32 v80,v80
v_mfma_f32_16x16x32_bf16 v[28:31],a[128:131],v[60:63],v[28:31]
s_waitcnt lgkmcnt(2)
v_fma_f32 v82,v98,v82,v97
v_fma_f32 v83,v98,v83,v97
v_mul_f32 v81,v41,v81
v_mul_f32 v82,v42,v82
v_mul_f32 v83,v43,v83
v_exp_f32 v81,v81
v_exp_f32 v82,v82
v_exp_f32 v83,v83
v_mfma_f32_16x16x32_bf16 v[32:35],a[132:135],v[60:63],v[32:35]
s_cmp_ge_u32 s66,s59
ds_read_b128 v[56:59],v2 offset:48128
buffer_load_dwordx4 v0,s[36:39],s67 offen offset:3072 sc1 lds
v_fma_f32 v81,v81,v99,v99
v_fma_f32 v82,v82,v99,v99
v_fma_f32 v83,v83,v99,v99
v_rcp_f32 v81,v81
v_rcp_f32 v82,v82
v_rcp_f32 v83,v83
v_mfma_f32_16x16x32_bf16 v[28:31],a[136:139],v[64:67],v[28:31]
v_mul_f32 v40,v40,v80
v_mul_f32 v41,v41,v81
v_mul_f32 v42,v42,v82
v_mul_f32 v43,v43,v83
v_cvt_pk_bf16_f32 v86,v40,v41
v_cvt_pk_bf16_f32 v87,v42,v43
v_mfma_f32_16x16x32_bf16 v[32:35],a[140:143],v[64:67],v[32:35]
ds_read_b128 v[60:63],v2 offset:40960
buffer_store_dwordx4 v[84:87],v3,s[36:39],s73 offen offset:0 sc0 sc1
v_mfma_f32_16x16x32_bf16 v[28:31],a[144:147],v[68:71],v[28:31]
ds_read_b128 v[64:67],v2 offset:41984
v_mfma_f32_16x16x32_bf16 v[32:35],a[148:151],v[68:71],v[32:35]
v_mfma_f32_16x16x32_bf16 v[28:31],a[152:155],v[72:75],v[28:31]
s_waitcnt lgkmcnt(2)
ds_read_b128 v[68:71],v2 offset:43008
v_mfma_f32_16x16x32_bf16 v[32:35],a[156:159],v[72:75],v[32:35]
s_cbranch_scc1 Lg1_slow1_%=
Lg1_d1w3_%=:
v_mfma_f32_16x16x32_bf16 v[36:39],a[96:99],v[44:47],0
s_add_u32 m0,s62,16384
ds_read_b128 v[72:75],v2 offset:44032
s_lshr_b32 s73,s48,0
v_mfma_f32_16x16x32_bf16 v[40:43],a[100:103],v[44:47],0
s_lshr_b32 s72,s67,6
s_lshr_b32 s71,s62,2
s_cmp_lt_u32 s67,4194304
v_mfma_f32_16x16x32_bf16 v[36:39],a[104:107],v[48:51],v[36:39]
s_cselect_b32 s72,s59,s72
ds_read_b128 v[44:47],v2 offset:36864
s_mov_b64 s[44:45],s[46:47]
v_mfma_f32_16x16x32_bf16 v[40:43],a[108:111],v[48:51],v[40:43]
v_fma_f32 v28,-v92,v20,v28
v_fma_f32 v29,-v92,v21,v29
v_mfma_f32_16x16x32_bf16 v[36:39],a[112:115],v[52:55],v[36:39]
buffer_load_dwordx4 v1,s[36:39],s67 offen offset:0 sc1 lds
v_mfma_f32_16x16x32_bf16 v[40:43],a[116:119],v[52:55],v[40:43]
ds_read_b128 v[48:51],v2 offset:37888
v_fma_f32 v30,-v92,v22,v30
v_mfma_f32_16x16x32_bf16 v[36:39],a[120:123],v[56:59],v[36:39]
ds_read_b128 v[52:55],v2 offset:38912
v_fma_f32 v31,-v92,v23,v31
v_mfma_f32_16x16x32_bf16 v[40:43],a[124:127],v[56:59],v[40:43]
s_waitcnt lgkmcnt(3)
v_mul_f32 v76,v28,v28
v_mul_f32 v77,v29,v29
v_mfma_f32_16x16x32_bf16 v[36:39],a[64:67],v[60:63],v[36:39]
buffer_load_dwordx4 v1,s[36:39],s67 offen offset:1024 sc1 lds
v_mfma_f32_16x16x32_bf16 v[40:43],a[68:71],v[60:63],v[40:43]
ds_read_b128 v[56:59],v2 offset:39936
v_mul_f32 v78,v30,v30
v_mfma_f32_16x16x32_bf16 v[36:39],a[72:75],v[64:67],v[36:39]
ds_read_b128 v[60:63],v2 offset:32768
v_mul_f32 v79,v31,v31
v_mfma_f32_16x16x32_bf16 v[40:43],a[76:79],v[64:67],v[40:43]
v_fma_f32 v76,v94,v76,v93
v_mul_f32 v76,v28,v76
v_mfma_f32_16x16x32_bf16 v[36:39],a[80:83],v[68:71],v[36:39]
buffer_load_dwordx4 v1,s[36:39],s67 offen offset:2048 sc1 lds
v_mfma_f32_16x16x32_bf16 v[40:43],a[84:87],v[68:71],v[40:43]
ds_read_b128 v[64:67],v2 offset:33792
v_fma_f32 v77,v94,v77,v93
v_mfma_f32_16x16x32_bf16 v[36:39],a[88:91],v[72:75],v[36:39]
ds_read_b128 v[68:71],v2 offset:34816
v_mul_f32 v77,v29,v77
v_mfma_f32_16x16x32_bf16 v[40:43],a[92:95],v[72:75],v[40:43]
s_waitcnt lgkmcnt(3)
v_exp_f32 v76,v76
v_mfma_f32_16x16x32_bf16 v[36:39],a[32:35],v[44:47],v[36:39]
s_waitcnt vmcnt(9)
ds_read_b128 v[72:75],v2 offset:35840
v_fma_f32 v76,v76,v95,v95
v_mfma_f32_16x16x32_bf16 v[40:43],a[36:39],v[44:47],v[40:43]
s_barrier
v_mfma_f32_16x16x32_bf16 v[36:39],a[40:43],v[48:51],v[36:39]
buffer_load_dwordx4 v1,s[36:39],s67 offen offset:3072 sc1 lds
v_mfma_f32_16x16x32_bf16 v[40:43],a[44:47],v[48:51],v[40:43]
s_add_u32 m0,s71,s88
ds_read_b128 v[44:47],v2 offset:61440
v_fma_f32 v78,v94,v78,v93
v_mfma_f32_16x16x32_bf16 v[36:39],a[48:51],v[52:55],v[36:39]
buffer_load_dword v10,s[36:39],s72 offen offset:0 sc1 lds
v_mfma_f32_16x16x32_bf16 v[40:43],a[52:55],v[52:55],v[40:43]
s_xor_b32 s87,s63,65536
ds_read_b128 v[48:51],v2 offset:62464
v_fma_f32 v79,v94,v79,v93
v_mfma_f32_16x16x32_bf16 v[36:39],a[56:59],v[56:59],v[36:39]
s_cmp_ge_u32 s67,s59
ds_read_b128 v[52:55],v2 offset:63488
v_mul_f32 v78,v30,v78
v_mfma_f32_16x16x32_bf16 v[40:43],a[60:63],v[56:59],v[40:43]
s_waitcnt lgkmcnt(3)
v_exp_f32 v77,v77
v_mfma_f32_16x16x32_bf16 v[36:39],a[0:3],v[60:63],v[36:39]
s_cselect_b32 s86,s50,s51
ds_read_b128 v[56:59],v2 offset:64512
v_mul_f32 v79,v31,v79
v_mfma_f32_16x16x32_bf16 v[40:43],a[4:7],v[60:63],v[40:43]
s_cselect_b32 s87,s87,s64
v_exp_f32 v78,v78
v_mfma_f32_16x16x32_bf16 v[36:39],a[8:11],v[64:67],v[36:39]
s_mov_b32 s62,s87
ds_read_b128 v[60:63],v2 offset:57344
v_fma_f32 v77,v77,v95,v95
v_mfma_f32_16x16x32_bf16 v[40:43],a[12:15],v[64:67],v[40:43]
s_mov_b32 m0,s62
v_exp_f32 v79,v79)"
R"(
v_mfma_f32_16x16x32_bf16 v[36:39],a[16:19],v[68:71],v[36:39]
ds_read_b128 v[64:67],v2 offset:58368
v_fma_f32 v78,v78,v95,v95
v_mfma_f32_16x16x32_bf16 v[40:43],a[20:23],v[68:71],v[40:43]
v_rcp_f32 v76,v76
v_mfma_f32_16x16x32_bf16 v[36:39],a[24:27],v[72:75],v[36:39]
ds_read_b128 v[68:71],v2 offset:59392
v_fma_f32 v79,v79,v95,v95
v_mfma_f32_16x16x32_bf16 v[40:43],a[28:31],v[72:75],v[40:43]
s_waitcnt lgkmcnt(3)
v_rcp_f32 v77,v77
v_mfma_f32_16x16x32_bf16 v[36:39],a[224:227],v[44:47],v[36:39]
s_nop 0
ds_read_b128 v[72:75],v2 offset:60416
v_mul_f32 v28,v28,v76
v_mfma_f32_16x16x32_bf16 v[40:43],a[228:231],v[44:47],v[40:43]
s_barrier
v_mfma_f32_16x16x32_bf16 v[36:39],a[232:235],v[48:51],v[36:39]
ds_read_b128 v[44:47],v2 offset:53248
ds_read_b32 v16,v7 offset:256
v_mfma_f32_16x16x32_bf16 v[40:43],a[236:239],v[48:51],v[40:43]
v_rcp_f32 v78,v78
v_mfma_f32_16x16x32_bf16 v[36:39],a[240:243],v[52:55],v[36:39]
ds_read_b128 v[48:51],v2 offset:54272
v_mul_f32 v29,v29,v77
v_mfma_f32_16x16x32_bf16 v[40:43],a[244:247],v[52:55],v[40:43]
v_rcp_f32 v79,v79
v_mfma_f32_16x16x32_bf16 v[36:39],a[248:251],v[56:59],v[36:39]
s_waitcnt lgkmcnt(1)
ds_read_b128 v[52:55],v2 offset:55296
v_mul_f32 v30,v30,v78
v_mfma_f32_16x16x32_bf16 v[40:43],a[252:255],v[56:59],v[40:43]
v_cmp_eq_u32 vcc,s60,v16
v_mul_f32 v31,v31,v79
v_cvt_pk_bf16_f32 v84,v28,v29
v_cvt_pk_bf16_f32 v85,v30,v31
v_mfma_f32_16x16x32_bf16 v[36:39],a[192:195],v[60:63],v[36:39]
s_cmp_eq_u32 vcc_lo,-1
ds_read_b128 v[56:59],v2 offset:56320
v_fma_f32 v32,-v92,v24,v32
v_mfma_f32_16x16x32_bf16 v[40:43],a[196:199],v[60:63],v[40:43]
s_cselect_b32 s66,s86,s59
v_fma_f32 v33,-v92,v25,v33
v_fma_f32 v34,-v92,v26,v34
v_mfma_f32_16x16x32_bf16 v[36:39],a[200:203],v[64:67],v[36:39]
s_lshr_b32 s70,s47,5
ds_read_b128 v[60:63],v2 offset:49152
v_fma_f32 v35,-v92,v27,v35
v_mfma_f32_16x16x32_bf16 v[40:43],a[204:207],v[64:67],v[40:43]
buffer_load_dwordx4 v0,s[36:39],s66 offen offset:0 sc1 lds
v_mfma_f32_16x16x32_bf16 v[36:39],a[208:211],v[68:71],v[36:39]
s_xor_b32 s64,s64,65536
ds_read_b128 v[64:67],v2 offset:50176
v_mul_f32 v80,v32,v32
v_mfma_f32_16x16x32_bf16 v[40:43],a[212:215],v[68:71],v[40:43]
s_add_u32 s74,s53,s55
v_mul_f32 v81,v33,v33
v_mul_f32 v82,v34,v34
v_mfma_f32_16x16x32_bf16 v[36:39],a[216:219],v[72:75],v[36:39]
s_cmp_eq_u32 s74,s56
ds_read_b128 v[68:71],v2 offset:51200
v_mul_f32 v83,v35,v35
v_mfma_f32_16x16x32_bf16 v[40:43],a[220:223],v[72:75],v[40:43]
s_waitcnt lgkmcnt(3)
buffer_load_dwordx4 v0,s[36:39],s66 offen offset:1024 sc1 lds
v_mfma_f32_16x16x32_bf16 v[36:39],a[160:163],v[44:47],v[36:39]
s_waitcnt vmcnt(7)
ds_read_b128 v[72:75],v2 offset:52224
v_xor_b32 v2,65536,v2
v_mfma_f32_16x16x32_bf16 v[40:43],a[164:167],v[44:47],v[40:43]
s_barrier
v_mfma_f32_16x16x32_bf16 v[36:39],a[168:171],v[48:51],v[36:39]
buffer_store_dword v5,v4,s[36:39],s70 offen offset:0 sc0 sc1
ds_read_b128 v[44:47],v2 offset:12288
ds_read_b128 v[96:99],v8 offset:8192
v_fma_f32 v80,v94,v80,v93
v_mfma_f32_16x16x32_bf16 v[40:43],a[172:175],v[48:51],v[40:43]
s_cselect_b32 s74,s57,s74
v_mul_f32 v80,v32,v80
v_fma_f32 v81,v94,v81,v93
v_mfma_f32_16x16x32_bf16 v[36:39],a[176:179],v[52:55],v[36:39]
s_cmp_eq_u32 s74,s58
ds_read_b128 v[48:51],v2 offset:13312
v_mul_f32 v81,v33,v81
v_mfma_f32_16x16x32_bf16 v[40:43],a[180:183],v[52:55],v[40:43]
buffer_load_dwordx4 v0,s[36:39],s66 offen offset:2048 sc1 lds
v_mfma_f32_16x16x32_bf16 v[36:39],a[184:187],v[56:59],v[36:39]
s_cselect_b32 s74,s59,s74
ds_read_b128 v[52:55],v2 offset:14336
v_fma_f32 v82,v94,v82,v93
v_mfma_f32_16x16x32_bf16 v[40:43],a[188:191],v[56:59],v[40:43]
s_waitcnt lgkmcnt(4)
v_exp_f32 v80,v80
v_mfma_f32_16x16x32_bf16 v[36:39],a[128:131],v[60:63],v[36:39]
s_waitcnt lgkmcnt(2)
v_fma_f32 v83,v94,v83,v93
v_mul_f32 v82,v34,v82
v_mul_f32 v83,v35,v83
v_exp_f32 v81,v81
v_exp_f32 v82,v82
v_exp_f32 v83,v83
v_mfma_f32_16x16x32_bf16 v[40:43],a[132:135],v[60:63],v[40:43]
s_mov_b64 s[46:47],s[48:49]
ds_read_b128 v[56:59],v2 offset:15360
buffer_load_dwordx4 v0,s[36:39],s66 offen offset:3072 sc1 lds
v_fma_f32 v80,v80,v95,v95
v_fma_f32 v81,v81,v95,v95
v_fma_f32 v82,v82,v95,v95
v_fma_f32 v83,v83,v95,v95
v_rcp_f32 v80,v80
v_rcp_f32 v81,v81
v_rcp_f32 v82,v82
v_rcp_f32 v83,v83
v_mfma_f32_16x16x32_bf16 v[36:39],a[136:139],v[64:67],v[36:39]
s_mov_b64 s[48:49],s[50:51]
v_mul_f32 v32,v32,v80
v_mul_f32 v33,v33,v81
v_mul_f32 v34,v34,v82
v_mul_f32 v35,v35,v83
v_cvt_pk_bf16_f32 v86,v32,v33
v_cvt_pk_bf16_f32 v87,v34,v35
v_mfma_f32_16x16x32_bf16 v[40:43],a[140:143],v[64:67],v[40:43]
s_mov_b64 s[50:51],s[52:53]
ds_read_b128 v[60:63],v2 offset:8192
buffer_store_dwordx4 v[84:87],v3,s[36:39],s73 offen offset:0 sc0 sc1
v_xor_b32 v8,16384,v8
s_mov_b32 s52,s74
s_cmp_lt_u32 s67,s59
v_mfma_f32_16x16x32_bf16 v[36:39],a[144:147],v[68:71],v[36:39]
ds_read_b128 v[64:67],v2 offset:9216
v_mfma_f32_16x16x32_bf16 v[40:43],a[148:151],v[68:71],v[40:43]
v_mfma_f32_16x16x32_bf16 v[36:39],a[152:155],v[72:75],v[36:39]
s_waitcnt lgkmcnt(2)
ds_read_b128 v[68:71],v2 offset:10240
v_mfma_f32_16x16x32_bf16 v[40:43],a[156:159],v[72:75],v[40:43]
s_cbranch_scc1 Lg1_d0w3_%=
s_branch Lg1_slow0_%=
Lg1_slow0_%=:
s_cmp_lt_u32 s66,s59
s_cbranch_scc1 Lg1_gs0_%=
Lg1_slowb0_%=:
s_cmp_eq_u32 s54,1
s_cselect_b32 s68,s49,s48
s_lshr_b32 s69,s68,5
s_cmp_lt_u32 s68,4194304
s_cselect_b32 s68,0xffffcfff,-1
s_and_b32 s68,s68,s69
s_add_u32 m0,s78,8192
s_nop 0
buffer_load_dword v6,s[36:39],s68 offen offset:0 sc1 lds
s_lshr_b32 s73,s47,0
v_fma_f32 v36,-v96,v20,v36
v_fma_f32 v37,-v96,v21,v37
v_fma_f32 v38,-v96,v22,v38
v_fma_f32 v39,-v96,v23,v39
v_mul_f32 v76,v36,v36
v_mul_f32 v77,v37,v37
v_mul_f32 v78,v38,v38
v_mul_f32 v79,v39,v39
v_fma_f32 v76,v98,v76,v97
v_fma_f32 v77,v98,v77,v97
v_fma_f32 v78,v98,v78,v97
v_fma_f32 v79,v98,v79,v97
v_mul_f32 v76,v36,v76
v_mul_f32 v77,v37,v77
v_mul_f32 v78,v38,v78
v_mul_f32 v79,v39,v79
v_exp_f32 v76,v76
v_exp_f32 v77,v77
v_exp_f32 v78,v78
v_exp_f32 v79,v79
v_fma_f32 v76,v76,v99,v99
v_fma_f32 v77,v77,v99,v99
v_fma_f32 v78,v78,v99,v99
v_fma_f32 v79,v79,v99,v99
v_rcp_f32 v76,v76
v_rcp_f32 v77,v77
v_rcp_f32 v78,v78
v_rcp_f32 v79,v79
v_mul_f32 v36,v36,v76
v_mul_f32 v37,v37,v77
v_mul_f32 v38,v38,v78
v_mul_f32 v39,v39,v79
v_cvt_pk_bf16_f32 v84,v36,v37
v_cvt_pk_bf16_f32 v85,v38,v39
s_add_u32 m0,s78,8448
s_nop 0
buffer_load_dword v6,s[36:39],s68 offen offset:0 sc1 lds
v_fma_f32 v40,-v96,v24,v40
v_fma_f32 v41,-v96,v25,v41
v_fma_f32 v42,-v96,v26,v42
v_fma_f32 v43,-v96,v27,v43
v_mul_f32 v80,v40,v40
v_mul_f32 v81,v41,v41
v_mul_f32 v82,v42,v42
v_mul_f32 v83,v43,v43
v_fma_f32 v80,v98,v80,v97
v_fma_f32 v81,v98,v81,v97
v_fma_f32 v82,v98,v82,v97
v_fma_f32 v83,v98,v83,v97)"
R"(
v_mul_f32 v80,v40,v80
v_mul_f32 v81,v41,v81
v_mul_f32 v82,v42,v82
v_mul_f32 v83,v43,v83
v_exp_f32 v80,v80
v_exp_f32 v81,v81
v_exp_f32 v82,v82
v_exp_f32 v83,v83
v_fma_f32 v80,v80,v99,v99
v_fma_f32 v81,v81,v99,v99
v_fma_f32 v82,v82,v99,v99
v_fma_f32 v83,v83,v99,v99
v_rcp_f32 v80,v80
v_rcp_f32 v81,v81
v_rcp_f32 v82,v82
v_rcp_f32 v83,v83
v_mul_f32 v40,v40,v80
v_mul_f32 v41,v41,v81
v_mul_f32 v42,v42,v82
v_mul_f32 v43,v43,v83
v_cvt_pk_bf16_f32 v86,v40,v41
v_cvt_pk_bf16_f32 v87,v42,v43
buffer_store_dwordx4 v[84:87],v3,s[36:39],s73 offen offset:0 sc0 sc1
s_cmp_ge_u32 s48,s59
s_cbranch_scc1 Lg1_done0_%=
s_add_u32 m0,s78,16384
s_nop 0
buffer_load_dword v6,s[36:39],s68 offen offset:0 sc1 lds
s_waitcnt vmcnt(3)
s_barrier
ds_read_b32 v16,v7 offset:8192
s_lshr_b32 s70,s46,5
buffer_store_dword v5,v4,s[36:39],s70 offen offset:0 sc0 sc1
s_mov_b32 s46,s59
s_mov_b32 s69,18432
s_waitcnt lgkmcnt(0)
v_cmp_eq_u32 vcc,s60,v16
s_nop 0
s_cmp_eq_u32 vcc_lo,-1
s_cbranch_scc1 Lg1_go0_%=
s_add_u32 m0,s78,16640
s_nop 0
buffer_load_dword v6,s[36:39],s68 offen offset:0 sc1 lds
s_waitcnt vmcnt(4)
s_barrier
ds_read_b32 v16,v7 offset:8448
s_mov_b32 s69,18688
s_waitcnt lgkmcnt(0)
v_cmp_eq_u32 vcc,s60,v16
s_nop 0
s_cmp_eq_u32 vcc_lo,-1
s_cbranch_scc1 Lg1_go0_%=
s_add_u32 m0,s78,8192
s_nop 0
buffer_load_dword v6,s[36:39],s68 offen offset:0 sc1 lds
s_waitcnt vmcnt(3)
s_barrier
ds_read_b32 v16,v7 offset:16384
s_lshr_b32 s70,s47,5
buffer_store_dword v5,v4,s[36:39],s70 offen offset:0 sc0 sc1
s_mov_b32 s47,s59
s_mov_b32 s69,10240
s_waitcnt lgkmcnt(0)
v_cmp_eq_u32 vcc,s60,v16
s_nop 0
s_cmp_eq_u32 vcc_lo,-1
s_cbranch_scc1 Lg1_go0_%=
s_add_u32 m0,s78,8448
s_nop 0
buffer_load_dword v6,s[36:39],s68 offen offset:0 sc1 lds
s_waitcnt vmcnt(3)
s_barrier
ds_read_b32 v16,v7 offset:16640
s_mov_b32 s69,10496
s_waitcnt lgkmcnt(0)
v_cmp_eq_u32 vcc,s60,v16
s_nop 0
s_cmp_eq_u32 vcc_lo,-1
s_cbranch_scc1 Lg1_go0_%=
Lg1_poll0_%=:
s_add_u32 m0,s78,16384
s_nop 0
buffer_load_dword v6,s[36:39],s68 offen offset:0 sc1 lds
s_waitcnt vmcnt(2)
s_barrier
ds_read_b32 v16,v7 offset:8192
s_mov_b32 s69,18432
s_waitcnt lgkmcnt(0)
v_cmp_eq_u32 vcc,s60,v16
s_nop 0
s_cmp_eq_u32 vcc_lo,-1
s_cbranch_scc1 Lg1_go0_%=
s_sleep 4
s_add_u32 m0,s78,16640
s_nop 0
buffer_load_dword v6,s[36:39],s68 offen offset:0 sc1 lds
s_waitcnt vmcnt(2)
s_barrier
ds_read_b32 v16,v7 offset:8448
s_mov_b32 s69,18688
s_waitcnt lgkmcnt(0)
v_cmp_eq_u32 vcc,s60,v16
s_nop 0
s_cmp_eq_u32 vcc_lo,-1
s_cbranch_scc1 Lg1_go0_%=
s_sleep 4
s_add_u32 m0,s78,8192
s_nop 0
buffer_load_dword v6,s[36:39],s68 offen offset:0 sc1 lds
s_waitcnt vmcnt(2)
s_barrier
ds_read_b32 v16,v7 offset:16384
s_mov_b32 s69,10240
s_waitcnt lgkmcnt(0)
v_cmp_eq_u32 vcc,s60,v16
s_nop 0
s_cmp_eq_u32 vcc_lo,-1
s_cbranch_scc1 Lg1_go0_%=
s_sleep 4
s_add_u32 m0,s78,8448
s_nop 0
buffer_load_dword v6,s[36:39],s68 offen offset:0 sc1 lds
s_waitcnt vmcnt(2)
s_barrier
ds_read_b32 v16,v7 offset:16640
s_mov_b32 s69,10496
s_waitcnt lgkmcnt(0)
v_cmp_eq_u32 vcc,s60,v16
s_nop 0
s_cmp_eq_u32 vcc_lo,-1
s_cbranch_scc1 Lg1_go0_%=
s_sleep 4
s_add_u32 s79,s79,1
s_cmp_lt_u32 s79,20000
s_cbranch_scc1 Lg1_poll0_%=
Lg1_go0_%=:
s_xor_b32 s62,s63,65536
s_mov_b32 m0,s62
s_nop 0
buffer_load_dwordx4 v0,s[36:39],s48 offen offset:0 sc1 lds
buffer_load_dwordx4 v0,s[36:39],s48 offen offset:1024 sc1 lds
buffer_load_dwordx4 v0,s[36:39],s48 offen offset:2048 sc1 lds
buffer_load_dwordx4 v0,s[36:39],s48 offen offset:3072 sc1 lds
s_add_u32 m0,s62,16384
s_nop 0
buffer_load_dwordx4 v1,s[36:39],s48 offen offset:0 sc1 lds
buffer_load_dwordx4 v1,s[36:39],s48 offen offset:1024 sc1 lds
buffer_load_dwordx4 v1,s[36:39],s48 offen offset:2048 sc1 lds
buffer_load_dwordx4 v1,s[36:39],s48 offen offset:3072 sc1 lds
s_lshr_b32 s72,s48,6
s_lshr_b32 s71,s62,2
s_cmp_lt_u32 s48,4194304
s_cselect_b32 s72,s59,s72
s_add_u32 m0,s71,s88
s_nop 0
buffer_load_dword v10,s[36:39],s72 offen offset:0 sc1 lds
s_cmp_eq_u32 vcc_hi,-1
s_cselect_b32 s66,s49,s59
s_xor_b32 s62,s64,65536
s_cmp_ge_u32 s66,s59
s_cselect_b32 s68,s49,s50
s_lshr_b32 s69,s68,5
s_cmp_lt_u32 s68,4194304
s_cselect_b32 s68,0xffffcfff,-1
s_and_b32 s68,s68,s69
s_add_u32 m0,s78,0
s_nop 0
buffer_load_dword v6,s[36:39],s68 offen offset:0 sc1 lds
s_mov_b32 m0,s62
s_nop 0
buffer_load_dwordx4 v0,s[36:39],s66 offen offset:0 sc1 lds
buffer_load_dwordx4 v0,s[36:39],s66 offen offset:1024 sc1 lds
buffer_load_dwordx4 v0,s[36:39],s66 offen offset:2048 sc1 lds
buffer_load_dwordx4 v0,s[36:39],s66 offen offset:3072 sc1 lds
s_waitcnt vmcnt(10)
s_barrier
s_lshl_b32 s70,s54,12
v_add_u32 v76,s70,v2
v_xor_b32 v77,4096,v76
ds_read_b128 v[44:47],v76 offset:0
ds_read_b128 v[48:51],v76 offset:1024
ds_read_b128 v[52:55],v76 offset:2048
ds_read_b128 v[56:59],v76 offset:3072
ds_read_b128 v[60:63],v77 offset:0
ds_read_b128 v[64:67],v77 offset:1024
ds_read_b128 v[68:71],v77 offset:2048
s_lshr_b32 s70,s46,5
buffer_store_dword v5,v4,s[36:39],s70 offen offset:0 sc0 sc1
s_mov_b32 s46,s59
s_lshr_b32 s70,s47,5
buffer_store_dword v5,v4,s[36:39],s70 offen offset:0 sc0 sc1
s_mov_b32 s47,s59
s_lshr_b32 s70,s59,5
buffer_store_dword v5,v4,s[36:39],s70 offen offset:0 sc0 sc1
s_waitcnt lgkmcnt(3)
s_setpc_b64 s[90:91]
Lg1_gs0_%=:
s_cmp_eq_u32 s54,1
s_cselect_b32 s68,s49,s48
s_lshr_b32 s69,s68,5)"
R"(
s_cmp_lt_u32 s68,4194304
s_cselect_b32 s68,0xffffcfff,-1
s_and_b32 s68,s68,s69
s_lshr_b32 s73,s47,0
s_add_u32 m0,s78,8192
s_nop 0
buffer_load_dword v6,s[36:39],s68 offen offset:0 sc1 lds
v_fma_f32 v36,-v96,v20,v36
v_fma_f32 v37,-v96,v21,v37
v_fma_f32 v38,-v96,v22,v38
v_fma_f32 v39,-v96,v23,v39
v_mul_f32 v76,v36,v36
v_mul_f32 v77,v37,v37
v_mul_f32 v78,v38,v38
v_mul_f32 v79,v39,v39
v_fma_f32 v76,v98,v76,v97
v_fma_f32 v77,v98,v77,v97
v_fma_f32 v78,v98,v78,v97
s_add_u32 m0,s62,16384
s_nop 0
buffer_load_dwordx4 v1,s[36:39],s66 offen offset:0 sc1 lds
v_fma_f32 v79,v98,v79,v97
v_mul_f32 v76,v36,v76
v_mul_f32 v77,v37,v77
v_mul_f32 v78,v38,v78
v_mul_f32 v79,v39,v79
v_exp_f32 v76,v76
v_exp_f32 v77,v77
v_exp_f32 v78,v78
v_exp_f32 v79,v79
v_fma_f32 v76,v76,v99,v99
v_fma_f32 v77,v77,v99,v99
buffer_load_dwordx4 v1,s[36:39],s66 offen offset:1024 sc1 lds
v_fma_f32 v78,v78,v99,v99
v_fma_f32 v79,v79,v99,v99
v_rcp_f32 v76,v76
v_rcp_f32 v77,v77
v_rcp_f32 v78,v78
v_rcp_f32 v79,v79
v_mul_f32 v36,v36,v76
v_mul_f32 v37,v37,v77
v_mul_f32 v38,v38,v78
v_mul_f32 v39,v39,v79
v_cvt_pk_bf16_f32 v84,v36,v37
buffer_load_dwordx4 v1,s[36:39],s66 offen offset:2048 sc1 lds
v_cvt_pk_bf16_f32 v85,v38,v39
v_fma_f32 v40,-v96,v24,v40
v_fma_f32 v41,-v96,v25,v41
v_fma_f32 v42,-v96,v26,v42
v_fma_f32 v43,-v96,v27,v43
v_mul_f32 v80,v40,v40
v_mul_f32 v81,v41,v41
v_mul_f32 v82,v42,v42
v_mul_f32 v83,v43,v43
v_fma_f32 v80,v98,v80,v97
v_fma_f32 v81,v98,v81,v97
buffer_load_dwordx4 v1,s[36:39],s66 offen offset:3072 sc1 lds
v_fma_f32 v82,v98,v82,v97
v_fma_f32 v83,v98,v83,v97
v_mul_f32 v80,v40,v80
v_mul_f32 v81,v41,v81
v_mul_f32 v82,v42,v82
v_mul_f32 v83,v43,v83
v_exp_f32 v80,v80
v_exp_f32 v81,v81
v_exp_f32 v82,v82
v_exp_f32 v83,v83
v_fma_f32 v80,v80,v99,v99
s_lshr_b32 s72,s66,6
s_lshr_b32 s71,s62,2
s_cmp_lt_u32 s66,4194304
s_cselect_b32 s72,s59,s72
s_add_u32 m0,s71,s88
s_nop 0
buffer_load_dword v10,s[36:39],s72 offen offset:0 sc1 lds
v_fma_f32 v81,v81,v99,v99
v_fma_f32 v82,v82,v99,v99
v_fma_f32 v83,v83,v99,v99
v_rcp_f32 v80,v80
v_rcp_f32 v81,v81
v_rcp_f32 v82,v82
v_rcp_f32 v83,v83
v_mul_f32 v40,v40,v80
v_mul_f32 v41,v41,v81
v_mul_f32 v42,v42,v82
v_mul_f32 v43,v43,v83
v_cvt_pk_bf16_f32 v86,v40,v41
v_cvt_pk_bf16_f32 v87,v42,v43
buffer_store_dwordx4 v[84:87],v3,s[36:39],s73 offen offset:0 sc0 sc1
s_waitcnt vmcnt(6)
s_barrier
ds_read_b32 v16,v7 offset:8192
s_lshl_b32 s70,s54,12
v_add_u32 v76,s70,v2
v_xor_b32 v77,4096,v76
ds_read_b128 v[44:47],v76 offset:0
ds_read_b128 v[48:51],v76 offset:1024
ds_read_b128 v[52:55],v76 offset:2048
ds_read_b128 v[56:59],v76 offset:3072
ds_read_b128 v[60:63],v77 offset:0
ds_read_b128 v[64:67],v77 offset:1024
ds_read_b128 v[68:71],v77 offset:2048
s_lshr_b32 s70,s46,5
buffer_store_dword v5,v4,s[36:39],s70 offen offset:0 sc0 sc1
s_waitcnt lgkmcnt(7)
v_cmp_eq_u32 vcc,s60,v16
s_nop 0
s_cmp_eq_u32 vcc_hi,-1
s_cselect_b32 s66,s49,s59
s_xor_b32 s62,s64,65536
s_cmp_ge_u32 s66,s59
s_cselect_b32 s68,s49,s50
s_lshr_b32 s69,s68,5
s_cmp_lt_u32 s68,4194304
s_cselect_b32 s68,0xffffcfff,-1
s_and_b32 s68,s68,s69
s_add_u32 m0,s78,0
s_nop 0
buffer_load_dword v6,s[36:39],s68 offen offset:0 sc1 lds
s_mov_b32 m0,s62
s_nop 0
buffer_load_dwordx4 v0,s[36:39],s66 offen offset:0 sc1 lds
buffer_load_dwordx4 v0,s[36:39],s66 offen offset:1024 sc1 lds
buffer_load_dwordx4 v0,s[36:39],s66 offen offset:2048 sc1 lds
buffer_load_dwordx4 v0,s[36:39],s66 offen offset:3072 sc1 lds
s_mov_b32 s46,s47
s_mov_b32 s47,s59
s_lshr_b32 s70,s59,5
buffer_store_dword v5,v4,s[36:39],s70 offen offset:0 sc0 sc1
s_waitcnt lgkmcnt(3)
s_setpc_b64 s[90:91]
Lg1_done0_%=:
s_waitcnt vmcnt(0)
s_barrier
s_lshr_b32 s70,s46,5
buffer_store_dword v5,v4,s[36:39],s70 offen offset:0 sc0 sc1
s_lshr_b32 s70,s47,5
buffer_store_dword v5,v4,s[36:39],s70 offen offset:0 sc0 sc1
s_branch Lg1_done_%=
Lg1_slow1_%=:
s_cmp_lt_u32 s67,s59
s_cbranch_scc1 Lg1_gs1_%=
Lg1_slowb1_%=:
s_cmp_eq_u32 s54,1
s_cselect_b32 s68,s50,s49
s_lshr_b32 s69,s68,5
s_cmp_lt_u32 s68,4194304
s_cselect_b32 s68,0xffffcfff,-1
s_and_b32 s68,s68,s69
s_add_u32 m0,s78,8192
s_nop 0
buffer_load_dword v6,s[36:39],s68 offen offset:0 sc1 lds
s_lshr_b32 s73,s48,0
v_fma_f32 v28,-v92,v20,v28
v_fma_f32 v29,-v92,v21,v29
v_fma_f32 v30,-v92,v22,v30
v_fma_f32 v31,-v92,v23,v31
v_mul_f32 v76,v28,v28
v_mul_f32 v77,v29,v29
v_mul_f32 v78,v30,v30
v_mul_f32 v79,v31,v31
v_fma_f32 v76,v94,v76,v93
v_fma_f32 v77,v94,v77,v93
v_fma_f32 v78,v94,v78,v93
v_fma_f32 v79,v94,v79,v93
v_mul_f32 v76,v28,v76
v_mul_f32 v77,v29,v77
v_mul_f32 v78,v30,v78
v_mul_f32 v79,v31,v79
v_exp_f32 v76,v76
v_exp_f32 v77,v77
v_exp_f32 v78,v78
v_exp_f32 v79,v79
v_fma_f32 v76,v76,v95,v95
v_fma_f32 v77,v77,v95,v95
v_fma_f32 v78,v78,v95,v95
v_fma_f32 v79,v79,v95,v95
v_rcp_f32 v76,v76
v_rcp_f32 v77,v77
v_rcp_f32 v78,v78
v_rcp_f32 v79,v79
v_mul_f32 v28,v28,v76
v_mul_f32 v29,v29,v77
v_mul_f32 v30,v30,v78
v_mul_f32 v31,v31,v79
v_cvt_pk_bf16_f32 v84,v28,v29
v_cvt_pk_bf16_f32 v85,v30,v31
s_add_u32 m0,s78,8448
s_nop 0
buffer_load_dword v6,s[36:39],s68 offen offset:0 sc1 lds
v_fma_f32 v32,-v92,v24,v32
v_fma_f32 v33,-v92,v25,v33
v_fma_f32 v34,-v92,v26,v34
v_fma_f32 v35,-v92,v27,v35
v_mul_f32 v80,v32,v32
v_mul_f32 v81,v33,v33
v_mul_f32 v82,v34,v34
v_mul_f32 v83,v35,v35
v_fma_f32 v80,v94,v80,v93
v_fma_f32 v81,v94,v81,v93)"
R"(
v_fma_f32 v82,v94,v82,v93
v_fma_f32 v83,v94,v83,v93
v_mul_f32 v80,v32,v80
v_mul_f32 v81,v33,v81
v_mul_f32 v82,v34,v82
v_mul_f32 v83,v35,v83
v_exp_f32 v80,v80
v_exp_f32 v81,v81
v_exp_f32 v82,v82
v_exp_f32 v83,v83
v_fma_f32 v80,v80,v95,v95
v_fma_f32 v81,v81,v95,v95
v_fma_f32 v82,v82,v95,v95
v_fma_f32 v83,v83,v95,v95
v_rcp_f32 v80,v80
v_rcp_f32 v81,v81
v_rcp_f32 v82,v82
v_rcp_f32 v83,v83
v_mul_f32 v32,v32,v80
v_mul_f32 v33,v33,v81
v_mul_f32 v34,v34,v82
v_mul_f32 v35,v35,v83
v_cvt_pk_bf16_f32 v86,v32,v33
v_cvt_pk_bf16_f32 v87,v34,v35
buffer_store_dwordx4 v[84:87],v3,s[36:39],s73 offen offset:0 sc0 sc1
s_cmp_ge_u32 s49,s59
s_cbranch_scc1 Lg1_done1_%=
s_add_u32 m0,s78,16384
s_nop 0
buffer_load_dword v6,s[36:39],s68 offen offset:0 sc1 lds
s_waitcnt vmcnt(3)
s_barrier
ds_read_b32 v16,v7 offset:8192
s_lshr_b32 s70,s47,5
buffer_store_dword v5,v4,s[36:39],s70 offen offset:0 sc0 sc1
s_mov_b32 s47,s59
s_mov_b32 s69,18432
s_waitcnt lgkmcnt(0)
v_cmp_eq_u32 vcc,s60,v16
s_nop 0
s_cmp_eq_u32 vcc_lo,-1
s_cbranch_scc1 Lg1_go1_%=
s_add_u32 m0,s78,16640
s_nop 0
buffer_load_dword v6,s[36:39],s68 offen offset:0 sc1 lds
s_waitcnt vmcnt(4)
s_barrier
ds_read_b32 v16,v7 offset:8448
s_mov_b32 s69,18688
s_waitcnt lgkmcnt(0)
v_cmp_eq_u32 vcc,s60,v16
s_nop 0
s_cmp_eq_u32 vcc_lo,-1
s_cbranch_scc1 Lg1_go1_%=
s_add_u32 m0,s78,8192
s_nop 0
buffer_load_dword v6,s[36:39],s68 offen offset:0 sc1 lds
s_waitcnt vmcnt(3)
s_barrier
ds_read_b32 v16,v7 offset:16384
s_lshr_b32 s70,s48,5
buffer_store_dword v5,v4,s[36:39],s70 offen offset:0 sc0 sc1
s_mov_b32 s48,s59
s_mov_b32 s69,10240
s_waitcnt lgkmcnt(0)
v_cmp_eq_u32 vcc,s60,v16
s_nop 0
s_cmp_eq_u32 vcc_lo,-1
s_cbranch_scc1 Lg1_go1_%=
s_add_u32 m0,s78,8448
s_nop 0
buffer_load_dword v6,s[36:39],s68 offen offset:0 sc1 lds
s_waitcnt vmcnt(3)
s_barrier
ds_read_b32 v16,v7 offset:16640
s_mov_b32 s69,10496
s_waitcnt lgkmcnt(0)
v_cmp_eq_u32 vcc,s60,v16
s_nop 0
s_cmp_eq_u32 vcc_lo,-1
s_cbranch_scc1 Lg1_go1_%=
Lg1_poll1_%=:
s_add_u32 m0,s78,16384
s_nop 0
buffer_load_dword v6,s[36:39],s68 offen offset:0 sc1 lds
s_waitcnt vmcnt(2)
s_barrier
ds_read_b32 v16,v7 offset:8192
s_mov_b32 s69,18432
s_waitcnt lgkmcnt(0)
v_cmp_eq_u32 vcc,s60,v16
s_nop 0
s_cmp_eq_u32 vcc_lo,-1
s_cbranch_scc1 Lg1_go1_%=
s_sleep 4
s_add_u32 m0,s78,16640
s_nop 0
buffer_load_dword v6,s[36:39],s68 offen offset:0 sc1 lds
s_waitcnt vmcnt(2)
s_barrier
ds_read_b32 v16,v7 offset:8448
s_mov_b32 s69,18688
s_waitcnt lgkmcnt(0)
v_cmp_eq_u32 vcc,s60,v16
s_nop 0
s_cmp_eq_u32 vcc_lo,-1
s_cbranch_scc1 Lg1_go1_%=
s_sleep 4
s_add_u32 m0,s78,8192
s_nop 0
buffer_load_dword v6,s[36:39],s68 offen offset:0 sc1 lds
s_waitcnt vmcnt(2)
s_barrier
ds_read_b32 v16,v7 offset:16384
s_mov_b32 s69,10240
s_waitcnt lgkmcnt(0)
v_cmp_eq_u32 vcc,s60,v16
s_nop 0
s_cmp_eq_u32 vcc_lo,-1
s_cbranch_scc1 Lg1_go1_%=
s_sleep 4
s_add_u32 m0,s78,8448
s_nop 0
buffer_load_dword v6,s[36:39],s68 offen offset:0 sc1 lds
s_waitcnt vmcnt(2)
s_barrier
ds_read_b32 v16,v7 offset:16640
s_mov_b32 s69,10496
s_waitcnt lgkmcnt(0)
v_cmp_eq_u32 vcc,s60,v16
s_nop 0
s_cmp_eq_u32 vcc_lo,-1
s_cbranch_scc1 Lg1_go1_%=
s_sleep 4
s_add_u32 s79,s79,1
s_cmp_lt_u32 s79,20000
s_cbranch_scc1 Lg1_poll1_%=
Lg1_go1_%=:
s_xor_b32 s62,s64,65536
s_mov_b32 m0,s62
s_nop 0
buffer_load_dwordx4 v0,s[36:39],s49 offen offset:0 sc1 lds
buffer_load_dwordx4 v0,s[36:39],s49 offen offset:1024 sc1 lds
buffer_load_dwordx4 v0,s[36:39],s49 offen offset:2048 sc1 lds
buffer_load_dwordx4 v0,s[36:39],s49 offen offset:3072 sc1 lds
s_add_u32 m0,s62,16384
s_nop 0
buffer_load_dwordx4 v1,s[36:39],s49 offen offset:0 sc1 lds
buffer_load_dwordx4 v1,s[36:39],s49 offen offset:1024 sc1 lds
buffer_load_dwordx4 v1,s[36:39],s49 offen offset:2048 sc1 lds
buffer_load_dwordx4 v1,s[36:39],s49 offen offset:3072 sc1 lds
s_lshr_b32 s72,s49,6
s_lshr_b32 s71,s62,2
s_cmp_lt_u32 s49,4194304
s_cselect_b32 s72,s59,s72
s_add_u32 m0,s71,s88
s_nop 0
buffer_load_dword v10,s[36:39],s72 offen offset:0 sc1 lds
s_cmp_eq_u32 vcc_hi,-1
s_cselect_b32 s67,s50,s59
s_xor_b32 s62,s63,65536
s_cmp_ge_u32 s67,s59
s_cselect_b32 s68,s50,s51
s_lshr_b32 s69,s68,5
s_cmp_lt_u32 s68,4194304
s_cselect_b32 s68,0xffffcfff,-1
s_and_b32 s68,s68,s69
s_add_u32 m0,s78,256
s_nop 0
buffer_load_dword v6,s[36:39],s68 offen offset:0 sc1 lds
s_mov_b32 m0,s62
s_nop 0
buffer_load_dwordx4 v0,s[36:39],s67 offen offset:0 sc1 lds
buffer_load_dwordx4 v0,s[36:39],s67 offen offset:1024 sc1 lds
buffer_load_dwordx4 v0,s[36:39],s67 offen offset:2048 sc1 lds
buffer_load_dwordx4 v0,s[36:39],s67 offen offset:3072 sc1 lds
s_waitcnt vmcnt(10)
s_barrier
s_lshl_b32 s70,s54,12
v_add_u32 v76,s70,v2
v_xor_b32 v77,4096,v76
ds_read_b128 v[44:47],v76 offset:32768
ds_read_b128 v[48:51],v76 offset:33792
ds_read_b128 v[52:55],v76 offset:34816
ds_read_b128 v[56:59],v76 offset:35840
ds_read_b128 v[60:63],v77 offset:32768
ds_read_b128 v[64:67],v77 offset:33792
ds_read_b128 v[68:71],v77 offset:34816
s_lshr_b32 s70,s47,5
buffer_store_dword v5,v4,s[36:39],s70 offen offset:0 sc0 sc1
s_mov_b32 s47,s59
s_lshr_b32 s70,s48,5
buffer_store_dword v5,v4,s[36:39],s70 offen offset:0 sc0 sc1
s_mov_b32 s48,s59
s_lshr_b32 s70,s59,5
buffer_store_dword v5,v4,s[36:39],s70 offen offset:0 sc0 sc1
s_waitcnt lgkmcnt(3)
s_setpc_b64 s[92:93]
Lg1_gs1_%=:
s_cmp_eq_u32 s54,1)"
R"(
s_cselect_b32 s68,s50,s49
s_lshr_b32 s69,s68,5
s_cmp_lt_u32 s68,4194304
s_cselect_b32 s68,0xffffcfff,-1
s_and_b32 s68,s68,s69
s_lshr_b32 s73,s48,0
s_add_u32 m0,s78,8192
s_nop 0
buffer_load_dword v6,s[36:39],s68 offen offset:0 sc1 lds
v_fma_f32 v28,-v92,v20,v28
v_fma_f32 v29,-v92,v21,v29
v_fma_f32 v30,-v92,v22,v30
v_fma_f32 v31,-v92,v23,v31
v_mul_f32 v76,v28,v28
v_mul_f32 v77,v29,v29
v_mul_f32 v78,v30,v30
v_mul_f32 v79,v31,v31
v_fma_f32 v76,v94,v76,v93
v_fma_f32 v77,v94,v77,v93
v_fma_f32 v78,v94,v78,v93
s_add_u32 m0,s62,16384
s_nop 0
buffer_load_dwordx4 v1,s[36:39],s67 offen offset:0 sc1 lds
v_fma_f32 v79,v94,v79,v93
v_mul_f32 v76,v28,v76
v_mul_f32 v77,v29,v77
v_mul_f32 v78,v30,v78
v_mul_f32 v79,v31,v79
v_exp_f32 v76,v76
v_exp_f32 v77,v77
v_exp_f32 v78,v78
v_exp_f32 v79,v79
v_fma_f32 v76,v76,v95,v95
v_fma_f32 v77,v77,v95,v95
buffer_load_dwordx4 v1,s[36:39],s67 offen offset:1024 sc1 lds
v_fma_f32 v78,v78,v95,v95
v_fma_f32 v79,v79,v95,v95
v_rcp_f32 v76,v76
v_rcp_f32 v77,v77
v_rcp_f32 v78,v78
v_rcp_f32 v79,v79
v_mul_f32 v28,v28,v76
v_mul_f32 v29,v29,v77
v_mul_f32 v30,v30,v78
v_mul_f32 v31,v31,v79
v_cvt_pk_bf16_f32 v84,v28,v29
buffer_load_dwordx4 v1,s[36:39],s67 offen offset:2048 sc1 lds
v_cvt_pk_bf16_f32 v85,v30,v31
v_fma_f32 v32,-v92,v24,v32
v_fma_f32 v33,-v92,v25,v33
v_fma_f32 v34,-v92,v26,v34
v_fma_f32 v35,-v92,v27,v35
v_mul_f32 v80,v32,v32
v_mul_f32 v81,v33,v33
v_mul_f32 v82,v34,v34
v_mul_f32 v83,v35,v35
v_fma_f32 v80,v94,v80,v93
v_fma_f32 v81,v94,v81,v93
buffer_load_dwordx4 v1,s[36:39],s67 offen offset:3072 sc1 lds
v_fma_f32 v82,v94,v82,v93
v_fma_f32 v83,v94,v83,v93
v_mul_f32 v80,v32,v80
v_mul_f32 v81,v33,v81
v_mul_f32 v82,v34,v82
v_mul_f32 v83,v35,v83
v_exp_f32 v80,v80
v_exp_f32 v81,v81
v_exp_f32 v82,v82
v_exp_f32 v83,v83
v_fma_f32 v80,v80,v95,v95
s_lshr_b32 s72,s67,6
s_lshr_b32 s71,s62,2
s_cmp_lt_u32 s67,4194304
s_cselect_b32 s72,s59,s72
s_add_u32 m0,s71,s88
s_nop 0
buffer_load_dword v10,s[36:39],s72 offen offset:0 sc1 lds
v_fma_f32 v81,v81,v95,v95
v_fma_f32 v82,v82,v95,v95
v_fma_f32 v83,v83,v95,v95
v_rcp_f32 v80,v80
v_rcp_f32 v81,v81
v_rcp_f32 v82,v82
v_rcp_f32 v83,v83
v_mul_f32 v32,v32,v80
v_mul_f32 v33,v33,v81
v_mul_f32 v34,v34,v82
v_mul_f32 v35,v35,v83
v_cvt_pk_bf16_f32 v86,v32,v33
v_cvt_pk_bf16_f32 v87,v34,v35
buffer_store_dwordx4 v[84:87],v3,s[36:39],s73 offen offset:0 sc0 sc1
s_waitcnt vmcnt(6)
s_barrier
ds_read_b32 v16,v7 offset:8192
s_lshl_b32 s70,s54,12
v_add_u32 v76,s70,v2
v_xor_b32 v77,4096,v76
ds_read_b128 v[44:47],v76 offset:32768
ds_read_b128 v[48:51],v76 offset:33792
ds_read_b128 v[52:55],v76 offset:34816
ds_read_b128 v[56:59],v76 offset:35840
ds_read_b128 v[60:63],v77 offset:32768
ds_read_b128 v[64:67],v77 offset:33792
ds_read_b128 v[68:71],v77 offset:34816
s_lshr_b32 s70,s47,5
buffer_store_dword v5,v4,s[36:39],s70 offen offset:0 sc0 sc1
s_waitcnt lgkmcnt(7)
v_cmp_eq_u32 vcc,s60,v16
s_nop 0
s_cmp_eq_u32 vcc_hi,-1
s_cselect_b32 s67,s50,s59
s_xor_b32 s62,s63,65536
s_cmp_ge_u32 s67,s59
s_cselect_b32 s68,s50,s51
s_lshr_b32 s69,s68,5
s_cmp_lt_u32 s68,4194304
s_cselect_b32 s68,0xffffcfff,-1
s_and_b32 s68,s68,s69
s_add_u32 m0,s78,256
s_nop 0
buffer_load_dword v6,s[36:39],s68 offen offset:0 sc1 lds
s_mov_b32 m0,s62
s_nop 0
buffer_load_dwordx4 v0,s[36:39],s67 offen offset:0 sc1 lds
buffer_load_dwordx4 v0,s[36:39],s67 offen offset:1024 sc1 lds
buffer_load_dwordx4 v0,s[36:39],s67 offen offset:2048 sc1 lds
buffer_load_dwordx4 v0,s[36:39],s67 offen offset:3072 sc1 lds
s_mov_b32 s47,s48
s_mov_b32 s48,s59
s_lshr_b32 s70,s59,5
buffer_store_dword v5,v4,s[36:39],s70 offen offset:0 sc0 sc1
s_waitcnt lgkmcnt(3)
s_setpc_b64 s[92:93]
Lg1_done1_%=:
s_waitcnt vmcnt(0)
s_barrier
s_lshr_b32 s70,s47,5
buffer_store_dword v5,v4,s[36:39],s70 offen offset:0 sc0 sc1
s_lshr_b32 s70,s48,5
buffer_store_dword v5,v4,s[36:39],s70 offen offset:0 sc0 sc1
s_branch Lg1_done_%=
Lg1_done_%=:
s_waitcnt vmcnt(0) lgkmcnt(0)
s_barrier
s_branch Lg2_end_%=
Lg2_begin_%=:
Lcode_g2_b:
s_mov_b64 s[36:37],%1
s_mov_b32 s38,122727552
s_mov_b32 s39,0x00020000
s_mov_b32 s54,%2
s_mov_b32 s55,%3
s_mov_b32 s56,%4
s_mov_b32 s58,%5
s_mov_b32 s83,%6
s_mov_b32 s82,%7
s_mov_b32 s84,%8
v_mov_b32 v0,%9
v_mov_b32 v1,%10
v_mov_b32 v2,%11
v_mov_b32 v3,%12
v_mov_b32 v4,%13
v_mov_b32 v6,%14
v_mov_b32 v7,%15
v_mov_b32 v8,%16
v_mov_b32 v9,%17
v_mov_b32 v10,%18
v_mov_b32 v11,%19
v_mov_b32 v12,%20
v_mov_b32 v13,%21
v_mov_b32 v14,%22
v_mov_b32 v16,%23
v_mov_b32 v17,%24
v_mov_b32 v18,%25
v_mov_b32 v19,%26
v_mov_b32 v20,%27
s_mov_b64 s[40:41],%28
s_mov_b32 s42,2097152
s_mov_b32 s43,0x00020000
s_lshl_b32 s88,s54,11
s_mov_b32 s69,122727424
buffer_load_dword v5,off,s[36:39],s69 sc1
s_add_u32 m0,s88,0
s_add_u32 s61,s82,0
buffer_load_dwordx4 v16,s[40:43],s61 offen offset:0  lds
s_add_u32 m0,s88,1024
s_nop 0
buffer_load_dwordx4 v17,s[40:43],s61 offen offset:0  lds
s_add_u32 m0,s88,8064
s_nop 0
buffer_load_dwordx4 v16,s[40:43],s61 offen offset:128  lds
s_add_u32 m0,s88,9088
s_nop 0
buffer_load_dwordx4 v17,s[40:43],s61 offen offset:128  lds
s_add_u32 m0,s88,16384
s_add_u32 s61,s82,131072
buffer_load_dwordx4 v16,s[40:43],s61 offen offset:0  lds
s_add_u32 m0,s88,17408
s_nop 0
buffer_load_dwordx4 v17,s[40:43],s61 offen offset:0  lds)"
R"(
s_add_u32 m0,s88,24448
s_nop 0
buffer_load_dwordx4 v16,s[40:43],s61 offen offset:128  lds
s_add_u32 m0,s88,25472
s_nop 0
buffer_load_dwordx4 v17,s[40:43],s61 offen offset:128  lds
s_add_u32 m0,s88,32768
s_add_u32 s61,s82,262144
buffer_load_dwordx4 v16,s[40:43],s61 offen offset:0  lds
s_add_u32 m0,s88,33792
s_nop 0
buffer_load_dwordx4 v17,s[40:43],s61 offen offset:0  lds
s_add_u32 m0,s88,40832
s_nop 0
buffer_load_dwordx4 v16,s[40:43],s61 offen offset:128  lds
s_add_u32 m0,s88,41856
s_nop 0
buffer_load_dwordx4 v17,s[40:43],s61 offen offset:128  lds
s_add_u32 m0,s88,49152
s_add_u32 s61,s82,393216
buffer_load_dwordx4 v16,s[40:43],s61 offen offset:0  lds
s_add_u32 m0,s88,50176
s_nop 0
buffer_load_dwordx4 v17,s[40:43],s61 offen offset:0  lds
s_add_u32 m0,s88,57216
s_nop 0
buffer_load_dwordx4 v16,s[40:43],s61 offen offset:128  lds
s_add_u32 m0,s88,58240
s_nop 0
buffer_load_dwordx4 v17,s[40:43],s61 offen offset:128  lds
s_add_u32 m0,s88,65536
s_add_u32 s61,s82,524288
buffer_load_dwordx4 v16,s[40:43],s61 offen offset:0  lds
s_add_u32 m0,s88,66560
s_nop 0
buffer_load_dwordx4 v17,s[40:43],s61 offen offset:0  lds
s_add_u32 m0,s88,73600
s_nop 0
buffer_load_dwordx4 v16,s[40:43],s61 offen offset:128  lds
s_add_u32 m0,s88,74624
s_nop 0
buffer_load_dwordx4 v17,s[40:43],s61 offen offset:128  lds
s_add_u32 m0,s88,81920
s_add_u32 s61,s82,655360
buffer_load_dwordx4 v16,s[40:43],s61 offen offset:0  lds
s_add_u32 m0,s88,82944
s_nop 0
buffer_load_dwordx4 v17,s[40:43],s61 offen offset:0  lds
s_add_u32 m0,s88,89984
s_nop 0
buffer_load_dwordx4 v16,s[40:43],s61 offen offset:128  lds
s_add_u32 m0,s88,91008
s_nop 0
buffer_load_dwordx4 v17,s[40:43],s61 offen offset:128  lds
s_add_u32 m0,s88,98304
s_add_u32 s61,s82,786432
buffer_load_dwordx4 v16,s[40:43],s61 offen offset:0  lds
s_add_u32 m0,s88,99328
s_nop 0
buffer_load_dwordx4 v17,s[40:43],s61 offen offset:0  lds
s_add_u32 m0,s88,106368
s_nop 0
buffer_load_dwordx4 v16,s[40:43],s61 offen offset:128  lds
s_add_u32 m0,s88,107392
s_nop 0
buffer_load_dwordx4 v17,s[40:43],s61 offen offset:128  lds
v_add_u32 v24,49152,v18
v_add_u32 v25,49152,v19
v_add_u32 v26,98304,v18
v_add_u32 v27,98304,v19
s_waitcnt vmcnt(24)
s_barrier
s_add_u32 m0,s88,114688
s_add_u32 s61,s82,917504
buffer_load_dwordx4 v16,s[40:43],s61 offen offset:0  lds
s_add_u32 m0,s88,115712
ds_read_b64_tr_b16 a[0:1],v18 offset:0
buffer_load_dwordx4 v17,s[40:43],s61 offen offset:0  lds
s_add_u32 m0,s88,122752
ds_read_b64_tr_b16 a[2:3],v18 offset:512
buffer_load_dwordx4 v16,s[40:43],s61 offen offset:128  lds
s_add_u32 m0,s88,123776
ds_read_b64_tr_b16 a[4:5],v19 offset:0
buffer_load_dwordx4 v17,s[40:43],s61 offen offset:128  lds
ds_read_b64_tr_b16 a[6:7],v19 offset:512
ds_read_b64_tr_b16 a[8:9],v18 offset:4096
ds_read_b64_tr_b16 a[10:11],v18 offset:4608
ds_read_b64_tr_b16 a[12:13],v19 offset:4096
ds_read_b64_tr_b16 a[14:15],v19 offset:4608
s_waitcnt vmcnt(24)
s_barrier
s_add_u32 m0,s88,131072
s_add_u32 s61,s82,1048576
buffer_load_dwordx4 v16,s[40:43],s61 offen offset:0  lds
s_add_u32 m0,s88,132096
ds_read_b64_tr_b16 a[16:17],v18 offset:16384
buffer_load_dwordx4 v17,s[40:43],s61 offen offset:0  lds
s_add_u32 m0,s88,139136
ds_read_b64_tr_b16 a[18:19],v18 offset:16896
buffer_load_dwordx4 v16,s[40:43],s61 offen offset:128  lds
s_add_u32 m0,s88,140160
ds_read_b64_tr_b16 a[20:21],v19 offset:16384
buffer_load_dwordx4 v17,s[40:43],s61 offen offset:128  lds
ds_read_b64_tr_b16 a[22:23],v19 offset:16896
ds_read_b64_tr_b16 a[24:25],v18 offset:20480
ds_read_b64_tr_b16 a[26:27],v18 offset:20992
ds_read_b64_tr_b16 a[28:29],v19 offset:20480
ds_read_b64_tr_b16 a[30:31],v19 offset:20992
s_waitcnt lgkmcnt(8)
s_waitcnt vmcnt(24)
s_barrier
s_add_u32 m0,s88,0
s_add_u32 s61,s82,1179648
buffer_load_dwordx4 v16,s[40:43],s61 offen offset:0  lds
s_add_u32 m0,s88,1024
ds_read_b64_tr_b16 a[32:33],v18 offset:32768
buffer_load_dwordx4 v17,s[40:43],s61 offen offset:0  lds
s_add_u32 m0,s88,8064
ds_read_b64_tr_b16 a[34:35],v18 offset:33280
buffer_load_dwordx4 v16,s[40:43],s61 offen offset:128  lds
s_add_u32 m0,s88,9088
ds_read_b64_tr_b16 a[36:37],v19 offset:32768
buffer_load_dwordx4 v17,s[40:43],s61 offen offset:128  lds
ds_read_b64_tr_b16 a[38:39],v19 offset:33280
ds_read_b64_tr_b16 a[40:41],v18 offset:36864
ds_read_b64_tr_b16 a[42:43],v18 offset:37376
ds_read_b64_tr_b16 a[44:45],v19 offset:36864
ds_read_b64_tr_b16 a[46:47],v19 offset:37376
s_waitcnt lgkmcnt(8)
s_waitcnt vmcnt(24)
s_barrier
s_add_u32 m0,s88,16384
s_add_u32 s61,s82,1310720
buffer_load_dwordx4 v16,s[40:43],s61 offen offset:0  lds
s_add_u32 m0,s88,17408
ds_read_b64_tr_b16 a[48:49],v24 offset:0
buffer_load_dwordx4 v17,s[40:43],s61 offen offset:0  lds
s_add_u32 m0,s88,24448
ds_read_b64_tr_b16 a[50:51],v24 offset:512
buffer_load_dwordx4 v16,s[40:43],s61 offen offset:128  lds
s_add_u32 m0,s88,25472
ds_read_b64_tr_b16 a[52:53],v25 offset:0
buffer_load_dwordx4 v17,s[40:43],s61 offen offset:128  lds
ds_read_b64_tr_b16 a[54:55],v25 offset:512
ds_read_b64_tr_b16 a[56:57],v24 offset:4096
ds_read_b64_tr_b16 a[58:59],v24 offset:4608
ds_read_b64_tr_b16 a[60:61],v25 offset:4096
ds_read_b64_tr_b16 a[62:63],v25 offset:4608
s_waitcnt lgkmcnt(8)
s_waitcnt vmcnt(24)
s_barrier
s_add_u32 m0,s88,32768
s_add_u32 s61,s82,1441792
buffer_load_dwordx4 v16,s[40:43],s61 offen offset:0  lds
s_add_u32 m0,s88,33792
ds_read_b64_tr_b16 a[64:65],v24 offset:16384
buffer_load_dwordx4 v17,s[40:43],s61 offen offset:0  lds
s_add_u32 m0,s88,40832
ds_read_b64_tr_b16 a[66:67],v24 offset:16896
buffer_load_dwordx4 v16,s[40:43],s61 offen offset:128  lds
s_add_u32 m0,s88,41856
ds_read_b64_tr_b16 a[68:69],v25 offset:16384
buffer_load_dwordx4 v17,s[40:43],s61 offen offset:128  lds
ds_read_b64_tr_b16 a[70:71],v25 offset:16896
ds_read_b64_tr_b16 a[72:73],v24 offset:20480
ds_read_b64_tr_b16 a[74:75],v24 offset:20992
ds_read_b64_tr_b16 a[76:77],v25 offset:20480
ds_read_b64_tr_b16 a[78:79],v25 offset:20992
s_waitcnt lgkmcnt(8)
s_waitcnt vmcnt(24)
s_barrier
s_add_u32 m0,s88,49152
s_add_u32 s61,s82,1572864
buffer_load_dwordx4 v16,s[40:43],s61 offen offset:0  lds
s_add_u32 m0,s88,50176
ds_read_b64_tr_b16 a[80:81],v24 offset:32768
buffer_load_dwordx4 v17,s[40:43],s61 offen offset:0  lds
s_add_u32 m0,s88,57216
ds_read_b64_tr_b16 a[82:83],v24 offset:33280
buffer_load_dwordx4 v16,s[40:43],s61 offen offset:128  lds
s_add_u32 m0,s88,58240
ds_read_b64_tr_b16 a[84:85],v25 offset:32768
buffer_load_dwordx4 v17,s[40:43],s61 offen offset:128  lds
ds_read_b64_tr_b16 a[86:87],v25 offset:33280
ds_read_b64_tr_b16 a[88:89],v24 offset:36864
ds_read_b64_tr_b16 a[90:91],v24 offset:37376
ds_read_b64_tr_b16 a[92:93],v25 offset:36864
ds_read_b64_tr_b16 a[94:95],v25 offset:37376
s_waitcnt lgkmcnt(8)
s_waitcnt vmcnt(24)
s_barrier
s_add_u32 m0,s88,65536
s_add_u32 s61,s82,1703936
buffer_load_dwordx4 v16,s[40:43],s61 offen offset:0  lds
s_add_u32 m0,s88,66560
ds_read_b64_tr_b16 a[96:97],v26 offset:0
buffer_load_dwordx4 v17,s[40:43],s61 offen offset:0  lds
s_add_u32 m0,s88,73600
ds_read_b64_tr_b16 a[98:99],v26 offset:512
buffer_load_dwordx4 v16,s[40:43],s61 offen offset:128  lds)"
R"(
s_add_u32 m0,s88,74624
ds_read_b64_tr_b16 a[100:101],v27 offset:0
buffer_load_dwordx4 v17,s[40:43],s61 offen offset:128  lds
ds_read_b64_tr_b16 a[102:103],v27 offset:512
ds_read_b64_tr_b16 a[104:105],v26 offset:4096
ds_read_b64_tr_b16 a[106:107],v26 offset:4608
ds_read_b64_tr_b16 a[108:109],v27 offset:4096
ds_read_b64_tr_b16 a[110:111],v27 offset:4608
s_waitcnt lgkmcnt(8)
s_waitcnt vmcnt(24)
s_barrier
s_add_u32 m0,s88,81920
s_add_u32 s61,s82,1835008
buffer_load_dwordx4 v16,s[40:43],s61 offen offset:0  lds
s_add_u32 m0,s88,82944
ds_read_b64_tr_b16 a[112:113],v26 offset:16384
buffer_load_dwordx4 v17,s[40:43],s61 offen offset:0  lds
s_add_u32 m0,s88,89984
ds_read_b64_tr_b16 a[114:115],v26 offset:16896
buffer_load_dwordx4 v16,s[40:43],s61 offen offset:128  lds
s_add_u32 m0,s88,91008
ds_read_b64_tr_b16 a[116:117],v27 offset:16384
buffer_load_dwordx4 v17,s[40:43],s61 offen offset:128  lds
ds_read_b64_tr_b16 a[118:119],v27 offset:16896
ds_read_b64_tr_b16 a[120:121],v26 offset:20480
ds_read_b64_tr_b16 a[122:123],v26 offset:20992
ds_read_b64_tr_b16 a[124:125],v27 offset:20480
ds_read_b64_tr_b16 a[126:127],v27 offset:20992
s_waitcnt lgkmcnt(8)
s_waitcnt vmcnt(24)
s_barrier
s_add_u32 m0,s88,98304
s_add_u32 s61,s82,1966080
buffer_load_dwordx4 v16,s[40:43],s61 offen offset:0  lds
s_add_u32 m0,s88,99328
ds_read_b64_tr_b16 a[128:129],v26 offset:32768
buffer_load_dwordx4 v17,s[40:43],s61 offen offset:0  lds
s_add_u32 m0,s88,106368
ds_read_b64_tr_b16 a[130:131],v26 offset:33280
buffer_load_dwordx4 v16,s[40:43],s61 offen offset:128  lds
s_add_u32 m0,s88,107392
ds_read_b64_tr_b16 a[132:133],v27 offset:32768
buffer_load_dwordx4 v17,s[40:43],s61 offen offset:128  lds
ds_read_b64_tr_b16 a[134:135],v27 offset:33280
ds_read_b64_tr_b16 a[136:137],v26 offset:36864
ds_read_b64_tr_b16 a[138:139],v26 offset:37376
ds_read_b64_tr_b16 a[140:141],v27 offset:36864
ds_read_b64_tr_b16 a[142:143],v27 offset:37376
s_waitcnt lgkmcnt(8)
s_waitcnt vmcnt(24)
s_barrier
ds_read_b64_tr_b16 a[144:145],v18 offset:0
ds_read_b64_tr_b16 a[146:147],v18 offset:512
ds_read_b64_tr_b16 a[148:149],v19 offset:0
ds_read_b64_tr_b16 a[150:151],v19 offset:512
ds_read_b64_tr_b16 a[152:153],v18 offset:4096
ds_read_b64_tr_b16 a[154:155],v18 offset:4608
ds_read_b64_tr_b16 a[156:157],v19 offset:4096
ds_read_b64_tr_b16 a[158:159],v19 offset:4608
s_waitcnt lgkmcnt(8)
s_waitcnt vmcnt(20)
s_barrier
ds_read_b64_tr_b16 a[160:161],v18 offset:16384
ds_read_b64_tr_b16 a[162:163],v18 offset:16896
ds_read_b64_tr_b16 a[164:165],v19 offset:16384
ds_read_b64_tr_b16 a[166:167],v19 offset:16896
ds_read_b64_tr_b16 a[168:169],v18 offset:20480
ds_read_b64_tr_b16 a[170:171],v18 offset:20992
ds_read_b64_tr_b16 a[172:173],v19 offset:20480
ds_read_b64_tr_b16 a[174:175],v19 offset:20992
s_waitcnt lgkmcnt(8)
s_waitcnt vmcnt(16)
s_barrier
ds_read_b64_tr_b16 a[176:177],v18 offset:32768
ds_read_b64_tr_b16 a[178:179],v18 offset:33280
ds_read_b64_tr_b16 a[180:181],v19 offset:32768
ds_read_b64_tr_b16 a[182:183],v19 offset:33280
ds_read_b64_tr_b16 a[184:185],v18 offset:36864
ds_read_b64_tr_b16 a[186:187],v18 offset:37376
ds_read_b64_tr_b16 a[188:189],v19 offset:36864
ds_read_b64_tr_b16 a[190:191],v19 offset:37376
s_getpc_b64 s[46:47]
Lg2_cp_%=:
s_add_u32 s46,s46,Lg2_init_%=-Lg2_cp_%=
s_addc_u32 s47,s47,0
s_lshl_b32 s68,s54,13
v_lshl_add_u32 v48,v2,3,s68
v_min_u32 v49,Lg2_done_%=-Lg2_init_%=,v48
global_load_dword v52,v49,s[46:47]
s_waitcnt lgkmcnt(8)
s_waitcnt vmcnt(13)
s_barrier
ds_read_b64_tr_b16 a[192:193],v24 offset:0
ds_read_b64_tr_b16 a[194:195],v24 offset:512
ds_read_b64_tr_b16 a[196:197],v25 offset:0
ds_read_b64_tr_b16 a[198:199],v25 offset:512
ds_read_b64_tr_b16 a[200:201],v24 offset:4096
ds_read_b64_tr_b16 a[202:203],v24 offset:4608
ds_read_b64_tr_b16 a[204:205],v25 offset:4096
ds_read_b64_tr_b16 a[206:207],v25 offset:4608
s_waitcnt lgkmcnt(8)
s_waitcnt vmcnt(9)
s_barrier
ds_read_b64_tr_b16 a[208:209],v24 offset:16384
ds_read_b64_tr_b16 a[210:211],v24 offset:16896
ds_read_b64_tr_b16 a[212:213],v25 offset:16384
ds_read_b64_tr_b16 a[214:215],v25 offset:16896
ds_read_b64_tr_b16 a[216:217],v24 offset:20480
ds_read_b64_tr_b16 a[218:219],v24 offset:20992
ds_read_b64_tr_b16 a[220:221],v25 offset:20480
ds_read_b64_tr_b16 a[222:223],v25 offset:20992
s_waitcnt lgkmcnt(8)
s_waitcnt vmcnt(5)
s_barrier
ds_read_b64_tr_b16 a[224:225],v24 offset:32768
ds_read_b64_tr_b16 a[226:227],v24 offset:33280
ds_read_b64_tr_b16 a[228:229],v25 offset:32768
ds_read_b64_tr_b16 a[230:231],v25 offset:33280
ds_read_b64_tr_b16 a[232:233],v24 offset:36864
ds_read_b64_tr_b16 a[234:235],v24 offset:37376
ds_read_b64_tr_b16 a[236:237],v25 offset:36864
ds_read_b64_tr_b16 a[238:239],v25 offset:37376
s_waitcnt lgkmcnt(8)
s_waitcnt vmcnt(1)
s_barrier
ds_read_b64_tr_b16 a[240:241],v26 offset:0
ds_read_b64_tr_b16 a[242:243],v26 offset:512
ds_read_b64_tr_b16 a[244:245],v27 offset:0
ds_read_b64_tr_b16 a[246:247],v27 offset:512
ds_read_b64_tr_b16 a[248:249],v26 offset:4096
ds_read_b64_tr_b16 a[250:251],v26 offset:4608
ds_read_b64_tr_b16 a[252:253],v27 offset:4096
ds_read_b64_tr_b16 a[254:255],v27 offset:4608
s_waitcnt lgkmcnt(8)
s_waitcnt lgkmcnt(0)
s_waitcnt lgkmcnt(0)
s_barrier
v_readfirstlane_b32 s60,v5
s_nop 3
s_add_u32 s60,s60,1
s_cmp_eq_u32 s60,0
s_cselect_b32 s60,1,s60
Lg2_init_%=:
v_mov_b32 v5,s60
s_mov_b32 s59,536870912
s_add_u32 s57,s56,131072
s_mov_b32 s79,0
s_mov_b32 s75,0x3a800000
s_mov_b32 s76,0xc0135761
s_mov_b32 s77,0x3d372713
s_mov_b32 s44,s59
s_mov_b32 s45,s59
s_mov_b32 s46,s59
s_mov_b32 s47,s59
s_mov_b32 s48,s83
s_add_u32 s49,s48,s55
s_cmp_eq_u32 s49,s56
s_cselect_b32 s49,s57,s49
s_cmp_eq_u32 s49,s58
s_cselect_b32 s49,s59,s49
s_add_u32 s50,s49,s55
s_cmp_eq_u32 s50,s56
s_cselect_b32 s50,s57,s50
s_cmp_eq_u32 s50,s58
s_cselect_b32 s50,s59,s50
s_add_u32 s51,s50,s55
s_cmp_eq_u32 s51,s56
s_cselect_b32 s51,s57,s51
s_cmp_eq_u32 s51,s58
s_cselect_b32 s51,s59,s51
s_add_u32 s52,s51,s55
s_cmp_eq_u32 s52,s56
s_cselect_b32 s52,s57,s52
s_cmp_eq_u32 s52,s58
s_cselect_b32 s52,s59,s52
s_lshl_b32 s63,s54,12
s_add_u32 s63,s63,65536
s_add_u32 s64,s63,32768
s_lshl_b32 s88,s54,10
s_add_u32 s88,s88,131072
s_lshl_b32 s78,s54,11
s_add_u32 s78,s78,132608
s_getpc_b64 s[90:91]
Lg2_pc_%=:
s_mov_b32 s68,Lg2_d1w0_%=-Lg2_pc_%=
s_cmp_eq_u32 s54,1
s_cselect_b32 s68,Lg2_d1w1_%=-Lg2_pc_%=,s68
s_cmp_eq_u32 s54,2
s_cselect_b32 s68,Lg2_d1w2_%=-Lg2_pc_%=,s68
s_cmp_eq_u32 s54,3
s_cselect_b32 s68,Lg2_d1w3_%=-Lg2_pc_%=,s68
s_add_u32 s92,s90,s68
s_addc_u32 s93,s91,0
s_mov_b32 s68,Lg2_d0w0_%=-Lg2_pc_%=
s_cmp_eq_u32 s54,1
s_cselect_b32 s68,Lg2_d0w1_%=-Lg2_pc_%=,s68
s_cmp_eq_u32 s54,2
s_cselect_b32 s68,Lg2_d0w2_%=-Lg2_pc_%=,s68
s_cmp_eq_u32 s54,3
s_cselect_b32 s68,Lg2_d0w3_%=-Lg2_pc_%=,s68)"
R"(
s_add_u32 s90,s90,s68
s_addc_u32 s91,s91,0
s_mov_b32 s66,s59
s_mov_b32 s65,0
s_mov_b32 s62,s63
s_branch Lg2_slow0_%=
Lg2_d0w0_%=:
v_mfma_f32_16x16x32_bf16 v[16:19],a[0:3],v[32:35],0
s_add_u32 m0,s62,16384
ds_read_b128 v[60:63],v2 offset:7168
v_lshlrev_b32 v64,16,v84
v_mfma_f32_16x16x32_bf16 v[20:23],a[4:7],v[32:35],0
buffer_load_dwordx4 v1,s[36:39],s66 offen offset:0 sc1 lds
v_mfma_f32_16x16x32_bf16 v[16:19],a[8:11],v[36:39],v[16:19]
s_lshr_b32 s73,s47,0
ds_read_b128 v[32:35],v2 offset:8192
v_and_b32 v65,0xffff0000,v84
v_mfma_f32_16x16x32_bf16 v[20:23],a[12:15],v[36:39],v[20:23]
s_xor_b32 s87,s64,65536
v_lshlrev_b32 v66,16,v85
v_and_b32 v67,0xffff0000,v85
v_mfma_f32_16x16x32_bf16 v[16:19],a[16:19],v[40:43],v[16:19]
s_cmp_ge_u32 s66,s59
ds_read_b128 v[36:39],v2 offset:9216
v_lshlrev_b32 v68,16,v86
v_mfma_f32_16x16x32_bf16 v[20:23],a[20:23],v[40:43],v[20:23]
buffer_load_dwordx4 v1,s[36:39],s66 offen offset:1024 sc1 lds
v_mfma_f32_16x16x32_bf16 v[16:19],a[24:27],v[44:47],v[16:19]
s_cselect_b32 s86,s49,s50
ds_read_b128 v[40:43],v2 offset:10240
v_and_b32 v69,0xffff0000,v86
v_mfma_f32_16x16x32_bf16 v[20:23],a[28:31],v[44:47],v[20:23]
s_waitcnt lgkmcnt(3)
v_lshlrev_b32 v70,16,v87
v_and_b32 v71,0xffff0000,v87
v_mfma_f32_16x16x32_bf16 v[16:19],a[32:35],v[48:51],v[16:19]
s_cselect_b32 s87,s87,s63
ds_read_b128 v[44:47],v2 offset:11264
v_fma_f32 v64,v64,v91,v90
v_mfma_f32_16x16x32_bf16 v[20:23],a[36:39],v[48:51],v[20:23]
buffer_load_dwordx4 v1,s[36:39],s66 offen offset:2048 sc1 lds
v_mfma_f32_16x16x32_bf16 v[16:19],a[40:43],v[52:55],v[16:19]
s_lshr_b32 s72,s66,6
ds_read_b128 v[48:51],v2 offset:12288
v_fma_f32 v65,v65,v91,v90
v_mfma_f32_16x16x32_bf16 v[20:23],a[44:47],v[52:55],v[20:23]
s_lshr_b32 s71,s62,2
v_fma_f32 v66,v66,v91,v90
v_fma_f32 v67,v67,v91,v90
v_mfma_f32_16x16x32_bf16 v[16:19],a[48:51],v[56:59],v[16:19]
s_mov_b32 s62,s87
ds_read_b128 v[52:55],v2 offset:13312
v_fma_f32 v68,v68,v91,v90
v_mfma_f32_16x16x32_bf16 v[20:23],a[52:55],v[56:59],v[20:23]
buffer_load_dwordx4 v1,s[36:39],s66 offen offset:3072 sc1 lds
v_mfma_f32_16x16x32_bf16 v[16:19],a[56:59],v[60:63],v[16:19]
s_add_u32 m0,s71,s88
ds_read_b128 v[56:59],v2 offset:14336
v_fma_f32 v69,v69,v91,v90
v_mfma_f32_16x16x32_bf16 v[20:23],a[60:63],v[60:63],v[20:23]
s_waitcnt lgkmcnt(3)
v_fma_f32 v70,v70,v91,v90
v_fma_f32 v71,v71,v91,v90
v_mfma_f32_16x16x32_bf16 v[16:19],a[64:67],v[32:35],v[16:19]
s_waitcnt vmcnt(10)
ds_read_b128 v[60:63],v2 offset:15360
v_add_f32 v24,v24,v64
v_mfma_f32_16x16x32_bf16 v[20:23],a[68:71],v[32:35],v[20:23]
s_barrier
v_mfma_f32_16x16x32_bf16 v[16:19],a[72:75],v[36:39],v[16:19]
s_lshr_b32 s68,s50,5
ds_read_b128 v[32:35],v2 offset:16384
v_add_f32 v25,v25,v65
v_mfma_f32_16x16x32_bf16 v[20:23],a[76:79],v[36:39],v[20:23]
buffer_load_dwordx4 v10,s[36:39],s66 offen offset:0 sc1 lds
v_mfma_f32_16x16x32_bf16 v[16:19],a[80:83],v[40:43],v[16:19]
buffer_load_dword v11,s[36:39],s72 offen offset:1024 sc1 lds
v_mfma_f32_16x16x32_bf16 v[20:23],a[84:87],v[40:43],v[20:23]
s_lshr_b32 s70,s51,5
ds_read_b128 v[36:39],v2 offset:17408
v_add_f32 v26,v26,v66
v_mfma_f32_16x16x32_bf16 v[16:19],a[88:91],v[44:47],v[16:19]
ds_read_b128 v[40:43],v2 offset:18432
v_add_f32 v27,v27,v67
v_mfma_f32_16x16x32_bf16 v[20:23],a[92:95],v[44:47],v[20:23]
s_waitcnt lgkmcnt(3)
v_add_f32 v28,v28,v68
v_add_f32 v29,v29,v69
v_add_f32 v30,v30,v70
v_add_f32 v31,v31,v71
v_cvt_pk_bf16_f32 v72,v24,v25
v_cvt_pk_bf16_f32 v73,v26,v27
v_cvt_pk_bf16_f32 v74,v28,v29
v_cvt_pk_bf16_f32 v75,v30,v31
v_mfma_f32_16x16x32_bf16 v[16:19],a[96:99],v[48:51],v[16:19]
buffer_store_dwordx4 v[72:75],v3,s[36:39],s73 offen offset:0 sc0 sc1
ds_read_b128 v[44:47],v2 offset:19456
v_add_f32 v64,v24,v25
v_mul_f32 v65,v24,v24
v_mfma_f32_16x16x32_bf16 v[20:23],a[100:103],v[48:51],v[20:23]
v_fma_f32 v65,v25,v25,v65
v_add_f32 v64,v64,v26
v_mfma_f32_16x16x32_bf16 v[16:19],a[104:107],v[52:55],v[16:19]
ds_read_b128 v[48:51],v2 offset:20480
v_fma_f32 v65,v26,v26,v65
v_mfma_f32_16x16x32_bf16 v[20:23],a[108:111],v[52:55],v[20:23]
v_add_f32 v64,v64,v27
v_fma_f32 v65,v27,v27,v65
v_mfma_f32_16x16x32_bf16 v[16:19],a[112:115],v[56:59],v[16:19]
ds_read_b128 v[52:55],v2 offset:21504
v_add_f32 v64,v64,v28
v_mfma_f32_16x16x32_bf16 v[20:23],a[116:119],v[56:59],v[20:23]
v_fma_f32 v65,v28,v28,v65
v_add_f32 v64,v64,v29
v_fma_f32 v65,v29,v29,v65
v_add_f32 v64,v64,v30
v_fma_f32 v65,v30,v30,v65
v_add_f32 v64,v64,v31
v_fma_f32 v65,v31,v31,v65
v_mfma_f32_16x16x32_bf16 v[16:19],a[120:123],v[60:63],v[16:19]
s_nop 0
v_permlane16_swap_b32 v64,v65
v_add_f32 v64,v64,v65
v_mov_b32 v65,v64
v_mfma_f32_16x16x32_bf16 v[20:23],a[124:127],v[60:63],v[20:23]
s_waitcnt lgkmcnt(2)
ds_read_b128 v[56:59],v2 offset:22528
v_permlane32_swap_b32 v64,v65
v_add_f32 v64,v64,v65
v_mfma_f32_16x16x32_bf16 v[16:19],a[128:131],v[32:35],v[16:19]
ds_write_b32 v13,v64 offset:0
v_mfma_f32_16x16x32_bf16 v[20:23],a[132:135],v[32:35],v[20:23]
ds_read_b128 v[60:63],v2 offset:23552
v_mfma_f32_16x16x32_bf16 v[16:19],a[136:139],v[36:39],v[16:19]
ds_read_b128 v[32:35],v2 offset:24576
v_mfma_f32_16x16x32_bf16 v[20:23],a[140:143],v[36:39],v[20:23]
v_mfma_f32_16x16x32_bf16 v[16:19],a[144:147],v[40:43],v[16:19]
s_waitcnt vmcnt(11) lgkmcnt(2)
ds_read_b128 v[36:39],v2 offset:25600
v_mfma_f32_16x16x32_bf16 v[20:23],a[148:151],v[40:43],v[20:23]
s_barrier
v_mfma_f32_16x16x32_bf16 v[16:19],a[152:155],v[44:47],v[16:19]
ds_read_b128 v[40:43],v2 offset:26624
ds_read_b32 v15,v7 offset:0
v_mfma_f32_16x16x32_bf16 v[20:23],a[156:159],v[44:47],v[20:23]
s_waitcnt lgkmcnt(4)
v_mfma_f32_16x16x32_bf16 v[16:19],a[160:163],v[48:51],v[16:19]
ds_read_b128 v[44:47],v2 offset:27648
v_mfma_f32_16x16x32_bf16 v[20:23],a[164:167],v[48:51],v[20:23]
v_mfma_f32_16x16x32_bf16 v[16:19],a[168:171],v[52:55],v[16:19]
s_waitcnt lgkmcnt(1)
ds_read_b128 v[48:51],v2 offset:28672
v_mfma_f32_16x16x32_bf16 v[20:23],a[172:175],v[52:55],v[20:23]
v_cmp_eq_u32 vcc,s60,v15
v_mfma_f32_16x16x32_bf16 v[16:19],a[176:179],v[56:59],v[16:19]
s_cmp_eq_u32 vcc_lo,-1
ds_read_b128 v[52:55],v2 offset:29696
s_cselect_b32 s67,s86,s59
v_mfma_f32_16x16x32_bf16 v[20:23],a[180:183],v[56:59],v[20:23]
s_cmp_ge_u32 s67,s59
s_cselect_b32 s68,s68,s70
s_add_u32 m0,s78,256
v_mfma_f32_16x16x32_bf16 v[16:19],a[184:187],v[60:63],v[16:19]
buffer_load_dword v6,s[36:39],s68 offen offset:0 sc1 lds
v_mfma_f32_16x16x32_bf16 v[20:23],a[188:191],v[60:63],v[20:23]
s_waitcnt lgkmcnt(2)
ds_read_b128 v[56:59],v2 offset:30720
s_xor_b32 s63,s63,65536
v_mfma_f32_16x16x32_bf16 v[16:19],a[192:195],v[32:35],v[16:19]
s_waitcnt vmcnt(8)
ds_read_b128 v[60:63],v2 offset:31744
s_mov_b32 m0,s62
v_mfma_f32_16x16x32_bf16 v[20:23],a[196:199],v[32:35],v[20:23]
s_barrier
v_mfma_f32_16x16x32_bf16 v[16:19],a[200:203],v[36:39],v[16:19]
buffer_load_dwordx4 v0,s[36:39],s67 offen offset:0 sc1 lds
v_mfma_f32_16x16x32_bf16 v[20:23],a[204:207],v[36:39],v[20:23]
s_add_u32 s53,s52,s55
ds_read_b128 v[32:35],v2 offset:32768
ds_read_b128 v[80:83],v8 offset:0
v_mfma_f32_16x16x32_bf16 v[16:19],a[208:211],v[40:43],v[16:19]
s_cmp_eq_u32 s53,s56
ds_read_b128 v[36:39],v2 offset:33792
ds_read_b64 v[88:89],v9 offset:1024
v_mfma_f32_16x16x32_bf16 v[20:23],a[212:215],v[40:43],v[20:23]
s_cselect_b32 s53,s57,s53
s_cmp_eq_u32 s53,s58
s_cselect_b32 s53,s59,s53
v_mfma_f32_16x16x32_bf16 v[16:19],a[216:219],v[44:47],v[16:19]
buffer_load_dwordx4 v0,s[36:39],s67 offen offset:1024 sc1 lds
v_mfma_f32_16x16x32_bf16 v[20:23],a[220:223],v[44:47],v[20:23]
s_waitcnt lgkmcnt(4)
ds_read_b128 v[40:43],v2 offset:34816
s_cmp_ge_u32 s66,s59
v_mfma_f32_16x16x32_bf16 v[16:19],a[224:227],v[48:51],v[16:19]
ds_read_b128 v[44:47],v2 offset:35840
v_mfma_f32_16x16x32_bf16 v[20:23],a[228:231],v[48:51],v[20:23]
v_mfma_f32_16x16x32_bf16 v[16:19],a[232:235],v[52:55],v[16:19]
s_waitcnt lgkmcnt(2)
buffer_load_dwordx4 v0,s[36:39],s67 offen offset:2048 sc1 lds)"
R"(
v_mfma_f32_16x16x32_bf16 v[20:23],a[236:239],v[52:55],v[20:23]
ds_read_b128 v[48:51],v2 offset:36864
v_mfma_f32_16x16x32_bf16 v[16:19],a[240:243],v[56:59],v[16:19]
ds_read_b128 v[52:55],v2 offset:37888
v_mfma_f32_16x16x32_bf16 v[20:23],a[244:247],v[56:59],v[20:23]
v_mfma_f32_16x16x32_bf16 v[16:19],a[248:251],v[60:63],v[16:19]
s_waitcnt lgkmcnt(2)
ds_read_b128 v[56:59],v2 offset:38912
buffer_load_dwordx4 v0,s[36:39],s67 offen offset:3072 sc1 lds
v_mfma_f32_16x16x32_bf16 v[20:23],a[252:255],v[60:63],v[20:23]
s_cbranch_scc1 Lg2_slow1_%=
Lg2_d1w0_%=:
v_mfma_f32_16x16x32_bf16 v[24:27],a[0:3],v[32:35],0
s_add_u32 m0,s62,16384
ds_read_b128 v[60:63],v2 offset:39936
v_lshlrev_b32 v64,16,v80
v_mfma_f32_16x16x32_bf16 v[28:31],a[4:7],v[32:35],0
buffer_load_dwordx4 v1,s[36:39],s67 offen offset:0 sc1 lds
v_mfma_f32_16x16x32_bf16 v[24:27],a[8:11],v[36:39],v[24:27]
s_lshr_b32 s73,s48,0
ds_read_b128 v[32:35],v2 offset:40960
v_and_b32 v65,0xffff0000,v80
v_mfma_f32_16x16x32_bf16 v[28:31],a[12:15],v[36:39],v[28:31]
s_xor_b32 s87,s63,65536
v_lshlrev_b32 v66,16,v81
v_and_b32 v67,0xffff0000,v81
v_mfma_f32_16x16x32_bf16 v[24:27],a[16:19],v[40:43],v[24:27]
s_cmp_ge_u32 s67,s59
ds_read_b128 v[36:39],v2 offset:41984
v_lshlrev_b32 v68,16,v82
v_mfma_f32_16x16x32_bf16 v[28:31],a[20:23],v[40:43],v[28:31]
buffer_load_dwordx4 v1,s[36:39],s67 offen offset:1024 sc1 lds
v_mfma_f32_16x16x32_bf16 v[24:27],a[24:27],v[44:47],v[24:27]
s_cselect_b32 s86,s50,s51
ds_read_b128 v[40:43],v2 offset:43008
v_and_b32 v69,0xffff0000,v82
v_mfma_f32_16x16x32_bf16 v[28:31],a[28:31],v[44:47],v[28:31]
s_waitcnt lgkmcnt(3)
v_lshlrev_b32 v70,16,v83
v_and_b32 v71,0xffff0000,v83
v_mfma_f32_16x16x32_bf16 v[24:27],a[32:35],v[48:51],v[24:27]
s_cselect_b32 s87,s87,s64
ds_read_b128 v[44:47],v2 offset:44032
v_fma_f32 v64,v64,v89,v88
v_mfma_f32_16x16x32_bf16 v[28:31],a[36:39],v[48:51],v[28:31]
buffer_load_dwordx4 v1,s[36:39],s67 offen offset:2048 sc1 lds
v_mfma_f32_16x16x32_bf16 v[24:27],a[40:43],v[52:55],v[24:27]
s_lshr_b32 s72,s67,6
ds_read_b128 v[48:51],v2 offset:45056
v_fma_f32 v65,v65,v89,v88
v_mfma_f32_16x16x32_bf16 v[28:31],a[44:47],v[52:55],v[28:31]
s_lshr_b32 s71,s62,2
v_fma_f32 v66,v66,v89,v88
v_fma_f32 v67,v67,v89,v88
v_mfma_f32_16x16x32_bf16 v[24:27],a[48:51],v[56:59],v[24:27]
s_mov_b32 s62,s87
ds_read_b128 v[52:55],v2 offset:46080
v_fma_f32 v68,v68,v89,v88
v_mfma_f32_16x16x32_bf16 v[28:31],a[52:55],v[56:59],v[28:31]
buffer_load_dwordx4 v1,s[36:39],s67 offen offset:3072 sc1 lds
v_mfma_f32_16x16x32_bf16 v[24:27],a[56:59],v[60:63],v[24:27]
s_add_u32 m0,s71,s88
ds_read_b128 v[56:59],v2 offset:47104
v_fma_f32 v69,v69,v89,v88
v_mfma_f32_16x16x32_bf16 v[28:31],a[60:63],v[60:63],v[28:31]
s_waitcnt lgkmcnt(3)
v_fma_f32 v70,v70,v89,v88
v_fma_f32 v71,v71,v89,v88
v_mfma_f32_16x16x32_bf16 v[24:27],a[64:67],v[32:35],v[24:27]
s_waitcnt vmcnt(10)
ds_read_b128 v[60:63],v2 offset:48128
v_add_f32 v16,v16,v64
v_mfma_f32_16x16x32_bf16 v[28:31],a[68:71],v[32:35],v[28:31]
s_barrier
v_mfma_f32_16x16x32_bf16 v[24:27],a[72:75],v[36:39],v[24:27]
s_lshr_b32 s68,s51,5
ds_read_b128 v[32:35],v2 offset:49152
v_add_f32 v17,v17,v65
v_mfma_f32_16x16x32_bf16 v[28:31],a[76:79],v[36:39],v[28:31]
buffer_load_dwordx4 v10,s[36:39],s67 offen offset:0 sc1 lds
v_mfma_f32_16x16x32_bf16 v[24:27],a[80:83],v[40:43],v[24:27]
buffer_load_dword v11,s[36:39],s72 offen offset:1024 sc1 lds
v_mfma_f32_16x16x32_bf16 v[28:31],a[84:87],v[40:43],v[28:31]
s_lshr_b32 s70,s52,5
ds_read_b128 v[36:39],v2 offset:50176
v_add_f32 v18,v18,v66
v_mfma_f32_16x16x32_bf16 v[24:27],a[88:91],v[44:47],v[24:27]
s_mov_b64 s[44:45],s[46:47]
ds_read_b128 v[40:43],v2 offset:51200
v_add_f32 v19,v19,v67
v_mfma_f32_16x16x32_bf16 v[28:31],a[92:95],v[44:47],v[28:31]
s_waitcnt lgkmcnt(3)
v_add_f32 v20,v20,v68
v_add_f32 v21,v21,v69
v_add_f32 v22,v22,v70
v_add_f32 v23,v23,v71
v_cvt_pk_bf16_f32 v72,v16,v17
v_cvt_pk_bf16_f32 v73,v18,v19
v_cvt_pk_bf16_f32 v74,v20,v21
v_cvt_pk_bf16_f32 v75,v22,v23
v_mfma_f32_16x16x32_bf16 v[24:27],a[96:99],v[48:51],v[24:27]
buffer_store_dwordx4 v[72:75],v3,s[36:39],s73 offen offset:0 sc0 sc1
ds_read_b128 v[44:47],v2 offset:52224
v_add_f32 v64,v16,v17
v_mul_f32 v65,v16,v16
v_mfma_f32_16x16x32_bf16 v[28:31],a[100:103],v[48:51],v[28:31]
s_mov_b64 s[46:47],s[48:49]
v_fma_f32 v65,v17,v17,v65
v_add_f32 v64,v64,v18
v_mfma_f32_16x16x32_bf16 v[24:27],a[104:107],v[52:55],v[24:27]
s_mov_b64 s[48:49],s[50:51]
ds_read_b128 v[48:51],v2 offset:53248
v_fma_f32 v65,v18,v18,v65
v_mfma_f32_16x16x32_bf16 v[28:31],a[108:111],v[52:55],v[28:31]
s_mov_b64 s[50:51],s[52:53]
v_add_f32 v64,v64,v19
v_fma_f32 v65,v19,v19,v65
v_mfma_f32_16x16x32_bf16 v[24:27],a[112:115],v[56:59],v[24:27]
ds_read_b128 v[52:55],v2 offset:54272
v_add_f32 v64,v64,v20
v_mfma_f32_16x16x32_bf16 v[28:31],a[116:119],v[56:59],v[28:31]
v_fma_f32 v65,v20,v20,v65
v_add_f32 v64,v64,v21
v_fma_f32 v65,v21,v21,v65
v_add_f32 v64,v64,v22
v_fma_f32 v65,v22,v22,v65
v_add_f32 v64,v64,v23
v_fma_f32 v65,v23,v23,v65
v_mfma_f32_16x16x32_bf16 v[24:27],a[120:123],v[60:63],v[24:27]
s_nop 0
v_permlane16_swap_b32 v64,v65
v_add_f32 v64,v64,v65
v_mov_b32 v65,v64
v_mfma_f32_16x16x32_bf16 v[28:31],a[124:127],v[60:63],v[28:31]
s_waitcnt lgkmcnt(2)
ds_read_b128 v[56:59],v2 offset:55296
v_permlane32_swap_b32 v64,v65
v_add_f32 v64,v64,v65
v_mfma_f32_16x16x32_bf16 v[24:27],a[128:131],v[32:35],v[24:27]
ds_write_b32 v13,v64 offset:2048
v_mfma_f32_16x16x32_bf16 v[28:31],a[132:135],v[32:35],v[28:31]
ds_read_b128 v[60:63],v2 offset:56320
v_mfma_f32_16x16x32_bf16 v[24:27],a[136:139],v[36:39],v[24:27]
ds_read_b128 v[32:35],v2 offset:57344
v_mfma_f32_16x16x32_bf16 v[28:31],a[140:143],v[36:39],v[28:31]
v_mfma_f32_16x16x32_bf16 v[24:27],a[144:147],v[40:43],v[24:27]
s_waitcnt vmcnt(11) lgkmcnt(2)
ds_read_b128 v[36:39],v2 offset:58368
v_mfma_f32_16x16x32_bf16 v[28:31],a[148:151],v[40:43],v[28:31]
s_barrier
v_mfma_f32_16x16x32_bf16 v[24:27],a[152:155],v[44:47],v[24:27]
ds_read_b128 v[40:43],v2 offset:59392
ds_read_b32 v15,v7 offset:256
v_mfma_f32_16x16x32_bf16 v[28:31],a[156:159],v[44:47],v[28:31]
s_waitcnt lgkmcnt(4)
v_mfma_f32_16x16x32_bf16 v[24:27],a[160:163],v[48:51],v[24:27]
ds_read_b128 v[44:47],v2 offset:60416
v_mfma_f32_16x16x32_bf16 v[28:31],a[164:167],v[48:51],v[28:31]
v_mfma_f32_16x16x32_bf16 v[24:27],a[168:171],v[52:55],v[24:27]
s_waitcnt lgkmcnt(1)
ds_read_b128 v[48:51],v2 offset:61440
v_mfma_f32_16x16x32_bf16 v[28:31],a[172:175],v[52:55],v[28:31]
v_cmp_eq_u32 vcc,s60,v15
v_mfma_f32_16x16x32_bf16 v[24:27],a[176:179],v[56:59],v[24:27]
s_cmp_eq_u32 vcc_lo,-1
ds_read_b128 v[52:55],v2 offset:62464
s_cselect_b32 s66,s86,s59
v_mfma_f32_16x16x32_bf16 v[28:31],a[180:183],v[56:59],v[28:31]
s_cmp_ge_u32 s66,s59
s_cselect_b32 s68,s68,s70
s_add_u32 m0,s78,0
v_mfma_f32_16x16x32_bf16 v[24:27],a[184:187],v[60:63],v[24:27]
buffer_load_dword v6,s[36:39],s68 offen offset:0 sc1 lds
v_mfma_f32_16x16x32_bf16 v[28:31],a[188:191],v[60:63],v[28:31]
s_waitcnt lgkmcnt(2)
ds_read_b128 v[56:59],v2 offset:63488
s_xor_b32 s64,s64,65536
v_mfma_f32_16x16x32_bf16 v[24:27],a[192:195],v[32:35],v[24:27]
s_waitcnt vmcnt(8)
ds_read_b128 v[60:63],v2 offset:64512
v_xor_b32 v2,65536,v2
v_mfma_f32_16x16x32_bf16 v[28:31],a[196:199],v[32:35],v[28:31]
s_barrier
s_mov_b32 m0,s62
v_mfma_f32_16x16x32_bf16 v[24:27],a[200:203],v[36:39],v[24:27]
buffer_load_dwordx4 v0,s[36:39],s66 offen offset:0 sc1 lds
v_mfma_f32_16x16x32_bf16 v[28:31],a[204:207],v[36:39],v[28:31]
s_add_u32 s74,s53,s55
ds_read_b128 v[32:35],v2 offset:0
ds_read_b128 v[84:87],v8 offset:8192
v_mfma_f32_16x16x32_bf16 v[24:27],a[208:211],v[40:43],v[24:27]
s_cmp_eq_u32 s74,s56
ds_read_b128 v[36:39],v2 offset:1024
ds_read_b64 v[90:91],v9 offset:9216
v_mfma_f32_16x16x32_bf16 v[28:31],a[212:215],v[40:43],v[28:31]
s_cselect_b32 s74,s57,s74
v_xor_b32 v8,16384,v8
v_xor_b32 v9,16384,v9
v_mfma_f32_16x16x32_bf16 v[24:27],a[216:219],v[44:47],v[24:27]
buffer_load_dwordx4 v0,s[36:39],s66 offen offset:1024 sc1 lds)"
R"(
v_mfma_f32_16x16x32_bf16 v[28:31],a[220:223],v[44:47],v[28:31]
s_waitcnt lgkmcnt(4)
ds_read_b128 v[40:43],v2 offset:2048
s_cmp_eq_u32 s74,s58
v_mfma_f32_16x16x32_bf16 v[24:27],a[224:227],v[48:51],v[24:27]
s_cselect_b32 s74,s59,s74
ds_read_b128 v[44:47],v2 offset:3072
s_mov_b32 s52,s74
v_mfma_f32_16x16x32_bf16 v[28:31],a[228:231],v[48:51],v[28:31]
s_cmp_lt_u32 s67,s59
v_mfma_f32_16x16x32_bf16 v[24:27],a[232:235],v[52:55],v[24:27]
s_waitcnt lgkmcnt(2)
buffer_load_dwordx4 v0,s[36:39],s66 offen offset:2048 sc1 lds
v_mfma_f32_16x16x32_bf16 v[28:31],a[236:239],v[52:55],v[28:31]
ds_read_b128 v[48:51],v2 offset:4096
v_mfma_f32_16x16x32_bf16 v[24:27],a[240:243],v[56:59],v[24:27]
ds_read_b128 v[52:55],v2 offset:5120
v_mfma_f32_16x16x32_bf16 v[28:31],a[244:247],v[56:59],v[28:31]
v_mfma_f32_16x16x32_bf16 v[24:27],a[248:251],v[60:63],v[24:27]
s_waitcnt lgkmcnt(2)
ds_read_b128 v[56:59],v2 offset:6144
buffer_load_dwordx4 v0,s[36:39],s66 offen offset:3072 sc1 lds
v_mfma_f32_16x16x32_bf16 v[28:31],a[252:255],v[60:63],v[28:31]
s_cbranch_scc1 Lg2_d0w0_%=
s_branch Lg2_slow0_%=
Lg2_d0w1_%=:
v_mfma_f32_16x16x32_bf16 v[16:19],a[0:3],v[32:35],0
s_add_u32 m0,s62,16384
ds_read_b128 v[60:63],v2 offset:7168
v_lshlrev_b32 v64,16,v84
v_mfma_f32_16x16x32_bf16 v[20:23],a[4:7],v[32:35],0
s_lshr_b32 s73,s47,0
v_and_b32 v65,0xffff0000,v84
v_lshlrev_b32 v66,16,v85
v_mfma_f32_16x16x32_bf16 v[16:19],a[8:11],v[36:39],v[16:19]
buffer_load_dwordx4 v1,s[36:39],s66 offen offset:0 sc1 lds
v_mfma_f32_16x16x32_bf16 v[20:23],a[12:15],v[36:39],v[20:23]
s_xor_b32 s87,s64,65536
ds_read_b128 v[32:35],v2 offset:8192
v_and_b32 v67,0xffff0000,v85
v_mfma_f32_16x16x32_bf16 v[16:19],a[16:19],v[40:43],v[16:19]
s_cmp_ge_u32 s66,s59
ds_read_b128 v[36:39],v2 offset:9216
v_lshlrev_b32 v68,16,v86
v_mfma_f32_16x16x32_bf16 v[20:23],a[20:23],v[40:43],v[20:23]
s_cselect_b32 s86,s49,s50
v_and_b32 v69,0xffff0000,v86
v_lshlrev_b32 v70,16,v87
v_mfma_f32_16x16x32_bf16 v[16:19],a[24:27],v[44:47],v[16:19]
buffer_load_dwordx4 v1,s[36:39],s66 offen offset:1024 sc1 lds
v_mfma_f32_16x16x32_bf16 v[20:23],a[28:31],v[44:47],v[20:23]
s_waitcnt lgkmcnt(2)
ds_read_b128 v[40:43],v2 offset:10240
v_and_b32 v71,0xffff0000,v87
v_mfma_f32_16x16x32_bf16 v[16:19],a[32:35],v[48:51],v[16:19]
s_cselect_b32 s87,s87,s63
ds_read_b128 v[44:47],v2 offset:11264
v_fma_f32 v64,v64,v91,v90
v_mfma_f32_16x16x32_bf16 v[20:23],a[36:39],v[48:51],v[20:23]
s_lshr_b32 s72,s66,6
v_fma_f32 v65,v65,v91,v90
v_fma_f32 v66,v66,v91,v90
v_mfma_f32_16x16x32_bf16 v[16:19],a[40:43],v[52:55],v[16:19]
buffer_load_dwordx4 v1,s[36:39],s66 offen offset:2048 sc1 lds
v_mfma_f32_16x16x32_bf16 v[20:23],a[44:47],v[52:55],v[20:23]
s_lshr_b32 s71,s62,2
ds_read_b128 v[48:51],v2 offset:12288
v_fma_f32 v67,v67,v91,v90
v_mfma_f32_16x16x32_bf16 v[16:19],a[48:51],v[56:59],v[16:19]
s_mov_b32 s62,s87
ds_read_b128 v[52:55],v2 offset:13312
v_fma_f32 v68,v68,v91,v90
v_mfma_f32_16x16x32_bf16 v[20:23],a[52:55],v[56:59],v[20:23]
v_fma_f32 v69,v69,v91,v90
v_fma_f32 v70,v70,v91,v90
v_mfma_f32_16x16x32_bf16 v[16:19],a[56:59],v[60:63],v[16:19]
buffer_load_dwordx4 v1,s[36:39],s66 offen offset:3072 sc1 lds
v_mfma_f32_16x16x32_bf16 v[20:23],a[60:63],v[60:63],v[20:23]
s_waitcnt lgkmcnt(2)
ds_read_b128 v[56:59],v2 offset:14336
v_fma_f32 v71,v71,v91,v90
v_mfma_f32_16x16x32_bf16 v[16:19],a[64:67],v[32:35],v[16:19]
s_waitcnt vmcnt(9)
ds_read_b128 v[60:63],v2 offset:15360
v_add_f32 v24,v24,v64
v_mfma_f32_16x16x32_bf16 v[20:23],a[68:71],v[32:35],v[20:23]
s_barrier
v_mfma_f32_16x16x32_bf16 v[16:19],a[72:75],v[36:39],v[16:19]
s_add_u32 m0,s71,s88
ds_read_b128 v[32:35],v2 offset:16384
v_add_f32 v25,v25,v65
v_mfma_f32_16x16x32_bf16 v[20:23],a[76:79],v[36:39],v[20:23]
v_add_f32 v26,v26,v66
v_add_f32 v27,v27,v67
v_mfma_f32_16x16x32_bf16 v[16:19],a[80:83],v[40:43],v[16:19]
buffer_load_dwordx4 v10,s[36:39],s66 offen offset:0 sc1 lds
v_mfma_f32_16x16x32_bf16 v[20:23],a[84:87],v[40:43],v[20:23]
ds_read_b128 v[36:39],v2 offset:17408
v_add_f32 v28,v28,v68
v_mfma_f32_16x16x32_bf16 v[16:19],a[88:91],v[44:47],v[16:19]
buffer_load_dword v11,s[36:39],s72 offen offset:1024 sc1 lds
v_mfma_f32_16x16x32_bf16 v[20:23],a[92:95],v[44:47],v[20:23]
s_waitcnt lgkmcnt(2)
ds_read_b128 v[40:43],v2 offset:18432
v_add_f32 v29,v29,v69
v_mfma_f32_16x16x32_bf16 v[16:19],a[96:99],v[48:51],v[16:19]
s_mov_b32 m0,s62
v_add_f32 v30,v30,v70
v_add_f32 v31,v31,v71
v_cvt_pk_bf16_f32 v72,v24,v25
v_cvt_pk_bf16_f32 v73,v26,v27
v_cvt_pk_bf16_f32 v74,v28,v29
v_cvt_pk_bf16_f32 v75,v30,v31
v_mfma_f32_16x16x32_bf16 v[20:23],a[100:103],v[48:51],v[20:23]
ds_read_b128 v[44:47],v2 offset:19456
buffer_store_dwordx4 v[72:75],v3,s[36:39],s73 offen offset:0 sc0 sc1
v_add_f32 v64,v24,v25
v_mul_f32 v65,v24,v24
v_fma_f32 v65,v25,v25,v65
v_mfma_f32_16x16x32_bf16 v[16:19],a[104:107],v[52:55],v[16:19]
ds_read_b128 v[48:51],v2 offset:20480
v_add_f32 v64,v64,v26
v_mfma_f32_16x16x32_bf16 v[20:23],a[108:111],v[52:55],v[20:23]
v_fma_f32 v65,v26,v26,v65
v_add_f32 v64,v64,v27
v_mfma_f32_16x16x32_bf16 v[16:19],a[112:115],v[56:59],v[16:19]
ds_read_b128 v[52:55],v2 offset:21504
v_fma_f32 v65,v27,v27,v65
v_mfma_f32_16x16x32_bf16 v[20:23],a[116:119],v[56:59],v[20:23]
v_add_f32 v64,v64,v28
v_fma_f32 v65,v28,v28,v65
v_add_f32 v64,v64,v29
v_fma_f32 v65,v29,v29,v65
v_add_f32 v64,v64,v30
v_fma_f32 v65,v30,v30,v65
v_add_f32 v64,v64,v31
v_fma_f32 v65,v31,v31,v65
v_mfma_f32_16x16x32_bf16 v[16:19],a[120:123],v[60:63],v[16:19]
s_nop 0
v_permlane16_swap_b32 v64,v65
v_add_f32 v64,v64,v65
v_mov_b32 v65,v64
v_mfma_f32_16x16x32_bf16 v[20:23],a[124:127],v[60:63],v[20:23]
s_waitcnt lgkmcnt(2)
ds_read_b128 v[56:59],v2 offset:22528
v_permlane32_swap_b32 v64,v65
v_add_f32 v64,v64,v65
v_mfma_f32_16x16x32_bf16 v[16:19],a[128:131],v[32:35],v[16:19]
ds_write_b32 v13,v64 offset:0
v_mfma_f32_16x16x32_bf16 v[20:23],a[132:135],v[32:35],v[20:23]
ds_read_b128 v[60:63],v2 offset:23552
v_mfma_f32_16x16x32_bf16 v[16:19],a[136:139],v[36:39],v[16:19]
ds_read_b128 v[32:35],v2 offset:24576
v_mfma_f32_16x16x32_bf16 v[20:23],a[140:143],v[36:39],v[20:23]
v_mfma_f32_16x16x32_bf16 v[16:19],a[144:147],v[40:43],v[16:19]
s_waitcnt vmcnt(11) lgkmcnt(2)
ds_read_b128 v[36:39],v2 offset:25600
v_mfma_f32_16x16x32_bf16 v[20:23],a[148:151],v[40:43],v[20:23]
s_barrier
v_mfma_f32_16x16x32_bf16 v[16:19],a[152:155],v[44:47],v[16:19]
ds_read_b128 v[40:43],v2 offset:26624
ds_read_b32 v15,v7 offset:0
v_mfma_f32_16x16x32_bf16 v[20:23],a[156:159],v[44:47],v[20:23]
s_waitcnt lgkmcnt(4)
v_mfma_f32_16x16x32_bf16 v[16:19],a[160:163],v[48:51],v[16:19]
ds_read_b128 v[44:47],v2 offset:27648
v_mfma_f32_16x16x32_bf16 v[20:23],a[164:167],v[48:51],v[20:23]
v_mfma_f32_16x16x32_bf16 v[16:19],a[168:171],v[52:55],v[16:19]
s_waitcnt lgkmcnt(1)
ds_read_b128 v[48:51],v2 offset:28672
v_mfma_f32_16x16x32_bf16 v[20:23],a[172:175],v[52:55],v[20:23]
v_cmp_eq_u32 vcc,s60,v15
v_mfma_f32_16x16x32_bf16 v[16:19],a[176:179],v[56:59],v[16:19]
s_cmp_eq_u32 vcc_lo,-1
ds_read_b128 v[52:55],v2 offset:29696
s_cselect_b32 s67,s86,s59
v_mfma_f32_16x16x32_bf16 v[20:23],a[180:183],v[56:59],v[20:23]
buffer_load_dwordx4 v0,s[36:39],s67 offen offset:0 sc1 lds
v_mfma_f32_16x16x32_bf16 v[16:19],a[184:187],v[60:63],v[16:19]
s_xor_b32 s63,s63,65536
ds_read_b128 v[56:59],v2 offset:30720
s_add_u32 s53,s52,s55
v_mfma_f32_16x16x32_bf16 v[20:23],a[188:191],v[60:63],v[20:23]
s_waitcnt lgkmcnt(3)
s_cmp_eq_u32 s53,s56
s_cselect_b32 s53,s57,s53
v_mfma_f32_16x16x32_bf16 v[16:19],a[192:195],v[32:35],v[16:19]
s_waitcnt vmcnt(8)
ds_read_b128 v[60:63],v2 offset:31744
s_cmp_eq_u32 s53,s58
v_mfma_f32_16x16x32_bf16 v[20:23],a[196:199],v[32:35],v[20:23]
s_barrier
v_mfma_f32_16x16x32_bf16 v[16:19],a[200:203],v[36:39],v[16:19]
s_cselect_b32 s53,s59,s53
ds_read_b128 v[32:35],v2 offset:32768
ds_read_b128 v[80:83],v8 offset:0
v_mfma_f32_16x16x32_bf16 v[20:23],a[204:207],v[36:39],v[20:23]
buffer_load_dwordx4 v0,s[36:39],s67 offen offset:1024 sc1 lds
v_mfma_f32_16x16x32_bf16 v[16:19],a[208:211],v[40:43],v[16:19]
s_cmp_ge_u32 s66,s59)"
R"(
ds_read_b128 v[36:39],v2 offset:33792
ds_read_b64 v[88:89],v9 offset:1024
v_mfma_f32_16x16x32_bf16 v[20:23],a[212:215],v[40:43],v[20:23]
v_mfma_f32_16x16x32_bf16 v[16:19],a[216:219],v[44:47],v[16:19]
ds_read_b128 v[40:43],v2 offset:34816
v_mfma_f32_16x16x32_bf16 v[20:23],a[220:223],v[44:47],v[20:23]
s_waitcnt lgkmcnt(5)
buffer_load_dwordx4 v0,s[36:39],s67 offen offset:2048 sc1 lds
v_mfma_f32_16x16x32_bf16 v[16:19],a[224:227],v[48:51],v[16:19]
ds_read_b128 v[44:47],v2 offset:35840
v_mfma_f32_16x16x32_bf16 v[20:23],a[228:231],v[48:51],v[20:23]
v_mfma_f32_16x16x32_bf16 v[16:19],a[232:235],v[52:55],v[16:19]
s_waitcnt lgkmcnt(2)
ds_read_b128 v[48:51],v2 offset:36864
v_mfma_f32_16x16x32_bf16 v[20:23],a[236:239],v[52:55],v[20:23]
buffer_load_dwordx4 v0,s[36:39],s67 offen offset:3072 sc1 lds
v_mfma_f32_16x16x32_bf16 v[16:19],a[240:243],v[56:59],v[16:19]
ds_read_b128 v[52:55],v2 offset:37888
v_mfma_f32_16x16x32_bf16 v[20:23],a[244:247],v[56:59],v[20:23]
v_mfma_f32_16x16x32_bf16 v[16:19],a[248:251],v[60:63],v[16:19]
s_waitcnt lgkmcnt(2)
ds_read_b128 v[56:59],v2 offset:38912
v_mfma_f32_16x16x32_bf16 v[20:23],a[252:255],v[60:63],v[20:23]
s_cbranch_scc1 Lg2_slow1_%=
Lg2_d1w1_%=:
v_mfma_f32_16x16x32_bf16 v[24:27],a[0:3],v[32:35],0
s_add_u32 m0,s62,16384
ds_read_b128 v[60:63],v2 offset:39936
v_lshlrev_b32 v64,16,v80
v_mfma_f32_16x16x32_bf16 v[28:31],a[4:7],v[32:35],0
s_lshr_b32 s73,s48,0
v_and_b32 v65,0xffff0000,v80
v_lshlrev_b32 v66,16,v81
v_mfma_f32_16x16x32_bf16 v[24:27],a[8:11],v[36:39],v[24:27]
buffer_load_dwordx4 v1,s[36:39],s67 offen offset:0 sc1 lds
v_mfma_f32_16x16x32_bf16 v[28:31],a[12:15],v[36:39],v[28:31]
s_xor_b32 s87,s63,65536
ds_read_b128 v[32:35],v2 offset:40960
v_and_b32 v67,0xffff0000,v81
v_mfma_f32_16x16x32_bf16 v[24:27],a[16:19],v[40:43],v[24:27]
s_cmp_ge_u32 s67,s59
ds_read_b128 v[36:39],v2 offset:41984
v_lshlrev_b32 v68,16,v82
v_mfma_f32_16x16x32_bf16 v[28:31],a[20:23],v[40:43],v[28:31]
s_cselect_b32 s86,s50,s51
v_and_b32 v69,0xffff0000,v82
v_lshlrev_b32 v70,16,v83
v_mfma_f32_16x16x32_bf16 v[24:27],a[24:27],v[44:47],v[24:27]
buffer_load_dwordx4 v1,s[36:39],s67 offen offset:1024 sc1 lds
v_mfma_f32_16x16x32_bf16 v[28:31],a[28:31],v[44:47],v[28:31]
s_waitcnt lgkmcnt(2)
ds_read_b128 v[40:43],v2 offset:43008
v_and_b32 v71,0xffff0000,v83
v_mfma_f32_16x16x32_bf16 v[24:27],a[32:35],v[48:51],v[24:27]
s_cselect_b32 s87,s87,s64
ds_read_b128 v[44:47],v2 offset:44032
v_fma_f32 v64,v64,v89,v88
v_mfma_f32_16x16x32_bf16 v[28:31],a[36:39],v[48:51],v[28:31]
s_lshr_b32 s72,s67,6
v_fma_f32 v65,v65,v89,v88
v_fma_f32 v66,v66,v89,v88
v_mfma_f32_16x16x32_bf16 v[24:27],a[40:43],v[52:55],v[24:27]
buffer_load_dwordx4 v1,s[36:39],s67 offen offset:2048 sc1 lds
v_mfma_f32_16x16x32_bf16 v[28:31],a[44:47],v[52:55],v[28:31]
s_lshr_b32 s71,s62,2
ds_read_b128 v[48:51],v2 offset:45056
v_fma_f32 v67,v67,v89,v88
v_mfma_f32_16x16x32_bf16 v[24:27],a[48:51],v[56:59],v[24:27]
s_mov_b32 s62,s87
ds_read_b128 v[52:55],v2 offset:46080
v_fma_f32 v68,v68,v89,v88
v_mfma_f32_16x16x32_bf16 v[28:31],a[52:55],v[56:59],v[28:31]
s_mov_b64 s[44:45],s[46:47]
v_fma_f32 v69,v69,v89,v88
v_fma_f32 v70,v70,v89,v88
v_mfma_f32_16x16x32_bf16 v[24:27],a[56:59],v[60:63],v[24:27]
buffer_load_dwordx4 v1,s[36:39],s67 offen offset:3072 sc1 lds
v_mfma_f32_16x16x32_bf16 v[28:31],a[60:63],v[60:63],v[28:31]
s_waitcnt lgkmcnt(2)
ds_read_b128 v[56:59],v2 offset:47104
v_fma_f32 v71,v71,v89,v88
v_mfma_f32_16x16x32_bf16 v[24:27],a[64:67],v[32:35],v[24:27]
s_waitcnt vmcnt(9)
ds_read_b128 v[60:63],v2 offset:48128
v_add_f32 v16,v16,v64
v_mfma_f32_16x16x32_bf16 v[28:31],a[68:71],v[32:35],v[28:31]
s_barrier
v_mfma_f32_16x16x32_bf16 v[24:27],a[72:75],v[36:39],v[24:27]
s_add_u32 m0,s71,s88
ds_read_b128 v[32:35],v2 offset:49152
v_add_f32 v17,v17,v65
v_mfma_f32_16x16x32_bf16 v[28:31],a[76:79],v[36:39],v[28:31]
s_mov_b64 s[46:47],s[48:49]
v_add_f32 v18,v18,v66
v_add_f32 v19,v19,v67
v_mfma_f32_16x16x32_bf16 v[24:27],a[80:83],v[40:43],v[24:27]
buffer_load_dwordx4 v10,s[36:39],s67 offen offset:0 sc1 lds
v_mfma_f32_16x16x32_bf16 v[28:31],a[84:87],v[40:43],v[28:31]
s_mov_b64 s[48:49],s[50:51]
ds_read_b128 v[36:39],v2 offset:50176
v_add_f32 v20,v20,v68
v_mfma_f32_16x16x32_bf16 v[24:27],a[88:91],v[44:47],v[24:27]
buffer_load_dword v11,s[36:39],s72 offen offset:1024 sc1 lds
v_mfma_f32_16x16x32_bf16 v[28:31],a[92:95],v[44:47],v[28:31]
s_waitcnt lgkmcnt(2)
ds_read_b128 v[40:43],v2 offset:51200
v_add_f32 v21,v21,v69
v_mfma_f32_16x16x32_bf16 v[24:27],a[96:99],v[48:51],v[24:27]
s_mov_b32 m0,s62
v_add_f32 v22,v22,v70
v_add_f32 v23,v23,v71
v_cvt_pk_bf16_f32 v72,v16,v17
v_cvt_pk_bf16_f32 v73,v18,v19
v_cvt_pk_bf16_f32 v74,v20,v21
v_cvt_pk_bf16_f32 v75,v22,v23
v_mfma_f32_16x16x32_bf16 v[28:31],a[100:103],v[48:51],v[28:31]
s_mov_b64 s[50:51],s[52:53]
ds_read_b128 v[44:47],v2 offset:52224
buffer_store_dwordx4 v[72:75],v3,s[36:39],s73 offen offset:0 sc0 sc1
v_add_f32 v64,v16,v17
v_mul_f32 v65,v16,v16
v_fma_f32 v65,v17,v17,v65
v_mfma_f32_16x16x32_bf16 v[24:27],a[104:107],v[52:55],v[24:27]
ds_read_b128 v[48:51],v2 offset:53248
v_add_f32 v64,v64,v18
v_mfma_f32_16x16x32_bf16 v[28:31],a[108:111],v[52:55],v[28:31]
v_fma_f32 v65,v18,v18,v65
v_add_f32 v64,v64,v19
v_mfma_f32_16x16x32_bf16 v[24:27],a[112:115],v[56:59],v[24:27]
ds_read_b128 v[52:55],v2 offset:54272
v_fma_f32 v65,v19,v19,v65
v_mfma_f32_16x16x32_bf16 v[28:31],a[116:119],v[56:59],v[28:31]
v_add_f32 v64,v64,v20
v_fma_f32 v65,v20,v20,v65
v_add_f32 v64,v64,v21
v_fma_f32 v65,v21,v21,v65
v_add_f32 v64,v64,v22
v_fma_f32 v65,v22,v22,v65
v_add_f32 v64,v64,v23
v_fma_f32 v65,v23,v23,v65
v_mfma_f32_16x16x32_bf16 v[24:27],a[120:123],v[60:63],v[24:27]
s_nop 0
v_permlane16_swap_b32 v64,v65
v_add_f32 v64,v64,v65
v_mov_b32 v65,v64
v_mfma_f32_16x16x32_bf16 v[28:31],a[124:127],v[60:63],v[28:31]
s_waitcnt lgkmcnt(2)
ds_read_b128 v[56:59],v2 offset:55296
v_permlane32_swap_b32 v64,v65
v_add_f32 v64,v64,v65
v_mfma_f32_16x16x32_bf16 v[24:27],a[128:131],v[32:35],v[24:27]
ds_write_b32 v13,v64 offset:2048
v_mfma_f32_16x16x32_bf16 v[28:31],a[132:135],v[32:35],v[28:31]
ds_read_b128 v[60:63],v2 offset:56320
v_mfma_f32_16x16x32_bf16 v[24:27],a[136:139],v[36:39],v[24:27]
ds_read_b128 v[32:35],v2 offset:57344
v_mfma_f32_16x16x32_bf16 v[28:31],a[140:143],v[36:39],v[28:31]
v_mfma_f32_16x16x32_bf16 v[24:27],a[144:147],v[40:43],v[24:27]
s_waitcnt vmcnt(11) lgkmcnt(2)
ds_read_b128 v[36:39],v2 offset:58368
v_mfma_f32_16x16x32_bf16 v[28:31],a[148:151],v[40:43],v[28:31]
s_barrier
v_mfma_f32_16x16x32_bf16 v[24:27],a[152:155],v[44:47],v[24:27]
ds_read_b128 v[40:43],v2 offset:59392
ds_read_b32 v15,v7 offset:256
v_mfma_f32_16x16x32_bf16 v[28:31],a[156:159],v[44:47],v[28:31]
s_waitcnt lgkmcnt(4)
v_mfma_f32_16x16x32_bf16 v[24:27],a[160:163],v[48:51],v[24:27]
ds_read_b128 v[44:47],v2 offset:60416
v_mfma_f32_16x16x32_bf16 v[28:31],a[164:167],v[48:51],v[28:31]
v_mfma_f32_16x16x32_bf16 v[24:27],a[168:171],v[52:55],v[24:27]
s_waitcnt lgkmcnt(1)
ds_read_b128 v[48:51],v2 offset:61440
v_mfma_f32_16x16x32_bf16 v[28:31],a[172:175],v[52:55],v[28:31]
v_cmp_eq_u32 vcc,s60,v15
v_mfma_f32_16x16x32_bf16 v[24:27],a[176:179],v[56:59],v[24:27]
s_cmp_eq_u32 vcc_lo,-1
ds_read_b128 v[52:55],v2 offset:62464
s_cselect_b32 s66,s86,s59
v_mfma_f32_16x16x32_bf16 v[28:31],a[180:183],v[56:59],v[28:31]
buffer_load_dwordx4 v0,s[36:39],s66 offen offset:0 sc1 lds
v_mfma_f32_16x16x32_bf16 v[24:27],a[184:187],v[60:63],v[24:27]
s_xor_b32 s64,s64,65536
ds_read_b128 v[56:59],v2 offset:63488
s_add_u32 s74,s53,s55
v_mfma_f32_16x16x32_bf16 v[28:31],a[188:191],v[60:63],v[28:31]
s_waitcnt lgkmcnt(3)
s_cmp_eq_u32 s74,s56
s_cselect_b32 s74,s57,s74
v_mfma_f32_16x16x32_bf16 v[24:27],a[192:195],v[32:35],v[24:27]
s_waitcnt vmcnt(8)
ds_read_b128 v[60:63],v2 offset:64512
v_xor_b32 v2,65536,v2
v_mfma_f32_16x16x32_bf16 v[28:31],a[196:199],v[32:35],v[28:31]
s_barrier
v_mfma_f32_16x16x32_bf16 v[24:27],a[200:203],v[36:39],v[24:27]
s_cmp_eq_u32 s74,s58
ds_read_b128 v[32:35],v2 offset:0
ds_read_b128 v[84:87],v8 offset:8192
v_mfma_f32_16x16x32_bf16 v[28:31],a[204:207],v[36:39],v[28:31])"
R"(
buffer_load_dwordx4 v0,s[36:39],s66 offen offset:1024 sc1 lds
v_mfma_f32_16x16x32_bf16 v[24:27],a[208:211],v[40:43],v[24:27]
s_cselect_b32 s74,s59,s74
ds_read_b128 v[36:39],v2 offset:1024
ds_read_b64 v[90:91],v9 offset:9216
v_mfma_f32_16x16x32_bf16 v[28:31],a[212:215],v[40:43],v[28:31]
s_mov_b32 s52,s74
v_xor_b32 v8,16384,v8
v_xor_b32 v9,16384,v9
v_mfma_f32_16x16x32_bf16 v[24:27],a[216:219],v[44:47],v[24:27]
s_cmp_lt_u32 s67,s59
ds_read_b128 v[40:43],v2 offset:2048
v_mfma_f32_16x16x32_bf16 v[28:31],a[220:223],v[44:47],v[28:31]
s_waitcnt lgkmcnt(5)
buffer_load_dwordx4 v0,s[36:39],s66 offen offset:2048 sc1 lds
v_mfma_f32_16x16x32_bf16 v[24:27],a[224:227],v[48:51],v[24:27]
ds_read_b128 v[44:47],v2 offset:3072
v_mfma_f32_16x16x32_bf16 v[28:31],a[228:231],v[48:51],v[28:31]
v_mfma_f32_16x16x32_bf16 v[24:27],a[232:235],v[52:55],v[24:27]
s_waitcnt lgkmcnt(2)
ds_read_b128 v[48:51],v2 offset:4096
v_mfma_f32_16x16x32_bf16 v[28:31],a[236:239],v[52:55],v[28:31]
buffer_load_dwordx4 v0,s[36:39],s66 offen offset:3072 sc1 lds
v_mfma_f32_16x16x32_bf16 v[24:27],a[240:243],v[56:59],v[24:27]
ds_read_b128 v[52:55],v2 offset:5120
v_mfma_f32_16x16x32_bf16 v[28:31],a[244:247],v[56:59],v[28:31]
v_mfma_f32_16x16x32_bf16 v[24:27],a[248:251],v[60:63],v[24:27]
s_waitcnt lgkmcnt(2)
ds_read_b128 v[56:59],v2 offset:6144
v_mfma_f32_16x16x32_bf16 v[28:31],a[252:255],v[60:63],v[28:31]
s_cbranch_scc1 Lg2_d0w1_%=
s_branch Lg2_slow0_%=
Lg2_d0w2_%=:
v_mfma_f32_16x16x32_bf16 v[16:19],a[0:3],v[32:35],0
s_add_u32 m0,s62,16384
ds_read_b128 v[60:63],v2 offset:7168
v_lshlrev_b32 v64,16,v84
v_mfma_f32_16x16x32_bf16 v[20:23],a[4:7],v[32:35],0
s_lshr_b32 s73,s47,0
v_and_b32 v65,0xffff0000,v84
v_lshlrev_b32 v66,16,v85
v_mfma_f32_16x16x32_bf16 v[16:19],a[8:11],v[36:39],v[16:19]
s_lshr_b32 s85,s47,5
ds_read_b128 v[32:35],v2 offset:8192
v_and_b32 v67,0xffff0000,v85
v_mfma_f32_16x16x32_bf16 v[20:23],a[12:15],v[36:39],v[20:23]
buffer_load_dwordx4 v1,s[36:39],s66 offen offset:0 sc1 lds
v_mfma_f32_16x16x32_bf16 v[16:19],a[16:19],v[40:43],v[16:19]
s_xor_b32 s87,s64,65536
ds_read_b128 v[36:39],v2 offset:9216
v_lshlrev_b32 v68,16,v86
v_mfma_f32_16x16x32_bf16 v[20:23],a[20:23],v[40:43],v[20:23]
s_cmp_ge_u32 s66,s59
v_and_b32 v69,0xffff0000,v86
v_lshlrev_b32 v70,16,v87
v_mfma_f32_16x16x32_bf16 v[16:19],a[24:27],v[44:47],v[16:19]
s_cselect_b32 s86,s49,s50
ds_read_b128 v[40:43],v2 offset:10240
v_and_b32 v71,0xffff0000,v87
v_mfma_f32_16x16x32_bf16 v[20:23],a[28:31],v[44:47],v[20:23]
s_waitcnt lgkmcnt(3)
buffer_load_dwordx4 v1,s[36:39],s66 offen offset:1024 sc1 lds
v_mfma_f32_16x16x32_bf16 v[16:19],a[32:35],v[48:51],v[16:19]
s_cselect_b32 s87,s87,s63
ds_read_b128 v[44:47],v2 offset:11264
v_fma_f32 v64,v64,v91,v90
v_mfma_f32_16x16x32_bf16 v[20:23],a[36:39],v[48:51],v[20:23]
s_lshr_b32 s72,s66,6
v_fma_f32 v65,v65,v91,v90
v_fma_f32 v66,v66,v91,v90
v_mfma_f32_16x16x32_bf16 v[16:19],a[40:43],v[52:55],v[16:19]
s_lshr_b32 s71,s62,2
ds_read_b128 v[48:51],v2 offset:12288
v_fma_f32 v67,v67,v91,v90
v_mfma_f32_16x16x32_bf16 v[20:23],a[44:47],v[52:55],v[20:23]
buffer_load_dwordx4 v1,s[36:39],s66 offen offset:2048 sc1 lds
v_mfma_f32_16x16x32_bf16 v[16:19],a[48:51],v[56:59],v[16:19]
s_mov_b32 s62,s87
ds_read_b128 v[52:55],v2 offset:13312
v_fma_f32 v68,v68,v91,v90
v_mfma_f32_16x16x32_bf16 v[20:23],a[52:55],v[56:59],v[20:23]
v_fma_f32 v69,v69,v91,v90
v_fma_f32 v70,v70,v91,v90
v_mfma_f32_16x16x32_bf16 v[16:19],a[56:59],v[60:63],v[16:19]
ds_read_b128 v[56:59],v2 offset:14336
v_fma_f32 v71,v71,v91,v90
v_mfma_f32_16x16x32_bf16 v[20:23],a[60:63],v[60:63],v[20:23]
s_waitcnt lgkmcnt(3)
buffer_load_dwordx4 v1,s[36:39],s66 offen offset:3072 sc1 lds
v_mfma_f32_16x16x32_bf16 v[16:19],a[64:67],v[32:35],v[16:19]
s_waitcnt vmcnt(10)
ds_read_b128 v[60:63],v2 offset:15360
v_add_f32 v24,v24,v64
v_mfma_f32_16x16x32_bf16 v[20:23],a[68:71],v[32:35],v[20:23]
s_barrier
v_mfma_f32_16x16x32_bf16 v[16:19],a[72:75],v[36:39],v[16:19]
s_add_u32 m0,s71,s88
ds_read_b128 v[32:35],v2 offset:16384
v_add_f32 v25,v25,v65
v_mfma_f32_16x16x32_bf16 v[20:23],a[76:79],v[36:39],v[20:23]
v_add_f32 v26,v26,v66
v_add_f32 v27,v27,v67
v_mfma_f32_16x16x32_bf16 v[16:19],a[80:83],v[40:43],v[16:19]
ds_read_b128 v[36:39],v2 offset:17408
v_add_f32 v28,v28,v68
v_mfma_f32_16x16x32_bf16 v[20:23],a[84:87],v[40:43],v[20:23]
buffer_load_dwordx4 v10,s[36:39],s66 offen offset:0 sc1 lds
v_mfma_f32_16x16x32_bf16 v[16:19],a[88:91],v[44:47],v[16:19]
buffer_load_dword v11,s[36:39],s72 offen offset:1024 sc1 lds
v_mfma_f32_16x16x32_bf16 v[20:23],a[92:95],v[44:47],v[20:23]
s_waitcnt lgkmcnt(2)
ds_read_b128 v[40:43],v2 offset:18432
v_add_f32 v29,v29,v69
v_mfma_f32_16x16x32_bf16 v[16:19],a[96:99],v[48:51],v[16:19]
s_mov_b32 m0,s62
ds_read_b128 v[44:47],v2 offset:19456
v_add_f32 v30,v30,v70
v_mfma_f32_16x16x32_bf16 v[20:23],a[100:103],v[48:51],v[20:23]
v_add_f32 v31,v31,v71
v_cvt_pk_bf16_f32 v72,v24,v25
v_cvt_pk_bf16_f32 v73,v26,v27
v_cvt_pk_bf16_f32 v74,v28,v29
v_cvt_pk_bf16_f32 v75,v30,v31
v_mfma_f32_16x16x32_bf16 v[16:19],a[104:107],v[52:55],v[16:19]
buffer_store_dwordx4 v[72:75],v3,s[36:39],s73 offen offset:0 sc0 sc1
ds_read_b128 v[48:51],v2 offset:20480
v_add_f32 v64,v24,v25
v_mul_f32 v65,v24,v24
v_mfma_f32_16x16x32_bf16 v[20:23],a[108:111],v[52:55],v[20:23]
v_fma_f32 v65,v25,v25,v65
v_add_f32 v64,v64,v26
v_mfma_f32_16x16x32_bf16 v[16:19],a[112:115],v[56:59],v[16:19]
ds_read_b128 v[52:55],v2 offset:21504
v_fma_f32 v65,v26,v26,v65
v_mfma_f32_16x16x32_bf16 v[20:23],a[116:119],v[56:59],v[20:23]
v_add_f32 v64,v64,v27
v_fma_f32 v65,v27,v27,v65
v_add_f32 v64,v64,v28
v_fma_f32 v65,v28,v28,v65
v_add_f32 v64,v64,v29
v_fma_f32 v65,v29,v29,v65
v_add_f32 v64,v64,v30
v_fma_f32 v65,v30,v30,v65
v_add_f32 v64,v64,v31
v_fma_f32 v65,v31,v31,v65
v_mfma_f32_16x16x32_bf16 v[16:19],a[120:123],v[60:63],v[16:19]
s_nop 0
v_permlane16_swap_b32 v64,v65
v_add_f32 v64,v64,v65
v_mov_b32 v65,v64
v_mfma_f32_16x16x32_bf16 v[20:23],a[124:127],v[60:63],v[20:23]
s_waitcnt lgkmcnt(2)
ds_read_b128 v[56:59],v2 offset:22528
v_permlane32_swap_b32 v64,v65
v_add_f32 v64,v64,v65
v_mfma_f32_16x16x32_bf16 v[16:19],a[128:131],v[32:35],v[16:19]
ds_write_b32 v13,v64 offset:0
v_mfma_f32_16x16x32_bf16 v[20:23],a[132:135],v[32:35],v[20:23]
ds_read_b128 v[60:63],v2 offset:23552
v_mfma_f32_16x16x32_bf16 v[16:19],a[136:139],v[36:39],v[16:19]
ds_read_b128 v[32:35],v2 offset:24576
v_mfma_f32_16x16x32_bf16 v[20:23],a[140:143],v[36:39],v[20:23]
v_mfma_f32_16x16x32_bf16 v[16:19],a[144:147],v[40:43],v[16:19]
s_waitcnt vmcnt(11) lgkmcnt(2)
ds_read_b128 v[36:39],v2 offset:25600
v_mfma_f32_16x16x32_bf16 v[20:23],a[148:151],v[40:43],v[20:23]
s_barrier
v_mfma_f32_16x16x32_bf16 v[16:19],a[152:155],v[44:47],v[16:19]
ds_read_b128 v[40:43],v2 offset:26624
ds_read_b32 v15,v7 offset:0
v_mfma_f32_16x16x32_bf16 v[20:23],a[156:159],v[44:47],v[20:23]
s_waitcnt lgkmcnt(4)
ds_read_b128 v[76:79],v14 offset:0
v_mfma_f32_16x16x32_bf16 v[16:19],a[160:163],v[48:51],v[16:19]
ds_read_b128 v[44:47],v2 offset:27648
v_mfma_f32_16x16x32_bf16 v[20:23],a[164:167],v[48:51],v[20:23]
v_mfma_f32_16x16x32_bf16 v[16:19],a[168:171],v[52:55],v[16:19]
s_waitcnt lgkmcnt(2)
ds_read_b128 v[48:51],v2 offset:28672
v_mfma_f32_16x16x32_bf16 v[20:23],a[172:175],v[52:55],v[20:23]
s_waitcnt lgkmcnt(2)
v_cmp_eq_u32 vcc,s60,v15
v_add_f32 v76,v76,v77
v_add_f32 v78,v78,v79
v_add_f32 v76,v76,v78
v_mfma_f32_16x16x32_bf16 v[16:19],a[176:179],v[56:59],v[16:19]
buffer_store_dword v76,v12,s[36:39],s85 offen offset:0 sc0 sc1
ds_read_b128 v[52:55],v2 offset:29696
v_mfma_f32_16x16x32_bf16 v[20:23],a[180:183],v[56:59],v[20:23]
s_cmp_eq_u32 vcc_lo,-1
s_cselect_b32 s67,s86,s59
s_xor_b32 s63,s63,65536
v_mfma_f32_16x16x32_bf16 v[16:19],a[184:187],v[60:63],v[16:19]
buffer_load_dwordx4 v0,s[36:39],s67 offen offset:0 sc1 lds
v_mfma_f32_16x16x32_bf16 v[20:23],a[188:191],v[60:63],v[20:23]
s_waitcnt lgkmcnt(2)
ds_read_b128 v[56:59],v2 offset:30720
s_add_u32 s53,s52,s55
v_mfma_f32_16x16x32_bf16 v[16:19],a[192:195],v[32:35],v[16:19]
s_waitcnt vmcnt(9))"
R"(
ds_read_b128 v[60:63],v2 offset:31744
s_cmp_eq_u32 s53,s56
v_mfma_f32_16x16x32_bf16 v[20:23],a[196:199],v[32:35],v[20:23]
s_barrier
v_mfma_f32_16x16x32_bf16 v[16:19],a[200:203],v[36:39],v[16:19]
s_cselect_b32 s53,s57,s53
ds_read_b128 v[32:35],v2 offset:32768
ds_read_b128 v[80:83],v8 offset:0
v_mfma_f32_16x16x32_bf16 v[20:23],a[204:207],v[36:39],v[20:23]
s_cmp_eq_u32 s53,s58
ds_read_b64 v[88:89],v9 offset:1024
s_cselect_b32 s53,s59,s53
v_mfma_f32_16x16x32_bf16 v[16:19],a[208:211],v[40:43],v[16:19]
buffer_load_dwordx4 v0,s[36:39],s67 offen offset:1024 sc1 lds
v_mfma_f32_16x16x32_bf16 v[20:23],a[212:215],v[40:43],v[20:23]
s_cmp_ge_u32 s66,s59
ds_read_b128 v[36:39],v2 offset:33792
v_mfma_f32_16x16x32_bf16 v[16:19],a[216:219],v[44:47],v[16:19]
ds_read_b128 v[40:43],v2 offset:34816
v_mfma_f32_16x16x32_bf16 v[20:23],a[220:223],v[44:47],v[20:23]
s_waitcnt lgkmcnt(5)
v_mfma_f32_16x16x32_bf16 v[16:19],a[224:227],v[48:51],v[16:19]
buffer_load_dwordx4 v0,s[36:39],s67 offen offset:2048 sc1 lds
v_mfma_f32_16x16x32_bf16 v[20:23],a[228:231],v[48:51],v[20:23]
s_waitcnt lgkmcnt(2)
ds_read_b128 v[44:47],v2 offset:35840
v_mfma_f32_16x16x32_bf16 v[16:19],a[232:235],v[52:55],v[16:19]
ds_read_b128 v[48:51],v2 offset:36864
v_mfma_f32_16x16x32_bf16 v[20:23],a[236:239],v[52:55],v[20:23]
v_mfma_f32_16x16x32_bf16 v[16:19],a[240:243],v[56:59],v[16:19]
buffer_load_dwordx4 v0,s[36:39],s67 offen offset:3072 sc1 lds
v_mfma_f32_16x16x32_bf16 v[20:23],a[244:247],v[56:59],v[20:23]
ds_read_b128 v[52:55],v2 offset:37888
v_mfma_f32_16x16x32_bf16 v[16:19],a[248:251],v[60:63],v[16:19]
s_waitcnt lgkmcnt(2)
ds_read_b128 v[56:59],v2 offset:38912
v_mfma_f32_16x16x32_bf16 v[20:23],a[252:255],v[60:63],v[20:23]
s_cbranch_scc1 Lg2_slow1_%=
Lg2_d1w2_%=:
v_mfma_f32_16x16x32_bf16 v[24:27],a[0:3],v[32:35],0
s_add_u32 m0,s62,16384
ds_read_b128 v[60:63],v2 offset:39936
v_lshlrev_b32 v64,16,v80
v_mfma_f32_16x16x32_bf16 v[28:31],a[4:7],v[32:35],0
s_lshr_b32 s73,s48,0
v_and_b32 v65,0xffff0000,v80
v_lshlrev_b32 v66,16,v81
v_mfma_f32_16x16x32_bf16 v[24:27],a[8:11],v[36:39],v[24:27]
s_lshr_b32 s85,s48,5
ds_read_b128 v[32:35],v2 offset:40960
v_and_b32 v67,0xffff0000,v81
v_mfma_f32_16x16x32_bf16 v[28:31],a[12:15],v[36:39],v[28:31]
buffer_load_dwordx4 v1,s[36:39],s67 offen offset:0 sc1 lds
v_mfma_f32_16x16x32_bf16 v[24:27],a[16:19],v[40:43],v[24:27]
s_xor_b32 s87,s63,65536
ds_read_b128 v[36:39],v2 offset:41984
v_lshlrev_b32 v68,16,v82
v_mfma_f32_16x16x32_bf16 v[28:31],a[20:23],v[40:43],v[28:31]
s_cmp_ge_u32 s67,s59
v_and_b32 v69,0xffff0000,v82
v_lshlrev_b32 v70,16,v83
v_mfma_f32_16x16x32_bf16 v[24:27],a[24:27],v[44:47],v[24:27]
s_cselect_b32 s86,s50,s51
ds_read_b128 v[40:43],v2 offset:43008
v_and_b32 v71,0xffff0000,v83
v_mfma_f32_16x16x32_bf16 v[28:31],a[28:31],v[44:47],v[28:31]
s_waitcnt lgkmcnt(3)
buffer_load_dwordx4 v1,s[36:39],s67 offen offset:1024 sc1 lds
v_mfma_f32_16x16x32_bf16 v[24:27],a[32:35],v[48:51],v[24:27]
s_cselect_b32 s87,s87,s64
ds_read_b128 v[44:47],v2 offset:44032
v_fma_f32 v64,v64,v89,v88
v_mfma_f32_16x16x32_bf16 v[28:31],a[36:39],v[48:51],v[28:31]
s_lshr_b32 s72,s67,6
v_fma_f32 v65,v65,v89,v88
v_fma_f32 v66,v66,v89,v88
v_mfma_f32_16x16x32_bf16 v[24:27],a[40:43],v[52:55],v[24:27]
s_lshr_b32 s71,s62,2
ds_read_b128 v[48:51],v2 offset:45056
v_fma_f32 v67,v67,v89,v88
v_mfma_f32_16x16x32_bf16 v[28:31],a[44:47],v[52:55],v[28:31]
buffer_load_dwordx4 v1,s[36:39],s67 offen offset:2048 sc1 lds
v_mfma_f32_16x16x32_bf16 v[24:27],a[48:51],v[56:59],v[24:27]
s_mov_b32 s62,s87
ds_read_b128 v[52:55],v2 offset:46080
v_fma_f32 v68,v68,v89,v88
v_mfma_f32_16x16x32_bf16 v[28:31],a[52:55],v[56:59],v[28:31]
s_mov_b64 s[44:45],s[46:47]
v_fma_f32 v69,v69,v89,v88
v_fma_f32 v70,v70,v89,v88
v_mfma_f32_16x16x32_bf16 v[24:27],a[56:59],v[60:63],v[24:27]
s_mov_b64 s[46:47],s[48:49]
ds_read_b128 v[56:59],v2 offset:47104
v_fma_f32 v71,v71,v89,v88
v_mfma_f32_16x16x32_bf16 v[28:31],a[60:63],v[60:63],v[28:31]
s_waitcnt lgkmcnt(3)
buffer_load_dwordx4 v1,s[36:39],s67 offen offset:3072 sc1 lds
v_mfma_f32_16x16x32_bf16 v[24:27],a[64:67],v[32:35],v[24:27]
s_waitcnt vmcnt(10)
ds_read_b128 v[60:63],v2 offset:48128
v_add_f32 v16,v16,v64
v_mfma_f32_16x16x32_bf16 v[28:31],a[68:71],v[32:35],v[28:31]
s_barrier
v_mfma_f32_16x16x32_bf16 v[24:27],a[72:75],v[36:39],v[24:27]
s_add_u32 m0,s71,s88
ds_read_b128 v[32:35],v2 offset:49152
v_add_f32 v17,v17,v65
v_mfma_f32_16x16x32_bf16 v[28:31],a[76:79],v[36:39],v[28:31]
s_mov_b64 s[48:49],s[50:51]
v_add_f32 v18,v18,v66
v_add_f32 v19,v19,v67
v_mfma_f32_16x16x32_bf16 v[24:27],a[80:83],v[40:43],v[24:27]
s_mov_b64 s[50:51],s[52:53]
ds_read_b128 v[36:39],v2 offset:50176
v_add_f32 v20,v20,v68
v_mfma_f32_16x16x32_bf16 v[28:31],a[84:87],v[40:43],v[28:31]
buffer_load_dwordx4 v10,s[36:39],s67 offen offset:0 sc1 lds
v_mfma_f32_16x16x32_bf16 v[24:27],a[88:91],v[44:47],v[24:27]
buffer_load_dword v11,s[36:39],s72 offen offset:1024 sc1 lds
v_mfma_f32_16x16x32_bf16 v[28:31],a[92:95],v[44:47],v[28:31]
s_waitcnt lgkmcnt(2)
ds_read_b128 v[40:43],v2 offset:51200
v_add_f32 v21,v21,v69
v_mfma_f32_16x16x32_bf16 v[24:27],a[96:99],v[48:51],v[24:27]
s_mov_b32 m0,s62
ds_read_b128 v[44:47],v2 offset:52224
v_add_f32 v22,v22,v70
v_mfma_f32_16x16x32_bf16 v[28:31],a[100:103],v[48:51],v[28:31]
v_add_f32 v23,v23,v71
v_cvt_pk_bf16_f32 v72,v16,v17
v_cvt_pk_bf16_f32 v73,v18,v19
v_cvt_pk_bf16_f32 v74,v20,v21
v_cvt_pk_bf16_f32 v75,v22,v23
v_mfma_f32_16x16x32_bf16 v[24:27],a[104:107],v[52:55],v[24:27]
buffer_store_dwordx4 v[72:75],v3,s[36:39],s73 offen offset:0 sc0 sc1
ds_read_b128 v[48:51],v2 offset:53248
v_add_f32 v64,v16,v17
v_mul_f32 v65,v16,v16
v_mfma_f32_16x16x32_bf16 v[28:31],a[108:111],v[52:55],v[28:31]
v_fma_f32 v65,v17,v17,v65
v_add_f32 v64,v64,v18
v_mfma_f32_16x16x32_bf16 v[24:27],a[112:115],v[56:59],v[24:27]
ds_read_b128 v[52:55],v2 offset:54272
v_fma_f32 v65,v18,v18,v65
v_mfma_f32_16x16x32_bf16 v[28:31],a[116:119],v[56:59],v[28:31]
v_add_f32 v64,v64,v19
v_fma_f32 v65,v19,v19,v65
v_add_f32 v64,v64,v20
v_fma_f32 v65,v20,v20,v65
v_add_f32 v64,v64,v21
v_fma_f32 v65,v21,v21,v65
v_add_f32 v64,v64,v22
v_fma_f32 v65,v22,v22,v65
v_add_f32 v64,v64,v23
v_fma_f32 v65,v23,v23,v65
v_mfma_f32_16x16x32_bf16 v[24:27],a[120:123],v[60:63],v[24:27]
s_nop 0
v_permlane16_swap_b32 v64,v65
v_add_f32 v64,v64,v65
v_mov_b32 v65,v64
v_mfma_f32_16x16x32_bf16 v[28:31],a[124:127],v[60:63],v[28:31]
s_waitcnt lgkmcnt(2)
ds_read_b128 v[56:59],v2 offset:55296
v_permlane32_swap_b32 v64,v65
v_add_f32 v64,v64,v65
v_mfma_f32_16x16x32_bf16 v[24:27],a[128:131],v[32:35],v[24:27]
ds_write_b32 v13,v64 offset:2048
v_mfma_f32_16x16x32_bf16 v[28:31],a[132:135],v[32:35],v[28:31]
ds_read_b128 v[60:63],v2 offset:56320
v_mfma_f32_16x16x32_bf16 v[24:27],a[136:139],v[36:39],v[24:27]
ds_read_b128 v[32:35],v2 offset:57344
v_mfma_f32_16x16x32_bf16 v[28:31],a[140:143],v[36:39],v[28:31]
v_mfma_f32_16x16x32_bf16 v[24:27],a[144:147],v[40:43],v[24:27]
s_waitcnt vmcnt(11) lgkmcnt(2)
ds_read_b128 v[36:39],v2 offset:58368
v_mfma_f32_16x16x32_bf16 v[28:31],a[148:151],v[40:43],v[28:31]
s_barrier
v_mfma_f32_16x16x32_bf16 v[24:27],a[152:155],v[44:47],v[24:27]
ds_read_b128 v[40:43],v2 offset:59392
ds_read_b32 v15,v7 offset:256
v_mfma_f32_16x16x32_bf16 v[28:31],a[156:159],v[44:47],v[28:31]
s_waitcnt lgkmcnt(4)
ds_read_b128 v[76:79],v14 offset:2048
v_mfma_f32_16x16x32_bf16 v[24:27],a[160:163],v[48:51],v[24:27]
ds_read_b128 v[44:47],v2 offset:60416
v_mfma_f32_16x16x32_bf16 v[28:31],a[164:167],v[48:51],v[28:31]
v_mfma_f32_16x16x32_bf16 v[24:27],a[168:171],v[52:55],v[24:27]
s_waitcnt lgkmcnt(2)
ds_read_b128 v[48:51],v2 offset:61440
v_mfma_f32_16x16x32_bf16 v[28:31],a[172:175],v[52:55],v[28:31]
s_waitcnt lgkmcnt(2)
v_cmp_eq_u32 vcc,s60,v15
v_add_f32 v76,v76,v77
v_add_f32 v78,v78,v79
v_add_f32 v76,v76,v78
v_mfma_f32_16x16x32_bf16 v[24:27],a[176:179],v[56:59],v[24:27]
buffer_store_dword v76,v12,s[36:39],s85 offen offset:0 sc0 sc1
ds_read_b128 v[52:55],v2 offset:62464
v_mfma_f32_16x16x32_bf16 v[28:31],a[180:183],v[56:59],v[28:31]
s_cmp_eq_u32 vcc_lo,-1)"
R"(
s_cselect_b32 s66,s86,s59
s_xor_b32 s64,s64,65536
v_mfma_f32_16x16x32_bf16 v[24:27],a[184:187],v[60:63],v[24:27]
buffer_load_dwordx4 v0,s[36:39],s66 offen offset:0 sc1 lds
v_mfma_f32_16x16x32_bf16 v[28:31],a[188:191],v[60:63],v[28:31]
s_waitcnt lgkmcnt(2)
ds_read_b128 v[56:59],v2 offset:63488
s_add_u32 s74,s53,s55
v_mfma_f32_16x16x32_bf16 v[24:27],a[192:195],v[32:35],v[24:27]
s_waitcnt vmcnt(9)
ds_read_b128 v[60:63],v2 offset:64512
v_xor_b32 v2,65536,v2
v_mfma_f32_16x16x32_bf16 v[28:31],a[196:199],v[32:35],v[28:31]
s_barrier
v_mfma_f32_16x16x32_bf16 v[24:27],a[200:203],v[36:39],v[24:27]
s_cmp_eq_u32 s74,s56
ds_read_b128 v[32:35],v2 offset:0
ds_read_b128 v[84:87],v8 offset:8192
v_mfma_f32_16x16x32_bf16 v[28:31],a[204:207],v[36:39],v[28:31]
s_cselect_b32 s74,s57,s74
ds_read_b64 v[90:91],v9 offset:9216
v_xor_b32 v8,16384,v8
v_mfma_f32_16x16x32_bf16 v[24:27],a[208:211],v[40:43],v[24:27]
buffer_load_dwordx4 v0,s[36:39],s66 offen offset:1024 sc1 lds
v_mfma_f32_16x16x32_bf16 v[28:31],a[212:215],v[40:43],v[28:31]
s_cmp_eq_u32 s74,s58
ds_read_b128 v[36:39],v2 offset:1024
v_xor_b32 v9,16384,v9
v_mfma_f32_16x16x32_bf16 v[24:27],a[216:219],v[44:47],v[24:27]
s_cselect_b32 s74,s59,s74
ds_read_b128 v[40:43],v2 offset:2048
s_mov_b32 s52,s74
v_mfma_f32_16x16x32_bf16 v[28:31],a[220:223],v[44:47],v[28:31]
s_waitcnt lgkmcnt(5)
s_cmp_lt_u32 s67,s59
v_mfma_f32_16x16x32_bf16 v[24:27],a[224:227],v[48:51],v[24:27]
buffer_load_dwordx4 v0,s[36:39],s66 offen offset:2048 sc1 lds
v_mfma_f32_16x16x32_bf16 v[28:31],a[228:231],v[48:51],v[28:31]
s_waitcnt lgkmcnt(2)
ds_read_b128 v[44:47],v2 offset:3072
v_mfma_f32_16x16x32_bf16 v[24:27],a[232:235],v[52:55],v[24:27]
ds_read_b128 v[48:51],v2 offset:4096
v_mfma_f32_16x16x32_bf16 v[28:31],a[236:239],v[52:55],v[28:31]
v_mfma_f32_16x16x32_bf16 v[24:27],a[240:243],v[56:59],v[24:27]
buffer_load_dwordx4 v0,s[36:39],s66 offen offset:3072 sc1 lds
v_mfma_f32_16x16x32_bf16 v[28:31],a[244:247],v[56:59],v[28:31]
ds_read_b128 v[52:55],v2 offset:5120
v_mfma_f32_16x16x32_bf16 v[24:27],a[248:251],v[60:63],v[24:27]
s_waitcnt lgkmcnt(2)
ds_read_b128 v[56:59],v2 offset:6144
v_mfma_f32_16x16x32_bf16 v[28:31],a[252:255],v[60:63],v[28:31]
s_cbranch_scc1 Lg2_d0w2_%=
s_branch Lg2_slow0_%=
Lg2_d0w3_%=:
v_mfma_f32_16x16x32_bf16 v[16:19],a[0:3],v[32:35],0
s_add_u32 m0,s62,16384
ds_read_b128 v[60:63],v2 offset:7168
v_lshlrev_b32 v64,16,v84
v_mfma_f32_16x16x32_bf16 v[20:23],a[4:7],v[32:35],0
s_lshr_b32 s73,s47,0
v_and_b32 v65,0xffff0000,v84
v_lshlrev_b32 v66,16,v85
v_mfma_f32_16x16x32_bf16 v[16:19],a[8:11],v[36:39],v[16:19]
s_xor_b32 s87,s64,65536
ds_read_b128 v[32:35],v2 offset:8192
v_and_b32 v67,0xffff0000,v85
v_mfma_f32_16x16x32_bf16 v[20:23],a[12:15],v[36:39],v[20:23]
s_cmp_ge_u32 s66,s59
v_lshlrev_b32 v68,16,v86
v_and_b32 v69,0xffff0000,v86
v_mfma_f32_16x16x32_bf16 v[16:19],a[16:19],v[40:43],v[16:19]
buffer_load_dwordx4 v1,s[36:39],s66 offen offset:0 sc1 lds
v_mfma_f32_16x16x32_bf16 v[20:23],a[20:23],v[40:43],v[20:23]
s_cselect_b32 s86,s49,s50
ds_read_b128 v[36:39],v2 offset:9216
v_lshlrev_b32 v70,16,v87
v_mfma_f32_16x16x32_bf16 v[16:19],a[24:27],v[44:47],v[16:19]
s_cselect_b32 s87,s87,s63
ds_read_b128 v[40:43],v2 offset:10240
v_and_b32 v71,0xffff0000,v87
v_mfma_f32_16x16x32_bf16 v[20:23],a[28:31],v[44:47],v[20:23]
s_waitcnt lgkmcnt(3)
v_fma_f32 v64,v64,v91,v90
v_fma_f32 v65,v65,v91,v90
v_mfma_f32_16x16x32_bf16 v[16:19],a[32:35],v[48:51],v[16:19]
buffer_load_dwordx4 v1,s[36:39],s66 offen offset:1024 sc1 lds
v_mfma_f32_16x16x32_bf16 v[20:23],a[36:39],v[48:51],v[20:23]
s_lshr_b32 s72,s66,6
ds_read_b128 v[44:47],v2 offset:11264
v_fma_f32 v66,v66,v91,v90
v_mfma_f32_16x16x32_bf16 v[16:19],a[40:43],v[52:55],v[16:19]
s_lshr_b32 s71,s62,2
ds_read_b128 v[48:51],v2 offset:12288
v_fma_f32 v67,v67,v91,v90
v_mfma_f32_16x16x32_bf16 v[20:23],a[44:47],v[52:55],v[20:23]
s_mov_b32 s62,s87
v_fma_f32 v68,v68,v91,v90
v_fma_f32 v69,v69,v91,v90
v_mfma_f32_16x16x32_bf16 v[16:19],a[48:51],v[56:59],v[16:19]
buffer_load_dwordx4 v1,s[36:39],s66 offen offset:2048 sc1 lds
v_mfma_f32_16x16x32_bf16 v[20:23],a[52:55],v[56:59],v[20:23]
ds_read_b128 v[52:55],v2 offset:13312
v_fma_f32 v70,v70,v91,v90
v_mfma_f32_16x16x32_bf16 v[16:19],a[56:59],v[60:63],v[16:19]
ds_read_b128 v[56:59],v2 offset:14336
v_fma_f32 v71,v71,v91,v90
v_mfma_f32_16x16x32_bf16 v[20:23],a[60:63],v[60:63],v[20:23]
s_waitcnt lgkmcnt(3)
v_add_f32 v24,v24,v64
v_add_f32 v25,v25,v65
v_mfma_f32_16x16x32_bf16 v[16:19],a[64:67],v[32:35],v[16:19]
s_waitcnt vmcnt(9)
ds_read_b128 v[60:63],v2 offset:15360
v_add_f32 v26,v26,v66
v_mfma_f32_16x16x32_bf16 v[20:23],a[68:71],v[32:35],v[20:23]
s_barrier
v_mfma_f32_16x16x32_bf16 v[16:19],a[72:75],v[36:39],v[16:19]
buffer_load_dwordx4 v1,s[36:39],s66 offen offset:3072 sc1 lds
v_mfma_f32_16x16x32_bf16 v[20:23],a[76:79],v[36:39],v[20:23]
s_add_u32 m0,s71,s88
ds_read_b128 v[32:35],v2 offset:16384
v_add_f32 v27,v27,v67
v_mfma_f32_16x16x32_bf16 v[16:19],a[80:83],v[40:43],v[16:19]
s_lshr_b32 s70,s46,5
ds_read_b128 v[36:39],v2 offset:17408
v_add_f32 v28,v28,v68
v_mfma_f32_16x16x32_bf16 v[20:23],a[84:87],v[40:43],v[20:23]
v_add_f32 v29,v29,v69
v_add_f32 v30,v30,v70
v_mfma_f32_16x16x32_bf16 v[16:19],a[88:91],v[44:47],v[16:19]
buffer_load_dwordx4 v10,s[36:39],s66 offen offset:0 sc1 lds
v_mfma_f32_16x16x32_bf16 v[20:23],a[92:95],v[44:47],v[20:23]
s_waitcnt lgkmcnt(2)
ds_read_b128 v[40:43],v2 offset:18432
v_add_f32 v31,v31,v71
v_mfma_f32_16x16x32_bf16 v[16:19],a[96:99],v[48:51],v[16:19]
buffer_load_dword v11,s[36:39],s72 offen offset:1024 sc1 lds
v_mfma_f32_16x16x32_bf16 v[20:23],a[100:103],v[48:51],v[20:23]
s_mov_b32 m0,s62
ds_read_b128 v[44:47],v2 offset:19456
v_cvt_pk_bf16_f32 v72,v24,v25
v_mfma_f32_16x16x32_bf16 v[16:19],a[104:107],v[52:55],v[16:19]
v_cvt_pk_bf16_f32 v73,v26,v27
v_cvt_pk_bf16_f32 v74,v28,v29
v_cvt_pk_bf16_f32 v75,v30,v31
v_mfma_f32_16x16x32_bf16 v[20:23],a[108:111],v[52:55],v[20:23]
ds_read_b128 v[48:51],v2 offset:20480
buffer_store_dwordx4 v[72:75],v3,s[36:39],s73 offen offset:0 sc0 sc1
v_add_f32 v64,v24,v25
v_mul_f32 v65,v24,v24
v_fma_f32 v65,v25,v25,v65
v_mfma_f32_16x16x32_bf16 v[16:19],a[112:115],v[56:59],v[16:19]
ds_read_b128 v[52:55],v2 offset:21504
v_add_f32 v64,v64,v26
v_mfma_f32_16x16x32_bf16 v[20:23],a[116:119],v[56:59],v[20:23]
v_fma_f32 v65,v26,v26,v65
v_add_f32 v64,v64,v27
v_fma_f32 v65,v27,v27,v65
v_add_f32 v64,v64,v28
v_fma_f32 v65,v28,v28,v65
v_add_f32 v64,v64,v29
v_fma_f32 v65,v29,v29,v65
v_add_f32 v64,v64,v30
v_fma_f32 v65,v30,v30,v65
v_add_f32 v64,v64,v31
v_fma_f32 v65,v31,v31,v65
v_mfma_f32_16x16x32_bf16 v[16:19],a[120:123],v[60:63],v[16:19]
s_nop 0
v_permlane16_swap_b32 v64,v65
v_add_f32 v64,v64,v65
v_mov_b32 v65,v64
v_mfma_f32_16x16x32_bf16 v[20:23],a[124:127],v[60:63],v[20:23]
s_waitcnt lgkmcnt(2)
ds_read_b128 v[56:59],v2 offset:22528
v_permlane32_swap_b32 v64,v65
v_add_f32 v64,v64,v65
v_mfma_f32_16x16x32_bf16 v[16:19],a[128:131],v[32:35],v[16:19]
ds_write_b32 v13,v64 offset:0
v_mfma_f32_16x16x32_bf16 v[20:23],a[132:135],v[32:35],v[20:23]
ds_read_b128 v[60:63],v2 offset:23552
v_mfma_f32_16x16x32_bf16 v[16:19],a[136:139],v[36:39],v[16:19]
ds_read_b128 v[32:35],v2 offset:24576
v_mfma_f32_16x16x32_bf16 v[20:23],a[140:143],v[36:39],v[20:23]
v_mfma_f32_16x16x32_bf16 v[16:19],a[144:147],v[40:43],v[16:19]
s_waitcnt vmcnt(12) lgkmcnt(2)
ds_read_b128 v[36:39],v2 offset:25600
v_mfma_f32_16x16x32_bf16 v[20:23],a[148:151],v[40:43],v[20:23]
s_barrier
v_mfma_f32_16x16x32_bf16 v[16:19],a[152:155],v[44:47],v[16:19]
buffer_store_dword v5,v4,s[36:39],s70 offen offset:0 sc0 sc1
ds_read_b128 v[40:43],v2 offset:26624
ds_read_b32 v15,v7 offset:0
v_mfma_f32_16x16x32_bf16 v[20:23],a[156:159],v[44:47],v[20:23]
s_waitcnt lgkmcnt(4)
v_mfma_f32_16x16x32_bf16 v[16:19],a[160:163],v[48:51],v[16:19]
ds_read_b128 v[44:47],v2 offset:27648
v_mfma_f32_16x16x32_bf16 v[20:23],a[164:167],v[48:51],v[20:23]
v_mfma_f32_16x16x32_bf16 v[16:19],a[168:171],v[52:55],v[16:19]
s_waitcnt lgkmcnt(1)
ds_read_b128 v[48:51],v2 offset:28672)"
R"(
v_mfma_f32_16x16x32_bf16 v[20:23],a[172:175],v[52:55],v[20:23]
v_cmp_eq_u32 vcc,s60,v15
v_mfma_f32_16x16x32_bf16 v[16:19],a[176:179],v[56:59],v[16:19]
s_cmp_eq_u32 vcc_lo,-1
ds_read_b128 v[52:55],v2 offset:29696
s_cselect_b32 s67,s86,s59
v_mfma_f32_16x16x32_bf16 v[20:23],a[180:183],v[56:59],v[20:23]
s_xor_b32 s63,s63,65536
s_add_u32 s53,s52,s55
s_cmp_eq_u32 s53,s56
v_mfma_f32_16x16x32_bf16 v[16:19],a[184:187],v[60:63],v[16:19]
s_cselect_b32 s53,s57,s53
ds_read_b128 v[56:59],v2 offset:30720
s_cmp_eq_u32 s53,s58
v_mfma_f32_16x16x32_bf16 v[20:23],a[188:191],v[60:63],v[20:23]
s_waitcnt lgkmcnt(3)
buffer_load_dwordx4 v0,s[36:39],s67 offen offset:0 sc1 lds
v_mfma_f32_16x16x32_bf16 v[16:19],a[192:195],v[32:35],v[16:19]
s_waitcnt vmcnt(9)
ds_read_b128 v[60:63],v2 offset:31744
s_cselect_b32 s53,s59,s53
v_mfma_f32_16x16x32_bf16 v[20:23],a[196:199],v[32:35],v[20:23]
s_barrier
v_mfma_f32_16x16x32_bf16 v[16:19],a[200:203],v[36:39],v[16:19]
s_cmp_ge_u32 s66,s59
ds_read_b128 v[32:35],v2 offset:32768
ds_read_b128 v[80:83],v8 offset:0
v_mfma_f32_16x16x32_bf16 v[20:23],a[204:207],v[36:39],v[20:23]
ds_read_b64 v[88:89],v9 offset:1024
v_mfma_f32_16x16x32_bf16 v[16:19],a[208:211],v[40:43],v[16:19]
ds_read_b128 v[36:39],v2 offset:33792
v_mfma_f32_16x16x32_bf16 v[20:23],a[212:215],v[40:43],v[20:23]
buffer_load_dwordx4 v0,s[36:39],s67 offen offset:1024 sc1 lds
v_mfma_f32_16x16x32_bf16 v[16:19],a[216:219],v[44:47],v[16:19]
ds_read_b128 v[40:43],v2 offset:34816
v_mfma_f32_16x16x32_bf16 v[20:23],a[220:223],v[44:47],v[20:23]
s_waitcnt lgkmcnt(5)
v_mfma_f32_16x16x32_bf16 v[16:19],a[224:227],v[48:51],v[16:19]
ds_read_b128 v[44:47],v2 offset:35840
v_mfma_f32_16x16x32_bf16 v[20:23],a[228:231],v[48:51],v[20:23]
s_waitcnt lgkmcnt(3)
buffer_load_dwordx4 v0,s[36:39],s67 offen offset:2048 sc1 lds
v_mfma_f32_16x16x32_bf16 v[16:19],a[232:235],v[52:55],v[16:19]
ds_read_b128 v[48:51],v2 offset:36864
v_mfma_f32_16x16x32_bf16 v[20:23],a[236:239],v[52:55],v[20:23]
v_mfma_f32_16x16x32_bf16 v[16:19],a[240:243],v[56:59],v[16:19]
ds_read_b128 v[52:55],v2 offset:37888
v_mfma_f32_16x16x32_bf16 v[20:23],a[244:247],v[56:59],v[20:23]
buffer_load_dwordx4 v0,s[36:39],s67 offen offset:3072 sc1 lds
v_mfma_f32_16x16x32_bf16 v[16:19],a[248:251],v[60:63],v[16:19]
s_waitcnt lgkmcnt(2)
ds_read_b128 v[56:59],v2 offset:38912
v_mfma_f32_16x16x32_bf16 v[20:23],a[252:255],v[60:63],v[20:23]
s_cbranch_scc1 Lg2_slow1_%=
Lg2_d1w3_%=:
v_mfma_f32_16x16x32_bf16 v[24:27],a[0:3],v[32:35],0
s_add_u32 m0,s62,16384
ds_read_b128 v[60:63],v2 offset:39936
v_lshlrev_b32 v64,16,v80
v_mfma_f32_16x16x32_bf16 v[28:31],a[4:7],v[32:35],0
s_lshr_b32 s73,s48,0
v_and_b32 v65,0xffff0000,v80
v_lshlrev_b32 v66,16,v81
v_mfma_f32_16x16x32_bf16 v[24:27],a[8:11],v[36:39],v[24:27]
s_xor_b32 s87,s63,65536
ds_read_b128 v[32:35],v2 offset:40960
v_and_b32 v67,0xffff0000,v81
v_mfma_f32_16x16x32_bf16 v[28:31],a[12:15],v[36:39],v[28:31]
s_cmp_ge_u32 s67,s59
v_lshlrev_b32 v68,16,v82
v_and_b32 v69,0xffff0000,v82
v_mfma_f32_16x16x32_bf16 v[24:27],a[16:19],v[40:43],v[24:27]
buffer_load_dwordx4 v1,s[36:39],s67 offen offset:0 sc1 lds
v_mfma_f32_16x16x32_bf16 v[28:31],a[20:23],v[40:43],v[28:31]
s_cselect_b32 s86,s50,s51
ds_read_b128 v[36:39],v2 offset:41984
v_lshlrev_b32 v70,16,v83
v_mfma_f32_16x16x32_bf16 v[24:27],a[24:27],v[44:47],v[24:27]
s_cselect_b32 s87,s87,s64
ds_read_b128 v[40:43],v2 offset:43008
v_and_b32 v71,0xffff0000,v83
v_mfma_f32_16x16x32_bf16 v[28:31],a[28:31],v[44:47],v[28:31]
s_waitcnt lgkmcnt(3)
v_fma_f32 v64,v64,v89,v88
v_fma_f32 v65,v65,v89,v88
v_mfma_f32_16x16x32_bf16 v[24:27],a[32:35],v[48:51],v[24:27]
buffer_load_dwordx4 v1,s[36:39],s67 offen offset:1024 sc1 lds
v_mfma_f32_16x16x32_bf16 v[28:31],a[36:39],v[48:51],v[28:31]
s_lshr_b32 s72,s67,6
ds_read_b128 v[44:47],v2 offset:44032
v_fma_f32 v66,v66,v89,v88
v_mfma_f32_16x16x32_bf16 v[24:27],a[40:43],v[52:55],v[24:27]
s_lshr_b32 s71,s62,2
ds_read_b128 v[48:51],v2 offset:45056
v_fma_f32 v67,v67,v89,v88
v_mfma_f32_16x16x32_bf16 v[28:31],a[44:47],v[52:55],v[28:31]
s_mov_b32 s62,s87
v_fma_f32 v68,v68,v89,v88
v_fma_f32 v69,v69,v89,v88
v_mfma_f32_16x16x32_bf16 v[24:27],a[48:51],v[56:59],v[24:27]
buffer_load_dwordx4 v1,s[36:39],s67 offen offset:2048 sc1 lds
v_mfma_f32_16x16x32_bf16 v[28:31],a[52:55],v[56:59],v[28:31]
s_mov_b64 s[44:45],s[46:47]
ds_read_b128 v[52:55],v2 offset:46080
v_fma_f32 v70,v70,v89,v88
v_mfma_f32_16x16x32_bf16 v[24:27],a[56:59],v[60:63],v[24:27]
ds_read_b128 v[56:59],v2 offset:47104
v_fma_f32 v71,v71,v89,v88
v_mfma_f32_16x16x32_bf16 v[28:31],a[60:63],v[60:63],v[28:31]
s_waitcnt lgkmcnt(3)
v_add_f32 v16,v16,v64
v_add_f32 v17,v17,v65
v_mfma_f32_16x16x32_bf16 v[24:27],a[64:67],v[32:35],v[24:27]
s_waitcnt vmcnt(9)
ds_read_b128 v[60:63],v2 offset:48128
v_add_f32 v18,v18,v66
v_mfma_f32_16x16x32_bf16 v[28:31],a[68:71],v[32:35],v[28:31]
s_barrier
v_mfma_f32_16x16x32_bf16 v[24:27],a[72:75],v[36:39],v[24:27]
buffer_load_dwordx4 v1,s[36:39],s67 offen offset:3072 sc1 lds
v_mfma_f32_16x16x32_bf16 v[28:31],a[76:79],v[36:39],v[28:31]
s_add_u32 m0,s71,s88
ds_read_b128 v[32:35],v2 offset:49152
v_add_f32 v19,v19,v67
v_mfma_f32_16x16x32_bf16 v[24:27],a[80:83],v[40:43],v[24:27]
s_lshr_b32 s70,s47,5
ds_read_b128 v[36:39],v2 offset:50176
v_add_f32 v20,v20,v68
v_mfma_f32_16x16x32_bf16 v[28:31],a[84:87],v[40:43],v[28:31]
s_mov_b64 s[46:47],s[48:49]
v_add_f32 v21,v21,v69
v_add_f32 v22,v22,v70
v_mfma_f32_16x16x32_bf16 v[24:27],a[88:91],v[44:47],v[24:27]
buffer_load_dwordx4 v10,s[36:39],s67 offen offset:0 sc1 lds
v_mfma_f32_16x16x32_bf16 v[28:31],a[92:95],v[44:47],v[28:31]
s_waitcnt lgkmcnt(2)
ds_read_b128 v[40:43],v2 offset:51200
v_add_f32 v23,v23,v71
v_mfma_f32_16x16x32_bf16 v[24:27],a[96:99],v[48:51],v[24:27]
buffer_load_dword v11,s[36:39],s72 offen offset:1024 sc1 lds
v_mfma_f32_16x16x32_bf16 v[28:31],a[100:103],v[48:51],v[28:31]
s_mov_b32 m0,s62
ds_read_b128 v[44:47],v2 offset:52224
v_cvt_pk_bf16_f32 v72,v16,v17
v_mfma_f32_16x16x32_bf16 v[24:27],a[104:107],v[52:55],v[24:27]
s_mov_b64 s[48:49],s[50:51]
v_cvt_pk_bf16_f32 v73,v18,v19
v_cvt_pk_bf16_f32 v74,v20,v21
v_cvt_pk_bf16_f32 v75,v22,v23
v_mfma_f32_16x16x32_bf16 v[28:31],a[108:111],v[52:55],v[28:31]
s_mov_b64 s[50:51],s[52:53]
ds_read_b128 v[48:51],v2 offset:53248
buffer_store_dwordx4 v[72:75],v3,s[36:39],s73 offen offset:0 sc0 sc1
v_add_f32 v64,v16,v17
v_mul_f32 v65,v16,v16
v_fma_f32 v65,v17,v17,v65
v_mfma_f32_16x16x32_bf16 v[24:27],a[112:115],v[56:59],v[24:27]
ds_read_b128 v[52:55],v2 offset:54272
v_add_f32 v64,v64,v18
v_mfma_f32_16x16x32_bf16 v[28:31],a[116:119],v[56:59],v[28:31]
v_fma_f32 v65,v18,v18,v65
v_add_f32 v64,v64,v19
v_fma_f32 v65,v19,v19,v65
v_add_f32 v64,v64,v20
v_fma_f32 v65,v20,v20,v65
v_add_f32 v64,v64,v21
v_fma_f32 v65,v21,v21,v65
v_add_f32 v64,v64,v22
v_fma_f32 v65,v22,v22,v65
v_add_f32 v64,v64,v23
v_fma_f32 v65,v23,v23,v65
v_mfma_f32_16x16x32_bf16 v[24:27],a[120:123],v[60:63],v[24:27]
s_nop 0
v_permlane16_swap_b32 v64,v65
v_add_f32 v64,v64,v65
v_mov_b32 v65,v64
v_mfma_f32_16x16x32_bf16 v[28:31],a[124:127],v[60:63],v[28:31]
s_waitcnt lgkmcnt(2)
ds_read_b128 v[56:59],v2 offset:55296
v_permlane32_swap_b32 v64,v65
v_add_f32 v64,v64,v65
v_mfma_f32_16x16x32_bf16 v[24:27],a[128:131],v[32:35],v[24:27]
ds_write_b32 v13,v64 offset:2048
v_mfma_f32_16x16x32_bf16 v[28:31],a[132:135],v[32:35],v[28:31]
ds_read_b128 v[60:63],v2 offset:56320
v_mfma_f32_16x16x32_bf16 v[24:27],a[136:139],v[36:39],v[24:27]
ds_read_b128 v[32:35],v2 offset:57344
v_mfma_f32_16x16x32_bf16 v[28:31],a[140:143],v[36:39],v[28:31]
v_mfma_f32_16x16x32_bf16 v[24:27],a[144:147],v[40:43],v[24:27]
s_waitcnt vmcnt(12) lgkmcnt(2)
ds_read_b128 v[36:39],v2 offset:58368
v_mfma_f32_16x16x32_bf16 v[28:31],a[148:151],v[40:43],v[28:31]
s_barrier
v_mfma_f32_16x16x32_bf16 v[24:27],a[152:155],v[44:47],v[24:27]
buffer_store_dword v5,v4,s[36:39],s70 offen offset:0 sc0 sc1
ds_read_b128 v[40:43],v2 offset:59392
ds_read_b32 v15,v7 offset:256
v_mfma_f32_16x16x32_bf16 v[28:31],a[156:159],v[44:47],v[28:31]
s_waitcnt lgkmcnt(4)
v_mfma_f32_16x16x32_bf16 v[24:27],a[160:163],v[48:51],v[24:27])"
R"(
ds_read_b128 v[44:47],v2 offset:60416
v_mfma_f32_16x16x32_bf16 v[28:31],a[164:167],v[48:51],v[28:31]
v_mfma_f32_16x16x32_bf16 v[24:27],a[168:171],v[52:55],v[24:27]
s_waitcnt lgkmcnt(1)
ds_read_b128 v[48:51],v2 offset:61440
v_mfma_f32_16x16x32_bf16 v[28:31],a[172:175],v[52:55],v[28:31]
v_cmp_eq_u32 vcc,s60,v15
v_mfma_f32_16x16x32_bf16 v[24:27],a[176:179],v[56:59],v[24:27]
s_cmp_eq_u32 vcc_lo,-1
ds_read_b128 v[52:55],v2 offset:62464
s_cselect_b32 s66,s86,s59
v_mfma_f32_16x16x32_bf16 v[28:31],a[180:183],v[56:59],v[28:31]
s_xor_b32 s64,s64,65536
s_add_u32 s74,s53,s55
s_cmp_eq_u32 s74,s56
v_mfma_f32_16x16x32_bf16 v[24:27],a[184:187],v[60:63],v[24:27]
s_cselect_b32 s74,s57,s74
ds_read_b128 v[56:59],v2 offset:63488
s_cmp_eq_u32 s74,s58
v_mfma_f32_16x16x32_bf16 v[28:31],a[188:191],v[60:63],v[28:31]
s_waitcnt lgkmcnt(3)
buffer_load_dwordx4 v0,s[36:39],s66 offen offset:0 sc1 lds
v_mfma_f32_16x16x32_bf16 v[24:27],a[192:195],v[32:35],v[24:27]
s_waitcnt vmcnt(9)
ds_read_b128 v[60:63],v2 offset:64512
v_xor_b32 v2,65536,v2
v_mfma_f32_16x16x32_bf16 v[28:31],a[196:199],v[32:35],v[28:31]
s_barrier
v_mfma_f32_16x16x32_bf16 v[24:27],a[200:203],v[36:39],v[24:27]
s_cselect_b32 s74,s59,s74
ds_read_b128 v[32:35],v2 offset:0
ds_read_b128 v[84:87],v8 offset:8192
v_mfma_f32_16x16x32_bf16 v[28:31],a[204:207],v[36:39],v[28:31]
s_mov_b32 s52,s74
ds_read_b64 v[90:91],v9 offset:9216
v_xor_b32 v8,16384,v8
v_mfma_f32_16x16x32_bf16 v[24:27],a[208:211],v[40:43],v[24:27]
s_cmp_lt_u32 s67,s59
ds_read_b128 v[36:39],v2 offset:1024
v_xor_b32 v9,16384,v9
v_mfma_f32_16x16x32_bf16 v[28:31],a[212:215],v[40:43],v[28:31]
buffer_load_dwordx4 v0,s[36:39],s66 offen offset:1024 sc1 lds
v_mfma_f32_16x16x32_bf16 v[24:27],a[216:219],v[44:47],v[24:27]
ds_read_b128 v[40:43],v2 offset:2048
v_mfma_f32_16x16x32_bf16 v[28:31],a[220:223],v[44:47],v[28:31]
s_waitcnt lgkmcnt(5)
v_mfma_f32_16x16x32_bf16 v[24:27],a[224:227],v[48:51],v[24:27]
ds_read_b128 v[44:47],v2 offset:3072
v_mfma_f32_16x16x32_bf16 v[28:31],a[228:231],v[48:51],v[28:31]
s_waitcnt lgkmcnt(3)
buffer_load_dwordx4 v0,s[36:39],s66 offen offset:2048 sc1 lds
v_mfma_f32_16x16x32_bf16 v[24:27],a[232:235],v[52:55],v[24:27]
ds_read_b128 v[48:51],v2 offset:4096
v_mfma_f32_16x16x32_bf16 v[28:31],a[236:239],v[52:55],v[28:31]
v_mfma_f32_16x16x32_bf16 v[24:27],a[240:243],v[56:59],v[24:27]
ds_read_b128 v[52:55],v2 offset:5120
v_mfma_f32_16x16x32_bf16 v[28:31],a[244:247],v[56:59],v[28:31]
buffer_load_dwordx4 v0,s[36:39],s66 offen offset:3072 sc1 lds
v_mfma_f32_16x16x32_bf16 v[24:27],a[248:251],v[60:63],v[24:27]
s_waitcnt lgkmcnt(2)
ds_read_b128 v[56:59],v2 offset:6144
v_mfma_f32_16x16x32_bf16 v[28:31],a[252:255],v[60:63],v[28:31]
s_cbranch_scc1 Lg2_d0w3_%=
s_branch Lg2_slow0_%=
Lg2_slow0_%=:
s_cmp_lt_u32 s66,s59
s_cbranch_scc1 Lg2_gs0_%=
Lg2_slowb0_%=:
s_cmp_eq_u32 s54,1
s_cselect_b32 s68,s49,s48
s_lshr_b32 s68,s68,5
s_add_u32 m0,s78,8192
s_nop 0
buffer_load_dword v6,s[36:39],s68 offen offset:0 sc1 lds
s_lshr_b32 s73,s47,0
s_lshr_b32 s85,s47,5
v_lshlrev_b32 v64,16,v84
v_and_b32 v65,0xffff0000,v84
v_lshlrev_b32 v66,16,v85
v_and_b32 v67,0xffff0000,v85
v_lshlrev_b32 v68,16,v86
v_and_b32 v69,0xffff0000,v86
v_lshlrev_b32 v70,16,v87
v_and_b32 v71,0xffff0000,v87
v_fma_f32 v64,v64,v91,v90
v_fma_f32 v65,v65,v91,v90
v_fma_f32 v66,v66,v91,v90
v_fma_f32 v67,v67,v91,v90
v_fma_f32 v68,v68,v91,v90
v_fma_f32 v69,v69,v91,v90
v_fma_f32 v70,v70,v91,v90
v_fma_f32 v71,v71,v91,v90
v_add_f32 v24,v24,v64
v_add_f32 v25,v25,v65
v_add_f32 v26,v26,v66
v_add_f32 v27,v27,v67
v_add_f32 v28,v28,v68
v_add_f32 v29,v29,v69
v_add_f32 v30,v30,v70
v_add_f32 v31,v31,v71
v_cvt_pk_bf16_f32 v72,v24,v25
v_cvt_pk_bf16_f32 v73,v26,v27
v_cvt_pk_bf16_f32 v74,v28,v29
v_cvt_pk_bf16_f32 v75,v30,v31
s_add_u32 m0,s78,8448
s_nop 0
buffer_load_dword v6,s[36:39],s68 offen offset:0 sc1 lds
buffer_store_dwordx4 v[72:75],v3,s[36:39],s73 offen offset:0 sc0 sc1
v_add_f32 v64,v24,v25
v_mul_f32 v65,v24,v24
v_fma_f32 v65,v25,v25,v65
v_add_f32 v64,v64,v26
v_fma_f32 v65,v26,v26,v65
v_add_f32 v64,v64,v27
v_fma_f32 v65,v27,v27,v65
v_add_f32 v64,v64,v28
v_fma_f32 v65,v28,v28,v65
v_add_f32 v64,v64,v29
v_fma_f32 v65,v29,v29,v65
v_add_f32 v64,v64,v30
v_fma_f32 v65,v30,v30,v65
v_add_f32 v64,v64,v31
v_fma_f32 v65,v31,v31,v65
s_nop 1
v_permlane16_swap_b32 v64,v65
v_add_f32 v64,v64,v65
v_mov_b32 v65,v64
s_nop 1
v_permlane32_swap_b32 v64,v65
v_add_f32 v64,v64,v65
ds_write_b32 v13,v64 offset:0
s_waitcnt lgkmcnt(0)
s_barrier
ds_read_b128 v[76:79],v14 offset:0
s_waitcnt lgkmcnt(0)
v_add_f32 v76,v76,v77
v_add_f32 v78,v78,v79
v_add_f32 v76,v76,v78
buffer_store_dword v76,v12,s[36:39],s85 offen offset:0 sc0 sc1
s_cmp_ge_u32 s48,s59
s_cbranch_scc1 Lg2_done0_%=
s_add_u32 m0,s78,16384
s_nop 0
buffer_load_dword v6,s[36:39],s68 offen offset:0 sc1 lds
s_waitcnt vmcnt(4)
s_barrier
ds_read_b32 v15,v7 offset:8192
s_lshr_b32 s70,s46,5
buffer_store_dword v5,v4,s[36:39],s70 offen offset:0 sc0 sc1
s_mov_b32 s46,s59
s_mov_b32 s69,18432
s_waitcnt lgkmcnt(0)
v_cmp_eq_u32 vcc,s60,v15
s_nop 0
s_cmp_eq_u32 vcc_lo,-1
s_cbranch_scc1 Lg2_go0_%=
s_add_u32 m0,s78,16640
s_nop 0
buffer_load_dword v6,s[36:39],s68 offen offset:0 sc1 lds
s_waitcnt vmcnt(5)
s_barrier
ds_read_b32 v15,v7 offset:8448
s_mov_b32 s69,18688
s_waitcnt lgkmcnt(0)
v_cmp_eq_u32 vcc,s60,v15
s_nop 0
s_cmp_eq_u32 vcc_lo,-1
s_cbranch_scc1 Lg2_go0_%=
s_add_u32 m0,s78,8192
s_nop 0
buffer_load_dword v6,s[36:39],s68 offen offset:0 sc1 lds
s_waitcnt vmcnt(3)
s_barrier
ds_read_b32 v15,v7 offset:16384
s_lshr_b32 s70,s47,5
buffer_store_dword v5,v4,s[36:39],s70 offen offset:0 sc0 sc1
s_mov_b32 s47,s59
s_mov_b32 s69,10240
s_waitcnt lgkmcnt(0)
v_cmp_eq_u32 vcc,s60,v15
s_nop 0
s_cmp_eq_u32 vcc_lo,-1
s_cbranch_scc1 Lg2_go0_%=
s_add_u32 m0,s78,8448
s_nop 0
buffer_load_dword v6,s[36:39],s68 offen offset:0 sc1 lds
s_waitcnt vmcnt(3)
s_barrier
ds_read_b32 v15,v7 offset:16640
s_mov_b32 s69,10496
s_waitcnt lgkmcnt(0)
v_cmp_eq_u32 vcc,s60,v15
s_nop 0
s_cmp_eq_u32 vcc_lo,-1
s_cbranch_scc1 Lg2_go0_%=
Lg2_poll0_%=:
s_add_u32 m0,s78,16384
s_nop 0
buffer_load_dword v6,s[36:39],s68 offen offset:0 sc1 lds
s_waitcnt vmcnt(2))"
R"(
s_barrier
ds_read_b32 v15,v7 offset:8192
s_mov_b32 s69,18432
s_waitcnt lgkmcnt(0)
v_cmp_eq_u32 vcc,s60,v15
s_nop 0
s_cmp_eq_u32 vcc_lo,-1
s_cbranch_scc1 Lg2_go0_%=
s_sleep 4
s_add_u32 m0,s78,16640
s_nop 0
buffer_load_dword v6,s[36:39],s68 offen offset:0 sc1 lds
s_waitcnt vmcnt(2)
s_barrier
ds_read_b32 v15,v7 offset:8448
s_mov_b32 s69,18688
s_waitcnt lgkmcnt(0)
v_cmp_eq_u32 vcc,s60,v15
s_nop 0
s_cmp_eq_u32 vcc_lo,-1
s_cbranch_scc1 Lg2_go0_%=
s_sleep 4
s_add_u32 m0,s78,8192
s_nop 0
buffer_load_dword v6,s[36:39],s68 offen offset:0 sc1 lds
s_waitcnt vmcnt(2)
s_barrier
ds_read_b32 v15,v7 offset:16384
s_mov_b32 s69,10240
s_waitcnt lgkmcnt(0)
v_cmp_eq_u32 vcc,s60,v15
s_nop 0
s_cmp_eq_u32 vcc_lo,-1
s_cbranch_scc1 Lg2_go0_%=
s_sleep 4
s_add_u32 m0,s78,8448
s_nop 0
buffer_load_dword v6,s[36:39],s68 offen offset:0 sc1 lds
s_waitcnt vmcnt(2)
s_barrier
ds_read_b32 v15,v7 offset:16640
s_mov_b32 s69,10496
s_waitcnt lgkmcnt(0)
v_cmp_eq_u32 vcc,s60,v15
s_nop 0
s_cmp_eq_u32 vcc_lo,-1
s_cbranch_scc1 Lg2_go0_%=
s_sleep 4
s_add_u32 s79,s79,1
s_cmp_lt_u32 s79,20000
s_cbranch_scc1 Lg2_poll0_%=
Lg2_go0_%=:
s_xor_b32 s62,s63,65536
s_mov_b32 m0,s62
s_nop 0
buffer_load_dwordx4 v0,s[36:39],s48 offen offset:0 sc1 lds
buffer_load_dwordx4 v0,s[36:39],s48 offen offset:1024 sc1 lds
buffer_load_dwordx4 v0,s[36:39],s48 offen offset:2048 sc1 lds
buffer_load_dwordx4 v0,s[36:39],s48 offen offset:3072 sc1 lds
s_add_u32 m0,s62,16384
s_nop 0
buffer_load_dwordx4 v1,s[36:39],s48 offen offset:0 sc1 lds
buffer_load_dwordx4 v1,s[36:39],s48 offen offset:1024 sc1 lds
buffer_load_dwordx4 v1,s[36:39],s48 offen offset:2048 sc1 lds
buffer_load_dwordx4 v1,s[36:39],s48 offen offset:3072 sc1 lds
s_lshr_b32 s72,s48,6
s_lshr_b32 s71,s62,2
s_add_u32 m0,s71,s88
s_nop 0
buffer_load_dwordx4 v10,s[36:39],s48 offen offset:0 sc1 lds
buffer_load_dword v11,s[36:39],s72 offen offset:1024 sc1 lds
s_cmp_eq_u32 vcc_hi,-1
s_cselect_b32 s66,s49,s59
s_xor_b32 s62,s64,65536
s_cmp_ge_u32 s66,s59
s_cselect_b32 s68,s49,s50
s_lshr_b32 s68,s68,5
s_add_u32 m0,s78,0
s_nop 0
buffer_load_dword v6,s[36:39],s68 offen offset:0 sc1 lds
s_mov_b32 m0,s62
s_nop 0
buffer_load_dwordx4 v0,s[36:39],s66 offen offset:0 sc1 lds
buffer_load_dwordx4 v0,s[36:39],s66 offen offset:1024 sc1 lds
buffer_load_dwordx4 v0,s[36:39],s66 offen offset:2048 sc1 lds
buffer_load_dwordx4 v0,s[36:39],s66 offen offset:3072 sc1 lds
s_waitcnt vmcnt(11)
s_barrier
ds_read_b128 v[32:35],v2 offset:0
ds_read_b128 v[36:39],v2 offset:1024
ds_read_b128 v[40:43],v2 offset:2048
ds_read_b128 v[44:47],v2 offset:3072
ds_read_b128 v[48:51],v2 offset:4096
ds_read_b128 v[52:55],v2 offset:5120
ds_read_b128 v[56:59],v2 offset:6144
s_lshr_b32 s70,s46,5
buffer_store_dword v5,v4,s[36:39],s70 offen offset:0 sc0 sc1
s_mov_b32 s46,s59
s_lshr_b32 s70,s47,5
buffer_store_dword v5,v4,s[36:39],s70 offen offset:0 sc0 sc1
s_mov_b32 s47,s59
s_lshr_b32 s70,s59,5
buffer_store_dword v5,v4,s[36:39],s70 offen offset:0 sc0 sc1
s_waitcnt lgkmcnt(3)
s_setpc_b64 s[90:91]
Lg2_gs0_%=:
s_cmp_eq_u32 s54,1
s_cselect_b32 s68,s49,s48
s_lshr_b32 s68,s68,5
s_lshr_b32 s73,s47,0
s_lshr_b32 s85,s47,5
s_add_u32 m0,s78,8192
s_nop 0
buffer_load_dword v6,s[36:39],s68 offen offset:0 sc1 lds
v_lshlrev_b32 v64,16,v84
v_and_b32 v65,0xffff0000,v84
v_lshlrev_b32 v66,16,v85
v_and_b32 v67,0xffff0000,v85
v_lshlrev_b32 v68,16,v86
v_and_b32 v69,0xffff0000,v86
v_lshlrev_b32 v70,16,v87
v_and_b32 v71,0xffff0000,v87
s_add_u32 m0,s62,16384
s_nop 0
buffer_load_dwordx4 v1,s[36:39],s66 offen offset:0 sc1 lds
v_fma_f32 v64,v64,v91,v90
v_fma_f32 v65,v65,v91,v90
v_fma_f32 v66,v66,v91,v90
v_fma_f32 v67,v67,v91,v90
v_fma_f32 v68,v68,v91,v90
v_fma_f32 v69,v69,v91,v90
v_fma_f32 v70,v70,v91,v90
v_fma_f32 v71,v71,v91,v90
buffer_load_dwordx4 v1,s[36:39],s66 offen offset:1024 sc1 lds
v_add_f32 v24,v24,v64
v_add_f32 v25,v25,v65
v_add_f32 v26,v26,v66
v_add_f32 v27,v27,v67
v_add_f32 v28,v28,v68
v_add_f32 v29,v29,v69
v_add_f32 v30,v30,v70
v_add_f32 v31,v31,v71
buffer_load_dwordx4 v1,s[36:39],s66 offen offset:2048 sc1 lds
v_cvt_pk_bf16_f32 v72,v24,v25
v_cvt_pk_bf16_f32 v73,v26,v27
v_cvt_pk_bf16_f32 v74,v28,v29
v_cvt_pk_bf16_f32 v75,v30,v31
buffer_store_dwordx4 v[72:75],v3,s[36:39],s73 offen offset:0 sc0 sc1
v_add_f32 v64,v24,v25
v_mul_f32 v65,v24,v24
v_fma_f32 v65,v25,v25,v65
buffer_load_dwordx4 v1,s[36:39],s66 offen offset:3072 sc1 lds
v_add_f32 v64,v64,v26
v_fma_f32 v65,v26,v26,v65
v_add_f32 v64,v64,v27
v_fma_f32 v65,v27,v27,v65
v_add_f32 v64,v64,v28
v_fma_f32 v65,v28,v28,v65
v_add_f32 v64,v64,v29
v_fma_f32 v65,v29,v29,v65
s_lshr_b32 s72,s66,6
s_lshr_b32 s71,s62,2
s_add_u32 m0,s71,s88
s_nop 0
buffer_load_dwordx4 v10,s[36:39],s66 offen offset:0 sc1 lds
v_add_f32 v64,v64,v30
v_fma_f32 v65,v30,v30,v65
v_add_f32 v64,v64,v31
v_fma_f32 v65,v31,v31,v65
s_nop 1
v_permlane16_swap_b32 v64,v65
v_add_f32 v64,v64,v65
v_mov_b32 v65,v64
s_nop 1
v_permlane32_swap_b32 v64,v65
buffer_load_dword v11,s[36:39],s72 offen offset:1024 sc1 lds
v_add_f32 v64,v64,v65
ds_write_b32 v13,v64 offset:0
s_waitcnt lgkmcnt(0)
s_barrier
ds_read_b128 v[76:79],v14 offset:0
s_waitcnt lgkmcnt(0)
v_add_f32 v76,v76,v77
v_add_f32 v78,v78,v79
v_add_f32 v76,v76,v78
buffer_store_dword v76,v12,s[36:39],s85 offen offset:0 sc0 sc1
s_waitcnt vmcnt(8)
s_barrier
ds_read_b32 v15,v7 offset:8192
ds_read_b128 v[32:35],v2 offset:0
ds_read_b128 v[36:39],v2 offset:1024
ds_read_b128 v[40:43],v2 offset:2048
ds_read_b128 v[44:47],v2 offset:3072
ds_read_b128 v[48:51],v2 offset:4096
ds_read_b128 v[52:55],v2 offset:5120
ds_read_b128 v[56:59],v2 offset:6144
s_lshr_b32 s70,s46,5
buffer_store_dword v5,v4,s[36:39],s70 offen offset:0 sc0 sc1
s_waitcnt lgkmcnt(7)
v_cmp_eq_u32 vcc,s60,v15)"
R"(
s_nop 0
s_cmp_eq_u32 vcc_hi,-1
s_cselect_b32 s66,s49,s59
s_xor_b32 s62,s64,65536
s_cmp_ge_u32 s66,s59
s_cselect_b32 s68,s49,s50
s_lshr_b32 s68,s68,5
s_add_u32 m0,s78,0
s_nop 0
buffer_load_dword v6,s[36:39],s68 offen offset:0 sc1 lds
s_mov_b32 m0,s62
s_nop 0
buffer_load_dwordx4 v0,s[36:39],s66 offen offset:0 sc1 lds
buffer_load_dwordx4 v0,s[36:39],s66 offen offset:1024 sc1 lds
buffer_load_dwordx4 v0,s[36:39],s66 offen offset:2048 sc1 lds
buffer_load_dwordx4 v0,s[36:39],s66 offen offset:3072 sc1 lds
s_mov_b32 s46,s47
s_mov_b32 s47,s59
s_lshr_b32 s70,s59,5
buffer_store_dword v5,v4,s[36:39],s70 offen offset:0 sc0 sc1
s_lshr_b32 s70,s59,5
buffer_store_dword v5,v4,s[36:39],s70 offen offset:0 sc0 sc1
s_waitcnt lgkmcnt(3)
s_setpc_b64 s[90:91]
Lg2_done0_%=:
s_waitcnt vmcnt(0)
s_barrier
s_lshr_b32 s70,s46,5
buffer_store_dword v5,v4,s[36:39],s70 offen offset:0 sc0 sc1
s_lshr_b32 s70,s47,5
buffer_store_dword v5,v4,s[36:39],s70 offen offset:0 sc0 sc1
s_branch Lg2_done_%=
Lg2_slow1_%=:
s_cmp_lt_u32 s67,s59
s_cbranch_scc1 Lg2_gs1_%=
Lg2_slowb1_%=:
s_cmp_eq_u32 s54,1
s_cselect_b32 s68,s50,s49
s_lshr_b32 s68,s68,5
s_add_u32 m0,s78,8192
s_nop 0
buffer_load_dword v6,s[36:39],s68 offen offset:0 sc1 lds
s_lshr_b32 s73,s48,0
s_lshr_b32 s85,s48,5
v_lshlrev_b32 v64,16,v80
v_and_b32 v65,0xffff0000,v80
v_lshlrev_b32 v66,16,v81
v_and_b32 v67,0xffff0000,v81
v_lshlrev_b32 v68,16,v82
v_and_b32 v69,0xffff0000,v82
v_lshlrev_b32 v70,16,v83
v_and_b32 v71,0xffff0000,v83
v_fma_f32 v64,v64,v89,v88
v_fma_f32 v65,v65,v89,v88
v_fma_f32 v66,v66,v89,v88
v_fma_f32 v67,v67,v89,v88
v_fma_f32 v68,v68,v89,v88
v_fma_f32 v69,v69,v89,v88
v_fma_f32 v70,v70,v89,v88
v_fma_f32 v71,v71,v89,v88
v_add_f32 v16,v16,v64
v_add_f32 v17,v17,v65
v_add_f32 v18,v18,v66
v_add_f32 v19,v19,v67
v_add_f32 v20,v20,v68
v_add_f32 v21,v21,v69
v_add_f32 v22,v22,v70
v_add_f32 v23,v23,v71
v_cvt_pk_bf16_f32 v72,v16,v17
v_cvt_pk_bf16_f32 v73,v18,v19
v_cvt_pk_bf16_f32 v74,v20,v21
v_cvt_pk_bf16_f32 v75,v22,v23
s_add_u32 m0,s78,8448
s_nop 0
buffer_load_dword v6,s[36:39],s68 offen offset:0 sc1 lds
buffer_store_dwordx4 v[72:75],v3,s[36:39],s73 offen offset:0 sc0 sc1
v_add_f32 v64,v16,v17
v_mul_f32 v65,v16,v16
v_fma_f32 v65,v17,v17,v65
v_add_f32 v64,v64,v18
v_fma_f32 v65,v18,v18,v65
v_add_f32 v64,v64,v19
v_fma_f32 v65,v19,v19,v65
v_add_f32 v64,v64,v20
v_fma_f32 v65,v20,v20,v65
v_add_f32 v64,v64,v21
v_fma_f32 v65,v21,v21,v65
v_add_f32 v64,v64,v22
v_fma_f32 v65,v22,v22,v65
v_add_f32 v64,v64,v23
v_fma_f32 v65,v23,v23,v65
s_nop 1
v_permlane16_swap_b32 v64,v65
v_add_f32 v64,v64,v65
v_mov_b32 v65,v64
s_nop 1
v_permlane32_swap_b32 v64,v65
v_add_f32 v64,v64,v65
ds_write_b32 v13,v64 offset:2048
s_waitcnt lgkmcnt(0)
s_barrier
ds_read_b128 v[76:79],v14 offset:2048
s_waitcnt lgkmcnt(0)
v_add_f32 v76,v76,v77
v_add_f32 v78,v78,v79
v_add_f32 v76,v76,v78
buffer_store_dword v76,v12,s[36:39],s85 offen offset:0 sc0 sc1
s_cmp_ge_u32 s49,s59
s_cbranch_scc1 Lg2_done1_%=
s_add_u32 m0,s78,16384
s_nop 0
buffer_load_dword v6,s[36:39],s68 offen offset:0 sc1 lds
s_waitcnt vmcnt(4)
s_barrier
ds_read_b32 v15,v7 offset:8192
s_lshr_b32 s70,s47,5
buffer_store_dword v5,v4,s[36:39],s70 offen offset:0 sc0 sc1
s_mov_b32 s47,s59
s_mov_b32 s69,18432
s_waitcnt lgkmcnt(0)
v_cmp_eq_u32 vcc,s60,v15
s_nop 0
s_cmp_eq_u32 vcc_lo,-1
s_cbranch_scc1 Lg2_go1_%=
s_add_u32 m0,s78,16640
s_nop 0
buffer_load_dword v6,s[36:39],s68 offen offset:0 sc1 lds
s_waitcnt vmcnt(5)
s_barrier
ds_read_b32 v15,v7 offset:8448
s_mov_b32 s69,18688
s_waitcnt lgkmcnt(0)
v_cmp_eq_u32 vcc,s60,v15
s_nop 0
s_cmp_eq_u32 vcc_lo,-1
s_cbranch_scc1 Lg2_go1_%=
s_add_u32 m0,s78,8192
s_nop 0
buffer_load_dword v6,s[36:39],s68 offen offset:0 sc1 lds
s_waitcnt vmcnt(3)
s_barrier
ds_read_b32 v15,v7 offset:16384
s_lshr_b32 s70,s48,5
buffer_store_dword v5,v4,s[36:39],s70 offen offset:0 sc0 sc1
s_mov_b32 s48,s59
s_mov_b32 s69,10240
s_waitcnt lgkmcnt(0)
v_cmp_eq_u32 vcc,s60,v15
s_nop 0
s_cmp_eq_u32 vcc_lo,-1
s_cbranch_scc1 Lg2_go1_%=
s_add_u32 m0,s78,8448
s_nop 0
buffer_load_dword v6,s[36:39],s68 offen offset:0 sc1 lds
s_waitcnt vmcnt(3)
s_barrier
ds_read_b32 v15,v7 offset:16640
s_mov_b32 s69,10496
s_waitcnt lgkmcnt(0)
v_cmp_eq_u32 vcc,s60,v15
s_nop 0
s_cmp_eq_u32 vcc_lo,-1
s_cbranch_scc1 Lg2_go1_%=
Lg2_poll1_%=:
s_add_u32 m0,s78,16384
s_nop 0
buffer_load_dword v6,s[36:39],s68 offen offset:0 sc1 lds
s_waitcnt vmcnt(2)
s_barrier
ds_read_b32 v15,v7 offset:8192
s_mov_b32 s69,18432
s_waitcnt lgkmcnt(0)
v_cmp_eq_u32 vcc,s60,v15
s_nop 0
s_cmp_eq_u32 vcc_lo,-1
s_cbranch_scc1 Lg2_go1_%=
s_sleep 4
s_add_u32 m0,s78,16640
s_nop 0
buffer_load_dword v6,s[36:39],s68 offen offset:0 sc1 lds
s_waitcnt vmcnt(2)
s_barrier
ds_read_b32 v15,v7 offset:8448
s_mov_b32 s69,18688
s_waitcnt lgkmcnt(0)
v_cmp_eq_u32 vcc,s60,v15
s_nop 0
s_cmp_eq_u32 vcc_lo,-1
s_cbranch_scc1 Lg2_go1_%=
s_sleep 4
s_add_u32 m0,s78,8192
s_nop 0
buffer_load_dword v6,s[36:39],s68 offen offset:0 sc1 lds
s_waitcnt vmcnt(2)
s_barrier
ds_read_b32 v15,v7 offset:16384
s_mov_b32 s69,10240
s_waitcnt lgkmcnt(0)
v_cmp_eq_u32 vcc,s60,v15
s_nop 0)"
R"(
s_cmp_eq_u32 vcc_lo,-1
s_cbranch_scc1 Lg2_go1_%=
s_sleep 4
s_add_u32 m0,s78,8448
s_nop 0
buffer_load_dword v6,s[36:39],s68 offen offset:0 sc1 lds
s_waitcnt vmcnt(2)
s_barrier
ds_read_b32 v15,v7 offset:16640
s_mov_b32 s69,10496
s_waitcnt lgkmcnt(0)
v_cmp_eq_u32 vcc,s60,v15
s_nop 0
s_cmp_eq_u32 vcc_lo,-1
s_cbranch_scc1 Lg2_go1_%=
s_sleep 4
s_add_u32 s79,s79,1
s_cmp_lt_u32 s79,20000
s_cbranch_scc1 Lg2_poll1_%=
Lg2_go1_%=:
s_xor_b32 s62,s64,65536
s_mov_b32 m0,s62
s_nop 0
buffer_load_dwordx4 v0,s[36:39],s49 offen offset:0 sc1 lds
buffer_load_dwordx4 v0,s[36:39],s49 offen offset:1024 sc1 lds
buffer_load_dwordx4 v0,s[36:39],s49 offen offset:2048 sc1 lds
buffer_load_dwordx4 v0,s[36:39],s49 offen offset:3072 sc1 lds
s_add_u32 m0,s62,16384
s_nop 0
buffer_load_dwordx4 v1,s[36:39],s49 offen offset:0 sc1 lds
buffer_load_dwordx4 v1,s[36:39],s49 offen offset:1024 sc1 lds
buffer_load_dwordx4 v1,s[36:39],s49 offen offset:2048 sc1 lds
buffer_load_dwordx4 v1,s[36:39],s49 offen offset:3072 sc1 lds
s_lshr_b32 s72,s49,6
s_lshr_b32 s71,s62,2
s_add_u32 m0,s71,s88
s_nop 0
buffer_load_dwordx4 v10,s[36:39],s49 offen offset:0 sc1 lds
buffer_load_dword v11,s[36:39],s72 offen offset:1024 sc1 lds
s_cmp_eq_u32 vcc_hi,-1
s_cselect_b32 s67,s50,s59
s_xor_b32 s62,s63,65536
s_cmp_ge_u32 s67,s59
s_cselect_b32 s68,s50,s51
s_lshr_b32 s68,s68,5
s_add_u32 m0,s78,256
s_nop 0
buffer_load_dword v6,s[36:39],s68 offen offset:0 sc1 lds
s_mov_b32 m0,s62
s_nop 0
buffer_load_dwordx4 v0,s[36:39],s67 offen offset:0 sc1 lds
buffer_load_dwordx4 v0,s[36:39],s67 offen offset:1024 sc1 lds
buffer_load_dwordx4 v0,s[36:39],s67 offen offset:2048 sc1 lds
buffer_load_dwordx4 v0,s[36:39],s67 offen offset:3072 sc1 lds
s_waitcnt vmcnt(11)
s_barrier
ds_read_b128 v[32:35],v2 offset:32768
ds_read_b128 v[36:39],v2 offset:33792
ds_read_b128 v[40:43],v2 offset:34816
ds_read_b128 v[44:47],v2 offset:35840
ds_read_b128 v[48:51],v2 offset:36864
ds_read_b128 v[52:55],v2 offset:37888
ds_read_b128 v[56:59],v2 offset:38912
s_lshr_b32 s70,s47,5
buffer_store_dword v5,v4,s[36:39],s70 offen offset:0 sc0 sc1
s_mov_b32 s47,s59
s_lshr_b32 s70,s48,5
buffer_store_dword v5,v4,s[36:39],s70 offen offset:0 sc0 sc1
s_mov_b32 s48,s59
s_lshr_b32 s70,s59,5
buffer_store_dword v5,v4,s[36:39],s70 offen offset:0 sc0 sc1
s_waitcnt lgkmcnt(3)
s_setpc_b64 s[92:93]
Lg2_gs1_%=:
s_cmp_eq_u32 s54,1
s_cselect_b32 s68,s50,s49
s_lshr_b32 s68,s68,5
s_lshr_b32 s73,s48,0
s_lshr_b32 s85,s48,5
s_add_u32 m0,s78,8192
s_nop 0
buffer_load_dword v6,s[36:39],s68 offen offset:0 sc1 lds
v_lshlrev_b32 v64,16,v80
v_and_b32 v65,0xffff0000,v80
v_lshlrev_b32 v66,16,v81
v_and_b32 v67,0xffff0000,v81
v_lshlrev_b32 v68,16,v82
v_and_b32 v69,0xffff0000,v82
v_lshlrev_b32 v70,16,v83
v_and_b32 v71,0xffff0000,v83
s_add_u32 m0,s62,16384
s_nop 0
buffer_load_dwordx4 v1,s[36:39],s67 offen offset:0 sc1 lds
v_fma_f32 v64,v64,v89,v88
v_fma_f32 v65,v65,v89,v88
v_fma_f32 v66,v66,v89,v88
v_fma_f32 v67,v67,v89,v88
v_fma_f32 v68,v68,v89,v88
v_fma_f32 v69,v69,v89,v88
v_fma_f32 v70,v70,v89,v88
v_fma_f32 v71,v71,v89,v88
buffer_load_dwordx4 v1,s[36:39],s67 offen offset:1024 sc1 lds
v_add_f32 v16,v16,v64
v_add_f32 v17,v17,v65
v_add_f32 v18,v18,v66
v_add_f32 v19,v19,v67
v_add_f32 v20,v20,v68
v_add_f32 v21,v21,v69
v_add_f32 v22,v22,v70
v_add_f32 v23,v23,v71
buffer_load_dwordx4 v1,s[36:39],s67 offen offset:2048 sc1 lds
v_cvt_pk_bf16_f32 v72,v16,v17
v_cvt_pk_bf16_f32 v73,v18,v19
v_cvt_pk_bf16_f32 v74,v20,v21
v_cvt_pk_bf16_f32 v75,v22,v23
buffer_store_dwordx4 v[72:75],v3,s[36:39],s73 offen offset:0 sc0 sc1
v_add_f32 v64,v16,v17
v_mul_f32 v65,v16,v16
v_fma_f32 v65,v17,v17,v65
buffer_load_dwordx4 v1,s[36:39],s67 offen offset:3072 sc1 lds
v_add_f32 v64,v64,v18
v_fma_f32 v65,v18,v18,v65
v_add_f32 v64,v64,v19
v_fma_f32 v65,v19,v19,v65
v_add_f32 v64,v64,v20
v_fma_f32 v65,v20,v20,v65
v_add_f32 v64,v64,v21
v_fma_f32 v65,v21,v21,v65
s_lshr_b32 s72,s67,6
s_lshr_b32 s71,s62,2
s_add_u32 m0,s71,s88
s_nop 0
buffer_load_dwordx4 v10,s[36:39],s67 offen offset:0 sc1 lds
v_add_f32 v64,v64,v22
v_fma_f32 v65,v22,v22,v65
v_add_f32 v64,v64,v23
v_fma_f32 v65,v23,v23,v65
s_nop 1
v_permlane16_swap_b32 v64,v65
v_add_f32 v64,v64,v65
v_mov_b32 v65,v64
s_nop 1
v_permlane32_swap_b32 v64,v65
buffer_load_dword v11,s[36:39],s72 offen offset:1024 sc1 lds
v_add_f32 v64,v64,v65
ds_write_b32 v13,v64 offset:2048
s_waitcnt lgkmcnt(0)
s_barrier
ds_read_b128 v[76:79],v14 offset:2048
s_waitcnt lgkmcnt(0)
v_add_f32 v76,v76,v77
v_add_f32 v78,v78,v79
v_add_f32 v76,v76,v78
buffer_store_dword v76,v12,s[36:39],s85 offen offset:0 sc0 sc1
s_waitcnt vmcnt(8)
s_barrier
ds_read_b32 v15,v7 offset:8192
ds_read_b128 v[32:35],v2 offset:32768
ds_read_b128 v[36:39],v2 offset:33792
ds_read_b128 v[40:43],v2 offset:34816
ds_read_b128 v[44:47],v2 offset:35840
ds_read_b128 v[48:51],v2 offset:36864
ds_read_b128 v[52:55],v2 offset:37888
ds_read_b128 v[56:59],v2 offset:38912
s_lshr_b32 s70,s47,5
buffer_store_dword v5,v4,s[36:39],s70 offen offset:0 sc0 sc1
s_waitcnt lgkmcnt(7)
v_cmp_eq_u32 vcc,s60,v15
s_nop 0
s_cmp_eq_u32 vcc_hi,-1
s_cselect_b32 s67,s50,s59
s_xor_b32 s62,s63,65536
s_cmp_ge_u32 s67,s59
s_cselect_b32 s68,s50,s51
s_lshr_b32 s68,s68,5
s_add_u32 m0,s78,256
s_nop 0
buffer_load_dword v6,s[36:39],s68 offen offset:0 sc1 lds
s_mov_b32 m0,s62
s_nop 0
buffer_load_dwordx4 v0,s[36:39],s67 offen offset:0 sc1 lds
buffer_load_dwordx4 v0,s[36:39],s67 offen offset:1024 sc1 lds
buffer_load_dwordx4 v0,s[36:39],s67 offen offset:2048 sc1 lds
buffer_load_dwordx4 v0,s[36:39],s67 offen offset:3072 sc1 lds
s_mov_b32 s47,s48
s_mov_b32 s48,s59
s_lshr_b32 s70,s59,5
buffer_store_dword v5,v4,s[36:39],s70 offen offset:0 sc0 sc1
s_lshr_b32 s70,s59,5
buffer_store_dword v5,v4,s[36:39],s70 offen offset:0 sc0 sc1
s_waitcnt lgkmcnt(3)
s_setpc_b64 s[92:93]
Lg2_done1_%=:
s_waitcnt vmcnt(0)
s_barrier
s_lshr_b32 s70,s47,5
buffer_store_dword v5,v4,s[36:39],s70 offen offset:0 sc0 sc1
s_lshr_b32 s70,s48,5
buffer_store_dword v5,v4,s[36:39],s70 offen offset:0 sc0 sc1
s_branch Lg2_done_%=)"
R"(
Lg2_done_%=:
s_waitcnt vmcnt(0) lgkmcnt(0)
s_barrier
Lg2_end_%=:
s_mov_b32 %0,s60
Lcode_g12_e:)"
  : "=&s"(epOut) : "s"(ws), "s"(wave), "s"(sStep), "s"(sX1), "s"(sX2), "s"(sU0), "s"(sWw), "s"(sBuf), "v"(vSrc), "v"(vSrc2), "v"(vA), "v"(vOut), "v"(vFlag), "v"(vPollOff), "v"(vSnapRd), "v"(vX), "v"(vX2), "v"(vAuxS), "v"(vAuxS2), "v"(vOut2), "v"(vStW), "v"(vStR), "v"(vWd0), "v"(vWd1), "v"(vWr0), "v"(vWr1), "v"(vWr2), "s"(w12) : "v0", "v1", "v2", "v3", "v4", "v5", "v6", "v7", "v8", "v9", "v10", "v11", "v12", "v13", "v14", "v15", "v16", "v17", "v18", "v19", "v20", "v21", "v22", "v23", "v24", "v25", "v26", "v27", "v28", "v29", "v30", "v31", "v32", "v33", "v34", "v35", "v36", "v37", "v38", "v39", "v40", "v41", "v42", "v43", "v44", "v45", "v46", "v47", "v48", "v49", "v50", "v51", "v52", "v53", "v54", "v55", "v56", "v57", "v58", "v59", "v60", "v61", "v62", "v63", "v64", "v65", "v66", "v67", "v68", "v69", "v70", "v71", "v72", "v73", "v74", "v75", "v76", "v77", "v78", "v79", "v80", "v81", "v82", "v83", "v84", "v85", "v86", "v87", "v88", "v89", "v90", "v91", "v92", "v93", "v94", "v95", "v96", "v97", "v98", "v99", "v100", "v101", "v102", "v103", "v104", "v105", "v106", "v107", "v108", "v109", "v110", "v111", "v112", "v113", "v114", "v115", "v116", "v117", "v118", "v119", "v120", "v121", "v122", "v123", "v124", "v125", "v126", "v127", "v128", "v129", "v130", "v131", "v132", "v133", "v134", "v135", "v136", "v137", "v138", "v139", "v140", "v141", "v142", "v143", "v144", "v145", "v146", "v147", "v148", "v149", "v150", "v151", "v152", "v153", "v154", "v155", "v156", "v157", "v158", "v159", "v160", "v161", "v162", "v163", "v164", "v165", "v166", "v167", "v168", "v169", "v170", "v171", "v172", "v173", "v174", "v175", "v176", "v177", "v178", "v179", "v180", "v181", "v182", "v183", "v184", "v185", "v186", "v187", "v188", "v189", "v190", "v191", "v192", "v193", "v194", "v195", "v196", "v197", "v198", "v199", "v200", "v201", "v202", "v203", "v204", "v205", "v206", "v207", "v208", "v209", "v210", "v211", "v212", "v213", "v214", "v215", "v216", "v217", "v218", "v219", "v220", "v221", "v222", "v223", "v224", "v225", "v226", "v227", "v228", "v229", "v230", "v231", "v255", "a0", "a1", "a2", "a3", "a4", "a5", "a6", "a7", "a8", "a9", "a10", "a11", "a12", "a13", "a14", "a15", "a16", "a17", "a18", "a19", "a20", "a21", "a22", "a23", "a24", "a25", "a26", "a27", "a28", "a29", "a30", "a31", "a32", "a33", "a34", "a35", "a36", "a37", "a38", "a39", "a40", "a41", "a42", "a43", "a44", "a45", "a46", "a47", "a48", "a49", "a50", "a51", "a52", "a53", "a54", "a55", "a56", "a57", "a58", "a59", "a60", "a61", "a62", "a63", "a64", "a65", "a66", "a67", "a68", "a69", "a70", "a71", "a72", "a73", "a74", "a75", "a76", "a77", "a78", "a79", "a80", "a81", "a82", "a83", "a84", "a85", "a86", "a87", "a88", "a89", "a90", "a91", "a92", "a93", "a94", "a95", "a96", "a97", "a98", "a99", "a100", "a101", "a102", "a103", "a104", "a105", "a106", "a107", "a108", "a109", "a110", "a111", "a112", "a113", "a114", "a115", "a116", "a117", "a118", "a119", "a120", "a121", "a122", "a123", "a124", "a125", "a126", "a127", "a128", "a129", "a130", "a131", "a132", "a133", "a134", "a135", "a136", "a137", "a138", "a139", "a140", "a141", "a142", "a143", "a144", "a145", "a146", "a147", "a148", "a149", "a150", "a151", "a152", "a153", "a154", "a155", "a156", "a157", "a158", "a159", "a160", "a161", "a162", "a163", "a164", "a165", "a166", "a167", "a168", "a169", "a170", "a171", "a172", "a173", "a174", "a175", "a176", "a177", "a178", "a179", "a180", "a181", "a182", "a183", "a184", "a185", "a186", "a187", "a188", "a189", "a190", "a191", "a192", "a193", "a194", "a195", "a196", "a197", "a198", "a199", "a200", "a201", "a202", "a203", "a204", "a205", "a206", "a207", "a208", "a209", "a210", "a211", "a212", "a213", "a214", "a215", "a216", "a217", "a218", "a219", "a220", "a221", "a222", "a223", "a224", "a225", "a226", "a227", "a228", "a229", "a230", "a231", "a232", "a233", "a234", "a235", "a236", "a237", "a238", "a239", "a240", "a241", "a242", "a243", "a244", "a245", "a246", "a247", "a248", "a249", "a250", "a251", "a252", "a253", "a254", "a255", "s36", "s37", "s38", "s39", "s40", "s41", "s42", "s43", "s44", "s45", "s46", "s47", "s48", "s49", "s50", "s51", "s52", "s53", "s54", "s55", "s56", "s57", "s58", "s59", "s60", "s61", "s62", "s63", "s64", "s65", "s66", "s67", "s68", "s69", "s70", "s71", "s72", "s73", "s74", "s75", "s76", "s77", "s78", "s79", "s80", "s81", "s82", "s83", "s84", "s85", "s86", "s87", "s88", "s89", "s90", "s91", "s92", "s93", "s94", "s95", "s96", "s97", "s98", "s99", "vcc", "scc", "memory");
    ep = epOut;
   }
    if (kind == 6)
    {
      LANE_ID(lr);
      wait_flags(ws + OFF_FXP + 7 * 16384 + wave * 1024, lr, 8, ep);
      const int r8 = lr & 7, seg = lr >> 3;
      const unsigned sEpR = __builtin_amdgcn_readfirstlane(ep), sK = slice, sStep = NRW, sK1 = RT1 + slice, sRq = OFF_FXP + LB * 2048 + wave * 128, sDbg = sDbg0 + (kind == 6 ? 0 : 1024);
      const unsigned vRF = (wave * 8 + (seg >> 2)) * 1024 + ((seg & 3) * 16 + r8) * 16;
      const unsigned vRwr = sLds + (seg == 0 ? r8 * 64 + wave * 4 : 512 + lr * 4), vRrd = sLds + r8 * 64;
      const unsigned vRP = sLds + ((lr >> 4) == ((lr & 15) >> 2) ? r8 * 64 + 16 + wave * 8 + (seg & 1) * 4 : 768 + lr * 4);
      const unsigned vRS = (wave == 0 && seg == 0) ? r8 * 16 : 0x80000000u, vFlag = lr == 0 ? 0u : 0x80000000u, vL4 = (lr & 15) * 128, vRL = wave * 4096 + lr * 16;
      asm volatile(R"(
Lcode_r_b:
s_mov_b64 s[36:37],%0
s_mov_b32 s38,122727552
s_mov_b32 s39,0x00020000
s_mov_b32 s54,%1
s_mov_b32 s50,%2
s_mov_b32 s92,%3
s_mov_b32 s48,%4
v_mov_b32 v0,%5
v_mov_b32 v1,%6
v_mov_b32 v2,%7
v_mov_b32 v3,%8
v_mov_b32 v4,%9
v_mov_b32 v5,%10
v_mov_b32 v6,%11
v_mov_b32 v11,%12
v_lshrrev_b32 v10,1,v4
v_mov_b32 v7,s48
v_mbcnt_lo_u32_b32 v9,-1,0
v_mbcnt_hi_u32_b32 v9,-1,v9
v_lshlrev_b32 v9,4,v9
s_mov_b32 s74,0
s_mov_b32 s96,0
s_mov_b32 s60,536870912
s_mov_b32 s46,0xaaaaaaaa
s_mov_b32 s47,0xaaaaaaaa
s_mov_b32 s56,0xcccccccc
s_mov_b32 s57,0xcccccccc
s_mov_b32 s76,0x3a800000
s_mov_b32 s77,0x3727c5ac
s_mov_b32 s78,0xc0135761
s_mov_b32 s79,0x3d372713
v_mov_b32 v192,0x3f803f80
v_mov_b32 v193,0x3f803f80
v_mov_b32 v194,0x3f803f80
v_mov_b32 v195,0x3f803f80
s_lshr_b32 s63,s54,1
s_and_b32 s64,s54,1
s_lshl_b32 s64,s64,7
s_lshl_b32 s87,s54,14
s_lshl_b32 s58,s63,15
s_add_u32 s58,s58,s64
s_add_u32 s58,s58,16908288
s_lshl_b32 s59,s63,9
s_add_u32 s59,s59,s64
s_add_u32 s59,s59,118556672
s_lshl_b32 s49,s54,9
s_add_u32 s49,s49,s92
s_lshl_b32 s91,s63,11
s_add_u32 s91,s91,121405440
s_lshl_b32 s69,s50,10
s_add_u32 s69,s69,s91
v_mov_b32 v20,0
v_mov_b32 v21,0
v_mov_b32 v22,0
v_mov_b32 v23,0
v_mov_b32 v24,0
v_mov_b32 v25,0
v_mov_b32 v26,0
v_mov_b32 v27,0
v_mov_b32 v16,0x3f80
v_mov_b32 v17,0x3f800000
v_cndmask_b32 v16,v16,v17,s[46:47]
s_mov_b32 s98,0x00300003
s_mov_b32 s99,0x30000300
v_cndmask_b32 v20,v20,v16,s[98:99]
s_lshl_b64 s[98:99],s[98:99],2
v_cndmask_b32 v21,v21,v16,s[98:99]
v_mov_b32 v26,v20
v_mov_b32 v27,v21
v_readfirstlane_b32 s52,v11
s_add_u32 s51,s87,8388608
s_mov_b32 s66,4096
s_nop 3
Lr_yp_%=:
s_add_u32 m0,s66,s52
s_cmp_ge_u32 s51,12582912
s_cbranch_scc1 Lr_ys1_%=
buffer_load_dwordx4 v11,s[36:39],s51 offen offset:0 sc1 lds
buffer_load_dwordx4 v11,s[36:39],s51 offen offset:1024 sc1 lds
buffer_load_dwordx4 v11,s[36:39],s51 offen offset:2048 sc1 lds
buffer_load_dwordx4 v11,s[36:39],s51 offen offset:3072 sc1 lds
s_lshl_b32 s63,s50,14
s_add_u32 s51,s51,s63
Lr_ys1_%=:
s_add_u32 s66,s66,16384
s_cmp_lt_u32 s66,151552
s_cbranch_scc1 Lr_yp_%=
s_mov_b32 s66,4096
v_add_u32 v18,4096,v11
Lr_bp9_%=:
buffer_load_dword v8,v6,s[36:39],s91 offen offset:0 sc1
s_waitcnt vmcnt(0)
v_cmp_eq_u32 vcc,s48,v8
s_nop 0
s_cmp_eq_u64 vcc,exec
s_cselect_b32 s63,1,s96
s_cmp_lg_u32 s63,0
s_cbranch_scc1 Lr_bq9_%=
s_add_u32 s74,s74,1
s_cmp_ge_u32 s74,20000
s_cselect_b32 s96,1,0
s_sleep 4
s_branch Lr_bp9_%=
Lr_bq9_%=:
s_add_u32 s63,s87,71303168
buffer_load_dwordx4 a[0:3],v11,s[36:39],s63 offen offset:0 sc1
buffer_load_dwordx4 a[4:7],v11,s[36:39],s63 offen offset:1024 sc1
buffer_load_dwordx4 a[8:11],v11,s[36:39],s63 offen offset:2048 sc1
buffer_load_dwordx4 a[12:15],v11,s[36:39],s63 offen offset:3072 sc1
s_add_u32 s63,s87,89128960
buffer_load_dwordx4 a[16:19],v11,s[36:39],s63 offen offset:0 sc1
buffer_load_dwordx4 a[20:23],v11,s[36:39],s63 offen offset:1024 sc1
buffer_load_dwordx4 a[24:27],v11,s[36:39],s63 offen offset:2048 sc1
buffer_load_dwordx4 a[28:31],v11,s[36:39],s63 offen offset:3072 sc1
s_add_u32 s63,s87,99614720
buffer_load_dwordx4 a[32:35],v11,s[36:39],s63 offen offset:0 sc1
buffer_load_dwordx4 a[36:39],v11,s[36:39],s63 offen offset:1024 sc1
buffer_load_dwordx4 a[40:43],v11,s[36:39],s63 offen offset:2048 sc1
buffer_load_dwordx4 a[44:47],v11,s[36:39],s63 offen offset:3072 sc1
s_add_u32 s63,s87,109051904
buffer_load_dwordx4 a[48:51],v11,s[36:39],s63 offen offset:0 sc1
buffer_load_dwordx4 a[52:55],v11,s[36:39],s63 offen offset:1024 sc1
buffer_load_dwordx4 a[56:59],v11,s[36:39],s63 offen offset:2048 sc1
buffer_load_dwordx4 a[60:63],v11,s[36:39],s63 offen offset:3072 sc1
buffer_load_dword v8,v6,s[36:39],s69 offen offset:0 sc1
s_waitcnt vmcnt(0)
Lr_it0_%=:
s_mov_b32 s88,s58
s_mov_b32 s89,s59
s_mov_b32 s90,s49
s_add_u32 s54,s54,s50
s_lshr_b32 s63,s54,1
s_and_b32 s64,s54,1
s_lshl_b32 s64,s64,7
s_lshl_b32 s87,s54,14
s_lshl_b32 s58,s63,15
s_add_u32 s58,s58,s64
s_add_u32 s58,s58,16908288
s_lshl_b32 s59,s63,9
s_add_u32 s59,s59,s64
s_add_u32 s59,s59,118556672
s_lshl_b32 s49,s54,9
s_add_u32 s49,s49,s92
s_lshl_b32 s91,s63,11
s_add_u32 s91,s91,121405440
s_lshl_b32 s69,s50,10
s_add_u32 s69,s69,s91
v_cmp_eq_u32 vcc,s48,v8
s_nop 0
s_cmp_eq_u64 vcc,exec
s_cselect_b32 s63,s54,256
s_cmp_lt_u32 s63,256
s_cbranch_scc1 Lr_ta0_%=
ds_read_b128 v[32:35],v18 offset:0
ds_read_b128 v[36:39],v18 offset:1024
ds_read_b128 v[40:43],v18 offset:2048
ds_read_b128 v[44:47],v18 offset:3072
v_mfma_f32_16x16x32_bf16 v[128:131],v[20:23],a[0:3],0
v_mfma_f32_16x16x32_bf16 v[132:135],v[24:27],a[0:3],0
v_mfma_f32_16x16x32_bf16 v[128:131],v[20:23],a[16:19],v[128:131]
v_mfma_f32_16x16x32_bf16 v[132:135],v[24:27],a[16:19],v[132:135]
v_mfma_f32_16x16x32_bf16 v[128:131],v[20:23],a[32:35],v[128:131]
v_mfma_f32_16x16x32_bf16 v[132:135],v[24:27],a[32:35],v[132:135]
v_mfma_f32_16x16x32_bf16 v[128:131],v[20:23],a[48:51],v[128:131]
v_mfma_f32_16x16x32_bf16 v[132:135],v[24:27],a[48:51],v[132:135]
v_mfma_f32_16x16x32_bf16 v[136:139],v[20:23],a[4:7],0
v_mfma_f32_16x16x32_bf16 v[140:143],v[24:27],a[4:7],0
v_mfma_f32_16x16x32_bf16 v[136:139],v[20:23],a[20:23],v[136:139]
v_mfma_f32_16x16x32_bf16 v[140:143],v[24:27],a[20:23],v[140:143]
v_mfma_f32_16x16x32_bf16 v[136:139],v[20:23],a[36:39],v[136:139]
s_nop 1
v_cvt_pk_bf16_f32 v208,v128,v129
v_cvt_pk_bf16_f32 v209,v130,v131
v_mfma_f32_16x16x32_bf16 v[140:143],v[24:27],a[36:39],v[140:143]
v_cvt_pk_bf16_f32 v210,v132,v133
v_cvt_pk_bf16_f32 v211,v134,v135
v_mfma_f32_16x16x32_bf16 v[136:139],v[20:23],a[52:55],v[136:139]
v_mfma_f32_16x16x32_bf16 v[140:143],v[24:27],a[52:55],v[140:143]
v_mfma_f32_16x16x32_bf16 v[144:147],v[20:23],a[8:11],0
v_mfma_f32_16x16x32_bf16 v[148:151],v[24:27],a[8:11],0
v_mfma_f32_16x16x32_bf16 v[144:147],v[20:23],a[24:27],v[144:147]
v_mfma_f32_16x16x32_bf16 v[148:151],v[24:27],a[24:27],v[148:151]
v_mfma_f32_16x16x32_bf16 v[144:147],v[20:23],a[40:43],v[144:147]
s_nop 1
v_cvt_pk_bf16_f32 v212,v136,v137
v_cvt_pk_bf16_f32 v213,v138,v139
v_mfma_f32_16x16x32_bf16 v[148:151],v[24:27],a[40:43],v[148:151]
v_cvt_pk_bf16_f32 v214,v140,v141
v_cvt_pk_bf16_f32 v215,v142,v143
v_mfma_f32_16x16x32_bf16 v[144:147],v[20:23],a[56:59],v[144:147]
v_mfma_f32_16x16x32_bf16 v[148:151],v[24:27],a[56:59],v[148:151]
v_mfma_f32_16x16x32_bf16 v[28:31],v[192:195],v[208:211],0
v_mfma_f32_16x16x32_bf16 v[204:207],v[208:211],v[208:211],0
v_mfma_f32_16x16x32_bf16 v[152:155],v[20:23],a[12:15],0
v_mfma_f32_16x16x32_bf16 v[156:159],v[24:27],a[12:15],0
v_mfma_f32_16x16x32_bf16 v[152:155],v[20:23],a[28:31],v[152:155]
v_mfma_f32_16x16x32_bf16 v[156:159],v[24:27],a[28:31],v[156:159]
v_mfma_f32_16x16x32_bf16 v[152:155],v[20:23],a[44:47],v[152:155]
v_cvt_pk_bf16_f32 v216,v144,v145)"
R"(
v_cvt_pk_bf16_f32 v217,v146,v147
v_mfma_f32_16x16x32_bf16 v[156:159],v[24:27],a[44:47],v[156:159]
v_cvt_pk_bf16_f32 v218,v148,v149
v_cvt_pk_bf16_f32 v219,v150,v151
v_mfma_f32_16x16x32_bf16 v[152:155],v[20:23],a[60:63],v[152:155]
v_mfma_f32_16x16x32_bf16 v[156:159],v[24:27],a[60:63],v[156:159]
v_mfma_f32_16x16x32_bf16 v[28:31],v[192:195],v[212:215],v[28:31]
v_mfma_f32_16x16x32_bf16 v[204:207],v[212:215],v[212:215],v[204:207]
v_mfma_f32_16x16x32_bf16 v[28:31],v[192:195],v[216:219],v[28:31]
v_mfma_f32_16x16x32_bf16 v[204:207],v[216:219],v[216:219],v[204:207]
s_waitcnt lgkmcnt(0)
v_mfma_f32_16x16x32_bf16 v[160:163],v[20:23],v[32:35],0
s_nop 0
v_cvt_pk_bf16_f32 v220,v152,v153
v_cvt_pk_bf16_f32 v221,v154,v155
v_mfma_f32_16x16x32_bf16 v[164:167],v[24:27],v[32:35],0
v_cvt_pk_bf16_f32 v222,v156,v157
v_cvt_pk_bf16_f32 v223,v158,v159
v_mfma_f32_16x16x32_bf16 v[168:171],v[20:23],v[36:39],0
s_nop 0
v_mfma_f32_16x16x32_bf16 v[28:31],v[192:195],v[220:223],v[28:31]
v_mfma_f32_16x16x32_bf16 v[204:207],v[220:223],v[220:223],v[204:207]
v_mfma_f32_16x16x32_bf16 v[172:175],v[24:27],v[36:39],0
v_mfma_f32_16x16x32_bf16 v[176:179],v[20:23],v[40:43],0
v_mfma_f32_16x16x32_bf16 v[180:183],v[24:27],v[40:43],0
s_nop 3
v_add_f32_dpp v224,v28,v28 row_ror:8 row_mask:0xf bank_mask:0xf
v_cndmask_b32 v225,v204,v205,s[46:47]
v_cndmask_b32 v226,v206,v207,s[46:47]
v_cndmask_b32 v225,v225,v226,s[56:57]
v_mfma_f32_16x16x32_bf16 v[184:187],v[20:23],v[44:47],0
ds_write_b32 v2,v224 offset:0
ds_write_b32 v0,v225 offset:0
v_mfma_f32_16x16x32_bf16 v[188:191],v[24:27],v[44:47],0
s_waitcnt lgkmcnt(0)
s_barrier
ds_read_b128 v[196:199],v3 offset:0
ds_read_b128 v[200:203],v3 offset:16
ds_read_b128 v[204:207],v3 offset:32
s_waitcnt lgkmcnt(0)
v_add_f32 v196,v196,v197
v_add_f32 v198,v198,v199
v_add_f32 v200,v200,v204
v_add_f32 v201,v201,v205
v_add_f32 v202,v202,v206
v_add_f32 v203,v203,v207
v_add_f32 v196,v196,v198
v_add_f32 v200,v200,v201
v_add_f32 v202,v202,v203
v_mul_f32 v196,s76,v196
v_add_f32 v197,v200,v202
v_mul_f32 v197,s76,v197
v_fma_f32 v197,-v196,v196,v197
v_max_f32 v197,0,v197
v_add_f32 v197,s77,v197
v_rsq_f32 v228,v197
s_nop 0
v_mul_f32_e64 v230,-v196,v228
v_mov_b32 v229,v228
v_mov_b32 v231,v230
v_pk_fma_f32 v[196:197],v[128:129],v[228:229],v[230:231]
v_pk_fma_f32 v[198:199],v[130:131],v[228:229],v[230:231]
v_pk_fma_f32 v[200:201],v[132:133],v[228:229],v[230:231]
v_pk_fma_f32 v[202:203],v[134:135],v[228:229],v[230:231]
v_pk_fma_f32 v[128:129],v[196:197],v[160:161],v[128:129]
v_pk_fma_f32 v[130:131],v[198:199],v[162:163],v[130:131]
v_pk_fma_f32 v[132:133],v[200:201],v[164:165],v[132:133]
v_pk_fma_f32 v[134:135],v[202:203],v[166:167],v[134:135]
v_pk_fma_f32 v[196:197],v[136:137],v[228:229],v[230:231]
v_pk_fma_f32 v[198:199],v[138:139],v[228:229],v[230:231]
v_pk_fma_f32 v[200:201],v[140:141],v[228:229],v[230:231]
v_pk_fma_f32 v[202:203],v[142:143],v[228:229],v[230:231]
v_pk_fma_f32 v[136:137],v[196:197],v[168:169],v[136:137]
v_pk_fma_f32 v[138:139],v[198:199],v[170:171],v[138:139]
v_pk_fma_f32 v[140:141],v[200:201],v[172:173],v[140:141]
v_pk_fma_f32 v[142:143],v[202:203],v[174:175],v[142:143]
v_pk_fma_f32 v[196:197],v[144:145],v[228:229],v[230:231]
v_pk_fma_f32 v[198:199],v[146:147],v[228:229],v[230:231]
v_pk_fma_f32 v[200:201],v[148:149],v[228:229],v[230:231]
v_pk_fma_f32 v[202:203],v[150:151],v[228:229],v[230:231]
v_pk_fma_f32 v[144:145],v[196:197],v[176:177],v[144:145]
v_pk_fma_f32 v[146:147],v[198:199],v[178:179],v[146:147]
v_pk_fma_f32 v[148:149],v[200:201],v[180:181],v[148:149]
v_pk_fma_f32 v[150:151],v[202:203],v[182:183],v[150:151]
v_pk_fma_f32 v[196:197],v[152:153],v[228:229],v[230:231]
v_pk_fma_f32 v[198:199],v[154:155],v[228:229],v[230:231]
v_pk_fma_f32 v[200:201],v[156:157],v[228:229],v[230:231]
v_pk_fma_f32 v[202:203],v[158:159],v[228:229],v[230:231]
v_pk_fma_f32 v[152:153],v[196:197],v[184:185],v[152:153]
v_pk_fma_f32 v[154:155],v[198:199],v[186:187],v[154:155]
v_pk_fma_f32 v[156:157],v[200:201],v[188:189],v[156:157]
v_pk_fma_f32 v[158:159],v[202:203],v[190:191],v[158:159]
v_cvt_pk_bf16_f32 v208,v128,v129
v_cvt_pk_bf16_f32 v209,v130,v131
v_cvt_pk_bf16_f32 v210,v132,v133
v_cvt_pk_bf16_f32 v211,v134,v135
v_cvt_pk_bf16_f32 v212,v136,v137
v_cvt_pk_bf16_f32 v213,v138,v139
v_cvt_pk_bf16_f32 v214,v140,v141
v_cvt_pk_bf16_f32 v215,v142,v143
v_mfma_f32_16x16x32_bf16 v[28:31],v[192:195],v[208:211],0
v_cvt_pk_bf16_f32 v216,v144,v145
v_cvt_pk_bf16_f32 v217,v146,v147
v_mfma_f32_16x16x32_bf16 v[204:207],v[208:211],v[208:211],0
v_cvt_pk_bf16_f32 v218,v148,v149
v_cvt_pk_bf16_f32 v219,v150,v151
v_mfma_f32_16x16x32_bf16 v[28:31],v[192:195],v[212:215],v[28:31]
v_cvt_pk_bf16_f32 v220,v152,v153
v_cvt_pk_bf16_f32 v221,v154,v155
v_mfma_f32_16x16x32_bf16 v[204:207],v[212:215],v[212:215],v[204:207]
v_cvt_pk_bf16_f32 v222,v156,v157
v_cvt_pk_bf16_f32 v223,v158,v159
v_mfma_f32_16x16x32_bf16 v[28:31],v[192:195],v[216:219],v[28:31]
v_mfma_f32_16x16x32_bf16 v[204:207],v[216:219],v[216:219],v[204:207]
v_mfma_f32_16x16x32_bf16 v[28:31],v[192:195],v[220:223],v[28:31]
v_mfma_f32_16x16x32_bf16 v[204:207],v[220:223],v[220:223],v[204:207]
s_waitcnt vmcnt(0)
buffer_store_dword v7,v5,s[36:39],s60 offen offset:0 sc0 sc1
s_mov_b32 s60,536870912
buffer_load_dword v8,v6,s[36:39],s91 offen offset:0 sc1
buffer_store_dwordx4 v[208:211],v1,s[36:39],s88 offen offset:0 sc0 sc1
buffer_store_dwordx4 v[212:215],v1,s[36:39],s88 offen offset:2048 sc0 sc1
s_add_u32 s88,s88,4096
v_add_f32_dpp v224,v28,v28 row_ror:8 row_mask:0xf bank_mask:0xf
v_cndmask_b32 v225,v204,v205,s[46:47]
v_cndmask_b32 v226,v206,v207,s[46:47]
v_cndmask_b32 v225,v225,v226,s[56:57]
s_and_b32 s63,s89,128
ds_write_b32 v2,v224 offset:1024
ds_write_b32 v0,v225 offset:1024
buffer_store_dwordx4 v[216:219],v1,s[36:39],s88 offen offset:0 sc0 sc1
buffer_store_dwordx4 v[220:223],v1,s[36:39],s88 offen offset:2048 sc0 sc1
s_waitcnt lgkmcnt(0)
s_barrier
ds_read_b128 v[196:199],v3 offset:1024
ds_read_b128 v[200:203],v3 offset:1040
ds_read_b128 v[204:207],v3 offset:1056
s_waitcnt lgkmcnt(0)
v_add_f32 v196,v196,v197
v_add_f32 v198,v198,v199
v_add_f32 v200,v200,v204
v_add_f32 v201,v201,v205
v_add_f32 v202,v202,v206
v_add_f32 v203,v203,v207
v_add_f32 v196,v196,v198
v_add_f32 v200,v200,v201
v_add_f32 v202,v202,v203
v_mul_f32 v196,s76,v196
v_add_f32 v197,v200,v202
v_mul_f32 v197,s76,v197
v_fma_f32 v197,-v196,v196,v197
v_max_f32 v197,0,v197
v_add_f32 v198,s77,v197
v_fma_f32 v199,s77,v198,v197
v_rsq_f32 v205,v198
v_rsq_f32 v198,v199
v_mov_b32 v200,v196
v_mul_f32_e64 v204,-v196,v205
v_mul_f32 v203,v199,v198
v_mul_f32 v201,s78,v198
v_mul_f32 v199,v198,v198
v_mul_f32 v202,s79,v199
v_mul_f32 v202,v202,v201
s_lshr_b32 s63,s63,1
s_sub_u32 s63,s89,s63
buffer_store_dwordx4 v[200:203],v4,s[36:39],s89 offen offset:0 sc0 sc1
buffer_store_dwordx2 v[204:205],v10,s[36:39],s63 offen offset:256 sc0 sc1
s_add_u32 m0,s66,s52
s_cmp_ge_u32 s51,12582912
s_cbranch_scc1 Lr_ys2_%=
buffer_load_dwordx4 v11,s[36:39],s51 offen offset:0 sc1 lds
buffer_load_dwordx4 v11,s[36:39],s51 offen offset:1024 sc1 lds
buffer_load_dwordx4 v11,s[36:39],s51 offen offset:2048 sc1 lds
buffer_load_dwordx4 v11,s[36:39],s51 offen offset:3072 sc1 lds
s_lshl_b32 s63,s50,14
s_add_u32 s51,s51,s63
Lr_ys2_%=:
s_add_u32 s66,s66,16384
s_cmp_eq_u32 s66,151552
s_cselect_b32 s66,4096,s66
v_add_u32 v18,s66,v11
s_mov_b32 s60,s90
s_waitcnt vmcnt(6)
v_cmp_eq_u32 vcc,s48,v8
s_nop 0
s_cmp_eq_u64 vcc,exec
s_cselect_b32 s63,s54,256
s_cmp_lt_u32 s63,256
s_cbranch_scc1 Lr_tc0_%=
buffer_load_dword v8,v6,s[36:39],s91 offen offset:0 sc1
s_waitcnt vmcnt(0)
buffer_store_dword v7,v5,s[36:39],s60 offen offset:0 sc0 sc1
s_mov_b32 s60,536870912
v_cmp_eq_u32 vcc,s48,v8
s_nop 0
s_cmp_eq_u64 vcc,exec
s_cselect_b32 s63,s54,256
s_cmp_lt_u32 s63,256
s_cbranch_scc1 Lr_bq0_%=
s_cmp_ge_u32 s54,256)"
R"(
s_cbranch_scc1 Lr_done_%=
Lr_bp0_%=:
buffer_load_dword v8,v6,s[36:39],s91 offen offset:0 sc1
s_waitcnt vmcnt(0)
v_cmp_eq_u32 vcc,s48,v8
s_nop 0
s_cmp_eq_u64 vcc,exec
s_cselect_b32 s63,1,s96
s_cmp_lg_u32 s63,0
s_cbranch_scc1 Lr_bq0_%=
s_add_u32 s74,s74,1
s_cmp_ge_u32 s74,20000
s_cselect_b32 s96,1,0
s_sleep 4
s_branch Lr_bp0_%=
Lr_bq0_%=:
s_add_u32 s63,s87,71303168
buffer_load_dwordx4 a[64:67],v11,s[36:39],s63 offen offset:0 sc1
buffer_load_dwordx4 a[68:71],v11,s[36:39],s63 offen offset:1024 sc1
buffer_load_dwordx4 a[72:75],v11,s[36:39],s63 offen offset:2048 sc1
buffer_load_dwordx4 a[76:79],v11,s[36:39],s63 offen offset:3072 sc1
s_add_u32 s63,s87,89128960
buffer_load_dwordx4 a[80:83],v11,s[36:39],s63 offen offset:0 sc1
buffer_load_dwordx4 a[84:87],v11,s[36:39],s63 offen offset:1024 sc1
buffer_load_dwordx4 a[88:91],v11,s[36:39],s63 offen offset:2048 sc1
buffer_load_dwordx4 a[92:95],v11,s[36:39],s63 offen offset:3072 sc1
s_add_u32 s63,s87,99614720
buffer_load_dwordx4 a[96:99],v11,s[36:39],s63 offen offset:0 sc1
buffer_load_dwordx4 a[100:103],v11,s[36:39],s63 offen offset:1024 sc1
buffer_load_dwordx4 a[104:107],v11,s[36:39],s63 offen offset:2048 sc1
buffer_load_dwordx4 a[108:111],v11,s[36:39],s63 offen offset:3072 sc1
s_add_u32 s63,s87,109051904
buffer_load_dwordx4 a[112:115],v11,s[36:39],s63 offen offset:0 sc1
buffer_load_dwordx4 a[116:119],v11,s[36:39],s63 offen offset:1024 sc1
buffer_load_dwordx4 a[120:123],v11,s[36:39],s63 offen offset:2048 sc1
buffer_load_dwordx4 a[124:127],v11,s[36:39],s63 offen offset:3072 sc1
buffer_load_dword v8,v6,s[36:39],s69 offen offset:0 sc1
s_waitcnt vmcnt(0)
s_branch Lr_it1_%=
Lr_ta0_%=:
ds_read_b128 v[32:35],v18 offset:0
ds_read_b128 v[36:39],v18 offset:1024
ds_read_b128 v[40:43],v18 offset:2048
ds_read_b128 v[44:47],v18 offset:3072
v_mfma_f32_16x16x32_bf16 v[128:131],v[20:23],a[0:3],0
s_add_u32 s63,s87,71303168
v_mfma_f32_16x16x32_bf16 v[132:135],v[24:27],a[0:3],0
buffer_load_dwordx4 a[64:67],v11,s[36:39],s63 offen offset:0 sc1
v_mfma_f32_16x16x32_bf16 v[128:131],v[20:23],a[16:19],v[128:131]
v_mfma_f32_16x16x32_bf16 v[132:135],v[24:27],a[16:19],v[132:135]
buffer_load_dwordx4 a[68:71],v11,s[36:39],s63 offen offset:1024 sc1
v_mfma_f32_16x16x32_bf16 v[128:131],v[20:23],a[32:35],v[128:131]
v_mfma_f32_16x16x32_bf16 v[132:135],v[24:27],a[32:35],v[132:135]
v_mfma_f32_16x16x32_bf16 v[128:131],v[20:23],a[48:51],v[128:131]
v_mfma_f32_16x16x32_bf16 v[132:135],v[24:27],a[48:51],v[132:135]
buffer_load_dwordx4 a[72:75],v11,s[36:39],s63 offen offset:2048 sc1
v_mfma_f32_16x16x32_bf16 v[136:139],v[20:23],a[4:7],0
v_mfma_f32_16x16x32_bf16 v[140:143],v[24:27],a[4:7],0
v_mfma_f32_16x16x32_bf16 v[136:139],v[20:23],a[20:23],v[136:139]
v_mfma_f32_16x16x32_bf16 v[140:143],v[24:27],a[20:23],v[140:143]
buffer_load_dwordx4 a[76:79],v11,s[36:39],s63 offen offset:3072 sc1
v_mfma_f32_16x16x32_bf16 v[136:139],v[20:23],a[36:39],v[136:139]
v_cvt_pk_bf16_f32 v208,v128,v129
v_cvt_pk_bf16_f32 v209,v130,v131
v_mfma_f32_16x16x32_bf16 v[140:143],v[24:27],a[36:39],v[140:143]
v_cvt_pk_bf16_f32 v210,v132,v133
v_cvt_pk_bf16_f32 v211,v134,v135
v_mfma_f32_16x16x32_bf16 v[136:139],v[20:23],a[52:55],v[136:139]
s_add_u32 s63,s87,89128960
v_mfma_f32_16x16x32_bf16 v[140:143],v[24:27],a[52:55],v[140:143]
buffer_load_dwordx4 a[80:83],v11,s[36:39],s63 offen offset:0 sc1
v_mfma_f32_16x16x32_bf16 v[144:147],v[20:23],a[8:11],0
v_mfma_f32_16x16x32_bf16 v[148:151],v[24:27],a[8:11],0
v_mfma_f32_16x16x32_bf16 v[144:147],v[20:23],a[24:27],v[144:147]
v_mfma_f32_16x16x32_bf16 v[148:151],v[24:27],a[24:27],v[148:151]
buffer_load_dwordx4 a[84:87],v11,s[36:39],s63 offen offset:1024 sc1
v_mfma_f32_16x16x32_bf16 v[144:147],v[20:23],a[40:43],v[144:147]
v_cvt_pk_bf16_f32 v212,v136,v137
v_cvt_pk_bf16_f32 v213,v138,v139
v_mfma_f32_16x16x32_bf16 v[148:151],v[24:27],a[40:43],v[148:151]
v_cvt_pk_bf16_f32 v214,v140,v141
v_cvt_pk_bf16_f32 v215,v142,v143
v_mfma_f32_16x16x32_bf16 v[144:147],v[20:23],a[56:59],v[144:147]
v_mfma_f32_16x16x32_bf16 v[148:151],v[24:27],a[56:59],v[148:151]
buffer_load_dwordx4 a[88:91],v11,s[36:39],s63 offen offset:2048 sc1
v_mfma_f32_16x16x32_bf16 v[28:31],v[192:195],v[208:211],0
v_mfma_f32_16x16x32_bf16 v[204:207],v[208:211],v[208:211],0
v_mfma_f32_16x16x32_bf16 v[152:155],v[20:23],a[12:15],0
v_mfma_f32_16x16x32_bf16 v[156:159],v[24:27],a[12:15],0
buffer_load_dwordx4 a[92:95],v11,s[36:39],s63 offen offset:3072 sc1
v_mfma_f32_16x16x32_bf16 v[152:155],v[20:23],a[28:31],v[152:155]
s_add_u32 s63,s87,99614720
v_mfma_f32_16x16x32_bf16 v[156:159],v[24:27],a[28:31],v[156:159]
v_mfma_f32_16x16x32_bf16 v[152:155],v[20:23],a[44:47],v[152:155]
v_cvt_pk_bf16_f32 v216,v144,v145
v_cvt_pk_bf16_f32 v217,v146,v147
v_mfma_f32_16x16x32_bf16 v[156:159],v[24:27],a[44:47],v[156:159]
v_cvt_pk_bf16_f32 v218,v148,v149
v_cvt_pk_bf16_f32 v219,v150,v151
v_mfma_f32_16x16x32_bf16 v[152:155],v[20:23],a[60:63],v[152:155]
buffer_load_dwordx4 a[96:99],v11,s[36:39],s63 offen offset:0 sc1
v_mfma_f32_16x16x32_bf16 v[156:159],v[24:27],a[60:63],v[156:159]
v_mfma_f32_16x16x32_bf16 v[28:31],v[192:195],v[212:215],v[28:31]
v_mfma_f32_16x16x32_bf16 v[204:207],v[212:215],v[212:215],v[204:207]
buffer_load_dwordx4 a[100:103],v11,s[36:39],s63 offen offset:1024 sc1
v_mfma_f32_16x16x32_bf16 v[28:31],v[192:195],v[216:219],v[28:31]
v_mfma_f32_16x16x32_bf16 v[204:207],v[216:219],v[216:219],v[204:207]
s_waitcnt lgkmcnt(0)
v_mfma_f32_16x16x32_bf16 v[160:163],v[20:23],v[32:35],0
v_cvt_pk_bf16_f32 v220,v152,v153
v_cvt_pk_bf16_f32 v221,v154,v155
v_mfma_f32_16x16x32_bf16 v[164:167],v[24:27],v[32:35],0
v_cvt_pk_bf16_f32 v222,v156,v157
v_cvt_pk_bf16_f32 v223,v158,v159
v_mfma_f32_16x16x32_bf16 v[168:171],v[20:23],v[36:39],0
buffer_load_dwordx4 a[104:107],v11,s[36:39],s63 offen offset:2048 sc1
v_mfma_f32_16x16x32_bf16 v[28:31],v[192:195],v[220:223],v[28:31]
buffer_load_dwordx4 a[108:111],v11,s[36:39],s63 offen offset:3072 sc1
v_mfma_f32_16x16x32_bf16 v[204:207],v[220:223],v[220:223],v[204:207]
s_add_u32 s63,s87,109051904
v_mfma_f32_16x16x32_bf16 v[172:175],v[24:27],v[36:39],0
v_mfma_f32_16x16x32_bf16 v[176:179],v[20:23],v[40:43],0
buffer_load_dwordx4 a[112:115],v11,s[36:39],s63 offen offset:0 sc1
v_mfma_f32_16x16x32_bf16 v[180:183],v[24:27],v[40:43],0
s_nop 0
v_add_f32_dpp v224,v28,v28 row_ror:8 row_mask:0xf bank_mask:0xf
s_nop 0
v_cndmask_b32 v225,v204,v205,s[46:47]
v_cndmask_b32 v226,v206,v207,s[46:47]
v_cndmask_b32 v225,v225,v226,s[56:57]
v_mfma_f32_16x16x32_bf16 v[184:187],v[20:23],v[44:47],0
ds_write_b32 v2,v224 offset:0
ds_write_b32 v0,v225 offset:0
v_mfma_f32_16x16x32_bf16 v[188:191],v[24:27],v[44:47],0
s_waitcnt lgkmcnt(0)
buffer_load_dwordx4 a[116:119],v11,s[36:39],s63 offen offset:1024 sc1
s_barrier
buffer_load_dwordx4 a[120:123],v11,s[36:39],s63 offen offset:2048 sc1
ds_read_b128 v[196:199],v3 offset:0
buffer_load_dwordx4 a[124:127],v11,s[36:39],s63 offen offset:3072 sc1
ds_read_b128 v[200:203],v3 offset:16
ds_read_b128 v[204:207],v3 offset:32
s_waitcnt lgkmcnt(0)
buffer_load_dword v8,v6,s[36:39],s69 offen offset:0 sc1
v_add_f32 v196,v196,v197
v_add_f32 v198,v198,v199
v_add_f32 v200,v200,v204
v_add_f32 v201,v201,v205
v_add_f32 v202,v202,v206
v_add_f32 v203,v203,v207
v_add_f32 v196,v196,v198
v_add_f32 v200,v200,v201
v_add_f32 v202,v202,v203
v_mul_f32 v196,s76,v196
v_add_f32 v197,v200,v202
v_mul_f32 v197,s76,v197
v_fma_f32 v197,-v196,v196,v197
v_max_f32 v197,0,v197
v_add_f32 v197,s77,v197
v_rsq_f32 v228,v197
s_nop 0
v_mul_f32_e64 v230,-v196,v228
v_mov_b32 v229,v228
v_mov_b32 v231,v230
v_pk_fma_f32 v[196:197],v[128:129],v[228:229],v[230:231]
v_pk_fma_f32 v[198:199],v[130:131],v[228:229],v[230:231]
v_pk_fma_f32 v[200:201],v[132:133],v[228:229],v[230:231]
v_pk_fma_f32 v[202:203],v[134:135],v[228:229],v[230:231]
v_pk_fma_f32 v[128:129],v[196:197],v[160:161],v[128:129]
v_pk_fma_f32 v[130:131],v[198:199],v[162:163],v[130:131]
v_pk_fma_f32 v[132:133],v[200:201],v[164:165],v[132:133]
v_pk_fma_f32 v[134:135],v[202:203],v[166:167],v[134:135]
v_pk_fma_f32 v[196:197],v[136:137],v[228:229],v[230:231]
v_pk_fma_f32 v[198:199],v[138:139],v[228:229],v[230:231]
v_pk_fma_f32 v[200:201],v[140:141],v[228:229],v[230:231]
v_pk_fma_f32 v[202:203],v[142:143],v[228:229],v[230:231]
v_pk_fma_f32 v[136:137],v[196:197],v[168:169],v[136:137]
v_pk_fma_f32 v[138:139],v[198:199],v[170:171],v[138:139]
v_pk_fma_f32 v[140:141],v[200:201],v[172:173],v[140:141]
v_pk_fma_f32 v[142:143],v[202:203],v[174:175],v[142:143]
v_pk_fma_f32 v[196:197],v[144:145],v[228:229],v[230:231]
v_pk_fma_f32 v[198:199],v[146:147],v[228:229],v[230:231]
v_pk_fma_f32 v[200:201],v[148:149],v[228:229],v[230:231]
v_pk_fma_f32 v[202:203],v[150:151],v[228:229],v[230:231]
v_pk_fma_f32 v[144:145],v[196:197],v[176:177],v[144:145]
v_pk_fma_f32 v[146:147],v[198:199],v[178:179],v[146:147]
v_pk_fma_f32 v[148:149],v[200:201],v[180:181],v[148:149]
v_pk_fma_f32 v[150:151],v[202:203],v[182:183],v[150:151]
v_pk_fma_f32 v[196:197],v[152:153],v[228:229],v[230:231]
v_pk_fma_f32 v[198:199],v[154:155],v[228:229],v[230:231]
v_pk_fma_f32 v[200:201],v[156:157],v[228:229],v[230:231]
v_pk_fma_f32 v[202:203],v[158:159],v[228:229],v[230:231]
v_pk_fma_f32 v[152:153],v[196:197],v[184:185],v[152:153]
v_pk_fma_f32 v[154:155],v[198:199],v[186:187],v[154:155]
v_pk_fma_f32 v[156:157],v[200:201],v[188:189],v[156:157]
v_pk_fma_f32 v[158:159],v[202:203],v[190:191],v[158:159]
v_cvt_pk_bf16_f32 v208,v128,v129
v_cvt_pk_bf16_f32 v209,v130,v131
v_cvt_pk_bf16_f32 v210,v132,v133
v_cvt_pk_bf16_f32 v211,v134,v135)"
R"(
v_cvt_pk_bf16_f32 v212,v136,v137
v_cvt_pk_bf16_f32 v213,v138,v139
v_cvt_pk_bf16_f32 v214,v140,v141
v_cvt_pk_bf16_f32 v215,v142,v143
v_mfma_f32_16x16x32_bf16 v[28:31],v[192:195],v[208:211],0
v_cvt_pk_bf16_f32 v216,v144,v145
v_cvt_pk_bf16_f32 v217,v146,v147
v_mfma_f32_16x16x32_bf16 v[204:207],v[208:211],v[208:211],0
v_cvt_pk_bf16_f32 v218,v148,v149
v_cvt_pk_bf16_f32 v219,v150,v151
v_mfma_f32_16x16x32_bf16 v[28:31],v[192:195],v[212:215],v[28:31]
v_cvt_pk_bf16_f32 v220,v152,v153
v_cvt_pk_bf16_f32 v221,v154,v155
v_mfma_f32_16x16x32_bf16 v[204:207],v[212:215],v[212:215],v[204:207]
v_cvt_pk_bf16_f32 v222,v156,v157
v_cvt_pk_bf16_f32 v223,v158,v159
v_mfma_f32_16x16x32_bf16 v[28:31],v[192:195],v[216:219],v[28:31]
v_mfma_f32_16x16x32_bf16 v[204:207],v[216:219],v[216:219],v[204:207]
v_mfma_f32_16x16x32_bf16 v[28:31],v[192:195],v[220:223],v[28:31]
v_mfma_f32_16x16x32_bf16 v[204:207],v[220:223],v[220:223],v[204:207]
s_waitcnt vmcnt(17)
buffer_store_dword v7,v5,s[36:39],s60 offen offset:0 sc0 sc1
s_mov_b32 s60,536870912
buffer_store_dwordx4 v[208:211],v1,s[36:39],s88 offen offset:0 sc0 sc1
buffer_store_dwordx4 v[212:215],v1,s[36:39],s88 offen offset:2048 sc0 sc1
s_add_u32 s88,s88,4096
s_nop 0
v_add_f32_dpp v224,v28,v28 row_ror:8 row_mask:0xf bank_mask:0xf
v_cndmask_b32 v225,v204,v205,s[46:47]
v_cndmask_b32 v226,v206,v207,s[46:47]
v_cndmask_b32 v225,v225,v226,s[56:57]
s_and_b32 s63,s89,128
ds_write_b32 v2,v224 offset:1024
ds_write_b32 v0,v225 offset:1024
buffer_store_dwordx4 v[216:219],v1,s[36:39],s88 offen offset:0 sc0 sc1
buffer_store_dwordx4 v[220:223],v1,s[36:39],s88 offen offset:2048 sc0 sc1
s_waitcnt lgkmcnt(0)
s_barrier
ds_read_b128 v[196:199],v3 offset:1024
ds_read_b128 v[200:203],v3 offset:1040
ds_read_b128 v[204:207],v3 offset:1056
s_waitcnt lgkmcnt(0)
v_add_f32 v196,v196,v197
v_add_f32 v198,v198,v199
v_add_f32 v200,v200,v204
v_add_f32 v201,v201,v205
v_add_f32 v202,v202,v206
v_add_f32 v203,v203,v207
v_add_f32 v196,v196,v198
v_add_f32 v200,v200,v201
v_add_f32 v202,v202,v203
v_mul_f32 v196,s76,v196
v_add_f32 v197,v200,v202
v_mul_f32 v197,s76,v197
v_fma_f32 v197,-v196,v196,v197
v_max_f32 v197,0,v197
v_add_f32 v198,s77,v197
v_fma_f32 v199,s77,v198,v197
v_rsq_f32 v205,v198
v_rsq_f32 v198,v199
v_mov_b32 v200,v196
v_mul_f32_e64 v204,-v196,v205
v_mul_f32 v203,v199,v198
v_mul_f32 v201,s78,v198
v_mul_f32 v199,v198,v198
v_mul_f32 v202,s79,v199
v_mul_f32 v202,v202,v201
s_lshr_b32 s63,s63,1
s_sub_u32 s63,s89,s63
buffer_store_dwordx4 v[200:203],v4,s[36:39],s89 offen offset:0 sc0 sc1
buffer_store_dwordx2 v[204:205],v10,s[36:39],s63 offen offset:256 sc0 sc1
s_add_u32 m0,s66,s52
s_cmp_ge_u32 s51,12582912
s_cbranch_scc1 Lr_ys3_%=
buffer_load_dwordx4 v11,s[36:39],s51 offen offset:0 sc1 lds
buffer_load_dwordx4 v11,s[36:39],s51 offen offset:1024 sc1 lds
buffer_load_dwordx4 v11,s[36:39],s51 offen offset:2048 sc1 lds
buffer_load_dwordx4 v11,s[36:39],s51 offen offset:3072 sc1 lds
s_lshl_b32 s63,s50,14
s_add_u32 s51,s51,s63
Lr_ys3_%=:
s_add_u32 s66,s66,16384
s_cmp_eq_u32 s66,151552
s_cselect_b32 s66,4096,s66
v_add_u32 v18,s66,v11
s_mov_b32 s60,s90
s_waitcnt vmcnt(7)
s_branch Lr_it1_%=
Lr_tc0_%=:
s_add_u32 s63,s87,71303168
buffer_load_dwordx4 a[64:67],v11,s[36:39],s63 offen offset:0 sc1
buffer_load_dwordx4 a[68:71],v11,s[36:39],s63 offen offset:1024 sc1
buffer_load_dwordx4 a[72:75],v11,s[36:39],s63 offen offset:2048 sc1
buffer_load_dwordx4 a[76:79],v11,s[36:39],s63 offen offset:3072 sc1
s_add_u32 s63,s87,89128960
buffer_load_dwordx4 a[80:83],v11,s[36:39],s63 offen offset:0 sc1
buffer_load_dwordx4 a[84:87],v11,s[36:39],s63 offen offset:1024 sc1
buffer_load_dwordx4 a[88:91],v11,s[36:39],s63 offen offset:2048 sc1
buffer_load_dwordx4 a[92:95],v11,s[36:39],s63 offen offset:3072 sc1
s_add_u32 s63,s87,99614720
buffer_load_dwordx4 a[96:99],v11,s[36:39],s63 offen offset:0 sc1
buffer_load_dwordx4 a[100:103],v11,s[36:39],s63 offen offset:1024 sc1
buffer_load_dwordx4 a[104:107],v11,s[36:39],s63 offen offset:2048 sc1
buffer_load_dwordx4 a[108:111],v11,s[36:39],s63 offen offset:3072 sc1
s_add_u32 s63,s87,109051904
buffer_load_dwordx4 a[112:115],v11,s[36:39],s63 offen offset:0 sc1
buffer_load_dwordx4 a[116:119],v11,s[36:39],s63 offen offset:1024 sc1
buffer_load_dwordx4 a[120:123],v11,s[36:39],s63 offen offset:2048 sc1
buffer_load_dwordx4 a[124:127],v11,s[36:39],s63 offen offset:3072 sc1
buffer_load_dword v8,v6,s[36:39],s69 offen offset:0 sc1
s_waitcnt vmcnt(17)
buffer_store_dword v7,v5,s[36:39],s60 offen offset:0 sc0 sc1
s_mov_b32 s60,536870912
s_waitcnt vmcnt(1)
s_branch Lr_it1_%=
Lr_it1_%=:
s_mov_b32 s88,s58
s_mov_b32 s89,s59
s_mov_b32 s90,s49
s_add_u32 s54,s54,s50
s_lshr_b32 s63,s54,1
s_and_b32 s64,s54,1
s_lshl_b32 s64,s64,7
s_lshl_b32 s87,s54,14
s_lshl_b32 s58,s63,15
s_add_u32 s58,s58,s64
s_add_u32 s58,s58,16908288
s_lshl_b32 s59,s63,9
s_add_u32 s59,s59,s64
s_add_u32 s59,s59,118556672
s_lshl_b32 s49,s54,9
s_add_u32 s49,s49,s92
s_lshl_b32 s91,s63,11
s_add_u32 s91,s91,121405440
s_lshl_b32 s69,s50,10
s_add_u32 s69,s69,s91
v_cmp_eq_u32 vcc,s48,v8
s_nop 0
s_cmp_eq_u64 vcc,exec
s_cselect_b32 s63,s54,256
s_cmp_lt_u32 s63,256
s_cbranch_scc1 Lr_ta1_%=
ds_read_b128 v[32:35],v18 offset:0
ds_read_b128 v[36:39],v18 offset:1024
ds_read_b128 v[40:43],v18 offset:2048
ds_read_b128 v[44:47],v18 offset:3072
v_mfma_f32_16x16x32_bf16 v[128:131],v[20:23],a[64:67],0
v_mfma_f32_16x16x32_bf16 v[132:135],v[24:27],a[64:67],0
v_mfma_f32_16x16x32_bf16 v[128:131],v[20:23],a[80:83],v[128:131]
v_mfma_f32_16x16x32_bf16 v[132:135],v[24:27],a[80:83],v[132:135]
v_mfma_f32_16x16x32_bf16 v[128:131],v[20:23],a[96:99],v[128:131]
v_mfma_f32_16x16x32_bf16 v[132:135],v[24:27],a[96:99],v[132:135]
v_mfma_f32_16x16x32_bf16 v[128:131],v[20:23],a[112:115],v[128:131]
v_mfma_f32_16x16x32_bf16 v[132:135],v[24:27],a[112:115],v[132:135]
v_mfma_f32_16x16x32_bf16 v[136:139],v[20:23],a[68:71],0
v_mfma_f32_16x16x32_bf16 v[140:143],v[24:27],a[68:71],0
v_mfma_f32_16x16x32_bf16 v[136:139],v[20:23],a[84:87],v[136:139]
v_mfma_f32_16x16x32_bf16 v[140:143],v[24:27],a[84:87],v[140:143]
v_mfma_f32_16x16x32_bf16 v[136:139],v[20:23],a[100:103],v[136:139]
s_nop 1
v_cvt_pk_bf16_f32 v208,v128,v129
v_cvt_pk_bf16_f32 v209,v130,v131
v_mfma_f32_16x16x32_bf16 v[140:143],v[24:27],a[100:103],v[140:143]
v_cvt_pk_bf16_f32 v210,v132,v133
v_cvt_pk_bf16_f32 v211,v134,v135
v_mfma_f32_16x16x32_bf16 v[136:139],v[20:23],a[116:119],v[136:139]
v_mfma_f32_16x16x32_bf16 v[140:143],v[24:27],a[116:119],v[140:143]
v_mfma_f32_16x16x32_bf16 v[144:147],v[20:23],a[72:75],0
v_mfma_f32_16x16x32_bf16 v[148:151],v[24:27],a[72:75],0
v_mfma_f32_16x16x32_bf16 v[144:147],v[20:23],a[88:91],v[144:147]
v_mfma_f32_16x16x32_bf16 v[148:151],v[24:27],a[88:91],v[148:151]
v_mfma_f32_16x16x32_bf16 v[144:147],v[20:23],a[104:107],v[144:147]
s_nop 1
v_cvt_pk_bf16_f32 v212,v136,v137
v_cvt_pk_bf16_f32 v213,v138,v139
v_mfma_f32_16x16x32_bf16 v[148:151],v[24:27],a[104:107],v[148:151]
v_cvt_pk_bf16_f32 v214,v140,v141
v_cvt_pk_bf16_f32 v215,v142,v143
v_mfma_f32_16x16x32_bf16 v[144:147],v[20:23],a[120:123],v[144:147]
v_mfma_f32_16x16x32_bf16 v[148:151],v[24:27],a[120:123],v[148:151]
v_mfma_f32_16x16x32_bf16 v[28:31],v[192:195],v[208:211],0
v_mfma_f32_16x16x32_bf16 v[204:207],v[208:211],v[208:211],0
v_mfma_f32_16x16x32_bf16 v[152:155],v[20:23],a[76:79],0
v_mfma_f32_16x16x32_bf16 v[156:159],v[24:27],a[76:79],0
v_mfma_f32_16x16x32_bf16 v[152:155],v[20:23],a[92:95],v[152:155]
v_mfma_f32_16x16x32_bf16 v[156:159],v[24:27],a[92:95],v[156:159]
v_mfma_f32_16x16x32_bf16 v[152:155],v[20:23],a[108:111],v[152:155]
v_cvt_pk_bf16_f32 v216,v144,v145
v_cvt_pk_bf16_f32 v217,v146,v147
v_mfma_f32_16x16x32_bf16 v[156:159],v[24:27],a[108:111],v[156:159]
v_cvt_pk_bf16_f32 v218,v148,v149
v_cvt_pk_bf16_f32 v219,v150,v151
v_mfma_f32_16x16x32_bf16 v[152:155],v[20:23],a[124:127],v[152:155]
v_mfma_f32_16x16x32_bf16 v[156:159],v[24:27],a[124:127],v[156:159]
v_mfma_f32_16x16x32_bf16 v[28:31],v[192:195],v[212:215],v[28:31]
v_mfma_f32_16x16x32_bf16 v[204:207],v[212:215],v[212:215],v[204:207]
v_mfma_f32_16x16x32_bf16 v[28:31],v[192:195],v[216:219],v[28:31]
v_mfma_f32_16x16x32_bf16 v[204:207],v[216:219],v[216:219],v[204:207]
s_waitcnt lgkmcnt(0)
v_mfma_f32_16x16x32_bf16 v[160:163],v[20:23],v[32:35],0)"
R"(
s_nop 0
v_cvt_pk_bf16_f32 v220,v152,v153
v_cvt_pk_bf16_f32 v221,v154,v155
v_mfma_f32_16x16x32_bf16 v[164:167],v[24:27],v[32:35],0
v_cvt_pk_bf16_f32 v222,v156,v157
v_cvt_pk_bf16_f32 v223,v158,v159
v_mfma_f32_16x16x32_bf16 v[168:171],v[20:23],v[36:39],0
s_nop 0
v_mfma_f32_16x16x32_bf16 v[28:31],v[192:195],v[220:223],v[28:31]
v_mfma_f32_16x16x32_bf16 v[204:207],v[220:223],v[220:223],v[204:207]
v_mfma_f32_16x16x32_bf16 v[172:175],v[24:27],v[36:39],0
v_mfma_f32_16x16x32_bf16 v[176:179],v[20:23],v[40:43],0
v_mfma_f32_16x16x32_bf16 v[180:183],v[24:27],v[40:43],0
s_nop 3
v_add_f32_dpp v224,v28,v28 row_ror:8 row_mask:0xf bank_mask:0xf
v_cndmask_b32 v225,v204,v205,s[46:47]
v_cndmask_b32 v226,v206,v207,s[46:47]
v_cndmask_b32 v225,v225,v226,s[56:57]
v_mfma_f32_16x16x32_bf16 v[184:187],v[20:23],v[44:47],0
ds_write_b32 v2,v224 offset:0
ds_write_b32 v0,v225 offset:0
v_mfma_f32_16x16x32_bf16 v[188:191],v[24:27],v[44:47],0
s_waitcnt lgkmcnt(0)
s_barrier
ds_read_b128 v[196:199],v3 offset:0
ds_read_b128 v[200:203],v3 offset:16
ds_read_b128 v[204:207],v3 offset:32
s_waitcnt lgkmcnt(0)
v_add_f32 v196,v196,v197
v_add_f32 v198,v198,v199
v_add_f32 v200,v200,v204
v_add_f32 v201,v201,v205
v_add_f32 v202,v202,v206
v_add_f32 v203,v203,v207
v_add_f32 v196,v196,v198
v_add_f32 v200,v200,v201
v_add_f32 v202,v202,v203
v_mul_f32 v196,s76,v196
v_add_f32 v197,v200,v202
v_mul_f32 v197,s76,v197
v_fma_f32 v197,-v196,v196,v197
v_max_f32 v197,0,v197
v_add_f32 v197,s77,v197
v_rsq_f32 v228,v197
s_nop 0
v_mul_f32_e64 v230,-v196,v228
v_mov_b32 v229,v228
v_mov_b32 v231,v230
v_pk_fma_f32 v[196:197],v[128:129],v[228:229],v[230:231]
v_pk_fma_f32 v[198:199],v[130:131],v[228:229],v[230:231]
v_pk_fma_f32 v[200:201],v[132:133],v[228:229],v[230:231]
v_pk_fma_f32 v[202:203],v[134:135],v[228:229],v[230:231]
v_pk_fma_f32 v[128:129],v[196:197],v[160:161],v[128:129]
v_pk_fma_f32 v[130:131],v[198:199],v[162:163],v[130:131]
v_pk_fma_f32 v[132:133],v[200:201],v[164:165],v[132:133]
v_pk_fma_f32 v[134:135],v[202:203],v[166:167],v[134:135]
v_pk_fma_f32 v[196:197],v[136:137],v[228:229],v[230:231]
v_pk_fma_f32 v[198:199],v[138:139],v[228:229],v[230:231]
v_pk_fma_f32 v[200:201],v[140:141],v[228:229],v[230:231]
v_pk_fma_f32 v[202:203],v[142:143],v[228:229],v[230:231]
v_pk_fma_f32 v[136:137],v[196:197],v[168:169],v[136:137]
v_pk_fma_f32 v[138:139],v[198:199],v[170:171],v[138:139]
v_pk_fma_f32 v[140:141],v[200:201],v[172:173],v[140:141]
v_pk_fma_f32 v[142:143],v[202:203],v[174:175],v[142:143]
v_pk_fma_f32 v[196:197],v[144:145],v[228:229],v[230:231]
v_pk_fma_f32 v[198:199],v[146:147],v[228:229],v[230:231]
v_pk_fma_f32 v[200:201],v[148:149],v[228:229],v[230:231]
v_pk_fma_f32 v[202:203],v[150:151],v[228:229],v[230:231]
v_pk_fma_f32 v[144:145],v[196:197],v[176:177],v[144:145]
v_pk_fma_f32 v[146:147],v[198:199],v[178:179],v[146:147]
v_pk_fma_f32 v[148:149],v[200:201],v[180:181],v[148:149]
v_pk_fma_f32 v[150:151],v[202:203],v[182:183],v[150:151]
v_pk_fma_f32 v[196:197],v[152:153],v[228:229],v[230:231]
v_pk_fma_f32 v[198:199],v[154:155],v[228:229],v[230:231]
v_pk_fma_f32 v[200:201],v[156:157],v[228:229],v[230:231]
v_pk_fma_f32 v[202:203],v[158:159],v[228:229],v[230:231]
v_pk_fma_f32 v[152:153],v[196:197],v[184:185],v[152:153]
v_pk_fma_f32 v[154:155],v[198:199],v[186:187],v[154:155]
v_pk_fma_f32 v[156:157],v[200:201],v[188:189],v[156:157]
v_pk_fma_f32 v[158:159],v[202:203],v[190:191],v[158:159]
v_cvt_pk_bf16_f32 v208,v128,v129
v_cvt_pk_bf16_f32 v209,v130,v131
v_cvt_pk_bf16_f32 v210,v132,v133
v_cvt_pk_bf16_f32 v211,v134,v135
v_cvt_pk_bf16_f32 v212,v136,v137
v_cvt_pk_bf16_f32 v213,v138,v139
v_cvt_pk_bf16_f32 v214,v140,v141
v_cvt_pk_bf16_f32 v215,v142,v143
v_mfma_f32_16x16x32_bf16 v[28:31],v[192:195],v[208:211],0
v_cvt_pk_bf16_f32 v216,v144,v145
v_cvt_pk_bf16_f32 v217,v146,v147
v_mfma_f32_16x16x32_bf16 v[204:207],v[208:211],v[208:211],0
v_cvt_pk_bf16_f32 v218,v148,v149
v_cvt_pk_bf16_f32 v219,v150,v151
v_mfma_f32_16x16x32_bf16 v[28:31],v[192:195],v[212:215],v[28:31]
v_cvt_pk_bf16_f32 v220,v152,v153
v_cvt_pk_bf16_f32 v221,v154,v155
v_mfma_f32_16x16x32_bf16 v[204:207],v[212:215],v[212:215],v[204:207]
v_cvt_pk_bf16_f32 v222,v156,v157
v_cvt_pk_bf16_f32 v223,v158,v159
v_mfma_f32_16x16x32_bf16 v[28:31],v[192:195],v[216:219],v[28:31]
v_mfma_f32_16x16x32_bf16 v[204:207],v[216:219],v[216:219],v[204:207]
v_mfma_f32_16x16x32_bf16 v[28:31],v[192:195],v[220:223],v[28:31]
v_mfma_f32_16x16x32_bf16 v[204:207],v[220:223],v[220:223],v[204:207]
s_waitcnt vmcnt(0)
buffer_store_dword v7,v5,s[36:39],s60 offen offset:0 sc0 sc1
s_mov_b32 s60,536870912
buffer_load_dword v8,v6,s[36:39],s91 offen offset:0 sc1
buffer_store_dwordx4 v[208:211],v1,s[36:39],s88 offen offset:0 sc0 sc1
buffer_store_dwordx4 v[212:215],v1,s[36:39],s88 offen offset:2048 sc0 sc1
s_add_u32 s88,s88,4096
v_add_f32_dpp v224,v28,v28 row_ror:8 row_mask:0xf bank_mask:0xf
v_cndmask_b32 v225,v204,v205,s[46:47]
v_cndmask_b32 v226,v206,v207,s[46:47]
v_cndmask_b32 v225,v225,v226,s[56:57]
s_and_b32 s63,s89,128
ds_write_b32 v2,v224 offset:1024
ds_write_b32 v0,v225 offset:1024
buffer_store_dwordx4 v[216:219],v1,s[36:39],s88 offen offset:0 sc0 sc1
buffer_store_dwordx4 v[220:223],v1,s[36:39],s88 offen offset:2048 sc0 sc1
s_waitcnt lgkmcnt(0)
s_barrier
ds_read_b128 v[196:199],v3 offset:1024
ds_read_b128 v[200:203],v3 offset:1040
ds_read_b128 v[204:207],v3 offset:1056
s_waitcnt lgkmcnt(0)
v_add_f32 v196,v196,v197
v_add_f32 v198,v198,v199
v_add_f32 v200,v200,v204
v_add_f32 v201,v201,v205
v_add_f32 v202,v202,v206
v_add_f32 v203,v203,v207
v_add_f32 v196,v196,v198
v_add_f32 v200,v200,v201
v_add_f32 v202,v202,v203
v_mul_f32 v196,s76,v196
v_add_f32 v197,v200,v202
v_mul_f32 v197,s76,v197
v_fma_f32 v197,-v196,v196,v197
v_max_f32 v197,0,v197
v_add_f32 v198,s77,v197
v_fma_f32 v199,s77,v198,v197
v_rsq_f32 v205,v198
v_rsq_f32 v198,v199
v_mov_b32 v200,v196
v_mul_f32_e64 v204,-v196,v205
v_mul_f32 v203,v199,v198
v_mul_f32 v201,s78,v198
v_mul_f32 v199,v198,v198
v_mul_f32 v202,s79,v199
v_mul_f32 v202,v202,v201
s_lshr_b32 s63,s63,1
s_sub_u32 s63,s89,s63
buffer_store_dwordx4 v[200:203],v4,s[36:39],s89 offen offset:0 sc0 sc1
buffer_store_dwordx2 v[204:205],v10,s[36:39],s63 offen offset:256 sc0 sc1
s_add_u32 m0,s66,s52
s_cmp_ge_u32 s51,12582912
s_cbranch_scc1 Lr_ys4_%=
buffer_load_dwordx4 v11,s[36:39],s51 offen offset:0 sc1 lds
buffer_load_dwordx4 v11,s[36:39],s51 offen offset:1024 sc1 lds
buffer_load_dwordx4 v11,s[36:39],s51 offen offset:2048 sc1 lds
buffer_load_dwordx4 v11,s[36:39],s51 offen offset:3072 sc1 lds
s_lshl_b32 s63,s50,14
s_add_u32 s51,s51,s63
Lr_ys4_%=:
s_add_u32 s66,s66,16384
s_cmp_eq_u32 s66,151552
s_cselect_b32 s66,4096,s66
v_add_u32 v18,s66,v11
s_mov_b32 s60,s90
s_waitcnt vmcnt(6)
v_cmp_eq_u32 vcc,s48,v8
s_nop 0
s_cmp_eq_u64 vcc,exec
s_cselect_b32 s63,s54,256
s_cmp_lt_u32 s63,256
s_cbranch_scc1 Lr_tc1_%=
buffer_load_dword v8,v6,s[36:39],s91 offen offset:0 sc1
s_waitcnt vmcnt(0)
buffer_store_dword v7,v5,s[36:39],s60 offen offset:0 sc0 sc1
s_mov_b32 s60,536870912
v_cmp_eq_u32 vcc,s48,v8
s_nop 0
s_cmp_eq_u64 vcc,exec
s_cselect_b32 s63,s54,256
s_cmp_lt_u32 s63,256
s_cbranch_scc1 Lr_bq1_%=
s_cmp_ge_u32 s54,256
s_cbranch_scc1 Lr_done_%=
Lr_bp1_%=:
buffer_load_dword v8,v6,s[36:39],s91 offen offset:0 sc1
s_waitcnt vmcnt(0)
v_cmp_eq_u32 vcc,s48,v8
s_nop 0
s_cmp_eq_u64 vcc,exec
s_cselect_b32 s63,1,s96
s_cmp_lg_u32 s63,0
s_cbranch_scc1 Lr_bq1_%=
s_add_u32 s74,s74,1
s_cmp_ge_u32 s74,20000)"
R"(
s_cselect_b32 s96,1,0
s_sleep 4
s_branch Lr_bp1_%=
Lr_bq1_%=:
s_add_u32 s63,s87,71303168
buffer_load_dwordx4 a[0:3],v11,s[36:39],s63 offen offset:0 sc1
buffer_load_dwordx4 a[4:7],v11,s[36:39],s63 offen offset:1024 sc1
buffer_load_dwordx4 a[8:11],v11,s[36:39],s63 offen offset:2048 sc1
buffer_load_dwordx4 a[12:15],v11,s[36:39],s63 offen offset:3072 sc1
s_add_u32 s63,s87,89128960
buffer_load_dwordx4 a[16:19],v11,s[36:39],s63 offen offset:0 sc1
buffer_load_dwordx4 a[20:23],v11,s[36:39],s63 offen offset:1024 sc1
buffer_load_dwordx4 a[24:27],v11,s[36:39],s63 offen offset:2048 sc1
buffer_load_dwordx4 a[28:31],v11,s[36:39],s63 offen offset:3072 sc1
s_add_u32 s63,s87,99614720
buffer_load_dwordx4 a[32:35],v11,s[36:39],s63 offen offset:0 sc1
buffer_load_dwordx4 a[36:39],v11,s[36:39],s63 offen offset:1024 sc1
buffer_load_dwordx4 a[40:43],v11,s[36:39],s63 offen offset:2048 sc1
buffer_load_dwordx4 a[44:47],v11,s[36:39],s63 offen offset:3072 sc1
s_add_u32 s63,s87,109051904
buffer_load_dwordx4 a[48:51],v11,s[36:39],s63 offen offset:0 sc1
buffer_load_dwordx4 a[52:55],v11,s[36:39],s63 offen offset:1024 sc1
buffer_load_dwordx4 a[56:59],v11,s[36:39],s63 offen offset:2048 sc1
buffer_load_dwordx4 a[60:63],v11,s[36:39],s63 offen offset:3072 sc1
buffer_load_dword v8,v6,s[36:39],s69 offen offset:0 sc1
s_waitcnt vmcnt(0)
s_branch Lr_it0_%=
Lr_ta1_%=:
ds_read_b128 v[32:35],v18 offset:0
ds_read_b128 v[36:39],v18 offset:1024
ds_read_b128 v[40:43],v18 offset:2048
ds_read_b128 v[44:47],v18 offset:3072
v_mfma_f32_16x16x32_bf16 v[128:131],v[20:23],a[64:67],0
s_add_u32 s63,s87,71303168
v_mfma_f32_16x16x32_bf16 v[132:135],v[24:27],a[64:67],0
buffer_load_dwordx4 a[0:3],v11,s[36:39],s63 offen offset:0 sc1
v_mfma_f32_16x16x32_bf16 v[128:131],v[20:23],a[80:83],v[128:131]
v_mfma_f32_16x16x32_bf16 v[132:135],v[24:27],a[80:83],v[132:135]
buffer_load_dwordx4 a[4:7],v11,s[36:39],s63 offen offset:1024 sc1
v_mfma_f32_16x16x32_bf16 v[128:131],v[20:23],a[96:99],v[128:131]
v_mfma_f32_16x16x32_bf16 v[132:135],v[24:27],a[96:99],v[132:135]
v_mfma_f32_16x16x32_bf16 v[128:131],v[20:23],a[112:115],v[128:131]
v_mfma_f32_16x16x32_bf16 v[132:135],v[24:27],a[112:115],v[132:135]
buffer_load_dwordx4 a[8:11],v11,s[36:39],s63 offen offset:2048 sc1
v_mfma_f32_16x16x32_bf16 v[136:139],v[20:23],a[68:71],0
v_mfma_f32_16x16x32_bf16 v[140:143],v[24:27],a[68:71],0
v_mfma_f32_16x16x32_bf16 v[136:139],v[20:23],a[84:87],v[136:139]
v_mfma_f32_16x16x32_bf16 v[140:143],v[24:27],a[84:87],v[140:143]
buffer_load_dwordx4 a[12:15],v11,s[36:39],s63 offen offset:3072 sc1
v_mfma_f32_16x16x32_bf16 v[136:139],v[20:23],a[100:103],v[136:139]
v_cvt_pk_bf16_f32 v208,v128,v129
v_cvt_pk_bf16_f32 v209,v130,v131
v_mfma_f32_16x16x32_bf16 v[140:143],v[24:27],a[100:103],v[140:143]
v_cvt_pk_bf16_f32 v210,v132,v133
v_cvt_pk_bf16_f32 v211,v134,v135
v_mfma_f32_16x16x32_bf16 v[136:139],v[20:23],a[116:119],v[136:139]
s_add_u32 s63,s87,89128960
v_mfma_f32_16x16x32_bf16 v[140:143],v[24:27],a[116:119],v[140:143]
buffer_load_dwordx4 a[16:19],v11,s[36:39],s63 offen offset:0 sc1
v_mfma_f32_16x16x32_bf16 v[144:147],v[20:23],a[72:75],0
v_mfma_f32_16x16x32_bf16 v[148:151],v[24:27],a[72:75],0
v_mfma_f32_16x16x32_bf16 v[144:147],v[20:23],a[88:91],v[144:147]
v_mfma_f32_16x16x32_bf16 v[148:151],v[24:27],a[88:91],v[148:151]
buffer_load_dwordx4 a[20:23],v11,s[36:39],s63 offen offset:1024 sc1
v_mfma_f32_16x16x32_bf16 v[144:147],v[20:23],a[104:107],v[144:147]
v_cvt_pk_bf16_f32 v212,v136,v137
v_cvt_pk_bf16_f32 v213,v138,v139
v_mfma_f32_16x16x32_bf16 v[148:151],v[24:27],a[104:107],v[148:151]
v_cvt_pk_bf16_f32 v214,v140,v141
v_cvt_pk_bf16_f32 v215,v142,v143
v_mfma_f32_16x16x32_bf16 v[144:147],v[20:23],a[120:123],v[144:147]
v_mfma_f32_16x16x32_bf16 v[148:151],v[24:27],a[120:123],v[148:151]
buffer_load_dwordx4 a[24:27],v11,s[36:39],s63 offen offset:2048 sc1
v_mfma_f32_16x16x32_bf16 v[28:31],v[192:195],v[208:211],0
v_mfma_f32_16x16x32_bf16 v[204:207],v[208:211],v[208:211],0
v_mfma_f32_16x16x32_bf16 v[152:155],v[20:23],a[76:79],0
v_mfma_f32_16x16x32_bf16 v[156:159],v[24:27],a[76:79],0
buffer_load_dwordx4 a[28:31],v11,s[36:39],s63 offen offset:3072 sc1
v_mfma_f32_16x16x32_bf16 v[152:155],v[20:23],a[92:95],v[152:155]
s_add_u32 s63,s87,99614720
v_mfma_f32_16x16x32_bf16 v[156:159],v[24:27],a[92:95],v[156:159]
v_mfma_f32_16x16x32_bf16 v[152:155],v[20:23],a[108:111],v[152:155]
v_cvt_pk_bf16_f32 v216,v144,v145
v_cvt_pk_bf16_f32 v217,v146,v147
v_mfma_f32_16x16x32_bf16 v[156:159],v[24:27],a[108:111],v[156:159]
v_cvt_pk_bf16_f32 v218,v148,v149
v_cvt_pk_bf16_f32 v219,v150,v151
v_mfma_f32_16x16x32_bf16 v[152:155],v[20:23],a[124:127],v[152:155]
buffer_load_dwordx4 a[32:35],v11,s[36:39],s63 offen offset:0 sc1
v_mfma_f32_16x16x32_bf16 v[156:159],v[24:27],a[124:127],v[156:159]
v_mfma_f32_16x16x32_bf16 v[28:31],v[192:195],v[212:215],v[28:31]
v_mfma_f32_16x16x32_bf16 v[204:207],v[212:215],v[212:215],v[204:207]
buffer_load_dwordx4 a[36:39],v11,s[36:39],s63 offen offset:1024 sc1
v_mfma_f32_16x16x32_bf16 v[28:31],v[192:195],v[216:219],v[28:31]
v_mfma_f32_16x16x32_bf16 v[204:207],v[216:219],v[216:219],v[204:207]
s_waitcnt lgkmcnt(0)
v_mfma_f32_16x16x32_bf16 v[160:163],v[20:23],v[32:35],0
v_cvt_pk_bf16_f32 v220,v152,v153
v_cvt_pk_bf16_f32 v221,v154,v155
v_mfma_f32_16x16x32_bf16 v[164:167],v[24:27],v[32:35],0
v_cvt_pk_bf16_f32 v222,v156,v157
v_cvt_pk_bf16_f32 v223,v158,v159
v_mfma_f32_16x16x32_bf16 v[168:171],v[20:23],v[36:39],0
buffer_load_dwordx4 a[40:43],v11,s[36:39],s63 offen offset:2048 sc1
v_mfma_f32_16x16x32_bf16 v[28:31],v[192:195],v[220:223],v[28:31]
buffer_load_dwordx4 a[44:47],v11,s[36:39],s63 offen offset:3072 sc1
v_mfma_f32_16x16x32_bf16 v[204:207],v[220:223],v[220:223],v[204:207]
s_add_u32 s63,s87,109051904
v_mfma_f32_16x16x32_bf16 v[172:175],v[24:27],v[36:39],0
v_mfma_f32_16x16x32_bf16 v[176:179],v[20:23],v[40:43],0
buffer_load_dwordx4 a[48:51],v11,s[36:39],s63 offen offset:0 sc1
v_mfma_f32_16x16x32_bf16 v[180:183],v[24:27],v[40:43],0
s_nop 0
v_add_f32_dpp v224,v28,v28 row_ror:8 row_mask:0xf bank_mask:0xf
s_nop 0
v_cndmask_b32 v225,v204,v205,s[46:47]
v_cndmask_b32 v226,v206,v207,s[46:47]
v_cndmask_b32 v225,v225,v226,s[56:57]
v_mfma_f32_16x16x32_bf16 v[184:187],v[20:23],v[44:47],0
ds_write_b32 v2,v224 offset:0
ds_write_b32 v0,v225 offset:0
v_mfma_f32_16x16x32_bf16 v[188:191],v[24:27],v[44:47],0
s_waitcnt lgkmcnt(0)
buffer_load_dwordx4 a[52:55],v11,s[36:39],s63 offen offset:1024 sc1
s_barrier
buffer_load_dwordx4 a[56:59],v11,s[36:39],s63 offen offset:2048 sc1
ds_read_b128 v[196:199],v3 offset:0
buffer_load_dwordx4 a[60:63],v11,s[36:39],s63 offen offset:3072 sc1
ds_read_b128 v[200:203],v3 offset:16
ds_read_b128 v[204:207],v3 offset:32
s_waitcnt lgkmcnt(0)
buffer_load_dword v8,v6,s[36:39],s69 offen offset:0 sc1
v_add_f32 v196,v196,v197
v_add_f32 v198,v198,v199
v_add_f32 v200,v200,v204
v_add_f32 v201,v201,v205
v_add_f32 v202,v202,v206
v_add_f32 v203,v203,v207
v_add_f32 v196,v196,v198
v_add_f32 v200,v200,v201
v_add_f32 v202,v202,v203
v_mul_f32 v196,s76,v196
v_add_f32 v197,v200,v202
v_mul_f32 v197,s76,v197
v_fma_f32 v197,-v196,v196,v197
v_max_f32 v197,0,v197
v_add_f32 v197,s77,v197
v_rsq_f32 v228,v197
s_nop 0
v_mul_f32_e64 v230,-v196,v228
v_mov_b32 v229,v228
v_mov_b32 v231,v230
v_pk_fma_f32 v[196:197],v[128:129],v[228:229],v[230:231]
v_pk_fma_f32 v[198:199],v[130:131],v[228:229],v[230:231]
v_pk_fma_f32 v[200:201],v[132:133],v[228:229],v[230:231]
v_pk_fma_f32 v[202:203],v[134:135],v[228:229],v[230:231]
v_pk_fma_f32 v[128:129],v[196:197],v[160:161],v[128:129]
v_pk_fma_f32 v[130:131],v[198:199],v[162:163],v[130:131]
v_pk_fma_f32 v[132:133],v[200:201],v[164:165],v[132:133]
v_pk_fma_f32 v[134:135],v[202:203],v[166:167],v[134:135]
v_pk_fma_f32 v[196:197],v[136:137],v[228:229],v[230:231]
v_pk_fma_f32 v[198:199],v[138:139],v[228:229],v[230:231]
v_pk_fma_f32 v[200:201],v[140:141],v[228:229],v[230:231]
v_pk_fma_f32 v[202:203],v[142:143],v[228:229],v[230:231]
v_pk_fma_f32 v[136:137],v[196:197],v[168:169],v[136:137]
v_pk_fma_f32 v[138:139],v[198:199],v[170:171],v[138:139]
v_pk_fma_f32 v[140:141],v[200:201],v[172:173],v[140:141]
v_pk_fma_f32 v[142:143],v[202:203],v[174:175],v[142:143]
v_pk_fma_f32 v[196:197],v[144:145],v[228:229],v[230:231]
v_pk_fma_f32 v[198:199],v[146:147],v[228:229],v[230:231]
v_pk_fma_f32 v[200:201],v[148:149],v[228:229],v[230:231]
v_pk_fma_f32 v[202:203],v[150:151],v[228:229],v[230:231]
v_pk_fma_f32 v[144:145],v[196:197],v[176:177],v[144:145]
v_pk_fma_f32 v[146:147],v[198:199],v[178:179],v[146:147]
v_pk_fma_f32 v[148:149],v[200:201],v[180:181],v[148:149]
v_pk_fma_f32 v[150:151],v[202:203],v[182:183],v[150:151]
v_pk_fma_f32 v[196:197],v[152:153],v[228:229],v[230:231]
v_pk_fma_f32 v[198:199],v[154:155],v[228:229],v[230:231]
v_pk_fma_f32 v[200:201],v[156:157],v[228:229],v[230:231]
v_pk_fma_f32 v[202:203],v[158:159],v[228:229],v[230:231]
v_pk_fma_f32 v[152:153],v[196:197],v[184:185],v[152:153]
v_pk_fma_f32 v[154:155],v[198:199],v[186:187],v[154:155]
v_pk_fma_f32 v[156:157],v[200:201],v[188:189],v[156:157]
v_pk_fma_f32 v[158:159],v[202:203],v[190:191],v[158:159]
v_cvt_pk_bf16_f32 v208,v128,v129
v_cvt_pk_bf16_f32 v209,v130,v131
v_cvt_pk_bf16_f32 v210,v132,v133
v_cvt_pk_bf16_f32 v211,v134,v135
v_cvt_pk_bf16_f32 v212,v136,v137
v_cvt_pk_bf16_f32 v213,v138,v139
v_cvt_pk_bf16_f32 v214,v140,v141
v_cvt_pk_bf16_f32 v215,v142,v143
v_mfma_f32_16x16x32_bf16 v[28:31],v[192:195],v[208:211],0
v_cvt_pk_bf16_f32 v216,v144,v145
v_cvt_pk_bf16_f32 v217,v146,v147
v_mfma_f32_16x16x32_bf16 v[204:207],v[208:211],v[208:211],0
v_cvt_pk_bf16_f32 v218,v148,v149
v_cvt_pk_bf16_f32 v219,v150,v151
v_mfma_f32_16x16x32_bf16 v[28:31],v[192:195],v[212:215],v[28:31]
v_cvt_pk_bf16_f32 v220,v152,v153)"
R"(
v_cvt_pk_bf16_f32 v221,v154,v155
v_mfma_f32_16x16x32_bf16 v[204:207],v[212:215],v[212:215],v[204:207]
v_cvt_pk_bf16_f32 v222,v156,v157
v_cvt_pk_bf16_f32 v223,v158,v159
v_mfma_f32_16x16x32_bf16 v[28:31],v[192:195],v[216:219],v[28:31]
v_mfma_f32_16x16x32_bf16 v[204:207],v[216:219],v[216:219],v[204:207]
v_mfma_f32_16x16x32_bf16 v[28:31],v[192:195],v[220:223],v[28:31]
v_mfma_f32_16x16x32_bf16 v[204:207],v[220:223],v[220:223],v[204:207]
s_waitcnt vmcnt(17)
buffer_store_dword v7,v5,s[36:39],s60 offen offset:0 sc0 sc1
s_mov_b32 s60,536870912
buffer_store_dwordx4 v[208:211],v1,s[36:39],s88 offen offset:0 sc0 sc1
buffer_store_dwordx4 v[212:215],v1,s[36:39],s88 offen offset:2048 sc0 sc1
s_add_u32 s88,s88,4096
s_nop 0
v_add_f32_dpp v224,v28,v28 row_ror:8 row_mask:0xf bank_mask:0xf
v_cndmask_b32 v225,v204,v205,s[46:47]
v_cndmask_b32 v226,v206,v207,s[46:47]
v_cndmask_b32 v225,v225,v226,s[56:57]
s_and_b32 s63,s89,128
ds_write_b32 v2,v224 offset:1024
ds_write_b32 v0,v225 offset:1024
buffer_store_dwordx4 v[216:219],v1,s[36:39],s88 offen offset:0 sc0 sc1
buffer_store_dwordx4 v[220:223],v1,s[36:39],s88 offen offset:2048 sc0 sc1
s_waitcnt lgkmcnt(0)
s_barrier
ds_read_b128 v[196:199],v3 offset:1024
ds_read_b128 v[200:203],v3 offset:1040
ds_read_b128 v[204:207],v3 offset:1056
s_waitcnt lgkmcnt(0)
v_add_f32 v196,v196,v197
v_add_f32 v198,v198,v199
v_add_f32 v200,v200,v204
v_add_f32 v201,v201,v205
v_add_f32 v202,v202,v206
v_add_f32 v203,v203,v207
v_add_f32 v196,v196,v198
v_add_f32 v200,v200,v201
v_add_f32 v202,v202,v203
v_mul_f32 v196,s76,v196
v_add_f32 v197,v200,v202
v_mul_f32 v197,s76,v197
v_fma_f32 v197,-v196,v196,v197
v_max_f32 v197,0,v197
v_add_f32 v198,s77,v197
v_fma_f32 v199,s77,v198,v197
v_rsq_f32 v205,v198
v_rsq_f32 v198,v199
v_mov_b32 v200,v196
v_mul_f32_e64 v204,-v196,v205
v_mul_f32 v203,v199,v198
v_mul_f32 v201,s78,v198
v_mul_f32 v199,v198,v198
v_mul_f32 v202,s79,v199
v_mul_f32 v202,v202,v201
s_lshr_b32 s63,s63,1
s_sub_u32 s63,s89,s63
buffer_store_dwordx4 v[200:203],v4,s[36:39],s89 offen offset:0 sc0 sc1
buffer_store_dwordx2 v[204:205],v10,s[36:39],s63 offen offset:256 sc0 sc1
s_add_u32 m0,s66,s52
s_cmp_ge_u32 s51,12582912
s_cbranch_scc1 Lr_ys5_%=
buffer_load_dwordx4 v11,s[36:39],s51 offen offset:0 sc1 lds
buffer_load_dwordx4 v11,s[36:39],s51 offen offset:1024 sc1 lds
buffer_load_dwordx4 v11,s[36:39],s51 offen offset:2048 sc1 lds
buffer_load_dwordx4 v11,s[36:39],s51 offen offset:3072 sc1 lds
s_lshl_b32 s63,s50,14
s_add_u32 s51,s51,s63
Lr_ys5_%=:
s_add_u32 s66,s66,16384
s_cmp_eq_u32 s66,151552
s_cselect_b32 s66,4096,s66
v_add_u32 v18,s66,v11
s_mov_b32 s60,s90
s_waitcnt vmcnt(7)
s_branch Lr_it0_%=
Lr_tc1_%=:
s_add_u32 s63,s87,71303168
buffer_load_dwordx4 a[0:3],v11,s[36:39],s63 offen offset:0 sc1
buffer_load_dwordx4 a[4:7],v11,s[36:39],s63 offen offset:1024 sc1
buffer_load_dwordx4 a[8:11],v11,s[36:39],s63 offen offset:2048 sc1
buffer_load_dwordx4 a[12:15],v11,s[36:39],s63 offen offset:3072 sc1
s_add_u32 s63,s87,89128960
buffer_load_dwordx4 a[16:19],v11,s[36:39],s63 offen offset:0 sc1
buffer_load_dwordx4 a[20:23],v11,s[36:39],s63 offen offset:1024 sc1
buffer_load_dwordx4 a[24:27],v11,s[36:39],s63 offen offset:2048 sc1
buffer_load_dwordx4 a[28:31],v11,s[36:39],s63 offen offset:3072 sc1
s_add_u32 s63,s87,99614720
buffer_load_dwordx4 a[32:35],v11,s[36:39],s63 offen offset:0 sc1
buffer_load_dwordx4 a[36:39],v11,s[36:39],s63 offen offset:1024 sc1
buffer_load_dwordx4 a[40:43],v11,s[36:39],s63 offen offset:2048 sc1
buffer_load_dwordx4 a[44:47],v11,s[36:39],s63 offen offset:3072 sc1
s_add_u32 s63,s87,109051904
buffer_load_dwordx4 a[48:51],v11,s[36:39],s63 offen offset:0 sc1
buffer_load_dwordx4 a[52:55],v11,s[36:39],s63 offen offset:1024 sc1
buffer_load_dwordx4 a[56:59],v11,s[36:39],s63 offen offset:2048 sc1
buffer_load_dwordx4 a[60:63],v11,s[36:39],s63 offen offset:3072 sc1
buffer_load_dword v8,v6,s[36:39],s69 offen offset:0 sc1
s_waitcnt vmcnt(17)
buffer_store_dword v7,v5,s[36:39],s60 offen offset:0 sc0 sc1
s_mov_b32 s60,536870912
s_waitcnt vmcnt(1)
s_branch Lr_it0_%=
Lr_done_%=:
s_waitcnt vmcnt(0) lgkmcnt(0)
s_barrier
Lcode_r_e:)"
  :: "s"(ws), "s"(sK), "s"(sStep), "s"(sRq), "s"(sEpR), "v"(vRP), "v"(vRF), "v"(vRwr), "v"(vRrd), "v"(vRS), "v"(vFlag), "v"(vL4), "v"(vRL) : "v0", "v1", "v2", "v3", "v4", "v5", "v6", "v7", "v8", "v9", "v10", "v11", "v12", "v13", "v14", "v15", "v16", "v17", "v18", "v19", "v20", "v21", "v22", "v23", "v24", "v25", "v26", "v27", "v28", "v29", "v30", "v31", "v32", "v33", "v34", "v35", "v36", "v37", "v38", "v39", "v40", "v41", "v42", "v43", "v44", "v45", "v46", "v47", "v48", "v49", "v50", "v51", "v52", "v53", "v54", "v55", "v56", "v57", "v58", "v59", "v60", "v61", "v62", "v63", "v64", "v65", "v66", "v67", "v68", "v69", "v70", "v71", "v72", "v73", "v74", "v75", "v76", "v77", "v78", "v79", "v80", "v81", "v82", "v83", "v84", "v85", "v86", "v87", "v88", "v89", "v90", "v91", "v92", "v93", "v94", "v95", "v96", "v97", "v98", "v99", "v100", "v101", "v102", "v103", "v104", "v105", "v106", "v107", "v108", "v109", "v110", "v111", "v112", "v113", "v114", "v115", "v116", "v117", "v118", "v119", "v120", "v121", "v122", "v123", "v124", "v125", "v126", "v127", "v128", "v129", "v130", "v131", "v132", "v133", "v134", "v135", "v136", "v137", "v138", "v139", "v140", "v141", "v142", "v143", "v144", "v145", "v146", "v147", "v148", "v149", "v150", "v151", "v152", "v153", "v154", "v155", "v156", "v157", "v158", "v159", "v160", "v161", "v162", "v163", "v164", "v165", "v166", "v167", "v168", "v169", "v170", "v171", "v172", "v173", "v174", "v175", "v176", "v177", "v178", "v179", "v180", "v181", "v182", "v183", "v184", "v185", "v186", "v187", "v188", "v189", "v190", "v191", "v192", "v193", "v194", "v195", "v196", "v197", "v198", "v199", "v200", "v201", "v202", "v203", "v204", "v205", "v206", "v207", "v208", "v209", "v210", "v211", "v212", "v213", "v214", "v215", "v216", "v217", "v218", "v219", "v220", "v221", "v222", "v223", "v224", "v225", "v226", "v227", "v228", "v229", "v230", "v231", "v255", "a0", "a1", "a2", "a3", "a4", "a5", "a6", "a7", "a8", "a9", "a10", "a11", "a12", "a13", "a14", "a15", "a16", "a17", "a18", "a19", "a20", "a21", "a22", "a23", "a24", "a25", "a26", "a27", "a28", "a29", "a30", "a31", "a32", "a33", "a34", "a35", "a36", "a37", "a38", "a39", "a40", "a41", "a42", "a43", "a44", "a45", "a46", "a47", "a48", "a49", "a50", "a51", "a52", "a53", "a54", "a55", "a56", "a57", "a58", "a59", "a60", "a61", "a62", "a63", "a64", "a65", "a66", "a67", "a68", "a69", "a70", "a71", "a72", "a73", "a74", "a75", "a76", "a77", "a78", "a79", "a80", "a81", "a82", "a83", "a84", "a85", "a86", "a87", "a88", "a89", "a90", "a91", "a92", "a93", "a94", "a95", "a96", "a97", "a98", "a99", "a100", "a101", "a102", "a103", "a104", "a105", "a106", "a107", "a108", "a109", "a110", "a111", "a112", "a113", "a114", "a115", "a116", "a117", "a118", "a119", "a120", "a121", "a122", "a123", "a124", "a125", "a126", "a127", "a128", "a129", "a130", "a131", "a132", "a133", "a134", "a135", "a136", "a137", "a138", "a139", "a140", "a141", "a142", "a143", "a144", "a145", "a146", "a147", "a148", "a149", "a150", "a151", "a152", "a153", "a154", "a155", "a156", "a157", "a158", "a159", "a160", "a161", "a162", "a163", "a164", "a165", "a166", "a167", "a168", "a169", "a170", "a171", "a172", "a173", "a174", "a175", "a176", "a177", "a178", "a179", "a180", "a181", "a182", "a183", "a184", "a185", "a186", "a187", "a188", "a189", "a190", "a191", "a192", "a193", "a194", "a195", "a196", "a197", "a198", "a199", "a200", "a201", "a202", "a203", "a204", "a205", "a206", "a207", "a208", "a209", "a210", "a211", "a212", "a213", "a214", "a215", "a216", "a217", "a218", "a219", "a220", "a221", "a222", "a223", "a224", "a225", "a226", "a227", "a228", "a229", "a230", "a231", "a232", "a233", "a234", "a235", "a236", "a237", "a238", "a239", "a240", "a241", "a242", "a243", "a244", "a245", "a246", "a247", "a248", "a249", "a250", "a251", "a252", "a253", "a254", "a255", "s36", "s37", "s38", "s39", "s40", "s41", "s42", "s43", "s44", "s45", "s46", "s47", "s48", "s49", "s50", "s51", "s52", "s53", "s54", "s55", "s56", "s57", "s58", "s59", "s60", "s61", "s62", "s63", "s64", "s65", "s66", "s67", "s68", "s69", "s70", "s71", "s72", "s73", "s74", "s75", "s76", "s77", "s78", "s79", "s80", "s81", "s82", "s83", "s84", "s85", "s86", "s87", "s88", "s89", "s90", "s91", "s92", "s93", "s94", "s95", "s96", "s97", "s98", "s99", "vcc", "scc", "memory");
    }
    LANE_ID(lane2);
    const int wk = kind == 6 ? slice : NRW + (kind - 1) * 32 + rep * 8 + slice;
    for (int t = wk; t < 256 - FINE; t += 82) row_last(ws, out, t, lane2, wave, ep);
    if (wk * 4 + wave < FINE * 16) chunk_last(ws, out, (256 - FINE) * 16 + wk * 4 + wave, lane2, ep);
    if (b == 0 && lane2 == 0 && wave == 0) __hip_atomic_store((gu32*)(ws + OFF_EP), ep, __ATOMIC_RELAXED, __HIP_MEMORY_SCOPE_AGENT);
    asm volatile("s_endpgm" ::: "memory");
    __builtin_unreachable();
  }
}

extern "C" void kernel_launch(void* const* d_in, const int* in_sizes, int n_in, void* d_out, int out_size,
                              void* d_ws, size_t ws_size, hipStream_t stream) {
  const u16* q = (const u16*)d_in[0];
  const u16* k = (const u16*)d_in[1];
  const u16* v = (const u16*)d_in[2];
  const u16* shift_k = (const u16*)d_in[3];
  const u16* ssm_k = (const u16*)d_in[4];
  const u16* ssm_D = (const u16*)d_in[5];
  const u16* w1 = (const u16*)d_in[7];
  const u16* w2 = (const u16*)d_in[8];
  const u16* w3 = (const u16*)d_in[9];
  const u16* w4 = (const u16*)d_in[10];
  flow_kernel<<<256, 256, 0, stream>>>(q, k, v, shift_k, ssm_k, ssm_D, w1, w2, w3, w4, (char*)d_ws, (u16*)d_out);
}
```

```cpp
#include <hip/hip_runtime.h>
#include <hip/hip_bf16.h>
#include <stdint.h>
#include <type_traits>

typedef short s16x8 __attribute__((ext_vector_type(8)));
typedef short s16x4 __attribute__((ext_vector_type(4)));
typedef float f32x4 __attribute__((ext_vector_type(4)));
typedef unsigned short u16;
typedef unsigned short u16x8 __attribute__((ext_vector_type(8)));
typedef unsigned short u16x4 __attribute__((ext_vector_type(4)));
typedef float f32x2 __attribute__((ext_vector_type(2)));
typedef __bf16 bf16x2 __attribute__((ext_vector_type(2)));
typedef unsigned u32x4v __attribute__((ext_vector_type(4)));
typedef __attribute__((address_space(1))) unsigned gu32;

constexpr int S_ = 2048, D_ = 1024, F_ = 3584;

constexpr int N1 = 195, PL1 = 264, KE = 1, RT1 = 0, LB = 66, FL_BYTES = 2141312, WS_USED = 122727552;
constexpr int OFF_KVR = 4194304, OFF_YF = 8388608, OFF_XP = 12582912, OFF_HS = 22085632, OFF_X1 = 31457280, OFF_G = 40894464, OFF_P0 = 71303168, OFF_DBG = 80740352, OFF_P1 = 89128960, OFF_QS = 98566144, OFF_P2 = 99614720, OFF_P3 = 109051904, OFF_S1 = 118489088, OFF_ST = 119013376, OFF_FL = 120586240, OFF_FXP = 120587264, OFF_FH = 120859648, OFF_FX1 = 121132032, OFF_FP = 121405440, OFF_FG = 121950720, OFF_FKV = 122629120, OFF_FY = 122661888, OFF_EP = 122727424;


__device__ __forceinline__ float bf2f(u16 v) { return __uint_as_float(((unsigned)v) << 16); }
__device__ __forceinline__ u16 f2bf(float f) { return __builtin_bit_cast(u16, (__bf16)f); }
__device__ __forceinline__ float rbf(float f) { return bf2f(f2bf(f)); }
__device__ __forceinline__ void st16(void* p, u16x8 v) {
  asm volatile("global_store_dwordx4 %0, %1, off sc0 sc1\n\ts_nop 1" :: "v"(p), "v"(v) : "memory");
}
__device__ __forceinline__ void bst16(u32x4v v, __amdgpu_buffer_rsrc_t rs, unsigned vo, unsigned so) {
  asm volatile("buffer_store_dwordx4 %0, %1, %2, %3 offen sc0 sc1\n\ts_nop 1" :: "v"(v), "v"(vo), "s"(rs), "s"(so) : "memory");
}
#define WAIT_VM(n) asm volatile("s_waitcnt vmcnt(%0)" ::"n"(n) : "memory")
#define WAIT_LGKM0() asm volatile("s_waitcnt lgkmcnt(0)" ::: "memory")

__device__ __forceinline__ void touch_lds(const char* p, unsigned a) {
  asm volatile("s_mov_b32 m0, %1\n\ts_nop 0\n\tglobal_load_lds_dword %0, off" :: "v"(p), "s"(a) : "memory");
}
#define YSLEEP 4
#define KSLEEP 2
__device__ __forceinline__ f32x2 up2(unsigned p) { return (f32x2){__uint_as_float(p << 16), __uint_as_float(p & 0xffff0000u)}; }
__device__ __forceinline__ unsigned pk2(f32x2 v) { return __builtin_bit_cast(unsigned, __builtin_convertvector(v, bf16x2)); }
__device__ __forceinline__ f32x2 rbf2(f32x2 v) { return up2(pk2(v)); }
#define BARRIER() asm volatile("s_waitcnt lgkmcnt(0)\n\ts_barrier" ::: "memory")
template <int N, class F> __device__ __forceinline__ void sfor(F&& f) {
  if constexpr (N > 0) { sfor<N - 1>(f); f(std::integral_constant<int, N - 1>{}); }
}
template <int NL>
__device__ __forceinline__ void poll_flags(const char* p, int n, int lane, unsigned ep) {
#pragma unroll 1
  for (int it = 0; it < 20000; ++it) {
    bool ok = true;
#pragma unroll
    for (int i = 0; i < NL; ++i) {
      const int f = lane + 64 * i;
      ok &= __hip_atomic_load((gu32*)(p + (f < n ? f : 0) * 128), __ATOMIC_RELAXED, __HIP_MEMORY_SCOPE_AGENT) == ep;
    }
    if (__all(ok)) break;
    __builtin_amdgcn_s_sleep(YSLEEP);
  }
  asm volatile("" ::: "memory");
}

constexpr int CV_HP = (2048 + 32) * 2;
constexpr int CV_UR = (2048 + 256) * 2;
constexpr int CV_CH = 2 * CV_HP + CV_UR;
#define T0 0
#define T0R 4
#define T1 0
#define LATEQ 4
constexpr int CV_STG = 8 * CV_CH;

struct KvRegs { u32x4v k0, k1, k2, k3, k4, k5, k6, v0, v1, v2, v3; };
struct SkRegs { u32x4v s0, s1, s2, s3; };
#define LD16(d, p, pol) asm volatile("global_load_dwordx4 %0, %1, off " pol : "=v"(d) : "v"(p) : "memory")
#define LDS16(d, vo, sp, o, pol) asm volatile("global_load_dwordx4 %0, %1, %2 offset:" #o " " pol : "=v"(d) : "v"(vo), "s"(sp) : "memory")
__device__ __forceinline__ void kv_load(KvRegs& R, const u16* __restrict__ kin, const u16* __restrict__ vin, int b, int tid) {
  const unsigned tr = (b >> 2) * 32 + 4 * (tid >> 5), oa = tr * 2048 + (b & 3) * 512 + 16 * (tid & 31) + 4096;
  LDS16(R.k3, oa, kin, -4096, "nt"); LDS16(R.k4, oa, kin, -2048, "nt"); LDS16(R.k5, oa, kin, 0, "nt"); LDS16(R.k6, oa, kin, 2048, "nt");
  LDS16(R.v0, oa, vin, -4096, "nt"); LDS16(R.v1, oa, vin, -2048, "nt"); LDS16(R.v2, oa, vin, 0, "nt"); LDS16(R.v3, oa, vin, 2048, "nt");
  const unsigned ob = tr ? oa - 6144 : oa;
  LDS16(R.k0, ob, kin, -4096, "nt"); LDS16(R.k1, ob, kin, -2048, "nt"); LDS16(R.k2, ob, kin, 0, "nt");
}
__device__ __forceinline__ void sk_load(SkRegs& R, const u16* __restrict__ shift_k, int b, int tid) {
  const unsigned o = (b & 3) * 2048 + 64 * (tid & 31);
  LDS16(R.s0, o, shift_k, 0, ""); LDS16(R.s1, o, shift_k, 16, ""); LDS16(R.s2, o, shift_k, 32, ""); LDS16(R.s3, o, shift_k, 48, "");
}
template <int N>
__device__ __forceinline__ void kv_wait(KvRegs& R, SkRegs& Q) {
  asm volatile("s_waitcnt vmcnt(%15)" : "+v"(R.k0), "+v"(R.k1), "+v"(R.k2), "+v"(R.k3), "+v"(R.k4), "+v"(R.k5), "+v"(R.k6), "+v"(R.v0), "+v"(R.v1), "+v"(R.v2),
               "+v"(R.v3), "+v"(Q.s0), "+v"(Q.s1), "+v"(Q.s2), "+v"(Q.s3) : "n"(N) : "memory");
}
__device__ __forceinline__ void kv_store(KvRegs& R, const SkRegs& Q, char* __restrict__ ws, char* T, int b, int tid) {
  const int t0 = (b >> 2) * 32, c0 = (b & 3) * 256, ch8 = tid & 31, rg = tid >> 5;
  if (t0 + 4 * rg < 3) R.k0 = R.k1 = R.k2 = (u32x4v){0u, 0u, 0u, 0u};
  typedef unsigned u32x2 __attribute__((ext_vector_type(2)));
  const u32x4v kr[7] = {R.k0, R.k1, R.k2, R.k3, R.k4, R.k5, R.k6}, vr[4] = {R.v0, R.v1, R.v2, R.v3}, sk[4] = {Q.s0, Q.s1, Q.s2, Q.s3};
#pragma unroll
  for (int j2 = 0; j2 < 4; ++j2) {
    const f32x2 sa = up2(sk[j2][0]), sb = up2(sk[j2][1]), sc = up2(sk[j2][2]), sd = up2(sk[j2][3]);
    const f32x2 s0 = {sa[0], sc[0]}, s1 = {sa[1], sc[1]}, s2 = {sb[0], sd[0]}, s3 = {sb[1], sd[1]};
    f32x2 kf[7], o[4];
#pragma unroll
    for (int r = 0; r < 7; ++r) kf[r] = up2(kr[r][j2]);
#pragma unroll
    for (int r = 0; r < 4; ++r)
      o[r] = __builtin_elementwise_fma(kf[r + 3], s0, __builtin_elementwise_fma(kf[r + 2], s1, __builtin_elementwise_fma(kf[r + 1], s2, kf[r] * s3))) * up2(vr[r][j2]);
#pragma unroll
    for (int e = 0; e < 2; ++e)
      *(u32x2*)(T + ch8 * 520 + (2 * j2 + e) * 64 + (28 - 4 * rg) * 2) = (u32x2){pk2((f32x2){o[3][e], o[2][e]}), pk2((f32x2){o[1][e], o[0][e]})};
  }
  BARRIER();
  u16* kvr = (u16*)(ws + OFF_KVR);
#pragma unroll
  for (int i = 0; i < 4; ++i) {
    const int ch = (tid >> 2) + 64 * i, cb = tid & 3;
    const char* p = T + ch * 64 + (ch >> 3) * 8 + cb * 16;
    const u16x4 lo = *(const u16x4*)p, hi = *(const u16x4*)(p + 8);
    st16(kvr + (size_t)(c0 + ch) * S_ + (2016 - t0) + cb * 8, __builtin_shufflevector(lo, hi, 0, 1, 2, 3, 4, 5, 6, 7));
  }
}

typedef unsigned u32x2v __attribute__((ext_vector_type(2)));
__device__ __forceinline__ void h_load(u32x4v (&hv)[8], unsigned& hx, unsigned& dD, const u16* __restrict__ ssm_k, const u16* __restrict__ ssm_D, int c0, int tid) {
  const __amdgpu_buffer_rsrc_t rs = __builtin_amdgcn_make_buffer_rsrc((void*)ssm_k, 0, c0 < 0 ? 0 : D_ * S_ * 2, 0x00020000);
  const unsigned cb = c0 < 0 ? 0 : c0, lane = tid & 63, wave = tid >> 6;
#pragma unroll
  for (int i = 0; i < 8; ++i) asm volatile("buffer_load_dwordx4 %0, %1, %2, %3 offen nt" : "=v"(hv[i]) : "v"(tid * 16), "s"(rs), "s"((cb + i) * 4096) : "memory");
  asm volatile("buffer_load_dword %0, %1, %2, %3 offen" : "=v"(hx) : "v"((lane & 7) * 4096 + ((wave + 1) & 3) * 1024), "s"(rs), "s"(cb * 4096) : "memory");
  asm volatile("global_load_dword %0, %1, %2" : "=v"(dD) : "v"(cb * 2 + wave * 4), "s"(ssm_D) : "memory");
}
template <int N>
__device__ __forceinline__ void h_wait(u32x4v (&hv)[8], unsigned& hx, unsigned& dD) {
  asm volatile("s_waitcnt vmcnt(%10)" : "+v"(hv[0]), "+v"(hv[1]), "+v"(hv[2]), "+v"(hv[3]), "+v"(hv[4]), "+v"(hv[5]), "+v"(hv[6]), "+v"(hv[7]), "+v"(hx), "+v"(dD) : "n"(N) : "memory");
}
__device__ __forceinline__ void h_spread(const u32x4v (&hv)[8], unsigned hx, char* lds, int tid) {
  if (tid < 32 && (tid & 3) != 3) *(u32x4v*)(lds + (tid >> 2) * CV_CH + ((tid >> 1) & 1) * CV_HP + (tid & 1) * 16) = (u32x4v){0u, 0u, 0u, 0u};
  *(u32x4v*)(lds + (tid >> 5) * CV_CH + 2 * CV_HP + 4096 + (tid & 31) * 16) = (u32x4v){0u, 0u, 0u, 0u};
#pragma unroll
  for (int i = 0; i < 8; ++i) {
    char* base = lds + i * CV_CH + 32 + tid * 16;
    unsigned hn = __builtin_amdgcn_update_dpp(0, (int)hv[i][0], 0x130, 0xf, 0xf, true);
    const unsigned last = __builtin_amdgcn_readlane(hx, i);
    if ((tid & 63) == 63) hn = last;
#define AB(a, b) __builtin_amdgcn_alignbyte(a, b, 2)
    if (tid == 0) *(u32x4v*)(lds + i * CV_CH + CV_HP + 16) = (u32x4v){0u, 0u, 0u, hv[i][0] << 16};
    *(u32x4v*)(base) = hv[i];
    *(u32x4v*)(base + CV_HP) = (u32x4v){AB(hv[i][1], hv[i][0]), AB(hv[i][2], hv[i][1]), AB(hv[i][3], hv[i][2]), AB(hn, hv[i][3])};
#undef AB
  }
}

__device__ __forceinline__ void pads(char* __restrict__ ws, int w, int lane, unsigned ep) {
  if (w < 132) {
    const int k = w < 128 ? w >> 5 : w - 128, u = k == 0 ? -1 : k == 1 ? 128 : k == 2 ? 131 : 260;
    st16(ws + (w < 128 ? OFF_HS + u * 32768 + (w & 31) * 1024 : OFF_FH + u * 1024) + lane * 16, __builtin_bit_cast(u16x8, (u32x4v){w >= 128 && !(lane & 7) ? ep : 0u, 0u, 0u, 0u}));
  }
}
__device__ __forceinline__ void prewrite(char* __restrict__ ws, unsigned off, unsigned str, int rep, int tid) {
#pragma unroll
  for (int i = 0; i < 2; ++i) st16(ws + off + (rep + 4 * (tid >> 3) + 132 * i) * str + (tid & 7) * 16, __builtin_bit_cast(u16x8, (u32x4v){0u, 0u, 0u, 0u}));
}

__device__ __forceinline__ void conv_part(char* __restrict__ ws, unsigned dD, char* lds, int g, int tid, int lane, int wave, unsigned ep, const char* wp, unsigned wstep) {
  const __amdgpu_buffer_rsrc_t rs = __builtin_amdgcn_make_buffer_rsrc(ws, 0, WS_USED, 0x00020000);
  const u16* kvr = (const u16*)(ws + OFF_KVR) + (size_t)(8 * g + 2 * wave) * S_;
  auto wait1 = [&](unsigned off) {
#pragma unroll 1
    for (int it = 0; it < 20000; ++it) {
      if (__all(__hip_atomic_load((gu32*)(ws + off), __ATOMIC_RELAXED, __HIP_MEMORY_SCOPE_AGENT) == ep)) break;
      __builtin_amdgcn_s_sleep(KSLEEP);
    }
    asm volatile("" ::: "memory");
  };
  const unsigned lb = (unsigned)(size_t)lds;
  auto u_fetch = [&](int part) {
#pragma unroll
    for (int j = 0; j < 2; ++j)
      asm volatile("s_mov_b32 m0, %1\n\ts_nop 0\n\tglobal_load_lds_dwordx4 %0, off sc1" :: "v"(kvr + j * S_ + part * 512 + lane * 8),
                   "s"(lb + (2 * wave + j) * CV_CH + 2 * CV_HP + part * 1024) : "memory");
  };
  const unsigned xo = (tid >> 4) * 32768 + g * 256 + (tid & 15) * 16;
  u32x4v qv[6];
  u32x2v qs[6];
  auto q_fetch = [&](int s0, int ns) {
#pragma unroll
    for (int i = 0; i < 6; ++i)
      if (i < ns) {
        asm volatile("buffer_load_dwordx4 %0, %1, %2, %3 offen sc1" : "=v"(qv[i]) : "v"(xo), "s"(rs), "s"(OFF_XP + (s0 + i) * 524288) : "memory");
        asm volatile("buffer_load_dwordx2 %0, %1, %2, %3 offen sc1" : "=v"(qs[i]) : "v"(tid * 8), "s"(rs), "s"(OFF_QS + (s0 + i) * 2048) : "memory");
      }
  };
  const int cg = g >> 5;
  const int l15 = lane & 15, lg = lane >> 4, cp = (l15 + 1) & 1;
  const unsigned ao = cp * CV_HP + 2 * (8 * lg + (l15 + 1 - cp)), bo = 2 * CV_HP + 2 * (2032 - 16 * l15 + 8 * lg) - 2 * 256 * 7;
  const unsigned own = lb + 2 * wave * CV_CH;
  char* stg = lds + CV_STG;
  s16x8 ad[2];
#pragma unroll
  for (int j = 0; j < 2; ++j) {
    const int e = 15 - l15 - 8 * lg;
    const unsigned d = (j ? dD >> 16 : dD & 0xffffu) << ((e & 1) * 16);
    ad[j] = __builtin_bit_cast(s16x8, (u32x4v){e >> 1 == 0 ? d : 0u, e >> 1 == 1 ? d : 0u, e >> 1 == 2 ? d : 0u, e >> 1 == 3 ? d : 0u});
  }
  f32x4 acc[2][6];
  unsigned f0, f1;
  bool mid;
  auto finish = [&](auto s0_c, auto ns_c) {
    constexpr int S0 = decltype(s0_c)::value, NS = decltype(ns_c)::value, A0 = S0 == 2 ? 0 : 4, Q0 = S0 == 6 ? 4 : 0;
#pragma unroll
    for (int s = 0; s < NS; ++s)
#pragma unroll
      for (int r = 0; r < 4; ++r) *(unsigned*)(stg + (Q0 + s) * 4352 + l15 * 272 + (4 * lg + r) * 16 + wave * 4) = pk2((f32x2){acc[0][A0 + s][r], acc[1][A0 + s][r]});
    if (S0 == 0) {
      asm volatile("s_waitcnt vmcnt(0)" : "+v"(qv[0]), "+v"(qv[1]), "+v"(qs[0]), "+v"(qs[1]), "+v"(f0) :: "memory");
      mid = __all(f0 == ep);
      if (mid) { u_fetch(2); u_fetch(1); }
    } else if (S0 == 2) asm volatile("" : "+v"(qv[0]), "+v"(qv[1]), "+v"(qv[2]), "+v"(qv[3]), "+v"(qv[4]), "+v"(qv[5]), "+v"(qs[0]), "+v"(qs[1]), "+v"(qs[2]), "+v"(qs[3]), "+v"(qs[4]), "+v"(qs[5]) :: "memory");
    BARRIER();
    u32x4v y4[NS];
#pragma unroll
    for (int s = 0; s < NS; ++s) {
      y4[s] = *(const u32x4v*)(stg + (Q0 + s) * 4352 + (tid >> 4) * 272 + (tid & 15) * 16);
      const f32x2 rd2 = {__uint_as_float(qs[Q0 + s][0]), __uint_as_float(qs[Q0 + s][0])}, nm2 = {__uint_as_float(qs[Q0 + s][1]), __uint_as_float(qs[Q0 + s][1])};
      u32x4v o;
#pragma unroll
      for (int e = 0; e < 4; ++e) {
        const f32x2 x = up2(qv[Q0 + s][e]);
        o[e] = pk2(__builtin_elementwise_fma(__builtin_elementwise_fma(x, rd2, nm2), up2(y4[s][e]), x));
      }
      bst16(o, rs, xo, OFF_XP + (S0 + s) * 524288);
    }
#pragma unroll
    for (int s = 0; s < NS; ++s) bst16(y4[s], rs, (tid >> 3) * 16384 + g * 128 + (tid & 7) * 16, OFF_YF + (S0 + s) * 524288);
  };
  auto touches = [&](int n) {
#pragma unroll
    for (int i = 0; i < 3; ++i)
      if (i < n) { touch_lds(wp, lb + 163584); wp += wstep; }
  };
  auto count = [&](int s0, int ns, auto n_c) {
    WAIT_VM(decltype(n_c)::value);
    BARRIER();
    if (tid < 4 * ns) __hip_atomic_fetch_add((gu32*)(ws + OFF_FXP + (s0 + (tid >> 2)) * 16384 + (tid & 3) * 1024 + (g >> 4) * 128), 1u, __ATOMIC_RELAXED, __HIP_MEMORY_SCOPE_AGENT);
  };
  auto pair = [&](unsigned bb) {
    u32x2v ax[2][2][2][2];
    s16x8 bx[2][2][2];
    auto rdA = [&](int q) {
#pragma unroll
      for (int j = 0; j < 2; ++j)
#pragma unroll
        for (int m = 0; m < 2; ++m) {
          asm volatile("ds_read2_b32 %0, %1 offset0:%2 offset1:%3" : "=v"(ax[q & 1][j][m][0]) : "v"(own + j * CV_CH + ao), "n"(128 * m + 16 * q), "n"(128 * m + 16 * q + 1));
          asm volatile("ds_read2_b32 %0, %1 offset0:%2 offset1:%3" : "=v"(ax[q & 1][j][m][1]) : "v"(own + j * CV_CH + ao), "n"(128 * m + 16 * q + 2), "n"(128 * m + 16 * q + 3));
          asm volatile("ds_read_b128 %0, %1 offset:%2" : "=v"(bx[q & 1][j][m]) : "v"(bb), "n"(j * CV_CH + 512 * (1 - m) + 64 * q));
        }
    };
    rdA(0);
    sfor<8>([&](auto q_c) {
      constexpr int q = decltype(q_c)::value, p = q & 1;
      if (q + 1 < 8) rdA(q + 1);
      asm volatile("s_waitcnt lgkmcnt(%0)" :: "n"(q + 1 < 8 ? 12 : 0) : "memory");
      __builtin_amdgcn_sched_barrier(0);
#pragma unroll
      for (int j = 0; j < 2; ++j) {
        const s16x8 a0f = __builtin_bit_cast(s16x8, __builtin_shufflevector(ax[p][j][0][0], ax[p][j][0][1], 0, 1, 2, 3)), a1f = __builtin_bit_cast(s16x8, __builtin_shufflevector(ax[p][j][1][0], ax[p][j][1][1], 0, 1, 2, 3));
        if (q == 0) {
          acc[j][4] = __builtin_amdgcn_mfma_f32_16x16x32_bf16(ad[j], bx[p][j][0], acc[j][4], 0, 0, 0);
          acc[j][5] = __builtin_amdgcn_mfma_f32_16x16x32_bf16(ad[j], bx[p][j][1], acc[j][5], 0, 0, 0);
        }
        acc[j][4] = __builtin_amdgcn_mfma_f32_16x16x32_bf16(a0f, bx[p][j][0], acc[j][4], 0, 0, 0);
        acc[j][5] = __builtin_amdgcn_mfma_f32_16x16x32_bf16(a0f, bx[p][j][1], acc[j][5], 0, 0, 0);
        acc[j][5] = __builtin_amdgcn_mfma_f32_16x16x32_bf16(a1f, bx[p][j][0], acc[j][5], 0, 0, 0);
      }
      __builtin_amdgcn_sched_barrier(0);
    });
  };
  wait1(OFF_FKV + ((lane & 15) * 4 + cg) * 128);
  u_fetch(3);
  wait1(OFF_FY + (lane & 31) * 128);
  q_fetch(0, 2);
  const unsigned fm = OFF_FKV + ((16 + (lane & 31)) * 4 + cg) * 128;
  asm volatile("global_load_dword %0, %1, %2 sc1" : "=v"(f0) : "v"(fm), "s"(ws) : "memory");
#pragma unroll
  for (int i = 0; i < 4; ++i) acc[i >> 1][4 + (i & 1)] = (f32x4){0.f, 0.f, 0.f, 0.f};
  pair(own + bo + 512 * 6);
  finish(std::integral_constant<int, 0>{}, std::integral_constant<int, 2>{});
  u32x2v a0[8][2], a1[8][2];
  s16x8 b0[6], b1[6];
  unsigned pa[4], pb = own + bo;
#pragma unroll
  for (int i = 0; i < 4; ++i) pa[i] = own + ao + 1024 * i;
  auto rd1 = [&](int rr, int i, u32x2v (&ah)[8][2], s16x8 (&bfr)[6]) {
    const int m = i < 18 ? i / 3 : 6 + (i - 18) / 2, e = i < 18 ? i % 3 : i & 1;
    if (e < 2) asm volatile("ds_read2_b32 %0, %1 offset0:%2 offset1:%3" : "=v"(ah[m][e & 1]) : "v"(pa[m >> 1]), "n"(128 * (m & 1) + 2 * e + 16 * rr), "n"(128 * (m & 1) + 2 * e + 16 * rr + 1));
    else asm volatile("ds_read_b128 %0, %1 offset:%2" : "=v"(bfr[m < 6 ? m : 0]) : "v"(pb), "n"(512 * (7 - m) + 64 * rr));
  };
  auto round = [&](f32x4 (&ac)[6], u32x2v (&ah)[8][2], s16x8 (&bfr)[6], u32x2v (&ahn)[8][2], s16x8 (&bfn)[6], int rr, unsigned step) {
    int k = 0;
#pragma unroll
    for (int s = 2; s <= 7; ++s)
#pragma unroll
      for (int m = 0; m <= s; ++m) {
        if (s - m > 5) continue;
        const s16x8 af = __builtin_bit_cast(s16x8, __builtin_shufflevector(ah[m][0], ah[m][1], 0, 1, 2, 3));
        ac[s - 2] = __builtin_amdgcn_mfma_f32_16x16x32_bf16(af, bfr[s - m > 5 ? 0 : s - m], ac[s - 2], 0, 0, 0);
        __builtin_amdgcn_sched_barrier(0);
        if (m == s) asm volatile("s_waitcnt lgkmcnt(12)" ::: "memory");
#pragma unroll
        for (int i = 0; i < 22; ++i)
          if (i * 4 / 3 == k) {
            rd1(rr, i, ahn, bfn);
            if (rr && (i == 4 || i == 10 || i == 16 || i == 21)) asm volatile("v_add_u32 %0, %1, %0" : "+v"(pa[i / 6]) : "s"(step));
            if (rr && i == 17) asm volatile("v_add_u32 %0, %1, %0" : "+v"(pb) : "s"(step));
          }
        ++k;
        __builtin_amdgcn_sched_barrier(0);
      }
  };
  if (!mid) { wait1(fm); u_fetch(2); u_fetch(1); WAIT_VM(0); }
  WAIT_VM(4);
#pragma unroll
  for (int i = 0; i < 22; ++i) rd1(0, i, a0, b0);
  WAIT_LGKM0();
  auto channel = [&](auto j_c, auto r0_c, auto r1_c) {
    constexpr int j = decltype(j_c)::value, r0 = decltype(r0_c)::value, r1 = decltype(r1_c)::value;
    __builtin_amdgcn_sched_barrier(0);
    if (r0 == 0) {
#pragma unroll
      for (int s = 2; s <= 7; ++s) acc[j][s - 2] = s < 6 ? __builtin_amdgcn_mfma_f32_16x16x32_bf16(ad[j], b0[s], (f32x4){0.f, 0.f, 0.f, 0.f}, 0, 0, 0) : (f32x4){0.f, 0.f, 0.f, 0.f};
    }
#pragma unroll 1
    for (int r = r0; r < r1; r += 2) {
      if (j == 0 && r == 2) {
        asm volatile("s_waitcnt vmcnt(0)\n\ts_barrier" ::: "memory");
        if (tid < 8) __hip_atomic_fetch_add((gu32*)(ws + OFF_FXP + (tid >> 2) * 16384 + (tid & 3) * 1024 + (g >> 4) * 128), 1u, __ATOMIC_RELAXED, __HIP_MEMORY_SCOPE_AGENT);
      }
      if (T0 && j == 0 && r == T0R) touches(T0);
      round(acc[j], a0, b0, a1, b1, 1, r < 6 ? 128 : j ? 0 : CV_CH - 384);
      round(acc[j], a1, b1, a0, b0, 0, 0);
    }
    WAIT_LGKM0();
    __builtin_amdgcn_sched_barrier(0);
  };
  typedef std::integral_constant<int, 0> I0;
  typedef std::integral_constant<int, 1> I1;
  channel(I0{}, I0{}, std::integral_constant<int, 8>{});
  channel(I1{}, I0{}, std::integral_constant<int, LATEQ - 2>{});
  const unsigned fa = OFF_FY + (32 + lane) * 128, fb = lane < 32 ? OFF_FY + (96 + lane) * 128 : OFF_FKV + ((48 + (lane & 15)) * 4 + cg) * 128;
  asm volatile("global_load_dword %0, %1, %2 sc1" : "=v"(f0) : "v"(fa), "s"(ws) : "memory");
  asm volatile("global_load_dword %0, %1, %2 sc1" : "=v"(f1) : "v"(fb), "s"(ws) : "memory");
  channel(I1{}, std::integral_constant<int, LATEQ - 2>{}, std::integral_constant<int, LATEQ>{});
  asm volatile("s_waitcnt vmcnt(0)" : "+v"(f0), "+v"(f1) :: "memory");
  if (!__all(f0 == ep && f1 == ep)) { wait1(fa); wait1(fb); }
  u_fetch(0);
  q_fetch(2, 6);
  touches(T1);
  channel(I1{}, std::integral_constant<int, LATEQ>{}, std::integral_constant<int, 8>{});
  WAIT_VM(T1);
  finish(std::integral_constant<int, 2>{}, std::integral_constant<int, 4>{});
  pair(own + bo);
  count(2, 4, I0{});
  finish(std::integral_constant<int, 6>{}, std::integral_constant<int, 2>{});
  touches(3 - T0 - T1);
  count(6, 2, std::integral_constant<int, 3 - T0 - T1>{});
}
__device__ __forceinline__ float sum2_seg(float s, float q) {
  s += __builtin_bit_cast(float, __builtin_amdgcn_update_dpp(0, __builtin_bit_cast(int, s), 0x128, 0xf, 0xf, true));
  q += __builtin_bit_cast(float, __builtin_amdgcn_update_dpp(0, __builtin_bit_cast(int, q), 0x128, 0xf, 0xf, true));
  asm volatile("s_nop 1\n\tv_permlane16_swap_b32 %0, %1" : "+v"(s), "+v"(q));
  s += q;
  q = s;
  asm volatile("s_nop 1\n\tv_permlane32_swap_b32 %0, %1" : "+v"(s), "+v"(q));
  return s + q;
}
#define LASTSLEEP 8
__device__ __forceinline__ void wait_flags(const char* p, int lane, int n, unsigned ep) {
  gu32* f = (gu32*)(p + (lane < n ? lane : 0) * 128);
#pragma unroll 1
  for (int it = 0; it < 10000; ++it) {
    if (__all(__hip_atomic_load(f, __ATOMIC_RELAXED, __HIP_MEMORY_SCOPE_AGENT) == ep)) break;
    __builtin_amdgcn_s_sleep(LASTSLEEP);
  }
  asm volatile("" ::: "memory");
}
struct QRegs { u32x4v a[4], b[4]; };
__device__ __forceinline__ void q_load(QRegs& R, const u16* __restrict__ q, int f, int lane, int wave) {
  const unsigned o = (f * 16 + (lane & 7)) * 2048 + wave * 512 + (lane >> 3) * 16, o2 = o + 16384;
  LDS16(R.a[0], o, q, 0, "nt"); LDS16(R.b[0], o2, q, 0, "nt"); LDS16(R.a[1], o, q, 128, "nt"); LDS16(R.b[1], o2, q, 128, "nt");
  LDS16(R.a[2], o, q, 256, "nt"); LDS16(R.b[2], o2, q, 256, "nt"); LDS16(R.a[3], o, q, 384, "nt"); LDS16(R.b[3], o2, q, 384, "nt");
}
template <int N>
__device__ __forceinline__ void q_wait(QRegs& R, unsigned& e) {
  asm volatile("s_waitcnt vmcnt(%9)" : "+v"(R.a[0]), "+v"(R.a[1]), "+v"(R.a[2]), "+v"(R.a[3]), "+v"(R.b[0]), "+v"(R.b[1]), "+v"(R.b[2]), "+v"(R.b[3]), "+v"(e) : "n"(N) : "memory");
}
__device__ __forceinline__ void row_stats(const u32x4v (&xq)[4], char* lds, int k, int lane, int wave, float& mean, float& var) {
  f32x2* red = (f32x2*)(lds + 16640);
  const int r8 = lane & 7, seg = lane >> 3;
  f32x2 s = {0.f, 0.f}, sq = {0.f, 0.f};
#pragma unroll
  for (int i = 0; i < 16; ++i) { const f32x2 x = up2(xq[i >> 2][i & 3]); s += x; sq = __builtin_elementwise_fma(x, x, sq); }
  const float a = sum2_seg(s[0] + s[1], sq[0] + sq[1]);
  if ((seg & 5) == 0) ((float*)red)[((k * 4 + wave) * 8 + r8) * 2 + (seg >> 1)] = a;
  BARRIER();
  const f32x2 t = (red[(k * 4) * 8 + r8] + red[(k * 4 + 1) * 8 + r8]) + (red[(k * 4 + 2) * 8 + r8] + red[(k * 4 + 3) * 8 + r8]);
  mean = t[0] * (1.0f / 1024.0f);
  var = fmaxf(t[1] * (1.0f / 1024.0f) - mean * mean, 0.f);
}
__device__ __forceinline__ void q_store(char* __restrict__ ws, int t, int lane, int wave, const u32x4v (&xq)[4], float mean, float var) {
  const __amdgpu_buffer_rsrc_t rs = __builtin_amdgcn_make_buffer_rsrc(ws, 0, WS_USED, 0x00020000);
  const int r8 = lane & 7, seg = lane >> 3;
  const unsigned fo = (t >> 1) * 32768 + (wave * 8 + (seg >> 2)) * 1024 + ((seg & 3) * 16 + (t & 1) * 8 + r8) * 16;
#pragma unroll
  for (int i = 0; i < 4; ++i) bst16(xq[i], rs, fo + i * 2048, OFF_XP);
  const float rd = __builtin_amdgcn_rsqf(var + 1e-5f);
  if (wave == 0 && seg == 0) __builtin_amdgcn_raw_buffer_store_b64((u32x2v){__float_as_uint(rd), __float_as_uint(-mean * rd)}, rs, t * 64 + r8 * 8, OFF_QS, 17);
}
__device__ __forceinline__ void row_last(char* __restrict__ ws, u16* __restrict__ out, int t, int lane, int wave, unsigned ep) {
  const __amdgpu_buffer_rsrc_t rs = __builtin_amdgcn_make_buffer_rsrc(ws, 0, WS_USED, 0x00020000);
  const int r8 = lane & 7, seg = lane >> 3;
  const unsigned fo = (PL1 + t) * 16384 + wave * 4096 + lane * 16;
  wait_flags(ws + OFF_FP + (2 * LB + (t >> 1)) * 2048, lane, 16, ep);
  u32x4v p0[4], p1[4], p2[4], p3[4];
#pragma unroll
  for (int i = 0; i < 4; ++i) {
    p0[i] = __builtin_bit_cast(u32x4v, __builtin_amdgcn_raw_buffer_load_b128(rs, fo + i * 1024, OFF_P0, 16));
    p1[i] = __builtin_bit_cast(u32x4v, __builtin_amdgcn_raw_buffer_load_b128(rs, fo + i * 1024, OFF_P1, 16));
    p2[i] = __builtin_bit_cast(u32x4v, __builtin_amdgcn_raw_buffer_load_b128(rs, fo + i * 1024, OFF_P2, 16));
    p3[i] = __builtin_bit_cast(u32x4v, __builtin_amdgcn_raw_buffer_load_b128(rs, fo + i * 1024, OFF_P3, 16));
  }
#pragma unroll
  for (int i = 0; i < 4; ++i) {
    u32x4v o;
#pragma unroll
    for (int e = 0; e < 4; ++e) o[e] = pk2((up2(p0[i][e]) + up2(p1[i][e])) + (up2(p2[i][e]) + up2(p3[i][e])));
    st16(out + (size_t)(t * 8 + r8) * D_ + wave * 256 + i * 64 + seg * 8, __builtin_bit_cast(u16x8, o));
  }
}

#define FINE (N1 ? 16 : 20)
__device__ __forceinline__ void chunk_last(char* __restrict__ ws, u16* __restrict__ out, int c, int lane, unsigned ep) {
  const __amdgpu_buffer_rsrc_t rs = __builtin_amdgcn_make_buffer_rsrc(ws, 0, WS_USED, 0x00020000);
  const unsigned fo = PL1 * 16384 + c * 1024 + lane * 16;
  gu32* f = (gu32*)(ws + OFF_FP + (2 * LB + (c >> 5)) * 2048 + ((lane & 3) * 4 + ((c >> 2) & 3)) * 128);
#pragma unroll 1
  for (int it = 0; it < 20000; ++it) {
    if (__all(__hip_atomic_load(f, __ATOMIC_RELAXED, __HIP_MEMORY_SCOPE_AGENT) == ep)) break;
    __builtin_amdgcn_s_sleep(4);
  }
  asm volatile("" ::: "memory");
  const u32x4v p0 = __builtin_bit_cast(u32x4v, __builtin_amdgcn_raw_buffer_load_b128(rs, fo, OFF_P0, 16)), p1 = __builtin_bit_cast(u32x4v, __builtin_amdgcn_raw_buffer_load_b128(rs, fo, OFF_P1, 16));
  const u32x4v p2 = __builtin_bit_cast(u32x4v, __builtin_amdgcn_raw_buffer_load_b128(rs, fo, OFF_P2, 16)), p3 = __builtin_bit_cast(u32x4v, __builtin_amdgcn_raw_buffer_load_b128(rs, fo, OFF_P3, 16));
  u32x4v o;
#pragma unroll
  for (int e = 0; e < 4; ++e) o[e] = pk2((up2(p0[e]) + up2(p1[e])) + (up2(p2[e]) + up2(p3[e])));
  st16(out + (size_t)((c >> 4) * 8 + (lane & 7)) * D_ + (c & 15) * 64 + (lane >> 3) * 8, __builtin_bit_cast(u16x8, o));
}

__device__ __forceinline__ int swzB(int r) { return ((((r >> 1) & 1) | (((r >> 3) & 1) << 1)) << 1); }
#define FLW 1
#define MAP3 1
#define MAP12 1
#define FLIP 0
#define KVL 0
#define RE 0
#define NRW 18
#define PFROWS 110
__global__ __launch_bounds__(256, 1) void flow_kernel(const u16* __restrict__ q, const u16* __restrict__ kin, const u16* __restrict__ vin,
                                                      const u16* __restrict__ shift_k, const u16* __restrict__ ssm_k, const u16* __restrict__ ssm_D,
                                                      const u16* __restrict__ w1, const u16* __restrict__ w2,
                                                      const u16* __restrict__ w3, const u16* __restrict__ w4, char* __restrict__ ws,
                                                      u16* __restrict__ out) {
  __shared__ __attribute__((aligned(16))) char lds[163840];
  const int tid = threadIdx.x, lane = tid & 63, wave = __builtin_amdgcn_readfirstlane(tid >> 6);
  const int b = blockIdx.x, xcd = b & 7, idx = b >> 3;
  const unsigned sLds = (unsigned)(size_t)lds, sScr = sLds + 163584;
  asm volatile("s_getpc_b64 s[98:99]\n\ts_mov_b32 m0, %1\n\ts_nop 0\n\tglobal_load_lds_dword %0, s[98:99]" :: "v"(tid * 128), "s"(sScr) : "s98", "s99", "memory");
  asm volatile("" :: "s"(q), "s"(kin), "s"(vin), "s"(shift_k), "s"(ssm_k), "s"(ssm_D), "s"(w1), "s"(w2), "s"(w3), "s"(w4), "s"(ws));
  int kind, rep, slice, kh = 0;
  const int P = xcd >> 1, par = (xcd & 1) ^ FLIP, li = idx - (par ? 20 + RE : 12 - RE);
  const int qq = (par ? 0 : 12 + RE) + idx - 8;
  auto code_touch = [&]() {
    unsigned long long cp;
    unsigned n;
    asm volatile("s_getpc_b64 %0\nLpd%=:\n"
                 "s_mov_b32 s98, Lcode_r_b-Lpd%=\ns_mov_b32 %1, Lcode_r_e-Lcode_r_b\n"
                 "s_cmp_eq_u32 %2, 1\ns_cselect_b32 s98, Lcode_g12_b-Lpd%=, s98\ns_cselect_b32 %1, Lcode_g2_b-Lcode_g12_b, %1\n"
                 "s_cmp_eq_u32 %2, 2\ns_cselect_b32 s98, Lcode_g2_b-Lpd%=, s98\ns_cselect_b32 %1, Lcode_g12_e-Lcode_g2_b, %1\n"
                 "s_cmp_eq_u32 %2, 3\ns_cselect_b32 s98, Lcode_g3_b-Lpd%=, s98\ns_cselect_b32 %1, Lcode_g3_e-Lcode_g3_b, %1\n"
                 "s_cmp_eq_u32 %2, 4\ns_cselect_b32 s98, Lcode_g4_b-Lpd%=, s98\ns_cselect_b32 %1, Lcode_g4_e-Lcode_g4_b, %1\n"
                 "s_cmp_eq_u32 %2, 5\ns_cselect_b32 s98, Lcode_g3o_b-Lpd%=, s98\ns_cselect_b32 %1, Lcode_g3o_e-Lcode_g3o_b, %1\n"
                 "s_ashr_i32 s99, s98, 31\ns_mov_b64 vcc, %0\ns_add_u32 s98, s98, vcc_lo\ns_addc_u32 s99, s99, vcc_hi\ns_mov_b64 %0, s[98:99]"
                 : "=&s"(cp), "=&s"(n) : "s"(kind) : "s98", "s99", "scc", "vcc");
    asm volatile("s_mov_b32 m0, %3\n\ts_nop 0\n\tglobal_load_lds_dword %0, %2\n\tglobal_load_lds_dword %1, %2"
                 :: "v"(min(tid * 128u, n - 4)), "v"(min(32768 + tid * 128u, n - 4)), "s"(cp), "s"(sScr) : "memory");
  };
  unsigned ep = 0;
#define EP_LOAD() asm volatile("global_load_dword %0, %1, %2 sc1" : "=v"(ep) : "v"(OFF_EP), "s"(ws) : "memory")
  auto epoch = [&]() { const unsigned e = __builtin_amdgcn_readfirstlane(ep) + 1u; return e ? e : 1u; };
  auto q_unit = [&](QRegs& QR, int f) {
    float ma, va, mb, vb_;
    row_stats(QR.a, lds + CV_STG, 0, lane, wave, ma, va);
    row_stats(QR.b, lds + CV_STG, 1, lane, wave, mb, vb_);
    q_store(ws, 2 * f, lane, wave, QR.a, ma, va);
    q_store(ws, 2 * f + 1, lane, wave, QR.b, mb, vb_);
  };
  auto publish = [&](unsigned off) {
    BARRIER();
    if (tid == 0) __hip_atomic_store((gu32*)(ws + off), ep, __ATOMIC_RELAXED, __HIP_MEMORY_SCOPE_AGENT);
  };
  if (idx < 8 && par == MAP12) {
    kind = 1; rep = P; slice = idx;
    code_touch();
    prewrite(ws, OFF_FH + slice * 128, 1024, rep, tid);
  } else {
    const int cv = (li + (par ? 20 + RE : 0)) * 4 + P, n = idx < 8 ? 64 + P * 8 + idx : qq * 4 + P;
    QRegs QR;
    if (idx >= 8) {
      KvRegs KR;
      SkRegs SK;
      if (li >= 0) {
        const int tile = 64 + cv;
        u32x4v hv[8];
        unsigned hx, dD;
        h_load(hv, hx, dD, ssm_k, ssm_D, 8 * cv, tid);
        EP_LOAD();
        sk_load(SK, shift_k, tile, tid);
        kv_load(KR, kin, vin, tile, tid);
        {
          int l2 = li, p2 = par;
          asm volatile("" : "+s"(l2), "+s"(p2));
          const int nl = 8 - (P & 1), l = ((15 * P + 1) >> 1) + l2, tl = (l * 205) >> 10, kl = l - 5 * tl;
          kind = l2 < 20 ? 4 : 6; rep = l2 < 20 ? l2 >> 2 : 0; slice = l2 < 20 ? l2 & 3 : ((9 * P) >> 1) + l2 - 20; kh = P;
          if (p2 == 1) {
            kind = l2 < nl ? 3 : 6; rep = l2 < nl ? 3 + ((kl & 1) | (kl >> 2)) : 0;
            slice = l2 < nl ? 3 * tl + (kl >> 1) : ((9 * P) >> 1) + RE + l2 - nl;
          }
        }
        touch_lds((const char*)((par ^ MAP12) ? w2 : w1) + (size_t)(par ? (li < 8 ? li * 256 + tid : 0) : (li < 16 ? li * 128 + (tid & 127) : 0)) * 1024 + 384, sScr);
        code_touch();
        h_wait<18>(hv, hx, dD);
        asm volatile("" : "+v"(ep));
        ep = epoch();
        h_spread(hv, hx, lds, tid);
        kv_wait<2>(KR, SK);
        kv_store(KR, SK, ws, lds + CV_STG, tile, tid);
        WAIT_VM(0);
        publish(OFF_FKV + tile * 128);
        const char* wp = (const char*)w4 + (kind != 4 ? 0 : (size_t)(kh * 896 + li * 15) * 2048 + min(tid, 239) * 128);
        conv_part(ws, dD, lds, cv, tid, lane, wave, ep, wp, kind != 4 ? 0 : 298 * 2048);
      } else {
        kv_load(KR, kin, vin, n, tid);
        sk_load(SK, shift_k, n, tid);
        EP_LOAD();
        {
          int q2 = qq;
          asm volatile("" : "+s"(q2));
          const int e = 15 * P + q2, t = (e * 205) >> 11, k = e - 10 * t, k3 = min((k * 11) >> 5, 2);
          kind = q2 < 15 ? 3 : 5; rep = q2 < 15 ? k - 3 * k3 : P; slice = q2 < 15 ? 3 * t + k3 : 18;
        }
        kv_wait<0>(KR, SK);
        asm volatile("" : "+v"(ep));
        ep = epoch();
        kv_store(KR, SK, ws, lds + CV_STG, n, tid);
        pads(ws, n * 4 + wave, lane, ep);
        if (n < 8)
          st16(ws + OFF_FXP + (lane >> 3) * 16384 + wave * 1024 + (2 * (n & 3) + (n >> 2)) * 128 + (lane & 7) * 16, __builtin_bit_cast(u16x8, (u32x4v){ep - 16u, 0u, 0u, 0u}));
        kv_load(KR, kin, vin, 192 + n, tid);
        WAIT_VM(11);
        publish(OFF_FKV + n * 128);
        q_load(QR, q, 32 + n, lane, wave);
        kv_wait<8>(KR, SK);
        kv_store(KR, SK, ws, lds, 192 + n, tid);
        q_wait<0>(QR, ep);
        publish(OFF_FKV + (192 + n) * 128);
      }
    } else {
      QRegs Q0;
      q_load(Q0, q, n - 64, lane, wave);
      EP_LOAD();
      kind = 2; rep = P; slice = idx;
      q_wait<0>(Q0, ep);
      ep = epoch();
      q_load(QR, q, 32 + n, lane, wave);
      q_unit(Q0, n - 64);
      q_wait<0>(QR, ep);
      publish(OFF_FY + (n - 64) * 128);
    }
    if (li < 0) {
      q_unit(QR, 32 + n);
      {
        const char* wp = (const char*)w2 + (size_t)(n < 64 ? 0 : (n - 64) * 512 + tid) * 128;
        touch_lds(wp, sScr);
        touch_lds(wp + (n < 64 ? 0 : 32768), sScr);
      }
      WAIT_VM(2);
      publish(OFF_FY + (32 + n) * 128);
      if (n >= 64) {
        prewrite(ws, OFF_FX1 + slice * 128, 1024, rep, tid);
        prewrite(ws, OFF_FP + slice * 128, 2048, rep, tid);
        prewrite(ws, OFF_FP + (8 + slice) * 128, 2048, rep, tid);
        prewrite(ws, OFF_FG + slice * 128, 2560, rep, tid);
        prewrite(ws, OFF_FG + (8 + slice) * 128, 2560, rep, tid);
        if (slice < 3) prewrite(ws, OFF_FG + (16 + slice) * 128, 2560, rep, tid);
        st16(ws + OFF_FXP + slice * 128 + (132 + rep + 4 * (tid >> 3)) * 1024 + (tid & 7) * 16, __builtin_bit_cast(u16x8, (u32x4v){0u, 0u, 0u, 0u}));
      }
      code_touch();
      if (n < 64) {
#pragma unroll
        for (int i = 0; i < 4; ++i) {
          const unsigned x = rep * 1024 + i * 256 + tid, row = kind == 3 ? (x * 21846) >> 16 : x >> 1;
          if (rep < (kind == 3 ? 3 : 2)) touch_lds((const char*)w3 + (size_t)row * 7168 + slice * 384 + (x - row * (kind == 3 ? 3 : 2)) * 128, sScr);
        }
      }
    }
    BARRIER();
  }
  typedef char* const __attribute__((address_space(4)))* KArgs;
#define KARG(x, i) char* x; { KArgs ka_ = (KArgs)__builtin_amdgcn_kernarg_segment_ptr(); asm volatile("" : "+s"(ka_)); x = ka_[i]; }
  const unsigned sDbg0 = OFF_DBG + (b * 4 + wave) * 8192;
#define LANE_ID(x) int x; asm volatile("v_mbcnt_lo_u32_b32 %0, -1, 0\n\tv_mbcnt_hi_u32_b32 %0, -1, %0" : "=v"(x))
#define OOB 0x80000000u
#define COMMON(NF, FIN, FOUT, SN0, WS, LDW, NREP)                                                                                    \
  LANE_ID(ln);                                                                                                                       \
  const int l15 = ln & 15, lg = ln >> 4, lq = l15 >> 2, lp = ln & 3, rr = 8 * lg + lq;                                               \
  const unsigned vA = ln * 16, vFlag = (ln < FLW && wave == 3) ? (FOUT) + ln * 4 : OOB;                                                        \
  const unsigned vPollOff = wave < 2 && ln < (NF) ? (FIN) + ((NF) == 16 && ln >= 8 ? (ln & 2 ? ln : ln - 16) : ln) * 128 : OOB, vSnapOff = wave == 0 ? vPollOff : OOB;                   \
  const unsigned vSnapRd = (SN0) + (ln >> 5) * (WS) + ((ln & 31) < (NF) ? (ln & 31) : 0) * 4;                                        \
  const unsigned vWd0 = (ln >> 3) * (LDW) + ((ln & 7) ^ swzB(ln >> 3)) * 16, vWd1 = ((ln >> 3) + 8) * (LDW) + ((ln & 7) ^ swzB(8 + (ln >> 3))) * 16; \
  const unsigned vWr0 = (kind == 4 ? wave * 4096 : (wave >> 1) * 8192) + rr * 128 + (((kind == 4 ? kh >> 1 : wave & 1) * 4 + lp) ^ swzB(rr)) * 16, vWr1 = vWr0 + 8;                  \
  const unsigned vWr2 = kind == 4 ? vWr0 ^ 64 : 16384 + rr * 128 + ((2 * wave + (lp >> 1)) ^ swzB(rr)) * 16 + (lp & 1) * 8;                                  \
                                            \
  const int res = kind == 3 ? rep + 3 * KE : rep;                     \
  const unsigned sStep = (rep == 5 ? 6 : (NREP)) << 15, sU0 = (rep < 3 ? rep : rep == 5 ? N1 + 9 : res) << 15, sX1 = ((NREP) == 4 ? 128 + rep : 128 + (res + 2) % 5) << 15; \
  const unsigned sX2 = ((NREP) == 4 ? 260 + rep : N1 && kind == 4 ? 260 + (N1 + rep + 2) % 6 : 260 + (res + 4) % 5) << 15, sDbg = sDbg0 + (rep == 5 ? 1024 : 0); \
  const unsigned sBuf = kind == 4 ? __builtin_amdgcn_readfirstlane(ep) : kind != 3 ? kind : (rep < 3 ? 3 : 5) << 15
  constexpr unsigned AUX = 131072;
    if (kind == 6)
    {
      KARG(ws, 10)
      LANE_ID(lr);
      wait_flags(ws + OFF_FXP + 7 * 16384 + wave * 1024, lr, 8, ep);
      const int r8 = lr & 7, seg = lr >> 3;
      const unsigned sEpR = __builtin_amdgcn_readfirstlane(ep), sK = slice, sStep = NRW, sK1 = RT1 + slice, sRq = OFF_FXP + LB * 2048 + wave * 128, sDbg = sDbg0 + (kind == 6 ? 0 : 1024);
      const unsigned vRF = (wave * 8 + (seg >> 2)) * 1024 + ((seg & 3) * 16 + r8) * 16;
      const unsigned vRwr = sLds + (seg == 0 ? r8 * 64 + wave * 4 : 512 + lr * 4), vRrd = sLds + r8 * 64;
      const unsigned vRP = sLds + ((lr >> 4) == ((lr & 15) >> 2) ? r8 * 64 + 16 + wave * 8 + (seg & 1) * 4 : 768 + lr * 4);
      const unsigned vRS = (wave == 0 && seg == 0) ? r8 * 16 : 0x80000000u, vFlag = lr == 0 ? 0u : 0x80000000u, vL4 = (lr & 15) * 128, vRL = wave * 4096 + lr * 16;
      asm volatile(R"(
Lcode_r_b:
s_mov_b64 s[36:37],%0
s_mov_b32 s38,122727552
s_mov_b32 s39,0x00020000
s_mov_b32 s54,%1
s_mov_b32 s50,%2
s_mov_b32 s92,%3
s_mov_b32 s48,%4
v_mov_b32 v0,%5
v_mov_b32 v1,%6
v_mov_b32 v2,%7
v_mov_b32 v3,%8
v_mov_b32 v4,%9
v_mov_b32 v5,%10
v_mov_b32 v6,%11
v_mov_b32 v11,%12
v_lshrrev_b32 v10,1,v4
v_mov_b32 v7,s48
v_mbcnt_lo_u32_b32 v9,-1,0
v_mbcnt_hi_u32_b32 v9,-1,v9
v_lshlrev_b32 v9,4,v9
s_mov_b32 s74,0
s_mov_b32 s96,0
s_mov_b32 s60,536870912
s_mov_b32 s46,0xaaaaaaaa
s_mov_b32 s47,0xaaaaaaaa
s_mov_b32 s56,0xcccccccc
s_mov_b32 s57,0xcccccccc
s_mov_b32 s76,0x3a800000
s_mov_b32 s77,0x3727c5ac
s_mov_b32 s78,0xc0135761
s_mov_b32 s79,0x3d372713
v_mov_b32 v192,0x3f803f80
v_mov_b32 v193,0x3f803f80
v_mov_b32 v194,0x3f803f80
v_mov_b32 v195,0x3f803f80
s_lshr_b32 s63,s54,1
s_and_b32 s64,s54,1
s_lshl_b32 s64,s64,7
s_lshl_b32 s87,s54,14
s_lshl_b32 s58,s63,15
s_add_u32 s58,s58,s64
s_add_u32 s58,s58,16908288
s_lshl_b32 s59,s63,9
s_add_u32 s59,s59,s64
s_add_u32 s59,s59,118556672
s_lshl_b32 s49,s54,9
s_add_u32 s49,s49,s92
s_lshl_b32 s91,s63,11
s_add_u32 s91,s91,121405440
s_lshl_b32 s69,s50,10
s_add_u32 s69,s69,s91
v_mov_b32 v20,0
v_mov_b32 v21,0
v_mov_b32 v22,0
v_mov_b32 v23,0
v_mov_b32 v24,0
v_mov_b32 v25,0
v_mov_b32 v26,0
v_mov_b32 v27,0
v_mov_b32 v16,0x3f80
v_mov_b32 v17,0x3f800000
v_cndmask_b32 v16,v16,v17,s[46:47]
s_mov_b32 s98,0x00300003
s_mov_b32 s99,0x30000300
v_cndmask_b32 v20,v20,v16,s[98:99]
s_lshl_b64 s[98:99],s[98:99],2
v_cndmask_b32 v21,v21,v16,s[98:99]
v_mov_b32 v26,v20
v_mov_b32 v27,v21
v_readfirstlane_b32 s52,v11
s_add_u32 s51,s87,8388608
s_mov_b32 s66,4096
s_nop 3
Lr_yp_%=:
s_add_u32 m0,s66,s52
s_cmp_ge_u32 s51,12582912
s_cbranch_scc1 Lr_ys1_%=
buffer_load_dwordx4 v11,s[36:39],s51 offen offset:0 sc1 lds
buffer_load_dwordx4 v11,s[36:39],s51 offen offset:1024 sc1 lds
buffer_load_dwordx4 v11,s[36:39],s51 offen offset:2048 sc1 lds
buffer_load_dwordx4 v11,s[36:39],s51 offen offset:3072 sc1 lds
s_lshl_b32 s63,s50,14
s_add_u32 s51,s51,s63
Lr_ys1_%=:
s_add_u32 s66,s66,16384
s_cmp_lt_u32 s66,151552
s_cbranch_scc1 Lr_yp_%=
s_mov_b32 s66,4096
v_add_u32 v18,4096,v11
Lr_bp9_%=:
buffer_load_dword v8,v6,s[36:39],s91 offen offset:0 sc1
s_waitcnt vmcnt(0)
v_cmp_eq_u32 vcc,s48,v8
s_nop 0
s_cmp_eq_u64 vcc,exec
s_cselect_b32 s63,1,s96
s_cmp_lg_u32 s63,0
s_cbranch_scc1 Lr_bq9_%=
s_add_u32 s74,s74,1
s_cmp_ge_u32 s74,20000
s_cselect_b32 s96,1,0
s_sleep 4
s_branch Lr_bp9_%=
Lr_bq9_%=:
s_add_u32 s63,s87,71303168
buffer_load_dwordx4 a[0:3],v11,s[36:39],s63 offen offset:0 sc1
buffer_load_dwordx4 a[4:7],v11,s[36:39],s63 offen offset:1024 sc1
buffer_load_dwordx4 a[8:11],v11,s[36:39],s63 offen offset:2048 sc1
buffer_load_dwordx4 a[12:15],v11,s[36:39],s63 offen offset:3072 sc1
s_add_u32 s63,s87,89128960
buffer_load_dwordx4 a[16:19],v11,s[36:39],s63 offen offset:0 sc1
buffer_load_dwordx4 a[20:23],v11,s[36:39],s63 offen offset:1024 sc1
buffer_load_dwordx4 a[24:27],v11,s[36:39],s63 offen offset:2048 sc1
buffer_load_dwordx4 a[28:31],v11,s[36:39],s63 offen offset:3072 sc1
s_add_u32 s63,s87,99614720
buffer_load_dwordx4 a[32:35],v11,s[36:39],s63 offen offset:0 sc1
buffer_load_dwordx4 a[36:39],v11,s[36:39],s63 offen offset:1024 sc1
buffer_load_dwordx4 a[40:43],v11,s[36:39],s63 offen offset:2048 sc1
buffer_load_dwordx4 a[44:47],v11,s[36:39],s63 offen offset:3072 sc1
s_add_u32 s63,s87,109051904
buffer_load_dwordx4 a[48:51],v11,s[36:39],s63 offen offset:0 sc1
buffer_load_dwordx4 a[52:55],v11,s[36:39],s63 offen offset:1024 sc1
buffer_load_dwordx4 a[56:59],v11,s[36:39],s63 offen offset:2048 sc1
buffer_load_dwordx4 a[60:63],v11,s[36:39],s63 offen offset:3072 sc1
buffer_load_dword v8,v6,s[36:39],s69 offen offset:0 sc1
s_waitcnt vmcnt(0)
Lr_it0_%=:
s_mov_b32 s88,s58
s_mov_b32 s89,s59
s_mov_b32 s90,s49
s_add_u32 s54,s54,s50
s_lshr_b32 s63,s54,1
s_and_b32 s64,s54,1
s_lshl_b32 s64,s64,7
s_lshl_b32 s87,s54,14
s_lshl_b32 s58,s63,15
s_add_u32 s58,s58,s64
s_add_u32 s58,s58,16908288
s_lshl_b32 s59,s63,9
s_add_u32 s59,s59,s64
s_add_u32 s59,s59,118556672
s_lshl_b32 s49,s54,9
s_add_u32 s49,s49,s92
s_lshl_b32 s91,s63,11
s_add_u32 s91,s91,121405440
s_lshl_b32 s69,s50,10
s_add_u32 s69,s69,s91
v_cmp_eq_u32 vcc,s48,v8
s_nop 0
s_cmp_eq_u64 vcc,exec
s_cselect_b32 s63,s54,256
s_cmp_lt_u32 s63,256
s_cbranch_scc1 Lr_ta0_%=
ds_read_b128 v[32:35],v18 offset:0
ds_read_b128 v[36:39],v18 offset:1024
ds_read_b128 v[40:43],v18 offset:2048
ds_read_b128 v[44:47],v18 offset:3072
v_mfma_f32_16x16x32_bf16 v[128:131],v[20:23],a[0:3],0
v_mfma_f32_16x16x32_bf16 v[132:135],v[24:27],a[0:3],0
v_mfma_f32_16x16x32_bf16 v[128:131],v[20:23],a[16:19],v[128:131]
v_mfma_f32_16x16x32_bf16 v[132:135],v[24:27],a[16:19],v[132:135]
v_mfma_f32_16x16x32_bf16 v[128:131],v[20:23],a[32:35],v[128:131]
v_mfma_f32_16x16x32_bf16 v[132:135],v[24:27],a[32:35],v[132:135]
v_mfma_f32_16x16x32_bf16 v[128:131],v[20:23],a[48:51],v[128:131]
v_mfma_f32_16x16x32_bf16 v[132:135],v[24:27],a[48:51],v[132:135]
v_mfma_f32_16x16x32_bf16 v[136:139],v[20:23],a[4:7],0
v_mfma_f32_16x16x32_bf16 v[140:143],v[24:27],a[4:7],0
v_mfma_f32_16x16x32_bf16 v[136:139],v[20:23],a[20:23],v[136:139]
v_mfma_f32_16x16x32_bf16 v[140:143],v[24:27],a[20:23],v[140:143]
v_mfma_f32_16x16x32_bf16 v[136:139],v[20:23],a[36:39],v[136:139]
s_nop 1
v_cvt_pk_bf16_f32 v208,v128,v129
v_cvt_pk_bf16_f32 v209,v130,v131
v_mfma_f32_16x16x32_bf16 v[140:143],v[24:27],a[36:39],v[140:143]
v_cvt_pk_bf16_f32 v210,v132,v133
v_cvt_pk_bf16_f32 v211,v134,v135
v_mfma_f32_16x16x32_bf16 v[136:139],v[20:23],a[52:55],v[136:139]
v_mfma_f32_16x16x32_bf16 v[140:143],v[24:27],a[52:55],v[140:143]
v_mfma_f32_16x16x32_bf16 v[144:147],v[20:23],a[8:11],0
v_mfma_f32_16x16x32_bf16 v[148:151],v[24:27],a[8:11],0
v_mfma_f32_16x16x32_bf16 v[144:147],v[20:23],a[24:27],v[144:147]
v_mfma_f32_16x16x32_bf16 v[148:151],v[24:27],a[24:27],v[148:151]
v_mfma_f32_16x16x32_bf16 v[144:147],v[20:23],a[40:43],v[144:147]
s_nop 1
v_cvt_pk_bf16_f32 v212,v136,v137
s_waitcnt vmcnt(0)
buffer_store_dword v7,v5,s[36:39],s60 offen offset:0 sc0 sc1
s_mov_b32 s60,536870912
v_cvt_pk_bf16_f32 v213,v138,v139
v_mfma_f32_16x16x32_bf16 v[148:151],v[24:27],a[40:43],v[148:151]
v_cvt_pk_bf16_f32 v214,v140,v141
v_cvt_pk_bf16_f32 v215,v142,v143
v_mfma_f32_16x16x32_bf16 v[144:147],v[20:23],a[56:59],v[144:147]
v_mfma_f32_16x16x32_bf16 v[148:151],v[24:27],a[56:59],v[148:151]
v_mfma_f32_16x16x32_bf16 v[28:31],v[192:195],v[208:211],0
v_mfma_f32_16x16x32_bf16 v[204:207],v[208:211],v[208:211],0
v_mfma_f32_16x16x32_bf16 v[152:155],v[20:23],a[12:15],0
v_mfma_f32_16x16x32_bf16 v[156:159],v[24:27],a[12:15],0
v_mfma_f32_16x16x32_bf16 v[152:155],v[20:23],a[28:31],v[152:155])"
R"(
v_mfma_f32_16x16x32_bf16 v[156:159],v[24:27],a[28:31],v[156:159]
v_mfma_f32_16x16x32_bf16 v[152:155],v[20:23],a[44:47],v[152:155]
v_cvt_pk_bf16_f32 v216,v144,v145
v_cvt_pk_bf16_f32 v217,v146,v147
v_mfma_f32_16x16x32_bf16 v[156:159],v[24:27],a[44:47],v[156:159]
v_cvt_pk_bf16_f32 v218,v148,v149
v_cvt_pk_bf16_f32 v219,v150,v151
v_mfma_f32_16x16x32_bf16 v[152:155],v[20:23],a[60:63],v[152:155]
v_mfma_f32_16x16x32_bf16 v[156:159],v[24:27],a[60:63],v[156:159]
v_mfma_f32_16x16x32_bf16 v[28:31],v[192:195],v[212:215],v[28:31]
v_mfma_f32_16x16x32_bf16 v[204:207],v[212:215],v[212:215],v[204:207]
v_mfma_f32_16x16x32_bf16 v[28:31],v[192:195],v[216:219],v[28:31]
v_mfma_f32_16x16x32_bf16 v[204:207],v[216:219],v[216:219],v[204:207]
s_waitcnt lgkmcnt(0)
v_mfma_f32_16x16x32_bf16 v[160:163],v[20:23],v[32:35],0
s_nop 0
v_cvt_pk_bf16_f32 v220,v152,v153
v_cvt_pk_bf16_f32 v221,v154,v155
v_mfma_f32_16x16x32_bf16 v[164:167],v[24:27],v[32:35],0
v_cvt_pk_bf16_f32 v222,v156,v157
v_cvt_pk_bf16_f32 v223,v158,v159
v_mfma_f32_16x16x32_bf16 v[168:171],v[20:23],v[36:39],0
s_nop 0
v_mfma_f32_16x16x32_bf16 v[28:31],v[192:195],v[220:223],v[28:31]
v_mfma_f32_16x16x32_bf16 v[204:207],v[220:223],v[220:223],v[204:207]
v_mfma_f32_16x16x32_bf16 v[172:175],v[24:27],v[36:39],0
v_mfma_f32_16x16x32_bf16 v[176:179],v[20:23],v[40:43],0
v_mfma_f32_16x16x32_bf16 v[180:183],v[24:27],v[40:43],0
s_nop 3
v_add_f32_dpp v224,v28,v28 row_ror:8 row_mask:0xf bank_mask:0xf
v_cndmask_b32 v225,v204,v205,s[46:47]
v_cndmask_b32 v226,v206,v207,s[46:47]
v_cndmask_b32 v225,v225,v226,s[56:57]
v_mfma_f32_16x16x32_bf16 v[184:187],v[20:23],v[44:47],0
ds_write_b32 v2,v224 offset:0
ds_write_b32 v0,v225 offset:0
v_mfma_f32_16x16x32_bf16 v[188:191],v[24:27],v[44:47],0
s_waitcnt lgkmcnt(0)
s_barrier
ds_read_b128 v[196:199],v3 offset:0
ds_read_b128 v[200:203],v3 offset:16
ds_read_b128 v[204:207],v3 offset:32
s_waitcnt lgkmcnt(0)
v_add_f32 v196,v196,v197
v_add_f32 v198,v198,v199
v_add_f32 v200,v200,v204
v_add_f32 v201,v201,v205
v_add_f32 v202,v202,v206
v_add_f32 v203,v203,v207
v_add_f32 v196,v196,v198
v_add_f32 v200,v200,v201
v_add_f32 v202,v202,v203
v_mul_f32 v196,s76,v196
v_add_f32 v197,v200,v202
v_mul_f32 v197,s76,v197
v_fma_f32 v197,-v196,v196,v197
v_max_f32 v197,0,v197
v_add_f32 v197,s77,v197
v_rsq_f32 v228,v197
s_nop 0
v_mul_f32_e64 v230,-v196,v228
v_mov_b32 v229,v228
v_mov_b32 v231,v230
v_pk_fma_f32 v[196:197],v[128:129],v[228:229],v[230:231]
v_pk_fma_f32 v[198:199],v[130:131],v[228:229],v[230:231]
v_pk_fma_f32 v[200:201],v[132:133],v[228:229],v[230:231]
v_pk_fma_f32 v[202:203],v[134:135],v[228:229],v[230:231]
v_pk_fma_f32 v[128:129],v[196:197],v[160:161],v[128:129]
v_pk_fma_f32 v[130:131],v[198:199],v[162:163],v[130:131]
v_pk_fma_f32 v[132:133],v[200:201],v[164:165],v[132:133]
v_pk_fma_f32 v[134:135],v[202:203],v[166:167],v[134:135]
v_pk_fma_f32 v[196:197],v[136:137],v[228:229],v[230:231]
v_pk_fma_f32 v[198:199],v[138:139],v[228:229],v[230:231]
v_pk_fma_f32 v[200:201],v[140:141],v[228:229],v[230:231]
v_pk_fma_f32 v[202:203],v[142:143],v[228:229],v[230:231]
v_pk_fma_f32 v[136:137],v[196:197],v[168:169],v[136:137]
v_pk_fma_f32 v[138:139],v[198:199],v[170:171],v[138:139]
v_pk_fma_f32 v[140:141],v[200:201],v[172:173],v[140:141]
v_pk_fma_f32 v[142:143],v[202:203],v[174:175],v[142:143]
v_pk_fma_f32 v[196:197],v[144:145],v[228:229],v[230:231]
v_pk_fma_f32 v[198:199],v[146:147],v[228:229],v[230:231]
v_pk_fma_f32 v[200:201],v[148:149],v[228:229],v[230:231]
v_pk_fma_f32 v[202:203],v[150:151],v[228:229],v[230:231]
v_pk_fma_f32 v[144:145],v[196:197],v[176:177],v[144:145]
v_pk_fma_f32 v[146:147],v[198:199],v[178:179],v[146:147]
v_pk_fma_f32 v[148:149],v[200:201],v[180:181],v[148:149]
v_pk_fma_f32 v[150:151],v[202:203],v[182:183],v[150:151]
v_pk_fma_f32 v[196:197],v[152:153],v[228:229],v[230:231]
v_pk_fma_f32 v[198:199],v[154:155],v[228:229],v[230:231]
v_pk_fma_f32 v[200:201],v[156:157],v[228:229],v[230:231]
v_pk_fma_f32 v[202:203],v[158:159],v[228:229],v[230:231]
v_pk_fma_f32 v[152:153],v[196:197],v[184:185],v[152:153]
v_pk_fma_f32 v[154:155],v[198:199],v[186:187],v[154:155]
v_pk_fma_f32 v[156:157],v[200:201],v[188:189],v[156:157]
v_pk_fma_f32 v[158:159],v[202:203],v[190:191],v[158:159]
v_cvt_pk_bf16_f32 v208,v128,v129
v_cvt_pk_bf16_f32 v209,v130,v131
v_cvt_pk_bf16_f32 v210,v132,v133
v_cvt_pk_bf16_f32 v211,v134,v135
v_cvt_pk_bf16_f32 v212,v136,v137
v_cvt_pk_bf16_f32 v213,v138,v139
v_cvt_pk_bf16_f32 v214,v140,v141
v_cvt_pk_bf16_f32 v215,v142,v143
v_mfma_f32_16x16x32_bf16 v[28:31],v[192:195],v[208:211],0
v_cvt_pk_bf16_f32 v216,v144,v145
v_cvt_pk_bf16_f32 v217,v146,v147
v_mfma_f32_16x16x32_bf16 v[204:207],v[208:211],v[208:211],0
v_cvt_pk_bf16_f32 v218,v148,v149
v_cvt_pk_bf16_f32 v219,v150,v151
v_mfma_f32_16x16x32_bf16 v[28:31],v[192:195],v[212:215],v[28:31]
v_cvt_pk_bf16_f32 v220,v152,v153
v_cvt_pk_bf16_f32 v221,v154,v155
v_mfma_f32_16x16x32_bf16 v[204:207],v[212:215],v[212:215],v[204:207]
v_cvt_pk_bf16_f32 v222,v156,v157
v_cvt_pk_bf16_f32 v223,v158,v159
v_mfma_f32_16x16x32_bf16 v[28:31],v[192:195],v[216:219],v[28:31]
v_mfma_f32_16x16x32_bf16 v[204:207],v[216:219],v[216:219],v[204:207]
v_mfma_f32_16x16x32_bf16 v[28:31],v[192:195],v[220:223],v[28:31]
v_mfma_f32_16x16x32_bf16 v[204:207],v[220:223],v[220:223],v[204:207]
buffer_load_dword v8,v6,s[36:39],s91 offen offset:0 sc1
buffer_store_dwordx4 v[208:211],v1,s[36:39],s88 offen offset:0 sc0 sc1
buffer_store_dwordx4 v[212:215],v1,s[36:39],s88 offen offset:2048 sc0 sc1
s_add_u32 s88,s88,4096
s_nop 2
v_add_f32_dpp v224,v28,v28 row_ror:8 row_mask:0xf bank_mask:0xf
v_cndmask_b32 v225,v204,v205,s[46:47]
v_cndmask_b32 v226,v206,v207,s[46:47]
v_cndmask_b32 v225,v225,v226,s[56:57]
s_and_b32 s63,s89,128
ds_write_b32 v2,v224 offset:1024
ds_write_b32 v0,v225 offset:1024
buffer_store_dwordx4 v[216:219],v1,s[36:39],s88 offen offset:0 sc0 sc1
buffer_store_dwordx4 v[220:223],v1,s[36:39],s88 offen offset:2048 sc0 sc1
s_waitcnt lgkmcnt(0)
s_barrier
ds_read_b128 v[196:199],v3 offset:1024
ds_read_b128 v[200:203],v3 offset:1040
ds_read_b128 v[204:207],v3 offset:1056
s_waitcnt lgkmcnt(0)
v_add_f32 v196,v196,v197
v_add_f32 v198,v198,v199
v_add_f32 v200,v200,v204
v_add_f32 v201,v201,v205
v_add_f32 v202,v202,v206
v_add_f32 v203,v203,v207
v_add_f32 v196,v196,v198
v_add_f32 v200,v200,v201
v_add_f32 v202,v202,v203
v_mul_f32 v196,s76,v196
v_add_f32 v197,v200,v202
v_mul_f32 v197,s76,v197
v_fma_f32 v197,-v196,v196,v197
v_max_f32 v197,0,v197
v_add_f32 v198,s77,v197
v_fma_f32 v199,s77,v198,v197
v_rsq_f32 v205,v198
v_rsq_f32 v198,v199
v_mov_b32 v200,v196
v_mul_f32_e64 v204,-v196,v205
v_mul_f32 v203,v199,v198
v_mul_f32 v201,s78,v198
v_mul_f32 v199,v198,v198
v_mul_f32 v202,s79,v199
v_mul_f32 v202,v202,v201
s_lshr_b32 s63,s63,1
s_sub_u32 s63,s89,s63
buffer_store_dwordx4 v[200:203],v4,s[36:39],s89 offen offset:0 sc0 sc1
buffer_store_dwordx2 v[204:205],v10,s[36:39],s63 offen offset:256 sc0 sc1
s_add_u32 m0,s66,s52
s_cmp_ge_u32 s51,12582912
s_cbranch_scc1 Lr_ys2_%=
buffer_load_dwordx4 v11,s[36:39],s51 offen offset:0 sc1 lds
buffer_load_dwordx4 v11,s[36:39],s51 offen offset:1024 sc1 lds
buffer_load_dwordx4 v11,s[36:39],s51 offen offset:2048 sc1 lds
buffer_load_dwordx4 v11,s[36:39],s51 offen offset:3072 sc1 lds
s_lshl_b32 s63,s50,14
s_add_u32 s51,s51,s63
Lr_ys2_%=:
s_add_u32 s66,s66,16384
s_cmp_eq_u32 s66,151552
s_cselect_b32 s66,4096,s66
v_add_u32 v18,s66,v11
s_mov_b32 s60,s90
s_waitcnt vmcnt(6)
v_cmp_eq_u32 vcc,s48,v8
s_nop 0
s_cmp_eq_u64 vcc,exec
s_cselect_b32 s63,s54,256
s_cmp_lt_u32 s63,256
s_cbranch_scc1 Lr_tc0_%=
buffer_load_dword v8,v6,s[36:39],s91 offen offset:0 sc1
s_waitcnt vmcnt(0)
buffer_store_dword v7,v5,s[36:39],s60 offen offset:0 sc0 sc1
s_mov_b32 s60,536870912
v_cmp_eq_u32 vcc,s48,v8
s_nop 0
s_cmp_eq_u64 vcc,exec
s_cselect_b32 s63,s54,256
s_cmp_lt_u32 s63,256
s_cbranch_scc1 Lr_bq0_%=)"
R"(
s_cmp_ge_u32 s54,256
s_cbranch_scc1 Lr_done_%=
Lr_bp0_%=:
buffer_load_dword v8,v6,s[36:39],s91 offen offset:0 sc1
s_waitcnt vmcnt(0)
v_cmp_eq_u32 vcc,s48,v8
s_nop 0
s_cmp_eq_u64 vcc,exec
s_cselect_b32 s63,1,s96
s_cmp_lg_u32 s63,0
s_cbranch_scc1 Lr_bq0_%=
s_add_u32 s74,s74,1
s_cmp_ge_u32 s74,20000
s_cselect_b32 s96,1,0
s_sleep 4
s_branch Lr_bp0_%=
Lr_bq0_%=:
s_add_u32 s63,s87,71303168
buffer_load_dwordx4 a[64:67],v11,s[36:39],s63 offen offset:0 sc1
buffer_load_dwordx4 a[68:71],v11,s[36:39],s63 offen offset:1024 sc1
buffer_load_dwordx4 a[72:75],v11,s[36:39],s63 offen offset:2048 sc1
buffer_load_dwordx4 a[76:79],v11,s[36:39],s63 offen offset:3072 sc1
s_add_u32 s63,s87,89128960
buffer_load_dwordx4 a[80:83],v11,s[36:39],s63 offen offset:0 sc1
buffer_load_dwordx4 a[84:87],v11,s[36:39],s63 offen offset:1024 sc1
buffer_load_dwordx4 a[88:91],v11,s[36:39],s63 offen offset:2048 sc1
buffer_load_dwordx4 a[92:95],v11,s[36:39],s63 offen offset:3072 sc1
s_add_u32 s63,s87,99614720
buffer_load_dwordx4 a[96:99],v11,s[36:39],s63 offen offset:0 sc1
buffer_load_dwordx4 a[100:103],v11,s[36:39],s63 offen offset:1024 sc1
buffer_load_dwordx4 a[104:107],v11,s[36:39],s63 offen offset:2048 sc1
buffer_load_dwordx4 a[108:111],v11,s[36:39],s63 offen offset:3072 sc1
s_add_u32 s63,s87,109051904
buffer_load_dwordx4 a[112:115],v11,s[36:39],s63 offen offset:0 sc1
buffer_load_dwordx4 a[116:119],v11,s[36:39],s63 offen offset:1024 sc1
buffer_load_dwordx4 a[120:123],v11,s[36:39],s63 offen offset:2048 sc1
buffer_load_dwordx4 a[124:127],v11,s[36:39],s63 offen offset:3072 sc1
buffer_load_dword v8,v6,s[36:39],s69 offen offset:0 sc1
s_waitcnt vmcnt(0)
s_branch Lr_it1_%=
Lr_ta0_%=:
ds_read_b128 v[32:35],v18 offset:0
ds_read_b128 v[36:39],v18 offset:1024
ds_read_b128 v[40:43],v18 offset:2048
ds_read_b128 v[44:47],v18 offset:3072
v_mfma_f32_16x16x32_bf16 v[128:131],v[20:23],a[0:3],0
s_add_u32 s63,s87,71303168
v_mfma_f32_16x16x32_bf16 v[132:135],v[24:27],a[0:3],0
buffer_load_dwordx4 a[64:67],v11,s[36:39],s63 offen offset:0 sc1
v_mfma_f32_16x16x32_bf16 v[128:131],v[20:23],a[16:19],v[128:131]
v_mfma_f32_16x16x32_bf16 v[132:135],v[24:27],a[16:19],v[132:135]
buffer_load_dwordx4 a[68:71],v11,s[36:39],s63 offen offset:1024 sc1
v_mfma_f32_16x16x32_bf16 v[128:131],v[20:23],a[32:35],v[128:131]
v_mfma_f32_16x16x32_bf16 v[132:135],v[24:27],a[32:35],v[132:135]
v_mfma_f32_16x16x32_bf16 v[128:131],v[20:23],a[48:51],v[128:131]
v_mfma_f32_16x16x32_bf16 v[132:135],v[24:27],a[48:51],v[132:135]
buffer_load_dwordx4 a[72:75],v11,s[36:39],s63 offen offset:2048 sc1
v_mfma_f32_16x16x32_bf16 v[136:139],v[20:23],a[4:7],0
v_mfma_f32_16x16x32_bf16 v[140:143],v[24:27],a[4:7],0
v_mfma_f32_16x16x32_bf16 v[136:139],v[20:23],a[20:23],v[136:139]
v_mfma_f32_16x16x32_bf16 v[140:143],v[24:27],a[20:23],v[140:143]
buffer_load_dwordx4 a[76:79],v11,s[36:39],s63 offen offset:3072 sc1
v_mfma_f32_16x16x32_bf16 v[136:139],v[20:23],a[36:39],v[136:139]
v_cvt_pk_bf16_f32 v208,v128,v129
v_cvt_pk_bf16_f32 v209,v130,v131
v_mfma_f32_16x16x32_bf16 v[140:143],v[24:27],a[36:39],v[140:143]
v_cvt_pk_bf16_f32 v210,v132,v133
v_cvt_pk_bf16_f32 v211,v134,v135
v_mfma_f32_16x16x32_bf16 v[136:139],v[20:23],a[52:55],v[136:139]
s_add_u32 s63,s87,89128960
v_mfma_f32_16x16x32_bf16 v[140:143],v[24:27],a[52:55],v[140:143]
buffer_load_dwordx4 a[80:83],v11,s[36:39],s63 offen offset:0 sc1
v_mfma_f32_16x16x32_bf16 v[144:147],v[20:23],a[8:11],0
v_mfma_f32_16x16x32_bf16 v[148:151],v[24:27],a[8:11],0
v_mfma_f32_16x16x32_bf16 v[144:147],v[20:23],a[24:27],v[144:147]
v_mfma_f32_16x16x32_bf16 v[148:151],v[24:27],a[24:27],v[148:151]
buffer_load_dwordx4 a[84:87],v11,s[36:39],s63 offen offset:1024 sc1
v_mfma_f32_16x16x32_bf16 v[144:147],v[20:23],a[40:43],v[144:147]
v_cvt_pk_bf16_f32 v212,v136,v137
v_cvt_pk_bf16_f32 v213,v138,v139
v_mfma_f32_16x16x32_bf16 v[148:151],v[24:27],a[40:43],v[148:151]
v_cvt_pk_bf16_f32 v214,v140,v141
v_cvt_pk_bf16_f32 v215,v142,v143
v_mfma_f32_16x16x32_bf16 v[144:147],v[20:23],a[56:59],v[144:147]
v_mfma_f32_16x16x32_bf16 v[148:151],v[24:27],a[56:59],v[148:151]
s_waitcnt vmcnt(6)
buffer_store_dword v7,v5,s[36:39],s60 offen offset:0 sc0 sc1
s_mov_b32 s60,536870912
buffer_load_dwordx4 a[88:91],v11,s[36:39],s63 offen offset:2048 sc1
v_mfma_f32_16x16x32_bf16 v[28:31],v[192:195],v[208:211],0
v_mfma_f32_16x16x32_bf16 v[204:207],v[208:211],v[208:211],0
v_mfma_f32_16x16x32_bf16 v[152:155],v[20:23],a[12:15],0
v_mfma_f32_16x16x32_bf16 v[156:159],v[24:27],a[12:15],0
buffer_load_dwordx4 a[92:95],v11,s[36:39],s63 offen offset:3072 sc1
v_mfma_f32_16x16x32_bf16 v[152:155],v[20:23],a[28:31],v[152:155]
s_add_u32 s63,s87,99614720
v_mfma_f32_16x16x32_bf16 v[156:159],v[24:27],a[28:31],v[156:159]
v_mfma_f32_16x16x32_bf16 v[152:155],v[20:23],a[44:47],v[152:155]
v_cvt_pk_bf16_f32 v216,v144,v145
v_cvt_pk_bf16_f32 v217,v146,v147
v_mfma_f32_16x16x32_bf16 v[156:159],v[24:27],a[44:47],v[156:159]
v_cvt_pk_bf16_f32 v218,v148,v149
v_cvt_pk_bf16_f32 v219,v150,v151
v_mfma_f32_16x16x32_bf16 v[152:155],v[20:23],a[60:63],v[152:155]
buffer_load_dwordx4 a[96:99],v11,s[36:39],s63 offen offset:0 sc1
v_mfma_f32_16x16x32_bf16 v[156:159],v[24:27],a[60:63],v[156:159]
v_mfma_f32_16x16x32_bf16 v[28:31],v[192:195],v[212:215],v[28:31]
v_mfma_f32_16x16x32_bf16 v[204:207],v[212:215],v[212:215],v[204:207]
buffer_load_dwordx4 a[100:103],v11,s[36:39],s63 offen offset:1024 sc1
v_mfma_f32_16x16x32_bf16 v[28:31],v[192:195],v[216:219],v[28:31]
v_mfma_f32_16x16x32_bf16 v[204:207],v[216:219],v[216:219],v[204:207]
s_waitcnt lgkmcnt(0)
v_mfma_f32_16x16x32_bf16 v[160:163],v[20:23],v[32:35],0
v_cvt_pk_bf16_f32 v220,v152,v153
v_cvt_pk_bf16_f32 v221,v154,v155
v_mfma_f32_16x16x32_bf16 v[164:167],v[24:27],v[32:35],0
v_cvt_pk_bf16_f32 v222,v156,v157
v_cvt_pk_bf16_f32 v223,v158,v159
v_mfma_f32_16x16x32_bf16 v[168:171],v[20:23],v[36:39],0
buffer_load_dwordx4 a[104:107],v11,s[36:39],s63 offen offset:2048 sc1
v_mfma_f32_16x16x32_bf16 v[28:31],v[192:195],v[220:223],v[28:31]
buffer_load_dwordx4 a[108:111],v11,s[36:39],s63 offen offset:3072 sc1
v_mfma_f32_16x16x32_bf16 v[204:207],v[220:223],v[220:223],v[204:207]
s_add_u32 s63,s87,109051904
v_mfma_f32_16x16x32_bf16 v[172:175],v[24:27],v[36:39],0
v_mfma_f32_16x16x32_bf16 v[176:179],v[20:23],v[40:43],0
buffer_load_dwordx4 a[112:115],v11,s[36:39],s63 offen offset:0 sc1
v_mfma_f32_16x16x32_bf16 v[180:183],v[24:27],v[40:43],0
s_nop 0
v_add_f32_dpp v224,v28,v28 row_ror:8 row_mask:0xf bank_mask:0xf
s_nop 0
v_cndmask_b32 v225,v204,v205,s[46:47]
v_cndmask_b32 v226,v206,v207,s[46:47]
v_cndmask_b32 v225,v225,v226,s[56:57]
v_mfma_f32_16x16x32_bf16 v[184:187],v[20:23],v[44:47],0
ds_write_b32 v2,v224 offset:0
ds_write_b32 v0,v225 offset:0
v_mfma_f32_16x16x32_bf16 v[188:191],v[24:27],v[44:47],0
s_waitcnt lgkmcnt(0)
buffer_load_dwordx4 a[116:119],v11,s[36:39],s63 offen offset:1024 sc1
s_barrier
buffer_load_dwordx4 a[120:123],v11,s[36:39],s63 offen offset:2048 sc1
ds_read_b128 v[196:199],v3 offset:0
buffer_load_dwordx4 a[124:127],v11,s[36:39],s63 offen offset:3072 sc1
ds_read_b128 v[200:203],v3 offset:16
ds_read_b128 v[204:207],v3 offset:32
s_waitcnt lgkmcnt(0)
buffer_load_dword v8,v6,s[36:39],s69 offen offset:0 sc1
v_add_f32 v196,v196,v197
v_add_f32 v198,v198,v199
v_add_f32 v200,v200,v204
v_add_f32 v201,v201,v205
v_add_f32 v202,v202,v206
v_add_f32 v203,v203,v207
v_add_f32 v196,v196,v198
v_add_f32 v200,v200,v201
v_add_f32 v202,v202,v203
v_mul_f32 v196,s76,v196
v_add_f32 v197,v200,v202
v_mul_f32 v197,s76,v197
v_fma_f32 v197,-v196,v196,v197
v_max_f32 v197,0,v197
v_add_f32 v197,s77,v197
v_rsq_f32 v228,v197
s_nop 0
v_mul_f32_e64 v230,-v196,v228
v_mov_b32 v229,v228
v_mov_b32 v231,v230
v_pk_fma_f32 v[196:197],v[128:129],v[228:229],v[230:231]
v_pk_fma_f32 v[198:199],v[130:131],v[228:229],v[230:231]
v_pk_fma_f32 v[200:201],v[132:133],v[228:229],v[230:231]
v_pk_fma_f32 v[202:203],v[134:135],v[228:229],v[230:231]
v_pk_fma_f32 v[128:129],v[196:197],v[160:161],v[128:129]
v_pk_fma_f32 v[130:131],v[198:199],v[162:163],v[130:131]
v_pk_fma_f32 v[132:133],v[200:201],v[164:165],v[132:133]
v_pk_fma_f32 v[134:135],v[202:203],v[166:167],v[134:135]
v_pk_fma_f32 v[196:197],v[136:137],v[228:229],v[230:231]
v_pk_fma_f32 v[198:199],v[138:139],v[228:229],v[230:231]
v_pk_fma_f32 v[200:201],v[140:141],v[228:229],v[230:231]
v_pk_fma_f32 v[202:203],v[142:143],v[228:229],v[230:231]
v_pk_fma_f32 v[136:137],v[196:197],v[168:169],v[136:137]
v_pk_fma_f32 v[138:139],v[198:199],v[170:171],v[138:139]
v_pk_fma_f32 v[140:141],v[200:201],v[172:173],v[140:141]
v_pk_fma_f32 v[142:143],v[202:203],v[174:175],v[142:143]
v_pk_fma_f32 v[196:197],v[144:145],v[228:229],v[230:231]
v_pk_fma_f32 v[198:199],v[146:147],v[228:229],v[230:231]
v_pk_fma_f32 v[200:201],v[148:149],v[228:229],v[230:231]
v_pk_fma_f32 v[202:203],v[150:151],v[228:229],v[230:231]
v_pk_fma_f32 v[144:145],v[196:197],v[176:177],v[144:145]
v_pk_fma_f32 v[146:147],v[198:199],v[178:179],v[146:147]
v_pk_fma_f32 v[148:149],v[200:201],v[180:181],v[148:149]
v_pk_fma_f32 v[150:151],v[202:203],v[182:183],v[150:151]
v_pk_fma_f32 v[196:197],v[152:153],v[228:229],v[230:231]
v_pk_fma_f32 v[198:199],v[154:155],v[228:229],v[230:231]
v_pk_fma_f32 v[200:201],v[156:157],v[228:229],v[230:231]
v_pk_fma_f32 v[202:203],v[158:159],v[228:229],v[230:231]
v_pk_fma_f32 v[152:153],v[196:197],v[184:185],v[152:153]
v_pk_fma_f32 v[154:155],v[198:199],v[186:187],v[154:155]
v_pk_fma_f32 v[156:157],v[200:201],v[188:189],v[156:157]
v_pk_fma_f32 v[158:159],v[202:203],v[190:191],v[158:159])"
R"(
v_cvt_pk_bf16_f32 v208,v128,v129
v_cvt_pk_bf16_f32 v209,v130,v131
v_cvt_pk_bf16_f32 v210,v132,v133
v_cvt_pk_bf16_f32 v211,v134,v135
v_cvt_pk_bf16_f32 v212,v136,v137
v_cvt_pk_bf16_f32 v213,v138,v139
v_cvt_pk_bf16_f32 v214,v140,v141
v_cvt_pk_bf16_f32 v215,v142,v143
v_mfma_f32_16x16x32_bf16 v[28:31],v[192:195],v[208:211],0
v_cvt_pk_bf16_f32 v216,v144,v145
v_cvt_pk_bf16_f32 v217,v146,v147
v_mfma_f32_16x16x32_bf16 v[204:207],v[208:211],v[208:211],0
v_cvt_pk_bf16_f32 v218,v148,v149
v_cvt_pk_bf16_f32 v219,v150,v151
v_mfma_f32_16x16x32_bf16 v[28:31],v[192:195],v[212:215],v[28:31]
v_cvt_pk_bf16_f32 v220,v152,v153
v_cvt_pk_bf16_f32 v221,v154,v155
v_mfma_f32_16x16x32_bf16 v[204:207],v[212:215],v[212:215],v[204:207]
v_cvt_pk_bf16_f32 v222,v156,v157
v_cvt_pk_bf16_f32 v223,v158,v159
v_mfma_f32_16x16x32_bf16 v[28:31],v[192:195],v[216:219],v[28:31]
v_mfma_f32_16x16x32_bf16 v[204:207],v[216:219],v[216:219],v[204:207]
v_mfma_f32_16x16x32_bf16 v[28:31],v[192:195],v[220:223],v[28:31]
v_mfma_f32_16x16x32_bf16 v[204:207],v[220:223],v[220:223],v[204:207]
buffer_store_dwordx4 v[208:211],v1,s[36:39],s88 offen offset:0 sc0 sc1
buffer_store_dwordx4 v[212:215],v1,s[36:39],s88 offen offset:2048 sc0 sc1
s_add_u32 s88,s88,4096
s_nop 3
v_add_f32_dpp v224,v28,v28 row_ror:8 row_mask:0xf bank_mask:0xf
v_cndmask_b32 v225,v204,v205,s[46:47]
v_cndmask_b32 v226,v206,v207,s[46:47]
v_cndmask_b32 v225,v225,v226,s[56:57]
s_and_b32 s63,s89,128
ds_write_b32 v2,v224 offset:1024
ds_write_b32 v0,v225 offset:1024
buffer_store_dwordx4 v[216:219],v1,s[36:39],s88 offen offset:0 sc0 sc1
buffer_store_dwordx4 v[220:223],v1,s[36:39],s88 offen offset:2048 sc0 sc1
s_waitcnt lgkmcnt(0)
s_barrier
ds_read_b128 v[196:199],v3 offset:1024
ds_read_b128 v[200:203],v3 offset:1040
ds_read_b128 v[204:207],v3 offset:1056
s_waitcnt lgkmcnt(0)
v_add_f32 v196,v196,v197
v_add_f32 v198,v198,v199
v_add_f32 v200,v200,v204
v_add_f32 v201,v201,v205
v_add_f32 v202,v202,v206
v_add_f32 v203,v203,v207
v_add_f32 v196,v196,v198
v_add_f32 v200,v200,v201
v_add_f32 v202,v202,v203
v_mul_f32 v196,s76,v196
v_add_f32 v197,v200,v202
v_mul_f32 v197,s76,v197
v_fma_f32 v197,-v196,v196,v197
v_max_f32 v197,0,v197
v_add_f32 v198,s77,v197
v_fma_f32 v199,s77,v198,v197
v_rsq_f32 v205,v198
v_rsq_f32 v198,v199
v_mov_b32 v200,v196
v_mul_f32_e64 v204,-v196,v205
v_mul_f32 v203,v199,v198
v_mul_f32 v201,s78,v198
v_mul_f32 v199,v198,v198
v_mul_f32 v202,s79,v199
v_mul_f32 v202,v202,v201
s_lshr_b32 s63,s63,1
s_sub_u32 s63,s89,s63
buffer_store_dwordx4 v[200:203],v4,s[36:39],s89 offen offset:0 sc0 sc1
buffer_store_dwordx2 v[204:205],v10,s[36:39],s63 offen offset:256 sc0 sc1
s_add_u32 m0,s66,s52
s_cmp_ge_u32 s51,12582912
s_cbranch_scc1 Lr_ys3_%=
buffer_load_dwordx4 v11,s[36:39],s51 offen offset:0 sc1 lds
buffer_load_dwordx4 v11,s[36:39],s51 offen offset:1024 sc1 lds
buffer_load_dwordx4 v11,s[36:39],s51 offen offset:2048 sc1 lds
buffer_load_dwordx4 v11,s[36:39],s51 offen offset:3072 sc1 lds
s_lshl_b32 s63,s50,14
s_add_u32 s51,s51,s63
Lr_ys3_%=:
s_add_u32 s66,s66,16384
s_cmp_eq_u32 s66,151552
s_cselect_b32 s66,4096,s66
v_add_u32 v18,s66,v11
s_mov_b32 s60,s90
s_waitcnt vmcnt(6)
s_branch Lr_it1_%=
Lr_tc0_%=:
s_add_u32 s63,s87,71303168
buffer_load_dwordx4 a[64:67],v11,s[36:39],s63 offen offset:0 sc1
buffer_load_dwordx4 a[68:71],v11,s[36:39],s63 offen offset:1024 sc1
buffer_load_dwordx4 a[72:75],v11,s[36:39],s63 offen offset:2048 sc1
buffer_load_dwordx4 a[76:79],v11,s[36:39],s63 offen offset:3072 sc1
s_add_u32 s63,s87,89128960
buffer_load_dwordx4 a[80:83],v11,s[36:39],s63 offen offset:0 sc1
buffer_load_dwordx4 a[84:87],v11,s[36:39],s63 offen offset:1024 sc1
buffer_load_dwordx4 a[88:91],v11,s[36:39],s63 offen offset:2048 sc1
buffer_load_dwordx4 a[92:95],v11,s[36:39],s63 offen offset:3072 sc1
s_add_u32 s63,s87,99614720
buffer_load_dwordx4 a[96:99],v11,s[36:39],s63 offen offset:0 sc1
buffer_load_dwordx4 a[100:103],v11,s[36:39],s63 offen offset:1024 sc1
buffer_load_dwordx4 a[104:107],v11,s[36:39],s63 offen offset:2048 sc1
buffer_load_dwordx4 a[108:111],v11,s[36:39],s63 offen offset:3072 sc1
s_add_u32 s63,s87,109051904
buffer_load_dwordx4 a[112:115],v11,s[36:39],s63 offen offset:0 sc1
buffer_load_dwordx4 a[116:119],v11,s[36:39],s63 offen offset:1024 sc1
buffer_load_dwordx4 a[120:123],v11,s[36:39],s63 offen offset:2048 sc1
buffer_load_dwordx4 a[124:127],v11,s[36:39],s63 offen offset:3072 sc1
buffer_load_dword v8,v6,s[36:39],s69 offen offset:0 sc1
s_waitcnt vmcnt(17)
buffer_store_dword v7,v5,s[36:39],s60 offen offset:0 sc0 sc1
s_mov_b32 s60,536870912
s_waitcnt vmcnt(1)
s_branch Lr_it1_%=
Lr_it1_%=:
s_mov_b32 s88,s58
s_mov_b32 s89,s59
s_mov_b32 s90,s49
s_add_u32 s54,s54,s50
s_lshr_b32 s63,s54,1
s_and_b32 s64,s54,1
s_lshl_b32 s64,s64,7
s_lshl_b32 s87,s54,14
s_lshl_b32 s58,s63,15
s_add_u32 s58,s58,s64
s_add_u32 s58,s58,16908288
s_lshl_b32 s59,s63,9
s_add_u32 s59,s59,s64
s_add_u32 s59,s59,118556672
s_lshl_b32 s49,s54,9
s_add_u32 s49,s49,s92
s_lshl_b32 s91,s63,11
s_add_u32 s91,s91,121405440
s_lshl_b32 s69,s50,10
s_add_u32 s69,s69,s91
v_cmp_eq_u32 vcc,s48,v8
s_nop 0
s_cmp_eq_u64 vcc,exec
s_cselect_b32 s63,s54,256
s_cmp_lt_u32 s63,256
s_cbranch_scc1 Lr_ta1_%=
ds_read_b128 v[32:35],v18 offset:0
ds_read_b128 v[36:39],v18 offset:1024
ds_read_b128 v[40:43],v18 offset:2048
ds_read_b128 v[44:47],v18 offset:3072
v_mfma_f32_16x16x32_bf16 v[128:131],v[20:23],a[64:67],0
v_mfma_f32_16x16x32_bf16 v[132:135],v[24:27],a[64:67],0
v_mfma_f32_16x16x32_bf16 v[128:131],v[20:23],a[80:83],v[128:131]
v_mfma_f32_16x16x32_bf16 v[132:135],v[24:27],a[80:83],v[132:135]
v_mfma_f32_16x16x32_bf16 v[128:131],v[20:23],a[96:99],v[128:131]
v_mfma_f32_16x16x32_bf16 v[132:135],v[24:27],a[96:99],v[132:135]
v_mfma_f32_16x16x32_bf16 v[128:131],v[20:23],a[112:115],v[128:131]
v_mfma_f32_16x16x32_bf16 v[132:135],v[24:27],a[112:115],v[132:135]
v_mfma_f32_16x16x32_bf16 v[136:139],v[20:23],a[68:71],0
v_mfma_f32_16x16x32_bf16 v[140:143],v[24:27],a[68:71],0
v_mfma_f32_16x16x32_bf16 v[136:139],v[20:23],a[84:87],v[136:139]
v_mfma_f32_16x16x32_bf16 v[140:143],v[24:27],a[84:87],v[140:143]
v_mfma_f32_16x16x32_bf16 v[136:139],v[20:23],a[100:103],v[136:139]
s_nop 1
v_cvt_pk_bf16_f32 v208,v128,v129
v_cvt_pk_bf16_f32 v209,v130,v131
v_mfma_f32_16x16x32_bf16 v[140:143],v[24:27],a[100:103],v[140:143]
v_cvt_pk_bf16_f32 v210,v132,v133
v_cvt_pk_bf16_f32 v211,v134,v135
v_mfma_f32_16x16x32_bf16 v[136:139],v[20:23],a[116:119],v[136:139]
v_mfma_f32_16x16x32_bf16 v[140:143],v[24:27],a[116:119],v[140:143]
v_mfma_f32_16x16x32_bf16 v[144:147],v[20:23],a[72:75],0
v_mfma_f32_16x16x32_bf16 v[148:151],v[24:27],a[72:75],0
v_mfma_f32_16x16x32_bf16 v[144:147],v[20:23],a[88:91],v[144:147]
v_mfma_f32_16x16x32_bf16 v[148:151],v[24:27],a[88:91],v[148:151]
v_mfma_f32_16x16x32_bf16 v[144:147],v[20:23],a[104:107],v[144:147]
s_nop 1
v_cvt_pk_bf16_f32 v212,v136,v137
s_waitcnt vmcnt(0)
buffer_store_dword v7,v5,s[36:39],s60 offen offset:0 sc0 sc1
s_mov_b32 s60,536870912
v_cvt_pk_bf16_f32 v213,v138,v139
v_mfma_f32_16x16x32_bf16 v[148:151],v[24:27],a[104:107],v[148:151]
v_cvt_pk_bf16_f32 v214,v140,v141
v_cvt_pk_bf16_f32 v215,v142,v143
v_mfma_f32_16x16x32_bf16 v[144:147],v[20:23],a[120:123],v[144:147]
v_mfma_f32_16x16x32_bf16 v[148:151],v[24:27],a[120:123],v[148:151]
v_mfma_f32_16x16x32_bf16 v[28:31],v[192:195],v[208:211],0
v_mfma_f32_16x16x32_bf16 v[204:207],v[208:211],v[208:211],0
v_mfma_f32_16x16x32_bf16 v[152:155],v[20:23],a[76:79],0
v_mfma_f32_16x16x32_bf16 v[156:159],v[24:27],a[76:79],0
v_mfma_f32_16x16x32_bf16 v[152:155],v[20:23],a[92:95],v[152:155]
v_mfma_f32_16x16x32_bf16 v[156:159],v[24:27],a[92:95],v[156:159]
v_mfma_f32_16x16x32_bf16 v[152:155],v[20:23],a[108:111],v[152:155]
v_cvt_pk_bf16_f32 v216,v144,v145
v_cvt_pk_bf16_f32 v217,v146,v147
v_mfma_f32_16x16x32_bf16 v[156:159],v[24:27],a[108:111],v[156:159]
v_cvt_pk_bf16_f32 v218,v148,v149
v_cvt_pk_bf16_f32 v219,v150,v151
v_mfma_f32_16x16x32_bf16 v[152:155],v[20:23],a[124:127],v[152:155]
v_mfma_f32_16x16x32_bf16 v[156:159],v[24:27],a[124:127],v[156:159]
v_mfma_f32_16x16x32_bf16 v[28:31],v[192:195],v[212:215],v[28:31]
v_mfma_f32_16x16x32_bf16 v[204:207],v[212:215],v[212:215],v[204:207])"
R"(
v_mfma_f32_16x16x32_bf16 v[28:31],v[192:195],v[216:219],v[28:31]
v_mfma_f32_16x16x32_bf16 v[204:207],v[216:219],v[216:219],v[204:207]
s_waitcnt lgkmcnt(0)
v_mfma_f32_16x16x32_bf16 v[160:163],v[20:23],v[32:35],0
s_nop 0
v_cvt_pk_bf16_f32 v220,v152,v153
v_cvt_pk_bf16_f32 v221,v154,v155
v_mfma_f32_16x16x32_bf16 v[164:167],v[24:27],v[32:35],0
v_cvt_pk_bf16_f32 v222,v156,v157
v_cvt_pk_bf16_f32 v223,v158,v159
v_mfma_f32_16x16x32_bf16 v[168:171],v[20:23],v[36:39],0
s_nop 0
v_mfma_f32_16x16x32_bf16 v[28:31],v[192:195],v[220:223],v[28:31]
v_mfma_f32_16x16x32_bf16 v[204:207],v[220:223],v[220:223],v[204:207]
v_mfma_f32_16x16x32_bf16 v[172:175],v[24:27],v[36:39],0
v_mfma_f32_16x16x32_bf16 v[176:179],v[20:23],v[40:43],0
v_mfma_f32_16x16x32_bf16 v[180:183],v[24:27],v[40:43],0
s_nop 3
v_add_f32_dpp v224,v28,v28 row_ror:8 row_mask:0xf bank_mask:0xf
v_cndmask_b32 v225,v204,v205,s[46:47]
v_cndmask_b32 v226,v206,v207,s[46:47]
v_cndmask_b32 v225,v225,v226,s[56:57]
v_mfma_f32_16x16x32_bf16 v[184:187],v[20:23],v[44:47],0
ds_write_b32 v2,v224 offset:0
ds_write_b32 v0,v225 offset:0
v_mfma_f32_16x16x32_bf16 v[188:191],v[24:27],v[44:47],0
s_waitcnt lgkmcnt(0)
s_barrier
ds_read_b128 v[196:199],v3 offset:0
ds_read_b128 v[200:203],v3 offset:16
ds_read_b128 v[204:207],v3 offset:32
s_waitcnt lgkmcnt(0)
v_add_f32 v196,v196,v197
v_add_f32 v198,v198,v199
v_add_f32 v200,v200,v204
v_add_f32 v201,v201,v205
v_add_f32 v202,v202,v206
v_add_f32 v203,v203,v207
v_add_f32 v196,v196,v198
v_add_f32 v200,v200,v201
v_add_f32 v202,v202,v203
v_mul_f32 v196,s76,v196
v_add_f32 v197,v200,v202
v_mul_f32 v197,s76,v197
v_fma_f32 v197,-v196,v196,v197
v_max_f32 v197,0,v197
v_add_f32 v197,s77,v197
v_rsq_f32 v228,v197
s_nop 0
v_mul_f32_e64 v230,-v196,v228
v_mov_b32 v229,v228
v_mov_b32 v231,v230
v_pk_fma_f32 v[196:197],v[128:129],v[228:229],v[230:231]
v_pk_fma_f32 v[198:199],v[130:131],v[228:229],v[230:231]
v_pk_fma_f32 v[200:201],v[132:133],v[228:229],v[230:231]
v_pk_fma_f32 v[202:203],v[134:135],v[228:229],v[230:231]
v_pk_fma_f32 v[128:129],v[196:197],v[160:161],v[128:129]
v_pk_fma_f32 v[130:131],v[198:199],v[162:163],v[130:131]
v_pk_fma_f32 v[132:133],v[200:201],v[164:165],v[132:133]
v_pk_fma_f32 v[134:135],v[202:203],v[166:167],v[134:135]
v_pk_fma_f32 v[196:197],v[136:137],v[228:229],v[230:231]
v_pk_fma_f32 v[198:199],v[138:139],v[228:229],v[230:231]
v_pk_fma_f32 v[200:201],v[140:141],v[228:229],v[230:231]
v_pk_fma_f32 v[202:203],v[142:143],v[228:229],v[230:231]
v_pk_fma_f32 v[136:137],v[196:197],v[168:169],v[136:137]
v_pk_fma_f32 v[138:139],v[198:199],v[170:171],v[138:139]
v_pk_fma_f32 v[140:141],v[200:201],v[172:173],v[140:141]
v_pk_fma_f32 v[142:143],v[202:203],v[174:175],v[142:143]
v_pk_fma_f32 v[196:197],v[144:145],v[228:229],v[230:231]
v_pk_fma_f32 v[198:199],v[146:147],v[228:229],v[230:231]
v_pk_fma_f32 v[200:201],v[148:149],v[228:229],v[230:231]
v_pk_fma_f32 v[202:203],v[150:151],v[228:229],v[230:231]
v_pk_fma_f32 v[144:145],v[196:197],v[176:177],v[144:145]
v_pk_fma_f32 v[146:147],v[198:199],v[178:179],v[146:147]
v_pk_fma_f32 v[148:149],v[200:201],v[180:181],v[148:149]
v_pk_fma_f32 v[150:151],v[202:203],v[182:183],v[150:151]
v_pk_fma_f32 v[196:197],v[152:153],v[228:229],v[230:231]
v_pk_fma_f32 v[198:199],v[154:155],v[228:229],v[230:231]
v_pk_fma_f32 v[200:201],v[156:157],v[228:229],v[230:231]
v_pk_fma_f32 v[202:203],v[158:159],v[228:229],v[230:231]
v_pk_fma_f32 v[152:153],v[196:197],v[184:185],v[152:153]
v_pk_fma_f32 v[154:155],v[198:199],v[186:187],v[154:155]
v_pk_fma_f32 v[156:157],v[200:201],v[188:189],v[156:157]
v_pk_fma_f32 v[158:159],v[202:203],v[190:191],v[158:159]
v_cvt_pk_bf16_f32 v208,v128,v129
v_cvt_pk_bf16_f32 v209,v130,v131
v_cvt_pk_bf16_f32 v210,v132,v133
v_cvt_pk_bf16_f32 v211,v134,v135
v_cvt_pk_bf16_f32 v212,v136,v137
v_cvt_pk_bf16_f32 v213,v138,v139
v_cvt_pk_bf16_f32 v214,v140,v141
v_cvt_pk_bf16_f32 v215,v142,v143
v_mfma_f32_16x16x32_bf16 v[28:31],v[192:195],v[208:211],0
v_cvt_pk_bf16_f32 v216,v144,v145
v_cvt_pk_bf16_f32 v217,v146,v147
v_mfma_f32_16x16x32_bf16 v[204:207],v[208:211],v[208:211],0
v_cvt_pk_bf16_f32 v218,v148,v149
v_cvt_pk_bf16_f32 v219,v150,v151
v_mfma_f32_16x16x32_bf16 v[28:31],v[192:195],v[212:215],v[28:31]
v_cvt_pk_bf16_f32 v220,v152,v153
v_cvt_pk_bf16_f32 v221,v154,v155
v_mfma_f32_16x16x32_bf16 v[204:207],v[212:215],v[212:215],v[204:207]
v_cvt_pk_bf16_f32 v222,v156,v157
v_cvt_pk_bf16_f32 v223,v158,v159
v_mfma_f32_16x16x32_bf16 v[28:31],v[192:195],v[216:219],v[28:31]
v_mfma_f32_16x16x32_bf16 v[204:207],v[216:219],v[216:219],v[204:207]
v_mfma_f32_16x16x32_bf16 v[28:31],v[192:195],v[220:223],v[28:31]
v_mfma_f32_16x16x32_bf16 v[204:207],v[220:223],v[220:223],v[204:207]
buffer_load_dword v8,v6,s[36:39],s91 offen offset:0 sc1
buffer_store_dwordx4 v[208:211],v1,s[36:39],s88 offen offset:0 sc0 sc1
buffer_store_dwordx4 v[212:215],v1,s[36:39],s88 offen offset:2048 sc0 sc1
s_add_u32 s88,s88,4096
s_nop 2
v_add_f32_dpp v224,v28,v28 row_ror:8 row_mask:0xf bank_mask:0xf
v_cndmask_b32 v225,v204,v205,s[46:47]
v_cndmask_b32 v226,v206,v207,s[46:47]
v_cndmask_b32 v225,v225,v226,s[56:57]
s_and_b32 s63,s89,128
ds_write_b32 v2,v224 offset:1024
ds_write_b32 v0,v225 offset:1024
buffer_store_dwordx4 v[216:219],v1,s[36:39],s88 offen offset:0 sc0 sc1
buffer_store_dwordx4 v[220:223],v1,s[36:39],s88 offen offset:2048 sc0 sc1
s_waitcnt lgkmcnt(0)
s_barrier
ds_read_b128 v[196:199],v3 offset:1024
ds_read_b128 v[200:203],v3 offset:1040
ds_read_b128 v[204:207],v3 offset:1056
s_waitcnt lgkmcnt(0)
v_add_f32 v196,v196,v197
v_add_f32 v198,v198,v199
v_add_f32 v200,v200,v204
v_add_f32 v201,v201,v205
v_add_f32 v202,v202,v206
v_add_f32 v203,v203,v207
v_add_f32 v196,v196,v198
v_add_f32 v200,v200,v201
v_add_f32 v202,v202,v203
v_mul_f32 v196,s76,v196
v_add_f32 v197,v200,v202
v_mul_f32 v197,s76,v197
v_fma_f32 v197,-v196,v196,v197
v_max_f32 v197,0,v197
v_add_f32 v198,s77,v197
v_fma_f32 v199,s77,v198,v197
v_rsq_f32 v205,v198
v_rsq_f32 v198,v199
v_mov_b32 v200,v196
v_mul_f32_e64 v204,-v196,v205
v_mul_f32 v203,v199,v198
v_mul_f32 v201,s78,v198
v_mul_f32 v199,v198,v198
v_mul_f32 v202,s79,v199
v_mul_f32 v202,v202,v201
s_lshr_b32 s63,s63,1
s_sub_u32 s63,s89,s63
buffer_store_dwordx4 v[200:203],v4,s[36:39],s89 offen offset:0 sc0 sc1
buffer_store_dwordx2 v[204:205],v10,s[36:39],s63 offen offset:256 sc0 sc1
s_add_u32 m0,s66,s52
s_cmp_ge_u32 s51,12582912
s_cbranch_scc1 Lr_ys4_%=
buffer_load_dwordx4 v11,s[36:39],s51 offen offset:0 sc1 lds
buffer_load_dwordx4 v11,s[36:39],s51 offen offset:1024 sc1 lds
buffer_load_dwordx4 v11,s[36:39],s51 offen offset:2048 sc1 lds
buffer_load_dwordx4 v11,s[36:39],s51 offen offset:3072 sc1 lds
s_lshl_b32 s63,s50,14
s_add_u32 s51,s51,s63
Lr_ys4_%=:
s_add_u32 s66,s66,16384
s_cmp_eq_u32 s66,151552
s_cselect_b32 s66,4096,s66
v_add_u32 v18,s66,v11
s_mov_b32 s60,s90
s_waitcnt vmcnt(6)
v_cmp_eq_u32 vcc,s48,v8
s_nop 0
s_cmp_eq_u64 vcc,exec
s_cselect_b32 s63,s54,256
s_cmp_lt_u32 s63,256
s_cbranch_scc1 Lr_tc1_%=
buffer_load_dword v8,v6,s[36:39],s91 offen offset:0 sc1
s_waitcnt vmcnt(0)
buffer_store_dword v7,v5,s[36:39],s60 offen offset:0 sc0 sc1
s_mov_b32 s60,536870912
v_cmp_eq_u32 vcc,s48,v8
s_nop 0
s_cmp_eq_u64 vcc,exec
s_cselect_b32 s63,s54,256
s_cmp_lt_u32 s63,256
s_cbranch_scc1 Lr_bq1_%=
s_cmp_ge_u32 s54,256
s_cbranch_scc1 Lr_done_%=
Lr_bp1_%=:
buffer_load_dword v8,v6,s[36:39],s91 offen offset:0 sc1
s_waitcnt vmcnt(0)
v_cmp_eq_u32 vcc,s48,v8
s_nop 0
s_cmp_eq_u64 vcc,exec
s_cselect_b32 s63,1,s96
s_cmp_lg_u32 s63,0
s_cbranch_scc1 Lr_bq1_%=)"
R"(
s_add_u32 s74,s74,1
s_cmp_ge_u32 s74,20000
s_cselect_b32 s96,1,0
s_sleep 4
s_branch Lr_bp1_%=
Lr_bq1_%=:
s_add_u32 s63,s87,71303168
buffer_load_dwordx4 a[0:3],v11,s[36:39],s63 offen offset:0 sc1
buffer_load_dwordx4 a[4:7],v11,s[36:39],s63 offen offset:1024 sc1
buffer_load_dwordx4 a[8:11],v11,s[36:39],s63 offen offset:2048 sc1
buffer_load_dwordx4 a[12:15],v11,s[36:39],s63 offen offset:3072 sc1
s_add_u32 s63,s87,89128960
buffer_load_dwordx4 a[16:19],v11,s[36:39],s63 offen offset:0 sc1
buffer_load_dwordx4 a[20:23],v11,s[36:39],s63 offen offset:1024 sc1
buffer_load_dwordx4 a[24:27],v11,s[36:39],s63 offen offset:2048 sc1
buffer_load_dwordx4 a[28:31],v11,s[36:39],s63 offen offset:3072 sc1
s_add_u32 s63,s87,99614720
buffer_load_dwordx4 a[32:35],v11,s[36:39],s63 offen offset:0 sc1
buffer_load_dwordx4 a[36:39],v11,s[36:39],s63 offen offset:1024 sc1
buffer_load_dwordx4 a[40:43],v11,s[36:39],s63 offen offset:2048 sc1
buffer_load_dwordx4 a[44:47],v11,s[36:39],s63 offen offset:3072 sc1
s_add_u32 s63,s87,109051904
buffer_load_dwordx4 a[48:51],v11,s[36:39],s63 offen offset:0 sc1
buffer_load_dwordx4 a[52:55],v11,s[36:39],s63 offen offset:1024 sc1
buffer_load_dwordx4 a[56:59],v11,s[36:39],s63 offen offset:2048 sc1
buffer_load_dwordx4 a[60:63],v11,s[36:39],s63 offen offset:3072 sc1
buffer_load_dword v8,v6,s[36:39],s69 offen offset:0 sc1
s_waitcnt vmcnt(0)
s_branch Lr_it0_%=
Lr_ta1_%=:
ds_read_b128 v[32:35],v18 offset:0
ds_read_b128 v[36:39],v18 offset:1024
ds_read_b128 v[40:43],v18 offset:2048
ds_read_b128 v[44:47],v18 offset:3072
v_mfma_f32_16x16x32_bf16 v[128:131],v[20:23],a[64:67],0
s_add_u32 s63,s87,71303168
v_mfma_f32_16x16x32_bf16 v[132:135],v[24:27],a[64:67],0
buffer_load_dwordx4 a[0:3],v11,s[36:39],s63 offen offset:0 sc1
v_mfma_f32_16x16x32_bf16 v[128:131],v[20:23],a[80:83],v[128:131]
v_mfma_f32_16x16x32_bf16 v[132:135],v[24:27],a[80:83],v[132:135]
buffer_load_dwordx4 a[4:7],v11,s[36:39],s63 offen offset:1024 sc1
v_mfma_f32_16x16x32_bf16 v[128:131],v[20:23],a[96:99],v[128:131]
v_mfma_f32_16x16x32_bf16 v[132:135],v[24:27],a[96:99],v[132:135]
v_mfma_f32_16x16x32_bf16 v[128:131],v[20:23],a[112:115],v[128:131]
v_mfma_f32_16x16x32_bf16 v[132:135],v[24:27],a[112:115],v[132:135]
buffer_load_dwordx4 a[8:11],v11,s[36:39],s63 offen offset:2048 sc1
v_mfma_f32_16x16x32_bf16 v[136:139],v[20:23],a[68:71],0
v_mfma_f32_16x16x32_bf16 v[140:143],v[24:27],a[68:71],0
v_mfma_f32_16x16x32_bf16 v[136:139],v[20:23],a[84:87],v[136:139]
v_mfma_f32_16x16x32_bf16 v[140:143],v[24:27],a[84:87],v[140:143]
buffer_load_dwordx4 a[12:15],v11,s[36:39],s63 offen offset:3072 sc1
v_mfma_f32_16x16x32_bf16 v[136:139],v[20:23],a[100:103],v[136:139]
v_cvt_pk_bf16_f32 v208,v128,v129
v_cvt_pk_bf16_f32 v209,v130,v131
v_mfma_f32_16x16x32_bf16 v[140:143],v[24:27],a[100:103],v[140:143]
v_cvt_pk_bf16_f32 v210,v132,v133
v_cvt_pk_bf16_f32 v211,v134,v135
v_mfma_f32_16x16x32_bf16 v[136:139],v[20:23],a[116:119],v[136:139]
s_add_u32 s63,s87,89128960
v_mfma_f32_16x16x32_bf16 v[140:143],v[24:27],a[116:119],v[140:143]
buffer_load_dwordx4 a[16:19],v11,s[36:39],s63 offen offset:0 sc1
v_mfma_f32_16x16x32_bf16 v[144:147],v[20:23],a[72:75],0
v_mfma_f32_16x16x32_bf16 v[148:151],v[24:27],a[72:75],0
v_mfma_f32_16x16x32_bf16 v[144:147],v[20:23],a[88:91],v[144:147]
v_mfma_f32_16x16x32_bf16 v[148:151],v[24:27],a[88:91],v[148:151]
buffer_load_dwordx4 a[20:23],v11,s[36:39],s63 offen offset:1024 sc1
v_mfma_f32_16x16x32_bf16 v[144:147],v[20:23],a[104:107],v[144:147]
v_cvt_pk_bf16_f32 v212,v136,v137
v_cvt_pk_bf16_f32 v213,v138,v139
v_mfma_f32_16x16x32_bf16 v[148:151],v[24:27],a[104:107],v[148:151]
v_cvt_pk_bf16_f32 v214,v140,v141
v_cvt_pk_bf16_f32 v215,v142,v143
v_mfma_f32_16x16x32_bf16 v[144:147],v[20:23],a[120:123],v[144:147]
v_mfma_f32_16x16x32_bf16 v[148:151],v[24:27],a[120:123],v[148:151]
s_waitcnt vmcnt(6)
buffer_store_dword v7,v5,s[36:39],s60 offen offset:0 sc0 sc1
s_mov_b32 s60,536870912
buffer_load_dwordx4 a[24:27],v11,s[36:39],s63 offen offset:2048 sc1
v_mfma_f32_16x16x32_bf16 v[28:31],v[192:195],v[208:211],0
v_mfma_f32_16x16x32_bf16 v[204:207],v[208:211],v[208:211],0
v_mfma_f32_16x16x32_bf16 v[152:155],v[20:23],a[76:79],0
v_mfma_f32_16x16x32_bf16 v[156:159],v[24:27],a[76:79],0
buffer_load_dwordx4 a[28:31],v11,s[36:39],s63 offen offset:3072 sc1
v_mfma_f32_16x16x32_bf16 v[152:155],v[20:23],a[92:95],v[152:155]
s_add_u32 s63,s87,99614720
v_mfma_f32_16x16x32_bf16 v[156:159],v[24:27],a[92:95],v[156:159]
v_mfma_f32_16x16x32_bf16 v[152:155],v[20:23],a[108:111],v[152:155]
v_cvt_pk_bf16_f32 v216,v144,v145
v_cvt_pk_bf16_f32 v217,v146,v147
v_mfma_f32_16x16x32_bf16 v[156:159],v[24:27],a[108:111],v[156:159]
v_cvt_pk_bf16_f32 v218,v148,v149
v_cvt_pk_bf16_f32 v219,v150,v151
v_mfma_f32_16x16x32_bf16 v[152:155],v[20:23],a[124:127],v[152:155]
buffer_load_dwordx4 a[32:35],v11,s[36:39],s63 offen offset:0 sc1
v_mfma_f32_16x16x32_bf16 v[156:159],v[24:27],a[124:127],v[156:159]
v_mfma_f32_16x16x32_bf16 v[28:31],v[192:195],v[212:215],v[28:31]
v_mfma_f32_16x16x32_bf16 v[204:207],v[212:215],v[212:215],v[204:207]
buffer_load_dwordx4 a[36:39],v11,s[36:39],s63 offen offset:1024 sc1
v_mfma_f32_16x16x32_bf16 v[28:31],v[192:195],v[216:219],v[28:31]
v_mfma_f32_16x16x32_bf16 v[204:207],v[216:219],v[216:219],v[204:207]
s_waitcnt lgkmcnt(0)
v_mfma_f32_16x16x32_bf16 v[160:163],v[20:23],v[32:35],0
v_cvt_pk_bf16_f32 v220,v152,v153
v_cvt_pk_bf16_f32 v221,v154,v155
v_mfma_f32_16x16x32_bf16 v[164:167],v[24:27],v[32:35],0
v_cvt_pk_bf16_f32 v222,v156,v157
v_cvt_pk_bf16_f32 v223,v158,v159
v_mfma_f32_16x16x32_bf16 v[168:171],v[20:23],v[36:39],0
buffer_load_dwordx4 a[40:43],v11,s[36:39],s63 offen offset:2048 sc1
v_mfma_f32_16x16x32_bf16 v[28:31],v[192:195],v[220:223],v[28:31]
buffer_load_dwordx4 a[44:47],v11,s[36:39],s63 offen offset:3072 sc1
v_mfma_f32_16x16x32_bf16 v[204:207],v[220:223],v[220:223],v[204:207]
s_add_u32 s63,s87,109051904
v_mfma_f32_16x16x32_bf16 v[172:175],v[24:27],v[36:39],0
v_mfma_f32_16x16x32_bf16 v[176:179],v[20:23],v[40:43],0
buffer_load_dwordx4 a[48:51],v11,s[36:39],s63 offen offset:0 sc1
v_mfma_f32_16x16x32_bf16 v[180:183],v[24:27],v[40:43],0
s_nop 0
v_add_f32_dpp v224,v28,v28 row_ror:8 row_mask:0xf bank_mask:0xf
s_nop 0
v_cndmask_b32 v225,v204,v205,s[46:47]
v_cndmask_b32 v226,v206,v207,s[46:47]
v_cndmask_b32 v225,v225,v226,s[56:57]
v_mfma_f32_16x16x32_bf16 v[184:187],v[20:23],v[44:47],0
ds_write_b32 v2,v224 offset:0
ds_write_b32 v0,v225 offset:0
v_mfma_f32_16x16x32_bf16 v[188:191],v[24:27],v[44:47],0
s_waitcnt lgkmcnt(0)
buffer_load_dwordx4 a[52:55],v11,s[36:39],s63 offen offset:1024 sc1
s_barrier
buffer_load_dwordx4 a[56:59],v11,s[36:39],s63 offen offset:2048 sc1
ds_read_b128 v[196:199],v3 offset:0
buffer_load_dwordx4 a[60:63],v11,s[36:39],s63 offen offset:3072 sc1
ds_read_b128 v[200:203],v3 offset:16
ds_read_b128 v[204:207],v3 offset:32
s_waitcnt lgkmcnt(0)
buffer_load_dword v8,v6,s[36:39],s69 offen offset:0 sc1
v_add_f32 v196,v196,v197
v_add_f32 v198,v198,v199
v_add_f32 v200,v200,v204
v_add_f32 v201,v201,v205
v_add_f32 v202,v202,v206
v_add_f32 v203,v203,v207
v_add_f32 v196,v196,v198
v_add_f32 v200,v200,v201
v_add_f32 v202,v202,v203
v_mul_f32 v196,s76,v196
v_add_f32 v197,v200,v202
v_mul_f32 v197,s76,v197
v_fma_f32 v197,-v196,v196,v197
v_max_f32 v197,0,v197
v_add_f32 v197,s77,v197
v_rsq_f32 v228,v197
s_nop 0
v_mul_f32_e64 v230,-v196,v228
v_mov_b32 v229,v228
v_mov_b32 v231,v230
v_pk_fma_f32 v[196:197],v[128:129],v[228:229],v[230:231]
v_pk_fma_f32 v[198:199],v[130:131],v[228:229],v[230:231]
v_pk_fma_f32 v[200:201],v[132:133],v[228:229],v[230:231]
v_pk_fma_f32 v[202:203],v[134:135],v[228:229],v[230:231]
v_pk_fma_f32 v[128:129],v[196:197],v[160:161],v[128:129]
v_pk_fma_f32 v[130:131],v[198:199],v[162:163],v[130:131]
v_pk_fma_f32 v[132:133],v[200:201],v[164:165],v[132:133]
v_pk_fma_f32 v[134:135],v[202:203],v[166:167],v[134:135]
v_pk_fma_f32 v[196:197],v[136:137],v[228:229],v[230:231]
v_pk_fma_f32 v[198:199],v[138:139],v[228:229],v[230:231]
v_pk_fma_f32 v[200:201],v[140:141],v[228:229],v[230:231]
v_pk_fma_f32 v[202:203],v[142:143],v[228:229],v[230:231]
v_pk_fma_f32 v[136:137],v[196:197],v[168:169],v[136:137]
v_pk_fma_f32 v[138:139],v[198:199],v[170:171],v[138:139]
v_pk_fma_f32 v[140:141],v[200:201],v[172:173],v[140:141]
v_pk_fma_f32 v[142:143],v[202:203],v[174:175],v[142:143]
v_pk_fma_f32 v[196:197],v[144:145],v[228:229],v[230:231]
v_pk_fma_f32 v[198:199],v[146:147],v[228:229],v[230:231]
v_pk_fma_f32 v[200:201],v[148:149],v[228:229],v[230:231]
v_pk_fma_f32 v[202:203],v[150:151],v[228:229],v[230:231]
v_pk_fma_f32 v[144:145],v[196:197],v[176:177],v[144:145]
v_pk_fma_f32 v[146:147],v[198:199],v[178:179],v[146:147]
v_pk_fma_f32 v[148:149],v[200:201],v[180:181],v[148:149]
v_pk_fma_f32 v[150:151],v[202:203],v[182:183],v[150:151]
v_pk_fma_f32 v[196:197],v[152:153],v[228:229],v[230:231]
v_pk_fma_f32 v[198:199],v[154:155],v[228:229],v[230:231]
v_pk_fma_f32 v[200:201],v[156:157],v[228:229],v[230:231]
v_pk_fma_f32 v[202:203],v[158:159],v[228:229],v[230:231]
v_pk_fma_f32 v[152:153],v[196:197],v[184:185],v[152:153]
v_pk_fma_f32 v[154:155],v[198:199],v[186:187],v[154:155]
v_pk_fma_f32 v[156:157],v[200:201],v[188:189],v[156:157]
v_pk_fma_f32 v[158:159],v[202:203],v[190:191],v[158:159]
v_cvt_pk_bf16_f32 v208,v128,v129
v_cvt_pk_bf16_f32 v209,v130,v131
v_cvt_pk_bf16_f32 v210,v132,v133
v_cvt_pk_bf16_f32 v211,v134,v135
v_cvt_pk_bf16_f32 v212,v136,v137
v_cvt_pk_bf16_f32 v213,v138,v139
v_cvt_pk_bf16_f32 v214,v140,v141
v_cvt_pk_bf16_f32 v215,v142,v143
v_mfma_f32_16x16x32_bf16 v[28:31],v[192:195],v[208:211],0
v_cvt_pk_bf16_f32 v216,v144,v145
v_cvt_pk_bf16_f32 v217,v146,v147)"
R"(
v_mfma_f32_16x16x32_bf16 v[204:207],v[208:211],v[208:211],0
v_cvt_pk_bf16_f32 v218,v148,v149
v_cvt_pk_bf16_f32 v219,v150,v151
v_mfma_f32_16x16x32_bf16 v[28:31],v[192:195],v[212:215],v[28:31]
v_cvt_pk_bf16_f32 v220,v152,v153
v_cvt_pk_bf16_f32 v221,v154,v155
v_mfma_f32_16x16x32_bf16 v[204:207],v[212:215],v[212:215],v[204:207]
v_cvt_pk_bf16_f32 v222,v156,v157
v_cvt_pk_bf16_f32 v223,v158,v159
v_mfma_f32_16x16x32_bf16 v[28:31],v[192:195],v[216:219],v[28:31]
v_mfma_f32_16x16x32_bf16 v[204:207],v[216:219],v[216:219],v[204:207]
v_mfma_f32_16x16x32_bf16 v[28:31],v[192:195],v[220:223],v[28:31]
v_mfma_f32_16x16x32_bf16 v[204:207],v[220:223],v[220:223],v[204:207]
buffer_store_dwordx4 v[208:211],v1,s[36:39],s88 offen offset:0 sc0 sc1
buffer_store_dwordx4 v[212:215],v1,s[36:39],s88 offen offset:2048 sc0 sc1
s_add_u32 s88,s88,4096
s_nop 3
v_add_f32_dpp v224,v28,v28 row_ror:8 row_mask:0xf bank_mask:0xf
v_cndmask_b32 v225,v204,v205,s[46:47]
v_cndmask_b32 v226,v206,v207,s[46:47]
v_cndmask_b32 v225,v225,v226,s[56:57]
s_and_b32 s63,s89,128
ds_write_b32 v2,v224 offset:1024
ds_write_b32 v0,v225 offset:1024
buffer_store_dwordx4 v[216:219],v1,s[36:39],s88 offen offset:0 sc0 sc1
buffer_store_dwordx4 v[220:223],v1,s[36:39],s88 offen offset:2048 sc0 sc1
s_waitcnt lgkmcnt(0)
s_barrier
ds_read_b128 v[196:199],v3 offset:1024
ds_read_b128 v[200:203],v3 offset:1040
ds_read_b128 v[204:207],v3 offset:1056
s_waitcnt lgkmcnt(0)
v_add_f32 v196,v196,v197
v_add_f32 v198,v198,v199
v_add_f32 v200,v200,v204
v_add_f32 v201,v201,v205
v_add_f32 v202,v202,v206
v_add_f32 v203,v203,v207
v_add_f32 v196,v196,v198
v_add_f32 v200,v200,v201
v_add_f32 v202,v202,v203
v_mul_f32 v196,s76,v196
v_add_f32 v197,v200,v202
v_mul_f32 v197,s76,v197
v_fma_f32 v197,-v196,v196,v197
v_max_f32 v197,0,v197
v_add_f32 v198,s77,v197
v_fma_f32 v199,s77,v198,v197
v_rsq_f32 v205,v198
v_rsq_f32 v198,v199
v_mov_b32 v200,v196
v_mul_f32_e64 v204,-v196,v205
v_mul_f32 v203,v199,v198
v_mul_f32 v201,s78,v198
v_mul_f32 v199,v198,v198
v_mul_f32 v202,s79,v199
v_mul_f32 v202,v202,v201
s_lshr_b32 s63,s63,1
s_sub_u32 s63,s89,s63
buffer_store_dwordx4 v[200:203],v4,s[36:39],s89 offen offset:0 sc0 sc1
buffer_store_dwordx2 v[204:205],v10,s[36:39],s63 offen offset:256 sc0 sc1
s_add_u32 m0,s66,s52
s_cmp_ge_u32 s51,12582912
s_cbranch_scc1 Lr_ys5_%=
buffer_load_dwordx4 v11,s[36:39],s51 offen offset:0 sc1 lds
buffer_load_dwordx4 v11,s[36:39],s51 offen offset:1024 sc1 lds
buffer_load_dwordx4 v11,s[36:39],s51 offen offset:2048 sc1 lds
buffer_load_dwordx4 v11,s[36:39],s51 offen offset:3072 sc1 lds
s_lshl_b32 s63,s50,14
s_add_u32 s51,s51,s63
Lr_ys5_%=:
s_add_u32 s66,s66,16384
s_cmp_eq_u32 s66,151552
s_cselect_b32 s66,4096,s66
v_add_u32 v18,s66,v11
s_mov_b32 s60,s90
s_waitcnt vmcnt(6)
s_branch Lr_it0_%=
Lr_tc1_%=:
s_add_u32 s63,s87,71303168
buffer_load_dwordx4 a[0:3],v11,s[36:39],s63 offen offset:0 sc1
buffer_load_dwordx4 a[4:7],v11,s[36:39],s63 offen offset:1024 sc1
buffer_load_dwordx4 a[8:11],v11,s[36:39],s63 offen offset:2048 sc1
buffer_load_dwordx4 a[12:15],v11,s[36:39],s63 offen offset:3072 sc1
s_add_u32 s63,s87,89128960
buffer_load_dwordx4 a[16:19],v11,s[36:39],s63 offen offset:0 sc1
buffer_load_dwordx4 a[20:23],v11,s[36:39],s63 offen offset:1024 sc1
buffer_load_dwordx4 a[24:27],v11,s[36:39],s63 offen offset:2048 sc1
buffer_load_dwordx4 a[28:31],v11,s[36:39],s63 offen offset:3072 sc1
s_add_u32 s63,s87,99614720
buffer_load_dwordx4 a[32:35],v11,s[36:39],s63 offen offset:0 sc1
buffer_load_dwordx4 a[36:39],v11,s[36:39],s63 offen offset:1024 sc1
buffer_load_dwordx4 a[40:43],v11,s[36:39],s63 offen offset:2048 sc1
buffer_load_dwordx4 a[44:47],v11,s[36:39],s63 offen offset:3072 sc1
s_add_u32 s63,s87,109051904
buffer_load_dwordx4 a[48:51],v11,s[36:39],s63 offen offset:0 sc1
buffer_load_dwordx4 a[52:55],v11,s[36:39],s63 offen offset:1024 sc1
buffer_load_dwordx4 a[56:59],v11,s[36:39],s63 offen offset:2048 sc1
buffer_load_dwordx4 a[60:63],v11,s[36:39],s63 offen offset:3072 sc1
buffer_load_dword v8,v6,s[36:39],s69 offen offset:0 sc1
s_waitcnt vmcnt(17)
buffer_store_dword v7,v5,s[36:39],s60 offen offset:0 sc0 sc1
s_mov_b32 s60,536870912
s_waitcnt vmcnt(1)
s_branch Lr_it0_%=
Lr_done_%=:
s_waitcnt vmcnt(0) lgkmcnt(0)
s_barrier
Lcode_r_e:)"
  :: "s"(ws), "s"(sK), "s"(sStep), "s"(sRq), "s"(sEpR), "v"(vRP), "v"(vRF), "v"(vRwr), "v"(vRrd), "v"(vRS), "v"(vFlag), "v"(vL4), "v"(vRL) : "v0", "v1", "v2", "v3", "v4", "v5", "v6", "v7", "v8", "v9", "v10", "v11", "v12", "v13", "v14", "v15", "v16", "v17", "v18", "v19", "v20", "v21", "v22", "v23", "v24", "v25", "v26", "v27", "v28", "v29", "v30", "v31", "v32", "v33", "v34", "v35", "v36", "v37", "v38", "v39", "v40", "v41", "v42", "v43", "v44", "v45", "v46", "v47", "v48", "v49", "v50", "v51", "v52", "v53", "v54", "v55", "v56", "v57", "v58", "v59", "v60", "v61", "v62", "v63", "v64", "v65", "v66", "v67", "v68", "v69", "v70", "v71", "v72", "v73", "v74", "v75", "v76", "v77", "v78", "v79", "v80", "v81", "v82", "v83", "v84", "v85", "v86", "v87", "v88", "v89", "v90", "v91", "v92", "v93", "v94", "v95", "v96", "v97", "v98", "v99", "v100", "v101", "v102", "v103", "v104", "v105", "v106", "v107", "v108", "v109", "v110", "v111", "v112", "v113", "v114", "v115", "v116", "v117", "v118", "v119", "v120", "v121", "v122", "v123", "v124", "v125", "v126", "v127", "v128", "v129", "v130", "v131", "v132", "v133", "v134", "v135", "v136", "v137", "v138", "v139", "v140", "v141", "v142", "v143", "v144", "v145", "v146", "v147", "v148", "v149", "v150", "v151", "v152", "v153", "v154", "v155", "v156", "v157", "v158", "v159", "v160", "v161", "v162", "v163", "v164", "v165", "v166", "v167", "v168", "v169", "v170", "v171", "v172", "v173", "v174", "v175", "v176", "v177", "v178", "v179", "v180", "v181", "v182", "v183", "v184", "v185", "v186", "v187", "v188", "v189", "v190", "v191", "v192", "v193", "v194", "v195", "v196", "v197", "v198", "v199", "v200", "v201", "v202", "v203", "v204", "v205", "v206", "v207", "v208", "v209", "v210", "v211", "v212", "v213", "v214", "v215", "v216", "v217", "v218", "v219", "v220", "v221", "v222", "v223", "v224", "v225", "v226", "v227", "v228", "v229", "v230", "v231", "v255", "a0", "a1", "a2", "a3", "a4", "a5", "a6", "a7", "a8", "a9", "a10", "a11", "a12", "a13", "a14", "a15", "a16", "a17", "a18", "a19", "a20", "a21", "a22", "a23", "a24", "a25", "a26", "a27", "a28", "a29", "a30", "a31", "a32", "a33", "a34", "a35", "a36", "a37", "a38", "a39", "a40", "a41", "a42", "a43", "a44", "a45", "a46", "a47", "a48", "a49", "a50", "a51", "a52", "a53", "a54", "a55", "a56", "a57", "a58", "a59", "a60", "a61", "a62", "a63", "a64", "a65", "a66", "a67", "a68", "a69", "a70", "a71", "a72", "a73", "a74", "a75", "a76", "a77", "a78", "a79", "a80", "a81", "a82", "a83", "a84", "a85", "a86", "a87", "a88", "a89", "a90", "a91", "a92", "a93", "a94", "a95", "a96", "a97", "a98", "a99", "a100", "a101", "a102", "a103", "a104", "a105", "a106", "a107", "a108", "a109", "a110", "a111", "a112", "a113", "a114", "a115", "a116", "a117", "a118", "a119", "a120", "a121", "a122", "a123", "a124", "a125", "a126", "a127", "a128", "a129", "a130", "a131", "a132", "a133", "a134", "a135", "a136", "a137", "a138", "a139", "a140", "a141", "a142", "a143", "a144", "a145", "a146", "a147", "a148", "a149", "a150", "a151", "a152", "a153", "a154", "a155", "a156", "a157", "a158", "a159", "a160", "a161", "a162", "a163", "a164", "a165", "a166", "a167", "a168", "a169", "a170", "a171", "a172", "a173", "a174", "a175", "a176", "a177", "a178", "a179", "a180", "a181", "a182", "a183", "a184", "a185", "a186", "a187", "a188", "a189", "a190", "a191", "a192", "a193", "a194", "a195", "a196", "a197", "a198", "a199", "a200", "a201", "a202", "a203", "a204", "a205", "a206", "a207", "a208", "a209", "a210", "a211", "a212", "a213", "a214", "a215", "a216", "a217", "a218", "a219", "a220", "a221", "a222", "a223", "a224", "a225", "a226", "a227", "a228", "a229", "a230", "a231", "a232", "a233", "a234", "a235", "a236", "a237", "a238", "a239", "a240", "a241", "a242", "a243", "a244", "a245", "a246", "a247", "a248", "a249", "a250", "a251", "a252", "a253", "a254", "a255", "s36", "s37", "s38", "s39", "s40", "s41", "s42", "s43", "s44", "s45", "s46", "s47", "s48", "s49", "s50", "s51", "s52", "s53", "s54", "s55", "s56", "s57", "s58", "s59", "s60", "s61", "s62", "s63", "s64", "s65", "s66", "s67", "s68", "s69", "s70", "s71", "s72", "s73", "s74", "s75", "s76", "s77", "s78", "s79", "s80", "s81", "s82", "s83", "s84", "s85", "s86", "s87", "s88", "s89", "s90", "s91", "s92", "s93", "s94", "s95", "s96", "s97", "s98", "s99", "vcc", "scc", "memory");
      if (N1 && slice >= 2) { kind = 4; rep = 5; kh = (slice - 2) >> 2; slice = (slice - 2) & 3; }
    }
  if (kind == 3 || kind == 5) {
    KARG(ws, 10) KARG(w3, 8)
    COMMON(8, OFF_FX1, OFF_FG + slice * 128, AUX + 1536, 2048, 7168, kind == 3 ? 5 : 4);
    const unsigned vSrc = OFF_X1 + wave * 4096 + vA, vOut = OFF_G + (slice * 6 + wave) * 1024 + vA;
    const unsigned vOut2 = OFF_G + (slice * 6 + 4 + (wave >> 1)) * 1024 + (((wave & 1) * 2 + (lg >> 1)) * 16 + l15) * 16 + (lg & 1) * 8;
    const unsigned vAuxS = OFF_ST + (ln & 7) * 128 + (wave * 8 + (ln >> 3)) * 4, vX = AUX + wave * 2048 + ln * 4;
    const unsigned vStW = (ln & 7) == 0 ? AUX + 1280 + (wave * 8 + (ln >> 3)) * 4 : AUX + 24576 + wave * 2048 + 1536 + ln * 4, vStR = AUX + 1280 + l15 * 8;
    const unsigned sWw = 16 * wave * 7168 + slice * 384;
    if (kind == 3) { asm volatile(R"(
Lcode_g3_b:
s_mov_b64 s[36:37],%0
s_mov_b32 s38,122727552
s_mov_b32 s39,0x00020000
s_mov_b32 s54,%1
s_mov_b32 s55,%2
s_mov_b32 s56,%3
s_mov_b32 s58,%4
s_mov_b32 s83,%5
s_mov_b32 s82,%6
s_mov_b32 s84,%7
v_mov_b32 v0,%8
v_mov_b32 v2,%9
v_mov_b32 v3,%10
v_mov_b32 v4,%11
v_mov_b32 v6,%12
v_mov_b32 v7,%13
v_mov_b32 v8,%14
v_mov_b32 v9,%15
v_mov_b32 v10,%16
v_mov_b32 v11,%17
v_mov_b32 v28,%18
v_mov_b32 v29,%19
v_mov_b32 v30,%20
v_mov_b32 v31,%21
v_mov_b32 v32,%22
s_mov_b64 s[40:41],%23
s_mov_b32 s42,7340032
s_mov_b32 s43,0x00020000
s_lshl_b32 s88,s54,11
s_mov_b32 s69,122727424
buffer_load_dword v5,off,s[36:39],s69 sc1
s_add_u32 m0,s88,0
s_add_u32 s61,s82,0
buffer_load_dwordx4 v28,s[40:43],s61 offen offset:0  lds
s_add_u32 m0,s88,1024
s_nop 0
buffer_load_dwordx4 v29,s[40:43],s61 offen offset:0  lds
s_add_u32 m0,s88,8064
s_nop 0
buffer_load_dwordx4 v28,s[40:43],s61 offen offset:128  lds
s_add_u32 m0,s88,9088
s_nop 0
buffer_load_dwordx4 v29,s[40:43],s61 offen offset:128  lds
s_add_u32 m0,s88,16128
s_nop 0
buffer_load_dwordx4 v28,s[40:43],s61 offen offset:256  lds
s_add_u32 m0,s88,17152
s_nop 0
buffer_load_dwordx4 v29,s[40:43],s61 offen offset:256  lds
s_add_u32 m0,s88,24576
s_add_u32 s61,s82,458752
buffer_load_dwordx4 v28,s[40:43],s61 offen offset:0  lds
s_add_u32 m0,s88,25600
s_nop 0
buffer_load_dwordx4 v29,s[40:43],s61 offen offset:0  lds
s_add_u32 m0,s88,32640
s_nop 0
buffer_load_dwordx4 v28,s[40:43],s61 offen offset:128  lds
s_add_u32 m0,s88,33664
s_nop 0
buffer_load_dwordx4 v29,s[40:43],s61 offen offset:128  lds
s_add_u32 m0,s88,40704
s_nop 0
buffer_load_dwordx4 v28,s[40:43],s61 offen offset:256  lds
s_add_u32 m0,s88,41728
s_nop 0
buffer_load_dwordx4 v29,s[40:43],s61 offen offset:256  lds
s_add_u32 m0,s88,49152
s_add_u32 s61,s82,917504
buffer_load_dwordx4 v28,s[40:43],s61 offen offset:0  lds
s_add_u32 m0,s88,50176
s_nop 0
buffer_load_dwordx4 v29,s[40:43],s61 offen offset:0  lds
s_add_u32 m0,s88,57216
s_nop 0
buffer_load_dwordx4 v28,s[40:43],s61 offen offset:128  lds
s_add_u32 m0,s88,58240
s_nop 0
buffer_load_dwordx4 v29,s[40:43],s61 offen offset:128  lds
s_add_u32 m0,s88,65280
s_nop 0
buffer_load_dwordx4 v28,s[40:43],s61 offen offset:256  lds
s_add_u32 m0,s88,66304
s_nop 0
buffer_load_dwordx4 v29,s[40:43],s61 offen offset:256  lds
s_add_u32 m0,s88,73728
s_add_u32 s61,s82,1376256
buffer_load_dwordx4 v28,s[40:43],s61 offen offset:0  lds
s_add_u32 m0,s88,74752
s_nop 0
buffer_load_dwordx4 v29,s[40:43],s61 offen offset:0  lds
s_add_u32 m0,s88,81792
s_nop 0
buffer_load_dwordx4 v28,s[40:43],s61 offen offset:128  lds
s_add_u32 m0,s88,82816
s_nop 0
buffer_load_dwordx4 v29,s[40:43],s61 offen offset:128  lds
s_add_u32 m0,s88,89856
s_nop 0
buffer_load_dwordx4 v28,s[40:43],s61 offen offset:256  lds
s_add_u32 m0,s88,90880
s_nop 0
buffer_load_dwordx4 v29,s[40:43],s61 offen offset:256  lds
v_add_u32 v40,49152,v30
v_add_u32 v41,49152,v31
v_add_u32 v42,49152,v32
v_add_u32 v43,98304,v30
v_add_u32 v44,98304,v31
v_add_u32 v45,98304,v32
v_mov_b32 v84,0x3f803f80
v_mov_b32 v85,0x3f803f80
v_mov_b32 v86,0x3f803f80
v_mov_b32 v87,0x3f803f80
s_waitcnt vmcnt(18)
s_barrier
s_add_u32 m0,s88,98304
s_add_u32 s61,s82,1835008
buffer_load_dwordx4 v28,s[40:43],s61 offen offset:0  lds
s_add_u32 m0,s88,99328
ds_read_b64_tr_b16 a[0:1],v30 offset:0
buffer_load_dwordx4 v29,s[40:43],s61 offen offset:0  lds
s_add_u32 m0,s88,106368
ds_read_b64_tr_b16 a[2:3],v30 offset:512
buffer_load_dwordx4 v28,s[40:43],s61 offen offset:128  lds
s_add_u32 m0,s88,107392
ds_read_b64_tr_b16 a[4:5],v31 offset:0
buffer_load_dwordx4 v29,s[40:43],s61 offen offset:128  lds
s_add_u32 m0,s88,114432
ds_read_b64_tr_b16 a[6:7],v31 offset:512
buffer_load_dwordx4 v28,s[40:43],s61 offen offset:256  lds
s_add_u32 m0,s88,115456
ds_read_b64_tr_b16 a[8:9],v32 offset:0
buffer_load_dwordx4 v29,s[40:43],s61 offen offset:256  lds
ds_read_b64_tr_b16 a[10:11],v32 offset:512
ds_read_b64_tr_b16 a[12:13],v30 offset:4096
ds_read_b64_tr_b16 a[14:15],v30 offset:4608
ds_read_b64_tr_b16 a[16:17],v31 offset:4096
ds_read_b64_tr_b16 a[18:19],v31 offset:4608
ds_read_b64_tr_b16 a[20:21],v32 offset:4096
ds_read_b64_tr_b16 a[22:23],v32 offset:4608
s_waitcnt vmcnt(18)
s_barrier
s_add_u32 m0,s88,122880
s_add_u32 s61,s82,2293760
buffer_load_dwordx4 v28,s[40:43],s61 offen offset:0  lds
s_add_u32 m0,s88,123904
ds_read_b64_tr_b16 a[24:25],v30 offset:24576
buffer_load_dwordx4 v29,s[40:43],s61 offen offset:0  lds
s_add_u32 m0,s88,130944
ds_read_b64_tr_b16 a[26:27],v30 offset:25088
buffer_load_dwordx4 v28,s[40:43],s61 offen offset:128  lds
s_add_u32 m0,s88,131968
ds_read_b64_tr_b16 a[28:29],v31 offset:24576
buffer_load_dwordx4 v29,s[40:43],s61 offen offset:128  lds
s_add_u32 m0,s88,139008
ds_read_b64_tr_b16 a[30:31],v31 offset:25088
buffer_load_dwordx4 v28,s[40:43],s61 offen offset:256  lds
s_add_u32 m0,s88,140032
ds_read_b64_tr_b16 a[32:33],v32 offset:24576
buffer_load_dwordx4 v29,s[40:43],s61 offen offset:256  lds
ds_read_b64_tr_b16 a[34:35],v32 offset:25088
ds_read_b64_tr_b16 a[36:37],v30 offset:28672
ds_read_b64_tr_b16 a[38:39],v30 offset:29184
ds_read_b64_tr_b16 a[40:41],v31 offset:28672
ds_read_b64_tr_b16 a[42:43],v31 offset:29184
ds_read_b64_tr_b16 a[44:45],v32 offset:28672
ds_read_b64_tr_b16 a[46:47],v32 offset:29184
s_waitcnt lgkmcnt(12)
v_mfma_f32_16x16x32_bf16 v[16:19],a[0:3],v[84:87],0
v_mfma_f32_16x16x32_bf16 v[20:23],a[4:7],v[84:87],0
v_mfma_f32_16x16x32_bf16 v[24:27],a[8:11],v[84:87],0
v_mfma_f32_16x16x32_bf16 v[16:19],a[12:15],v[84:87],v[16:19]
v_mfma_f32_16x16x32_bf16 v[20:23],a[16:19],v[84:87],v[20:23]
v_mfma_f32_16x16x32_bf16 v[24:27],a[20:23],v[84:87],v[24:27]
s_waitcnt vmcnt(18)
s_barrier
s_add_u32 m0,s88,0
s_add_u32 s61,s82,2752512
buffer_load_dwordx4 v28,s[40:43],s61 offen offset:0  lds
s_add_u32 m0,s88,1024
ds_read_b64_tr_b16 a[48:49],v40 offset:0
buffer_load_dwordx4 v29,s[40:43],s61 offen offset:0  lds
s_add_u32 m0,s88,8064
ds_read_b64_tr_b16 a[50:51],v40 offset:512
buffer_load_dwordx4 v28,s[40:43],s61 offen offset:128  lds
s_add_u32 m0,s88,9088
ds_read_b64_tr_b16 a[52:53],v41 offset:0
buffer_load_dwordx4 v29,s[40:43],s61 offen offset:128  lds
s_add_u32 m0,s88,16128
ds_read_b64_tr_b16 a[54:55],v41 offset:512
buffer_load_dwordx4 v28,s[40:43],s61 offen offset:256  lds
s_add_u32 m0,s88,17152
ds_read_b64_tr_b16 a[56:57],v42 offset:0
buffer_load_dwordx4 v29,s[40:43],s61 offen offset:256  lds
ds_read_b64_tr_b16 a[58:59],v42 offset:512
ds_read_b64_tr_b16 a[60:61],v40 offset:4096
ds_read_b64_tr_b16 a[62:63],v40 offset:4608
ds_read_b64_tr_b16 a[64:65],v41 offset:4096
ds_read_b64_tr_b16 a[66:67],v41 offset:4608)"
R"(
ds_read_b64_tr_b16 a[68:69],v42 offset:4096
ds_read_b64_tr_b16 a[70:71],v42 offset:4608
s_waitcnt lgkmcnt(12)
v_mfma_f32_16x16x32_bf16 v[16:19],a[24:27],v[84:87],v[16:19]
v_mfma_f32_16x16x32_bf16 v[20:23],a[28:31],v[84:87],v[20:23]
v_mfma_f32_16x16x32_bf16 v[24:27],a[32:35],v[84:87],v[24:27]
v_mfma_f32_16x16x32_bf16 v[16:19],a[36:39],v[84:87],v[16:19]
v_mfma_f32_16x16x32_bf16 v[20:23],a[40:43],v[84:87],v[20:23]
v_mfma_f32_16x16x32_bf16 v[24:27],a[44:47],v[84:87],v[24:27]
s_waitcnt vmcnt(18)
s_barrier
s_add_u32 m0,s88,24576
s_add_u32 s61,s82,3211264
buffer_load_dwordx4 v28,s[40:43],s61 offen offset:0  lds
s_add_u32 m0,s88,25600
ds_read_b64_tr_b16 a[72:73],v40 offset:24576
buffer_load_dwordx4 v29,s[40:43],s61 offen offset:0  lds
s_add_u32 m0,s88,32640
ds_read_b64_tr_b16 a[74:75],v40 offset:25088
buffer_load_dwordx4 v28,s[40:43],s61 offen offset:128  lds
s_add_u32 m0,s88,33664
ds_read_b64_tr_b16 a[76:77],v41 offset:24576
buffer_load_dwordx4 v29,s[40:43],s61 offen offset:128  lds
s_add_u32 m0,s88,40704
ds_read_b64_tr_b16 a[78:79],v41 offset:25088
buffer_load_dwordx4 v28,s[40:43],s61 offen offset:256  lds
s_add_u32 m0,s88,41728
ds_read_b64_tr_b16 a[80:81],v42 offset:24576
buffer_load_dwordx4 v29,s[40:43],s61 offen offset:256  lds
ds_read_b64_tr_b16 a[82:83],v42 offset:25088
ds_read_b64_tr_b16 a[84:85],v40 offset:28672
ds_read_b64_tr_b16 a[86:87],v40 offset:29184
ds_read_b64_tr_b16 a[88:89],v41 offset:28672
ds_read_b64_tr_b16 a[90:91],v41 offset:29184
ds_read_b64_tr_b16 a[92:93],v42 offset:28672
ds_read_b64_tr_b16 a[94:95],v42 offset:29184
s_waitcnt lgkmcnt(12)
v_mfma_f32_16x16x32_bf16 v[16:19],a[48:51],v[84:87],v[16:19]
v_mfma_f32_16x16x32_bf16 v[20:23],a[52:55],v[84:87],v[20:23]
v_mfma_f32_16x16x32_bf16 v[24:27],a[56:59],v[84:87],v[24:27]
v_mfma_f32_16x16x32_bf16 v[16:19],a[60:63],v[84:87],v[16:19]
v_mfma_f32_16x16x32_bf16 v[20:23],a[64:67],v[84:87],v[20:23]
v_mfma_f32_16x16x32_bf16 v[24:27],a[68:71],v[84:87],v[24:27]
s_waitcnt vmcnt(18)
s_barrier
s_add_u32 m0,s88,49152
s_add_u32 s61,s82,3670016
buffer_load_dwordx4 v28,s[40:43],s61 offen offset:0  lds
s_add_u32 m0,s88,50176
ds_read_b64_tr_b16 a[96:97],v43 offset:0
buffer_load_dwordx4 v29,s[40:43],s61 offen offset:0  lds
s_add_u32 m0,s88,57216
ds_read_b64_tr_b16 a[98:99],v43 offset:512
buffer_load_dwordx4 v28,s[40:43],s61 offen offset:128  lds
s_add_u32 m0,s88,58240
ds_read_b64_tr_b16 a[100:101],v44 offset:0
buffer_load_dwordx4 v29,s[40:43],s61 offen offset:128  lds
s_add_u32 m0,s88,65280
ds_read_b64_tr_b16 a[102:103],v44 offset:512
buffer_load_dwordx4 v28,s[40:43],s61 offen offset:256  lds
s_add_u32 m0,s88,66304
ds_read_b64_tr_b16 a[104:105],v45 offset:0
buffer_load_dwordx4 v29,s[40:43],s61 offen offset:256  lds
ds_read_b64_tr_b16 a[106:107],v45 offset:512
ds_read_b64_tr_b16 a[108:109],v43 offset:4096
ds_read_b64_tr_b16 a[110:111],v43 offset:4608
ds_read_b64_tr_b16 a[112:113],v44 offset:4096
ds_read_b64_tr_b16 a[114:115],v44 offset:4608
ds_read_b64_tr_b16 a[116:117],v45 offset:4096
ds_read_b64_tr_b16 a[118:119],v45 offset:4608
s_waitcnt lgkmcnt(12)
v_mfma_f32_16x16x32_bf16 v[16:19],a[72:75],v[84:87],v[16:19]
v_mfma_f32_16x16x32_bf16 v[20:23],a[76:79],v[84:87],v[20:23]
v_mfma_f32_16x16x32_bf16 v[24:27],a[80:83],v[84:87],v[24:27]
v_mfma_f32_16x16x32_bf16 v[16:19],a[84:87],v[84:87],v[16:19]
v_mfma_f32_16x16x32_bf16 v[20:23],a[88:91],v[84:87],v[20:23]
v_mfma_f32_16x16x32_bf16 v[24:27],a[92:95],v[84:87],v[24:27]
s_waitcnt vmcnt(18)
s_barrier
s_add_u32 m0,s88,73728
s_add_u32 s61,s82,4128768
buffer_load_dwordx4 v28,s[40:43],s61 offen offset:0  lds
s_add_u32 m0,s88,74752
ds_read_b64_tr_b16 a[120:121],v43 offset:24576
buffer_load_dwordx4 v29,s[40:43],s61 offen offset:0  lds
s_add_u32 m0,s88,81792
ds_read_b64_tr_b16 a[122:123],v43 offset:25088
buffer_load_dwordx4 v28,s[40:43],s61 offen offset:128  lds
s_add_u32 m0,s88,82816
ds_read_b64_tr_b16 a[124:125],v44 offset:24576
buffer_load_dwordx4 v29,s[40:43],s61 offen offset:128  lds
s_add_u32 m0,s88,89856
ds_read_b64_tr_b16 a[126:127],v44 offset:25088
buffer_load_dwordx4 v28,s[40:43],s61 offen offset:256  lds
s_add_u32 m0,s88,90880
ds_read_b64_tr_b16 a[128:129],v45 offset:24576
buffer_load_dwordx4 v29,s[40:43],s61 offen offset:256  lds
ds_read_b64_tr_b16 a[130:131],v45 offset:25088
ds_read_b64_tr_b16 a[132:133],v43 offset:28672
ds_read_b64_tr_b16 a[134:135],v43 offset:29184
ds_read_b64_tr_b16 a[136:137],v44 offset:28672
ds_read_b64_tr_b16 a[138:139],v44 offset:29184
ds_read_b64_tr_b16 a[140:141],v45 offset:28672
ds_read_b64_tr_b16 a[142:143],v45 offset:29184
s_waitcnt lgkmcnt(12)
v_mfma_f32_16x16x32_bf16 v[16:19],a[96:99],v[84:87],v[16:19]
v_mfma_f32_16x16x32_bf16 v[20:23],a[100:103],v[84:87],v[20:23]
v_mfma_f32_16x16x32_bf16 v[24:27],a[104:107],v[84:87],v[24:27]
v_mfma_f32_16x16x32_bf16 v[16:19],a[108:111],v[84:87],v[16:19]
v_mfma_f32_16x16x32_bf16 v[20:23],a[112:115],v[84:87],v[20:23]
v_mfma_f32_16x16x32_bf16 v[24:27],a[116:119],v[84:87],v[24:27]
s_waitcnt vmcnt(18)
s_barrier
s_add_u32 m0,s88,98304
s_add_u32 s61,s82,4587520
buffer_load_dwordx4 v28,s[40:43],s61 offen offset:0  lds
s_add_u32 m0,s88,99328
ds_read_b64_tr_b16 a[144:145],v30 offset:0
buffer_load_dwordx4 v29,s[40:43],s61 offen offset:0  lds
s_add_u32 m0,s88,106368
ds_read_b64_tr_b16 a[146:147],v30 offset:512
buffer_load_dwordx4 v28,s[40:43],s61 offen offset:128  lds
s_add_u32 m0,s88,107392
ds_read_b64_tr_b16 a[148:149],v31 offset:0
buffer_load_dwordx4 v29,s[40:43],s61 offen offset:128  lds
s_add_u32 m0,s88,114432
ds_read_b64_tr_b16 a[150:151],v31 offset:512
buffer_load_dwordx4 v28,s[40:43],s61 offen offset:256  lds
s_add_u32 m0,s88,115456
ds_read_b64_tr_b16 a[152:153],v32 offset:0
buffer_load_dwordx4 v29,s[40:43],s61 offen offset:256  lds
ds_read_b64_tr_b16 a[154:155],v32 offset:512
ds_read_b64_tr_b16 a[156:157],v30 offset:4096
ds_read_b64_tr_b16 a[158:159],v30 offset:4608
ds_read_b64_tr_b16 a[160:161],v31 offset:4096
ds_read_b64_tr_b16 a[162:163],v31 offset:4608
ds_read_b64_tr_b16 a[164:165],v32 offset:4096
ds_read_b64_tr_b16 a[166:167],v32 offset:4608
s_waitcnt lgkmcnt(12)
v_mfma_f32_16x16x32_bf16 v[16:19],a[120:123],v[84:87],v[16:19]
v_mfma_f32_16x16x32_bf16 v[20:23],a[124:127],v[84:87],v[20:23]
v_mfma_f32_16x16x32_bf16 v[24:27],a[128:131],v[84:87],v[24:27]
v_mfma_f32_16x16x32_bf16 v[16:19],a[132:135],v[84:87],v[16:19]
v_mfma_f32_16x16x32_bf16 v[20:23],a[136:139],v[84:87],v[20:23]
v_mfma_f32_16x16x32_bf16 v[24:27],a[140:143],v[84:87],v[24:27]
s_waitcnt vmcnt(18)
s_barrier
s_add_u32 m0,s88,122880
s_add_u32 s61,s82,5046272
buffer_load_dwordx4 v28,s[40:43],s61 offen offset:0  lds
s_add_u32 m0,s88,123904
ds_read_b64_tr_b16 a[168:169],v30 offset:24576
buffer_load_dwordx4 v29,s[40:43],s61 offen offset:0  lds
s_add_u32 m0,s88,130944
ds_read_b64_tr_b16 a[170:171],v30 offset:25088
buffer_load_dwordx4 v28,s[40:43],s61 offen offset:128  lds
s_add_u32 m0,s88,131968
ds_read_b64_tr_b16 a[172:173],v31 offset:24576
buffer_load_dwordx4 v29,s[40:43],s61 offen offset:128  lds
s_add_u32 m0,s88,139008
ds_read_b64_tr_b16 a[174:175],v31 offset:25088
buffer_load_dwordx4 v28,s[40:43],s61 offen offset:256  lds
s_add_u32 m0,s88,140032
ds_read_b64_tr_b16 a[176:177],v32 offset:24576
buffer_load_dwordx4 v29,s[40:43],s61 offen offset:256  lds
ds_read_b64_tr_b16 a[178:179],v32 offset:25088
ds_read_b64_tr_b16 a[180:181],v30 offset:28672
ds_read_b64_tr_b16 a[182:183],v30 offset:29184
ds_read_b64_tr_b16 a[184:185],v31 offset:28672
ds_read_b64_tr_b16 a[186:187],v31 offset:29184
ds_read_b64_tr_b16 a[188:189],v32 offset:28672
ds_read_b64_tr_b16 a[190:191],v32 offset:29184
s_waitcnt lgkmcnt(12)
v_mfma_f32_16x16x32_bf16 v[16:19],a[144:147],v[84:87],v[16:19]
v_mfma_f32_16x16x32_bf16 v[20:23],a[148:151],v[84:87],v[20:23]
v_mfma_f32_16x16x32_bf16 v[24:27],a[152:155],v[84:87],v[24:27]
v_mfma_f32_16x16x32_bf16 v[16:19],a[156:159],v[84:87],v[16:19]
v_mfma_f32_16x16x32_bf16 v[20:23],a[160:163],v[84:87],v[20:23]
v_mfma_f32_16x16x32_bf16 v[24:27],a[164:167],v[84:87],v[24:27]
s_waitcnt vmcnt(18)
s_barrier
s_add_u32 m0,s88,0
s_add_u32 s61,s82,5505024
buffer_load_dwordx4 v28,s[40:43],s61 offen offset:0  lds
s_add_u32 m0,s88,1024
ds_read_b64_tr_b16 a[192:193],v40 offset:0
buffer_load_dwordx4 v29,s[40:43],s61 offen offset:0  lds
s_add_u32 m0,s88,8064
ds_read_b64_tr_b16 a[194:195],v40 offset:512
buffer_load_dwordx4 v28,s[40:43],s61 offen offset:128  lds
s_add_u32 m0,s88,9088
ds_read_b64_tr_b16 a[196:197],v41 offset:0
buffer_load_dwordx4 v29,s[40:43],s61 offen offset:128  lds
s_add_u32 m0,s88,16128
ds_read_b64_tr_b16 a[198:199],v41 offset:512
buffer_load_dwordx4 v28,s[40:43],s61 offen offset:256  lds
s_add_u32 m0,s88,17152
ds_read_b64_tr_b16 a[200:201],v42 offset:0
buffer_load_dwordx4 v29,s[40:43],s61 offen offset:256  lds
ds_read_b64_tr_b16 a[202:203],v42 offset:512)"
R"(
ds_read_b64_tr_b16 a[204:205],v40 offset:4096
ds_read_b64_tr_b16 a[206:207],v40 offset:4608
ds_read_b64_tr_b16 a[208:209],v41 offset:4096
ds_read_b64_tr_b16 a[210:211],v41 offset:4608
ds_read_b64_tr_b16 a[212:213],v42 offset:4096
ds_read_b64_tr_b16 a[214:215],v42 offset:4608
s_waitcnt lgkmcnt(12)
v_mfma_f32_16x16x32_bf16 v[16:19],a[168:171],v[84:87],v[16:19]
v_mfma_f32_16x16x32_bf16 v[20:23],a[172:175],v[84:87],v[20:23]
v_mfma_f32_16x16x32_bf16 v[24:27],a[176:179],v[84:87],v[24:27]
v_mfma_f32_16x16x32_bf16 v[16:19],a[180:183],v[84:87],v[16:19]
v_mfma_f32_16x16x32_bf16 v[20:23],a[184:187],v[84:87],v[20:23]
v_mfma_f32_16x16x32_bf16 v[24:27],a[188:191],v[84:87],v[24:27]
s_waitcnt vmcnt(18)
s_barrier
s_add_u32 m0,s88,24576
s_add_u32 s61,s82,5963776
buffer_load_dwordx4 v28,s[40:43],s61 offen offset:0  lds
s_add_u32 m0,s88,25600
ds_read_b64_tr_b16 a[216:217],v40 offset:24576
buffer_load_dwordx4 v29,s[40:43],s61 offen offset:0  lds
s_add_u32 m0,s88,32640
ds_read_b64_tr_b16 a[218:219],v40 offset:25088
buffer_load_dwordx4 v28,s[40:43],s61 offen offset:128  lds
s_add_u32 m0,s88,33664
ds_read_b64_tr_b16 a[220:221],v41 offset:24576
buffer_load_dwordx4 v29,s[40:43],s61 offen offset:128  lds
s_add_u32 m0,s88,40704
ds_read_b64_tr_b16 a[222:223],v41 offset:25088
buffer_load_dwordx4 v28,s[40:43],s61 offen offset:256  lds
s_add_u32 m0,s88,41728
ds_read_b64_tr_b16 a[224:225],v42 offset:24576
buffer_load_dwordx4 v29,s[40:43],s61 offen offset:256  lds
ds_read_b64_tr_b16 a[226:227],v42 offset:25088
ds_read_b64_tr_b16 a[228:229],v40 offset:28672
ds_read_b64_tr_b16 a[230:231],v40 offset:29184
ds_read_b64_tr_b16 a[232:233],v41 offset:28672
ds_read_b64_tr_b16 a[234:235],v41 offset:29184
ds_read_b64_tr_b16 a[236:237],v42 offset:28672
ds_read_b64_tr_b16 a[238:239],v42 offset:29184
s_waitcnt lgkmcnt(12)
v_mfma_f32_16x16x32_bf16 v[16:19],a[192:195],v[84:87],v[16:19]
v_mfma_f32_16x16x32_bf16 v[20:23],a[196:199],v[84:87],v[20:23]
v_mfma_f32_16x16x32_bf16 v[24:27],a[200:203],v[84:87],v[24:27]
v_mfma_f32_16x16x32_bf16 v[16:19],a[204:207],v[84:87],v[16:19]
v_mfma_f32_16x16x32_bf16 v[20:23],a[208:211],v[84:87],v[20:23]
v_mfma_f32_16x16x32_bf16 v[24:27],a[212:215],v[84:87],v[24:27]
s_waitcnt vmcnt(18)
s_barrier
s_add_u32 m0,s88,49152
s_add_u32 s61,s82,6422528
buffer_load_dwordx4 v28,s[40:43],s61 offen offset:0  lds
s_add_u32 m0,s88,50176
ds_read_b64_tr_b16 a[240:241],v43 offset:0
buffer_load_dwordx4 v29,s[40:43],s61 offen offset:0  lds
s_add_u32 m0,s88,57216
ds_read_b64_tr_b16 a[242:243],v43 offset:512
buffer_load_dwordx4 v28,s[40:43],s61 offen offset:128  lds
s_add_u32 m0,s88,58240
ds_read_b64_tr_b16 a[244:245],v44 offset:0
buffer_load_dwordx4 v29,s[40:43],s61 offen offset:128  lds
s_add_u32 m0,s88,65280
ds_read_b64_tr_b16 a[246:247],v44 offset:512
buffer_load_dwordx4 v28,s[40:43],s61 offen offset:256  lds
s_add_u32 m0,s88,66304
ds_read_b64_tr_b16 a[248:249],v45 offset:0
buffer_load_dwordx4 v29,s[40:43],s61 offen offset:256  lds
ds_read_b64_tr_b16 a[250:251],v45 offset:512
ds_read_b64_tr_b16 a[252:253],v43 offset:4096
ds_read_b64_tr_b16 a[254:255],v43 offset:4608
ds_read_b64_tr_b16 v[128:129],v44 offset:4096
ds_read_b64_tr_b16 v[130:131],v44 offset:4608
ds_read_b64_tr_b16 v[132:133],v45 offset:4096
ds_read_b64_tr_b16 v[134:135],v45 offset:4608
s_waitcnt lgkmcnt(12)
v_mfma_f32_16x16x32_bf16 v[16:19],a[216:219],v[84:87],v[16:19]
v_mfma_f32_16x16x32_bf16 v[20:23],a[220:223],v[84:87],v[20:23]
v_mfma_f32_16x16x32_bf16 v[24:27],a[224:227],v[84:87],v[24:27]
v_mfma_f32_16x16x32_bf16 v[16:19],a[228:231],v[84:87],v[16:19]
v_mfma_f32_16x16x32_bf16 v[20:23],a[232:235],v[84:87],v[20:23]
v_mfma_f32_16x16x32_bf16 v[24:27],a[236:239],v[84:87],v[24:27]
s_waitcnt vmcnt(18)
s_barrier
s_add_u32 m0,s88,73728
s_add_u32 s61,s82,6881280
buffer_load_dwordx4 v28,s[40:43],s61 offen offset:0  lds
s_add_u32 m0,s88,74752
ds_read_b64_tr_b16 v[136:137],v43 offset:24576
buffer_load_dwordx4 v29,s[40:43],s61 offen offset:0  lds
s_add_u32 m0,s88,81792
ds_read_b64_tr_b16 v[138:139],v43 offset:25088
buffer_load_dwordx4 v28,s[40:43],s61 offen offset:128  lds
s_add_u32 m0,s88,82816
ds_read_b64_tr_b16 v[140:141],v44 offset:24576
buffer_load_dwordx4 v29,s[40:43],s61 offen offset:128  lds
s_add_u32 m0,s88,89856
ds_read_b64_tr_b16 v[142:143],v44 offset:25088
buffer_load_dwordx4 v28,s[40:43],s61 offen offset:256  lds
s_add_u32 m0,s88,90880
ds_read_b64_tr_b16 v[144:145],v45 offset:24576
buffer_load_dwordx4 v29,s[40:43],s61 offen offset:256  lds
ds_read_b64_tr_b16 v[146:147],v45 offset:25088
ds_read_b64_tr_b16 v[148:149],v43 offset:28672
ds_read_b64_tr_b16 v[150:151],v43 offset:29184
ds_read_b64_tr_b16 v[152:153],v44 offset:28672
ds_read_b64_tr_b16 v[154:155],v44 offset:29184
ds_read_b64_tr_b16 v[156:157],v45 offset:28672
ds_read_b64_tr_b16 v[158:159],v45 offset:29184
s_getpc_b64 s[46:47]
Lg3_cp_%=:
s_add_u32 s46,s46,Lg3_init_%=-Lg3_cp_%=
s_addc_u32 s47,s47,0
s_lshl_b32 s68,s54,13
v_lshl_add_u32 v68,v2,3,s68
v_min_u32 v69,Lg3_done_%=-Lg3_init_%=,v68
global_load_dword v72,v69,s[46:47]
s_waitcnt lgkmcnt(12)
v_mfma_f32_16x16x32_bf16 v[16:19],a[240:243],v[84:87],v[16:19]
v_mfma_f32_16x16x32_bf16 v[20:23],a[244:247],v[84:87],v[20:23]
v_mfma_f32_16x16x32_bf16 v[24:27],a[248:251],v[84:87],v[24:27]
v_mfma_f32_16x16x32_bf16 v[16:19],a[252:255],v[84:87],v[16:19]
v_mfma_f32_16x16x32_bf16 v[20:23],v[128:131],v[84:87],v[20:23]
v_mfma_f32_16x16x32_bf16 v[24:27],v[132:135],v[84:87],v[24:27]
s_waitcnt vmcnt(19)
s_barrier
ds_read_b64_tr_b16 v[160:161],v30 offset:0
ds_read_b64_tr_b16 v[162:163],v30 offset:512
ds_read_b64_tr_b16 v[164:165],v31 offset:0
ds_read_b64_tr_b16 v[166:167],v31 offset:512
ds_read_b64_tr_b16 v[168:169],v32 offset:0
ds_read_b64_tr_b16 v[170:171],v32 offset:512
ds_read_b64_tr_b16 v[172:173],v30 offset:4096
ds_read_b64_tr_b16 v[174:175],v30 offset:4608
ds_read_b64_tr_b16 v[176:177],v31 offset:4096
ds_read_b64_tr_b16 v[178:179],v31 offset:4608
ds_read_b64_tr_b16 v[180:181],v32 offset:4096
ds_read_b64_tr_b16 v[182:183],v32 offset:4608
s_waitcnt lgkmcnt(12)
v_mfma_f32_16x16x32_bf16 v[16:19],v[136:139],v[84:87],v[16:19]
v_mfma_f32_16x16x32_bf16 v[20:23],v[140:143],v[84:87],v[20:23]
v_mfma_f32_16x16x32_bf16 v[24:27],v[144:147],v[84:87],v[24:27]
v_mfma_f32_16x16x32_bf16 v[16:19],v[148:151],v[84:87],v[16:19]
v_mfma_f32_16x16x32_bf16 v[20:23],v[152:155],v[84:87],v[20:23]
v_mfma_f32_16x16x32_bf16 v[24:27],v[156:159],v[84:87],v[24:27]
s_waitcnt vmcnt(13)
s_barrier
ds_read_b64_tr_b16 v[184:185],v30 offset:24576
ds_read_b64_tr_b16 v[186:187],v30 offset:25088
ds_read_b64_tr_b16 v[188:189],v31 offset:24576
ds_read_b64_tr_b16 v[190:191],v31 offset:25088
ds_read_b64_tr_b16 v[192:193],v32 offset:24576
ds_read_b64_tr_b16 v[194:195],v32 offset:25088
ds_read_b64_tr_b16 v[196:197],v30 offset:28672
ds_read_b64_tr_b16 v[198:199],v30 offset:29184
ds_read_b64_tr_b16 v[200:201],v31 offset:28672
ds_read_b64_tr_b16 v[202:203],v31 offset:29184
ds_read_b64_tr_b16 v[204:205],v32 offset:28672
ds_read_b64_tr_b16 v[206:207],v32 offset:29184
s_waitcnt lgkmcnt(12)
v_mfma_f32_16x16x32_bf16 v[16:19],v[160:163],v[84:87],v[16:19]
v_mfma_f32_16x16x32_bf16 v[20:23],v[164:167],v[84:87],v[20:23]
v_mfma_f32_16x16x32_bf16 v[24:27],v[168:171],v[84:87],v[24:27]
v_mfma_f32_16x16x32_bf16 v[16:19],v[172:175],v[84:87],v[16:19]
v_mfma_f32_16x16x32_bf16 v[20:23],v[176:179],v[84:87],v[20:23]
v_mfma_f32_16x16x32_bf16 v[24:27],v[180:183],v[84:87],v[24:27]
s_waitcnt vmcnt(7)
s_barrier
ds_read_b64_tr_b16 v[208:209],v40 offset:0
ds_read_b64_tr_b16 v[210:211],v40 offset:512
ds_read_b64_tr_b16 v[212:213],v41 offset:0
ds_read_b64_tr_b16 v[214:215],v41 offset:512
ds_read_b64_tr_b16 v[216:217],v42 offset:0
ds_read_b64_tr_b16 v[218:219],v42 offset:512
ds_read_b64_tr_b16 v[220:221],v40 offset:4096
ds_read_b64_tr_b16 v[222:223],v40 offset:4608
ds_read_b64_tr_b16 v[224:225],v41 offset:4096
ds_read_b64_tr_b16 v[226:227],v41 offset:4608
ds_read_b64_tr_b16 v[228:229],v42 offset:4096
ds_read_b64_tr_b16 v[230:231],v42 offset:4608
s_waitcnt lgkmcnt(12)
v_mfma_f32_16x16x32_bf16 v[16:19],v[184:187],v[84:87],v[16:19]
v_mfma_f32_16x16x32_bf16 v[20:23],v[188:191],v[84:87],v[20:23]
v_mfma_f32_16x16x32_bf16 v[24:27],v[192:195],v[84:87],v[24:27]
v_mfma_f32_16x16x32_bf16 v[16:19],v[196:199],v[84:87],v[16:19]
v_mfma_f32_16x16x32_bf16 v[20:23],v[200:203],v[84:87],v[20:23]
v_mfma_f32_16x16x32_bf16 v[24:27],v[204:207],v[84:87],v[24:27]
s_waitcnt vmcnt(1)
s_barrier
ds_read_b64_tr_b16 v[232:233],v40 offset:24576
ds_read_b64_tr_b16 v[234:235],v40 offset:25088
ds_read_b64_tr_b16 v[236:237],v41 offset:24576
ds_read_b64_tr_b16 v[238:239],v41 offset:25088
ds_read_b64_tr_b16 v[240:241],v42 offset:24576
ds_read_b64_tr_b16 v[242:243],v42 offset:25088
ds_read_b64_tr_b16 v[244:245],v40 offset:28672
ds_read_b64_tr_b16 v[246:247],v40 offset:29184
ds_read_b64_tr_b16 v[248:249],v41 offset:28672
ds_read_b64_tr_b16 v[250:251],v41 offset:29184
ds_read_b64_tr_b16 v[252:253],v42 offset:28672
ds_read_b64_tr_b16 v[254:255],v42 offset:29184)"
R"(
s_waitcnt lgkmcnt(12)
v_mfma_f32_16x16x32_bf16 v[16:19],v[208:211],v[84:87],v[16:19]
v_mfma_f32_16x16x32_bf16 v[20:23],v[212:215],v[84:87],v[20:23]
v_mfma_f32_16x16x32_bf16 v[24:27],v[216:219],v[84:87],v[24:27]
v_mfma_f32_16x16x32_bf16 v[16:19],v[220:223],v[84:87],v[16:19]
v_mfma_f32_16x16x32_bf16 v[20:23],v[224:227],v[84:87],v[20:23]
v_mfma_f32_16x16x32_bf16 v[24:27],v[228:231],v[84:87],v[24:27]
s_waitcnt lgkmcnt(0)
v_mfma_f32_16x16x32_bf16 v[16:19],v[232:235],v[84:87],v[16:19]
v_mfma_f32_16x16x32_bf16 v[20:23],v[236:239],v[84:87],v[20:23]
v_mfma_f32_16x16x32_bf16 v[24:27],v[240:243],v[84:87],v[24:27]
v_mfma_f32_16x16x32_bf16 v[16:19],v[244:247],v[84:87],v[16:19]
v_mfma_f32_16x16x32_bf16 v[20:23],v[248:251],v[84:87],v[20:23]
v_mfma_f32_16x16x32_bf16 v[24:27],v[252:255],v[84:87],v[24:27]
s_waitcnt lgkmcnt(0)
s_barrier
v_readfirstlane_b32 s60,v5
s_nop 3
s_add_u32 s60,s60,1
s_cmp_eq_u32 s60,0
s_cselect_b32 s60,1,s60
Lg3_init_%=:
v_mov_b32 v5,s60
s_mov_b32 s59,536870912
s_add_u32 s57,s56,131072
s_mov_b32 s79,0
s_mov_b32 s75,0x3a800000
s_mov_b32 s76,0xc0135761
s_mov_b32 s77,0x3d372713
s_mov_b32 s44,s59
s_mov_b32 s45,s59
s_mov_b32 s46,s59
s_mov_b32 s47,s59
s_mov_b32 s48,s83
s_add_u32 s49,s48,s84
s_add_u32 s50,s49,s55
s_cmp_eq_u32 s50,s56
s_cselect_b32 s50,s57,s50
s_cmp_eq_u32 s50,s58
s_cselect_b32 s50,s59,s50
s_add_u32 s51,s50,s55
s_cmp_eq_u32 s51,s56
s_cselect_b32 s51,s57,s51
s_cmp_eq_u32 s51,s58
s_cselect_b32 s51,s59,s51
s_add_u32 s52,s51,s55
s_cmp_eq_u32 s52,s56
s_cselect_b32 s52,s57,s52
s_cmp_eq_u32 s52,s58
s_cselect_b32 s52,s59,s52
s_lshl_b32 s63,s54,12
s_add_u32 s63,s63,65536
s_add_u32 s64,s63,32768
s_lshl_b32 s88,s54,10
s_add_u32 s88,s88,131072
s_lshl_b32 s78,s54,11
s_add_u32 s78,s78,132608
v_add_u32 v1,16384,v0
v_mov_b32 v12,0x3727c5ac
s_getpc_b64 s[90:91]
Lg3_pc_%=:
s_mov_b32 s68,Lg3_d1w0_%=-Lg3_pc_%=
s_cmp_eq_u32 s54,1
s_cselect_b32 s68,Lg3_d1w1_%=-Lg3_pc_%=,s68
s_cmp_eq_u32 s54,2
s_cselect_b32 s68,Lg3_d1w2_%=-Lg3_pc_%=,s68
s_cmp_eq_u32 s54,3
s_cselect_b32 s68,Lg3_d1w3_%=-Lg3_pc_%=,s68
s_add_u32 s92,s90,s68
s_addc_u32 s93,s91,0
s_mov_b32 s68,Lg3_d0w0_%=-Lg3_pc_%=
s_cmp_eq_u32 s54,1
s_cselect_b32 s68,Lg3_d0w1_%=-Lg3_pc_%=,s68
s_cmp_eq_u32 s54,2
s_cselect_b32 s68,Lg3_d0w2_%=-Lg3_pc_%=,s68
s_cmp_eq_u32 s54,3
s_cselect_b32 s68,Lg3_d0w3_%=-Lg3_pc_%=,s68
s_add_u32 s90,s90,s68
s_addc_u32 s91,s91,0
s_mov_b32 s66,s59
s_mov_b32 s65,0
s_mov_b32 s62,s63
s_branch Lg3_slow0_%=
Lg3_d0w0_%=:
v_mfma_f32_16x16x32_bf16 v[28:31],a[0:3],v[52:55],0
s_mov_b32 m0,s62
ds_read_b128 v[72:75],v2 offset:5120
v_mul_f32 v116,s75,v112
v_mfma_f32_16x16x32_bf16 v[32:35],a[4:7],v[52:55],0
buffer_load_dwordx4 v0,s[36:39],s61 offen offset:0 sc1 lds
v_mfma_f32_16x16x32_bf16 v[36:39],a[8:11],v[52:55],0
s_mul_i32 s73,s47,7
v_fma_f32 v113,v113,s75,v12
v_fma_f32 v113,-v116,v116,v113
v_mfma_f32_16x16x32_bf16 v[28:31],a[12:15],v[56:59],v[28:31]
s_lshr_b32 s73,s73,1
ds_read_b128 v[76:79],v2 offset:6144
v_max_f32 v113,v113,v12
v_mfma_f32_16x16x32_bf16 v[32:35],a[16:19],v[56:59],v[32:35]
s_add_u32 s53,s52,s55
v_rsq_f32 v112,v113
v_mfma_f32_16x16x32_bf16 v[36:39],a[20:23],v[56:59],v[36:39]
s_waitcnt lgkmcnt(3)
buffer_load_dwordx4 v0,s[36:39],s61 offen offset:1024 sc1 lds
v_mfma_f32_16x16x32_bf16 v[28:31],a[24:27],v[60:63],v[28:31]
s_cmp_eq_u32 s53,s56
ds_read_b128 v[80:83],v2 offset:7168
v_mul_f32 v117,s76,v112
v_mfma_f32_16x16x32_bf16 v[32:35],a[28:31],v[60:63],v[32:35]
s_cselect_b32 s53,s57,s53
v_mul_f32 v118,v112,v112
v_mul_f32 v119,v113,v112
v_mfma_f32_16x16x32_bf16 v[36:39],a[32:35],v[60:63],v[36:39]
s_cmp_eq_u32 s53,s58
v_mul_f32 v118,s77,v118
v_mul_f32 v118,v118,v117
v_mfma_f32_16x16x32_bf16 v[28:31],a[36:39],v[64:67],v[28:31]
buffer_load_dwordx4 v0,s[36:39],s61 offen offset:2048 sc1 lds
v_mfma_f32_16x16x32_bf16 v[32:35],a[40:43],v[64:67],v[32:35]
s_cselect_b32 s53,s59,s53
ds_read_b128 v[52:55],v2 offset:8192
v_fma_f32 v40,-v116,v16,v40
v_mfma_f32_16x16x32_bf16 v[36:39],a[44:47],v[64:67],v[36:39]
s_waitcnt lgkmcnt(3)
v_fma_f32 v41,-v116,v17,v41
v_fma_f32 v42,-v116,v18,v42
v_mfma_f32_16x16x32_bf16 v[28:31],a[48:51],v[68:71],v[28:31]
s_cmp_ge_u32 s66,s59
ds_read_b128 v[56:59],v2 offset:9216
v_fma_f32 v43,-v116,v19,v43
v_mfma_f32_16x16x32_bf16 v[32:35],a[52:55],v[68:71],v[32:35]
buffer_load_dwordx4 v0,s[36:39],s61 offen offset:3072 sc1 lds
v_mfma_f32_16x16x32_bf16 v[36:39],a[56:59],v[68:71],v[36:39]
s_cselect_b32 s86,s49,s50
v_mul_f32 v84,v40,v40
v_mul_f32 v85,v41,v41
v_mfma_f32_16x16x32_bf16 v[28:31],a[60:63],v[72:75],v[28:31]
s_lshr_b32 s68,s86,5
ds_read_b128 v[60:63],v2 offset:10240
v_mul_f32 v86,v42,v42
v_mfma_f32_16x16x32_bf16 v[32:35],a[64:67],v[72:75],v[32:35]
s_add_u32 m0,s78,0
v_mul_f32 v87,v43,v43
v_fma_f32 v84,v118,v84,v117
v_mfma_f32_16x16x32_bf16 v[36:39],a[68:71],v[72:75],v[36:39]
s_waitcnt lgkmcnt(3)
v_mul_f32 v84,v40,v84
v_fma_f32 v85,v118,v85,v117
v_mfma_f32_16x16x32_bf16 v[28:31],a[72:75],v[76:79],v[28:31]
ds_read_b128 v[64:67],v2 offset:11264
v_mul_f32 v85,v41,v85
v_mfma_f32_16x16x32_bf16 v[32:35],a[76:79],v[76:79],v[32:35]
v_exp_f32 v84,v84
v_mfma_f32_16x16x32_bf16 v[36:39],a[80:83],v[76:79],v[36:39]
v_exp_f32 v85,v85
v_mfma_f32_16x16x32_bf16 v[28:31],a[84:87],v[80:83],v[28:31]
ds_read_b128 v[68:71],v2 offset:12288
v_fma_f32 v84,v84,v119,v119
v_mfma_f32_16x16x32_bf16 v[32:35],a[88:91],v[80:83],v[32:35]
v_rcp_f32 v84,v84
v_mfma_f32_16x16x32_bf16 v[36:39],a[92:95],v[80:83],v[36:39]
s_waitcnt lgkmcnt(3)
v_fma_f32 v85,v85,v119,v119
v_fma_f32 v86,v118,v86,v117
v_mfma_f32_16x16x32_bf16 v[28:31],a[96:99],v[52:55],v[28:31]
ds_read_b128 v[72:75],v2 offset:13312
v_mul_f32 v86,v42,v86
v_mfma_f32_16x16x32_bf16 v[32:35],a[100:103],v[52:55],v[32:35]
v_exp_f32 v86,v86
v_mfma_f32_16x16x32_bf16 v[36:39],a[104:107],v[52:55],v[36:39]
v_rcp_f32 v85,v85
v_mfma_f32_16x16x32_bf16 v[28:31],a[108:111],v[56:59],v[28:31]
ds_read_b128 v[76:79],v2 offset:14336
v_fma_f32 v86,v86,v119,v119
v_mfma_f32_16x16x32_bf16 v[32:35],a[112:115],v[56:59],v[32:35]
v_rcp_f32 v86,v86
v_mfma_f32_16x16x32_bf16 v[36:39],a[116:119],v[56:59],v[36:39]
s_waitcnt lgkmcnt(3)
v_fma_f32 v87,v118,v87,v117
v_mul_f32 v87,v43,v87
v_mfma_f32_16x16x32_bf16 v[28:31],a[120:123],v[60:63],v[28:31]
ds_read_b128 v[80:83],v2 offset:15360
v_mul_f32 v40,v40,v84
v_mfma_f32_16x16x32_bf16 v[32:35],a[124:127],v[60:63],v[32:35]
s_waitcnt vmcnt(6)
v_exp_f32 v87,v87
v_mfma_f32_16x16x32_bf16 v[36:39],a[128:131],v[60:63],v[36:39]
s_barrier
v_mfma_f32_16x16x32_bf16 v[28:31],a[132:135],v[64:67],v[28:31]
ds_read_b128 v[52:55],v2 offset:16384
v_fma_f32 v87,v87,v119,v119
v_mfma_f32_16x16x32_bf16 v[32:35],a[136:139],v[64:67],v[32:35]
v_rcp_f32 v87,v87
v_mfma_f32_16x16x32_bf16 v[36:39],a[140:143],v[64:67],v[36:39]
s_waitcnt lgkmcnt(3)
v_add_f32_dpp v104,v104,v104 quad_perm:[1,0,3,2] row_mask:0xf bank_mask:0xf
v_mul_f32 v41,v41,v85
v_mfma_f32_16x16x32_bf16 v[28:31],a[144:147],v[68:71],v[28:31]
ds_read_b128 v[56:59],v2 offset:17408
v_add_f32_dpp v104,v104,v104 quad_perm:[2,3,0,1] row_mask:0xf bank_mask:0xf)"
R"(
v_mfma_f32_16x16x32_bf16 v[32:35],a[148:151],v[68:71],v[32:35]
s_nop 0
v_add_f32_dpp v104,v104,v104 row_half_mirror row_mask:0xf bank_mask:0xf
v_mul_f32 v42,v42,v86
v_mfma_f32_16x16x32_bf16 v[36:39],a[152:155],v[68:71],v[36:39]
v_mul_f32 v43,v43,v87
v_cvt_pk_bf16_f32 v96,v40,v41
v_mfma_f32_16x16x32_bf16 v[28:31],a[156:159],v[72:75],v[28:31]
ds_read_b128 v[60:63],v2 offset:18432
v_cvt_pk_bf16_f32 v97,v42,v43
v_mfma_f32_16x16x32_bf16 v[32:35],a[160:163],v[72:75],v[32:35]
buffer_load_dword v6,s[36:39],s68 offen offset:0 sc1 lds
v_mfma_f32_16x16x32_bf16 v[36:39],a[164:167],v[72:75],v[36:39]
s_waitcnt lgkmcnt(3)
v_fma_f32 v44,-v116,v20,v44
v_fma_f32 v45,-v116,v21,v45
v_mfma_f32_16x16x32_bf16 v[28:31],a[168:171],v[76:79],v[28:31]
s_add_u32 m0,s62,16384
ds_read_b128 v[64:67],v2 offset:19456
v_fma_f32 v46,-v116,v22,v46
v_mfma_f32_16x16x32_bf16 v[32:35],a[172:175],v[76:79],v[32:35]
buffer_load_dwordx4 v1,s[36:39],s61 offen offset:0 sc1 lds
v_mfma_f32_16x16x32_bf16 v[36:39],a[176:179],v[76:79],v[36:39]
s_lshr_b32 s72,s61,5
v_fma_f32 v47,-v116,v23,v47
v_mul_f32 v88,v44,v44
v_mfma_f32_16x16x32_bf16 v[28:31],a[180:183],v[80:83],v[28:31]
s_mov_b32 s62,s63
ds_read_b128 v[68:71],v2 offset:20480
v_mul_f32 v89,v45,v45
v_mfma_f32_16x16x32_bf16 v[32:35],a[184:187],v[80:83],v[32:35]
ds_write_b32 v10,v104 offset:0
v_mfma_f32_16x16x32_bf16 v[36:39],a[188:191],v[80:83],v[36:39]
s_waitcnt lgkmcnt(4)
buffer_load_dwordx4 v1,s[36:39],s61 offen offset:1024 sc1 lds
v_mfma_f32_16x16x32_bf16 v[28:31],a[192:195],v[52:55],v[28:31]
ds_read_b128 v[72:75],v2 offset:21504
v_mul_f32 v90,v46,v46
v_mfma_f32_16x16x32_bf16 v[32:35],a[196:199],v[52:55],v[32:35]
v_mul_f32 v91,v47,v47
v_fma_f32 v88,v118,v88,v117
v_mfma_f32_16x16x32_bf16 v[36:39],a[200:203],v[52:55],v[36:39]
v_mul_f32 v88,v44,v88
v_fma_f32 v89,v118,v89,v117
v_mfma_f32_16x16x32_bf16 v[28:31],a[204:207],v[56:59],v[28:31]
buffer_load_dwordx4 v1,s[36:39],s61 offen offset:2048 sc1 lds
v_mfma_f32_16x16x32_bf16 v[32:35],a[208:211],v[56:59],v[32:35]
ds_read_b128 v[76:79],v2 offset:22528
v_mul_f32 v89,v45,v89
v_mfma_f32_16x16x32_bf16 v[36:39],a[212:215],v[56:59],v[36:39]
s_waitcnt lgkmcnt(4)
v_exp_f32 v88,v88
v_mfma_f32_16x16x32_bf16 v[28:31],a[216:219],v[60:63],v[28:31]
ds_read_b128 v[80:83],v2 offset:23552
v_fma_f32 v88,v88,v119,v119
v_mfma_f32_16x16x32_bf16 v[32:35],a[220:223],v[60:63],v[32:35]
buffer_load_dwordx4 v1,s[36:39],s61 offen offset:3072 sc1 lds
v_mfma_f32_16x16x32_bf16 v[36:39],a[224:227],v[60:63],v[36:39]
buffer_load_dword v108,v8,s[36:39],s72 offen offset:0 sc1
v_mfma_f32_16x16x32_bf16 v[28:31],a[228:231],v[64:67],v[28:31]
ds_read_b128 v[52:55],v2 offset:24576
v_fma_f32 v90,v118,v90,v117
v_mfma_f32_16x16x32_bf16 v[32:35],a[232:235],v[64:67],v[32:35]
v_exp_f32 v89,v89
v_mfma_f32_16x16x32_bf16 v[36:39],a[236:239],v[64:67],v[36:39]
s_waitcnt lgkmcnt(3)
v_rcp_f32 v88,v88
v_mfma_f32_16x16x32_bf16 v[28:31],a[240:243],v[68:71],v[28:31]
ds_read_b128 v[56:59],v2 offset:25600
v_mul_f32 v90,v46,v90
v_mfma_f32_16x16x32_bf16 v[32:35],a[244:247],v[68:71],v[32:35]
v_exp_f32 v90,v90
v_mfma_f32_16x16x32_bf16 v[36:39],a[248:251],v[68:71],v[36:39]
v_fma_f32 v89,v89,v119,v119
v_fma_f32 v90,v90,v119,v119
v_mfma_f32_16x16x32_bf16 v[28:31],a[252:255],v[72:75],v[28:31]
ds_read_b128 v[60:63],v2 offset:26624
v_fma_f32 v91,v118,v91,v117
v_mfma_f32_16x16x32_bf16 v[32:35],v[128:131],v[72:75],v[32:35]
v_rcp_f32 v89,v89
v_mfma_f32_16x16x32_bf16 v[36:39],v[132:135],v[72:75],v[36:39]
s_waitcnt lgkmcnt(3)
v_rcp_f32 v90,v90
v_mfma_f32_16x16x32_bf16 v[28:31],v[136:139],v[76:79],v[28:31]
ds_read_b128 v[64:67],v2 offset:27648
v_mul_f32 v91,v47,v91
v_mfma_f32_16x16x32_bf16 v[32:35],v[140:143],v[76:79],v[32:35]
v_exp_f32 v91,v91
v_mfma_f32_16x16x32_bf16 v[36:39],v[144:147],v[76:79],v[36:39]
v_fma_f32 v91,v91,v119,v119
v_mul_f32 v44,v44,v88
v_mfma_f32_16x16x32_bf16 v[28:31],v[148:151],v[80:83],v[28:31]
ds_read_b128 v[68:71],v2 offset:28672
v_mul_f32 v45,v45,v89
v_mfma_f32_16x16x32_bf16 v[32:35],v[152:155],v[80:83],v[32:35]
v_rcp_f32 v91,v91
v_mfma_f32_16x16x32_bf16 v[36:39],v[156:159],v[80:83],v[36:39]
s_waitcnt lgkmcnt(3)
v_mul_f32 v46,v46,v90
v_mul_f32 v47,v47,v91
v_mfma_f32_16x16x32_bf16 v[28:31],v[160:163],v[52:55],v[28:31]
ds_read_b128 v[72:75],v2 offset:29696
v_cvt_pk_bf16_f32 v98,v44,v45
v_mfma_f32_16x16x32_bf16 v[32:35],v[164:167],v[52:55],v[32:35]
v_cvt_pk_bf16_f32 v99,v46,v47
v_fma_f32 v48,-v116,v24,v48
v_mfma_f32_16x16x32_bf16 v[36:39],v[168:171],v[52:55],v[36:39]
v_fma_f32 v49,-v116,v25,v49
v_fma_f32 v50,-v116,v26,v50
v_mfma_f32_16x16x32_bf16 v[28:31],v[172:175],v[56:59],v[28:31]
ds_read_b128 v[76:79],v2 offset:30720
v_fma_f32 v51,-v116,v27,v51
v_mfma_f32_16x16x32_bf16 v[32:35],v[176:179],v[56:59],v[32:35]
v_mul_f32 v92,v48,v48
v_mul_f32 v93,v49,v49
v_mfma_f32_16x16x32_bf16 v[36:39],v[180:183],v[56:59],v[36:39]
s_waitcnt lgkmcnt(3)
buffer_store_dwordx4 v[96:99],v3,s[36:39],s73 offen offset:0 sc0 sc1
v_mul_f32 v94,v50,v50
v_mul_f32 v95,v51,v51
v_fma_f32 v92,v118,v92,v117
v_mfma_f32_16x16x32_bf16 v[28:31],v[184:187],v[60:63],v[28:31]
ds_read_b128 v[80:83],v2 offset:31744
v_mul_f32 v92,v48,v92
v_mfma_f32_16x16x32_bf16 v[32:35],v[188:191],v[60:63],v[32:35]
s_waitcnt vmcnt(6)
v_exp_f32 v92,v92
v_mfma_f32_16x16x32_bf16 v[36:39],v[192:195],v[60:63],v[36:39]
s_barrier
v_mfma_f32_16x16x32_bf16 v[28:31],v[196:199],v[64:67],v[28:31]
ds_read_b128 v[52:55],v2 offset:32768
ds_read_b32 v13,v7 offset:0
v_mfma_f32_16x16x32_bf16 v[32:35],v[200:203],v[64:67],v[32:35]
ds_read_b64 v[112:113],v11 offset:0
v_fma_f32 v92,v92,v119,v119
v_mfma_f32_16x16x32_bf16 v[36:39],v[204:207],v[64:67],v[36:39]
s_waitcnt lgkmcnt(5)
v_rcp_f32 v92,v92
v_mfma_f32_16x16x32_bf16 v[28:31],v[208:211],v[68:71],v[28:31]
ds_read_b128 v[56:59],v2 offset:33792
v_fma_f32 v93,v118,v93,v117
v_mfma_f32_16x16x32_bf16 v[32:35],v[212:215],v[68:71],v[32:35]
s_waitcnt lgkmcnt(2)
v_mul_f32 v93,v49,v93
v_fma_f32 v94,v118,v94,v117
v_mfma_f32_16x16x32_bf16 v[36:39],v[216:219],v[68:71],v[36:39]
s_waitcnt lgkmcnt(1)
v_cmp_eq_u32 vcc,s60,v13
v_exp_f32 v93,v93
v_mul_f32 v94,v50,v94
v_mfma_f32_16x16x32_bf16 v[28:31],v[220:223],v[72:75],v[28:31]
s_cmp_eq_u32 vcc_lo,-1
ds_read_b128 v[60:63],v2 offset:34816
v_fma_f32 v93,v93,v119,v119
v_mfma_f32_16x16x32_bf16 v[32:35],v[224:227],v[72:75],v[32:35]
s_cselect_b32 s67,s86,s59
v_exp_f32 v94,v94
v_mfma_f32_16x16x32_bf16 v[36:39],v[228:231],v[72:75],v[36:39]
v_rcp_f32 v93,v93
v_mfma_f32_16x16x32_bf16 v[28:31],v[232:235],v[76:79],v[28:31]
s_lshr_b32 s61,s67,0
ds_read_b128 v[64:67],v2 offset:35840
v_fma_f32 v94,v94,v119,v119
v_mfma_f32_16x16x32_bf16 v[32:35],v[236:239],v[76:79],v[32:35]
s_xor_b32 s63,s63,65536
v_fma_f32 v95,v118,v95,v117
v_mul_f32 v95,v51,v95
v_exp_f32 v95,v95
v_mfma_f32_16x16x32_bf16 v[36:39],v[240:243],v[76:79],v[36:39]
s_cmp_ge_u32 s66,s59
v_fma_f32 v95,v95,v119,v119
v_rcp_f32 v94,v94
v_rcp_f32 v95,v95
v_mfma_f32_16x16x32_bf16 v[28:31],v[244:247],v[80:83],v[28:31]
v_mul_f32 v48,v48,v92
v_mul_f32 v49,v49,v93
v_mul_f32 v50,v50,v94
v_mul_f32 v51,v51,v95
v_cvt_pk_bf16_f32 v100,v48,v49
v_cvt_pk_bf16_f32 v101,v50,v51
v_mfma_f32_16x16x32_bf16 v[32:35],v[248:251],v[80:83],v[32:35]
s_waitcnt lgkmcnt(2)
ds_read_b128 v[68:71],v2 offset:36864
buffer_store_dwordx2 v[100:101],v9,s[36:39],s73 offen offset:0 sc0 sc1
v_mfma_f32_16x16x32_bf16 v[36:39],v[252:255],v[80:83],v[36:39]
s_cbranch_scc1 Lg3_slow1_%=
Lg3_d1w0_%=:
v_mfma_f32_16x16x32_bf16 v[40:43],a[0:3],v[52:55],0
s_mov_b32 m0,s62
ds_read_b128 v[72:75],v2 offset:37888
v_mul_f32 v116,s75,v112
v_mfma_f32_16x16x32_bf16 v[44:47],a[4:7],v[52:55],0
buffer_load_dwordx4 v0,s[36:39],s61 offen offset:0 sc1 lds
v_mfma_f32_16x16x32_bf16 v[48:51],a[8:11],v[52:55],0
s_mul_i32 s73,s48,7
v_fma_f32 v113,v113,s75,v12
v_fma_f32 v113,-v116,v116,v113
v_mfma_f32_16x16x32_bf16 v[40:43],a[12:15],v[56:59],v[40:43]
s_lshr_b32 s73,s73,1
ds_read_b128 v[76:79],v2 offset:38912)"
R"(
v_max_f32 v113,v113,v12
v_mfma_f32_16x16x32_bf16 v[44:47],a[16:19],v[56:59],v[44:47]
s_add_u32 s74,s53,s55
v_rsq_f32 v112,v113
v_mfma_f32_16x16x32_bf16 v[48:51],a[20:23],v[56:59],v[48:51]
s_waitcnt lgkmcnt(3)
buffer_load_dwordx4 v0,s[36:39],s61 offen offset:1024 sc1 lds
v_mfma_f32_16x16x32_bf16 v[40:43],a[24:27],v[60:63],v[40:43]
s_cmp_eq_u32 s74,s56
ds_read_b128 v[80:83],v2 offset:39936
v_mul_f32 v117,s76,v112
v_mfma_f32_16x16x32_bf16 v[44:47],a[28:31],v[60:63],v[44:47]
s_cselect_b32 s74,s57,s74
v_mul_f32 v118,v112,v112
v_mul_f32 v119,v113,v112
v_mfma_f32_16x16x32_bf16 v[48:51],a[32:35],v[60:63],v[48:51]
s_cmp_eq_u32 s74,s58
v_mul_f32 v118,s77,v118
v_mul_f32 v118,v118,v117
v_mfma_f32_16x16x32_bf16 v[40:43],a[36:39],v[64:67],v[40:43]
buffer_load_dwordx4 v0,s[36:39],s61 offen offset:2048 sc1 lds
v_mfma_f32_16x16x32_bf16 v[44:47],a[40:43],v[64:67],v[44:47]
s_cselect_b32 s74,s59,s74
ds_read_b128 v[52:55],v2 offset:40960
v_fma_f32 v28,-v116,v16,v28
v_mfma_f32_16x16x32_bf16 v[48:51],a[44:47],v[64:67],v[48:51]
s_waitcnt lgkmcnt(3)
v_fma_f32 v29,-v116,v17,v29
v_fma_f32 v30,-v116,v18,v30
v_mfma_f32_16x16x32_bf16 v[40:43],a[48:51],v[68:71],v[40:43]
s_cmp_ge_u32 s67,s59
ds_read_b128 v[56:59],v2 offset:41984
v_fma_f32 v31,-v116,v19,v31
v_mfma_f32_16x16x32_bf16 v[44:47],a[52:55],v[68:71],v[44:47]
buffer_load_dwordx4 v0,s[36:39],s61 offen offset:3072 sc1 lds
v_mfma_f32_16x16x32_bf16 v[48:51],a[56:59],v[68:71],v[48:51]
s_cselect_b32 s86,s50,s51
v_mul_f32 v84,v28,v28
v_mul_f32 v85,v29,v29
v_mfma_f32_16x16x32_bf16 v[40:43],a[60:63],v[72:75],v[40:43]
s_lshr_b32 s68,s86,5
ds_read_b128 v[60:63],v2 offset:43008
v_mul_f32 v86,v30,v30
v_mfma_f32_16x16x32_bf16 v[44:47],a[64:67],v[72:75],v[44:47]
s_add_u32 m0,s78,256
v_mul_f32 v87,v31,v31
v_fma_f32 v84,v118,v84,v117
v_mfma_f32_16x16x32_bf16 v[48:51],a[68:71],v[72:75],v[48:51]
s_waitcnt lgkmcnt(3)
v_mul_f32 v84,v28,v84
v_fma_f32 v85,v118,v85,v117
v_mfma_f32_16x16x32_bf16 v[40:43],a[72:75],v[76:79],v[40:43]
s_mov_b64 s[44:45],s[46:47]
ds_read_b128 v[64:67],v2 offset:44032
v_mul_f32 v85,v29,v85
v_mfma_f32_16x16x32_bf16 v[44:47],a[76:79],v[76:79],v[44:47]
s_mov_b64 s[46:47],s[48:49]
v_exp_f32 v84,v84
v_mfma_f32_16x16x32_bf16 v[48:51],a[80:83],v[76:79],v[48:51]
s_mov_b64 s[48:49],s[50:51]
v_exp_f32 v85,v85
v_mfma_f32_16x16x32_bf16 v[40:43],a[84:87],v[80:83],v[40:43]
s_mov_b64 s[50:51],s[52:53]
ds_read_b128 v[68:71],v2 offset:45056
v_fma_f32 v84,v84,v119,v119
v_mfma_f32_16x16x32_bf16 v[44:47],a[88:91],v[80:83],v[44:47]
s_mov_b32 s52,s74
v_rcp_f32 v84,v84
v_mfma_f32_16x16x32_bf16 v[48:51],a[92:95],v[80:83],v[48:51]
s_waitcnt lgkmcnt(3)
v_fma_f32 v85,v85,v119,v119
v_fma_f32 v86,v118,v86,v117
v_mfma_f32_16x16x32_bf16 v[40:43],a[96:99],v[52:55],v[40:43]
ds_read_b128 v[72:75],v2 offset:46080
v_mul_f32 v86,v30,v86
v_mfma_f32_16x16x32_bf16 v[44:47],a[100:103],v[52:55],v[44:47]
v_exp_f32 v86,v86
v_mfma_f32_16x16x32_bf16 v[48:51],a[104:107],v[52:55],v[48:51]
v_rcp_f32 v85,v85
v_mfma_f32_16x16x32_bf16 v[40:43],a[108:111],v[56:59],v[40:43]
ds_read_b128 v[76:79],v2 offset:47104
v_fma_f32 v86,v86,v119,v119
v_mfma_f32_16x16x32_bf16 v[44:47],a[112:115],v[56:59],v[44:47]
v_rcp_f32 v86,v86
v_mfma_f32_16x16x32_bf16 v[48:51],a[116:119],v[56:59],v[48:51]
s_waitcnt lgkmcnt(3)
v_fma_f32 v87,v118,v87,v117
v_mul_f32 v87,v31,v87
v_mfma_f32_16x16x32_bf16 v[40:43],a[120:123],v[60:63],v[40:43]
ds_read_b128 v[80:83],v2 offset:48128
v_mul_f32 v28,v28,v84
v_mfma_f32_16x16x32_bf16 v[44:47],a[124:127],v[60:63],v[44:47]
s_waitcnt vmcnt(6)
v_exp_f32 v87,v87
v_mfma_f32_16x16x32_bf16 v[48:51],a[128:131],v[60:63],v[48:51]
s_barrier
v_mfma_f32_16x16x32_bf16 v[40:43],a[132:135],v[64:67],v[40:43]
ds_read_b128 v[52:55],v2 offset:49152
v_fma_f32 v87,v87,v119,v119
v_mfma_f32_16x16x32_bf16 v[44:47],a[136:139],v[64:67],v[44:47]
v_rcp_f32 v87,v87
v_mfma_f32_16x16x32_bf16 v[48:51],a[140:143],v[64:67],v[48:51]
s_waitcnt lgkmcnt(3)
v_add_f32_dpp v108,v108,v108 quad_perm:[1,0,3,2] row_mask:0xf bank_mask:0xf
v_mul_f32 v29,v29,v85
v_mfma_f32_16x16x32_bf16 v[40:43],a[144:147],v[68:71],v[40:43]
ds_read_b128 v[56:59],v2 offset:50176
v_add_f32_dpp v108,v108,v108 quad_perm:[2,3,0,1] row_mask:0xf bank_mask:0xf
v_mfma_f32_16x16x32_bf16 v[44:47],a[148:151],v[68:71],v[44:47]
s_nop 0
v_add_f32_dpp v108,v108,v108 row_half_mirror row_mask:0xf bank_mask:0xf
v_mul_f32 v30,v30,v86
v_mfma_f32_16x16x32_bf16 v[48:51],a[152:155],v[68:71],v[48:51]
v_mul_f32 v31,v31,v87
v_cvt_pk_bf16_f32 v96,v28,v29
v_mfma_f32_16x16x32_bf16 v[40:43],a[156:159],v[72:75],v[40:43]
ds_read_b128 v[60:63],v2 offset:51200
v_cvt_pk_bf16_f32 v97,v30,v31
v_mfma_f32_16x16x32_bf16 v[44:47],a[160:163],v[72:75],v[44:47]
buffer_load_dword v6,s[36:39],s68 offen offset:0 sc1 lds
v_mfma_f32_16x16x32_bf16 v[48:51],a[164:167],v[72:75],v[48:51]
s_waitcnt lgkmcnt(3)
v_fma_f32 v32,-v116,v20,v32
v_fma_f32 v33,-v116,v21,v33
v_mfma_f32_16x16x32_bf16 v[40:43],a[168:171],v[76:79],v[40:43]
s_add_u32 m0,s62,16384
ds_read_b128 v[64:67],v2 offset:52224
v_fma_f32 v34,-v116,v22,v34
v_mfma_f32_16x16x32_bf16 v[44:47],a[172:175],v[76:79],v[44:47]
buffer_load_dwordx4 v1,s[36:39],s61 offen offset:0 sc1 lds
v_mfma_f32_16x16x32_bf16 v[48:51],a[176:179],v[76:79],v[48:51]
s_lshr_b32 s72,s61,5
v_fma_f32 v35,-v116,v23,v35
v_mul_f32 v88,v32,v32
v_mfma_f32_16x16x32_bf16 v[40:43],a[180:183],v[80:83],v[40:43]
s_mov_b32 s62,s64
ds_read_b128 v[68:71],v2 offset:53248
v_mul_f32 v89,v33,v33
v_mfma_f32_16x16x32_bf16 v[44:47],a[184:187],v[80:83],v[44:47]
ds_write_b32 v10,v108 offset:128
v_mfma_f32_16x16x32_bf16 v[48:51],a[188:191],v[80:83],v[48:51]
s_waitcnt lgkmcnt(4)
buffer_load_dwordx4 v1,s[36:39],s61 offen offset:1024 sc1 lds
v_mfma_f32_16x16x32_bf16 v[40:43],a[192:195],v[52:55],v[40:43]
ds_read_b128 v[72:75],v2 offset:54272
v_mul_f32 v90,v34,v34
v_mfma_f32_16x16x32_bf16 v[44:47],a[196:199],v[52:55],v[44:47]
v_mul_f32 v91,v35,v35
v_fma_f32 v88,v118,v88,v117
v_mfma_f32_16x16x32_bf16 v[48:51],a[200:203],v[52:55],v[48:51]
v_mul_f32 v88,v32,v88
v_fma_f32 v89,v118,v89,v117
v_mfma_f32_16x16x32_bf16 v[40:43],a[204:207],v[56:59],v[40:43]
buffer_load_dwordx4 v1,s[36:39],s61 offen offset:2048 sc1 lds
v_mfma_f32_16x16x32_bf16 v[44:47],a[208:211],v[56:59],v[44:47]
ds_read_b128 v[76:79],v2 offset:55296
v_mul_f32 v89,v33,v89
v_mfma_f32_16x16x32_bf16 v[48:51],a[212:215],v[56:59],v[48:51]
s_waitcnt lgkmcnt(4)
v_exp_f32 v88,v88
v_mfma_f32_16x16x32_bf16 v[40:43],a[216:219],v[60:63],v[40:43]
ds_read_b128 v[80:83],v2 offset:56320
v_fma_f32 v88,v88,v119,v119
v_mfma_f32_16x16x32_bf16 v[44:47],a[220:223],v[60:63],v[44:47]
buffer_load_dwordx4 v1,s[36:39],s61 offen offset:3072 sc1 lds
v_mfma_f32_16x16x32_bf16 v[48:51],a[224:227],v[60:63],v[48:51]
buffer_load_dword v104,v8,s[36:39],s72 offen offset:0 sc1
v_mfma_f32_16x16x32_bf16 v[40:43],a[228:231],v[64:67],v[40:43]
ds_read_b128 v[52:55],v2 offset:57344
v_fma_f32 v90,v118,v90,v117
v_mfma_f32_16x16x32_bf16 v[44:47],a[232:235],v[64:67],v[44:47]
v_exp_f32 v89,v89
v_mfma_f32_16x16x32_bf16 v[48:51],a[236:239],v[64:67],v[48:51]
s_waitcnt lgkmcnt(3)
v_rcp_f32 v88,v88
v_mfma_f32_16x16x32_bf16 v[40:43],a[240:243],v[68:71],v[40:43]
ds_read_b128 v[56:59],v2 offset:58368
v_mul_f32 v90,v34,v90
v_mfma_f32_16x16x32_bf16 v[44:47],a[244:247],v[68:71],v[44:47]
v_exp_f32 v90,v90
v_mfma_f32_16x16x32_bf16 v[48:51],a[248:251],v[68:71],v[48:51]
v_fma_f32 v89,v89,v119,v119
v_fma_f32 v90,v90,v119,v119
v_mfma_f32_16x16x32_bf16 v[40:43],a[252:255],v[72:75],v[40:43]
ds_read_b128 v[60:63],v2 offset:59392
v_fma_f32 v91,v118,v91,v117
v_mfma_f32_16x16x32_bf16 v[44:47],v[128:131],v[72:75],v[44:47]
v_rcp_f32 v89,v89
v_mfma_f32_16x16x32_bf16 v[48:51],v[132:135],v[72:75],v[48:51]
s_waitcnt lgkmcnt(3)
v_rcp_f32 v90,v90
v_mfma_f32_16x16x32_bf16 v[40:43],v[136:139],v[76:79],v[40:43]
ds_read_b128 v[64:67],v2 offset:60416
v_mul_f32 v91,v35,v91
v_mfma_f32_16x16x32_bf16 v[44:47],v[140:143],v[76:79],v[44:47]
v_exp_f32 v91,v91
v_mfma_f32_16x16x32_bf16 v[48:51],v[144:147],v[76:79],v[48:51]
v_fma_f32 v91,v91,v119,v119
v_mul_f32 v32,v32,v88
v_mfma_f32_16x16x32_bf16 v[40:43],v[148:151],v[80:83],v[40:43])"
R"(
ds_read_b128 v[68:71],v2 offset:61440
v_mul_f32 v33,v33,v89
v_mfma_f32_16x16x32_bf16 v[44:47],v[152:155],v[80:83],v[44:47]
v_rcp_f32 v91,v91
v_mfma_f32_16x16x32_bf16 v[48:51],v[156:159],v[80:83],v[48:51]
s_waitcnt lgkmcnt(3)
v_mul_f32 v34,v34,v90
v_mul_f32 v35,v35,v91
v_mfma_f32_16x16x32_bf16 v[40:43],v[160:163],v[52:55],v[40:43]
ds_read_b128 v[72:75],v2 offset:62464
v_cvt_pk_bf16_f32 v98,v32,v33
v_mfma_f32_16x16x32_bf16 v[44:47],v[164:167],v[52:55],v[44:47]
v_cvt_pk_bf16_f32 v99,v34,v35
v_fma_f32 v36,-v116,v24,v36
v_mfma_f32_16x16x32_bf16 v[48:51],v[168:171],v[52:55],v[48:51]
v_fma_f32 v37,-v116,v25,v37
v_fma_f32 v38,-v116,v26,v38
v_mfma_f32_16x16x32_bf16 v[40:43],v[172:175],v[56:59],v[40:43]
ds_read_b128 v[76:79],v2 offset:63488
v_fma_f32 v39,-v116,v27,v39
v_mfma_f32_16x16x32_bf16 v[44:47],v[176:179],v[56:59],v[44:47]
v_mul_f32 v92,v36,v36
v_mul_f32 v93,v37,v37
v_mfma_f32_16x16x32_bf16 v[48:51],v[180:183],v[56:59],v[48:51]
s_waitcnt lgkmcnt(3)
buffer_store_dwordx4 v[96:99],v3,s[36:39],s73 offen offset:0 sc0 sc1
v_mul_f32 v94,v38,v38
v_mul_f32 v95,v39,v39
v_fma_f32 v92,v118,v92,v117
v_mfma_f32_16x16x32_bf16 v[40:43],v[184:187],v[60:63],v[40:43]
ds_read_b128 v[80:83],v2 offset:64512
v_mul_f32 v92,v36,v92
v_mfma_f32_16x16x32_bf16 v[44:47],v[188:191],v[60:63],v[44:47]
s_waitcnt vmcnt(6)
v_xor_b32 v2,65536,v2
v_fma_f32 v93,v118,v93,v117
v_mfma_f32_16x16x32_bf16 v[48:51],v[192:195],v[60:63],v[48:51]
s_barrier
v_mfma_f32_16x16x32_bf16 v[40:43],v[196:199],v[64:67],v[40:43]
ds_read_b128 v[52:55],v2 offset:0
ds_read_b32 v13,v7 offset:256
v_mfma_f32_16x16x32_bf16 v[44:47],v[200:203],v[64:67],v[44:47]
ds_read_b64 v[112:113],v11 offset:128
v_mul_f32 v93,v37,v93
v_mfma_f32_16x16x32_bf16 v[48:51],v[204:207],v[64:67],v[48:51]
s_waitcnt lgkmcnt(5)
v_exp_f32 v92,v92
v_mfma_f32_16x16x32_bf16 v[40:43],v[208:211],v[68:71],v[40:43]
ds_read_b128 v[56:59],v2 offset:1024
v_fma_f32 v92,v92,v119,v119
v_mfma_f32_16x16x32_bf16 v[44:47],v[212:215],v[68:71],v[44:47]
s_waitcnt lgkmcnt(2)
v_exp_f32 v93,v93
v_mfma_f32_16x16x32_bf16 v[48:51],v[216:219],v[68:71],v[48:51]
s_waitcnt lgkmcnt(1)
v_cmp_eq_u32 vcc,s60,v13
v_rcp_f32 v92,v92
v_fma_f32 v93,v93,v119,v119
v_mfma_f32_16x16x32_bf16 v[40:43],v[220:223],v[72:75],v[40:43]
s_cmp_eq_u32 vcc_lo,-1
ds_read_b128 v[60:63],v2 offset:2048
v_fma_f32 v94,v118,v94,v117
v_mfma_f32_16x16x32_bf16 v[44:47],v[224:227],v[72:75],v[44:47]
s_cselect_b32 s66,s86,s59
v_rcp_f32 v93,v93
v_mfma_f32_16x16x32_bf16 v[48:51],v[228:231],v[72:75],v[48:51]
v_mul_f32 v94,v38,v94
v_fma_f32 v95,v118,v95,v117
v_mfma_f32_16x16x32_bf16 v[40:43],v[232:235],v[76:79],v[40:43]
s_lshr_b32 s61,s66,0
ds_read_b128 v[64:67],v2 offset:3072
v_mul_f32 v95,v39,v95
v_mfma_f32_16x16x32_bf16 v[44:47],v[236:239],v[76:79],v[44:47]
s_xor_b32 s64,s64,65536
v_exp_f32 v94,v94
v_exp_f32 v95,v95
v_mfma_f32_16x16x32_bf16 v[48:51],v[240:243],v[76:79],v[48:51]
s_cmp_lt_u32 s67,s59
v_fma_f32 v94,v94,v119,v119
v_fma_f32 v95,v95,v119,v119
v_rcp_f32 v94,v94
v_rcp_f32 v95,v95
v_mfma_f32_16x16x32_bf16 v[40:43],v[244:247],v[80:83],v[40:43]
v_mul_f32 v36,v36,v92
v_mul_f32 v37,v37,v93
v_mul_f32 v38,v38,v94
v_mul_f32 v39,v39,v95
v_cvt_pk_bf16_f32 v100,v36,v37
v_cvt_pk_bf16_f32 v101,v38,v39
v_mfma_f32_16x16x32_bf16 v[44:47],v[248:251],v[80:83],v[44:47]
s_waitcnt lgkmcnt(2)
ds_read_b128 v[68:71],v2 offset:4096
buffer_store_dwordx2 v[100:101],v9,s[36:39],s73 offen offset:0 sc0 sc1
v_mfma_f32_16x16x32_bf16 v[48:51],v[252:255],v[80:83],v[48:51]
s_cbranch_scc1 Lg3_d0w0_%=
s_branch Lg3_slow0_%=
Lg3_d0w1_%=:
v_mfma_f32_16x16x32_bf16 v[28:31],a[0:3],v[52:55],0
s_mov_b32 m0,s62
ds_read_b128 v[72:75],v2 offset:5120
v_mul_f32 v116,s75,v112
v_mfma_f32_16x16x32_bf16 v[32:35],a[4:7],v[52:55],0
s_mul_i32 s73,s47,7
v_fma_f32 v113,v113,s75,v12
v_fma_f32 v113,-v116,v116,v113
v_mfma_f32_16x16x32_bf16 v[36:39],a[8:11],v[52:55],0
buffer_load_dwordx4 v0,s[36:39],s61 offen offset:0 sc1 lds
v_mfma_f32_16x16x32_bf16 v[28:31],a[12:15],v[56:59],v[28:31]
s_lshr_b32 s73,s73,1
ds_read_b128 v[76:79],v2 offset:6144
v_max_f32 v113,v113,v12
v_mfma_f32_16x16x32_bf16 v[32:35],a[16:19],v[56:59],v[32:35]
s_add_u32 s53,s52,s55
v_rsq_f32 v112,v113
v_mfma_f32_16x16x32_bf16 v[36:39],a[20:23],v[56:59],v[36:39]
s_waitcnt lgkmcnt(3)
v_mul_f32 v117,s76,v112
v_mul_f32 v118,v112,v112
v_mfma_f32_16x16x32_bf16 v[28:31],a[24:27],v[60:63],v[28:31]
buffer_load_dwordx4 v0,s[36:39],s61 offen offset:1024 sc1 lds
v_mfma_f32_16x16x32_bf16 v[32:35],a[28:31],v[60:63],v[32:35]
s_cmp_eq_u32 s53,s56
ds_read_b128 v[80:83],v2 offset:7168
v_mul_f32 v119,v113,v112
v_mfma_f32_16x16x32_bf16 v[36:39],a[32:35],v[60:63],v[36:39]
s_cselect_b32 s53,s57,s53
v_mul_f32 v118,s77,v118
v_mul_f32 v118,v118,v117
v_mfma_f32_16x16x32_bf16 v[28:31],a[36:39],v[64:67],v[28:31]
s_cmp_eq_u32 s53,s58
ds_read_b128 v[52:55],v2 offset:8192
v_fma_f32 v40,-v116,v16,v40
v_mfma_f32_16x16x32_bf16 v[32:35],a[40:43],v[64:67],v[32:35]
buffer_load_dwordx4 v0,s[36:39],s61 offen offset:2048 sc1 lds
v_mfma_f32_16x16x32_bf16 v[36:39],a[44:47],v[64:67],v[36:39]
s_waitcnt lgkmcnt(3)
v_fma_f32 v41,-v116,v17,v41
v_fma_f32 v42,-v116,v18,v42
v_mfma_f32_16x16x32_bf16 v[28:31],a[48:51],v[68:71],v[28:31]
s_cselect_b32 s53,s59,s53
ds_read_b128 v[56:59],v2 offset:9216
v_fma_f32 v43,-v116,v19,v43
v_mfma_f32_16x16x32_bf16 v[32:35],a[52:55],v[68:71],v[32:35]
s_cmp_ge_u32 s66,s59
v_mul_f32 v84,v40,v40
v_mul_f32 v85,v41,v41
v_mfma_f32_16x16x32_bf16 v[36:39],a[56:59],v[68:71],v[36:39]
buffer_load_dwordx4 v0,s[36:39],s61 offen offset:3072 sc1 lds
v_mfma_f32_16x16x32_bf16 v[28:31],a[60:63],v[72:75],v[28:31]
s_cselect_b32 s86,s49,s50
ds_read_b128 v[60:63],v2 offset:10240
v_mul_f32 v86,v42,v42
v_mfma_f32_16x16x32_bf16 v[32:35],a[64:67],v[72:75],v[32:35]
s_add_u32 m0,s62,16384
v_mul_f32 v87,v43,v43
v_fma_f32 v84,v118,v84,v117
v_mfma_f32_16x16x32_bf16 v[36:39],a[68:71],v[72:75],v[36:39]
s_waitcnt lgkmcnt(3)
v_mul_f32 v84,v40,v84
v_fma_f32 v85,v118,v85,v117
v_mfma_f32_16x16x32_bf16 v[28:31],a[72:75],v[76:79],v[28:31]
s_lshr_b32 s72,s61,5
ds_read_b128 v[64:67],v2 offset:11264
v_mul_f32 v85,v41,v85
v_mfma_f32_16x16x32_bf16 v[32:35],a[76:79],v[76:79],v[32:35]
s_mov_b32 s62,s63
v_exp_f32 v84,v84
v_mfma_f32_16x16x32_bf16 v[36:39],a[80:83],v[76:79],v[36:39]
v_exp_f32 v85,v85
v_mfma_f32_16x16x32_bf16 v[28:31],a[84:87],v[80:83],v[28:31]
ds_read_b128 v[68:71],v2 offset:12288
v_fma_f32 v84,v84,v119,v119
v_mfma_f32_16x16x32_bf16 v[32:35],a[88:91],v[80:83],v[32:35]
v_rcp_f32 v84,v84
v_mfma_f32_16x16x32_bf16 v[36:39],a[92:95],v[80:83],v[36:39]
s_waitcnt lgkmcnt(3)
v_fma_f32 v85,v85,v119,v119
v_fma_f32 v86,v118,v86,v117
v_mfma_f32_16x16x32_bf16 v[28:31],a[96:99],v[52:55],v[28:31]
ds_read_b128 v[72:75],v2 offset:13312
v_mul_f32 v86,v42,v86
v_mfma_f32_16x16x32_bf16 v[32:35],a[100:103],v[52:55],v[32:35]
v_exp_f32 v86,v86
v_mfma_f32_16x16x32_bf16 v[36:39],a[104:107],v[52:55],v[36:39]
v_rcp_f32 v85,v85
v_mfma_f32_16x16x32_bf16 v[28:31],a[108:111],v[56:59],v[28:31]
ds_read_b128 v[76:79],v2 offset:14336
v_fma_f32 v86,v86,v119,v119
v_mfma_f32_16x16x32_bf16 v[32:35],a[112:115],v[56:59],v[32:35]
v_rcp_f32 v86,v86
v_mfma_f32_16x16x32_bf16 v[36:39],a[116:119],v[56:59],v[36:39]
s_waitcnt lgkmcnt(3)
v_fma_f32 v87,v118,v87,v117
v_mul_f32 v87,v43,v87
v_mfma_f32_16x16x32_bf16 v[28:31],a[120:123],v[60:63],v[28:31]
ds_read_b128 v[80:83],v2 offset:15360
v_mul_f32 v40,v40,v84
v_mfma_f32_16x16x32_bf16 v[32:35],a[124:127],v[60:63],v[32:35]
s_waitcnt vmcnt(6)
v_exp_f32 v87,v87)"
R"(
v_mfma_f32_16x16x32_bf16 v[36:39],a[128:131],v[60:63],v[36:39]
s_barrier
v_mfma_f32_16x16x32_bf16 v[28:31],a[132:135],v[64:67],v[28:31]
ds_read_b128 v[52:55],v2 offset:16384
v_fma_f32 v87,v87,v119,v119
v_mfma_f32_16x16x32_bf16 v[32:35],a[136:139],v[64:67],v[32:35]
v_rcp_f32 v87,v87
v_mfma_f32_16x16x32_bf16 v[36:39],a[140:143],v[64:67],v[36:39]
s_waitcnt lgkmcnt(3)
v_add_f32_dpp v104,v104,v104 quad_perm:[1,0,3,2] row_mask:0xf bank_mask:0xf
v_mul_f32 v41,v41,v85
v_mfma_f32_16x16x32_bf16 v[28:31],a[144:147],v[68:71],v[28:31]
ds_read_b128 v[56:59],v2 offset:17408
v_add_f32_dpp v104,v104,v104 quad_perm:[2,3,0,1] row_mask:0xf bank_mask:0xf
v_mfma_f32_16x16x32_bf16 v[32:35],a[148:151],v[68:71],v[32:35]
s_nop 0
v_add_f32_dpp v104,v104,v104 row_half_mirror row_mask:0xf bank_mask:0xf
v_mul_f32 v42,v42,v86
v_mfma_f32_16x16x32_bf16 v[36:39],a[152:155],v[68:71],v[36:39]
v_mul_f32 v43,v43,v87
v_cvt_pk_bf16_f32 v96,v40,v41
v_mfma_f32_16x16x32_bf16 v[28:31],a[156:159],v[72:75],v[28:31]
ds_read_b128 v[60:63],v2 offset:18432
v_cvt_pk_bf16_f32 v97,v42,v43
v_mfma_f32_16x16x32_bf16 v[32:35],a[160:163],v[72:75],v[32:35]
v_fma_f32 v44,-v116,v20,v44
v_fma_f32 v45,-v116,v21,v45
v_mfma_f32_16x16x32_bf16 v[36:39],a[164:167],v[72:75],v[36:39]
s_waitcnt lgkmcnt(3)
v_fma_f32 v46,-v116,v22,v46
v_fma_f32 v47,-v116,v23,v47
v_mfma_f32_16x16x32_bf16 v[28:31],a[168:171],v[76:79],v[28:31]
ds_read_b128 v[64:67],v2 offset:19456
v_mul_f32 v88,v44,v44
v_mfma_f32_16x16x32_bf16 v[32:35],a[172:175],v[76:79],v[32:35]
v_mul_f32 v89,v45,v45
v_mul_f32 v90,v46,v46
v_mfma_f32_16x16x32_bf16 v[36:39],a[176:179],v[76:79],v[36:39]
buffer_load_dwordx4 v1,s[36:39],s61 offen offset:0 sc1 lds
v_mfma_f32_16x16x32_bf16 v[28:31],a[180:183],v[80:83],v[28:31]
ds_read_b128 v[68:71],v2 offset:20480
v_mul_f32 v91,v47,v47
v_mfma_f32_16x16x32_bf16 v[32:35],a[184:187],v[80:83],v[32:35]
ds_write_b32 v10,v104 offset:0
v_mfma_f32_16x16x32_bf16 v[36:39],a[188:191],v[80:83],v[36:39]
s_waitcnt lgkmcnt(4)
v_fma_f32 v88,v118,v88,v117
v_mul_f32 v88,v44,v88
v_mfma_f32_16x16x32_bf16 v[28:31],a[192:195],v[52:55],v[28:31]
buffer_load_dwordx4 v1,s[36:39],s61 offen offset:1024 sc1 lds
v_mfma_f32_16x16x32_bf16 v[32:35],a[196:199],v[52:55],v[32:35]
ds_read_b128 v[72:75],v2 offset:21504
v_fma_f32 v89,v118,v89,v117
v_mfma_f32_16x16x32_bf16 v[36:39],a[200:203],v[52:55],v[36:39]
v_exp_f32 v88,v88
v_mfma_f32_16x16x32_bf16 v[28:31],a[204:207],v[56:59],v[28:31]
ds_read_b128 v[76:79],v2 offset:22528
v_mul_f32 v89,v45,v89
v_mfma_f32_16x16x32_bf16 v[32:35],a[208:211],v[56:59],v[32:35]
buffer_load_dwordx4 v1,s[36:39],s61 offen offset:2048 sc1 lds
v_mfma_f32_16x16x32_bf16 v[36:39],a[212:215],v[56:59],v[36:39]
s_waitcnt lgkmcnt(4)
v_exp_f32 v89,v89
v_mfma_f32_16x16x32_bf16 v[28:31],a[216:219],v[60:63],v[28:31]
ds_read_b128 v[80:83],v2 offset:23552
v_fma_f32 v88,v88,v119,v119
v_mfma_f32_16x16x32_bf16 v[32:35],a[220:223],v[60:63],v[32:35]
v_rcp_f32 v88,v88
v_mfma_f32_16x16x32_bf16 v[36:39],a[224:227],v[60:63],v[36:39]
buffer_load_dwordx4 v1,s[36:39],s61 offen offset:3072 sc1 lds
v_mfma_f32_16x16x32_bf16 v[28:31],a[228:231],v[64:67],v[28:31]
buffer_load_dword v108,v8,s[36:39],s72 offen offset:0 sc1
v_mfma_f32_16x16x32_bf16 v[32:35],a[232:235],v[64:67],v[32:35]
ds_read_b128 v[52:55],v2 offset:24576
v_fma_f32 v89,v89,v119,v119
v_mfma_f32_16x16x32_bf16 v[36:39],a[236:239],v[64:67],v[36:39]
s_waitcnt lgkmcnt(3)
v_rcp_f32 v89,v89
v_mfma_f32_16x16x32_bf16 v[28:31],a[240:243],v[68:71],v[28:31]
ds_read_b128 v[56:59],v2 offset:25600
v_fma_f32 v90,v118,v90,v117
v_mfma_f32_16x16x32_bf16 v[32:35],a[244:247],v[68:71],v[32:35]
v_mul_f32 v90,v46,v90
v_fma_f32 v91,v118,v91,v117
v_mfma_f32_16x16x32_bf16 v[36:39],a[248:251],v[68:71],v[36:39]
v_exp_f32 v90,v90
v_mfma_f32_16x16x32_bf16 v[28:31],a[252:255],v[72:75],v[28:31]
ds_read_b128 v[60:63],v2 offset:26624
v_mul_f32 v91,v47,v91
v_mfma_f32_16x16x32_bf16 v[32:35],v[128:131],v[72:75],v[32:35]
v_exp_f32 v91,v91
v_mfma_f32_16x16x32_bf16 v[36:39],v[132:135],v[72:75],v[36:39]
s_waitcnt lgkmcnt(3)
v_fma_f32 v90,v90,v119,v119
v_fma_f32 v91,v91,v119,v119
v_mfma_f32_16x16x32_bf16 v[28:31],v[136:139],v[76:79],v[28:31]
ds_read_b128 v[64:67],v2 offset:27648
v_mul_f32 v44,v44,v88
v_mfma_f32_16x16x32_bf16 v[32:35],v[140:143],v[76:79],v[32:35]
v_rcp_f32 v90,v90
v_mfma_f32_16x16x32_bf16 v[36:39],v[144:147],v[76:79],v[36:39]
v_rcp_f32 v91,v91
v_mfma_f32_16x16x32_bf16 v[28:31],v[148:151],v[80:83],v[28:31]
ds_read_b128 v[68:71],v2 offset:28672
v_mul_f32 v45,v45,v89
v_mfma_f32_16x16x32_bf16 v[32:35],v[152:155],v[80:83],v[32:35]
v_mul_f32 v46,v46,v90
v_mul_f32 v47,v47,v91
v_mfma_f32_16x16x32_bf16 v[36:39],v[156:159],v[80:83],v[36:39]
s_waitcnt lgkmcnt(3)
v_cvt_pk_bf16_f32 v98,v44,v45
v_cvt_pk_bf16_f32 v99,v46,v47
v_mfma_f32_16x16x32_bf16 v[28:31],v[160:163],v[52:55],v[28:31]
ds_read_b128 v[72:75],v2 offset:29696
v_fma_f32 v48,-v116,v24,v48
v_mfma_f32_16x16x32_bf16 v[32:35],v[164:167],v[52:55],v[32:35]
v_fma_f32 v49,-v116,v25,v49
v_fma_f32 v50,-v116,v26,v50
v_mfma_f32_16x16x32_bf16 v[36:39],v[168:171],v[52:55],v[36:39]
v_fma_f32 v51,-v116,v27,v51
v_mul_f32 v92,v48,v48
v_mfma_f32_16x16x32_bf16 v[28:31],v[172:175],v[56:59],v[28:31]
buffer_store_dwordx4 v[96:99],v3,s[36:39],s73 offen offset:0 sc0 sc1
ds_read_b128 v[76:79],v2 offset:30720
v_mul_f32 v93,v49,v49
v_mul_f32 v94,v50,v50
v_mfma_f32_16x16x32_bf16 v[32:35],v[176:179],v[56:59],v[32:35]
v_mul_f32 v95,v51,v51
v_fma_f32 v92,v118,v92,v117
v_mfma_f32_16x16x32_bf16 v[36:39],v[180:183],v[56:59],v[36:39]
s_waitcnt lgkmcnt(3)
v_mul_f32 v92,v48,v92
v_fma_f32 v93,v118,v93,v117
v_mfma_f32_16x16x32_bf16 v[28:31],v[184:187],v[60:63],v[28:31]
ds_read_b128 v[80:83],v2 offset:31744
v_mul_f32 v93,v49,v93
v_mfma_f32_16x16x32_bf16 v[32:35],v[188:191],v[60:63],v[32:35]
s_waitcnt vmcnt(6)
v_exp_f32 v92,v92
v_mfma_f32_16x16x32_bf16 v[36:39],v[192:195],v[60:63],v[36:39]
s_barrier
v_mfma_f32_16x16x32_bf16 v[28:31],v[196:199],v[64:67],v[28:31]
ds_read_b128 v[52:55],v2 offset:32768
ds_read_b32 v13,v7 offset:0
v_mfma_f32_16x16x32_bf16 v[32:35],v[200:203],v[64:67],v[32:35]
ds_read_b64 v[112:113],v11 offset:0
v_fma_f32 v92,v92,v119,v119
v_mfma_f32_16x16x32_bf16 v[36:39],v[204:207],v[64:67],v[36:39]
s_waitcnt lgkmcnt(5)
v_exp_f32 v93,v93
v_mfma_f32_16x16x32_bf16 v[28:31],v[208:211],v[68:71],v[28:31]
ds_read_b128 v[56:59],v2 offset:33792
v_fma_f32 v93,v93,v119,v119
v_mfma_f32_16x16x32_bf16 v[32:35],v[212:215],v[68:71],v[32:35]
s_waitcnt lgkmcnt(2)
v_rcp_f32 v92,v92
v_mfma_f32_16x16x32_bf16 v[36:39],v[216:219],v[68:71],v[36:39]
s_waitcnt lgkmcnt(1)
v_cmp_eq_u32 vcc,s60,v13
v_rcp_f32 v93,v93
v_fma_f32 v94,v118,v94,v117
v_mfma_f32_16x16x32_bf16 v[28:31],v[220:223],v[72:75],v[28:31]
s_cmp_eq_u32 vcc_lo,-1
ds_read_b128 v[60:63],v2 offset:34816
v_mul_f32 v94,v50,v94
v_mfma_f32_16x16x32_bf16 v[32:35],v[224:227],v[72:75],v[32:35]
s_cselect_b32 s67,s86,s59
v_exp_f32 v94,v94
v_mfma_f32_16x16x32_bf16 v[36:39],v[228:231],v[72:75],v[36:39]
v_fma_f32 v95,v118,v95,v117
v_mul_f32 v95,v51,v95
v_exp_f32 v95,v95
v_mfma_f32_16x16x32_bf16 v[28:31],v[232:235],v[76:79],v[28:31]
s_lshr_b32 s61,s67,0
v_fma_f32 v94,v94,v119,v119
v_fma_f32 v95,v95,v119,v119
v_rcp_f32 v94,v94
v_rcp_f32 v95,v95
v_mfma_f32_16x16x32_bf16 v[32:35],v[236:239],v[76:79],v[32:35]
s_xor_b32 s63,s63,65536
v_mul_f32 v48,v48,v92
v_mul_f32 v49,v49,v93
v_mul_f32 v50,v50,v94
v_mul_f32 v51,v51,v95
v_cvt_pk_bf16_f32 v100,v48,v49
v_cvt_pk_bf16_f32 v101,v50,v51
v_mfma_f32_16x16x32_bf16 v[36:39],v[240:243],v[76:79],v[36:39]
s_cmp_ge_u32 s66,s59
ds_read_b128 v[64:67],v2 offset:35840
buffer_store_dwordx2 v[100:101],v9,s[36:39],s73 offen offset:0 sc0 sc1
v_mfma_f32_16x16x32_bf16 v[28:31],v[244:247],v[80:83],v[28:31]
ds_read_b128 v[68:71],v2 offset:36864
v_mfma_f32_16x16x32_bf16 v[32:35],v[248:251],v[80:83],v[32:35]
s_waitcnt lgkmcnt(3)
v_mfma_f32_16x16x32_bf16 v[36:39],v[252:255],v[80:83],v[36:39]
s_cbranch_scc1 Lg3_slow1_%=
Lg3_d1w1_%=:
v_mfma_f32_16x16x32_bf16 v[40:43],a[0:3],v[52:55],0
s_mov_b32 m0,s62
ds_read_b128 v[72:75],v2 offset:37888)"
R"(
v_mul_f32 v116,s75,v112
v_mfma_f32_16x16x32_bf16 v[44:47],a[4:7],v[52:55],0
s_mul_i32 s73,s48,7
v_fma_f32 v113,v113,s75,v12
v_fma_f32 v113,-v116,v116,v113
v_mfma_f32_16x16x32_bf16 v[48:51],a[8:11],v[52:55],0
buffer_load_dwordx4 v0,s[36:39],s61 offen offset:0 sc1 lds
v_mfma_f32_16x16x32_bf16 v[40:43],a[12:15],v[56:59],v[40:43]
s_lshr_b32 s73,s73,1
ds_read_b128 v[76:79],v2 offset:38912
v_max_f32 v113,v113,v12
v_mfma_f32_16x16x32_bf16 v[44:47],a[16:19],v[56:59],v[44:47]
s_add_u32 s74,s53,s55
v_rsq_f32 v112,v113
v_mfma_f32_16x16x32_bf16 v[48:51],a[20:23],v[56:59],v[48:51]
s_waitcnt lgkmcnt(3)
v_mul_f32 v117,s76,v112
v_mul_f32 v118,v112,v112
v_mfma_f32_16x16x32_bf16 v[40:43],a[24:27],v[60:63],v[40:43]
buffer_load_dwordx4 v0,s[36:39],s61 offen offset:1024 sc1 lds
v_mfma_f32_16x16x32_bf16 v[44:47],a[28:31],v[60:63],v[44:47]
s_cmp_eq_u32 s74,s56
ds_read_b128 v[80:83],v2 offset:39936
v_mul_f32 v119,v113,v112
v_mfma_f32_16x16x32_bf16 v[48:51],a[32:35],v[60:63],v[48:51]
s_cselect_b32 s74,s57,s74
v_mul_f32 v118,s77,v118
v_mul_f32 v118,v118,v117
v_mfma_f32_16x16x32_bf16 v[40:43],a[36:39],v[64:67],v[40:43]
s_cmp_eq_u32 s74,s58
ds_read_b128 v[52:55],v2 offset:40960
v_fma_f32 v28,-v116,v16,v28
v_mfma_f32_16x16x32_bf16 v[44:47],a[40:43],v[64:67],v[44:47]
buffer_load_dwordx4 v0,s[36:39],s61 offen offset:2048 sc1 lds
v_mfma_f32_16x16x32_bf16 v[48:51],a[44:47],v[64:67],v[48:51]
s_waitcnt lgkmcnt(3)
v_fma_f32 v29,-v116,v17,v29
v_fma_f32 v30,-v116,v18,v30
v_mfma_f32_16x16x32_bf16 v[40:43],a[48:51],v[68:71],v[40:43]
s_cselect_b32 s74,s59,s74
ds_read_b128 v[56:59],v2 offset:41984
v_fma_f32 v31,-v116,v19,v31
v_mfma_f32_16x16x32_bf16 v[44:47],a[52:55],v[68:71],v[44:47]
s_cmp_ge_u32 s67,s59
v_mul_f32 v84,v28,v28
v_mul_f32 v85,v29,v29
v_mfma_f32_16x16x32_bf16 v[48:51],a[56:59],v[68:71],v[48:51]
buffer_load_dwordx4 v0,s[36:39],s61 offen offset:3072 sc1 lds
v_mfma_f32_16x16x32_bf16 v[40:43],a[60:63],v[72:75],v[40:43]
s_cselect_b32 s86,s50,s51
ds_read_b128 v[60:63],v2 offset:43008
v_mul_f32 v86,v30,v30
v_mfma_f32_16x16x32_bf16 v[44:47],a[64:67],v[72:75],v[44:47]
s_add_u32 m0,s62,16384
v_mul_f32 v87,v31,v31
v_fma_f32 v84,v118,v84,v117
v_mfma_f32_16x16x32_bf16 v[48:51],a[68:71],v[72:75],v[48:51]
s_waitcnt lgkmcnt(3)
v_mul_f32 v84,v28,v84
v_fma_f32 v85,v118,v85,v117
v_mfma_f32_16x16x32_bf16 v[40:43],a[72:75],v[76:79],v[40:43]
s_lshr_b32 s72,s61,5
ds_read_b128 v[64:67],v2 offset:44032
v_mul_f32 v85,v29,v85
v_mfma_f32_16x16x32_bf16 v[44:47],a[76:79],v[76:79],v[44:47]
s_mov_b32 s62,s64
v_exp_f32 v84,v84
v_mfma_f32_16x16x32_bf16 v[48:51],a[80:83],v[76:79],v[48:51]
s_mov_b64 s[44:45],s[46:47]
v_exp_f32 v85,v85
v_mfma_f32_16x16x32_bf16 v[40:43],a[84:87],v[80:83],v[40:43]
s_mov_b64 s[46:47],s[48:49]
ds_read_b128 v[68:71],v2 offset:45056
v_fma_f32 v84,v84,v119,v119
v_mfma_f32_16x16x32_bf16 v[44:47],a[88:91],v[80:83],v[44:47]
s_mov_b64 s[48:49],s[50:51]
v_rcp_f32 v84,v84
v_mfma_f32_16x16x32_bf16 v[48:51],a[92:95],v[80:83],v[48:51]
s_waitcnt lgkmcnt(3)
v_fma_f32 v85,v85,v119,v119
v_fma_f32 v86,v118,v86,v117
v_mfma_f32_16x16x32_bf16 v[40:43],a[96:99],v[52:55],v[40:43]
s_mov_b64 s[50:51],s[52:53]
ds_read_b128 v[72:75],v2 offset:46080
v_mul_f32 v86,v30,v86
v_mfma_f32_16x16x32_bf16 v[44:47],a[100:103],v[52:55],v[44:47]
s_mov_b32 s52,s74
v_exp_f32 v86,v86
v_mfma_f32_16x16x32_bf16 v[48:51],a[104:107],v[52:55],v[48:51]
v_rcp_f32 v85,v85
v_mfma_f32_16x16x32_bf16 v[40:43],a[108:111],v[56:59],v[40:43]
ds_read_b128 v[76:79],v2 offset:47104
v_fma_f32 v86,v86,v119,v119
v_mfma_f32_16x16x32_bf16 v[44:47],a[112:115],v[56:59],v[44:47]
v_rcp_f32 v86,v86
v_mfma_f32_16x16x32_bf16 v[48:51],a[116:119],v[56:59],v[48:51]
s_waitcnt lgkmcnt(3)
v_fma_f32 v87,v118,v87,v117
v_mul_f32 v87,v31,v87
v_mfma_f32_16x16x32_bf16 v[40:43],a[120:123],v[60:63],v[40:43]
ds_read_b128 v[80:83],v2 offset:48128
v_mul_f32 v28,v28,v84
v_mfma_f32_16x16x32_bf16 v[44:47],a[124:127],v[60:63],v[44:47]
s_waitcnt vmcnt(6)
v_exp_f32 v87,v87
v_mfma_f32_16x16x32_bf16 v[48:51],a[128:131],v[60:63],v[48:51]
s_barrier
v_mfma_f32_16x16x32_bf16 v[40:43],a[132:135],v[64:67],v[40:43]
ds_read_b128 v[52:55],v2 offset:49152
v_fma_f32 v87,v87,v119,v119
v_mfma_f32_16x16x32_bf16 v[44:47],a[136:139],v[64:67],v[44:47]
v_rcp_f32 v87,v87
v_mfma_f32_16x16x32_bf16 v[48:51],a[140:143],v[64:67],v[48:51]
s_waitcnt lgkmcnt(3)
v_add_f32_dpp v108,v108,v108 quad_perm:[1,0,3,2] row_mask:0xf bank_mask:0xf
v_mul_f32 v29,v29,v85
v_mfma_f32_16x16x32_bf16 v[40:43],a[144:147],v[68:71],v[40:43]
ds_read_b128 v[56:59],v2 offset:50176
v_add_f32_dpp v108,v108,v108 quad_perm:[2,3,0,1] row_mask:0xf bank_mask:0xf
v_mfma_f32_16x16x32_bf16 v[44:47],a[148:151],v[68:71],v[44:47]
s_nop 0
v_add_f32_dpp v108,v108,v108 row_half_mirror row_mask:0xf bank_mask:0xf
v_mul_f32 v30,v30,v86
v_mfma_f32_16x16x32_bf16 v[48:51],a[152:155],v[68:71],v[48:51]
v_mul_f32 v31,v31,v87
v_cvt_pk_bf16_f32 v96,v28,v29
v_mfma_f32_16x16x32_bf16 v[40:43],a[156:159],v[72:75],v[40:43]
ds_read_b128 v[60:63],v2 offset:51200
v_cvt_pk_bf16_f32 v97,v30,v31
v_mfma_f32_16x16x32_bf16 v[44:47],a[160:163],v[72:75],v[44:47]
v_fma_f32 v32,-v116,v20,v32
v_fma_f32 v33,-v116,v21,v33
v_mfma_f32_16x16x32_bf16 v[48:51],a[164:167],v[72:75],v[48:51]
s_waitcnt lgkmcnt(3)
v_fma_f32 v34,-v116,v22,v34
v_fma_f32 v35,-v116,v23,v35
v_mfma_f32_16x16x32_bf16 v[40:43],a[168:171],v[76:79],v[40:43]
ds_read_b128 v[64:67],v2 offset:52224
v_mul_f32 v88,v32,v32
v_mfma_f32_16x16x32_bf16 v[44:47],a[172:175],v[76:79],v[44:47]
v_mul_f32 v89,v33,v33
v_mul_f32 v90,v34,v34
v_mfma_f32_16x16x32_bf16 v[48:51],a[176:179],v[76:79],v[48:51]
buffer_load_dwordx4 v1,s[36:39],s61 offen offset:0 sc1 lds
v_mfma_f32_16x16x32_bf16 v[40:43],a[180:183],v[80:83],v[40:43]
ds_read_b128 v[68:71],v2 offset:53248
v_mul_f32 v91,v35,v35
v_mfma_f32_16x16x32_bf16 v[44:47],a[184:187],v[80:83],v[44:47]
ds_write_b32 v10,v108 offset:128
v_mfma_f32_16x16x32_bf16 v[48:51],a[188:191],v[80:83],v[48:51]
s_waitcnt lgkmcnt(4)
v_fma_f32 v88,v118,v88,v117
v_mul_f32 v88,v32,v88
v_mfma_f32_16x16x32_bf16 v[40:43],a[192:195],v[52:55],v[40:43]
buffer_load_dwordx4 v1,s[36:39],s61 offen offset:1024 sc1 lds
v_mfma_f32_16x16x32_bf16 v[44:47],a[196:199],v[52:55],v[44:47]
ds_read_b128 v[72:75],v2 offset:54272
v_fma_f32 v89,v118,v89,v117
v_mfma_f32_16x16x32_bf16 v[48:51],a[200:203],v[52:55],v[48:51]
v_exp_f32 v88,v88
v_mfma_f32_16x16x32_bf16 v[40:43],a[204:207],v[56:59],v[40:43]
ds_read_b128 v[76:79],v2 offset:55296
v_mul_f32 v89,v33,v89
v_mfma_f32_16x16x32_bf16 v[44:47],a[208:211],v[56:59],v[44:47]
buffer_load_dwordx4 v1,s[36:39],s61 offen offset:2048 sc1 lds
v_mfma_f32_16x16x32_bf16 v[48:51],a[212:215],v[56:59],v[48:51]
s_waitcnt lgkmcnt(4)
v_exp_f32 v89,v89
v_mfma_f32_16x16x32_bf16 v[40:43],a[216:219],v[60:63],v[40:43]
ds_read_b128 v[80:83],v2 offset:56320
v_fma_f32 v88,v88,v119,v119
v_mfma_f32_16x16x32_bf16 v[44:47],a[220:223],v[60:63],v[44:47]
v_rcp_f32 v88,v88
v_mfma_f32_16x16x32_bf16 v[48:51],a[224:227],v[60:63],v[48:51]
buffer_load_dwordx4 v1,s[36:39],s61 offen offset:3072 sc1 lds
v_mfma_f32_16x16x32_bf16 v[40:43],a[228:231],v[64:67],v[40:43]
buffer_load_dword v104,v8,s[36:39],s72 offen offset:0 sc1
v_mfma_f32_16x16x32_bf16 v[44:47],a[232:235],v[64:67],v[44:47]
ds_read_b128 v[52:55],v2 offset:57344
v_fma_f32 v89,v89,v119,v119
v_mfma_f32_16x16x32_bf16 v[48:51],a[236:239],v[64:67],v[48:51]
s_waitcnt lgkmcnt(3)
v_rcp_f32 v89,v89
v_mfma_f32_16x16x32_bf16 v[40:43],a[240:243],v[68:71],v[40:43]
ds_read_b128 v[56:59],v2 offset:58368
v_fma_f32 v90,v118,v90,v117
v_mfma_f32_16x16x32_bf16 v[44:47],a[244:247],v[68:71],v[44:47]
v_mul_f32 v90,v34,v90
v_fma_f32 v91,v118,v91,v117
v_mfma_f32_16x16x32_bf16 v[48:51],a[248:251],v[68:71],v[48:51]
v_exp_f32 v90,v90
v_mfma_f32_16x16x32_bf16 v[40:43],a[252:255],v[72:75],v[40:43]
ds_read_b128 v[60:63],v2 offset:59392
v_mul_f32 v91,v35,v91
v_mfma_f32_16x16x32_bf16 v[44:47],v[128:131],v[72:75],v[44:47]
v_exp_f32 v91,v91
v_mfma_f32_16x16x32_bf16 v[48:51],v[132:135],v[72:75],v[48:51]
s_waitcnt lgkmcnt(3)
v_fma_f32 v90,v90,v119,v119
v_fma_f32 v91,v91,v119,v119)"
R"(
v_mfma_f32_16x16x32_bf16 v[40:43],v[136:139],v[76:79],v[40:43]
ds_read_b128 v[64:67],v2 offset:60416
v_mul_f32 v32,v32,v88
v_mfma_f32_16x16x32_bf16 v[44:47],v[140:143],v[76:79],v[44:47]
v_rcp_f32 v90,v90
v_mfma_f32_16x16x32_bf16 v[48:51],v[144:147],v[76:79],v[48:51]
v_rcp_f32 v91,v91
v_mfma_f32_16x16x32_bf16 v[40:43],v[148:151],v[80:83],v[40:43]
ds_read_b128 v[68:71],v2 offset:61440
v_mul_f32 v33,v33,v89
v_mfma_f32_16x16x32_bf16 v[44:47],v[152:155],v[80:83],v[44:47]
v_mul_f32 v34,v34,v90
v_mul_f32 v35,v35,v91
v_mfma_f32_16x16x32_bf16 v[48:51],v[156:159],v[80:83],v[48:51]
s_waitcnt lgkmcnt(3)
v_cvt_pk_bf16_f32 v98,v32,v33
v_cvt_pk_bf16_f32 v99,v34,v35
v_mfma_f32_16x16x32_bf16 v[40:43],v[160:163],v[52:55],v[40:43]
ds_read_b128 v[72:75],v2 offset:62464
v_fma_f32 v36,-v116,v24,v36
v_mfma_f32_16x16x32_bf16 v[44:47],v[164:167],v[52:55],v[44:47]
v_fma_f32 v37,-v116,v25,v37
v_fma_f32 v38,-v116,v26,v38
v_mfma_f32_16x16x32_bf16 v[48:51],v[168:171],v[52:55],v[48:51]
v_fma_f32 v39,-v116,v27,v39
v_mul_f32 v92,v36,v36
v_mfma_f32_16x16x32_bf16 v[40:43],v[172:175],v[56:59],v[40:43]
buffer_store_dwordx4 v[96:99],v3,s[36:39],s73 offen offset:0 sc0 sc1
ds_read_b128 v[76:79],v2 offset:63488
v_mul_f32 v93,v37,v37
v_mul_f32 v94,v38,v38
v_mfma_f32_16x16x32_bf16 v[44:47],v[176:179],v[56:59],v[44:47]
v_mul_f32 v95,v39,v39
v_fma_f32 v92,v118,v92,v117
v_mfma_f32_16x16x32_bf16 v[48:51],v[180:183],v[56:59],v[48:51]
s_waitcnt lgkmcnt(3)
v_mul_f32 v92,v36,v92
v_fma_f32 v93,v118,v93,v117
v_mfma_f32_16x16x32_bf16 v[40:43],v[184:187],v[60:63],v[40:43]
ds_read_b128 v[80:83],v2 offset:64512
v_mul_f32 v93,v37,v93
v_mfma_f32_16x16x32_bf16 v[44:47],v[188:191],v[60:63],v[44:47]
s_waitcnt vmcnt(6)
v_xor_b32 v2,65536,v2
v_fma_f32 v94,v118,v94,v117
v_mfma_f32_16x16x32_bf16 v[48:51],v[192:195],v[60:63],v[48:51]
s_barrier
v_mfma_f32_16x16x32_bf16 v[40:43],v[196:199],v[64:67],v[40:43]
ds_read_b128 v[52:55],v2 offset:0
ds_read_b32 v13,v7 offset:256
v_mfma_f32_16x16x32_bf16 v[44:47],v[200:203],v[64:67],v[44:47]
ds_read_b64 v[112:113],v11 offset:128
v_fma_f32 v95,v118,v95,v117
v_mfma_f32_16x16x32_bf16 v[48:51],v[204:207],v[64:67],v[48:51]
s_waitcnt lgkmcnt(5)
v_exp_f32 v92,v92
v_mfma_f32_16x16x32_bf16 v[40:43],v[208:211],v[68:71],v[40:43]
ds_read_b128 v[56:59],v2 offset:1024
v_mul_f32 v94,v38,v94
v_mfma_f32_16x16x32_bf16 v[44:47],v[212:215],v[68:71],v[44:47]
s_waitcnt lgkmcnt(2)
v_exp_f32 v93,v93
v_mfma_f32_16x16x32_bf16 v[48:51],v[216:219],v[68:71],v[48:51]
s_waitcnt lgkmcnt(1)
v_cmp_eq_u32 vcc,s60,v13
v_exp_f32 v94,v94
v_mul_f32 v95,v39,v95
v_mfma_f32_16x16x32_bf16 v[40:43],v[220:223],v[72:75],v[40:43]
s_cmp_eq_u32 vcc_lo,-1
ds_read_b128 v[60:63],v2 offset:2048
v_fma_f32 v92,v92,v119,v119
v_mfma_f32_16x16x32_bf16 v[44:47],v[224:227],v[72:75],v[44:47]
s_cselect_b32 s66,s86,s59
v_exp_f32 v95,v95
v_mfma_f32_16x16x32_bf16 v[48:51],v[228:231],v[72:75],v[48:51]
v_rcp_f32 v92,v92
v_mfma_f32_16x16x32_bf16 v[40:43],v[232:235],v[76:79],v[40:43]
s_lshr_b32 s61,s66,0
v_fma_f32 v93,v93,v119,v119
v_fma_f32 v94,v94,v119,v119
v_fma_f32 v95,v95,v119,v119
v_rcp_f32 v93,v93
v_rcp_f32 v94,v94
v_rcp_f32 v95,v95
v_mfma_f32_16x16x32_bf16 v[44:47],v[236:239],v[76:79],v[44:47]
s_xor_b32 s64,s64,65536
v_mul_f32 v36,v36,v92
v_mul_f32 v37,v37,v93
v_mul_f32 v38,v38,v94
v_mul_f32 v39,v39,v95
v_cvt_pk_bf16_f32 v100,v36,v37
v_cvt_pk_bf16_f32 v101,v38,v39
v_mfma_f32_16x16x32_bf16 v[48:51],v[240:243],v[76:79],v[48:51]
s_cmp_lt_u32 s67,s59
ds_read_b128 v[64:67],v2 offset:3072
buffer_store_dwordx2 v[100:101],v9,s[36:39],s73 offen offset:0 sc0 sc1
v_mfma_f32_16x16x32_bf16 v[40:43],v[244:247],v[80:83],v[40:43]
ds_read_b128 v[68:71],v2 offset:4096
v_mfma_f32_16x16x32_bf16 v[44:47],v[248:251],v[80:83],v[44:47]
s_waitcnt lgkmcnt(3)
v_mfma_f32_16x16x32_bf16 v[48:51],v[252:255],v[80:83],v[48:51]
s_cbranch_scc1 Lg3_d0w1_%=
s_branch Lg3_slow0_%=
Lg3_d0w2_%=:
v_mfma_f32_16x16x32_bf16 v[28:31],a[0:3],v[52:55],0
s_mov_b32 m0,s62
ds_read_b128 v[72:75],v2 offset:5120
v_mul_f32 v116,s75,v112
v_mfma_f32_16x16x32_bf16 v[32:35],a[4:7],v[52:55],0
s_mul_i32 s73,s47,7
v_fma_f32 v113,v113,s75,v12
v_fma_f32 v113,-v116,v116,v113
v_mfma_f32_16x16x32_bf16 v[36:39],a[8:11],v[52:55],0
s_lshr_b32 s73,s73,1
v_max_f32 v113,v113,v12
v_rsq_f32 v112,v113
v_mfma_f32_16x16x32_bf16 v[28:31],a[12:15],v[56:59],v[28:31]
buffer_load_dwordx4 v0,s[36:39],s61 offen offset:0 sc1 lds
v_mfma_f32_16x16x32_bf16 v[32:35],a[16:19],v[56:59],v[32:35]
s_add_u32 s53,s52,s55
ds_read_b128 v[76:79],v2 offset:6144
v_mul_f32 v117,s76,v112
v_mfma_f32_16x16x32_bf16 v[36:39],a[20:23],v[56:59],v[36:39]
s_waitcnt lgkmcnt(3)
v_mul_f32 v118,v112,v112
v_mul_f32 v119,v113,v112
v_mfma_f32_16x16x32_bf16 v[28:31],a[24:27],v[60:63],v[28:31]
s_cmp_eq_u32 s53,s56
ds_read_b128 v[80:83],v2 offset:7168
v_mul_f32 v118,s77,v118
v_mfma_f32_16x16x32_bf16 v[32:35],a[28:31],v[60:63],v[32:35]
buffer_load_dwordx4 v0,s[36:39],s61 offen offset:1024 sc1 lds
v_mfma_f32_16x16x32_bf16 v[36:39],a[32:35],v[60:63],v[36:39]
s_cselect_b32 s53,s57,s53
v_mul_f32 v118,v118,v117
v_fma_f32 v40,-v116,v16,v40
v_mfma_f32_16x16x32_bf16 v[28:31],a[36:39],v[64:67],v[28:31]
s_cmp_eq_u32 s53,s58
ds_read_b128 v[52:55],v2 offset:8192
v_fma_f32 v41,-v116,v17,v41
v_mfma_f32_16x16x32_bf16 v[32:35],a[40:43],v[64:67],v[32:35]
s_cselect_b32 s53,s59,s53
v_fma_f32 v42,-v116,v18,v42
v_fma_f32 v43,-v116,v19,v43
v_mfma_f32_16x16x32_bf16 v[36:39],a[44:47],v[64:67],v[36:39]
s_waitcnt lgkmcnt(3)
buffer_load_dwordx4 v0,s[36:39],s61 offen offset:2048 sc1 lds
v_mfma_f32_16x16x32_bf16 v[28:31],a[48:51],v[68:71],v[28:31]
s_cmp_ge_u32 s66,s59
ds_read_b128 v[56:59],v2 offset:9216
v_mul_f32 v84,v40,v40
v_mfma_f32_16x16x32_bf16 v[32:35],a[52:55],v[68:71],v[32:35]
s_cselect_b32 s86,s49,s50
v_mul_f32 v85,v41,v41
v_mul_f32 v86,v42,v42
v_mfma_f32_16x16x32_bf16 v[36:39],a[56:59],v[68:71],v[36:39]
v_mul_f32 v87,v43,v43
v_fma_f32 v84,v118,v84,v117
v_mfma_f32_16x16x32_bf16 v[28:31],a[60:63],v[72:75],v[28:31]
buffer_load_dwordx4 v0,s[36:39],s61 offen offset:3072 sc1 lds
v_mfma_f32_16x16x32_bf16 v[32:35],a[64:67],v[72:75],v[32:35]
s_add_u32 m0,s62,16384
ds_read_b128 v[60:63],v2 offset:10240
v_mul_f32 v84,v40,v84
v_mfma_f32_16x16x32_bf16 v[36:39],a[68:71],v[72:75],v[36:39]
s_waitcnt lgkmcnt(3)
v_exp_f32 v84,v84
v_mfma_f32_16x16x32_bf16 v[28:31],a[72:75],v[76:79],v[28:31]
s_lshr_b32 s72,s61,5
ds_read_b128 v[64:67],v2 offset:11264
v_fma_f32 v84,v84,v119,v119
v_mfma_f32_16x16x32_bf16 v[32:35],a[76:79],v[76:79],v[32:35]
s_mov_b32 s62,s63
v_rcp_f32 v84,v84
v_mfma_f32_16x16x32_bf16 v[36:39],a[80:83],v[76:79],v[36:39]
v_fma_f32 v85,v118,v85,v117
v_mul_f32 v85,v41,v85
v_fma_f32 v86,v118,v86,v117
v_mfma_f32_16x16x32_bf16 v[28:31],a[84:87],v[80:83],v[28:31]
ds_read_b128 v[68:71],v2 offset:12288
v_mul_f32 v86,v42,v86
v_mfma_f32_16x16x32_bf16 v[32:35],a[88:91],v[80:83],v[32:35]
v_exp_f32 v85,v85
v_mfma_f32_16x16x32_bf16 v[36:39],a[92:95],v[80:83],v[36:39]
s_waitcnt lgkmcnt(3)
v_exp_f32 v86,v86
v_mfma_f32_16x16x32_bf16 v[28:31],a[96:99],v[52:55],v[28:31]
ds_read_b128 v[72:75],v2 offset:13312
v_fma_f32 v85,v85,v119,v119
v_mfma_f32_16x16x32_bf16 v[32:35],a[100:103],v[52:55],v[32:35]
v_rcp_f32 v85,v85
v_mfma_f32_16x16x32_bf16 v[36:39],a[104:107],v[52:55],v[36:39]
v_fma_f32 v86,v86,v119,v119
v_fma_f32 v87,v118,v87,v117
v_mfma_f32_16x16x32_bf16 v[28:31],a[108:111],v[56:59],v[28:31]
ds_read_b128 v[76:79],v2 offset:14336
v_mul_f32 v87,v43,v87
v_mfma_f32_16x16x32_bf16 v[32:35],a[112:115],v[56:59],v[32:35]
v_exp_f32 v87,v87
v_mfma_f32_16x16x32_bf16 v[36:39],a[116:119],v[56:59],v[36:39])"
R"(
s_waitcnt lgkmcnt(3)
v_rcp_f32 v86,v86
v_mfma_f32_16x16x32_bf16 v[28:31],a[120:123],v[60:63],v[28:31]
ds_read_b128 v[80:83],v2 offset:15360
v_fma_f32 v87,v87,v119,v119
v_mfma_f32_16x16x32_bf16 v[32:35],a[124:127],v[60:63],v[32:35]
s_waitcnt vmcnt(6)
v_rcp_f32 v87,v87
v_mfma_f32_16x16x32_bf16 v[36:39],a[128:131],v[60:63],v[36:39]
s_barrier
v_mfma_f32_16x16x32_bf16 v[28:31],a[132:135],v[64:67],v[28:31]
ds_read_b128 v[52:55],v2 offset:16384
v_add_f32_dpp v104,v104,v104 quad_perm:[1,0,3,2] row_mask:0xf bank_mask:0xf
v_mfma_f32_16x16x32_bf16 v[32:35],a[136:139],v[64:67],v[32:35]
s_nop 0
v_add_f32_dpp v104,v104,v104 quad_perm:[2,3,0,1] row_mask:0xf bank_mask:0xf
v_mul_f32 v40,v40,v84
v_mfma_f32_16x16x32_bf16 v[36:39],a[140:143],v[64:67],v[36:39]
s_waitcnt lgkmcnt(3)
v_add_f32_dpp v104,v104,v104 row_half_mirror row_mask:0xf bank_mask:0xf
v_mul_f32 v41,v41,v85
v_mfma_f32_16x16x32_bf16 v[28:31],a[144:147],v[68:71],v[28:31]
ds_read_b128 v[56:59],v2 offset:17408
v_mul_f32 v42,v42,v86
v_mfma_f32_16x16x32_bf16 v[32:35],a[148:151],v[68:71],v[32:35]
v_mul_f32 v43,v43,v87
v_cvt_pk_bf16_f32 v96,v40,v41
v_mfma_f32_16x16x32_bf16 v[36:39],a[152:155],v[68:71],v[36:39]
v_cvt_pk_bf16_f32 v97,v42,v43
v_fma_f32 v44,-v116,v20,v44
v_fma_f32 v45,-v116,v21,v45
v_mfma_f32_16x16x32_bf16 v[28:31],a[156:159],v[72:75],v[28:31]
ds_read_b128 v[60:63],v2 offset:18432
v_fma_f32 v46,-v116,v22,v46
v_mfma_f32_16x16x32_bf16 v[32:35],a[160:163],v[72:75],v[32:35]
v_fma_f32 v47,-v116,v23,v47
v_mul_f32 v88,v44,v44
v_mfma_f32_16x16x32_bf16 v[36:39],a[164:167],v[72:75],v[36:39]
s_waitcnt lgkmcnt(3)
v_mul_f32 v89,v45,v45
v_mul_f32 v90,v46,v46
v_mfma_f32_16x16x32_bf16 v[28:31],a[168:171],v[76:79],v[28:31]
ds_read_b128 v[64:67],v2 offset:19456
v_mul_f32 v91,v47,v47
v_mfma_f32_16x16x32_bf16 v[32:35],a[172:175],v[76:79],v[32:35]
v_fma_f32 v88,v118,v88,v117
v_mul_f32 v88,v44,v88
v_mfma_f32_16x16x32_bf16 v[36:39],a[176:179],v[76:79],v[36:39]
v_exp_f32 v88,v88
v_mfma_f32_16x16x32_bf16 v[28:31],a[180:183],v[80:83],v[28:31]
buffer_load_dwordx4 v1,s[36:39],s61 offen offset:0 sc1 lds
v_mfma_f32_16x16x32_bf16 v[32:35],a[184:187],v[80:83],v[32:35]
ds_write_b32 v10,v104 offset:0
v_mfma_f32_16x16x32_bf16 v[36:39],a[188:191],v[80:83],v[36:39]
s_waitcnt lgkmcnt(3)
ds_read_b128 v[68:71],v2 offset:20480
v_fma_f32 v88,v88,v119,v119
v_mfma_f32_16x16x32_bf16 v[28:31],a[192:195],v[52:55],v[28:31]
ds_read_b128 v[72:75],v2 offset:21504
v_fma_f32 v89,v118,v89,v117
v_mfma_f32_16x16x32_bf16 v[32:35],a[196:199],v[52:55],v[32:35]
buffer_load_dwordx4 v1,s[36:39],s61 offen offset:1024 sc1 lds
v_mfma_f32_16x16x32_bf16 v[36:39],a[200:203],v[52:55],v[36:39]
v_rcp_f32 v88,v88
v_mfma_f32_16x16x32_bf16 v[28:31],a[204:207],v[56:59],v[28:31]
ds_read_b128 v[76:79],v2 offset:22528
v_mul_f32 v89,v45,v89
v_mfma_f32_16x16x32_bf16 v[32:35],a[208:211],v[56:59],v[32:35]
v_exp_f32 v89,v89
v_mfma_f32_16x16x32_bf16 v[36:39],a[212:215],v[56:59],v[36:39]
s_waitcnt lgkmcnt(4)
buffer_load_dwordx4 v1,s[36:39],s61 offen offset:2048 sc1 lds
v_mfma_f32_16x16x32_bf16 v[28:31],a[216:219],v[60:63],v[28:31]
ds_read_b128 v[80:83],v2 offset:23552
v_fma_f32 v89,v89,v119,v119
v_mfma_f32_16x16x32_bf16 v[32:35],a[220:223],v[60:63],v[32:35]
v_rcp_f32 v89,v89
v_mfma_f32_16x16x32_bf16 v[36:39],a[224:227],v[60:63],v[36:39]
v_fma_f32 v90,v118,v90,v117
v_mul_f32 v90,v46,v90
v_fma_f32 v91,v118,v91,v117
v_mfma_f32_16x16x32_bf16 v[28:31],a[228:231],v[64:67],v[28:31]
buffer_load_dwordx4 v1,s[36:39],s61 offen offset:3072 sc1 lds
v_mfma_f32_16x16x32_bf16 v[32:35],a[232:235],v[64:67],v[32:35]
buffer_load_dword v108,v8,s[36:39],s72 offen offset:0 sc1
v_mfma_f32_16x16x32_bf16 v[36:39],a[236:239],v[64:67],v[36:39]
s_waitcnt lgkmcnt(2)
ds_read_b128 v[52:55],v2 offset:24576
v_mul_f32 v91,v47,v91
v_mfma_f32_16x16x32_bf16 v[28:31],a[240:243],v[68:71],v[28:31]
ds_read_b128 v[56:59],v2 offset:25600
v_mul_f32 v44,v44,v88
v_mfma_f32_16x16x32_bf16 v[32:35],a[244:247],v[68:71],v[32:35]
v_exp_f32 v90,v90
v_mfma_f32_16x16x32_bf16 v[36:39],a[248:251],v[68:71],v[36:39]
v_exp_f32 v91,v91
v_mfma_f32_16x16x32_bf16 v[28:31],a[252:255],v[72:75],v[28:31]
ds_read_b128 v[60:63],v2 offset:26624
v_fma_f32 v90,v90,v119,v119
v_mfma_f32_16x16x32_bf16 v[32:35],v[128:131],v[72:75],v[32:35]
v_rcp_f32 v90,v90
v_mfma_f32_16x16x32_bf16 v[36:39],v[132:135],v[72:75],v[36:39]
s_waitcnt lgkmcnt(3)
v_fma_f32 v91,v91,v119,v119
v_mul_f32 v45,v45,v89
v_mfma_f32_16x16x32_bf16 v[28:31],v[136:139],v[76:79],v[28:31]
ds_read_b128 v[64:67],v2 offset:27648
v_mul_f32 v46,v46,v90
v_mfma_f32_16x16x32_bf16 v[32:35],v[140:143],v[76:79],v[32:35]
v_rcp_f32 v91,v91
v_mfma_f32_16x16x32_bf16 v[36:39],v[144:147],v[76:79],v[36:39]
v_mul_f32 v47,v47,v91
v_cvt_pk_bf16_f32 v98,v44,v45
v_mfma_f32_16x16x32_bf16 v[28:31],v[148:151],v[80:83],v[28:31]
ds_read_b128 v[68:71],v2 offset:28672
v_cvt_pk_bf16_f32 v99,v46,v47
v_mfma_f32_16x16x32_bf16 v[32:35],v[152:155],v[80:83],v[32:35]
v_fma_f32 v48,-v116,v24,v48
v_fma_f32 v49,-v116,v25,v49
v_mfma_f32_16x16x32_bf16 v[36:39],v[156:159],v[80:83],v[36:39]
s_waitcnt lgkmcnt(3)
v_fma_f32 v50,-v116,v26,v50
v_fma_f32 v51,-v116,v27,v51
v_mfma_f32_16x16x32_bf16 v[28:31],v[160:163],v[52:55],v[28:31]
ds_read_b128 v[72:75],v2 offset:29696
v_mul_f32 v92,v48,v48
v_mfma_f32_16x16x32_bf16 v[32:35],v[164:167],v[52:55],v[32:35]
v_mul_f32 v93,v49,v49
v_mul_f32 v94,v50,v50
v_mfma_f32_16x16x32_bf16 v[36:39],v[168:171],v[52:55],v[36:39]
buffer_store_dwordx4 v[96:99],v3,s[36:39],s73 offen offset:0 sc0 sc1
v_mul_f32 v95,v51,v51
v_fma_f32 v92,v118,v92,v117
v_mul_f32 v92,v48,v92
v_mfma_f32_16x16x32_bf16 v[28:31],v[172:175],v[56:59],v[28:31]
ds_read_b128 v[76:79],v2 offset:30720
v_fma_f32 v93,v118,v93,v117
v_mfma_f32_16x16x32_bf16 v[32:35],v[176:179],v[56:59],v[32:35]
v_exp_f32 v92,v92
v_mfma_f32_16x16x32_bf16 v[36:39],v[180:183],v[56:59],v[36:39]
s_waitcnt lgkmcnt(3)
v_mul_f32 v93,v49,v93
v_fma_f32 v92,v92,v119,v119
v_mfma_f32_16x16x32_bf16 v[28:31],v[184:187],v[60:63],v[28:31]
ds_read_b128 v[80:83],v2 offset:31744
v_fma_f32 v94,v118,v94,v117
v_mfma_f32_16x16x32_bf16 v[32:35],v[188:191],v[60:63],v[32:35]
s_waitcnt vmcnt(6)
v_exp_f32 v93,v93
v_mfma_f32_16x16x32_bf16 v[36:39],v[192:195],v[60:63],v[36:39]
s_barrier
v_mfma_f32_16x16x32_bf16 v[28:31],v[196:199],v[64:67],v[28:31]
ds_read_b128 v[52:55],v2 offset:32768
ds_read_b32 v13,v7 offset:0
v_mfma_f32_16x16x32_bf16 v[32:35],v[200:203],v[64:67],v[32:35]
ds_read_b64 v[112:113],v11 offset:0
v_mul_f32 v94,v50,v94
v_mfma_f32_16x16x32_bf16 v[36:39],v[204:207],v[64:67],v[36:39]
s_waitcnt lgkmcnt(5)
v_exp_f32 v94,v94
v_mfma_f32_16x16x32_bf16 v[28:31],v[208:211],v[68:71],v[28:31]
ds_read_b128 v[56:59],v2 offset:33792
v_fma_f32 v93,v93,v119,v119
v_mfma_f32_16x16x32_bf16 v[32:35],v[212:215],v[68:71],v[32:35]
s_waitcnt lgkmcnt(2)
v_rcp_f32 v92,v92
v_mfma_f32_16x16x32_bf16 v[36:39],v[216:219],v[68:71],v[36:39]
s_waitcnt lgkmcnt(1)
v_cmp_eq_u32 vcc,s60,v13
v_rcp_f32 v93,v93
v_fma_f32 v94,v94,v119,v119
v_mfma_f32_16x16x32_bf16 v[28:31],v[220:223],v[72:75],v[28:31]
s_cmp_eq_u32 vcc_lo,-1
ds_read_b128 v[60:63],v2 offset:34816
v_fma_f32 v95,v118,v95,v117
v_mfma_f32_16x16x32_bf16 v[32:35],v[224:227],v[72:75],v[32:35]
s_cselect_b32 s67,s86,s59
v_mul_f32 v95,v51,v95
v_exp_f32 v95,v95
v_mfma_f32_16x16x32_bf16 v[36:39],v[228:231],v[72:75],v[36:39]
v_fma_f32 v95,v95,v119,v119
v_rcp_f32 v94,v94
v_rcp_f32 v95,v95
v_mfma_f32_16x16x32_bf16 v[28:31],v[232:235],v[76:79],v[28:31]
s_lshr_b32 s61,s67,0
v_mul_f32 v48,v48,v92
v_mul_f32 v49,v49,v93
v_mul_f32 v50,v50,v94
v_mul_f32 v51,v51,v95
v_cvt_pk_bf16_f32 v100,v48,v49
v_cvt_pk_bf16_f32 v101,v50,v51
v_mfma_f32_16x16x32_bf16 v[32:35],v[236:239],v[76:79],v[32:35]
buffer_store_dwordx2 v[100:101],v9,s[36:39],s73 offen offset:0 sc0 sc1
ds_read_b128 v[64:67],v2 offset:35840
v_mfma_f32_16x16x32_bf16 v[36:39],v[240:243],v[76:79],v[36:39]
s_xor_b32 s63,s63,65536
s_cmp_ge_u32 s66,s59
v_mfma_f32_16x16x32_bf16 v[28:31],v[244:247],v[80:83],v[28:31]
ds_read_b128 v[68:71],v2 offset:36864
v_mfma_f32_16x16x32_bf16 v[32:35],v[248:251],v[80:83],v[32:35])"
R"(
s_waitcnt lgkmcnt(3)
v_mfma_f32_16x16x32_bf16 v[36:39],v[252:255],v[80:83],v[36:39]
s_cbranch_scc1 Lg3_slow1_%=
Lg3_d1w2_%=:
v_mfma_f32_16x16x32_bf16 v[40:43],a[0:3],v[52:55],0
s_mov_b32 m0,s62
ds_read_b128 v[72:75],v2 offset:37888
v_mul_f32 v116,s75,v112
v_mfma_f32_16x16x32_bf16 v[44:47],a[4:7],v[52:55],0
s_mul_i32 s73,s48,7
v_fma_f32 v113,v113,s75,v12
v_fma_f32 v113,-v116,v116,v113
v_mfma_f32_16x16x32_bf16 v[48:51],a[8:11],v[52:55],0
s_lshr_b32 s73,s73,1
v_max_f32 v113,v113,v12
v_rsq_f32 v112,v113
v_mfma_f32_16x16x32_bf16 v[40:43],a[12:15],v[56:59],v[40:43]
buffer_load_dwordx4 v0,s[36:39],s61 offen offset:0 sc1 lds
v_mfma_f32_16x16x32_bf16 v[44:47],a[16:19],v[56:59],v[44:47]
s_add_u32 s74,s53,s55
ds_read_b128 v[76:79],v2 offset:38912
v_mul_f32 v117,s76,v112
v_mfma_f32_16x16x32_bf16 v[48:51],a[20:23],v[56:59],v[48:51]
s_waitcnt lgkmcnt(3)
v_mul_f32 v118,v112,v112
v_mul_f32 v119,v113,v112
v_mfma_f32_16x16x32_bf16 v[40:43],a[24:27],v[60:63],v[40:43]
s_cmp_eq_u32 s74,s56
ds_read_b128 v[80:83],v2 offset:39936
v_mul_f32 v118,s77,v118
v_mfma_f32_16x16x32_bf16 v[44:47],a[28:31],v[60:63],v[44:47]
buffer_load_dwordx4 v0,s[36:39],s61 offen offset:1024 sc1 lds
v_mfma_f32_16x16x32_bf16 v[48:51],a[32:35],v[60:63],v[48:51]
s_cselect_b32 s74,s57,s74
v_mul_f32 v118,v118,v117
v_fma_f32 v28,-v116,v16,v28
v_mfma_f32_16x16x32_bf16 v[40:43],a[36:39],v[64:67],v[40:43]
s_cmp_eq_u32 s74,s58
ds_read_b128 v[52:55],v2 offset:40960
v_fma_f32 v29,-v116,v17,v29
v_mfma_f32_16x16x32_bf16 v[44:47],a[40:43],v[64:67],v[44:47]
s_cselect_b32 s74,s59,s74
v_fma_f32 v30,-v116,v18,v30
v_fma_f32 v31,-v116,v19,v31
v_mfma_f32_16x16x32_bf16 v[48:51],a[44:47],v[64:67],v[48:51]
s_waitcnt lgkmcnt(3)
buffer_load_dwordx4 v0,s[36:39],s61 offen offset:2048 sc1 lds
v_mfma_f32_16x16x32_bf16 v[40:43],a[48:51],v[68:71],v[40:43]
s_cmp_ge_u32 s67,s59
ds_read_b128 v[56:59],v2 offset:41984
v_mul_f32 v84,v28,v28
v_mfma_f32_16x16x32_bf16 v[44:47],a[52:55],v[68:71],v[44:47]
s_cselect_b32 s86,s50,s51
v_mul_f32 v85,v29,v29
v_mul_f32 v86,v30,v30
v_mfma_f32_16x16x32_bf16 v[48:51],a[56:59],v[68:71],v[48:51]
s_mov_b64 s[44:45],s[46:47]
v_mul_f32 v87,v31,v31
v_fma_f32 v84,v118,v84,v117
v_mfma_f32_16x16x32_bf16 v[40:43],a[60:63],v[72:75],v[40:43]
buffer_load_dwordx4 v0,s[36:39],s61 offen offset:3072 sc1 lds
v_mfma_f32_16x16x32_bf16 v[44:47],a[64:67],v[72:75],v[44:47]
s_add_u32 m0,s62,16384
ds_read_b128 v[60:63],v2 offset:43008
v_mul_f32 v84,v28,v84
v_mfma_f32_16x16x32_bf16 v[48:51],a[68:71],v[72:75],v[48:51]
s_waitcnt lgkmcnt(3)
v_exp_f32 v84,v84
v_fma_f32 v85,v118,v85,v117
v_mfma_f32_16x16x32_bf16 v[40:43],a[72:75],v[76:79],v[40:43]
s_lshr_b32 s72,s61,5
ds_read_b128 v[64:67],v2 offset:44032
v_mul_f32 v85,v29,v85
v_mfma_f32_16x16x32_bf16 v[44:47],a[76:79],v[76:79],v[44:47]
s_mov_b32 s62,s64
v_exp_f32 v85,v85
v_mfma_f32_16x16x32_bf16 v[48:51],a[80:83],v[76:79],v[48:51]
s_mov_b64 s[46:47],s[48:49]
v_fma_f32 v84,v84,v119,v119
v_fma_f32 v85,v85,v119,v119
v_mfma_f32_16x16x32_bf16 v[40:43],a[84:87],v[80:83],v[40:43]
s_mov_b64 s[48:49],s[50:51]
ds_read_b128 v[68:71],v2 offset:45056
v_fma_f32 v86,v118,v86,v117
v_mfma_f32_16x16x32_bf16 v[44:47],a[88:91],v[80:83],v[44:47]
s_mov_b64 s[50:51],s[52:53]
v_rcp_f32 v84,v84
v_mfma_f32_16x16x32_bf16 v[48:51],a[92:95],v[80:83],v[48:51]
s_waitcnt lgkmcnt(3)
v_rcp_f32 v85,v85
v_mfma_f32_16x16x32_bf16 v[40:43],a[96:99],v[52:55],v[40:43]
s_mov_b32 s52,s74
ds_read_b128 v[72:75],v2 offset:46080
v_mul_f32 v86,v30,v86
v_mfma_f32_16x16x32_bf16 v[44:47],a[100:103],v[52:55],v[44:47]
v_exp_f32 v86,v86
v_mfma_f32_16x16x32_bf16 v[48:51],a[104:107],v[52:55],v[48:51]
v_fma_f32 v86,v86,v119,v119
v_fma_f32 v87,v118,v87,v117
v_mfma_f32_16x16x32_bf16 v[40:43],a[108:111],v[56:59],v[40:43]
ds_read_b128 v[76:79],v2 offset:47104
v_mul_f32 v87,v31,v87
v_mfma_f32_16x16x32_bf16 v[44:47],a[112:115],v[56:59],v[44:47]
v_exp_f32 v87,v87
v_mfma_f32_16x16x32_bf16 v[48:51],a[116:119],v[56:59],v[48:51]
s_waitcnt lgkmcnt(3)
v_rcp_f32 v86,v86
v_mfma_f32_16x16x32_bf16 v[40:43],a[120:123],v[60:63],v[40:43]
ds_read_b128 v[80:83],v2 offset:48128
v_fma_f32 v87,v87,v119,v119
v_mfma_f32_16x16x32_bf16 v[44:47],a[124:127],v[60:63],v[44:47]
s_waitcnt vmcnt(6)
v_rcp_f32 v87,v87
v_mfma_f32_16x16x32_bf16 v[48:51],a[128:131],v[60:63],v[48:51]
s_barrier
v_mfma_f32_16x16x32_bf16 v[40:43],a[132:135],v[64:67],v[40:43]
ds_read_b128 v[52:55],v2 offset:49152
v_add_f32_dpp v108,v108,v108 quad_perm:[1,0,3,2] row_mask:0xf bank_mask:0xf
v_mfma_f32_16x16x32_bf16 v[44:47],a[136:139],v[64:67],v[44:47]
s_nop 0
v_add_f32_dpp v108,v108,v108 quad_perm:[2,3,0,1] row_mask:0xf bank_mask:0xf
v_mul_f32 v28,v28,v84
v_mfma_f32_16x16x32_bf16 v[48:51],a[140:143],v[64:67],v[48:51]
s_waitcnt lgkmcnt(3)
v_add_f32_dpp v108,v108,v108 row_half_mirror row_mask:0xf bank_mask:0xf
v_mul_f32 v29,v29,v85
v_mfma_f32_16x16x32_bf16 v[40:43],a[144:147],v[68:71],v[40:43]
ds_read_b128 v[56:59],v2 offset:50176
v_mul_f32 v30,v30,v86
v_mfma_f32_16x16x32_bf16 v[44:47],a[148:151],v[68:71],v[44:47]
v_mul_f32 v31,v31,v87
v_cvt_pk_bf16_f32 v96,v28,v29
v_mfma_f32_16x16x32_bf16 v[48:51],a[152:155],v[68:71],v[48:51]
v_cvt_pk_bf16_f32 v97,v30,v31
v_fma_f32 v32,-v116,v20,v32
v_mfma_f32_16x16x32_bf16 v[40:43],a[156:159],v[72:75],v[40:43]
ds_read_b128 v[60:63],v2 offset:51200
v_fma_f32 v33,-v116,v21,v33
v_mfma_f32_16x16x32_bf16 v[44:47],a[160:163],v[72:75],v[44:47]
v_fma_f32 v34,-v116,v22,v34
v_fma_f32 v35,-v116,v23,v35
v_mfma_f32_16x16x32_bf16 v[48:51],a[164:167],v[72:75],v[48:51]
s_waitcnt lgkmcnt(3)
v_mul_f32 v88,v32,v32
v_mul_f32 v89,v33,v33
v_mfma_f32_16x16x32_bf16 v[40:43],a[168:171],v[76:79],v[40:43]
ds_read_b128 v[64:67],v2 offset:52224
v_mul_f32 v90,v34,v34
v_mfma_f32_16x16x32_bf16 v[44:47],a[172:175],v[76:79],v[44:47]
v_mul_f32 v91,v35,v35
v_fma_f32 v88,v118,v88,v117
v_mfma_f32_16x16x32_bf16 v[48:51],a[176:179],v[76:79],v[48:51]
v_mul_f32 v88,v32,v88
v_fma_f32 v89,v118,v89,v117
v_mfma_f32_16x16x32_bf16 v[40:43],a[180:183],v[80:83],v[40:43]
buffer_load_dwordx4 v1,s[36:39],s61 offen offset:0 sc1 lds
v_mfma_f32_16x16x32_bf16 v[44:47],a[184:187],v[80:83],v[44:47]
ds_write_b32 v10,v108 offset:128
v_mfma_f32_16x16x32_bf16 v[48:51],a[188:191],v[80:83],v[48:51]
s_waitcnt lgkmcnt(3)
ds_read_b128 v[68:71],v2 offset:53248
v_exp_f32 v88,v88
v_mfma_f32_16x16x32_bf16 v[40:43],a[192:195],v[52:55],v[40:43]
ds_read_b128 v[72:75],v2 offset:54272
v_mul_f32 v89,v33,v89
v_mfma_f32_16x16x32_bf16 v[44:47],a[196:199],v[52:55],v[44:47]
buffer_load_dwordx4 v1,s[36:39],s61 offen offset:1024 sc1 lds
v_mfma_f32_16x16x32_bf16 v[48:51],a[200:203],v[52:55],v[48:51]
v_exp_f32 v89,v89
v_mfma_f32_16x16x32_bf16 v[40:43],a[204:207],v[56:59],v[40:43]
ds_read_b128 v[76:79],v2 offset:55296
v_fma_f32 v88,v88,v119,v119
v_mfma_f32_16x16x32_bf16 v[44:47],a[208:211],v[56:59],v[44:47]
v_rcp_f32 v88,v88
v_mfma_f32_16x16x32_bf16 v[48:51],a[212:215],v[56:59],v[48:51]
s_waitcnt lgkmcnt(4)
buffer_load_dwordx4 v1,s[36:39],s61 offen offset:2048 sc1 lds
v_mfma_f32_16x16x32_bf16 v[40:43],a[216:219],v[60:63],v[40:43]
ds_read_b128 v[80:83],v2 offset:56320
v_fma_f32 v89,v89,v119,v119
v_mfma_f32_16x16x32_bf16 v[44:47],a[220:223],v[60:63],v[44:47]
v_rcp_f32 v89,v89
v_mfma_f32_16x16x32_bf16 v[48:51],a[224:227],v[60:63],v[48:51]
v_fma_f32 v90,v118,v90,v117
v_mul_f32 v90,v34,v90
v_mfma_f32_16x16x32_bf16 v[40:43],a[228:231],v[64:67],v[40:43]
buffer_load_dwordx4 v1,s[36:39],s61 offen offset:3072 sc1 lds
v_mfma_f32_16x16x32_bf16 v[44:47],a[232:235],v[64:67],v[44:47]
buffer_load_dword v104,v8,s[36:39],s72 offen offset:0 sc1
v_mfma_f32_16x16x32_bf16 v[48:51],a[236:239],v[64:67],v[48:51]
s_waitcnt lgkmcnt(2)
ds_read_b128 v[52:55],v2 offset:57344
v_fma_f32 v91,v118,v91,v117
v_mfma_f32_16x16x32_bf16 v[40:43],a[240:243],v[68:71],v[40:43]
ds_read_b128 v[56:59],v2 offset:58368
v_mul_f32 v91,v35,v91
v_mfma_f32_16x16x32_bf16 v[44:47],a[244:247],v[68:71],v[44:47]
v_exp_f32 v90,v90
v_mfma_f32_16x16x32_bf16 v[48:51],a[248:251],v[68:71],v[48:51]
v_exp_f32 v91,v91)"
R"(
v_fma_f32 v90,v90,v119,v119
v_mfma_f32_16x16x32_bf16 v[40:43],a[252:255],v[72:75],v[40:43]
ds_read_b128 v[60:63],v2 offset:59392
v_fma_f32 v91,v91,v119,v119
v_mfma_f32_16x16x32_bf16 v[44:47],v[128:131],v[72:75],v[44:47]
v_rcp_f32 v90,v90
v_mfma_f32_16x16x32_bf16 v[48:51],v[132:135],v[72:75],v[48:51]
s_waitcnt lgkmcnt(3)
v_rcp_f32 v91,v91
v_mfma_f32_16x16x32_bf16 v[40:43],v[136:139],v[76:79],v[40:43]
ds_read_b128 v[64:67],v2 offset:60416
v_mul_f32 v32,v32,v88
v_mfma_f32_16x16x32_bf16 v[44:47],v[140:143],v[76:79],v[44:47]
v_mul_f32 v33,v33,v89
v_mul_f32 v34,v34,v90
v_mfma_f32_16x16x32_bf16 v[48:51],v[144:147],v[76:79],v[48:51]
v_mul_f32 v35,v35,v91
v_cvt_pk_bf16_f32 v98,v32,v33
v_mfma_f32_16x16x32_bf16 v[40:43],v[148:151],v[80:83],v[40:43]
ds_read_b128 v[68:71],v2 offset:61440
v_cvt_pk_bf16_f32 v99,v34,v35
v_mfma_f32_16x16x32_bf16 v[44:47],v[152:155],v[80:83],v[44:47]
v_fma_f32 v36,-v116,v24,v36
v_fma_f32 v37,-v116,v25,v37
v_mfma_f32_16x16x32_bf16 v[48:51],v[156:159],v[80:83],v[48:51]
s_waitcnt lgkmcnt(3)
v_fma_f32 v38,-v116,v26,v38
v_fma_f32 v39,-v116,v27,v39
v_mfma_f32_16x16x32_bf16 v[40:43],v[160:163],v[52:55],v[40:43]
ds_read_b128 v[72:75],v2 offset:62464
v_mul_f32 v92,v36,v36
v_mfma_f32_16x16x32_bf16 v[44:47],v[164:167],v[52:55],v[44:47]
v_mul_f32 v93,v37,v37
v_mul_f32 v94,v38,v38
v_mfma_f32_16x16x32_bf16 v[48:51],v[168:171],v[52:55],v[48:51]
buffer_store_dwordx4 v[96:99],v3,s[36:39],s73 offen offset:0 sc0 sc1
v_mul_f32 v95,v39,v39
v_fma_f32 v92,v118,v92,v117
v_mul_f32 v92,v36,v92
v_mfma_f32_16x16x32_bf16 v[40:43],v[172:175],v[56:59],v[40:43]
ds_read_b128 v[76:79],v2 offset:63488
v_fma_f32 v93,v118,v93,v117
v_mfma_f32_16x16x32_bf16 v[44:47],v[176:179],v[56:59],v[44:47]
v_exp_f32 v92,v92
v_mfma_f32_16x16x32_bf16 v[48:51],v[180:183],v[56:59],v[48:51]
s_waitcnt lgkmcnt(3)
v_mul_f32 v93,v37,v93
v_exp_f32 v93,v93
v_mfma_f32_16x16x32_bf16 v[40:43],v[184:187],v[60:63],v[40:43]
ds_read_b128 v[80:83],v2 offset:64512
v_fma_f32 v92,v92,v119,v119
v_mfma_f32_16x16x32_bf16 v[44:47],v[188:191],v[60:63],v[44:47]
s_waitcnt vmcnt(6)
v_xor_b32 v2,65536,v2
v_fma_f32 v93,v93,v119,v119
v_mfma_f32_16x16x32_bf16 v[48:51],v[192:195],v[60:63],v[48:51]
s_barrier
v_mfma_f32_16x16x32_bf16 v[40:43],v[196:199],v[64:67],v[40:43]
ds_read_b128 v[52:55],v2 offset:0
ds_read_b32 v13,v7 offset:256
v_mfma_f32_16x16x32_bf16 v[44:47],v[200:203],v[64:67],v[44:47]
ds_read_b64 v[112:113],v11 offset:128
v_fma_f32 v94,v118,v94,v117
v_mfma_f32_16x16x32_bf16 v[48:51],v[204:207],v[64:67],v[48:51]
s_waitcnt lgkmcnt(5)
v_rcp_f32 v92,v92
v_mfma_f32_16x16x32_bf16 v[40:43],v[208:211],v[68:71],v[40:43]
ds_read_b128 v[56:59],v2 offset:1024
v_mul_f32 v94,v38,v94
v_mfma_f32_16x16x32_bf16 v[44:47],v[212:215],v[68:71],v[44:47]
s_waitcnt lgkmcnt(2)
v_exp_f32 v94,v94
v_mfma_f32_16x16x32_bf16 v[48:51],v[216:219],v[68:71],v[48:51]
s_waitcnt lgkmcnt(1)
v_cmp_eq_u32 vcc,s60,v13
v_rcp_f32 v93,v93
v_fma_f32 v94,v94,v119,v119
v_mfma_f32_16x16x32_bf16 v[40:43],v[220:223],v[72:75],v[40:43]
s_cmp_eq_u32 vcc_lo,-1
ds_read_b128 v[60:63],v2 offset:2048
v_fma_f32 v95,v118,v95,v117
v_mfma_f32_16x16x32_bf16 v[44:47],v[224:227],v[72:75],v[44:47]
s_cselect_b32 s66,s86,s59
v_mul_f32 v95,v39,v95
v_exp_f32 v95,v95
v_mfma_f32_16x16x32_bf16 v[48:51],v[228:231],v[72:75],v[48:51]
v_fma_f32 v95,v95,v119,v119
v_rcp_f32 v94,v94
v_rcp_f32 v95,v95
v_mfma_f32_16x16x32_bf16 v[40:43],v[232:235],v[76:79],v[40:43]
s_lshr_b32 s61,s66,0
v_mul_f32 v36,v36,v92
v_mul_f32 v37,v37,v93
v_mul_f32 v38,v38,v94
v_mul_f32 v39,v39,v95
v_cvt_pk_bf16_f32 v100,v36,v37
v_cvt_pk_bf16_f32 v101,v38,v39
v_mfma_f32_16x16x32_bf16 v[44:47],v[236:239],v[76:79],v[44:47]
buffer_store_dwordx2 v[100:101],v9,s[36:39],s73 offen offset:0 sc0 sc1
ds_read_b128 v[64:67],v2 offset:3072
v_mfma_f32_16x16x32_bf16 v[48:51],v[240:243],v[76:79],v[48:51]
s_xor_b32 s64,s64,65536
s_cmp_lt_u32 s67,s59
v_mfma_f32_16x16x32_bf16 v[40:43],v[244:247],v[80:83],v[40:43]
ds_read_b128 v[68:71],v2 offset:4096
v_mfma_f32_16x16x32_bf16 v[44:47],v[248:251],v[80:83],v[44:47]
s_waitcnt lgkmcnt(3)
v_mfma_f32_16x16x32_bf16 v[48:51],v[252:255],v[80:83],v[48:51]
s_cbranch_scc1 Lg3_d0w2_%=
s_branch Lg3_slow0_%=
Lg3_d0w3_%=:
v_mfma_f32_16x16x32_bf16 v[28:31],a[0:3],v[52:55],0
s_mov_b32 m0,s62
ds_read_b128 v[72:75],v2 offset:5120
v_mul_f32 v116,s75,v112
v_mfma_f32_16x16x32_bf16 v[32:35],a[4:7],v[52:55],0
s_mul_i32 s73,s47,7
v_fma_f32 v113,v113,s75,v12
v_fma_f32 v113,-v116,v116,v113
v_mfma_f32_16x16x32_bf16 v[36:39],a[8:11],v[52:55],0
s_lshr_b32 s73,s73,1
v_max_f32 v113,v113,v12
v_rsq_f32 v112,v113
v_mfma_f32_16x16x32_bf16 v[28:31],a[12:15],v[56:59],v[28:31]
s_add_u32 s53,s52,s55
ds_read_b128 v[76:79],v2 offset:6144
v_mul_f32 v117,s76,v112
v_mfma_f32_16x16x32_bf16 v[32:35],a[16:19],v[56:59],v[32:35]
buffer_load_dwordx4 v0,s[36:39],s61 offen offset:0 sc1 lds
v_mfma_f32_16x16x32_bf16 v[36:39],a[20:23],v[56:59],v[36:39]
s_waitcnt lgkmcnt(3)
v_mul_f32 v118,v112,v112
v_mul_f32 v119,v113,v112
v_mfma_f32_16x16x32_bf16 v[28:31],a[24:27],v[60:63],v[28:31]
s_cmp_eq_u32 s53,s56
ds_read_b128 v[80:83],v2 offset:7168
v_mul_f32 v118,s77,v118
v_mfma_f32_16x16x32_bf16 v[32:35],a[28:31],v[60:63],v[32:35]
s_cselect_b32 s53,s57,s53
v_mul_f32 v118,v118,v117
v_fma_f32 v40,-v116,v16,v40
v_mfma_f32_16x16x32_bf16 v[36:39],a[32:35],v[60:63],v[36:39]
buffer_load_dwordx4 v0,s[36:39],s61 offen offset:1024 sc1 lds
v_mfma_f32_16x16x32_bf16 v[28:31],a[36:39],v[64:67],v[28:31]
s_cmp_eq_u32 s53,s58
ds_read_b128 v[52:55],v2 offset:8192
v_fma_f32 v41,-v116,v17,v41
v_mfma_f32_16x16x32_bf16 v[32:35],a[40:43],v[64:67],v[32:35]
s_cselect_b32 s53,s59,s53
v_fma_f32 v42,-v116,v18,v42
v_fma_f32 v43,-v116,v19,v43
v_mfma_f32_16x16x32_bf16 v[36:39],a[44:47],v[64:67],v[36:39]
s_waitcnt lgkmcnt(3)
v_mul_f32 v84,v40,v40
v_mul_f32 v85,v41,v41
v_mfma_f32_16x16x32_bf16 v[28:31],a[48:51],v[68:71],v[28:31]
buffer_load_dwordx4 v0,s[36:39],s61 offen offset:2048 sc1 lds
v_mfma_f32_16x16x32_bf16 v[32:35],a[52:55],v[68:71],v[32:35]
s_cmp_ge_u32 s66,s59
ds_read_b128 v[56:59],v2 offset:9216
v_mul_f32 v86,v42,v42
v_mfma_f32_16x16x32_bf16 v[36:39],a[56:59],v[68:71],v[36:39]
s_cselect_b32 s86,s49,s50
v_mul_f32 v87,v43,v43
v_fma_f32 v84,v118,v84,v117
v_mfma_f32_16x16x32_bf16 v[28:31],a[60:63],v[72:75],v[28:31]
s_mul_i32 s70,s46,5
ds_read_b128 v[60:63],v2 offset:10240
v_mul_f32 v84,v40,v84
v_mfma_f32_16x16x32_bf16 v[32:35],a[64:67],v[72:75],v[32:35]
buffer_load_dwordx4 v0,s[36:39],s61 offen offset:3072 sc1 lds
v_mfma_f32_16x16x32_bf16 v[36:39],a[68:71],v[72:75],v[36:39]
s_waitcnt lgkmcnt(3)
v_exp_f32 v84,v84
v_mfma_f32_16x16x32_bf16 v[28:31],a[72:75],v[76:79],v[28:31]
s_add_u32 m0,s62,16384
ds_read_b128 v[64:67],v2 offset:11264
v_fma_f32 v84,v84,v119,v119
v_mfma_f32_16x16x32_bf16 v[32:35],a[76:79],v[76:79],v[32:35]
s_lshr_b32 s72,s61,5
v_rcp_f32 v84,v84
v_mfma_f32_16x16x32_bf16 v[36:39],a[80:83],v[76:79],v[36:39]
s_lshr_b32 s70,s70,6
v_fma_f32 v85,v118,v85,v117
v_mul_f32 v85,v41,v85
v_mfma_f32_16x16x32_bf16 v[28:31],a[84:87],v[80:83],v[28:31]
s_mov_b32 s62,s63
ds_read_b128 v[68:71],v2 offset:12288
v_fma_f32 v86,v118,v86,v117
v_mfma_f32_16x16x32_bf16 v[32:35],a[88:91],v[80:83],v[32:35]
v_exp_f32 v85,v85
v_mfma_f32_16x16x32_bf16 v[36:39],a[92:95],v[80:83],v[36:39]
s_waitcnt lgkmcnt(3)
v_mul_f32 v86,v42,v86
v_fma_f32 v85,v85,v119,v119
v_mfma_f32_16x16x32_bf16 v[28:31],a[96:99],v[52:55],v[28:31]
ds_read_b128 v[72:75],v2 offset:13312
v_fma_f32 v87,v118,v87,v117
v_mfma_f32_16x16x32_bf16 v[32:35],a[100:103],v[52:55],v[32:35]
v_exp_f32 v86,v86)"
R"(
v_mfma_f32_16x16x32_bf16 v[36:39],a[104:107],v[52:55],v[36:39]
v_rcp_f32 v85,v85
v_mfma_f32_16x16x32_bf16 v[28:31],a[108:111],v[56:59],v[28:31]
ds_read_b128 v[76:79],v2 offset:14336
v_mul_f32 v87,v43,v87
v_mfma_f32_16x16x32_bf16 v[32:35],a[112:115],v[56:59],v[32:35]
v_exp_f32 v87,v87
v_mfma_f32_16x16x32_bf16 v[36:39],a[116:119],v[56:59],v[36:39]
s_waitcnt lgkmcnt(3)
v_fma_f32 v86,v86,v119,v119
v_fma_f32 v87,v87,v119,v119
v_mfma_f32_16x16x32_bf16 v[28:31],a[120:123],v[60:63],v[28:31]
ds_read_b128 v[80:83],v2 offset:15360
v_mul_f32 v40,v40,v84
v_mfma_f32_16x16x32_bf16 v[32:35],a[124:127],v[60:63],v[32:35]
s_waitcnt vmcnt(7)
v_rcp_f32 v86,v86
v_mfma_f32_16x16x32_bf16 v[36:39],a[128:131],v[60:63],v[36:39]
s_barrier
v_mfma_f32_16x16x32_bf16 v[28:31],a[132:135],v[64:67],v[28:31]
ds_read_b128 v[52:55],v2 offset:16384
v_add_f32_dpp v104,v104,v104 quad_perm:[1,0,3,2] row_mask:0xf bank_mask:0xf
v_mfma_f32_16x16x32_bf16 v[32:35],a[136:139],v[64:67],v[32:35]
v_rcp_f32 v87,v87
v_mfma_f32_16x16x32_bf16 v[36:39],a[140:143],v[64:67],v[36:39]
s_waitcnt lgkmcnt(3)
v_add_f32_dpp v104,v104,v104 quad_perm:[2,3,0,1] row_mask:0xf bank_mask:0xf
v_mul_f32 v41,v41,v85
v_mfma_f32_16x16x32_bf16 v[28:31],a[144:147],v[68:71],v[28:31]
ds_read_b128 v[56:59],v2 offset:17408
v_add_f32_dpp v104,v104,v104 row_half_mirror row_mask:0xf bank_mask:0xf
v_mfma_f32_16x16x32_bf16 v[32:35],a[148:151],v[68:71],v[32:35]
v_mul_f32 v42,v42,v86
v_mul_f32 v43,v43,v87
v_mfma_f32_16x16x32_bf16 v[36:39],a[152:155],v[68:71],v[36:39]
v_cvt_pk_bf16_f32 v96,v40,v41
v_cvt_pk_bf16_f32 v97,v42,v43
v_mfma_f32_16x16x32_bf16 v[28:31],a[156:159],v[72:75],v[28:31]
ds_read_b128 v[60:63],v2 offset:18432
v_fma_f32 v44,-v116,v20,v44
v_mfma_f32_16x16x32_bf16 v[32:35],a[160:163],v[72:75],v[32:35]
v_fma_f32 v45,-v116,v21,v45
v_fma_f32 v46,-v116,v22,v46
v_mfma_f32_16x16x32_bf16 v[36:39],a[164:167],v[72:75],v[36:39]
s_waitcnt lgkmcnt(3)
v_fma_f32 v47,-v116,v23,v47
v_mul_f32 v88,v44,v44
v_mfma_f32_16x16x32_bf16 v[28:31],a[168:171],v[76:79],v[28:31]
ds_read_b128 v[64:67],v2 offset:19456
v_mul_f32 v89,v45,v45
v_mfma_f32_16x16x32_bf16 v[32:35],a[172:175],v[76:79],v[32:35]
v_mul_f32 v90,v46,v46
v_mul_f32 v91,v47,v47
v_mfma_f32_16x16x32_bf16 v[36:39],a[176:179],v[76:79],v[36:39]
v_fma_f32 v88,v118,v88,v117
v_mul_f32 v88,v44,v88
v_mfma_f32_16x16x32_bf16 v[28:31],a[180:183],v[80:83],v[28:31]
ds_read_b128 v[68:71],v2 offset:20480
v_fma_f32 v89,v118,v89,v117
v_mfma_f32_16x16x32_bf16 v[32:35],a[184:187],v[80:83],v[32:35]
buffer_load_dwordx4 v1,s[36:39],s61 offen offset:0 sc1 lds
v_mfma_f32_16x16x32_bf16 v[36:39],a[188:191],v[80:83],v[36:39]
s_waitcnt lgkmcnt(3)
ds_write_b32 v10,v104 offset:0
v_mfma_f32_16x16x32_bf16 v[28:31],a[192:195],v[52:55],v[28:31]
ds_read_b128 v[72:75],v2 offset:21504
v_mul_f32 v89,v45,v89
v_mfma_f32_16x16x32_bf16 v[32:35],a[196:199],v[52:55],v[32:35]
v_exp_f32 v88,v88
v_mfma_f32_16x16x32_bf16 v[36:39],a[200:203],v[52:55],v[36:39]
buffer_load_dwordx4 v1,s[36:39],s61 offen offset:1024 sc1 lds
v_mfma_f32_16x16x32_bf16 v[28:31],a[204:207],v[56:59],v[28:31]
ds_read_b128 v[76:79],v2 offset:22528
v_fma_f32 v88,v88,v119,v119
v_mfma_f32_16x16x32_bf16 v[32:35],a[208:211],v[56:59],v[32:35]
v_exp_f32 v89,v89
v_mfma_f32_16x16x32_bf16 v[36:39],a[212:215],v[56:59],v[36:39]
s_waitcnt lgkmcnt(4)
v_rcp_f32 v88,v88
v_mfma_f32_16x16x32_bf16 v[28:31],a[216:219],v[60:63],v[28:31]
buffer_load_dwordx4 v1,s[36:39],s61 offen offset:2048 sc1 lds
v_mfma_f32_16x16x32_bf16 v[32:35],a[220:223],v[60:63],v[32:35]
ds_read_b128 v[80:83],v2 offset:23552
v_fma_f32 v89,v89,v119,v119
v_mfma_f32_16x16x32_bf16 v[36:39],a[224:227],v[60:63],v[36:39]
v_rcp_f32 v89,v89
v_mfma_f32_16x16x32_bf16 v[28:31],a[228:231],v[64:67],v[28:31]
ds_read_b128 v[52:55],v2 offset:24576
v_fma_f32 v90,v118,v90,v117
v_mfma_f32_16x16x32_bf16 v[32:35],a[232:235],v[64:67],v[32:35]
buffer_load_dwordx4 v1,s[36:39],s61 offen offset:3072 sc1 lds
v_mfma_f32_16x16x32_bf16 v[36:39],a[236:239],v[64:67],v[36:39]
s_waitcnt lgkmcnt(3)
buffer_load_dword v108,v8,s[36:39],s72 offen offset:0 sc1
v_mfma_f32_16x16x32_bf16 v[28:31],a[240:243],v[68:71],v[28:31]
ds_read_b128 v[56:59],v2 offset:25600
v_mul_f32 v90,v46,v90
v_mfma_f32_16x16x32_bf16 v[32:35],a[244:247],v[68:71],v[32:35]
v_exp_f32 v90,v90
v_mfma_f32_16x16x32_bf16 v[36:39],a[248:251],v[68:71],v[36:39]
v_fma_f32 v90,v90,v119,v119
v_fma_f32 v91,v118,v91,v117
v_mfma_f32_16x16x32_bf16 v[28:31],a[252:255],v[72:75],v[28:31]
ds_read_b128 v[60:63],v2 offset:26624
v_mul_f32 v91,v47,v91
v_mfma_f32_16x16x32_bf16 v[32:35],v[128:131],v[72:75],v[32:35]
v_exp_f32 v91,v91
v_mfma_f32_16x16x32_bf16 v[36:39],v[132:135],v[72:75],v[36:39]
s_waitcnt lgkmcnt(3)
v_rcp_f32 v90,v90
v_mfma_f32_16x16x32_bf16 v[28:31],v[136:139],v[76:79],v[28:31]
ds_read_b128 v[64:67],v2 offset:27648
v_fma_f32 v91,v91,v119,v119
v_mfma_f32_16x16x32_bf16 v[32:35],v[140:143],v[76:79],v[32:35]
v_rcp_f32 v91,v91
v_mfma_f32_16x16x32_bf16 v[36:39],v[144:147],v[76:79],v[36:39]
v_mul_f32 v44,v44,v88
v_mul_f32 v45,v45,v89
v_mfma_f32_16x16x32_bf16 v[28:31],v[148:151],v[80:83],v[28:31]
ds_read_b128 v[68:71],v2 offset:28672
v_mul_f32 v46,v46,v90
v_mfma_f32_16x16x32_bf16 v[32:35],v[152:155],v[80:83],v[32:35]
v_mul_f32 v47,v47,v91
v_cvt_pk_bf16_f32 v98,v44,v45
v_mfma_f32_16x16x32_bf16 v[36:39],v[156:159],v[80:83],v[36:39]
s_waitcnt lgkmcnt(3)
v_cvt_pk_bf16_f32 v99,v46,v47
v_fma_f32 v48,-v116,v24,v48
v_mfma_f32_16x16x32_bf16 v[28:31],v[160:163],v[52:55],v[28:31]
ds_read_b128 v[72:75],v2 offset:29696
v_fma_f32 v49,-v116,v25,v49
v_mfma_f32_16x16x32_bf16 v[32:35],v[164:167],v[52:55],v[32:35]
v_fma_f32 v50,-v116,v26,v50
v_fma_f32 v51,-v116,v27,v51
v_mfma_f32_16x16x32_bf16 v[36:39],v[168:171],v[52:55],v[36:39]
v_mul_f32 v92,v48,v48
v_mul_f32 v93,v49,v49
v_mfma_f32_16x16x32_bf16 v[28:31],v[172:175],v[56:59],v[28:31]
ds_read_b128 v[76:79],v2 offset:30720
v_mul_f32 v94,v50,v50
v_mfma_f32_16x16x32_bf16 v[32:35],v[176:179],v[56:59],v[32:35]
buffer_store_dwordx4 v[96:99],v3,s[36:39],s73 offen offset:0 sc0 sc1
v_mul_f32 v95,v51,v51
v_fma_f32 v92,v118,v92,v117
v_mul_f32 v92,v48,v92
v_mfma_f32_16x16x32_bf16 v[36:39],v[180:183],v[56:59],v[36:39]
s_waitcnt lgkmcnt(3)
v_exp_f32 v92,v92
v_mfma_f32_16x16x32_bf16 v[28:31],v[184:187],v[60:63],v[28:31]
ds_read_b128 v[80:83],v2 offset:31744
v_fma_f32 v92,v92,v119,v119
v_mfma_f32_16x16x32_bf16 v[32:35],v[188:191],v[60:63],v[32:35]
s_waitcnt vmcnt(6)
v_rcp_f32 v92,v92
v_mfma_f32_16x16x32_bf16 v[36:39],v[192:195],v[60:63],v[36:39]
s_barrier
v_mfma_f32_16x16x32_bf16 v[28:31],v[196:199],v[64:67],v[28:31]
buffer_store_dword v5,v4,s[36:39],s70 offen offset:0 sc0 sc1
ds_read_b128 v[52:55],v2 offset:32768
ds_read_b32 v13,v7 offset:0
ds_read_b64 v[112:113],v11 offset:0
v_mfma_f32_16x16x32_bf16 v[32:35],v[200:203],v[64:67],v[32:35]
v_fma_f32 v93,v118,v93,v117
v_mul_f32 v93,v49,v93
v_mfma_f32_16x16x32_bf16 v[36:39],v[204:207],v[64:67],v[36:39]
s_waitcnt lgkmcnt(5)
v_exp_f32 v93,v93
v_mfma_f32_16x16x32_bf16 v[28:31],v[208:211],v[68:71],v[28:31]
ds_read_b128 v[56:59],v2 offset:33792
v_fma_f32 v93,v93,v119,v119
v_mfma_f32_16x16x32_bf16 v[32:35],v[212:215],v[68:71],v[32:35]
s_waitcnt lgkmcnt(2)
v_rcp_f32 v93,v93
v_mfma_f32_16x16x32_bf16 v[36:39],v[216:219],v[68:71],v[36:39]
s_waitcnt lgkmcnt(1)
v_cmp_eq_u32 vcc,s60,v13
v_fma_f32 v94,v118,v94,v117
v_mul_f32 v94,v50,v94
v_fma_f32 v95,v118,v95,v117
v_mfma_f32_16x16x32_bf16 v[28:31],v[220:223],v[72:75],v[28:31]
s_cmp_eq_u32 vcc_lo,-1
ds_read_b128 v[60:63],v2 offset:34816
v_mul_f32 v95,v51,v95
v_mfma_f32_16x16x32_bf16 v[32:35],v[224:227],v[72:75],v[32:35]
s_cselect_b32 s67,s86,s59
v_exp_f32 v94,v94
v_mfma_f32_16x16x32_bf16 v[36:39],v[228:231],v[72:75],v[36:39]
v_exp_f32 v95,v95
v_mfma_f32_16x16x32_bf16 v[28:31],v[232:235],v[76:79],v[28:31]
s_lshr_b32 s61,s67,0
ds_read_b128 v[64:67],v2 offset:35840
v_fma_f32 v94,v94,v119,v119
v_mfma_f32_16x16x32_bf16 v[32:35],v[236:239],v[76:79],v[32:35]
s_xor_b32 s63,s63,65536
v_fma_f32 v95,v95,v119,v119
v_rcp_f32 v94,v94
v_rcp_f32 v95,v95
v_mfma_f32_16x16x32_bf16 v[36:39],v[240:243],v[76:79],v[36:39]
s_cmp_ge_u32 s66,s59
v_mul_f32 v48,v48,v92)"
R"(
v_mul_f32 v49,v49,v93
v_mul_f32 v50,v50,v94
v_mul_f32 v51,v51,v95
v_cvt_pk_bf16_f32 v100,v48,v49
v_cvt_pk_bf16_f32 v101,v50,v51
v_mfma_f32_16x16x32_bf16 v[28:31],v[244:247],v[80:83],v[28:31]
buffer_store_dwordx2 v[100:101],v9,s[36:39],s73 offen offset:0 sc0 sc1
ds_read_b128 v[68:71],v2 offset:36864
v_mfma_f32_16x16x32_bf16 v[32:35],v[248:251],v[80:83],v[32:35]
s_waitcnt lgkmcnt(3)
v_mfma_f32_16x16x32_bf16 v[36:39],v[252:255],v[80:83],v[36:39]
s_cbranch_scc1 Lg3_slow1_%=
Lg3_d1w3_%=:
v_mfma_f32_16x16x32_bf16 v[40:43],a[0:3],v[52:55],0
s_mov_b32 m0,s62
ds_read_b128 v[72:75],v2 offset:37888
v_mul_f32 v116,s75,v112
v_mfma_f32_16x16x32_bf16 v[44:47],a[4:7],v[52:55],0
s_mul_i32 s73,s48,7
v_fma_f32 v113,v113,s75,v12
v_fma_f32 v113,-v116,v116,v113
v_mfma_f32_16x16x32_bf16 v[48:51],a[8:11],v[52:55],0
s_lshr_b32 s73,s73,1
v_max_f32 v113,v113,v12
v_rsq_f32 v112,v113
v_mfma_f32_16x16x32_bf16 v[40:43],a[12:15],v[56:59],v[40:43]
s_add_u32 s74,s53,s55
ds_read_b128 v[76:79],v2 offset:38912
v_mul_f32 v117,s76,v112
v_mfma_f32_16x16x32_bf16 v[44:47],a[16:19],v[56:59],v[44:47]
buffer_load_dwordx4 v0,s[36:39],s61 offen offset:0 sc1 lds
v_mfma_f32_16x16x32_bf16 v[48:51],a[20:23],v[56:59],v[48:51]
s_waitcnt lgkmcnt(3)
v_mul_f32 v118,v112,v112
v_mul_f32 v119,v113,v112
v_mfma_f32_16x16x32_bf16 v[40:43],a[24:27],v[60:63],v[40:43]
s_cmp_eq_u32 s74,s56
ds_read_b128 v[80:83],v2 offset:39936
v_mul_f32 v118,s77,v118
v_mfma_f32_16x16x32_bf16 v[44:47],a[28:31],v[60:63],v[44:47]
s_cselect_b32 s74,s57,s74
v_mul_f32 v118,v118,v117
v_fma_f32 v28,-v116,v16,v28
v_mfma_f32_16x16x32_bf16 v[48:51],a[32:35],v[60:63],v[48:51]
buffer_load_dwordx4 v0,s[36:39],s61 offen offset:1024 sc1 lds
v_mfma_f32_16x16x32_bf16 v[40:43],a[36:39],v[64:67],v[40:43]
s_cmp_eq_u32 s74,s58
ds_read_b128 v[52:55],v2 offset:40960
v_fma_f32 v29,-v116,v17,v29
v_mfma_f32_16x16x32_bf16 v[44:47],a[40:43],v[64:67],v[44:47]
s_cselect_b32 s74,s59,s74
v_fma_f32 v30,-v116,v18,v30
v_fma_f32 v31,-v116,v19,v31
v_mfma_f32_16x16x32_bf16 v[48:51],a[44:47],v[64:67],v[48:51]
s_waitcnt lgkmcnt(3)
v_mul_f32 v84,v28,v28
v_mul_f32 v85,v29,v29
v_mfma_f32_16x16x32_bf16 v[40:43],a[48:51],v[68:71],v[40:43]
buffer_load_dwordx4 v0,s[36:39],s61 offen offset:2048 sc1 lds
v_mfma_f32_16x16x32_bf16 v[44:47],a[52:55],v[68:71],v[44:47]
s_cmp_ge_u32 s67,s59
ds_read_b128 v[56:59],v2 offset:41984
v_mul_f32 v86,v30,v30
v_mfma_f32_16x16x32_bf16 v[48:51],a[56:59],v[68:71],v[48:51]
s_cselect_b32 s86,s50,s51
v_mul_f32 v87,v31,v31
v_fma_f32 v84,v118,v84,v117
v_mfma_f32_16x16x32_bf16 v[40:43],a[60:63],v[72:75],v[40:43]
s_mul_i32 s70,s47,5
ds_read_b128 v[60:63],v2 offset:43008
v_mul_f32 v84,v28,v84
v_mfma_f32_16x16x32_bf16 v[44:47],a[64:67],v[72:75],v[44:47]
buffer_load_dwordx4 v0,s[36:39],s61 offen offset:3072 sc1 lds
v_mfma_f32_16x16x32_bf16 v[48:51],a[68:71],v[72:75],v[48:51]
s_waitcnt lgkmcnt(3)
v_exp_f32 v84,v84
v_mfma_f32_16x16x32_bf16 v[40:43],a[72:75],v[76:79],v[40:43]
s_add_u32 m0,s62,16384
ds_read_b128 v[64:67],v2 offset:44032
v_fma_f32 v84,v84,v119,v119
v_mfma_f32_16x16x32_bf16 v[44:47],a[76:79],v[76:79],v[44:47]
s_lshr_b32 s72,s61,5
v_rcp_f32 v84,v84
v_mfma_f32_16x16x32_bf16 v[48:51],a[80:83],v[76:79],v[48:51]
s_lshr_b32 s70,s70,6
v_fma_f32 v85,v118,v85,v117
v_mul_f32 v85,v29,v85
v_mfma_f32_16x16x32_bf16 v[40:43],a[84:87],v[80:83],v[40:43]
s_mov_b32 s62,s64
ds_read_b128 v[68:71],v2 offset:45056
v_fma_f32 v86,v118,v86,v117
v_mfma_f32_16x16x32_bf16 v[44:47],a[88:91],v[80:83],v[44:47]
s_mov_b64 s[44:45],s[46:47]
v_exp_f32 v85,v85
v_mfma_f32_16x16x32_bf16 v[48:51],a[92:95],v[80:83],v[48:51]
s_waitcnt lgkmcnt(3)
v_mul_f32 v86,v30,v86
v_fma_f32 v85,v85,v119,v119
v_mfma_f32_16x16x32_bf16 v[40:43],a[96:99],v[52:55],v[40:43]
s_mov_b64 s[46:47],s[48:49]
ds_read_b128 v[72:75],v2 offset:46080
v_fma_f32 v87,v118,v87,v117
v_mfma_f32_16x16x32_bf16 v[44:47],a[100:103],v[52:55],v[44:47]
s_mov_b64 s[48:49],s[50:51]
v_exp_f32 v86,v86
v_mfma_f32_16x16x32_bf16 v[48:51],a[104:107],v[52:55],v[48:51]
s_mov_b64 s[50:51],s[52:53]
v_rcp_f32 v85,v85
v_mfma_f32_16x16x32_bf16 v[40:43],a[108:111],v[56:59],v[40:43]
s_mov_b32 s52,s74
ds_read_b128 v[76:79],v2 offset:47104
v_mul_f32 v87,v31,v87
v_mfma_f32_16x16x32_bf16 v[44:47],a[112:115],v[56:59],v[44:47]
v_exp_f32 v87,v87
v_mfma_f32_16x16x32_bf16 v[48:51],a[116:119],v[56:59],v[48:51]
s_waitcnt lgkmcnt(3)
v_fma_f32 v86,v86,v119,v119
v_fma_f32 v87,v87,v119,v119
v_mfma_f32_16x16x32_bf16 v[40:43],a[120:123],v[60:63],v[40:43]
ds_read_b128 v[80:83],v2 offset:48128
v_mul_f32 v28,v28,v84
v_mfma_f32_16x16x32_bf16 v[44:47],a[124:127],v[60:63],v[44:47]
s_waitcnt vmcnt(7)
v_rcp_f32 v86,v86
v_mfma_f32_16x16x32_bf16 v[48:51],a[128:131],v[60:63],v[48:51]
s_barrier
v_mfma_f32_16x16x32_bf16 v[40:43],a[132:135],v[64:67],v[40:43]
ds_read_b128 v[52:55],v2 offset:49152
v_add_f32_dpp v108,v108,v108 quad_perm:[1,0,3,2] row_mask:0xf bank_mask:0xf
v_mfma_f32_16x16x32_bf16 v[44:47],a[136:139],v[64:67],v[44:47]
v_rcp_f32 v87,v87
v_mfma_f32_16x16x32_bf16 v[48:51],a[140:143],v[64:67],v[48:51]
s_waitcnt lgkmcnt(3)
v_add_f32_dpp v108,v108,v108 quad_perm:[2,3,0,1] row_mask:0xf bank_mask:0xf
v_mul_f32 v29,v29,v85
v_mfma_f32_16x16x32_bf16 v[40:43],a[144:147],v[68:71],v[40:43]
ds_read_b128 v[56:59],v2 offset:50176
v_add_f32_dpp v108,v108,v108 row_half_mirror row_mask:0xf bank_mask:0xf
v_mfma_f32_16x16x32_bf16 v[44:47],a[148:151],v[68:71],v[44:47]
v_mul_f32 v30,v30,v86
v_mul_f32 v31,v31,v87
v_mfma_f32_16x16x32_bf16 v[48:51],a[152:155],v[68:71],v[48:51]
v_cvt_pk_bf16_f32 v96,v28,v29
v_cvt_pk_bf16_f32 v97,v30,v31
v_mfma_f32_16x16x32_bf16 v[40:43],a[156:159],v[72:75],v[40:43]
ds_read_b128 v[60:63],v2 offset:51200
v_fma_f32 v32,-v116,v20,v32
v_mfma_f32_16x16x32_bf16 v[44:47],a[160:163],v[72:75],v[44:47]
v_fma_f32 v33,-v116,v21,v33
v_fma_f32 v34,-v116,v22,v34
v_mfma_f32_16x16x32_bf16 v[48:51],a[164:167],v[72:75],v[48:51]
s_waitcnt lgkmcnt(3)
v_fma_f32 v35,-v116,v23,v35
v_mul_f32 v88,v32,v32
v_mfma_f32_16x16x32_bf16 v[40:43],a[168:171],v[76:79],v[40:43]
ds_read_b128 v[64:67],v2 offset:52224
v_mul_f32 v89,v33,v33
v_mfma_f32_16x16x32_bf16 v[44:47],a[172:175],v[76:79],v[44:47]
v_mul_f32 v90,v34,v34
v_mul_f32 v91,v35,v35
v_mfma_f32_16x16x32_bf16 v[48:51],a[176:179],v[76:79],v[48:51]
v_fma_f32 v88,v118,v88,v117
v_mul_f32 v88,v32,v88
v_mfma_f32_16x16x32_bf16 v[40:43],a[180:183],v[80:83],v[40:43]
ds_read_b128 v[68:71],v2 offset:53248
v_fma_f32 v89,v118,v89,v117
v_mfma_f32_16x16x32_bf16 v[44:47],a[184:187],v[80:83],v[44:47]
buffer_load_dwordx4 v1,s[36:39],s61 offen offset:0 sc1 lds
v_mfma_f32_16x16x32_bf16 v[48:51],a[188:191],v[80:83],v[48:51]
s_waitcnt lgkmcnt(3)
ds_write_b32 v10,v108 offset:128
v_mul_f32 v89,v33,v89
v_mfma_f32_16x16x32_bf16 v[40:43],a[192:195],v[52:55],v[40:43]
ds_read_b128 v[72:75],v2 offset:54272
v_fma_f32 v90,v118,v90,v117
v_mfma_f32_16x16x32_bf16 v[44:47],a[196:199],v[52:55],v[44:47]
v_exp_f32 v88,v88
v_mfma_f32_16x16x32_bf16 v[48:51],a[200:203],v[52:55],v[48:51]
buffer_load_dwordx4 v1,s[36:39],s61 offen offset:1024 sc1 lds
v_mfma_f32_16x16x32_bf16 v[40:43],a[204:207],v[56:59],v[40:43]
ds_read_b128 v[76:79],v2 offset:55296
v_mul_f32 v90,v34,v90
v_mfma_f32_16x16x32_bf16 v[44:47],a[208:211],v[56:59],v[44:47]
v_exp_f32 v89,v89
v_mfma_f32_16x16x32_bf16 v[48:51],a[212:215],v[56:59],v[48:51]
s_waitcnt lgkmcnt(4)
v_exp_f32 v90,v90
v_mfma_f32_16x16x32_bf16 v[40:43],a[216:219],v[60:63],v[40:43]
buffer_load_dwordx4 v1,s[36:39],s61 offen offset:2048 sc1 lds
v_mfma_f32_16x16x32_bf16 v[44:47],a[220:223],v[60:63],v[44:47]
ds_read_b128 v[80:83],v2 offset:56320
v_fma_f32 v88,v88,v119,v119
v_mfma_f32_16x16x32_bf16 v[48:51],a[224:227],v[60:63],v[48:51]
v_rcp_f32 v88,v88
v_mfma_f32_16x16x32_bf16 v[40:43],a[228:231],v[64:67],v[40:43]
ds_read_b128 v[52:55],v2 offset:57344
v_fma_f32 v89,v89,v119,v119
v_mfma_f32_16x16x32_bf16 v[44:47],a[232:235],v[64:67],v[44:47]
buffer_load_dwordx4 v1,s[36:39],s61 offen offset:3072 sc1 lds
v_mfma_f32_16x16x32_bf16 v[48:51],a[236:239],v[64:67],v[48:51])"
R"(
s_waitcnt lgkmcnt(3)
buffer_load_dword v104,v8,s[36:39],s72 offen offset:0 sc1
v_mfma_f32_16x16x32_bf16 v[40:43],a[240:243],v[68:71],v[40:43]
ds_read_b128 v[56:59],v2 offset:58368
v_fma_f32 v90,v90,v119,v119
v_mfma_f32_16x16x32_bf16 v[44:47],a[244:247],v[68:71],v[44:47]
v_rcp_f32 v89,v89
v_mfma_f32_16x16x32_bf16 v[48:51],a[248:251],v[68:71],v[48:51]
v_rcp_f32 v90,v90
v_mfma_f32_16x16x32_bf16 v[40:43],a[252:255],v[72:75],v[40:43]
ds_read_b128 v[60:63],v2 offset:59392
v_fma_f32 v91,v118,v91,v117
v_mfma_f32_16x16x32_bf16 v[44:47],v[128:131],v[72:75],v[44:47]
v_mul_f32 v91,v35,v91
v_mul_f32 v32,v32,v88
v_mfma_f32_16x16x32_bf16 v[48:51],v[132:135],v[72:75],v[48:51]
s_waitcnt lgkmcnt(3)
v_exp_f32 v91,v91
v_mfma_f32_16x16x32_bf16 v[40:43],v[136:139],v[76:79],v[40:43]
ds_read_b128 v[64:67],v2 offset:60416
v_fma_f32 v91,v91,v119,v119
v_mfma_f32_16x16x32_bf16 v[44:47],v[140:143],v[76:79],v[44:47]
v_rcp_f32 v91,v91
v_mfma_f32_16x16x32_bf16 v[48:51],v[144:147],v[76:79],v[48:51]
v_mul_f32 v33,v33,v89
v_mul_f32 v34,v34,v90
v_mfma_f32_16x16x32_bf16 v[40:43],v[148:151],v[80:83],v[40:43]
ds_read_b128 v[68:71],v2 offset:61440
v_mul_f32 v35,v35,v91
v_mfma_f32_16x16x32_bf16 v[44:47],v[152:155],v[80:83],v[44:47]
v_cvt_pk_bf16_f32 v98,v32,v33
v_cvt_pk_bf16_f32 v99,v34,v35
v_mfma_f32_16x16x32_bf16 v[48:51],v[156:159],v[80:83],v[48:51]
s_waitcnt lgkmcnt(3)
v_fma_f32 v36,-v116,v24,v36
v_fma_f32 v37,-v116,v25,v37
v_mfma_f32_16x16x32_bf16 v[40:43],v[160:163],v[52:55],v[40:43]
ds_read_b128 v[72:75],v2 offset:62464
v_fma_f32 v38,-v116,v26,v38
v_mfma_f32_16x16x32_bf16 v[44:47],v[164:167],v[52:55],v[44:47]
v_fma_f32 v39,-v116,v27,v39
v_mul_f32 v92,v36,v36
v_mfma_f32_16x16x32_bf16 v[48:51],v[168:171],v[52:55],v[48:51]
v_mul_f32 v93,v37,v37
v_mul_f32 v94,v38,v38
v_mfma_f32_16x16x32_bf16 v[40:43],v[172:175],v[56:59],v[40:43]
ds_read_b128 v[76:79],v2 offset:63488
v_mul_f32 v95,v39,v39
v_mfma_f32_16x16x32_bf16 v[44:47],v[176:179],v[56:59],v[44:47]
buffer_store_dwordx4 v[96:99],v3,s[36:39],s73 offen offset:0 sc0 sc1
v_fma_f32 v92,v118,v92,v117
v_mul_f32 v92,v36,v92
v_fma_f32 v93,v118,v93,v117
v_mfma_f32_16x16x32_bf16 v[48:51],v[180:183],v[56:59],v[48:51]
s_waitcnt lgkmcnt(3)
v_exp_f32 v92,v92
v_mfma_f32_16x16x32_bf16 v[40:43],v[184:187],v[60:63],v[40:43]
ds_read_b128 v[80:83],v2 offset:64512
v_mul_f32 v93,v37,v93
v_mfma_f32_16x16x32_bf16 v[44:47],v[188:191],v[60:63],v[44:47]
s_waitcnt vmcnt(6)
v_xor_b32 v2,65536,v2
v_fma_f32 v92,v92,v119,v119
v_mfma_f32_16x16x32_bf16 v[48:51],v[192:195],v[60:63],v[48:51]
s_barrier
v_mfma_f32_16x16x32_bf16 v[40:43],v[196:199],v[64:67],v[40:43]
buffer_store_dword v5,v4,s[36:39],s70 offen offset:0 sc0 sc1
ds_read_b128 v[52:55],v2 offset:0
ds_read_b32 v13,v7 offset:256
ds_read_b64 v[112:113],v11 offset:128
v_mfma_f32_16x16x32_bf16 v[44:47],v[200:203],v[64:67],v[44:47]
v_exp_f32 v93,v93
v_mfma_f32_16x16x32_bf16 v[48:51],v[204:207],v[64:67],v[48:51]
s_waitcnt lgkmcnt(5)
v_rcp_f32 v92,v92
v_mfma_f32_16x16x32_bf16 v[40:43],v[208:211],v[68:71],v[40:43]
ds_read_b128 v[56:59],v2 offset:1024
v_fma_f32 v93,v93,v119,v119
v_mfma_f32_16x16x32_bf16 v[44:47],v[212:215],v[68:71],v[44:47]
s_waitcnt lgkmcnt(2)
v_rcp_f32 v93,v93
v_mfma_f32_16x16x32_bf16 v[48:51],v[216:219],v[68:71],v[48:51]
s_waitcnt lgkmcnt(1)
v_cmp_eq_u32 vcc,s60,v13
v_fma_f32 v94,v118,v94,v117
v_mul_f32 v94,v38,v94
v_fma_f32 v95,v118,v95,v117
v_mfma_f32_16x16x32_bf16 v[40:43],v[220:223],v[72:75],v[40:43]
s_cmp_eq_u32 vcc_lo,-1
ds_read_b128 v[60:63],v2 offset:2048
v_mul_f32 v95,v39,v95
v_mfma_f32_16x16x32_bf16 v[44:47],v[224:227],v[72:75],v[44:47]
s_cselect_b32 s66,s86,s59
v_exp_f32 v94,v94
v_mfma_f32_16x16x32_bf16 v[48:51],v[228:231],v[72:75],v[48:51]
v_exp_f32 v95,v95
v_mfma_f32_16x16x32_bf16 v[40:43],v[232:235],v[76:79],v[40:43]
s_lshr_b32 s61,s66,0
ds_read_b128 v[64:67],v2 offset:3072
v_fma_f32 v94,v94,v119,v119
v_mfma_f32_16x16x32_bf16 v[44:47],v[236:239],v[76:79],v[44:47]
s_xor_b32 s64,s64,65536
v_fma_f32 v95,v95,v119,v119
v_rcp_f32 v94,v94
v_rcp_f32 v95,v95
v_mfma_f32_16x16x32_bf16 v[48:51],v[240:243],v[76:79],v[48:51]
s_cmp_lt_u32 s67,s59
v_mul_f32 v36,v36,v92
v_mul_f32 v37,v37,v93
v_mul_f32 v38,v38,v94
v_mul_f32 v39,v39,v95
v_cvt_pk_bf16_f32 v100,v36,v37
v_cvt_pk_bf16_f32 v101,v38,v39
v_mfma_f32_16x16x32_bf16 v[40:43],v[244:247],v[80:83],v[40:43]
buffer_store_dwordx2 v[100:101],v9,s[36:39],s73 offen offset:0 sc0 sc1
ds_read_b128 v[68:71],v2 offset:4096
v_mfma_f32_16x16x32_bf16 v[44:47],v[248:251],v[80:83],v[44:47]
s_waitcnt lgkmcnt(3)
v_mfma_f32_16x16x32_bf16 v[48:51],v[252:255],v[80:83],v[48:51]
s_cbranch_scc1 Lg3_d0w3_%=
s_branch Lg3_slow0_%=
Lg3_slow0_%=:
s_cmp_lt_u32 s66,s59
s_cbranch_scc1 Lg3_gs0_%=
Lg3_slowb0_%=:
s_cmp_eq_u32 s54,1
s_cselect_b32 s68,s49,s48
s_lshr_b32 s68,s68,5
s_add_u32 m0,s78,8192
s_nop 0
buffer_load_dword v6,s[36:39],s68 offen offset:0 sc1 lds
s_mul_i32 s73,s47,7
s_lshr_b32 s73,s73,1
v_mul_f32 v116,s75,v112
v_fma_f32 v113,v113,s75,v12
v_fma_f32 v113,-v116,v116,v113
v_max_f32 v113,v113,v12
v_rsq_f32 v112,v113
s_nop 0
v_mul_f32 v117,s76,v112
v_mul_f32 v118,v112,v112
v_mul_f32 v119,v113,v112
v_mul_f32 v118,s77,v118
v_mul_f32 v118,v118,v117
v_fma_f32 v40,-v116,v16,v40
v_fma_f32 v41,-v116,v17,v41
v_fma_f32 v42,-v116,v18,v42
v_fma_f32 v43,-v116,v19,v43
v_mul_f32 v84,v40,v40
v_mul_f32 v85,v41,v41
v_mul_f32 v86,v42,v42
v_mul_f32 v87,v43,v43
v_fma_f32 v84,v118,v84,v117
v_fma_f32 v85,v118,v85,v117
v_fma_f32 v86,v118,v86,v117
v_fma_f32 v87,v118,v87,v117
v_mul_f32 v84,v40,v84
v_mul_f32 v85,v41,v85
v_mul_f32 v86,v42,v86
v_mul_f32 v87,v43,v87
v_exp_f32 v84,v84
v_exp_f32 v85,v85
v_exp_f32 v86,v86
v_exp_f32 v87,v87
v_fma_f32 v84,v84,v119,v119
v_fma_f32 v85,v85,v119,v119
v_fma_f32 v86,v86,v119,v119
v_fma_f32 v87,v87,v119,v119
v_rcp_f32 v84,v84
v_rcp_f32 v85,v85
v_rcp_f32 v86,v86
v_rcp_f32 v87,v87
v_mul_f32 v40,v40,v84
v_mul_f32 v41,v41,v85
v_mul_f32 v42,v42,v86
v_mul_f32 v43,v43,v87
v_cvt_pk_bf16_f32 v96,v40,v41
v_cvt_pk_bf16_f32 v97,v42,v43
v_fma_f32 v44,-v116,v20,v44
v_fma_f32 v45,-v116,v21,v45
v_fma_f32 v46,-v116,v22,v46
v_fma_f32 v47,-v116,v23,v47
v_mul_f32 v88,v44,v44
v_mul_f32 v89,v45,v45
v_mul_f32 v90,v46,v46
v_mul_f32 v91,v47,v47
v_fma_f32 v88,v118,v88,v117
v_fma_f32 v89,v118,v89,v117
v_fma_f32 v90,v118,v90,v117
v_fma_f32 v91,v118,v91,v117
v_mul_f32 v88,v44,v88
s_add_u32 m0,s78,8448
s_nop 0
buffer_load_dword v6,s[36:39],s68 offen offset:0 sc1 lds
v_mul_f32 v89,v45,v89
v_mul_f32 v90,v46,v90
v_mul_f32 v91,v47,v91
v_exp_f32 v88,v88
v_exp_f32 v89,v89
v_exp_f32 v90,v90)"
R"(
v_exp_f32 v91,v91
v_fma_f32 v88,v88,v119,v119
v_fma_f32 v89,v89,v119,v119
v_fma_f32 v90,v90,v119,v119
v_fma_f32 v91,v91,v119,v119
v_rcp_f32 v88,v88
v_rcp_f32 v89,v89
v_rcp_f32 v90,v90
v_rcp_f32 v91,v91
v_mul_f32 v44,v44,v88
v_mul_f32 v45,v45,v89
v_mul_f32 v46,v46,v90
v_mul_f32 v47,v47,v91
v_cvt_pk_bf16_f32 v98,v44,v45
v_cvt_pk_bf16_f32 v99,v46,v47
buffer_store_dwordx4 v[96:99],v3,s[36:39],s73 offen offset:0 sc0 sc1
v_fma_f32 v48,-v116,v24,v48
v_fma_f32 v49,-v116,v25,v49
v_fma_f32 v50,-v116,v26,v50
v_fma_f32 v51,-v116,v27,v51
v_mul_f32 v92,v48,v48
v_mul_f32 v93,v49,v49
v_mul_f32 v94,v50,v50
v_mul_f32 v95,v51,v51
v_fma_f32 v92,v118,v92,v117
v_fma_f32 v93,v118,v93,v117
v_fma_f32 v94,v118,v94,v117
v_fma_f32 v95,v118,v95,v117
v_mul_f32 v92,v48,v92
v_mul_f32 v93,v49,v93
v_mul_f32 v94,v50,v94
v_mul_f32 v95,v51,v95
v_exp_f32 v92,v92
v_exp_f32 v93,v93
v_exp_f32 v94,v94
v_exp_f32 v95,v95
v_fma_f32 v92,v92,v119,v119
v_fma_f32 v93,v93,v119,v119
v_fma_f32 v94,v94,v119,v119
v_fma_f32 v95,v95,v119,v119
v_rcp_f32 v92,v92
v_rcp_f32 v93,v93
v_rcp_f32 v94,v94
v_rcp_f32 v95,v95
v_mul_f32 v48,v48,v92
v_mul_f32 v49,v49,v93
v_mul_f32 v50,v50,v94
v_mul_f32 v51,v51,v95
v_cvt_pk_bf16_f32 v100,v48,v49
v_cvt_pk_bf16_f32 v101,v50,v51
buffer_store_dwordx2 v[100:101],v9,s[36:39],s73 offen offset:0 sc0 sc1
s_cmp_ge_u32 s48,s59
s_cbranch_scc1 Lg3_done0_%=
s_add_u32 m0,s78,16384
s_nop 0
buffer_load_dword v6,s[36:39],s68 offen offset:0 sc1 lds
s_waitcnt vmcnt(4)
s_barrier
ds_read_b32 v13,v7 offset:8192
s_mul_i32 s70,s46,5
s_lshr_b32 s70,s70,6
buffer_store_dword v5,v4,s[36:39],s70 offen offset:0 sc0 sc1
s_mov_b32 s46,s59
s_mov_b32 s69,18432
s_waitcnt lgkmcnt(0)
v_cmp_eq_u32 vcc,s60,v13
s_nop 0
s_cmp_eq_u32 vcc_lo,-1
s_cbranch_scc1 Lg3_go0_%=
s_add_u32 m0,s78,16640
s_nop 0
buffer_load_dword v6,s[36:39],s68 offen offset:0 sc1 lds
s_waitcnt vmcnt(5)
s_barrier
ds_read_b32 v13,v7 offset:8448
s_mov_b32 s69,18688
s_waitcnt lgkmcnt(0)
v_cmp_eq_u32 vcc,s60,v13
s_nop 0
s_cmp_eq_u32 vcc_lo,-1
s_cbranch_scc1 Lg3_go0_%=
s_add_u32 m0,s78,8192
s_nop 0
buffer_load_dword v6,s[36:39],s68 offen offset:0 sc1 lds
s_waitcnt vmcnt(3)
s_barrier
ds_read_b32 v13,v7 offset:16384
s_mul_i32 s70,s47,5
s_lshr_b32 s70,s70,6
buffer_store_dword v5,v4,s[36:39],s70 offen offset:0 sc0 sc1
s_mov_b32 s47,s59
s_mov_b32 s69,10240
s_waitcnt lgkmcnt(0)
v_cmp_eq_u32 vcc,s60,v13
s_nop 0
s_cmp_eq_u32 vcc_lo,-1
s_cbranch_scc1 Lg3_go0_%=
s_add_u32 m0,s78,8448
s_nop 0
buffer_load_dword v6,s[36:39],s68 offen offset:0 sc1 lds
s_waitcnt vmcnt(3)
s_barrier
ds_read_b32 v13,v7 offset:16640
s_mov_b32 s69,10496
s_waitcnt lgkmcnt(0)
v_cmp_eq_u32 vcc,s60,v13
s_nop 0
s_cmp_eq_u32 vcc_lo,-1
s_cbranch_scc1 Lg3_go0_%=
Lg3_poll0_%=:
s_add_u32 m0,s78,16384
s_nop 0
buffer_load_dword v6,s[36:39],s68 offen offset:0 sc1 lds
s_waitcnt vmcnt(2)
s_barrier
ds_read_b32 v13,v7 offset:8192
s_mov_b32 s69,18432
s_waitcnt lgkmcnt(0)
v_cmp_eq_u32 vcc,s60,v13
s_nop 0
s_cmp_eq_u32 vcc_lo,-1
s_cbranch_scc1 Lg3_go0_%=
s_sleep 4
s_add_u32 m0,s78,16640
s_nop 0
buffer_load_dword v6,s[36:39],s68 offen offset:0 sc1 lds
s_waitcnt vmcnt(2)
s_barrier
ds_read_b32 v13,v7 offset:8448
s_mov_b32 s69,18688
s_waitcnt lgkmcnt(0)
v_cmp_eq_u32 vcc,s60,v13
s_nop 0
s_cmp_eq_u32 vcc_lo,-1
s_cbranch_scc1 Lg3_go0_%=
s_sleep 4
s_add_u32 m0,s78,8192
s_nop 0
buffer_load_dword v6,s[36:39],s68 offen offset:0 sc1 lds
s_waitcnt vmcnt(2)
s_barrier
ds_read_b32 v13,v7 offset:16384
s_mov_b32 s69,10240
s_waitcnt lgkmcnt(0)
v_cmp_eq_u32 vcc,s60,v13
s_nop 0
s_cmp_eq_u32 vcc_lo,-1
s_cbranch_scc1 Lg3_go0_%=
s_sleep 4
s_add_u32 m0,s78,8448
s_nop 0
buffer_load_dword v6,s[36:39],s68 offen offset:0 sc1 lds
s_waitcnt vmcnt(2)
s_barrier
ds_read_b32 v13,v7 offset:16640
s_mov_b32 s69,10496
s_waitcnt lgkmcnt(0)
v_cmp_eq_u32 vcc,s60,v13
s_nop 0
s_cmp_eq_u32 vcc_lo,-1
s_cbranch_scc1 Lg3_go0_%=
s_sleep 4
s_add_u32 s79,s79,1
s_cmp_lt_u32 s79,20000
s_cbranch_scc1 Lg3_poll0_%=
Lg3_go0_%=:
s_xor_b32 s62,s63,65536
s_lshr_b32 s61,s48,0
s_mov_b32 m0,s62
s_nop 0
buffer_load_dwordx4 v0,s[36:39],s61 offen offset:0 sc1 lds
buffer_load_dwordx4 v0,s[36:39],s61 offen offset:1024 sc1 lds
buffer_load_dwordx4 v0,s[36:39],s61 offen offset:2048 sc1 lds
buffer_load_dwordx4 v0,s[36:39],s61 offen offset:3072 sc1 lds
s_add_u32 m0,s62,16384
s_nop 0
buffer_load_dwordx4 v1,s[36:39],s61 offen offset:0 sc1 lds
buffer_load_dwordx4 v1,s[36:39],s61 offen offset:1024 sc1 lds
buffer_load_dwordx4 v1,s[36:39],s61 offen offset:2048 sc1 lds
buffer_load_dwordx4 v1,s[36:39],s61 offen offset:3072 sc1 lds
s_lshr_b32 s72,s61,5
buffer_load_dword v104,v8,s[36:39],s72 offen offset:0 sc1
v_add_u32 v84,s69,v7
s_waitcnt vmcnt(5)
s_barrier
ds_read_b32 v13,v84 offset:0
ds_read_b128 v[52:55],v2 offset:0
ds_read_b128 v[56:59],v2 offset:1024
ds_read_b128 v[60:63],v2 offset:2048
ds_read_b128 v[64:67],v2 offset:3072
ds_read_b128 v[68:71],v2 offset:4096
s_mul_i32 s70,s46,5
s_lshr_b32 s70,s70,6
buffer_store_dword v5,v4,s[36:39],s70 offen offset:0 sc0 sc1
s_mov_b32 s46,s59
s_mul_i32 s70,s47,5
s_lshr_b32 s70,s70,6
buffer_store_dword v5,v4,s[36:39],s70 offen offset:0 sc0 sc1
s_mov_b32 s47,s59
s_mul_i32 s70,s59,5)"
R"(
s_lshr_b32 s70,s70,6
buffer_store_dword v5,v4,s[36:39],s70 offen offset:0 sc0 sc1
s_waitcnt lgkmcnt(5)
v_cmp_eq_u32 vcc,s60,v13
s_nop 0
s_cmp_eq_u32 vcc_lo,-1
s_cselect_b32 s66,s49,s59
s_lshr_b32 s61,s66,0
s_xor_b32 s62,s64,65536
s_waitcnt lgkmcnt(3)
s_setpc_b64 s[90:91]
Lg3_gs0_%=:
s_xor_b32 s62,s63,65536
s_lshr_b32 s61,s48,0
s_cmp_eq_u32 s54,1
s_cselect_b32 s68,s49,s48
s_lshr_b32 s68,s68,5
s_mul_i32 s73,s47,7
s_lshr_b32 s73,s73,1
s_mov_b32 m0,s62
s_nop 0
buffer_load_dwordx4 v0,s[36:39],s61 offen offset:0 sc1 lds
v_mul_f32 v116,s75,v112
v_fma_f32 v113,v113,s75,v12
v_fma_f32 v113,-v116,v116,v113
v_max_f32 v113,v113,v12
v_rsq_f32 v112,v113
s_nop 0
v_mul_f32 v117,s76,v112
v_mul_f32 v118,v112,v112
v_mul_f32 v119,v113,v112
v_mul_f32 v118,s77,v118
v_mul_f32 v118,v118,v117
v_fma_f32 v40,-v116,v16,v40
buffer_load_dwordx4 v0,s[36:39],s61 offen offset:1024 sc1 lds
v_fma_f32 v41,-v116,v17,v41
v_fma_f32 v42,-v116,v18,v42
v_fma_f32 v43,-v116,v19,v43
v_mul_f32 v84,v40,v40
v_mul_f32 v85,v41,v41
v_mul_f32 v86,v42,v42
v_mul_f32 v87,v43,v43
v_fma_f32 v84,v118,v84,v117
v_fma_f32 v85,v118,v85,v117
v_fma_f32 v86,v118,v86,v117
v_fma_f32 v87,v118,v87,v117
buffer_load_dwordx4 v0,s[36:39],s61 offen offset:2048 sc1 lds
v_mul_f32 v84,v40,v84
v_mul_f32 v85,v41,v85
v_mul_f32 v86,v42,v86
v_mul_f32 v87,v43,v87
v_exp_f32 v84,v84
v_exp_f32 v85,v85
v_exp_f32 v86,v86
v_exp_f32 v87,v87
v_fma_f32 v84,v84,v119,v119
v_fma_f32 v85,v85,v119,v119
v_fma_f32 v86,v86,v119,v119
buffer_load_dwordx4 v0,s[36:39],s61 offen offset:3072 sc1 lds
v_fma_f32 v87,v87,v119,v119
v_rcp_f32 v84,v84
v_rcp_f32 v85,v85
v_rcp_f32 v86,v86
v_rcp_f32 v87,v87
v_mul_f32 v40,v40,v84
v_mul_f32 v41,v41,v85
v_mul_f32 v42,v42,v86
v_mul_f32 v43,v43,v87
v_cvt_pk_bf16_f32 v96,v40,v41
v_cvt_pk_bf16_f32 v97,v42,v43
s_add_u32 m0,s78,8192
s_nop 0
buffer_load_dword v6,s[36:39],s68 offen offset:0 sc1 lds
v_fma_f32 v44,-v116,v20,v44
v_fma_f32 v45,-v116,v21,v45
v_fma_f32 v46,-v116,v22,v46
v_fma_f32 v47,-v116,v23,v47
v_mul_f32 v88,v44,v44
v_mul_f32 v89,v45,v45
v_mul_f32 v90,v46,v46
v_mul_f32 v91,v47,v47
v_fma_f32 v88,v118,v88,v117
v_fma_f32 v89,v118,v89,v117
v_fma_f32 v90,v118,v90,v117
s_add_u32 m0,s62,16384
s_nop 0
buffer_load_dwordx4 v1,s[36:39],s61 offen offset:0 sc1 lds
v_fma_f32 v91,v118,v91,v117
v_mul_f32 v88,v44,v88
v_mul_f32 v89,v45,v89
v_mul_f32 v90,v46,v90
v_mul_f32 v91,v47,v91
v_exp_f32 v88,v88
v_exp_f32 v89,v89
v_exp_f32 v90,v90
v_exp_f32 v91,v91
v_fma_f32 v88,v88,v119,v119
v_fma_f32 v89,v89,v119,v119
buffer_load_dwordx4 v1,s[36:39],s61 offen offset:1024 sc1 lds
v_fma_f32 v90,v90,v119,v119
v_fma_f32 v91,v91,v119,v119
v_rcp_f32 v88,v88
v_rcp_f32 v89,v89
v_rcp_f32 v90,v90
v_rcp_f32 v91,v91
v_mul_f32 v44,v44,v88
v_mul_f32 v45,v45,v89
v_mul_f32 v46,v46,v90
v_mul_f32 v47,v47,v91
v_cvt_pk_bf16_f32 v98,v44,v45
buffer_load_dwordx4 v1,s[36:39],s61 offen offset:2048 sc1 lds
v_cvt_pk_bf16_f32 v99,v46,v47
buffer_store_dwordx4 v[96:99],v3,s[36:39],s73 offen offset:0 sc0 sc1
v_fma_f32 v48,-v116,v24,v48
v_fma_f32 v49,-v116,v25,v49
v_fma_f32 v50,-v116,v26,v50
v_fma_f32 v51,-v116,v27,v51
v_mul_f32 v92,v48,v48
v_mul_f32 v93,v49,v49
v_mul_f32 v94,v50,v50
v_mul_f32 v95,v51,v51
v_fma_f32 v92,v118,v92,v117
buffer_load_dwordx4 v1,s[36:39],s61 offen offset:3072 sc1 lds
v_fma_f32 v93,v118,v93,v117
v_fma_f32 v94,v118,v94,v117
v_fma_f32 v95,v118,v95,v117
v_mul_f32 v92,v48,v92
v_mul_f32 v93,v49,v93
v_mul_f32 v94,v50,v94
v_mul_f32 v95,v51,v95
v_exp_f32 v92,v92
v_exp_f32 v93,v93
v_exp_f32 v94,v94
v_exp_f32 v95,v95
s_lshr_b32 s72,s61,5
buffer_load_dword v104,v8,s[36:39],s72 offen offset:0 sc1
v_fma_f32 v92,v92,v119,v119
v_fma_f32 v93,v93,v119,v119
v_fma_f32 v94,v94,v119,v119
v_fma_f32 v95,v95,v119,v119
v_rcp_f32 v92,v92
v_rcp_f32 v93,v93
v_rcp_f32 v94,v94
v_rcp_f32 v95,v95
v_mul_f32 v48,v48,v92
v_mul_f32 v49,v49,v93
v_mul_f32 v50,v50,v94
v_mul_f32 v51,v51,v95
v_cvt_pk_bf16_f32 v100,v48,v49
v_cvt_pk_bf16_f32 v101,v50,v51
buffer_store_dwordx2 v[100:101],v9,s[36:39],s73 offen offset:0 sc0 sc1
s_waitcnt vmcnt(7)
s_barrier
ds_read_b32 v13,v7 offset:8192
ds_read_b128 v[52:55],v2 offset:0
ds_read_b128 v[56:59],v2 offset:1024
ds_read_b128 v[60:63],v2 offset:2048
ds_read_b128 v[64:67],v2 offset:3072
ds_read_b128 v[68:71],v2 offset:4096
s_mul_i32 s70,s46,5
s_lshr_b32 s70,s70,6
buffer_store_dword v5,v4,s[36:39],s70 offen offset:0 sc0 sc1
s_waitcnt lgkmcnt(5)
v_cmp_eq_u32 vcc,s60,v13
s_nop 0
s_cmp_eq_u32 vcc_hi,-1
s_cselect_b32 s66,s49,s59
s_lshr_b32 s61,s66,0
s_xor_b32 s62,s64,65536
s_mov_b32 s46,s47
s_mov_b32 s47,s59
s_mul_i32 s70,s59,5
s_lshr_b32 s70,s70,6
buffer_store_dword v5,v4,s[36:39],s70 offen offset:0 sc0 sc1
s_waitcnt lgkmcnt(3)
s_setpc_b64 s[90:91]
Lg3_done0_%=:
s_waitcnt vmcnt(0)
s_barrier
s_mul_i32 s70,s46,5
s_lshr_b32 s70,s70,6
buffer_store_dword v5,v4,s[36:39],s70 offen offset:0 sc0 sc1
s_mul_i32 s70,s47,5
s_lshr_b32 s70,s70,6
buffer_store_dword v5,v4,s[36:39],s70 offen offset:0 sc0 sc1
s_branch Lg3_done_%=
Lg3_slow1_%=:
s_cmp_lt_u32 s67,s59
s_cbranch_scc1 Lg3_gs1_%=
Lg3_slowb1_%=:
s_cmp_eq_u32 s54,1
s_cselect_b32 s68,s50,s49
s_lshr_b32 s68,s68,5
s_add_u32 m0,s78,8192
s_nop 0
buffer_load_dword v6,s[36:39],s68 offen offset:0 sc1 lds
s_mul_i32 s73,s48,7
s_lshr_b32 s73,s73,1
v_mul_f32 v116,s75,v112
v_fma_f32 v113,v113,s75,v12)"
R"(
v_fma_f32 v113,-v116,v116,v113
v_max_f32 v113,v113,v12
v_rsq_f32 v112,v113
s_nop 0
v_mul_f32 v117,s76,v112
v_mul_f32 v118,v112,v112
v_mul_f32 v119,v113,v112
v_mul_f32 v118,s77,v118
v_mul_f32 v118,v118,v117
v_fma_f32 v28,-v116,v16,v28
v_fma_f32 v29,-v116,v17,v29
v_fma_f32 v30,-v116,v18,v30
v_fma_f32 v31,-v116,v19,v31
v_mul_f32 v84,v28,v28
v_mul_f32 v85,v29,v29
v_mul_f32 v86,v30,v30
v_mul_f32 v87,v31,v31
v_fma_f32 v84,v118,v84,v117
v_fma_f32 v85,v118,v85,v117
v_fma_f32 v86,v118,v86,v117
v_fma_f32 v87,v118,v87,v117
v_mul_f32 v84,v28,v84
v_mul_f32 v85,v29,v85
v_mul_f32 v86,v30,v86
v_mul_f32 v87,v31,v87
v_exp_f32 v84,v84
v_exp_f32 v85,v85
v_exp_f32 v86,v86
v_exp_f32 v87,v87
v_fma_f32 v84,v84,v119,v119
v_fma_f32 v85,v85,v119,v119
v_fma_f32 v86,v86,v119,v119
v_fma_f32 v87,v87,v119,v119
v_rcp_f32 v84,v84
v_rcp_f32 v85,v85
v_rcp_f32 v86,v86
v_rcp_f32 v87,v87
v_mul_f32 v28,v28,v84
v_mul_f32 v29,v29,v85
v_mul_f32 v30,v30,v86
v_mul_f32 v31,v31,v87
v_cvt_pk_bf16_f32 v96,v28,v29
v_cvt_pk_bf16_f32 v97,v30,v31
v_fma_f32 v32,-v116,v20,v32
v_fma_f32 v33,-v116,v21,v33
v_fma_f32 v34,-v116,v22,v34
v_fma_f32 v35,-v116,v23,v35
v_mul_f32 v88,v32,v32
v_mul_f32 v89,v33,v33
v_mul_f32 v90,v34,v34
v_mul_f32 v91,v35,v35
v_fma_f32 v88,v118,v88,v117
v_fma_f32 v89,v118,v89,v117
v_fma_f32 v90,v118,v90,v117
v_fma_f32 v91,v118,v91,v117
v_mul_f32 v88,v32,v88
s_add_u32 m0,s78,8448
s_nop 0
buffer_load_dword v6,s[36:39],s68 offen offset:0 sc1 lds
v_mul_f32 v89,v33,v89
v_mul_f32 v90,v34,v90
v_mul_f32 v91,v35,v91
v_exp_f32 v88,v88
v_exp_f32 v89,v89
v_exp_f32 v90,v90
v_exp_f32 v91,v91
v_fma_f32 v88,v88,v119,v119
v_fma_f32 v89,v89,v119,v119
v_fma_f32 v90,v90,v119,v119
v_fma_f32 v91,v91,v119,v119
v_rcp_f32 v88,v88
v_rcp_f32 v89,v89
v_rcp_f32 v90,v90
v_rcp_f32 v91,v91
v_mul_f32 v32,v32,v88
v_mul_f32 v33,v33,v89
v_mul_f32 v34,v34,v90
v_mul_f32 v35,v35,v91
v_cvt_pk_bf16_f32 v98,v32,v33
v_cvt_pk_bf16_f32 v99,v34,v35
buffer_store_dwordx4 v[96:99],v3,s[36:39],s73 offen offset:0 sc0 sc1
v_fma_f32 v36,-v116,v24,v36
v_fma_f32 v37,-v116,v25,v37
v_fma_f32 v38,-v116,v26,v38
v_fma_f32 v39,-v116,v27,v39
v_mul_f32 v92,v36,v36
v_mul_f32 v93,v37,v37
v_mul_f32 v94,v38,v38
v_mul_f32 v95,v39,v39
v_fma_f32 v92,v118,v92,v117
v_fma_f32 v93,v118,v93,v117
v_fma_f32 v94,v118,v94,v117
v_fma_f32 v95,v118,v95,v117
v_mul_f32 v92,v36,v92
v_mul_f32 v93,v37,v93
v_mul_f32 v94,v38,v94
v_mul_f32 v95,v39,v95
v_exp_f32 v92,v92
v_exp_f32 v93,v93
v_exp_f32 v94,v94
v_exp_f32 v95,v95
v_fma_f32 v92,v92,v119,v119
v_fma_f32 v93,v93,v119,v119
v_fma_f32 v94,v94,v119,v119
v_fma_f32 v95,v95,v119,v119
v_rcp_f32 v92,v92
v_rcp_f32 v93,v93
v_rcp_f32 v94,v94
v_rcp_f32 v95,v95
v_mul_f32 v36,v36,v92
v_mul_f32 v37,v37,v93
v_mul_f32 v38,v38,v94
v_mul_f32 v39,v39,v95
v_cvt_pk_bf16_f32 v100,v36,v37
v_cvt_pk_bf16_f32 v101,v38,v39
buffer_store_dwordx2 v[100:101],v9,s[36:39],s73 offen offset:0 sc0 sc1
s_cmp_ge_u32 s49,s59
s_cbranch_scc1 Lg3_done1_%=
s_add_u32 m0,s78,16384
s_nop 0
buffer_load_dword v6,s[36:39],s68 offen offset:0 sc1 lds
s_waitcnt vmcnt(4)
s_barrier
ds_read_b32 v13,v7 offset:8192
s_mul_i32 s70,s47,5
s_lshr_b32 s70,s70,6
buffer_store_dword v5,v4,s[36:39],s70 offen offset:0 sc0 sc1
s_mov_b32 s47,s59
s_mov_b32 s69,18432
s_waitcnt lgkmcnt(0)
v_cmp_eq_u32 vcc,s60,v13
s_nop 0
s_cmp_eq_u32 vcc_lo,-1
s_cbranch_scc1 Lg3_go1_%=
s_add_u32 m0,s78,16640
s_nop 0
buffer_load_dword v6,s[36:39],s68 offen offset:0 sc1 lds
s_waitcnt vmcnt(5)
s_barrier
ds_read_b32 v13,v7 offset:8448
s_mov_b32 s69,18688
s_waitcnt lgkmcnt(0)
v_cmp_eq_u32 vcc,s60,v13
s_nop 0
s_cmp_eq_u32 vcc_lo,-1
s_cbranch_scc1 Lg3_go1_%=
s_add_u32 m0,s78,8192
s_nop 0
buffer_load_dword v6,s[36:39],s68 offen offset:0 sc1 lds
s_waitcnt vmcnt(3)
s_barrier
ds_read_b32 v13,v7 offset:16384
s_mul_i32 s70,s48,5
s_lshr_b32 s70,s70,6
buffer_store_dword v5,v4,s[36:39],s70 offen offset:0 sc0 sc1
s_mov_b32 s48,s59
s_mov_b32 s69,10240
s_waitcnt lgkmcnt(0)
v_cmp_eq_u32 vcc,s60,v13
s_nop 0
s_cmp_eq_u32 vcc_lo,-1
s_cbranch_scc1 Lg3_go1_%=
s_add_u32 m0,s78,8448
s_nop 0
buffer_load_dword v6,s[36:39],s68 offen offset:0 sc1 lds
s_waitcnt vmcnt(3)
s_barrier
ds_read_b32 v13,v7 offset:16640
s_mov_b32 s69,10496
s_waitcnt lgkmcnt(0)
v_cmp_eq_u32 vcc,s60,v13
s_nop 0
s_cmp_eq_u32 vcc_lo,-1
s_cbranch_scc1 Lg3_go1_%=
Lg3_poll1_%=:
s_add_u32 m0,s78,16384
s_nop 0
buffer_load_dword v6,s[36:39],s68 offen offset:0 sc1 lds
s_waitcnt vmcnt(2)
s_barrier
ds_read_b32 v13,v7 offset:8192
s_mov_b32 s69,18432
s_waitcnt lgkmcnt(0)
v_cmp_eq_u32 vcc,s60,v13
s_nop 0
s_cmp_eq_u32 vcc_lo,-1
s_cbranch_scc1 Lg3_go1_%=
s_sleep 4
s_add_u32 m0,s78,16640
s_nop 0
buffer_load_dword v6,s[36:39],s68 offen offset:0 sc1 lds
s_waitcnt vmcnt(2)
s_barrier
ds_read_b32 v13,v7 offset:8448
s_mov_b32 s69,18688
s_waitcnt lgkmcnt(0)
v_cmp_eq_u32 vcc,s60,v13
s_nop 0
s_cmp_eq_u32 vcc_lo,-1
s_cbranch_scc1 Lg3_go1_%=)"
R"(
s_sleep 4
s_add_u32 m0,s78,8192
s_nop 0
buffer_load_dword v6,s[36:39],s68 offen offset:0 sc1 lds
s_waitcnt vmcnt(2)
s_barrier
ds_read_b32 v13,v7 offset:16384
s_mov_b32 s69,10240
s_waitcnt lgkmcnt(0)
v_cmp_eq_u32 vcc,s60,v13
s_nop 0
s_cmp_eq_u32 vcc_lo,-1
s_cbranch_scc1 Lg3_go1_%=
s_sleep 4
s_add_u32 m0,s78,8448
s_nop 0
buffer_load_dword v6,s[36:39],s68 offen offset:0 sc1 lds
s_waitcnt vmcnt(2)
s_barrier
ds_read_b32 v13,v7 offset:16640
s_mov_b32 s69,10496
s_waitcnt lgkmcnt(0)
v_cmp_eq_u32 vcc,s60,v13
s_nop 0
s_cmp_eq_u32 vcc_lo,-1
s_cbranch_scc1 Lg3_go1_%=
s_sleep 4
s_add_u32 s79,s79,1
s_cmp_lt_u32 s79,20000
s_cbranch_scc1 Lg3_poll1_%=
Lg3_go1_%=:
s_xor_b32 s62,s64,65536
s_lshr_b32 s61,s49,0
s_mov_b32 m0,s62
s_nop 0
buffer_load_dwordx4 v0,s[36:39],s61 offen offset:0 sc1 lds
buffer_load_dwordx4 v0,s[36:39],s61 offen offset:1024 sc1 lds
buffer_load_dwordx4 v0,s[36:39],s61 offen offset:2048 sc1 lds
buffer_load_dwordx4 v0,s[36:39],s61 offen offset:3072 sc1 lds
s_add_u32 m0,s62,16384
s_nop 0
buffer_load_dwordx4 v1,s[36:39],s61 offen offset:0 sc1 lds
buffer_load_dwordx4 v1,s[36:39],s61 offen offset:1024 sc1 lds
buffer_load_dwordx4 v1,s[36:39],s61 offen offset:2048 sc1 lds
buffer_load_dwordx4 v1,s[36:39],s61 offen offset:3072 sc1 lds
s_lshr_b32 s72,s61,5
buffer_load_dword v108,v8,s[36:39],s72 offen offset:0 sc1
v_add_u32 v84,s69,v7
s_waitcnt vmcnt(5)
s_barrier
ds_read_b32 v13,v84 offset:0
ds_read_b128 v[52:55],v2 offset:32768
ds_read_b128 v[56:59],v2 offset:33792
ds_read_b128 v[60:63],v2 offset:34816
ds_read_b128 v[64:67],v2 offset:35840
ds_read_b128 v[68:71],v2 offset:36864
s_mul_i32 s70,s47,5
s_lshr_b32 s70,s70,6
buffer_store_dword v5,v4,s[36:39],s70 offen offset:0 sc0 sc1
s_mov_b32 s47,s59
s_mul_i32 s70,s48,5
s_lshr_b32 s70,s70,6
buffer_store_dword v5,v4,s[36:39],s70 offen offset:0 sc0 sc1
s_mov_b32 s48,s59
s_mul_i32 s70,s59,5
s_lshr_b32 s70,s70,6
buffer_store_dword v5,v4,s[36:39],s70 offen offset:0 sc0 sc1
s_waitcnt lgkmcnt(5)
v_cmp_eq_u32 vcc,s60,v13
s_nop 0
s_cmp_eq_u32 vcc_lo,-1
s_cselect_b32 s67,s50,s59
s_lshr_b32 s61,s67,0
s_xor_b32 s62,s63,65536
s_waitcnt lgkmcnt(3)
s_setpc_b64 s[92:93]
Lg3_gs1_%=:
s_xor_b32 s62,s64,65536
s_lshr_b32 s61,s49,0
s_cmp_eq_u32 s54,1
s_cselect_b32 s68,s50,s49
s_lshr_b32 s68,s68,5
s_mul_i32 s73,s48,7
s_lshr_b32 s73,s73,1
s_mov_b32 m0,s62
s_nop 0
buffer_load_dwordx4 v0,s[36:39],s61 offen offset:0 sc1 lds
v_mul_f32 v116,s75,v112
v_fma_f32 v113,v113,s75,v12
v_fma_f32 v113,-v116,v116,v113
v_max_f32 v113,v113,v12
v_rsq_f32 v112,v113
s_nop 0
v_mul_f32 v117,s76,v112
v_mul_f32 v118,v112,v112
v_mul_f32 v119,v113,v112
v_mul_f32 v118,s77,v118
v_mul_f32 v118,v118,v117
v_fma_f32 v28,-v116,v16,v28
buffer_load_dwordx4 v0,s[36:39],s61 offen offset:1024 sc1 lds
v_fma_f32 v29,-v116,v17,v29
v_fma_f32 v30,-v116,v18,v30
v_fma_f32 v31,-v116,v19,v31
v_mul_f32 v84,v28,v28
v_mul_f32 v85,v29,v29
v_mul_f32 v86,v30,v30
v_mul_f32 v87,v31,v31
v_fma_f32 v84,v118,v84,v117
v_fma_f32 v85,v118,v85,v117
v_fma_f32 v86,v118,v86,v117
v_fma_f32 v87,v118,v87,v117
buffer_load_dwordx4 v0,s[36:39],s61 offen offset:2048 sc1 lds
v_mul_f32 v84,v28,v84
v_mul_f32 v85,v29,v85
v_mul_f32 v86,v30,v86
v_mul_f32 v87,v31,v87
v_exp_f32 v84,v84
v_exp_f32 v85,v85
v_exp_f32 v86,v86
v_exp_f32 v87,v87
v_fma_f32 v84,v84,v119,v119
v_fma_f32 v85,v85,v119,v119
v_fma_f32 v86,v86,v119,v119
buffer_load_dwordx4 v0,s[36:39],s61 offen offset:3072 sc1 lds
v_fma_f32 v87,v87,v119,v119
v_rcp_f32 v84,v84
v_rcp_f32 v85,v85
v_rcp_f32 v86,v86
v_rcp_f32 v87,v87
v_mul_f32 v28,v28,v84
v_mul_f32 v29,v29,v85
v_mul_f32 v30,v30,v86
v_mul_f32 v31,v31,v87
v_cvt_pk_bf16_f32 v96,v28,v29
v_cvt_pk_bf16_f32 v97,v30,v31
s_add_u32 m0,s78,8192
s_nop 0
buffer_load_dword v6,s[36:39],s68 offen offset:0 sc1 lds
v_fma_f32 v32,-v116,v20,v32
v_fma_f32 v33,-v116,v21,v33
v_fma_f32 v34,-v116,v22,v34
v_fma_f32 v35,-v116,v23,v35
v_mul_f32 v88,v32,v32
v_mul_f32 v89,v33,v33
v_mul_f32 v90,v34,v34
v_mul_f32 v91,v35,v35
v_fma_f32 v88,v118,v88,v117
v_fma_f32 v89,v118,v89,v117
v_fma_f32 v90,v118,v90,v117
s_add_u32 m0,s62,16384
s_nop 0
buffer_load_dwordx4 v1,s[36:39],s61 offen offset:0 sc1 lds
v_fma_f32 v91,v118,v91,v117
v_mul_f32 v88,v32,v88
v_mul_f32 v89,v33,v89
v_mul_f32 v90,v34,v90
v_mul_f32 v91,v35,v91
v_exp_f32 v88,v88
v_exp_f32 v89,v89
v_exp_f32 v90,v90
v_exp_f32 v91,v91
v_fma_f32 v88,v88,v119,v119
v_fma_f32 v89,v89,v119,v119
buffer_load_dwordx4 v1,s[36:39],s61 offen offset:1024 sc1 lds
v_fma_f32 v90,v90,v119,v119
v_fma_f32 v91,v91,v119,v119
v_rcp_f32 v88,v88
v_rcp_f32 v89,v89
v_rcp_f32 v90,v90
v_rcp_f32 v91,v91
v_mul_f32 v32,v32,v88
v_mul_f32 v33,v33,v89
v_mul_f32 v34,v34,v90
v_mul_f32 v35,v35,v91
v_cvt_pk_bf16_f32 v98,v32,v33
buffer_load_dwordx4 v1,s[36:39],s61 offen offset:2048 sc1 lds
v_cvt_pk_bf16_f32 v99,v34,v35
buffer_store_dwordx4 v[96:99],v3,s[36:39],s73 offen offset:0 sc0 sc1
v_fma_f32 v36,-v116,v24,v36
v_fma_f32 v37,-v116,v25,v37
v_fma_f32 v38,-v116,v26,v38
v_fma_f32 v39,-v116,v27,v39
v_mul_f32 v92,v36,v36
v_mul_f32 v93,v37,v37
v_mul_f32 v94,v38,v38
v_mul_f32 v95,v39,v39
v_fma_f32 v92,v118,v92,v117
buffer_load_dwordx4 v1,s[36:39],s61 offen offset:3072 sc1 lds
v_fma_f32 v93,v118,v93,v117
v_fma_f32 v94,v118,v94,v117
v_fma_f32 v95,v118,v95,v117
v_mul_f32 v92,v36,v92
v_mul_f32 v93,v37,v93
v_mul_f32 v94,v38,v94
v_mul_f32 v95,v39,v95
v_exp_f32 v92,v92
v_exp_f32 v93,v93
v_exp_f32 v94,v94
v_exp_f32 v95,v95
s_lshr_b32 s72,s61,5)"
R"(
buffer_load_dword v108,v8,s[36:39],s72 offen offset:0 sc1
v_fma_f32 v92,v92,v119,v119
v_fma_f32 v93,v93,v119,v119
v_fma_f32 v94,v94,v119,v119
v_fma_f32 v95,v95,v119,v119
v_rcp_f32 v92,v92
v_rcp_f32 v93,v93
v_rcp_f32 v94,v94
v_rcp_f32 v95,v95
v_mul_f32 v36,v36,v92
v_mul_f32 v37,v37,v93
v_mul_f32 v38,v38,v94
v_mul_f32 v39,v39,v95
v_cvt_pk_bf16_f32 v100,v36,v37
v_cvt_pk_bf16_f32 v101,v38,v39
buffer_store_dwordx2 v[100:101],v9,s[36:39],s73 offen offset:0 sc0 sc1
s_waitcnt vmcnt(7)
s_barrier
ds_read_b32 v13,v7 offset:8192
ds_read_b128 v[52:55],v2 offset:32768
ds_read_b128 v[56:59],v2 offset:33792
ds_read_b128 v[60:63],v2 offset:34816
ds_read_b128 v[64:67],v2 offset:35840
ds_read_b128 v[68:71],v2 offset:36864
s_mul_i32 s70,s47,5
s_lshr_b32 s70,s70,6
buffer_store_dword v5,v4,s[36:39],s70 offen offset:0 sc0 sc1
s_waitcnt lgkmcnt(5)
v_cmp_eq_u32 vcc,s60,v13
s_nop 0
s_cmp_eq_u32 vcc_hi,-1
s_cselect_b32 s67,s50,s59
s_lshr_b32 s61,s67,0
s_xor_b32 s62,s63,65536
s_mov_b32 s47,s48
s_mov_b32 s48,s59
s_mul_i32 s70,s59,5
s_lshr_b32 s70,s70,6
buffer_store_dword v5,v4,s[36:39],s70 offen offset:0 sc0 sc1
s_waitcnt lgkmcnt(3)
s_setpc_b64 s[92:93]
Lg3_done1_%=:
s_waitcnt vmcnt(0)
s_barrier
s_mul_i32 s70,s47,5
s_lshr_b32 s70,s70,6
buffer_store_dword v5,v4,s[36:39],s70 offen offset:0 sc0 sc1
s_mul_i32 s70,s48,5
s_lshr_b32 s70,s70,6
buffer_store_dword v5,v4,s[36:39],s70 offen offset:0 sc0 sc1
s_branch Lg3_done_%=
Lg3_done_%=:
s_waitcnt vmcnt(0) lgkmcnt(0)
s_barrier
s_endpgm
Lcode_g3_e:)"
  :: "s"(ws), "s"(wave), "s"(sStep), "s"(sX1), "s"(sX2), "s"(sU0), "s"(sWw), "s"(sBuf), "v"(vSrc), "v"(vA), "v"(vOut), "v"(vFlag), "v"(vPollOff), "v"(vSnapRd), "v"(vAuxS), "v"(vOut2), "v"(vStW), "v"(vStR), "v"(vWd0), "v"(vWd1), "v"(vWr0), "v"(vWr1), "v"(vWr2), "s"(w3) : "v0", "v1", "v2", "v3", "v4", "v5", "v6", "v7", "v8", "v9", "v10", "v11", "v12", "v13", "v14", "v15", "v16", "v17", "v18", "v19", "v20", "v21", "v22", "v23", "v24", "v25", "v26", "v27", "v28", "v29", "v30", "v31", "v32", "v33", "v34", "v35", "v36", "v37", "v38", "v39", "v40", "v41", "v42", "v43", "v44", "v45", "v46", "v47", "v48", "v49", "v50", "v51", "v52", "v53", "v54", "v55", "v56", "v57", "v58", "v59", "v60", "v61", "v62", "v63", "v64", "v65", "v66", "v67", "v68", "v69", "v70", "v71", "v72", "v73", "v74", "v75", "v76", "v77", "v78", "v79", "v80", "v81", "v82", "v83", "v84", "v85", "v86", "v87", "v88", "v89", "v90", "v91", "v92", "v93", "v94", "v95", "v96", "v97", "v98", "v99", "v100", "v101", "v102", "v103", "v104", "v105", "v106", "v107", "v108", "v109", "v110", "v111", "v112", "v113", "v114", "v115", "v116", "v117", "v118", "v119", "v120", "v121", "v122", "v123", "v124", "v125", "v126", "v127", "v128", "v129", "v130", "v131", "v132", "v133", "v134", "v135", "v136", "v137", "v138", "v139", "v140", "v141", "v142", "v143", "v144", "v145", "v146", "v147", "v148", "v149", "v150", "v151", "v152", "v153", "v154", "v155", "v156", "v157", "v158", "v159", "v160", "v161", "v162", "v163", "v164", "v165", "v166", "v167", "v168", "v169", "v170", "v171", "v172", "v173", "v174", "v175", "v176", "v177", "v178", "v179", "v180", "v181", "v182", "v183", "v184", "v185", "v186", "v187", "v188", "v189", "v190", "v191", "v192", "v193", "v194", "v195", "v196", "v197", "v198", "v199", "v200", "v201", "v202", "v203", "v204", "v205", "v206", "v207", "v208", "v209", "v210", "v211", "v212", "v213", "v214", "v215", "v216", "v217", "v218", "v219", "v220", "v221", "v222", "v223", "v224", "v225", "v226", "v227", "v228", "v229", "v230", "v231", "v255", "a0", "a1", "a2", "a3", "a4", "a5", "a6", "a7", "a8", "a9", "a10", "a11", "a12", "a13", "a14", "a15", "a16", "a17", "a18", "a19", "a20", "a21", "a22", "a23", "a24", "a25", "a26", "a27", "a28", "a29", "a30", "a31", "a32", "a33", "a34", "a35", "a36", "a37", "a38", "a39", "a40", "a41", "a42", "a43", "a44", "a45", "a46", "a47", "a48", "a49", "a50", "a51", "a52", "a53", "a54", "a55", "a56", "a57", "a58", "a59", "a60", "a61", "a62", "a63", "a64", "a65", "a66", "a67", "a68", "a69", "a70", "a71", "a72", "a73", "a74", "a75", "a76", "a77", "a78", "a79", "a80", "a81", "a82", "a83", "a84", "a85", "a86", "a87", "a88", "a89", "a90", "a91", "a92", "a93", "a94", "a95", "a96", "a97", "a98", "a99", "a100", "a101", "a102", "a103", "a104", "a105", "a106", "a107", "a108", "a109", "a110", "a111", "a112", "a113", "a114", "a115", "a116", "a117", "a118", "a119", "a120", "a121", "a122", "a123", "a124", "a125", "a126", "a127", "a128", "a129", "a130", "a131", "a132", "a133", "a134", "a135", "a136", "a137", "a138", "a139", "a140", "a141", "a142", "a143", "a144", "a145", "a146", "a147", "a148", "a149", "a150", "a151", "a152", "a153", "a154", "a155", "a156", "a157", "a158", "a159", "a160", "a161", "a162", "a163", "a164", "a165", "a166", "a167", "a168", "a169", "a170", "a171", "a172", "a173", "a174", "a175", "a176", "a177", "a178", "a179", "a180", "a181", "a182", "a183", "a184", "a185", "a186", "a187", "a188", "a189", "a190", "a191", "a192", "a193", "a194", "a195", "a196", "a197", "a198", "a199", "a200", "a201", "a202", "a203", "a204", "a205", "a206", "a207", "a208", "a209", "a210", "a211", "a212", "a213", "a214", "a215", "a216", "a217", "a218", "a219", "a220", "a221", "a222", "a223", "a224", "a225", "a226", "a227", "a228", "a229", "a230", "a231", "a232", "a233", "a234", "a235", "a236", "a237", "a238", "a239", "a240", "a241", "a242", "a243", "a244", "a245", "a246", "a247", "a248", "a249", "a250", "a251", "a252", "a253", "a254", "a255", "s36", "s37", "s38", "s39", "s40", "s41", "s42", "s43", "s44", "s45", "s46", "s47", "s48", "s49", "s50", "s51", "s52", "s53", "s54", "s55", "s56", "s57", "s58", "s59", "s60", "s61", "s62", "s63", "s64", "s65", "s66", "s67", "s68", "s69", "s70", "s71", "s72", "s73", "s74", "s75", "s76", "s77", "s78", "s79", "s80", "s81", "s82", "s83", "s84", "s85", "s86", "s87", "s88", "s89", "s90", "s91", "s92", "s93", "s94", "s95", "s96", "s97", "s98", "s99", "vcc", "scc", "memory"); } else { asm volatile(R"(
Lcode_g3o_b:
s_mov_b64 s[36:37],%0
s_mov_b32 s38,122727552
s_mov_b32 s39,0x00020000
s_mov_b32 s54,%1
s_mov_b32 s55,%2
s_mov_b32 s56,%3
s_mov_b32 s58,%4
s_mov_b32 s83,%5
s_mov_b32 s82,%6
s_mov_b32 s84,%7
v_mov_b32 v0,%8
v_mov_b32 v2,%9
v_mov_b32 v3,%10
v_mov_b32 v4,%11
v_mov_b32 v6,%12
v_mov_b32 v7,%13
v_mov_b32 v8,%14
v_mov_b32 v9,%15
v_mov_b32 v10,%16
v_mov_b32 v11,%17
v_mov_b32 v24,%18
v_mov_b32 v25,%19
v_mov_b32 v26,%20
v_mov_b32 v27,%21
v_mov_b32 v28,%22
s_mov_b64 s[40:41],%23
s_mov_b32 s42,7340032
s_mov_b32 s43,0x00020000
s_lshl_b32 s88,s54,11
s_mov_b32 s69,122727424
buffer_load_dword v5,off,s[36:39],s69 sc1
s_add_u32 m0,s88,0
s_add_u32 s61,s82,0
buffer_load_dwordx4 v24,s[40:43],s61 offen offset:0  lds
s_add_u32 m0,s88,1024
s_nop 0
buffer_load_dwordx4 v25,s[40:43],s61 offen offset:0  lds
s_add_u32 m0,s88,8064
s_nop 0
buffer_load_dwordx4 v24,s[40:43],s61 offen offset:128  lds
s_add_u32 m0,s88,9088
s_nop 0
buffer_load_dwordx4 v25,s[40:43],s61 offen offset:128  lds
s_add_u32 m0,s88,16384
s_add_u32 s61,s82,458752
buffer_load_dwordx4 v24,s[40:43],s61 offen offset:0  lds
s_add_u32 m0,s88,17408
s_nop 0
buffer_load_dwordx4 v25,s[40:43],s61 offen offset:0  lds
s_add_u32 m0,s88,24448
s_nop 0
buffer_load_dwordx4 v24,s[40:43],s61 offen offset:128  lds
s_add_u32 m0,s88,25472
s_nop 0
buffer_load_dwordx4 v25,s[40:43],s61 offen offset:128  lds
s_add_u32 m0,s88,32768
s_add_u32 s61,s82,917504
buffer_load_dwordx4 v24,s[40:43],s61 offen offset:0  lds
s_add_u32 m0,s88,33792
s_nop 0
buffer_load_dwordx4 v25,s[40:43],s61 offen offset:0  lds
s_add_u32 m0,s88,40832
s_nop 0
buffer_load_dwordx4 v24,s[40:43],s61 offen offset:128  lds
s_add_u32 m0,s88,41856
s_nop 0
buffer_load_dwordx4 v25,s[40:43],s61 offen offset:128  lds
s_add_u32 m0,s88,49152
s_add_u32 s61,s82,1376256
buffer_load_dwordx4 v24,s[40:43],s61 offen offset:0  lds
s_add_u32 m0,s88,50176
s_nop 0
buffer_load_dwordx4 v25,s[40:43],s61 offen offset:0  lds
s_add_u32 m0,s88,57216
s_nop 0
buffer_load_dwordx4 v24,s[40:43],s61 offen offset:128  lds
s_add_u32 m0,s88,58240
s_nop 0
buffer_load_dwordx4 v25,s[40:43],s61 offen offset:128  lds
s_add_u32 m0,s88,65536
s_add_u32 s61,s82,1835008
buffer_load_dwordx4 v24,s[40:43],s61 offen offset:0  lds
s_add_u32 m0,s88,66560
s_nop 0
buffer_load_dwordx4 v25,s[40:43],s61 offen offset:0  lds
s_add_u32 m0,s88,73600
s_nop 0
buffer_load_dwordx4 v24,s[40:43],s61 offen offset:128  lds
s_add_u32 m0,s88,74624
s_nop 0
buffer_load_dwordx4 v25,s[40:43],s61 offen offset:128  lds
s_add_u32 m0,s88,81920
s_add_u32 s61,s82,2293760
buffer_load_dwordx4 v24,s[40:43],s61 offen offset:0  lds
s_add_u32 m0,s88,82944
s_nop 0
buffer_load_dwordx4 v25,s[40:43],s61 offen offset:0  lds
s_add_u32 m0,s88,89984
s_nop 0
buffer_load_dwordx4 v24,s[40:43],s61 offen offset:128  lds
s_add_u32 m0,s88,91008
s_nop 0
buffer_load_dwordx4 v25,s[40:43],s61 offen offset:128  lds
s_add_u32 m0,s88,98304
s_add_u32 s61,s82,2752512
buffer_load_dwordx4 v24,s[40:43],s61 offen offset:0  lds
s_add_u32 m0,s88,99328
s_nop 0
buffer_load_dwordx4 v25,s[40:43],s61 offen offset:0  lds
s_add_u32 m0,s88,106368
s_nop 0
buffer_load_dwordx4 v24,s[40:43],s61 offen offset:128  lds
s_add_u32 m0,s88,107392
s_nop 0
buffer_load_dwordx4 v25,s[40:43],s61 offen offset:128  lds
v_add_u32 v32,49152,v26
v_add_u32 v33,49152,v27
v_add_u32 v34,98304,v26
v_add_u32 v35,98304,v27
v_mov_b32 v72,0x3f803f80
v_mov_b32 v73,0x3f803f80
v_mov_b32 v74,0x3f803f80
v_mov_b32 v75,0x3f803f80
s_waitcnt vmcnt(24)
s_barrier
s_add_u32 m0,s88,114688
s_add_u32 s61,s82,3211264
buffer_load_dwordx4 v24,s[40:43],s61 offen offset:0  lds
s_add_u32 m0,s88,115712
ds_read_b64_tr_b16 a[0:1],v26 offset:0
buffer_load_dwordx4 v25,s[40:43],s61 offen offset:0  lds
s_add_u32 m0,s88,122752
ds_read_b64_tr_b16 a[2:3],v26 offset:512
buffer_load_dwordx4 v24,s[40:43],s61 offen offset:128  lds
s_add_u32 m0,s88,123776
ds_read_b64_tr_b16 a[4:5],v27 offset:0
buffer_load_dwordx4 v25,s[40:43],s61 offen offset:128  lds
ds_read_b64_tr_b16 a[6:7],v27 offset:512
ds_read_b64_tr_b16 a[8:9],v26 offset:4096
ds_read_b64_tr_b16 a[10:11],v26 offset:4608
ds_read_b64_tr_b16 a[12:13],v27 offset:4096
ds_read_b64_tr_b16 a[14:15],v27 offset:4608
s_waitcnt vmcnt(24)
s_barrier
s_add_u32 m0,s88,131072
s_add_u32 s61,s82,3670016
buffer_load_dwordx4 v24,s[40:43],s61 offen offset:0  lds
s_add_u32 m0,s88,132096
ds_read_b64_tr_b16 a[16:17],v26 offset:16384
buffer_load_dwordx4 v25,s[40:43],s61 offen offset:0  lds
s_add_u32 m0,s88,139136
ds_read_b64_tr_b16 a[18:19],v26 offset:16896
buffer_load_dwordx4 v24,s[40:43],s61 offen offset:128  lds
s_add_u32 m0,s88,140160
ds_read_b64_tr_b16 a[20:21],v27 offset:16384
buffer_load_dwordx4 v25,s[40:43],s61 offen offset:128  lds
ds_read_b64_tr_b16 a[22:23],v27 offset:16896
ds_read_b64_tr_b16 a[24:25],v26 offset:20480
ds_read_b64_tr_b16 a[26:27],v26 offset:20992
ds_read_b64_tr_b16 a[28:29],v27 offset:20480
ds_read_b64_tr_b16 a[30:31],v27 offset:20992
s_waitcnt lgkmcnt(8)
v_mfma_f32_16x16x32_bf16 v[16:19],a[0:3],v[72:75],0
v_mfma_f32_16x16x32_bf16 v[20:23],a[4:7],v[72:75],0
v_mfma_f32_16x16x32_bf16 v[16:19],a[8:11],v[72:75],v[16:19]
v_mfma_f32_16x16x32_bf16 v[20:23],a[12:15],v[72:75],v[20:23]
s_waitcnt vmcnt(24)
s_barrier
s_add_u32 m0,s88,0
s_add_u32 s61,s82,4128768
buffer_load_dwordx4 v24,s[40:43],s61 offen offset:0  lds
s_add_u32 m0,s88,1024
ds_read_b64_tr_b16 a[32:33],v26 offset:32768
buffer_load_dwordx4 v25,s[40:43],s61 offen offset:0  lds
s_add_u32 m0,s88,8064
ds_read_b64_tr_b16 a[34:35],v26 offset:33280
buffer_load_dwordx4 v24,s[40:43],s61 offen offset:128  lds
s_add_u32 m0,s88,9088
ds_read_b64_tr_b16 a[36:37],v27 offset:32768
buffer_load_dwordx4 v25,s[40:43],s61 offen offset:128  lds
ds_read_b64_tr_b16 a[38:39],v27 offset:33280
ds_read_b64_tr_b16 a[40:41],v26 offset:36864
ds_read_b64_tr_b16 a[42:43],v26 offset:37376
ds_read_b64_tr_b16 a[44:45],v27 offset:36864
ds_read_b64_tr_b16 a[46:47],v27 offset:37376
s_waitcnt lgkmcnt(8)
v_mfma_f32_16x16x32_bf16 v[16:19],a[16:19],v[72:75],v[16:19]
v_mfma_f32_16x16x32_bf16 v[20:23],a[20:23],v[72:75],v[20:23]
v_mfma_f32_16x16x32_bf16 v[16:19],a[24:27],v[72:75],v[16:19]
v_mfma_f32_16x16x32_bf16 v[20:23],a[28:31],v[72:75],v[20:23]
s_waitcnt vmcnt(24)
s_barrier
s_add_u32 m0,s88,16384
s_add_u32 s61,s82,4587520
buffer_load_dwordx4 v24,s[40:43],s61 offen offset:0  lds
s_add_u32 m0,s88,17408
ds_read_b64_tr_b16 a[48:49],v32 offset:0
buffer_load_dwordx4 v25,s[40:43],s61 offen offset:0  lds
s_add_u32 m0,s88,24448)"
R"(
ds_read_b64_tr_b16 a[50:51],v32 offset:512
buffer_load_dwordx4 v24,s[40:43],s61 offen offset:128  lds
s_add_u32 m0,s88,25472
ds_read_b64_tr_b16 a[52:53],v33 offset:0
buffer_load_dwordx4 v25,s[40:43],s61 offen offset:128  lds
ds_read_b64_tr_b16 a[54:55],v33 offset:512
ds_read_b64_tr_b16 a[56:57],v32 offset:4096
ds_read_b64_tr_b16 a[58:59],v32 offset:4608
ds_read_b64_tr_b16 a[60:61],v33 offset:4096
ds_read_b64_tr_b16 a[62:63],v33 offset:4608
s_waitcnt lgkmcnt(8)
v_mfma_f32_16x16x32_bf16 v[16:19],a[32:35],v[72:75],v[16:19]
v_mfma_f32_16x16x32_bf16 v[20:23],a[36:39],v[72:75],v[20:23]
v_mfma_f32_16x16x32_bf16 v[16:19],a[40:43],v[72:75],v[16:19]
v_mfma_f32_16x16x32_bf16 v[20:23],a[44:47],v[72:75],v[20:23]
s_waitcnt vmcnt(24)
s_barrier
s_add_u32 m0,s88,32768
s_add_u32 s61,s82,5046272
buffer_load_dwordx4 v24,s[40:43],s61 offen offset:0  lds
s_add_u32 m0,s88,33792
ds_read_b64_tr_b16 a[64:65],v32 offset:16384
buffer_load_dwordx4 v25,s[40:43],s61 offen offset:0  lds
s_add_u32 m0,s88,40832
ds_read_b64_tr_b16 a[66:67],v32 offset:16896
buffer_load_dwordx4 v24,s[40:43],s61 offen offset:128  lds
s_add_u32 m0,s88,41856
ds_read_b64_tr_b16 a[68:69],v33 offset:16384
buffer_load_dwordx4 v25,s[40:43],s61 offen offset:128  lds
ds_read_b64_tr_b16 a[70:71],v33 offset:16896
ds_read_b64_tr_b16 a[72:73],v32 offset:20480
ds_read_b64_tr_b16 a[74:75],v32 offset:20992
ds_read_b64_tr_b16 a[76:77],v33 offset:20480
ds_read_b64_tr_b16 a[78:79],v33 offset:20992
s_waitcnt lgkmcnt(8)
v_mfma_f32_16x16x32_bf16 v[16:19],a[48:51],v[72:75],v[16:19]
v_mfma_f32_16x16x32_bf16 v[20:23],a[52:55],v[72:75],v[20:23]
v_mfma_f32_16x16x32_bf16 v[16:19],a[56:59],v[72:75],v[16:19]
v_mfma_f32_16x16x32_bf16 v[20:23],a[60:63],v[72:75],v[20:23]
s_waitcnt vmcnt(24)
s_barrier
s_add_u32 m0,s88,49152
s_add_u32 s61,s82,5505024
buffer_load_dwordx4 v24,s[40:43],s61 offen offset:0  lds
s_add_u32 m0,s88,50176
ds_read_b64_tr_b16 a[80:81],v32 offset:32768
buffer_load_dwordx4 v25,s[40:43],s61 offen offset:0  lds
s_add_u32 m0,s88,57216
ds_read_b64_tr_b16 a[82:83],v32 offset:33280
buffer_load_dwordx4 v24,s[40:43],s61 offen offset:128  lds
s_add_u32 m0,s88,58240
ds_read_b64_tr_b16 a[84:85],v33 offset:32768
buffer_load_dwordx4 v25,s[40:43],s61 offen offset:128  lds
ds_read_b64_tr_b16 a[86:87],v33 offset:33280
ds_read_b64_tr_b16 a[88:89],v32 offset:36864
ds_read_b64_tr_b16 a[90:91],v32 offset:37376
ds_read_b64_tr_b16 a[92:93],v33 offset:36864
ds_read_b64_tr_b16 a[94:95],v33 offset:37376
s_waitcnt lgkmcnt(8)
v_mfma_f32_16x16x32_bf16 v[16:19],a[64:67],v[72:75],v[16:19]
v_mfma_f32_16x16x32_bf16 v[20:23],a[68:71],v[72:75],v[20:23]
v_mfma_f32_16x16x32_bf16 v[16:19],a[72:75],v[72:75],v[16:19]
v_mfma_f32_16x16x32_bf16 v[20:23],a[76:79],v[72:75],v[20:23]
s_waitcnt vmcnt(24)
s_barrier
s_add_u32 m0,s88,65536
s_add_u32 s61,s82,5963776
buffer_load_dwordx4 v24,s[40:43],s61 offen offset:0  lds
s_add_u32 m0,s88,66560
ds_read_b64_tr_b16 a[96:97],v34 offset:0
buffer_load_dwordx4 v25,s[40:43],s61 offen offset:0  lds
s_add_u32 m0,s88,73600
ds_read_b64_tr_b16 a[98:99],v34 offset:512
buffer_load_dwordx4 v24,s[40:43],s61 offen offset:128  lds
s_add_u32 m0,s88,74624
ds_read_b64_tr_b16 a[100:101],v35 offset:0
buffer_load_dwordx4 v25,s[40:43],s61 offen offset:128  lds
ds_read_b64_tr_b16 a[102:103],v35 offset:512
ds_read_b64_tr_b16 a[104:105],v34 offset:4096
ds_read_b64_tr_b16 a[106:107],v34 offset:4608
ds_read_b64_tr_b16 a[108:109],v35 offset:4096
ds_read_b64_tr_b16 a[110:111],v35 offset:4608
s_waitcnt lgkmcnt(8)
v_mfma_f32_16x16x32_bf16 v[16:19],a[80:83],v[72:75],v[16:19]
v_mfma_f32_16x16x32_bf16 v[20:23],a[84:87],v[72:75],v[20:23]
v_mfma_f32_16x16x32_bf16 v[16:19],a[88:91],v[72:75],v[16:19]
v_mfma_f32_16x16x32_bf16 v[20:23],a[92:95],v[72:75],v[20:23]
s_waitcnt vmcnt(24)
s_barrier
s_add_u32 m0,s88,81920
s_add_u32 s61,s82,6422528
buffer_load_dwordx4 v24,s[40:43],s61 offen offset:0  lds
s_add_u32 m0,s88,82944
ds_read_b64_tr_b16 a[112:113],v34 offset:16384
buffer_load_dwordx4 v25,s[40:43],s61 offen offset:0  lds
s_add_u32 m0,s88,89984
ds_read_b64_tr_b16 a[114:115],v34 offset:16896
buffer_load_dwordx4 v24,s[40:43],s61 offen offset:128  lds
s_add_u32 m0,s88,91008
ds_read_b64_tr_b16 a[116:117],v35 offset:16384
buffer_load_dwordx4 v25,s[40:43],s61 offen offset:128  lds
ds_read_b64_tr_b16 a[118:119],v35 offset:16896
ds_read_b64_tr_b16 a[120:121],v34 offset:20480
ds_read_b64_tr_b16 a[122:123],v34 offset:20992
ds_read_b64_tr_b16 a[124:125],v35 offset:20480
ds_read_b64_tr_b16 a[126:127],v35 offset:20992
s_waitcnt lgkmcnt(8)
v_mfma_f32_16x16x32_bf16 v[16:19],a[96:99],v[72:75],v[16:19]
v_mfma_f32_16x16x32_bf16 v[20:23],a[100:103],v[72:75],v[20:23]
v_mfma_f32_16x16x32_bf16 v[16:19],a[104:107],v[72:75],v[16:19]
v_mfma_f32_16x16x32_bf16 v[20:23],a[108:111],v[72:75],v[20:23]
s_waitcnt vmcnt(24)
s_barrier
s_add_u32 m0,s88,98304
s_add_u32 s61,s82,6881280
buffer_load_dwordx4 v24,s[40:43],s61 offen offset:0  lds
s_add_u32 m0,s88,99328
ds_read_b64_tr_b16 a[128:129],v34 offset:32768
buffer_load_dwordx4 v25,s[40:43],s61 offen offset:0  lds
s_add_u32 m0,s88,106368
ds_read_b64_tr_b16 a[130:131],v34 offset:33280
buffer_load_dwordx4 v24,s[40:43],s61 offen offset:128  lds
s_add_u32 m0,s88,107392
ds_read_b64_tr_b16 a[132:133],v35 offset:32768
buffer_load_dwordx4 v25,s[40:43],s61 offen offset:128  lds
ds_read_b64_tr_b16 a[134:135],v35 offset:33280
ds_read_b64_tr_b16 a[136:137],v34 offset:36864
ds_read_b64_tr_b16 a[138:139],v34 offset:37376
ds_read_b64_tr_b16 a[140:141],v35 offset:36864
ds_read_b64_tr_b16 a[142:143],v35 offset:37376
s_waitcnt lgkmcnt(8)
v_mfma_f32_16x16x32_bf16 v[16:19],a[112:115],v[72:75],v[16:19]
v_mfma_f32_16x16x32_bf16 v[20:23],a[116:119],v[72:75],v[20:23]
v_mfma_f32_16x16x32_bf16 v[16:19],a[120:123],v[72:75],v[16:19]
v_mfma_f32_16x16x32_bf16 v[20:23],a[124:127],v[72:75],v[20:23]
s_waitcnt vmcnt(24)
s_barrier
ds_read_b64_tr_b16 a[144:145],v26 offset:0
ds_read_b64_tr_b16 a[146:147],v26 offset:512
ds_read_b64_tr_b16 a[148:149],v27 offset:0
ds_read_b64_tr_b16 a[150:151],v27 offset:512
ds_read_b64_tr_b16 a[152:153],v26 offset:4096
ds_read_b64_tr_b16 a[154:155],v26 offset:4608
ds_read_b64_tr_b16 a[156:157],v27 offset:4096
ds_read_b64_tr_b16 a[158:159],v27 offset:4608
s_waitcnt lgkmcnt(8)
v_mfma_f32_16x16x32_bf16 v[16:19],a[128:131],v[72:75],v[16:19]
v_mfma_f32_16x16x32_bf16 v[20:23],a[132:135],v[72:75],v[20:23]
v_mfma_f32_16x16x32_bf16 v[16:19],a[136:139],v[72:75],v[16:19]
v_mfma_f32_16x16x32_bf16 v[20:23],a[140:143],v[72:75],v[20:23]
s_waitcnt vmcnt(20)
s_barrier
ds_read_b64_tr_b16 a[160:161],v26 offset:16384
ds_read_b64_tr_b16 a[162:163],v26 offset:16896
ds_read_b64_tr_b16 a[164:165],v27 offset:16384
ds_read_b64_tr_b16 a[166:167],v27 offset:16896
ds_read_b64_tr_b16 a[168:169],v26 offset:20480
ds_read_b64_tr_b16 a[170:171],v26 offset:20992
ds_read_b64_tr_b16 a[172:173],v27 offset:20480
ds_read_b64_tr_b16 a[174:175],v27 offset:20992
s_waitcnt lgkmcnt(8)
v_mfma_f32_16x16x32_bf16 v[16:19],a[144:147],v[72:75],v[16:19]
v_mfma_f32_16x16x32_bf16 v[20:23],a[148:151],v[72:75],v[20:23]
v_mfma_f32_16x16x32_bf16 v[16:19],a[152:155],v[72:75],v[16:19]
v_mfma_f32_16x16x32_bf16 v[20:23],a[156:159],v[72:75],v[20:23]
s_waitcnt vmcnt(16)
s_barrier
ds_read_b64_tr_b16 a[176:177],v26 offset:32768
ds_read_b64_tr_b16 a[178:179],v26 offset:33280
ds_read_b64_tr_b16 a[180:181],v27 offset:32768
ds_read_b64_tr_b16 a[182:183],v27 offset:33280
ds_read_b64_tr_b16 a[184:185],v26 offset:36864
ds_read_b64_tr_b16 a[186:187],v26 offset:37376
ds_read_b64_tr_b16 a[188:189],v27 offset:36864
ds_read_b64_tr_b16 a[190:191],v27 offset:37376
s_getpc_b64 s[46:47]
Lg3o_cp_%=:
s_add_u32 s46,s46,Lg3o_init_%=-Lg3o_cp_%=
s_addc_u32 s47,s47,0
s_lshl_b32 s68,s54,13
v_lshl_add_u32 v56,v2,3,s68
v_min_u32 v57,Lg3o_done_%=-Lg3o_init_%=,v56
global_load_dword v60,v57,s[46:47]
s_waitcnt lgkmcnt(8)
v_mfma_f32_16x16x32_bf16 v[16:19],a[160:163],v[72:75],v[16:19]
v_mfma_f32_16x16x32_bf16 v[20:23],a[164:167],v[72:75],v[20:23]
v_mfma_f32_16x16x32_bf16 v[16:19],a[168:171],v[72:75],v[16:19]
v_mfma_f32_16x16x32_bf16 v[20:23],a[172:175],v[72:75],v[20:23]
s_waitcnt vmcnt(13)
s_barrier
ds_read_b64_tr_b16 a[192:193],v32 offset:0
ds_read_b64_tr_b16 a[194:195],v32 offset:512
ds_read_b64_tr_b16 a[196:197],v33 offset:0
ds_read_b64_tr_b16 a[198:199],v33 offset:512
ds_read_b64_tr_b16 a[200:201],v32 offset:4096
ds_read_b64_tr_b16 a[202:203],v32 offset:4608
ds_read_b64_tr_b16 a[204:205],v33 offset:4096
ds_read_b64_tr_b16 a[206:207],v33 offset:4608
s_waitcnt lgkmcnt(8)
v_mfma_f32_16x16x32_bf16 v[16:19],a[176:179],v[72:75],v[16:19])"
R"(
v_mfma_f32_16x16x32_bf16 v[20:23],a[180:183],v[72:75],v[20:23]
v_mfma_f32_16x16x32_bf16 v[16:19],a[184:187],v[72:75],v[16:19]
v_mfma_f32_16x16x32_bf16 v[20:23],a[188:191],v[72:75],v[20:23]
s_waitcnt vmcnt(9)
s_barrier
ds_read_b64_tr_b16 a[208:209],v32 offset:16384
ds_read_b64_tr_b16 a[210:211],v32 offset:16896
ds_read_b64_tr_b16 a[212:213],v33 offset:16384
ds_read_b64_tr_b16 a[214:215],v33 offset:16896
ds_read_b64_tr_b16 a[216:217],v32 offset:20480
ds_read_b64_tr_b16 a[218:219],v32 offset:20992
ds_read_b64_tr_b16 a[220:221],v33 offset:20480
ds_read_b64_tr_b16 a[222:223],v33 offset:20992
s_waitcnt lgkmcnt(8)
v_mfma_f32_16x16x32_bf16 v[16:19],a[192:195],v[72:75],v[16:19]
v_mfma_f32_16x16x32_bf16 v[20:23],a[196:199],v[72:75],v[20:23]
v_mfma_f32_16x16x32_bf16 v[16:19],a[200:203],v[72:75],v[16:19]
v_mfma_f32_16x16x32_bf16 v[20:23],a[204:207],v[72:75],v[20:23]
s_waitcnt vmcnt(5)
s_barrier
ds_read_b64_tr_b16 a[224:225],v32 offset:32768
ds_read_b64_tr_b16 a[226:227],v32 offset:33280
ds_read_b64_tr_b16 a[228:229],v33 offset:32768
ds_read_b64_tr_b16 a[230:231],v33 offset:33280
ds_read_b64_tr_b16 a[232:233],v32 offset:36864
ds_read_b64_tr_b16 a[234:235],v32 offset:37376
ds_read_b64_tr_b16 a[236:237],v33 offset:36864
ds_read_b64_tr_b16 a[238:239],v33 offset:37376
s_waitcnt lgkmcnt(8)
v_mfma_f32_16x16x32_bf16 v[16:19],a[208:211],v[72:75],v[16:19]
v_mfma_f32_16x16x32_bf16 v[20:23],a[212:215],v[72:75],v[20:23]
v_mfma_f32_16x16x32_bf16 v[16:19],a[216:219],v[72:75],v[16:19]
v_mfma_f32_16x16x32_bf16 v[20:23],a[220:223],v[72:75],v[20:23]
s_waitcnt vmcnt(1)
s_barrier
ds_read_b64_tr_b16 a[240:241],v34 offset:0
ds_read_b64_tr_b16 a[242:243],v34 offset:512
ds_read_b64_tr_b16 a[244:245],v35 offset:0
ds_read_b64_tr_b16 a[246:247],v35 offset:512
ds_read_b64_tr_b16 a[248:249],v34 offset:4096
ds_read_b64_tr_b16 a[250:251],v34 offset:4608
ds_read_b64_tr_b16 a[252:253],v35 offset:4096
ds_read_b64_tr_b16 a[254:255],v35 offset:4608
s_waitcnt lgkmcnt(8)
v_mfma_f32_16x16x32_bf16 v[16:19],a[224:227],v[72:75],v[16:19]
v_mfma_f32_16x16x32_bf16 v[20:23],a[228:231],v[72:75],v[20:23]
v_mfma_f32_16x16x32_bf16 v[16:19],a[232:235],v[72:75],v[16:19]
v_mfma_f32_16x16x32_bf16 v[20:23],a[236:239],v[72:75],v[20:23]
s_waitcnt lgkmcnt(0)
v_mfma_f32_16x16x32_bf16 v[16:19],a[240:243],v[72:75],v[16:19]
v_mfma_f32_16x16x32_bf16 v[20:23],a[244:247],v[72:75],v[20:23]
v_mfma_f32_16x16x32_bf16 v[16:19],a[248:251],v[72:75],v[16:19]
v_mfma_f32_16x16x32_bf16 v[20:23],a[252:255],v[72:75],v[20:23]
s_waitcnt lgkmcnt(0)
s_barrier
v_readfirstlane_b32 s60,v5
s_nop 3
s_add_u32 s60,s60,1
s_cmp_eq_u32 s60,0
s_cselect_b32 s60,1,s60
Lg3o_init_%=:
v_mov_b32 v5,s60
s_mov_b32 s59,536870912
s_add_u32 s57,s56,131072
s_mov_b32 s79,0
s_mov_b32 s75,0x3a800000
s_mov_b32 s76,0xc0135761
s_mov_b32 s77,0x3d372713
s_mov_b32 s44,s59
s_mov_b32 s45,s59
s_mov_b32 s46,s59
s_mov_b32 s47,s59
s_mov_b32 s48,s83
s_add_u32 s49,s48,s55
s_cmp_eq_u32 s49,s56
s_cselect_b32 s49,s57,s49
s_cmp_eq_u32 s49,s58
s_cselect_b32 s49,s59,s49
s_add_u32 s50,s49,s55
s_cmp_eq_u32 s50,s56
s_cselect_b32 s50,s57,s50
s_cmp_eq_u32 s50,s58
s_cselect_b32 s50,s59,s50
s_add_u32 s51,s50,s55
s_cmp_eq_u32 s51,s56
s_cselect_b32 s51,s57,s51
s_cmp_eq_u32 s51,s58
s_cselect_b32 s51,s59,s51
s_add_u32 s52,s51,s55
s_cmp_eq_u32 s52,s56
s_cselect_b32 s52,s57,s52
s_cmp_eq_u32 s52,s58
s_cselect_b32 s52,s59,s52
s_lshl_b32 s63,s54,12
s_add_u32 s63,s63,65536
s_add_u32 s64,s63,32768
s_lshl_b32 s88,s54,10
s_add_u32 s88,s88,131072
s_lshl_b32 s78,s54,11
s_add_u32 s78,s78,132608
v_add_u32 v1,16384,v0
v_mov_b32 v12,0x3727c5ac
s_getpc_b64 s[90:91]
Lg3o_pc_%=:
s_mov_b32 s68,Lg3o_d1w0_%=-Lg3o_pc_%=
s_cmp_eq_u32 s54,1
s_cselect_b32 s68,Lg3o_d1w1_%=-Lg3o_pc_%=,s68
s_cmp_eq_u32 s54,2
s_cselect_b32 s68,Lg3o_d1w2_%=-Lg3o_pc_%=,s68
s_cmp_eq_u32 s54,3
s_cselect_b32 s68,Lg3o_d1w3_%=-Lg3o_pc_%=,s68
s_add_u32 s92,s90,s68
s_addc_u32 s93,s91,0
s_mov_b32 s68,Lg3o_d0w0_%=-Lg3o_pc_%=
s_cmp_eq_u32 s54,1
s_cselect_b32 s68,Lg3o_d0w1_%=-Lg3o_pc_%=,s68
s_cmp_eq_u32 s54,2
s_cselect_b32 s68,Lg3o_d0w2_%=-Lg3o_pc_%=,s68
s_cmp_eq_u32 s54,3
s_cselect_b32 s68,Lg3o_d0w3_%=-Lg3o_pc_%=,s68
s_add_u32 s90,s90,s68
s_addc_u32 s91,s91,0
s_mov_b32 s66,s59
s_mov_b32 s65,0
s_mov_b32 s62,s63
s_branch Lg3o_slow0_%=
Lg3o_d0w0_%=:
v_mfma_f32_16x16x32_bf16 v[24:27],a[0:3],v[40:43],0
s_add_u32 m0,s62,16384
ds_read_b128 v[68:71],v2 offset:7168
v_mul_f32 v96,s75,v92
v_mfma_f32_16x16x32_bf16 v[28:31],a[4:7],v[40:43],0
buffer_load_dwordx4 v1,s[36:39],s66 offen offset:0 sc1 lds
v_mfma_f32_16x16x32_bf16 v[24:27],a[8:11],v[44:47],v[24:27]
s_mul_i32 s73,s47,7
ds_read_b128 v[40:43],v2 offset:8192
v_fma_f32 v93,v93,s75,v12
v_mfma_f32_16x16x32_bf16 v[28:31],a[12:15],v[44:47],v[28:31]
s_lshr_b32 s73,s73,1
v_fma_f32 v93,-v96,v96,v93
v_max_f32 v93,v93,v12
v_mfma_f32_16x16x32_bf16 v[24:27],a[16:19],v[48:51],v[24:27]
s_lshr_b32 s72,s66,5
ds_read_b128 v[44:47],v2 offset:9216
v_fma_f32 v32,-v96,v16,v32
v_mfma_f32_16x16x32_bf16 v[28:31],a[20:23],v[48:51],v[28:31]
buffer_load_dwordx4 v1,s[36:39],s66 offen offset:1024 sc1 lds
v_mfma_f32_16x16x32_bf16 v[24:27],a[24:27],v[52:55],v[24:27]
s_lshr_b32 s71,s62,2
ds_read_b128 v[48:51],v2 offset:10240
v_fma_f32 v33,-v96,v17,v33
v_mfma_f32_16x16x32_bf16 v[28:31],a[28:31],v[52:55],v[28:31]
s_waitcnt lgkmcnt(3)
v_rsq_f32 v92,v93
v_mfma_f32_16x16x32_bf16 v[24:27],a[32:35],v[56:59],v[24:27]
ds_read_b128 v[52:55],v2 offset:11264
v_mul_f32 v97,s76,v92
v_mfma_f32_16x16x32_bf16 v[28:31],a[36:39],v[56:59],v[28:31]
buffer_load_dwordx4 v1,s[36:39],s66 offen offset:2048 sc1 lds
v_mfma_f32_16x16x32_bf16 v[24:27],a[40:43],v[60:63],v[24:27]
ds_read_b128 v[56:59],v2 offset:12288
v_mul_f32 v98,v92,v92
v_mfma_f32_16x16x32_bf16 v[28:31],a[44:47],v[60:63],v[28:31]
v_mul_f32 v99,v93,v92
v_mul_f32 v98,s77,v98
v_mfma_f32_16x16x32_bf16 v[24:27],a[48:51],v[64:67],v[24:27]
ds_read_b128 v[60:63],v2 offset:13312
v_mul_f32 v98,v98,v97
v_mfma_f32_16x16x32_bf16 v[28:31],a[52:55],v[64:67],v[28:31]
buffer_load_dwordx4 v1,s[36:39],s66 offen offset:3072 sc1 lds
v_mfma_f32_16x16x32_bf16 v[24:27],a[56:59],v[68:71],v[24:27]
s_add_u32 m0,s71,s88
ds_read_b128 v[64:67],v2 offset:14336
v_fma_f32 v34,-v96,v18,v34
v_mfma_f32_16x16x32_bf16 v[28:31],a[60:63],v[68:71],v[28:31]
s_waitcnt lgkmcnt(3)
buffer_load_dword v9,s[36:39],s72 offen offset:0 sc1 lds
v_mfma_f32_16x16x32_bf16 v[24:27],a[64:67],v[40:43],v[24:27]
s_waitcnt vmcnt(11)
ds_read_b128 v[68:71],v2 offset:15360
v_fma_f32 v35,-v96,v19,v35
v_mfma_f32_16x16x32_bf16 v[28:31],a[68:71],v[40:43],v[28:31]
s_barrier
v_mfma_f32_16x16x32_bf16 v[24:27],a[72:75],v[44:47],v[24:27]
s_xor_b32 s87,s64,65536
ds_read_b128 v[40:43],v2 offset:16384
ds_read_b32 v88,v8 offset:0
v_mfma_f32_16x16x32_bf16 v[28:31],a[76:79],v[44:47],v[28:31]
s_cmp_ge_u32 s66,s59
v_mul_f32 v72,v32,v32
v_mul_f32 v73,v33,v33
v_mfma_f32_16x16x32_bf16 v[24:27],a[80:83],v[48:51],v[24:27]
s_cselect_b32 s86,s49,s50
ds_read_b128 v[44:47],v2 offset:17408
v_mul_f32 v74,v34,v34
v_mfma_f32_16x16x32_bf16 v[28:31],a[84:87],v[48:51],v[28:31]
s_cselect_b32 s87,s87,s63
v_mul_f32 v75,v35,v35
v_fma_f32 v72,v98,v72,v97
v_mfma_f32_16x16x32_bf16 v[24:27],a[88:91],v[52:55],v[24:27])"
R"(
s_lshr_b32 s68,s50,5
ds_read_b128 v[48:51],v2 offset:18432
v_mul_f32 v72,v32,v72
v_mfma_f32_16x16x32_bf16 v[28:31],a[92:95],v[52:55],v[28:31]
s_waitcnt lgkmcnt(4)
v_exp_f32 v72,v72
v_mfma_f32_16x16x32_bf16 v[24:27],a[96:99],v[56:59],v[24:27]
s_waitcnt vmcnt(10) lgkmcnt(2)
ds_read_b128 v[52:55],v2 offset:19456
v_fma_f32 v72,v72,v99,v99
v_mfma_f32_16x16x32_bf16 v[28:31],a[100:103],v[56:59],v[28:31]
s_barrier
v_mfma_f32_16x16x32_bf16 v[24:27],a[104:107],v[60:63],v[24:27]
s_lshr_b32 s70,s51,5
ds_read_b128 v[56:59],v2 offset:20480
ds_read_b32 v13,v7 offset:0
v_mfma_f32_16x16x32_bf16 v[28:31],a[108:111],v[60:63],v[28:31]
s_mov_b32 s62,s87
v_rcp_f32 v72,v72
v_mfma_f32_16x16x32_bf16 v[24:27],a[112:115],v[64:67],v[24:27]
ds_read_b128 v[60:63],v2 offset:21504
v_add_f32_dpp v88,v88,v88 quad_perm:[1,0,3,2] row_mask:0xf bank_mask:0xf
v_mfma_f32_16x16x32_bf16 v[28:31],a[116:119],v[64:67],v[28:31]
s_nop 0
v_add_f32_dpp v88,v88,v88 quad_perm:[2,3,0,1] row_mask:0xf bank_mask:0xf
v_fma_f32 v73,v98,v73,v97
v_mfma_f32_16x16x32_bf16 v[24:27],a[120:123],v[68:71],v[24:27]
s_waitcnt lgkmcnt(1)
ds_read_b128 v[64:67],v2 offset:22528
v_mul_f32 v73,v33,v73
v_mfma_f32_16x16x32_bf16 v[28:31],a[124:127],v[68:71],v[28:31]
v_cmp_eq_u32 vcc,s60,v13
v_exp_f32 v73,v73
v_add_f32_dpp v88,v88,v88 row_half_mirror row_mask:0xf bank_mask:0xf
v_mfma_f32_16x16x32_bf16 v[24:27],a[128:131],v[40:43],v[24:27]
s_cmp_eq_u32 vcc_lo,-1
ds_write_b32 v10,v88 offset:0
v_mfma_f32_16x16x32_bf16 v[28:31],a[132:135],v[40:43],v[28:31]
s_cselect_b32 s67,s86,s59
s_cmp_ge_u32 s67,s59
s_cselect_b32 s68,s68,s70
ds_read_b128 v[68:71],v2 offset:23552
s_add_u32 m0,s78,256
v_mfma_f32_16x16x32_bf16 v[24:27],a[136:139],v[44:47],v[24:27]
buffer_load_dword v6,s[36:39],s68 offen offset:0 sc1 lds
v_mfma_f32_16x16x32_bf16 v[28:31],a[140:143],v[44:47],v[28:31]
s_mov_b32 m0,s62
ds_read_b128 v[40:43],v2 offset:24576
v_fma_f32 v73,v73,v99,v99
v_mfma_f32_16x16x32_bf16 v[24:27],a[144:147],v[48:51],v[24:27]
buffer_load_dwordx4 v0,s[36:39],s67 offen offset:0 sc1 lds
v_mfma_f32_16x16x32_bf16 v[28:31],a[148:151],v[48:51],v[28:31]
s_xor_b32 s63,s63,65536
ds_read_b128 v[44:47],v2 offset:25600
v_fma_f32 v74,v98,v74,v97
v_mfma_f32_16x16x32_bf16 v[24:27],a[152:155],v[52:55],v[24:27]
s_add_u32 s53,s52,s55
ds_read_b128 v[48:51],v2 offset:26624
v_mul_f32 v74,v34,v74
v_mfma_f32_16x16x32_bf16 v[28:31],a[156:159],v[52:55],v[28:31]
s_waitcnt lgkmcnt(3)
v_exp_f32 v74,v74
v_mfma_f32_16x16x32_bf16 v[24:27],a[160:163],v[56:59],v[24:27]
buffer_load_dwordx4 v0,s[36:39],s67 offen offset:1024 sc1 lds
v_mfma_f32_16x16x32_bf16 v[28:31],a[164:167],v[56:59],v[28:31]
s_cmp_eq_u32 s53,s56
ds_read_b128 v[52:55],v2 offset:27648
v_fma_f32 v74,v74,v99,v99
v_mfma_f32_16x16x32_bf16 v[24:27],a[168:171],v[60:63],v[24:27]
s_cselect_b32 s53,s57,s53
ds_read_b128 v[56:59],v2 offset:28672
v_fma_f32 v75,v98,v75,v97
v_mfma_f32_16x16x32_bf16 v[28:31],a[172:175],v[60:63],v[28:31]
s_cmp_eq_u32 s53,s58
v_rcp_f32 v73,v73
v_mfma_f32_16x16x32_bf16 v[24:27],a[176:179],v[64:67],v[24:27]
buffer_load_dwordx4 v0,s[36:39],s67 offen offset:2048 sc1 lds
v_mfma_f32_16x16x32_bf16 v[28:31],a[180:183],v[64:67],v[28:31]
s_cselect_b32 s53,s59,s53
ds_read_b128 v[60:63],v2 offset:29696
v_mul_f32 v75,v35,v75
v_mfma_f32_16x16x32_bf16 v[24:27],a[184:187],v[68:71],v[24:27]
s_cmp_ge_u32 s66,s59
ds_read_b128 v[64:67],v2 offset:30720
v_mul_f32 v32,v32,v72
v_mfma_f32_16x16x32_bf16 v[28:31],a[188:191],v[68:71],v[28:31]
s_waitcnt lgkmcnt(3)
v_exp_f32 v75,v75
v_mfma_f32_16x16x32_bf16 v[24:27],a[192:195],v[40:43],v[24:27]
s_waitcnt vmcnt(9)
ds_read_b128 v[68:71],v2 offset:31744
v_fma_f32 v75,v75,v99,v99
v_mfma_f32_16x16x32_bf16 v[28:31],a[196:199],v[40:43],v[28:31]
s_barrier
v_fma_f32 v36,-v96,v20,v36
v_fma_f32 v37,-v96,v21,v37
v_fma_f32 v38,-v96,v22,v38
v_fma_f32 v39,-v96,v23,v39
v_mul_f32 v76,v36,v36
v_mul_f32 v77,v37,v37
v_mul_f32 v78,v38,v38
v_mul_f32 v79,v39,v39
v_fma_f32 v76,v98,v76,v97
v_fma_f32 v77,v98,v77,v97
v_fma_f32 v78,v98,v78,v97
v_fma_f32 v79,v98,v79,v97
v_mul_f32 v76,v36,v76
v_mul_f32 v77,v37,v77
v_mul_f32 v78,v38,v78
v_mul_f32 v79,v39,v79
v_exp_f32 v76,v76
v_exp_f32 v77,v77
v_exp_f32 v78,v78
v_exp_f32 v79,v79
v_mfma_f32_16x16x32_bf16 v[24:27],a[200:203],v[44:47],v[24:27]
buffer_load_dwordx4 v0,s[36:39],s67 offen offset:3072 sc1 lds
v_rcp_f32 v74,v74
v_rcp_f32 v75,v75
v_fma_f32 v76,v76,v99,v99
v_fma_f32 v77,v77,v99,v99
v_fma_f32 v78,v78,v99,v99
v_fma_f32 v79,v79,v99,v99
v_rcp_f32 v76,v76
v_rcp_f32 v77,v77
v_rcp_f32 v78,v78
v_rcp_f32 v79,v79
v_mfma_f32_16x16x32_bf16 v[28:31],a[204:207],v[44:47],v[28:31]
ds_read_b128 v[40:43],v2 offset:32768
v_mul_f32 v33,v33,v73
v_mul_f32 v34,v34,v74
v_mul_f32 v35,v35,v75
v_cvt_pk_bf16_f32 v80,v32,v33
v_cvt_pk_bf16_f32 v81,v34,v35
v_mul_f32 v36,v36,v76
v_mul_f32 v37,v37,v77
v_mul_f32 v38,v38,v78
v_mul_f32 v39,v39,v79
v_cvt_pk_bf16_f32 v82,v36,v37
v_cvt_pk_bf16_f32 v83,v38,v39
v_mfma_f32_16x16x32_bf16 v[24:27],a[208:211],v[48:51],v[24:27]
buffer_store_dwordx4 v[80:83],v3,s[36:39],s73 offen offset:0 sc0 sc1
ds_read_b128 v[44:47],v2 offset:33792
ds_read_b64 v[92:93],v11 offset:0
v_mfma_f32_16x16x32_bf16 v[28:31],a[212:215],v[48:51],v[28:31]
v_mfma_f32_16x16x32_bf16 v[24:27],a[216:219],v[52:55],v[24:27]
ds_read_b128 v[48:51],v2 offset:34816
v_mfma_f32_16x16x32_bf16 v[28:31],a[220:223],v[52:55],v[28:31]
s_waitcnt lgkmcnt(4)
v_mfma_f32_16x16x32_bf16 v[24:27],a[224:227],v[56:59],v[24:27]
s_waitcnt lgkmcnt(1)
ds_read_b128 v[52:55],v2 offset:35840
v_mfma_f32_16x16x32_bf16 v[28:31],a[228:231],v[56:59],v[28:31]
v_mfma_f32_16x16x32_bf16 v[24:27],a[232:235],v[60:63],v[24:27]
ds_read_b128 v[56:59],v2 offset:36864
v_mfma_f32_16x16x32_bf16 v[28:31],a[236:239],v[60:63],v[28:31]
v_mfma_f32_16x16x32_bf16 v[24:27],a[240:243],v[64:67],v[24:27]
ds_read_b128 v[60:63],v2 offset:37888
v_mfma_f32_16x16x32_bf16 v[28:31],a[244:247],v[64:67],v[28:31]
v_mfma_f32_16x16x32_bf16 v[24:27],a[248:251],v[68:71],v[24:27]
s_waitcnt lgkmcnt(2)
ds_read_b128 v[64:67],v2 offset:38912
v_mfma_f32_16x16x32_bf16 v[28:31],a[252:255],v[68:71],v[28:31]
s_cbranch_scc1 Lg3o_slow1_%=
Lg3o_d1w0_%=:
v_mfma_f32_16x16x32_bf16 v[32:35],a[0:3],v[40:43],0
s_add_u32 m0,s62,16384
ds_read_b128 v[68:71],v2 offset:39936
v_mul_f32 v96,s75,v92
v_mfma_f32_16x16x32_bf16 v[36:39],a[4:7],v[40:43],0
buffer_load_dwordx4 v1,s[36:39],s67 offen offset:0 sc1 lds
v_mfma_f32_16x16x32_bf16 v[32:35],a[8:11],v[44:47],v[32:35]
s_mul_i32 s73,s48,7
ds_read_b128 v[40:43],v2 offset:40960
v_fma_f32 v93,v93,s75,v12
v_mfma_f32_16x16x32_bf16 v[36:39],a[12:15],v[44:47],v[36:39]
s_lshr_b32 s73,s73,1
v_fma_f32 v93,-v96,v96,v93
v_max_f32 v93,v93,v12
v_mfma_f32_16x16x32_bf16 v[32:35],a[16:19],v[48:51],v[32:35]
s_lshr_b32 s72,s67,5
ds_read_b128 v[44:47],v2 offset:41984
v_fma_f32 v24,-v96,v16,v24
v_mfma_f32_16x16x32_bf16 v[36:39],a[20:23],v[48:51],v[36:39]
buffer_load_dwordx4 v1,s[36:39],s67 offen offset:1024 sc1 lds
v_mfma_f32_16x16x32_bf16 v[32:35],a[24:27],v[52:55],v[32:35]
s_lshr_b32 s71,s62,2
ds_read_b128 v[48:51],v2 offset:43008
v_fma_f32 v25,-v96,v17,v25
v_mfma_f32_16x16x32_bf16 v[36:39],a[28:31],v[52:55],v[36:39]
s_waitcnt lgkmcnt(3)
v_rsq_f32 v92,v93
v_mfma_f32_16x16x32_bf16 v[32:35],a[32:35],v[56:59],v[32:35]
s_mov_b64 s[44:45],s[46:47]
ds_read_b128 v[52:55],v2 offset:44032
v_mul_f32 v97,s76,v92
v_mfma_f32_16x16x32_bf16 v[36:39],a[36:39],v[56:59],v[36:39]
buffer_load_dwordx4 v1,s[36:39],s67 offen offset:2048 sc1 lds
v_mfma_f32_16x16x32_bf16 v[32:35],a[40:43],v[60:63],v[32:35]
s_mov_b64 s[46:47],s[48:49]
ds_read_b128 v[56:59],v2 offset:45056)"
R"(
v_mul_f32 v98,v92,v92
v_mfma_f32_16x16x32_bf16 v[36:39],a[44:47],v[60:63],v[36:39]
s_mov_b64 s[48:49],s[50:51]
v_mul_f32 v99,v93,v92
v_mul_f32 v98,s77,v98
v_mfma_f32_16x16x32_bf16 v[32:35],a[48:51],v[64:67],v[32:35]
ds_read_b128 v[60:63],v2 offset:46080
v_mul_f32 v98,v98,v97
v_mfma_f32_16x16x32_bf16 v[36:39],a[52:55],v[64:67],v[36:39]
buffer_load_dwordx4 v1,s[36:39],s67 offen offset:3072 sc1 lds
v_mfma_f32_16x16x32_bf16 v[32:35],a[56:59],v[68:71],v[32:35]
s_add_u32 m0,s71,s88
ds_read_b128 v[64:67],v2 offset:47104
v_fma_f32 v26,-v96,v18,v26
v_mfma_f32_16x16x32_bf16 v[36:39],a[60:63],v[68:71],v[36:39]
s_waitcnt lgkmcnt(3)
buffer_load_dword v9,s[36:39],s72 offen offset:0 sc1 lds
v_mfma_f32_16x16x32_bf16 v[32:35],a[64:67],v[40:43],v[32:35]
s_waitcnt vmcnt(11)
ds_read_b128 v[68:71],v2 offset:48128
v_fma_f32 v27,-v96,v19,v27
v_mfma_f32_16x16x32_bf16 v[36:39],a[68:71],v[40:43],v[36:39]
s_barrier
v_mfma_f32_16x16x32_bf16 v[32:35],a[72:75],v[44:47],v[32:35]
s_xor_b32 s87,s63,65536
ds_read_b128 v[40:43],v2 offset:49152
ds_read_b32 v88,v8 offset:8192
v_mfma_f32_16x16x32_bf16 v[36:39],a[76:79],v[44:47],v[36:39]
s_cmp_ge_u32 s67,s59
v_mul_f32 v72,v24,v24
v_mul_f32 v73,v25,v25
v_mfma_f32_16x16x32_bf16 v[32:35],a[80:83],v[48:51],v[32:35]
s_cselect_b32 s86,s50,s51
ds_read_b128 v[44:47],v2 offset:50176
v_mul_f32 v74,v26,v26
v_mfma_f32_16x16x32_bf16 v[36:39],a[84:87],v[48:51],v[36:39]
s_cselect_b32 s87,s87,s64
v_mul_f32 v75,v27,v27
v_fma_f32 v72,v98,v72,v97
v_mfma_f32_16x16x32_bf16 v[32:35],a[88:91],v[52:55],v[32:35]
s_lshr_b32 s68,s51,5
ds_read_b128 v[48:51],v2 offset:51200
v_mul_f32 v72,v24,v72
v_mfma_f32_16x16x32_bf16 v[36:39],a[92:95],v[52:55],v[36:39]
s_waitcnt lgkmcnt(4)
v_exp_f32 v72,v72
v_mfma_f32_16x16x32_bf16 v[32:35],a[96:99],v[56:59],v[32:35]
s_waitcnt vmcnt(10) lgkmcnt(2)
ds_read_b128 v[52:55],v2 offset:52224
v_fma_f32 v72,v72,v99,v99
v_mfma_f32_16x16x32_bf16 v[36:39],a[100:103],v[56:59],v[36:39]
s_barrier
v_mfma_f32_16x16x32_bf16 v[32:35],a[104:107],v[60:63],v[32:35]
s_lshr_b32 s70,s52,5
ds_read_b128 v[56:59],v2 offset:53248
ds_read_b32 v13,v7 offset:256
v_mfma_f32_16x16x32_bf16 v[36:39],a[108:111],v[60:63],v[36:39]
s_mov_b32 s62,s87
v_rcp_f32 v72,v72
v_mfma_f32_16x16x32_bf16 v[32:35],a[112:115],v[64:67],v[32:35]
s_mov_b64 s[50:51],s[52:53]
ds_read_b128 v[60:63],v2 offset:54272
v_add_f32_dpp v88,v88,v88 quad_perm:[1,0,3,2] row_mask:0xf bank_mask:0xf
v_mfma_f32_16x16x32_bf16 v[36:39],a[116:119],v[64:67],v[36:39]
s_nop 0
v_add_f32_dpp v88,v88,v88 quad_perm:[2,3,0,1] row_mask:0xf bank_mask:0xf
v_fma_f32 v73,v98,v73,v97
v_mfma_f32_16x16x32_bf16 v[32:35],a[120:123],v[68:71],v[32:35]
s_waitcnt lgkmcnt(1)
ds_read_b128 v[64:67],v2 offset:55296
v_mul_f32 v73,v25,v73
v_mfma_f32_16x16x32_bf16 v[36:39],a[124:127],v[68:71],v[36:39]
v_cmp_eq_u32 vcc,s60,v13
v_exp_f32 v73,v73
v_add_f32_dpp v88,v88,v88 row_half_mirror row_mask:0xf bank_mask:0xf
v_mfma_f32_16x16x32_bf16 v[32:35],a[128:131],v[40:43],v[32:35]
s_cmp_eq_u32 vcc_lo,-1
ds_write_b32 v10,v88 offset:128
v_mfma_f32_16x16x32_bf16 v[36:39],a[132:135],v[40:43],v[36:39]
s_cselect_b32 s66,s86,s59
s_cmp_ge_u32 s66,s59
s_cselect_b32 s68,s68,s70
ds_read_b128 v[68:71],v2 offset:56320
s_add_u32 m0,s78,0
v_mfma_f32_16x16x32_bf16 v[32:35],a[136:139],v[44:47],v[32:35]
buffer_load_dword v6,s[36:39],s68 offen offset:0 sc1 lds
v_mfma_f32_16x16x32_bf16 v[36:39],a[140:143],v[44:47],v[36:39]
s_mov_b32 m0,s62
ds_read_b128 v[40:43],v2 offset:57344
v_fma_f32 v73,v73,v99,v99
v_mfma_f32_16x16x32_bf16 v[32:35],a[144:147],v[48:51],v[32:35]
buffer_load_dwordx4 v0,s[36:39],s66 offen offset:0 sc1 lds
v_mfma_f32_16x16x32_bf16 v[36:39],a[148:151],v[48:51],v[36:39]
s_xor_b32 s64,s64,65536
ds_read_b128 v[44:47],v2 offset:58368
v_fma_f32 v74,v98,v74,v97
v_mfma_f32_16x16x32_bf16 v[32:35],a[152:155],v[52:55],v[32:35]
s_add_u32 s74,s53,s55
ds_read_b128 v[48:51],v2 offset:59392
v_mul_f32 v74,v26,v74
v_mfma_f32_16x16x32_bf16 v[36:39],a[156:159],v[52:55],v[36:39]
s_waitcnt lgkmcnt(3)
v_exp_f32 v74,v74
v_mfma_f32_16x16x32_bf16 v[32:35],a[160:163],v[56:59],v[32:35]
buffer_load_dwordx4 v0,s[36:39],s66 offen offset:1024 sc1 lds
v_mfma_f32_16x16x32_bf16 v[36:39],a[164:167],v[56:59],v[36:39]
s_cmp_eq_u32 s74,s56
ds_read_b128 v[52:55],v2 offset:60416
v_fma_f32 v74,v74,v99,v99
v_mfma_f32_16x16x32_bf16 v[32:35],a[168:171],v[60:63],v[32:35]
s_cselect_b32 s74,s57,s74
ds_read_b128 v[56:59],v2 offset:61440
v_fma_f32 v75,v98,v75,v97
v_mfma_f32_16x16x32_bf16 v[36:39],a[172:175],v[60:63],v[36:39]
s_cmp_eq_u32 s74,s58
v_rcp_f32 v73,v73
v_mfma_f32_16x16x32_bf16 v[32:35],a[176:179],v[64:67],v[32:35]
buffer_load_dwordx4 v0,s[36:39],s66 offen offset:2048 sc1 lds
v_mfma_f32_16x16x32_bf16 v[36:39],a[180:183],v[64:67],v[36:39]
s_cselect_b32 s74,s59,s74
ds_read_b128 v[60:63],v2 offset:62464
v_mul_f32 v75,v27,v75
v_mfma_f32_16x16x32_bf16 v[32:35],a[184:187],v[68:71],v[32:35]
s_mov_b32 s52,s74
ds_read_b128 v[64:67],v2 offset:63488
v_mul_f32 v24,v24,v72
v_mfma_f32_16x16x32_bf16 v[36:39],a[188:191],v[68:71],v[36:39]
s_waitcnt lgkmcnt(3)
v_exp_f32 v75,v75
v_mfma_f32_16x16x32_bf16 v[32:35],a[192:195],v[40:43],v[32:35]
s_waitcnt vmcnt(9)
ds_read_b128 v[68:71],v2 offset:64512
v_xor_b32 v2,65536,v2
v_mfma_f32_16x16x32_bf16 v[36:39],a[196:199],v[40:43],v[36:39]
s_barrier
v_fma_f32 v28,-v96,v20,v28
v_fma_f32 v29,-v96,v21,v29
v_fma_f32 v30,-v96,v22,v30
v_fma_f32 v31,-v96,v23,v31
v_mul_f32 v76,v28,v28
v_mul_f32 v77,v29,v29
v_mul_f32 v78,v30,v30
v_mul_f32 v79,v31,v31
v_fma_f32 v76,v98,v76,v97
v_fma_f32 v77,v98,v77,v97
v_fma_f32 v78,v98,v78,v97
v_fma_f32 v79,v98,v79,v97
v_mul_f32 v76,v28,v76
v_mul_f32 v77,v29,v77
v_mul_f32 v78,v30,v78
v_mul_f32 v79,v31,v79
v_exp_f32 v76,v76
v_exp_f32 v77,v77
v_exp_f32 v78,v78
v_exp_f32 v79,v79
v_mfma_f32_16x16x32_bf16 v[32:35],a[200:203],v[44:47],v[32:35]
buffer_load_dwordx4 v0,s[36:39],s66 offen offset:3072 sc1 lds
v_fma_f32 v75,v75,v99,v99
v_rcp_f32 v74,v74
v_rcp_f32 v75,v75
v_fma_f32 v76,v76,v99,v99
v_fma_f32 v77,v77,v99,v99
v_fma_f32 v78,v78,v99,v99
v_fma_f32 v79,v79,v99,v99
v_rcp_f32 v76,v76
v_rcp_f32 v77,v77
v_rcp_f32 v78,v78
v_rcp_f32 v79,v79
v_mfma_f32_16x16x32_bf16 v[36:39],a[204:207],v[44:47],v[36:39]
s_cmp_lt_u32 s67,s59
ds_read_b128 v[40:43],v2 offset:0
v_mul_f32 v25,v25,v73
v_mul_f32 v26,v26,v74
v_mul_f32 v27,v27,v75
v_cvt_pk_bf16_f32 v80,v24,v25
v_cvt_pk_bf16_f32 v81,v26,v27
v_mul_f32 v28,v28,v76
v_mul_f32 v29,v29,v77
v_mul_f32 v30,v30,v78
v_mul_f32 v31,v31,v79
v_cvt_pk_bf16_f32 v82,v28,v29
v_cvt_pk_bf16_f32 v83,v30,v31
v_mfma_f32_16x16x32_bf16 v[32:35],a[208:211],v[48:51],v[32:35]
buffer_store_dwordx4 v[80:83],v3,s[36:39],s73 offen offset:0 sc0 sc1
ds_read_b128 v[44:47],v2 offset:1024
ds_read_b64 v[92:93],v11 offset:128
v_xor_b32 v8,16384,v8
v_mfma_f32_16x16x32_bf16 v[36:39],a[212:215],v[48:51],v[36:39]
v_mfma_f32_16x16x32_bf16 v[32:35],a[216:219],v[52:55],v[32:35]
ds_read_b128 v[48:51],v2 offset:2048
v_mfma_f32_16x16x32_bf16 v[36:39],a[220:223],v[52:55],v[36:39]
s_waitcnt lgkmcnt(4)
v_mfma_f32_16x16x32_bf16 v[32:35],a[224:227],v[56:59],v[32:35]
s_waitcnt lgkmcnt(1)
ds_read_b128 v[52:55],v2 offset:3072
v_mfma_f32_16x16x32_bf16 v[36:39],a[228:231],v[56:59],v[36:39]
v_mfma_f32_16x16x32_bf16 v[32:35],a[232:235],v[60:63],v[32:35]
ds_read_b128 v[56:59],v2 offset:4096
v_mfma_f32_16x16x32_bf16 v[36:39],a[236:239],v[60:63],v[36:39]
v_mfma_f32_16x16x32_bf16 v[32:35],a[240:243],v[64:67],v[32:35])"
R"(
ds_read_b128 v[60:63],v2 offset:5120
v_mfma_f32_16x16x32_bf16 v[36:39],a[244:247],v[64:67],v[36:39]
v_mfma_f32_16x16x32_bf16 v[32:35],a[248:251],v[68:71],v[32:35]
s_waitcnt lgkmcnt(2)
ds_read_b128 v[64:67],v2 offset:6144
v_mfma_f32_16x16x32_bf16 v[36:39],a[252:255],v[68:71],v[36:39]
s_cbranch_scc1 Lg3o_d0w0_%=
s_branch Lg3o_slow0_%=
Lg3o_d0w1_%=:
v_mfma_f32_16x16x32_bf16 v[24:27],a[0:3],v[40:43],0
s_add_u32 m0,s62,16384
ds_read_b128 v[68:71],v2 offset:7168
v_mul_f32 v96,s75,v92
v_mfma_f32_16x16x32_bf16 v[28:31],a[4:7],v[40:43],0
s_mul_i32 s73,s47,7
v_fma_f32 v93,v93,s75,v12
v_fma_f32 v93,-v96,v96,v93
v_mfma_f32_16x16x32_bf16 v[24:27],a[8:11],v[44:47],v[24:27]
buffer_load_dwordx4 v1,s[36:39],s66 offen offset:0 sc1 lds
v_mfma_f32_16x16x32_bf16 v[28:31],a[12:15],v[44:47],v[28:31]
s_lshr_b32 s73,s73,1
ds_read_b128 v[40:43],v2 offset:8192
v_max_f32 v93,v93,v12
v_mfma_f32_16x16x32_bf16 v[24:27],a[16:19],v[48:51],v[24:27]
s_lshr_b32 s72,s66,5
ds_read_b128 v[44:47],v2 offset:9216
v_fma_f32 v32,-v96,v16,v32
v_mfma_f32_16x16x32_bf16 v[28:31],a[20:23],v[48:51],v[28:31]
s_lshr_b32 s71,s62,2
v_rsq_f32 v92,v93
v_mfma_f32_16x16x32_bf16 v[24:27],a[24:27],v[52:55],v[24:27]
buffer_load_dwordx4 v1,s[36:39],s66 offen offset:1024 sc1 lds
v_mfma_f32_16x16x32_bf16 v[28:31],a[28:31],v[52:55],v[28:31]
s_waitcnt lgkmcnt(2)
ds_read_b128 v[48:51],v2 offset:10240
v_mul_f32 v97,s76,v92
v_mfma_f32_16x16x32_bf16 v[24:27],a[32:35],v[56:59],v[24:27]
ds_read_b128 v[52:55],v2 offset:11264
v_mul_f32 v98,v92,v92
v_mfma_f32_16x16x32_bf16 v[28:31],a[36:39],v[56:59],v[28:31]
v_mul_f32 v99,v93,v92
v_mul_f32 v98,s77,v98
v_mfma_f32_16x16x32_bf16 v[24:27],a[40:43],v[60:63],v[24:27]
buffer_load_dwordx4 v1,s[36:39],s66 offen offset:2048 sc1 lds
v_mfma_f32_16x16x32_bf16 v[28:31],a[44:47],v[60:63],v[28:31]
ds_read_b128 v[56:59],v2 offset:12288
v_mul_f32 v98,v98,v97
v_mfma_f32_16x16x32_bf16 v[24:27],a[48:51],v[64:67],v[24:27]
ds_read_b128 v[60:63],v2 offset:13312
v_fma_f32 v33,-v96,v17,v33
v_mfma_f32_16x16x32_bf16 v[28:31],a[52:55],v[64:67],v[28:31]
v_fma_f32 v34,-v96,v18,v34
v_fma_f32 v35,-v96,v19,v35
v_mfma_f32_16x16x32_bf16 v[24:27],a[56:59],v[68:71],v[24:27]
buffer_load_dwordx4 v1,s[36:39],s66 offen offset:3072 sc1 lds
v_mfma_f32_16x16x32_bf16 v[28:31],a[60:63],v[68:71],v[28:31]
s_waitcnt lgkmcnt(2)
ds_read_b128 v[64:67],v2 offset:14336
s_add_u32 m0,s71,s88
v_mfma_f32_16x16x32_bf16 v[24:27],a[64:67],v[40:43],v[24:27]
s_xor_b32 s87,s64,65536
ds_read_b128 v[68:71],v2 offset:15360
buffer_load_dword v9,s[36:39],s72 offen offset:0 sc1 lds
s_waitcnt vmcnt(10)
v_mfma_f32_16x16x32_bf16 v[28:31],a[68:71],v[40:43],v[28:31]
s_barrier
v_mfma_f32_16x16x32_bf16 v[24:27],a[72:75],v[44:47],v[24:27]
s_cmp_ge_u32 s66,s59
ds_read_b128 v[40:43],v2 offset:16384
ds_read_b32 v88,v8 offset:0
v_mfma_f32_16x16x32_bf16 v[28:31],a[76:79],v[44:47],v[28:31]
s_cselect_b32 s86,s49,s50
v_mul_f32 v72,v32,v32
v_mul_f32 v73,v33,v33
v_mfma_f32_16x16x32_bf16 v[24:27],a[80:83],v[48:51],v[24:27]
s_cselect_b32 s87,s87,s63
ds_read_b128 v[44:47],v2 offset:17408
v_mul_f32 v74,v34,v34
v_mfma_f32_16x16x32_bf16 v[28:31],a[84:87],v[48:51],v[28:31]
s_mov_b32 s62,s87
v_mul_f32 v75,v35,v35
v_fma_f32 v72,v98,v72,v97
v_mfma_f32_16x16x32_bf16 v[24:27],a[88:91],v[52:55],v[24:27]
s_mov_b32 m0,s62
ds_read_b128 v[48:51],v2 offset:18432
v_mul_f32 v72,v32,v72
v_mfma_f32_16x16x32_bf16 v[28:31],a[92:95],v[52:55],v[28:31]
s_waitcnt lgkmcnt(4)
v_exp_f32 v72,v72
v_mfma_f32_16x16x32_bf16 v[24:27],a[96:99],v[56:59],v[24:27]
s_nop 0
s_waitcnt lgkmcnt(2)
ds_read_b128 v[52:55],v2 offset:19456
v_fma_f32 v72,v72,v99,v99
v_mfma_f32_16x16x32_bf16 v[28:31],a[100:103],v[56:59],v[28:31]
s_barrier
v_mfma_f32_16x16x32_bf16 v[24:27],a[104:107],v[60:63],v[24:27]
ds_read_b128 v[56:59],v2 offset:20480
ds_read_b32 v13,v7 offset:0
v_mfma_f32_16x16x32_bf16 v[28:31],a[108:111],v[60:63],v[28:31]
v_rcp_f32 v72,v72
v_mfma_f32_16x16x32_bf16 v[24:27],a[112:115],v[64:67],v[24:27]
ds_read_b128 v[60:63],v2 offset:21504
v_add_f32_dpp v88,v88,v88 quad_perm:[1,0,3,2] row_mask:0xf bank_mask:0xf
v_mfma_f32_16x16x32_bf16 v[28:31],a[116:119],v[64:67],v[28:31]
s_nop 0
v_add_f32_dpp v88,v88,v88 quad_perm:[2,3,0,1] row_mask:0xf bank_mask:0xf
v_fma_f32 v73,v98,v73,v97
v_mfma_f32_16x16x32_bf16 v[24:27],a[120:123],v[68:71],v[24:27]
s_waitcnt lgkmcnt(1)
ds_read_b128 v[64:67],v2 offset:22528
v_mul_f32 v73,v33,v73
v_mfma_f32_16x16x32_bf16 v[28:31],a[124:127],v[68:71],v[28:31]
v_cmp_eq_u32 vcc,s60,v13
v_exp_f32 v73,v73
v_add_f32_dpp v88,v88,v88 row_half_mirror row_mask:0xf bank_mask:0xf
v_mfma_f32_16x16x32_bf16 v[24:27],a[128:131],v[40:43],v[24:27]
s_cmp_eq_u32 vcc_lo,-1
ds_write_b32 v10,v88 offset:0
s_cselect_b32 s67,s86,s59
v_mfma_f32_16x16x32_bf16 v[28:31],a[132:135],v[40:43],v[28:31]
s_xor_b32 s63,s63,65536
ds_read_b128 v[68:71],v2 offset:23552
buffer_load_dwordx4 v0,s[36:39],s67 offen offset:0 sc1 lds
v_mfma_f32_16x16x32_bf16 v[24:27],a[136:139],v[44:47],v[24:27]
s_add_u32 s53,s52,s55
ds_read_b128 v[40:43],v2 offset:24576
v_fma_f32 v73,v73,v99,v99
v_mfma_f32_16x16x32_bf16 v[28:31],a[140:143],v[44:47],v[28:31]
s_cmp_eq_u32 s53,s56
v_rcp_f32 v73,v73
v_mfma_f32_16x16x32_bf16 v[24:27],a[144:147],v[48:51],v[24:27]
s_cselect_b32 s53,s57,s53
ds_read_b128 v[44:47],v2 offset:25600
v_fma_f32 v74,v98,v74,v97
v_mfma_f32_16x16x32_bf16 v[28:31],a[148:151],v[48:51],v[28:31]
buffer_load_dwordx4 v0,s[36:39],s67 offen offset:1024 sc1 lds
v_mfma_f32_16x16x32_bf16 v[24:27],a[152:155],v[52:55],v[24:27]
s_cmp_eq_u32 s53,s58
ds_read_b128 v[48:51],v2 offset:26624
v_mul_f32 v74,v34,v74
v_mfma_f32_16x16x32_bf16 v[28:31],a[156:159],v[52:55],v[28:31]
s_waitcnt lgkmcnt(3)
v_exp_f32 v74,v74
v_mfma_f32_16x16x32_bf16 v[24:27],a[160:163],v[56:59],v[24:27]
s_cselect_b32 s53,s59,s53
ds_read_b128 v[52:55],v2 offset:27648
v_fma_f32 v74,v74,v99,v99
v_mfma_f32_16x16x32_bf16 v[28:31],a[164:167],v[56:59],v[28:31]
buffer_load_dwordx4 v0,s[36:39],s67 offen offset:2048 sc1 lds
v_mfma_f32_16x16x32_bf16 v[24:27],a[168:171],v[60:63],v[24:27]
s_cmp_ge_u32 s66,s59
ds_read_b128 v[56:59],v2 offset:28672
v_fma_f32 v75,v98,v75,v97
v_mfma_f32_16x16x32_bf16 v[28:31],a[172:175],v[60:63],v[28:31]
v_rcp_f32 v74,v74
v_mfma_f32_16x16x32_bf16 v[24:27],a[176:179],v[64:67],v[24:27]
ds_read_b128 v[60:63],v2 offset:29696
v_mul_f32 v75,v35,v75
v_mfma_f32_16x16x32_bf16 v[28:31],a[180:183],v[64:67],v[28:31]
buffer_load_dwordx4 v0,s[36:39],s67 offen offset:3072 sc1 lds
v_mfma_f32_16x16x32_bf16 v[24:27],a[184:187],v[68:71],v[24:27]
ds_read_b128 v[64:67],v2 offset:30720
v_mul_f32 v32,v32,v72
v_mfma_f32_16x16x32_bf16 v[28:31],a[188:191],v[68:71],v[28:31]
s_waitcnt lgkmcnt(3)
v_exp_f32 v75,v75
v_mfma_f32_16x16x32_bf16 v[24:27],a[192:195],v[40:43],v[24:27]
s_waitcnt vmcnt(9)
ds_read_b128 v[68:71],v2 offset:31744
v_fma_f32 v75,v75,v99,v99
v_mfma_f32_16x16x32_bf16 v[28:31],a[196:199],v[40:43],v[28:31]
s_barrier
v_mfma_f32_16x16x32_bf16 v[24:27],a[200:203],v[44:47],v[24:27]
v_fma_f32 v36,-v96,v20,v36
v_fma_f32 v37,-v96,v21,v37
v_fma_f32 v38,-v96,v22,v38
v_fma_f32 v39,-v96,v23,v39
v_mul_f32 v76,v36,v36
v_mul_f32 v77,v37,v37
v_mul_f32 v78,v38,v38
v_mul_f32 v79,v39,v39
v_fma_f32 v76,v98,v76,v97
v_fma_f32 v77,v98,v77,v97
v_fma_f32 v78,v98,v78,v97
v_fma_f32 v79,v98,v79,v97
v_mul_f32 v76,v36,v76
v_mul_f32 v77,v37,v77
v_mul_f32 v78,v38,v78
v_mul_f32 v79,v39,v79
v_exp_f32 v76,v76
v_exp_f32 v77,v77
v_exp_f32 v78,v78
v_exp_f32 v79,v79
v_mfma_f32_16x16x32_bf16 v[28:31],a[204:207],v[44:47],v[28:31]
ds_read_b128 v[40:43],v2 offset:32768
v_rcp_f32 v75,v75
v_fma_f32 v76,v76,v99,v99
v_fma_f32 v77,v77,v99,v99
v_fma_f32 v78,v78,v99,v99)"
R"(
v_fma_f32 v79,v79,v99,v99
v_rcp_f32 v76,v76
v_rcp_f32 v77,v77
v_rcp_f32 v78,v78
v_rcp_f32 v79,v79
v_mfma_f32_16x16x32_bf16 v[24:27],a[208:211],v[48:51],v[24:27]
v_mul_f32 v33,v33,v73
v_mul_f32 v34,v34,v74
v_mul_f32 v35,v35,v75
v_cvt_pk_bf16_f32 v80,v32,v33
v_cvt_pk_bf16_f32 v81,v34,v35
v_mul_f32 v36,v36,v76
v_mul_f32 v37,v37,v77
v_mul_f32 v38,v38,v78
v_mul_f32 v39,v39,v79
v_cvt_pk_bf16_f32 v82,v36,v37
v_cvt_pk_bf16_f32 v83,v38,v39
v_mfma_f32_16x16x32_bf16 v[28:31],a[212:215],v[48:51],v[28:31]
ds_read_b128 v[44:47],v2 offset:33792
buffer_store_dwordx4 v[80:83],v3,s[36:39],s73 offen offset:0 sc0 sc1
ds_read_b64 v[92:93],v11 offset:0
v_mfma_f32_16x16x32_bf16 v[24:27],a[216:219],v[52:55],v[24:27]
ds_read_b128 v[48:51],v2 offset:34816
v_mfma_f32_16x16x32_bf16 v[28:31],a[220:223],v[52:55],v[28:31]
s_waitcnt lgkmcnt(4)
v_mfma_f32_16x16x32_bf16 v[24:27],a[224:227],v[56:59],v[24:27]
ds_read_b128 v[52:55],v2 offset:35840
v_mfma_f32_16x16x32_bf16 v[28:31],a[228:231],v[56:59],v[28:31]
s_waitcnt lgkmcnt(2)
v_mfma_f32_16x16x32_bf16 v[24:27],a[232:235],v[60:63],v[24:27]
ds_read_b128 v[56:59],v2 offset:36864
v_mfma_f32_16x16x32_bf16 v[28:31],a[236:239],v[60:63],v[28:31]
v_mfma_f32_16x16x32_bf16 v[24:27],a[240:243],v[64:67],v[24:27]
ds_read_b128 v[60:63],v2 offset:37888
v_mfma_f32_16x16x32_bf16 v[28:31],a[244:247],v[64:67],v[28:31]
v_mfma_f32_16x16x32_bf16 v[24:27],a[248:251],v[68:71],v[24:27]
s_waitcnt lgkmcnt(2)
ds_read_b128 v[64:67],v2 offset:38912
v_mfma_f32_16x16x32_bf16 v[28:31],a[252:255],v[68:71],v[28:31]
s_cbranch_scc1 Lg3o_slow1_%=
Lg3o_d1w1_%=:
v_mfma_f32_16x16x32_bf16 v[32:35],a[0:3],v[40:43],0
s_add_u32 m0,s62,16384
ds_read_b128 v[68:71],v2 offset:39936
v_mul_f32 v96,s75,v92
v_mfma_f32_16x16x32_bf16 v[36:39],a[4:7],v[40:43],0
s_mul_i32 s73,s48,7
v_fma_f32 v93,v93,s75,v12
v_fma_f32 v93,-v96,v96,v93
v_mfma_f32_16x16x32_bf16 v[32:35],a[8:11],v[44:47],v[32:35]
buffer_load_dwordx4 v1,s[36:39],s67 offen offset:0 sc1 lds
v_mfma_f32_16x16x32_bf16 v[36:39],a[12:15],v[44:47],v[36:39]
s_lshr_b32 s73,s73,1
ds_read_b128 v[40:43],v2 offset:40960
v_max_f32 v93,v93,v12
v_mfma_f32_16x16x32_bf16 v[32:35],a[16:19],v[48:51],v[32:35]
s_lshr_b32 s72,s67,5
ds_read_b128 v[44:47],v2 offset:41984
v_fma_f32 v24,-v96,v16,v24
v_mfma_f32_16x16x32_bf16 v[36:39],a[20:23],v[48:51],v[36:39]
s_lshr_b32 s71,s62,2
v_rsq_f32 v92,v93
v_mfma_f32_16x16x32_bf16 v[32:35],a[24:27],v[52:55],v[32:35]
buffer_load_dwordx4 v1,s[36:39],s67 offen offset:1024 sc1 lds
v_mfma_f32_16x16x32_bf16 v[36:39],a[28:31],v[52:55],v[36:39]
s_waitcnt lgkmcnt(2)
ds_read_b128 v[48:51],v2 offset:43008
v_mul_f32 v97,s76,v92
v_mfma_f32_16x16x32_bf16 v[32:35],a[32:35],v[56:59],v[32:35]
s_mov_b64 s[44:45],s[46:47]
ds_read_b128 v[52:55],v2 offset:44032
v_mul_f32 v98,v92,v92
v_mfma_f32_16x16x32_bf16 v[36:39],a[36:39],v[56:59],v[36:39]
s_mov_b64 s[46:47],s[48:49]
v_mul_f32 v99,v93,v92
v_mul_f32 v98,s77,v98
v_mfma_f32_16x16x32_bf16 v[32:35],a[40:43],v[60:63],v[32:35]
buffer_load_dwordx4 v1,s[36:39],s67 offen offset:2048 sc1 lds
v_mfma_f32_16x16x32_bf16 v[36:39],a[44:47],v[60:63],v[36:39]
s_mov_b64 s[48:49],s[50:51]
ds_read_b128 v[56:59],v2 offset:45056
v_mul_f32 v98,v98,v97
v_mfma_f32_16x16x32_bf16 v[32:35],a[48:51],v[64:67],v[32:35]
ds_read_b128 v[60:63],v2 offset:46080
v_fma_f32 v25,-v96,v17,v25
v_mfma_f32_16x16x32_bf16 v[36:39],a[52:55],v[64:67],v[36:39]
v_fma_f32 v26,-v96,v18,v26
v_fma_f32 v27,-v96,v19,v27
v_mfma_f32_16x16x32_bf16 v[32:35],a[56:59],v[68:71],v[32:35]
buffer_load_dwordx4 v1,s[36:39],s67 offen offset:3072 sc1 lds
v_mfma_f32_16x16x32_bf16 v[36:39],a[60:63],v[68:71],v[36:39]
s_waitcnt lgkmcnt(2)
ds_read_b128 v[64:67],v2 offset:47104
s_add_u32 m0,s71,s88
v_mfma_f32_16x16x32_bf16 v[32:35],a[64:67],v[40:43],v[32:35]
s_xor_b32 s87,s63,65536
ds_read_b128 v[68:71],v2 offset:48128
buffer_load_dword v9,s[36:39],s72 offen offset:0 sc1 lds
s_waitcnt vmcnt(10)
v_mfma_f32_16x16x32_bf16 v[36:39],a[68:71],v[40:43],v[36:39]
s_barrier
v_mfma_f32_16x16x32_bf16 v[32:35],a[72:75],v[44:47],v[32:35]
s_cmp_ge_u32 s67,s59
ds_read_b128 v[40:43],v2 offset:49152
ds_read_b32 v88,v8 offset:8192
v_mfma_f32_16x16x32_bf16 v[36:39],a[76:79],v[44:47],v[36:39]
s_cselect_b32 s86,s50,s51
v_mul_f32 v72,v24,v24
v_mul_f32 v73,v25,v25
v_mfma_f32_16x16x32_bf16 v[32:35],a[80:83],v[48:51],v[32:35]
s_cselect_b32 s87,s87,s64
ds_read_b128 v[44:47],v2 offset:50176
v_mul_f32 v74,v26,v26
v_mfma_f32_16x16x32_bf16 v[36:39],a[84:87],v[48:51],v[36:39]
s_mov_b32 s62,s87
v_mul_f32 v75,v27,v27
v_fma_f32 v72,v98,v72,v97
v_mfma_f32_16x16x32_bf16 v[32:35],a[88:91],v[52:55],v[32:35]
s_mov_b32 m0,s62
ds_read_b128 v[48:51],v2 offset:51200
v_mul_f32 v72,v24,v72
v_mfma_f32_16x16x32_bf16 v[36:39],a[92:95],v[52:55],v[36:39]
s_waitcnt lgkmcnt(4)
v_exp_f32 v72,v72
v_mfma_f32_16x16x32_bf16 v[32:35],a[96:99],v[56:59],v[32:35]
s_nop 0
s_waitcnt lgkmcnt(2)
ds_read_b128 v[52:55],v2 offset:52224
v_fma_f32 v72,v72,v99,v99
v_mfma_f32_16x16x32_bf16 v[36:39],a[100:103],v[56:59],v[36:39]
s_barrier
v_mfma_f32_16x16x32_bf16 v[32:35],a[104:107],v[60:63],v[32:35]
s_mov_b64 s[50:51],s[52:53]
ds_read_b128 v[56:59],v2 offset:53248
ds_read_b32 v13,v7 offset:256
v_mfma_f32_16x16x32_bf16 v[36:39],a[108:111],v[60:63],v[36:39]
v_rcp_f32 v72,v72
v_mfma_f32_16x16x32_bf16 v[32:35],a[112:115],v[64:67],v[32:35]
ds_read_b128 v[60:63],v2 offset:54272
v_add_f32_dpp v88,v88,v88 quad_perm:[1,0,3,2] row_mask:0xf bank_mask:0xf
v_mfma_f32_16x16x32_bf16 v[36:39],a[116:119],v[64:67],v[36:39]
s_nop 0
v_add_f32_dpp v88,v88,v88 quad_perm:[2,3,0,1] row_mask:0xf bank_mask:0xf
v_fma_f32 v73,v98,v73,v97
v_mfma_f32_16x16x32_bf16 v[32:35],a[120:123],v[68:71],v[32:35]
s_waitcnt lgkmcnt(1)
ds_read_b128 v[64:67],v2 offset:55296
v_mul_f32 v73,v25,v73
v_mfma_f32_16x16x32_bf16 v[36:39],a[124:127],v[68:71],v[36:39]
v_cmp_eq_u32 vcc,s60,v13
v_exp_f32 v73,v73
v_add_f32_dpp v88,v88,v88 row_half_mirror row_mask:0xf bank_mask:0xf
v_mfma_f32_16x16x32_bf16 v[32:35],a[128:131],v[40:43],v[32:35]
s_cmp_eq_u32 vcc_lo,-1
ds_write_b32 v10,v88 offset:128
s_cselect_b32 s66,s86,s59
v_mfma_f32_16x16x32_bf16 v[36:39],a[132:135],v[40:43],v[36:39]
s_xor_b32 s64,s64,65536
ds_read_b128 v[68:71],v2 offset:56320
buffer_load_dwordx4 v0,s[36:39],s66 offen offset:0 sc1 lds
v_mfma_f32_16x16x32_bf16 v[32:35],a[136:139],v[44:47],v[32:35]
s_add_u32 s74,s53,s55
ds_read_b128 v[40:43],v2 offset:57344
v_fma_f32 v73,v73,v99,v99
v_mfma_f32_16x16x32_bf16 v[36:39],a[140:143],v[44:47],v[36:39]
s_cmp_eq_u32 s74,s56
v_rcp_f32 v73,v73
v_mfma_f32_16x16x32_bf16 v[32:35],a[144:147],v[48:51],v[32:35]
s_cselect_b32 s74,s57,s74
ds_read_b128 v[44:47],v2 offset:58368
v_fma_f32 v74,v98,v74,v97
v_mfma_f32_16x16x32_bf16 v[36:39],a[148:151],v[48:51],v[36:39]
buffer_load_dwordx4 v0,s[36:39],s66 offen offset:1024 sc1 lds
v_mfma_f32_16x16x32_bf16 v[32:35],a[152:155],v[52:55],v[32:35]
s_cmp_eq_u32 s74,s58
ds_read_b128 v[48:51],v2 offset:59392
v_mul_f32 v74,v26,v74
v_mfma_f32_16x16x32_bf16 v[36:39],a[156:159],v[52:55],v[36:39]
s_waitcnt lgkmcnt(3)
v_exp_f32 v74,v74
v_mfma_f32_16x16x32_bf16 v[32:35],a[160:163],v[56:59],v[32:35]
s_cselect_b32 s74,s59,s74
ds_read_b128 v[52:55],v2 offset:60416
v_fma_f32 v74,v74,v99,v99
v_mfma_f32_16x16x32_bf16 v[36:39],a[164:167],v[56:59],v[36:39]
buffer_load_dwordx4 v0,s[36:39],s66 offen offset:2048 sc1 lds
v_mfma_f32_16x16x32_bf16 v[32:35],a[168:171],v[60:63],v[32:35]
s_mov_b32 s52,s74
ds_read_b128 v[56:59],v2 offset:61440
v_fma_f32 v75,v98,v75,v97
v_mfma_f32_16x16x32_bf16 v[36:39],a[172:175],v[60:63],v[36:39]
s_cmp_lt_u32 s67,s59
v_rcp_f32 v74,v74
v_mfma_f32_16x16x32_bf16 v[32:35],a[176:179],v[64:67],v[32:35]
ds_read_b128 v[60:63],v2 offset:62464
v_mul_f32 v75,v27,v75
v_mfma_f32_16x16x32_bf16 v[36:39],a[180:183],v[64:67],v[36:39]
buffer_load_dwordx4 v0,s[36:39],s66 offen offset:3072 sc1 lds
v_mfma_f32_16x16x32_bf16 v[32:35],a[184:187],v[68:71],v[32:35]
ds_read_b128 v[64:67],v2 offset:63488)"
R"(
v_mul_f32 v24,v24,v72
v_mfma_f32_16x16x32_bf16 v[36:39],a[188:191],v[68:71],v[36:39]
s_waitcnt lgkmcnt(3)
v_exp_f32 v75,v75
v_mfma_f32_16x16x32_bf16 v[32:35],a[192:195],v[40:43],v[32:35]
s_waitcnt vmcnt(9)
ds_read_b128 v[68:71],v2 offset:64512
v_xor_b32 v2,65536,v2
v_mfma_f32_16x16x32_bf16 v[36:39],a[196:199],v[40:43],v[36:39]
s_barrier
v_mfma_f32_16x16x32_bf16 v[32:35],a[200:203],v[44:47],v[32:35]
v_fma_f32 v28,-v96,v20,v28
v_fma_f32 v29,-v96,v21,v29
v_fma_f32 v30,-v96,v22,v30
v_fma_f32 v31,-v96,v23,v31
v_mul_f32 v76,v28,v28
v_mul_f32 v77,v29,v29
v_mul_f32 v78,v30,v30
v_mul_f32 v79,v31,v31
v_fma_f32 v76,v98,v76,v97
v_fma_f32 v77,v98,v77,v97
v_fma_f32 v78,v98,v78,v97
v_fma_f32 v79,v98,v79,v97
v_mul_f32 v76,v28,v76
v_mul_f32 v77,v29,v77
v_mul_f32 v78,v30,v78
v_mul_f32 v79,v31,v79
v_exp_f32 v76,v76
v_exp_f32 v77,v77
v_exp_f32 v78,v78
v_exp_f32 v79,v79
v_mfma_f32_16x16x32_bf16 v[36:39],a[204:207],v[44:47],v[36:39]
ds_read_b128 v[40:43],v2 offset:0
v_fma_f32 v75,v75,v99,v99
v_rcp_f32 v75,v75
v_fma_f32 v76,v76,v99,v99
v_fma_f32 v77,v77,v99,v99
v_fma_f32 v78,v78,v99,v99
v_fma_f32 v79,v79,v99,v99
v_rcp_f32 v76,v76
v_rcp_f32 v77,v77
v_rcp_f32 v78,v78
v_rcp_f32 v79,v79
v_mfma_f32_16x16x32_bf16 v[32:35],a[208:211],v[48:51],v[32:35]
v_mul_f32 v25,v25,v73
v_mul_f32 v26,v26,v74
v_mul_f32 v27,v27,v75
v_cvt_pk_bf16_f32 v80,v24,v25
v_cvt_pk_bf16_f32 v81,v26,v27
v_mul_f32 v28,v28,v76
v_mul_f32 v29,v29,v77
v_mul_f32 v30,v30,v78
v_mul_f32 v31,v31,v79
v_cvt_pk_bf16_f32 v82,v28,v29
v_cvt_pk_bf16_f32 v83,v30,v31
v_mfma_f32_16x16x32_bf16 v[36:39],a[212:215],v[48:51],v[36:39]
ds_read_b128 v[44:47],v2 offset:1024
buffer_store_dwordx4 v[80:83],v3,s[36:39],s73 offen offset:0 sc0 sc1
ds_read_b64 v[92:93],v11 offset:128
v_xor_b32 v8,16384,v8
v_mfma_f32_16x16x32_bf16 v[32:35],a[216:219],v[52:55],v[32:35]
ds_read_b128 v[48:51],v2 offset:2048
v_mfma_f32_16x16x32_bf16 v[36:39],a[220:223],v[52:55],v[36:39]
s_waitcnt lgkmcnt(4)
v_mfma_f32_16x16x32_bf16 v[32:35],a[224:227],v[56:59],v[32:35]
ds_read_b128 v[52:55],v2 offset:3072
v_mfma_f32_16x16x32_bf16 v[36:39],a[228:231],v[56:59],v[36:39]
s_waitcnt lgkmcnt(2)
v_mfma_f32_16x16x32_bf16 v[32:35],a[232:235],v[60:63],v[32:35]
ds_read_b128 v[56:59],v2 offset:4096
v_mfma_f32_16x16x32_bf16 v[36:39],a[236:239],v[60:63],v[36:39]
v_mfma_f32_16x16x32_bf16 v[32:35],a[240:243],v[64:67],v[32:35]
ds_read_b128 v[60:63],v2 offset:5120
v_mfma_f32_16x16x32_bf16 v[36:39],a[244:247],v[64:67],v[36:39]
v_mfma_f32_16x16x32_bf16 v[32:35],a[248:251],v[68:71],v[32:35]
s_waitcnt lgkmcnt(2)
ds_read_b128 v[64:67],v2 offset:6144
v_mfma_f32_16x16x32_bf16 v[36:39],a[252:255],v[68:71],v[36:39]
s_cbranch_scc1 Lg3o_d0w1_%=
s_branch Lg3o_slow0_%=
Lg3o_d0w2_%=:
v_mfma_f32_16x16x32_bf16 v[24:27],a[0:3],v[40:43],0
s_add_u32 m0,s62,16384
ds_read_b128 v[68:71],v2 offset:7168
v_mul_f32 v96,s75,v92
v_mfma_f32_16x16x32_bf16 v[28:31],a[4:7],v[40:43],0
s_mul_i32 s73,s47,7
v_fma_f32 v93,v93,s75,v12
v_fma_f32 v93,-v96,v96,v93
v_mfma_f32_16x16x32_bf16 v[24:27],a[8:11],v[44:47],v[24:27]
s_lshr_b32 s73,s73,1
ds_read_b128 v[40:43],v2 offset:8192
v_max_f32 v93,v93,v12
v_mfma_f32_16x16x32_bf16 v[28:31],a[12:15],v[44:47],v[28:31]
buffer_load_dwordx4 v1,s[36:39],s66 offen offset:0 sc1 lds
v_mfma_f32_16x16x32_bf16 v[24:27],a[16:19],v[48:51],v[24:27]
s_lshr_b32 s72,s66,5
ds_read_b128 v[44:47],v2 offset:9216
v_fma_f32 v32,-v96,v16,v32
v_mfma_f32_16x16x32_bf16 v[28:31],a[20:23],v[48:51],v[28:31]
s_lshr_b32 s71,s62,2
v_rsq_f32 v92,v93
v_mfma_f32_16x16x32_bf16 v[24:27],a[24:27],v[52:55],v[24:27]
ds_read_b128 v[48:51],v2 offset:10240
v_mul_f32 v97,s76,v92
v_mfma_f32_16x16x32_bf16 v[28:31],a[28:31],v[52:55],v[28:31]
s_waitcnt lgkmcnt(3)
buffer_load_dwordx4 v1,s[36:39],s66 offen offset:1024 sc1 lds
v_mfma_f32_16x16x32_bf16 v[24:27],a[32:35],v[56:59],v[24:27]
ds_read_b128 v[52:55],v2 offset:11264
v_mul_f32 v98,v92,v92
v_mfma_f32_16x16x32_bf16 v[28:31],a[36:39],v[56:59],v[28:31]
v_mul_f32 v99,v93,v92
v_mul_f32 v98,s77,v98
v_mfma_f32_16x16x32_bf16 v[24:27],a[40:43],v[60:63],v[24:27]
ds_read_b128 v[56:59],v2 offset:12288
v_mul_f32 v98,v98,v97
v_mfma_f32_16x16x32_bf16 v[28:31],a[44:47],v[60:63],v[28:31]
buffer_load_dwordx4 v1,s[36:39],s66 offen offset:2048 sc1 lds
v_mfma_f32_16x16x32_bf16 v[24:27],a[48:51],v[64:67],v[24:27]
ds_read_b128 v[60:63],v2 offset:13312
v_fma_f32 v33,-v96,v17,v33
v_mfma_f32_16x16x32_bf16 v[28:31],a[52:55],v[64:67],v[28:31]
v_fma_f32 v34,-v96,v18,v34
v_fma_f32 v35,-v96,v19,v35
v_mfma_f32_16x16x32_bf16 v[24:27],a[56:59],v[68:71],v[24:27]
ds_read_b128 v[64:67],v2 offset:14336
v_mul_f32 v72,v32,v32
v_mfma_f32_16x16x32_bf16 v[28:31],a[60:63],v[68:71],v[28:31]
s_waitcnt lgkmcnt(3)
buffer_load_dwordx4 v1,s[36:39],s66 offen offset:3072 sc1 lds
s_add_u32 m0,s71,s88
v_mfma_f32_16x16x32_bf16 v[24:27],a[64:67],v[40:43],v[24:27]
s_xor_b32 s87,s64,65536
ds_read_b128 v[68:71],v2 offset:15360
buffer_load_dword v9,s[36:39],s72 offen offset:0 sc1 lds
s_waitcnt vmcnt(10)
v_mfma_f32_16x16x32_bf16 v[28:31],a[68:71],v[40:43],v[28:31]
s_barrier
v_mfma_f32_16x16x32_bf16 v[24:27],a[72:75],v[44:47],v[24:27]
s_cmp_ge_u32 s66,s59
ds_read_b128 v[40:43],v2 offset:16384
ds_read_b32 v88,v8 offset:0
v_mfma_f32_16x16x32_bf16 v[28:31],a[76:79],v[44:47],v[28:31]
s_cselect_b32 s86,s49,s50
v_mul_f32 v73,v33,v33
v_mul_f32 v74,v34,v34
v_mfma_f32_16x16x32_bf16 v[24:27],a[80:83],v[48:51],v[24:27]
s_cselect_b32 s87,s87,s63
ds_read_b128 v[44:47],v2 offset:17408
v_mul_f32 v75,v35,v35
v_mfma_f32_16x16x32_bf16 v[28:31],a[84:87],v[48:51],v[28:31]
s_mov_b32 s62,s87
v_fma_f32 v72,v98,v72,v97
v_mul_f32 v72,v32,v72
v_mfma_f32_16x16x32_bf16 v[24:27],a[88:91],v[52:55],v[24:27]
s_mov_b32 m0,s62
ds_read_b128 v[48:51],v2 offset:18432
v_fma_f32 v73,v98,v73,v97
v_mfma_f32_16x16x32_bf16 v[28:31],a[92:95],v[52:55],v[28:31]
s_waitcnt lgkmcnt(4)
v_exp_f32 v72,v72
v_mfma_f32_16x16x32_bf16 v[24:27],a[96:99],v[56:59],v[24:27]
s_nop 0
s_waitcnt lgkmcnt(2)
ds_read_b128 v[52:55],v2 offset:19456
v_mul_f32 v73,v33,v73
v_mfma_f32_16x16x32_bf16 v[28:31],a[100:103],v[56:59],v[28:31]
s_barrier
v_mfma_f32_16x16x32_bf16 v[24:27],a[104:107],v[60:63],v[24:27]
ds_read_b128 v[56:59],v2 offset:20480
ds_read_b32 v13,v7 offset:0
v_mfma_f32_16x16x32_bf16 v[28:31],a[108:111],v[60:63],v[28:31]
v_exp_f32 v73,v73
v_mfma_f32_16x16x32_bf16 v[24:27],a[112:115],v[64:67],v[24:27]
ds_read_b128 v[60:63],v2 offset:21504
v_fma_f32 v72,v72,v99,v99
v_mfma_f32_16x16x32_bf16 v[28:31],a[116:119],v[64:67],v[28:31]
v_rcp_f32 v72,v72
v_mfma_f32_16x16x32_bf16 v[24:27],a[120:123],v[68:71],v[24:27]
s_waitcnt lgkmcnt(1)
ds_read_b128 v[64:67],v2 offset:22528
v_fma_f32 v73,v73,v99,v99
v_mfma_f32_16x16x32_bf16 v[28:31],a[124:127],v[68:71],v[28:31]
v_cmp_eq_u32 vcc,s60,v13
v_rcp_f32 v73,v73
v_add_f32_dpp v88,v88,v88 quad_perm:[1,0,3,2] row_mask:0xf bank_mask:0xf
v_mfma_f32_16x16x32_bf16 v[24:27],a[128:131],v[40:43],v[24:27]
s_cmp_eq_u32 vcc_lo,-1
ds_read_b128 v[68:71],v2 offset:23552
v_add_f32_dpp v88,v88,v88 quad_perm:[2,3,0,1] row_mask:0xf bank_mask:0xf
v_mfma_f32_16x16x32_bf16 v[28:31],a[132:135],v[40:43],v[28:31]
s_cselect_b32 s67,s86,s59
v_add_f32_dpp v88,v88,v88 row_half_mirror row_mask:0xf bank_mask:0xf
v_fma_f32 v74,v98,v74,v97
v_mfma_f32_16x16x32_bf16 v[24:27],a[136:139],v[44:47],v[24:27]
buffer_load_dwordx4 v0,s[36:39],s67 offen offset:0 sc1 lds
v_mfma_f32_16x16x32_bf16 v[28:31],a[140:143],v[44:47],v[28:31]
s_xor_b32 s63,s63,65536
ds_read_b128 v[40:43],v2 offset:24576)"
R"(
v_mul_f32 v74,v34,v74
v_mfma_f32_16x16x32_bf16 v[24:27],a[144:147],v[48:51],v[24:27]
s_add_u32 s53,s52,s55
ds_write_b32 v10,v88 offset:0
v_mfma_f32_16x16x32_bf16 v[28:31],a[148:151],v[48:51],v[28:31]
s_cmp_eq_u32 s53,s56
ds_read_b128 v[44:47],v2 offset:25600
v_fma_f32 v75,v98,v75,v97
v_mfma_f32_16x16x32_bf16 v[24:27],a[152:155],v[52:55],v[24:27]
buffer_load_dwordx4 v0,s[36:39],s67 offen offset:1024 sc1 lds
v_mfma_f32_16x16x32_bf16 v[28:31],a[156:159],v[52:55],v[28:31]
s_waitcnt lgkmcnt(3)
ds_read_b128 v[48:51],v2 offset:26624
v_mul_f32 v75,v35,v75
v_mfma_f32_16x16x32_bf16 v[24:27],a[160:163],v[56:59],v[24:27]
s_cselect_b32 s53,s57,s53
ds_read_b128 v[52:55],v2 offset:27648
v_mul_f32 v32,v32,v72
v_mfma_f32_16x16x32_bf16 v[28:31],a[164:167],v[56:59],v[28:31]
s_cmp_eq_u32 s53,s58
v_exp_f32 v74,v74
v_mfma_f32_16x16x32_bf16 v[24:27],a[168:171],v[60:63],v[24:27]
buffer_load_dwordx4 v0,s[36:39],s67 offen offset:2048 sc1 lds
v_mfma_f32_16x16x32_bf16 v[28:31],a[172:175],v[60:63],v[28:31]
s_cselect_b32 s53,s59,s53
ds_read_b128 v[56:59],v2 offset:28672
v_fma_f32 v74,v74,v99,v99
v_mfma_f32_16x16x32_bf16 v[24:27],a[176:179],v[64:67],v[24:27]
s_cmp_ge_u32 s66,s59
ds_read_b128 v[60:63],v2 offset:29696
v_mul_f32 v33,v33,v73
v_mfma_f32_16x16x32_bf16 v[28:31],a[180:183],v[64:67],v[28:31]
v_exp_f32 v75,v75
v_mfma_f32_16x16x32_bf16 v[24:27],a[184:187],v[68:71],v[24:27]
buffer_load_dwordx4 v0,s[36:39],s67 offen offset:3072 sc1 lds
v_mfma_f32_16x16x32_bf16 v[28:31],a[188:191],v[68:71],v[28:31]
s_waitcnt lgkmcnt(2)
ds_read_b128 v[64:67],v2 offset:30720
v_fma_f32 v75,v75,v99,v99
v_mfma_f32_16x16x32_bf16 v[24:27],a[192:195],v[40:43],v[24:27]
s_waitcnt vmcnt(9)
ds_read_b128 v[68:71],v2 offset:31744
v_cvt_pk_bf16_f32 v80,v32,v33
v_mfma_f32_16x16x32_bf16 v[28:31],a[196:199],v[40:43],v[28:31]
s_barrier
v_mfma_f32_16x16x32_bf16 v[24:27],a[200:203],v[44:47],v[24:27]
ds_read_b128 v[40:43],v2 offset:32768
ds_read_b64 v[92:93],v11 offset:0
v_mfma_f32_16x16x32_bf16 v[28:31],a[204:207],v[44:47],v[28:31]
v_fma_f32 v36,-v96,v20,v36
v_fma_f32 v37,-v96,v21,v37
v_fma_f32 v38,-v96,v22,v38
v_fma_f32 v39,-v96,v23,v39
v_mul_f32 v76,v36,v36
v_mul_f32 v77,v37,v37
v_mul_f32 v78,v38,v38
v_mul_f32 v79,v39,v39
v_fma_f32 v76,v98,v76,v97
v_fma_f32 v77,v98,v77,v97
v_fma_f32 v78,v98,v78,v97
v_fma_f32 v79,v98,v79,v97
v_mul_f32 v76,v36,v76
v_mul_f32 v77,v37,v77
v_mul_f32 v78,v38,v78
v_mul_f32 v79,v39,v79
v_exp_f32 v76,v76
v_exp_f32 v77,v77
v_exp_f32 v78,v78
v_exp_f32 v79,v79
v_mfma_f32_16x16x32_bf16 v[24:27],a[208:211],v[48:51],v[24:27]
v_rcp_f32 v74,v74
v_rcp_f32 v75,v75
v_fma_f32 v76,v76,v99,v99
v_fma_f32 v77,v77,v99,v99
v_fma_f32 v78,v78,v99,v99
v_fma_f32 v79,v79,v99,v99
v_rcp_f32 v76,v76
v_rcp_f32 v77,v77
v_rcp_f32 v78,v78
v_rcp_f32 v79,v79
v_mfma_f32_16x16x32_bf16 v[28:31],a[212:215],v[48:51],v[28:31]
ds_read_b128 v[44:47],v2 offset:33792
v_mul_f32 v34,v34,v74
v_mul_f32 v35,v35,v75
v_cvt_pk_bf16_f32 v81,v34,v35
v_mul_f32 v36,v36,v76
v_mul_f32 v37,v37,v77
v_mul_f32 v38,v38,v78
v_mul_f32 v39,v39,v79
v_cvt_pk_bf16_f32 v82,v36,v37
v_cvt_pk_bf16_f32 v83,v38,v39
v_mfma_f32_16x16x32_bf16 v[24:27],a[216:219],v[52:55],v[24:27]
s_waitcnt lgkmcnt(1)
buffer_store_dwordx4 v[80:83],v3,s[36:39],s73 offen offset:0 sc0 sc1
ds_read_b128 v[48:51],v2 offset:34816
v_mfma_f32_16x16x32_bf16 v[28:31],a[220:223],v[52:55],v[28:31]
v_mfma_f32_16x16x32_bf16 v[24:27],a[224:227],v[56:59],v[24:27]
ds_read_b128 v[52:55],v2 offset:35840
v_mfma_f32_16x16x32_bf16 v[28:31],a[228:231],v[56:59],v[28:31]
v_mfma_f32_16x16x32_bf16 v[24:27],a[232:235],v[60:63],v[24:27]
ds_read_b128 v[56:59],v2 offset:36864
v_mfma_f32_16x16x32_bf16 v[28:31],a[236:239],v[60:63],v[28:31]
v_mfma_f32_16x16x32_bf16 v[24:27],a[240:243],v[64:67],v[24:27]
ds_read_b128 v[60:63],v2 offset:37888
v_mfma_f32_16x16x32_bf16 v[28:31],a[244:247],v[64:67],v[28:31]
v_mfma_f32_16x16x32_bf16 v[24:27],a[248:251],v[68:71],v[24:27]
s_waitcnt lgkmcnt(2)
ds_read_b128 v[64:67],v2 offset:38912
v_mfma_f32_16x16x32_bf16 v[28:31],a[252:255],v[68:71],v[28:31]
s_cbranch_scc1 Lg3o_slow1_%=
Lg3o_d1w2_%=:
v_mfma_f32_16x16x32_bf16 v[32:35],a[0:3],v[40:43],0
s_add_u32 m0,s62,16384
ds_read_b128 v[68:71],v2 offset:39936
v_mul_f32 v96,s75,v92
v_mfma_f32_16x16x32_bf16 v[36:39],a[4:7],v[40:43],0
s_mul_i32 s73,s48,7
v_fma_f32 v93,v93,s75,v12
v_fma_f32 v93,-v96,v96,v93
v_mfma_f32_16x16x32_bf16 v[32:35],a[8:11],v[44:47],v[32:35]
s_lshr_b32 s73,s73,1
ds_read_b128 v[40:43],v2 offset:40960
v_max_f32 v93,v93,v12
v_mfma_f32_16x16x32_bf16 v[36:39],a[12:15],v[44:47],v[36:39]
buffer_load_dwordx4 v1,s[36:39],s67 offen offset:0 sc1 lds
v_mfma_f32_16x16x32_bf16 v[32:35],a[16:19],v[48:51],v[32:35]
s_lshr_b32 s72,s67,5
ds_read_b128 v[44:47],v2 offset:41984
v_fma_f32 v24,-v96,v16,v24
v_mfma_f32_16x16x32_bf16 v[36:39],a[20:23],v[48:51],v[36:39]
s_lshr_b32 s71,s62,2
v_rsq_f32 v92,v93
v_mfma_f32_16x16x32_bf16 v[32:35],a[24:27],v[52:55],v[32:35]
s_mov_b64 s[44:45],s[46:47]
ds_read_b128 v[48:51],v2 offset:43008
v_mul_f32 v97,s76,v92
v_mfma_f32_16x16x32_bf16 v[36:39],a[28:31],v[52:55],v[36:39]
s_waitcnt lgkmcnt(3)
buffer_load_dwordx4 v1,s[36:39],s67 offen offset:1024 sc1 lds
v_mfma_f32_16x16x32_bf16 v[32:35],a[32:35],v[56:59],v[32:35]
s_mov_b64 s[46:47],s[48:49]
ds_read_b128 v[52:55],v2 offset:44032
v_mul_f32 v98,v92,v92
v_mfma_f32_16x16x32_bf16 v[36:39],a[36:39],v[56:59],v[36:39]
s_mov_b64 s[48:49],s[50:51]
v_mul_f32 v99,v93,v92
v_mul_f32 v98,s77,v98
v_mfma_f32_16x16x32_bf16 v[32:35],a[40:43],v[60:63],v[32:35]
ds_read_b128 v[56:59],v2 offset:45056
v_mul_f32 v98,v98,v97
v_mfma_f32_16x16x32_bf16 v[36:39],a[44:47],v[60:63],v[36:39]
buffer_load_dwordx4 v1,s[36:39],s67 offen offset:2048 sc1 lds
v_mfma_f32_16x16x32_bf16 v[32:35],a[48:51],v[64:67],v[32:35]
ds_read_b128 v[60:63],v2 offset:46080
v_fma_f32 v25,-v96,v17,v25
v_mfma_f32_16x16x32_bf16 v[36:39],a[52:55],v[64:67],v[36:39]
v_fma_f32 v26,-v96,v18,v26
v_fma_f32 v27,-v96,v19,v27
v_mfma_f32_16x16x32_bf16 v[32:35],a[56:59],v[68:71],v[32:35]
ds_read_b128 v[64:67],v2 offset:47104
v_mul_f32 v72,v24,v24
v_mfma_f32_16x16x32_bf16 v[36:39],a[60:63],v[68:71],v[36:39]
s_waitcnt lgkmcnt(3)
buffer_load_dwordx4 v1,s[36:39],s67 offen offset:3072 sc1 lds
s_add_u32 m0,s71,s88
v_mfma_f32_16x16x32_bf16 v[32:35],a[64:67],v[40:43],v[32:35]
s_xor_b32 s87,s63,65536
ds_read_b128 v[68:71],v2 offset:48128
buffer_load_dword v9,s[36:39],s72 offen offset:0 sc1 lds
s_waitcnt vmcnt(10)
v_mfma_f32_16x16x32_bf16 v[36:39],a[68:71],v[40:43],v[36:39]
s_barrier
v_mfma_f32_16x16x32_bf16 v[32:35],a[72:75],v[44:47],v[32:35]
s_cmp_ge_u32 s67,s59
ds_read_b128 v[40:43],v2 offset:49152
ds_read_b32 v88,v8 offset:8192
v_mfma_f32_16x16x32_bf16 v[36:39],a[76:79],v[44:47],v[36:39]
s_cselect_b32 s86,s50,s51
v_mul_f32 v73,v25,v25
v_mul_f32 v74,v26,v26
v_mfma_f32_16x16x32_bf16 v[32:35],a[80:83],v[48:51],v[32:35]
s_cselect_b32 s87,s87,s64
ds_read_b128 v[44:47],v2 offset:50176
v_mul_f32 v75,v27,v27
v_mfma_f32_16x16x32_bf16 v[36:39],a[84:87],v[48:51],v[36:39]
s_mov_b32 s62,s87
v_fma_f32 v72,v98,v72,v97
v_mul_f32 v72,v24,v72
v_mfma_f32_16x16x32_bf16 v[32:35],a[88:91],v[52:55],v[32:35]
s_mov_b32 m0,s62
ds_read_b128 v[48:51],v2 offset:51200
v_fma_f32 v73,v98,v73,v97
v_mfma_f32_16x16x32_bf16 v[36:39],a[92:95],v[52:55],v[36:39]
s_waitcnt lgkmcnt(4)
v_exp_f32 v72,v72
v_mfma_f32_16x16x32_bf16 v[32:35],a[96:99],v[56:59],v[32:35]
s_nop 0
s_waitcnt lgkmcnt(2)
ds_read_b128 v[52:55],v2 offset:52224
v_mul_f32 v73,v25,v73)"
R"(
v_mfma_f32_16x16x32_bf16 v[36:39],a[100:103],v[56:59],v[36:39]
s_barrier
v_mfma_f32_16x16x32_bf16 v[32:35],a[104:107],v[60:63],v[32:35]
s_mov_b64 s[50:51],s[52:53]
ds_read_b128 v[56:59],v2 offset:53248
ds_read_b32 v13,v7 offset:256
v_mfma_f32_16x16x32_bf16 v[36:39],a[108:111],v[60:63],v[36:39]
v_exp_f32 v73,v73
v_mfma_f32_16x16x32_bf16 v[32:35],a[112:115],v[64:67],v[32:35]
ds_read_b128 v[60:63],v2 offset:54272
v_fma_f32 v72,v72,v99,v99
v_mfma_f32_16x16x32_bf16 v[36:39],a[116:119],v[64:67],v[36:39]
v_rcp_f32 v72,v72
v_mfma_f32_16x16x32_bf16 v[32:35],a[120:123],v[68:71],v[32:35]
s_waitcnt lgkmcnt(1)
ds_read_b128 v[64:67],v2 offset:55296
v_fma_f32 v73,v73,v99,v99
v_mfma_f32_16x16x32_bf16 v[36:39],a[124:127],v[68:71],v[36:39]
v_cmp_eq_u32 vcc,s60,v13
v_rcp_f32 v73,v73
v_add_f32_dpp v88,v88,v88 quad_perm:[1,0,3,2] row_mask:0xf bank_mask:0xf
v_mfma_f32_16x16x32_bf16 v[32:35],a[128:131],v[40:43],v[32:35]
s_cmp_eq_u32 vcc_lo,-1
ds_read_b128 v[68:71],v2 offset:56320
v_add_f32_dpp v88,v88,v88 quad_perm:[2,3,0,1] row_mask:0xf bank_mask:0xf
v_mfma_f32_16x16x32_bf16 v[36:39],a[132:135],v[40:43],v[36:39]
s_cselect_b32 s66,s86,s59
v_add_f32_dpp v88,v88,v88 row_half_mirror row_mask:0xf bank_mask:0xf
v_fma_f32 v74,v98,v74,v97
v_mfma_f32_16x16x32_bf16 v[32:35],a[136:139],v[44:47],v[32:35]
buffer_load_dwordx4 v0,s[36:39],s66 offen offset:0 sc1 lds
v_mfma_f32_16x16x32_bf16 v[36:39],a[140:143],v[44:47],v[36:39]
s_xor_b32 s64,s64,65536
ds_read_b128 v[40:43],v2 offset:57344
v_mul_f32 v74,v26,v74
v_mfma_f32_16x16x32_bf16 v[32:35],a[144:147],v[48:51],v[32:35]
s_add_u32 s74,s53,s55
ds_write_b32 v10,v88 offset:128
v_mfma_f32_16x16x32_bf16 v[36:39],a[148:151],v[48:51],v[36:39]
s_cmp_eq_u32 s74,s56
ds_read_b128 v[44:47],v2 offset:58368
v_fma_f32 v75,v98,v75,v97
v_mfma_f32_16x16x32_bf16 v[32:35],a[152:155],v[52:55],v[32:35]
buffer_load_dwordx4 v0,s[36:39],s66 offen offset:1024 sc1 lds
v_mfma_f32_16x16x32_bf16 v[36:39],a[156:159],v[52:55],v[36:39]
s_waitcnt lgkmcnt(3)
ds_read_b128 v[48:51],v2 offset:59392
v_mul_f32 v75,v27,v75
v_mfma_f32_16x16x32_bf16 v[32:35],a[160:163],v[56:59],v[32:35]
s_cselect_b32 s74,s57,s74
ds_read_b128 v[52:55],v2 offset:60416
v_mul_f32 v24,v24,v72
v_mfma_f32_16x16x32_bf16 v[36:39],a[164:167],v[56:59],v[36:39]
s_cmp_eq_u32 s74,s58
v_exp_f32 v74,v74
v_mfma_f32_16x16x32_bf16 v[32:35],a[168:171],v[60:63],v[32:35]
buffer_load_dwordx4 v0,s[36:39],s66 offen offset:2048 sc1 lds
v_mfma_f32_16x16x32_bf16 v[36:39],a[172:175],v[60:63],v[36:39]
s_cselect_b32 s74,s59,s74
ds_read_b128 v[56:59],v2 offset:61440
v_fma_f32 v74,v74,v99,v99
v_mfma_f32_16x16x32_bf16 v[32:35],a[176:179],v[64:67],v[32:35]
s_mov_b32 s52,s74
ds_read_b128 v[60:63],v2 offset:62464
v_mul_f32 v25,v25,v73
v_mfma_f32_16x16x32_bf16 v[36:39],a[180:183],v[64:67],v[36:39]
s_cmp_lt_u32 s67,s59
v_exp_f32 v75,v75
v_mfma_f32_16x16x32_bf16 v[32:35],a[184:187],v[68:71],v[32:35]
buffer_load_dwordx4 v0,s[36:39],s66 offen offset:3072 sc1 lds
v_mfma_f32_16x16x32_bf16 v[36:39],a[188:191],v[68:71],v[36:39]
s_waitcnt lgkmcnt(2)
ds_read_b128 v[64:67],v2 offset:63488
v_fma_f32 v75,v75,v99,v99
v_mfma_f32_16x16x32_bf16 v[32:35],a[192:195],v[40:43],v[32:35]
s_waitcnt vmcnt(9)
ds_read_b128 v[68:71],v2 offset:64512
v_xor_b32 v2,65536,v2
v_mfma_f32_16x16x32_bf16 v[36:39],a[196:199],v[40:43],v[36:39]
s_barrier
v_mfma_f32_16x16x32_bf16 v[32:35],a[200:203],v[44:47],v[32:35]
ds_read_b128 v[40:43],v2 offset:0
ds_read_b64 v[92:93],v11 offset:128
v_mfma_f32_16x16x32_bf16 v[36:39],a[204:207],v[44:47],v[36:39]
v_fma_f32 v28,-v96,v20,v28
v_fma_f32 v29,-v96,v21,v29
v_fma_f32 v30,-v96,v22,v30
v_fma_f32 v31,-v96,v23,v31
v_mul_f32 v76,v28,v28
v_mul_f32 v77,v29,v29
v_mul_f32 v78,v30,v30
v_mul_f32 v79,v31,v31
v_fma_f32 v76,v98,v76,v97
v_fma_f32 v77,v98,v77,v97
v_fma_f32 v78,v98,v78,v97
v_fma_f32 v79,v98,v79,v97
v_mul_f32 v76,v28,v76
v_mul_f32 v77,v29,v77
v_mul_f32 v78,v30,v78
v_mul_f32 v79,v31,v79
v_exp_f32 v76,v76
v_exp_f32 v77,v77
v_exp_f32 v78,v78
v_exp_f32 v79,v79
v_mfma_f32_16x16x32_bf16 v[32:35],a[208:211],v[48:51],v[32:35]
v_rcp_f32 v74,v74
v_rcp_f32 v75,v75
v_fma_f32 v76,v76,v99,v99
v_fma_f32 v77,v77,v99,v99
v_fma_f32 v78,v78,v99,v99
v_fma_f32 v79,v79,v99,v99
v_rcp_f32 v76,v76
v_rcp_f32 v77,v77
v_rcp_f32 v78,v78
v_rcp_f32 v79,v79
v_mfma_f32_16x16x32_bf16 v[36:39],a[212:215],v[48:51],v[36:39]
ds_read_b128 v[44:47],v2 offset:1024
v_mul_f32 v26,v26,v74
v_mul_f32 v27,v27,v75
v_cvt_pk_bf16_f32 v80,v24,v25
v_cvt_pk_bf16_f32 v81,v26,v27
v_mul_f32 v28,v28,v76
v_mul_f32 v29,v29,v77
v_mul_f32 v30,v30,v78
v_mul_f32 v31,v31,v79
v_cvt_pk_bf16_f32 v82,v28,v29
v_cvt_pk_bf16_f32 v83,v30,v31
v_mfma_f32_16x16x32_bf16 v[32:35],a[216:219],v[52:55],v[32:35]
s_waitcnt lgkmcnt(1)
buffer_store_dwordx4 v[80:83],v3,s[36:39],s73 offen offset:0 sc0 sc1
ds_read_b128 v[48:51],v2 offset:2048
v_xor_b32 v8,16384,v8
v_mfma_f32_16x16x32_bf16 v[36:39],a[220:223],v[52:55],v[36:39]
v_mfma_f32_16x16x32_bf16 v[32:35],a[224:227],v[56:59],v[32:35]
ds_read_b128 v[52:55],v2 offset:3072
v_mfma_f32_16x16x32_bf16 v[36:39],a[228:231],v[56:59],v[36:39]
v_mfma_f32_16x16x32_bf16 v[32:35],a[232:235],v[60:63],v[32:35]
ds_read_b128 v[56:59],v2 offset:4096
v_mfma_f32_16x16x32_bf16 v[36:39],a[236:239],v[60:63],v[36:39]
v_mfma_f32_16x16x32_bf16 v[32:35],a[240:243],v[64:67],v[32:35]
ds_read_b128 v[60:63],v2 offset:5120
v_mfma_f32_16x16x32_bf16 v[36:39],a[244:247],v[64:67],v[36:39]
v_mfma_f32_16x16x32_bf16 v[32:35],a[248:251],v[68:71],v[32:35]
s_waitcnt lgkmcnt(2)
ds_read_b128 v[64:67],v2 offset:6144
v_mfma_f32_16x16x32_bf16 v[36:39],a[252:255],v[68:71],v[36:39]
s_cbranch_scc1 Lg3o_d0w2_%=
s_branch Lg3o_slow0_%=
Lg3o_d0w3_%=:
v_mfma_f32_16x16x32_bf16 v[24:27],a[0:3],v[40:43],0
s_add_u32 m0,s62,16384
ds_read_b128 v[68:71],v2 offset:7168
v_mul_f32 v96,s75,v92
v_mfma_f32_16x16x32_bf16 v[28:31],a[4:7],v[40:43],0
s_mul_i32 s73,s47,7
v_fma_f32 v93,v93,s75,v12
v_fma_f32 v93,-v96,v96,v93
v_mfma_f32_16x16x32_bf16 v[24:27],a[8:11],v[44:47],v[24:27]
s_lshr_b32 s73,s73,1
ds_read_b128 v[40:43],v2 offset:8192
v_max_f32 v93,v93,v12
v_mfma_f32_16x16x32_bf16 v[28:31],a[12:15],v[44:47],v[28:31]
s_lshr_b32 s72,s66,5
v_rsq_f32 v92,v93
v_mfma_f32_16x16x32_bf16 v[24:27],a[16:19],v[48:51],v[24:27]
buffer_load_dwordx4 v1,s[36:39],s66 offen offset:0 sc1 lds
v_mfma_f32_16x16x32_bf16 v[28:31],a[20:23],v[48:51],v[28:31]
s_lshr_b32 s71,s62,2
ds_read_b128 v[44:47],v2 offset:9216
v_mul_f32 v97,s76,v92
v_mfma_f32_16x16x32_bf16 v[24:27],a[24:27],v[52:55],v[24:27]
s_mul_i32 s70,s46,5
ds_read_b128 v[48:51],v2 offset:10240
v_mul_f32 v98,v92,v92
v_mfma_f32_16x16x32_bf16 v[28:31],a[28:31],v[52:55],v[28:31]
s_waitcnt lgkmcnt(3)
v_mul_f32 v99,v93,v92
v_mul_f32 v98,s77,v98
v_mfma_f32_16x16x32_bf16 v[24:27],a[32:35],v[56:59],v[24:27]
buffer_load_dwordx4 v1,s[36:39],s66 offen offset:1024 sc1 lds
v_mfma_f32_16x16x32_bf16 v[28:31],a[36:39],v[56:59],v[28:31]
ds_read_b128 v[52:55],v2 offset:11264
v_mul_f32 v98,v98,v97
v_mfma_f32_16x16x32_bf16 v[24:27],a[40:43],v[60:63],v[24:27]
ds_read_b128 v[56:59],v2 offset:12288
v_fma_f32 v32,-v96,v16,v32
v_mfma_f32_16x16x32_bf16 v[28:31],a[44:47],v[60:63],v[28:31]
v_fma_f32 v33,-v96,v17,v33
v_fma_f32 v34,-v96,v18,v34
v_mfma_f32_16x16x32_bf16 v[24:27],a[48:51],v[64:67],v[24:27]
buffer_load_dwordx4 v1,s[36:39],s66 offen offset:2048 sc1 lds
v_mfma_f32_16x16x32_bf16 v[28:31],a[52:55],v[64:67],v[28:31]
ds_read_b128 v[60:63],v2 offset:13312
v_fma_f32 v35,-v96,v19,v35
v_mfma_f32_16x16x32_bf16 v[24:27],a[56:59],v[68:71],v[24:27]
ds_read_b128 v[64:67],v2 offset:14336
v_mul_f32 v72,v32,v32
v_mfma_f32_16x16x32_bf16 v[28:31],a[60:63],v[68:71],v[28:31]
s_waitcnt lgkmcnt(3)
v_mul_f32 v73,v33,v33)"
R"(
v_mul_f32 v74,v34,v34
v_mfma_f32_16x16x32_bf16 v[24:27],a[64:67],v[40:43],v[24:27]
s_waitcnt vmcnt(9)
ds_read_b128 v[68:71],v2 offset:15360
v_mul_f32 v75,v35,v35
v_mfma_f32_16x16x32_bf16 v[28:31],a[68:71],v[40:43],v[28:31]
s_barrier
v_mfma_f32_16x16x32_bf16 v[24:27],a[72:75],v[44:47],v[24:27]
buffer_load_dwordx4 v1,s[36:39],s66 offen offset:3072 sc1 lds
v_mfma_f32_16x16x32_bf16 v[28:31],a[76:79],v[44:47],v[28:31]
s_add_u32 m0,s71,s88
ds_read_b128 v[40:43],v2 offset:16384
ds_read_b32 v88,v8 offset:0
v_mfma_f32_16x16x32_bf16 v[24:27],a[80:83],v[48:51],v[24:27]
buffer_load_dword v9,s[36:39],s72 offen offset:0 sc1 lds
v_mfma_f32_16x16x32_bf16 v[28:31],a[84:87],v[48:51],v[28:31]
s_xor_b32 s87,s64,65536
ds_read_b128 v[44:47],v2 offset:17408
v_fma_f32 v72,v98,v72,v97
v_mfma_f32_16x16x32_bf16 v[24:27],a[88:91],v[52:55],v[24:27]
s_cmp_ge_u32 s66,s59
ds_read_b128 v[48:51],v2 offset:18432
v_mul_f32 v72,v32,v72
v_mfma_f32_16x16x32_bf16 v[28:31],a[92:95],v[52:55],v[28:31]
s_waitcnt lgkmcnt(4)
v_exp_f32 v72,v72
v_mfma_f32_16x16x32_bf16 v[24:27],a[96:99],v[56:59],v[24:27]
s_nop 0
ds_read_b128 v[52:55],v2 offset:19456
v_fma_f32 v72,v72,v99,v99
v_mfma_f32_16x16x32_bf16 v[28:31],a[100:103],v[56:59],v[28:31]
s_barrier
v_mfma_f32_16x16x32_bf16 v[24:27],a[104:107],v[60:63],v[24:27]
s_waitcnt lgkmcnt(3)
ds_read_b128 v[56:59],v2 offset:20480
ds_read_b32 v13,v7 offset:0
v_mfma_f32_16x16x32_bf16 v[28:31],a[108:111],v[60:63],v[28:31]
s_cselect_b32 s86,s49,s50
v_rcp_f32 v72,v72
v_mfma_f32_16x16x32_bf16 v[24:27],a[112:115],v[64:67],v[24:27]
s_cselect_b32 s87,s87,s63
ds_read_b128 v[60:63],v2 offset:21504
v_add_f32_dpp v88,v88,v88 quad_perm:[1,0,3,2] row_mask:0xf bank_mask:0xf
v_mfma_f32_16x16x32_bf16 v[28:31],a[116:119],v[64:67],v[28:31]
s_mov_b32 s62,s87
v_add_f32_dpp v88,v88,v88 quad_perm:[2,3,0,1] row_mask:0xf bank_mask:0xf
v_fma_f32 v73,v98,v73,v97
v_mfma_f32_16x16x32_bf16 v[24:27],a[120:123],v[68:71],v[24:27]
s_waitcnt lgkmcnt(1)
ds_read_b128 v[64:67],v2 offset:22528
v_mul_f32 v73,v33,v73
v_mfma_f32_16x16x32_bf16 v[28:31],a[124:127],v[68:71],v[28:31]
v_cmp_eq_u32 vcc,s60,v13
v_exp_f32 v73,v73
v_add_f32_dpp v88,v88,v88 row_half_mirror row_mask:0xf bank_mask:0xf
v_mfma_f32_16x16x32_bf16 v[24:27],a[128:131],v[40:43],v[24:27]
s_cmp_eq_u32 vcc_lo,-1
ds_write_b32 v10,v88 offset:0
v_mfma_f32_16x16x32_bf16 v[28:31],a[132:135],v[40:43],v[28:31]
s_cselect_b32 s67,s86,s59
ds_read_b128 v[68:71],v2 offset:23552
v_fma_f32 v73,v73,v99,v99
v_mfma_f32_16x16x32_bf16 v[24:27],a[136:139],v[44:47],v[24:27]
s_mov_b32 m0,s62
ds_read_b128 v[40:43],v2 offset:24576
v_fma_f32 v74,v98,v74,v97
v_mfma_f32_16x16x32_bf16 v[28:31],a[140:143],v[44:47],v[28:31]
buffer_load_dwordx4 v0,s[36:39],s67 offen offset:0 sc1 lds
v_mfma_f32_16x16x32_bf16 v[24:27],a[144:147],v[48:51],v[24:27]
s_xor_b32 s63,s63,65536
ds_read_b128 v[44:47],v2 offset:25600
v_mul_f32 v74,v34,v74
v_mfma_f32_16x16x32_bf16 v[28:31],a[148:151],v[48:51],v[28:31]
s_add_u32 s53,s52,s55
v_exp_f32 v74,v74
v_mfma_f32_16x16x32_bf16 v[24:27],a[152:155],v[52:55],v[24:27]
s_cmp_eq_u32 s53,s56
ds_read_b128 v[48:51],v2 offset:26624
v_fma_f32 v74,v74,v99,v99
v_mfma_f32_16x16x32_bf16 v[28:31],a[156:159],v[52:55],v[28:31]
s_waitcnt lgkmcnt(3)
buffer_load_dwordx4 v0,s[36:39],s67 offen offset:1024 sc1 lds
v_mfma_f32_16x16x32_bf16 v[24:27],a[160:163],v[56:59],v[24:27]
s_cselect_b32 s53,s57,s53
ds_read_b128 v[52:55],v2 offset:27648
v_fma_f32 v75,v98,v75,v97
v_mfma_f32_16x16x32_bf16 v[28:31],a[164:167],v[56:59],v[28:31]
s_cmp_eq_u32 s53,s58
v_rcp_f32 v73,v73
v_mfma_f32_16x16x32_bf16 v[24:27],a[168:171],v[60:63],v[24:27]
s_cselect_b32 s53,s59,s53
ds_read_b128 v[56:59],v2 offset:28672
v_mul_f32 v75,v35,v75
v_mfma_f32_16x16x32_bf16 v[28:31],a[172:175],v[60:63],v[28:31]
buffer_load_dwordx4 v0,s[36:39],s67 offen offset:2048 sc1 lds
v_mfma_f32_16x16x32_bf16 v[24:27],a[176:179],v[64:67],v[24:27]
s_lshr_b32 s70,s70,6
ds_read_b128 v[60:63],v2 offset:29696
v_mul_f32 v32,v32,v72
v_mfma_f32_16x16x32_bf16 v[28:31],a[180:183],v[64:67],v[28:31]
s_cmp_ge_u32 s66,s59
v_exp_f32 v75,v75
v_mfma_f32_16x16x32_bf16 v[24:27],a[184:187],v[68:71],v[24:27]
ds_read_b128 v[64:67],v2 offset:30720
v_fma_f32 v75,v75,v99,v99
v_mfma_f32_16x16x32_bf16 v[28:31],a[188:191],v[68:71],v[28:31]
s_waitcnt lgkmcnt(3)
buffer_load_dwordx4 v0,s[36:39],s67 offen offset:3072 sc1 lds
v_mfma_f32_16x16x32_bf16 v[24:27],a[192:195],v[40:43],v[24:27]
s_waitcnt vmcnt(9)
ds_read_b128 v[68:71],v2 offset:31744
v_mul_f32 v33,v33,v73
v_mfma_f32_16x16x32_bf16 v[28:31],a[196:199],v[40:43],v[28:31]
s_barrier
v_mfma_f32_16x16x32_bf16 v[24:27],a[200:203],v[44:47],v[24:27]
buffer_store_dword v5,v4,s[36:39],s70 offen offset:0 sc0 sc1
ds_read_b128 v[40:43],v2 offset:32768
ds_read_b64 v[92:93],v11 offset:0
v_cvt_pk_bf16_f32 v80,v32,v33
v_mfma_f32_16x16x32_bf16 v[28:31],a[204:207],v[44:47],v[28:31]
v_rcp_f32 v74,v74
v_mfma_f32_16x16x32_bf16 v[24:27],a[208:211],v[48:51],v[24:27]
v_fma_f32 v36,-v96,v20,v36
v_fma_f32 v37,-v96,v21,v37
v_fma_f32 v38,-v96,v22,v38
v_fma_f32 v39,-v96,v23,v39
v_mul_f32 v76,v36,v36
v_mul_f32 v77,v37,v37
v_mul_f32 v78,v38,v38
v_mul_f32 v79,v39,v39
v_fma_f32 v76,v98,v76,v97
v_fma_f32 v77,v98,v77,v97
v_fma_f32 v78,v98,v78,v97
v_fma_f32 v79,v98,v79,v97
v_mul_f32 v76,v36,v76
v_mul_f32 v77,v37,v77
v_mul_f32 v78,v38,v78
v_mul_f32 v79,v39,v79
v_exp_f32 v76,v76
v_exp_f32 v77,v77
v_exp_f32 v78,v78
v_exp_f32 v79,v79
v_mfma_f32_16x16x32_bf16 v[28:31],a[212:215],v[48:51],v[28:31]
ds_read_b128 v[44:47],v2 offset:33792
v_rcp_f32 v75,v75
v_fma_f32 v76,v76,v99,v99
v_fma_f32 v77,v77,v99,v99
v_fma_f32 v78,v78,v99,v99
v_fma_f32 v79,v79,v99,v99
v_rcp_f32 v76,v76
v_rcp_f32 v77,v77
v_rcp_f32 v78,v78
v_rcp_f32 v79,v79
v_mfma_f32_16x16x32_bf16 v[24:27],a[216:219],v[52:55],v[24:27]
s_waitcnt lgkmcnt(1)
v_mul_f32 v34,v34,v74
v_mul_f32 v35,v35,v75
v_cvt_pk_bf16_f32 v81,v34,v35
v_mul_f32 v36,v36,v76
v_mul_f32 v37,v37,v77
v_mul_f32 v38,v38,v78
v_mul_f32 v39,v39,v79
v_cvt_pk_bf16_f32 v82,v36,v37
v_cvt_pk_bf16_f32 v83,v38,v39
v_mfma_f32_16x16x32_bf16 v[28:31],a[220:223],v[52:55],v[28:31]
ds_read_b128 v[48:51],v2 offset:34816
buffer_store_dwordx4 v[80:83],v3,s[36:39],s73 offen offset:0 sc0 sc1
v_mfma_f32_16x16x32_bf16 v[24:27],a[224:227],v[56:59],v[24:27]
ds_read_b128 v[52:55],v2 offset:35840
v_mfma_f32_16x16x32_bf16 v[28:31],a[228:231],v[56:59],v[28:31]
v_mfma_f32_16x16x32_bf16 v[24:27],a[232:235],v[60:63],v[24:27]
ds_read_b128 v[56:59],v2 offset:36864
v_mfma_f32_16x16x32_bf16 v[28:31],a[236:239],v[60:63],v[28:31]
v_mfma_f32_16x16x32_bf16 v[24:27],a[240:243],v[64:67],v[24:27]
ds_read_b128 v[60:63],v2 offset:37888
v_mfma_f32_16x16x32_bf16 v[28:31],a[244:247],v[64:67],v[28:31]
v_mfma_f32_16x16x32_bf16 v[24:27],a[248:251],v[68:71],v[24:27]
s_waitcnt lgkmcnt(2)
ds_read_b128 v[64:67],v2 offset:38912
v_mfma_f32_16x16x32_bf16 v[28:31],a[252:255],v[68:71],v[28:31]
s_cbranch_scc1 Lg3o_slow1_%=
Lg3o_d1w3_%=:
v_mfma_f32_16x16x32_bf16 v[32:35],a[0:3],v[40:43],0
s_add_u32 m0,s62,16384
ds_read_b128 v[68:71],v2 offset:39936
v_mul_f32 v96,s75,v92
v_mfma_f32_16x16x32_bf16 v[36:39],a[4:7],v[40:43],0
s_mul_i32 s73,s48,7
v_fma_f32 v93,v93,s75,v12
v_fma_f32 v93,-v96,v96,v93
v_mfma_f32_16x16x32_bf16 v[32:35],a[8:11],v[44:47],v[32:35]
s_lshr_b32 s73,s73,1
ds_read_b128 v[40:43],v2 offset:40960
v_max_f32 v93,v93,v12
v_mfma_f32_16x16x32_bf16 v[36:39],a[12:15],v[44:47],v[36:39]
s_lshr_b32 s72,s67,5
v_rsq_f32 v92,v93
v_mfma_f32_16x16x32_bf16 v[32:35],a[16:19],v[48:51],v[32:35]
buffer_load_dwordx4 v1,s[36:39],s67 offen offset:0 sc1 lds
v_mfma_f32_16x16x32_bf16 v[36:39],a[20:23],v[48:51],v[36:39])"
R"(
s_lshr_b32 s71,s62,2
ds_read_b128 v[44:47],v2 offset:41984
v_mul_f32 v97,s76,v92
v_mfma_f32_16x16x32_bf16 v[32:35],a[24:27],v[52:55],v[32:35]
s_mul_i32 s70,s47,5
ds_read_b128 v[48:51],v2 offset:43008
v_mul_f32 v98,v92,v92
v_mfma_f32_16x16x32_bf16 v[36:39],a[28:31],v[52:55],v[36:39]
s_waitcnt lgkmcnt(3)
v_mul_f32 v99,v93,v92
v_mul_f32 v98,s77,v98
v_mfma_f32_16x16x32_bf16 v[32:35],a[32:35],v[56:59],v[32:35]
buffer_load_dwordx4 v1,s[36:39],s67 offen offset:1024 sc1 lds
v_mfma_f32_16x16x32_bf16 v[36:39],a[36:39],v[56:59],v[36:39]
s_mov_b64 s[44:45],s[46:47]
ds_read_b128 v[52:55],v2 offset:44032
v_mul_f32 v98,v98,v97
v_mfma_f32_16x16x32_bf16 v[32:35],a[40:43],v[60:63],v[32:35]
s_mov_b64 s[46:47],s[48:49]
ds_read_b128 v[56:59],v2 offset:45056
v_fma_f32 v24,-v96,v16,v24
v_mfma_f32_16x16x32_bf16 v[36:39],a[44:47],v[60:63],v[36:39]
s_mov_b64 s[48:49],s[50:51]
v_fma_f32 v25,-v96,v17,v25
v_fma_f32 v26,-v96,v18,v26
v_mfma_f32_16x16x32_bf16 v[32:35],a[48:51],v[64:67],v[32:35]
buffer_load_dwordx4 v1,s[36:39],s67 offen offset:2048 sc1 lds
v_mfma_f32_16x16x32_bf16 v[36:39],a[52:55],v[64:67],v[36:39]
ds_read_b128 v[60:63],v2 offset:46080
v_fma_f32 v27,-v96,v19,v27
v_mfma_f32_16x16x32_bf16 v[32:35],a[56:59],v[68:71],v[32:35]
ds_read_b128 v[64:67],v2 offset:47104
v_mul_f32 v72,v24,v24
v_mfma_f32_16x16x32_bf16 v[36:39],a[60:63],v[68:71],v[36:39]
s_waitcnt lgkmcnt(3)
v_mul_f32 v73,v25,v25
v_mul_f32 v74,v26,v26
v_mfma_f32_16x16x32_bf16 v[32:35],a[64:67],v[40:43],v[32:35]
s_waitcnt vmcnt(9)
ds_read_b128 v[68:71],v2 offset:48128
v_mul_f32 v75,v27,v27
v_mfma_f32_16x16x32_bf16 v[36:39],a[68:71],v[40:43],v[36:39]
s_barrier
v_mfma_f32_16x16x32_bf16 v[32:35],a[72:75],v[44:47],v[32:35]
buffer_load_dwordx4 v1,s[36:39],s67 offen offset:3072 sc1 lds
v_mfma_f32_16x16x32_bf16 v[36:39],a[76:79],v[44:47],v[36:39]
s_add_u32 m0,s71,s88
ds_read_b128 v[40:43],v2 offset:49152
ds_read_b32 v88,v8 offset:8192
v_mfma_f32_16x16x32_bf16 v[32:35],a[80:83],v[48:51],v[32:35]
buffer_load_dword v9,s[36:39],s72 offen offset:0 sc1 lds
v_mfma_f32_16x16x32_bf16 v[36:39],a[84:87],v[48:51],v[36:39]
s_xor_b32 s87,s63,65536
ds_read_b128 v[44:47],v2 offset:50176
v_fma_f32 v72,v98,v72,v97
v_mfma_f32_16x16x32_bf16 v[32:35],a[88:91],v[52:55],v[32:35]
s_cmp_ge_u32 s67,s59
ds_read_b128 v[48:51],v2 offset:51200
v_mul_f32 v72,v24,v72
v_mfma_f32_16x16x32_bf16 v[36:39],a[92:95],v[52:55],v[36:39]
s_waitcnt lgkmcnt(4)
v_exp_f32 v72,v72
v_mfma_f32_16x16x32_bf16 v[32:35],a[96:99],v[56:59],v[32:35]
s_nop 0
ds_read_b128 v[52:55],v2 offset:52224
v_fma_f32 v72,v72,v99,v99
v_mfma_f32_16x16x32_bf16 v[36:39],a[100:103],v[56:59],v[36:39]
s_barrier
v_mfma_f32_16x16x32_bf16 v[32:35],a[104:107],v[60:63],v[32:35]
s_waitcnt lgkmcnt(3)
ds_read_b128 v[56:59],v2 offset:53248
ds_read_b32 v13,v7 offset:256
v_mfma_f32_16x16x32_bf16 v[36:39],a[108:111],v[60:63],v[36:39]
s_cselect_b32 s86,s50,s51
v_rcp_f32 v72,v72
v_mfma_f32_16x16x32_bf16 v[32:35],a[112:115],v[64:67],v[32:35]
s_cselect_b32 s87,s87,s64
ds_read_b128 v[60:63],v2 offset:54272
v_add_f32_dpp v88,v88,v88 quad_perm:[1,0,3,2] row_mask:0xf bank_mask:0xf
v_mfma_f32_16x16x32_bf16 v[36:39],a[116:119],v[64:67],v[36:39]
s_mov_b32 s62,s87
v_add_f32_dpp v88,v88,v88 quad_perm:[2,3,0,1] row_mask:0xf bank_mask:0xf
v_fma_f32 v73,v98,v73,v97
v_mfma_f32_16x16x32_bf16 v[32:35],a[120:123],v[68:71],v[32:35]
s_waitcnt lgkmcnt(1)
ds_read_b128 v[64:67],v2 offset:55296
v_mul_f32 v73,v25,v73
v_mfma_f32_16x16x32_bf16 v[36:39],a[124:127],v[68:71],v[36:39]
v_cmp_eq_u32 vcc,s60,v13
v_exp_f32 v73,v73
v_add_f32_dpp v88,v88,v88 row_half_mirror row_mask:0xf bank_mask:0xf
v_mfma_f32_16x16x32_bf16 v[32:35],a[128:131],v[40:43],v[32:35]
s_cmp_eq_u32 vcc_lo,-1
ds_write_b32 v10,v88 offset:128
v_mfma_f32_16x16x32_bf16 v[36:39],a[132:135],v[40:43],v[36:39]
s_cselect_b32 s66,s86,s59
ds_read_b128 v[68:71],v2 offset:56320
v_fma_f32 v73,v73,v99,v99
v_mfma_f32_16x16x32_bf16 v[32:35],a[136:139],v[44:47],v[32:35]
s_mov_b32 m0,s62
ds_read_b128 v[40:43],v2 offset:57344
v_fma_f32 v74,v98,v74,v97
v_mfma_f32_16x16x32_bf16 v[36:39],a[140:143],v[44:47],v[36:39]
buffer_load_dwordx4 v0,s[36:39],s66 offen offset:0 sc1 lds
v_mfma_f32_16x16x32_bf16 v[32:35],a[144:147],v[48:51],v[32:35]
s_xor_b32 s64,s64,65536
ds_read_b128 v[44:47],v2 offset:58368
v_mul_f32 v74,v26,v74
v_mfma_f32_16x16x32_bf16 v[36:39],a[148:151],v[48:51],v[36:39]
s_add_u32 s74,s53,s55
v_exp_f32 v74,v74
v_mfma_f32_16x16x32_bf16 v[32:35],a[152:155],v[52:55],v[32:35]
s_cmp_eq_u32 s74,s56
ds_read_b128 v[48:51],v2 offset:59392
v_fma_f32 v74,v74,v99,v99
v_mfma_f32_16x16x32_bf16 v[36:39],a[156:159],v[52:55],v[36:39]
s_waitcnt lgkmcnt(3)
buffer_load_dwordx4 v0,s[36:39],s66 offen offset:1024 sc1 lds
v_mfma_f32_16x16x32_bf16 v[32:35],a[160:163],v[56:59],v[32:35]
s_cselect_b32 s74,s57,s74
ds_read_b128 v[52:55],v2 offset:60416
v_fma_f32 v75,v98,v75,v97
v_mfma_f32_16x16x32_bf16 v[36:39],a[164:167],v[56:59],v[36:39]
s_cmp_eq_u32 s74,s58
v_rcp_f32 v73,v73
v_mfma_f32_16x16x32_bf16 v[32:35],a[168:171],v[60:63],v[32:35]
s_cselect_b32 s74,s59,s74
ds_read_b128 v[56:59],v2 offset:61440
v_mul_f32 v75,v27,v75
v_mfma_f32_16x16x32_bf16 v[36:39],a[172:175],v[60:63],v[36:39]
buffer_load_dwordx4 v0,s[36:39],s66 offen offset:2048 sc1 lds
v_mfma_f32_16x16x32_bf16 v[32:35],a[176:179],v[64:67],v[32:35]
s_lshr_b32 s70,s70,6
ds_read_b128 v[60:63],v2 offset:62464
v_mul_f32 v24,v24,v72
v_mfma_f32_16x16x32_bf16 v[36:39],a[180:183],v[64:67],v[36:39]
s_mov_b64 s[50:51],s[52:53]
v_exp_f32 v75,v75
v_mfma_f32_16x16x32_bf16 v[32:35],a[184:187],v[68:71],v[32:35]
s_mov_b32 s52,s74
ds_read_b128 v[64:67],v2 offset:63488
v_fma_f32 v75,v75,v99,v99
v_mfma_f32_16x16x32_bf16 v[36:39],a[188:191],v[68:71],v[36:39]
s_waitcnt lgkmcnt(3)
buffer_load_dwordx4 v0,s[36:39],s66 offen offset:3072 sc1 lds
v_mfma_f32_16x16x32_bf16 v[32:35],a[192:195],v[40:43],v[32:35]
s_waitcnt vmcnt(9)
ds_read_b128 v[68:71],v2 offset:64512
v_xor_b32 v2,65536,v2
v_mfma_f32_16x16x32_bf16 v[36:39],a[196:199],v[40:43],v[36:39]
s_barrier
v_mfma_f32_16x16x32_bf16 v[32:35],a[200:203],v[44:47],v[32:35]
buffer_store_dword v5,v4,s[36:39],s70 offen offset:0 sc0 sc1
ds_read_b128 v[40:43],v2 offset:0
ds_read_b64 v[92:93],v11 offset:128
v_mul_f32 v25,v25,v73
v_mfma_f32_16x16x32_bf16 v[36:39],a[204:207],v[44:47],v[36:39]
s_cmp_lt_u32 s67,s59
v_rcp_f32 v74,v74
v_mfma_f32_16x16x32_bf16 v[32:35],a[208:211],v[48:51],v[32:35]
v_fma_f32 v28,-v96,v20,v28
v_fma_f32 v29,-v96,v21,v29
v_fma_f32 v30,-v96,v22,v30
v_fma_f32 v31,-v96,v23,v31
v_mul_f32 v76,v28,v28
v_mul_f32 v77,v29,v29
v_mul_f32 v78,v30,v30
v_mul_f32 v79,v31,v31
v_fma_f32 v76,v98,v76,v97
v_fma_f32 v77,v98,v77,v97
v_fma_f32 v78,v98,v78,v97
v_fma_f32 v79,v98,v79,v97
v_mul_f32 v76,v28,v76
v_mul_f32 v77,v29,v77
v_mul_f32 v78,v30,v78
v_mul_f32 v79,v31,v79
v_exp_f32 v76,v76
v_exp_f32 v77,v77
v_exp_f32 v78,v78
v_exp_f32 v79,v79
v_mfma_f32_16x16x32_bf16 v[36:39],a[212:215],v[48:51],v[36:39]
ds_read_b128 v[44:47],v2 offset:1024
v_rcp_f32 v75,v75
v_fma_f32 v76,v76,v99,v99
v_fma_f32 v77,v77,v99,v99
v_fma_f32 v78,v78,v99,v99
v_fma_f32 v79,v79,v99,v99
v_rcp_f32 v76,v76
v_rcp_f32 v77,v77
v_rcp_f32 v78,v78
v_rcp_f32 v79,v79
v_mfma_f32_16x16x32_bf16 v[32:35],a[216:219],v[52:55],v[32:35]
s_waitcnt lgkmcnt(1)
v_mul_f32 v26,v26,v74
v_mul_f32 v27,v27,v75
v_cvt_pk_bf16_f32 v80,v24,v25
v_cvt_pk_bf16_f32 v81,v26,v27
v_mul_f32 v28,v28,v76
v_mul_f32 v29,v29,v77
v_mul_f32 v30,v30,v78)"
R"(
v_mul_f32 v31,v31,v79
v_cvt_pk_bf16_f32 v82,v28,v29
v_cvt_pk_bf16_f32 v83,v30,v31
v_mfma_f32_16x16x32_bf16 v[36:39],a[220:223],v[52:55],v[36:39]
ds_read_b128 v[48:51],v2 offset:2048
buffer_store_dwordx4 v[80:83],v3,s[36:39],s73 offen offset:0 sc0 sc1
v_xor_b32 v8,16384,v8
v_mfma_f32_16x16x32_bf16 v[32:35],a[224:227],v[56:59],v[32:35]
ds_read_b128 v[52:55],v2 offset:3072
v_mfma_f32_16x16x32_bf16 v[36:39],a[228:231],v[56:59],v[36:39]
v_mfma_f32_16x16x32_bf16 v[32:35],a[232:235],v[60:63],v[32:35]
ds_read_b128 v[56:59],v2 offset:4096
v_mfma_f32_16x16x32_bf16 v[36:39],a[236:239],v[60:63],v[36:39]
v_mfma_f32_16x16x32_bf16 v[32:35],a[240:243],v[64:67],v[32:35]
ds_read_b128 v[60:63],v2 offset:5120
v_mfma_f32_16x16x32_bf16 v[36:39],a[244:247],v[64:67],v[36:39]
v_mfma_f32_16x16x32_bf16 v[32:35],a[248:251],v[68:71],v[32:35]
s_waitcnt lgkmcnt(2)
ds_read_b128 v[64:67],v2 offset:6144
v_mfma_f32_16x16x32_bf16 v[36:39],a[252:255],v[68:71],v[36:39]
s_cbranch_scc1 Lg3o_d0w3_%=
s_branch Lg3o_slow0_%=
Lg3o_slow0_%=:
s_cmp_lt_u32 s66,s59
s_cbranch_scc1 Lg3o_gs0_%=
Lg3o_slowb0_%=:
s_cmp_eq_u32 s54,1
s_cselect_b32 s68,s49,s48
s_lshr_b32 s68,s68,5
s_add_u32 m0,s78,8192
s_nop 0
buffer_load_dword v6,s[36:39],s68 offen offset:0 sc1 lds
s_mul_i32 s73,s47,7
s_lshr_b32 s73,s73,1
v_mul_f32 v96,s75,v92
v_fma_f32 v93,v93,s75,v12
v_fma_f32 v93,-v96,v96,v93
v_max_f32 v93,v93,v12
v_rsq_f32 v92,v93
s_nop 0
v_mul_f32 v97,s76,v92
v_mul_f32 v98,v92,v92
v_mul_f32 v99,v93,v92
v_mul_f32 v98,s77,v98
v_mul_f32 v98,v98,v97
v_fma_f32 v32,-v96,v16,v32
v_fma_f32 v33,-v96,v17,v33
v_fma_f32 v34,-v96,v18,v34
v_fma_f32 v35,-v96,v19,v35
v_mul_f32 v72,v32,v32
v_mul_f32 v73,v33,v33
v_mul_f32 v74,v34,v34
v_mul_f32 v75,v35,v35
v_fma_f32 v72,v98,v72,v97
v_fma_f32 v73,v98,v73,v97
v_fma_f32 v74,v98,v74,v97
v_fma_f32 v75,v98,v75,v97
v_mul_f32 v72,v32,v72
v_mul_f32 v73,v33,v73
v_mul_f32 v74,v34,v74
v_mul_f32 v75,v35,v75
v_exp_f32 v72,v72
v_exp_f32 v73,v73
v_exp_f32 v74,v74
v_exp_f32 v75,v75
v_fma_f32 v72,v72,v99,v99
v_fma_f32 v73,v73,v99,v99
v_fma_f32 v74,v74,v99,v99
v_fma_f32 v75,v75,v99,v99
v_rcp_f32 v72,v72
v_rcp_f32 v73,v73
v_rcp_f32 v74,v74
v_rcp_f32 v75,v75
v_mul_f32 v32,v32,v72
s_add_u32 m0,s78,8448
s_nop 0
buffer_load_dword v6,s[36:39],s68 offen offset:0 sc1 lds
v_mul_f32 v33,v33,v73
v_mul_f32 v34,v34,v74
v_mul_f32 v35,v35,v75
v_cvt_pk_bf16_f32 v80,v32,v33
v_cvt_pk_bf16_f32 v81,v34,v35
v_fma_f32 v36,-v96,v20,v36
v_fma_f32 v37,-v96,v21,v37
v_fma_f32 v38,-v96,v22,v38
v_fma_f32 v39,-v96,v23,v39
v_mul_f32 v76,v36,v36
v_mul_f32 v77,v37,v37
v_mul_f32 v78,v38,v38
v_mul_f32 v79,v39,v39
v_fma_f32 v76,v98,v76,v97
v_fma_f32 v77,v98,v77,v97
v_fma_f32 v78,v98,v78,v97
v_fma_f32 v79,v98,v79,v97
v_mul_f32 v76,v36,v76
v_mul_f32 v77,v37,v77
v_mul_f32 v78,v38,v78
v_mul_f32 v79,v39,v79
v_exp_f32 v76,v76
v_exp_f32 v77,v77
v_exp_f32 v78,v78
v_exp_f32 v79,v79
v_fma_f32 v76,v76,v99,v99
v_fma_f32 v77,v77,v99,v99
v_fma_f32 v78,v78,v99,v99
v_fma_f32 v79,v79,v99,v99
v_rcp_f32 v76,v76
v_rcp_f32 v77,v77
v_rcp_f32 v78,v78
v_rcp_f32 v79,v79
v_mul_f32 v36,v36,v76
v_mul_f32 v37,v37,v77
v_mul_f32 v38,v38,v78
v_mul_f32 v39,v39,v79
v_cvt_pk_bf16_f32 v82,v36,v37
v_cvt_pk_bf16_f32 v83,v38,v39
buffer_store_dwordx4 v[80:83],v3,s[36:39],s73 offen offset:0 sc0 sc1
s_cmp_ge_u32 s48,s59
s_cbranch_scc1 Lg3o_done0_%=
s_add_u32 m0,s78,16384
s_nop 0
buffer_load_dword v6,s[36:39],s68 offen offset:0 sc1 lds
s_waitcnt vmcnt(3)
s_barrier
ds_read_b32 v13,v7 offset:8192
s_mul_i32 s70,s46,5
s_lshr_b32 s70,s70,6
buffer_store_dword v5,v4,s[36:39],s70 offen offset:0 sc0 sc1
s_mov_b32 s46,s59
s_mov_b32 s69,18432
s_waitcnt lgkmcnt(0)
v_cmp_eq_u32 vcc,s60,v13
s_nop 0
s_cmp_eq_u32 vcc_lo,-1
s_cbranch_scc1 Lg3o_go0_%=
s_add_u32 m0,s78,16640
s_nop 0
buffer_load_dword v6,s[36:39],s68 offen offset:0 sc1 lds
s_waitcnt vmcnt(4)
s_barrier
ds_read_b32 v13,v7 offset:8448
s_mov_b32 s69,18688
s_waitcnt lgkmcnt(0)
v_cmp_eq_u32 vcc,s60,v13
s_nop 0
s_cmp_eq_u32 vcc_lo,-1
s_cbranch_scc1 Lg3o_go0_%=
s_add_u32 m0,s78,8192
s_nop 0
buffer_load_dword v6,s[36:39],s68 offen offset:0 sc1 lds
s_waitcnt vmcnt(3)
s_barrier
ds_read_b32 v13,v7 offset:16384
s_mul_i32 s70,s47,5
s_lshr_b32 s70,s70,6
buffer_store_dword v5,v4,s[36:39],s70 offen offset:0 sc0 sc1
s_mov_b32 s47,s59
s_mov_b32 s69,10240
s_waitcnt lgkmcnt(0)
v_cmp_eq_u32 vcc,s60,v13
s_nop 0
s_cmp_eq_u32 vcc_lo,-1
s_cbranch_scc1 Lg3o_go0_%=
s_add_u32 m0,s78,8448
s_nop 0
buffer_load_dword v6,s[36:39],s68 offen offset:0 sc1 lds
s_waitcnt vmcnt(3)
s_barrier
ds_read_b32 v13,v7 offset:16640
s_mov_b32 s69,10496
s_waitcnt lgkmcnt(0)
v_cmp_eq_u32 vcc,s60,v13
s_nop 0
s_cmp_eq_u32 vcc_lo,-1
s_cbranch_scc1 Lg3o_go0_%=
Lg3o_poll0_%=:
s_add_u32 m0,s78,16384
s_nop 0
buffer_load_dword v6,s[36:39],s68 offen offset:0 sc1 lds
s_waitcnt vmcnt(2)
s_barrier
ds_read_b32 v13,v7 offset:8192
s_mov_b32 s69,18432
s_waitcnt lgkmcnt(0)
v_cmp_eq_u32 vcc,s60,v13
s_nop 0
s_cmp_eq_u32 vcc_lo,-1
s_cbranch_scc1 Lg3o_go0_%=
s_sleep 4
s_add_u32 m0,s78,16640
s_nop 0
buffer_load_dword v6,s[36:39],s68 offen offset:0 sc1 lds
s_waitcnt vmcnt(2)
s_barrier
ds_read_b32 v13,v7 offset:8448
s_mov_b32 s69,18688
s_waitcnt lgkmcnt(0)
v_cmp_eq_u32 vcc,s60,v13
s_nop 0
s_cmp_eq_u32 vcc_lo,-1)"
R"(
s_cbranch_scc1 Lg3o_go0_%=
s_sleep 4
s_add_u32 m0,s78,8192
s_nop 0
buffer_load_dword v6,s[36:39],s68 offen offset:0 sc1 lds
s_waitcnt vmcnt(2)
s_barrier
ds_read_b32 v13,v7 offset:16384
s_mov_b32 s69,10240
s_waitcnt lgkmcnt(0)
v_cmp_eq_u32 vcc,s60,v13
s_nop 0
s_cmp_eq_u32 vcc_lo,-1
s_cbranch_scc1 Lg3o_go0_%=
s_sleep 4
s_add_u32 m0,s78,8448
s_nop 0
buffer_load_dword v6,s[36:39],s68 offen offset:0 sc1 lds
s_waitcnt vmcnt(2)
s_barrier
ds_read_b32 v13,v7 offset:16640
s_mov_b32 s69,10496
s_waitcnt lgkmcnt(0)
v_cmp_eq_u32 vcc,s60,v13
s_nop 0
s_cmp_eq_u32 vcc_lo,-1
s_cbranch_scc1 Lg3o_go0_%=
s_sleep 4
s_add_u32 s79,s79,1
s_cmp_lt_u32 s79,20000
s_cbranch_scc1 Lg3o_poll0_%=
Lg3o_go0_%=:
s_xor_b32 s62,s63,65536
s_mov_b32 m0,s62
s_nop 0
buffer_load_dwordx4 v0,s[36:39],s48 offen offset:0 sc1 lds
buffer_load_dwordx4 v0,s[36:39],s48 offen offset:1024 sc1 lds
buffer_load_dwordx4 v0,s[36:39],s48 offen offset:2048 sc1 lds
buffer_load_dwordx4 v0,s[36:39],s48 offen offset:3072 sc1 lds
s_add_u32 m0,s62,16384
s_nop 0
buffer_load_dwordx4 v1,s[36:39],s48 offen offset:0 sc1 lds
buffer_load_dwordx4 v1,s[36:39],s48 offen offset:1024 sc1 lds
buffer_load_dwordx4 v1,s[36:39],s48 offen offset:2048 sc1 lds
buffer_load_dwordx4 v1,s[36:39],s48 offen offset:3072 sc1 lds
s_lshr_b32 s72,s48,5
s_lshr_b32 s71,s62,2
s_add_u32 m0,s71,s88
s_nop 0
buffer_load_dword v9,s[36:39],s72 offen offset:0 sc1 lds
s_cmp_eq_u32 vcc_hi,-1
s_cselect_b32 s66,s49,s59
s_xor_b32 s62,s64,65536
s_cmp_ge_u32 s66,s59
s_cselect_b32 s68,s49,s50
s_lshr_b32 s68,s68,5
s_add_u32 m0,s78,0
s_nop 0
buffer_load_dword v6,s[36:39],s68 offen offset:0 sc1 lds
s_mov_b32 m0,s62
s_nop 0
buffer_load_dwordx4 v0,s[36:39],s66 offen offset:0 sc1 lds
buffer_load_dwordx4 v0,s[36:39],s66 offen offset:1024 sc1 lds
buffer_load_dwordx4 v0,s[36:39],s66 offen offset:2048 sc1 lds
buffer_load_dwordx4 v0,s[36:39],s66 offen offset:3072 sc1 lds
s_waitcnt vmcnt(10)
s_barrier
ds_read_b128 v[40:43],v2 offset:0
ds_read_b128 v[44:47],v2 offset:1024
ds_read_b128 v[48:51],v2 offset:2048
ds_read_b128 v[52:55],v2 offset:3072
ds_read_b128 v[56:59],v2 offset:4096
ds_read_b128 v[60:63],v2 offset:5120
ds_read_b128 v[64:67],v2 offset:6144
s_mul_i32 s70,s46,5
s_lshr_b32 s70,s70,6
buffer_store_dword v5,v4,s[36:39],s70 offen offset:0 sc0 sc1
s_mov_b32 s46,s59
s_mul_i32 s70,s47,5
s_lshr_b32 s70,s70,6
buffer_store_dword v5,v4,s[36:39],s70 offen offset:0 sc0 sc1
s_mov_b32 s47,s59
s_mul_i32 s70,s59,5
s_lshr_b32 s70,s70,6
buffer_store_dword v5,v4,s[36:39],s70 offen offset:0 sc0 sc1
s_waitcnt lgkmcnt(3)
s_setpc_b64 s[90:91]
Lg3o_gs0_%=:
s_cmp_eq_u32 s54,1
s_cselect_b32 s68,s49,s48
s_lshr_b32 s68,s68,5
s_mul_i32 s73,s47,7
s_lshr_b32 s73,s73,1
s_add_u32 m0,s78,8192
s_nop 0
buffer_load_dword v6,s[36:39],s68 offen offset:0 sc1 lds
v_mul_f32 v96,s75,v92
v_fma_f32 v93,v93,s75,v12
v_fma_f32 v93,-v96,v96,v93
v_max_f32 v93,v93,v12
v_rsq_f32 v92,v93
s_nop 0
v_mul_f32 v97,s76,v92
v_mul_f32 v98,v92,v92
v_mul_f32 v99,v93,v92
v_mul_f32 v98,s77,v98
v_mul_f32 v98,v98,v97
v_fma_f32 v32,-v96,v16,v32
v_fma_f32 v33,-v96,v17,v33
v_fma_f32 v34,-v96,v18,v34
s_add_u32 m0,s62,16384
s_nop 0
buffer_load_dwordx4 v1,s[36:39],s66 offen offset:0 sc1 lds
v_fma_f32 v35,-v96,v19,v35
v_mul_f32 v72,v32,v32
v_mul_f32 v73,v33,v33
v_mul_f32 v74,v34,v34
v_mul_f32 v75,v35,v35
v_fma_f32 v72,v98,v72,v97
v_fma_f32 v73,v98,v73,v97
v_fma_f32 v74,v98,v74,v97
v_fma_f32 v75,v98,v75,v97
v_mul_f32 v72,v32,v72
v_mul_f32 v73,v33,v73
v_mul_f32 v74,v34,v74
v_mul_f32 v75,v35,v75
buffer_load_dwordx4 v1,s[36:39],s66 offen offset:1024 sc1 lds
v_exp_f32 v72,v72
v_exp_f32 v73,v73
v_exp_f32 v74,v74
v_exp_f32 v75,v75
v_fma_f32 v72,v72,v99,v99
v_fma_f32 v73,v73,v99,v99
v_fma_f32 v74,v74,v99,v99
v_fma_f32 v75,v75,v99,v99
v_rcp_f32 v72,v72
v_rcp_f32 v73,v73
v_rcp_f32 v74,v74
v_rcp_f32 v75,v75
v_mul_f32 v32,v32,v72
buffer_load_dwordx4 v1,s[36:39],s66 offen offset:2048 sc1 lds
v_mul_f32 v33,v33,v73
v_mul_f32 v34,v34,v74
v_mul_f32 v35,v35,v75
v_cvt_pk_bf16_f32 v80,v32,v33
v_cvt_pk_bf16_f32 v81,v34,v35
v_fma_f32 v36,-v96,v20,v36
v_fma_f32 v37,-v96,v21,v37
v_fma_f32 v38,-v96,v22,v38
v_fma_f32 v39,-v96,v23,v39
v_mul_f32 v76,v36,v36
v_mul_f32 v77,v37,v37
v_mul_f32 v78,v38,v38
v_mul_f32 v79,v39,v39
buffer_load_dwordx4 v1,s[36:39],s66 offen offset:3072 sc1 lds
v_fma_f32 v76,v98,v76,v97
v_fma_f32 v77,v98,v77,v97
v_fma_f32 v78,v98,v78,v97
v_fma_f32 v79,v98,v79,v97
v_mul_f32 v76,v36,v76
v_mul_f32 v77,v37,v77
v_mul_f32 v78,v38,v78
v_mul_f32 v79,v39,v79
v_exp_f32 v76,v76
v_exp_f32 v77,v77
v_exp_f32 v78,v78
v_exp_f32 v79,v79
v_fma_f32 v76,v76,v99,v99
s_lshr_b32 s72,s66,5
s_lshr_b32 s71,s62,2
s_add_u32 m0,s71,s88
s_nop 0
buffer_load_dword v9,s[36:39],s72 offen offset:0 sc1 lds
v_fma_f32 v77,v77,v99,v99
v_fma_f32 v78,v78,v99,v99
v_fma_f32 v79,v79,v99,v99
v_rcp_f32 v76,v76
v_rcp_f32 v77,v77
v_rcp_f32 v78,v78
v_rcp_f32 v79,v79
v_mul_f32 v36,v36,v76
v_mul_f32 v37,v37,v77
v_mul_f32 v38,v38,v78
v_mul_f32 v39,v39,v79
v_cvt_pk_bf16_f32 v82,v36,v37
v_cvt_pk_bf16_f32 v83,v38,v39
buffer_store_dwordx4 v[80:83],v3,s[36:39],s73 offen offset:0 sc0 sc1
s_waitcnt vmcnt(6)
s_barrier
ds_read_b32 v13,v7 offset:8192
ds_read_b128 v[40:43],v2 offset:0
ds_read_b128 v[44:47],v2 offset:1024
ds_read_b128 v[48:51],v2 offset:2048
ds_read_b128 v[52:55],v2 offset:3072
ds_read_b128 v[56:59],v2 offset:4096
ds_read_b128 v[60:63],v2 offset:5120
ds_read_b128 v[64:67],v2 offset:6144
s_mul_i32 s70,s46,5
s_lshr_b32 s70,s70,6
buffer_store_dword v5,v4,s[36:39],s70 offen offset:0 sc0 sc1)"
R"(
s_waitcnt lgkmcnt(7)
v_cmp_eq_u32 vcc,s60,v13
s_nop 0
s_cmp_eq_u32 vcc_hi,-1
s_cselect_b32 s66,s49,s59
s_xor_b32 s62,s64,65536
s_cmp_ge_u32 s66,s59
s_cselect_b32 s68,s49,s50
s_lshr_b32 s68,s68,5
s_add_u32 m0,s78,0
s_nop 0
buffer_load_dword v6,s[36:39],s68 offen offset:0 sc1 lds
s_mov_b32 m0,s62
s_nop 0
buffer_load_dwordx4 v0,s[36:39],s66 offen offset:0 sc1 lds
buffer_load_dwordx4 v0,s[36:39],s66 offen offset:1024 sc1 lds
buffer_load_dwordx4 v0,s[36:39],s66 offen offset:2048 sc1 lds
buffer_load_dwordx4 v0,s[36:39],s66 offen offset:3072 sc1 lds
s_mov_b32 s46,s47
s_mov_b32 s47,s59
s_mul_i32 s70,s59,5
s_lshr_b32 s70,s70,6
buffer_store_dword v5,v4,s[36:39],s70 offen offset:0 sc0 sc1
s_waitcnt lgkmcnt(3)
s_setpc_b64 s[90:91]
Lg3o_done0_%=:
s_waitcnt vmcnt(0)
s_barrier
s_mul_i32 s70,s46,5
s_lshr_b32 s70,s70,6
buffer_store_dword v5,v4,s[36:39],s70 offen offset:0 sc0 sc1
s_mul_i32 s70,s47,5
s_lshr_b32 s70,s70,6
buffer_store_dword v5,v4,s[36:39],s70 offen offset:0 sc0 sc1
s_branch Lg3o_done_%=
Lg3o_slow1_%=:
s_cmp_lt_u32 s67,s59
s_cbranch_scc1 Lg3o_gs1_%=
Lg3o_slowb1_%=:
s_cmp_eq_u32 s54,1
s_cselect_b32 s68,s50,s49
s_lshr_b32 s68,s68,5
s_add_u32 m0,s78,8192
s_nop 0
buffer_load_dword v6,s[36:39],s68 offen offset:0 sc1 lds
s_mul_i32 s73,s48,7
s_lshr_b32 s73,s73,1
v_mul_f32 v96,s75,v92
v_fma_f32 v93,v93,s75,v12
v_fma_f32 v93,-v96,v96,v93
v_max_f32 v93,v93,v12
v_rsq_f32 v92,v93
s_nop 0
v_mul_f32 v97,s76,v92
v_mul_f32 v98,v92,v92
v_mul_f32 v99,v93,v92
v_mul_f32 v98,s77,v98
v_mul_f32 v98,v98,v97
v_fma_f32 v24,-v96,v16,v24
v_fma_f32 v25,-v96,v17,v25
v_fma_f32 v26,-v96,v18,v26
v_fma_f32 v27,-v96,v19,v27
v_mul_f32 v72,v24,v24
v_mul_f32 v73,v25,v25
v_mul_f32 v74,v26,v26
v_mul_f32 v75,v27,v27
v_fma_f32 v72,v98,v72,v97
v_fma_f32 v73,v98,v73,v97
v_fma_f32 v74,v98,v74,v97
v_fma_f32 v75,v98,v75,v97
v_mul_f32 v72,v24,v72
v_mul_f32 v73,v25,v73
v_mul_f32 v74,v26,v74
v_mul_f32 v75,v27,v75
v_exp_f32 v72,v72
v_exp_f32 v73,v73
v_exp_f32 v74,v74
v_exp_f32 v75,v75
v_fma_f32 v72,v72,v99,v99
v_fma_f32 v73,v73,v99,v99
v_fma_f32 v74,v74,v99,v99
v_fma_f32 v75,v75,v99,v99
v_rcp_f32 v72,v72
v_rcp_f32 v73,v73
v_rcp_f32 v74,v74
v_rcp_f32 v75,v75
v_mul_f32 v24,v24,v72
s_add_u32 m0,s78,8448
s_nop 0
buffer_load_dword v6,s[36:39],s68 offen offset:0 sc1 lds
v_mul_f32 v25,v25,v73
v_mul_f32 v26,v26,v74
v_mul_f32 v27,v27,v75
v_cvt_pk_bf16_f32 v80,v24,v25
v_cvt_pk_bf16_f32 v81,v26,v27
v_fma_f32 v28,-v96,v20,v28
v_fma_f32 v29,-v96,v21,v29
v_fma_f32 v30,-v96,v22,v30
v_fma_f32 v31,-v96,v23,v31
v_mul_f32 v76,v28,v28
v_mul_f32 v77,v29,v29
v_mul_f32 v78,v30,v30
v_mul_f32 v79,v31,v31
v_fma_f32 v76,v98,v76,v97
v_fma_f32 v77,v98,v77,v97
v_fma_f32 v78,v98,v78,v97
v_fma_f32 v79,v98,v79,v97
v_mul_f32 v76,v28,v76
v_mul_f32 v77,v29,v77
v_mul_f32 v78,v30,v78
v_mul_f32 v79,v31,v79
v_exp_f32 v76,v76
v_exp_f32 v77,v77
v_exp_f32 v78,v78
v_exp_f32 v79,v79
v_fma_f32 v76,v76,v99,v99
v_fma_f32 v77,v77,v99,v99
v_fma_f32 v78,v78,v99,v99
v_fma_f32 v79,v79,v99,v99
v_rcp_f32 v76,v76
v_rcp_f32 v77,v77
v_rcp_f32 v78,v78
v_rcp_f32 v79,v79
v_mul_f32 v28,v28,v76
v_mul_f32 v29,v29,v77
v_mul_f32 v30,v30,v78
v_mul_f32 v31,v31,v79
v_cvt_pk_bf16_f32 v82,v28,v29
v_cvt_pk_bf16_f32 v83,v30,v31
buffer_store_dwordx4 v[80:83],v3,s[36:39],s73 offen offset:0 sc0 sc1
s_cmp_ge_u32 s49,s59
s_cbranch_scc1 Lg3o_done1_%=
s_add_u32 m0,s78,16384
s_nop 0
buffer_load_dword v6,s[36:39],s68 offen offset:0 sc1 lds
s_waitcnt vmcnt(3)
s_barrier
ds_read_b32 v13,v7 offset:8192
s_mul_i32 s70,s47,5
s_lshr_b32 s70,s70,6
buffer_store_dword v5,v4,s[36:39],s70 offen offset:0 sc0 sc1
s_mov_b32 s47,s59
s_mov_b32 s69,18432
s_waitcnt lgkmcnt(0)
v_cmp_eq_u32 vcc,s60,v13
s_nop 0
s_cmp_eq_u32 vcc_lo,-1
s_cbranch_scc1 Lg3o_go1_%=
s_add_u32 m0,s78,16640
s_nop 0
buffer_load_dword v6,s[36:39],s68 offen offset:0 sc1 lds
s_waitcnt vmcnt(4)
s_barrier
ds_read_b32 v13,v7 offset:8448
s_mov_b32 s69,18688
s_waitcnt lgkmcnt(0)
v_cmp_eq_u32 vcc,s60,v13
s_nop 0
s_cmp_eq_u32 vcc_lo,-1
s_cbranch_scc1 Lg3o_go1_%=
s_add_u32 m0,s78,8192
s_nop 0
buffer_load_dword v6,s[36:39],s68 offen offset:0 sc1 lds
s_waitcnt vmcnt(3)
s_barrier
ds_read_b32 v13,v7 offset:16384
s_mul_i32 s70,s48,5
s_lshr_b32 s70,s70,6
buffer_store_dword v5,v4,s[36:39],s70 offen offset:0 sc0 sc1
s_mov_b32 s48,s59
s_mov_b32 s69,10240
s_waitcnt lgkmcnt(0)
v_cmp_eq_u32 vcc,s60,v13
s_nop 0
s_cmp_eq_u32 vcc_lo,-1
s_cbranch_scc1 Lg3o_go1_%=
s_add_u32 m0,s78,8448
s_nop 0
buffer_load_dword v6,s[36:39],s68 offen offset:0 sc1 lds
s_waitcnt vmcnt(3)
s_barrier
ds_read_b32 v13,v7 offset:16640
s_mov_b32 s69,10496
s_waitcnt lgkmcnt(0)
v_cmp_eq_u32 vcc,s60,v13
s_nop 0
s_cmp_eq_u32 vcc_lo,-1
s_cbranch_scc1 Lg3o_go1_%=
Lg3o_poll1_%=:
s_add_u32 m0,s78,16384
s_nop 0
buffer_load_dword v6,s[36:39],s68 offen offset:0 sc1 lds
s_waitcnt vmcnt(2)
s_barrier
ds_read_b32 v13,v7 offset:8192
s_mov_b32 s69,18432
s_waitcnt lgkmcnt(0)
v_cmp_eq_u32 vcc,s60,v13
s_nop 0
s_cmp_eq_u32 vcc_lo,-1)"
R"(
s_cbranch_scc1 Lg3o_go1_%=
s_sleep 4
s_add_u32 m0,s78,16640
s_nop 0
buffer_load_dword v6,s[36:39],s68 offen offset:0 sc1 lds
s_waitcnt vmcnt(2)
s_barrier
ds_read_b32 v13,v7 offset:8448
s_mov_b32 s69,18688
s_waitcnt lgkmcnt(0)
v_cmp_eq_u32 vcc,s60,v13
s_nop 0
s_cmp_eq_u32 vcc_lo,-1
s_cbranch_scc1 Lg3o_go1_%=
s_sleep 4
s_add_u32 m0,s78,8192
s_nop 0
buffer_load_dword v6,s[36:39],s68 offen offset:0 sc1 lds
s_waitcnt vmcnt(2)
s_barrier
ds_read_b32 v13,v7 offset:16384
s_mov_b32 s69,10240
s_waitcnt lgkmcnt(0)
v_cmp_eq_u32 vcc,s60,v13
s_nop 0
s_cmp_eq_u32 vcc_lo,-1
s_cbranch_scc1 Lg3o_go1_%=
s_sleep 4
s_add_u32 m0,s78,8448
s_nop 0
buffer_load_dword v6,s[36:39],s68 offen offset:0 sc1 lds
s_waitcnt vmcnt(2)
s_barrier
ds_read_b32 v13,v7 offset:16640
s_mov_b32 s69,10496
s_waitcnt lgkmcnt(0)
v_cmp_eq_u32 vcc,s60,v13
s_nop 0
s_cmp_eq_u32 vcc_lo,-1
s_cbranch_scc1 Lg3o_go1_%=
s_sleep 4
s_add_u32 s79,s79,1
s_cmp_lt_u32 s79,20000
s_cbranch_scc1 Lg3o_poll1_%=
Lg3o_go1_%=:
s_xor_b32 s62,s64,65536
s_mov_b32 m0,s62
s_nop 0
buffer_load_dwordx4 v0,s[36:39],s49 offen offset:0 sc1 lds
buffer_load_dwordx4 v0,s[36:39],s49 offen offset:1024 sc1 lds
buffer_load_dwordx4 v0,s[36:39],s49 offen offset:2048 sc1 lds
buffer_load_dwordx4 v0,s[36:39],s49 offen offset:3072 sc1 lds
s_add_u32 m0,s62,16384
s_nop 0
buffer_load_dwordx4 v1,s[36:39],s49 offen offset:0 sc1 lds
buffer_load_dwordx4 v1,s[36:39],s49 offen offset:1024 sc1 lds
buffer_load_dwordx4 v1,s[36:39],s49 offen offset:2048 sc1 lds
buffer_load_dwordx4 v1,s[36:39],s49 offen offset:3072 sc1 lds
s_lshr_b32 s72,s49,5
s_lshr_b32 s71,s62,2
s_add_u32 m0,s71,s88
s_nop 0
buffer_load_dword v9,s[36:39],s72 offen offset:0 sc1 lds
s_cmp_eq_u32 vcc_hi,-1
s_cselect_b32 s67,s50,s59
s_xor_b32 s62,s63,65536
s_cmp_ge_u32 s67,s59
s_cselect_b32 s68,s50,s51
s_lshr_b32 s68,s68,5
s_add_u32 m0,s78,256
s_nop 0
buffer_load_dword v6,s[36:39],s68 offen offset:0 sc1 lds
s_mov_b32 m0,s62
s_nop 0
buffer_load_dwordx4 v0,s[36:39],s67 offen offset:0 sc1 lds
buffer_load_dwordx4 v0,s[36:39],s67 offen offset:1024 sc1 lds
buffer_load_dwordx4 v0,s[36:39],s67 offen offset:2048 sc1 lds
buffer_load_dwordx4 v0,s[36:39],s67 offen offset:3072 sc1 lds
s_waitcnt vmcnt(10)
s_barrier
ds_read_b128 v[40:43],v2 offset:32768
ds_read_b128 v[44:47],v2 offset:33792
ds_read_b128 v[48:51],v2 offset:34816
ds_read_b128 v[52:55],v2 offset:35840
ds_read_b128 v[56:59],v2 offset:36864
ds_read_b128 v[60:63],v2 offset:37888
ds_read_b128 v[64:67],v2 offset:38912
s_mul_i32 s70,s47,5
s_lshr_b32 s70,s70,6
buffer_store_dword v5,v4,s[36:39],s70 offen offset:0 sc0 sc1
s_mov_b32 s47,s59
s_mul_i32 s70,s48,5
s_lshr_b32 s70,s70,6
buffer_store_dword v5,v4,s[36:39],s70 offen offset:0 sc0 sc1
s_mov_b32 s48,s59
s_mul_i32 s70,s59,5
s_lshr_b32 s70,s70,6
buffer_store_dword v5,v4,s[36:39],s70 offen offset:0 sc0 sc1
s_waitcnt lgkmcnt(3)
s_setpc_b64 s[92:93]
Lg3o_gs1_%=:
s_cmp_eq_u32 s54,1
s_cselect_b32 s68,s50,s49
s_lshr_b32 s68,s68,5
s_mul_i32 s73,s48,7
s_lshr_b32 s73,s73,1
s_add_u32 m0,s78,8192
s_nop 0
buffer_load_dword v6,s[36:39],s68 offen offset:0 sc1 lds
v_mul_f32 v96,s75,v92
v_fma_f32 v93,v93,s75,v12
v_fma_f32 v93,-v96,v96,v93
v_max_f32 v93,v93,v12
v_rsq_f32 v92,v93
s_nop 0
v_mul_f32 v97,s76,v92
v_mul_f32 v98,v92,v92
v_mul_f32 v99,v93,v92
v_mul_f32 v98,s77,v98
v_mul_f32 v98,v98,v97
v_fma_f32 v24,-v96,v16,v24
v_fma_f32 v25,-v96,v17,v25
v_fma_f32 v26,-v96,v18,v26
s_add_u32 m0,s62,16384
s_nop 0
buffer_load_dwordx4 v1,s[36:39],s67 offen offset:0 sc1 lds
v_fma_f32 v27,-v96,v19,v27
v_mul_f32 v72,v24,v24
v_mul_f32 v73,v25,v25
v_mul_f32 v74,v26,v26
v_mul_f32 v75,v27,v27
v_fma_f32 v72,v98,v72,v97
v_fma_f32 v73,v98,v73,v97
v_fma_f32 v74,v98,v74,v97
v_fma_f32 v75,v98,v75,v97
v_mul_f32 v72,v24,v72
v_mul_f32 v73,v25,v73
v_mul_f32 v74,v26,v74
v_mul_f32 v75,v27,v75
buffer_load_dwordx4 v1,s[36:39],s67 offen offset:1024 sc1 lds
v_exp_f32 v72,v72
v_exp_f32 v73,v73
v_exp_f32 v74,v74
v_exp_f32 v75,v75
v_fma_f32 v72,v72,v99,v99
v_fma_f32 v73,v73,v99,v99
v_fma_f32 v74,v74,v99,v99
v_fma_f32 v75,v75,v99,v99
v_rcp_f32 v72,v72
v_rcp_f32 v73,v73
v_rcp_f32 v74,v74
v_rcp_f32 v75,v75
v_mul_f32 v24,v24,v72
buffer_load_dwordx4 v1,s[36:39],s67 offen offset:2048 sc1 lds
v_mul_f32 v25,v25,v73
v_mul_f32 v26,v26,v74
v_mul_f32 v27,v27,v75
v_cvt_pk_bf16_f32 v80,v24,v25
v_cvt_pk_bf16_f32 v81,v26,v27
v_fma_f32 v28,-v96,v20,v28
v_fma_f32 v29,-v96,v21,v29
v_fma_f32 v30,-v96,v22,v30
v_fma_f32 v31,-v96,v23,v31
v_mul_f32 v76,v28,v28
v_mul_f32 v77,v29,v29
v_mul_f32 v78,v30,v30
v_mul_f32 v79,v31,v31
buffer_load_dwordx4 v1,s[36:39],s67 offen offset:3072 sc1 lds
v_fma_f32 v76,v98,v76,v97
v_fma_f32 v77,v98,v77,v97
v_fma_f32 v78,v98,v78,v97
v_fma_f32 v79,v98,v79,v97
v_mul_f32 v76,v28,v76
v_mul_f32 v77,v29,v77
v_mul_f32 v78,v30,v78
v_mul_f32 v79,v31,v79
v_exp_f32 v76,v76
v_exp_f32 v77,v77
v_exp_f32 v78,v78
v_exp_f32 v79,v79
v_fma_f32 v76,v76,v99,v99
s_lshr_b32 s72,s67,5
s_lshr_b32 s71,s62,2
s_add_u32 m0,s71,s88
s_nop 0
buffer_load_dword v9,s[36:39],s72 offen offset:0 sc1 lds
v_fma_f32 v77,v77,v99,v99
v_fma_f32 v78,v78,v99,v99
v_fma_f32 v79,v79,v99,v99
v_rcp_f32 v76,v76
v_rcp_f32 v77,v77
v_rcp_f32 v78,v78
v_rcp_f32 v79,v79
v_mul_f32 v28,v28,v76
v_mul_f32 v29,v29,v77
v_mul_f32 v30,v30,v78
v_mul_f32 v31,v31,v79
v_cvt_pk_bf16_f32 v82,v28,v29
v_cvt_pk_bf16_f32 v83,v30,v31
buffer_store_dwordx4 v[80:83],v3,s[36:39],s73 offen offset:0 sc0 sc1)"
R"(
s_waitcnt vmcnt(6)
s_barrier
ds_read_b32 v13,v7 offset:8192
ds_read_b128 v[40:43],v2 offset:32768
ds_read_b128 v[44:47],v2 offset:33792
ds_read_b128 v[48:51],v2 offset:34816
ds_read_b128 v[52:55],v2 offset:35840
ds_read_b128 v[56:59],v2 offset:36864
ds_read_b128 v[60:63],v2 offset:37888
ds_read_b128 v[64:67],v2 offset:38912
s_mul_i32 s70,s47,5
s_lshr_b32 s70,s70,6
buffer_store_dword v5,v4,s[36:39],s70 offen offset:0 sc0 sc1
s_waitcnt lgkmcnt(7)
v_cmp_eq_u32 vcc,s60,v13
s_nop 0
s_cmp_eq_u32 vcc_hi,-1
s_cselect_b32 s67,s50,s59
s_xor_b32 s62,s63,65536
s_cmp_ge_u32 s67,s59
s_cselect_b32 s68,s50,s51
s_lshr_b32 s68,s68,5
s_add_u32 m0,s78,256
s_nop 0
buffer_load_dword v6,s[36:39],s68 offen offset:0 sc1 lds
s_mov_b32 m0,s62
s_nop 0
buffer_load_dwordx4 v0,s[36:39],s67 offen offset:0 sc1 lds
buffer_load_dwordx4 v0,s[36:39],s67 offen offset:1024 sc1 lds
buffer_load_dwordx4 v0,s[36:39],s67 offen offset:2048 sc1 lds
buffer_load_dwordx4 v0,s[36:39],s67 offen offset:3072 sc1 lds
s_mov_b32 s47,s48
s_mov_b32 s48,s59
s_mul_i32 s70,s59,5
s_lshr_b32 s70,s70,6
buffer_store_dword v5,v4,s[36:39],s70 offen offset:0 sc0 sc1
s_waitcnt lgkmcnt(3)
s_setpc_b64 s[92:93]
Lg3o_done1_%=:
s_waitcnt vmcnt(0)
s_barrier
s_mul_i32 s70,s47,5
s_lshr_b32 s70,s70,6
buffer_store_dword v5,v4,s[36:39],s70 offen offset:0 sc0 sc1
s_mul_i32 s70,s48,5
s_lshr_b32 s70,s70,6
buffer_store_dword v5,v4,s[36:39],s70 offen offset:0 sc0 sc1
s_branch Lg3o_done_%=
Lg3o_done_%=:
s_waitcnt vmcnt(0) lgkmcnt(0)
s_barrier
s_endpgm
Lcode_g3o_e:)"
  :: "s"(ws), "s"(wave), "s"(sStep), "s"(sX1), "s"(sX2), "s"(sU0), "s"(sWw), "s"(sBuf), "v"(vSrc), "v"(vA), "v"(vOut), "v"(vFlag), "v"(vPollOff), "v"(vSnapRd), "v"(vX), "v"(vAuxS), "v"(vStW), "v"(vStR), "v"(vWd0), "v"(vWd1), "v"(vWr0), "v"(vWr1), "v"(vWr2), "s"(w3) : "v0", "v1", "v2", "v3", "v4", "v5", "v6", "v7", "v8", "v9", "v10", "v11", "v12", "v13", "v14", "v15", "v16", "v17", "v18", "v19", "v20", "v21", "v22", "v23", "v24", "v25", "v26", "v27", "v28", "v29", "v30", "v31", "v32", "v33", "v34", "v35", "v36", "v37", "v38", "v39", "v40", "v41", "v42", "v43", "v44", "v45", "v46", "v47", "v48", "v49", "v50", "v51", "v52", "v53", "v54", "v55", "v56", "v57", "v58", "v59", "v60", "v61", "v62", "v63", "v64", "v65", "v66", "v67", "v68", "v69", "v70", "v71", "v72", "v73", "v74", "v75", "v76", "v77", "v78", "v79", "v80", "v81", "v82", "v83", "v84", "v85", "v86", "v87", "v88", "v89", "v90", "v91", "v92", "v93", "v94", "v95", "v96", "v97", "v98", "v99", "v100", "v101", "v102", "v103", "v104", "v105", "v106", "v107", "v108", "v109", "v110", "v111", "v112", "v113", "v114", "v115", "v116", "v117", "v118", "v119", "v120", "v121", "v122", "v123", "v124", "v125", "v126", "v127", "v128", "v129", "v130", "v131", "v132", "v133", "v134", "v135", "v136", "v137", "v138", "v139", "v140", "v141", "v142", "v143", "v144", "v145", "v146", "v147", "v148", "v149", "v150", "v151", "v152", "v153", "v154", "v155", "v156", "v157", "v158", "v159", "v160", "v161", "v162", "v163", "v164", "v165", "v166", "v167", "v168", "v169", "v170", "v171", "v172", "v173", "v174", "v175", "v176", "v177", "v178", "v179", "v180", "v181", "v182", "v183", "v184", "v185", "v186", "v187", "v188", "v189", "v190", "v191", "v192", "v193", "v194", "v195", "v196", "v197", "v198", "v199", "v200", "v201", "v202", "v203", "v204", "v205", "v206", "v207", "v208", "v209", "v210", "v211", "v212", "v213", "v214", "v215", "v216", "v217", "v218", "v219", "v220", "v221", "v222", "v223", "v224", "v225", "v226", "v227", "v228", "v229", "v230", "v231", "v255", "a0", "a1", "a2", "a3", "a4", "a5", "a6", "a7", "a8", "a9", "a10", "a11", "a12", "a13", "a14", "a15", "a16", "a17", "a18", "a19", "a20", "a21", "a22", "a23", "a24", "a25", "a26", "a27", "a28", "a29", "a30", "a31", "a32", "a33", "a34", "a35", "a36", "a37", "a38", "a39", "a40", "a41", "a42", "a43", "a44", "a45", "a46", "a47", "a48", "a49", "a50", "a51", "a52", "a53", "a54", "a55", "a56", "a57", "a58", "a59", "a60", "a61", "a62", "a63", "a64", "a65", "a66", "a67", "a68", "a69", "a70", "a71", "a72", "a73", "a74", "a75", "a76", "a77", "a78", "a79", "a80", "a81", "a82", "a83", "a84", "a85", "a86", "a87", "a88", "a89", "a90", "a91", "a92", "a93", "a94", "a95", "a96", "a97", "a98", "a99", "a100", "a101", "a102", "a103", "a104", "a105", "a106", "a107", "a108", "a109", "a110", "a111", "a112", "a113", "a114", "a115", "a116", "a117", "a118", "a119", "a120", "a121", "a122", "a123", "a124", "a125", "a126", "a127", "a128", "a129", "a130", "a131", "a132", "a133", "a134", "a135", "a136", "a137", "a138", "a139", "a140", "a141", "a142", "a143", "a144", "a145", "a146", "a147", "a148", "a149", "a150", "a151", "a152", "a153", "a154", "a155", "a156", "a157", "a158", "a159", "a160", "a161", "a162", "a163", "a164", "a165", "a166", "a167", "a168", "a169", "a170", "a171", "a172", "a173", "a174", "a175", "a176", "a177", "a178", "a179", "a180", "a181", "a182", "a183", "a184", "a185", "a186", "a187", "a188", "a189", "a190", "a191", "a192", "a193", "a194", "a195", "a196", "a197", "a198", "a199", "a200", "a201", "a202", "a203", "a204", "a205", "a206", "a207", "a208", "a209", "a210", "a211", "a212", "a213", "a214", "a215", "a216", "a217", "a218", "a219", "a220", "a221", "a222", "a223", "a224", "a225", "a226", "a227", "a228", "a229", "a230", "a231", "a232", "a233", "a234", "a235", "a236", "a237", "a238", "a239", "a240", "a241", "a242", "a243", "a244", "a245", "a246", "a247", "a248", "a249", "a250", "a251", "a252", "a253", "a254", "a255", "s36", "s37", "s38", "s39", "s40", "s41", "s42", "s43", "s44", "s45", "s46", "s47", "s48", "s49", "s50", "s51", "s52", "s53", "s54", "s55", "s56", "s57", "s58", "s59", "s60", "s61", "s62", "s63", "s64", "s65", "s66", "s67", "s68", "s69", "s70", "s71", "s72", "s73", "s74", "s75", "s76", "s77", "s78", "s79", "s80", "s81", "s82", "s83", "s84", "s85", "s86", "s87", "s88", "s89", "s90", "s91", "s92", "s93", "s94", "s95", "s96", "s97", "s98", "s99", "vcc", "scc", "memory"); }
    __builtin_unreachable();
  } else if (kind == 4) {
    KARG(ws, 10) KARG(w4, 9)
    COMMON(kh == 1 ? 6 : 5, OFF_FG + ((14 * kh) / 3) * 128, OFF_FP + (kh * 4 + slice) * 128, 65536, 256, 2048, 5);
    const unsigned vSrc = OFF_G + kh * 28672 + wave * 4096 + vA, vSrc2 = OFF_G + kh * 28672 + 16384 + wave * 3072 + vA;
    const unsigned vOut = (kh == 0 ? OFF_P0 : kh == 1 ? OFF_P1 : kh == 2 ? OFF_P2 : OFF_P3) + (l15 >> 3) * 16384 + (slice * 8 + wave * 2 + (kh >> 1)) * 512 + lg * 128 + (l15 & 7) * 16;
    const unsigned vOut2 = vOut ^ 512;
    const unsigned vAuxS = ((kh & 1) ? OFF_XP : OFF_X1) + (slice * 8 + wave * 2 + 1 - (kh >> 1)) * 1024 + vA;
    const unsigned sWw = __builtin_amdgcn_readfirstlane(kh * 896 * 2048 + slice * 512 + wave * 128);
    asm volatile(R"(
Lcode_g4_b:
s_mov_b64 s[36:37],%0
s_mov_b32 s38,122727552
s_mov_b32 s39,0x00020000
s_mov_b32 s54,%1
s_mov_b32 s55,%2
s_mov_b32 s56,%3
s_mov_b32 s58,%4
s_mov_b32 s83,%5
s_mov_b32 s82,%6
s_mov_b32 s84,%7
v_mov_b32 v0,%8
v_mov_b32 v1,%9
v_mov_b32 v2,%10
v_mov_b32 v3,%11
v_mov_b32 v4,%12
v_mov_b32 v5,%13
v_mov_b32 v7,%14
v_mov_b32 v8,%15
v_mov_b32 v9,%16
v_mov_b32 v12,%17
v_mov_b32 v13,%18
v_mov_b32 v14,%19
v_mov_b32 v15,%20
v_mov_b32 v16,%21
s_mov_b64 s[40:41],%22
s_mov_b32 s42,7340032
s_mov_b32 s43,0x00020000
s_lshl_b32 s88,s54,12
s_mul_i32 s68,s54,9216
s_add_u32 s68,s68,122880
v_add_u32 v10,s68,v2
s_add_u32 m0,s88,0
s_add_u32 s61,s82,0
buffer_load_dwordx4 v12,s[40:43],s61 offen offset:0  lds
s_add_u32 m0,s88,1024
s_nop 0
buffer_load_dwordx4 v13,s[40:43],s61 offen offset:0  lds
s_add_u32 m0,s88,2048
s_add_u32 s61,s82,32768
buffer_load_dwordx4 v12,s[40:43],s61 offen offset:0  lds
s_add_u32 m0,s88,3072
s_nop 0
buffer_load_dwordx4 v13,s[40:43],s61 offen offset:0  lds
s_add_u32 m0,s88,16384
s_add_u32 s61,s82,65536
buffer_load_dwordx4 v12,s[40:43],s61 offen offset:0  lds
s_add_u32 m0,s88,17408
s_nop 0
buffer_load_dwordx4 v13,s[40:43],s61 offen offset:0  lds
s_add_u32 m0,s88,18432
s_add_u32 s61,s82,98304
buffer_load_dwordx4 v12,s[40:43],s61 offen offset:0  lds
s_add_u32 m0,s88,19456
s_nop 0
buffer_load_dwordx4 v13,s[40:43],s61 offen offset:0  lds
s_add_u32 m0,s88,32768
s_add_u32 s61,s82,131072
buffer_load_dwordx4 v12,s[40:43],s61 offen offset:0  lds
s_add_u32 m0,s88,33792
s_nop 0
buffer_load_dwordx4 v13,s[40:43],s61 offen offset:0  lds
s_add_u32 m0,s88,34816
s_add_u32 s61,s82,163840
buffer_load_dwordx4 v12,s[40:43],s61 offen offset:0  lds
s_add_u32 m0,s88,35840
s_nop 0
buffer_load_dwordx4 v13,s[40:43],s61 offen offset:0  lds
s_add_u32 m0,s88,49152
s_add_u32 s61,s82,196608
buffer_load_dwordx4 v12,s[40:43],s61 offen offset:0  lds
s_add_u32 m0,s88,50176
s_nop 0
buffer_load_dwordx4 v13,s[40:43],s61 offen offset:0  lds
s_add_u32 m0,s88,51200
s_add_u32 s61,s82,229376
buffer_load_dwordx4 v12,s[40:43],s61 offen offset:0  lds
s_add_u32 m0,s88,52224
s_nop 0
buffer_load_dwordx4 v13,s[40:43],s61 offen offset:0  lds
s_add_u32 m0,s88,65536
s_add_u32 s61,s82,262144
buffer_load_dwordx4 v12,s[40:43],s61 offen offset:0  lds
s_add_u32 m0,s88,66560
s_nop 0
buffer_load_dwordx4 v13,s[40:43],s61 offen offset:0  lds
s_add_u32 m0,s88,67584
s_add_u32 s61,s82,294912
buffer_load_dwordx4 v12,s[40:43],s61 offen offset:0  lds
s_add_u32 m0,s88,68608
s_nop 0
buffer_load_dwordx4 v13,s[40:43],s61 offen offset:0  lds
v_add_u32 v28,49152,v14
v_add_u32 v30,49152,v16
v_add_u32 v32,98304,v14
v_add_u32 v34,98304,v16
s_waitcnt vmcnt(16)
s_add_u32 m0,s88,81920
s_add_u32 s61,s82,327680
buffer_load_dwordx4 v12,s[40:43],s61 offen offset:0  lds
s_add_u32 m0,s88,82944
ds_read_b64_tr_b16 a[0:1],v14 offset:0
buffer_load_dwordx4 v13,s[40:43],s61 offen offset:0  lds
s_add_u32 m0,s88,83968
s_add_u32 s61,s82,360448
buffer_load_dwordx4 v12,s[40:43],s61 offen offset:0  lds
s_add_u32 m0,s88,84992
ds_read_b64_tr_b16 a[2:3],v14 offset:512
buffer_load_dwordx4 v13,s[40:43],s61 offen offset:0  lds
ds_read_b64_tr_b16 a[4:5],v14 offset:8
ds_read_b64_tr_b16 a[6:7],v14 offset:520
ds_read_b64_tr_b16 a[8:9],v16 offset:0
ds_read_b64_tr_b16 a[10:11],v16 offset:512
ds_read_b64_tr_b16 a[12:13],v16 offset:8
ds_read_b64_tr_b16 a[14:15],v16 offset:520
s_waitcnt vmcnt(16)
s_add_u32 m0,s88,98304
s_add_u32 s61,s82,393216
buffer_load_dwordx4 v12,s[40:43],s61 offen offset:0  lds
s_add_u32 m0,s88,99328
ds_read_b64_tr_b16 a[16:17],v14 offset:16384
buffer_load_dwordx4 v13,s[40:43],s61 offen offset:0  lds
s_add_u32 m0,s88,100352
s_add_u32 s61,s82,425984
buffer_load_dwordx4 v12,s[40:43],s61 offen offset:0  lds
s_add_u32 m0,s88,101376
ds_read_b64_tr_b16 a[18:19],v14 offset:16896
buffer_load_dwordx4 v13,s[40:43],s61 offen offset:0  lds
ds_read_b64_tr_b16 a[20:21],v14 offset:16392
ds_read_b64_tr_b16 a[22:23],v14 offset:16904
ds_read_b64_tr_b16 a[24:25],v16 offset:16384
ds_read_b64_tr_b16 a[26:27],v16 offset:16896
ds_read_b64_tr_b16 a[28:29],v16 offset:16392
ds_read_b64_tr_b16 a[30:31],v16 offset:16904
s_waitcnt lgkmcnt(8)
s_waitcnt vmcnt(16)
s_add_u32 m0,s88,0
s_add_u32 s61,s82,458752
buffer_load_dwordx4 v12,s[40:43],s61 offen offset:0  lds
s_add_u32 m0,s88,1024
ds_read_b64_tr_b16 a[32:33],v14 offset:32768
buffer_load_dwordx4 v13,s[40:43],s61 offen offset:0  lds
s_add_u32 m0,s88,2048
s_add_u32 s61,s82,491520
buffer_load_dwordx4 v12,s[40:43],s61 offen offset:0  lds
s_add_u32 m0,s88,3072
ds_read_b64_tr_b16 a[34:35],v14 offset:33280
buffer_load_dwordx4 v13,s[40:43],s61 offen offset:0  lds
ds_read_b64_tr_b16 a[36:37],v14 offset:32776
ds_read_b64_tr_b16 a[38:39],v14 offset:33288
ds_read_b64_tr_b16 a[40:41],v16 offset:32768
ds_read_b64_tr_b16 a[42:43],v16 offset:33280
ds_read_b64_tr_b16 a[44:45],v16 offset:32776
ds_read_b64_tr_b16 a[46:47],v16 offset:33288
s_waitcnt lgkmcnt(8)
s_waitcnt vmcnt(16)
s_add_u32 m0,s88,16384
s_add_u32 s61,s82,524288
buffer_load_dwordx4 v12,s[40:43],s61 offen offset:0  lds
s_add_u32 m0,s88,17408
ds_read_b64_tr_b16 a[48:49],v28 offset:0
buffer_load_dwordx4 v13,s[40:43],s61 offen offset:0  lds
s_add_u32 m0,s88,18432
s_add_u32 s61,s82,557056
buffer_load_dwordx4 v12,s[40:43],s61 offen offset:0  lds
s_add_u32 m0,s88,19456
ds_read_b64_tr_b16 a[50:51],v28 offset:512
buffer_load_dwordx4 v13,s[40:43],s61 offen offset:0  lds
ds_read_b64_tr_b16 a[52:53],v28 offset:8
ds_read_b64_tr_b16 a[54:55],v28 offset:520
ds_read_b64_tr_b16 a[56:57],v30 offset:0
ds_read_b64_tr_b16 a[58:59],v30 offset:512
ds_read_b64_tr_b16 a[60:61],v30 offset:8
ds_read_b64_tr_b16 a[62:63],v30 offset:520
s_waitcnt lgkmcnt(8)
s_waitcnt vmcnt(16)
s_add_u32 m0,s88,32768
s_add_u32 s61,s82,589824
buffer_load_dwordx4 v12,s[40:43],s61 offen offset:0  lds
s_add_u32 m0,s88,33792
ds_read_b64_tr_b16 a[64:65],v28 offset:16384
buffer_load_dwordx4 v13,s[40:43],s61 offen offset:0  lds
s_add_u32 m0,s88,34816
s_add_u32 s61,s82,622592
buffer_load_dwordx4 v12,s[40:43],s61 offen offset:0  lds
s_add_u32 m0,s88,35840
ds_read_b64_tr_b16 a[66:67],v28 offset:16896
buffer_load_dwordx4 v13,s[40:43],s61 offen offset:0  lds
ds_read_b64_tr_b16 a[68:69],v28 offset:16392
ds_read_b64_tr_b16 a[70:71],v28 offset:16904
ds_read_b64_tr_b16 a[72:73],v30 offset:16384
ds_read_b64_tr_b16 a[74:75],v30 offset:16896
ds_read_b64_tr_b16 a[76:77],v30 offset:16392
ds_read_b64_tr_b16 a[78:79],v30 offset:16904
s_waitcnt lgkmcnt(8)
s_waitcnt vmcnt(16)
s_add_u32 m0,s88,49152
s_add_u32 s61,s82,655360
buffer_load_dwordx4 v12,s[40:43],s61 offen offset:0  lds
s_add_u32 m0,s88,50176)"
R"(
ds_read_b64_tr_b16 a[80:81],v28 offset:32768
buffer_load_dwordx4 v13,s[40:43],s61 offen offset:0  lds
s_add_u32 m0,s88,51200
s_add_u32 s61,s82,688128
buffer_load_dwordx4 v12,s[40:43],s61 offen offset:0  lds
s_add_u32 m0,s88,52224
ds_read_b64_tr_b16 a[82:83],v28 offset:33280
buffer_load_dwordx4 v13,s[40:43],s61 offen offset:0  lds
ds_read_b64_tr_b16 a[84:85],v28 offset:32776
ds_read_b64_tr_b16 a[86:87],v28 offset:33288
ds_read_b64_tr_b16 a[88:89],v30 offset:32768
ds_read_b64_tr_b16 a[90:91],v30 offset:33280
ds_read_b64_tr_b16 a[92:93],v30 offset:32776
ds_read_b64_tr_b16 a[94:95],v30 offset:33288
s_waitcnt lgkmcnt(8)
s_waitcnt vmcnt(16)
s_add_u32 m0,s88,65536
s_add_u32 s61,s82,720896
buffer_load_dwordx4 v12,s[40:43],s61 offen offset:0  lds
s_add_u32 m0,s88,66560
ds_read_b64_tr_b16 a[96:97],v32 offset:0
buffer_load_dwordx4 v13,s[40:43],s61 offen offset:0  lds
s_add_u32 m0,s88,67584
s_add_u32 s61,s82,753664
buffer_load_dwordx4 v12,s[40:43],s61 offen offset:0  lds
s_add_u32 m0,s88,68608
ds_read_b64_tr_b16 a[98:99],v32 offset:512
buffer_load_dwordx4 v13,s[40:43],s61 offen offset:0  lds
ds_read_b64_tr_b16 a[100:101],v32 offset:8
ds_read_b64_tr_b16 a[102:103],v32 offset:520
ds_read_b64_tr_b16 a[104:105],v34 offset:0
ds_read_b64_tr_b16 a[106:107],v34 offset:512
ds_read_b64_tr_b16 a[108:109],v34 offset:8
ds_read_b64_tr_b16 a[110:111],v34 offset:520
s_waitcnt lgkmcnt(8)
s_waitcnt vmcnt(16)
s_add_u32 m0,s88,81920
s_add_u32 s61,s82,786432
buffer_load_dwordx4 v12,s[40:43],s61 offen offset:0  lds
s_add_u32 m0,s88,82944
ds_read_b64_tr_b16 a[112:113],v14 offset:0
buffer_load_dwordx4 v13,s[40:43],s61 offen offset:0  lds
s_add_u32 m0,s88,83968
s_add_u32 s61,s82,819200
buffer_load_dwordx4 v12,s[40:43],s61 offen offset:0  lds
s_add_u32 m0,s88,84992
ds_read_b64_tr_b16 a[114:115],v14 offset:512
buffer_load_dwordx4 v13,s[40:43],s61 offen offset:0  lds
ds_read_b64_tr_b16 a[116:117],v14 offset:8
ds_read_b64_tr_b16 a[118:119],v14 offset:520
ds_read_b64_tr_b16 a[120:121],v16 offset:0
ds_read_b64_tr_b16 a[122:123],v16 offset:512
ds_read_b64_tr_b16 a[124:125],v16 offset:8
ds_read_b64_tr_b16 a[126:127],v16 offset:520
s_waitcnt lgkmcnt(8)
s_waitcnt vmcnt(16)
s_add_u32 m0,s88,98304
s_add_u32 s61,s82,851968
buffer_load_dwordx4 v12,s[40:43],s61 offen offset:0  lds
s_add_u32 m0,s88,99328
ds_read_b64_tr_b16 a[128:129],v14 offset:16384
buffer_load_dwordx4 v13,s[40:43],s61 offen offset:0  lds
s_add_u32 m0,s88,100352
s_add_u32 s61,s82,884736
buffer_load_dwordx4 v12,s[40:43],s61 offen offset:0  lds
s_add_u32 m0,s88,101376
ds_read_b64_tr_b16 a[130:131],v14 offset:16896
buffer_load_dwordx4 v13,s[40:43],s61 offen offset:0  lds
ds_read_b64_tr_b16 a[132:133],v14 offset:16392
ds_read_b64_tr_b16 a[134:135],v14 offset:16904
ds_read_b64_tr_b16 a[136:137],v16 offset:16384
ds_read_b64_tr_b16 a[138:139],v16 offset:16896
ds_read_b64_tr_b16 a[140:141],v16 offset:16392
ds_read_b64_tr_b16 a[142:143],v16 offset:16904
s_waitcnt lgkmcnt(8)
s_waitcnt vmcnt(16)
s_add_u32 m0,s88,0
s_add_u32 s61,s82,917504
buffer_load_dwordx4 v12,s[40:43],s61 offen offset:0  lds
s_add_u32 m0,s88,1024
ds_read_b64_tr_b16 a[144:145],v14 offset:32768
buffer_load_dwordx4 v13,s[40:43],s61 offen offset:0  lds
s_add_u32 m0,s88,2048
s_add_u32 s61,s82,950272
buffer_load_dwordx4 v12,s[40:43],s61 offen offset:0  lds
s_add_u32 m0,s88,3072
ds_read_b64_tr_b16 a[146:147],v14 offset:33280
buffer_load_dwordx4 v13,s[40:43],s61 offen offset:0  lds
ds_read_b64_tr_b16 a[148:149],v14 offset:32776
ds_read_b64_tr_b16 a[150:151],v14 offset:33288
ds_read_b64_tr_b16 a[152:153],v16 offset:32768
ds_read_b64_tr_b16 a[154:155],v16 offset:33280
ds_read_b64_tr_b16 a[156:157],v16 offset:32776
ds_read_b64_tr_b16 a[158:159],v16 offset:33288
s_waitcnt lgkmcnt(8)
s_waitcnt vmcnt(16)
s_add_u32 m0,s88,16384
s_add_u32 s61,s82,983040
buffer_load_dwordx4 v12,s[40:43],s61 offen offset:0  lds
s_add_u32 m0,s88,17408
ds_read_b64_tr_b16 a[160:161],v28 offset:0
buffer_load_dwordx4 v13,s[40:43],s61 offen offset:0  lds
s_add_u32 m0,s88,18432
s_add_u32 s61,s82,1015808
buffer_load_dwordx4 v12,s[40:43],s61 offen offset:0  lds
s_add_u32 m0,s88,19456
ds_read_b64_tr_b16 a[162:163],v28 offset:512
buffer_load_dwordx4 v13,s[40:43],s61 offen offset:0  lds
ds_read_b64_tr_b16 a[164:165],v28 offset:8
ds_read_b64_tr_b16 a[166:167],v28 offset:520
ds_read_b64_tr_b16 a[168:169],v30 offset:0
ds_read_b64_tr_b16 a[170:171],v30 offset:512
ds_read_b64_tr_b16 a[172:173],v30 offset:8
ds_read_b64_tr_b16 a[174:175],v30 offset:520
s_waitcnt lgkmcnt(8)
s_waitcnt vmcnt(16)
s_add_u32 m0,s88,32768
s_add_u32 s61,s82,1048576
buffer_load_dwordx4 v12,s[40:43],s61 offen offset:0  lds
s_add_u32 m0,s88,33792
ds_read_b64_tr_b16 a[176:177],v28 offset:16384
buffer_load_dwordx4 v13,s[40:43],s61 offen offset:0  lds
s_add_u32 m0,s88,34816
s_add_u32 s61,s82,1081344
buffer_load_dwordx4 v12,s[40:43],s61 offen offset:0  lds
s_add_u32 m0,s88,35840
ds_read_b64_tr_b16 a[178:179],v28 offset:16896
buffer_load_dwordx4 v13,s[40:43],s61 offen offset:0  lds
ds_read_b64_tr_b16 a[180:181],v28 offset:16392
ds_read_b64_tr_b16 a[182:183],v28 offset:16904
ds_read_b64_tr_b16 a[184:185],v30 offset:16384
ds_read_b64_tr_b16 a[186:187],v30 offset:16896
ds_read_b64_tr_b16 a[188:189],v30 offset:16392
ds_read_b64_tr_b16 a[190:191],v30 offset:16904
s_waitcnt lgkmcnt(8)
s_waitcnt vmcnt(16)
s_add_u32 m0,s88,49152
s_add_u32 s61,s82,1114112
buffer_load_dwordx4 v12,s[40:43],s61 offen offset:0  lds
s_add_u32 m0,s88,50176
ds_read_b64_tr_b16 a[192:193],v28 offset:32768
buffer_load_dwordx4 v13,s[40:43],s61 offen offset:0  lds
s_add_u32 m0,s88,51200
s_add_u32 s61,s82,1146880
buffer_load_dwordx4 v12,s[40:43],s61 offen offset:0  lds
s_add_u32 m0,s88,52224
ds_read_b64_tr_b16 a[194:195],v28 offset:33280
buffer_load_dwordx4 v13,s[40:43],s61 offen offset:0  lds
ds_read_b64_tr_b16 a[196:197],v28 offset:32776
ds_read_b64_tr_b16 a[198:199],v28 offset:33288
ds_read_b64_tr_b16 a[200:201],v30 offset:32768
ds_read_b64_tr_b16 a[202:203],v30 offset:33280
ds_read_b64_tr_b16 a[204:205],v30 offset:32776
ds_read_b64_tr_b16 a[206:207],v30 offset:33288
s_waitcnt lgkmcnt(8)
s_waitcnt vmcnt(16)
s_add_u32 m0,s88,65536
s_add_u32 s61,s82,1179648
buffer_load_dwordx4 v12,s[40:43],s61 offen offset:0  lds
s_add_u32 m0,s88,66560
ds_read_b64_tr_b16 a[208:209],v32 offset:0
buffer_load_dwordx4 v13,s[40:43],s61 offen offset:0  lds
s_add_u32 m0,s88,67584
s_add_u32 s61,s82,1212416
buffer_load_dwordx4 v12,s[40:43],s61 offen offset:0  lds
s_add_u32 m0,s88,68608
ds_read_b64_tr_b16 a[210:211],v32 offset:512
buffer_load_dwordx4 v13,s[40:43],s61 offen offset:0  lds
ds_read_b64_tr_b16 a[212:213],v32 offset:8
ds_read_b64_tr_b16 a[214:215],v32 offset:520
ds_read_b64_tr_b16 a[216:217],v34 offset:0
ds_read_b64_tr_b16 a[218:219],v34 offset:512
ds_read_b64_tr_b16 a[220:221],v34 offset:8
ds_read_b64_tr_b16 a[222:223],v34 offset:520
s_waitcnt lgkmcnt(8)
s_waitcnt vmcnt(16)
s_add_u32 m0,s88,81920
s_add_u32 s61,s82,1245184
buffer_load_dwordx4 v12,s[40:43],s61 offen offset:0  lds
s_add_u32 m0,s88,82944
ds_read_b64_tr_b16 a[224:225],v14 offset:0
buffer_load_dwordx4 v13,s[40:43],s61 offen offset:0  lds
s_add_u32 m0,s88,83968
s_add_u32 s61,s82,1277952
buffer_load_dwordx4 v12,s[40:43],s61 offen offset:0  lds
s_add_u32 m0,s88,84992
ds_read_b64_tr_b16 a[226:227],v14 offset:512
buffer_load_dwordx4 v13,s[40:43],s61 offen offset:0  lds
ds_read_b64_tr_b16 a[228:229],v14 offset:8
ds_read_b64_tr_b16 a[230:231],v14 offset:520
ds_read_b64_tr_b16 a[232:233],v16 offset:0
ds_read_b64_tr_b16 a[234:235],v16 offset:512
ds_read_b64_tr_b16 a[236:237],v16 offset:8
ds_read_b64_tr_b16 a[238:239],v16 offset:520
s_waitcnt lgkmcnt(8)
s_waitcnt vmcnt(16)
s_add_u32 m0,s88,98304
s_add_u32 s61,s82,1310720
buffer_load_dwordx4 v12,s[40:43],s61 offen offset:0  lds
s_add_u32 m0,s88,99328)"
R"(
ds_read_b64_tr_b16 a[240:241],v14 offset:16384
buffer_load_dwordx4 v13,s[40:43],s61 offen offset:0  lds
s_add_u32 m0,s88,100352
s_add_u32 s61,s82,1343488
buffer_load_dwordx4 v12,s[40:43],s61 offen offset:0  lds
s_add_u32 m0,s88,101376
ds_read_b64_tr_b16 a[242:243],v14 offset:16896
buffer_load_dwordx4 v13,s[40:43],s61 offen offset:0  lds
ds_read_b64_tr_b16 a[244:245],v14 offset:16392
ds_read_b64_tr_b16 a[246:247],v14 offset:16904
ds_read_b64_tr_b16 a[248:249],v16 offset:16384
ds_read_b64_tr_b16 a[250:251],v16 offset:16896
ds_read_b64_tr_b16 a[252:253],v16 offset:16392
ds_read_b64_tr_b16 a[254:255],v16 offset:16904
s_waitcnt lgkmcnt(8)
s_waitcnt vmcnt(16)
s_add_u32 m0,s88,0
s_add_u32 s61,s82,1376256
buffer_load_dwordx4 v12,s[40:43],s61 offen offset:0  lds
s_add_u32 m0,s88,1024
ds_read_b64_tr_b16 v[100:101],v14 offset:32768
buffer_load_dwordx4 v13,s[40:43],s61 offen offset:0  lds
s_add_u32 m0,s88,2048
s_add_u32 s61,s82,1409024
buffer_load_dwordx4 v12,s[40:43],s61 offen offset:0  lds
s_add_u32 m0,s88,3072
ds_read_b64_tr_b16 v[102:103],v14 offset:33280
buffer_load_dwordx4 v13,s[40:43],s61 offen offset:0  lds
ds_read_b64_tr_b16 v[104:105],v14 offset:32776
ds_read_b64_tr_b16 v[106:107],v14 offset:33288
ds_read_b64_tr_b16 v[108:109],v16 offset:32768
ds_read_b64_tr_b16 v[110:111],v16 offset:33280
ds_read_b64_tr_b16 v[112:113],v16 offset:32776
ds_read_b64_tr_b16 v[114:115],v16 offset:33288
s_waitcnt lgkmcnt(8)
s_waitcnt vmcnt(16)
s_add_u32 m0,s88,16384
s_add_u32 s61,s82,1441792
buffer_load_dwordx4 v12,s[40:43],s61 offen offset:0  lds
s_add_u32 m0,s88,17408
ds_read_b64_tr_b16 v[116:117],v28 offset:0
buffer_load_dwordx4 v13,s[40:43],s61 offen offset:0  lds
s_add_u32 m0,s88,18432
s_add_u32 s61,s82,1474560
buffer_load_dwordx4 v12,s[40:43],s61 offen offset:0  lds
s_add_u32 m0,s88,19456
ds_read_b64_tr_b16 v[118:119],v28 offset:512
buffer_load_dwordx4 v13,s[40:43],s61 offen offset:0  lds
ds_read_b64_tr_b16 v[120:121],v28 offset:8
ds_read_b64_tr_b16 v[122:123],v28 offset:520
ds_read_b64_tr_b16 v[124:125],v30 offset:0
ds_read_b64_tr_b16 v[126:127],v30 offset:512
ds_read_b64_tr_b16 v[128:129],v30 offset:8
ds_read_b64_tr_b16 v[130:131],v30 offset:520
s_waitcnt lgkmcnt(8)
s_waitcnt vmcnt(16)
s_add_u32 m0,s88,32768
s_add_u32 s61,s82,1507328
buffer_load_dwordx4 v12,s[40:43],s61 offen offset:0  lds
s_add_u32 m0,s88,33792
ds_read_b64_tr_b16 v[132:133],v28 offset:16384
buffer_load_dwordx4 v13,s[40:43],s61 offen offset:0  lds
s_add_u32 m0,s88,34816
s_add_u32 s61,s82,1540096
buffer_load_dwordx4 v12,s[40:43],s61 offen offset:0  lds
s_add_u32 m0,s88,35840
ds_read_b64_tr_b16 v[134:135],v28 offset:16896
buffer_load_dwordx4 v13,s[40:43],s61 offen offset:0  lds
ds_read_b64_tr_b16 v[136:137],v28 offset:16392
ds_read_b64_tr_b16 v[138:139],v28 offset:16904
ds_read_b64_tr_b16 v[140:141],v30 offset:16384
ds_read_b64_tr_b16 v[142:143],v30 offset:16896
ds_read_b64_tr_b16 v[144:145],v30 offset:16392
ds_read_b64_tr_b16 v[146:147],v30 offset:16904
s_waitcnt lgkmcnt(8)
s_waitcnt vmcnt(16)
s_add_u32 m0,s88,49152
s_add_u32 s61,s82,1572864
buffer_load_dwordx4 v12,s[40:43],s61 offen offset:0  lds
s_add_u32 m0,s88,50176
ds_read_b64_tr_b16 v[148:149],v28 offset:32768
buffer_load_dwordx4 v13,s[40:43],s61 offen offset:0  lds
s_add_u32 m0,s88,51200
s_add_u32 s61,s82,1605632
buffer_load_dwordx4 v12,s[40:43],s61 offen offset:0  lds
s_add_u32 m0,s88,52224
ds_read_b64_tr_b16 v[150:151],v28 offset:33280
buffer_load_dwordx4 v13,s[40:43],s61 offen offset:0  lds
ds_read_b64_tr_b16 v[152:153],v28 offset:32776
ds_read_b64_tr_b16 v[154:155],v28 offset:33288
ds_read_b64_tr_b16 v[156:157],v30 offset:32768
ds_read_b64_tr_b16 v[158:159],v30 offset:33280
ds_read_b64_tr_b16 v[52:53],v30 offset:32776
ds_read_b64_tr_b16 v[54:55],v30 offset:33288
s_waitcnt lgkmcnt(8)
s_waitcnt vmcnt(16)
s_add_u32 m0,s88,65536
s_add_u32 s61,s82,1638400
buffer_load_dwordx4 v12,s[40:43],s61 offen offset:0  lds
s_add_u32 m0,s88,66560
ds_read_b64_tr_b16 v[160:161],v32 offset:0
buffer_load_dwordx4 v13,s[40:43],s61 offen offset:0  lds
s_add_u32 m0,s88,67584
s_add_u32 s61,s82,1671168
buffer_load_dwordx4 v12,s[40:43],s61 offen offset:0  lds
s_add_u32 m0,s88,68608
ds_read_b64_tr_b16 v[162:163],v32 offset:512
buffer_load_dwordx4 v13,s[40:43],s61 offen offset:0  lds
ds_read_b64_tr_b16 v[164:165],v32 offset:8
ds_read_b64_tr_b16 v[166:167],v32 offset:520
ds_read_b64_tr_b16 v[168:169],v34 offset:0
ds_read_b64_tr_b16 v[170:171],v34 offset:512
ds_read_b64_tr_b16 v[44:45],v34 offset:8
ds_read_b64_tr_b16 v[46:47],v34 offset:520
s_waitcnt lgkmcnt(8)
ds_write_b128 v10,v[52:55] offset:0
s_waitcnt vmcnt(16)
s_add_u32 m0,s88,81920
s_add_u32 s61,s82,1703936
buffer_load_dwordx4 v12,s[40:43],s61 offen offset:0  lds
s_add_u32 m0,s88,82944
ds_read_b64_tr_b16 v[172:173],v14 offset:0
buffer_load_dwordx4 v13,s[40:43],s61 offen offset:0  lds
s_add_u32 m0,s88,83968
s_add_u32 s61,s82,1736704
buffer_load_dwordx4 v12,s[40:43],s61 offen offset:0  lds
s_add_u32 m0,s88,84992
ds_read_b64_tr_b16 v[174:175],v14 offset:512
buffer_load_dwordx4 v13,s[40:43],s61 offen offset:0  lds
ds_read_b64_tr_b16 v[176:177],v14 offset:8
ds_read_b64_tr_b16 v[178:179],v14 offset:520
ds_read_b64_tr_b16 v[180:181],v16 offset:0
ds_read_b64_tr_b16 v[182:183],v16 offset:512
ds_read_b64_tr_b16 v[52:53],v16 offset:8
ds_read_b64_tr_b16 v[54:55],v16 offset:520
s_waitcnt lgkmcnt(8)
ds_write_b128 v10,v[44:47] offset:1024
s_waitcnt vmcnt(16)
s_add_u32 m0,s88,98304
s_add_u32 s61,s82,1769472
buffer_load_dwordx4 v12,s[40:43],s61 offen offset:0  lds
s_add_u32 m0,s88,99328
ds_read_b64_tr_b16 v[184:185],v14 offset:16384
buffer_load_dwordx4 v13,s[40:43],s61 offen offset:0  lds
s_add_u32 m0,s88,100352
s_add_u32 s61,s82,1802240
buffer_load_dwordx4 v12,s[40:43],s61 offen offset:0  lds
s_add_u32 m0,s88,101376
ds_read_b64_tr_b16 v[186:187],v14 offset:16896
buffer_load_dwordx4 v13,s[40:43],s61 offen offset:0  lds
ds_read_b64_tr_b16 v[188:189],v14 offset:16392
ds_read_b64_tr_b16 v[190:191],v14 offset:16904
ds_read_b64_tr_b16 v[192:193],v16 offset:16384
ds_read_b64_tr_b16 v[194:195],v16 offset:16896
ds_read_b64_tr_b16 v[44:45],v16 offset:16392
ds_read_b64_tr_b16 v[46:47],v16 offset:16904
s_waitcnt lgkmcnt(8)
ds_write_b128 v10,v[52:55] offset:2048
s_waitcnt vmcnt(16)
ds_read_b64_tr_b16 v[196:197],v14 offset:32768
ds_read_b64_tr_b16 v[198:199],v14 offset:33280
ds_read_b64_tr_b16 v[200:201],v14 offset:32776
ds_read_b64_tr_b16 v[202:203],v14 offset:33288
ds_read_b64_tr_b16 v[204:205],v16 offset:32768
ds_read_b64_tr_b16 v[206:207],v16 offset:33280
ds_read_b64_tr_b16 v[52:53],v16 offset:32776
ds_read_b64_tr_b16 v[54:55],v16 offset:33288
s_getpc_b64 s[46:47]
Lg4_cp_%=:
s_add_u32 s46,s46,Lg4_init_%=-Lg4_cp_%=
s_addc_u32 s47,s47,0
s_lshl_b32 s68,s54,13
v_lshl_add_u32 v76,v2,3,s68
v_min_u32 v77,Lg4_done_%=-Lg4_init_%=,v76
global_load_dword v80,v77,s[46:47]
s_waitcnt lgkmcnt(8)
ds_write_b128 v10,v[44:47] offset:3072
s_waitcnt vmcnt(13)
ds_read_b64_tr_b16 v[208:209],v28 offset:0
ds_read_b64_tr_b16 v[210:211],v28 offset:512
ds_read_b64_tr_b16 v[212:213],v28 offset:8
ds_read_b64_tr_b16 v[214:215],v28 offset:520
ds_read_b64_tr_b16 v[216:217],v30 offset:0
ds_read_b64_tr_b16 v[218:219],v30 offset:512
ds_read_b64_tr_b16 v[44:45],v30 offset:8
ds_read_b64_tr_b16 v[46:47],v30 offset:520
s_waitcnt lgkmcnt(8)
ds_write_b128 v10,v[52:55] offset:4096
s_waitcnt vmcnt(9)
ds_read_b64_tr_b16 v[220:221],v28 offset:16384
ds_read_b64_tr_b16 v[222:223],v28 offset:16896
ds_read_b64_tr_b16 v[224:225],v28 offset:16392
ds_read_b64_tr_b16 v[226:227],v28 offset:16904
ds_read_b64_tr_b16 v[228:229],v30 offset:16384
ds_read_b64_tr_b16 v[230:231],v30 offset:16896
ds_read_b64_tr_b16 v[52:53],v30 offset:16392
ds_read_b64_tr_b16 v[54:55],v30 offset:16904
s_waitcnt lgkmcnt(8)
ds_write_b128 v10,v[44:47] offset:5120
s_waitcnt vmcnt(5))"
R"(
ds_read_b64_tr_b16 v[232:233],v28 offset:32768
ds_read_b64_tr_b16 v[234:235],v28 offset:33280
ds_read_b64_tr_b16 v[236:237],v28 offset:32776
ds_read_b64_tr_b16 v[238:239],v28 offset:33288
ds_read_b64_tr_b16 v[240:241],v30 offset:32768
ds_read_b64_tr_b16 v[242:243],v30 offset:33280
ds_read_b64_tr_b16 v[44:45],v30 offset:32776
ds_read_b64_tr_b16 v[46:47],v30 offset:33288
s_waitcnt lgkmcnt(8)
ds_write_b128 v10,v[52:55] offset:6144
s_waitcnt vmcnt(1)
ds_read_b64_tr_b16 v[244:245],v32 offset:0
ds_read_b64_tr_b16 v[246:247],v32 offset:512
ds_read_b64_tr_b16 v[248:249],v32 offset:8
ds_read_b64_tr_b16 v[250:251],v32 offset:520
ds_read_b64_tr_b16 v[252:253],v34 offset:0
ds_read_b64_tr_b16 v[254:255],v34 offset:512
ds_read_b64_tr_b16 v[52:53],v34 offset:8
ds_read_b64_tr_b16 v[54:55],v34 offset:520
s_waitcnt lgkmcnt(8)
ds_write_b128 v10,v[44:47] offset:7168
s_waitcnt lgkmcnt(0)
ds_write_b128 v10,v[52:55] offset:8192
s_waitcnt lgkmcnt(0)
s_barrier
s_mov_b32 s60,s84
Lg4_init_%=:
v_mov_b32 v6,s60
s_mov_b32 s59,536870912
s_add_u32 s57,s56,131072
s_mov_b32 s79,0
s_mov_b32 s75,0x3a800000
s_mov_b32 s76,0xc0135761
s_mov_b32 s77,0x3d372713
s_mov_b32 s44,s59
s_mov_b32 s45,s59
s_mov_b32 s46,s59
s_mov_b32 s47,s59
s_mov_b32 s48,s83
s_add_u32 s49,s48,s55
s_cmp_eq_u32 s49,s56
s_cselect_b32 s49,s57,s49
s_cmp_eq_u32 s49,s58
s_cselect_b32 s49,s59,s49
s_cmp_ge_u32 s49,6520832
s_cselect_b32 s55,196608,s55
s_add_u32 s50,s49,s55
s_cmp_eq_u32 s50,s56
s_cselect_b32 s50,s57,s50
s_cmp_eq_u32 s50,s58
s_cselect_b32 s50,s59,s50
s_cmp_ge_u32 s50,6520832
s_cselect_b32 s55,196608,s55
s_add_u32 s51,s50,s55
s_cmp_eq_u32 s51,s56
s_cselect_b32 s51,s57,s51
s_cmp_eq_u32 s51,s58
s_cselect_b32 s51,s59,s51
s_cmp_ge_u32 s51,6520832
s_cselect_b32 s55,196608,s55
s_add_u32 s52,s51,s55
s_cmp_eq_u32 s52,s56
s_cselect_b32 s52,s57,s52
s_cmp_eq_u32 s52,s58
s_cselect_b32 s52,s59,s52
s_cmp_ge_u32 s52,6520832
s_cselect_b32 s55,196608,s55
s_lshl_b32 s63,s54,12
s_mul_i32 s64,s54,3072
s_add_u32 s64,s64,16384
s_lshl_b32 s78,s54,8
s_add_u32 s78,s78,65536
s_getpc_b64 s[90:91]
Lg4_pc_%=:
s_mov_b32 s68,Lg4_d1w0_%=-Lg4_pc_%=
s_cmp_eq_u32 s54,1
s_cselect_b32 s68,Lg4_d1w1_%=-Lg4_pc_%=,s68
s_cmp_eq_u32 s54,2
s_cselect_b32 s68,Lg4_d1w2_%=-Lg4_pc_%=,s68
s_cmp_eq_u32 s54,3
s_cselect_b32 s68,Lg4_d1w3_%=-Lg4_pc_%=,s68
s_add_u32 s92,s90,s68
s_addc_u32 s93,s91,0
s_mov_b32 s68,Lg4_d0w0_%=-Lg4_pc_%=
s_cmp_eq_u32 s54,1
s_cselect_b32 s68,Lg4_d0w1_%=-Lg4_pc_%=,s68
s_cmp_eq_u32 s54,2
s_cselect_b32 s68,Lg4_d0w2_%=-Lg4_pc_%=,s68
s_cmp_eq_u32 s54,3
s_cselect_b32 s68,Lg4_d0w3_%=-Lg4_pc_%=,s68
s_add_u32 s90,s90,s68
s_addc_u32 s91,s91,0
s_mov_b32 s66,s59
s_mov_b32 s65,0
s_mov_b32 s62,s63
s_branch Lg4_slowb0_%=
Lg4_d0w0_%=:
v_mfma_f32_16x16x32_bf16 v[12:15],a[0:3],v[44:47],0
s_mov_b32 s73,s47
ds_read_b128 v[56:59],v2 offset:3072
v_lshlrev_b32 v60,16,v68
v_mfma_f32_16x16x32_bf16 v[16:19],a[4:7],v[44:47],0
s_max_u32 s73,s73,s65
v_and_b32 v61,0xffff0000,v68
v_lshlrev_b32 v62,16,v69
v_mfma_f32_16x16x32_bf16 v[20:23],a[8:11],v[44:47],0
s_mov_b32 s65,0
v_and_b32 v63,0xffff0000,v69
v_lshlrev_b32 v64,16,v70
v_mfma_f32_16x16x32_bf16 v[24:27],a[12:15],v[44:47],0
s_waitcnt lgkmcnt(2)
v_and_b32 v65,0xffff0000,v70
v_lshlrev_b32 v66,16,v71
v_mfma_f32_16x16x32_bf16 v[12:15],a[16:19],v[48:51],v[12:15]
s_mul_i32 s61,s49,7
ds_read_b128 v[44:47],v2 offset:4096
v_and_b32 v67,0xffff0000,v71
v_mfma_f32_16x16x32_bf16 v[16:19],a[20:23],v[48:51],v[16:19]
s_lshr_b32 s61,s61,1
v_cvt_pk_bf16_f32 v28,v28,v29
v_cvt_pk_bf16_f32 v29,v30,v31
v_mfma_f32_16x16x32_bf16 v[20:23],a[24:27],v[48:51],v[20:23]
v_cvt_pk_bf16_f32 v30,v32,v33
v_cvt_pk_bf16_f32 v31,v34,v35
v_mfma_f32_16x16x32_bf16 v[24:27],a[28:31],v[48:51],v[24:27]
s_waitcnt lgkmcnt(2)
v_add_f32 v36,v36,v60
v_add_f32 v37,v37,v61
v_mfma_f32_16x16x32_bf16 v[12:15],a[32:35],v[52:55],v[12:15]
buffer_store_dwordx4 v[28:31],v3,s[36:39],s73 offen offset:0 sc0 sc1
ds_read_b128 v[48:51],v2 offset:5120
v_add_f32 v38,v38,v62
v_add_f32 v39,v39,v63
v_mfma_f32_16x16x32_bf16 v[16:19],a[36:39],v[52:55],v[16:19]
v_add_f32 v40,v40,v64
v_add_f32 v41,v41,v65
v_mfma_f32_16x16x32_bf16 v[20:23],a[40:43],v[52:55],v[20:23]
v_add_f32 v42,v42,v66
v_add_f32 v43,v43,v67
v_mfma_f32_16x16x32_bf16 v[24:27],a[44:47],v[52:55],v[24:27]
s_waitcnt lgkmcnt(2)
v_cvt_pk_bf16_f32 v36,v36,v37
v_cvt_pk_bf16_f32 v37,v38,v39
v_mfma_f32_16x16x32_bf16 v[12:15],a[48:51],v[56:59],v[12:15]
ds_read_b128 v[52:55],v2 offset:6144
v_cvt_pk_bf16_f32 v38,v40,v41
v_mfma_f32_16x16x32_bf16 v[16:19],a[52:55],v[56:59],v[16:19]
v_cvt_pk_bf16_f32 v39,v42,v43
v_mfma_f32_16x16x32_bf16 v[20:23],a[56:59],v[56:59],v[20:23]
s_waitcnt vmcnt(1) lgkmcnt(2)
v_mfma_f32_16x16x32_bf16 v[24:27],a[60:63],v[56:59],v[24:27]
s_barrier
v_mfma_f32_16x16x32_bf16 v[12:15],a[64:67],v[44:47],v[12:15]
ds_read_b32 v11,v8 offset:4096
ds_read_b128 v[56:59],v2 offset:7168
v_mfma_f32_16x16x32_bf16 v[16:19],a[68:71],v[44:47],v[16:19]
v_mfma_f32_16x16x32_bf16 v[20:23],a[72:75],v[44:47],v[20:23]
v_mfma_f32_16x16x32_bf16 v[24:27],a[76:79],v[44:47],v[24:27]
s_waitcnt lgkmcnt(3)
v_mfma_f32_16x16x32_bf16 v[12:15],a[80:83],v[48:51],v[12:15]
s_waitcnt lgkmcnt(1)
buffer_store_dwordx4 v[36:39],v4,s[36:39],s73 offen offset:0 sc0 sc1
v_cmp_eq_u32 vcc,s60,v11
v_mfma_f32_16x16x32_bf16 v[16:19],a[84:87],v[48:51],v[16:19]
s_cmp_eq_u32 vcc_lo,-1
ds_read_b128 v[44:47],v2 offset:8192
s_cselect_b32 s66,s49,s59
v_mfma_f32_16x16x32_bf16 v[20:23],a[88:91],v[48:51],v[20:23]
s_cselect_b32 s61,s61,s59
s_add_u32 m0,s63,28672
s_cmp_ge_u32 s66,s59
v_mfma_f32_16x16x32_bf16 v[24:27],a[92:95],v[48:51],v[24:27]
s_cselect_b32 s68,s49,s59
s_mul_i32 s68,s68,5
v_mfma_f32_16x16x32_bf16 v[12:15],a[96:99],v[52:55],v[12:15]
buffer_load_dwordx4 v0,s[36:39],s61 offen offset:0 sc1 lds
v_mfma_f32_16x16x32_bf16 v[16:19],a[100:103],v[52:55],v[16:19]
s_lshr_b32 s68,s68,6
ds_read_b128 v[48:51],v2 offset:9216
v_mfma_f32_16x16x32_bf16 v[20:23],a[104:107],v[52:55],v[20:23]
v_mfma_f32_16x16x32_bf16 v[24:27],a[108:111],v[52:55],v[24:27]
s_waitcnt lgkmcnt(2)
v_mfma_f32_16x16x32_bf16 v[12:15],a[112:115],v[56:59],v[12:15]
buffer_load_dwordx4 v0,s[36:39],s61 offen offset:1024 sc1 lds
v_mfma_f32_16x16x32_bf16 v[16:19],a[116:119],v[56:59],v[16:19]
ds_read_b128 v[52:55],v2 offset:10240
v_mfma_f32_16x16x32_bf16 v[20:23],a[120:123],v[56:59],v[20:23]
v_mfma_f32_16x16x32_bf16 v[24:27],a[124:127],v[56:59],v[24:27]
s_waitcnt lgkmcnt(2)
v_mfma_f32_16x16x32_bf16 v[12:15],a[128:131],v[44:47],v[12:15]
buffer_load_dwordx4 v0,s[36:39],s61 offen offset:2048 sc1 lds
v_mfma_f32_16x16x32_bf16 v[16:19],a[132:135],v[44:47],v[16:19]
ds_read_b128 v[56:59],v2 offset:11264
v_mfma_f32_16x16x32_bf16 v[20:23],a[136:139],v[44:47],v[20:23]
v_mfma_f32_16x16x32_bf16 v[24:27],a[140:143],v[44:47],v[24:27]
s_waitcnt lgkmcnt(2)
v_mfma_f32_16x16x32_bf16 v[12:15],a[144:147],v[48:51],v[12:15]
buffer_load_dwordx4 v0,s[36:39],s61 offen offset:3072 sc1 lds
v_mfma_f32_16x16x32_bf16 v[16:19],a[148:151],v[48:51],v[16:19]
s_add_u32 m0,s78,6144)"
R"(
ds_read_b128 v[44:47],v2 offset:12288
s_add_u32 s53,s52,s55
v_mfma_f32_16x16x32_bf16 v[20:23],a[152:155],v[48:51],v[20:23]
s_cmp_eq_u32 s53,s56
s_cselect_b32 s53,s57,s53
s_cmp_eq_u32 s53,s58
v_mfma_f32_16x16x32_bf16 v[24:27],a[156:159],v[48:51],v[24:27]
s_waitcnt lgkmcnt(2)
s_cselect_b32 s53,s59,s53
s_cmp_ge_u32 s53,6520832
v_mfma_f32_16x16x32_bf16 v[12:15],a[160:163],v[52:55],v[12:15]
buffer_load_dwordx4 v[68:71],v9,s[36:39],s48 offen offset:0 sc1
v_mfma_f32_16x16x32_bf16 v[16:19],a[164:167],v[52:55],v[16:19]
s_cselect_b32 s55,196608,s55
ds_read_b128 v[48:51],v2 offset:13312
v_mfma_f32_16x16x32_bf16 v[20:23],a[168:171],v[52:55],v[20:23]
v_mfma_f32_16x16x32_bf16 v[24:27],a[172:175],v[52:55],v[24:27]
s_waitcnt lgkmcnt(2)
v_mfma_f32_16x16x32_bf16 v[12:15],a[176:179],v[56:59],v[12:15]
ds_read_b128 v[52:55],v2 offset:14336
v_mfma_f32_16x16x32_bf16 v[16:19],a[180:183],v[56:59],v[16:19]
buffer_load_dword v7,s[36:39],s68 offen offset:0 sc1 lds
v_mfma_f32_16x16x32_bf16 v[20:23],a[184:187],v[56:59],v[20:23]
s_add_u32 m0,s64,28672
s_mul_i32 s68,s50,5
s_lshr_b32 s68,s68,6
v_mfma_f32_16x16x32_bf16 v[24:27],a[188:191],v[56:59],v[24:27]
s_waitcnt lgkmcnt(2)
v_mfma_f32_16x16x32_bf16 v[12:15],a[192:195],v[44:47],v[12:15]
ds_read_b128 v[56:59],v2 offset:15360
v_mfma_f32_16x16x32_bf16 v[16:19],a[196:199],v[44:47],v[16:19]
v_mfma_f32_16x16x32_bf16 v[20:23],a[200:203],v[44:47],v[20:23]
s_waitcnt vmcnt(9) lgkmcnt(2)
v_mfma_f32_16x16x32_bf16 v[24:27],a[204:207],v[44:47],v[24:27]
s_barrier
v_mfma_f32_16x16x32_bf16 v[12:15],a[208:211],v[48:51],v[12:15]
ds_read_b128 v[44:47],v2 offset:16384
v_mfma_f32_16x16x32_bf16 v[16:19],a[212:215],v[48:51],v[16:19]
buffer_load_dwordx4 v1,s[36:39],s61 offen offset:0 sc1 lds
v_mfma_f32_16x16x32_bf16 v[20:23],a[216:219],v[48:51],v[20:23]
v_mfma_f32_16x16x32_bf16 v[24:27],a[220:223],v[48:51],v[24:27]
s_waitcnt lgkmcnt(2)
v_mfma_f32_16x16x32_bf16 v[12:15],a[224:227],v[52:55],v[12:15]
ds_read_b128 v[48:51],v2 offset:17408
v_mfma_f32_16x16x32_bf16 v[16:19],a[228:231],v[52:55],v[16:19]
v_mfma_f32_16x16x32_bf16 v[20:23],a[232:235],v[52:55],v[20:23]
v_mfma_f32_16x16x32_bf16 v[24:27],a[236:239],v[52:55],v[24:27]
s_waitcnt lgkmcnt(2)
buffer_load_dwordx4 v1,s[36:39],s61 offen offset:1024 sc1 lds
v_mfma_f32_16x16x32_bf16 v[12:15],a[240:243],v[56:59],v[12:15]
ds_read_b128 v[52:55],v2 offset:18432
v_mfma_f32_16x16x32_bf16 v[16:19],a[244:247],v[56:59],v[16:19]
v_mfma_f32_16x16x32_bf16 v[20:23],a[248:251],v[56:59],v[20:23]
v_mfma_f32_16x16x32_bf16 v[24:27],a[252:255],v[56:59],v[24:27]
s_waitcnt lgkmcnt(2)
v_mfma_f32_16x16x32_bf16 v[12:15],v[100:103],v[44:47],v[12:15]
ds_read_b128 v[56:59],v2 offset:19456
v_mfma_f32_16x16x32_bf16 v[16:19],v[104:107],v[44:47],v[16:19]
buffer_load_dwordx4 v1,s[36:39],s61 offen offset:2048 sc1 lds
v_mfma_f32_16x16x32_bf16 v[20:23],v[108:111],v[44:47],v[20:23]
s_add_u32 m0,s78,5120
ds_read_b128 v[88:91],v10 offset:0
s_cmp_ge_u32 s66,s59
v_mfma_f32_16x16x32_bf16 v[24:27],v[112:115],v[44:47],v[24:27]
s_waitcnt lgkmcnt(3)
v_mfma_f32_16x16x32_bf16 v[12:15],v[116:119],v[48:51],v[12:15]
ds_read_b128 v[44:47],v2 offset:20480
v_mfma_f32_16x16x32_bf16 v[16:19],v[120:123],v[48:51],v[16:19]
ds_read_b128 v[76:79],v10 offset:1024
v_mfma_f32_16x16x32_bf16 v[20:23],v[124:127],v[48:51],v[20:23]
v_mfma_f32_16x16x32_bf16 v[24:27],v[128:131],v[48:51],v[24:27]
s_waitcnt lgkmcnt(4)
v_mfma_f32_16x16x32_bf16 v[12:15],v[132:135],v[52:55],v[12:15]
buffer_load_dword v7,s[36:39],s68 offen offset:0 sc1 lds
v_mfma_f32_16x16x32_bf16 v[16:19],v[136:139],v[52:55],v[16:19]
ds_read_b128 v[48:51],v2 offset:21504
ds_read_b128 v[80:83],v10 offset:2048
v_mfma_f32_16x16x32_bf16 v[20:23],v[140:143],v[52:55],v[20:23]
v_mfma_f32_16x16x32_bf16 v[24:27],v[144:147],v[52:55],v[24:27]
s_waitcnt lgkmcnt(4)
v_mfma_f32_16x16x32_bf16 v[12:15],v[148:151],v[56:59],v[12:15]
ds_read_b128 v[52:55],v2 offset:22528
v_mfma_f32_16x16x32_bf16 v[16:19],v[152:155],v[56:59],v[16:19]
ds_read_b128 v[84:87],v10 offset:3072
v_mfma_f32_16x16x32_bf16 v[20:23],v[156:159],v[56:59],v[20:23]
v_mfma_f32_16x16x32_bf16 v[24:27],v[88:91],v[56:59],v[24:27]
s_waitcnt lgkmcnt(4)
v_mfma_f32_16x16x32_bf16 v[12:15],v[160:163],v[44:47],v[12:15]
ds_read_b128 v[56:59],v2 offset:23552
v_mfma_f32_16x16x32_bf16 v[16:19],v[164:167],v[44:47],v[16:19]
ds_read_b128 v[88:91],v10 offset:4096
v_mfma_f32_16x16x32_bf16 v[20:23],v[168:171],v[44:47],v[20:23]
v_mfma_f32_16x16x32_bf16 v[24:27],v[76:79],v[44:47],v[24:27]
s_waitcnt lgkmcnt(4)
v_mfma_f32_16x16x32_bf16 v[12:15],v[172:175],v[48:51],v[12:15]
ds_read_b128 v[44:47],v2 offset:24576
v_mfma_f32_16x16x32_bf16 v[16:19],v[176:179],v[48:51],v[16:19]
ds_read_b128 v[76:79],v10 offset:5120
v_mfma_f32_16x16x32_bf16 v[20:23],v[180:183],v[48:51],v[20:23]
s_waitcnt vmcnt(10) lgkmcnt(4)
v_mfma_f32_16x16x32_bf16 v[24:27],v[80:83],v[48:51],v[24:27]
s_barrier
v_mfma_f32_16x16x32_bf16 v[12:15],v[184:187],v[52:55],v[12:15]
ds_read_b128 v[48:51],v2 offset:25600
v_mfma_f32_16x16x32_bf16 v[16:19],v[188:191],v[52:55],v[16:19]
ds_read_b128 v[80:83],v10 offset:6144
v_mfma_f32_16x16x32_bf16 v[20:23],v[192:195],v[52:55],v[20:23]
v_mfma_f32_16x16x32_bf16 v[24:27],v[84:87],v[52:55],v[24:27]
s_waitcnt lgkmcnt(4)
v_mfma_f32_16x16x32_bf16 v[12:15],v[196:199],v[56:59],v[12:15]
ds_read_b128 v[52:55],v2 offset:26624
v_mfma_f32_16x16x32_bf16 v[16:19],v[200:203],v[56:59],v[16:19]
ds_read_b128 v[84:87],v10 offset:7168
v_mfma_f32_16x16x32_bf16 v[20:23],v[204:207],v[56:59],v[20:23]
v_mfma_f32_16x16x32_bf16 v[24:27],v[88:91],v[56:59],v[24:27]
s_waitcnt lgkmcnt(4)
v_mfma_f32_16x16x32_bf16 v[12:15],v[208:211],v[44:47],v[12:15]
ds_read_b128 v[56:59],v2 offset:27648
v_mfma_f32_16x16x32_bf16 v[16:19],v[212:215],v[44:47],v[16:19]
ds_read_b128 v[88:91],v10 offset:8192
v_mfma_f32_16x16x32_bf16 v[20:23],v[216:219],v[44:47],v[20:23]
s_waitcnt vmcnt(4) lgkmcnt(4)
v_mfma_f32_16x16x32_bf16 v[24:27],v[76:79],v[44:47],v[24:27]
s_barrier
v_mfma_f32_16x16x32_bf16 v[12:15],v[220:223],v[48:51],v[12:15]
ds_read_b128 v[44:47],v2 offset:28672
v_mfma_f32_16x16x32_bf16 v[16:19],v[224:227],v[48:51],v[16:19]
v_mfma_f32_16x16x32_bf16 v[20:23],v[228:231],v[48:51],v[20:23]
v_mfma_f32_16x16x32_bf16 v[24:27],v[80:83],v[48:51],v[24:27]
s_waitcnt lgkmcnt(3)
v_mfma_f32_16x16x32_bf16 v[12:15],v[232:235],v[52:55],v[12:15]
ds_read_b128 v[48:51],v2 offset:29696
v_mfma_f32_16x16x32_bf16 v[16:19],v[236:239],v[52:55],v[16:19]
v_mfma_f32_16x16x32_bf16 v[20:23],v[240:243],v[52:55],v[20:23]
v_mfma_f32_16x16x32_bf16 v[24:27],v[84:87],v[52:55],v[24:27]
s_waitcnt lgkmcnt(2)
v_mfma_f32_16x16x32_bf16 v[12:15],v[244:247],v[56:59],v[12:15]
ds_read_b128 v[52:55],v2 offset:30720
v_mfma_f32_16x16x32_bf16 v[16:19],v[248:251],v[56:59],v[16:19]
v_mfma_f32_16x16x32_bf16 v[20:23],v[252:255],v[56:59],v[20:23]
s_waitcnt lgkmcnt(2)
v_mfma_f32_16x16x32_bf16 v[24:27],v[88:91],v[56:59],v[24:27]
s_cbranch_scc1 Lg4_slow1_%=
Lg4_d1w0_%=:
v_mfma_f32_16x16x32_bf16 v[28:31],a[0:3],v[44:47],0
s_mov_b32 s73,s48
ds_read_b128 v[56:59],v2 offset:31744
v_lshlrev_b32 v60,16,v68
v_mfma_f32_16x16x32_bf16 v[32:35],a[4:7],v[44:47],0
s_max_u32 s73,s73,s65
v_and_b32 v61,0xffff0000,v68
v_lshlrev_b32 v62,16,v69
v_mfma_f32_16x16x32_bf16 v[36:39],a[8:11],v[44:47],0
s_mov_b32 s65,0
v_and_b32 v63,0xffff0000,v69
v_lshlrev_b32 v64,16,v70
v_mfma_f32_16x16x32_bf16 v[40:43],a[12:15],v[44:47],0
s_waitcnt lgkmcnt(2)
v_and_b32 v65,0xffff0000,v70
v_lshlrev_b32 v66,16,v71
v_mfma_f32_16x16x32_bf16 v[28:31],a[16:19],v[48:51],v[28:31]
s_mul_i32 s61,s50,7
ds_read_b128 v[44:47],v2 offset:32768
v_and_b32 v67,0xffff0000,v71
v_mfma_f32_16x16x32_bf16 v[32:35],a[20:23],v[48:51],v[32:35]
s_lshr_b32 s61,s61,1
v_cvt_pk_bf16_f32 v12,v12,v13
v_cvt_pk_bf16_f32 v13,v14,v15
v_mfma_f32_16x16x32_bf16 v[36:39],a[24:27],v[48:51],v[36:39]
s_mov_b64 s[44:45],s[46:47]
v_cvt_pk_bf16_f32 v14,v16,v17
v_cvt_pk_bf16_f32 v15,v18,v19
v_mfma_f32_16x16x32_bf16 v[40:43],a[28:31],v[48:51],v[40:43]
s_waitcnt lgkmcnt(2)
v_add_f32 v20,v20,v60
v_add_f32 v21,v21,v61
v_mfma_f32_16x16x32_bf16 v[28:31],a[32:35],v[52:55],v[28:31]
buffer_store_dwordx4 v[12:15],v3,s[36:39],s73 offen offset:0 sc0 sc1
ds_read_b128 v[48:51],v2 offset:33792
v_add_f32 v22,v22,v62
v_add_f32 v23,v23,v63
v_mfma_f32_16x16x32_bf16 v[32:35],a[36:39],v[52:55],v[32:35]
s_mov_b64 s[46:47],s[48:49]
v_add_f32 v24,v24,v64
v_add_f32 v25,v25,v65
v_mfma_f32_16x16x32_bf16 v[36:39],a[40:43],v[52:55],v[36:39]
v_add_f32 v26,v26,v66
v_add_f32 v27,v27,v67
v_mfma_f32_16x16x32_bf16 v[40:43],a[44:47],v[52:55],v[40:43]
s_waitcnt lgkmcnt(2)
v_cvt_pk_bf16_f32 v20,v20,v21
v_cvt_pk_bf16_f32 v21,v22,v23
v_mfma_f32_16x16x32_bf16 v[28:31],a[48:51],v[56:59],v[28:31]
ds_read_b128 v[52:55],v2 offset:34816
v_cvt_pk_bf16_f32 v22,v24,v25
v_mfma_f32_16x16x32_bf16 v[32:35],a[52:55],v[56:59],v[32:35]
v_cvt_pk_bf16_f32 v23,v26,v27
v_mfma_f32_16x16x32_bf16 v[36:39],a[56:59],v[56:59],v[36:39]
s_waitcnt vmcnt(1) lgkmcnt(2)
v_mfma_f32_16x16x32_bf16 v[40:43],a[60:63],v[56:59],v[40:43])"
R"(
s_barrier
v_mfma_f32_16x16x32_bf16 v[28:31],a[64:67],v[44:47],v[28:31]
ds_read_b32 v11,v8 offset:5120
ds_read_b128 v[56:59],v2 offset:35840
v_mfma_f32_16x16x32_bf16 v[32:35],a[68:71],v[44:47],v[32:35]
v_mfma_f32_16x16x32_bf16 v[36:39],a[72:75],v[44:47],v[36:39]
v_mfma_f32_16x16x32_bf16 v[40:43],a[76:79],v[44:47],v[40:43]
s_waitcnt lgkmcnt(3)
v_mfma_f32_16x16x32_bf16 v[28:31],a[80:83],v[48:51],v[28:31]
s_waitcnt lgkmcnt(1)
buffer_store_dwordx4 v[20:23],v4,s[36:39],s73 offen offset:0 sc0 sc1
v_cmp_eq_u32 vcc,s60,v11
v_mfma_f32_16x16x32_bf16 v[32:35],a[84:87],v[48:51],v[32:35]
s_cmp_eq_u32 vcc_lo,-1
ds_read_b128 v[44:47],v2 offset:36864
s_cselect_b32 s67,s50,s59
v_mfma_f32_16x16x32_bf16 v[36:39],a[88:91],v[48:51],v[36:39]
s_cselect_b32 s61,s61,s59
s_add_u32 m0,s63,0
s_cmp_ge_u32 s67,s59
v_mfma_f32_16x16x32_bf16 v[40:43],a[92:95],v[48:51],v[40:43]
s_cselect_b32 s68,s50,s59
s_mul_i32 s68,s68,5
v_mfma_f32_16x16x32_bf16 v[28:31],a[96:99],v[52:55],v[28:31]
buffer_load_dwordx4 v0,s[36:39],s61 offen offset:0 sc1 lds
v_mfma_f32_16x16x32_bf16 v[32:35],a[100:103],v[52:55],v[32:35]
s_lshr_b32 s68,s68,6
ds_read_b128 v[48:51],v2 offset:37888
v_mfma_f32_16x16x32_bf16 v[36:39],a[104:107],v[52:55],v[36:39]
v_mfma_f32_16x16x32_bf16 v[40:43],a[108:111],v[52:55],v[40:43]
s_waitcnt lgkmcnt(2)
v_mfma_f32_16x16x32_bf16 v[28:31],a[112:115],v[56:59],v[28:31]
buffer_load_dwordx4 v0,s[36:39],s61 offen offset:1024 sc1 lds
v_mfma_f32_16x16x32_bf16 v[32:35],a[116:119],v[56:59],v[32:35]
ds_read_b128 v[52:55],v2 offset:38912
v_mfma_f32_16x16x32_bf16 v[36:39],a[120:123],v[56:59],v[36:39]
v_mfma_f32_16x16x32_bf16 v[40:43],a[124:127],v[56:59],v[40:43]
s_waitcnt lgkmcnt(2)
v_mfma_f32_16x16x32_bf16 v[28:31],a[128:131],v[44:47],v[28:31]
buffer_load_dwordx4 v0,s[36:39],s61 offen offset:2048 sc1 lds
v_mfma_f32_16x16x32_bf16 v[32:35],a[132:135],v[44:47],v[32:35]
ds_read_b128 v[56:59],v2 offset:39936
v_mfma_f32_16x16x32_bf16 v[36:39],a[136:139],v[44:47],v[36:39]
v_mfma_f32_16x16x32_bf16 v[40:43],a[140:143],v[44:47],v[40:43]
s_waitcnt lgkmcnt(2)
v_mfma_f32_16x16x32_bf16 v[28:31],a[144:147],v[48:51],v[28:31]
buffer_load_dwordx4 v0,s[36:39],s61 offen offset:3072 sc1 lds
v_mfma_f32_16x16x32_bf16 v[32:35],a[148:151],v[48:51],v[32:35]
s_add_u32 m0,s78,6144
ds_read_b128 v[44:47],v2 offset:40960
s_add_u32 s74,s53,s55
v_mfma_f32_16x16x32_bf16 v[36:39],a[152:155],v[48:51],v[36:39]
s_cmp_eq_u32 s74,s56
s_cselect_b32 s74,s57,s74
s_cmp_eq_u32 s74,s58
v_mfma_f32_16x16x32_bf16 v[40:43],a[156:159],v[48:51],v[40:43]
s_waitcnt lgkmcnt(2)
s_cselect_b32 s74,s59,s74
s_cmp_ge_u32 s74,6520832
v_mfma_f32_16x16x32_bf16 v[28:31],a[160:163],v[52:55],v[28:31]
buffer_load_dwordx4 v[68:71],v9,s[36:39],s49 offen offset:0 sc1
v_mfma_f32_16x16x32_bf16 v[32:35],a[164:167],v[52:55],v[32:35]
s_cselect_b32 s55,196608,s55
ds_read_b128 v[48:51],v2 offset:41984
s_mov_b64 s[48:49],s[50:51]
v_mfma_f32_16x16x32_bf16 v[36:39],a[168:171],v[52:55],v[36:39]
v_mfma_f32_16x16x32_bf16 v[40:43],a[172:175],v[52:55],v[40:43]
s_waitcnt lgkmcnt(2)
v_mfma_f32_16x16x32_bf16 v[28:31],a[176:179],v[56:59],v[28:31]
ds_read_b128 v[52:55],v2 offset:43008
v_mfma_f32_16x16x32_bf16 v[32:35],a[180:183],v[56:59],v[32:35]
buffer_load_dword v7,s[36:39],s68 offen offset:0 sc1 lds
v_mfma_f32_16x16x32_bf16 v[36:39],a[184:187],v[56:59],v[36:39]
s_add_u32 m0,s64,0
s_mul_i32 s68,s51,5
s_lshr_b32 s68,s68,6
v_mfma_f32_16x16x32_bf16 v[40:43],a[188:191],v[56:59],v[40:43]
s_waitcnt lgkmcnt(2)
s_mov_b64 s[50:51],s[52:53]
s_mov_b32 s52,s74
v_mfma_f32_16x16x32_bf16 v[28:31],a[192:195],v[44:47],v[28:31]
ds_read_b128 v[56:59],v2 offset:44032
v_mfma_f32_16x16x32_bf16 v[32:35],a[196:199],v[44:47],v[32:35]
v_mfma_f32_16x16x32_bf16 v[36:39],a[200:203],v[44:47],v[36:39]
s_waitcnt vmcnt(9) lgkmcnt(2)
v_mfma_f32_16x16x32_bf16 v[40:43],a[204:207],v[44:47],v[40:43]
s_barrier
v_mfma_f32_16x16x32_bf16 v[28:31],a[208:211],v[48:51],v[28:31]
ds_read_b128 v[44:47],v2 offset:45056
v_mfma_f32_16x16x32_bf16 v[32:35],a[212:215],v[48:51],v[32:35]
buffer_load_dwordx4 v1,s[36:39],s61 offen offset:0 sc1 lds
v_mfma_f32_16x16x32_bf16 v[36:39],a[216:219],v[48:51],v[36:39]
v_mfma_f32_16x16x32_bf16 v[40:43],a[220:223],v[48:51],v[40:43]
s_waitcnt lgkmcnt(2)
v_mfma_f32_16x16x32_bf16 v[28:31],a[224:227],v[52:55],v[28:31]
ds_read_b128 v[48:51],v2 offset:46080
v_mfma_f32_16x16x32_bf16 v[32:35],a[228:231],v[52:55],v[32:35]
v_mfma_f32_16x16x32_bf16 v[36:39],a[232:235],v[52:55],v[36:39]
v_mfma_f32_16x16x32_bf16 v[40:43],a[236:239],v[52:55],v[40:43]
s_waitcnt lgkmcnt(2)
buffer_load_dwordx4 v1,s[36:39],s61 offen offset:1024 sc1 lds
v_mfma_f32_16x16x32_bf16 v[28:31],a[240:243],v[56:59],v[28:31]
ds_read_b128 v[52:55],v2 offset:47104
v_mfma_f32_16x16x32_bf16 v[32:35],a[244:247],v[56:59],v[32:35]
v_mfma_f32_16x16x32_bf16 v[36:39],a[248:251],v[56:59],v[36:39]
v_mfma_f32_16x16x32_bf16 v[40:43],a[252:255],v[56:59],v[40:43]
s_waitcnt lgkmcnt(2)
v_mfma_f32_16x16x32_bf16 v[28:31],v[100:103],v[44:47],v[28:31]
ds_read_b128 v[56:59],v2 offset:48128
v_mfma_f32_16x16x32_bf16 v[32:35],v[104:107],v[44:47],v[32:35]
buffer_load_dwordx4 v1,s[36:39],s61 offen offset:2048 sc1 lds
v_mfma_f32_16x16x32_bf16 v[36:39],v[108:111],v[44:47],v[36:39]
s_add_u32 m0,s78,4096
ds_read_b128 v[88:91],v10 offset:0
s_cmp_lt_u32 s67,s59
v_mfma_f32_16x16x32_bf16 v[40:43],v[112:115],v[44:47],v[40:43]
s_waitcnt lgkmcnt(3)
v_mfma_f32_16x16x32_bf16 v[28:31],v[116:119],v[48:51],v[28:31]
ds_read_b128 v[44:47],v2 offset:49152
v_mfma_f32_16x16x32_bf16 v[32:35],v[120:123],v[48:51],v[32:35]
ds_read_b128 v[76:79],v10 offset:1024
v_mfma_f32_16x16x32_bf16 v[36:39],v[124:127],v[48:51],v[36:39]
v_mfma_f32_16x16x32_bf16 v[40:43],v[128:131],v[48:51],v[40:43]
s_waitcnt lgkmcnt(4)
v_mfma_f32_16x16x32_bf16 v[28:31],v[132:135],v[52:55],v[28:31]
buffer_load_dword v7,s[36:39],s68 offen offset:0 sc1 lds
v_mfma_f32_16x16x32_bf16 v[32:35],v[136:139],v[52:55],v[32:35]
ds_read_b128 v[48:51],v2 offset:50176
ds_read_b128 v[80:83],v10 offset:2048
v_mfma_f32_16x16x32_bf16 v[36:39],v[140:143],v[52:55],v[36:39]
v_mfma_f32_16x16x32_bf16 v[40:43],v[144:147],v[52:55],v[40:43]
s_waitcnt lgkmcnt(4)
v_mfma_f32_16x16x32_bf16 v[28:31],v[148:151],v[56:59],v[28:31]
ds_read_b128 v[52:55],v2 offset:51200
v_mfma_f32_16x16x32_bf16 v[32:35],v[152:155],v[56:59],v[32:35]
ds_read_b128 v[84:87],v10 offset:3072
v_mfma_f32_16x16x32_bf16 v[36:39],v[156:159],v[56:59],v[36:39]
v_mfma_f32_16x16x32_bf16 v[40:43],v[88:91],v[56:59],v[40:43]
s_waitcnt lgkmcnt(4)
v_mfma_f32_16x16x32_bf16 v[28:31],v[160:163],v[44:47],v[28:31]
ds_read_b128 v[56:59],v2 offset:52224
v_mfma_f32_16x16x32_bf16 v[32:35],v[164:167],v[44:47],v[32:35]
ds_read_b128 v[88:91],v10 offset:4096
v_mfma_f32_16x16x32_bf16 v[36:39],v[168:171],v[44:47],v[36:39]
v_mfma_f32_16x16x32_bf16 v[40:43],v[76:79],v[44:47],v[40:43]
s_waitcnt lgkmcnt(4)
v_mfma_f32_16x16x32_bf16 v[28:31],v[172:175],v[48:51],v[28:31]
ds_read_b128 v[44:47],v2 offset:53248
v_mfma_f32_16x16x32_bf16 v[32:35],v[176:179],v[48:51],v[32:35]
ds_read_b128 v[76:79],v10 offset:5120
v_mfma_f32_16x16x32_bf16 v[36:39],v[180:183],v[48:51],v[36:39]
s_waitcnt vmcnt(10) lgkmcnt(4)
v_mfma_f32_16x16x32_bf16 v[40:43],v[80:83],v[48:51],v[40:43]
s_barrier
v_mfma_f32_16x16x32_bf16 v[28:31],v[184:187],v[52:55],v[28:31]
ds_read_b128 v[48:51],v2 offset:54272
v_mfma_f32_16x16x32_bf16 v[32:35],v[188:191],v[52:55],v[32:35]
ds_read_b128 v[80:83],v10 offset:6144
v_mfma_f32_16x16x32_bf16 v[36:39],v[192:195],v[52:55],v[36:39]
v_mfma_f32_16x16x32_bf16 v[40:43],v[84:87],v[52:55],v[40:43]
s_waitcnt lgkmcnt(4)
v_mfma_f32_16x16x32_bf16 v[28:31],v[196:199],v[56:59],v[28:31]
ds_read_b128 v[52:55],v2 offset:55296
v_mfma_f32_16x16x32_bf16 v[32:35],v[200:203],v[56:59],v[32:35]
ds_read_b128 v[84:87],v10 offset:7168
v_mfma_f32_16x16x32_bf16 v[36:39],v[204:207],v[56:59],v[36:39]
v_mfma_f32_16x16x32_bf16 v[40:43],v[88:91],v[56:59],v[40:43]
s_waitcnt lgkmcnt(4)
v_mfma_f32_16x16x32_bf16 v[28:31],v[208:211],v[44:47],v[28:31]
ds_read_b128 v[56:59],v2 offset:56320
v_mfma_f32_16x16x32_bf16 v[32:35],v[212:215],v[44:47],v[32:35]
ds_read_b128 v[88:91],v10 offset:8192
v_mfma_f32_16x16x32_bf16 v[36:39],v[216:219],v[44:47],v[36:39]
s_waitcnt vmcnt(4) lgkmcnt(4)
v_mfma_f32_16x16x32_bf16 v[40:43],v[76:79],v[44:47],v[40:43]
s_barrier
v_mfma_f32_16x16x32_bf16 v[28:31],v[220:223],v[48:51],v[28:31]
ds_read_b128 v[44:47],v2 offset:0
v_mfma_f32_16x16x32_bf16 v[32:35],v[224:227],v[48:51],v[32:35]
v_mfma_f32_16x16x32_bf16 v[36:39],v[228:231],v[48:51],v[36:39]
v_mfma_f32_16x16x32_bf16 v[40:43],v[80:83],v[48:51],v[40:43]
s_waitcnt lgkmcnt(3)
v_mfma_f32_16x16x32_bf16 v[28:31],v[232:235],v[52:55],v[28:31]
ds_read_b128 v[48:51],v2 offset:1024
v_mfma_f32_16x16x32_bf16 v[32:35],v[236:239],v[52:55],v[32:35]
v_mfma_f32_16x16x32_bf16 v[36:39],v[240:243],v[52:55],v[36:39]
v_mfma_f32_16x16x32_bf16 v[40:43],v[84:87],v[52:55],v[40:43]
s_waitcnt lgkmcnt(2)
v_mfma_f32_16x16x32_bf16 v[28:31],v[244:247],v[56:59],v[28:31]
ds_read_b128 v[52:55],v2 offset:2048
v_mfma_f32_16x16x32_bf16 v[32:35],v[248:251],v[56:59],v[32:35]
v_mfma_f32_16x16x32_bf16 v[36:39],v[252:255],v[56:59],v[36:39]
s_waitcnt lgkmcnt(2)
v_mfma_f32_16x16x32_bf16 v[40:43],v[88:91],v[56:59],v[40:43]
s_cbranch_scc1 Lg4_d0w0_%=
s_branch Lg4_slow0_%=
Lg4_d0w1_%=:
v_mfma_f32_16x16x32_bf16 v[12:15],a[0:3],v[44:47],0
s_mov_b32 s73,s47
ds_read_b128 v[56:59],v2 offset:3072)"
R"(
v_lshlrev_b32 v60,16,v68
v_mfma_f32_16x16x32_bf16 v[16:19],a[4:7],v[44:47],0
s_max_u32 s73,s73,s65
v_and_b32 v61,0xffff0000,v68
v_lshlrev_b32 v62,16,v69
v_mfma_f32_16x16x32_bf16 v[20:23],a[8:11],v[44:47],0
s_mov_b32 s65,0
v_and_b32 v63,0xffff0000,v69
v_lshlrev_b32 v64,16,v70
v_mfma_f32_16x16x32_bf16 v[24:27],a[12:15],v[44:47],0
s_waitcnt lgkmcnt(2)
v_and_b32 v65,0xffff0000,v70
v_lshlrev_b32 v66,16,v71
v_mfma_f32_16x16x32_bf16 v[12:15],a[16:19],v[48:51],v[12:15]
s_mul_i32 s61,s49,7
ds_read_b128 v[44:47],v2 offset:4096
v_and_b32 v67,0xffff0000,v71
v_mfma_f32_16x16x32_bf16 v[16:19],a[20:23],v[48:51],v[16:19]
s_lshr_b32 s61,s61,1
v_cvt_pk_bf16_f32 v28,v28,v29
v_cvt_pk_bf16_f32 v29,v30,v31
v_mfma_f32_16x16x32_bf16 v[20:23],a[24:27],v[48:51],v[20:23]
v_cvt_pk_bf16_f32 v30,v32,v33
v_cvt_pk_bf16_f32 v31,v34,v35
v_mfma_f32_16x16x32_bf16 v[24:27],a[28:31],v[48:51],v[24:27]
s_waitcnt lgkmcnt(2)
v_add_f32 v36,v36,v60
v_add_f32 v37,v37,v61
v_mfma_f32_16x16x32_bf16 v[12:15],a[32:35],v[52:55],v[12:15]
ds_read_b128 v[48:51],v2 offset:5120
v_add_f32 v38,v38,v62
v_mfma_f32_16x16x32_bf16 v[16:19],a[36:39],v[52:55],v[16:19]
buffer_store_dwordx4 v[28:31],v3,s[36:39],s73 offen offset:0 sc0 sc1
v_add_f32 v39,v39,v63
v_add_f32 v40,v40,v64
v_add_f32 v41,v41,v65
v_mfma_f32_16x16x32_bf16 v[20:23],a[40:43],v[52:55],v[20:23]
v_add_f32 v42,v42,v66
v_add_f32 v43,v43,v67
v_mfma_f32_16x16x32_bf16 v[24:27],a[44:47],v[52:55],v[24:27]
s_waitcnt lgkmcnt(2)
v_cvt_pk_bf16_f32 v36,v36,v37
v_cvt_pk_bf16_f32 v37,v38,v39
v_mfma_f32_16x16x32_bf16 v[12:15],a[48:51],v[56:59],v[12:15]
ds_read_b128 v[52:55],v2 offset:6144
v_cvt_pk_bf16_f32 v38,v40,v41
v_mfma_f32_16x16x32_bf16 v[16:19],a[52:55],v[56:59],v[16:19]
v_cvt_pk_bf16_f32 v39,v42,v43
v_mfma_f32_16x16x32_bf16 v[20:23],a[56:59],v[56:59],v[20:23]
s_waitcnt lgkmcnt(2)
s_nop 0
v_mfma_f32_16x16x32_bf16 v[24:27],a[60:63],v[56:59],v[24:27]
s_barrier
v_mfma_f32_16x16x32_bf16 v[12:15],a[64:67],v[44:47],v[12:15]
ds_read_b128 v[56:59],v2 offset:7168
ds_read_b32 v11,v8 offset:4096
v_mfma_f32_16x16x32_bf16 v[16:19],a[68:71],v[44:47],v[16:19]
v_mfma_f32_16x16x32_bf16 v[20:23],a[72:75],v[44:47],v[20:23]
v_mfma_f32_16x16x32_bf16 v[24:27],a[76:79],v[44:47],v[24:27]
s_waitcnt lgkmcnt(3)
v_mfma_f32_16x16x32_bf16 v[12:15],a[80:83],v[48:51],v[12:15]
s_waitcnt lgkmcnt(0)
ds_read_b128 v[44:47],v2 offset:8192
v_mfma_f32_16x16x32_bf16 v[16:19],a[84:87],v[48:51],v[16:19]
buffer_store_dwordx4 v[36:39],v4,s[36:39],s73 offen offset:0 sc0 sc1
v_cmp_eq_u32 vcc,s60,v11
v_mfma_f32_16x16x32_bf16 v[20:23],a[88:91],v[48:51],v[20:23]
s_cmp_eq_u32 vcc_lo,-1
s_cselect_b32 s66,s49,s59
s_cselect_b32 s61,s61,s59
v_mfma_f32_16x16x32_bf16 v[24:27],a[92:95],v[48:51],v[24:27]
s_add_u32 m0,s63,28672
s_add_u32 s53,s52,s55
v_mfma_f32_16x16x32_bf16 v[12:15],a[96:99],v[52:55],v[12:15]
s_cmp_eq_u32 s53,s56
ds_read_b128 v[48:51],v2 offset:9216
s_cselect_b32 s53,s57,s53
v_mfma_f32_16x16x32_bf16 v[16:19],a[100:103],v[52:55],v[16:19]
buffer_load_dwordx4 v0,s[36:39],s61 offen offset:0 sc1 lds
v_mfma_f32_16x16x32_bf16 v[20:23],a[104:107],v[52:55],v[20:23]
s_cmp_eq_u32 s53,s58
s_cselect_b32 s53,s59,s53
s_cmp_ge_u32 s53,6520832
v_mfma_f32_16x16x32_bf16 v[24:27],a[108:111],v[52:55],v[24:27]
s_cselect_b32 s55,196608,s55
v_mfma_f32_16x16x32_bf16 v[12:15],a[112:115],v[56:59],v[12:15]
ds_read_b128 v[52:55],v2 offset:10240
v_mfma_f32_16x16x32_bf16 v[16:19],a[116:119],v[56:59],v[16:19]
buffer_load_dwordx4 v0,s[36:39],s61 offen offset:1024 sc1 lds
v_mfma_f32_16x16x32_bf16 v[20:23],a[120:123],v[56:59],v[20:23]
v_mfma_f32_16x16x32_bf16 v[24:27],a[124:127],v[56:59],v[24:27]
s_waitcnt lgkmcnt(2)
v_mfma_f32_16x16x32_bf16 v[12:15],a[128:131],v[44:47],v[12:15]
ds_read_b128 v[56:59],v2 offset:11264
v_mfma_f32_16x16x32_bf16 v[16:19],a[132:135],v[44:47],v[16:19]
buffer_load_dwordx4 v0,s[36:39],s61 offen offset:2048 sc1 lds
v_mfma_f32_16x16x32_bf16 v[20:23],a[136:139],v[44:47],v[20:23]
v_mfma_f32_16x16x32_bf16 v[24:27],a[140:143],v[44:47],v[24:27]
s_waitcnt lgkmcnt(2)
v_mfma_f32_16x16x32_bf16 v[12:15],a[144:147],v[48:51],v[12:15]
ds_read_b128 v[44:47],v2 offset:12288
v_mfma_f32_16x16x32_bf16 v[16:19],a[148:151],v[48:51],v[16:19]
buffer_load_dwordx4 v0,s[36:39],s61 offen offset:3072 sc1 lds
v_mfma_f32_16x16x32_bf16 v[20:23],a[152:155],v[48:51],v[20:23]
s_add_u32 m0,s64,28672
s_cmp_ge_u32 s66,s59
v_mfma_f32_16x16x32_bf16 v[24:27],a[156:159],v[48:51],v[24:27]
s_waitcnt lgkmcnt(2)
v_mfma_f32_16x16x32_bf16 v[12:15],a[160:163],v[52:55],v[12:15]
ds_read_b128 v[48:51],v2 offset:13312
v_mfma_f32_16x16x32_bf16 v[16:19],a[164:167],v[52:55],v[16:19]
buffer_load_dwordx4 v[68:71],v9,s[36:39],s48 offen offset:0 sc1
v_mfma_f32_16x16x32_bf16 v[20:23],a[168:171],v[52:55],v[20:23]
v_mfma_f32_16x16x32_bf16 v[24:27],a[172:175],v[52:55],v[24:27]
s_waitcnt lgkmcnt(2)
v_mfma_f32_16x16x32_bf16 v[12:15],a[176:179],v[56:59],v[12:15]
ds_read_b128 v[52:55],v2 offset:14336
v_mfma_f32_16x16x32_bf16 v[16:19],a[180:183],v[56:59],v[16:19]
v_mfma_f32_16x16x32_bf16 v[20:23],a[184:187],v[56:59],v[20:23]
v_mfma_f32_16x16x32_bf16 v[24:27],a[188:191],v[56:59],v[24:27]
s_waitcnt lgkmcnt(2)
v_mfma_f32_16x16x32_bf16 v[12:15],a[192:195],v[44:47],v[12:15]
ds_read_b128 v[56:59],v2 offset:15360
v_mfma_f32_16x16x32_bf16 v[16:19],a[196:199],v[44:47],v[16:19]
v_mfma_f32_16x16x32_bf16 v[20:23],a[200:203],v[44:47],v[20:23]
s_waitcnt vmcnt(7) lgkmcnt(2)
v_mfma_f32_16x16x32_bf16 v[24:27],a[204:207],v[44:47],v[24:27]
s_barrier
v_mfma_f32_16x16x32_bf16 v[12:15],a[208:211],v[48:51],v[12:15]
ds_read_b128 v[44:47],v2 offset:16384
v_mfma_f32_16x16x32_bf16 v[16:19],a[212:215],v[48:51],v[16:19]
v_mfma_f32_16x16x32_bf16 v[20:23],a[216:219],v[48:51],v[20:23]
buffer_load_dwordx4 v1,s[36:39],s61 offen offset:0 sc1 lds
v_mfma_f32_16x16x32_bf16 v[24:27],a[220:223],v[48:51],v[24:27]
s_waitcnt lgkmcnt(2)
v_mfma_f32_16x16x32_bf16 v[12:15],a[224:227],v[52:55],v[12:15]
ds_read_b128 v[48:51],v2 offset:17408
v_mfma_f32_16x16x32_bf16 v[16:19],a[228:231],v[52:55],v[16:19]
v_mfma_f32_16x16x32_bf16 v[20:23],a[232:235],v[52:55],v[20:23]
v_mfma_f32_16x16x32_bf16 v[24:27],a[236:239],v[52:55],v[24:27]
s_waitcnt lgkmcnt(2)
v_mfma_f32_16x16x32_bf16 v[12:15],a[240:243],v[56:59],v[12:15]
buffer_load_dwordx4 v1,s[36:39],s61 offen offset:1024 sc1 lds
v_mfma_f32_16x16x32_bf16 v[16:19],a[244:247],v[56:59],v[16:19]
ds_read_b128 v[52:55],v2 offset:18432
v_mfma_f32_16x16x32_bf16 v[20:23],a[248:251],v[56:59],v[20:23]
v_mfma_f32_16x16x32_bf16 v[24:27],a[252:255],v[56:59],v[24:27]
s_waitcnt lgkmcnt(2)
v_mfma_f32_16x16x32_bf16 v[12:15],v[100:103],v[44:47],v[12:15]
ds_read_b128 v[56:59],v2 offset:19456
v_mfma_f32_16x16x32_bf16 v[16:19],v[104:107],v[44:47],v[16:19]
ds_read_b128 v[88:91],v10 offset:0
v_mfma_f32_16x16x32_bf16 v[20:23],v[108:111],v[44:47],v[20:23]
buffer_load_dwordx4 v1,s[36:39],s61 offen offset:2048 sc1 lds
v_mfma_f32_16x16x32_bf16 v[24:27],v[112:115],v[44:47],v[24:27]
s_waitcnt lgkmcnt(3)
v_mfma_f32_16x16x32_bf16 v[12:15],v[116:119],v[48:51],v[12:15]
ds_read_b128 v[44:47],v2 offset:20480
v_mfma_f32_16x16x32_bf16 v[16:19],v[120:123],v[48:51],v[16:19]
ds_read_b128 v[76:79],v10 offset:1024
v_mfma_f32_16x16x32_bf16 v[20:23],v[124:127],v[48:51],v[20:23]
v_mfma_f32_16x16x32_bf16 v[24:27],v[128:131],v[48:51],v[24:27]
s_waitcnt lgkmcnt(4)
v_mfma_f32_16x16x32_bf16 v[12:15],v[132:135],v[52:55],v[12:15]
ds_read_b128 v[48:51],v2 offset:21504
v_mfma_f32_16x16x32_bf16 v[16:19],v[136:139],v[52:55],v[16:19]
ds_read_b128 v[80:83],v10 offset:2048
v_mfma_f32_16x16x32_bf16 v[20:23],v[140:143],v[52:55],v[20:23]
v_mfma_f32_16x16x32_bf16 v[24:27],v[144:147],v[52:55],v[24:27]
s_waitcnt lgkmcnt(4)
v_mfma_f32_16x16x32_bf16 v[12:15],v[148:151],v[56:59],v[12:15]
ds_read_b128 v[52:55],v2 offset:22528
v_mfma_f32_16x16x32_bf16 v[16:19],v[152:155],v[56:59],v[16:19]
ds_read_b128 v[84:87],v10 offset:3072
v_mfma_f32_16x16x32_bf16 v[20:23],v[156:159],v[56:59],v[20:23]
v_mfma_f32_16x16x32_bf16 v[24:27],v[88:91],v[56:59],v[24:27]
s_waitcnt lgkmcnt(4)
v_mfma_f32_16x16x32_bf16 v[12:15],v[160:163],v[44:47],v[12:15]
ds_read_b128 v[56:59],v2 offset:23552
v_mfma_f32_16x16x32_bf16 v[16:19],v[164:167],v[44:47],v[16:19]
ds_read_b128 v[88:91],v10 offset:4096
v_mfma_f32_16x16x32_bf16 v[20:23],v[168:171],v[44:47],v[20:23]
v_mfma_f32_16x16x32_bf16 v[24:27],v[76:79],v[44:47],v[24:27]
s_waitcnt lgkmcnt(4)
v_mfma_f32_16x16x32_bf16 v[12:15],v[172:175],v[48:51],v[12:15]
ds_read_b128 v[44:47],v2 offset:24576
v_mfma_f32_16x16x32_bf16 v[16:19],v[176:179],v[48:51],v[16:19]
ds_read_b128 v[76:79],v10 offset:5120
v_mfma_f32_16x16x32_bf16 v[20:23],v[180:183],v[48:51],v[20:23]
s_waitcnt vmcnt(8) lgkmcnt(4)
v_mfma_f32_16x16x32_bf16 v[24:27],v[80:83],v[48:51],v[24:27]
s_barrier
v_mfma_f32_16x16x32_bf16 v[12:15],v[184:187],v[52:55],v[12:15]
ds_read_b128 v[48:51],v2 offset:25600
v_mfma_f32_16x16x32_bf16 v[16:19],v[188:191],v[52:55],v[16:19]
ds_read_b128 v[80:83],v10 offset:6144
v_mfma_f32_16x16x32_bf16 v[20:23],v[192:195],v[52:55],v[20:23]
v_mfma_f32_16x16x32_bf16 v[24:27],v[84:87],v[52:55],v[24:27]
s_waitcnt lgkmcnt(4)
v_mfma_f32_16x16x32_bf16 v[12:15],v[196:199],v[56:59],v[12:15])"
R"(
ds_read_b128 v[52:55],v2 offset:26624
v_mfma_f32_16x16x32_bf16 v[16:19],v[200:203],v[56:59],v[16:19]
ds_read_b128 v[84:87],v10 offset:7168
v_mfma_f32_16x16x32_bf16 v[20:23],v[204:207],v[56:59],v[20:23]
v_mfma_f32_16x16x32_bf16 v[24:27],v[88:91],v[56:59],v[24:27]
s_waitcnt lgkmcnt(4)
v_mfma_f32_16x16x32_bf16 v[12:15],v[208:211],v[44:47],v[12:15]
ds_read_b128 v[56:59],v2 offset:27648
v_mfma_f32_16x16x32_bf16 v[16:19],v[212:215],v[44:47],v[16:19]
ds_read_b128 v[88:91],v10 offset:8192
v_mfma_f32_16x16x32_bf16 v[20:23],v[216:219],v[44:47],v[20:23]
s_waitcnt vmcnt(3) lgkmcnt(4)
v_mfma_f32_16x16x32_bf16 v[24:27],v[76:79],v[44:47],v[24:27]
s_barrier
v_mfma_f32_16x16x32_bf16 v[12:15],v[220:223],v[48:51],v[12:15]
ds_read_b128 v[44:47],v2 offset:28672
v_mfma_f32_16x16x32_bf16 v[16:19],v[224:227],v[48:51],v[16:19]
v_mfma_f32_16x16x32_bf16 v[20:23],v[228:231],v[48:51],v[20:23]
v_mfma_f32_16x16x32_bf16 v[24:27],v[80:83],v[48:51],v[24:27]
s_waitcnt lgkmcnt(3)
v_mfma_f32_16x16x32_bf16 v[12:15],v[232:235],v[52:55],v[12:15]
ds_read_b128 v[48:51],v2 offset:29696
v_mfma_f32_16x16x32_bf16 v[16:19],v[236:239],v[52:55],v[16:19]
v_mfma_f32_16x16x32_bf16 v[20:23],v[240:243],v[52:55],v[20:23]
v_mfma_f32_16x16x32_bf16 v[24:27],v[84:87],v[52:55],v[24:27]
s_waitcnt lgkmcnt(2)
v_mfma_f32_16x16x32_bf16 v[12:15],v[244:247],v[56:59],v[12:15]
ds_read_b128 v[52:55],v2 offset:30720
v_mfma_f32_16x16x32_bf16 v[16:19],v[248:251],v[56:59],v[16:19]
v_mfma_f32_16x16x32_bf16 v[20:23],v[252:255],v[56:59],v[20:23]
s_waitcnt lgkmcnt(2)
v_mfma_f32_16x16x32_bf16 v[24:27],v[88:91],v[56:59],v[24:27]
s_cbranch_scc1 Lg4_slow1_%=
Lg4_d1w1_%=:
v_mfma_f32_16x16x32_bf16 v[28:31],a[0:3],v[44:47],0
s_mov_b32 s73,s48
ds_read_b128 v[56:59],v2 offset:31744
v_lshlrev_b32 v60,16,v68
v_mfma_f32_16x16x32_bf16 v[32:35],a[4:7],v[44:47],0
s_max_u32 s73,s73,s65
v_and_b32 v61,0xffff0000,v68
v_lshlrev_b32 v62,16,v69
v_mfma_f32_16x16x32_bf16 v[36:39],a[8:11],v[44:47],0
s_mov_b32 s65,0
v_and_b32 v63,0xffff0000,v69
v_lshlrev_b32 v64,16,v70
v_mfma_f32_16x16x32_bf16 v[40:43],a[12:15],v[44:47],0
s_waitcnt lgkmcnt(2)
v_and_b32 v65,0xffff0000,v70
v_lshlrev_b32 v66,16,v71
v_mfma_f32_16x16x32_bf16 v[28:31],a[16:19],v[48:51],v[28:31]
s_mul_i32 s61,s50,7
ds_read_b128 v[44:47],v2 offset:32768
v_and_b32 v67,0xffff0000,v71
v_mfma_f32_16x16x32_bf16 v[32:35],a[20:23],v[48:51],v[32:35]
s_lshr_b32 s61,s61,1
v_cvt_pk_bf16_f32 v12,v12,v13
v_cvt_pk_bf16_f32 v13,v14,v15
v_mfma_f32_16x16x32_bf16 v[36:39],a[24:27],v[48:51],v[36:39]
s_mov_b64 s[44:45],s[46:47]
v_cvt_pk_bf16_f32 v14,v16,v17
v_cvt_pk_bf16_f32 v15,v18,v19
v_mfma_f32_16x16x32_bf16 v[40:43],a[28:31],v[48:51],v[40:43]
s_waitcnt lgkmcnt(2)
v_add_f32 v20,v20,v60
v_add_f32 v21,v21,v61
v_mfma_f32_16x16x32_bf16 v[28:31],a[32:35],v[52:55],v[28:31]
s_mov_b64 s[46:47],s[48:49]
ds_read_b128 v[48:51],v2 offset:33792
v_add_f32 v22,v22,v62
v_mfma_f32_16x16x32_bf16 v[32:35],a[36:39],v[52:55],v[32:35]
buffer_store_dwordx4 v[12:15],v3,s[36:39],s73 offen offset:0 sc0 sc1
v_add_f32 v23,v23,v63
v_add_f32 v24,v24,v64
v_add_f32 v25,v25,v65
v_mfma_f32_16x16x32_bf16 v[36:39],a[40:43],v[52:55],v[36:39]
v_add_f32 v26,v26,v66
v_add_f32 v27,v27,v67
v_mfma_f32_16x16x32_bf16 v[40:43],a[44:47],v[52:55],v[40:43]
s_waitcnt lgkmcnt(2)
v_cvt_pk_bf16_f32 v20,v20,v21
v_cvt_pk_bf16_f32 v21,v22,v23
v_mfma_f32_16x16x32_bf16 v[28:31],a[48:51],v[56:59],v[28:31]
ds_read_b128 v[52:55],v2 offset:34816
v_cvt_pk_bf16_f32 v22,v24,v25
v_mfma_f32_16x16x32_bf16 v[32:35],a[52:55],v[56:59],v[32:35]
v_cvt_pk_bf16_f32 v23,v26,v27
v_mfma_f32_16x16x32_bf16 v[36:39],a[56:59],v[56:59],v[36:39]
s_waitcnt lgkmcnt(2)
s_nop 0
v_mfma_f32_16x16x32_bf16 v[40:43],a[60:63],v[56:59],v[40:43]
s_barrier
v_mfma_f32_16x16x32_bf16 v[28:31],a[64:67],v[44:47],v[28:31]
ds_read_b128 v[56:59],v2 offset:35840
ds_read_b32 v11,v8 offset:5120
v_mfma_f32_16x16x32_bf16 v[32:35],a[68:71],v[44:47],v[32:35]
v_mfma_f32_16x16x32_bf16 v[36:39],a[72:75],v[44:47],v[36:39]
v_mfma_f32_16x16x32_bf16 v[40:43],a[76:79],v[44:47],v[40:43]
s_waitcnt lgkmcnt(3)
v_mfma_f32_16x16x32_bf16 v[28:31],a[80:83],v[48:51],v[28:31]
s_waitcnt lgkmcnt(0)
ds_read_b128 v[44:47],v2 offset:36864
v_mfma_f32_16x16x32_bf16 v[32:35],a[84:87],v[48:51],v[32:35]
buffer_store_dwordx4 v[20:23],v4,s[36:39],s73 offen offset:0 sc0 sc1
v_cmp_eq_u32 vcc,s60,v11
v_mfma_f32_16x16x32_bf16 v[36:39],a[88:91],v[48:51],v[36:39]
s_cmp_eq_u32 vcc_lo,-1
s_cselect_b32 s67,s50,s59
s_cselect_b32 s61,s61,s59
v_mfma_f32_16x16x32_bf16 v[40:43],a[92:95],v[48:51],v[40:43]
s_add_u32 m0,s63,0
s_add_u32 s74,s53,s55
v_mfma_f32_16x16x32_bf16 v[28:31],a[96:99],v[52:55],v[28:31]
s_cmp_eq_u32 s74,s56
ds_read_b128 v[48:51],v2 offset:37888
s_cselect_b32 s74,s57,s74
v_mfma_f32_16x16x32_bf16 v[32:35],a[100:103],v[52:55],v[32:35]
buffer_load_dwordx4 v0,s[36:39],s61 offen offset:0 sc1 lds
v_mfma_f32_16x16x32_bf16 v[36:39],a[104:107],v[52:55],v[36:39]
s_cmp_eq_u32 s74,s58
s_cselect_b32 s74,s59,s74
s_cmp_ge_u32 s74,6520832
v_mfma_f32_16x16x32_bf16 v[40:43],a[108:111],v[52:55],v[40:43]
s_cselect_b32 s55,196608,s55
v_mfma_f32_16x16x32_bf16 v[28:31],a[112:115],v[56:59],v[28:31]
ds_read_b128 v[52:55],v2 offset:38912
v_mfma_f32_16x16x32_bf16 v[32:35],a[116:119],v[56:59],v[32:35]
buffer_load_dwordx4 v0,s[36:39],s61 offen offset:1024 sc1 lds
v_mfma_f32_16x16x32_bf16 v[36:39],a[120:123],v[56:59],v[36:39]
v_mfma_f32_16x16x32_bf16 v[40:43],a[124:127],v[56:59],v[40:43]
s_waitcnt lgkmcnt(2)
v_mfma_f32_16x16x32_bf16 v[28:31],a[128:131],v[44:47],v[28:31]
ds_read_b128 v[56:59],v2 offset:39936
v_mfma_f32_16x16x32_bf16 v[32:35],a[132:135],v[44:47],v[32:35]
buffer_load_dwordx4 v0,s[36:39],s61 offen offset:2048 sc1 lds
v_mfma_f32_16x16x32_bf16 v[36:39],a[136:139],v[44:47],v[36:39]
v_mfma_f32_16x16x32_bf16 v[40:43],a[140:143],v[44:47],v[40:43]
s_waitcnt lgkmcnt(2)
v_mfma_f32_16x16x32_bf16 v[28:31],a[144:147],v[48:51],v[28:31]
ds_read_b128 v[44:47],v2 offset:40960
v_mfma_f32_16x16x32_bf16 v[32:35],a[148:151],v[48:51],v[32:35]
buffer_load_dwordx4 v0,s[36:39],s61 offen offset:3072 sc1 lds
v_mfma_f32_16x16x32_bf16 v[36:39],a[152:155],v[48:51],v[36:39]
s_add_u32 m0,s64,0
s_cmp_lt_u32 s67,s59
v_mfma_f32_16x16x32_bf16 v[40:43],a[156:159],v[48:51],v[40:43]
s_waitcnt lgkmcnt(2)
v_mfma_f32_16x16x32_bf16 v[28:31],a[160:163],v[52:55],v[28:31]
ds_read_b128 v[48:51],v2 offset:41984
v_mfma_f32_16x16x32_bf16 v[32:35],a[164:167],v[52:55],v[32:35]
buffer_load_dwordx4 v[68:71],v9,s[36:39],s49 offen offset:0 sc1
v_mfma_f32_16x16x32_bf16 v[36:39],a[168:171],v[52:55],v[36:39]
s_mov_b64 s[48:49],s[50:51]
s_mov_b64 s[50:51],s[52:53]
s_mov_b32 s52,s74
v_mfma_f32_16x16x32_bf16 v[40:43],a[172:175],v[52:55],v[40:43]
s_waitcnt lgkmcnt(2)
v_mfma_f32_16x16x32_bf16 v[28:31],a[176:179],v[56:59],v[28:31]
ds_read_b128 v[52:55],v2 offset:43008
v_mfma_f32_16x16x32_bf16 v[32:35],a[180:183],v[56:59],v[32:35]
v_mfma_f32_16x16x32_bf16 v[36:39],a[184:187],v[56:59],v[36:39]
v_mfma_f32_16x16x32_bf16 v[40:43],a[188:191],v[56:59],v[40:43]
s_waitcnt lgkmcnt(2)
v_mfma_f32_16x16x32_bf16 v[28:31],a[192:195],v[44:47],v[28:31]
ds_read_b128 v[56:59],v2 offset:44032
v_mfma_f32_16x16x32_bf16 v[32:35],a[196:199],v[44:47],v[32:35]
v_mfma_f32_16x16x32_bf16 v[36:39],a[200:203],v[44:47],v[36:39]
s_waitcnt vmcnt(7) lgkmcnt(2)
v_mfma_f32_16x16x32_bf16 v[40:43],a[204:207],v[44:47],v[40:43]
s_barrier
v_mfma_f32_16x16x32_bf16 v[28:31],a[208:211],v[48:51],v[28:31]
ds_read_b128 v[44:47],v2 offset:45056
v_mfma_f32_16x16x32_bf16 v[32:35],a[212:215],v[48:51],v[32:35]
v_mfma_f32_16x16x32_bf16 v[36:39],a[216:219],v[48:51],v[36:39]
buffer_load_dwordx4 v1,s[36:39],s61 offen offset:0 sc1 lds
v_mfma_f32_16x16x32_bf16 v[40:43],a[220:223],v[48:51],v[40:43]
s_waitcnt lgkmcnt(2)
v_mfma_f32_16x16x32_bf16 v[28:31],a[224:227],v[52:55],v[28:31]
ds_read_b128 v[48:51],v2 offset:46080
v_mfma_f32_16x16x32_bf16 v[32:35],a[228:231],v[52:55],v[32:35]
v_mfma_f32_16x16x32_bf16 v[36:39],a[232:235],v[52:55],v[36:39]
v_mfma_f32_16x16x32_bf16 v[40:43],a[236:239],v[52:55],v[40:43]
s_waitcnt lgkmcnt(2)
v_mfma_f32_16x16x32_bf16 v[28:31],a[240:243],v[56:59],v[28:31]
buffer_load_dwordx4 v1,s[36:39],s61 offen offset:1024 sc1 lds
v_mfma_f32_16x16x32_bf16 v[32:35],a[244:247],v[56:59],v[32:35]
ds_read_b128 v[52:55],v2 offset:47104
v_mfma_f32_16x16x32_bf16 v[36:39],a[248:251],v[56:59],v[36:39]
v_mfma_f32_16x16x32_bf16 v[40:43],a[252:255],v[56:59],v[40:43]
s_waitcnt lgkmcnt(2)
v_mfma_f32_16x16x32_bf16 v[28:31],v[100:103],v[44:47],v[28:31]
ds_read_b128 v[56:59],v2 offset:48128
v_mfma_f32_16x16x32_bf16 v[32:35],v[104:107],v[44:47],v[32:35]
ds_read_b128 v[88:91],v10 offset:0
v_mfma_f32_16x16x32_bf16 v[36:39],v[108:111],v[44:47],v[36:39]
buffer_load_dwordx4 v1,s[36:39],s61 offen offset:2048 sc1 lds
v_mfma_f32_16x16x32_bf16 v[40:43],v[112:115],v[44:47],v[40:43]
s_waitcnt lgkmcnt(3)
v_mfma_f32_16x16x32_bf16 v[28:31],v[116:119],v[48:51],v[28:31]
ds_read_b128 v[44:47],v2 offset:49152)"
R"(
v_mfma_f32_16x16x32_bf16 v[32:35],v[120:123],v[48:51],v[32:35]
ds_read_b128 v[76:79],v10 offset:1024
v_mfma_f32_16x16x32_bf16 v[36:39],v[124:127],v[48:51],v[36:39]
v_mfma_f32_16x16x32_bf16 v[40:43],v[128:131],v[48:51],v[40:43]
s_waitcnt lgkmcnt(4)
v_mfma_f32_16x16x32_bf16 v[28:31],v[132:135],v[52:55],v[28:31]
ds_read_b128 v[48:51],v2 offset:50176
v_mfma_f32_16x16x32_bf16 v[32:35],v[136:139],v[52:55],v[32:35]
ds_read_b128 v[80:83],v10 offset:2048
v_mfma_f32_16x16x32_bf16 v[36:39],v[140:143],v[52:55],v[36:39]
v_mfma_f32_16x16x32_bf16 v[40:43],v[144:147],v[52:55],v[40:43]
s_waitcnt lgkmcnt(4)
v_mfma_f32_16x16x32_bf16 v[28:31],v[148:151],v[56:59],v[28:31]
ds_read_b128 v[52:55],v2 offset:51200
v_mfma_f32_16x16x32_bf16 v[32:35],v[152:155],v[56:59],v[32:35]
ds_read_b128 v[84:87],v10 offset:3072
v_mfma_f32_16x16x32_bf16 v[36:39],v[156:159],v[56:59],v[36:39]
v_mfma_f32_16x16x32_bf16 v[40:43],v[88:91],v[56:59],v[40:43]
s_waitcnt lgkmcnt(4)
v_mfma_f32_16x16x32_bf16 v[28:31],v[160:163],v[44:47],v[28:31]
ds_read_b128 v[56:59],v2 offset:52224
v_mfma_f32_16x16x32_bf16 v[32:35],v[164:167],v[44:47],v[32:35]
ds_read_b128 v[88:91],v10 offset:4096
v_mfma_f32_16x16x32_bf16 v[36:39],v[168:171],v[44:47],v[36:39]
v_mfma_f32_16x16x32_bf16 v[40:43],v[76:79],v[44:47],v[40:43]
s_waitcnt lgkmcnt(4)
v_mfma_f32_16x16x32_bf16 v[28:31],v[172:175],v[48:51],v[28:31]
ds_read_b128 v[44:47],v2 offset:53248
v_mfma_f32_16x16x32_bf16 v[32:35],v[176:179],v[48:51],v[32:35]
ds_read_b128 v[76:79],v10 offset:5120
v_mfma_f32_16x16x32_bf16 v[36:39],v[180:183],v[48:51],v[36:39]
s_waitcnt vmcnt(8) lgkmcnt(4)
v_mfma_f32_16x16x32_bf16 v[40:43],v[80:83],v[48:51],v[40:43]
s_barrier
v_mfma_f32_16x16x32_bf16 v[28:31],v[184:187],v[52:55],v[28:31]
ds_read_b128 v[48:51],v2 offset:54272
v_mfma_f32_16x16x32_bf16 v[32:35],v[188:191],v[52:55],v[32:35]
ds_read_b128 v[80:83],v10 offset:6144
v_mfma_f32_16x16x32_bf16 v[36:39],v[192:195],v[52:55],v[36:39]
v_mfma_f32_16x16x32_bf16 v[40:43],v[84:87],v[52:55],v[40:43]
s_waitcnt lgkmcnt(4)
v_mfma_f32_16x16x32_bf16 v[28:31],v[196:199],v[56:59],v[28:31]
ds_read_b128 v[52:55],v2 offset:55296
v_mfma_f32_16x16x32_bf16 v[32:35],v[200:203],v[56:59],v[32:35]
ds_read_b128 v[84:87],v10 offset:7168
v_mfma_f32_16x16x32_bf16 v[36:39],v[204:207],v[56:59],v[36:39]
v_mfma_f32_16x16x32_bf16 v[40:43],v[88:91],v[56:59],v[40:43]
s_waitcnt lgkmcnt(4)
v_mfma_f32_16x16x32_bf16 v[28:31],v[208:211],v[44:47],v[28:31]
ds_read_b128 v[56:59],v2 offset:56320
v_mfma_f32_16x16x32_bf16 v[32:35],v[212:215],v[44:47],v[32:35]
ds_read_b128 v[88:91],v10 offset:8192
v_mfma_f32_16x16x32_bf16 v[36:39],v[216:219],v[44:47],v[36:39]
s_waitcnt vmcnt(3) lgkmcnt(4)
v_mfma_f32_16x16x32_bf16 v[40:43],v[76:79],v[44:47],v[40:43]
s_barrier
v_mfma_f32_16x16x32_bf16 v[28:31],v[220:223],v[48:51],v[28:31]
ds_read_b128 v[44:47],v2 offset:0
v_mfma_f32_16x16x32_bf16 v[32:35],v[224:227],v[48:51],v[32:35]
v_mfma_f32_16x16x32_bf16 v[36:39],v[228:231],v[48:51],v[36:39]
v_mfma_f32_16x16x32_bf16 v[40:43],v[80:83],v[48:51],v[40:43]
s_waitcnt lgkmcnt(3)
v_mfma_f32_16x16x32_bf16 v[28:31],v[232:235],v[52:55],v[28:31]
ds_read_b128 v[48:51],v2 offset:1024
v_mfma_f32_16x16x32_bf16 v[32:35],v[236:239],v[52:55],v[32:35]
v_mfma_f32_16x16x32_bf16 v[36:39],v[240:243],v[52:55],v[36:39]
v_mfma_f32_16x16x32_bf16 v[40:43],v[84:87],v[52:55],v[40:43]
s_waitcnt lgkmcnt(2)
v_mfma_f32_16x16x32_bf16 v[28:31],v[244:247],v[56:59],v[28:31]
ds_read_b128 v[52:55],v2 offset:2048
v_mfma_f32_16x16x32_bf16 v[32:35],v[248:251],v[56:59],v[32:35]
v_mfma_f32_16x16x32_bf16 v[36:39],v[252:255],v[56:59],v[36:39]
s_waitcnt lgkmcnt(2)
v_mfma_f32_16x16x32_bf16 v[40:43],v[88:91],v[56:59],v[40:43]
s_cbranch_scc1 Lg4_d0w1_%=
s_branch Lg4_slow0_%=
Lg4_d0w2_%=:
v_mfma_f32_16x16x32_bf16 v[12:15],a[0:3],v[44:47],0
s_mov_b32 s73,s47
ds_read_b128 v[56:59],v2 offset:3072
v_lshlrev_b32 v60,16,v68
v_mfma_f32_16x16x32_bf16 v[16:19],a[4:7],v[44:47],0
s_max_u32 s73,s73,s65
v_and_b32 v61,0xffff0000,v68
v_lshlrev_b32 v62,16,v69
v_mfma_f32_16x16x32_bf16 v[20:23],a[8:11],v[44:47],0
s_mov_b32 s65,0
v_and_b32 v63,0xffff0000,v69
v_lshlrev_b32 v64,16,v70
v_mfma_f32_16x16x32_bf16 v[24:27],a[12:15],v[44:47],0
s_waitcnt lgkmcnt(2)
v_and_b32 v65,0xffff0000,v70
v_lshlrev_b32 v66,16,v71
v_mfma_f32_16x16x32_bf16 v[12:15],a[16:19],v[48:51],v[12:15]
s_mul_i32 s61,s49,7
ds_read_b128 v[44:47],v2 offset:4096
v_and_b32 v67,0xffff0000,v71
v_mfma_f32_16x16x32_bf16 v[16:19],a[20:23],v[48:51],v[16:19]
s_lshr_b32 s61,s61,1
v_cvt_pk_bf16_f32 v28,v28,v29
v_cvt_pk_bf16_f32 v29,v30,v31
v_mfma_f32_16x16x32_bf16 v[20:23],a[24:27],v[48:51],v[20:23]
v_cvt_pk_bf16_f32 v30,v32,v33
v_cvt_pk_bf16_f32 v31,v34,v35
v_mfma_f32_16x16x32_bf16 v[24:27],a[28:31],v[48:51],v[24:27]
s_waitcnt lgkmcnt(2)
v_add_f32 v36,v36,v60
v_add_f32 v37,v37,v61
v_mfma_f32_16x16x32_bf16 v[12:15],a[32:35],v[52:55],v[12:15]
ds_read_b128 v[48:51],v2 offset:5120
v_add_f32 v38,v38,v62
v_mfma_f32_16x16x32_bf16 v[16:19],a[36:39],v[52:55],v[16:19]
v_add_f32 v39,v39,v63
v_add_f32 v40,v40,v64
v_mfma_f32_16x16x32_bf16 v[20:23],a[40:43],v[52:55],v[20:23]
buffer_store_dwordx4 v[28:31],v3,s[36:39],s73 offen offset:0 sc0 sc1
v_add_f32 v41,v41,v65
v_add_f32 v42,v42,v66
v_add_f32 v43,v43,v67
v_mfma_f32_16x16x32_bf16 v[24:27],a[44:47],v[52:55],v[24:27]
s_waitcnt lgkmcnt(2)
v_cvt_pk_bf16_f32 v36,v36,v37
v_cvt_pk_bf16_f32 v37,v38,v39
v_mfma_f32_16x16x32_bf16 v[12:15],a[48:51],v[56:59],v[12:15]
ds_read_b128 v[52:55],v2 offset:6144
v_cvt_pk_bf16_f32 v38,v40,v41
v_mfma_f32_16x16x32_bf16 v[16:19],a[52:55],v[56:59],v[16:19]
v_cvt_pk_bf16_f32 v39,v42,v43
v_mfma_f32_16x16x32_bf16 v[20:23],a[56:59],v[56:59],v[20:23]
s_waitcnt lgkmcnt(2)
s_nop 0
v_mfma_f32_16x16x32_bf16 v[24:27],a[60:63],v[56:59],v[24:27]
s_barrier
v_mfma_f32_16x16x32_bf16 v[12:15],a[64:67],v[44:47],v[12:15]
ds_read_b128 v[56:59],v2 offset:7168
ds_read_b32 v11,v8 offset:4096
v_mfma_f32_16x16x32_bf16 v[16:19],a[68:71],v[44:47],v[16:19]
v_mfma_f32_16x16x32_bf16 v[20:23],a[72:75],v[44:47],v[20:23]
v_mfma_f32_16x16x32_bf16 v[24:27],a[76:79],v[44:47],v[24:27]
s_waitcnt lgkmcnt(3)
v_mfma_f32_16x16x32_bf16 v[12:15],a[80:83],v[48:51],v[12:15]
s_waitcnt lgkmcnt(0)
ds_read_b128 v[44:47],v2 offset:8192
v_mfma_f32_16x16x32_bf16 v[16:19],a[84:87],v[48:51],v[16:19]
v_mfma_f32_16x16x32_bf16 v[20:23],a[88:91],v[48:51],v[20:23]
buffer_store_dwordx4 v[36:39],v4,s[36:39],s73 offen offset:0 sc0 sc1
v_cmp_eq_u32 vcc,s60,v11
v_mfma_f32_16x16x32_bf16 v[24:27],a[92:95],v[48:51],v[24:27]
s_cmp_eq_u32 vcc_lo,-1
s_cselect_b32 s66,s49,s59
v_mfma_f32_16x16x32_bf16 v[12:15],a[96:99],v[52:55],v[12:15]
s_cselect_b32 s61,s61,s59
ds_read_b128 v[48:51],v2 offset:9216
s_add_u32 m0,s63,28672
v_mfma_f32_16x16x32_bf16 v[16:19],a[100:103],v[52:55],v[16:19]
s_add_u32 s53,s52,s55
s_cmp_eq_u32 s53,s56
s_cselect_b32 s53,s57,s53
v_mfma_f32_16x16x32_bf16 v[20:23],a[104:107],v[52:55],v[20:23]
buffer_load_dwordx4 v0,s[36:39],s61 offen offset:0 sc1 lds
v_mfma_f32_16x16x32_bf16 v[24:27],a[108:111],v[52:55],v[24:27]
s_cmp_eq_u32 s53,s58
s_cselect_b32 s53,s59,s53
v_mfma_f32_16x16x32_bf16 v[12:15],a[112:115],v[56:59],v[12:15]
s_cmp_ge_u32 s53,6520832
ds_read_b128 v[52:55],v2 offset:10240
s_cselect_b32 s55,196608,s55
v_mfma_f32_16x16x32_bf16 v[16:19],a[116:119],v[56:59],v[16:19]
v_mfma_f32_16x16x32_bf16 v[20:23],a[120:123],v[56:59],v[20:23]
buffer_load_dwordx4 v0,s[36:39],s61 offen offset:1024 sc1 lds
v_mfma_f32_16x16x32_bf16 v[24:27],a[124:127],v[56:59],v[24:27]
s_waitcnt lgkmcnt(2)
v_mfma_f32_16x16x32_bf16 v[12:15],a[128:131],v[44:47],v[12:15]
ds_read_b128 v[56:59],v2 offset:11264
v_mfma_f32_16x16x32_bf16 v[16:19],a[132:135],v[44:47],v[16:19]
v_mfma_f32_16x16x32_bf16 v[20:23],a[136:139],v[44:47],v[20:23]
buffer_load_dwordx4 v0,s[36:39],s61 offen offset:2048 sc1 lds
v_mfma_f32_16x16x32_bf16 v[24:27],a[140:143],v[44:47],v[24:27]
s_waitcnt lgkmcnt(2)
v_mfma_f32_16x16x32_bf16 v[12:15],a[144:147],v[48:51],v[12:15]
ds_read_b128 v[44:47],v2 offset:12288
v_mfma_f32_16x16x32_bf16 v[16:19],a[148:151],v[48:51],v[16:19]
v_mfma_f32_16x16x32_bf16 v[20:23],a[152:155],v[48:51],v[20:23]
buffer_load_dwordx4 v0,s[36:39],s61 offen offset:3072 sc1 lds
v_mfma_f32_16x16x32_bf16 v[24:27],a[156:159],v[48:51],v[24:27]
s_waitcnt lgkmcnt(2)
s_add_u32 m0,s64,28672
s_cmp_ge_u32 s66,s59
v_mfma_f32_16x16x32_bf16 v[12:15],a[160:163],v[52:55],v[12:15]
ds_read_b128 v[48:51],v2 offset:13312
v_mfma_f32_16x16x32_bf16 v[16:19],a[164:167],v[52:55],v[16:19]
v_mfma_f32_16x16x32_bf16 v[20:23],a[168:171],v[52:55],v[20:23]
buffer_load_dwordx4 v[68:71],v9,s[36:39],s48 offen offset:0 sc1
v_mfma_f32_16x16x32_bf16 v[24:27],a[172:175],v[52:55],v[24:27]
s_waitcnt lgkmcnt(2)
v_mfma_f32_16x16x32_bf16 v[12:15],a[176:179],v[56:59],v[12:15]
ds_read_b128 v[52:55],v2 offset:14336
v_mfma_f32_16x16x32_bf16 v[16:19],a[180:183],v[56:59],v[16:19]
v_mfma_f32_16x16x32_bf16 v[20:23],a[184:187],v[56:59],v[20:23]
v_mfma_f32_16x16x32_bf16 v[24:27],a[188:191],v[56:59],v[24:27])"
R"(
s_waitcnt lgkmcnt(2)
v_mfma_f32_16x16x32_bf16 v[12:15],a[192:195],v[44:47],v[12:15]
ds_read_b128 v[56:59],v2 offset:15360
v_mfma_f32_16x16x32_bf16 v[16:19],a[196:199],v[44:47],v[16:19]
v_mfma_f32_16x16x32_bf16 v[20:23],a[200:203],v[44:47],v[20:23]
s_waitcnt vmcnt(7) lgkmcnt(2)
v_mfma_f32_16x16x32_bf16 v[24:27],a[204:207],v[44:47],v[24:27]
s_barrier
v_mfma_f32_16x16x32_bf16 v[12:15],a[208:211],v[48:51],v[12:15]
ds_read_b128 v[44:47],v2 offset:16384
v_mfma_f32_16x16x32_bf16 v[16:19],a[212:215],v[48:51],v[16:19]
v_mfma_f32_16x16x32_bf16 v[20:23],a[216:219],v[48:51],v[20:23]
v_mfma_f32_16x16x32_bf16 v[24:27],a[220:223],v[48:51],v[24:27]
s_waitcnt lgkmcnt(2)
buffer_load_dwordx4 v1,s[36:39],s61 offen offset:0 sc1 lds
v_mfma_f32_16x16x32_bf16 v[12:15],a[224:227],v[52:55],v[12:15]
ds_read_b128 v[48:51],v2 offset:17408
v_mfma_f32_16x16x32_bf16 v[16:19],a[228:231],v[52:55],v[16:19]
v_mfma_f32_16x16x32_bf16 v[20:23],a[232:235],v[52:55],v[20:23]
v_mfma_f32_16x16x32_bf16 v[24:27],a[236:239],v[52:55],v[24:27]
s_waitcnt lgkmcnt(2)
v_mfma_f32_16x16x32_bf16 v[12:15],a[240:243],v[56:59],v[12:15]
ds_read_b128 v[52:55],v2 offset:18432
v_mfma_f32_16x16x32_bf16 v[16:19],a[244:247],v[56:59],v[16:19]
buffer_load_dwordx4 v1,s[36:39],s61 offen offset:1024 sc1 lds
v_mfma_f32_16x16x32_bf16 v[20:23],a[248:251],v[56:59],v[20:23]
v_mfma_f32_16x16x32_bf16 v[24:27],a[252:255],v[56:59],v[24:27]
s_waitcnt lgkmcnt(2)
v_mfma_f32_16x16x32_bf16 v[12:15],v[100:103],v[44:47],v[12:15]
ds_read_b128 v[56:59],v2 offset:19456
v_mfma_f32_16x16x32_bf16 v[16:19],v[104:107],v[44:47],v[16:19]
ds_read_b128 v[88:91],v10 offset:0
v_mfma_f32_16x16x32_bf16 v[20:23],v[108:111],v[44:47],v[20:23]
v_mfma_f32_16x16x32_bf16 v[24:27],v[112:115],v[44:47],v[24:27]
s_waitcnt lgkmcnt(3)
buffer_load_dwordx4 v1,s[36:39],s61 offen offset:2048 sc1 lds
v_mfma_f32_16x16x32_bf16 v[12:15],v[116:119],v[48:51],v[12:15]
ds_read_b128 v[44:47],v2 offset:20480
v_mfma_f32_16x16x32_bf16 v[16:19],v[120:123],v[48:51],v[16:19]
ds_read_b128 v[76:79],v10 offset:1024
v_mfma_f32_16x16x32_bf16 v[20:23],v[124:127],v[48:51],v[20:23]
v_mfma_f32_16x16x32_bf16 v[24:27],v[128:131],v[48:51],v[24:27]
s_waitcnt lgkmcnt(4)
v_mfma_f32_16x16x32_bf16 v[12:15],v[132:135],v[52:55],v[12:15]
ds_read_b128 v[48:51],v2 offset:21504
v_mfma_f32_16x16x32_bf16 v[16:19],v[136:139],v[52:55],v[16:19]
ds_read_b128 v[80:83],v10 offset:2048
v_mfma_f32_16x16x32_bf16 v[20:23],v[140:143],v[52:55],v[20:23]
v_mfma_f32_16x16x32_bf16 v[24:27],v[144:147],v[52:55],v[24:27]
s_waitcnt lgkmcnt(4)
v_mfma_f32_16x16x32_bf16 v[12:15],v[148:151],v[56:59],v[12:15]
ds_read_b128 v[52:55],v2 offset:22528
v_mfma_f32_16x16x32_bf16 v[16:19],v[152:155],v[56:59],v[16:19]
ds_read_b128 v[84:87],v10 offset:3072
v_mfma_f32_16x16x32_bf16 v[20:23],v[156:159],v[56:59],v[20:23]
v_mfma_f32_16x16x32_bf16 v[24:27],v[88:91],v[56:59],v[24:27]
s_waitcnt lgkmcnt(4)
v_mfma_f32_16x16x32_bf16 v[12:15],v[160:163],v[44:47],v[12:15]
ds_read_b128 v[56:59],v2 offset:23552
v_mfma_f32_16x16x32_bf16 v[16:19],v[164:167],v[44:47],v[16:19]
ds_read_b128 v[88:91],v10 offset:4096
v_mfma_f32_16x16x32_bf16 v[20:23],v[168:171],v[44:47],v[20:23]
v_mfma_f32_16x16x32_bf16 v[24:27],v[76:79],v[44:47],v[24:27]
s_waitcnt lgkmcnt(4)
v_mfma_f32_16x16x32_bf16 v[12:15],v[172:175],v[48:51],v[12:15]
ds_read_b128 v[44:47],v2 offset:24576
v_mfma_f32_16x16x32_bf16 v[16:19],v[176:179],v[48:51],v[16:19]
ds_read_b128 v[76:79],v10 offset:5120
v_mfma_f32_16x16x32_bf16 v[20:23],v[180:183],v[48:51],v[20:23]
s_waitcnt vmcnt(8) lgkmcnt(4)
v_mfma_f32_16x16x32_bf16 v[24:27],v[80:83],v[48:51],v[24:27]
s_barrier
v_mfma_f32_16x16x32_bf16 v[12:15],v[184:187],v[52:55],v[12:15]
ds_read_b128 v[48:51],v2 offset:25600
v_mfma_f32_16x16x32_bf16 v[16:19],v[188:191],v[52:55],v[16:19]
ds_read_b128 v[80:83],v10 offset:6144
v_mfma_f32_16x16x32_bf16 v[20:23],v[192:195],v[52:55],v[20:23]
v_mfma_f32_16x16x32_bf16 v[24:27],v[84:87],v[52:55],v[24:27]
s_waitcnt lgkmcnt(4)
v_mfma_f32_16x16x32_bf16 v[12:15],v[196:199],v[56:59],v[12:15]
ds_read_b128 v[52:55],v2 offset:26624
v_mfma_f32_16x16x32_bf16 v[16:19],v[200:203],v[56:59],v[16:19]
ds_read_b128 v[84:87],v10 offset:7168
v_mfma_f32_16x16x32_bf16 v[20:23],v[204:207],v[56:59],v[20:23]
v_mfma_f32_16x16x32_bf16 v[24:27],v[88:91],v[56:59],v[24:27]
s_waitcnt lgkmcnt(4)
v_mfma_f32_16x16x32_bf16 v[12:15],v[208:211],v[44:47],v[12:15]
ds_read_b128 v[56:59],v2 offset:27648
v_mfma_f32_16x16x32_bf16 v[16:19],v[212:215],v[44:47],v[16:19]
ds_read_b128 v[88:91],v10 offset:8192
v_mfma_f32_16x16x32_bf16 v[20:23],v[216:219],v[44:47],v[20:23]
s_waitcnt vmcnt(3) lgkmcnt(4)
v_mfma_f32_16x16x32_bf16 v[24:27],v[76:79],v[44:47],v[24:27]
s_barrier
v_mfma_f32_16x16x32_bf16 v[12:15],v[220:223],v[48:51],v[12:15]
ds_read_b128 v[44:47],v2 offset:28672
v_mfma_f32_16x16x32_bf16 v[16:19],v[224:227],v[48:51],v[16:19]
v_mfma_f32_16x16x32_bf16 v[20:23],v[228:231],v[48:51],v[20:23]
v_mfma_f32_16x16x32_bf16 v[24:27],v[80:83],v[48:51],v[24:27]
s_waitcnt lgkmcnt(3)
v_mfma_f32_16x16x32_bf16 v[12:15],v[232:235],v[52:55],v[12:15]
ds_read_b128 v[48:51],v2 offset:29696
v_mfma_f32_16x16x32_bf16 v[16:19],v[236:239],v[52:55],v[16:19]
v_mfma_f32_16x16x32_bf16 v[20:23],v[240:243],v[52:55],v[20:23]
v_mfma_f32_16x16x32_bf16 v[24:27],v[84:87],v[52:55],v[24:27]
s_waitcnt lgkmcnt(2)
v_mfma_f32_16x16x32_bf16 v[12:15],v[244:247],v[56:59],v[12:15]
ds_read_b128 v[52:55],v2 offset:30720
v_mfma_f32_16x16x32_bf16 v[16:19],v[248:251],v[56:59],v[16:19]
v_mfma_f32_16x16x32_bf16 v[20:23],v[252:255],v[56:59],v[20:23]
s_waitcnt lgkmcnt(2)
v_mfma_f32_16x16x32_bf16 v[24:27],v[88:91],v[56:59],v[24:27]
s_cbranch_scc1 Lg4_slow1_%=
Lg4_d1w2_%=:
v_mfma_f32_16x16x32_bf16 v[28:31],a[0:3],v[44:47],0
s_mov_b32 s73,s48
ds_read_b128 v[56:59],v2 offset:31744
v_lshlrev_b32 v60,16,v68
v_mfma_f32_16x16x32_bf16 v[32:35],a[4:7],v[44:47],0
s_max_u32 s73,s73,s65
v_and_b32 v61,0xffff0000,v68
v_lshlrev_b32 v62,16,v69
v_mfma_f32_16x16x32_bf16 v[36:39],a[8:11],v[44:47],0
s_mov_b32 s65,0
v_and_b32 v63,0xffff0000,v69
v_lshlrev_b32 v64,16,v70
v_mfma_f32_16x16x32_bf16 v[40:43],a[12:15],v[44:47],0
s_waitcnt lgkmcnt(2)
v_and_b32 v65,0xffff0000,v70
v_lshlrev_b32 v66,16,v71
v_mfma_f32_16x16x32_bf16 v[28:31],a[16:19],v[48:51],v[28:31]
s_mul_i32 s61,s50,7
ds_read_b128 v[44:47],v2 offset:32768
v_and_b32 v67,0xffff0000,v71
v_mfma_f32_16x16x32_bf16 v[32:35],a[20:23],v[48:51],v[32:35]
s_lshr_b32 s61,s61,1
v_cvt_pk_bf16_f32 v12,v12,v13
v_cvt_pk_bf16_f32 v13,v14,v15
v_mfma_f32_16x16x32_bf16 v[36:39],a[24:27],v[48:51],v[36:39]
s_mov_b64 s[44:45],s[46:47]
v_cvt_pk_bf16_f32 v14,v16,v17
v_cvt_pk_bf16_f32 v15,v18,v19
v_mfma_f32_16x16x32_bf16 v[40:43],a[28:31],v[48:51],v[40:43]
s_waitcnt lgkmcnt(2)
v_add_f32 v20,v20,v60
v_add_f32 v21,v21,v61
v_mfma_f32_16x16x32_bf16 v[28:31],a[32:35],v[52:55],v[28:31]
s_mov_b64 s[46:47],s[48:49]
ds_read_b128 v[48:51],v2 offset:33792
v_add_f32 v22,v22,v62
v_mfma_f32_16x16x32_bf16 v[32:35],a[36:39],v[52:55],v[32:35]
v_add_f32 v23,v23,v63
v_add_f32 v24,v24,v64
v_mfma_f32_16x16x32_bf16 v[36:39],a[40:43],v[52:55],v[36:39]
buffer_store_dwordx4 v[12:15],v3,s[36:39],s73 offen offset:0 sc0 sc1
v_add_f32 v25,v25,v65
v_add_f32 v26,v26,v66
v_add_f32 v27,v27,v67
v_mfma_f32_16x16x32_bf16 v[40:43],a[44:47],v[52:55],v[40:43]
s_waitcnt lgkmcnt(2)
v_cvt_pk_bf16_f32 v20,v20,v21
v_cvt_pk_bf16_f32 v21,v22,v23
v_mfma_f32_16x16x32_bf16 v[28:31],a[48:51],v[56:59],v[28:31]
ds_read_b128 v[52:55],v2 offset:34816
v_cvt_pk_bf16_f32 v22,v24,v25
v_mfma_f32_16x16x32_bf16 v[32:35],a[52:55],v[56:59],v[32:35]
v_cvt_pk_bf16_f32 v23,v26,v27
v_mfma_f32_16x16x32_bf16 v[36:39],a[56:59],v[56:59],v[36:39]
s_waitcnt lgkmcnt(2)
s_nop 0
v_mfma_f32_16x16x32_bf16 v[40:43],a[60:63],v[56:59],v[40:43]
s_barrier
v_mfma_f32_16x16x32_bf16 v[28:31],a[64:67],v[44:47],v[28:31]
ds_read_b128 v[56:59],v2 offset:35840
ds_read_b32 v11,v8 offset:5120
v_mfma_f32_16x16x32_bf16 v[32:35],a[68:71],v[44:47],v[32:35]
v_mfma_f32_16x16x32_bf16 v[36:39],a[72:75],v[44:47],v[36:39]
v_mfma_f32_16x16x32_bf16 v[40:43],a[76:79],v[44:47],v[40:43]
s_waitcnt lgkmcnt(3)
v_mfma_f32_16x16x32_bf16 v[28:31],a[80:83],v[48:51],v[28:31]
s_waitcnt lgkmcnt(0)
ds_read_b128 v[44:47],v2 offset:36864
v_mfma_f32_16x16x32_bf16 v[32:35],a[84:87],v[48:51],v[32:35]
v_mfma_f32_16x16x32_bf16 v[36:39],a[88:91],v[48:51],v[36:39]
buffer_store_dwordx4 v[20:23],v4,s[36:39],s73 offen offset:0 sc0 sc1
v_cmp_eq_u32 vcc,s60,v11
v_mfma_f32_16x16x32_bf16 v[40:43],a[92:95],v[48:51],v[40:43]
s_cmp_eq_u32 vcc_lo,-1
s_cselect_b32 s67,s50,s59
v_mfma_f32_16x16x32_bf16 v[28:31],a[96:99],v[52:55],v[28:31]
s_cselect_b32 s61,s61,s59
ds_read_b128 v[48:51],v2 offset:37888
s_add_u32 m0,s63,0
v_mfma_f32_16x16x32_bf16 v[32:35],a[100:103],v[52:55],v[32:35]
s_add_u32 s74,s53,s55
s_cmp_eq_u32 s74,s56
s_cselect_b32 s74,s57,s74
v_mfma_f32_16x16x32_bf16 v[36:39],a[104:107],v[52:55],v[36:39]
buffer_load_dwordx4 v0,s[36:39],s61 offen offset:0 sc1 lds
v_mfma_f32_16x16x32_bf16 v[40:43],a[108:111],v[52:55],v[40:43])"
R"(
s_cmp_eq_u32 s74,s58
s_cselect_b32 s74,s59,s74
v_mfma_f32_16x16x32_bf16 v[28:31],a[112:115],v[56:59],v[28:31]
s_cmp_ge_u32 s74,6520832
ds_read_b128 v[52:55],v2 offset:38912
s_cselect_b32 s55,196608,s55
v_mfma_f32_16x16x32_bf16 v[32:35],a[116:119],v[56:59],v[32:35]
v_mfma_f32_16x16x32_bf16 v[36:39],a[120:123],v[56:59],v[36:39]
buffer_load_dwordx4 v0,s[36:39],s61 offen offset:1024 sc1 lds
v_mfma_f32_16x16x32_bf16 v[40:43],a[124:127],v[56:59],v[40:43]
s_waitcnt lgkmcnt(2)
v_mfma_f32_16x16x32_bf16 v[28:31],a[128:131],v[44:47],v[28:31]
ds_read_b128 v[56:59],v2 offset:39936
v_mfma_f32_16x16x32_bf16 v[32:35],a[132:135],v[44:47],v[32:35]
v_mfma_f32_16x16x32_bf16 v[36:39],a[136:139],v[44:47],v[36:39]
buffer_load_dwordx4 v0,s[36:39],s61 offen offset:2048 sc1 lds
v_mfma_f32_16x16x32_bf16 v[40:43],a[140:143],v[44:47],v[40:43]
s_waitcnt lgkmcnt(2)
v_mfma_f32_16x16x32_bf16 v[28:31],a[144:147],v[48:51],v[28:31]
ds_read_b128 v[44:47],v2 offset:40960
v_mfma_f32_16x16x32_bf16 v[32:35],a[148:151],v[48:51],v[32:35]
v_mfma_f32_16x16x32_bf16 v[36:39],a[152:155],v[48:51],v[36:39]
buffer_load_dwordx4 v0,s[36:39],s61 offen offset:3072 sc1 lds
v_mfma_f32_16x16x32_bf16 v[40:43],a[156:159],v[48:51],v[40:43]
s_waitcnt lgkmcnt(2)
s_add_u32 m0,s64,0
s_cmp_lt_u32 s67,s59
v_mfma_f32_16x16x32_bf16 v[28:31],a[160:163],v[52:55],v[28:31]
ds_read_b128 v[48:51],v2 offset:41984
v_mfma_f32_16x16x32_bf16 v[32:35],a[164:167],v[52:55],v[32:35]
v_mfma_f32_16x16x32_bf16 v[36:39],a[168:171],v[52:55],v[36:39]
buffer_load_dwordx4 v[68:71],v9,s[36:39],s49 offen offset:0 sc1
v_mfma_f32_16x16x32_bf16 v[40:43],a[172:175],v[52:55],v[40:43]
s_waitcnt lgkmcnt(2)
s_mov_b64 s[48:49],s[50:51]
s_mov_b64 s[50:51],s[52:53]
v_mfma_f32_16x16x32_bf16 v[28:31],a[176:179],v[56:59],v[28:31]
s_mov_b32 s52,s74
ds_read_b128 v[52:55],v2 offset:43008
v_mfma_f32_16x16x32_bf16 v[32:35],a[180:183],v[56:59],v[32:35]
v_mfma_f32_16x16x32_bf16 v[36:39],a[184:187],v[56:59],v[36:39]
v_mfma_f32_16x16x32_bf16 v[40:43],a[188:191],v[56:59],v[40:43]
s_waitcnt lgkmcnt(2)
v_mfma_f32_16x16x32_bf16 v[28:31],a[192:195],v[44:47],v[28:31]
ds_read_b128 v[56:59],v2 offset:44032
v_mfma_f32_16x16x32_bf16 v[32:35],a[196:199],v[44:47],v[32:35]
v_mfma_f32_16x16x32_bf16 v[36:39],a[200:203],v[44:47],v[36:39]
s_waitcnt vmcnt(7) lgkmcnt(2)
v_mfma_f32_16x16x32_bf16 v[40:43],a[204:207],v[44:47],v[40:43]
s_barrier
v_mfma_f32_16x16x32_bf16 v[28:31],a[208:211],v[48:51],v[28:31]
ds_read_b128 v[44:47],v2 offset:45056
v_mfma_f32_16x16x32_bf16 v[32:35],a[212:215],v[48:51],v[32:35]
v_mfma_f32_16x16x32_bf16 v[36:39],a[216:219],v[48:51],v[36:39]
v_mfma_f32_16x16x32_bf16 v[40:43],a[220:223],v[48:51],v[40:43]
s_waitcnt lgkmcnt(2)
buffer_load_dwordx4 v1,s[36:39],s61 offen offset:0 sc1 lds
v_mfma_f32_16x16x32_bf16 v[28:31],a[224:227],v[52:55],v[28:31]
ds_read_b128 v[48:51],v2 offset:46080
v_mfma_f32_16x16x32_bf16 v[32:35],a[228:231],v[52:55],v[32:35]
v_mfma_f32_16x16x32_bf16 v[36:39],a[232:235],v[52:55],v[36:39]
v_mfma_f32_16x16x32_bf16 v[40:43],a[236:239],v[52:55],v[40:43]
s_waitcnt lgkmcnt(2)
v_mfma_f32_16x16x32_bf16 v[28:31],a[240:243],v[56:59],v[28:31]
ds_read_b128 v[52:55],v2 offset:47104
v_mfma_f32_16x16x32_bf16 v[32:35],a[244:247],v[56:59],v[32:35]
buffer_load_dwordx4 v1,s[36:39],s61 offen offset:1024 sc1 lds
v_mfma_f32_16x16x32_bf16 v[36:39],a[248:251],v[56:59],v[36:39]
v_mfma_f32_16x16x32_bf16 v[40:43],a[252:255],v[56:59],v[40:43]
s_waitcnt lgkmcnt(2)
v_mfma_f32_16x16x32_bf16 v[28:31],v[100:103],v[44:47],v[28:31]
ds_read_b128 v[56:59],v2 offset:48128
v_mfma_f32_16x16x32_bf16 v[32:35],v[104:107],v[44:47],v[32:35]
ds_read_b128 v[88:91],v10 offset:0
v_mfma_f32_16x16x32_bf16 v[36:39],v[108:111],v[44:47],v[36:39]
v_mfma_f32_16x16x32_bf16 v[40:43],v[112:115],v[44:47],v[40:43]
s_waitcnt lgkmcnt(3)
buffer_load_dwordx4 v1,s[36:39],s61 offen offset:2048 sc1 lds
v_mfma_f32_16x16x32_bf16 v[28:31],v[116:119],v[48:51],v[28:31]
ds_read_b128 v[44:47],v2 offset:49152
v_mfma_f32_16x16x32_bf16 v[32:35],v[120:123],v[48:51],v[32:35]
ds_read_b128 v[76:79],v10 offset:1024
v_mfma_f32_16x16x32_bf16 v[36:39],v[124:127],v[48:51],v[36:39]
v_mfma_f32_16x16x32_bf16 v[40:43],v[128:131],v[48:51],v[40:43]
s_waitcnt lgkmcnt(4)
v_mfma_f32_16x16x32_bf16 v[28:31],v[132:135],v[52:55],v[28:31]
ds_read_b128 v[48:51],v2 offset:50176
v_mfma_f32_16x16x32_bf16 v[32:35],v[136:139],v[52:55],v[32:35]
ds_read_b128 v[80:83],v10 offset:2048
v_mfma_f32_16x16x32_bf16 v[36:39],v[140:143],v[52:55],v[36:39]
v_mfma_f32_16x16x32_bf16 v[40:43],v[144:147],v[52:55],v[40:43]
s_waitcnt lgkmcnt(4)
v_mfma_f32_16x16x32_bf16 v[28:31],v[148:151],v[56:59],v[28:31]
ds_read_b128 v[52:55],v2 offset:51200
v_mfma_f32_16x16x32_bf16 v[32:35],v[152:155],v[56:59],v[32:35]
ds_read_b128 v[84:87],v10 offset:3072
v_mfma_f32_16x16x32_bf16 v[36:39],v[156:159],v[56:59],v[36:39]
v_mfma_f32_16x16x32_bf16 v[40:43],v[88:91],v[56:59],v[40:43]
s_waitcnt lgkmcnt(4)
v_mfma_f32_16x16x32_bf16 v[28:31],v[160:163],v[44:47],v[28:31]
ds_read_b128 v[56:59],v2 offset:52224
v_mfma_f32_16x16x32_bf16 v[32:35],v[164:167],v[44:47],v[32:35]
ds_read_b128 v[88:91],v10 offset:4096
v_mfma_f32_16x16x32_bf16 v[36:39],v[168:171],v[44:47],v[36:39]
v_mfma_f32_16x16x32_bf16 v[40:43],v[76:79],v[44:47],v[40:43]
s_waitcnt lgkmcnt(4)
v_mfma_f32_16x16x32_bf16 v[28:31],v[172:175],v[48:51],v[28:31]
ds_read_b128 v[44:47],v2 offset:53248
v_mfma_f32_16x16x32_bf16 v[32:35],v[176:179],v[48:51],v[32:35]
ds_read_b128 v[76:79],v10 offset:5120
v_mfma_f32_16x16x32_bf16 v[36:39],v[180:183],v[48:51],v[36:39]
s_waitcnt vmcnt(8) lgkmcnt(4)
v_mfma_f32_16x16x32_bf16 v[40:43],v[80:83],v[48:51],v[40:43]
s_barrier
v_mfma_f32_16x16x32_bf16 v[28:31],v[184:187],v[52:55],v[28:31]
ds_read_b128 v[48:51],v2 offset:54272
v_mfma_f32_16x16x32_bf16 v[32:35],v[188:191],v[52:55],v[32:35]
ds_read_b128 v[80:83],v10 offset:6144
v_mfma_f32_16x16x32_bf16 v[36:39],v[192:195],v[52:55],v[36:39]
v_mfma_f32_16x16x32_bf16 v[40:43],v[84:87],v[52:55],v[40:43]
s_waitcnt lgkmcnt(4)
v_mfma_f32_16x16x32_bf16 v[28:31],v[196:199],v[56:59],v[28:31]
ds_read_b128 v[52:55],v2 offset:55296
v_mfma_f32_16x16x32_bf16 v[32:35],v[200:203],v[56:59],v[32:35]
ds_read_b128 v[84:87],v10 offset:7168
v_mfma_f32_16x16x32_bf16 v[36:39],v[204:207],v[56:59],v[36:39]
v_mfma_f32_16x16x32_bf16 v[40:43],v[88:91],v[56:59],v[40:43]
s_waitcnt lgkmcnt(4)
v_mfma_f32_16x16x32_bf16 v[28:31],v[208:211],v[44:47],v[28:31]
ds_read_b128 v[56:59],v2 offset:56320
v_mfma_f32_16x16x32_bf16 v[32:35],v[212:215],v[44:47],v[32:35]
ds_read_b128 v[88:91],v10 offset:8192
v_mfma_f32_16x16x32_bf16 v[36:39],v[216:219],v[44:47],v[36:39]
s_waitcnt vmcnt(3) lgkmcnt(4)
v_mfma_f32_16x16x32_bf16 v[40:43],v[76:79],v[44:47],v[40:43]
s_barrier
v_mfma_f32_16x16x32_bf16 v[28:31],v[220:223],v[48:51],v[28:31]
ds_read_b128 v[44:47],v2 offset:0
v_mfma_f32_16x16x32_bf16 v[32:35],v[224:227],v[48:51],v[32:35]
v_mfma_f32_16x16x32_bf16 v[36:39],v[228:231],v[48:51],v[36:39]
v_mfma_f32_16x16x32_bf16 v[40:43],v[80:83],v[48:51],v[40:43]
s_waitcnt lgkmcnt(3)
v_mfma_f32_16x16x32_bf16 v[28:31],v[232:235],v[52:55],v[28:31]
ds_read_b128 v[48:51],v2 offset:1024
v_mfma_f32_16x16x32_bf16 v[32:35],v[236:239],v[52:55],v[32:35]
v_mfma_f32_16x16x32_bf16 v[36:39],v[240:243],v[52:55],v[36:39]
v_mfma_f32_16x16x32_bf16 v[40:43],v[84:87],v[52:55],v[40:43]
s_waitcnt lgkmcnt(2)
v_mfma_f32_16x16x32_bf16 v[28:31],v[244:247],v[56:59],v[28:31]
ds_read_b128 v[52:55],v2 offset:2048
v_mfma_f32_16x16x32_bf16 v[32:35],v[248:251],v[56:59],v[32:35]
v_mfma_f32_16x16x32_bf16 v[36:39],v[252:255],v[56:59],v[36:39]
s_waitcnt lgkmcnt(2)
v_mfma_f32_16x16x32_bf16 v[40:43],v[88:91],v[56:59],v[40:43]
s_cbranch_scc1 Lg4_d0w2_%=
s_branch Lg4_slow0_%=
Lg4_d0w3_%=:
v_mfma_f32_16x16x32_bf16 v[12:15],a[0:3],v[44:47],0
s_mov_b32 s73,s47
ds_read_b128 v[56:59],v2 offset:3072
v_lshlrev_b32 v60,16,v68
v_mfma_f32_16x16x32_bf16 v[16:19],a[4:7],v[44:47],0
s_max_u32 s73,s73,s65
v_and_b32 v61,0xffff0000,v68
v_lshlrev_b32 v62,16,v69
v_mfma_f32_16x16x32_bf16 v[20:23],a[8:11],v[44:47],0
s_mov_b32 s65,0
v_and_b32 v63,0xffff0000,v69
v_lshlrev_b32 v64,16,v70
v_mfma_f32_16x16x32_bf16 v[24:27],a[12:15],v[44:47],0
s_waitcnt lgkmcnt(2)
v_and_b32 v65,0xffff0000,v70
v_lshlrev_b32 v66,16,v71
v_mfma_f32_16x16x32_bf16 v[12:15],a[16:19],v[48:51],v[12:15]
s_mul_i32 s61,s49,7
ds_read_b128 v[44:47],v2 offset:4096
v_and_b32 v67,0xffff0000,v71
v_mfma_f32_16x16x32_bf16 v[16:19],a[20:23],v[48:51],v[16:19]
s_lshr_b32 s61,s61,1
v_cvt_pk_bf16_f32 v28,v28,v29
v_cvt_pk_bf16_f32 v29,v30,v31
v_mfma_f32_16x16x32_bf16 v[20:23],a[24:27],v[48:51],v[20:23]
v_cvt_pk_bf16_f32 v30,v32,v33
v_cvt_pk_bf16_f32 v31,v34,v35
v_mfma_f32_16x16x32_bf16 v[24:27],a[28:31],v[48:51],v[24:27]
s_waitcnt lgkmcnt(2)
v_add_f32 v36,v36,v60
v_add_f32 v37,v37,v61
v_mfma_f32_16x16x32_bf16 v[12:15],a[32:35],v[52:55],v[12:15]
ds_read_b128 v[48:51],v2 offset:5120
v_add_f32 v38,v38,v62
v_mfma_f32_16x16x32_bf16 v[16:19],a[36:39],v[52:55],v[16:19]
v_add_f32 v39,v39,v63
v_add_f32 v40,v40,v64
v_mfma_f32_16x16x32_bf16 v[20:23],a[40:43],v[52:55],v[20:23]
v_add_f32 v41,v41,v65
v_add_f32 v42,v42,v66
v_mfma_f32_16x16x32_bf16 v[24:27],a[44:47],v[52:55],v[24:27]
s_waitcnt lgkmcnt(2)
buffer_store_dwordx4 v[28:31],v3,s[36:39],s73 offen offset:0 sc0 sc1)"
R"(
v_add_f32 v43,v43,v67
v_cvt_pk_bf16_f32 v36,v36,v37
v_cvt_pk_bf16_f32 v37,v38,v39
v_mfma_f32_16x16x32_bf16 v[12:15],a[48:51],v[56:59],v[12:15]
ds_read_b128 v[52:55],v2 offset:6144
v_cvt_pk_bf16_f32 v38,v40,v41
v_mfma_f32_16x16x32_bf16 v[16:19],a[52:55],v[56:59],v[16:19]
v_cvt_pk_bf16_f32 v39,v42,v43
v_mfma_f32_16x16x32_bf16 v[20:23],a[56:59],v[56:59],v[20:23]
s_waitcnt lgkmcnt(2)
s_nop 0
v_mfma_f32_16x16x32_bf16 v[24:27],a[60:63],v[56:59],v[24:27]
s_barrier
v_mfma_f32_16x16x32_bf16 v[12:15],a[64:67],v[44:47],v[12:15]
ds_read_b128 v[56:59],v2 offset:7168
ds_read_b32 v11,v8 offset:4096
v_mfma_f32_16x16x32_bf16 v[16:19],a[68:71],v[44:47],v[16:19]
v_mfma_f32_16x16x32_bf16 v[20:23],a[72:75],v[44:47],v[20:23]
v_mfma_f32_16x16x32_bf16 v[24:27],a[76:79],v[44:47],v[24:27]
s_waitcnt lgkmcnt(3)
v_mfma_f32_16x16x32_bf16 v[12:15],a[80:83],v[48:51],v[12:15]
s_waitcnt lgkmcnt(0)
ds_read_b128 v[44:47],v2 offset:8192
v_mfma_f32_16x16x32_bf16 v[16:19],a[84:87],v[48:51],v[16:19]
v_mfma_f32_16x16x32_bf16 v[20:23],a[88:91],v[48:51],v[20:23]
v_mfma_f32_16x16x32_bf16 v[24:27],a[92:95],v[48:51],v[24:27]
buffer_store_dwordx4 v[36:39],v4,s[36:39],s73 offen offset:0 sc0 sc1
v_cmp_eq_u32 vcc,s60,v11
v_mfma_f32_16x16x32_bf16 v[12:15],a[96:99],v[52:55],v[12:15]
s_cmp_eq_u32 vcc_lo,-1
ds_read_b128 v[48:51],v2 offset:9216
s_cselect_b32 s66,s49,s59
v_mfma_f32_16x16x32_bf16 v[16:19],a[100:103],v[52:55],v[16:19]
s_cselect_b32 s61,s61,s59
s_add_u32 m0,s63,28672
s_add_u32 s53,s52,s55
v_mfma_f32_16x16x32_bf16 v[20:23],a[104:107],v[52:55],v[20:23]
s_cmp_eq_u32 s53,s56
s_cselect_b32 s53,s57,s53
s_cmp_eq_u32 s53,s58
v_mfma_f32_16x16x32_bf16 v[24:27],a[108:111],v[52:55],v[24:27]
buffer_load_dwordx4 v0,s[36:39],s61 offen offset:0 sc1 lds
v_mfma_f32_16x16x32_bf16 v[12:15],a[112:115],v[56:59],v[12:15]
s_cselect_b32 s53,s59,s53
ds_read_b128 v[52:55],v2 offset:10240
s_cmp_ge_u32 s53,6520832
v_mfma_f32_16x16x32_bf16 v[16:19],a[116:119],v[56:59],v[16:19]
s_cselect_b32 s55,196608,s55
v_mfma_f32_16x16x32_bf16 v[20:23],a[120:123],v[56:59],v[20:23]
v_mfma_f32_16x16x32_bf16 v[24:27],a[124:127],v[56:59],v[24:27]
s_waitcnt lgkmcnt(2)
buffer_load_dwordx4 v0,s[36:39],s61 offen offset:1024 sc1 lds
v_mfma_f32_16x16x32_bf16 v[12:15],a[128:131],v[44:47],v[12:15]
ds_read_b128 v[56:59],v2 offset:11264
v_mfma_f32_16x16x32_bf16 v[16:19],a[132:135],v[44:47],v[16:19]
v_mfma_f32_16x16x32_bf16 v[20:23],a[136:139],v[44:47],v[20:23]
v_mfma_f32_16x16x32_bf16 v[24:27],a[140:143],v[44:47],v[24:27]
s_waitcnt lgkmcnt(2)
buffer_load_dwordx4 v0,s[36:39],s61 offen offset:2048 sc1 lds
v_mfma_f32_16x16x32_bf16 v[12:15],a[144:147],v[48:51],v[12:15]
ds_read_b128 v[44:47],v2 offset:12288
v_mfma_f32_16x16x32_bf16 v[16:19],a[148:151],v[48:51],v[16:19]
v_mfma_f32_16x16x32_bf16 v[20:23],a[152:155],v[48:51],v[20:23]
v_mfma_f32_16x16x32_bf16 v[24:27],a[156:159],v[48:51],v[24:27]
s_waitcnt lgkmcnt(2)
buffer_load_dwordx4 v0,s[36:39],s61 offen offset:3072 sc1 lds
v_mfma_f32_16x16x32_bf16 v[12:15],a[160:163],v[52:55],v[12:15]
s_add_u32 m0,s64,28672
ds_read_b128 v[48:51],v2 offset:13312
s_lshr_b32 s70,s47,4
v_mfma_f32_16x16x32_bf16 v[16:19],a[164:167],v[52:55],v[16:19]
s_cmp_ge_u32 s66,s59
v_mfma_f32_16x16x32_bf16 v[20:23],a[168:171],v[52:55],v[20:23]
v_mfma_f32_16x16x32_bf16 v[24:27],a[172:175],v[52:55],v[24:27]
s_waitcnt lgkmcnt(2)
buffer_load_dwordx4 v[68:71],v9,s[36:39],s48 offen offset:0 sc1
v_mfma_f32_16x16x32_bf16 v[12:15],a[176:179],v[56:59],v[12:15]
ds_read_b128 v[52:55],v2 offset:14336
v_mfma_f32_16x16x32_bf16 v[16:19],a[180:183],v[56:59],v[16:19]
v_mfma_f32_16x16x32_bf16 v[20:23],a[184:187],v[56:59],v[20:23]
v_mfma_f32_16x16x32_bf16 v[24:27],a[188:191],v[56:59],v[24:27]
s_waitcnt lgkmcnt(2)
v_mfma_f32_16x16x32_bf16 v[12:15],a[192:195],v[44:47],v[12:15]
ds_read_b128 v[56:59],v2 offset:15360
v_mfma_f32_16x16x32_bf16 v[16:19],a[196:199],v[44:47],v[16:19]
v_mfma_f32_16x16x32_bf16 v[20:23],a[200:203],v[44:47],v[20:23]
s_waitcnt vmcnt(8) lgkmcnt(2)
v_mfma_f32_16x16x32_bf16 v[24:27],a[204:207],v[44:47],v[24:27]
s_barrier
v_mfma_f32_16x16x32_bf16 v[12:15],a[208:211],v[48:51],v[12:15]
ds_read_b128 v[44:47],v2 offset:16384
v_mfma_f32_16x16x32_bf16 v[16:19],a[212:215],v[48:51],v[16:19]
v_mfma_f32_16x16x32_bf16 v[20:23],a[216:219],v[48:51],v[20:23]
v_mfma_f32_16x16x32_bf16 v[24:27],a[220:223],v[48:51],v[24:27]
s_waitcnt lgkmcnt(2)
v_mfma_f32_16x16x32_bf16 v[12:15],a[224:227],v[52:55],v[12:15]
buffer_load_dwordx4 v1,s[36:39],s61 offen offset:0 sc1 lds
v_mfma_f32_16x16x32_bf16 v[16:19],a[228:231],v[52:55],v[16:19]
ds_read_b128 v[48:51],v2 offset:17408
v_mfma_f32_16x16x32_bf16 v[20:23],a[232:235],v[52:55],v[20:23]
v_mfma_f32_16x16x32_bf16 v[24:27],a[236:239],v[52:55],v[24:27]
s_waitcnt lgkmcnt(2)
v_mfma_f32_16x16x32_bf16 v[12:15],a[240:243],v[56:59],v[12:15]
ds_read_b128 v[52:55],v2 offset:18432
v_mfma_f32_16x16x32_bf16 v[16:19],a[244:247],v[56:59],v[16:19]
v_mfma_f32_16x16x32_bf16 v[20:23],a[248:251],v[56:59],v[20:23]
buffer_load_dwordx4 v1,s[36:39],s61 offen offset:1024 sc1 lds
v_mfma_f32_16x16x32_bf16 v[24:27],a[252:255],v[56:59],v[24:27]
s_waitcnt lgkmcnt(2)
v_mfma_f32_16x16x32_bf16 v[12:15],v[100:103],v[44:47],v[12:15]
ds_read_b128 v[56:59],v2 offset:19456
v_mfma_f32_16x16x32_bf16 v[16:19],v[104:107],v[44:47],v[16:19]
ds_read_b128 v[88:91],v10 offset:0
v_mfma_f32_16x16x32_bf16 v[20:23],v[108:111],v[44:47],v[20:23]
v_mfma_f32_16x16x32_bf16 v[24:27],v[112:115],v[44:47],v[24:27]
s_waitcnt lgkmcnt(3)
v_mfma_f32_16x16x32_bf16 v[12:15],v[116:119],v[48:51],v[12:15]
buffer_load_dwordx4 v1,s[36:39],s61 offen offset:2048 sc1 lds
v_mfma_f32_16x16x32_bf16 v[16:19],v[120:123],v[48:51],v[16:19]
ds_read_b128 v[44:47],v2 offset:20480
ds_read_b128 v[76:79],v10 offset:1024
v_mfma_f32_16x16x32_bf16 v[20:23],v[124:127],v[48:51],v[20:23]
v_mfma_f32_16x16x32_bf16 v[24:27],v[128:131],v[48:51],v[24:27]
s_waitcnt lgkmcnt(4)
v_mfma_f32_16x16x32_bf16 v[12:15],v[132:135],v[52:55],v[12:15]
ds_read_b128 v[48:51],v2 offset:21504
v_mfma_f32_16x16x32_bf16 v[16:19],v[136:139],v[52:55],v[16:19]
ds_read_b128 v[80:83],v10 offset:2048
v_mfma_f32_16x16x32_bf16 v[20:23],v[140:143],v[52:55],v[20:23]
v_mfma_f32_16x16x32_bf16 v[24:27],v[144:147],v[52:55],v[24:27]
s_waitcnt lgkmcnt(4)
v_mfma_f32_16x16x32_bf16 v[12:15],v[148:151],v[56:59],v[12:15]
ds_read_b128 v[52:55],v2 offset:22528
v_mfma_f32_16x16x32_bf16 v[16:19],v[152:155],v[56:59],v[16:19]
ds_read_b128 v[84:87],v10 offset:3072
v_mfma_f32_16x16x32_bf16 v[20:23],v[156:159],v[56:59],v[20:23]
v_mfma_f32_16x16x32_bf16 v[24:27],v[88:91],v[56:59],v[24:27]
s_waitcnt lgkmcnt(4)
v_mfma_f32_16x16x32_bf16 v[12:15],v[160:163],v[44:47],v[12:15]
ds_read_b128 v[56:59],v2 offset:23552
v_mfma_f32_16x16x32_bf16 v[16:19],v[164:167],v[44:47],v[16:19]
ds_read_b128 v[88:91],v10 offset:4096
v_mfma_f32_16x16x32_bf16 v[20:23],v[168:171],v[44:47],v[20:23]
v_mfma_f32_16x16x32_bf16 v[24:27],v[76:79],v[44:47],v[24:27]
s_waitcnt lgkmcnt(4)
v_mfma_f32_16x16x32_bf16 v[12:15],v[172:175],v[48:51],v[12:15]
ds_read_b128 v[44:47],v2 offset:24576
v_mfma_f32_16x16x32_bf16 v[16:19],v[176:179],v[48:51],v[16:19]
ds_read_b128 v[76:79],v10 offset:5120
v_mfma_f32_16x16x32_bf16 v[20:23],v[180:183],v[48:51],v[20:23]
s_waitcnt vmcnt(8) lgkmcnt(4)
v_mfma_f32_16x16x32_bf16 v[24:27],v[80:83],v[48:51],v[24:27]
s_barrier
v_mfma_f32_16x16x32_bf16 v[12:15],v[184:187],v[52:55],v[12:15]
buffer_store_dword v6,v5,s[36:39],s70 offen offset:0 sc0 sc1
ds_read_b128 v[48:51],v2 offset:25600
v_mfma_f32_16x16x32_bf16 v[16:19],v[188:191],v[52:55],v[16:19]
ds_read_b128 v[80:83],v10 offset:6144
v_mfma_f32_16x16x32_bf16 v[20:23],v[192:195],v[52:55],v[20:23]
v_mfma_f32_16x16x32_bf16 v[24:27],v[84:87],v[52:55],v[24:27]
s_waitcnt lgkmcnt(4)
v_mfma_f32_16x16x32_bf16 v[12:15],v[196:199],v[56:59],v[12:15]
ds_read_b128 v[52:55],v2 offset:26624
v_mfma_f32_16x16x32_bf16 v[16:19],v[200:203],v[56:59],v[16:19]
ds_read_b128 v[84:87],v10 offset:7168
v_mfma_f32_16x16x32_bf16 v[20:23],v[204:207],v[56:59],v[20:23]
v_mfma_f32_16x16x32_bf16 v[24:27],v[88:91],v[56:59],v[24:27]
s_waitcnt lgkmcnt(4)
v_mfma_f32_16x16x32_bf16 v[12:15],v[208:211],v[44:47],v[12:15]
ds_read_b128 v[56:59],v2 offset:27648
v_mfma_f32_16x16x32_bf16 v[16:19],v[212:215],v[44:47],v[16:19]
ds_read_b128 v[88:91],v10 offset:8192
v_mfma_f32_16x16x32_bf16 v[20:23],v[216:219],v[44:47],v[20:23]
s_waitcnt vmcnt(4) lgkmcnt(4)
v_mfma_f32_16x16x32_bf16 v[24:27],v[76:79],v[44:47],v[24:27]
s_barrier
v_mfma_f32_16x16x32_bf16 v[12:15],v[220:223],v[48:51],v[12:15]
ds_read_b128 v[44:47],v2 offset:28672
v_mfma_f32_16x16x32_bf16 v[16:19],v[224:227],v[48:51],v[16:19]
v_mfma_f32_16x16x32_bf16 v[20:23],v[228:231],v[48:51],v[20:23]
v_mfma_f32_16x16x32_bf16 v[24:27],v[80:83],v[48:51],v[24:27]
s_waitcnt lgkmcnt(3)
v_mfma_f32_16x16x32_bf16 v[12:15],v[232:235],v[52:55],v[12:15]
ds_read_b128 v[48:51],v2 offset:29696
v_mfma_f32_16x16x32_bf16 v[16:19],v[236:239],v[52:55],v[16:19]
v_mfma_f32_16x16x32_bf16 v[20:23],v[240:243],v[52:55],v[20:23]
v_mfma_f32_16x16x32_bf16 v[24:27],v[84:87],v[52:55],v[24:27]
s_waitcnt lgkmcnt(2)
v_mfma_f32_16x16x32_bf16 v[12:15],v[244:247],v[56:59],v[12:15]
ds_read_b128 v[52:55],v2 offset:30720
v_mfma_f32_16x16x32_bf16 v[16:19],v[248:251],v[56:59],v[16:19]
v_mfma_f32_16x16x32_bf16 v[20:23],v[252:255],v[56:59],v[20:23]
s_waitcnt lgkmcnt(2)
v_mfma_f32_16x16x32_bf16 v[24:27],v[88:91],v[56:59],v[24:27]
s_cbranch_scc1 Lg4_slow1_%=
Lg4_d1w3_%=:
v_mfma_f32_16x16x32_bf16 v[28:31],a[0:3],v[44:47],0
s_mov_b32 s73,s48
ds_read_b128 v[56:59],v2 offset:31744
v_lshlrev_b32 v60,16,v68)"
R"(
v_mfma_f32_16x16x32_bf16 v[32:35],a[4:7],v[44:47],0
s_max_u32 s73,s73,s65
v_and_b32 v61,0xffff0000,v68
v_lshlrev_b32 v62,16,v69
v_mfma_f32_16x16x32_bf16 v[36:39],a[8:11],v[44:47],0
s_mov_b32 s65,0
v_and_b32 v63,0xffff0000,v69
v_lshlrev_b32 v64,16,v70
v_mfma_f32_16x16x32_bf16 v[40:43],a[12:15],v[44:47],0
s_waitcnt lgkmcnt(2)
v_and_b32 v65,0xffff0000,v70
v_lshlrev_b32 v66,16,v71
v_mfma_f32_16x16x32_bf16 v[28:31],a[16:19],v[48:51],v[28:31]
s_mul_i32 s61,s50,7
ds_read_b128 v[44:47],v2 offset:32768
v_and_b32 v67,0xffff0000,v71
v_mfma_f32_16x16x32_bf16 v[32:35],a[20:23],v[48:51],v[32:35]
s_lshr_b32 s61,s61,1
v_cvt_pk_bf16_f32 v12,v12,v13
v_cvt_pk_bf16_f32 v13,v14,v15
v_mfma_f32_16x16x32_bf16 v[36:39],a[24:27],v[48:51],v[36:39]
s_mov_b64 s[44:45],s[46:47]
v_cvt_pk_bf16_f32 v14,v16,v17
v_cvt_pk_bf16_f32 v15,v18,v19
v_mfma_f32_16x16x32_bf16 v[40:43],a[28:31],v[48:51],v[40:43]
s_waitcnt lgkmcnt(2)
v_add_f32 v20,v20,v60
v_add_f32 v21,v21,v61
v_mfma_f32_16x16x32_bf16 v[28:31],a[32:35],v[52:55],v[28:31]
s_mov_b64 s[46:47],s[48:49]
ds_read_b128 v[48:51],v2 offset:33792
v_add_f32 v22,v22,v62
v_mfma_f32_16x16x32_bf16 v[32:35],a[36:39],v[52:55],v[32:35]
v_add_f32 v23,v23,v63
v_add_f32 v24,v24,v64
v_mfma_f32_16x16x32_bf16 v[36:39],a[40:43],v[52:55],v[36:39]
v_add_f32 v25,v25,v65
v_add_f32 v26,v26,v66
v_mfma_f32_16x16x32_bf16 v[40:43],a[44:47],v[52:55],v[40:43]
s_waitcnt lgkmcnt(2)
buffer_store_dwordx4 v[12:15],v3,s[36:39],s73 offen offset:0 sc0 sc1
v_add_f32 v27,v27,v67
v_cvt_pk_bf16_f32 v20,v20,v21
v_cvt_pk_bf16_f32 v21,v22,v23
v_mfma_f32_16x16x32_bf16 v[28:31],a[48:51],v[56:59],v[28:31]
ds_read_b128 v[52:55],v2 offset:34816
v_cvt_pk_bf16_f32 v22,v24,v25
v_mfma_f32_16x16x32_bf16 v[32:35],a[52:55],v[56:59],v[32:35]
v_cvt_pk_bf16_f32 v23,v26,v27
v_mfma_f32_16x16x32_bf16 v[36:39],a[56:59],v[56:59],v[36:39]
s_waitcnt lgkmcnt(2)
s_nop 0
v_mfma_f32_16x16x32_bf16 v[40:43],a[60:63],v[56:59],v[40:43]
s_barrier
v_mfma_f32_16x16x32_bf16 v[28:31],a[64:67],v[44:47],v[28:31]
ds_read_b128 v[56:59],v2 offset:35840
ds_read_b32 v11,v8 offset:5120
v_mfma_f32_16x16x32_bf16 v[32:35],a[68:71],v[44:47],v[32:35]
v_mfma_f32_16x16x32_bf16 v[36:39],a[72:75],v[44:47],v[36:39]
v_mfma_f32_16x16x32_bf16 v[40:43],a[76:79],v[44:47],v[40:43]
s_waitcnt lgkmcnt(3)
v_mfma_f32_16x16x32_bf16 v[28:31],a[80:83],v[48:51],v[28:31]
s_waitcnt lgkmcnt(0)
ds_read_b128 v[44:47],v2 offset:36864
v_mfma_f32_16x16x32_bf16 v[32:35],a[84:87],v[48:51],v[32:35]
v_mfma_f32_16x16x32_bf16 v[36:39],a[88:91],v[48:51],v[36:39]
v_mfma_f32_16x16x32_bf16 v[40:43],a[92:95],v[48:51],v[40:43]
buffer_store_dwordx4 v[20:23],v4,s[36:39],s73 offen offset:0 sc0 sc1
v_cmp_eq_u32 vcc,s60,v11
v_mfma_f32_16x16x32_bf16 v[28:31],a[96:99],v[52:55],v[28:31]
s_cmp_eq_u32 vcc_lo,-1
ds_read_b128 v[48:51],v2 offset:37888
s_cselect_b32 s67,s50,s59
v_mfma_f32_16x16x32_bf16 v[32:35],a[100:103],v[52:55],v[32:35]
s_cselect_b32 s61,s61,s59
s_add_u32 m0,s63,0
s_add_u32 s74,s53,s55
v_mfma_f32_16x16x32_bf16 v[36:39],a[104:107],v[52:55],v[36:39]
s_cmp_eq_u32 s74,s56
s_cselect_b32 s74,s57,s74
s_cmp_eq_u32 s74,s58
v_mfma_f32_16x16x32_bf16 v[40:43],a[108:111],v[52:55],v[40:43]
buffer_load_dwordx4 v0,s[36:39],s61 offen offset:0 sc1 lds
v_mfma_f32_16x16x32_bf16 v[28:31],a[112:115],v[56:59],v[28:31]
s_cselect_b32 s74,s59,s74
ds_read_b128 v[52:55],v2 offset:38912
s_cmp_ge_u32 s74,6520832
v_mfma_f32_16x16x32_bf16 v[32:35],a[116:119],v[56:59],v[32:35]
s_cselect_b32 s55,196608,s55
v_mfma_f32_16x16x32_bf16 v[36:39],a[120:123],v[56:59],v[36:39]
v_mfma_f32_16x16x32_bf16 v[40:43],a[124:127],v[56:59],v[40:43]
s_waitcnt lgkmcnt(2)
buffer_load_dwordx4 v0,s[36:39],s61 offen offset:1024 sc1 lds
v_mfma_f32_16x16x32_bf16 v[28:31],a[128:131],v[44:47],v[28:31]
ds_read_b128 v[56:59],v2 offset:39936
v_mfma_f32_16x16x32_bf16 v[32:35],a[132:135],v[44:47],v[32:35]
v_mfma_f32_16x16x32_bf16 v[36:39],a[136:139],v[44:47],v[36:39]
v_mfma_f32_16x16x32_bf16 v[40:43],a[140:143],v[44:47],v[40:43]
s_waitcnt lgkmcnt(2)
buffer_load_dwordx4 v0,s[36:39],s61 offen offset:2048 sc1 lds
v_mfma_f32_16x16x32_bf16 v[28:31],a[144:147],v[48:51],v[28:31]
ds_read_b128 v[44:47],v2 offset:40960
v_mfma_f32_16x16x32_bf16 v[32:35],a[148:151],v[48:51],v[32:35]
v_mfma_f32_16x16x32_bf16 v[36:39],a[152:155],v[48:51],v[36:39]
v_mfma_f32_16x16x32_bf16 v[40:43],a[156:159],v[48:51],v[40:43]
s_waitcnt lgkmcnt(2)
buffer_load_dwordx4 v0,s[36:39],s61 offen offset:3072 sc1 lds
v_mfma_f32_16x16x32_bf16 v[28:31],a[160:163],v[52:55],v[28:31]
s_add_u32 m0,s64,0
ds_read_b128 v[48:51],v2 offset:41984
s_lshr_b32 s70,s48,4
v_mfma_f32_16x16x32_bf16 v[32:35],a[164:167],v[52:55],v[32:35]
s_cmp_lt_u32 s67,s59
v_mfma_f32_16x16x32_bf16 v[36:39],a[168:171],v[52:55],v[36:39]
v_mfma_f32_16x16x32_bf16 v[40:43],a[172:175],v[52:55],v[40:43]
s_waitcnt lgkmcnt(2)
buffer_load_dwordx4 v[68:71],v9,s[36:39],s49 offen offset:0 sc1
v_mfma_f32_16x16x32_bf16 v[28:31],a[176:179],v[56:59],v[28:31]
s_mov_b64 s[48:49],s[50:51]
ds_read_b128 v[52:55],v2 offset:43008
s_mov_b64 s[50:51],s[52:53]
v_mfma_f32_16x16x32_bf16 v[32:35],a[180:183],v[56:59],v[32:35]
s_mov_b32 s52,s74
v_mfma_f32_16x16x32_bf16 v[36:39],a[184:187],v[56:59],v[36:39]
v_mfma_f32_16x16x32_bf16 v[40:43],a[188:191],v[56:59],v[40:43]
s_waitcnt lgkmcnt(2)
v_mfma_f32_16x16x32_bf16 v[28:31],a[192:195],v[44:47],v[28:31]
ds_read_b128 v[56:59],v2 offset:44032
v_mfma_f32_16x16x32_bf16 v[32:35],a[196:199],v[44:47],v[32:35]
v_mfma_f32_16x16x32_bf16 v[36:39],a[200:203],v[44:47],v[36:39]
s_waitcnt vmcnt(8) lgkmcnt(2)
v_mfma_f32_16x16x32_bf16 v[40:43],a[204:207],v[44:47],v[40:43]
s_barrier
v_mfma_f32_16x16x32_bf16 v[28:31],a[208:211],v[48:51],v[28:31]
ds_read_b128 v[44:47],v2 offset:45056
v_mfma_f32_16x16x32_bf16 v[32:35],a[212:215],v[48:51],v[32:35]
v_mfma_f32_16x16x32_bf16 v[36:39],a[216:219],v[48:51],v[36:39]
v_mfma_f32_16x16x32_bf16 v[40:43],a[220:223],v[48:51],v[40:43]
s_waitcnt lgkmcnt(2)
v_mfma_f32_16x16x32_bf16 v[28:31],a[224:227],v[52:55],v[28:31]
buffer_load_dwordx4 v1,s[36:39],s61 offen offset:0 sc1 lds
v_mfma_f32_16x16x32_bf16 v[32:35],a[228:231],v[52:55],v[32:35]
ds_read_b128 v[48:51],v2 offset:46080
v_mfma_f32_16x16x32_bf16 v[36:39],a[232:235],v[52:55],v[36:39]
v_mfma_f32_16x16x32_bf16 v[40:43],a[236:239],v[52:55],v[40:43]
s_waitcnt lgkmcnt(2)
v_mfma_f32_16x16x32_bf16 v[28:31],a[240:243],v[56:59],v[28:31]
ds_read_b128 v[52:55],v2 offset:47104
v_mfma_f32_16x16x32_bf16 v[32:35],a[244:247],v[56:59],v[32:35]
v_mfma_f32_16x16x32_bf16 v[36:39],a[248:251],v[56:59],v[36:39]
buffer_load_dwordx4 v1,s[36:39],s61 offen offset:1024 sc1 lds
v_mfma_f32_16x16x32_bf16 v[40:43],a[252:255],v[56:59],v[40:43]
s_waitcnt lgkmcnt(2)
v_mfma_f32_16x16x32_bf16 v[28:31],v[100:103],v[44:47],v[28:31]
ds_read_b128 v[56:59],v2 offset:48128
v_mfma_f32_16x16x32_bf16 v[32:35],v[104:107],v[44:47],v[32:35]
ds_read_b128 v[88:91],v10 offset:0
v_mfma_f32_16x16x32_bf16 v[36:39],v[108:111],v[44:47],v[36:39]
v_mfma_f32_16x16x32_bf16 v[40:43],v[112:115],v[44:47],v[40:43]
s_waitcnt lgkmcnt(3)
v_mfma_f32_16x16x32_bf16 v[28:31],v[116:119],v[48:51],v[28:31]
buffer_load_dwordx4 v1,s[36:39],s61 offen offset:2048 sc1 lds
v_mfma_f32_16x16x32_bf16 v[32:35],v[120:123],v[48:51],v[32:35]
ds_read_b128 v[44:47],v2 offset:49152
ds_read_b128 v[76:79],v10 offset:1024
v_mfma_f32_16x16x32_bf16 v[36:39],v[124:127],v[48:51],v[36:39]
v_mfma_f32_16x16x32_bf16 v[40:43],v[128:131],v[48:51],v[40:43]
s_waitcnt lgkmcnt(4)
v_mfma_f32_16x16x32_bf16 v[28:31],v[132:135],v[52:55],v[28:31]
ds_read_b128 v[48:51],v2 offset:50176
v_mfma_f32_16x16x32_bf16 v[32:35],v[136:139],v[52:55],v[32:35]
ds_read_b128 v[80:83],v10 offset:2048
v_mfma_f32_16x16x32_bf16 v[36:39],v[140:143],v[52:55],v[36:39]
v_mfma_f32_16x16x32_bf16 v[40:43],v[144:147],v[52:55],v[40:43]
s_waitcnt lgkmcnt(4)
v_mfma_f32_16x16x32_bf16 v[28:31],v[148:151],v[56:59],v[28:31]
ds_read_b128 v[52:55],v2 offset:51200
v_mfma_f32_16x16x32_bf16 v[32:35],v[152:155],v[56:59],v[32:35]
ds_read_b128 v[84:87],v10 offset:3072
v_mfma_f32_16x16x32_bf16 v[36:39],v[156:159],v[56:59],v[36:39]
v_mfma_f32_16x16x32_bf16 v[40:43],v[88:91],v[56:59],v[40:43]
s_waitcnt lgkmcnt(4)
v_mfma_f32_16x16x32_bf16 v[28:31],v[160:163],v[44:47],v[28:31]
ds_read_b128 v[56:59],v2 offset:52224
v_mfma_f32_16x16x32_bf16 v[32:35],v[164:167],v[44:47],v[32:35]
ds_read_b128 v[88:91],v10 offset:4096
v_mfma_f32_16x16x32_bf16 v[36:39],v[168:171],v[44:47],v[36:39]
v_mfma_f32_16x16x32_bf16 v[40:43],v[76:79],v[44:47],v[40:43]
s_waitcnt lgkmcnt(4)
v_mfma_f32_16x16x32_bf16 v[28:31],v[172:175],v[48:51],v[28:31]
ds_read_b128 v[44:47],v2 offset:53248
v_mfma_f32_16x16x32_bf16 v[32:35],v[176:179],v[48:51],v[32:35]
ds_read_b128 v[76:79],v10 offset:5120
v_mfma_f32_16x16x32_bf16 v[36:39],v[180:183],v[48:51],v[36:39]
s_waitcnt vmcnt(8) lgkmcnt(4)
v_mfma_f32_16x16x32_bf16 v[40:43],v[80:83],v[48:51],v[40:43]
s_barrier
v_mfma_f32_16x16x32_bf16 v[28:31],v[184:187],v[52:55],v[28:31]
buffer_store_dword v6,v5,s[36:39],s70 offen offset:0 sc0 sc1
ds_read_b128 v[48:51],v2 offset:54272)"
R"(
v_mfma_f32_16x16x32_bf16 v[32:35],v[188:191],v[52:55],v[32:35]
ds_read_b128 v[80:83],v10 offset:6144
v_mfma_f32_16x16x32_bf16 v[36:39],v[192:195],v[52:55],v[36:39]
v_mfma_f32_16x16x32_bf16 v[40:43],v[84:87],v[52:55],v[40:43]
s_waitcnt lgkmcnt(4)
v_mfma_f32_16x16x32_bf16 v[28:31],v[196:199],v[56:59],v[28:31]
ds_read_b128 v[52:55],v2 offset:55296
v_mfma_f32_16x16x32_bf16 v[32:35],v[200:203],v[56:59],v[32:35]
ds_read_b128 v[84:87],v10 offset:7168
v_mfma_f32_16x16x32_bf16 v[36:39],v[204:207],v[56:59],v[36:39]
v_mfma_f32_16x16x32_bf16 v[40:43],v[88:91],v[56:59],v[40:43]
s_waitcnt lgkmcnt(4)
v_mfma_f32_16x16x32_bf16 v[28:31],v[208:211],v[44:47],v[28:31]
ds_read_b128 v[56:59],v2 offset:56320
v_mfma_f32_16x16x32_bf16 v[32:35],v[212:215],v[44:47],v[32:35]
ds_read_b128 v[88:91],v10 offset:8192
v_mfma_f32_16x16x32_bf16 v[36:39],v[216:219],v[44:47],v[36:39]
s_waitcnt vmcnt(4) lgkmcnt(4)
v_mfma_f32_16x16x32_bf16 v[40:43],v[76:79],v[44:47],v[40:43]
s_barrier
v_mfma_f32_16x16x32_bf16 v[28:31],v[220:223],v[48:51],v[28:31]
ds_read_b128 v[44:47],v2 offset:0
v_mfma_f32_16x16x32_bf16 v[32:35],v[224:227],v[48:51],v[32:35]
v_mfma_f32_16x16x32_bf16 v[36:39],v[228:231],v[48:51],v[36:39]
v_mfma_f32_16x16x32_bf16 v[40:43],v[80:83],v[48:51],v[40:43]
s_waitcnt lgkmcnt(3)
v_mfma_f32_16x16x32_bf16 v[28:31],v[232:235],v[52:55],v[28:31]
ds_read_b128 v[48:51],v2 offset:1024
v_mfma_f32_16x16x32_bf16 v[32:35],v[236:239],v[52:55],v[32:35]
v_mfma_f32_16x16x32_bf16 v[36:39],v[240:243],v[52:55],v[36:39]
v_mfma_f32_16x16x32_bf16 v[40:43],v[84:87],v[52:55],v[40:43]
s_waitcnt lgkmcnt(2)
v_mfma_f32_16x16x32_bf16 v[28:31],v[244:247],v[56:59],v[28:31]
ds_read_b128 v[52:55],v2 offset:2048
v_mfma_f32_16x16x32_bf16 v[32:35],v[248:251],v[56:59],v[32:35]
v_mfma_f32_16x16x32_bf16 v[36:39],v[252:255],v[56:59],v[36:39]
s_waitcnt lgkmcnt(2)
v_mfma_f32_16x16x32_bf16 v[40:43],v[88:91],v[56:59],v[40:43]
s_cbranch_scc1 Lg4_d0w3_%=
s_branch Lg4_slow0_%=
Lg4_slow0_%=:
ds_read_b32 v11,v8 offset:6144
s_waitcnt lgkmcnt(0)
v_cmp_eq_u32 vcc,s60,v11
s_nop 0
s_cmp_eq_u32 vcc_lo,-1
s_cbranch_scc1 Lg4_gs0_%=
Lg4_slowb0_%=:
s_cmp_eq_u32 s54,1
s_cselect_b32 s68,s49,s48
s_mul_i32 s68,s68,5
s_lshr_b32 s68,s68,6
s_add_u32 m0,s78,0
s_nop 0
buffer_load_dword v7,s[36:39],s68 offen offset:0 sc1 lds
s_mov_b32 s73,s47
v_lshlrev_b32 v60,16,v68
v_and_b32 v61,0xffff0000,v68
v_lshlrev_b32 v62,16,v69
v_and_b32 v63,0xffff0000,v69
v_lshlrev_b32 v64,16,v70
v_and_b32 v65,0xffff0000,v70
v_lshlrev_b32 v66,16,v71
v_and_b32 v67,0xffff0000,v71
v_cvt_pk_bf16_f32 v28,v28,v29
v_cvt_pk_bf16_f32 v29,v30,v31
v_cvt_pk_bf16_f32 v30,v32,v33
v_cvt_pk_bf16_f32 v31,v34,v35
s_add_u32 m0,s78,1024
s_nop 0
buffer_load_dword v7,s[36:39],s68 offen offset:0 sc1 lds
buffer_store_dwordx4 v[28:31],v3,s[36:39],s73 offen offset:0 sc0 sc1
v_add_f32 v36,v36,v60
v_add_f32 v37,v37,v61
v_add_f32 v38,v38,v62
v_add_f32 v39,v39,v63
v_add_f32 v40,v40,v64
v_add_f32 v41,v41,v65
v_add_f32 v42,v42,v66
v_add_f32 v43,v43,v67
v_cvt_pk_bf16_f32 v36,v36,v37
v_cvt_pk_bf16_f32 v37,v38,v39
v_cvt_pk_bf16_f32 v38,v40,v41
v_cvt_pk_bf16_f32 v39,v42,v43
buffer_store_dwordx4 v[36:39],v4,s[36:39],s73 offen offset:0 sc0 sc1
s_cmp_ge_u32 s48,s59
s_cbranch_scc1 Lg4_done0_%=
s_add_u32 m0,s78,2048
s_nop 0
buffer_load_dword v7,s[36:39],s68 offen offset:0 sc1 lds
s_waitcnt vmcnt(4)
s_barrier
ds_read_b32 v11,v8 offset:0
s_mov_b32 s69,2304
s_waitcnt lgkmcnt(0)
v_cmp_eq_u32 vcc,s60,v11
s_nop 0
s_cmp_eq_u32 vcc_lo,-1
s_cbranch_scc1 Lg4_go0_%=
s_add_u32 m0,s78,3072
s_nop 0
buffer_load_dword v7,s[36:39],s68 offen offset:0 sc1 lds
s_waitcnt vmcnt(4)
s_barrier
ds_read_b32 v11,v8 offset:1024
s_mov_b32 s69,3328
s_waitcnt lgkmcnt(0)
v_cmp_eq_u32 vcc,s60,v11
s_nop 0
s_cmp_eq_u32 vcc_lo,-1
s_cbranch_scc1 Lg4_go0_%=
s_add_u32 m0,s78,0
s_nop 0
buffer_load_dword v7,s[36:39],s68 offen offset:0 sc1 lds
s_waitcnt vmcnt(2)
s_barrier
ds_read_b32 v11,v8 offset:2048
s_lshr_b32 s70,s47,4
buffer_store_dword v6,v5,s[36:39],s70 offen offset:0 sc0 sc1
s_mov_b32 s47,s59
s_mov_b32 s69,256
s_waitcnt lgkmcnt(0)
v_cmp_eq_u32 vcc,s60,v11
s_nop 0
s_cmp_eq_u32 vcc_lo,-1
s_cbranch_scc1 Lg4_go0_%=
s_add_u32 m0,s78,1024
s_nop 0
buffer_load_dword v7,s[36:39],s68 offen offset:0 sc1 lds
s_waitcnt vmcnt(3)
s_barrier
ds_read_b32 v11,v8 offset:3072
s_mov_b32 s69,1280
s_waitcnt lgkmcnt(0)
v_cmp_eq_u32 vcc,s60,v11
s_nop 0
s_cmp_eq_u32 vcc_lo,-1
s_cbranch_scc1 Lg4_go0_%=
Lg4_poll0_%=:
s_add_u32 m0,s78,2048
s_nop 0
buffer_load_dword v7,s[36:39],s68 offen offset:0 sc1 lds
s_waitcnt vmcnt(2)
s_barrier
ds_read_b32 v11,v8 offset:0
s_mov_b32 s69,2304
s_waitcnt lgkmcnt(0)
v_cmp_eq_u32 vcc,s60,v11
s_nop 0
s_cmp_eq_u32 vcc_lo,-1
s_cbranch_scc1 Lg4_go0_%=
s_sleep 4
s_add_u32 m0,s78,3072
s_nop 0
buffer_load_dword v7,s[36:39],s68 offen offset:0 sc1 lds
s_waitcnt vmcnt(2)
s_barrier
ds_read_b32 v11,v8 offset:1024
s_mov_b32 s69,3328
s_waitcnt lgkmcnt(0)
v_cmp_eq_u32 vcc,s60,v11
s_nop 0
s_cmp_eq_u32 vcc_lo,-1
s_cbranch_scc1 Lg4_go0_%=
s_sleep 4
s_add_u32 m0,s78,0
s_nop 0
buffer_load_dword v7,s[36:39],s68 offen offset:0 sc1 lds
s_waitcnt vmcnt(2)
s_barrier
ds_read_b32 v11,v8 offset:2048
s_mov_b32 s69,256
s_waitcnt lgkmcnt(0)
v_cmp_eq_u32 vcc,s60,v11
s_nop 0
s_cmp_eq_u32 vcc_lo,-1
s_cbranch_scc1 Lg4_go0_%=
s_sleep 4
s_add_u32 m0,s78,1024
s_nop 0
buffer_load_dword v7,s[36:39],s68 offen offset:0 sc1 lds
s_waitcnt vmcnt(2)
s_barrier
ds_read_b32 v11,v8 offset:3072
s_mov_b32 s69,1280
s_waitcnt lgkmcnt(0)
v_cmp_eq_u32 vcc,s60,v11
s_nop 0
s_cmp_eq_u32 vcc_lo,-1
s_cbranch_scc1 Lg4_go0_%=
s_sleep 4
s_add_u32 s79,s79,1
s_cmp_lt_u32 s79,20000
s_cbranch_scc1 Lg4_poll0_%=
Lg4_go0_%=:
s_mul_i32 s61,s48,7
s_lshr_b32 s61,s61,1
s_add_u32 m0,s63,0
s_nop 0
buffer_load_dwordx4 v0,s[36:39],s61 offen offset:0 sc1 lds)"
R"(
buffer_load_dwordx4 v0,s[36:39],s61 offen offset:1024 sc1 lds
buffer_load_dwordx4 v0,s[36:39],s61 offen offset:2048 sc1 lds
buffer_load_dwordx4 v0,s[36:39],s61 offen offset:3072 sc1 lds
s_add_u32 m0,s64,0
s_nop 0
buffer_load_dwordx4 v1,s[36:39],s61 offen offset:0 sc1 lds
buffer_load_dwordx4 v1,s[36:39],s61 offen offset:1024 sc1 lds
buffer_load_dwordx4 v1,s[36:39],s61 offen offset:2048 sc1 lds
s_mul_i32 s68,s49,5
s_lshr_b32 s68,s68,6
s_add_u32 m0,s78,4096
s_nop 0
buffer_load_dword v7,s[36:39],s68 offen offset:0 sc1 lds
s_waitcnt vmcnt(4)
s_barrier
ds_read_b128 v[44:47],v2 offset:0
ds_read_b128 v[48:51],v2 offset:1024
ds_read_b128 v[52:55],v2 offset:2048
s_lshr_b32 s70,s47,4
buffer_store_dword v6,v5,s[36:39],s70 offen offset:0 sc0 sc1
s_mov_b32 s47,s59
s_waitcnt lgkmcnt(2)
s_setpc_b64 s[90:91]
Lg4_gs0_%=:
s_mul_i32 s61,s48,7
s_lshr_b32 s61,s61,1
s_cmp_eq_u32 s54,1
s_cselect_b32 s68,s49,s48
s_mul_i32 s68,s68,5
s_lshr_b32 s68,s68,6
s_mov_b32 s73,s47
s_add_u32 m0,s63,0
s_nop 0
buffer_load_dwordx4 v0,s[36:39],s61 offen offset:0 sc1 lds
v_lshlrev_b32 v60,16,v68
v_and_b32 v61,0xffff0000,v68
v_lshlrev_b32 v62,16,v69
v_and_b32 v63,0xffff0000,v69
buffer_load_dwordx4 v0,s[36:39],s61 offen offset:1024 sc1 lds
v_lshlrev_b32 v64,16,v70
v_and_b32 v65,0xffff0000,v70
v_lshlrev_b32 v66,16,v71
v_and_b32 v67,0xffff0000,v71
buffer_load_dwordx4 v0,s[36:39],s61 offen offset:2048 sc1 lds
v_cvt_pk_bf16_f32 v28,v28,v29
v_cvt_pk_bf16_f32 v29,v30,v31
v_cvt_pk_bf16_f32 v30,v32,v33
v_cvt_pk_bf16_f32 v31,v34,v35
buffer_load_dwordx4 v0,s[36:39],s61 offen offset:3072 sc1 lds
buffer_store_dwordx4 v[28:31],v3,s[36:39],s73 offen offset:0 sc0 sc1
v_add_f32 v36,v36,v60
v_add_f32 v37,v37,v61
v_add_f32 v38,v38,v62
s_add_u32 m0,s64,0
s_nop 0
buffer_load_dwordx4 v1,s[36:39],s61 offen offset:0 sc1 lds
v_add_f32 v39,v39,v63
v_add_f32 v40,v40,v64
v_add_f32 v41,v41,v65
v_add_f32 v42,v42,v66
buffer_load_dwordx4 v1,s[36:39],s61 offen offset:1024 sc1 lds
v_add_f32 v43,v43,v67
v_cvt_pk_bf16_f32 v36,v36,v37
v_cvt_pk_bf16_f32 v37,v38,v39
v_cvt_pk_bf16_f32 v38,v40,v41
buffer_load_dwordx4 v1,s[36:39],s61 offen offset:2048 sc1 lds
v_cvt_pk_bf16_f32 v39,v42,v43
buffer_store_dwordx4 v[36:39],v4,s[36:39],s73 offen offset:0 sc0 sc1
s_mul_i32 s68,s49,5
s_lshr_b32 s68,s68,6
s_add_u32 m0,s78,4096
s_nop 0
buffer_load_dword v7,s[36:39],s68 offen offset:0 sc1 lds
s_waitcnt vmcnt(6)
s_barrier
ds_read_b128 v[44:47],v2 offset:0
ds_read_b128 v[48:51],v2 offset:1024
ds_read_b128 v[52:55],v2 offset:2048
s_mov_b32 s65,s59
s_lshr_b32 s70,s59,4
buffer_store_dword v6,v5,s[36:39],s70 offen offset:0 sc0 sc1
s_waitcnt lgkmcnt(2)
s_setpc_b64 s[90:91]
Lg4_done0_%=:
s_waitcnt vmcnt(0)
s_barrier
s_lshr_b32 s70,s47,4
buffer_store_dword v6,v5,s[36:39],s70 offen offset:0 sc0 sc1
s_branch Lg4_done_%=
Lg4_slow1_%=:
ds_read_b32 v11,v8 offset:6144
s_waitcnt lgkmcnt(0)
v_cmp_eq_u32 vcc,s60,v11
s_nop 0
s_cmp_eq_u32 vcc_lo,-1
s_cbranch_scc1 Lg4_gs1_%=
Lg4_slowb1_%=:
s_cmp_eq_u32 s54,1
s_cselect_b32 s68,s50,s49
s_mul_i32 s68,s68,5
s_lshr_b32 s68,s68,6
s_add_u32 m0,s78,0
s_nop 0
buffer_load_dword v7,s[36:39],s68 offen offset:0 sc1 lds
s_mov_b32 s73,s48
v_lshlrev_b32 v60,16,v68
v_and_b32 v61,0xffff0000,v68
v_lshlrev_b32 v62,16,v69
v_and_b32 v63,0xffff0000,v69
v_lshlrev_b32 v64,16,v70
v_and_b32 v65,0xffff0000,v70
v_lshlrev_b32 v66,16,v71
v_and_b32 v67,0xffff0000,v71
v_cvt_pk_bf16_f32 v12,v12,v13
v_cvt_pk_bf16_f32 v13,v14,v15
v_cvt_pk_bf16_f32 v14,v16,v17
v_cvt_pk_bf16_f32 v15,v18,v19
s_add_u32 m0,s78,1024
s_nop 0
buffer_load_dword v7,s[36:39],s68 offen offset:0 sc1 lds
buffer_store_dwordx4 v[12:15],v3,s[36:39],s73 offen offset:0 sc0 sc1
v_add_f32 v20,v20,v60
v_add_f32 v21,v21,v61
v_add_f32 v22,v22,v62
v_add_f32 v23,v23,v63
v_add_f32 v24,v24,v64
v_add_f32 v25,v25,v65
v_add_f32 v26,v26,v66
v_add_f32 v27,v27,v67
v_cvt_pk_bf16_f32 v20,v20,v21
v_cvt_pk_bf16_f32 v21,v22,v23
v_cvt_pk_bf16_f32 v22,v24,v25
v_cvt_pk_bf16_f32 v23,v26,v27
buffer_store_dwordx4 v[20:23],v4,s[36:39],s73 offen offset:0 sc0 sc1
s_cmp_ge_u32 s49,s59
s_cbranch_scc1 Lg4_done1_%=
s_add_u32 m0,s78,2048
s_nop 0
buffer_load_dword v7,s[36:39],s68 offen offset:0 sc1 lds
s_waitcnt vmcnt(4)
s_barrier
ds_read_b32 v11,v8 offset:0
s_mov_b32 s69,2304
s_waitcnt lgkmcnt(0)
v_cmp_eq_u32 vcc,s60,v11
s_nop 0
s_cmp_eq_u32 vcc_lo,-1
s_cbranch_scc1 Lg4_go1_%=
s_add_u32 m0,s78,3072
s_nop 0
buffer_load_dword v7,s[36:39],s68 offen offset:0 sc1 lds
s_waitcnt vmcnt(4)
s_barrier
ds_read_b32 v11,v8 offset:1024
s_mov_b32 s69,3328
s_waitcnt lgkmcnt(0)
v_cmp_eq_u32 vcc,s60,v11
s_nop 0
s_cmp_eq_u32 vcc_lo,-1
s_cbranch_scc1 Lg4_go1_%=
s_add_u32 m0,s78,0
s_nop 0
buffer_load_dword v7,s[36:39],s68 offen offset:0 sc1 lds
s_waitcnt vmcnt(2)
s_barrier
ds_read_b32 v11,v8 offset:2048
s_lshr_b32 s70,s48,4
buffer_store_dword v6,v5,s[36:39],s70 offen offset:0 sc0 sc1
s_mov_b32 s48,s59
s_mov_b32 s69,256
s_waitcnt lgkmcnt(0)
v_cmp_eq_u32 vcc,s60,v11
s_nop 0
s_cmp_eq_u32 vcc_lo,-1
s_cbranch_scc1 Lg4_go1_%=
s_add_u32 m0,s78,1024
s_nop 0
buffer_load_dword v7,s[36:39],s68 offen offset:0 sc1 lds
s_waitcnt vmcnt(3)
s_barrier
ds_read_b32 v11,v8 offset:3072
s_mov_b32 s69,1280
s_waitcnt lgkmcnt(0)
v_cmp_eq_u32 vcc,s60,v11
s_nop 0
s_cmp_eq_u32 vcc_lo,-1
s_cbranch_scc1 Lg4_go1_%=
Lg4_poll1_%=:
s_add_u32 m0,s78,2048
s_nop 0
buffer_load_dword v7,s[36:39],s68 offen offset:0 sc1 lds
s_waitcnt vmcnt(2)
s_barrier
ds_read_b32 v11,v8 offset:0
s_mov_b32 s69,2304
s_waitcnt lgkmcnt(0)
v_cmp_eq_u32 vcc,s60,v11
s_nop 0
s_cmp_eq_u32 vcc_lo,-1
s_cbranch_scc1 Lg4_go1_%=)"
R"(
s_sleep 4
s_add_u32 m0,s78,3072
s_nop 0
buffer_load_dword v7,s[36:39],s68 offen offset:0 sc1 lds
s_waitcnt vmcnt(2)
s_barrier
ds_read_b32 v11,v8 offset:1024
s_mov_b32 s69,3328
s_waitcnt lgkmcnt(0)
v_cmp_eq_u32 vcc,s60,v11
s_nop 0
s_cmp_eq_u32 vcc_lo,-1
s_cbranch_scc1 Lg4_go1_%=
s_sleep 4
s_add_u32 m0,s78,0
s_nop 0
buffer_load_dword v7,s[36:39],s68 offen offset:0 sc1 lds
s_waitcnt vmcnt(2)
s_barrier
ds_read_b32 v11,v8 offset:2048
s_mov_b32 s69,256
s_waitcnt lgkmcnt(0)
v_cmp_eq_u32 vcc,s60,v11
s_nop 0
s_cmp_eq_u32 vcc_lo,-1
s_cbranch_scc1 Lg4_go1_%=
s_sleep 4
s_add_u32 m0,s78,1024
s_nop 0
buffer_load_dword v7,s[36:39],s68 offen offset:0 sc1 lds
s_waitcnt vmcnt(2)
s_barrier
ds_read_b32 v11,v8 offset:3072
s_mov_b32 s69,1280
s_waitcnt lgkmcnt(0)
v_cmp_eq_u32 vcc,s60,v11
s_nop 0
s_cmp_eq_u32 vcc_lo,-1
s_cbranch_scc1 Lg4_go1_%=
s_sleep 4
s_add_u32 s79,s79,1
s_cmp_lt_u32 s79,20000
s_cbranch_scc1 Lg4_poll1_%=
Lg4_go1_%=:
s_mul_i32 s61,s49,7
s_lshr_b32 s61,s61,1
s_add_u32 m0,s63,28672
s_nop 0
buffer_load_dwordx4 v0,s[36:39],s61 offen offset:0 sc1 lds
buffer_load_dwordx4 v0,s[36:39],s61 offen offset:1024 sc1 lds
buffer_load_dwordx4 v0,s[36:39],s61 offen offset:2048 sc1 lds
buffer_load_dwordx4 v0,s[36:39],s61 offen offset:3072 sc1 lds
s_add_u32 m0,s64,28672
s_nop 0
buffer_load_dwordx4 v1,s[36:39],s61 offen offset:0 sc1 lds
buffer_load_dwordx4 v1,s[36:39],s61 offen offset:1024 sc1 lds
buffer_load_dwordx4 v1,s[36:39],s61 offen offset:2048 sc1 lds
s_mul_i32 s68,s50,5
s_lshr_b32 s68,s68,6
s_add_u32 m0,s78,5120
s_nop 0
buffer_load_dword v7,s[36:39],s68 offen offset:0 sc1 lds
s_waitcnt vmcnt(4)
s_barrier
ds_read_b128 v[44:47],v2 offset:28672
ds_read_b128 v[48:51],v2 offset:29696
ds_read_b128 v[52:55],v2 offset:30720
s_lshr_b32 s70,s48,4
buffer_store_dword v6,v5,s[36:39],s70 offen offset:0 sc0 sc1
s_mov_b32 s48,s59
s_waitcnt lgkmcnt(2)
s_setpc_b64 s[92:93]
Lg4_gs1_%=:
s_mul_i32 s61,s49,7
s_lshr_b32 s61,s61,1
s_cmp_eq_u32 s54,1
s_cselect_b32 s68,s50,s49
s_mul_i32 s68,s68,5
s_lshr_b32 s68,s68,6
s_mov_b32 s73,s48
s_add_u32 m0,s63,28672
s_nop 0
buffer_load_dwordx4 v0,s[36:39],s61 offen offset:0 sc1 lds
v_lshlrev_b32 v60,16,v68
v_and_b32 v61,0xffff0000,v68
v_lshlrev_b32 v62,16,v69
v_and_b32 v63,0xffff0000,v69
buffer_load_dwordx4 v0,s[36:39],s61 offen offset:1024 sc1 lds
v_lshlrev_b32 v64,16,v70
v_and_b32 v65,0xffff0000,v70
v_lshlrev_b32 v66,16,v71
v_and_b32 v67,0xffff0000,v71
buffer_load_dwordx4 v0,s[36:39],s61 offen offset:2048 sc1 lds
v_cvt_pk_bf16_f32 v12,v12,v13
v_cvt_pk_bf16_f32 v13,v14,v15
v_cvt_pk_bf16_f32 v14,v16,v17
v_cvt_pk_bf16_f32 v15,v18,v19
buffer_load_dwordx4 v0,s[36:39],s61 offen offset:3072 sc1 lds
buffer_store_dwordx4 v[12:15],v3,s[36:39],s73 offen offset:0 sc0 sc1
v_add_f32 v20,v20,v60
v_add_f32 v21,v21,v61
v_add_f32 v22,v22,v62
s_add_u32 m0,s64,28672
s_nop 0
buffer_load_dwordx4 v1,s[36:39],s61 offen offset:0 sc1 lds
v_add_f32 v23,v23,v63
v_add_f32 v24,v24,v64
v_add_f32 v25,v25,v65
v_add_f32 v26,v26,v66
buffer_load_dwordx4 v1,s[36:39],s61 offen offset:1024 sc1 lds
v_add_f32 v27,v27,v67
v_cvt_pk_bf16_f32 v20,v20,v21
v_cvt_pk_bf16_f32 v21,v22,v23
v_cvt_pk_bf16_f32 v22,v24,v25
buffer_load_dwordx4 v1,s[36:39],s61 offen offset:2048 sc1 lds
v_cvt_pk_bf16_f32 v23,v26,v27
buffer_store_dwordx4 v[20:23],v4,s[36:39],s73 offen offset:0 sc0 sc1
s_mul_i32 s68,s50,5
s_lshr_b32 s68,s68,6
s_add_u32 m0,s78,5120
s_nop 0
buffer_load_dword v7,s[36:39],s68 offen offset:0 sc1 lds
s_waitcnt vmcnt(6)
s_barrier
ds_read_b128 v[44:47],v2 offset:28672
ds_read_b128 v[48:51],v2 offset:29696
ds_read_b128 v[52:55],v2 offset:30720
s_mov_b32 s65,s59
s_lshr_b32 s70,s59,4
buffer_store_dword v6,v5,s[36:39],s70 offen offset:0 sc0 sc1
s_waitcnt lgkmcnt(2)
s_setpc_b64 s[92:93]
Lg4_done1_%=:
s_waitcnt vmcnt(0)
s_barrier
s_lshr_b32 s70,s48,4
buffer_store_dword v6,v5,s[36:39],s70 offen offset:0 sc0 sc1
s_branch Lg4_done_%=
Lg4_done_%=:
s_waitcnt vmcnt(0) lgkmcnt(0)
s_barrier
s_endpgm
Lcode_g4_e:)"
  :: "s"(ws), "s"(wave), "s"(sStep), "s"(sX1), "s"(sX2), "s"(sU0), "s"(sWw), "s"(sBuf), "v"(vSrc), "v"(vSrc2), "v"(vA), "v"(vOut), "v"(vOut2), "v"(vFlag), "v"(vPollOff), "v"(vSnapRd), "v"(vAuxS), "v"(vWd0), "v"(vWd1), "v"(vWr0), "v"(vWr1), "v"(vWr2), "s"(w4) : "v0", "v1", "v2", "v3", "v4", "v5", "v6", "v7", "v8", "v9", "v10", "v11", "v12", "v13", "v14", "v15", "v16", "v17", "v18", "v19", "v20", "v21", "v22", "v23", "v24", "v25", "v26", "v27", "v28", "v29", "v30", "v31", "v32", "v33", "v34", "v35", "v36", "v37", "v38", "v39", "v40", "v41", "v42", "v43", "v44", "v45", "v46", "v47", "v48", "v49", "v50", "v51", "v52", "v53", "v54", "v55", "v56", "v57", "v58", "v59", "v60", "v61", "v62", "v63", "v64", "v65", "v66", "v67", "v68", "v69", "v70", "v71", "v72", "v73", "v74", "v75", "v76", "v77", "v78", "v79", "v80", "v81", "v82", "v83", "v84", "v85", "v86", "v87", "v88", "v89", "v90", "v91", "v92", "v93", "v94", "v95", "v96", "v97", "v98", "v99", "v100", "v101", "v102", "v103", "v104", "v105", "v106", "v107", "v108", "v109", "v110", "v111", "v112", "v113", "v114", "v115", "v116", "v117", "v118", "v119", "v120", "v121", "v122", "v123", "v124", "v125", "v126", "v127", "v128", "v129", "v130", "v131", "v132", "v133", "v134", "v135", "v136", "v137", "v138", "v139", "v140", "v141", "v142", "v143", "v144", "v145", "v146", "v147", "v148", "v149", "v150", "v151", "v152", "v153", "v154", "v155", "v156", "v157", "v158", "v159", "v160", "v161", "v162", "v163", "v164", "v165", "v166", "v167", "v168", "v169", "v170", "v171", "v172", "v173", "v174", "v175", "v176", "v177", "v178", "v179", "v180", "v181", "v182", "v183", "v184", "v185", "v186", "v187", "v188", "v189", "v190", "v191", "v192", "v193", "v194", "v195", "v196", "v197", "v198", "v199", "v200", "v201", "v202", "v203", "v204", "v205", "v206", "v207", "v208", "v209", "v210", "v211", "v212", "v213", "v214", "v215", "v216", "v217", "v218", "v219", "v220", "v221", "v222", "v223", "v224", "v225", "v226", "v227", "v228", "v229", "v230", "v231", "v255", "a0", "a1", "a2", "a3", "a4", "a5", "a6", "a7", "a8", "a9", "a10", "a11", "a12", "a13", "a14", "a15", "a16", "a17", "a18", "a19", "a20", "a21", "a22", "a23", "a24", "a25", "a26", "a27", "a28", "a29", "a30", "a31", "a32", "a33", "a34", "a35", "a36", "a37", "a38", "a39", "a40", "a41", "a42", "a43", "a44", "a45", "a46", "a47", "a48", "a49", "a50", "a51", "a52", "a53", "a54", "a55", "a56", "a57", "a58", "a59", "a60", "a61", "a62", "a63", "a64", "a65", "a66", "a67", "a68", "a69", "a70", "a71", "a72", "a73", "a74", "a75", "a76", "a77", "a78", "a79", "a80", "a81", "a82", "a83", "a84", "a85", "a86", "a87", "a88", "a89", "a90", "a91", "a92", "a93", "a94", "a95", "a96", "a97", "a98", "a99", "a100", "a101", "a102", "a103", "a104", "a105", "a106", "a107", "a108", "a109", "a110", "a111", "a112", "a113", "a114", "a115", "a116", "a117", "a118", "a119", "a120", "a121", "a122", "a123", "a124", "a125", "a126", "a127", "a128", "a129", "a130", "a131", "a132", "a133", "a134", "a135", "a136", "a137", "a138", "a139", "a140", "a141", "a142", "a143", "a144", "a145", "a146", "a147", "a148", "a149", "a150", "a151", "a152", "a153", "a154", "a155", "a156", "a157", "a158", "a159", "a160", "a161", "a162", "a163", "a164", "a165", "a166", "a167", "a168", "a169", "a170", "a171", "a172", "a173", "a174", "a175", "a176", "a177", "a178", "a179", "a180", "a181", "a182", "a183", "a184", "a185", "a186", "a187", "a188", "a189", "a190", "a191", "a192", "a193", "a194", "a195", "a196", "a197", "a198", "a199", "a200", "a201", "a202", "a203", "a204", "a205", "a206", "a207", "a208", "a209", "a210", "a211", "a212", "a213", "a214", "a215", "a216", "a217", "a218", "a219", "a220", "a221", "a222", "a223", "a224", "a225", "a226", "a227", "a228", "a229", "a230", "a231", "a232", "a233", "a234", "a235", "a236", "a237", "a238", "a239", "a240", "a241", "a242", "a243", "a244", "a245", "a246", "a247", "a248", "a249", "a250", "a251", "a252", "a253", "a254", "a255", "s36", "s37", "s38", "s39", "s40", "s41", "s42", "s43", "s44", "s45", "s46", "s47", "s48", "s49", "s50", "s51", "s52", "s53", "s54", "s55", "s56", "s57", "s58", "s59", "s60", "s61", "s62", "s63", "s64", "s65", "s66", "s67", "s68", "s69", "s70", "s71", "s72", "s73", "s74", "s75", "s76", "s77", "s78", "s79", "s80", "s81", "s82", "s83", "s84", "s85", "s86", "s87", "s88", "s89", "s90", "s91", "s92", "s93", "s94", "s95", "s96", "s97", "s98", "s99", "vcc", "scc", "memory");
    __builtin_unreachable();
  } else {
   if (kind != 6) {
    KARG(ws, 10) KARG(w12, 5 + kind)
    COMMON(kind == 1 ? 8 : 16, kind == 1 ? OFF_FXP : OFF_FH, (kind == 1 ? OFF_FH : OFF_FX1) + slice * 128, AUX + 1536, 2048, 2048, 4);
    const int dl = kind == 2 ? ((wave & 2) ? -1 : 1) : 0, r0 = 16 + l15 - dl, r1 = 16 + l15 - 2 * dl;
    const unsigned sb = (kind == 1 ? OFF_XP : OFF_HS) - 32768 + wave * 4096 + lg * 256;
    const unsigned vSrc = sb + (r0 >> 4) * 32768 + (r0 & 15) * 16, vSrc2 = sb + 16384 + (r1 >> 4) * 32768 + (r1 & 15) * 16;
    const unsigned vOut = (kind == 1 ? OFF_HS : OFF_X1) + (slice * 4 + wave) * 1024 + vA;
    const unsigned vX = AUX + wave * 2048 + (kind == 1 ? l15 * 16 : vA), vX2 = AUX + wave * 2048 + l15 * 8;
    const unsigned vAuxS = kind == 1 ? OFF_S1 + ln * 4 : OFF_XP + (slice * 4 + wave) * 1024 + vA;
    const unsigned vAuxS2 = ln < 32 ? OFF_S1 - 1024 + 256 + ln * 4 : OOB;
    constexpr unsigned RED = AUX + 24576 + 1536;
    const unsigned vOut2 = wave != 2 ? OOB : kind == 1 ? (slice == 0 && ln < 16 ? OFF_S1 + 256 + ln * 8 : OOB) : ln < 32 ? OFF_ST + slice * 128 + ln * 4 : OOB;
    const unsigned vStW = RED + wave * 4 + (kind == 1 ? l15 * 32 + (lg == lq ? 16 : 0) : (l15 * 2 + (lg & 1)) * 16), vStR = RED + (kind == 1 ? l15 * 32 : (ln & 31) * 16);
    const unsigned sWw = 16 * wave * 2048 + slice * 256;
    unsigned epOut;
    asm volatile(R"(
Lcode_g12_b:
s_cmp_eq_u32 %8,2
s_cbranch_scc1 Lg2_begin_%=
s_mov_b64 s[36:37],%1
s_mov_b32 s38,122727552
s_mov_b32 s39,0x00020000
s_mov_b32 s54,%2
s_mov_b32 s55,%3
s_mov_b32 s56,%4
s_mov_b32 s58,%5
s_mov_b32 s83,%6
s_mov_b32 s82,%7
s_mov_b32 s84,%8
v_mov_b32 v0,%9
v_mov_b32 v1,%10
v_mov_b32 v2,%11
v_mov_b32 v3,%12
v_mov_b32 v4,%13
v_mov_b32 v6,%14
v_mov_b32 v7,%15
v_mov_b32 v8,%16
v_mov_b32 v9,%17
v_mov_b32 v10,%18
v_mov_b32 v11,%19
v_mov_b32 v12,%20
v_mov_b32 v13,%21
v_mov_b32 v14,%22
v_mov_b32 v28,%23
v_mov_b32 v29,%24
v_mov_b32 v30,%25
v_mov_b32 v31,%26
v_mov_b32 v32,%27
s_mov_b64 s[40:41],%28
s_mov_b32 s42,2097152
s_mov_b32 s43,0x00020000
s_lshl_b32 s88,s54,11
s_mov_b32 s69,122727424
buffer_load_dword v5,off,s[36:39],s69 sc1
s_add_u32 m0,s88,0
s_add_u32 s61,s82,0
buffer_load_dwordx4 v28,s[40:43],s61 offen offset:0  lds
s_add_u32 m0,s88,1024
s_nop 0
buffer_load_dwordx4 v29,s[40:43],s61 offen offset:0  lds
s_add_u32 m0,s88,8064
s_nop 0
buffer_load_dwordx4 v28,s[40:43],s61 offen offset:128  lds
s_add_u32 m0,s88,9088
s_nop 0
buffer_load_dwordx4 v29,s[40:43],s61 offen offset:128  lds
s_add_u32 m0,s88,16384
s_add_u32 s61,s82,131072
buffer_load_dwordx4 v28,s[40:43],s61 offen offset:0  lds
s_add_u32 m0,s88,17408
s_nop 0
buffer_load_dwordx4 v29,s[40:43],s61 offen offset:0  lds
s_add_u32 m0,s88,24448
s_nop 0
buffer_load_dwordx4 v28,s[40:43],s61 offen offset:128  lds
s_add_u32 m0,s88,25472
s_nop 0
buffer_load_dwordx4 v29,s[40:43],s61 offen offset:128  lds
s_add_u32 m0,s88,32768
s_add_u32 s61,s82,262144
buffer_load_dwordx4 v28,s[40:43],s61 offen offset:0  lds
s_add_u32 m0,s88,33792
s_nop 0
buffer_load_dwordx4 v29,s[40:43],s61 offen offset:0  lds
s_add_u32 m0,s88,40832
s_nop 0
buffer_load_dwordx4 v28,s[40:43],s61 offen offset:128  lds
s_add_u32 m0,s88,41856
s_nop 0
buffer_load_dwordx4 v29,s[40:43],s61 offen offset:128  lds
s_add_u32 m0,s88,49152
s_add_u32 s61,s82,393216
buffer_load_dwordx4 v28,s[40:43],s61 offen offset:0  lds
s_add_u32 m0,s88,50176
s_nop 0
buffer_load_dwordx4 v29,s[40:43],s61 offen offset:0  lds
s_add_u32 m0,s88,57216
s_nop 0
buffer_load_dwordx4 v28,s[40:43],s61 offen offset:128  lds
s_add_u32 m0,s88,58240
s_nop 0
buffer_load_dwordx4 v29,s[40:43],s61 offen offset:128  lds
s_add_u32 m0,s88,65536
s_add_u32 s61,s82,524288
buffer_load_dwordx4 v28,s[40:43],s61 offen offset:0  lds
s_add_u32 m0,s88,66560
s_nop 0
buffer_load_dwordx4 v29,s[40:43],s61 offen offset:0  lds
s_add_u32 m0,s88,73600
s_nop 0
buffer_load_dwordx4 v28,s[40:43],s61 offen offset:128  lds
s_add_u32 m0,s88,74624
s_nop 0
buffer_load_dwordx4 v29,s[40:43],s61 offen offset:128  lds
s_add_u32 m0,s88,81920
s_add_u32 s61,s82,655360
buffer_load_dwordx4 v28,s[40:43],s61 offen offset:0  lds
s_add_u32 m0,s88,82944
s_nop 0
buffer_load_dwordx4 v29,s[40:43],s61 offen offset:0  lds
s_add_u32 m0,s88,89984
s_nop 0
buffer_load_dwordx4 v28,s[40:43],s61 offen offset:128  lds
s_add_u32 m0,s88,91008
s_nop 0
buffer_load_dwordx4 v29,s[40:43],s61 offen offset:128  lds
s_add_u32 m0,s88,98304
s_add_u32 s61,s82,786432
buffer_load_dwordx4 v28,s[40:43],s61 offen offset:0  lds
s_add_u32 m0,s88,99328
s_nop 0
buffer_load_dwordx4 v29,s[40:43],s61 offen offset:0  lds
s_add_u32 m0,s88,106368
s_nop 0
buffer_load_dwordx4 v28,s[40:43],s61 offen offset:128  lds
s_add_u32 m0,s88,107392
s_nop 0
buffer_load_dwordx4 v29,s[40:43],s61 offen offset:128  lds
v_add_u32 v36,49152,v30
v_add_u32 v37,49152,v31
v_add_u32 v38,98304,v30
v_add_u32 v39,98304,v31
v_mov_b32 v76,0x3f803f80
v_mov_b32 v77,0x3f803f80
v_mov_b32 v78,0x3f803f80
v_mov_b32 v79,0x3f803f80
s_waitcnt vmcnt(24)
s_barrier
s_add_u32 m0,s88,114688
s_add_u32 s61,s82,917504
buffer_load_dwordx4 v28,s[40:43],s61 offen offset:0  lds
s_add_u32 m0,s88,115712
ds_read_b64_tr_b16 a[0:1],v30 offset:0
buffer_load_dwordx4 v29,s[40:43],s61 offen offset:0  lds
s_add_u32 m0,s88,122752
ds_read_b64_tr_b16 a[2:3],v30 offset:512
buffer_load_dwordx4 v28,s[40:43],s61 offen offset:128  lds
s_add_u32 m0,s88,123776
ds_read_b64_tr_b16 a[4:5],v31 offset:0
buffer_load_dwordx4 v29,s[40:43],s61 offen offset:128  lds
ds_read_b64_tr_b16 a[6:7],v31 offset:512
ds_read_b64_tr_b16 a[8:9],v30 offset:4096
ds_read_b64_tr_b16 a[10:11],v30 offset:4608
ds_read_b64_tr_b16 a[12:13],v31 offset:4096
ds_read_b64_tr_b16 a[14:15],v31 offset:4608
s_waitcnt vmcnt(24)
s_barrier
s_add_u32 m0,s88,131072
s_add_u32 s61,s82,1048576
buffer_load_dwordx4 v28,s[40:43],s61 offen offset:0  lds
s_add_u32 m0,s88,132096
ds_read_b64_tr_b16 a[16:17],v30 offset:16384
buffer_load_dwordx4 v29,s[40:43],s61 offen offset:0  lds
s_add_u32 m0,s88,139136
ds_read_b64_tr_b16 a[18:19],v30 offset:16896
buffer_load_dwordx4 v28,s[40:43],s61 offen offset:128  lds
s_add_u32 m0,s88,140160
ds_read_b64_tr_b16 a[20:21],v31 offset:16384
buffer_load_dwordx4 v29,s[40:43],s61 offen offset:128  lds
ds_read_b64_tr_b16 a[22:23],v31 offset:16896
ds_read_b64_tr_b16 a[24:25],v30 offset:20480
ds_read_b64_tr_b16 a[26:27],v30 offset:20992
ds_read_b64_tr_b16 a[28:29],v31 offset:20480
ds_read_b64_tr_b16 a[30:31],v31 offset:20992
s_waitcnt lgkmcnt(8)
v_mfma_f32_16x16x32_bf16 v[20:23],a[0:3],v[76:79],0
v_mfma_f32_16x16x32_bf16 v[24:27],a[4:7],v[76:79],0
v_mfma_f32_16x16x32_bf16 v[20:23],a[8:11],v[76:79],v[20:23]
v_mfma_f32_16x16x32_bf16 v[24:27],a[12:15],v[76:79],v[24:27]
s_waitcnt vmcnt(24)
s_barrier
s_add_u32 m0,s88,0
s_add_u32 s61,s82,1179648
buffer_load_dwordx4 v28,s[40:43],s61 offen offset:0  lds
s_add_u32 m0,s88,1024
ds_read_b64_tr_b16 a[32:33],v30 offset:32768
buffer_load_dwordx4 v29,s[40:43],s61 offen offset:0  lds
s_add_u32 m0,s88,8064
ds_read_b64_tr_b16 a[34:35],v30 offset:33280
buffer_load_dwordx4 v28,s[40:43],s61 offen offset:128  lds
s_add_u32 m0,s88,9088
ds_read_b64_tr_b16 a[36:37],v31 offset:32768
buffer_load_dwordx4 v29,s[40:43],s61 offen offset:128  lds
ds_read_b64_tr_b16 a[38:39],v31 offset:33280
ds_read_b64_tr_b16 a[40:41],v30 offset:36864
ds_read_b64_tr_b16 a[42:43],v30 offset:37376
ds_read_b64_tr_b16 a[44:45],v31 offset:36864
ds_read_b64_tr_b16 a[46:47],v31 offset:37376
s_waitcnt lgkmcnt(8)
v_mfma_f32_16x16x32_bf16 v[20:23],a[16:19],v[76:79],v[20:23]
v_mfma_f32_16x16x32_bf16 v[24:27],a[20:23],v[76:79],v[24:27]
v_mfma_f32_16x16x32_bf16 v[20:23],a[24:27],v[76:79],v[20:23]
v_mfma_f32_16x16x32_bf16 v[24:27],a[28:31],v[76:79],v[24:27]
s_waitcnt vmcnt(24)
s_barrier
s_add_u32 m0,s88,16384)"
R"(
s_add_u32 s61,s82,1310720
buffer_load_dwordx4 v28,s[40:43],s61 offen offset:0  lds
s_add_u32 m0,s88,17408
ds_read_b64_tr_b16 a[48:49],v36 offset:0
buffer_load_dwordx4 v29,s[40:43],s61 offen offset:0  lds
s_add_u32 m0,s88,24448
ds_read_b64_tr_b16 a[50:51],v36 offset:512
buffer_load_dwordx4 v28,s[40:43],s61 offen offset:128  lds
s_add_u32 m0,s88,25472
ds_read_b64_tr_b16 a[52:53],v37 offset:0
buffer_load_dwordx4 v29,s[40:43],s61 offen offset:128  lds
ds_read_b64_tr_b16 a[54:55],v37 offset:512
ds_read_b64_tr_b16 a[56:57],v36 offset:4096
ds_read_b64_tr_b16 a[58:59],v36 offset:4608
ds_read_b64_tr_b16 a[60:61],v37 offset:4096
ds_read_b64_tr_b16 a[62:63],v37 offset:4608
s_waitcnt lgkmcnt(8)
v_mfma_f32_16x16x32_bf16 v[20:23],a[32:35],v[76:79],v[20:23]
v_mfma_f32_16x16x32_bf16 v[24:27],a[36:39],v[76:79],v[24:27]
v_mfma_f32_16x16x32_bf16 v[20:23],a[40:43],v[76:79],v[20:23]
v_mfma_f32_16x16x32_bf16 v[24:27],a[44:47],v[76:79],v[24:27]
s_waitcnt vmcnt(24)
s_barrier
s_add_u32 m0,s88,32768
s_add_u32 s61,s82,1441792
buffer_load_dwordx4 v28,s[40:43],s61 offen offset:0  lds
s_add_u32 m0,s88,33792
ds_read_b64_tr_b16 a[64:65],v36 offset:16384
buffer_load_dwordx4 v29,s[40:43],s61 offen offset:0  lds
s_add_u32 m0,s88,40832
ds_read_b64_tr_b16 a[66:67],v36 offset:16896
buffer_load_dwordx4 v28,s[40:43],s61 offen offset:128  lds
s_add_u32 m0,s88,41856
ds_read_b64_tr_b16 a[68:69],v37 offset:16384
buffer_load_dwordx4 v29,s[40:43],s61 offen offset:128  lds
ds_read_b64_tr_b16 a[70:71],v37 offset:16896
ds_read_b64_tr_b16 a[72:73],v36 offset:20480
ds_read_b64_tr_b16 a[74:75],v36 offset:20992
ds_read_b64_tr_b16 a[76:77],v37 offset:20480
ds_read_b64_tr_b16 a[78:79],v37 offset:20992
s_waitcnt lgkmcnt(8)
v_mfma_f32_16x16x32_bf16 v[20:23],a[48:51],v[76:79],v[20:23]
v_mfma_f32_16x16x32_bf16 v[24:27],a[52:55],v[76:79],v[24:27]
v_mfma_f32_16x16x32_bf16 v[20:23],a[56:59],v[76:79],v[20:23]
v_mfma_f32_16x16x32_bf16 v[24:27],a[60:63],v[76:79],v[24:27]
s_waitcnt vmcnt(24)
s_barrier
s_add_u32 m0,s88,49152
s_add_u32 s61,s82,1572864
buffer_load_dwordx4 v28,s[40:43],s61 offen offset:0  lds
s_add_u32 m0,s88,50176
ds_read_b64_tr_b16 a[80:81],v36 offset:32768
buffer_load_dwordx4 v29,s[40:43],s61 offen offset:0  lds
s_add_u32 m0,s88,57216
ds_read_b64_tr_b16 a[82:83],v36 offset:33280
buffer_load_dwordx4 v28,s[40:43],s61 offen offset:128  lds
s_add_u32 m0,s88,58240
ds_read_b64_tr_b16 a[84:85],v37 offset:32768
buffer_load_dwordx4 v29,s[40:43],s61 offen offset:128  lds
ds_read_b64_tr_b16 a[86:87],v37 offset:33280
ds_read_b64_tr_b16 a[88:89],v36 offset:36864
ds_read_b64_tr_b16 a[90:91],v36 offset:37376
ds_read_b64_tr_b16 a[92:93],v37 offset:36864
ds_read_b64_tr_b16 a[94:95],v37 offset:37376
s_waitcnt lgkmcnt(8)
v_mfma_f32_16x16x32_bf16 v[20:23],a[64:67],v[76:79],v[20:23]
v_mfma_f32_16x16x32_bf16 v[24:27],a[68:71],v[76:79],v[24:27]
v_mfma_f32_16x16x32_bf16 v[20:23],a[72:75],v[76:79],v[20:23]
v_mfma_f32_16x16x32_bf16 v[24:27],a[76:79],v[76:79],v[24:27]
s_waitcnt vmcnt(24)
s_barrier
s_add_u32 m0,s88,65536
s_add_u32 s61,s82,1703936
buffer_load_dwordx4 v28,s[40:43],s61 offen offset:0  lds
s_add_u32 m0,s88,66560
ds_read_b64_tr_b16 a[96:97],v38 offset:0
buffer_load_dwordx4 v29,s[40:43],s61 offen offset:0  lds
s_add_u32 m0,s88,73600
ds_read_b64_tr_b16 a[98:99],v38 offset:512
buffer_load_dwordx4 v28,s[40:43],s61 offen offset:128  lds
s_add_u32 m0,s88,74624
ds_read_b64_tr_b16 a[100:101],v39 offset:0
buffer_load_dwordx4 v29,s[40:43],s61 offen offset:128  lds
ds_read_b64_tr_b16 a[102:103],v39 offset:512
ds_read_b64_tr_b16 a[104:105],v38 offset:4096
ds_read_b64_tr_b16 a[106:107],v38 offset:4608
ds_read_b64_tr_b16 a[108:109],v39 offset:4096
ds_read_b64_tr_b16 a[110:111],v39 offset:4608
s_waitcnt lgkmcnt(8)
v_mfma_f32_16x16x32_bf16 v[20:23],a[80:83],v[76:79],v[20:23]
v_mfma_f32_16x16x32_bf16 v[24:27],a[84:87],v[76:79],v[24:27]
v_mfma_f32_16x16x32_bf16 v[20:23],a[88:91],v[76:79],v[20:23]
v_mfma_f32_16x16x32_bf16 v[24:27],a[92:95],v[76:79],v[24:27]
s_waitcnt vmcnt(24)
s_barrier
s_add_u32 m0,s88,81920
s_add_u32 s61,s82,1835008
buffer_load_dwordx4 v28,s[40:43],s61 offen offset:0  lds
s_add_u32 m0,s88,82944
ds_read_b64_tr_b16 a[112:113],v38 offset:16384
buffer_load_dwordx4 v29,s[40:43],s61 offen offset:0  lds
s_add_u32 m0,s88,89984
ds_read_b64_tr_b16 a[114:115],v38 offset:16896
buffer_load_dwordx4 v28,s[40:43],s61 offen offset:128  lds
s_add_u32 m0,s88,91008
ds_read_b64_tr_b16 a[116:117],v39 offset:16384
buffer_load_dwordx4 v29,s[40:43],s61 offen offset:128  lds
ds_read_b64_tr_b16 a[118:119],v39 offset:16896
ds_read_b64_tr_b16 a[120:121],v38 offset:20480
ds_read_b64_tr_b16 a[122:123],v38 offset:20992
ds_read_b64_tr_b16 a[124:125],v39 offset:20480
ds_read_b64_tr_b16 a[126:127],v39 offset:20992
s_waitcnt lgkmcnt(8)
v_mfma_f32_16x16x32_bf16 v[20:23],a[96:99],v[76:79],v[20:23]
v_mfma_f32_16x16x32_bf16 v[24:27],a[100:103],v[76:79],v[24:27]
v_mfma_f32_16x16x32_bf16 v[20:23],a[104:107],v[76:79],v[20:23]
v_mfma_f32_16x16x32_bf16 v[24:27],a[108:111],v[76:79],v[24:27]
s_waitcnt vmcnt(24)
s_barrier
s_add_u32 m0,s88,98304
s_add_u32 s61,s82,1966080
buffer_load_dwordx4 v28,s[40:43],s61 offen offset:0  lds
s_add_u32 m0,s88,99328
ds_read_b64_tr_b16 a[128:129],v38 offset:32768
buffer_load_dwordx4 v29,s[40:43],s61 offen offset:0  lds
s_add_u32 m0,s88,106368
ds_read_b64_tr_b16 a[130:131],v38 offset:33280
buffer_load_dwordx4 v28,s[40:43],s61 offen offset:128  lds
s_add_u32 m0,s88,107392
ds_read_b64_tr_b16 a[132:133],v39 offset:32768
buffer_load_dwordx4 v29,s[40:43],s61 offen offset:128  lds
ds_read_b64_tr_b16 a[134:135],v39 offset:33280
ds_read_b64_tr_b16 a[136:137],v38 offset:36864
ds_read_b64_tr_b16 a[138:139],v38 offset:37376
ds_read_b64_tr_b16 a[140:141],v39 offset:36864
ds_read_b64_tr_b16 a[142:143],v39 offset:37376
s_waitcnt lgkmcnt(8)
v_mfma_f32_16x16x32_bf16 v[20:23],a[112:115],v[76:79],v[20:23]
v_mfma_f32_16x16x32_bf16 v[24:27],a[116:119],v[76:79],v[24:27]
v_mfma_f32_16x16x32_bf16 v[20:23],a[120:123],v[76:79],v[20:23]
v_mfma_f32_16x16x32_bf16 v[24:27],a[124:127],v[76:79],v[24:27]
s_waitcnt vmcnt(24)
s_barrier
ds_read_b64_tr_b16 a[144:145],v30 offset:0
ds_read_b64_tr_b16 a[146:147],v30 offset:512
ds_read_b64_tr_b16 a[148:149],v31 offset:0
ds_read_b64_tr_b16 a[150:151],v31 offset:512
ds_read_b64_tr_b16 a[152:153],v30 offset:4096
ds_read_b64_tr_b16 a[154:155],v30 offset:4608
ds_read_b64_tr_b16 a[156:157],v31 offset:4096
ds_read_b64_tr_b16 a[158:159],v31 offset:4608
s_waitcnt lgkmcnt(8)
v_mfma_f32_16x16x32_bf16 v[20:23],a[128:131],v[76:79],v[20:23]
v_mfma_f32_16x16x32_bf16 v[24:27],a[132:135],v[76:79],v[24:27]
v_mfma_f32_16x16x32_bf16 v[20:23],a[136:139],v[76:79],v[20:23]
v_mfma_f32_16x16x32_bf16 v[24:27],a[140:143],v[76:79],v[24:27]
s_waitcnt vmcnt(20)
s_barrier
ds_read_b64_tr_b16 a[160:161],v30 offset:16384
ds_read_b64_tr_b16 a[162:163],v30 offset:16896
ds_read_b64_tr_b16 a[164:165],v31 offset:16384
ds_read_b64_tr_b16 a[166:167],v31 offset:16896
ds_read_b64_tr_b16 a[168:169],v30 offset:20480
ds_read_b64_tr_b16 a[170:171],v30 offset:20992
ds_read_b64_tr_b16 a[172:173],v31 offset:20480
ds_read_b64_tr_b16 a[174:175],v31 offset:20992
s_waitcnt lgkmcnt(8)
v_mfma_f32_16x16x32_bf16 v[20:23],a[144:147],v[76:79],v[20:23]
v_mfma_f32_16x16x32_bf16 v[24:27],a[148:151],v[76:79],v[24:27]
v_mfma_f32_16x16x32_bf16 v[20:23],a[152:155],v[76:79],v[20:23]
v_mfma_f32_16x16x32_bf16 v[24:27],a[156:159],v[76:79],v[24:27]
s_waitcnt vmcnt(16)
s_barrier
ds_read_b64_tr_b16 a[176:177],v30 offset:32768
ds_read_b64_tr_b16 a[178:179],v30 offset:33280
ds_read_b64_tr_b16 a[180:181],v31 offset:32768
ds_read_b64_tr_b16 a[182:183],v31 offset:33280
ds_read_b64_tr_b16 a[184:185],v30 offset:36864
ds_read_b64_tr_b16 a[186:187],v30 offset:37376
ds_read_b64_tr_b16 a[188:189],v31 offset:36864
ds_read_b64_tr_b16 a[190:191],v31 offset:37376
s_getpc_b64 s[46:47]
Lg1_cp_%=:
s_add_u32 s46,s46,Lg1_init_%=-Lg1_cp_%=
s_addc_u32 s47,s47,0
s_lshl_b32 s68,s54,13
v_lshl_add_u32 v60,v2,3,s68
v_min_u32 v61,Lg1_done_%=-Lg1_init_%=,v60
global_load_dword v64,v61,s[46:47]
v_add_u32 v60,32768,v60
v_min_u32 v62,Lg1_done_%=-Lg1_init_%=,v60
global_load_dword v65,v62,s[46:47]
s_waitcnt lgkmcnt(8)
v_mfma_f32_16x16x32_bf16 v[20:23],a[160:163],v[76:79],v[20:23]
v_mfma_f32_16x16x32_bf16 v[24:27],a[164:167],v[76:79],v[24:27]
v_mfma_f32_16x16x32_bf16 v[20:23],a[168:171],v[76:79],v[20:23]
v_mfma_f32_16x16x32_bf16 v[24:27],a[172:175],v[76:79],v[24:27]
s_waitcnt vmcnt(14)
s_barrier
ds_read_b64_tr_b16 a[192:193],v36 offset:0)"
R"(
ds_read_b64_tr_b16 a[194:195],v36 offset:512
ds_read_b64_tr_b16 a[196:197],v37 offset:0
ds_read_b64_tr_b16 a[198:199],v37 offset:512
ds_read_b64_tr_b16 a[200:201],v36 offset:4096
ds_read_b64_tr_b16 a[202:203],v36 offset:4608
ds_read_b64_tr_b16 a[204:205],v37 offset:4096
ds_read_b64_tr_b16 a[206:207],v37 offset:4608
s_waitcnt lgkmcnt(8)
v_mfma_f32_16x16x32_bf16 v[20:23],a[176:179],v[76:79],v[20:23]
v_mfma_f32_16x16x32_bf16 v[24:27],a[180:183],v[76:79],v[24:27]
v_mfma_f32_16x16x32_bf16 v[20:23],a[184:187],v[76:79],v[20:23]
v_mfma_f32_16x16x32_bf16 v[24:27],a[188:191],v[76:79],v[24:27]
s_waitcnt vmcnt(10)
s_barrier
ds_read_b64_tr_b16 a[208:209],v36 offset:16384
ds_read_b64_tr_b16 a[210:211],v36 offset:16896
ds_read_b64_tr_b16 a[212:213],v37 offset:16384
ds_read_b64_tr_b16 a[214:215],v37 offset:16896
ds_read_b64_tr_b16 a[216:217],v36 offset:20480
ds_read_b64_tr_b16 a[218:219],v36 offset:20992
ds_read_b64_tr_b16 a[220:221],v37 offset:20480
ds_read_b64_tr_b16 a[222:223],v37 offset:20992
s_waitcnt lgkmcnt(8)
v_mfma_f32_16x16x32_bf16 v[20:23],a[192:195],v[76:79],v[20:23]
v_mfma_f32_16x16x32_bf16 v[24:27],a[196:199],v[76:79],v[24:27]
v_mfma_f32_16x16x32_bf16 v[20:23],a[200:203],v[76:79],v[20:23]
v_mfma_f32_16x16x32_bf16 v[24:27],a[204:207],v[76:79],v[24:27]
s_waitcnt vmcnt(6)
s_barrier
ds_read_b64_tr_b16 a[224:225],v36 offset:32768
ds_read_b64_tr_b16 a[226:227],v36 offset:33280
ds_read_b64_tr_b16 a[228:229],v37 offset:32768
ds_read_b64_tr_b16 a[230:231],v37 offset:33280
ds_read_b64_tr_b16 a[232:233],v36 offset:36864
ds_read_b64_tr_b16 a[234:235],v36 offset:37376
ds_read_b64_tr_b16 a[236:237],v37 offset:36864
ds_read_b64_tr_b16 a[238:239],v37 offset:37376
s_waitcnt lgkmcnt(8)
v_mfma_f32_16x16x32_bf16 v[20:23],a[208:211],v[76:79],v[20:23]
v_mfma_f32_16x16x32_bf16 v[24:27],a[212:215],v[76:79],v[24:27]
v_mfma_f32_16x16x32_bf16 v[20:23],a[216:219],v[76:79],v[20:23]
v_mfma_f32_16x16x32_bf16 v[24:27],a[220:223],v[76:79],v[24:27]
s_waitcnt vmcnt(2)
s_barrier
ds_read_b64_tr_b16 a[240:241],v38 offset:0
ds_read_b64_tr_b16 a[242:243],v38 offset:512
ds_read_b64_tr_b16 a[244:245],v39 offset:0
ds_read_b64_tr_b16 a[246:247],v39 offset:512
ds_read_b64_tr_b16 a[248:249],v38 offset:4096
ds_read_b64_tr_b16 a[250:251],v38 offset:4608
ds_read_b64_tr_b16 a[252:253],v39 offset:4096
ds_read_b64_tr_b16 a[254:255],v39 offset:4608
s_waitcnt lgkmcnt(8)
v_mfma_f32_16x16x32_bf16 v[20:23],a[224:227],v[76:79],v[20:23]
v_mfma_f32_16x16x32_bf16 v[24:27],a[228:231],v[76:79],v[24:27]
v_mfma_f32_16x16x32_bf16 v[20:23],a[232:235],v[76:79],v[20:23]
v_mfma_f32_16x16x32_bf16 v[24:27],a[236:239],v[76:79],v[24:27]
s_waitcnt lgkmcnt(0)
v_mfma_f32_16x16x32_bf16 v[20:23],a[240:243],v[76:79],v[20:23]
v_mfma_f32_16x16x32_bf16 v[24:27],a[244:247],v[76:79],v[24:27]
v_mfma_f32_16x16x32_bf16 v[20:23],a[248:251],v[76:79],v[20:23]
v_mfma_f32_16x16x32_bf16 v[24:27],a[252:255],v[76:79],v[24:27]
s_waitcnt lgkmcnt(0)
s_barrier
v_readfirstlane_b32 s60,v5
s_nop 3
s_add_u32 s60,s60,1
s_cmp_eq_u32 s60,0
s_cselect_b32 s60,1,s60
Lg1_init_%=:
v_mov_b32 v5,s60
s_mov_b32 s59,536870912
s_add_u32 s57,s56,131072
s_mov_b32 s79,0
s_mov_b32 s75,0x3a800000
s_mov_b32 s76,0xc0135761
s_mov_b32 s77,0x3d372713
s_mov_b32 s44,s59
s_mov_b32 s45,s59
s_mov_b32 s46,s59
s_mov_b32 s47,s59
s_mov_b32 s48,s83
s_add_u32 s49,s48,s55
s_cmp_eq_u32 s49,s56
s_cselect_b32 s49,s57,s49
s_cmp_eq_u32 s49,s58
s_cselect_b32 s49,s59,s49
s_add_u32 s50,s49,s55
s_cmp_eq_u32 s50,s56
s_cselect_b32 s50,s57,s50
s_cmp_eq_u32 s50,s58
s_cselect_b32 s50,s59,s50
s_add_u32 s51,s50,s55
s_cmp_eq_u32 s51,s56
s_cselect_b32 s51,s57,s51
s_cmp_eq_u32 s51,s58
s_cselect_b32 s51,s59,s51
s_add_u32 s52,s51,s55
s_cmp_eq_u32 s52,s56
s_cselect_b32 s52,s57,s52
s_cmp_eq_u32 s52,s58
s_cselect_b32 s52,s59,s52
s_lshl_b32 s63,s54,12
s_add_u32 s63,s63,65536
s_add_u32 s64,s63,32768
s_lshl_b32 s88,s54,10
s_add_u32 s88,s88,131072
s_lshl_b32 s78,s54,11
s_add_u32 s78,s78,132608
v_mov_b32 v15,0x3727c5ac
v_mov_b32 v100,0x3f803f80
v_mov_b32 v101,0x3f803f80
v_mov_b32 v102,0x3f803f80
v_mov_b32 v103,0x3f803f80
s_mov_b32 s89,1048576
s_mov_b32 s82,0xaaaaaaaa
s_mov_b32 s83,0xaaaaaaaa
s_mov_b32 s84,0xcccccccc
s_mov_b32 s85,0xcccccccc
s_mov_b32 s98,0x00f0000f
s_mov_b32 s99,0xf0000f00
s_getpc_b64 s[90:91]
Lg1_pc_%=:
s_mov_b32 s68,Lg1_d1w0_%=-Lg1_pc_%=
s_cmp_eq_u32 s54,1
s_cselect_b32 s68,Lg1_d1w1_%=-Lg1_pc_%=,s68
s_cmp_eq_u32 s54,2
s_cselect_b32 s68,Lg1_d1w2_%=-Lg1_pc_%=,s68
s_cmp_eq_u32 s54,3
s_cselect_b32 s68,Lg1_d1w3_%=-Lg1_pc_%=,s68
s_add_u32 s96,s90,s68
s_addc_u32 s97,s91,0
s_mov_b32 s68,Lg1_d0w0_%=-Lg1_pc_%=
s_cmp_eq_u32 s54,1
s_cselect_b32 s68,Lg1_d0w1_%=-Lg1_pc_%=,s68
s_cmp_eq_u32 s54,2
s_cselect_b32 s68,Lg1_d0w2_%=-Lg1_pc_%=,s68
s_cmp_eq_u32 s54,3
s_cselect_b32 s68,Lg1_d0w3_%=-Lg1_pc_%=,s68
s_add_u32 s94,s90,s68
s_addc_u32 s95,s91,0
s_mov_b32 s68,Lg1_a1w0_%=-Lg1_pc_%=
s_cmp_eq_u32 s54,1
s_cselect_b32 s68,Lg1_a1w1_%=-Lg1_pc_%=,s68
s_cmp_eq_u32 s54,2
s_cselect_b32 s68,Lg1_a1w2_%=-Lg1_pc_%=,s68
s_cmp_eq_u32 s54,3
s_cselect_b32 s68,Lg1_a1w3_%=-Lg1_pc_%=,s68
s_add_u32 s92,s90,s68
s_addc_u32 s93,s91,0
s_mov_b32 s68,Lg1_a0w0_%=-Lg1_pc_%=
s_cmp_eq_u32 s54,1
s_cselect_b32 s68,Lg1_a0w1_%=-Lg1_pc_%=,s68
s_cmp_eq_u32 s54,2
s_cselect_b32 s68,Lg1_a0w2_%=-Lg1_pc_%=,s68
s_cmp_eq_u32 s54,3
s_cselect_b32 s68,Lg1_a0w3_%=-Lg1_pc_%=,s68
s_add_u32 s90,s90,s68
s_addc_u32 s91,s91,0
s_mov_b32 s66,s59
s_mov_b32 s65,0
s_mov_b32 s62,s63
s_branch Lg1_slow0_%=
Lg1_a0w0_%=:
v_mfma_f32_16x16x32_bf16 v[28:31],a[0:3],v[44:47],0
s_add_u32 m0,s62,16384
ds_read_b128 v[72:75],v2 offset:7168
s_sub_u32 s69,s66,1048576
v_mfma_f32_16x16x32_bf16 v[32:35],a[4:7],v[44:47],0
buffer_load_dwordx4 v1,s[36:39],s66 offen offset:0 sc1 lds
v_mfma_f32_16x16x32_bf16 v[104:107],v[100:103],v[44:47],0
s_cmp_lt_u32 s69,2097152
s_cselect_b32 s89,3145728,s89
s_sub_u32 s69,s66,3145728
v_mfma_f32_16x16x32_bf16 v[108:111],v[44:47],v[44:47],0
s_cmp_lt_u32 s69,1048576
v_fma_f32 v36,-v96,v20,v36
v_fma_f32 v37,-v96,v21,v37
v_mfma_f32_16x16x32_bf16 v[28:31],a[8:11],v[48:51],v[28:31]
s_cselect_b32 s89,4194304,s89
ds_read_b128 v[44:47],v2 offset:8192
v_fma_f32 v38,-v96,v22,v38
v_mfma_f32_16x16x32_bf16 v[32:35],a[12:15],v[48:51],v[32:35]
buffer_load_dwordx4 v1,s[36:39],s66 offen offset:1024 sc1 lds
v_mfma_f32_16x16x32_bf16 v[104:107],v[100:103],v[48:51],v[104:107]
s_lshr_b32 s73,s47,0
v_fma_f32 v39,-v96,v23,v39
v_mul_f32 v76,v36,v36
v_mfma_f32_16x16x32_bf16 v[108:111],v[48:51],v[48:51],v[108:111]
s_lshr_b32 s72,s66,6
v_mul_f32 v77,v37,v37
v_mul_f32 v78,v38,v38
v_mfma_f32_16x16x32_bf16 v[28:31],a[16:19],v[52:55],v[28:31]
s_lshr_b32 s71,s62,2
ds_read_b128 v[48:51],v2 offset:9216
v_mul_f32 v79,v39,v39
v_mfma_f32_16x16x32_bf16 v[32:35],a[20:23],v[52:55],v[32:35]
buffer_load_dwordx4 v1,s[36:39],s66 offen offset:2048 sc1 lds
v_mfma_f32_16x16x32_bf16 v[104:107],v[100:103],v[52:55],v[104:107]
s_cmp_lt_u32 s66,4194304)"
R"(
v_fma_f32 v76,v98,v76,v97
v_mul_f32 v76,v36,v76
v_mfma_f32_16x16x32_bf16 v[108:111],v[52:55],v[52:55],v[108:111]
s_cselect_b32 s72,s59,s72
v_exp_f32 v76,v76
v_mfma_f32_16x16x32_bf16 v[28:31],a[24:27],v[56:59],v[28:31]
ds_read_b128 v[52:55],v2 offset:10240
v_fma_f32 v76,v76,v99,v99
v_mfma_f32_16x16x32_bf16 v[32:35],a[28:31],v[56:59],v[32:35]
buffer_load_dwordx4 v1,s[36:39],s66 offen offset:3072 sc1 lds
v_mfma_f32_16x16x32_bf16 v[104:107],v[100:103],v[56:59],v[104:107]
s_add_u32 m0,s71,s88
v_rcp_f32 v76,v76
v_mfma_f32_16x16x32_bf16 v[108:111],v[56:59],v[56:59],v[108:111]
s_waitcnt lgkmcnt(3)
buffer_load_dword v10,s[36:39],s72 offen offset:0 sc1 lds
v_mfma_f32_16x16x32_bf16 v[28:31],a[32:35],v[60:63],v[28:31]
s_xor_b32 s87,s64,65536
ds_read_b128 v[56:59],v2 offset:11264
v_fma_f32 v77,v98,v77,v97
v_mfma_f32_16x16x32_bf16 v[32:35],a[36:39],v[60:63],v[32:35]
s_cmp_ge_u32 s66,s59
v_mul_f32 v77,v37,v77
v_fma_f32 v78,v98,v78,v97
v_mfma_f32_16x16x32_bf16 v[28:31],a[40:43],v[64:67],v[28:31]
s_cselect_b32 s86,s49,s50
ds_read_b128 v[60:63],v2 offset:12288
v_mul_f32 v78,v38,v78
v_mfma_f32_16x16x32_bf16 v[32:35],a[44:47],v[64:67],v[32:35]
s_cselect_b32 s87,s87,s63
v_exp_f32 v77,v77
v_mfma_f32_16x16x32_bf16 v[28:31],a[48:51],v[68:71],v[28:31]
s_mov_b32 s62,s87
ds_read_b128 v[64:67],v2 offset:13312
v_fma_f32 v77,v77,v99,v99
v_mfma_f32_16x16x32_bf16 v[32:35],a[52:55],v[68:71],v[32:35]
s_cmp_lt_u32 s86,s89
v_exp_f32 v78,v78
v_mfma_f32_16x16x32_bf16 v[28:31],a[56:59],v[72:75],v[28:31]
ds_read_b128 v[68:71],v2 offset:14336
v_fma_f32 v78,v78,v99,v99
v_mfma_f32_16x16x32_bf16 v[32:35],a[60:63],v[72:75],v[32:35]
s_waitcnt lgkmcnt(3)
v_rcp_f32 v77,v77
v_mfma_f32_16x16x32_bf16 v[28:31],a[64:67],v[44:47],v[28:31]
s_waitcnt vmcnt(10)
ds_read_b128 v[72:75],v2 offset:15360
v_fma_f32 v79,v98,v79,v97
v_mfma_f32_16x16x32_bf16 v[32:35],a[68:71],v[44:47],v[32:35]
s_barrier
v_mfma_f32_16x16x32_bf16 v[28:31],a[72:75],v[48:51],v[28:31]
ds_read_b128 v[44:47],v2 offset:16384
ds_read_b32 v16,v7 offset:0
v_mfma_f32_16x16x32_bf16 v[32:35],a[76:79],v[48:51],v[32:35]
v_rcp_f32 v78,v78
v_mfma_f32_16x16x32_bf16 v[28:31],a[80:83],v[52:55],v[28:31]
ds_read_b128 v[48:51],v2 offset:17408
v_mul_f32 v79,v39,v79
v_mfma_f32_16x16x32_bf16 v[32:35],a[84:87],v[52:55],v[32:35]
v_exp_f32 v79,v79
v_mfma_f32_16x16x32_bf16 v[28:31],a[88:91],v[56:59],v[28:31]
s_waitcnt lgkmcnt(1)
ds_read_b128 v[52:55],v2 offset:18432
v_fma_f32 v79,v79,v99,v99
v_mfma_f32_16x16x32_bf16 v[32:35],a[92:95],v[56:59],v[32:35]
v_cmp_eq_u32 vcc,s60,v16
v_rcp_f32 v79,v79
v_mul_f32 v36,v36,v76
v_mfma_f32_16x16x32_bf16 v[28:31],a[96:99],v[60:63],v[28:31]
s_cselect_b32 vcc_lo,-1,vcc_lo
ds_read_b128 v[56:59],v2 offset:19456
v_mul_f32 v37,v37,v77
v_mfma_f32_16x16x32_bf16 v[32:35],a[100:103],v[60:63],v[32:35]
s_cmp_eq_u32 vcc_lo,-1
v_mul_f32 v38,v38,v78
v_mul_f32 v39,v39,v79
v_mfma_f32_16x16x32_bf16 v[28:31],a[104:107],v[64:67],v[28:31]
s_cselect_b32 s67,s86,s59
ds_read_b128 v[60:63],v2 offset:20480
v_cvt_pk_bf16_f32 v84,v36,v37
v_mfma_f32_16x16x32_bf16 v[32:35],a[108:111],v[64:67],v[32:35]
s_lshr_b32 s69,s50,5
v_cvt_pk_bf16_f32 v85,v38,v39
v_fma_f32 v40,-v96,v24,v40
v_mfma_f32_16x16x32_bf16 v[28:31],a[112:115],v[68:71],v[28:31]
s_cmp_lt_u32 s50,4194304
ds_read_b128 v[64:67],v2 offset:21504
v_fma_f32 v41,-v96,v25,v41
v_mfma_f32_16x16x32_bf16 v[32:35],a[116:119],v[68:71],v[32:35]
s_cselect_b32 s68,0xffffcfff,-1
v_fma_f32 v42,-v96,v26,v42
v_fma_f32 v43,-v96,v27,v43
v_mfma_f32_16x16x32_bf16 v[28:31],a[120:123],v[72:75],v[28:31]
s_and_b32 s68,s68,s69
ds_read_b128 v[68:71],v2 offset:22528
v_mul_f32 v80,v40,v40
v_mfma_f32_16x16x32_bf16 v[32:35],a[124:127],v[72:75],v[32:35]
s_waitcnt lgkmcnt(3)
v_mul_f32 v81,v41,v41
v_mul_f32 v82,v42,v42
v_mfma_f32_16x16x32_bf16 v[28:31],a[128:131],v[44:47],v[28:31]
s_cmp_lt_u32 s50,s89
ds_read_b128 v[72:75],v2 offset:23552
v_mul_f32 v83,v43,v43
v_mfma_f32_16x16x32_bf16 v[32:35],a[132:135],v[44:47],v[32:35]
s_cselect_b32 s68,s59,s68
v_fma_f32 v80,v98,v80,v97
v_mul_f32 v80,v40,v80
v_mfma_f32_16x16x32_bf16 v[104:107],v[100:103],v[44:47],v[104:107]
s_lshr_b32 s69,s51,5
v_exp_f32 v80,v80
v_mfma_f32_16x16x32_bf16 v[108:111],v[44:47],v[44:47],v[108:111]
s_cmp_lt_u32 s51,4194304
v_fma_f32 v80,v80,v99,v99
v_fma_f32 v81,v98,v81,v97
v_mfma_f32_16x16x32_bf16 v[28:31],a[136:139],v[48:51],v[28:31]
s_cselect_b32 s70,0xffffcfff,-1
ds_read_b128 v[44:47],v2 offset:24576
v_mul_f32 v81,v41,v81
v_mfma_f32_16x16x32_bf16 v[32:35],a[140:143],v[48:51],v[32:35]
s_and_b32 s70,s70,s69
v_exp_f32 v81,v81
v_mfma_f32_16x16x32_bf16 v[104:107],v[100:103],v[48:51],v[104:107]
s_cmp_lt_u32 s51,s89
v_rcp_f32 v80,v80
v_mfma_f32_16x16x32_bf16 v[108:111],v[48:51],v[48:51],v[108:111]
s_cselect_b32 s70,s59,s70
v_fma_f32 v81,v81,v99,v99
v_fma_f32 v82,v98,v82,v97
v_mfma_f32_16x16x32_bf16 v[28:31],a[144:147],v[52:55],v[28:31]
s_add_u32 s53,s52,s55
ds_read_b128 v[48:51],v2 offset:25600
v_mul_f32 v82,v42,v82
v_mfma_f32_16x16x32_bf16 v[32:35],a[148:151],v[52:55],v[32:35]
s_cmp_eq_u32 s53,s56
v_exp_f32 v82,v82
v_mfma_f32_16x16x32_bf16 v[104:107],v[100:103],v[52:55],v[104:107]
s_cselect_b32 s53,s57,s53
v_rcp_f32 v81,v81
v_mfma_f32_16x16x32_bf16 v[108:111],v[52:55],v[52:55],v[108:111]
s_cmp_eq_u32 s53,s58
v_fma_f32 v82,v82,v99,v99
v_fma_f32 v83,v98,v83,v97
v_mfma_f32_16x16x32_bf16 v[28:31],a[152:155],v[56:59],v[28:31]
s_cselect_b32 s53,s59,s53
ds_read_b128 v[52:55],v2 offset:26624
v_mul_f32 v83,v43,v83
v_mfma_f32_16x16x32_bf16 v[32:35],a[156:159],v[56:59],v[32:35]
s_xor_b32 s63,s63,65536
v_exp_f32 v83,v83
v_mfma_f32_16x16x32_bf16 v[104:107],v[100:103],v[56:59],v[104:107]
s_cmp_ge_u32 s67,s59
v_rcp_f32 v82,v82
v_mfma_f32_16x16x32_bf16 v[108:111],v[56:59],v[56:59],v[108:111]
s_waitcnt lgkmcnt(3)
v_fma_f32 v83,v83,v99,v99
v_mul_f32 v40,v40,v80
v_mfma_f32_16x16x32_bf16 v[28:31],a[160:163],v[60:63],v[28:31]
s_cselect_b32 s68,s68,s70
ds_read_b128 v[56:59],v2 offset:27648
v_mul_f32 v41,v41,v81
v_mfma_f32_16x16x32_bf16 v[32:35],a[164:167],v[60:63],v[32:35]
s_add_u32 m0,s78,256
v_rcp_f32 v83,v83
v_mfma_f32_16x16x32_bf16 v[28:31],a[168:171],v[64:67],v[28:31]
buffer_load_dword v6,s[36:39],s68 offen offset:0 sc1 lds
v_cndmask_b32 v112,v108,v109,s[82:83]
v_cndmask_b32 v113,v110,v111,s[82:83]
v_cndmask_b32 v112,v112,v113,s[84:85]
v_cndmask_b32 v112,v104,v112,s[98:99]
v_mfma_f32_16x16x32_bf16 v[32:35],a[172:175],v[64:67],v[32:35]
s_mov_b32 m0,s62
ds_read_b128 v[60:63],v2 offset:28672
ds_write_b32 v13,v112 offset:0
v_mfma_f32_16x16x32_bf16 v[28:31],a[176:179],v[68:71],v[28:31]
buffer_load_dwordx4 v0,s[36:39],s67 offen offset:0 sc1 lds
v_mfma_f32_16x16x32_bf16 v[32:35],a[180:183],v[68:71],v[32:35]
s_cmp_ge_u32 s66,s59
ds_read_b128 v[64:67],v2 offset:29696
v_mul_f32 v42,v42,v82
v_mfma_f32_16x16x32_bf16 v[28:31],a[184:187],v[72:75],v[28:31]
ds_read_b128 v[68:71],v2 offset:30720
v_mul_f32 v43,v43,v83
v_mfma_f32_16x16x32_bf16 v[32:35],a[188:191],v[72:75],v[32:35]
s_waitcnt lgkmcnt(4)
v_cvt_pk_bf16_f32 v86,v40,v41
v_cvt_pk_bf16_f32 v87,v42,v43
v_mfma_f32_16x16x32_bf16 v[28:31],a[192:195],v[44:47],v[28:31]
s_waitcnt vmcnt(7) lgkmcnt(2)
ds_read_b128 v[72:75],v2 offset:31744
v_mfma_f32_16x16x32_bf16 v[32:35],a[196:199],v[44:47],v[32:35]
s_barrier
v_mfma_f32_16x16x32_bf16 v[28:31],a[200:203],v[48:51],v[28:31]
buffer_load_dwordx4 v0,s[36:39],s67 offen offset:1024 sc1 lds
v_mfma_f32_16x16x32_bf16 v[32:35],a[204:207],v[48:51],v[32:35]
ds_read_b128 v[44:47],v2 offset:32768
ds_read_b128 v[116:119],v14 offset:0
v_mfma_f32_16x16x32_bf16 v[28:31],a[208:211],v[52:55],v[28:31]
ds_read_b128 v[120:123],v14 offset:16
ds_read_b128 v[48:51],v2 offset:33792)"
R"(
v_mfma_f32_16x16x32_bf16 v[32:35],a[212:215],v[52:55],v[32:35]
v_mfma_f32_16x16x32_bf16 v[28:31],a[216:219],v[56:59],v[28:31]
buffer_load_dwordx4 v0,s[36:39],s67 offen offset:2048 sc1 lds
v_mfma_f32_16x16x32_bf16 v[32:35],a[220:223],v[56:59],v[32:35]
s_waitcnt lgkmcnt(4)
ds_read_b128 v[52:55],v2 offset:34816
v_mfma_f32_16x16x32_bf16 v[28:31],a[224:227],v[60:63],v[28:31]
s_waitcnt lgkmcnt(2)
ds_read_b128 v[56:59],v2 offset:35840
v_add_f32 v116,v116,v117
v_mfma_f32_16x16x32_bf16 v[32:35],a[228:231],v[60:63],v[32:35]
v_add_f32 v118,v118,v119
v_add_f32 v116,v116,v118
v_mfma_f32_16x16x32_bf16 v[28:31],a[232:235],v[64:67],v[28:31]
buffer_load_dwordx4 v0,s[36:39],s67 offen offset:3072 sc1 lds
v_mfma_f32_16x16x32_bf16 v[32:35],a[236:239],v[64:67],v[32:35]
ds_read_b128 v[60:63],v2 offset:36864
v_mul_f32 v92,s75,v116
v_mfma_f32_16x16x32_bf16 v[28:31],a[240:243],v[68:71],v[28:31]
ds_read_b128 v[64:67],v2 offset:37888
v_add_f32 v120,v120,v121
v_mfma_f32_16x16x32_bf16 v[32:35],a[244:247],v[68:71],v[32:35]
v_add_f32 v122,v122,v123
v_add_f32 v120,v120,v122
v_mul_f32 v120,s75,v120
v_fma_f32 v120,-v92,v92,v120
v_max_f32 v120,0,v120
v_add_f32 v115,v15,v120
v_fma_f32 v120,v15,v115,v120
v_rsq_f32 v116,v120
v_mfma_f32_16x16x32_bf16 v[28:31],a[248:251],v[72:75],v[28:31]
s_waitcnt lgkmcnt(2)
ds_read_b128 v[68:71],v2 offset:38912
buffer_store_dwordx4 v[84:87],v3,s[36:39],s73 offen offset:0 sc0 sc1
v_mul_f32 v93,s76,v116
v_mul_f32 v94,v116,v116
v_mul_f32 v95,v120,v116
v_mul_f32 v94,s77,v94
v_mul_f32 v94,v94,v93
v_mfma_f32_16x16x32_bf16 v[32:35],a[252:255],v[72:75],v[32:35]
s_cbranch_scc1 Lg1_slow1_%=
Lg1_a1w0_%=:
v_mfma_f32_16x16x32_bf16 v[36:39],a[0:3],v[44:47],0
s_add_u32 m0,s62,16384
ds_read_b128 v[72:75],v2 offset:39936
s_sub_u32 s69,s67,1048576
v_mfma_f32_16x16x32_bf16 v[40:43],a[4:7],v[44:47],0
buffer_load_dwordx4 v1,s[36:39],s67 offen offset:0 sc1 lds
v_mfma_f32_16x16x32_bf16 v[104:107],v[100:103],v[44:47],0
s_cmp_lt_u32 s69,2097152
s_cselect_b32 s89,3145728,s89
s_sub_u32 s69,s67,3145728
v_mfma_f32_16x16x32_bf16 v[108:111],v[44:47],v[44:47],0
s_cmp_lt_u32 s69,1048576
v_fma_f32 v28,-v92,v20,v28
v_fma_f32 v29,-v92,v21,v29
v_mfma_f32_16x16x32_bf16 v[36:39],a[8:11],v[48:51],v[36:39]
s_cselect_b32 s89,4194304,s89
ds_read_b128 v[44:47],v2 offset:40960
v_fma_f32 v30,-v92,v22,v30
v_mfma_f32_16x16x32_bf16 v[40:43],a[12:15],v[48:51],v[40:43]
buffer_load_dwordx4 v1,s[36:39],s67 offen offset:1024 sc1 lds
v_mfma_f32_16x16x32_bf16 v[104:107],v[100:103],v[48:51],v[104:107]
s_lshr_b32 s73,s48,0
v_fma_f32 v31,-v92,v23,v31
v_mul_f32 v76,v28,v28
v_mfma_f32_16x16x32_bf16 v[108:111],v[48:51],v[48:51],v[108:111]
s_lshr_b32 s72,s67,6
v_mul_f32 v77,v29,v29
v_mul_f32 v78,v30,v30
v_mfma_f32_16x16x32_bf16 v[36:39],a[16:19],v[52:55],v[36:39]
s_lshr_b32 s71,s62,2
ds_read_b128 v[48:51],v2 offset:41984
v_mul_f32 v79,v31,v31
v_mfma_f32_16x16x32_bf16 v[40:43],a[20:23],v[52:55],v[40:43]
buffer_load_dwordx4 v1,s[36:39],s67 offen offset:2048 sc1 lds
v_mfma_f32_16x16x32_bf16 v[104:107],v[100:103],v[52:55],v[104:107]
s_cmp_lt_u32 s67,4194304
v_fma_f32 v76,v94,v76,v93
v_mul_f32 v76,v28,v76
v_mfma_f32_16x16x32_bf16 v[108:111],v[52:55],v[52:55],v[108:111]
s_cselect_b32 s72,s59,s72
v_exp_f32 v76,v76
v_mfma_f32_16x16x32_bf16 v[36:39],a[24:27],v[56:59],v[36:39]
s_mov_b64 s[44:45],s[46:47]
ds_read_b128 v[52:55],v2 offset:43008
v_fma_f32 v76,v76,v95,v95
v_mfma_f32_16x16x32_bf16 v[40:43],a[28:31],v[56:59],v[40:43]
buffer_load_dwordx4 v1,s[36:39],s67 offen offset:3072 sc1 lds
v_mfma_f32_16x16x32_bf16 v[104:107],v[100:103],v[56:59],v[104:107]
s_add_u32 m0,s71,s88
v_rcp_f32 v76,v76
v_mfma_f32_16x16x32_bf16 v[108:111],v[56:59],v[56:59],v[108:111]
s_waitcnt lgkmcnt(3)
buffer_load_dword v10,s[36:39],s72 offen offset:0 sc1 lds
v_mfma_f32_16x16x32_bf16 v[36:39],a[32:35],v[60:63],v[36:39]
s_xor_b32 s87,s63,65536
ds_read_b128 v[56:59],v2 offset:44032
v_fma_f32 v77,v94,v77,v93
v_mfma_f32_16x16x32_bf16 v[40:43],a[36:39],v[60:63],v[40:43]
s_cmp_ge_u32 s67,s59
v_mul_f32 v77,v29,v77
v_fma_f32 v78,v94,v78,v93
v_mfma_f32_16x16x32_bf16 v[36:39],a[40:43],v[64:67],v[36:39]
s_cselect_b32 s86,s50,s51
ds_read_b128 v[60:63],v2 offset:45056
v_mul_f32 v78,v30,v78
v_mfma_f32_16x16x32_bf16 v[40:43],a[44:47],v[64:67],v[40:43]
s_cselect_b32 s87,s87,s64
v_exp_f32 v77,v77
v_mfma_f32_16x16x32_bf16 v[36:39],a[48:51],v[68:71],v[36:39]
s_mov_b32 s62,s87
ds_read_b128 v[64:67],v2 offset:46080
v_fma_f32 v77,v77,v95,v95
v_mfma_f32_16x16x32_bf16 v[40:43],a[52:55],v[68:71],v[40:43]
s_cmp_lt_u32 s86,s89
v_exp_f32 v78,v78
v_mfma_f32_16x16x32_bf16 v[36:39],a[56:59],v[72:75],v[36:39]
s_mov_b64 s[46:47],s[48:49]
ds_read_b128 v[68:71],v2 offset:47104
v_fma_f32 v78,v78,v95,v95
v_mfma_f32_16x16x32_bf16 v[40:43],a[60:63],v[72:75],v[40:43]
s_waitcnt lgkmcnt(3)
v_rcp_f32 v77,v77
v_mfma_f32_16x16x32_bf16 v[36:39],a[64:67],v[44:47],v[36:39]
s_waitcnt vmcnt(10)
ds_read_b128 v[72:75],v2 offset:48128
v_fma_f32 v79,v94,v79,v93
v_mfma_f32_16x16x32_bf16 v[40:43],a[68:71],v[44:47],v[40:43]
s_barrier
v_mfma_f32_16x16x32_bf16 v[36:39],a[72:75],v[48:51],v[36:39]
s_mov_b64 s[48:49],s[50:51]
ds_read_b128 v[44:47],v2 offset:49152
ds_read_b32 v16,v7 offset:256
v_mfma_f32_16x16x32_bf16 v[40:43],a[76:79],v[48:51],v[40:43]
v_rcp_f32 v78,v78
v_mfma_f32_16x16x32_bf16 v[36:39],a[80:83],v[52:55],v[36:39]
ds_read_b128 v[48:51],v2 offset:50176
v_mul_f32 v79,v31,v79
v_mfma_f32_16x16x32_bf16 v[40:43],a[84:87],v[52:55],v[40:43]
v_exp_f32 v79,v79
v_mfma_f32_16x16x32_bf16 v[36:39],a[88:91],v[56:59],v[36:39]
s_waitcnt lgkmcnt(1)
ds_read_b128 v[52:55],v2 offset:51200
v_fma_f32 v79,v79,v95,v95
v_mfma_f32_16x16x32_bf16 v[40:43],a[92:95],v[56:59],v[40:43]
v_cmp_eq_u32 vcc,s60,v16
v_rcp_f32 v79,v79
v_mul_f32 v28,v28,v76
v_mfma_f32_16x16x32_bf16 v[36:39],a[96:99],v[60:63],v[36:39]
s_cselect_b32 vcc_lo,-1,vcc_lo
ds_read_b128 v[56:59],v2 offset:52224
v_mul_f32 v29,v29,v77
v_mfma_f32_16x16x32_bf16 v[40:43],a[100:103],v[60:63],v[40:43]
s_cmp_eq_u32 vcc_lo,-1
v_mul_f32 v30,v30,v78
v_mul_f32 v31,v31,v79
v_mfma_f32_16x16x32_bf16 v[36:39],a[104:107],v[64:67],v[36:39]
s_cselect_b32 s66,s86,s59
ds_read_b128 v[60:63],v2 offset:53248
v_cvt_pk_bf16_f32 v84,v28,v29
v_mfma_f32_16x16x32_bf16 v[40:43],a[108:111],v[64:67],v[40:43]
s_lshr_b32 s69,s51,5
v_cvt_pk_bf16_f32 v85,v30,v31
v_fma_f32 v32,-v92,v24,v32
v_mfma_f32_16x16x32_bf16 v[36:39],a[112:115],v[68:71],v[36:39]
s_cmp_lt_u32 s51,4194304
ds_read_b128 v[64:67],v2 offset:54272
v_fma_f32 v33,-v92,v25,v33
v_mfma_f32_16x16x32_bf16 v[40:43],a[116:119],v[68:71],v[40:43]
s_cselect_b32 s68,0xffffcfff,-1
v_fma_f32 v34,-v92,v26,v34
v_fma_f32 v35,-v92,v27,v35
v_mfma_f32_16x16x32_bf16 v[36:39],a[120:123],v[72:75],v[36:39]
s_and_b32 s68,s68,s69
ds_read_b128 v[68:71],v2 offset:55296
v_mul_f32 v80,v32,v32
v_mfma_f32_16x16x32_bf16 v[40:43],a[124:127],v[72:75],v[40:43]
s_waitcnt lgkmcnt(3)
v_mul_f32 v81,v33,v33
v_mul_f32 v82,v34,v34
v_mfma_f32_16x16x32_bf16 v[36:39],a[128:131],v[44:47],v[36:39]
s_cmp_lt_u32 s51,s89
ds_read_b128 v[72:75],v2 offset:56320
v_mul_f32 v83,v35,v35
v_mfma_f32_16x16x32_bf16 v[40:43],a[132:135],v[44:47],v[40:43]
s_cselect_b32 s68,s59,s68
v_fma_f32 v80,v94,v80,v93
v_mul_f32 v80,v32,v80
v_mfma_f32_16x16x32_bf16 v[104:107],v[100:103],v[44:47],v[104:107]
s_lshr_b32 s69,s52,5
v_exp_f32 v80,v80
v_mfma_f32_16x16x32_bf16 v[108:111],v[44:47],v[44:47],v[108:111]
s_cmp_lt_u32 s52,4194304
v_fma_f32 v80,v80,v95,v95
v_fma_f32 v81,v94,v81,v93
v_mfma_f32_16x16x32_bf16 v[36:39],a[136:139],v[48:51],v[36:39]
s_cselect_b32 s70,0xffffcfff,-1
ds_read_b128 v[44:47],v2 offset:57344
v_mul_f32 v81,v33,v81)"
R"(
v_mfma_f32_16x16x32_bf16 v[40:43],a[140:143],v[48:51],v[40:43]
s_and_b32 s70,s70,s69
v_exp_f32 v81,v81
v_mfma_f32_16x16x32_bf16 v[104:107],v[100:103],v[48:51],v[104:107]
s_cmp_lt_u32 s52,s89
v_rcp_f32 v80,v80
v_mfma_f32_16x16x32_bf16 v[108:111],v[48:51],v[48:51],v[108:111]
s_cselect_b32 s70,s59,s70
v_fma_f32 v81,v81,v95,v95
v_fma_f32 v82,v94,v82,v93
v_mfma_f32_16x16x32_bf16 v[36:39],a[144:147],v[52:55],v[36:39]
s_add_u32 s74,s53,s55
ds_read_b128 v[48:51],v2 offset:58368
v_mul_f32 v82,v34,v82
v_mfma_f32_16x16x32_bf16 v[40:43],a[148:151],v[52:55],v[40:43]
s_cmp_eq_u32 s74,s56
v_exp_f32 v82,v82
v_mfma_f32_16x16x32_bf16 v[104:107],v[100:103],v[52:55],v[104:107]
s_cselect_b32 s74,s57,s74
v_rcp_f32 v81,v81
v_mfma_f32_16x16x32_bf16 v[108:111],v[52:55],v[52:55],v[108:111]
s_cmp_eq_u32 s74,s58
v_fma_f32 v82,v82,v95,v95
v_fma_f32 v83,v94,v83,v93
v_mfma_f32_16x16x32_bf16 v[36:39],a[152:155],v[56:59],v[36:39]
s_cselect_b32 s74,s59,s74
ds_read_b128 v[52:55],v2 offset:59392
v_mul_f32 v83,v35,v83
v_mfma_f32_16x16x32_bf16 v[40:43],a[156:159],v[56:59],v[40:43]
s_xor_b32 s64,s64,65536
v_exp_f32 v83,v83
v_mfma_f32_16x16x32_bf16 v[104:107],v[100:103],v[56:59],v[104:107]
s_cmp_ge_u32 s66,s59
v_rcp_f32 v82,v82
v_mfma_f32_16x16x32_bf16 v[108:111],v[56:59],v[56:59],v[108:111]
s_waitcnt lgkmcnt(3)
v_fma_f32 v83,v83,v95,v95
v_mul_f32 v32,v32,v80
v_mfma_f32_16x16x32_bf16 v[36:39],a[160:163],v[60:63],v[36:39]
s_cselect_b32 s68,s68,s70
ds_read_b128 v[56:59],v2 offset:60416
v_mul_f32 v33,v33,v81
v_mfma_f32_16x16x32_bf16 v[40:43],a[164:167],v[60:63],v[40:43]
s_add_u32 m0,s78,0
v_rcp_f32 v83,v83
v_mfma_f32_16x16x32_bf16 v[36:39],a[168:171],v[64:67],v[36:39]
buffer_load_dword v6,s[36:39],s68 offen offset:0 sc1 lds
v_cndmask_b32 v112,v108,v109,s[82:83]
v_cndmask_b32 v113,v110,v111,s[82:83]
v_cndmask_b32 v112,v112,v113,s[84:85]
v_cndmask_b32 v112,v104,v112,s[98:99]
v_mfma_f32_16x16x32_bf16 v[40:43],a[172:175],v[64:67],v[40:43]
s_mov_b32 m0,s62
ds_read_b128 v[60:63],v2 offset:61440
ds_write_b32 v13,v112 offset:2048
v_mfma_f32_16x16x32_bf16 v[36:39],a[176:179],v[68:71],v[36:39]
buffer_load_dwordx4 v0,s[36:39],s66 offen offset:0 sc1 lds
v_mfma_f32_16x16x32_bf16 v[40:43],a[180:183],v[68:71],v[40:43]
s_mov_b64 s[50:51],s[52:53]
ds_read_b128 v[64:67],v2 offset:62464
v_mul_f32 v34,v34,v82
v_mfma_f32_16x16x32_bf16 v[36:39],a[184:187],v[72:75],v[36:39]
s_mov_b32 s52,s74
ds_read_b128 v[68:71],v2 offset:63488
v_mul_f32 v35,v35,v83
v_mfma_f32_16x16x32_bf16 v[40:43],a[188:191],v[72:75],v[40:43]
s_waitcnt lgkmcnt(4)
v_cvt_pk_bf16_f32 v86,v32,v33
v_cvt_pk_bf16_f32 v87,v34,v35
v_mfma_f32_16x16x32_bf16 v[36:39],a[192:195],v[44:47],v[36:39]
s_waitcnt vmcnt(7) lgkmcnt(2)
ds_read_b128 v[72:75],v2 offset:64512
v_xor_b32 v2,65536,v2
v_mfma_f32_16x16x32_bf16 v[40:43],a[196:199],v[44:47],v[40:43]
s_barrier
v_mfma_f32_16x16x32_bf16 v[36:39],a[200:203],v[48:51],v[36:39]
buffer_load_dwordx4 v0,s[36:39],s66 offen offset:1024 sc1 lds
v_mfma_f32_16x16x32_bf16 v[40:43],a[204:207],v[48:51],v[40:43]
s_cmp_lt_u32 s67,4194304
ds_read_b128 v[44:47],v2 offset:0
ds_read_b128 v[116:119],v14 offset:2048
v_mfma_f32_16x16x32_bf16 v[36:39],a[208:211],v[52:55],v[36:39]
ds_read_b128 v[120:123],v14 offset:2064
ds_read_b128 v[48:51],v2 offset:1024
v_mfma_f32_16x16x32_bf16 v[40:43],a[212:215],v[52:55],v[40:43]
v_xor_b32 v8,16384,v8
v_mfma_f32_16x16x32_bf16 v[36:39],a[216:219],v[56:59],v[36:39]
buffer_load_dwordx4 v0,s[36:39],s66 offen offset:2048 sc1 lds
v_mfma_f32_16x16x32_bf16 v[40:43],a[220:223],v[56:59],v[40:43]
s_waitcnt lgkmcnt(4)
ds_read_b128 v[52:55],v2 offset:2048
v_mfma_f32_16x16x32_bf16 v[36:39],a[224:227],v[60:63],v[36:39]
s_waitcnt lgkmcnt(2)
ds_read_b128 v[56:59],v2 offset:3072
v_add_f32 v116,v116,v117
v_mfma_f32_16x16x32_bf16 v[40:43],a[228:231],v[60:63],v[40:43]
v_add_f32 v118,v118,v119
v_add_f32 v116,v116,v118
v_mfma_f32_16x16x32_bf16 v[36:39],a[232:235],v[64:67],v[36:39]
buffer_load_dwordx4 v0,s[36:39],s66 offen offset:3072 sc1 lds
v_mfma_f32_16x16x32_bf16 v[40:43],a[236:239],v[64:67],v[40:43]
ds_read_b128 v[60:63],v2 offset:4096
v_mul_f32 v96,s75,v116
v_mfma_f32_16x16x32_bf16 v[36:39],a[240:243],v[68:71],v[36:39]
ds_read_b128 v[64:67],v2 offset:5120
v_add_f32 v120,v120,v121
v_mfma_f32_16x16x32_bf16 v[40:43],a[244:247],v[68:71],v[40:43]
v_add_f32 v122,v122,v123
v_add_f32 v120,v120,v122
v_mul_f32 v120,s75,v120
v_fma_f32 v120,-v96,v96,v120
v_max_f32 v120,0,v120
v_add_f32 v115,v15,v120
v_fma_f32 v120,v15,v115,v120
v_rsq_f32 v116,v120
v_mfma_f32_16x16x32_bf16 v[36:39],a[248:251],v[72:75],v[36:39]
s_waitcnt lgkmcnt(2)
ds_read_b128 v[68:71],v2 offset:6144
buffer_store_dwordx4 v[84:87],v3,s[36:39],s73 offen offset:0 sc0 sc1
v_mul_f32 v97,s76,v116
v_mul_f32 v98,v116,v116
v_mul_f32 v99,v120,v116
v_mul_f32 v98,s77,v98
v_mul_f32 v98,v98,v97
v_mfma_f32_16x16x32_bf16 v[40:43],a[252:255],v[72:75],v[40:43]
s_cbranch_scc1 Lg1_a0w0_%=
s_cmp_ge_u32 s48,4194304
s_cselect_b64 s[90:91],s[94:95],s[90:91]
s_cselect_b64 s[92:93],s[96:97],s[92:93]
s_cmp_lt_u32 s67,s59
s_cbranch_scc1 Lg1_d0w0_%=
s_branch Lg1_slow0_%=
Lg1_a0w1_%=:
v_mfma_f32_16x16x32_bf16 v[28:31],a[32:35],v[44:47],0
s_add_u32 m0,s62,16384
ds_read_b128 v[72:75],v2 offset:3072
s_sub_u32 s69,s66,1048576
v_mfma_f32_16x16x32_bf16 v[32:35],a[36:39],v[44:47],0
s_cmp_lt_u32 s69,2097152
s_cselect_b32 s89,3145728,s89
s_sub_u32 s69,s66,3145728
v_mfma_f32_16x16x32_bf16 v[104:107],v[100:103],v[44:47],0
buffer_load_dwordx4 v1,s[36:39],s66 offen offset:0 sc1 lds
v_mfma_f32_16x16x32_bf16 v[108:111],v[44:47],v[44:47],0
s_cmp_lt_u32 s69,1048576
v_fma_f32 v36,-v96,v20,v36
v_fma_f32 v37,-v96,v21,v37
v_mfma_f32_16x16x32_bf16 v[28:31],a[40:43],v[48:51],v[28:31]
s_cselect_b32 s89,4194304,s89
ds_read_b128 v[44:47],v2 offset:12288
v_fma_f32 v38,-v96,v22,v38
v_mfma_f32_16x16x32_bf16 v[32:35],a[44:47],v[48:51],v[32:35]
s_lshr_b32 s73,s47,0
v_fma_f32 v39,-v96,v23,v39
v_mul_f32 v76,v36,v36
v_mfma_f32_16x16x32_bf16 v[104:107],v[100:103],v[48:51],v[104:107]
buffer_load_dwordx4 v1,s[36:39],s66 offen offset:1024 sc1 lds
v_mfma_f32_16x16x32_bf16 v[108:111],v[48:51],v[48:51],v[108:111]
s_lshr_b32 s72,s66,6
v_mul_f32 v77,v37,v37
v_mul_f32 v78,v38,v38
v_mfma_f32_16x16x32_bf16 v[28:31],a[48:51],v[52:55],v[28:31]
s_lshr_b32 s71,s62,2
ds_read_b128 v[48:51],v2 offset:13312
v_mul_f32 v79,v39,v39
v_mfma_f32_16x16x32_bf16 v[32:35],a[52:55],v[52:55],v[32:35]
s_cmp_lt_u32 s66,4194304
v_fma_f32 v76,v98,v76,v97
v_mul_f32 v76,v36,v76
v_mfma_f32_16x16x32_bf16 v[104:107],v[100:103],v[52:55],v[104:107]
buffer_load_dwordx4 v1,s[36:39],s66 offen offset:2048 sc1 lds
v_mfma_f32_16x16x32_bf16 v[108:111],v[52:55],v[52:55],v[108:111]
s_cselect_b32 s72,s59,s72
v_exp_f32 v76,v76
v_mfma_f32_16x16x32_bf16 v[28:31],a[56:59],v[56:59],v[28:31]
ds_read_b128 v[52:55],v2 offset:14336
v_fma_f32 v76,v76,v99,v99
v_mfma_f32_16x16x32_bf16 v[32:35],a[60:63],v[56:59],v[32:35]
v_rcp_f32 v76,v76
v_mfma_f32_16x16x32_bf16 v[104:107],v[100:103],v[56:59],v[104:107]
buffer_load_dwordx4 v1,s[36:39],s66 offen offset:3072 sc1 lds
v_mfma_f32_16x16x32_bf16 v[108:111],v[56:59],v[56:59],v[108:111]
s_waitcnt lgkmcnt(3)
v_fma_f32 v77,v98,v77,v97
v_mul_f32 v77,v37,v77
v_mfma_f32_16x16x32_bf16 v[28:31],a[0:3],v[60:63],v[28:31]
s_add_u32 m0,s71,s88
ds_read_b128 v[56:59],v2 offset:15360
v_fma_f32 v78,v98,v78,v97
v_mfma_f32_16x16x32_bf16 v[32:35],a[4:7],v[60:63],v[32:35]
buffer_load_dword v10,s[36:39],s72 offen offset:0 sc1 lds
v_mfma_f32_16x16x32_bf16 v[28:31],a[8:11],v[64:67],v[28:31]
s_xor_b32 s87,s64,65536
ds_read_b128 v[60:63],v2 offset:8192
v_mul_f32 v78,v38,v78
v_mfma_f32_16x16x32_bf16 v[32:35],a[12:15],v[64:67],v[32:35]
s_cmp_ge_u32 s66,s59
v_exp_f32 v77,v77
v_mfma_f32_16x16x32_bf16 v[28:31],a[16:19],v[68:71],v[28:31]
s_cselect_b32 s86,s49,s50)"
R"(
ds_read_b128 v[64:67],v2 offset:9216
v_fma_f32 v77,v77,v99,v99
v_mfma_f32_16x16x32_bf16 v[32:35],a[20:23],v[68:71],v[32:35]
s_cselect_b32 s87,s87,s63
v_exp_f32 v78,v78
v_mfma_f32_16x16x32_bf16 v[28:31],a[24:27],v[72:75],v[28:31]
s_mov_b32 s62,s87
ds_read_b128 v[68:71],v2 offset:10240
v_fma_f32 v78,v78,v99,v99
v_mfma_f32_16x16x32_bf16 v[32:35],a[28:31],v[72:75],v[32:35]
s_waitcnt lgkmcnt(3)
v_rcp_f32 v77,v77
v_mfma_f32_16x16x32_bf16 v[28:31],a[96:99],v[44:47],v[28:31]
s_waitcnt vmcnt(10)
ds_read_b128 v[72:75],v2 offset:11264
v_fma_f32 v79,v98,v79,v97
v_mfma_f32_16x16x32_bf16 v[32:35],a[100:103],v[44:47],v[32:35]
s_barrier
v_mfma_f32_16x16x32_bf16 v[28:31],a[104:107],v[48:51],v[28:31]
s_cmp_lt_u32 s86,s89
ds_read_b128 v[44:47],v2 offset:20480
ds_read_b32 v16,v7 offset:0
v_mfma_f32_16x16x32_bf16 v[32:35],a[108:111],v[48:51],v[32:35]
s_mov_b32 m0,s62
v_rcp_f32 v78,v78
v_mfma_f32_16x16x32_bf16 v[28:31],a[112:115],v[52:55],v[28:31]
ds_read_b128 v[48:51],v2 offset:21504
v_mul_f32 v79,v39,v79
v_mfma_f32_16x16x32_bf16 v[32:35],a[116:119],v[52:55],v[32:35]
v_exp_f32 v79,v79
v_mfma_f32_16x16x32_bf16 v[28:31],a[120:123],v[56:59],v[28:31]
s_waitcnt lgkmcnt(1)
ds_read_b128 v[52:55],v2 offset:22528
v_fma_f32 v79,v79,v99,v99
v_mfma_f32_16x16x32_bf16 v[32:35],a[124:127],v[56:59],v[32:35]
v_cmp_eq_u32 vcc,s60,v16
v_rcp_f32 v79,v79
v_mul_f32 v36,v36,v76
v_mfma_f32_16x16x32_bf16 v[28:31],a[64:67],v[60:63],v[28:31]
s_cselect_b32 vcc_lo,-1,vcc_lo
ds_read_b128 v[56:59],v2 offset:23552
v_mul_f32 v37,v37,v77
v_mfma_f32_16x16x32_bf16 v[32:35],a[68:71],v[60:63],v[32:35]
s_cmp_eq_u32 vcc_lo,-1
v_mul_f32 v38,v38,v78
v_mul_f32 v39,v39,v79
v_mfma_f32_16x16x32_bf16 v[28:31],a[72:75],v[64:67],v[28:31]
s_cselect_b32 s67,s86,s59
ds_read_b128 v[60:63],v2 offset:16384
v_cvt_pk_bf16_f32 v84,v36,v37
v_mfma_f32_16x16x32_bf16 v[32:35],a[76:79],v[64:67],v[32:35]
s_add_u32 s53,s52,s55
v_cvt_pk_bf16_f32 v85,v38,v39
v_fma_f32 v40,-v96,v24,v40
v_mfma_f32_16x16x32_bf16 v[28:31],a[80:83],v[68:71],v[28:31]
s_cmp_eq_u32 s53,s56
ds_read_b128 v[64:67],v2 offset:17408
v_fma_f32 v41,-v96,v25,v41
v_mfma_f32_16x16x32_bf16 v[32:35],a[84:87],v[68:71],v[32:35]
s_cselect_b32 s53,s57,s53
v_fma_f32 v42,-v96,v26,v42
v_fma_f32 v43,-v96,v27,v43
v_mfma_f32_16x16x32_bf16 v[28:31],a[88:91],v[72:75],v[28:31]
s_cmp_eq_u32 s53,s58
ds_read_b128 v[68:71],v2 offset:18432
v_mul_f32 v80,v40,v40
v_mfma_f32_16x16x32_bf16 v[32:35],a[92:95],v[72:75],v[32:35]
s_waitcnt lgkmcnt(3)
v_mul_f32 v81,v41,v41
v_mul_f32 v82,v42,v42
v_mfma_f32_16x16x32_bf16 v[28:31],a[160:163],v[44:47],v[28:31]
s_cselect_b32 s53,s59,s53
ds_read_b128 v[72:75],v2 offset:19456
v_mul_f32 v83,v43,v43
v_mfma_f32_16x16x32_bf16 v[32:35],a[164:167],v[44:47],v[32:35]
s_xor_b32 s63,s63,65536
v_fma_f32 v80,v98,v80,v97
v_mul_f32 v80,v40,v80
v_mfma_f32_16x16x32_bf16 v[104:107],v[100:103],v[44:47],v[104:107]
s_cmp_ge_u32 s66,s59
v_exp_f32 v80,v80
v_mfma_f32_16x16x32_bf16 v[108:111],v[44:47],v[44:47],v[108:111]
v_fma_f32 v80,v80,v99,v99
v_fma_f32 v81,v98,v81,v97
v_mfma_f32_16x16x32_bf16 v[28:31],a[168:171],v[48:51],v[28:31]
ds_read_b128 v[44:47],v2 offset:28672
v_mul_f32 v81,v41,v81
v_mfma_f32_16x16x32_bf16 v[32:35],a[172:175],v[48:51],v[32:35]
v_exp_f32 v81,v81
v_mfma_f32_16x16x32_bf16 v[104:107],v[100:103],v[48:51],v[104:107]
v_rcp_f32 v80,v80
v_mfma_f32_16x16x32_bf16 v[108:111],v[48:51],v[48:51],v[108:111]
v_fma_f32 v81,v81,v99,v99
v_fma_f32 v82,v98,v82,v97
v_mfma_f32_16x16x32_bf16 v[28:31],a[176:179],v[52:55],v[28:31]
ds_read_b128 v[48:51],v2 offset:29696
v_mul_f32 v82,v42,v82
v_mfma_f32_16x16x32_bf16 v[32:35],a[180:183],v[52:55],v[32:35]
v_exp_f32 v82,v82
v_mfma_f32_16x16x32_bf16 v[104:107],v[100:103],v[52:55],v[104:107]
v_rcp_f32 v81,v81
v_mfma_f32_16x16x32_bf16 v[108:111],v[52:55],v[52:55],v[108:111]
v_fma_f32 v82,v82,v99,v99
v_fma_f32 v83,v98,v83,v97
v_mfma_f32_16x16x32_bf16 v[28:31],a[184:187],v[56:59],v[28:31]
ds_read_b128 v[52:55],v2 offset:30720
v_mul_f32 v83,v43,v83
v_mfma_f32_16x16x32_bf16 v[32:35],a[188:191],v[56:59],v[32:35]
v_exp_f32 v83,v83
v_mfma_f32_16x16x32_bf16 v[104:107],v[100:103],v[56:59],v[104:107]
v_rcp_f32 v82,v82
v_mfma_f32_16x16x32_bf16 v[108:111],v[56:59],v[56:59],v[108:111]
s_waitcnt lgkmcnt(3)
v_fma_f32 v83,v83,v99,v99
v_mul_f32 v40,v40,v80
v_mfma_f32_16x16x32_bf16 v[28:31],a[128:131],v[60:63],v[28:31]
ds_read_b128 v[56:59],v2 offset:31744
v_mul_f32 v41,v41,v81
v_mfma_f32_16x16x32_bf16 v[32:35],a[132:135],v[60:63],v[32:35]
buffer_load_dwordx4 v0,s[36:39],s67 offen offset:0 sc1 lds
v_mfma_f32_16x16x32_bf16 v[28:31],a[136:139],v[64:67],v[28:31]
v_cndmask_b32 v112,v108,v109,s[82:83]
v_cndmask_b32 v113,v110,v111,s[82:83]
v_cndmask_b32 v112,v112,v113,s[84:85]
v_cndmask_b32 v112,v104,v112,s[98:99]
v_mfma_f32_16x16x32_bf16 v[32:35],a[140:143],v[64:67],v[32:35]
ds_read_b128 v[60:63],v2 offset:24576
ds_write_b32 v13,v112 offset:0
v_mfma_f32_16x16x32_bf16 v[28:31],a[144:147],v[68:71],v[28:31]
ds_read_b128 v[64:67],v2 offset:25600
v_mul_f32 v42,v42,v82
v_mfma_f32_16x16x32_bf16 v[32:35],a[148:151],v[68:71],v[32:35]
buffer_load_dwordx4 v0,s[36:39],s67 offen offset:1024 sc1 lds
v_mfma_f32_16x16x32_bf16 v[28:31],a[152:155],v[72:75],v[28:31]
ds_read_b128 v[68:71],v2 offset:26624
v_cvt_pk_bf16_f32 v86,v40,v41
v_mfma_f32_16x16x32_bf16 v[32:35],a[156:159],v[72:75],v[32:35]
s_waitcnt lgkmcnt(4)
v_rcp_f32 v83,v83
v_mfma_f32_16x16x32_bf16 v[28:31],a[224:227],v[44:47],v[28:31]
s_waitcnt vmcnt(7) lgkmcnt(2)
ds_read_b128 v[72:75],v2 offset:27648
v_mul_f32 v43,v43,v83
v_mfma_f32_16x16x32_bf16 v[32:35],a[228:231],v[44:47],v[32:35]
s_barrier
v_mfma_f32_16x16x32_bf16 v[28:31],a[232:235],v[48:51],v[28:31]
ds_read_b128 v[116:119],v14 offset:0
ds_read_b128 v[120:123],v14 offset:16
v_mfma_f32_16x16x32_bf16 v[32:35],a[236:239],v[48:51],v[32:35]
ds_read_b128 v[44:47],v2 offset:36864
buffer_load_dwordx4 v0,s[36:39],s67 offen offset:2048 sc1 lds
v_mfma_f32_16x16x32_bf16 v[28:31],a[240:243],v[52:55],v[28:31]
ds_read_b128 v[48:51],v2 offset:37888
v_cvt_pk_bf16_f32 v87,v42,v43
v_mfma_f32_16x16x32_bf16 v[32:35],a[244:247],v[52:55],v[32:35]
v_mfma_f32_16x16x32_bf16 v[28:31],a[248:251],v[56:59],v[28:31]
s_waitcnt lgkmcnt(2)
ds_read_b128 v[52:55],v2 offset:38912
v_add_f32 v116,v116,v117
v_mfma_f32_16x16x32_bf16 v[32:35],a[252:255],v[56:59],v[32:35]
buffer_load_dwordx4 v0,s[36:39],s67 offen offset:3072 sc1 lds
v_mfma_f32_16x16x32_bf16 v[28:31],a[192:195],v[60:63],v[28:31]
ds_read_b128 v[56:59],v2 offset:39936
v_add_f32 v118,v118,v119
v_mfma_f32_16x16x32_bf16 v[32:35],a[196:199],v[60:63],v[32:35]
v_add_f32 v116,v116,v118
v_mul_f32 v92,s75,v116
v_mfma_f32_16x16x32_bf16 v[28:31],a[200:203],v[64:67],v[28:31]
ds_read_b128 v[60:63],v2 offset:32768
v_add_f32 v120,v120,v121
v_mfma_f32_16x16x32_bf16 v[32:35],a[204:207],v[64:67],v[32:35]
buffer_store_dwordx4 v[84:87],v3,s[36:39],s73 offen offset:0 sc0 sc1
v_add_f32 v122,v122,v123
v_add_f32 v120,v120,v122
v_mul_f32 v120,s75,v120
v_mfma_f32_16x16x32_bf16 v[28:31],a[208:211],v[68:71],v[28:31]
ds_read_b128 v[64:67],v2 offset:33792
v_fma_f32 v120,-v92,v92,v120
v_mfma_f32_16x16x32_bf16 v[32:35],a[212:215],v[68:71],v[32:35]
v_max_f32 v120,0,v120
v_add_f32 v115,v15,v120
v_fma_f32 v120,v15,v115,v120
v_rsq_f32 v116,v120
v_mfma_f32_16x16x32_bf16 v[28:31],a[216:219],v[72:75],v[28:31]
s_waitcnt lgkmcnt(2)
ds_read_b128 v[68:71],v2 offset:34816
v_mul_f32 v93,s76,v116
v_mul_f32 v94,v116,v116
v_mul_f32 v95,v120,v116
v_mul_f32 v94,s77,v94
v_mul_f32 v94,v94,v93
v_mfma_f32_16x16x32_bf16 v[32:35],a[220:223],v[72:75],v[32:35]
s_cbranch_scc1 Lg1_slow1_%=
Lg1_a1w1_%=:
v_mfma_f32_16x16x32_bf16 v[36:39],a[32:35],v[44:47],0
s_add_u32 m0,s62,16384
ds_read_b128 v[72:75],v2 offset:35840
s_sub_u32 s69,s67,1048576
v_mfma_f32_16x16x32_bf16 v[40:43],a[36:39],v[44:47],0
s_cmp_lt_u32 s69,2097152)"
R"(
s_cselect_b32 s89,3145728,s89
s_sub_u32 s69,s67,3145728
v_mfma_f32_16x16x32_bf16 v[104:107],v[100:103],v[44:47],0
buffer_load_dwordx4 v1,s[36:39],s67 offen offset:0 sc1 lds
v_mfma_f32_16x16x32_bf16 v[108:111],v[44:47],v[44:47],0
s_cmp_lt_u32 s69,1048576
v_fma_f32 v28,-v92,v20,v28
v_fma_f32 v29,-v92,v21,v29
v_mfma_f32_16x16x32_bf16 v[36:39],a[40:43],v[48:51],v[36:39]
s_cselect_b32 s89,4194304,s89
ds_read_b128 v[44:47],v2 offset:45056
v_fma_f32 v30,-v92,v22,v30
v_mfma_f32_16x16x32_bf16 v[40:43],a[44:47],v[48:51],v[40:43]
s_lshr_b32 s73,s48,0
v_fma_f32 v31,-v92,v23,v31
v_mul_f32 v76,v28,v28
v_mfma_f32_16x16x32_bf16 v[104:107],v[100:103],v[48:51],v[104:107]
buffer_load_dwordx4 v1,s[36:39],s67 offen offset:1024 sc1 lds
v_mfma_f32_16x16x32_bf16 v[108:111],v[48:51],v[48:51],v[108:111]
s_lshr_b32 s72,s67,6
v_mul_f32 v77,v29,v29
v_mul_f32 v78,v30,v30
v_mfma_f32_16x16x32_bf16 v[36:39],a[48:51],v[52:55],v[36:39]
s_lshr_b32 s71,s62,2
ds_read_b128 v[48:51],v2 offset:46080
v_mul_f32 v79,v31,v31
v_mfma_f32_16x16x32_bf16 v[40:43],a[52:55],v[52:55],v[40:43]
s_cmp_lt_u32 s67,4194304
v_fma_f32 v76,v94,v76,v93
v_mul_f32 v76,v28,v76
v_mfma_f32_16x16x32_bf16 v[104:107],v[100:103],v[52:55],v[104:107]
buffer_load_dwordx4 v1,s[36:39],s67 offen offset:2048 sc1 lds
v_mfma_f32_16x16x32_bf16 v[108:111],v[52:55],v[52:55],v[108:111]
s_cselect_b32 s72,s59,s72
v_exp_f32 v76,v76
v_mfma_f32_16x16x32_bf16 v[36:39],a[56:59],v[56:59],v[36:39]
s_mov_b64 s[44:45],s[46:47]
ds_read_b128 v[52:55],v2 offset:47104
v_fma_f32 v76,v76,v95,v95
v_mfma_f32_16x16x32_bf16 v[40:43],a[60:63],v[56:59],v[40:43]
s_mov_b64 s[46:47],s[48:49]
v_rcp_f32 v76,v76
v_mfma_f32_16x16x32_bf16 v[104:107],v[100:103],v[56:59],v[104:107]
buffer_load_dwordx4 v1,s[36:39],s67 offen offset:3072 sc1 lds
v_mfma_f32_16x16x32_bf16 v[108:111],v[56:59],v[56:59],v[108:111]
s_waitcnt lgkmcnt(3)
v_fma_f32 v77,v94,v77,v93
v_mul_f32 v77,v29,v77
v_mfma_f32_16x16x32_bf16 v[36:39],a[0:3],v[60:63],v[36:39]
s_add_u32 m0,s71,s88
ds_read_b128 v[56:59],v2 offset:48128
v_fma_f32 v78,v94,v78,v93
v_mfma_f32_16x16x32_bf16 v[40:43],a[4:7],v[60:63],v[40:43]
buffer_load_dword v10,s[36:39],s72 offen offset:0 sc1 lds
v_mfma_f32_16x16x32_bf16 v[36:39],a[8:11],v[64:67],v[36:39]
s_xor_b32 s87,s63,65536
ds_read_b128 v[60:63],v2 offset:40960
v_mul_f32 v78,v30,v78
v_mfma_f32_16x16x32_bf16 v[40:43],a[12:15],v[64:67],v[40:43]
s_cmp_ge_u32 s67,s59
v_exp_f32 v77,v77
v_mfma_f32_16x16x32_bf16 v[36:39],a[16:19],v[68:71],v[36:39]
s_cselect_b32 s86,s50,s51
ds_read_b128 v[64:67],v2 offset:41984
v_fma_f32 v77,v77,v95,v95
v_mfma_f32_16x16x32_bf16 v[40:43],a[20:23],v[68:71],v[40:43]
s_cselect_b32 s87,s87,s64
v_exp_f32 v78,v78
v_mfma_f32_16x16x32_bf16 v[36:39],a[24:27],v[72:75],v[36:39]
s_mov_b32 s62,s87
ds_read_b128 v[68:71],v2 offset:43008
v_fma_f32 v78,v78,v95,v95
v_mfma_f32_16x16x32_bf16 v[40:43],a[28:31],v[72:75],v[40:43]
s_waitcnt lgkmcnt(3)
v_rcp_f32 v77,v77
v_mfma_f32_16x16x32_bf16 v[36:39],a[96:99],v[44:47],v[36:39]
s_waitcnt vmcnt(10)
ds_read_b128 v[72:75],v2 offset:44032
v_fma_f32 v79,v94,v79,v93
v_mfma_f32_16x16x32_bf16 v[40:43],a[100:103],v[44:47],v[40:43]
s_barrier
v_mfma_f32_16x16x32_bf16 v[36:39],a[104:107],v[48:51],v[36:39]
s_cmp_lt_u32 s86,s89
ds_read_b128 v[44:47],v2 offset:53248
ds_read_b32 v16,v7 offset:256
v_mfma_f32_16x16x32_bf16 v[40:43],a[108:111],v[48:51],v[40:43]
s_mov_b32 m0,s62
v_rcp_f32 v78,v78
v_mfma_f32_16x16x32_bf16 v[36:39],a[112:115],v[52:55],v[36:39]
s_mov_b64 s[48:49],s[50:51]
ds_read_b128 v[48:51],v2 offset:54272
v_mul_f32 v79,v31,v79
v_mfma_f32_16x16x32_bf16 v[40:43],a[116:119],v[52:55],v[40:43]
s_mov_b64 s[50:51],s[52:53]
v_exp_f32 v79,v79
v_mfma_f32_16x16x32_bf16 v[36:39],a[120:123],v[56:59],v[36:39]
s_waitcnt lgkmcnt(1)
ds_read_b128 v[52:55],v2 offset:55296
v_fma_f32 v79,v79,v95,v95
v_mfma_f32_16x16x32_bf16 v[40:43],a[124:127],v[56:59],v[40:43]
v_cmp_eq_u32 vcc,s60,v16
v_rcp_f32 v79,v79
v_mul_f32 v28,v28,v76
v_mfma_f32_16x16x32_bf16 v[36:39],a[64:67],v[60:63],v[36:39]
s_cselect_b32 vcc_lo,-1,vcc_lo
ds_read_b128 v[56:59],v2 offset:56320
v_mul_f32 v29,v29,v77
v_mfma_f32_16x16x32_bf16 v[40:43],a[68:71],v[60:63],v[40:43]
s_cmp_eq_u32 vcc_lo,-1
v_mul_f32 v30,v30,v78
v_mul_f32 v31,v31,v79
v_mfma_f32_16x16x32_bf16 v[36:39],a[72:75],v[64:67],v[36:39]
s_cselect_b32 s66,s86,s59
ds_read_b128 v[60:63],v2 offset:49152
v_cvt_pk_bf16_f32 v84,v28,v29
v_mfma_f32_16x16x32_bf16 v[40:43],a[76:79],v[64:67],v[40:43]
s_add_u32 s74,s53,s55
v_cvt_pk_bf16_f32 v85,v30,v31
v_fma_f32 v32,-v92,v24,v32
v_mfma_f32_16x16x32_bf16 v[36:39],a[80:83],v[68:71],v[36:39]
s_cmp_eq_u32 s74,s56
ds_read_b128 v[64:67],v2 offset:50176
v_fma_f32 v33,-v92,v25,v33
v_mfma_f32_16x16x32_bf16 v[40:43],a[84:87],v[68:71],v[40:43]
s_cselect_b32 s74,s57,s74
v_fma_f32 v34,-v92,v26,v34
v_fma_f32 v35,-v92,v27,v35
v_mfma_f32_16x16x32_bf16 v[36:39],a[88:91],v[72:75],v[36:39]
s_cmp_eq_u32 s74,s58
ds_read_b128 v[68:71],v2 offset:51200
v_mul_f32 v80,v32,v32
v_mfma_f32_16x16x32_bf16 v[40:43],a[92:95],v[72:75],v[40:43]
s_waitcnt lgkmcnt(3)
v_mul_f32 v81,v33,v33
v_mul_f32 v82,v34,v34
v_mfma_f32_16x16x32_bf16 v[36:39],a[160:163],v[44:47],v[36:39]
s_cselect_b32 s74,s59,s74
ds_read_b128 v[72:75],v2 offset:52224
v_mul_f32 v83,v35,v35
v_mfma_f32_16x16x32_bf16 v[40:43],a[164:167],v[44:47],v[40:43]
s_xor_b32 s64,s64,65536
v_fma_f32 v80,v94,v80,v93
v_mul_f32 v80,v32,v80
v_mfma_f32_16x16x32_bf16 v[104:107],v[100:103],v[44:47],v[104:107]
s_mov_b32 s52,s74
v_exp_f32 v80,v80
v_mfma_f32_16x16x32_bf16 v[108:111],v[44:47],v[44:47],v[108:111]
s_cmp_lt_u32 s67,4194304
v_fma_f32 v80,v80,v95,v95
v_fma_f32 v81,v94,v81,v93
v_mfma_f32_16x16x32_bf16 v[36:39],a[168:171],v[48:51],v[36:39]
ds_read_b128 v[44:47],v2 offset:61440
v_mul_f32 v81,v33,v81
v_mfma_f32_16x16x32_bf16 v[40:43],a[172:175],v[48:51],v[40:43]
v_exp_f32 v81,v81
v_mfma_f32_16x16x32_bf16 v[104:107],v[100:103],v[48:51],v[104:107]
v_rcp_f32 v80,v80
v_mfma_f32_16x16x32_bf16 v[108:111],v[48:51],v[48:51],v[108:111]
v_fma_f32 v81,v81,v95,v95
v_fma_f32 v82,v94,v82,v93
v_mfma_f32_16x16x32_bf16 v[36:39],a[176:179],v[52:55],v[36:39]
ds_read_b128 v[48:51],v2 offset:62464
v_mul_f32 v82,v34,v82
v_mfma_f32_16x16x32_bf16 v[40:43],a[180:183],v[52:55],v[40:43]
v_exp_f32 v82,v82
v_mfma_f32_16x16x32_bf16 v[104:107],v[100:103],v[52:55],v[104:107]
v_rcp_f32 v81,v81
v_mfma_f32_16x16x32_bf16 v[108:111],v[52:55],v[52:55],v[108:111]
v_fma_f32 v82,v82,v95,v95
v_fma_f32 v83,v94,v83,v93
v_mfma_f32_16x16x32_bf16 v[36:39],a[184:187],v[56:59],v[36:39]
ds_read_b128 v[52:55],v2 offset:63488
v_mul_f32 v83,v35,v83
v_mfma_f32_16x16x32_bf16 v[40:43],a[188:191],v[56:59],v[40:43]
v_exp_f32 v83,v83
v_mfma_f32_16x16x32_bf16 v[104:107],v[100:103],v[56:59],v[104:107]
v_rcp_f32 v82,v82
v_mfma_f32_16x16x32_bf16 v[108:111],v[56:59],v[56:59],v[108:111]
s_waitcnt lgkmcnt(3)
v_fma_f32 v83,v83,v95,v95
v_mul_f32 v32,v32,v80
v_mfma_f32_16x16x32_bf16 v[36:39],a[128:131],v[60:63],v[36:39]
ds_read_b128 v[56:59],v2 offset:64512
v_mul_f32 v33,v33,v81
v_mfma_f32_16x16x32_bf16 v[40:43],a[132:135],v[60:63],v[40:43]
buffer_load_dwordx4 v0,s[36:39],s66 offen offset:0 sc1 lds
v_mfma_f32_16x16x32_bf16 v[36:39],a[136:139],v[64:67],v[36:39]
v_cndmask_b32 v112,v108,v109,s[82:83]
v_cndmask_b32 v113,v110,v111,s[82:83]
v_cndmask_b32 v112,v112,v113,s[84:85]
v_cndmask_b32 v112,v104,v112,s[98:99]
v_mfma_f32_16x16x32_bf16 v[40:43],a[140:143],v[64:67],v[40:43]
ds_read_b128 v[60:63],v2 offset:57344
ds_write_b32 v13,v112 offset:2048
v_mfma_f32_16x16x32_bf16 v[36:39],a[144:147],v[68:71],v[36:39]
ds_read_b128 v[64:67],v2 offset:58368
v_mul_f32 v34,v34,v82
v_mfma_f32_16x16x32_bf16 v[40:43],a[148:151],v[68:71],v[40:43]
buffer_load_dwordx4 v0,s[36:39],s66 offen offset:1024 sc1 lds
v_mfma_f32_16x16x32_bf16 v[36:39],a[152:155],v[72:75],v[36:39])"
R"(
ds_read_b128 v[68:71],v2 offset:59392
v_cvt_pk_bf16_f32 v86,v32,v33
v_mfma_f32_16x16x32_bf16 v[40:43],a[156:159],v[72:75],v[40:43]
s_waitcnt lgkmcnt(4)
v_rcp_f32 v83,v83
v_mfma_f32_16x16x32_bf16 v[36:39],a[224:227],v[44:47],v[36:39]
s_waitcnt vmcnt(7) lgkmcnt(2)
ds_read_b128 v[72:75],v2 offset:60416
v_xor_b32 v2,65536,v2
v_mfma_f32_16x16x32_bf16 v[40:43],a[228:231],v[44:47],v[40:43]
s_barrier
v_mfma_f32_16x16x32_bf16 v[36:39],a[232:235],v[48:51],v[36:39]
ds_read_b128 v[116:119],v14 offset:2048
ds_read_b128 v[120:123],v14 offset:2064
v_mfma_f32_16x16x32_bf16 v[40:43],a[236:239],v[48:51],v[40:43]
ds_read_b128 v[44:47],v2 offset:4096
buffer_load_dwordx4 v0,s[36:39],s66 offen offset:2048 sc1 lds
v_mfma_f32_16x16x32_bf16 v[36:39],a[240:243],v[52:55],v[36:39]
ds_read_b128 v[48:51],v2 offset:5120
v_mul_f32 v35,v35,v83
v_mfma_f32_16x16x32_bf16 v[40:43],a[244:247],v[52:55],v[40:43]
v_cvt_pk_bf16_f32 v87,v34,v35
v_xor_b32 v8,16384,v8
v_mfma_f32_16x16x32_bf16 v[36:39],a[248:251],v[56:59],v[36:39]
s_waitcnt lgkmcnt(2)
ds_read_b128 v[52:55],v2 offset:6144
v_add_f32 v116,v116,v117
v_mfma_f32_16x16x32_bf16 v[40:43],a[252:255],v[56:59],v[40:43]
buffer_load_dwordx4 v0,s[36:39],s66 offen offset:3072 sc1 lds
v_mfma_f32_16x16x32_bf16 v[36:39],a[192:195],v[60:63],v[36:39]
ds_read_b128 v[56:59],v2 offset:7168
v_add_f32 v118,v118,v119
v_mfma_f32_16x16x32_bf16 v[40:43],a[196:199],v[60:63],v[40:43]
v_add_f32 v116,v116,v118
v_mul_f32 v96,s75,v116
v_mfma_f32_16x16x32_bf16 v[36:39],a[200:203],v[64:67],v[36:39]
ds_read_b128 v[60:63],v2 offset:0
v_add_f32 v120,v120,v121
v_mfma_f32_16x16x32_bf16 v[40:43],a[204:207],v[64:67],v[40:43]
buffer_store_dwordx4 v[84:87],v3,s[36:39],s73 offen offset:0 sc0 sc1
v_add_f32 v122,v122,v123
v_add_f32 v120,v120,v122
v_mul_f32 v120,s75,v120
v_mfma_f32_16x16x32_bf16 v[36:39],a[208:211],v[68:71],v[36:39]
ds_read_b128 v[64:67],v2 offset:1024
v_fma_f32 v120,-v96,v96,v120
v_mfma_f32_16x16x32_bf16 v[40:43],a[212:215],v[68:71],v[40:43]
v_max_f32 v120,0,v120
v_add_f32 v115,v15,v120
v_fma_f32 v120,v15,v115,v120
v_rsq_f32 v116,v120
v_mfma_f32_16x16x32_bf16 v[36:39],a[216:219],v[72:75],v[36:39]
s_waitcnt lgkmcnt(2)
ds_read_b128 v[68:71],v2 offset:2048
v_mul_f32 v97,s76,v116
v_mul_f32 v98,v116,v116
v_mul_f32 v99,v120,v116
v_mul_f32 v98,s77,v98
v_mul_f32 v98,v98,v97
v_mfma_f32_16x16x32_bf16 v[40:43],a[220:223],v[72:75],v[40:43]
s_cbranch_scc1 Lg1_a0w1_%=
s_cmp_ge_u32 s48,4194304
s_cselect_b64 s[90:91],s[94:95],s[90:91]
s_cselect_b64 s[92:93],s[96:97],s[92:93]
s_cmp_lt_u32 s67,s59
s_cbranch_scc1 Lg1_d0w1_%=
s_branch Lg1_slow0_%=
Lg1_a0w2_%=:
v_mfma_f32_16x16x32_bf16 v[28:31],a[64:67],v[44:47],0
s_sub_u32 s69,s66,1048576
ds_read_b128 v[72:75],v2 offset:15360
s_cmp_lt_u32 s69,2097152
v_mfma_f32_16x16x32_bf16 v[32:35],a[68:71],v[44:47],0
s_cselect_b32 s89,3145728,s89
s_sub_u32 s69,s66,3145728
s_cmp_lt_u32 s69,1048576
v_mfma_f32_16x16x32_bf16 v[104:107],v[100:103],v[44:47],0
s_cselect_b32 s89,4194304,s89
s_add_u32 m0,s62,16384
s_lshr_b32 s73,s47,0
v_mfma_f32_16x16x32_bf16 v[108:111],v[44:47],v[44:47],0
buffer_load_dwordx4 v1,s[36:39],s66 offen offset:0 sc1 lds
v_mfma_f32_16x16x32_bf16 v[28:31],a[72:75],v[48:51],v[28:31]
s_lshr_b32 s65,s48,6
ds_read_b128 v[44:47],v2 offset:0
v_fma_f32 v36,-v96,v20,v36
v_mfma_f32_16x16x32_bf16 v[32:35],a[76:79],v[48:51],v[32:35]
s_lshr_b32 s72,s66,6
v_fma_f32 v37,-v96,v21,v37
v_fma_f32 v38,-v96,v22,v38
v_mfma_f32_16x16x32_bf16 v[104:107],v[100:103],v[48:51],v[104:107]
s_lshr_b32 s71,s62,2
v_fma_f32 v39,-v96,v23,v39
v_mul_f32 v76,v36,v36
v_mfma_f32_16x16x32_bf16 v[108:111],v[48:51],v[48:51],v[108:111]
buffer_load_dwordx4 v1,s[36:39],s66 offen offset:1024 sc1 lds
v_mfma_f32_16x16x32_bf16 v[28:31],a[80:83],v[52:55],v[28:31]
s_cmp_lt_u32 s66,4194304
ds_read_b128 v[48:51],v2 offset:1024
v_mul_f32 v77,v37,v37
v_mfma_f32_16x16x32_bf16 v[32:35],a[84:87],v[52:55],v[32:35]
s_cselect_b32 s72,s59,s72
v_mul_f32 v78,v38,v38
v_mul_f32 v79,v39,v39
v_mfma_f32_16x16x32_bf16 v[104:107],v[100:103],v[52:55],v[104:107]
v_fma_f32 v76,v98,v76,v97
v_mul_f32 v76,v36,v76
v_mfma_f32_16x16x32_bf16 v[108:111],v[52:55],v[52:55],v[108:111]
buffer_load_dwordx4 v1,s[36:39],s66 offen offset:2048 sc1 lds
v_mfma_f32_16x16x32_bf16 v[28:31],a[88:91],v[56:59],v[28:31]
ds_read_b128 v[52:55],v2 offset:2048
v_fma_f32 v77,v98,v77,v97
v_mfma_f32_16x16x32_bf16 v[32:35],a[92:95],v[56:59],v[32:35]
v_exp_f32 v76,v76
v_mfma_f32_16x16x32_bf16 v[104:107],v[100:103],v[56:59],v[104:107]
v_mul_f32 v77,v37,v77
v_fma_f32 v76,v76,v99,v99
v_mfma_f32_16x16x32_bf16 v[108:111],v[56:59],v[56:59],v[108:111]
s_waitcnt lgkmcnt(3)
buffer_load_dwordx4 v1,s[36:39],s66 offen offset:3072 sc1 lds
v_mfma_f32_16x16x32_bf16 v[28:31],a[96:99],v[60:63],v[28:31]
s_add_u32 m0,s71,s88
ds_read_b128 v[56:59],v2 offset:3072
v_fma_f32 v78,v98,v78,v97
v_mfma_f32_16x16x32_bf16 v[32:35],a[100:103],v[60:63],v[32:35]
buffer_load_dword v10,s[36:39],s72 offen offset:0 sc1 lds
v_mfma_f32_16x16x32_bf16 v[28:31],a[104:107],v[64:67],v[28:31]
s_xor_b32 s87,s64,65536
ds_read_b128 v[60:63],v2 offset:4096
v_fma_f32 v79,v98,v79,v97
v_mfma_f32_16x16x32_bf16 v[32:35],a[108:111],v[64:67],v[32:35]
s_cmp_ge_u32 s66,s59
v_exp_f32 v77,v77
v_mfma_f32_16x16x32_bf16 v[28:31],a[112:115],v[68:71],v[28:31]
s_cselect_b32 s86,s49,s50
ds_read_b128 v[64:67],v2 offset:5120
v_mul_f32 v78,v38,v78
v_mfma_f32_16x16x32_bf16 v[32:35],a[116:119],v[68:71],v[32:35]
s_cselect_b32 s87,s87,s63
v_exp_f32 v78,v78
v_mfma_f32_16x16x32_bf16 v[28:31],a[120:123],v[72:75],v[28:31]
s_mov_b32 s62,s87
ds_read_b128 v[68:71],v2 offset:6144
v_mul_f32 v79,v39,v79
v_mfma_f32_16x16x32_bf16 v[32:35],a[124:127],v[72:75],v[32:35]
s_waitcnt lgkmcnt(3)
v_exp_f32 v79,v79
v_mfma_f32_16x16x32_bf16 v[28:31],a[0:3],v[44:47],v[28:31]
s_waitcnt vmcnt(10)
ds_read_b128 v[72:75],v2 offset:7168
v_fma_f32 v77,v77,v99,v99
v_mfma_f32_16x16x32_bf16 v[32:35],a[4:7],v[44:47],v[32:35]
s_barrier
v_mfma_f32_16x16x32_bf16 v[28:31],a[8:11],v[48:51],v[28:31]
s_cmp_lt_u32 s86,s89
ds_read_b128 v[44:47],v2 offset:24576
ds_read_b32 v16,v7 offset:0
v_mfma_f32_16x16x32_bf16 v[32:35],a[12:15],v[48:51],v[32:35]
s_mov_b32 m0,s62
v_rcp_f32 v76,v76
v_mfma_f32_16x16x32_bf16 v[28:31],a[16:19],v[52:55],v[28:31]
ds_read_b128 v[48:51],v2 offset:25600
v_fma_f32 v78,v78,v99,v99
v_mfma_f32_16x16x32_bf16 v[32:35],a[20:23],v[52:55],v[32:35]
v_rcp_f32 v77,v77
v_mfma_f32_16x16x32_bf16 v[28:31],a[24:27],v[56:59],v[28:31]
s_waitcnt lgkmcnt(1)
ds_read_b128 v[52:55],v2 offset:26624
v_fma_f32 v79,v79,v99,v99
v_mfma_f32_16x16x32_bf16 v[32:35],a[28:31],v[56:59],v[32:35]
v_cmp_eq_u32 vcc,s60,v16
v_rcp_f32 v78,v78
v_mul_f32 v36,v36,v76
v_mfma_f32_16x16x32_bf16 v[28:31],a[32:35],v[60:63],v[28:31]
s_cselect_b32 vcc_lo,-1,vcc_lo
ds_read_b128 v[56:59],v2 offset:27648
v_mul_f32 v37,v37,v77
v_mfma_f32_16x16x32_bf16 v[32:35],a[36:39],v[60:63],v[32:35]
s_cmp_eq_u32 vcc_lo,-1
v_rcp_f32 v79,v79
v_mfma_f32_16x16x32_bf16 v[28:31],a[40:43],v[64:67],v[28:31]
s_cselect_b32 s67,s86,s59
ds_read_b128 v[60:63],v2 offset:28672
v_mul_f32 v38,v38,v78
v_mfma_f32_16x16x32_bf16 v[32:35],a[44:47],v[64:67],v[32:35]
s_add_u32 s53,s52,s55
v_mul_f32 v39,v39,v79
v_cvt_pk_bf16_f32 v84,v36,v37
v_mfma_f32_16x16x32_bf16 v[28:31],a[48:51],v[68:71],v[28:31]
s_cmp_eq_u32 s53,s56
ds_read_b128 v[64:67],v2 offset:29696
v_cvt_pk_bf16_f32 v85,v38,v39
v_mfma_f32_16x16x32_bf16 v[32:35],a[52:55],v[68:71],v[32:35]
s_cselect_b32 s53,s57,s53
v_fma_f32 v40,-v96,v24,v40
v_fma_f32 v41,-v96,v25,v41
v_mfma_f32_16x16x32_bf16 v[28:31],a[56:59],v[72:75],v[28:31]
s_cmp_eq_u32 s53,s58
ds_read_b128 v[68:71],v2 offset:30720
v_fma_f32 v42,-v96,v26,v42)"
R"(
v_mfma_f32_16x16x32_bf16 v[32:35],a[60:63],v[72:75],v[32:35]
s_waitcnt lgkmcnt(3)
v_fma_f32 v43,-v96,v27,v43
v_mul_f32 v80,v40,v40
v_mfma_f32_16x16x32_bf16 v[28:31],a[192:195],v[44:47],v[28:31]
s_cselect_b32 s53,s59,s53
ds_read_b128 v[72:75],v2 offset:31744
v_mul_f32 v81,v41,v41
v_mfma_f32_16x16x32_bf16 v[32:35],a[196:199],v[44:47],v[32:35]
s_xor_b32 s63,s63,65536
v_mul_f32 v82,v42,v42
v_mul_f32 v83,v43,v43
v_mfma_f32_16x16x32_bf16 v[104:107],v[100:103],v[44:47],v[104:107]
s_cmp_ge_u32 s66,s59
v_fma_f32 v80,v98,v80,v97
v_mul_f32 v80,v40,v80
v_mfma_f32_16x16x32_bf16 v[108:111],v[44:47],v[44:47],v[108:111]
v_exp_f32 v80,v80
v_mfma_f32_16x16x32_bf16 v[28:31],a[200:203],v[48:51],v[28:31]
ds_read_b128 v[44:47],v2 offset:16384
v_fma_f32 v80,v80,v99,v99
v_mfma_f32_16x16x32_bf16 v[32:35],a[204:207],v[48:51],v[32:35]
v_rcp_f32 v80,v80
v_mfma_f32_16x16x32_bf16 v[104:107],v[100:103],v[48:51],v[104:107]
v_fma_f32 v81,v98,v81,v97
v_mul_f32 v81,v41,v81
v_mfma_f32_16x16x32_bf16 v[108:111],v[48:51],v[48:51],v[108:111]
v_exp_f32 v81,v81
v_mfma_f32_16x16x32_bf16 v[28:31],a[208:211],v[52:55],v[28:31]
ds_read_b128 v[48:51],v2 offset:17408
v_fma_f32 v81,v81,v99,v99
v_mfma_f32_16x16x32_bf16 v[32:35],a[212:215],v[52:55],v[32:35]
v_rcp_f32 v81,v81
v_mfma_f32_16x16x32_bf16 v[104:107],v[100:103],v[52:55],v[104:107]
v_fma_f32 v82,v98,v82,v97
v_mul_f32 v82,v42,v82
v_mfma_f32_16x16x32_bf16 v[108:111],v[52:55],v[52:55],v[108:111]
v_exp_f32 v82,v82
v_mfma_f32_16x16x32_bf16 v[28:31],a[216:219],v[56:59],v[28:31]
ds_read_b128 v[52:55],v2 offset:18432
v_fma_f32 v82,v82,v99,v99
v_mfma_f32_16x16x32_bf16 v[32:35],a[220:223],v[56:59],v[32:35]
v_rcp_f32 v82,v82
v_mfma_f32_16x16x32_bf16 v[104:107],v[100:103],v[56:59],v[104:107]
v_fma_f32 v83,v98,v83,v97
v_mul_f32 v83,v43,v83
v_mfma_f32_16x16x32_bf16 v[108:111],v[56:59],v[56:59],v[108:111]
s_waitcnt lgkmcnt(3)
v_exp_f32 v83,v83
v_mfma_f32_16x16x32_bf16 v[28:31],a[224:227],v[60:63],v[28:31]
ds_read_b128 v[56:59],v2 offset:19456
v_fma_f32 v83,v83,v99,v99
v_mfma_f32_16x16x32_bf16 v[32:35],a[228:231],v[60:63],v[32:35]
v_rcp_f32 v83,v83
v_mfma_f32_16x16x32_bf16 v[28:31],a[232:235],v[64:67],v[28:31]
buffer_load_dwordx4 v0,s[36:39],s67 offen offset:0 sc1 lds
v_cndmask_b32 v112,v108,v109,s[82:83]
v_cndmask_b32 v113,v110,v111,s[82:83]
v_cndmask_b32 v112,v112,v113,s[84:85]
v_cndmask_b32 v112,v104,v112,s[98:99]
v_mfma_f32_16x16x32_bf16 v[32:35],a[236:239],v[64:67],v[32:35]
ds_read_b128 v[60:63],v2 offset:20480
ds_write_b32 v13,v112 offset:0
v_mfma_f32_16x16x32_bf16 v[28:31],a[240:243],v[68:71],v[28:31]
ds_read_b128 v[64:67],v2 offset:21504
v_mul_f32 v40,v40,v80
v_mfma_f32_16x16x32_bf16 v[32:35],a[244:247],v[68:71],v[32:35]
v_mul_f32 v41,v41,v81
v_mul_f32 v42,v42,v82
v_mfma_f32_16x16x32_bf16 v[28:31],a[248:251],v[72:75],v[28:31]
buffer_load_dwordx4 v0,s[36:39],s67 offen offset:1024 sc1 lds
v_mfma_f32_16x16x32_bf16 v[32:35],a[252:255],v[72:75],v[32:35]
s_waitcnt lgkmcnt(3)
ds_read_b128 v[68:71],v2 offset:22528
v_mul_f32 v43,v43,v83
v_mfma_f32_16x16x32_bf16 v[28:31],a[128:131],v[44:47],v[28:31]
s_waitcnt vmcnt(7) lgkmcnt(2)
ds_read_b128 v[72:75],v2 offset:23552
v_cvt_pk_bf16_f32 v86,v40,v41
v_mfma_f32_16x16x32_bf16 v[32:35],a[132:135],v[44:47],v[32:35]
s_barrier
v_mfma_f32_16x16x32_bf16 v[28:31],a[136:139],v[48:51],v[28:31]
ds_read_b128 v[116:119],v14 offset:0
ds_read_b128 v[120:123],v14 offset:16
v_mfma_f32_16x16x32_bf16 v[32:35],a[140:143],v[48:51],v[32:35]
ds_read_b128 v[44:47],v2 offset:40960
v_cvt_pk_bf16_f32 v87,v42,v43
v_mfma_f32_16x16x32_bf16 v[28:31],a[144:147],v[52:55],v[28:31]
buffer_load_dwordx4 v0,s[36:39],s67 offen offset:2048 sc1 lds
v_mfma_f32_16x16x32_bf16 v[32:35],a[148:151],v[52:55],v[32:35]
ds_read_b128 v[48:51],v2 offset:41984
v_mfma_f32_16x16x32_bf16 v[28:31],a[152:155],v[56:59],v[28:31]
s_waitcnt lgkmcnt(2)
ds_read_b128 v[52:55],v2 offset:43008
v_add_f32 v116,v116,v117
v_mfma_f32_16x16x32_bf16 v[32:35],a[156:159],v[56:59],v[32:35]
v_add_f32 v118,v118,v119
v_add_f32 v116,v116,v118
v_mfma_f32_16x16x32_bf16 v[28:31],a[160:163],v[60:63],v[28:31]
buffer_load_dwordx4 v0,s[36:39],s67 offen offset:3072 sc1 lds
v_mfma_f32_16x16x32_bf16 v[32:35],a[164:167],v[60:63],v[32:35]
ds_read_b128 v[56:59],v2 offset:44032
v_mul_f32 v92,s75,v116
v_mfma_f32_16x16x32_bf16 v[28:31],a[168:171],v[64:67],v[28:31]
ds_read_b128 v[60:63],v2 offset:45056
v_add_f32 v120,v120,v121
v_mfma_f32_16x16x32_bf16 v[32:35],a[172:175],v[64:67],v[32:35]
v_add_f32 v122,v122,v123
v_add_f32 v120,v120,v122
v_mfma_f32_16x16x32_bf16 v[28:31],a[176:179],v[68:71],v[28:31]
buffer_store_dwordx4 v[84:87],v3,s[36:39],s73 offen offset:0 sc0 sc1
v_mul_f32 v120,s75,v120
v_fma_f32 v120,-v92,v92,v120
v_max_f32 v120,0,v120
v_add_f32 v115,v15,v120
v_fma_f32 v120,v15,v115,v120
v_rsq_f32 v115,v115
v_mfma_f32_16x16x32_bf16 v[32:35],a[180:183],v[68:71],v[32:35]
ds_read_b128 v[64:67],v2 offset:46080
v_rsq_f32 v116,v120
v_mul_f32_e64 v114,-v92,v115
v_mfma_f32_16x16x32_bf16 v[28:31],a[184:187],v[72:75],v[28:31]
s_waitcnt lgkmcnt(2)
ds_read_b128 v[68:71],v2 offset:47104
buffer_store_dwordx2 v[114:115],v12,s[36:39],s65 offen offset:0 sc0 sc1
v_mul_f32 v93,s76,v116
v_mul_f32 v94,v116,v116
v_mul_f32 v95,v120,v116
v_mul_f32 v94,s77,v94
v_mul_f32 v94,v94,v93
v_mfma_f32_16x16x32_bf16 v[32:35],a[188:191],v[72:75],v[32:35]
s_cbranch_scc1 Lg1_slow1_%=
Lg1_a1w2_%=:
v_mfma_f32_16x16x32_bf16 v[36:39],a[64:67],v[44:47],0
s_sub_u32 s69,s67,1048576
ds_read_b128 v[72:75],v2 offset:48128
s_cmp_lt_u32 s69,2097152
v_mfma_f32_16x16x32_bf16 v[40:43],a[68:71],v[44:47],0
s_cselect_b32 s89,3145728,s89
s_sub_u32 s69,s67,3145728
s_cmp_lt_u32 s69,1048576
v_mfma_f32_16x16x32_bf16 v[104:107],v[100:103],v[44:47],0
s_cselect_b32 s89,4194304,s89
s_add_u32 m0,s62,16384
s_lshr_b32 s73,s48,0
v_mfma_f32_16x16x32_bf16 v[108:111],v[44:47],v[44:47],0
buffer_load_dwordx4 v1,s[36:39],s67 offen offset:0 sc1 lds
v_mfma_f32_16x16x32_bf16 v[36:39],a[72:75],v[48:51],v[36:39]
s_lshr_b32 s65,s49,6
ds_read_b128 v[44:47],v2 offset:32768
v_fma_f32 v28,-v92,v20,v28
v_mfma_f32_16x16x32_bf16 v[40:43],a[76:79],v[48:51],v[40:43]
s_lshr_b32 s72,s67,6
v_fma_f32 v29,-v92,v21,v29
v_fma_f32 v30,-v92,v22,v30
v_mfma_f32_16x16x32_bf16 v[104:107],v[100:103],v[48:51],v[104:107]
s_lshr_b32 s71,s62,2
v_fma_f32 v31,-v92,v23,v31
v_mul_f32 v76,v28,v28
v_mfma_f32_16x16x32_bf16 v[108:111],v[48:51],v[48:51],v[108:111]
buffer_load_dwordx4 v1,s[36:39],s67 offen offset:1024 sc1 lds
v_mfma_f32_16x16x32_bf16 v[36:39],a[80:83],v[52:55],v[36:39]
s_cmp_lt_u32 s67,4194304
ds_read_b128 v[48:51],v2 offset:33792
v_mul_f32 v77,v29,v29
v_mfma_f32_16x16x32_bf16 v[40:43],a[84:87],v[52:55],v[40:43]
s_cselect_b32 s72,s59,s72
v_mul_f32 v78,v30,v30
v_mul_f32 v79,v31,v31
v_mfma_f32_16x16x32_bf16 v[104:107],v[100:103],v[52:55],v[104:107]
s_mov_b64 s[44:45],s[46:47]
v_fma_f32 v76,v94,v76,v93
v_mul_f32 v76,v28,v76
v_mfma_f32_16x16x32_bf16 v[108:111],v[52:55],v[52:55],v[108:111]
buffer_load_dwordx4 v1,s[36:39],s67 offen offset:2048 sc1 lds
v_mfma_f32_16x16x32_bf16 v[36:39],a[88:91],v[56:59],v[36:39]
s_mov_b64 s[46:47],s[48:49]
ds_read_b128 v[52:55],v2 offset:34816
v_fma_f32 v77,v94,v77,v93
v_mfma_f32_16x16x32_bf16 v[40:43],a[92:95],v[56:59],v[40:43]
s_mov_b64 s[48:49],s[50:51]
v_exp_f32 v76,v76
v_mfma_f32_16x16x32_bf16 v[104:107],v[100:103],v[56:59],v[104:107]
v_mul_f32 v77,v29,v77
v_fma_f32 v76,v76,v95,v95
v_mfma_f32_16x16x32_bf16 v[108:111],v[56:59],v[56:59],v[108:111]
s_waitcnt lgkmcnt(3)
buffer_load_dwordx4 v1,s[36:39],s67 offen offset:3072 sc1 lds
v_mfma_f32_16x16x32_bf16 v[36:39],a[96:99],v[60:63],v[36:39]
s_add_u32 m0,s71,s88
ds_read_b128 v[56:59],v2 offset:35840
v_fma_f32 v78,v94,v78,v93
v_mfma_f32_16x16x32_bf16 v[40:43],a[100:103],v[60:63],v[40:43]
buffer_load_dword v10,s[36:39],s72 offen offset:0 sc1 lds
v_mfma_f32_16x16x32_bf16 v[36:39],a[104:107],v[64:67],v[36:39]
s_xor_b32 s87,s63,65536
ds_read_b128 v[60:63],v2 offset:36864
v_fma_f32 v79,v94,v79,v93
v_mfma_f32_16x16x32_bf16 v[40:43],a[108:111],v[64:67],v[40:43]
s_cmp_ge_u32 s67,s59)"
R"(
v_exp_f32 v77,v77
v_mfma_f32_16x16x32_bf16 v[36:39],a[112:115],v[68:71],v[36:39]
s_cselect_b32 s86,s50,s51
ds_read_b128 v[64:67],v2 offset:37888
v_mul_f32 v78,v30,v78
v_mfma_f32_16x16x32_bf16 v[40:43],a[116:119],v[68:71],v[40:43]
s_cselect_b32 s87,s87,s64
v_exp_f32 v78,v78
v_mfma_f32_16x16x32_bf16 v[36:39],a[120:123],v[72:75],v[36:39]
s_mov_b32 s62,s87
ds_read_b128 v[68:71],v2 offset:38912
v_mul_f32 v79,v31,v79
v_mfma_f32_16x16x32_bf16 v[40:43],a[124:127],v[72:75],v[40:43]
s_waitcnt lgkmcnt(3)
v_exp_f32 v79,v79
v_mfma_f32_16x16x32_bf16 v[36:39],a[0:3],v[44:47],v[36:39]
s_waitcnt vmcnt(10)
ds_read_b128 v[72:75],v2 offset:39936
v_fma_f32 v77,v77,v95,v95
v_mfma_f32_16x16x32_bf16 v[40:43],a[4:7],v[44:47],v[40:43]
s_barrier
v_mfma_f32_16x16x32_bf16 v[36:39],a[8:11],v[48:51],v[36:39]
s_cmp_lt_u32 s86,s89
ds_read_b128 v[44:47],v2 offset:57344
ds_read_b32 v16,v7 offset:256
v_mfma_f32_16x16x32_bf16 v[40:43],a[12:15],v[48:51],v[40:43]
s_mov_b32 m0,s62
v_rcp_f32 v76,v76
v_mfma_f32_16x16x32_bf16 v[36:39],a[16:19],v[52:55],v[36:39]
s_mov_b64 s[50:51],s[52:53]
ds_read_b128 v[48:51],v2 offset:58368
v_fma_f32 v78,v78,v95,v95
v_mfma_f32_16x16x32_bf16 v[40:43],a[20:23],v[52:55],v[40:43]
v_rcp_f32 v77,v77
v_mfma_f32_16x16x32_bf16 v[36:39],a[24:27],v[56:59],v[36:39]
s_waitcnt lgkmcnt(1)
ds_read_b128 v[52:55],v2 offset:59392
v_fma_f32 v79,v79,v95,v95
v_mfma_f32_16x16x32_bf16 v[40:43],a[28:31],v[56:59],v[40:43]
v_cmp_eq_u32 vcc,s60,v16
v_rcp_f32 v78,v78
v_mul_f32 v28,v28,v76
v_mfma_f32_16x16x32_bf16 v[36:39],a[32:35],v[60:63],v[36:39]
s_cselect_b32 vcc_lo,-1,vcc_lo
ds_read_b128 v[56:59],v2 offset:60416
v_mul_f32 v29,v29,v77
v_mfma_f32_16x16x32_bf16 v[40:43],a[36:39],v[60:63],v[40:43]
s_cmp_eq_u32 vcc_lo,-1
v_rcp_f32 v79,v79
v_mfma_f32_16x16x32_bf16 v[36:39],a[40:43],v[64:67],v[36:39]
s_cselect_b32 s66,s86,s59
ds_read_b128 v[60:63],v2 offset:61440
v_mul_f32 v30,v30,v78
v_mfma_f32_16x16x32_bf16 v[40:43],a[44:47],v[64:67],v[40:43]
s_add_u32 s74,s53,s55
v_mul_f32 v31,v31,v79
v_cvt_pk_bf16_f32 v84,v28,v29
v_mfma_f32_16x16x32_bf16 v[36:39],a[48:51],v[68:71],v[36:39]
s_cmp_eq_u32 s74,s56
ds_read_b128 v[64:67],v2 offset:62464
v_cvt_pk_bf16_f32 v85,v30,v31
v_mfma_f32_16x16x32_bf16 v[40:43],a[52:55],v[68:71],v[40:43]
s_cselect_b32 s74,s57,s74
v_fma_f32 v32,-v92,v24,v32
v_fma_f32 v33,-v92,v25,v33
v_mfma_f32_16x16x32_bf16 v[36:39],a[56:59],v[72:75],v[36:39]
s_cmp_eq_u32 s74,s58
ds_read_b128 v[68:71],v2 offset:63488
v_fma_f32 v34,-v92,v26,v34
v_mfma_f32_16x16x32_bf16 v[40:43],a[60:63],v[72:75],v[40:43]
s_waitcnt lgkmcnt(3)
v_fma_f32 v35,-v92,v27,v35
v_mul_f32 v80,v32,v32
v_mfma_f32_16x16x32_bf16 v[36:39],a[192:195],v[44:47],v[36:39]
s_cselect_b32 s74,s59,s74
ds_read_b128 v[72:75],v2 offset:64512
v_mul_f32 v81,v33,v33
v_mfma_f32_16x16x32_bf16 v[40:43],a[196:199],v[44:47],v[40:43]
s_xor_b32 s64,s64,65536
v_mul_f32 v82,v34,v34
v_mul_f32 v83,v35,v35
v_mfma_f32_16x16x32_bf16 v[104:107],v[100:103],v[44:47],v[104:107]
s_mov_b32 s52,s74
v_fma_f32 v80,v94,v80,v93
v_mul_f32 v80,v32,v80
v_mfma_f32_16x16x32_bf16 v[108:111],v[44:47],v[44:47],v[108:111]
s_cmp_lt_u32 s67,4194304
v_exp_f32 v80,v80
v_mfma_f32_16x16x32_bf16 v[36:39],a[200:203],v[48:51],v[36:39]
ds_read_b128 v[44:47],v2 offset:49152
v_fma_f32 v80,v80,v95,v95
v_mfma_f32_16x16x32_bf16 v[40:43],a[204:207],v[48:51],v[40:43]
v_rcp_f32 v80,v80
v_mfma_f32_16x16x32_bf16 v[104:107],v[100:103],v[48:51],v[104:107]
v_fma_f32 v81,v94,v81,v93
v_mul_f32 v81,v33,v81
v_mfma_f32_16x16x32_bf16 v[108:111],v[48:51],v[48:51],v[108:111]
v_exp_f32 v81,v81
v_mfma_f32_16x16x32_bf16 v[36:39],a[208:211],v[52:55],v[36:39]
ds_read_b128 v[48:51],v2 offset:50176
v_fma_f32 v81,v81,v95,v95
v_mfma_f32_16x16x32_bf16 v[40:43],a[212:215],v[52:55],v[40:43]
v_rcp_f32 v81,v81
v_mfma_f32_16x16x32_bf16 v[104:107],v[100:103],v[52:55],v[104:107]
v_fma_f32 v82,v94,v82,v93
v_mul_f32 v82,v34,v82
v_mfma_f32_16x16x32_bf16 v[108:111],v[52:55],v[52:55],v[108:111]
v_exp_f32 v82,v82
v_mfma_f32_16x16x32_bf16 v[36:39],a[216:219],v[56:59],v[36:39]
ds_read_b128 v[52:55],v2 offset:51200
v_fma_f32 v82,v82,v95,v95
v_mfma_f32_16x16x32_bf16 v[40:43],a[220:223],v[56:59],v[40:43]
v_rcp_f32 v82,v82
v_mfma_f32_16x16x32_bf16 v[104:107],v[100:103],v[56:59],v[104:107]
v_fma_f32 v83,v94,v83,v93
v_mul_f32 v83,v35,v83
v_mfma_f32_16x16x32_bf16 v[108:111],v[56:59],v[56:59],v[108:111]
s_waitcnt lgkmcnt(3)
v_exp_f32 v83,v83
v_mfma_f32_16x16x32_bf16 v[36:39],a[224:227],v[60:63],v[36:39]
ds_read_b128 v[56:59],v2 offset:52224
v_fma_f32 v83,v83,v95,v95
v_mfma_f32_16x16x32_bf16 v[40:43],a[228:231],v[60:63],v[40:43]
v_rcp_f32 v83,v83
v_mfma_f32_16x16x32_bf16 v[36:39],a[232:235],v[64:67],v[36:39]
buffer_load_dwordx4 v0,s[36:39],s66 offen offset:0 sc1 lds
v_cndmask_b32 v112,v108,v109,s[82:83]
v_cndmask_b32 v113,v110,v111,s[82:83]
v_cndmask_b32 v112,v112,v113,s[84:85]
v_cndmask_b32 v112,v104,v112,s[98:99]
v_mfma_f32_16x16x32_bf16 v[40:43],a[236:239],v[64:67],v[40:43]
ds_read_b128 v[60:63],v2 offset:53248
ds_write_b32 v13,v112 offset:2048
v_mfma_f32_16x16x32_bf16 v[36:39],a[240:243],v[68:71],v[36:39]
ds_read_b128 v[64:67],v2 offset:54272
v_mul_f32 v32,v32,v80
v_mfma_f32_16x16x32_bf16 v[40:43],a[244:247],v[68:71],v[40:43]
v_mul_f32 v33,v33,v81
v_mul_f32 v34,v34,v82
v_mfma_f32_16x16x32_bf16 v[36:39],a[248:251],v[72:75],v[36:39]
buffer_load_dwordx4 v0,s[36:39],s66 offen offset:1024 sc1 lds
v_mfma_f32_16x16x32_bf16 v[40:43],a[252:255],v[72:75],v[40:43]
s_waitcnt lgkmcnt(3)
ds_read_b128 v[68:71],v2 offset:55296
v_mul_f32 v35,v35,v83
v_mfma_f32_16x16x32_bf16 v[36:39],a[128:131],v[44:47],v[36:39]
s_waitcnt vmcnt(7) lgkmcnt(2)
ds_read_b128 v[72:75],v2 offset:56320
v_xor_b32 v2,65536,v2
v_mfma_f32_16x16x32_bf16 v[40:43],a[132:135],v[44:47],v[40:43]
s_barrier
v_mfma_f32_16x16x32_bf16 v[36:39],a[136:139],v[48:51],v[36:39]
ds_read_b128 v[116:119],v14 offset:2048
ds_read_b128 v[120:123],v14 offset:2064
v_mfma_f32_16x16x32_bf16 v[40:43],a[140:143],v[48:51],v[40:43]
ds_read_b128 v[44:47],v2 offset:8192
v_cvt_pk_bf16_f32 v86,v32,v33
v_mfma_f32_16x16x32_bf16 v[36:39],a[144:147],v[52:55],v[36:39]
buffer_load_dwordx4 v0,s[36:39],s66 offen offset:2048 sc1 lds
v_mfma_f32_16x16x32_bf16 v[40:43],a[148:151],v[52:55],v[40:43]
ds_read_b128 v[48:51],v2 offset:9216
v_cvt_pk_bf16_f32 v87,v34,v35
v_mfma_f32_16x16x32_bf16 v[36:39],a[152:155],v[56:59],v[36:39]
s_waitcnt lgkmcnt(2)
ds_read_b128 v[52:55],v2 offset:10240
v_xor_b32 v8,16384,v8
v_mfma_f32_16x16x32_bf16 v[40:43],a[156:159],v[56:59],v[40:43]
v_add_f32 v116,v116,v117
v_add_f32 v118,v118,v119
v_mfma_f32_16x16x32_bf16 v[36:39],a[160:163],v[60:63],v[36:39]
buffer_load_dwordx4 v0,s[36:39],s66 offen offset:3072 sc1 lds
v_mfma_f32_16x16x32_bf16 v[40:43],a[164:167],v[60:63],v[40:43]
ds_read_b128 v[56:59],v2 offset:11264
v_add_f32 v116,v116,v118
v_mfma_f32_16x16x32_bf16 v[36:39],a[168:171],v[64:67],v[36:39]
ds_read_b128 v[60:63],v2 offset:12288
v_mul_f32 v96,s75,v116
v_mfma_f32_16x16x32_bf16 v[40:43],a[172:175],v[64:67],v[40:43]
v_add_f32 v120,v120,v121
v_add_f32 v122,v122,v123
v_mfma_f32_16x16x32_bf16 v[36:39],a[176:179],v[68:71],v[36:39]
buffer_store_dwordx4 v[84:87],v3,s[36:39],s73 offen offset:0 sc0 sc1
v_add_f32 v120,v120,v122
v_mul_f32 v120,s75,v120
v_fma_f32 v120,-v96,v96,v120
v_max_f32 v120,0,v120
v_add_f32 v115,v15,v120
v_fma_f32 v120,v15,v115,v120
v_rsq_f32 v115,v115
v_mfma_f32_16x16x32_bf16 v[40:43],a[180:183],v[68:71],v[40:43]
ds_read_b128 v[64:67],v2 offset:13312
v_rsq_f32 v116,v120
v_mul_f32_e64 v114,-v96,v115
v_mfma_f32_16x16x32_bf16 v[36:39],a[184:187],v[72:75],v[36:39]
s_waitcnt lgkmcnt(2)
ds_read_b128 v[68:71],v2 offset:14336
buffer_store_dwordx2 v[114:115],v12,s[36:39],s65 offen offset:0 sc0 sc1
v_mul_f32 v97,s76,v116
v_mul_f32 v98,v116,v116
v_mul_f32 v99,v120,v116)"
R"(
v_mul_f32 v98,s77,v98
v_mul_f32 v98,v98,v97
v_mfma_f32_16x16x32_bf16 v[40:43],a[188:191],v[72:75],v[40:43]
s_cbranch_scc1 Lg1_a0w2_%=
s_cmp_ge_u32 s48,4194304
s_cselect_b64 s[90:91],s[94:95],s[90:91]
s_cselect_b64 s[92:93],s[96:97],s[92:93]
s_cmp_lt_u32 s67,s59
s_cbranch_scc1 Lg1_d0w2_%=
s_branch Lg1_slow0_%=
Lg1_a0w3_%=:
v_mfma_f32_16x16x32_bf16 v[28:31],a[96:99],v[44:47],0
s_sub_u32 s69,s66,1048576
ds_read_b128 v[72:75],v2 offset:11264
s_cmp_lt_u32 s69,2097152
v_mfma_f32_16x16x32_bf16 v[32:35],a[100:103],v[44:47],0
s_cselect_b32 s89,3145728,s89
s_sub_u32 s69,s66,3145728
s_cmp_lt_u32 s69,1048576
v_mfma_f32_16x16x32_bf16 v[104:107],v[100:103],v[44:47],0
s_cselect_b32 s89,4194304,s89
s_add_u32 m0,s62,16384
s_lshr_b32 s73,s47,0
v_mfma_f32_16x16x32_bf16 v[108:111],v[44:47],v[44:47],0
s_lshr_b32 s72,s66,6
v_fma_f32 v36,-v96,v20,v36
v_fma_f32 v37,-v96,v21,v37
v_mfma_f32_16x16x32_bf16 v[28:31],a[104:107],v[48:51],v[28:31]
buffer_load_dwordx4 v1,s[36:39],s66 offen offset:0 sc1 lds
v_mfma_f32_16x16x32_bf16 v[32:35],a[108:111],v[48:51],v[32:35]
s_lshr_b32 s71,s62,2
ds_read_b128 v[44:47],v2 offset:4096
v_fma_f32 v38,-v96,v22,v38
v_mfma_f32_16x16x32_bf16 v[104:107],v[100:103],v[48:51],v[104:107]
s_cmp_lt_u32 s66,4194304
v_fma_f32 v39,-v96,v23,v39
v_mul_f32 v76,v36,v36
v_mfma_f32_16x16x32_bf16 v[108:111],v[48:51],v[48:51],v[108:111]
s_cselect_b32 s72,s59,s72
v_mul_f32 v77,v37,v37
v_mul_f32 v78,v38,v38
v_mfma_f32_16x16x32_bf16 v[28:31],a[112:115],v[52:55],v[28:31]
buffer_load_dwordx4 v1,s[36:39],s66 offen offset:1024 sc1 lds
v_mfma_f32_16x16x32_bf16 v[32:35],a[116:119],v[52:55],v[32:35]
ds_read_b128 v[48:51],v2 offset:5120
v_mul_f32 v79,v39,v39
v_mfma_f32_16x16x32_bf16 v[104:107],v[100:103],v[52:55],v[104:107]
v_fma_f32 v76,v98,v76,v97
v_mul_f32 v76,v36,v76
v_mfma_f32_16x16x32_bf16 v[108:111],v[52:55],v[52:55],v[108:111]
v_exp_f32 v76,v76
v_mfma_f32_16x16x32_bf16 v[28:31],a[120:123],v[56:59],v[28:31]
buffer_load_dwordx4 v1,s[36:39],s66 offen offset:2048 sc1 lds
v_mfma_f32_16x16x32_bf16 v[32:35],a[124:127],v[56:59],v[32:35]
ds_read_b128 v[52:55],v2 offset:6144
v_fma_f32 v76,v76,v99,v99
v_mfma_f32_16x16x32_bf16 v[104:107],v[100:103],v[56:59],v[104:107]
v_rcp_f32 v76,v76
v_mfma_f32_16x16x32_bf16 v[108:111],v[56:59],v[56:59],v[108:111]
s_waitcnt lgkmcnt(3)
v_fma_f32 v77,v98,v77,v97
v_mul_f32 v77,v37,v77
v_mfma_f32_16x16x32_bf16 v[28:31],a[64:67],v[60:63],v[28:31]
buffer_load_dwordx4 v1,s[36:39],s66 offen offset:3072 sc1 lds
v_mfma_f32_16x16x32_bf16 v[32:35],a[68:71],v[60:63],v[32:35]
s_add_u32 m0,s71,s88
ds_read_b128 v[56:59],v2 offset:7168
v_fma_f32 v78,v98,v78,v97
v_mfma_f32_16x16x32_bf16 v[28:31],a[72:75],v[64:67],v[28:31]
buffer_load_dword v10,s[36:39],s72 offen offset:0 sc1 lds
v_mfma_f32_16x16x32_bf16 v[32:35],a[76:79],v[64:67],v[32:35]
s_xor_b32 s87,s64,65536
ds_read_b128 v[60:63],v2 offset:0
v_mul_f32 v78,v38,v78
v_mfma_f32_16x16x32_bf16 v[28:31],a[80:83],v[68:71],v[28:31]
s_cmp_ge_u32 s66,s59
ds_read_b128 v[64:67],v2 offset:1024
v_fma_f32 v79,v98,v79,v97
v_mfma_f32_16x16x32_bf16 v[32:35],a[84:87],v[68:71],v[32:35]
s_cselect_b32 s86,s49,s50
v_exp_f32 v77,v77
v_mfma_f32_16x16x32_bf16 v[28:31],a[88:91],v[72:75],v[28:31]
s_cselect_b32 s87,s87,s63
ds_read_b128 v[68:71],v2 offset:2048
v_mul_f32 v79,v39,v79
v_mfma_f32_16x16x32_bf16 v[32:35],a[92:95],v[72:75],v[32:35]
s_waitcnt lgkmcnt(3)
v_exp_f32 v78,v78
v_mfma_f32_16x16x32_bf16 v[28:31],a[32:35],v[44:47],v[28:31]
s_waitcnt vmcnt(11)
ds_read_b128 v[72:75],v2 offset:3072
v_fma_f32 v77,v77,v99,v99
v_mfma_f32_16x16x32_bf16 v[32:35],a[36:39],v[44:47],v[32:35]
s_barrier
v_mfma_f32_16x16x32_bf16 v[28:31],a[40:43],v[48:51],v[28:31]
s_mov_b32 s62,s87
ds_read_b128 v[44:47],v2 offset:28672
ds_read_b32 v16,v7 offset:0
v_mfma_f32_16x16x32_bf16 v[32:35],a[44:47],v[48:51],v[32:35]
s_cmp_lt_u32 s86,s89
v_exp_f32 v79,v79
v_mfma_f32_16x16x32_bf16 v[28:31],a[48:51],v[52:55],v[28:31]
s_mov_b32 m0,s62
ds_read_b128 v[48:51],v2 offset:29696
v_fma_f32 v78,v78,v99,v99
v_mfma_f32_16x16x32_bf16 v[32:35],a[52:55],v[52:55],v[32:35]
v_rcp_f32 v77,v77
v_mfma_f32_16x16x32_bf16 v[28:31],a[56:59],v[56:59],v[28:31]
s_waitcnt lgkmcnt(1)
ds_read_b128 v[52:55],v2 offset:30720
v_fma_f32 v79,v79,v99,v99
v_mfma_f32_16x16x32_bf16 v[32:35],a[60:63],v[56:59],v[32:35]
v_cmp_eq_u32 vcc,s60,v16
v_rcp_f32 v78,v78
v_mul_f32 v36,v36,v76
v_mfma_f32_16x16x32_bf16 v[28:31],a[0:3],v[60:63],v[28:31]
s_cselect_b32 vcc_lo,-1,vcc_lo
ds_read_b128 v[56:59],v2 offset:31744
v_mul_f32 v37,v37,v77
v_mfma_f32_16x16x32_bf16 v[32:35],a[4:7],v[60:63],v[32:35]
s_cmp_eq_u32 vcc_lo,-1
v_rcp_f32 v79,v79
v_mfma_f32_16x16x32_bf16 v[28:31],a[8:11],v[64:67],v[28:31]
s_cselect_b32 s67,s86,s59
ds_read_b128 v[60:63],v2 offset:24576
v_mul_f32 v38,v38,v78
v_mfma_f32_16x16x32_bf16 v[32:35],a[12:15],v[64:67],v[32:35]
s_add_u32 s53,s52,s55
v_mul_f32 v39,v39,v79
v_cvt_pk_bf16_f32 v84,v36,v37
v_mfma_f32_16x16x32_bf16 v[28:31],a[16:19],v[68:71],v[28:31]
s_cmp_eq_u32 s53,s56
ds_read_b128 v[64:67],v2 offset:25600
v_cvt_pk_bf16_f32 v85,v38,v39
v_mfma_f32_16x16x32_bf16 v[32:35],a[20:23],v[68:71],v[32:35]
s_cselect_b32 s53,s57,s53
v_fma_f32 v40,-v96,v24,v40
v_fma_f32 v41,-v96,v25,v41
v_mfma_f32_16x16x32_bf16 v[28:31],a[24:27],v[72:75],v[28:31]
s_cmp_eq_u32 s53,s58
ds_read_b128 v[68:71],v2 offset:26624
v_fma_f32 v42,-v96,v26,v42
v_mfma_f32_16x16x32_bf16 v[32:35],a[28:31],v[72:75],v[32:35]
s_waitcnt lgkmcnt(3)
v_fma_f32 v43,-v96,v27,v43
v_mul_f32 v80,v40,v40
v_mfma_f32_16x16x32_bf16 v[28:31],a[224:227],v[44:47],v[28:31]
s_cselect_b32 s53,s59,s53
ds_read_b128 v[72:75],v2 offset:27648
v_mul_f32 v81,v41,v41
v_mfma_f32_16x16x32_bf16 v[32:35],a[228:231],v[44:47],v[32:35]
s_xor_b32 s63,s63,65536
v_mul_f32 v82,v42,v42
v_mul_f32 v83,v43,v43
v_mfma_f32_16x16x32_bf16 v[104:107],v[100:103],v[44:47],v[104:107]
s_lshr_b32 s70,s46,5
v_fma_f32 v80,v98,v80,v97
v_mul_f32 v80,v40,v80
v_mfma_f32_16x16x32_bf16 v[108:111],v[44:47],v[44:47],v[108:111]
s_cmp_ge_u32 s66,s59
v_exp_f32 v80,v80
v_mfma_f32_16x16x32_bf16 v[28:31],a[232:235],v[48:51],v[28:31]
ds_read_b128 v[44:47],v2 offset:20480
v_fma_f32 v80,v80,v99,v99
v_mfma_f32_16x16x32_bf16 v[32:35],a[236:239],v[48:51],v[32:35]
v_rcp_f32 v80,v80
v_mfma_f32_16x16x32_bf16 v[104:107],v[100:103],v[48:51],v[104:107]
v_fma_f32 v81,v98,v81,v97
v_mul_f32 v81,v41,v81
v_mfma_f32_16x16x32_bf16 v[108:111],v[48:51],v[48:51],v[108:111]
v_exp_f32 v81,v81
v_mfma_f32_16x16x32_bf16 v[28:31],a[240:243],v[52:55],v[28:31]
ds_read_b128 v[48:51],v2 offset:21504
v_fma_f32 v81,v81,v99,v99
v_mfma_f32_16x16x32_bf16 v[32:35],a[244:247],v[52:55],v[32:35]
v_rcp_f32 v81,v81
v_mfma_f32_16x16x32_bf16 v[104:107],v[100:103],v[52:55],v[104:107]
v_fma_f32 v82,v98,v82,v97
v_mul_f32 v82,v42,v82
v_mfma_f32_16x16x32_bf16 v[108:111],v[52:55],v[52:55],v[108:111]
v_exp_f32 v82,v82
v_mfma_f32_16x16x32_bf16 v[28:31],a[248:251],v[56:59],v[28:31]
ds_read_b128 v[52:55],v2 offset:22528
v_fma_f32 v82,v82,v99,v99
v_mfma_f32_16x16x32_bf16 v[32:35],a[252:255],v[56:59],v[32:35]
v_rcp_f32 v82,v82
v_mfma_f32_16x16x32_bf16 v[104:107],v[100:103],v[56:59],v[104:107]
v_fma_f32 v83,v98,v83,v97
v_mul_f32 v83,v43,v83
v_mfma_f32_16x16x32_bf16 v[108:111],v[56:59],v[56:59],v[108:111]
s_waitcnt lgkmcnt(3)
v_exp_f32 v83,v83
v_mfma_f32_16x16x32_bf16 v[28:31],a[192:195],v[60:63],v[28:31]
ds_read_b128 v[56:59],v2 offset:23552
v_fma_f32 v83,v83,v99,v99
v_mfma_f32_16x16x32_bf16 v[32:35],a[196:199],v[60:63],v[32:35]
v_rcp_f32 v83,v83
v_mfma_f32_16x16x32_bf16 v[28:31],a[200:203],v[64:67],v[28:31]
v_cndmask_b32 v112,v108,v109,s[82:83]
v_cndmask_b32 v113,v110,v111,s[82:83])"
R"(
v_cndmask_b32 v112,v112,v113,s[84:85]
v_cndmask_b32 v112,v104,v112,s[98:99]
v_mfma_f32_16x16x32_bf16 v[32:35],a[204:207],v[64:67],v[32:35]
ds_read_b128 v[60:63],v2 offset:16384
ds_write_b32 v13,v112 offset:0
buffer_load_dwordx4 v0,s[36:39],s67 offen offset:0 sc1 lds
v_mfma_f32_16x16x32_bf16 v[28:31],a[208:211],v[68:71],v[28:31]
ds_read_b128 v[64:67],v2 offset:17408
v_mul_f32 v40,v40,v80
v_mfma_f32_16x16x32_bf16 v[32:35],a[212:215],v[68:71],v[32:35]
v_mul_f32 v41,v41,v81
v_mul_f32 v42,v42,v82
v_mfma_f32_16x16x32_bf16 v[28:31],a[216:219],v[72:75],v[28:31]
ds_read_b128 v[68:71],v2 offset:18432
v_mul_f32 v43,v43,v83
v_mfma_f32_16x16x32_bf16 v[32:35],a[220:223],v[72:75],v[32:35]
s_waitcnt lgkmcnt(4)
buffer_load_dwordx4 v0,s[36:39],s67 offen offset:1024 sc1 lds
v_mfma_f32_16x16x32_bf16 v[28:31],a[160:163],v[44:47],v[28:31]
s_waitcnt vmcnt(7) lgkmcnt(2)
ds_read_b128 v[72:75],v2 offset:19456
v_cvt_pk_bf16_f32 v86,v40,v41
v_mfma_f32_16x16x32_bf16 v[32:35],a[164:167],v[44:47],v[32:35]
s_barrier
v_mfma_f32_16x16x32_bf16 v[28:31],a[168:171],v[48:51],v[28:31]
buffer_store_dword v5,v4,s[36:39],s70 offen offset:0 sc0 sc1
ds_read_b128 v[116:119],v14 offset:0
ds_read_b128 v[120:123],v14 offset:16
ds_read_b128 v[44:47],v2 offset:45056
v_mfma_f32_16x16x32_bf16 v[32:35],a[172:175],v[48:51],v[32:35]
v_cvt_pk_bf16_f32 v87,v42,v43
v_mfma_f32_16x16x32_bf16 v[28:31],a[176:179],v[52:55],v[28:31]
ds_read_b128 v[48:51],v2 offset:46080
v_mfma_f32_16x16x32_bf16 v[32:35],a[180:183],v[52:55],v[32:35]
buffer_load_dwordx4 v0,s[36:39],s67 offen offset:2048 sc1 lds
v_mfma_f32_16x16x32_bf16 v[28:31],a[184:187],v[56:59],v[28:31]
s_waitcnt lgkmcnt(2)
ds_read_b128 v[52:55],v2 offset:47104
v_add_f32 v116,v116,v117
v_mfma_f32_16x16x32_bf16 v[32:35],a[188:191],v[56:59],v[32:35]
v_add_f32 v118,v118,v119
v_add_f32 v116,v116,v118
v_mfma_f32_16x16x32_bf16 v[28:31],a[128:131],v[60:63],v[28:31]
ds_read_b128 v[56:59],v2 offset:48128
v_mul_f32 v92,s75,v116
v_mfma_f32_16x16x32_bf16 v[32:35],a[132:135],v[60:63],v[32:35]
buffer_load_dwordx4 v0,s[36:39],s67 offen offset:3072 sc1 lds
v_mfma_f32_16x16x32_bf16 v[28:31],a[136:139],v[64:67],v[28:31]
ds_read_b128 v[60:63],v2 offset:40960
v_add_f32 v120,v120,v121
v_mfma_f32_16x16x32_bf16 v[32:35],a[140:143],v[64:67],v[32:35]
v_add_f32 v122,v122,v123
v_add_f32 v120,v120,v122
v_mfma_f32_16x16x32_bf16 v[28:31],a[144:147],v[68:71],v[28:31]
ds_read_b128 v[64:67],v2 offset:41984
v_mul_f32 v120,s75,v120
v_mfma_f32_16x16x32_bf16 v[32:35],a[148:151],v[68:71],v[32:35]
buffer_store_dwordx4 v[84:87],v3,s[36:39],s73 offen offset:0 sc0 sc1
v_fma_f32 v120,-v92,v92,v120
v_max_f32 v120,0,v120
v_add_f32 v115,v15,v120
v_fma_f32 v120,v15,v115,v120
v_rsq_f32 v116,v120
v_mfma_f32_16x16x32_bf16 v[28:31],a[152:155],v[72:75],v[28:31]
s_waitcnt lgkmcnt(2)
ds_read_b128 v[68:71],v2 offset:43008
v_mul_f32 v93,s76,v116
v_mul_f32 v94,v116,v116
v_mul_f32 v95,v120,v116
v_mul_f32 v94,s77,v94
v_mul_f32 v94,v94,v93
v_mfma_f32_16x16x32_bf16 v[32:35],a[156:159],v[72:75],v[32:35]
s_cbranch_scc1 Lg1_slow1_%=
Lg1_a1w3_%=:
v_mfma_f32_16x16x32_bf16 v[36:39],a[96:99],v[44:47],0
s_sub_u32 s69,s67,1048576
ds_read_b128 v[72:75],v2 offset:44032
s_cmp_lt_u32 s69,2097152
v_mfma_f32_16x16x32_bf16 v[40:43],a[100:103],v[44:47],0
s_cselect_b32 s89,3145728,s89
s_sub_u32 s69,s67,3145728
s_cmp_lt_u32 s69,1048576
v_mfma_f32_16x16x32_bf16 v[104:107],v[100:103],v[44:47],0
s_cselect_b32 s89,4194304,s89
s_add_u32 m0,s62,16384
s_lshr_b32 s73,s48,0
v_mfma_f32_16x16x32_bf16 v[108:111],v[44:47],v[44:47],0
s_lshr_b32 s72,s67,6
v_fma_f32 v28,-v92,v20,v28
v_fma_f32 v29,-v92,v21,v29
v_mfma_f32_16x16x32_bf16 v[36:39],a[104:107],v[48:51],v[36:39]
buffer_load_dwordx4 v1,s[36:39],s67 offen offset:0 sc1 lds
v_mfma_f32_16x16x32_bf16 v[40:43],a[108:111],v[48:51],v[40:43]
s_lshr_b32 s71,s62,2
ds_read_b128 v[44:47],v2 offset:36864
v_fma_f32 v30,-v92,v22,v30
v_mfma_f32_16x16x32_bf16 v[104:107],v[100:103],v[48:51],v[104:107]
s_cmp_lt_u32 s67,4194304
v_fma_f32 v31,-v92,v23,v31
v_mul_f32 v76,v28,v28
v_mfma_f32_16x16x32_bf16 v[108:111],v[48:51],v[48:51],v[108:111]
s_cselect_b32 s72,s59,s72
v_mul_f32 v77,v29,v29
v_mul_f32 v78,v30,v30
v_mfma_f32_16x16x32_bf16 v[36:39],a[112:115],v[52:55],v[36:39]
buffer_load_dwordx4 v1,s[36:39],s67 offen offset:1024 sc1 lds
v_mfma_f32_16x16x32_bf16 v[40:43],a[116:119],v[52:55],v[40:43]
s_mov_b64 s[44:45],s[46:47]
ds_read_b128 v[48:51],v2 offset:37888
v_mul_f32 v79,v31,v31
v_mfma_f32_16x16x32_bf16 v[104:107],v[100:103],v[52:55],v[104:107]
v_fma_f32 v76,v94,v76,v93
v_mul_f32 v76,v28,v76
v_mfma_f32_16x16x32_bf16 v[108:111],v[52:55],v[52:55],v[108:111]
v_exp_f32 v76,v76
v_mfma_f32_16x16x32_bf16 v[36:39],a[120:123],v[56:59],v[36:39]
buffer_load_dwordx4 v1,s[36:39],s67 offen offset:2048 sc1 lds
v_mfma_f32_16x16x32_bf16 v[40:43],a[124:127],v[56:59],v[40:43]
ds_read_b128 v[52:55],v2 offset:38912
v_fma_f32 v76,v76,v95,v95
v_mfma_f32_16x16x32_bf16 v[104:107],v[100:103],v[56:59],v[104:107]
v_rcp_f32 v76,v76
v_mfma_f32_16x16x32_bf16 v[108:111],v[56:59],v[56:59],v[108:111]
s_waitcnt lgkmcnt(3)
v_fma_f32 v77,v94,v77,v93
v_mul_f32 v77,v29,v77
v_mfma_f32_16x16x32_bf16 v[36:39],a[64:67],v[60:63],v[36:39]
buffer_load_dwordx4 v1,s[36:39],s67 offen offset:3072 sc1 lds
v_mfma_f32_16x16x32_bf16 v[40:43],a[68:71],v[60:63],v[40:43]
s_add_u32 m0,s71,s88
ds_read_b128 v[56:59],v2 offset:39936
v_fma_f32 v78,v94,v78,v93
v_mfma_f32_16x16x32_bf16 v[36:39],a[72:75],v[64:67],v[36:39]
buffer_load_dword v10,s[36:39],s72 offen offset:0 sc1 lds
v_mfma_f32_16x16x32_bf16 v[40:43],a[76:79],v[64:67],v[40:43]
s_xor_b32 s87,s63,65536
ds_read_b128 v[60:63],v2 offset:32768
v_mul_f32 v78,v30,v78
v_mfma_f32_16x16x32_bf16 v[36:39],a[80:83],v[68:71],v[36:39]
s_cmp_ge_u32 s67,s59
ds_read_b128 v[64:67],v2 offset:33792
v_fma_f32 v79,v94,v79,v93
v_mfma_f32_16x16x32_bf16 v[40:43],a[84:87],v[68:71],v[40:43]
s_cselect_b32 s86,s50,s51
v_exp_f32 v77,v77
v_mfma_f32_16x16x32_bf16 v[36:39],a[88:91],v[72:75],v[36:39]
s_cselect_b32 s87,s87,s64
ds_read_b128 v[68:71],v2 offset:34816
v_mul_f32 v79,v31,v79
v_mfma_f32_16x16x32_bf16 v[40:43],a[92:95],v[72:75],v[40:43]
s_waitcnt lgkmcnt(3)
v_exp_f32 v78,v78
v_mfma_f32_16x16x32_bf16 v[36:39],a[32:35],v[44:47],v[36:39]
s_waitcnt vmcnt(11)
ds_read_b128 v[72:75],v2 offset:35840
v_fma_f32 v77,v77,v95,v95
v_mfma_f32_16x16x32_bf16 v[40:43],a[36:39],v[44:47],v[40:43]
s_barrier
v_mfma_f32_16x16x32_bf16 v[36:39],a[40:43],v[48:51],v[36:39]
s_mov_b32 s62,s87
ds_read_b128 v[44:47],v2 offset:61440
ds_read_b32 v16,v7 offset:256
v_mfma_f32_16x16x32_bf16 v[40:43],a[44:47],v[48:51],v[40:43]
s_cmp_lt_u32 s86,s89
v_exp_f32 v79,v79
v_mfma_f32_16x16x32_bf16 v[36:39],a[48:51],v[52:55],v[36:39]
s_mov_b32 m0,s62
ds_read_b128 v[48:51],v2 offset:62464
v_fma_f32 v78,v78,v95,v95
v_mfma_f32_16x16x32_bf16 v[40:43],a[52:55],v[52:55],v[40:43]
v_rcp_f32 v77,v77
v_mfma_f32_16x16x32_bf16 v[36:39],a[56:59],v[56:59],v[36:39]
s_waitcnt lgkmcnt(1)
ds_read_b128 v[52:55],v2 offset:63488
v_fma_f32 v79,v79,v95,v95
v_mfma_f32_16x16x32_bf16 v[40:43],a[60:63],v[56:59],v[40:43]
v_cmp_eq_u32 vcc,s60,v16
v_rcp_f32 v78,v78
v_mul_f32 v28,v28,v76
v_mfma_f32_16x16x32_bf16 v[36:39],a[0:3],v[60:63],v[36:39]
s_cselect_b32 vcc_lo,-1,vcc_lo
ds_read_b128 v[56:59],v2 offset:64512
v_mul_f32 v29,v29,v77
v_mfma_f32_16x16x32_bf16 v[40:43],a[4:7],v[60:63],v[40:43]
s_cmp_eq_u32 vcc_lo,-1
v_rcp_f32 v79,v79
v_mfma_f32_16x16x32_bf16 v[36:39],a[8:11],v[64:67],v[36:39]
s_cselect_b32 s66,s86,s59
ds_read_b128 v[60:63],v2 offset:57344
v_mul_f32 v30,v30,v78
v_mfma_f32_16x16x32_bf16 v[40:43],a[12:15],v[64:67],v[40:43]
s_add_u32 s74,s53,s55
v_mul_f32 v31,v31,v79
v_cvt_pk_bf16_f32 v84,v28,v29
v_mfma_f32_16x16x32_bf16 v[36:39],a[16:19],v[68:71],v[36:39]
s_cmp_eq_u32 s74,s56
ds_read_b128 v[64:67],v2 offset:58368
v_cvt_pk_bf16_f32 v85,v30,v31
v_mfma_f32_16x16x32_bf16 v[40:43],a[20:23],v[68:71],v[40:43]
s_cselect_b32 s74,s57,s74)"
R"(
v_fma_f32 v32,-v92,v24,v32
v_fma_f32 v33,-v92,v25,v33
v_mfma_f32_16x16x32_bf16 v[36:39],a[24:27],v[72:75],v[36:39]
s_cmp_eq_u32 s74,s58
ds_read_b128 v[68:71],v2 offset:59392
v_fma_f32 v34,-v92,v26,v34
v_mfma_f32_16x16x32_bf16 v[40:43],a[28:31],v[72:75],v[40:43]
s_waitcnt lgkmcnt(3)
v_fma_f32 v35,-v92,v27,v35
v_mul_f32 v80,v32,v32
v_mfma_f32_16x16x32_bf16 v[36:39],a[224:227],v[44:47],v[36:39]
s_cselect_b32 s74,s59,s74
ds_read_b128 v[72:75],v2 offset:60416
v_mul_f32 v81,v33,v33
v_mfma_f32_16x16x32_bf16 v[40:43],a[228:231],v[44:47],v[40:43]
s_xor_b32 s64,s64,65536
v_mul_f32 v82,v34,v34
v_mul_f32 v83,v35,v35
v_mfma_f32_16x16x32_bf16 v[104:107],v[100:103],v[44:47],v[104:107]
s_lshr_b32 s70,s47,5
v_fma_f32 v80,v94,v80,v93
v_mul_f32 v80,v32,v80
v_mfma_f32_16x16x32_bf16 v[108:111],v[44:47],v[44:47],v[108:111]
s_mov_b64 s[46:47],s[48:49]
v_exp_f32 v80,v80
v_mfma_f32_16x16x32_bf16 v[36:39],a[232:235],v[48:51],v[36:39]
s_mov_b64 s[48:49],s[50:51]
ds_read_b128 v[44:47],v2 offset:53248
v_fma_f32 v80,v80,v95,v95
v_mfma_f32_16x16x32_bf16 v[40:43],a[236:239],v[48:51],v[40:43]
s_mov_b64 s[50:51],s[52:53]
v_rcp_f32 v80,v80
v_mfma_f32_16x16x32_bf16 v[104:107],v[100:103],v[48:51],v[104:107]
s_mov_b32 s52,s74
v_fma_f32 v81,v94,v81,v93
v_mul_f32 v81,v33,v81
v_mfma_f32_16x16x32_bf16 v[108:111],v[48:51],v[48:51],v[108:111]
s_cmp_lt_u32 s67,4194304
v_exp_f32 v81,v81
v_mfma_f32_16x16x32_bf16 v[36:39],a[240:243],v[52:55],v[36:39]
ds_read_b128 v[48:51],v2 offset:54272
v_fma_f32 v81,v81,v95,v95
v_mfma_f32_16x16x32_bf16 v[40:43],a[244:247],v[52:55],v[40:43]
v_rcp_f32 v81,v81
v_mfma_f32_16x16x32_bf16 v[104:107],v[100:103],v[52:55],v[104:107]
v_fma_f32 v82,v94,v82,v93
v_mul_f32 v82,v34,v82
v_mfma_f32_16x16x32_bf16 v[108:111],v[52:55],v[52:55],v[108:111]
v_exp_f32 v82,v82
v_mfma_f32_16x16x32_bf16 v[36:39],a[248:251],v[56:59],v[36:39]
ds_read_b128 v[52:55],v2 offset:55296
v_fma_f32 v82,v82,v95,v95
v_mfma_f32_16x16x32_bf16 v[40:43],a[252:255],v[56:59],v[40:43]
v_rcp_f32 v82,v82
v_mfma_f32_16x16x32_bf16 v[104:107],v[100:103],v[56:59],v[104:107]
v_fma_f32 v83,v94,v83,v93
v_mul_f32 v83,v35,v83
v_mfma_f32_16x16x32_bf16 v[108:111],v[56:59],v[56:59],v[108:111]
s_waitcnt lgkmcnt(3)
v_exp_f32 v83,v83
v_mfma_f32_16x16x32_bf16 v[36:39],a[192:195],v[60:63],v[36:39]
ds_read_b128 v[56:59],v2 offset:56320
v_fma_f32 v83,v83,v95,v95
v_mfma_f32_16x16x32_bf16 v[40:43],a[196:199],v[60:63],v[40:43]
v_rcp_f32 v83,v83
v_mfma_f32_16x16x32_bf16 v[36:39],a[200:203],v[64:67],v[36:39]
v_cndmask_b32 v112,v108,v109,s[82:83]
v_cndmask_b32 v113,v110,v111,s[82:83]
v_cndmask_b32 v112,v112,v113,s[84:85]
v_cndmask_b32 v112,v104,v112,s[98:99]
v_mfma_f32_16x16x32_bf16 v[40:43],a[204:207],v[64:67],v[40:43]
ds_read_b128 v[60:63],v2 offset:49152
ds_write_b32 v13,v112 offset:2048
buffer_load_dwordx4 v0,s[36:39],s66 offen offset:0 sc1 lds
v_mfma_f32_16x16x32_bf16 v[36:39],a[208:211],v[68:71],v[36:39]
ds_read_b128 v[64:67],v2 offset:50176
v_mul_f32 v32,v32,v80
v_mfma_f32_16x16x32_bf16 v[40:43],a[212:215],v[68:71],v[40:43]
v_mul_f32 v33,v33,v81
v_mul_f32 v34,v34,v82
v_mfma_f32_16x16x32_bf16 v[36:39],a[216:219],v[72:75],v[36:39]
ds_read_b128 v[68:71],v2 offset:51200
v_mul_f32 v35,v35,v83
v_mfma_f32_16x16x32_bf16 v[40:43],a[220:223],v[72:75],v[40:43]
s_waitcnt lgkmcnt(4)
buffer_load_dwordx4 v0,s[36:39],s66 offen offset:1024 sc1 lds
v_mfma_f32_16x16x32_bf16 v[36:39],a[160:163],v[44:47],v[36:39]
s_waitcnt vmcnt(7) lgkmcnt(2)
ds_read_b128 v[72:75],v2 offset:52224
v_xor_b32 v2,65536,v2
v_mfma_f32_16x16x32_bf16 v[40:43],a[164:167],v[44:47],v[40:43]
s_barrier
v_mfma_f32_16x16x32_bf16 v[36:39],a[168:171],v[48:51],v[36:39]
buffer_store_dword v5,v4,s[36:39],s70 offen offset:0 sc0 sc1
ds_read_b128 v[116:119],v14 offset:2048
ds_read_b128 v[120:123],v14 offset:2064
ds_read_b128 v[44:47],v2 offset:12288
v_mfma_f32_16x16x32_bf16 v[40:43],a[172:175],v[48:51],v[40:43]
v_cvt_pk_bf16_f32 v86,v32,v33
v_cvt_pk_bf16_f32 v87,v34,v35
v_mfma_f32_16x16x32_bf16 v[36:39],a[176:179],v[52:55],v[36:39]
ds_read_b128 v[48:51],v2 offset:13312
v_xor_b32 v8,16384,v8
v_mfma_f32_16x16x32_bf16 v[40:43],a[180:183],v[52:55],v[40:43]
buffer_load_dwordx4 v0,s[36:39],s66 offen offset:2048 sc1 lds
v_mfma_f32_16x16x32_bf16 v[36:39],a[184:187],v[56:59],v[36:39]
s_waitcnt lgkmcnt(2)
ds_read_b128 v[52:55],v2 offset:14336
v_add_f32 v116,v116,v117
v_mfma_f32_16x16x32_bf16 v[40:43],a[188:191],v[56:59],v[40:43]
v_add_f32 v118,v118,v119
v_add_f32 v116,v116,v118
v_mfma_f32_16x16x32_bf16 v[36:39],a[128:131],v[60:63],v[36:39]
ds_read_b128 v[56:59],v2 offset:15360
v_mul_f32 v96,s75,v116
v_mfma_f32_16x16x32_bf16 v[40:43],a[132:135],v[60:63],v[40:43]
buffer_load_dwordx4 v0,s[36:39],s66 offen offset:3072 sc1 lds
v_mfma_f32_16x16x32_bf16 v[36:39],a[136:139],v[64:67],v[36:39]
ds_read_b128 v[60:63],v2 offset:8192
v_add_f32 v120,v120,v121
v_mfma_f32_16x16x32_bf16 v[40:43],a[140:143],v[64:67],v[40:43]
v_add_f32 v122,v122,v123
v_add_f32 v120,v120,v122
v_mfma_f32_16x16x32_bf16 v[36:39],a[144:147],v[68:71],v[36:39]
ds_read_b128 v[64:67],v2 offset:9216
v_mul_f32 v120,s75,v120
v_mfma_f32_16x16x32_bf16 v[40:43],a[148:151],v[68:71],v[40:43]
buffer_store_dwordx4 v[84:87],v3,s[36:39],s73 offen offset:0 sc0 sc1
v_fma_f32 v120,-v96,v96,v120
v_max_f32 v120,0,v120
v_add_f32 v115,v15,v120
v_fma_f32 v120,v15,v115,v120
v_rsq_f32 v116,v120
v_mfma_f32_16x16x32_bf16 v[36:39],a[152:155],v[72:75],v[36:39]
s_waitcnt lgkmcnt(2)
ds_read_b128 v[68:71],v2 offset:10240
v_mul_f32 v97,s76,v116
v_mul_f32 v98,v116,v116
v_mul_f32 v99,v120,v116
v_mul_f32 v98,s77,v98
v_mul_f32 v98,v98,v97
v_mfma_f32_16x16x32_bf16 v[40:43],a[156:159],v[72:75],v[40:43]
s_cbranch_scc1 Lg1_a0w3_%=
s_cmp_ge_u32 s48,4194304
s_cselect_b64 s[90:91],s[94:95],s[90:91]
s_cselect_b64 s[92:93],s[96:97],s[92:93]
s_cmp_lt_u32 s67,s59
s_cbranch_scc1 Lg1_d0w3_%=
s_branch Lg1_slow0_%=
Lg1_d0w0_%=:
v_mfma_f32_16x16x32_bf16 v[28:31],a[0:3],v[44:47],0
s_add_u32 m0,s62,16384
ds_read_b128 v[72:75],v2 offset:7168
s_lshr_b32 s73,s47,0
v_mfma_f32_16x16x32_bf16 v[32:35],a[4:7],v[44:47],0
buffer_load_dwordx4 v1,s[36:39],s66 offen offset:0 sc1 lds
v_mfma_f32_16x16x32_bf16 v[28:31],a[8:11],v[48:51],v[28:31]
s_lshr_b32 s72,s66,6
ds_read_b128 v[44:47],v2 offset:8192
s_lshr_b32 s71,s62,2
v_mfma_f32_16x16x32_bf16 v[32:35],a[12:15],v[48:51],v[32:35]
s_cmp_lt_u32 s66,4194304
v_fma_f32 v36,-v96,v20,v36
v_fma_f32 v37,-v96,v21,v37
v_mfma_f32_16x16x32_bf16 v[28:31],a[16:19],v[52:55],v[28:31]
s_cselect_b32 s72,s59,s72
ds_read_b128 v[48:51],v2 offset:9216
v_fma_f32 v38,-v96,v22,v38
v_mfma_f32_16x16x32_bf16 v[32:35],a[20:23],v[52:55],v[32:35]
buffer_load_dwordx4 v1,s[36:39],s66 offen offset:1024 sc1 lds
v_mfma_f32_16x16x32_bf16 v[28:31],a[24:27],v[56:59],v[28:31]
ds_read_b128 v[52:55],v2 offset:10240
v_fma_f32 v39,-v96,v23,v39
v_mfma_f32_16x16x32_bf16 v[32:35],a[28:31],v[56:59],v[32:35]
s_waitcnt lgkmcnt(3)
v_mul_f32 v76,v36,v36
v_mul_f32 v77,v37,v37
v_mfma_f32_16x16x32_bf16 v[28:31],a[32:35],v[60:63],v[28:31]
ds_read_b128 v[56:59],v2 offset:11264
v_mul_f32 v78,v38,v38
v_mfma_f32_16x16x32_bf16 v[32:35],a[36:39],v[60:63],v[32:35]
buffer_load_dwordx4 v1,s[36:39],s66 offen offset:2048 sc1 lds
v_mfma_f32_16x16x32_bf16 v[28:31],a[40:43],v[64:67],v[28:31]
ds_read_b128 v[60:63],v2 offset:12288
v_mul_f32 v79,v39,v39
v_mfma_f32_16x16x32_bf16 v[32:35],a[44:47],v[64:67],v[32:35]
v_fma_f32 v76,v98,v76,v97
v_mul_f32 v76,v36,v76
v_mfma_f32_16x16x32_bf16 v[28:31],a[48:51],v[68:71],v[28:31]
ds_read_b128 v[64:67],v2 offset:13312
v_fma_f32 v77,v98,v77,v97
v_mfma_f32_16x16x32_bf16 v[32:35],a[52:55],v[68:71],v[32:35]
buffer_load_dwordx4 v1,s[36:39],s66 offen offset:3072 sc1 lds
v_mfma_f32_16x16x32_bf16 v[28:31],a[56:59],v[72:75],v[28:31]
s_add_u32 m0,s71,s88
ds_read_b128 v[68:71],v2 offset:14336
v_mul_f32 v77,v37,v77
v_mfma_f32_16x16x32_bf16 v[32:35],a[60:63],v[72:75],v[32:35]
s_waitcnt lgkmcnt(3)
buffer_load_dword v10,s[36:39],s72 offen offset:0 sc1 lds)"
R"(
v_mfma_f32_16x16x32_bf16 v[28:31],a[64:67],v[44:47],v[28:31]
s_waitcnt vmcnt(11)
ds_read_b128 v[72:75],v2 offset:15360
v_fma_f32 v78,v98,v78,v97
v_mfma_f32_16x16x32_bf16 v[32:35],a[68:71],v[44:47],v[32:35]
s_barrier
v_mfma_f32_16x16x32_bf16 v[28:31],a[72:75],v[48:51],v[28:31]
s_xor_b32 s87,s64,65536
ds_read_b128 v[44:47],v2 offset:16384
v_fma_f32 v79,v98,v79,v97
v_mfma_f32_16x16x32_bf16 v[32:35],a[76:79],v[48:51],v[32:35]
s_cmp_ge_u32 s66,s59
v_exp_f32 v76,v76
v_mfma_f32_16x16x32_bf16 v[28:31],a[80:83],v[52:55],v[28:31]
s_cselect_b32 s86,s49,s50
ds_read_b128 v[48:51],v2 offset:17408
v_mul_f32 v78,v38,v78
v_mfma_f32_16x16x32_bf16 v[32:35],a[84:87],v[52:55],v[32:35]
s_cselect_b32 s87,s87,s63
v_exp_f32 v77,v77
v_mfma_f32_16x16x32_bf16 v[28:31],a[88:91],v[56:59],v[28:31]
s_lshr_b32 s69,s50,5
ds_read_b128 v[52:55],v2 offset:18432
v_mul_f32 v79,v39,v79
v_mfma_f32_16x16x32_bf16 v[32:35],a[92:95],v[56:59],v[32:35]
s_waitcnt lgkmcnt(3)
v_exp_f32 v78,v78
v_mfma_f32_16x16x32_bf16 v[28:31],a[96:99],v[60:63],v[28:31]
s_cmp_lt_u32 s50,4194304
ds_read_b128 v[56:59],v2 offset:19456
v_fma_f32 v76,v76,v99,v99
v_mfma_f32_16x16x32_bf16 v[32:35],a[100:103],v[60:63],v[32:35]
s_cselect_b32 s68,0xffffcfff,-1
v_exp_f32 v79,v79
v_mfma_f32_16x16x32_bf16 v[28:31],a[104:107],v[64:67],v[28:31]
s_and_b32 s68,s68,s69
ds_read_b128 v[60:63],v2 offset:20480
v_fma_f32 v77,v77,v99,v99
v_mfma_f32_16x16x32_bf16 v[32:35],a[108:111],v[64:67],v[32:35]
s_lshr_b32 s69,s51,5
v_rcp_f32 v76,v76
v_mfma_f32_16x16x32_bf16 v[28:31],a[112:115],v[68:71],v[28:31]
s_cmp_lt_u32 s51,4194304
ds_read_b128 v[64:67],v2 offset:21504
v_fma_f32 v78,v78,v99,v99
v_mfma_f32_16x16x32_bf16 v[32:35],a[116:119],v[68:71],v[32:35]
s_cselect_b32 s70,0xffffcfff,-1
v_rcp_f32 v77,v77
v_mfma_f32_16x16x32_bf16 v[28:31],a[120:123],v[72:75],v[28:31]
s_and_b32 s70,s70,s69
ds_read_b128 v[68:71],v2 offset:22528
v_fma_f32 v79,v79,v99,v99
v_mfma_f32_16x16x32_bf16 v[32:35],a[124:127],v[72:75],v[32:35]
s_waitcnt lgkmcnt(3)
v_rcp_f32 v78,v78
v_mfma_f32_16x16x32_bf16 v[28:31],a[128:131],v[44:47],v[28:31]
s_waitcnt vmcnt(10)
ds_read_b128 v[72:75],v2 offset:23552
v_mul_f32 v36,v36,v76
v_mfma_f32_16x16x32_bf16 v[32:35],a[132:135],v[44:47],v[32:35]
s_barrier
v_mfma_f32_16x16x32_bf16 v[28:31],a[136:139],v[48:51],v[28:31]
s_mov_b32 s62,s87
ds_read_b128 v[44:47],v2 offset:24576
ds_read_b32 v16,v7 offset:0
v_mfma_f32_16x16x32_bf16 v[32:35],a[140:143],v[48:51],v[32:35]
v_rcp_f32 v79,v79
v_mfma_f32_16x16x32_bf16 v[28:31],a[144:147],v[52:55],v[28:31]
ds_read_b128 v[48:51],v2 offset:25600
v_mul_f32 v37,v37,v77
v_mfma_f32_16x16x32_bf16 v[32:35],a[148:151],v[52:55],v[32:35]
v_mul_f32 v38,v38,v78
v_mul_f32 v39,v39,v79
v_mfma_f32_16x16x32_bf16 v[28:31],a[152:155],v[56:59],v[28:31]
s_waitcnt lgkmcnt(1)
ds_read_b128 v[52:55],v2 offset:26624
v_cvt_pk_bf16_f32 v84,v36,v37
v_mfma_f32_16x16x32_bf16 v[32:35],a[156:159],v[56:59],v[32:35]
v_cmp_eq_u32 vcc,s60,v16
v_cvt_pk_bf16_f32 v85,v38,v39
v_fma_f32 v40,-v96,v24,v40
v_fma_f32 v41,-v96,v25,v41
v_mfma_f32_16x16x32_bf16 v[28:31],a[160:163],v[60:63],v[28:31]
s_cmp_eq_u32 vcc_lo,-1
ds_read_b128 v[56:59],v2 offset:27648
v_fma_f32 v42,-v96,v26,v42
v_mfma_f32_16x16x32_bf16 v[32:35],a[164:167],v[60:63],v[32:35]
s_cselect_b32 s67,s86,s59
s_cmp_ge_u32 s67,s59
s_cselect_b32 s68,s68,s70
s_add_u32 m0,s78,256
v_mfma_f32_16x16x32_bf16 v[28:31],a[168:171],v[64:67],v[28:31]
buffer_load_dword v6,s[36:39],s68 offen offset:0 sc1 lds
v_mfma_f32_16x16x32_bf16 v[32:35],a[172:175],v[64:67],v[32:35]
s_mov_b32 m0,s62
ds_read_b128 v[60:63],v2 offset:28672
v_fma_f32 v43,-v96,v27,v43
v_mfma_f32_16x16x32_bf16 v[28:31],a[176:179],v[68:71],v[28:31]
buffer_load_dwordx4 v0,s[36:39],s67 offen offset:0 sc1 lds
v_mfma_f32_16x16x32_bf16 v[32:35],a[180:183],v[68:71],v[32:35]
s_xor_b32 s63,s63,65536
ds_read_b128 v[64:67],v2 offset:29696
v_mul_f32 v80,v40,v40
v_mfma_f32_16x16x32_bf16 v[28:31],a[184:187],v[72:75],v[28:31]
s_add_u32 s53,s52,s55
ds_read_b128 v[68:71],v2 offset:30720
v_mul_f32 v81,v41,v41
v_mfma_f32_16x16x32_bf16 v[32:35],a[188:191],v[72:75],v[32:35]
s_waitcnt lgkmcnt(3)
v_mul_f32 v82,v42,v42
v_mul_f32 v83,v43,v43
v_mfma_f32_16x16x32_bf16 v[28:31],a[192:195],v[44:47],v[28:31]
s_waitcnt vmcnt(7)
ds_read_b128 v[72:75],v2 offset:31744
v_fma_f32 v80,v98,v80,v97
v_mfma_f32_16x16x32_bf16 v[32:35],a[196:199],v[44:47],v[32:35]
s_barrier
v_mfma_f32_16x16x32_bf16 v[28:31],a[200:203],v[48:51],v[28:31]
buffer_load_dwordx4 v0,s[36:39],s67 offen offset:1024 sc1 lds
v_mfma_f32_16x16x32_bf16 v[32:35],a[204:207],v[48:51],v[32:35]
s_cmp_eq_u32 s53,s56
ds_read_b128 v[44:47],v2 offset:32768
ds_read_b128 v[92:95],v8 offset:0
v_mfma_f32_16x16x32_bf16 v[28:31],a[208:211],v[52:55],v[28:31]
s_cselect_b32 s53,s57,s53
ds_read_b128 v[48:51],v2 offset:33792
v_mul_f32 v80,v40,v80
v_mfma_f32_16x16x32_bf16 v[32:35],a[212:215],v[52:55],v[32:35]
s_cmp_eq_u32 s53,s58
v_fma_f32 v81,v98,v81,v97
v_fma_f32 v82,v98,v82,v97
v_fma_f32 v83,v98,v83,v97
v_mul_f32 v81,v41,v81
v_mul_f32 v82,v42,v82
v_mul_f32 v83,v43,v83
v_exp_f32 v80,v80
v_exp_f32 v81,v81
v_exp_f32 v82,v82
v_exp_f32 v83,v83
v_mfma_f32_16x16x32_bf16 v[28:31],a[216:219],v[56:59],v[28:31]
buffer_load_dwordx4 v0,s[36:39],s67 offen offset:2048 sc1 lds
v_fma_f32 v80,v80,v99,v99
v_fma_f32 v81,v81,v99,v99
v_fma_f32 v82,v82,v99,v99
v_fma_f32 v83,v83,v99,v99
v_rcp_f32 v80,v80
v_rcp_f32 v81,v81
v_rcp_f32 v82,v82
v_rcp_f32 v83,v83
v_mfma_f32_16x16x32_bf16 v[32:35],a[220:223],v[56:59],v[32:35]
s_waitcnt lgkmcnt(3)
ds_read_b128 v[52:55],v2 offset:34816
v_mul_f32 v40,v40,v80
v_mul_f32 v41,v41,v81
v_mul_f32 v42,v42,v82
v_mul_f32 v43,v43,v83
v_cvt_pk_bf16_f32 v86,v40,v41
v_cvt_pk_bf16_f32 v87,v42,v43
v_mfma_f32_16x16x32_bf16 v[28:31],a[224:227],v[60:63],v[28:31]
buffer_store_dwordx4 v[84:87],v3,s[36:39],s73 offen offset:0 sc0 sc1
ds_read_b128 v[56:59],v2 offset:35840
v_mfma_f32_16x16x32_bf16 v[32:35],a[228:231],v[60:63],v[32:35]
s_waitcnt lgkmcnt(3)
s_cselect_b32 s53,s59,s53
s_cmp_ge_u32 s66,s59
v_mfma_f32_16x16x32_bf16 v[28:31],a[232:235],v[64:67],v[28:31]
buffer_load_dwordx4 v0,s[36:39],s67 offen offset:3072 sc1 lds
v_mfma_f32_16x16x32_bf16 v[32:35],a[236:239],v[64:67],v[32:35]
ds_read_b128 v[60:63],v2 offset:36864
v_mfma_f32_16x16x32_bf16 v[28:31],a[240:243],v[68:71],v[28:31]
ds_read_b128 v[64:67],v2 offset:37888
v_mfma_f32_16x16x32_bf16 v[32:35],a[244:247],v[68:71],v[32:35]
v_mfma_f32_16x16x32_bf16 v[28:31],a[248:251],v[72:75],v[28:31]
s_waitcnt lgkmcnt(2)
ds_read_b128 v[68:71],v2 offset:38912
v_mfma_f32_16x16x32_bf16 v[32:35],a[252:255],v[72:75],v[32:35]
s_cbranch_scc1 Lg1_slow1_%=
Lg1_d1w0_%=:
v_mfma_f32_16x16x32_bf16 v[36:39],a[0:3],v[44:47],0
s_add_u32 m0,s62,16384
ds_read_b128 v[72:75],v2 offset:39936
s_lshr_b32 s73,s48,0
v_mfma_f32_16x16x32_bf16 v[40:43],a[4:7],v[44:47],0
buffer_load_dwordx4 v1,s[36:39],s67 offen offset:0 sc1 lds
v_mfma_f32_16x16x32_bf16 v[36:39],a[8:11],v[48:51],v[36:39]
s_lshr_b32 s72,s67,6
ds_read_b128 v[44:47],v2 offset:40960
s_lshr_b32 s71,s62,2
v_mfma_f32_16x16x32_bf16 v[40:43],a[12:15],v[48:51],v[40:43]
s_cmp_lt_u32 s67,4194304
v_fma_f32 v28,-v92,v20,v28
v_fma_f32 v29,-v92,v21,v29
v_mfma_f32_16x16x32_bf16 v[36:39],a[16:19],v[52:55],v[36:39]
s_cselect_b32 s72,s59,s72
ds_read_b128 v[48:51],v2 offset:41984
v_fma_f32 v30,-v92,v22,v30
v_mfma_f32_16x16x32_bf16 v[40:43],a[20:23],v[52:55],v[40:43]
buffer_load_dwordx4 v1,s[36:39],s67 offen offset:1024 sc1 lds
v_mfma_f32_16x16x32_bf16 v[36:39],a[24:27],v[56:59],v[36:39]
s_mov_b64 s[44:45],s[46:47])"
R"(
ds_read_b128 v[52:55],v2 offset:43008
v_fma_f32 v31,-v92,v23,v31
v_mfma_f32_16x16x32_bf16 v[40:43],a[28:31],v[56:59],v[40:43]
s_waitcnt lgkmcnt(3)
v_mul_f32 v76,v28,v28
v_mul_f32 v77,v29,v29
v_mfma_f32_16x16x32_bf16 v[36:39],a[32:35],v[60:63],v[36:39]
s_mov_b64 s[46:47],s[48:49]
ds_read_b128 v[56:59],v2 offset:44032
v_mul_f32 v78,v30,v30
v_mfma_f32_16x16x32_bf16 v[40:43],a[36:39],v[60:63],v[40:43]
buffer_load_dwordx4 v1,s[36:39],s67 offen offset:2048 sc1 lds
v_mfma_f32_16x16x32_bf16 v[36:39],a[40:43],v[64:67],v[36:39]
s_mov_b64 s[48:49],s[50:51]
ds_read_b128 v[60:63],v2 offset:45056
v_mul_f32 v79,v31,v31
v_mfma_f32_16x16x32_bf16 v[40:43],a[44:47],v[64:67],v[40:43]
v_fma_f32 v76,v94,v76,v93
v_mul_f32 v76,v28,v76
v_mfma_f32_16x16x32_bf16 v[36:39],a[48:51],v[68:71],v[36:39]
ds_read_b128 v[64:67],v2 offset:46080
v_fma_f32 v77,v94,v77,v93
v_mfma_f32_16x16x32_bf16 v[40:43],a[52:55],v[68:71],v[40:43]
buffer_load_dwordx4 v1,s[36:39],s67 offen offset:3072 sc1 lds
v_mfma_f32_16x16x32_bf16 v[36:39],a[56:59],v[72:75],v[36:39]
s_add_u32 m0,s71,s88
ds_read_b128 v[68:71],v2 offset:47104
v_mul_f32 v77,v29,v77
v_mfma_f32_16x16x32_bf16 v[40:43],a[60:63],v[72:75],v[40:43]
s_waitcnt lgkmcnt(3)
buffer_load_dword v10,s[36:39],s72 offen offset:0 sc1 lds
v_mfma_f32_16x16x32_bf16 v[36:39],a[64:67],v[44:47],v[36:39]
s_waitcnt vmcnt(11)
ds_read_b128 v[72:75],v2 offset:48128
v_fma_f32 v78,v94,v78,v93
v_mfma_f32_16x16x32_bf16 v[40:43],a[68:71],v[44:47],v[40:43]
s_barrier
v_mfma_f32_16x16x32_bf16 v[36:39],a[72:75],v[48:51],v[36:39]
s_xor_b32 s87,s63,65536
ds_read_b128 v[44:47],v2 offset:49152
v_fma_f32 v79,v94,v79,v93
v_mfma_f32_16x16x32_bf16 v[40:43],a[76:79],v[48:51],v[40:43]
s_cmp_ge_u32 s67,s59
v_exp_f32 v76,v76
v_mfma_f32_16x16x32_bf16 v[36:39],a[80:83],v[52:55],v[36:39]
s_cselect_b32 s86,s50,s51
ds_read_b128 v[48:51],v2 offset:50176
v_mul_f32 v78,v30,v78
v_mfma_f32_16x16x32_bf16 v[40:43],a[84:87],v[52:55],v[40:43]
s_cselect_b32 s87,s87,s64
v_exp_f32 v77,v77
v_mfma_f32_16x16x32_bf16 v[36:39],a[88:91],v[56:59],v[36:39]
s_lshr_b32 s69,s51,5
ds_read_b128 v[52:55],v2 offset:51200
v_mul_f32 v79,v31,v79
v_mfma_f32_16x16x32_bf16 v[40:43],a[92:95],v[56:59],v[40:43]
s_waitcnt lgkmcnt(3)
v_exp_f32 v78,v78
v_mfma_f32_16x16x32_bf16 v[36:39],a[96:99],v[60:63],v[36:39]
s_cmp_lt_u32 s51,4194304
ds_read_b128 v[56:59],v2 offset:52224
v_fma_f32 v76,v76,v95,v95
v_mfma_f32_16x16x32_bf16 v[40:43],a[100:103],v[60:63],v[40:43]
s_cselect_b32 s68,0xffffcfff,-1
v_exp_f32 v79,v79
v_mfma_f32_16x16x32_bf16 v[36:39],a[104:107],v[64:67],v[36:39]
s_and_b32 s68,s68,s69
ds_read_b128 v[60:63],v2 offset:53248
v_fma_f32 v77,v77,v95,v95
v_mfma_f32_16x16x32_bf16 v[40:43],a[108:111],v[64:67],v[40:43]
s_lshr_b32 s69,s52,5
v_rcp_f32 v76,v76
v_mfma_f32_16x16x32_bf16 v[36:39],a[112:115],v[68:71],v[36:39]
s_cmp_lt_u32 s52,4194304
ds_read_b128 v[64:67],v2 offset:54272
v_fma_f32 v78,v78,v95,v95
v_mfma_f32_16x16x32_bf16 v[40:43],a[116:119],v[68:71],v[40:43]
s_cselect_b32 s70,0xffffcfff,-1
v_rcp_f32 v77,v77
v_mfma_f32_16x16x32_bf16 v[36:39],a[120:123],v[72:75],v[36:39]
s_and_b32 s70,s70,s69
ds_read_b128 v[68:71],v2 offset:55296
v_fma_f32 v79,v79,v95,v95
v_mfma_f32_16x16x32_bf16 v[40:43],a[124:127],v[72:75],v[40:43]
s_waitcnt lgkmcnt(3)
v_rcp_f32 v78,v78
v_mfma_f32_16x16x32_bf16 v[36:39],a[128:131],v[44:47],v[36:39]
s_waitcnt vmcnt(10)
ds_read_b128 v[72:75],v2 offset:56320
v_mul_f32 v28,v28,v76
v_mfma_f32_16x16x32_bf16 v[40:43],a[132:135],v[44:47],v[40:43]
s_barrier
v_mfma_f32_16x16x32_bf16 v[36:39],a[136:139],v[48:51],v[36:39]
s_mov_b32 s62,s87
ds_read_b128 v[44:47],v2 offset:57344
ds_read_b32 v16,v7 offset:256
v_mfma_f32_16x16x32_bf16 v[40:43],a[140:143],v[48:51],v[40:43]
s_mov_b64 s[50:51],s[52:53]
v_rcp_f32 v79,v79
v_mfma_f32_16x16x32_bf16 v[36:39],a[144:147],v[52:55],v[36:39]
ds_read_b128 v[48:51],v2 offset:58368
v_mul_f32 v29,v29,v77
v_mfma_f32_16x16x32_bf16 v[40:43],a[148:151],v[52:55],v[40:43]
v_mul_f32 v30,v30,v78
v_mul_f32 v31,v31,v79
v_mfma_f32_16x16x32_bf16 v[36:39],a[152:155],v[56:59],v[36:39]
s_waitcnt lgkmcnt(1)
ds_read_b128 v[52:55],v2 offset:59392
v_cvt_pk_bf16_f32 v84,v28,v29
v_mfma_f32_16x16x32_bf16 v[40:43],a[156:159],v[56:59],v[40:43]
v_cmp_eq_u32 vcc,s60,v16
v_cvt_pk_bf16_f32 v85,v30,v31
v_fma_f32 v32,-v92,v24,v32
v_fma_f32 v33,-v92,v25,v33
v_mfma_f32_16x16x32_bf16 v[36:39],a[160:163],v[60:63],v[36:39]
s_cmp_eq_u32 vcc_lo,-1
ds_read_b128 v[56:59],v2 offset:60416
v_fma_f32 v34,-v92,v26,v34
v_mfma_f32_16x16x32_bf16 v[40:43],a[164:167],v[60:63],v[40:43]
s_cselect_b32 s66,s86,s59
s_cmp_ge_u32 s66,s59
s_cselect_b32 s68,s68,s70
s_add_u32 m0,s78,0
v_mfma_f32_16x16x32_bf16 v[36:39],a[168:171],v[64:67],v[36:39]
buffer_load_dword v6,s[36:39],s68 offen offset:0 sc1 lds
v_mfma_f32_16x16x32_bf16 v[40:43],a[172:175],v[64:67],v[40:43]
s_mov_b32 m0,s62
ds_read_b128 v[60:63],v2 offset:61440
v_fma_f32 v35,-v92,v27,v35
v_mfma_f32_16x16x32_bf16 v[36:39],a[176:179],v[68:71],v[36:39]
buffer_load_dwordx4 v0,s[36:39],s66 offen offset:0 sc1 lds
v_mfma_f32_16x16x32_bf16 v[40:43],a[180:183],v[68:71],v[40:43]
s_xor_b32 s64,s64,65536
ds_read_b128 v[64:67],v2 offset:62464
v_mul_f32 v80,v32,v32
v_mfma_f32_16x16x32_bf16 v[36:39],a[184:187],v[72:75],v[36:39]
s_add_u32 s74,s53,s55
ds_read_b128 v[68:71],v2 offset:63488
v_mul_f32 v81,v33,v33
v_mfma_f32_16x16x32_bf16 v[40:43],a[188:191],v[72:75],v[40:43]
s_waitcnt lgkmcnt(3)
v_mul_f32 v82,v34,v34
v_mul_f32 v83,v35,v35
v_mfma_f32_16x16x32_bf16 v[36:39],a[192:195],v[44:47],v[36:39]
s_waitcnt vmcnt(7)
ds_read_b128 v[72:75],v2 offset:64512
v_xor_b32 v2,65536,v2
v_mfma_f32_16x16x32_bf16 v[40:43],a[196:199],v[44:47],v[40:43]
s_barrier
v_mfma_f32_16x16x32_bf16 v[36:39],a[200:203],v[48:51],v[36:39]
buffer_load_dwordx4 v0,s[36:39],s66 offen offset:1024 sc1 lds
v_mfma_f32_16x16x32_bf16 v[40:43],a[204:207],v[48:51],v[40:43]
s_cmp_eq_u32 s74,s56
ds_read_b128 v[44:47],v2 offset:0
ds_read_b128 v[96:99],v8 offset:8192
v_mfma_f32_16x16x32_bf16 v[36:39],a[208:211],v[52:55],v[36:39]
s_cselect_b32 s74,s57,s74
ds_read_b128 v[48:51],v2 offset:1024
v_fma_f32 v80,v94,v80,v93
v_mfma_f32_16x16x32_bf16 v[40:43],a[212:215],v[52:55],v[40:43]
s_cmp_eq_u32 s74,s58
v_fma_f32 v81,v94,v81,v93
v_fma_f32 v82,v94,v82,v93
v_fma_f32 v83,v94,v83,v93
v_mul_f32 v80,v32,v80
v_mul_f32 v81,v33,v81
v_mul_f32 v82,v34,v82
v_mul_f32 v83,v35,v83
v_exp_f32 v80,v80
v_exp_f32 v81,v81
v_exp_f32 v82,v82
v_exp_f32 v83,v83
v_mfma_f32_16x16x32_bf16 v[36:39],a[216:219],v[56:59],v[36:39]
buffer_load_dwordx4 v0,s[36:39],s66 offen offset:2048 sc1 lds
v_fma_f32 v80,v80,v95,v95
v_fma_f32 v81,v81,v95,v95
v_fma_f32 v82,v82,v95,v95
v_fma_f32 v83,v83,v95,v95
v_rcp_f32 v80,v80
v_rcp_f32 v81,v81
v_rcp_f32 v82,v82
v_rcp_f32 v83,v83
v_mfma_f32_16x16x32_bf16 v[40:43],a[220:223],v[56:59],v[40:43]
s_waitcnt lgkmcnt(3)
ds_read_b128 v[52:55],v2 offset:2048
v_mul_f32 v32,v32,v80
v_mul_f32 v33,v33,v81
v_mul_f32 v34,v34,v82
v_mul_f32 v35,v35,v83
v_cvt_pk_bf16_f32 v86,v32,v33
v_cvt_pk_bf16_f32 v87,v34,v35
v_mfma_f32_16x16x32_bf16 v[36:39],a[224:227],v[60:63],v[36:39]
buffer_store_dwordx4 v[84:87],v3,s[36:39],s73 offen offset:0 sc0 sc1
ds_read_b128 v[56:59],v2 offset:3072
v_xor_b32 v8,16384,v8
v_mfma_f32_16x16x32_bf16 v[40:43],a[228:231],v[60:63],v[40:43]
s_waitcnt lgkmcnt(3)
s_cselect_b32 s74,s59,s74
s_mov_b32 s52,s74
v_mfma_f32_16x16x32_bf16 v[36:39],a[232:235],v[64:67],v[36:39])"
R"(
buffer_load_dwordx4 v0,s[36:39],s66 offen offset:3072 sc1 lds
v_mfma_f32_16x16x32_bf16 v[40:43],a[236:239],v[64:67],v[40:43]
s_cmp_lt_u32 s67,s59
ds_read_b128 v[60:63],v2 offset:4096
v_mfma_f32_16x16x32_bf16 v[36:39],a[240:243],v[68:71],v[36:39]
ds_read_b128 v[64:67],v2 offset:5120
v_mfma_f32_16x16x32_bf16 v[40:43],a[244:247],v[68:71],v[40:43]
v_mfma_f32_16x16x32_bf16 v[36:39],a[248:251],v[72:75],v[36:39]
s_waitcnt lgkmcnt(2)
ds_read_b128 v[68:71],v2 offset:6144
v_mfma_f32_16x16x32_bf16 v[40:43],a[252:255],v[72:75],v[40:43]
s_cbranch_scc1 Lg1_d0w0_%=
s_branch Lg1_slow0_%=
Lg1_d0w1_%=:
v_mfma_f32_16x16x32_bf16 v[28:31],a[32:35],v[44:47],0
s_add_u32 m0,s62,16384
ds_read_b128 v[72:75],v2 offset:3072
s_lshr_b32 s73,s47,0
v_mfma_f32_16x16x32_bf16 v[32:35],a[36:39],v[44:47],0
s_lshr_b32 s72,s66,6
s_lshr_b32 s71,s62,2
s_cmp_lt_u32 s66,4194304
v_mfma_f32_16x16x32_bf16 v[28:31],a[40:43],v[48:51],v[28:31]
buffer_load_dwordx4 v1,s[36:39],s66 offen offset:0 sc1 lds
v_mfma_f32_16x16x32_bf16 v[32:35],a[44:47],v[48:51],v[32:35]
s_cselect_b32 s72,s59,s72
ds_read_b128 v[44:47],v2 offset:12288
v_fma_f32 v36,-v96,v20,v36
v_mfma_f32_16x16x32_bf16 v[28:31],a[48:51],v[52:55],v[28:31]
ds_read_b128 v[48:51],v2 offset:13312
v_fma_f32 v37,-v96,v21,v37
v_mfma_f32_16x16x32_bf16 v[32:35],a[52:55],v[52:55],v[32:35]
v_fma_f32 v38,-v96,v22,v38
v_fma_f32 v39,-v96,v23,v39
v_mfma_f32_16x16x32_bf16 v[28:31],a[56:59],v[56:59],v[28:31]
buffer_load_dwordx4 v1,s[36:39],s66 offen offset:1024 sc1 lds
v_mfma_f32_16x16x32_bf16 v[32:35],a[60:63],v[56:59],v[32:35]
s_waitcnt lgkmcnt(2)
ds_read_b128 v[52:55],v2 offset:14336
v_mul_f32 v76,v36,v36
v_mfma_f32_16x16x32_bf16 v[28:31],a[0:3],v[60:63],v[28:31]
ds_read_b128 v[56:59],v2 offset:15360
v_mul_f32 v77,v37,v37
v_mfma_f32_16x16x32_bf16 v[32:35],a[4:7],v[60:63],v[32:35]
v_mul_f32 v78,v38,v38
v_mul_f32 v79,v39,v39
v_mfma_f32_16x16x32_bf16 v[28:31],a[8:11],v[64:67],v[28:31]
buffer_load_dwordx4 v1,s[36:39],s66 offen offset:2048 sc1 lds
v_mfma_f32_16x16x32_bf16 v[32:35],a[12:15],v[64:67],v[32:35]
ds_read_b128 v[60:63],v2 offset:8192
v_fma_f32 v76,v98,v76,v97
v_mfma_f32_16x16x32_bf16 v[28:31],a[16:19],v[68:71],v[28:31]
ds_read_b128 v[64:67],v2 offset:9216
v_mul_f32 v76,v36,v76
v_mfma_f32_16x16x32_bf16 v[32:35],a[20:23],v[68:71],v[32:35]
v_exp_f32 v76,v76
v_mfma_f32_16x16x32_bf16 v[28:31],a[24:27],v[72:75],v[28:31]
buffer_load_dwordx4 v1,s[36:39],s66 offen offset:3072 sc1 lds
v_mfma_f32_16x16x32_bf16 v[32:35],a[28:31],v[72:75],v[32:35]
s_waitcnt lgkmcnt(2)
ds_read_b128 v[68:71],v2 offset:10240
s_add_u32 m0,s71,s88
v_mfma_f32_16x16x32_bf16 v[28:31],a[96:99],v[44:47],v[28:31]
s_xor_b32 s87,s64,65536
ds_read_b128 v[72:75],v2 offset:11264
buffer_load_dword v10,s[36:39],s72 offen offset:0 sc1 lds
s_waitcnt vmcnt(10)
v_mfma_f32_16x16x32_bf16 v[32:35],a[100:103],v[44:47],v[32:35]
s_barrier
v_mfma_f32_16x16x32_bf16 v[28:31],a[104:107],v[48:51],v[28:31]
s_cmp_ge_u32 s66,s59
ds_read_b128 v[44:47],v2 offset:20480
v_fma_f32 v76,v76,v99,v99
v_mfma_f32_16x16x32_bf16 v[32:35],a[108:111],v[48:51],v[32:35]
s_cselect_b32 s86,s49,s50
v_rcp_f32 v76,v76
v_mfma_f32_16x16x32_bf16 v[28:31],a[112:115],v[52:55],v[28:31]
s_cselect_b32 s87,s87,s63
ds_read_b128 v[48:51],v2 offset:21504
v_fma_f32 v77,v98,v77,v97
v_mfma_f32_16x16x32_bf16 v[32:35],a[116:119],v[52:55],v[32:35]
s_mov_b32 s62,s87
v_mul_f32 v77,v37,v77
v_fma_f32 v78,v98,v78,v97
v_mfma_f32_16x16x32_bf16 v[28:31],a[120:123],v[56:59],v[28:31]
s_mov_b32 m0,s62
ds_read_b128 v[52:55],v2 offset:22528
v_mul_f32 v78,v38,v78
v_mfma_f32_16x16x32_bf16 v[32:35],a[124:127],v[56:59],v[32:35]
s_waitcnt lgkmcnt(3)
v_exp_f32 v77,v77
v_mfma_f32_16x16x32_bf16 v[28:31],a[64:67],v[60:63],v[28:31]
ds_read_b128 v[56:59],v2 offset:23552
v_fma_f32 v77,v77,v99,v99
v_mfma_f32_16x16x32_bf16 v[32:35],a[68:71],v[60:63],v[32:35]
v_exp_f32 v78,v78
v_mfma_f32_16x16x32_bf16 v[28:31],a[72:75],v[64:67],v[28:31]
ds_read_b128 v[60:63],v2 offset:16384
v_fma_f32 v78,v78,v99,v99
v_mfma_f32_16x16x32_bf16 v[32:35],a[76:79],v[64:67],v[32:35]
v_rcp_f32 v77,v77
v_mfma_f32_16x16x32_bf16 v[28:31],a[80:83],v[68:71],v[28:31]
ds_read_b128 v[64:67],v2 offset:17408
v_fma_f32 v79,v98,v79,v97
v_mfma_f32_16x16x32_bf16 v[32:35],a[84:87],v[68:71],v[32:35]
v_rcp_f32 v78,v78
v_mfma_f32_16x16x32_bf16 v[28:31],a[88:91],v[72:75],v[28:31]
ds_read_b128 v[68:71],v2 offset:18432
v_mul_f32 v79,v39,v79
v_mfma_f32_16x16x32_bf16 v[32:35],a[92:95],v[72:75],v[32:35]
s_waitcnt lgkmcnt(3)
v_exp_f32 v79,v79
v_mfma_f32_16x16x32_bf16 v[28:31],a[160:163],v[44:47],v[28:31]
s_nop 0
ds_read_b128 v[72:75],v2 offset:19456
v_fma_f32 v79,v79,v99,v99
v_mfma_f32_16x16x32_bf16 v[32:35],a[164:167],v[44:47],v[32:35]
s_barrier
v_mfma_f32_16x16x32_bf16 v[28:31],a[168:171],v[48:51],v[28:31]
ds_read_b128 v[44:47],v2 offset:28672
ds_read_b32 v16,v7 offset:0
v_mfma_f32_16x16x32_bf16 v[32:35],a[172:175],v[48:51],v[32:35]
v_rcp_f32 v79,v79
v_mfma_f32_16x16x32_bf16 v[28:31],a[176:179],v[52:55],v[28:31]
ds_read_b128 v[48:51],v2 offset:29696
v_mul_f32 v36,v36,v76
v_mfma_f32_16x16x32_bf16 v[32:35],a[180:183],v[52:55],v[32:35]
v_mul_f32 v37,v37,v77
v_mul_f32 v38,v38,v78
v_mfma_f32_16x16x32_bf16 v[28:31],a[184:187],v[56:59],v[28:31]
s_waitcnt lgkmcnt(1)
ds_read_b128 v[52:55],v2 offset:30720
v_mul_f32 v39,v39,v79
v_mfma_f32_16x16x32_bf16 v[32:35],a[188:191],v[56:59],v[32:35]
v_cmp_eq_u32 vcc,s60,v16
v_cvt_pk_bf16_f32 v84,v36,v37
v_cvt_pk_bf16_f32 v85,v38,v39
v_fma_f32 v40,-v96,v24,v40
v_mfma_f32_16x16x32_bf16 v[28:31],a[128:131],v[60:63],v[28:31]
s_cmp_eq_u32 vcc_lo,-1
ds_read_b128 v[56:59],v2 offset:31744
s_cselect_b32 s67,s86,s59
v_mfma_f32_16x16x32_bf16 v[32:35],a[132:135],v[60:63],v[32:35]
buffer_load_dwordx4 v0,s[36:39],s67 offen offset:0 sc1 lds
v_mfma_f32_16x16x32_bf16 v[28:31],a[136:139],v[64:67],v[28:31]
s_xor_b32 s63,s63,65536
ds_read_b128 v[60:63],v2 offset:24576
v_fma_f32 v41,-v96,v25,v41
v_mfma_f32_16x16x32_bf16 v[32:35],a[140:143],v[64:67],v[32:35]
s_add_u32 s53,s52,s55
v_fma_f32 v42,-v96,v26,v42
v_fma_f32 v43,-v96,v27,v43
v_mfma_f32_16x16x32_bf16 v[28:31],a[144:147],v[68:71],v[28:31]
s_cmp_eq_u32 s53,s56
ds_read_b128 v[64:67],v2 offset:25600
v_mul_f32 v80,v40,v40
v_mfma_f32_16x16x32_bf16 v[32:35],a[148:151],v[68:71],v[32:35]
buffer_load_dwordx4 v0,s[36:39],s67 offen offset:1024 sc1 lds
v_mfma_f32_16x16x32_bf16 v[28:31],a[152:155],v[72:75],v[28:31]
s_cselect_b32 s53,s57,s53
ds_read_b128 v[68:71],v2 offset:26624
v_mul_f32 v81,v41,v41
v_mfma_f32_16x16x32_bf16 v[32:35],a[156:159],v[72:75],v[32:35]
s_waitcnt lgkmcnt(3)
v_mul_f32 v82,v42,v42
v_mul_f32 v83,v43,v43
v_mfma_f32_16x16x32_bf16 v[28:31],a[224:227],v[44:47],v[28:31]
s_waitcnt vmcnt(7)
ds_read_b128 v[72:75],v2 offset:27648
v_fma_f32 v80,v98,v80,v97
v_mfma_f32_16x16x32_bf16 v[32:35],a[228:231],v[44:47],v[32:35]
s_barrier
v_mfma_f32_16x16x32_bf16 v[28:31],a[232:235],v[48:51],v[28:31]
s_cmp_eq_u32 s53,s58
ds_read_b128 v[44:47],v2 offset:36864
ds_read_b128 v[92:95],v8 offset:0
v_mfma_f32_16x16x32_bf16 v[32:35],a[236:239],v[48:51],v[32:35]
buffer_load_dwordx4 v0,s[36:39],s67 offen offset:2048 sc1 lds
v_mfma_f32_16x16x32_bf16 v[28:31],a[240:243],v[52:55],v[28:31]
s_cselect_b32 s53,s59,s53
ds_read_b128 v[48:51],v2 offset:37888
v_mul_f32 v80,v40,v80
v_mfma_f32_16x16x32_bf16 v[32:35],a[244:247],v[52:55],v[32:35]
s_cmp_ge_u32 s66,s59
v_exp_f32 v80,v80
v_mfma_f32_16x16x32_bf16 v[28:31],a[248:251],v[56:59],v[28:31]
v_fma_f32 v81,v98,v81,v97
v_fma_f32 v82,v98,v82,v97
v_fma_f32 v83,v98,v83,v97
v_mul_f32 v81,v41,v81
v_mul_f32 v82,v42,v82
v_mul_f32 v83,v43,v83
v_exp_f32 v81,v81
v_exp_f32 v82,v82
v_exp_f32 v83,v83
v_mfma_f32_16x16x32_bf16 v[32:35],a[252:255],v[56:59],v[32:35]
s_waitcnt lgkmcnt(3)
ds_read_b128 v[52:55],v2 offset:38912
buffer_load_dwordx4 v0,s[36:39],s67 offen offset:3072 sc1 lds
v_fma_f32 v80,v80,v99,v99)"
R"(
v_fma_f32 v81,v81,v99,v99
v_fma_f32 v82,v82,v99,v99
v_fma_f32 v83,v83,v99,v99
v_rcp_f32 v80,v80
v_rcp_f32 v81,v81
v_rcp_f32 v82,v82
v_rcp_f32 v83,v83
v_mfma_f32_16x16x32_bf16 v[28:31],a[192:195],v[60:63],v[28:31]
s_waitcnt lgkmcnt(2)
v_mul_f32 v40,v40,v80
v_mul_f32 v41,v41,v81
v_mul_f32 v42,v42,v82
v_mul_f32 v43,v43,v83
v_cvt_pk_bf16_f32 v86,v40,v41
v_cvt_pk_bf16_f32 v87,v42,v43
v_mfma_f32_16x16x32_bf16 v[32:35],a[196:199],v[60:63],v[32:35]
ds_read_b128 v[56:59],v2 offset:39936
buffer_store_dwordx4 v[84:87],v3,s[36:39],s73 offen offset:0 sc0 sc1
v_mfma_f32_16x16x32_bf16 v[28:31],a[200:203],v[64:67],v[28:31]
ds_read_b128 v[60:63],v2 offset:32768
v_mfma_f32_16x16x32_bf16 v[32:35],a[204:207],v[64:67],v[32:35]
v_mfma_f32_16x16x32_bf16 v[28:31],a[208:211],v[68:71],v[28:31]
ds_read_b128 v[64:67],v2 offset:33792
v_mfma_f32_16x16x32_bf16 v[32:35],a[212:215],v[68:71],v[32:35]
v_mfma_f32_16x16x32_bf16 v[28:31],a[216:219],v[72:75],v[28:31]
s_waitcnt lgkmcnt(2)
ds_read_b128 v[68:71],v2 offset:34816
v_mfma_f32_16x16x32_bf16 v[32:35],a[220:223],v[72:75],v[32:35]
s_cbranch_scc1 Lg1_slow1_%=
Lg1_d1w1_%=:
v_mfma_f32_16x16x32_bf16 v[36:39],a[32:35],v[44:47],0
s_add_u32 m0,s62,16384
ds_read_b128 v[72:75],v2 offset:35840
s_lshr_b32 s73,s48,0
v_mfma_f32_16x16x32_bf16 v[40:43],a[36:39],v[44:47],0
s_lshr_b32 s72,s67,6
s_lshr_b32 s71,s62,2
s_cmp_lt_u32 s67,4194304
v_mfma_f32_16x16x32_bf16 v[36:39],a[40:43],v[48:51],v[36:39]
buffer_load_dwordx4 v1,s[36:39],s67 offen offset:0 sc1 lds
v_mfma_f32_16x16x32_bf16 v[40:43],a[44:47],v[48:51],v[40:43]
s_cselect_b32 s72,s59,s72
ds_read_b128 v[44:47],v2 offset:45056
v_fma_f32 v28,-v92,v20,v28
v_mfma_f32_16x16x32_bf16 v[36:39],a[48:51],v[52:55],v[36:39]
s_mov_b64 s[44:45],s[46:47]
ds_read_b128 v[48:51],v2 offset:46080
v_fma_f32 v29,-v92,v21,v29
v_mfma_f32_16x16x32_bf16 v[40:43],a[52:55],v[52:55],v[40:43]
s_mov_b64 s[46:47],s[48:49]
v_fma_f32 v30,-v92,v22,v30
v_fma_f32 v31,-v92,v23,v31
v_mfma_f32_16x16x32_bf16 v[36:39],a[56:59],v[56:59],v[36:39]
buffer_load_dwordx4 v1,s[36:39],s67 offen offset:1024 sc1 lds
v_mfma_f32_16x16x32_bf16 v[40:43],a[60:63],v[56:59],v[40:43]
s_waitcnt lgkmcnt(2)
ds_read_b128 v[52:55],v2 offset:47104
v_mul_f32 v76,v28,v28
v_mfma_f32_16x16x32_bf16 v[36:39],a[0:3],v[60:63],v[36:39]
s_mov_b64 s[48:49],s[50:51]
ds_read_b128 v[56:59],v2 offset:48128
v_mul_f32 v77,v29,v29
v_mfma_f32_16x16x32_bf16 v[40:43],a[4:7],v[60:63],v[40:43]
v_mul_f32 v78,v30,v30
v_mul_f32 v79,v31,v31
v_mfma_f32_16x16x32_bf16 v[36:39],a[8:11],v[64:67],v[36:39]
buffer_load_dwordx4 v1,s[36:39],s67 offen offset:2048 sc1 lds
v_mfma_f32_16x16x32_bf16 v[40:43],a[12:15],v[64:67],v[40:43]
ds_read_b128 v[60:63],v2 offset:40960
v_fma_f32 v76,v94,v76,v93
v_mfma_f32_16x16x32_bf16 v[36:39],a[16:19],v[68:71],v[36:39]
ds_read_b128 v[64:67],v2 offset:41984
v_mul_f32 v76,v28,v76
v_mfma_f32_16x16x32_bf16 v[40:43],a[20:23],v[68:71],v[40:43]
v_exp_f32 v76,v76
v_mfma_f32_16x16x32_bf16 v[36:39],a[24:27],v[72:75],v[36:39]
buffer_load_dwordx4 v1,s[36:39],s67 offen offset:3072 sc1 lds
v_mfma_f32_16x16x32_bf16 v[40:43],a[28:31],v[72:75],v[40:43]
s_waitcnt lgkmcnt(2)
ds_read_b128 v[68:71],v2 offset:43008
s_add_u32 m0,s71,s88
v_mfma_f32_16x16x32_bf16 v[36:39],a[96:99],v[44:47],v[36:39]
s_xor_b32 s87,s63,65536
ds_read_b128 v[72:75],v2 offset:44032
buffer_load_dword v10,s[36:39],s72 offen offset:0 sc1 lds
s_waitcnt vmcnt(10)
v_mfma_f32_16x16x32_bf16 v[40:43],a[100:103],v[44:47],v[40:43]
s_barrier
v_mfma_f32_16x16x32_bf16 v[36:39],a[104:107],v[48:51],v[36:39]
s_cmp_ge_u32 s67,s59
ds_read_b128 v[44:47],v2 offset:53248
v_fma_f32 v76,v76,v95,v95
v_mfma_f32_16x16x32_bf16 v[40:43],a[108:111],v[48:51],v[40:43]
s_cselect_b32 s86,s50,s51
v_rcp_f32 v76,v76
v_mfma_f32_16x16x32_bf16 v[36:39],a[112:115],v[52:55],v[36:39]
s_cselect_b32 s87,s87,s64
ds_read_b128 v[48:51],v2 offset:54272
v_fma_f32 v77,v94,v77,v93
v_mfma_f32_16x16x32_bf16 v[40:43],a[116:119],v[52:55],v[40:43]
s_mov_b32 s62,s87
v_mul_f32 v77,v29,v77
v_fma_f32 v78,v94,v78,v93
v_mfma_f32_16x16x32_bf16 v[36:39],a[120:123],v[56:59],v[36:39]
s_mov_b32 m0,s62
ds_read_b128 v[52:55],v2 offset:55296
v_mul_f32 v78,v30,v78
v_mfma_f32_16x16x32_bf16 v[40:43],a[124:127],v[56:59],v[40:43]
s_waitcnt lgkmcnt(3)
v_exp_f32 v77,v77
v_mfma_f32_16x16x32_bf16 v[36:39],a[64:67],v[60:63],v[36:39]
s_mov_b64 s[50:51],s[52:53]
ds_read_b128 v[56:59],v2 offset:56320
v_fma_f32 v77,v77,v95,v95
v_mfma_f32_16x16x32_bf16 v[40:43],a[68:71],v[60:63],v[40:43]
v_exp_f32 v78,v78
v_mfma_f32_16x16x32_bf16 v[36:39],a[72:75],v[64:67],v[36:39]
ds_read_b128 v[60:63],v2 offset:49152
v_fma_f32 v78,v78,v95,v95
v_mfma_f32_16x16x32_bf16 v[40:43],a[76:79],v[64:67],v[40:43]
v_rcp_f32 v77,v77
v_mfma_f32_16x16x32_bf16 v[36:39],a[80:83],v[68:71],v[36:39]
ds_read_b128 v[64:67],v2 offset:50176
v_fma_f32 v79,v94,v79,v93
v_mfma_f32_16x16x32_bf16 v[40:43],a[84:87],v[68:71],v[40:43]
v_rcp_f32 v78,v78
v_mfma_f32_16x16x32_bf16 v[36:39],a[88:91],v[72:75],v[36:39]
ds_read_b128 v[68:71],v2 offset:51200
v_mul_f32 v79,v31,v79
v_mfma_f32_16x16x32_bf16 v[40:43],a[92:95],v[72:75],v[40:43]
s_waitcnt lgkmcnt(3)
v_exp_f32 v79,v79
v_mfma_f32_16x16x32_bf16 v[36:39],a[160:163],v[44:47],v[36:39]
s_nop 0
ds_read_b128 v[72:75],v2 offset:52224
v_fma_f32 v79,v79,v95,v95
v_mfma_f32_16x16x32_bf16 v[40:43],a[164:167],v[44:47],v[40:43]
s_barrier
v_mfma_f32_16x16x32_bf16 v[36:39],a[168:171],v[48:51],v[36:39]
ds_read_b128 v[44:47],v2 offset:61440
ds_read_b32 v16,v7 offset:256
v_mfma_f32_16x16x32_bf16 v[40:43],a[172:175],v[48:51],v[40:43]
v_rcp_f32 v79,v79
v_mfma_f32_16x16x32_bf16 v[36:39],a[176:179],v[52:55],v[36:39]
ds_read_b128 v[48:51],v2 offset:62464
v_mul_f32 v28,v28,v76
v_mfma_f32_16x16x32_bf16 v[40:43],a[180:183],v[52:55],v[40:43]
v_mul_f32 v29,v29,v77
v_mul_f32 v30,v30,v78
v_mfma_f32_16x16x32_bf16 v[36:39],a[184:187],v[56:59],v[36:39]
s_waitcnt lgkmcnt(1)
ds_read_b128 v[52:55],v2 offset:63488
v_mul_f32 v31,v31,v79
v_mfma_f32_16x16x32_bf16 v[40:43],a[188:191],v[56:59],v[40:43]
v_cmp_eq_u32 vcc,s60,v16
v_cvt_pk_bf16_f32 v84,v28,v29
v_cvt_pk_bf16_f32 v85,v30,v31
v_fma_f32 v32,-v92,v24,v32
v_mfma_f32_16x16x32_bf16 v[36:39],a[128:131],v[60:63],v[36:39]
s_cmp_eq_u32 vcc_lo,-1
ds_read_b128 v[56:59],v2 offset:64512
s_cselect_b32 s66,s86,s59
v_mfma_f32_16x16x32_bf16 v[40:43],a[132:135],v[60:63],v[40:43]
buffer_load_dwordx4 v0,s[36:39],s66 offen offset:0 sc1 lds
v_mfma_f32_16x16x32_bf16 v[36:39],a[136:139],v[64:67],v[36:39]
s_xor_b32 s64,s64,65536
ds_read_b128 v[60:63],v2 offset:57344
v_fma_f32 v33,-v92,v25,v33
v_mfma_f32_16x16x32_bf16 v[40:43],a[140:143],v[64:67],v[40:43]
s_add_u32 s74,s53,s55
v_fma_f32 v34,-v92,v26,v34
v_fma_f32 v35,-v92,v27,v35
v_mfma_f32_16x16x32_bf16 v[36:39],a[144:147],v[68:71],v[36:39]
s_cmp_eq_u32 s74,s56
ds_read_b128 v[64:67],v2 offset:58368
v_mul_f32 v80,v32,v32
v_mfma_f32_16x16x32_bf16 v[40:43],a[148:151],v[68:71],v[40:43]
buffer_load_dwordx4 v0,s[36:39],s66 offen offset:1024 sc1 lds
v_mfma_f32_16x16x32_bf16 v[36:39],a[152:155],v[72:75],v[36:39]
s_cselect_b32 s74,s57,s74
ds_read_b128 v[68:71],v2 offset:59392
v_mul_f32 v81,v33,v33
v_mfma_f32_16x16x32_bf16 v[40:43],a[156:159],v[72:75],v[40:43]
s_waitcnt lgkmcnt(3)
v_mul_f32 v82,v34,v34
v_mul_f32 v83,v35,v35
v_mfma_f32_16x16x32_bf16 v[36:39],a[224:227],v[44:47],v[36:39]
s_waitcnt vmcnt(7)
ds_read_b128 v[72:75],v2 offset:60416
v_xor_b32 v2,65536,v2
v_mfma_f32_16x16x32_bf16 v[40:43],a[228:231],v[44:47],v[40:43]
s_barrier
v_mfma_f32_16x16x32_bf16 v[36:39],a[232:235],v[48:51],v[36:39]
s_cmp_eq_u32 s74,s58
ds_read_b128 v[44:47],v2 offset:4096
ds_read_b128 v[96:99],v8 offset:8192
v_mfma_f32_16x16x32_bf16 v[40:43],a[236:239],v[48:51],v[40:43]
buffer_load_dwordx4 v0,s[36:39],s66 offen offset:2048 sc1 lds
v_mfma_f32_16x16x32_bf16 v[36:39],a[240:243],v[52:55],v[36:39]
s_cselect_b32 s74,s59,s74)"
R"(
ds_read_b128 v[48:51],v2 offset:5120
v_fma_f32 v80,v94,v80,v93
v_mfma_f32_16x16x32_bf16 v[40:43],a[244:247],v[52:55],v[40:43]
s_mov_b32 s52,s74
v_mul_f32 v80,v32,v80
v_fma_f32 v81,v94,v81,v93
v_mfma_f32_16x16x32_bf16 v[36:39],a[248:251],v[56:59],v[36:39]
s_cmp_lt_u32 s67,s59
v_fma_f32 v82,v94,v82,v93
v_fma_f32 v83,v94,v83,v93
v_mul_f32 v81,v33,v81
v_mul_f32 v82,v34,v82
v_mul_f32 v83,v35,v83
v_exp_f32 v80,v80
v_exp_f32 v81,v81
v_exp_f32 v82,v82
v_exp_f32 v83,v83
v_mfma_f32_16x16x32_bf16 v[40:43],a[252:255],v[56:59],v[40:43]
s_waitcnt lgkmcnt(3)
ds_read_b128 v[52:55],v2 offset:6144
buffer_load_dwordx4 v0,s[36:39],s66 offen offset:3072 sc1 lds
v_fma_f32 v80,v80,v95,v95
v_fma_f32 v81,v81,v95,v95
v_fma_f32 v82,v82,v95,v95
v_fma_f32 v83,v83,v95,v95
v_rcp_f32 v80,v80
v_rcp_f32 v81,v81
v_rcp_f32 v82,v82
v_rcp_f32 v83,v83
v_mfma_f32_16x16x32_bf16 v[36:39],a[192:195],v[60:63],v[36:39]
s_waitcnt lgkmcnt(2)
v_mul_f32 v32,v32,v80
v_mul_f32 v33,v33,v81
v_mul_f32 v34,v34,v82
v_mul_f32 v35,v35,v83
v_cvt_pk_bf16_f32 v86,v32,v33
v_cvt_pk_bf16_f32 v87,v34,v35
v_mfma_f32_16x16x32_bf16 v[40:43],a[196:199],v[60:63],v[40:43]
ds_read_b128 v[56:59],v2 offset:7168
buffer_store_dwordx4 v[84:87],v3,s[36:39],s73 offen offset:0 sc0 sc1
v_xor_b32 v8,16384,v8
v_mfma_f32_16x16x32_bf16 v[36:39],a[200:203],v[64:67],v[36:39]
ds_read_b128 v[60:63],v2 offset:0
v_mfma_f32_16x16x32_bf16 v[40:43],a[204:207],v[64:67],v[40:43]
v_mfma_f32_16x16x32_bf16 v[36:39],a[208:211],v[68:71],v[36:39]
ds_read_b128 v[64:67],v2 offset:1024
v_mfma_f32_16x16x32_bf16 v[40:43],a[212:215],v[68:71],v[40:43]
v_mfma_f32_16x16x32_bf16 v[36:39],a[216:219],v[72:75],v[36:39]
s_waitcnt lgkmcnt(2)
ds_read_b128 v[68:71],v2 offset:2048
v_mfma_f32_16x16x32_bf16 v[40:43],a[220:223],v[72:75],v[40:43]
s_cbranch_scc1 Lg1_d0w1_%=
s_branch Lg1_slow0_%=
Lg1_d0w2_%=:
v_mfma_f32_16x16x32_bf16 v[28:31],a[64:67],v[44:47],0
s_add_u32 m0,s62,16384
ds_read_b128 v[72:75],v2 offset:15360
s_lshr_b32 s73,s47,0
v_mfma_f32_16x16x32_bf16 v[32:35],a[68:71],v[44:47],0
s_lshr_b32 s72,s66,6
s_lshr_b32 s71,s62,2
s_cmp_lt_u32 s66,4194304
v_mfma_f32_16x16x32_bf16 v[28:31],a[72:75],v[48:51],v[28:31]
s_cselect_b32 s72,s59,s72
ds_read_b128 v[44:47],v2 offset:0
v_mfma_f32_16x16x32_bf16 v[32:35],a[76:79],v[48:51],v[32:35]
buffer_load_dwordx4 v1,s[36:39],s66 offen offset:0 sc1 lds
v_mfma_f32_16x16x32_bf16 v[28:31],a[80:83],v[52:55],v[28:31]
ds_read_b128 v[48:51],v2 offset:1024
v_fma_f32 v36,-v96,v20,v36
v_mfma_f32_16x16x32_bf16 v[32:35],a[84:87],v[52:55],v[32:35]
v_fma_f32 v37,-v96,v21,v37
v_fma_f32 v38,-v96,v22,v38
v_mfma_f32_16x16x32_bf16 v[28:31],a[88:91],v[56:59],v[28:31]
ds_read_b128 v[52:55],v2 offset:2048
v_fma_f32 v39,-v96,v23,v39
v_mfma_f32_16x16x32_bf16 v[32:35],a[92:95],v[56:59],v[32:35]
s_waitcnt lgkmcnt(3)
buffer_load_dwordx4 v1,s[36:39],s66 offen offset:1024 sc1 lds
v_mfma_f32_16x16x32_bf16 v[28:31],a[96:99],v[60:63],v[28:31]
ds_read_b128 v[56:59],v2 offset:3072
v_mul_f32 v76,v36,v36
v_mfma_f32_16x16x32_bf16 v[32:35],a[100:103],v[60:63],v[32:35]
v_mul_f32 v77,v37,v37
v_mul_f32 v78,v38,v38
v_mfma_f32_16x16x32_bf16 v[28:31],a[104:107],v[64:67],v[28:31]
ds_read_b128 v[60:63],v2 offset:4096
v_mul_f32 v79,v39,v39
v_mfma_f32_16x16x32_bf16 v[32:35],a[108:111],v[64:67],v[32:35]
buffer_load_dwordx4 v1,s[36:39],s66 offen offset:2048 sc1 lds
v_mfma_f32_16x16x32_bf16 v[28:31],a[112:115],v[68:71],v[28:31]
ds_read_b128 v[64:67],v2 offset:5120
v_fma_f32 v76,v98,v76,v97
v_mfma_f32_16x16x32_bf16 v[32:35],a[116:119],v[68:71],v[32:35]
v_mul_f32 v76,v36,v76
v_fma_f32 v77,v98,v77,v97
v_mfma_f32_16x16x32_bf16 v[28:31],a[120:123],v[72:75],v[28:31]
ds_read_b128 v[68:71],v2 offset:6144
v_mul_f32 v77,v37,v77
v_mfma_f32_16x16x32_bf16 v[32:35],a[124:127],v[72:75],v[32:35]
s_waitcnt lgkmcnt(3)
buffer_load_dwordx4 v1,s[36:39],s66 offen offset:3072 sc1 lds
s_add_u32 m0,s71,s88
v_mfma_f32_16x16x32_bf16 v[28:31],a[0:3],v[44:47],v[28:31]
s_xor_b32 s87,s64,65536
ds_read_b128 v[72:75],v2 offset:7168
buffer_load_dword v10,s[36:39],s72 offen offset:0 sc1 lds
s_waitcnt vmcnt(10)
v_mfma_f32_16x16x32_bf16 v[32:35],a[4:7],v[44:47],v[32:35]
s_barrier
v_mfma_f32_16x16x32_bf16 v[28:31],a[8:11],v[48:51],v[28:31]
s_cmp_ge_u32 s66,s59
ds_read_b128 v[44:47],v2 offset:24576
v_fma_f32 v78,v98,v78,v97
v_mfma_f32_16x16x32_bf16 v[32:35],a[12:15],v[48:51],v[32:35]
s_cselect_b32 s86,s49,s50
v_exp_f32 v76,v76
v_mfma_f32_16x16x32_bf16 v[28:31],a[16:19],v[52:55],v[28:31]
s_cselect_b32 s87,s87,s63
ds_read_b128 v[48:51],v2 offset:25600
v_mul_f32 v78,v38,v78
v_mfma_f32_16x16x32_bf16 v[32:35],a[20:23],v[52:55],v[32:35]
s_mov_b32 s62,s87
v_exp_f32 v77,v77
v_mfma_f32_16x16x32_bf16 v[28:31],a[24:27],v[56:59],v[28:31]
s_mov_b32 m0,s62
ds_read_b128 v[52:55],v2 offset:26624
v_fma_f32 v76,v76,v99,v99
v_mfma_f32_16x16x32_bf16 v[32:35],a[28:31],v[56:59],v[32:35]
s_waitcnt lgkmcnt(3)
v_exp_f32 v78,v78
v_mfma_f32_16x16x32_bf16 v[28:31],a[32:35],v[60:63],v[28:31]
ds_read_b128 v[56:59],v2 offset:27648
v_fma_f32 v77,v77,v99,v99
v_mfma_f32_16x16x32_bf16 v[32:35],a[36:39],v[60:63],v[32:35]
v_rcp_f32 v76,v76
v_mfma_f32_16x16x32_bf16 v[28:31],a[40:43],v[64:67],v[28:31]
ds_read_b128 v[60:63],v2 offset:28672
v_fma_f32 v78,v78,v99,v99
v_mfma_f32_16x16x32_bf16 v[32:35],a[44:47],v[64:67],v[32:35]
v_rcp_f32 v77,v77
v_mfma_f32_16x16x32_bf16 v[28:31],a[48:51],v[68:71],v[28:31]
ds_read_b128 v[64:67],v2 offset:29696
v_fma_f32 v79,v98,v79,v97
v_mfma_f32_16x16x32_bf16 v[32:35],a[52:55],v[68:71],v[32:35]
v_rcp_f32 v78,v78
v_mfma_f32_16x16x32_bf16 v[28:31],a[56:59],v[72:75],v[28:31]
ds_read_b128 v[68:71],v2 offset:30720
v_mul_f32 v79,v39,v79
v_mfma_f32_16x16x32_bf16 v[32:35],a[60:63],v[72:75],v[32:35]
s_waitcnt lgkmcnt(3)
v_exp_f32 v79,v79
v_mfma_f32_16x16x32_bf16 v[28:31],a[192:195],v[44:47],v[28:31]
s_nop 0
ds_read_b128 v[72:75],v2 offset:31744
v_fma_f32 v79,v79,v99,v99
v_mfma_f32_16x16x32_bf16 v[32:35],a[196:199],v[44:47],v[32:35]
s_barrier
v_mfma_f32_16x16x32_bf16 v[28:31],a[200:203],v[48:51],v[28:31]
ds_read_b128 v[44:47],v2 offset:16384
ds_read_b32 v16,v7 offset:0
v_mfma_f32_16x16x32_bf16 v[32:35],a[204:207],v[48:51],v[32:35]
v_rcp_f32 v79,v79
v_mfma_f32_16x16x32_bf16 v[28:31],a[208:211],v[52:55],v[28:31]
ds_read_b128 v[48:51],v2 offset:17408
v_mul_f32 v36,v36,v76
v_mfma_f32_16x16x32_bf16 v[32:35],a[212:215],v[52:55],v[32:35]
v_mul_f32 v37,v37,v77
v_mul_f32 v38,v38,v78
v_mfma_f32_16x16x32_bf16 v[28:31],a[216:219],v[56:59],v[28:31]
s_waitcnt lgkmcnt(1)
ds_read_b128 v[52:55],v2 offset:18432
v_mul_f32 v39,v39,v79
v_mfma_f32_16x16x32_bf16 v[32:35],a[220:223],v[56:59],v[32:35]
v_cmp_eq_u32 vcc,s60,v16
v_cvt_pk_bf16_f32 v84,v36,v37
v_cvt_pk_bf16_f32 v85,v38,v39
v_fma_f32 v40,-v96,v24,v40
v_mfma_f32_16x16x32_bf16 v[28:31],a[224:227],v[60:63],v[28:31]
s_cmp_eq_u32 vcc_lo,-1
ds_read_b128 v[56:59],v2 offset:19456
v_fma_f32 v41,-v96,v25,v41
v_mfma_f32_16x16x32_bf16 v[32:35],a[228:231],v[60:63],v[32:35]
s_cselect_b32 s67,s86,s59
v_fma_f32 v42,-v96,v26,v42
v_fma_f32 v43,-v96,v27,v43
v_mfma_f32_16x16x32_bf16 v[28:31],a[232:235],v[64:67],v[28:31]
buffer_load_dwordx4 v0,s[36:39],s67 offen offset:0 sc1 lds
v_mfma_f32_16x16x32_bf16 v[32:35],a[236:239],v[64:67],v[32:35]
s_xor_b32 s63,s63,65536
ds_read_b128 v[60:63],v2 offset:20480
v_mul_f32 v80,v40,v40
v_mfma_f32_16x16x32_bf16 v[28:31],a[240:243],v[68:71],v[28:31]
s_add_u32 s53,s52,s55
ds_read_b128 v[64:67],v2 offset:21504
v_mul_f32 v81,v41,v41
v_mfma_f32_16x16x32_bf16 v[32:35],a[244:247],v[68:71],v[32:35]
s_cmp_eq_u32 s53,s56
v_mul_f32 v82,v42,v42
v_mul_f32 v83,v43,v43)"
R"(
v_mfma_f32_16x16x32_bf16 v[28:31],a[248:251],v[72:75],v[28:31]
buffer_load_dwordx4 v0,s[36:39],s67 offen offset:1024 sc1 lds
v_mfma_f32_16x16x32_bf16 v[32:35],a[252:255],v[72:75],v[32:35]
s_waitcnt lgkmcnt(2)
ds_read_b128 v[68:71],v2 offset:22528
v_fma_f32 v80,v98,v80,v97
v_mfma_f32_16x16x32_bf16 v[28:31],a[128:131],v[44:47],v[28:31]
s_waitcnt vmcnt(7)
ds_read_b128 v[72:75],v2 offset:23552
v_mul_f32 v80,v40,v80
v_mfma_f32_16x16x32_bf16 v[32:35],a[132:135],v[44:47],v[32:35]
s_barrier
v_mfma_f32_16x16x32_bf16 v[28:31],a[136:139],v[48:51],v[28:31]
s_cselect_b32 s53,s57,s53
ds_read_b128 v[44:47],v2 offset:40960
ds_read_b128 v[92:95],v8 offset:0
v_mfma_f32_16x16x32_bf16 v[32:35],a[140:143],v[48:51],v[32:35]
s_cmp_eq_u32 s53,s58
v_exp_f32 v80,v80
v_mfma_f32_16x16x32_bf16 v[28:31],a[144:147],v[52:55],v[28:31]
buffer_load_dwordx4 v0,s[36:39],s67 offen offset:2048 sc1 lds
v_mfma_f32_16x16x32_bf16 v[32:35],a[148:151],v[52:55],v[32:35]
s_cselect_b32 s53,s59,s53
ds_read_b128 v[48:51],v2 offset:41984
v_fma_f32 v80,v80,v99,v99
v_mfma_f32_16x16x32_bf16 v[28:31],a[152:155],v[56:59],v[28:31]
s_cmp_ge_u32 s66,s59
ds_read_b128 v[52:55],v2 offset:43008
v_fma_f32 v81,v98,v81,v97
v_mfma_f32_16x16x32_bf16 v[32:35],a[156:159],v[56:59],v[32:35]
s_waitcnt lgkmcnt(4)
v_fma_f32 v82,v98,v82,v97
v_fma_f32 v83,v98,v83,v97
v_mul_f32 v81,v41,v81
v_mul_f32 v82,v42,v82
v_mul_f32 v83,v43,v83
v_exp_f32 v81,v81
v_exp_f32 v82,v82
v_exp_f32 v83,v83
v_mfma_f32_16x16x32_bf16 v[28:31],a[160:163],v[60:63],v[28:31]
s_waitcnt lgkmcnt(2)
buffer_load_dwordx4 v0,s[36:39],s67 offen offset:3072 sc1 lds
v_fma_f32 v81,v81,v99,v99
v_fma_f32 v82,v82,v99,v99
v_fma_f32 v83,v83,v99,v99
v_rcp_f32 v80,v80
v_rcp_f32 v81,v81
v_rcp_f32 v82,v82
v_rcp_f32 v83,v83
v_mfma_f32_16x16x32_bf16 v[32:35],a[164:167],v[60:63],v[32:35]
ds_read_b128 v[56:59],v2 offset:44032
v_mul_f32 v40,v40,v80
v_mul_f32 v41,v41,v81
v_mul_f32 v42,v42,v82
v_mul_f32 v43,v43,v83
v_cvt_pk_bf16_f32 v86,v40,v41
v_cvt_pk_bf16_f32 v87,v42,v43
v_mfma_f32_16x16x32_bf16 v[28:31],a[168:171],v[64:67],v[28:31]
buffer_store_dwordx4 v[84:87],v3,s[36:39],s73 offen offset:0 sc0 sc1
ds_read_b128 v[60:63],v2 offset:45056
v_mfma_f32_16x16x32_bf16 v[32:35],a[172:175],v[64:67],v[32:35]
v_mfma_f32_16x16x32_bf16 v[28:31],a[176:179],v[68:71],v[28:31]
ds_read_b128 v[64:67],v2 offset:46080
v_mfma_f32_16x16x32_bf16 v[32:35],a[180:183],v[68:71],v[32:35]
v_mfma_f32_16x16x32_bf16 v[28:31],a[184:187],v[72:75],v[28:31]
s_waitcnt lgkmcnt(2)
ds_read_b128 v[68:71],v2 offset:47104
v_mfma_f32_16x16x32_bf16 v[32:35],a[188:191],v[72:75],v[32:35]
s_cbranch_scc1 Lg1_slow1_%=
Lg1_d1w2_%=:
v_mfma_f32_16x16x32_bf16 v[36:39],a[64:67],v[44:47],0
s_add_u32 m0,s62,16384
ds_read_b128 v[72:75],v2 offset:48128
s_lshr_b32 s73,s48,0
v_mfma_f32_16x16x32_bf16 v[40:43],a[68:71],v[44:47],0
s_lshr_b32 s72,s67,6
s_lshr_b32 s71,s62,2
s_cmp_lt_u32 s67,4194304
v_mfma_f32_16x16x32_bf16 v[36:39],a[72:75],v[48:51],v[36:39]
s_cselect_b32 s72,s59,s72
ds_read_b128 v[44:47],v2 offset:32768
s_mov_b64 s[44:45],s[46:47]
v_mfma_f32_16x16x32_bf16 v[40:43],a[76:79],v[48:51],v[40:43]
buffer_load_dwordx4 v1,s[36:39],s67 offen offset:0 sc1 lds
v_mfma_f32_16x16x32_bf16 v[36:39],a[80:83],v[52:55],v[36:39]
s_mov_b64 s[46:47],s[48:49]
ds_read_b128 v[48:51],v2 offset:33792
v_fma_f32 v28,-v92,v20,v28
v_mfma_f32_16x16x32_bf16 v[40:43],a[84:87],v[52:55],v[40:43]
s_mov_b64 s[48:49],s[50:51]
v_fma_f32 v29,-v92,v21,v29
v_fma_f32 v30,-v92,v22,v30
v_mfma_f32_16x16x32_bf16 v[36:39],a[88:91],v[56:59],v[36:39]
ds_read_b128 v[52:55],v2 offset:34816
v_fma_f32 v31,-v92,v23,v31
v_mfma_f32_16x16x32_bf16 v[40:43],a[92:95],v[56:59],v[40:43]
s_waitcnt lgkmcnt(3)
buffer_load_dwordx4 v1,s[36:39],s67 offen offset:1024 sc1 lds
v_mfma_f32_16x16x32_bf16 v[36:39],a[96:99],v[60:63],v[36:39]
ds_read_b128 v[56:59],v2 offset:35840
v_mul_f32 v76,v28,v28
v_mfma_f32_16x16x32_bf16 v[40:43],a[100:103],v[60:63],v[40:43]
v_mul_f32 v77,v29,v29
v_mul_f32 v78,v30,v30
v_mfma_f32_16x16x32_bf16 v[36:39],a[104:107],v[64:67],v[36:39]
ds_read_b128 v[60:63],v2 offset:36864
v_mul_f32 v79,v31,v31
v_mfma_f32_16x16x32_bf16 v[40:43],a[108:111],v[64:67],v[40:43]
buffer_load_dwordx4 v1,s[36:39],s67 offen offset:2048 sc1 lds
v_mfma_f32_16x16x32_bf16 v[36:39],a[112:115],v[68:71],v[36:39]
ds_read_b128 v[64:67],v2 offset:37888
v_fma_f32 v76,v94,v76,v93
v_mfma_f32_16x16x32_bf16 v[40:43],a[116:119],v[68:71],v[40:43]
v_mul_f32 v76,v28,v76
v_fma_f32 v77,v94,v77,v93
v_mfma_f32_16x16x32_bf16 v[36:39],a[120:123],v[72:75],v[36:39]
ds_read_b128 v[68:71],v2 offset:38912
v_mul_f32 v77,v29,v77
v_mfma_f32_16x16x32_bf16 v[40:43],a[124:127],v[72:75],v[40:43]
s_waitcnt lgkmcnt(3)
buffer_load_dwordx4 v1,s[36:39],s67 offen offset:3072 sc1 lds
s_add_u32 m0,s71,s88
v_mfma_f32_16x16x32_bf16 v[36:39],a[0:3],v[44:47],v[36:39]
s_xor_b32 s87,s63,65536
ds_read_b128 v[72:75],v2 offset:39936
buffer_load_dword v10,s[36:39],s72 offen offset:0 sc1 lds
s_waitcnt vmcnt(10)
v_mfma_f32_16x16x32_bf16 v[40:43],a[4:7],v[44:47],v[40:43]
s_barrier
v_mfma_f32_16x16x32_bf16 v[36:39],a[8:11],v[48:51],v[36:39]
s_cmp_ge_u32 s67,s59
ds_read_b128 v[44:47],v2 offset:57344
v_fma_f32 v78,v94,v78,v93
v_mfma_f32_16x16x32_bf16 v[40:43],a[12:15],v[48:51],v[40:43]
s_cselect_b32 s86,s50,s51
v_exp_f32 v76,v76
v_mfma_f32_16x16x32_bf16 v[36:39],a[16:19],v[52:55],v[36:39]
s_cselect_b32 s87,s87,s64
ds_read_b128 v[48:51],v2 offset:58368
v_mul_f32 v78,v30,v78
v_mfma_f32_16x16x32_bf16 v[40:43],a[20:23],v[52:55],v[40:43]
s_mov_b32 s62,s87
v_exp_f32 v77,v77
v_mfma_f32_16x16x32_bf16 v[36:39],a[24:27],v[56:59],v[36:39]
s_mov_b32 m0,s62
ds_read_b128 v[52:55],v2 offset:59392
v_fma_f32 v76,v76,v95,v95
v_mfma_f32_16x16x32_bf16 v[40:43],a[28:31],v[56:59],v[40:43]
s_waitcnt lgkmcnt(3)
v_exp_f32 v78,v78
v_mfma_f32_16x16x32_bf16 v[36:39],a[32:35],v[60:63],v[36:39]
s_mov_b64 s[50:51],s[52:53]
ds_read_b128 v[56:59],v2 offset:60416
v_fma_f32 v77,v77,v95,v95
v_mfma_f32_16x16x32_bf16 v[40:43],a[36:39],v[60:63],v[40:43]
v_rcp_f32 v76,v76
v_mfma_f32_16x16x32_bf16 v[36:39],a[40:43],v[64:67],v[36:39]
ds_read_b128 v[60:63],v2 offset:61440
v_fma_f32 v78,v78,v95,v95
v_mfma_f32_16x16x32_bf16 v[40:43],a[44:47],v[64:67],v[40:43]
v_rcp_f32 v77,v77
v_mfma_f32_16x16x32_bf16 v[36:39],a[48:51],v[68:71],v[36:39]
ds_read_b128 v[64:67],v2 offset:62464
v_fma_f32 v79,v94,v79,v93
v_mfma_f32_16x16x32_bf16 v[40:43],a[52:55],v[68:71],v[40:43]
v_rcp_f32 v78,v78
v_mfma_f32_16x16x32_bf16 v[36:39],a[56:59],v[72:75],v[36:39]
ds_read_b128 v[68:71],v2 offset:63488
v_mul_f32 v79,v31,v79
v_mfma_f32_16x16x32_bf16 v[40:43],a[60:63],v[72:75],v[40:43]
s_waitcnt lgkmcnt(3)
v_exp_f32 v79,v79
v_mfma_f32_16x16x32_bf16 v[36:39],a[192:195],v[44:47],v[36:39]
s_nop 0
ds_read_b128 v[72:75],v2 offset:64512
v_fma_f32 v79,v79,v95,v95
v_mfma_f32_16x16x32_bf16 v[40:43],a[196:199],v[44:47],v[40:43]
s_barrier
v_mfma_f32_16x16x32_bf16 v[36:39],a[200:203],v[48:51],v[36:39]
ds_read_b128 v[44:47],v2 offset:49152
ds_read_b32 v16,v7 offset:256
v_mfma_f32_16x16x32_bf16 v[40:43],a[204:207],v[48:51],v[40:43]
v_rcp_f32 v79,v79
v_mfma_f32_16x16x32_bf16 v[36:39],a[208:211],v[52:55],v[36:39]
ds_read_b128 v[48:51],v2 offset:50176
v_mul_f32 v28,v28,v76
v_mfma_f32_16x16x32_bf16 v[40:43],a[212:215],v[52:55],v[40:43]
v_mul_f32 v29,v29,v77
v_mul_f32 v30,v30,v78
v_mfma_f32_16x16x32_bf16 v[36:39],a[216:219],v[56:59],v[36:39]
s_waitcnt lgkmcnt(1)
ds_read_b128 v[52:55],v2 offset:51200
v_mul_f32 v31,v31,v79
v_mfma_f32_16x16x32_bf16 v[40:43],a[220:223],v[56:59],v[40:43]
v_cmp_eq_u32 vcc,s60,v16
v_cvt_pk_bf16_f32 v84,v28,v29
v_cvt_pk_bf16_f32 v85,v30,v31
v_fma_f32 v32,-v92,v24,v32
v_mfma_f32_16x16x32_bf16 v[36:39],a[224:227],v[60:63],v[36:39]
s_cmp_eq_u32 vcc_lo,-1)"
R"(
ds_read_b128 v[56:59],v2 offset:52224
v_fma_f32 v33,-v92,v25,v33
v_mfma_f32_16x16x32_bf16 v[40:43],a[228:231],v[60:63],v[40:43]
s_cselect_b32 s66,s86,s59
v_fma_f32 v34,-v92,v26,v34
v_fma_f32 v35,-v92,v27,v35
v_mfma_f32_16x16x32_bf16 v[36:39],a[232:235],v[64:67],v[36:39]
buffer_load_dwordx4 v0,s[36:39],s66 offen offset:0 sc1 lds
v_mfma_f32_16x16x32_bf16 v[40:43],a[236:239],v[64:67],v[40:43]
s_xor_b32 s64,s64,65536
ds_read_b128 v[60:63],v2 offset:53248
v_mul_f32 v80,v32,v32
v_mfma_f32_16x16x32_bf16 v[36:39],a[240:243],v[68:71],v[36:39]
s_add_u32 s74,s53,s55
ds_read_b128 v[64:67],v2 offset:54272
v_mul_f32 v81,v33,v33
v_mfma_f32_16x16x32_bf16 v[40:43],a[244:247],v[68:71],v[40:43]
s_cmp_eq_u32 s74,s56
v_mul_f32 v82,v34,v34
v_mul_f32 v83,v35,v35
v_mfma_f32_16x16x32_bf16 v[36:39],a[248:251],v[72:75],v[36:39]
buffer_load_dwordx4 v0,s[36:39],s66 offen offset:1024 sc1 lds
v_mfma_f32_16x16x32_bf16 v[40:43],a[252:255],v[72:75],v[40:43]
s_waitcnt lgkmcnt(2)
ds_read_b128 v[68:71],v2 offset:55296
v_fma_f32 v80,v94,v80,v93
v_mfma_f32_16x16x32_bf16 v[36:39],a[128:131],v[44:47],v[36:39]
s_waitcnt vmcnt(7)
ds_read_b128 v[72:75],v2 offset:56320
v_xor_b32 v2,65536,v2
v_mfma_f32_16x16x32_bf16 v[40:43],a[132:135],v[44:47],v[40:43]
s_barrier
v_mfma_f32_16x16x32_bf16 v[36:39],a[136:139],v[48:51],v[36:39]
s_cselect_b32 s74,s57,s74
ds_read_b128 v[44:47],v2 offset:8192
ds_read_b128 v[96:99],v8 offset:8192
v_mfma_f32_16x16x32_bf16 v[40:43],a[140:143],v[48:51],v[40:43]
s_cmp_eq_u32 s74,s58
v_mul_f32 v80,v32,v80
v_fma_f32 v81,v94,v81,v93
v_mfma_f32_16x16x32_bf16 v[36:39],a[144:147],v[52:55],v[36:39]
buffer_load_dwordx4 v0,s[36:39],s66 offen offset:2048 sc1 lds
v_mfma_f32_16x16x32_bf16 v[40:43],a[148:151],v[52:55],v[40:43]
s_cselect_b32 s74,s59,s74
ds_read_b128 v[48:51],v2 offset:9216
v_mul_f32 v81,v33,v81
v_mfma_f32_16x16x32_bf16 v[36:39],a[152:155],v[56:59],v[36:39]
s_mov_b32 s52,s74
ds_read_b128 v[52:55],v2 offset:10240
v_fma_f32 v82,v94,v82,v93
v_mfma_f32_16x16x32_bf16 v[40:43],a[156:159],v[56:59],v[40:43]
s_waitcnt lgkmcnt(4)
v_fma_f32 v83,v94,v83,v93
v_mul_f32 v82,v34,v82
v_mul_f32 v83,v35,v83
v_exp_f32 v80,v80
v_exp_f32 v81,v81
v_exp_f32 v82,v82
v_exp_f32 v83,v83
v_mfma_f32_16x16x32_bf16 v[36:39],a[160:163],v[60:63],v[36:39]
s_waitcnt lgkmcnt(2)
buffer_load_dwordx4 v0,s[36:39],s66 offen offset:3072 sc1 lds
v_fma_f32 v80,v80,v95,v95
v_fma_f32 v81,v81,v95,v95
v_fma_f32 v82,v82,v95,v95
v_fma_f32 v83,v83,v95,v95
v_rcp_f32 v80,v80
v_rcp_f32 v81,v81
v_rcp_f32 v82,v82
v_rcp_f32 v83,v83
v_mfma_f32_16x16x32_bf16 v[40:43],a[164:167],v[60:63],v[40:43]
s_cmp_lt_u32 s67,s59
ds_read_b128 v[56:59],v2 offset:11264
v_mul_f32 v32,v32,v80
v_mul_f32 v33,v33,v81
v_mul_f32 v34,v34,v82
v_mul_f32 v35,v35,v83
v_cvt_pk_bf16_f32 v86,v32,v33
v_cvt_pk_bf16_f32 v87,v34,v35
v_mfma_f32_16x16x32_bf16 v[36:39],a[168:171],v[64:67],v[36:39]
buffer_store_dwordx4 v[84:87],v3,s[36:39],s73 offen offset:0 sc0 sc1
ds_read_b128 v[60:63],v2 offset:12288
v_xor_b32 v8,16384,v8
v_mfma_f32_16x16x32_bf16 v[40:43],a[172:175],v[64:67],v[40:43]
v_mfma_f32_16x16x32_bf16 v[36:39],a[176:179],v[68:71],v[36:39]
ds_read_b128 v[64:67],v2 offset:13312
v_mfma_f32_16x16x32_bf16 v[40:43],a[180:183],v[68:71],v[40:43]
v_mfma_f32_16x16x32_bf16 v[36:39],a[184:187],v[72:75],v[36:39]
s_waitcnt lgkmcnt(2)
ds_read_b128 v[68:71],v2 offset:14336
v_mfma_f32_16x16x32_bf16 v[40:43],a[188:191],v[72:75],v[40:43]
s_cbranch_scc1 Lg1_d0w2_%=
s_branch Lg1_slow0_%=
Lg1_d0w3_%=:
v_mfma_f32_16x16x32_bf16 v[28:31],a[96:99],v[44:47],0
s_add_u32 m0,s62,16384
ds_read_b128 v[72:75],v2 offset:11264
s_lshr_b32 s73,s47,0
v_mfma_f32_16x16x32_bf16 v[32:35],a[100:103],v[44:47],0
s_lshr_b32 s72,s66,6
s_lshr_b32 s71,s62,2
s_cmp_lt_u32 s66,4194304
v_mfma_f32_16x16x32_bf16 v[28:31],a[104:107],v[48:51],v[28:31]
s_cselect_b32 s72,s59,s72
ds_read_b128 v[44:47],v2 offset:4096
v_mfma_f32_16x16x32_bf16 v[32:35],a[108:111],v[48:51],v[32:35]
v_fma_f32 v36,-v96,v20,v36
v_fma_f32 v37,-v96,v21,v37
v_mfma_f32_16x16x32_bf16 v[28:31],a[112:115],v[52:55],v[28:31]
buffer_load_dwordx4 v1,s[36:39],s66 offen offset:0 sc1 lds
v_mfma_f32_16x16x32_bf16 v[32:35],a[116:119],v[52:55],v[32:35]
ds_read_b128 v[48:51],v2 offset:5120
v_fma_f32 v38,-v96,v22,v38
v_mfma_f32_16x16x32_bf16 v[28:31],a[120:123],v[56:59],v[28:31]
ds_read_b128 v[52:55],v2 offset:6144
v_fma_f32 v39,-v96,v23,v39
v_mfma_f32_16x16x32_bf16 v[32:35],a[124:127],v[56:59],v[32:35]
s_waitcnt lgkmcnt(3)
v_mul_f32 v76,v36,v36
v_mul_f32 v77,v37,v37
v_mfma_f32_16x16x32_bf16 v[28:31],a[64:67],v[60:63],v[28:31]
buffer_load_dwordx4 v1,s[36:39],s66 offen offset:1024 sc1 lds
v_mfma_f32_16x16x32_bf16 v[32:35],a[68:71],v[60:63],v[32:35]
ds_read_b128 v[56:59],v2 offset:7168
v_mul_f32 v78,v38,v38
v_mfma_f32_16x16x32_bf16 v[28:31],a[72:75],v[64:67],v[28:31]
ds_read_b128 v[60:63],v2 offset:0
v_mul_f32 v79,v39,v39
v_mfma_f32_16x16x32_bf16 v[32:35],a[76:79],v[64:67],v[32:35]
v_fma_f32 v76,v98,v76,v97
v_mul_f32 v76,v36,v76
v_mfma_f32_16x16x32_bf16 v[28:31],a[80:83],v[68:71],v[28:31]
buffer_load_dwordx4 v1,s[36:39],s66 offen offset:2048 sc1 lds
v_mfma_f32_16x16x32_bf16 v[32:35],a[84:87],v[68:71],v[32:35]
ds_read_b128 v[64:67],v2 offset:1024
v_fma_f32 v77,v98,v77,v97
v_mfma_f32_16x16x32_bf16 v[28:31],a[88:91],v[72:75],v[28:31]
ds_read_b128 v[68:71],v2 offset:2048
v_mul_f32 v77,v37,v77
v_mfma_f32_16x16x32_bf16 v[32:35],a[92:95],v[72:75],v[32:35]
s_waitcnt lgkmcnt(3)
v_exp_f32 v76,v76
v_mfma_f32_16x16x32_bf16 v[28:31],a[32:35],v[44:47],v[28:31]
s_waitcnt vmcnt(9)
ds_read_b128 v[72:75],v2 offset:3072
v_fma_f32 v76,v76,v99,v99
v_mfma_f32_16x16x32_bf16 v[32:35],a[36:39],v[44:47],v[32:35]
s_barrier
v_mfma_f32_16x16x32_bf16 v[28:31],a[40:43],v[48:51],v[28:31]
buffer_load_dwordx4 v1,s[36:39],s66 offen offset:3072 sc1 lds
v_mfma_f32_16x16x32_bf16 v[32:35],a[44:47],v[48:51],v[32:35]
s_add_u32 m0,s71,s88
ds_read_b128 v[44:47],v2 offset:28672
v_fma_f32 v78,v98,v78,v97
v_mfma_f32_16x16x32_bf16 v[28:31],a[48:51],v[52:55],v[28:31]
buffer_load_dword v10,s[36:39],s72 offen offset:0 sc1 lds
v_mfma_f32_16x16x32_bf16 v[32:35],a[52:55],v[52:55],v[32:35]
s_xor_b32 s87,s64,65536
ds_read_b128 v[48:51],v2 offset:29696
v_fma_f32 v79,v98,v79,v97
v_mfma_f32_16x16x32_bf16 v[28:31],a[56:59],v[56:59],v[28:31]
s_cmp_ge_u32 s66,s59
ds_read_b128 v[52:55],v2 offset:30720
v_mul_f32 v78,v38,v78
v_mfma_f32_16x16x32_bf16 v[32:35],a[60:63],v[56:59],v[32:35]
s_waitcnt lgkmcnt(3)
v_exp_f32 v77,v77
v_mfma_f32_16x16x32_bf16 v[28:31],a[0:3],v[60:63],v[28:31]
s_cselect_b32 s86,s49,s50
ds_read_b128 v[56:59],v2 offset:31744
v_mul_f32 v79,v39,v79
v_mfma_f32_16x16x32_bf16 v[32:35],a[4:7],v[60:63],v[32:35]
s_cselect_b32 s87,s87,s63
v_exp_f32 v78,v78
v_mfma_f32_16x16x32_bf16 v[28:31],a[8:11],v[64:67],v[28:31]
s_mov_b32 s62,s87
ds_read_b128 v[60:63],v2 offset:24576
v_fma_f32 v77,v77,v99,v99
v_mfma_f32_16x16x32_bf16 v[32:35],a[12:15],v[64:67],v[32:35]
s_mov_b32 m0,s62
v_exp_f32 v79,v79
v_mfma_f32_16x16x32_bf16 v[28:31],a[16:19],v[68:71],v[28:31]
ds_read_b128 v[64:67],v2 offset:25600
v_fma_f32 v78,v78,v99,v99
v_mfma_f32_16x16x32_bf16 v[32:35],a[20:23],v[68:71],v[32:35]
v_rcp_f32 v76,v76
v_mfma_f32_16x16x32_bf16 v[28:31],a[24:27],v[72:75],v[28:31]
ds_read_b128 v[68:71],v2 offset:26624
v_fma_f32 v79,v79,v99,v99
v_mfma_f32_16x16x32_bf16 v[32:35],a[28:31],v[72:75],v[32:35]
s_waitcnt lgkmcnt(3)
v_rcp_f32 v77,v77
v_mfma_f32_16x16x32_bf16 v[28:31],a[224:227],v[44:47],v[28:31]
s_nop 0
ds_read_b128 v[72:75],v2 offset:27648
v_mul_f32 v36,v36,v76
v_mfma_f32_16x16x32_bf16 v[32:35],a[228:231],v[44:47],v[32:35]
s_barrier
v_mfma_f32_16x16x32_bf16 v[28:31],a[232:235],v[48:51],v[28:31]
ds_read_b128 v[44:47],v2 offset:20480)"
R"(
ds_read_b32 v16,v7 offset:0
v_mfma_f32_16x16x32_bf16 v[32:35],a[236:239],v[48:51],v[32:35]
v_rcp_f32 v78,v78
v_mfma_f32_16x16x32_bf16 v[28:31],a[240:243],v[52:55],v[28:31]
ds_read_b128 v[48:51],v2 offset:21504
v_mul_f32 v37,v37,v77
v_mfma_f32_16x16x32_bf16 v[32:35],a[244:247],v[52:55],v[32:35]
v_rcp_f32 v79,v79
v_mfma_f32_16x16x32_bf16 v[28:31],a[248:251],v[56:59],v[28:31]
s_waitcnt lgkmcnt(1)
ds_read_b128 v[52:55],v2 offset:22528
v_mul_f32 v38,v38,v78
v_mfma_f32_16x16x32_bf16 v[32:35],a[252:255],v[56:59],v[32:35]
v_cmp_eq_u32 vcc,s60,v16
v_mul_f32 v39,v39,v79
v_cvt_pk_bf16_f32 v84,v36,v37
v_cvt_pk_bf16_f32 v85,v38,v39
v_mfma_f32_16x16x32_bf16 v[28:31],a[192:195],v[60:63],v[28:31]
s_cmp_eq_u32 vcc_lo,-1
ds_read_b128 v[56:59],v2 offset:23552
v_fma_f32 v40,-v96,v24,v40
v_mfma_f32_16x16x32_bf16 v[32:35],a[196:199],v[60:63],v[32:35]
s_cselect_b32 s67,s86,s59
v_fma_f32 v41,-v96,v25,v41
v_fma_f32 v42,-v96,v26,v42
v_mfma_f32_16x16x32_bf16 v[28:31],a[200:203],v[64:67],v[28:31]
s_lshr_b32 s70,s46,5
ds_read_b128 v[60:63],v2 offset:16384
v_fma_f32 v43,-v96,v27,v43
v_mfma_f32_16x16x32_bf16 v[32:35],a[204:207],v[64:67],v[32:35]
buffer_load_dwordx4 v0,s[36:39],s67 offen offset:0 sc1 lds
v_mfma_f32_16x16x32_bf16 v[28:31],a[208:211],v[68:71],v[28:31]
s_xor_b32 s63,s63,65536
ds_read_b128 v[64:67],v2 offset:17408
v_mul_f32 v80,v40,v40
v_mfma_f32_16x16x32_bf16 v[32:35],a[212:215],v[68:71],v[32:35]
s_add_u32 s53,s52,s55
v_mul_f32 v81,v41,v41
v_mul_f32 v82,v42,v42
v_mfma_f32_16x16x32_bf16 v[28:31],a[216:219],v[72:75],v[28:31]
s_cmp_eq_u32 s53,s56
ds_read_b128 v[68:71],v2 offset:18432
v_mul_f32 v83,v43,v43
v_mfma_f32_16x16x32_bf16 v[32:35],a[220:223],v[72:75],v[32:35]
s_waitcnt lgkmcnt(3)
buffer_load_dwordx4 v0,s[36:39],s67 offen offset:1024 sc1 lds
v_mfma_f32_16x16x32_bf16 v[28:31],a[160:163],v[44:47],v[28:31]
s_waitcnt vmcnt(7)
ds_read_b128 v[72:75],v2 offset:19456
v_fma_f32 v80,v98,v80,v97
v_mfma_f32_16x16x32_bf16 v[32:35],a[164:167],v[44:47],v[32:35]
s_barrier
v_mfma_f32_16x16x32_bf16 v[28:31],a[168:171],v[48:51],v[28:31]
buffer_store_dword v5,v4,s[36:39],s70 offen offset:0 sc0 sc1
ds_read_b128 v[44:47],v2 offset:45056
ds_read_b128 v[92:95],v8 offset:0
v_mul_f32 v80,v40,v80
v_mfma_f32_16x16x32_bf16 v[32:35],a[172:175],v[48:51],v[32:35]
s_cselect_b32 s53,s57,s53
v_exp_f32 v80,v80
v_mfma_f32_16x16x32_bf16 v[28:31],a[176:179],v[52:55],v[28:31]
s_cmp_eq_u32 s53,s58
ds_read_b128 v[48:51],v2 offset:46080
v_fma_f32 v80,v80,v99,v99
v_mfma_f32_16x16x32_bf16 v[32:35],a[180:183],v[52:55],v[32:35]
buffer_load_dwordx4 v0,s[36:39],s67 offen offset:2048 sc1 lds
v_mfma_f32_16x16x32_bf16 v[28:31],a[184:187],v[56:59],v[28:31]
s_cselect_b32 s53,s59,s53
ds_read_b128 v[52:55],v2 offset:47104
v_fma_f32 v81,v98,v81,v97
v_mfma_f32_16x16x32_bf16 v[32:35],a[188:191],v[56:59],v[32:35]
s_waitcnt lgkmcnt(4)
v_rcp_f32 v80,v80
v_mfma_f32_16x16x32_bf16 v[28:31],a[128:131],v[60:63],v[28:31]
s_waitcnt lgkmcnt(2)
v_fma_f32 v82,v98,v82,v97
v_fma_f32 v83,v98,v83,v97
v_mul_f32 v81,v41,v81
v_mul_f32 v82,v42,v82
v_mul_f32 v83,v43,v83
v_exp_f32 v81,v81
v_exp_f32 v82,v82
v_exp_f32 v83,v83
v_mfma_f32_16x16x32_bf16 v[32:35],a[132:135],v[60:63],v[32:35]
s_cmp_ge_u32 s66,s59
ds_read_b128 v[56:59],v2 offset:48128
buffer_load_dwordx4 v0,s[36:39],s67 offen offset:3072 sc1 lds
v_fma_f32 v81,v81,v99,v99
v_fma_f32 v82,v82,v99,v99
v_fma_f32 v83,v83,v99,v99
v_rcp_f32 v81,v81
v_rcp_f32 v82,v82
v_rcp_f32 v83,v83
v_mfma_f32_16x16x32_bf16 v[28:31],a[136:139],v[64:67],v[28:31]
v_mul_f32 v40,v40,v80
v_mul_f32 v41,v41,v81
v_mul_f32 v42,v42,v82
v_mul_f32 v43,v43,v83
v_cvt_pk_bf16_f32 v86,v40,v41
v_cvt_pk_bf16_f32 v87,v42,v43
v_mfma_f32_16x16x32_bf16 v[32:35],a[140:143],v[64:67],v[32:35]
ds_read_b128 v[60:63],v2 offset:40960
buffer_store_dwordx4 v[84:87],v3,s[36:39],s73 offen offset:0 sc0 sc1
v_mfma_f32_16x16x32_bf16 v[28:31],a[144:147],v[68:71],v[28:31]
ds_read_b128 v[64:67],v2 offset:41984
v_mfma_f32_16x16x32_bf16 v[32:35],a[148:151],v[68:71],v[32:35]
v_mfma_f32_16x16x32_bf16 v[28:31],a[152:155],v[72:75],v[28:31]
s_waitcnt lgkmcnt(2)
ds_read_b128 v[68:71],v2 offset:43008
v_mfma_f32_16x16x32_bf16 v[32:35],a[156:159],v[72:75],v[32:35]
s_cbranch_scc1 Lg1_slow1_%=
Lg1_d1w3_%=:
v_mfma_f32_16x16x32_bf16 v[36:39],a[96:99],v[44:47],0
s_add_u32 m0,s62,16384
ds_read_b128 v[72:75],v2 offset:44032
s_lshr_b32 s73,s48,0
v_mfma_f32_16x16x32_bf16 v[40:43],a[100:103],v[44:47],0
s_lshr_b32 s72,s67,6
s_lshr_b32 s71,s62,2
s_cmp_lt_u32 s67,4194304
v_mfma_f32_16x16x32_bf16 v[36:39],a[104:107],v[48:51],v[36:39]
s_cselect_b32 s72,s59,s72
ds_read_b128 v[44:47],v2 offset:36864
s_mov_b64 s[44:45],s[46:47]
v_mfma_f32_16x16x32_bf16 v[40:43],a[108:111],v[48:51],v[40:43]
v_fma_f32 v28,-v92,v20,v28
v_fma_f32 v29,-v92,v21,v29
v_mfma_f32_16x16x32_bf16 v[36:39],a[112:115],v[52:55],v[36:39]
buffer_load_dwordx4 v1,s[36:39],s67 offen offset:0 sc1 lds
v_mfma_f32_16x16x32_bf16 v[40:43],a[116:119],v[52:55],v[40:43]
ds_read_b128 v[48:51],v2 offset:37888
v_fma_f32 v30,-v92,v22,v30
v_mfma_f32_16x16x32_bf16 v[36:39],a[120:123],v[56:59],v[36:39]
ds_read_b128 v[52:55],v2 offset:38912
v_fma_f32 v31,-v92,v23,v31
v_mfma_f32_16x16x32_bf16 v[40:43],a[124:127],v[56:59],v[40:43]
s_waitcnt lgkmcnt(3)
v_mul_f32 v76,v28,v28
v_mul_f32 v77,v29,v29
v_mfma_f32_16x16x32_bf16 v[36:39],a[64:67],v[60:63],v[36:39]
buffer_load_dwordx4 v1,s[36:39],s67 offen offset:1024 sc1 lds
v_mfma_f32_16x16x32_bf16 v[40:43],a[68:71],v[60:63],v[40:43]
ds_read_b128 v[56:59],v2 offset:39936
v_mul_f32 v78,v30,v30
v_mfma_f32_16x16x32_bf16 v[36:39],a[72:75],v[64:67],v[36:39]
ds_read_b128 v[60:63],v2 offset:32768
v_mul_f32 v79,v31,v31
v_mfma_f32_16x16x32_bf16 v[40:43],a[76:79],v[64:67],v[40:43]
v_fma_f32 v76,v94,v76,v93
v_mul_f32 v76,v28,v76
v_mfma_f32_16x16x32_bf16 v[36:39],a[80:83],v[68:71],v[36:39]
buffer_load_dwordx4 v1,s[36:39],s67 offen offset:2048 sc1 lds
v_mfma_f32_16x16x32_bf16 v[40:43],a[84:87],v[68:71],v[40:43]
ds_read_b128 v[64:67],v2 offset:33792
v_fma_f32 v77,v94,v77,v93
v_mfma_f32_16x16x32_bf16 v[36:39],a[88:91],v[72:75],v[36:39]
ds_read_b128 v[68:71],v2 offset:34816
v_mul_f32 v77,v29,v77
v_mfma_f32_16x16x32_bf16 v[40:43],a[92:95],v[72:75],v[40:43]
s_waitcnt lgkmcnt(3)
v_exp_f32 v76,v76
v_mfma_f32_16x16x32_bf16 v[36:39],a[32:35],v[44:47],v[36:39]
s_waitcnt vmcnt(9)
ds_read_b128 v[72:75],v2 offset:35840
v_fma_f32 v76,v76,v95,v95
v_mfma_f32_16x16x32_bf16 v[40:43],a[36:39],v[44:47],v[40:43]
s_barrier
v_mfma_f32_16x16x32_bf16 v[36:39],a[40:43],v[48:51],v[36:39]
buffer_load_dwordx4 v1,s[36:39],s67 offen offset:3072 sc1 lds
v_mfma_f32_16x16x32_bf16 v[40:43],a[44:47],v[48:51],v[40:43]
s_add_u32 m0,s71,s88
ds_read_b128 v[44:47],v2 offset:61440
v_fma_f32 v78,v94,v78,v93
v_mfma_f32_16x16x32_bf16 v[36:39],a[48:51],v[52:55],v[36:39]
buffer_load_dword v10,s[36:39],s72 offen offset:0 sc1 lds
v_mfma_f32_16x16x32_bf16 v[40:43],a[52:55],v[52:55],v[40:43]
s_xor_b32 s87,s63,65536
ds_read_b128 v[48:51],v2 offset:62464
v_fma_f32 v79,v94,v79,v93
v_mfma_f32_16x16x32_bf16 v[36:39],a[56:59],v[56:59],v[36:39]
s_cmp_ge_u32 s67,s59
ds_read_b128 v[52:55],v2 offset:63488
v_mul_f32 v78,v30,v78
v_mfma_f32_16x16x32_bf16 v[40:43],a[60:63],v[56:59],v[40:43]
s_waitcnt lgkmcnt(3)
v_exp_f32 v77,v77
v_mfma_f32_16x16x32_bf16 v[36:39],a[0:3],v[60:63],v[36:39]
s_cselect_b32 s86,s50,s51
ds_read_b128 v[56:59],v2 offset:64512
v_mul_f32 v79,v31,v79
v_mfma_f32_16x16x32_bf16 v[40:43],a[4:7],v[60:63],v[40:43]
s_cselect_b32 s87,s87,s64
v_exp_f32 v78,v78
v_mfma_f32_16x16x32_bf16 v[36:39],a[8:11],v[64:67],v[36:39]
s_mov_b32 s62,s87
ds_read_b128 v[60:63],v2 offset:57344
v_fma_f32 v77,v77,v95,v95
v_mfma_f32_16x16x32_bf16 v[40:43],a[12:15],v[64:67],v[40:43]
s_mov_b32 m0,s62
v_exp_f32 v79,v79)"
R"(
v_mfma_f32_16x16x32_bf16 v[36:39],a[16:19],v[68:71],v[36:39]
ds_read_b128 v[64:67],v2 offset:58368
v_fma_f32 v78,v78,v95,v95
v_mfma_f32_16x16x32_bf16 v[40:43],a[20:23],v[68:71],v[40:43]
v_rcp_f32 v76,v76
v_mfma_f32_16x16x32_bf16 v[36:39],a[24:27],v[72:75],v[36:39]
ds_read_b128 v[68:71],v2 offset:59392
v_fma_f32 v79,v79,v95,v95
v_mfma_f32_16x16x32_bf16 v[40:43],a[28:31],v[72:75],v[40:43]
s_waitcnt lgkmcnt(3)
v_rcp_f32 v77,v77
v_mfma_f32_16x16x32_bf16 v[36:39],a[224:227],v[44:47],v[36:39]
s_nop 0
ds_read_b128 v[72:75],v2 offset:60416
v_mul_f32 v28,v28,v76
v_mfma_f32_16x16x32_bf16 v[40:43],a[228:231],v[44:47],v[40:43]
s_barrier
v_mfma_f32_16x16x32_bf16 v[36:39],a[232:235],v[48:51],v[36:39]
ds_read_b128 v[44:47],v2 offset:53248
ds_read_b32 v16,v7 offset:256
v_mfma_f32_16x16x32_bf16 v[40:43],a[236:239],v[48:51],v[40:43]
v_rcp_f32 v78,v78
v_mfma_f32_16x16x32_bf16 v[36:39],a[240:243],v[52:55],v[36:39]
ds_read_b128 v[48:51],v2 offset:54272
v_mul_f32 v29,v29,v77
v_mfma_f32_16x16x32_bf16 v[40:43],a[244:247],v[52:55],v[40:43]
v_rcp_f32 v79,v79
v_mfma_f32_16x16x32_bf16 v[36:39],a[248:251],v[56:59],v[36:39]
s_waitcnt lgkmcnt(1)
ds_read_b128 v[52:55],v2 offset:55296
v_mul_f32 v30,v30,v78
v_mfma_f32_16x16x32_bf16 v[40:43],a[252:255],v[56:59],v[40:43]
v_cmp_eq_u32 vcc,s60,v16
v_mul_f32 v31,v31,v79
v_cvt_pk_bf16_f32 v84,v28,v29
v_cvt_pk_bf16_f32 v85,v30,v31
v_mfma_f32_16x16x32_bf16 v[36:39],a[192:195],v[60:63],v[36:39]
s_cmp_eq_u32 vcc_lo,-1
ds_read_b128 v[56:59],v2 offset:56320
v_fma_f32 v32,-v92,v24,v32
v_mfma_f32_16x16x32_bf16 v[40:43],a[196:199],v[60:63],v[40:43]
s_cselect_b32 s66,s86,s59
v_fma_f32 v33,-v92,v25,v33
v_fma_f32 v34,-v92,v26,v34
v_mfma_f32_16x16x32_bf16 v[36:39],a[200:203],v[64:67],v[36:39]
s_lshr_b32 s70,s47,5
ds_read_b128 v[60:63],v2 offset:49152
v_fma_f32 v35,-v92,v27,v35
v_mfma_f32_16x16x32_bf16 v[40:43],a[204:207],v[64:67],v[40:43]
buffer_load_dwordx4 v0,s[36:39],s66 offen offset:0 sc1 lds
v_mfma_f32_16x16x32_bf16 v[36:39],a[208:211],v[68:71],v[36:39]
s_xor_b32 s64,s64,65536
ds_read_b128 v[64:67],v2 offset:50176
v_mul_f32 v80,v32,v32
v_mfma_f32_16x16x32_bf16 v[40:43],a[212:215],v[68:71],v[40:43]
s_add_u32 s74,s53,s55
v_mul_f32 v81,v33,v33
v_mul_f32 v82,v34,v34
v_mfma_f32_16x16x32_bf16 v[36:39],a[216:219],v[72:75],v[36:39]
s_cmp_eq_u32 s74,s56
ds_read_b128 v[68:71],v2 offset:51200
v_mul_f32 v83,v35,v35
v_mfma_f32_16x16x32_bf16 v[40:43],a[220:223],v[72:75],v[40:43]
s_waitcnt lgkmcnt(3)
buffer_load_dwordx4 v0,s[36:39],s66 offen offset:1024 sc1 lds
v_mfma_f32_16x16x32_bf16 v[36:39],a[160:163],v[44:47],v[36:39]
s_waitcnt vmcnt(7)
ds_read_b128 v[72:75],v2 offset:52224
v_xor_b32 v2,65536,v2
v_mfma_f32_16x16x32_bf16 v[40:43],a[164:167],v[44:47],v[40:43]
s_barrier
v_mfma_f32_16x16x32_bf16 v[36:39],a[168:171],v[48:51],v[36:39]
buffer_store_dword v5,v4,s[36:39],s70 offen offset:0 sc0 sc1
ds_read_b128 v[44:47],v2 offset:12288
ds_read_b128 v[96:99],v8 offset:8192
v_fma_f32 v80,v94,v80,v93
v_mfma_f32_16x16x32_bf16 v[40:43],a[172:175],v[48:51],v[40:43]
s_cselect_b32 s74,s57,s74
v_mul_f32 v80,v32,v80
v_fma_f32 v81,v94,v81,v93
v_mfma_f32_16x16x32_bf16 v[36:39],a[176:179],v[52:55],v[36:39]
s_cmp_eq_u32 s74,s58
ds_read_b128 v[48:51],v2 offset:13312
v_mul_f32 v81,v33,v81
v_mfma_f32_16x16x32_bf16 v[40:43],a[180:183],v[52:55],v[40:43]
buffer_load_dwordx4 v0,s[36:39],s66 offen offset:2048 sc1 lds
v_mfma_f32_16x16x32_bf16 v[36:39],a[184:187],v[56:59],v[36:39]
s_cselect_b32 s74,s59,s74
ds_read_b128 v[52:55],v2 offset:14336
v_fma_f32 v82,v94,v82,v93
v_mfma_f32_16x16x32_bf16 v[40:43],a[188:191],v[56:59],v[40:43]
s_waitcnt lgkmcnt(4)
v_exp_f32 v80,v80
v_mfma_f32_16x16x32_bf16 v[36:39],a[128:131],v[60:63],v[36:39]
s_waitcnt lgkmcnt(2)
v_fma_f32 v83,v94,v83,v93
v_mul_f32 v82,v34,v82
v_mul_f32 v83,v35,v83
v_exp_f32 v81,v81
v_exp_f32 v82,v82
v_exp_f32 v83,v83
v_mfma_f32_16x16x32_bf16 v[40:43],a[132:135],v[60:63],v[40:43]
s_mov_b64 s[46:47],s[48:49]
ds_read_b128 v[56:59],v2 offset:15360
buffer_load_dwordx4 v0,s[36:39],s66 offen offset:3072 sc1 lds
v_fma_f32 v80,v80,v95,v95
v_fma_f32 v81,v81,v95,v95
v_fma_f32 v82,v82,v95,v95
v_fma_f32 v83,v83,v95,v95
v_rcp_f32 v80,v80
v_rcp_f32 v81,v81
v_rcp_f32 v82,v82
v_rcp_f32 v83,v83
v_mfma_f32_16x16x32_bf16 v[36:39],a[136:139],v[64:67],v[36:39]
s_mov_b64 s[48:49],s[50:51]
v_mul_f32 v32,v32,v80
v_mul_f32 v33,v33,v81
v_mul_f32 v34,v34,v82
v_mul_f32 v35,v35,v83
v_cvt_pk_bf16_f32 v86,v32,v33
v_cvt_pk_bf16_f32 v87,v34,v35
v_mfma_f32_16x16x32_bf16 v[40:43],a[140:143],v[64:67],v[40:43]
s_mov_b64 s[50:51],s[52:53]
ds_read_b128 v[60:63],v2 offset:8192
buffer_store_dwordx4 v[84:87],v3,s[36:39],s73 offen offset:0 sc0 sc1
v_xor_b32 v8,16384,v8
s_mov_b32 s52,s74
s_cmp_lt_u32 s67,s59
v_mfma_f32_16x16x32_bf16 v[36:39],a[144:147],v[68:71],v[36:39]
ds_read_b128 v[64:67],v2 offset:9216
v_mfma_f32_16x16x32_bf16 v[40:43],a[148:151],v[68:71],v[40:43]
v_mfma_f32_16x16x32_bf16 v[36:39],a[152:155],v[72:75],v[36:39]
s_waitcnt lgkmcnt(2)
ds_read_b128 v[68:71],v2 offset:10240
v_mfma_f32_16x16x32_bf16 v[40:43],a[156:159],v[72:75],v[40:43]
s_cbranch_scc1 Lg1_d0w3_%=
s_branch Lg1_slow0_%=
Lg1_slow0_%=:
s_cmp_lt_u32 s66,s59
s_cbranch_scc1 Lg1_gs0_%=
Lg1_slowb0_%=:
s_cmp_eq_u32 s54,1
s_cselect_b32 s68,s49,s48
s_lshr_b32 s69,s68,5
s_cmp_lt_u32 s68,4194304
s_cselect_b32 s68,0xffffcfff,-1
s_and_b32 s68,s68,s69
s_add_u32 m0,s78,8192
s_nop 0
buffer_load_dword v6,s[36:39],s68 offen offset:0 sc1 lds
s_lshr_b32 s73,s47,0
v_fma_f32 v36,-v96,v20,v36
v_fma_f32 v37,-v96,v21,v37
v_fma_f32 v38,-v96,v22,v38
v_fma_f32 v39,-v96,v23,v39
v_mul_f32 v76,v36,v36
v_mul_f32 v77,v37,v37
v_mul_f32 v78,v38,v38
v_mul_f32 v79,v39,v39
v_fma_f32 v76,v98,v76,v97
v_fma_f32 v77,v98,v77,v97
v_fma_f32 v78,v98,v78,v97
v_fma_f32 v79,v98,v79,v97
v_mul_f32 v76,v36,v76
v_mul_f32 v77,v37,v77
v_mul_f32 v78,v38,v78
v_mul_f32 v79,v39,v79
v_exp_f32 v76,v76
v_exp_f32 v77,v77
v_exp_f32 v78,v78
v_exp_f32 v79,v79
v_fma_f32 v76,v76,v99,v99
v_fma_f32 v77,v77,v99,v99
v_fma_f32 v78,v78,v99,v99
v_fma_f32 v79,v79,v99,v99
v_rcp_f32 v76,v76
v_rcp_f32 v77,v77
v_rcp_f32 v78,v78
v_rcp_f32 v79,v79
v_mul_f32 v36,v36,v76
v_mul_f32 v37,v37,v77
v_mul_f32 v38,v38,v78
v_mul_f32 v39,v39,v79
v_cvt_pk_bf16_f32 v84,v36,v37
v_cvt_pk_bf16_f32 v85,v38,v39
s_add_u32 m0,s78,8448
s_nop 0
buffer_load_dword v6,s[36:39],s68 offen offset:0 sc1 lds
v_fma_f32 v40,-v96,v24,v40
v_fma_f32 v41,-v96,v25,v41
v_fma_f32 v42,-v96,v26,v42
v_fma_f32 v43,-v96,v27,v43
v_mul_f32 v80,v40,v40
v_mul_f32 v81,v41,v41
v_mul_f32 v82,v42,v42
v_mul_f32 v83,v43,v43
v_fma_f32 v80,v98,v80,v97
v_fma_f32 v81,v98,v81,v97
v_fma_f32 v82,v98,v82,v97
v_fma_f32 v83,v98,v83,v97)"
R"(
v_mul_f32 v80,v40,v80
v_mul_f32 v81,v41,v81
v_mul_f32 v82,v42,v82
v_mul_f32 v83,v43,v83
v_exp_f32 v80,v80
v_exp_f32 v81,v81
v_exp_f32 v82,v82
v_exp_f32 v83,v83
v_fma_f32 v80,v80,v99,v99
v_fma_f32 v81,v81,v99,v99
v_fma_f32 v82,v82,v99,v99
v_fma_f32 v83,v83,v99,v99
v_rcp_f32 v80,v80
v_rcp_f32 v81,v81
v_rcp_f32 v82,v82
v_rcp_f32 v83,v83
v_mul_f32 v40,v40,v80
v_mul_f32 v41,v41,v81
v_mul_f32 v42,v42,v82
v_mul_f32 v43,v43,v83
v_cvt_pk_bf16_f32 v86,v40,v41
v_cvt_pk_bf16_f32 v87,v42,v43
buffer_store_dwordx4 v[84:87],v3,s[36:39],s73 offen offset:0 sc0 sc1
s_cmp_ge_u32 s48,s59
s_cbranch_scc1 Lg1_done0_%=
s_add_u32 m0,s78,16384
s_nop 0
buffer_load_dword v6,s[36:39],s68 offen offset:0 sc1 lds
s_waitcnt vmcnt(3)
s_barrier
ds_read_b32 v16,v7 offset:8192
s_lshr_b32 s70,s46,5
buffer_store_dword v5,v4,s[36:39],s70 offen offset:0 sc0 sc1
s_mov_b32 s46,s59
s_mov_b32 s69,18432
s_waitcnt lgkmcnt(0)
v_cmp_eq_u32 vcc,s60,v16
s_nop 0
s_cmp_eq_u32 vcc_lo,-1
s_cbranch_scc1 Lg1_go0_%=
s_add_u32 m0,s78,16640
s_nop 0
buffer_load_dword v6,s[36:39],s68 offen offset:0 sc1 lds
s_waitcnt vmcnt(4)
s_barrier
ds_read_b32 v16,v7 offset:8448
s_mov_b32 s69,18688
s_waitcnt lgkmcnt(0)
v_cmp_eq_u32 vcc,s60,v16
s_nop 0
s_cmp_eq_u32 vcc_lo,-1
s_cbranch_scc1 Lg1_go0_%=
s_add_u32 m0,s78,8192
s_nop 0
buffer_load_dword v6,s[36:39],s68 offen offset:0 sc1 lds
s_waitcnt vmcnt(3)
s_barrier
ds_read_b32 v16,v7 offset:16384
s_lshr_b32 s70,s47,5
buffer_store_dword v5,v4,s[36:39],s70 offen offset:0 sc0 sc1
s_mov_b32 s47,s59
s_mov_b32 s69,10240
s_waitcnt lgkmcnt(0)
v_cmp_eq_u32 vcc,s60,v16
s_nop 0
s_cmp_eq_u32 vcc_lo,-1
s_cbranch_scc1 Lg1_go0_%=
s_add_u32 m0,s78,8448
s_nop 0
buffer_load_dword v6,s[36:39],s68 offen offset:0 sc1 lds
s_waitcnt vmcnt(3)
s_barrier
ds_read_b32 v16,v7 offset:16640
s_mov_b32 s69,10496
s_waitcnt lgkmcnt(0)
v_cmp_eq_u32 vcc,s60,v16
s_nop 0
s_cmp_eq_u32 vcc_lo,-1
s_cbranch_scc1 Lg1_go0_%=
Lg1_poll0_%=:
s_add_u32 m0,s78,16384
s_nop 0
buffer_load_dword v6,s[36:39],s68 offen offset:0 sc1 lds
s_waitcnt vmcnt(2)
s_barrier
ds_read_b32 v16,v7 offset:8192
s_mov_b32 s69,18432
s_waitcnt lgkmcnt(0)
v_cmp_eq_u32 vcc,s60,v16
s_nop 0
s_cmp_eq_u32 vcc_lo,-1
s_cbranch_scc1 Lg1_go0_%=
s_sleep 4
s_add_u32 m0,s78,16640
s_nop 0
buffer_load_dword v6,s[36:39],s68 offen offset:0 sc1 lds
s_waitcnt vmcnt(2)
s_barrier
ds_read_b32 v16,v7 offset:8448
s_mov_b32 s69,18688
s_waitcnt lgkmcnt(0)
v_cmp_eq_u32 vcc,s60,v16
s_nop 0
s_cmp_eq_u32 vcc_lo,-1
s_cbranch_scc1 Lg1_go0_%=
s_sleep 4
s_add_u32 m0,s78,8192
s_nop 0
buffer_load_dword v6,s[36:39],s68 offen offset:0 sc1 lds
s_waitcnt vmcnt(2)
s_barrier
ds_read_b32 v16,v7 offset:16384
s_mov_b32 s69,10240
s_waitcnt lgkmcnt(0)
v_cmp_eq_u32 vcc,s60,v16
s_nop 0
s_cmp_eq_u32 vcc_lo,-1
s_cbranch_scc1 Lg1_go0_%=
s_sleep 4
s_add_u32 m0,s78,8448
s_nop 0
buffer_load_dword v6,s[36:39],s68 offen offset:0 sc1 lds
s_waitcnt vmcnt(2)
s_barrier
ds_read_b32 v16,v7 offset:16640
s_mov_b32 s69,10496
s_waitcnt lgkmcnt(0)
v_cmp_eq_u32 vcc,s60,v16
s_nop 0
s_cmp_eq_u32 vcc_lo,-1
s_cbranch_scc1 Lg1_go0_%=
s_sleep 4
s_add_u32 s79,s79,1
s_cmp_lt_u32 s79,20000
s_cbranch_scc1 Lg1_poll0_%=
Lg1_go0_%=:
s_xor_b32 s62,s63,65536
s_mov_b32 m0,s62
s_nop 0
buffer_load_dwordx4 v0,s[36:39],s48 offen offset:0 sc1 lds
buffer_load_dwordx4 v0,s[36:39],s48 offen offset:1024 sc1 lds
buffer_load_dwordx4 v0,s[36:39],s48 offen offset:2048 sc1 lds
buffer_load_dwordx4 v0,s[36:39],s48 offen offset:3072 sc1 lds
s_add_u32 m0,s62,16384
s_nop 0
buffer_load_dwordx4 v1,s[36:39],s48 offen offset:0 sc1 lds
buffer_load_dwordx4 v1,s[36:39],s48 offen offset:1024 sc1 lds
buffer_load_dwordx4 v1,s[36:39],s48 offen offset:2048 sc1 lds
buffer_load_dwordx4 v1,s[36:39],s48 offen offset:3072 sc1 lds
s_lshr_b32 s72,s48,6
s_lshr_b32 s71,s62,2
s_cmp_lt_u32 s48,4194304
s_cselect_b32 s72,s59,s72
s_add_u32 m0,s71,s88
s_nop 0
buffer_load_dword v10,s[36:39],s72 offen offset:0 sc1 lds
s_cmp_eq_u32 vcc_hi,-1
s_cselect_b32 s66,s49,s59
s_xor_b32 s62,s64,65536
s_cmp_ge_u32 s66,s59
s_cselect_b32 s68,s49,s50
s_lshr_b32 s69,s68,5
s_cmp_lt_u32 s68,4194304
s_cselect_b32 s68,0xffffcfff,-1
s_and_b32 s68,s68,s69
s_add_u32 m0,s78,0
s_nop 0
buffer_load_dword v6,s[36:39],s68 offen offset:0 sc1 lds
s_mov_b32 m0,s62
s_nop 0
buffer_load_dwordx4 v0,s[36:39],s66 offen offset:0 sc1 lds
buffer_load_dwordx4 v0,s[36:39],s66 offen offset:1024 sc1 lds
buffer_load_dwordx4 v0,s[36:39],s66 offen offset:2048 sc1 lds
buffer_load_dwordx4 v0,s[36:39],s66 offen offset:3072 sc1 lds
s_waitcnt vmcnt(10)
s_barrier
s_lshl_b32 s70,s54,12
v_add_u32 v76,s70,v2
v_xor_b32 v77,4096,v76
ds_read_b128 v[44:47],v76 offset:0
ds_read_b128 v[48:51],v76 offset:1024
ds_read_b128 v[52:55],v76 offset:2048
ds_read_b128 v[56:59],v76 offset:3072
ds_read_b128 v[60:63],v77 offset:0
ds_read_b128 v[64:67],v77 offset:1024
ds_read_b128 v[68:71],v77 offset:2048
s_lshr_b32 s70,s46,5
buffer_store_dword v5,v4,s[36:39],s70 offen offset:0 sc0 sc1
s_mov_b32 s46,s59
s_lshr_b32 s70,s47,5
buffer_store_dword v5,v4,s[36:39],s70 offen offset:0 sc0 sc1
s_mov_b32 s47,s59
s_lshr_b32 s70,s59,5
buffer_store_dword v5,v4,s[36:39],s70 offen offset:0 sc0 sc1
s_waitcnt lgkmcnt(3)
s_setpc_b64 s[90:91]
Lg1_gs0_%=:
s_cmp_eq_u32 s54,1
s_cselect_b32 s68,s49,s48
s_lshr_b32 s69,s68,5)"
R"(
s_cmp_lt_u32 s68,4194304
s_cselect_b32 s68,0xffffcfff,-1
s_and_b32 s68,s68,s69
s_lshr_b32 s73,s47,0
s_add_u32 m0,s78,8192
s_nop 0
buffer_load_dword v6,s[36:39],s68 offen offset:0 sc1 lds
v_fma_f32 v36,-v96,v20,v36
v_fma_f32 v37,-v96,v21,v37
v_fma_f32 v38,-v96,v22,v38
v_fma_f32 v39,-v96,v23,v39
v_mul_f32 v76,v36,v36
v_mul_f32 v77,v37,v37
v_mul_f32 v78,v38,v38
v_mul_f32 v79,v39,v39
v_fma_f32 v76,v98,v76,v97
v_fma_f32 v77,v98,v77,v97
v_fma_f32 v78,v98,v78,v97
s_add_u32 m0,s62,16384
s_nop 0
buffer_load_dwordx4 v1,s[36:39],s66 offen offset:0 sc1 lds
v_fma_f32 v79,v98,v79,v97
v_mul_f32 v76,v36,v76
v_mul_f32 v77,v37,v77
v_mul_f32 v78,v38,v78
v_mul_f32 v79,v39,v79
v_exp_f32 v76,v76
v_exp_f32 v77,v77
v_exp_f32 v78,v78
v_exp_f32 v79,v79
v_fma_f32 v76,v76,v99,v99
v_fma_f32 v77,v77,v99,v99
buffer_load_dwordx4 v1,s[36:39],s66 offen offset:1024 sc1 lds
v_fma_f32 v78,v78,v99,v99
v_fma_f32 v79,v79,v99,v99
v_rcp_f32 v76,v76
v_rcp_f32 v77,v77
v_rcp_f32 v78,v78
v_rcp_f32 v79,v79
v_mul_f32 v36,v36,v76
v_mul_f32 v37,v37,v77
v_mul_f32 v38,v38,v78
v_mul_f32 v39,v39,v79
v_cvt_pk_bf16_f32 v84,v36,v37
buffer_load_dwordx4 v1,s[36:39],s66 offen offset:2048 sc1 lds
v_cvt_pk_bf16_f32 v85,v38,v39
v_fma_f32 v40,-v96,v24,v40
v_fma_f32 v41,-v96,v25,v41
v_fma_f32 v42,-v96,v26,v42
v_fma_f32 v43,-v96,v27,v43
v_mul_f32 v80,v40,v40
v_mul_f32 v81,v41,v41
v_mul_f32 v82,v42,v42
v_mul_f32 v83,v43,v43
v_fma_f32 v80,v98,v80,v97
v_fma_f32 v81,v98,v81,v97
buffer_load_dwordx4 v1,s[36:39],s66 offen offset:3072 sc1 lds
v_fma_f32 v82,v98,v82,v97
v_fma_f32 v83,v98,v83,v97
v_mul_f32 v80,v40,v80
v_mul_f32 v81,v41,v81
v_mul_f32 v82,v42,v82
v_mul_f32 v83,v43,v83
v_exp_f32 v80,v80
v_exp_f32 v81,v81
v_exp_f32 v82,v82
v_exp_f32 v83,v83
v_fma_f32 v80,v80,v99,v99
s_lshr_b32 s72,s66,6
s_lshr_b32 s71,s62,2
s_cmp_lt_u32 s66,4194304
s_cselect_b32 s72,s59,s72
s_add_u32 m0,s71,s88
s_nop 0
buffer_load_dword v10,s[36:39],s72 offen offset:0 sc1 lds
v_fma_f32 v81,v81,v99,v99
v_fma_f32 v82,v82,v99,v99
v_fma_f32 v83,v83,v99,v99
v_rcp_f32 v80,v80
v_rcp_f32 v81,v81
v_rcp_f32 v82,v82
v_rcp_f32 v83,v83
v_mul_f32 v40,v40,v80
v_mul_f32 v41,v41,v81
v_mul_f32 v42,v42,v82
v_mul_f32 v43,v43,v83
v_cvt_pk_bf16_f32 v86,v40,v41
v_cvt_pk_bf16_f32 v87,v42,v43
buffer_store_dwordx4 v[84:87],v3,s[36:39],s73 offen offset:0 sc0 sc1
s_waitcnt vmcnt(6)
s_barrier
ds_read_b32 v16,v7 offset:8192
s_lshl_b32 s70,s54,12
v_add_u32 v76,s70,v2
v_xor_b32 v77,4096,v76
ds_read_b128 v[44:47],v76 offset:0
ds_read_b128 v[48:51],v76 offset:1024
ds_read_b128 v[52:55],v76 offset:2048
ds_read_b128 v[56:59],v76 offset:3072
ds_read_b128 v[60:63],v77 offset:0
ds_read_b128 v[64:67],v77 offset:1024
ds_read_b128 v[68:71],v77 offset:2048
s_lshr_b32 s70,s46,5
buffer_store_dword v5,v4,s[36:39],s70 offen offset:0 sc0 sc1
s_waitcnt lgkmcnt(7)
v_cmp_eq_u32 vcc,s60,v16
s_nop 0
s_cmp_eq_u32 vcc_hi,-1
s_cselect_b32 s66,s49,s59
s_xor_b32 s62,s64,65536
s_cmp_ge_u32 s66,s59
s_cselect_b32 s68,s49,s50
s_lshr_b32 s69,s68,5
s_cmp_lt_u32 s68,4194304
s_cselect_b32 s68,0xffffcfff,-1
s_and_b32 s68,s68,s69
s_add_u32 m0,s78,0
s_nop 0
buffer_load_dword v6,s[36:39],s68 offen offset:0 sc1 lds
s_mov_b32 m0,s62
s_nop 0
buffer_load_dwordx4 v0,s[36:39],s66 offen offset:0 sc1 lds
buffer_load_dwordx4 v0,s[36:39],s66 offen offset:1024 sc1 lds
buffer_load_dwordx4 v0,s[36:39],s66 offen offset:2048 sc1 lds
buffer_load_dwordx4 v0,s[36:39],s66 offen offset:3072 sc1 lds
s_mov_b32 s46,s47
s_mov_b32 s47,s59
s_lshr_b32 s70,s59,5
buffer_store_dword v5,v4,s[36:39],s70 offen offset:0 sc0 sc1
s_waitcnt lgkmcnt(3)
s_setpc_b64 s[90:91]
Lg1_done0_%=:
s_waitcnt vmcnt(0)
s_barrier
s_lshr_b32 s70,s46,5
buffer_store_dword v5,v4,s[36:39],s70 offen offset:0 sc0 sc1
s_lshr_b32 s70,s47,5
buffer_store_dword v5,v4,s[36:39],s70 offen offset:0 sc0 sc1
s_branch Lg1_done_%=
Lg1_slow1_%=:
s_cmp_lt_u32 s67,s59
s_cbranch_scc1 Lg1_gs1_%=
Lg1_slowb1_%=:
s_cmp_eq_u32 s54,1
s_cselect_b32 s68,s50,s49
s_lshr_b32 s69,s68,5
s_cmp_lt_u32 s68,4194304
s_cselect_b32 s68,0xffffcfff,-1
s_and_b32 s68,s68,s69
s_add_u32 m0,s78,8192
s_nop 0
buffer_load_dword v6,s[36:39],s68 offen offset:0 sc1 lds
s_lshr_b32 s73,s48,0
v_fma_f32 v28,-v92,v20,v28
v_fma_f32 v29,-v92,v21,v29
v_fma_f32 v30,-v92,v22,v30
v_fma_f32 v31,-v92,v23,v31
v_mul_f32 v76,v28,v28
v_mul_f32 v77,v29,v29
v_mul_f32 v78,v30,v30
v_mul_f32 v79,v31,v31
v_fma_f32 v76,v94,v76,v93
v_fma_f32 v77,v94,v77,v93
v_fma_f32 v78,v94,v78,v93
v_fma_f32 v79,v94,v79,v93
v_mul_f32 v76,v28,v76
v_mul_f32 v77,v29,v77
v_mul_f32 v78,v30,v78
v_mul_f32 v79,v31,v79
v_exp_f32 v76,v76
v_exp_f32 v77,v77
v_exp_f32 v78,v78
v_exp_f32 v79,v79
v_fma_f32 v76,v76,v95,v95
v_fma_f32 v77,v77,v95,v95
v_fma_f32 v78,v78,v95,v95
v_fma_f32 v79,v79,v95,v95
v_rcp_f32 v76,v76
v_rcp_f32 v77,v77
v_rcp_f32 v78,v78
v_rcp_f32 v79,v79
v_mul_f32 v28,v28,v76
v_mul_f32 v29,v29,v77
v_mul_f32 v30,v30,v78
v_mul_f32 v31,v31,v79
v_cvt_pk_bf16_f32 v84,v28,v29
v_cvt_pk_bf16_f32 v85,v30,v31
s_add_u32 m0,s78,8448
s_nop 0
buffer_load_dword v6,s[36:39],s68 offen offset:0 sc1 lds
v_fma_f32 v32,-v92,v24,v32
v_fma_f32 v33,-v92,v25,v33
v_fma_f32 v34,-v92,v26,v34
v_fma_f32 v35,-v92,v27,v35
v_mul_f32 v80,v32,v32
v_mul_f32 v81,v33,v33
v_mul_f32 v82,v34,v34
v_mul_f32 v83,v35,v35
v_fma_f32 v80,v94,v80,v93
v_fma_f32 v81,v94,v81,v93)"
R"(
v_fma_f32 v82,v94,v82,v93
v_fma_f32 v83,v94,v83,v93
v_mul_f32 v80,v32,v80
v_mul_f32 v81,v33,v81
v_mul_f32 v82,v34,v82
v_mul_f32 v83,v35,v83
v_exp_f32 v80,v80
v_exp_f32 v81,v81
v_exp_f32 v82,v82
v_exp_f32 v83,v83
v_fma_f32 v80,v80,v95,v95
v_fma_f32 v81,v81,v95,v95
v_fma_f32 v82,v82,v95,v95
v_fma_f32 v83,v83,v95,v95
v_rcp_f32 v80,v80
v_rcp_f32 v81,v81
v_rcp_f32 v82,v82
v_rcp_f32 v83,v83
v_mul_f32 v32,v32,v80
v_mul_f32 v33,v33,v81
v_mul_f32 v34,v34,v82
v_mul_f32 v35,v35,v83
v_cvt_pk_bf16_f32 v86,v32,v33
v_cvt_pk_bf16_f32 v87,v34,v35
buffer_store_dwordx4 v[84:87],v3,s[36:39],s73 offen offset:0 sc0 sc1
s_cmp_ge_u32 s49,s59
s_cbranch_scc1 Lg1_done1_%=
s_add_u32 m0,s78,16384
s_nop 0
buffer_load_dword v6,s[36:39],s68 offen offset:0 sc1 lds
s_waitcnt vmcnt(3)
s_barrier
ds_read_b32 v16,v7 offset:8192
s_lshr_b32 s70,s47,5
buffer_store_dword v5,v4,s[36:39],s70 offen offset:0 sc0 sc1
s_mov_b32 s47,s59
s_mov_b32 s69,18432
s_waitcnt lgkmcnt(0)
v_cmp_eq_u32 vcc,s60,v16
s_nop 0
s_cmp_eq_u32 vcc_lo,-1
s_cbranch_scc1 Lg1_go1_%=
s_add_u32 m0,s78,16640
s_nop 0
buffer_load_dword v6,s[36:39],s68 offen offset:0 sc1 lds
s_waitcnt vmcnt(4)
s_barrier
ds_read_b32 v16,v7 offset:8448
s_mov_b32 s69,18688
s_waitcnt lgkmcnt(0)
v_cmp_eq_u32 vcc,s60,v16
s_nop 0
s_cmp_eq_u32 vcc_lo,-1
s_cbranch_scc1 Lg1_go1_%=
s_add_u32 m0,s78,8192
s_nop 0
buffer_load_dword v6,s[36:39],s68 offen offset:0 sc1 lds
s_waitcnt vmcnt(3)
s_barrier
ds_read_b32 v16,v7 offset:16384
s_lshr_b32 s70,s48,5
buffer_store_dword v5,v4,s[36:39],s70 offen offset:0 sc0 sc1
s_mov_b32 s48,s59
s_mov_b32 s69,10240
s_waitcnt lgkmcnt(0)
v_cmp_eq_u32 vcc,s60,v16
s_nop 0
s_cmp_eq_u32 vcc_lo,-1
s_cbranch_scc1 Lg1_go1_%=
s_add_u32 m0,s78,8448
s_nop 0
buffer_load_dword v6,s[36:39],s68 offen offset:0 sc1 lds
s_waitcnt vmcnt(3)
s_barrier
ds_read_b32 v16,v7 offset:16640
s_mov_b32 s69,10496
s_waitcnt lgkmcnt(0)
v_cmp_eq_u32 vcc,s60,v16
s_nop 0
s_cmp_eq_u32 vcc_lo,-1
s_cbranch_scc1 Lg1_go1_%=
Lg1_poll1_%=:
s_add_u32 m0,s78,16384
s_nop 0
buffer_load_dword v6,s[36:39],s68 offen offset:0 sc1 lds
s_waitcnt vmcnt(2)
s_barrier
ds_read_b32 v16,v7 offset:8192
s_mov_b32 s69,18432
s_waitcnt lgkmcnt(0)
v_cmp_eq_u32 vcc,s60,v16
s_nop 0
s_cmp_eq_u32 vcc_lo,-1
s_cbranch_scc1 Lg1_go1_%=
s_sleep 4
s_add_u32 m0,s78,16640
s_nop 0
buffer_load_dword v6,s[36:39],s68 offen offset:0 sc1 lds
s_waitcnt vmcnt(2)
s_barrier
ds_read_b32 v16,v7 offset:8448
s_mov_b32 s69,18688
s_waitcnt lgkmcnt(0)
v_cmp_eq_u32 vcc,s60,v16
s_nop 0
s_cmp_eq_u32 vcc_lo,-1
s_cbranch_scc1 Lg1_go1_%=
s_sleep 4
s_add_u32 m0,s78,8192
s_nop 0
buffer_load_dword v6,s[36:39],s68 offen offset:0 sc1 lds
s_waitcnt vmcnt(2)
s_barrier
ds_read_b32 v16,v7 offset:16384
s_mov_b32 s69,10240
s_waitcnt lgkmcnt(0)
v_cmp_eq_u32 vcc,s60,v16
s_nop 0
s_cmp_eq_u32 vcc_lo,-1
s_cbranch_scc1 Lg1_go1_%=
s_sleep 4
s_add_u32 m0,s78,8448
s_nop 0
buffer_load_dword v6,s[36:39],s68 offen offset:0 sc1 lds
s_waitcnt vmcnt(2)
s_barrier
ds_read_b32 v16,v7 offset:16640
s_mov_b32 s69,10496
s_waitcnt lgkmcnt(0)
v_cmp_eq_u32 vcc,s60,v16
s_nop 0
s_cmp_eq_u32 vcc_lo,-1
s_cbranch_scc1 Lg1_go1_%=
s_sleep 4
s_add_u32 s79,s79,1
s_cmp_lt_u32 s79,20000
s_cbranch_scc1 Lg1_poll1_%=
Lg1_go1_%=:
s_xor_b32 s62,s64,65536
s_mov_b32 m0,s62
s_nop 0
buffer_load_dwordx4 v0,s[36:39],s49 offen offset:0 sc1 lds
buffer_load_dwordx4 v0,s[36:39],s49 offen offset:1024 sc1 lds
buffer_load_dwordx4 v0,s[36:39],s49 offen offset:2048 sc1 lds
buffer_load_dwordx4 v0,s[36:39],s49 offen offset:3072 sc1 lds
s_add_u32 m0,s62,16384
s_nop 0
buffer_load_dwordx4 v1,s[36:39],s49 offen offset:0 sc1 lds
buffer_load_dwordx4 v1,s[36:39],s49 offen offset:1024 sc1 lds
buffer_load_dwordx4 v1,s[36:39],s49 offen offset:2048 sc1 lds
buffer_load_dwordx4 v1,s[36:39],s49 offen offset:3072 sc1 lds
s_lshr_b32 s72,s49,6
s_lshr_b32 s71,s62,2
s_cmp_lt_u32 s49,4194304
s_cselect_b32 s72,s59,s72
s_add_u32 m0,s71,s88
s_nop 0
buffer_load_dword v10,s[36:39],s72 offen offset:0 sc1 lds
s_cmp_eq_u32 vcc_hi,-1
s_cselect_b32 s67,s50,s59
s_xor_b32 s62,s63,65536
s_cmp_ge_u32 s67,s59
s_cselect_b32 s68,s50,s51
s_lshr_b32 s69,s68,5
s_cmp_lt_u32 s68,4194304
s_cselect_b32 s68,0xffffcfff,-1
s_and_b32 s68,s68,s69
s_add_u32 m0,s78,256
s_nop 0
buffer_load_dword v6,s[36:39],s68 offen offset:0 sc1 lds
s_mov_b32 m0,s62
s_nop 0
buffer_load_dwordx4 v0,s[36:39],s67 offen offset:0 sc1 lds
buffer_load_dwordx4 v0,s[36:39],s67 offen offset:1024 sc1 lds
buffer_load_dwordx4 v0,s[36:39],s67 offen offset:2048 sc1 lds
buffer_load_dwordx4 v0,s[36:39],s67 offen offset:3072 sc1 lds
s_waitcnt vmcnt(10)
s_barrier
s_lshl_b32 s70,s54,12
v_add_u32 v76,s70,v2
v_xor_b32 v77,4096,v76
ds_read_b128 v[44:47],v76 offset:32768
ds_read_b128 v[48:51],v76 offset:33792
ds_read_b128 v[52:55],v76 offset:34816
ds_read_b128 v[56:59],v76 offset:35840
ds_read_b128 v[60:63],v77 offset:32768
ds_read_b128 v[64:67],v77 offset:33792
ds_read_b128 v[68:71],v77 offset:34816
s_lshr_b32 s70,s47,5
buffer_store_dword v5,v4,s[36:39],s70 offen offset:0 sc0 sc1
s_mov_b32 s47,s59
s_lshr_b32 s70,s48,5
buffer_store_dword v5,v4,s[36:39],s70 offen offset:0 sc0 sc1
s_mov_b32 s48,s59
s_lshr_b32 s70,s59,5
buffer_store_dword v5,v4,s[36:39],s70 offen offset:0 sc0 sc1
s_waitcnt lgkmcnt(3)
s_setpc_b64 s[92:93]
Lg1_gs1_%=:
s_cmp_eq_u32 s54,1)"
R"(
s_cselect_b32 s68,s50,s49
s_lshr_b32 s69,s68,5
s_cmp_lt_u32 s68,4194304
s_cselect_b32 s68,0xffffcfff,-1
s_and_b32 s68,s68,s69
s_lshr_b32 s73,s48,0
s_add_u32 m0,s78,8192
s_nop 0
buffer_load_dword v6,s[36:39],s68 offen offset:0 sc1 lds
v_fma_f32 v28,-v92,v20,v28
v_fma_f32 v29,-v92,v21,v29
v_fma_f32 v30,-v92,v22,v30
v_fma_f32 v31,-v92,v23,v31
v_mul_f32 v76,v28,v28
v_mul_f32 v77,v29,v29
v_mul_f32 v78,v30,v30
v_mul_f32 v79,v31,v31
v_fma_f32 v76,v94,v76,v93
v_fma_f32 v77,v94,v77,v93
v_fma_f32 v78,v94,v78,v93
s_add_u32 m0,s62,16384
s_nop 0
buffer_load_dwordx4 v1,s[36:39],s67 offen offset:0 sc1 lds
v_fma_f32 v79,v94,v79,v93
v_mul_f32 v76,v28,v76
v_mul_f32 v77,v29,v77
v_mul_f32 v78,v30,v78
v_mul_f32 v79,v31,v79
v_exp_f32 v76,v76
v_exp_f32 v77,v77
v_exp_f32 v78,v78
v_exp_f32 v79,v79
v_fma_f32 v76,v76,v95,v95
v_fma_f32 v77,v77,v95,v95
buffer_load_dwordx4 v1,s[36:39],s67 offen offset:1024 sc1 lds
v_fma_f32 v78,v78,v95,v95
v_fma_f32 v79,v79,v95,v95
v_rcp_f32 v76,v76
v_rcp_f32 v77,v77
v_rcp_f32 v78,v78
v_rcp_f32 v79,v79
v_mul_f32 v28,v28,v76
v_mul_f32 v29,v29,v77
v_mul_f32 v30,v30,v78
v_mul_f32 v31,v31,v79
v_cvt_pk_bf16_f32 v84,v28,v29
buffer_load_dwordx4 v1,s[36:39],s67 offen offset:2048 sc1 lds
v_cvt_pk_bf16_f32 v85,v30,v31
v_fma_f32 v32,-v92,v24,v32
v_fma_f32 v33,-v92,v25,v33
v_fma_f32 v34,-v92,v26,v34
v_fma_f32 v35,-v92,v27,v35
v_mul_f32 v80,v32,v32
v_mul_f32 v81,v33,v33
v_mul_f32 v82,v34,v34
v_mul_f32 v83,v35,v35
v_fma_f32 v80,v94,v80,v93
v_fma_f32 v81,v94,v81,v93
buffer_load_dwordx4 v1,s[36:39],s67 offen offset:3072 sc1 lds
v_fma_f32 v82,v94,v82,v93
v_fma_f32 v83,v94,v83,v93
v_mul_f32 v80,v32,v80
v_mul_f32 v81,v33,v81
v_mul_f32 v82,v34,v82
v_mul_f32 v83,v35,v83
v_exp_f32 v80,v80
v_exp_f32 v81,v81
v_exp_f32 v82,v82
v_exp_f32 v83,v83
v_fma_f32 v80,v80,v95,v95
s_lshr_b32 s72,s67,6
s_lshr_b32 s71,s62,2
s_cmp_lt_u32 s67,4194304
s_cselect_b32 s72,s59,s72
s_add_u32 m0,s71,s88
s_nop 0
buffer_load_dword v10,s[36:39],s72 offen offset:0 sc1 lds
v_fma_f32 v81,v81,v95,v95
v_fma_f32 v82,v82,v95,v95
v_fma_f32 v83,v83,v95,v95
v_rcp_f32 v80,v80
v_rcp_f32 v81,v81
v_rcp_f32 v82,v82
v_rcp_f32 v83,v83
v_mul_f32 v32,v32,v80
v_mul_f32 v33,v33,v81
v_mul_f32 v34,v34,v82
v_mul_f32 v35,v35,v83
v_cvt_pk_bf16_f32 v86,v32,v33
v_cvt_pk_bf16_f32 v87,v34,v35
buffer_store_dwordx4 v[84:87],v3,s[36:39],s73 offen offset:0 sc0 sc1
s_waitcnt vmcnt(6)
s_barrier
ds_read_b32 v16,v7 offset:8192
s_lshl_b32 s70,s54,12
v_add_u32 v76,s70,v2
v_xor_b32 v77,4096,v76
ds_read_b128 v[44:47],v76 offset:32768
ds_read_b128 v[48:51],v76 offset:33792
ds_read_b128 v[52:55],v76 offset:34816
ds_read_b128 v[56:59],v76 offset:35840
ds_read_b128 v[60:63],v77 offset:32768
ds_read_b128 v[64:67],v77 offset:33792
ds_read_b128 v[68:71],v77 offset:34816
s_lshr_b32 s70,s47,5
buffer_store_dword v5,v4,s[36:39],s70 offen offset:0 sc0 sc1
s_waitcnt lgkmcnt(7)
v_cmp_eq_u32 vcc,s60,v16
s_nop 0
s_cmp_eq_u32 vcc_hi,-1
s_cselect_b32 s67,s50,s59
s_xor_b32 s62,s63,65536
s_cmp_ge_u32 s67,s59
s_cselect_b32 s68,s50,s51
s_lshr_b32 s69,s68,5
s_cmp_lt_u32 s68,4194304
s_cselect_b32 s68,0xffffcfff,-1
s_and_b32 s68,s68,s69
s_add_u32 m0,s78,256
s_nop 0
buffer_load_dword v6,s[36:39],s68 offen offset:0 sc1 lds
s_mov_b32 m0,s62
s_nop 0
buffer_load_dwordx4 v0,s[36:39],s67 offen offset:0 sc1 lds
buffer_load_dwordx4 v0,s[36:39],s67 offen offset:1024 sc1 lds
buffer_load_dwordx4 v0,s[36:39],s67 offen offset:2048 sc1 lds
buffer_load_dwordx4 v0,s[36:39],s67 offen offset:3072 sc1 lds
s_mov_b32 s47,s48
s_mov_b32 s48,s59
s_lshr_b32 s70,s59,5
buffer_store_dword v5,v4,s[36:39],s70 offen offset:0 sc0 sc1
s_waitcnt lgkmcnt(3)
s_setpc_b64 s[92:93]
Lg1_done1_%=:
s_waitcnt vmcnt(0)
s_barrier
s_lshr_b32 s70,s47,5
buffer_store_dword v5,v4,s[36:39],s70 offen offset:0 sc0 sc1
s_lshr_b32 s70,s48,5
buffer_store_dword v5,v4,s[36:39],s70 offen offset:0 sc0 sc1
s_branch Lg1_done_%=
Lg1_done_%=:
s_waitcnt vmcnt(0) lgkmcnt(0)
s_barrier
s_branch Lg2_end_%=
Lg2_begin_%=:
Lcode_g2_b:
s_mov_b64 s[36:37],%1
s_mov_b32 s38,122727552
s_mov_b32 s39,0x00020000
s_mov_b32 s54,%2
s_mov_b32 s55,%3
s_mov_b32 s56,%4
s_mov_b32 s58,%5
s_mov_b32 s83,%6
s_mov_b32 s82,%7
s_mov_b32 s84,%8
v_mov_b32 v0,%9
v_mov_b32 v1,%10
v_mov_b32 v2,%11
v_mov_b32 v3,%12
v_mov_b32 v4,%13
v_mov_b32 v6,%14
v_mov_b32 v7,%15
v_mov_b32 v8,%16
v_mov_b32 v9,%17
v_mov_b32 v10,%18
v_mov_b32 v11,%19
v_mov_b32 v12,%20
v_mov_b32 v13,%21
v_mov_b32 v14,%22
v_mov_b32 v16,%23
v_mov_b32 v17,%24
v_mov_b32 v18,%25
v_mov_b32 v19,%26
v_mov_b32 v20,%27
s_mov_b64 s[40:41],%28
s_mov_b32 s42,2097152
s_mov_b32 s43,0x00020000
s_lshl_b32 s88,s54,11
s_mov_b32 s69,122727424
buffer_load_dword v5,off,s[36:39],s69 sc1
s_add_u32 m0,s88,0
s_add_u32 s61,s82,0
buffer_load_dwordx4 v16,s[40:43],s61 offen offset:0  lds
s_add_u32 m0,s88,1024
s_nop 0
buffer_load_dwordx4 v17,s[40:43],s61 offen offset:0  lds
s_add_u32 m0,s88,8064
s_nop 0
buffer_load_dwordx4 v16,s[40:43],s61 offen offset:128  lds
s_add_u32 m0,s88,9088
s_nop 0
buffer_load_dwordx4 v17,s[40:43],s61 offen offset:128  lds
s_add_u32 m0,s88,16384
s_add_u32 s61,s82,131072
buffer_load_dwordx4 v16,s[40:43],s61 offen offset:0  lds
s_add_u32 m0,s88,17408
s_nop 0
buffer_load_dwordx4 v17,s[40:43],s61 offen offset:0  lds)"
R"(
s_add_u32 m0,s88,24448
s_nop 0
buffer_load_dwordx4 v16,s[40:43],s61 offen offset:128  lds
s_add_u32 m0,s88,25472
s_nop 0
buffer_load_dwordx4 v17,s[40:43],s61 offen offset:128  lds
s_add_u32 m0,s88,32768
s_add_u32 s61,s82,262144
buffer_load_dwordx4 v16,s[40:43],s61 offen offset:0  lds
s_add_u32 m0,s88,33792
s_nop 0
buffer_load_dwordx4 v17,s[40:43],s61 offen offset:0  lds
s_add_u32 m0,s88,40832
s_nop 0
buffer_load_dwordx4 v16,s[40:43],s61 offen offset:128  lds
s_add_u32 m0,s88,41856
s_nop 0
buffer_load_dwordx4 v17,s[40:43],s61 offen offset:128  lds
s_add_u32 m0,s88,49152
s_add_u32 s61,s82,393216
buffer_load_dwordx4 v16,s[40:43],s61 offen offset:0  lds
s_add_u32 m0,s88,50176
s_nop 0
buffer_load_dwordx4 v17,s[40:43],s61 offen offset:0  lds
s_add_u32 m0,s88,57216
s_nop 0
buffer_load_dwordx4 v16,s[40:43],s61 offen offset:128  lds
s_add_u32 m0,s88,58240
s_nop 0
buffer_load_dwordx4 v17,s[40:43],s61 offen offset:128  lds
s_add_u32 m0,s88,65536
s_add_u32 s61,s82,524288
buffer_load_dwordx4 v16,s[40:43],s61 offen offset:0  lds
s_add_u32 m0,s88,66560
s_nop 0
buffer_load_dwordx4 v17,s[40:43],s61 offen offset:0  lds
s_add_u32 m0,s88,73600
s_nop 0
buffer_load_dwordx4 v16,s[40:43],s61 offen offset:128  lds
s_add_u32 m0,s88,74624
s_nop 0
buffer_load_dwordx4 v17,s[40:43],s61 offen offset:128  lds
s_add_u32 m0,s88,81920
s_add_u32 s61,s82,655360
buffer_load_dwordx4 v16,s[40:43],s61 offen offset:0  lds
s_add_u32 m0,s88,82944
s_nop 0
buffer_load_dwordx4 v17,s[40:43],s61 offen offset:0  lds
s_add_u32 m0,s88,89984
s_nop 0
buffer_load_dwordx4 v16,s[40:43],s61 offen offset:128  lds
s_add_u32 m0,s88,91008
s_nop 0
buffer_load_dwordx4 v17,s[40:43],s61 offen offset:128  lds
s_add_u32 m0,s88,98304
s_add_u32 s61,s82,786432
buffer_load_dwordx4 v16,s[40:43],s61 offen offset:0  lds
s_add_u32 m0,s88,99328
s_nop 0
buffer_load_dwordx4 v17,s[40:43],s61 offen offset:0  lds
s_add_u32 m0,s88,106368
s_nop 0
buffer_load_dwordx4 v16,s[40:43],s61 offen offset:128  lds
s_add_u32 m0,s88,107392
s_nop 0
buffer_load_dwordx4 v17,s[40:43],s61 offen offset:128  lds
v_add_u32 v24,49152,v18
v_add_u32 v25,49152,v19
v_add_u32 v26,98304,v18
v_add_u32 v27,98304,v19
s_waitcnt vmcnt(24)
s_barrier
s_add_u32 m0,s88,114688
s_add_u32 s61,s82,917504
buffer_load_dwordx4 v16,s[40:43],s61 offen offset:0  lds
s_add_u32 m0,s88,115712
ds_read_b64_tr_b16 a[0:1],v18 offset:0
buffer_load_dwordx4 v17,s[40:43],s61 offen offset:0  lds
s_add_u32 m0,s88,122752
ds_read_b64_tr_b16 a[2:3],v18 offset:512
buffer_load_dwordx4 v16,s[40:43],s61 offen offset:128  lds
s_add_u32 m0,s88,123776
ds_read_b64_tr_b16 a[4:5],v19 offset:0
buffer_load_dwordx4 v17,s[40:43],s61 offen offset:128  lds
ds_read_b64_tr_b16 a[6:7],v19 offset:512
ds_read_b64_tr_b16 a[8:9],v18 offset:4096
ds_read_b64_tr_b16 a[10:11],v18 offset:4608
ds_read_b64_tr_b16 a[12:13],v19 offset:4096
ds_read_b64_tr_b16 a[14:15],v19 offset:4608
s_waitcnt vmcnt(24)
s_barrier
s_add_u32 m0,s88,131072
s_add_u32 s61,s82,1048576
buffer_load_dwordx4 v16,s[40:43],s61 offen offset:0  lds
s_add_u32 m0,s88,132096
ds_read_b64_tr_b16 a[16:17],v18 offset:16384
buffer_load_dwordx4 v17,s[40:43],s61 offen offset:0  lds
s_add_u32 m0,s88,139136
ds_read_b64_tr_b16 a[18:19],v18 offset:16896
buffer_load_dwordx4 v16,s[40:43],s61 offen offset:128  lds
s_add_u32 m0,s88,140160
ds_read_b64_tr_b16 a[20:21],v19 offset:16384
buffer_load_dwordx4 v17,s[40:43],s61 offen offset:128  lds
ds_read_b64_tr_b16 a[22:23],v19 offset:16896
ds_read_b64_tr_b16 a[24:25],v18 offset:20480
ds_read_b64_tr_b16 a[26:27],v18 offset:20992
ds_read_b64_tr_b16 a[28:29],v19 offset:20480
ds_read_b64_tr_b16 a[30:31],v19 offset:20992
s_waitcnt lgkmcnt(8)
s_waitcnt vmcnt(24)
s_barrier
s_add_u32 m0,s88,0
s_add_u32 s61,s82,1179648
buffer_load_dwordx4 v16,s[40:43],s61 offen offset:0  lds
s_add_u32 m0,s88,1024
ds_read_b64_tr_b16 a[32:33],v18 offset:32768
buffer_load_dwordx4 v17,s[40:43],s61 offen offset:0  lds
s_add_u32 m0,s88,8064
ds_read_b64_tr_b16 a[34:35],v18 offset:33280
buffer_load_dwordx4 v16,s[40:43],s61 offen offset:128  lds
s_add_u32 m0,s88,9088
ds_read_b64_tr_b16 a[36:37],v19 offset:32768
buffer_load_dwordx4 v17,s[40:43],s61 offen offset:128  lds
ds_read_b64_tr_b16 a[38:39],v19 offset:33280
ds_read_b64_tr_b16 a[40:41],v18 offset:36864
ds_read_b64_tr_b16 a[42:43],v18 offset:37376
ds_read_b64_tr_b16 a[44:45],v19 offset:36864
ds_read_b64_tr_b16 a[46:47],v19 offset:37376
s_waitcnt lgkmcnt(8)
s_waitcnt vmcnt(24)
s_barrier
s_add_u32 m0,s88,16384
s_add_u32 s61,s82,1310720
buffer_load_dwordx4 v16,s[40:43],s61 offen offset:0  lds
s_add_u32 m0,s88,17408
ds_read_b64_tr_b16 a[48:49],v24 offset:0
buffer_load_dwordx4 v17,s[40:43],s61 offen offset:0  lds
s_add_u32 m0,s88,24448
ds_read_b64_tr_b16 a[50:51],v24 offset:512
buffer_load_dwordx4 v16,s[40:43],s61 offen offset:128  lds
s_add_u32 m0,s88,25472
ds_read_b64_tr_b16 a[52:53],v25 offset:0
buffer_load_dwordx4 v17,s[40:43],s61 offen offset:128  lds
ds_read_b64_tr_b16 a[54:55],v25 offset:512
ds_read_b64_tr_b16 a[56:57],v24 offset:4096
ds_read_b64_tr_b16 a[58:59],v24 offset:4608
ds_read_b64_tr_b16 a[60:61],v25 offset:4096
ds_read_b64_tr_b16 a[62:63],v25 offset:4608
s_waitcnt lgkmcnt(8)
s_waitcnt vmcnt(24)
s_barrier
s_add_u32 m0,s88,32768
s_add_u32 s61,s82,1441792
buffer_load_dwordx4 v16,s[40:43],s61 offen offset:0  lds
s_add_u32 m0,s88,33792
ds_read_b64_tr_b16 a[64:65],v24 offset:16384
buffer_load_dwordx4 v17,s[40:43],s61 offen offset:0  lds
s_add_u32 m0,s88,40832
ds_read_b64_tr_b16 a[66:67],v24 offset:16896
buffer_load_dwordx4 v16,s[40:43],s61 offen offset:128  lds
s_add_u32 m0,s88,41856
ds_read_b64_tr_b16 a[68:69],v25 offset:16384
buffer_load_dwordx4 v17,s[40:43],s61 offen offset:128  lds
ds_read_b64_tr_b16 a[70:71],v25 offset:16896
ds_read_b64_tr_b16 a[72:73],v24 offset:20480
ds_read_b64_tr_b16 a[74:75],v24 offset:20992
ds_read_b64_tr_b16 a[76:77],v25 offset:20480
ds_read_b64_tr_b16 a[78:79],v25 offset:20992
s_waitcnt lgkmcnt(8)
s_waitcnt vmcnt(24)
s_barrier
s_add_u32 m0,s88,49152
s_add_u32 s61,s82,1572864
buffer_load_dwordx4 v16,s[40:43],s61 offen offset:0  lds
s_add_u32 m0,s88,50176
ds_read_b64_tr_b16 a[80:81],v24 offset:32768
buffer_load_dwordx4 v17,s[40:43],s61 offen offset:0  lds
s_add_u32 m0,s88,57216
ds_read_b64_tr_b16 a[82:83],v24 offset:33280
buffer_load_dwordx4 v16,s[40:43],s61 offen offset:128  lds
s_add_u32 m0,s88,58240
ds_read_b64_tr_b16 a[84:85],v25 offset:32768
buffer_load_dwordx4 v17,s[40:43],s61 offen offset:128  lds
ds_read_b64_tr_b16 a[86:87],v25 offset:33280
ds_read_b64_tr_b16 a[88:89],v24 offset:36864
ds_read_b64_tr_b16 a[90:91],v24 offset:37376
ds_read_b64_tr_b16 a[92:93],v25 offset:36864
ds_read_b64_tr_b16 a[94:95],v25 offset:37376
s_waitcnt lgkmcnt(8)
s_waitcnt vmcnt(24)
s_barrier
s_add_u32 m0,s88,65536
s_add_u32 s61,s82,1703936
buffer_load_dwordx4 v16,s[40:43],s61 offen offset:0  lds
s_add_u32 m0,s88,66560
ds_read_b64_tr_b16 a[96:97],v26 offset:0
buffer_load_dwordx4 v17,s[40:43],s61 offen offset:0  lds
s_add_u32 m0,s88,73600
ds_read_b64_tr_b16 a[98:99],v26 offset:512
buffer_load_dwordx4 v16,s[40:43],s61 offen offset:128  lds)"
R"(
s_add_u32 m0,s88,74624
ds_read_b64_tr_b16 a[100:101],v27 offset:0
buffer_load_dwordx4 v17,s[40:43],s61 offen offset:128  lds
ds_read_b64_tr_b16 a[102:103],v27 offset:512
ds_read_b64_tr_b16 a[104:105],v26 offset:4096
ds_read_b64_tr_b16 a[106:107],v26 offset:4608
ds_read_b64_tr_b16 a[108:109],v27 offset:4096
ds_read_b64_tr_b16 a[110:111],v27 offset:4608
s_waitcnt lgkmcnt(8)
s_waitcnt vmcnt(24)
s_barrier
s_add_u32 m0,s88,81920
s_add_u32 s61,s82,1835008
buffer_load_dwordx4 v16,s[40:43],s61 offen offset:0  lds
s_add_u32 m0,s88,82944
ds_read_b64_tr_b16 a[112:113],v26 offset:16384
buffer_load_dwordx4 v17,s[40:43],s61 offen offset:0  lds
s_add_u32 m0,s88,89984
ds_read_b64_tr_b16 a[114:115],v26 offset:16896
buffer_load_dwordx4 v16,s[40:43],s61 offen offset:128  lds
s_add_u32 m0,s88,91008
ds_read_b64_tr_b16 a[116:117],v27 offset:16384
buffer_load_dwordx4 v17,s[40:43],s61 offen offset:128  lds
ds_read_b64_tr_b16 a[118:119],v27 offset:16896
ds_read_b64_tr_b16 a[120:121],v26 offset:20480
ds_read_b64_tr_b16 a[122:123],v26 offset:20992
ds_read_b64_tr_b16 a[124:125],v27 offset:20480
ds_read_b64_tr_b16 a[126:127],v27 offset:20992
s_waitcnt lgkmcnt(8)
s_waitcnt vmcnt(24)
s_barrier
s_add_u32 m0,s88,98304
s_add_u32 s61,s82,1966080
buffer_load_dwordx4 v16,s[40:43],s61 offen offset:0  lds
s_add_u32 m0,s88,99328
ds_read_b64_tr_b16 a[128:129],v26 offset:32768
buffer_load_dwordx4 v17,s[40:43],s61 offen offset:0  lds
s_add_u32 m0,s88,106368
ds_read_b64_tr_b16 a[130:131],v26 offset:33280
buffer_load_dwordx4 v16,s[40:43],s61 offen offset:128  lds
s_add_u32 m0,s88,107392
ds_read_b64_tr_b16 a[132:133],v27 offset:32768
buffer_load_dwordx4 v17,s[40:43],s61 offen offset:128  lds
ds_read_b64_tr_b16 a[134:135],v27 offset:33280
ds_read_b64_tr_b16 a[136:137],v26 offset:36864
ds_read_b64_tr_b16 a[138:139],v26 offset:37376
ds_read_b64_tr_b16 a[140:141],v27 offset:36864
ds_read_b64_tr_b16 a[142:143],v27 offset:37376
s_waitcnt lgkmcnt(8)
s_waitcnt vmcnt(24)
s_barrier
ds_read_b64_tr_b16 a[144:145],v18 offset:0
ds_read_b64_tr_b16 a[146:147],v18 offset:512
ds_read_b64_tr_b16 a[148:149],v19 offset:0
ds_read_b64_tr_b16 a[150:151],v19 offset:512
ds_read_b64_tr_b16 a[152:153],v18 offset:4096
ds_read_b64_tr_b16 a[154:155],v18 offset:4608
ds_read_b64_tr_b16 a[156:157],v19 offset:4096
ds_read_b64_tr_b16 a[158:159],v19 offset:4608
s_waitcnt lgkmcnt(8)
s_waitcnt vmcnt(20)
s_barrier
ds_read_b64_tr_b16 a[160:161],v18 offset:16384
ds_read_b64_tr_b16 a[162:163],v18 offset:16896
ds_read_b64_tr_b16 a[164:165],v19 offset:16384
ds_read_b64_tr_b16 a[166:167],v19 offset:16896
ds_read_b64_tr_b16 a[168:169],v18 offset:20480
ds_read_b64_tr_b16 a[170:171],v18 offset:20992
ds_read_b64_tr_b16 a[172:173],v19 offset:20480
ds_read_b64_tr_b16 a[174:175],v19 offset:20992
s_waitcnt lgkmcnt(8)
s_waitcnt vmcnt(16)
s_barrier
ds_read_b64_tr_b16 a[176:177],v18 offset:32768
ds_read_b64_tr_b16 a[178:179],v18 offset:33280
ds_read_b64_tr_b16 a[180:181],v19 offset:32768
ds_read_b64_tr_b16 a[182:183],v19 offset:33280
ds_read_b64_tr_b16 a[184:185],v18 offset:36864
ds_read_b64_tr_b16 a[186:187],v18 offset:37376
ds_read_b64_tr_b16 a[188:189],v19 offset:36864
ds_read_b64_tr_b16 a[190:191],v19 offset:37376
s_getpc_b64 s[46:47]
Lg2_cp_%=:
s_add_u32 s46,s46,Lg2_init_%=-Lg2_cp_%=
s_addc_u32 s47,s47,0
s_lshl_b32 s68,s54,13
v_lshl_add_u32 v48,v2,3,s68
v_min_u32 v49,Lg2_done_%=-Lg2_init_%=,v48
global_load_dword v52,v49,s[46:47]
s_waitcnt lgkmcnt(8)
s_waitcnt vmcnt(13)
s_barrier
ds_read_b64_tr_b16 a[192:193],v24 offset:0
ds_read_b64_tr_b16 a[194:195],v24 offset:512
ds_read_b64_tr_b16 a[196:197],v25 offset:0
ds_read_b64_tr_b16 a[198:199],v25 offset:512
ds_read_b64_tr_b16 a[200:201],v24 offset:4096
ds_read_b64_tr_b16 a[202:203],v24 offset:4608
ds_read_b64_tr_b16 a[204:205],v25 offset:4096
ds_read_b64_tr_b16 a[206:207],v25 offset:4608
s_waitcnt lgkmcnt(8)
s_waitcnt vmcnt(9)
s_barrier
ds_read_b64_tr_b16 a[208:209],v24 offset:16384
ds_read_b64_tr_b16 a[210:211],v24 offset:16896
ds_read_b64_tr_b16 a[212:213],v25 offset:16384
ds_read_b64_tr_b16 a[214:215],v25 offset:16896
ds_read_b64_tr_b16 a[216:217],v24 offset:20480
ds_read_b64_tr_b16 a[218:219],v24 offset:20992
ds_read_b64_tr_b16 a[220:221],v25 offset:20480
ds_read_b64_tr_b16 a[222:223],v25 offset:20992
s_waitcnt lgkmcnt(8)
s_waitcnt vmcnt(5)
s_barrier
ds_read_b64_tr_b16 a[224:225],v24 offset:32768
ds_read_b64_tr_b16 a[226:227],v24 offset:33280
ds_read_b64_tr_b16 a[228:229],v25 offset:32768
ds_read_b64_tr_b16 a[230:231],v25 offset:33280
ds_read_b64_tr_b16 a[232:233],v24 offset:36864
ds_read_b64_tr_b16 a[234:235],v24 offset:37376
ds_read_b64_tr_b16 a[236:237],v25 offset:36864
ds_read_b64_tr_b16 a[238:239],v25 offset:37376
s_waitcnt lgkmcnt(8)
s_waitcnt vmcnt(1)
s_barrier
ds_read_b64_tr_b16 a[240:241],v26 offset:0
ds_read_b64_tr_b16 a[242:243],v26 offset:512
ds_read_b64_tr_b16 a[244:245],v27 offset:0
ds_read_b64_tr_b16 a[246:247],v27 offset:512
ds_read_b64_tr_b16 a[248:249],v26 offset:4096
ds_read_b64_tr_b16 a[250:251],v26 offset:4608
ds_read_b64_tr_b16 a[252:253],v27 offset:4096
ds_read_b64_tr_b16 a[254:255],v27 offset:4608
s_waitcnt lgkmcnt(8)
s_waitcnt lgkmcnt(0)
s_waitcnt lgkmcnt(0)
s_barrier
v_readfirstlane_b32 s60,v5
s_nop 3
s_add_u32 s60,s60,1
s_cmp_eq_u32 s60,0
s_cselect_b32 s60,1,s60
Lg2_init_%=:
v_mov_b32 v5,s60
s_mov_b32 s59,536870912
s_add_u32 s57,s56,131072
s_mov_b32 s79,0
s_mov_b32 s75,0x3a800000
s_mov_b32 s76,0xc0135761
s_mov_b32 s77,0x3d372713
s_mov_b32 s44,s59
s_mov_b32 s45,s59
s_mov_b32 s46,s59
s_mov_b32 s47,s59
s_mov_b32 s48,s83
s_add_u32 s49,s48,s55
s_cmp_eq_u32 s49,s56
s_cselect_b32 s49,s57,s49
s_cmp_eq_u32 s49,s58
s_cselect_b32 s49,s59,s49
s_add_u32 s50,s49,s55
s_cmp_eq_u32 s50,s56
s_cselect_b32 s50,s57,s50
s_cmp_eq_u32 s50,s58
s_cselect_b32 s50,s59,s50
s_add_u32 s51,s50,s55
s_cmp_eq_u32 s51,s56
s_cselect_b32 s51,s57,s51
s_cmp_eq_u32 s51,s58
s_cselect_b32 s51,s59,s51
s_add_u32 s52,s51,s55
s_cmp_eq_u32 s52,s56
s_cselect_b32 s52,s57,s52
s_cmp_eq_u32 s52,s58
s_cselect_b32 s52,s59,s52
s_lshl_b32 s63,s54,12
s_add_u32 s63,s63,65536
s_add_u32 s64,s63,32768
s_lshl_b32 s88,s54,10
s_add_u32 s88,s88,131072
s_lshl_b32 s78,s54,11
s_add_u32 s78,s78,132608
s_getpc_b64 s[90:91]
Lg2_pc_%=:
s_mov_b32 s68,Lg2_d1w0_%=-Lg2_pc_%=
s_cmp_eq_u32 s54,1
s_cselect_b32 s68,Lg2_d1w1_%=-Lg2_pc_%=,s68
s_cmp_eq_u32 s54,2
s_cselect_b32 s68,Lg2_d1w2_%=-Lg2_pc_%=,s68
s_cmp_eq_u32 s54,3
s_cselect_b32 s68,Lg2_d1w3_%=-Lg2_pc_%=,s68
s_add_u32 s92,s90,s68
s_addc_u32 s93,s91,0
s_mov_b32 s68,Lg2_d0w0_%=-Lg2_pc_%=
s_cmp_eq_u32 s54,1
s_cselect_b32 s68,Lg2_d0w1_%=-Lg2_pc_%=,s68
s_cmp_eq_u32 s54,2
s_cselect_b32 s68,Lg2_d0w2_%=-Lg2_pc_%=,s68
s_cmp_eq_u32 s54,3
s_cselect_b32 s68,Lg2_d0w3_%=-Lg2_pc_%=,s68)"
R"(
s_add_u32 s90,s90,s68
s_addc_u32 s91,s91,0
s_mov_b32 s66,s59
s_mov_b32 s65,0
s_mov_b32 s62,s63
s_branch Lg2_slow0_%=
Lg2_d0w0_%=:
v_mfma_f32_16x16x32_bf16 v[16:19],a[0:3],v[32:35],0
s_add_u32 m0,s62,16384
ds_read_b128 v[60:63],v2 offset:7168
v_lshlrev_b32 v64,16,v84
v_mfma_f32_16x16x32_bf16 v[20:23],a[4:7],v[32:35],0
buffer_load_dwordx4 v1,s[36:39],s66 offen offset:0 sc1 lds
v_mfma_f32_16x16x32_bf16 v[16:19],a[8:11],v[36:39],v[16:19]
s_lshr_b32 s73,s47,0
ds_read_b128 v[32:35],v2 offset:8192
v_and_b32 v65,0xffff0000,v84
v_mfma_f32_16x16x32_bf16 v[20:23],a[12:15],v[36:39],v[20:23]
s_xor_b32 s87,s64,65536
v_lshlrev_b32 v66,16,v85
v_and_b32 v67,0xffff0000,v85
v_mfma_f32_16x16x32_bf16 v[16:19],a[16:19],v[40:43],v[16:19]
s_cmp_ge_u32 s66,s59
ds_read_b128 v[36:39],v2 offset:9216
v_lshlrev_b32 v68,16,v86
v_mfma_f32_16x16x32_bf16 v[20:23],a[20:23],v[40:43],v[20:23]
buffer_load_dwordx4 v1,s[36:39],s66 offen offset:1024 sc1 lds
v_mfma_f32_16x16x32_bf16 v[16:19],a[24:27],v[44:47],v[16:19]
s_cselect_b32 s86,s49,s50
ds_read_b128 v[40:43],v2 offset:10240
v_and_b32 v69,0xffff0000,v86
v_mfma_f32_16x16x32_bf16 v[20:23],a[28:31],v[44:47],v[20:23]
s_waitcnt lgkmcnt(3)
v_lshlrev_b32 v70,16,v87
v_and_b32 v71,0xffff0000,v87
v_mfma_f32_16x16x32_bf16 v[16:19],a[32:35],v[48:51],v[16:19]
s_cselect_b32 s87,s87,s63
ds_read_b128 v[44:47],v2 offset:11264
v_fma_f32 v64,v64,v91,v90
v_mfma_f32_16x16x32_bf16 v[20:23],a[36:39],v[48:51],v[20:23]
buffer_load_dwordx4 v1,s[36:39],s66 offen offset:2048 sc1 lds
v_mfma_f32_16x16x32_bf16 v[16:19],a[40:43],v[52:55],v[16:19]
s_lshr_b32 s72,s66,6
ds_read_b128 v[48:51],v2 offset:12288
v_fma_f32 v65,v65,v91,v90
v_mfma_f32_16x16x32_bf16 v[20:23],a[44:47],v[52:55],v[20:23]
s_lshr_b32 s71,s62,2
v_fma_f32 v66,v66,v91,v90
v_fma_f32 v67,v67,v91,v90
v_mfma_f32_16x16x32_bf16 v[16:19],a[48:51],v[56:59],v[16:19]
s_mov_b32 s62,s87
ds_read_b128 v[52:55],v2 offset:13312
v_fma_f32 v68,v68,v91,v90
v_mfma_f32_16x16x32_bf16 v[20:23],a[52:55],v[56:59],v[20:23]
buffer_load_dwordx4 v1,s[36:39],s66 offen offset:3072 sc1 lds
v_mfma_f32_16x16x32_bf16 v[16:19],a[56:59],v[60:63],v[16:19]
s_add_u32 m0,s71,s88
ds_read_b128 v[56:59],v2 offset:14336
v_fma_f32 v69,v69,v91,v90
v_mfma_f32_16x16x32_bf16 v[20:23],a[60:63],v[60:63],v[20:23]
s_waitcnt lgkmcnt(3)
v_fma_f32 v70,v70,v91,v90
v_fma_f32 v71,v71,v91,v90
v_mfma_f32_16x16x32_bf16 v[16:19],a[64:67],v[32:35],v[16:19]
s_waitcnt vmcnt(10)
ds_read_b128 v[60:63],v2 offset:15360
v_add_f32 v24,v24,v64
v_mfma_f32_16x16x32_bf16 v[20:23],a[68:71],v[32:35],v[20:23]
s_barrier
v_mfma_f32_16x16x32_bf16 v[16:19],a[72:75],v[36:39],v[16:19]
s_lshr_b32 s68,s50,5
ds_read_b128 v[32:35],v2 offset:16384
v_add_f32 v25,v25,v65
v_mfma_f32_16x16x32_bf16 v[20:23],a[76:79],v[36:39],v[20:23]
buffer_load_dwordx4 v10,s[36:39],s66 offen offset:0 sc1 lds
v_mfma_f32_16x16x32_bf16 v[16:19],a[80:83],v[40:43],v[16:19]
buffer_load_dword v11,s[36:39],s72 offen offset:1024 sc1 lds
v_mfma_f32_16x16x32_bf16 v[20:23],a[84:87],v[40:43],v[20:23]
s_lshr_b32 s70,s51,5
ds_read_b128 v[36:39],v2 offset:17408
v_add_f32 v26,v26,v66
v_mfma_f32_16x16x32_bf16 v[16:19],a[88:91],v[44:47],v[16:19]
ds_read_b128 v[40:43],v2 offset:18432
v_add_f32 v27,v27,v67
v_mfma_f32_16x16x32_bf16 v[20:23],a[92:95],v[44:47],v[20:23]
s_waitcnt lgkmcnt(3)
v_add_f32 v28,v28,v68
v_add_f32 v29,v29,v69
v_add_f32 v30,v30,v70
v_add_f32 v31,v31,v71
v_cvt_pk_bf16_f32 v72,v24,v25
v_cvt_pk_bf16_f32 v73,v26,v27
v_cvt_pk_bf16_f32 v74,v28,v29
v_cvt_pk_bf16_f32 v75,v30,v31
v_mfma_f32_16x16x32_bf16 v[16:19],a[96:99],v[48:51],v[16:19]
buffer_store_dwordx4 v[72:75],v3,s[36:39],s73 offen offset:0 sc0 sc1
ds_read_b128 v[44:47],v2 offset:19456
v_add_f32 v64,v24,v25
v_mul_f32 v65,v24,v24
v_mfma_f32_16x16x32_bf16 v[20:23],a[100:103],v[48:51],v[20:23]
v_fma_f32 v65,v25,v25,v65
v_add_f32 v64,v64,v26
v_mfma_f32_16x16x32_bf16 v[16:19],a[104:107],v[52:55],v[16:19]
ds_read_b128 v[48:51],v2 offset:20480
v_fma_f32 v65,v26,v26,v65
v_mfma_f32_16x16x32_bf16 v[20:23],a[108:111],v[52:55],v[20:23]
v_add_f32 v64,v64,v27
v_fma_f32 v65,v27,v27,v65
v_mfma_f32_16x16x32_bf16 v[16:19],a[112:115],v[56:59],v[16:19]
ds_read_b128 v[52:55],v2 offset:21504
v_add_f32 v64,v64,v28
v_mfma_f32_16x16x32_bf16 v[20:23],a[116:119],v[56:59],v[20:23]
v_fma_f32 v65,v28,v28,v65
v_add_f32 v64,v64,v29
v_fma_f32 v65,v29,v29,v65
v_add_f32 v64,v64,v30
v_fma_f32 v65,v30,v30,v65
v_add_f32 v64,v64,v31
v_fma_f32 v65,v31,v31,v65
v_mfma_f32_16x16x32_bf16 v[16:19],a[120:123],v[60:63],v[16:19]
s_nop 0
v_permlane16_swap_b32 v64,v65
v_add_f32 v64,v64,v65
v_mov_b32 v65,v64
v_mfma_f32_16x16x32_bf16 v[20:23],a[124:127],v[60:63],v[20:23]
s_waitcnt lgkmcnt(2)
ds_read_b128 v[56:59],v2 offset:22528
v_permlane32_swap_b32 v64,v65
v_add_f32 v64,v64,v65
v_mfma_f32_16x16x32_bf16 v[16:19],a[128:131],v[32:35],v[16:19]
ds_write_b32 v13,v64 offset:0
v_mfma_f32_16x16x32_bf16 v[20:23],a[132:135],v[32:35],v[20:23]
ds_read_b128 v[60:63],v2 offset:23552
v_mfma_f32_16x16x32_bf16 v[16:19],a[136:139],v[36:39],v[16:19]
ds_read_b128 v[32:35],v2 offset:24576
v_mfma_f32_16x16x32_bf16 v[20:23],a[140:143],v[36:39],v[20:23]
v_mfma_f32_16x16x32_bf16 v[16:19],a[144:147],v[40:43],v[16:19]
s_waitcnt vmcnt(11) lgkmcnt(2)
ds_read_b128 v[36:39],v2 offset:25600
v_mfma_f32_16x16x32_bf16 v[20:23],a[148:151],v[40:43],v[20:23]
s_barrier
v_mfma_f32_16x16x32_bf16 v[16:19],a[152:155],v[44:47],v[16:19]
ds_read_b128 v[40:43],v2 offset:26624
ds_read_b32 v15,v7 offset:0
v_mfma_f32_16x16x32_bf16 v[20:23],a[156:159],v[44:47],v[20:23]
s_waitcnt lgkmcnt(4)
v_mfma_f32_16x16x32_bf16 v[16:19],a[160:163],v[48:51],v[16:19]
ds_read_b128 v[44:47],v2 offset:27648
v_mfma_f32_16x16x32_bf16 v[20:23],a[164:167],v[48:51],v[20:23]
v_mfma_f32_16x16x32_bf16 v[16:19],a[168:171],v[52:55],v[16:19]
s_waitcnt lgkmcnt(1)
ds_read_b128 v[48:51],v2 offset:28672
v_mfma_f32_16x16x32_bf16 v[20:23],a[172:175],v[52:55],v[20:23]
v_cmp_eq_u32 vcc,s60,v15
v_mfma_f32_16x16x32_bf16 v[16:19],a[176:179],v[56:59],v[16:19]
s_cmp_eq_u32 vcc_lo,-1
ds_read_b128 v[52:55],v2 offset:29696
s_cselect_b32 s67,s86,s59
v_mfma_f32_16x16x32_bf16 v[20:23],a[180:183],v[56:59],v[20:23]
s_cmp_ge_u32 s67,s59
s_cselect_b32 s68,s68,s70
s_add_u32 m0,s78,256
v_mfma_f32_16x16x32_bf16 v[16:19],a[184:187],v[60:63],v[16:19]
buffer_load_dword v6,s[36:39],s68 offen offset:0 sc1 lds
v_mfma_f32_16x16x32_bf16 v[20:23],a[188:191],v[60:63],v[20:23]
s_waitcnt lgkmcnt(2)
ds_read_b128 v[56:59],v2 offset:30720
s_xor_b32 s63,s63,65536
v_mfma_f32_16x16x32_bf16 v[16:19],a[192:195],v[32:35],v[16:19]
s_waitcnt vmcnt(8)
ds_read_b128 v[60:63],v2 offset:31744
s_mov_b32 m0,s62
v_mfma_f32_16x16x32_bf16 v[20:23],a[196:199],v[32:35],v[20:23]
s_barrier
v_mfma_f32_16x16x32_bf16 v[16:19],a[200:203],v[36:39],v[16:19]
buffer_load_dwordx4 v0,s[36:39],s67 offen offset:0 sc1 lds
v_mfma_f32_16x16x32_bf16 v[20:23],a[204:207],v[36:39],v[20:23]
s_add_u32 s53,s52,s55
ds_read_b128 v[32:35],v2 offset:32768
ds_read_b128 v[80:83],v8 offset:0
v_mfma_f32_16x16x32_bf16 v[16:19],a[208:211],v[40:43],v[16:19]
s_cmp_eq_u32 s53,s56
ds_read_b128 v[36:39],v2 offset:33792
ds_read_b64 v[88:89],v9 offset:1024
v_mfma_f32_16x16x32_bf16 v[20:23],a[212:215],v[40:43],v[20:23]
s_cselect_b32 s53,s57,s53
s_cmp_eq_u32 s53,s58
s_cselect_b32 s53,s59,s53
v_mfma_f32_16x16x32_bf16 v[16:19],a[216:219],v[44:47],v[16:19]
buffer_load_dwordx4 v0,s[36:39],s67 offen offset:1024 sc1 lds
v_mfma_f32_16x16x32_bf16 v[20:23],a[220:223],v[44:47],v[20:23]
s_waitcnt lgkmcnt(4)
ds_read_b128 v[40:43],v2 offset:34816
s_cmp_ge_u32 s66,s59
v_mfma_f32_16x16x32_bf16 v[16:19],a[224:227],v[48:51],v[16:19]
ds_read_b128 v[44:47],v2 offset:35840
v_mfma_f32_16x16x32_bf16 v[20:23],a[228:231],v[48:51],v[20:23]
v_mfma_f32_16x16x32_bf16 v[16:19],a[232:235],v[52:55],v[16:19]
s_waitcnt lgkmcnt(2)
buffer_load_dwordx4 v0,s[36:39],s67 offen offset:2048 sc1 lds)"
R"(
v_mfma_f32_16x16x32_bf16 v[20:23],a[236:239],v[52:55],v[20:23]
ds_read_b128 v[48:51],v2 offset:36864
v_mfma_f32_16x16x32_bf16 v[16:19],a[240:243],v[56:59],v[16:19]
ds_read_b128 v[52:55],v2 offset:37888
v_mfma_f32_16x16x32_bf16 v[20:23],a[244:247],v[56:59],v[20:23]
v_mfma_f32_16x16x32_bf16 v[16:19],a[248:251],v[60:63],v[16:19]
s_waitcnt lgkmcnt(2)
ds_read_b128 v[56:59],v2 offset:38912
buffer_load_dwordx4 v0,s[36:39],s67 offen offset:3072 sc1 lds
v_mfma_f32_16x16x32_bf16 v[20:23],a[252:255],v[60:63],v[20:23]
s_cbranch_scc1 Lg2_slow1_%=
Lg2_d1w0_%=:
v_mfma_f32_16x16x32_bf16 v[24:27],a[0:3],v[32:35],0
s_add_u32 m0,s62,16384
ds_read_b128 v[60:63],v2 offset:39936
v_lshlrev_b32 v64,16,v80
v_mfma_f32_16x16x32_bf16 v[28:31],a[4:7],v[32:35],0
buffer_load_dwordx4 v1,s[36:39],s67 offen offset:0 sc1 lds
v_mfma_f32_16x16x32_bf16 v[24:27],a[8:11],v[36:39],v[24:27]
s_lshr_b32 s73,s48,0
ds_read_b128 v[32:35],v2 offset:40960
v_and_b32 v65,0xffff0000,v80
v_mfma_f32_16x16x32_bf16 v[28:31],a[12:15],v[36:39],v[28:31]
s_xor_b32 s87,s63,65536
v_lshlrev_b32 v66,16,v81
v_and_b32 v67,0xffff0000,v81
v_mfma_f32_16x16x32_bf16 v[24:27],a[16:19],v[40:43],v[24:27]
s_cmp_ge_u32 s67,s59
ds_read_b128 v[36:39],v2 offset:41984
v_lshlrev_b32 v68,16,v82
v_mfma_f32_16x16x32_bf16 v[28:31],a[20:23],v[40:43],v[28:31]
buffer_load_dwordx4 v1,s[36:39],s67 offen offset:1024 sc1 lds
v_mfma_f32_16x16x32_bf16 v[24:27],a[24:27],v[44:47],v[24:27]
s_cselect_b32 s86,s50,s51
ds_read_b128 v[40:43],v2 offset:43008
v_and_b32 v69,0xffff0000,v82
v_mfma_f32_16x16x32_bf16 v[28:31],a[28:31],v[44:47],v[28:31]
s_waitcnt lgkmcnt(3)
v_lshlrev_b32 v70,16,v83
v_and_b32 v71,0xffff0000,v83
v_mfma_f32_16x16x32_bf16 v[24:27],a[32:35],v[48:51],v[24:27]
s_cselect_b32 s87,s87,s64
ds_read_b128 v[44:47],v2 offset:44032
v_fma_f32 v64,v64,v89,v88
v_mfma_f32_16x16x32_bf16 v[28:31],a[36:39],v[48:51],v[28:31]
buffer_load_dwordx4 v1,s[36:39],s67 offen offset:2048 sc1 lds
v_mfma_f32_16x16x32_bf16 v[24:27],a[40:43],v[52:55],v[24:27]
s_lshr_b32 s72,s67,6
ds_read_b128 v[48:51],v2 offset:45056
v_fma_f32 v65,v65,v89,v88
v_mfma_f32_16x16x32_bf16 v[28:31],a[44:47],v[52:55],v[28:31]
s_lshr_b32 s71,s62,2
v_fma_f32 v66,v66,v89,v88
v_fma_f32 v67,v67,v89,v88
v_mfma_f32_16x16x32_bf16 v[24:27],a[48:51],v[56:59],v[24:27]
s_mov_b32 s62,s87
ds_read_b128 v[52:55],v2 offset:46080
v_fma_f32 v68,v68,v89,v88
v_mfma_f32_16x16x32_bf16 v[28:31],a[52:55],v[56:59],v[28:31]
buffer_load_dwordx4 v1,s[36:39],s67 offen offset:3072 sc1 lds
v_mfma_f32_16x16x32_bf16 v[24:27],a[56:59],v[60:63],v[24:27]
s_add_u32 m0,s71,s88
ds_read_b128 v[56:59],v2 offset:47104
v_fma_f32 v69,v69,v89,v88
v_mfma_f32_16x16x32_bf16 v[28:31],a[60:63],v[60:63],v[28:31]
s_waitcnt lgkmcnt(3)
v_fma_f32 v70,v70,v89,v88
v_fma_f32 v71,v71,v89,v88
v_mfma_f32_16x16x32_bf16 v[24:27],a[64:67],v[32:35],v[24:27]
s_waitcnt vmcnt(10)
ds_read_b128 v[60:63],v2 offset:48128
v_add_f32 v16,v16,v64
v_mfma_f32_16x16x32_bf16 v[28:31],a[68:71],v[32:35],v[28:31]
s_barrier
v_mfma_f32_16x16x32_bf16 v[24:27],a[72:75],v[36:39],v[24:27]
s_lshr_b32 s68,s51,5
ds_read_b128 v[32:35],v2 offset:49152
v_add_f32 v17,v17,v65
v_mfma_f32_16x16x32_bf16 v[28:31],a[76:79],v[36:39],v[28:31]
buffer_load_dwordx4 v10,s[36:39],s67 offen offset:0 sc1 lds
v_mfma_f32_16x16x32_bf16 v[24:27],a[80:83],v[40:43],v[24:27]
buffer_load_dword v11,s[36:39],s72 offen offset:1024 sc1 lds
v_mfma_f32_16x16x32_bf16 v[28:31],a[84:87],v[40:43],v[28:31]
s_lshr_b32 s70,s52,5
ds_read_b128 v[36:39],v2 offset:50176
v_add_f32 v18,v18,v66
v_mfma_f32_16x16x32_bf16 v[24:27],a[88:91],v[44:47],v[24:27]
s_mov_b64 s[44:45],s[46:47]
ds_read_b128 v[40:43],v2 offset:51200
v_add_f32 v19,v19,v67
v_mfma_f32_16x16x32_bf16 v[28:31],a[92:95],v[44:47],v[28:31]
s_waitcnt lgkmcnt(3)
v_add_f32 v20,v20,v68
v_add_f32 v21,v21,v69
v_add_f32 v22,v22,v70
v_add_f32 v23,v23,v71
v_cvt_pk_bf16_f32 v72,v16,v17
v_cvt_pk_bf16_f32 v73,v18,v19
v_cvt_pk_bf16_f32 v74,v20,v21
v_cvt_pk_bf16_f32 v75,v22,v23
v_mfma_f32_16x16x32_bf16 v[24:27],a[96:99],v[48:51],v[24:27]
buffer_store_dwordx4 v[72:75],v3,s[36:39],s73 offen offset:0 sc0 sc1
ds_read_b128 v[44:47],v2 offset:52224
v_add_f32 v64,v16,v17
v_mul_f32 v65,v16,v16
v_mfma_f32_16x16x32_bf16 v[28:31],a[100:103],v[48:51],v[28:31]
s_mov_b64 s[46:47],s[48:49]
v_fma_f32 v65,v17,v17,v65
v_add_f32 v64,v64,v18
v_mfma_f32_16x16x32_bf16 v[24:27],a[104:107],v[52:55],v[24:27]
s_mov_b64 s[48:49],s[50:51]
ds_read_b128 v[48:51],v2 offset:53248
v_fma_f32 v65,v18,v18,v65
v_mfma_f32_16x16x32_bf16 v[28:31],a[108:111],v[52:55],v[28:31]
s_mov_b64 s[50:51],s[52:53]
v_add_f32 v64,v64,v19
v_fma_f32 v65,v19,v19,v65
v_mfma_f32_16x16x32_bf16 v[24:27],a[112:115],v[56:59],v[24:27]
ds_read_b128 v[52:55],v2 offset:54272
v_add_f32 v64,v64,v20
v_mfma_f32_16x16x32_bf16 v[28:31],a[116:119],v[56:59],v[28:31]
v_fma_f32 v65,v20,v20,v65
v_add_f32 v64,v64,v21
v_fma_f32 v65,v21,v21,v65
v_add_f32 v64,v64,v22
v_fma_f32 v65,v22,v22,v65
v_add_f32 v64,v64,v23
v_fma_f32 v65,v23,v23,v65
v_mfma_f32_16x16x32_bf16 v[24:27],a[120:123],v[60:63],v[24:27]
s_nop 0
v_permlane16_swap_b32 v64,v65
v_add_f32 v64,v64,v65
v_mov_b32 v65,v64
v_mfma_f32_16x16x32_bf16 v[28:31],a[124:127],v[60:63],v[28:31]
s_waitcnt lgkmcnt(2)
ds_read_b128 v[56:59],v2 offset:55296
v_permlane32_swap_b32 v64,v65
v_add_f32 v64,v64,v65
v_mfma_f32_16x16x32_bf16 v[24:27],a[128:131],v[32:35],v[24:27]
ds_write_b32 v13,v64 offset:2048
v_mfma_f32_16x16x32_bf16 v[28:31],a[132:135],v[32:35],v[28:31]
ds_read_b128 v[60:63],v2 offset:56320
v_mfma_f32_16x16x32_bf16 v[24:27],a[136:139],v[36:39],v[24:27]
ds_read_b128 v[32:35],v2 offset:57344
v_mfma_f32_16x16x32_bf16 v[28:31],a[140:143],v[36:39],v[28:31]
v_mfma_f32_16x16x32_bf16 v[24:27],a[144:147],v[40:43],v[24:27]
s_waitcnt vmcnt(11) lgkmcnt(2)
ds_read_b128 v[36:39],v2 offset:58368
v_mfma_f32_16x16x32_bf16 v[28:31],a[148:151],v[40:43],v[28:31]
s_barrier
v_mfma_f32_16x16x32_bf16 v[24:27],a[152:155],v[44:47],v[24:27]
ds_read_b128 v[40:43],v2 offset:59392
ds_read_b32 v15,v7 offset:256
v_mfma_f32_16x16x32_bf16 v[28:31],a[156:159],v[44:47],v[28:31]
s_waitcnt lgkmcnt(4)
v_mfma_f32_16x16x32_bf16 v[24:27],a[160:163],v[48:51],v[24:27]
ds_read_b128 v[44:47],v2 offset:60416
v_mfma_f32_16x16x32_bf16 v[28:31],a[164:167],v[48:51],v[28:31]
v_mfma_f32_16x16x32_bf16 v[24:27],a[168:171],v[52:55],v[24:27]
s_waitcnt lgkmcnt(1)
ds_read_b128 v[48:51],v2 offset:61440
v_mfma_f32_16x16x32_bf16 v[28:31],a[172:175],v[52:55],v[28:31]
v_cmp_eq_u32 vcc,s60,v15
v_mfma_f32_16x16x32_bf16 v[24:27],a[176:179],v[56:59],v[24:27]
s_cmp_eq_u32 vcc_lo,-1
ds_read_b128 v[52:55],v2 offset:62464
s_cselect_b32 s66,s86,s59
v_mfma_f32_16x16x32_bf16 v[28:31],a[180:183],v[56:59],v[28:31]
s_cmp_ge_u32 s66,s59
s_cselect_b32 s68,s68,s70
s_add_u32 m0,s78,0
v_mfma_f32_16x16x32_bf16 v[24:27],a[184:187],v[60:63],v[24:27]
buffer_load_dword v6,s[36:39],s68 offen offset:0 sc1 lds
v_mfma_f32_16x16x32_bf16 v[28:31],a[188:191],v[60:63],v[28:31]
s_waitcnt lgkmcnt(2)
ds_read_b128 v[56:59],v2 offset:63488
s_xor_b32 s64,s64,65536
v_mfma_f32_16x16x32_bf16 v[24:27],a[192:195],v[32:35],v[24:27]
s_waitcnt vmcnt(8)
ds_read_b128 v[60:63],v2 offset:64512
v_xor_b32 v2,65536,v2
v_mfma_f32_16x16x32_bf16 v[28:31],a[196:199],v[32:35],v[28:31]
s_barrier
s_mov_b32 m0,s62
v_mfma_f32_16x16x32_bf16 v[24:27],a[200:203],v[36:39],v[24:27]
buffer_load_dwordx4 v0,s[36:39],s66 offen offset:0 sc1 lds
v_mfma_f32_16x16x32_bf16 v[28:31],a[204:207],v[36:39],v[28:31]
s_add_u32 s74,s53,s55
ds_read_b128 v[32:35],v2 offset:0
ds_read_b128 v[84:87],v8 offset:8192
v_mfma_f32_16x16x32_bf16 v[24:27],a[208:211],v[40:43],v[24:27]
s_cmp_eq_u32 s74,s56
ds_read_b128 v[36:39],v2 offset:1024
ds_read_b64 v[90:91],v9 offset:9216
v_mfma_f32_16x16x32_bf16 v[28:31],a[212:215],v[40:43],v[28:31]
s_cselect_b32 s74,s57,s74
v_xor_b32 v8,16384,v8
v_xor_b32 v9,16384,v9
v_mfma_f32_16x16x32_bf16 v[24:27],a[216:219],v[44:47],v[24:27]
buffer_load_dwordx4 v0,s[36:39],s66 offen offset:1024 sc1 lds)"
R"(
v_mfma_f32_16x16x32_bf16 v[28:31],a[220:223],v[44:47],v[28:31]
s_waitcnt lgkmcnt(4)
ds_read_b128 v[40:43],v2 offset:2048
s_cmp_eq_u32 s74,s58
v_mfma_f32_16x16x32_bf16 v[24:27],a[224:227],v[48:51],v[24:27]
s_cselect_b32 s74,s59,s74
ds_read_b128 v[44:47],v2 offset:3072
s_mov_b32 s52,s74
v_mfma_f32_16x16x32_bf16 v[28:31],a[228:231],v[48:51],v[28:31]
s_cmp_lt_u32 s67,s59
v_mfma_f32_16x16x32_bf16 v[24:27],a[232:235],v[52:55],v[24:27]
s_waitcnt lgkmcnt(2)
buffer_load_dwordx4 v0,s[36:39],s66 offen offset:2048 sc1 lds
v_mfma_f32_16x16x32_bf16 v[28:31],a[236:239],v[52:55],v[28:31]
ds_read_b128 v[48:51],v2 offset:4096
v_mfma_f32_16x16x32_bf16 v[24:27],a[240:243],v[56:59],v[24:27]
ds_read_b128 v[52:55],v2 offset:5120
v_mfma_f32_16x16x32_bf16 v[28:31],a[244:247],v[56:59],v[28:31]
v_mfma_f32_16x16x32_bf16 v[24:27],a[248:251],v[60:63],v[24:27]
s_waitcnt lgkmcnt(2)
ds_read_b128 v[56:59],v2 offset:6144
buffer_load_dwordx4 v0,s[36:39],s66 offen offset:3072 sc1 lds
v_mfma_f32_16x16x32_bf16 v[28:31],a[252:255],v[60:63],v[28:31]
s_cbranch_scc1 Lg2_d0w0_%=
s_branch Lg2_slow0_%=
Lg2_d0w1_%=:
v_mfma_f32_16x16x32_bf16 v[16:19],a[0:3],v[32:35],0
s_add_u32 m0,s62,16384
ds_read_b128 v[60:63],v2 offset:7168
v_lshlrev_b32 v64,16,v84
v_mfma_f32_16x16x32_bf16 v[20:23],a[4:7],v[32:35],0
s_lshr_b32 s73,s47,0
v_and_b32 v65,0xffff0000,v84
v_lshlrev_b32 v66,16,v85
v_mfma_f32_16x16x32_bf16 v[16:19],a[8:11],v[36:39],v[16:19]
buffer_load_dwordx4 v1,s[36:39],s66 offen offset:0 sc1 lds
v_mfma_f32_16x16x32_bf16 v[20:23],a[12:15],v[36:39],v[20:23]
s_xor_b32 s87,s64,65536
ds_read_b128 v[32:35],v2 offset:8192
v_and_b32 v67,0xffff0000,v85
v_mfma_f32_16x16x32_bf16 v[16:19],a[16:19],v[40:43],v[16:19]
s_cmp_ge_u32 s66,s59
ds_read_b128 v[36:39],v2 offset:9216
v_lshlrev_b32 v68,16,v86
v_mfma_f32_16x16x32_bf16 v[20:23],a[20:23],v[40:43],v[20:23]
s_cselect_b32 s86,s49,s50
v_and_b32 v69,0xffff0000,v86
v_lshlrev_b32 v70,16,v87
v_mfma_f32_16x16x32_bf16 v[16:19],a[24:27],v[44:47],v[16:19]
buffer_load_dwordx4 v1,s[36:39],s66 offen offset:1024 sc1 lds
v_mfma_f32_16x16x32_bf16 v[20:23],a[28:31],v[44:47],v[20:23]
s_waitcnt lgkmcnt(2)
ds_read_b128 v[40:43],v2 offset:10240
v_and_b32 v71,0xffff0000,v87
v_mfma_f32_16x16x32_bf16 v[16:19],a[32:35],v[48:51],v[16:19]
s_cselect_b32 s87,s87,s63
ds_read_b128 v[44:47],v2 offset:11264
v_fma_f32 v64,v64,v91,v90
v_mfma_f32_16x16x32_bf16 v[20:23],a[36:39],v[48:51],v[20:23]
s_lshr_b32 s72,s66,6
v_fma_f32 v65,v65,v91,v90
v_fma_f32 v66,v66,v91,v90
v_mfma_f32_16x16x32_bf16 v[16:19],a[40:43],v[52:55],v[16:19]
buffer_load_dwordx4 v1,s[36:39],s66 offen offset:2048 sc1 lds
v_mfma_f32_16x16x32_bf16 v[20:23],a[44:47],v[52:55],v[20:23]
s_lshr_b32 s71,s62,2
ds_read_b128 v[48:51],v2 offset:12288
v_fma_f32 v67,v67,v91,v90
v_mfma_f32_16x16x32_bf16 v[16:19],a[48:51],v[56:59],v[16:19]
s_mov_b32 s62,s87
ds_read_b128 v[52:55],v2 offset:13312
v_fma_f32 v68,v68,v91,v90
v_mfma_f32_16x16x32_bf16 v[20:23],a[52:55],v[56:59],v[20:23]
v_fma_f32 v69,v69,v91,v90
v_fma_f32 v70,v70,v91,v90
v_mfma_f32_16x16x32_bf16 v[16:19],a[56:59],v[60:63],v[16:19]
buffer_load_dwordx4 v1,s[36:39],s66 offen offset:3072 sc1 lds
v_mfma_f32_16x16x32_bf16 v[20:23],a[60:63],v[60:63],v[20:23]
s_waitcnt lgkmcnt(2)
ds_read_b128 v[56:59],v2 offset:14336
v_fma_f32 v71,v71,v91,v90
v_mfma_f32_16x16x32_bf16 v[16:19],a[64:67],v[32:35],v[16:19]
s_waitcnt vmcnt(9)
ds_read_b128 v[60:63],v2 offset:15360
v_add_f32 v24,v24,v64
v_mfma_f32_16x16x32_bf16 v[20:23],a[68:71],v[32:35],v[20:23]
s_barrier
v_mfma_f32_16x16x32_bf16 v[16:19],a[72:75],v[36:39],v[16:19]
s_add_u32 m0,s71,s88
ds_read_b128 v[32:35],v2 offset:16384
v_add_f32 v25,v25,v65
v_mfma_f32_16x16x32_bf16 v[20:23],a[76:79],v[36:39],v[20:23]
v_add_f32 v26,v26,v66
v_add_f32 v27,v27,v67
v_mfma_f32_16x16x32_bf16 v[16:19],a[80:83],v[40:43],v[16:19]
buffer_load_dwordx4 v10,s[36:39],s66 offen offset:0 sc1 lds
v_mfma_f32_16x16x32_bf16 v[20:23],a[84:87],v[40:43],v[20:23]
ds_read_b128 v[36:39],v2 offset:17408
v_add_f32 v28,v28,v68
v_mfma_f32_16x16x32_bf16 v[16:19],a[88:91],v[44:47],v[16:19]
buffer_load_dword v11,s[36:39],s72 offen offset:1024 sc1 lds
v_mfma_f32_16x16x32_bf16 v[20:23],a[92:95],v[44:47],v[20:23]
s_waitcnt lgkmcnt(2)
ds_read_b128 v[40:43],v2 offset:18432
v_add_f32 v29,v29,v69
v_mfma_f32_16x16x32_bf16 v[16:19],a[96:99],v[48:51],v[16:19]
s_mov_b32 m0,s62
v_add_f32 v30,v30,v70
v_add_f32 v31,v31,v71
v_cvt_pk_bf16_f32 v72,v24,v25
v_cvt_pk_bf16_f32 v73,v26,v27
v_cvt_pk_bf16_f32 v74,v28,v29
v_cvt_pk_bf16_f32 v75,v30,v31
v_mfma_f32_16x16x32_bf16 v[20:23],a[100:103],v[48:51],v[20:23]
ds_read_b128 v[44:47],v2 offset:19456
buffer_store_dwordx4 v[72:75],v3,s[36:39],s73 offen offset:0 sc0 sc1
v_add_f32 v64,v24,v25
v_mul_f32 v65,v24,v24
v_fma_f32 v65,v25,v25,v65
v_mfma_f32_16x16x32_bf16 v[16:19],a[104:107],v[52:55],v[16:19]
ds_read_b128 v[48:51],v2 offset:20480
v_add_f32 v64,v64,v26
v_mfma_f32_16x16x32_bf16 v[20:23],a[108:111],v[52:55],v[20:23]
v_fma_f32 v65,v26,v26,v65
v_add_f32 v64,v64,v27
v_mfma_f32_16x16x32_bf16 v[16:19],a[112:115],v[56:59],v[16:19]
ds_read_b128 v[52:55],v2 offset:21504
v_fma_f32 v65,v27,v27,v65
v_mfma_f32_16x16x32_bf16 v[20:23],a[116:119],v[56:59],v[20:23]
v_add_f32 v64,v64,v28
v_fma_f32 v65,v28,v28,v65
v_add_f32 v64,v64,v29
v_fma_f32 v65,v29,v29,v65
v_add_f32 v64,v64,v30
v_fma_f32 v65,v30,v30,v65
v_add_f32 v64,v64,v31
v_fma_f32 v65,v31,v31,v65
v_mfma_f32_16x16x32_bf16 v[16:19],a[120:123],v[60:63],v[16:19]
s_nop 0
v_permlane16_swap_b32 v64,v65
v_add_f32 v64,v64,v65
v_mov_b32 v65,v64
v_mfma_f32_16x16x32_bf16 v[20:23],a[124:127],v[60:63],v[20:23]
s_waitcnt lgkmcnt(2)
ds_read_b128 v[56:59],v2 offset:22528
v_permlane32_swap_b32 v64,v65
v_add_f32 v64,v64,v65
v_mfma_f32_16x16x32_bf16 v[16:19],a[128:131],v[32:35],v[16:19]
ds_write_b32 v13,v64 offset:0
v_mfma_f32_16x16x32_bf16 v[20:23],a[132:135],v[32:35],v[20:23]
ds_read_b128 v[60:63],v2 offset:23552
v_mfma_f32_16x16x32_bf16 v[16:19],a[136:139],v[36:39],v[16:19]
ds_read_b128 v[32:35],v2 offset:24576
v_mfma_f32_16x16x32_bf16 v[20:23],a[140:143],v[36:39],v[20:23]
v_mfma_f32_16x16x32_bf16 v[16:19],a[144:147],v[40:43],v[16:19]
s_waitcnt vmcnt(11) lgkmcnt(2)
ds_read_b128 v[36:39],v2 offset:25600
v_mfma_f32_16x16x32_bf16 v[20:23],a[148:151],v[40:43],v[20:23]
s_barrier
v_mfma_f32_16x16x32_bf16 v[16:19],a[152:155],v[44:47],v[16:19]
ds_read_b128 v[40:43],v2 offset:26624
ds_read_b32 v15,v7 offset:0
v_mfma_f32_16x16x32_bf16 v[20:23],a[156:159],v[44:47],v[20:23]
s_waitcnt lgkmcnt(4)
v_mfma_f32_16x16x32_bf16 v[16:19],a[160:163],v[48:51],v[16:19]
ds_read_b128 v[44:47],v2 offset:27648
v_mfma_f32_16x16x32_bf16 v[20:23],a[164:167],v[48:51],v[20:23]
v_mfma_f32_16x16x32_bf16 v[16:19],a[168:171],v[52:55],v[16:19]
s_waitcnt lgkmcnt(1)
ds_read_b128 v[48:51],v2 offset:28672
v_mfma_f32_16x16x32_bf16 v[20:23],a[172:175],v[52:55],v[20:23]
v_cmp_eq_u32 vcc,s60,v15
v_mfma_f32_16x16x32_bf16 v[16:19],a[176:179],v[56:59],v[16:19]
s_cmp_eq_u32 vcc_lo,-1
ds_read_b128 v[52:55],v2 offset:29696
s_cselect_b32 s67,s86,s59
v_mfma_f32_16x16x32_bf16 v[20:23],a[180:183],v[56:59],v[20:23]
buffer_load_dwordx4 v0,s[36:39],s67 offen offset:0 sc1 lds
v_mfma_f32_16x16x32_bf16 v[16:19],a[184:187],v[60:63],v[16:19]
s_xor_b32 s63,s63,65536
ds_read_b128 v[56:59],v2 offset:30720
s_add_u32 s53,s52,s55
v_mfma_f32_16x16x32_bf16 v[20:23],a[188:191],v[60:63],v[20:23]
s_waitcnt lgkmcnt(3)
s_cmp_eq_u32 s53,s56
s_cselect_b32 s53,s57,s53
v_mfma_f32_16x16x32_bf16 v[16:19],a[192:195],v[32:35],v[16:19]
s_waitcnt vmcnt(8)
ds_read_b128 v[60:63],v2 offset:31744
s_cmp_eq_u32 s53,s58
v_mfma_f32_16x16x32_bf16 v[20:23],a[196:199],v[32:35],v[20:23]
s_barrier
v_mfma_f32_16x16x32_bf16 v[16:19],a[200:203],v[36:39],v[16:19]
s_cselect_b32 s53,s59,s53
ds_read_b128 v[32:35],v2 offset:32768
ds_read_b128 v[80:83],v8 offset:0
v_mfma_f32_16x16x32_bf16 v[20:23],a[204:207],v[36:39],v[20:23]
buffer_load_dwordx4 v0,s[36:39],s67 offen offset:1024 sc1 lds
v_mfma_f32_16x16x32_bf16 v[16:19],a[208:211],v[40:43],v[16:19]
s_cmp_ge_u32 s66,s59)"
R"(
ds_read_b128 v[36:39],v2 offset:33792
ds_read_b64 v[88:89],v9 offset:1024
v_mfma_f32_16x16x32_bf16 v[20:23],a[212:215],v[40:43],v[20:23]
v_mfma_f32_16x16x32_bf16 v[16:19],a[216:219],v[44:47],v[16:19]
ds_read_b128 v[40:43],v2 offset:34816
v_mfma_f32_16x16x32_bf16 v[20:23],a[220:223],v[44:47],v[20:23]
s_waitcnt lgkmcnt(5)
buffer_load_dwordx4 v0,s[36:39],s67 offen offset:2048 sc1 lds
v_mfma_f32_16x16x32_bf16 v[16:19],a[224:227],v[48:51],v[16:19]
ds_read_b128 v[44:47],v2 offset:35840
v_mfma_f32_16x16x32_bf16 v[20:23],a[228:231],v[48:51],v[20:23]
v_mfma_f32_16x16x32_bf16 v[16:19],a[232:235],v[52:55],v[16:19]
s_waitcnt lgkmcnt(2)
ds_read_b128 v[48:51],v2 offset:36864
v_mfma_f32_16x16x32_bf16 v[20:23],a[236:239],v[52:55],v[20:23]
buffer_load_dwordx4 v0,s[36:39],s67 offen offset:3072 sc1 lds
v_mfma_f32_16x16x32_bf16 v[16:19],a[240:243],v[56:59],v[16:19]
ds_read_b128 v[52:55],v2 offset:37888
v_mfma_f32_16x16x32_bf16 v[20:23],a[244:247],v[56:59],v[20:23]
v_mfma_f32_16x16x32_bf16 v[16:19],a[248:251],v[60:63],v[16:19]
s_waitcnt lgkmcnt(2)
ds_read_b128 v[56:59],v2 offset:38912
v_mfma_f32_16x16x32_bf16 v[20:23],a[252:255],v[60:63],v[20:23]
s_cbranch_scc1 Lg2_slow1_%=
Lg2_d1w1_%=:
v_mfma_f32_16x16x32_bf16 v[24:27],a[0:3],v[32:35],0
s_add_u32 m0,s62,16384
ds_read_b128 v[60:63],v2 offset:39936
v_lshlrev_b32 v64,16,v80
v_mfma_f32_16x16x32_bf16 v[28:31],a[4:7],v[32:35],0
s_lshr_b32 s73,s48,0
v_and_b32 v65,0xffff0000,v80
v_lshlrev_b32 v66,16,v81
v_mfma_f32_16x16x32_bf16 v[24:27],a[8:11],v[36:39],v[24:27]
buffer_load_dwordx4 v1,s[36:39],s67 offen offset:0 sc1 lds
v_mfma_f32_16x16x32_bf16 v[28:31],a[12:15],v[36:39],v[28:31]
s_xor_b32 s87,s63,65536
ds_read_b128 v[32:35],v2 offset:40960
v_and_b32 v67,0xffff0000,v81
v_mfma_f32_16x16x32_bf16 v[24:27],a[16:19],v[40:43],v[24:27]
s_cmp_ge_u32 s67,s59
ds_read_b128 v[36:39],v2 offset:41984
v_lshlrev_b32 v68,16,v82
v_mfma_f32_16x16x32_bf16 v[28:31],a[20:23],v[40:43],v[28:31]
s_cselect_b32 s86,s50,s51
v_and_b32 v69,0xffff0000,v82
v_lshlrev_b32 v70,16,v83
v_mfma_f32_16x16x32_bf16 v[24:27],a[24:27],v[44:47],v[24:27]
buffer_load_dwordx4 v1,s[36:39],s67 offen offset:1024 sc1 lds
v_mfma_f32_16x16x32_bf16 v[28:31],a[28:31],v[44:47],v[28:31]
s_waitcnt lgkmcnt(2)
ds_read_b128 v[40:43],v2 offset:43008
v_and_b32 v71,0xffff0000,v83
v_mfma_f32_16x16x32_bf16 v[24:27],a[32:35],v[48:51],v[24:27]
s_cselect_b32 s87,s87,s64
ds_read_b128 v[44:47],v2 offset:44032
v_fma_f32 v64,v64,v89,v88
v_mfma_f32_16x16x32_bf16 v[28:31],a[36:39],v[48:51],v[28:31]
s_lshr_b32 s72,s67,6
v_fma_f32 v65,v65,v89,v88
v_fma_f32 v66,v66,v89,v88
v_mfma_f32_16x16x32_bf16 v[24:27],a[40:43],v[52:55],v[24:27]
buffer_load_dwordx4 v1,s[36:39],s67 offen offset:2048 sc1 lds
v_mfma_f32_16x16x32_bf16 v[28:31],a[44:47],v[52:55],v[28:31]
s_lshr_b32 s71,s62,2
ds_read_b128 v[48:51],v2 offset:45056
v_fma_f32 v67,v67,v89,v88
v_mfma_f32_16x16x32_bf16 v[24:27],a[48:51],v[56:59],v[24:27]
s_mov_b32 s62,s87
ds_read_b128 v[52:55],v2 offset:46080
v_fma_f32 v68,v68,v89,v88
v_mfma_f32_16x16x32_bf16 v[28:31],a[52:55],v[56:59],v[28:31]
s_mov_b64 s[44:45],s[46:47]
v_fma_f32 v69,v69,v89,v88
v_fma_f32 v70,v70,v89,v88
v_mfma_f32_16x16x32_bf16 v[24:27],a[56:59],v[60:63],v[24:27]
buffer_load_dwordx4 v1,s[36:39],s67 offen offset:3072 sc1 lds
v_mfma_f32_16x16x32_bf16 v[28:31],a[60:63],v[60:63],v[28:31]
s_waitcnt lgkmcnt(2)
ds_read_b128 v[56:59],v2 offset:47104
v_fma_f32 v71,v71,v89,v88
v_mfma_f32_16x16x32_bf16 v[24:27],a[64:67],v[32:35],v[24:27]
s_waitcnt vmcnt(9)
ds_read_b128 v[60:63],v2 offset:48128
v_add_f32 v16,v16,v64
v_mfma_f32_16x16x32_bf16 v[28:31],a[68:71],v[32:35],v[28:31]
s_barrier
v_mfma_f32_16x16x32_bf16 v[24:27],a[72:75],v[36:39],v[24:27]
s_add_u32 m0,s71,s88
ds_read_b128 v[32:35],v2 offset:49152
v_add_f32 v17,v17,v65
v_mfma_f32_16x16x32_bf16 v[28:31],a[76:79],v[36:39],v[28:31]
s_mov_b64 s[46:47],s[48:49]
v_add_f32 v18,v18,v66
v_add_f32 v19,v19,v67
v_mfma_f32_16x16x32_bf16 v[24:27],a[80:83],v[40:43],v[24:27]
buffer_load_dwordx4 v10,s[36:39],s67 offen offset:0 sc1 lds
v_mfma_f32_16x16x32_bf16 v[28:31],a[84:87],v[40:43],v[28:31]
s_mov_b64 s[48:49],s[50:51]
ds_read_b128 v[36:39],v2 offset:50176
v_add_f32 v20,v20,v68
v_mfma_f32_16x16x32_bf16 v[24:27],a[88:91],v[44:47],v[24:27]
buffer_load_dword v11,s[36:39],s72 offen offset:1024 sc1 lds
v_mfma_f32_16x16x32_bf16 v[28:31],a[92:95],v[44:47],v[28:31]
s_waitcnt lgkmcnt(2)
ds_read_b128 v[40:43],v2 offset:51200
v_add_f32 v21,v21,v69
v_mfma_f32_16x16x32_bf16 v[24:27],a[96:99],v[48:51],v[24:27]
s_mov_b32 m0,s62
v_add_f32 v22,v22,v70
v_add_f32 v23,v23,v71
v_cvt_pk_bf16_f32 v72,v16,v17
v_cvt_pk_bf16_f32 v73,v18,v19
v_cvt_pk_bf16_f32 v74,v20,v21
v_cvt_pk_bf16_f32 v75,v22,v23
v_mfma_f32_16x16x32_bf16 v[28:31],a[100:103],v[48:51],v[28:31]
s_mov_b64 s[50:51],s[52:53]
ds_read_b128 v[44:47],v2 offset:52224
buffer_store_dwordx4 v[72:75],v3,s[36:39],s73 offen offset:0 sc0 sc1
v_add_f32 v64,v16,v17
v_mul_f32 v65,v16,v16
v_fma_f32 v65,v17,v17,v65
v_mfma_f32_16x16x32_bf16 v[24:27],a[104:107],v[52:55],v[24:27]
ds_read_b128 v[48:51],v2 offset:53248
v_add_f32 v64,v64,v18
v_mfma_f32_16x16x32_bf16 v[28:31],a[108:111],v[52:55],v[28:31]
v_fma_f32 v65,v18,v18,v65
v_add_f32 v64,v64,v19
v_mfma_f32_16x16x32_bf16 v[24:27],a[112:115],v[56:59],v[24:27]
ds_read_b128 v[52:55],v2 offset:54272
v_fma_f32 v65,v19,v19,v65
v_mfma_f32_16x16x32_bf16 v[28:31],a[116:119],v[56:59],v[28:31]
v_add_f32 v64,v64,v20
v_fma_f32 v65,v20,v20,v65
v_add_f32 v64,v64,v21
v_fma_f32 v65,v21,v21,v65
v_add_f32 v64,v64,v22
v_fma_f32 v65,v22,v22,v65
v_add_f32 v64,v64,v23
v_fma_f32 v65,v23,v23,v65
v_mfma_f32_16x16x32_bf16 v[24:27],a[120:123],v[60:63],v[24:27]
s_nop 0
v_permlane16_swap_b32 v64,v65
v_add_f32 v64,v64,v65
v_mov_b32 v65,v64
v_mfma_f32_16x16x32_bf16 v[28:31],a[124:127],v[60:63],v[28:31]
s_waitcnt lgkmcnt(2)
ds_read_b128 v[56:59],v2 offset:55296
v_permlane32_swap_b32 v64,v65
v_add_f32 v64,v64,v65
v_mfma_f32_16x16x32_bf16 v[24:27],a[128:131],v[32:35],v[24:27]
ds_write_b32 v13,v64 offset:2048
v_mfma_f32_16x16x32_bf16 v[28:31],a[132:135],v[32:35],v[28:31]
ds_read_b128 v[60:63],v2 offset:56320
v_mfma_f32_16x16x32_bf16 v[24:27],a[136:139],v[36:39],v[24:27]
ds_read_b128 v[32:35],v2 offset:57344
v_mfma_f32_16x16x32_bf16 v[28:31],a[140:143],v[36:39],v[28:31]
v_mfma_f32_16x16x32_bf16 v[24:27],a[144:147],v[40:43],v[24:27]
s_waitcnt vmcnt(11) lgkmcnt(2)
ds_read_b128 v[36:39],v2 offset:58368
v_mfma_f32_16x16x32_bf16 v[28:31],a[148:151],v[40:43],v[28:31]
s_barrier
v_mfma_f32_16x16x32_bf16 v[24:27],a[152:155],v[44:47],v[24:27]
ds_read_b128 v[40:43],v2 offset:59392
ds_read_b32 v15,v7 offset:256
v_mfma_f32_16x16x32_bf16 v[28:31],a[156:159],v[44:47],v[28:31]
s_waitcnt lgkmcnt(4)
v_mfma_f32_16x16x32_bf16 v[24:27],a[160:163],v[48:51],v[24:27]
ds_read_b128 v[44:47],v2 offset:60416
v_mfma_f32_16x16x32_bf16 v[28:31],a[164:167],v[48:51],v[28:31]
v_mfma_f32_16x16x32_bf16 v[24:27],a[168:171],v[52:55],v[24:27]
s_waitcnt lgkmcnt(1)
ds_read_b128 v[48:51],v2 offset:61440
v_mfma_f32_16x16x32_bf16 v[28:31],a[172:175],v[52:55],v[28:31]
v_cmp_eq_u32 vcc,s60,v15
v_mfma_f32_16x16x32_bf16 v[24:27],a[176:179],v[56:59],v[24:27]
s_cmp_eq_u32 vcc_lo,-1
ds_read_b128 v[52:55],v2 offset:62464
s_cselect_b32 s66,s86,s59
v_mfma_f32_16x16x32_bf16 v[28:31],a[180:183],v[56:59],v[28:31]
buffer_load_dwordx4 v0,s[36:39],s66 offen offset:0 sc1 lds
v_mfma_f32_16x16x32_bf16 v[24:27],a[184:187],v[60:63],v[24:27]
s_xor_b32 s64,s64,65536
ds_read_b128 v[56:59],v2 offset:63488
s_add_u32 s74,s53,s55
v_mfma_f32_16x16x32_bf16 v[28:31],a[188:191],v[60:63],v[28:31]
s_waitcnt lgkmcnt(3)
s_cmp_eq_u32 s74,s56
s_cselect_b32 s74,s57,s74
v_mfma_f32_16x16x32_bf16 v[24:27],a[192:195],v[32:35],v[24:27]
s_waitcnt vmcnt(8)
ds_read_b128 v[60:63],v2 offset:64512
v_xor_b32 v2,65536,v2
v_mfma_f32_16x16x32_bf16 v[28:31],a[196:199],v[32:35],v[28:31]
s_barrier
v_mfma_f32_16x16x32_bf16 v[24:27],a[200:203],v[36:39],v[24:27]
s_cmp_eq_u32 s74,s58
ds_read_b128 v[32:35],v2 offset:0
ds_read_b128 v[84:87],v8 offset:8192
v_mfma_f32_16x16x32_bf16 v[28:31],a[204:207],v[36:39],v[28:31])"
R"(
buffer_load_dwordx4 v0,s[36:39],s66 offen offset:1024 sc1 lds
v_mfma_f32_16x16x32_bf16 v[24:27],a[208:211],v[40:43],v[24:27]
s_cselect_b32 s74,s59,s74
ds_read_b128 v[36:39],v2 offset:1024
ds_read_b64 v[90:91],v9 offset:9216
v_mfma_f32_16x16x32_bf16 v[28:31],a[212:215],v[40:43],v[28:31]
s_mov_b32 s52,s74
v_xor_b32 v8,16384,v8
v_xor_b32 v9,16384,v9
v_mfma_f32_16x16x32_bf16 v[24:27],a[216:219],v[44:47],v[24:27]
s_cmp_lt_u32 s67,s59
ds_read_b128 v[40:43],v2 offset:2048
v_mfma_f32_16x16x32_bf16 v[28:31],a[220:223],v[44:47],v[28:31]
s_waitcnt lgkmcnt(5)
buffer_load_dwordx4 v0,s[36:39],s66 offen offset:2048 sc1 lds
v_mfma_f32_16x16x32_bf16 v[24:27],a[224:227],v[48:51],v[24:27]
ds_read_b128 v[44:47],v2 offset:3072
v_mfma_f32_16x16x32_bf16 v[28:31],a[228:231],v[48:51],v[28:31]
v_mfma_f32_16x16x32_bf16 v[24:27],a[232:235],v[52:55],v[24:27]
s_waitcnt lgkmcnt(2)
ds_read_b128 v[48:51],v2 offset:4096
v_mfma_f32_16x16x32_bf16 v[28:31],a[236:239],v[52:55],v[28:31]
buffer_load_dwordx4 v0,s[36:39],s66 offen offset:3072 sc1 lds
v_mfma_f32_16x16x32_bf16 v[24:27],a[240:243],v[56:59],v[24:27]
ds_read_b128 v[52:55],v2 offset:5120
v_mfma_f32_16x16x32_bf16 v[28:31],a[244:247],v[56:59],v[28:31]
v_mfma_f32_16x16x32_bf16 v[24:27],a[248:251],v[60:63],v[24:27]
s_waitcnt lgkmcnt(2)
ds_read_b128 v[56:59],v2 offset:6144
v_mfma_f32_16x16x32_bf16 v[28:31],a[252:255],v[60:63],v[28:31]
s_cbranch_scc1 Lg2_d0w1_%=
s_branch Lg2_slow0_%=
Lg2_d0w2_%=:
v_mfma_f32_16x16x32_bf16 v[16:19],a[0:3],v[32:35],0
s_add_u32 m0,s62,16384
ds_read_b128 v[60:63],v2 offset:7168
v_lshlrev_b32 v64,16,v84
v_mfma_f32_16x16x32_bf16 v[20:23],a[4:7],v[32:35],0
s_lshr_b32 s73,s47,0
v_and_b32 v65,0xffff0000,v84
v_lshlrev_b32 v66,16,v85
v_mfma_f32_16x16x32_bf16 v[16:19],a[8:11],v[36:39],v[16:19]
s_lshr_b32 s85,s47,5
ds_read_b128 v[32:35],v2 offset:8192
v_and_b32 v67,0xffff0000,v85
v_mfma_f32_16x16x32_bf16 v[20:23],a[12:15],v[36:39],v[20:23]
buffer_load_dwordx4 v1,s[36:39],s66 offen offset:0 sc1 lds
v_mfma_f32_16x16x32_bf16 v[16:19],a[16:19],v[40:43],v[16:19]
s_xor_b32 s87,s64,65536
ds_read_b128 v[36:39],v2 offset:9216
v_lshlrev_b32 v68,16,v86
v_mfma_f32_16x16x32_bf16 v[20:23],a[20:23],v[40:43],v[20:23]
s_cmp_ge_u32 s66,s59
v_and_b32 v69,0xffff0000,v86
v_lshlrev_b32 v70,16,v87
v_mfma_f32_16x16x32_bf16 v[16:19],a[24:27],v[44:47],v[16:19]
s_cselect_b32 s86,s49,s50
ds_read_b128 v[40:43],v2 offset:10240
v_and_b32 v71,0xffff0000,v87
v_mfma_f32_16x16x32_bf16 v[20:23],a[28:31],v[44:47],v[20:23]
s_waitcnt lgkmcnt(3)
buffer_load_dwordx4 v1,s[36:39],s66 offen offset:1024 sc1 lds
v_mfma_f32_16x16x32_bf16 v[16:19],a[32:35],v[48:51],v[16:19]
s_cselect_b32 s87,s87,s63
ds_read_b128 v[44:47],v2 offset:11264
v_fma_f32 v64,v64,v91,v90
v_mfma_f32_16x16x32_bf16 v[20:23],a[36:39],v[48:51],v[20:23]
s_lshr_b32 s72,s66,6
v_fma_f32 v65,v65,v91,v90
v_fma_f32 v66,v66,v91,v90
v_mfma_f32_16x16x32_bf16 v[16:19],a[40:43],v[52:55],v[16:19]
s_lshr_b32 s71,s62,2
ds_read_b128 v[48:51],v2 offset:12288
v_fma_f32 v67,v67,v91,v90
v_mfma_f32_16x16x32_bf16 v[20:23],a[44:47],v[52:55],v[20:23]
buffer_load_dwordx4 v1,s[36:39],s66 offen offset:2048 sc1 lds
v_mfma_f32_16x16x32_bf16 v[16:19],a[48:51],v[56:59],v[16:19]
s_mov_b32 s62,s87
ds_read_b128 v[52:55],v2 offset:13312
v_fma_f32 v68,v68,v91,v90
v_mfma_f32_16x16x32_bf16 v[20:23],a[52:55],v[56:59],v[20:23]
v_fma_f32 v69,v69,v91,v90
v_fma_f32 v70,v70,v91,v90
v_mfma_f32_16x16x32_bf16 v[16:19],a[56:59],v[60:63],v[16:19]
ds_read_b128 v[56:59],v2 offset:14336
v_fma_f32 v71,v71,v91,v90
v_mfma_f32_16x16x32_bf16 v[20:23],a[60:63],v[60:63],v[20:23]
s_waitcnt lgkmcnt(3)
buffer_load_dwordx4 v1,s[36:39],s66 offen offset:3072 sc1 lds
v_mfma_f32_16x16x32_bf16 v[16:19],a[64:67],v[32:35],v[16:19]
s_waitcnt vmcnt(10)
ds_read_b128 v[60:63],v2 offset:15360
v_add_f32 v24,v24,v64
v_mfma_f32_16x16x32_bf16 v[20:23],a[68:71],v[32:35],v[20:23]
s_barrier
v_mfma_f32_16x16x32_bf16 v[16:19],a[72:75],v[36:39],v[16:19]
s_add_u32 m0,s71,s88
ds_read_b128 v[32:35],v2 offset:16384
v_add_f32 v25,v25,v65
v_mfma_f32_16x16x32_bf16 v[20:23],a[76:79],v[36:39],v[20:23]
v_add_f32 v26,v26,v66
v_add_f32 v27,v27,v67
v_mfma_f32_16x16x32_bf16 v[16:19],a[80:83],v[40:43],v[16:19]
ds_read_b128 v[36:39],v2 offset:17408
v_add_f32 v28,v28,v68
v_mfma_f32_16x16x32_bf16 v[20:23],a[84:87],v[40:43],v[20:23]
buffer_load_dwordx4 v10,s[36:39],s66 offen offset:0 sc1 lds
v_mfma_f32_16x16x32_bf16 v[16:19],a[88:91],v[44:47],v[16:19]
buffer_load_dword v11,s[36:39],s72 offen offset:1024 sc1 lds
v_mfma_f32_16x16x32_bf16 v[20:23],a[92:95],v[44:47],v[20:23]
s_waitcnt lgkmcnt(2)
ds_read_b128 v[40:43],v2 offset:18432
v_add_f32 v29,v29,v69
v_mfma_f32_16x16x32_bf16 v[16:19],a[96:99],v[48:51],v[16:19]
s_mov_b32 m0,s62
ds_read_b128 v[44:47],v2 offset:19456
v_add_f32 v30,v30,v70
v_mfma_f32_16x16x32_bf16 v[20:23],a[100:103],v[48:51],v[20:23]
v_add_f32 v31,v31,v71
v_cvt_pk_bf16_f32 v72,v24,v25
v_cvt_pk_bf16_f32 v73,v26,v27
v_cvt_pk_bf16_f32 v74,v28,v29
v_cvt_pk_bf16_f32 v75,v30,v31
v_mfma_f32_16x16x32_bf16 v[16:19],a[104:107],v[52:55],v[16:19]
buffer_store_dwordx4 v[72:75],v3,s[36:39],s73 offen offset:0 sc0 sc1
ds_read_b128 v[48:51],v2 offset:20480
v_add_f32 v64,v24,v25
v_mul_f32 v65,v24,v24
v_mfma_f32_16x16x32_bf16 v[20:23],a[108:111],v[52:55],v[20:23]
v_fma_f32 v65,v25,v25,v65
v_add_f32 v64,v64,v26
v_mfma_f32_16x16x32_bf16 v[16:19],a[112:115],v[56:59],v[16:19]
ds_read_b128 v[52:55],v2 offset:21504
v_fma_f32 v65,v26,v26,v65
v_mfma_f32_16x16x32_bf16 v[20:23],a[116:119],v[56:59],v[20:23]
v_add_f32 v64,v64,v27
v_fma_f32 v65,v27,v27,v65
v_add_f32 v64,v64,v28
v_fma_f32 v65,v28,v28,v65
v_add_f32 v64,v64,v29
v_fma_f32 v65,v29,v29,v65
v_add_f32 v64,v64,v30
v_fma_f32 v65,v30,v30,v65
v_add_f32 v64,v64,v31
v_fma_f32 v65,v31,v31,v65
v_mfma_f32_16x16x32_bf16 v[16:19],a[120:123],v[60:63],v[16:19]
s_nop 0
v_permlane16_swap_b32 v64,v65
v_add_f32 v64,v64,v65
v_mov_b32 v65,v64
v_mfma_f32_16x16x32_bf16 v[20:23],a[124:127],v[60:63],v[20:23]
s_waitcnt lgkmcnt(2)
ds_read_b128 v[56:59],v2 offset:22528
v_permlane32_swap_b32 v64,v65
v_add_f32 v64,v64,v65
v_mfma_f32_16x16x32_bf16 v[16:19],a[128:131],v[32:35],v[16:19]
ds_write_b32 v13,v64 offset:0
v_mfma_f32_16x16x32_bf16 v[20:23],a[132:135],v[32:35],v[20:23]
ds_read_b128 v[60:63],v2 offset:23552
v_mfma_f32_16x16x32_bf16 v[16:19],a[136:139],v[36:39],v[16:19]
ds_read_b128 v[32:35],v2 offset:24576
v_mfma_f32_16x16x32_bf16 v[20:23],a[140:143],v[36:39],v[20:23]
v_mfma_f32_16x16x32_bf16 v[16:19],a[144:147],v[40:43],v[16:19]
s_waitcnt vmcnt(11) lgkmcnt(2)
ds_read_b128 v[36:39],v2 offset:25600
v_mfma_f32_16x16x32_bf16 v[20:23],a[148:151],v[40:43],v[20:23]
s_barrier
v_mfma_f32_16x16x32_bf16 v[16:19],a[152:155],v[44:47],v[16:19]
ds_read_b128 v[40:43],v2 offset:26624
ds_read_b32 v15,v7 offset:0
v_mfma_f32_16x16x32_bf16 v[20:23],a[156:159],v[44:47],v[20:23]
s_waitcnt lgkmcnt(4)
ds_read_b128 v[76:79],v14 offset:0
v_mfma_f32_16x16x32_bf16 v[16:19],a[160:163],v[48:51],v[16:19]
ds_read_b128 v[44:47],v2 offset:27648
v_mfma_f32_16x16x32_bf16 v[20:23],a[164:167],v[48:51],v[20:23]
v_mfma_f32_16x16x32_bf16 v[16:19],a[168:171],v[52:55],v[16:19]
s_waitcnt lgkmcnt(2)
ds_read_b128 v[48:51],v2 offset:28672
v_mfma_f32_16x16x32_bf16 v[20:23],a[172:175],v[52:55],v[20:23]
s_waitcnt lgkmcnt(2)
v_cmp_eq_u32 vcc,s60,v15
v_add_f32 v76,v76,v77
v_add_f32 v78,v78,v79
v_add_f32 v76,v76,v78
v_mfma_f32_16x16x32_bf16 v[16:19],a[176:179],v[56:59],v[16:19]
buffer_store_dword v76,v12,s[36:39],s85 offen offset:0 sc0 sc1
ds_read_b128 v[52:55],v2 offset:29696
v_mfma_f32_16x16x32_bf16 v[20:23],a[180:183],v[56:59],v[20:23]
s_cmp_eq_u32 vcc_lo,-1
s_cselect_b32 s67,s86,s59
s_xor_b32 s63,s63,65536
v_mfma_f32_16x16x32_bf16 v[16:19],a[184:187],v[60:63],v[16:19]
buffer_load_dwordx4 v0,s[36:39],s67 offen offset:0 sc1 lds
v_mfma_f32_16x16x32_bf16 v[20:23],a[188:191],v[60:63],v[20:23]
s_waitcnt lgkmcnt(2)
ds_read_b128 v[56:59],v2 offset:30720
s_add_u32 s53,s52,s55
v_mfma_f32_16x16x32_bf16 v[16:19],a[192:195],v[32:35],v[16:19]
s_waitcnt vmcnt(9))"
R"(
ds_read_b128 v[60:63],v2 offset:31744
s_cmp_eq_u32 s53,s56
v_mfma_f32_16x16x32_bf16 v[20:23],a[196:199],v[32:35],v[20:23]
s_barrier
v_mfma_f32_16x16x32_bf16 v[16:19],a[200:203],v[36:39],v[16:19]
s_cselect_b32 s53,s57,s53
ds_read_b128 v[32:35],v2 offset:32768
ds_read_b128 v[80:83],v8 offset:0
v_mfma_f32_16x16x32_bf16 v[20:23],a[204:207],v[36:39],v[20:23]
s_cmp_eq_u32 s53,s58
ds_read_b64 v[88:89],v9 offset:1024
s_cselect_b32 s53,s59,s53
v_mfma_f32_16x16x32_bf16 v[16:19],a[208:211],v[40:43],v[16:19]
buffer_load_dwordx4 v0,s[36:39],s67 offen offset:1024 sc1 lds
v_mfma_f32_16x16x32_bf16 v[20:23],a[212:215],v[40:43],v[20:23]
s_cmp_ge_u32 s66,s59
ds_read_b128 v[36:39],v2 offset:33792
v_mfma_f32_16x16x32_bf16 v[16:19],a[216:219],v[44:47],v[16:19]
ds_read_b128 v[40:43],v2 offset:34816
v_mfma_f32_16x16x32_bf16 v[20:23],a[220:223],v[44:47],v[20:23]
s_waitcnt lgkmcnt(5)
v_mfma_f32_16x16x32_bf16 v[16:19],a[224:227],v[48:51],v[16:19]
buffer_load_dwordx4 v0,s[36:39],s67 offen offset:2048 sc1 lds
v_mfma_f32_16x16x32_bf16 v[20:23],a[228:231],v[48:51],v[20:23]
s_waitcnt lgkmcnt(2)
ds_read_b128 v[44:47],v2 offset:35840
v_mfma_f32_16x16x32_bf16 v[16:19],a[232:235],v[52:55],v[16:19]
ds_read_b128 v[48:51],v2 offset:36864
v_mfma_f32_16x16x32_bf16 v[20:23],a[236:239],v[52:55],v[20:23]
v_mfma_f32_16x16x32_bf16 v[16:19],a[240:243],v[56:59],v[16:19]
buffer_load_dwordx4 v0,s[36:39],s67 offen offset:3072 sc1 lds
v_mfma_f32_16x16x32_bf16 v[20:23],a[244:247],v[56:59],v[20:23]
ds_read_b128 v[52:55],v2 offset:37888
v_mfma_f32_16x16x32_bf16 v[16:19],a[248:251],v[60:63],v[16:19]
s_waitcnt lgkmcnt(2)
ds_read_b128 v[56:59],v2 offset:38912
v_mfma_f32_16x16x32_bf16 v[20:23],a[252:255],v[60:63],v[20:23]
s_cbranch_scc1 Lg2_slow1_%=
Lg2_d1w2_%=:
v_mfma_f32_16x16x32_bf16 v[24:27],a[0:3],v[32:35],0
s_add_u32 m0,s62,16384
ds_read_b128 v[60:63],v2 offset:39936
v_lshlrev_b32 v64,16,v80
v_mfma_f32_16x16x32_bf16 v[28:31],a[4:7],v[32:35],0
s_lshr_b32 s73,s48,0
v_and_b32 v65,0xffff0000,v80
v_lshlrev_b32 v66,16,v81
v_mfma_f32_16x16x32_bf16 v[24:27],a[8:11],v[36:39],v[24:27]
s_lshr_b32 s85,s48,5
ds_read_b128 v[32:35],v2 offset:40960
v_and_b32 v67,0xffff0000,v81
v_mfma_f32_16x16x32_bf16 v[28:31],a[12:15],v[36:39],v[28:31]
buffer_load_dwordx4 v1,s[36:39],s67 offen offset:0 sc1 lds
v_mfma_f32_16x16x32_bf16 v[24:27],a[16:19],v[40:43],v[24:27]
s_xor_b32 s87,s63,65536
ds_read_b128 v[36:39],v2 offset:41984
v_lshlrev_b32 v68,16,v82
v_mfma_f32_16x16x32_bf16 v[28:31],a[20:23],v[40:43],v[28:31]
s_cmp_ge_u32 s67,s59
v_and_b32 v69,0xffff0000,v82
v_lshlrev_b32 v70,16,v83
v_mfma_f32_16x16x32_bf16 v[24:27],a[24:27],v[44:47],v[24:27]
s_cselect_b32 s86,s50,s51
ds_read_b128 v[40:43],v2 offset:43008
v_and_b32 v71,0xffff0000,v83
v_mfma_f32_16x16x32_bf16 v[28:31],a[28:31],v[44:47],v[28:31]
s_waitcnt lgkmcnt(3)
buffer_load_dwordx4 v1,s[36:39],s67 offen offset:1024 sc1 lds
v_mfma_f32_16x16x32_bf16 v[24:27],a[32:35],v[48:51],v[24:27]
s_cselect_b32 s87,s87,s64
ds_read_b128 v[44:47],v2 offset:44032
v_fma_f32 v64,v64,v89,v88
v_mfma_f32_16x16x32_bf16 v[28:31],a[36:39],v[48:51],v[28:31]
s_lshr_b32 s72,s67,6
v_fma_f32 v65,v65,v89,v88
v_fma_f32 v66,v66,v89,v88
v_mfma_f32_16x16x32_bf16 v[24:27],a[40:43],v[52:55],v[24:27]
s_lshr_b32 s71,s62,2
ds_read_b128 v[48:51],v2 offset:45056
v_fma_f32 v67,v67,v89,v88
v_mfma_f32_16x16x32_bf16 v[28:31],a[44:47],v[52:55],v[28:31]
buffer_load_dwordx4 v1,s[36:39],s67 offen offset:2048 sc1 lds
v_mfma_f32_16x16x32_bf16 v[24:27],a[48:51],v[56:59],v[24:27]
s_mov_b32 s62,s87
ds_read_b128 v[52:55],v2 offset:46080
v_fma_f32 v68,v68,v89,v88
v_mfma_f32_16x16x32_bf16 v[28:31],a[52:55],v[56:59],v[28:31]
s_mov_b64 s[44:45],s[46:47]
v_fma_f32 v69,v69,v89,v88
v_fma_f32 v70,v70,v89,v88
v_mfma_f32_16x16x32_bf16 v[24:27],a[56:59],v[60:63],v[24:27]
s_mov_b64 s[46:47],s[48:49]
ds_read_b128 v[56:59],v2 offset:47104
v_fma_f32 v71,v71,v89,v88
v_mfma_f32_16x16x32_bf16 v[28:31],a[60:63],v[60:63],v[28:31]
s_waitcnt lgkmcnt(3)
buffer_load_dwordx4 v1,s[36:39],s67 offen offset:3072 sc1 lds
v_mfma_f32_16x16x32_bf16 v[24:27],a[64:67],v[32:35],v[24:27]
s_waitcnt vmcnt(10)
ds_read_b128 v[60:63],v2 offset:48128
v_add_f32 v16,v16,v64
v_mfma_f32_16x16x32_bf16 v[28:31],a[68:71],v[32:35],v[28:31]
s_barrier
v_mfma_f32_16x16x32_bf16 v[24:27],a[72:75],v[36:39],v[24:27]
s_add_u32 m0,s71,s88
ds_read_b128 v[32:35],v2 offset:49152
v_add_f32 v17,v17,v65
v_mfma_f32_16x16x32_bf16 v[28:31],a[76:79],v[36:39],v[28:31]
s_mov_b64 s[48:49],s[50:51]
v_add_f32 v18,v18,v66
v_add_f32 v19,v19,v67
v_mfma_f32_16x16x32_bf16 v[24:27],a[80:83],v[40:43],v[24:27]
s_mov_b64 s[50:51],s[52:53]
ds_read_b128 v[36:39],v2 offset:50176
v_add_f32 v20,v20,v68
v_mfma_f32_16x16x32_bf16 v[28:31],a[84:87],v[40:43],v[28:31]
buffer_load_dwordx4 v10,s[36:39],s67 offen offset:0 sc1 lds
v_mfma_f32_16x16x32_bf16 v[24:27],a[88:91],v[44:47],v[24:27]
buffer_load_dword v11,s[36:39],s72 offen offset:1024 sc1 lds
v_mfma_f32_16x16x32_bf16 v[28:31],a[92:95],v[44:47],v[28:31]
s_waitcnt lgkmcnt(2)
ds_read_b128 v[40:43],v2 offset:51200
v_add_f32 v21,v21,v69
v_mfma_f32_16x16x32_bf16 v[24:27],a[96:99],v[48:51],v[24:27]
s_mov_b32 m0,s62
ds_read_b128 v[44:47],v2 offset:52224
v_add_f32 v22,v22,v70
v_mfma_f32_16x16x32_bf16 v[28:31],a[100:103],v[48:51],v[28:31]
v_add_f32 v23,v23,v71
v_cvt_pk_bf16_f32 v72,v16,v17
v_cvt_pk_bf16_f32 v73,v18,v19
v_cvt_pk_bf16_f32 v74,v20,v21
v_cvt_pk_bf16_f32 v75,v22,v23
v_mfma_f32_16x16x32_bf16 v[24:27],a[104:107],v[52:55],v[24:27]
buffer_store_dwordx4 v[72:75],v3,s[36:39],s73 offen offset:0 sc0 sc1
ds_read_b128 v[48:51],v2 offset:53248
v_add_f32 v64,v16,v17
v_mul_f32 v65,v16,v16
v_mfma_f32_16x16x32_bf16 v[28:31],a[108:111],v[52:55],v[28:31]
v_fma_f32 v65,v17,v17,v65
v_add_f32 v64,v64,v18
v_mfma_f32_16x16x32_bf16 v[24:27],a[112:115],v[56:59],v[24:27]
ds_read_b128 v[52:55],v2 offset:54272
v_fma_f32 v65,v18,v18,v65
v_mfma_f32_16x16x32_bf16 v[28:31],a[116:119],v[56:59],v[28:31]
v_add_f32 v64,v64,v19
v_fma_f32 v65,v19,v19,v65
v_add_f32 v64,v64,v20
v_fma_f32 v65,v20,v20,v65
v_add_f32 v64,v64,v21
v_fma_f32 v65,v21,v21,v65
v_add_f32 v64,v64,v22
v_fma_f32 v65,v22,v22,v65
v_add_f32 v64,v64,v23
v_fma_f32 v65,v23,v23,v65
v_mfma_f32_16x16x32_bf16 v[24:27],a[120:123],v[60:63],v[24:27]
s_nop 0
v_permlane16_swap_b32 v64,v65
v_add_f32 v64,v64,v65
v_mov_b32 v65,v64
v_mfma_f32_16x16x32_bf16 v[28:31],a[124:127],v[60:63],v[28:31]
s_waitcnt lgkmcnt(2)
ds_read_b128 v[56:59],v2 offset:55296
v_permlane32_swap_b32 v64,v65
v_add_f32 v64,v64,v65
v_mfma_f32_16x16x32_bf16 v[24:27],a[128:131],v[32:35],v[24:27]
ds_write_b32 v13,v64 offset:2048
v_mfma_f32_16x16x32_bf16 v[28:31],a[132:135],v[32:35],v[28:31]
ds_read_b128 v[60:63],v2 offset:56320
v_mfma_f32_16x16x32_bf16 v[24:27],a[136:139],v[36:39],v[24:27]
ds_read_b128 v[32:35],v2 offset:57344
v_mfma_f32_16x16x32_bf16 v[28:31],a[140:143],v[36:39],v[28:31]
v_mfma_f32_16x16x32_bf16 v[24:27],a[144:147],v[40:43],v[24:27]
s_waitcnt vmcnt(11) lgkmcnt(2)
ds_read_b128 v[36:39],v2 offset:58368
v_mfma_f32_16x16x32_bf16 v[28:31],a[148:151],v[40:43],v[28:31]
s_barrier
v_mfma_f32_16x16x32_bf16 v[24:27],a[152:155],v[44:47],v[24:27]
ds_read_b128 v[40:43],v2 offset:59392
ds_read_b32 v15,v7 offset:256
v_mfma_f32_16x16x32_bf16 v[28:31],a[156:159],v[44:47],v[28:31]
s_waitcnt lgkmcnt(4)
ds_read_b128 v[76:79],v14 offset:2048
v_mfma_f32_16x16x32_bf16 v[24:27],a[160:163],v[48:51],v[24:27]
ds_read_b128 v[44:47],v2 offset:60416
v_mfma_f32_16x16x32_bf16 v[28:31],a[164:167],v[48:51],v[28:31]
v_mfma_f32_16x16x32_bf16 v[24:27],a[168:171],v[52:55],v[24:27]
s_waitcnt lgkmcnt(2)
ds_read_b128 v[48:51],v2 offset:61440
v_mfma_f32_16x16x32_bf16 v[28:31],a[172:175],v[52:55],v[28:31]
s_waitcnt lgkmcnt(2)
v_cmp_eq_u32 vcc,s60,v15
v_add_f32 v76,v76,v77
v_add_f32 v78,v78,v79
v_add_f32 v76,v76,v78
v_mfma_f32_16x16x32_bf16 v[24:27],a[176:179],v[56:59],v[24:27]
buffer_store_dword v76,v12,s[36:39],s85 offen offset:0 sc0 sc1
ds_read_b128 v[52:55],v2 offset:62464
v_mfma_f32_16x16x32_bf16 v[28:31],a[180:183],v[56:59],v[28:31]
s_cmp_eq_u32 vcc_lo,-1)"
R"(
s_cselect_b32 s66,s86,s59
s_xor_b32 s64,s64,65536
v_mfma_f32_16x16x32_bf16 v[24:27],a[184:187],v[60:63],v[24:27]
buffer_load_dwordx4 v0,s[36:39],s66 offen offset:0 sc1 lds
v_mfma_f32_16x16x32_bf16 v[28:31],a[188:191],v[60:63],v[28:31]
s_waitcnt lgkmcnt(2)
ds_read_b128 v[56:59],v2 offset:63488
s_add_u32 s74,s53,s55
v_mfma_f32_16x16x32_bf16 v[24:27],a[192:195],v[32:35],v[24:27]
s_waitcnt vmcnt(9)
ds_read_b128 v[60:63],v2 offset:64512
v_xor_b32 v2,65536,v2
v_mfma_f32_16x16x32_bf16 v[28:31],a[196:199],v[32:35],v[28:31]
s_barrier
v_mfma_f32_16x16x32_bf16 v[24:27],a[200:203],v[36:39],v[24:27]
s_cmp_eq_u32 s74,s56
ds_read_b128 v[32:35],v2 offset:0
ds_read_b128 v[84:87],v8 offset:8192
v_mfma_f32_16x16x32_bf16 v[28:31],a[204:207],v[36:39],v[28:31]
s_cselect_b32 s74,s57,s74
ds_read_b64 v[90:91],v9 offset:9216
v_xor_b32 v8,16384,v8
v_mfma_f32_16x16x32_bf16 v[24:27],a[208:211],v[40:43],v[24:27]
buffer_load_dwordx4 v0,s[36:39],s66 offen offset:1024 sc1 lds
v_mfma_f32_16x16x32_bf16 v[28:31],a[212:215],v[40:43],v[28:31]
s_cmp_eq_u32 s74,s58
ds_read_b128 v[36:39],v2 offset:1024
v_xor_b32 v9,16384,v9
v_mfma_f32_16x16x32_bf16 v[24:27],a[216:219],v[44:47],v[24:27]
s_cselect_b32 s74,s59,s74
ds_read_b128 v[40:43],v2 offset:2048
s_mov_b32 s52,s74
v_mfma_f32_16x16x32_bf16 v[28:31],a[220:223],v[44:47],v[28:31]
s_waitcnt lgkmcnt(5)
s_cmp_lt_u32 s67,s59
v_mfma_f32_16x16x32_bf16 v[24:27],a[224:227],v[48:51],v[24:27]
buffer_load_dwordx4 v0,s[36:39],s66 offen offset:2048 sc1 lds
v_mfma_f32_16x16x32_bf16 v[28:31],a[228:231],v[48:51],v[28:31]
s_waitcnt lgkmcnt(2)
ds_read_b128 v[44:47],v2 offset:3072
v_mfma_f32_16x16x32_bf16 v[24:27],a[232:235],v[52:55],v[24:27]
ds_read_b128 v[48:51],v2 offset:4096
v_mfma_f32_16x16x32_bf16 v[28:31],a[236:239],v[52:55],v[28:31]
v_mfma_f32_16x16x32_bf16 v[24:27],a[240:243],v[56:59],v[24:27]
buffer_load_dwordx4 v0,s[36:39],s66 offen offset:3072 sc1 lds
v_mfma_f32_16x16x32_bf16 v[28:31],a[244:247],v[56:59],v[28:31]
ds_read_b128 v[52:55],v2 offset:5120
v_mfma_f32_16x16x32_bf16 v[24:27],a[248:251],v[60:63],v[24:27]
s_waitcnt lgkmcnt(2)
ds_read_b128 v[56:59],v2 offset:6144
v_mfma_f32_16x16x32_bf16 v[28:31],a[252:255],v[60:63],v[28:31]
s_cbranch_scc1 Lg2_d0w2_%=
s_branch Lg2_slow0_%=
Lg2_d0w3_%=:
v_mfma_f32_16x16x32_bf16 v[16:19],a[0:3],v[32:35],0
s_add_u32 m0,s62,16384
ds_read_b128 v[60:63],v2 offset:7168
v_lshlrev_b32 v64,16,v84
v_mfma_f32_16x16x32_bf16 v[20:23],a[4:7],v[32:35],0
s_lshr_b32 s73,s47,0
v_and_b32 v65,0xffff0000,v84
v_lshlrev_b32 v66,16,v85
v_mfma_f32_16x16x32_bf16 v[16:19],a[8:11],v[36:39],v[16:19]
s_xor_b32 s87,s64,65536
ds_read_b128 v[32:35],v2 offset:8192
v_and_b32 v67,0xffff0000,v85
v_mfma_f32_16x16x32_bf16 v[20:23],a[12:15],v[36:39],v[20:23]
s_cmp_ge_u32 s66,s59
v_lshlrev_b32 v68,16,v86
v_and_b32 v69,0xffff0000,v86
v_mfma_f32_16x16x32_bf16 v[16:19],a[16:19],v[40:43],v[16:19]
buffer_load_dwordx4 v1,s[36:39],s66 offen offset:0 sc1 lds
v_mfma_f32_16x16x32_bf16 v[20:23],a[20:23],v[40:43],v[20:23]
s_cselect_b32 s86,s49,s50
ds_read_b128 v[36:39],v2 offset:9216
v_lshlrev_b32 v70,16,v87
v_mfma_f32_16x16x32_bf16 v[16:19],a[24:27],v[44:47],v[16:19]
s_cselect_b32 s87,s87,s63
ds_read_b128 v[40:43],v2 offset:10240
v_and_b32 v71,0xffff0000,v87
v_mfma_f32_16x16x32_bf16 v[20:23],a[28:31],v[44:47],v[20:23]
s_waitcnt lgkmcnt(3)
v_fma_f32 v64,v64,v91,v90
v_fma_f32 v65,v65,v91,v90
v_mfma_f32_16x16x32_bf16 v[16:19],a[32:35],v[48:51],v[16:19]
buffer_load_dwordx4 v1,s[36:39],s66 offen offset:1024 sc1 lds
v_mfma_f32_16x16x32_bf16 v[20:23],a[36:39],v[48:51],v[20:23]
s_lshr_b32 s72,s66,6
ds_read_b128 v[44:47],v2 offset:11264
v_fma_f32 v66,v66,v91,v90
v_mfma_f32_16x16x32_bf16 v[16:19],a[40:43],v[52:55],v[16:19]
s_lshr_b32 s71,s62,2
ds_read_b128 v[48:51],v2 offset:12288
v_fma_f32 v67,v67,v91,v90
v_mfma_f32_16x16x32_bf16 v[20:23],a[44:47],v[52:55],v[20:23]
s_mov_b32 s62,s87
v_fma_f32 v68,v68,v91,v90
v_fma_f32 v69,v69,v91,v90
v_mfma_f32_16x16x32_bf16 v[16:19],a[48:51],v[56:59],v[16:19]
buffer_load_dwordx4 v1,s[36:39],s66 offen offset:2048 sc1 lds
v_mfma_f32_16x16x32_bf16 v[20:23],a[52:55],v[56:59],v[20:23]
ds_read_b128 v[52:55],v2 offset:13312
v_fma_f32 v70,v70,v91,v90
v_mfma_f32_16x16x32_bf16 v[16:19],a[56:59],v[60:63],v[16:19]
ds_read_b128 v[56:59],v2 offset:14336
v_fma_f32 v71,v71,v91,v90
v_mfma_f32_16x16x32_bf16 v[20:23],a[60:63],v[60:63],v[20:23]
s_waitcnt lgkmcnt(3)
v_add_f32 v24,v24,v64
v_add_f32 v25,v25,v65
v_mfma_f32_16x16x32_bf16 v[16:19],a[64:67],v[32:35],v[16:19]
s_waitcnt vmcnt(9)
ds_read_b128 v[60:63],v2 offset:15360
v_add_f32 v26,v26,v66
v_mfma_f32_16x16x32_bf16 v[20:23],a[68:71],v[32:35],v[20:23]
s_barrier
v_mfma_f32_16x16x32_bf16 v[16:19],a[72:75],v[36:39],v[16:19]
buffer_load_dwordx4 v1,s[36:39],s66 offen offset:3072 sc1 lds
v_mfma_f32_16x16x32_bf16 v[20:23],a[76:79],v[36:39],v[20:23]
s_add_u32 m0,s71,s88
ds_read_b128 v[32:35],v2 offset:16384
v_add_f32 v27,v27,v67
v_mfma_f32_16x16x32_bf16 v[16:19],a[80:83],v[40:43],v[16:19]
s_lshr_b32 s70,s46,5
ds_read_b128 v[36:39],v2 offset:17408
v_add_f32 v28,v28,v68
v_mfma_f32_16x16x32_bf16 v[20:23],a[84:87],v[40:43],v[20:23]
v_add_f32 v29,v29,v69
v_add_f32 v30,v30,v70
v_mfma_f32_16x16x32_bf16 v[16:19],a[88:91],v[44:47],v[16:19]
buffer_load_dwordx4 v10,s[36:39],s66 offen offset:0 sc1 lds
v_mfma_f32_16x16x32_bf16 v[20:23],a[92:95],v[44:47],v[20:23]
s_waitcnt lgkmcnt(2)
ds_read_b128 v[40:43],v2 offset:18432
v_add_f32 v31,v31,v71
v_mfma_f32_16x16x32_bf16 v[16:19],a[96:99],v[48:51],v[16:19]
buffer_load_dword v11,s[36:39],s72 offen offset:1024 sc1 lds
v_mfma_f32_16x16x32_bf16 v[20:23],a[100:103],v[48:51],v[20:23]
s_mov_b32 m0,s62
ds_read_b128 v[44:47],v2 offset:19456
v_cvt_pk_bf16_f32 v72,v24,v25
v_mfma_f32_16x16x32_bf16 v[16:19],a[104:107],v[52:55],v[16:19]
v_cvt_pk_bf16_f32 v73,v26,v27
v_cvt_pk_bf16_f32 v74,v28,v29
v_cvt_pk_bf16_f32 v75,v30,v31
v_mfma_f32_16x16x32_bf16 v[20:23],a[108:111],v[52:55],v[20:23]
ds_read_b128 v[48:51],v2 offset:20480
buffer_store_dwordx4 v[72:75],v3,s[36:39],s73 offen offset:0 sc0 sc1
v_add_f32 v64,v24,v25
v_mul_f32 v65,v24,v24
v_fma_f32 v65,v25,v25,v65
v_mfma_f32_16x16x32_bf16 v[16:19],a[112:115],v[56:59],v[16:19]
ds_read_b128 v[52:55],v2 offset:21504
v_add_f32 v64,v64,v26
v_mfma_f32_16x16x32_bf16 v[20:23],a[116:119],v[56:59],v[20:23]
v_fma_f32 v65,v26,v26,v65
v_add_f32 v64,v64,v27
v_fma_f32 v65,v27,v27,v65
v_add_f32 v64,v64,v28
v_fma_f32 v65,v28,v28,v65
v_add_f32 v64,v64,v29
v_fma_f32 v65,v29,v29,v65
v_add_f32 v64,v64,v30
v_fma_f32 v65,v30,v30,v65
v_add_f32 v64,v64,v31
v_fma_f32 v65,v31,v31,v65
v_mfma_f32_16x16x32_bf16 v[16:19],a[120:123],v[60:63],v[16:19]
s_nop 0
v_permlane16_swap_b32 v64,v65
v_add_f32 v64,v64,v65
v_mov_b32 v65,v64
v_mfma_f32_16x16x32_bf16 v[20:23],a[124:127],v[60:63],v[20:23]
s_waitcnt lgkmcnt(2)
ds_read_b128 v[56:59],v2 offset:22528
v_permlane32_swap_b32 v64,v65
v_add_f32 v64,v64,v65
v_mfma_f32_16x16x32_bf16 v[16:19],a[128:131],v[32:35],v[16:19]
ds_write_b32 v13,v64 offset:0
v_mfma_f32_16x16x32_bf16 v[20:23],a[132:135],v[32:35],v[20:23]
ds_read_b128 v[60:63],v2 offset:23552
v_mfma_f32_16x16x32_bf16 v[16:19],a[136:139],v[36:39],v[16:19]
ds_read_b128 v[32:35],v2 offset:24576
v_mfma_f32_16x16x32_bf16 v[20:23],a[140:143],v[36:39],v[20:23]
v_mfma_f32_16x16x32_bf16 v[16:19],a[144:147],v[40:43],v[16:19]
s_waitcnt vmcnt(12) lgkmcnt(2)
ds_read_b128 v[36:39],v2 offset:25600
v_mfma_f32_16x16x32_bf16 v[20:23],a[148:151],v[40:43],v[20:23]
s_barrier
v_mfma_f32_16x16x32_bf16 v[16:19],a[152:155],v[44:47],v[16:19]
buffer_store_dword v5,v4,s[36:39],s70 offen offset:0 sc0 sc1
ds_read_b128 v[40:43],v2 offset:26624
ds_read_b32 v15,v7 offset:0
v_mfma_f32_16x16x32_bf16 v[20:23],a[156:159],v[44:47],v[20:23]
s_waitcnt lgkmcnt(4)
v_mfma_f32_16x16x32_bf16 v[16:19],a[160:163],v[48:51],v[16:19]
ds_read_b128 v[44:47],v2 offset:27648
v_mfma_f32_16x16x32_bf16 v[20:23],a[164:167],v[48:51],v[20:23]
v_mfma_f32_16x16x32_bf16 v[16:19],a[168:171],v[52:55],v[16:19]
s_waitcnt lgkmcnt(1)
ds_read_b128 v[48:51],v2 offset:28672)"
R"(
v_mfma_f32_16x16x32_bf16 v[20:23],a[172:175],v[52:55],v[20:23]
v_cmp_eq_u32 vcc,s60,v15
v_mfma_f32_16x16x32_bf16 v[16:19],a[176:179],v[56:59],v[16:19]
s_cmp_eq_u32 vcc_lo,-1
ds_read_b128 v[52:55],v2 offset:29696
s_cselect_b32 s67,s86,s59
v_mfma_f32_16x16x32_bf16 v[20:23],a[180:183],v[56:59],v[20:23]
s_xor_b32 s63,s63,65536
s_add_u32 s53,s52,s55
s_cmp_eq_u32 s53,s56
v_mfma_f32_16x16x32_bf16 v[16:19],a[184:187],v[60:63],v[16:19]
s_cselect_b32 s53,s57,s53
ds_read_b128 v[56:59],v2 offset:30720
s_cmp_eq_u32 s53,s58
v_mfma_f32_16x16x32_bf16 v[20:23],a[188:191],v[60:63],v[20:23]
s_waitcnt lgkmcnt(3)
buffer_load_dwordx4 v0,s[36:39],s67 offen offset:0 sc1 lds
v_mfma_f32_16x16x32_bf16 v[16:19],a[192:195],v[32:35],v[16:19]
s_waitcnt vmcnt(9)
ds_read_b128 v[60:63],v2 offset:31744
s_cselect_b32 s53,s59,s53
v_mfma_f32_16x16x32_bf16 v[20:23],a[196:199],v[32:35],v[20:23]
s_barrier
v_mfma_f32_16x16x32_bf16 v[16:19],a[200:203],v[36:39],v[16:19]
s_cmp_ge_u32 s66,s59
ds_read_b128 v[32:35],v2 offset:32768
ds_read_b128 v[80:83],v8 offset:0
v_mfma_f32_16x16x32_bf16 v[20:23],a[204:207],v[36:39],v[20:23]
ds_read_b64 v[88:89],v9 offset:1024
v_mfma_f32_16x16x32_bf16 v[16:19],a[208:211],v[40:43],v[16:19]
ds_read_b128 v[36:39],v2 offset:33792
v_mfma_f32_16x16x32_bf16 v[20:23],a[212:215],v[40:43],v[20:23]
buffer_load_dwordx4 v0,s[36:39],s67 offen offset:1024 sc1 lds
v_mfma_f32_16x16x32_bf16 v[16:19],a[216:219],v[44:47],v[16:19]
ds_read_b128 v[40:43],v2 offset:34816
v_mfma_f32_16x16x32_bf16 v[20:23],a[220:223],v[44:47],v[20:23]
s_waitcnt lgkmcnt(5)
v_mfma_f32_16x16x32_bf16 v[16:19],a[224:227],v[48:51],v[16:19]
ds_read_b128 v[44:47],v2 offset:35840
v_mfma_f32_16x16x32_bf16 v[20:23],a[228:231],v[48:51],v[20:23]
s_waitcnt lgkmcnt(3)
buffer_load_dwordx4 v0,s[36:39],s67 offen offset:2048 sc1 lds
v_mfma_f32_16x16x32_bf16 v[16:19],a[232:235],v[52:55],v[16:19]
ds_read_b128 v[48:51],v2 offset:36864
v_mfma_f32_16x16x32_bf16 v[20:23],a[236:239],v[52:55],v[20:23]
v_mfma_f32_16x16x32_bf16 v[16:19],a[240:243],v[56:59],v[16:19]
ds_read_b128 v[52:55],v2 offset:37888
v_mfma_f32_16x16x32_bf16 v[20:23],a[244:247],v[56:59],v[20:23]
buffer_load_dwordx4 v0,s[36:39],s67 offen offset:3072 sc1 lds
v_mfma_f32_16x16x32_bf16 v[16:19],a[248:251],v[60:63],v[16:19]
s_waitcnt lgkmcnt(2)
ds_read_b128 v[56:59],v2 offset:38912
v_mfma_f32_16x16x32_bf16 v[20:23],a[252:255],v[60:63],v[20:23]
s_cbranch_scc1 Lg2_slow1_%=
Lg2_d1w3_%=:
v_mfma_f32_16x16x32_bf16 v[24:27],a[0:3],v[32:35],0
s_add_u32 m0,s62,16384
ds_read_b128 v[60:63],v2 offset:39936
v_lshlrev_b32 v64,16,v80
v_mfma_f32_16x16x32_bf16 v[28:31],a[4:7],v[32:35],0
s_lshr_b32 s73,s48,0
v_and_b32 v65,0xffff0000,v80
v_lshlrev_b32 v66,16,v81
v_mfma_f32_16x16x32_bf16 v[24:27],a[8:11],v[36:39],v[24:27]
s_xor_b32 s87,s63,65536
ds_read_b128 v[32:35],v2 offset:40960
v_and_b32 v67,0xffff0000,v81
v_mfma_f32_16x16x32_bf16 v[28:31],a[12:15],v[36:39],v[28:31]
s_cmp_ge_u32 s67,s59
v_lshlrev_b32 v68,16,v82
v_and_b32 v69,0xffff0000,v82
v_mfma_f32_16x16x32_bf16 v[24:27],a[16:19],v[40:43],v[24:27]
buffer_load_dwordx4 v1,s[36:39],s67 offen offset:0 sc1 lds
v_mfma_f32_16x16x32_bf16 v[28:31],a[20:23],v[40:43],v[28:31]
s_cselect_b32 s86,s50,s51
ds_read_b128 v[36:39],v2 offset:41984
v_lshlrev_b32 v70,16,v83
v_mfma_f32_16x16x32_bf16 v[24:27],a[24:27],v[44:47],v[24:27]
s_cselect_b32 s87,s87,s64
ds_read_b128 v[40:43],v2 offset:43008
v_and_b32 v71,0xffff0000,v83
v_mfma_f32_16x16x32_bf16 v[28:31],a[28:31],v[44:47],v[28:31]
s_waitcnt lgkmcnt(3)
v_fma_f32 v64,v64,v89,v88
v_fma_f32 v65,v65,v89,v88
v_mfma_f32_16x16x32_bf16 v[24:27],a[32:35],v[48:51],v[24:27]
buffer_load_dwordx4 v1,s[36:39],s67 offen offset:1024 sc1 lds
v_mfma_f32_16x16x32_bf16 v[28:31],a[36:39],v[48:51],v[28:31]
s_lshr_b32 s72,s67,6
ds_read_b128 v[44:47],v2 offset:44032
v_fma_f32 v66,v66,v89,v88
v_mfma_f32_16x16x32_bf16 v[24:27],a[40:43],v[52:55],v[24:27]
s_lshr_b32 s71,s62,2
ds_read_b128 v[48:51],v2 offset:45056
v_fma_f32 v67,v67,v89,v88
v_mfma_f32_16x16x32_bf16 v[28:31],a[44:47],v[52:55],v[28:31]
s_mov_b32 s62,s87
v_fma_f32 v68,v68,v89,v88
v_fma_f32 v69,v69,v89,v88
v_mfma_f32_16x16x32_bf16 v[24:27],a[48:51],v[56:59],v[24:27]
buffer_load_dwordx4 v1,s[36:39],s67 offen offset:2048 sc1 lds
v_mfma_f32_16x16x32_bf16 v[28:31],a[52:55],v[56:59],v[28:31]
s_mov_b64 s[44:45],s[46:47]
ds_read_b128 v[52:55],v2 offset:46080
v_fma_f32 v70,v70,v89,v88
v_mfma_f32_16x16x32_bf16 v[24:27],a[56:59],v[60:63],v[24:27]
ds_read_b128 v[56:59],v2 offset:47104
v_fma_f32 v71,v71,v89,v88
v_mfma_f32_16x16x32_bf16 v[28:31],a[60:63],v[60:63],v[28:31]
s_waitcnt lgkmcnt(3)
v_add_f32 v16,v16,v64
v_add_f32 v17,v17,v65
v_mfma_f32_16x16x32_bf16 v[24:27],a[64:67],v[32:35],v[24:27]
s_waitcnt vmcnt(9)
ds_read_b128 v[60:63],v2 offset:48128
v_add_f32 v18,v18,v66
v_mfma_f32_16x16x32_bf16 v[28:31],a[68:71],v[32:35],v[28:31]
s_barrier
v_mfma_f32_16x16x32_bf16 v[24:27],a[72:75],v[36:39],v[24:27]
buffer_load_dwordx4 v1,s[36:39],s67 offen offset:3072 sc1 lds
v_mfma_f32_16x16x32_bf16 v[28:31],a[76:79],v[36:39],v[28:31]
s_add_u32 m0,s71,s88
ds_read_b128 v[32:35],v2 offset:49152
v_add_f32 v19,v19,v67
v_mfma_f32_16x16x32_bf16 v[24:27],a[80:83],v[40:43],v[24:27]
s_lshr_b32 s70,s47,5
ds_read_b128 v[36:39],v2 offset:50176
v_add_f32 v20,v20,v68
v_mfma_f32_16x16x32_bf16 v[28:31],a[84:87],v[40:43],v[28:31]
s_mov_b64 s[46:47],s[48:49]
v_add_f32 v21,v21,v69
v_add_f32 v22,v22,v70
v_mfma_f32_16x16x32_bf16 v[24:27],a[88:91],v[44:47],v[24:27]
buffer_load_dwordx4 v10,s[36:39],s67 offen offset:0 sc1 lds
v_mfma_f32_16x16x32_bf16 v[28:31],a[92:95],v[44:47],v[28:31]
s_waitcnt lgkmcnt(2)
ds_read_b128 v[40:43],v2 offset:51200
v_add_f32 v23,v23,v71
v_mfma_f32_16x16x32_bf16 v[24:27],a[96:99],v[48:51],v[24:27]
buffer_load_dword v11,s[36:39],s72 offen offset:1024 sc1 lds
v_mfma_f32_16x16x32_bf16 v[28:31],a[100:103],v[48:51],v[28:31]
s_mov_b32 m0,s62
ds_read_b128 v[44:47],v2 offset:52224
v_cvt_pk_bf16_f32 v72,v16,v17
v_mfma_f32_16x16x32_bf16 v[24:27],a[104:107],v[52:55],v[24:27]
s_mov_b64 s[48:49],s[50:51]
v_cvt_pk_bf16_f32 v73,v18,v19
v_cvt_pk_bf16_f32 v74,v20,v21
v_cvt_pk_bf16_f32 v75,v22,v23
v_mfma_f32_16x16x32_bf16 v[28:31],a[108:111],v[52:55],v[28:31]
s_mov_b64 s[50:51],s[52:53]
ds_read_b128 v[48:51],v2 offset:53248
buffer_store_dwordx4 v[72:75],v3,s[36:39],s73 offen offset:0 sc0 sc1
v_add_f32 v64,v16,v17
v_mul_f32 v65,v16,v16
v_fma_f32 v65,v17,v17,v65
v_mfma_f32_16x16x32_bf16 v[24:27],a[112:115],v[56:59],v[24:27]
ds_read_b128 v[52:55],v2 offset:54272
v_add_f32 v64,v64,v18
v_mfma_f32_16x16x32_bf16 v[28:31],a[116:119],v[56:59],v[28:31]
v_fma_f32 v65,v18,v18,v65
v_add_f32 v64,v64,v19
v_fma_f32 v65,v19,v19,v65
v_add_f32 v64,v64,v20
v_fma_f32 v65,v20,v20,v65
v_add_f32 v64,v64,v21
v_fma_f32 v65,v21,v21,v65
v_add_f32 v64,v64,v22
v_fma_f32 v65,v22,v22,v65
v_add_f32 v64,v64,v23
v_fma_f32 v65,v23,v23,v65
v_mfma_f32_16x16x32_bf16 v[24:27],a[120:123],v[60:63],v[24:27]
s_nop 0
v_permlane16_swap_b32 v64,v65
v_add_f32 v64,v64,v65
v_mov_b32 v65,v64
v_mfma_f32_16x16x32_bf16 v[28:31],a[124:127],v[60:63],v[28:31]
s_waitcnt lgkmcnt(2)
ds_read_b128 v[56:59],v2 offset:55296
v_permlane32_swap_b32 v64,v65
v_add_f32 v64,v64,v65
v_mfma_f32_16x16x32_bf16 v[24:27],a[128:131],v[32:35],v[24:27]
ds_write_b32 v13,v64 offset:2048
v_mfma_f32_16x16x32_bf16 v[28:31],a[132:135],v[32:35],v[28:31]
ds_read_b128 v[60:63],v2 offset:56320
v_mfma_f32_16x16x32_bf16 v[24:27],a[136:139],v[36:39],v[24:27]
ds_read_b128 v[32:35],v2 offset:57344
v_mfma_f32_16x16x32_bf16 v[28:31],a[140:143],v[36:39],v[28:31]
v_mfma_f32_16x16x32_bf16 v[24:27],a[144:147],v[40:43],v[24:27]
s_waitcnt vmcnt(12) lgkmcnt(2)
ds_read_b128 v[36:39],v2 offset:58368
v_mfma_f32_16x16x32_bf16 v[28:31],a[148:151],v[40:43],v[28:31]
s_barrier
v_mfma_f32_16x16x32_bf16 v[24:27],a[152:155],v[44:47],v[24:27]
buffer_store_dword v5,v4,s[36:39],s70 offen offset:0 sc0 sc1
ds_read_b128 v[40:43],v2 offset:59392
ds_read_b32 v15,v7 offset:256
v_mfma_f32_16x16x32_bf16 v[28:31],a[156:159],v[44:47],v[28:31]
s_waitcnt lgkmcnt(4)
v_mfma_f32_16x16x32_bf16 v[24:27],a[160:163],v[48:51],v[24:27])"
R"(
ds_read_b128 v[44:47],v2 offset:60416
v_mfma_f32_16x16x32_bf16 v[28:31],a[164:167],v[48:51],v[28:31]
v_mfma_f32_16x16x32_bf16 v[24:27],a[168:171],v[52:55],v[24:27]
s_waitcnt lgkmcnt(1)
ds_read_b128 v[48:51],v2 offset:61440
v_mfma_f32_16x16x32_bf16 v[28:31],a[172:175],v[52:55],v[28:31]
v_cmp_eq_u32 vcc,s60,v15
v_mfma_f32_16x16x32_bf16 v[24:27],a[176:179],v[56:59],v[24:27]
s_cmp_eq_u32 vcc_lo,-1
ds_read_b128 v[52:55],v2 offset:62464
s_cselect_b32 s66,s86,s59
v_mfma_f32_16x16x32_bf16 v[28:31],a[180:183],v[56:59],v[28:31]
s_xor_b32 s64,s64,65536
s_add_u32 s74,s53,s55
s_cmp_eq_u32 s74,s56
v_mfma_f32_16x16x32_bf16 v[24:27],a[184:187],v[60:63],v[24:27]
s_cselect_b32 s74,s57,s74
ds_read_b128 v[56:59],v2 offset:63488
s_cmp_eq_u32 s74,s58
v_mfma_f32_16x16x32_bf16 v[28:31],a[188:191],v[60:63],v[28:31]
s_waitcnt lgkmcnt(3)
buffer_load_dwordx4 v0,s[36:39],s66 offen offset:0 sc1 lds
v_mfma_f32_16x16x32_bf16 v[24:27],a[192:195],v[32:35],v[24:27]
s_waitcnt vmcnt(9)
ds_read_b128 v[60:63],v2 offset:64512
v_xor_b32 v2,65536,v2
v_mfma_f32_16x16x32_bf16 v[28:31],a[196:199],v[32:35],v[28:31]
s_barrier
v_mfma_f32_16x16x32_bf16 v[24:27],a[200:203],v[36:39],v[24:27]
s_cselect_b32 s74,s59,s74
ds_read_b128 v[32:35],v2 offset:0
ds_read_b128 v[84:87],v8 offset:8192
v_mfma_f32_16x16x32_bf16 v[28:31],a[204:207],v[36:39],v[28:31]
s_mov_b32 s52,s74
ds_read_b64 v[90:91],v9 offset:9216
v_xor_b32 v8,16384,v8
v_mfma_f32_16x16x32_bf16 v[24:27],a[208:211],v[40:43],v[24:27]
s_cmp_lt_u32 s67,s59
ds_read_b128 v[36:39],v2 offset:1024
v_xor_b32 v9,16384,v9
v_mfma_f32_16x16x32_bf16 v[28:31],a[212:215],v[40:43],v[28:31]
buffer_load_dwordx4 v0,s[36:39],s66 offen offset:1024 sc1 lds
v_mfma_f32_16x16x32_bf16 v[24:27],a[216:219],v[44:47],v[24:27]
ds_read_b128 v[40:43],v2 offset:2048
v_mfma_f32_16x16x32_bf16 v[28:31],a[220:223],v[44:47],v[28:31]
s_waitcnt lgkmcnt(5)
v_mfma_f32_16x16x32_bf16 v[24:27],a[224:227],v[48:51],v[24:27]
ds_read_b128 v[44:47],v2 offset:3072
v_mfma_f32_16x16x32_bf16 v[28:31],a[228:231],v[48:51],v[28:31]
s_waitcnt lgkmcnt(3)
buffer_load_dwordx4 v0,s[36:39],s66 offen offset:2048 sc1 lds
v_mfma_f32_16x16x32_bf16 v[24:27],a[232:235],v[52:55],v[24:27]
ds_read_b128 v[48:51],v2 offset:4096
v_mfma_f32_16x16x32_bf16 v[28:31],a[236:239],v[52:55],v[28:31]
v_mfma_f32_16x16x32_bf16 v[24:27],a[240:243],v[56:59],v[24:27]
ds_read_b128 v[52:55],v2 offset:5120
v_mfma_f32_16x16x32_bf16 v[28:31],a[244:247],v[56:59],v[28:31]
buffer_load_dwordx4 v0,s[36:39],s66 offen offset:3072 sc1 lds
v_mfma_f32_16x16x32_bf16 v[24:27],a[248:251],v[60:63],v[24:27]
s_waitcnt lgkmcnt(2)
ds_read_b128 v[56:59],v2 offset:6144
v_mfma_f32_16x16x32_bf16 v[28:31],a[252:255],v[60:63],v[28:31]
s_cbranch_scc1 Lg2_d0w3_%=
s_branch Lg2_slow0_%=
Lg2_slow0_%=:
s_cmp_lt_u32 s66,s59
s_cbranch_scc1 Lg2_gs0_%=
Lg2_slowb0_%=:
s_cmp_eq_u32 s54,1
s_cselect_b32 s68,s49,s48
s_lshr_b32 s68,s68,5
s_add_u32 m0,s78,8192
s_nop 0
buffer_load_dword v6,s[36:39],s68 offen offset:0 sc1 lds
s_lshr_b32 s73,s47,0
s_lshr_b32 s85,s47,5
v_lshlrev_b32 v64,16,v84
v_and_b32 v65,0xffff0000,v84
v_lshlrev_b32 v66,16,v85
v_and_b32 v67,0xffff0000,v85
v_lshlrev_b32 v68,16,v86
v_and_b32 v69,0xffff0000,v86
v_lshlrev_b32 v70,16,v87
v_and_b32 v71,0xffff0000,v87
v_fma_f32 v64,v64,v91,v90
v_fma_f32 v65,v65,v91,v90
v_fma_f32 v66,v66,v91,v90
v_fma_f32 v67,v67,v91,v90
v_fma_f32 v68,v68,v91,v90
v_fma_f32 v69,v69,v91,v90
v_fma_f32 v70,v70,v91,v90
v_fma_f32 v71,v71,v91,v90
v_add_f32 v24,v24,v64
v_add_f32 v25,v25,v65
v_add_f32 v26,v26,v66
v_add_f32 v27,v27,v67
v_add_f32 v28,v28,v68
v_add_f32 v29,v29,v69
v_add_f32 v30,v30,v70
v_add_f32 v31,v31,v71
v_cvt_pk_bf16_f32 v72,v24,v25
v_cvt_pk_bf16_f32 v73,v26,v27
v_cvt_pk_bf16_f32 v74,v28,v29
v_cvt_pk_bf16_f32 v75,v30,v31
s_add_u32 m0,s78,8448
s_nop 0
buffer_load_dword v6,s[36:39],s68 offen offset:0 sc1 lds
buffer_store_dwordx4 v[72:75],v3,s[36:39],s73 offen offset:0 sc0 sc1
v_add_f32 v64,v24,v25
v_mul_f32 v65,v24,v24
v_fma_f32 v65,v25,v25,v65
v_add_f32 v64,v64,v26
v_fma_f32 v65,v26,v26,v65
v_add_f32 v64,v64,v27
v_fma_f32 v65,v27,v27,v65
v_add_f32 v64,v64,v28
v_fma_f32 v65,v28,v28,v65
v_add_f32 v64,v64,v29
v_fma_f32 v65,v29,v29,v65
v_add_f32 v64,v64,v30
v_fma_f32 v65,v30,v30,v65
v_add_f32 v64,v64,v31
v_fma_f32 v65,v31,v31,v65
s_nop 1
v_permlane16_swap_b32 v64,v65
v_add_f32 v64,v64,v65
v_mov_b32 v65,v64
s_nop 1
v_permlane32_swap_b32 v64,v65
v_add_f32 v64,v64,v65
ds_write_b32 v13,v64 offset:0
s_waitcnt lgkmcnt(0)
s_barrier
ds_read_b128 v[76:79],v14 offset:0
s_waitcnt lgkmcnt(0)
v_add_f32 v76,v76,v77
v_add_f32 v78,v78,v79
v_add_f32 v76,v76,v78
buffer_store_dword v76,v12,s[36:39],s85 offen offset:0 sc0 sc1
s_cmp_ge_u32 s48,s59
s_cbranch_scc1 Lg2_done0_%=
s_add_u32 m0,s78,16384
s_nop 0
buffer_load_dword v6,s[36:39],s68 offen offset:0 sc1 lds
s_waitcnt vmcnt(4)
s_barrier
ds_read_b32 v15,v7 offset:8192
s_lshr_b32 s70,s46,5
buffer_store_dword v5,v4,s[36:39],s70 offen offset:0 sc0 sc1
s_mov_b32 s46,s59
s_mov_b32 s69,18432
s_waitcnt lgkmcnt(0)
v_cmp_eq_u32 vcc,s60,v15
s_nop 0
s_cmp_eq_u32 vcc_lo,-1
s_cbranch_scc1 Lg2_go0_%=
s_add_u32 m0,s78,16640
s_nop 0
buffer_load_dword v6,s[36:39],s68 offen offset:0 sc1 lds
s_waitcnt vmcnt(5)
s_barrier
ds_read_b32 v15,v7 offset:8448
s_mov_b32 s69,18688
s_waitcnt lgkmcnt(0)
v_cmp_eq_u32 vcc,s60,v15
s_nop 0
s_cmp_eq_u32 vcc_lo,-1
s_cbranch_scc1 Lg2_go0_%=
s_add_u32 m0,s78,8192
s_nop 0
buffer_load_dword v6,s[36:39],s68 offen offset:0 sc1 lds
s_waitcnt vmcnt(3)
s_barrier
ds_read_b32 v15,v7 offset:16384
s_lshr_b32 s70,s47,5
buffer_store_dword v5,v4,s[36:39],s70 offen offset:0 sc0 sc1
s_mov_b32 s47,s59
s_mov_b32 s69,10240
s_waitcnt lgkmcnt(0)
v_cmp_eq_u32 vcc,s60,v15
s_nop 0
s_cmp_eq_u32 vcc_lo,-1
s_cbranch_scc1 Lg2_go0_%=
s_add_u32 m0,s78,8448
s_nop 0
buffer_load_dword v6,s[36:39],s68 offen offset:0 sc1 lds
s_waitcnt vmcnt(3)
s_barrier
ds_read_b32 v15,v7 offset:16640
s_mov_b32 s69,10496
s_waitcnt lgkmcnt(0)
v_cmp_eq_u32 vcc,s60,v15
s_nop 0
s_cmp_eq_u32 vcc_lo,-1
s_cbranch_scc1 Lg2_go0_%=
Lg2_poll0_%=:
s_add_u32 m0,s78,16384
s_nop 0
buffer_load_dword v6,s[36:39],s68 offen offset:0 sc1 lds
s_waitcnt vmcnt(2))"
R"(
s_barrier
ds_read_b32 v15,v7 offset:8192
s_mov_b32 s69,18432
s_waitcnt lgkmcnt(0)
v_cmp_eq_u32 vcc,s60,v15
s_nop 0
s_cmp_eq_u32 vcc_lo,-1
s_cbranch_scc1 Lg2_go0_%=
s_sleep 4
s_add_u32 m0,s78,16640
s_nop 0
buffer_load_dword v6,s[36:39],s68 offen offset:0 sc1 lds
s_waitcnt vmcnt(2)
s_barrier
ds_read_b32 v15,v7 offset:8448
s_mov_b32 s69,18688
s_waitcnt lgkmcnt(0)
v_cmp_eq_u32 vcc,s60,v15
s_nop 0
s_cmp_eq_u32 vcc_lo,-1
s_cbranch_scc1 Lg2_go0_%=
s_sleep 4
s_add_u32 m0,s78,8192
s_nop 0
buffer_load_dword v6,s[36:39],s68 offen offset:0 sc1 lds
s_waitcnt vmcnt(2)
s_barrier
ds_read_b32 v15,v7 offset:16384
s_mov_b32 s69,10240
s_waitcnt lgkmcnt(0)
v_cmp_eq_u32 vcc,s60,v15
s_nop 0
s_cmp_eq_u32 vcc_lo,-1
s_cbranch_scc1 Lg2_go0_%=
s_sleep 4
s_add_u32 m0,s78,8448
s_nop 0
buffer_load_dword v6,s[36:39],s68 offen offset:0 sc1 lds
s_waitcnt vmcnt(2)
s_barrier
ds_read_b32 v15,v7 offset:16640
s_mov_b32 s69,10496
s_waitcnt lgkmcnt(0)
v_cmp_eq_u32 vcc,s60,v15
s_nop 0
s_cmp_eq_u32 vcc_lo,-1
s_cbranch_scc1 Lg2_go0_%=
s_sleep 4
s_add_u32 s79,s79,1
s_cmp_lt_u32 s79,20000
s_cbranch_scc1 Lg2_poll0_%=
Lg2_go0_%=:
s_xor_b32 s62,s63,65536
s_mov_b32 m0,s62
s_nop 0
buffer_load_dwordx4 v0,s[36:39],s48 offen offset:0 sc1 lds
buffer_load_dwordx4 v0,s[36:39],s48 offen offset:1024 sc1 lds
buffer_load_dwordx4 v0,s[36:39],s48 offen offset:2048 sc1 lds
buffer_load_dwordx4 v0,s[36:39],s48 offen offset:3072 sc1 lds
s_add_u32 m0,s62,16384
s_nop 0
buffer_load_dwordx4 v1,s[36:39],s48 offen offset:0 sc1 lds
buffer_load_dwordx4 v1,s[36:39],s48 offen offset:1024 sc1 lds
buffer_load_dwordx4 v1,s[36:39],s48 offen offset:2048 sc1 lds
buffer_load_dwordx4 v1,s[36:39],s48 offen offset:3072 sc1 lds
s_lshr_b32 s72,s48,6
s_lshr_b32 s71,s62,2
s_add_u32 m0,s71,s88
s_nop 0
buffer_load_dwordx4 v10,s[36:39],s48 offen offset:0 sc1 lds
buffer_load_dword v11,s[36:39],s72 offen offset:1024 sc1 lds
s_cmp_eq_u32 vcc_hi,-1
s_cselect_b32 s66,s49,s59
s_xor_b32 s62,s64,65536
s_cmp_ge_u32 s66,s59
s_cselect_b32 s68,s49,s50
s_lshr_b32 s68,s68,5
s_add_u32 m0,s78,0
s_nop 0
buffer_load_dword v6,s[36:39],s68 offen offset:0 sc1 lds
s_mov_b32 m0,s62
s_nop 0
buffer_load_dwordx4 v0,s[36:39],s66 offen offset:0 sc1 lds
buffer_load_dwordx4 v0,s[36:39],s66 offen offset:1024 sc1 lds
buffer_load_dwordx4 v0,s[36:39],s66 offen offset:2048 sc1 lds
buffer_load_dwordx4 v0,s[36:39],s66 offen offset:3072 sc1 lds
s_waitcnt vmcnt(11)
s_barrier
ds_read_b128 v[32:35],v2 offset:0
ds_read_b128 v[36:39],v2 offset:1024
ds_read_b128 v[40:43],v2 offset:2048
ds_read_b128 v[44:47],v2 offset:3072
ds_read_b128 v[48:51],v2 offset:4096
ds_read_b128 v[52:55],v2 offset:5120
ds_read_b128 v[56:59],v2 offset:6144
s_lshr_b32 s70,s46,5
buffer_store_dword v5,v4,s[36:39],s70 offen offset:0 sc0 sc1
s_mov_b32 s46,s59
s_lshr_b32 s70,s47,5
buffer_store_dword v5,v4,s[36:39],s70 offen offset:0 sc0 sc1
s_mov_b32 s47,s59
s_lshr_b32 s70,s59,5
buffer_store_dword v5,v4,s[36:39],s70 offen offset:0 sc0 sc1
s_waitcnt lgkmcnt(3)
s_setpc_b64 s[90:91]
Lg2_gs0_%=:
s_cmp_eq_u32 s54,1
s_cselect_b32 s68,s49,s48
s_lshr_b32 s68,s68,5
s_lshr_b32 s73,s47,0
s_lshr_b32 s85,s47,5
s_add_u32 m0,s78,8192
s_nop 0
buffer_load_dword v6,s[36:39],s68 offen offset:0 sc1 lds
v_lshlrev_b32 v64,16,v84
v_and_b32 v65,0xffff0000,v84
v_lshlrev_b32 v66,16,v85
v_and_b32 v67,0xffff0000,v85
v_lshlrev_b32 v68,16,v86
v_and_b32 v69,0xffff0000,v86
v_lshlrev_b32 v70,16,v87
v_and_b32 v71,0xffff0000,v87
s_add_u32 m0,s62,16384
s_nop 0
buffer_load_dwordx4 v1,s[36:39],s66 offen offset:0 sc1 lds
v_fma_f32 v64,v64,v91,v90
v_fma_f32 v65,v65,v91,v90
v_fma_f32 v66,v66,v91,v90
v_fma_f32 v67,v67,v91,v90
v_fma_f32 v68,v68,v91,v90
v_fma_f32 v69,v69,v91,v90
v_fma_f32 v70,v70,v91,v90
v_fma_f32 v71,v71,v91,v90
buffer_load_dwordx4 v1,s[36:39],s66 offen offset:1024 sc1 lds
v_add_f32 v24,v24,v64
v_add_f32 v25,v25,v65
v_add_f32 v26,v26,v66
v_add_f32 v27,v27,v67
v_add_f32 v28,v28,v68
v_add_f32 v29,v29,v69
v_add_f32 v30,v30,v70
v_add_f32 v31,v31,v71
buffer_load_dwordx4 v1,s[36:39],s66 offen offset:2048 sc1 lds
v_cvt_pk_bf16_f32 v72,v24,v25
v_cvt_pk_bf16_f32 v73,v26,v27
v_cvt_pk_bf16_f32 v74,v28,v29
v_cvt_pk_bf16_f32 v75,v30,v31
buffer_store_dwordx4 v[72:75],v3,s[36:39],s73 offen offset:0 sc0 sc1
v_add_f32 v64,v24,v25
v_mul_f32 v65,v24,v24
v_fma_f32 v65,v25,v25,v65
buffer_load_dwordx4 v1,s[36:39],s66 offen offset:3072 sc1 lds
v_add_f32 v64,v64,v26
v_fma_f32 v65,v26,v26,v65
v_add_f32 v64,v64,v27
v_fma_f32 v65,v27,v27,v65
v_add_f32 v64,v64,v28
v_fma_f32 v65,v28,v28,v65
v_add_f32 v64,v64,v29
v_fma_f32 v65,v29,v29,v65
s_lshr_b32 s72,s66,6
s_lshr_b32 s71,s62,2
s_add_u32 m0,s71,s88
s_nop 0
buffer_load_dwordx4 v10,s[36:39],s66 offen offset:0 sc1 lds
v_add_f32 v64,v64,v30
v_fma_f32 v65,v30,v30,v65
v_add_f32 v64,v64,v31
v_fma_f32 v65,v31,v31,v65
s_nop 1
v_permlane16_swap_b32 v64,v65
v_add_f32 v64,v64,v65
v_mov_b32 v65,v64
s_nop 1
v_permlane32_swap_b32 v64,v65
buffer_load_dword v11,s[36:39],s72 offen offset:1024 sc1 lds
v_add_f32 v64,v64,v65
ds_write_b32 v13,v64 offset:0
s_waitcnt lgkmcnt(0)
s_barrier
ds_read_b128 v[76:79],v14 offset:0
s_waitcnt lgkmcnt(0)
v_add_f32 v76,v76,v77
v_add_f32 v78,v78,v79
v_add_f32 v76,v76,v78
buffer_store_dword v76,v12,s[36:39],s85 offen offset:0 sc0 sc1
s_waitcnt vmcnt(8)
s_barrier
ds_read_b32 v15,v7 offset:8192
ds_read_b128 v[32:35],v2 offset:0
ds_read_b128 v[36:39],v2 offset:1024
ds_read_b128 v[40:43],v2 offset:2048
ds_read_b128 v[44:47],v2 offset:3072
ds_read_b128 v[48:51],v2 offset:4096
ds_read_b128 v[52:55],v2 offset:5120
ds_read_b128 v[56:59],v2 offset:6144
s_lshr_b32 s70,s46,5
buffer_store_dword v5,v4,s[36:39],s70 offen offset:0 sc0 sc1
s_waitcnt lgkmcnt(7)
v_cmp_eq_u32 vcc,s60,v15)"
R"(
s_nop 0
s_cmp_eq_u32 vcc_hi,-1
s_cselect_b32 s66,s49,s59
s_xor_b32 s62,s64,65536
s_cmp_ge_u32 s66,s59
s_cselect_b32 s68,s49,s50
s_lshr_b32 s68,s68,5
s_add_u32 m0,s78,0
s_nop 0
buffer_load_dword v6,s[36:39],s68 offen offset:0 sc1 lds
s_mov_b32 m0,s62
s_nop 0
buffer_load_dwordx4 v0,s[36:39],s66 offen offset:0 sc1 lds
buffer_load_dwordx4 v0,s[36:39],s66 offen offset:1024 sc1 lds
buffer_load_dwordx4 v0,s[36:39],s66 offen offset:2048 sc1 lds
buffer_load_dwordx4 v0,s[36:39],s66 offen offset:3072 sc1 lds
s_mov_b32 s46,s47
s_mov_b32 s47,s59
s_lshr_b32 s70,s59,5
buffer_store_dword v5,v4,s[36:39],s70 offen offset:0 sc0 sc1
s_lshr_b32 s70,s59,5
buffer_store_dword v5,v4,s[36:39],s70 offen offset:0 sc0 sc1
s_waitcnt lgkmcnt(3)
s_setpc_b64 s[90:91]
Lg2_done0_%=:
s_waitcnt vmcnt(0)
s_barrier
s_lshr_b32 s70,s46,5
buffer_store_dword v5,v4,s[36:39],s70 offen offset:0 sc0 sc1
s_lshr_b32 s70,s47,5
buffer_store_dword v5,v4,s[36:39],s70 offen offset:0 sc0 sc1
s_branch Lg2_done_%=
Lg2_slow1_%=:
s_cmp_lt_u32 s67,s59
s_cbranch_scc1 Lg2_gs1_%=
Lg2_slowb1_%=:
s_cmp_eq_u32 s54,1
s_cselect_b32 s68,s50,s49
s_lshr_b32 s68,s68,5
s_add_u32 m0,s78,8192
s_nop 0
buffer_load_dword v6,s[36:39],s68 offen offset:0 sc1 lds
s_lshr_b32 s73,s48,0
s_lshr_b32 s85,s48,5
v_lshlrev_b32 v64,16,v80
v_and_b32 v65,0xffff0000,v80
v_lshlrev_b32 v66,16,v81
v_and_b32 v67,0xffff0000,v81
v_lshlrev_b32 v68,16,v82
v_and_b32 v69,0xffff0000,v82
v_lshlrev_b32 v70,16,v83
v_and_b32 v71,0xffff0000,v83
v_fma_f32 v64,v64,v89,v88
v_fma_f32 v65,v65,v89,v88
v_fma_f32 v66,v66,v89,v88
v_fma_f32 v67,v67,v89,v88
v_fma_f32 v68,v68,v89,v88
v_fma_f32 v69,v69,v89,v88
v_fma_f32 v70,v70,v89,v88
v_fma_f32 v71,v71,v89,v88
v_add_f32 v16,v16,v64
v_add_f32 v17,v17,v65
v_add_f32 v18,v18,v66
v_add_f32 v19,v19,v67
v_add_f32 v20,v20,v68
v_add_f32 v21,v21,v69
v_add_f32 v22,v22,v70
v_add_f32 v23,v23,v71
v_cvt_pk_bf16_f32 v72,v16,v17
v_cvt_pk_bf16_f32 v73,v18,v19
v_cvt_pk_bf16_f32 v74,v20,v21
v_cvt_pk_bf16_f32 v75,v22,v23
s_add_u32 m0,s78,8448
s_nop 0
buffer_load_dword v6,s[36:39],s68 offen offset:0 sc1 lds
buffer_store_dwordx4 v[72:75],v3,s[36:39],s73 offen offset:0 sc0 sc1
v_add_f32 v64,v16,v17
v_mul_f32 v65,v16,v16
v_fma_f32 v65,v17,v17,v65
v_add_f32 v64,v64,v18
v_fma_f32 v65,v18,v18,v65
v_add_f32 v64,v64,v19
v_fma_f32 v65,v19,v19,v65
v_add_f32 v64,v64,v20
v_fma_f32 v65,v20,v20,v65
v_add_f32 v64,v64,v21
v_fma_f32 v65,v21,v21,v65
v_add_f32 v64,v64,v22
v_fma_f32 v65,v22,v22,v65
v_add_f32 v64,v64,v23
v_fma_f32 v65,v23,v23,v65
s_nop 1
v_permlane16_swap_b32 v64,v65
v_add_f32 v64,v64,v65
v_mov_b32 v65,v64
s_nop 1
v_permlane32_swap_b32 v64,v65
v_add_f32 v64,v64,v65
ds_write_b32 v13,v64 offset:2048
s_waitcnt lgkmcnt(0)
s_barrier
ds_read_b128 v[76:79],v14 offset:2048
s_waitcnt lgkmcnt(0)
v_add_f32 v76,v76,v77
v_add_f32 v78,v78,v79
v_add_f32 v76,v76,v78
buffer_store_dword v76,v12,s[36:39],s85 offen offset:0 sc0 sc1
s_cmp_ge_u32 s49,s59
s_cbranch_scc1 Lg2_done1_%=
s_add_u32 m0,s78,16384
s_nop 0
buffer_load_dword v6,s[36:39],s68 offen offset:0 sc1 lds
s_waitcnt vmcnt(4)
s_barrier
ds_read_b32 v15,v7 offset:8192
s_lshr_b32 s70,s47,5
buffer_store_dword v5,v4,s[36:39],s70 offen offset:0 sc0 sc1
s_mov_b32 s47,s59
s_mov_b32 s69,18432
s_waitcnt lgkmcnt(0)
v_cmp_eq_u32 vcc,s60,v15
s_nop 0
s_cmp_eq_u32 vcc_lo,-1
s_cbranch_scc1 Lg2_go1_%=
s_add_u32 m0,s78,16640
s_nop 0
buffer_load_dword v6,s[36:39],s68 offen offset:0 sc1 lds
s_waitcnt vmcnt(5)
s_barrier
ds_read_b32 v15,v7 offset:8448
s_mov_b32 s69,18688
s_waitcnt lgkmcnt(0)
v_cmp_eq_u32 vcc,s60,v15
s_nop 0
s_cmp_eq_u32 vcc_lo,-1
s_cbranch_scc1 Lg2_go1_%=
s_add_u32 m0,s78,8192
s_nop 0
buffer_load_dword v6,s[36:39],s68 offen offset:0 sc1 lds
s_waitcnt vmcnt(3)
s_barrier
ds_read_b32 v15,v7 offset:16384
s_lshr_b32 s70,s48,5
buffer_store_dword v5,v4,s[36:39],s70 offen offset:0 sc0 sc1
s_mov_b32 s48,s59
s_mov_b32 s69,10240
s_waitcnt lgkmcnt(0)
v_cmp_eq_u32 vcc,s60,v15
s_nop 0
s_cmp_eq_u32 vcc_lo,-1
s_cbranch_scc1 Lg2_go1_%=
s_add_u32 m0,s78,8448
s_nop 0
buffer_load_dword v6,s[36:39],s68 offen offset:0 sc1 lds
s_waitcnt vmcnt(3)
s_barrier
ds_read_b32 v15,v7 offset:16640
s_mov_b32 s69,10496
s_waitcnt lgkmcnt(0)
v_cmp_eq_u32 vcc,s60,v15
s_nop 0
s_cmp_eq_u32 vcc_lo,-1
s_cbranch_scc1 Lg2_go1_%=
Lg2_poll1_%=:
s_add_u32 m0,s78,16384
s_nop 0
buffer_load_dword v6,s[36:39],s68 offen offset:0 sc1 lds
s_waitcnt vmcnt(2)
s_barrier
ds_read_b32 v15,v7 offset:8192
s_mov_b32 s69,18432
s_waitcnt lgkmcnt(0)
v_cmp_eq_u32 vcc,s60,v15
s_nop 0
s_cmp_eq_u32 vcc_lo,-1
s_cbranch_scc1 Lg2_go1_%=
s_sleep 4
s_add_u32 m0,s78,16640
s_nop 0
buffer_load_dword v6,s[36:39],s68 offen offset:0 sc1 lds
s_waitcnt vmcnt(2)
s_barrier
ds_read_b32 v15,v7 offset:8448
s_mov_b32 s69,18688
s_waitcnt lgkmcnt(0)
v_cmp_eq_u32 vcc,s60,v15
s_nop 0
s_cmp_eq_u32 vcc_lo,-1
s_cbranch_scc1 Lg2_go1_%=
s_sleep 4
s_add_u32 m0,s78,8192
s_nop 0
buffer_load_dword v6,s[36:39],s68 offen offset:0 sc1 lds
s_waitcnt vmcnt(2)
s_barrier
ds_read_b32 v15,v7 offset:16384
s_mov_b32 s69,10240
s_waitcnt lgkmcnt(0)
v_cmp_eq_u32 vcc,s60,v15
s_nop 0)"
R"(
s_cmp_eq_u32 vcc_lo,-1
s_cbranch_scc1 Lg2_go1_%=
s_sleep 4
s_add_u32 m0,s78,8448
s_nop 0
buffer_load_dword v6,s[36:39],s68 offen offset:0 sc1 lds
s_waitcnt vmcnt(2)
s_barrier
ds_read_b32 v15,v7 offset:16640
s_mov_b32 s69,10496
s_waitcnt lgkmcnt(0)
v_cmp_eq_u32 vcc,s60,v15
s_nop 0
s_cmp_eq_u32 vcc_lo,-1
s_cbranch_scc1 Lg2_go1_%=
s_sleep 4
s_add_u32 s79,s79,1
s_cmp_lt_u32 s79,20000
s_cbranch_scc1 Lg2_poll1_%=
Lg2_go1_%=:
s_xor_b32 s62,s64,65536
s_mov_b32 m0,s62
s_nop 0
buffer_load_dwordx4 v0,s[36:39],s49 offen offset:0 sc1 lds
buffer_load_dwordx4 v0,s[36:39],s49 offen offset:1024 sc1 lds
buffer_load_dwordx4 v0,s[36:39],s49 offen offset:2048 sc1 lds
buffer_load_dwordx4 v0,s[36:39],s49 offen offset:3072 sc1 lds
s_add_u32 m0,s62,16384
s_nop 0
buffer_load_dwordx4 v1,s[36:39],s49 offen offset:0 sc1 lds
buffer_load_dwordx4 v1,s[36:39],s49 offen offset:1024 sc1 lds
buffer_load_dwordx4 v1,s[36:39],s49 offen offset:2048 sc1 lds
buffer_load_dwordx4 v1,s[36:39],s49 offen offset:3072 sc1 lds
s_lshr_b32 s72,s49,6
s_lshr_b32 s71,s62,2
s_add_u32 m0,s71,s88
s_nop 0
buffer_load_dwordx4 v10,s[36:39],s49 offen offset:0 sc1 lds
buffer_load_dword v11,s[36:39],s72 offen offset:1024 sc1 lds
s_cmp_eq_u32 vcc_hi,-1
s_cselect_b32 s67,s50,s59
s_xor_b32 s62,s63,65536
s_cmp_ge_u32 s67,s59
s_cselect_b32 s68,s50,s51
s_lshr_b32 s68,s68,5
s_add_u32 m0,s78,256
s_nop 0
buffer_load_dword v6,s[36:39],s68 offen offset:0 sc1 lds
s_mov_b32 m0,s62
s_nop 0
buffer_load_dwordx4 v0,s[36:39],s67 offen offset:0 sc1 lds
buffer_load_dwordx4 v0,s[36:39],s67 offen offset:1024 sc1 lds
buffer_load_dwordx4 v0,s[36:39],s67 offen offset:2048 sc1 lds
buffer_load_dwordx4 v0,s[36:39],s67 offen offset:3072 sc1 lds
s_waitcnt vmcnt(11)
s_barrier
ds_read_b128 v[32:35],v2 offset:32768
ds_read_b128 v[36:39],v2 offset:33792
ds_read_b128 v[40:43],v2 offset:34816
ds_read_b128 v[44:47],v2 offset:35840
ds_read_b128 v[48:51],v2 offset:36864
ds_read_b128 v[52:55],v2 offset:37888
ds_read_b128 v[56:59],v2 offset:38912
s_lshr_b32 s70,s47,5
buffer_store_dword v5,v4,s[36:39],s70 offen offset:0 sc0 sc1
s_mov_b32 s47,s59
s_lshr_b32 s70,s48,5
buffer_store_dword v5,v4,s[36:39],s70 offen offset:0 sc0 sc1
s_mov_b32 s48,s59
s_lshr_b32 s70,s59,5
buffer_store_dword v5,v4,s[36:39],s70 offen offset:0 sc0 sc1
s_waitcnt lgkmcnt(3)
s_setpc_b64 s[92:93]
Lg2_gs1_%=:
s_cmp_eq_u32 s54,1
s_cselect_b32 s68,s50,s49
s_lshr_b32 s68,s68,5
s_lshr_b32 s73,s48,0
s_lshr_b32 s85,s48,5
s_add_u32 m0,s78,8192
s_nop 0
buffer_load_dword v6,s[36:39],s68 offen offset:0 sc1 lds
v_lshlrev_b32 v64,16,v80
v_and_b32 v65,0xffff0000,v80
v_lshlrev_b32 v66,16,v81
v_and_b32 v67,0xffff0000,v81
v_lshlrev_b32 v68,16,v82
v_and_b32 v69,0xffff0000,v82
v_lshlrev_b32 v70,16,v83
v_and_b32 v71,0xffff0000,v83
s_add_u32 m0,s62,16384
s_nop 0
buffer_load_dwordx4 v1,s[36:39],s67 offen offset:0 sc1 lds
v_fma_f32 v64,v64,v89,v88
v_fma_f32 v65,v65,v89,v88
v_fma_f32 v66,v66,v89,v88
v_fma_f32 v67,v67,v89,v88
v_fma_f32 v68,v68,v89,v88
v_fma_f32 v69,v69,v89,v88
v_fma_f32 v70,v70,v89,v88
v_fma_f32 v71,v71,v89,v88
buffer_load_dwordx4 v1,s[36:39],s67 offen offset:1024 sc1 lds
v_add_f32 v16,v16,v64
v_add_f32 v17,v17,v65
v_add_f32 v18,v18,v66
v_add_f32 v19,v19,v67
v_add_f32 v20,v20,v68
v_add_f32 v21,v21,v69
v_add_f32 v22,v22,v70
v_add_f32 v23,v23,v71
buffer_load_dwordx4 v1,s[36:39],s67 offen offset:2048 sc1 lds
v_cvt_pk_bf16_f32 v72,v16,v17
v_cvt_pk_bf16_f32 v73,v18,v19
v_cvt_pk_bf16_f32 v74,v20,v21
v_cvt_pk_bf16_f32 v75,v22,v23
buffer_store_dwordx4 v[72:75],v3,s[36:39],s73 offen offset:0 sc0 sc1
v_add_f32 v64,v16,v17
v_mul_f32 v65,v16,v16
v_fma_f32 v65,v17,v17,v65
buffer_load_dwordx4 v1,s[36:39],s67 offen offset:3072 sc1 lds
v_add_f32 v64,v64,v18
v_fma_f32 v65,v18,v18,v65
v_add_f32 v64,v64,v19
v_fma_f32 v65,v19,v19,v65
v_add_f32 v64,v64,v20
v_fma_f32 v65,v20,v20,v65
v_add_f32 v64,v64,v21
v_fma_f32 v65,v21,v21,v65
s_lshr_b32 s72,s67,6
s_lshr_b32 s71,s62,2
s_add_u32 m0,s71,s88
s_nop 0
buffer_load_dwordx4 v10,s[36:39],s67 offen offset:0 sc1 lds
v_add_f32 v64,v64,v22
v_fma_f32 v65,v22,v22,v65
v_add_f32 v64,v64,v23
v_fma_f32 v65,v23,v23,v65
s_nop 1
v_permlane16_swap_b32 v64,v65
v_add_f32 v64,v64,v65
v_mov_b32 v65,v64
s_nop 1
v_permlane32_swap_b32 v64,v65
buffer_load_dword v11,s[36:39],s72 offen offset:1024 sc1 lds
v_add_f32 v64,v64,v65
ds_write_b32 v13,v64 offset:2048
s_waitcnt lgkmcnt(0)
s_barrier
ds_read_b128 v[76:79],v14 offset:2048
s_waitcnt lgkmcnt(0)
v_add_f32 v76,v76,v77
v_add_f32 v78,v78,v79
v_add_f32 v76,v76,v78
buffer_store_dword v76,v12,s[36:39],s85 offen offset:0 sc0 sc1
s_waitcnt vmcnt(8)
s_barrier
ds_read_b32 v15,v7 offset:8192
ds_read_b128 v[32:35],v2 offset:32768
ds_read_b128 v[36:39],v2 offset:33792
ds_read_b128 v[40:43],v2 offset:34816
ds_read_b128 v[44:47],v2 offset:35840
ds_read_b128 v[48:51],v2 offset:36864
ds_read_b128 v[52:55],v2 offset:37888
ds_read_b128 v[56:59],v2 offset:38912
s_lshr_b32 s70,s47,5
buffer_store_dword v5,v4,s[36:39],s70 offen offset:0 sc0 sc1
s_waitcnt lgkmcnt(7)
v_cmp_eq_u32 vcc,s60,v15
s_nop 0
s_cmp_eq_u32 vcc_hi,-1
s_cselect_b32 s67,s50,s59
s_xor_b32 s62,s63,65536
s_cmp_ge_u32 s67,s59
s_cselect_b32 s68,s50,s51
s_lshr_b32 s68,s68,5
s_add_u32 m0,s78,256
s_nop 0
buffer_load_dword v6,s[36:39],s68 offen offset:0 sc1 lds
s_mov_b32 m0,s62
s_nop 0
buffer_load_dwordx4 v0,s[36:39],s67 offen offset:0 sc1 lds
buffer_load_dwordx4 v0,s[36:39],s67 offen offset:1024 sc1 lds
buffer_load_dwordx4 v0,s[36:39],s67 offen offset:2048 sc1 lds
buffer_load_dwordx4 v0,s[36:39],s67 offen offset:3072 sc1 lds
s_mov_b32 s47,s48
s_mov_b32 s48,s59
s_lshr_b32 s70,s59,5
buffer_store_dword v5,v4,s[36:39],s70 offen offset:0 sc0 sc1
s_lshr_b32 s70,s59,5
buffer_store_dword v5,v4,s[36:39],s70 offen offset:0 sc0 sc1
s_waitcnt lgkmcnt(3)
s_setpc_b64 s[92:93]
Lg2_done1_%=:
s_waitcnt vmcnt(0)
s_barrier
s_lshr_b32 s70,s47,5
buffer_store_dword v5,v4,s[36:39],s70 offen offset:0 sc0 sc1
s_lshr_b32 s70,s48,5
buffer_store_dword v5,v4,s[36:39],s70 offen offset:0 sc0 sc1
s_branch Lg2_done_%=)"
R"(
Lg2_done_%=:
s_waitcnt vmcnt(0) lgkmcnt(0)
s_barrier
Lg2_end_%=:
s_mov_b32 %0,s60
Lcode_g12_e:)"
  : "=&s"(epOut) : "s"(ws), "s"(wave), "s"(sStep), "s"(sX1), "s"(sX2), "s"(sU0), "s"(sWw), "s"(sBuf), "v"(vSrc), "v"(vSrc2), "v"(vA), "v"(vOut), "v"(vFlag), "v"(vPollOff), "v"(vSnapRd), "v"(vX), "v"(vX2), "v"(vAuxS), "v"(vAuxS2), "v"(vOut2), "v"(vStW), "v"(vStR), "v"(vWd0), "v"(vWd1), "v"(vWr0), "v"(vWr1), "v"(vWr2), "s"(w12) : "v0", "v1", "v2", "v3", "v4", "v5", "v6", "v7", "v8", "v9", "v10", "v11", "v12", "v13", "v14", "v15", "v16", "v17", "v18", "v19", "v20", "v21", "v22", "v23", "v24", "v25", "v26", "v27", "v28", "v29", "v30", "v31", "v32", "v33", "v34", "v35", "v36", "v37", "v38", "v39", "v40", "v41", "v42", "v43", "v44", "v45", "v46", "v47", "v48", "v49", "v50", "v51", "v52", "v53", "v54", "v55", "v56", "v57", "v58", "v59", "v60", "v61", "v62", "v63", "v64", "v65", "v66", "v67", "v68", "v69", "v70", "v71", "v72", "v73", "v74", "v75", "v76", "v77", "v78", "v79", "v80", "v81", "v82", "v83", "v84", "v85", "v86", "v87", "v88", "v89", "v90", "v91", "v92", "v93", "v94", "v95", "v96", "v97", "v98", "v99", "v100", "v101", "v102", "v103", "v104", "v105", "v106", "v107", "v108", "v109", "v110", "v111", "v112", "v113", "v114", "v115", "v116", "v117", "v118", "v119", "v120", "v121", "v122", "v123", "v124", "v125", "v126", "v127", "v128", "v129", "v130", "v131", "v132", "v133", "v134", "v135", "v136", "v137", "v138", "v139", "v140", "v141", "v142", "v143", "v144", "v145", "v146", "v147", "v148", "v149", "v150", "v151", "v152", "v153", "v154", "v155", "v156", "v157", "v158", "v159", "v160", "v161", "v162", "v163", "v164", "v165", "v166", "v167", "v168", "v169", "v170", "v171", "v172", "v173", "v174", "v175", "v176", "v177", "v178", "v179", "v180", "v181", "v182", "v183", "v184", "v185", "v186", "v187", "v188", "v189", "v190", "v191", "v192", "v193", "v194", "v195", "v196", "v197", "v198", "v199", "v200", "v201", "v202", "v203", "v204", "v205", "v206", "v207", "v208", "v209", "v210", "v211", "v212", "v213", "v214", "v215", "v216", "v217", "v218", "v219", "v220", "v221", "v222", "v223", "v224", "v225", "v226", "v227", "v228", "v229", "v230", "v231", "v255", "a0", "a1", "a2", "a3", "a4", "a5", "a6", "a7", "a8", "a9", "a10", "a11", "a12", "a13", "a14", "a15", "a16", "a17", "a18", "a19", "a20", "a21", "a22", "a23", "a24", "a25", "a26", "a27", "a28", "a29", "a30", "a31", "a32", "a33", "a34", "a35", "a36", "a37", "a38", "a39", "a40", "a41", "a42", "a43", "a44", "a45", "a46", "a47", "a48", "a49", "a50", "a51", "a52", "a53", "a54", "a55", "a56", "a57", "a58", "a59", "a60", "a61", "a62", "a63", "a64", "a65", "a66", "a67", "a68", "a69", "a70", "a71", "a72", "a73", "a74", "a75", "a76", "a77", "a78", "a79", "a80", "a81", "a82", "a83", "a84", "a85", "a86", "a87", "a88", "a89", "a90", "a91", "a92", "a93", "a94", "a95", "a96", "a97", "a98", "a99", "a100", "a101", "a102", "a103", "a104", "a105", "a106", "a107", "a108", "a109", "a110", "a111", "a112", "a113", "a114", "a115", "a116", "a117", "a118", "a119", "a120", "a121", "a122", "a123", "a124", "a125", "a126", "a127", "a128", "a129", "a130", "a131", "a132", "a133", "a134", "a135", "a136", "a137", "a138", "a139", "a140", "a141", "a142", "a143", "a144", "a145", "a146", "a147", "a148", "a149", "a150", "a151", "a152", "a153", "a154", "a155", "a156", "a157", "a158", "a159", "a160", "a161", "a162", "a163", "a164", "a165", "a166", "a167", "a168", "a169", "a170", "a171", "a172", "a173", "a174", "a175", "a176", "a177", "a178", "a179", "a180", "a181", "a182", "a183", "a184", "a185", "a186", "a187", "a188", "a189", "a190", "a191", "a192", "a193", "a194", "a195", "a196", "a197", "a198", "a199", "a200", "a201", "a202", "a203", "a204", "a205", "a206", "a207", "a208", "a209", "a210", "a211", "a212", "a213", "a214", "a215", "a216", "a217", "a218", "a219", "a220", "a221", "a222", "a223", "a224", "a225", "a226", "a227", "a228", "a229", "a230", "a231", "a232", "a233", "a234", "a235", "a236", "a237", "a238", "a239", "a240", "a241", "a242", "a243", "a244", "a245", "a246", "a247", "a248", "a249", "a250", "a251", "a252", "a253", "a254", "a255", "s36", "s37", "s38", "s39", "s40", "s41", "s42", "s43", "s44", "s45", "s46", "s47", "s48", "s49", "s50", "s51", "s52", "s53", "s54", "s55", "s56", "s57", "s58", "s59", "s60", "s61", "s62", "s63", "s64", "s65", "s66", "s67", "s68", "s69", "s70", "s71", "s72", "s73", "s74", "s75", "s76", "s77", "s78", "s79", "s80", "s81", "s82", "s83", "s84", "s85", "s86", "s87", "s88", "s89", "s90", "s91", "s92", "s93", "s94", "s95", "s96", "s97", "s98", "s99", "vcc", "scc", "memory");
    ep = epOut;
   }
    KARG(ws, 10) KARG(out_, 11)
    u16* out = (u16*)out_;
    LANE_ID(lane2);
    constexpr int NRF = N1 ? 2 : NRW;
    const int wk = kind == 6 ? slice : NRF + (kind - 1) * 32 + rep * 8 + slice;
    for (int t = wk; t < 256 - FINE; t += 64 + NRF) row_last(ws, out, t, lane2, wave, ep);
    if (wk * 4 + wave < FINE * 16) chunk_last(ws, out, (256 - FINE) * 16 + wk * 4 + wave, lane2, ep);
    if (b == 0 && lane2 == 0 && wave == 0) __hip_atomic_store((gu32*)(ws + OFF_EP), ep, __ATOMIC_RELAXED, __HIP_MEMORY_SCOPE_AGENT);
    asm volatile("s_endpgm" ::: "memory");
    __builtin_unreachable();
  }
}

extern "C" void kernel_launch(void* const* d_in, const int* in_sizes, int n_in, void* d_out, int out_size,
                              void* d_ws, size_t ws_size, hipStream_t stream) {
  const u16* q = (const u16*)d_in[0];
  const u16* k = (const u16*)d_in[1];
  const u16* v = (const u16*)d_in[2];
  const u16* shift_k = (const u16*)d_in[3];
  const u16* ssm_k = (const u16*)d_in[4];
  const u16* ssm_D = (const u16*)d_in[5];
  const u16* w1 = (const u16*)d_in[7];
  const u16* w2 = (const u16*)d_in[8];
  const u16* w3 = (const u16*)d_in[9];
  const u16* w4 = (const u16*)d_in[10];
  flow_kernel<<<256, 256, 0, stream>>>(q, k, v, shift_k, ssm_k, ssm_D, w1, w2, w3, w4, (char*)d_ws, (u16*)d_out);
}
```

```cpp
#include <hip/hip_runtime.h>
#include <hip/hip_bf16.h>
#include <stdint.h>
#include <type_traits>

typedef short s16x8 __attribute__((ext_vector_type(8)));
typedef short s16x4 __attribute__((ext_vector_type(4)));
typedef float f32x4 __attribute__((ext_vector_type(4)));
typedef unsigned short u16;
typedef unsigned short u16x8 __attribute__((ext_vector_type(8)));
typedef unsigned short u16x4 __attribute__((ext_vector_type(4)));
typedef float f32x2 __attribute__((ext_vector_type(2)));
typedef __bf16 bf16x2 __attribute__((ext_vector_type(2)));
typedef unsigned u32x4v __attribute__((ext_vector_type(4)));
typedef __attribute__((address_space(1))) unsigned gu32;

constexpr int S_ = 2048, D_ = 1024, F_ = 3584;

constexpr int N1 = 195, N3 = 0, NX = 1, PL1 = 264, KE = 1, RT1 = 0, LB = 66, FL_BYTES = 2141312, WS_USED = 122727552;
constexpr int OFF_KVR = 4194304, OFF_YF = 8388608, OFF_XP = 12582912, OFF_HS = 22085632, OFF_X1 = 31457280, OFF_G = 40894464, OFF_P0 = 71303168, OFF_DBG = 80740352, OFF_P1 = 89128960, OFF_QS = 98566144, OFF_P2 = 99614720, OFF_P3 = 109051904, OFF_S1 = 118489088, OFF_ST = 119013376, OFF_FL = 120586240, OFF_FXP = 120587264, OFF_FH = 120859648, OFF_FX1 = 121132032, OFF_FP = 121405440, OFF_FG = 121950720, OFF_FKV = 122629120, OFF_FY = 122661888, OFF_EP = 122727424;


__device__ __forceinline__ float bf2f(u16 v) { return __uint_as_float(((unsigned)v) << 16); }
__device__ __forceinline__ u16 f2bf(float f) { return __builtin_bit_cast(u16, (__bf16)f); }
__device__ __forceinline__ float rbf(float f) { return bf2f(f2bf(f)); }
__device__ __forceinline__ void st16(void* p, u16x8 v) {
  asm volatile("global_store_dwordx4 %0, %1, off sc1\n\ts_nop 1" :: "v"(p), "v"(v) : "memory");
}
__device__ __forceinline__ void bst16(u32x4v v, __amdgpu_buffer_rsrc_t rs, unsigned vo, unsigned so) {
  asm volatile("buffer_store_dwordx4 %0, %1, %2, %3 offen sc1\n\ts_nop 1" :: "v"(v), "v"(vo), "s"(rs), "s"(so) : "memory");
}
#define WAIT_VM(n) asm volatile("s_waitcnt vmcnt(%0)" ::"n"(n) : "memory")
#define WAIT_LGKM0() asm volatile("s_waitcnt lgkmcnt(0)" ::: "memory")

__device__ __forceinline__ void touch_lds(const char* p, unsigned a) {
  asm volatile("s_mov_b32 m0, %1\n\ts_nop 0\n\tglobal_load_lds_dword %0, off" :: "v"(p), "s"(a) : "memory");
}
#define YSLEEP 4
#define KSLEEP 2
__device__ __forceinline__ f32x2 up2(unsigned p) { return (f32x2){__uint_as_float(p << 16), __uint_as_float(p & 0xffff0000u)}; }
__device__ __forceinline__ unsigned pk2(f32x2 v) { return __builtin_bit_cast(unsigned, __builtin_convertvector(v, bf16x2)); }
__device__ __forceinline__ f32x2 rbf2(f32x2 v) { return up2(pk2(v)); }
#define BARRIER() asm volatile("s_waitcnt lgkmcnt(0)\n\ts_barrier" ::: "memory")
template <int N, class F> __device__ __forceinline__ void sfor(F&& f) {
  if constexpr (N > 0) { sfor<N - 1>(f); f(std::integral_constant<int, N - 1>{}); }
}
template <int NL>
__device__ __forceinline__ void poll_flags(const char* p, int n, int lane, unsigned ep) {
#pragma unroll 1
  for (int it = 0; it < 20000; ++it) {
    bool ok = true;
#pragma unroll
    for (int i = 0; i < NL; ++i) {
      const int f = lane + 64 * i;
      ok &= __hip_atomic_load((gu32*)(p + (f < n ? f : 0) * 128), __ATOMIC_RELAXED, __HIP_MEMORY_SCOPE_AGENT) == ep;
    }
    if (__all(ok)) break;
    __builtin_amdgcn_s_sleep(YSLEEP);
  }
  asm volatile("" ::: "memory");
}

constexpr int CV_HP = (2048 + 32) * 2;
constexpr int CV_UR = (2048 + 256) * 2;
constexpr int CV_CH = 2 * CV_HP + CV_UR;
#define T0 0
#define T0R 4
#define T1 0
#define LATEQ 4
constexpr int CV_STG = 8 * CV_CH;

struct KvRegs { u32x4v k0, k1, k2, k3, k4, k5, k6, v0, v1, v2, v3; };
struct SkRegs { u32x4v s0, s1, s2, s3; };
#define LD16(d, p, pol) asm volatile("global_load_dwordx4 %0, %1, off " pol : "=v"(d) : "v"(p) : "memory")
#define LDS16(d, vo, sp, o, pol) asm volatile("global_load_dwordx4 %0, %1, %2 offset:" #o " " pol : "=v"(d) : "v"(vo), "s"(sp) : "memory")
__device__ __forceinline__ void kv_load(KvRegs& R, const u16* __restrict__ kin, const u16* __restrict__ vin, int b, int tid) {
  const unsigned tr = (b >> 2) * 32 + 4 * (tid >> 5), oa = tr * 2048 + (b & 3) * 512 + 16 * (tid & 31) + 4096;
  LDS16(R.k3, oa, kin, -4096, "nt"); LDS16(R.k4, oa, kin, -2048, "nt"); LDS16(R.k5, oa, kin, 0, "nt"); LDS16(R.k6, oa, kin, 2048, "nt");
  LDS16(R.v0, oa, vin, -4096, "nt"); LDS16(R.v1, oa, vin, -2048, "nt"); LDS16(R.v2, oa, vin, 0, "nt"); LDS16(R.v3, oa, vin, 2048, "nt");
  const unsigned ob = tr ? oa - 6144 : oa;
  LDS16(R.k0, ob, kin, -4096, "nt"); LDS16(R.k1, ob, kin, -2048, "nt"); LDS16(R.k2, ob, kin, 0, "nt");
}
__device__ __forceinline__ void sk_load(SkRegs& R, const u16* __restrict__ shift_k, int b, int tid) {
  const unsigned o = (b & 3) * 2048 + 64 * (tid & 31);
  LDS16(R.s0, o, shift_k, 0, ""); LDS16(R.s1, o, shift_k, 16, ""); LDS16(R.s2, o, shift_k, 32, ""); LDS16(R.s3, o, shift_k, 48, "");
}
template <int N>
__device__ __forceinline__ void kv_wait(KvRegs& R, SkRegs& Q) {
  asm volatile("s_waitcnt vmcnt(%15)" : "+v"(R.k0), "+v"(R.k1), "+v"(R.k2), "+v"(R.k3), "+v"(R.k4), "+v"(R.k5), "+v"(R.k6), "+v"(R.v0), "+v"(R.v1), "+v"(R.v2),
               "+v"(R.v3), "+v"(Q.s0), "+v"(Q.s1), "+v"(Q.s2), "+v"(Q.s3) : "n"(N) : "memory");
}
__device__ __forceinline__ void kv_store(KvRegs& R, const SkRegs& Q, char* __restrict__ ws, char* T, int b, int tid) {
  const int t0 = (b >> 2) * 32, c0 = (b & 3) * 256, ch8 = tid & 31, rg = tid >> 5;
  if (t0 + 4 * rg < 3) R.k0 = R.k1 = R.k2 = (u32x4v){0u, 0u, 0u, 0u};
  typedef unsigned u32x2 __attribute__((ext_vector_type(2)));
  const u32x4v kr[7] = {R.k0, R.k1, R.k2, R.k3, R.k4, R.k5, R.k6}, vr[4] = {R.v0, R.v1, R.v2, R.v3}, sk[4] = {Q.s0, Q.s1, Q.s2, Q.s3};
#pragma unroll
  for (int j2 = 0; j2 < 4; ++j2) {
    const f32x2 sa = up2(sk[j2][0]), sb = up2(sk[j2][1]), sc = up2(sk[j2][2]), sd = up2(sk[j2][3]);
    const f32x2 s0 = {sa[0], sc[0]}, s1 = {sa[1], sc[1]}, s2 = {sb[0], sd[0]}, s3 = {sb[1], sd[1]};
    f32x2 kf[7], o[4];
#pragma unroll
    for (int r = 0; r < 7; ++r) kf[r] = up2(kr[r][j2]);
#pragma unroll
    for (int r = 0; r < 4; ++r)
      o[r] = __builtin_elementwise_fma(kf[r + 3], s0, __builtin_elementwise_fma(kf[r + 2], s1, __builtin_elementwise_fma(kf[r + 1], s2, kf[r] * s3))) * up2(vr[r][j2]);
#pragma unroll
    for (int e = 0; e < 2; ++e)
      *(u32x2*)(T + ch8 * 520 + (2 * j2 + e) * 64 + (28 - 4 * rg) * 2) = (u32x2){pk2((f32x2){o[3][e], o[2][e]}), pk2((f32x2){o[1][e], o[0][e]})};
  }
  BARRIER();
  u16* kvr = (u16*)(ws + OFF_KVR);
#pragma unroll
  for (int i = 0; i < 4; ++i) {
    const int ch = (tid >> 2) + 64 * i, cb = tid & 3;
    const char* p = T + ch * 64 + (ch >> 3) * 8 + cb * 16;
    const u16x4 lo = *(const u16x4*)p, hi = *(const u16x4*)(p + 8);
    st16(kvr + (size_t)(c0 + ch) * S_ + (2016 - t0) + cb * 8, __builtin_shufflevector(lo, hi, 0, 1, 2, 3, 4, 5, 6, 7));
  }
}

typedef unsigned u32x2v __attribute__((ext_vector_type(2)));
__device__ __forceinline__ void h_load(u32x4v (&hv)[8], unsigned& hx, unsigned& dD, const u16* __restrict__ ssm_k, const u16* __restrict__ ssm_D, int c0, int tid) {
  const __amdgpu_buffer_rsrc_t rs = __builtin_amdgcn_make_buffer_rsrc((void*)ssm_k, 0, c0 < 0 ? 0 : D_ * S_ * 2, 0x00020000);
  const unsigned cb = c0 < 0 ? 0 : c0, lane = tid & 63, wave = tid >> 6;
#pragma unroll
  for (int i = 0; i < 8; ++i) asm volatile("buffer_load_dwordx4 %0, %1, %2, %3 offen nt" : "=v"(hv[i]) : "v"(tid * 16), "s"(rs), "s"((cb + i) * 4096) : "memory");
  asm volatile("buffer_load_dword %0, %1, %2, %3 offen" : "=v"(hx) : "v"((lane & 7) * 4096 + ((wave + 1) & 3) * 1024), "s"(rs), "s"(cb * 4096) : "memory");
  asm volatile("global_load_dword %0, %1, %2" : "=v"(dD) : "v"(cb * 2 + wave * 4), "s"(ssm_D) : "memory");
}
template <int N>
__device__ __forceinline__ void h_wait(u32x4v (&hv)[8], unsigned& hx, unsigned& dD) {
  asm volatile("s_waitcnt vmcnt(%10)" : "+v"(hv[0]), "+v"(hv[1]), "+v"(hv[2]), "+v"(hv[3]), "+v"(hv[4]), "+v"(hv[5]), "+v"(hv[6]), "+v"(hv[7]), "+v"(hx), "+v"(dD) : "n"(N) : "memory");
}
__device__ __forceinline__ void h_spread(const u32x4v (&hv)[8], unsigned hx, char* lds, int tid) {
  if (tid < 32 && (tid & 3) != 3) *(u32x4v*)(lds + (tid >> 2) * CV_CH + ((tid >> 1) & 1) * CV_HP + (tid & 1) * 16) = (u32x4v){0u, 0u, 0u, 0u};
  *(u32x4v*)(lds + (tid >> 5) * CV_CH + 2 * CV_HP + 4096 + (tid & 31) * 16) = (u32x4v){0u, 0u, 0u, 0u};
#pragma unroll
  for (int i = 0; i < 8; ++i) {
    char* base = lds + i * CV_CH + 32 + tid * 16;
    unsigned hn = __builtin_amdgcn_update_dpp(0, (int)hv[i][0], 0x130, 0xf, 0xf, true);
    const unsigned last = __builtin_amdgcn_readlane(hx, i);
    if ((tid & 63) == 63) hn = last;
#define AB(a, b) __builtin_amdgcn_alignbyte(a, b, 2)
    if (tid == 0) *(u32x4v*)(lds + i * CV_CH + CV_HP + 16) = (u32x4v){0u, 0u, 0u, hv[i][0] << 16};
    *(u32x4v*)(base) = hv[i];
    *(u32x4v*)(base + CV_HP) = (u32x4v){AB(hv[i][1], hv[i][0]), AB(hv[i][2], hv[i][1]), AB(hv[i][3], hv[i][2]), AB(hn, hv[i][3])};
#undef AB
  }
}

__device__ __forceinline__ void pads(char* __restrict__ ws, int w, int lane, unsigned ep) {
  if (w < 132) {
    const int k = w < 128 ? w >> 5 : w - 128, u = k == 0 ? -1 : k == 1 ? 128 : k == 2 ? 131 : 260;
    st16(ws + (w < 128 ? OFF_HS + u * 32768 + (w & 31) * 1024 : OFF_FH + u * 1024) + lane * 16, __builtin_bit_cast(u16x8, (u32x4v){w >= 128 && !(lane & 7) ? ep : 0u, 0u, 0u, 0u}));
  }
}
__device__ __forceinline__ void prewrite(char* __restrict__ ws, unsigned off, unsigned str, int rep, int tid) {
#pragma unroll
  for (int i = 0; i < 2; ++i) st16(ws + off + (rep + 4 * (tid >> 3) + 132 * i) * str + (tid & 7) * 16, __builtin_bit_cast(u16x8, (u32x4v){0u, 0u, 0u, 0u}));
}

__device__ __forceinline__ void conv_part(char* __restrict__ ws, unsigned dD, char* lds, int g, int tid, int lane, int wave, unsigned ep, const char* wp, unsigned wstep) {
  const __amdgpu_buffer_rsrc_t rs = __builtin_amdgcn_make_buffer_rsrc(ws, 0, WS_USED, 0x00020000);
  const u16* kvr = (const u16*)(ws + OFF_KVR) + (size_t)(8 * g + 2 * wave) * S_;
  auto wait1 = [&](unsigned off) {
#pragma unroll 1
    for (int it = 0; it < 20000; ++it) {
      if (__all(__hip_atomic_load((gu32*)(ws + off), __ATOMIC_RELAXED, __HIP_MEMORY_SCOPE_AGENT) == ep)) break;
      __builtin_amdgcn_s_sleep(KSLEEP);
    }
    asm volatile("" ::: "memory");
  };
  const unsigned lb = (unsigned)(size_t)lds;
  auto u_fetch = [&](int part) {
#pragma unroll
    for (int j = 0; j < 2; ++j)
      asm volatile("s_mov_b32 m0, %1\n\ts_nop 0\n\tglobal_load_lds_dwordx4 %0, off sc1" :: "v"(kvr + j * S_ + part * 512 + lane * 8),
                   "s"(lb + (2 * wave + j) * CV_CH + 2 * CV_HP + part * 1024) : "memory");
  };
  const unsigned xo = (tid >> 4) * 32768 + g * 256 + (tid & 15) * 16;
  u32x4v qv[6];
  u32x2v qs[6];
  auto q_fetch = [&](int s0, int ns) {
#pragma unroll
    for (int i = 0; i < 6; ++i)
      if (i < ns) {
        asm volatile("buffer_load_dwordx4 %0, %1, %2, %3 offen sc1" : "=v"(qv[i]) : "v"(xo), "s"(rs), "s"(OFF_XP + (s0 + i) * 524288) : "memory");
        asm volatile("buffer_load_dwordx2 %0, %1, %2, %3 offen sc1" : "=v"(qs[i]) : "v"(tid * 8), "s"(rs), "s"(OFF_QS + (s0 + i) * 2048) : "memory");
      }
  };
  const int cg = g >> 5;
  const int l15 = lane & 15, lg = lane >> 4, cp = (l15 + 1) & 1;
  const unsigned ao = cp * CV_HP + 2 * (8 * lg + (l15 + 1 - cp)), bo = 2 * CV_HP + 2 * (2032 - 16 * l15 + 8 * lg) - 2 * 256 * 7;
  const unsigned own = lb + 2 * wave * CV_CH;
  char* stg = lds + CV_STG;
  s16x8 ad[2];
#pragma unroll
  for (int j = 0; j < 2; ++j) {
    const int e = 15 - l15 - 8 * lg;
    const unsigned d = (j ? dD >> 16 : dD & 0xffffu) << ((e & 1) * 16);
    ad[j] = __builtin_bit_cast(s16x8, (u32x4v){e >> 1 == 0 ? d : 0u, e >> 1 == 1 ? d : 0u, e >> 1 == 2 ? d : 0u, e >> 1 == 3 ? d : 0u});
  }
  f32x4 acc[2][6];
  unsigned f0, f1;
  bool mid;
  auto finish = [&](auto s0_c, auto ns_c) {
    constexpr int S0 = decltype(s0_c)::value, NS = decltype(ns_c)::value, A0 = S0 == 2 ? 0 : 4, Q0 = S0 == 6 ? 4 : 0;
#pragma unroll
    for (int s = 0; s < NS; ++s)
#pragma unroll
      for (int r = 0; r < 4; ++r) *(unsigned*)(stg + (Q0 + s) * 4352 + l15 * 272 + (4 * lg + r) * 16 + wave * 4) = pk2((f32x2){acc[0][A0 + s][r], acc[1][A0 + s][r]});
    if (S0 == 0) {
      asm volatile("s_waitcnt vmcnt(0)" : "+v"(qv[0]), "+v"(qv[1]), "+v"(qs[0]), "+v"(qs[1]), "+v"(f0) :: "memory");
      mid = __all(f0 == ep);
      if (mid) { u_fetch(2); u_fetch(1); }
    } else if (S0 == 2) asm volatile("" : "+v"(qv[0]), "+v"(qv[1]), "+v"(qv[2]), "+v"(qv[3]), "+v"(qv[4]), "+v"(qv[5]), "+v"(qs[0]), "+v"(qs[1]), "+v"(qs[2]), "+v"(qs[3]), "+v"(qs[4]), "+v"(qs[5]) :: "memory");
    BARRIER();
    u32x4v y4[NS];
#pragma unroll
    for (int s = 0; s < NS; ++s) {
      y4[s] = *(const u32x4v*)(stg + (Q0 + s) * 4352 + (tid >> 4) * 272 + (tid & 15) * 16);
      const f32x2 rd2 = {__uint_as_float(qs[Q0 + s][0]), __uint_as_float(qs[Q0 + s][0])}, nm2 = {__uint_as_float(qs[Q0 + s][1]), __uint_as_float(qs[Q0 + s][1])};
      u32x4v o;
#pragma unroll
      for (int e = 0; e < 4; ++e) {
        const f32x2 x = up2(qv[Q0 + s][e]);
        o[e] = pk2(__builtin_elementwise_fma(__builtin_elementwise_fma(x, rd2, nm2), up2(y4[s][e]), x));
      }
      bst16(o, rs, xo, OFF_XP + (S0 + s) * 524288);
    }
#pragma unroll
    for (int s = 0; s < NS; ++s) bst16(y4[s], rs, (tid >> 3) * 16384 + g * 128 + (tid & 7) * 16, OFF_YF + (S0 + s) * 524288);
  };
  auto touches = [&](int n) {
#pragma unroll
    for (int i = 0; i < 3; ++i)
      if (i < n) { touch_lds(wp, lb + 163584); wp += wstep; }
  };
  auto count = [&](int s0, int ns, auto n_c) {
    WAIT_VM(decltype(n_c)::value);
    BARRIER();
    if (tid < 4 * ns) __hip_atomic_fetch_add((gu32*)(ws + OFF_FXP + (s0 + (tid >> 2)) * 16384 + (tid & 3) * 1024 + (g >> 4) * 128), 1u, __ATOMIC_RELAXED, __HIP_MEMORY_SCOPE_AGENT);
  };
  auto pair = [&](unsigned bb) {
    u32x2v ax[2][2][2][2];
    s16x8 bx[2][2][2];
    auto rdA = [&](int q) {
#pragma unroll
      for (int j = 0; j < 2; ++j)
#pragma unroll
        for (int m = 0; m < 2; ++m) {
          asm volatile("ds_read2_b32 %0, %1 offset0:%2 offset1:%3" : "=v"(ax[q & 1][j][m][0]) : "v"(own + j * CV_CH + ao), "n"(128 * m + 16 * q), "n"(128 * m + 16 * q + 1));
          asm volatile("ds_read2_b32 %0, %1 offset0:%2 offset1:%3" : "=v"(ax[q & 1][j][m][1]) : "v"(own + j * CV_CH + ao), "n"(128 * m + 16 * q + 2), "n"(128 * m + 16 * q + 3));
          asm volatile("ds_read_b128 %0, %1 offset:%2" : "=v"(bx[q & 1][j][m]) : "v"(bb), "n"(j * CV_CH + 512 * (1 - m) + 64 * q));
        }
    };
    rdA(0);
    sfor<8>([&](auto q_c) {
      constexpr int q = decltype(q_c)::value, p = q & 1;
      if (q + 1 < 8) rdA(q + 1);
      asm volatile("s_waitcnt lgkmcnt(%0)" :: "n"(q + 1 < 8 ? 12 : 0) : "memory");
      __builtin_amdgcn_sched_barrier(0);
#pragma unroll
      for (int j = 0; j < 2; ++j) {
        const s16x8 a0f = __builtin_bit_cast(s16x8, __builtin_shufflevector(ax[p][j][0][0], ax[p][j][0][1], 0, 1, 2, 3)), a1f = __builtin_bit_cast(s16x8, __builtin_shufflevector(ax[p][j][1][0], ax[p][j][1][1], 0, 1, 2, 3));
        if (q == 0) {
          acc[j][4] = __builtin_amdgcn_mfma_f32_16x16x32_bf16(ad[j], bx[p][j][0], acc[j][4], 0, 0, 0);
          acc[j][5] = __builtin_amdgcn_mfma_f32_16x16x32_bf16(ad[j], bx[p][j][1], acc[j][5], 0, 0, 0);
        }
        acc[j][4] = __builtin_amdgcn_mfma_f32_16x16x32_bf16(a0f, bx[p][j][0], acc[j][4], 0, 0, 0);
        acc[j][5] = __builtin_amdgcn_mfma_f32_16x16x32_bf16(a0f, bx[p][j][1], acc[j][5], 0, 0, 0);
        acc[j][5] = __builtin_amdgcn_mfma_f32_16x16x32_bf16(a1f, bx[p][j][0], acc[j][5], 0, 0, 0);
      }
      __builtin_amdgcn_sched_barrier(0);
    });
  };
  wait1(OFF_FKV + ((lane & 15) * 4 + cg) * 128);
  u_fetch(3);
  wait1(OFF_FY + (lane & 31) * 128);
  q_fetch(0, 2);
  const unsigned fm = OFF_FKV + ((16 + (lane & 31)) * 4 + cg) * 128;
  asm volatile("global_load_dword %0, %1, %2 sc1" : "=v"(f0) : "v"(fm), "s"(ws) : "memory");
#pragma unroll
  for (int i = 0; i < 4; ++i) acc[i >> 1][4 + (i & 1)] = (f32x4){0.f, 0.f, 0.f, 0.f};
  pair(own + bo + 512 * 6);
  finish(std::integral_constant<int, 0>{}, std::integral_constant<int, 2>{});
  u32x2v a0[8][2], a1[8][2];
  s16x8 b0[6], b1[6];
  unsigned pa[4], pb = own + bo;
#pragma unroll
  for (int i = 0; i < 4; ++i) pa[i] = own + ao + 1024 * i;
  auto rd1 = [&](int rr, int i, u32x2v (&ah)[8][2], s16x8 (&bfr)[6]) {
    const int m = i < 18 ? i / 3 : 6 + (i - 18) / 2, e = i < 18 ? i % 3 : i & 1;
    if (e < 2) asm volatile("ds_read2_b32 %0, %1 offset0:%2 offset1:%3" : "=v"(ah[m][e & 1]) : "v"(pa[m >> 1]), "n"(128 * (m & 1) + 2 * e + 16 * rr), "n"(128 * (m & 1) + 2 * e + 16 * rr + 1));
    else asm volatile("ds_read_b128 %0, %1 offset:%2" : "=v"(bfr[m < 6 ? m : 0]) : "v"(pb), "n"(512 * (7 - m) + 64 * rr));
  };
  auto round = [&](f32x4 (&ac)[6], u32x2v (&ah)[8][2], s16x8 (&bfr)[6], u32x2v (&ahn)[8][2], s16x8 (&bfn)[6], int rr, unsigned step) {
    int k = 0;
#pragma unroll
    for (int s = 2; s <= 7; ++s)
#pragma unroll
      for (int m = 0; m <= s; ++m) {
        if (s - m > 5) continue;
        const s16x8 af = __builtin_bit_cast(s16x8, __builtin_shufflevector(ah[m][0], ah[m][1], 0, 1, 2, 3));
        ac[s - 2] = __builtin_amdgcn_mfma_f32_16x16x32_bf16(af, bfr[s - m > 5 ? 0 : s - m], ac[s - 2], 0, 0, 0);
        __builtin_amdgcn_sched_barrier(0);
        if (m == s) asm volatile("s_waitcnt lgkmcnt(12)" ::: "memory");
#pragma unroll
        for (int i = 0; i < 22; ++i)
          if (i * 4 / 3 == k) {
            rd1(rr, i, ahn, bfn);
            if (rr && (i == 4 || i == 10 || i == 16 || i == 21)) asm volatile("v_add_u32 %0, %1, %0" : "+v"(pa[i / 6]) : "s"(step));
            if (rr && i == 17) asm volatile("v_add_u32 %0, %1, %0" : "+v"(pb) : "s"(step));
          }
        ++k;
        __builtin_amdgcn_sched_barrier(0);
      }
  };
  if (!mid) { wait1(fm); u_fetch(2); u_fetch(1); WAIT_VM(0); }
  WAIT_VM(4);
#pragma unroll
  for (int i = 0; i < 22; ++i) rd1(0, i, a0, b0);
  WAIT_LGKM0();
  auto channel = [&](auto j_c, auto r0_c, auto r1_c) {
    constexpr int j = decltype(j_c)::value, r0 = decltype(r0_c)::value, r1 = decltype(r1_c)::value;
    __builtin_amdgcn_sched_barrier(0);
    if (r0 == 0) {
#pragma unroll
      for (int s = 2; s <= 7; ++s) acc[j][s - 2] = s < 6 ? __builtin_amdgcn_mfma_f32_16x16x32_bf16(ad[j], b0[s], (f32x4){0.f, 0.f, 0.f, 0.f}, 0, 0, 0) : (f32x4){0.f, 0.f, 0.f, 0.f};
    }
#pragma unroll 1
    for (int r = r0; r < r1; r += 2) {
      if (j == 0 && r == 2) {
        asm volatile("s_waitcnt vmcnt(0)\n\ts_barrier" ::: "memory");
        if (tid < 8) __hip_atomic_fetch_add((gu32*)(ws + OFF_FXP + (tid >> 2) * 16384 + (tid & 3) * 1024 + (g >> 4) * 128), 1u, __ATOMIC_RELAXED, __HIP_MEMORY_SCOPE_AGENT);
      }
      if (T0 && j == 0 && r == T0R) touches(T0);
      round(acc[j], a0, b0, a1, b1, 1, r < 6 ? 128 : j ? 0 : CV_CH - 384);
      round(acc[j], a1, b1, a0, b0, 0, 0);
    }
    WAIT_LGKM0();
    __builtin_amdgcn_sched_barrier(0);
  };
  typedef std::integral_constant<int, 0> I0;
  typedef std::integral_constant<int, 1> I1;
  channel(I0{}, I0{}, std::integral_constant<int, 8>{});
  channel(I1{}, I0{}, std::integral_constant<int, LATEQ - 2>{});
  const unsigned fa = OFF_FY + (32 + lane) * 128, fb = lane < 32 ? OFF_FY + (96 + lane) * 128 : OFF_FKV + ((48 + (lane & 15)) * 4 + cg) * 128;
  asm volatile("global_load_dword %0, %1, %2 sc1" : "=v"(f0) : "v"(fa), "s"(ws) : "memory");
  asm volatile("global_load_dword %0, %1, %2 sc1" : "=v"(f1) : "v"(fb), "s"(ws) : "memory");
  channel(I1{}, std::integral_constant<int, LATEQ - 2>{}, std::integral_constant<int, LATEQ>{});
  asm volatile("s_waitcnt vmcnt(0)" : "+v"(f0), "+v"(f1) :: "memory");
  if (!__all(f0 == ep && f1 == ep)) { wait1(fa); wait1(fb); }
  u_fetch(0);
  q_fetch(2, 6);
  touches(T1);
  channel(I1{}, std::integral_constant<int, LATEQ>{}, std::integral_constant<int, 8>{});
  WAIT_VM(T1);
  finish(std::integral_constant<int, 2>{}, std::integral_constant<int, 4>{});
  pair(own + bo);
  count(2, 4, I0{});
  finish(std::integral_constant<int, 6>{}, std::integral_constant<int, 2>{});
  touches(3 - T0 - T1);
  count(6, 2, std::integral_constant<int, 3 - T0 - T1>{});
}
__device__ __forceinline__ float sum2_seg(float s, float q) {
  s += __builtin_bit_cast(float, __builtin_amdgcn_update_dpp(0, __builtin_bit_cast(int, s), 0x128, 0xf, 0xf, true));
  q += __builtin_bit_cast(float, __builtin_amdgcn_update_dpp(0, __builtin_bit_cast(int, q), 0x128, 0xf, 0xf, true));
  asm volatile("s_nop 1\n\tv_permlane16_swap_b32 %0, %1" : "+v"(s), "+v"(q));
  s += q;
  q = s;
  asm volatile("s_nop 1\n\tv_permlane32_swap_b32 %0, %1" : "+v"(s), "+v"(q));
  return s + q;
}
#define LASTSLEEP 8
__device__ __forceinline__ void wait_flags(const char* p, int lane, int n, unsigned ep) {
  gu32* f = (gu32*)(p + (lane < n ? lane : 0) * 128);
#pragma unroll 1
  for (int it = 0; it < 10000; ++it) {
    if (__all(__hip_atomic_load(f, __ATOMIC_RELAXED, __HIP_MEMORY_SCOPE_AGENT) == ep)) break;
    __builtin_amdgcn_s_sleep(LASTSLEEP);
  }
  asm volatile("" ::: "memory");
}
struct QRegs { u32x4v a[4], b[4]; };
__device__ __forceinline__ void q_load(QRegs& R, const u16* __restrict__ q, int f, int lane, int wave) {
  const unsigned o = (f * 16 + (lane & 7)) * 2048 + wave * 512 + (lane >> 3) * 16, o2 = o + 16384;
  LDS16(R.a[0], o, q, 0, "nt"); LDS16(R.b[0], o2, q, 0, "nt"); LDS16(R.a[1], o, q, 128, "nt"); LDS16(R.b[1], o2, q, 128, "nt");
  LDS16(R.a[2], o, q, 256, "nt"); LDS16(R.b[2], o2, q, 256, "nt"); LDS16(R.a[3], o, q, 384, "nt"); LDS16(R.b[3], o2, q, 384, "nt");
}
template <int N>
__device__ __forceinline__ void q_wait(QRegs& R, unsigned& e) {
  asm volatile("s_waitcnt vmcnt(%9)" : "+v"(R.a[0]), "+v"(R.a[1]), "+v"(R.a[2]), "+v"(R.a[3]), "+v"(R.b[0]), "+v"(R.b[1]), "+v"(R.b[2]), "+v"(R.b[3]), "+v"(e) : "n"(N) : "memory");
}
__device__ __forceinline__ void row_stats(const u32x4v (&xq)[4], char* lds, int k, int lane, int wave, float& mean, float& var) {
  f32x2* red = (f32x2*)(lds + 16640);
  const int r8 = lane & 7, seg = lane >> 3;
  f32x2 s = {0.f, 0.f}, sq = {0.f, 0.f};
#pragma unroll
  for (int i = 0; i < 16; ++i) { const f32x2 x = up2(xq[i >> 2][i & 3]); s += x; sq = __builtin_elementwise_fma(x, x, sq); }
  const float a = sum2_seg(s[0] + s[1], sq[0] + sq[1]);
  if ((seg & 5) == 0) ((float*)red)[((k * 4 + wave) * 8 + r8) * 2 + (seg >> 1)] = a;
  BARRIER();
  const f32x2 t = (red[(k * 4) * 8 + r8] + red[(k * 4 + 1) * 8 + r8]) + (red[(k * 4 + 2) * 8 + r8] + red[(k * 4 + 3) * 8 + r8]);
  mean = t[0] * (1.0f / 1024.0f);
  var = fmaxf(t[1] * (1.0f / 1024.0f) - mean * mean, 0.f);
}
__device__ __forceinline__ void q_store(char* __restrict__ ws, int t, int lane, int wave, const u32x4v (&xq)[4], float mean, float var) {
  const __amdgpu_buffer_rsrc_t rs = __builtin_amdgcn_make_buffer_rsrc(ws, 0, WS_USED, 0x00020000);
  const int r8 = lane & 7, seg = lane >> 3;
  const unsigned fo = (t >> 1) * 32768 + (wave * 8 + (seg >> 2)) * 1024 + ((seg & 3) * 16 + (t & 1) * 8 + r8) * 16;
#pragma unroll
  for (int i = 0; i < 4; ++i) bst16(xq[i], rs, fo + i * 2048, OFF_XP);
  const float rd = __builtin_amdgcn_rsqf(var + 1e-5f);
  if (wave == 0 && seg == 0) __builtin_amdgcn_raw_buffer_store_b64((u32x2v){__float_as_uint(rd), __float_as_uint(-mean * rd)}, rs, t * 64 + r8 * 8, OFF_QS, 16);
}
__device__ __forceinline__ void rows_last(char* __restrict__ ws, u16* __restrict__ out, int t, int step, int end, int lane, int wave, unsigned ep) {
  const __amdgpu_buffer_rsrc_t rs = __builtin_amdgcn_make_buffer_rsrc(ws, 0, WS_USED, 0x00020000);
  const int r8 = lane & 7, seg = lane >> 3;
  const char* fb = ws + OFF_FP + 2 * LB * 2048 + ((lane & 3) * 4 + wave) * 128;
  unsigned f = ep + 1;
#pragma unroll 1
  for (; t < end; t += step) {
#pragma unroll 1
    for (int it = 0; it < 10000 && !__all(f == ep); ++it) {
      if (it) __builtin_amdgcn_s_sleep(LASTSLEEP);
      f = __hip_atomic_load((gu32*)(fb + (t >> 1) * 2048), __ATOMIC_RELAXED, __HIP_MEMORY_SCOPE_AGENT);
    }
    asm volatile("" ::: "memory");
    const unsigned fo = (PL1 + t) * 16384 + wave * 4096 + lane * 16;
    u32x4v p0[4], p1[4], p2[4], p3[4];
#pragma unroll
    for (int i = 0; i < 4; ++i) {
      p0[i] = __builtin_bit_cast(u32x4v, __builtin_amdgcn_raw_buffer_load_b128(rs, fo + i * 1024, OFF_P0, 16));
      p1[i] = __builtin_bit_cast(u32x4v, __builtin_amdgcn_raw_buffer_load_b128(rs, fo + i * 1024, OFF_P1, 16));
      p2[i] = __builtin_bit_cast(u32x4v, __builtin_amdgcn_raw_buffer_load_b128(rs, fo + i * 1024, OFF_P2, 16));
      p3[i] = __builtin_bit_cast(u32x4v, __builtin_amdgcn_raw_buffer_load_b128(rs, fo + i * 1024, OFF_P3, 16));
    }
    f = __hip_atomic_load((gu32*)(fb + (min(t + step, 255) >> 1) * 2048), __ATOMIC_RELAXED, __HIP_MEMORY_SCOPE_AGENT);
    u32x4v o[4];
#pragma unroll
    for (int i = 0; i < 4; ++i)
#pragma unroll
      for (int e = 0; e < 4; ++e) o[i][e] = pk2((up2(p0[i][e]) + up2(p1[i][e])) + (up2(p2[i][e]) + up2(p3[i][e])));
    asm volatile("" : "+v"(f));
#pragma unroll
    for (int i = 0; i < 4; ++i) st16(out + (size_t)(t * 8 + r8) * D_ + wave * 256 + i * 64 + seg * 8, __builtin_bit_cast(u16x8, o[i]));
  }
}

#define CSLEEP 4
#define FINE (N3 ? 16 - 4 * NX : N1 ? 16 : 20)
__device__ __forceinline__ void chunk_last(char* __restrict__ ws, u16* __restrict__ out, int c, int lane, unsigned ep) {
  const __amdgpu_buffer_rsrc_t rs = __builtin_amdgcn_make_buffer_rsrc(ws, 0, WS_USED, 0x00020000);
  const unsigned fo = PL1 * 16384 + c * 1024 + lane * 16;
  gu32* f = (gu32*)(ws + OFF_FP + (2 * LB + (c >> 5)) * 2048 + ((lane & 3) * 4 + ((c >> 2) & 3)) * 128);
#pragma unroll 1
  for (int it = 0; it < 20000; ++it) {
    if (__all(__hip_atomic_load(f, __ATOMIC_RELAXED, __HIP_MEMORY_SCOPE_AGENT) == ep)) break;
    __builtin_amdgcn_s_sleep(CSLEEP);
  }
  asm volatile("" ::: "memory");
  const u32x4v p0 = __builtin_bit_cast(u32x4v, __builtin_amdgcn_raw_buffer_load_b128(rs, fo, OFF_P0, 16)), p1 = __builtin_bit_cast(u32x4v, __builtin_amdgcn_raw_buffer_load_b128(rs, fo, OFF_P1, 16));
  const u32x4v p2 = __builtin_bit_cast(u32x4v, __builtin_amdgcn_raw_buffer_load_b128(rs, fo, OFF_P2, 16)), p3 = __builtin_bit_cast(u32x4v, __builtin_amdgcn_raw_buffer_load_b128(rs, fo, OFF_P3, 16));
  u32x4v o;
#pragma unroll
  for (int e = 0; e < 4; ++e) o[e] = pk2((up2(p0[e]) + up2(p1[e])) + (up2(p2[e]) + up2(p3[e])));
  st16(out + (size_t)((c >> 4) * 8 + (lane & 7)) * D_ + (c & 15) * 64 + (lane >> 3) * 8, __builtin_bit_cast(u16x8, o));
}

__device__ __forceinline__ int swzB(int r) { return ((((r >> 1) & 1) | (((r >> 3) & 1) << 1)) << 1); }
#define FLW 1
#define MAP3 1
#define MAP12 1
#define FLIP 0
#define KVL 0
#define RE 0
#define ROT 0
#define TE 126
#define FR 4
#define NRW 18
#define PFROWS 110
__global__ __launch_bounds__(256, 1) void flow_kernel(const u16* __restrict__ q, const u16* __restrict__ kin, const u16* __restrict__ vin,
                                                      const u16* __restrict__ shift_k, const u16* __restrict__ ssm_k, const u16* __restrict__ ssm_D,
                                                      const u16* __restrict__ w1, const u16* __restrict__ w2,
                                                      const u16* __restrict__ w3, const u16* __restrict__ w4, char* __restrict__ ws,
                                                      u16* __restrict__ out) {
  __shared__ __attribute__((aligned(16))) char lds[163840];
  const int tid = threadIdx.x, lane = tid & 63, wave = __builtin_amdgcn_readfirstlane(tid >> 6);
  const int b = blockIdx.x, xcd = b & 7, idx = b >> 3;
  const unsigned sLds = (unsigned)(size_t)lds, sScr = sLds + 163584;
  asm volatile("s_getpc_b64 s[98:99]\n\ts_mov_b32 m0, %1\n\ts_nop 0\n\tglobal_load_lds_dword %0, s[98:99]" :: "v"(tid * 128), "s"(sScr) : "s98", "s99", "memory");
  asm volatile("" :: "s"(q), "s"(kin), "s"(vin), "s"(shift_k), "s"(ssm_k), "s"(ssm_D), "s"(w1), "s"(w2), "s"(w3), "s"(w4), "s"(ws));
  int kind, rep, slice, kh = 0;
  const int P = xcd >> 1, par = (xcd & 1) ^ FLIP, li = idx - (par ? 20 + RE : 12 - RE);
  const int qq = (par ? 0 : 12 + RE) + idx - 8;
  auto code_touch = [&]() {
    unsigned long long cp;
    unsigned n;
    asm volatile("s_getpc_b64 %0\nLpd%=:\n"
                 "s_mov_b32 s98, Lcode_r_b-Lpd%=\ns_mov_b32 %1, Lcode_r_e-Lcode_r_b\n"
                 "s_cmp_eq_u32 %2, 1\ns_cselect_b32 s98, Lcode_g12_b-Lpd%=, s98\ns_cselect_b32 %1, Lcode_g2_b-Lcode_g12_b, %1\n"
                 "s_cmp_eq_u32 %2, 2\ns_cselect_b32 s98, Lcode_g2_b-Lpd%=, s98\ns_cselect_b32 %1, Lcode_g12_e-Lcode_g2_b, %1\n"
                 "s_cmp_eq_u32 %2, 3\ns_cselect_b32 s98, Lcode_g3_b-Lpd%=, s98\ns_cselect_b32 %1, Lcode_g3_e-Lcode_g3_b, %1\n"
                 "s_cmp_eq_u32 %2, 4\ns_cselect_b32 s98, Lcode_g4_b-Lpd%=, s98\ns_cselect_b32 %1, Lcode_g4_e-Lcode_g4_b, %1\n"
                 "s_cmp_eq_u32 %2, 5\ns_cselect_b32 s98, Lcode_g3o_b-Lpd%=, s98\ns_cselect_b32 %1, Lcode_g3o_e-Lcode_g3o_b, %1\n"
                 "s_ashr_i32 s99, s98, 31\ns_mov_b64 vcc, %0\ns_add_u32 s98, s98, vcc_lo\ns_addc_u32 s99, s99, vcc_hi\ns_mov_b64 %0, s[98:99]"
                 : "=&s"(cp), "=&s"(n) : "s"(kind) : "s98", "s99", "scc", "vcc");
    asm volatile("s_mov_b32 m0, %3\n\ts_nop 0\n\tglobal_load_lds_dword %0, %2\n\tglobal_load_lds_dword %1, %2"
                 :: "v"(min(tid * 128u, n - 4)), "v"(min(32768 + tid * 128u, n - 4)), "s"(cp), "s"(sScr) : "memory");
  };
  unsigned ep = 0;
#define EP_LOAD() asm volatile("global_load_dword %0, %1, %2 sc1" : "=v"(ep) : "v"(OFF_EP), "s"(ws) : "memory")
  auto epoch = [&]() { const unsigned e = __builtin_amdgcn_readfirstlane(ep) + 1u; return e ? e : 1u; };
  auto q_unit = [&](QRegs& QR, int f) {
    float ma, va, mb, vb_;
    row_stats(QR.a, lds + CV_STG, 0, lane, wave, ma, va);
    row_stats(QR.b, lds + CV_STG, 1, lane, wave, mb, vb_);
    q_store(ws, 2 * f, lane, wave, QR.a, ma, va);
    q_store(ws, 2 * f + 1, lane, wave, QR.b, mb, vb_);
  };
  auto publish = [&](unsigned off) {
    BARRIER();
    if (tid == 0) __hip_atomic_store((gu32*)(ws + off), ep, __ATOMIC_RELAXED, __HIP_MEMORY_SCOPE_AGENT);
  };
  if (idx < 8 && par == MAP12) {
    kind = 1; rep = P; slice = idx;
    code_touch();
    prewrite(ws, OFF_FH + slice * 128, 1024, rep, tid);
  } else {
    const int cv = (li + (par ? 20 + RE : 0)) * 4 + P, n = idx < 8 ? 64 + P * 8 + idx : qq * 4 + P;
    QRegs QR;
    if (idx >= 8) {
      KvRegs KR;
      SkRegs SK;
      if (li >= 0) {
        const int tile = 64 + cv;
        u32x4v hv[8];
        unsigned hx, dD;
        h_load(hv, hx, dD, ssm_k, ssm_D, 8 * cv, tid);
        EP_LOAD();
        sk_load(SK, shift_k, tile, tid);
        kv_load(KR, kin, vin, tile, tid);
        {
          int l2 = li, p2 = par;
          asm volatile("" : "+s"(l2), "+s"(p2));
          const int nl = 8 - (P & 1), l = ((15 * P + 1) >> 1) + l2, tl = (l * 205) >> 10, kl = l - 5 * tl;
          kind = l2 < 20 ? 4 : 6; rep = l2 < 20 ? l2 >> 2 : 0; slice = l2 < 20 ? l2 & 3 : ((9 * P) >> 1) + l2 - 20; kh = P;
          if (p2 == 1) {
            kind = l2 < nl ? 3 : 6; rep = l2 < nl ? 3 + ((kl & 1) | (kl >> 2)) : 0;
            slice = l2 < nl ? 3 * tl + (kl >> 1) : ((9 * P) >> 1) + RE + l2 - nl;
          }
        }
        touch_lds((const char*)((par ^ MAP12) ? w2 : w1) + (size_t)(par ? (li < 8 ? li * 256 + tid : 0) : (li < 16 ? li * 128 + (tid & 127) : 0)) * 1024 + 384, sScr);
        code_touch();
        h_wait<18>(hv, hx, dD);
        asm volatile("" : "+v"(ep));
        ep = epoch();
        h_spread(hv, hx, lds, tid);
        kv_wait<2>(KR, SK);
        kv_store(KR, SK, ws, lds + CV_STG, tile, tid);
        WAIT_VM(0);
        publish(OFF_FKV + tile * 128);
        const char* wp = (const char*)w4 + (kind != 4 ? 0 : (size_t)(kh * 896 + li * 15) * 2048 + min(tid, 239) * 128);
        conv_part(ws, dD, lds, cv, tid, lane, wave, ep, wp, kind != 4 ? 0 : 298 * 2048);
      } else {
        kv_load(KR, kin, vin, n, tid);
        sk_load(SK, shift_k, n, tid);
        EP_LOAD();
        {
          int q2 = qq;
          asm volatile("" : "+s"(q2));
          const int e = 15 * P + q2, t = (e * 205) >> 11, k = e - 10 * t, k3 = min((k * 11) >> 5, 2);
          kind = q2 < 15 ? 3 : 5; rep = q2 < 15 ? k - 3 * k3 : P; slice = q2 < 15 ? 3 * t + k3 : 18;
        }
        kv_wait<0>(KR, SK);
        asm volatile("" : "+v"(ep));
        ep = epoch();
        kv_store(KR, SK, ws, lds + CV_STG, n, tid);
        pads(ws, n * 4 + wave, lane, ep);
        if (n < 8)
          st16(ws + OFF_FXP + (lane >> 3) * 16384 + wave * 1024 + (2 * (n & 3) + (n >> 2)) * 128 + (lane & 7) * 16, __builtin_bit_cast(u16x8, (u32x4v){ep - 16u, 0u, 0u, 0u}));
        kv_load(KR, kin, vin, 192 + n, tid);
        WAIT_VM(11);
        publish(OFF_FKV + n * 128);
        q_load(QR, q, 32 + n, lane, wave);
        kv_wait<8>(KR, SK);
        kv_store(KR, SK, ws, lds, 192 + n, tid);
        q_wait<0>(QR, ep);
        publish(OFF_FKV + (192 + n) * 128);
      }
    } else {
      QRegs Q0;
      q_load(Q0, q, n - 64, lane, wave);
      EP_LOAD();
      kind = 2; rep = P; slice = idx;
      q_wait<0>(Q0, ep);
      ep = epoch();
      q_load(QR, q, 32 + n, lane, wave);
      q_unit(Q0, n - 64);
      q_wait<0>(QR, ep);
      publish(OFF_FY + (n - 64) * 128);
    }
    if (li < 0) {
      q_unit(QR, 32 + n);
      {
        const char* wp = (const char*)w2 + (size_t)(n < 64 ? 0 : (n - 64) * 512 + tid) * 128;
        if (!(ROT & 2)) {
          touch_lds(wp, sScr);
          touch_lds(wp + (n < 64 ? 0 : 32768), sScr);
        }
      }
      WAIT_VM((ROT & 2) ? 0 : 2);
      publish(OFF_FY + (32 + n) * 128);
      if (n >= 64) {
        prewrite(ws, OFF_FX1 + slice * 128, 1024, rep, tid);
        prewrite(ws, OFF_FP + slice * 128, 2048, rep, tid);
        prewrite(ws, OFF_FP + (8 + slice) * 128, 2048, rep, tid);
        prewrite(ws, OFF_FG + slice * 128, 2560, rep, tid);
        prewrite(ws, OFF_FG + (8 + slice) * 128, 2560, rep, tid);
        if (slice < 3) prewrite(ws, OFF_FG + (16 + slice) * 128, 2560, rep, tid);
        st16(ws + OFF_FXP + slice * 128 + (132 + rep + 4 * (tid >> 3)) * 1024 + (tid & 7) * 16, __builtin_bit_cast(u16x8, (u32x4v){0u, 0u, 0u, 0u}));
      }
      code_touch();
      if (n < 64) {
#pragma unroll
        for (int i = 0; i < 4; ++i) {
          const unsigned x = rep * 1024 + i * 256 + tid, row = kind == 3 ? (x * 21846) >> 16 : x >> 1;
          if (rep < (kind == 3 ? 3 : 2)) touch_lds((const char*)w3 + (size_t)row * 7168 + slice * 384 + (x - row * (kind == 3 ? 3 : 2)) * 128, sScr);
        }
      }
    }
    BARRIER();
  }
  typedef char* const __attribute__((address_space(4)))* KArgs;
#define KARG(x, i) char* x; { KArgs ka_ = (KArgs)__builtin_amdgcn_kernarg_segment_ptr(); asm volatile("" : "+s"(ka_)); x = ka_[i]; }
  const unsigned sDbg0 = OFF_DBG + (b * 4 + wave) * 8192;
#define LANE_ID(x) int x; asm volatile("v_mbcnt_lo_u32_b32 %0, -1, 0\n\tv_mbcnt_hi_u32_b32 %0, -1, %0" : "=v"(x))
#define OOB 0x80000000u
#define COMMON(NF, FIN, FOUT, SN0, WS, LDW, NREP)                                                                                    \
  LANE_ID(ln);                                                                                                                       \
  const int l15 = ln & 15, lg = ln >> 4, lq = l15 >> 2, lp = ln & 3, rr = 8 * lg + lq;                                               \
  const unsigned vA = ln * 16, vFlag = (ln < FLW && wave == 3) ? (FOUT) + ln * 4 : OOB;                                                        \
  const unsigned vPollOff = wave < 2 && ln < (NF) ? (FIN) + ((NF) == 16 && ln >= 8 ? (ln & 2 ? ln : ln - 16) : ln) * 128 : OOB, vSnapOff = wave == 0 ? vPollOff : OOB;                   \
  const unsigned vSnapRd = (SN0) + (ln >> 5) * (WS) + ((ln & 31) < (NF) ? (ln & 31) : 0) * 4;                                        \
  const unsigned vWd0 = (ln >> 3) * (LDW) + ((ln & 7) ^ swzB(ln >> 3)) * 16, vWd1 = ((ln >> 3) + 8) * (LDW) + ((ln & 7) ^ swzB(8 + (ln >> 3))) * 16; \
  const unsigned vWr0 = (kind == 4 ? wave * 4096 : (wave >> 1) * 8192) + rr * 128 + (((kind == 4 ? kh >> 1 : wave & 1) * 4 + lp) ^ swzB(rr)) * 16, vWr1 = vWr0 + 8;                  \
  const unsigned vWr2 = kind == 4 ? vWr0 ^ 64 : 16384 + rr * 128 + ((2 * wave + (lp >> 1)) ^ swzB(rr)) * 16 + (lp & 1) * 8;                                  \
                                            \
  const int res = kind == 3 ? rep + 3 * KE : rep;                     \
  const int nm = kind == 4 ? N1 : kind == 3 ? N3 : 0, nr = kind == 4 ? 5 + NX : 6;                  \
  const unsigned sStep = (rep >= 5 ? nr : (NREP)) << 15, sU0 = (rep < 3 ? rep : rep >= 5 ? nm + rep + 4 : res) << 15;                 \
  const unsigned sX1 = ((NREP) == 4 ? 128 + rep : 128 + (res + 2) % 5) << 15, sDbg = sDbg0 + (rep >= 5 ? 1024 : 0);                   \
  const unsigned sX2 = ((NREP) == 4 ? 260 + rep : nm ? 260 + (nm + rep + 164) % nr : 260 + (res + 4) % 5) << 15;                      \
  const unsigned sEp = __builtin_amdgcn_readfirstlane(ep), sBuf = kind != 3 ? sEp : (rep < 3 ? 3 : rep < 5 ? 5 : 6) << 15
  constexpr unsigned AUX = 131072;
    if (kind == 6)
    {
      KARG(ws, 10)
      LANE_ID(lr);
      wait_flags(ws + OFF_FXP + 7 * 16384 + wave * 1024, lr, 8, ep);
      const int r8 = lr & 7, seg = lr >> 3;
      const unsigned sEpR = __builtin_amdgcn_readfirstlane(ep), sK = slice, sStep = NRW, sK1 = RT1 + slice, sRq = OFF_FXP + LB * 2048 + wave * 128, sDbg = sDbg0 + (kind == 6 ? 0 : 1024);
      const unsigned vRF = (wave * 8 + (seg >> 2)) * 1024 + ((seg & 3) * 16 + r8) * 16;
      const unsigned vRwr = sLds + (seg == 0 ? r8 * 64 + wave * 4 : 512 + lr * 4), vRrd = sLds + r8 * 64;
      const unsigned vRP = sLds + ((lr >> 4) == ((lr & 15) >> 2) ? r8 * 64 + 16 + wave * 8 + (seg & 1) * 4 : 768 + lr * 4);
      const unsigned vRS = (wave == 0 && seg == 0) ? r8 * 16 : 0x80000000u, vFlag = lr == 0 ? 0u : 0x80000000u, vL4 = (lr & 15) * 128, vRL = wave * 4096 + lr * 16;
      asm volatile(R"(
Lcode_r_b:
s_mov_b64 s[36:37],%0
s_mov_b32 s38,122727552
s_mov_b32 s39,0x00020000
s_mov_b32 s54,%1
s_mov_b32 s50,%2
s_mov_b32 s92,%3
s_mov_b32 s48,%4
v_mov_b32 v0,%5
v_mov_b32 v1,%6
v_mov_b32 v2,%7
v_mov_b32 v3,%8
v_mov_b32 v4,%9
v_mov_b32 v5,%10
v_mov_b32 v6,%11
v_mov_b32 v11,%12
v_lshrrev_b32 v10,1,v4
v_mov_b32 v7,s48
v_mbcnt_lo_u32_b32 v9,-1,0
v_mbcnt_hi_u32_b32 v9,-1,v9
v_lshlrev_b32 v9,4,v9
s_mov_b32 s74,0
s_mov_b32 s96,0
s_mov_b32 s60,536870912
s_mov_b32 s46,0xaaaaaaaa
s_mov_b32 s47,0xaaaaaaaa
s_mov_b32 s56,0xcccccccc
s_mov_b32 s57,0xcccccccc
s_mov_b32 s76,0x3a800000
s_mov_b32 s77,0x3727c5ac
s_mov_b32 s78,0xc0135761
s_mov_b32 s79,0x3d372713
v_mov_b32 v192,0x3f803f80
v_mov_b32 v193,0x3f803f80
v_mov_b32 v194,0x3f803f80
v_mov_b32 v195,0x3f803f80
s_lshr_b32 s63,s54,1
s_and_b32 s64,s54,1
s_lshl_b32 s64,s64,7
s_lshl_b32 s87,s54,14
s_lshl_b32 s58,s63,15
s_add_u32 s58,s58,s64
s_add_u32 s58,s58,16908288
s_lshl_b32 s59,s63,9
s_add_u32 s59,s59,s64
s_add_u32 s59,s59,118556672
s_lshl_b32 s49,s54,9
s_add_u32 s49,s49,s92
s_lshl_b32 s91,s63,11
s_add_u32 s91,s91,121405440
s_lshl_b32 s69,s50,10
s_add_u32 s69,s69,s91
v_mov_b32 v20,0
v_mov_b32 v21,0
v_mov_b32 v22,0
v_mov_b32 v23,0
v_mov_b32 v24,0
v_mov_b32 v25,0
v_mov_b32 v26,0
v_mov_b32 v27,0
v_mov_b32 v16,0x3f80
v_mov_b32 v17,0x3f800000
v_cndmask_b32 v16,v16,v17,s[46:47]
s_mov_b32 s98,0x00300003
s_mov_b32 s99,0x30000300
v_cndmask_b32 v20,v20,v16,s[98:99]
s_lshl_b64 s[98:99],s[98:99],2
v_cndmask_b32 v21,v21,v16,s[98:99]
v_mov_b32 v26,v20
v_mov_b32 v27,v21
v_readfirstlane_b32 s52,v11
s_add_u32 s51,s87,8388608
s_mov_b32 s66,4096
s_nop 3
Lr_yp_%=:
s_add_u32 m0,s66,s52
s_cmp_ge_u32 s51,12582912
s_cbranch_scc1 Lr_ys1_%=
buffer_load_dwordx4 v11,s[36:39],s51 offen offset:0 sc1 lds
buffer_load_dwordx4 v11,s[36:39],s51 offen offset:1024 sc1 lds
buffer_load_dwordx4 v11,s[36:39],s51 offen offset:2048 sc1 lds
buffer_load_dwordx4 v11,s[36:39],s51 offen offset:3072 sc1 lds
s_lshl_b32 s63,s50,14
s_add_u32 s51,s51,s63
Lr_ys1_%=:
s_add_u32 s66,s66,16384
s_cmp_lt_u32 s66,151552
s_cbranch_scc1 Lr_yp_%=
s_mov_b32 s66,4096
v_add_u32 v18,4096,v11
Lr_bp9_%=:
buffer_load_dword v8,v6,s[36:39],s91 offen offset:0 sc1
s_waitcnt vmcnt(0)
v_cmp_eq_u32 vcc,s48,v8
s_nop 0
s_cmp_eq_u64 vcc,exec
s_cselect_b32 s63,1,s96
s_cmp_lg_u32 s63,0
s_cbranch_scc1 Lr_bq9_%=
s_add_u32 s74,s74,1
s_cmp_ge_u32 s74,20000
s_cselect_b32 s96,1,0
s_sleep 4
s_branch Lr_bp9_%=
Lr_bq9_%=:
s_add_u32 s63,s87,71303168
buffer_load_dwordx4 a[0:3],v11,s[36:39],s63 offen offset:0 sc1
buffer_load_dwordx4 a[4:7],v11,s[36:39],s63 offen offset:1024 sc1
buffer_load_dwordx4 a[8:11],v11,s[36:39],s63 offen offset:2048 sc1
buffer_load_dwordx4 a[12:15],v11,s[36:39],s63 offen offset:3072 sc1
s_add_u32 s63,s87,89128960
buffer_load_dwordx4 a[16:19],v11,s[36:39],s63 offen offset:0 sc1
buffer_load_dwordx4 a[20:23],v11,s[36:39],s63 offen offset:1024 sc1
buffer_load_dwordx4 a[24:27],v11,s[36:39],s63 offen offset:2048 sc1
buffer_load_dwordx4 a[28:31],v11,s[36:39],s63 offen offset:3072 sc1
s_add_u32 s63,s87,99614720
buffer_load_dwordx4 a[32:35],v11,s[36:39],s63 offen offset:0 sc1
buffer_load_dwordx4 a[36:39],v11,s[36:39],s63 offen offset:1024 sc1
buffer_load_dwordx4 a[40:43],v11,s[36:39],s63 offen offset:2048 sc1
buffer_load_dwordx4 a[44:47],v11,s[36:39],s63 offen offset:3072 sc1
s_add_u32 s63,s87,109051904
buffer_load_dwordx4 a[48:51],v11,s[36:39],s63 offen offset:0 sc1
buffer_load_dwordx4 a[52:55],v11,s[36:39],s63 offen offset:1024 sc1
buffer_load_dwordx4 a[56:59],v11,s[36:39],s63 offen offset:2048 sc1
buffer_load_dwordx4 a[60:63],v11,s[36:39],s63 offen offset:3072 sc1
buffer_load_dword v8,v6,s[36:39],s69 offen offset:0 sc1
s_waitcnt vmcnt(0)
Lr_it0_%=:
s_mov_b32 s88,s58
s_mov_b32 s89,s59
s_mov_b32 s90,s49
s_add_u32 s54,s54,s50
s_lshr_b32 s63,s54,1
s_and_b32 s64,s54,1
s_lshl_b32 s64,s64,7
s_lshl_b32 s87,s54,14
s_lshl_b32 s58,s63,15
s_add_u32 s58,s58,s64
s_add_u32 s58,s58,16908288
s_lshl_b32 s59,s63,9
s_add_u32 s59,s59,s64
s_add_u32 s59,s59,118556672
s_lshl_b32 s49,s54,9
s_add_u32 s49,s49,s92
s_lshl_b32 s91,s63,11
s_add_u32 s91,s91,121405440
s_lshl_b32 s69,s50,10
s_add_u32 s69,s69,s91
v_cmp_eq_u32 vcc,s48,v8
s_nop 0
s_cmp_eq_u64 vcc,exec
s_cselect_b32 s63,s54,256
s_cmp_lt_u32 s63,256
s_cbranch_scc1 Lr_ta0_%=
ds_read_b128 v[32:35],v18 offset:0
ds_read_b128 v[36:39],v18 offset:1024
ds_read_b128 v[40:43],v18 offset:2048
ds_read_b128 v[44:47],v18 offset:3072
v_mfma_f32_16x16x32_bf16 v[128:131],v[20:23],a[0:3],0
v_mfma_f32_16x16x32_bf16 v[132:135],v[24:27],a[0:3],0
v_mfma_f32_16x16x32_bf16 v[128:131],v[20:23],a[16:19],v[128:131]
v_mfma_f32_16x16x32_bf16 v[132:135],v[24:27],a[16:19],v[132:135]
v_mfma_f32_16x16x32_bf16 v[128:131],v[20:23],a[32:35],v[128:131]
v_mfma_f32_16x16x32_bf16 v[132:135],v[24:27],a[32:35],v[132:135]
v_mfma_f32_16x16x32_bf16 v[128:131],v[20:23],a[48:51],v[128:131]
v_mfma_f32_16x16x32_bf16 v[132:135],v[24:27],a[48:51],v[132:135]
v_mfma_f32_16x16x32_bf16 v[136:139],v[20:23],a[4:7],0
v_mfma_f32_16x16x32_bf16 v[140:143],v[24:27],a[4:7],0
v_mfma_f32_16x16x32_bf16 v[136:139],v[20:23],a[20:23],v[136:139]
v_mfma_f32_16x16x32_bf16 v[140:143],v[24:27],a[20:23],v[140:143]
v_mfma_f32_16x16x32_bf16 v[136:139],v[20:23],a[36:39],v[136:139]
s_nop 1
v_cvt_pk_bf16_f32 v208,v128,v129
v_cvt_pk_bf16_f32 v209,v130,v131
v_mfma_f32_16x16x32_bf16 v[140:143],v[24:27],a[36:39],v[140:143]
v_cvt_pk_bf16_f32 v210,v132,v133
v_cvt_pk_bf16_f32 v211,v134,v135
v_mfma_f32_16x16x32_bf16 v[136:139],v[20:23],a[52:55],v[136:139]
v_mfma_f32_16x16x32_bf16 v[140:143],v[24:27],a[52:55],v[140:143]
v_mfma_f32_16x16x32_bf16 v[144:147],v[20:23],a[8:11],0
v_mfma_f32_16x16x32_bf16 v[148:151],v[24:27],a[8:11],0
v_mfma_f32_16x16x32_bf16 v[144:147],v[20:23],a[24:27],v[144:147]
v_mfma_f32_16x16x32_bf16 v[148:151],v[24:27],a[24:27],v[148:151]
v_mfma_f32_16x16x32_bf16 v[144:147],v[20:23],a[40:43],v[144:147]
s_nop 1
v_cvt_pk_bf16_f32 v212,v136,v137
s_waitcnt vmcnt(0)
buffer_store_dword v7,v5,s[36:39],s60 offen offset:0 sc1
s_mov_b32 s60,536870912
v_cvt_pk_bf16_f32 v213,v138,v139
v_mfma_f32_16x16x32_bf16 v[148:151],v[24:27],a[40:43],v[148:151]
v_cvt_pk_bf16_f32 v214,v140,v141
v_cvt_pk_bf16_f32 v215,v142,v143
v_mfma_f32_16x16x32_bf16 v[144:147],v[20:23],a[56:59],v[144:147]
v_mfma_f32_16x16x32_bf16 v[148:151],v[24:27],a[56:59],v[148:151]
v_mfma_f32_16x16x32_bf16 v[28:31],v[192:195],v[208:211],0
v_mfma_f32_16x16x32_bf16 v[204:207],v[208:211],v[208:211],0
v_mfma_f32_16x16x32_bf16 v[152:155],v[20:23],a[12:15],0
v_mfma_f32_16x16x32_bf16 v[156:159],v[24:27],a[12:15],0
v_mfma_f32_16x16x32_bf16 v[152:155],v[20:23],a[28:31],v[152:155])"
R"(
v_mfma_f32_16x16x32_bf16 v[156:159],v[24:27],a[28:31],v[156:159]
v_mfma_f32_16x16x32_bf16 v[152:155],v[20:23],a[44:47],v[152:155]
v_cvt_pk_bf16_f32 v216,v144,v145
v_cvt_pk_bf16_f32 v217,v146,v147
v_mfma_f32_16x16x32_bf16 v[156:159],v[24:27],a[44:47],v[156:159]
v_cvt_pk_bf16_f32 v218,v148,v149
v_cvt_pk_bf16_f32 v219,v150,v151
v_mfma_f32_16x16x32_bf16 v[152:155],v[20:23],a[60:63],v[152:155]
v_mfma_f32_16x16x32_bf16 v[156:159],v[24:27],a[60:63],v[156:159]
v_mfma_f32_16x16x32_bf16 v[28:31],v[192:195],v[212:215],v[28:31]
v_mfma_f32_16x16x32_bf16 v[204:207],v[212:215],v[212:215],v[204:207]
v_mfma_f32_16x16x32_bf16 v[28:31],v[192:195],v[216:219],v[28:31]
v_mfma_f32_16x16x32_bf16 v[204:207],v[216:219],v[216:219],v[204:207]
s_waitcnt lgkmcnt(0)
v_mfma_f32_16x16x32_bf16 v[160:163],v[20:23],v[32:35],0
s_nop 0
v_cvt_pk_bf16_f32 v220,v152,v153
v_cvt_pk_bf16_f32 v221,v154,v155
v_mfma_f32_16x16x32_bf16 v[164:167],v[24:27],v[32:35],0
v_cvt_pk_bf16_f32 v222,v156,v157
v_cvt_pk_bf16_f32 v223,v158,v159
v_mfma_f32_16x16x32_bf16 v[168:171],v[20:23],v[36:39],0
s_nop 0
v_mfma_f32_16x16x32_bf16 v[28:31],v[192:195],v[220:223],v[28:31]
v_mfma_f32_16x16x32_bf16 v[204:207],v[220:223],v[220:223],v[204:207]
v_mfma_f32_16x16x32_bf16 v[172:175],v[24:27],v[36:39],0
v_mfma_f32_16x16x32_bf16 v[176:179],v[20:23],v[40:43],0
v_mfma_f32_16x16x32_bf16 v[180:183],v[24:27],v[40:43],0
s_nop 3
v_add_f32_dpp v224,v28,v28 row_ror:8 row_mask:0xf bank_mask:0xf
v_cndmask_b32 v225,v204,v205,s[46:47]
v_cndmask_b32 v226,v206,v207,s[46:47]
v_cndmask_b32 v225,v225,v226,s[56:57]
v_mfma_f32_16x16x32_bf16 v[184:187],v[20:23],v[44:47],0
ds_write_b32 v2,v224 offset:0
ds_write_b32 v0,v225 offset:0
v_mfma_f32_16x16x32_bf16 v[188:191],v[24:27],v[44:47],0
s_waitcnt lgkmcnt(0)
s_barrier
ds_read_b128 v[196:199],v3 offset:0
ds_read_b128 v[200:203],v3 offset:16
ds_read_b128 v[204:207],v3 offset:32
s_waitcnt lgkmcnt(0)
v_add_f32 v196,v196,v197
v_add_f32 v198,v198,v199
v_add_f32 v200,v200,v204
v_add_f32 v201,v201,v205
v_add_f32 v202,v202,v206
v_add_f32 v203,v203,v207
v_add_f32 v196,v196,v198
v_add_f32 v200,v200,v201
v_add_f32 v202,v202,v203
v_mul_f32 v196,s76,v196
v_add_f32 v197,v200,v202
v_mul_f32 v197,s76,v197
v_fma_f32 v197,-v196,v196,v197
v_max_f32 v197,0,v197
v_add_f32 v197,s77,v197
v_rsq_f32 v228,v197
s_nop 0
v_mul_f32_e64 v230,-v196,v228
v_mov_b32 v229,v228
v_mov_b32 v231,v230
v_pk_fma_f32 v[196:197],v[128:129],v[228:229],v[230:231]
v_pk_fma_f32 v[198:199],v[130:131],v[228:229],v[230:231]
v_pk_fma_f32 v[200:201],v[132:133],v[228:229],v[230:231]
v_pk_fma_f32 v[202:203],v[134:135],v[228:229],v[230:231]
v_pk_fma_f32 v[128:129],v[196:197],v[160:161],v[128:129]
v_pk_fma_f32 v[130:131],v[198:199],v[162:163],v[130:131]
v_pk_fma_f32 v[132:133],v[200:201],v[164:165],v[132:133]
v_pk_fma_f32 v[134:135],v[202:203],v[166:167],v[134:135]
v_pk_fma_f32 v[196:197],v[136:137],v[228:229],v[230:231]
v_pk_fma_f32 v[198:199],v[138:139],v[228:229],v[230:231]
v_pk_fma_f32 v[200:201],v[140:141],v[228:229],v[230:231]
v_pk_fma_f32 v[202:203],v[142:143],v[228:229],v[230:231]
v_pk_fma_f32 v[136:137],v[196:197],v[168:169],v[136:137]
v_pk_fma_f32 v[138:139],v[198:199],v[170:171],v[138:139]
v_pk_fma_f32 v[140:141],v[200:201],v[172:173],v[140:141]
v_pk_fma_f32 v[142:143],v[202:203],v[174:175],v[142:143]
v_pk_fma_f32 v[196:197],v[144:145],v[228:229],v[230:231]
v_pk_fma_f32 v[198:199],v[146:147],v[228:229],v[230:231]
v_pk_fma_f32 v[200:201],v[148:149],v[228:229],v[230:231]
v_pk_fma_f32 v[202:203],v[150:151],v[228:229],v[230:231]
v_pk_fma_f32 v[144:145],v[196:197],v[176:177],v[144:145]
v_pk_fma_f32 v[146:147],v[198:199],v[178:179],v[146:147]
v_pk_fma_f32 v[148:149],v[200:201],v[180:181],v[148:149]
v_pk_fma_f32 v[150:151],v[202:203],v[182:183],v[150:151]
v_pk_fma_f32 v[196:197],v[152:153],v[228:229],v[230:231]
v_pk_fma_f32 v[198:199],v[154:155],v[228:229],v[230:231]
v_pk_fma_f32 v[200:201],v[156:157],v[228:229],v[230:231]
v_pk_fma_f32 v[202:203],v[158:159],v[228:229],v[230:231]
v_pk_fma_f32 v[152:153],v[196:197],v[184:185],v[152:153]
v_pk_fma_f32 v[154:155],v[198:199],v[186:187],v[154:155]
v_pk_fma_f32 v[156:157],v[200:201],v[188:189],v[156:157]
v_pk_fma_f32 v[158:159],v[202:203],v[190:191],v[158:159]
v_cvt_pk_bf16_f32 v208,v128,v129
v_cvt_pk_bf16_f32 v209,v130,v131
v_cvt_pk_bf16_f32 v210,v132,v133
v_cvt_pk_bf16_f32 v211,v134,v135
v_cvt_pk_bf16_f32 v212,v136,v137
v_cvt_pk_bf16_f32 v213,v138,v139
v_cvt_pk_bf16_f32 v214,v140,v141
v_cvt_pk_bf16_f32 v215,v142,v143
v_mfma_f32_16x16x32_bf16 v[28:31],v[192:195],v[208:211],0
v_cvt_pk_bf16_f32 v216,v144,v145
v_cvt_pk_bf16_f32 v217,v146,v147
v_mfma_f32_16x16x32_bf16 v[204:207],v[208:211],v[208:211],0
v_cvt_pk_bf16_f32 v218,v148,v149
v_cvt_pk_bf16_f32 v219,v150,v151
v_mfma_f32_16x16x32_bf16 v[28:31],v[192:195],v[212:215],v[28:31]
v_cvt_pk_bf16_f32 v220,v152,v153
v_cvt_pk_bf16_f32 v221,v154,v155
v_mfma_f32_16x16x32_bf16 v[204:207],v[212:215],v[212:215],v[204:207]
v_cvt_pk_bf16_f32 v222,v156,v157
v_cvt_pk_bf16_f32 v223,v158,v159
v_mfma_f32_16x16x32_bf16 v[28:31],v[192:195],v[216:219],v[28:31]
v_mfma_f32_16x16x32_bf16 v[204:207],v[216:219],v[216:219],v[204:207]
v_mfma_f32_16x16x32_bf16 v[28:31],v[192:195],v[220:223],v[28:31]
v_mfma_f32_16x16x32_bf16 v[204:207],v[220:223],v[220:223],v[204:207]
buffer_load_dword v8,v6,s[36:39],s91 offen offset:0 sc1
buffer_store_dwordx4 v[208:211],v1,s[36:39],s88 offen offset:0 sc1
buffer_store_dwordx4 v[212:215],v1,s[36:39],s88 offen offset:2048 sc1
s_add_u32 s88,s88,4096
s_nop 2
v_add_f32_dpp v224,v28,v28 row_ror:8 row_mask:0xf bank_mask:0xf
v_cndmask_b32 v225,v204,v205,s[46:47]
v_cndmask_b32 v226,v206,v207,s[46:47]
v_cndmask_b32 v225,v225,v226,s[56:57]
s_and_b32 s63,s89,128
ds_write_b32 v2,v224 offset:1024
ds_write_b32 v0,v225 offset:1024
buffer_store_dwordx4 v[216:219],v1,s[36:39],s88 offen offset:0 sc1
buffer_store_dwordx4 v[220:223],v1,s[36:39],s88 offen offset:2048 sc1
s_waitcnt lgkmcnt(0)
s_barrier
ds_read_b128 v[196:199],v3 offset:1024
ds_read_b128 v[200:203],v3 offset:1040
ds_read_b128 v[204:207],v3 offset:1056
s_waitcnt lgkmcnt(0)
v_add_f32 v196,v196,v197
v_add_f32 v198,v198,v199
v_add_f32 v200,v200,v204
v_add_f32 v201,v201,v205
v_add_f32 v202,v202,v206
v_add_f32 v203,v203,v207
v_add_f32 v196,v196,v198
v_add_f32 v200,v200,v201
v_add_f32 v202,v202,v203
v_mul_f32 v196,s76,v196
v_add_f32 v197,v200,v202
v_mul_f32 v197,s76,v197
v_fma_f32 v197,-v196,v196,v197
v_max_f32 v197,0,v197
v_add_f32 v198,s77,v197
v_fma_f32 v199,s77,v198,v197
v_rsq_f32 v205,v198
v_rsq_f32 v198,v199
v_mov_b32 v200,v196
v_mul_f32_e64 v204,-v196,v205
v_mul_f32 v203,v199,v198
v_mul_f32 v201,s78,v198
v_mul_f32 v199,v198,v198
v_mul_f32 v202,s79,v199
v_mul_f32 v202,v202,v201
s_lshr_b32 s63,s63,1
s_sub_u32 s63,s89,s63
buffer_store_dwordx4 v[200:203],v4,s[36:39],s89 offen offset:0 sc1
buffer_store_dwordx2 v[204:205],v10,s[36:39],s63 offen offset:256 sc1
s_add_u32 m0,s66,s52
s_cmp_ge_u32 s51,12582912
s_cbranch_scc1 Lr_ys2_%=
buffer_load_dwordx4 v11,s[36:39],s51 offen offset:0 sc1 lds
buffer_load_dwordx4 v11,s[36:39],s51 offen offset:1024 sc1 lds
buffer_load_dwordx4 v11,s[36:39],s51 offen offset:2048 sc1 lds
buffer_load_dwordx4 v11,s[36:39],s51 offen offset:3072 sc1 lds
s_lshl_b32 s63,s50,14
s_add_u32 s51,s51,s63
Lr_ys2_%=:
s_add_u32 s66,s66,16384
s_cmp_eq_u32 s66,151552
s_cselect_b32 s66,4096,s66
v_add_u32 v18,s66,v11
s_mov_b32 s60,s90
s_waitcnt vmcnt(6)
v_cmp_eq_u32 vcc,s48,v8
s_nop 0
s_cmp_eq_u64 vcc,exec
s_cselect_b32 s63,s54,256
s_cmp_lt_u32 s63,256
s_cbranch_scc1 Lr_tc0_%=
buffer_load_dword v8,v6,s[36:39],s91 offen offset:0 sc1
s_waitcnt vmcnt(0)
buffer_store_dword v7,v5,s[36:39],s60 offen offset:0 sc1
s_mov_b32 s60,536870912
v_cmp_eq_u32 vcc,s48,v8
s_nop 0
s_cmp_eq_u64 vcc,exec
s_cselect_b32 s63,s54,256
s_cmp_lt_u32 s63,256
s_cbranch_scc1 Lr_bq0_%=)"
R"(
s_cmp_ge_u32 s54,256
s_cbranch_scc1 Lr_done_%=
Lr_bp0_%=:
buffer_load_dword v8,v6,s[36:39],s91 offen offset:0 sc1
s_waitcnt vmcnt(0)
v_cmp_eq_u32 vcc,s48,v8
s_nop 0
s_cmp_eq_u64 vcc,exec
s_cselect_b32 s63,1,s96
s_cmp_lg_u32 s63,0
s_cbranch_scc1 Lr_bq0_%=
s_add_u32 s74,s74,1
s_cmp_ge_u32 s74,20000
s_cselect_b32 s96,1,0
s_sleep 4
s_branch Lr_bp0_%=
Lr_bq0_%=:
s_add_u32 s63,s87,71303168
buffer_load_dwordx4 a[64:67],v11,s[36:39],s63 offen offset:0 sc1
buffer_load_dwordx4 a[68:71],v11,s[36:39],s63 offen offset:1024 sc1
buffer_load_dwordx4 a[72:75],v11,s[36:39],s63 offen offset:2048 sc1
buffer_load_dwordx4 a[76:79],v11,s[36:39],s63 offen offset:3072 sc1
s_add_u32 s63,s87,89128960
buffer_load_dwordx4 a[80:83],v11,s[36:39],s63 offen offset:0 sc1
buffer_load_dwordx4 a[84:87],v11,s[36:39],s63 offen offset:1024 sc1
buffer_load_dwordx4 a[88:91],v11,s[36:39],s63 offen offset:2048 sc1
buffer_load_dwordx4 a[92:95],v11,s[36:39],s63 offen offset:3072 sc1
s_add_u32 s63,s87,99614720
buffer_load_dwordx4 a[96:99],v11,s[36:39],s63 offen offset:0 sc1
buffer_load_dwordx4 a[100:103],v11,s[36:39],s63 offen offset:1024 sc1
buffer_load_dwordx4 a[104:107],v11,s[36:39],s63 offen offset:2048 sc1
buffer_load_dwordx4 a[108:111],v11,s[36:39],s63 offen offset:3072 sc1
s_add_u32 s63,s87,109051904
buffer_load_dwordx4 a[112:115],v11,s[36:39],s63 offen offset:0 sc1
buffer_load_dwordx4 a[116:119],v11,s[36:39],s63 offen offset:1024 sc1
buffer_load_dwordx4 a[120:123],v11,s[36:39],s63 offen offset:2048 sc1
buffer_load_dwordx4 a[124:127],v11,s[36:39],s63 offen offset:3072 sc1
buffer_load_dword v8,v6,s[36:39],s69 offen offset:0 sc1
s_waitcnt vmcnt(0)
s_branch Lr_it1_%=
Lr_ta0_%=:
ds_read_b128 v[32:35],v18 offset:0
ds_read_b128 v[36:39],v18 offset:1024
ds_read_b128 v[40:43],v18 offset:2048
ds_read_b128 v[44:47],v18 offset:3072
v_mfma_f32_16x16x32_bf16 v[128:131],v[20:23],a[0:3],0
s_add_u32 s63,s87,71303168
v_mfma_f32_16x16x32_bf16 v[132:135],v[24:27],a[0:3],0
buffer_load_dwordx4 a[64:67],v11,s[36:39],s63 offen offset:0 sc1
v_mfma_f32_16x16x32_bf16 v[128:131],v[20:23],a[16:19],v[128:131]
v_mfma_f32_16x16x32_bf16 v[132:135],v[24:27],a[16:19],v[132:135]
buffer_load_dwordx4 a[68:71],v11,s[36:39],s63 offen offset:1024 sc1
v_mfma_f32_16x16x32_bf16 v[128:131],v[20:23],a[32:35],v[128:131]
v_mfma_f32_16x16x32_bf16 v[132:135],v[24:27],a[32:35],v[132:135]
v_mfma_f32_16x16x32_bf16 v[128:131],v[20:23],a[48:51],v[128:131]
v_mfma_f32_16x16x32_bf16 v[132:135],v[24:27],a[48:51],v[132:135]
buffer_load_dwordx4 a[72:75],v11,s[36:39],s63 offen offset:2048 sc1
v_mfma_f32_16x16x32_bf16 v[136:139],v[20:23],a[4:7],0
v_mfma_f32_16x16x32_bf16 v[140:143],v[24:27],a[4:7],0
v_mfma_f32_16x16x32_bf16 v[136:139],v[20:23],a[20:23],v[136:139]
v_mfma_f32_16x16x32_bf16 v[140:143],v[24:27],a[20:23],v[140:143]
buffer_load_dwordx4 a[76:79],v11,s[36:39],s63 offen offset:3072 sc1
v_mfma_f32_16x16x32_bf16 v[136:139],v[20:23],a[36:39],v[136:139]
v_cvt_pk_bf16_f32 v208,v128,v129
v_cvt_pk_bf16_f32 v209,v130,v131
v_mfma_f32_16x16x32_bf16 v[140:143],v[24:27],a[36:39],v[140:143]
v_cvt_pk_bf16_f32 v210,v132,v133
v_cvt_pk_bf16_f32 v211,v134,v135
v_mfma_f32_16x16x32_bf16 v[136:139],v[20:23],a[52:55],v[136:139]
s_add_u32 s63,s87,89128960
v_mfma_f32_16x16x32_bf16 v[140:143],v[24:27],a[52:55],v[140:143]
buffer_load_dwordx4 a[80:83],v11,s[36:39],s63 offen offset:0 sc1
v_mfma_f32_16x16x32_bf16 v[144:147],v[20:23],a[8:11],0
v_mfma_f32_16x16x32_bf16 v[148:151],v[24:27],a[8:11],0
v_mfma_f32_16x16x32_bf16 v[144:147],v[20:23],a[24:27],v[144:147]
v_mfma_f32_16x16x32_bf16 v[148:151],v[24:27],a[24:27],v[148:151]
buffer_load_dwordx4 a[84:87],v11,s[36:39],s63 offen offset:1024 sc1
v_mfma_f32_16x16x32_bf16 v[144:147],v[20:23],a[40:43],v[144:147]
v_cvt_pk_bf16_f32 v212,v136,v137
v_cvt_pk_bf16_f32 v213,v138,v139
v_mfma_f32_16x16x32_bf16 v[148:151],v[24:27],a[40:43],v[148:151]
v_cvt_pk_bf16_f32 v214,v140,v141
v_cvt_pk_bf16_f32 v215,v142,v143
v_mfma_f32_16x16x32_bf16 v[144:147],v[20:23],a[56:59],v[144:147]
v_mfma_f32_16x16x32_bf16 v[148:151],v[24:27],a[56:59],v[148:151]
s_waitcnt vmcnt(6)
buffer_store_dword v7,v5,s[36:39],s60 offen offset:0 sc1
s_mov_b32 s60,536870912
buffer_load_dwordx4 a[88:91],v11,s[36:39],s63 offen offset:2048 sc1
v_mfma_f32_16x16x32_bf16 v[28:31],v[192:195],v[208:211],0
v_mfma_f32_16x16x32_bf16 v[204:207],v[208:211],v[208:211],0
v_mfma_f32_16x16x32_bf16 v[152:155],v[20:23],a[12:15],0
v_mfma_f32_16x16x32_bf16 v[156:159],v[24:27],a[12:15],0
buffer_load_dwordx4 a[92:95],v11,s[36:39],s63 offen offset:3072 sc1
v_mfma_f32_16x16x32_bf16 v[152:155],v[20:23],a[28:31],v[152:155]
s_add_u32 s63,s87,99614720
v_mfma_f32_16x16x32_bf16 v[156:159],v[24:27],a[28:31],v[156:159]
v_mfma_f32_16x16x32_bf16 v[152:155],v[20:23],a[44:47],v[152:155]
v_cvt_pk_bf16_f32 v216,v144,v145
v_cvt_pk_bf16_f32 v217,v146,v147
v_mfma_f32_16x16x32_bf16 v[156:159],v[24:27],a[44:47],v[156:159]
v_cvt_pk_bf16_f32 v218,v148,v149
v_cvt_pk_bf16_f32 v219,v150,v151
v_mfma_f32_16x16x32_bf16 v[152:155],v[20:23],a[60:63],v[152:155]
buffer_load_dwordx4 a[96:99],v11,s[36:39],s63 offen offset:0 sc1
v_mfma_f32_16x16x32_bf16 v[156:159],v[24:27],a[60:63],v[156:159]
v_mfma_f32_16x16x32_bf16 v[28:31],v[192:195],v[212:215],v[28:31]
v_mfma_f32_16x16x32_bf16 v[204:207],v[212:215],v[212:215],v[204:207]
buffer_load_dwordx4 a[100:103],v11,s[36:39],s63 offen offset:1024 sc1
v_mfma_f32_16x16x32_bf16 v[28:31],v[192:195],v[216:219],v[28:31]
v_mfma_f32_16x16x32_bf16 v[204:207],v[216:219],v[216:219],v[204:207]
s_waitcnt lgkmcnt(0)
v_mfma_f32_16x16x32_bf16 v[160:163],v[20:23],v[32:35],0
v_cvt_pk_bf16_f32 v220,v152,v153
v_cvt_pk_bf16_f32 v221,v154,v155
v_mfma_f32_16x16x32_bf16 v[164:167],v[24:27],v[32:35],0
v_cvt_pk_bf16_f32 v222,v156,v157
v_cvt_pk_bf16_f32 v223,v158,v159
v_mfma_f32_16x16x32_bf16 v[168:171],v[20:23],v[36:39],0
buffer_load_dwordx4 a[104:107],v11,s[36:39],s63 offen offset:2048 sc1
v_mfma_f32_16x16x32_bf16 v[28:31],v[192:195],v[220:223],v[28:31]
buffer_load_dwordx4 a[108:111],v11,s[36:39],s63 offen offset:3072 sc1
v_mfma_f32_16x16x32_bf16 v[204:207],v[220:223],v[220:223],v[204:207]
s_add_u32 s63,s87,109051904
v_mfma_f32_16x16x32_bf16 v[172:175],v[24:27],v[36:39],0
v_mfma_f32_16x16x32_bf16 v[176:179],v[20:23],v[40:43],0
buffer_load_dwordx4 a[112:115],v11,s[36:39],s63 offen offset:0 sc1
v_mfma_f32_16x16x32_bf16 v[180:183],v[24:27],v[40:43],0
s_nop 0
v_add_f32_dpp v224,v28,v28 row_ror:8 row_mask:0xf bank_mask:0xf
s_nop 0
v_cndmask_b32 v225,v204,v205,s[46:47]
v_cndmask_b32 v226,v206,v207,s[46:47]
v_cndmask_b32 v225,v225,v226,s[56:57]
v_mfma_f32_16x16x32_bf16 v[184:187],v[20:23],v[44:47],0
ds_write_b32 v2,v224 offset:0
ds_write_b32 v0,v225 offset:0
v_mfma_f32_16x16x32_bf16 v[188:191],v[24:27],v[44:47],0
s_waitcnt lgkmcnt(0)
buffer_load_dwordx4 a[116:119],v11,s[36:39],s63 offen offset:1024 sc1
s_barrier
buffer_load_dwordx4 a[120:123],v11,s[36:39],s63 offen offset:2048 sc1
ds_read_b128 v[196:199],v3 offset:0
buffer_load_dwordx4 a[124:127],v11,s[36:39],s63 offen offset:3072 sc1
ds_read_b128 v[200:203],v3 offset:16
ds_read_b128 v[204:207],v3 offset:32
s_waitcnt lgkmcnt(0)
buffer_load_dword v8,v6,s[36:39],s69 offen offset:0 sc1
v_add_f32 v196,v196,v197
v_add_f32 v198,v198,v199
v_add_f32 v200,v200,v204
v_add_f32 v201,v201,v205
v_add_f32 v202,v202,v206
v_add_f32 v203,v203,v207
v_add_f32 v196,v196,v198
v_add_f32 v200,v200,v201
v_add_f32 v202,v202,v203
v_mul_f32 v196,s76,v196
v_add_f32 v197,v200,v202
v_mul_f32 v197,s76,v197
v_fma_f32 v197,-v196,v196,v197
v_max_f32 v197,0,v197
v_add_f32 v197,s77,v197
v_rsq_f32 v228,v197
s_nop 0
v_mul_f32_e64 v230,-v196,v228
v_mov_b32 v229,v228
v_mov_b32 v231,v230
v_pk_fma_f32 v[196:197],v[128:129],v[228:229],v[230:231]
v_pk_fma_f32 v[198:199],v[130:131],v[228:229],v[230:231]
v_pk_fma_f32 v[200:201],v[132:133],v[228:229],v[230:231]
v_pk_fma_f32 v[202:203],v[134:135],v[228:229],v[230:231]
v_pk_fma_f32 v[128:129],v[196:197],v[160:161],v[128:129]
v_pk_fma_f32 v[130:131],v[198:199],v[162:163],v[130:131]
v_pk_fma_f32 v[132:133],v[200:201],v[164:165],v[132:133]
v_pk_fma_f32 v[134:135],v[202:203],v[166:167],v[134:135]
v_pk_fma_f32 v[196:197],v[136:137],v[228:229],v[230:231]
v_pk_fma_f32 v[198:199],v[138:139],v[228:229],v[230:231]
v_pk_fma_f32 v[200:201],v[140:141],v[228:229],v[230:231]
v_pk_fma_f32 v[202:203],v[142:143],v[228:229],v[230:231]
v_pk_fma_f32 v[136:137],v[196:197],v[168:169],v[136:137]
v_pk_fma_f32 v[138:139],v[198:199],v[170:171],v[138:139]
v_pk_fma_f32 v[140:141],v[200:201],v[172:173],v[140:141]
v_pk_fma_f32 v[142:143],v[202:203],v[174:175],v[142:143]
v_pk_fma_f32 v[196:197],v[144:145],v[228:229],v[230:231]
v_pk_fma_f32 v[198:199],v[146:147],v[228:229],v[230:231]
v_pk_fma_f32 v[200:201],v[148:149],v[228:229],v[230:231]
v_pk_fma_f32 v[202:203],v[150:151],v[228:229],v[230:231]
v_pk_fma_f32 v[144:145],v[196:197],v[176:177],v[144:145]
v_pk_fma_f32 v[146:147],v[198:199],v[178:179],v[146:147]
v_pk_fma_f32 v[148:149],v[200:201],v[180:181],v[148:149]
v_pk_fma_f32 v[150:151],v[202:203],v[182:183],v[150:151]
v_pk_fma_f32 v[196:197],v[152:153],v[228:229],v[230:231]
v_pk_fma_f32 v[198:199],v[154:155],v[228:229],v[230:231]
v_pk_fma_f32 v[200:201],v[156:157],v[228:229],v[230:231]
v_pk_fma_f32 v[202:203],v[158:159],v[228:229],v[230:231]
v_pk_fma_f32 v[152:153],v[196:197],v[184:185],v[152:153]
v_pk_fma_f32 v[154:155],v[198:199],v[186:187],v[154:155]
v_pk_fma_f32 v[156:157],v[200:201],v[188:189],v[156:157]
v_pk_fma_f32 v[158:159],v[202:203],v[190:191],v[158:159])"
R"(
v_cvt_pk_bf16_f32 v208,v128,v129
v_cvt_pk_bf16_f32 v209,v130,v131
v_cvt_pk_bf16_f32 v210,v132,v133
v_cvt_pk_bf16_f32 v211,v134,v135
v_cvt_pk_bf16_f32 v212,v136,v137
v_cvt_pk_bf16_f32 v213,v138,v139
v_cvt_pk_bf16_f32 v214,v140,v141
v_cvt_pk_bf16_f32 v215,v142,v143
v_mfma_f32_16x16x32_bf16 v[28:31],v[192:195],v[208:211],0
v_cvt_pk_bf16_f32 v216,v144,v145
v_cvt_pk_bf16_f32 v217,v146,v147
v_mfma_f32_16x16x32_bf16 v[204:207],v[208:211],v[208:211],0
v_cvt_pk_bf16_f32 v218,v148,v149
v_cvt_pk_bf16_f32 v219,v150,v151
v_mfma_f32_16x16x32_bf16 v[28:31],v[192:195],v[212:215],v[28:31]
v_cvt_pk_bf16_f32 v220,v152,v153
v_cvt_pk_bf16_f32 v221,v154,v155
v_mfma_f32_16x16x32_bf16 v[204:207],v[212:215],v[212:215],v[204:207]
v_cvt_pk_bf16_f32 v222,v156,v157
v_cvt_pk_bf16_f32 v223,v158,v159
v_mfma_f32_16x16x32_bf16 v[28:31],v[192:195],v[216:219],v[28:31]
v_mfma_f32_16x16x32_bf16 v[204:207],v[216:219],v[216:219],v[204:207]
v_mfma_f32_16x16x32_bf16 v[28:31],v[192:195],v[220:223],v[28:31]
v_mfma_f32_16x16x32_bf16 v[204:207],v[220:223],v[220:223],v[204:207]
buffer_store_dwordx4 v[208:211],v1,s[36:39],s88 offen offset:0 sc1
buffer_store_dwordx4 v[212:215],v1,s[36:39],s88 offen offset:2048 sc1
s_add_u32 s88,s88,4096
s_nop 3
v_add_f32_dpp v224,v28,v28 row_ror:8 row_mask:0xf bank_mask:0xf
v_cndmask_b32 v225,v204,v205,s[46:47]
v_cndmask_b32 v226,v206,v207,s[46:47]
v_cndmask_b32 v225,v225,v226,s[56:57]
s_and_b32 s63,s89,128
ds_write_b32 v2,v224 offset:1024
ds_write_b32 v0,v225 offset:1024
buffer_store_dwordx4 v[216:219],v1,s[36:39],s88 offen offset:0 sc1
buffer_store_dwordx4 v[220:223],v1,s[36:39],s88 offen offset:2048 sc1
s_waitcnt lgkmcnt(0)
s_barrier
ds_read_b128 v[196:199],v3 offset:1024
ds_read_b128 v[200:203],v3 offset:1040
ds_read_b128 v[204:207],v3 offset:1056
s_waitcnt lgkmcnt(0)
v_add_f32 v196,v196,v197
v_add_f32 v198,v198,v199
v_add_f32 v200,v200,v204
v_add_f32 v201,v201,v205
v_add_f32 v202,v202,v206
v_add_f32 v203,v203,v207
v_add_f32 v196,v196,v198
v_add_f32 v200,v200,v201
v_add_f32 v202,v202,v203
v_mul_f32 v196,s76,v196
v_add_f32 v197,v200,v202
v_mul_f32 v197,s76,v197
v_fma_f32 v197,-v196,v196,v197
v_max_f32 v197,0,v197
v_add_f32 v198,s77,v197
v_fma_f32 v199,s77,v198,v197
v_rsq_f32 v205,v198
v_rsq_f32 v198,v199
v_mov_b32 v200,v196
v_mul_f32_e64 v204,-v196,v205
v_mul_f32 v203,v199,v198
v_mul_f32 v201,s78,v198
v_mul_f32 v199,v198,v198
v_mul_f32 v202,s79,v199
v_mul_f32 v202,v202,v201
s_lshr_b32 s63,s63,1
s_sub_u32 s63,s89,s63
buffer_store_dwordx4 v[200:203],v4,s[36:39],s89 offen offset:0 sc1
buffer_store_dwordx2 v[204:205],v10,s[36:39],s63 offen offset:256 sc1
s_add_u32 m0,s66,s52
s_cmp_ge_u32 s51,12582912
s_cbranch_scc1 Lr_ys3_%=
buffer_load_dwordx4 v11,s[36:39],s51 offen offset:0 sc1 lds
buffer_load_dwordx4 v11,s[36:39],s51 offen offset:1024 sc1 lds
buffer_load_dwordx4 v11,s[36:39],s51 offen offset:2048 sc1 lds
buffer_load_dwordx4 v11,s[36:39],s51 offen offset:3072 sc1 lds
s_lshl_b32 s63,s50,14
s_add_u32 s51,s51,s63
Lr_ys3_%=:
s_add_u32 s66,s66,16384
s_cmp_eq_u32 s66,151552
s_cselect_b32 s66,4096,s66
v_add_u32 v18,s66,v11
s_mov_b32 s60,s90
s_waitcnt vmcnt(6)
s_branch Lr_it1_%=
Lr_tc0_%=:
s_add_u32 s63,s87,71303168
buffer_load_dwordx4 a[64:67],v11,s[36:39],s63 offen offset:0 sc1
buffer_load_dwordx4 a[68:71],v11,s[36:39],s63 offen offset:1024 sc1
buffer_load_dwordx4 a[72:75],v11,s[36:39],s63 offen offset:2048 sc1
buffer_load_dwordx4 a[76:79],v11,s[36:39],s63 offen offset:3072 sc1
s_add_u32 s63,s87,89128960
buffer_load_dwordx4 a[80:83],v11,s[36:39],s63 offen offset:0 sc1
buffer_load_dwordx4 a[84:87],v11,s[36:39],s63 offen offset:1024 sc1
buffer_load_dwordx4 a[88:91],v11,s[36:39],s63 offen offset:2048 sc1
buffer_load_dwordx4 a[92:95],v11,s[36:39],s63 offen offset:3072 sc1
s_add_u32 s63,s87,99614720
buffer_load_dwordx4 a[96:99],v11,s[36:39],s63 offen offset:0 sc1
buffer_load_dwordx4 a[100:103],v11,s[36:39],s63 offen offset:1024 sc1
buffer_load_dwordx4 a[104:107],v11,s[36:39],s63 offen offset:2048 sc1
buffer_load_dwordx4 a[108:111],v11,s[36:39],s63 offen offset:3072 sc1
s_add_u32 s63,s87,109051904
buffer_load_dwordx4 a[112:115],v11,s[36:39],s63 offen offset:0 sc1
buffer_load_dwordx4 a[116:119],v11,s[36:39],s63 offen offset:1024 sc1
buffer_load_dwordx4 a[120:123],v11,s[36:39],s63 offen offset:2048 sc1
buffer_load_dwordx4 a[124:127],v11,s[36:39],s63 offen offset:3072 sc1
buffer_load_dword v8,v6,s[36:39],s69 offen offset:0 sc1
s_waitcnt vmcnt(17)
buffer_store_dword v7,v5,s[36:39],s60 offen offset:0 sc1
s_mov_b32 s60,536870912
s_waitcnt vmcnt(1)
s_branch Lr_it1_%=
Lr_it1_%=:
s_mov_b32 s88,s58
s_mov_b32 s89,s59
s_mov_b32 s90,s49
s_add_u32 s54,s54,s50
s_lshr_b32 s63,s54,1
s_and_b32 s64,s54,1
s_lshl_b32 s64,s64,7
s_lshl_b32 s87,s54,14
s_lshl_b32 s58,s63,15
s_add_u32 s58,s58,s64
s_add_u32 s58,s58,16908288
s_lshl_b32 s59,s63,9
s_add_u32 s59,s59,s64
s_add_u32 s59,s59,118556672
s_lshl_b32 s49,s54,9
s_add_u32 s49,s49,s92
s_lshl_b32 s91,s63,11
s_add_u32 s91,s91,121405440
s_lshl_b32 s69,s50,10
s_add_u32 s69,s69,s91
v_cmp_eq_u32 vcc,s48,v8
s_nop 0
s_cmp_eq_u64 vcc,exec
s_cselect_b32 s63,s54,256
s_cmp_lt_u32 s63,256
s_cbranch_scc1 Lr_ta1_%=
ds_read_b128 v[32:35],v18 offset:0
ds_read_b128 v[36:39],v18 offset:1024
ds_read_b128 v[40:43],v18 offset:2048
ds_read_b128 v[44:47],v18 offset:3072
v_mfma_f32_16x16x32_bf16 v[128:131],v[20:23],a[64:67],0
v_mfma_f32_16x16x32_bf16 v[132:135],v[24:27],a[64:67],0
v_mfma_f32_16x16x32_bf16 v[128:131],v[20:23],a[80:83],v[128:131]
v_mfma_f32_16x16x32_bf16 v[132:135],v[24:27],a[80:83],v[132:135]
v_mfma_f32_16x16x32_bf16 v[128:131],v[20:23],a[96:99],v[128:131]
v_mfma_f32_16x16x32_bf16 v[132:135],v[24:27],a[96:99],v[132:135]
v_mfma_f32_16x16x32_bf16 v[128:131],v[20:23],a[112:115],v[128:131]
v_mfma_f32_16x16x32_bf16 v[132:135],v[24:27],a[112:115],v[132:135]
v_mfma_f32_16x16x32_bf16 v[136:139],v[20:23],a[68:71],0
v_mfma_f32_16x16x32_bf16 v[140:143],v[24:27],a[68:71],0
v_mfma_f32_16x16x32_bf16 v[136:139],v[20:23],a[84:87],v[136:139]
v_mfma_f32_16x16x32_bf16 v[140:143],v[24:27],a[84:87],v[140:143]
v_mfma_f32_16x16x32_bf16 v[136:139],v[20:23],a[100:103],v[136:139]
s_nop 1
v_cvt_pk_bf16_f32 v208,v128,v129
v_cvt_pk_bf16_f32 v209,v130,v131
v_mfma_f32_16x16x32_bf16 v[140:143],v[24:27],a[100:103],v[140:143]
v_cvt_pk_bf16_f32 v210,v132,v133
v_cvt_pk_bf16_f32 v211,v134,v135
v_mfma_f32_16x16x32_bf16 v[136:139],v[20:23],a[116:119],v[136:139]
v_mfma_f32_16x16x32_bf16 v[140:143],v[24:27],a[116:119],v[140:143]
v_mfma_f32_16x16x32_bf16 v[144:147],v[20:23],a[72:75],0
v_mfma_f32_16x16x32_bf16 v[148:151],v[24:27],a[72:75],0
v_mfma_f32_16x16x32_bf16 v[144:147],v[20:23],a[88:91],v[144:147]
v_mfma_f32_16x16x32_bf16 v[148:151],v[24:27],a[88:91],v[148:151]
v_mfma_f32_16x16x32_bf16 v[144:147],v[20:23],a[104:107],v[144:147]
s_nop 1
v_cvt_pk_bf16_f32 v212,v136,v137
s_waitcnt vmcnt(0)
buffer_store_dword v7,v5,s[36:39],s60 offen offset:0 sc1
s_mov_b32 s60,536870912
v_cvt_pk_bf16_f32 v213,v138,v139
v_mfma_f32_16x16x32_bf16 v[148:151],v[24:27],a[104:107],v[148:151]
v_cvt_pk_bf16_f32 v214,v140,v141
v_cvt_pk_bf16_f32 v215,v142,v143
v_mfma_f32_16x16x32_bf16 v[144:147],v[20:23],a[120:123],v[144:147]
v_mfma_f32_16x16x32_bf16 v[148:151],v[24:27],a[120:123],v[148:151]
v_mfma_f32_16x16x32_bf16 v[28:31],v[192:195],v[208:211],0
v_mfma_f32_16x16x32_bf16 v[204:207],v[208:211],v[208:211],0
v_mfma_f32_16x16x32_bf16 v[152:155],v[20:23],a[76:79],0
v_mfma_f32_16x16x32_bf16 v[156:159],v[24:27],a[76:79],0
v_mfma_f32_16x16x32_bf16 v[152:155],v[20:23],a[92:95],v[152:155]
v_mfma_f32_16x16x32_bf16 v[156:159],v[24:27],a[92:95],v[156:159]
v_mfma_f32_16x16x32_bf16 v[152:155],v[20:23],a[108:111],v[152:155]
v_cvt_pk_bf16_f32 v216,v144,v145
v_cvt_pk_bf16_f32 v217,v146,v147
v_mfma_f32_16x16x32_bf16 v[156:159],v[24:27],a[108:111],v[156:159]
v_cvt_pk_bf16_f32 v218,v148,v149
v_cvt_pk_bf16_f32 v219,v150,v151
v_mfma_f32_16x16x32_bf16 v[152:155],v[20:23],a[124:127],v[152:155]
v_mfma_f32_16x16x32_bf16 v[156:159],v[24:27],a[124:127],v[156:159]
v_mfma_f32_16x16x32_bf16 v[28:31],v[192:195],v[212:215],v[28:31]
v_mfma_f32_16x16x32_bf16 v[204:207],v[212:215],v[212:215],v[204:207])"
R"(
v_mfma_f32_16x16x32_bf16 v[28:31],v[192:195],v[216:219],v[28:31]
v_mfma_f32_16x16x32_bf16 v[204:207],v[216:219],v[216:219],v[204:207]
s_waitcnt lgkmcnt(0)
v_mfma_f32_16x16x32_bf16 v[160:163],v[20:23],v[32:35],0
s_nop 0
v_cvt_pk_bf16_f32 v220,v152,v153
v_cvt_pk_bf16_f32 v221,v154,v155
v_mfma_f32_16x16x32_bf16 v[164:167],v[24:27],v[32:35],0
v_cvt_pk_bf16_f32 v222,v156,v157
v_cvt_pk_bf16_f32 v223,v158,v159
v_mfma_f32_16x16x32_bf16 v[168:171],v[20:23],v[36:39],0
s_nop 0
v_mfma_f32_16x16x32_bf16 v[28:31],v[192:195],v[220:223],v[28:31]
v_mfma_f32_16x16x32_bf16 v[204:207],v[220:223],v[220:223],v[204:207]
v_mfma_f32_16x16x32_bf16 v[172:175],v[24:27],v[36:39],0
v_mfma_f32_16x16x32_bf16 v[176:179],v[20:23],v[40:43],0
v_mfma_f32_16x16x32_bf16 v[180:183],v[24:27],v[40:43],0
s_nop 3
v_add_f32_dpp v224,v28,v28 row_ror:8 row_mask:0xf bank_mask:0xf
v_cndmask_b32 v225,v204,v205,s[46:47]
v_cndmask_b32 v226,v206,v207,s[46:47]
v_cndmask_b32 v225,v225,v226,s[56:57]
v_mfma_f32_16x16x32_bf16 v[184:187],v[20:23],v[44:47],0
ds_write_b32 v2,v224 offset:0
ds_write_b32 v0,v225 offset:0
v_mfma_f32_16x16x32_bf16 v[188:191],v[24:27],v[44:47],0
s_waitcnt lgkmcnt(0)
s_barrier
ds_read_b128 v[196:199],v3 offset:0
ds_read_b128 v[200:203],v3 offset:16
ds_read_b128 v[204:207],v3 offset:32
s_waitcnt lgkmcnt(0)
v_add_f32 v196,v196,v197
v_add_f32 v198,v198,v199
v_add_f32 v200,v200,v204
v_add_f32 v201,v201,v205
v_add_f32 v202,v202,v206
v_add_f32 v203,v203,v207
v_add_f32 v196,v196,v198
v_add_f32 v200,v200,v201
v_add_f32 v202,v202,v203
v_mul_f32 v196,s76,v196
v_add_f32 v197,v200,v202
v_mul_f32 v197,s76,v197
v_fma_f32 v197,-v196,v196,v197
v_max_f32 v197,0,v197
v_add_f32 v197,s77,v197
v_rsq_f32 v228,v197
s_nop 0
v_mul_f32_e64 v230,-v196,v228
v_mov_b32 v229,v228
v_mov_b32 v231,v230
v_pk_fma_f32 v[196:197],v[128:129],v[228:229],v[230:231]
v_pk_fma_f32 v[198:199],v[130:131],v[228:229],v[230:231]
v_pk_fma_f32 v[200:201],v[132:133],v[228:229],v[230:231]
v_pk_fma_f32 v[202:203],v[134:135],v[228:229],v[230:231]
v_pk_fma_f32 v[128:129],v[196:197],v[160:161],v[128:129]
v_pk_fma_f32 v[130:131],v[198:199],v[162:163],v[130:131]
v_pk_fma_f32 v[132:133],v[200:201],v[164:165],v[132:133]
v_pk_fma_f32 v[134:135],v[202:203],v[166:167],v[134:135]
v_pk_fma_f32 v[196:197],v[136:137],v[228:229],v[230:231]
v_pk_fma_f32 v[198:199],v[138:139],v[228:229],v[230:231]
v_pk_fma_f32 v[200:201],v[140:141],v[228:229],v[230:231]
v_pk_fma_f32 v[202:203],v[142:143],v[228:229],v[230:231]
v_pk_fma_f32 v[136:137],v[196:197],v[168:169],v[136:137]
v_pk_fma_f32 v[138:139],v[198:199],v[170:171],v[138:139]
v_pk_fma_f32 v[140:141],v[200:201],v[172:173],v[140:141]
v_pk_fma_f32 v[142:143],v[202:203],v[174:175],v[142:143]
v_pk_fma_f32 v[196:197],v[144:145],v[228:229],v[230:231]
v_pk_fma_f32 v[198:199],v[146:147],v[228:229],v[230:231]
v_pk_fma_f32 v[200:201],v[148:149],v[228:229],v[230:231]
v_pk_fma_f32 v[202:203],v[150:151],v[228:229],v[230:231]
v_pk_fma_f32 v[144:145],v[196:197],v[176:177],v[144:145]
v_pk_fma_f32 v[146:147],v[198:199],v[178:179],v[146:147]
v_pk_fma_f32 v[148:149],v[200:201],v[180:181],v[148:149]
v_pk_fma_f32 v[150:151],v[202:203],v[182:183],v[150:151]
v_pk_fma_f32 v[196:197],v[152:153],v[228:229],v[230:231]
v_pk_fma_f32 v[198:199],v[154:155],v[228:229],v[230:231]
v_pk_fma_f32 v[200:201],v[156:157],v[228:229],v[230:231]
v_pk_fma_f32 v[202:203],v[158:159],v[228:229],v[230:231]
v_pk_fma_f32 v[152:153],v[196:197],v[184:185],v[152:153]
v_pk_fma_f32 v[154:155],v[198:199],v[186:187],v[154:155]
v_pk_fma_f32 v[156:157],v[200:201],v[188:189],v[156:157]
v_pk_fma_f32 v[158:159],v[202:203],v[190:191],v[158:159]
v_cvt_pk_bf16_f32 v208,v128,v129
v_cvt_pk_bf16_f32 v209,v130,v131
v_cvt_pk_bf16_f32 v210,v132,v133
v_cvt_pk_bf16_f32 v211,v134,v135
v_cvt_pk_bf16_f32 v212,v136,v137
v_cvt_pk_bf16_f32 v213,v138,v139
v_cvt_pk_bf16_f32 v214,v140,v141
v_cvt_pk_bf16_f32 v215,v142,v143
v_mfma_f32_16x16x32_bf16 v[28:31],v[192:195],v[208:211],0
v_cvt_pk_bf16_f32 v216,v144,v145
v_cvt_pk_bf16_f32 v217,v146,v147
v_mfma_f32_16x16x32_bf16 v[204:207],v[208:211],v[208:211],0
v_cvt_pk_bf16_f32 v218,v148,v149
v_cvt_pk_bf16_f32 v219,v150,v151
v_mfma_f32_16x16x32_bf16 v[28:31],v[192:195],v[212:215],v[28:31]
v_cvt_pk_bf16_f32 v220,v152,v153
v_cvt_pk_bf16_f32 v221,v154,v155
v_mfma_f32_16x16x32_bf16 v[204:207],v[212:215],v[212:215],v[204:207]
v_cvt_pk_bf16_f32 v222,v156,v157
v_cvt_pk_bf16_f32 v223,v158,v159
v_mfma_f32_16x16x32_bf16 v[28:31],v[192:195],v[216:219],v[28:31]
v_mfma_f32_16x16x32_bf16 v[204:207],v[216:219],v[216:219],v[204:207]
v_mfma_f32_16x16x32_bf16 v[28:31],v[192:195],v[220:223],v[28:31]
v_mfma_f32_16x16x32_bf16 v[204:207],v[220:223],v[220:223],v[204:207]
buffer_load_dword v8,v6,s[36:39],s91 offen offset:0 sc1
buffer_store_dwordx4 v[208:211],v1,s[36:39],s88 offen offset:0 sc1
buffer_store_dwordx4 v[212:215],v1,s[36:39],s88 offen offset:2048 sc1
s_add_u32 s88,s88,4096
s_nop 2
v_add_f32_dpp v224,v28,v28 row_ror:8 row_mask:0xf bank_mask:0xf
v_cndmask_b32 v225,v204,v205,s[46:47]
v_cndmask_b32 v226,v206,v207,s[46:47]
v_cndmask_b32 v225,v225,v226,s[56:57]
s_and_b32 s63,s89,128
ds_write_b32 v2,v224 offset:1024
ds_write_b32 v0,v225 offset:1024
buffer_store_dwordx4 v[216:219],v1,s[36:39],s88 offen offset:0 sc1
buffer_store_dwordx4 v[220:223],v1,s[36:39],s88 offen offset:2048 sc1
s_waitcnt lgkmcnt(0)
s_barrier
ds_read_b128 v[196:199],v3 offset:1024
ds_read_b128 v[200:203],v3 offset:1040
ds_read_b128 v[204:207],v3 offset:1056
s_waitcnt lgkmcnt(0)
v_add_f32 v196,v196,v197
v_add_f32 v198,v198,v199
v_add_f32 v200,v200,v204
v_add_f32 v201,v201,v205
v_add_f32 v202,v202,v206
v_add_f32 v203,v203,v207
v_add_f32 v196,v196,v198
v_add_f32 v200,v200,v201
v_add_f32 v202,v202,v203
v_mul_f32 v196,s76,v196
v_add_f32 v197,v200,v202
v_mul_f32 v197,s76,v197
v_fma_f32 v197,-v196,v196,v197
v_max_f32 v197,0,v197
v_add_f32 v198,s77,v197
v_fma_f32 v199,s77,v198,v197
v_rsq_f32 v205,v198
v_rsq_f32 v198,v199
v_mov_b32 v200,v196
v_mul_f32_e64 v204,-v196,v205
v_mul_f32 v203,v199,v198
v_mul_f32 v201,s78,v198
v_mul_f32 v199,v198,v198
v_mul_f32 v202,s79,v199
v_mul_f32 v202,v202,v201
s_lshr_b32 s63,s63,1
s_sub_u32 s63,s89,s63
buffer_store_dwordx4 v[200:203],v4,s[36:39],s89 offen offset:0 sc1
buffer_store_dwordx2 v[204:205],v10,s[36:39],s63 offen offset:256 sc1
s_add_u32 m0,s66,s52
s_cmp_ge_u32 s51,12582912
s_cbranch_scc1 Lr_ys4_%=
buffer_load_dwordx4 v11,s[36:39],s51 offen offset:0 sc1 lds
buffer_load_dwordx4 v11,s[36:39],s51 offen offset:1024 sc1 lds
buffer_load_dwordx4 v11,s[36:39],s51 offen offset:2048 sc1 lds
buffer_load_dwordx4 v11,s[36:39],s51 offen offset:3072 sc1 lds
s_lshl_b32 s63,s50,14
s_add_u32 s51,s51,s63
Lr_ys4_%=:
s_add_u32 s66,s66,16384
s_cmp_eq_u32 s66,151552
s_cselect_b32 s66,4096,s66
v_add_u32 v18,s66,v11
s_mov_b32 s60,s90
s_waitcnt vmcnt(6)
v_cmp_eq_u32 vcc,s48,v8
s_nop 0
s_cmp_eq_u64 vcc,exec
s_cselect_b32 s63,s54,256
s_cmp_lt_u32 s63,256
s_cbranch_scc1 Lr_tc1_%=
buffer_load_dword v8,v6,s[36:39],s91 offen offset:0 sc1
s_waitcnt vmcnt(0)
buffer_store_dword v7,v5,s[36:39],s60 offen offset:0 sc1
s_mov_b32 s60,536870912
v_cmp_eq_u32 vcc,s48,v8
s_nop 0
s_cmp_eq_u64 vcc,exec
s_cselect_b32 s63,s54,256
s_cmp_lt_u32 s63,256
s_cbranch_scc1 Lr_bq1_%=
s_cmp_ge_u32 s54,256
s_cbranch_scc1 Lr_done_%=
Lr_bp1_%=:
buffer_load_dword v8,v6,s[36:39],s91 offen offset:0 sc1
s_waitcnt vmcnt(0)
v_cmp_eq_u32 vcc,s48,v8
s_nop 0
s_cmp_eq_u64 vcc,exec
s_cselect_b32 s63,1,s96
s_cmp_lg_u32 s63,0
s_cbranch_scc1 Lr_bq1_%=)"
R"(
s_add_u32 s74,s74,1
s_cmp_ge_u32 s74,20000
s_cselect_b32 s96,1,0
s_sleep 4
s_branch Lr_bp1_%=
Lr_bq1_%=:
s_add_u32 s63,s87,71303168
buffer_load_dwordx4 a[0:3],v11,s[36:39],s63 offen offset:0 sc1
buffer_load_dwordx4 a[4:7],v11,s[36:39],s63 offen offset:1024 sc1
buffer_load_dwordx4 a[8:11],v11,s[36:39],s63 offen offset:2048 sc1
buffer_load_dwordx4 a[12:15],v11,s[36:39],s63 offen offset:3072 sc1
s_add_u32 s63,s87,89128960
buffer_load_dwordx4 a[16:19],v11,s[36:39],s63 offen offset:0 sc1
buffer_load_dwordx4 a[20:23],v11,s[36:39],s63 offen offset:1024 sc1
buffer_load_dwordx4 a[24:27],v11,s[36:39],s63 offen offset:2048 sc1
buffer_load_dwordx4 a[28:31],v11,s[36:39],s63 offen offset:3072 sc1
s_add_u32 s63,s87,99614720
buffer_load_dwordx4 a[32:35],v11,s[36:39],s63 offen offset:0 sc1
buffer_load_dwordx4 a[36:39],v11,s[36:39],s63 offen offset:1024 sc1
buffer_load_dwordx4 a[40:43],v11,s[36:39],s63 offen offset:2048 sc1
buffer_load_dwordx4 a[44:47],v11,s[36:39],s63 offen offset:3072 sc1
s_add_u32 s63,s87,109051904
buffer_load_dwordx4 a[48:51],v11,s[36:39],s63 offen offset:0 sc1
buffer_load_dwordx4 a[52:55],v11,s[36:39],s63 offen offset:1024 sc1
buffer_load_dwordx4 a[56:59],v11,s[36:39],s63 offen offset:2048 sc1
buffer_load_dwordx4 a[60:63],v11,s[36:39],s63 offen offset:3072 sc1
buffer_load_dword v8,v6,s[36:39],s69 offen offset:0 sc1
s_waitcnt vmcnt(0)
s_branch Lr_it0_%=
Lr_ta1_%=:
ds_read_b128 v[32:35],v18 offset:0
ds_read_b128 v[36:39],v18 offset:1024
ds_read_b128 v[40:43],v18 offset:2048
ds_read_b128 v[44:47],v18 offset:3072
v_mfma_f32_16x16x32_bf16 v[128:131],v[20:23],a[64:67],0
s_add_u32 s63,s87,71303168
v_mfma_f32_16x16x32_bf16 v[132:135],v[24:27],a[64:67],0
buffer_load_dwordx4 a[0:3],v11,s[36:39],s63 offen offset:0 sc1
v_mfma_f32_16x16x32_bf16 v[128:131],v[20:23],a[80:83],v[128:131]
v_mfma_f32_16x16x32_bf16 v[132:135],v[24:27],a[80:83],v[132:135]
buffer_load_dwordx4 a[4:7],v11,s[36:39],s63 offen offset:1024 sc1
v_mfma_f32_16x16x32_bf16 v[128:131],v[20:23],a[96:99],v[128:131]
v_mfma_f32_16x16x32_bf16 v[132:135],v[24:27],a[96:99],v[132:135]
v_mfma_f32_16x16x32_bf16 v[128:131],v[20:23],a[112:115],v[128:131]
v_mfma_f32_16x16x32_bf16 v[132:135],v[24:27],a[112:115],v[132:135]
buffer_load_dwordx4 a[8:11],v11,s[36:39],s63 offen offset:2048 sc1
v_mfma_f32_16x16x32_bf16 v[136:139],v[20:23],a[68:71],0
v_mfma_f32_16x16x32_bf16 v[140:143],v[24:27],a[68:71],0
v_mfma_f32_16x16x32_bf16 v[136:139],v[20:23],a[84:87],v[136:139]
v_mfma_f32_16x16x32_bf16 v[140:143],v[24:27],a[84:87],v[140:143]
buffer_load_dwordx4 a[12:15],v11,s[36:39],s63 offen offset:3072 sc1
v_mfma_f32_16x16x32_bf16 v[136:139],v[20:23],a[100:103],v[136:139]
v_cvt_pk_bf16_f32 v208,v128,v129
v_cvt_pk_bf16_f32 v209,v130,v131
v_mfma_f32_16x16x32_bf16 v[140:143],v[24:27],a[100:103],v[140:143]
v_cvt_pk_bf16_f32 v210,v132,v133
v_cvt_pk_bf16_f32 v211,v134,v135
v_mfma_f32_16x16x32_bf16 v[136:139],v[20:23],a[116:119],v[136:139]
s_add_u32 s63,s87,89128960
v_mfma_f32_16x16x32_bf16 v[140:143],v[24:27],a[116:119],v[140:143]
buffer_load_dwordx4 a[16:19],v11,s[36:39],s63 offen offset:0 sc1
v_mfma_f32_16x16x32_bf16 v[144:147],v[20:23],a[72:75],0
v_mfma_f32_16x16x32_bf16 v[148:151],v[24:27],a[72:75],0
v_mfma_f32_16x16x32_bf16 v[144:147],v[20:23],a[88:91],v[144:147]
v_mfma_f32_16x16x32_bf16 v[148:151],v[24:27],a[88:91],v[148:151]
buffer_load_dwordx4 a[20:23],v11,s[36:39],s63 offen offset:1024 sc1
v_mfma_f32_16x16x32_bf16 v[144:147],v[20:23],a[104:107],v[144:147]
v_cvt_pk_bf16_f32 v212,v136,v137
v_cvt_pk_bf16_f32 v213,v138,v139
v_mfma_f32_16x16x32_bf16 v[148:151],v[24:27],a[104:107],v[148:151]
v_cvt_pk_bf16_f32 v214,v140,v141
v_cvt_pk_bf16_f32 v215,v142,v143
v_mfma_f32_16x16x32_bf16 v[144:147],v[20:23],a[120:123],v[144:147]
v_mfma_f32_16x16x32_bf16 v[148:151],v[24:27],a[120:123],v[148:151]
s_waitcnt vmcnt(6)
buffer_store_dword v7,v5,s[36:39],s60 offen offset:0 sc1
s_mov_b32 s60,536870912
buffer_load_dwordx4 a[24:27],v11,s[36:39],s63 offen offset:2048 sc1
v_mfma_f32_16x16x32_bf16 v[28:31],v[192:195],v[208:211],0
v_mfma_f32_16x16x32_bf16 v[204:207],v[208:211],v[208:211],0
v_mfma_f32_16x16x32_bf16 v[152:155],v[20:23],a[76:79],0
v_mfma_f32_16x16x32_bf16 v[156:159],v[24:27],a[76:79],0
buffer_load_dwordx4 a[28:31],v11,s[36:39],s63 offen offset:3072 sc1
v_mfma_f32_16x16x32_bf16 v[152:155],v[20:23],a[92:95],v[152:155]
s_add_u32 s63,s87,99614720
v_mfma_f32_16x16x32_bf16 v[156:159],v[24:27],a[92:95],v[156:159]
v_mfma_f32_16x16x32_bf16 v[152:155],v[20:23],a[108:111],v[152:155]
v_cvt_pk_bf16_f32 v216,v144,v145
v_cvt_pk_bf16_f32 v217,v146,v147
v_mfma_f32_16x16x32_bf16 v[156:159],v[24:27],a[108:111],v[156:159]
v_cvt_pk_bf16_f32 v218,v148,v149
v_cvt_pk_bf16_f32 v219,v150,v151
v_mfma_f32_16x16x32_bf16 v[152:155],v[20:23],a[124:127],v[152:155]
buffer_load_dwordx4 a[32:35],v11,s[36:39],s63 offen offset:0 sc1
v_mfma_f32_16x16x32_bf16 v[156:159],v[24:27],a[124:127],v[156:159]
v_mfma_f32_16x16x32_bf16 v[28:31],v[192:195],v[212:215],v[28:31]
v_mfma_f32_16x16x32_bf16 v[204:207],v[212:215],v[212:215],v[204:207]
buffer_load_dwordx4 a[36:39],v11,s[36:39],s63 offen offset:1024 sc1
v_mfma_f32_16x16x32_bf16 v[28:31],v[192:195],v[216:219],v[28:31]
v_mfma_f32_16x16x32_bf16 v[204:207],v[216:219],v[216:219],v[204:207]
s_waitcnt lgkmcnt(0)
v_mfma_f32_16x16x32_bf16 v[160:163],v[20:23],v[32:35],0
v_cvt_pk_bf16_f32 v220,v152,v153
v_cvt_pk_bf16_f32 v221,v154,v155
v_mfma_f32_16x16x32_bf16 v[164:167],v[24:27],v[32:35],0
v_cvt_pk_bf16_f32 v222,v156,v157
v_cvt_pk_bf16_f32 v223,v158,v159
v_mfma_f32_16x16x32_bf16 v[168:171],v[20:23],v[36:39],0
buffer_load_dwordx4 a[40:43],v11,s[36:39],s63 offen offset:2048 sc1
v_mfma_f32_16x16x32_bf16 v[28:31],v[192:195],v[220:223],v[28:31]
buffer_load_dwordx4 a[44:47],v11,s[36:39],s63 offen offset:3072 sc1
v_mfma_f32_16x16x32_bf16 v[204:207],v[220:223],v[220:223],v[204:207]
s_add_u32 s63,s87,109051904
v_mfma_f32_16x16x32_bf16 v[172:175],v[24:27],v[36:39],0
v_mfma_f32_16x16x32_bf16 v[176:179],v[20:23],v[40:43],0
buffer_load_dwordx4 a[48:51],v11,s[36:39],s63 offen offset:0 sc1
v_mfma_f32_16x16x32_bf16 v[180:183],v[24:27],v[40:43],0
s_nop 0
v_add_f32_dpp v224,v28,v28 row_ror:8 row_mask:0xf bank_mask:0xf
s_nop 0
v_cndmask_b32 v225,v204,v205,s[46:47]
v_cndmask_b32 v226,v206,v207,s[46:47]
v_cndmask_b32 v225,v225,v226,s[56:57]
v_mfma_f32_16x16x32_bf16 v[184:187],v[20:23],v[44:47],0
ds_write_b32 v2,v224 offset:0
ds_write_b32 v0,v225 offset:0
v_mfma_f32_16x16x32_bf16 v[188:191],v[24:27],v[44:47],0
s_waitcnt lgkmcnt(0)
buffer_load_dwordx4 a[52:55],v11,s[36:39],s63 offen offset:1024 sc1
s_barrier
buffer_load_dwordx4 a[56:59],v11,s[36:39],s63 offen offset:2048 sc1
ds_read_b128 v[196:199],v3 offset:0
buffer_load_dwordx4 a[60:63],v11,s[36:39],s63 offen offset:3072 sc1
ds_read_b128 v[200:203],v3 offset:16
ds_read_b128 v[204:207],v3 offset:32
s_waitcnt lgkmcnt(0)
buffer_load_dword v8,v6,s[36:39],s69 offen offset:0 sc1
v_add_f32 v196,v196,v197
v_add_f32 v198,v198,v199
v_add_f32 v200,v200,v204
v_add_f32 v201,v201,v205
v_add_f32 v202,v202,v206
v_add_f32 v203,v203,v207
v_add_f32 v196,v196,v198
v_add_f32 v200,v200,v201
v_add_f32 v202,v202,v203
v_mul_f32 v196,s76,v196
v_add_f32 v197,v200,v202
v_mul_f32 v197,s76,v197
v_fma_f32 v197,-v196,v196,v197
v_max_f32 v197,0,v197
v_add_f32 v197,s77,v197
v_rsq_f32 v228,v197
s_nop 0
v_mul_f32_e64 v230,-v196,v228
v_mov_b32 v229,v228
v_mov_b32 v231,v230
v_pk_fma_f32 v[196:197],v[128:129],v[228:229],v[230:231]
v_pk_fma_f32 v[198:199],v[130:131],v[228:229],v[230:231]
v_pk_fma_f32 v[200:201],v[132:133],v[228:229],v[230:231]
v_pk_fma_f32 v[202:203],v[134:135],v[228:229],v[230:231]
v_pk_fma_f32 v[128:129],v[196:197],v[160:161],v[128:129]
v_pk_fma_f32 v[130:131],v[198:199],v[162:163],v[130:131]
v_pk_fma_f32 v[132:133],v[200:201],v[164:165],v[132:133]
v_pk_fma_f32 v[134:135],v[202:203],v[166:167],v[134:135]
v_pk_fma_f32 v[196:197],v[136:137],v[228:229],v[230:231]
v_pk_fma_f32 v[198:199],v[138:139],v[228:229],v[230:231]
v_pk_fma_f32 v[200:201],v[140:141],v[228:229],v[230:231]
v_pk_fma_f32 v[202:203],v[142:143],v[228:229],v[230:231]
v_pk_fma_f32 v[136:137],v[196:197],v[168:169],v[136:137]
v_pk_fma_f32 v[138:139],v[198:199],v[170:171],v[138:139]
v_pk_fma_f32 v[140:141],v[200:201],v[172:173],v[140:141]
v_pk_fma_f32 v[142:143],v[202:203],v[174:175],v[142:143]
v_pk_fma_f32 v[196:197],v[144:145],v[228:229],v[230:231]
v_pk_fma_f32 v[198:199],v[146:147],v[228:229],v[230:231]
v_pk_fma_f32 v[200:201],v[148:149],v[228:229],v[230:231]
v_pk_fma_f32 v[202:203],v[150:151],v[228:229],v[230:231]
v_pk_fma_f32 v[144:145],v[196:197],v[176:177],v[144:145]
v_pk_fma_f32 v[146:147],v[198:199],v[178:179],v[146:147]
v_pk_fma_f32 v[148:149],v[200:201],v[180:181],v[148:149]
v_pk_fma_f32 v[150:151],v[202:203],v[182:183],v[150:151]
v_pk_fma_f32 v[196:197],v[152:153],v[228:229],v[230:231]
v_pk_fma_f32 v[198:199],v[154:155],v[228:229],v[230:231]
v_pk_fma_f32 v[200:201],v[156:157],v[228:229],v[230:231]
v_pk_fma_f32 v[202:203],v[158:159],v[228:229],v[230:231]
v_pk_fma_f32 v[152:153],v[196:197],v[184:185],v[152:153]
v_pk_fma_f32 v[154:155],v[198:199],v[186:187],v[154:155]
v_pk_fma_f32 v[156:157],v[200:201],v[188:189],v[156:157]
v_pk_fma_f32 v[158:159],v[202:203],v[190:191],v[158:159]
v_cvt_pk_bf16_f32 v208,v128,v129
v_cvt_pk_bf16_f32 v209,v130,v131
v_cvt_pk_bf16_f32 v210,v132,v133
v_cvt_pk_bf16_f32 v211,v134,v135
v_cvt_pk_bf16_f32 v212,v136,v137
v_cvt_pk_bf16_f32 v213,v138,v139
v_cvt_pk_bf16_f32 v214,v140,v141
v_cvt_pk_bf16_f32 v215,v142,v143
v_mfma_f32_16x16x32_bf16 v[28:31],v[192:195],v[208:211],0
v_cvt_pk_bf16_f32 v216,v144,v145
v_cvt_pk_bf16_f32 v217,v146,v147)"
R"(
v_mfma_f32_16x16x32_bf16 v[204:207],v[208:211],v[208:211],0
v_cvt_pk_bf16_f32 v218,v148,v149
v_cvt_pk_bf16_f32 v219,v150,v151
v_mfma_f32_16x16x32_bf16 v[28:31],v[192:195],v[212:215],v[28:31]
v_cvt_pk_bf16_f32 v220,v152,v153
v_cvt_pk_bf16_f32 v221,v154,v155
v_mfma_f32_16x16x32_bf16 v[204:207],v[212:215],v[212:215],v[204:207]
v_cvt_pk_bf16_f32 v222,v156,v157
v_cvt_pk_bf16_f32 v223,v158,v159
v_mfma_f32_16x16x32_bf16 v[28:31],v[192:195],v[216:219],v[28:31]
v_mfma_f32_16x16x32_bf16 v[204:207],v[216:219],v[216:219],v[204:207]
v_mfma_f32_16x16x32_bf16 v[28:31],v[192:195],v[220:223],v[28:31]
v_mfma_f32_16x16x32_bf16 v[204:207],v[220:223],v[220:223],v[204:207]
buffer_store_dwordx4 v[208:211],v1,s[36:39],s88 offen offset:0 sc1
buffer_store_dwordx4 v[212:215],v1,s[36:39],s88 offen offset:2048 sc1
s_add_u32 s88,s88,4096
s_nop 3
v_add_f32_dpp v224,v28,v28 row_ror:8 row_mask:0xf bank_mask:0xf
v_cndmask_b32 v225,v204,v205,s[46:47]
v_cndmask_b32 v226,v206,v207,s[46:47]
v_cndmask_b32 v225,v225,v226,s[56:57]
s_and_b32 s63,s89,128
ds_write_b32 v2,v224 offset:1024
ds_write_b32 v0,v225 offset:1024
buffer_store_dwordx4 v[216:219],v1,s[36:39],s88 offen offset:0 sc1
buffer_store_dwordx4 v[220:223],v1,s[36:39],s88 offen offset:2048 sc1
s_waitcnt lgkmcnt(0)
s_barrier
ds_read_b128 v[196:199],v3 offset:1024
ds_read_b128 v[200:203],v3 offset:1040
ds_read_b128 v[204:207],v3 offset:1056
s_waitcnt lgkmcnt(0)
v_add_f32 v196,v196,v197
v_add_f32 v198,v198,v199
v_add_f32 v200,v200,v204
v_add_f32 v201,v201,v205
v_add_f32 v202,v202,v206
v_add_f32 v203,v203,v207
v_add_f32 v196,v196,v198
v_add_f32 v200,v200,v201
v_add_f32 v202,v202,v203
v_mul_f32 v196,s76,v196
v_add_f32 v197,v200,v202
v_mul_f32 v197,s76,v197
v_fma_f32 v197,-v196,v196,v197
v_max_f32 v197,0,v197
v_add_f32 v198,s77,v197
v_fma_f32 v199,s77,v198,v197
v_rsq_f32 v205,v198
v_rsq_f32 v198,v199
v_mov_b32 v200,v196
v_mul_f32_e64 v204,-v196,v205
v_mul_f32 v203,v199,v198
v_mul_f32 v201,s78,v198
v_mul_f32 v199,v198,v198
v_mul_f32 v202,s79,v199
v_mul_f32 v202,v202,v201
s_lshr_b32 s63,s63,1
s_sub_u32 s63,s89,s63
buffer_store_dwordx4 v[200:203],v4,s[36:39],s89 offen offset:0 sc1
buffer_store_dwordx2 v[204:205],v10,s[36:39],s63 offen offset:256 sc1
s_add_u32 m0,s66,s52
s_cmp_ge_u32 s51,12582912
s_cbranch_scc1 Lr_ys5_%=
buffer_load_dwordx4 v11,s[36:39],s51 offen offset:0 sc1 lds
buffer_load_dwordx4 v11,s[36:39],s51 offen offset:1024 sc1 lds
buffer_load_dwordx4 v11,s[36:39],s51 offen offset:2048 sc1 lds
buffer_load_dwordx4 v11,s[36:39],s51 offen offset:3072 sc1 lds
s_lshl_b32 s63,s50,14
s_add_u32 s51,s51,s63
Lr_ys5_%=:
s_add_u32 s66,s66,16384
s_cmp_eq_u32 s66,151552
s_cselect_b32 s66,4096,s66
v_add_u32 v18,s66,v11
s_mov_b32 s60,s90
s_waitcnt vmcnt(6)
s_branch Lr_it0_%=
Lr_tc1_%=:
s_add_u32 s63,s87,71303168
buffer_load_dwordx4 a[0:3],v11,s[36:39],s63 offen offset:0 sc1
buffer_load_dwordx4 a[4:7],v11,s[36:39],s63 offen offset:1024 sc1
buffer_load_dwordx4 a[8:11],v11,s[36:39],s63 offen offset:2048 sc1
buffer_load_dwordx4 a[12:15],v11,s[36:39],s63 offen offset:3072 sc1
s_add_u32 s63,s87,89128960
buffer_load_dwordx4 a[16:19],v11,s[36:39],s63 offen offset:0 sc1
buffer_load_dwordx4 a[20:23],v11,s[36:39],s63 offen offset:1024 sc1
buffer_load_dwordx4 a[24:27],v11,s[36:39],s63 offen offset:2048 sc1
buffer_load_dwordx4 a[28:31],v11,s[36:39],s63 offen offset:3072 sc1
s_add_u32 s63,s87,99614720
buffer_load_dwordx4 a[32:35],v11,s[36:39],s63 offen offset:0 sc1
buffer_load_dwordx4 a[36:39],v11,s[36:39],s63 offen offset:1024 sc1
buffer_load_dwordx4 a[40:43],v11,s[36:39],s63 offen offset:2048 sc1
buffer_load_dwordx4 a[44:47],v11,s[36:39],s63 offen offset:3072 sc1
s_add_u32 s63,s87,109051904
buffer_load_dwordx4 a[48:51],v11,s[36:39],s63 offen offset:0 sc1
buffer_load_dwordx4 a[52:55],v11,s[36:39],s63 offen offset:1024 sc1
buffer_load_dwordx4 a[56:59],v11,s[36:39],s63 offen offset:2048 sc1
buffer_load_dwordx4 a[60:63],v11,s[36:39],s63 offen offset:3072 sc1
buffer_load_dword v8,v6,s[36:39],s69 offen offset:0 sc1
s_waitcnt vmcnt(17)
buffer_store_dword v7,v5,s[36:39],s60 offen offset:0 sc1
s_mov_b32 s60,536870912
s_waitcnt vmcnt(1)
s_branch Lr_it0_%=
Lr_done_%=:
s_waitcnt vmcnt(0) lgkmcnt(0)
s_barrier
Lcode_r_e:)"
  :: "s"(ws), "s"(sK), "s"(sStep), "s"(sRq), "s"(sEpR), "v"(vRP), "v"(vRF), "v"(vRwr), "v"(vRrd), "v"(vRS), "v"(vFlag), "v"(vL4), "v"(vRL) : "v0", "v1", "v2", "v3", "v4", "v5", "v6", "v7", "v8", "v9", "v10", "v11", "v12", "v13", "v14", "v15", "v16", "v17", "v18", "v19", "v20", "v21", "v22", "v23", "v24", "v25", "v26", "v27", "v28", "v29", "v30", "v31", "v32", "v33", "v34", "v35", "v36", "v37", "v38", "v39", "v40", "v41", "v42", "v43", "v44", "v45", "v46", "v47", "v48", "v49", "v50", "v51", "v52", "v53", "v54", "v55", "v56", "v57", "v58", "v59", "v60", "v61", "v62", "v63", "v64", "v65", "v66", "v67", "v68", "v69", "v70", "v71", "v72", "v73", "v74", "v75", "v76", "v77", "v78", "v79", "v80", "v81", "v82", "v83", "v84", "v85", "v86", "v87", "v88", "v89", "v90", "v91", "v92", "v93", "v94", "v95", "v96", "v97", "v98", "v99", "v100", "v101", "v102", "v103", "v104", "v105", "v106", "v107", "v108", "v109", "v110", "v111", "v112", "v113", "v114", "v115", "v116", "v117", "v118", "v119", "v120", "v121", "v122", "v123", "v124", "v125", "v126", "v127", "v128", "v129", "v130", "v131", "v132", "v133", "v134", "v135", "v136", "v137", "v138", "v139", "v140", "v141", "v142", "v143", "v144", "v145", "v146", "v147", "v148", "v149", "v150", "v151", "v152", "v153", "v154", "v155", "v156", "v157", "v158", "v159", "v160", "v161", "v162", "v163", "v164", "v165", "v166", "v167", "v168", "v169", "v170", "v171", "v172", "v173", "v174", "v175", "v176", "v177", "v178", "v179", "v180", "v181", "v182", "v183", "v184", "v185", "v186", "v187", "v188", "v189", "v190", "v191", "v192", "v193", "v194", "v195", "v196", "v197", "v198", "v199", "v200", "v201", "v202", "v203", "v204", "v205", "v206", "v207", "v208", "v209", "v210", "v211", "v212", "v213", "v214", "v215", "v216", "v217", "v218", "v219", "v220", "v221", "v222", "v223", "v224", "v225", "v226", "v227", "v228", "v229", "v230", "v231", "v255", "a0", "a1", "a2", "a3", "a4", "a5", "a6", "a7", "a8", "a9", "a10", "a11", "a12", "a13", "a14", "a15", "a16", "a17", "a18", "a19", "a20", "a21", "a22", "a23", "a24", "a25", "a26", "a27", "a28", "a29", "a30", "a31", "a32", "a33", "a34", "a35", "a36", "a37", "a38", "a39", "a40", "a41", "a42", "a43", "a44", "a45", "a46", "a47", "a48", "a49", "a50", "a51", "a52", "a53", "a54", "a55", "a56", "a57", "a58", "a59", "a60", "a61", "a62", "a63", "a64", "a65", "a66", "a67", "a68", "a69", "a70", "a71", "a72", "a73", "a74", "a75", "a76", "a77", "a78", "a79", "a80", "a81", "a82", "a83", "a84", "a85", "a86", "a87", "a88", "a89", "a90", "a91", "a92", "a93", "a94", "a95", "a96", "a97", "a98", "a99", "a100", "a101", "a102", "a103", "a104", "a105", "a106", "a107", "a108", "a109", "a110", "a111", "a112", "a113", "a114", "a115", "a116", "a117", "a118", "a119", "a120", "a121", "a122", "a123", "a124", "a125", "a126", "a127", "a128", "a129", "a130", "a131", "a132", "a133", "a134", "a135", "a136", "a137", "a138", "a139", "a140", "a141", "a142", "a143", "a144", "a145", "a146", "a147", "a148", "a149", "a150", "a151", "a152", "a153", "a154", "a155", "a156", "a157", "a158", "a159", "a160", "a161", "a162", "a163", "a164", "a165", "a166", "a167", "a168", "a169", "a170", "a171", "a172", "a173", "a174", "a175", "a176", "a177", "a178", "a179", "a180", "a181", "a182", "a183", "a184", "a185", "a186", "a187", "a188", "a189", "a190", "a191", "a192", "a193", "a194", "a195", "a196", "a197", "a198", "a199", "a200", "a201", "a202", "a203", "a204", "a205", "a206", "a207", "a208", "a209", "a210", "a211", "a212", "a213", "a214", "a215", "a216", "a217", "a218", "a219", "a220", "a221", "a222", "a223", "a224", "a225", "a226", "a227", "a228", "a229", "a230", "a231", "a232", "a233", "a234", "a235", "a236", "a237", "a238", "a239", "a240", "a241", "a242", "a243", "a244", "a245", "a246", "a247", "a248", "a249", "a250", "a251", "a252", "a253", "a254", "a255", "s36", "s37", "s38", "s39", "s40", "s41", "s42", "s43", "s44", "s45", "s46", "s47", "s48", "s49", "s50", "s51", "s52", "s53", "s54", "s55", "s56", "s57", "s58", "s59", "s60", "s61", "s62", "s63", "s64", "s65", "s66", "s67", "s68", "s69", "s70", "s71", "s72", "s73", "s74", "s75", "s76", "s77", "s78", "s79", "s80", "s81", "s82", "s83", "s84", "s85", "s86", "s87", "s88", "s89", "s90", "s91", "s92", "s93", "s94", "s95", "s96", "s97", "s98", "s99", "vcc", "scc", "memory");
      if (N3) { kind = 3; rep = 5; }
      else if (N1 && slice >= 2) { kind = 4; rep = 5; kh = (slice - 2) >> 2; slice = (slice - 2) & 3; }
    }
   if (kind < 3) {
    KARG(ws, 10) KARG(w12, 5 + kind)
    COMMON(kind == 1 ? 8 : 16, kind == 1 ? OFF_FXP : OFF_FH, (kind == 1 ? OFF_FH : OFF_FX1) + slice * 128, AUX + 1536, 2048, 2048, 4);
    const int rot = (ROT >> (kind - 1) & 1) * rep, g0 = ((wave >> 1) + rot) & 3, g1 = g0 ^ 2;
    const int r0 = 16 + l15 - (kind == 2 ? ((g0 & 1) ? -1 : 1) * (1 + (g0 >> 1)) : 0), r1 = 16 + l15 - (kind == 2 ? ((g1 & 1) ? -1 : 1) * (1 + (g1 >> 1)) : 0);
    const unsigned sb = (kind == 1 ? OFF_XP : OFF_HS) - 32768 + (wave & 1) * 4096 + lg * 256;
    const unsigned vSrc = sb + g0 * 8192 + (r0 >> 4) * 32768 + (r0 & 15) * 16, vSrc2 = sb + g1 * 8192 + (r1 >> 4) * 32768 + (r1 & 15) * 16;
    const unsigned vOut = (kind == 1 ? OFF_HS : OFF_X1) + (slice * 4 + wave) * 1024 + vA;
    const unsigned vX = AUX + wave * 2048 + (kind == 1 ? l15 * 16 : vA), vX2 = AUX + wave * 2048 + l15 * 8;
    const unsigned vAuxS = kind == 1 ? OFF_S1 + ln * 4 : OFF_XP + (slice * 4 + wave) * 1024 + vA;
    const unsigned vAuxS2 = ln < 32 ? OFF_S1 - 1024 + 256 + ln * 4 : OOB;
    constexpr unsigned RED = AUX + 24576 + 1536;
    const unsigned vOut2 = wave != 2 ? OOB : kind == 1 ? (slice == 0 && ln < 16 ? OFF_S1 + 256 + ln * 8 : OOB) : ln < 32 ? OFF_ST + slice * 128 + ln * 4 : OOB;
    const unsigned vStW = RED + wave * 4 + (kind == 1 ? l15 * 32 + (lg == lq ? 16 : 0) : (l15 * 2 + (lg & 1)) * 16), vStR = RED + (kind == 1 ? l15 * 32 : (ln & 31) * 16);
    const unsigned sWw = (rot * 256 + 16 * wave) * 2048 + slice * 256;
    unsigned epOut;
    asm volatile(R"(
Lcode_g12_b:
s_cmp_lg_u32 %8,0
s_cbranch_scc1 Lg2_begin_%=
s_mov_b64 s[36:37],%1
s_mov_b32 s38,122727552
s_mov_b32 s39,0x00020000
s_mov_b32 s54,%2
s_mov_b32 s55,%3
s_mov_b32 s56,%4
s_mov_b32 s58,%5
s_mov_b32 s83,%6
s_mov_b32 s82,%7
s_mov_b32 s84,%8
v_mov_b32 v0,%9
v_mov_b32 v1,%10
v_mov_b32 v2,%11
v_mov_b32 v3,%12
v_mov_b32 v4,%13
v_mov_b32 v6,%14
v_mov_b32 v7,%15
v_mov_b32 v8,%16
v_mov_b32 v9,%17
v_mov_b32 v10,%18
v_mov_b32 v11,%19
v_mov_b32 v12,%20
v_mov_b32 v13,%21
v_mov_b32 v14,%22
v_mov_b32 v28,%23
v_mov_b32 v29,%24
v_mov_b32 v30,%25
v_mov_b32 v31,%26
v_mov_b32 v32,%27
s_mov_b64 s[40:41],%28
s_mov_b32 s42,2097152
s_mov_b32 s43,0x00020000
s_lshl_b32 s88,s54,11
s_mov_b32 s69,122727424
buffer_load_dword v5,off,s[36:39],s69 sc1
s_add_u32 m0,s88,0
s_add_u32 s61,s82,0
s_and_b32 s61,s61,2097151
buffer_load_dwordx4 v28,s[40:43],s61 offen offset:0  lds
s_add_u32 m0,s88,1024
s_nop 0
buffer_load_dwordx4 v29,s[40:43],s61 offen offset:0  lds
s_add_u32 m0,s88,8064
s_nop 0
buffer_load_dwordx4 v28,s[40:43],s61 offen offset:128  lds
s_add_u32 m0,s88,9088
s_nop 0
buffer_load_dwordx4 v29,s[40:43],s61 offen offset:128  lds
s_add_u32 m0,s88,16384
s_add_u32 s61,s82,131072
s_and_b32 s61,s61,2097151
buffer_load_dwordx4 v28,s[40:43],s61 offen offset:0  lds
s_add_u32 m0,s88,17408
s_nop 0
buffer_load_dwordx4 v29,s[40:43],s61 offen offset:0  lds
s_add_u32 m0,s88,24448
s_nop 0
buffer_load_dwordx4 v28,s[40:43],s61 offen offset:128  lds
s_add_u32 m0,s88,25472
s_nop 0
buffer_load_dwordx4 v29,s[40:43],s61 offen offset:128  lds
s_add_u32 m0,s88,32768
s_add_u32 s61,s82,262144
s_and_b32 s61,s61,2097151
buffer_load_dwordx4 v28,s[40:43],s61 offen offset:0  lds
s_add_u32 m0,s88,33792
s_nop 0
buffer_load_dwordx4 v29,s[40:43],s61 offen offset:0  lds
s_add_u32 m0,s88,40832
s_nop 0
buffer_load_dwordx4 v28,s[40:43],s61 offen offset:128  lds
s_add_u32 m0,s88,41856
s_nop 0
buffer_load_dwordx4 v29,s[40:43],s61 offen offset:128  lds
s_add_u32 m0,s88,49152
s_add_u32 s61,s82,393216
s_and_b32 s61,s61,2097151
buffer_load_dwordx4 v28,s[40:43],s61 offen offset:0  lds
s_add_u32 m0,s88,50176
s_nop 0
buffer_load_dwordx4 v29,s[40:43],s61 offen offset:0  lds
s_add_u32 m0,s88,57216
s_nop 0
buffer_load_dwordx4 v28,s[40:43],s61 offen offset:128  lds
s_add_u32 m0,s88,58240
s_nop 0
buffer_load_dwordx4 v29,s[40:43],s61 offen offset:128  lds
s_add_u32 m0,s88,65536
s_add_u32 s61,s82,524288
s_and_b32 s61,s61,2097151
buffer_load_dwordx4 v28,s[40:43],s61 offen offset:0  lds
s_add_u32 m0,s88,66560
s_nop 0
buffer_load_dwordx4 v29,s[40:43],s61 offen offset:0  lds
s_add_u32 m0,s88,73600
s_nop 0
buffer_load_dwordx4 v28,s[40:43],s61 offen offset:128  lds
s_add_u32 m0,s88,74624
s_nop 0
buffer_load_dwordx4 v29,s[40:43],s61 offen offset:128  lds
s_add_u32 m0,s88,81920
s_add_u32 s61,s82,655360
s_and_b32 s61,s61,2097151
buffer_load_dwordx4 v28,s[40:43],s61 offen offset:0  lds
s_add_u32 m0,s88,82944
s_nop 0
buffer_load_dwordx4 v29,s[40:43],s61 offen offset:0  lds
s_add_u32 m0,s88,89984
s_nop 0
buffer_load_dwordx4 v28,s[40:43],s61 offen offset:128  lds
s_add_u32 m0,s88,91008
s_nop 0
buffer_load_dwordx4 v29,s[40:43],s61 offen offset:128  lds
s_add_u32 m0,s88,98304
s_add_u32 s61,s82,786432
s_and_b32 s61,s61,2097151
buffer_load_dwordx4 v28,s[40:43],s61 offen offset:0  lds
s_add_u32 m0,s88,99328
s_nop 0
buffer_load_dwordx4 v29,s[40:43],s61 offen offset:0  lds
s_add_u32 m0,s88,106368
s_nop 0
buffer_load_dwordx4 v28,s[40:43],s61 offen offset:128  lds
s_add_u32 m0,s88,107392
s_nop 0
buffer_load_dwordx4 v29,s[40:43],s61 offen offset:128  lds
v_add_u32 v36,49152,v30
v_add_u32 v37,49152,v31
v_add_u32 v38,98304,v30
v_add_u32 v39,98304,v31
v_mov_b32 v76,0x3f803f80
v_mov_b32 v77,0x3f803f80
v_mov_b32 v78,0x3f803f80
v_mov_b32 v79,0x3f803f80
s_waitcnt vmcnt(24)
s_barrier
s_add_u32 m0,s88,114688
s_add_u32 s61,s82,917504
s_and_b32 s61,s61,2097151
buffer_load_dwordx4 v28,s[40:43],s61 offen offset:0  lds
s_add_u32 m0,s88,115712
ds_read_b64_tr_b16 a[0:1],v30 offset:0
buffer_load_dwordx4 v29,s[40:43],s61 offen offset:0  lds
s_add_u32 m0,s88,122752
ds_read_b64_tr_b16 a[2:3],v30 offset:512
buffer_load_dwordx4 v28,s[40:43],s61 offen offset:128  lds
s_add_u32 m0,s88,123776
ds_read_b64_tr_b16 a[4:5],v31 offset:0
buffer_load_dwordx4 v29,s[40:43],s61 offen offset:128  lds
ds_read_b64_tr_b16 a[6:7],v31 offset:512
ds_read_b64_tr_b16 a[8:9],v30 offset:4096
ds_read_b64_tr_b16 a[10:11],v30 offset:4608
ds_read_b64_tr_b16 a[12:13],v31 offset:4096
ds_read_b64_tr_b16 a[14:15],v31 offset:4608
s_waitcnt vmcnt(24)
s_barrier
s_add_u32 m0,s88,131072
s_add_u32 s61,s82,1048576
s_and_b32 s61,s61,2097151
buffer_load_dwordx4 v28,s[40:43],s61 offen offset:0  lds
s_add_u32 m0,s88,132096
ds_read_b64_tr_b16 a[16:17],v30 offset:16384
buffer_load_dwordx4 v29,s[40:43],s61 offen offset:0  lds
s_add_u32 m0,s88,139136
ds_read_b64_tr_b16 a[18:19],v30 offset:16896
buffer_load_dwordx4 v28,s[40:43],s61 offen offset:128  lds
s_add_u32 m0,s88,140160
ds_read_b64_tr_b16 a[20:21],v31 offset:16384
buffer_load_dwordx4 v29,s[40:43],s61 offen offset:128  lds
ds_read_b64_tr_b16 a[22:23],v31 offset:16896
ds_read_b64_tr_b16 a[24:25],v30 offset:20480
ds_read_b64_tr_b16 a[26:27],v30 offset:20992
ds_read_b64_tr_b16 a[28:29],v31 offset:20480
ds_read_b64_tr_b16 a[30:31],v31 offset:20992
s_waitcnt lgkmcnt(8)
v_mfma_f32_16x16x32_bf16 v[20:23],a[0:3],v[76:79],0
v_mfma_f32_16x16x32_bf16 v[24:27],a[4:7],v[76:79],0
v_mfma_f32_16x16x32_bf16 v[20:23],a[8:11],v[76:79],v[20:23]
v_mfma_f32_16x16x32_bf16 v[24:27],a[12:15],v[76:79],v[24:27]
s_waitcnt vmcnt(24)
s_barrier
s_add_u32 m0,s88,0
s_add_u32 s61,s82,1179648
s_and_b32 s61,s61,2097151
buffer_load_dwordx4 v28,s[40:43],s61 offen offset:0  lds
s_add_u32 m0,s88,1024
ds_read_b64_tr_b16 a[32:33],v30 offset:32768
buffer_load_dwordx4 v29,s[40:43],s61 offen offset:0  lds
s_add_u32 m0,s88,8064
ds_read_b64_tr_b16 a[34:35],v30 offset:33280
buffer_load_dwordx4 v28,s[40:43],s61 offen offset:128  lds
s_add_u32 m0,s88,9088
ds_read_b64_tr_b16 a[36:37],v31 offset:32768
buffer_load_dwordx4 v29,s[40:43],s61 offen offset:128  lds
ds_read_b64_tr_b16 a[38:39],v31 offset:33280
ds_read_b64_tr_b16 a[40:41],v30 offset:36864
ds_read_b64_tr_b16 a[42:43],v30 offset:37376)"
R"(
ds_read_b64_tr_b16 a[44:45],v31 offset:36864
ds_read_b64_tr_b16 a[46:47],v31 offset:37376
s_waitcnt lgkmcnt(8)
v_mfma_f32_16x16x32_bf16 v[20:23],a[16:19],v[76:79],v[20:23]
v_mfma_f32_16x16x32_bf16 v[24:27],a[20:23],v[76:79],v[24:27]
v_mfma_f32_16x16x32_bf16 v[20:23],a[24:27],v[76:79],v[20:23]
v_mfma_f32_16x16x32_bf16 v[24:27],a[28:31],v[76:79],v[24:27]
s_waitcnt vmcnt(24)
s_barrier
s_add_u32 m0,s88,16384
s_add_u32 s61,s82,1310720
s_and_b32 s61,s61,2097151
buffer_load_dwordx4 v28,s[40:43],s61 offen offset:0  lds
s_add_u32 m0,s88,17408
ds_read_b64_tr_b16 a[48:49],v36 offset:0
buffer_load_dwordx4 v29,s[40:43],s61 offen offset:0  lds
s_add_u32 m0,s88,24448
ds_read_b64_tr_b16 a[50:51],v36 offset:512
buffer_load_dwordx4 v28,s[40:43],s61 offen offset:128  lds
s_add_u32 m0,s88,25472
ds_read_b64_tr_b16 a[52:53],v37 offset:0
buffer_load_dwordx4 v29,s[40:43],s61 offen offset:128  lds
ds_read_b64_tr_b16 a[54:55],v37 offset:512
ds_read_b64_tr_b16 a[56:57],v36 offset:4096
ds_read_b64_tr_b16 a[58:59],v36 offset:4608
ds_read_b64_tr_b16 a[60:61],v37 offset:4096
ds_read_b64_tr_b16 a[62:63],v37 offset:4608
s_waitcnt lgkmcnt(8)
v_mfma_f32_16x16x32_bf16 v[20:23],a[32:35],v[76:79],v[20:23]
v_mfma_f32_16x16x32_bf16 v[24:27],a[36:39],v[76:79],v[24:27]
v_mfma_f32_16x16x32_bf16 v[20:23],a[40:43],v[76:79],v[20:23]
v_mfma_f32_16x16x32_bf16 v[24:27],a[44:47],v[76:79],v[24:27]
s_waitcnt vmcnt(24)
s_barrier
s_add_u32 m0,s88,32768
s_add_u32 s61,s82,1441792
s_and_b32 s61,s61,2097151
buffer_load_dwordx4 v28,s[40:43],s61 offen offset:0  lds
s_add_u32 m0,s88,33792
ds_read_b64_tr_b16 a[64:65],v36 offset:16384
buffer_load_dwordx4 v29,s[40:43],s61 offen offset:0  lds
s_add_u32 m0,s88,40832
ds_read_b64_tr_b16 a[66:67],v36 offset:16896
buffer_load_dwordx4 v28,s[40:43],s61 offen offset:128  lds
s_add_u32 m0,s88,41856
ds_read_b64_tr_b16 a[68:69],v37 offset:16384
buffer_load_dwordx4 v29,s[40:43],s61 offen offset:128  lds
ds_read_b64_tr_b16 a[70:71],v37 offset:16896
ds_read_b64_tr_b16 a[72:73],v36 offset:20480
ds_read_b64_tr_b16 a[74:75],v36 offset:20992
ds_read_b64_tr_b16 a[76:77],v37 offset:20480
ds_read_b64_tr_b16 a[78:79],v37 offset:20992
s_waitcnt lgkmcnt(8)
v_mfma_f32_16x16x32_bf16 v[20:23],a[48:51],v[76:79],v[20:23]
v_mfma_f32_16x16x32_bf16 v[24:27],a[52:55],v[76:79],v[24:27]
v_mfma_f32_16x16x32_bf16 v[20:23],a[56:59],v[76:79],v[20:23]
v_mfma_f32_16x16x32_bf16 v[24:27],a[60:63],v[76:79],v[24:27]
s_waitcnt vmcnt(24)
s_barrier
s_add_u32 m0,s88,49152
s_add_u32 s61,s82,1572864
s_and_b32 s61,s61,2097151
buffer_load_dwordx4 v28,s[40:43],s61 offen offset:0  lds
s_add_u32 m0,s88,50176
ds_read_b64_tr_b16 a[80:81],v36 offset:32768
buffer_load_dwordx4 v29,s[40:43],s61 offen offset:0  lds
s_add_u32 m0,s88,57216
ds_read_b64_tr_b16 a[82:83],v36 offset:33280
buffer_load_dwordx4 v28,s[40:43],s61 offen offset:128  lds
s_add_u32 m0,s88,58240
ds_read_b64_tr_b16 a[84:85],v37 offset:32768
buffer_load_dwordx4 v29,s[40:43],s61 offen offset:128  lds
ds_read_b64_tr_b16 a[86:87],v37 offset:33280
ds_read_b64_tr_b16 a[88:89],v36 offset:36864
ds_read_b64_tr_b16 a[90:91],v36 offset:37376
ds_read_b64_tr_b16 a[92:93],v37 offset:36864
ds_read_b64_tr_b16 a[94:95],v37 offset:37376
s_waitcnt lgkmcnt(8)
v_mfma_f32_16x16x32_bf16 v[20:23],a[64:67],v[76:79],v[20:23]
v_mfma_f32_16x16x32_bf16 v[24:27],a[68:71],v[76:79],v[24:27]
v_mfma_f32_16x16x32_bf16 v[20:23],a[72:75],v[76:79],v[20:23]
v_mfma_f32_16x16x32_bf16 v[24:27],a[76:79],v[76:79],v[24:27]
s_waitcnt vmcnt(24)
s_barrier
s_add_u32 m0,s88,65536
s_add_u32 s61,s82,1703936
s_and_b32 s61,s61,2097151
buffer_load_dwordx4 v28,s[40:43],s61 offen offset:0  lds
s_add_u32 m0,s88,66560
ds_read_b64_tr_b16 a[96:97],v38 offset:0
buffer_load_dwordx4 v29,s[40:43],s61 offen offset:0  lds
s_add_u32 m0,s88,73600
ds_read_b64_tr_b16 a[98:99],v38 offset:512
buffer_load_dwordx4 v28,s[40:43],s61 offen offset:128  lds
s_add_u32 m0,s88,74624
ds_read_b64_tr_b16 a[100:101],v39 offset:0
buffer_load_dwordx4 v29,s[40:43],s61 offen offset:128  lds
ds_read_b64_tr_b16 a[102:103],v39 offset:512
ds_read_b64_tr_b16 a[104:105],v38 offset:4096
ds_read_b64_tr_b16 a[106:107],v38 offset:4608
ds_read_b64_tr_b16 a[108:109],v39 offset:4096
ds_read_b64_tr_b16 a[110:111],v39 offset:4608
s_waitcnt lgkmcnt(8)
v_mfma_f32_16x16x32_bf16 v[20:23],a[80:83],v[76:79],v[20:23]
v_mfma_f32_16x16x32_bf16 v[24:27],a[84:87],v[76:79],v[24:27]
v_mfma_f32_16x16x32_bf16 v[20:23],a[88:91],v[76:79],v[20:23]
v_mfma_f32_16x16x32_bf16 v[24:27],a[92:95],v[76:79],v[24:27]
s_waitcnt vmcnt(24)
s_barrier
s_add_u32 m0,s88,81920
s_add_u32 s61,s82,1835008
s_and_b32 s61,s61,2097151
buffer_load_dwordx4 v28,s[40:43],s61 offen offset:0  lds
s_add_u32 m0,s88,82944
ds_read_b64_tr_b16 a[112:113],v38 offset:16384
buffer_load_dwordx4 v29,s[40:43],s61 offen offset:0  lds
s_add_u32 m0,s88,89984
ds_read_b64_tr_b16 a[114:115],v38 offset:16896
buffer_load_dwordx4 v28,s[40:43],s61 offen offset:128  lds
s_add_u32 m0,s88,91008
ds_read_b64_tr_b16 a[116:117],v39 offset:16384
buffer_load_dwordx4 v29,s[40:43],s61 offen offset:128  lds
ds_read_b64_tr_b16 a[118:119],v39 offset:16896
ds_read_b64_tr_b16 a[120:121],v38 offset:20480
ds_read_b64_tr_b16 a[122:123],v38 offset:20992
ds_read_b64_tr_b16 a[124:125],v39 offset:20480
ds_read_b64_tr_b16 a[126:127],v39 offset:20992
s_waitcnt lgkmcnt(8)
v_mfma_f32_16x16x32_bf16 v[20:23],a[96:99],v[76:79],v[20:23]
v_mfma_f32_16x16x32_bf16 v[24:27],a[100:103],v[76:79],v[24:27]
v_mfma_f32_16x16x32_bf16 v[20:23],a[104:107],v[76:79],v[20:23]
v_mfma_f32_16x16x32_bf16 v[24:27],a[108:111],v[76:79],v[24:27]
s_waitcnt vmcnt(24)
s_barrier
s_add_u32 m0,s88,98304
s_add_u32 s61,s82,1966080
s_and_b32 s61,s61,2097151
buffer_load_dwordx4 v28,s[40:43],s61 offen offset:0  lds
s_add_u32 m0,s88,99328
ds_read_b64_tr_b16 a[128:129],v38 offset:32768
buffer_load_dwordx4 v29,s[40:43],s61 offen offset:0  lds
s_add_u32 m0,s88,106368
ds_read_b64_tr_b16 a[130:131],v38 offset:33280
buffer_load_dwordx4 v28,s[40:43],s61 offen offset:128  lds
s_add_u32 m0,s88,107392
ds_read_b64_tr_b16 a[132:133],v39 offset:32768
buffer_load_dwordx4 v29,s[40:43],s61 offen offset:128  lds
ds_read_b64_tr_b16 a[134:135],v39 offset:33280
ds_read_b64_tr_b16 a[136:137],v38 offset:36864
ds_read_b64_tr_b16 a[138:139],v38 offset:37376
ds_read_b64_tr_b16 a[140:141],v39 offset:36864
ds_read_b64_tr_b16 a[142:143],v39 offset:37376
s_waitcnt lgkmcnt(8)
v_mfma_f32_16x16x32_bf16 v[20:23],a[112:115],v[76:79],v[20:23]
v_mfma_f32_16x16x32_bf16 v[24:27],a[116:119],v[76:79],v[24:27]
v_mfma_f32_16x16x32_bf16 v[20:23],a[120:123],v[76:79],v[20:23]
v_mfma_f32_16x16x32_bf16 v[24:27],a[124:127],v[76:79],v[24:27]
s_waitcnt vmcnt(24)
s_barrier
ds_read_b64_tr_b16 a[144:145],v30 offset:0
ds_read_b64_tr_b16 a[146:147],v30 offset:512
ds_read_b64_tr_b16 a[148:149],v31 offset:0
ds_read_b64_tr_b16 a[150:151],v31 offset:512
ds_read_b64_tr_b16 a[152:153],v30 offset:4096
ds_read_b64_tr_b16 a[154:155],v30 offset:4608
ds_read_b64_tr_b16 a[156:157],v31 offset:4096
ds_read_b64_tr_b16 a[158:159],v31 offset:4608
s_waitcnt lgkmcnt(8)
v_mfma_f32_16x16x32_bf16 v[20:23],a[128:131],v[76:79],v[20:23]
v_mfma_f32_16x16x32_bf16 v[24:27],a[132:135],v[76:79],v[24:27]
v_mfma_f32_16x16x32_bf16 v[20:23],a[136:139],v[76:79],v[20:23]
v_mfma_f32_16x16x32_bf16 v[24:27],a[140:143],v[76:79],v[24:27]
s_waitcnt vmcnt(20)
s_barrier
ds_read_b64_tr_b16 a[160:161],v30 offset:16384
ds_read_b64_tr_b16 a[162:163],v30 offset:16896
ds_read_b64_tr_b16 a[164:165],v31 offset:16384
ds_read_b64_tr_b16 a[166:167],v31 offset:16896
ds_read_b64_tr_b16 a[168:169],v30 offset:20480
ds_read_b64_tr_b16 a[170:171],v30 offset:20992
ds_read_b64_tr_b16 a[172:173],v31 offset:20480
ds_read_b64_tr_b16 a[174:175],v31 offset:20992
s_waitcnt lgkmcnt(8)
v_mfma_f32_16x16x32_bf16 v[20:23],a[144:147],v[76:79],v[20:23]
v_mfma_f32_16x16x32_bf16 v[24:27],a[148:151],v[76:79],v[24:27]
v_mfma_f32_16x16x32_bf16 v[20:23],a[152:155],v[76:79],v[20:23]
v_mfma_f32_16x16x32_bf16 v[24:27],a[156:159],v[76:79],v[24:27]
s_waitcnt vmcnt(16)
s_barrier
ds_read_b64_tr_b16 a[176:177],v30 offset:32768
ds_read_b64_tr_b16 a[178:179],v30 offset:33280
ds_read_b64_tr_b16 a[180:181],v31 offset:32768
ds_read_b64_tr_b16 a[182:183],v31 offset:33280
ds_read_b64_tr_b16 a[184:185],v30 offset:36864
ds_read_b64_tr_b16 a[186:187],v30 offset:37376
ds_read_b64_tr_b16 a[188:189],v31 offset:36864
ds_read_b64_tr_b16 a[190:191],v31 offset:37376
s_getpc_b64 s[46:47]
Lg1_cp_%=:
s_add_u32 s46,s46,Lg1_init_%=-Lg1_cp_%=)"
R"(
s_addc_u32 s47,s47,0
s_lshl_b32 s68,s54,13
v_lshl_add_u32 v60,v2,3,s68
v_min_u32 v61,Lg1_done_%=-Lg1_init_%=,v60
global_load_dword v64,v61,s[46:47]
v_add_u32 v60,32768,v60
v_min_u32 v62,Lg1_done_%=-Lg1_init_%=,v60
global_load_dword v65,v62,s[46:47]
s_waitcnt lgkmcnt(8)
v_mfma_f32_16x16x32_bf16 v[20:23],a[160:163],v[76:79],v[20:23]
v_mfma_f32_16x16x32_bf16 v[24:27],a[164:167],v[76:79],v[24:27]
v_mfma_f32_16x16x32_bf16 v[20:23],a[168:171],v[76:79],v[20:23]
v_mfma_f32_16x16x32_bf16 v[24:27],a[172:175],v[76:79],v[24:27]
s_waitcnt vmcnt(14)
s_barrier
ds_read_b64_tr_b16 a[192:193],v36 offset:0
ds_read_b64_tr_b16 a[194:195],v36 offset:512
ds_read_b64_tr_b16 a[196:197],v37 offset:0
ds_read_b64_tr_b16 a[198:199],v37 offset:512
ds_read_b64_tr_b16 a[200:201],v36 offset:4096
ds_read_b64_tr_b16 a[202:203],v36 offset:4608
ds_read_b64_tr_b16 a[204:205],v37 offset:4096
ds_read_b64_tr_b16 a[206:207],v37 offset:4608
s_waitcnt lgkmcnt(8)
v_mfma_f32_16x16x32_bf16 v[20:23],a[176:179],v[76:79],v[20:23]
v_mfma_f32_16x16x32_bf16 v[24:27],a[180:183],v[76:79],v[24:27]
v_mfma_f32_16x16x32_bf16 v[20:23],a[184:187],v[76:79],v[20:23]
v_mfma_f32_16x16x32_bf16 v[24:27],a[188:191],v[76:79],v[24:27]
s_waitcnt vmcnt(10)
s_barrier
ds_read_b64_tr_b16 a[208:209],v36 offset:16384
ds_read_b64_tr_b16 a[210:211],v36 offset:16896
ds_read_b64_tr_b16 a[212:213],v37 offset:16384
ds_read_b64_tr_b16 a[214:215],v37 offset:16896
ds_read_b64_tr_b16 a[216:217],v36 offset:20480
ds_read_b64_tr_b16 a[218:219],v36 offset:20992
ds_read_b64_tr_b16 a[220:221],v37 offset:20480
ds_read_b64_tr_b16 a[222:223],v37 offset:20992
s_waitcnt lgkmcnt(8)
v_mfma_f32_16x16x32_bf16 v[20:23],a[192:195],v[76:79],v[20:23]
v_mfma_f32_16x16x32_bf16 v[24:27],a[196:199],v[76:79],v[24:27]
v_mfma_f32_16x16x32_bf16 v[20:23],a[200:203],v[76:79],v[20:23]
v_mfma_f32_16x16x32_bf16 v[24:27],a[204:207],v[76:79],v[24:27]
s_waitcnt vmcnt(6)
s_barrier
ds_read_b64_tr_b16 a[224:225],v36 offset:32768
ds_read_b64_tr_b16 a[226:227],v36 offset:33280
ds_read_b64_tr_b16 a[228:229],v37 offset:32768
ds_read_b64_tr_b16 a[230:231],v37 offset:33280
ds_read_b64_tr_b16 a[232:233],v36 offset:36864
ds_read_b64_tr_b16 a[234:235],v36 offset:37376
ds_read_b64_tr_b16 a[236:237],v37 offset:36864
ds_read_b64_tr_b16 a[238:239],v37 offset:37376
s_waitcnt lgkmcnt(8)
v_mfma_f32_16x16x32_bf16 v[20:23],a[208:211],v[76:79],v[20:23]
v_mfma_f32_16x16x32_bf16 v[24:27],a[212:215],v[76:79],v[24:27]
v_mfma_f32_16x16x32_bf16 v[20:23],a[216:219],v[76:79],v[20:23]
v_mfma_f32_16x16x32_bf16 v[24:27],a[220:223],v[76:79],v[24:27]
s_waitcnt vmcnt(2)
s_barrier
ds_read_b64_tr_b16 a[240:241],v38 offset:0
ds_read_b64_tr_b16 a[242:243],v38 offset:512
ds_read_b64_tr_b16 a[244:245],v39 offset:0
ds_read_b64_tr_b16 a[246:247],v39 offset:512
ds_read_b64_tr_b16 a[248:249],v38 offset:4096
ds_read_b64_tr_b16 a[250:251],v38 offset:4608
ds_read_b64_tr_b16 a[252:253],v39 offset:4096
ds_read_b64_tr_b16 a[254:255],v39 offset:4608
s_waitcnt lgkmcnt(8)
v_mfma_f32_16x16x32_bf16 v[20:23],a[224:227],v[76:79],v[20:23]
v_mfma_f32_16x16x32_bf16 v[24:27],a[228:231],v[76:79],v[24:27]
v_mfma_f32_16x16x32_bf16 v[20:23],a[232:235],v[76:79],v[20:23]
v_mfma_f32_16x16x32_bf16 v[24:27],a[236:239],v[76:79],v[24:27]
s_waitcnt lgkmcnt(0)
v_mfma_f32_16x16x32_bf16 v[20:23],a[240:243],v[76:79],v[20:23]
v_mfma_f32_16x16x32_bf16 v[24:27],a[244:247],v[76:79],v[24:27]
v_mfma_f32_16x16x32_bf16 v[20:23],a[248:251],v[76:79],v[20:23]
v_mfma_f32_16x16x32_bf16 v[24:27],a[252:255],v[76:79],v[24:27]
s_waitcnt lgkmcnt(0)
s_barrier
v_readfirstlane_b32 s60,v5
s_nop 3
s_add_u32 s60,s60,1
s_cmp_eq_u32 s60,0
s_cselect_b32 s60,1,s60
Lg1_init_%=:
v_mov_b32 v5,s60
s_mov_b32 s59,536870912
s_add_u32 s57,s56,131072
s_mov_b32 s79,0
s_mov_b32 s75,0x3a800000
s_mov_b32 s76,0xc0135761
s_mov_b32 s77,0x3d372713
s_mov_b32 s44,s59
s_mov_b32 s45,s59
s_mov_b32 s46,s59
s_mov_b32 s47,s59
s_mov_b32 s48,s83
s_add_u32 s49,s48,s55
s_cmp_eq_u32 s49,s56
s_cselect_b32 s49,s57,s49
s_cmp_eq_u32 s49,s58
s_cselect_b32 s49,s59,s49
s_add_u32 s50,s49,s55
s_cmp_eq_u32 s50,s56
s_cselect_b32 s50,s57,s50
s_cmp_eq_u32 s50,s58
s_cselect_b32 s50,s59,s50
s_add_u32 s51,s50,s55
s_cmp_eq_u32 s51,s56
s_cselect_b32 s51,s57,s51
s_cmp_eq_u32 s51,s58
s_cselect_b32 s51,s59,s51
s_add_u32 s52,s51,s55
s_cmp_eq_u32 s52,s56
s_cselect_b32 s52,s57,s52
s_cmp_eq_u32 s52,s58
s_cselect_b32 s52,s59,s52
s_lshl_b32 s63,s54,12
s_add_u32 s63,s63,65536
s_add_u32 s64,s63,32768
s_lshl_b32 s88,s54,10
s_add_u32 s88,s88,131072
s_lshl_b32 s78,s54,11
s_add_u32 s78,s78,132608
v_mov_b32 v15,0x3727c5ac
v_mov_b32 v100,0x3f803f80
v_mov_b32 v101,0x3f803f80
v_mov_b32 v102,0x3f803f80
v_mov_b32 v103,0x3f803f80
s_mov_b32 s89,1048576
s_mov_b32 s82,0xaaaaaaaa
s_mov_b32 s83,0xaaaaaaaa
s_mov_b32 s84,0xcccccccc
s_mov_b32 s85,0xcccccccc
s_mov_b32 s98,0x00f0000f
s_mov_b32 s99,0xf0000f00
s_getpc_b64 s[90:91]
Lg1_pc_%=:
s_mov_b32 s68,Lg1_d1w0_%=-Lg1_pc_%=
s_cmp_eq_u32 s54,1
s_cselect_b32 s68,Lg1_d1w1_%=-Lg1_pc_%=,s68
s_cmp_eq_u32 s54,2
s_cselect_b32 s68,Lg1_d1w2_%=-Lg1_pc_%=,s68
s_cmp_eq_u32 s54,3
s_cselect_b32 s68,Lg1_d1w3_%=-Lg1_pc_%=,s68
s_add_u32 s96,s90,s68
s_addc_u32 s97,s91,0
s_mov_b32 s68,Lg1_d0w0_%=-Lg1_pc_%=
s_cmp_eq_u32 s54,1
s_cselect_b32 s68,Lg1_d0w1_%=-Lg1_pc_%=,s68
s_cmp_eq_u32 s54,2
s_cselect_b32 s68,Lg1_d0w2_%=-Lg1_pc_%=,s68
s_cmp_eq_u32 s54,3
s_cselect_b32 s68,Lg1_d0w3_%=-Lg1_pc_%=,s68
s_add_u32 s94,s90,s68
s_addc_u32 s95,s91,0
s_mov_b32 s68,Lg1_a1w0_%=-Lg1_pc_%=
s_cmp_eq_u32 s54,1
s_cselect_b32 s68,Lg1_a1w1_%=-Lg1_pc_%=,s68
s_cmp_eq_u32 s54,2
s_cselect_b32 s68,Lg1_a1w2_%=-Lg1_pc_%=,s68
s_cmp_eq_u32 s54,3
s_cselect_b32 s68,Lg1_a1w3_%=-Lg1_pc_%=,s68
s_add_u32 s92,s90,s68
s_addc_u32 s93,s91,0
s_mov_b32 s68,Lg1_a0w0_%=-Lg1_pc_%=
s_cmp_eq_u32 s54,1
s_cselect_b32 s68,Lg1_a0w1_%=-Lg1_pc_%=,s68
s_cmp_eq_u32 s54,2
s_cselect_b32 s68,Lg1_a0w2_%=-Lg1_pc_%=,s68
s_cmp_eq_u32 s54,3
s_cselect_b32 s68,Lg1_a0w3_%=-Lg1_pc_%=,s68
s_add_u32 s90,s90,s68
s_addc_u32 s91,s91,0
s_mov_b32 s66,s59
s_mov_b32 s65,0
s_mov_b32 s62,s63
s_branch Lg1_slow0_%=
Lg1_a0w0_%=:
v_mfma_f32_16x16x32_bf16 v[28:31],a[0:3],v[44:47],0
s_add_u32 m0,s62,16384
ds_read_b128 v[72:75],v2 offset:7168
s_sub_u32 s69,s66,1048576
v_mfma_f32_16x16x32_bf16 v[32:35],a[4:7],v[44:47],0
buffer_load_dwordx4 v1,s[36:39],s66 offen offset:0 sc1 lds
v_mfma_f32_16x16x32_bf16 v[104:107],v[100:103],v[44:47],0
s_cmp_lt_u32 s69,2097152
s_cselect_b32 s89,3145728,s89
s_sub_u32 s69,s66,3145728
v_mfma_f32_16x16x32_bf16 v[108:111],v[44:47],v[44:47],0
s_cmp_lt_u32 s69,1048576
v_fma_f32 v36,-v96,v20,v36
v_fma_f32 v37,-v96,v21,v37
v_mfma_f32_16x16x32_bf16 v[28:31],a[8:11],v[48:51],v[28:31]
s_cselect_b32 s89,4194304,s89
ds_read_b128 v[44:47],v2 offset:8192
v_fma_f32 v38,-v96,v22,v38
v_mfma_f32_16x16x32_bf16 v[32:35],a[12:15],v[48:51],v[32:35]
buffer_load_dwordx4 v1,s[36:39],s66 offen offset:1024 sc1 lds)"
R"(
v_mfma_f32_16x16x32_bf16 v[104:107],v[100:103],v[48:51],v[104:107]
s_lshr_b32 s73,s47,0
v_fma_f32 v39,-v96,v23,v39
v_mul_f32 v76,v36,v36
v_mfma_f32_16x16x32_bf16 v[108:111],v[48:51],v[48:51],v[108:111]
s_lshr_b32 s72,s66,6
v_mul_f32 v77,v37,v37
v_mul_f32 v78,v38,v38
v_mfma_f32_16x16x32_bf16 v[28:31],a[16:19],v[52:55],v[28:31]
s_lshr_b32 s71,s62,2
ds_read_b128 v[48:51],v2 offset:9216
v_mul_f32 v79,v39,v39
v_mfma_f32_16x16x32_bf16 v[32:35],a[20:23],v[52:55],v[32:35]
buffer_load_dwordx4 v1,s[36:39],s66 offen offset:2048 sc1 lds
v_mfma_f32_16x16x32_bf16 v[104:107],v[100:103],v[52:55],v[104:107]
s_cmp_lt_u32 s66,4194304
v_fma_f32 v76,v98,v76,v97
v_mul_f32 v76,v36,v76
v_mfma_f32_16x16x32_bf16 v[108:111],v[52:55],v[52:55],v[108:111]
s_cselect_b32 s72,s59,s72
v_exp_f32 v76,v76
v_mfma_f32_16x16x32_bf16 v[28:31],a[24:27],v[56:59],v[28:31]
ds_read_b128 v[52:55],v2 offset:10240
v_fma_f32 v76,v76,v99,v99
v_mfma_f32_16x16x32_bf16 v[32:35],a[28:31],v[56:59],v[32:35]
buffer_load_dwordx4 v1,s[36:39],s66 offen offset:3072 sc1 lds
v_mfma_f32_16x16x32_bf16 v[104:107],v[100:103],v[56:59],v[104:107]
s_add_u32 m0,s71,s88
v_rcp_f32 v76,v76
v_mfma_f32_16x16x32_bf16 v[108:111],v[56:59],v[56:59],v[108:111]
s_waitcnt lgkmcnt(3)
buffer_load_dword v10,s[36:39],s72 offen offset:0 sc1 lds
v_mfma_f32_16x16x32_bf16 v[28:31],a[32:35],v[60:63],v[28:31]
s_xor_b32 s87,s64,65536
ds_read_b128 v[56:59],v2 offset:11264
v_fma_f32 v77,v98,v77,v97
v_mfma_f32_16x16x32_bf16 v[32:35],a[36:39],v[60:63],v[32:35]
s_cmp_ge_u32 s66,s59
v_mul_f32 v77,v37,v77
v_fma_f32 v78,v98,v78,v97
v_mfma_f32_16x16x32_bf16 v[28:31],a[40:43],v[64:67],v[28:31]
s_cselect_b32 s86,s49,s50
ds_read_b128 v[60:63],v2 offset:12288
v_mul_f32 v78,v38,v78
v_mfma_f32_16x16x32_bf16 v[32:35],a[44:47],v[64:67],v[32:35]
s_cselect_b32 s87,s87,s63
v_exp_f32 v77,v77
v_mfma_f32_16x16x32_bf16 v[28:31],a[48:51],v[68:71],v[28:31]
s_mov_b32 s62,s87
ds_read_b128 v[64:67],v2 offset:13312
v_fma_f32 v77,v77,v99,v99
v_mfma_f32_16x16x32_bf16 v[32:35],a[52:55],v[68:71],v[32:35]
s_cmp_lt_u32 s86,s89
v_exp_f32 v78,v78
v_mfma_f32_16x16x32_bf16 v[28:31],a[56:59],v[72:75],v[28:31]
ds_read_b128 v[68:71],v2 offset:14336
v_fma_f32 v78,v78,v99,v99
v_mfma_f32_16x16x32_bf16 v[32:35],a[60:63],v[72:75],v[32:35]
s_waitcnt lgkmcnt(3)
v_rcp_f32 v77,v77
v_mfma_f32_16x16x32_bf16 v[28:31],a[64:67],v[44:47],v[28:31]
s_waitcnt vmcnt(10)
ds_read_b128 v[72:75],v2 offset:15360
v_fma_f32 v79,v98,v79,v97
v_mfma_f32_16x16x32_bf16 v[32:35],a[68:71],v[44:47],v[32:35]
s_barrier
v_mfma_f32_16x16x32_bf16 v[28:31],a[72:75],v[48:51],v[28:31]
ds_read_b128 v[44:47],v2 offset:16384
ds_read_b32 v16,v7 offset:0
v_mfma_f32_16x16x32_bf16 v[32:35],a[76:79],v[48:51],v[32:35]
v_rcp_f32 v78,v78
v_mfma_f32_16x16x32_bf16 v[28:31],a[80:83],v[52:55],v[28:31]
ds_read_b128 v[48:51],v2 offset:17408
v_mul_f32 v79,v39,v79
v_mfma_f32_16x16x32_bf16 v[32:35],a[84:87],v[52:55],v[32:35]
v_exp_f32 v79,v79
v_mfma_f32_16x16x32_bf16 v[28:31],a[88:91],v[56:59],v[28:31]
s_waitcnt lgkmcnt(1)
ds_read_b128 v[52:55],v2 offset:18432
v_fma_f32 v79,v79,v99,v99
v_mfma_f32_16x16x32_bf16 v[32:35],a[92:95],v[56:59],v[32:35]
v_cmp_eq_u32 vcc,s60,v16
v_rcp_f32 v79,v79
v_mul_f32 v36,v36,v76
v_mfma_f32_16x16x32_bf16 v[28:31],a[96:99],v[60:63],v[28:31]
s_cselect_b32 vcc_lo,-1,vcc_lo
ds_read_b128 v[56:59],v2 offset:19456
v_mul_f32 v37,v37,v77
v_mfma_f32_16x16x32_bf16 v[32:35],a[100:103],v[60:63],v[32:35]
s_cmp_eq_u32 vcc_lo,-1
v_mul_f32 v38,v38,v78
v_mul_f32 v39,v39,v79
v_mfma_f32_16x16x32_bf16 v[28:31],a[104:107],v[64:67],v[28:31]
s_cselect_b32 s67,s86,s59
ds_read_b128 v[60:63],v2 offset:20480
v_cvt_pk_bf16_f32 v84,v36,v37
v_mfma_f32_16x16x32_bf16 v[32:35],a[108:111],v[64:67],v[32:35]
s_lshr_b32 s69,s50,5
v_cvt_pk_bf16_f32 v85,v38,v39
v_fma_f32 v40,-v96,v24,v40
v_mfma_f32_16x16x32_bf16 v[28:31],a[112:115],v[68:71],v[28:31]
s_cmp_lt_u32 s50,4194304
ds_read_b128 v[64:67],v2 offset:21504
v_fma_f32 v41,-v96,v25,v41
v_mfma_f32_16x16x32_bf16 v[32:35],a[116:119],v[68:71],v[32:35]
s_cselect_b32 s68,0xffffcfff,-1
v_fma_f32 v42,-v96,v26,v42
v_fma_f32 v43,-v96,v27,v43
v_mfma_f32_16x16x32_bf16 v[28:31],a[120:123],v[72:75],v[28:31]
s_and_b32 s68,s68,s69
ds_read_b128 v[68:71],v2 offset:22528
v_mul_f32 v80,v40,v40
v_mfma_f32_16x16x32_bf16 v[32:35],a[124:127],v[72:75],v[32:35]
s_waitcnt lgkmcnt(3)
v_mul_f32 v81,v41,v41
v_mul_f32 v82,v42,v42
v_mfma_f32_16x16x32_bf16 v[28:31],a[128:131],v[44:47],v[28:31]
s_cmp_lt_u32 s50,s89
ds_read_b128 v[72:75],v2 offset:23552
v_mul_f32 v83,v43,v43
v_mfma_f32_16x16x32_bf16 v[32:35],a[132:135],v[44:47],v[32:35]
s_cselect_b32 s68,s59,s68
v_fma_f32 v80,v98,v80,v97
v_mul_f32 v80,v40,v80
v_mfma_f32_16x16x32_bf16 v[104:107],v[100:103],v[44:47],v[104:107]
s_lshr_b32 s69,s51,5
v_exp_f32 v80,v80
v_mfma_f32_16x16x32_bf16 v[108:111],v[44:47],v[44:47],v[108:111]
s_cmp_lt_u32 s51,4194304
v_fma_f32 v80,v80,v99,v99
v_fma_f32 v81,v98,v81,v97
v_mfma_f32_16x16x32_bf16 v[28:31],a[136:139],v[48:51],v[28:31]
s_cselect_b32 s70,0xffffcfff,-1
ds_read_b128 v[44:47],v2 offset:24576
v_mul_f32 v81,v41,v81
v_mfma_f32_16x16x32_bf16 v[32:35],a[140:143],v[48:51],v[32:35]
s_and_b32 s70,s70,s69
v_exp_f32 v81,v81
v_mfma_f32_16x16x32_bf16 v[104:107],v[100:103],v[48:51],v[104:107]
s_cmp_lt_u32 s51,s89
v_rcp_f32 v80,v80
v_mfma_f32_16x16x32_bf16 v[108:111],v[48:51],v[48:51],v[108:111]
s_cselect_b32 s70,s59,s70
v_fma_f32 v81,v81,v99,v99
v_fma_f32 v82,v98,v82,v97
v_mfma_f32_16x16x32_bf16 v[28:31],a[144:147],v[52:55],v[28:31]
s_add_u32 s53,s52,s55
ds_read_b128 v[48:51],v2 offset:25600
v_mul_f32 v82,v42,v82
v_mfma_f32_16x16x32_bf16 v[32:35],a[148:151],v[52:55],v[32:35]
s_cmp_eq_u32 s53,s56
v_exp_f32 v82,v82
v_mfma_f32_16x16x32_bf16 v[104:107],v[100:103],v[52:55],v[104:107]
s_cselect_b32 s53,s57,s53
v_rcp_f32 v81,v81
v_mfma_f32_16x16x32_bf16 v[108:111],v[52:55],v[52:55],v[108:111]
s_cmp_eq_u32 s53,s58
v_fma_f32 v82,v82,v99,v99
v_fma_f32 v83,v98,v83,v97
v_mfma_f32_16x16x32_bf16 v[28:31],a[152:155],v[56:59],v[28:31]
s_cselect_b32 s53,s59,s53
ds_read_b128 v[52:55],v2 offset:26624
v_mul_f32 v83,v43,v83
v_mfma_f32_16x16x32_bf16 v[32:35],a[156:159],v[56:59],v[32:35]
s_xor_b32 s63,s63,65536
v_exp_f32 v83,v83
v_mfma_f32_16x16x32_bf16 v[104:107],v[100:103],v[56:59],v[104:107]
s_cmp_ge_u32 s67,s59
v_rcp_f32 v82,v82
v_mfma_f32_16x16x32_bf16 v[108:111],v[56:59],v[56:59],v[108:111]
s_waitcnt lgkmcnt(3)
v_fma_f32 v83,v83,v99,v99
v_mul_f32 v40,v40,v80
v_mfma_f32_16x16x32_bf16 v[28:31],a[160:163],v[60:63],v[28:31]
s_cselect_b32 s68,s68,s70
ds_read_b128 v[56:59],v2 offset:27648
v_mul_f32 v41,v41,v81
v_mfma_f32_16x16x32_bf16 v[32:35],a[164:167],v[60:63],v[32:35]
s_add_u32 m0,s78,256
v_rcp_f32 v83,v83
v_mfma_f32_16x16x32_bf16 v[28:31],a[168:171],v[64:67],v[28:31]
buffer_load_dword v6,s[36:39],s68 offen offset:0 sc1 lds
v_cndmask_b32 v112,v108,v109,s[82:83]
v_cndmask_b32 v113,v110,v111,s[82:83]
v_cndmask_b32 v112,v112,v113,s[84:85]
v_cndmask_b32 v112,v104,v112,s[98:99]
v_mfma_f32_16x16x32_bf16 v[32:35],a[172:175],v[64:67],v[32:35]
s_mov_b32 m0,s62
ds_read_b128 v[60:63],v2 offset:28672
ds_write_b32 v13,v112 offset:0
v_mfma_f32_16x16x32_bf16 v[28:31],a[176:179],v[68:71],v[28:31]
buffer_load_dwordx4 v0,s[36:39],s67 offen offset:0 sc1 lds
v_mfma_f32_16x16x32_bf16 v[32:35],a[180:183],v[68:71],v[32:35]
s_cmp_ge_u32 s66,s59
ds_read_b128 v[64:67],v2 offset:29696
v_mul_f32 v42,v42,v82
v_mfma_f32_16x16x32_bf16 v[28:31],a[184:187],v[72:75],v[28:31]
ds_read_b128 v[68:71],v2 offset:30720
v_mul_f32 v43,v43,v83
v_mfma_f32_16x16x32_bf16 v[32:35],a[188:191],v[72:75],v[32:35])"
R"(
s_waitcnt lgkmcnt(4)
v_cvt_pk_bf16_f32 v86,v40,v41
v_cvt_pk_bf16_f32 v87,v42,v43
v_mfma_f32_16x16x32_bf16 v[28:31],a[192:195],v[44:47],v[28:31]
s_waitcnt vmcnt(7) lgkmcnt(2)
ds_read_b128 v[72:75],v2 offset:31744
v_mfma_f32_16x16x32_bf16 v[32:35],a[196:199],v[44:47],v[32:35]
s_barrier
v_mfma_f32_16x16x32_bf16 v[28:31],a[200:203],v[48:51],v[28:31]
buffer_load_dwordx4 v0,s[36:39],s67 offen offset:1024 sc1 lds
v_mfma_f32_16x16x32_bf16 v[32:35],a[204:207],v[48:51],v[32:35]
ds_read_b128 v[44:47],v2 offset:32768
ds_read_b128 v[116:119],v14 offset:0
v_mfma_f32_16x16x32_bf16 v[28:31],a[208:211],v[52:55],v[28:31]
ds_read_b128 v[120:123],v14 offset:16
ds_read_b128 v[48:51],v2 offset:33792
v_mfma_f32_16x16x32_bf16 v[32:35],a[212:215],v[52:55],v[32:35]
v_mfma_f32_16x16x32_bf16 v[28:31],a[216:219],v[56:59],v[28:31]
buffer_load_dwordx4 v0,s[36:39],s67 offen offset:2048 sc1 lds
v_mfma_f32_16x16x32_bf16 v[32:35],a[220:223],v[56:59],v[32:35]
s_waitcnt lgkmcnt(4)
ds_read_b128 v[52:55],v2 offset:34816
v_mfma_f32_16x16x32_bf16 v[28:31],a[224:227],v[60:63],v[28:31]
s_waitcnt lgkmcnt(2)
ds_read_b128 v[56:59],v2 offset:35840
v_add_f32 v116,v116,v117
v_mfma_f32_16x16x32_bf16 v[32:35],a[228:231],v[60:63],v[32:35]
v_add_f32 v118,v118,v119
v_add_f32 v116,v116,v118
v_mfma_f32_16x16x32_bf16 v[28:31],a[232:235],v[64:67],v[28:31]
buffer_load_dwordx4 v0,s[36:39],s67 offen offset:3072 sc1 lds
v_mfma_f32_16x16x32_bf16 v[32:35],a[236:239],v[64:67],v[32:35]
ds_read_b128 v[60:63],v2 offset:36864
v_mul_f32 v92,s75,v116
v_mfma_f32_16x16x32_bf16 v[28:31],a[240:243],v[68:71],v[28:31]
ds_read_b128 v[64:67],v2 offset:37888
v_add_f32 v120,v120,v121
v_mfma_f32_16x16x32_bf16 v[32:35],a[244:247],v[68:71],v[32:35]
v_add_f32 v122,v122,v123
v_add_f32 v120,v120,v122
v_mul_f32 v120,s75,v120
v_fma_f32 v120,-v92,v92,v120
v_max_f32 v120,0,v120
v_add_f32 v115,v15,v120
v_fma_f32 v120,v15,v115,v120
v_rsq_f32 v116,v120
v_mfma_f32_16x16x32_bf16 v[28:31],a[248:251],v[72:75],v[28:31]
s_waitcnt lgkmcnt(2)
ds_read_b128 v[68:71],v2 offset:38912
buffer_store_dwordx4 v[84:87],v3,s[36:39],s73 offen offset:0 sc1
v_mul_f32 v93,s76,v116
v_mul_f32 v94,v116,v116
v_mul_f32 v95,v120,v116
v_mul_f32 v94,s77,v94
v_mul_f32 v94,v94,v93
v_mfma_f32_16x16x32_bf16 v[32:35],a[252:255],v[72:75],v[32:35]
s_cbranch_scc1 Lg1_slow1_%=
Lg1_a1w0_%=:
v_mfma_f32_16x16x32_bf16 v[36:39],a[0:3],v[44:47],0
s_add_u32 m0,s62,16384
ds_read_b128 v[72:75],v2 offset:39936
s_sub_u32 s69,s67,1048576
v_mfma_f32_16x16x32_bf16 v[40:43],a[4:7],v[44:47],0
buffer_load_dwordx4 v1,s[36:39],s67 offen offset:0 sc1 lds
v_mfma_f32_16x16x32_bf16 v[104:107],v[100:103],v[44:47],0
s_cmp_lt_u32 s69,2097152
s_cselect_b32 s89,3145728,s89
s_sub_u32 s69,s67,3145728
v_mfma_f32_16x16x32_bf16 v[108:111],v[44:47],v[44:47],0
s_cmp_lt_u32 s69,1048576
v_fma_f32 v28,-v92,v20,v28
v_fma_f32 v29,-v92,v21,v29
v_mfma_f32_16x16x32_bf16 v[36:39],a[8:11],v[48:51],v[36:39]
s_cselect_b32 s89,4194304,s89
ds_read_b128 v[44:47],v2 offset:40960
v_fma_f32 v30,-v92,v22,v30
v_mfma_f32_16x16x32_bf16 v[40:43],a[12:15],v[48:51],v[40:43]
buffer_load_dwordx4 v1,s[36:39],s67 offen offset:1024 sc1 lds
v_mfma_f32_16x16x32_bf16 v[104:107],v[100:103],v[48:51],v[104:107]
s_lshr_b32 s73,s48,0
v_fma_f32 v31,-v92,v23,v31
v_mul_f32 v76,v28,v28
v_mfma_f32_16x16x32_bf16 v[108:111],v[48:51],v[48:51],v[108:111]
s_lshr_b32 s72,s67,6
v_mul_f32 v77,v29,v29
v_mul_f32 v78,v30,v30
v_mfma_f32_16x16x32_bf16 v[36:39],a[16:19],v[52:55],v[36:39]
s_lshr_b32 s71,s62,2
ds_read_b128 v[48:51],v2 offset:41984
v_mul_f32 v79,v31,v31
v_mfma_f32_16x16x32_bf16 v[40:43],a[20:23],v[52:55],v[40:43]
buffer_load_dwordx4 v1,s[36:39],s67 offen offset:2048 sc1 lds
v_mfma_f32_16x16x32_bf16 v[104:107],v[100:103],v[52:55],v[104:107]
s_cmp_lt_u32 s67,4194304
v_fma_f32 v76,v94,v76,v93
v_mul_f32 v76,v28,v76
v_mfma_f32_16x16x32_bf16 v[108:111],v[52:55],v[52:55],v[108:111]
s_cselect_b32 s72,s59,s72
v_exp_f32 v76,v76
v_mfma_f32_16x16x32_bf16 v[36:39],a[24:27],v[56:59],v[36:39]
s_mov_b64 s[44:45],s[46:47]
ds_read_b128 v[52:55],v2 offset:43008
v_fma_f32 v76,v76,v95,v95
v_mfma_f32_16x16x32_bf16 v[40:43],a[28:31],v[56:59],v[40:43]
buffer_load_dwordx4 v1,s[36:39],s67 offen offset:3072 sc1 lds
v_mfma_f32_16x16x32_bf16 v[104:107],v[100:103],v[56:59],v[104:107]
s_add_u32 m0,s71,s88
v_rcp_f32 v76,v76
v_mfma_f32_16x16x32_bf16 v[108:111],v[56:59],v[56:59],v[108:111]
s_waitcnt lgkmcnt(3)
buffer_load_dword v10,s[36:39],s72 offen offset:0 sc1 lds
v_mfma_f32_16x16x32_bf16 v[36:39],a[32:35],v[60:63],v[36:39]
s_xor_b32 s87,s63,65536
ds_read_b128 v[56:59],v2 offset:44032
v_fma_f32 v77,v94,v77,v93
v_mfma_f32_16x16x32_bf16 v[40:43],a[36:39],v[60:63],v[40:43]
s_cmp_ge_u32 s67,s59
v_mul_f32 v77,v29,v77
v_fma_f32 v78,v94,v78,v93
v_mfma_f32_16x16x32_bf16 v[36:39],a[40:43],v[64:67],v[36:39]
s_cselect_b32 s86,s50,s51
ds_read_b128 v[60:63],v2 offset:45056
v_mul_f32 v78,v30,v78
v_mfma_f32_16x16x32_bf16 v[40:43],a[44:47],v[64:67],v[40:43]
s_cselect_b32 s87,s87,s64
v_exp_f32 v77,v77
v_mfma_f32_16x16x32_bf16 v[36:39],a[48:51],v[68:71],v[36:39]
s_mov_b32 s62,s87
ds_read_b128 v[64:67],v2 offset:46080
v_fma_f32 v77,v77,v95,v95
v_mfma_f32_16x16x32_bf16 v[40:43],a[52:55],v[68:71],v[40:43]
s_cmp_lt_u32 s86,s89
v_exp_f32 v78,v78
v_mfma_f32_16x16x32_bf16 v[36:39],a[56:59],v[72:75],v[36:39]
s_mov_b64 s[46:47],s[48:49]
ds_read_b128 v[68:71],v2 offset:47104
v_fma_f32 v78,v78,v95,v95
v_mfma_f32_16x16x32_bf16 v[40:43],a[60:63],v[72:75],v[40:43]
s_waitcnt lgkmcnt(3)
v_rcp_f32 v77,v77
v_mfma_f32_16x16x32_bf16 v[36:39],a[64:67],v[44:47],v[36:39]
s_waitcnt vmcnt(10)
ds_read_b128 v[72:75],v2 offset:48128
v_fma_f32 v79,v94,v79,v93
v_mfma_f32_16x16x32_bf16 v[40:43],a[68:71],v[44:47],v[40:43]
s_barrier
v_mfma_f32_16x16x32_bf16 v[36:39],a[72:75],v[48:51],v[36:39]
s_mov_b64 s[48:49],s[50:51]
ds_read_b128 v[44:47],v2 offset:49152
ds_read_b32 v16,v7 offset:256
v_mfma_f32_16x16x32_bf16 v[40:43],a[76:79],v[48:51],v[40:43]
v_rcp_f32 v78,v78
v_mfma_f32_16x16x32_bf16 v[36:39],a[80:83],v[52:55],v[36:39]
ds_read_b128 v[48:51],v2 offset:50176
v_mul_f32 v79,v31,v79
v_mfma_f32_16x16x32_bf16 v[40:43],a[84:87],v[52:55],v[40:43]
v_exp_f32 v79,v79
v_mfma_f32_16x16x32_bf16 v[36:39],a[88:91],v[56:59],v[36:39]
s_waitcnt lgkmcnt(1)
ds_read_b128 v[52:55],v2 offset:51200
v_fma_f32 v79,v79,v95,v95
v_mfma_f32_16x16x32_bf16 v[40:43],a[92:95],v[56:59],v[40:43]
v_cmp_eq_u32 vcc,s60,v16
v_rcp_f32 v79,v79
v_mul_f32 v28,v28,v76
v_mfma_f32_16x16x32_bf16 v[36:39],a[96:99],v[60:63],v[36:39]
s_cselect_b32 vcc_lo,-1,vcc_lo
ds_read_b128 v[56:59],v2 offset:52224
v_mul_f32 v29,v29,v77
v_mfma_f32_16x16x32_bf16 v[40:43],a[100:103],v[60:63],v[40:43]
s_cmp_eq_u32 vcc_lo,-1
v_mul_f32 v30,v30,v78
v_mul_f32 v31,v31,v79
v_mfma_f32_16x16x32_bf16 v[36:39],a[104:107],v[64:67],v[36:39]
s_cselect_b32 s66,s86,s59
ds_read_b128 v[60:63],v2 offset:53248
v_cvt_pk_bf16_f32 v84,v28,v29
v_mfma_f32_16x16x32_bf16 v[40:43],a[108:111],v[64:67],v[40:43]
s_lshr_b32 s69,s51,5
v_cvt_pk_bf16_f32 v85,v30,v31
v_fma_f32 v32,-v92,v24,v32
v_mfma_f32_16x16x32_bf16 v[36:39],a[112:115],v[68:71],v[36:39]
s_cmp_lt_u32 s51,4194304
ds_read_b128 v[64:67],v2 offset:54272
v_fma_f32 v33,-v92,v25,v33
v_mfma_f32_16x16x32_bf16 v[40:43],a[116:119],v[68:71],v[40:43]
s_cselect_b32 s68,0xffffcfff,-1
v_fma_f32 v34,-v92,v26,v34
v_fma_f32 v35,-v92,v27,v35
v_mfma_f32_16x16x32_bf16 v[36:39],a[120:123],v[72:75],v[36:39]
s_and_b32 s68,s68,s69
ds_read_b128 v[68:71],v2 offset:55296
v_mul_f32 v80,v32,v32
v_mfma_f32_16x16x32_bf16 v[40:43],a[124:127],v[72:75],v[40:43]
s_waitcnt lgkmcnt(3)
v_mul_f32 v81,v33,v33
v_mul_f32 v82,v34,v34
v_mfma_f32_16x16x32_bf16 v[36:39],a[128:131],v[44:47],v[36:39]
s_cmp_lt_u32 s51,s89
ds_read_b128 v[72:75],v2 offset:56320)"
R"(
v_mul_f32 v83,v35,v35
v_mfma_f32_16x16x32_bf16 v[40:43],a[132:135],v[44:47],v[40:43]
s_cselect_b32 s68,s59,s68
v_fma_f32 v80,v94,v80,v93
v_mul_f32 v80,v32,v80
v_mfma_f32_16x16x32_bf16 v[104:107],v[100:103],v[44:47],v[104:107]
s_lshr_b32 s69,s52,5
v_exp_f32 v80,v80
v_mfma_f32_16x16x32_bf16 v[108:111],v[44:47],v[44:47],v[108:111]
s_cmp_lt_u32 s52,4194304
v_fma_f32 v80,v80,v95,v95
v_fma_f32 v81,v94,v81,v93
v_mfma_f32_16x16x32_bf16 v[36:39],a[136:139],v[48:51],v[36:39]
s_cselect_b32 s70,0xffffcfff,-1
ds_read_b128 v[44:47],v2 offset:57344
v_mul_f32 v81,v33,v81
v_mfma_f32_16x16x32_bf16 v[40:43],a[140:143],v[48:51],v[40:43]
s_and_b32 s70,s70,s69
v_exp_f32 v81,v81
v_mfma_f32_16x16x32_bf16 v[104:107],v[100:103],v[48:51],v[104:107]
s_cmp_lt_u32 s52,s89
v_rcp_f32 v80,v80
v_mfma_f32_16x16x32_bf16 v[108:111],v[48:51],v[48:51],v[108:111]
s_cselect_b32 s70,s59,s70
v_fma_f32 v81,v81,v95,v95
v_fma_f32 v82,v94,v82,v93
v_mfma_f32_16x16x32_bf16 v[36:39],a[144:147],v[52:55],v[36:39]
s_add_u32 s74,s53,s55
ds_read_b128 v[48:51],v2 offset:58368
v_mul_f32 v82,v34,v82
v_mfma_f32_16x16x32_bf16 v[40:43],a[148:151],v[52:55],v[40:43]
s_cmp_eq_u32 s74,s56
v_exp_f32 v82,v82
v_mfma_f32_16x16x32_bf16 v[104:107],v[100:103],v[52:55],v[104:107]
s_cselect_b32 s74,s57,s74
v_rcp_f32 v81,v81
v_mfma_f32_16x16x32_bf16 v[108:111],v[52:55],v[52:55],v[108:111]
s_cmp_eq_u32 s74,s58
v_fma_f32 v82,v82,v95,v95
v_fma_f32 v83,v94,v83,v93
v_mfma_f32_16x16x32_bf16 v[36:39],a[152:155],v[56:59],v[36:39]
s_cselect_b32 s74,s59,s74
ds_read_b128 v[52:55],v2 offset:59392
v_mul_f32 v83,v35,v83
v_mfma_f32_16x16x32_bf16 v[40:43],a[156:159],v[56:59],v[40:43]
s_xor_b32 s64,s64,65536
v_exp_f32 v83,v83
v_mfma_f32_16x16x32_bf16 v[104:107],v[100:103],v[56:59],v[104:107]
s_cmp_ge_u32 s66,s59
v_rcp_f32 v82,v82
v_mfma_f32_16x16x32_bf16 v[108:111],v[56:59],v[56:59],v[108:111]
s_waitcnt lgkmcnt(3)
v_fma_f32 v83,v83,v95,v95
v_mul_f32 v32,v32,v80
v_mfma_f32_16x16x32_bf16 v[36:39],a[160:163],v[60:63],v[36:39]
s_cselect_b32 s68,s68,s70
ds_read_b128 v[56:59],v2 offset:60416
v_mul_f32 v33,v33,v81
v_mfma_f32_16x16x32_bf16 v[40:43],a[164:167],v[60:63],v[40:43]
s_add_u32 m0,s78,0
v_rcp_f32 v83,v83
v_mfma_f32_16x16x32_bf16 v[36:39],a[168:171],v[64:67],v[36:39]
buffer_load_dword v6,s[36:39],s68 offen offset:0 sc1 lds
v_cndmask_b32 v112,v108,v109,s[82:83]
v_cndmask_b32 v113,v110,v111,s[82:83]
v_cndmask_b32 v112,v112,v113,s[84:85]
v_cndmask_b32 v112,v104,v112,s[98:99]
v_mfma_f32_16x16x32_bf16 v[40:43],a[172:175],v[64:67],v[40:43]
s_mov_b32 m0,s62
ds_read_b128 v[60:63],v2 offset:61440
ds_write_b32 v13,v112 offset:2048
v_mfma_f32_16x16x32_bf16 v[36:39],a[176:179],v[68:71],v[36:39]
buffer_load_dwordx4 v0,s[36:39],s66 offen offset:0 sc1 lds
v_mfma_f32_16x16x32_bf16 v[40:43],a[180:183],v[68:71],v[40:43]
s_mov_b64 s[50:51],s[52:53]
ds_read_b128 v[64:67],v2 offset:62464
v_mul_f32 v34,v34,v82
v_mfma_f32_16x16x32_bf16 v[36:39],a[184:187],v[72:75],v[36:39]
s_mov_b32 s52,s74
ds_read_b128 v[68:71],v2 offset:63488
v_mul_f32 v35,v35,v83
v_mfma_f32_16x16x32_bf16 v[40:43],a[188:191],v[72:75],v[40:43]
s_waitcnt lgkmcnt(4)
v_cvt_pk_bf16_f32 v86,v32,v33
v_cvt_pk_bf16_f32 v87,v34,v35
v_mfma_f32_16x16x32_bf16 v[36:39],a[192:195],v[44:47],v[36:39]
s_waitcnt vmcnt(7) lgkmcnt(2)
ds_read_b128 v[72:75],v2 offset:64512
v_xor_b32 v2,65536,v2
v_mfma_f32_16x16x32_bf16 v[40:43],a[196:199],v[44:47],v[40:43]
s_barrier
v_mfma_f32_16x16x32_bf16 v[36:39],a[200:203],v[48:51],v[36:39]
buffer_load_dwordx4 v0,s[36:39],s66 offen offset:1024 sc1 lds
v_mfma_f32_16x16x32_bf16 v[40:43],a[204:207],v[48:51],v[40:43]
s_cmp_lt_u32 s67,4194304
ds_read_b128 v[44:47],v2 offset:0
ds_read_b128 v[116:119],v14 offset:2048
v_mfma_f32_16x16x32_bf16 v[36:39],a[208:211],v[52:55],v[36:39]
ds_read_b128 v[120:123],v14 offset:2064
ds_read_b128 v[48:51],v2 offset:1024
v_mfma_f32_16x16x32_bf16 v[40:43],a[212:215],v[52:55],v[40:43]
v_xor_b32 v8,16384,v8
v_mfma_f32_16x16x32_bf16 v[36:39],a[216:219],v[56:59],v[36:39]
buffer_load_dwordx4 v0,s[36:39],s66 offen offset:2048 sc1 lds
v_mfma_f32_16x16x32_bf16 v[40:43],a[220:223],v[56:59],v[40:43]
s_waitcnt lgkmcnt(4)
ds_read_b128 v[52:55],v2 offset:2048
v_mfma_f32_16x16x32_bf16 v[36:39],a[224:227],v[60:63],v[36:39]
s_waitcnt lgkmcnt(2)
ds_read_b128 v[56:59],v2 offset:3072
v_add_f32 v116,v116,v117
v_mfma_f32_16x16x32_bf16 v[40:43],a[228:231],v[60:63],v[40:43]
v_add_f32 v118,v118,v119
v_add_f32 v116,v116,v118
v_mfma_f32_16x16x32_bf16 v[36:39],a[232:235],v[64:67],v[36:39]
buffer_load_dwordx4 v0,s[36:39],s66 offen offset:3072 sc1 lds
v_mfma_f32_16x16x32_bf16 v[40:43],a[236:239],v[64:67],v[40:43]
ds_read_b128 v[60:63],v2 offset:4096
v_mul_f32 v96,s75,v116
v_mfma_f32_16x16x32_bf16 v[36:39],a[240:243],v[68:71],v[36:39]
ds_read_b128 v[64:67],v2 offset:5120
v_add_f32 v120,v120,v121
v_mfma_f32_16x16x32_bf16 v[40:43],a[244:247],v[68:71],v[40:43]
v_add_f32 v122,v122,v123
v_add_f32 v120,v120,v122
v_mul_f32 v120,s75,v120
v_fma_f32 v120,-v96,v96,v120
v_max_f32 v120,0,v120
v_add_f32 v115,v15,v120
v_fma_f32 v120,v15,v115,v120
v_rsq_f32 v116,v120
v_mfma_f32_16x16x32_bf16 v[36:39],a[248:251],v[72:75],v[36:39]
s_waitcnt lgkmcnt(2)
ds_read_b128 v[68:71],v2 offset:6144
buffer_store_dwordx4 v[84:87],v3,s[36:39],s73 offen offset:0 sc1
v_mul_f32 v97,s76,v116
v_mul_f32 v98,v116,v116
v_mul_f32 v99,v120,v116
v_mul_f32 v98,s77,v98
v_mul_f32 v98,v98,v97
v_mfma_f32_16x16x32_bf16 v[40:43],a[252:255],v[72:75],v[40:43]
s_cbranch_scc1 Lg1_a0w0_%=
s_cmp_ge_u32 s48,4194304
s_cselect_b64 s[90:91],s[94:95],s[90:91]
s_cselect_b64 s[92:93],s[96:97],s[92:93]
s_cmp_lt_u32 s67,s59
s_cbranch_scc1 Lg1_d0w0_%=
s_branch Lg1_slow0_%=
Lg1_a0w1_%=:
v_mfma_f32_16x16x32_bf16 v[28:31],a[32:35],v[44:47],0
s_add_u32 m0,s62,16384
ds_read_b128 v[72:75],v2 offset:3072
s_sub_u32 s69,s66,1048576
v_mfma_f32_16x16x32_bf16 v[32:35],a[36:39],v[44:47],0
s_cmp_lt_u32 s69,2097152
s_cselect_b32 s89,3145728,s89
s_sub_u32 s69,s66,3145728
v_mfma_f32_16x16x32_bf16 v[104:107],v[100:103],v[44:47],0
buffer_load_dwordx4 v1,s[36:39],s66 offen offset:0 sc1 lds
v_mfma_f32_16x16x32_bf16 v[108:111],v[44:47],v[44:47],0
s_cmp_lt_u32 s69,1048576
v_fma_f32 v36,-v96,v20,v36
v_fma_f32 v37,-v96,v21,v37
v_mfma_f32_16x16x32_bf16 v[28:31],a[40:43],v[48:51],v[28:31]
s_cselect_b32 s89,4194304,s89
ds_read_b128 v[44:47],v2 offset:12288
v_fma_f32 v38,-v96,v22,v38
v_mfma_f32_16x16x32_bf16 v[32:35],a[44:47],v[48:51],v[32:35]
s_lshr_b32 s73,s47,0
v_fma_f32 v39,-v96,v23,v39
v_mul_f32 v76,v36,v36
v_mfma_f32_16x16x32_bf16 v[104:107],v[100:103],v[48:51],v[104:107]
buffer_load_dwordx4 v1,s[36:39],s66 offen offset:1024 sc1 lds
v_mfma_f32_16x16x32_bf16 v[108:111],v[48:51],v[48:51],v[108:111]
s_lshr_b32 s72,s66,6
v_mul_f32 v77,v37,v37
v_mul_f32 v78,v38,v38
v_mfma_f32_16x16x32_bf16 v[28:31],a[48:51],v[52:55],v[28:31]
s_lshr_b32 s71,s62,2
ds_read_b128 v[48:51],v2 offset:13312
v_mul_f32 v79,v39,v39
v_mfma_f32_16x16x32_bf16 v[32:35],a[52:55],v[52:55],v[32:35]
s_cmp_lt_u32 s66,4194304
v_fma_f32 v76,v98,v76,v97
v_mul_f32 v76,v36,v76
v_mfma_f32_16x16x32_bf16 v[104:107],v[100:103],v[52:55],v[104:107]
buffer_load_dwordx4 v1,s[36:39],s66 offen offset:2048 sc1 lds
v_mfma_f32_16x16x32_bf16 v[108:111],v[52:55],v[52:55],v[108:111]
s_cselect_b32 s72,s59,s72
v_exp_f32 v76,v76
v_mfma_f32_16x16x32_bf16 v[28:31],a[56:59],v[56:59],v[28:31]
ds_read_b128 v[52:55],v2 offset:14336
v_fma_f32 v76,v76,v99,v99
v_mfma_f32_16x16x32_bf16 v[32:35],a[60:63],v[56:59],v[32:35]
v_rcp_f32 v76,v76
v_mfma_f32_16x16x32_bf16 v[104:107],v[100:103],v[56:59],v[104:107]
buffer_load_dwordx4 v1,s[36:39],s66 offen offset:3072 sc1 lds
v_mfma_f32_16x16x32_bf16 v[108:111],v[56:59],v[56:59],v[108:111]
s_waitcnt lgkmcnt(3)
v_fma_f32 v77,v98,v77,v97)"
R"(
v_mul_f32 v77,v37,v77
v_mfma_f32_16x16x32_bf16 v[28:31],a[0:3],v[60:63],v[28:31]
s_add_u32 m0,s71,s88
ds_read_b128 v[56:59],v2 offset:15360
v_fma_f32 v78,v98,v78,v97
v_mfma_f32_16x16x32_bf16 v[32:35],a[4:7],v[60:63],v[32:35]
buffer_load_dword v10,s[36:39],s72 offen offset:0 sc1 lds
v_mfma_f32_16x16x32_bf16 v[28:31],a[8:11],v[64:67],v[28:31]
s_xor_b32 s87,s64,65536
ds_read_b128 v[60:63],v2 offset:8192
v_mul_f32 v78,v38,v78
v_mfma_f32_16x16x32_bf16 v[32:35],a[12:15],v[64:67],v[32:35]
s_cmp_ge_u32 s66,s59
v_exp_f32 v77,v77
v_mfma_f32_16x16x32_bf16 v[28:31],a[16:19],v[68:71],v[28:31]
s_cselect_b32 s86,s49,s50
ds_read_b128 v[64:67],v2 offset:9216
v_fma_f32 v77,v77,v99,v99
v_mfma_f32_16x16x32_bf16 v[32:35],a[20:23],v[68:71],v[32:35]
s_cselect_b32 s87,s87,s63
v_exp_f32 v78,v78
v_mfma_f32_16x16x32_bf16 v[28:31],a[24:27],v[72:75],v[28:31]
s_mov_b32 s62,s87
ds_read_b128 v[68:71],v2 offset:10240
v_fma_f32 v78,v78,v99,v99
v_mfma_f32_16x16x32_bf16 v[32:35],a[28:31],v[72:75],v[32:35]
s_waitcnt lgkmcnt(3)
v_rcp_f32 v77,v77
v_mfma_f32_16x16x32_bf16 v[28:31],a[96:99],v[44:47],v[28:31]
s_waitcnt vmcnt(10)
ds_read_b128 v[72:75],v2 offset:11264
v_fma_f32 v79,v98,v79,v97
v_mfma_f32_16x16x32_bf16 v[32:35],a[100:103],v[44:47],v[32:35]
s_barrier
v_mfma_f32_16x16x32_bf16 v[28:31],a[104:107],v[48:51],v[28:31]
s_cmp_lt_u32 s86,s89
ds_read_b128 v[44:47],v2 offset:20480
ds_read_b32 v16,v7 offset:0
v_mfma_f32_16x16x32_bf16 v[32:35],a[108:111],v[48:51],v[32:35]
s_mov_b32 m0,s62
v_rcp_f32 v78,v78
v_mfma_f32_16x16x32_bf16 v[28:31],a[112:115],v[52:55],v[28:31]
ds_read_b128 v[48:51],v2 offset:21504
v_mul_f32 v79,v39,v79
v_mfma_f32_16x16x32_bf16 v[32:35],a[116:119],v[52:55],v[32:35]
v_exp_f32 v79,v79
v_mfma_f32_16x16x32_bf16 v[28:31],a[120:123],v[56:59],v[28:31]
s_waitcnt lgkmcnt(1)
ds_read_b128 v[52:55],v2 offset:22528
v_fma_f32 v79,v79,v99,v99
v_mfma_f32_16x16x32_bf16 v[32:35],a[124:127],v[56:59],v[32:35]
v_cmp_eq_u32 vcc,s60,v16
v_rcp_f32 v79,v79
v_mul_f32 v36,v36,v76
v_mfma_f32_16x16x32_bf16 v[28:31],a[64:67],v[60:63],v[28:31]
s_cselect_b32 vcc_lo,-1,vcc_lo
ds_read_b128 v[56:59],v2 offset:23552
v_mul_f32 v37,v37,v77
v_mfma_f32_16x16x32_bf16 v[32:35],a[68:71],v[60:63],v[32:35]
s_cmp_eq_u32 vcc_lo,-1
v_mul_f32 v38,v38,v78
v_mul_f32 v39,v39,v79
v_mfma_f32_16x16x32_bf16 v[28:31],a[72:75],v[64:67],v[28:31]
s_cselect_b32 s67,s86,s59
ds_read_b128 v[60:63],v2 offset:16384
v_cvt_pk_bf16_f32 v84,v36,v37
v_mfma_f32_16x16x32_bf16 v[32:35],a[76:79],v[64:67],v[32:35]
s_add_u32 s53,s52,s55
v_cvt_pk_bf16_f32 v85,v38,v39
v_fma_f32 v40,-v96,v24,v40
v_mfma_f32_16x16x32_bf16 v[28:31],a[80:83],v[68:71],v[28:31]
s_cmp_eq_u32 s53,s56
ds_read_b128 v[64:67],v2 offset:17408
v_fma_f32 v41,-v96,v25,v41
v_mfma_f32_16x16x32_bf16 v[32:35],a[84:87],v[68:71],v[32:35]
s_cselect_b32 s53,s57,s53
v_fma_f32 v42,-v96,v26,v42
v_fma_f32 v43,-v96,v27,v43
v_mfma_f32_16x16x32_bf16 v[28:31],a[88:91],v[72:75],v[28:31]
s_cmp_eq_u32 s53,s58
ds_read_b128 v[68:71],v2 offset:18432
v_mul_f32 v80,v40,v40
v_mfma_f32_16x16x32_bf16 v[32:35],a[92:95],v[72:75],v[32:35]
s_waitcnt lgkmcnt(3)
v_mul_f32 v81,v41,v41
v_mul_f32 v82,v42,v42
v_mfma_f32_16x16x32_bf16 v[28:31],a[160:163],v[44:47],v[28:31]
s_cselect_b32 s53,s59,s53
ds_read_b128 v[72:75],v2 offset:19456
v_mul_f32 v83,v43,v43
v_mfma_f32_16x16x32_bf16 v[32:35],a[164:167],v[44:47],v[32:35]
s_xor_b32 s63,s63,65536
v_fma_f32 v80,v98,v80,v97
v_mul_f32 v80,v40,v80
v_mfma_f32_16x16x32_bf16 v[104:107],v[100:103],v[44:47],v[104:107]
s_cmp_ge_u32 s66,s59
v_exp_f32 v80,v80
v_mfma_f32_16x16x32_bf16 v[108:111],v[44:47],v[44:47],v[108:111]
v_fma_f32 v80,v80,v99,v99
v_fma_f32 v81,v98,v81,v97
v_mfma_f32_16x16x32_bf16 v[28:31],a[168:171],v[48:51],v[28:31]
ds_read_b128 v[44:47],v2 offset:28672
v_mul_f32 v81,v41,v81
v_mfma_f32_16x16x32_bf16 v[32:35],a[172:175],v[48:51],v[32:35]
v_exp_f32 v81,v81
v_mfma_f32_16x16x32_bf16 v[104:107],v[100:103],v[48:51],v[104:107]
v_rcp_f32 v80,v80
v_mfma_f32_16x16x32_bf16 v[108:111],v[48:51],v[48:51],v[108:111]
v_fma_f32 v81,v81,v99,v99
v_fma_f32 v82,v98,v82,v97
v_mfma_f32_16x16x32_bf16 v[28:31],a[176:179],v[52:55],v[28:31]
ds_read_b128 v[48:51],v2 offset:29696
v_mul_f32 v82,v42,v82
v_mfma_f32_16x16x32_bf16 v[32:35],a[180:183],v[52:55],v[32:35]
v_exp_f32 v82,v82
v_mfma_f32_16x16x32_bf16 v[104:107],v[100:103],v[52:55],v[104:107]
v_rcp_f32 v81,v81
v_mfma_f32_16x16x32_bf16 v[108:111],v[52:55],v[52:55],v[108:111]
v_fma_f32 v82,v82,v99,v99
v_fma_f32 v83,v98,v83,v97
v_mfma_f32_16x16x32_bf16 v[28:31],a[184:187],v[56:59],v[28:31]
ds_read_b128 v[52:55],v2 offset:30720
v_mul_f32 v83,v43,v83
v_mfma_f32_16x16x32_bf16 v[32:35],a[188:191],v[56:59],v[32:35]
v_exp_f32 v83,v83
v_mfma_f32_16x16x32_bf16 v[104:107],v[100:103],v[56:59],v[104:107]
v_rcp_f32 v82,v82
v_mfma_f32_16x16x32_bf16 v[108:111],v[56:59],v[56:59],v[108:111]
s_waitcnt lgkmcnt(3)
v_fma_f32 v83,v83,v99,v99
v_mul_f32 v40,v40,v80
v_mfma_f32_16x16x32_bf16 v[28:31],a[128:131],v[60:63],v[28:31]
ds_read_b128 v[56:59],v2 offset:31744
v_mul_f32 v41,v41,v81
v_mfma_f32_16x16x32_bf16 v[32:35],a[132:135],v[60:63],v[32:35]
buffer_load_dwordx4 v0,s[36:39],s67 offen offset:0 sc1 lds
v_mfma_f32_16x16x32_bf16 v[28:31],a[136:139],v[64:67],v[28:31]
v_cndmask_b32 v112,v108,v109,s[82:83]
v_cndmask_b32 v113,v110,v111,s[82:83]
v_cndmask_b32 v112,v112,v113,s[84:85]
v_cndmask_b32 v112,v104,v112,s[98:99]
v_mfma_f32_16x16x32_bf16 v[32:35],a[140:143],v[64:67],v[32:35]
ds_read_b128 v[60:63],v2 offset:24576
ds_write_b32 v13,v112 offset:0
v_mfma_f32_16x16x32_bf16 v[28:31],a[144:147],v[68:71],v[28:31]
ds_read_b128 v[64:67],v2 offset:25600
v_mul_f32 v42,v42,v82
v_mfma_f32_16x16x32_bf16 v[32:35],a[148:151],v[68:71],v[32:35]
buffer_load_dwordx4 v0,s[36:39],s67 offen offset:1024 sc1 lds
v_mfma_f32_16x16x32_bf16 v[28:31],a[152:155],v[72:75],v[28:31]
ds_read_b128 v[68:71],v2 offset:26624
v_cvt_pk_bf16_f32 v86,v40,v41
v_mfma_f32_16x16x32_bf16 v[32:35],a[156:159],v[72:75],v[32:35]
s_waitcnt lgkmcnt(4)
v_rcp_f32 v83,v83
v_mfma_f32_16x16x32_bf16 v[28:31],a[224:227],v[44:47],v[28:31]
s_waitcnt vmcnt(7) lgkmcnt(2)
ds_read_b128 v[72:75],v2 offset:27648
v_mul_f32 v43,v43,v83
v_mfma_f32_16x16x32_bf16 v[32:35],a[228:231],v[44:47],v[32:35]
s_barrier
v_mfma_f32_16x16x32_bf16 v[28:31],a[232:235],v[48:51],v[28:31]
ds_read_b128 v[116:119],v14 offset:0
ds_read_b128 v[120:123],v14 offset:16
v_mfma_f32_16x16x32_bf16 v[32:35],a[236:239],v[48:51],v[32:35]
ds_read_b128 v[44:47],v2 offset:36864
buffer_load_dwordx4 v0,s[36:39],s67 offen offset:2048 sc1 lds
v_mfma_f32_16x16x32_bf16 v[28:31],a[240:243],v[52:55],v[28:31]
ds_read_b128 v[48:51],v2 offset:37888
v_cvt_pk_bf16_f32 v87,v42,v43
v_mfma_f32_16x16x32_bf16 v[32:35],a[244:247],v[52:55],v[32:35]
v_mfma_f32_16x16x32_bf16 v[28:31],a[248:251],v[56:59],v[28:31]
s_waitcnt lgkmcnt(2)
ds_read_b128 v[52:55],v2 offset:38912
v_add_f32 v116,v116,v117
v_mfma_f32_16x16x32_bf16 v[32:35],a[252:255],v[56:59],v[32:35]
buffer_load_dwordx4 v0,s[36:39],s67 offen offset:3072 sc1 lds
v_mfma_f32_16x16x32_bf16 v[28:31],a[192:195],v[60:63],v[28:31]
ds_read_b128 v[56:59],v2 offset:39936
v_add_f32 v118,v118,v119
v_mfma_f32_16x16x32_bf16 v[32:35],a[196:199],v[60:63],v[32:35]
v_add_f32 v116,v116,v118
v_mul_f32 v92,s75,v116
v_mfma_f32_16x16x32_bf16 v[28:31],a[200:203],v[64:67],v[28:31]
ds_read_b128 v[60:63],v2 offset:32768
v_add_f32 v120,v120,v121
v_mfma_f32_16x16x32_bf16 v[32:35],a[204:207],v[64:67],v[32:35]
buffer_store_dwordx4 v[84:87],v3,s[36:39],s73 offen offset:0 sc1
v_add_f32 v122,v122,v123
v_add_f32 v120,v120,v122
v_mul_f32 v120,s75,v120
v_mfma_f32_16x16x32_bf16 v[28:31],a[208:211],v[68:71],v[28:31]
ds_read_b128 v[64:67],v2 offset:33792
v_fma_f32 v120,-v92,v92,v120
v_mfma_f32_16x16x32_bf16 v[32:35],a[212:215],v[68:71],v[32:35]
v_max_f32 v120,0,v120
v_add_f32 v115,v15,v120
v_fma_f32 v120,v15,v115,v120
v_rsq_f32 v116,v120
v_mfma_f32_16x16x32_bf16 v[28:31],a[216:219],v[72:75],v[28:31])"
R"(
s_waitcnt lgkmcnt(2)
ds_read_b128 v[68:71],v2 offset:34816
v_mul_f32 v93,s76,v116
v_mul_f32 v94,v116,v116
v_mul_f32 v95,v120,v116
v_mul_f32 v94,s77,v94
v_mul_f32 v94,v94,v93
v_mfma_f32_16x16x32_bf16 v[32:35],a[220:223],v[72:75],v[32:35]
s_cbranch_scc1 Lg1_slow1_%=
Lg1_a1w1_%=:
v_mfma_f32_16x16x32_bf16 v[36:39],a[32:35],v[44:47],0
s_add_u32 m0,s62,16384
ds_read_b128 v[72:75],v2 offset:35840
s_sub_u32 s69,s67,1048576
v_mfma_f32_16x16x32_bf16 v[40:43],a[36:39],v[44:47],0
s_cmp_lt_u32 s69,2097152
s_cselect_b32 s89,3145728,s89
s_sub_u32 s69,s67,3145728
v_mfma_f32_16x16x32_bf16 v[104:107],v[100:103],v[44:47],0
buffer_load_dwordx4 v1,s[36:39],s67 offen offset:0 sc1 lds
v_mfma_f32_16x16x32_bf16 v[108:111],v[44:47],v[44:47],0
s_cmp_lt_u32 s69,1048576
v_fma_f32 v28,-v92,v20,v28
v_fma_f32 v29,-v92,v21,v29
v_mfma_f32_16x16x32_bf16 v[36:39],a[40:43],v[48:51],v[36:39]
s_cselect_b32 s89,4194304,s89
ds_read_b128 v[44:47],v2 offset:45056
v_fma_f32 v30,-v92,v22,v30
v_mfma_f32_16x16x32_bf16 v[40:43],a[44:47],v[48:51],v[40:43]
s_lshr_b32 s73,s48,0
v_fma_f32 v31,-v92,v23,v31
v_mul_f32 v76,v28,v28
v_mfma_f32_16x16x32_bf16 v[104:107],v[100:103],v[48:51],v[104:107]
buffer_load_dwordx4 v1,s[36:39],s67 offen offset:1024 sc1 lds
v_mfma_f32_16x16x32_bf16 v[108:111],v[48:51],v[48:51],v[108:111]
s_lshr_b32 s72,s67,6
v_mul_f32 v77,v29,v29
v_mul_f32 v78,v30,v30
v_mfma_f32_16x16x32_bf16 v[36:39],a[48:51],v[52:55],v[36:39]
s_lshr_b32 s71,s62,2
ds_read_b128 v[48:51],v2 offset:46080
v_mul_f32 v79,v31,v31
v_mfma_f32_16x16x32_bf16 v[40:43],a[52:55],v[52:55],v[40:43]
s_cmp_lt_u32 s67,4194304
v_fma_f32 v76,v94,v76,v93
v_mul_f32 v76,v28,v76
v_mfma_f32_16x16x32_bf16 v[104:107],v[100:103],v[52:55],v[104:107]
buffer_load_dwordx4 v1,s[36:39],s67 offen offset:2048 sc1 lds
v_mfma_f32_16x16x32_bf16 v[108:111],v[52:55],v[52:55],v[108:111]
s_cselect_b32 s72,s59,s72
v_exp_f32 v76,v76
v_mfma_f32_16x16x32_bf16 v[36:39],a[56:59],v[56:59],v[36:39]
s_mov_b64 s[44:45],s[46:47]
ds_read_b128 v[52:55],v2 offset:47104
v_fma_f32 v76,v76,v95,v95
v_mfma_f32_16x16x32_bf16 v[40:43],a[60:63],v[56:59],v[40:43]
s_mov_b64 s[46:47],s[48:49]
v_rcp_f32 v76,v76
v_mfma_f32_16x16x32_bf16 v[104:107],v[100:103],v[56:59],v[104:107]
buffer_load_dwordx4 v1,s[36:39],s67 offen offset:3072 sc1 lds
v_mfma_f32_16x16x32_bf16 v[108:111],v[56:59],v[56:59],v[108:111]
s_waitcnt lgkmcnt(3)
v_fma_f32 v77,v94,v77,v93
v_mul_f32 v77,v29,v77
v_mfma_f32_16x16x32_bf16 v[36:39],a[0:3],v[60:63],v[36:39]
s_add_u32 m0,s71,s88
ds_read_b128 v[56:59],v2 offset:48128
v_fma_f32 v78,v94,v78,v93
v_mfma_f32_16x16x32_bf16 v[40:43],a[4:7],v[60:63],v[40:43]
buffer_load_dword v10,s[36:39],s72 offen offset:0 sc1 lds
v_mfma_f32_16x16x32_bf16 v[36:39],a[8:11],v[64:67],v[36:39]
s_xor_b32 s87,s63,65536
ds_read_b128 v[60:63],v2 offset:40960
v_mul_f32 v78,v30,v78
v_mfma_f32_16x16x32_bf16 v[40:43],a[12:15],v[64:67],v[40:43]
s_cmp_ge_u32 s67,s59
v_exp_f32 v77,v77
v_mfma_f32_16x16x32_bf16 v[36:39],a[16:19],v[68:71],v[36:39]
s_cselect_b32 s86,s50,s51
ds_read_b128 v[64:67],v2 offset:41984
v_fma_f32 v77,v77,v95,v95
v_mfma_f32_16x16x32_bf16 v[40:43],a[20:23],v[68:71],v[40:43]
s_cselect_b32 s87,s87,s64
v_exp_f32 v78,v78
v_mfma_f32_16x16x32_bf16 v[36:39],a[24:27],v[72:75],v[36:39]
s_mov_b32 s62,s87
ds_read_b128 v[68:71],v2 offset:43008
v_fma_f32 v78,v78,v95,v95
v_mfma_f32_16x16x32_bf16 v[40:43],a[28:31],v[72:75],v[40:43]
s_waitcnt lgkmcnt(3)
v_rcp_f32 v77,v77
v_mfma_f32_16x16x32_bf16 v[36:39],a[96:99],v[44:47],v[36:39]
s_waitcnt vmcnt(10)
ds_read_b128 v[72:75],v2 offset:44032
v_fma_f32 v79,v94,v79,v93
v_mfma_f32_16x16x32_bf16 v[40:43],a[100:103],v[44:47],v[40:43]
s_barrier
v_mfma_f32_16x16x32_bf16 v[36:39],a[104:107],v[48:51],v[36:39]
s_cmp_lt_u32 s86,s89
ds_read_b128 v[44:47],v2 offset:53248
ds_read_b32 v16,v7 offset:256
v_mfma_f32_16x16x32_bf16 v[40:43],a[108:111],v[48:51],v[40:43]
s_mov_b32 m0,s62
v_rcp_f32 v78,v78
v_mfma_f32_16x16x32_bf16 v[36:39],a[112:115],v[52:55],v[36:39]
s_mov_b64 s[48:49],s[50:51]
ds_read_b128 v[48:51],v2 offset:54272
v_mul_f32 v79,v31,v79
v_mfma_f32_16x16x32_bf16 v[40:43],a[116:119],v[52:55],v[40:43]
s_mov_b64 s[50:51],s[52:53]
v_exp_f32 v79,v79
v_mfma_f32_16x16x32_bf16 v[36:39],a[120:123],v[56:59],v[36:39]
s_waitcnt lgkmcnt(1)
ds_read_b128 v[52:55],v2 offset:55296
v_fma_f32 v79,v79,v95,v95
v_mfma_f32_16x16x32_bf16 v[40:43],a[124:127],v[56:59],v[40:43]
v_cmp_eq_u32 vcc,s60,v16
v_rcp_f32 v79,v79
v_mul_f32 v28,v28,v76
v_mfma_f32_16x16x32_bf16 v[36:39],a[64:67],v[60:63],v[36:39]
s_cselect_b32 vcc_lo,-1,vcc_lo
ds_read_b128 v[56:59],v2 offset:56320
v_mul_f32 v29,v29,v77
v_mfma_f32_16x16x32_bf16 v[40:43],a[68:71],v[60:63],v[40:43]
s_cmp_eq_u32 vcc_lo,-1
v_mul_f32 v30,v30,v78
v_mul_f32 v31,v31,v79
v_mfma_f32_16x16x32_bf16 v[36:39],a[72:75],v[64:67],v[36:39]
s_cselect_b32 s66,s86,s59
ds_read_b128 v[60:63],v2 offset:49152
v_cvt_pk_bf16_f32 v84,v28,v29
v_mfma_f32_16x16x32_bf16 v[40:43],a[76:79],v[64:67],v[40:43]
s_add_u32 s74,s53,s55
v_cvt_pk_bf16_f32 v85,v30,v31
v_fma_f32 v32,-v92,v24,v32
v_mfma_f32_16x16x32_bf16 v[36:39],a[80:83],v[68:71],v[36:39]
s_cmp_eq_u32 s74,s56
ds_read_b128 v[64:67],v2 offset:50176
v_fma_f32 v33,-v92,v25,v33
v_mfma_f32_16x16x32_bf16 v[40:43],a[84:87],v[68:71],v[40:43]
s_cselect_b32 s74,s57,s74
v_fma_f32 v34,-v92,v26,v34
v_fma_f32 v35,-v92,v27,v35
v_mfma_f32_16x16x32_bf16 v[36:39],a[88:91],v[72:75],v[36:39]
s_cmp_eq_u32 s74,s58
ds_read_b128 v[68:71],v2 offset:51200
v_mul_f32 v80,v32,v32
v_mfma_f32_16x16x32_bf16 v[40:43],a[92:95],v[72:75],v[40:43]
s_waitcnt lgkmcnt(3)
v_mul_f32 v81,v33,v33
v_mul_f32 v82,v34,v34
v_mfma_f32_16x16x32_bf16 v[36:39],a[160:163],v[44:47],v[36:39]
s_cselect_b32 s74,s59,s74
ds_read_b128 v[72:75],v2 offset:52224
v_mul_f32 v83,v35,v35
v_mfma_f32_16x16x32_bf16 v[40:43],a[164:167],v[44:47],v[40:43]
s_xor_b32 s64,s64,65536
v_fma_f32 v80,v94,v80,v93
v_mul_f32 v80,v32,v80
v_mfma_f32_16x16x32_bf16 v[104:107],v[100:103],v[44:47],v[104:107]
s_mov_b32 s52,s74
v_exp_f32 v80,v80
v_mfma_f32_16x16x32_bf16 v[108:111],v[44:47],v[44:47],v[108:111]
s_cmp_lt_u32 s67,4194304
v_fma_f32 v80,v80,v95,v95
v_fma_f32 v81,v94,v81,v93
v_mfma_f32_16x16x32_bf16 v[36:39],a[168:171],v[48:51],v[36:39]
ds_read_b128 v[44:47],v2 offset:61440
v_mul_f32 v81,v33,v81
v_mfma_f32_16x16x32_bf16 v[40:43],a[172:175],v[48:51],v[40:43]
v_exp_f32 v81,v81
v_mfma_f32_16x16x32_bf16 v[104:107],v[100:103],v[48:51],v[104:107]
v_rcp_f32 v80,v80
v_mfma_f32_16x16x32_bf16 v[108:111],v[48:51],v[48:51],v[108:111]
v_fma_f32 v81,v81,v95,v95
v_fma_f32 v82,v94,v82,v93
v_mfma_f32_16x16x32_bf16 v[36:39],a[176:179],v[52:55],v[36:39]
ds_read_b128 v[48:51],v2 offset:62464
v_mul_f32 v82,v34,v82
v_mfma_f32_16x16x32_bf16 v[40:43],a[180:183],v[52:55],v[40:43]
v_exp_f32 v82,v82
v_mfma_f32_16x16x32_bf16 v[104:107],v[100:103],v[52:55],v[104:107]
v_rcp_f32 v81,v81
v_mfma_f32_16x16x32_bf16 v[108:111],v[52:55],v[52:55],v[108:111]
v_fma_f32 v82,v82,v95,v95
v_fma_f32 v83,v94,v83,v93
v_mfma_f32_16x16x32_bf16 v[36:39],a[184:187],v[56:59],v[36:39]
ds_read_b128 v[52:55],v2 offset:63488
v_mul_f32 v83,v35,v83
v_mfma_f32_16x16x32_bf16 v[40:43],a[188:191],v[56:59],v[40:43]
v_exp_f32 v83,v83
v_mfma_f32_16x16x32_bf16 v[104:107],v[100:103],v[56:59],v[104:107]
v_rcp_f32 v82,v82
v_mfma_f32_16x16x32_bf16 v[108:111],v[56:59],v[56:59],v[108:111]
s_waitcnt lgkmcnt(3)
v_fma_f32 v83,v83,v95,v95
v_mul_f32 v32,v32,v80
v_mfma_f32_16x16x32_bf16 v[36:39],a[128:131],v[60:63],v[36:39]
ds_read_b128 v[56:59],v2 offset:64512
v_mul_f32 v33,v33,v81)"
R"(
v_mfma_f32_16x16x32_bf16 v[40:43],a[132:135],v[60:63],v[40:43]
buffer_load_dwordx4 v0,s[36:39],s66 offen offset:0 sc1 lds
v_mfma_f32_16x16x32_bf16 v[36:39],a[136:139],v[64:67],v[36:39]
v_cndmask_b32 v112,v108,v109,s[82:83]
v_cndmask_b32 v113,v110,v111,s[82:83]
v_cndmask_b32 v112,v112,v113,s[84:85]
v_cndmask_b32 v112,v104,v112,s[98:99]
v_mfma_f32_16x16x32_bf16 v[40:43],a[140:143],v[64:67],v[40:43]
ds_read_b128 v[60:63],v2 offset:57344
ds_write_b32 v13,v112 offset:2048
v_mfma_f32_16x16x32_bf16 v[36:39],a[144:147],v[68:71],v[36:39]
ds_read_b128 v[64:67],v2 offset:58368
v_mul_f32 v34,v34,v82
v_mfma_f32_16x16x32_bf16 v[40:43],a[148:151],v[68:71],v[40:43]
buffer_load_dwordx4 v0,s[36:39],s66 offen offset:1024 sc1 lds
v_mfma_f32_16x16x32_bf16 v[36:39],a[152:155],v[72:75],v[36:39]
ds_read_b128 v[68:71],v2 offset:59392
v_cvt_pk_bf16_f32 v86,v32,v33
v_mfma_f32_16x16x32_bf16 v[40:43],a[156:159],v[72:75],v[40:43]
s_waitcnt lgkmcnt(4)
v_rcp_f32 v83,v83
v_mfma_f32_16x16x32_bf16 v[36:39],a[224:227],v[44:47],v[36:39]
s_waitcnt vmcnt(7) lgkmcnt(2)
ds_read_b128 v[72:75],v2 offset:60416
v_xor_b32 v2,65536,v2
v_mfma_f32_16x16x32_bf16 v[40:43],a[228:231],v[44:47],v[40:43]
s_barrier
v_mfma_f32_16x16x32_bf16 v[36:39],a[232:235],v[48:51],v[36:39]
ds_read_b128 v[116:119],v14 offset:2048
ds_read_b128 v[120:123],v14 offset:2064
v_mfma_f32_16x16x32_bf16 v[40:43],a[236:239],v[48:51],v[40:43]
ds_read_b128 v[44:47],v2 offset:4096
buffer_load_dwordx4 v0,s[36:39],s66 offen offset:2048 sc1 lds
v_mfma_f32_16x16x32_bf16 v[36:39],a[240:243],v[52:55],v[36:39]
ds_read_b128 v[48:51],v2 offset:5120
v_mul_f32 v35,v35,v83
v_mfma_f32_16x16x32_bf16 v[40:43],a[244:247],v[52:55],v[40:43]
v_cvt_pk_bf16_f32 v87,v34,v35
v_xor_b32 v8,16384,v8
v_mfma_f32_16x16x32_bf16 v[36:39],a[248:251],v[56:59],v[36:39]
s_waitcnt lgkmcnt(2)
ds_read_b128 v[52:55],v2 offset:6144
v_add_f32 v116,v116,v117
v_mfma_f32_16x16x32_bf16 v[40:43],a[252:255],v[56:59],v[40:43]
buffer_load_dwordx4 v0,s[36:39],s66 offen offset:3072 sc1 lds
v_mfma_f32_16x16x32_bf16 v[36:39],a[192:195],v[60:63],v[36:39]
ds_read_b128 v[56:59],v2 offset:7168
v_add_f32 v118,v118,v119
v_mfma_f32_16x16x32_bf16 v[40:43],a[196:199],v[60:63],v[40:43]
v_add_f32 v116,v116,v118
v_mul_f32 v96,s75,v116
v_mfma_f32_16x16x32_bf16 v[36:39],a[200:203],v[64:67],v[36:39]
ds_read_b128 v[60:63],v2 offset:0
v_add_f32 v120,v120,v121
v_mfma_f32_16x16x32_bf16 v[40:43],a[204:207],v[64:67],v[40:43]
buffer_store_dwordx4 v[84:87],v3,s[36:39],s73 offen offset:0 sc1
v_add_f32 v122,v122,v123
v_add_f32 v120,v120,v122
v_mul_f32 v120,s75,v120
v_mfma_f32_16x16x32_bf16 v[36:39],a[208:211],v[68:71],v[36:39]
ds_read_b128 v[64:67],v2 offset:1024
v_fma_f32 v120,-v96,v96,v120
v_mfma_f32_16x16x32_bf16 v[40:43],a[212:215],v[68:71],v[40:43]
v_max_f32 v120,0,v120
v_add_f32 v115,v15,v120
v_fma_f32 v120,v15,v115,v120
v_rsq_f32 v116,v120
v_mfma_f32_16x16x32_bf16 v[36:39],a[216:219],v[72:75],v[36:39]
s_waitcnt lgkmcnt(2)
ds_read_b128 v[68:71],v2 offset:2048
v_mul_f32 v97,s76,v116
v_mul_f32 v98,v116,v116
v_mul_f32 v99,v120,v116
v_mul_f32 v98,s77,v98
v_mul_f32 v98,v98,v97
v_mfma_f32_16x16x32_bf16 v[40:43],a[220:223],v[72:75],v[40:43]
s_cbranch_scc1 Lg1_a0w1_%=
s_cmp_ge_u32 s48,4194304
s_cselect_b64 s[90:91],s[94:95],s[90:91]
s_cselect_b64 s[92:93],s[96:97],s[92:93]
s_cmp_lt_u32 s67,s59
s_cbranch_scc1 Lg1_d0w1_%=
s_branch Lg1_slow0_%=
Lg1_a0w2_%=:
v_mfma_f32_16x16x32_bf16 v[28:31],a[64:67],v[44:47],0
s_sub_u32 s69,s66,1048576
ds_read_b128 v[72:75],v2 offset:15360
s_cmp_lt_u32 s69,2097152
v_mfma_f32_16x16x32_bf16 v[32:35],a[68:71],v[44:47],0
s_cselect_b32 s89,3145728,s89
s_sub_u32 s69,s66,3145728
s_cmp_lt_u32 s69,1048576
v_mfma_f32_16x16x32_bf16 v[104:107],v[100:103],v[44:47],0
s_cselect_b32 s89,4194304,s89
s_add_u32 m0,s62,16384
s_lshr_b32 s73,s47,0
v_mfma_f32_16x16x32_bf16 v[108:111],v[44:47],v[44:47],0
buffer_load_dwordx4 v1,s[36:39],s66 offen offset:0 sc1 lds
v_mfma_f32_16x16x32_bf16 v[28:31],a[72:75],v[48:51],v[28:31]
s_lshr_b32 s65,s48,6
ds_read_b128 v[44:47],v2 offset:0
v_fma_f32 v36,-v96,v20,v36
v_mfma_f32_16x16x32_bf16 v[32:35],a[76:79],v[48:51],v[32:35]
s_lshr_b32 s72,s66,6
v_fma_f32 v37,-v96,v21,v37
v_fma_f32 v38,-v96,v22,v38
v_mfma_f32_16x16x32_bf16 v[104:107],v[100:103],v[48:51],v[104:107]
s_lshr_b32 s71,s62,2
v_fma_f32 v39,-v96,v23,v39
v_mul_f32 v76,v36,v36
v_mfma_f32_16x16x32_bf16 v[108:111],v[48:51],v[48:51],v[108:111]
buffer_load_dwordx4 v1,s[36:39],s66 offen offset:1024 sc1 lds
v_mfma_f32_16x16x32_bf16 v[28:31],a[80:83],v[52:55],v[28:31]
s_cmp_lt_u32 s66,4194304
ds_read_b128 v[48:51],v2 offset:1024
v_mul_f32 v77,v37,v37
v_mfma_f32_16x16x32_bf16 v[32:35],a[84:87],v[52:55],v[32:35]
s_cselect_b32 s72,s59,s72
v_mul_f32 v78,v38,v38
v_mul_f32 v79,v39,v39
v_mfma_f32_16x16x32_bf16 v[104:107],v[100:103],v[52:55],v[104:107]
v_fma_f32 v76,v98,v76,v97
v_mul_f32 v76,v36,v76
v_mfma_f32_16x16x32_bf16 v[108:111],v[52:55],v[52:55],v[108:111]
buffer_load_dwordx4 v1,s[36:39],s66 offen offset:2048 sc1 lds
v_mfma_f32_16x16x32_bf16 v[28:31],a[88:91],v[56:59],v[28:31]
ds_read_b128 v[52:55],v2 offset:2048
v_fma_f32 v77,v98,v77,v97
v_mfma_f32_16x16x32_bf16 v[32:35],a[92:95],v[56:59],v[32:35]
v_exp_f32 v76,v76
v_mfma_f32_16x16x32_bf16 v[104:107],v[100:103],v[56:59],v[104:107]
v_mul_f32 v77,v37,v77
v_fma_f32 v76,v76,v99,v99
v_mfma_f32_16x16x32_bf16 v[108:111],v[56:59],v[56:59],v[108:111]
s_waitcnt lgkmcnt(3)
buffer_load_dwordx4 v1,s[36:39],s66 offen offset:3072 sc1 lds
v_mfma_f32_16x16x32_bf16 v[28:31],a[96:99],v[60:63],v[28:31]
s_add_u32 m0,s71,s88
ds_read_b128 v[56:59],v2 offset:3072
v_fma_f32 v78,v98,v78,v97
v_mfma_f32_16x16x32_bf16 v[32:35],a[100:103],v[60:63],v[32:35]
buffer_load_dword v10,s[36:39],s72 offen offset:0 sc1 lds
v_mfma_f32_16x16x32_bf16 v[28:31],a[104:107],v[64:67],v[28:31]
s_xor_b32 s87,s64,65536
ds_read_b128 v[60:63],v2 offset:4096
v_fma_f32 v79,v98,v79,v97
v_mfma_f32_16x16x32_bf16 v[32:35],a[108:111],v[64:67],v[32:35]
s_cmp_ge_u32 s66,s59
v_exp_f32 v77,v77
v_mfma_f32_16x16x32_bf16 v[28:31],a[112:115],v[68:71],v[28:31]
s_cselect_b32 s86,s49,s50
ds_read_b128 v[64:67],v2 offset:5120
v_mul_f32 v78,v38,v78
v_mfma_f32_16x16x32_bf16 v[32:35],a[116:119],v[68:71],v[32:35]
s_cselect_b32 s87,s87,s63
v_exp_f32 v78,v78
v_mfma_f32_16x16x32_bf16 v[28:31],a[120:123],v[72:75],v[28:31]
s_mov_b32 s62,s87
ds_read_b128 v[68:71],v2 offset:6144
v_mul_f32 v79,v39,v79
v_mfma_f32_16x16x32_bf16 v[32:35],a[124:127],v[72:75],v[32:35]
s_waitcnt lgkmcnt(3)
v_exp_f32 v79,v79
v_mfma_f32_16x16x32_bf16 v[28:31],a[0:3],v[44:47],v[28:31]
s_waitcnt vmcnt(10)
ds_read_b128 v[72:75],v2 offset:7168
v_fma_f32 v77,v77,v99,v99
v_mfma_f32_16x16x32_bf16 v[32:35],a[4:7],v[44:47],v[32:35]
s_barrier
v_mfma_f32_16x16x32_bf16 v[28:31],a[8:11],v[48:51],v[28:31]
s_cmp_lt_u32 s86,s89
ds_read_b128 v[44:47],v2 offset:24576
ds_read_b32 v16,v7 offset:0
v_mfma_f32_16x16x32_bf16 v[32:35],a[12:15],v[48:51],v[32:35]
s_mov_b32 m0,s62
v_rcp_f32 v76,v76
v_mfma_f32_16x16x32_bf16 v[28:31],a[16:19],v[52:55],v[28:31]
ds_read_b128 v[48:51],v2 offset:25600
v_fma_f32 v78,v78,v99,v99
v_mfma_f32_16x16x32_bf16 v[32:35],a[20:23],v[52:55],v[32:35]
v_rcp_f32 v77,v77
v_mfma_f32_16x16x32_bf16 v[28:31],a[24:27],v[56:59],v[28:31]
s_waitcnt lgkmcnt(1)
ds_read_b128 v[52:55],v2 offset:26624
v_fma_f32 v79,v79,v99,v99
v_mfma_f32_16x16x32_bf16 v[32:35],a[28:31],v[56:59],v[32:35]
v_cmp_eq_u32 vcc,s60,v16
v_rcp_f32 v78,v78
v_mul_f32 v36,v36,v76
v_mfma_f32_16x16x32_bf16 v[28:31],a[32:35],v[60:63],v[28:31]
s_cselect_b32 vcc_lo,-1,vcc_lo
ds_read_b128 v[56:59],v2 offset:27648
v_mul_f32 v37,v37,v77
v_mfma_f32_16x16x32_bf16 v[32:35],a[36:39],v[60:63],v[32:35]
s_cmp_eq_u32 vcc_lo,-1
v_rcp_f32 v79,v79
v_mfma_f32_16x16x32_bf16 v[28:31],a[40:43],v[64:67],v[28:31]
s_cselect_b32 s67,s86,s59
ds_read_b128 v[60:63],v2 offset:28672
v_mul_f32 v38,v38,v78)"
R"(
v_mfma_f32_16x16x32_bf16 v[32:35],a[44:47],v[64:67],v[32:35]
s_add_u32 s53,s52,s55
v_mul_f32 v39,v39,v79
v_cvt_pk_bf16_f32 v84,v36,v37
v_mfma_f32_16x16x32_bf16 v[28:31],a[48:51],v[68:71],v[28:31]
s_cmp_eq_u32 s53,s56
ds_read_b128 v[64:67],v2 offset:29696
v_cvt_pk_bf16_f32 v85,v38,v39
v_mfma_f32_16x16x32_bf16 v[32:35],a[52:55],v[68:71],v[32:35]
s_cselect_b32 s53,s57,s53
v_fma_f32 v40,-v96,v24,v40
v_fma_f32 v41,-v96,v25,v41
v_mfma_f32_16x16x32_bf16 v[28:31],a[56:59],v[72:75],v[28:31]
s_cmp_eq_u32 s53,s58
ds_read_b128 v[68:71],v2 offset:30720
v_fma_f32 v42,-v96,v26,v42
v_mfma_f32_16x16x32_bf16 v[32:35],a[60:63],v[72:75],v[32:35]
s_waitcnt lgkmcnt(3)
v_fma_f32 v43,-v96,v27,v43
v_mul_f32 v80,v40,v40
v_mfma_f32_16x16x32_bf16 v[28:31],a[192:195],v[44:47],v[28:31]
s_cselect_b32 s53,s59,s53
ds_read_b128 v[72:75],v2 offset:31744
v_mul_f32 v81,v41,v41
v_mfma_f32_16x16x32_bf16 v[32:35],a[196:199],v[44:47],v[32:35]
s_xor_b32 s63,s63,65536
v_mul_f32 v82,v42,v42
v_mul_f32 v83,v43,v43
v_mfma_f32_16x16x32_bf16 v[104:107],v[100:103],v[44:47],v[104:107]
s_cmp_ge_u32 s66,s59
v_fma_f32 v80,v98,v80,v97
v_mul_f32 v80,v40,v80
v_mfma_f32_16x16x32_bf16 v[108:111],v[44:47],v[44:47],v[108:111]
v_exp_f32 v80,v80
v_mfma_f32_16x16x32_bf16 v[28:31],a[200:203],v[48:51],v[28:31]
ds_read_b128 v[44:47],v2 offset:16384
v_fma_f32 v80,v80,v99,v99
v_mfma_f32_16x16x32_bf16 v[32:35],a[204:207],v[48:51],v[32:35]
v_rcp_f32 v80,v80
v_mfma_f32_16x16x32_bf16 v[104:107],v[100:103],v[48:51],v[104:107]
v_fma_f32 v81,v98,v81,v97
v_mul_f32 v81,v41,v81
v_mfma_f32_16x16x32_bf16 v[108:111],v[48:51],v[48:51],v[108:111]
v_exp_f32 v81,v81
v_mfma_f32_16x16x32_bf16 v[28:31],a[208:211],v[52:55],v[28:31]
ds_read_b128 v[48:51],v2 offset:17408
v_fma_f32 v81,v81,v99,v99
v_mfma_f32_16x16x32_bf16 v[32:35],a[212:215],v[52:55],v[32:35]
v_rcp_f32 v81,v81
v_mfma_f32_16x16x32_bf16 v[104:107],v[100:103],v[52:55],v[104:107]
v_fma_f32 v82,v98,v82,v97
v_mul_f32 v82,v42,v82
v_mfma_f32_16x16x32_bf16 v[108:111],v[52:55],v[52:55],v[108:111]
v_exp_f32 v82,v82
v_mfma_f32_16x16x32_bf16 v[28:31],a[216:219],v[56:59],v[28:31]
ds_read_b128 v[52:55],v2 offset:18432
v_fma_f32 v82,v82,v99,v99
v_mfma_f32_16x16x32_bf16 v[32:35],a[220:223],v[56:59],v[32:35]
v_rcp_f32 v82,v82
v_mfma_f32_16x16x32_bf16 v[104:107],v[100:103],v[56:59],v[104:107]
v_fma_f32 v83,v98,v83,v97
v_mul_f32 v83,v43,v83
v_mfma_f32_16x16x32_bf16 v[108:111],v[56:59],v[56:59],v[108:111]
s_waitcnt lgkmcnt(3)
v_exp_f32 v83,v83
v_mfma_f32_16x16x32_bf16 v[28:31],a[224:227],v[60:63],v[28:31]
ds_read_b128 v[56:59],v2 offset:19456
v_fma_f32 v83,v83,v99,v99
v_mfma_f32_16x16x32_bf16 v[32:35],a[228:231],v[60:63],v[32:35]
v_rcp_f32 v83,v83
v_mfma_f32_16x16x32_bf16 v[28:31],a[232:235],v[64:67],v[28:31]
buffer_load_dwordx4 v0,s[36:39],s67 offen offset:0 sc1 lds
v_cndmask_b32 v112,v108,v109,s[82:83]
v_cndmask_b32 v113,v110,v111,s[82:83]
v_cndmask_b32 v112,v112,v113,s[84:85]
v_cndmask_b32 v112,v104,v112,s[98:99]
v_mfma_f32_16x16x32_bf16 v[32:35],a[236:239],v[64:67],v[32:35]
ds_read_b128 v[60:63],v2 offset:20480
ds_write_b32 v13,v112 offset:0
v_mfma_f32_16x16x32_bf16 v[28:31],a[240:243],v[68:71],v[28:31]
ds_read_b128 v[64:67],v2 offset:21504
v_mul_f32 v40,v40,v80
v_mfma_f32_16x16x32_bf16 v[32:35],a[244:247],v[68:71],v[32:35]
v_mul_f32 v41,v41,v81
v_mul_f32 v42,v42,v82
v_mfma_f32_16x16x32_bf16 v[28:31],a[248:251],v[72:75],v[28:31]
buffer_load_dwordx4 v0,s[36:39],s67 offen offset:1024 sc1 lds
v_mfma_f32_16x16x32_bf16 v[32:35],a[252:255],v[72:75],v[32:35]
s_waitcnt lgkmcnt(3)
ds_read_b128 v[68:71],v2 offset:22528
v_mul_f32 v43,v43,v83
v_mfma_f32_16x16x32_bf16 v[28:31],a[128:131],v[44:47],v[28:31]
s_waitcnt vmcnt(7) lgkmcnt(2)
ds_read_b128 v[72:75],v2 offset:23552
v_cvt_pk_bf16_f32 v86,v40,v41
v_mfma_f32_16x16x32_bf16 v[32:35],a[132:135],v[44:47],v[32:35]
s_barrier
v_mfma_f32_16x16x32_bf16 v[28:31],a[136:139],v[48:51],v[28:31]
ds_read_b128 v[116:119],v14 offset:0
ds_read_b128 v[120:123],v14 offset:16
v_mfma_f32_16x16x32_bf16 v[32:35],a[140:143],v[48:51],v[32:35]
ds_read_b128 v[44:47],v2 offset:40960
v_cvt_pk_bf16_f32 v87,v42,v43
v_mfma_f32_16x16x32_bf16 v[28:31],a[144:147],v[52:55],v[28:31]
buffer_load_dwordx4 v0,s[36:39],s67 offen offset:2048 sc1 lds
v_mfma_f32_16x16x32_bf16 v[32:35],a[148:151],v[52:55],v[32:35]
ds_read_b128 v[48:51],v2 offset:41984
v_mfma_f32_16x16x32_bf16 v[28:31],a[152:155],v[56:59],v[28:31]
s_waitcnt lgkmcnt(2)
ds_read_b128 v[52:55],v2 offset:43008
v_add_f32 v116,v116,v117
v_mfma_f32_16x16x32_bf16 v[32:35],a[156:159],v[56:59],v[32:35]
v_add_f32 v118,v118,v119
v_add_f32 v116,v116,v118
v_mfma_f32_16x16x32_bf16 v[28:31],a[160:163],v[60:63],v[28:31]
buffer_load_dwordx4 v0,s[36:39],s67 offen offset:3072 sc1 lds
v_mfma_f32_16x16x32_bf16 v[32:35],a[164:167],v[60:63],v[32:35]
ds_read_b128 v[56:59],v2 offset:44032
v_mul_f32 v92,s75,v116
v_mfma_f32_16x16x32_bf16 v[28:31],a[168:171],v[64:67],v[28:31]
ds_read_b128 v[60:63],v2 offset:45056
v_add_f32 v120,v120,v121
v_mfma_f32_16x16x32_bf16 v[32:35],a[172:175],v[64:67],v[32:35]
v_add_f32 v122,v122,v123
v_add_f32 v120,v120,v122
v_mfma_f32_16x16x32_bf16 v[28:31],a[176:179],v[68:71],v[28:31]
buffer_store_dwordx4 v[84:87],v3,s[36:39],s73 offen offset:0 sc1
v_mul_f32 v120,s75,v120
v_fma_f32 v120,-v92,v92,v120
v_max_f32 v120,0,v120
v_add_f32 v115,v15,v120
v_fma_f32 v120,v15,v115,v120
v_rsq_f32 v115,v115
v_mfma_f32_16x16x32_bf16 v[32:35],a[180:183],v[68:71],v[32:35]
ds_read_b128 v[64:67],v2 offset:46080
v_rsq_f32 v116,v120
v_mul_f32_e64 v114,-v92,v115
v_mfma_f32_16x16x32_bf16 v[28:31],a[184:187],v[72:75],v[28:31]
s_waitcnt lgkmcnt(2)
ds_read_b128 v[68:71],v2 offset:47104
buffer_store_dwordx2 v[114:115],v12,s[36:39],s65 offen offset:0 sc1
v_mul_f32 v93,s76,v116
v_mul_f32 v94,v116,v116
v_mul_f32 v95,v120,v116
v_mul_f32 v94,s77,v94
v_mul_f32 v94,v94,v93
v_mfma_f32_16x16x32_bf16 v[32:35],a[188:191],v[72:75],v[32:35]
s_cbranch_scc1 Lg1_slow1_%=
Lg1_a1w2_%=:
v_mfma_f32_16x16x32_bf16 v[36:39],a[64:67],v[44:47],0
s_sub_u32 s69,s67,1048576
ds_read_b128 v[72:75],v2 offset:48128
s_cmp_lt_u32 s69,2097152
v_mfma_f32_16x16x32_bf16 v[40:43],a[68:71],v[44:47],0
s_cselect_b32 s89,3145728,s89
s_sub_u32 s69,s67,3145728
s_cmp_lt_u32 s69,1048576
v_mfma_f32_16x16x32_bf16 v[104:107],v[100:103],v[44:47],0
s_cselect_b32 s89,4194304,s89
s_add_u32 m0,s62,16384
s_lshr_b32 s73,s48,0
v_mfma_f32_16x16x32_bf16 v[108:111],v[44:47],v[44:47],0
buffer_load_dwordx4 v1,s[36:39],s67 offen offset:0 sc1 lds
v_mfma_f32_16x16x32_bf16 v[36:39],a[72:75],v[48:51],v[36:39]
s_lshr_b32 s65,s49,6
ds_read_b128 v[44:47],v2 offset:32768
v_fma_f32 v28,-v92,v20,v28
v_mfma_f32_16x16x32_bf16 v[40:43],a[76:79],v[48:51],v[40:43]
s_lshr_b32 s72,s67,6
v_fma_f32 v29,-v92,v21,v29
v_fma_f32 v30,-v92,v22,v30
v_mfma_f32_16x16x32_bf16 v[104:107],v[100:103],v[48:51],v[104:107]
s_lshr_b32 s71,s62,2
v_fma_f32 v31,-v92,v23,v31
v_mul_f32 v76,v28,v28
v_mfma_f32_16x16x32_bf16 v[108:111],v[48:51],v[48:51],v[108:111]
buffer_load_dwordx4 v1,s[36:39],s67 offen offset:1024 sc1 lds
v_mfma_f32_16x16x32_bf16 v[36:39],a[80:83],v[52:55],v[36:39]
s_cmp_lt_u32 s67,4194304
ds_read_b128 v[48:51],v2 offset:33792
v_mul_f32 v77,v29,v29
v_mfma_f32_16x16x32_bf16 v[40:43],a[84:87],v[52:55],v[40:43]
s_cselect_b32 s72,s59,s72
v_mul_f32 v78,v30,v30
v_mul_f32 v79,v31,v31
v_mfma_f32_16x16x32_bf16 v[104:107],v[100:103],v[52:55],v[104:107]
s_mov_b64 s[44:45],s[46:47]
v_fma_f32 v76,v94,v76,v93
v_mul_f32 v76,v28,v76
v_mfma_f32_16x16x32_bf16 v[108:111],v[52:55],v[52:55],v[108:111]
buffer_load_dwordx4 v1,s[36:39],s67 offen offset:2048 sc1 lds
v_mfma_f32_16x16x32_bf16 v[36:39],a[88:91],v[56:59],v[36:39]
s_mov_b64 s[46:47],s[48:49]
ds_read_b128 v[52:55],v2 offset:34816
v_fma_f32 v77,v94,v77,v93
v_mfma_f32_16x16x32_bf16 v[40:43],a[92:95],v[56:59],v[40:43]
s_mov_b64 s[48:49],s[50:51]
v_exp_f32 v76,v76
v_mfma_f32_16x16x32_bf16 v[104:107],v[100:103],v[56:59],v[104:107]
v_mul_f32 v77,v29,v77)"
R"(
v_fma_f32 v76,v76,v95,v95
v_mfma_f32_16x16x32_bf16 v[108:111],v[56:59],v[56:59],v[108:111]
s_waitcnt lgkmcnt(3)
buffer_load_dwordx4 v1,s[36:39],s67 offen offset:3072 sc1 lds
v_mfma_f32_16x16x32_bf16 v[36:39],a[96:99],v[60:63],v[36:39]
s_add_u32 m0,s71,s88
ds_read_b128 v[56:59],v2 offset:35840
v_fma_f32 v78,v94,v78,v93
v_mfma_f32_16x16x32_bf16 v[40:43],a[100:103],v[60:63],v[40:43]
buffer_load_dword v10,s[36:39],s72 offen offset:0 sc1 lds
v_mfma_f32_16x16x32_bf16 v[36:39],a[104:107],v[64:67],v[36:39]
s_xor_b32 s87,s63,65536
ds_read_b128 v[60:63],v2 offset:36864
v_fma_f32 v79,v94,v79,v93
v_mfma_f32_16x16x32_bf16 v[40:43],a[108:111],v[64:67],v[40:43]
s_cmp_ge_u32 s67,s59
v_exp_f32 v77,v77
v_mfma_f32_16x16x32_bf16 v[36:39],a[112:115],v[68:71],v[36:39]
s_cselect_b32 s86,s50,s51
ds_read_b128 v[64:67],v2 offset:37888
v_mul_f32 v78,v30,v78
v_mfma_f32_16x16x32_bf16 v[40:43],a[116:119],v[68:71],v[40:43]
s_cselect_b32 s87,s87,s64
v_exp_f32 v78,v78
v_mfma_f32_16x16x32_bf16 v[36:39],a[120:123],v[72:75],v[36:39]
s_mov_b32 s62,s87
ds_read_b128 v[68:71],v2 offset:38912
v_mul_f32 v79,v31,v79
v_mfma_f32_16x16x32_bf16 v[40:43],a[124:127],v[72:75],v[40:43]
s_waitcnt lgkmcnt(3)
v_exp_f32 v79,v79
v_mfma_f32_16x16x32_bf16 v[36:39],a[0:3],v[44:47],v[36:39]
s_waitcnt vmcnt(10)
ds_read_b128 v[72:75],v2 offset:39936
v_fma_f32 v77,v77,v95,v95
v_mfma_f32_16x16x32_bf16 v[40:43],a[4:7],v[44:47],v[40:43]
s_barrier
v_mfma_f32_16x16x32_bf16 v[36:39],a[8:11],v[48:51],v[36:39]
s_cmp_lt_u32 s86,s89
ds_read_b128 v[44:47],v2 offset:57344
ds_read_b32 v16,v7 offset:256
v_mfma_f32_16x16x32_bf16 v[40:43],a[12:15],v[48:51],v[40:43]
s_mov_b32 m0,s62
v_rcp_f32 v76,v76
v_mfma_f32_16x16x32_bf16 v[36:39],a[16:19],v[52:55],v[36:39]
s_mov_b64 s[50:51],s[52:53]
ds_read_b128 v[48:51],v2 offset:58368
v_fma_f32 v78,v78,v95,v95
v_mfma_f32_16x16x32_bf16 v[40:43],a[20:23],v[52:55],v[40:43]
v_rcp_f32 v77,v77
v_mfma_f32_16x16x32_bf16 v[36:39],a[24:27],v[56:59],v[36:39]
s_waitcnt lgkmcnt(1)
ds_read_b128 v[52:55],v2 offset:59392
v_fma_f32 v79,v79,v95,v95
v_mfma_f32_16x16x32_bf16 v[40:43],a[28:31],v[56:59],v[40:43]
v_cmp_eq_u32 vcc,s60,v16
v_rcp_f32 v78,v78
v_mul_f32 v28,v28,v76
v_mfma_f32_16x16x32_bf16 v[36:39],a[32:35],v[60:63],v[36:39]
s_cselect_b32 vcc_lo,-1,vcc_lo
ds_read_b128 v[56:59],v2 offset:60416
v_mul_f32 v29,v29,v77
v_mfma_f32_16x16x32_bf16 v[40:43],a[36:39],v[60:63],v[40:43]
s_cmp_eq_u32 vcc_lo,-1
v_rcp_f32 v79,v79
v_mfma_f32_16x16x32_bf16 v[36:39],a[40:43],v[64:67],v[36:39]
s_cselect_b32 s66,s86,s59
ds_read_b128 v[60:63],v2 offset:61440
v_mul_f32 v30,v30,v78
v_mfma_f32_16x16x32_bf16 v[40:43],a[44:47],v[64:67],v[40:43]
s_add_u32 s74,s53,s55
v_mul_f32 v31,v31,v79
v_cvt_pk_bf16_f32 v84,v28,v29
v_mfma_f32_16x16x32_bf16 v[36:39],a[48:51],v[68:71],v[36:39]
s_cmp_eq_u32 s74,s56
ds_read_b128 v[64:67],v2 offset:62464
v_cvt_pk_bf16_f32 v85,v30,v31
v_mfma_f32_16x16x32_bf16 v[40:43],a[52:55],v[68:71],v[40:43]
s_cselect_b32 s74,s57,s74
v_fma_f32 v32,-v92,v24,v32
v_fma_f32 v33,-v92,v25,v33
v_mfma_f32_16x16x32_bf16 v[36:39],a[56:59],v[72:75],v[36:39]
s_cmp_eq_u32 s74,s58
ds_read_b128 v[68:71],v2 offset:63488
v_fma_f32 v34,-v92,v26,v34
v_mfma_f32_16x16x32_bf16 v[40:43],a[60:63],v[72:75],v[40:43]
s_waitcnt lgkmcnt(3)
v_fma_f32 v35,-v92,v27,v35
v_mul_f32 v80,v32,v32
v_mfma_f32_16x16x32_bf16 v[36:39],a[192:195],v[44:47],v[36:39]
s_cselect_b32 s74,s59,s74
ds_read_b128 v[72:75],v2 offset:64512
v_mul_f32 v81,v33,v33
v_mfma_f32_16x16x32_bf16 v[40:43],a[196:199],v[44:47],v[40:43]
s_xor_b32 s64,s64,65536
v_mul_f32 v82,v34,v34
v_mul_f32 v83,v35,v35
v_mfma_f32_16x16x32_bf16 v[104:107],v[100:103],v[44:47],v[104:107]
s_mov_b32 s52,s74
v_fma_f32 v80,v94,v80,v93
v_mul_f32 v80,v32,v80
v_mfma_f32_16x16x32_bf16 v[108:111],v[44:47],v[44:47],v[108:111]
s_cmp_lt_u32 s67,4194304
v_exp_f32 v80,v80
v_mfma_f32_16x16x32_bf16 v[36:39],a[200:203],v[48:51],v[36:39]
ds_read_b128 v[44:47],v2 offset:49152
v_fma_f32 v80,v80,v95,v95
v_mfma_f32_16x16x32_bf16 v[40:43],a[204:207],v[48:51],v[40:43]
v_rcp_f32 v80,v80
v_mfma_f32_16x16x32_bf16 v[104:107],v[100:103],v[48:51],v[104:107]
v_fma_f32 v81,v94,v81,v93
v_mul_f32 v81,v33,v81
v_mfma_f32_16x16x32_bf16 v[108:111],v[48:51],v[48:51],v[108:111]
v_exp_f32 v81,v81
v_mfma_f32_16x16x32_bf16 v[36:39],a[208:211],v[52:55],v[36:39]
ds_read_b128 v[48:51],v2 offset:50176
v_fma_f32 v81,v81,v95,v95
v_mfma_f32_16x16x32_bf16 v[40:43],a[212:215],v[52:55],v[40:43]
v_rcp_f32 v81,v81
v_mfma_f32_16x16x32_bf16 v[104:107],v[100:103],v[52:55],v[104:107]
v_fma_f32 v82,v94,v82,v93
v_mul_f32 v82,v34,v82
v_mfma_f32_16x16x32_bf16 v[108:111],v[52:55],v[52:55],v[108:111]
v_exp_f32 v82,v82
v_mfma_f32_16x16x32_bf16 v[36:39],a[216:219],v[56:59],v[36:39]
ds_read_b128 v[52:55],v2 offset:51200
v_fma_f32 v82,v82,v95,v95
v_mfma_f32_16x16x32_bf16 v[40:43],a[220:223],v[56:59],v[40:43]
v_rcp_f32 v82,v82
v_mfma_f32_16x16x32_bf16 v[104:107],v[100:103],v[56:59],v[104:107]
v_fma_f32 v83,v94,v83,v93
v_mul_f32 v83,v35,v83
v_mfma_f32_16x16x32_bf16 v[108:111],v[56:59],v[56:59],v[108:111]
s_waitcnt lgkmcnt(3)
v_exp_f32 v83,v83
v_mfma_f32_16x16x32_bf16 v[36:39],a[224:227],v[60:63],v[36:39]
ds_read_b128 v[56:59],v2 offset:52224
v_fma_f32 v83,v83,v95,v95
v_mfma_f32_16x16x32_bf16 v[40:43],a[228:231],v[60:63],v[40:43]
v_rcp_f32 v83,v83
v_mfma_f32_16x16x32_bf16 v[36:39],a[232:235],v[64:67],v[36:39]
buffer_load_dwordx4 v0,s[36:39],s66 offen offset:0 sc1 lds
v_cndmask_b32 v112,v108,v109,s[82:83]
v_cndmask_b32 v113,v110,v111,s[82:83]
v_cndmask_b32 v112,v112,v113,s[84:85]
v_cndmask_b32 v112,v104,v112,s[98:99]
v_mfma_f32_16x16x32_bf16 v[40:43],a[236:239],v[64:67],v[40:43]
ds_read_b128 v[60:63],v2 offset:53248
ds_write_b32 v13,v112 offset:2048
v_mfma_f32_16x16x32_bf16 v[36:39],a[240:243],v[68:71],v[36:39]
ds_read_b128 v[64:67],v2 offset:54272
v_mul_f32 v32,v32,v80
v_mfma_f32_16x16x32_bf16 v[40:43],a[244:247],v[68:71],v[40:43]
v_mul_f32 v33,v33,v81
v_mul_f32 v34,v34,v82
v_mfma_f32_16x16x32_bf16 v[36:39],a[248:251],v[72:75],v[36:39]
buffer_load_dwordx4 v0,s[36:39],s66 offen offset:1024 sc1 lds
v_mfma_f32_16x16x32_bf16 v[40:43],a[252:255],v[72:75],v[40:43]
s_waitcnt lgkmcnt(3)
ds_read_b128 v[68:71],v2 offset:55296
v_mul_f32 v35,v35,v83
v_mfma_f32_16x16x32_bf16 v[36:39],a[128:131],v[44:47],v[36:39]
s_waitcnt vmcnt(7) lgkmcnt(2)
ds_read_b128 v[72:75],v2 offset:56320
v_xor_b32 v2,65536,v2
v_mfma_f32_16x16x32_bf16 v[40:43],a[132:135],v[44:47],v[40:43]
s_barrier
v_mfma_f32_16x16x32_bf16 v[36:39],a[136:139],v[48:51],v[36:39]
ds_read_b128 v[116:119],v14 offset:2048
ds_read_b128 v[120:123],v14 offset:2064
v_mfma_f32_16x16x32_bf16 v[40:43],a[140:143],v[48:51],v[40:43]
ds_read_b128 v[44:47],v2 offset:8192
v_cvt_pk_bf16_f32 v86,v32,v33
v_mfma_f32_16x16x32_bf16 v[36:39],a[144:147],v[52:55],v[36:39]
buffer_load_dwordx4 v0,s[36:39],s66 offen offset:2048 sc1 lds
v_mfma_f32_16x16x32_bf16 v[40:43],a[148:151],v[52:55],v[40:43]
ds_read_b128 v[48:51],v2 offset:9216
v_cvt_pk_bf16_f32 v87,v34,v35
v_mfma_f32_16x16x32_bf16 v[36:39],a[152:155],v[56:59],v[36:39]
s_waitcnt lgkmcnt(2)
ds_read_b128 v[52:55],v2 offset:10240
v_xor_b32 v8,16384,v8
v_mfma_f32_16x16x32_bf16 v[40:43],a[156:159],v[56:59],v[40:43]
v_add_f32 v116,v116,v117
v_add_f32 v118,v118,v119
v_mfma_f32_16x16x32_bf16 v[36:39],a[160:163],v[60:63],v[36:39]
buffer_load_dwordx4 v0,s[36:39],s66 offen offset:3072 sc1 lds
v_mfma_f32_16x16x32_bf16 v[40:43],a[164:167],v[60:63],v[40:43]
ds_read_b128 v[56:59],v2 offset:11264
v_add_f32 v116,v116,v118
v_mfma_f32_16x16x32_bf16 v[36:39],a[168:171],v[64:67],v[36:39]
ds_read_b128 v[60:63],v2 offset:12288
v_mul_f32 v96,s75,v116
v_mfma_f32_16x16x32_bf16 v[40:43],a[172:175],v[64:67],v[40:43]
v_add_f32 v120,v120,v121
v_add_f32 v122,v122,v123
v_mfma_f32_16x16x32_bf16 v[36:39],a[176:179],v[68:71],v[36:39]
buffer_store_dwordx4 v[84:87],v3,s[36:39],s73 offen offset:0 sc1
v_add_f32 v120,v120,v122
v_mul_f32 v120,s75,v120)"
R"(
v_fma_f32 v120,-v96,v96,v120
v_max_f32 v120,0,v120
v_add_f32 v115,v15,v120
v_fma_f32 v120,v15,v115,v120
v_rsq_f32 v115,v115
v_mfma_f32_16x16x32_bf16 v[40:43],a[180:183],v[68:71],v[40:43]
ds_read_b128 v[64:67],v2 offset:13312
v_rsq_f32 v116,v120
v_mul_f32_e64 v114,-v96,v115
v_mfma_f32_16x16x32_bf16 v[36:39],a[184:187],v[72:75],v[36:39]
s_waitcnt lgkmcnt(2)
ds_read_b128 v[68:71],v2 offset:14336
buffer_store_dwordx2 v[114:115],v12,s[36:39],s65 offen offset:0 sc1
v_mul_f32 v97,s76,v116
v_mul_f32 v98,v116,v116
v_mul_f32 v99,v120,v116
v_mul_f32 v98,s77,v98
v_mul_f32 v98,v98,v97
v_mfma_f32_16x16x32_bf16 v[40:43],a[188:191],v[72:75],v[40:43]
s_cbranch_scc1 Lg1_a0w2_%=
s_cmp_ge_u32 s48,4194304
s_cselect_b64 s[90:91],s[94:95],s[90:91]
s_cselect_b64 s[92:93],s[96:97],s[92:93]
s_cmp_lt_u32 s67,s59
s_cbranch_scc1 Lg1_d0w2_%=
s_branch Lg1_slow0_%=
Lg1_a0w3_%=:
v_mfma_f32_16x16x32_bf16 v[28:31],a[96:99],v[44:47],0
s_sub_u32 s69,s66,1048576
ds_read_b128 v[72:75],v2 offset:11264
s_cmp_lt_u32 s69,2097152
v_mfma_f32_16x16x32_bf16 v[32:35],a[100:103],v[44:47],0
s_cselect_b32 s89,3145728,s89
s_sub_u32 s69,s66,3145728
s_cmp_lt_u32 s69,1048576
v_mfma_f32_16x16x32_bf16 v[104:107],v[100:103],v[44:47],0
s_cselect_b32 s89,4194304,s89
s_add_u32 m0,s62,16384
s_lshr_b32 s73,s47,0
v_mfma_f32_16x16x32_bf16 v[108:111],v[44:47],v[44:47],0
s_lshr_b32 s72,s66,6
v_fma_f32 v36,-v96,v20,v36
v_fma_f32 v37,-v96,v21,v37
v_mfma_f32_16x16x32_bf16 v[28:31],a[104:107],v[48:51],v[28:31]
buffer_load_dwordx4 v1,s[36:39],s66 offen offset:0 sc1 lds
v_mfma_f32_16x16x32_bf16 v[32:35],a[108:111],v[48:51],v[32:35]
s_lshr_b32 s71,s62,2
ds_read_b128 v[44:47],v2 offset:4096
v_fma_f32 v38,-v96,v22,v38
v_mfma_f32_16x16x32_bf16 v[104:107],v[100:103],v[48:51],v[104:107]
s_cmp_lt_u32 s66,4194304
v_fma_f32 v39,-v96,v23,v39
v_mul_f32 v76,v36,v36
v_mfma_f32_16x16x32_bf16 v[108:111],v[48:51],v[48:51],v[108:111]
s_cselect_b32 s72,s59,s72
v_mul_f32 v77,v37,v37
v_mul_f32 v78,v38,v38
v_mfma_f32_16x16x32_bf16 v[28:31],a[112:115],v[52:55],v[28:31]
buffer_load_dwordx4 v1,s[36:39],s66 offen offset:1024 sc1 lds
v_mfma_f32_16x16x32_bf16 v[32:35],a[116:119],v[52:55],v[32:35]
ds_read_b128 v[48:51],v2 offset:5120
v_mul_f32 v79,v39,v39
v_mfma_f32_16x16x32_bf16 v[104:107],v[100:103],v[52:55],v[104:107]
v_fma_f32 v76,v98,v76,v97
v_mul_f32 v76,v36,v76
v_mfma_f32_16x16x32_bf16 v[108:111],v[52:55],v[52:55],v[108:111]
v_exp_f32 v76,v76
v_mfma_f32_16x16x32_bf16 v[28:31],a[120:123],v[56:59],v[28:31]
buffer_load_dwordx4 v1,s[36:39],s66 offen offset:2048 sc1 lds
v_mfma_f32_16x16x32_bf16 v[32:35],a[124:127],v[56:59],v[32:35]
ds_read_b128 v[52:55],v2 offset:6144
v_fma_f32 v76,v76,v99,v99
v_mfma_f32_16x16x32_bf16 v[104:107],v[100:103],v[56:59],v[104:107]
v_rcp_f32 v76,v76
v_mfma_f32_16x16x32_bf16 v[108:111],v[56:59],v[56:59],v[108:111]
s_waitcnt lgkmcnt(3)
v_fma_f32 v77,v98,v77,v97
v_mul_f32 v77,v37,v77
v_mfma_f32_16x16x32_bf16 v[28:31],a[64:67],v[60:63],v[28:31]
buffer_load_dwordx4 v1,s[36:39],s66 offen offset:3072 sc1 lds
v_mfma_f32_16x16x32_bf16 v[32:35],a[68:71],v[60:63],v[32:35]
s_add_u32 m0,s71,s88
ds_read_b128 v[56:59],v2 offset:7168
v_fma_f32 v78,v98,v78,v97
v_mfma_f32_16x16x32_bf16 v[28:31],a[72:75],v[64:67],v[28:31]
buffer_load_dword v10,s[36:39],s72 offen offset:0 sc1 lds
v_mfma_f32_16x16x32_bf16 v[32:35],a[76:79],v[64:67],v[32:35]
s_xor_b32 s87,s64,65536
ds_read_b128 v[60:63],v2 offset:0
v_mul_f32 v78,v38,v78
v_mfma_f32_16x16x32_bf16 v[28:31],a[80:83],v[68:71],v[28:31]
s_cmp_ge_u32 s66,s59
ds_read_b128 v[64:67],v2 offset:1024
v_fma_f32 v79,v98,v79,v97
v_mfma_f32_16x16x32_bf16 v[32:35],a[84:87],v[68:71],v[32:35]
s_cselect_b32 s86,s49,s50
v_exp_f32 v77,v77
v_mfma_f32_16x16x32_bf16 v[28:31],a[88:91],v[72:75],v[28:31]
s_cselect_b32 s87,s87,s63
ds_read_b128 v[68:71],v2 offset:2048
v_mul_f32 v79,v39,v79
v_mfma_f32_16x16x32_bf16 v[32:35],a[92:95],v[72:75],v[32:35]
s_waitcnt lgkmcnt(3)
v_exp_f32 v78,v78
v_mfma_f32_16x16x32_bf16 v[28:31],a[32:35],v[44:47],v[28:31]
s_waitcnt vmcnt(11)
ds_read_b128 v[72:75],v2 offset:3072
v_fma_f32 v77,v77,v99,v99
v_mfma_f32_16x16x32_bf16 v[32:35],a[36:39],v[44:47],v[32:35]
s_barrier
v_mfma_f32_16x16x32_bf16 v[28:31],a[40:43],v[48:51],v[28:31]
s_mov_b32 s62,s87
ds_read_b128 v[44:47],v2 offset:28672
ds_read_b32 v16,v7 offset:0
v_mfma_f32_16x16x32_bf16 v[32:35],a[44:47],v[48:51],v[32:35]
s_cmp_lt_u32 s86,s89
v_exp_f32 v79,v79
v_mfma_f32_16x16x32_bf16 v[28:31],a[48:51],v[52:55],v[28:31]
s_mov_b32 m0,s62
ds_read_b128 v[48:51],v2 offset:29696
v_fma_f32 v78,v78,v99,v99
v_mfma_f32_16x16x32_bf16 v[32:35],a[52:55],v[52:55],v[32:35]
v_rcp_f32 v77,v77
v_mfma_f32_16x16x32_bf16 v[28:31],a[56:59],v[56:59],v[28:31]
s_waitcnt lgkmcnt(1)
ds_read_b128 v[52:55],v2 offset:30720
v_fma_f32 v79,v79,v99,v99
v_mfma_f32_16x16x32_bf16 v[32:35],a[60:63],v[56:59],v[32:35]
v_cmp_eq_u32 vcc,s60,v16
v_rcp_f32 v78,v78
v_mul_f32 v36,v36,v76
v_mfma_f32_16x16x32_bf16 v[28:31],a[0:3],v[60:63],v[28:31]
s_cselect_b32 vcc_lo,-1,vcc_lo
ds_read_b128 v[56:59],v2 offset:31744
v_mul_f32 v37,v37,v77
v_mfma_f32_16x16x32_bf16 v[32:35],a[4:7],v[60:63],v[32:35]
s_cmp_eq_u32 vcc_lo,-1
v_rcp_f32 v79,v79
v_mfma_f32_16x16x32_bf16 v[28:31],a[8:11],v[64:67],v[28:31]
s_cselect_b32 s67,s86,s59
ds_read_b128 v[60:63],v2 offset:24576
v_mul_f32 v38,v38,v78
v_mfma_f32_16x16x32_bf16 v[32:35],a[12:15],v[64:67],v[32:35]
s_add_u32 s53,s52,s55
v_mul_f32 v39,v39,v79
v_cvt_pk_bf16_f32 v84,v36,v37
v_mfma_f32_16x16x32_bf16 v[28:31],a[16:19],v[68:71],v[28:31]
s_cmp_eq_u32 s53,s56
ds_read_b128 v[64:67],v2 offset:25600
v_cvt_pk_bf16_f32 v85,v38,v39
v_mfma_f32_16x16x32_bf16 v[32:35],a[20:23],v[68:71],v[32:35]
s_cselect_b32 s53,s57,s53
v_fma_f32 v40,-v96,v24,v40
v_fma_f32 v41,-v96,v25,v41
v_mfma_f32_16x16x32_bf16 v[28:31],a[24:27],v[72:75],v[28:31]
s_cmp_eq_u32 s53,s58
ds_read_b128 v[68:71],v2 offset:26624
v_fma_f32 v42,-v96,v26,v42
v_mfma_f32_16x16x32_bf16 v[32:35],a[28:31],v[72:75],v[32:35]
s_waitcnt lgkmcnt(3)
v_fma_f32 v43,-v96,v27,v43
v_mul_f32 v80,v40,v40
v_mfma_f32_16x16x32_bf16 v[28:31],a[224:227],v[44:47],v[28:31]
s_cselect_b32 s53,s59,s53
ds_read_b128 v[72:75],v2 offset:27648
v_mul_f32 v81,v41,v41
v_mfma_f32_16x16x32_bf16 v[32:35],a[228:231],v[44:47],v[32:35]
s_xor_b32 s63,s63,65536
v_mul_f32 v82,v42,v42
v_mul_f32 v83,v43,v43
v_mfma_f32_16x16x32_bf16 v[104:107],v[100:103],v[44:47],v[104:107]
s_lshr_b32 s70,s46,5
v_fma_f32 v80,v98,v80,v97
v_mul_f32 v80,v40,v80
v_mfma_f32_16x16x32_bf16 v[108:111],v[44:47],v[44:47],v[108:111]
s_cmp_ge_u32 s66,s59
v_exp_f32 v80,v80
v_mfma_f32_16x16x32_bf16 v[28:31],a[232:235],v[48:51],v[28:31]
ds_read_b128 v[44:47],v2 offset:20480
v_fma_f32 v80,v80,v99,v99
v_mfma_f32_16x16x32_bf16 v[32:35],a[236:239],v[48:51],v[32:35]
v_rcp_f32 v80,v80
v_mfma_f32_16x16x32_bf16 v[104:107],v[100:103],v[48:51],v[104:107]
v_fma_f32 v81,v98,v81,v97
v_mul_f32 v81,v41,v81
v_mfma_f32_16x16x32_bf16 v[108:111],v[48:51],v[48:51],v[108:111]
v_exp_f32 v81,v81
v_mfma_f32_16x16x32_bf16 v[28:31],a[240:243],v[52:55],v[28:31]
ds_read_b128 v[48:51],v2 offset:21504
v_fma_f32 v81,v81,v99,v99
v_mfma_f32_16x16x32_bf16 v[32:35],a[244:247],v[52:55],v[32:35]
v_rcp_f32 v81,v81
v_mfma_f32_16x16x32_bf16 v[104:107],v[100:103],v[52:55],v[104:107]
v_fma_f32 v82,v98,v82,v97
v_mul_f32 v82,v42,v82
v_mfma_f32_16x16x32_bf16 v[108:111],v[52:55],v[52:55],v[108:111]
v_exp_f32 v82,v82
v_mfma_f32_16x16x32_bf16 v[28:31],a[248:251],v[56:59],v[28:31]
ds_read_b128 v[52:55],v2 offset:22528
v_fma_f32 v82,v82,v99,v99)"
R"(
v_mfma_f32_16x16x32_bf16 v[32:35],a[252:255],v[56:59],v[32:35]
v_rcp_f32 v82,v82
v_mfma_f32_16x16x32_bf16 v[104:107],v[100:103],v[56:59],v[104:107]
v_fma_f32 v83,v98,v83,v97
v_mul_f32 v83,v43,v83
v_mfma_f32_16x16x32_bf16 v[108:111],v[56:59],v[56:59],v[108:111]
s_waitcnt lgkmcnt(3)
v_exp_f32 v83,v83
v_mfma_f32_16x16x32_bf16 v[28:31],a[192:195],v[60:63],v[28:31]
ds_read_b128 v[56:59],v2 offset:23552
v_fma_f32 v83,v83,v99,v99
v_mfma_f32_16x16x32_bf16 v[32:35],a[196:199],v[60:63],v[32:35]
v_rcp_f32 v83,v83
v_mfma_f32_16x16x32_bf16 v[28:31],a[200:203],v[64:67],v[28:31]
v_cndmask_b32 v112,v108,v109,s[82:83]
v_cndmask_b32 v113,v110,v111,s[82:83]
v_cndmask_b32 v112,v112,v113,s[84:85]
v_cndmask_b32 v112,v104,v112,s[98:99]
v_mfma_f32_16x16x32_bf16 v[32:35],a[204:207],v[64:67],v[32:35]
ds_read_b128 v[60:63],v2 offset:16384
ds_write_b32 v13,v112 offset:0
buffer_load_dwordx4 v0,s[36:39],s67 offen offset:0 sc1 lds
v_mfma_f32_16x16x32_bf16 v[28:31],a[208:211],v[68:71],v[28:31]
ds_read_b128 v[64:67],v2 offset:17408
v_mul_f32 v40,v40,v80
v_mfma_f32_16x16x32_bf16 v[32:35],a[212:215],v[68:71],v[32:35]
v_mul_f32 v41,v41,v81
v_mul_f32 v42,v42,v82
v_mfma_f32_16x16x32_bf16 v[28:31],a[216:219],v[72:75],v[28:31]
ds_read_b128 v[68:71],v2 offset:18432
v_mul_f32 v43,v43,v83
v_mfma_f32_16x16x32_bf16 v[32:35],a[220:223],v[72:75],v[32:35]
s_waitcnt lgkmcnt(4)
buffer_load_dwordx4 v0,s[36:39],s67 offen offset:1024 sc1 lds
v_mfma_f32_16x16x32_bf16 v[28:31],a[160:163],v[44:47],v[28:31]
s_waitcnt vmcnt(7) lgkmcnt(2)
ds_read_b128 v[72:75],v2 offset:19456
v_cvt_pk_bf16_f32 v86,v40,v41
v_mfma_f32_16x16x32_bf16 v[32:35],a[164:167],v[44:47],v[32:35]
s_barrier
v_mfma_f32_16x16x32_bf16 v[28:31],a[168:171],v[48:51],v[28:31]
buffer_store_dword v5,v4,s[36:39],s70 offen offset:0 sc1
ds_read_b128 v[116:119],v14 offset:0
ds_read_b128 v[120:123],v14 offset:16
ds_read_b128 v[44:47],v2 offset:45056
v_mfma_f32_16x16x32_bf16 v[32:35],a[172:175],v[48:51],v[32:35]
v_cvt_pk_bf16_f32 v87,v42,v43
v_mfma_f32_16x16x32_bf16 v[28:31],a[176:179],v[52:55],v[28:31]
ds_read_b128 v[48:51],v2 offset:46080
v_mfma_f32_16x16x32_bf16 v[32:35],a[180:183],v[52:55],v[32:35]
buffer_load_dwordx4 v0,s[36:39],s67 offen offset:2048 sc1 lds
v_mfma_f32_16x16x32_bf16 v[28:31],a[184:187],v[56:59],v[28:31]
s_waitcnt lgkmcnt(2)
ds_read_b128 v[52:55],v2 offset:47104
v_add_f32 v116,v116,v117
v_mfma_f32_16x16x32_bf16 v[32:35],a[188:191],v[56:59],v[32:35]
v_add_f32 v118,v118,v119
v_add_f32 v116,v116,v118
v_mfma_f32_16x16x32_bf16 v[28:31],a[128:131],v[60:63],v[28:31]
ds_read_b128 v[56:59],v2 offset:48128
v_mul_f32 v92,s75,v116
v_mfma_f32_16x16x32_bf16 v[32:35],a[132:135],v[60:63],v[32:35]
buffer_load_dwordx4 v0,s[36:39],s67 offen offset:3072 sc1 lds
v_mfma_f32_16x16x32_bf16 v[28:31],a[136:139],v[64:67],v[28:31]
ds_read_b128 v[60:63],v2 offset:40960
v_add_f32 v120,v120,v121
v_mfma_f32_16x16x32_bf16 v[32:35],a[140:143],v[64:67],v[32:35]
v_add_f32 v122,v122,v123
v_add_f32 v120,v120,v122
v_mfma_f32_16x16x32_bf16 v[28:31],a[144:147],v[68:71],v[28:31]
ds_read_b128 v[64:67],v2 offset:41984
v_mul_f32 v120,s75,v120
v_mfma_f32_16x16x32_bf16 v[32:35],a[148:151],v[68:71],v[32:35]
buffer_store_dwordx4 v[84:87],v3,s[36:39],s73 offen offset:0 sc1
v_fma_f32 v120,-v92,v92,v120
v_max_f32 v120,0,v120
v_add_f32 v115,v15,v120
v_fma_f32 v120,v15,v115,v120
v_rsq_f32 v116,v120
v_mfma_f32_16x16x32_bf16 v[28:31],a[152:155],v[72:75],v[28:31]
s_waitcnt lgkmcnt(2)
ds_read_b128 v[68:71],v2 offset:43008
v_mul_f32 v93,s76,v116
v_mul_f32 v94,v116,v116
v_mul_f32 v95,v120,v116
v_mul_f32 v94,s77,v94
v_mul_f32 v94,v94,v93
v_mfma_f32_16x16x32_bf16 v[32:35],a[156:159],v[72:75],v[32:35]
s_cbranch_scc1 Lg1_slow1_%=
Lg1_a1w3_%=:
v_mfma_f32_16x16x32_bf16 v[36:39],a[96:99],v[44:47],0
s_sub_u32 s69,s67,1048576
ds_read_b128 v[72:75],v2 offset:44032
s_cmp_lt_u32 s69,2097152
v_mfma_f32_16x16x32_bf16 v[40:43],a[100:103],v[44:47],0
s_cselect_b32 s89,3145728,s89
s_sub_u32 s69,s67,3145728
s_cmp_lt_u32 s69,1048576
v_mfma_f32_16x16x32_bf16 v[104:107],v[100:103],v[44:47],0
s_cselect_b32 s89,4194304,s89
s_add_u32 m0,s62,16384
s_lshr_b32 s73,s48,0
v_mfma_f32_16x16x32_bf16 v[108:111],v[44:47],v[44:47],0
s_lshr_b32 s72,s67,6
v_fma_f32 v28,-v92,v20,v28
v_fma_f32 v29,-v92,v21,v29
v_mfma_f32_16x16x32_bf16 v[36:39],a[104:107],v[48:51],v[36:39]
buffer_load_dwordx4 v1,s[36:39],s67 offen offset:0 sc1 lds
v_mfma_f32_16x16x32_bf16 v[40:43],a[108:111],v[48:51],v[40:43]
s_lshr_b32 s71,s62,2
ds_read_b128 v[44:47],v2 offset:36864
v_fma_f32 v30,-v92,v22,v30
v_mfma_f32_16x16x32_bf16 v[104:107],v[100:103],v[48:51],v[104:107]
s_cmp_lt_u32 s67,4194304
v_fma_f32 v31,-v92,v23,v31
v_mul_f32 v76,v28,v28
v_mfma_f32_16x16x32_bf16 v[108:111],v[48:51],v[48:51],v[108:111]
s_cselect_b32 s72,s59,s72
v_mul_f32 v77,v29,v29
v_mul_f32 v78,v30,v30
v_mfma_f32_16x16x32_bf16 v[36:39],a[112:115],v[52:55],v[36:39]
buffer_load_dwordx4 v1,s[36:39],s67 offen offset:1024 sc1 lds
v_mfma_f32_16x16x32_bf16 v[40:43],a[116:119],v[52:55],v[40:43]
s_mov_b64 s[44:45],s[46:47]
ds_read_b128 v[48:51],v2 offset:37888
v_mul_f32 v79,v31,v31
v_mfma_f32_16x16x32_bf16 v[104:107],v[100:103],v[52:55],v[104:107]
v_fma_f32 v76,v94,v76,v93
v_mul_f32 v76,v28,v76
v_mfma_f32_16x16x32_bf16 v[108:111],v[52:55],v[52:55],v[108:111]
v_exp_f32 v76,v76
v_mfma_f32_16x16x32_bf16 v[36:39],a[120:123],v[56:59],v[36:39]
buffer_load_dwordx4 v1,s[36:39],s67 offen offset:2048 sc1 lds
v_mfma_f32_16x16x32_bf16 v[40:43],a[124:127],v[56:59],v[40:43]
ds_read_b128 v[52:55],v2 offset:38912
v_fma_f32 v76,v76,v95,v95
v_mfma_f32_16x16x32_bf16 v[104:107],v[100:103],v[56:59],v[104:107]
v_rcp_f32 v76,v76
v_mfma_f32_16x16x32_bf16 v[108:111],v[56:59],v[56:59],v[108:111]
s_waitcnt lgkmcnt(3)
v_fma_f32 v77,v94,v77,v93
v_mul_f32 v77,v29,v77
v_mfma_f32_16x16x32_bf16 v[36:39],a[64:67],v[60:63],v[36:39]
buffer_load_dwordx4 v1,s[36:39],s67 offen offset:3072 sc1 lds
v_mfma_f32_16x16x32_bf16 v[40:43],a[68:71],v[60:63],v[40:43]
s_add_u32 m0,s71,s88
ds_read_b128 v[56:59],v2 offset:39936
v_fma_f32 v78,v94,v78,v93
v_mfma_f32_16x16x32_bf16 v[36:39],a[72:75],v[64:67],v[36:39]
buffer_load_dword v10,s[36:39],s72 offen offset:0 sc1 lds
v_mfma_f32_16x16x32_bf16 v[40:43],a[76:79],v[64:67],v[40:43]
s_xor_b32 s87,s63,65536
ds_read_b128 v[60:63],v2 offset:32768
v_mul_f32 v78,v30,v78
v_mfma_f32_16x16x32_bf16 v[36:39],a[80:83],v[68:71],v[36:39]
s_cmp_ge_u32 s67,s59
ds_read_b128 v[64:67],v2 offset:33792
v_fma_f32 v79,v94,v79,v93
v_mfma_f32_16x16x32_bf16 v[40:43],a[84:87],v[68:71],v[40:43]
s_cselect_b32 s86,s50,s51
v_exp_f32 v77,v77
v_mfma_f32_16x16x32_bf16 v[36:39],a[88:91],v[72:75],v[36:39]
s_cselect_b32 s87,s87,s64
ds_read_b128 v[68:71],v2 offset:34816
v_mul_f32 v79,v31,v79
v_mfma_f32_16x16x32_bf16 v[40:43],a[92:95],v[72:75],v[40:43]
s_waitcnt lgkmcnt(3)
v_exp_f32 v78,v78
v_mfma_f32_16x16x32_bf16 v[36:39],a[32:35],v[44:47],v[36:39]
s_waitcnt vmcnt(11)
ds_read_b128 v[72:75],v2 offset:35840
v_fma_f32 v77,v77,v95,v95
v_mfma_f32_16x16x32_bf16 v[40:43],a[36:39],v[44:47],v[40:43]
s_barrier
v_mfma_f32_16x16x32_bf16 v[36:39],a[40:43],v[48:51],v[36:39]
s_mov_b32 s62,s87
ds_read_b128 v[44:47],v2 offset:61440
ds_read_b32 v16,v7 offset:256
v_mfma_f32_16x16x32_bf16 v[40:43],a[44:47],v[48:51],v[40:43]
s_cmp_lt_u32 s86,s89
v_exp_f32 v79,v79
v_mfma_f32_16x16x32_bf16 v[36:39],a[48:51],v[52:55],v[36:39]
s_mov_b32 m0,s62
ds_read_b128 v[48:51],v2 offset:62464
v_fma_f32 v78,v78,v95,v95
v_mfma_f32_16x16x32_bf16 v[40:43],a[52:55],v[52:55],v[40:43]
v_rcp_f32 v77,v77
v_mfma_f32_16x16x32_bf16 v[36:39],a[56:59],v[56:59],v[36:39]
s_waitcnt lgkmcnt(1)
ds_read_b128 v[52:55],v2 offset:63488
v_fma_f32 v79,v79,v95,v95
v_mfma_f32_16x16x32_bf16 v[40:43],a[60:63],v[56:59],v[40:43]
v_cmp_eq_u32 vcc,s60,v16
v_rcp_f32 v78,v78
v_mul_f32 v28,v28,v76
v_mfma_f32_16x16x32_bf16 v[36:39],a[0:3],v[60:63],v[36:39]
s_cselect_b32 vcc_lo,-1,vcc_lo
ds_read_b128 v[56:59],v2 offset:64512
v_mul_f32 v29,v29,v77
v_mfma_f32_16x16x32_bf16 v[40:43],a[4:7],v[60:63],v[40:43])"
R"(
s_cmp_eq_u32 vcc_lo,-1
v_rcp_f32 v79,v79
v_mfma_f32_16x16x32_bf16 v[36:39],a[8:11],v[64:67],v[36:39]
s_cselect_b32 s66,s86,s59
ds_read_b128 v[60:63],v2 offset:57344
v_mul_f32 v30,v30,v78
v_mfma_f32_16x16x32_bf16 v[40:43],a[12:15],v[64:67],v[40:43]
s_add_u32 s74,s53,s55
v_mul_f32 v31,v31,v79
v_cvt_pk_bf16_f32 v84,v28,v29
v_mfma_f32_16x16x32_bf16 v[36:39],a[16:19],v[68:71],v[36:39]
s_cmp_eq_u32 s74,s56
ds_read_b128 v[64:67],v2 offset:58368
v_cvt_pk_bf16_f32 v85,v30,v31
v_mfma_f32_16x16x32_bf16 v[40:43],a[20:23],v[68:71],v[40:43]
s_cselect_b32 s74,s57,s74
v_fma_f32 v32,-v92,v24,v32
v_fma_f32 v33,-v92,v25,v33
v_mfma_f32_16x16x32_bf16 v[36:39],a[24:27],v[72:75],v[36:39]
s_cmp_eq_u32 s74,s58
ds_read_b128 v[68:71],v2 offset:59392
v_fma_f32 v34,-v92,v26,v34
v_mfma_f32_16x16x32_bf16 v[40:43],a[28:31],v[72:75],v[40:43]
s_waitcnt lgkmcnt(3)
v_fma_f32 v35,-v92,v27,v35
v_mul_f32 v80,v32,v32
v_mfma_f32_16x16x32_bf16 v[36:39],a[224:227],v[44:47],v[36:39]
s_cselect_b32 s74,s59,s74
ds_read_b128 v[72:75],v2 offset:60416
v_mul_f32 v81,v33,v33
v_mfma_f32_16x16x32_bf16 v[40:43],a[228:231],v[44:47],v[40:43]
s_xor_b32 s64,s64,65536
v_mul_f32 v82,v34,v34
v_mul_f32 v83,v35,v35
v_mfma_f32_16x16x32_bf16 v[104:107],v[100:103],v[44:47],v[104:107]
s_lshr_b32 s70,s47,5
v_fma_f32 v80,v94,v80,v93
v_mul_f32 v80,v32,v80
v_mfma_f32_16x16x32_bf16 v[108:111],v[44:47],v[44:47],v[108:111]
s_mov_b64 s[46:47],s[48:49]
v_exp_f32 v80,v80
v_mfma_f32_16x16x32_bf16 v[36:39],a[232:235],v[48:51],v[36:39]
s_mov_b64 s[48:49],s[50:51]
ds_read_b128 v[44:47],v2 offset:53248
v_fma_f32 v80,v80,v95,v95
v_mfma_f32_16x16x32_bf16 v[40:43],a[236:239],v[48:51],v[40:43]
s_mov_b64 s[50:51],s[52:53]
v_rcp_f32 v80,v80
v_mfma_f32_16x16x32_bf16 v[104:107],v[100:103],v[48:51],v[104:107]
s_mov_b32 s52,s74
v_fma_f32 v81,v94,v81,v93
v_mul_f32 v81,v33,v81
v_mfma_f32_16x16x32_bf16 v[108:111],v[48:51],v[48:51],v[108:111]
s_cmp_lt_u32 s67,4194304
v_exp_f32 v81,v81
v_mfma_f32_16x16x32_bf16 v[36:39],a[240:243],v[52:55],v[36:39]
ds_read_b128 v[48:51],v2 offset:54272
v_fma_f32 v81,v81,v95,v95
v_mfma_f32_16x16x32_bf16 v[40:43],a[244:247],v[52:55],v[40:43]
v_rcp_f32 v81,v81
v_mfma_f32_16x16x32_bf16 v[104:107],v[100:103],v[52:55],v[104:107]
v_fma_f32 v82,v94,v82,v93
v_mul_f32 v82,v34,v82
v_mfma_f32_16x16x32_bf16 v[108:111],v[52:55],v[52:55],v[108:111]
v_exp_f32 v82,v82
v_mfma_f32_16x16x32_bf16 v[36:39],a[248:251],v[56:59],v[36:39]
ds_read_b128 v[52:55],v2 offset:55296
v_fma_f32 v82,v82,v95,v95
v_mfma_f32_16x16x32_bf16 v[40:43],a[252:255],v[56:59],v[40:43]
v_rcp_f32 v82,v82
v_mfma_f32_16x16x32_bf16 v[104:107],v[100:103],v[56:59],v[104:107]
v_fma_f32 v83,v94,v83,v93
v_mul_f32 v83,v35,v83
v_mfma_f32_16x16x32_bf16 v[108:111],v[56:59],v[56:59],v[108:111]
s_waitcnt lgkmcnt(3)
v_exp_f32 v83,v83
v_mfma_f32_16x16x32_bf16 v[36:39],a[192:195],v[60:63],v[36:39]
ds_read_b128 v[56:59],v2 offset:56320
v_fma_f32 v83,v83,v95,v95
v_mfma_f32_16x16x32_bf16 v[40:43],a[196:199],v[60:63],v[40:43]
v_rcp_f32 v83,v83
v_mfma_f32_16x16x32_bf16 v[36:39],a[200:203],v[64:67],v[36:39]
v_cndmask_b32 v112,v108,v109,s[82:83]
v_cndmask_b32 v113,v110,v111,s[82:83]
v_cndmask_b32 v112,v112,v113,s[84:85]
v_cndmask_b32 v112,v104,v112,s[98:99]
v_mfma_f32_16x16x32_bf16 v[40:43],a[204:207],v[64:67],v[40:43]
ds_read_b128 v[60:63],v2 offset:49152
ds_write_b32 v13,v112 offset:2048
buffer_load_dwordx4 v0,s[36:39],s66 offen offset:0 sc1 lds
v_mfma_f32_16x16x32_bf16 v[36:39],a[208:211],v[68:71],v[36:39]
ds_read_b128 v[64:67],v2 offset:50176
v_mul_f32 v32,v32,v80
v_mfma_f32_16x16x32_bf16 v[40:43],a[212:215],v[68:71],v[40:43]
v_mul_f32 v33,v33,v81
v_mul_f32 v34,v34,v82
v_mfma_f32_16x16x32_bf16 v[36:39],a[216:219],v[72:75],v[36:39]
ds_read_b128 v[68:71],v2 offset:51200
v_mul_f32 v35,v35,v83
v_mfma_f32_16x16x32_bf16 v[40:43],a[220:223],v[72:75],v[40:43]
s_waitcnt lgkmcnt(4)
buffer_load_dwordx4 v0,s[36:39],s66 offen offset:1024 sc1 lds
v_mfma_f32_16x16x32_bf16 v[36:39],a[160:163],v[44:47],v[36:39]
s_waitcnt vmcnt(7) lgkmcnt(2)
ds_read_b128 v[72:75],v2 offset:52224
v_xor_b32 v2,65536,v2
v_mfma_f32_16x16x32_bf16 v[40:43],a[164:167],v[44:47],v[40:43]
s_barrier
v_mfma_f32_16x16x32_bf16 v[36:39],a[168:171],v[48:51],v[36:39]
buffer_store_dword v5,v4,s[36:39],s70 offen offset:0 sc1
ds_read_b128 v[116:119],v14 offset:2048
ds_read_b128 v[120:123],v14 offset:2064
ds_read_b128 v[44:47],v2 offset:12288
v_mfma_f32_16x16x32_bf16 v[40:43],a[172:175],v[48:51],v[40:43]
v_cvt_pk_bf16_f32 v86,v32,v33
v_cvt_pk_bf16_f32 v87,v34,v35
v_mfma_f32_16x16x32_bf16 v[36:39],a[176:179],v[52:55],v[36:39]
ds_read_b128 v[48:51],v2 offset:13312
v_xor_b32 v8,16384,v8
v_mfma_f32_16x16x32_bf16 v[40:43],a[180:183],v[52:55],v[40:43]
buffer_load_dwordx4 v0,s[36:39],s66 offen offset:2048 sc1 lds
v_mfma_f32_16x16x32_bf16 v[36:39],a[184:187],v[56:59],v[36:39]
s_waitcnt lgkmcnt(2)
ds_read_b128 v[52:55],v2 offset:14336
v_add_f32 v116,v116,v117
v_mfma_f32_16x16x32_bf16 v[40:43],a[188:191],v[56:59],v[40:43]
v_add_f32 v118,v118,v119
v_add_f32 v116,v116,v118
v_mfma_f32_16x16x32_bf16 v[36:39],a[128:131],v[60:63],v[36:39]
ds_read_b128 v[56:59],v2 offset:15360
v_mul_f32 v96,s75,v116
v_mfma_f32_16x16x32_bf16 v[40:43],a[132:135],v[60:63],v[40:43]
buffer_load_dwordx4 v0,s[36:39],s66 offen offset:3072 sc1 lds
v_mfma_f32_16x16x32_bf16 v[36:39],a[136:139],v[64:67],v[36:39]
ds_read_b128 v[60:63],v2 offset:8192
v_add_f32 v120,v120,v121
v_mfma_f32_16x16x32_bf16 v[40:43],a[140:143],v[64:67],v[40:43]
v_add_f32 v122,v122,v123
v_add_f32 v120,v120,v122
v_mfma_f32_16x16x32_bf16 v[36:39],a[144:147],v[68:71],v[36:39]
ds_read_b128 v[64:67],v2 offset:9216
v_mul_f32 v120,s75,v120
v_mfma_f32_16x16x32_bf16 v[40:43],a[148:151],v[68:71],v[40:43]
buffer_store_dwordx4 v[84:87],v3,s[36:39],s73 offen offset:0 sc1
v_fma_f32 v120,-v96,v96,v120
v_max_f32 v120,0,v120
v_add_f32 v115,v15,v120
v_fma_f32 v120,v15,v115,v120
v_rsq_f32 v116,v120
v_mfma_f32_16x16x32_bf16 v[36:39],a[152:155],v[72:75],v[36:39]
s_waitcnt lgkmcnt(2)
ds_read_b128 v[68:71],v2 offset:10240
v_mul_f32 v97,s76,v116
v_mul_f32 v98,v116,v116
v_mul_f32 v99,v120,v116
v_mul_f32 v98,s77,v98
v_mul_f32 v98,v98,v97
v_mfma_f32_16x16x32_bf16 v[40:43],a[156:159],v[72:75],v[40:43]
s_cbranch_scc1 Lg1_a0w3_%=
s_cmp_ge_u32 s48,4194304
s_cselect_b64 s[90:91],s[94:95],s[90:91]
s_cselect_b64 s[92:93],s[96:97],s[92:93]
s_cmp_lt_u32 s67,s59
s_cbranch_scc1 Lg1_d0w3_%=
s_branch Lg1_slow0_%=
Lg1_d0w0_%=:
v_mfma_f32_16x16x32_bf16 v[28:31],a[0:3],v[44:47],0
s_add_u32 m0,s62,16384
ds_read_b128 v[72:75],v2 offset:7168
s_lshr_b32 s73,s47,0
v_mfma_f32_16x16x32_bf16 v[32:35],a[4:7],v[44:47],0
buffer_load_dwordx4 v1,s[36:39],s66 offen offset:0 sc1 lds
v_mfma_f32_16x16x32_bf16 v[28:31],a[8:11],v[48:51],v[28:31]
s_lshr_b32 s72,s66,6
ds_read_b128 v[44:47],v2 offset:8192
s_lshr_b32 s71,s62,2
v_mfma_f32_16x16x32_bf16 v[32:35],a[12:15],v[48:51],v[32:35]
s_cmp_lt_u32 s66,4194304
v_fma_f32 v36,-v96,v20,v36
v_fma_f32 v37,-v96,v21,v37
v_mfma_f32_16x16x32_bf16 v[28:31],a[16:19],v[52:55],v[28:31]
s_cselect_b32 s72,s59,s72
ds_read_b128 v[48:51],v2 offset:9216
v_fma_f32 v38,-v96,v22,v38
v_mfma_f32_16x16x32_bf16 v[32:35],a[20:23],v[52:55],v[32:35]
buffer_load_dwordx4 v1,s[36:39],s66 offen offset:1024 sc1 lds
v_mfma_f32_16x16x32_bf16 v[28:31],a[24:27],v[56:59],v[28:31]
ds_read_b128 v[52:55],v2 offset:10240
v_fma_f32 v39,-v96,v23,v39
v_mfma_f32_16x16x32_bf16 v[32:35],a[28:31],v[56:59],v[32:35]
s_waitcnt lgkmcnt(3)
v_mul_f32 v76,v36,v36
v_mul_f32 v77,v37,v37
v_mfma_f32_16x16x32_bf16 v[28:31],a[32:35],v[60:63],v[28:31]
ds_read_b128 v[56:59],v2 offset:11264
v_mul_f32 v78,v38,v38
v_mfma_f32_16x16x32_bf16 v[32:35],a[36:39],v[60:63],v[32:35]
buffer_load_dwordx4 v1,s[36:39],s66 offen offset:2048 sc1 lds
v_mfma_f32_16x16x32_bf16 v[28:31],a[40:43],v[64:67],v[28:31]
ds_read_b128 v[60:63],v2 offset:12288)"
R"(
v_mul_f32 v79,v39,v39
v_mfma_f32_16x16x32_bf16 v[32:35],a[44:47],v[64:67],v[32:35]
v_fma_f32 v76,v98,v76,v97
v_mul_f32 v76,v36,v76
v_mfma_f32_16x16x32_bf16 v[28:31],a[48:51],v[68:71],v[28:31]
ds_read_b128 v[64:67],v2 offset:13312
v_fma_f32 v77,v98,v77,v97
v_mfma_f32_16x16x32_bf16 v[32:35],a[52:55],v[68:71],v[32:35]
buffer_load_dwordx4 v1,s[36:39],s66 offen offset:3072 sc1 lds
v_mfma_f32_16x16x32_bf16 v[28:31],a[56:59],v[72:75],v[28:31]
s_add_u32 m0,s71,s88
ds_read_b128 v[68:71],v2 offset:14336
v_mul_f32 v77,v37,v77
v_mfma_f32_16x16x32_bf16 v[32:35],a[60:63],v[72:75],v[32:35]
s_waitcnt lgkmcnt(3)
buffer_load_dword v10,s[36:39],s72 offen offset:0 sc1 lds
v_mfma_f32_16x16x32_bf16 v[28:31],a[64:67],v[44:47],v[28:31]
s_waitcnt vmcnt(11)
ds_read_b128 v[72:75],v2 offset:15360
v_fma_f32 v78,v98,v78,v97
v_mfma_f32_16x16x32_bf16 v[32:35],a[68:71],v[44:47],v[32:35]
s_barrier
v_mfma_f32_16x16x32_bf16 v[28:31],a[72:75],v[48:51],v[28:31]
s_xor_b32 s87,s64,65536
ds_read_b128 v[44:47],v2 offset:16384
v_fma_f32 v79,v98,v79,v97
v_mfma_f32_16x16x32_bf16 v[32:35],a[76:79],v[48:51],v[32:35]
s_cmp_ge_u32 s66,s59
v_exp_f32 v76,v76
v_mfma_f32_16x16x32_bf16 v[28:31],a[80:83],v[52:55],v[28:31]
s_cselect_b32 s86,s49,s50
ds_read_b128 v[48:51],v2 offset:17408
v_mul_f32 v78,v38,v78
v_mfma_f32_16x16x32_bf16 v[32:35],a[84:87],v[52:55],v[32:35]
s_cselect_b32 s87,s87,s63
v_exp_f32 v77,v77
v_mfma_f32_16x16x32_bf16 v[28:31],a[88:91],v[56:59],v[28:31]
s_lshr_b32 s69,s50,5
ds_read_b128 v[52:55],v2 offset:18432
v_mul_f32 v79,v39,v79
v_mfma_f32_16x16x32_bf16 v[32:35],a[92:95],v[56:59],v[32:35]
s_waitcnt lgkmcnt(3)
v_exp_f32 v78,v78
v_mfma_f32_16x16x32_bf16 v[28:31],a[96:99],v[60:63],v[28:31]
s_cmp_lt_u32 s50,4194304
ds_read_b128 v[56:59],v2 offset:19456
v_fma_f32 v76,v76,v99,v99
v_mfma_f32_16x16x32_bf16 v[32:35],a[100:103],v[60:63],v[32:35]
s_cselect_b32 s68,0xffffcfff,-1
v_exp_f32 v79,v79
v_mfma_f32_16x16x32_bf16 v[28:31],a[104:107],v[64:67],v[28:31]
s_and_b32 s68,s68,s69
ds_read_b128 v[60:63],v2 offset:20480
v_fma_f32 v77,v77,v99,v99
v_mfma_f32_16x16x32_bf16 v[32:35],a[108:111],v[64:67],v[32:35]
s_lshr_b32 s69,s51,5
v_rcp_f32 v76,v76
v_mfma_f32_16x16x32_bf16 v[28:31],a[112:115],v[68:71],v[28:31]
s_cmp_lt_u32 s51,4194304
ds_read_b128 v[64:67],v2 offset:21504
v_fma_f32 v78,v78,v99,v99
v_mfma_f32_16x16x32_bf16 v[32:35],a[116:119],v[68:71],v[32:35]
s_cselect_b32 s70,0xffffcfff,-1
v_rcp_f32 v77,v77
v_mfma_f32_16x16x32_bf16 v[28:31],a[120:123],v[72:75],v[28:31]
s_and_b32 s70,s70,s69
ds_read_b128 v[68:71],v2 offset:22528
v_fma_f32 v79,v79,v99,v99
v_mfma_f32_16x16x32_bf16 v[32:35],a[124:127],v[72:75],v[32:35]
s_waitcnt lgkmcnt(3)
v_rcp_f32 v78,v78
v_mfma_f32_16x16x32_bf16 v[28:31],a[128:131],v[44:47],v[28:31]
s_waitcnt vmcnt(10)
ds_read_b128 v[72:75],v2 offset:23552
v_mul_f32 v36,v36,v76
v_mfma_f32_16x16x32_bf16 v[32:35],a[132:135],v[44:47],v[32:35]
s_barrier
v_mfma_f32_16x16x32_bf16 v[28:31],a[136:139],v[48:51],v[28:31]
s_mov_b32 s62,s87
ds_read_b128 v[44:47],v2 offset:24576
ds_read_b32 v16,v7 offset:0
v_mfma_f32_16x16x32_bf16 v[32:35],a[140:143],v[48:51],v[32:35]
v_rcp_f32 v79,v79
v_mfma_f32_16x16x32_bf16 v[28:31],a[144:147],v[52:55],v[28:31]
ds_read_b128 v[48:51],v2 offset:25600
v_mul_f32 v37,v37,v77
v_mfma_f32_16x16x32_bf16 v[32:35],a[148:151],v[52:55],v[32:35]
v_mul_f32 v38,v38,v78
v_mul_f32 v39,v39,v79
v_mfma_f32_16x16x32_bf16 v[28:31],a[152:155],v[56:59],v[28:31]
s_waitcnt lgkmcnt(1)
ds_read_b128 v[52:55],v2 offset:26624
v_cvt_pk_bf16_f32 v84,v36,v37
v_mfma_f32_16x16x32_bf16 v[32:35],a[156:159],v[56:59],v[32:35]
v_cmp_eq_u32 vcc,s60,v16
v_cvt_pk_bf16_f32 v85,v38,v39
v_fma_f32 v40,-v96,v24,v40
v_fma_f32 v41,-v96,v25,v41
v_mfma_f32_16x16x32_bf16 v[28:31],a[160:163],v[60:63],v[28:31]
s_cmp_eq_u32 vcc_lo,-1
ds_read_b128 v[56:59],v2 offset:27648
v_fma_f32 v42,-v96,v26,v42
v_mfma_f32_16x16x32_bf16 v[32:35],a[164:167],v[60:63],v[32:35]
s_cselect_b32 s67,s86,s59
s_cmp_ge_u32 s67,s59
s_cselect_b32 s68,s68,s70
s_add_u32 m0,s78,256
v_mfma_f32_16x16x32_bf16 v[28:31],a[168:171],v[64:67],v[28:31]
buffer_load_dword v6,s[36:39],s68 offen offset:0 sc1 lds
v_mfma_f32_16x16x32_bf16 v[32:35],a[172:175],v[64:67],v[32:35]
s_mov_b32 m0,s62
ds_read_b128 v[60:63],v2 offset:28672
v_fma_f32 v43,-v96,v27,v43
v_mfma_f32_16x16x32_bf16 v[28:31],a[176:179],v[68:71],v[28:31]
buffer_load_dwordx4 v0,s[36:39],s67 offen offset:0 sc1 lds
v_mfma_f32_16x16x32_bf16 v[32:35],a[180:183],v[68:71],v[32:35]
s_xor_b32 s63,s63,65536
ds_read_b128 v[64:67],v2 offset:29696
v_mul_f32 v80,v40,v40
v_mfma_f32_16x16x32_bf16 v[28:31],a[184:187],v[72:75],v[28:31]
s_add_u32 s53,s52,s55
ds_read_b128 v[68:71],v2 offset:30720
v_mul_f32 v81,v41,v41
v_mfma_f32_16x16x32_bf16 v[32:35],a[188:191],v[72:75],v[32:35]
s_waitcnt lgkmcnt(3)
v_mul_f32 v82,v42,v42
v_mul_f32 v83,v43,v43
v_mfma_f32_16x16x32_bf16 v[28:31],a[192:195],v[44:47],v[28:31]
s_waitcnt vmcnt(7)
ds_read_b128 v[72:75],v2 offset:31744
v_fma_f32 v80,v98,v80,v97
v_mfma_f32_16x16x32_bf16 v[32:35],a[196:199],v[44:47],v[32:35]
s_barrier
v_mfma_f32_16x16x32_bf16 v[28:31],a[200:203],v[48:51],v[28:31]
buffer_load_dwordx4 v0,s[36:39],s67 offen offset:1024 sc1 lds
v_mfma_f32_16x16x32_bf16 v[32:35],a[204:207],v[48:51],v[32:35]
s_cmp_eq_u32 s53,s56
ds_read_b128 v[44:47],v2 offset:32768
ds_read_b128 v[92:95],v8 offset:0
v_mfma_f32_16x16x32_bf16 v[28:31],a[208:211],v[52:55],v[28:31]
s_cselect_b32 s53,s57,s53
ds_read_b128 v[48:51],v2 offset:33792
v_mul_f32 v80,v40,v80
v_mfma_f32_16x16x32_bf16 v[32:35],a[212:215],v[52:55],v[32:35]
s_cmp_eq_u32 s53,s58
v_fma_f32 v81,v98,v81,v97
v_fma_f32 v82,v98,v82,v97
v_fma_f32 v83,v98,v83,v97
v_mul_f32 v81,v41,v81
v_mul_f32 v82,v42,v82
v_mul_f32 v83,v43,v83
v_exp_f32 v80,v80
v_exp_f32 v81,v81
v_exp_f32 v82,v82
v_exp_f32 v83,v83
v_mfma_f32_16x16x32_bf16 v[28:31],a[216:219],v[56:59],v[28:31]
buffer_load_dwordx4 v0,s[36:39],s67 offen offset:2048 sc1 lds
v_fma_f32 v80,v80,v99,v99
v_fma_f32 v81,v81,v99,v99
v_fma_f32 v82,v82,v99,v99
v_fma_f32 v83,v83,v99,v99
v_rcp_f32 v80,v80
v_rcp_f32 v81,v81
v_rcp_f32 v82,v82
v_rcp_f32 v83,v83
v_mfma_f32_16x16x32_bf16 v[32:35],a[220:223],v[56:59],v[32:35]
s_waitcnt lgkmcnt(3)
ds_read_b128 v[52:55],v2 offset:34816
v_mul_f32 v40,v40,v80
v_mul_f32 v41,v41,v81
v_mul_f32 v42,v42,v82
v_mul_f32 v43,v43,v83
v_cvt_pk_bf16_f32 v86,v40,v41
v_cvt_pk_bf16_f32 v87,v42,v43
v_mfma_f32_16x16x32_bf16 v[28:31],a[224:227],v[60:63],v[28:31]
buffer_store_dwordx4 v[84:87],v3,s[36:39],s73 offen offset:0 sc1
ds_read_b128 v[56:59],v2 offset:35840
v_mfma_f32_16x16x32_bf16 v[32:35],a[228:231],v[60:63],v[32:35]
s_waitcnt lgkmcnt(3)
s_cselect_b32 s53,s59,s53
s_cmp_ge_u32 s66,s59
v_mfma_f32_16x16x32_bf16 v[28:31],a[232:235],v[64:67],v[28:31]
buffer_load_dwordx4 v0,s[36:39],s67 offen offset:3072 sc1 lds
v_mfma_f32_16x16x32_bf16 v[32:35],a[236:239],v[64:67],v[32:35]
ds_read_b128 v[60:63],v2 offset:36864
v_mfma_f32_16x16x32_bf16 v[28:31],a[240:243],v[68:71],v[28:31]
ds_read_b128 v[64:67],v2 offset:37888
v_mfma_f32_16x16x32_bf16 v[32:35],a[244:247],v[68:71],v[32:35]
v_mfma_f32_16x16x32_bf16 v[28:31],a[248:251],v[72:75],v[28:31]
s_waitcnt lgkmcnt(2)
ds_read_b128 v[68:71],v2 offset:38912
v_mfma_f32_16x16x32_bf16 v[32:35],a[252:255],v[72:75],v[32:35]
s_cbranch_scc1 Lg1_slow1_%=
Lg1_d1w0_%=:
v_mfma_f32_16x16x32_bf16 v[36:39],a[0:3],v[44:47],0
s_add_u32 m0,s62,16384
ds_read_b128 v[72:75],v2 offset:39936
s_lshr_b32 s73,s48,0
v_mfma_f32_16x16x32_bf16 v[40:43],a[4:7],v[44:47],0
buffer_load_dwordx4 v1,s[36:39],s67 offen offset:0 sc1 lds)"
R"(
v_mfma_f32_16x16x32_bf16 v[36:39],a[8:11],v[48:51],v[36:39]
s_lshr_b32 s72,s67,6
ds_read_b128 v[44:47],v2 offset:40960
s_lshr_b32 s71,s62,2
v_mfma_f32_16x16x32_bf16 v[40:43],a[12:15],v[48:51],v[40:43]
s_cmp_lt_u32 s67,4194304
v_fma_f32 v28,-v92,v20,v28
v_fma_f32 v29,-v92,v21,v29
v_mfma_f32_16x16x32_bf16 v[36:39],a[16:19],v[52:55],v[36:39]
s_cselect_b32 s72,s59,s72
ds_read_b128 v[48:51],v2 offset:41984
v_fma_f32 v30,-v92,v22,v30
v_mfma_f32_16x16x32_bf16 v[40:43],a[20:23],v[52:55],v[40:43]
buffer_load_dwordx4 v1,s[36:39],s67 offen offset:1024 sc1 lds
v_mfma_f32_16x16x32_bf16 v[36:39],a[24:27],v[56:59],v[36:39]
s_mov_b64 s[44:45],s[46:47]
ds_read_b128 v[52:55],v2 offset:43008
v_fma_f32 v31,-v92,v23,v31
v_mfma_f32_16x16x32_bf16 v[40:43],a[28:31],v[56:59],v[40:43]
s_waitcnt lgkmcnt(3)
v_mul_f32 v76,v28,v28
v_mul_f32 v77,v29,v29
v_mfma_f32_16x16x32_bf16 v[36:39],a[32:35],v[60:63],v[36:39]
s_mov_b64 s[46:47],s[48:49]
ds_read_b128 v[56:59],v2 offset:44032
v_mul_f32 v78,v30,v30
v_mfma_f32_16x16x32_bf16 v[40:43],a[36:39],v[60:63],v[40:43]
buffer_load_dwordx4 v1,s[36:39],s67 offen offset:2048 sc1 lds
v_mfma_f32_16x16x32_bf16 v[36:39],a[40:43],v[64:67],v[36:39]
s_mov_b64 s[48:49],s[50:51]
ds_read_b128 v[60:63],v2 offset:45056
v_mul_f32 v79,v31,v31
v_mfma_f32_16x16x32_bf16 v[40:43],a[44:47],v[64:67],v[40:43]
v_fma_f32 v76,v94,v76,v93
v_mul_f32 v76,v28,v76
v_mfma_f32_16x16x32_bf16 v[36:39],a[48:51],v[68:71],v[36:39]
ds_read_b128 v[64:67],v2 offset:46080
v_fma_f32 v77,v94,v77,v93
v_mfma_f32_16x16x32_bf16 v[40:43],a[52:55],v[68:71],v[40:43]
buffer_load_dwordx4 v1,s[36:39],s67 offen offset:3072 sc1 lds
v_mfma_f32_16x16x32_bf16 v[36:39],a[56:59],v[72:75],v[36:39]
s_add_u32 m0,s71,s88
ds_read_b128 v[68:71],v2 offset:47104
v_mul_f32 v77,v29,v77
v_mfma_f32_16x16x32_bf16 v[40:43],a[60:63],v[72:75],v[40:43]
s_waitcnt lgkmcnt(3)
buffer_load_dword v10,s[36:39],s72 offen offset:0 sc1 lds
v_mfma_f32_16x16x32_bf16 v[36:39],a[64:67],v[44:47],v[36:39]
s_waitcnt vmcnt(11)
ds_read_b128 v[72:75],v2 offset:48128
v_fma_f32 v78,v94,v78,v93
v_mfma_f32_16x16x32_bf16 v[40:43],a[68:71],v[44:47],v[40:43]
s_barrier
v_mfma_f32_16x16x32_bf16 v[36:39],a[72:75],v[48:51],v[36:39]
s_xor_b32 s87,s63,65536
ds_read_b128 v[44:47],v2 offset:49152
v_fma_f32 v79,v94,v79,v93
v_mfma_f32_16x16x32_bf16 v[40:43],a[76:79],v[48:51],v[40:43]
s_cmp_ge_u32 s67,s59
v_exp_f32 v76,v76
v_mfma_f32_16x16x32_bf16 v[36:39],a[80:83],v[52:55],v[36:39]
s_cselect_b32 s86,s50,s51
ds_read_b128 v[48:51],v2 offset:50176
v_mul_f32 v78,v30,v78
v_mfma_f32_16x16x32_bf16 v[40:43],a[84:87],v[52:55],v[40:43]
s_cselect_b32 s87,s87,s64
v_exp_f32 v77,v77
v_mfma_f32_16x16x32_bf16 v[36:39],a[88:91],v[56:59],v[36:39]
s_lshr_b32 s69,s51,5
ds_read_b128 v[52:55],v2 offset:51200
v_mul_f32 v79,v31,v79
v_mfma_f32_16x16x32_bf16 v[40:43],a[92:95],v[56:59],v[40:43]
s_waitcnt lgkmcnt(3)
v_exp_f32 v78,v78
v_mfma_f32_16x16x32_bf16 v[36:39],a[96:99],v[60:63],v[36:39]
s_cmp_lt_u32 s51,4194304
ds_read_b128 v[56:59],v2 offset:52224
v_fma_f32 v76,v76,v95,v95
v_mfma_f32_16x16x32_bf16 v[40:43],a[100:103],v[60:63],v[40:43]
s_cselect_b32 s68,0xffffcfff,-1
v_exp_f32 v79,v79
v_mfma_f32_16x16x32_bf16 v[36:39],a[104:107],v[64:67],v[36:39]
s_and_b32 s68,s68,s69
ds_read_b128 v[60:63],v2 offset:53248
v_fma_f32 v77,v77,v95,v95
v_mfma_f32_16x16x32_bf16 v[40:43],a[108:111],v[64:67],v[40:43]
s_lshr_b32 s69,s52,5
v_rcp_f32 v76,v76
v_mfma_f32_16x16x32_bf16 v[36:39],a[112:115],v[68:71],v[36:39]
s_cmp_lt_u32 s52,4194304
ds_read_b128 v[64:67],v2 offset:54272
v_fma_f32 v78,v78,v95,v95
v_mfma_f32_16x16x32_bf16 v[40:43],a[116:119],v[68:71],v[40:43]
s_cselect_b32 s70,0xffffcfff,-1
v_rcp_f32 v77,v77
v_mfma_f32_16x16x32_bf16 v[36:39],a[120:123],v[72:75],v[36:39]
s_and_b32 s70,s70,s69
ds_read_b128 v[68:71],v2 offset:55296
v_fma_f32 v79,v79,v95,v95
v_mfma_f32_16x16x32_bf16 v[40:43],a[124:127],v[72:75],v[40:43]
s_waitcnt lgkmcnt(3)
v_rcp_f32 v78,v78
v_mfma_f32_16x16x32_bf16 v[36:39],a[128:131],v[44:47],v[36:39]
s_waitcnt vmcnt(10)
ds_read_b128 v[72:75],v2 offset:56320
v_mul_f32 v28,v28,v76
v_mfma_f32_16x16x32_bf16 v[40:43],a[132:135],v[44:47],v[40:43]
s_barrier
v_mfma_f32_16x16x32_bf16 v[36:39],a[136:139],v[48:51],v[36:39]
s_mov_b32 s62,s87
ds_read_b128 v[44:47],v2 offset:57344
ds_read_b32 v16,v7 offset:256
v_mfma_f32_16x16x32_bf16 v[40:43],a[140:143],v[48:51],v[40:43]
s_mov_b64 s[50:51],s[52:53]
v_rcp_f32 v79,v79
v_mfma_f32_16x16x32_bf16 v[36:39],a[144:147],v[52:55],v[36:39]
ds_read_b128 v[48:51],v2 offset:58368
v_mul_f32 v29,v29,v77
v_mfma_f32_16x16x32_bf16 v[40:43],a[148:151],v[52:55],v[40:43]
v_mul_f32 v30,v30,v78
v_mul_f32 v31,v31,v79
v_mfma_f32_16x16x32_bf16 v[36:39],a[152:155],v[56:59],v[36:39]
s_waitcnt lgkmcnt(1)
ds_read_b128 v[52:55],v2 offset:59392
v_cvt_pk_bf16_f32 v84,v28,v29
v_mfma_f32_16x16x32_bf16 v[40:43],a[156:159],v[56:59],v[40:43]
v_cmp_eq_u32 vcc,s60,v16
v_cvt_pk_bf16_f32 v85,v30,v31
v_fma_f32 v32,-v92,v24,v32
v_fma_f32 v33,-v92,v25,v33
v_mfma_f32_16x16x32_bf16 v[36:39],a[160:163],v[60:63],v[36:39]
s_cmp_eq_u32 vcc_lo,-1
ds_read_b128 v[56:59],v2 offset:60416
v_fma_f32 v34,-v92,v26,v34
v_mfma_f32_16x16x32_bf16 v[40:43],a[164:167],v[60:63],v[40:43]
s_cselect_b32 s66,s86,s59
s_cmp_ge_u32 s66,s59
s_cselect_b32 s68,s68,s70
s_add_u32 m0,s78,0
v_mfma_f32_16x16x32_bf16 v[36:39],a[168:171],v[64:67],v[36:39]
buffer_load_dword v6,s[36:39],s68 offen offset:0 sc1 lds
v_mfma_f32_16x16x32_bf16 v[40:43],a[172:175],v[64:67],v[40:43]
s_mov_b32 m0,s62
ds_read_b128 v[60:63],v2 offset:61440
v_fma_f32 v35,-v92,v27,v35
v_mfma_f32_16x16x32_bf16 v[36:39],a[176:179],v[68:71],v[36:39]
buffer_load_dwordx4 v0,s[36:39],s66 offen offset:0 sc1 lds
v_mfma_f32_16x16x32_bf16 v[40:43],a[180:183],v[68:71],v[40:43]
s_xor_b32 s64,s64,65536
ds_read_b128 v[64:67],v2 offset:62464
v_mul_f32 v80,v32,v32
v_mfma_f32_16x16x32_bf16 v[36:39],a[184:187],v[72:75],v[36:39]
s_add_u32 s74,s53,s55
ds_read_b128 v[68:71],v2 offset:63488
v_mul_f32 v81,v33,v33
v_mfma_f32_16x16x32_bf16 v[40:43],a[188:191],v[72:75],v[40:43]
s_waitcnt lgkmcnt(3)
v_mul_f32 v82,v34,v34
v_mul_f32 v83,v35,v35
v_mfma_f32_16x16x32_bf16 v[36:39],a[192:195],v[44:47],v[36:39]
s_waitcnt vmcnt(7)
ds_read_b128 v[72:75],v2 offset:64512
v_xor_b32 v2,65536,v2
v_mfma_f32_16x16x32_bf16 v[40:43],a[196:199],v[44:47],v[40:43]
s_barrier
v_mfma_f32_16x16x32_bf16 v[36:39],a[200:203],v[48:51],v[36:39]
buffer_load_dwordx4 v0,s[36:39],s66 offen offset:1024 sc1 lds
v_mfma_f32_16x16x32_bf16 v[40:43],a[204:207],v[48:51],v[40:43]
s_cmp_eq_u32 s74,s56
ds_read_b128 v[44:47],v2 offset:0
ds_read_b128 v[96:99],v8 offset:8192
v_mfma_f32_16x16x32_bf16 v[36:39],a[208:211],v[52:55],v[36:39]
s_cselect_b32 s74,s57,s74
ds_read_b128 v[48:51],v2 offset:1024
v_fma_f32 v80,v94,v80,v93
v_mfma_f32_16x16x32_bf16 v[40:43],a[212:215],v[52:55],v[40:43]
s_cmp_eq_u32 s74,s58
v_fma_f32 v81,v94,v81,v93
v_fma_f32 v82,v94,v82,v93
v_fma_f32 v83,v94,v83,v93
v_mul_f32 v80,v32,v80
v_mul_f32 v81,v33,v81
v_mul_f32 v82,v34,v82
v_mul_f32 v83,v35,v83
v_exp_f32 v80,v80
v_exp_f32 v81,v81
v_exp_f32 v82,v82
v_exp_f32 v83,v83
v_mfma_f32_16x16x32_bf16 v[36:39],a[216:219],v[56:59],v[36:39]
buffer_load_dwordx4 v0,s[36:39],s66 offen offset:2048 sc1 lds
v_fma_f32 v80,v80,v95,v95
v_fma_f32 v81,v81,v95,v95
v_fma_f32 v82,v82,v95,v95
v_fma_f32 v83,v83,v95,v95
v_rcp_f32 v80,v80
v_rcp_f32 v81,v81
v_rcp_f32 v82,v82
v_rcp_f32 v83,v83
v_mfma_f32_16x16x32_bf16 v[40:43],a[220:223],v[56:59],v[40:43]
s_waitcnt lgkmcnt(3))"
R"(
ds_read_b128 v[52:55],v2 offset:2048
v_mul_f32 v32,v32,v80
v_mul_f32 v33,v33,v81
v_mul_f32 v34,v34,v82
v_mul_f32 v35,v35,v83
v_cvt_pk_bf16_f32 v86,v32,v33
v_cvt_pk_bf16_f32 v87,v34,v35
v_mfma_f32_16x16x32_bf16 v[36:39],a[224:227],v[60:63],v[36:39]
buffer_store_dwordx4 v[84:87],v3,s[36:39],s73 offen offset:0 sc1
ds_read_b128 v[56:59],v2 offset:3072
v_xor_b32 v8,16384,v8
v_mfma_f32_16x16x32_bf16 v[40:43],a[228:231],v[60:63],v[40:43]
s_waitcnt lgkmcnt(3)
s_cselect_b32 s74,s59,s74
s_mov_b32 s52,s74
v_mfma_f32_16x16x32_bf16 v[36:39],a[232:235],v[64:67],v[36:39]
buffer_load_dwordx4 v0,s[36:39],s66 offen offset:3072 sc1 lds
v_mfma_f32_16x16x32_bf16 v[40:43],a[236:239],v[64:67],v[40:43]
s_cmp_lt_u32 s67,s59
ds_read_b128 v[60:63],v2 offset:4096
v_mfma_f32_16x16x32_bf16 v[36:39],a[240:243],v[68:71],v[36:39]
ds_read_b128 v[64:67],v2 offset:5120
v_mfma_f32_16x16x32_bf16 v[40:43],a[244:247],v[68:71],v[40:43]
v_mfma_f32_16x16x32_bf16 v[36:39],a[248:251],v[72:75],v[36:39]
s_waitcnt lgkmcnt(2)
ds_read_b128 v[68:71],v2 offset:6144
v_mfma_f32_16x16x32_bf16 v[40:43],a[252:255],v[72:75],v[40:43]
s_cbranch_scc1 Lg1_d0w0_%=
s_branch Lg1_slow0_%=
Lg1_d0w1_%=:
v_mfma_f32_16x16x32_bf16 v[28:31],a[32:35],v[44:47],0
s_add_u32 m0,s62,16384
ds_read_b128 v[72:75],v2 offset:3072
s_lshr_b32 s73,s47,0
v_mfma_f32_16x16x32_bf16 v[32:35],a[36:39],v[44:47],0
s_lshr_b32 s72,s66,6
s_lshr_b32 s71,s62,2
s_cmp_lt_u32 s66,4194304
v_mfma_f32_16x16x32_bf16 v[28:31],a[40:43],v[48:51],v[28:31]
buffer_load_dwordx4 v1,s[36:39],s66 offen offset:0 sc1 lds
v_mfma_f32_16x16x32_bf16 v[32:35],a[44:47],v[48:51],v[32:35]
s_cselect_b32 s72,s59,s72
ds_read_b128 v[44:47],v2 offset:12288
v_fma_f32 v36,-v96,v20,v36
v_mfma_f32_16x16x32_bf16 v[28:31],a[48:51],v[52:55],v[28:31]
ds_read_b128 v[48:51],v2 offset:13312
v_fma_f32 v37,-v96,v21,v37
v_mfma_f32_16x16x32_bf16 v[32:35],a[52:55],v[52:55],v[32:35]
v_fma_f32 v38,-v96,v22,v38
v_fma_f32 v39,-v96,v23,v39
v_mfma_f32_16x16x32_bf16 v[28:31],a[56:59],v[56:59],v[28:31]
buffer_load_dwordx4 v1,s[36:39],s66 offen offset:1024 sc1 lds
v_mfma_f32_16x16x32_bf16 v[32:35],a[60:63],v[56:59],v[32:35]
s_waitcnt lgkmcnt(2)
ds_read_b128 v[52:55],v2 offset:14336
v_mul_f32 v76,v36,v36
v_mfma_f32_16x16x32_bf16 v[28:31],a[0:3],v[60:63],v[28:31]
ds_read_b128 v[56:59],v2 offset:15360
v_mul_f32 v77,v37,v37
v_mfma_f32_16x16x32_bf16 v[32:35],a[4:7],v[60:63],v[32:35]
v_mul_f32 v78,v38,v38
v_mul_f32 v79,v39,v39
v_mfma_f32_16x16x32_bf16 v[28:31],a[8:11],v[64:67],v[28:31]
buffer_load_dwordx4 v1,s[36:39],s66 offen offset:2048 sc1 lds
v_mfma_f32_16x16x32_bf16 v[32:35],a[12:15],v[64:67],v[32:35]
ds_read_b128 v[60:63],v2 offset:8192
v_fma_f32 v76,v98,v76,v97
v_mfma_f32_16x16x32_bf16 v[28:31],a[16:19],v[68:71],v[28:31]
ds_read_b128 v[64:67],v2 offset:9216
v_mul_f32 v76,v36,v76
v_mfma_f32_16x16x32_bf16 v[32:35],a[20:23],v[68:71],v[32:35]
v_exp_f32 v76,v76
v_mfma_f32_16x16x32_bf16 v[28:31],a[24:27],v[72:75],v[28:31]
buffer_load_dwordx4 v1,s[36:39],s66 offen offset:3072 sc1 lds
v_mfma_f32_16x16x32_bf16 v[32:35],a[28:31],v[72:75],v[32:35]
s_waitcnt lgkmcnt(2)
ds_read_b128 v[68:71],v2 offset:10240
s_add_u32 m0,s71,s88
v_mfma_f32_16x16x32_bf16 v[28:31],a[96:99],v[44:47],v[28:31]
s_xor_b32 s87,s64,65536
ds_read_b128 v[72:75],v2 offset:11264
buffer_load_dword v10,s[36:39],s72 offen offset:0 sc1 lds
s_waitcnt vmcnt(10)
v_mfma_f32_16x16x32_bf16 v[32:35],a[100:103],v[44:47],v[32:35]
s_barrier
v_mfma_f32_16x16x32_bf16 v[28:31],a[104:107],v[48:51],v[28:31]
s_cmp_ge_u32 s66,s59
ds_read_b128 v[44:47],v2 offset:20480
v_fma_f32 v76,v76,v99,v99
v_mfma_f32_16x16x32_bf16 v[32:35],a[108:111],v[48:51],v[32:35]
s_cselect_b32 s86,s49,s50
v_rcp_f32 v76,v76
v_mfma_f32_16x16x32_bf16 v[28:31],a[112:115],v[52:55],v[28:31]
s_cselect_b32 s87,s87,s63
ds_read_b128 v[48:51],v2 offset:21504
v_fma_f32 v77,v98,v77,v97
v_mfma_f32_16x16x32_bf16 v[32:35],a[116:119],v[52:55],v[32:35]
s_mov_b32 s62,s87
v_mul_f32 v77,v37,v77
v_fma_f32 v78,v98,v78,v97
v_mfma_f32_16x16x32_bf16 v[28:31],a[120:123],v[56:59],v[28:31]
s_mov_b32 m0,s62
ds_read_b128 v[52:55],v2 offset:22528
v_mul_f32 v78,v38,v78
v_mfma_f32_16x16x32_bf16 v[32:35],a[124:127],v[56:59],v[32:35]
s_waitcnt lgkmcnt(3)
v_exp_f32 v77,v77
v_mfma_f32_16x16x32_bf16 v[28:31],a[64:67],v[60:63],v[28:31]
ds_read_b128 v[56:59],v2 offset:23552
v_fma_f32 v77,v77,v99,v99
v_mfma_f32_16x16x32_bf16 v[32:35],a[68:71],v[60:63],v[32:35]
v_exp_f32 v78,v78
v_mfma_f32_16x16x32_bf16 v[28:31],a[72:75],v[64:67],v[28:31]
ds_read_b128 v[60:63],v2 offset:16384
v_fma_f32 v78,v78,v99,v99
v_mfma_f32_16x16x32_bf16 v[32:35],a[76:79],v[64:67],v[32:35]
v_rcp_f32 v77,v77
v_mfma_f32_16x16x32_bf16 v[28:31],a[80:83],v[68:71],v[28:31]
ds_read_b128 v[64:67],v2 offset:17408
v_fma_f32 v79,v98,v79,v97
v_mfma_f32_16x16x32_bf16 v[32:35],a[84:87],v[68:71],v[32:35]
v_rcp_f32 v78,v78
v_mfma_f32_16x16x32_bf16 v[28:31],a[88:91],v[72:75],v[28:31]
ds_read_b128 v[68:71],v2 offset:18432
v_mul_f32 v79,v39,v79
v_mfma_f32_16x16x32_bf16 v[32:35],a[92:95],v[72:75],v[32:35]
s_waitcnt lgkmcnt(3)
v_exp_f32 v79,v79
v_mfma_f32_16x16x32_bf16 v[28:31],a[160:163],v[44:47],v[28:31]
s_nop 0
ds_read_b128 v[72:75],v2 offset:19456
v_fma_f32 v79,v79,v99,v99
v_mfma_f32_16x16x32_bf16 v[32:35],a[164:167],v[44:47],v[32:35]
s_barrier
v_mfma_f32_16x16x32_bf16 v[28:31],a[168:171],v[48:51],v[28:31]
ds_read_b128 v[44:47],v2 offset:28672
ds_read_b32 v16,v7 offset:0
v_mfma_f32_16x16x32_bf16 v[32:35],a[172:175],v[48:51],v[32:35]
v_rcp_f32 v79,v79
v_mfma_f32_16x16x32_bf16 v[28:31],a[176:179],v[52:55],v[28:31]
ds_read_b128 v[48:51],v2 offset:29696
v_mul_f32 v36,v36,v76
v_mfma_f32_16x16x32_bf16 v[32:35],a[180:183],v[52:55],v[32:35]
v_mul_f32 v37,v37,v77
v_mul_f32 v38,v38,v78
v_mfma_f32_16x16x32_bf16 v[28:31],a[184:187],v[56:59],v[28:31]
s_waitcnt lgkmcnt(1)
ds_read_b128 v[52:55],v2 offset:30720
v_mul_f32 v39,v39,v79
v_mfma_f32_16x16x32_bf16 v[32:35],a[188:191],v[56:59],v[32:35]
v_cmp_eq_u32 vcc,s60,v16
v_cvt_pk_bf16_f32 v84,v36,v37
v_cvt_pk_bf16_f32 v85,v38,v39
v_fma_f32 v40,-v96,v24,v40
v_mfma_f32_16x16x32_bf16 v[28:31],a[128:131],v[60:63],v[28:31]
s_cmp_eq_u32 vcc_lo,-1
ds_read_b128 v[56:59],v2 offset:31744
s_cselect_b32 s67,s86,s59
v_mfma_f32_16x16x32_bf16 v[32:35],a[132:135],v[60:63],v[32:35]
buffer_load_dwordx4 v0,s[36:39],s67 offen offset:0 sc1 lds
v_mfma_f32_16x16x32_bf16 v[28:31],a[136:139],v[64:67],v[28:31]
s_xor_b32 s63,s63,65536
ds_read_b128 v[60:63],v2 offset:24576
v_fma_f32 v41,-v96,v25,v41
v_mfma_f32_16x16x32_bf16 v[32:35],a[140:143],v[64:67],v[32:35]
s_add_u32 s53,s52,s55
v_fma_f32 v42,-v96,v26,v42
v_fma_f32 v43,-v96,v27,v43
v_mfma_f32_16x16x32_bf16 v[28:31],a[144:147],v[68:71],v[28:31]
s_cmp_eq_u32 s53,s56
ds_read_b128 v[64:67],v2 offset:25600
v_mul_f32 v80,v40,v40
v_mfma_f32_16x16x32_bf16 v[32:35],a[148:151],v[68:71],v[32:35]
buffer_load_dwordx4 v0,s[36:39],s67 offen offset:1024 sc1 lds
v_mfma_f32_16x16x32_bf16 v[28:31],a[152:155],v[72:75],v[28:31]
s_cselect_b32 s53,s57,s53
ds_read_b128 v[68:71],v2 offset:26624
v_mul_f32 v81,v41,v41
v_mfma_f32_16x16x32_bf16 v[32:35],a[156:159],v[72:75],v[32:35]
s_waitcnt lgkmcnt(3)
v_mul_f32 v82,v42,v42
v_mul_f32 v83,v43,v43
v_mfma_f32_16x16x32_bf16 v[28:31],a[224:227],v[44:47],v[28:31]
s_waitcnt vmcnt(7)
ds_read_b128 v[72:75],v2 offset:27648
v_fma_f32 v80,v98,v80,v97
v_mfma_f32_16x16x32_bf16 v[32:35],a[228:231],v[44:47],v[32:35]
s_barrier
v_mfma_f32_16x16x32_bf16 v[28:31],a[232:235],v[48:51],v[28:31]
s_cmp_eq_u32 s53,s58
ds_read_b128 v[44:47],v2 offset:36864
ds_read_b128 v[92:95],v8 offset:0
v_mfma_f32_16x16x32_bf16 v[32:35],a[236:239],v[48:51],v[32:35]
buffer_load_dwordx4 v0,s[36:39],s67 offen offset:2048 sc1 lds
v_mfma_f32_16x16x32_bf16 v[28:31],a[240:243],v[52:55],v[28:31]
s_cselect_b32 s53,s59,s53
ds_read_b128 v[48:51],v2 offset:37888
v_mul_f32 v80,v40,v80
v_mfma_f32_16x16x32_bf16 v[32:35],a[244:247],v[52:55],v[32:35]
s_cmp_ge_u32 s66,s59)"
R"(
v_exp_f32 v80,v80
v_mfma_f32_16x16x32_bf16 v[28:31],a[248:251],v[56:59],v[28:31]
v_fma_f32 v81,v98,v81,v97
v_fma_f32 v82,v98,v82,v97
v_fma_f32 v83,v98,v83,v97
v_mul_f32 v81,v41,v81
v_mul_f32 v82,v42,v82
v_mul_f32 v83,v43,v83
v_exp_f32 v81,v81
v_exp_f32 v82,v82
v_exp_f32 v83,v83
v_mfma_f32_16x16x32_bf16 v[32:35],a[252:255],v[56:59],v[32:35]
s_waitcnt lgkmcnt(3)
ds_read_b128 v[52:55],v2 offset:38912
buffer_load_dwordx4 v0,s[36:39],s67 offen offset:3072 sc1 lds
v_fma_f32 v80,v80,v99,v99
v_fma_f32 v81,v81,v99,v99
v_fma_f32 v82,v82,v99,v99
v_fma_f32 v83,v83,v99,v99
v_rcp_f32 v80,v80
v_rcp_f32 v81,v81
v_rcp_f32 v82,v82
v_rcp_f32 v83,v83
v_mfma_f32_16x16x32_bf16 v[28:31],a[192:195],v[60:63],v[28:31]
s_waitcnt lgkmcnt(2)
v_mul_f32 v40,v40,v80
v_mul_f32 v41,v41,v81
v_mul_f32 v42,v42,v82
v_mul_f32 v43,v43,v83
v_cvt_pk_bf16_f32 v86,v40,v41
v_cvt_pk_bf16_f32 v87,v42,v43
v_mfma_f32_16x16x32_bf16 v[32:35],a[196:199],v[60:63],v[32:35]
ds_read_b128 v[56:59],v2 offset:39936
buffer_store_dwordx4 v[84:87],v3,s[36:39],s73 offen offset:0 sc1
v_mfma_f32_16x16x32_bf16 v[28:31],a[200:203],v[64:67],v[28:31]
ds_read_b128 v[60:63],v2 offset:32768
v_mfma_f32_16x16x32_bf16 v[32:35],a[204:207],v[64:67],v[32:35]
v_mfma_f32_16x16x32_bf16 v[28:31],a[208:211],v[68:71],v[28:31]
ds_read_b128 v[64:67],v2 offset:33792
v_mfma_f32_16x16x32_bf16 v[32:35],a[212:215],v[68:71],v[32:35]
v_mfma_f32_16x16x32_bf16 v[28:31],a[216:219],v[72:75],v[28:31]
s_waitcnt lgkmcnt(2)
ds_read_b128 v[68:71],v2 offset:34816
v_mfma_f32_16x16x32_bf16 v[32:35],a[220:223],v[72:75],v[32:35]
s_cbranch_scc1 Lg1_slow1_%=
Lg1_d1w1_%=:
v_mfma_f32_16x16x32_bf16 v[36:39],a[32:35],v[44:47],0
s_add_u32 m0,s62,16384
ds_read_b128 v[72:75],v2 offset:35840
s_lshr_b32 s73,s48,0
v_mfma_f32_16x16x32_bf16 v[40:43],a[36:39],v[44:47],0
s_lshr_b32 s72,s67,6
s_lshr_b32 s71,s62,2
s_cmp_lt_u32 s67,4194304
v_mfma_f32_16x16x32_bf16 v[36:39],a[40:43],v[48:51],v[36:39]
buffer_load_dwordx4 v1,s[36:39],s67 offen offset:0 sc1 lds
v_mfma_f32_16x16x32_bf16 v[40:43],a[44:47],v[48:51],v[40:43]
s_cselect_b32 s72,s59,s72
ds_read_b128 v[44:47],v2 offset:45056
v_fma_f32 v28,-v92,v20,v28
v_mfma_f32_16x16x32_bf16 v[36:39],a[48:51],v[52:55],v[36:39]
s_mov_b64 s[44:45],s[46:47]
ds_read_b128 v[48:51],v2 offset:46080
v_fma_f32 v29,-v92,v21,v29
v_mfma_f32_16x16x32_bf16 v[40:43],a[52:55],v[52:55],v[40:43]
s_mov_b64 s[46:47],s[48:49]
v_fma_f32 v30,-v92,v22,v30
v_fma_f32 v31,-v92,v23,v31
v_mfma_f32_16x16x32_bf16 v[36:39],a[56:59],v[56:59],v[36:39]
buffer_load_dwordx4 v1,s[36:39],s67 offen offset:1024 sc1 lds
v_mfma_f32_16x16x32_bf16 v[40:43],a[60:63],v[56:59],v[40:43]
s_waitcnt lgkmcnt(2)
ds_read_b128 v[52:55],v2 offset:47104
v_mul_f32 v76,v28,v28
v_mfma_f32_16x16x32_bf16 v[36:39],a[0:3],v[60:63],v[36:39]
s_mov_b64 s[48:49],s[50:51]
ds_read_b128 v[56:59],v2 offset:48128
v_mul_f32 v77,v29,v29
v_mfma_f32_16x16x32_bf16 v[40:43],a[4:7],v[60:63],v[40:43]
v_mul_f32 v78,v30,v30
v_mul_f32 v79,v31,v31
v_mfma_f32_16x16x32_bf16 v[36:39],a[8:11],v[64:67],v[36:39]
buffer_load_dwordx4 v1,s[36:39],s67 offen offset:2048 sc1 lds
v_mfma_f32_16x16x32_bf16 v[40:43],a[12:15],v[64:67],v[40:43]
ds_read_b128 v[60:63],v2 offset:40960
v_fma_f32 v76,v94,v76,v93
v_mfma_f32_16x16x32_bf16 v[36:39],a[16:19],v[68:71],v[36:39]
ds_read_b128 v[64:67],v2 offset:41984
v_mul_f32 v76,v28,v76
v_mfma_f32_16x16x32_bf16 v[40:43],a[20:23],v[68:71],v[40:43]
v_exp_f32 v76,v76
v_mfma_f32_16x16x32_bf16 v[36:39],a[24:27],v[72:75],v[36:39]
buffer_load_dwordx4 v1,s[36:39],s67 offen offset:3072 sc1 lds
v_mfma_f32_16x16x32_bf16 v[40:43],a[28:31],v[72:75],v[40:43]
s_waitcnt lgkmcnt(2)
ds_read_b128 v[68:71],v2 offset:43008
s_add_u32 m0,s71,s88
v_mfma_f32_16x16x32_bf16 v[36:39],a[96:99],v[44:47],v[36:39]
s_xor_b32 s87,s63,65536
ds_read_b128 v[72:75],v2 offset:44032
buffer_load_dword v10,s[36:39],s72 offen offset:0 sc1 lds
s_waitcnt vmcnt(10)
v_mfma_f32_16x16x32_bf16 v[40:43],a[100:103],v[44:47],v[40:43]
s_barrier
v_mfma_f32_16x16x32_bf16 v[36:39],a[104:107],v[48:51],v[36:39]
s_cmp_ge_u32 s67,s59
ds_read_b128 v[44:47],v2 offset:53248
v_fma_f32 v76,v76,v95,v95
v_mfma_f32_16x16x32_bf16 v[40:43],a[108:111],v[48:51],v[40:43]
s_cselect_b32 s86,s50,s51
v_rcp_f32 v76,v76
v_mfma_f32_16x16x32_bf16 v[36:39],a[112:115],v[52:55],v[36:39]
s_cselect_b32 s87,s87,s64
ds_read_b128 v[48:51],v2 offset:54272
v_fma_f32 v77,v94,v77,v93
v_mfma_f32_16x16x32_bf16 v[40:43],a[116:119],v[52:55],v[40:43]
s_mov_b32 s62,s87
v_mul_f32 v77,v29,v77
v_fma_f32 v78,v94,v78,v93
v_mfma_f32_16x16x32_bf16 v[36:39],a[120:123],v[56:59],v[36:39]
s_mov_b32 m0,s62
ds_read_b128 v[52:55],v2 offset:55296
v_mul_f32 v78,v30,v78
v_mfma_f32_16x16x32_bf16 v[40:43],a[124:127],v[56:59],v[40:43]
s_waitcnt lgkmcnt(3)
v_exp_f32 v77,v77
v_mfma_f32_16x16x32_bf16 v[36:39],a[64:67],v[60:63],v[36:39]
s_mov_b64 s[50:51],s[52:53]
ds_read_b128 v[56:59],v2 offset:56320
v_fma_f32 v77,v77,v95,v95
v_mfma_f32_16x16x32_bf16 v[40:43],a[68:71],v[60:63],v[40:43]
v_exp_f32 v78,v78
v_mfma_f32_16x16x32_bf16 v[36:39],a[72:75],v[64:67],v[36:39]
ds_read_b128 v[60:63],v2 offset:49152
v_fma_f32 v78,v78,v95,v95
v_mfma_f32_16x16x32_bf16 v[40:43],a[76:79],v[64:67],v[40:43]
v_rcp_f32 v77,v77
v_mfma_f32_16x16x32_bf16 v[36:39],a[80:83],v[68:71],v[36:39]
ds_read_b128 v[64:67],v2 offset:50176
v_fma_f32 v79,v94,v79,v93
v_mfma_f32_16x16x32_bf16 v[40:43],a[84:87],v[68:71],v[40:43]
v_rcp_f32 v78,v78
v_mfma_f32_16x16x32_bf16 v[36:39],a[88:91],v[72:75],v[36:39]
ds_read_b128 v[68:71],v2 offset:51200
v_mul_f32 v79,v31,v79
v_mfma_f32_16x16x32_bf16 v[40:43],a[92:95],v[72:75],v[40:43]
s_waitcnt lgkmcnt(3)
v_exp_f32 v79,v79
v_mfma_f32_16x16x32_bf16 v[36:39],a[160:163],v[44:47],v[36:39]
s_nop 0
ds_read_b128 v[72:75],v2 offset:52224
v_fma_f32 v79,v79,v95,v95
v_mfma_f32_16x16x32_bf16 v[40:43],a[164:167],v[44:47],v[40:43]
s_barrier
v_mfma_f32_16x16x32_bf16 v[36:39],a[168:171],v[48:51],v[36:39]
ds_read_b128 v[44:47],v2 offset:61440
ds_read_b32 v16,v7 offset:256
v_mfma_f32_16x16x32_bf16 v[40:43],a[172:175],v[48:51],v[40:43]
v_rcp_f32 v79,v79
v_mfma_f32_16x16x32_bf16 v[36:39],a[176:179],v[52:55],v[36:39]
ds_read_b128 v[48:51],v2 offset:62464
v_mul_f32 v28,v28,v76
v_mfma_f32_16x16x32_bf16 v[40:43],a[180:183],v[52:55],v[40:43]
v_mul_f32 v29,v29,v77
v_mul_f32 v30,v30,v78
v_mfma_f32_16x16x32_bf16 v[36:39],a[184:187],v[56:59],v[36:39]
s_waitcnt lgkmcnt(1)
ds_read_b128 v[52:55],v2 offset:63488
v_mul_f32 v31,v31,v79
v_mfma_f32_16x16x32_bf16 v[40:43],a[188:191],v[56:59],v[40:43]
v_cmp_eq_u32 vcc,s60,v16
v_cvt_pk_bf16_f32 v84,v28,v29
v_cvt_pk_bf16_f32 v85,v30,v31
v_fma_f32 v32,-v92,v24,v32
v_mfma_f32_16x16x32_bf16 v[36:39],a[128:131],v[60:63],v[36:39]
s_cmp_eq_u32 vcc_lo,-1
ds_read_b128 v[56:59],v2 offset:64512
s_cselect_b32 s66,s86,s59
v_mfma_f32_16x16x32_bf16 v[40:43],a[132:135],v[60:63],v[40:43]
buffer_load_dwordx4 v0,s[36:39],s66 offen offset:0 sc1 lds
v_mfma_f32_16x16x32_bf16 v[36:39],a[136:139],v[64:67],v[36:39]
s_xor_b32 s64,s64,65536
ds_read_b128 v[60:63],v2 offset:57344
v_fma_f32 v33,-v92,v25,v33
v_mfma_f32_16x16x32_bf16 v[40:43],a[140:143],v[64:67],v[40:43]
s_add_u32 s74,s53,s55
v_fma_f32 v34,-v92,v26,v34
v_fma_f32 v35,-v92,v27,v35
v_mfma_f32_16x16x32_bf16 v[36:39],a[144:147],v[68:71],v[36:39]
s_cmp_eq_u32 s74,s56
ds_read_b128 v[64:67],v2 offset:58368
v_mul_f32 v80,v32,v32
v_mfma_f32_16x16x32_bf16 v[40:43],a[148:151],v[68:71],v[40:43]
buffer_load_dwordx4 v0,s[36:39],s66 offen offset:1024 sc1 lds
v_mfma_f32_16x16x32_bf16 v[36:39],a[152:155],v[72:75],v[36:39]
s_cselect_b32 s74,s57,s74
ds_read_b128 v[68:71],v2 offset:59392
v_mul_f32 v81,v33,v33
v_mfma_f32_16x16x32_bf16 v[40:43],a[156:159],v[72:75],v[40:43]
s_waitcnt lgkmcnt(3))"
R"(
v_mul_f32 v82,v34,v34
v_mul_f32 v83,v35,v35
v_mfma_f32_16x16x32_bf16 v[36:39],a[224:227],v[44:47],v[36:39]
s_waitcnt vmcnt(7)
ds_read_b128 v[72:75],v2 offset:60416
v_xor_b32 v2,65536,v2
v_mfma_f32_16x16x32_bf16 v[40:43],a[228:231],v[44:47],v[40:43]
s_barrier
v_mfma_f32_16x16x32_bf16 v[36:39],a[232:235],v[48:51],v[36:39]
s_cmp_eq_u32 s74,s58
ds_read_b128 v[44:47],v2 offset:4096
ds_read_b128 v[96:99],v8 offset:8192
v_mfma_f32_16x16x32_bf16 v[40:43],a[236:239],v[48:51],v[40:43]
buffer_load_dwordx4 v0,s[36:39],s66 offen offset:2048 sc1 lds
v_mfma_f32_16x16x32_bf16 v[36:39],a[240:243],v[52:55],v[36:39]
s_cselect_b32 s74,s59,s74
ds_read_b128 v[48:51],v2 offset:5120
v_fma_f32 v80,v94,v80,v93
v_mfma_f32_16x16x32_bf16 v[40:43],a[244:247],v[52:55],v[40:43]
s_mov_b32 s52,s74
v_mul_f32 v80,v32,v80
v_fma_f32 v81,v94,v81,v93
v_mfma_f32_16x16x32_bf16 v[36:39],a[248:251],v[56:59],v[36:39]
s_cmp_lt_u32 s67,s59
v_fma_f32 v82,v94,v82,v93
v_fma_f32 v83,v94,v83,v93
v_mul_f32 v81,v33,v81
v_mul_f32 v82,v34,v82
v_mul_f32 v83,v35,v83
v_exp_f32 v80,v80
v_exp_f32 v81,v81
v_exp_f32 v82,v82
v_exp_f32 v83,v83
v_mfma_f32_16x16x32_bf16 v[40:43],a[252:255],v[56:59],v[40:43]
s_waitcnt lgkmcnt(3)
ds_read_b128 v[52:55],v2 offset:6144
buffer_load_dwordx4 v0,s[36:39],s66 offen offset:3072 sc1 lds
v_fma_f32 v80,v80,v95,v95
v_fma_f32 v81,v81,v95,v95
v_fma_f32 v82,v82,v95,v95
v_fma_f32 v83,v83,v95,v95
v_rcp_f32 v80,v80
v_rcp_f32 v81,v81
v_rcp_f32 v82,v82
v_rcp_f32 v83,v83
v_mfma_f32_16x16x32_bf16 v[36:39],a[192:195],v[60:63],v[36:39]
s_waitcnt lgkmcnt(2)
v_mul_f32 v32,v32,v80
v_mul_f32 v33,v33,v81
v_mul_f32 v34,v34,v82
v_mul_f32 v35,v35,v83
v_cvt_pk_bf16_f32 v86,v32,v33
v_cvt_pk_bf16_f32 v87,v34,v35
v_mfma_f32_16x16x32_bf16 v[40:43],a[196:199],v[60:63],v[40:43]
ds_read_b128 v[56:59],v2 offset:7168
buffer_store_dwordx4 v[84:87],v3,s[36:39],s73 offen offset:0 sc1
v_xor_b32 v8,16384,v8
v_mfma_f32_16x16x32_bf16 v[36:39],a[200:203],v[64:67],v[36:39]
ds_read_b128 v[60:63],v2 offset:0
v_mfma_f32_16x16x32_bf16 v[40:43],a[204:207],v[64:67],v[40:43]
v_mfma_f32_16x16x32_bf16 v[36:39],a[208:211],v[68:71],v[36:39]
ds_read_b128 v[64:67],v2 offset:1024
v_mfma_f32_16x16x32_bf16 v[40:43],a[212:215],v[68:71],v[40:43]
v_mfma_f32_16x16x32_bf16 v[36:39],a[216:219],v[72:75],v[36:39]
s_waitcnt lgkmcnt(2)
ds_read_b128 v[68:71],v2 offset:2048
v_mfma_f32_16x16x32_bf16 v[40:43],a[220:223],v[72:75],v[40:43]
s_cbranch_scc1 Lg1_d0w1_%=
s_branch Lg1_slow0_%=
Lg1_d0w2_%=:
v_mfma_f32_16x16x32_bf16 v[28:31],a[64:67],v[44:47],0
s_add_u32 m0,s62,16384
ds_read_b128 v[72:75],v2 offset:15360
s_lshr_b32 s73,s47,0
v_mfma_f32_16x16x32_bf16 v[32:35],a[68:71],v[44:47],0
s_lshr_b32 s72,s66,6
s_lshr_b32 s71,s62,2
s_cmp_lt_u32 s66,4194304
v_mfma_f32_16x16x32_bf16 v[28:31],a[72:75],v[48:51],v[28:31]
s_cselect_b32 s72,s59,s72
ds_read_b128 v[44:47],v2 offset:0
v_mfma_f32_16x16x32_bf16 v[32:35],a[76:79],v[48:51],v[32:35]
buffer_load_dwordx4 v1,s[36:39],s66 offen offset:0 sc1 lds
v_mfma_f32_16x16x32_bf16 v[28:31],a[80:83],v[52:55],v[28:31]
ds_read_b128 v[48:51],v2 offset:1024
v_fma_f32 v36,-v96,v20,v36
v_mfma_f32_16x16x32_bf16 v[32:35],a[84:87],v[52:55],v[32:35]
v_fma_f32 v37,-v96,v21,v37
v_fma_f32 v38,-v96,v22,v38
v_mfma_f32_16x16x32_bf16 v[28:31],a[88:91],v[56:59],v[28:31]
ds_read_b128 v[52:55],v2 offset:2048
v_fma_f32 v39,-v96,v23,v39
v_mfma_f32_16x16x32_bf16 v[32:35],a[92:95],v[56:59],v[32:35]
s_waitcnt lgkmcnt(3)
buffer_load_dwordx4 v1,s[36:39],s66 offen offset:1024 sc1 lds
v_mfma_f32_16x16x32_bf16 v[28:31],a[96:99],v[60:63],v[28:31]
ds_read_b128 v[56:59],v2 offset:3072
v_mul_f32 v76,v36,v36
v_mfma_f32_16x16x32_bf16 v[32:35],a[100:103],v[60:63],v[32:35]
v_mul_f32 v77,v37,v37
v_mul_f32 v78,v38,v38
v_mfma_f32_16x16x32_bf16 v[28:31],a[104:107],v[64:67],v[28:31]
ds_read_b128 v[60:63],v2 offset:4096
v_mul_f32 v79,v39,v39
v_mfma_f32_16x16x32_bf16 v[32:35],a[108:111],v[64:67],v[32:35]
buffer_load_dwordx4 v1,s[36:39],s66 offen offset:2048 sc1 lds
v_mfma_f32_16x16x32_bf16 v[28:31],a[112:115],v[68:71],v[28:31]
ds_read_b128 v[64:67],v2 offset:5120
v_fma_f32 v76,v98,v76,v97
v_mfma_f32_16x16x32_bf16 v[32:35],a[116:119],v[68:71],v[32:35]
v_mul_f32 v76,v36,v76
v_fma_f32 v77,v98,v77,v97
v_mfma_f32_16x16x32_bf16 v[28:31],a[120:123],v[72:75],v[28:31]
ds_read_b128 v[68:71],v2 offset:6144
v_mul_f32 v77,v37,v77
v_mfma_f32_16x16x32_bf16 v[32:35],a[124:127],v[72:75],v[32:35]
s_waitcnt lgkmcnt(3)
buffer_load_dwordx4 v1,s[36:39],s66 offen offset:3072 sc1 lds
s_add_u32 m0,s71,s88
v_mfma_f32_16x16x32_bf16 v[28:31],a[0:3],v[44:47],v[28:31]
s_xor_b32 s87,s64,65536
ds_read_b128 v[72:75],v2 offset:7168
buffer_load_dword v10,s[36:39],s72 offen offset:0 sc1 lds
s_waitcnt vmcnt(10)
v_mfma_f32_16x16x32_bf16 v[32:35],a[4:7],v[44:47],v[32:35]
s_barrier
v_mfma_f32_16x16x32_bf16 v[28:31],a[8:11],v[48:51],v[28:31]
s_cmp_ge_u32 s66,s59
ds_read_b128 v[44:47],v2 offset:24576
v_fma_f32 v78,v98,v78,v97
v_mfma_f32_16x16x32_bf16 v[32:35],a[12:15],v[48:51],v[32:35]
s_cselect_b32 s86,s49,s50
v_exp_f32 v76,v76
v_mfma_f32_16x16x32_bf16 v[28:31],a[16:19],v[52:55],v[28:31]
s_cselect_b32 s87,s87,s63
ds_read_b128 v[48:51],v2 offset:25600
v_mul_f32 v78,v38,v78
v_mfma_f32_16x16x32_bf16 v[32:35],a[20:23],v[52:55],v[32:35]
s_mov_b32 s62,s87
v_exp_f32 v77,v77
v_mfma_f32_16x16x32_bf16 v[28:31],a[24:27],v[56:59],v[28:31]
s_mov_b32 m0,s62
ds_read_b128 v[52:55],v2 offset:26624
v_fma_f32 v76,v76,v99,v99
v_mfma_f32_16x16x32_bf16 v[32:35],a[28:31],v[56:59],v[32:35]
s_waitcnt lgkmcnt(3)
v_exp_f32 v78,v78
v_mfma_f32_16x16x32_bf16 v[28:31],a[32:35],v[60:63],v[28:31]
ds_read_b128 v[56:59],v2 offset:27648
v_fma_f32 v77,v77,v99,v99
v_mfma_f32_16x16x32_bf16 v[32:35],a[36:39],v[60:63],v[32:35]
v_rcp_f32 v76,v76
v_mfma_f32_16x16x32_bf16 v[28:31],a[40:43],v[64:67],v[28:31]
ds_read_b128 v[60:63],v2 offset:28672
v_fma_f32 v78,v78,v99,v99
v_mfma_f32_16x16x32_bf16 v[32:35],a[44:47],v[64:67],v[32:35]
v_rcp_f32 v77,v77
v_mfma_f32_16x16x32_bf16 v[28:31],a[48:51],v[68:71],v[28:31]
ds_read_b128 v[64:67],v2 offset:29696
v_fma_f32 v79,v98,v79,v97
v_mfma_f32_16x16x32_bf16 v[32:35],a[52:55],v[68:71],v[32:35]
v_rcp_f32 v78,v78
v_mfma_f32_16x16x32_bf16 v[28:31],a[56:59],v[72:75],v[28:31]
ds_read_b128 v[68:71],v2 offset:30720
v_mul_f32 v79,v39,v79
v_mfma_f32_16x16x32_bf16 v[32:35],a[60:63],v[72:75],v[32:35]
s_waitcnt lgkmcnt(3)
v_exp_f32 v79,v79
v_mfma_f32_16x16x32_bf16 v[28:31],a[192:195],v[44:47],v[28:31]
s_nop 0
ds_read_b128 v[72:75],v2 offset:31744
v_fma_f32 v79,v79,v99,v99
v_mfma_f32_16x16x32_bf16 v[32:35],a[196:199],v[44:47],v[32:35]
s_barrier
v_mfma_f32_16x16x32_bf16 v[28:31],a[200:203],v[48:51],v[28:31]
ds_read_b128 v[44:47],v2 offset:16384
ds_read_b32 v16,v7 offset:0
v_mfma_f32_16x16x32_bf16 v[32:35],a[204:207],v[48:51],v[32:35]
v_rcp_f32 v79,v79
v_mfma_f32_16x16x32_bf16 v[28:31],a[208:211],v[52:55],v[28:31]
ds_read_b128 v[48:51],v2 offset:17408
v_mul_f32 v36,v36,v76
v_mfma_f32_16x16x32_bf16 v[32:35],a[212:215],v[52:55],v[32:35]
v_mul_f32 v37,v37,v77
v_mul_f32 v38,v38,v78
v_mfma_f32_16x16x32_bf16 v[28:31],a[216:219],v[56:59],v[28:31]
s_waitcnt lgkmcnt(1)
ds_read_b128 v[52:55],v2 offset:18432
v_mul_f32 v39,v39,v79
v_mfma_f32_16x16x32_bf16 v[32:35],a[220:223],v[56:59],v[32:35]
v_cmp_eq_u32 vcc,s60,v16
v_cvt_pk_bf16_f32 v84,v36,v37
v_cvt_pk_bf16_f32 v85,v38,v39
v_fma_f32 v40,-v96,v24,v40
v_mfma_f32_16x16x32_bf16 v[28:31],a[224:227],v[60:63],v[28:31]
s_cmp_eq_u32 vcc_lo,-1
ds_read_b128 v[56:59],v2 offset:19456
v_fma_f32 v41,-v96,v25,v41
v_mfma_f32_16x16x32_bf16 v[32:35],a[228:231],v[60:63],v[32:35]
s_cselect_b32 s67,s86,s59)"
R"(
v_fma_f32 v42,-v96,v26,v42
v_fma_f32 v43,-v96,v27,v43
v_mfma_f32_16x16x32_bf16 v[28:31],a[232:235],v[64:67],v[28:31]
buffer_load_dwordx4 v0,s[36:39],s67 offen offset:0 sc1 lds
v_mfma_f32_16x16x32_bf16 v[32:35],a[236:239],v[64:67],v[32:35]
s_xor_b32 s63,s63,65536
ds_read_b128 v[60:63],v2 offset:20480
v_mul_f32 v80,v40,v40
v_mfma_f32_16x16x32_bf16 v[28:31],a[240:243],v[68:71],v[28:31]
s_add_u32 s53,s52,s55
ds_read_b128 v[64:67],v2 offset:21504
v_mul_f32 v81,v41,v41
v_mfma_f32_16x16x32_bf16 v[32:35],a[244:247],v[68:71],v[32:35]
s_cmp_eq_u32 s53,s56
v_mul_f32 v82,v42,v42
v_mul_f32 v83,v43,v43
v_mfma_f32_16x16x32_bf16 v[28:31],a[248:251],v[72:75],v[28:31]
buffer_load_dwordx4 v0,s[36:39],s67 offen offset:1024 sc1 lds
v_mfma_f32_16x16x32_bf16 v[32:35],a[252:255],v[72:75],v[32:35]
s_waitcnt lgkmcnt(2)
ds_read_b128 v[68:71],v2 offset:22528
v_fma_f32 v80,v98,v80,v97
v_mfma_f32_16x16x32_bf16 v[28:31],a[128:131],v[44:47],v[28:31]
s_waitcnt vmcnt(7)
ds_read_b128 v[72:75],v2 offset:23552
v_mul_f32 v80,v40,v80
v_mfma_f32_16x16x32_bf16 v[32:35],a[132:135],v[44:47],v[32:35]
s_barrier
v_mfma_f32_16x16x32_bf16 v[28:31],a[136:139],v[48:51],v[28:31]
s_cselect_b32 s53,s57,s53
ds_read_b128 v[44:47],v2 offset:40960
ds_read_b128 v[92:95],v8 offset:0
v_mfma_f32_16x16x32_bf16 v[32:35],a[140:143],v[48:51],v[32:35]
s_cmp_eq_u32 s53,s58
v_exp_f32 v80,v80
v_mfma_f32_16x16x32_bf16 v[28:31],a[144:147],v[52:55],v[28:31]
buffer_load_dwordx4 v0,s[36:39],s67 offen offset:2048 sc1 lds
v_mfma_f32_16x16x32_bf16 v[32:35],a[148:151],v[52:55],v[32:35]
s_cselect_b32 s53,s59,s53
ds_read_b128 v[48:51],v2 offset:41984
v_fma_f32 v80,v80,v99,v99
v_mfma_f32_16x16x32_bf16 v[28:31],a[152:155],v[56:59],v[28:31]
s_cmp_ge_u32 s66,s59
ds_read_b128 v[52:55],v2 offset:43008
v_fma_f32 v81,v98,v81,v97
v_mfma_f32_16x16x32_bf16 v[32:35],a[156:159],v[56:59],v[32:35]
s_waitcnt lgkmcnt(4)
v_fma_f32 v82,v98,v82,v97
v_fma_f32 v83,v98,v83,v97
v_mul_f32 v81,v41,v81
v_mul_f32 v82,v42,v82
v_mul_f32 v83,v43,v83
v_exp_f32 v81,v81
v_exp_f32 v82,v82
v_exp_f32 v83,v83
v_mfma_f32_16x16x32_bf16 v[28:31],a[160:163],v[60:63],v[28:31]
s_waitcnt lgkmcnt(2)
buffer_load_dwordx4 v0,s[36:39],s67 offen offset:3072 sc1 lds
v_fma_f32 v81,v81,v99,v99
v_fma_f32 v82,v82,v99,v99
v_fma_f32 v83,v83,v99,v99
v_rcp_f32 v80,v80
v_rcp_f32 v81,v81
v_rcp_f32 v82,v82
v_rcp_f32 v83,v83
v_mfma_f32_16x16x32_bf16 v[32:35],a[164:167],v[60:63],v[32:35]
ds_read_b128 v[56:59],v2 offset:44032
v_mul_f32 v40,v40,v80
v_mul_f32 v41,v41,v81
v_mul_f32 v42,v42,v82
v_mul_f32 v43,v43,v83
v_cvt_pk_bf16_f32 v86,v40,v41
v_cvt_pk_bf16_f32 v87,v42,v43
v_mfma_f32_16x16x32_bf16 v[28:31],a[168:171],v[64:67],v[28:31]
buffer_store_dwordx4 v[84:87],v3,s[36:39],s73 offen offset:0 sc1
ds_read_b128 v[60:63],v2 offset:45056
v_mfma_f32_16x16x32_bf16 v[32:35],a[172:175],v[64:67],v[32:35]
v_mfma_f32_16x16x32_bf16 v[28:31],a[176:179],v[68:71],v[28:31]
ds_read_b128 v[64:67],v2 offset:46080
v_mfma_f32_16x16x32_bf16 v[32:35],a[180:183],v[68:71],v[32:35]
v_mfma_f32_16x16x32_bf16 v[28:31],a[184:187],v[72:75],v[28:31]
s_waitcnt lgkmcnt(2)
ds_read_b128 v[68:71],v2 offset:47104
v_mfma_f32_16x16x32_bf16 v[32:35],a[188:191],v[72:75],v[32:35]
s_cbranch_scc1 Lg1_slow1_%=
Lg1_d1w2_%=:
v_mfma_f32_16x16x32_bf16 v[36:39],a[64:67],v[44:47],0
s_add_u32 m0,s62,16384
ds_read_b128 v[72:75],v2 offset:48128
s_lshr_b32 s73,s48,0
v_mfma_f32_16x16x32_bf16 v[40:43],a[68:71],v[44:47],0
s_lshr_b32 s72,s67,6
s_lshr_b32 s71,s62,2
s_cmp_lt_u32 s67,4194304
v_mfma_f32_16x16x32_bf16 v[36:39],a[72:75],v[48:51],v[36:39]
s_cselect_b32 s72,s59,s72
ds_read_b128 v[44:47],v2 offset:32768
s_mov_b64 s[44:45],s[46:47]
v_mfma_f32_16x16x32_bf16 v[40:43],a[76:79],v[48:51],v[40:43]
buffer_load_dwordx4 v1,s[36:39],s67 offen offset:0 sc1 lds
v_mfma_f32_16x16x32_bf16 v[36:39],a[80:83],v[52:55],v[36:39]
s_mov_b64 s[46:47],s[48:49]
ds_read_b128 v[48:51],v2 offset:33792
v_fma_f32 v28,-v92,v20,v28
v_mfma_f32_16x16x32_bf16 v[40:43],a[84:87],v[52:55],v[40:43]
s_mov_b64 s[48:49],s[50:51]
v_fma_f32 v29,-v92,v21,v29
v_fma_f32 v30,-v92,v22,v30
v_mfma_f32_16x16x32_bf16 v[36:39],a[88:91],v[56:59],v[36:39]
ds_read_b128 v[52:55],v2 offset:34816
v_fma_f32 v31,-v92,v23,v31
v_mfma_f32_16x16x32_bf16 v[40:43],a[92:95],v[56:59],v[40:43]
s_waitcnt lgkmcnt(3)
buffer_load_dwordx4 v1,s[36:39],s67 offen offset:1024 sc1 lds
v_mfma_f32_16x16x32_bf16 v[36:39],a[96:99],v[60:63],v[36:39]
ds_read_b128 v[56:59],v2 offset:35840
v_mul_f32 v76,v28,v28
v_mfma_f32_16x16x32_bf16 v[40:43],a[100:103],v[60:63],v[40:43]
v_mul_f32 v77,v29,v29
v_mul_f32 v78,v30,v30
v_mfma_f32_16x16x32_bf16 v[36:39],a[104:107],v[64:67],v[36:39]
ds_read_b128 v[60:63],v2 offset:36864
v_mul_f32 v79,v31,v31
v_mfma_f32_16x16x32_bf16 v[40:43],a[108:111],v[64:67],v[40:43]
buffer_load_dwordx4 v1,s[36:39],s67 offen offset:2048 sc1 lds
v_mfma_f32_16x16x32_bf16 v[36:39],a[112:115],v[68:71],v[36:39]
ds_read_b128 v[64:67],v2 offset:37888
v_fma_f32 v76,v94,v76,v93
v_mfma_f32_16x16x32_bf16 v[40:43],a[116:119],v[68:71],v[40:43]
v_mul_f32 v76,v28,v76
v_fma_f32 v77,v94,v77,v93
v_mfma_f32_16x16x32_bf16 v[36:39],a[120:123],v[72:75],v[36:39]
ds_read_b128 v[68:71],v2 offset:38912
v_mul_f32 v77,v29,v77
v_mfma_f32_16x16x32_bf16 v[40:43],a[124:127],v[72:75],v[40:43]
s_waitcnt lgkmcnt(3)
buffer_load_dwordx4 v1,s[36:39],s67 offen offset:3072 sc1 lds
s_add_u32 m0,s71,s88
v_mfma_f32_16x16x32_bf16 v[36:39],a[0:3],v[44:47],v[36:39]
s_xor_b32 s87,s63,65536
ds_read_b128 v[72:75],v2 offset:39936
buffer_load_dword v10,s[36:39],s72 offen offset:0 sc1 lds
s_waitcnt vmcnt(10)
v_mfma_f32_16x16x32_bf16 v[40:43],a[4:7],v[44:47],v[40:43]
s_barrier
v_mfma_f32_16x16x32_bf16 v[36:39],a[8:11],v[48:51],v[36:39]
s_cmp_ge_u32 s67,s59
ds_read_b128 v[44:47],v2 offset:57344
v_fma_f32 v78,v94,v78,v93
v_mfma_f32_16x16x32_bf16 v[40:43],a[12:15],v[48:51],v[40:43]
s_cselect_b32 s86,s50,s51
v_exp_f32 v76,v76
v_mfma_f32_16x16x32_bf16 v[36:39],a[16:19],v[52:55],v[36:39]
s_cselect_b32 s87,s87,s64
ds_read_b128 v[48:51],v2 offset:58368
v_mul_f32 v78,v30,v78
v_mfma_f32_16x16x32_bf16 v[40:43],a[20:23],v[52:55],v[40:43]
s_mov_b32 s62,s87
v_exp_f32 v77,v77
v_mfma_f32_16x16x32_bf16 v[36:39],a[24:27],v[56:59],v[36:39]
s_mov_b32 m0,s62
ds_read_b128 v[52:55],v2 offset:59392
v_fma_f32 v76,v76,v95,v95
v_mfma_f32_16x16x32_bf16 v[40:43],a[28:31],v[56:59],v[40:43]
s_waitcnt lgkmcnt(3)
v_exp_f32 v78,v78
v_mfma_f32_16x16x32_bf16 v[36:39],a[32:35],v[60:63],v[36:39]
s_mov_b64 s[50:51],s[52:53]
ds_read_b128 v[56:59],v2 offset:60416
v_fma_f32 v77,v77,v95,v95
v_mfma_f32_16x16x32_bf16 v[40:43],a[36:39],v[60:63],v[40:43]
v_rcp_f32 v76,v76
v_mfma_f32_16x16x32_bf16 v[36:39],a[40:43],v[64:67],v[36:39]
ds_read_b128 v[60:63],v2 offset:61440
v_fma_f32 v78,v78,v95,v95
v_mfma_f32_16x16x32_bf16 v[40:43],a[44:47],v[64:67],v[40:43]
v_rcp_f32 v77,v77
v_mfma_f32_16x16x32_bf16 v[36:39],a[48:51],v[68:71],v[36:39]
ds_read_b128 v[64:67],v2 offset:62464
v_fma_f32 v79,v94,v79,v93
v_mfma_f32_16x16x32_bf16 v[40:43],a[52:55],v[68:71],v[40:43]
v_rcp_f32 v78,v78
v_mfma_f32_16x16x32_bf16 v[36:39],a[56:59],v[72:75],v[36:39]
ds_read_b128 v[68:71],v2 offset:63488
v_mul_f32 v79,v31,v79
v_mfma_f32_16x16x32_bf16 v[40:43],a[60:63],v[72:75],v[40:43]
s_waitcnt lgkmcnt(3)
v_exp_f32 v79,v79
v_mfma_f32_16x16x32_bf16 v[36:39],a[192:195],v[44:47],v[36:39]
s_nop 0
ds_read_b128 v[72:75],v2 offset:64512
v_fma_f32 v79,v79,v95,v95
v_mfma_f32_16x16x32_bf16 v[40:43],a[196:199],v[44:47],v[40:43]
s_barrier
v_mfma_f32_16x16x32_bf16 v[36:39],a[200:203],v[48:51],v[36:39]
ds_read_b128 v[44:47],v2 offset:49152
ds_read_b32 v16,v7 offset:256
v_mfma_f32_16x16x32_bf16 v[40:43],a[204:207],v[48:51],v[40:43]
v_rcp_f32 v79,v79
v_mfma_f32_16x16x32_bf16 v[36:39],a[208:211],v[52:55],v[36:39])"
R"(
ds_read_b128 v[48:51],v2 offset:50176
v_mul_f32 v28,v28,v76
v_mfma_f32_16x16x32_bf16 v[40:43],a[212:215],v[52:55],v[40:43]
v_mul_f32 v29,v29,v77
v_mul_f32 v30,v30,v78
v_mfma_f32_16x16x32_bf16 v[36:39],a[216:219],v[56:59],v[36:39]
s_waitcnt lgkmcnt(1)
ds_read_b128 v[52:55],v2 offset:51200
v_mul_f32 v31,v31,v79
v_mfma_f32_16x16x32_bf16 v[40:43],a[220:223],v[56:59],v[40:43]
v_cmp_eq_u32 vcc,s60,v16
v_cvt_pk_bf16_f32 v84,v28,v29
v_cvt_pk_bf16_f32 v85,v30,v31
v_fma_f32 v32,-v92,v24,v32
v_mfma_f32_16x16x32_bf16 v[36:39],a[224:227],v[60:63],v[36:39]
s_cmp_eq_u32 vcc_lo,-1
ds_read_b128 v[56:59],v2 offset:52224
v_fma_f32 v33,-v92,v25,v33
v_mfma_f32_16x16x32_bf16 v[40:43],a[228:231],v[60:63],v[40:43]
s_cselect_b32 s66,s86,s59
v_fma_f32 v34,-v92,v26,v34
v_fma_f32 v35,-v92,v27,v35
v_mfma_f32_16x16x32_bf16 v[36:39],a[232:235],v[64:67],v[36:39]
buffer_load_dwordx4 v0,s[36:39],s66 offen offset:0 sc1 lds
v_mfma_f32_16x16x32_bf16 v[40:43],a[236:239],v[64:67],v[40:43]
s_xor_b32 s64,s64,65536
ds_read_b128 v[60:63],v2 offset:53248
v_mul_f32 v80,v32,v32
v_mfma_f32_16x16x32_bf16 v[36:39],a[240:243],v[68:71],v[36:39]
s_add_u32 s74,s53,s55
ds_read_b128 v[64:67],v2 offset:54272
v_mul_f32 v81,v33,v33
v_mfma_f32_16x16x32_bf16 v[40:43],a[244:247],v[68:71],v[40:43]
s_cmp_eq_u32 s74,s56
v_mul_f32 v82,v34,v34
v_mul_f32 v83,v35,v35
v_mfma_f32_16x16x32_bf16 v[36:39],a[248:251],v[72:75],v[36:39]
buffer_load_dwordx4 v0,s[36:39],s66 offen offset:1024 sc1 lds
v_mfma_f32_16x16x32_bf16 v[40:43],a[252:255],v[72:75],v[40:43]
s_waitcnt lgkmcnt(2)
ds_read_b128 v[68:71],v2 offset:55296
v_fma_f32 v80,v94,v80,v93
v_mfma_f32_16x16x32_bf16 v[36:39],a[128:131],v[44:47],v[36:39]
s_waitcnt vmcnt(7)
ds_read_b128 v[72:75],v2 offset:56320
v_xor_b32 v2,65536,v2
v_mfma_f32_16x16x32_bf16 v[40:43],a[132:135],v[44:47],v[40:43]
s_barrier
v_mfma_f32_16x16x32_bf16 v[36:39],a[136:139],v[48:51],v[36:39]
s_cselect_b32 s74,s57,s74
ds_read_b128 v[44:47],v2 offset:8192
ds_read_b128 v[96:99],v8 offset:8192
v_mfma_f32_16x16x32_bf16 v[40:43],a[140:143],v[48:51],v[40:43]
s_cmp_eq_u32 s74,s58
v_mul_f32 v80,v32,v80
v_fma_f32 v81,v94,v81,v93
v_mfma_f32_16x16x32_bf16 v[36:39],a[144:147],v[52:55],v[36:39]
buffer_load_dwordx4 v0,s[36:39],s66 offen offset:2048 sc1 lds
v_mfma_f32_16x16x32_bf16 v[40:43],a[148:151],v[52:55],v[40:43]
s_cselect_b32 s74,s59,s74
ds_read_b128 v[48:51],v2 offset:9216
v_mul_f32 v81,v33,v81
v_mfma_f32_16x16x32_bf16 v[36:39],a[152:155],v[56:59],v[36:39]
s_mov_b32 s52,s74
ds_read_b128 v[52:55],v2 offset:10240
v_fma_f32 v82,v94,v82,v93
v_mfma_f32_16x16x32_bf16 v[40:43],a[156:159],v[56:59],v[40:43]
s_waitcnt lgkmcnt(4)
v_fma_f32 v83,v94,v83,v93
v_mul_f32 v82,v34,v82
v_mul_f32 v83,v35,v83
v_exp_f32 v80,v80
v_exp_f32 v81,v81
v_exp_f32 v82,v82
v_exp_f32 v83,v83
v_mfma_f32_16x16x32_bf16 v[36:39],a[160:163],v[60:63],v[36:39]
s_waitcnt lgkmcnt(2)
buffer_load_dwordx4 v0,s[36:39],s66 offen offset:3072 sc1 lds
v_fma_f32 v80,v80,v95,v95
v_fma_f32 v81,v81,v95,v95
v_fma_f32 v82,v82,v95,v95
v_fma_f32 v83,v83,v95,v95
v_rcp_f32 v80,v80
v_rcp_f32 v81,v81
v_rcp_f32 v82,v82
v_rcp_f32 v83,v83
v_mfma_f32_16x16x32_bf16 v[40:43],a[164:167],v[60:63],v[40:43]
s_cmp_lt_u32 s67,s59
ds_read_b128 v[56:59],v2 offset:11264
v_mul_f32 v32,v32,v80
v_mul_f32 v33,v33,v81
v_mul_f32 v34,v34,v82
v_mul_f32 v35,v35,v83
v_cvt_pk_bf16_f32 v86,v32,v33
v_cvt_pk_bf16_f32 v87,v34,v35
v_mfma_f32_16x16x32_bf16 v[36:39],a[168:171],v[64:67],v[36:39]
buffer_store_dwordx4 v[84:87],v3,s[36:39],s73 offen offset:0 sc1
ds_read_b128 v[60:63],v2 offset:12288
v_xor_b32 v8,16384,v8
v_mfma_f32_16x16x32_bf16 v[40:43],a[172:175],v[64:67],v[40:43]
v_mfma_f32_16x16x32_bf16 v[36:39],a[176:179],v[68:71],v[36:39]
ds_read_b128 v[64:67],v2 offset:13312
v_mfma_f32_16x16x32_bf16 v[40:43],a[180:183],v[68:71],v[40:43]
v_mfma_f32_16x16x32_bf16 v[36:39],a[184:187],v[72:75],v[36:39]
s_waitcnt lgkmcnt(2)
ds_read_b128 v[68:71],v2 offset:14336
v_mfma_f32_16x16x32_bf16 v[40:43],a[188:191],v[72:75],v[40:43]
s_cbranch_scc1 Lg1_d0w2_%=
s_branch Lg1_slow0_%=
Lg1_d0w3_%=:
v_mfma_f32_16x16x32_bf16 v[28:31],a[96:99],v[44:47],0
s_add_u32 m0,s62,16384
ds_read_b128 v[72:75],v2 offset:11264
s_lshr_b32 s73,s47,0
v_mfma_f32_16x16x32_bf16 v[32:35],a[100:103],v[44:47],0
s_lshr_b32 s72,s66,6
s_lshr_b32 s71,s62,2
s_cmp_lt_u32 s66,4194304
v_mfma_f32_16x16x32_bf16 v[28:31],a[104:107],v[48:51],v[28:31]
s_cselect_b32 s72,s59,s72
ds_read_b128 v[44:47],v2 offset:4096
v_mfma_f32_16x16x32_bf16 v[32:35],a[108:111],v[48:51],v[32:35]
v_fma_f32 v36,-v96,v20,v36
v_fma_f32 v37,-v96,v21,v37
v_mfma_f32_16x16x32_bf16 v[28:31],a[112:115],v[52:55],v[28:31]
buffer_load_dwordx4 v1,s[36:39],s66 offen offset:0 sc1 lds
v_mfma_f32_16x16x32_bf16 v[32:35],a[116:119],v[52:55],v[32:35]
ds_read_b128 v[48:51],v2 offset:5120
v_fma_f32 v38,-v96,v22,v38
v_mfma_f32_16x16x32_bf16 v[28:31],a[120:123],v[56:59],v[28:31]
ds_read_b128 v[52:55],v2 offset:6144
v_fma_f32 v39,-v96,v23,v39
v_mfma_f32_16x16x32_bf16 v[32:35],a[124:127],v[56:59],v[32:35]
s_waitcnt lgkmcnt(3)
v_mul_f32 v76,v36,v36
v_mul_f32 v77,v37,v37
v_mfma_f32_16x16x32_bf16 v[28:31],a[64:67],v[60:63],v[28:31]
buffer_load_dwordx4 v1,s[36:39],s66 offen offset:1024 sc1 lds
v_mfma_f32_16x16x32_bf16 v[32:35],a[68:71],v[60:63],v[32:35]
ds_read_b128 v[56:59],v2 offset:7168
v_mul_f32 v78,v38,v38
v_mfma_f32_16x16x32_bf16 v[28:31],a[72:75],v[64:67],v[28:31]
ds_read_b128 v[60:63],v2 offset:0
v_mul_f32 v79,v39,v39
v_mfma_f32_16x16x32_bf16 v[32:35],a[76:79],v[64:67],v[32:35]
v_fma_f32 v76,v98,v76,v97
v_mul_f32 v76,v36,v76
v_mfma_f32_16x16x32_bf16 v[28:31],a[80:83],v[68:71],v[28:31]
buffer_load_dwordx4 v1,s[36:39],s66 offen offset:2048 sc1 lds
v_mfma_f32_16x16x32_bf16 v[32:35],a[84:87],v[68:71],v[32:35]
ds_read_b128 v[64:67],v2 offset:1024
v_fma_f32 v77,v98,v77,v97
v_mfma_f32_16x16x32_bf16 v[28:31],a[88:91],v[72:75],v[28:31]
ds_read_b128 v[68:71],v2 offset:2048
v_mul_f32 v77,v37,v77
v_mfma_f32_16x16x32_bf16 v[32:35],a[92:95],v[72:75],v[32:35]
s_waitcnt lgkmcnt(3)
v_exp_f32 v76,v76
v_mfma_f32_16x16x32_bf16 v[28:31],a[32:35],v[44:47],v[28:31]
s_waitcnt vmcnt(9)
ds_read_b128 v[72:75],v2 offset:3072
v_fma_f32 v76,v76,v99,v99
v_mfma_f32_16x16x32_bf16 v[32:35],a[36:39],v[44:47],v[32:35]
s_barrier
v_mfma_f32_16x16x32_bf16 v[28:31],a[40:43],v[48:51],v[28:31]
buffer_load_dwordx4 v1,s[36:39],s66 offen offset:3072 sc1 lds
v_mfma_f32_16x16x32_bf16 v[32:35],a[44:47],v[48:51],v[32:35]
s_add_u32 m0,s71,s88
ds_read_b128 v[44:47],v2 offset:28672
v_fma_f32 v78,v98,v78,v97
v_mfma_f32_16x16x32_bf16 v[28:31],a[48:51],v[52:55],v[28:31]
buffer_load_dword v10,s[36:39],s72 offen offset:0 sc1 lds
v_mfma_f32_16x16x32_bf16 v[32:35],a[52:55],v[52:55],v[32:35]
s_xor_b32 s87,s64,65536
ds_read_b128 v[48:51],v2 offset:29696
v_fma_f32 v79,v98,v79,v97
v_mfma_f32_16x16x32_bf16 v[28:31],a[56:59],v[56:59],v[28:31]
s_cmp_ge_u32 s66,s59
ds_read_b128 v[52:55],v2 offset:30720
v_mul_f32 v78,v38,v78
v_mfma_f32_16x16x32_bf16 v[32:35],a[60:63],v[56:59],v[32:35]
s_waitcnt lgkmcnt(3)
v_exp_f32 v77,v77
v_mfma_f32_16x16x32_bf16 v[28:31],a[0:3],v[60:63],v[28:31]
s_cselect_b32 s86,s49,s50
ds_read_b128 v[56:59],v2 offset:31744
v_mul_f32 v79,v39,v79
v_mfma_f32_16x16x32_bf16 v[32:35],a[4:7],v[60:63],v[32:35]
s_cselect_b32 s87,s87,s63
v_exp_f32 v78,v78
v_mfma_f32_16x16x32_bf16 v[28:31],a[8:11],v[64:67],v[28:31]
s_mov_b32 s62,s87
ds_read_b128 v[60:63],v2 offset:24576
v_fma_f32 v77,v77,v99,v99
v_mfma_f32_16x16x32_bf16 v[32:35],a[12:15],v[64:67],v[32:35]
s_mov_b32 m0,s62
v_exp_f32 v79,v79
v_mfma_f32_16x16x32_bf16 v[28:31],a[16:19],v[68:71],v[28:31]
ds_read_b128 v[64:67],v2 offset:25600
v_fma_f32 v78,v78,v99,v99)"
R"(
v_mfma_f32_16x16x32_bf16 v[32:35],a[20:23],v[68:71],v[32:35]
v_rcp_f32 v76,v76
v_mfma_f32_16x16x32_bf16 v[28:31],a[24:27],v[72:75],v[28:31]
ds_read_b128 v[68:71],v2 offset:26624
v_fma_f32 v79,v79,v99,v99
v_mfma_f32_16x16x32_bf16 v[32:35],a[28:31],v[72:75],v[32:35]
s_waitcnt lgkmcnt(3)
v_rcp_f32 v77,v77
v_mfma_f32_16x16x32_bf16 v[28:31],a[224:227],v[44:47],v[28:31]
s_nop 0
ds_read_b128 v[72:75],v2 offset:27648
v_mul_f32 v36,v36,v76
v_mfma_f32_16x16x32_bf16 v[32:35],a[228:231],v[44:47],v[32:35]
s_barrier
v_mfma_f32_16x16x32_bf16 v[28:31],a[232:235],v[48:51],v[28:31]
ds_read_b128 v[44:47],v2 offset:20480
ds_read_b32 v16,v7 offset:0
v_mfma_f32_16x16x32_bf16 v[32:35],a[236:239],v[48:51],v[32:35]
v_rcp_f32 v78,v78
v_mfma_f32_16x16x32_bf16 v[28:31],a[240:243],v[52:55],v[28:31]
ds_read_b128 v[48:51],v2 offset:21504
v_mul_f32 v37,v37,v77
v_mfma_f32_16x16x32_bf16 v[32:35],a[244:247],v[52:55],v[32:35]
v_rcp_f32 v79,v79
v_mfma_f32_16x16x32_bf16 v[28:31],a[248:251],v[56:59],v[28:31]
s_waitcnt lgkmcnt(1)
ds_read_b128 v[52:55],v2 offset:22528
v_mul_f32 v38,v38,v78
v_mfma_f32_16x16x32_bf16 v[32:35],a[252:255],v[56:59],v[32:35]
v_cmp_eq_u32 vcc,s60,v16
v_mul_f32 v39,v39,v79
v_cvt_pk_bf16_f32 v84,v36,v37
v_cvt_pk_bf16_f32 v85,v38,v39
v_mfma_f32_16x16x32_bf16 v[28:31],a[192:195],v[60:63],v[28:31]
s_cmp_eq_u32 vcc_lo,-1
ds_read_b128 v[56:59],v2 offset:23552
v_fma_f32 v40,-v96,v24,v40
v_mfma_f32_16x16x32_bf16 v[32:35],a[196:199],v[60:63],v[32:35]
s_cselect_b32 s67,s86,s59
v_fma_f32 v41,-v96,v25,v41
v_fma_f32 v42,-v96,v26,v42
v_mfma_f32_16x16x32_bf16 v[28:31],a[200:203],v[64:67],v[28:31]
s_lshr_b32 s70,s46,5
ds_read_b128 v[60:63],v2 offset:16384
v_fma_f32 v43,-v96,v27,v43
v_mfma_f32_16x16x32_bf16 v[32:35],a[204:207],v[64:67],v[32:35]
buffer_load_dwordx4 v0,s[36:39],s67 offen offset:0 sc1 lds
v_mfma_f32_16x16x32_bf16 v[28:31],a[208:211],v[68:71],v[28:31]
s_xor_b32 s63,s63,65536
ds_read_b128 v[64:67],v2 offset:17408
v_mul_f32 v80,v40,v40
v_mfma_f32_16x16x32_bf16 v[32:35],a[212:215],v[68:71],v[32:35]
s_add_u32 s53,s52,s55
v_mul_f32 v81,v41,v41
v_mul_f32 v82,v42,v42
v_mfma_f32_16x16x32_bf16 v[28:31],a[216:219],v[72:75],v[28:31]
s_cmp_eq_u32 s53,s56
ds_read_b128 v[68:71],v2 offset:18432
v_mul_f32 v83,v43,v43
v_mfma_f32_16x16x32_bf16 v[32:35],a[220:223],v[72:75],v[32:35]
s_waitcnt lgkmcnt(3)
buffer_load_dwordx4 v0,s[36:39],s67 offen offset:1024 sc1 lds
v_mfma_f32_16x16x32_bf16 v[28:31],a[160:163],v[44:47],v[28:31]
s_waitcnt vmcnt(7)
ds_read_b128 v[72:75],v2 offset:19456
v_fma_f32 v80,v98,v80,v97
v_mfma_f32_16x16x32_bf16 v[32:35],a[164:167],v[44:47],v[32:35]
s_barrier
v_mfma_f32_16x16x32_bf16 v[28:31],a[168:171],v[48:51],v[28:31]
buffer_store_dword v5,v4,s[36:39],s70 offen offset:0 sc1
ds_read_b128 v[44:47],v2 offset:45056
ds_read_b128 v[92:95],v8 offset:0
v_mul_f32 v80,v40,v80
v_mfma_f32_16x16x32_bf16 v[32:35],a[172:175],v[48:51],v[32:35]
s_cselect_b32 s53,s57,s53
v_exp_f32 v80,v80
v_mfma_f32_16x16x32_bf16 v[28:31],a[176:179],v[52:55],v[28:31]
s_cmp_eq_u32 s53,s58
ds_read_b128 v[48:51],v2 offset:46080
v_fma_f32 v80,v80,v99,v99
v_mfma_f32_16x16x32_bf16 v[32:35],a[180:183],v[52:55],v[32:35]
buffer_load_dwordx4 v0,s[36:39],s67 offen offset:2048 sc1 lds
v_mfma_f32_16x16x32_bf16 v[28:31],a[184:187],v[56:59],v[28:31]
s_cselect_b32 s53,s59,s53
ds_read_b128 v[52:55],v2 offset:47104
v_fma_f32 v81,v98,v81,v97
v_mfma_f32_16x16x32_bf16 v[32:35],a[188:191],v[56:59],v[32:35]
s_waitcnt lgkmcnt(4)
v_rcp_f32 v80,v80
v_mfma_f32_16x16x32_bf16 v[28:31],a[128:131],v[60:63],v[28:31]
s_waitcnt lgkmcnt(2)
v_fma_f32 v82,v98,v82,v97
v_fma_f32 v83,v98,v83,v97
v_mul_f32 v81,v41,v81
v_mul_f32 v82,v42,v82
v_mul_f32 v83,v43,v83
v_exp_f32 v81,v81
v_exp_f32 v82,v82
v_exp_f32 v83,v83
v_mfma_f32_16x16x32_bf16 v[32:35],a[132:135],v[60:63],v[32:35]
s_cmp_ge_u32 s66,s59
ds_read_b128 v[56:59],v2 offset:48128
buffer_load_dwordx4 v0,s[36:39],s67 offen offset:3072 sc1 lds
v_fma_f32 v81,v81,v99,v99
v_fma_f32 v82,v82,v99,v99
v_fma_f32 v83,v83,v99,v99
v_rcp_f32 v81,v81
v_rcp_f32 v82,v82
v_rcp_f32 v83,v83
v_mfma_f32_16x16x32_bf16 v[28:31],a[136:139],v[64:67],v[28:31]
v_mul_f32 v40,v40,v80
v_mul_f32 v41,v41,v81
v_mul_f32 v42,v42,v82
v_mul_f32 v43,v43,v83
v_cvt_pk_bf16_f32 v86,v40,v41
v_cvt_pk_bf16_f32 v87,v42,v43
v_mfma_f32_16x16x32_bf16 v[32:35],a[140:143],v[64:67],v[32:35]
ds_read_b128 v[60:63],v2 offset:40960
buffer_store_dwordx4 v[84:87],v3,s[36:39],s73 offen offset:0 sc1
v_mfma_f32_16x16x32_bf16 v[28:31],a[144:147],v[68:71],v[28:31]
ds_read_b128 v[64:67],v2 offset:41984
v_mfma_f32_16x16x32_bf16 v[32:35],a[148:151],v[68:71],v[32:35]
v_mfma_f32_16x16x32_bf16 v[28:31],a[152:155],v[72:75],v[28:31]
s_waitcnt lgkmcnt(2)
ds_read_b128 v[68:71],v2 offset:43008
v_mfma_f32_16x16x32_bf16 v[32:35],a[156:159],v[72:75],v[32:35]
s_cbranch_scc1 Lg1_slow1_%=
Lg1_d1w3_%=:
v_mfma_f32_16x16x32_bf16 v[36:39],a[96:99],v[44:47],0
s_add_u32 m0,s62,16384
ds_read_b128 v[72:75],v2 offset:44032
s_lshr_b32 s73,s48,0
v_mfma_f32_16x16x32_bf16 v[40:43],a[100:103],v[44:47],0
s_lshr_b32 s72,s67,6
s_lshr_b32 s71,s62,2
s_cmp_lt_u32 s67,4194304
v_mfma_f32_16x16x32_bf16 v[36:39],a[104:107],v[48:51],v[36:39]
s_cselect_b32 s72,s59,s72
ds_read_b128 v[44:47],v2 offset:36864
s_mov_b64 s[44:45],s[46:47]
v_mfma_f32_16x16x32_bf16 v[40:43],a[108:111],v[48:51],v[40:43]
v_fma_f32 v28,-v92,v20,v28
v_fma_f32 v29,-v92,v21,v29
v_mfma_f32_16x16x32_bf16 v[36:39],a[112:115],v[52:55],v[36:39]
buffer_load_dwordx4 v1,s[36:39],s67 offen offset:0 sc1 lds
v_mfma_f32_16x16x32_bf16 v[40:43],a[116:119],v[52:55],v[40:43]
ds_read_b128 v[48:51],v2 offset:37888
v_fma_f32 v30,-v92,v22,v30
v_mfma_f32_16x16x32_bf16 v[36:39],a[120:123],v[56:59],v[36:39]
ds_read_b128 v[52:55],v2 offset:38912
v_fma_f32 v31,-v92,v23,v31
v_mfma_f32_16x16x32_bf16 v[40:43],a[124:127],v[56:59],v[40:43]
s_waitcnt lgkmcnt(3)
v_mul_f32 v76,v28,v28
v_mul_f32 v77,v29,v29
v_mfma_f32_16x16x32_bf16 v[36:39],a[64:67],v[60:63],v[36:39]
buffer_load_dwordx4 v1,s[36:39],s67 offen offset:1024 sc1 lds
v_mfma_f32_16x16x32_bf16 v[40:43],a[68:71],v[60:63],v[40:43]
ds_read_b128 v[56:59],v2 offset:39936
v_mul_f32 v78,v30,v30
v_mfma_f32_16x16x32_bf16 v[36:39],a[72:75],v[64:67],v[36:39]
ds_read_b128 v[60:63],v2 offset:32768
v_mul_f32 v79,v31,v31
v_mfma_f32_16x16x32_bf16 v[40:43],a[76:79],v[64:67],v[40:43]
v_fma_f32 v76,v94,v76,v93
v_mul_f32 v76,v28,v76
v_mfma_f32_16x16x32_bf16 v[36:39],a[80:83],v[68:71],v[36:39]
buffer_load_dwordx4 v1,s[36:39],s67 offen offset:2048 sc1 lds
v_mfma_f32_16x16x32_bf16 v[40:43],a[84:87],v[68:71],v[40:43]
ds_read_b128 v[64:67],v2 offset:33792
v_fma_f32 v77,v94,v77,v93
v_mfma_f32_16x16x32_bf16 v[36:39],a[88:91],v[72:75],v[36:39]
ds_read_b128 v[68:71],v2 offset:34816
v_mul_f32 v77,v29,v77
v_mfma_f32_16x16x32_bf16 v[40:43],a[92:95],v[72:75],v[40:43]
s_waitcnt lgkmcnt(3)
v_exp_f32 v76,v76
v_mfma_f32_16x16x32_bf16 v[36:39],a[32:35],v[44:47],v[36:39]
s_waitcnt vmcnt(9)
ds_read_b128 v[72:75],v2 offset:35840
v_fma_f32 v76,v76,v95,v95
v_mfma_f32_16x16x32_bf16 v[40:43],a[36:39],v[44:47],v[40:43]
s_barrier
v_mfma_f32_16x16x32_bf16 v[36:39],a[40:43],v[48:51],v[36:39]
buffer_load_dwordx4 v1,s[36:39],s67 offen offset:3072 sc1 lds
v_mfma_f32_16x16x32_bf16 v[40:43],a[44:47],v[48:51],v[40:43]
s_add_u32 m0,s71,s88
ds_read_b128 v[44:47],v2 offset:61440
v_fma_f32 v78,v94,v78,v93
v_mfma_f32_16x16x32_bf16 v[36:39],a[48:51],v[52:55],v[36:39]
buffer_load_dword v10,s[36:39],s72 offen offset:0 sc1 lds
v_mfma_f32_16x16x32_bf16 v[40:43],a[52:55],v[52:55],v[40:43]
s_xor_b32 s87,s63,65536
ds_read_b128 v[48:51],v2 offset:62464
v_fma_f32 v79,v94,v79,v93
v_mfma_f32_16x16x32_bf16 v[36:39],a[56:59],v[56:59],v[36:39]
s_cmp_ge_u32 s67,s59
ds_read_b128 v[52:55],v2 offset:63488
v_mul_f32 v78,v30,v78
v_mfma_f32_16x16x32_bf16 v[40:43],a[60:63],v[56:59],v[40:43])"
R"(
s_waitcnt lgkmcnt(3)
v_exp_f32 v77,v77
v_mfma_f32_16x16x32_bf16 v[36:39],a[0:3],v[60:63],v[36:39]
s_cselect_b32 s86,s50,s51
ds_read_b128 v[56:59],v2 offset:64512
v_mul_f32 v79,v31,v79
v_mfma_f32_16x16x32_bf16 v[40:43],a[4:7],v[60:63],v[40:43]
s_cselect_b32 s87,s87,s64
v_exp_f32 v78,v78
v_mfma_f32_16x16x32_bf16 v[36:39],a[8:11],v[64:67],v[36:39]
s_mov_b32 s62,s87
ds_read_b128 v[60:63],v2 offset:57344
v_fma_f32 v77,v77,v95,v95
v_mfma_f32_16x16x32_bf16 v[40:43],a[12:15],v[64:67],v[40:43]
s_mov_b32 m0,s62
v_exp_f32 v79,v79
v_mfma_f32_16x16x32_bf16 v[36:39],a[16:19],v[68:71],v[36:39]
ds_read_b128 v[64:67],v2 offset:58368
v_fma_f32 v78,v78,v95,v95
v_mfma_f32_16x16x32_bf16 v[40:43],a[20:23],v[68:71],v[40:43]
v_rcp_f32 v76,v76
v_mfma_f32_16x16x32_bf16 v[36:39],a[24:27],v[72:75],v[36:39]
ds_read_b128 v[68:71],v2 offset:59392
v_fma_f32 v79,v79,v95,v95
v_mfma_f32_16x16x32_bf16 v[40:43],a[28:31],v[72:75],v[40:43]
s_waitcnt lgkmcnt(3)
v_rcp_f32 v77,v77
v_mfma_f32_16x16x32_bf16 v[36:39],a[224:227],v[44:47],v[36:39]
s_nop 0
ds_read_b128 v[72:75],v2 offset:60416
v_mul_f32 v28,v28,v76
v_mfma_f32_16x16x32_bf16 v[40:43],a[228:231],v[44:47],v[40:43]
s_barrier
v_mfma_f32_16x16x32_bf16 v[36:39],a[232:235],v[48:51],v[36:39]
ds_read_b128 v[44:47],v2 offset:53248
ds_read_b32 v16,v7 offset:256
v_mfma_f32_16x16x32_bf16 v[40:43],a[236:239],v[48:51],v[40:43]
v_rcp_f32 v78,v78
v_mfma_f32_16x16x32_bf16 v[36:39],a[240:243],v[52:55],v[36:39]
ds_read_b128 v[48:51],v2 offset:54272
v_mul_f32 v29,v29,v77
v_mfma_f32_16x16x32_bf16 v[40:43],a[244:247],v[52:55],v[40:43]
v_rcp_f32 v79,v79
v_mfma_f32_16x16x32_bf16 v[36:39],a[248:251],v[56:59],v[36:39]
s_waitcnt lgkmcnt(1)
ds_read_b128 v[52:55],v2 offset:55296
v_mul_f32 v30,v30,v78
v_mfma_f32_16x16x32_bf16 v[40:43],a[252:255],v[56:59],v[40:43]
v_cmp_eq_u32 vcc,s60,v16
v_mul_f32 v31,v31,v79
v_cvt_pk_bf16_f32 v84,v28,v29
v_cvt_pk_bf16_f32 v85,v30,v31
v_mfma_f32_16x16x32_bf16 v[36:39],a[192:195],v[60:63],v[36:39]
s_cmp_eq_u32 vcc_lo,-1
ds_read_b128 v[56:59],v2 offset:56320
v_fma_f32 v32,-v92,v24,v32
v_mfma_f32_16x16x32_bf16 v[40:43],a[196:199],v[60:63],v[40:43]
s_cselect_b32 s66,s86,s59
v_fma_f32 v33,-v92,v25,v33
v_fma_f32 v34,-v92,v26,v34
v_mfma_f32_16x16x32_bf16 v[36:39],a[200:203],v[64:67],v[36:39]
s_lshr_b32 s70,s47,5
ds_read_b128 v[60:63],v2 offset:49152
v_fma_f32 v35,-v92,v27,v35
v_mfma_f32_16x16x32_bf16 v[40:43],a[204:207],v[64:67],v[40:43]
buffer_load_dwordx4 v0,s[36:39],s66 offen offset:0 sc1 lds
v_mfma_f32_16x16x32_bf16 v[36:39],a[208:211],v[68:71],v[36:39]
s_xor_b32 s64,s64,65536
ds_read_b128 v[64:67],v2 offset:50176
v_mul_f32 v80,v32,v32
v_mfma_f32_16x16x32_bf16 v[40:43],a[212:215],v[68:71],v[40:43]
s_add_u32 s74,s53,s55
v_mul_f32 v81,v33,v33
v_mul_f32 v82,v34,v34
v_mfma_f32_16x16x32_bf16 v[36:39],a[216:219],v[72:75],v[36:39]
s_cmp_eq_u32 s74,s56
ds_read_b128 v[68:71],v2 offset:51200
v_mul_f32 v83,v35,v35
v_mfma_f32_16x16x32_bf16 v[40:43],a[220:223],v[72:75],v[40:43]
s_waitcnt lgkmcnt(3)
buffer_load_dwordx4 v0,s[36:39],s66 offen offset:1024 sc1 lds
v_mfma_f32_16x16x32_bf16 v[36:39],a[160:163],v[44:47],v[36:39]
s_waitcnt vmcnt(7)
ds_read_b128 v[72:75],v2 offset:52224
v_xor_b32 v2,65536,v2
v_mfma_f32_16x16x32_bf16 v[40:43],a[164:167],v[44:47],v[40:43]
s_barrier
v_mfma_f32_16x16x32_bf16 v[36:39],a[168:171],v[48:51],v[36:39]
buffer_store_dword v5,v4,s[36:39],s70 offen offset:0 sc1
ds_read_b128 v[44:47],v2 offset:12288
ds_read_b128 v[96:99],v8 offset:8192
v_fma_f32 v80,v94,v80,v93
v_mfma_f32_16x16x32_bf16 v[40:43],a[172:175],v[48:51],v[40:43]
s_cselect_b32 s74,s57,s74
v_mul_f32 v80,v32,v80
v_fma_f32 v81,v94,v81,v93
v_mfma_f32_16x16x32_bf16 v[36:39],a[176:179],v[52:55],v[36:39]
s_cmp_eq_u32 s74,s58
ds_read_b128 v[48:51],v2 offset:13312
v_mul_f32 v81,v33,v81
v_mfma_f32_16x16x32_bf16 v[40:43],a[180:183],v[52:55],v[40:43]
buffer_load_dwordx4 v0,s[36:39],s66 offen offset:2048 sc1 lds
v_mfma_f32_16x16x32_bf16 v[36:39],a[184:187],v[56:59],v[36:39]
s_cselect_b32 s74,s59,s74
ds_read_b128 v[52:55],v2 offset:14336
v_fma_f32 v82,v94,v82,v93
v_mfma_f32_16x16x32_bf16 v[40:43],a[188:191],v[56:59],v[40:43]
s_waitcnt lgkmcnt(4)
v_exp_f32 v80,v80
v_mfma_f32_16x16x32_bf16 v[36:39],a[128:131],v[60:63],v[36:39]
s_waitcnt lgkmcnt(2)
v_fma_f32 v83,v94,v83,v93
v_mul_f32 v82,v34,v82
v_mul_f32 v83,v35,v83
v_exp_f32 v81,v81
v_exp_f32 v82,v82
v_exp_f32 v83,v83
v_mfma_f32_16x16x32_bf16 v[40:43],a[132:135],v[60:63],v[40:43]
s_mov_b64 s[46:47],s[48:49]
ds_read_b128 v[56:59],v2 offset:15360
buffer_load_dwordx4 v0,s[36:39],s66 offen offset:3072 sc1 lds
v_fma_f32 v80,v80,v95,v95
v_fma_f32 v81,v81,v95,v95
v_fma_f32 v82,v82,v95,v95
v_fma_f32 v83,v83,v95,v95
v_rcp_f32 v80,v80
v_rcp_f32 v81,v81
v_rcp_f32 v82,v82
v_rcp_f32 v83,v83
v_mfma_f32_16x16x32_bf16 v[36:39],a[136:139],v[64:67],v[36:39]
s_mov_b64 s[48:49],s[50:51]
v_mul_f32 v32,v32,v80
v_mul_f32 v33,v33,v81
v_mul_f32 v34,v34,v82
v_mul_f32 v35,v35,v83
v_cvt_pk_bf16_f32 v86,v32,v33
v_cvt_pk_bf16_f32 v87,v34,v35
v_mfma_f32_16x16x32_bf16 v[40:43],a[140:143],v[64:67],v[40:43]
s_mov_b64 s[50:51],s[52:53]
ds_read_b128 v[60:63],v2 offset:8192
buffer_store_dwordx4 v[84:87],v3,s[36:39],s73 offen offset:0 sc1
v_xor_b32 v8,16384,v8
s_mov_b32 s52,s74
s_cmp_lt_u32 s67,s59
v_mfma_f32_16x16x32_bf16 v[36:39],a[144:147],v[68:71],v[36:39]
ds_read_b128 v[64:67],v2 offset:9216
v_mfma_f32_16x16x32_bf16 v[40:43],a[148:151],v[68:71],v[40:43]
v_mfma_f32_16x16x32_bf16 v[36:39],a[152:155],v[72:75],v[36:39]
s_waitcnt lgkmcnt(2)
ds_read_b128 v[68:71],v2 offset:10240
v_mfma_f32_16x16x32_bf16 v[40:43],a[156:159],v[72:75],v[40:43]
s_cbranch_scc1 Lg1_d0w3_%=
s_branch Lg1_slow0_%=
Lg1_slow0_%=:
s_cmp_lt_u32 s66,s59
s_cbranch_scc1 Lg1_gs0_%=
Lg1_slowb0_%=:
s_cmp_eq_u32 s54,1
s_cselect_b32 s68,s49,s48
s_lshr_b32 s69,s68,5
s_cmp_lt_u32 s68,4194304
s_cselect_b32 s68,0xffffcfff,-1
s_and_b32 s68,s68,s69
s_add_u32 m0,s78,8192
s_nop 0
buffer_load_dword v6,s[36:39],s68 offen offset:0 sc1 lds
s_lshr_b32 s73,s47,0
v_fma_f32 v36,-v96,v20,v36
v_fma_f32 v37,-v96,v21,v37
v_fma_f32 v38,-v96,v22,v38
v_fma_f32 v39,-v96,v23,v39
v_mul_f32 v76,v36,v36
v_mul_f32 v77,v37,v37
v_mul_f32 v78,v38,v38
v_mul_f32 v79,v39,v39
v_fma_f32 v76,v98,v76,v97
v_fma_f32 v77,v98,v77,v97
v_fma_f32 v78,v98,v78,v97
v_fma_f32 v79,v98,v79,v97
v_mul_f32 v76,v36,v76
v_mul_f32 v77,v37,v77
v_mul_f32 v78,v38,v78
v_mul_f32 v79,v39,v79
v_exp_f32 v76,v76
v_exp_f32 v77,v77
v_exp_f32 v78,v78
v_exp_f32 v79,v79
v_fma_f32 v76,v76,v99,v99
v_fma_f32 v77,v77,v99,v99
v_fma_f32 v78,v78,v99,v99
v_fma_f32 v79,v79,v99,v99
v_rcp_f32 v76,v76
v_rcp_f32 v77,v77
v_rcp_f32 v78,v78
v_rcp_f32 v79,v79
v_mul_f32 v36,v36,v76
v_mul_f32 v37,v37,v77
v_mul_f32 v38,v38,v78
v_mul_f32 v39,v39,v79
v_cvt_pk_bf16_f32 v84,v36,v37)"
R"(
v_cvt_pk_bf16_f32 v85,v38,v39
s_add_u32 m0,s78,8448
s_nop 0
buffer_load_dword v6,s[36:39],s68 offen offset:0 sc1 lds
v_fma_f32 v40,-v96,v24,v40
v_fma_f32 v41,-v96,v25,v41
v_fma_f32 v42,-v96,v26,v42
v_fma_f32 v43,-v96,v27,v43
v_mul_f32 v80,v40,v40
v_mul_f32 v81,v41,v41
v_mul_f32 v82,v42,v42
v_mul_f32 v83,v43,v43
v_fma_f32 v80,v98,v80,v97
v_fma_f32 v81,v98,v81,v97
v_fma_f32 v82,v98,v82,v97
v_fma_f32 v83,v98,v83,v97
v_mul_f32 v80,v40,v80
v_mul_f32 v81,v41,v81
v_mul_f32 v82,v42,v82
v_mul_f32 v83,v43,v83
v_exp_f32 v80,v80
v_exp_f32 v81,v81
v_exp_f32 v82,v82
v_exp_f32 v83,v83
v_fma_f32 v80,v80,v99,v99
v_fma_f32 v81,v81,v99,v99
v_fma_f32 v82,v82,v99,v99
v_fma_f32 v83,v83,v99,v99
v_rcp_f32 v80,v80
v_rcp_f32 v81,v81
v_rcp_f32 v82,v82
v_rcp_f32 v83,v83
v_mul_f32 v40,v40,v80
v_mul_f32 v41,v41,v81
v_mul_f32 v42,v42,v82
v_mul_f32 v43,v43,v83
v_cvt_pk_bf16_f32 v86,v40,v41
v_cvt_pk_bf16_f32 v87,v42,v43
buffer_store_dwordx4 v[84:87],v3,s[36:39],s73 offen offset:0 sc1
s_cmp_ge_u32 s48,s59
s_cbranch_scc1 Lg1_done0_%=
s_add_u32 m0,s78,16384
s_nop 0
buffer_load_dword v6,s[36:39],s68 offen offset:0 sc1 lds
s_waitcnt vmcnt(3)
s_barrier
ds_read_b32 v16,v7 offset:8192
s_lshr_b32 s70,s46,5
buffer_store_dword v5,v4,s[36:39],s70 offen offset:0 sc1
s_mov_b32 s46,s59
s_mov_b32 s69,18432
s_waitcnt lgkmcnt(0)
v_cmp_eq_u32 vcc,s60,v16
s_nop 0
s_cmp_eq_u32 vcc_lo,-1
s_cbranch_scc1 Lg1_go0_%=
s_add_u32 m0,s78,16640
s_nop 0
buffer_load_dword v6,s[36:39],s68 offen offset:0 sc1 lds
s_waitcnt vmcnt(4)
s_barrier
ds_read_b32 v16,v7 offset:8448
s_mov_b32 s69,18688
s_waitcnt lgkmcnt(0)
v_cmp_eq_u32 vcc,s60,v16
s_nop 0
s_cmp_eq_u32 vcc_lo,-1
s_cbranch_scc1 Lg1_go0_%=
s_add_u32 m0,s78,8192
s_nop 0
buffer_load_dword v6,s[36:39],s68 offen offset:0 sc1 lds
s_waitcnt vmcnt(3)
s_barrier
ds_read_b32 v16,v7 offset:16384
s_lshr_b32 s70,s47,5
buffer_store_dword v5,v4,s[36:39],s70 offen offset:0 sc1
s_mov_b32 s47,s59
s_mov_b32 s69,10240
s_waitcnt lgkmcnt(0)
v_cmp_eq_u32 vcc,s60,v16
s_nop 0
s_cmp_eq_u32 vcc_lo,-1
s_cbranch_scc1 Lg1_go0_%=
s_add_u32 m0,s78,8448
s_nop 0
buffer_load_dword v6,s[36:39],s68 offen offset:0 sc1 lds
s_waitcnt vmcnt(3)
s_barrier
ds_read_b32 v16,v7 offset:16640
s_mov_b32 s69,10496
s_waitcnt lgkmcnt(0)
v_cmp_eq_u32 vcc,s60,v16
s_nop 0
s_cmp_eq_u32 vcc_lo,-1
s_cbranch_scc1 Lg1_go0_%=
Lg1_poll0_%=:
s_add_u32 m0,s78,16384
s_nop 0
buffer_load_dword v6,s[36:39],s68 offen offset:0 sc1 lds
s_waitcnt vmcnt(2)
s_barrier
ds_read_b32 v16,v7 offset:8192
s_mov_b32 s69,18432
s_waitcnt lgkmcnt(0)
v_cmp_eq_u32 vcc,s60,v16
s_nop 0
s_cmp_eq_u32 vcc_lo,-1
s_cbranch_scc1 Lg1_go0_%=
s_sleep 4
s_add_u32 m0,s78,16640
s_nop 0
buffer_load_dword v6,s[36:39],s68 offen offset:0 sc1 lds
s_waitcnt vmcnt(2)
s_barrier
ds_read_b32 v16,v7 offset:8448
s_mov_b32 s69,18688
s_waitcnt lgkmcnt(0)
v_cmp_eq_u32 vcc,s60,v16
s_nop 0
s_cmp_eq_u32 vcc_lo,-1
s_cbranch_scc1 Lg1_go0_%=
s_sleep 4
s_add_u32 m0,s78,8192
s_nop 0
buffer_load_dword v6,s[36:39],s68 offen offset:0 sc1 lds
s_waitcnt vmcnt(2)
s_barrier
ds_read_b32 v16,v7 offset:16384
s_mov_b32 s69,10240
s_waitcnt lgkmcnt(0)
v_cmp_eq_u32 vcc,s60,v16
s_nop 0
s_cmp_eq_u32 vcc_lo,-1
s_cbranch_scc1 Lg1_go0_%=
s_sleep 4
s_add_u32 m0,s78,8448
s_nop 0
buffer_load_dword v6,s[36:39],s68 offen offset:0 sc1 lds
s_waitcnt vmcnt(2)
s_barrier
ds_read_b32 v16,v7 offset:16640
s_mov_b32 s69,10496
s_waitcnt lgkmcnt(0)
v_cmp_eq_u32 vcc,s60,v16
s_nop 0
s_cmp_eq_u32 vcc_lo,-1
s_cbranch_scc1 Lg1_go0_%=
s_sleep 4
s_add_u32 s79,s79,1
s_cmp_lt_u32 s79,20000
s_cbranch_scc1 Lg1_poll0_%=
Lg1_go0_%=:
s_xor_b32 s62,s63,65536
s_mov_b32 m0,s62
s_nop 0
buffer_load_dwordx4 v0,s[36:39],s48 offen offset:0 sc1 lds
buffer_load_dwordx4 v0,s[36:39],s48 offen offset:1024 sc1 lds
buffer_load_dwordx4 v0,s[36:39],s48 offen offset:2048 sc1 lds
buffer_load_dwordx4 v0,s[36:39],s48 offen offset:3072 sc1 lds
s_add_u32 m0,s62,16384
s_nop 0
buffer_load_dwordx4 v1,s[36:39],s48 offen offset:0 sc1 lds
buffer_load_dwordx4 v1,s[36:39],s48 offen offset:1024 sc1 lds
buffer_load_dwordx4 v1,s[36:39],s48 offen offset:2048 sc1 lds
buffer_load_dwordx4 v1,s[36:39],s48 offen offset:3072 sc1 lds
s_lshr_b32 s72,s48,6
s_lshr_b32 s71,s62,2
s_cmp_lt_u32 s48,4194304
s_cselect_b32 s72,s59,s72
s_add_u32 m0,s71,s88
s_nop 0
buffer_load_dword v10,s[36:39],s72 offen offset:0 sc1 lds
s_cmp_eq_u32 vcc_hi,-1
s_cselect_b32 s66,s49,s59
s_xor_b32 s62,s64,65536
s_cmp_ge_u32 s66,s59
s_cselect_b32 s68,s49,s50
s_lshr_b32 s69,s68,5
s_cmp_lt_u32 s68,4194304
s_cselect_b32 s68,0xffffcfff,-1
s_and_b32 s68,s68,s69
s_add_u32 m0,s78,0
s_nop 0
buffer_load_dword v6,s[36:39],s68 offen offset:0 sc1 lds
s_mov_b32 m0,s62
s_nop 0
buffer_load_dwordx4 v0,s[36:39],s66 offen offset:0 sc1 lds
buffer_load_dwordx4 v0,s[36:39],s66 offen offset:1024 sc1 lds
buffer_load_dwordx4 v0,s[36:39],s66 offen offset:2048 sc1 lds
buffer_load_dwordx4 v0,s[36:39],s66 offen offset:3072 sc1 lds
s_waitcnt vmcnt(10)
s_barrier
s_lshl_b32 s70,s54,12
v_add_u32 v76,s70,v2
v_xor_b32 v77,4096,v76
ds_read_b128 v[44:47],v76 offset:0
ds_read_b128 v[48:51],v76 offset:1024
ds_read_b128 v[52:55],v76 offset:2048
ds_read_b128 v[56:59],v76 offset:3072
ds_read_b128 v[60:63],v77 offset:0)"
R"(
ds_read_b128 v[64:67],v77 offset:1024
ds_read_b128 v[68:71],v77 offset:2048
s_lshr_b32 s70,s46,5
buffer_store_dword v5,v4,s[36:39],s70 offen offset:0 sc1
s_mov_b32 s46,s59
s_lshr_b32 s70,s47,5
buffer_store_dword v5,v4,s[36:39],s70 offen offset:0 sc1
s_mov_b32 s47,s59
s_lshr_b32 s70,s59,5
buffer_store_dword v5,v4,s[36:39],s70 offen offset:0 sc1
s_waitcnt lgkmcnt(3)
s_setpc_b64 s[90:91]
Lg1_gs0_%=:
s_cmp_eq_u32 s54,1
s_cselect_b32 s68,s49,s48
s_lshr_b32 s69,s68,5
s_cmp_lt_u32 s68,4194304
s_cselect_b32 s68,0xffffcfff,-1
s_and_b32 s68,s68,s69
s_lshr_b32 s73,s47,0
s_add_u32 m0,s78,8192
s_nop 0
buffer_load_dword v6,s[36:39],s68 offen offset:0 sc1 lds
v_fma_f32 v36,-v96,v20,v36
v_fma_f32 v37,-v96,v21,v37
v_fma_f32 v38,-v96,v22,v38
v_fma_f32 v39,-v96,v23,v39
v_mul_f32 v76,v36,v36
v_mul_f32 v77,v37,v37
v_mul_f32 v78,v38,v38
v_mul_f32 v79,v39,v39
v_fma_f32 v76,v98,v76,v97
v_fma_f32 v77,v98,v77,v97
v_fma_f32 v78,v98,v78,v97
s_add_u32 m0,s62,16384
s_nop 0
buffer_load_dwordx4 v1,s[36:39],s66 offen offset:0 sc1 lds
v_fma_f32 v79,v98,v79,v97
v_mul_f32 v76,v36,v76
v_mul_f32 v77,v37,v77
v_mul_f32 v78,v38,v78
v_mul_f32 v79,v39,v79
v_exp_f32 v76,v76
v_exp_f32 v77,v77
v_exp_f32 v78,v78
v_exp_f32 v79,v79
v_fma_f32 v76,v76,v99,v99
v_fma_f32 v77,v77,v99,v99
buffer_load_dwordx4 v1,s[36:39],s66 offen offset:1024 sc1 lds
v_fma_f32 v78,v78,v99,v99
v_fma_f32 v79,v79,v99,v99
v_rcp_f32 v76,v76
v_rcp_f32 v77,v77
v_rcp_f32 v78,v78
v_rcp_f32 v79,v79
v_mul_f32 v36,v36,v76
v_mul_f32 v37,v37,v77
v_mul_f32 v38,v38,v78
v_mul_f32 v39,v39,v79
v_cvt_pk_bf16_f32 v84,v36,v37
buffer_load_dwordx4 v1,s[36:39],s66 offen offset:2048 sc1 lds
v_cvt_pk_bf16_f32 v85,v38,v39
v_fma_f32 v40,-v96,v24,v40
v_fma_f32 v41,-v96,v25,v41
v_fma_f32 v42,-v96,v26,v42
v_fma_f32 v43,-v96,v27,v43
v_mul_f32 v80,v40,v40
v_mul_f32 v81,v41,v41
v_mul_f32 v82,v42,v42
v_mul_f32 v83,v43,v43
v_fma_f32 v80,v98,v80,v97
v_fma_f32 v81,v98,v81,v97
buffer_load_dwordx4 v1,s[36:39],s66 offen offset:3072 sc1 lds
v_fma_f32 v82,v98,v82,v97
v_fma_f32 v83,v98,v83,v97
v_mul_f32 v80,v40,v80
v_mul_f32 v81,v41,v81
v_mul_f32 v82,v42,v82
v_mul_f32 v83,v43,v83
v_exp_f32 v80,v80
v_exp_f32 v81,v81
v_exp_f32 v82,v82
v_exp_f32 v83,v83
v_fma_f32 v80,v80,v99,v99
s_lshr_b32 s72,s66,6
s_lshr_b32 s71,s62,2
s_cmp_lt_u32 s66,4194304
s_cselect_b32 s72,s59,s72
s_add_u32 m0,s71,s88
s_nop 0
buffer_load_dword v10,s[36:39],s72 offen offset:0 sc1 lds
v_fma_f32 v81,v81,v99,v99
v_fma_f32 v82,v82,v99,v99
v_fma_f32 v83,v83,v99,v99
v_rcp_f32 v80,v80
v_rcp_f32 v81,v81
v_rcp_f32 v82,v82
v_rcp_f32 v83,v83
v_mul_f32 v40,v40,v80
v_mul_f32 v41,v41,v81
v_mul_f32 v42,v42,v82
v_mul_f32 v43,v43,v83
v_cvt_pk_bf16_f32 v86,v40,v41
v_cvt_pk_bf16_f32 v87,v42,v43
buffer_store_dwordx4 v[84:87],v3,s[36:39],s73 offen offset:0 sc1
s_waitcnt vmcnt(6)
s_barrier
ds_read_b32 v16,v7 offset:8192
s_lshl_b32 s70,s54,12
v_add_u32 v76,s70,v2
v_xor_b32 v77,4096,v76
ds_read_b128 v[44:47],v76 offset:0
ds_read_b128 v[48:51],v76 offset:1024
ds_read_b128 v[52:55],v76 offset:2048
ds_read_b128 v[56:59],v76 offset:3072
ds_read_b128 v[60:63],v77 offset:0
ds_read_b128 v[64:67],v77 offset:1024
ds_read_b128 v[68:71],v77 offset:2048
s_lshr_b32 s70,s46,5
buffer_store_dword v5,v4,s[36:39],s70 offen offset:0 sc1
s_waitcnt lgkmcnt(7)
v_cmp_eq_u32 vcc,s60,v16
s_nop 0
s_cmp_eq_u32 vcc_hi,-1
s_cselect_b32 s66,s49,s59
s_xor_b32 s62,s64,65536
s_cmp_ge_u32 s66,s59
s_cselect_b32 s68,s49,s50
s_lshr_b32 s69,s68,5
s_cmp_lt_u32 s68,4194304
s_cselect_b32 s68,0xffffcfff,-1
s_and_b32 s68,s68,s69
s_add_u32 m0,s78,0
s_nop 0
buffer_load_dword v6,s[36:39],s68 offen offset:0 sc1 lds
s_mov_b32 m0,s62
s_nop 0
buffer_load_dwordx4 v0,s[36:39],s66 offen offset:0 sc1 lds
buffer_load_dwordx4 v0,s[36:39],s66 offen offset:1024 sc1 lds
buffer_load_dwordx4 v0,s[36:39],s66 offen offset:2048 sc1 lds
buffer_load_dwordx4 v0,s[36:39],s66 offen offset:3072 sc1 lds
s_mov_b32 s46,s47
s_mov_b32 s47,s59
s_lshr_b32 s70,s59,5
buffer_store_dword v5,v4,s[36:39],s70 offen offset:0 sc1
s_waitcnt lgkmcnt(3)
s_setpc_b64 s[90:91]
Lg1_done0_%=:
s_waitcnt vmcnt(0)
s_barrier
s_lshr_b32 s70,s46,5
buffer_store_dword v5,v4,s[36:39],s70 offen offset:0 sc1
s_lshr_b32 s70,s47,5
buffer_store_dword v5,v4,s[36:39],s70 offen offset:0 sc1
s_branch Lg1_done_%=
Lg1_slow1_%=:
s_cmp_lt_u32 s67,s59
s_cbranch_scc1 Lg1_gs1_%=
Lg1_slowb1_%=:
s_cmp_eq_u32 s54,1
s_cselect_b32 s68,s50,s49
s_lshr_b32 s69,s68,5
s_cmp_lt_u32 s68,4194304
s_cselect_b32 s68,0xffffcfff,-1
s_and_b32 s68,s68,s69
s_add_u32 m0,s78,8192
s_nop 0
buffer_load_dword v6,s[36:39],s68 offen offset:0 sc1 lds
s_lshr_b32 s73,s48,0
v_fma_f32 v28,-v92,v20,v28
v_fma_f32 v29,-v92,v21,v29
v_fma_f32 v30,-v92,v22,v30
v_fma_f32 v31,-v92,v23,v31
v_mul_f32 v76,v28,v28
v_mul_f32 v77,v29,v29
v_mul_f32 v78,v30,v30
v_mul_f32 v79,v31,v31
v_fma_f32 v76,v94,v76,v93
v_fma_f32 v77,v94,v77,v93
v_fma_f32 v78,v94,v78,v93
v_fma_f32 v79,v94,v79,v93
v_mul_f32 v76,v28,v76
v_mul_f32 v77,v29,v77
v_mul_f32 v78,v30,v78
v_mul_f32 v79,v31,v79
v_exp_f32 v76,v76
v_exp_f32 v77,v77
v_exp_f32 v78,v78
v_exp_f32 v79,v79
v_fma_f32 v76,v76,v95,v95
v_fma_f32 v77,v77,v95,v95
v_fma_f32 v78,v78,v95,v95
v_fma_f32 v79,v79,v95,v95
v_rcp_f32 v76,v76
v_rcp_f32 v77,v77
v_rcp_f32 v78,v78
v_rcp_f32 v79,v79
v_mul_f32 v28,v28,v76
v_mul_f32 v29,v29,v77
v_mul_f32 v30,v30,v78)"
R"(
v_mul_f32 v31,v31,v79
v_cvt_pk_bf16_f32 v84,v28,v29
v_cvt_pk_bf16_f32 v85,v30,v31
s_add_u32 m0,s78,8448
s_nop 0
buffer_load_dword v6,s[36:39],s68 offen offset:0 sc1 lds
v_fma_f32 v32,-v92,v24,v32
v_fma_f32 v33,-v92,v25,v33
v_fma_f32 v34,-v92,v26,v34
v_fma_f32 v35,-v92,v27,v35
v_mul_f32 v80,v32,v32
v_mul_f32 v81,v33,v33
v_mul_f32 v82,v34,v34
v_mul_f32 v83,v35,v35
v_fma_f32 v80,v94,v80,v93
v_fma_f32 v81,v94,v81,v93
v_fma_f32 v82,v94,v82,v93
v_fma_f32 v83,v94,v83,v93
v_mul_f32 v80,v32,v80
v_mul_f32 v81,v33,v81
v_mul_f32 v82,v34,v82
v_mul_f32 v83,v35,v83
v_exp_f32 v80,v80
v_exp_f32 v81,v81
v_exp_f32 v82,v82
v_exp_f32 v83,v83
v_fma_f32 v80,v80,v95,v95
v_fma_f32 v81,v81,v95,v95
v_fma_f32 v82,v82,v95,v95
v_fma_f32 v83,v83,v95,v95
v_rcp_f32 v80,v80
v_rcp_f32 v81,v81
v_rcp_f32 v82,v82
v_rcp_f32 v83,v83
v_mul_f32 v32,v32,v80
v_mul_f32 v33,v33,v81
v_mul_f32 v34,v34,v82
v_mul_f32 v35,v35,v83
v_cvt_pk_bf16_f32 v86,v32,v33
v_cvt_pk_bf16_f32 v87,v34,v35
buffer_store_dwordx4 v[84:87],v3,s[36:39],s73 offen offset:0 sc1
s_cmp_ge_u32 s49,s59
s_cbranch_scc1 Lg1_done1_%=
s_add_u32 m0,s78,16384
s_nop 0
buffer_load_dword v6,s[36:39],s68 offen offset:0 sc1 lds
s_waitcnt vmcnt(3)
s_barrier
ds_read_b32 v16,v7 offset:8192
s_lshr_b32 s70,s47,5
buffer_store_dword v5,v4,s[36:39],s70 offen offset:0 sc1
s_mov_b32 s47,s59
s_mov_b32 s69,18432
s_waitcnt lgkmcnt(0)
v_cmp_eq_u32 vcc,s60,v16
s_nop 0
s_cmp_eq_u32 vcc_lo,-1
s_cbranch_scc1 Lg1_go1_%=
s_add_u32 m0,s78,16640
s_nop 0
buffer_load_dword v6,s[36:39],s68 offen offset:0 sc1 lds
s_waitcnt vmcnt(4)
s_barrier
ds_read_b32 v16,v7 offset:8448
s_mov_b32 s69,18688
s_waitcnt lgkmcnt(0)
v_cmp_eq_u32 vcc,s60,v16
s_nop 0
s_cmp_eq_u32 vcc_lo,-1
s_cbranch_scc1 Lg1_go1_%=
s_add_u32 m0,s78,8192
s_nop 0
buffer_load_dword v6,s[36:39],s68 offen offset:0 sc1 lds
s_waitcnt vmcnt(3)
s_barrier
ds_read_b32 v16,v7 offset:16384
s_lshr_b32 s70,s48,5
buffer_store_dword v5,v4,s[36:39],s70 offen offset:0 sc1
s_mov_b32 s48,s59
s_mov_b32 s69,10240
s_waitcnt lgkmcnt(0)
v_cmp_eq_u32 vcc,s60,v16
s_nop 0
s_cmp_eq_u32 vcc_lo,-1
s_cbranch_scc1 Lg1_go1_%=
s_add_u32 m0,s78,8448
s_nop 0
buffer_load_dword v6,s[36:39],s68 offen offset:0 sc1 lds
s_waitcnt vmcnt(3)
s_barrier
ds_read_b32 v16,v7 offset:16640
s_mov_b32 s69,10496
s_waitcnt lgkmcnt(0)
v_cmp_eq_u32 vcc,s60,v16
s_nop 0
s_cmp_eq_u32 vcc_lo,-1
s_cbranch_scc1 Lg1_go1_%=
Lg1_poll1_%=:
s_add_u32 m0,s78,16384
s_nop 0
buffer_load_dword v6,s[36:39],s68 offen offset:0 sc1 lds
s_waitcnt vmcnt(2)
s_barrier
ds_read_b32 v16,v7 offset:8192
s_mov_b32 s69,18432
s_waitcnt lgkmcnt(0)
v_cmp_eq_u32 vcc,s60,v16
s_nop 0
s_cmp_eq_u32 vcc_lo,-1
s_cbranch_scc1 Lg1_go1_%=
s_sleep 4
s_add_u32 m0,s78,16640
s_nop 0
buffer_load_dword v6,s[36:39],s68 offen offset:0 sc1 lds
s_waitcnt vmcnt(2)
s_barrier
ds_read_b32 v16,v7 offset:8448
s_mov_b32 s69,18688
s_waitcnt lgkmcnt(0)
v_cmp_eq_u32 vcc,s60,v16
s_nop 0
s_cmp_eq_u32 vcc_lo,-1
s_cbranch_scc1 Lg1_go1_%=
s_sleep 4
s_add_u32 m0,s78,8192
s_nop 0
buffer_load_dword v6,s[36:39],s68 offen offset:0 sc1 lds
s_waitcnt vmcnt(2)
s_barrier
ds_read_b32 v16,v7 offset:16384
s_mov_b32 s69,10240
s_waitcnt lgkmcnt(0)
v_cmp_eq_u32 vcc,s60,v16
s_nop 0
s_cmp_eq_u32 vcc_lo,-1
s_cbranch_scc1 Lg1_go1_%=
s_sleep 4
s_add_u32 m0,s78,8448
s_nop 0
buffer_load_dword v6,s[36:39],s68 offen offset:0 sc1 lds
s_waitcnt vmcnt(2)
s_barrier
ds_read_b32 v16,v7 offset:16640
s_mov_b32 s69,10496
s_waitcnt lgkmcnt(0)
v_cmp_eq_u32 vcc,s60,v16
s_nop 0
s_cmp_eq_u32 vcc_lo,-1
s_cbranch_scc1 Lg1_go1_%=
s_sleep 4
s_add_u32 s79,s79,1
s_cmp_lt_u32 s79,20000
s_cbranch_scc1 Lg1_poll1_%=
Lg1_go1_%=:
s_xor_b32 s62,s64,65536
s_mov_b32 m0,s62
s_nop 0
buffer_load_dwordx4 v0,s[36:39],s49 offen offset:0 sc1 lds
buffer_load_dwordx4 v0,s[36:39],s49 offen offset:1024 sc1 lds
buffer_load_dwordx4 v0,s[36:39],s49 offen offset:2048 sc1 lds
buffer_load_dwordx4 v0,s[36:39],s49 offen offset:3072 sc1 lds
s_add_u32 m0,s62,16384
s_nop 0
buffer_load_dwordx4 v1,s[36:39],s49 offen offset:0 sc1 lds
buffer_load_dwordx4 v1,s[36:39],s49 offen offset:1024 sc1 lds
buffer_load_dwordx4 v1,s[36:39],s49 offen offset:2048 sc1 lds
buffer_load_dwordx4 v1,s[36:39],s49 offen offset:3072 sc1 lds
s_lshr_b32 s72,s49,6
s_lshr_b32 s71,s62,2
s_cmp_lt_u32 s49,4194304
s_cselect_b32 s72,s59,s72
s_add_u32 m0,s71,s88
s_nop 0
buffer_load_dword v10,s[36:39],s72 offen offset:0 sc1 lds
s_cmp_eq_u32 vcc_hi,-1
s_cselect_b32 s67,s50,s59
s_xor_b32 s62,s63,65536
s_cmp_ge_u32 s67,s59
s_cselect_b32 s68,s50,s51
s_lshr_b32 s69,s68,5
s_cmp_lt_u32 s68,4194304
s_cselect_b32 s68,0xffffcfff,-1
s_and_b32 s68,s68,s69
s_add_u32 m0,s78,256
s_nop 0
buffer_load_dword v6,s[36:39],s68 offen offset:0 sc1 lds
s_mov_b32 m0,s62
s_nop 0
buffer_load_dwordx4 v0,s[36:39],s67 offen offset:0 sc1 lds
buffer_load_dwordx4 v0,s[36:39],s67 offen offset:1024 sc1 lds
buffer_load_dwordx4 v0,s[36:39],s67 offen offset:2048 sc1 lds
buffer_load_dwordx4 v0,s[36:39],s67 offen offset:3072 sc1 lds
s_waitcnt vmcnt(10)
s_barrier
s_lshl_b32 s70,s54,12
v_add_u32 v76,s70,v2
v_xor_b32 v77,4096,v76
ds_read_b128 v[44:47],v76 offset:32768
ds_read_b128 v[48:51],v76 offset:33792
ds_read_b128 v[52:55],v76 offset:34816)"
R"(
ds_read_b128 v[56:59],v76 offset:35840
ds_read_b128 v[60:63],v77 offset:32768
ds_read_b128 v[64:67],v77 offset:33792
ds_read_b128 v[68:71],v77 offset:34816
s_lshr_b32 s70,s47,5
buffer_store_dword v5,v4,s[36:39],s70 offen offset:0 sc1
s_mov_b32 s47,s59
s_lshr_b32 s70,s48,5
buffer_store_dword v5,v4,s[36:39],s70 offen offset:0 sc1
s_mov_b32 s48,s59
s_lshr_b32 s70,s59,5
buffer_store_dword v5,v4,s[36:39],s70 offen offset:0 sc1
s_waitcnt lgkmcnt(3)
s_setpc_b64 s[92:93]
Lg1_gs1_%=:
s_cmp_eq_u32 s54,1
s_cselect_b32 s68,s50,s49
s_lshr_b32 s69,s68,5
s_cmp_lt_u32 s68,4194304
s_cselect_b32 s68,0xffffcfff,-1
s_and_b32 s68,s68,s69
s_lshr_b32 s73,s48,0
s_add_u32 m0,s78,8192
s_nop 0
buffer_load_dword v6,s[36:39],s68 offen offset:0 sc1 lds
v_fma_f32 v28,-v92,v20,v28
v_fma_f32 v29,-v92,v21,v29
v_fma_f32 v30,-v92,v22,v30
v_fma_f32 v31,-v92,v23,v31
v_mul_f32 v76,v28,v28
v_mul_f32 v77,v29,v29
v_mul_f32 v78,v30,v30
v_mul_f32 v79,v31,v31
v_fma_f32 v76,v94,v76,v93
v_fma_f32 v77,v94,v77,v93
v_fma_f32 v78,v94,v78,v93
s_add_u32 m0,s62,16384
s_nop 0
buffer_load_dwordx4 v1,s[36:39],s67 offen offset:0 sc1 lds
v_fma_f32 v79,v94,v79,v93
v_mul_f32 v76,v28,v76
v_mul_f32 v77,v29,v77
v_mul_f32 v78,v30,v78
v_mul_f32 v79,v31,v79
v_exp_f32 v76,v76
v_exp_f32 v77,v77
v_exp_f32 v78,v78
v_exp_f32 v79,v79
v_fma_f32 v76,v76,v95,v95
v_fma_f32 v77,v77,v95,v95
buffer_load_dwordx4 v1,s[36:39],s67 offen offset:1024 sc1 lds
v_fma_f32 v78,v78,v95,v95
v_fma_f32 v79,v79,v95,v95
v_rcp_f32 v76,v76
v_rcp_f32 v77,v77
v_rcp_f32 v78,v78
v_rcp_f32 v79,v79
v_mul_f32 v28,v28,v76
v_mul_f32 v29,v29,v77
v_mul_f32 v30,v30,v78
v_mul_f32 v31,v31,v79
v_cvt_pk_bf16_f32 v84,v28,v29
buffer_load_dwordx4 v1,s[36:39],s67 offen offset:2048 sc1 lds
v_cvt_pk_bf16_f32 v85,v30,v31
v_fma_f32 v32,-v92,v24,v32
v_fma_f32 v33,-v92,v25,v33
v_fma_f32 v34,-v92,v26,v34
v_fma_f32 v35,-v92,v27,v35
v_mul_f32 v80,v32,v32
v_mul_f32 v81,v33,v33
v_mul_f32 v82,v34,v34
v_mul_f32 v83,v35,v35
v_fma_f32 v80,v94,v80,v93
v_fma_f32 v81,v94,v81,v93
buffer_load_dwordx4 v1,s[36:39],s67 offen offset:3072 sc1 lds
v_fma_f32 v82,v94,v82,v93
v_fma_f32 v83,v94,v83,v93
v_mul_f32 v80,v32,v80
v_mul_f32 v81,v33,v81
v_mul_f32 v82,v34,v82
v_mul_f32 v83,v35,v83
v_exp_f32 v80,v80
v_exp_f32 v81,v81
v_exp_f32 v82,v82
v_exp_f32 v83,v83
v_fma_f32 v80,v80,v95,v95
s_lshr_b32 s72,s67,6
s_lshr_b32 s71,s62,2
s_cmp_lt_u32 s67,4194304
s_cselect_b32 s72,s59,s72
s_add_u32 m0,s71,s88
s_nop 0
buffer_load_dword v10,s[36:39],s72 offen offset:0 sc1 lds
v_fma_f32 v81,v81,v95,v95
v_fma_f32 v82,v82,v95,v95
v_fma_f32 v83,v83,v95,v95
v_rcp_f32 v80,v80
v_rcp_f32 v81,v81
v_rcp_f32 v82,v82
v_rcp_f32 v83,v83
v_mul_f32 v32,v32,v80
v_mul_f32 v33,v33,v81
v_mul_f32 v34,v34,v82
v_mul_f32 v35,v35,v83
v_cvt_pk_bf16_f32 v86,v32,v33
v_cvt_pk_bf16_f32 v87,v34,v35
buffer_store_dwordx4 v[84:87],v3,s[36:39],s73 offen offset:0 sc1
s_waitcnt vmcnt(6)
s_barrier
ds_read_b32 v16,v7 offset:8192
s_lshl_b32 s70,s54,12
v_add_u32 v76,s70,v2
v_xor_b32 v77,4096,v76
ds_read_b128 v[44:47],v76 offset:32768
ds_read_b128 v[48:51],v76 offset:33792
ds_read_b128 v[52:55],v76 offset:34816
ds_read_b128 v[56:59],v76 offset:35840
ds_read_b128 v[60:63],v77 offset:32768
ds_read_b128 v[64:67],v77 offset:33792
ds_read_b128 v[68:71],v77 offset:34816
s_lshr_b32 s70,s47,5
buffer_store_dword v5,v4,s[36:39],s70 offen offset:0 sc1
s_waitcnt lgkmcnt(7)
v_cmp_eq_u32 vcc,s60,v16
s_nop 0
s_cmp_eq_u32 vcc_hi,-1
s_cselect_b32 s67,s50,s59
s_xor_b32 s62,s63,65536
s_cmp_ge_u32 s67,s59
s_cselect_b32 s68,s50,s51
s_lshr_b32 s69,s68,5
s_cmp_lt_u32 s68,4194304
s_cselect_b32 s68,0xffffcfff,-1
s_and_b32 s68,s68,s69
s_add_u32 m0,s78,256
s_nop 0
buffer_load_dword v6,s[36:39],s68 offen offset:0 sc1 lds
s_mov_b32 m0,s62
s_nop 0
buffer_load_dwordx4 v0,s[36:39],s67 offen offset:0 sc1 lds
buffer_load_dwordx4 v0,s[36:39],s67 offen offset:1024 sc1 lds
buffer_load_dwordx4 v0,s[36:39],s67 offen offset:2048 sc1 lds
buffer_load_dwordx4 v0,s[36:39],s67 offen offset:3072 sc1 lds
s_mov_b32 s47,s48
s_mov_b32 s48,s59
s_lshr_b32 s70,s59,5
buffer_store_dword v5,v4,s[36:39],s70 offen offset:0 sc1
s_waitcnt lgkmcnt(3)
s_setpc_b64 s[92:93]
Lg1_done1_%=:
s_waitcnt vmcnt(0)
s_barrier
s_lshr_b32 s70,s47,5
buffer_store_dword v5,v4,s[36:39],s70 offen offset:0 sc1
s_lshr_b32 s70,s48,5
buffer_store_dword v5,v4,s[36:39],s70 offen offset:0 sc1
s_branch Lg1_done_%=
Lg1_done_%=:
s_waitcnt vmcnt(0) lgkmcnt(0)
s_barrier
s_branch Lg2_end_%=
Lg2_begin_%=:
Lcode_g2_b:
s_mov_b64 s[36:37],%1
s_mov_b32 s38,122727552
s_mov_b32 s39,0x00020000
s_mov_b32 s54,%2
s_mov_b32 s55,%3
s_mov_b32 s56,%4
s_mov_b32 s58,%5
s_mov_b32 s83,%6
s_mov_b32 s82,%7
s_mov_b32 s84,%8
v_mov_b32 v0,%9
v_mov_b32 v1,%10
v_mov_b32 v2,%11
v_mov_b32 v3,%12
v_mov_b32 v4,%13
v_mov_b32 v6,%14
v_mov_b32 v7,%15
v_mov_b32 v8,%16
v_mov_b32 v9,%17
v_mov_b32 v10,%18
v_mov_b32 v11,%19
v_mov_b32 v12,%20
v_mov_b32 v13,%21
v_mov_b32 v14,%22
v_mov_b32 v16,%23
v_mov_b32 v17,%24
v_mov_b32 v18,%25
v_mov_b32 v19,%26
v_mov_b32 v20,%27
s_mov_b64 s[40:41],%28
s_mov_b32 s42,2097152
s_mov_b32 s43,0x00020000
s_lshl_b32 s88,s54,11
s_add_u32 m0,s88,0
s_add_u32 s61,s82,0
s_and_b32 s61,s61,2097151
buffer_load_dwordx4 v16,s[40:43],s61 offen offset:0  lds)"
R"(
s_add_u32 m0,s88,1024
s_nop 0
buffer_load_dwordx4 v17,s[40:43],s61 offen offset:0  lds
s_add_u32 m0,s88,8064
s_nop 0
buffer_load_dwordx4 v16,s[40:43],s61 offen offset:128  lds
s_add_u32 m0,s88,9088
s_nop 0
buffer_load_dwordx4 v17,s[40:43],s61 offen offset:128  lds
s_add_u32 m0,s88,16384
s_add_u32 s61,s82,131072
s_and_b32 s61,s61,2097151
buffer_load_dwordx4 v16,s[40:43],s61 offen offset:0  lds
s_add_u32 m0,s88,17408
s_nop 0
buffer_load_dwordx4 v17,s[40:43],s61 offen offset:0  lds
s_add_u32 m0,s88,24448
s_nop 0
buffer_load_dwordx4 v16,s[40:43],s61 offen offset:128  lds
s_add_u32 m0,s88,25472
s_nop 0
buffer_load_dwordx4 v17,s[40:43],s61 offen offset:128  lds
s_add_u32 m0,s88,32768
s_add_u32 s61,s82,262144
s_and_b32 s61,s61,2097151
buffer_load_dwordx4 v16,s[40:43],s61 offen offset:0  lds
s_add_u32 m0,s88,33792
s_nop 0
buffer_load_dwordx4 v17,s[40:43],s61 offen offset:0  lds
s_add_u32 m0,s88,40832
s_nop 0
buffer_load_dwordx4 v16,s[40:43],s61 offen offset:128  lds
s_add_u32 m0,s88,41856
s_nop 0
buffer_load_dwordx4 v17,s[40:43],s61 offen offset:128  lds
s_add_u32 m0,s88,49152
s_add_u32 s61,s82,393216
s_and_b32 s61,s61,2097151
buffer_load_dwordx4 v16,s[40:43],s61 offen offset:0  lds
s_add_u32 m0,s88,50176
s_nop 0
buffer_load_dwordx4 v17,s[40:43],s61 offen offset:0  lds
s_add_u32 m0,s88,57216
s_nop 0
buffer_load_dwordx4 v16,s[40:43],s61 offen offset:128  lds
s_add_u32 m0,s88,58240
s_nop 0
buffer_load_dwordx4 v17,s[40:43],s61 offen offset:128  lds
s_add_u32 m0,s88,65536
s_add_u32 s61,s82,524288
s_and_b32 s61,s61,2097151
buffer_load_dwordx4 v16,s[40:43],s61 offen offset:0  lds
s_add_u32 m0,s88,66560
s_nop 0
buffer_load_dwordx4 v17,s[40:43],s61 offen offset:0  lds
s_add_u32 m0,s88,73600
s_nop 0
buffer_load_dwordx4 v16,s[40:43],s61 offen offset:128  lds
s_add_u32 m0,s88,74624
s_nop 0
buffer_load_dwordx4 v17,s[40:43],s61 offen offset:128  lds
s_add_u32 m0,s88,81920
s_add_u32 s61,s82,655360
s_and_b32 s61,s61,2097151
buffer_load_dwordx4 v16,s[40:43],s61 offen offset:0  lds
s_add_u32 m0,s88,82944
s_nop 0
buffer_load_dwordx4 v17,s[40:43],s61 offen offset:0  lds
s_add_u32 m0,s88,89984
s_nop 0
buffer_load_dwordx4 v16,s[40:43],s61 offen offset:128  lds
s_add_u32 m0,s88,91008
s_nop 0
buffer_load_dwordx4 v17,s[40:43],s61 offen offset:128  lds
s_add_u32 m0,s88,98304
s_add_u32 s61,s82,786432
s_and_b32 s61,s61,2097151
buffer_load_dwordx4 v16,s[40:43],s61 offen offset:0  lds
s_add_u32 m0,s88,99328
s_nop 0
buffer_load_dwordx4 v17,s[40:43],s61 offen offset:0  lds
s_add_u32 m0,s88,106368
s_nop 0
buffer_load_dwordx4 v16,s[40:43],s61 offen offset:128  lds
s_add_u32 m0,s88,107392
s_nop 0
buffer_load_dwordx4 v17,s[40:43],s61 offen offset:128  lds
v_add_u32 v24,49152,v18
v_add_u32 v25,49152,v19
v_add_u32 v26,98304,v18
v_add_u32 v27,98304,v19
s_waitcnt vmcnt(24)
s_barrier
s_add_u32 m0,s88,114688
s_add_u32 s61,s82,917504
s_and_b32 s61,s61,2097151
buffer_load_dwordx4 v16,s[40:43],s61 offen offset:0  lds
s_add_u32 m0,s88,115712
ds_read_b64_tr_b16 a[0:1],v18 offset:0
buffer_load_dwordx4 v17,s[40:43],s61 offen offset:0  lds
s_add_u32 m0,s88,122752
ds_read_b64_tr_b16 a[2:3],v18 offset:512
buffer_load_dwordx4 v16,s[40:43],s61 offen offset:128  lds
s_add_u32 m0,s88,123776
ds_read_b64_tr_b16 a[4:5],v19 offset:0
buffer_load_dwordx4 v17,s[40:43],s61 offen offset:128  lds
ds_read_b64_tr_b16 a[6:7],v19 offset:512
ds_read_b64_tr_b16 a[8:9],v18 offset:4096
ds_read_b64_tr_b16 a[10:11],v18 offset:4608
ds_read_b64_tr_b16 a[12:13],v19 offset:4096
ds_read_b64_tr_b16 a[14:15],v19 offset:4608
s_waitcnt vmcnt(24)
s_barrier
s_add_u32 m0,s88,131072
s_add_u32 s61,s82,1048576
s_and_b32 s61,s61,2097151
buffer_load_dwordx4 v16,s[40:43],s61 offen offset:0  lds
s_add_u32 m0,s88,132096
ds_read_b64_tr_b16 a[16:17],v18 offset:16384
buffer_load_dwordx4 v17,s[40:43],s61 offen offset:0  lds
s_add_u32 m0,s88,139136
ds_read_b64_tr_b16 a[18:19],v18 offset:16896
buffer_load_dwordx4 v16,s[40:43],s61 offen offset:128  lds
s_add_u32 m0,s88,140160
ds_read_b64_tr_b16 a[20:21],v19 offset:16384
buffer_load_dwordx4 v17,s[40:43],s61 offen offset:128  lds
ds_read_b64_tr_b16 a[22:23],v19 offset:16896
ds_read_b64_tr_b16 a[24:25],v18 offset:20480
ds_read_b64_tr_b16 a[26:27],v18 offset:20992
ds_read_b64_tr_b16 a[28:29],v19 offset:20480
ds_read_b64_tr_b16 a[30:31],v19 offset:20992
s_waitcnt lgkmcnt(8)
s_waitcnt vmcnt(24)
s_barrier
s_add_u32 m0,s88,0
s_add_u32 s61,s82,1179648
s_and_b32 s61,s61,2097151
buffer_load_dwordx4 v16,s[40:43],s61 offen offset:0  lds
s_add_u32 m0,s88,1024
ds_read_b64_tr_b16 a[32:33],v18 offset:32768
buffer_load_dwordx4 v17,s[40:43],s61 offen offset:0  lds
s_add_u32 m0,s88,8064
ds_read_b64_tr_b16 a[34:35],v18 offset:33280
buffer_load_dwordx4 v16,s[40:43],s61 offen offset:128  lds
s_add_u32 m0,s88,9088
ds_read_b64_tr_b16 a[36:37],v19 offset:32768
buffer_load_dwordx4 v17,s[40:43],s61 offen offset:128  lds
ds_read_b64_tr_b16 a[38:39],v19 offset:33280
ds_read_b64_tr_b16 a[40:41],v18 offset:36864
ds_read_b64_tr_b16 a[42:43],v18 offset:37376
ds_read_b64_tr_b16 a[44:45],v19 offset:36864
ds_read_b64_tr_b16 a[46:47],v19 offset:37376
s_waitcnt lgkmcnt(8)
s_waitcnt vmcnt(24)
s_barrier
s_add_u32 m0,s88,16384
s_add_u32 s61,s82,1310720
s_and_b32 s61,s61,2097151
buffer_load_dwordx4 v16,s[40:43],s61 offen offset:0  lds
s_add_u32 m0,s88,17408
ds_read_b64_tr_b16 a[48:49],v24 offset:0
buffer_load_dwordx4 v17,s[40:43],s61 offen offset:0  lds
s_add_u32 m0,s88,24448
ds_read_b64_tr_b16 a[50:51],v24 offset:512
buffer_load_dwordx4 v16,s[40:43],s61 offen offset:128  lds
s_add_u32 m0,s88,25472
ds_read_b64_tr_b16 a[52:53],v25 offset:0
buffer_load_dwordx4 v17,s[40:43],s61 offen offset:128  lds
ds_read_b64_tr_b16 a[54:55],v25 offset:512
ds_read_b64_tr_b16 a[56:57],v24 offset:4096
ds_read_b64_tr_b16 a[58:59],v24 offset:4608
ds_read_b64_tr_b16 a[60:61],v25 offset:4096
ds_read_b64_tr_b16 a[62:63],v25 offset:4608
s_waitcnt lgkmcnt(8)
s_waitcnt vmcnt(24)
s_barrier
s_add_u32 m0,s88,32768
s_add_u32 s61,s82,1441792
s_and_b32 s61,s61,2097151
buffer_load_dwordx4 v16,s[40:43],s61 offen offset:0  lds
s_add_u32 m0,s88,33792
ds_read_b64_tr_b16 a[64:65],v24 offset:16384
buffer_load_dwordx4 v17,s[40:43],s61 offen offset:0  lds
s_add_u32 m0,s88,40832
ds_read_b64_tr_b16 a[66:67],v24 offset:16896
buffer_load_dwordx4 v16,s[40:43],s61 offen offset:128  lds
s_add_u32 m0,s88,41856
ds_read_b64_tr_b16 a[68:69],v25 offset:16384
buffer_load_dwordx4 v17,s[40:43],s61 offen offset:128  lds
ds_read_b64_tr_b16 a[70:71],v25 offset:16896
ds_read_b64_tr_b16 a[72:73],v24 offset:20480
ds_read_b64_tr_b16 a[74:75],v24 offset:20992
ds_read_b64_tr_b16 a[76:77],v25 offset:20480
ds_read_b64_tr_b16 a[78:79],v25 offset:20992
s_waitcnt lgkmcnt(8)
s_waitcnt vmcnt(24)
s_barrier
s_add_u32 m0,s88,49152
s_add_u32 s61,s82,1572864
s_and_b32 s61,s61,2097151)"
R"(
buffer_load_dwordx4 v16,s[40:43],s61 offen offset:0  lds
s_add_u32 m0,s88,50176
ds_read_b64_tr_b16 a[80:81],v24 offset:32768
buffer_load_dwordx4 v17,s[40:43],s61 offen offset:0  lds
s_add_u32 m0,s88,57216
ds_read_b64_tr_b16 a[82:83],v24 offset:33280
buffer_load_dwordx4 v16,s[40:43],s61 offen offset:128  lds
s_add_u32 m0,s88,58240
ds_read_b64_tr_b16 a[84:85],v25 offset:32768
buffer_load_dwordx4 v17,s[40:43],s61 offen offset:128  lds
ds_read_b64_tr_b16 a[86:87],v25 offset:33280
ds_read_b64_tr_b16 a[88:89],v24 offset:36864
ds_read_b64_tr_b16 a[90:91],v24 offset:37376
ds_read_b64_tr_b16 a[92:93],v25 offset:36864
ds_read_b64_tr_b16 a[94:95],v25 offset:37376
s_waitcnt lgkmcnt(8)
s_waitcnt vmcnt(24)
s_barrier
s_add_u32 m0,s88,65536
s_add_u32 s61,s82,1703936
s_and_b32 s61,s61,2097151
buffer_load_dwordx4 v16,s[40:43],s61 offen offset:0  lds
s_add_u32 m0,s88,66560
ds_read_b64_tr_b16 a[96:97],v26 offset:0
buffer_load_dwordx4 v17,s[40:43],s61 offen offset:0  lds
s_add_u32 m0,s88,73600
ds_read_b64_tr_b16 a[98:99],v26 offset:512
buffer_load_dwordx4 v16,s[40:43],s61 offen offset:128  lds
s_add_u32 m0,s88,74624
ds_read_b64_tr_b16 a[100:101],v27 offset:0
buffer_load_dwordx4 v17,s[40:43],s61 offen offset:128  lds
ds_read_b64_tr_b16 a[102:103],v27 offset:512
ds_read_b64_tr_b16 a[104:105],v26 offset:4096
ds_read_b64_tr_b16 a[106:107],v26 offset:4608
ds_read_b64_tr_b16 a[108:109],v27 offset:4096
ds_read_b64_tr_b16 a[110:111],v27 offset:4608
s_waitcnt lgkmcnt(8)
s_waitcnt vmcnt(24)
s_barrier
s_add_u32 m0,s88,81920
s_add_u32 s61,s82,1835008
s_and_b32 s61,s61,2097151
buffer_load_dwordx4 v16,s[40:43],s61 offen offset:0  lds
s_add_u32 m0,s88,82944
ds_read_b64_tr_b16 a[112:113],v26 offset:16384
buffer_load_dwordx4 v17,s[40:43],s61 offen offset:0  lds
s_add_u32 m0,s88,89984
ds_read_b64_tr_b16 a[114:115],v26 offset:16896
buffer_load_dwordx4 v16,s[40:43],s61 offen offset:128  lds
s_add_u32 m0,s88,91008
ds_read_b64_tr_b16 a[116:117],v27 offset:16384
buffer_load_dwordx4 v17,s[40:43],s61 offen offset:128  lds
ds_read_b64_tr_b16 a[118:119],v27 offset:16896
ds_read_b64_tr_b16 a[120:121],v26 offset:20480
ds_read_b64_tr_b16 a[122:123],v26 offset:20992
ds_read_b64_tr_b16 a[124:125],v27 offset:20480
ds_read_b64_tr_b16 a[126:127],v27 offset:20992
s_waitcnt lgkmcnt(8)
s_waitcnt vmcnt(24)
s_barrier
s_add_u32 m0,s88,98304
s_add_u32 s61,s82,1966080
s_and_b32 s61,s61,2097151
buffer_load_dwordx4 v16,s[40:43],s61 offen offset:0  lds
s_add_u32 m0,s88,99328
ds_read_b64_tr_b16 a[128:129],v26 offset:32768
buffer_load_dwordx4 v17,s[40:43],s61 offen offset:0  lds
s_add_u32 m0,s88,106368
ds_read_b64_tr_b16 a[130:131],v26 offset:33280
buffer_load_dwordx4 v16,s[40:43],s61 offen offset:128  lds
s_add_u32 m0,s88,107392
ds_read_b64_tr_b16 a[132:133],v27 offset:32768
buffer_load_dwordx4 v17,s[40:43],s61 offen offset:128  lds
ds_read_b64_tr_b16 a[134:135],v27 offset:33280
ds_read_b64_tr_b16 a[136:137],v26 offset:36864
ds_read_b64_tr_b16 a[138:139],v26 offset:37376
ds_read_b64_tr_b16 a[140:141],v27 offset:36864
ds_read_b64_tr_b16 a[142:143],v27 offset:37376
s_waitcnt lgkmcnt(8)
s_waitcnt vmcnt(24)
s_barrier
ds_read_b64_tr_b16 a[144:145],v18 offset:0
ds_read_b64_tr_b16 a[146:147],v18 offset:512
ds_read_b64_tr_b16 a[148:149],v19 offset:0
ds_read_b64_tr_b16 a[150:151],v19 offset:512
ds_read_b64_tr_b16 a[152:153],v18 offset:4096
ds_read_b64_tr_b16 a[154:155],v18 offset:4608
ds_read_b64_tr_b16 a[156:157],v19 offset:4096
ds_read_b64_tr_b16 a[158:159],v19 offset:4608
s_waitcnt lgkmcnt(8)
s_waitcnt vmcnt(20)
s_barrier
ds_read_b64_tr_b16 a[160:161],v18 offset:16384
ds_read_b64_tr_b16 a[162:163],v18 offset:16896
ds_read_b64_tr_b16 a[164:165],v19 offset:16384
ds_read_b64_tr_b16 a[166:167],v19 offset:16896
ds_read_b64_tr_b16 a[168:169],v18 offset:20480
ds_read_b64_tr_b16 a[170:171],v18 offset:20992
ds_read_b64_tr_b16 a[172:173],v19 offset:20480
ds_read_b64_tr_b16 a[174:175],v19 offset:20992
s_waitcnt lgkmcnt(8)
s_waitcnt vmcnt(16)
s_barrier
ds_read_b64_tr_b16 a[176:177],v18 offset:32768
ds_read_b64_tr_b16 a[178:179],v18 offset:33280
ds_read_b64_tr_b16 a[180:181],v19 offset:32768
ds_read_b64_tr_b16 a[182:183],v19 offset:33280
ds_read_b64_tr_b16 a[184:185],v18 offset:36864
ds_read_b64_tr_b16 a[186:187],v18 offset:37376
ds_read_b64_tr_b16 a[188:189],v19 offset:36864
ds_read_b64_tr_b16 a[190:191],v19 offset:37376
s_getpc_b64 s[46:47]
Lg2_cp_%=:
s_add_u32 s46,s46,Lg2_init_%=-Lg2_cp_%=
s_addc_u32 s47,s47,0
s_lshl_b32 s68,s54,13
v_lshl_add_u32 v48,v2,3,s68
v_min_u32 v49,Lg2_done_%=-Lg2_init_%=,v48
global_load_dword v52,v49,s[46:47]
s_waitcnt lgkmcnt(8)
s_waitcnt vmcnt(13)
s_barrier
ds_read_b64_tr_b16 a[192:193],v24 offset:0
ds_read_b64_tr_b16 a[194:195],v24 offset:512
ds_read_b64_tr_b16 a[196:197],v25 offset:0
ds_read_b64_tr_b16 a[198:199],v25 offset:512
ds_read_b64_tr_b16 a[200:201],v24 offset:4096
ds_read_b64_tr_b16 a[202:203],v24 offset:4608
ds_read_b64_tr_b16 a[204:205],v25 offset:4096
ds_read_b64_tr_b16 a[206:207],v25 offset:4608
s_waitcnt lgkmcnt(8)
s_waitcnt vmcnt(9)
s_barrier
ds_read_b64_tr_b16 a[208:209],v24 offset:16384
ds_read_b64_tr_b16 a[210:211],v24 offset:16896
ds_read_b64_tr_b16 a[212:213],v25 offset:16384
ds_read_b64_tr_b16 a[214:215],v25 offset:16896
ds_read_b64_tr_b16 a[216:217],v24 offset:20480
ds_read_b64_tr_b16 a[218:219],v24 offset:20992
ds_read_b64_tr_b16 a[220:221],v25 offset:20480
ds_read_b64_tr_b16 a[222:223],v25 offset:20992
s_waitcnt lgkmcnt(8)
s_waitcnt vmcnt(5)
s_barrier
ds_read_b64_tr_b16 a[224:225],v24 offset:32768
ds_read_b64_tr_b16 a[226:227],v24 offset:33280
ds_read_b64_tr_b16 a[228:229],v25 offset:32768
ds_read_b64_tr_b16 a[230:231],v25 offset:33280
ds_read_b64_tr_b16 a[232:233],v24 offset:36864
ds_read_b64_tr_b16 a[234:235],v24 offset:37376
ds_read_b64_tr_b16 a[236:237],v25 offset:36864
ds_read_b64_tr_b16 a[238:239],v25 offset:37376
s_waitcnt lgkmcnt(8)
s_waitcnt vmcnt(1)
s_barrier
ds_read_b64_tr_b16 a[240:241],v26 offset:0
ds_read_b64_tr_b16 a[242:243],v26 offset:512
ds_read_b64_tr_b16 a[244:245],v27 offset:0
ds_read_b64_tr_b16 a[246:247],v27 offset:512
ds_read_b64_tr_b16 a[248:249],v26 offset:4096
ds_read_b64_tr_b16 a[250:251],v26 offset:4608
ds_read_b64_tr_b16 a[252:253],v27 offset:4096
ds_read_b64_tr_b16 a[254:255],v27 offset:4608
s_waitcnt lgkmcnt(8)
s_waitcnt lgkmcnt(0)
s_waitcnt lgkmcnt(0)
s_barrier
s_mov_b32 s60,s84
Lg2_init_%=:
v_mov_b32 v5,s60
s_mov_b32 s59,536870912
s_add_u32 s57,s56,131072
s_mov_b32 s79,0
s_mov_b32 s75,0x3a800000
s_mov_b32 s76,0xc0135761
s_mov_b32 s77,0x3d372713
s_mov_b32 s44,s59
s_mov_b32 s45,s59
s_mov_b32 s46,s59
s_mov_b32 s47,s59
s_mov_b32 s48,s83
s_add_u32 s49,s48,s55
s_cmp_eq_u32 s49,s56
s_cselect_b32 s49,s57,s49
s_cmp_eq_u32 s49,s58
s_cselect_b32 s49,s59,s49
s_add_u32 s50,s49,s55
s_cmp_eq_u32 s50,s56
s_cselect_b32 s50,s57,s50
s_cmp_eq_u32 s50,s58
s_cselect_b32 s50,s59,s50
s_add_u32 s51,s50,s55
s_cmp_eq_u32 s51,s56
s_cselect_b32 s51,s57,s51
s_cmp_eq_u32 s51,s58
s_cselect_b32 s51,s59,s51
s_add_u32 s52,s51,s55
s_cmp_eq_u32 s52,s56
s_cselect_b32 s52,s57,s52
s_cmp_eq_u32 s52,s58)"
R"(
s_cselect_b32 s52,s59,s52
s_lshl_b32 s63,s54,12
s_add_u32 s63,s63,65536
s_add_u32 s64,s63,32768
s_lshl_b32 s88,s54,10
s_add_u32 s88,s88,131072
s_lshl_b32 s78,s54,11
s_add_u32 s78,s78,132608
s_getpc_b64 s[90:91]
Lg2_pc_%=:
s_mov_b32 s68,Lg2_d1w0_%=-Lg2_pc_%=
s_cmp_eq_u32 s54,1
s_cselect_b32 s68,Lg2_d1w1_%=-Lg2_pc_%=,s68
s_cmp_eq_u32 s54,2
s_cselect_b32 s68,Lg2_d1w2_%=-Lg2_pc_%=,s68
s_cmp_eq_u32 s54,3
s_cselect_b32 s68,Lg2_d1w3_%=-Lg2_pc_%=,s68
s_add_u32 s92,s90,s68
s_addc_u32 s93,s91,0
s_mov_b32 s68,Lg2_d0w0_%=-Lg2_pc_%=
s_cmp_eq_u32 s54,1
s_cselect_b32 s68,Lg2_d0w1_%=-Lg2_pc_%=,s68
s_cmp_eq_u32 s54,2
s_cselect_b32 s68,Lg2_d0w2_%=-Lg2_pc_%=,s68
s_cmp_eq_u32 s54,3
s_cselect_b32 s68,Lg2_d0w3_%=-Lg2_pc_%=,s68
s_add_u32 s90,s90,s68
s_addc_u32 s91,s91,0
s_mov_b32 s66,s59
s_mov_b32 s65,0
s_mov_b32 s62,s63
s_branch Lg2_slow0_%=
Lg2_d0w0_%=:
v_mfma_f32_16x16x32_bf16 v[16:19],a[0:3],v[32:35],0
s_add_u32 m0,s62,16384
ds_read_b128 v[60:63],v2 offset:7168
v_lshlrev_b32 v64,16,v84
v_mfma_f32_16x16x32_bf16 v[20:23],a[4:7],v[32:35],0
buffer_load_dwordx4 v1,s[36:39],s66 offen offset:0 sc1 lds
v_mfma_f32_16x16x32_bf16 v[16:19],a[8:11],v[36:39],v[16:19]
s_lshr_b32 s73,s47,0
ds_read_b128 v[32:35],v2 offset:8192
v_and_b32 v65,0xffff0000,v84
v_mfma_f32_16x16x32_bf16 v[20:23],a[12:15],v[36:39],v[20:23]
s_xor_b32 s87,s64,65536
v_lshlrev_b32 v66,16,v85
v_and_b32 v67,0xffff0000,v85
v_mfma_f32_16x16x32_bf16 v[16:19],a[16:19],v[40:43],v[16:19]
s_cmp_ge_u32 s66,s59
ds_read_b128 v[36:39],v2 offset:9216
v_lshlrev_b32 v68,16,v86
v_mfma_f32_16x16x32_bf16 v[20:23],a[20:23],v[40:43],v[20:23]
buffer_load_dwordx4 v1,s[36:39],s66 offen offset:1024 sc1 lds
v_mfma_f32_16x16x32_bf16 v[16:19],a[24:27],v[44:47],v[16:19]
s_cselect_b32 s86,s49,s50
ds_read_b128 v[40:43],v2 offset:10240
v_and_b32 v69,0xffff0000,v86
v_mfma_f32_16x16x32_bf16 v[20:23],a[28:31],v[44:47],v[20:23]
s_waitcnt lgkmcnt(3)
v_lshlrev_b32 v70,16,v87
v_and_b32 v71,0xffff0000,v87
v_mfma_f32_16x16x32_bf16 v[16:19],a[32:35],v[48:51],v[16:19]
s_cselect_b32 s87,s87,s63
ds_read_b128 v[44:47],v2 offset:11264
v_fma_f32 v64,v64,v91,v90
v_mfma_f32_16x16x32_bf16 v[20:23],a[36:39],v[48:51],v[20:23]
buffer_load_dwordx4 v1,s[36:39],s66 offen offset:2048 sc1 lds
v_mfma_f32_16x16x32_bf16 v[16:19],a[40:43],v[52:55],v[16:19]
s_lshr_b32 s72,s66,6
ds_read_b128 v[48:51],v2 offset:12288
v_fma_f32 v65,v65,v91,v90
v_mfma_f32_16x16x32_bf16 v[20:23],a[44:47],v[52:55],v[20:23]
s_lshr_b32 s71,s62,2
v_fma_f32 v66,v66,v91,v90
v_fma_f32 v67,v67,v91,v90
v_mfma_f32_16x16x32_bf16 v[16:19],a[48:51],v[56:59],v[16:19]
s_mov_b32 s62,s87
ds_read_b128 v[52:55],v2 offset:13312
v_fma_f32 v68,v68,v91,v90
v_mfma_f32_16x16x32_bf16 v[20:23],a[52:55],v[56:59],v[20:23]
buffer_load_dwordx4 v1,s[36:39],s66 offen offset:3072 sc1 lds
v_mfma_f32_16x16x32_bf16 v[16:19],a[56:59],v[60:63],v[16:19]
s_add_u32 m0,s71,s88
ds_read_b128 v[56:59],v2 offset:14336
v_fma_f32 v69,v69,v91,v90
v_mfma_f32_16x16x32_bf16 v[20:23],a[60:63],v[60:63],v[20:23]
s_waitcnt lgkmcnt(3)
v_fma_f32 v70,v70,v91,v90
v_fma_f32 v71,v71,v91,v90
v_mfma_f32_16x16x32_bf16 v[16:19],a[64:67],v[32:35],v[16:19]
s_waitcnt vmcnt(10)
ds_read_b128 v[60:63],v2 offset:15360
v_add_f32 v24,v24,v64
v_mfma_f32_16x16x32_bf16 v[20:23],a[68:71],v[32:35],v[20:23]
s_barrier
v_mfma_f32_16x16x32_bf16 v[16:19],a[72:75],v[36:39],v[16:19]
s_lshr_b32 s68,s50,5
ds_read_b128 v[32:35],v2 offset:16384
v_add_f32 v25,v25,v65
v_mfma_f32_16x16x32_bf16 v[20:23],a[76:79],v[36:39],v[20:23]
buffer_load_dwordx4 v10,s[36:39],s66 offen offset:0 sc1 lds
v_mfma_f32_16x16x32_bf16 v[16:19],a[80:83],v[40:43],v[16:19]
buffer_load_dword v11,s[36:39],s72 offen offset:1024 sc1 lds
v_mfma_f32_16x16x32_bf16 v[20:23],a[84:87],v[40:43],v[20:23]
s_lshr_b32 s70,s51,5
ds_read_b128 v[36:39],v2 offset:17408
v_add_f32 v26,v26,v66
v_mfma_f32_16x16x32_bf16 v[16:19],a[88:91],v[44:47],v[16:19]
ds_read_b128 v[40:43],v2 offset:18432
v_add_f32 v27,v27,v67
v_mfma_f32_16x16x32_bf16 v[20:23],a[92:95],v[44:47],v[20:23]
s_waitcnt lgkmcnt(3)
v_add_f32 v28,v28,v68
v_add_f32 v29,v29,v69
v_add_f32 v30,v30,v70
v_add_f32 v31,v31,v71
v_cvt_pk_bf16_f32 v72,v24,v25
v_cvt_pk_bf16_f32 v73,v26,v27
v_cvt_pk_bf16_f32 v74,v28,v29
v_cvt_pk_bf16_f32 v75,v30,v31
v_mfma_f32_16x16x32_bf16 v[16:19],a[96:99],v[48:51],v[16:19]
buffer_store_dwordx4 v[72:75],v3,s[36:39],s73 offen offset:0 sc1
ds_read_b128 v[44:47],v2 offset:19456
v_add_f32 v64,v24,v25
v_mul_f32 v65,v24,v24
v_mfma_f32_16x16x32_bf16 v[20:23],a[100:103],v[48:51],v[20:23]
v_fma_f32 v65,v25,v25,v65
v_add_f32 v64,v64,v26
v_mfma_f32_16x16x32_bf16 v[16:19],a[104:107],v[52:55],v[16:19]
ds_read_b128 v[48:51],v2 offset:20480
v_fma_f32 v65,v26,v26,v65
v_mfma_f32_16x16x32_bf16 v[20:23],a[108:111],v[52:55],v[20:23]
v_add_f32 v64,v64,v27
v_fma_f32 v65,v27,v27,v65
v_mfma_f32_16x16x32_bf16 v[16:19],a[112:115],v[56:59],v[16:19]
ds_read_b128 v[52:55],v2 offset:21504
v_add_f32 v64,v64,v28
v_mfma_f32_16x16x32_bf16 v[20:23],a[116:119],v[56:59],v[20:23]
v_fma_f32 v65,v28,v28,v65
v_add_f32 v64,v64,v29
v_fma_f32 v65,v29,v29,v65
v_add_f32 v64,v64,v30
v_fma_f32 v65,v30,v30,v65
v_add_f32 v64,v64,v31
v_fma_f32 v65,v31,v31,v65
v_mfma_f32_16x16x32_bf16 v[16:19],a[120:123],v[60:63],v[16:19]
s_nop 0
v_permlane16_swap_b32 v64,v65
v_add_f32 v64,v64,v65
v_mov_b32 v65,v64
v_mfma_f32_16x16x32_bf16 v[20:23],a[124:127],v[60:63],v[20:23]
s_waitcnt lgkmcnt(2)
ds_read_b128 v[56:59],v2 offset:22528
v_permlane32_swap_b32 v64,v65
v_add_f32 v64,v64,v65
v_mfma_f32_16x16x32_bf16 v[16:19],a[128:131],v[32:35],v[16:19]
ds_write_b32 v13,v64 offset:0
v_mfma_f32_16x16x32_bf16 v[20:23],a[132:135],v[32:35],v[20:23]
ds_read_b128 v[60:63],v2 offset:23552
v_mfma_f32_16x16x32_bf16 v[16:19],a[136:139],v[36:39],v[16:19]
ds_read_b128 v[32:35],v2 offset:24576
v_mfma_f32_16x16x32_bf16 v[20:23],a[140:143],v[36:39],v[20:23]
v_mfma_f32_16x16x32_bf16 v[16:19],a[144:147],v[40:43],v[16:19]
s_waitcnt vmcnt(11) lgkmcnt(2)
ds_read_b128 v[36:39],v2 offset:25600
v_mfma_f32_16x16x32_bf16 v[20:23],a[148:151],v[40:43],v[20:23]
s_barrier
v_mfma_f32_16x16x32_bf16 v[16:19],a[152:155],v[44:47],v[16:19]
ds_read_b128 v[40:43],v2 offset:26624
ds_read_b32 v15,v7 offset:0
v_mfma_f32_16x16x32_bf16 v[20:23],a[156:159],v[44:47],v[20:23]
s_waitcnt lgkmcnt(4)
v_mfma_f32_16x16x32_bf16 v[16:19],a[160:163],v[48:51],v[16:19]
ds_read_b128 v[44:47],v2 offset:27648
v_mfma_f32_16x16x32_bf16 v[20:23],a[164:167],v[48:51],v[20:23]
v_mfma_f32_16x16x32_bf16 v[16:19],a[168:171],v[52:55],v[16:19]
s_waitcnt lgkmcnt(1)
ds_read_b128 v[48:51],v2 offset:28672
v_mfma_f32_16x16x32_bf16 v[20:23],a[172:175],v[52:55],v[20:23]
v_cmp_eq_u32 vcc,s60,v15
v_mfma_f32_16x16x32_bf16 v[16:19],a[176:179],v[56:59],v[16:19]
s_cmp_eq_u32 vcc_lo,-1
ds_read_b128 v[52:55],v2 offset:29696
s_cselect_b32 s67,s86,s59
v_mfma_f32_16x16x32_bf16 v[20:23],a[180:183],v[56:59],v[20:23]
s_cmp_ge_u32 s67,s59
s_cselect_b32 s68,s68,s70
s_add_u32 m0,s78,256
v_mfma_f32_16x16x32_bf16 v[16:19],a[184:187],v[60:63],v[16:19]
buffer_load_dword v6,s[36:39],s68 offen offset:0 sc1 lds
v_mfma_f32_16x16x32_bf16 v[20:23],a[188:191],v[60:63],v[20:23]
s_waitcnt lgkmcnt(2)
ds_read_b128 v[56:59],v2 offset:30720
s_xor_b32 s63,s63,65536
v_mfma_f32_16x16x32_bf16 v[16:19],a[192:195],v[32:35],v[16:19]
s_waitcnt vmcnt(8)
ds_read_b128 v[60:63],v2 offset:31744
s_mov_b32 m0,s62
v_mfma_f32_16x16x32_bf16 v[20:23],a[196:199],v[32:35],v[20:23]
s_barrier)"
R"(
v_mfma_f32_16x16x32_bf16 v[16:19],a[200:203],v[36:39],v[16:19]
buffer_load_dwordx4 v0,s[36:39],s67 offen offset:0 sc1 lds
v_mfma_f32_16x16x32_bf16 v[20:23],a[204:207],v[36:39],v[20:23]
s_add_u32 s53,s52,s55
ds_read_b128 v[32:35],v2 offset:32768
ds_read_b128 v[80:83],v8 offset:0
v_mfma_f32_16x16x32_bf16 v[16:19],a[208:211],v[40:43],v[16:19]
s_cmp_eq_u32 s53,s56
ds_read_b128 v[36:39],v2 offset:33792
ds_read_b64 v[88:89],v9 offset:1024
v_mfma_f32_16x16x32_bf16 v[20:23],a[212:215],v[40:43],v[20:23]
s_cselect_b32 s53,s57,s53
s_cmp_eq_u32 s53,s58
s_cselect_b32 s53,s59,s53
v_mfma_f32_16x16x32_bf16 v[16:19],a[216:219],v[44:47],v[16:19]
buffer_load_dwordx4 v0,s[36:39],s67 offen offset:1024 sc1 lds
v_mfma_f32_16x16x32_bf16 v[20:23],a[220:223],v[44:47],v[20:23]
s_waitcnt lgkmcnt(4)
ds_read_b128 v[40:43],v2 offset:34816
s_cmp_ge_u32 s66,s59
v_mfma_f32_16x16x32_bf16 v[16:19],a[224:227],v[48:51],v[16:19]
ds_read_b128 v[44:47],v2 offset:35840
v_mfma_f32_16x16x32_bf16 v[20:23],a[228:231],v[48:51],v[20:23]
v_mfma_f32_16x16x32_bf16 v[16:19],a[232:235],v[52:55],v[16:19]
s_waitcnt lgkmcnt(2)
buffer_load_dwordx4 v0,s[36:39],s67 offen offset:2048 sc1 lds
v_mfma_f32_16x16x32_bf16 v[20:23],a[236:239],v[52:55],v[20:23]
ds_read_b128 v[48:51],v2 offset:36864
v_mfma_f32_16x16x32_bf16 v[16:19],a[240:243],v[56:59],v[16:19]
ds_read_b128 v[52:55],v2 offset:37888
v_mfma_f32_16x16x32_bf16 v[20:23],a[244:247],v[56:59],v[20:23]
v_mfma_f32_16x16x32_bf16 v[16:19],a[248:251],v[60:63],v[16:19]
s_waitcnt lgkmcnt(2)
ds_read_b128 v[56:59],v2 offset:38912
buffer_load_dwordx4 v0,s[36:39],s67 offen offset:3072 sc1 lds
v_mfma_f32_16x16x32_bf16 v[20:23],a[252:255],v[60:63],v[20:23]
s_cbranch_scc1 Lg2_slow1_%=
Lg2_d1w0_%=:
v_mfma_f32_16x16x32_bf16 v[24:27],a[0:3],v[32:35],0
s_add_u32 m0,s62,16384
ds_read_b128 v[60:63],v2 offset:39936
v_lshlrev_b32 v64,16,v80
v_mfma_f32_16x16x32_bf16 v[28:31],a[4:7],v[32:35],0
buffer_load_dwordx4 v1,s[36:39],s67 offen offset:0 sc1 lds
v_mfma_f32_16x16x32_bf16 v[24:27],a[8:11],v[36:39],v[24:27]
s_lshr_b32 s73,s48,0
ds_read_b128 v[32:35],v2 offset:40960
v_and_b32 v65,0xffff0000,v80
v_mfma_f32_16x16x32_bf16 v[28:31],a[12:15],v[36:39],v[28:31]
s_xor_b32 s87,s63,65536
v_lshlrev_b32 v66,16,v81
v_and_b32 v67,0xffff0000,v81
v_mfma_f32_16x16x32_bf16 v[24:27],a[16:19],v[40:43],v[24:27]
s_cmp_ge_u32 s67,s59
ds_read_b128 v[36:39],v2 offset:41984
v_lshlrev_b32 v68,16,v82
v_mfma_f32_16x16x32_bf16 v[28:31],a[20:23],v[40:43],v[28:31]
buffer_load_dwordx4 v1,s[36:39],s67 offen offset:1024 sc1 lds
v_mfma_f32_16x16x32_bf16 v[24:27],a[24:27],v[44:47],v[24:27]
s_cselect_b32 s86,s50,s51
ds_read_b128 v[40:43],v2 offset:43008
v_and_b32 v69,0xffff0000,v82
v_mfma_f32_16x16x32_bf16 v[28:31],a[28:31],v[44:47],v[28:31]
s_waitcnt lgkmcnt(3)
v_lshlrev_b32 v70,16,v83
v_and_b32 v71,0xffff0000,v83
v_mfma_f32_16x16x32_bf16 v[24:27],a[32:35],v[48:51],v[24:27]
s_cselect_b32 s87,s87,s64
ds_read_b128 v[44:47],v2 offset:44032
v_fma_f32 v64,v64,v89,v88
v_mfma_f32_16x16x32_bf16 v[28:31],a[36:39],v[48:51],v[28:31]
buffer_load_dwordx4 v1,s[36:39],s67 offen offset:2048 sc1 lds
v_mfma_f32_16x16x32_bf16 v[24:27],a[40:43],v[52:55],v[24:27]
s_lshr_b32 s72,s67,6
ds_read_b128 v[48:51],v2 offset:45056
v_fma_f32 v65,v65,v89,v88
v_mfma_f32_16x16x32_bf16 v[28:31],a[44:47],v[52:55],v[28:31]
s_lshr_b32 s71,s62,2
v_fma_f32 v66,v66,v89,v88
v_fma_f32 v67,v67,v89,v88
v_mfma_f32_16x16x32_bf16 v[24:27],a[48:51],v[56:59],v[24:27]
s_mov_b32 s62,s87
ds_read_b128 v[52:55],v2 offset:46080
v_fma_f32 v68,v68,v89,v88
v_mfma_f32_16x16x32_bf16 v[28:31],a[52:55],v[56:59],v[28:31]
buffer_load_dwordx4 v1,s[36:39],s67 offen offset:3072 sc1 lds
v_mfma_f32_16x16x32_bf16 v[24:27],a[56:59],v[60:63],v[24:27]
s_add_u32 m0,s71,s88
ds_read_b128 v[56:59],v2 offset:47104
v_fma_f32 v69,v69,v89,v88
v_mfma_f32_16x16x32_bf16 v[28:31],a[60:63],v[60:63],v[28:31]
s_waitcnt lgkmcnt(3)
v_fma_f32 v70,v70,v89,v88
v_fma_f32 v71,v71,v89,v88
v_mfma_f32_16x16x32_bf16 v[24:27],a[64:67],v[32:35],v[24:27]
s_waitcnt vmcnt(10)
ds_read_b128 v[60:63],v2 offset:48128
v_add_f32 v16,v16,v64
v_mfma_f32_16x16x32_bf16 v[28:31],a[68:71],v[32:35],v[28:31]
s_barrier
v_mfma_f32_16x16x32_bf16 v[24:27],a[72:75],v[36:39],v[24:27]
s_lshr_b32 s68,s51,5
ds_read_b128 v[32:35],v2 offset:49152
v_add_f32 v17,v17,v65
v_mfma_f32_16x16x32_bf16 v[28:31],a[76:79],v[36:39],v[28:31]
buffer_load_dwordx4 v10,s[36:39],s67 offen offset:0 sc1 lds
v_mfma_f32_16x16x32_bf16 v[24:27],a[80:83],v[40:43],v[24:27]
buffer_load_dword v11,s[36:39],s72 offen offset:1024 sc1 lds
v_mfma_f32_16x16x32_bf16 v[28:31],a[84:87],v[40:43],v[28:31]
s_lshr_b32 s70,s52,5
ds_read_b128 v[36:39],v2 offset:50176
v_add_f32 v18,v18,v66
v_mfma_f32_16x16x32_bf16 v[24:27],a[88:91],v[44:47],v[24:27]
s_mov_b64 s[44:45],s[46:47]
ds_read_b128 v[40:43],v2 offset:51200
v_add_f32 v19,v19,v67
v_mfma_f32_16x16x32_bf16 v[28:31],a[92:95],v[44:47],v[28:31]
s_waitcnt lgkmcnt(3)
v_add_f32 v20,v20,v68
v_add_f32 v21,v21,v69
v_add_f32 v22,v22,v70
v_add_f32 v23,v23,v71
v_cvt_pk_bf16_f32 v72,v16,v17
v_cvt_pk_bf16_f32 v73,v18,v19
v_cvt_pk_bf16_f32 v74,v20,v21
v_cvt_pk_bf16_f32 v75,v22,v23
v_mfma_f32_16x16x32_bf16 v[24:27],a[96:99],v[48:51],v[24:27]
buffer_store_dwordx4 v[72:75],v3,s[36:39],s73 offen offset:0 sc1
ds_read_b128 v[44:47],v2 offset:52224
v_add_f32 v64,v16,v17
v_mul_f32 v65,v16,v16
v_mfma_f32_16x16x32_bf16 v[28:31],a[100:103],v[48:51],v[28:31]
s_mov_b64 s[46:47],s[48:49]
v_fma_f32 v65,v17,v17,v65
v_add_f32 v64,v64,v18
v_mfma_f32_16x16x32_bf16 v[24:27],a[104:107],v[52:55],v[24:27]
s_mov_b64 s[48:49],s[50:51]
ds_read_b128 v[48:51],v2 offset:53248
v_fma_f32 v65,v18,v18,v65
v_mfma_f32_16x16x32_bf16 v[28:31],a[108:111],v[52:55],v[28:31]
s_mov_b64 s[50:51],s[52:53]
v_add_f32 v64,v64,v19
v_fma_f32 v65,v19,v19,v65
v_mfma_f32_16x16x32_bf16 v[24:27],a[112:115],v[56:59],v[24:27]
ds_read_b128 v[52:55],v2 offset:54272
v_add_f32 v64,v64,v20
v_mfma_f32_16x16x32_bf16 v[28:31],a[116:119],v[56:59],v[28:31]
v_fma_f32 v65,v20,v20,v65
v_add_f32 v64,v64,v21
v_fma_f32 v65,v21,v21,v65
v_add_f32 v64,v64,v22
v_fma_f32 v65,v22,v22,v65
v_add_f32 v64,v64,v23
v_fma_f32 v65,v23,v23,v65
v_mfma_f32_16x16x32_bf16 v[24:27],a[120:123],v[60:63],v[24:27]
s_nop 0
v_permlane16_swap_b32 v64,v65
v_add_f32 v64,v64,v65
v_mov_b32 v65,v64
v_mfma_f32_16x16x32_bf16 v[28:31],a[124:127],v[60:63],v[28:31]
s_waitcnt lgkmcnt(2)
ds_read_b128 v[56:59],v2 offset:55296
v_permlane32_swap_b32 v64,v65
v_add_f32 v64,v64,v65
v_mfma_f32_16x16x32_bf16 v[24:27],a[128:131],v[32:35],v[24:27]
ds_write_b32 v13,v64 offset:2048
v_mfma_f32_16x16x32_bf16 v[28:31],a[132:135],v[32:35],v[28:31]
ds_read_b128 v[60:63],v2 offset:56320
v_mfma_f32_16x16x32_bf16 v[24:27],a[136:139],v[36:39],v[24:27]
ds_read_b128 v[32:35],v2 offset:57344
v_mfma_f32_16x16x32_bf16 v[28:31],a[140:143],v[36:39],v[28:31]
v_mfma_f32_16x16x32_bf16 v[24:27],a[144:147],v[40:43],v[24:27]
s_waitcnt vmcnt(11) lgkmcnt(2)
ds_read_b128 v[36:39],v2 offset:58368
v_mfma_f32_16x16x32_bf16 v[28:31],a[148:151],v[40:43],v[28:31]
s_barrier
v_mfma_f32_16x16x32_bf16 v[24:27],a[152:155],v[44:47],v[24:27]
ds_read_b128 v[40:43],v2 offset:59392
ds_read_b32 v15,v7 offset:256
v_mfma_f32_16x16x32_bf16 v[28:31],a[156:159],v[44:47],v[28:31]
s_waitcnt lgkmcnt(4)
v_mfma_f32_16x16x32_bf16 v[24:27],a[160:163],v[48:51],v[24:27]
ds_read_b128 v[44:47],v2 offset:60416
v_mfma_f32_16x16x32_bf16 v[28:31],a[164:167],v[48:51],v[28:31]
v_mfma_f32_16x16x32_bf16 v[24:27],a[168:171],v[52:55],v[24:27]
s_waitcnt lgkmcnt(1)
ds_read_b128 v[48:51],v2 offset:61440
v_mfma_f32_16x16x32_bf16 v[28:31],a[172:175],v[52:55],v[28:31]
v_cmp_eq_u32 vcc,s60,v15
v_mfma_f32_16x16x32_bf16 v[24:27],a[176:179],v[56:59],v[24:27]
s_cmp_eq_u32 vcc_lo,-1
ds_read_b128 v[52:55],v2 offset:62464
s_cselect_b32 s66,s86,s59
v_mfma_f32_16x16x32_bf16 v[28:31],a[180:183],v[56:59],v[28:31]
s_cmp_ge_u32 s66,s59
s_cselect_b32 s68,s68,s70
s_add_u32 m0,s78,0
v_mfma_f32_16x16x32_bf16 v[24:27],a[184:187],v[60:63],v[24:27]
buffer_load_dword v6,s[36:39],s68 offen offset:0 sc1 lds
v_mfma_f32_16x16x32_bf16 v[28:31],a[188:191],v[60:63],v[28:31])"
R"(
s_waitcnt lgkmcnt(2)
ds_read_b128 v[56:59],v2 offset:63488
s_xor_b32 s64,s64,65536
v_mfma_f32_16x16x32_bf16 v[24:27],a[192:195],v[32:35],v[24:27]
s_waitcnt vmcnt(8)
ds_read_b128 v[60:63],v2 offset:64512
v_xor_b32 v2,65536,v2
v_mfma_f32_16x16x32_bf16 v[28:31],a[196:199],v[32:35],v[28:31]
s_barrier
s_mov_b32 m0,s62
v_mfma_f32_16x16x32_bf16 v[24:27],a[200:203],v[36:39],v[24:27]
buffer_load_dwordx4 v0,s[36:39],s66 offen offset:0 sc1 lds
v_mfma_f32_16x16x32_bf16 v[28:31],a[204:207],v[36:39],v[28:31]
s_add_u32 s74,s53,s55
ds_read_b128 v[32:35],v2 offset:0
ds_read_b128 v[84:87],v8 offset:8192
v_mfma_f32_16x16x32_bf16 v[24:27],a[208:211],v[40:43],v[24:27]
s_cmp_eq_u32 s74,s56
ds_read_b128 v[36:39],v2 offset:1024
ds_read_b64 v[90:91],v9 offset:9216
v_mfma_f32_16x16x32_bf16 v[28:31],a[212:215],v[40:43],v[28:31]
s_cselect_b32 s74,s57,s74
v_xor_b32 v8,16384,v8
v_xor_b32 v9,16384,v9
v_mfma_f32_16x16x32_bf16 v[24:27],a[216:219],v[44:47],v[24:27]
buffer_load_dwordx4 v0,s[36:39],s66 offen offset:1024 sc1 lds
v_mfma_f32_16x16x32_bf16 v[28:31],a[220:223],v[44:47],v[28:31]
s_waitcnt lgkmcnt(4)
ds_read_b128 v[40:43],v2 offset:2048
s_cmp_eq_u32 s74,s58
v_mfma_f32_16x16x32_bf16 v[24:27],a[224:227],v[48:51],v[24:27]
s_cselect_b32 s74,s59,s74
ds_read_b128 v[44:47],v2 offset:3072
s_mov_b32 s52,s74
v_mfma_f32_16x16x32_bf16 v[28:31],a[228:231],v[48:51],v[28:31]
s_cmp_lt_u32 s67,s59
v_mfma_f32_16x16x32_bf16 v[24:27],a[232:235],v[52:55],v[24:27]
s_waitcnt lgkmcnt(2)
buffer_load_dwordx4 v0,s[36:39],s66 offen offset:2048 sc1 lds
v_mfma_f32_16x16x32_bf16 v[28:31],a[236:239],v[52:55],v[28:31]
ds_read_b128 v[48:51],v2 offset:4096
v_mfma_f32_16x16x32_bf16 v[24:27],a[240:243],v[56:59],v[24:27]
ds_read_b128 v[52:55],v2 offset:5120
v_mfma_f32_16x16x32_bf16 v[28:31],a[244:247],v[56:59],v[28:31]
v_mfma_f32_16x16x32_bf16 v[24:27],a[248:251],v[60:63],v[24:27]
s_waitcnt lgkmcnt(2)
ds_read_b128 v[56:59],v2 offset:6144
buffer_load_dwordx4 v0,s[36:39],s66 offen offset:3072 sc1 lds
v_mfma_f32_16x16x32_bf16 v[28:31],a[252:255],v[60:63],v[28:31]
s_cbranch_scc1 Lg2_d0w0_%=
s_branch Lg2_slow0_%=
Lg2_d0w1_%=:
v_mfma_f32_16x16x32_bf16 v[16:19],a[0:3],v[32:35],0
s_add_u32 m0,s62,16384
ds_read_b128 v[60:63],v2 offset:7168
v_lshlrev_b32 v64,16,v84
v_mfma_f32_16x16x32_bf16 v[20:23],a[4:7],v[32:35],0
s_lshr_b32 s73,s47,0
v_and_b32 v65,0xffff0000,v84
v_lshlrev_b32 v66,16,v85
v_mfma_f32_16x16x32_bf16 v[16:19],a[8:11],v[36:39],v[16:19]
buffer_load_dwordx4 v1,s[36:39],s66 offen offset:0 sc1 lds
v_mfma_f32_16x16x32_bf16 v[20:23],a[12:15],v[36:39],v[20:23]
s_xor_b32 s87,s64,65536
ds_read_b128 v[32:35],v2 offset:8192
v_and_b32 v67,0xffff0000,v85
v_mfma_f32_16x16x32_bf16 v[16:19],a[16:19],v[40:43],v[16:19]
s_cmp_ge_u32 s66,s59
ds_read_b128 v[36:39],v2 offset:9216
v_lshlrev_b32 v68,16,v86
v_mfma_f32_16x16x32_bf16 v[20:23],a[20:23],v[40:43],v[20:23]
s_cselect_b32 s86,s49,s50
v_and_b32 v69,0xffff0000,v86
v_lshlrev_b32 v70,16,v87
v_mfma_f32_16x16x32_bf16 v[16:19],a[24:27],v[44:47],v[16:19]
buffer_load_dwordx4 v1,s[36:39],s66 offen offset:1024 sc1 lds
v_mfma_f32_16x16x32_bf16 v[20:23],a[28:31],v[44:47],v[20:23]
s_waitcnt lgkmcnt(2)
ds_read_b128 v[40:43],v2 offset:10240
v_and_b32 v71,0xffff0000,v87
v_mfma_f32_16x16x32_bf16 v[16:19],a[32:35],v[48:51],v[16:19]
s_cselect_b32 s87,s87,s63
ds_read_b128 v[44:47],v2 offset:11264
v_fma_f32 v64,v64,v91,v90
v_mfma_f32_16x16x32_bf16 v[20:23],a[36:39],v[48:51],v[20:23]
s_lshr_b32 s72,s66,6
v_fma_f32 v65,v65,v91,v90
v_fma_f32 v66,v66,v91,v90
v_mfma_f32_16x16x32_bf16 v[16:19],a[40:43],v[52:55],v[16:19]
buffer_load_dwordx4 v1,s[36:39],s66 offen offset:2048 sc1 lds
v_mfma_f32_16x16x32_bf16 v[20:23],a[44:47],v[52:55],v[20:23]
s_lshr_b32 s71,s62,2
ds_read_b128 v[48:51],v2 offset:12288
v_fma_f32 v67,v67,v91,v90
v_mfma_f32_16x16x32_bf16 v[16:19],a[48:51],v[56:59],v[16:19]
s_mov_b32 s62,s87
ds_read_b128 v[52:55],v2 offset:13312
v_fma_f32 v68,v68,v91,v90
v_mfma_f32_16x16x32_bf16 v[20:23],a[52:55],v[56:59],v[20:23]
v_fma_f32 v69,v69,v91,v90
v_fma_f32 v70,v70,v91,v90
v_mfma_f32_16x16x32_bf16 v[16:19],a[56:59],v[60:63],v[16:19]
buffer_load_dwordx4 v1,s[36:39],s66 offen offset:3072 sc1 lds
v_mfma_f32_16x16x32_bf16 v[20:23],a[60:63],v[60:63],v[20:23]
s_waitcnt lgkmcnt(2)
ds_read_b128 v[56:59],v2 offset:14336
v_fma_f32 v71,v71,v91,v90
v_mfma_f32_16x16x32_bf16 v[16:19],a[64:67],v[32:35],v[16:19]
s_waitcnt vmcnt(9)
ds_read_b128 v[60:63],v2 offset:15360
v_add_f32 v24,v24,v64
v_mfma_f32_16x16x32_bf16 v[20:23],a[68:71],v[32:35],v[20:23]
s_barrier
v_mfma_f32_16x16x32_bf16 v[16:19],a[72:75],v[36:39],v[16:19]
s_add_u32 m0,s71,s88
ds_read_b128 v[32:35],v2 offset:16384
v_add_f32 v25,v25,v65
v_mfma_f32_16x16x32_bf16 v[20:23],a[76:79],v[36:39],v[20:23]
v_add_f32 v26,v26,v66
v_add_f32 v27,v27,v67
v_mfma_f32_16x16x32_bf16 v[16:19],a[80:83],v[40:43],v[16:19]
buffer_load_dwordx4 v10,s[36:39],s66 offen offset:0 sc1 lds
v_mfma_f32_16x16x32_bf16 v[20:23],a[84:87],v[40:43],v[20:23]
ds_read_b128 v[36:39],v2 offset:17408
v_add_f32 v28,v28,v68
v_mfma_f32_16x16x32_bf16 v[16:19],a[88:91],v[44:47],v[16:19]
buffer_load_dword v11,s[36:39],s72 offen offset:1024 sc1 lds
v_mfma_f32_16x16x32_bf16 v[20:23],a[92:95],v[44:47],v[20:23]
s_waitcnt lgkmcnt(2)
ds_read_b128 v[40:43],v2 offset:18432
v_add_f32 v29,v29,v69
v_mfma_f32_16x16x32_bf16 v[16:19],a[96:99],v[48:51],v[16:19]
s_mov_b32 m0,s62
v_add_f32 v30,v30,v70
v_add_f32 v31,v31,v71
v_cvt_pk_bf16_f32 v72,v24,v25
v_cvt_pk_bf16_f32 v73,v26,v27
v_cvt_pk_bf16_f32 v74,v28,v29
v_cvt_pk_bf16_f32 v75,v30,v31
v_mfma_f32_16x16x32_bf16 v[20:23],a[100:103],v[48:51],v[20:23]
ds_read_b128 v[44:47],v2 offset:19456
buffer_store_dwordx4 v[72:75],v3,s[36:39],s73 offen offset:0 sc1
v_add_f32 v64,v24,v25
v_mul_f32 v65,v24,v24
v_fma_f32 v65,v25,v25,v65
v_mfma_f32_16x16x32_bf16 v[16:19],a[104:107],v[52:55],v[16:19]
ds_read_b128 v[48:51],v2 offset:20480
v_add_f32 v64,v64,v26
v_mfma_f32_16x16x32_bf16 v[20:23],a[108:111],v[52:55],v[20:23]
v_fma_f32 v65,v26,v26,v65
v_add_f32 v64,v64,v27
v_mfma_f32_16x16x32_bf16 v[16:19],a[112:115],v[56:59],v[16:19]
ds_read_b128 v[52:55],v2 offset:21504
v_fma_f32 v65,v27,v27,v65
v_mfma_f32_16x16x32_bf16 v[20:23],a[116:119],v[56:59],v[20:23]
v_add_f32 v64,v64,v28
v_fma_f32 v65,v28,v28,v65
v_add_f32 v64,v64,v29
v_fma_f32 v65,v29,v29,v65
v_add_f32 v64,v64,v30
v_fma_f32 v65,v30,v30,v65
v_add_f32 v64,v64,v31
v_fma_f32 v65,v31,v31,v65
v_mfma_f32_16x16x32_bf16 v[16:19],a[120:123],v[60:63],v[16:19]
s_nop 0
v_permlane16_swap_b32 v64,v65
v_add_f32 v64,v64,v65
v_mov_b32 v65,v64
v_mfma_f32_16x16x32_bf16 v[20:23],a[124:127],v[60:63],v[20:23]
s_waitcnt lgkmcnt(2)
ds_read_b128 v[56:59],v2 offset:22528
v_permlane32_swap_b32 v64,v65
v_add_f32 v64,v64,v65
v_mfma_f32_16x16x32_bf16 v[16:19],a[128:131],v[32:35],v[16:19]
ds_write_b32 v13,v64 offset:0
v_mfma_f32_16x16x32_bf16 v[20:23],a[132:135],v[32:35],v[20:23]
ds_read_b128 v[60:63],v2 offset:23552
v_mfma_f32_16x16x32_bf16 v[16:19],a[136:139],v[36:39],v[16:19]
ds_read_b128 v[32:35],v2 offset:24576
v_mfma_f32_16x16x32_bf16 v[20:23],a[140:143],v[36:39],v[20:23]
v_mfma_f32_16x16x32_bf16 v[16:19],a[144:147],v[40:43],v[16:19]
s_waitcnt vmcnt(11) lgkmcnt(2)
ds_read_b128 v[36:39],v2 offset:25600
v_mfma_f32_16x16x32_bf16 v[20:23],a[148:151],v[40:43],v[20:23]
s_barrier
v_mfma_f32_16x16x32_bf16 v[16:19],a[152:155],v[44:47],v[16:19]
ds_read_b128 v[40:43],v2 offset:26624
ds_read_b32 v15,v7 offset:0
v_mfma_f32_16x16x32_bf16 v[20:23],a[156:159],v[44:47],v[20:23]
s_waitcnt lgkmcnt(4)
v_mfma_f32_16x16x32_bf16 v[16:19],a[160:163],v[48:51],v[16:19]
ds_read_b128 v[44:47],v2 offset:27648
v_mfma_f32_16x16x32_bf16 v[20:23],a[164:167],v[48:51],v[20:23]
v_mfma_f32_16x16x32_bf16 v[16:19],a[168:171],v[52:55],v[16:19]
s_waitcnt lgkmcnt(1)
ds_read_b128 v[48:51],v2 offset:28672
v_mfma_f32_16x16x32_bf16 v[20:23],a[172:175],v[52:55],v[20:23]
v_cmp_eq_u32 vcc,s60,v15
v_mfma_f32_16x16x32_bf16 v[16:19],a[176:179],v[56:59],v[16:19]
s_cmp_eq_u32 vcc_lo,-1)"
R"(
ds_read_b128 v[52:55],v2 offset:29696
s_cselect_b32 s67,s86,s59
v_mfma_f32_16x16x32_bf16 v[20:23],a[180:183],v[56:59],v[20:23]
buffer_load_dwordx4 v0,s[36:39],s67 offen offset:0 sc1 lds
v_mfma_f32_16x16x32_bf16 v[16:19],a[184:187],v[60:63],v[16:19]
s_xor_b32 s63,s63,65536
ds_read_b128 v[56:59],v2 offset:30720
s_add_u32 s53,s52,s55
v_mfma_f32_16x16x32_bf16 v[20:23],a[188:191],v[60:63],v[20:23]
s_waitcnt lgkmcnt(3)
s_cmp_eq_u32 s53,s56
s_cselect_b32 s53,s57,s53
v_mfma_f32_16x16x32_bf16 v[16:19],a[192:195],v[32:35],v[16:19]
s_waitcnt vmcnt(8)
ds_read_b128 v[60:63],v2 offset:31744
s_cmp_eq_u32 s53,s58
v_mfma_f32_16x16x32_bf16 v[20:23],a[196:199],v[32:35],v[20:23]
s_barrier
v_mfma_f32_16x16x32_bf16 v[16:19],a[200:203],v[36:39],v[16:19]
s_cselect_b32 s53,s59,s53
ds_read_b128 v[32:35],v2 offset:32768
ds_read_b128 v[80:83],v8 offset:0
v_mfma_f32_16x16x32_bf16 v[20:23],a[204:207],v[36:39],v[20:23]
buffer_load_dwordx4 v0,s[36:39],s67 offen offset:1024 sc1 lds
v_mfma_f32_16x16x32_bf16 v[16:19],a[208:211],v[40:43],v[16:19]
s_cmp_ge_u32 s66,s59
ds_read_b128 v[36:39],v2 offset:33792
ds_read_b64 v[88:89],v9 offset:1024
v_mfma_f32_16x16x32_bf16 v[20:23],a[212:215],v[40:43],v[20:23]
v_mfma_f32_16x16x32_bf16 v[16:19],a[216:219],v[44:47],v[16:19]
ds_read_b128 v[40:43],v2 offset:34816
v_mfma_f32_16x16x32_bf16 v[20:23],a[220:223],v[44:47],v[20:23]
s_waitcnt lgkmcnt(5)
buffer_load_dwordx4 v0,s[36:39],s67 offen offset:2048 sc1 lds
v_mfma_f32_16x16x32_bf16 v[16:19],a[224:227],v[48:51],v[16:19]
ds_read_b128 v[44:47],v2 offset:35840
v_mfma_f32_16x16x32_bf16 v[20:23],a[228:231],v[48:51],v[20:23]
v_mfma_f32_16x16x32_bf16 v[16:19],a[232:235],v[52:55],v[16:19]
s_waitcnt lgkmcnt(2)
ds_read_b128 v[48:51],v2 offset:36864
v_mfma_f32_16x16x32_bf16 v[20:23],a[236:239],v[52:55],v[20:23]
buffer_load_dwordx4 v0,s[36:39],s67 offen offset:3072 sc1 lds
v_mfma_f32_16x16x32_bf16 v[16:19],a[240:243],v[56:59],v[16:19]
ds_read_b128 v[52:55],v2 offset:37888
v_mfma_f32_16x16x32_bf16 v[20:23],a[244:247],v[56:59],v[20:23]
v_mfma_f32_16x16x32_bf16 v[16:19],a[248:251],v[60:63],v[16:19]
s_waitcnt lgkmcnt(2)
ds_read_b128 v[56:59],v2 offset:38912
v_mfma_f32_16x16x32_bf16 v[20:23],a[252:255],v[60:63],v[20:23]
s_cbranch_scc1 Lg2_slow1_%=
Lg2_d1w1_%=:
v_mfma_f32_16x16x32_bf16 v[24:27],a[0:3],v[32:35],0
s_add_u32 m0,s62,16384
ds_read_b128 v[60:63],v2 offset:39936
v_lshlrev_b32 v64,16,v80
v_mfma_f32_16x16x32_bf16 v[28:31],a[4:7],v[32:35],0
s_lshr_b32 s73,s48,0
v_and_b32 v65,0xffff0000,v80
v_lshlrev_b32 v66,16,v81
v_mfma_f32_16x16x32_bf16 v[24:27],a[8:11],v[36:39],v[24:27]
buffer_load_dwordx4 v1,s[36:39],s67 offen offset:0 sc1 lds
v_mfma_f32_16x16x32_bf16 v[28:31],a[12:15],v[36:39],v[28:31]
s_xor_b32 s87,s63,65536
ds_read_b128 v[32:35],v2 offset:40960
v_and_b32 v67,0xffff0000,v81
v_mfma_f32_16x16x32_bf16 v[24:27],a[16:19],v[40:43],v[24:27]
s_cmp_ge_u32 s67,s59
ds_read_b128 v[36:39],v2 offset:41984
v_lshlrev_b32 v68,16,v82
v_mfma_f32_16x16x32_bf16 v[28:31],a[20:23],v[40:43],v[28:31]
s_cselect_b32 s86,s50,s51
v_and_b32 v69,0xffff0000,v82
v_lshlrev_b32 v70,16,v83
v_mfma_f32_16x16x32_bf16 v[24:27],a[24:27],v[44:47],v[24:27]
buffer_load_dwordx4 v1,s[36:39],s67 offen offset:1024 sc1 lds
v_mfma_f32_16x16x32_bf16 v[28:31],a[28:31],v[44:47],v[28:31]
s_waitcnt lgkmcnt(2)
ds_read_b128 v[40:43],v2 offset:43008
v_and_b32 v71,0xffff0000,v83
v_mfma_f32_16x16x32_bf16 v[24:27],a[32:35],v[48:51],v[24:27]
s_cselect_b32 s87,s87,s64
ds_read_b128 v[44:47],v2 offset:44032
v_fma_f32 v64,v64,v89,v88
v_mfma_f32_16x16x32_bf16 v[28:31],a[36:39],v[48:51],v[28:31]
s_lshr_b32 s72,s67,6
v_fma_f32 v65,v65,v89,v88
v_fma_f32 v66,v66,v89,v88
v_mfma_f32_16x16x32_bf16 v[24:27],a[40:43],v[52:55],v[24:27]
buffer_load_dwordx4 v1,s[36:39],s67 offen offset:2048 sc1 lds
v_mfma_f32_16x16x32_bf16 v[28:31],a[44:47],v[52:55],v[28:31]
s_lshr_b32 s71,s62,2
ds_read_b128 v[48:51],v2 offset:45056
v_fma_f32 v67,v67,v89,v88
v_mfma_f32_16x16x32_bf16 v[24:27],a[48:51],v[56:59],v[24:27]
s_mov_b32 s62,s87
ds_read_b128 v[52:55],v2 offset:46080
v_fma_f32 v68,v68,v89,v88
v_mfma_f32_16x16x32_bf16 v[28:31],a[52:55],v[56:59],v[28:31]
s_mov_b64 s[44:45],s[46:47]
v_fma_f32 v69,v69,v89,v88
v_fma_f32 v70,v70,v89,v88
v_mfma_f32_16x16x32_bf16 v[24:27],a[56:59],v[60:63],v[24:27]
buffer_load_dwordx4 v1,s[36:39],s67 offen offset:3072 sc1 lds
v_mfma_f32_16x16x32_bf16 v[28:31],a[60:63],v[60:63],v[28:31]
s_waitcnt lgkmcnt(2)
ds_read_b128 v[56:59],v2 offset:47104
v_fma_f32 v71,v71,v89,v88
v_mfma_f32_16x16x32_bf16 v[24:27],a[64:67],v[32:35],v[24:27]
s_waitcnt vmcnt(9)
ds_read_b128 v[60:63],v2 offset:48128
v_add_f32 v16,v16,v64
v_mfma_f32_16x16x32_bf16 v[28:31],a[68:71],v[32:35],v[28:31]
s_barrier
v_mfma_f32_16x16x32_bf16 v[24:27],a[72:75],v[36:39],v[24:27]
s_add_u32 m0,s71,s88
ds_read_b128 v[32:35],v2 offset:49152
v_add_f32 v17,v17,v65
v_mfma_f32_16x16x32_bf16 v[28:31],a[76:79],v[36:39],v[28:31]
s_mov_b64 s[46:47],s[48:49]
v_add_f32 v18,v18,v66
v_add_f32 v19,v19,v67
v_mfma_f32_16x16x32_bf16 v[24:27],a[80:83],v[40:43],v[24:27]
buffer_load_dwordx4 v10,s[36:39],s67 offen offset:0 sc1 lds
v_mfma_f32_16x16x32_bf16 v[28:31],a[84:87],v[40:43],v[28:31]
s_mov_b64 s[48:49],s[50:51]
ds_read_b128 v[36:39],v2 offset:50176
v_add_f32 v20,v20,v68
v_mfma_f32_16x16x32_bf16 v[24:27],a[88:91],v[44:47],v[24:27]
buffer_load_dword v11,s[36:39],s72 offen offset:1024 sc1 lds
v_mfma_f32_16x16x32_bf16 v[28:31],a[92:95],v[44:47],v[28:31]
s_waitcnt lgkmcnt(2)
ds_read_b128 v[40:43],v2 offset:51200
v_add_f32 v21,v21,v69
v_mfma_f32_16x16x32_bf16 v[24:27],a[96:99],v[48:51],v[24:27]
s_mov_b32 m0,s62
v_add_f32 v22,v22,v70
v_add_f32 v23,v23,v71
v_cvt_pk_bf16_f32 v72,v16,v17
v_cvt_pk_bf16_f32 v73,v18,v19
v_cvt_pk_bf16_f32 v74,v20,v21
v_cvt_pk_bf16_f32 v75,v22,v23
v_mfma_f32_16x16x32_bf16 v[28:31],a[100:103],v[48:51],v[28:31]
s_mov_b64 s[50:51],s[52:53]
ds_read_b128 v[44:47],v2 offset:52224
buffer_store_dwordx4 v[72:75],v3,s[36:39],s73 offen offset:0 sc1
v_add_f32 v64,v16,v17
v_mul_f32 v65,v16,v16
v_fma_f32 v65,v17,v17,v65
v_mfma_f32_16x16x32_bf16 v[24:27],a[104:107],v[52:55],v[24:27]
ds_read_b128 v[48:51],v2 offset:53248
v_add_f32 v64,v64,v18
v_mfma_f32_16x16x32_bf16 v[28:31],a[108:111],v[52:55],v[28:31]
v_fma_f32 v65,v18,v18,v65
v_add_f32 v64,v64,v19
v_mfma_f32_16x16x32_bf16 v[24:27],a[112:115],v[56:59],v[24:27]
ds_read_b128 v[52:55],v2 offset:54272
v_fma_f32 v65,v19,v19,v65
v_mfma_f32_16x16x32_bf16 v[28:31],a[116:119],v[56:59],v[28:31]
v_add_f32 v64,v64,v20
v_fma_f32 v65,v20,v20,v65
v_add_f32 v64,v64,v21
v_fma_f32 v65,v21,v21,v65
v_add_f32 v64,v64,v22
v_fma_f32 v65,v22,v22,v65
v_add_f32 v64,v64,v23
v_fma_f32 v65,v23,v23,v65
v_mfma_f32_16x16x32_bf16 v[24:27],a[120:123],v[60:63],v[24:27]
s_nop 0
v_permlane16_swap_b32 v64,v65
v_add_f32 v64,v64,v65
v_mov_b32 v65,v64
v_mfma_f32_16x16x32_bf16 v[28:31],a[124:127],v[60:63],v[28:31]
s_waitcnt lgkmcnt(2)
ds_read_b128 v[56:59],v2 offset:55296
v_permlane32_swap_b32 v64,v65
v_add_f32 v64,v64,v65
v_mfma_f32_16x16x32_bf16 v[24:27],a[128:131],v[32:35],v[24:27]
ds_write_b32 v13,v64 offset:2048
v_mfma_f32_16x16x32_bf16 v[28:31],a[132:135],v[32:35],v[28:31]
ds_read_b128 v[60:63],v2 offset:56320
v_mfma_f32_16x16x32_bf16 v[24:27],a[136:139],v[36:39],v[24:27]
ds_read_b128 v[32:35],v2 offset:57344
v_mfma_f32_16x16x32_bf16 v[28:31],a[140:143],v[36:39],v[28:31]
v_mfma_f32_16x16x32_bf16 v[24:27],a[144:147],v[40:43],v[24:27]
s_waitcnt vmcnt(11) lgkmcnt(2)
ds_read_b128 v[36:39],v2 offset:58368
v_mfma_f32_16x16x32_bf16 v[28:31],a[148:151],v[40:43],v[28:31]
s_barrier
v_mfma_f32_16x16x32_bf16 v[24:27],a[152:155],v[44:47],v[24:27]
ds_read_b128 v[40:43],v2 offset:59392
ds_read_b32 v15,v7 offset:256
v_mfma_f32_16x16x32_bf16 v[28:31],a[156:159],v[44:47],v[28:31]
s_waitcnt lgkmcnt(4)
v_mfma_f32_16x16x32_bf16 v[24:27],a[160:163],v[48:51],v[24:27]
ds_read_b128 v[44:47],v2 offset:60416
v_mfma_f32_16x16x32_bf16 v[28:31],a[164:167],v[48:51],v[28:31]
v_mfma_f32_16x16x32_bf16 v[24:27],a[168:171],v[52:55],v[24:27]
s_waitcnt lgkmcnt(1)
ds_read_b128 v[48:51],v2 offset:61440
v_mfma_f32_16x16x32_bf16 v[28:31],a[172:175],v[52:55],v[28:31])"
R"(
v_cmp_eq_u32 vcc,s60,v15
v_mfma_f32_16x16x32_bf16 v[24:27],a[176:179],v[56:59],v[24:27]
s_cmp_eq_u32 vcc_lo,-1
ds_read_b128 v[52:55],v2 offset:62464
s_cselect_b32 s66,s86,s59
v_mfma_f32_16x16x32_bf16 v[28:31],a[180:183],v[56:59],v[28:31]
buffer_load_dwordx4 v0,s[36:39],s66 offen offset:0 sc1 lds
v_mfma_f32_16x16x32_bf16 v[24:27],a[184:187],v[60:63],v[24:27]
s_xor_b32 s64,s64,65536
ds_read_b128 v[56:59],v2 offset:63488
s_add_u32 s74,s53,s55
v_mfma_f32_16x16x32_bf16 v[28:31],a[188:191],v[60:63],v[28:31]
s_waitcnt lgkmcnt(3)
s_cmp_eq_u32 s74,s56
s_cselect_b32 s74,s57,s74
v_mfma_f32_16x16x32_bf16 v[24:27],a[192:195],v[32:35],v[24:27]
s_waitcnt vmcnt(8)
ds_read_b128 v[60:63],v2 offset:64512
v_xor_b32 v2,65536,v2
v_mfma_f32_16x16x32_bf16 v[28:31],a[196:199],v[32:35],v[28:31]
s_barrier
v_mfma_f32_16x16x32_bf16 v[24:27],a[200:203],v[36:39],v[24:27]
s_cmp_eq_u32 s74,s58
ds_read_b128 v[32:35],v2 offset:0
ds_read_b128 v[84:87],v8 offset:8192
v_mfma_f32_16x16x32_bf16 v[28:31],a[204:207],v[36:39],v[28:31]
buffer_load_dwordx4 v0,s[36:39],s66 offen offset:1024 sc1 lds
v_mfma_f32_16x16x32_bf16 v[24:27],a[208:211],v[40:43],v[24:27]
s_cselect_b32 s74,s59,s74
ds_read_b128 v[36:39],v2 offset:1024
ds_read_b64 v[90:91],v9 offset:9216
v_mfma_f32_16x16x32_bf16 v[28:31],a[212:215],v[40:43],v[28:31]
s_mov_b32 s52,s74
v_xor_b32 v8,16384,v8
v_xor_b32 v9,16384,v9
v_mfma_f32_16x16x32_bf16 v[24:27],a[216:219],v[44:47],v[24:27]
s_cmp_lt_u32 s67,s59
ds_read_b128 v[40:43],v2 offset:2048
v_mfma_f32_16x16x32_bf16 v[28:31],a[220:223],v[44:47],v[28:31]
s_waitcnt lgkmcnt(5)
buffer_load_dwordx4 v0,s[36:39],s66 offen offset:2048 sc1 lds
v_mfma_f32_16x16x32_bf16 v[24:27],a[224:227],v[48:51],v[24:27]
ds_read_b128 v[44:47],v2 offset:3072
v_mfma_f32_16x16x32_bf16 v[28:31],a[228:231],v[48:51],v[28:31]
v_mfma_f32_16x16x32_bf16 v[24:27],a[232:235],v[52:55],v[24:27]
s_waitcnt lgkmcnt(2)
ds_read_b128 v[48:51],v2 offset:4096
v_mfma_f32_16x16x32_bf16 v[28:31],a[236:239],v[52:55],v[28:31]
buffer_load_dwordx4 v0,s[36:39],s66 offen offset:3072 sc1 lds
v_mfma_f32_16x16x32_bf16 v[24:27],a[240:243],v[56:59],v[24:27]
ds_read_b128 v[52:55],v2 offset:5120
v_mfma_f32_16x16x32_bf16 v[28:31],a[244:247],v[56:59],v[28:31]
v_mfma_f32_16x16x32_bf16 v[24:27],a[248:251],v[60:63],v[24:27]
s_waitcnt lgkmcnt(2)
ds_read_b128 v[56:59],v2 offset:6144
v_mfma_f32_16x16x32_bf16 v[28:31],a[252:255],v[60:63],v[28:31]
s_cbranch_scc1 Lg2_d0w1_%=
s_branch Lg2_slow0_%=
Lg2_d0w2_%=:
v_mfma_f32_16x16x32_bf16 v[16:19],a[0:3],v[32:35],0
s_add_u32 m0,s62,16384
ds_read_b128 v[60:63],v2 offset:7168
v_lshlrev_b32 v64,16,v84
v_mfma_f32_16x16x32_bf16 v[20:23],a[4:7],v[32:35],0
s_lshr_b32 s73,s47,0
v_and_b32 v65,0xffff0000,v84
v_lshlrev_b32 v66,16,v85
v_mfma_f32_16x16x32_bf16 v[16:19],a[8:11],v[36:39],v[16:19]
s_lshr_b32 s85,s47,5
ds_read_b128 v[32:35],v2 offset:8192
v_and_b32 v67,0xffff0000,v85
v_mfma_f32_16x16x32_bf16 v[20:23],a[12:15],v[36:39],v[20:23]
buffer_load_dwordx4 v1,s[36:39],s66 offen offset:0 sc1 lds
v_mfma_f32_16x16x32_bf16 v[16:19],a[16:19],v[40:43],v[16:19]
s_xor_b32 s87,s64,65536
ds_read_b128 v[36:39],v2 offset:9216
v_lshlrev_b32 v68,16,v86
v_mfma_f32_16x16x32_bf16 v[20:23],a[20:23],v[40:43],v[20:23]
s_cmp_ge_u32 s66,s59
v_and_b32 v69,0xffff0000,v86
v_lshlrev_b32 v70,16,v87
v_mfma_f32_16x16x32_bf16 v[16:19],a[24:27],v[44:47],v[16:19]
s_cselect_b32 s86,s49,s50
ds_read_b128 v[40:43],v2 offset:10240
v_and_b32 v71,0xffff0000,v87
v_mfma_f32_16x16x32_bf16 v[20:23],a[28:31],v[44:47],v[20:23]
s_waitcnt lgkmcnt(3)
buffer_load_dwordx4 v1,s[36:39],s66 offen offset:1024 sc1 lds
v_mfma_f32_16x16x32_bf16 v[16:19],a[32:35],v[48:51],v[16:19]
s_cselect_b32 s87,s87,s63
ds_read_b128 v[44:47],v2 offset:11264
v_fma_f32 v64,v64,v91,v90
v_mfma_f32_16x16x32_bf16 v[20:23],a[36:39],v[48:51],v[20:23]
s_lshr_b32 s72,s66,6
v_fma_f32 v65,v65,v91,v90
v_fma_f32 v66,v66,v91,v90
v_mfma_f32_16x16x32_bf16 v[16:19],a[40:43],v[52:55],v[16:19]
s_lshr_b32 s71,s62,2
ds_read_b128 v[48:51],v2 offset:12288
v_fma_f32 v67,v67,v91,v90
v_mfma_f32_16x16x32_bf16 v[20:23],a[44:47],v[52:55],v[20:23]
buffer_load_dwordx4 v1,s[36:39],s66 offen offset:2048 sc1 lds
v_mfma_f32_16x16x32_bf16 v[16:19],a[48:51],v[56:59],v[16:19]
s_mov_b32 s62,s87
ds_read_b128 v[52:55],v2 offset:13312
v_fma_f32 v68,v68,v91,v90
v_mfma_f32_16x16x32_bf16 v[20:23],a[52:55],v[56:59],v[20:23]
v_fma_f32 v69,v69,v91,v90
v_fma_f32 v70,v70,v91,v90
v_mfma_f32_16x16x32_bf16 v[16:19],a[56:59],v[60:63],v[16:19]
ds_read_b128 v[56:59],v2 offset:14336
v_fma_f32 v71,v71,v91,v90
v_mfma_f32_16x16x32_bf16 v[20:23],a[60:63],v[60:63],v[20:23]
s_waitcnt lgkmcnt(3)
buffer_load_dwordx4 v1,s[36:39],s66 offen offset:3072 sc1 lds
v_mfma_f32_16x16x32_bf16 v[16:19],a[64:67],v[32:35],v[16:19]
s_waitcnt vmcnt(10)
ds_read_b128 v[60:63],v2 offset:15360
v_add_f32 v24,v24,v64
v_mfma_f32_16x16x32_bf16 v[20:23],a[68:71],v[32:35],v[20:23]
s_barrier
v_mfma_f32_16x16x32_bf16 v[16:19],a[72:75],v[36:39],v[16:19]
s_add_u32 m0,s71,s88
ds_read_b128 v[32:35],v2 offset:16384
v_add_f32 v25,v25,v65
v_mfma_f32_16x16x32_bf16 v[20:23],a[76:79],v[36:39],v[20:23]
v_add_f32 v26,v26,v66
v_add_f32 v27,v27,v67
v_mfma_f32_16x16x32_bf16 v[16:19],a[80:83],v[40:43],v[16:19]
ds_read_b128 v[36:39],v2 offset:17408
v_add_f32 v28,v28,v68
v_mfma_f32_16x16x32_bf16 v[20:23],a[84:87],v[40:43],v[20:23]
buffer_load_dwordx4 v10,s[36:39],s66 offen offset:0 sc1 lds
v_mfma_f32_16x16x32_bf16 v[16:19],a[88:91],v[44:47],v[16:19]
buffer_load_dword v11,s[36:39],s72 offen offset:1024 sc1 lds
v_mfma_f32_16x16x32_bf16 v[20:23],a[92:95],v[44:47],v[20:23]
s_waitcnt lgkmcnt(2)
ds_read_b128 v[40:43],v2 offset:18432
v_add_f32 v29,v29,v69
v_mfma_f32_16x16x32_bf16 v[16:19],a[96:99],v[48:51],v[16:19]
s_mov_b32 m0,s62
ds_read_b128 v[44:47],v2 offset:19456
v_add_f32 v30,v30,v70
v_mfma_f32_16x16x32_bf16 v[20:23],a[100:103],v[48:51],v[20:23]
v_add_f32 v31,v31,v71
v_cvt_pk_bf16_f32 v72,v24,v25
v_cvt_pk_bf16_f32 v73,v26,v27
v_cvt_pk_bf16_f32 v74,v28,v29
v_cvt_pk_bf16_f32 v75,v30,v31
v_mfma_f32_16x16x32_bf16 v[16:19],a[104:107],v[52:55],v[16:19]
buffer_store_dwordx4 v[72:75],v3,s[36:39],s73 offen offset:0 sc1
ds_read_b128 v[48:51],v2 offset:20480
v_add_f32 v64,v24,v25
v_mul_f32 v65,v24,v24
v_mfma_f32_16x16x32_bf16 v[20:23],a[108:111],v[52:55],v[20:23]
v_fma_f32 v65,v25,v25,v65
v_add_f32 v64,v64,v26
v_mfma_f32_16x16x32_bf16 v[16:19],a[112:115],v[56:59],v[16:19]
ds_read_b128 v[52:55],v2 offset:21504
v_fma_f32 v65,v26,v26,v65
v_mfma_f32_16x16x32_bf16 v[20:23],a[116:119],v[56:59],v[20:23]
v_add_f32 v64,v64,v27
v_fma_f32 v65,v27,v27,v65
v_add_f32 v64,v64,v28
v_fma_f32 v65,v28,v28,v65
v_add_f32 v64,v64,v29
v_fma_f32 v65,v29,v29,v65
v_add_f32 v64,v64,v30
v_fma_f32 v65,v30,v30,v65
v_add_f32 v64,v64,v31
v_fma_f32 v65,v31,v31,v65
v_mfma_f32_16x16x32_bf16 v[16:19],a[120:123],v[60:63],v[16:19]
s_nop 0
v_permlane16_swap_b32 v64,v65
v_add_f32 v64,v64,v65
v_mov_b32 v65,v64
v_mfma_f32_16x16x32_bf16 v[20:23],a[124:127],v[60:63],v[20:23]
s_waitcnt lgkmcnt(2)
ds_read_b128 v[56:59],v2 offset:22528
v_permlane32_swap_b32 v64,v65
v_add_f32 v64,v64,v65
v_mfma_f32_16x16x32_bf16 v[16:19],a[128:131],v[32:35],v[16:19]
ds_write_b32 v13,v64 offset:0
v_mfma_f32_16x16x32_bf16 v[20:23],a[132:135],v[32:35],v[20:23]
ds_read_b128 v[60:63],v2 offset:23552
v_mfma_f32_16x16x32_bf16 v[16:19],a[136:139],v[36:39],v[16:19]
ds_read_b128 v[32:35],v2 offset:24576
v_mfma_f32_16x16x32_bf16 v[20:23],a[140:143],v[36:39],v[20:23]
v_mfma_f32_16x16x32_bf16 v[16:19],a[144:147],v[40:43],v[16:19]
s_waitcnt vmcnt(11) lgkmcnt(2)
ds_read_b128 v[36:39],v2 offset:25600
v_mfma_f32_16x16x32_bf16 v[20:23],a[148:151],v[40:43],v[20:23]
s_barrier
v_mfma_f32_16x16x32_bf16 v[16:19],a[152:155],v[44:47],v[16:19]
ds_read_b128 v[40:43],v2 offset:26624
ds_read_b32 v15,v7 offset:0
v_mfma_f32_16x16x32_bf16 v[20:23],a[156:159],v[44:47],v[20:23]
s_waitcnt lgkmcnt(4)
ds_read_b128 v[76:79],v14 offset:0
v_mfma_f32_16x16x32_bf16 v[16:19],a[160:163],v[48:51],v[16:19])"
R"(
ds_read_b128 v[44:47],v2 offset:27648
v_mfma_f32_16x16x32_bf16 v[20:23],a[164:167],v[48:51],v[20:23]
v_mfma_f32_16x16x32_bf16 v[16:19],a[168:171],v[52:55],v[16:19]
s_waitcnt lgkmcnt(2)
ds_read_b128 v[48:51],v2 offset:28672
v_mfma_f32_16x16x32_bf16 v[20:23],a[172:175],v[52:55],v[20:23]
s_waitcnt lgkmcnt(2)
v_cmp_eq_u32 vcc,s60,v15
v_add_f32 v76,v76,v77
v_add_f32 v78,v78,v79
v_add_f32 v76,v76,v78
v_mfma_f32_16x16x32_bf16 v[16:19],a[176:179],v[56:59],v[16:19]
buffer_store_dword v76,v12,s[36:39],s85 offen offset:0 sc1
ds_read_b128 v[52:55],v2 offset:29696
v_mfma_f32_16x16x32_bf16 v[20:23],a[180:183],v[56:59],v[20:23]
s_cmp_eq_u32 vcc_lo,-1
s_cselect_b32 s67,s86,s59
s_xor_b32 s63,s63,65536
v_mfma_f32_16x16x32_bf16 v[16:19],a[184:187],v[60:63],v[16:19]
buffer_load_dwordx4 v0,s[36:39],s67 offen offset:0 sc1 lds
v_mfma_f32_16x16x32_bf16 v[20:23],a[188:191],v[60:63],v[20:23]
s_waitcnt lgkmcnt(2)
ds_read_b128 v[56:59],v2 offset:30720
s_add_u32 s53,s52,s55
v_mfma_f32_16x16x32_bf16 v[16:19],a[192:195],v[32:35],v[16:19]
s_waitcnt vmcnt(9)
ds_read_b128 v[60:63],v2 offset:31744
s_cmp_eq_u32 s53,s56
v_mfma_f32_16x16x32_bf16 v[20:23],a[196:199],v[32:35],v[20:23]
s_barrier
v_mfma_f32_16x16x32_bf16 v[16:19],a[200:203],v[36:39],v[16:19]
s_cselect_b32 s53,s57,s53
ds_read_b128 v[32:35],v2 offset:32768
ds_read_b128 v[80:83],v8 offset:0
v_mfma_f32_16x16x32_bf16 v[20:23],a[204:207],v[36:39],v[20:23]
s_cmp_eq_u32 s53,s58
ds_read_b64 v[88:89],v9 offset:1024
s_cselect_b32 s53,s59,s53
v_mfma_f32_16x16x32_bf16 v[16:19],a[208:211],v[40:43],v[16:19]
buffer_load_dwordx4 v0,s[36:39],s67 offen offset:1024 sc1 lds
v_mfma_f32_16x16x32_bf16 v[20:23],a[212:215],v[40:43],v[20:23]
s_cmp_ge_u32 s66,s59
ds_read_b128 v[36:39],v2 offset:33792
v_mfma_f32_16x16x32_bf16 v[16:19],a[216:219],v[44:47],v[16:19]
ds_read_b128 v[40:43],v2 offset:34816
v_mfma_f32_16x16x32_bf16 v[20:23],a[220:223],v[44:47],v[20:23]
s_waitcnt lgkmcnt(5)
v_mfma_f32_16x16x32_bf16 v[16:19],a[224:227],v[48:51],v[16:19]
buffer_load_dwordx4 v0,s[36:39],s67 offen offset:2048 sc1 lds
v_mfma_f32_16x16x32_bf16 v[20:23],a[228:231],v[48:51],v[20:23]
s_waitcnt lgkmcnt(2)
ds_read_b128 v[44:47],v2 offset:35840
v_mfma_f32_16x16x32_bf16 v[16:19],a[232:235],v[52:55],v[16:19]
ds_read_b128 v[48:51],v2 offset:36864
v_mfma_f32_16x16x32_bf16 v[20:23],a[236:239],v[52:55],v[20:23]
v_mfma_f32_16x16x32_bf16 v[16:19],a[240:243],v[56:59],v[16:19]
buffer_load_dwordx4 v0,s[36:39],s67 offen offset:3072 sc1 lds
v_mfma_f32_16x16x32_bf16 v[20:23],a[244:247],v[56:59],v[20:23]
ds_read_b128 v[52:55],v2 offset:37888
v_mfma_f32_16x16x32_bf16 v[16:19],a[248:251],v[60:63],v[16:19]
s_waitcnt lgkmcnt(2)
ds_read_b128 v[56:59],v2 offset:38912
v_mfma_f32_16x16x32_bf16 v[20:23],a[252:255],v[60:63],v[20:23]
s_cbranch_scc1 Lg2_slow1_%=
Lg2_d1w2_%=:
v_mfma_f32_16x16x32_bf16 v[24:27],a[0:3],v[32:35],0
s_add_u32 m0,s62,16384
ds_read_b128 v[60:63],v2 offset:39936
v_lshlrev_b32 v64,16,v80
v_mfma_f32_16x16x32_bf16 v[28:31],a[4:7],v[32:35],0
s_lshr_b32 s73,s48,0
v_and_b32 v65,0xffff0000,v80
v_lshlrev_b32 v66,16,v81
v_mfma_f32_16x16x32_bf16 v[24:27],a[8:11],v[36:39],v[24:27]
s_lshr_b32 s85,s48,5
ds_read_b128 v[32:35],v2 offset:40960
v_and_b32 v67,0xffff0000,v81
v_mfma_f32_16x16x32_bf16 v[28:31],a[12:15],v[36:39],v[28:31]
buffer_load_dwordx4 v1,s[36:39],s67 offen offset:0 sc1 lds
v_mfma_f32_16x16x32_bf16 v[24:27],a[16:19],v[40:43],v[24:27]
s_xor_b32 s87,s63,65536
ds_read_b128 v[36:39],v2 offset:41984
v_lshlrev_b32 v68,16,v82
v_mfma_f32_16x16x32_bf16 v[28:31],a[20:23],v[40:43],v[28:31]
s_cmp_ge_u32 s67,s59
v_and_b32 v69,0xffff0000,v82
v_lshlrev_b32 v70,16,v83
v_mfma_f32_16x16x32_bf16 v[24:27],a[24:27],v[44:47],v[24:27]
s_cselect_b32 s86,s50,s51
ds_read_b128 v[40:43],v2 offset:43008
v_and_b32 v71,0xffff0000,v83
v_mfma_f32_16x16x32_bf16 v[28:31],a[28:31],v[44:47],v[28:31]
s_waitcnt lgkmcnt(3)
buffer_load_dwordx4 v1,s[36:39],s67 offen offset:1024 sc1 lds
v_mfma_f32_16x16x32_bf16 v[24:27],a[32:35],v[48:51],v[24:27]
s_cselect_b32 s87,s87,s64
ds_read_b128 v[44:47],v2 offset:44032
v_fma_f32 v64,v64,v89,v88
v_mfma_f32_16x16x32_bf16 v[28:31],a[36:39],v[48:51],v[28:31]
s_lshr_b32 s72,s67,6
v_fma_f32 v65,v65,v89,v88
v_fma_f32 v66,v66,v89,v88
v_mfma_f32_16x16x32_bf16 v[24:27],a[40:43],v[52:55],v[24:27]
s_lshr_b32 s71,s62,2
ds_read_b128 v[48:51],v2 offset:45056
v_fma_f32 v67,v67,v89,v88
v_mfma_f32_16x16x32_bf16 v[28:31],a[44:47],v[52:55],v[28:31]
buffer_load_dwordx4 v1,s[36:39],s67 offen offset:2048 sc1 lds
v_mfma_f32_16x16x32_bf16 v[24:27],a[48:51],v[56:59],v[24:27]
s_mov_b32 s62,s87
ds_read_b128 v[52:55],v2 offset:46080
v_fma_f32 v68,v68,v89,v88
v_mfma_f32_16x16x32_bf16 v[28:31],a[52:55],v[56:59],v[28:31]
s_mov_b64 s[44:45],s[46:47]
v_fma_f32 v69,v69,v89,v88
v_fma_f32 v70,v70,v89,v88
v_mfma_f32_16x16x32_bf16 v[24:27],a[56:59],v[60:63],v[24:27]
s_mov_b64 s[46:47],s[48:49]
ds_read_b128 v[56:59],v2 offset:47104
v_fma_f32 v71,v71,v89,v88
v_mfma_f32_16x16x32_bf16 v[28:31],a[60:63],v[60:63],v[28:31]
s_waitcnt lgkmcnt(3)
buffer_load_dwordx4 v1,s[36:39],s67 offen offset:3072 sc1 lds
v_mfma_f32_16x16x32_bf16 v[24:27],a[64:67],v[32:35],v[24:27]
s_waitcnt vmcnt(10)
ds_read_b128 v[60:63],v2 offset:48128
v_add_f32 v16,v16,v64
v_mfma_f32_16x16x32_bf16 v[28:31],a[68:71],v[32:35],v[28:31]
s_barrier
v_mfma_f32_16x16x32_bf16 v[24:27],a[72:75],v[36:39],v[24:27]
s_add_u32 m0,s71,s88
ds_read_b128 v[32:35],v2 offset:49152
v_add_f32 v17,v17,v65
v_mfma_f32_16x16x32_bf16 v[28:31],a[76:79],v[36:39],v[28:31]
s_mov_b64 s[48:49],s[50:51]
v_add_f32 v18,v18,v66
v_add_f32 v19,v19,v67
v_mfma_f32_16x16x32_bf16 v[24:27],a[80:83],v[40:43],v[24:27]
s_mov_b64 s[50:51],s[52:53]
ds_read_b128 v[36:39],v2 offset:50176
v_add_f32 v20,v20,v68
v_mfma_f32_16x16x32_bf16 v[28:31],a[84:87],v[40:43],v[28:31]
buffer_load_dwordx4 v10,s[36:39],s67 offen offset:0 sc1 lds
v_mfma_f32_16x16x32_bf16 v[24:27],a[88:91],v[44:47],v[24:27]
buffer_load_dword v11,s[36:39],s72 offen offset:1024 sc1 lds
v_mfma_f32_16x16x32_bf16 v[28:31],a[92:95],v[44:47],v[28:31]
s_waitcnt lgkmcnt(2)
ds_read_b128 v[40:43],v2 offset:51200
v_add_f32 v21,v21,v69
v_mfma_f32_16x16x32_bf16 v[24:27],a[96:99],v[48:51],v[24:27]
s_mov_b32 m0,s62
ds_read_b128 v[44:47],v2 offset:52224
v_add_f32 v22,v22,v70
v_mfma_f32_16x16x32_bf16 v[28:31],a[100:103],v[48:51],v[28:31]
v_add_f32 v23,v23,v71
v_cvt_pk_bf16_f32 v72,v16,v17
v_cvt_pk_bf16_f32 v73,v18,v19
v_cvt_pk_bf16_f32 v74,v20,v21
v_cvt_pk_bf16_f32 v75,v22,v23
v_mfma_f32_16x16x32_bf16 v[24:27],a[104:107],v[52:55],v[24:27]
buffer_store_dwordx4 v[72:75],v3,s[36:39],s73 offen offset:0 sc1
ds_read_b128 v[48:51],v2 offset:53248
v_add_f32 v64,v16,v17
v_mul_f32 v65,v16,v16
v_mfma_f32_16x16x32_bf16 v[28:31],a[108:111],v[52:55],v[28:31]
v_fma_f32 v65,v17,v17,v65
v_add_f32 v64,v64,v18
v_mfma_f32_16x16x32_bf16 v[24:27],a[112:115],v[56:59],v[24:27]
ds_read_b128 v[52:55],v2 offset:54272
v_fma_f32 v65,v18,v18,v65
v_mfma_f32_16x16x32_bf16 v[28:31],a[116:119],v[56:59],v[28:31]
v_add_f32 v64,v64,v19
v_fma_f32 v65,v19,v19,v65
v_add_f32 v64,v64,v20
v_fma_f32 v65,v20,v20,v65
v_add_f32 v64,v64,v21
v_fma_f32 v65,v21,v21,v65
v_add_f32 v64,v64,v22
v_fma_f32 v65,v22,v22,v65
v_add_f32 v64,v64,v23
v_fma_f32 v65,v23,v23,v65
v_mfma_f32_16x16x32_bf16 v[24:27],a[120:123],v[60:63],v[24:27]
s_nop 0
v_permlane16_swap_b32 v64,v65
v_add_f32 v64,v64,v65
v_mov_b32 v65,v64
v_mfma_f32_16x16x32_bf16 v[28:31],a[124:127],v[60:63],v[28:31]
s_waitcnt lgkmcnt(2)
ds_read_b128 v[56:59],v2 offset:55296
v_permlane32_swap_b32 v64,v65
v_add_f32 v64,v64,v65
v_mfma_f32_16x16x32_bf16 v[24:27],a[128:131],v[32:35],v[24:27]
ds_write_b32 v13,v64 offset:2048
v_mfma_f32_16x16x32_bf16 v[28:31],a[132:135],v[32:35],v[28:31]
ds_read_b128 v[60:63],v2 offset:56320
v_mfma_f32_16x16x32_bf16 v[24:27],a[136:139],v[36:39],v[24:27]
ds_read_b128 v[32:35],v2 offset:57344
v_mfma_f32_16x16x32_bf16 v[28:31],a[140:143],v[36:39],v[28:31]
v_mfma_f32_16x16x32_bf16 v[24:27],a[144:147],v[40:43],v[24:27]
s_waitcnt vmcnt(11) lgkmcnt(2))"
R"(
ds_read_b128 v[36:39],v2 offset:58368
v_mfma_f32_16x16x32_bf16 v[28:31],a[148:151],v[40:43],v[28:31]
s_barrier
v_mfma_f32_16x16x32_bf16 v[24:27],a[152:155],v[44:47],v[24:27]
ds_read_b128 v[40:43],v2 offset:59392
ds_read_b32 v15,v7 offset:256
v_mfma_f32_16x16x32_bf16 v[28:31],a[156:159],v[44:47],v[28:31]
s_waitcnt lgkmcnt(4)
ds_read_b128 v[76:79],v14 offset:2048
v_mfma_f32_16x16x32_bf16 v[24:27],a[160:163],v[48:51],v[24:27]
ds_read_b128 v[44:47],v2 offset:60416
v_mfma_f32_16x16x32_bf16 v[28:31],a[164:167],v[48:51],v[28:31]
v_mfma_f32_16x16x32_bf16 v[24:27],a[168:171],v[52:55],v[24:27]
s_waitcnt lgkmcnt(2)
ds_read_b128 v[48:51],v2 offset:61440
v_mfma_f32_16x16x32_bf16 v[28:31],a[172:175],v[52:55],v[28:31]
s_waitcnt lgkmcnt(2)
v_cmp_eq_u32 vcc,s60,v15
v_add_f32 v76,v76,v77
v_add_f32 v78,v78,v79
v_add_f32 v76,v76,v78
v_mfma_f32_16x16x32_bf16 v[24:27],a[176:179],v[56:59],v[24:27]
buffer_store_dword v76,v12,s[36:39],s85 offen offset:0 sc1
ds_read_b128 v[52:55],v2 offset:62464
v_mfma_f32_16x16x32_bf16 v[28:31],a[180:183],v[56:59],v[28:31]
s_cmp_eq_u32 vcc_lo,-1
s_cselect_b32 s66,s86,s59
s_xor_b32 s64,s64,65536
v_mfma_f32_16x16x32_bf16 v[24:27],a[184:187],v[60:63],v[24:27]
buffer_load_dwordx4 v0,s[36:39],s66 offen offset:0 sc1 lds
v_mfma_f32_16x16x32_bf16 v[28:31],a[188:191],v[60:63],v[28:31]
s_waitcnt lgkmcnt(2)
ds_read_b128 v[56:59],v2 offset:63488
s_add_u32 s74,s53,s55
v_mfma_f32_16x16x32_bf16 v[24:27],a[192:195],v[32:35],v[24:27]
s_waitcnt vmcnt(9)
ds_read_b128 v[60:63],v2 offset:64512
v_xor_b32 v2,65536,v2
v_mfma_f32_16x16x32_bf16 v[28:31],a[196:199],v[32:35],v[28:31]
s_barrier
v_mfma_f32_16x16x32_bf16 v[24:27],a[200:203],v[36:39],v[24:27]
s_cmp_eq_u32 s74,s56
ds_read_b128 v[32:35],v2 offset:0
ds_read_b128 v[84:87],v8 offset:8192
v_mfma_f32_16x16x32_bf16 v[28:31],a[204:207],v[36:39],v[28:31]
s_cselect_b32 s74,s57,s74
ds_read_b64 v[90:91],v9 offset:9216
v_xor_b32 v8,16384,v8
v_mfma_f32_16x16x32_bf16 v[24:27],a[208:211],v[40:43],v[24:27]
buffer_load_dwordx4 v0,s[36:39],s66 offen offset:1024 sc1 lds
v_mfma_f32_16x16x32_bf16 v[28:31],a[212:215],v[40:43],v[28:31]
s_cmp_eq_u32 s74,s58
ds_read_b128 v[36:39],v2 offset:1024
v_xor_b32 v9,16384,v9
v_mfma_f32_16x16x32_bf16 v[24:27],a[216:219],v[44:47],v[24:27]
s_cselect_b32 s74,s59,s74
ds_read_b128 v[40:43],v2 offset:2048
s_mov_b32 s52,s74
v_mfma_f32_16x16x32_bf16 v[28:31],a[220:223],v[44:47],v[28:31]
s_waitcnt lgkmcnt(5)
s_cmp_lt_u32 s67,s59
v_mfma_f32_16x16x32_bf16 v[24:27],a[224:227],v[48:51],v[24:27]
buffer_load_dwordx4 v0,s[36:39],s66 offen offset:2048 sc1 lds
v_mfma_f32_16x16x32_bf16 v[28:31],a[228:231],v[48:51],v[28:31]
s_waitcnt lgkmcnt(2)
ds_read_b128 v[44:47],v2 offset:3072
v_mfma_f32_16x16x32_bf16 v[24:27],a[232:235],v[52:55],v[24:27]
ds_read_b128 v[48:51],v2 offset:4096
v_mfma_f32_16x16x32_bf16 v[28:31],a[236:239],v[52:55],v[28:31]
v_mfma_f32_16x16x32_bf16 v[24:27],a[240:243],v[56:59],v[24:27]
buffer_load_dwordx4 v0,s[36:39],s66 offen offset:3072 sc1 lds
v_mfma_f32_16x16x32_bf16 v[28:31],a[244:247],v[56:59],v[28:31]
ds_read_b128 v[52:55],v2 offset:5120
v_mfma_f32_16x16x32_bf16 v[24:27],a[248:251],v[60:63],v[24:27]
s_waitcnt lgkmcnt(2)
ds_read_b128 v[56:59],v2 offset:6144
v_mfma_f32_16x16x32_bf16 v[28:31],a[252:255],v[60:63],v[28:31]
s_cbranch_scc1 Lg2_d0w2_%=
s_branch Lg2_slow0_%=
Lg2_d0w3_%=:
v_mfma_f32_16x16x32_bf16 v[16:19],a[0:3],v[32:35],0
s_add_u32 m0,s62,16384
ds_read_b128 v[60:63],v2 offset:7168
v_lshlrev_b32 v64,16,v84
v_mfma_f32_16x16x32_bf16 v[20:23],a[4:7],v[32:35],0
s_lshr_b32 s73,s47,0
v_and_b32 v65,0xffff0000,v84
v_lshlrev_b32 v66,16,v85
v_mfma_f32_16x16x32_bf16 v[16:19],a[8:11],v[36:39],v[16:19]
s_xor_b32 s87,s64,65536
ds_read_b128 v[32:35],v2 offset:8192
v_and_b32 v67,0xffff0000,v85
v_mfma_f32_16x16x32_bf16 v[20:23],a[12:15],v[36:39],v[20:23]
s_cmp_ge_u32 s66,s59
v_lshlrev_b32 v68,16,v86
v_and_b32 v69,0xffff0000,v86
v_mfma_f32_16x16x32_bf16 v[16:19],a[16:19],v[40:43],v[16:19]
buffer_load_dwordx4 v1,s[36:39],s66 offen offset:0 sc1 lds
v_mfma_f32_16x16x32_bf16 v[20:23],a[20:23],v[40:43],v[20:23]
s_cselect_b32 s86,s49,s50
ds_read_b128 v[36:39],v2 offset:9216
v_lshlrev_b32 v70,16,v87
v_mfma_f32_16x16x32_bf16 v[16:19],a[24:27],v[44:47],v[16:19]
s_cselect_b32 s87,s87,s63
ds_read_b128 v[40:43],v2 offset:10240
v_and_b32 v71,0xffff0000,v87
v_mfma_f32_16x16x32_bf16 v[20:23],a[28:31],v[44:47],v[20:23]
s_waitcnt lgkmcnt(3)
v_fma_f32 v64,v64,v91,v90
v_fma_f32 v65,v65,v91,v90
v_mfma_f32_16x16x32_bf16 v[16:19],a[32:35],v[48:51],v[16:19]
buffer_load_dwordx4 v1,s[36:39],s66 offen offset:1024 sc1 lds
v_mfma_f32_16x16x32_bf16 v[20:23],a[36:39],v[48:51],v[20:23]
s_lshr_b32 s72,s66,6
ds_read_b128 v[44:47],v2 offset:11264
v_fma_f32 v66,v66,v91,v90
v_mfma_f32_16x16x32_bf16 v[16:19],a[40:43],v[52:55],v[16:19]
s_lshr_b32 s71,s62,2
ds_read_b128 v[48:51],v2 offset:12288
v_fma_f32 v67,v67,v91,v90
v_mfma_f32_16x16x32_bf16 v[20:23],a[44:47],v[52:55],v[20:23]
s_mov_b32 s62,s87
v_fma_f32 v68,v68,v91,v90
v_fma_f32 v69,v69,v91,v90
v_mfma_f32_16x16x32_bf16 v[16:19],a[48:51],v[56:59],v[16:19]
buffer_load_dwordx4 v1,s[36:39],s66 offen offset:2048 sc1 lds
v_mfma_f32_16x16x32_bf16 v[20:23],a[52:55],v[56:59],v[20:23]
ds_read_b128 v[52:55],v2 offset:13312
v_fma_f32 v70,v70,v91,v90
v_mfma_f32_16x16x32_bf16 v[16:19],a[56:59],v[60:63],v[16:19]
ds_read_b128 v[56:59],v2 offset:14336
v_fma_f32 v71,v71,v91,v90
v_mfma_f32_16x16x32_bf16 v[20:23],a[60:63],v[60:63],v[20:23]
s_waitcnt lgkmcnt(3)
v_add_f32 v24,v24,v64
v_add_f32 v25,v25,v65
v_mfma_f32_16x16x32_bf16 v[16:19],a[64:67],v[32:35],v[16:19]
s_waitcnt vmcnt(9)
ds_read_b128 v[60:63],v2 offset:15360
v_add_f32 v26,v26,v66
v_mfma_f32_16x16x32_bf16 v[20:23],a[68:71],v[32:35],v[20:23]
s_barrier
v_mfma_f32_16x16x32_bf16 v[16:19],a[72:75],v[36:39],v[16:19]
buffer_load_dwordx4 v1,s[36:39],s66 offen offset:3072 sc1 lds
v_mfma_f32_16x16x32_bf16 v[20:23],a[76:79],v[36:39],v[20:23]
s_add_u32 m0,s71,s88
ds_read_b128 v[32:35],v2 offset:16384
v_add_f32 v27,v27,v67
v_mfma_f32_16x16x32_bf16 v[16:19],a[80:83],v[40:43],v[16:19]
s_lshr_b32 s70,s46,5
ds_read_b128 v[36:39],v2 offset:17408
v_add_f32 v28,v28,v68
v_mfma_f32_16x16x32_bf16 v[20:23],a[84:87],v[40:43],v[20:23]
v_add_f32 v29,v29,v69
v_add_f32 v30,v30,v70
v_mfma_f32_16x16x32_bf16 v[16:19],a[88:91],v[44:47],v[16:19]
buffer_load_dwordx4 v10,s[36:39],s66 offen offset:0 sc1 lds
v_mfma_f32_16x16x32_bf16 v[20:23],a[92:95],v[44:47],v[20:23]
s_waitcnt lgkmcnt(2)
ds_read_b128 v[40:43],v2 offset:18432
v_add_f32 v31,v31,v71
v_mfma_f32_16x16x32_bf16 v[16:19],a[96:99],v[48:51],v[16:19]
buffer_load_dword v11,s[36:39],s72 offen offset:1024 sc1 lds
v_mfma_f32_16x16x32_bf16 v[20:23],a[100:103],v[48:51],v[20:23]
s_mov_b32 m0,s62
ds_read_b128 v[44:47],v2 offset:19456
v_cvt_pk_bf16_f32 v72,v24,v25
v_mfma_f32_16x16x32_bf16 v[16:19],a[104:107],v[52:55],v[16:19]
v_cvt_pk_bf16_f32 v73,v26,v27
v_cvt_pk_bf16_f32 v74,v28,v29
v_cvt_pk_bf16_f32 v75,v30,v31
v_mfma_f32_16x16x32_bf16 v[20:23],a[108:111],v[52:55],v[20:23]
ds_read_b128 v[48:51],v2 offset:20480
buffer_store_dwordx4 v[72:75],v3,s[36:39],s73 offen offset:0 sc1
v_add_f32 v64,v24,v25
v_mul_f32 v65,v24,v24
v_fma_f32 v65,v25,v25,v65
v_mfma_f32_16x16x32_bf16 v[16:19],a[112:115],v[56:59],v[16:19]
ds_read_b128 v[52:55],v2 offset:21504
v_add_f32 v64,v64,v26
v_mfma_f32_16x16x32_bf16 v[20:23],a[116:119],v[56:59],v[20:23]
v_fma_f32 v65,v26,v26,v65
v_add_f32 v64,v64,v27
v_fma_f32 v65,v27,v27,v65
v_add_f32 v64,v64,v28
v_fma_f32 v65,v28,v28,v65
v_add_f32 v64,v64,v29
v_fma_f32 v65,v29,v29,v65
v_add_f32 v64,v64,v30
v_fma_f32 v65,v30,v30,v65
v_add_f32 v64,v64,v31
v_fma_f32 v65,v31,v31,v65
v_mfma_f32_16x16x32_bf16 v[16:19],a[120:123],v[60:63],v[16:19]
s_nop 0
v_permlane16_swap_b32 v64,v65
v_add_f32 v64,v64,v65
v_mov_b32 v65,v64
v_mfma_f32_16x16x32_bf16 v[20:23],a[124:127],v[60:63],v[20:23]
s_waitcnt lgkmcnt(2)
ds_read_b128 v[56:59],v2 offset:22528)"
R"(
v_permlane32_swap_b32 v64,v65
v_add_f32 v64,v64,v65
v_mfma_f32_16x16x32_bf16 v[16:19],a[128:131],v[32:35],v[16:19]
ds_write_b32 v13,v64 offset:0
v_mfma_f32_16x16x32_bf16 v[20:23],a[132:135],v[32:35],v[20:23]
ds_read_b128 v[60:63],v2 offset:23552
v_mfma_f32_16x16x32_bf16 v[16:19],a[136:139],v[36:39],v[16:19]
ds_read_b128 v[32:35],v2 offset:24576
v_mfma_f32_16x16x32_bf16 v[20:23],a[140:143],v[36:39],v[20:23]
v_mfma_f32_16x16x32_bf16 v[16:19],a[144:147],v[40:43],v[16:19]
s_waitcnt vmcnt(12) lgkmcnt(2)
ds_read_b128 v[36:39],v2 offset:25600
v_mfma_f32_16x16x32_bf16 v[20:23],a[148:151],v[40:43],v[20:23]
s_barrier
v_mfma_f32_16x16x32_bf16 v[16:19],a[152:155],v[44:47],v[16:19]
buffer_store_dword v5,v4,s[36:39],s70 offen offset:0 sc1
ds_read_b128 v[40:43],v2 offset:26624
ds_read_b32 v15,v7 offset:0
v_mfma_f32_16x16x32_bf16 v[20:23],a[156:159],v[44:47],v[20:23]
s_waitcnt lgkmcnt(4)
v_mfma_f32_16x16x32_bf16 v[16:19],a[160:163],v[48:51],v[16:19]
ds_read_b128 v[44:47],v2 offset:27648
v_mfma_f32_16x16x32_bf16 v[20:23],a[164:167],v[48:51],v[20:23]
v_mfma_f32_16x16x32_bf16 v[16:19],a[168:171],v[52:55],v[16:19]
s_waitcnt lgkmcnt(1)
ds_read_b128 v[48:51],v2 offset:28672
v_mfma_f32_16x16x32_bf16 v[20:23],a[172:175],v[52:55],v[20:23]
v_cmp_eq_u32 vcc,s60,v15
v_mfma_f32_16x16x32_bf16 v[16:19],a[176:179],v[56:59],v[16:19]
s_cmp_eq_u32 vcc_lo,-1
ds_read_b128 v[52:55],v2 offset:29696
s_cselect_b32 s67,s86,s59
v_mfma_f32_16x16x32_bf16 v[20:23],a[180:183],v[56:59],v[20:23]
s_xor_b32 s63,s63,65536
s_add_u32 s53,s52,s55
s_cmp_eq_u32 s53,s56
v_mfma_f32_16x16x32_bf16 v[16:19],a[184:187],v[60:63],v[16:19]
s_cselect_b32 s53,s57,s53
ds_read_b128 v[56:59],v2 offset:30720
s_cmp_eq_u32 s53,s58
v_mfma_f32_16x16x32_bf16 v[20:23],a[188:191],v[60:63],v[20:23]
s_waitcnt lgkmcnt(3)
buffer_load_dwordx4 v0,s[36:39],s67 offen offset:0 sc1 lds
v_mfma_f32_16x16x32_bf16 v[16:19],a[192:195],v[32:35],v[16:19]
s_waitcnt vmcnt(9)
ds_read_b128 v[60:63],v2 offset:31744
s_cselect_b32 s53,s59,s53
v_mfma_f32_16x16x32_bf16 v[20:23],a[196:199],v[32:35],v[20:23]
s_barrier
v_mfma_f32_16x16x32_bf16 v[16:19],a[200:203],v[36:39],v[16:19]
s_cmp_ge_u32 s66,s59
ds_read_b128 v[32:35],v2 offset:32768
ds_read_b128 v[80:83],v8 offset:0
v_mfma_f32_16x16x32_bf16 v[20:23],a[204:207],v[36:39],v[20:23]
ds_read_b64 v[88:89],v9 offset:1024
v_mfma_f32_16x16x32_bf16 v[16:19],a[208:211],v[40:43],v[16:19]
ds_read_b128 v[36:39],v2 offset:33792
v_mfma_f32_16x16x32_bf16 v[20:23],a[212:215],v[40:43],v[20:23]
buffer_load_dwordx4 v0,s[36:39],s67 offen offset:1024 sc1 lds
v_mfma_f32_16x16x32_bf16 v[16:19],a[216:219],v[44:47],v[16:19]
ds_read_b128 v[40:43],v2 offset:34816
v_mfma_f32_16x16x32_bf16 v[20:23],a[220:223],v[44:47],v[20:23]
s_waitcnt lgkmcnt(5)
v_mfma_f32_16x16x32_bf16 v[16:19],a[224:227],v[48:51],v[16:19]
ds_read_b128 v[44:47],v2 offset:35840
v_mfma_f32_16x16x32_bf16 v[20:23],a[228:231],v[48:51],v[20:23]
s_waitcnt lgkmcnt(3)
buffer_load_dwordx4 v0,s[36:39],s67 offen offset:2048 sc1 lds
v_mfma_f32_16x16x32_bf16 v[16:19],a[232:235],v[52:55],v[16:19]
ds_read_b128 v[48:51],v2 offset:36864
v_mfma_f32_16x16x32_bf16 v[20:23],a[236:239],v[52:55],v[20:23]
v_mfma_f32_16x16x32_bf16 v[16:19],a[240:243],v[56:59],v[16:19]
ds_read_b128 v[52:55],v2 offset:37888
v_mfma_f32_16x16x32_bf16 v[20:23],a[244:247],v[56:59],v[20:23]
buffer_load_dwordx4 v0,s[36:39],s67 offen offset:3072 sc1 lds
v_mfma_f32_16x16x32_bf16 v[16:19],a[248:251],v[60:63],v[16:19]
s_waitcnt lgkmcnt(2)
ds_read_b128 v[56:59],v2 offset:38912
v_mfma_f32_16x16x32_bf16 v[20:23],a[252:255],v[60:63],v[20:23]
s_cbranch_scc1 Lg2_slow1_%=
Lg2_d1w3_%=:
v_mfma_f32_16x16x32_bf16 v[24:27],a[0:3],v[32:35],0
s_add_u32 m0,s62,16384
ds_read_b128 v[60:63],v2 offset:39936
v_lshlrev_b32 v64,16,v80
v_mfma_f32_16x16x32_bf16 v[28:31],a[4:7],v[32:35],0
s_lshr_b32 s73,s48,0
v_and_b32 v65,0xffff0000,v80
v_lshlrev_b32 v66,16,v81
v_mfma_f32_16x16x32_bf16 v[24:27],a[8:11],v[36:39],v[24:27]
s_xor_b32 s87,s63,65536
ds_read_b128 v[32:35],v2 offset:40960
v_and_b32 v67,0xffff0000,v81
v_mfma_f32_16x16x32_bf16 v[28:31],a[12:15],v[36:39],v[28:31]
s_cmp_ge_u32 s67,s59
v_lshlrev_b32 v68,16,v82
v_and_b32 v69,0xffff0000,v82
v_mfma_f32_16x16x32_bf16 v[24:27],a[16:19],v[40:43],v[24:27]
buffer_load_dwordx4 v1,s[36:39],s67 offen offset:0 sc1 lds
v_mfma_f32_16x16x32_bf16 v[28:31],a[20:23],v[40:43],v[28:31]
s_cselect_b32 s86,s50,s51
ds_read_b128 v[36:39],v2 offset:41984
v_lshlrev_b32 v70,16,v83
v_mfma_f32_16x16x32_bf16 v[24:27],a[24:27],v[44:47],v[24:27]
s_cselect_b32 s87,s87,s64
ds_read_b128 v[40:43],v2 offset:43008
v_and_b32 v71,0xffff0000,v83
v_mfma_f32_16x16x32_bf16 v[28:31],a[28:31],v[44:47],v[28:31]
s_waitcnt lgkmcnt(3)
v_fma_f32 v64,v64,v89,v88
v_fma_f32 v65,v65,v89,v88
v_mfma_f32_16x16x32_bf16 v[24:27],a[32:35],v[48:51],v[24:27]
buffer_load_dwordx4 v1,s[36:39],s67 offen offset:1024 sc1 lds
v_mfma_f32_16x16x32_bf16 v[28:31],a[36:39],v[48:51],v[28:31]
s_lshr_b32 s72,s67,6
ds_read_b128 v[44:47],v2 offset:44032
v_fma_f32 v66,v66,v89,v88
v_mfma_f32_16x16x32_bf16 v[24:27],a[40:43],v[52:55],v[24:27]
s_lshr_b32 s71,s62,2
ds_read_b128 v[48:51],v2 offset:45056
v_fma_f32 v67,v67,v89,v88
v_mfma_f32_16x16x32_bf16 v[28:31],a[44:47],v[52:55],v[28:31]
s_mov_b32 s62,s87
v_fma_f32 v68,v68,v89,v88
v_fma_f32 v69,v69,v89,v88
v_mfma_f32_16x16x32_bf16 v[24:27],a[48:51],v[56:59],v[24:27]
buffer_load_dwordx4 v1,s[36:39],s67 offen offset:2048 sc1 lds
v_mfma_f32_16x16x32_bf16 v[28:31],a[52:55],v[56:59],v[28:31]
s_mov_b64 s[44:45],s[46:47]
ds_read_b128 v[52:55],v2 offset:46080
v_fma_f32 v70,v70,v89,v88
v_mfma_f32_16x16x32_bf16 v[24:27],a[56:59],v[60:63],v[24:27]
ds_read_b128 v[56:59],v2 offset:47104
v_fma_f32 v71,v71,v89,v88
v_mfma_f32_16x16x32_bf16 v[28:31],a[60:63],v[60:63],v[28:31]
s_waitcnt lgkmcnt(3)
v_add_f32 v16,v16,v64
v_add_f32 v17,v17,v65
v_mfma_f32_16x16x32_bf16 v[24:27],a[64:67],v[32:35],v[24:27]
s_waitcnt vmcnt(9)
ds_read_b128 v[60:63],v2 offset:48128
v_add_f32 v18,v18,v66
v_mfma_f32_16x16x32_bf16 v[28:31],a[68:71],v[32:35],v[28:31]
s_barrier
v_mfma_f32_16x16x32_bf16 v[24:27],a[72:75],v[36:39],v[24:27]
buffer_load_dwordx4 v1,s[36:39],s67 offen offset:3072 sc1 lds
v_mfma_f32_16x16x32_bf16 v[28:31],a[76:79],v[36:39],v[28:31]
s_add_u32 m0,s71,s88
ds_read_b128 v[32:35],v2 offset:49152
v_add_f32 v19,v19,v67
v_mfma_f32_16x16x32_bf16 v[24:27],a[80:83],v[40:43],v[24:27]
s_lshr_b32 s70,s47,5
ds_read_b128 v[36:39],v2 offset:50176
v_add_f32 v20,v20,v68
v_mfma_f32_16x16x32_bf16 v[28:31],a[84:87],v[40:43],v[28:31]
s_mov_b64 s[46:47],s[48:49]
v_add_f32 v21,v21,v69
v_add_f32 v22,v22,v70
v_mfma_f32_16x16x32_bf16 v[24:27],a[88:91],v[44:47],v[24:27]
buffer_load_dwordx4 v10,s[36:39],s67 offen offset:0 sc1 lds
v_mfma_f32_16x16x32_bf16 v[28:31],a[92:95],v[44:47],v[28:31]
s_waitcnt lgkmcnt(2)
ds_read_b128 v[40:43],v2 offset:51200
v_add_f32 v23,v23,v71
v_mfma_f32_16x16x32_bf16 v[24:27],a[96:99],v[48:51],v[24:27]
buffer_load_dword v11,s[36:39],s72 offen offset:1024 sc1 lds
v_mfma_f32_16x16x32_bf16 v[28:31],a[100:103],v[48:51],v[28:31]
s_mov_b32 m0,s62
ds_read_b128 v[44:47],v2 offset:52224
v_cvt_pk_bf16_f32 v72,v16,v17
v_mfma_f32_16x16x32_bf16 v[24:27],a[104:107],v[52:55],v[24:27]
s_mov_b64 s[48:49],s[50:51]
v_cvt_pk_bf16_f32 v73,v18,v19
v_cvt_pk_bf16_f32 v74,v20,v21
v_cvt_pk_bf16_f32 v75,v22,v23
v_mfma_f32_16x16x32_bf16 v[28:31],a[108:111],v[52:55],v[28:31]
s_mov_b64 s[50:51],s[52:53]
ds_read_b128 v[48:51],v2 offset:53248
buffer_store_dwordx4 v[72:75],v3,s[36:39],s73 offen offset:0 sc1
v_add_f32 v64,v16,v17
v_mul_f32 v65,v16,v16
v_fma_f32 v65,v17,v17,v65
v_mfma_f32_16x16x32_bf16 v[24:27],a[112:115],v[56:59],v[24:27]
ds_read_b128 v[52:55],v2 offset:54272
v_add_f32 v64,v64,v18
v_mfma_f32_16x16x32_bf16 v[28:31],a[116:119],v[56:59],v[28:31]
v_fma_f32 v65,v18,v18,v65
v_add_f32 v64,v64,v19
v_fma_f32 v65,v19,v19,v65
v_add_f32 v64,v64,v20
v_fma_f32 v65,v20,v20,v65
v_add_f32 v64,v64,v21
v_fma_f32 v65,v21,v21,v65
v_add_f32 v64,v64,v22
v_fma_f32 v65,v22,v22,v65
v_add_f32 v64,v64,v23
v_fma_f32 v65,v23,v23,v65
v_mfma_f32_16x16x32_bf16 v[24:27],a[120:123],v[60:63],v[24:27]
s_nop 0
v_permlane16_swap_b32 v64,v65)"
R"(
v_add_f32 v64,v64,v65
v_mov_b32 v65,v64
v_mfma_f32_16x16x32_bf16 v[28:31],a[124:127],v[60:63],v[28:31]
s_waitcnt lgkmcnt(2)
ds_read_b128 v[56:59],v2 offset:55296
v_permlane32_swap_b32 v64,v65
v_add_f32 v64,v64,v65
v_mfma_f32_16x16x32_bf16 v[24:27],a[128:131],v[32:35],v[24:27]
ds_write_b32 v13,v64 offset:2048
v_mfma_f32_16x16x32_bf16 v[28:31],a[132:135],v[32:35],v[28:31]
ds_read_b128 v[60:63],v2 offset:56320
v_mfma_f32_16x16x32_bf16 v[24:27],a[136:139],v[36:39],v[24:27]
ds_read_b128 v[32:35],v2 offset:57344
v_mfma_f32_16x16x32_bf16 v[28:31],a[140:143],v[36:39],v[28:31]
v_mfma_f32_16x16x32_bf16 v[24:27],a[144:147],v[40:43],v[24:27]
s_waitcnt vmcnt(12) lgkmcnt(2)
ds_read_b128 v[36:39],v2 offset:58368
v_mfma_f32_16x16x32_bf16 v[28:31],a[148:151],v[40:43],v[28:31]
s_barrier
v_mfma_f32_16x16x32_bf16 v[24:27],a[152:155],v[44:47],v[24:27]
buffer_store_dword v5,v4,s[36:39],s70 offen offset:0 sc1
ds_read_b128 v[40:43],v2 offset:59392
ds_read_b32 v15,v7 offset:256
v_mfma_f32_16x16x32_bf16 v[28:31],a[156:159],v[44:47],v[28:31]
s_waitcnt lgkmcnt(4)
v_mfma_f32_16x16x32_bf16 v[24:27],a[160:163],v[48:51],v[24:27]
ds_read_b128 v[44:47],v2 offset:60416
v_mfma_f32_16x16x32_bf16 v[28:31],a[164:167],v[48:51],v[28:31]
v_mfma_f32_16x16x32_bf16 v[24:27],a[168:171],v[52:55],v[24:27]
s_waitcnt lgkmcnt(1)
ds_read_b128 v[48:51],v2 offset:61440
v_mfma_f32_16x16x32_bf16 v[28:31],a[172:175],v[52:55],v[28:31]
v_cmp_eq_u32 vcc,s60,v15
v_mfma_f32_16x16x32_bf16 v[24:27],a[176:179],v[56:59],v[24:27]
s_cmp_eq_u32 vcc_lo,-1
ds_read_b128 v[52:55],v2 offset:62464
s_cselect_b32 s66,s86,s59
v_mfma_f32_16x16x32_bf16 v[28:31],a[180:183],v[56:59],v[28:31]
s_xor_b32 s64,s64,65536
s_add_u32 s74,s53,s55
s_cmp_eq_u32 s74,s56
v_mfma_f32_16x16x32_bf16 v[24:27],a[184:187],v[60:63],v[24:27]
s_cselect_b32 s74,s57,s74
ds_read_b128 v[56:59],v2 offset:63488
s_cmp_eq_u32 s74,s58
v_mfma_f32_16x16x32_bf16 v[28:31],a[188:191],v[60:63],v[28:31]
s_waitcnt lgkmcnt(3)
buffer_load_dwordx4 v0,s[36:39],s66 offen offset:0 sc1 lds
v_mfma_f32_16x16x32_bf16 v[24:27],a[192:195],v[32:35],v[24:27]
s_waitcnt vmcnt(9)
ds_read_b128 v[60:63],v2 offset:64512
v_xor_b32 v2,65536,v2
v_mfma_f32_16x16x32_bf16 v[28:31],a[196:199],v[32:35],v[28:31]
s_barrier
v_mfma_f32_16x16x32_bf16 v[24:27],a[200:203],v[36:39],v[24:27]
s_cselect_b32 s74,s59,s74
ds_read_b128 v[32:35],v2 offset:0
ds_read_b128 v[84:87],v8 offset:8192
v_mfma_f32_16x16x32_bf16 v[28:31],a[204:207],v[36:39],v[28:31]
s_mov_b32 s52,s74
ds_read_b64 v[90:91],v9 offset:9216
v_xor_b32 v8,16384,v8
v_mfma_f32_16x16x32_bf16 v[24:27],a[208:211],v[40:43],v[24:27]
s_cmp_lt_u32 s67,s59
ds_read_b128 v[36:39],v2 offset:1024
v_xor_b32 v9,16384,v9
v_mfma_f32_16x16x32_bf16 v[28:31],a[212:215],v[40:43],v[28:31]
buffer_load_dwordx4 v0,s[36:39],s66 offen offset:1024 sc1 lds
v_mfma_f32_16x16x32_bf16 v[24:27],a[216:219],v[44:47],v[24:27]
ds_read_b128 v[40:43],v2 offset:2048
v_mfma_f32_16x16x32_bf16 v[28:31],a[220:223],v[44:47],v[28:31]
s_waitcnt lgkmcnt(5)
v_mfma_f32_16x16x32_bf16 v[24:27],a[224:227],v[48:51],v[24:27]
ds_read_b128 v[44:47],v2 offset:3072
v_mfma_f32_16x16x32_bf16 v[28:31],a[228:231],v[48:51],v[28:31]
s_waitcnt lgkmcnt(3)
buffer_load_dwordx4 v0,s[36:39],s66 offen offset:2048 sc1 lds
v_mfma_f32_16x16x32_bf16 v[24:27],a[232:235],v[52:55],v[24:27]
ds_read_b128 v[48:51],v2 offset:4096
v_mfma_f32_16x16x32_bf16 v[28:31],a[236:239],v[52:55],v[28:31]
v_mfma_f32_16x16x32_bf16 v[24:27],a[240:243],v[56:59],v[24:27]
ds_read_b128 v[52:55],v2 offset:5120
v_mfma_f32_16x16x32_bf16 v[28:31],a[244:247],v[56:59],v[28:31]
buffer_load_dwordx4 v0,s[36:39],s66 offen offset:3072 sc1 lds
v_mfma_f32_16x16x32_bf16 v[24:27],a[248:251],v[60:63],v[24:27]
s_waitcnt lgkmcnt(2)
ds_read_b128 v[56:59],v2 offset:6144
v_mfma_f32_16x16x32_bf16 v[28:31],a[252:255],v[60:63],v[28:31]
s_cbranch_scc1 Lg2_d0w3_%=
s_branch Lg2_slow0_%=
Lg2_slow0_%=:
s_cmp_lt_u32 s66,s59
s_cbranch_scc1 Lg2_gs0_%=
Lg2_slowb0_%=:
s_cmp_eq_u32 s54,1
s_cselect_b32 s68,s49,s48
s_lshr_b32 s68,s68,5
s_add_u32 m0,s78,8192
s_nop 0
buffer_load_dword v6,s[36:39],s68 offen offset:0 sc1 lds
s_lshr_b32 s73,s47,0
s_lshr_b32 s85,s47,5
v_lshlrev_b32 v64,16,v84
v_and_b32 v65,0xffff0000,v84
v_lshlrev_b32 v66,16,v85
v_and_b32 v67,0xffff0000,v85
v_lshlrev_b32 v68,16,v86
v_and_b32 v69,0xffff0000,v86
v_lshlrev_b32 v70,16,v87
v_and_b32 v71,0xffff0000,v87
v_fma_f32 v64,v64,v91,v90
v_fma_f32 v65,v65,v91,v90
v_fma_f32 v66,v66,v91,v90
v_fma_f32 v67,v67,v91,v90
v_fma_f32 v68,v68,v91,v90
v_fma_f32 v69,v69,v91,v90
v_fma_f32 v70,v70,v91,v90
v_fma_f32 v71,v71,v91,v90
v_add_f32 v24,v24,v64
v_add_f32 v25,v25,v65
v_add_f32 v26,v26,v66
v_add_f32 v27,v27,v67
v_add_f32 v28,v28,v68
v_add_f32 v29,v29,v69
v_add_f32 v30,v30,v70
v_add_f32 v31,v31,v71
v_cvt_pk_bf16_f32 v72,v24,v25
v_cvt_pk_bf16_f32 v73,v26,v27
v_cvt_pk_bf16_f32 v74,v28,v29
v_cvt_pk_bf16_f32 v75,v30,v31
s_add_u32 m0,s78,8448
s_nop 0
buffer_load_dword v6,s[36:39],s68 offen offset:0 sc1 lds
buffer_store_dwordx4 v[72:75],v3,s[36:39],s73 offen offset:0 sc1
v_add_f32 v64,v24,v25
v_mul_f32 v65,v24,v24
v_fma_f32 v65,v25,v25,v65
v_add_f32 v64,v64,v26
v_fma_f32 v65,v26,v26,v65
v_add_f32 v64,v64,v27
v_fma_f32 v65,v27,v27,v65
v_add_f32 v64,v64,v28
v_fma_f32 v65,v28,v28,v65
v_add_f32 v64,v64,v29
v_fma_f32 v65,v29,v29,v65
v_add_f32 v64,v64,v30
v_fma_f32 v65,v30,v30,v65
v_add_f32 v64,v64,v31
v_fma_f32 v65,v31,v31,v65
s_nop 1
v_permlane16_swap_b32 v64,v65
v_add_f32 v64,v64,v65
v_mov_b32 v65,v64
s_nop 1
v_permlane32_swap_b32 v64,v65
v_add_f32 v64,v64,v65
ds_write_b32 v13,v64 offset:0
s_waitcnt lgkmcnt(0)
s_barrier
ds_read_b128 v[76:79],v14 offset:0
s_waitcnt lgkmcnt(0)
v_add_f32 v76,v76,v77
v_add_f32 v78,v78,v79
v_add_f32 v76,v76,v78
buffer_store_dword v76,v12,s[36:39],s85 offen offset:0 sc1
s_cmp_ge_u32 s48,s59
s_cbranch_scc1 Lg2_done0_%=
s_add_u32 m0,s78,16384
s_nop 0
buffer_load_dword v6,s[36:39],s68 offen offset:0 sc1 lds
s_waitcnt vmcnt(4)
s_barrier
ds_read_b32 v15,v7 offset:8192
s_lshr_b32 s70,s46,5
buffer_store_dword v5,v4,s[36:39],s70 offen offset:0 sc1
s_mov_b32 s46,s59
s_mov_b32 s69,18432
s_waitcnt lgkmcnt(0)
v_cmp_eq_u32 vcc,s60,v15
s_nop 0
s_cmp_eq_u32 vcc_lo,-1
s_cbranch_scc1 Lg2_go0_%=
s_add_u32 m0,s78,16640
s_nop 0
buffer_load_dword v6,s[36:39],s68 offen offset:0 sc1 lds
s_waitcnt vmcnt(5)
s_barrier
ds_read_b32 v15,v7 offset:8448
s_mov_b32 s69,18688
s_waitcnt lgkmcnt(0)
v_cmp_eq_u32 vcc,s60,v15
s_nop 0
s_cmp_eq_u32 vcc_lo,-1
s_cbranch_scc1 Lg2_go0_%=
s_add_u32 m0,s78,8192
s_nop 0
buffer_load_dword v6,s[36:39],s68 offen offset:0 sc1 lds
s_waitcnt vmcnt(3)
s_barrier
ds_read_b32 v15,v7 offset:16384)"
R"(
s_lshr_b32 s70,s47,5
buffer_store_dword v5,v4,s[36:39],s70 offen offset:0 sc1
s_mov_b32 s47,s59
s_mov_b32 s69,10240
s_waitcnt lgkmcnt(0)
v_cmp_eq_u32 vcc,s60,v15
s_nop 0
s_cmp_eq_u32 vcc_lo,-1
s_cbranch_scc1 Lg2_go0_%=
s_add_u32 m0,s78,8448
s_nop 0
buffer_load_dword v6,s[36:39],s68 offen offset:0 sc1 lds
s_waitcnt vmcnt(3)
s_barrier
ds_read_b32 v15,v7 offset:16640
s_mov_b32 s69,10496
s_waitcnt lgkmcnt(0)
v_cmp_eq_u32 vcc,s60,v15
s_nop 0
s_cmp_eq_u32 vcc_lo,-1
s_cbranch_scc1 Lg2_go0_%=
Lg2_poll0_%=:
s_add_u32 m0,s78,16384
s_nop 0
buffer_load_dword v6,s[36:39],s68 offen offset:0 sc1 lds
s_waitcnt vmcnt(2)
s_barrier
ds_read_b32 v15,v7 offset:8192
s_mov_b32 s69,18432
s_waitcnt lgkmcnt(0)
v_cmp_eq_u32 vcc,s60,v15
s_nop 0
s_cmp_eq_u32 vcc_lo,-1
s_cbranch_scc1 Lg2_go0_%=
s_sleep 4
s_add_u32 m0,s78,16640
s_nop 0
buffer_load_dword v6,s[36:39],s68 offen offset:0 sc1 lds
s_waitcnt vmcnt(2)
s_barrier
ds_read_b32 v15,v7 offset:8448
s_mov_b32 s69,18688
s_waitcnt lgkmcnt(0)
v_cmp_eq_u32 vcc,s60,v15
s_nop 0
s_cmp_eq_u32 vcc_lo,-1
s_cbranch_scc1 Lg2_go0_%=
s_sleep 4
s_add_u32 m0,s78,8192
s_nop 0
buffer_load_dword v6,s[36:39],s68 offen offset:0 sc1 lds
s_waitcnt vmcnt(2)
s_barrier
ds_read_b32 v15,v7 offset:16384
s_mov_b32 s69,10240
s_waitcnt lgkmcnt(0)
v_cmp_eq_u32 vcc,s60,v15
s_nop 0
s_cmp_eq_u32 vcc_lo,-1
s_cbranch_scc1 Lg2_go0_%=
s_sleep 4
s_add_u32 m0,s78,8448
s_nop 0
buffer_load_dword v6,s[36:39],s68 offen offset:0 sc1 lds
s_waitcnt vmcnt(2)
s_barrier
ds_read_b32 v15,v7 offset:16640
s_mov_b32 s69,10496
s_waitcnt lgkmcnt(0)
v_cmp_eq_u32 vcc,s60,v15
s_nop 0
s_cmp_eq_u32 vcc_lo,-1
s_cbranch_scc1 Lg2_go0_%=
s_sleep 4
s_add_u32 s79,s79,1
s_cmp_lt_u32 s79,20000
s_cbranch_scc1 Lg2_poll0_%=
Lg2_go0_%=:
s_xor_b32 s62,s63,65536
s_mov_b32 m0,s62
s_nop 0
buffer_load_dwordx4 v0,s[36:39],s48 offen offset:0 sc1 lds
buffer_load_dwordx4 v0,s[36:39],s48 offen offset:1024 sc1 lds
buffer_load_dwordx4 v0,s[36:39],s48 offen offset:2048 sc1 lds
buffer_load_dwordx4 v0,s[36:39],s48 offen offset:3072 sc1 lds
s_add_u32 m0,s62,16384
s_nop 0
buffer_load_dwordx4 v1,s[36:39],s48 offen offset:0 sc1 lds
buffer_load_dwordx4 v1,s[36:39],s48 offen offset:1024 sc1 lds
buffer_load_dwordx4 v1,s[36:39],s48 offen offset:2048 sc1 lds
buffer_load_dwordx4 v1,s[36:39],s48 offen offset:3072 sc1 lds
s_lshr_b32 s72,s48,6
s_lshr_b32 s71,s62,2
s_add_u32 m0,s71,s88
s_nop 0
buffer_load_dwordx4 v10,s[36:39],s48 offen offset:0 sc1 lds
buffer_load_dword v11,s[36:39],s72 offen offset:1024 sc1 lds
s_cmp_eq_u32 vcc_hi,-1
s_cselect_b32 s66,s49,s59
s_xor_b32 s62,s64,65536
s_cmp_ge_u32 s66,s59
s_cselect_b32 s68,s49,s50
s_lshr_b32 s68,s68,5
s_add_u32 m0,s78,0
s_nop 0
buffer_load_dword v6,s[36:39],s68 offen offset:0 sc1 lds
s_mov_b32 m0,s62
s_nop 0
buffer_load_dwordx4 v0,s[36:39],s66 offen offset:0 sc1 lds
buffer_load_dwordx4 v0,s[36:39],s66 offen offset:1024 sc1 lds
buffer_load_dwordx4 v0,s[36:39],s66 offen offset:2048 sc1 lds
buffer_load_dwordx4 v0,s[36:39],s66 offen offset:3072 sc1 lds
s_waitcnt vmcnt(11)
s_barrier
ds_read_b128 v[32:35],v2 offset:0
ds_read_b128 v[36:39],v2 offset:1024
ds_read_b128 v[40:43],v2 offset:2048
ds_read_b128 v[44:47],v2 offset:3072
ds_read_b128 v[48:51],v2 offset:4096
ds_read_b128 v[52:55],v2 offset:5120
ds_read_b128 v[56:59],v2 offset:6144
s_lshr_b32 s70,s46,5
buffer_store_dword v5,v4,s[36:39],s70 offen offset:0 sc1
s_mov_b32 s46,s59
s_lshr_b32 s70,s47,5
buffer_store_dword v5,v4,s[36:39],s70 offen offset:0 sc1
s_mov_b32 s47,s59
s_lshr_b32 s70,s59,5
buffer_store_dword v5,v4,s[36:39],s70 offen offset:0 sc1
s_waitcnt lgkmcnt(3)
s_setpc_b64 s[90:91]
Lg2_gs0_%=:
s_cmp_eq_u32 s54,1
s_cselect_b32 s68,s49,s48
s_lshr_b32 s68,s68,5
s_lshr_b32 s73,s47,0
s_lshr_b32 s85,s47,5
s_add_u32 m0,s78,8192
s_nop 0
buffer_load_dword v6,s[36:39],s68 offen offset:0 sc1 lds
v_lshlrev_b32 v64,16,v84
v_and_b32 v65,0xffff0000,v84
v_lshlrev_b32 v66,16,v85
v_and_b32 v67,0xffff0000,v85
v_lshlrev_b32 v68,16,v86
v_and_b32 v69,0xffff0000,v86
v_lshlrev_b32 v70,16,v87
v_and_b32 v71,0xffff0000,v87
s_add_u32 m0,s62,16384
s_nop 0
buffer_load_dwordx4 v1,s[36:39],s66 offen offset:0 sc1 lds
v_fma_f32 v64,v64,v91,v90
v_fma_f32 v65,v65,v91,v90
v_fma_f32 v66,v66,v91,v90
v_fma_f32 v67,v67,v91,v90
v_fma_f32 v68,v68,v91,v90
v_fma_f32 v69,v69,v91,v90
v_fma_f32 v70,v70,v91,v90
v_fma_f32 v71,v71,v91,v90
buffer_load_dwordx4 v1,s[36:39],s66 offen offset:1024 sc1 lds
v_add_f32 v24,v24,v64
v_add_f32 v25,v25,v65
v_add_f32 v26,v26,v66
v_add_f32 v27,v27,v67
v_add_f32 v28,v28,v68
v_add_f32 v29,v29,v69
v_add_f32 v30,v30,v70
v_add_f32 v31,v31,v71
buffer_load_dwordx4 v1,s[36:39],s66 offen offset:2048 sc1 lds
v_cvt_pk_bf16_f32 v72,v24,v25
v_cvt_pk_bf16_f32 v73,v26,v27
v_cvt_pk_bf16_f32 v74,v28,v29
v_cvt_pk_bf16_f32 v75,v30,v31
buffer_store_dwordx4 v[72:75],v3,s[36:39],s73 offen offset:0 sc1
v_add_f32 v64,v24,v25
v_mul_f32 v65,v24,v24
v_fma_f32 v65,v25,v25,v65
buffer_load_dwordx4 v1,s[36:39],s66 offen offset:3072 sc1 lds
v_add_f32 v64,v64,v26
v_fma_f32 v65,v26,v26,v65
v_add_f32 v64,v64,v27
v_fma_f32 v65,v27,v27,v65
v_add_f32 v64,v64,v28
v_fma_f32 v65,v28,v28,v65
v_add_f32 v64,v64,v29
v_fma_f32 v65,v29,v29,v65
s_lshr_b32 s72,s66,6
s_lshr_b32 s71,s62,2
s_add_u32 m0,s71,s88
s_nop 0
buffer_load_dwordx4 v10,s[36:39],s66 offen offset:0 sc1 lds
v_add_f32 v64,v64,v30
v_fma_f32 v65,v30,v30,v65
v_add_f32 v64,v64,v31
v_fma_f32 v65,v31,v31,v65
s_nop 1
v_permlane16_swap_b32 v64,v65
v_add_f32 v64,v64,v65
v_mov_b32 v65,v64
s_nop 1)"
R"(
v_permlane32_swap_b32 v64,v65
buffer_load_dword v11,s[36:39],s72 offen offset:1024 sc1 lds
v_add_f32 v64,v64,v65
ds_write_b32 v13,v64 offset:0
s_waitcnt lgkmcnt(0)
s_barrier
ds_read_b128 v[76:79],v14 offset:0
s_waitcnt lgkmcnt(0)
v_add_f32 v76,v76,v77
v_add_f32 v78,v78,v79
v_add_f32 v76,v76,v78
buffer_store_dword v76,v12,s[36:39],s85 offen offset:0 sc1
s_waitcnt vmcnt(8)
s_barrier
ds_read_b32 v15,v7 offset:8192
ds_read_b128 v[32:35],v2 offset:0
ds_read_b128 v[36:39],v2 offset:1024
ds_read_b128 v[40:43],v2 offset:2048
ds_read_b128 v[44:47],v2 offset:3072
ds_read_b128 v[48:51],v2 offset:4096
ds_read_b128 v[52:55],v2 offset:5120
ds_read_b128 v[56:59],v2 offset:6144
s_lshr_b32 s70,s46,5
buffer_store_dword v5,v4,s[36:39],s70 offen offset:0 sc1
s_waitcnt lgkmcnt(7)
v_cmp_eq_u32 vcc,s60,v15
s_nop 0
s_cmp_eq_u32 vcc_hi,-1
s_cselect_b32 s66,s49,s59
s_xor_b32 s62,s64,65536
s_cmp_ge_u32 s66,s59
s_cselect_b32 s68,s49,s50
s_lshr_b32 s68,s68,5
s_add_u32 m0,s78,0
s_nop 0
buffer_load_dword v6,s[36:39],s68 offen offset:0 sc1 lds
s_mov_b32 m0,s62
s_nop 0
buffer_load_dwordx4 v0,s[36:39],s66 offen offset:0 sc1 lds
buffer_load_dwordx4 v0,s[36:39],s66 offen offset:1024 sc1 lds
buffer_load_dwordx4 v0,s[36:39],s66 offen offset:2048 sc1 lds
buffer_load_dwordx4 v0,s[36:39],s66 offen offset:3072 sc1 lds
s_mov_b32 s46,s47
s_mov_b32 s47,s59
s_lshr_b32 s70,s59,5
buffer_store_dword v5,v4,s[36:39],s70 offen offset:0 sc1
s_lshr_b32 s70,s59,5
buffer_store_dword v5,v4,s[36:39],s70 offen offset:0 sc1
s_waitcnt lgkmcnt(3)
s_setpc_b64 s[90:91]
Lg2_done0_%=:
s_waitcnt vmcnt(0)
s_barrier
s_lshr_b32 s70,s46,5
buffer_store_dword v5,v4,s[36:39],s70 offen offset:0 sc1
s_lshr_b32 s70,s47,5
buffer_store_dword v5,v4,s[36:39],s70 offen offset:0 sc1
s_branch Lg2_done_%=
Lg2_slow1_%=:
s_cmp_lt_u32 s67,s59
s_cbranch_scc1 Lg2_gs1_%=
Lg2_slowb1_%=:
s_cmp_eq_u32 s54,1
s_cselect_b32 s68,s50,s49
s_lshr_b32 s68,s68,5
s_add_u32 m0,s78,8192
s_nop 0
buffer_load_dword v6,s[36:39],s68 offen offset:0 sc1 lds
s_lshr_b32 s73,s48,0
s_lshr_b32 s85,s48,5
v_lshlrev_b32 v64,16,v80
v_and_b32 v65,0xffff0000,v80
v_lshlrev_b32 v66,16,v81
v_and_b32 v67,0xffff0000,v81
v_lshlrev_b32 v68,16,v82
v_and_b32 v69,0xffff0000,v82
v_lshlrev_b32 v70,16,v83
v_and_b32 v71,0xffff0000,v83
v_fma_f32 v64,v64,v89,v88
v_fma_f32 v65,v65,v89,v88
v_fma_f32 v66,v66,v89,v88
v_fma_f32 v67,v67,v89,v88
v_fma_f32 v68,v68,v89,v88
v_fma_f32 v69,v69,v89,v88
v_fma_f32 v70,v70,v89,v88
v_fma_f32 v71,v71,v89,v88
v_add_f32 v16,v16,v64
v_add_f32 v17,v17,v65
v_add_f32 v18,v18,v66
v_add_f32 v19,v19,v67
v_add_f32 v20,v20,v68
v_add_f32 v21,v21,v69
v_add_f32 v22,v22,v70
v_add_f32 v23,v23,v71
v_cvt_pk_bf16_f32 v72,v16,v17
v_cvt_pk_bf16_f32 v73,v18,v19
v_cvt_pk_bf16_f32 v74,v20,v21
v_cvt_pk_bf16_f32 v75,v22,v23
s_add_u32 m0,s78,8448
s_nop 0
buffer_load_dword v6,s[36:39],s68 offen offset:0 sc1 lds
buffer_store_dwordx4 v[72:75],v3,s[36:39],s73 offen offset:0 sc1
v_add_f32 v64,v16,v17
v_mul_f32 v65,v16,v16
v_fma_f32 v65,v17,v17,v65
v_add_f32 v64,v64,v18
v_fma_f32 v65,v18,v18,v65
v_add_f32 v64,v64,v19
v_fma_f32 v65,v19,v19,v65
v_add_f32 v64,v64,v20
v_fma_f32 v65,v20,v20,v65
v_add_f32 v64,v64,v21
v_fma_f32 v65,v21,v21,v65
v_add_f32 v64,v64,v22
v_fma_f32 v65,v22,v22,v65
v_add_f32 v64,v64,v23
v_fma_f32 v65,v23,v23,v65
s_nop 1
v_permlane16_swap_b32 v64,v65
v_add_f32 v64,v64,v65
v_mov_b32 v65,v64
s_nop 1
v_permlane32_swap_b32 v64,v65
v_add_f32 v64,v64,v65
ds_write_b32 v13,v64 offset:2048
s_waitcnt lgkmcnt(0)
s_barrier
ds_read_b128 v[76:79],v14 offset:2048
s_waitcnt lgkmcnt(0)
v_add_f32 v76,v76,v77
v_add_f32 v78,v78,v79
v_add_f32 v76,v76,v78
buffer_store_dword v76,v12,s[36:39],s85 offen offset:0 sc1
s_cmp_ge_u32 s49,s59
s_cbranch_scc1 Lg2_done1_%=
s_add_u32 m0,s78,16384
s_nop 0
buffer_load_dword v6,s[36:39],s68 offen offset:0 sc1 lds
s_waitcnt vmcnt(4)
s_barrier
ds_read_b32 v15,v7 offset:8192
s_lshr_b32 s70,s47,5
buffer_store_dword v5,v4,s[36:39],s70 offen offset:0 sc1
s_mov_b32 s47,s59
s_mov_b32 s69,18432
s_waitcnt lgkmcnt(0)
v_cmp_eq_u32 vcc,s60,v15
s_nop 0
s_cmp_eq_u32 vcc_lo,-1
s_cbranch_scc1 Lg2_go1_%=
s_add_u32 m0,s78,16640
s_nop 0
buffer_load_dword v6,s[36:39],s68 offen offset:0 sc1 lds
s_waitcnt vmcnt(5)
s_barrier
ds_read_b32 v15,v7 offset:8448
s_mov_b32 s69,18688
s_waitcnt lgkmcnt(0)
v_cmp_eq_u32 vcc,s60,v15
s_nop 0
s_cmp_eq_u32 vcc_lo,-1
s_cbranch_scc1 Lg2_go1_%=
s_add_u32 m0,s78,8192
s_nop 0
buffer_load_dword v6,s[36:39],s68 offen offset:0 sc1 lds
s_waitcnt vmcnt(3)
s_barrier
ds_read_b32 v15,v7 offset:16384
s_lshr_b32 s70,s48,5
buffer_store_dword v5,v4,s[36:39],s70 offen offset:0 sc1
s_mov_b32 s48,s59
s_mov_b32 s69,10240
s_waitcnt lgkmcnt(0)
v_cmp_eq_u32 vcc,s60,v15
s_nop 0
s_cmp_eq_u32 vcc_lo,-1
s_cbranch_scc1 Lg2_go1_%=
s_add_u32 m0,s78,8448
s_nop 0
buffer_load_dword v6,s[36:39],s68 offen offset:0 sc1 lds
s_waitcnt vmcnt(3)
s_barrier
ds_read_b32 v15,v7 offset:16640
s_mov_b32 s69,10496
s_waitcnt lgkmcnt(0)
v_cmp_eq_u32 vcc,s60,v15
s_nop 0
s_cmp_eq_u32 vcc_lo,-1
s_cbranch_scc1 Lg2_go1_%=
Lg2_poll1_%=:
s_add_u32 m0,s78,16384
s_nop 0
buffer_load_dword v6,s[36:39],s68 offen offset:0 sc1 lds
s_waitcnt vmcnt(2)
s_barrier
ds_read_b32 v15,v7 offset:8192
s_mov_b32 s69,18432
s_waitcnt lgkmcnt(0)
v_cmp_eq_u32 vcc,s60,v15
s_nop 0)"
R"(
s_cmp_eq_u32 vcc_lo,-1
s_cbranch_scc1 Lg2_go1_%=
s_sleep 4
s_add_u32 m0,s78,16640
s_nop 0
buffer_load_dword v6,s[36:39],s68 offen offset:0 sc1 lds
s_waitcnt vmcnt(2)
s_barrier
ds_read_b32 v15,v7 offset:8448
s_mov_b32 s69,18688
s_waitcnt lgkmcnt(0)
v_cmp_eq_u32 vcc,s60,v15
s_nop 0
s_cmp_eq_u32 vcc_lo,-1
s_cbranch_scc1 Lg2_go1_%=
s_sleep 4
s_add_u32 m0,s78,8192
s_nop 0
buffer_load_dword v6,s[36:39],s68 offen offset:0 sc1 lds
s_waitcnt vmcnt(2)
s_barrier
ds_read_b32 v15,v7 offset:16384
s_mov_b32 s69,10240
s_waitcnt lgkmcnt(0)
v_cmp_eq_u32 vcc,s60,v15
s_nop 0
s_cmp_eq_u32 vcc_lo,-1
s_cbranch_scc1 Lg2_go1_%=
s_sleep 4
s_add_u32 m0,s78,8448
s_nop 0
buffer_load_dword v6,s[36:39],s68 offen offset:0 sc1 lds
s_waitcnt vmcnt(2)
s_barrier
ds_read_b32 v15,v7 offset:16640
s_mov_b32 s69,10496
s_waitcnt lgkmcnt(0)
v_cmp_eq_u32 vcc,s60,v15
s_nop 0
s_cmp_eq_u32 vcc_lo,-1
s_cbranch_scc1 Lg2_go1_%=
s_sleep 4
s_add_u32 s79,s79,1
s_cmp_lt_u32 s79,20000
s_cbranch_scc1 Lg2_poll1_%=
Lg2_go1_%=:
s_xor_b32 s62,s64,65536
s_mov_b32 m0,s62
s_nop 0
buffer_load_dwordx4 v0,s[36:39],s49 offen offset:0 sc1 lds
buffer_load_dwordx4 v0,s[36:39],s49 offen offset:1024 sc1 lds
buffer_load_dwordx4 v0,s[36:39],s49 offen offset:2048 sc1 lds
buffer_load_dwordx4 v0,s[36:39],s49 offen offset:3072 sc1 lds
s_add_u32 m0,s62,16384
s_nop 0
buffer_load_dwordx4 v1,s[36:39],s49 offen offset:0 sc1 lds
buffer_load_dwordx4 v1,s[36:39],s49 offen offset:1024 sc1 lds
buffer_load_dwordx4 v1,s[36:39],s49 offen offset:2048 sc1 lds
buffer_load_dwordx4 v1,s[36:39],s49 offen offset:3072 sc1 lds
s_lshr_b32 s72,s49,6
s_lshr_b32 s71,s62,2
s_add_u32 m0,s71,s88
s_nop 0
buffer_load_dwordx4 v10,s[36:39],s49 offen offset:0 sc1 lds
buffer_load_dword v11,s[36:39],s72 offen offset:1024 sc1 lds
s_cmp_eq_u32 vcc_hi,-1
s_cselect_b32 s67,s50,s59
s_xor_b32 s62,s63,65536
s_cmp_ge_u32 s67,s59
s_cselect_b32 s68,s50,s51
s_lshr_b32 s68,s68,5
s_add_u32 m0,s78,256
s_nop 0
buffer_load_dword v6,s[36:39],s68 offen offset:0 sc1 lds
s_mov_b32 m0,s62
s_nop 0
buffer_load_dwordx4 v0,s[36:39],s67 offen offset:0 sc1 lds
buffer_load_dwordx4 v0,s[36:39],s67 offen offset:1024 sc1 lds
buffer_load_dwordx4 v0,s[36:39],s67 offen offset:2048 sc1 lds
buffer_load_dwordx4 v0,s[36:39],s67 offen offset:3072 sc1 lds
s_waitcnt vmcnt(11)
s_barrier
ds_read_b128 v[32:35],v2 offset:32768
ds_read_b128 v[36:39],v2 offset:33792
ds_read_b128 v[40:43],v2 offset:34816
ds_read_b128 v[44:47],v2 offset:35840
ds_read_b128 v[48:51],v2 offset:36864
ds_read_b128 v[52:55],v2 offset:37888
ds_read_b128 v[56:59],v2 offset:38912
s_lshr_b32 s70,s47,5
buffer_store_dword v5,v4,s[36:39],s70 offen offset:0 sc1
s_mov_b32 s47,s59
s_lshr_b32 s70,s48,5
buffer_store_dword v5,v4,s[36:39],s70 offen offset:0 sc1
s_mov_b32 s48,s59
s_lshr_b32 s70,s59,5
buffer_store_dword v5,v4,s[36:39],s70 offen offset:0 sc1
s_waitcnt lgkmcnt(3)
s_setpc_b64 s[92:93]
Lg2_gs1_%=:
s_cmp_eq_u32 s54,1
s_cselect_b32 s68,s50,s49
s_lshr_b32 s68,s68,5
s_lshr_b32 s73,s48,0
s_lshr_b32 s85,s48,5
s_add_u32 m0,s78,8192
s_nop 0
buffer_load_dword v6,s[36:39],s68 offen offset:0 sc1 lds
v_lshlrev_b32 v64,16,v80
v_and_b32 v65,0xffff0000,v80
v_lshlrev_b32 v66,16,v81
v_and_b32 v67,0xffff0000,v81
v_lshlrev_b32 v68,16,v82
v_and_b32 v69,0xffff0000,v82
v_lshlrev_b32 v70,16,v83
v_and_b32 v71,0xffff0000,v83
s_add_u32 m0,s62,16384
s_nop 0
buffer_load_dwordx4 v1,s[36:39],s67 offen offset:0 sc1 lds
v_fma_f32 v64,v64,v89,v88
v_fma_f32 v65,v65,v89,v88
v_fma_f32 v66,v66,v89,v88
v_fma_f32 v67,v67,v89,v88
v_fma_f32 v68,v68,v89,v88
v_fma_f32 v69,v69,v89,v88
v_fma_f32 v70,v70,v89,v88
v_fma_f32 v71,v71,v89,v88
buffer_load_dwordx4 v1,s[36:39],s67 offen offset:1024 sc1 lds
v_add_f32 v16,v16,v64
v_add_f32 v17,v17,v65
v_add_f32 v18,v18,v66
v_add_f32 v19,v19,v67
v_add_f32 v20,v20,v68
v_add_f32 v21,v21,v69
v_add_f32 v22,v22,v70
v_add_f32 v23,v23,v71
buffer_load_dwordx4 v1,s[36:39],s67 offen offset:2048 sc1 lds
v_cvt_pk_bf16_f32 v72,v16,v17
v_cvt_pk_bf16_f32 v73,v18,v19
v_cvt_pk_bf16_f32 v74,v20,v21
v_cvt_pk_bf16_f32 v75,v22,v23
buffer_store_dwordx4 v[72:75],v3,s[36:39],s73 offen offset:0 sc1
v_add_f32 v64,v16,v17
v_mul_f32 v65,v16,v16
v_fma_f32 v65,v17,v17,v65
buffer_load_dwordx4 v1,s[36:39],s67 offen offset:3072 sc1 lds
v_add_f32 v64,v64,v18
v_fma_f32 v65,v18,v18,v65
v_add_f32 v64,v64,v19
v_fma_f32 v65,v19,v19,v65
v_add_f32 v64,v64,v20
v_fma_f32 v65,v20,v20,v65
v_add_f32 v64,v64,v21
v_fma_f32 v65,v21,v21,v65
s_lshr_b32 s72,s67,6
s_lshr_b32 s71,s62,2
s_add_u32 m0,s71,s88
s_nop 0
buffer_load_dwordx4 v10,s[36:39],s67 offen offset:0 sc1 lds
v_add_f32 v64,v64,v22
v_fma_f32 v65,v22,v22,v65
v_add_f32 v64,v64,v23
v_fma_f32 v65,v23,v23,v65
s_nop 1
v_permlane16_swap_b32 v64,v65
v_add_f32 v64,v64,v65
v_mov_b32 v65,v64
s_nop 1
v_permlane32_swap_b32 v64,v65
buffer_load_dword v11,s[36:39],s72 offen offset:1024 sc1 lds
v_add_f32 v64,v64,v65
ds_write_b32 v13,v64 offset:2048
s_waitcnt lgkmcnt(0)
s_barrier
ds_read_b128 v[76:79],v14 offset:2048
s_waitcnt lgkmcnt(0)
v_add_f32 v76,v76,v77
v_add_f32 v78,v78,v79
v_add_f32 v76,v76,v78
buffer_store_dword v76,v12,s[36:39],s85 offen offset:0 sc1
s_waitcnt vmcnt(8)
s_barrier
ds_read_b32 v15,v7 offset:8192
ds_read_b128 v[32:35],v2 offset:32768
ds_read_b128 v[36:39],v2 offset:33792
ds_read_b128 v[40:43],v2 offset:34816
ds_read_b128 v[44:47],v2 offset:35840
ds_read_b128 v[48:51],v2 offset:36864
ds_read_b128 v[52:55],v2 offset:37888
ds_read_b128 v[56:59],v2 offset:38912
s_lshr_b32 s70,s47,5
buffer_store_dword v5,v4,s[36:39],s70 offen offset:0 sc1
s_waitcnt lgkmcnt(7)
v_cmp_eq_u32 vcc,s60,v15
s_nop 0
s_cmp_eq_u32 vcc_hi,-1
s_cselect_b32 s67,s50,s59
s_xor_b32 s62,s63,65536
s_cmp_ge_u32 s67,s59
s_cselect_b32 s68,s50,s51)"
R"(
s_lshr_b32 s68,s68,5
s_add_u32 m0,s78,256
s_nop 0
buffer_load_dword v6,s[36:39],s68 offen offset:0 sc1 lds
s_mov_b32 m0,s62
s_nop 0
buffer_load_dwordx4 v0,s[36:39],s67 offen offset:0 sc1 lds
buffer_load_dwordx4 v0,s[36:39],s67 offen offset:1024 sc1 lds
buffer_load_dwordx4 v0,s[36:39],s67 offen offset:2048 sc1 lds
buffer_load_dwordx4 v0,s[36:39],s67 offen offset:3072 sc1 lds
s_mov_b32 s47,s48
s_mov_b32 s48,s59
s_lshr_b32 s70,s59,5
buffer_store_dword v5,v4,s[36:39],s70 offen offset:0 sc1
s_lshr_b32 s70,s59,5
buffer_store_dword v5,v4,s[36:39],s70 offen offset:0 sc1
s_waitcnt lgkmcnt(3)
s_setpc_b64 s[92:93]
Lg2_done1_%=:
s_waitcnt vmcnt(0)
s_barrier
s_lshr_b32 s70,s47,5
buffer_store_dword v5,v4,s[36:39],s70 offen offset:0 sc1
s_lshr_b32 s70,s48,5
buffer_store_dword v5,v4,s[36:39],s70 offen offset:0 sc1
s_branch Lg2_done_%=
Lg2_done_%=:
s_waitcnt vmcnt(0) lgkmcnt(0)
s_barrier
Lg2_end_%=:
s_mov_b32 %0,s60
Lcode_g12_e:)"
  : "=&s"(epOut) : "s"(ws), "s"(wave), "s"(sStep), "s"(sX1), "s"(sX2), "s"(sU0), "s"(sWw), "s"(sBuf), "v"(vSrc), "v"(vSrc2), "v"(vA), "v"(vOut), "v"(vFlag), "v"(vPollOff), "v"(vSnapRd), "v"(vX), "v"(vX2), "v"(vAuxS), "v"(vAuxS2), "v"(vOut2), "v"(vStW), "v"(vStR), "v"(vWd0), "v"(vWd1), "v"(vWr0), "v"(vWr1), "v"(vWr2), "s"(w12) : "v0", "v1", "v2", "v3", "v4", "v5", "v6", "v7", "v8", "v9", "v10", "v11", "v12", "v13", "v14", "v15", "v16", "v17", "v18", "v19", "v20", "v21", "v22", "v23", "v24", "v25", "v26", "v27", "v28", "v29", "v30", "v31", "v32", "v33", "v34", "v35", "v36", "v37", "v38", "v39", "v40", "v41", "v42", "v43", "v44", "v45", "v46", "v47", "v48", "v49", "v50", "v51", "v52", "v53", "v54", "v55", "v56", "v57", "v58", "v59", "v60", "v61", "v62", "v63", "v64", "v65", "v66", "v67", "v68", "v69", "v70", "v71", "v72", "v73", "v74", "v75", "v76", "v77", "v78", "v79", "v80", "v81", "v82", "v83", "v84", "v85", "v86", "v87", "v88", "v89", "v90", "v91", "v92", "v93", "v94", "v95", "v96", "v97", "v98", "v99", "v100", "v101", "v102", "v103", "v104", "v105", "v106", "v107", "v108", "v109", "v110", "v111", "v112", "v113", "v114", "v115", "v116", "v117", "v118", "v119", "v120", "v121", "v122", "v123", "v124", "v125", "v126", "v127", "v128", "v129", "v130", "v131", "v132", "v133", "v134", "v135", "v136", "v137", "v138", "v139", "v140", "v141", "v142", "v143", "v144", "v145", "v146", "v147", "v148", "v149", "v150", "v151", "v152", "v153", "v154", "v155", "v156", "v157", "v158", "v159", "v160", "v161", "v162", "v163", "v164", "v165", "v166", "v167", "v168", "v169", "v170", "v171", "v172", "v173", "v174", "v175", "v176", "v177", "v178", "v179", "v180", "v181", "v182", "v183", "v184", "v185", "v186", "v187", "v188", "v189", "v190", "v191", "v192", "v193", "v194", "v195", "v196", "v197", "v198", "v199", "v200", "v201", "v202", "v203", "v204", "v205", "v206", "v207", "v208", "v209", "v210", "v211", "v212", "v213", "v214", "v215", "v216", "v217", "v218", "v219", "v220", "v221", "v222", "v223", "v224", "v225", "v226", "v227", "v228", "v229", "v230", "v231", "v255", "a0", "a1", "a2", "a3", "a4", "a5", "a6", "a7", "a8", "a9", "a10", "a11", "a12", "a13", "a14", "a15", "a16", "a17", "a18", "a19", "a20", "a21", "a22", "a23", "a24", "a25", "a26", "a27", "a28", "a29", "a30", "a31", "a32", "a33", "a34", "a35", "a36", "a37", "a38", "a39", "a40", "a41", "a42", "a43", "a44", "a45", "a46", "a47", "a48", "a49", "a50", "a51", "a52", "a53", "a54", "a55", "a56", "a57", "a58", "a59", "a60", "a61", "a62", "a63", "a64", "a65", "a66", "a67", "a68", "a69", "a70", "a71", "a72", "a73", "a74", "a75", "a76", "a77", "a78", "a79", "a80", "a81", "a82", "a83", "a84", "a85", "a86", "a87", "a88", "a89", "a90", "a91", "a92", "a93", "a94", "a95", "a96", "a97", "a98", "a99", "a100", "a101", "a102", "a103", "a104", "a105", "a106", "a107", "a108", "a109", "a110", "a111", "a112", "a113", "a114", "a115", "a116", "a117", "a118", "a119", "a120", "a121", "a122", "a123", "a124", "a125", "a126", "a127", "a128", "a129", "a130", "a131", "a132", "a133", "a134", "a135", "a136", "a137", "a138", "a139", "a140", "a141", "a142", "a143", "a144", "a145", "a146", "a147", "a148", "a149", "a150", "a151", "a152", "a153", "a154", "a155", "a156", "a157", "a158", "a159", "a160", "a161", "a162", "a163", "a164", "a165", "a166", "a167", "a168", "a169", "a170", "a171", "a172", "a173", "a174", "a175", "a176", "a177", "a178", "a179", "a180", "a181", "a182", "a183", "a184", "a185", "a186", "a187", "a188", "a189", "a190", "a191", "a192", "a193", "a194", "a195", "a196", "a197", "a198", "a199", "a200", "a201", "a202", "a203", "a204", "a205", "a206", "a207", "a208", "a209", "a210", "a211", "a212", "a213", "a214", "a215", "a216", "a217", "a218", "a219", "a220", "a221", "a222", "a223", "a224", "a225", "a226", "a227", "a228", "a229", "a230", "a231", "a232", "a233", "a234", "a235", "a236", "a237", "a238", "a239", "a240", "a241", "a242", "a243", "a244", "a245", "a246", "a247", "a248", "a249", "a250", "a251", "a252", "a253", "a254", "a255", "s36", "s37", "s38", "s39", "s40", "s41", "s42", "s43", "s44", "s45", "s46", "s47", "s48", "s49", "s50", "s51", "s52", "s53", "s54", "s55", "s56", "s57", "s58", "s59", "s60", "s61", "s62", "s63", "s64", "s65", "s66", "s67", "s68", "s69", "s70", "s71", "s72", "s73", "s74", "s75", "s76", "s77", "s78", "s79", "s80", "s81", "s82", "s83", "s84", "s85", "s86", "s87", "s88", "s89", "s90", "s91", "s92", "s93", "s94", "s95", "s96", "s97", "s98", "s99", "vcc", "scc", "memory");
    ep = epOut;
    if (N3 && kind == 1 && rep < 2 * NX) { kind = 4; kh = (rep & 1) * 2 + (slice >> 2); slice &= 3; rep = 5 + (rep >> 1); }
   }
  if (kind == 3 || kind == 5) {
    KARG(ws, 10) KARG(w3, 8)
    COMMON(8, OFF_FX1, OFF_FG + slice * 128, AUX + 1536, 2048, 7168, kind == 3 ? 5 : 4);
    const unsigned vSrc = OFF_X1 + wave * 4096 + vA, vOut = OFF_G + (slice * 6 + wave) * 1024 + vA;
    const unsigned vOut2 = OFF_G + (slice * 6 + 4 + (wave >> 1)) * 1024 + (((wave & 1) * 2 + (lg >> 1)) * 16 + l15) * 16 + (lg & 1) * 8;
    const unsigned vAuxS = OFF_ST + (ln & 7) * 128 + (wave * 8 + (ln >> 3)) * 4, vX = AUX + wave * 2048 + ln * 4;
    const unsigned vStW = (ln & 7) == 0 ? AUX + 1280 + (wave * 8 + (ln >> 3)) * 4 : AUX + 24576 + wave * 2048 + 1536 + ln * 4, vStR = AUX + 1280 + l15 * 8;
    const unsigned sWw = 16 * wave * 7168 + slice * 384;
    if (kind == 3) { asm volatile(R"(
Lcode_g3_b:
s_mov_b64 s[36:37],%0
s_mov_b32 s38,122727552
s_mov_b32 s39,0x00020000
s_mov_b32 s54,%1
s_mov_b32 s55,%2
s_mov_b32 s56,%3
s_mov_b32 s58,%4
s_mov_b32 s83,%5
s_mov_b32 s82,%6
s_mov_b32 s84,%7
s_mov_b32 s60,%8
v_mov_b32 v0,%9
v_mov_b32 v2,%10
v_mov_b32 v3,%11
v_mov_b32 v4,%12
v_mov_b32 v6,%13
v_mov_b32 v7,%14
v_mov_b32 v8,%15
v_mov_b32 v9,%16
v_mov_b32 v10,%17
v_mov_b32 v11,%18
v_mov_b32 v28,%19
v_mov_b32 v29,%20
v_mov_b32 v30,%21
v_mov_b32 v31,%22
v_mov_b32 v32,%23
s_mov_b64 s[40:41],%24
s_mov_b32 s42,7340032
s_mov_b32 s43,0x00020000
s_lshl_b32 s88,s54,11
s_add_u32 m0,s88,0
s_add_u32 s61,s82,0
buffer_load_dwordx4 v28,s[40:43],s61 offen offset:0  lds
s_add_u32 m0,s88,1024
s_nop 0
buffer_load_dwordx4 v29,s[40:43],s61 offen offset:0  lds
s_add_u32 m0,s88,8064
s_nop 0
buffer_load_dwordx4 v28,s[40:43],s61 offen offset:128  lds
s_add_u32 m0,s88,9088
s_nop 0
buffer_load_dwordx4 v29,s[40:43],s61 offen offset:128  lds
s_add_u32 m0,s88,16128
s_nop 0
buffer_load_dwordx4 v28,s[40:43],s61 offen offset:256  lds
s_add_u32 m0,s88,17152
s_nop 0
buffer_load_dwordx4 v29,s[40:43],s61 offen offset:256  lds
s_add_u32 m0,s88,24576
s_add_u32 s61,s82,458752
buffer_load_dwordx4 v28,s[40:43],s61 offen offset:0  lds
s_add_u32 m0,s88,25600
s_nop 0
buffer_load_dwordx4 v29,s[40:43],s61 offen offset:0  lds
s_add_u32 m0,s88,32640
s_nop 0
buffer_load_dwordx4 v28,s[40:43],s61 offen offset:128  lds
s_add_u32 m0,s88,33664
s_nop 0
buffer_load_dwordx4 v29,s[40:43],s61 offen offset:128  lds
s_add_u32 m0,s88,40704
s_nop 0
buffer_load_dwordx4 v28,s[40:43],s61 offen offset:256  lds
s_add_u32 m0,s88,41728
s_nop 0
buffer_load_dwordx4 v29,s[40:43],s61 offen offset:256  lds
s_add_u32 m0,s88,49152
s_add_u32 s61,s82,917504
buffer_load_dwordx4 v28,s[40:43],s61 offen offset:0  lds
s_add_u32 m0,s88,50176
s_nop 0
buffer_load_dwordx4 v29,s[40:43],s61 offen offset:0  lds
s_add_u32 m0,s88,57216
s_nop 0
buffer_load_dwordx4 v28,s[40:43],s61 offen offset:128  lds
s_add_u32 m0,s88,58240
s_nop 0
buffer_load_dwordx4 v29,s[40:43],s61 offen offset:128  lds
s_add_u32 m0,s88,65280
s_nop 0
buffer_load_dwordx4 v28,s[40:43],s61 offen offset:256  lds
s_add_u32 m0,s88,66304
s_nop 0
buffer_load_dwordx4 v29,s[40:43],s61 offen offset:256  lds
s_add_u32 m0,s88,73728
s_add_u32 s61,s82,1376256
buffer_load_dwordx4 v28,s[40:43],s61 offen offset:0  lds
s_add_u32 m0,s88,74752
s_nop 0
buffer_load_dwordx4 v29,s[40:43],s61 offen offset:0  lds
s_add_u32 m0,s88,81792
s_nop 0
buffer_load_dwordx4 v28,s[40:43],s61 offen offset:128  lds
s_add_u32 m0,s88,82816
s_nop 0
buffer_load_dwordx4 v29,s[40:43],s61 offen offset:128  lds
s_add_u32 m0,s88,89856
s_nop 0
buffer_load_dwordx4 v28,s[40:43],s61 offen offset:256  lds
s_add_u32 m0,s88,90880
s_nop 0
buffer_load_dwordx4 v29,s[40:43],s61 offen offset:256  lds
v_add_u32 v40,49152,v30
v_add_u32 v41,49152,v31
v_add_u32 v42,49152,v32
v_add_u32 v43,98304,v30
v_add_u32 v44,98304,v31
v_add_u32 v45,98304,v32
v_mov_b32 v84,0x3f803f80
v_mov_b32 v85,0x3f803f80
v_mov_b32 v86,0x3f803f80
v_mov_b32 v87,0x3f803f80
s_waitcnt vmcnt(18)
s_barrier
s_add_u32 m0,s88,98304
s_add_u32 s61,s82,1835008
buffer_load_dwordx4 v28,s[40:43],s61 offen offset:0  lds
s_add_u32 m0,s88,99328
ds_read_b64_tr_b16 a[0:1],v30 offset:0
buffer_load_dwordx4 v29,s[40:43],s61 offen offset:0  lds
s_add_u32 m0,s88,106368
ds_read_b64_tr_b16 a[2:3],v30 offset:512
buffer_load_dwordx4 v28,s[40:43],s61 offen offset:128  lds
s_add_u32 m0,s88,107392
ds_read_b64_tr_b16 a[4:5],v31 offset:0
buffer_load_dwordx4 v29,s[40:43],s61 offen offset:128  lds
s_add_u32 m0,s88,114432
ds_read_b64_tr_b16 a[6:7],v31 offset:512
buffer_load_dwordx4 v28,s[40:43],s61 offen offset:256  lds
s_add_u32 m0,s88,115456
ds_read_b64_tr_b16 a[8:9],v32 offset:0
buffer_load_dwordx4 v29,s[40:43],s61 offen offset:256  lds
ds_read_b64_tr_b16 a[10:11],v32 offset:512
ds_read_b64_tr_b16 a[12:13],v30 offset:4096
ds_read_b64_tr_b16 a[14:15],v30 offset:4608
ds_read_b64_tr_b16 a[16:17],v31 offset:4096
ds_read_b64_tr_b16 a[18:19],v31 offset:4608
ds_read_b64_tr_b16 a[20:21],v32 offset:4096
ds_read_b64_tr_b16 a[22:23],v32 offset:4608
s_waitcnt vmcnt(18)
s_barrier
s_add_u32 m0,s88,122880
s_add_u32 s61,s82,2293760
buffer_load_dwordx4 v28,s[40:43],s61 offen offset:0  lds
s_add_u32 m0,s88,123904
ds_read_b64_tr_b16 a[24:25],v30 offset:24576
buffer_load_dwordx4 v29,s[40:43],s61 offen offset:0  lds
s_add_u32 m0,s88,130944
ds_read_b64_tr_b16 a[26:27],v30 offset:25088
buffer_load_dwordx4 v28,s[40:43],s61 offen offset:128  lds
s_add_u32 m0,s88,131968
ds_read_b64_tr_b16 a[28:29],v31 offset:24576
buffer_load_dwordx4 v29,s[40:43],s61 offen offset:128  lds
s_add_u32 m0,s88,139008
ds_read_b64_tr_b16 a[30:31],v31 offset:25088
buffer_load_dwordx4 v28,s[40:43],s61 offen offset:256  lds
s_add_u32 m0,s88,140032
ds_read_b64_tr_b16 a[32:33],v32 offset:24576
buffer_load_dwordx4 v29,s[40:43],s61 offen offset:256  lds
ds_read_b64_tr_b16 a[34:35],v32 offset:25088
ds_read_b64_tr_b16 a[36:37],v30 offset:28672
ds_read_b64_tr_b16 a[38:39],v30 offset:29184
ds_read_b64_tr_b16 a[40:41],v31 offset:28672
ds_read_b64_tr_b16 a[42:43],v31 offset:29184
ds_read_b64_tr_b16 a[44:45],v32 offset:28672
ds_read_b64_tr_b16 a[46:47],v32 offset:29184
s_waitcnt lgkmcnt(12)
v_mfma_f32_16x16x32_bf16 v[16:19],a[0:3],v[84:87],0
v_mfma_f32_16x16x32_bf16 v[20:23],a[4:7],v[84:87],0
v_mfma_f32_16x16x32_bf16 v[24:27],a[8:11],v[84:87],0
v_mfma_f32_16x16x32_bf16 v[16:19],a[12:15],v[84:87],v[16:19]
v_mfma_f32_16x16x32_bf16 v[20:23],a[16:19],v[84:87],v[20:23]
v_mfma_f32_16x16x32_bf16 v[24:27],a[20:23],v[84:87],v[24:27]
s_waitcnt vmcnt(18)
s_barrier
s_add_u32 m0,s88,0
s_add_u32 s61,s82,2752512
buffer_load_dwordx4 v28,s[40:43],s61 offen offset:0  lds
s_add_u32 m0,s88,1024
ds_read_b64_tr_b16 a[48:49],v40 offset:0
buffer_load_dwordx4 v29,s[40:43],s61 offen offset:0  lds
s_add_u32 m0,s88,8064
ds_read_b64_tr_b16 a[50:51],v40 offset:512
buffer_load_dwordx4 v28,s[40:43],s61 offen offset:128  lds
s_add_u32 m0,s88,9088
ds_read_b64_tr_b16 a[52:53],v41 offset:0
buffer_load_dwordx4 v29,s[40:43],s61 offen offset:128  lds
s_add_u32 m0,s88,16128
ds_read_b64_tr_b16 a[54:55],v41 offset:512
buffer_load_dwordx4 v28,s[40:43],s61 offen offset:256  lds
s_add_u32 m0,s88,17152
ds_read_b64_tr_b16 a[56:57],v42 offset:0
buffer_load_dwordx4 v29,s[40:43],s61 offen offset:256  lds
ds_read_b64_tr_b16 a[58:59],v42 offset:512
ds_read_b64_tr_b16 a[60:61],v40 offset:4096
ds_read_b64_tr_b16 a[62:63],v40 offset:4608
ds_read_b64_tr_b16 a[64:65],v41 offset:4096
ds_read_b64_tr_b16 a[66:67],v41 offset:4608
ds_read_b64_tr_b16 a[68:69],v42 offset:4096)"
R"(
ds_read_b64_tr_b16 a[70:71],v42 offset:4608
s_waitcnt lgkmcnt(12)
v_mfma_f32_16x16x32_bf16 v[16:19],a[24:27],v[84:87],v[16:19]
v_mfma_f32_16x16x32_bf16 v[20:23],a[28:31],v[84:87],v[20:23]
v_mfma_f32_16x16x32_bf16 v[24:27],a[32:35],v[84:87],v[24:27]
v_mfma_f32_16x16x32_bf16 v[16:19],a[36:39],v[84:87],v[16:19]
v_mfma_f32_16x16x32_bf16 v[20:23],a[40:43],v[84:87],v[20:23]
v_mfma_f32_16x16x32_bf16 v[24:27],a[44:47],v[84:87],v[24:27]
s_waitcnt vmcnt(18)
s_barrier
s_add_u32 m0,s88,24576
s_add_u32 s61,s82,3211264
buffer_load_dwordx4 v28,s[40:43],s61 offen offset:0  lds
s_add_u32 m0,s88,25600
ds_read_b64_tr_b16 a[72:73],v40 offset:24576
buffer_load_dwordx4 v29,s[40:43],s61 offen offset:0  lds
s_add_u32 m0,s88,32640
ds_read_b64_tr_b16 a[74:75],v40 offset:25088
buffer_load_dwordx4 v28,s[40:43],s61 offen offset:128  lds
s_add_u32 m0,s88,33664
ds_read_b64_tr_b16 a[76:77],v41 offset:24576
buffer_load_dwordx4 v29,s[40:43],s61 offen offset:128  lds
s_add_u32 m0,s88,40704
ds_read_b64_tr_b16 a[78:79],v41 offset:25088
buffer_load_dwordx4 v28,s[40:43],s61 offen offset:256  lds
s_add_u32 m0,s88,41728
ds_read_b64_tr_b16 a[80:81],v42 offset:24576
buffer_load_dwordx4 v29,s[40:43],s61 offen offset:256  lds
ds_read_b64_tr_b16 a[82:83],v42 offset:25088
ds_read_b64_tr_b16 a[84:85],v40 offset:28672
ds_read_b64_tr_b16 a[86:87],v40 offset:29184
ds_read_b64_tr_b16 a[88:89],v41 offset:28672
ds_read_b64_tr_b16 a[90:91],v41 offset:29184
ds_read_b64_tr_b16 a[92:93],v42 offset:28672
ds_read_b64_tr_b16 a[94:95],v42 offset:29184
s_waitcnt lgkmcnt(12)
v_mfma_f32_16x16x32_bf16 v[16:19],a[48:51],v[84:87],v[16:19]
v_mfma_f32_16x16x32_bf16 v[20:23],a[52:55],v[84:87],v[20:23]
v_mfma_f32_16x16x32_bf16 v[24:27],a[56:59],v[84:87],v[24:27]
v_mfma_f32_16x16x32_bf16 v[16:19],a[60:63],v[84:87],v[16:19]
v_mfma_f32_16x16x32_bf16 v[20:23],a[64:67],v[84:87],v[20:23]
v_mfma_f32_16x16x32_bf16 v[24:27],a[68:71],v[84:87],v[24:27]
s_waitcnt vmcnt(18)
s_barrier
s_add_u32 m0,s88,49152
s_add_u32 s61,s82,3670016
buffer_load_dwordx4 v28,s[40:43],s61 offen offset:0  lds
s_add_u32 m0,s88,50176
ds_read_b64_tr_b16 a[96:97],v43 offset:0
buffer_load_dwordx4 v29,s[40:43],s61 offen offset:0  lds
s_add_u32 m0,s88,57216
ds_read_b64_tr_b16 a[98:99],v43 offset:512
buffer_load_dwordx4 v28,s[40:43],s61 offen offset:128  lds
s_add_u32 m0,s88,58240
ds_read_b64_tr_b16 a[100:101],v44 offset:0
buffer_load_dwordx4 v29,s[40:43],s61 offen offset:128  lds
s_add_u32 m0,s88,65280
ds_read_b64_tr_b16 a[102:103],v44 offset:512
buffer_load_dwordx4 v28,s[40:43],s61 offen offset:256  lds
s_add_u32 m0,s88,66304
ds_read_b64_tr_b16 a[104:105],v45 offset:0
buffer_load_dwordx4 v29,s[40:43],s61 offen offset:256  lds
ds_read_b64_tr_b16 a[106:107],v45 offset:512
ds_read_b64_tr_b16 a[108:109],v43 offset:4096
ds_read_b64_tr_b16 a[110:111],v43 offset:4608
ds_read_b64_tr_b16 a[112:113],v44 offset:4096
ds_read_b64_tr_b16 a[114:115],v44 offset:4608
ds_read_b64_tr_b16 a[116:117],v45 offset:4096
ds_read_b64_tr_b16 a[118:119],v45 offset:4608
s_waitcnt lgkmcnt(12)
v_mfma_f32_16x16x32_bf16 v[16:19],a[72:75],v[84:87],v[16:19]
v_mfma_f32_16x16x32_bf16 v[20:23],a[76:79],v[84:87],v[20:23]
v_mfma_f32_16x16x32_bf16 v[24:27],a[80:83],v[84:87],v[24:27]
v_mfma_f32_16x16x32_bf16 v[16:19],a[84:87],v[84:87],v[16:19]
v_mfma_f32_16x16x32_bf16 v[20:23],a[88:91],v[84:87],v[20:23]
v_mfma_f32_16x16x32_bf16 v[24:27],a[92:95],v[84:87],v[24:27]
s_waitcnt vmcnt(18)
s_barrier
s_add_u32 m0,s88,73728
s_add_u32 s61,s82,4128768
buffer_load_dwordx4 v28,s[40:43],s61 offen offset:0  lds
s_add_u32 m0,s88,74752
ds_read_b64_tr_b16 a[120:121],v43 offset:24576
buffer_load_dwordx4 v29,s[40:43],s61 offen offset:0  lds
s_add_u32 m0,s88,81792
ds_read_b64_tr_b16 a[122:123],v43 offset:25088
buffer_load_dwordx4 v28,s[40:43],s61 offen offset:128  lds
s_add_u32 m0,s88,82816
ds_read_b64_tr_b16 a[124:125],v44 offset:24576
buffer_load_dwordx4 v29,s[40:43],s61 offen offset:128  lds
s_add_u32 m0,s88,89856
ds_read_b64_tr_b16 a[126:127],v44 offset:25088
buffer_load_dwordx4 v28,s[40:43],s61 offen offset:256  lds
s_add_u32 m0,s88,90880
ds_read_b64_tr_b16 a[128:129],v45 offset:24576
buffer_load_dwordx4 v29,s[40:43],s61 offen offset:256  lds
ds_read_b64_tr_b16 a[130:131],v45 offset:25088
ds_read_b64_tr_b16 a[132:133],v43 offset:28672
ds_read_b64_tr_b16 a[134:135],v43 offset:29184
ds_read_b64_tr_b16 a[136:137],v44 offset:28672
ds_read_b64_tr_b16 a[138:139],v44 offset:29184
ds_read_b64_tr_b16 a[140:141],v45 offset:28672
ds_read_b64_tr_b16 a[142:143],v45 offset:29184
s_waitcnt lgkmcnt(12)
v_mfma_f32_16x16x32_bf16 v[16:19],a[96:99],v[84:87],v[16:19]
v_mfma_f32_16x16x32_bf16 v[20:23],a[100:103],v[84:87],v[20:23]
v_mfma_f32_16x16x32_bf16 v[24:27],a[104:107],v[84:87],v[24:27]
v_mfma_f32_16x16x32_bf16 v[16:19],a[108:111],v[84:87],v[16:19]
v_mfma_f32_16x16x32_bf16 v[20:23],a[112:115],v[84:87],v[20:23]
v_mfma_f32_16x16x32_bf16 v[24:27],a[116:119],v[84:87],v[24:27]
s_waitcnt vmcnt(18)
s_barrier
s_add_u32 m0,s88,98304
s_add_u32 s61,s82,4587520
buffer_load_dwordx4 v28,s[40:43],s61 offen offset:0  lds
s_add_u32 m0,s88,99328
ds_read_b64_tr_b16 a[144:145],v30 offset:0
buffer_load_dwordx4 v29,s[40:43],s61 offen offset:0  lds
s_add_u32 m0,s88,106368
ds_read_b64_tr_b16 a[146:147],v30 offset:512
buffer_load_dwordx4 v28,s[40:43],s61 offen offset:128  lds
s_add_u32 m0,s88,107392
ds_read_b64_tr_b16 a[148:149],v31 offset:0
buffer_load_dwordx4 v29,s[40:43],s61 offen offset:128  lds
s_add_u32 m0,s88,114432
ds_read_b64_tr_b16 a[150:151],v31 offset:512
buffer_load_dwordx4 v28,s[40:43],s61 offen offset:256  lds
s_add_u32 m0,s88,115456
ds_read_b64_tr_b16 a[152:153],v32 offset:0
buffer_load_dwordx4 v29,s[40:43],s61 offen offset:256  lds
ds_read_b64_tr_b16 a[154:155],v32 offset:512
ds_read_b64_tr_b16 a[156:157],v30 offset:4096
ds_read_b64_tr_b16 a[158:159],v30 offset:4608
ds_read_b64_tr_b16 a[160:161],v31 offset:4096
ds_read_b64_tr_b16 a[162:163],v31 offset:4608
ds_read_b64_tr_b16 a[164:165],v32 offset:4096
ds_read_b64_tr_b16 a[166:167],v32 offset:4608
s_waitcnt lgkmcnt(12)
v_mfma_f32_16x16x32_bf16 v[16:19],a[120:123],v[84:87],v[16:19]
v_mfma_f32_16x16x32_bf16 v[20:23],a[124:127],v[84:87],v[20:23]
v_mfma_f32_16x16x32_bf16 v[24:27],a[128:131],v[84:87],v[24:27]
v_mfma_f32_16x16x32_bf16 v[16:19],a[132:135],v[84:87],v[16:19]
v_mfma_f32_16x16x32_bf16 v[20:23],a[136:139],v[84:87],v[20:23]
v_mfma_f32_16x16x32_bf16 v[24:27],a[140:143],v[84:87],v[24:27]
s_waitcnt vmcnt(18)
s_barrier
s_add_u32 m0,s88,122880
s_add_u32 s61,s82,5046272
buffer_load_dwordx4 v28,s[40:43],s61 offen offset:0  lds
s_add_u32 m0,s88,123904
ds_read_b64_tr_b16 a[168:169],v30 offset:24576
buffer_load_dwordx4 v29,s[40:43],s61 offen offset:0  lds
s_add_u32 m0,s88,130944
ds_read_b64_tr_b16 a[170:171],v30 offset:25088
buffer_load_dwordx4 v28,s[40:43],s61 offen offset:128  lds
s_add_u32 m0,s88,131968
ds_read_b64_tr_b16 a[172:173],v31 offset:24576
buffer_load_dwordx4 v29,s[40:43],s61 offen offset:128  lds
s_add_u32 m0,s88,139008
ds_read_b64_tr_b16 a[174:175],v31 offset:25088
buffer_load_dwordx4 v28,s[40:43],s61 offen offset:256  lds
s_add_u32 m0,s88,140032
ds_read_b64_tr_b16 a[176:177],v32 offset:24576
buffer_load_dwordx4 v29,s[40:43],s61 offen offset:256  lds
ds_read_b64_tr_b16 a[178:179],v32 offset:25088
ds_read_b64_tr_b16 a[180:181],v30 offset:28672
ds_read_b64_tr_b16 a[182:183],v30 offset:29184
ds_read_b64_tr_b16 a[184:185],v31 offset:28672
ds_read_b64_tr_b16 a[186:187],v31 offset:29184
ds_read_b64_tr_b16 a[188:189],v32 offset:28672
ds_read_b64_tr_b16 a[190:191],v32 offset:29184
s_waitcnt lgkmcnt(12)
v_mfma_f32_16x16x32_bf16 v[16:19],a[144:147],v[84:87],v[16:19]
v_mfma_f32_16x16x32_bf16 v[20:23],a[148:151],v[84:87],v[20:23]
v_mfma_f32_16x16x32_bf16 v[24:27],a[152:155],v[84:87],v[24:27]
v_mfma_f32_16x16x32_bf16 v[16:19],a[156:159],v[84:87],v[16:19]
v_mfma_f32_16x16x32_bf16 v[20:23],a[160:163],v[84:87],v[20:23]
v_mfma_f32_16x16x32_bf16 v[24:27],a[164:167],v[84:87],v[24:27]
s_waitcnt vmcnt(18)
s_barrier
s_add_u32 m0,s88,0
s_add_u32 s61,s82,5505024
buffer_load_dwordx4 v28,s[40:43],s61 offen offset:0  lds
s_add_u32 m0,s88,1024
ds_read_b64_tr_b16 a[192:193],v40 offset:0
buffer_load_dwordx4 v29,s[40:43],s61 offen offset:0  lds
s_add_u32 m0,s88,8064
ds_read_b64_tr_b16 a[194:195],v40 offset:512
buffer_load_dwordx4 v28,s[40:43],s61 offen offset:128  lds
s_add_u32 m0,s88,9088
ds_read_b64_tr_b16 a[196:197],v41 offset:0
buffer_load_dwordx4 v29,s[40:43],s61 offen offset:128  lds
s_add_u32 m0,s88,16128
ds_read_b64_tr_b16 a[198:199],v41 offset:512
buffer_load_dwordx4 v28,s[40:43],s61 offen offset:256  lds
s_add_u32 m0,s88,17152
ds_read_b64_tr_b16 a[200:201],v42 offset:0
buffer_load_dwordx4 v29,s[40:43],s61 offen offset:256  lds
ds_read_b64_tr_b16 a[202:203],v42 offset:512
ds_read_b64_tr_b16 a[204:205],v40 offset:4096)"
R"(
ds_read_b64_tr_b16 a[206:207],v40 offset:4608
ds_read_b64_tr_b16 a[208:209],v41 offset:4096
ds_read_b64_tr_b16 a[210:211],v41 offset:4608
ds_read_b64_tr_b16 a[212:213],v42 offset:4096
ds_read_b64_tr_b16 a[214:215],v42 offset:4608
s_waitcnt lgkmcnt(12)
v_mfma_f32_16x16x32_bf16 v[16:19],a[168:171],v[84:87],v[16:19]
v_mfma_f32_16x16x32_bf16 v[20:23],a[172:175],v[84:87],v[20:23]
v_mfma_f32_16x16x32_bf16 v[24:27],a[176:179],v[84:87],v[24:27]
v_mfma_f32_16x16x32_bf16 v[16:19],a[180:183],v[84:87],v[16:19]
v_mfma_f32_16x16x32_bf16 v[20:23],a[184:187],v[84:87],v[20:23]
v_mfma_f32_16x16x32_bf16 v[24:27],a[188:191],v[84:87],v[24:27]
s_waitcnt vmcnt(18)
s_barrier
s_add_u32 m0,s88,24576
s_add_u32 s61,s82,5963776
buffer_load_dwordx4 v28,s[40:43],s61 offen offset:0  lds
s_add_u32 m0,s88,25600
ds_read_b64_tr_b16 a[216:217],v40 offset:24576
buffer_load_dwordx4 v29,s[40:43],s61 offen offset:0  lds
s_add_u32 m0,s88,32640
ds_read_b64_tr_b16 a[218:219],v40 offset:25088
buffer_load_dwordx4 v28,s[40:43],s61 offen offset:128  lds
s_add_u32 m0,s88,33664
ds_read_b64_tr_b16 a[220:221],v41 offset:24576
buffer_load_dwordx4 v29,s[40:43],s61 offen offset:128  lds
s_add_u32 m0,s88,40704
ds_read_b64_tr_b16 a[222:223],v41 offset:25088
buffer_load_dwordx4 v28,s[40:43],s61 offen offset:256  lds
s_add_u32 m0,s88,41728
ds_read_b64_tr_b16 a[224:225],v42 offset:24576
buffer_load_dwordx4 v29,s[40:43],s61 offen offset:256  lds
ds_read_b64_tr_b16 a[226:227],v42 offset:25088
ds_read_b64_tr_b16 a[228:229],v40 offset:28672
ds_read_b64_tr_b16 a[230:231],v40 offset:29184
ds_read_b64_tr_b16 a[232:233],v41 offset:28672
ds_read_b64_tr_b16 a[234:235],v41 offset:29184
ds_read_b64_tr_b16 a[236:237],v42 offset:28672
ds_read_b64_tr_b16 a[238:239],v42 offset:29184
s_waitcnt lgkmcnt(12)
v_mfma_f32_16x16x32_bf16 v[16:19],a[192:195],v[84:87],v[16:19]
v_mfma_f32_16x16x32_bf16 v[20:23],a[196:199],v[84:87],v[20:23]
v_mfma_f32_16x16x32_bf16 v[24:27],a[200:203],v[84:87],v[24:27]
v_mfma_f32_16x16x32_bf16 v[16:19],a[204:207],v[84:87],v[16:19]
v_mfma_f32_16x16x32_bf16 v[20:23],a[208:211],v[84:87],v[20:23]
v_mfma_f32_16x16x32_bf16 v[24:27],a[212:215],v[84:87],v[24:27]
s_waitcnt vmcnt(18)
s_barrier
s_add_u32 m0,s88,49152
s_add_u32 s61,s82,6422528
buffer_load_dwordx4 v28,s[40:43],s61 offen offset:0  lds
s_add_u32 m0,s88,50176
ds_read_b64_tr_b16 a[240:241],v43 offset:0
buffer_load_dwordx4 v29,s[40:43],s61 offen offset:0  lds
s_add_u32 m0,s88,57216
ds_read_b64_tr_b16 a[242:243],v43 offset:512
buffer_load_dwordx4 v28,s[40:43],s61 offen offset:128  lds
s_add_u32 m0,s88,58240
ds_read_b64_tr_b16 a[244:245],v44 offset:0
buffer_load_dwordx4 v29,s[40:43],s61 offen offset:128  lds
s_add_u32 m0,s88,65280
ds_read_b64_tr_b16 a[246:247],v44 offset:512
buffer_load_dwordx4 v28,s[40:43],s61 offen offset:256  lds
s_add_u32 m0,s88,66304
ds_read_b64_tr_b16 a[248:249],v45 offset:0
buffer_load_dwordx4 v29,s[40:43],s61 offen offset:256  lds
ds_read_b64_tr_b16 a[250:251],v45 offset:512
ds_read_b64_tr_b16 a[252:253],v43 offset:4096
ds_read_b64_tr_b16 a[254:255],v43 offset:4608
ds_read_b64_tr_b16 v[128:129],v44 offset:4096
ds_read_b64_tr_b16 v[130:131],v44 offset:4608
ds_read_b64_tr_b16 v[132:133],v45 offset:4096
ds_read_b64_tr_b16 v[134:135],v45 offset:4608
s_waitcnt lgkmcnt(12)
v_mfma_f32_16x16x32_bf16 v[16:19],a[216:219],v[84:87],v[16:19]
v_mfma_f32_16x16x32_bf16 v[20:23],a[220:223],v[84:87],v[20:23]
v_mfma_f32_16x16x32_bf16 v[24:27],a[224:227],v[84:87],v[24:27]
v_mfma_f32_16x16x32_bf16 v[16:19],a[228:231],v[84:87],v[16:19]
v_mfma_f32_16x16x32_bf16 v[20:23],a[232:235],v[84:87],v[20:23]
v_mfma_f32_16x16x32_bf16 v[24:27],a[236:239],v[84:87],v[24:27]
s_waitcnt vmcnt(18)
s_barrier
s_add_u32 m0,s88,73728
s_add_u32 s61,s82,6881280
buffer_load_dwordx4 v28,s[40:43],s61 offen offset:0  lds
s_add_u32 m0,s88,74752
ds_read_b64_tr_b16 v[136:137],v43 offset:24576
buffer_load_dwordx4 v29,s[40:43],s61 offen offset:0  lds
s_add_u32 m0,s88,81792
ds_read_b64_tr_b16 v[138:139],v43 offset:25088
buffer_load_dwordx4 v28,s[40:43],s61 offen offset:128  lds
s_add_u32 m0,s88,82816
ds_read_b64_tr_b16 v[140:141],v44 offset:24576
buffer_load_dwordx4 v29,s[40:43],s61 offen offset:128  lds
s_add_u32 m0,s88,89856
ds_read_b64_tr_b16 v[142:143],v44 offset:25088
buffer_load_dwordx4 v28,s[40:43],s61 offen offset:256  lds
s_add_u32 m0,s88,90880
ds_read_b64_tr_b16 v[144:145],v45 offset:24576
buffer_load_dwordx4 v29,s[40:43],s61 offen offset:256  lds
ds_read_b64_tr_b16 v[146:147],v45 offset:25088
ds_read_b64_tr_b16 v[148:149],v43 offset:28672
ds_read_b64_tr_b16 v[150:151],v43 offset:29184
ds_read_b64_tr_b16 v[152:153],v44 offset:28672
ds_read_b64_tr_b16 v[154:155],v44 offset:29184
ds_read_b64_tr_b16 v[156:157],v45 offset:28672
ds_read_b64_tr_b16 v[158:159],v45 offset:29184
s_getpc_b64 s[46:47]
Lg3_cp_%=:
s_add_u32 s46,s46,Lg3_init_%=-Lg3_cp_%=
s_addc_u32 s47,s47,0
s_lshl_b32 s68,s54,13
v_lshl_add_u32 v68,v2,3,s68
v_min_u32 v69,Lg3_done_%=-Lg3_init_%=,v68
global_load_dword v72,v69,s[46:47]
s_waitcnt lgkmcnt(12)
v_mfma_f32_16x16x32_bf16 v[16:19],a[240:243],v[84:87],v[16:19]
v_mfma_f32_16x16x32_bf16 v[20:23],a[244:247],v[84:87],v[20:23]
v_mfma_f32_16x16x32_bf16 v[24:27],a[248:251],v[84:87],v[24:27]
v_mfma_f32_16x16x32_bf16 v[16:19],a[252:255],v[84:87],v[16:19]
v_mfma_f32_16x16x32_bf16 v[20:23],v[128:131],v[84:87],v[20:23]
v_mfma_f32_16x16x32_bf16 v[24:27],v[132:135],v[84:87],v[24:27]
s_waitcnt vmcnt(19)
s_barrier
ds_read_b64_tr_b16 v[160:161],v30 offset:0
ds_read_b64_tr_b16 v[162:163],v30 offset:512
ds_read_b64_tr_b16 v[164:165],v31 offset:0
ds_read_b64_tr_b16 v[166:167],v31 offset:512
ds_read_b64_tr_b16 v[168:169],v32 offset:0
ds_read_b64_tr_b16 v[170:171],v32 offset:512
ds_read_b64_tr_b16 v[172:173],v30 offset:4096
ds_read_b64_tr_b16 v[174:175],v30 offset:4608
ds_read_b64_tr_b16 v[176:177],v31 offset:4096
ds_read_b64_tr_b16 v[178:179],v31 offset:4608
ds_read_b64_tr_b16 v[180:181],v32 offset:4096
ds_read_b64_tr_b16 v[182:183],v32 offset:4608
s_waitcnt lgkmcnt(12)
v_mfma_f32_16x16x32_bf16 v[16:19],v[136:139],v[84:87],v[16:19]
v_mfma_f32_16x16x32_bf16 v[20:23],v[140:143],v[84:87],v[20:23]
v_mfma_f32_16x16x32_bf16 v[24:27],v[144:147],v[84:87],v[24:27]
v_mfma_f32_16x16x32_bf16 v[16:19],v[148:151],v[84:87],v[16:19]
v_mfma_f32_16x16x32_bf16 v[20:23],v[152:155],v[84:87],v[20:23]
v_mfma_f32_16x16x32_bf16 v[24:27],v[156:159],v[84:87],v[24:27]
s_waitcnt vmcnt(13)
s_barrier
ds_read_b64_tr_b16 v[184:185],v30 offset:24576
ds_read_b64_tr_b16 v[186:187],v30 offset:25088
ds_read_b64_tr_b16 v[188:189],v31 offset:24576
ds_read_b64_tr_b16 v[190:191],v31 offset:25088
ds_read_b64_tr_b16 v[192:193],v32 offset:24576
ds_read_b64_tr_b16 v[194:195],v32 offset:25088
ds_read_b64_tr_b16 v[196:197],v30 offset:28672
ds_read_b64_tr_b16 v[198:199],v30 offset:29184
ds_read_b64_tr_b16 v[200:201],v31 offset:28672
ds_read_b64_tr_b16 v[202:203],v31 offset:29184
ds_read_b64_tr_b16 v[204:205],v32 offset:28672
ds_read_b64_tr_b16 v[206:207],v32 offset:29184
s_waitcnt lgkmcnt(12)
v_mfma_f32_16x16x32_bf16 v[16:19],v[160:163],v[84:87],v[16:19]
v_mfma_f32_16x16x32_bf16 v[20:23],v[164:167],v[84:87],v[20:23]
v_mfma_f32_16x16x32_bf16 v[24:27],v[168:171],v[84:87],v[24:27]
v_mfma_f32_16x16x32_bf16 v[16:19],v[172:175],v[84:87],v[16:19]
v_mfma_f32_16x16x32_bf16 v[20:23],v[176:179],v[84:87],v[20:23]
v_mfma_f32_16x16x32_bf16 v[24:27],v[180:183],v[84:87],v[24:27]
s_waitcnt vmcnt(7)
s_barrier
ds_read_b64_tr_b16 v[208:209],v40 offset:0
ds_read_b64_tr_b16 v[210:211],v40 offset:512
ds_read_b64_tr_b16 v[212:213],v41 offset:0
ds_read_b64_tr_b16 v[214:215],v41 offset:512
ds_read_b64_tr_b16 v[216:217],v42 offset:0
ds_read_b64_tr_b16 v[218:219],v42 offset:512
ds_read_b64_tr_b16 v[220:221],v40 offset:4096
ds_read_b64_tr_b16 v[222:223],v40 offset:4608
ds_read_b64_tr_b16 v[224:225],v41 offset:4096
ds_read_b64_tr_b16 v[226:227],v41 offset:4608
ds_read_b64_tr_b16 v[228:229],v42 offset:4096
ds_read_b64_tr_b16 v[230:231],v42 offset:4608
s_waitcnt lgkmcnt(12)
v_mfma_f32_16x16x32_bf16 v[16:19],v[184:187],v[84:87],v[16:19]
v_mfma_f32_16x16x32_bf16 v[20:23],v[188:191],v[84:87],v[20:23]
v_mfma_f32_16x16x32_bf16 v[24:27],v[192:195],v[84:87],v[24:27]
v_mfma_f32_16x16x32_bf16 v[16:19],v[196:199],v[84:87],v[16:19]
v_mfma_f32_16x16x32_bf16 v[20:23],v[200:203],v[84:87],v[20:23]
v_mfma_f32_16x16x32_bf16 v[24:27],v[204:207],v[84:87],v[24:27]
s_waitcnt vmcnt(1)
s_barrier
ds_read_b64_tr_b16 v[232:233],v40 offset:24576
ds_read_b64_tr_b16 v[234:235],v40 offset:25088
ds_read_b64_tr_b16 v[236:237],v41 offset:24576
ds_read_b64_tr_b16 v[238:239],v41 offset:25088
ds_read_b64_tr_b16 v[240:241],v42 offset:24576
ds_read_b64_tr_b16 v[242:243],v42 offset:25088
ds_read_b64_tr_b16 v[244:245],v40 offset:28672
ds_read_b64_tr_b16 v[246:247],v40 offset:29184
ds_read_b64_tr_b16 v[248:249],v41 offset:28672
ds_read_b64_tr_b16 v[250:251],v41 offset:29184
ds_read_b64_tr_b16 v[252:253],v42 offset:28672
ds_read_b64_tr_b16 v[254:255],v42 offset:29184
s_waitcnt lgkmcnt(12))"
R"(
v_mfma_f32_16x16x32_bf16 v[16:19],v[208:211],v[84:87],v[16:19]
v_mfma_f32_16x16x32_bf16 v[20:23],v[212:215],v[84:87],v[20:23]
v_mfma_f32_16x16x32_bf16 v[24:27],v[216:219],v[84:87],v[24:27]
v_mfma_f32_16x16x32_bf16 v[16:19],v[220:223],v[84:87],v[16:19]
v_mfma_f32_16x16x32_bf16 v[20:23],v[224:227],v[84:87],v[20:23]
v_mfma_f32_16x16x32_bf16 v[24:27],v[228:231],v[84:87],v[24:27]
s_waitcnt lgkmcnt(0)
v_mfma_f32_16x16x32_bf16 v[16:19],v[232:235],v[84:87],v[16:19]
v_mfma_f32_16x16x32_bf16 v[20:23],v[236:239],v[84:87],v[20:23]
v_mfma_f32_16x16x32_bf16 v[24:27],v[240:243],v[84:87],v[24:27]
v_mfma_f32_16x16x32_bf16 v[16:19],v[244:247],v[84:87],v[16:19]
v_mfma_f32_16x16x32_bf16 v[20:23],v[248:251],v[84:87],v[20:23]
v_mfma_f32_16x16x32_bf16 v[24:27],v[252:255],v[84:87],v[24:27]
s_waitcnt lgkmcnt(0)
s_barrier
Lg3_init_%=:
v_mov_b32 v5,s60
s_mov_b32 s59,536870912
s_add_u32 s57,s56,131072
s_mov_b32 s79,0
s_mov_b32 s75,0x3a800000
s_mov_b32 s76,0xc0135761
s_mov_b32 s77,0x3d372713
s_mov_b32 s44,s59
s_mov_b32 s45,s59
s_mov_b32 s46,s59
s_mov_b32 s47,s59
s_mov_b32 s48,s83
s_add_u32 s49,s48,s84
s_add_u32 s50,s49,s55
s_cmp_eq_u32 s50,s56
s_cselect_b32 s50,s57,s50
s_cmp_eq_u32 s50,s58
s_cselect_b32 s50,s59,s50
s_add_u32 s51,s50,s55
s_cmp_eq_u32 s51,s56
s_cselect_b32 s51,s57,s51
s_cmp_eq_u32 s51,s58
s_cselect_b32 s51,s59,s51
s_add_u32 s52,s51,s55
s_cmp_eq_u32 s52,s56
s_cselect_b32 s52,s57,s52
s_cmp_eq_u32 s52,s58
s_cselect_b32 s52,s59,s52
s_lshl_b32 s63,s54,12
s_add_u32 s63,s63,65536
s_add_u32 s64,s63,32768
s_lshl_b32 s88,s54,10
s_add_u32 s88,s88,131072
s_lshl_b32 s78,s54,11
s_add_u32 s78,s78,132608
v_add_u32 v1,16384,v0
v_mov_b32 v12,0x3727c5ac
s_getpc_b64 s[90:91]
Lg3_pc_%=:
s_mov_b32 s68,Lg3_d1w0_%=-Lg3_pc_%=
s_cmp_eq_u32 s54,1
s_cselect_b32 s68,Lg3_d1w1_%=-Lg3_pc_%=,s68
s_cmp_eq_u32 s54,2
s_cselect_b32 s68,Lg3_d1w2_%=-Lg3_pc_%=,s68
s_cmp_eq_u32 s54,3
s_cselect_b32 s68,Lg3_d1w3_%=-Lg3_pc_%=,s68
s_add_u32 s92,s90,s68
s_addc_u32 s93,s91,0
s_mov_b32 s68,Lg3_d0w0_%=-Lg3_pc_%=
s_cmp_eq_u32 s54,1
s_cselect_b32 s68,Lg3_d0w1_%=-Lg3_pc_%=,s68
s_cmp_eq_u32 s54,2
s_cselect_b32 s68,Lg3_d0w2_%=-Lg3_pc_%=,s68
s_cmp_eq_u32 s54,3
s_cselect_b32 s68,Lg3_d0w3_%=-Lg3_pc_%=,s68
s_add_u32 s90,s90,s68
s_addc_u32 s91,s91,0
s_mov_b32 s66,s59
s_mov_b32 s65,0
s_mov_b32 s62,s63
s_branch Lg3_slow0_%=
Lg3_d0w0_%=:
v_mfma_f32_16x16x32_bf16 v[28:31],a[0:3],v[52:55],0
s_mov_b32 m0,s62
ds_read_b128 v[72:75],v2 offset:5120
v_mul_f32 v117,s76,v112
v_mfma_f32_16x16x32_bf16 v[32:35],a[4:7],v[52:55],0
buffer_load_dwordx4 v0,s[36:39],s61 offen offset:0 sc1 lds
v_mfma_f32_16x16x32_bf16 v[36:39],a[8:11],v[52:55],0
s_mul_i32 s73,s47,7
v_mul_f32 v118,v112,v112
v_mul_f32 v119,v113,v112
v_mfma_f32_16x16x32_bf16 v[28:31],a[12:15],v[56:59],v[28:31]
s_lshr_b32 s73,s73,1
ds_read_b128 v[76:79],v2 offset:6144
v_mul_f32 v118,s77,v118
v_mfma_f32_16x16x32_bf16 v[32:35],a[16:19],v[56:59],v[32:35]
s_add_u32 s53,s52,s55
v_mul_f32 v118,v118,v117
v_fma_f32 v40,-v116,v16,v40
v_mfma_f32_16x16x32_bf16 v[36:39],a[20:23],v[56:59],v[36:39]
s_waitcnt lgkmcnt(3)
buffer_load_dwordx4 v0,s[36:39],s61 offen offset:1024 sc1 lds
v_mfma_f32_16x16x32_bf16 v[28:31],a[24:27],v[60:63],v[28:31]
s_cmp_eq_u32 s53,s56
ds_read_b128 v[80:83],v2 offset:7168
v_fma_f32 v41,-v116,v17,v41
v_mfma_f32_16x16x32_bf16 v[32:35],a[28:31],v[60:63],v[32:35]
s_cselect_b32 s53,s57,s53
v_fma_f32 v42,-v116,v18,v42
v_fma_f32 v43,-v116,v19,v43
v_mfma_f32_16x16x32_bf16 v[36:39],a[32:35],v[60:63],v[36:39]
s_cmp_eq_u32 s53,s58
v_mul_f32 v84,v40,v40
v_mul_f32 v85,v41,v41
v_mfma_f32_16x16x32_bf16 v[28:31],a[36:39],v[64:67],v[28:31]
buffer_load_dwordx4 v0,s[36:39],s61 offen offset:2048 sc1 lds
v_mfma_f32_16x16x32_bf16 v[32:35],a[40:43],v[64:67],v[32:35]
s_cselect_b32 s53,s59,s53
ds_read_b128 v[52:55],v2 offset:8192
v_mul_f32 v86,v42,v42
v_mfma_f32_16x16x32_bf16 v[36:39],a[44:47],v[64:67],v[36:39]
s_waitcnt lgkmcnt(3)
v_mul_f32 v87,v43,v43
v_fma_f32 v84,v118,v84,v117
v_mfma_f32_16x16x32_bf16 v[28:31],a[48:51],v[68:71],v[28:31]
s_cmp_ge_u32 s66,s59
ds_read_b128 v[56:59],v2 offset:9216
v_mul_f32 v84,v40,v84
v_mfma_f32_16x16x32_bf16 v[32:35],a[52:55],v[68:71],v[32:35]
buffer_load_dwordx4 v0,s[36:39],s61 offen offset:3072 sc1 lds
v_mfma_f32_16x16x32_bf16 v[36:39],a[56:59],v[68:71],v[36:39]
s_cselect_b32 s86,s49,s50
v_exp_f32 v84,v84
v_mfma_f32_16x16x32_bf16 v[28:31],a[60:63],v[72:75],v[28:31]
s_lshr_b32 s68,s86,5
ds_read_b128 v[60:63],v2 offset:10240
v_fma_f32 v84,v84,v119,v119
v_mfma_f32_16x16x32_bf16 v[32:35],a[64:67],v[72:75],v[32:35]
s_add_u32 m0,s78,0
v_rcp_f32 v84,v84
v_mfma_f32_16x16x32_bf16 v[36:39],a[68:71],v[72:75],v[36:39]
s_waitcnt lgkmcnt(3)
v_fma_f32 v85,v118,v85,v117
v_mul_f32 v85,v41,v85
v_mfma_f32_16x16x32_bf16 v[28:31],a[72:75],v[76:79],v[28:31]
ds_read_b128 v[64:67],v2 offset:11264
v_fma_f32 v86,v118,v86,v117
v_mfma_f32_16x16x32_bf16 v[32:35],a[76:79],v[76:79],v[32:35]
v_exp_f32 v85,v85
v_mfma_f32_16x16x32_bf16 v[36:39],a[80:83],v[76:79],v[36:39]
v_mul_f32 v86,v42,v86
v_fma_f32 v85,v85,v119,v119
v_mfma_f32_16x16x32_bf16 v[28:31],a[84:87],v[80:83],v[28:31]
ds_read_b128 v[68:71],v2 offset:12288
v_fma_f32 v87,v118,v87,v117
v_mfma_f32_16x16x32_bf16 v[32:35],a[88:91],v[80:83],v[32:35]
v_exp_f32 v86,v86
v_mfma_f32_16x16x32_bf16 v[36:39],a[92:95],v[80:83],v[36:39]
s_waitcnt lgkmcnt(3)
v_rcp_f32 v85,v85
v_mfma_f32_16x16x32_bf16 v[28:31],a[96:99],v[52:55],v[28:31]
ds_read_b128 v[72:75],v2 offset:13312
v_mul_f32 v87,v43,v87
v_mfma_f32_16x16x32_bf16 v[32:35],a[100:103],v[52:55],v[32:35]
v_exp_f32 v87,v87
v_mfma_f32_16x16x32_bf16 v[36:39],a[104:107],v[52:55],v[36:39]
v_fma_f32 v86,v86,v119,v119
v_fma_f32 v87,v87,v119,v119
v_mfma_f32_16x16x32_bf16 v[28:31],a[108:111],v[56:59],v[28:31]
ds_read_b128 v[76:79],v2 offset:14336
v_mul_f32 v40,v40,v84
v_mfma_f32_16x16x32_bf16 v[32:35],a[112:115],v[56:59],v[32:35]
v_rcp_f32 v86,v86
v_mfma_f32_16x16x32_bf16 v[36:39],a[116:119],v[56:59],v[36:39]
s_waitcnt lgkmcnt(3)
v_rcp_f32 v87,v87
v_mfma_f32_16x16x32_bf16 v[28:31],a[120:123],v[60:63],v[28:31]
ds_read_b128 v[80:83],v2 offset:15360
v_mul_f32 v41,v41,v85
v_mfma_f32_16x16x32_bf16 v[32:35],a[124:127],v[60:63],v[32:35]
s_waitcnt vmcnt(6)
v_mul_f32 v42,v42,v86
v_mul_f32 v43,v43,v87
v_mfma_f32_16x16x32_bf16 v[36:39],a[128:131],v[60:63],v[36:39]
s_barrier
v_mfma_f32_16x16x32_bf16 v[28:31],a[132:135],v[64:67],v[28:31]
ds_read_b128 v[52:55],v2 offset:16384
v_add_f32_dpp v104,v104,v104 quad_perm:[1,0,3,2] row_mask:0xf bank_mask:0xf
v_mfma_f32_16x16x32_bf16 v[32:35],a[136:139],v[64:67],v[32:35]
s_nop 0
v_add_f32_dpp v104,v104,v104 quad_perm:[2,3,0,1] row_mask:0xf bank_mask:0xf
v_cvt_pk_bf16_f32 v96,v40,v41
v_mfma_f32_16x16x32_bf16 v[36:39],a[140:143],v[64:67],v[36:39]
s_waitcnt lgkmcnt(3)
v_add_f32_dpp v104,v104,v104 row_half_mirror row_mask:0xf bank_mask:0xf
v_cvt_pk_bf16_f32 v97,v42,v43
v_mfma_f32_16x16x32_bf16 v[28:31],a[144:147],v[68:71],v[28:31]
ds_read_b128 v[56:59],v2 offset:17408
v_fma_f32 v44,-v116,v20,v44
v_mfma_f32_16x16x32_bf16 v[32:35],a[148:151],v[68:71],v[32:35]
v_fma_f32 v45,-v116,v21,v45
v_fma_f32 v46,-v116,v22,v46
v_mfma_f32_16x16x32_bf16 v[36:39],a[152:155],v[68:71],v[36:39])"
R"(
v_fma_f32 v47,-v116,v23,v47
v_mul_f32 v88,v44,v44
v_mfma_f32_16x16x32_bf16 v[28:31],a[156:159],v[72:75],v[28:31]
ds_read_b128 v[60:63],v2 offset:18432
v_mul_f32 v89,v45,v45
v_mfma_f32_16x16x32_bf16 v[32:35],a[160:163],v[72:75],v[32:35]
buffer_load_dword v6,s[36:39],s68 offen offset:0 sc1 lds
v_mfma_f32_16x16x32_bf16 v[36:39],a[164:167],v[72:75],v[36:39]
s_waitcnt lgkmcnt(3)
v_mul_f32 v90,v46,v46
v_mul_f32 v91,v47,v47
v_mfma_f32_16x16x32_bf16 v[28:31],a[168:171],v[76:79],v[28:31]
s_add_u32 m0,s62,16384
ds_read_b128 v[64:67],v2 offset:19456
v_fma_f32 v88,v118,v88,v117
v_mfma_f32_16x16x32_bf16 v[32:35],a[172:175],v[76:79],v[32:35]
buffer_load_dwordx4 v1,s[36:39],s61 offen offset:0 sc1 lds
v_mfma_f32_16x16x32_bf16 v[36:39],a[176:179],v[76:79],v[36:39]
s_lshr_b32 s72,s61,5
v_mul_f32 v88,v44,v88
v_fma_f32 v89,v118,v89,v117
v_mfma_f32_16x16x32_bf16 v[28:31],a[180:183],v[80:83],v[28:31]
s_mov_b32 s62,s63
ds_read_b128 v[68:71],v2 offset:20480
v_mul_f32 v89,v45,v89
v_mfma_f32_16x16x32_bf16 v[32:35],a[184:187],v[80:83],v[32:35]
ds_write_b32 v10,v104 offset:0
v_mfma_f32_16x16x32_bf16 v[36:39],a[188:191],v[80:83],v[36:39]
s_waitcnt lgkmcnt(4)
buffer_load_dwordx4 v1,s[36:39],s61 offen offset:1024 sc1 lds
v_mfma_f32_16x16x32_bf16 v[28:31],a[192:195],v[52:55],v[28:31]
ds_read_b128 v[72:75],v2 offset:21504
v_fma_f32 v90,v118,v90,v117
v_mfma_f32_16x16x32_bf16 v[32:35],a[196:199],v[52:55],v[32:35]
v_exp_f32 v88,v88
v_mfma_f32_16x16x32_bf16 v[36:39],a[200:203],v[52:55],v[36:39]
v_exp_f32 v89,v89
v_mfma_f32_16x16x32_bf16 v[28:31],a[204:207],v[56:59],v[28:31]
buffer_load_dwordx4 v1,s[36:39],s61 offen offset:2048 sc1 lds
v_mfma_f32_16x16x32_bf16 v[32:35],a[208:211],v[56:59],v[32:35]
ds_read_b128 v[76:79],v2 offset:22528
v_mul_f32 v90,v46,v90
v_mfma_f32_16x16x32_bf16 v[36:39],a[212:215],v[56:59],v[36:39]
s_waitcnt lgkmcnt(4)
v_exp_f32 v90,v90
v_mfma_f32_16x16x32_bf16 v[28:31],a[216:219],v[60:63],v[28:31]
ds_read_b128 v[80:83],v2 offset:23552
v_fma_f32 v88,v88,v119,v119
v_mfma_f32_16x16x32_bf16 v[32:35],a[220:223],v[60:63],v[32:35]
buffer_load_dwordx4 v1,s[36:39],s61 offen offset:3072 sc1 lds
v_mfma_f32_16x16x32_bf16 v[36:39],a[224:227],v[60:63],v[36:39]
buffer_load_dword v108,v8,s[36:39],s72 offen offset:0 sc1
v_mfma_f32_16x16x32_bf16 v[28:31],a[228:231],v[64:67],v[28:31]
ds_read_b128 v[52:55],v2 offset:24576
v_fma_f32 v89,v89,v119,v119
v_mfma_f32_16x16x32_bf16 v[32:35],a[232:235],v[64:67],v[32:35]
v_rcp_f32 v88,v88
v_mfma_f32_16x16x32_bf16 v[36:39],a[236:239],v[64:67],v[36:39]
s_waitcnt lgkmcnt(3)
v_rcp_f32 v89,v89
v_mfma_f32_16x16x32_bf16 v[28:31],a[240:243],v[68:71],v[28:31]
ds_read_b128 v[56:59],v2 offset:25600
v_fma_f32 v90,v90,v119,v119
v_mfma_f32_16x16x32_bf16 v[32:35],a[244:247],v[68:71],v[32:35]
v_rcp_f32 v90,v90
v_mfma_f32_16x16x32_bf16 v[36:39],a[248:251],v[68:71],v[36:39]
v_fma_f32 v91,v118,v91,v117
v_mul_f32 v91,v47,v91
v_mfma_f32_16x16x32_bf16 v[28:31],a[252:255],v[72:75],v[28:31]
ds_read_b128 v[60:63],v2 offset:26624
v_mul_f32 v44,v44,v88
v_mfma_f32_16x16x32_bf16 v[32:35],v[128:131],v[72:75],v[32:35]
v_exp_f32 v91,v91
v_mfma_f32_16x16x32_bf16 v[36:39],v[132:135],v[72:75],v[36:39]
s_waitcnt lgkmcnt(3)
v_fma_f32 v91,v91,v119,v119
v_mul_f32 v45,v45,v89
v_mfma_f32_16x16x32_bf16 v[28:31],v[136:139],v[76:79],v[28:31]
ds_read_b128 v[64:67],v2 offset:27648
v_mul_f32 v46,v46,v90
v_mfma_f32_16x16x32_bf16 v[32:35],v[140:143],v[76:79],v[32:35]
v_rcp_f32 v91,v91
v_mfma_f32_16x16x32_bf16 v[36:39],v[144:147],v[76:79],v[36:39]
v_mul_f32 v47,v47,v91
v_cvt_pk_bf16_f32 v98,v44,v45
v_mfma_f32_16x16x32_bf16 v[28:31],v[148:151],v[80:83],v[28:31]
ds_read_b128 v[68:71],v2 offset:28672
v_cvt_pk_bf16_f32 v99,v46,v47
v_mfma_f32_16x16x32_bf16 v[32:35],v[152:155],v[80:83],v[32:35]
v_fma_f32 v48,-v116,v24,v48
v_fma_f32 v49,-v116,v25,v49
v_mfma_f32_16x16x32_bf16 v[36:39],v[156:159],v[80:83],v[36:39]
s_waitcnt lgkmcnt(3)
v_fma_f32 v50,-v116,v26,v50
v_fma_f32 v51,-v116,v27,v51
v_mfma_f32_16x16x32_bf16 v[28:31],v[160:163],v[52:55],v[28:31]
ds_read_b128 v[72:75],v2 offset:29696
v_mul_f32 v92,v48,v48
v_mfma_f32_16x16x32_bf16 v[32:35],v[164:167],v[52:55],v[32:35]
v_mul_f32 v93,v49,v49
v_mul_f32 v94,v50,v50
v_mfma_f32_16x16x32_bf16 v[36:39],v[168:171],v[52:55],v[36:39]
v_mul_f32 v95,v51,v51
v_fma_f32 v92,v118,v92,v117
v_mfma_f32_16x16x32_bf16 v[28:31],v[172:175],v[56:59],v[28:31]
ds_read_b128 v[76:79],v2 offset:30720
v_mul_f32 v92,v48,v92
v_mfma_f32_16x16x32_bf16 v[32:35],v[176:179],v[56:59],v[32:35]
v_exp_f32 v92,v92
v_mfma_f32_16x16x32_bf16 v[36:39],v[180:183],v[56:59],v[36:39]
s_waitcnt lgkmcnt(3)
buffer_store_dwordx4 v[96:99],v3,s[36:39],s73 offen offset:0 sc1
v_fma_f32 v92,v92,v119,v119
v_rcp_f32 v92,v92
v_mfma_f32_16x16x32_bf16 v[28:31],v[184:187],v[60:63],v[28:31]
ds_read_b128 v[80:83],v2 offset:31744
v_fma_f32 v93,v118,v93,v117
v_mfma_f32_16x16x32_bf16 v[32:35],v[188:191],v[60:63],v[32:35]
s_waitcnt vmcnt(6)
v_mul_f32 v93,v49,v93
v_fma_f32 v94,v118,v94,v117
v_mfma_f32_16x16x32_bf16 v[36:39],v[192:195],v[60:63],v[36:39]
s_barrier
v_mfma_f32_16x16x32_bf16 v[28:31],v[196:199],v[64:67],v[28:31]
ds_read_b128 v[52:55],v2 offset:32768
ds_read_b32 v13,v7 offset:0
v_mfma_f32_16x16x32_bf16 v[32:35],v[200:203],v[64:67],v[32:35]
ds_read_b64 v[112:113],v11 offset:0
v_mul_f32 v94,v50,v94
v_mfma_f32_16x16x32_bf16 v[36:39],v[204:207],v[64:67],v[36:39]
s_waitcnt lgkmcnt(5)
v_exp_f32 v93,v93
v_mfma_f32_16x16x32_bf16 v[28:31],v[208:211],v[68:71],v[28:31]
ds_read_b128 v[56:59],v2 offset:33792
v_fma_f32 v93,v93,v119,v119
v_mfma_f32_16x16x32_bf16 v[32:35],v[212:215],v[68:71],v[32:35]
s_waitcnt lgkmcnt(2)
v_exp_f32 v94,v94
v_mfma_f32_16x16x32_bf16 v[36:39],v[216:219],v[68:71],v[36:39]
s_waitcnt lgkmcnt(1)
v_cmp_eq_u32 vcc,s60,v13
v_rcp_f32 v93,v93
v_fma_f32 v94,v94,v119,v119
v_mfma_f32_16x16x32_bf16 v[28:31],v[220:223],v[72:75],v[28:31]
s_cmp_eq_u32 vcc_lo,-1
ds_read_b128 v[60:63],v2 offset:34816
v_mul_f32 v116,s75,v112
v_mfma_f32_16x16x32_bf16 v[32:35],v[224:227],v[72:75],v[32:35]
s_cselect_b32 s67,s86,s59
v_rcp_f32 v94,v94
v_mfma_f32_16x16x32_bf16 v[36:39],v[228:231],v[72:75],v[36:39]
v_fma_f32 v95,v118,v95,v117
v_mul_f32 v95,v51,v95
v_mfma_f32_16x16x32_bf16 v[28:31],v[232:235],v[76:79],v[28:31]
s_lshr_b32 s61,s67,0
ds_read_b128 v[64:67],v2 offset:35840
v_mul_f32 v48,v48,v92
v_mfma_f32_16x16x32_bf16 v[32:35],v[236:239],v[76:79],v[32:35]
s_xor_b32 s63,s63,65536
v_exp_f32 v95,v95
v_mfma_f32_16x16x32_bf16 v[36:39],v[240:243],v[76:79],v[36:39]
s_cmp_ge_u32 s66,s59
v_fma_f32 v95,v95,v119,v119
v_rcp_f32 v95,v95
v_mfma_f32_16x16x32_bf16 v[28:31],v[244:247],v[80:83],v[28:31]
v_mul_f32 v49,v49,v93
v_mul_f32 v50,v50,v94
v_mul_f32 v51,v51,v95
v_cvt_pk_bf16_f32 v100,v48,v49
v_cvt_pk_bf16_f32 v101,v50,v51
v_mfma_f32_16x16x32_bf16 v[32:35],v[248:251],v[80:83],v[32:35]
s_waitcnt lgkmcnt(2)
ds_read_b128 v[68:71],v2 offset:36864
buffer_store_dwordx2 v[100:101],v9,s[36:39],s73 offen offset:0 sc1
v_fma_f32 v113,v113,s75,v12
v_fma_f32 v113,-v116,v116,v113
v_max_f32 v113,v113,v12
v_rsq_f32 v112,v113
v_mfma_f32_16x16x32_bf16 v[36:39],v[252:255],v[80:83],v[36:39]
s_cbranch_scc1 Lg3_slow1_%=
Lg3_d1w0_%=:
v_mfma_f32_16x16x32_bf16 v[40:43],a[0:3],v[52:55],0
s_mov_b32 m0,s62
ds_read_b128 v[72:75],v2 offset:37888
v_mul_f32 v117,s76,v112
v_mfma_f32_16x16x32_bf16 v[44:47],a[4:7],v[52:55],0
buffer_load_dwordx4 v0,s[36:39],s61 offen offset:0 sc1 lds
v_mfma_f32_16x16x32_bf16 v[48:51],a[8:11],v[52:55],0
s_mul_i32 s73,s48,7
v_mul_f32 v118,v112,v112
v_mul_f32 v119,v113,v112
v_mfma_f32_16x16x32_bf16 v[40:43],a[12:15],v[56:59],v[40:43]
s_lshr_b32 s73,s73,1
ds_read_b128 v[76:79],v2 offset:38912
v_mul_f32 v118,s77,v118
v_mfma_f32_16x16x32_bf16 v[44:47],a[16:19],v[56:59],v[44:47]
s_add_u32 s74,s53,s55
v_mul_f32 v118,v118,v117
v_fma_f32 v28,-v116,v16,v28
v_mfma_f32_16x16x32_bf16 v[48:51],a[20:23],v[56:59],v[48:51])"
R"(
s_waitcnt lgkmcnt(3)
buffer_load_dwordx4 v0,s[36:39],s61 offen offset:1024 sc1 lds
v_mfma_f32_16x16x32_bf16 v[40:43],a[24:27],v[60:63],v[40:43]
s_cmp_eq_u32 s74,s56
ds_read_b128 v[80:83],v2 offset:39936
v_fma_f32 v29,-v116,v17,v29
v_mfma_f32_16x16x32_bf16 v[44:47],a[28:31],v[60:63],v[44:47]
s_cselect_b32 s74,s57,s74
v_fma_f32 v30,-v116,v18,v30
v_fma_f32 v31,-v116,v19,v31
v_mfma_f32_16x16x32_bf16 v[48:51],a[32:35],v[60:63],v[48:51]
s_cmp_eq_u32 s74,s58
v_mul_f32 v84,v28,v28
v_mul_f32 v85,v29,v29
v_mfma_f32_16x16x32_bf16 v[40:43],a[36:39],v[64:67],v[40:43]
buffer_load_dwordx4 v0,s[36:39],s61 offen offset:2048 sc1 lds
v_mfma_f32_16x16x32_bf16 v[44:47],a[40:43],v[64:67],v[44:47]
s_cselect_b32 s74,s59,s74
ds_read_b128 v[52:55],v2 offset:40960
v_mul_f32 v86,v30,v30
v_mfma_f32_16x16x32_bf16 v[48:51],a[44:47],v[64:67],v[48:51]
s_waitcnt lgkmcnt(3)
v_mul_f32 v87,v31,v31
v_fma_f32 v84,v118,v84,v117
v_mfma_f32_16x16x32_bf16 v[40:43],a[48:51],v[68:71],v[40:43]
s_cmp_ge_u32 s67,s59
ds_read_b128 v[56:59],v2 offset:41984
v_mul_f32 v84,v28,v84
v_mfma_f32_16x16x32_bf16 v[44:47],a[52:55],v[68:71],v[44:47]
buffer_load_dwordx4 v0,s[36:39],s61 offen offset:3072 sc1 lds
v_mfma_f32_16x16x32_bf16 v[48:51],a[56:59],v[68:71],v[48:51]
s_cselect_b32 s86,s50,s51
v_exp_f32 v84,v84
v_mfma_f32_16x16x32_bf16 v[40:43],a[60:63],v[72:75],v[40:43]
s_lshr_b32 s68,s86,5
ds_read_b128 v[60:63],v2 offset:43008
v_fma_f32 v84,v84,v119,v119
v_mfma_f32_16x16x32_bf16 v[44:47],a[64:67],v[72:75],v[44:47]
s_add_u32 m0,s78,256
v_rcp_f32 v84,v84
v_mfma_f32_16x16x32_bf16 v[48:51],a[68:71],v[72:75],v[48:51]
s_waitcnt lgkmcnt(3)
v_fma_f32 v85,v118,v85,v117
v_mul_f32 v85,v29,v85
v_mfma_f32_16x16x32_bf16 v[40:43],a[72:75],v[76:79],v[40:43]
s_mov_b64 s[44:45],s[46:47]
ds_read_b128 v[64:67],v2 offset:44032
v_fma_f32 v86,v118,v86,v117
v_mfma_f32_16x16x32_bf16 v[44:47],a[76:79],v[76:79],v[44:47]
s_mov_b64 s[46:47],s[48:49]
v_exp_f32 v85,v85
v_mfma_f32_16x16x32_bf16 v[48:51],a[80:83],v[76:79],v[48:51]
s_mov_b64 s[48:49],s[50:51]
v_mul_f32 v86,v30,v86
v_fma_f32 v85,v85,v119,v119
v_mfma_f32_16x16x32_bf16 v[40:43],a[84:87],v[80:83],v[40:43]
s_mov_b64 s[50:51],s[52:53]
ds_read_b128 v[68:71],v2 offset:45056
v_fma_f32 v87,v118,v87,v117
v_mfma_f32_16x16x32_bf16 v[44:47],a[88:91],v[80:83],v[44:47]
s_mov_b32 s52,s74
v_exp_f32 v86,v86
v_mfma_f32_16x16x32_bf16 v[48:51],a[92:95],v[80:83],v[48:51]
s_waitcnt lgkmcnt(3)
v_rcp_f32 v85,v85
v_mfma_f32_16x16x32_bf16 v[40:43],a[96:99],v[52:55],v[40:43]
ds_read_b128 v[72:75],v2 offset:46080
v_mul_f32 v87,v31,v87
v_mfma_f32_16x16x32_bf16 v[44:47],a[100:103],v[52:55],v[44:47]
v_exp_f32 v87,v87
v_mfma_f32_16x16x32_bf16 v[48:51],a[104:107],v[52:55],v[48:51]
v_fma_f32 v86,v86,v119,v119
v_fma_f32 v87,v87,v119,v119
v_mfma_f32_16x16x32_bf16 v[40:43],a[108:111],v[56:59],v[40:43]
ds_read_b128 v[76:79],v2 offset:47104
v_mul_f32 v28,v28,v84
v_mfma_f32_16x16x32_bf16 v[44:47],a[112:115],v[56:59],v[44:47]
v_rcp_f32 v86,v86
v_mfma_f32_16x16x32_bf16 v[48:51],a[116:119],v[56:59],v[48:51]
s_waitcnt lgkmcnt(3)
v_rcp_f32 v87,v87
v_mfma_f32_16x16x32_bf16 v[40:43],a[120:123],v[60:63],v[40:43]
ds_read_b128 v[80:83],v2 offset:48128
v_mul_f32 v29,v29,v85
v_mfma_f32_16x16x32_bf16 v[44:47],a[124:127],v[60:63],v[44:47]
s_waitcnt vmcnt(6)
v_mul_f32 v30,v30,v86
v_mul_f32 v31,v31,v87
v_mfma_f32_16x16x32_bf16 v[48:51],a[128:131],v[60:63],v[48:51]
s_barrier
v_mfma_f32_16x16x32_bf16 v[40:43],a[132:135],v[64:67],v[40:43]
ds_read_b128 v[52:55],v2 offset:49152
v_add_f32_dpp v108,v108,v108 quad_perm:[1,0,3,2] row_mask:0xf bank_mask:0xf
v_mfma_f32_16x16x32_bf16 v[44:47],a[136:139],v[64:67],v[44:47]
s_nop 0
v_add_f32_dpp v108,v108,v108 quad_perm:[2,3,0,1] row_mask:0xf bank_mask:0xf
v_cvt_pk_bf16_f32 v96,v28,v29
v_mfma_f32_16x16x32_bf16 v[48:51],a[140:143],v[64:67],v[48:51]
s_waitcnt lgkmcnt(3)
v_add_f32_dpp v108,v108,v108 row_half_mirror row_mask:0xf bank_mask:0xf
v_cvt_pk_bf16_f32 v97,v30,v31
v_mfma_f32_16x16x32_bf16 v[40:43],a[144:147],v[68:71],v[40:43]
ds_read_b128 v[56:59],v2 offset:50176
v_fma_f32 v32,-v116,v20,v32
v_mfma_f32_16x16x32_bf16 v[44:47],a[148:151],v[68:71],v[44:47]
v_fma_f32 v33,-v116,v21,v33
v_fma_f32 v34,-v116,v22,v34
v_mfma_f32_16x16x32_bf16 v[48:51],a[152:155],v[68:71],v[48:51]
v_fma_f32 v35,-v116,v23,v35
v_mul_f32 v88,v32,v32
v_mfma_f32_16x16x32_bf16 v[40:43],a[156:159],v[72:75],v[40:43]
ds_read_b128 v[60:63],v2 offset:51200
v_mul_f32 v89,v33,v33
v_mfma_f32_16x16x32_bf16 v[44:47],a[160:163],v[72:75],v[44:47]
buffer_load_dword v6,s[36:39],s68 offen offset:0 sc1 lds
v_mfma_f32_16x16x32_bf16 v[48:51],a[164:167],v[72:75],v[48:51]
s_waitcnt lgkmcnt(3)
v_mul_f32 v90,v34,v34
v_mul_f32 v91,v35,v35
v_mfma_f32_16x16x32_bf16 v[40:43],a[168:171],v[76:79],v[40:43]
s_add_u32 m0,s62,16384
ds_read_b128 v[64:67],v2 offset:52224
v_fma_f32 v88,v118,v88,v117
v_mfma_f32_16x16x32_bf16 v[44:47],a[172:175],v[76:79],v[44:47]
buffer_load_dwordx4 v1,s[36:39],s61 offen offset:0 sc1 lds
v_mfma_f32_16x16x32_bf16 v[48:51],a[176:179],v[76:79],v[48:51]
s_lshr_b32 s72,s61,5
v_mul_f32 v88,v32,v88
v_fma_f32 v89,v118,v89,v117
v_mfma_f32_16x16x32_bf16 v[40:43],a[180:183],v[80:83],v[40:43]
s_mov_b32 s62,s64
ds_read_b128 v[68:71],v2 offset:53248
v_mul_f32 v89,v33,v89
v_mfma_f32_16x16x32_bf16 v[44:47],a[184:187],v[80:83],v[44:47]
ds_write_b32 v10,v108 offset:128
v_mfma_f32_16x16x32_bf16 v[48:51],a[188:191],v[80:83],v[48:51]
s_waitcnt lgkmcnt(4)
buffer_load_dwordx4 v1,s[36:39],s61 offen offset:1024 sc1 lds
v_mfma_f32_16x16x32_bf16 v[40:43],a[192:195],v[52:55],v[40:43]
ds_read_b128 v[72:75],v2 offset:54272
v_fma_f32 v90,v118,v90,v117
v_mfma_f32_16x16x32_bf16 v[44:47],a[196:199],v[52:55],v[44:47]
v_exp_f32 v88,v88
v_mfma_f32_16x16x32_bf16 v[48:51],a[200:203],v[52:55],v[48:51]
v_exp_f32 v89,v89
v_mfma_f32_16x16x32_bf16 v[40:43],a[204:207],v[56:59],v[40:43]
buffer_load_dwordx4 v1,s[36:39],s61 offen offset:2048 sc1 lds
v_mfma_f32_16x16x32_bf16 v[44:47],a[208:211],v[56:59],v[44:47]
ds_read_b128 v[76:79],v2 offset:55296
v_mul_f32 v90,v34,v90
v_mfma_f32_16x16x32_bf16 v[48:51],a[212:215],v[56:59],v[48:51]
s_waitcnt lgkmcnt(4)
v_exp_f32 v90,v90
v_mfma_f32_16x16x32_bf16 v[40:43],a[216:219],v[60:63],v[40:43]
ds_read_b128 v[80:83],v2 offset:56320
v_fma_f32 v88,v88,v119,v119
v_mfma_f32_16x16x32_bf16 v[44:47],a[220:223],v[60:63],v[44:47]
buffer_load_dwordx4 v1,s[36:39],s61 offen offset:3072 sc1 lds
v_mfma_f32_16x16x32_bf16 v[48:51],a[224:227],v[60:63],v[48:51]
buffer_load_dword v104,v8,s[36:39],s72 offen offset:0 sc1
v_mfma_f32_16x16x32_bf16 v[40:43],a[228:231],v[64:67],v[40:43]
ds_read_b128 v[52:55],v2 offset:57344
v_fma_f32 v89,v89,v119,v119
v_mfma_f32_16x16x32_bf16 v[44:47],a[232:235],v[64:67],v[44:47]
v_rcp_f32 v88,v88
v_mfma_f32_16x16x32_bf16 v[48:51],a[236:239],v[64:67],v[48:51]
s_waitcnt lgkmcnt(3)
v_rcp_f32 v89,v89
v_mfma_f32_16x16x32_bf16 v[40:43],a[240:243],v[68:71],v[40:43]
ds_read_b128 v[56:59],v2 offset:58368
v_fma_f32 v90,v90,v119,v119
v_mfma_f32_16x16x32_bf16 v[44:47],a[244:247],v[68:71],v[44:47]
v_rcp_f32 v90,v90
v_mfma_f32_16x16x32_bf16 v[48:51],a[248:251],v[68:71],v[48:51]
v_fma_f32 v91,v118,v91,v117
v_mul_f32 v91,v35,v91
v_mfma_f32_16x16x32_bf16 v[40:43],a[252:255],v[72:75],v[40:43]
ds_read_b128 v[60:63],v2 offset:59392
v_mul_f32 v32,v32,v88
v_mfma_f32_16x16x32_bf16 v[44:47],v[128:131],v[72:75],v[44:47]
v_exp_f32 v91,v91
v_mfma_f32_16x16x32_bf16 v[48:51],v[132:135],v[72:75],v[48:51]
s_waitcnt lgkmcnt(3)
v_fma_f32 v91,v91,v119,v119
v_mul_f32 v33,v33,v89
v_mfma_f32_16x16x32_bf16 v[40:43],v[136:139],v[76:79],v[40:43]
ds_read_b128 v[64:67],v2 offset:60416
v_mul_f32 v34,v34,v90
v_mfma_f32_16x16x32_bf16 v[44:47],v[140:143],v[76:79],v[44:47]
v_rcp_f32 v91,v91
v_mfma_f32_16x16x32_bf16 v[48:51],v[144:147],v[76:79],v[48:51]
v_mul_f32 v35,v35,v91
v_cvt_pk_bf16_f32 v98,v32,v33
v_mfma_f32_16x16x32_bf16 v[40:43],v[148:151],v[80:83],v[40:43]
ds_read_b128 v[68:71],v2 offset:61440
v_cvt_pk_bf16_f32 v99,v34,v35
v_mfma_f32_16x16x32_bf16 v[44:47],v[152:155],v[80:83],v[44:47]
v_fma_f32 v36,-v116,v24,v36
v_fma_f32 v37,-v116,v25,v37
v_mfma_f32_16x16x32_bf16 v[48:51],v[156:159],v[80:83],v[48:51])"
R"(
s_waitcnt lgkmcnt(3)
v_fma_f32 v38,-v116,v26,v38
v_fma_f32 v39,-v116,v27,v39
v_mfma_f32_16x16x32_bf16 v[40:43],v[160:163],v[52:55],v[40:43]
ds_read_b128 v[72:75],v2 offset:62464
v_mul_f32 v92,v36,v36
v_mfma_f32_16x16x32_bf16 v[44:47],v[164:167],v[52:55],v[44:47]
v_mul_f32 v93,v37,v37
v_mul_f32 v94,v38,v38
v_mfma_f32_16x16x32_bf16 v[48:51],v[168:171],v[52:55],v[48:51]
v_mul_f32 v95,v39,v39
v_fma_f32 v92,v118,v92,v117
v_mfma_f32_16x16x32_bf16 v[40:43],v[172:175],v[56:59],v[40:43]
ds_read_b128 v[76:79],v2 offset:63488
v_mul_f32 v92,v36,v92
v_mfma_f32_16x16x32_bf16 v[44:47],v[176:179],v[56:59],v[44:47]
v_exp_f32 v92,v92
v_mfma_f32_16x16x32_bf16 v[48:51],v[180:183],v[56:59],v[48:51]
s_waitcnt lgkmcnt(3)
buffer_store_dwordx4 v[96:99],v3,s[36:39],s73 offen offset:0 sc1
v_fma_f32 v92,v92,v119,v119
v_rcp_f32 v92,v92
v_mfma_f32_16x16x32_bf16 v[40:43],v[184:187],v[60:63],v[40:43]
ds_read_b128 v[80:83],v2 offset:64512
v_xor_b32 v2,65536,v2
v_mfma_f32_16x16x32_bf16 v[44:47],v[188:191],v[60:63],v[44:47]
s_waitcnt vmcnt(6)
v_fma_f32 v93,v118,v93,v117
v_mul_f32 v93,v37,v93
v_mfma_f32_16x16x32_bf16 v[48:51],v[192:195],v[60:63],v[48:51]
s_barrier
v_mfma_f32_16x16x32_bf16 v[40:43],v[196:199],v[64:67],v[40:43]
ds_read_b128 v[52:55],v2 offset:0
ds_read_b32 v13,v7 offset:256
v_mfma_f32_16x16x32_bf16 v[44:47],v[200:203],v[64:67],v[44:47]
ds_read_b64 v[112:113],v11 offset:128
v_fma_f32 v94,v118,v94,v117
v_mfma_f32_16x16x32_bf16 v[48:51],v[204:207],v[64:67],v[48:51]
s_waitcnt lgkmcnt(5)
v_exp_f32 v93,v93
v_mfma_f32_16x16x32_bf16 v[40:43],v[208:211],v[68:71],v[40:43]
ds_read_b128 v[56:59],v2 offset:1024
v_mul_f32 v94,v38,v94
v_mfma_f32_16x16x32_bf16 v[44:47],v[212:215],v[68:71],v[44:47]
s_waitcnt lgkmcnt(2)
v_exp_f32 v94,v94
v_mfma_f32_16x16x32_bf16 v[48:51],v[216:219],v[68:71],v[48:51]
s_waitcnt lgkmcnt(1)
v_cmp_eq_u32 vcc,s60,v13
v_fma_f32 v93,v93,v119,v119
v_rcp_f32 v93,v93
v_mfma_f32_16x16x32_bf16 v[40:43],v[220:223],v[72:75],v[40:43]
s_cmp_eq_u32 vcc_lo,-1
ds_read_b128 v[60:63],v2 offset:2048
v_fma_f32 v94,v94,v119,v119
v_mfma_f32_16x16x32_bf16 v[44:47],v[224:227],v[72:75],v[44:47]
s_cselect_b32 s66,s86,s59
v_rcp_f32 v94,v94
v_mfma_f32_16x16x32_bf16 v[48:51],v[228:231],v[72:75],v[48:51]
v_mul_f32 v116,s75,v112
v_fma_f32 v95,v118,v95,v117
v_mfma_f32_16x16x32_bf16 v[40:43],v[232:235],v[76:79],v[40:43]
s_lshr_b32 s61,s66,0
ds_read_b128 v[64:67],v2 offset:3072
v_mul_f32 v95,v39,v95
v_mfma_f32_16x16x32_bf16 v[44:47],v[236:239],v[76:79],v[44:47]
s_xor_b32 s64,s64,65536
v_exp_f32 v95,v95
v_mfma_f32_16x16x32_bf16 v[48:51],v[240:243],v[76:79],v[48:51]
s_cmp_lt_u32 s67,s59
v_fma_f32 v95,v95,v119,v119
v_rcp_f32 v95,v95
v_mfma_f32_16x16x32_bf16 v[40:43],v[244:247],v[80:83],v[40:43]
v_mul_f32 v36,v36,v92
v_mul_f32 v37,v37,v93
v_mul_f32 v38,v38,v94
v_mul_f32 v39,v39,v95
v_cvt_pk_bf16_f32 v100,v36,v37
v_cvt_pk_bf16_f32 v101,v38,v39
v_mfma_f32_16x16x32_bf16 v[44:47],v[248:251],v[80:83],v[44:47]
s_waitcnt lgkmcnt(2)
ds_read_b128 v[68:71],v2 offset:4096
buffer_store_dwordx2 v[100:101],v9,s[36:39],s73 offen offset:0 sc1
v_fma_f32 v113,v113,s75,v12
v_fma_f32 v113,-v116,v116,v113
v_max_f32 v113,v113,v12
v_rsq_f32 v112,v113
v_mfma_f32_16x16x32_bf16 v[48:51],v[252:255],v[80:83],v[48:51]
s_cbranch_scc1 Lg3_d0w0_%=
s_branch Lg3_slow0_%=
Lg3_d0w1_%=:
v_mfma_f32_16x16x32_bf16 v[28:31],a[0:3],v[52:55],0
s_mov_b32 m0,s62
ds_read_b128 v[72:75],v2 offset:5120
v_mul_f32 v117,s76,v112
v_mfma_f32_16x16x32_bf16 v[32:35],a[4:7],v[52:55],0
s_mul_i32 s73,s47,7
v_mul_f32 v118,v112,v112
v_mul_f32 v119,v113,v112
v_mfma_f32_16x16x32_bf16 v[36:39],a[8:11],v[52:55],0
buffer_load_dwordx4 v0,s[36:39],s61 offen offset:0 sc1 lds
v_mfma_f32_16x16x32_bf16 v[28:31],a[12:15],v[56:59],v[28:31]
s_lshr_b32 s73,s73,1
ds_read_b128 v[76:79],v2 offset:6144
v_mul_f32 v118,s77,v118
v_mfma_f32_16x16x32_bf16 v[32:35],a[16:19],v[56:59],v[32:35]
s_add_u32 s53,s52,s55
v_mul_f32 v118,v118,v117
v_fma_f32 v40,-v116,v16,v40
v_mfma_f32_16x16x32_bf16 v[36:39],a[20:23],v[56:59],v[36:39]
s_waitcnt lgkmcnt(3)
v_fma_f32 v41,-v116,v17,v41
v_fma_f32 v42,-v116,v18,v42
v_mfma_f32_16x16x32_bf16 v[28:31],a[24:27],v[60:63],v[28:31]
buffer_load_dwordx4 v0,s[36:39],s61 offen offset:1024 sc1 lds
v_mfma_f32_16x16x32_bf16 v[32:35],a[28:31],v[60:63],v[32:35]
s_cmp_eq_u32 s53,s56
ds_read_b128 v[80:83],v2 offset:7168
v_fma_f32 v43,-v116,v19,v43
v_mfma_f32_16x16x32_bf16 v[36:39],a[32:35],v[60:63],v[36:39]
s_cselect_b32 s53,s57,s53
v_mul_f32 v84,v40,v40
v_mul_f32 v85,v41,v41
v_mfma_f32_16x16x32_bf16 v[28:31],a[36:39],v[64:67],v[28:31]
s_cmp_eq_u32 s53,s58
ds_read_b128 v[52:55],v2 offset:8192
v_mul_f32 v86,v42,v42
v_mfma_f32_16x16x32_bf16 v[32:35],a[40:43],v[64:67],v[32:35]
buffer_load_dwordx4 v0,s[36:39],s61 offen offset:2048 sc1 lds
v_mfma_f32_16x16x32_bf16 v[36:39],a[44:47],v[64:67],v[36:39]
s_waitcnt lgkmcnt(3)
v_mul_f32 v87,v43,v43
v_fma_f32 v84,v118,v84,v117
v_mfma_f32_16x16x32_bf16 v[28:31],a[48:51],v[68:71],v[28:31]
s_cselect_b32 s53,s59,s53
ds_read_b128 v[56:59],v2 offset:9216
v_mul_f32 v84,v40,v84
v_mfma_f32_16x16x32_bf16 v[32:35],a[52:55],v[68:71],v[32:35]
s_cmp_ge_u32 s66,s59
v_exp_f32 v84,v84
v_mfma_f32_16x16x32_bf16 v[36:39],a[56:59],v[68:71],v[36:39]
buffer_load_dwordx4 v0,s[36:39],s61 offen offset:3072 sc1 lds
v_mfma_f32_16x16x32_bf16 v[28:31],a[60:63],v[72:75],v[28:31]
s_cselect_b32 s86,s49,s50
ds_read_b128 v[60:63],v2 offset:10240
v_fma_f32 v84,v84,v119,v119
v_mfma_f32_16x16x32_bf16 v[32:35],a[64:67],v[72:75],v[32:35]
s_add_u32 m0,s62,16384
v_rcp_f32 v84,v84
v_mfma_f32_16x16x32_bf16 v[36:39],a[68:71],v[72:75],v[36:39]
s_waitcnt lgkmcnt(3)
v_fma_f32 v85,v118,v85,v117
v_mul_f32 v85,v41,v85
v_mfma_f32_16x16x32_bf16 v[28:31],a[72:75],v[76:79],v[28:31]
s_lshr_b32 s72,s61,5
ds_read_b128 v[64:67],v2 offset:11264
v_fma_f32 v86,v118,v86,v117
v_mfma_f32_16x16x32_bf16 v[32:35],a[76:79],v[76:79],v[32:35]
s_mov_b32 s62,s63
v_exp_f32 v85,v85
v_mfma_f32_16x16x32_bf16 v[36:39],a[80:83],v[76:79],v[36:39]
v_mul_f32 v86,v42,v86
v_fma_f32 v85,v85,v119,v119
v_mfma_f32_16x16x32_bf16 v[28:31],a[84:87],v[80:83],v[28:31]
ds_read_b128 v[68:71],v2 offset:12288
v_fma_f32 v87,v118,v87,v117
v_mfma_f32_16x16x32_bf16 v[32:35],a[88:91],v[80:83],v[32:35]
v_exp_f32 v86,v86
v_mfma_f32_16x16x32_bf16 v[36:39],a[92:95],v[80:83],v[36:39]
s_waitcnt lgkmcnt(3)
v_rcp_f32 v85,v85
v_mfma_f32_16x16x32_bf16 v[28:31],a[96:99],v[52:55],v[28:31]
ds_read_b128 v[72:75],v2 offset:13312
v_mul_f32 v87,v43,v87
v_mfma_f32_16x16x32_bf16 v[32:35],a[100:103],v[52:55],v[32:35]
v_exp_f32 v87,v87
v_mfma_f32_16x16x32_bf16 v[36:39],a[104:107],v[52:55],v[36:39]
v_fma_f32 v86,v86,v119,v119
v_fma_f32 v87,v87,v119,v119
v_mfma_f32_16x16x32_bf16 v[28:31],a[108:111],v[56:59],v[28:31]
ds_read_b128 v[76:79],v2 offset:14336
v_mul_f32 v40,v40,v84
v_mfma_f32_16x16x32_bf16 v[32:35],a[112:115],v[56:59],v[32:35]
v_rcp_f32 v86,v86
v_mfma_f32_16x16x32_bf16 v[36:39],a[116:119],v[56:59],v[36:39]
s_waitcnt lgkmcnt(3)
v_rcp_f32 v87,v87
v_mfma_f32_16x16x32_bf16 v[28:31],a[120:123],v[60:63],v[28:31]
ds_read_b128 v[80:83],v2 offset:15360
v_mul_f32 v41,v41,v85
v_mfma_f32_16x16x32_bf16 v[32:35],a[124:127],v[60:63],v[32:35]
s_waitcnt vmcnt(6)
v_mul_f32 v42,v42,v86
v_mul_f32 v43,v43,v87
v_mfma_f32_16x16x32_bf16 v[36:39],a[128:131],v[60:63],v[36:39]
s_barrier
v_mfma_f32_16x16x32_bf16 v[28:31],a[132:135],v[64:67],v[28:31]
ds_read_b128 v[52:55],v2 offset:16384
v_add_f32_dpp v104,v104,v104 quad_perm:[1,0,3,2] row_mask:0xf bank_mask:0xf
v_mfma_f32_16x16x32_bf16 v[32:35],a[136:139],v[64:67],v[32:35])"
R"(
s_nop 0
v_add_f32_dpp v104,v104,v104 quad_perm:[2,3,0,1] row_mask:0xf bank_mask:0xf
v_cvt_pk_bf16_f32 v96,v40,v41
v_mfma_f32_16x16x32_bf16 v[36:39],a[140:143],v[64:67],v[36:39]
s_waitcnt lgkmcnt(3)
v_add_f32_dpp v104,v104,v104 row_half_mirror row_mask:0xf bank_mask:0xf
v_cvt_pk_bf16_f32 v97,v42,v43
v_mfma_f32_16x16x32_bf16 v[28:31],a[144:147],v[68:71],v[28:31]
ds_read_b128 v[56:59],v2 offset:17408
v_fma_f32 v44,-v116,v20,v44
v_mfma_f32_16x16x32_bf16 v[32:35],a[148:151],v[68:71],v[32:35]
v_fma_f32 v45,-v116,v21,v45
v_fma_f32 v46,-v116,v22,v46
v_mfma_f32_16x16x32_bf16 v[36:39],a[152:155],v[68:71],v[36:39]
v_fma_f32 v47,-v116,v23,v47
v_mul_f32 v88,v44,v44
v_mfma_f32_16x16x32_bf16 v[28:31],a[156:159],v[72:75],v[28:31]
ds_read_b128 v[60:63],v2 offset:18432
v_mul_f32 v89,v45,v45
v_mfma_f32_16x16x32_bf16 v[32:35],a[160:163],v[72:75],v[32:35]
v_mul_f32 v90,v46,v46
v_mul_f32 v91,v47,v47
v_mfma_f32_16x16x32_bf16 v[36:39],a[164:167],v[72:75],v[36:39]
s_waitcnt lgkmcnt(3)
v_fma_f32 v88,v118,v88,v117
v_mul_f32 v88,v44,v88
v_mfma_f32_16x16x32_bf16 v[28:31],a[168:171],v[76:79],v[28:31]
ds_read_b128 v[64:67],v2 offset:19456
v_fma_f32 v89,v118,v89,v117
v_mfma_f32_16x16x32_bf16 v[32:35],a[172:175],v[76:79],v[32:35]
v_exp_f32 v88,v88
v_mfma_f32_16x16x32_bf16 v[36:39],a[176:179],v[76:79],v[36:39]
buffer_load_dwordx4 v1,s[36:39],s61 offen offset:0 sc1 lds
v_mfma_f32_16x16x32_bf16 v[28:31],a[180:183],v[80:83],v[28:31]
ds_read_b128 v[68:71],v2 offset:20480
v_mul_f32 v89,v45,v89
v_mfma_f32_16x16x32_bf16 v[32:35],a[184:187],v[80:83],v[32:35]
ds_write_b32 v10,v104 offset:0
v_mfma_f32_16x16x32_bf16 v[36:39],a[188:191],v[80:83],v[36:39]
s_waitcnt lgkmcnt(4)
v_exp_f32 v89,v89
v_mfma_f32_16x16x32_bf16 v[28:31],a[192:195],v[52:55],v[28:31]
buffer_load_dwordx4 v1,s[36:39],s61 offen offset:1024 sc1 lds
v_mfma_f32_16x16x32_bf16 v[32:35],a[196:199],v[52:55],v[32:35]
ds_read_b128 v[72:75],v2 offset:21504
v_fma_f32 v88,v88,v119,v119
v_mfma_f32_16x16x32_bf16 v[36:39],a[200:203],v[52:55],v[36:39]
v_rcp_f32 v88,v88
v_mfma_f32_16x16x32_bf16 v[28:31],a[204:207],v[56:59],v[28:31]
ds_read_b128 v[76:79],v2 offset:22528
v_fma_f32 v89,v89,v119,v119
v_mfma_f32_16x16x32_bf16 v[32:35],a[208:211],v[56:59],v[32:35]
buffer_load_dwordx4 v1,s[36:39],s61 offen offset:2048 sc1 lds
v_mfma_f32_16x16x32_bf16 v[36:39],a[212:215],v[56:59],v[36:39]
s_waitcnt lgkmcnt(4)
v_rcp_f32 v89,v89
v_mfma_f32_16x16x32_bf16 v[28:31],a[216:219],v[60:63],v[28:31]
ds_read_b128 v[80:83],v2 offset:23552
v_fma_f32 v90,v118,v90,v117
v_mfma_f32_16x16x32_bf16 v[32:35],a[220:223],v[60:63],v[32:35]
v_mul_f32 v90,v46,v90
v_fma_f32 v91,v118,v91,v117
v_mfma_f32_16x16x32_bf16 v[36:39],a[224:227],v[60:63],v[36:39]
buffer_load_dwordx4 v1,s[36:39],s61 offen offset:3072 sc1 lds
v_mfma_f32_16x16x32_bf16 v[28:31],a[228:231],v[64:67],v[28:31]
buffer_load_dword v108,v8,s[36:39],s72 offen offset:0 sc1
v_mfma_f32_16x16x32_bf16 v[32:35],a[232:235],v[64:67],v[32:35]
ds_read_b128 v[52:55],v2 offset:24576
v_mul_f32 v91,v47,v91
v_mfma_f32_16x16x32_bf16 v[36:39],a[236:239],v[64:67],v[36:39]
s_waitcnt lgkmcnt(3)
v_exp_f32 v90,v90
v_mfma_f32_16x16x32_bf16 v[28:31],a[240:243],v[68:71],v[28:31]
ds_read_b128 v[56:59],v2 offset:25600
v_fma_f32 v90,v90,v119,v119
v_mfma_f32_16x16x32_bf16 v[32:35],a[244:247],v[68:71],v[32:35]
v_exp_f32 v91,v91
v_mfma_f32_16x16x32_bf16 v[36:39],a[248:251],v[68:71],v[36:39]
v_rcp_f32 v90,v90
v_mfma_f32_16x16x32_bf16 v[28:31],a[252:255],v[72:75],v[28:31]
ds_read_b128 v[60:63],v2 offset:26624
v_fma_f32 v91,v91,v119,v119
v_mfma_f32_16x16x32_bf16 v[32:35],v[128:131],v[72:75],v[32:35]
v_rcp_f32 v91,v91
v_mfma_f32_16x16x32_bf16 v[36:39],v[132:135],v[72:75],v[36:39]
s_waitcnt lgkmcnt(3)
v_mul_f32 v44,v44,v88
v_mul_f32 v45,v45,v89
v_mfma_f32_16x16x32_bf16 v[28:31],v[136:139],v[76:79],v[28:31]
ds_read_b128 v[64:67],v2 offset:27648
v_mul_f32 v46,v46,v90
v_mfma_f32_16x16x32_bf16 v[32:35],v[140:143],v[76:79],v[32:35]
v_mul_f32 v47,v47,v91
v_cvt_pk_bf16_f32 v98,v44,v45
v_mfma_f32_16x16x32_bf16 v[36:39],v[144:147],v[76:79],v[36:39]
v_cvt_pk_bf16_f32 v99,v46,v47
v_fma_f32 v48,-v116,v24,v48
v_mfma_f32_16x16x32_bf16 v[28:31],v[148:151],v[80:83],v[28:31]
ds_read_b128 v[68:71],v2 offset:28672
v_fma_f32 v49,-v116,v25,v49
v_mfma_f32_16x16x32_bf16 v[32:35],v[152:155],v[80:83],v[32:35]
v_fma_f32 v50,-v116,v26,v50
v_fma_f32 v51,-v116,v27,v51
v_mfma_f32_16x16x32_bf16 v[36:39],v[156:159],v[80:83],v[36:39]
s_waitcnt lgkmcnt(3)
v_mul_f32 v92,v48,v48
v_mul_f32 v93,v49,v49
v_mfma_f32_16x16x32_bf16 v[28:31],v[160:163],v[52:55],v[28:31]
ds_read_b128 v[72:75],v2 offset:29696
v_mul_f32 v94,v50,v50
v_mfma_f32_16x16x32_bf16 v[32:35],v[164:167],v[52:55],v[32:35]
v_mul_f32 v95,v51,v51
v_fma_f32 v92,v118,v92,v117
v_mfma_f32_16x16x32_bf16 v[36:39],v[168:171],v[52:55],v[36:39]
v_mul_f32 v92,v48,v92
v_fma_f32 v93,v118,v93,v117
v_mfma_f32_16x16x32_bf16 v[28:31],v[172:175],v[56:59],v[28:31]
buffer_store_dwordx4 v[96:99],v3,s[36:39],s73 offen offset:0 sc1
ds_read_b128 v[76:79],v2 offset:30720
v_exp_f32 v92,v92
v_mfma_f32_16x16x32_bf16 v[32:35],v[176:179],v[56:59],v[32:35]
v_mul_f32 v93,v49,v93
v_fma_f32 v92,v92,v119,v119
v_mfma_f32_16x16x32_bf16 v[36:39],v[180:183],v[56:59],v[36:39]
s_waitcnt lgkmcnt(3)
v_exp_f32 v93,v93
v_mfma_f32_16x16x32_bf16 v[28:31],v[184:187],v[60:63],v[28:31]
ds_read_b128 v[80:83],v2 offset:31744
v_fma_f32 v93,v93,v119,v119
v_mfma_f32_16x16x32_bf16 v[32:35],v[188:191],v[60:63],v[32:35]
s_waitcnt vmcnt(6)
v_rcp_f32 v92,v92
v_mfma_f32_16x16x32_bf16 v[36:39],v[192:195],v[60:63],v[36:39]
s_barrier
v_mfma_f32_16x16x32_bf16 v[28:31],v[196:199],v[64:67],v[28:31]
ds_read_b128 v[52:55],v2 offset:32768
ds_read_b32 v13,v7 offset:0
v_mfma_f32_16x16x32_bf16 v[32:35],v[200:203],v[64:67],v[32:35]
ds_read_b64 v[112:113],v11 offset:0
v_fma_f32 v94,v118,v94,v117
v_mfma_f32_16x16x32_bf16 v[36:39],v[204:207],v[64:67],v[36:39]
s_waitcnt lgkmcnt(5)
v_rcp_f32 v93,v93
v_mfma_f32_16x16x32_bf16 v[28:31],v[208:211],v[68:71],v[28:31]
ds_read_b128 v[56:59],v2 offset:33792
v_mul_f32 v94,v50,v94
v_mfma_f32_16x16x32_bf16 v[32:35],v[212:215],v[68:71],v[32:35]
s_waitcnt lgkmcnt(2)
v_exp_f32 v94,v94
v_mfma_f32_16x16x32_bf16 v[36:39],v[216:219],v[68:71],v[36:39]
s_waitcnt lgkmcnt(1)
v_cmp_eq_u32 vcc,s60,v13
v_fma_f32 v94,v94,v119,v119
v_rcp_f32 v94,v94
v_mfma_f32_16x16x32_bf16 v[28:31],v[220:223],v[72:75],v[28:31]
s_cmp_eq_u32 vcc_lo,-1
ds_read_b128 v[60:63],v2 offset:34816
v_mul_f32 v116,s75,v112
v_mfma_f32_16x16x32_bf16 v[32:35],v[224:227],v[72:75],v[32:35]
s_cselect_b32 s67,s86,s59
v_fma_f32 v95,v118,v95,v117
v_mul_f32 v95,v51,v95
v_mfma_f32_16x16x32_bf16 v[36:39],v[228:231],v[72:75],v[36:39]
v_exp_f32 v95,v95
v_mfma_f32_16x16x32_bf16 v[28:31],v[232:235],v[76:79],v[28:31]
s_lshr_b32 s61,s67,0
v_fma_f32 v95,v95,v119,v119
v_rcp_f32 v95,v95
v_mfma_f32_16x16x32_bf16 v[32:35],v[236:239],v[76:79],v[32:35]
s_xor_b32 s63,s63,65536
v_mul_f32 v48,v48,v92
v_mul_f32 v49,v49,v93
v_mul_f32 v50,v50,v94
v_mul_f32 v51,v51,v95
v_cvt_pk_bf16_f32 v100,v48,v49
v_cvt_pk_bf16_f32 v101,v50,v51
v_mfma_f32_16x16x32_bf16 v[36:39],v[240:243],v[76:79],v[36:39]
s_cmp_ge_u32 s66,s59
ds_read_b128 v[64:67],v2 offset:35840
buffer_store_dwordx2 v[100:101],v9,s[36:39],s73 offen offset:0 sc1
v_fma_f32 v113,v113,s75,v12
v_fma_f32 v113,-v116,v116,v113
v_max_f32 v113,v113,v12
v_mfma_f32_16x16x32_bf16 v[28:31],v[244:247],v[80:83],v[28:31]
ds_read_b128 v[68:71],v2 offset:36864
v_mfma_f32_16x16x32_bf16 v[32:35],v[248:251],v[80:83],v[32:35]
s_waitcnt lgkmcnt(3)
v_rsq_f32 v112,v113
v_mfma_f32_16x16x32_bf16 v[36:39],v[252:255],v[80:83],v[36:39]
s_cbranch_scc1 Lg3_slow1_%=
Lg3_d1w1_%=:
v_mfma_f32_16x16x32_bf16 v[40:43],a[0:3],v[52:55],0
s_mov_b32 m0,s62
ds_read_b128 v[72:75],v2 offset:37888
v_mul_f32 v117,s76,v112
v_mfma_f32_16x16x32_bf16 v[44:47],a[4:7],v[52:55],0
s_mul_i32 s73,s48,7
v_mul_f32 v118,v112,v112
v_mul_f32 v119,v113,v112
v_mfma_f32_16x16x32_bf16 v[48:51],a[8:11],v[52:55],0)"
R"(
buffer_load_dwordx4 v0,s[36:39],s61 offen offset:0 sc1 lds
v_mfma_f32_16x16x32_bf16 v[40:43],a[12:15],v[56:59],v[40:43]
s_lshr_b32 s73,s73,1
ds_read_b128 v[76:79],v2 offset:38912
v_mul_f32 v118,s77,v118
v_mfma_f32_16x16x32_bf16 v[44:47],a[16:19],v[56:59],v[44:47]
s_add_u32 s74,s53,s55
v_mul_f32 v118,v118,v117
v_fma_f32 v28,-v116,v16,v28
v_mfma_f32_16x16x32_bf16 v[48:51],a[20:23],v[56:59],v[48:51]
s_waitcnt lgkmcnt(3)
v_fma_f32 v29,-v116,v17,v29
v_fma_f32 v30,-v116,v18,v30
v_mfma_f32_16x16x32_bf16 v[40:43],a[24:27],v[60:63],v[40:43]
buffer_load_dwordx4 v0,s[36:39],s61 offen offset:1024 sc1 lds
v_mfma_f32_16x16x32_bf16 v[44:47],a[28:31],v[60:63],v[44:47]
s_cmp_eq_u32 s74,s56
ds_read_b128 v[80:83],v2 offset:39936
v_fma_f32 v31,-v116,v19,v31
v_mfma_f32_16x16x32_bf16 v[48:51],a[32:35],v[60:63],v[48:51]
s_cselect_b32 s74,s57,s74
v_mul_f32 v84,v28,v28
v_mul_f32 v85,v29,v29
v_mfma_f32_16x16x32_bf16 v[40:43],a[36:39],v[64:67],v[40:43]
s_cmp_eq_u32 s74,s58
ds_read_b128 v[52:55],v2 offset:40960
v_mul_f32 v86,v30,v30
v_mfma_f32_16x16x32_bf16 v[44:47],a[40:43],v[64:67],v[44:47]
buffer_load_dwordx4 v0,s[36:39],s61 offen offset:2048 sc1 lds
v_mfma_f32_16x16x32_bf16 v[48:51],a[44:47],v[64:67],v[48:51]
s_waitcnt lgkmcnt(3)
v_mul_f32 v87,v31,v31
v_fma_f32 v84,v118,v84,v117
v_mfma_f32_16x16x32_bf16 v[40:43],a[48:51],v[68:71],v[40:43]
s_cselect_b32 s74,s59,s74
ds_read_b128 v[56:59],v2 offset:41984
v_mul_f32 v84,v28,v84
v_mfma_f32_16x16x32_bf16 v[44:47],a[52:55],v[68:71],v[44:47]
s_cmp_ge_u32 s67,s59
v_exp_f32 v84,v84
v_mfma_f32_16x16x32_bf16 v[48:51],a[56:59],v[68:71],v[48:51]
buffer_load_dwordx4 v0,s[36:39],s61 offen offset:3072 sc1 lds
v_mfma_f32_16x16x32_bf16 v[40:43],a[60:63],v[72:75],v[40:43]
s_cselect_b32 s86,s50,s51
ds_read_b128 v[60:63],v2 offset:43008
v_fma_f32 v84,v84,v119,v119
v_mfma_f32_16x16x32_bf16 v[44:47],a[64:67],v[72:75],v[44:47]
s_add_u32 m0,s62,16384
v_rcp_f32 v84,v84
v_mfma_f32_16x16x32_bf16 v[48:51],a[68:71],v[72:75],v[48:51]
s_waitcnt lgkmcnt(3)
v_fma_f32 v85,v118,v85,v117
v_mul_f32 v85,v29,v85
v_mfma_f32_16x16x32_bf16 v[40:43],a[72:75],v[76:79],v[40:43]
s_lshr_b32 s72,s61,5
ds_read_b128 v[64:67],v2 offset:44032
v_fma_f32 v86,v118,v86,v117
v_mfma_f32_16x16x32_bf16 v[44:47],a[76:79],v[76:79],v[44:47]
s_mov_b32 s62,s64
v_exp_f32 v85,v85
v_mfma_f32_16x16x32_bf16 v[48:51],a[80:83],v[76:79],v[48:51]
s_mov_b64 s[44:45],s[46:47]
v_mul_f32 v86,v30,v86
v_fma_f32 v85,v85,v119,v119
v_mfma_f32_16x16x32_bf16 v[40:43],a[84:87],v[80:83],v[40:43]
s_mov_b64 s[46:47],s[48:49]
ds_read_b128 v[68:71],v2 offset:45056
v_fma_f32 v87,v118,v87,v117
v_mfma_f32_16x16x32_bf16 v[44:47],a[88:91],v[80:83],v[44:47]
s_mov_b64 s[48:49],s[50:51]
v_exp_f32 v86,v86
v_mfma_f32_16x16x32_bf16 v[48:51],a[92:95],v[80:83],v[48:51]
s_waitcnt lgkmcnt(3)
v_rcp_f32 v85,v85
v_mfma_f32_16x16x32_bf16 v[40:43],a[96:99],v[52:55],v[40:43]
s_mov_b64 s[50:51],s[52:53]
ds_read_b128 v[72:75],v2 offset:46080
v_mul_f32 v87,v31,v87
v_mfma_f32_16x16x32_bf16 v[44:47],a[100:103],v[52:55],v[44:47]
s_mov_b32 s52,s74
v_exp_f32 v87,v87
v_mfma_f32_16x16x32_bf16 v[48:51],a[104:107],v[52:55],v[48:51]
v_fma_f32 v86,v86,v119,v119
v_fma_f32 v87,v87,v119,v119
v_mfma_f32_16x16x32_bf16 v[40:43],a[108:111],v[56:59],v[40:43]
ds_read_b128 v[76:79],v2 offset:47104
v_mul_f32 v28,v28,v84
v_mfma_f32_16x16x32_bf16 v[44:47],a[112:115],v[56:59],v[44:47]
v_rcp_f32 v86,v86
v_mfma_f32_16x16x32_bf16 v[48:51],a[116:119],v[56:59],v[48:51]
s_waitcnt lgkmcnt(3)
v_rcp_f32 v87,v87
v_mfma_f32_16x16x32_bf16 v[40:43],a[120:123],v[60:63],v[40:43]
ds_read_b128 v[80:83],v2 offset:48128
v_mul_f32 v29,v29,v85
v_mfma_f32_16x16x32_bf16 v[44:47],a[124:127],v[60:63],v[44:47]
s_waitcnt vmcnt(6)
v_mul_f32 v30,v30,v86
v_mul_f32 v31,v31,v87
v_mfma_f32_16x16x32_bf16 v[48:51],a[128:131],v[60:63],v[48:51]
s_barrier
v_mfma_f32_16x16x32_bf16 v[40:43],a[132:135],v[64:67],v[40:43]
ds_read_b128 v[52:55],v2 offset:49152
v_add_f32_dpp v108,v108,v108 quad_perm:[1,0,3,2] row_mask:0xf bank_mask:0xf
v_mfma_f32_16x16x32_bf16 v[44:47],a[136:139],v[64:67],v[44:47]
s_nop 0
v_add_f32_dpp v108,v108,v108 quad_perm:[2,3,0,1] row_mask:0xf bank_mask:0xf
v_cvt_pk_bf16_f32 v96,v28,v29
v_mfma_f32_16x16x32_bf16 v[48:51],a[140:143],v[64:67],v[48:51]
s_waitcnt lgkmcnt(3)
v_add_f32_dpp v108,v108,v108 row_half_mirror row_mask:0xf bank_mask:0xf
v_cvt_pk_bf16_f32 v97,v30,v31
v_mfma_f32_16x16x32_bf16 v[40:43],a[144:147],v[68:71],v[40:43]
ds_read_b128 v[56:59],v2 offset:50176
v_fma_f32 v32,-v116,v20,v32
v_mfma_f32_16x16x32_bf16 v[44:47],a[148:151],v[68:71],v[44:47]
v_fma_f32 v33,-v116,v21,v33
v_fma_f32 v34,-v116,v22,v34
v_mfma_f32_16x16x32_bf16 v[48:51],a[152:155],v[68:71],v[48:51]
v_fma_f32 v35,-v116,v23,v35
v_mul_f32 v88,v32,v32
v_mfma_f32_16x16x32_bf16 v[40:43],a[156:159],v[72:75],v[40:43]
ds_read_b128 v[60:63],v2 offset:51200
v_mul_f32 v89,v33,v33
v_mfma_f32_16x16x32_bf16 v[44:47],a[160:163],v[72:75],v[44:47]
v_mul_f32 v90,v34,v34
v_mul_f32 v91,v35,v35
v_mfma_f32_16x16x32_bf16 v[48:51],a[164:167],v[72:75],v[48:51]
s_waitcnt lgkmcnt(3)
v_fma_f32 v88,v118,v88,v117
v_mul_f32 v88,v32,v88
v_mfma_f32_16x16x32_bf16 v[40:43],a[168:171],v[76:79],v[40:43]
ds_read_b128 v[64:67],v2 offset:52224
v_fma_f32 v89,v118,v89,v117
v_mfma_f32_16x16x32_bf16 v[44:47],a[172:175],v[76:79],v[44:47]
v_exp_f32 v88,v88
v_mfma_f32_16x16x32_bf16 v[48:51],a[176:179],v[76:79],v[48:51]
buffer_load_dwordx4 v1,s[36:39],s61 offen offset:0 sc1 lds
v_mfma_f32_16x16x32_bf16 v[40:43],a[180:183],v[80:83],v[40:43]
ds_read_b128 v[68:71],v2 offset:53248
v_mul_f32 v89,v33,v89
v_mfma_f32_16x16x32_bf16 v[44:47],a[184:187],v[80:83],v[44:47]
ds_write_b32 v10,v108 offset:128
v_mfma_f32_16x16x32_bf16 v[48:51],a[188:191],v[80:83],v[48:51]
s_waitcnt lgkmcnt(4)
v_exp_f32 v89,v89
v_mfma_f32_16x16x32_bf16 v[40:43],a[192:195],v[52:55],v[40:43]
buffer_load_dwordx4 v1,s[36:39],s61 offen offset:1024 sc1 lds
v_mfma_f32_16x16x32_bf16 v[44:47],a[196:199],v[52:55],v[44:47]
ds_read_b128 v[72:75],v2 offset:54272
v_fma_f32 v88,v88,v119,v119
v_mfma_f32_16x16x32_bf16 v[48:51],a[200:203],v[52:55],v[48:51]
v_rcp_f32 v88,v88
v_mfma_f32_16x16x32_bf16 v[40:43],a[204:207],v[56:59],v[40:43]
ds_read_b128 v[76:79],v2 offset:55296
v_fma_f32 v89,v89,v119,v119
v_mfma_f32_16x16x32_bf16 v[44:47],a[208:211],v[56:59],v[44:47]
buffer_load_dwordx4 v1,s[36:39],s61 offen offset:2048 sc1 lds
v_mfma_f32_16x16x32_bf16 v[48:51],a[212:215],v[56:59],v[48:51]
s_waitcnt lgkmcnt(4)
v_rcp_f32 v89,v89
v_mfma_f32_16x16x32_bf16 v[40:43],a[216:219],v[60:63],v[40:43]
ds_read_b128 v[80:83],v2 offset:56320
v_fma_f32 v90,v118,v90,v117
v_mfma_f32_16x16x32_bf16 v[44:47],a[220:223],v[60:63],v[44:47]
v_mul_f32 v90,v34,v90
v_fma_f32 v91,v118,v91,v117
v_mfma_f32_16x16x32_bf16 v[48:51],a[224:227],v[60:63],v[48:51]
buffer_load_dwordx4 v1,s[36:39],s61 offen offset:3072 sc1 lds
v_mfma_f32_16x16x32_bf16 v[40:43],a[228:231],v[64:67],v[40:43]
buffer_load_dword v104,v8,s[36:39],s72 offen offset:0 sc1
v_mfma_f32_16x16x32_bf16 v[44:47],a[232:235],v[64:67],v[44:47]
ds_read_b128 v[52:55],v2 offset:57344
v_mul_f32 v91,v35,v91
v_mfma_f32_16x16x32_bf16 v[48:51],a[236:239],v[64:67],v[48:51]
s_waitcnt lgkmcnt(3)
v_exp_f32 v90,v90
v_mfma_f32_16x16x32_bf16 v[40:43],a[240:243],v[68:71],v[40:43]
ds_read_b128 v[56:59],v2 offset:58368
v_fma_f32 v90,v90,v119,v119
v_mfma_f32_16x16x32_bf16 v[44:47],a[244:247],v[68:71],v[44:47]
v_exp_f32 v91,v91
v_mfma_f32_16x16x32_bf16 v[48:51],a[248:251],v[68:71],v[48:51]
v_rcp_f32 v90,v90
v_mfma_f32_16x16x32_bf16 v[40:43],a[252:255],v[72:75],v[40:43]
ds_read_b128 v[60:63],v2 offset:59392
v_fma_f32 v91,v91,v119,v119
v_mfma_f32_16x16x32_bf16 v[44:47],v[128:131],v[72:75],v[44:47]
v_rcp_f32 v91,v91
v_mfma_f32_16x16x32_bf16 v[48:51],v[132:135],v[72:75],v[48:51]
s_waitcnt lgkmcnt(3)
v_mul_f32 v32,v32,v88
v_mul_f32 v33,v33,v89
v_mfma_f32_16x16x32_bf16 v[40:43],v[136:139],v[76:79],v[40:43]
ds_read_b128 v[64:67],v2 offset:60416
v_mul_f32 v34,v34,v90
v_mfma_f32_16x16x32_bf16 v[44:47],v[140:143],v[76:79],v[44:47]
v_mul_f32 v35,v35,v91
v_cvt_pk_bf16_f32 v98,v32,v33
v_mfma_f32_16x16x32_bf16 v[48:51],v[144:147],v[76:79],v[48:51])"
R"(
v_cvt_pk_bf16_f32 v99,v34,v35
v_fma_f32 v36,-v116,v24,v36
v_mfma_f32_16x16x32_bf16 v[40:43],v[148:151],v[80:83],v[40:43]
ds_read_b128 v[68:71],v2 offset:61440
v_fma_f32 v37,-v116,v25,v37
v_mfma_f32_16x16x32_bf16 v[44:47],v[152:155],v[80:83],v[44:47]
v_fma_f32 v38,-v116,v26,v38
v_fma_f32 v39,-v116,v27,v39
v_mfma_f32_16x16x32_bf16 v[48:51],v[156:159],v[80:83],v[48:51]
s_waitcnt lgkmcnt(3)
v_mul_f32 v92,v36,v36
v_mul_f32 v93,v37,v37
v_mfma_f32_16x16x32_bf16 v[40:43],v[160:163],v[52:55],v[40:43]
ds_read_b128 v[72:75],v2 offset:62464
v_mul_f32 v94,v38,v38
v_mfma_f32_16x16x32_bf16 v[44:47],v[164:167],v[52:55],v[44:47]
v_mul_f32 v95,v39,v39
v_fma_f32 v92,v118,v92,v117
v_mfma_f32_16x16x32_bf16 v[48:51],v[168:171],v[52:55],v[48:51]
v_mul_f32 v92,v36,v92
v_fma_f32 v93,v118,v93,v117
v_mfma_f32_16x16x32_bf16 v[40:43],v[172:175],v[56:59],v[40:43]
buffer_store_dwordx4 v[96:99],v3,s[36:39],s73 offen offset:0 sc1
ds_read_b128 v[76:79],v2 offset:63488
v_exp_f32 v92,v92
v_mfma_f32_16x16x32_bf16 v[44:47],v[176:179],v[56:59],v[44:47]
v_mul_f32 v93,v37,v93
v_fma_f32 v92,v92,v119,v119
v_mfma_f32_16x16x32_bf16 v[48:51],v[180:183],v[56:59],v[48:51]
s_waitcnt lgkmcnt(3)
v_exp_f32 v93,v93
v_mfma_f32_16x16x32_bf16 v[40:43],v[184:187],v[60:63],v[40:43]
ds_read_b128 v[80:83],v2 offset:64512
v_fma_f32 v93,v93,v119,v119
v_mfma_f32_16x16x32_bf16 v[44:47],v[188:191],v[60:63],v[44:47]
s_waitcnt vmcnt(6)
v_xor_b32 v2,65536,v2
v_fma_f32 v94,v118,v94,v117
v_mfma_f32_16x16x32_bf16 v[48:51],v[192:195],v[60:63],v[48:51]
s_barrier
v_mfma_f32_16x16x32_bf16 v[40:43],v[196:199],v[64:67],v[40:43]
ds_read_b128 v[52:55],v2 offset:0
ds_read_b32 v13,v7 offset:256
v_mfma_f32_16x16x32_bf16 v[44:47],v[200:203],v[64:67],v[44:47]
ds_read_b64 v[112:113],v11 offset:128
v_fma_f32 v95,v118,v95,v117
v_mfma_f32_16x16x32_bf16 v[48:51],v[204:207],v[64:67],v[48:51]
s_waitcnt lgkmcnt(5)
v_rcp_f32 v92,v92
v_mfma_f32_16x16x32_bf16 v[40:43],v[208:211],v[68:71],v[40:43]
ds_read_b128 v[56:59],v2 offset:1024
v_mul_f32 v94,v38,v94
v_mfma_f32_16x16x32_bf16 v[44:47],v[212:215],v[68:71],v[44:47]
s_waitcnt lgkmcnt(2)
v_exp_f32 v94,v94
v_mfma_f32_16x16x32_bf16 v[48:51],v[216:219],v[68:71],v[48:51]
s_waitcnt lgkmcnt(1)
v_cmp_eq_u32 vcc,s60,v13
v_rcp_f32 v93,v93
v_mul_f32 v95,v39,v95
v_mfma_f32_16x16x32_bf16 v[40:43],v[220:223],v[72:75],v[40:43]
s_cmp_eq_u32 vcc_lo,-1
ds_read_b128 v[60:63],v2 offset:2048
v_fma_f32 v94,v94,v119,v119
v_mfma_f32_16x16x32_bf16 v[44:47],v[224:227],v[72:75],v[44:47]
s_cselect_b32 s66,s86,s59
v_exp_f32 v95,v95
v_mfma_f32_16x16x32_bf16 v[48:51],v[228:231],v[72:75],v[48:51]
v_rcp_f32 v94,v94
v_mfma_f32_16x16x32_bf16 v[40:43],v[232:235],v[76:79],v[40:43]
s_lshr_b32 s61,s66,0
v_fma_f32 v95,v95,v119,v119
v_rcp_f32 v95,v95
v_mfma_f32_16x16x32_bf16 v[44:47],v[236:239],v[76:79],v[44:47]
s_xor_b32 s64,s64,65536
v_mul_f32 v36,v36,v92
v_mul_f32 v37,v37,v93
v_mul_f32 v38,v38,v94
v_mul_f32 v39,v39,v95
v_cvt_pk_bf16_f32 v100,v36,v37
v_cvt_pk_bf16_f32 v101,v38,v39
v_mfma_f32_16x16x32_bf16 v[48:51],v[240:243],v[76:79],v[48:51]
s_cmp_lt_u32 s67,s59
ds_read_b128 v[64:67],v2 offset:3072
buffer_store_dwordx2 v[100:101],v9,s[36:39],s73 offen offset:0 sc1
v_mul_f32 v116,s75,v112
v_fma_f32 v113,v113,s75,v12
v_fma_f32 v113,-v116,v116,v113
v_mfma_f32_16x16x32_bf16 v[40:43],v[244:247],v[80:83],v[40:43]
ds_read_b128 v[68:71],v2 offset:4096
v_max_f32 v113,v113,v12
v_mfma_f32_16x16x32_bf16 v[44:47],v[248:251],v[80:83],v[44:47]
s_waitcnt lgkmcnt(3)
v_rsq_f32 v112,v113
v_mfma_f32_16x16x32_bf16 v[48:51],v[252:255],v[80:83],v[48:51]
s_cbranch_scc1 Lg3_d0w1_%=
s_branch Lg3_slow0_%=
Lg3_d0w2_%=:
v_mfma_f32_16x16x32_bf16 v[28:31],a[0:3],v[52:55],0
s_mov_b32 m0,s62
ds_read_b128 v[72:75],v2 offset:5120
v_mul_f32 v117,s76,v112
v_mfma_f32_16x16x32_bf16 v[32:35],a[4:7],v[52:55],0
s_mul_i32 s73,s47,7
v_mul_f32 v118,v112,v112
v_mul_f32 v119,v113,v112
v_mfma_f32_16x16x32_bf16 v[36:39],a[8:11],v[52:55],0
s_lshr_b32 s73,s73,1
v_mul_f32 v118,s77,v118
v_mul_f32 v118,v118,v117
v_mfma_f32_16x16x32_bf16 v[28:31],a[12:15],v[56:59],v[28:31]
buffer_load_dwordx4 v0,s[36:39],s61 offen offset:0 sc1 lds
v_mfma_f32_16x16x32_bf16 v[32:35],a[16:19],v[56:59],v[32:35]
s_add_u32 s53,s52,s55
ds_read_b128 v[76:79],v2 offset:6144
v_fma_f32 v40,-v116,v16,v40
v_mfma_f32_16x16x32_bf16 v[36:39],a[20:23],v[56:59],v[36:39]
s_waitcnt lgkmcnt(3)
v_fma_f32 v41,-v116,v17,v41
v_fma_f32 v42,-v116,v18,v42
v_mfma_f32_16x16x32_bf16 v[28:31],a[24:27],v[60:63],v[28:31]
s_cmp_eq_u32 s53,s56
ds_read_b128 v[80:83],v2 offset:7168
v_fma_f32 v43,-v116,v19,v43
v_mfma_f32_16x16x32_bf16 v[32:35],a[28:31],v[60:63],v[32:35]
buffer_load_dwordx4 v0,s[36:39],s61 offen offset:1024 sc1 lds
v_mfma_f32_16x16x32_bf16 v[36:39],a[32:35],v[60:63],v[36:39]
s_cselect_b32 s53,s57,s53
v_mul_f32 v84,v40,v40
v_mul_f32 v85,v41,v41
v_mfma_f32_16x16x32_bf16 v[28:31],a[36:39],v[64:67],v[28:31]
s_cmp_eq_u32 s53,s58
ds_read_b128 v[52:55],v2 offset:8192
v_mul_f32 v86,v42,v42
v_mfma_f32_16x16x32_bf16 v[32:35],a[40:43],v[64:67],v[32:35]
s_cselect_b32 s53,s59,s53
v_mul_f32 v87,v43,v43
v_fma_f32 v84,v118,v84,v117
v_mfma_f32_16x16x32_bf16 v[36:39],a[44:47],v[64:67],v[36:39]
s_waitcnt lgkmcnt(3)
buffer_load_dwordx4 v0,s[36:39],s61 offen offset:2048 sc1 lds
v_mfma_f32_16x16x32_bf16 v[28:31],a[48:51],v[68:71],v[28:31]
s_cmp_ge_u32 s66,s59
ds_read_b128 v[56:59],v2 offset:9216
v_mul_f32 v84,v40,v84
v_mfma_f32_16x16x32_bf16 v[32:35],a[52:55],v[68:71],v[32:35]
s_cselect_b32 s86,s49,s50
v_exp_f32 v84,v84
v_mfma_f32_16x16x32_bf16 v[36:39],a[56:59],v[68:71],v[36:39]
v_fma_f32 v84,v84,v119,v119
v_fma_f32 v85,v118,v85,v117
v_mfma_f32_16x16x32_bf16 v[28:31],a[60:63],v[72:75],v[28:31]
buffer_load_dwordx4 v0,s[36:39],s61 offen offset:3072 sc1 lds
v_mfma_f32_16x16x32_bf16 v[32:35],a[64:67],v[72:75],v[32:35]
s_add_u32 m0,s62,16384
ds_read_b128 v[60:63],v2 offset:10240
v_mul_f32 v85,v41,v85
v_mfma_f32_16x16x32_bf16 v[36:39],a[68:71],v[72:75],v[36:39]
s_waitcnt lgkmcnt(3)
v_exp_f32 v85,v85
v_mfma_f32_16x16x32_bf16 v[28:31],a[72:75],v[76:79],v[28:31]
s_lshr_b32 s72,s61,5
ds_read_b128 v[64:67],v2 offset:11264
v_fma_f32 v85,v85,v119,v119
v_mfma_f32_16x16x32_bf16 v[32:35],a[76:79],v[76:79],v[32:35]
s_mov_b32 s62,s63
v_rcp_f32 v84,v84
v_mfma_f32_16x16x32_bf16 v[36:39],a[80:83],v[76:79],v[36:39]
v_rcp_f32 v85,v85
v_mfma_f32_16x16x32_bf16 v[28:31],a[84:87],v[80:83],v[28:31]
ds_read_b128 v[68:71],v2 offset:12288
v_fma_f32 v86,v118,v86,v117
v_mfma_f32_16x16x32_bf16 v[32:35],a[88:91],v[80:83],v[32:35]
v_mul_f32 v86,v42,v86
v_fma_f32 v87,v118,v87,v117
v_mfma_f32_16x16x32_bf16 v[36:39],a[92:95],v[80:83],v[36:39]
s_waitcnt lgkmcnt(3)
v_exp_f32 v86,v86
v_mfma_f32_16x16x32_bf16 v[28:31],a[96:99],v[52:55],v[28:31]
ds_read_b128 v[72:75],v2 offset:13312
v_mul_f32 v87,v43,v87
v_mfma_f32_16x16x32_bf16 v[32:35],a[100:103],v[52:55],v[32:35]
v_exp_f32 v87,v87
v_mfma_f32_16x16x32_bf16 v[36:39],a[104:107],v[52:55],v[36:39]
v_fma_f32 v86,v86,v119,v119
v_fma_f32 v87,v87,v119,v119
v_mfma_f32_16x16x32_bf16 v[28:31],a[108:111],v[56:59],v[28:31]
ds_read_b128 v[76:79],v2 offset:14336
v_mul_f32 v40,v40,v84
v_mfma_f32_16x16x32_bf16 v[32:35],a[112:115],v[56:59],v[32:35]
v_rcp_f32 v86,v86
v_mfma_f32_16x16x32_bf16 v[36:39],a[116:119],v[56:59],v[36:39]
s_waitcnt lgkmcnt(3)
v_rcp_f32 v87,v87
v_mfma_f32_16x16x32_bf16 v[28:31],a[120:123],v[60:63],v[28:31]
ds_read_b128 v[80:83],v2 offset:15360
v_mul_f32 v41,v41,v85
v_mfma_f32_16x16x32_bf16 v[32:35],a[124:127],v[60:63],v[32:35]
s_waitcnt vmcnt(6)
v_mul_f32 v42,v42,v86)"
R"(
v_mul_f32 v43,v43,v87
v_mfma_f32_16x16x32_bf16 v[36:39],a[128:131],v[60:63],v[36:39]
s_barrier
v_mfma_f32_16x16x32_bf16 v[28:31],a[132:135],v[64:67],v[28:31]
ds_read_b128 v[52:55],v2 offset:16384
v_add_f32_dpp v104,v104,v104 quad_perm:[1,0,3,2] row_mask:0xf bank_mask:0xf
v_mfma_f32_16x16x32_bf16 v[32:35],a[136:139],v[64:67],v[32:35]
s_nop 0
v_add_f32_dpp v104,v104,v104 quad_perm:[2,3,0,1] row_mask:0xf bank_mask:0xf
v_cvt_pk_bf16_f32 v96,v40,v41
v_mfma_f32_16x16x32_bf16 v[36:39],a[140:143],v[64:67],v[36:39]
s_waitcnt lgkmcnt(3)
v_add_f32_dpp v104,v104,v104 row_half_mirror row_mask:0xf bank_mask:0xf
v_cvt_pk_bf16_f32 v97,v42,v43
v_mfma_f32_16x16x32_bf16 v[28:31],a[144:147],v[68:71],v[28:31]
ds_read_b128 v[56:59],v2 offset:17408
v_fma_f32 v44,-v116,v20,v44
v_mfma_f32_16x16x32_bf16 v[32:35],a[148:151],v[68:71],v[32:35]
v_fma_f32 v45,-v116,v21,v45
v_fma_f32 v46,-v116,v22,v46
v_mfma_f32_16x16x32_bf16 v[36:39],a[152:155],v[68:71],v[36:39]
v_fma_f32 v47,-v116,v23,v47
v_mul_f32 v88,v44,v44
v_mfma_f32_16x16x32_bf16 v[28:31],a[156:159],v[72:75],v[28:31]
ds_read_b128 v[60:63],v2 offset:18432
v_mul_f32 v89,v45,v45
v_mfma_f32_16x16x32_bf16 v[32:35],a[160:163],v[72:75],v[32:35]
v_mul_f32 v90,v46,v46
v_mul_f32 v91,v47,v47
v_mfma_f32_16x16x32_bf16 v[36:39],a[164:167],v[72:75],v[36:39]
s_waitcnt lgkmcnt(3)
v_fma_f32 v88,v118,v88,v117
v_mul_f32 v88,v44,v88
v_mfma_f32_16x16x32_bf16 v[28:31],a[168:171],v[76:79],v[28:31]
ds_read_b128 v[64:67],v2 offset:19456
v_fma_f32 v89,v118,v89,v117
v_mfma_f32_16x16x32_bf16 v[32:35],a[172:175],v[76:79],v[32:35]
v_exp_f32 v88,v88
v_mfma_f32_16x16x32_bf16 v[36:39],a[176:179],v[76:79],v[36:39]
v_mul_f32 v89,v45,v89
v_fma_f32 v88,v88,v119,v119
v_mfma_f32_16x16x32_bf16 v[28:31],a[180:183],v[80:83],v[28:31]
buffer_load_dwordx4 v1,s[36:39],s61 offen offset:0 sc1 lds
v_mfma_f32_16x16x32_bf16 v[32:35],a[184:187],v[80:83],v[32:35]
ds_write_b32 v10,v104 offset:0
v_mfma_f32_16x16x32_bf16 v[36:39],a[188:191],v[80:83],v[36:39]
s_waitcnt lgkmcnt(3)
ds_read_b128 v[68:71],v2 offset:20480
v_exp_f32 v89,v89
v_mfma_f32_16x16x32_bf16 v[28:31],a[192:195],v[52:55],v[28:31]
ds_read_b128 v[72:75],v2 offset:21504
v_fma_f32 v89,v89,v119,v119
v_mfma_f32_16x16x32_bf16 v[32:35],a[196:199],v[52:55],v[32:35]
buffer_load_dwordx4 v1,s[36:39],s61 offen offset:1024 sc1 lds
v_mfma_f32_16x16x32_bf16 v[36:39],a[200:203],v[52:55],v[36:39]
v_rcp_f32 v88,v88
v_mfma_f32_16x16x32_bf16 v[28:31],a[204:207],v[56:59],v[28:31]
ds_read_b128 v[76:79],v2 offset:22528
v_fma_f32 v90,v118,v90,v117
v_mfma_f32_16x16x32_bf16 v[32:35],a[208:211],v[56:59],v[32:35]
v_rcp_f32 v89,v89
v_mfma_f32_16x16x32_bf16 v[36:39],a[212:215],v[56:59],v[36:39]
s_waitcnt lgkmcnt(4)
buffer_load_dwordx4 v1,s[36:39],s61 offen offset:2048 sc1 lds
v_mfma_f32_16x16x32_bf16 v[28:31],a[216:219],v[60:63],v[28:31]
ds_read_b128 v[80:83],v2 offset:23552
v_mul_f32 v90,v46,v90
v_mfma_f32_16x16x32_bf16 v[32:35],a[220:223],v[60:63],v[32:35]
v_exp_f32 v90,v90
v_mfma_f32_16x16x32_bf16 v[36:39],a[224:227],v[60:63],v[36:39]
v_fma_f32 v90,v90,v119,v119
v_fma_f32 v91,v118,v91,v117
v_mfma_f32_16x16x32_bf16 v[28:31],a[228:231],v[64:67],v[28:31]
buffer_load_dwordx4 v1,s[36:39],s61 offen offset:3072 sc1 lds
v_mfma_f32_16x16x32_bf16 v[32:35],a[232:235],v[64:67],v[32:35]
buffer_load_dword v108,v8,s[36:39],s72 offen offset:0 sc1
v_mfma_f32_16x16x32_bf16 v[36:39],a[236:239],v[64:67],v[36:39]
s_waitcnt lgkmcnt(2)
ds_read_b128 v[52:55],v2 offset:24576
v_mul_f32 v91,v47,v91
v_mfma_f32_16x16x32_bf16 v[28:31],a[240:243],v[68:71],v[28:31]
ds_read_b128 v[56:59],v2 offset:25600
v_mul_f32 v44,v44,v88
v_mfma_f32_16x16x32_bf16 v[32:35],a[244:247],v[68:71],v[32:35]
v_exp_f32 v91,v91
v_mfma_f32_16x16x32_bf16 v[36:39],a[248:251],v[68:71],v[36:39]
v_rcp_f32 v90,v90
v_mfma_f32_16x16x32_bf16 v[28:31],a[252:255],v[72:75],v[28:31]
ds_read_b128 v[60:63],v2 offset:26624
v_fma_f32 v91,v91,v119,v119
v_mfma_f32_16x16x32_bf16 v[32:35],v[128:131],v[72:75],v[32:35]
v_rcp_f32 v91,v91
v_mfma_f32_16x16x32_bf16 v[36:39],v[132:135],v[72:75],v[36:39]
s_waitcnt lgkmcnt(3)
v_mul_f32 v45,v45,v89
v_mul_f32 v46,v46,v90
v_mfma_f32_16x16x32_bf16 v[28:31],v[136:139],v[76:79],v[28:31]
ds_read_b128 v[64:67],v2 offset:27648
v_mul_f32 v47,v47,v91
v_mfma_f32_16x16x32_bf16 v[32:35],v[140:143],v[76:79],v[32:35]
v_cvt_pk_bf16_f32 v98,v44,v45
v_cvt_pk_bf16_f32 v99,v46,v47
v_mfma_f32_16x16x32_bf16 v[36:39],v[144:147],v[76:79],v[36:39]
v_fma_f32 v48,-v116,v24,v48
v_fma_f32 v49,-v116,v25,v49
v_mfma_f32_16x16x32_bf16 v[28:31],v[148:151],v[80:83],v[28:31]
ds_read_b128 v[68:71],v2 offset:28672
v_fma_f32 v50,-v116,v26,v50
v_mfma_f32_16x16x32_bf16 v[32:35],v[152:155],v[80:83],v[32:35]
v_fma_f32 v51,-v116,v27,v51
v_mul_f32 v92,v48,v48
v_mfma_f32_16x16x32_bf16 v[36:39],v[156:159],v[80:83],v[36:39]
s_waitcnt lgkmcnt(3)
v_mul_f32 v93,v49,v49
v_mul_f32 v94,v50,v50
v_mfma_f32_16x16x32_bf16 v[28:31],v[160:163],v[52:55],v[28:31]
ds_read_b128 v[72:75],v2 offset:29696
v_mul_f32 v95,v51,v51
v_mfma_f32_16x16x32_bf16 v[32:35],v[164:167],v[52:55],v[32:35]
v_fma_f32 v92,v118,v92,v117
v_mul_f32 v92,v48,v92
v_mfma_f32_16x16x32_bf16 v[36:39],v[168:171],v[52:55],v[36:39]
buffer_store_dwordx4 v[96:99],v3,s[36:39],s73 offen offset:0 sc1
v_exp_f32 v92,v92
v_fma_f32 v93,v118,v93,v117
v_mfma_f32_16x16x32_bf16 v[28:31],v[172:175],v[56:59],v[28:31]
ds_read_b128 v[76:79],v2 offset:30720
v_mul_f32 v93,v49,v93
v_mfma_f32_16x16x32_bf16 v[32:35],v[176:179],v[56:59],v[32:35]
v_exp_f32 v93,v93
v_mfma_f32_16x16x32_bf16 v[36:39],v[180:183],v[56:59],v[36:39]
s_waitcnt lgkmcnt(3)
v_fma_f32 v92,v92,v119,v119
v_fma_f32 v93,v93,v119,v119
v_mfma_f32_16x16x32_bf16 v[28:31],v[184:187],v[60:63],v[28:31]
ds_read_b128 v[80:83],v2 offset:31744
v_fma_f32 v94,v118,v94,v117
v_mfma_f32_16x16x32_bf16 v[32:35],v[188:191],v[60:63],v[32:35]
s_waitcnt vmcnt(6)
v_rcp_f32 v92,v92
v_mfma_f32_16x16x32_bf16 v[36:39],v[192:195],v[60:63],v[36:39]
s_barrier
v_mfma_f32_16x16x32_bf16 v[28:31],v[196:199],v[64:67],v[28:31]
ds_read_b128 v[52:55],v2 offset:32768
ds_read_b32 v13,v7 offset:0
v_mfma_f32_16x16x32_bf16 v[32:35],v[200:203],v[64:67],v[32:35]
ds_read_b64 v[112:113],v11 offset:0
v_mul_f32 v94,v50,v94
v_mfma_f32_16x16x32_bf16 v[36:39],v[204:207],v[64:67],v[36:39]
s_waitcnt lgkmcnt(5)
v_exp_f32 v94,v94
v_mfma_f32_16x16x32_bf16 v[28:31],v[208:211],v[68:71],v[28:31]
ds_read_b128 v[56:59],v2 offset:33792
v_fma_f32 v94,v94,v119,v119
v_mfma_f32_16x16x32_bf16 v[32:35],v[212:215],v[68:71],v[32:35]
s_waitcnt lgkmcnt(2)
v_rcp_f32 v93,v93
v_mfma_f32_16x16x32_bf16 v[36:39],v[216:219],v[68:71],v[36:39]
s_waitcnt lgkmcnt(1)
v_cmp_eq_u32 vcc,s60,v13
v_rcp_f32 v94,v94
v_fma_f32 v95,v118,v95,v117
v_mfma_f32_16x16x32_bf16 v[28:31],v[220:223],v[72:75],v[28:31]
s_cmp_eq_u32 vcc_lo,-1
ds_read_b128 v[60:63],v2 offset:34816
v_mul_f32 v95,v51,v95
v_mfma_f32_16x16x32_bf16 v[32:35],v[224:227],v[72:75],v[32:35]
s_cselect_b32 s67,s86,s59
v_exp_f32 v95,v95
v_mfma_f32_16x16x32_bf16 v[36:39],v[228:231],v[72:75],v[36:39]
v_fma_f32 v95,v95,v119,v119
v_rcp_f32 v95,v95
v_mfma_f32_16x16x32_bf16 v[28:31],v[232:235],v[76:79],v[28:31]
s_lshr_b32 s61,s67,0
v_mul_f32 v48,v48,v92
v_mul_f32 v49,v49,v93
v_mul_f32 v50,v50,v94
v_mul_f32 v51,v51,v95
v_cvt_pk_bf16_f32 v100,v48,v49
v_cvt_pk_bf16_f32 v101,v50,v51
v_mfma_f32_16x16x32_bf16 v[32:35],v[236:239],v[76:79],v[32:35]
buffer_store_dwordx2 v[100:101],v9,s[36:39],s73 offen offset:0 sc1
ds_read_b128 v[64:67],v2 offset:35840
v_mul_f32 v116,s75,v112
v_fma_f32 v113,v113,s75,v12
v_mfma_f32_16x16x32_bf16 v[36:39],v[240:243],v[76:79],v[36:39]
s_xor_b32 s63,s63,65536
v_fma_f32 v113,-v116,v116,v113
v_max_f32 v113,v113,v12
v_mfma_f32_16x16x32_bf16 v[28:31],v[244:247],v[80:83],v[28:31]
s_cmp_ge_u32 s66,s59
ds_read_b128 v[68:71],v2 offset:36864
v_mfma_f32_16x16x32_bf16 v[32:35],v[248:251],v[80:83],v[32:35]
s_waitcnt lgkmcnt(3)
v_rsq_f32 v112,v113
v_mfma_f32_16x16x32_bf16 v[36:39],v[252:255],v[80:83],v[36:39]
s_cbranch_scc1 Lg3_slow1_%=
Lg3_d1w2_%=:
v_mfma_f32_16x16x32_bf16 v[40:43],a[0:3],v[52:55],0
s_mov_b32 m0,s62)"
R"(
ds_read_b128 v[72:75],v2 offset:37888
v_mul_f32 v117,s76,v112
v_mfma_f32_16x16x32_bf16 v[44:47],a[4:7],v[52:55],0
s_mul_i32 s73,s48,7
v_mul_f32 v118,v112,v112
v_mul_f32 v119,v113,v112
v_mfma_f32_16x16x32_bf16 v[48:51],a[8:11],v[52:55],0
s_lshr_b32 s73,s73,1
v_mul_f32 v118,s77,v118
v_mul_f32 v118,v118,v117
v_mfma_f32_16x16x32_bf16 v[40:43],a[12:15],v[56:59],v[40:43]
buffer_load_dwordx4 v0,s[36:39],s61 offen offset:0 sc1 lds
v_mfma_f32_16x16x32_bf16 v[44:47],a[16:19],v[56:59],v[44:47]
s_add_u32 s74,s53,s55
ds_read_b128 v[76:79],v2 offset:38912
v_fma_f32 v28,-v116,v16,v28
v_mfma_f32_16x16x32_bf16 v[48:51],a[20:23],v[56:59],v[48:51]
s_waitcnt lgkmcnt(3)
v_fma_f32 v29,-v116,v17,v29
v_fma_f32 v30,-v116,v18,v30
v_mfma_f32_16x16x32_bf16 v[40:43],a[24:27],v[60:63],v[40:43]
s_cmp_eq_u32 s74,s56
ds_read_b128 v[80:83],v2 offset:39936
v_fma_f32 v31,-v116,v19,v31
v_mfma_f32_16x16x32_bf16 v[44:47],a[28:31],v[60:63],v[44:47]
buffer_load_dwordx4 v0,s[36:39],s61 offen offset:1024 sc1 lds
v_mfma_f32_16x16x32_bf16 v[48:51],a[32:35],v[60:63],v[48:51]
s_cselect_b32 s74,s57,s74
v_mul_f32 v84,v28,v28
v_mul_f32 v85,v29,v29
v_mfma_f32_16x16x32_bf16 v[40:43],a[36:39],v[64:67],v[40:43]
s_cmp_eq_u32 s74,s58
ds_read_b128 v[52:55],v2 offset:40960
v_mul_f32 v86,v30,v30
v_mfma_f32_16x16x32_bf16 v[44:47],a[40:43],v[64:67],v[44:47]
s_cselect_b32 s74,s59,s74
v_mul_f32 v87,v31,v31
v_fma_f32 v84,v118,v84,v117
v_mfma_f32_16x16x32_bf16 v[48:51],a[44:47],v[64:67],v[48:51]
s_waitcnt lgkmcnt(3)
buffer_load_dwordx4 v0,s[36:39],s61 offen offset:2048 sc1 lds
v_mfma_f32_16x16x32_bf16 v[40:43],a[48:51],v[68:71],v[40:43]
s_cmp_ge_u32 s67,s59
ds_read_b128 v[56:59],v2 offset:41984
v_mul_f32 v84,v28,v84
v_mfma_f32_16x16x32_bf16 v[44:47],a[52:55],v[68:71],v[44:47]
s_cselect_b32 s86,s50,s51
v_exp_f32 v84,v84
v_mfma_f32_16x16x32_bf16 v[48:51],a[56:59],v[68:71],v[48:51]
s_mov_b64 s[44:45],s[46:47]
v_fma_f32 v84,v84,v119,v119
v_fma_f32 v85,v118,v85,v117
v_mfma_f32_16x16x32_bf16 v[40:43],a[60:63],v[72:75],v[40:43]
buffer_load_dwordx4 v0,s[36:39],s61 offen offset:3072 sc1 lds
v_mfma_f32_16x16x32_bf16 v[44:47],a[64:67],v[72:75],v[44:47]
s_add_u32 m0,s62,16384
ds_read_b128 v[60:63],v2 offset:43008
v_mul_f32 v85,v29,v85
v_mfma_f32_16x16x32_bf16 v[48:51],a[68:71],v[72:75],v[48:51]
s_waitcnt lgkmcnt(3)
v_exp_f32 v85,v85
v_mfma_f32_16x16x32_bf16 v[40:43],a[72:75],v[76:79],v[40:43]
s_lshr_b32 s72,s61,5
ds_read_b128 v[64:67],v2 offset:44032
v_fma_f32 v85,v85,v119,v119
v_mfma_f32_16x16x32_bf16 v[44:47],a[76:79],v[76:79],v[44:47]
s_mov_b32 s62,s64
v_rcp_f32 v84,v84
v_mfma_f32_16x16x32_bf16 v[48:51],a[80:83],v[76:79],v[48:51]
s_mov_b64 s[46:47],s[48:49]
v_rcp_f32 v85,v85
v_mfma_f32_16x16x32_bf16 v[40:43],a[84:87],v[80:83],v[40:43]
s_mov_b64 s[48:49],s[50:51]
ds_read_b128 v[68:71],v2 offset:45056
v_fma_f32 v86,v118,v86,v117
v_mfma_f32_16x16x32_bf16 v[44:47],a[88:91],v[80:83],v[44:47]
s_mov_b64 s[50:51],s[52:53]
v_mul_f32 v86,v30,v86
v_fma_f32 v87,v118,v87,v117
v_mfma_f32_16x16x32_bf16 v[48:51],a[92:95],v[80:83],v[48:51]
s_waitcnt lgkmcnt(3)
v_exp_f32 v86,v86
v_mfma_f32_16x16x32_bf16 v[40:43],a[96:99],v[52:55],v[40:43]
s_mov_b32 s52,s74
ds_read_b128 v[72:75],v2 offset:46080
v_mul_f32 v87,v31,v87
v_mfma_f32_16x16x32_bf16 v[44:47],a[100:103],v[52:55],v[44:47]
v_exp_f32 v87,v87
v_mfma_f32_16x16x32_bf16 v[48:51],a[104:107],v[52:55],v[48:51]
v_fma_f32 v86,v86,v119,v119
v_fma_f32 v87,v87,v119,v119
v_mfma_f32_16x16x32_bf16 v[40:43],a[108:111],v[56:59],v[40:43]
ds_read_b128 v[76:79],v2 offset:47104
v_mul_f32 v28,v28,v84
v_mfma_f32_16x16x32_bf16 v[44:47],a[112:115],v[56:59],v[44:47]
v_rcp_f32 v86,v86
v_mfma_f32_16x16x32_bf16 v[48:51],a[116:119],v[56:59],v[48:51]
s_waitcnt lgkmcnt(3)
v_rcp_f32 v87,v87
v_mfma_f32_16x16x32_bf16 v[40:43],a[120:123],v[60:63],v[40:43]
ds_read_b128 v[80:83],v2 offset:48128
v_mul_f32 v29,v29,v85
v_mfma_f32_16x16x32_bf16 v[44:47],a[124:127],v[60:63],v[44:47]
s_waitcnt vmcnt(6)
v_mul_f32 v30,v30,v86
v_mul_f32 v31,v31,v87
v_mfma_f32_16x16x32_bf16 v[48:51],a[128:131],v[60:63],v[48:51]
s_barrier
v_mfma_f32_16x16x32_bf16 v[40:43],a[132:135],v[64:67],v[40:43]
ds_read_b128 v[52:55],v2 offset:49152
v_add_f32_dpp v108,v108,v108 quad_perm:[1,0,3,2] row_mask:0xf bank_mask:0xf
v_mfma_f32_16x16x32_bf16 v[44:47],a[136:139],v[64:67],v[44:47]
s_nop 0
v_add_f32_dpp v108,v108,v108 quad_perm:[2,3,0,1] row_mask:0xf bank_mask:0xf
v_cvt_pk_bf16_f32 v96,v28,v29
v_mfma_f32_16x16x32_bf16 v[48:51],a[140:143],v[64:67],v[48:51]
s_waitcnt lgkmcnt(3)
v_add_f32_dpp v108,v108,v108 row_half_mirror row_mask:0xf bank_mask:0xf
v_cvt_pk_bf16_f32 v97,v30,v31
v_mfma_f32_16x16x32_bf16 v[40:43],a[144:147],v[68:71],v[40:43]
ds_read_b128 v[56:59],v2 offset:50176
v_fma_f32 v32,-v116,v20,v32
v_mfma_f32_16x16x32_bf16 v[44:47],a[148:151],v[68:71],v[44:47]
v_fma_f32 v33,-v116,v21,v33
v_fma_f32 v34,-v116,v22,v34
v_mfma_f32_16x16x32_bf16 v[48:51],a[152:155],v[68:71],v[48:51]
v_fma_f32 v35,-v116,v23,v35
v_mul_f32 v88,v32,v32
v_mfma_f32_16x16x32_bf16 v[40:43],a[156:159],v[72:75],v[40:43]
ds_read_b128 v[60:63],v2 offset:51200
v_mul_f32 v89,v33,v33
v_mfma_f32_16x16x32_bf16 v[44:47],a[160:163],v[72:75],v[44:47]
v_mul_f32 v90,v34,v34
v_mul_f32 v91,v35,v35
v_mfma_f32_16x16x32_bf16 v[48:51],a[164:167],v[72:75],v[48:51]
s_waitcnt lgkmcnt(3)
v_fma_f32 v88,v118,v88,v117
v_mul_f32 v88,v32,v88
v_mfma_f32_16x16x32_bf16 v[40:43],a[168:171],v[76:79],v[40:43]
ds_read_b128 v[64:67],v2 offset:52224
v_fma_f32 v89,v118,v89,v117
v_mfma_f32_16x16x32_bf16 v[44:47],a[172:175],v[76:79],v[44:47]
v_exp_f32 v88,v88
v_mfma_f32_16x16x32_bf16 v[48:51],a[176:179],v[76:79],v[48:51]
v_mul_f32 v89,v33,v89
v_fma_f32 v88,v88,v119,v119
v_mfma_f32_16x16x32_bf16 v[40:43],a[180:183],v[80:83],v[40:43]
buffer_load_dwordx4 v1,s[36:39],s61 offen offset:0 sc1 lds
v_mfma_f32_16x16x32_bf16 v[44:47],a[184:187],v[80:83],v[44:47]
ds_write_b32 v10,v108 offset:128
v_mfma_f32_16x16x32_bf16 v[48:51],a[188:191],v[80:83],v[48:51]
s_waitcnt lgkmcnt(3)
ds_read_b128 v[68:71],v2 offset:53248
v_fma_f32 v90,v118,v90,v117
v_mfma_f32_16x16x32_bf16 v[40:43],a[192:195],v[52:55],v[40:43]
ds_read_b128 v[72:75],v2 offset:54272
v_fma_f32 v91,v118,v91,v117
v_mfma_f32_16x16x32_bf16 v[44:47],a[196:199],v[52:55],v[44:47]
buffer_load_dwordx4 v1,s[36:39],s61 offen offset:1024 sc1 lds
v_mfma_f32_16x16x32_bf16 v[48:51],a[200:203],v[52:55],v[48:51]
v_exp_f32 v89,v89
v_mfma_f32_16x16x32_bf16 v[40:43],a[204:207],v[56:59],v[40:43]
ds_read_b128 v[76:79],v2 offset:55296
v_mul_f32 v90,v34,v90
v_mfma_f32_16x16x32_bf16 v[44:47],a[208:211],v[56:59],v[44:47]
v_exp_f32 v90,v90
v_mfma_f32_16x16x32_bf16 v[48:51],a[212:215],v[56:59],v[48:51]
s_waitcnt lgkmcnt(4)
buffer_load_dwordx4 v1,s[36:39],s61 offen offset:2048 sc1 lds
v_mfma_f32_16x16x32_bf16 v[40:43],a[216:219],v[60:63],v[40:43]
ds_read_b128 v[80:83],v2 offset:56320
v_mul_f32 v91,v35,v91
v_mfma_f32_16x16x32_bf16 v[44:47],a[220:223],v[60:63],v[44:47]
v_exp_f32 v91,v91
v_mfma_f32_16x16x32_bf16 v[48:51],a[224:227],v[60:63],v[48:51]
v_rcp_f32 v88,v88
v_mfma_f32_16x16x32_bf16 v[40:43],a[228:231],v[64:67],v[40:43]
buffer_load_dwordx4 v1,s[36:39],s61 offen offset:3072 sc1 lds
v_mfma_f32_16x16x32_bf16 v[44:47],a[232:235],v[64:67],v[44:47]
buffer_load_dword v104,v8,s[36:39],s72 offen offset:0 sc1
v_mfma_f32_16x16x32_bf16 v[48:51],a[236:239],v[64:67],v[48:51]
s_waitcnt lgkmcnt(2)
ds_read_b128 v[52:55],v2 offset:57344
v_fma_f32 v89,v89,v119,v119
v_mfma_f32_16x16x32_bf16 v[40:43],a[240:243],v[68:71],v[40:43]
ds_read_b128 v[56:59],v2 offset:58368
v_fma_f32 v90,v90,v119,v119
v_mfma_f32_16x16x32_bf16 v[44:47],a[244:247],v[68:71],v[44:47]
v_rcp_f32 v89,v89
v_mfma_f32_16x16x32_bf16 v[48:51],a[248:251],v[68:71],v[48:51]
v_rcp_f32 v90,v90
v_mfma_f32_16x16x32_bf16 v[40:43],a[252:255],v[72:75],v[40:43]
ds_read_b128 v[60:63],v2 offset:59392
v_fma_f32 v91,v91,v119,v119
v_mfma_f32_16x16x32_bf16 v[44:47],v[128:131],v[72:75],v[44:47]
v_rcp_f32 v91,v91
v_mfma_f32_16x16x32_bf16 v[48:51],v[132:135],v[72:75],v[48:51]
s_waitcnt lgkmcnt(3)
v_mul_f32 v32,v32,v88
v_mul_f32 v33,v33,v89)"
R"(
v_mfma_f32_16x16x32_bf16 v[40:43],v[136:139],v[76:79],v[40:43]
ds_read_b128 v[64:67],v2 offset:60416
v_mul_f32 v34,v34,v90
v_mfma_f32_16x16x32_bf16 v[44:47],v[140:143],v[76:79],v[44:47]
v_mul_f32 v35,v35,v91
v_cvt_pk_bf16_f32 v98,v32,v33
v_mfma_f32_16x16x32_bf16 v[48:51],v[144:147],v[76:79],v[48:51]
v_cvt_pk_bf16_f32 v99,v34,v35
v_fma_f32 v36,-v116,v24,v36
v_mfma_f32_16x16x32_bf16 v[40:43],v[148:151],v[80:83],v[40:43]
ds_read_b128 v[68:71],v2 offset:61440
v_fma_f32 v37,-v116,v25,v37
v_mfma_f32_16x16x32_bf16 v[44:47],v[152:155],v[80:83],v[44:47]
v_fma_f32 v38,-v116,v26,v38
v_fma_f32 v39,-v116,v27,v39
v_mfma_f32_16x16x32_bf16 v[48:51],v[156:159],v[80:83],v[48:51]
s_waitcnt lgkmcnt(3)
v_mul_f32 v92,v36,v36
v_mul_f32 v93,v37,v37
v_mfma_f32_16x16x32_bf16 v[40:43],v[160:163],v[52:55],v[40:43]
ds_read_b128 v[72:75],v2 offset:62464
v_mul_f32 v94,v38,v38
v_mfma_f32_16x16x32_bf16 v[44:47],v[164:167],v[52:55],v[44:47]
v_mul_f32 v95,v39,v39
v_fma_f32 v92,v118,v92,v117
v_mfma_f32_16x16x32_bf16 v[48:51],v[168:171],v[52:55],v[48:51]
buffer_store_dwordx4 v[96:99],v3,s[36:39],s73 offen offset:0 sc1
v_mul_f32 v92,v36,v92
v_exp_f32 v92,v92
v_mfma_f32_16x16x32_bf16 v[40:43],v[172:175],v[56:59],v[40:43]
ds_read_b128 v[76:79],v2 offset:63488
v_fma_f32 v92,v92,v119,v119
v_mfma_f32_16x16x32_bf16 v[44:47],v[176:179],v[56:59],v[44:47]
v_rcp_f32 v92,v92
v_mfma_f32_16x16x32_bf16 v[48:51],v[180:183],v[56:59],v[48:51]
s_waitcnt lgkmcnt(3)
v_fma_f32 v93,v118,v93,v117
v_mul_f32 v93,v37,v93
v_mfma_f32_16x16x32_bf16 v[40:43],v[184:187],v[60:63],v[40:43]
ds_read_b128 v[80:83],v2 offset:64512
v_xor_b32 v2,65536,v2
v_mfma_f32_16x16x32_bf16 v[44:47],v[188:191],v[60:63],v[44:47]
s_waitcnt vmcnt(6)
v_exp_f32 v93,v93
v_mfma_f32_16x16x32_bf16 v[48:51],v[192:195],v[60:63],v[48:51]
s_barrier
v_mfma_f32_16x16x32_bf16 v[40:43],v[196:199],v[64:67],v[40:43]
ds_read_b128 v[52:55],v2 offset:0
ds_read_b32 v13,v7 offset:256
v_mfma_f32_16x16x32_bf16 v[44:47],v[200:203],v[64:67],v[44:47]
ds_read_b64 v[112:113],v11 offset:128
v_fma_f32 v93,v93,v119,v119
v_mfma_f32_16x16x32_bf16 v[48:51],v[204:207],v[64:67],v[48:51]
s_waitcnt lgkmcnt(5)
v_rcp_f32 v93,v93
v_mfma_f32_16x16x32_bf16 v[40:43],v[208:211],v[68:71],v[40:43]
ds_read_b128 v[56:59],v2 offset:1024
v_fma_f32 v94,v118,v94,v117
v_mfma_f32_16x16x32_bf16 v[44:47],v[212:215],v[68:71],v[44:47]
s_waitcnt lgkmcnt(2)
v_mul_f32 v94,v38,v94
v_fma_f32 v95,v118,v95,v117
v_mfma_f32_16x16x32_bf16 v[48:51],v[216:219],v[68:71],v[48:51]
s_waitcnt lgkmcnt(1)
v_cmp_eq_u32 vcc,s60,v13
v_exp_f32 v94,v94
v_mul_f32 v95,v39,v95
v_mfma_f32_16x16x32_bf16 v[40:43],v[220:223],v[72:75],v[40:43]
s_cmp_eq_u32 vcc_lo,-1
ds_read_b128 v[60:63],v2 offset:2048
v_fma_f32 v94,v94,v119,v119
v_mfma_f32_16x16x32_bf16 v[44:47],v[224:227],v[72:75],v[44:47]
s_cselect_b32 s66,s86,s59
v_exp_f32 v95,v95
v_mfma_f32_16x16x32_bf16 v[48:51],v[228:231],v[72:75],v[48:51]
v_fma_f32 v95,v95,v119,v119
v_rcp_f32 v94,v94
v_rcp_f32 v95,v95
v_mfma_f32_16x16x32_bf16 v[40:43],v[232:235],v[76:79],v[40:43]
s_lshr_b32 s61,s66,0
v_mul_f32 v36,v36,v92
v_mul_f32 v37,v37,v93
v_mul_f32 v38,v38,v94
v_mul_f32 v39,v39,v95
v_cvt_pk_bf16_f32 v100,v36,v37
v_cvt_pk_bf16_f32 v101,v38,v39
v_mfma_f32_16x16x32_bf16 v[44:47],v[236:239],v[76:79],v[44:47]
buffer_store_dwordx2 v[100:101],v9,s[36:39],s73 offen offset:0 sc1
ds_read_b128 v[64:67],v2 offset:3072
v_mul_f32 v116,s75,v112
v_fma_f32 v113,v113,s75,v12
v_mfma_f32_16x16x32_bf16 v[48:51],v[240:243],v[76:79],v[48:51]
s_xor_b32 s64,s64,65536
v_fma_f32 v113,-v116,v116,v113
v_max_f32 v113,v113,v12
v_mfma_f32_16x16x32_bf16 v[40:43],v[244:247],v[80:83],v[40:43]
s_cmp_lt_u32 s67,s59
ds_read_b128 v[68:71],v2 offset:4096
v_mfma_f32_16x16x32_bf16 v[44:47],v[248:251],v[80:83],v[44:47]
s_waitcnt lgkmcnt(3)
v_rsq_f32 v112,v113
v_mfma_f32_16x16x32_bf16 v[48:51],v[252:255],v[80:83],v[48:51]
s_cbranch_scc1 Lg3_d0w2_%=
s_branch Lg3_slow0_%=
Lg3_d0w3_%=:
v_mfma_f32_16x16x32_bf16 v[28:31],a[0:3],v[52:55],0
s_mov_b32 m0,s62
ds_read_b128 v[72:75],v2 offset:5120
v_mul_f32 v117,s76,v112
v_mfma_f32_16x16x32_bf16 v[32:35],a[4:7],v[52:55],0
s_mul_i32 s73,s47,7
v_mul_f32 v118,v112,v112
v_mul_f32 v119,v113,v112
v_mfma_f32_16x16x32_bf16 v[36:39],a[8:11],v[52:55],0
s_lshr_b32 s73,s73,1
v_mul_f32 v118,s77,v118
v_mul_f32 v118,v118,v117
v_mfma_f32_16x16x32_bf16 v[28:31],a[12:15],v[56:59],v[28:31]
s_add_u32 s53,s52,s55
ds_read_b128 v[76:79],v2 offset:6144
v_fma_f32 v40,-v116,v16,v40
v_mfma_f32_16x16x32_bf16 v[32:35],a[16:19],v[56:59],v[32:35]
buffer_load_dwordx4 v0,s[36:39],s61 offen offset:0 sc1 lds
v_mfma_f32_16x16x32_bf16 v[36:39],a[20:23],v[56:59],v[36:39]
s_waitcnt lgkmcnt(3)
v_fma_f32 v41,-v116,v17,v41
v_fma_f32 v42,-v116,v18,v42
v_mfma_f32_16x16x32_bf16 v[28:31],a[24:27],v[60:63],v[28:31]
s_cmp_eq_u32 s53,s56
ds_read_b128 v[80:83],v2 offset:7168
v_fma_f32 v43,-v116,v19,v43
v_mfma_f32_16x16x32_bf16 v[32:35],a[28:31],v[60:63],v[32:35]
s_cselect_b32 s53,s57,s53
v_mul_f32 v84,v40,v40
v_mul_f32 v85,v41,v41
v_mfma_f32_16x16x32_bf16 v[36:39],a[32:35],v[60:63],v[36:39]
buffer_load_dwordx4 v0,s[36:39],s61 offen offset:1024 sc1 lds
v_mfma_f32_16x16x32_bf16 v[28:31],a[36:39],v[64:67],v[28:31]
s_cmp_eq_u32 s53,s58
ds_read_b128 v[52:55],v2 offset:8192
v_mul_f32 v86,v42,v42
v_mfma_f32_16x16x32_bf16 v[32:35],a[40:43],v[64:67],v[32:35]
s_cselect_b32 s53,s59,s53
v_mul_f32 v87,v43,v43
v_fma_f32 v84,v118,v84,v117
v_mfma_f32_16x16x32_bf16 v[36:39],a[44:47],v[64:67],v[36:39]
s_waitcnt lgkmcnt(3)
v_mul_f32 v84,v40,v84
v_fma_f32 v85,v118,v85,v117
v_mfma_f32_16x16x32_bf16 v[28:31],a[48:51],v[68:71],v[28:31]
buffer_load_dwordx4 v0,s[36:39],s61 offen offset:2048 sc1 lds
v_mfma_f32_16x16x32_bf16 v[32:35],a[52:55],v[68:71],v[32:35]
s_cmp_ge_u32 s66,s59
ds_read_b128 v[56:59],v2 offset:9216
v_mul_f32 v85,v41,v85
v_mfma_f32_16x16x32_bf16 v[36:39],a[56:59],v[68:71],v[36:39]
s_cselect_b32 s86,s49,s50
v_exp_f32 v84,v84
v_mfma_f32_16x16x32_bf16 v[28:31],a[60:63],v[72:75],v[28:31]
s_mul_i32 s70,s46,5
ds_read_b128 v[60:63],v2 offset:10240
v_fma_f32 v84,v84,v119,v119
v_mfma_f32_16x16x32_bf16 v[32:35],a[64:67],v[72:75],v[32:35]
buffer_load_dwordx4 v0,s[36:39],s61 offen offset:3072 sc1 lds
v_mfma_f32_16x16x32_bf16 v[36:39],a[68:71],v[72:75],v[36:39]
s_waitcnt lgkmcnt(3)
v_exp_f32 v85,v85
v_mfma_f32_16x16x32_bf16 v[28:31],a[72:75],v[76:79],v[28:31]
s_add_u32 m0,s62,16384
ds_read_b128 v[64:67],v2 offset:11264
v_fma_f32 v85,v85,v119,v119
v_mfma_f32_16x16x32_bf16 v[32:35],a[76:79],v[76:79],v[32:35]
s_lshr_b32 s72,s61,5
v_rcp_f32 v84,v84
v_mfma_f32_16x16x32_bf16 v[36:39],a[80:83],v[76:79],v[36:39]
s_lshr_b32 s70,s70,6
v_rcp_f32 v85,v85
v_mfma_f32_16x16x32_bf16 v[28:31],a[84:87],v[80:83],v[28:31]
s_mov_b32 s62,s63
ds_read_b128 v[68:71],v2 offset:12288
v_fma_f32 v86,v118,v86,v117
v_mfma_f32_16x16x32_bf16 v[32:35],a[88:91],v[80:83],v[32:35]
v_mul_f32 v86,v42,v86
v_fma_f32 v87,v118,v87,v117
v_mfma_f32_16x16x32_bf16 v[36:39],a[92:95],v[80:83],v[36:39]
s_waitcnt lgkmcnt(3)
v_exp_f32 v86,v86
v_mfma_f32_16x16x32_bf16 v[28:31],a[96:99],v[52:55],v[28:31]
ds_read_b128 v[72:75],v2 offset:13312
v_mul_f32 v87,v43,v87
v_mfma_f32_16x16x32_bf16 v[32:35],a[100:103],v[52:55],v[32:35]
v_exp_f32 v87,v87
v_mfma_f32_16x16x32_bf16 v[36:39],a[104:107],v[52:55],v[36:39]
v_fma_f32 v86,v86,v119,v119
v_fma_f32 v87,v87,v119,v119
v_mfma_f32_16x16x32_bf16 v[28:31],a[108:111],v[56:59],v[28:31]
ds_read_b128 v[76:79],v2 offset:14336
v_mul_f32 v40,v40,v84
v_mfma_f32_16x16x32_bf16 v[32:35],a[112:115],v[56:59],v[32:35]
v_rcp_f32 v86,v86)"
R"(
v_mfma_f32_16x16x32_bf16 v[36:39],a[116:119],v[56:59],v[36:39]
s_waitcnt lgkmcnt(3)
v_rcp_f32 v87,v87
v_mfma_f32_16x16x32_bf16 v[28:31],a[120:123],v[60:63],v[28:31]
ds_read_b128 v[80:83],v2 offset:15360
v_mul_f32 v41,v41,v85
v_mfma_f32_16x16x32_bf16 v[32:35],a[124:127],v[60:63],v[32:35]
s_waitcnt vmcnt(7)
v_mul_f32 v42,v42,v86
v_mul_f32 v43,v43,v87
v_mfma_f32_16x16x32_bf16 v[36:39],a[128:131],v[60:63],v[36:39]
s_barrier
v_mfma_f32_16x16x32_bf16 v[28:31],a[132:135],v[64:67],v[28:31]
ds_read_b128 v[52:55],v2 offset:16384
v_add_f32_dpp v104,v104,v104 quad_perm:[1,0,3,2] row_mask:0xf bank_mask:0xf
v_mfma_f32_16x16x32_bf16 v[32:35],a[136:139],v[64:67],v[32:35]
s_nop 0
v_add_f32_dpp v104,v104,v104 quad_perm:[2,3,0,1] row_mask:0xf bank_mask:0xf
v_cvt_pk_bf16_f32 v96,v40,v41
v_mfma_f32_16x16x32_bf16 v[36:39],a[140:143],v[64:67],v[36:39]
s_waitcnt lgkmcnt(3)
v_add_f32_dpp v104,v104,v104 row_half_mirror row_mask:0xf bank_mask:0xf
v_cvt_pk_bf16_f32 v97,v42,v43
v_mfma_f32_16x16x32_bf16 v[28:31],a[144:147],v[68:71],v[28:31]
ds_read_b128 v[56:59],v2 offset:17408
v_fma_f32 v44,-v116,v20,v44
v_mfma_f32_16x16x32_bf16 v[32:35],a[148:151],v[68:71],v[32:35]
v_fma_f32 v45,-v116,v21,v45
v_fma_f32 v46,-v116,v22,v46
v_mfma_f32_16x16x32_bf16 v[36:39],a[152:155],v[68:71],v[36:39]
v_fma_f32 v47,-v116,v23,v47
v_mul_f32 v88,v44,v44
v_mfma_f32_16x16x32_bf16 v[28:31],a[156:159],v[72:75],v[28:31]
ds_read_b128 v[60:63],v2 offset:18432
v_mul_f32 v89,v45,v45
v_mfma_f32_16x16x32_bf16 v[32:35],a[160:163],v[72:75],v[32:35]
v_mul_f32 v90,v46,v46
v_mul_f32 v91,v47,v47
v_mfma_f32_16x16x32_bf16 v[36:39],a[164:167],v[72:75],v[36:39]
s_waitcnt lgkmcnt(3)
v_fma_f32 v88,v118,v88,v117
v_mul_f32 v88,v44,v88
v_mfma_f32_16x16x32_bf16 v[28:31],a[168:171],v[76:79],v[28:31]
ds_read_b128 v[64:67],v2 offset:19456
v_fma_f32 v89,v118,v89,v117
v_mfma_f32_16x16x32_bf16 v[32:35],a[172:175],v[76:79],v[32:35]
v_exp_f32 v88,v88
v_mfma_f32_16x16x32_bf16 v[36:39],a[176:179],v[76:79],v[36:39]
v_mul_f32 v89,v45,v89
v_fma_f32 v88,v88,v119,v119
v_mfma_f32_16x16x32_bf16 v[28:31],a[180:183],v[80:83],v[28:31]
ds_read_b128 v[68:71],v2 offset:20480
v_fma_f32 v90,v118,v90,v117
v_mfma_f32_16x16x32_bf16 v[32:35],a[184:187],v[80:83],v[32:35]
buffer_load_dwordx4 v1,s[36:39],s61 offen offset:0 sc1 lds
v_mfma_f32_16x16x32_bf16 v[36:39],a[188:191],v[80:83],v[36:39]
s_waitcnt lgkmcnt(3)
ds_write_b32 v10,v104 offset:0
v_mfma_f32_16x16x32_bf16 v[28:31],a[192:195],v[52:55],v[28:31]
ds_read_b128 v[72:75],v2 offset:21504
v_fma_f32 v91,v118,v91,v117
v_mfma_f32_16x16x32_bf16 v[32:35],a[196:199],v[52:55],v[32:35]
v_exp_f32 v89,v89
v_mfma_f32_16x16x32_bf16 v[36:39],a[200:203],v[52:55],v[36:39]
buffer_load_dwordx4 v1,s[36:39],s61 offen offset:1024 sc1 lds
v_mfma_f32_16x16x32_bf16 v[28:31],a[204:207],v[56:59],v[28:31]
ds_read_b128 v[76:79],v2 offset:22528
v_mul_f32 v90,v46,v90
v_mfma_f32_16x16x32_bf16 v[32:35],a[208:211],v[56:59],v[32:35]
v_exp_f32 v90,v90
v_mfma_f32_16x16x32_bf16 v[36:39],a[212:215],v[56:59],v[36:39]
s_waitcnt lgkmcnt(4)
v_rcp_f32 v88,v88
v_mfma_f32_16x16x32_bf16 v[28:31],a[216:219],v[60:63],v[28:31]
buffer_load_dwordx4 v1,s[36:39],s61 offen offset:2048 sc1 lds
v_mfma_f32_16x16x32_bf16 v[32:35],a[220:223],v[60:63],v[32:35]
ds_read_b128 v[80:83],v2 offset:23552
v_mul_f32 v91,v47,v91
v_mfma_f32_16x16x32_bf16 v[36:39],a[224:227],v[60:63],v[36:39]
v_exp_f32 v91,v91
v_mfma_f32_16x16x32_bf16 v[28:31],a[228:231],v[64:67],v[28:31]
ds_read_b128 v[52:55],v2 offset:24576
v_fma_f32 v89,v89,v119,v119
v_mfma_f32_16x16x32_bf16 v[32:35],a[232:235],v[64:67],v[32:35]
buffer_load_dwordx4 v1,s[36:39],s61 offen offset:3072 sc1 lds
v_mfma_f32_16x16x32_bf16 v[36:39],a[236:239],v[64:67],v[36:39]
s_waitcnt lgkmcnt(3)
buffer_load_dword v108,v8,s[36:39],s72 offen offset:0 sc1
v_mfma_f32_16x16x32_bf16 v[28:31],a[240:243],v[68:71],v[28:31]
ds_read_b128 v[56:59],v2 offset:25600
v_fma_f32 v90,v90,v119,v119
v_mfma_f32_16x16x32_bf16 v[32:35],a[244:247],v[68:71],v[32:35]
v_rcp_f32 v89,v89
v_mfma_f32_16x16x32_bf16 v[36:39],a[248:251],v[68:71],v[36:39]
v_rcp_f32 v90,v90
v_mfma_f32_16x16x32_bf16 v[28:31],a[252:255],v[72:75],v[28:31]
ds_read_b128 v[60:63],v2 offset:26624
v_fma_f32 v91,v91,v119,v119
v_mfma_f32_16x16x32_bf16 v[32:35],v[128:131],v[72:75],v[32:35]
v_rcp_f32 v91,v91
v_mfma_f32_16x16x32_bf16 v[36:39],v[132:135],v[72:75],v[36:39]
s_waitcnt lgkmcnt(3)
v_mul_f32 v44,v44,v88
v_mul_f32 v45,v45,v89
v_mfma_f32_16x16x32_bf16 v[28:31],v[136:139],v[76:79],v[28:31]
ds_read_b128 v[64:67],v2 offset:27648
v_mul_f32 v46,v46,v90
v_mfma_f32_16x16x32_bf16 v[32:35],v[140:143],v[76:79],v[32:35]
v_mul_f32 v47,v47,v91
v_cvt_pk_bf16_f32 v98,v44,v45
v_mfma_f32_16x16x32_bf16 v[36:39],v[144:147],v[76:79],v[36:39]
v_cvt_pk_bf16_f32 v99,v46,v47
v_fma_f32 v48,-v116,v24,v48
v_mfma_f32_16x16x32_bf16 v[28:31],v[148:151],v[80:83],v[28:31]
ds_read_b128 v[68:71],v2 offset:28672
v_fma_f32 v49,-v116,v25,v49
v_mfma_f32_16x16x32_bf16 v[32:35],v[152:155],v[80:83],v[32:35]
v_fma_f32 v50,-v116,v26,v50
v_fma_f32 v51,-v116,v27,v51
v_mfma_f32_16x16x32_bf16 v[36:39],v[156:159],v[80:83],v[36:39]
s_waitcnt lgkmcnt(3)
v_mul_f32 v92,v48,v48
v_mul_f32 v93,v49,v49
v_mfma_f32_16x16x32_bf16 v[28:31],v[160:163],v[52:55],v[28:31]
ds_read_b128 v[72:75],v2 offset:29696
v_mul_f32 v94,v50,v50
v_mfma_f32_16x16x32_bf16 v[32:35],v[164:167],v[52:55],v[32:35]
v_mul_f32 v95,v51,v51
v_fma_f32 v92,v118,v92,v117
v_mfma_f32_16x16x32_bf16 v[36:39],v[168:171],v[52:55],v[36:39]
v_mul_f32 v92,v48,v92
v_fma_f32 v93,v118,v93,v117
v_mfma_f32_16x16x32_bf16 v[28:31],v[172:175],v[56:59],v[28:31]
ds_read_b128 v[76:79],v2 offset:30720
v_mul_f32 v93,v49,v93
v_mfma_f32_16x16x32_bf16 v[32:35],v[176:179],v[56:59],v[32:35]
buffer_store_dwordx4 v[96:99],v3,s[36:39],s73 offen offset:0 sc1
v_exp_f32 v92,v92
v_fma_f32 v94,v118,v94,v117
v_mfma_f32_16x16x32_bf16 v[36:39],v[180:183],v[56:59],v[36:39]
s_waitcnt lgkmcnt(3)
v_exp_f32 v93,v93
v_mfma_f32_16x16x32_bf16 v[28:31],v[184:187],v[60:63],v[28:31]
ds_read_b128 v[80:83],v2 offset:31744
v_mul_f32 v94,v50,v94
v_mfma_f32_16x16x32_bf16 v[32:35],v[188:191],v[60:63],v[32:35]
s_waitcnt vmcnt(6)
v_exp_f32 v94,v94
v_mfma_f32_16x16x32_bf16 v[36:39],v[192:195],v[60:63],v[36:39]
s_barrier
v_mfma_f32_16x16x32_bf16 v[28:31],v[196:199],v[64:67],v[28:31]
buffer_store_dword v5,v4,s[36:39],s70 offen offset:0 sc1
ds_read_b128 v[52:55],v2 offset:32768
ds_read_b32 v13,v7 offset:0
ds_read_b64 v[112:113],v11 offset:0
v_mfma_f32_16x16x32_bf16 v[32:35],v[200:203],v[64:67],v[32:35]
v_fma_f32 v92,v92,v119,v119
v_fma_f32 v93,v93,v119,v119
v_mfma_f32_16x16x32_bf16 v[36:39],v[204:207],v[64:67],v[36:39]
s_waitcnt lgkmcnt(5)
v_rcp_f32 v92,v92
v_mfma_f32_16x16x32_bf16 v[28:31],v[208:211],v[68:71],v[28:31]
ds_read_b128 v[56:59],v2 offset:33792
v_fma_f32 v94,v94,v119,v119
v_mfma_f32_16x16x32_bf16 v[32:35],v[212:215],v[68:71],v[32:35]
s_waitcnt lgkmcnt(2)
v_rcp_f32 v93,v93
v_mfma_f32_16x16x32_bf16 v[36:39],v[216:219],v[68:71],v[36:39]
s_waitcnt lgkmcnt(1)
v_cmp_eq_u32 vcc,s60,v13
v_rcp_f32 v94,v94
v_fma_f32 v95,v118,v95,v117
v_mfma_f32_16x16x32_bf16 v[28:31],v[220:223],v[72:75],v[28:31]
s_cmp_eq_u32 vcc_lo,-1
ds_read_b128 v[60:63],v2 offset:34816
v_mul_f32 v95,v51,v95
v_mfma_f32_16x16x32_bf16 v[32:35],v[224:227],v[72:75],v[32:35]
s_cselect_b32 s67,s86,s59
v_exp_f32 v95,v95
v_mfma_f32_16x16x32_bf16 v[36:39],v[228:231],v[72:75],v[36:39]
v_fma_f32 v95,v95,v119,v119
v_mul_f32 v116,s75,v112
v_mfma_f32_16x16x32_bf16 v[28:31],v[232:235],v[76:79],v[28:31]
s_lshr_b32 s61,s67,0
ds_read_b128 v[64:67],v2 offset:35840
v_mul_f32 v48,v48,v92
v_mfma_f32_16x16x32_bf16 v[32:35],v[236:239],v[76:79],v[32:35]
s_xor_b32 s63,s63,65536
v_rcp_f32 v95,v95
v_mfma_f32_16x16x32_bf16 v[36:39],v[240:243],v[76:79],v[36:39]
s_cmp_ge_u32 s66,s59
v_mul_f32 v49,v49,v93
v_mul_f32 v50,v50,v94
v_mul_f32 v51,v51,v95
v_cvt_pk_bf16_f32 v100,v48,v49
v_cvt_pk_bf16_f32 v101,v50,v51
v_mfma_f32_16x16x32_bf16 v[28:31],v[244:247],v[80:83],v[28:31]
buffer_store_dwordx2 v[100:101],v9,s[36:39],s73 offen offset:0 sc1
ds_read_b128 v[68:71],v2 offset:36864
v_fma_f32 v113,v113,s75,v12)"
R"(
v_fma_f32 v113,-v116,v116,v113
v_mfma_f32_16x16x32_bf16 v[32:35],v[248:251],v[80:83],v[32:35]
s_waitcnt lgkmcnt(3)
v_max_f32 v113,v113,v12
v_rsq_f32 v112,v113
v_mfma_f32_16x16x32_bf16 v[36:39],v[252:255],v[80:83],v[36:39]
s_cbranch_scc1 Lg3_slow1_%=
Lg3_d1w3_%=:
v_mfma_f32_16x16x32_bf16 v[40:43],a[0:3],v[52:55],0
s_mov_b32 m0,s62
ds_read_b128 v[72:75],v2 offset:37888
v_mul_f32 v117,s76,v112
v_mfma_f32_16x16x32_bf16 v[44:47],a[4:7],v[52:55],0
s_mul_i32 s73,s48,7
v_mul_f32 v118,v112,v112
v_mul_f32 v119,v113,v112
v_mfma_f32_16x16x32_bf16 v[48:51],a[8:11],v[52:55],0
s_lshr_b32 s73,s73,1
v_mul_f32 v118,s77,v118
v_mul_f32 v118,v118,v117
v_mfma_f32_16x16x32_bf16 v[40:43],a[12:15],v[56:59],v[40:43]
s_add_u32 s74,s53,s55
ds_read_b128 v[76:79],v2 offset:38912
v_fma_f32 v28,-v116,v16,v28
v_mfma_f32_16x16x32_bf16 v[44:47],a[16:19],v[56:59],v[44:47]
buffer_load_dwordx4 v0,s[36:39],s61 offen offset:0 sc1 lds
v_mfma_f32_16x16x32_bf16 v[48:51],a[20:23],v[56:59],v[48:51]
s_waitcnt lgkmcnt(3)
v_fma_f32 v29,-v116,v17,v29
v_fma_f32 v30,-v116,v18,v30
v_mfma_f32_16x16x32_bf16 v[40:43],a[24:27],v[60:63],v[40:43]
s_cmp_eq_u32 s74,s56
ds_read_b128 v[80:83],v2 offset:39936
v_fma_f32 v31,-v116,v19,v31
v_mfma_f32_16x16x32_bf16 v[44:47],a[28:31],v[60:63],v[44:47]
s_cselect_b32 s74,s57,s74
v_mul_f32 v84,v28,v28
v_mul_f32 v85,v29,v29
v_mfma_f32_16x16x32_bf16 v[48:51],a[32:35],v[60:63],v[48:51]
buffer_load_dwordx4 v0,s[36:39],s61 offen offset:1024 sc1 lds
v_mfma_f32_16x16x32_bf16 v[40:43],a[36:39],v[64:67],v[40:43]
s_cmp_eq_u32 s74,s58
ds_read_b128 v[52:55],v2 offset:40960
v_mul_f32 v86,v30,v30
v_mfma_f32_16x16x32_bf16 v[44:47],a[40:43],v[64:67],v[44:47]
s_cselect_b32 s74,s59,s74
v_mul_f32 v87,v31,v31
v_fma_f32 v84,v118,v84,v117
v_mfma_f32_16x16x32_bf16 v[48:51],a[44:47],v[64:67],v[48:51]
s_waitcnt lgkmcnt(3)
v_mul_f32 v84,v28,v84
v_fma_f32 v85,v118,v85,v117
v_mfma_f32_16x16x32_bf16 v[40:43],a[48:51],v[68:71],v[40:43]
buffer_load_dwordx4 v0,s[36:39],s61 offen offset:2048 sc1 lds
v_mfma_f32_16x16x32_bf16 v[44:47],a[52:55],v[68:71],v[44:47]
s_cmp_ge_u32 s67,s59
ds_read_b128 v[56:59],v2 offset:41984
v_mul_f32 v85,v29,v85
v_mfma_f32_16x16x32_bf16 v[48:51],a[56:59],v[68:71],v[48:51]
s_cselect_b32 s86,s50,s51
v_exp_f32 v84,v84
v_mfma_f32_16x16x32_bf16 v[40:43],a[60:63],v[72:75],v[40:43]
s_mul_i32 s70,s47,5
ds_read_b128 v[60:63],v2 offset:43008
v_fma_f32 v84,v84,v119,v119
v_mfma_f32_16x16x32_bf16 v[44:47],a[64:67],v[72:75],v[44:47]
buffer_load_dwordx4 v0,s[36:39],s61 offen offset:3072 sc1 lds
v_mfma_f32_16x16x32_bf16 v[48:51],a[68:71],v[72:75],v[48:51]
s_waitcnt lgkmcnt(3)
v_exp_f32 v85,v85
v_mfma_f32_16x16x32_bf16 v[40:43],a[72:75],v[76:79],v[40:43]
s_add_u32 m0,s62,16384
ds_read_b128 v[64:67],v2 offset:44032
v_fma_f32 v85,v85,v119,v119
v_mfma_f32_16x16x32_bf16 v[44:47],a[76:79],v[76:79],v[44:47]
s_lshr_b32 s72,s61,5
v_rcp_f32 v84,v84
v_mfma_f32_16x16x32_bf16 v[48:51],a[80:83],v[76:79],v[48:51]
s_lshr_b32 s70,s70,6
v_rcp_f32 v85,v85
v_mfma_f32_16x16x32_bf16 v[40:43],a[84:87],v[80:83],v[40:43]
s_mov_b32 s62,s64
ds_read_b128 v[68:71],v2 offset:45056
v_fma_f32 v86,v118,v86,v117
v_mfma_f32_16x16x32_bf16 v[44:47],a[88:91],v[80:83],v[44:47]
s_mov_b64 s[44:45],s[46:47]
v_mul_f32 v86,v30,v86
v_fma_f32 v87,v118,v87,v117
v_mfma_f32_16x16x32_bf16 v[48:51],a[92:95],v[80:83],v[48:51]
s_waitcnt lgkmcnt(3)
v_exp_f32 v86,v86
v_mfma_f32_16x16x32_bf16 v[40:43],a[96:99],v[52:55],v[40:43]
s_mov_b64 s[46:47],s[48:49]
ds_read_b128 v[72:75],v2 offset:46080
v_mul_f32 v87,v31,v87
v_mfma_f32_16x16x32_bf16 v[44:47],a[100:103],v[52:55],v[44:47]
s_mov_b64 s[48:49],s[50:51]
v_exp_f32 v87,v87
v_mfma_f32_16x16x32_bf16 v[48:51],a[104:107],v[52:55],v[48:51]
s_mov_b64 s[50:51],s[52:53]
v_fma_f32 v86,v86,v119,v119
v_fma_f32 v87,v87,v119,v119
v_mfma_f32_16x16x32_bf16 v[40:43],a[108:111],v[56:59],v[40:43]
s_mov_b32 s52,s74
ds_read_b128 v[76:79],v2 offset:47104
v_mul_f32 v28,v28,v84
v_mfma_f32_16x16x32_bf16 v[44:47],a[112:115],v[56:59],v[44:47]
v_rcp_f32 v86,v86
v_mfma_f32_16x16x32_bf16 v[48:51],a[116:119],v[56:59],v[48:51]
s_waitcnt lgkmcnt(3)
v_rcp_f32 v87,v87
v_mfma_f32_16x16x32_bf16 v[40:43],a[120:123],v[60:63],v[40:43]
ds_read_b128 v[80:83],v2 offset:48128
v_mul_f32 v29,v29,v85
v_mfma_f32_16x16x32_bf16 v[44:47],a[124:127],v[60:63],v[44:47]
s_waitcnt vmcnt(7)
v_mul_f32 v30,v30,v86
v_mul_f32 v31,v31,v87
v_mfma_f32_16x16x32_bf16 v[48:51],a[128:131],v[60:63],v[48:51]
s_barrier
v_mfma_f32_16x16x32_bf16 v[40:43],a[132:135],v[64:67],v[40:43]
ds_read_b128 v[52:55],v2 offset:49152
v_add_f32_dpp v108,v108,v108 quad_perm:[1,0,3,2] row_mask:0xf bank_mask:0xf
v_mfma_f32_16x16x32_bf16 v[44:47],a[136:139],v[64:67],v[44:47]
s_nop 0
v_add_f32_dpp v108,v108,v108 quad_perm:[2,3,0,1] row_mask:0xf bank_mask:0xf
v_cvt_pk_bf16_f32 v96,v28,v29
v_mfma_f32_16x16x32_bf16 v[48:51],a[140:143],v[64:67],v[48:51]
s_waitcnt lgkmcnt(3)
v_add_f32_dpp v108,v108,v108 row_half_mirror row_mask:0xf bank_mask:0xf
v_cvt_pk_bf16_f32 v97,v30,v31
v_mfma_f32_16x16x32_bf16 v[40:43],a[144:147],v[68:71],v[40:43]
ds_read_b128 v[56:59],v2 offset:50176
v_fma_f32 v32,-v116,v20,v32
v_mfma_f32_16x16x32_bf16 v[44:47],a[148:151],v[68:71],v[44:47]
v_fma_f32 v33,-v116,v21,v33
v_fma_f32 v34,-v116,v22,v34
v_mfma_f32_16x16x32_bf16 v[48:51],a[152:155],v[68:71],v[48:51]
v_fma_f32 v35,-v116,v23,v35
v_mul_f32 v88,v32,v32
v_mfma_f32_16x16x32_bf16 v[40:43],a[156:159],v[72:75],v[40:43]
ds_read_b128 v[60:63],v2 offset:51200
v_mul_f32 v89,v33,v33
v_mfma_f32_16x16x32_bf16 v[44:47],a[160:163],v[72:75],v[44:47]
v_mul_f32 v90,v34,v34
v_mul_f32 v91,v35,v35
v_mfma_f32_16x16x32_bf16 v[48:51],a[164:167],v[72:75],v[48:51]
s_waitcnt lgkmcnt(3)
v_fma_f32 v88,v118,v88,v117
v_mul_f32 v88,v32,v88
v_mfma_f32_16x16x32_bf16 v[40:43],a[168:171],v[76:79],v[40:43]
ds_read_b128 v[64:67],v2 offset:52224
v_fma_f32 v89,v118,v89,v117
v_mfma_f32_16x16x32_bf16 v[44:47],a[172:175],v[76:79],v[44:47]
v_exp_f32 v88,v88
v_mfma_f32_16x16x32_bf16 v[48:51],a[176:179],v[76:79],v[48:51]
v_mul_f32 v89,v33,v89
v_fma_f32 v88,v88,v119,v119
v_mfma_f32_16x16x32_bf16 v[40:43],a[180:183],v[80:83],v[40:43]
ds_read_b128 v[68:71],v2 offset:53248
v_fma_f32 v90,v118,v90,v117
v_mfma_f32_16x16x32_bf16 v[44:47],a[184:187],v[80:83],v[44:47]
buffer_load_dwordx4 v1,s[36:39],s61 offen offset:0 sc1 lds
v_mfma_f32_16x16x32_bf16 v[48:51],a[188:191],v[80:83],v[48:51]
s_waitcnt lgkmcnt(3)
ds_write_b32 v10,v108 offset:128
v_mfma_f32_16x16x32_bf16 v[40:43],a[192:195],v[52:55],v[40:43]
ds_read_b128 v[72:75],v2 offset:54272
v_fma_f32 v91,v118,v91,v117
v_mfma_f32_16x16x32_bf16 v[44:47],a[196:199],v[52:55],v[44:47]
v_exp_f32 v89,v89
v_mfma_f32_16x16x32_bf16 v[48:51],a[200:203],v[52:55],v[48:51]
buffer_load_dwordx4 v1,s[36:39],s61 offen offset:1024 sc1 lds
v_mfma_f32_16x16x32_bf16 v[40:43],a[204:207],v[56:59],v[40:43]
ds_read_b128 v[76:79],v2 offset:55296
v_mul_f32 v90,v34,v90
v_mfma_f32_16x16x32_bf16 v[44:47],a[208:211],v[56:59],v[44:47]
v_exp_f32 v90,v90
v_mfma_f32_16x16x32_bf16 v[48:51],a[212:215],v[56:59],v[48:51]
s_waitcnt lgkmcnt(4)
v_rcp_f32 v88,v88
v_mfma_f32_16x16x32_bf16 v[40:43],a[216:219],v[60:63],v[40:43]
buffer_load_dwordx4 v1,s[36:39],s61 offen offset:2048 sc1 lds
v_mfma_f32_16x16x32_bf16 v[44:47],a[220:223],v[60:63],v[44:47]
ds_read_b128 v[80:83],v2 offset:56320
v_mul_f32 v91,v35,v91
v_mfma_f32_16x16x32_bf16 v[48:51],a[224:227],v[60:63],v[48:51]
v_exp_f32 v91,v91
v_mfma_f32_16x16x32_bf16 v[40:43],a[228:231],v[64:67],v[40:43]
ds_read_b128 v[52:55],v2 offset:57344
v_fma_f32 v89,v89,v119,v119
v_mfma_f32_16x16x32_bf16 v[44:47],a[232:235],v[64:67],v[44:47]
buffer_load_dwordx4 v1,s[36:39],s61 offen offset:3072 sc1 lds
v_mfma_f32_16x16x32_bf16 v[48:51],a[236:239],v[64:67],v[48:51]
s_waitcnt lgkmcnt(3)
buffer_load_dword v104,v8,s[36:39],s72 offen offset:0 sc1
v_mfma_f32_16x16x32_bf16 v[40:43],a[240:243],v[68:71],v[40:43]
ds_read_b128 v[56:59],v2 offset:58368
v_fma_f32 v90,v90,v119,v119
v_mfma_f32_16x16x32_bf16 v[44:47],a[244:247],v[68:71],v[44:47])"
R"(
v_rcp_f32 v89,v89
v_mfma_f32_16x16x32_bf16 v[48:51],a[248:251],v[68:71],v[48:51]
v_rcp_f32 v90,v90
v_mfma_f32_16x16x32_bf16 v[40:43],a[252:255],v[72:75],v[40:43]
ds_read_b128 v[60:63],v2 offset:59392
v_fma_f32 v91,v91,v119,v119
v_mfma_f32_16x16x32_bf16 v[44:47],v[128:131],v[72:75],v[44:47]
v_rcp_f32 v91,v91
v_mfma_f32_16x16x32_bf16 v[48:51],v[132:135],v[72:75],v[48:51]
s_waitcnt lgkmcnt(3)
v_mul_f32 v32,v32,v88
v_mul_f32 v33,v33,v89
v_mfma_f32_16x16x32_bf16 v[40:43],v[136:139],v[76:79],v[40:43]
ds_read_b128 v[64:67],v2 offset:60416
v_mul_f32 v34,v34,v90
v_mfma_f32_16x16x32_bf16 v[44:47],v[140:143],v[76:79],v[44:47]
v_mul_f32 v35,v35,v91
v_cvt_pk_bf16_f32 v98,v32,v33
v_mfma_f32_16x16x32_bf16 v[48:51],v[144:147],v[76:79],v[48:51]
v_cvt_pk_bf16_f32 v99,v34,v35
v_fma_f32 v36,-v116,v24,v36
v_mfma_f32_16x16x32_bf16 v[40:43],v[148:151],v[80:83],v[40:43]
ds_read_b128 v[68:71],v2 offset:61440
v_fma_f32 v37,-v116,v25,v37
v_mfma_f32_16x16x32_bf16 v[44:47],v[152:155],v[80:83],v[44:47]
v_fma_f32 v38,-v116,v26,v38
v_fma_f32 v39,-v116,v27,v39
v_mfma_f32_16x16x32_bf16 v[48:51],v[156:159],v[80:83],v[48:51]
s_waitcnt lgkmcnt(3)
v_mul_f32 v92,v36,v36
v_mul_f32 v93,v37,v37
v_mfma_f32_16x16x32_bf16 v[40:43],v[160:163],v[52:55],v[40:43]
ds_read_b128 v[72:75],v2 offset:62464
v_mul_f32 v94,v38,v38
v_mfma_f32_16x16x32_bf16 v[44:47],v[164:167],v[52:55],v[44:47]
v_mul_f32 v95,v39,v39
v_fma_f32 v92,v118,v92,v117
v_mfma_f32_16x16x32_bf16 v[48:51],v[168:171],v[52:55],v[48:51]
v_mul_f32 v92,v36,v92
v_fma_f32 v93,v118,v93,v117
v_mfma_f32_16x16x32_bf16 v[40:43],v[172:175],v[56:59],v[40:43]
ds_read_b128 v[76:79],v2 offset:63488
v_mul_f32 v93,v37,v93
v_mfma_f32_16x16x32_bf16 v[44:47],v[176:179],v[56:59],v[44:47]
buffer_store_dwordx4 v[96:99],v3,s[36:39],s73 offen offset:0 sc1
v_exp_f32 v92,v92
v_fma_f32 v94,v118,v94,v117
v_mfma_f32_16x16x32_bf16 v[48:51],v[180:183],v[56:59],v[48:51]
s_waitcnt lgkmcnt(3)
v_exp_f32 v93,v93
v_mfma_f32_16x16x32_bf16 v[40:43],v[184:187],v[60:63],v[40:43]
ds_read_b128 v[80:83],v2 offset:64512
v_mul_f32 v94,v38,v94
v_mfma_f32_16x16x32_bf16 v[44:47],v[188:191],v[60:63],v[44:47]
s_waitcnt vmcnt(6)
v_xor_b32 v2,65536,v2
v_fma_f32 v92,v92,v119,v119
v_mfma_f32_16x16x32_bf16 v[48:51],v[192:195],v[60:63],v[48:51]
s_barrier
v_mfma_f32_16x16x32_bf16 v[40:43],v[196:199],v[64:67],v[40:43]
buffer_store_dword v5,v4,s[36:39],s70 offen offset:0 sc1
ds_read_b128 v[52:55],v2 offset:0
ds_read_b32 v13,v7 offset:256
ds_read_b64 v[112:113],v11 offset:128
v_mfma_f32_16x16x32_bf16 v[44:47],v[200:203],v[64:67],v[44:47]
v_exp_f32 v94,v94
v_mfma_f32_16x16x32_bf16 v[48:51],v[204:207],v[64:67],v[48:51]
s_waitcnt lgkmcnt(5)
v_rcp_f32 v92,v92
v_mfma_f32_16x16x32_bf16 v[40:43],v[208:211],v[68:71],v[40:43]
ds_read_b128 v[56:59],v2 offset:1024
v_fma_f32 v93,v93,v119,v119
v_mfma_f32_16x16x32_bf16 v[44:47],v[212:215],v[68:71],v[44:47]
s_waitcnt lgkmcnt(2)
v_rcp_f32 v93,v93
v_mfma_f32_16x16x32_bf16 v[48:51],v[216:219],v[68:71],v[48:51]
s_waitcnt lgkmcnt(1)
v_cmp_eq_u32 vcc,s60,v13
v_fma_f32 v94,v94,v119,v119
v_rcp_f32 v94,v94
v_mfma_f32_16x16x32_bf16 v[40:43],v[220:223],v[72:75],v[40:43]
s_cmp_eq_u32 vcc_lo,-1
ds_read_b128 v[60:63],v2 offset:2048
v_mul_f32 v116,s75,v112
v_mfma_f32_16x16x32_bf16 v[44:47],v[224:227],v[72:75],v[44:47]
s_cselect_b32 s66,s86,s59
v_fma_f32 v95,v118,v95,v117
v_mul_f32 v95,v39,v95
v_mfma_f32_16x16x32_bf16 v[48:51],v[228:231],v[72:75],v[48:51]
v_exp_f32 v95,v95
v_mfma_f32_16x16x32_bf16 v[40:43],v[232:235],v[76:79],v[40:43]
s_lshr_b32 s61,s66,0
ds_read_b128 v[64:67],v2 offset:3072
v_fma_f32 v95,v95,v119,v119
v_mfma_f32_16x16x32_bf16 v[44:47],v[236:239],v[76:79],v[44:47]
s_xor_b32 s64,s64,65536
v_rcp_f32 v95,v95
v_mfma_f32_16x16x32_bf16 v[48:51],v[240:243],v[76:79],v[48:51]
s_cmp_lt_u32 s67,s59
v_mul_f32 v36,v36,v92
v_mul_f32 v37,v37,v93
v_mul_f32 v38,v38,v94
v_mul_f32 v39,v39,v95
v_cvt_pk_bf16_f32 v100,v36,v37
v_cvt_pk_bf16_f32 v101,v38,v39
v_mfma_f32_16x16x32_bf16 v[40:43],v[244:247],v[80:83],v[40:43]
buffer_store_dwordx2 v[100:101],v9,s[36:39],s73 offen offset:0 sc1
ds_read_b128 v[68:71],v2 offset:4096
v_fma_f32 v113,v113,s75,v12
v_fma_f32 v113,-v116,v116,v113
v_mfma_f32_16x16x32_bf16 v[44:47],v[248:251],v[80:83],v[44:47]
s_waitcnt lgkmcnt(3)
v_max_f32 v113,v113,v12
v_rsq_f32 v112,v113
v_mfma_f32_16x16x32_bf16 v[48:51],v[252:255],v[80:83],v[48:51]
s_cbranch_scc1 Lg3_d0w3_%=
s_branch Lg3_slow0_%=
Lg3_slow0_%=:
s_cmp_lt_u32 s66,s59
s_cbranch_scc1 Lg3_gs0_%=
Lg3_slowb0_%=:
s_cmp_eq_u32 s54,1
s_cselect_b32 s68,s49,s48
s_lshr_b32 s68,s68,5
s_add_u32 m0,s78,8192
s_nop 0
buffer_load_dword v6,s[36:39],s68 offen offset:0 sc1 lds
s_mul_i32 s73,s47,7
s_lshr_b32 s73,s73,1
v_mul_f32 v117,s76,v112
v_mul_f32 v118,v112,v112
v_mul_f32 v119,v113,v112
v_mul_f32 v118,s77,v118
v_mul_f32 v118,v118,v117
v_fma_f32 v40,-v116,v16,v40
v_fma_f32 v41,-v116,v17,v41
v_fma_f32 v42,-v116,v18,v42
v_fma_f32 v43,-v116,v19,v43
v_mul_f32 v84,v40,v40
v_mul_f32 v85,v41,v41
v_mul_f32 v86,v42,v42
v_mul_f32 v87,v43,v43
v_fma_f32 v84,v118,v84,v117
v_fma_f32 v85,v118,v85,v117
v_fma_f32 v86,v118,v86,v117
v_fma_f32 v87,v118,v87,v117
v_mul_f32 v84,v40,v84
v_mul_f32 v85,v41,v85
v_mul_f32 v86,v42,v86
v_mul_f32 v87,v43,v87
v_exp_f32 v84,v84
v_exp_f32 v85,v85
v_exp_f32 v86,v86
v_exp_f32 v87,v87
v_fma_f32 v84,v84,v119,v119
v_fma_f32 v85,v85,v119,v119
v_fma_f32 v86,v86,v119,v119
v_fma_f32 v87,v87,v119,v119
v_rcp_f32 v84,v84
v_rcp_f32 v85,v85
v_rcp_f32 v86,v86
v_rcp_f32 v87,v87
v_mul_f32 v40,v40,v84
v_mul_f32 v41,v41,v85
v_mul_f32 v42,v42,v86
v_mul_f32 v43,v43,v87
v_cvt_pk_bf16_f32 v96,v40,v41
v_cvt_pk_bf16_f32 v97,v42,v43
v_fma_f32 v44,-v116,v20,v44
v_fma_f32 v45,-v116,v21,v45
v_fma_f32 v46,-v116,v22,v46
v_fma_f32 v47,-v116,v23,v47
v_mul_f32 v88,v44,v44
v_mul_f32 v89,v45,v45
v_mul_f32 v90,v46,v46
v_mul_f32 v91,v47,v47
v_fma_f32 v88,v118,v88,v117
v_fma_f32 v89,v118,v89,v117
v_fma_f32 v90,v118,v90,v117
v_fma_f32 v91,v118,v91,v117
v_mul_f32 v88,v44,v88
v_mul_f32 v89,v45,v89
v_mul_f32 v90,v46,v90
s_add_u32 m0,s78,8448
s_nop 0
buffer_load_dword v6,s[36:39],s68 offen offset:0 sc1 lds
v_mul_f32 v91,v47,v91
v_exp_f32 v88,v88
v_exp_f32 v89,v89
v_exp_f32 v90,v90
v_exp_f32 v91,v91
v_fma_f32 v88,v88,v119,v119
v_fma_f32 v89,v89,v119,v119
v_fma_f32 v90,v90,v119,v119
v_fma_f32 v91,v91,v119,v119
v_rcp_f32 v88,v88
v_rcp_f32 v89,v89
v_rcp_f32 v90,v90
v_rcp_f32 v91,v91
v_mul_f32 v44,v44,v88)"
R"(
v_mul_f32 v45,v45,v89
v_mul_f32 v46,v46,v90
v_mul_f32 v47,v47,v91
v_cvt_pk_bf16_f32 v98,v44,v45
v_cvt_pk_bf16_f32 v99,v46,v47
buffer_store_dwordx4 v[96:99],v3,s[36:39],s73 offen offset:0 sc1
v_fma_f32 v48,-v116,v24,v48
v_fma_f32 v49,-v116,v25,v49
v_fma_f32 v50,-v116,v26,v50
v_fma_f32 v51,-v116,v27,v51
v_mul_f32 v92,v48,v48
v_mul_f32 v93,v49,v49
v_mul_f32 v94,v50,v50
v_mul_f32 v95,v51,v51
v_fma_f32 v92,v118,v92,v117
v_fma_f32 v93,v118,v93,v117
v_fma_f32 v94,v118,v94,v117
v_fma_f32 v95,v118,v95,v117
v_mul_f32 v92,v48,v92
v_mul_f32 v93,v49,v93
v_mul_f32 v94,v50,v94
v_mul_f32 v95,v51,v95
v_exp_f32 v92,v92
v_exp_f32 v93,v93
v_exp_f32 v94,v94
v_exp_f32 v95,v95
v_fma_f32 v92,v92,v119,v119
v_fma_f32 v93,v93,v119,v119
v_fma_f32 v94,v94,v119,v119
v_fma_f32 v95,v95,v119,v119
v_rcp_f32 v92,v92
v_rcp_f32 v93,v93
v_rcp_f32 v94,v94
v_rcp_f32 v95,v95
v_mul_f32 v48,v48,v92
v_mul_f32 v49,v49,v93
v_mul_f32 v50,v50,v94
v_mul_f32 v51,v51,v95
v_cvt_pk_bf16_f32 v100,v48,v49
v_cvt_pk_bf16_f32 v101,v50,v51
buffer_store_dwordx2 v[100:101],v9,s[36:39],s73 offen offset:0 sc1
s_cmp_ge_u32 s48,s59
s_cbranch_scc1 Lg3_done0_%=
s_add_u32 m0,s78,16384
s_nop 0
buffer_load_dword v6,s[36:39],s68 offen offset:0 sc1 lds
s_waitcnt vmcnt(4)
s_barrier
ds_read_b32 v13,v7 offset:8192
s_mul_i32 s70,s46,5
s_lshr_b32 s70,s70,6
buffer_store_dword v5,v4,s[36:39],s70 offen offset:0 sc1
s_mov_b32 s46,s59
s_mov_b32 s69,18432
s_waitcnt lgkmcnt(0)
v_cmp_eq_u32 vcc,s60,v13
s_nop 0
s_cmp_eq_u32 vcc_lo,-1
s_cbranch_scc1 Lg3_go0_%=
s_add_u32 m0,s78,16640
s_nop 0
buffer_load_dword v6,s[36:39],s68 offen offset:0 sc1 lds
s_waitcnt vmcnt(5)
s_barrier
ds_read_b32 v13,v7 offset:8448
s_mov_b32 s69,18688
s_waitcnt lgkmcnt(0)
v_cmp_eq_u32 vcc,s60,v13
s_nop 0
s_cmp_eq_u32 vcc_lo,-1
s_cbranch_scc1 Lg3_go0_%=
s_add_u32 m0,s78,8192
s_nop 0
buffer_load_dword v6,s[36:39],s68 offen offset:0 sc1 lds
s_waitcnt vmcnt(3)
s_barrier
ds_read_b32 v13,v7 offset:16384
s_mul_i32 s70,s47,5
s_lshr_b32 s70,s70,6
buffer_store_dword v5,v4,s[36:39],s70 offen offset:0 sc1
s_mov_b32 s47,s59
s_mov_b32 s69,10240
s_waitcnt lgkmcnt(0)
v_cmp_eq_u32 vcc,s60,v13
s_nop 0
s_cmp_eq_u32 vcc_lo,-1
s_cbranch_scc1 Lg3_go0_%=
s_add_u32 m0,s78,8448
s_nop 0
buffer_load_dword v6,s[36:39],s68 offen offset:0 sc1 lds
s_waitcnt vmcnt(3)
s_barrier
ds_read_b32 v13,v7 offset:16640
s_mov_b32 s69,10496
s_waitcnt lgkmcnt(0)
v_cmp_eq_u32 vcc,s60,v13
s_nop 0
s_cmp_eq_u32 vcc_lo,-1
s_cbranch_scc1 Lg3_go0_%=
Lg3_poll0_%=:
s_add_u32 m0,s78,16384
s_nop 0
buffer_load_dword v6,s[36:39],s68 offen offset:0 sc1 lds
s_waitcnt vmcnt(2)
s_barrier
ds_read_b32 v13,v7 offset:8192
s_mov_b32 s69,18432
s_waitcnt lgkmcnt(0)
v_cmp_eq_u32 vcc,s60,v13
s_nop 0
s_cmp_eq_u32 vcc_lo,-1
s_cbranch_scc1 Lg3_go0_%=
s_sleep 4
s_add_u32 m0,s78,16640
s_nop 0
buffer_load_dword v6,s[36:39],s68 offen offset:0 sc1 lds
s_waitcnt vmcnt(2)
s_barrier
ds_read_b32 v13,v7 offset:8448
s_mov_b32 s69,18688
s_waitcnt lgkmcnt(0)
v_cmp_eq_u32 vcc,s60,v13
s_nop 0
s_cmp_eq_u32 vcc_lo,-1
s_cbranch_scc1 Lg3_go0_%=
s_sleep 4
s_add_u32 m0,s78,8192
s_nop 0
buffer_load_dword v6,s[36:39],s68 offen offset:0 sc1 lds
s_waitcnt vmcnt(2)
s_barrier
ds_read_b32 v13,v7 offset:16384
s_mov_b32 s69,10240
s_waitcnt lgkmcnt(0)
v_cmp_eq_u32 vcc,s60,v13
s_nop 0
s_cmp_eq_u32 vcc_lo,-1
s_cbranch_scc1 Lg3_go0_%=
s_sleep 4
s_add_u32 m0,s78,8448
s_nop 0
buffer_load_dword v6,s[36:39],s68 offen offset:0 sc1 lds
s_waitcnt vmcnt(2)
s_barrier
ds_read_b32 v13,v7 offset:16640
s_mov_b32 s69,10496
s_waitcnt lgkmcnt(0)
v_cmp_eq_u32 vcc,s60,v13
s_nop 0
s_cmp_eq_u32 vcc_lo,-1
s_cbranch_scc1 Lg3_go0_%=
s_sleep 4
s_add_u32 s79,s79,1
s_cmp_lt_u32 s79,20000
s_cbranch_scc1 Lg3_poll0_%=
Lg3_go0_%=:
s_xor_b32 s62,s63,65536
s_lshr_b32 s61,s48,0
s_mov_b32 m0,s62
s_nop 0
buffer_load_dwordx4 v0,s[36:39],s61 offen offset:0 sc1 lds
buffer_load_dwordx4 v0,s[36:39],s61 offen offset:1024 sc1 lds
buffer_load_dwordx4 v0,s[36:39],s61 offen offset:2048 sc1 lds
buffer_load_dwordx4 v0,s[36:39],s61 offen offset:3072 sc1 lds
s_add_u32 m0,s62,16384
s_nop 0
buffer_load_dwordx4 v1,s[36:39],s61 offen offset:0 sc1 lds
buffer_load_dwordx4 v1,s[36:39],s61 offen offset:1024 sc1 lds
buffer_load_dwordx4 v1,s[36:39],s61 offen offset:2048 sc1 lds
buffer_load_dwordx4 v1,s[36:39],s61 offen offset:3072 sc1 lds
s_lshr_b32 s72,s61,5
buffer_load_dword v104,v8,s[36:39],s72 offen offset:0 sc1
v_add_u32 v84,s69,v7
s_waitcnt vmcnt(5)
s_barrier
ds_read_b32 v13,v84 offset:0
ds_read_b128 v[52:55],v2 offset:0
ds_read_b128 v[56:59],v2 offset:1024
ds_read_b128 v[60:63],v2 offset:2048
ds_read_b128 v[64:67],v2 offset:3072
ds_read_b128 v[68:71],v2 offset:4096
s_mul_i32 s70,s46,5
s_lshr_b32 s70,s70,6
buffer_store_dword v5,v4,s[36:39],s70 offen offset:0 sc1
s_mov_b32 s46,s59
s_mul_i32 s70,s47,5
s_lshr_b32 s70,s70,6
buffer_store_dword v5,v4,s[36:39],s70 offen offset:0 sc1
s_mov_b32 s47,s59
s_mul_i32 s70,s59,5
s_lshr_b32 s70,s70,6
buffer_store_dword v5,v4,s[36:39],s70 offen offset:0 sc1
s_waitcnt lgkmcnt(5)
v_cmp_eq_u32 vcc,s60,v13
s_nop 0
s_cmp_eq_u32 vcc_lo,-1
s_cselect_b32 s66,s49,s59
s_lshr_b32 s61,s66,0
s_xor_b32 s62,s64,65536
s_waitcnt lgkmcnt(3))"
R"(
s_setpc_b64 s[90:91]
Lg3_gs0_%=:
s_xor_b32 s62,s63,65536
s_lshr_b32 s61,s48,0
s_cmp_eq_u32 s54,1
s_cselect_b32 s68,s49,s48
s_lshr_b32 s68,s68,5
s_mul_i32 s73,s47,7
s_lshr_b32 s73,s73,1
s_mov_b32 m0,s62
s_nop 0
buffer_load_dwordx4 v0,s[36:39],s61 offen offset:0 sc1 lds
v_mul_f32 v117,s76,v112
v_mul_f32 v118,v112,v112
v_mul_f32 v119,v113,v112
v_mul_f32 v118,s77,v118
v_mul_f32 v118,v118,v117
v_fma_f32 v40,-v116,v16,v40
v_fma_f32 v41,-v116,v17,v41
v_fma_f32 v42,-v116,v18,v42
v_fma_f32 v43,-v116,v19,v43
v_mul_f32 v84,v40,v40
buffer_load_dwordx4 v0,s[36:39],s61 offen offset:1024 sc1 lds
v_mul_f32 v85,v41,v41
v_mul_f32 v86,v42,v42
v_mul_f32 v87,v43,v43
v_fma_f32 v84,v118,v84,v117
v_fma_f32 v85,v118,v85,v117
v_fma_f32 v86,v118,v86,v117
v_fma_f32 v87,v118,v87,v117
v_mul_f32 v84,v40,v84
v_mul_f32 v85,v41,v85
v_mul_f32 v86,v42,v86
buffer_load_dwordx4 v0,s[36:39],s61 offen offset:2048 sc1 lds
v_mul_f32 v87,v43,v87
v_exp_f32 v84,v84
v_exp_f32 v85,v85
v_exp_f32 v86,v86
v_exp_f32 v87,v87
v_fma_f32 v84,v84,v119,v119
v_fma_f32 v85,v85,v119,v119
v_fma_f32 v86,v86,v119,v119
v_fma_f32 v87,v87,v119,v119
v_rcp_f32 v84,v84
buffer_load_dwordx4 v0,s[36:39],s61 offen offset:3072 sc1 lds
v_rcp_f32 v85,v85
v_rcp_f32 v86,v86
v_rcp_f32 v87,v87
v_mul_f32 v40,v40,v84
v_mul_f32 v41,v41,v85
v_mul_f32 v42,v42,v86
v_mul_f32 v43,v43,v87
v_cvt_pk_bf16_f32 v96,v40,v41
v_cvt_pk_bf16_f32 v97,v42,v43
v_fma_f32 v44,-v116,v20,v44
s_add_u32 m0,s78,8192
s_nop 0
buffer_load_dword v6,s[36:39],s68 offen offset:0 sc1 lds
v_fma_f32 v45,-v116,v21,v45
v_fma_f32 v46,-v116,v22,v46
v_fma_f32 v47,-v116,v23,v47
v_mul_f32 v88,v44,v44
v_mul_f32 v89,v45,v45
v_mul_f32 v90,v46,v46
v_mul_f32 v91,v47,v47
v_fma_f32 v88,v118,v88,v117
v_fma_f32 v89,v118,v89,v117
v_fma_f32 v90,v118,v90,v117
s_add_u32 m0,s62,16384
s_nop 0
buffer_load_dwordx4 v1,s[36:39],s61 offen offset:0 sc1 lds
v_fma_f32 v91,v118,v91,v117
v_mul_f32 v88,v44,v88
v_mul_f32 v89,v45,v89
v_mul_f32 v90,v46,v90
v_mul_f32 v91,v47,v91
v_exp_f32 v88,v88
v_exp_f32 v89,v89
v_exp_f32 v90,v90
v_exp_f32 v91,v91
v_fma_f32 v88,v88,v119,v119
buffer_load_dwordx4 v1,s[36:39],s61 offen offset:1024 sc1 lds
v_fma_f32 v89,v89,v119,v119
v_fma_f32 v90,v90,v119,v119
v_fma_f32 v91,v91,v119,v119
v_rcp_f32 v88,v88
v_rcp_f32 v89,v89
v_rcp_f32 v90,v90
v_rcp_f32 v91,v91
v_mul_f32 v44,v44,v88
v_mul_f32 v45,v45,v89
v_mul_f32 v46,v46,v90
buffer_load_dwordx4 v1,s[36:39],s61 offen offset:2048 sc1 lds
v_mul_f32 v47,v47,v91
v_cvt_pk_bf16_f32 v98,v44,v45
v_cvt_pk_bf16_f32 v99,v46,v47
buffer_store_dwordx4 v[96:99],v3,s[36:39],s73 offen offset:0 sc1
v_fma_f32 v48,-v116,v24,v48
v_fma_f32 v49,-v116,v25,v49
v_fma_f32 v50,-v116,v26,v50
v_fma_f32 v51,-v116,v27,v51
v_mul_f32 v92,v48,v48
v_mul_f32 v93,v49,v49
buffer_load_dwordx4 v1,s[36:39],s61 offen offset:3072 sc1 lds
v_mul_f32 v94,v50,v50
v_mul_f32 v95,v51,v51
v_fma_f32 v92,v118,v92,v117
v_fma_f32 v93,v118,v93,v117
v_fma_f32 v94,v118,v94,v117
v_fma_f32 v95,v118,v95,v117
v_mul_f32 v92,v48,v92
v_mul_f32 v93,v49,v93
v_mul_f32 v94,v50,v94
v_mul_f32 v95,v51,v95
s_lshr_b32 s72,s61,5
buffer_load_dword v104,v8,s[36:39],s72 offen offset:0 sc1
v_exp_f32 v92,v92
v_exp_f32 v93,v93
v_exp_f32 v94,v94
v_exp_f32 v95,v95
v_fma_f32 v92,v92,v119,v119
v_fma_f32 v93,v93,v119,v119
v_fma_f32 v94,v94,v119,v119
v_fma_f32 v95,v95,v119,v119
v_rcp_f32 v92,v92
v_rcp_f32 v93,v93
v_rcp_f32 v94,v94
v_rcp_f32 v95,v95
v_mul_f32 v48,v48,v92
v_mul_f32 v49,v49,v93
v_mul_f32 v50,v50,v94
v_mul_f32 v51,v51,v95
v_cvt_pk_bf16_f32 v100,v48,v49
v_cvt_pk_bf16_f32 v101,v50,v51
buffer_store_dwordx2 v[100:101],v9,s[36:39],s73 offen offset:0 sc1
s_waitcnt vmcnt(7)
s_barrier
ds_read_b32 v13,v7 offset:8192
ds_read_b128 v[52:55],v2 offset:0
ds_read_b128 v[56:59],v2 offset:1024
ds_read_b128 v[60:63],v2 offset:2048
ds_read_b128 v[64:67],v2 offset:3072
ds_read_b128 v[68:71],v2 offset:4096
s_mul_i32 s70,s46,5
s_lshr_b32 s70,s70,6
buffer_store_dword v5,v4,s[36:39],s70 offen offset:0 sc1
s_waitcnt lgkmcnt(5)
v_cmp_eq_u32 vcc,s60,v13
s_nop 0
s_cmp_eq_u32 vcc_hi,-1
s_cselect_b32 s66,s49,s59
s_lshr_b32 s61,s66,0
s_xor_b32 s62,s64,65536
s_mov_b32 s46,s47
s_mov_b32 s47,s59
s_mul_i32 s70,s59,5
s_lshr_b32 s70,s70,6
buffer_store_dword v5,v4,s[36:39],s70 offen offset:0 sc1
s_waitcnt lgkmcnt(3)
s_setpc_b64 s[90:91]
Lg3_done0_%=:
s_waitcnt vmcnt(0)
s_barrier
s_mul_i32 s70,s46,5
s_lshr_b32 s70,s70,6
buffer_store_dword v5,v4,s[36:39],s70 offen offset:0 sc1
s_mul_i32 s70,s47,5
s_lshr_b32 s70,s70,6
buffer_store_dword v5,v4,s[36:39],s70 offen offset:0 sc1
s_branch Lg3_done_%=
Lg3_slow1_%=:
s_cmp_lt_u32 s67,s59
s_cbranch_scc1 Lg3_gs1_%=
Lg3_slowb1_%=:
s_cmp_eq_u32 s54,1
s_cselect_b32 s68,s50,s49
s_lshr_b32 s68,s68,5
s_add_u32 m0,s78,8192
s_nop 0
buffer_load_dword v6,s[36:39],s68 offen offset:0 sc1 lds
s_mul_i32 s73,s48,7
s_lshr_b32 s73,s73,1
v_mul_f32 v117,s76,v112
v_mul_f32 v118,v112,v112
v_mul_f32 v119,v113,v112
v_mul_f32 v118,s77,v118
v_mul_f32 v118,v118,v117
v_fma_f32 v28,-v116,v16,v28
v_fma_f32 v29,-v116,v17,v29
v_fma_f32 v30,-v116,v18,v30
v_fma_f32 v31,-v116,v19,v31
v_mul_f32 v84,v28,v28
v_mul_f32 v85,v29,v29
v_mul_f32 v86,v30,v30
v_mul_f32 v87,v31,v31
v_fma_f32 v84,v118,v84,v117
v_fma_f32 v85,v118,v85,v117
v_fma_f32 v86,v118,v86,v117
v_fma_f32 v87,v118,v87,v117
v_mul_f32 v84,v28,v84)"
R"(
v_mul_f32 v85,v29,v85
v_mul_f32 v86,v30,v86
v_mul_f32 v87,v31,v87
v_exp_f32 v84,v84
v_exp_f32 v85,v85
v_exp_f32 v86,v86
v_exp_f32 v87,v87
v_fma_f32 v84,v84,v119,v119
v_fma_f32 v85,v85,v119,v119
v_fma_f32 v86,v86,v119,v119
v_fma_f32 v87,v87,v119,v119
v_rcp_f32 v84,v84
v_rcp_f32 v85,v85
v_rcp_f32 v86,v86
v_rcp_f32 v87,v87
v_mul_f32 v28,v28,v84
v_mul_f32 v29,v29,v85
v_mul_f32 v30,v30,v86
v_mul_f32 v31,v31,v87
v_cvt_pk_bf16_f32 v96,v28,v29
v_cvt_pk_bf16_f32 v97,v30,v31
v_fma_f32 v32,-v116,v20,v32
v_fma_f32 v33,-v116,v21,v33
v_fma_f32 v34,-v116,v22,v34
v_fma_f32 v35,-v116,v23,v35
v_mul_f32 v88,v32,v32
v_mul_f32 v89,v33,v33
v_mul_f32 v90,v34,v34
v_mul_f32 v91,v35,v35
v_fma_f32 v88,v118,v88,v117
v_fma_f32 v89,v118,v89,v117
v_fma_f32 v90,v118,v90,v117
v_fma_f32 v91,v118,v91,v117
v_mul_f32 v88,v32,v88
v_mul_f32 v89,v33,v89
v_mul_f32 v90,v34,v90
s_add_u32 m0,s78,8448
s_nop 0
buffer_load_dword v6,s[36:39],s68 offen offset:0 sc1 lds
v_mul_f32 v91,v35,v91
v_exp_f32 v88,v88
v_exp_f32 v89,v89
v_exp_f32 v90,v90
v_exp_f32 v91,v91
v_fma_f32 v88,v88,v119,v119
v_fma_f32 v89,v89,v119,v119
v_fma_f32 v90,v90,v119,v119
v_fma_f32 v91,v91,v119,v119
v_rcp_f32 v88,v88
v_rcp_f32 v89,v89
v_rcp_f32 v90,v90
v_rcp_f32 v91,v91
v_mul_f32 v32,v32,v88
v_mul_f32 v33,v33,v89
v_mul_f32 v34,v34,v90
v_mul_f32 v35,v35,v91
v_cvt_pk_bf16_f32 v98,v32,v33
v_cvt_pk_bf16_f32 v99,v34,v35
buffer_store_dwordx4 v[96:99],v3,s[36:39],s73 offen offset:0 sc1
v_fma_f32 v36,-v116,v24,v36
v_fma_f32 v37,-v116,v25,v37
v_fma_f32 v38,-v116,v26,v38
v_fma_f32 v39,-v116,v27,v39
v_mul_f32 v92,v36,v36
v_mul_f32 v93,v37,v37
v_mul_f32 v94,v38,v38
v_mul_f32 v95,v39,v39
v_fma_f32 v92,v118,v92,v117
v_fma_f32 v93,v118,v93,v117
v_fma_f32 v94,v118,v94,v117
v_fma_f32 v95,v118,v95,v117
v_mul_f32 v92,v36,v92
v_mul_f32 v93,v37,v93
v_mul_f32 v94,v38,v94
v_mul_f32 v95,v39,v95
v_exp_f32 v92,v92
v_exp_f32 v93,v93
v_exp_f32 v94,v94
v_exp_f32 v95,v95
v_fma_f32 v92,v92,v119,v119
v_fma_f32 v93,v93,v119,v119
v_fma_f32 v94,v94,v119,v119
v_fma_f32 v95,v95,v119,v119
v_rcp_f32 v92,v92
v_rcp_f32 v93,v93
v_rcp_f32 v94,v94
v_rcp_f32 v95,v95
v_mul_f32 v36,v36,v92
v_mul_f32 v37,v37,v93
v_mul_f32 v38,v38,v94
v_mul_f32 v39,v39,v95
v_cvt_pk_bf16_f32 v100,v36,v37
v_cvt_pk_bf16_f32 v101,v38,v39
buffer_store_dwordx2 v[100:101],v9,s[36:39],s73 offen offset:0 sc1
s_cmp_ge_u32 s49,s59
s_cbranch_scc1 Lg3_done1_%=
s_add_u32 m0,s78,16384
s_nop 0
buffer_load_dword v6,s[36:39],s68 offen offset:0 sc1 lds
s_waitcnt vmcnt(4)
s_barrier
ds_read_b32 v13,v7 offset:8192
s_mul_i32 s70,s47,5
s_lshr_b32 s70,s70,6
buffer_store_dword v5,v4,s[36:39],s70 offen offset:0 sc1
s_mov_b32 s47,s59
s_mov_b32 s69,18432
s_waitcnt lgkmcnt(0)
v_cmp_eq_u32 vcc,s60,v13
s_nop 0
s_cmp_eq_u32 vcc_lo,-1
s_cbranch_scc1 Lg3_go1_%=
s_add_u32 m0,s78,16640
s_nop 0
buffer_load_dword v6,s[36:39],s68 offen offset:0 sc1 lds
s_waitcnt vmcnt(5)
s_barrier
ds_read_b32 v13,v7 offset:8448
s_mov_b32 s69,18688
s_waitcnt lgkmcnt(0)
v_cmp_eq_u32 vcc,s60,v13
s_nop 0
s_cmp_eq_u32 vcc_lo,-1
s_cbranch_scc1 Lg3_go1_%=
s_add_u32 m0,s78,8192
s_nop 0
buffer_load_dword v6,s[36:39],s68 offen offset:0 sc1 lds
s_waitcnt vmcnt(3)
s_barrier
ds_read_b32 v13,v7 offset:16384
s_mul_i32 s70,s48,5
s_lshr_b32 s70,s70,6
buffer_store_dword v5,v4,s[36:39],s70 offen offset:0 sc1
s_mov_b32 s48,s59
s_mov_b32 s69,10240
s_waitcnt lgkmcnt(0)
v_cmp_eq_u32 vcc,s60,v13
s_nop 0
s_cmp_eq_u32 vcc_lo,-1
s_cbranch_scc1 Lg3_go1_%=
s_add_u32 m0,s78,8448
s_nop 0
buffer_load_dword v6,s[36:39],s68 offen offset:0 sc1 lds
s_waitcnt vmcnt(3)
s_barrier
ds_read_b32 v13,v7 offset:16640
s_mov_b32 s69,10496
s_waitcnt lgkmcnt(0)
v_cmp_eq_u32 vcc,s60,v13
s_nop 0
s_cmp_eq_u32 vcc_lo,-1
s_cbranch_scc1 Lg3_go1_%=
Lg3_poll1_%=:
s_add_u32 m0,s78,16384
s_nop 0
buffer_load_dword v6,s[36:39],s68 offen offset:0 sc1 lds
s_waitcnt vmcnt(2)
s_barrier
ds_read_b32 v13,v7 offset:8192
s_mov_b32 s69,18432
s_waitcnt lgkmcnt(0)
v_cmp_eq_u32 vcc,s60,v13
s_nop 0
s_cmp_eq_u32 vcc_lo,-1
s_cbranch_scc1 Lg3_go1_%=
s_sleep 4
s_add_u32 m0,s78,16640
s_nop 0
buffer_load_dword v6,s[36:39],s68 offen offset:0 sc1 lds
s_waitcnt vmcnt(2)
s_barrier
ds_read_b32 v13,v7 offset:8448
s_mov_b32 s69,18688
s_waitcnt lgkmcnt(0)
v_cmp_eq_u32 vcc,s60,v13
s_nop 0
s_cmp_eq_u32 vcc_lo,-1
s_cbranch_scc1 Lg3_go1_%=
s_sleep 4
s_add_u32 m0,s78,8192
s_nop 0
buffer_load_dword v6,s[36:39],s68 offen offset:0 sc1 lds
s_waitcnt vmcnt(2)
s_barrier
ds_read_b32 v13,v7 offset:16384
s_mov_b32 s69,10240
s_waitcnt lgkmcnt(0)
v_cmp_eq_u32 vcc,s60,v13
s_nop 0
s_cmp_eq_u32 vcc_lo,-1
s_cbranch_scc1 Lg3_go1_%=
s_sleep 4
s_add_u32 m0,s78,8448
s_nop 0
buffer_load_dword v6,s[36:39],s68 offen offset:0 sc1 lds
s_waitcnt vmcnt(2)
s_barrier
ds_read_b32 v13,v7 offset:16640
s_mov_b32 s69,10496
s_waitcnt lgkmcnt(0))"
R"(
v_cmp_eq_u32 vcc,s60,v13
s_nop 0
s_cmp_eq_u32 vcc_lo,-1
s_cbranch_scc1 Lg3_go1_%=
s_sleep 4
s_add_u32 s79,s79,1
s_cmp_lt_u32 s79,20000
s_cbranch_scc1 Lg3_poll1_%=
Lg3_go1_%=:
s_xor_b32 s62,s64,65536
s_lshr_b32 s61,s49,0
s_mov_b32 m0,s62
s_nop 0
buffer_load_dwordx4 v0,s[36:39],s61 offen offset:0 sc1 lds
buffer_load_dwordx4 v0,s[36:39],s61 offen offset:1024 sc1 lds
buffer_load_dwordx4 v0,s[36:39],s61 offen offset:2048 sc1 lds
buffer_load_dwordx4 v0,s[36:39],s61 offen offset:3072 sc1 lds
s_add_u32 m0,s62,16384
s_nop 0
buffer_load_dwordx4 v1,s[36:39],s61 offen offset:0 sc1 lds
buffer_load_dwordx4 v1,s[36:39],s61 offen offset:1024 sc1 lds
buffer_load_dwordx4 v1,s[36:39],s61 offen offset:2048 sc1 lds
buffer_load_dwordx4 v1,s[36:39],s61 offen offset:3072 sc1 lds
s_lshr_b32 s72,s61,5
buffer_load_dword v108,v8,s[36:39],s72 offen offset:0 sc1
v_add_u32 v84,s69,v7
s_waitcnt vmcnt(5)
s_barrier
ds_read_b32 v13,v84 offset:0
ds_read_b128 v[52:55],v2 offset:32768
ds_read_b128 v[56:59],v2 offset:33792
ds_read_b128 v[60:63],v2 offset:34816
ds_read_b128 v[64:67],v2 offset:35840
ds_read_b128 v[68:71],v2 offset:36864
s_mul_i32 s70,s47,5
s_lshr_b32 s70,s70,6
buffer_store_dword v5,v4,s[36:39],s70 offen offset:0 sc1
s_mov_b32 s47,s59
s_mul_i32 s70,s48,5
s_lshr_b32 s70,s70,6
buffer_store_dword v5,v4,s[36:39],s70 offen offset:0 sc1
s_mov_b32 s48,s59
s_mul_i32 s70,s59,5
s_lshr_b32 s70,s70,6
buffer_store_dword v5,v4,s[36:39],s70 offen offset:0 sc1
s_waitcnt lgkmcnt(5)
v_cmp_eq_u32 vcc,s60,v13
s_nop 0
s_cmp_eq_u32 vcc_lo,-1
s_cselect_b32 s67,s50,s59
s_lshr_b32 s61,s67,0
s_xor_b32 s62,s63,65536
s_waitcnt lgkmcnt(3)
s_setpc_b64 s[92:93]
Lg3_gs1_%=:
s_xor_b32 s62,s64,65536
s_lshr_b32 s61,s49,0
s_cmp_eq_u32 s54,1
s_cselect_b32 s68,s50,s49
s_lshr_b32 s68,s68,5
s_mul_i32 s73,s48,7
s_lshr_b32 s73,s73,1
s_mov_b32 m0,s62
s_nop 0
buffer_load_dwordx4 v0,s[36:39],s61 offen offset:0 sc1 lds
v_mul_f32 v117,s76,v112
v_mul_f32 v118,v112,v112
v_mul_f32 v119,v113,v112
v_mul_f32 v118,s77,v118
v_mul_f32 v118,v118,v117
v_fma_f32 v28,-v116,v16,v28
v_fma_f32 v29,-v116,v17,v29
v_fma_f32 v30,-v116,v18,v30
v_fma_f32 v31,-v116,v19,v31
v_mul_f32 v84,v28,v28
buffer_load_dwordx4 v0,s[36:39],s61 offen offset:1024 sc1 lds
v_mul_f32 v85,v29,v29
v_mul_f32 v86,v30,v30
v_mul_f32 v87,v31,v31
v_fma_f32 v84,v118,v84,v117
v_fma_f32 v85,v118,v85,v117
v_fma_f32 v86,v118,v86,v117
v_fma_f32 v87,v118,v87,v117
v_mul_f32 v84,v28,v84
v_mul_f32 v85,v29,v85
v_mul_f32 v86,v30,v86
buffer_load_dwordx4 v0,s[36:39],s61 offen offset:2048 sc1 lds
v_mul_f32 v87,v31,v87
v_exp_f32 v84,v84
v_exp_f32 v85,v85
v_exp_f32 v86,v86
v_exp_f32 v87,v87
v_fma_f32 v84,v84,v119,v119
v_fma_f32 v85,v85,v119,v119
v_fma_f32 v86,v86,v119,v119
v_fma_f32 v87,v87,v119,v119
v_rcp_f32 v84,v84
buffer_load_dwordx4 v0,s[36:39],s61 offen offset:3072 sc1 lds
v_rcp_f32 v85,v85
v_rcp_f32 v86,v86
v_rcp_f32 v87,v87
v_mul_f32 v28,v28,v84
v_mul_f32 v29,v29,v85
v_mul_f32 v30,v30,v86
v_mul_f32 v31,v31,v87
v_cvt_pk_bf16_f32 v96,v28,v29
v_cvt_pk_bf16_f32 v97,v30,v31
v_fma_f32 v32,-v116,v20,v32
s_add_u32 m0,s78,8192
s_nop 0
buffer_load_dword v6,s[36:39],s68 offen offset:0 sc1 lds
v_fma_f32 v33,-v116,v21,v33
v_fma_f32 v34,-v116,v22,v34
v_fma_f32 v35,-v116,v23,v35
v_mul_f32 v88,v32,v32
v_mul_f32 v89,v33,v33
v_mul_f32 v90,v34,v34
v_mul_f32 v91,v35,v35
v_fma_f32 v88,v118,v88,v117
v_fma_f32 v89,v118,v89,v117
v_fma_f32 v90,v118,v90,v117
s_add_u32 m0,s62,16384
s_nop 0
buffer_load_dwordx4 v1,s[36:39],s61 offen offset:0 sc1 lds
v_fma_f32 v91,v118,v91,v117
v_mul_f32 v88,v32,v88
v_mul_f32 v89,v33,v89
v_mul_f32 v90,v34,v90
v_mul_f32 v91,v35,v91
v_exp_f32 v88,v88
v_exp_f32 v89,v89
v_exp_f32 v90,v90
v_exp_f32 v91,v91
v_fma_f32 v88,v88,v119,v119
buffer_load_dwordx4 v1,s[36:39],s61 offen offset:1024 sc1 lds
v_fma_f32 v89,v89,v119,v119
v_fma_f32 v90,v90,v119,v119
v_fma_f32 v91,v91,v119,v119
v_rcp_f32 v88,v88
v_rcp_f32 v89,v89
v_rcp_f32 v90,v90
v_rcp_f32 v91,v91
v_mul_f32 v32,v32,v88
v_mul_f32 v33,v33,v89
v_mul_f32 v34,v34,v90
buffer_load_dwordx4 v1,s[36:39],s61 offen offset:2048 sc1 lds
v_mul_f32 v35,v35,v91
v_cvt_pk_bf16_f32 v98,v32,v33
v_cvt_pk_bf16_f32 v99,v34,v35
buffer_store_dwordx4 v[96:99],v3,s[36:39],s73 offen offset:0 sc1
v_fma_f32 v36,-v116,v24,v36
v_fma_f32 v37,-v116,v25,v37
v_fma_f32 v38,-v116,v26,v38
v_fma_f32 v39,-v116,v27,v39
v_mul_f32 v92,v36,v36
v_mul_f32 v93,v37,v37
buffer_load_dwordx4 v1,s[36:39],s61 offen offset:3072 sc1 lds
v_mul_f32 v94,v38,v38
v_mul_f32 v95,v39,v39
v_fma_f32 v92,v118,v92,v117
v_fma_f32 v93,v118,v93,v117
v_fma_f32 v94,v118,v94,v117
v_fma_f32 v95,v118,v95,v117
v_mul_f32 v92,v36,v92
v_mul_f32 v93,v37,v93
v_mul_f32 v94,v38,v94
v_mul_f32 v95,v39,v95
s_lshr_b32 s72,s61,5
buffer_load_dword v108,v8,s[36:39],s72 offen offset:0 sc1
v_exp_f32 v92,v92
v_exp_f32 v93,v93
v_exp_f32 v94,v94
v_exp_f32 v95,v95
v_fma_f32 v92,v92,v119,v119
v_fma_f32 v93,v93,v119,v119
v_fma_f32 v94,v94,v119,v119
v_fma_f32 v95,v95,v119,v119
v_rcp_f32 v92,v92
v_rcp_f32 v93,v93
v_rcp_f32 v94,v94
v_rcp_f32 v95,v95
v_mul_f32 v36,v36,v92
v_mul_f32 v37,v37,v93
v_mul_f32 v38,v38,v94
v_mul_f32 v39,v39,v95
v_cvt_pk_bf16_f32 v100,v36,v37
v_cvt_pk_bf16_f32 v101,v38,v39
buffer_store_dwordx2 v[100:101],v9,s[36:39],s73 offen offset:0 sc1
s_waitcnt vmcnt(7)
s_barrier
ds_read_b32 v13,v7 offset:8192
ds_read_b128 v[52:55],v2 offset:32768
ds_read_b128 v[56:59],v2 offset:33792
ds_read_b128 v[60:63],v2 offset:34816
ds_read_b128 v[64:67],v2 offset:35840
ds_read_b128 v[68:71],v2 offset:36864
s_mul_i32 s70,s47,5
s_lshr_b32 s70,s70,6
buffer_store_dword v5,v4,s[36:39],s70 offen offset:0 sc1
s_waitcnt lgkmcnt(5))"
R"(
v_cmp_eq_u32 vcc,s60,v13
s_nop 0
s_cmp_eq_u32 vcc_hi,-1
s_cselect_b32 s67,s50,s59
s_lshr_b32 s61,s67,0
s_xor_b32 s62,s63,65536
s_mov_b32 s47,s48
s_mov_b32 s48,s59
s_mul_i32 s70,s59,5
s_lshr_b32 s70,s70,6
buffer_store_dword v5,v4,s[36:39],s70 offen offset:0 sc1
s_waitcnt lgkmcnt(3)
s_setpc_b64 s[92:93]
Lg3_done1_%=:
s_waitcnt vmcnt(0)
s_barrier
s_mul_i32 s70,s47,5
s_lshr_b32 s70,s70,6
buffer_store_dword v5,v4,s[36:39],s70 offen offset:0 sc1
s_mul_i32 s70,s48,5
s_lshr_b32 s70,s70,6
buffer_store_dword v5,v4,s[36:39],s70 offen offset:0 sc1
s_branch Lg3_done_%=
Lg3_done_%=:
s_waitcnt vmcnt(0) lgkmcnt(0)
s_barrier
s_endpgm
Lcode_g3_e:)"
  :: "s"(ws), "s"(wave), "s"(sStep), "s"(sX1), "s"(sX2), "s"(sU0), "s"(sWw), "s"(sBuf), "s"(sEp), "v"(vSrc), "v"(vA), "v"(vOut), "v"(vFlag), "v"(vPollOff), "v"(vSnapRd), "v"(vAuxS), "v"(vOut2), "v"(vStW), "v"(vStR), "v"(vWd0), "v"(vWd1), "v"(vWr0), "v"(vWr1), "v"(vWr2), "s"(w3) : "v0", "v1", "v2", "v3", "v4", "v5", "v6", "v7", "v8", "v9", "v10", "v11", "v12", "v13", "v14", "v15", "v16", "v17", "v18", "v19", "v20", "v21", "v22", "v23", "v24", "v25", "v26", "v27", "v28", "v29", "v30", "v31", "v32", "v33", "v34", "v35", "v36", "v37", "v38", "v39", "v40", "v41", "v42", "v43", "v44", "v45", "v46", "v47", "v48", "v49", "v50", "v51", "v52", "v53", "v54", "v55", "v56", "v57", "v58", "v59", "v60", "v61", "v62", "v63", "v64", "v65", "v66", "v67", "v68", "v69", "v70", "v71", "v72", "v73", "v74", "v75", "v76", "v77", "v78", "v79", "v80", "v81", "v82", "v83", "v84", "v85", "v86", "v87", "v88", "v89", "v90", "v91", "v92", "v93", "v94", "v95", "v96", "v97", "v98", "v99", "v100", "v101", "v102", "v103", "v104", "v105", "v106", "v107", "v108", "v109", "v110", "v111", "v112", "v113", "v114", "v115", "v116", "v117", "v118", "v119", "v120", "v121", "v122", "v123", "v124", "v125", "v126", "v127", "v128", "v129", "v130", "v131", "v132", "v133", "v134", "v135", "v136", "v137", "v138", "v139", "v140", "v141", "v142", "v143", "v144", "v145", "v146", "v147", "v148", "v149", "v150", "v151", "v152", "v153", "v154", "v155", "v156", "v157", "v158", "v159", "v160", "v161", "v162", "v163", "v164", "v165", "v166", "v167", "v168", "v169", "v170", "v171", "v172", "v173", "v174", "v175", "v176", "v177", "v178", "v179", "v180", "v181", "v182", "v183", "v184", "v185", "v186", "v187", "v188", "v189", "v190", "v191", "v192", "v193", "v194", "v195", "v196", "v197", "v198", "v199", "v200", "v201", "v202", "v203", "v204", "v205", "v206", "v207", "v208", "v209", "v210", "v211", "v212", "v213", "v214", "v215", "v216", "v217", "v218", "v219", "v220", "v221", "v222", "v223", "v224", "v225", "v226", "v227", "v228", "v229", "v230", "v231", "v255", "a0", "a1", "a2", "a3", "a4", "a5", "a6", "a7", "a8", "a9", "a10", "a11", "a12", "a13", "a14", "a15", "a16", "a17", "a18", "a19", "a20", "a21", "a22", "a23", "a24", "a25", "a26", "a27", "a28", "a29", "a30", "a31", "a32", "a33", "a34", "a35", "a36", "a37", "a38", "a39", "a40", "a41", "a42", "a43", "a44", "a45", "a46", "a47", "a48", "a49", "a50", "a51", "a52", "a53", "a54", "a55", "a56", "a57", "a58", "a59", "a60", "a61", "a62", "a63", "a64", "a65", "a66", "a67", "a68", "a69", "a70", "a71", "a72", "a73", "a74", "a75", "a76", "a77", "a78", "a79", "a80", "a81", "a82", "a83", "a84", "a85", "a86", "a87", "a88", "a89", "a90", "a91", "a92", "a93", "a94", "a95", "a96", "a97", "a98", "a99", "a100", "a101", "a102", "a103", "a104", "a105", "a106", "a107", "a108", "a109", "a110", "a111", "a112", "a113", "a114", "a115", "a116", "a117", "a118", "a119", "a120", "a121", "a122", "a123", "a124", "a125", "a126", "a127", "a128", "a129", "a130", "a131", "a132", "a133", "a134", "a135", "a136", "a137", "a138", "a139", "a140", "a141", "a142", "a143", "a144", "a145", "a146", "a147", "a148", "a149", "a150", "a151", "a152", "a153", "a154", "a155", "a156", "a157", "a158", "a159", "a160", "a161", "a162", "a163", "a164", "a165", "a166", "a167", "a168", "a169", "a170", "a171", "a172", "a173", "a174", "a175", "a176", "a177", "a178", "a179", "a180", "a181", "a182", "a183", "a184", "a185", "a186", "a187", "a188", "a189", "a190", "a191", "a192", "a193", "a194", "a195", "a196", "a197", "a198", "a199", "a200", "a201", "a202", "a203", "a204", "a205", "a206", "a207", "a208", "a209", "a210", "a211", "a212", "a213", "a214", "a215", "a216", "a217", "a218", "a219", "a220", "a221", "a222", "a223", "a224", "a225", "a226", "a227", "a228", "a229", "a230", "a231", "a232", "a233", "a234", "a235", "a236", "a237", "a238", "a239", "a240", "a241", "a242", "a243", "a244", "a245", "a246", "a247", "a248", "a249", "a250", "a251", "a252", "a253", "a254", "a255", "s36", "s37", "s38", "s39", "s40", "s41", "s42", "s43", "s44", "s45", "s46", "s47", "s48", "s49", "s50", "s51", "s52", "s53", "s54", "s55", "s56", "s57", "s58", "s59", "s60", "s61", "s62", "s63", "s64", "s65", "s66", "s67", "s68", "s69", "s70", "s71", "s72", "s73", "s74", "s75", "s76", "s77", "s78", "s79", "s80", "s81", "s82", "s83", "s84", "s85", "s86", "s87", "s88", "s89", "s90", "s91", "s92", "s93", "s94", "s95", "s96", "s97", "s98", "s99", "vcc", "scc", "memory"); } else { asm volatile(R"(
Lcode_g3o_b:
s_mov_b64 s[36:37],%0
s_mov_b32 s38,122727552
s_mov_b32 s39,0x00020000
s_mov_b32 s54,%1
s_mov_b32 s55,%2
s_mov_b32 s56,%3
s_mov_b32 s58,%4
s_mov_b32 s83,%5
s_mov_b32 s82,%6
s_mov_b32 s84,%7
s_mov_b32 s60,%8
v_mov_b32 v0,%9
v_mov_b32 v2,%10
v_mov_b32 v3,%11
v_mov_b32 v4,%12
v_mov_b32 v6,%13
v_mov_b32 v7,%14
v_mov_b32 v8,%15
v_mov_b32 v9,%16
v_mov_b32 v10,%17
v_mov_b32 v11,%18
v_mov_b32 v24,%19
v_mov_b32 v25,%20
v_mov_b32 v26,%21
v_mov_b32 v27,%22
v_mov_b32 v28,%23
s_mov_b64 s[40:41],%24
s_mov_b32 s42,7340032
s_mov_b32 s43,0x00020000
s_lshl_b32 s88,s54,11
s_add_u32 m0,s88,0
s_add_u32 s61,s82,0
buffer_load_dwordx4 v24,s[40:43],s61 offen offset:0  lds
s_add_u32 m0,s88,1024
s_nop 0
buffer_load_dwordx4 v25,s[40:43],s61 offen offset:0  lds
s_add_u32 m0,s88,8064
s_nop 0
buffer_load_dwordx4 v24,s[40:43],s61 offen offset:128  lds
s_add_u32 m0,s88,9088
s_nop 0
buffer_load_dwordx4 v25,s[40:43],s61 offen offset:128  lds
s_add_u32 m0,s88,16384
s_add_u32 s61,s82,458752
buffer_load_dwordx4 v24,s[40:43],s61 offen offset:0  lds
s_add_u32 m0,s88,17408
s_nop 0
buffer_load_dwordx4 v25,s[40:43],s61 offen offset:0  lds
s_add_u32 m0,s88,24448
s_nop 0
buffer_load_dwordx4 v24,s[40:43],s61 offen offset:128  lds
s_add_u32 m0,s88,25472
s_nop 0
buffer_load_dwordx4 v25,s[40:43],s61 offen offset:128  lds
s_add_u32 m0,s88,32768
s_add_u32 s61,s82,917504
buffer_load_dwordx4 v24,s[40:43],s61 offen offset:0  lds
s_add_u32 m0,s88,33792
s_nop 0
buffer_load_dwordx4 v25,s[40:43],s61 offen offset:0  lds
s_add_u32 m0,s88,40832
s_nop 0
buffer_load_dwordx4 v24,s[40:43],s61 offen offset:128  lds
s_add_u32 m0,s88,41856
s_nop 0
buffer_load_dwordx4 v25,s[40:43],s61 offen offset:128  lds
s_add_u32 m0,s88,49152
s_add_u32 s61,s82,1376256
buffer_load_dwordx4 v24,s[40:43],s61 offen offset:0  lds
s_add_u32 m0,s88,50176
s_nop 0
buffer_load_dwordx4 v25,s[40:43],s61 offen offset:0  lds
s_add_u32 m0,s88,57216
s_nop 0
buffer_load_dwordx4 v24,s[40:43],s61 offen offset:128  lds
s_add_u32 m0,s88,58240
s_nop 0
buffer_load_dwordx4 v25,s[40:43],s61 offen offset:128  lds
s_add_u32 m0,s88,65536
s_add_u32 s61,s82,1835008
buffer_load_dwordx4 v24,s[40:43],s61 offen offset:0  lds
s_add_u32 m0,s88,66560
s_nop 0
buffer_load_dwordx4 v25,s[40:43],s61 offen offset:0  lds
s_add_u32 m0,s88,73600
s_nop 0
buffer_load_dwordx4 v24,s[40:43],s61 offen offset:128  lds
s_add_u32 m0,s88,74624
s_nop 0
buffer_load_dwordx4 v25,s[40:43],s61 offen offset:128  lds
s_add_u32 m0,s88,81920
s_add_u32 s61,s82,2293760
buffer_load_dwordx4 v24,s[40:43],s61 offen offset:0  lds
s_add_u32 m0,s88,82944
s_nop 0
buffer_load_dwordx4 v25,s[40:43],s61 offen offset:0  lds
s_add_u32 m0,s88,89984
s_nop 0
buffer_load_dwordx4 v24,s[40:43],s61 offen offset:128  lds
s_add_u32 m0,s88,91008
s_nop 0
buffer_load_dwordx4 v25,s[40:43],s61 offen offset:128  lds
s_add_u32 m0,s88,98304
s_add_u32 s61,s82,2752512
buffer_load_dwordx4 v24,s[40:43],s61 offen offset:0  lds
s_add_u32 m0,s88,99328
s_nop 0
buffer_load_dwordx4 v25,s[40:43],s61 offen offset:0  lds
s_add_u32 m0,s88,106368
s_nop 0
buffer_load_dwordx4 v24,s[40:43],s61 offen offset:128  lds
s_add_u32 m0,s88,107392
s_nop 0
buffer_load_dwordx4 v25,s[40:43],s61 offen offset:128  lds
v_add_u32 v32,49152,v26
v_add_u32 v33,49152,v27
v_add_u32 v34,98304,v26
v_add_u32 v35,98304,v27
v_mov_b32 v72,0x3f803f80
v_mov_b32 v73,0x3f803f80
v_mov_b32 v74,0x3f803f80
v_mov_b32 v75,0x3f803f80
s_waitcnt vmcnt(24)
s_barrier
s_add_u32 m0,s88,114688
s_add_u32 s61,s82,3211264
buffer_load_dwordx4 v24,s[40:43],s61 offen offset:0  lds
s_add_u32 m0,s88,115712
ds_read_b64_tr_b16 a[0:1],v26 offset:0
buffer_load_dwordx4 v25,s[40:43],s61 offen offset:0  lds
s_add_u32 m0,s88,122752
ds_read_b64_tr_b16 a[2:3],v26 offset:512
buffer_load_dwordx4 v24,s[40:43],s61 offen offset:128  lds
s_add_u32 m0,s88,123776
ds_read_b64_tr_b16 a[4:5],v27 offset:0
buffer_load_dwordx4 v25,s[40:43],s61 offen offset:128  lds
ds_read_b64_tr_b16 a[6:7],v27 offset:512
ds_read_b64_tr_b16 a[8:9],v26 offset:4096
ds_read_b64_tr_b16 a[10:11],v26 offset:4608
ds_read_b64_tr_b16 a[12:13],v27 offset:4096
ds_read_b64_tr_b16 a[14:15],v27 offset:4608
s_waitcnt vmcnt(24)
s_barrier
s_add_u32 m0,s88,131072
s_add_u32 s61,s82,3670016
buffer_load_dwordx4 v24,s[40:43],s61 offen offset:0  lds
s_add_u32 m0,s88,132096
ds_read_b64_tr_b16 a[16:17],v26 offset:16384
buffer_load_dwordx4 v25,s[40:43],s61 offen offset:0  lds
s_add_u32 m0,s88,139136
ds_read_b64_tr_b16 a[18:19],v26 offset:16896
buffer_load_dwordx4 v24,s[40:43],s61 offen offset:128  lds
s_add_u32 m0,s88,140160
ds_read_b64_tr_b16 a[20:21],v27 offset:16384
buffer_load_dwordx4 v25,s[40:43],s61 offen offset:128  lds
ds_read_b64_tr_b16 a[22:23],v27 offset:16896
ds_read_b64_tr_b16 a[24:25],v26 offset:20480
ds_read_b64_tr_b16 a[26:27],v26 offset:20992
ds_read_b64_tr_b16 a[28:29],v27 offset:20480
ds_read_b64_tr_b16 a[30:31],v27 offset:20992
s_waitcnt lgkmcnt(8)
v_mfma_f32_16x16x32_bf16 v[16:19],a[0:3],v[72:75],0
v_mfma_f32_16x16x32_bf16 v[20:23],a[4:7],v[72:75],0
v_mfma_f32_16x16x32_bf16 v[16:19],a[8:11],v[72:75],v[16:19]
v_mfma_f32_16x16x32_bf16 v[20:23],a[12:15],v[72:75],v[20:23]
s_waitcnt vmcnt(24)
s_barrier
s_add_u32 m0,s88,0
s_add_u32 s61,s82,4128768
buffer_load_dwordx4 v24,s[40:43],s61 offen offset:0  lds
s_add_u32 m0,s88,1024
ds_read_b64_tr_b16 a[32:33],v26 offset:32768
buffer_load_dwordx4 v25,s[40:43],s61 offen offset:0  lds
s_add_u32 m0,s88,8064
ds_read_b64_tr_b16 a[34:35],v26 offset:33280
buffer_load_dwordx4 v24,s[40:43],s61 offen offset:128  lds
s_add_u32 m0,s88,9088
ds_read_b64_tr_b16 a[36:37],v27 offset:32768
buffer_load_dwordx4 v25,s[40:43],s61 offen offset:128  lds
ds_read_b64_tr_b16 a[38:39],v27 offset:33280
ds_read_b64_tr_b16 a[40:41],v26 offset:36864
ds_read_b64_tr_b16 a[42:43],v26 offset:37376
ds_read_b64_tr_b16 a[44:45],v27 offset:36864
ds_read_b64_tr_b16 a[46:47],v27 offset:37376
s_waitcnt lgkmcnt(8)
v_mfma_f32_16x16x32_bf16 v[16:19],a[16:19],v[72:75],v[16:19]
v_mfma_f32_16x16x32_bf16 v[20:23],a[20:23],v[72:75],v[20:23]
v_mfma_f32_16x16x32_bf16 v[16:19],a[24:27],v[72:75],v[16:19]
v_mfma_f32_16x16x32_bf16 v[20:23],a[28:31],v[72:75],v[20:23]
s_waitcnt vmcnt(24)
s_barrier
s_add_u32 m0,s88,16384
s_add_u32 s61,s82,4587520
buffer_load_dwordx4 v24,s[40:43],s61 offen offset:0  lds
s_add_u32 m0,s88,17408
ds_read_b64_tr_b16 a[48:49],v32 offset:0
buffer_load_dwordx4 v25,s[40:43],s61 offen offset:0  lds
s_add_u32 m0,s88,24448
ds_read_b64_tr_b16 a[50:51],v32 offset:512)"
R"(
buffer_load_dwordx4 v24,s[40:43],s61 offen offset:128  lds
s_add_u32 m0,s88,25472
ds_read_b64_tr_b16 a[52:53],v33 offset:0
buffer_load_dwordx4 v25,s[40:43],s61 offen offset:128  lds
ds_read_b64_tr_b16 a[54:55],v33 offset:512
ds_read_b64_tr_b16 a[56:57],v32 offset:4096
ds_read_b64_tr_b16 a[58:59],v32 offset:4608
ds_read_b64_tr_b16 a[60:61],v33 offset:4096
ds_read_b64_tr_b16 a[62:63],v33 offset:4608
s_waitcnt lgkmcnt(8)
v_mfma_f32_16x16x32_bf16 v[16:19],a[32:35],v[72:75],v[16:19]
v_mfma_f32_16x16x32_bf16 v[20:23],a[36:39],v[72:75],v[20:23]
v_mfma_f32_16x16x32_bf16 v[16:19],a[40:43],v[72:75],v[16:19]
v_mfma_f32_16x16x32_bf16 v[20:23],a[44:47],v[72:75],v[20:23]
s_waitcnt vmcnt(24)
s_barrier
s_add_u32 m0,s88,32768
s_add_u32 s61,s82,5046272
buffer_load_dwordx4 v24,s[40:43],s61 offen offset:0  lds
s_add_u32 m0,s88,33792
ds_read_b64_tr_b16 a[64:65],v32 offset:16384
buffer_load_dwordx4 v25,s[40:43],s61 offen offset:0  lds
s_add_u32 m0,s88,40832
ds_read_b64_tr_b16 a[66:67],v32 offset:16896
buffer_load_dwordx4 v24,s[40:43],s61 offen offset:128  lds
s_add_u32 m0,s88,41856
ds_read_b64_tr_b16 a[68:69],v33 offset:16384
buffer_load_dwordx4 v25,s[40:43],s61 offen offset:128  lds
ds_read_b64_tr_b16 a[70:71],v33 offset:16896
ds_read_b64_tr_b16 a[72:73],v32 offset:20480
ds_read_b64_tr_b16 a[74:75],v32 offset:20992
ds_read_b64_tr_b16 a[76:77],v33 offset:20480
ds_read_b64_tr_b16 a[78:79],v33 offset:20992
s_waitcnt lgkmcnt(8)
v_mfma_f32_16x16x32_bf16 v[16:19],a[48:51],v[72:75],v[16:19]
v_mfma_f32_16x16x32_bf16 v[20:23],a[52:55],v[72:75],v[20:23]
v_mfma_f32_16x16x32_bf16 v[16:19],a[56:59],v[72:75],v[16:19]
v_mfma_f32_16x16x32_bf16 v[20:23],a[60:63],v[72:75],v[20:23]
s_waitcnt vmcnt(24)
s_barrier
s_add_u32 m0,s88,49152
s_add_u32 s61,s82,5505024
buffer_load_dwordx4 v24,s[40:43],s61 offen offset:0  lds
s_add_u32 m0,s88,50176
ds_read_b64_tr_b16 a[80:81],v32 offset:32768
buffer_load_dwordx4 v25,s[40:43],s61 offen offset:0  lds
s_add_u32 m0,s88,57216
ds_read_b64_tr_b16 a[82:83],v32 offset:33280
buffer_load_dwordx4 v24,s[40:43],s61 offen offset:128  lds
s_add_u32 m0,s88,58240
ds_read_b64_tr_b16 a[84:85],v33 offset:32768
buffer_load_dwordx4 v25,s[40:43],s61 offen offset:128  lds
ds_read_b64_tr_b16 a[86:87],v33 offset:33280
ds_read_b64_tr_b16 a[88:89],v32 offset:36864
ds_read_b64_tr_b16 a[90:91],v32 offset:37376
ds_read_b64_tr_b16 a[92:93],v33 offset:36864
ds_read_b64_tr_b16 a[94:95],v33 offset:37376
s_waitcnt lgkmcnt(8)
v_mfma_f32_16x16x32_bf16 v[16:19],a[64:67],v[72:75],v[16:19]
v_mfma_f32_16x16x32_bf16 v[20:23],a[68:71],v[72:75],v[20:23]
v_mfma_f32_16x16x32_bf16 v[16:19],a[72:75],v[72:75],v[16:19]
v_mfma_f32_16x16x32_bf16 v[20:23],a[76:79],v[72:75],v[20:23]
s_waitcnt vmcnt(24)
s_barrier
s_add_u32 m0,s88,65536
s_add_u32 s61,s82,5963776
buffer_load_dwordx4 v24,s[40:43],s61 offen offset:0  lds
s_add_u32 m0,s88,66560
ds_read_b64_tr_b16 a[96:97],v34 offset:0
buffer_load_dwordx4 v25,s[40:43],s61 offen offset:0  lds
s_add_u32 m0,s88,73600
ds_read_b64_tr_b16 a[98:99],v34 offset:512
buffer_load_dwordx4 v24,s[40:43],s61 offen offset:128  lds
s_add_u32 m0,s88,74624
ds_read_b64_tr_b16 a[100:101],v35 offset:0
buffer_load_dwordx4 v25,s[40:43],s61 offen offset:128  lds
ds_read_b64_tr_b16 a[102:103],v35 offset:512
ds_read_b64_tr_b16 a[104:105],v34 offset:4096
ds_read_b64_tr_b16 a[106:107],v34 offset:4608
ds_read_b64_tr_b16 a[108:109],v35 offset:4096
ds_read_b64_tr_b16 a[110:111],v35 offset:4608
s_waitcnt lgkmcnt(8)
v_mfma_f32_16x16x32_bf16 v[16:19],a[80:83],v[72:75],v[16:19]
v_mfma_f32_16x16x32_bf16 v[20:23],a[84:87],v[72:75],v[20:23]
v_mfma_f32_16x16x32_bf16 v[16:19],a[88:91],v[72:75],v[16:19]
v_mfma_f32_16x16x32_bf16 v[20:23],a[92:95],v[72:75],v[20:23]
s_waitcnt vmcnt(24)
s_barrier
s_add_u32 m0,s88,81920
s_add_u32 s61,s82,6422528
buffer_load_dwordx4 v24,s[40:43],s61 offen offset:0  lds
s_add_u32 m0,s88,82944
ds_read_b64_tr_b16 a[112:113],v34 offset:16384
buffer_load_dwordx4 v25,s[40:43],s61 offen offset:0  lds
s_add_u32 m0,s88,89984
ds_read_b64_tr_b16 a[114:115],v34 offset:16896
buffer_load_dwordx4 v24,s[40:43],s61 offen offset:128  lds
s_add_u32 m0,s88,91008
ds_read_b64_tr_b16 a[116:117],v35 offset:16384
buffer_load_dwordx4 v25,s[40:43],s61 offen offset:128  lds
ds_read_b64_tr_b16 a[118:119],v35 offset:16896
ds_read_b64_tr_b16 a[120:121],v34 offset:20480
ds_read_b64_tr_b16 a[122:123],v34 offset:20992
ds_read_b64_tr_b16 a[124:125],v35 offset:20480
ds_read_b64_tr_b16 a[126:127],v35 offset:20992
s_waitcnt lgkmcnt(8)
v_mfma_f32_16x16x32_bf16 v[16:19],a[96:99],v[72:75],v[16:19]
v_mfma_f32_16x16x32_bf16 v[20:23],a[100:103],v[72:75],v[20:23]
v_mfma_f32_16x16x32_bf16 v[16:19],a[104:107],v[72:75],v[16:19]
v_mfma_f32_16x16x32_bf16 v[20:23],a[108:111],v[72:75],v[20:23]
s_waitcnt vmcnt(24)
s_barrier
s_add_u32 m0,s88,98304
s_add_u32 s61,s82,6881280
buffer_load_dwordx4 v24,s[40:43],s61 offen offset:0  lds
s_add_u32 m0,s88,99328
ds_read_b64_tr_b16 a[128:129],v34 offset:32768
buffer_load_dwordx4 v25,s[40:43],s61 offen offset:0  lds
s_add_u32 m0,s88,106368
ds_read_b64_tr_b16 a[130:131],v34 offset:33280
buffer_load_dwordx4 v24,s[40:43],s61 offen offset:128  lds
s_add_u32 m0,s88,107392
ds_read_b64_tr_b16 a[132:133],v35 offset:32768
buffer_load_dwordx4 v25,s[40:43],s61 offen offset:128  lds
ds_read_b64_tr_b16 a[134:135],v35 offset:33280
ds_read_b64_tr_b16 a[136:137],v34 offset:36864
ds_read_b64_tr_b16 a[138:139],v34 offset:37376
ds_read_b64_tr_b16 a[140:141],v35 offset:36864
ds_read_b64_tr_b16 a[142:143],v35 offset:37376
s_waitcnt lgkmcnt(8)
v_mfma_f32_16x16x32_bf16 v[16:19],a[112:115],v[72:75],v[16:19]
v_mfma_f32_16x16x32_bf16 v[20:23],a[116:119],v[72:75],v[20:23]
v_mfma_f32_16x16x32_bf16 v[16:19],a[120:123],v[72:75],v[16:19]
v_mfma_f32_16x16x32_bf16 v[20:23],a[124:127],v[72:75],v[20:23]
s_waitcnt vmcnt(24)
s_barrier
ds_read_b64_tr_b16 a[144:145],v26 offset:0
ds_read_b64_tr_b16 a[146:147],v26 offset:512
ds_read_b64_tr_b16 a[148:149],v27 offset:0
ds_read_b64_tr_b16 a[150:151],v27 offset:512
ds_read_b64_tr_b16 a[152:153],v26 offset:4096
ds_read_b64_tr_b16 a[154:155],v26 offset:4608
ds_read_b64_tr_b16 a[156:157],v27 offset:4096
ds_read_b64_tr_b16 a[158:159],v27 offset:4608
s_waitcnt lgkmcnt(8)
v_mfma_f32_16x16x32_bf16 v[16:19],a[128:131],v[72:75],v[16:19]
v_mfma_f32_16x16x32_bf16 v[20:23],a[132:135],v[72:75],v[20:23]
v_mfma_f32_16x16x32_bf16 v[16:19],a[136:139],v[72:75],v[16:19]
v_mfma_f32_16x16x32_bf16 v[20:23],a[140:143],v[72:75],v[20:23]
s_waitcnt vmcnt(20)
s_barrier
ds_read_b64_tr_b16 a[160:161],v26 offset:16384
ds_read_b64_tr_b16 a[162:163],v26 offset:16896
ds_read_b64_tr_b16 a[164:165],v27 offset:16384
ds_read_b64_tr_b16 a[166:167],v27 offset:16896
ds_read_b64_tr_b16 a[168:169],v26 offset:20480
ds_read_b64_tr_b16 a[170:171],v26 offset:20992
ds_read_b64_tr_b16 a[172:173],v27 offset:20480
ds_read_b64_tr_b16 a[174:175],v27 offset:20992
s_waitcnt lgkmcnt(8)
v_mfma_f32_16x16x32_bf16 v[16:19],a[144:147],v[72:75],v[16:19]
v_mfma_f32_16x16x32_bf16 v[20:23],a[148:151],v[72:75],v[20:23]
v_mfma_f32_16x16x32_bf16 v[16:19],a[152:155],v[72:75],v[16:19]
v_mfma_f32_16x16x32_bf16 v[20:23],a[156:159],v[72:75],v[20:23]
s_waitcnt vmcnt(16)
s_barrier
ds_read_b64_tr_b16 a[176:177],v26 offset:32768
ds_read_b64_tr_b16 a[178:179],v26 offset:33280
ds_read_b64_tr_b16 a[180:181],v27 offset:32768
ds_read_b64_tr_b16 a[182:183],v27 offset:33280
ds_read_b64_tr_b16 a[184:185],v26 offset:36864
ds_read_b64_tr_b16 a[186:187],v26 offset:37376
ds_read_b64_tr_b16 a[188:189],v27 offset:36864
ds_read_b64_tr_b16 a[190:191],v27 offset:37376
s_getpc_b64 s[46:47]
Lg3o_cp_%=:
s_add_u32 s46,s46,Lg3o_init_%=-Lg3o_cp_%=
s_addc_u32 s47,s47,0
s_lshl_b32 s68,s54,13
v_lshl_add_u32 v56,v2,3,s68
v_min_u32 v57,Lg3o_done_%=-Lg3o_init_%=,v56
global_load_dword v60,v57,s[46:47]
s_waitcnt lgkmcnt(8)
v_mfma_f32_16x16x32_bf16 v[16:19],a[160:163],v[72:75],v[16:19]
v_mfma_f32_16x16x32_bf16 v[20:23],a[164:167],v[72:75],v[20:23]
v_mfma_f32_16x16x32_bf16 v[16:19],a[168:171],v[72:75],v[16:19]
v_mfma_f32_16x16x32_bf16 v[20:23],a[172:175],v[72:75],v[20:23]
s_waitcnt vmcnt(13)
s_barrier
ds_read_b64_tr_b16 a[192:193],v32 offset:0
ds_read_b64_tr_b16 a[194:195],v32 offset:512
ds_read_b64_tr_b16 a[196:197],v33 offset:0
ds_read_b64_tr_b16 a[198:199],v33 offset:512
ds_read_b64_tr_b16 a[200:201],v32 offset:4096
ds_read_b64_tr_b16 a[202:203],v32 offset:4608
ds_read_b64_tr_b16 a[204:205],v33 offset:4096
ds_read_b64_tr_b16 a[206:207],v33 offset:4608
s_waitcnt lgkmcnt(8)
v_mfma_f32_16x16x32_bf16 v[16:19],a[176:179],v[72:75],v[16:19]
v_mfma_f32_16x16x32_bf16 v[20:23],a[180:183],v[72:75],v[20:23])"
R"(
v_mfma_f32_16x16x32_bf16 v[16:19],a[184:187],v[72:75],v[16:19]
v_mfma_f32_16x16x32_bf16 v[20:23],a[188:191],v[72:75],v[20:23]
s_waitcnt vmcnt(9)
s_barrier
ds_read_b64_tr_b16 a[208:209],v32 offset:16384
ds_read_b64_tr_b16 a[210:211],v32 offset:16896
ds_read_b64_tr_b16 a[212:213],v33 offset:16384
ds_read_b64_tr_b16 a[214:215],v33 offset:16896
ds_read_b64_tr_b16 a[216:217],v32 offset:20480
ds_read_b64_tr_b16 a[218:219],v32 offset:20992
ds_read_b64_tr_b16 a[220:221],v33 offset:20480
ds_read_b64_tr_b16 a[222:223],v33 offset:20992
s_waitcnt lgkmcnt(8)
v_mfma_f32_16x16x32_bf16 v[16:19],a[192:195],v[72:75],v[16:19]
v_mfma_f32_16x16x32_bf16 v[20:23],a[196:199],v[72:75],v[20:23]
v_mfma_f32_16x16x32_bf16 v[16:19],a[200:203],v[72:75],v[16:19]
v_mfma_f32_16x16x32_bf16 v[20:23],a[204:207],v[72:75],v[20:23]
s_waitcnt vmcnt(5)
s_barrier
ds_read_b64_tr_b16 a[224:225],v32 offset:32768
ds_read_b64_tr_b16 a[226:227],v32 offset:33280
ds_read_b64_tr_b16 a[228:229],v33 offset:32768
ds_read_b64_tr_b16 a[230:231],v33 offset:33280
ds_read_b64_tr_b16 a[232:233],v32 offset:36864
ds_read_b64_tr_b16 a[234:235],v32 offset:37376
ds_read_b64_tr_b16 a[236:237],v33 offset:36864
ds_read_b64_tr_b16 a[238:239],v33 offset:37376
s_waitcnt lgkmcnt(8)
v_mfma_f32_16x16x32_bf16 v[16:19],a[208:211],v[72:75],v[16:19]
v_mfma_f32_16x16x32_bf16 v[20:23],a[212:215],v[72:75],v[20:23]
v_mfma_f32_16x16x32_bf16 v[16:19],a[216:219],v[72:75],v[16:19]
v_mfma_f32_16x16x32_bf16 v[20:23],a[220:223],v[72:75],v[20:23]
s_waitcnt vmcnt(1)
s_barrier
ds_read_b64_tr_b16 a[240:241],v34 offset:0
ds_read_b64_tr_b16 a[242:243],v34 offset:512
ds_read_b64_tr_b16 a[244:245],v35 offset:0
ds_read_b64_tr_b16 a[246:247],v35 offset:512
ds_read_b64_tr_b16 a[248:249],v34 offset:4096
ds_read_b64_tr_b16 a[250:251],v34 offset:4608
ds_read_b64_tr_b16 a[252:253],v35 offset:4096
ds_read_b64_tr_b16 a[254:255],v35 offset:4608
s_waitcnt lgkmcnt(8)
v_mfma_f32_16x16x32_bf16 v[16:19],a[224:227],v[72:75],v[16:19]
v_mfma_f32_16x16x32_bf16 v[20:23],a[228:231],v[72:75],v[20:23]
v_mfma_f32_16x16x32_bf16 v[16:19],a[232:235],v[72:75],v[16:19]
v_mfma_f32_16x16x32_bf16 v[20:23],a[236:239],v[72:75],v[20:23]
s_waitcnt lgkmcnt(0)
v_mfma_f32_16x16x32_bf16 v[16:19],a[240:243],v[72:75],v[16:19]
v_mfma_f32_16x16x32_bf16 v[20:23],a[244:247],v[72:75],v[20:23]
v_mfma_f32_16x16x32_bf16 v[16:19],a[248:251],v[72:75],v[16:19]
v_mfma_f32_16x16x32_bf16 v[20:23],a[252:255],v[72:75],v[20:23]
s_waitcnt lgkmcnt(0)
s_barrier
Lg3o_init_%=:
v_mov_b32 v5,s60
s_mov_b32 s59,536870912
s_add_u32 s57,s56,131072
s_mov_b32 s79,0
s_mov_b32 s75,0x3a800000
s_mov_b32 s76,0xc0135761
s_mov_b32 s77,0x3d372713
s_mov_b32 s44,s59
s_mov_b32 s45,s59
s_mov_b32 s46,s59
s_mov_b32 s47,s59
s_mov_b32 s48,s83
s_add_u32 s49,s48,s55
s_cmp_eq_u32 s49,s56
s_cselect_b32 s49,s57,s49
s_cmp_eq_u32 s49,s58
s_cselect_b32 s49,s59,s49
s_add_u32 s50,s49,s55
s_cmp_eq_u32 s50,s56
s_cselect_b32 s50,s57,s50
s_cmp_eq_u32 s50,s58
s_cselect_b32 s50,s59,s50
s_add_u32 s51,s50,s55
s_cmp_eq_u32 s51,s56
s_cselect_b32 s51,s57,s51
s_cmp_eq_u32 s51,s58
s_cselect_b32 s51,s59,s51
s_add_u32 s52,s51,s55
s_cmp_eq_u32 s52,s56
s_cselect_b32 s52,s57,s52
s_cmp_eq_u32 s52,s58
s_cselect_b32 s52,s59,s52
s_lshl_b32 s63,s54,12
s_add_u32 s63,s63,65536
s_add_u32 s64,s63,32768
s_lshl_b32 s88,s54,10
s_add_u32 s88,s88,131072
s_lshl_b32 s78,s54,11
s_add_u32 s78,s78,132608
v_add_u32 v1,16384,v0
v_mov_b32 v12,0x3727c5ac
s_getpc_b64 s[90:91]
Lg3o_pc_%=:
s_mov_b32 s68,Lg3o_d1w0_%=-Lg3o_pc_%=
s_cmp_eq_u32 s54,1
s_cselect_b32 s68,Lg3o_d1w1_%=-Lg3o_pc_%=,s68
s_cmp_eq_u32 s54,2
s_cselect_b32 s68,Lg3o_d1w2_%=-Lg3o_pc_%=,s68
s_cmp_eq_u32 s54,3
s_cselect_b32 s68,Lg3o_d1w3_%=-Lg3o_pc_%=,s68
s_add_u32 s92,s90,s68
s_addc_u32 s93,s91,0
s_mov_b32 s68,Lg3o_d0w0_%=-Lg3o_pc_%=
s_cmp_eq_u32 s54,1
s_cselect_b32 s68,Lg3o_d0w1_%=-Lg3o_pc_%=,s68
s_cmp_eq_u32 s54,2
s_cselect_b32 s68,Lg3o_d0w2_%=-Lg3o_pc_%=,s68
s_cmp_eq_u32 s54,3
s_cselect_b32 s68,Lg3o_d0w3_%=-Lg3o_pc_%=,s68
s_add_u32 s90,s90,s68
s_addc_u32 s91,s91,0
s_mov_b32 s66,s59
s_mov_b32 s65,0
s_mov_b32 s62,s63
s_branch Lg3o_slow0_%=
Lg3o_d0w0_%=:
v_mfma_f32_16x16x32_bf16 v[24:27],a[0:3],v[40:43],0
s_add_u32 m0,s62,16384
ds_read_b128 v[68:71],v2 offset:7168
v_mul_f32 v99,v93,v92
v_mfma_f32_16x16x32_bf16 v[28:31],a[4:7],v[40:43],0
buffer_load_dwordx4 v1,s[36:39],s66 offen offset:0 sc1 lds
v_mfma_f32_16x16x32_bf16 v[24:27],a[8:11],v[44:47],v[24:27]
s_mul_i32 s73,s47,7
ds_read_b128 v[40:43],v2 offset:8192
v_mul_f32 v98,s77,v98
v_mfma_f32_16x16x32_bf16 v[28:31],a[12:15],v[44:47],v[28:31]
s_lshr_b32 s73,s73,1
v_mul_f32 v98,v98,v97
v_fma_f32 v32,-v96,v16,v32
v_mfma_f32_16x16x32_bf16 v[24:27],a[16:19],v[48:51],v[24:27]
s_lshr_b32 s72,s66,5
ds_read_b128 v[44:47],v2 offset:9216
v_fma_f32 v33,-v96,v17,v33
v_mfma_f32_16x16x32_bf16 v[28:31],a[20:23],v[48:51],v[28:31]
buffer_load_dwordx4 v1,s[36:39],s66 offen offset:1024 sc1 lds
v_mfma_f32_16x16x32_bf16 v[24:27],a[24:27],v[52:55],v[24:27]
s_lshr_b32 s71,s62,2
ds_read_b128 v[48:51],v2 offset:10240
v_fma_f32 v34,-v96,v18,v34
v_mfma_f32_16x16x32_bf16 v[28:31],a[28:31],v[52:55],v[28:31]
s_waitcnt lgkmcnt(3)
v_fma_f32 v35,-v96,v19,v35
v_mul_f32 v72,v32,v32
v_mfma_f32_16x16x32_bf16 v[24:27],a[32:35],v[56:59],v[24:27]
ds_read_b128 v[52:55],v2 offset:11264
v_mul_f32 v73,v33,v33
v_mfma_f32_16x16x32_bf16 v[28:31],a[36:39],v[56:59],v[28:31]
buffer_load_dwordx4 v1,s[36:39],s66 offen offset:2048 sc1 lds
v_mfma_f32_16x16x32_bf16 v[24:27],a[40:43],v[60:63],v[24:27]
ds_read_b128 v[56:59],v2 offset:12288
v_mul_f32 v74,v34,v34
v_mfma_f32_16x16x32_bf16 v[28:31],a[44:47],v[60:63],v[28:31]
v_mul_f32 v75,v35,v35
v_fma_f32 v72,v98,v72,v97
v_mfma_f32_16x16x32_bf16 v[24:27],a[48:51],v[64:67],v[24:27]
ds_read_b128 v[60:63],v2 offset:13312
v_mul_f32 v72,v32,v72
v_mfma_f32_16x16x32_bf16 v[28:31],a[52:55],v[64:67],v[28:31]
buffer_load_dwordx4 v1,s[36:39],s66 offen offset:3072 sc1 lds
v_mfma_f32_16x16x32_bf16 v[24:27],a[56:59],v[68:71],v[24:27]
s_add_u32 m0,s71,s88
ds_read_b128 v[64:67],v2 offset:14336
v_fma_f32 v73,v98,v73,v97
v_mfma_f32_16x16x32_bf16 v[28:31],a[60:63],v[68:71],v[28:31]
s_waitcnt lgkmcnt(3)
buffer_load_dword v9,s[36:39],s72 offen offset:0 sc1 lds
v_mfma_f32_16x16x32_bf16 v[24:27],a[64:67],v[40:43],v[24:27]
s_waitcnt vmcnt(11)
ds_read_b128 v[68:71],v2 offset:15360
v_mul_f32 v73,v33,v73
v_mfma_f32_16x16x32_bf16 v[28:31],a[68:71],v[40:43],v[28:31]
s_barrier
v_mfma_f32_16x16x32_bf16 v[24:27],a[72:75],v[44:47],v[24:27]
s_xor_b32 s87,s64,65536
ds_read_b128 v[40:43],v2 offset:16384
ds_read_b32 v88,v8 offset:0
v_mfma_f32_16x16x32_bf16 v[28:31],a[76:79],v[44:47],v[28:31]
s_cmp_ge_u32 s66,s59
v_exp_f32 v72,v72
v_mfma_f32_16x16x32_bf16 v[24:27],a[80:83],v[48:51],v[24:27]
s_cselect_b32 s86,s49,s50
ds_read_b128 v[44:47],v2 offset:17408
v_fma_f32 v72,v72,v99,v99
v_mfma_f32_16x16x32_bf16 v[28:31],a[84:87],v[48:51],v[28:31]
s_cselect_b32 s87,s87,s63
v_exp_f32 v73,v73
v_mfma_f32_16x16x32_bf16 v[24:27],a[88:91],v[52:55],v[24:27]
s_lshr_b32 s68,s50,5
ds_read_b128 v[48:51],v2 offset:18432
v_fma_f32 v73,v73,v99,v99
v_mfma_f32_16x16x32_bf16 v[28:31],a[92:95],v[52:55],v[28:31]
s_waitcnt lgkmcnt(4)
v_rcp_f32 v72,v72
v_mfma_f32_16x16x32_bf16 v[24:27],a[96:99],v[56:59],v[24:27])"
R"(
s_waitcnt vmcnt(10) lgkmcnt(2)
ds_read_b128 v[52:55],v2 offset:19456
v_fma_f32 v74,v98,v74,v97
v_mfma_f32_16x16x32_bf16 v[28:31],a[100:103],v[56:59],v[28:31]
s_barrier
v_mfma_f32_16x16x32_bf16 v[24:27],a[104:107],v[60:63],v[24:27]
s_lshr_b32 s70,s51,5
ds_read_b128 v[56:59],v2 offset:20480
ds_read_b32 v13,v7 offset:0
v_mfma_f32_16x16x32_bf16 v[28:31],a[108:111],v[60:63],v[28:31]
s_mov_b32 s62,s87
v_rcp_f32 v73,v73
v_mfma_f32_16x16x32_bf16 v[24:27],a[112:115],v[64:67],v[24:27]
ds_read_b128 v[60:63],v2 offset:21504
v_mul_f32 v74,v34,v74
v_mfma_f32_16x16x32_bf16 v[28:31],a[116:119],v[64:67],v[28:31]
v_exp_f32 v74,v74
v_mfma_f32_16x16x32_bf16 v[24:27],a[120:123],v[68:71],v[24:27]
s_waitcnt lgkmcnt(1)
ds_read_b128 v[64:67],v2 offset:22528
v_fma_f32 v74,v74,v99,v99
v_mfma_f32_16x16x32_bf16 v[28:31],a[124:127],v[68:71],v[28:31]
v_cmp_eq_u32 vcc,s60,v13
v_rcp_f32 v74,v74
v_add_f32_dpp v88,v88,v88 quad_perm:[1,0,3,2] row_mask:0xf bank_mask:0xf
v_mfma_f32_16x16x32_bf16 v[24:27],a[128:131],v[40:43],v[24:27]
s_cmp_eq_u32 vcc_lo,-1
ds_read_b128 v[68:71],v2 offset:23552
v_add_f32_dpp v88,v88,v88 quad_perm:[2,3,0,1] row_mask:0xf bank_mask:0xf
v_mfma_f32_16x16x32_bf16 v[28:31],a[132:135],v[40:43],v[28:31]
s_cselect_b32 s67,s86,s59
s_cmp_ge_u32 s67,s59
s_cselect_b32 s68,s68,s70
s_add_u32 m0,s78,256
v_mfma_f32_16x16x32_bf16 v[24:27],a[136:139],v[44:47],v[24:27]
buffer_load_dword v6,s[36:39],s68 offen offset:0 sc1 lds
v_mfma_f32_16x16x32_bf16 v[28:31],a[140:143],v[44:47],v[28:31]
s_mov_b32 m0,s62
ds_read_b128 v[40:43],v2 offset:24576
v_add_f32_dpp v88,v88,v88 row_half_mirror row_mask:0xf bank_mask:0xf
v_mfma_f32_16x16x32_bf16 v[24:27],a[144:147],v[48:51],v[24:27]
buffer_load_dwordx4 v0,s[36:39],s67 offen offset:0 sc1 lds
v_mfma_f32_16x16x32_bf16 v[28:31],a[148:151],v[48:51],v[28:31]
s_xor_b32 s63,s63,65536
ds_read_b128 v[44:47],v2 offset:25600
v_fma_f32 v75,v98,v75,v97
v_mfma_f32_16x16x32_bf16 v[24:27],a[152:155],v[52:55],v[24:27]
s_add_u32 s53,s52,s55
ds_write_b32 v10,v88 offset:0
v_mfma_f32_16x16x32_bf16 v[28:31],a[156:159],v[52:55],v[28:31]
s_waitcnt lgkmcnt(3)
ds_read_b128 v[48:51],v2 offset:26624
v_mul_f32 v75,v35,v75
v_mfma_f32_16x16x32_bf16 v[24:27],a[160:163],v[56:59],v[24:27]
buffer_load_dwordx4 v0,s[36:39],s67 offen offset:1024 sc1 lds
v_mfma_f32_16x16x32_bf16 v[28:31],a[164:167],v[56:59],v[28:31]
s_cmp_eq_u32 s53,s56
ds_read_b128 v[52:55],v2 offset:27648
v_mul_f32 v32,v32,v72
v_mfma_f32_16x16x32_bf16 v[24:27],a[168:171],v[60:63],v[24:27]
s_cselect_b32 s53,s57,s53
ds_read_b128 v[56:59],v2 offset:28672
v_mul_f32 v33,v33,v73
v_mfma_f32_16x16x32_bf16 v[28:31],a[172:175],v[60:63],v[28:31]
s_cmp_eq_u32 s53,s58
v_exp_f32 v75,v75
v_mfma_f32_16x16x32_bf16 v[24:27],a[176:179],v[64:67],v[24:27]
buffer_load_dwordx4 v0,s[36:39],s67 offen offset:2048 sc1 lds
v_mfma_f32_16x16x32_bf16 v[28:31],a[180:183],v[64:67],v[28:31]
s_cselect_b32 s53,s59,s53
ds_read_b128 v[60:63],v2 offset:29696
v_fma_f32 v75,v75,v99,v99
v_mfma_f32_16x16x32_bf16 v[24:27],a[184:187],v[68:71],v[24:27]
s_cmp_ge_u32 s66,s59
ds_read_b128 v[64:67],v2 offset:30720
v_mul_f32 v34,v34,v74
v_mfma_f32_16x16x32_bf16 v[28:31],a[188:191],v[68:71],v[28:31]
s_waitcnt lgkmcnt(3)
v_rcp_f32 v75,v75
v_mfma_f32_16x16x32_bf16 v[24:27],a[192:195],v[40:43],v[24:27]
s_waitcnt vmcnt(9)
ds_read_b128 v[68:71],v2 offset:31744
v_mul_f32 v35,v35,v75
v_mfma_f32_16x16x32_bf16 v[28:31],a[196:199],v[40:43],v[28:31]
s_barrier
v_fma_f32 v36,-v96,v20,v36
v_fma_f32 v37,-v96,v21,v37
v_fma_f32 v38,-v96,v22,v38
v_fma_f32 v39,-v96,v23,v39
v_mul_f32 v76,v36,v36
v_mul_f32 v77,v37,v37
v_mul_f32 v78,v38,v38
v_mul_f32 v79,v39,v39
v_fma_f32 v76,v98,v76,v97
v_fma_f32 v77,v98,v77,v97
v_fma_f32 v78,v98,v78,v97
v_fma_f32 v79,v98,v79,v97
v_mul_f32 v76,v36,v76
v_mul_f32 v77,v37,v77
v_mul_f32 v78,v38,v78
v_mul_f32 v79,v39,v79
v_exp_f32 v76,v76
v_exp_f32 v77,v77
v_exp_f32 v78,v78
v_exp_f32 v79,v79
v_mfma_f32_16x16x32_bf16 v[24:27],a[200:203],v[44:47],v[24:27]
buffer_load_dwordx4 v0,s[36:39],s67 offen offset:3072 sc1 lds
v_fma_f32 v76,v76,v99,v99
v_fma_f32 v77,v77,v99,v99
v_fma_f32 v78,v78,v99,v99
v_fma_f32 v79,v79,v99,v99
v_rcp_f32 v76,v76
v_rcp_f32 v77,v77
v_rcp_f32 v78,v78
v_rcp_f32 v79,v79
v_mfma_f32_16x16x32_bf16 v[28:31],a[204:207],v[44:47],v[28:31]
ds_read_b128 v[40:43],v2 offset:32768
v_cvt_pk_bf16_f32 v80,v32,v33
v_cvt_pk_bf16_f32 v81,v34,v35
v_mul_f32 v36,v36,v76
v_mul_f32 v37,v37,v77
v_mul_f32 v38,v38,v78
v_mul_f32 v39,v39,v79
v_cvt_pk_bf16_f32 v82,v36,v37
v_cvt_pk_bf16_f32 v83,v38,v39
v_mfma_f32_16x16x32_bf16 v[24:27],a[208:211],v[48:51],v[24:27]
buffer_store_dwordx4 v[80:83],v3,s[36:39],s73 offen offset:0 sc1
ds_read_b128 v[44:47],v2 offset:33792
ds_read_b64 v[92:93],v11 offset:0
v_mfma_f32_16x16x32_bf16 v[28:31],a[212:215],v[48:51],v[28:31]
v_mfma_f32_16x16x32_bf16 v[24:27],a[216:219],v[52:55],v[24:27]
ds_read_b128 v[48:51],v2 offset:34816
v_mfma_f32_16x16x32_bf16 v[28:31],a[220:223],v[52:55],v[28:31]
s_waitcnt lgkmcnt(4)
v_mfma_f32_16x16x32_bf16 v[24:27],a[224:227],v[56:59],v[24:27]
s_waitcnt lgkmcnt(1)
ds_read_b128 v[52:55],v2 offset:35840
v_mul_f32 v96,s75,v92
v_mfma_f32_16x16x32_bf16 v[28:31],a[228:231],v[56:59],v[28:31]
v_fma_f32 v93,v93,s75,v12
v_fma_f32 v93,-v96,v96,v93
v_mfma_f32_16x16x32_bf16 v[24:27],a[232:235],v[60:63],v[24:27]
ds_read_b128 v[56:59],v2 offset:36864
v_max_f32 v93,v93,v12
v_mfma_f32_16x16x32_bf16 v[28:31],a[236:239],v[60:63],v[28:31]
v_rsq_f32 v92,v93
v_mfma_f32_16x16x32_bf16 v[24:27],a[240:243],v[64:67],v[24:27]
ds_read_b128 v[60:63],v2 offset:37888
v_mul_f32 v97,s76,v92
v_mfma_f32_16x16x32_bf16 v[28:31],a[244:247],v[64:67],v[28:31]
v_mul_f32 v98,v92,v92
v_mfma_f32_16x16x32_bf16 v[24:27],a[248:251],v[68:71],v[24:27]
s_waitcnt lgkmcnt(2)
ds_read_b128 v[64:67],v2 offset:38912
v_mfma_f32_16x16x32_bf16 v[28:31],a[252:255],v[68:71],v[28:31]
s_cbranch_scc1 Lg3o_slow1_%=
Lg3o_d1w0_%=:
v_mfma_f32_16x16x32_bf16 v[32:35],a[0:3],v[40:43],0
s_add_u32 m0,s62,16384
ds_read_b128 v[68:71],v2 offset:39936
v_mul_f32 v99,v93,v92
v_mfma_f32_16x16x32_bf16 v[36:39],a[4:7],v[40:43],0
buffer_load_dwordx4 v1,s[36:39],s67 offen offset:0 sc1 lds
v_mfma_f32_16x16x32_bf16 v[32:35],a[8:11],v[44:47],v[32:35]
s_mul_i32 s73,s48,7
ds_read_b128 v[40:43],v2 offset:40960
v_mul_f32 v98,s77,v98
v_mfma_f32_16x16x32_bf16 v[36:39],a[12:15],v[44:47],v[36:39]
s_lshr_b32 s73,s73,1
v_mul_f32 v98,v98,v97
v_fma_f32 v24,-v96,v16,v24
v_mfma_f32_16x16x32_bf16 v[32:35],a[16:19],v[48:51],v[32:35]
s_lshr_b32 s72,s67,5
ds_read_b128 v[44:47],v2 offset:41984
v_fma_f32 v25,-v96,v17,v25
v_mfma_f32_16x16x32_bf16 v[36:39],a[20:23],v[48:51],v[36:39]
buffer_load_dwordx4 v1,s[36:39],s67 offen offset:1024 sc1 lds
v_mfma_f32_16x16x32_bf16 v[32:35],a[24:27],v[52:55],v[32:35]
s_lshr_b32 s71,s62,2
ds_read_b128 v[48:51],v2 offset:43008
v_fma_f32 v26,-v96,v18,v26
v_mfma_f32_16x16x32_bf16 v[36:39],a[28:31],v[52:55],v[36:39]
s_waitcnt lgkmcnt(3)
v_fma_f32 v27,-v96,v19,v27
v_mul_f32 v72,v24,v24
v_mfma_f32_16x16x32_bf16 v[32:35],a[32:35],v[56:59],v[32:35]
s_mov_b64 s[44:45],s[46:47]
ds_read_b128 v[52:55],v2 offset:44032
v_mul_f32 v73,v25,v25
v_mfma_f32_16x16x32_bf16 v[36:39],a[36:39],v[56:59],v[36:39]
buffer_load_dwordx4 v1,s[36:39],s67 offen offset:2048 sc1 lds
v_mfma_f32_16x16x32_bf16 v[32:35],a[40:43],v[60:63],v[32:35]
s_mov_b64 s[46:47],s[48:49]
ds_read_b128 v[56:59],v2 offset:45056
v_mul_f32 v74,v26,v26
v_mfma_f32_16x16x32_bf16 v[36:39],a[44:47],v[60:63],v[36:39]
s_mov_b64 s[48:49],s[50:51]
v_mul_f32 v75,v27,v27
v_fma_f32 v72,v98,v72,v97
v_mfma_f32_16x16x32_bf16 v[32:35],a[48:51],v[64:67],v[32:35])"
R"(
ds_read_b128 v[60:63],v2 offset:46080
v_mul_f32 v72,v24,v72
v_mfma_f32_16x16x32_bf16 v[36:39],a[52:55],v[64:67],v[36:39]
buffer_load_dwordx4 v1,s[36:39],s67 offen offset:3072 sc1 lds
v_mfma_f32_16x16x32_bf16 v[32:35],a[56:59],v[68:71],v[32:35]
s_add_u32 m0,s71,s88
ds_read_b128 v[64:67],v2 offset:47104
v_fma_f32 v73,v98,v73,v97
v_mfma_f32_16x16x32_bf16 v[36:39],a[60:63],v[68:71],v[36:39]
s_waitcnt lgkmcnt(3)
buffer_load_dword v9,s[36:39],s72 offen offset:0 sc1 lds
v_mfma_f32_16x16x32_bf16 v[32:35],a[64:67],v[40:43],v[32:35]
s_waitcnt vmcnt(11)
ds_read_b128 v[68:71],v2 offset:48128
v_mul_f32 v73,v25,v73
v_mfma_f32_16x16x32_bf16 v[36:39],a[68:71],v[40:43],v[36:39]
s_barrier
v_mfma_f32_16x16x32_bf16 v[32:35],a[72:75],v[44:47],v[32:35]
s_xor_b32 s87,s63,65536
ds_read_b128 v[40:43],v2 offset:49152
ds_read_b32 v88,v8 offset:8192
v_mfma_f32_16x16x32_bf16 v[36:39],a[76:79],v[44:47],v[36:39]
s_cmp_ge_u32 s67,s59
v_exp_f32 v72,v72
v_mfma_f32_16x16x32_bf16 v[32:35],a[80:83],v[48:51],v[32:35]
s_cselect_b32 s86,s50,s51
ds_read_b128 v[44:47],v2 offset:50176
v_fma_f32 v72,v72,v99,v99
v_mfma_f32_16x16x32_bf16 v[36:39],a[84:87],v[48:51],v[36:39]
s_cselect_b32 s87,s87,s64
v_exp_f32 v73,v73
v_mfma_f32_16x16x32_bf16 v[32:35],a[88:91],v[52:55],v[32:35]
s_lshr_b32 s68,s51,5
ds_read_b128 v[48:51],v2 offset:51200
v_fma_f32 v73,v73,v99,v99
v_mfma_f32_16x16x32_bf16 v[36:39],a[92:95],v[52:55],v[36:39]
s_waitcnt lgkmcnt(4)
v_rcp_f32 v72,v72
v_mfma_f32_16x16x32_bf16 v[32:35],a[96:99],v[56:59],v[32:35]
s_waitcnt vmcnt(10) lgkmcnt(2)
ds_read_b128 v[52:55],v2 offset:52224
v_fma_f32 v74,v98,v74,v97
v_mfma_f32_16x16x32_bf16 v[36:39],a[100:103],v[56:59],v[36:39]
s_barrier
v_mfma_f32_16x16x32_bf16 v[32:35],a[104:107],v[60:63],v[32:35]
s_lshr_b32 s70,s52,5
ds_read_b128 v[56:59],v2 offset:53248
ds_read_b32 v13,v7 offset:256
v_mfma_f32_16x16x32_bf16 v[36:39],a[108:111],v[60:63],v[36:39]
s_mov_b32 s62,s87
v_rcp_f32 v73,v73
v_mfma_f32_16x16x32_bf16 v[32:35],a[112:115],v[64:67],v[32:35]
s_mov_b64 s[50:51],s[52:53]
ds_read_b128 v[60:63],v2 offset:54272
v_mul_f32 v74,v26,v74
v_mfma_f32_16x16x32_bf16 v[36:39],a[116:119],v[64:67],v[36:39]
v_exp_f32 v74,v74
v_mfma_f32_16x16x32_bf16 v[32:35],a[120:123],v[68:71],v[32:35]
s_waitcnt lgkmcnt(1)
ds_read_b128 v[64:67],v2 offset:55296
v_fma_f32 v74,v74,v99,v99
v_mfma_f32_16x16x32_bf16 v[36:39],a[124:127],v[68:71],v[36:39]
v_cmp_eq_u32 vcc,s60,v13
v_rcp_f32 v74,v74
v_add_f32_dpp v88,v88,v88 quad_perm:[1,0,3,2] row_mask:0xf bank_mask:0xf
v_mfma_f32_16x16x32_bf16 v[32:35],a[128:131],v[40:43],v[32:35]
s_cmp_eq_u32 vcc_lo,-1
ds_read_b128 v[68:71],v2 offset:56320
v_add_f32_dpp v88,v88,v88 quad_perm:[2,3,0,1] row_mask:0xf bank_mask:0xf
v_mfma_f32_16x16x32_bf16 v[36:39],a[132:135],v[40:43],v[36:39]
s_cselect_b32 s66,s86,s59
s_cmp_ge_u32 s66,s59
s_cselect_b32 s68,s68,s70
s_add_u32 m0,s78,0
v_mfma_f32_16x16x32_bf16 v[32:35],a[136:139],v[44:47],v[32:35]
buffer_load_dword v6,s[36:39],s68 offen offset:0 sc1 lds
v_mfma_f32_16x16x32_bf16 v[36:39],a[140:143],v[44:47],v[36:39]
s_mov_b32 m0,s62
ds_read_b128 v[40:43],v2 offset:57344
v_add_f32_dpp v88,v88,v88 row_half_mirror row_mask:0xf bank_mask:0xf
v_mfma_f32_16x16x32_bf16 v[32:35],a[144:147],v[48:51],v[32:35]
buffer_load_dwordx4 v0,s[36:39],s66 offen offset:0 sc1 lds
v_mfma_f32_16x16x32_bf16 v[36:39],a[148:151],v[48:51],v[36:39]
s_xor_b32 s64,s64,65536
ds_read_b128 v[44:47],v2 offset:58368
v_fma_f32 v75,v98,v75,v97
v_mfma_f32_16x16x32_bf16 v[32:35],a[152:155],v[52:55],v[32:35]
s_add_u32 s74,s53,s55
ds_write_b32 v10,v88 offset:128
v_mfma_f32_16x16x32_bf16 v[36:39],a[156:159],v[52:55],v[36:39]
s_waitcnt lgkmcnt(3)
ds_read_b128 v[48:51],v2 offset:59392
v_mul_f32 v75,v27,v75
v_mfma_f32_16x16x32_bf16 v[32:35],a[160:163],v[56:59],v[32:35]
buffer_load_dwordx4 v0,s[36:39],s66 offen offset:1024 sc1 lds
v_mfma_f32_16x16x32_bf16 v[36:39],a[164:167],v[56:59],v[36:39]
s_cmp_eq_u32 s74,s56
ds_read_b128 v[52:55],v2 offset:60416
v_mul_f32 v24,v24,v72
v_mfma_f32_16x16x32_bf16 v[32:35],a[168:171],v[60:63],v[32:35]
s_cselect_b32 s74,s57,s74
ds_read_b128 v[56:59],v2 offset:61440
v_mul_f32 v25,v25,v73
v_mfma_f32_16x16x32_bf16 v[36:39],a[172:175],v[60:63],v[36:39]
s_cmp_eq_u32 s74,s58
v_exp_f32 v75,v75
v_mfma_f32_16x16x32_bf16 v[32:35],a[176:179],v[64:67],v[32:35]
buffer_load_dwordx4 v0,s[36:39],s66 offen offset:2048 sc1 lds
v_mfma_f32_16x16x32_bf16 v[36:39],a[180:183],v[64:67],v[36:39]
s_cselect_b32 s74,s59,s74
ds_read_b128 v[60:63],v2 offset:62464
v_fma_f32 v75,v75,v99,v99
v_mfma_f32_16x16x32_bf16 v[32:35],a[184:187],v[68:71],v[32:35]
s_mov_b32 s52,s74
ds_read_b128 v[64:67],v2 offset:63488
v_mul_f32 v26,v26,v74
v_mfma_f32_16x16x32_bf16 v[36:39],a[188:191],v[68:71],v[36:39]
s_waitcnt lgkmcnt(3)
v_rcp_f32 v75,v75
v_mfma_f32_16x16x32_bf16 v[32:35],a[192:195],v[40:43],v[32:35]
s_waitcnt vmcnt(9)
ds_read_b128 v[68:71],v2 offset:64512
v_xor_b32 v2,65536,v2
v_mfma_f32_16x16x32_bf16 v[36:39],a[196:199],v[40:43],v[36:39]
s_barrier
v_fma_f32 v28,-v96,v20,v28
v_fma_f32 v29,-v96,v21,v29
v_fma_f32 v30,-v96,v22,v30
v_fma_f32 v31,-v96,v23,v31
v_mul_f32 v76,v28,v28
v_mul_f32 v77,v29,v29
v_mul_f32 v78,v30,v30
v_mul_f32 v79,v31,v31
v_fma_f32 v76,v98,v76,v97
v_fma_f32 v77,v98,v77,v97
v_fma_f32 v78,v98,v78,v97
v_fma_f32 v79,v98,v79,v97
v_mul_f32 v76,v28,v76
v_mul_f32 v77,v29,v77
v_mul_f32 v78,v30,v78
v_mul_f32 v79,v31,v79
v_exp_f32 v76,v76
v_exp_f32 v77,v77
v_exp_f32 v78,v78
v_exp_f32 v79,v79
v_mfma_f32_16x16x32_bf16 v[32:35],a[200:203],v[44:47],v[32:35]
buffer_load_dwordx4 v0,s[36:39],s66 offen offset:3072 sc1 lds
v_fma_f32 v76,v76,v99,v99
v_fma_f32 v77,v77,v99,v99
v_fma_f32 v78,v78,v99,v99
v_fma_f32 v79,v79,v99,v99
v_rcp_f32 v76,v76
v_rcp_f32 v77,v77
v_rcp_f32 v78,v78
v_rcp_f32 v79,v79
v_mfma_f32_16x16x32_bf16 v[36:39],a[204:207],v[44:47],v[36:39]
s_cmp_lt_u32 s67,s59
ds_read_b128 v[40:43],v2 offset:0
v_mul_f32 v27,v27,v75
v_cvt_pk_bf16_f32 v80,v24,v25
v_cvt_pk_bf16_f32 v81,v26,v27
v_mul_f32 v28,v28,v76
v_mul_f32 v29,v29,v77
v_mul_f32 v30,v30,v78
v_mul_f32 v31,v31,v79
v_cvt_pk_bf16_f32 v82,v28,v29
v_cvt_pk_bf16_f32 v83,v30,v31
v_mfma_f32_16x16x32_bf16 v[32:35],a[208:211],v[48:51],v[32:35]
buffer_store_dwordx4 v[80:83],v3,s[36:39],s73 offen offset:0 sc1
ds_read_b128 v[44:47],v2 offset:1024
ds_read_b64 v[92:93],v11 offset:128
v_xor_b32 v8,16384,v8
v_mfma_f32_16x16x32_bf16 v[36:39],a[212:215],v[48:51],v[36:39]
v_mfma_f32_16x16x32_bf16 v[32:35],a[216:219],v[52:55],v[32:35]
ds_read_b128 v[48:51],v2 offset:2048
v_mfma_f32_16x16x32_bf16 v[36:39],a[220:223],v[52:55],v[36:39]
s_waitcnt lgkmcnt(4)
v_mfma_f32_16x16x32_bf16 v[32:35],a[224:227],v[56:59],v[32:35]
s_waitcnt lgkmcnt(1)
ds_read_b128 v[52:55],v2 offset:3072
v_mul_f32 v96,s75,v92
v_mfma_f32_16x16x32_bf16 v[36:39],a[228:231],v[56:59],v[36:39]
v_fma_f32 v93,v93,s75,v12
v_fma_f32 v93,-v96,v96,v93
v_mfma_f32_16x16x32_bf16 v[32:35],a[232:235],v[60:63],v[32:35]
ds_read_b128 v[56:59],v2 offset:4096
v_max_f32 v93,v93,v12
v_mfma_f32_16x16x32_bf16 v[36:39],a[236:239],v[60:63],v[36:39]
v_rsq_f32 v92,v93
v_mfma_f32_16x16x32_bf16 v[32:35],a[240:243],v[64:67],v[32:35]
ds_read_b128 v[60:63],v2 offset:5120
v_mul_f32 v97,s76,v92
v_mfma_f32_16x16x32_bf16 v[36:39],a[244:247],v[64:67],v[36:39]
v_mul_f32 v98,v92,v92
v_mfma_f32_16x16x32_bf16 v[32:35],a[248:251],v[68:71],v[32:35]
s_waitcnt lgkmcnt(2)
ds_read_b128 v[64:67],v2 offset:6144
v_mfma_f32_16x16x32_bf16 v[36:39],a[252:255],v[68:71],v[36:39]
s_cbranch_scc1 Lg3o_d0w0_%=
s_branch Lg3o_slow0_%=)"
R"(
Lg3o_d0w1_%=:
v_mfma_f32_16x16x32_bf16 v[24:27],a[0:3],v[40:43],0
s_add_u32 m0,s62,16384
ds_read_b128 v[68:71],v2 offset:7168
v_mul_f32 v99,v93,v92
v_mfma_f32_16x16x32_bf16 v[28:31],a[4:7],v[40:43],0
s_mul_i32 s73,s47,7
v_mul_f32 v98,s77,v98
v_mul_f32 v98,v98,v97
v_mfma_f32_16x16x32_bf16 v[24:27],a[8:11],v[44:47],v[24:27]
buffer_load_dwordx4 v1,s[36:39],s66 offen offset:0 sc1 lds
v_mfma_f32_16x16x32_bf16 v[28:31],a[12:15],v[44:47],v[28:31]
s_lshr_b32 s73,s73,1
ds_read_b128 v[40:43],v2 offset:8192
v_fma_f32 v32,-v96,v16,v32
v_mfma_f32_16x16x32_bf16 v[24:27],a[16:19],v[48:51],v[24:27]
s_lshr_b32 s72,s66,5
ds_read_b128 v[44:47],v2 offset:9216
v_fma_f32 v33,-v96,v17,v33
v_mfma_f32_16x16x32_bf16 v[28:31],a[20:23],v[48:51],v[28:31]
s_lshr_b32 s71,s62,2
v_fma_f32 v34,-v96,v18,v34
v_fma_f32 v35,-v96,v19,v35
v_mfma_f32_16x16x32_bf16 v[24:27],a[24:27],v[52:55],v[24:27]
buffer_load_dwordx4 v1,s[36:39],s66 offen offset:1024 sc1 lds
v_mfma_f32_16x16x32_bf16 v[28:31],a[28:31],v[52:55],v[28:31]
s_waitcnt lgkmcnt(2)
ds_read_b128 v[48:51],v2 offset:10240
v_mul_f32 v72,v32,v32
v_mfma_f32_16x16x32_bf16 v[24:27],a[32:35],v[56:59],v[24:27]
ds_read_b128 v[52:55],v2 offset:11264
v_mul_f32 v73,v33,v33
v_mfma_f32_16x16x32_bf16 v[28:31],a[36:39],v[56:59],v[28:31]
v_mul_f32 v74,v34,v34
v_mul_f32 v75,v35,v35
v_mfma_f32_16x16x32_bf16 v[24:27],a[40:43],v[60:63],v[24:27]
buffer_load_dwordx4 v1,s[36:39],s66 offen offset:2048 sc1 lds
v_mfma_f32_16x16x32_bf16 v[28:31],a[44:47],v[60:63],v[28:31]
ds_read_b128 v[56:59],v2 offset:12288
v_fma_f32 v72,v98,v72,v97
v_mfma_f32_16x16x32_bf16 v[24:27],a[48:51],v[64:67],v[24:27]
ds_read_b128 v[60:63],v2 offset:13312
v_mul_f32 v72,v32,v72
v_mfma_f32_16x16x32_bf16 v[28:31],a[52:55],v[64:67],v[28:31]
v_exp_f32 v72,v72
v_mfma_f32_16x16x32_bf16 v[24:27],a[56:59],v[68:71],v[24:27]
buffer_load_dwordx4 v1,s[36:39],s66 offen offset:3072 sc1 lds
v_mfma_f32_16x16x32_bf16 v[28:31],a[60:63],v[68:71],v[28:31]
s_waitcnt lgkmcnt(2)
ds_read_b128 v[64:67],v2 offset:14336
s_add_u32 m0,s71,s88
v_mfma_f32_16x16x32_bf16 v[24:27],a[64:67],v[40:43],v[24:27]
s_xor_b32 s87,s64,65536
ds_read_b128 v[68:71],v2 offset:15360
buffer_load_dword v9,s[36:39],s72 offen offset:0 sc1 lds
s_waitcnt vmcnt(10)
v_mfma_f32_16x16x32_bf16 v[28:31],a[68:71],v[40:43],v[28:31]
s_barrier
v_mfma_f32_16x16x32_bf16 v[24:27],a[72:75],v[44:47],v[24:27]
s_cmp_ge_u32 s66,s59
ds_read_b128 v[40:43],v2 offset:16384
ds_read_b32 v88,v8 offset:0
v_mfma_f32_16x16x32_bf16 v[28:31],a[76:79],v[44:47],v[28:31]
s_cselect_b32 s86,s49,s50
v_fma_f32 v72,v72,v99,v99
v_fma_f32 v73,v98,v73,v97
v_mfma_f32_16x16x32_bf16 v[24:27],a[80:83],v[48:51],v[24:27]
s_cselect_b32 s87,s87,s63
ds_read_b128 v[44:47],v2 offset:17408
v_mul_f32 v73,v33,v73
v_mfma_f32_16x16x32_bf16 v[28:31],a[84:87],v[48:51],v[28:31]
s_mov_b32 s62,s87
v_exp_f32 v73,v73
v_mfma_f32_16x16x32_bf16 v[24:27],a[88:91],v[52:55],v[24:27]
s_mov_b32 m0,s62
ds_read_b128 v[48:51],v2 offset:18432
v_fma_f32 v73,v73,v99,v99
v_mfma_f32_16x16x32_bf16 v[28:31],a[92:95],v[52:55],v[28:31]
s_waitcnt lgkmcnt(4)
v_rcp_f32 v72,v72
v_mfma_f32_16x16x32_bf16 v[24:27],a[96:99],v[56:59],v[24:27]
s_nop 0
s_waitcnt lgkmcnt(2)
ds_read_b128 v[52:55],v2 offset:19456
v_fma_f32 v74,v98,v74,v97
v_mfma_f32_16x16x32_bf16 v[28:31],a[100:103],v[56:59],v[28:31]
s_barrier
v_mfma_f32_16x16x32_bf16 v[24:27],a[104:107],v[60:63],v[24:27]
ds_read_b128 v[56:59],v2 offset:20480
ds_read_b32 v13,v7 offset:0
v_mfma_f32_16x16x32_bf16 v[28:31],a[108:111],v[60:63],v[28:31]
v_rcp_f32 v73,v73
v_mfma_f32_16x16x32_bf16 v[24:27],a[112:115],v[64:67],v[24:27]
ds_read_b128 v[60:63],v2 offset:21504
v_mul_f32 v74,v34,v74
v_mfma_f32_16x16x32_bf16 v[28:31],a[116:119],v[64:67],v[28:31]
v_exp_f32 v74,v74
v_mfma_f32_16x16x32_bf16 v[24:27],a[120:123],v[68:71],v[24:27]
s_waitcnt lgkmcnt(1)
ds_read_b128 v[64:67],v2 offset:22528
v_fma_f32 v74,v74,v99,v99
v_mfma_f32_16x16x32_bf16 v[28:31],a[124:127],v[68:71],v[28:31]
v_cmp_eq_u32 vcc,s60,v13
v_rcp_f32 v74,v74
v_add_f32_dpp v88,v88,v88 quad_perm:[1,0,3,2] row_mask:0xf bank_mask:0xf
v_mfma_f32_16x16x32_bf16 v[24:27],a[128:131],v[40:43],v[24:27]
s_cmp_eq_u32 vcc_lo,-1
ds_read_b128 v[68:71],v2 offset:23552
s_cselect_b32 s67,s86,s59
v_mfma_f32_16x16x32_bf16 v[28:31],a[132:135],v[40:43],v[28:31]
buffer_load_dwordx4 v0,s[36:39],s67 offen offset:0 sc1 lds
v_mfma_f32_16x16x32_bf16 v[24:27],a[136:139],v[44:47],v[24:27]
s_xor_b32 s63,s63,65536
ds_read_b128 v[40:43],v2 offset:24576
v_add_f32_dpp v88,v88,v88 quad_perm:[2,3,0,1] row_mask:0xf bank_mask:0xf
v_mfma_f32_16x16x32_bf16 v[28:31],a[140:143],v[44:47],v[28:31]
s_add_u32 s53,s52,s55
v_add_f32_dpp v88,v88,v88 row_half_mirror row_mask:0xf bank_mask:0xf
v_fma_f32 v75,v98,v75,v97
v_mfma_f32_16x16x32_bf16 v[24:27],a[144:147],v[48:51],v[24:27]
s_cmp_eq_u32 s53,s56
ds_write_b32 v10,v88 offset:0
v_mfma_f32_16x16x32_bf16 v[28:31],a[148:151],v[48:51],v[28:31]
s_cselect_b32 s53,s57,s53
ds_read_b128 v[44:47],v2 offset:25600
buffer_load_dwordx4 v0,s[36:39],s67 offen offset:1024 sc1 lds
v_mfma_f32_16x16x32_bf16 v[24:27],a[152:155],v[52:55],v[24:27]
s_cmp_eq_u32 s53,s58
ds_read_b128 v[48:51],v2 offset:26624
v_mul_f32 v75,v35,v75
v_mfma_f32_16x16x32_bf16 v[28:31],a[156:159],v[52:55],v[28:31]
s_waitcnt lgkmcnt(4)
v_exp_f32 v75,v75
v_mfma_f32_16x16x32_bf16 v[24:27],a[160:163],v[56:59],v[24:27]
s_cselect_b32 s53,s59,s53
ds_read_b128 v[52:55],v2 offset:27648
v_fma_f32 v75,v75,v99,v99
v_mfma_f32_16x16x32_bf16 v[28:31],a[164:167],v[56:59],v[28:31]
buffer_load_dwordx4 v0,s[36:39],s67 offen offset:2048 sc1 lds
v_mfma_f32_16x16x32_bf16 v[24:27],a[168:171],v[60:63],v[24:27]
s_cmp_ge_u32 s66,s59
ds_read_b128 v[56:59],v2 offset:28672
v_mul_f32 v32,v32,v72
v_mfma_f32_16x16x32_bf16 v[28:31],a[172:175],v[60:63],v[28:31]
v_rcp_f32 v75,v75
v_mfma_f32_16x16x32_bf16 v[24:27],a[176:179],v[64:67],v[24:27]
ds_read_b128 v[60:63],v2 offset:29696
v_mul_f32 v33,v33,v73
v_mfma_f32_16x16x32_bf16 v[28:31],a[180:183],v[64:67],v[28:31]
buffer_load_dwordx4 v0,s[36:39],s67 offen offset:3072 sc1 lds
v_mfma_f32_16x16x32_bf16 v[24:27],a[184:187],v[68:71],v[24:27]
ds_read_b128 v[64:67],v2 offset:30720
v_mul_f32 v34,v34,v74
v_mfma_f32_16x16x32_bf16 v[28:31],a[188:191],v[68:71],v[28:31]
s_waitcnt lgkmcnt(3)
v_mul_f32 v35,v35,v75
v_cvt_pk_bf16_f32 v80,v32,v33
v_mfma_f32_16x16x32_bf16 v[24:27],a[192:195],v[40:43],v[24:27]
s_waitcnt vmcnt(9)
ds_read_b128 v[68:71],v2 offset:31744
v_cvt_pk_bf16_f32 v81,v34,v35
v_mfma_f32_16x16x32_bf16 v[28:31],a[196:199],v[40:43],v[28:31]
s_barrier
v_mfma_f32_16x16x32_bf16 v[24:27],a[200:203],v[44:47],v[24:27]
v_fma_f32 v36,-v96,v20,v36
v_fma_f32 v37,-v96,v21,v37
v_fma_f32 v38,-v96,v22,v38
v_fma_f32 v39,-v96,v23,v39
v_mul_f32 v76,v36,v36
v_mul_f32 v77,v37,v37
v_mul_f32 v78,v38,v38
v_mul_f32 v79,v39,v39
v_fma_f32 v76,v98,v76,v97
v_fma_f32 v77,v98,v77,v97
v_fma_f32 v78,v98,v78,v97
v_fma_f32 v79,v98,v79,v97
v_mul_f32 v76,v36,v76
v_mul_f32 v77,v37,v77
v_mul_f32 v78,v38,v78
v_mul_f32 v79,v39,v79
v_exp_f32 v76,v76
v_exp_f32 v77,v77
v_exp_f32 v78,v78
v_exp_f32 v79,v79
v_mfma_f32_16x16x32_bf16 v[28:31],a[204:207],v[44:47],v[28:31]
ds_read_b128 v[40:43],v2 offset:32768
v_fma_f32 v76,v76,v99,v99
v_fma_f32 v77,v77,v99,v99
v_fma_f32 v78,v78,v99,v99
v_fma_f32 v79,v79,v99,v99
v_rcp_f32 v76,v76
v_rcp_f32 v77,v77
v_rcp_f32 v78,v78
v_rcp_f32 v79,v79
v_mfma_f32_16x16x32_bf16 v[24:27],a[208:211],v[48:51],v[24:27]
v_mul_f32 v36,v36,v76
v_mul_f32 v37,v37,v77
v_mul_f32 v38,v38,v78
v_mul_f32 v39,v39,v79
v_cvt_pk_bf16_f32 v82,v36,v37)"
R"(
v_cvt_pk_bf16_f32 v83,v38,v39
v_mfma_f32_16x16x32_bf16 v[28:31],a[212:215],v[48:51],v[28:31]
ds_read_b128 v[44:47],v2 offset:33792
buffer_store_dwordx4 v[80:83],v3,s[36:39],s73 offen offset:0 sc1
ds_read_b64 v[92:93],v11 offset:0
v_mfma_f32_16x16x32_bf16 v[24:27],a[216:219],v[52:55],v[24:27]
ds_read_b128 v[48:51],v2 offset:34816
v_mfma_f32_16x16x32_bf16 v[28:31],a[220:223],v[52:55],v[28:31]
s_waitcnt lgkmcnt(4)
v_mfma_f32_16x16x32_bf16 v[24:27],a[224:227],v[56:59],v[24:27]
ds_read_b128 v[52:55],v2 offset:35840
v_mfma_f32_16x16x32_bf16 v[28:31],a[228:231],v[56:59],v[28:31]
s_waitcnt lgkmcnt(2)
v_mul_f32 v96,s75,v92
v_fma_f32 v93,v93,s75,v12
v_mfma_f32_16x16x32_bf16 v[24:27],a[232:235],v[60:63],v[24:27]
ds_read_b128 v[56:59],v2 offset:36864
v_fma_f32 v93,-v96,v96,v93
v_mfma_f32_16x16x32_bf16 v[28:31],a[236:239],v[60:63],v[28:31]
v_max_f32 v93,v93,v12
v_mfma_f32_16x16x32_bf16 v[24:27],a[240:243],v[64:67],v[24:27]
ds_read_b128 v[60:63],v2 offset:37888
v_mfma_f32_16x16x32_bf16 v[28:31],a[244:247],v[64:67],v[28:31]
v_rsq_f32 v92,v93
v_mfma_f32_16x16x32_bf16 v[24:27],a[248:251],v[68:71],v[24:27]
s_waitcnt lgkmcnt(2)
ds_read_b128 v[64:67],v2 offset:38912
v_mul_f32 v97,s76,v92
v_mul_f32 v98,v92,v92
v_mfma_f32_16x16x32_bf16 v[28:31],a[252:255],v[68:71],v[28:31]
s_cbranch_scc1 Lg3o_slow1_%=
Lg3o_d1w1_%=:
v_mfma_f32_16x16x32_bf16 v[32:35],a[0:3],v[40:43],0
s_add_u32 m0,s62,16384
ds_read_b128 v[68:71],v2 offset:39936
v_mul_f32 v99,v93,v92
v_mfma_f32_16x16x32_bf16 v[36:39],a[4:7],v[40:43],0
s_mul_i32 s73,s48,7
v_mul_f32 v98,s77,v98
v_mul_f32 v98,v98,v97
v_mfma_f32_16x16x32_bf16 v[32:35],a[8:11],v[44:47],v[32:35]
buffer_load_dwordx4 v1,s[36:39],s67 offen offset:0 sc1 lds
v_mfma_f32_16x16x32_bf16 v[36:39],a[12:15],v[44:47],v[36:39]
s_lshr_b32 s73,s73,1
ds_read_b128 v[40:43],v2 offset:40960
v_fma_f32 v24,-v96,v16,v24
v_mfma_f32_16x16x32_bf16 v[32:35],a[16:19],v[48:51],v[32:35]
s_lshr_b32 s72,s67,5
ds_read_b128 v[44:47],v2 offset:41984
v_fma_f32 v25,-v96,v17,v25
v_mfma_f32_16x16x32_bf16 v[36:39],a[20:23],v[48:51],v[36:39]
s_lshr_b32 s71,s62,2
v_fma_f32 v26,-v96,v18,v26
v_fma_f32 v27,-v96,v19,v27
v_mfma_f32_16x16x32_bf16 v[32:35],a[24:27],v[52:55],v[32:35]
buffer_load_dwordx4 v1,s[36:39],s67 offen offset:1024 sc1 lds
v_mfma_f32_16x16x32_bf16 v[36:39],a[28:31],v[52:55],v[36:39]
s_waitcnt lgkmcnt(2)
ds_read_b128 v[48:51],v2 offset:43008
v_mul_f32 v72,v24,v24
v_mfma_f32_16x16x32_bf16 v[32:35],a[32:35],v[56:59],v[32:35]
s_mov_b64 s[44:45],s[46:47]
ds_read_b128 v[52:55],v2 offset:44032
v_mul_f32 v73,v25,v25
v_mfma_f32_16x16x32_bf16 v[36:39],a[36:39],v[56:59],v[36:39]
s_mov_b64 s[46:47],s[48:49]
v_mul_f32 v74,v26,v26
v_mul_f32 v75,v27,v27
v_mfma_f32_16x16x32_bf16 v[32:35],a[40:43],v[60:63],v[32:35]
buffer_load_dwordx4 v1,s[36:39],s67 offen offset:2048 sc1 lds
v_mfma_f32_16x16x32_bf16 v[36:39],a[44:47],v[60:63],v[36:39]
s_mov_b64 s[48:49],s[50:51]
ds_read_b128 v[56:59],v2 offset:45056
v_fma_f32 v72,v98,v72,v97
v_mfma_f32_16x16x32_bf16 v[32:35],a[48:51],v[64:67],v[32:35]
ds_read_b128 v[60:63],v2 offset:46080
v_mul_f32 v72,v24,v72
v_mfma_f32_16x16x32_bf16 v[36:39],a[52:55],v[64:67],v[36:39]
v_exp_f32 v72,v72
v_mfma_f32_16x16x32_bf16 v[32:35],a[56:59],v[68:71],v[32:35]
buffer_load_dwordx4 v1,s[36:39],s67 offen offset:3072 sc1 lds
v_mfma_f32_16x16x32_bf16 v[36:39],a[60:63],v[68:71],v[36:39]
s_waitcnt lgkmcnt(2)
ds_read_b128 v[64:67],v2 offset:47104
s_add_u32 m0,s71,s88
v_mfma_f32_16x16x32_bf16 v[32:35],a[64:67],v[40:43],v[32:35]
s_xor_b32 s87,s63,65536
ds_read_b128 v[68:71],v2 offset:48128
buffer_load_dword v9,s[36:39],s72 offen offset:0 sc1 lds
s_waitcnt vmcnt(10)
v_mfma_f32_16x16x32_bf16 v[36:39],a[68:71],v[40:43],v[36:39]
s_barrier
v_mfma_f32_16x16x32_bf16 v[32:35],a[72:75],v[44:47],v[32:35]
s_cmp_ge_u32 s67,s59
ds_read_b128 v[40:43],v2 offset:49152
ds_read_b32 v88,v8 offset:8192
v_mfma_f32_16x16x32_bf16 v[36:39],a[76:79],v[44:47],v[36:39]
s_cselect_b32 s86,s50,s51
v_fma_f32 v72,v72,v99,v99
v_fma_f32 v73,v98,v73,v97
v_mfma_f32_16x16x32_bf16 v[32:35],a[80:83],v[48:51],v[32:35]
s_cselect_b32 s87,s87,s64
ds_read_b128 v[44:47],v2 offset:50176
v_mul_f32 v73,v25,v73
v_mfma_f32_16x16x32_bf16 v[36:39],a[84:87],v[48:51],v[36:39]
s_mov_b32 s62,s87
v_exp_f32 v73,v73
v_mfma_f32_16x16x32_bf16 v[32:35],a[88:91],v[52:55],v[32:35]
s_mov_b32 m0,s62
ds_read_b128 v[48:51],v2 offset:51200
v_fma_f32 v73,v73,v99,v99
v_mfma_f32_16x16x32_bf16 v[36:39],a[92:95],v[52:55],v[36:39]
s_waitcnt lgkmcnt(4)
v_rcp_f32 v72,v72
v_mfma_f32_16x16x32_bf16 v[32:35],a[96:99],v[56:59],v[32:35]
s_nop 0
s_waitcnt lgkmcnt(2)
ds_read_b128 v[52:55],v2 offset:52224
v_fma_f32 v74,v98,v74,v97
v_mfma_f32_16x16x32_bf16 v[36:39],a[100:103],v[56:59],v[36:39]
s_barrier
v_mfma_f32_16x16x32_bf16 v[32:35],a[104:107],v[60:63],v[32:35]
s_mov_b64 s[50:51],s[52:53]
ds_read_b128 v[56:59],v2 offset:53248
ds_read_b32 v13,v7 offset:256
v_mfma_f32_16x16x32_bf16 v[36:39],a[108:111],v[60:63],v[36:39]
v_rcp_f32 v73,v73
v_mfma_f32_16x16x32_bf16 v[32:35],a[112:115],v[64:67],v[32:35]
ds_read_b128 v[60:63],v2 offset:54272
v_mul_f32 v74,v26,v74
v_mfma_f32_16x16x32_bf16 v[36:39],a[116:119],v[64:67],v[36:39]
v_exp_f32 v74,v74
v_mfma_f32_16x16x32_bf16 v[32:35],a[120:123],v[68:71],v[32:35]
s_waitcnt lgkmcnt(1)
ds_read_b128 v[64:67],v2 offset:55296
v_fma_f32 v74,v74,v99,v99
v_mfma_f32_16x16x32_bf16 v[36:39],a[124:127],v[68:71],v[36:39]
v_cmp_eq_u32 vcc,s60,v13
v_rcp_f32 v74,v74
v_add_f32_dpp v88,v88,v88 quad_perm:[1,0,3,2] row_mask:0xf bank_mask:0xf
v_mfma_f32_16x16x32_bf16 v[32:35],a[128:131],v[40:43],v[32:35]
s_cmp_eq_u32 vcc_lo,-1
ds_read_b128 v[68:71],v2 offset:56320
s_cselect_b32 s66,s86,s59
v_mfma_f32_16x16x32_bf16 v[36:39],a[132:135],v[40:43],v[36:39]
buffer_load_dwordx4 v0,s[36:39],s66 offen offset:0 sc1 lds
v_mfma_f32_16x16x32_bf16 v[32:35],a[136:139],v[44:47],v[32:35]
s_xor_b32 s64,s64,65536
ds_read_b128 v[40:43],v2 offset:57344
v_add_f32_dpp v88,v88,v88 quad_perm:[2,3,0,1] row_mask:0xf bank_mask:0xf
v_mfma_f32_16x16x32_bf16 v[36:39],a[140:143],v[44:47],v[36:39]
s_add_u32 s74,s53,s55
v_add_f32_dpp v88,v88,v88 row_half_mirror row_mask:0xf bank_mask:0xf
v_fma_f32 v75,v98,v75,v97
v_mfma_f32_16x16x32_bf16 v[32:35],a[144:147],v[48:51],v[32:35]
s_cmp_eq_u32 s74,s56
ds_write_b32 v10,v88 offset:128
v_mfma_f32_16x16x32_bf16 v[36:39],a[148:151],v[48:51],v[36:39]
s_cselect_b32 s74,s57,s74
ds_read_b128 v[44:47],v2 offset:58368
buffer_load_dwordx4 v0,s[36:39],s66 offen offset:1024 sc1 lds
v_mfma_f32_16x16x32_bf16 v[32:35],a[152:155],v[52:55],v[32:35]
s_cmp_eq_u32 s74,s58
ds_read_b128 v[48:51],v2 offset:59392
v_mul_f32 v75,v27,v75
v_mfma_f32_16x16x32_bf16 v[36:39],a[156:159],v[52:55],v[36:39]
s_waitcnt lgkmcnt(4)
v_exp_f32 v75,v75
v_mfma_f32_16x16x32_bf16 v[32:35],a[160:163],v[56:59],v[32:35]
s_cselect_b32 s74,s59,s74
ds_read_b128 v[52:55],v2 offset:60416
v_fma_f32 v75,v75,v99,v99
v_mfma_f32_16x16x32_bf16 v[36:39],a[164:167],v[56:59],v[36:39]
buffer_load_dwordx4 v0,s[36:39],s66 offen offset:2048 sc1 lds
v_mfma_f32_16x16x32_bf16 v[32:35],a[168:171],v[60:63],v[32:35]
s_mov_b32 s52,s74
ds_read_b128 v[56:59],v2 offset:61440
v_mul_f32 v24,v24,v72
v_mfma_f32_16x16x32_bf16 v[36:39],a[172:175],v[60:63],v[36:39]
s_cmp_lt_u32 s67,s59
v_rcp_f32 v75,v75
v_mfma_f32_16x16x32_bf16 v[32:35],a[176:179],v[64:67],v[32:35]
ds_read_b128 v[60:63],v2 offset:62464
v_mul_f32 v25,v25,v73
v_mfma_f32_16x16x32_bf16 v[36:39],a[180:183],v[64:67],v[36:39]
buffer_load_dwordx4 v0,s[36:39],s66 offen offset:3072 sc1 lds
v_mfma_f32_16x16x32_bf16 v[32:35],a[184:187],v[68:71],v[32:35]
ds_read_b128 v[64:67],v2 offset:63488
v_mul_f32 v26,v26,v74
v_mfma_f32_16x16x32_bf16 v[36:39],a[188:191],v[68:71],v[36:39]
s_waitcnt lgkmcnt(3)
v_mul_f32 v27,v27,v75
v_cvt_pk_bf16_f32 v80,v24,v25
v_mfma_f32_16x16x32_bf16 v[32:35],a[192:195],v[40:43],v[32:35]
s_waitcnt vmcnt(9)
ds_read_b128 v[68:71],v2 offset:64512
v_xor_b32 v2,65536,v2
v_mfma_f32_16x16x32_bf16 v[36:39],a[196:199],v[40:43],v[36:39]
s_barrier
v_mfma_f32_16x16x32_bf16 v[32:35],a[200:203],v[44:47],v[32:35])"
R"(
v_fma_f32 v28,-v96,v20,v28
v_fma_f32 v29,-v96,v21,v29
v_fma_f32 v30,-v96,v22,v30
v_fma_f32 v31,-v96,v23,v31
v_mul_f32 v76,v28,v28
v_mul_f32 v77,v29,v29
v_mul_f32 v78,v30,v30
v_mul_f32 v79,v31,v31
v_fma_f32 v76,v98,v76,v97
v_fma_f32 v77,v98,v77,v97
v_fma_f32 v78,v98,v78,v97
v_fma_f32 v79,v98,v79,v97
v_mul_f32 v76,v28,v76
v_mul_f32 v77,v29,v77
v_mul_f32 v78,v30,v78
v_mul_f32 v79,v31,v79
v_exp_f32 v76,v76
v_exp_f32 v77,v77
v_exp_f32 v78,v78
v_exp_f32 v79,v79
v_mfma_f32_16x16x32_bf16 v[36:39],a[204:207],v[44:47],v[36:39]
ds_read_b128 v[40:43],v2 offset:0
v_fma_f32 v76,v76,v99,v99
v_fma_f32 v77,v77,v99,v99
v_fma_f32 v78,v78,v99,v99
v_fma_f32 v79,v79,v99,v99
v_rcp_f32 v76,v76
v_rcp_f32 v77,v77
v_rcp_f32 v78,v78
v_rcp_f32 v79,v79
v_mfma_f32_16x16x32_bf16 v[32:35],a[208:211],v[48:51],v[32:35]
v_cvt_pk_bf16_f32 v81,v26,v27
v_mul_f32 v28,v28,v76
v_mul_f32 v29,v29,v77
v_mul_f32 v30,v30,v78
v_mul_f32 v31,v31,v79
v_cvt_pk_bf16_f32 v82,v28,v29
v_cvt_pk_bf16_f32 v83,v30,v31
v_mfma_f32_16x16x32_bf16 v[36:39],a[212:215],v[48:51],v[36:39]
ds_read_b128 v[44:47],v2 offset:1024
buffer_store_dwordx4 v[80:83],v3,s[36:39],s73 offen offset:0 sc1
ds_read_b64 v[92:93],v11 offset:128
v_xor_b32 v8,16384,v8
v_mfma_f32_16x16x32_bf16 v[32:35],a[216:219],v[52:55],v[32:35]
ds_read_b128 v[48:51],v2 offset:2048
v_mfma_f32_16x16x32_bf16 v[36:39],a[220:223],v[52:55],v[36:39]
s_waitcnt lgkmcnt(4)
v_mfma_f32_16x16x32_bf16 v[32:35],a[224:227],v[56:59],v[32:35]
ds_read_b128 v[52:55],v2 offset:3072
v_mfma_f32_16x16x32_bf16 v[36:39],a[228:231],v[56:59],v[36:39]
s_waitcnt lgkmcnt(2)
v_mul_f32 v96,s75,v92
v_fma_f32 v93,v93,s75,v12
v_mfma_f32_16x16x32_bf16 v[32:35],a[232:235],v[60:63],v[32:35]
ds_read_b128 v[56:59],v2 offset:4096
v_fma_f32 v93,-v96,v96,v93
v_mfma_f32_16x16x32_bf16 v[36:39],a[236:239],v[60:63],v[36:39]
v_max_f32 v93,v93,v12
v_mfma_f32_16x16x32_bf16 v[32:35],a[240:243],v[64:67],v[32:35]
ds_read_b128 v[60:63],v2 offset:5120
v_mfma_f32_16x16x32_bf16 v[36:39],a[244:247],v[64:67],v[36:39]
v_rsq_f32 v92,v93
v_mfma_f32_16x16x32_bf16 v[32:35],a[248:251],v[68:71],v[32:35]
s_waitcnt lgkmcnt(2)
ds_read_b128 v[64:67],v2 offset:6144
v_mul_f32 v97,s76,v92
v_mul_f32 v98,v92,v92
v_mfma_f32_16x16x32_bf16 v[36:39],a[252:255],v[68:71],v[36:39]
s_cbranch_scc1 Lg3o_d0w1_%=
s_branch Lg3o_slow0_%=
Lg3o_d0w2_%=:
v_mfma_f32_16x16x32_bf16 v[24:27],a[0:3],v[40:43],0
s_add_u32 m0,s62,16384
ds_read_b128 v[68:71],v2 offset:7168
v_mul_f32 v99,v93,v92
v_mfma_f32_16x16x32_bf16 v[28:31],a[4:7],v[40:43],0
s_mul_i32 s73,s47,7
v_mul_f32 v98,s77,v98
v_mul_f32 v98,v98,v97
v_mfma_f32_16x16x32_bf16 v[24:27],a[8:11],v[44:47],v[24:27]
s_lshr_b32 s73,s73,1
ds_read_b128 v[40:43],v2 offset:8192
s_lshr_b32 s72,s66,5
v_mfma_f32_16x16x32_bf16 v[28:31],a[12:15],v[44:47],v[28:31]
buffer_load_dwordx4 v1,s[36:39],s66 offen offset:0 sc1 lds
v_mfma_f32_16x16x32_bf16 v[24:27],a[16:19],v[48:51],v[24:27]
s_lshr_b32 s71,s62,2
ds_read_b128 v[44:47],v2 offset:9216
v_fma_f32 v32,-v96,v16,v32
v_mfma_f32_16x16x32_bf16 v[28:31],a[20:23],v[48:51],v[28:31]
v_fma_f32 v33,-v96,v17,v33
v_fma_f32 v34,-v96,v18,v34
v_mfma_f32_16x16x32_bf16 v[24:27],a[24:27],v[52:55],v[24:27]
ds_read_b128 v[48:51],v2 offset:10240
v_fma_f32 v35,-v96,v19,v35
v_mfma_f32_16x16x32_bf16 v[28:31],a[28:31],v[52:55],v[28:31]
s_waitcnt lgkmcnt(3)
buffer_load_dwordx4 v1,s[36:39],s66 offen offset:1024 sc1 lds
v_mfma_f32_16x16x32_bf16 v[24:27],a[32:35],v[56:59],v[24:27]
ds_read_b128 v[52:55],v2 offset:11264
v_mul_f32 v72,v32,v32
v_mfma_f32_16x16x32_bf16 v[28:31],a[36:39],v[56:59],v[28:31]
v_mul_f32 v73,v33,v33
v_mul_f32 v74,v34,v34
v_mfma_f32_16x16x32_bf16 v[24:27],a[40:43],v[60:63],v[24:27]
ds_read_b128 v[56:59],v2 offset:12288
v_mul_f32 v75,v35,v35
v_mfma_f32_16x16x32_bf16 v[28:31],a[44:47],v[60:63],v[28:31]
buffer_load_dwordx4 v1,s[36:39],s66 offen offset:2048 sc1 lds
v_mfma_f32_16x16x32_bf16 v[24:27],a[48:51],v[64:67],v[24:27]
ds_read_b128 v[60:63],v2 offset:13312
v_fma_f32 v72,v98,v72,v97
v_mfma_f32_16x16x32_bf16 v[28:31],a[52:55],v[64:67],v[28:31]
v_mul_f32 v72,v32,v72
v_fma_f32 v73,v98,v73,v97
v_mfma_f32_16x16x32_bf16 v[24:27],a[56:59],v[68:71],v[24:27]
ds_read_b128 v[64:67],v2 offset:14336
v_mul_f32 v73,v33,v73
v_mfma_f32_16x16x32_bf16 v[28:31],a[60:63],v[68:71],v[28:31]
s_waitcnt lgkmcnt(3)
buffer_load_dwordx4 v1,s[36:39],s66 offen offset:3072 sc1 lds
s_add_u32 m0,s71,s88
v_mfma_f32_16x16x32_bf16 v[24:27],a[64:67],v[40:43],v[24:27]
s_xor_b32 s87,s64,65536
ds_read_b128 v[68:71],v2 offset:15360
buffer_load_dword v9,s[36:39],s72 offen offset:0 sc1 lds
s_waitcnt vmcnt(10)
v_mfma_f32_16x16x32_bf16 v[28:31],a[68:71],v[40:43],v[28:31]
s_barrier
v_mfma_f32_16x16x32_bf16 v[24:27],a[72:75],v[44:47],v[24:27]
s_cmp_ge_u32 s66,s59
ds_read_b128 v[40:43],v2 offset:16384
ds_read_b32 v88,v8 offset:0
v_mfma_f32_16x16x32_bf16 v[28:31],a[76:79],v[44:47],v[28:31]
s_cselect_b32 s86,s49,s50
v_exp_f32 v72,v72
v_mfma_f32_16x16x32_bf16 v[24:27],a[80:83],v[48:51],v[24:27]
s_cselect_b32 s87,s87,s63
ds_read_b128 v[44:47],v2 offset:17408
v_fma_f32 v72,v72,v99,v99
v_mfma_f32_16x16x32_bf16 v[28:31],a[84:87],v[48:51],v[28:31]
s_mov_b32 s62,s87
v_exp_f32 v73,v73
v_mfma_f32_16x16x32_bf16 v[24:27],a[88:91],v[52:55],v[24:27]
s_mov_b32 m0,s62
ds_read_b128 v[48:51],v2 offset:18432
v_fma_f32 v73,v73,v99,v99
v_mfma_f32_16x16x32_bf16 v[28:31],a[92:95],v[52:55],v[28:31]
s_waitcnt lgkmcnt(4)
v_rcp_f32 v72,v72
v_mfma_f32_16x16x32_bf16 v[24:27],a[96:99],v[56:59],v[24:27]
s_nop 0
s_waitcnt lgkmcnt(2)
ds_read_b128 v[52:55],v2 offset:19456
v_fma_f32 v74,v98,v74,v97
v_mfma_f32_16x16x32_bf16 v[28:31],a[100:103],v[56:59],v[28:31]
s_barrier
v_mfma_f32_16x16x32_bf16 v[24:27],a[104:107],v[60:63],v[24:27]
ds_read_b128 v[56:59],v2 offset:20480
ds_read_b32 v13,v7 offset:0
v_mfma_f32_16x16x32_bf16 v[28:31],a[108:111],v[60:63],v[28:31]
v_rcp_f32 v73,v73
v_mfma_f32_16x16x32_bf16 v[24:27],a[112:115],v[64:67],v[24:27]
ds_read_b128 v[60:63],v2 offset:21504
v_mul_f32 v74,v34,v74
v_mfma_f32_16x16x32_bf16 v[28:31],a[116:119],v[64:67],v[28:31]
v_exp_f32 v74,v74
v_mfma_f32_16x16x32_bf16 v[24:27],a[120:123],v[68:71],v[24:27]
s_waitcnt lgkmcnt(1)
ds_read_b128 v[64:67],v2 offset:22528
v_fma_f32 v74,v74,v99,v99
v_mfma_f32_16x16x32_bf16 v[28:31],a[124:127],v[68:71],v[28:31]
v_cmp_eq_u32 vcc,s60,v13
v_rcp_f32 v74,v74
v_add_f32_dpp v88,v88,v88 quad_perm:[1,0,3,2] row_mask:0xf bank_mask:0xf
v_mfma_f32_16x16x32_bf16 v[24:27],a[128:131],v[40:43],v[24:27]
s_cmp_eq_u32 vcc_lo,-1
ds_read_b128 v[68:71],v2 offset:23552
v_add_f32_dpp v88,v88,v88 quad_perm:[2,3,0,1] row_mask:0xf bank_mask:0xf
v_mfma_f32_16x16x32_bf16 v[28:31],a[132:135],v[40:43],v[28:31]
s_cselect_b32 s67,s86,s59
v_add_f32_dpp v88,v88,v88 row_half_mirror row_mask:0xf bank_mask:0xf
v_fma_f32 v75,v98,v75,v97
v_mfma_f32_16x16x32_bf16 v[24:27],a[136:139],v[44:47],v[24:27]
buffer_load_dwordx4 v0,s[36:39],s67 offen offset:0 sc1 lds
v_mfma_f32_16x16x32_bf16 v[28:31],a[140:143],v[44:47],v[28:31]
s_xor_b32 s63,s63,65536
ds_read_b128 v[40:43],v2 offset:24576
v_mul_f32 v75,v35,v75
v_mfma_f32_16x16x32_bf16 v[24:27],a[144:147],v[48:51],v[24:27]
s_add_u32 s53,s52,s55
ds_write_b32 v10,v88 offset:0
v_mfma_f32_16x16x32_bf16 v[28:31],a[148:151],v[48:51],v[28:31]
s_cmp_eq_u32 s53,s56
ds_read_b128 v[44:47],v2 offset:25600
v_mul_f32 v32,v32,v72
v_mfma_f32_16x16x32_bf16 v[24:27],a[152:155],v[52:55],v[24:27]
buffer_load_dwordx4 v0,s[36:39],s67 offen offset:1024 sc1 lds
v_mfma_f32_16x16x32_bf16 v[28:31],a[156:159],v[52:55],v[28:31]
s_waitcnt lgkmcnt(3))"
R"(
ds_read_b128 v[48:51],v2 offset:26624
v_mul_f32 v33,v33,v73
v_mfma_f32_16x16x32_bf16 v[24:27],a[160:163],v[56:59],v[24:27]
s_cselect_b32 s53,s57,s53
ds_read_b128 v[52:55],v2 offset:27648
v_mul_f32 v34,v34,v74
v_mfma_f32_16x16x32_bf16 v[28:31],a[164:167],v[56:59],v[28:31]
s_cmp_eq_u32 s53,s58
v_exp_f32 v75,v75
v_mfma_f32_16x16x32_bf16 v[24:27],a[168:171],v[60:63],v[24:27]
buffer_load_dwordx4 v0,s[36:39],s67 offen offset:2048 sc1 lds
v_mfma_f32_16x16x32_bf16 v[28:31],a[172:175],v[60:63],v[28:31]
s_cselect_b32 s53,s59,s53
ds_read_b128 v[56:59],v2 offset:28672
v_fma_f32 v75,v75,v99,v99
v_mfma_f32_16x16x32_bf16 v[24:27],a[176:179],v[64:67],v[24:27]
s_cmp_ge_u32 s66,s59
ds_read_b128 v[60:63],v2 offset:29696
v_cvt_pk_bf16_f32 v80,v32,v33
v_mfma_f32_16x16x32_bf16 v[28:31],a[180:183],v[64:67],v[28:31]
v_rcp_f32 v75,v75
v_mfma_f32_16x16x32_bf16 v[24:27],a[184:187],v[68:71],v[24:27]
buffer_load_dwordx4 v0,s[36:39],s67 offen offset:3072 sc1 lds
v_mfma_f32_16x16x32_bf16 v[28:31],a[188:191],v[68:71],v[28:31]
s_waitcnt lgkmcnt(2)
ds_read_b128 v[64:67],v2 offset:30720
v_mul_f32 v35,v35,v75
v_mfma_f32_16x16x32_bf16 v[24:27],a[192:195],v[40:43],v[24:27]
s_waitcnt vmcnt(9)
ds_read_b128 v[68:71],v2 offset:31744
v_cvt_pk_bf16_f32 v81,v34,v35
v_mfma_f32_16x16x32_bf16 v[28:31],a[196:199],v[40:43],v[28:31]
s_barrier
v_mfma_f32_16x16x32_bf16 v[24:27],a[200:203],v[44:47],v[24:27]
ds_read_b128 v[40:43],v2 offset:32768
ds_read_b64 v[92:93],v11 offset:0
v_mfma_f32_16x16x32_bf16 v[28:31],a[204:207],v[44:47],v[28:31]
v_fma_f32 v36,-v96,v20,v36
v_fma_f32 v37,-v96,v21,v37
v_fma_f32 v38,-v96,v22,v38
v_fma_f32 v39,-v96,v23,v39
v_mul_f32 v76,v36,v36
v_mul_f32 v77,v37,v37
v_mul_f32 v78,v38,v38
v_mul_f32 v79,v39,v39
v_fma_f32 v76,v98,v76,v97
v_fma_f32 v77,v98,v77,v97
v_fma_f32 v78,v98,v78,v97
v_fma_f32 v79,v98,v79,v97
v_mul_f32 v76,v36,v76
v_mul_f32 v77,v37,v77
v_mul_f32 v78,v38,v78
v_mul_f32 v79,v39,v79
v_exp_f32 v76,v76
v_exp_f32 v77,v77
v_exp_f32 v78,v78
v_exp_f32 v79,v79
v_mfma_f32_16x16x32_bf16 v[24:27],a[208:211],v[48:51],v[24:27]
v_fma_f32 v76,v76,v99,v99
v_fma_f32 v77,v77,v99,v99
v_fma_f32 v78,v78,v99,v99
v_fma_f32 v79,v79,v99,v99
v_rcp_f32 v76,v76
v_rcp_f32 v77,v77
v_rcp_f32 v78,v78
v_rcp_f32 v79,v79
v_mfma_f32_16x16x32_bf16 v[28:31],a[212:215],v[48:51],v[28:31]
ds_read_b128 v[44:47],v2 offset:33792
v_mul_f32 v36,v36,v76
v_mul_f32 v37,v37,v77
v_mul_f32 v38,v38,v78
v_mul_f32 v39,v39,v79
v_cvt_pk_bf16_f32 v82,v36,v37
v_cvt_pk_bf16_f32 v83,v38,v39
v_mfma_f32_16x16x32_bf16 v[24:27],a[216:219],v[52:55],v[24:27]
s_waitcnt lgkmcnt(1)
buffer_store_dwordx4 v[80:83],v3,s[36:39],s73 offen offset:0 sc1
ds_read_b128 v[48:51],v2 offset:34816
v_mul_f32 v96,s75,v92
v_fma_f32 v93,v93,s75,v12
v_mfma_f32_16x16x32_bf16 v[28:31],a[220:223],v[52:55],v[28:31]
v_fma_f32 v93,-v96,v96,v93
v_max_f32 v93,v93,v12
v_mfma_f32_16x16x32_bf16 v[24:27],a[224:227],v[56:59],v[24:27]
ds_read_b128 v[52:55],v2 offset:35840
v_mfma_f32_16x16x32_bf16 v[28:31],a[228:231],v[56:59],v[28:31]
v_rsq_f32 v92,v93
v_mfma_f32_16x16x32_bf16 v[24:27],a[232:235],v[60:63],v[24:27]
ds_read_b128 v[56:59],v2 offset:36864
v_mul_f32 v97,s76,v92
v_mfma_f32_16x16x32_bf16 v[28:31],a[236:239],v[60:63],v[28:31]
v_mul_f32 v98,v92,v92
v_mfma_f32_16x16x32_bf16 v[24:27],a[240:243],v[64:67],v[24:27]
ds_read_b128 v[60:63],v2 offset:37888
v_mfma_f32_16x16x32_bf16 v[28:31],a[244:247],v[64:67],v[28:31]
v_mfma_f32_16x16x32_bf16 v[24:27],a[248:251],v[68:71],v[24:27]
s_waitcnt lgkmcnt(2)
ds_read_b128 v[64:67],v2 offset:38912
v_mfma_f32_16x16x32_bf16 v[28:31],a[252:255],v[68:71],v[28:31]
s_cbranch_scc1 Lg3o_slow1_%=
Lg3o_d1w2_%=:
v_mfma_f32_16x16x32_bf16 v[32:35],a[0:3],v[40:43],0
s_add_u32 m0,s62,16384
ds_read_b128 v[68:71],v2 offset:39936
v_mul_f32 v99,v93,v92
v_mfma_f32_16x16x32_bf16 v[36:39],a[4:7],v[40:43],0
s_mul_i32 s73,s48,7
v_mul_f32 v98,s77,v98
v_mul_f32 v98,v98,v97
v_mfma_f32_16x16x32_bf16 v[32:35],a[8:11],v[44:47],v[32:35]
s_lshr_b32 s73,s73,1
ds_read_b128 v[40:43],v2 offset:40960
s_lshr_b32 s72,s67,5
v_mfma_f32_16x16x32_bf16 v[36:39],a[12:15],v[44:47],v[36:39]
buffer_load_dwordx4 v1,s[36:39],s67 offen offset:0 sc1 lds
v_mfma_f32_16x16x32_bf16 v[32:35],a[16:19],v[48:51],v[32:35]
s_lshr_b32 s71,s62,2
ds_read_b128 v[44:47],v2 offset:41984
v_fma_f32 v24,-v96,v16,v24
v_mfma_f32_16x16x32_bf16 v[36:39],a[20:23],v[48:51],v[36:39]
s_mov_b64 s[44:45],s[46:47]
v_fma_f32 v25,-v96,v17,v25
v_fma_f32 v26,-v96,v18,v26
v_mfma_f32_16x16x32_bf16 v[32:35],a[24:27],v[52:55],v[32:35]
s_mov_b64 s[46:47],s[48:49]
ds_read_b128 v[48:51],v2 offset:43008
v_fma_f32 v27,-v96,v19,v27
v_mfma_f32_16x16x32_bf16 v[36:39],a[28:31],v[52:55],v[36:39]
s_waitcnt lgkmcnt(3)
buffer_load_dwordx4 v1,s[36:39],s67 offen offset:1024 sc1 lds
v_mfma_f32_16x16x32_bf16 v[32:35],a[32:35],v[56:59],v[32:35]
s_mov_b64 s[48:49],s[50:51]
ds_read_b128 v[52:55],v2 offset:44032
v_mul_f32 v72,v24,v24
v_mfma_f32_16x16x32_bf16 v[36:39],a[36:39],v[56:59],v[36:39]
v_mul_f32 v73,v25,v25
v_mul_f32 v74,v26,v26
v_mfma_f32_16x16x32_bf16 v[32:35],a[40:43],v[60:63],v[32:35]
ds_read_b128 v[56:59],v2 offset:45056
v_mul_f32 v75,v27,v27
v_mfma_f32_16x16x32_bf16 v[36:39],a[44:47],v[60:63],v[36:39]
buffer_load_dwordx4 v1,s[36:39],s67 offen offset:2048 sc1 lds
v_mfma_f32_16x16x32_bf16 v[32:35],a[48:51],v[64:67],v[32:35]
ds_read_b128 v[60:63],v2 offset:46080
v_fma_f32 v72,v98,v72,v97
v_mfma_f32_16x16x32_bf16 v[36:39],a[52:55],v[64:67],v[36:39]
v_mul_f32 v72,v24,v72
v_fma_f32 v73,v98,v73,v97
v_mfma_f32_16x16x32_bf16 v[32:35],a[56:59],v[68:71],v[32:35]
ds_read_b128 v[64:67],v2 offset:47104
v_mul_f32 v73,v25,v73
v_mfma_f32_16x16x32_bf16 v[36:39],a[60:63],v[68:71],v[36:39]
s_waitcnt lgkmcnt(3)
buffer_load_dwordx4 v1,s[36:39],s67 offen offset:3072 sc1 lds
s_add_u32 m0,s71,s88
v_mfma_f32_16x16x32_bf16 v[32:35],a[64:67],v[40:43],v[32:35]
s_xor_b32 s87,s63,65536
ds_read_b128 v[68:71],v2 offset:48128
buffer_load_dword v9,s[36:39],s72 offen offset:0 sc1 lds
s_waitcnt vmcnt(10)
v_mfma_f32_16x16x32_bf16 v[36:39],a[68:71],v[40:43],v[36:39]
s_barrier
v_mfma_f32_16x16x32_bf16 v[32:35],a[72:75],v[44:47],v[32:35]
s_cmp_ge_u32 s67,s59
ds_read_b128 v[40:43],v2 offset:49152
ds_read_b32 v88,v8 offset:8192
v_mfma_f32_16x16x32_bf16 v[36:39],a[76:79],v[44:47],v[36:39]
s_cselect_b32 s86,s50,s51
v_exp_f32 v72,v72
v_mfma_f32_16x16x32_bf16 v[32:35],a[80:83],v[48:51],v[32:35]
s_cselect_b32 s87,s87,s64
ds_read_b128 v[44:47],v2 offset:50176
v_fma_f32 v72,v72,v99,v99
v_mfma_f32_16x16x32_bf16 v[36:39],a[84:87],v[48:51],v[36:39]
s_mov_b32 s62,s87
v_exp_f32 v73,v73
v_mfma_f32_16x16x32_bf16 v[32:35],a[88:91],v[52:55],v[32:35]
s_mov_b32 m0,s62
ds_read_b128 v[48:51],v2 offset:51200
v_fma_f32 v73,v73,v99,v99
v_mfma_f32_16x16x32_bf16 v[36:39],a[92:95],v[52:55],v[36:39]
s_waitcnt lgkmcnt(4)
v_rcp_f32 v72,v72
v_mfma_f32_16x16x32_bf16 v[32:35],a[96:99],v[56:59],v[32:35]
s_nop 0
s_waitcnt lgkmcnt(2)
ds_read_b128 v[52:55],v2 offset:52224
v_fma_f32 v74,v98,v74,v97
v_mfma_f32_16x16x32_bf16 v[36:39],a[100:103],v[56:59],v[36:39]
s_barrier
v_mfma_f32_16x16x32_bf16 v[32:35],a[104:107],v[60:63],v[32:35]
s_mov_b64 s[50:51],s[52:53]
ds_read_b128 v[56:59],v2 offset:53248
ds_read_b32 v13,v7 offset:256
v_mfma_f32_16x16x32_bf16 v[36:39],a[108:111],v[60:63],v[36:39]
v_rcp_f32 v73,v73
v_mfma_f32_16x16x32_bf16 v[32:35],a[112:115],v[64:67],v[32:35]
ds_read_b128 v[60:63],v2 offset:54272
v_mul_f32 v74,v26,v74
v_mfma_f32_16x16x32_bf16 v[36:39],a[116:119],v[64:67],v[36:39])"
R"(
v_exp_f32 v74,v74
v_mfma_f32_16x16x32_bf16 v[32:35],a[120:123],v[68:71],v[32:35]
s_waitcnt lgkmcnt(1)
ds_read_b128 v[64:67],v2 offset:55296
v_fma_f32 v74,v74,v99,v99
v_mfma_f32_16x16x32_bf16 v[36:39],a[124:127],v[68:71],v[36:39]
v_cmp_eq_u32 vcc,s60,v13
v_rcp_f32 v74,v74
v_add_f32_dpp v88,v88,v88 quad_perm:[1,0,3,2] row_mask:0xf bank_mask:0xf
v_mfma_f32_16x16x32_bf16 v[32:35],a[128:131],v[40:43],v[32:35]
s_cmp_eq_u32 vcc_lo,-1
ds_read_b128 v[68:71],v2 offset:56320
v_add_f32_dpp v88,v88,v88 quad_perm:[2,3,0,1] row_mask:0xf bank_mask:0xf
v_mfma_f32_16x16x32_bf16 v[36:39],a[132:135],v[40:43],v[36:39]
s_cselect_b32 s66,s86,s59
v_add_f32_dpp v88,v88,v88 row_half_mirror row_mask:0xf bank_mask:0xf
v_fma_f32 v75,v98,v75,v97
v_mfma_f32_16x16x32_bf16 v[32:35],a[136:139],v[44:47],v[32:35]
buffer_load_dwordx4 v0,s[36:39],s66 offen offset:0 sc1 lds
v_mfma_f32_16x16x32_bf16 v[36:39],a[140:143],v[44:47],v[36:39]
s_xor_b32 s64,s64,65536
ds_read_b128 v[40:43],v2 offset:57344
v_mul_f32 v75,v27,v75
v_mfma_f32_16x16x32_bf16 v[32:35],a[144:147],v[48:51],v[32:35]
s_add_u32 s74,s53,s55
ds_write_b32 v10,v88 offset:128
v_mfma_f32_16x16x32_bf16 v[36:39],a[148:151],v[48:51],v[36:39]
s_cmp_eq_u32 s74,s56
ds_read_b128 v[44:47],v2 offset:58368
v_mul_f32 v24,v24,v72
v_mfma_f32_16x16x32_bf16 v[32:35],a[152:155],v[52:55],v[32:35]
buffer_load_dwordx4 v0,s[36:39],s66 offen offset:1024 sc1 lds
v_mfma_f32_16x16x32_bf16 v[36:39],a[156:159],v[52:55],v[36:39]
s_waitcnt lgkmcnt(3)
ds_read_b128 v[48:51],v2 offset:59392
v_mul_f32 v25,v25,v73
v_mfma_f32_16x16x32_bf16 v[32:35],a[160:163],v[56:59],v[32:35]
s_cselect_b32 s74,s57,s74
ds_read_b128 v[52:55],v2 offset:60416
v_mul_f32 v26,v26,v74
v_mfma_f32_16x16x32_bf16 v[36:39],a[164:167],v[56:59],v[36:39]
s_cmp_eq_u32 s74,s58
v_exp_f32 v75,v75
v_mfma_f32_16x16x32_bf16 v[32:35],a[168:171],v[60:63],v[32:35]
buffer_load_dwordx4 v0,s[36:39],s66 offen offset:2048 sc1 lds
v_mfma_f32_16x16x32_bf16 v[36:39],a[172:175],v[60:63],v[36:39]
s_cselect_b32 s74,s59,s74
ds_read_b128 v[56:59],v2 offset:61440
v_fma_f32 v75,v75,v99,v99
v_mfma_f32_16x16x32_bf16 v[32:35],a[176:179],v[64:67],v[32:35]
s_mov_b32 s52,s74
ds_read_b128 v[60:63],v2 offset:62464
v_cvt_pk_bf16_f32 v80,v24,v25
v_mfma_f32_16x16x32_bf16 v[36:39],a[180:183],v[64:67],v[36:39]
s_cmp_lt_u32 s67,s59
v_rcp_f32 v75,v75
v_mfma_f32_16x16x32_bf16 v[32:35],a[184:187],v[68:71],v[32:35]
buffer_load_dwordx4 v0,s[36:39],s66 offen offset:3072 sc1 lds
v_mfma_f32_16x16x32_bf16 v[36:39],a[188:191],v[68:71],v[36:39]
s_waitcnt lgkmcnt(2)
ds_read_b128 v[64:67],v2 offset:63488
v_mul_f32 v27,v27,v75
v_mfma_f32_16x16x32_bf16 v[32:35],a[192:195],v[40:43],v[32:35]
s_waitcnt vmcnt(9)
ds_read_b128 v[68:71],v2 offset:64512
v_xor_b32 v2,65536,v2
v_mfma_f32_16x16x32_bf16 v[36:39],a[196:199],v[40:43],v[36:39]
s_barrier
v_mfma_f32_16x16x32_bf16 v[32:35],a[200:203],v[44:47],v[32:35]
ds_read_b128 v[40:43],v2 offset:0
ds_read_b64 v[92:93],v11 offset:128
v_mfma_f32_16x16x32_bf16 v[36:39],a[204:207],v[44:47],v[36:39]
v_fma_f32 v28,-v96,v20,v28
v_fma_f32 v29,-v96,v21,v29
v_fma_f32 v30,-v96,v22,v30
v_fma_f32 v31,-v96,v23,v31
v_mul_f32 v76,v28,v28
v_mul_f32 v77,v29,v29
v_mul_f32 v78,v30,v30
v_mul_f32 v79,v31,v31
v_fma_f32 v76,v98,v76,v97
v_fma_f32 v77,v98,v77,v97
v_fma_f32 v78,v98,v78,v97
v_fma_f32 v79,v98,v79,v97
v_mul_f32 v76,v28,v76
v_mul_f32 v77,v29,v77
v_mul_f32 v78,v30,v78
v_mul_f32 v79,v31,v79
v_exp_f32 v76,v76
v_exp_f32 v77,v77
v_exp_f32 v78,v78
v_exp_f32 v79,v79
v_mfma_f32_16x16x32_bf16 v[32:35],a[208:211],v[48:51],v[32:35]
v_fma_f32 v76,v76,v99,v99
v_fma_f32 v77,v77,v99,v99
v_fma_f32 v78,v78,v99,v99
v_fma_f32 v79,v79,v99,v99
v_rcp_f32 v76,v76
v_rcp_f32 v77,v77
v_rcp_f32 v78,v78
v_rcp_f32 v79,v79
v_mfma_f32_16x16x32_bf16 v[36:39],a[212:215],v[48:51],v[36:39]
ds_read_b128 v[44:47],v2 offset:1024
v_cvt_pk_bf16_f32 v81,v26,v27
v_mul_f32 v28,v28,v76
v_mul_f32 v29,v29,v77
v_mul_f32 v30,v30,v78
v_mul_f32 v31,v31,v79
v_cvt_pk_bf16_f32 v82,v28,v29
v_cvt_pk_bf16_f32 v83,v30,v31
v_mfma_f32_16x16x32_bf16 v[32:35],a[216:219],v[52:55],v[32:35]
s_waitcnt lgkmcnt(1)
buffer_store_dwordx4 v[80:83],v3,s[36:39],s73 offen offset:0 sc1
ds_read_b128 v[48:51],v2 offset:2048
v_xor_b32 v8,16384,v8
v_mul_f32 v96,s75,v92
v_mfma_f32_16x16x32_bf16 v[36:39],a[220:223],v[52:55],v[36:39]
v_fma_f32 v93,v93,s75,v12
v_fma_f32 v93,-v96,v96,v93
v_mfma_f32_16x16x32_bf16 v[32:35],a[224:227],v[56:59],v[32:35]
ds_read_b128 v[52:55],v2 offset:3072
v_max_f32 v93,v93,v12
v_mfma_f32_16x16x32_bf16 v[36:39],a[228:231],v[56:59],v[36:39]
v_rsq_f32 v92,v93
v_mfma_f32_16x16x32_bf16 v[32:35],a[232:235],v[60:63],v[32:35]
ds_read_b128 v[56:59],v2 offset:4096
v_mul_f32 v97,s76,v92
v_mfma_f32_16x16x32_bf16 v[36:39],a[236:239],v[60:63],v[36:39]
v_mul_f32 v98,v92,v92
v_mfma_f32_16x16x32_bf16 v[32:35],a[240:243],v[64:67],v[32:35]
ds_read_b128 v[60:63],v2 offset:5120
v_mfma_f32_16x16x32_bf16 v[36:39],a[244:247],v[64:67],v[36:39]
v_mfma_f32_16x16x32_bf16 v[32:35],a[248:251],v[68:71],v[32:35]
s_waitcnt lgkmcnt(2)
ds_read_b128 v[64:67],v2 offset:6144
v_mfma_f32_16x16x32_bf16 v[36:39],a[252:255],v[68:71],v[36:39]
s_cbranch_scc1 Lg3o_d0w2_%=
s_branch Lg3o_slow0_%=
Lg3o_d0w3_%=:
v_mfma_f32_16x16x32_bf16 v[24:27],a[0:3],v[40:43],0
s_add_u32 m0,s62,16384
ds_read_b128 v[68:71],v2 offset:7168
v_mul_f32 v99,v93,v92
v_mfma_f32_16x16x32_bf16 v[28:31],a[4:7],v[40:43],0
s_mul_i32 s73,s47,7
v_mul_f32 v98,s77,v98
v_mul_f32 v98,v98,v97
v_mfma_f32_16x16x32_bf16 v[24:27],a[8:11],v[44:47],v[24:27]
s_lshr_b32 s73,s73,1
ds_read_b128 v[40:43],v2 offset:8192
s_lshr_b32 s72,s66,5
v_mfma_f32_16x16x32_bf16 v[28:31],a[12:15],v[44:47],v[28:31]
s_lshr_b32 s71,s62,2
v_fma_f32 v32,-v96,v16,v32
v_fma_f32 v33,-v96,v17,v33
v_mfma_f32_16x16x32_bf16 v[24:27],a[16:19],v[48:51],v[24:27]
buffer_load_dwordx4 v1,s[36:39],s66 offen offset:0 sc1 lds
v_mfma_f32_16x16x32_bf16 v[28:31],a[20:23],v[48:51],v[28:31]
s_mul_i32 s70,s46,5
ds_read_b128 v[44:47],v2 offset:9216
v_fma_f32 v34,-v96,v18,v34
v_mfma_f32_16x16x32_bf16 v[24:27],a[24:27],v[52:55],v[24:27]
ds_read_b128 v[48:51],v2 offset:10240
v_fma_f32 v35,-v96,v19,v35
v_mfma_f32_16x16x32_bf16 v[28:31],a[28:31],v[52:55],v[28:31]
s_waitcnt lgkmcnt(3)
v_mul_f32 v72,v32,v32
v_mul_f32 v73,v33,v33
v_mfma_f32_16x16x32_bf16 v[24:27],a[32:35],v[56:59],v[24:27]
buffer_load_dwordx4 v1,s[36:39],s66 offen offset:1024 sc1 lds
v_mfma_f32_16x16x32_bf16 v[28:31],a[36:39],v[56:59],v[28:31]
ds_read_b128 v[52:55],v2 offset:11264
v_mul_f32 v74,v34,v34
v_mfma_f32_16x16x32_bf16 v[24:27],a[40:43],v[60:63],v[24:27]
ds_read_b128 v[56:59],v2 offset:12288
v_mul_f32 v75,v35,v35
v_mfma_f32_16x16x32_bf16 v[28:31],a[44:47],v[60:63],v[28:31]
v_fma_f32 v72,v98,v72,v97
v_mul_f32 v72,v32,v72
v_mfma_f32_16x16x32_bf16 v[24:27],a[48:51],v[64:67],v[24:27]
buffer_load_dwordx4 v1,s[36:39],s66 offen offset:2048 sc1 lds
v_mfma_f32_16x16x32_bf16 v[28:31],a[52:55],v[64:67],v[28:31]
ds_read_b128 v[60:63],v2 offset:13312
v_fma_f32 v73,v98,v73,v97
v_mfma_f32_16x16x32_bf16 v[24:27],a[56:59],v[68:71],v[24:27]
ds_read_b128 v[64:67],v2 offset:14336
v_mul_f32 v73,v33,v73
v_mfma_f32_16x16x32_bf16 v[28:31],a[60:63],v[68:71],v[28:31]
s_waitcnt lgkmcnt(3)
v_exp_f32 v72,v72
v_mfma_f32_16x16x32_bf16 v[24:27],a[64:67],v[40:43],v[24:27]
s_waitcnt vmcnt(9)
ds_read_b128 v[68:71],v2 offset:15360
v_fma_f32 v72,v72,v99,v99
v_mfma_f32_16x16x32_bf16 v[28:31],a[68:71],v[40:43],v[28:31]
s_barrier
v_mfma_f32_16x16x32_bf16 v[24:27],a[72:75],v[44:47],v[24:27]
buffer_load_dwordx4 v1,s[36:39],s66 offen offset:3072 sc1 lds
v_mfma_f32_16x16x32_bf16 v[28:31],a[76:79],v[44:47],v[28:31]
s_add_u32 m0,s71,s88)"
R"(
ds_read_b128 v[40:43],v2 offset:16384
ds_read_b32 v88,v8 offset:0
v_mfma_f32_16x16x32_bf16 v[24:27],a[80:83],v[48:51],v[24:27]
buffer_load_dword v9,s[36:39],s72 offen offset:0 sc1 lds
v_mfma_f32_16x16x32_bf16 v[28:31],a[84:87],v[48:51],v[28:31]
s_xor_b32 s87,s64,65536
ds_read_b128 v[44:47],v2 offset:17408
v_fma_f32 v74,v98,v74,v97
v_mfma_f32_16x16x32_bf16 v[24:27],a[88:91],v[52:55],v[24:27]
s_cmp_ge_u32 s66,s59
ds_read_b128 v[48:51],v2 offset:18432
v_fma_f32 v75,v98,v75,v97
v_mfma_f32_16x16x32_bf16 v[28:31],a[92:95],v[52:55],v[28:31]
s_waitcnt lgkmcnt(4)
v_exp_f32 v73,v73
v_mfma_f32_16x16x32_bf16 v[24:27],a[96:99],v[56:59],v[24:27]
s_nop 0
ds_read_b128 v[52:55],v2 offset:19456
v_mul_f32 v74,v34,v74
v_mfma_f32_16x16x32_bf16 v[28:31],a[100:103],v[56:59],v[28:31]
s_barrier
v_mfma_f32_16x16x32_bf16 v[24:27],a[104:107],v[60:63],v[24:27]
s_waitcnt lgkmcnt(3)
ds_read_b128 v[56:59],v2 offset:20480
ds_read_b32 v13,v7 offset:0
v_mfma_f32_16x16x32_bf16 v[28:31],a[108:111],v[60:63],v[28:31]
s_cselect_b32 s86,s49,s50
v_exp_f32 v74,v74
v_mfma_f32_16x16x32_bf16 v[24:27],a[112:115],v[64:67],v[24:27]
s_cselect_b32 s87,s87,s63
ds_read_b128 v[60:63],v2 offset:21504
v_mul_f32 v75,v35,v75
v_mfma_f32_16x16x32_bf16 v[28:31],a[116:119],v[64:67],v[28:31]
s_mov_b32 s62,s87
v_exp_f32 v75,v75
v_mfma_f32_16x16x32_bf16 v[24:27],a[120:123],v[68:71],v[24:27]
s_waitcnt lgkmcnt(1)
ds_read_b128 v[64:67],v2 offset:22528
v_fma_f32 v73,v73,v99,v99
v_mfma_f32_16x16x32_bf16 v[28:31],a[124:127],v[68:71],v[28:31]
v_cmp_eq_u32 vcc,s60,v13
v_rcp_f32 v72,v72
v_fma_f32 v74,v74,v99,v99
v_mfma_f32_16x16x32_bf16 v[24:27],a[128:131],v[40:43],v[24:27]
s_cmp_eq_u32 vcc_lo,-1
ds_read_b128 v[68:71],v2 offset:23552
v_add_f32_dpp v88,v88,v88 quad_perm:[1,0,3,2] row_mask:0xf bank_mask:0xf
v_mfma_f32_16x16x32_bf16 v[28:31],a[132:135],v[40:43],v[28:31]
s_cselect_b32 s67,s86,s59
v_rcp_f32 v73,v73
v_mfma_f32_16x16x32_bf16 v[24:27],a[136:139],v[44:47],v[24:27]
s_mov_b32 m0,s62
ds_read_b128 v[40:43],v2 offset:24576
v_fma_f32 v75,v75,v99,v99
v_mfma_f32_16x16x32_bf16 v[28:31],a[140:143],v[44:47],v[28:31]
buffer_load_dwordx4 v0,s[36:39],s67 offen offset:0 sc1 lds
v_mfma_f32_16x16x32_bf16 v[24:27],a[144:147],v[48:51],v[24:27]
s_xor_b32 s63,s63,65536
ds_read_b128 v[44:47],v2 offset:25600
v_add_f32_dpp v88,v88,v88 quad_perm:[2,3,0,1] row_mask:0xf bank_mask:0xf
v_mfma_f32_16x16x32_bf16 v[28:31],a[148:151],v[48:51],v[28:31]
s_add_u32 s53,s52,s55
v_rcp_f32 v74,v74
v_mfma_f32_16x16x32_bf16 v[24:27],a[152:155],v[52:55],v[24:27]
s_cmp_eq_u32 s53,s56
ds_read_b128 v[48:51],v2 offset:26624
v_add_f32_dpp v88,v88,v88 row_half_mirror row_mask:0xf bank_mask:0xf
v_mfma_f32_16x16x32_bf16 v[28:31],a[156:159],v[52:55],v[28:31]
s_waitcnt lgkmcnt(3)
buffer_load_dwordx4 v0,s[36:39],s67 offen offset:1024 sc1 lds
v_mfma_f32_16x16x32_bf16 v[24:27],a[160:163],v[56:59],v[24:27]
s_cselect_b32 s53,s57,s53
ds_write_b32 v10,v88 offset:0
v_mfma_f32_16x16x32_bf16 v[28:31],a[164:167],v[56:59],v[28:31]
s_cmp_eq_u32 s53,s58
ds_read_b128 v[52:55],v2 offset:27648
v_mul_f32 v32,v32,v72
v_mfma_f32_16x16x32_bf16 v[24:27],a[168:171],v[60:63],v[24:27]
s_cselect_b32 s53,s59,s53
ds_read_b128 v[56:59],v2 offset:28672
v_mul_f32 v33,v33,v73
v_mfma_f32_16x16x32_bf16 v[28:31],a[172:175],v[60:63],v[28:31]
buffer_load_dwordx4 v0,s[36:39],s67 offen offset:2048 sc1 lds
v_mfma_f32_16x16x32_bf16 v[24:27],a[176:179],v[64:67],v[24:27]
s_lshr_b32 s70,s70,6
ds_read_b128 v[60:63],v2 offset:29696
v_mul_f32 v34,v34,v74
v_mfma_f32_16x16x32_bf16 v[28:31],a[180:183],v[64:67],v[28:31]
s_cmp_ge_u32 s66,s59
v_rcp_f32 v75,v75
v_mfma_f32_16x16x32_bf16 v[24:27],a[184:187],v[68:71],v[24:27]
ds_read_b128 v[64:67],v2 offset:30720
v_mul_f32 v35,v35,v75
v_mfma_f32_16x16x32_bf16 v[28:31],a[188:191],v[68:71],v[28:31]
s_waitcnt lgkmcnt(3)
buffer_load_dwordx4 v0,s[36:39],s67 offen offset:3072 sc1 lds
v_mfma_f32_16x16x32_bf16 v[24:27],a[192:195],v[40:43],v[24:27]
s_waitcnt vmcnt(9)
ds_read_b128 v[68:71],v2 offset:31744
v_cvt_pk_bf16_f32 v80,v32,v33
v_mfma_f32_16x16x32_bf16 v[28:31],a[196:199],v[40:43],v[28:31]
s_barrier
v_mfma_f32_16x16x32_bf16 v[24:27],a[200:203],v[44:47],v[24:27]
buffer_store_dword v5,v4,s[36:39],s70 offen offset:0 sc1
ds_read_b128 v[40:43],v2 offset:32768
ds_read_b64 v[92:93],v11 offset:0
v_cvt_pk_bf16_f32 v81,v34,v35
v_mfma_f32_16x16x32_bf16 v[28:31],a[204:207],v[44:47],v[28:31]
v_fma_f32 v36,-v96,v20,v36
v_fma_f32 v37,-v96,v21,v37
v_mfma_f32_16x16x32_bf16 v[24:27],a[208:211],v[48:51],v[24:27]
v_fma_f32 v38,-v96,v22,v38
v_fma_f32 v39,-v96,v23,v39
v_mul_f32 v76,v36,v36
v_mul_f32 v77,v37,v37
v_mul_f32 v78,v38,v38
v_mul_f32 v79,v39,v39
v_fma_f32 v76,v98,v76,v97
v_fma_f32 v77,v98,v77,v97
v_fma_f32 v78,v98,v78,v97
v_fma_f32 v79,v98,v79,v97
v_mul_f32 v76,v36,v76
v_mul_f32 v77,v37,v77
v_mul_f32 v78,v38,v78
v_mul_f32 v79,v39,v79
v_exp_f32 v76,v76
v_exp_f32 v77,v77
v_exp_f32 v78,v78
v_exp_f32 v79,v79
v_mfma_f32_16x16x32_bf16 v[28:31],a[212:215],v[48:51],v[28:31]
ds_read_b128 v[44:47],v2 offset:33792
v_fma_f32 v76,v76,v99,v99
v_fma_f32 v77,v77,v99,v99
v_fma_f32 v78,v78,v99,v99
v_fma_f32 v79,v79,v99,v99
v_rcp_f32 v76,v76
v_rcp_f32 v77,v77
v_rcp_f32 v78,v78
v_rcp_f32 v79,v79
v_mfma_f32_16x16x32_bf16 v[24:27],a[216:219],v[52:55],v[24:27]
s_waitcnt lgkmcnt(1)
v_mul_f32 v36,v36,v76
v_mul_f32 v37,v37,v77
v_mul_f32 v38,v38,v78
v_mul_f32 v39,v39,v79
v_cvt_pk_bf16_f32 v82,v36,v37
v_cvt_pk_bf16_f32 v83,v38,v39
v_mfma_f32_16x16x32_bf16 v[28:31],a[220:223],v[52:55],v[28:31]
ds_read_b128 v[48:51],v2 offset:34816
buffer_store_dwordx4 v[80:83],v3,s[36:39],s73 offen offset:0 sc1
v_mul_f32 v96,s75,v92
v_fma_f32 v93,v93,s75,v12
v_fma_f32 v93,-v96,v96,v93
v_mfma_f32_16x16x32_bf16 v[24:27],a[224:227],v[56:59],v[24:27]
ds_read_b128 v[52:55],v2 offset:35840
v_max_f32 v93,v93,v12
v_mfma_f32_16x16x32_bf16 v[28:31],a[228:231],v[56:59],v[28:31]
v_rsq_f32 v92,v93
v_mfma_f32_16x16x32_bf16 v[24:27],a[232:235],v[60:63],v[24:27]
ds_read_b128 v[56:59],v2 offset:36864
v_mul_f32 v97,s76,v92
v_mfma_f32_16x16x32_bf16 v[28:31],a[236:239],v[60:63],v[28:31]
v_mul_f32 v98,v92,v92
v_mfma_f32_16x16x32_bf16 v[24:27],a[240:243],v[64:67],v[24:27]
ds_read_b128 v[60:63],v2 offset:37888
v_mfma_f32_16x16x32_bf16 v[28:31],a[244:247],v[64:67],v[28:31]
v_mfma_f32_16x16x32_bf16 v[24:27],a[248:251],v[68:71],v[24:27]
s_waitcnt lgkmcnt(2)
ds_read_b128 v[64:67],v2 offset:38912
v_mfma_f32_16x16x32_bf16 v[28:31],a[252:255],v[68:71],v[28:31]
s_cbranch_scc1 Lg3o_slow1_%=
Lg3o_d1w3_%=:
v_mfma_f32_16x16x32_bf16 v[32:35],a[0:3],v[40:43],0
s_add_u32 m0,s62,16384
ds_read_b128 v[68:71],v2 offset:39936
v_mul_f32 v99,v93,v92
v_mfma_f32_16x16x32_bf16 v[36:39],a[4:7],v[40:43],0
s_mul_i32 s73,s48,7
v_mul_f32 v98,s77,v98
v_mul_f32 v98,v98,v97
v_mfma_f32_16x16x32_bf16 v[32:35],a[8:11],v[44:47],v[32:35]
s_lshr_b32 s73,s73,1
ds_read_b128 v[40:43],v2 offset:40960
s_lshr_b32 s72,s67,5
v_mfma_f32_16x16x32_bf16 v[36:39],a[12:15],v[44:47],v[36:39]
s_lshr_b32 s71,s62,2
v_fma_f32 v24,-v96,v16,v24
v_fma_f32 v25,-v96,v17,v25
v_mfma_f32_16x16x32_bf16 v[32:35],a[16:19],v[48:51],v[32:35]
buffer_load_dwordx4 v1,s[36:39],s67 offen offset:0 sc1 lds
v_mfma_f32_16x16x32_bf16 v[36:39],a[20:23],v[48:51],v[36:39]
s_mul_i32 s70,s47,5
ds_read_b128 v[44:47],v2 offset:41984
v_fma_f32 v26,-v96,v18,v26
v_mfma_f32_16x16x32_bf16 v[32:35],a[24:27],v[52:55],v[32:35]
s_mov_b64 s[44:45],s[46:47]
ds_read_b128 v[48:51],v2 offset:43008
v_fma_f32 v27,-v96,v19,v27
v_mfma_f32_16x16x32_bf16 v[36:39],a[28:31],v[52:55],v[36:39]
s_waitcnt lgkmcnt(3))"
R"(
v_mul_f32 v72,v24,v24
v_mul_f32 v73,v25,v25
v_mfma_f32_16x16x32_bf16 v[32:35],a[32:35],v[56:59],v[32:35]
buffer_load_dwordx4 v1,s[36:39],s67 offen offset:1024 sc1 lds
v_mfma_f32_16x16x32_bf16 v[36:39],a[36:39],v[56:59],v[36:39]
s_mov_b64 s[46:47],s[48:49]
ds_read_b128 v[52:55],v2 offset:44032
v_mul_f32 v74,v26,v26
v_mfma_f32_16x16x32_bf16 v[32:35],a[40:43],v[60:63],v[32:35]
s_mov_b64 s[48:49],s[50:51]
ds_read_b128 v[56:59],v2 offset:45056
v_mul_f32 v75,v27,v27
v_mfma_f32_16x16x32_bf16 v[36:39],a[44:47],v[60:63],v[36:39]
v_fma_f32 v72,v98,v72,v97
v_mul_f32 v72,v24,v72
v_mfma_f32_16x16x32_bf16 v[32:35],a[48:51],v[64:67],v[32:35]
buffer_load_dwordx4 v1,s[36:39],s67 offen offset:2048 sc1 lds
v_mfma_f32_16x16x32_bf16 v[36:39],a[52:55],v[64:67],v[36:39]
ds_read_b128 v[60:63],v2 offset:46080
v_fma_f32 v73,v98,v73,v97
v_mfma_f32_16x16x32_bf16 v[32:35],a[56:59],v[68:71],v[32:35]
ds_read_b128 v[64:67],v2 offset:47104
v_mul_f32 v73,v25,v73
v_mfma_f32_16x16x32_bf16 v[36:39],a[60:63],v[68:71],v[36:39]
s_waitcnt lgkmcnt(3)
v_exp_f32 v72,v72
v_mfma_f32_16x16x32_bf16 v[32:35],a[64:67],v[40:43],v[32:35]
s_waitcnt vmcnt(9)
ds_read_b128 v[68:71],v2 offset:48128
v_fma_f32 v72,v72,v99,v99
v_mfma_f32_16x16x32_bf16 v[36:39],a[68:71],v[40:43],v[36:39]
s_barrier
v_mfma_f32_16x16x32_bf16 v[32:35],a[72:75],v[44:47],v[32:35]
buffer_load_dwordx4 v1,s[36:39],s67 offen offset:3072 sc1 lds
v_mfma_f32_16x16x32_bf16 v[36:39],a[76:79],v[44:47],v[36:39]
s_add_u32 m0,s71,s88
ds_read_b128 v[40:43],v2 offset:49152
ds_read_b32 v88,v8 offset:8192
v_mfma_f32_16x16x32_bf16 v[32:35],a[80:83],v[48:51],v[32:35]
buffer_load_dword v9,s[36:39],s72 offen offset:0 sc1 lds
v_mfma_f32_16x16x32_bf16 v[36:39],a[84:87],v[48:51],v[36:39]
s_xor_b32 s87,s63,65536
ds_read_b128 v[44:47],v2 offset:50176
v_fma_f32 v74,v98,v74,v97
v_mfma_f32_16x16x32_bf16 v[32:35],a[88:91],v[52:55],v[32:35]
s_cmp_ge_u32 s67,s59
ds_read_b128 v[48:51],v2 offset:51200
v_fma_f32 v75,v98,v75,v97
v_mfma_f32_16x16x32_bf16 v[36:39],a[92:95],v[52:55],v[36:39]
s_waitcnt lgkmcnt(4)
v_exp_f32 v73,v73
v_mfma_f32_16x16x32_bf16 v[32:35],a[96:99],v[56:59],v[32:35]
s_nop 0
ds_read_b128 v[52:55],v2 offset:52224
v_mul_f32 v74,v26,v74
v_mfma_f32_16x16x32_bf16 v[36:39],a[100:103],v[56:59],v[36:39]
s_barrier
v_mfma_f32_16x16x32_bf16 v[32:35],a[104:107],v[60:63],v[32:35]
s_waitcnt lgkmcnt(3)
ds_read_b128 v[56:59],v2 offset:53248
ds_read_b32 v13,v7 offset:256
v_mfma_f32_16x16x32_bf16 v[36:39],a[108:111],v[60:63],v[36:39]
s_cselect_b32 s86,s50,s51
v_exp_f32 v74,v74
v_mfma_f32_16x16x32_bf16 v[32:35],a[112:115],v[64:67],v[32:35]
s_cselect_b32 s87,s87,s64
ds_read_b128 v[60:63],v2 offset:54272
v_mul_f32 v75,v27,v75
v_mfma_f32_16x16x32_bf16 v[36:39],a[116:119],v[64:67],v[36:39]
s_mov_b32 s62,s87
v_exp_f32 v75,v75
v_mfma_f32_16x16x32_bf16 v[32:35],a[120:123],v[68:71],v[32:35]
s_waitcnt lgkmcnt(1)
ds_read_b128 v[64:67],v2 offset:55296
v_fma_f32 v73,v73,v99,v99
v_mfma_f32_16x16x32_bf16 v[36:39],a[124:127],v[68:71],v[36:39]
v_cmp_eq_u32 vcc,s60,v13
v_rcp_f32 v72,v72
v_fma_f32 v74,v74,v99,v99
v_mfma_f32_16x16x32_bf16 v[32:35],a[128:131],v[40:43],v[32:35]
s_cmp_eq_u32 vcc_lo,-1
ds_read_b128 v[68:71],v2 offset:56320
v_add_f32_dpp v88,v88,v88 quad_perm:[1,0,3,2] row_mask:0xf bank_mask:0xf
v_mfma_f32_16x16x32_bf16 v[36:39],a[132:135],v[40:43],v[36:39]
s_cselect_b32 s66,s86,s59
v_rcp_f32 v73,v73
v_mfma_f32_16x16x32_bf16 v[32:35],a[136:139],v[44:47],v[32:35]
s_mov_b32 m0,s62
ds_read_b128 v[40:43],v2 offset:57344
v_fma_f32 v75,v75,v99,v99
v_mfma_f32_16x16x32_bf16 v[36:39],a[140:143],v[44:47],v[36:39]
buffer_load_dwordx4 v0,s[36:39],s66 offen offset:0 sc1 lds
v_mfma_f32_16x16x32_bf16 v[32:35],a[144:147],v[48:51],v[32:35]
s_xor_b32 s64,s64,65536
ds_read_b128 v[44:47],v2 offset:58368
v_add_f32_dpp v88,v88,v88 quad_perm:[2,3,0,1] row_mask:0xf bank_mask:0xf
v_mfma_f32_16x16x32_bf16 v[36:39],a[148:151],v[48:51],v[36:39]
s_add_u32 s74,s53,s55
v_rcp_f32 v74,v74
v_mfma_f32_16x16x32_bf16 v[32:35],a[152:155],v[52:55],v[32:35]
s_cmp_eq_u32 s74,s56
ds_read_b128 v[48:51],v2 offset:59392
v_add_f32_dpp v88,v88,v88 row_half_mirror row_mask:0xf bank_mask:0xf
v_mfma_f32_16x16x32_bf16 v[36:39],a[156:159],v[52:55],v[36:39]
s_waitcnt lgkmcnt(3)
buffer_load_dwordx4 v0,s[36:39],s66 offen offset:1024 sc1 lds
v_mfma_f32_16x16x32_bf16 v[32:35],a[160:163],v[56:59],v[32:35]
s_cselect_b32 s74,s57,s74
ds_write_b32 v10,v88 offset:128
v_mfma_f32_16x16x32_bf16 v[36:39],a[164:167],v[56:59],v[36:39]
s_cmp_eq_u32 s74,s58
ds_read_b128 v[52:55],v2 offset:60416
v_mul_f32 v24,v24,v72
v_mfma_f32_16x16x32_bf16 v[32:35],a[168:171],v[60:63],v[32:35]
s_cselect_b32 s74,s59,s74
ds_read_b128 v[56:59],v2 offset:61440
v_mul_f32 v25,v25,v73
v_mfma_f32_16x16x32_bf16 v[36:39],a[172:175],v[60:63],v[36:39]
buffer_load_dwordx4 v0,s[36:39],s66 offen offset:2048 sc1 lds
v_mfma_f32_16x16x32_bf16 v[32:35],a[176:179],v[64:67],v[32:35]
s_lshr_b32 s70,s70,6
ds_read_b128 v[60:63],v2 offset:62464
v_mul_f32 v26,v26,v74
v_mfma_f32_16x16x32_bf16 v[36:39],a[180:183],v[64:67],v[36:39]
s_mov_b64 s[50:51],s[52:53]
v_rcp_f32 v75,v75
v_mfma_f32_16x16x32_bf16 v[32:35],a[184:187],v[68:71],v[32:35]
s_mov_b32 s52,s74
ds_read_b128 v[64:67],v2 offset:63488
v_mul_f32 v27,v27,v75
v_mfma_f32_16x16x32_bf16 v[36:39],a[188:191],v[68:71],v[36:39]
s_waitcnt lgkmcnt(3)
buffer_load_dwordx4 v0,s[36:39],s66 offen offset:3072 sc1 lds
v_mfma_f32_16x16x32_bf16 v[32:35],a[192:195],v[40:43],v[32:35]
s_waitcnt vmcnt(9)
ds_read_b128 v[68:71],v2 offset:64512
v_xor_b32 v2,65536,v2
v_mfma_f32_16x16x32_bf16 v[36:39],a[196:199],v[40:43],v[36:39]
s_barrier
v_mfma_f32_16x16x32_bf16 v[32:35],a[200:203],v[44:47],v[32:35]
buffer_store_dword v5,v4,s[36:39],s70 offen offset:0 sc1
ds_read_b128 v[40:43],v2 offset:0
ds_read_b64 v[92:93],v11 offset:128
v_cvt_pk_bf16_f32 v80,v24,v25
v_mfma_f32_16x16x32_bf16 v[36:39],a[204:207],v[44:47],v[36:39]
s_cmp_lt_u32 s67,s59
v_cvt_pk_bf16_f32 v81,v26,v27
v_fma_f32 v28,-v96,v20,v28
v_mfma_f32_16x16x32_bf16 v[32:35],a[208:211],v[48:51],v[32:35]
v_fma_f32 v29,-v96,v21,v29
v_fma_f32 v30,-v96,v22,v30
v_fma_f32 v31,-v96,v23,v31
v_mul_f32 v76,v28,v28
v_mul_f32 v77,v29,v29
v_mul_f32 v78,v30,v30
v_mul_f32 v79,v31,v31
v_fma_f32 v76,v98,v76,v97
v_fma_f32 v77,v98,v77,v97
v_fma_f32 v78,v98,v78,v97
v_fma_f32 v79,v98,v79,v97
v_mul_f32 v76,v28,v76
v_mul_f32 v77,v29,v77
v_mul_f32 v78,v30,v78
v_mul_f32 v79,v31,v79
v_exp_f32 v76,v76
v_exp_f32 v77,v77
v_exp_f32 v78,v78
v_exp_f32 v79,v79
v_mfma_f32_16x16x32_bf16 v[36:39],a[212:215],v[48:51],v[36:39]
ds_read_b128 v[44:47],v2 offset:1024
v_fma_f32 v76,v76,v99,v99
v_fma_f32 v77,v77,v99,v99
v_fma_f32 v78,v78,v99,v99
v_fma_f32 v79,v79,v99,v99
v_rcp_f32 v76,v76
v_rcp_f32 v77,v77
v_rcp_f32 v78,v78
v_rcp_f32 v79,v79
v_mfma_f32_16x16x32_bf16 v[32:35],a[216:219],v[52:55],v[32:35]
s_waitcnt lgkmcnt(1)
v_mul_f32 v28,v28,v76
v_mul_f32 v29,v29,v77
v_mul_f32 v30,v30,v78
v_mul_f32 v31,v31,v79
v_cvt_pk_bf16_f32 v82,v28,v29
v_cvt_pk_bf16_f32 v83,v30,v31
v_mfma_f32_16x16x32_bf16 v[36:39],a[220:223],v[52:55],v[36:39]
ds_read_b128 v[48:51],v2 offset:2048
buffer_store_dwordx4 v[80:83],v3,s[36:39],s73 offen offset:0 sc1
v_mul_f32 v96,s75,v92
v_xor_b32 v8,16384,v8
v_fma_f32 v93,v93,s75,v12
v_mfma_f32_16x16x32_bf16 v[32:35],a[224:227],v[56:59],v[32:35]
ds_read_b128 v[52:55],v2 offset:3072
v_fma_f32 v93,-v96,v96,v93
v_mfma_f32_16x16x32_bf16 v[36:39],a[228:231],v[56:59],v[36:39]
v_max_f32 v93,v93,v12
v_mfma_f32_16x16x32_bf16 v[32:35],a[232:235],v[60:63],v[32:35]
ds_read_b128 v[56:59],v2 offset:4096
v_mfma_f32_16x16x32_bf16 v[36:39],a[236:239],v[60:63],v[36:39])"
R"(
v_rsq_f32 v92,v93
v_mfma_f32_16x16x32_bf16 v[32:35],a[240:243],v[64:67],v[32:35]
ds_read_b128 v[60:63],v2 offset:5120
v_mul_f32 v97,s76,v92
v_mfma_f32_16x16x32_bf16 v[36:39],a[244:247],v[64:67],v[36:39]
v_mul_f32 v98,v92,v92
v_mfma_f32_16x16x32_bf16 v[32:35],a[248:251],v[68:71],v[32:35]
s_waitcnt lgkmcnt(2)
ds_read_b128 v[64:67],v2 offset:6144
v_mfma_f32_16x16x32_bf16 v[36:39],a[252:255],v[68:71],v[36:39]
s_cbranch_scc1 Lg3o_d0w3_%=
s_branch Lg3o_slow0_%=
Lg3o_slow0_%=:
s_cmp_lt_u32 s66,s59
s_cbranch_scc1 Lg3o_gs0_%=
Lg3o_slowb0_%=:
s_cmp_eq_u32 s54,1
s_cselect_b32 s68,s49,s48
s_lshr_b32 s68,s68,5
s_add_u32 m0,s78,8192
s_nop 0
buffer_load_dword v6,s[36:39],s68 offen offset:0 sc1 lds
s_mul_i32 s73,s47,7
s_lshr_b32 s73,s73,1
v_mul_f32 v99,v93,v92
v_mul_f32 v98,s77,v98
v_mul_f32 v98,v98,v97
v_fma_f32 v32,-v96,v16,v32
v_fma_f32 v33,-v96,v17,v33
v_fma_f32 v34,-v96,v18,v34
v_fma_f32 v35,-v96,v19,v35
v_mul_f32 v72,v32,v32
v_mul_f32 v73,v33,v33
v_mul_f32 v74,v34,v34
v_mul_f32 v75,v35,v35
v_fma_f32 v72,v98,v72,v97
v_fma_f32 v73,v98,v73,v97
v_fma_f32 v74,v98,v74,v97
v_fma_f32 v75,v98,v75,v97
v_mul_f32 v72,v32,v72
v_mul_f32 v73,v33,v73
v_mul_f32 v74,v34,v74
v_mul_f32 v75,v35,v75
v_exp_f32 v72,v72
v_exp_f32 v73,v73
v_exp_f32 v74,v74
v_exp_f32 v75,v75
v_fma_f32 v72,v72,v99,v99
v_fma_f32 v73,v73,v99,v99
v_fma_f32 v74,v74,v99,v99
v_fma_f32 v75,v75,v99,v99
v_rcp_f32 v72,v72
v_rcp_f32 v73,v73
v_rcp_f32 v74,v74
v_rcp_f32 v75,v75
v_mul_f32 v32,v32,v72
v_mul_f32 v33,v33,v73
v_mul_f32 v34,v34,v74
v_mul_f32 v35,v35,v75
v_cvt_pk_bf16_f32 v80,v32,v33
s_add_u32 m0,s78,8448
s_nop 0
buffer_load_dword v6,s[36:39],s68 offen offset:0 sc1 lds
v_cvt_pk_bf16_f32 v81,v34,v35
v_fma_f32 v36,-v96,v20,v36
v_fma_f32 v37,-v96,v21,v37
v_fma_f32 v38,-v96,v22,v38
v_fma_f32 v39,-v96,v23,v39
v_mul_f32 v76,v36,v36
v_mul_f32 v77,v37,v37
v_mul_f32 v78,v38,v38
v_mul_f32 v79,v39,v39
v_fma_f32 v76,v98,v76,v97
v_fma_f32 v77,v98,v77,v97
v_fma_f32 v78,v98,v78,v97
v_fma_f32 v79,v98,v79,v97
v_mul_f32 v76,v36,v76
v_mul_f32 v77,v37,v77
v_mul_f32 v78,v38,v78
v_mul_f32 v79,v39,v79
v_exp_f32 v76,v76
v_exp_f32 v77,v77
v_exp_f32 v78,v78
v_exp_f32 v79,v79
v_fma_f32 v76,v76,v99,v99
v_fma_f32 v77,v77,v99,v99
v_fma_f32 v78,v78,v99,v99
v_fma_f32 v79,v79,v99,v99
v_rcp_f32 v76,v76
v_rcp_f32 v77,v77
v_rcp_f32 v78,v78
v_rcp_f32 v79,v79
v_mul_f32 v36,v36,v76
v_mul_f32 v37,v37,v77
v_mul_f32 v38,v38,v78
v_mul_f32 v39,v39,v79
v_cvt_pk_bf16_f32 v82,v36,v37
v_cvt_pk_bf16_f32 v83,v38,v39
buffer_store_dwordx4 v[80:83],v3,s[36:39],s73 offen offset:0 sc1
s_cmp_ge_u32 s48,s59
s_cbranch_scc1 Lg3o_done0_%=
s_add_u32 m0,s78,16384
s_nop 0
buffer_load_dword v6,s[36:39],s68 offen offset:0 sc1 lds
s_waitcnt vmcnt(3)
s_barrier
ds_read_b32 v13,v7 offset:8192
s_mul_i32 s70,s46,5
s_lshr_b32 s70,s70,6
buffer_store_dword v5,v4,s[36:39],s70 offen offset:0 sc1
s_mov_b32 s46,s59
s_mov_b32 s69,18432
s_waitcnt lgkmcnt(0)
v_cmp_eq_u32 vcc,s60,v13
s_nop 0
s_cmp_eq_u32 vcc_lo,-1
s_cbranch_scc1 Lg3o_go0_%=
s_add_u32 m0,s78,16640
s_nop 0
buffer_load_dword v6,s[36:39],s68 offen offset:0 sc1 lds
s_waitcnt vmcnt(4)
s_barrier
ds_read_b32 v13,v7 offset:8448
s_mov_b32 s69,18688
s_waitcnt lgkmcnt(0)
v_cmp_eq_u32 vcc,s60,v13
s_nop 0
s_cmp_eq_u32 vcc_lo,-1
s_cbranch_scc1 Lg3o_go0_%=
s_add_u32 m0,s78,8192
s_nop 0
buffer_load_dword v6,s[36:39],s68 offen offset:0 sc1 lds
s_waitcnt vmcnt(3)
s_barrier
ds_read_b32 v13,v7 offset:16384
s_mul_i32 s70,s47,5
s_lshr_b32 s70,s70,6
buffer_store_dword v5,v4,s[36:39],s70 offen offset:0 sc1
s_mov_b32 s47,s59
s_mov_b32 s69,10240
s_waitcnt lgkmcnt(0)
v_cmp_eq_u32 vcc,s60,v13
s_nop 0
s_cmp_eq_u32 vcc_lo,-1
s_cbranch_scc1 Lg3o_go0_%=
s_add_u32 m0,s78,8448
s_nop 0
buffer_load_dword v6,s[36:39],s68 offen offset:0 sc1 lds
s_waitcnt vmcnt(3)
s_barrier
ds_read_b32 v13,v7 offset:16640
s_mov_b32 s69,10496
s_waitcnt lgkmcnt(0)
v_cmp_eq_u32 vcc,s60,v13
s_nop 0
s_cmp_eq_u32 vcc_lo,-1
s_cbranch_scc1 Lg3o_go0_%=
Lg3o_poll0_%=:
s_add_u32 m0,s78,16384
s_nop 0
buffer_load_dword v6,s[36:39],s68 offen offset:0 sc1 lds
s_waitcnt vmcnt(2)
s_barrier
ds_read_b32 v13,v7 offset:8192
s_mov_b32 s69,18432
s_waitcnt lgkmcnt(0)
v_cmp_eq_u32 vcc,s60,v13
s_nop 0
s_cmp_eq_u32 vcc_lo,-1
s_cbranch_scc1 Lg3o_go0_%=
s_sleep 4
s_add_u32 m0,s78,16640
s_nop 0
buffer_load_dword v6,s[36:39],s68 offen offset:0 sc1 lds
s_waitcnt vmcnt(2)
s_barrier
ds_read_b32 v13,v7 offset:8448
s_mov_b32 s69,18688
s_waitcnt lgkmcnt(0)
v_cmp_eq_u32 vcc,s60,v13
s_nop 0
s_cmp_eq_u32 vcc_lo,-1
s_cbranch_scc1 Lg3o_go0_%=
s_sleep 4
s_add_u32 m0,s78,8192
s_nop 0
buffer_load_dword v6,s[36:39],s68 offen offset:0 sc1 lds
s_waitcnt vmcnt(2)
s_barrier
ds_read_b32 v13,v7 offset:16384
s_mov_b32 s69,10240
s_waitcnt lgkmcnt(0)
v_cmp_eq_u32 vcc,s60,v13
s_nop 0
s_cmp_eq_u32 vcc_lo,-1
s_cbranch_scc1 Lg3o_go0_%=
s_sleep 4
s_add_u32 m0,s78,8448
s_nop 0
buffer_load_dword v6,s[36:39],s68 offen offset:0 sc1 lds)"
R"(
s_waitcnt vmcnt(2)
s_barrier
ds_read_b32 v13,v7 offset:16640
s_mov_b32 s69,10496
s_waitcnt lgkmcnt(0)
v_cmp_eq_u32 vcc,s60,v13
s_nop 0
s_cmp_eq_u32 vcc_lo,-1
s_cbranch_scc1 Lg3o_go0_%=
s_sleep 4
s_add_u32 s79,s79,1
s_cmp_lt_u32 s79,20000
s_cbranch_scc1 Lg3o_poll0_%=
Lg3o_go0_%=:
s_xor_b32 s62,s63,65536
s_mov_b32 m0,s62
s_nop 0
buffer_load_dwordx4 v0,s[36:39],s48 offen offset:0 sc1 lds
buffer_load_dwordx4 v0,s[36:39],s48 offen offset:1024 sc1 lds
buffer_load_dwordx4 v0,s[36:39],s48 offen offset:2048 sc1 lds
buffer_load_dwordx4 v0,s[36:39],s48 offen offset:3072 sc1 lds
s_add_u32 m0,s62,16384
s_nop 0
buffer_load_dwordx4 v1,s[36:39],s48 offen offset:0 sc1 lds
buffer_load_dwordx4 v1,s[36:39],s48 offen offset:1024 sc1 lds
buffer_load_dwordx4 v1,s[36:39],s48 offen offset:2048 sc1 lds
buffer_load_dwordx4 v1,s[36:39],s48 offen offset:3072 sc1 lds
s_lshr_b32 s72,s48,5
s_lshr_b32 s71,s62,2
s_add_u32 m0,s71,s88
s_nop 0
buffer_load_dword v9,s[36:39],s72 offen offset:0 sc1 lds
s_cmp_eq_u32 vcc_hi,-1
s_cselect_b32 s66,s49,s59
s_xor_b32 s62,s64,65536
s_cmp_ge_u32 s66,s59
s_cselect_b32 s68,s49,s50
s_lshr_b32 s68,s68,5
s_add_u32 m0,s78,0
s_nop 0
buffer_load_dword v6,s[36:39],s68 offen offset:0 sc1 lds
s_mov_b32 m0,s62
s_nop 0
buffer_load_dwordx4 v0,s[36:39],s66 offen offset:0 sc1 lds
buffer_load_dwordx4 v0,s[36:39],s66 offen offset:1024 sc1 lds
buffer_load_dwordx4 v0,s[36:39],s66 offen offset:2048 sc1 lds
buffer_load_dwordx4 v0,s[36:39],s66 offen offset:3072 sc1 lds
s_waitcnt vmcnt(10)
s_barrier
ds_read_b128 v[40:43],v2 offset:0
ds_read_b128 v[44:47],v2 offset:1024
ds_read_b128 v[48:51],v2 offset:2048
ds_read_b128 v[52:55],v2 offset:3072
ds_read_b128 v[56:59],v2 offset:4096
ds_read_b128 v[60:63],v2 offset:5120
ds_read_b128 v[64:67],v2 offset:6144
s_mul_i32 s70,s46,5
s_lshr_b32 s70,s70,6
buffer_store_dword v5,v4,s[36:39],s70 offen offset:0 sc1
s_mov_b32 s46,s59
s_mul_i32 s70,s47,5
s_lshr_b32 s70,s70,6
buffer_store_dword v5,v4,s[36:39],s70 offen offset:0 sc1
s_mov_b32 s47,s59
s_mul_i32 s70,s59,5
s_lshr_b32 s70,s70,6
buffer_store_dword v5,v4,s[36:39],s70 offen offset:0 sc1
s_waitcnt lgkmcnt(3)
s_setpc_b64 s[90:91]
Lg3o_gs0_%=:
s_cmp_eq_u32 s54,1
s_cselect_b32 s68,s49,s48
s_lshr_b32 s68,s68,5
s_mul_i32 s73,s47,7
s_lshr_b32 s73,s73,1
s_add_u32 m0,s78,8192
s_nop 0
buffer_load_dword v6,s[36:39],s68 offen offset:0 sc1 lds
v_mul_f32 v99,v93,v92
v_mul_f32 v98,s77,v98
v_mul_f32 v98,v98,v97
v_fma_f32 v32,-v96,v16,v32
v_fma_f32 v33,-v96,v17,v33
v_fma_f32 v34,-v96,v18,v34
v_fma_f32 v35,-v96,v19,v35
v_mul_f32 v72,v32,v32
v_mul_f32 v73,v33,v33
v_mul_f32 v74,v34,v34
v_mul_f32 v75,v35,v35
v_fma_f32 v72,v98,v72,v97
s_add_u32 m0,s62,16384
s_nop 0
buffer_load_dwordx4 v1,s[36:39],s66 offen offset:0 sc1 lds
v_fma_f32 v73,v98,v73,v97
v_fma_f32 v74,v98,v74,v97
v_fma_f32 v75,v98,v75,v97
v_mul_f32 v72,v32,v72
v_mul_f32 v73,v33,v73
v_mul_f32 v74,v34,v74
v_mul_f32 v75,v35,v75
v_exp_f32 v72,v72
v_exp_f32 v73,v73
v_exp_f32 v74,v74
v_exp_f32 v75,v75
v_fma_f32 v72,v72,v99,v99
buffer_load_dwordx4 v1,s[36:39],s66 offen offset:1024 sc1 lds
v_fma_f32 v73,v73,v99,v99
v_fma_f32 v74,v74,v99,v99
v_fma_f32 v75,v75,v99,v99
v_rcp_f32 v72,v72
v_rcp_f32 v73,v73
v_rcp_f32 v74,v74
v_rcp_f32 v75,v75
v_mul_f32 v32,v32,v72
v_mul_f32 v33,v33,v73
v_mul_f32 v34,v34,v74
v_mul_f32 v35,v35,v75
v_cvt_pk_bf16_f32 v80,v32,v33
buffer_load_dwordx4 v1,s[36:39],s66 offen offset:2048 sc1 lds
v_cvt_pk_bf16_f32 v81,v34,v35
v_fma_f32 v36,-v96,v20,v36
v_fma_f32 v37,-v96,v21,v37
v_fma_f32 v38,-v96,v22,v38
v_fma_f32 v39,-v96,v23,v39
v_mul_f32 v76,v36,v36
v_mul_f32 v77,v37,v37
v_mul_f32 v78,v38,v38
v_mul_f32 v79,v39,v39
v_fma_f32 v76,v98,v76,v97
v_fma_f32 v77,v98,v77,v97
v_fma_f32 v78,v98,v78,v97
buffer_load_dwordx4 v1,s[36:39],s66 offen offset:3072 sc1 lds
v_fma_f32 v79,v98,v79,v97
v_mul_f32 v76,v36,v76
v_mul_f32 v77,v37,v77
v_mul_f32 v78,v38,v78
v_mul_f32 v79,v39,v79
v_exp_f32 v76,v76
v_exp_f32 v77,v77
v_exp_f32 v78,v78
v_exp_f32 v79,v79
v_fma_f32 v76,v76,v99,v99
v_fma_f32 v77,v77,v99,v99
v_fma_f32 v78,v78,v99,v99
s_lshr_b32 s72,s66,5
s_lshr_b32 s71,s62,2
s_add_u32 m0,s71,s88
s_nop 0
buffer_load_dword v9,s[36:39],s72 offen offset:0 sc1 lds
v_fma_f32 v79,v79,v99,v99
v_rcp_f32 v76,v76
v_rcp_f32 v77,v77
v_rcp_f32 v78,v78
v_rcp_f32 v79,v79
v_mul_f32 v36,v36,v76
v_mul_f32 v37,v37,v77
v_mul_f32 v38,v38,v78
v_mul_f32 v39,v39,v79
v_cvt_pk_bf16_f32 v82,v36,v37
v_cvt_pk_bf16_f32 v83,v38,v39
buffer_store_dwordx4 v[80:83],v3,s[36:39],s73 offen offset:0 sc1
s_waitcnt vmcnt(6)
s_barrier
ds_read_b32 v13,v7 offset:8192
ds_read_b128 v[40:43],v2 offset:0
ds_read_b128 v[44:47],v2 offset:1024
ds_read_b128 v[48:51],v2 offset:2048
ds_read_b128 v[52:55],v2 offset:3072
ds_read_b128 v[56:59],v2 offset:4096
ds_read_b128 v[60:63],v2 offset:5120
ds_read_b128 v[64:67],v2 offset:6144
s_mul_i32 s70,s46,5
s_lshr_b32 s70,s70,6
buffer_store_dword v5,v4,s[36:39],s70 offen offset:0 sc1
s_waitcnt lgkmcnt(7)
v_cmp_eq_u32 vcc,s60,v13
s_nop 0
s_cmp_eq_u32 vcc_hi,-1
s_cselect_b32 s66,s49,s59
s_xor_b32 s62,s64,65536
s_cmp_ge_u32 s66,s59
s_cselect_b32 s68,s49,s50
s_lshr_b32 s68,s68,5
s_add_u32 m0,s78,0
s_nop 0
buffer_load_dword v6,s[36:39],s68 offen offset:0 sc1 lds
s_mov_b32 m0,s62
s_nop 0
buffer_load_dwordx4 v0,s[36:39],s66 offen offset:0 sc1 lds
buffer_load_dwordx4 v0,s[36:39],s66 offen offset:1024 sc1 lds
buffer_load_dwordx4 v0,s[36:39],s66 offen offset:2048 sc1 lds
buffer_load_dwordx4 v0,s[36:39],s66 offen offset:3072 sc1 lds
s_mov_b32 s46,s47
s_mov_b32 s47,s59
s_mul_i32 s70,s59,5
s_lshr_b32 s70,s70,6
buffer_store_dword v5,v4,s[36:39],s70 offen offset:0 sc1
s_waitcnt lgkmcnt(3)
s_setpc_b64 s[90:91]
Lg3o_done0_%=:)"
R"(
s_waitcnt vmcnt(0)
s_barrier
s_mul_i32 s70,s46,5
s_lshr_b32 s70,s70,6
buffer_store_dword v5,v4,s[36:39],s70 offen offset:0 sc1
s_mul_i32 s70,s47,5
s_lshr_b32 s70,s70,6
buffer_store_dword v5,v4,s[36:39],s70 offen offset:0 sc1
s_branch Lg3o_done_%=
Lg3o_slow1_%=:
s_cmp_lt_u32 s67,s59
s_cbranch_scc1 Lg3o_gs1_%=
Lg3o_slowb1_%=:
s_cmp_eq_u32 s54,1
s_cselect_b32 s68,s50,s49
s_lshr_b32 s68,s68,5
s_add_u32 m0,s78,8192
s_nop 0
buffer_load_dword v6,s[36:39],s68 offen offset:0 sc1 lds
s_mul_i32 s73,s48,7
s_lshr_b32 s73,s73,1
v_mul_f32 v99,v93,v92
v_mul_f32 v98,s77,v98
v_mul_f32 v98,v98,v97
v_fma_f32 v24,-v96,v16,v24
v_fma_f32 v25,-v96,v17,v25
v_fma_f32 v26,-v96,v18,v26
v_fma_f32 v27,-v96,v19,v27
v_mul_f32 v72,v24,v24
v_mul_f32 v73,v25,v25
v_mul_f32 v74,v26,v26
v_mul_f32 v75,v27,v27
v_fma_f32 v72,v98,v72,v97
v_fma_f32 v73,v98,v73,v97
v_fma_f32 v74,v98,v74,v97
v_fma_f32 v75,v98,v75,v97
v_mul_f32 v72,v24,v72
v_mul_f32 v73,v25,v73
v_mul_f32 v74,v26,v74
v_mul_f32 v75,v27,v75
v_exp_f32 v72,v72
v_exp_f32 v73,v73
v_exp_f32 v74,v74
v_exp_f32 v75,v75
v_fma_f32 v72,v72,v99,v99
v_fma_f32 v73,v73,v99,v99
v_fma_f32 v74,v74,v99,v99
v_fma_f32 v75,v75,v99,v99
v_rcp_f32 v72,v72
v_rcp_f32 v73,v73
v_rcp_f32 v74,v74
v_rcp_f32 v75,v75
v_mul_f32 v24,v24,v72
v_mul_f32 v25,v25,v73
v_mul_f32 v26,v26,v74
v_mul_f32 v27,v27,v75
v_cvt_pk_bf16_f32 v80,v24,v25
s_add_u32 m0,s78,8448
s_nop 0
buffer_load_dword v6,s[36:39],s68 offen offset:0 sc1 lds
v_cvt_pk_bf16_f32 v81,v26,v27
v_fma_f32 v28,-v96,v20,v28
v_fma_f32 v29,-v96,v21,v29
v_fma_f32 v30,-v96,v22,v30
v_fma_f32 v31,-v96,v23,v31
v_mul_f32 v76,v28,v28
v_mul_f32 v77,v29,v29
v_mul_f32 v78,v30,v30
v_mul_f32 v79,v31,v31
v_fma_f32 v76,v98,v76,v97
v_fma_f32 v77,v98,v77,v97
v_fma_f32 v78,v98,v78,v97
v_fma_f32 v79,v98,v79,v97
v_mul_f32 v76,v28,v76
v_mul_f32 v77,v29,v77
v_mul_f32 v78,v30,v78
v_mul_f32 v79,v31,v79
v_exp_f32 v76,v76
v_exp_f32 v77,v77
v_exp_f32 v78,v78
v_exp_f32 v79,v79
v_fma_f32 v76,v76,v99,v99
v_fma_f32 v77,v77,v99,v99
v_fma_f32 v78,v78,v99,v99
v_fma_f32 v79,v79,v99,v99
v_rcp_f32 v76,v76
v_rcp_f32 v77,v77
v_rcp_f32 v78,v78
v_rcp_f32 v79,v79
v_mul_f32 v28,v28,v76
v_mul_f32 v29,v29,v77
v_mul_f32 v30,v30,v78
v_mul_f32 v31,v31,v79
v_cvt_pk_bf16_f32 v82,v28,v29
v_cvt_pk_bf16_f32 v83,v30,v31
buffer_store_dwordx4 v[80:83],v3,s[36:39],s73 offen offset:0 sc1
s_cmp_ge_u32 s49,s59
s_cbranch_scc1 Lg3o_done1_%=
s_add_u32 m0,s78,16384
s_nop 0
buffer_load_dword v6,s[36:39],s68 offen offset:0 sc1 lds
s_waitcnt vmcnt(3)
s_barrier
ds_read_b32 v13,v7 offset:8192
s_mul_i32 s70,s47,5
s_lshr_b32 s70,s70,6
buffer_store_dword v5,v4,s[36:39],s70 offen offset:0 sc1
s_mov_b32 s47,s59
s_mov_b32 s69,18432
s_waitcnt lgkmcnt(0)
v_cmp_eq_u32 vcc,s60,v13
s_nop 0
s_cmp_eq_u32 vcc_lo,-1
s_cbranch_scc1 Lg3o_go1_%=
s_add_u32 m0,s78,16640
s_nop 0
buffer_load_dword v6,s[36:39],s68 offen offset:0 sc1 lds
s_waitcnt vmcnt(4)
s_barrier
ds_read_b32 v13,v7 offset:8448
s_mov_b32 s69,18688
s_waitcnt lgkmcnt(0)
v_cmp_eq_u32 vcc,s60,v13
s_nop 0
s_cmp_eq_u32 vcc_lo,-1
s_cbranch_scc1 Lg3o_go1_%=
s_add_u32 m0,s78,8192
s_nop 0
buffer_load_dword v6,s[36:39],s68 offen offset:0 sc1 lds
s_waitcnt vmcnt(3)
s_barrier
ds_read_b32 v13,v7 offset:16384
s_mul_i32 s70,s48,5
s_lshr_b32 s70,s70,6
buffer_store_dword v5,v4,s[36:39],s70 offen offset:0 sc1
s_mov_b32 s48,s59
s_mov_b32 s69,10240
s_waitcnt lgkmcnt(0)
v_cmp_eq_u32 vcc,s60,v13
s_nop 0
s_cmp_eq_u32 vcc_lo,-1
s_cbranch_scc1 Lg3o_go1_%=
s_add_u32 m0,s78,8448
s_nop 0
buffer_load_dword v6,s[36:39],s68 offen offset:0 sc1 lds
s_waitcnt vmcnt(3)
s_barrier
ds_read_b32 v13,v7 offset:16640
s_mov_b32 s69,10496
s_waitcnt lgkmcnt(0)
v_cmp_eq_u32 vcc,s60,v13
s_nop 0
s_cmp_eq_u32 vcc_lo,-1
s_cbranch_scc1 Lg3o_go1_%=
Lg3o_poll1_%=:
s_add_u32 m0,s78,16384
s_nop 0
buffer_load_dword v6,s[36:39],s68 offen offset:0 sc1 lds
s_waitcnt vmcnt(2)
s_barrier
ds_read_b32 v13,v7 offset:8192
s_mov_b32 s69,18432
s_waitcnt lgkmcnt(0)
v_cmp_eq_u32 vcc,s60,v13
s_nop 0
s_cmp_eq_u32 vcc_lo,-1
s_cbranch_scc1 Lg3o_go1_%=
s_sleep 4
s_add_u32 m0,s78,16640
s_nop 0
buffer_load_dword v6,s[36:39],s68 offen offset:0 sc1 lds
s_waitcnt vmcnt(2)
s_barrier
ds_read_b32 v13,v7 offset:8448
s_mov_b32 s69,18688
s_waitcnt lgkmcnt(0)
v_cmp_eq_u32 vcc,s60,v13
s_nop 0
s_cmp_eq_u32 vcc_lo,-1
s_cbranch_scc1 Lg3o_go1_%=
s_sleep 4
s_add_u32 m0,s78,8192
s_nop 0
buffer_load_dword v6,s[36:39],s68 offen offset:0 sc1 lds
s_waitcnt vmcnt(2)
s_barrier
ds_read_b32 v13,v7 offset:16384
s_mov_b32 s69,10240
s_waitcnt lgkmcnt(0)
v_cmp_eq_u32 vcc,s60,v13
s_nop 0
s_cmp_eq_u32 vcc_lo,-1
s_cbranch_scc1 Lg3o_go1_%=
s_sleep 4
s_add_u32 m0,s78,8448
s_nop 0
buffer_load_dword v6,s[36:39],s68 offen offset:0 sc1 lds
s_waitcnt vmcnt(2)
s_barrier
ds_read_b32 v13,v7 offset:16640)"
R"(
s_mov_b32 s69,10496
s_waitcnt lgkmcnt(0)
v_cmp_eq_u32 vcc,s60,v13
s_nop 0
s_cmp_eq_u32 vcc_lo,-1
s_cbranch_scc1 Lg3o_go1_%=
s_sleep 4
s_add_u32 s79,s79,1
s_cmp_lt_u32 s79,20000
s_cbranch_scc1 Lg3o_poll1_%=
Lg3o_go1_%=:
s_xor_b32 s62,s64,65536
s_mov_b32 m0,s62
s_nop 0
buffer_load_dwordx4 v0,s[36:39],s49 offen offset:0 sc1 lds
buffer_load_dwordx4 v0,s[36:39],s49 offen offset:1024 sc1 lds
buffer_load_dwordx4 v0,s[36:39],s49 offen offset:2048 sc1 lds
buffer_load_dwordx4 v0,s[36:39],s49 offen offset:3072 sc1 lds
s_add_u32 m0,s62,16384
s_nop 0
buffer_load_dwordx4 v1,s[36:39],s49 offen offset:0 sc1 lds
buffer_load_dwordx4 v1,s[36:39],s49 offen offset:1024 sc1 lds
buffer_load_dwordx4 v1,s[36:39],s49 offen offset:2048 sc1 lds
buffer_load_dwordx4 v1,s[36:39],s49 offen offset:3072 sc1 lds
s_lshr_b32 s72,s49,5
s_lshr_b32 s71,s62,2
s_add_u32 m0,s71,s88
s_nop 0
buffer_load_dword v9,s[36:39],s72 offen offset:0 sc1 lds
s_cmp_eq_u32 vcc_hi,-1
s_cselect_b32 s67,s50,s59
s_xor_b32 s62,s63,65536
s_cmp_ge_u32 s67,s59
s_cselect_b32 s68,s50,s51
s_lshr_b32 s68,s68,5
s_add_u32 m0,s78,256
s_nop 0
buffer_load_dword v6,s[36:39],s68 offen offset:0 sc1 lds
s_mov_b32 m0,s62
s_nop 0
buffer_load_dwordx4 v0,s[36:39],s67 offen offset:0 sc1 lds
buffer_load_dwordx4 v0,s[36:39],s67 offen offset:1024 sc1 lds
buffer_load_dwordx4 v0,s[36:39],s67 offen offset:2048 sc1 lds
buffer_load_dwordx4 v0,s[36:39],s67 offen offset:3072 sc1 lds
s_waitcnt vmcnt(10)
s_barrier
ds_read_b128 v[40:43],v2 offset:32768
ds_read_b128 v[44:47],v2 offset:33792
ds_read_b128 v[48:51],v2 offset:34816
ds_read_b128 v[52:55],v2 offset:35840
ds_read_b128 v[56:59],v2 offset:36864
ds_read_b128 v[60:63],v2 offset:37888
ds_read_b128 v[64:67],v2 offset:38912
s_mul_i32 s70,s47,5
s_lshr_b32 s70,s70,6
buffer_store_dword v5,v4,s[36:39],s70 offen offset:0 sc1
s_mov_b32 s47,s59
s_mul_i32 s70,s48,5
s_lshr_b32 s70,s70,6
buffer_store_dword v5,v4,s[36:39],s70 offen offset:0 sc1
s_mov_b32 s48,s59
s_mul_i32 s70,s59,5
s_lshr_b32 s70,s70,6
buffer_store_dword v5,v4,s[36:39],s70 offen offset:0 sc1
s_waitcnt lgkmcnt(3)
s_setpc_b64 s[92:93]
Lg3o_gs1_%=:
s_cmp_eq_u32 s54,1
s_cselect_b32 s68,s50,s49
s_lshr_b32 s68,s68,5
s_mul_i32 s73,s48,7
s_lshr_b32 s73,s73,1
s_add_u32 m0,s78,8192
s_nop 0
buffer_load_dword v6,s[36:39],s68 offen offset:0 sc1 lds
v_mul_f32 v99,v93,v92
v_mul_f32 v98,s77,v98
v_mul_f32 v98,v98,v97
v_fma_f32 v24,-v96,v16,v24
v_fma_f32 v25,-v96,v17,v25
v_fma_f32 v26,-v96,v18,v26
v_fma_f32 v27,-v96,v19,v27
v_mul_f32 v72,v24,v24
v_mul_f32 v73,v25,v25
v_mul_f32 v74,v26,v26
v_mul_f32 v75,v27,v27
v_fma_f32 v72,v98,v72,v97
s_add_u32 m0,s62,16384
s_nop 0
buffer_load_dwordx4 v1,s[36:39],s67 offen offset:0 sc1 lds
v_fma_f32 v73,v98,v73,v97
v_fma_f32 v74,v98,v74,v97
v_fma_f32 v75,v98,v75,v97
v_mul_f32 v72,v24,v72
v_mul_f32 v73,v25,v73
v_mul_f32 v74,v26,v74
v_mul_f32 v75,v27,v75
v_exp_f32 v72,v72
v_exp_f32 v73,v73
v_exp_f32 v74,v74
v_exp_f32 v75,v75
v_fma_f32 v72,v72,v99,v99
buffer_load_dwordx4 v1,s[36:39],s67 offen offset:1024 sc1 lds
v_fma_f32 v73,v73,v99,v99
v_fma_f32 v74,v74,v99,v99
v_fma_f32 v75,v75,v99,v99
v_rcp_f32 v72,v72
v_rcp_f32 v73,v73
v_rcp_f32 v74,v74
v_rcp_f32 v75,v75
v_mul_f32 v24,v24,v72
v_mul_f32 v25,v25,v73
v_mul_f32 v26,v26,v74
v_mul_f32 v27,v27,v75
v_cvt_pk_bf16_f32 v80,v24,v25
buffer_load_dwordx4 v1,s[36:39],s67 offen offset:2048 sc1 lds
v_cvt_pk_bf16_f32 v81,v26,v27
v_fma_f32 v28,-v96,v20,v28
v_fma_f32 v29,-v96,v21,v29
v_fma_f32 v30,-v96,v22,v30
v_fma_f32 v31,-v96,v23,v31
v_mul_f32 v76,v28,v28
v_mul_f32 v77,v29,v29
v_mul_f32 v78,v30,v30
v_mul_f32 v79,v31,v31
v_fma_f32 v76,v98,v76,v97
v_fma_f32 v77,v98,v77,v97
v_fma_f32 v78,v98,v78,v97
buffer_load_dwordx4 v1,s[36:39],s67 offen offset:3072 sc1 lds
v_fma_f32 v79,v98,v79,v97
v_mul_f32 v76,v28,v76
v_mul_f32 v77,v29,v77
v_mul_f32 v78,v30,v78
v_mul_f32 v79,v31,v79
v_exp_f32 v76,v76
v_exp_f32 v77,v77
v_exp_f32 v78,v78
v_exp_f32 v79,v79
v_fma_f32 v76,v76,v99,v99
v_fma_f32 v77,v77,v99,v99
v_fma_f32 v78,v78,v99,v99
s_lshr_b32 s72,s67,5
s_lshr_b32 s71,s62,2
s_add_u32 m0,s71,s88
s_nop 0
buffer_load_dword v9,s[36:39],s72 offen offset:0 sc1 lds
v_fma_f32 v79,v79,v99,v99
v_rcp_f32 v76,v76
v_rcp_f32 v77,v77
v_rcp_f32 v78,v78
v_rcp_f32 v79,v79
v_mul_f32 v28,v28,v76
v_mul_f32 v29,v29,v77
v_mul_f32 v30,v30,v78
v_mul_f32 v31,v31,v79
v_cvt_pk_bf16_f32 v82,v28,v29
v_cvt_pk_bf16_f32 v83,v30,v31
buffer_store_dwordx4 v[80:83],v3,s[36:39],s73 offen offset:0 sc1
s_waitcnt vmcnt(6)
s_barrier
ds_read_b32 v13,v7 offset:8192
ds_read_b128 v[40:43],v2 offset:32768
ds_read_b128 v[44:47],v2 offset:33792
ds_read_b128 v[48:51],v2 offset:34816
ds_read_b128 v[52:55],v2 offset:35840
ds_read_b128 v[56:59],v2 offset:36864
ds_read_b128 v[60:63],v2 offset:37888
ds_read_b128 v[64:67],v2 offset:38912
s_mul_i32 s70,s47,5
s_lshr_b32 s70,s70,6
buffer_store_dword v5,v4,s[36:39],s70 offen offset:0 sc1
s_waitcnt lgkmcnt(7)
v_cmp_eq_u32 vcc,s60,v13
s_nop 0
s_cmp_eq_u32 vcc_hi,-1
s_cselect_b32 s67,s50,s59
s_xor_b32 s62,s63,65536
s_cmp_ge_u32 s67,s59
s_cselect_b32 s68,s50,s51
s_lshr_b32 s68,s68,5
s_add_u32 m0,s78,256
s_nop 0
buffer_load_dword v6,s[36:39],s68 offen offset:0 sc1 lds
s_mov_b32 m0,s62
s_nop 0
buffer_load_dwordx4 v0,s[36:39],s67 offen offset:0 sc1 lds
buffer_load_dwordx4 v0,s[36:39],s67 offen offset:1024 sc1 lds
buffer_load_dwordx4 v0,s[36:39],s67 offen offset:2048 sc1 lds
buffer_load_dwordx4 v0,s[36:39],s67 offen offset:3072 sc1 lds
s_mov_b32 s47,s48
s_mov_b32 s48,s59
s_mul_i32 s70,s59,5
s_lshr_b32 s70,s70,6
buffer_store_dword v5,v4,s[36:39],s70 offen offset:0 sc1
s_waitcnt lgkmcnt(3)
s_setpc_b64 s[92:93]
Lg3o_done1_%=:
s_waitcnt vmcnt(0)
s_barrier
s_mul_i32 s70,s47,5)"
R"(
s_lshr_b32 s70,s70,6
buffer_store_dword v5,v4,s[36:39],s70 offen offset:0 sc1
s_mul_i32 s70,s48,5
s_lshr_b32 s70,s70,6
buffer_store_dword v5,v4,s[36:39],s70 offen offset:0 sc1
s_branch Lg3o_done_%=
Lg3o_done_%=:
s_waitcnt vmcnt(0) lgkmcnt(0)
s_barrier
s_endpgm
Lcode_g3o_e:)"
  :: "s"(ws), "s"(wave), "s"(sStep), "s"(sX1), "s"(sX2), "s"(sU0), "s"(sWw), "s"(sBuf), "s"(sEp), "v"(vSrc), "v"(vA), "v"(vOut), "v"(vFlag), "v"(vPollOff), "v"(vSnapRd), "v"(vX), "v"(vAuxS), "v"(vStW), "v"(vStR), "v"(vWd0), "v"(vWd1), "v"(vWr0), "v"(vWr1), "v"(vWr2), "s"(w3) : "v0", "v1", "v2", "v3", "v4", "v5", "v6", "v7", "v8", "v9", "v10", "v11", "v12", "v13", "v14", "v15", "v16", "v17", "v18", "v19", "v20", "v21", "v22", "v23", "v24", "v25", "v26", "v27", "v28", "v29", "v30", "v31", "v32", "v33", "v34", "v35", "v36", "v37", "v38", "v39", "v40", "v41", "v42", "v43", "v44", "v45", "v46", "v47", "v48", "v49", "v50", "v51", "v52", "v53", "v54", "v55", "v56", "v57", "v58", "v59", "v60", "v61", "v62", "v63", "v64", "v65", "v66", "v67", "v68", "v69", "v70", "v71", "v72", "v73", "v74", "v75", "v76", "v77", "v78", "v79", "v80", "v81", "v82", "v83", "v84", "v85", "v86", "v87", "v88", "v89", "v90", "v91", "v92", "v93", "v94", "v95", "v96", "v97", "v98", "v99", "v100", "v101", "v102", "v103", "v104", "v105", "v106", "v107", "v108", "v109", "v110", "v111", "v112", "v113", "v114", "v115", "v116", "v117", "v118", "v119", "v120", "v121", "v122", "v123", "v124", "v125", "v126", "v127", "v128", "v129", "v130", "v131", "v132", "v133", "v134", "v135", "v136", "v137", "v138", "v139", "v140", "v141", "v142", "v143", "v144", "v145", "v146", "v147", "v148", "v149", "v150", "v151", "v152", "v153", "v154", "v155", "v156", "v157", "v158", "v159", "v160", "v161", "v162", "v163", "v164", "v165", "v166", "v167", "v168", "v169", "v170", "v171", "v172", "v173", "v174", "v175", "v176", "v177", "v178", "v179", "v180", "v181", "v182", "v183", "v184", "v185", "v186", "v187", "v188", "v189", "v190", "v191", "v192", "v193", "v194", "v195", "v196", "v197", "v198", "v199", "v200", "v201", "v202", "v203", "v204", "v205", "v206", "v207", "v208", "v209", "v210", "v211", "v212", "v213", "v214", "v215", "v216", "v217", "v218", "v219", "v220", "v221", "v222", "v223", "v224", "v225", "v226", "v227", "v228", "v229", "v230", "v231", "v255", "a0", "a1", "a2", "a3", "a4", "a5", "a6", "a7", "a8", "a9", "a10", "a11", "a12", "a13", "a14", "a15", "a16", "a17", "a18", "a19", "a20", "a21", "a22", "a23", "a24", "a25", "a26", "a27", "a28", "a29", "a30", "a31", "a32", "a33", "a34", "a35", "a36", "a37", "a38", "a39", "a40", "a41", "a42", "a43", "a44", "a45", "a46", "a47", "a48", "a49", "a50", "a51", "a52", "a53", "a54", "a55", "a56", "a57", "a58", "a59", "a60", "a61", "a62", "a63", "a64", "a65", "a66", "a67", "a68", "a69", "a70", "a71", "a72", "a73", "a74", "a75", "a76", "a77", "a78", "a79", "a80", "a81", "a82", "a83", "a84", "a85", "a86", "a87", "a88", "a89", "a90", "a91", "a92", "a93", "a94", "a95", "a96", "a97", "a98", "a99", "a100", "a101", "a102", "a103", "a104", "a105", "a106", "a107", "a108", "a109", "a110", "a111", "a112", "a113", "a114", "a115", "a116", "a117", "a118", "a119", "a120", "a121", "a122", "a123", "a124", "a125", "a126", "a127", "a128", "a129", "a130", "a131", "a132", "a133", "a134", "a135", "a136", "a137", "a138", "a139", "a140", "a141", "a142", "a143", "a144", "a145", "a146", "a147", "a148", "a149", "a150", "a151", "a152", "a153", "a154", "a155", "a156", "a157", "a158", "a159", "a160", "a161", "a162", "a163", "a164", "a165", "a166", "a167", "a168", "a169", "a170", "a171", "a172", "a173", "a174", "a175", "a176", "a177", "a178", "a179", "a180", "a181", "a182", "a183", "a184", "a185", "a186", "a187", "a188", "a189", "a190", "a191", "a192", "a193", "a194", "a195", "a196", "a197", "a198", "a199", "a200", "a201", "a202", "a203", "a204", "a205", "a206", "a207", "a208", "a209", "a210", "a211", "a212", "a213", "a214", "a215", "a216", "a217", "a218", "a219", "a220", "a221", "a222", "a223", "a224", "a225", "a226", "a227", "a228", "a229", "a230", "a231", "a232", "a233", "a234", "a235", "a236", "a237", "a238", "a239", "a240", "a241", "a242", "a243", "a244", "a245", "a246", "a247", "a248", "a249", "a250", "a251", "a252", "a253", "a254", "a255", "s36", "s37", "s38", "s39", "s40", "s41", "s42", "s43", "s44", "s45", "s46", "s47", "s48", "s49", "s50", "s51", "s52", "s53", "s54", "s55", "s56", "s57", "s58", "s59", "s60", "s61", "s62", "s63", "s64", "s65", "s66", "s67", "s68", "s69", "s70", "s71", "s72", "s73", "s74", "s75", "s76", "s77", "s78", "s79", "s80", "s81", "s82", "s83", "s84", "s85", "s86", "s87", "s88", "s89", "s90", "s91", "s92", "s93", "s94", "s95", "s96", "s97", "s98", "s99", "vcc", "scc", "memory"); }
    __builtin_unreachable();
  } else if (kind == 4) {
    KARG(ws, 10) KARG(w4, 9)
    COMMON(kh == 1 ? 6 : 5, OFF_FG + ((14 * kh) / 3) * 128, OFF_FP + (kh * 4 + slice) * 128, 65536, 256, 2048, 5);
    const unsigned vSrc = OFF_G + kh * 28672 + wave * 4096 + vA, vSrc2 = OFF_G + kh * 28672 + 16384 + wave * 3072 + vA;
    const unsigned vOut = (kh == 0 ? OFF_P0 : kh == 1 ? OFF_P1 : kh == 2 ? OFF_P2 : OFF_P3) + (l15 >> 3) * 16384 + (slice * 8 + wave * 2 + (kh >> 1)) * 512 + lg * 128 + (l15 & 7) * 16;
    const unsigned vOut2 = vOut ^ 512;
    const unsigned vAuxS = ((kh & 1) ? OFF_XP : OFF_X1) + (slice * 8 + wave * 2 + 1 - (kh >> 1)) * 1024 + vA;
    const unsigned sWw = __builtin_amdgcn_readfirstlane(kh * 896 * 2048 + slice * 512 + wave * 128);
    asm volatile(R"(
Lcode_g4_b:
s_mov_b64 s[36:37],%0
s_mov_b32 s38,122727552
s_mov_b32 s39,0x00020000
s_mov_b32 s54,%1
s_mov_b32 s55,%2
s_mov_b32 s56,%3
s_mov_b32 s58,%4
s_mov_b32 s83,%5
s_mov_b32 s82,%6
s_mov_b32 s84,%7
s_mov_b32 s60,%8
v_mov_b32 v0,%9
v_mov_b32 v1,%10
v_mov_b32 v2,%11
v_mov_b32 v3,%12
v_mov_b32 v4,%13
v_mov_b32 v5,%14
v_mov_b32 v7,%15
v_mov_b32 v8,%16
v_mov_b32 v9,%17
v_mov_b32 v12,%18
v_mov_b32 v13,%19
v_mov_b32 v14,%20
v_mov_b32 v15,%21
v_mov_b32 v16,%22
s_mov_b64 s[40:41],%23
s_mov_b32 s42,7340032
s_mov_b32 s43,0x00020000
s_lshl_b32 s88,s54,12
s_mul_i32 s68,s54,9216
s_add_u32 s68,s68,122880
v_add_u32 v10,s68,v2
s_add_u32 m0,s88,0
s_add_u32 s61,s82,0
buffer_load_dwordx4 v12,s[40:43],s61 offen offset:0  lds
s_add_u32 m0,s88,1024
s_nop 0
buffer_load_dwordx4 v13,s[40:43],s61 offen offset:0  lds
s_add_u32 m0,s88,2048
s_add_u32 s61,s82,32768
buffer_load_dwordx4 v12,s[40:43],s61 offen offset:0  lds
s_add_u32 m0,s88,3072
s_nop 0
buffer_load_dwordx4 v13,s[40:43],s61 offen offset:0  lds
s_add_u32 m0,s88,16384
s_add_u32 s61,s82,65536
buffer_load_dwordx4 v12,s[40:43],s61 offen offset:0  lds
s_add_u32 m0,s88,17408
s_nop 0
buffer_load_dwordx4 v13,s[40:43],s61 offen offset:0  lds
s_add_u32 m0,s88,18432
s_add_u32 s61,s82,98304
buffer_load_dwordx4 v12,s[40:43],s61 offen offset:0  lds
s_add_u32 m0,s88,19456
s_nop 0
buffer_load_dwordx4 v13,s[40:43],s61 offen offset:0  lds
s_add_u32 m0,s88,32768
s_add_u32 s61,s82,131072
buffer_load_dwordx4 v12,s[40:43],s61 offen offset:0  lds
s_add_u32 m0,s88,33792
s_nop 0
buffer_load_dwordx4 v13,s[40:43],s61 offen offset:0  lds
s_add_u32 m0,s88,34816
s_add_u32 s61,s82,163840
buffer_load_dwordx4 v12,s[40:43],s61 offen offset:0  lds
s_add_u32 m0,s88,35840
s_nop 0
buffer_load_dwordx4 v13,s[40:43],s61 offen offset:0  lds
s_add_u32 m0,s88,49152
s_add_u32 s61,s82,196608
buffer_load_dwordx4 v12,s[40:43],s61 offen offset:0  lds
s_add_u32 m0,s88,50176
s_nop 0
buffer_load_dwordx4 v13,s[40:43],s61 offen offset:0  lds
s_add_u32 m0,s88,51200
s_add_u32 s61,s82,229376
buffer_load_dwordx4 v12,s[40:43],s61 offen offset:0  lds
s_add_u32 m0,s88,52224
s_nop 0
buffer_load_dwordx4 v13,s[40:43],s61 offen offset:0  lds
s_add_u32 m0,s88,65536
s_add_u32 s61,s82,262144
buffer_load_dwordx4 v12,s[40:43],s61 offen offset:0  lds
s_add_u32 m0,s88,66560
s_nop 0
buffer_load_dwordx4 v13,s[40:43],s61 offen offset:0  lds
s_add_u32 m0,s88,67584
s_add_u32 s61,s82,294912
buffer_load_dwordx4 v12,s[40:43],s61 offen offset:0  lds
s_add_u32 m0,s88,68608
s_nop 0
buffer_load_dwordx4 v13,s[40:43],s61 offen offset:0  lds
v_add_u32 v28,49152,v14
v_add_u32 v30,49152,v16
v_add_u32 v32,98304,v14
v_add_u32 v34,98304,v16
s_waitcnt vmcnt(16)
s_add_u32 m0,s88,81920
s_add_u32 s61,s82,327680
buffer_load_dwordx4 v12,s[40:43],s61 offen offset:0  lds
s_add_u32 m0,s88,82944
ds_read_b64_tr_b16 a[0:1],v14 offset:0
buffer_load_dwordx4 v13,s[40:43],s61 offen offset:0  lds
s_add_u32 m0,s88,83968
s_add_u32 s61,s82,360448
buffer_load_dwordx4 v12,s[40:43],s61 offen offset:0  lds
s_add_u32 m0,s88,84992
ds_read_b64_tr_b16 a[2:3],v14 offset:512
buffer_load_dwordx4 v13,s[40:43],s61 offen offset:0  lds
ds_read_b64_tr_b16 a[4:5],v14 offset:8
ds_read_b64_tr_b16 a[6:7],v14 offset:520
ds_read_b64_tr_b16 a[8:9],v16 offset:0
ds_read_b64_tr_b16 a[10:11],v16 offset:512
ds_read_b64_tr_b16 a[12:13],v16 offset:8
ds_read_b64_tr_b16 a[14:15],v16 offset:520
s_waitcnt vmcnt(16)
s_add_u32 m0,s88,98304
s_add_u32 s61,s82,393216
buffer_load_dwordx4 v12,s[40:43],s61 offen offset:0  lds
s_add_u32 m0,s88,99328
ds_read_b64_tr_b16 a[16:17],v14 offset:16384
buffer_load_dwordx4 v13,s[40:43],s61 offen offset:0  lds
s_add_u32 m0,s88,100352
s_add_u32 s61,s82,425984
buffer_load_dwordx4 v12,s[40:43],s61 offen offset:0  lds
s_add_u32 m0,s88,101376
ds_read_b64_tr_b16 a[18:19],v14 offset:16896
buffer_load_dwordx4 v13,s[40:43],s61 offen offset:0  lds
ds_read_b64_tr_b16 a[20:21],v14 offset:16392
ds_read_b64_tr_b16 a[22:23],v14 offset:16904
ds_read_b64_tr_b16 a[24:25],v16 offset:16384
ds_read_b64_tr_b16 a[26:27],v16 offset:16896
ds_read_b64_tr_b16 a[28:29],v16 offset:16392
ds_read_b64_tr_b16 a[30:31],v16 offset:16904
s_waitcnt lgkmcnt(8)
s_waitcnt vmcnt(16)
s_add_u32 m0,s88,0
s_add_u32 s61,s82,458752
buffer_load_dwordx4 v12,s[40:43],s61 offen offset:0  lds
s_add_u32 m0,s88,1024
ds_read_b64_tr_b16 a[32:33],v14 offset:32768
buffer_load_dwordx4 v13,s[40:43],s61 offen offset:0  lds
s_add_u32 m0,s88,2048
s_add_u32 s61,s82,491520
buffer_load_dwordx4 v12,s[40:43],s61 offen offset:0  lds
s_add_u32 m0,s88,3072
ds_read_b64_tr_b16 a[34:35],v14 offset:33280
buffer_load_dwordx4 v13,s[40:43],s61 offen offset:0  lds
ds_read_b64_tr_b16 a[36:37],v14 offset:32776
ds_read_b64_tr_b16 a[38:39],v14 offset:33288
ds_read_b64_tr_b16 a[40:41],v16 offset:32768
ds_read_b64_tr_b16 a[42:43],v16 offset:33280
ds_read_b64_tr_b16 a[44:45],v16 offset:32776
ds_read_b64_tr_b16 a[46:47],v16 offset:33288
s_waitcnt lgkmcnt(8)
s_waitcnt vmcnt(16)
s_add_u32 m0,s88,16384
s_add_u32 s61,s82,524288
buffer_load_dwordx4 v12,s[40:43],s61 offen offset:0  lds
s_add_u32 m0,s88,17408
ds_read_b64_tr_b16 a[48:49],v28 offset:0
buffer_load_dwordx4 v13,s[40:43],s61 offen offset:0  lds
s_add_u32 m0,s88,18432
s_add_u32 s61,s82,557056
buffer_load_dwordx4 v12,s[40:43],s61 offen offset:0  lds
s_add_u32 m0,s88,19456
ds_read_b64_tr_b16 a[50:51],v28 offset:512
buffer_load_dwordx4 v13,s[40:43],s61 offen offset:0  lds
ds_read_b64_tr_b16 a[52:53],v28 offset:8
ds_read_b64_tr_b16 a[54:55],v28 offset:520
ds_read_b64_tr_b16 a[56:57],v30 offset:0
ds_read_b64_tr_b16 a[58:59],v30 offset:512
ds_read_b64_tr_b16 a[60:61],v30 offset:8
ds_read_b64_tr_b16 a[62:63],v30 offset:520
s_waitcnt lgkmcnt(8)
s_waitcnt vmcnt(16)
s_add_u32 m0,s88,32768
s_add_u32 s61,s82,589824
buffer_load_dwordx4 v12,s[40:43],s61 offen offset:0  lds
s_add_u32 m0,s88,33792
ds_read_b64_tr_b16 a[64:65],v28 offset:16384
buffer_load_dwordx4 v13,s[40:43],s61 offen offset:0  lds
s_add_u32 m0,s88,34816
s_add_u32 s61,s82,622592
buffer_load_dwordx4 v12,s[40:43],s61 offen offset:0  lds
s_add_u32 m0,s88,35840
ds_read_b64_tr_b16 a[66:67],v28 offset:16896
buffer_load_dwordx4 v13,s[40:43],s61 offen offset:0  lds
ds_read_b64_tr_b16 a[68:69],v28 offset:16392
ds_read_b64_tr_b16 a[70:71],v28 offset:16904
ds_read_b64_tr_b16 a[72:73],v30 offset:16384
ds_read_b64_tr_b16 a[74:75],v30 offset:16896
ds_read_b64_tr_b16 a[76:77],v30 offset:16392
ds_read_b64_tr_b16 a[78:79],v30 offset:16904
s_waitcnt lgkmcnt(8)
s_waitcnt vmcnt(16)
s_add_u32 m0,s88,49152
s_add_u32 s61,s82,655360
buffer_load_dwordx4 v12,s[40:43],s61 offen offset:0  lds)"
R"(
s_add_u32 m0,s88,50176
ds_read_b64_tr_b16 a[80:81],v28 offset:32768
buffer_load_dwordx4 v13,s[40:43],s61 offen offset:0  lds
s_add_u32 m0,s88,51200
s_add_u32 s61,s82,688128
buffer_load_dwordx4 v12,s[40:43],s61 offen offset:0  lds
s_add_u32 m0,s88,52224
ds_read_b64_tr_b16 a[82:83],v28 offset:33280
buffer_load_dwordx4 v13,s[40:43],s61 offen offset:0  lds
ds_read_b64_tr_b16 a[84:85],v28 offset:32776
ds_read_b64_tr_b16 a[86:87],v28 offset:33288
ds_read_b64_tr_b16 a[88:89],v30 offset:32768
ds_read_b64_tr_b16 a[90:91],v30 offset:33280
ds_read_b64_tr_b16 a[92:93],v30 offset:32776
ds_read_b64_tr_b16 a[94:95],v30 offset:33288
s_waitcnt lgkmcnt(8)
s_waitcnt vmcnt(16)
s_add_u32 m0,s88,65536
s_add_u32 s61,s82,720896
buffer_load_dwordx4 v12,s[40:43],s61 offen offset:0  lds
s_add_u32 m0,s88,66560
ds_read_b64_tr_b16 a[96:97],v32 offset:0
buffer_load_dwordx4 v13,s[40:43],s61 offen offset:0  lds
s_add_u32 m0,s88,67584
s_add_u32 s61,s82,753664
buffer_load_dwordx4 v12,s[40:43],s61 offen offset:0  lds
s_add_u32 m0,s88,68608
ds_read_b64_tr_b16 a[98:99],v32 offset:512
buffer_load_dwordx4 v13,s[40:43],s61 offen offset:0  lds
ds_read_b64_tr_b16 a[100:101],v32 offset:8
ds_read_b64_tr_b16 a[102:103],v32 offset:520
ds_read_b64_tr_b16 a[104:105],v34 offset:0
ds_read_b64_tr_b16 a[106:107],v34 offset:512
ds_read_b64_tr_b16 a[108:109],v34 offset:8
ds_read_b64_tr_b16 a[110:111],v34 offset:520
s_waitcnt lgkmcnt(8)
s_waitcnt vmcnt(16)
s_add_u32 m0,s88,81920
s_add_u32 s61,s82,786432
buffer_load_dwordx4 v12,s[40:43],s61 offen offset:0  lds
s_add_u32 m0,s88,82944
ds_read_b64_tr_b16 a[112:113],v14 offset:0
buffer_load_dwordx4 v13,s[40:43],s61 offen offset:0  lds
s_add_u32 m0,s88,83968
s_add_u32 s61,s82,819200
buffer_load_dwordx4 v12,s[40:43],s61 offen offset:0  lds
s_add_u32 m0,s88,84992
ds_read_b64_tr_b16 a[114:115],v14 offset:512
buffer_load_dwordx4 v13,s[40:43],s61 offen offset:0  lds
ds_read_b64_tr_b16 a[116:117],v14 offset:8
ds_read_b64_tr_b16 a[118:119],v14 offset:520
ds_read_b64_tr_b16 a[120:121],v16 offset:0
ds_read_b64_tr_b16 a[122:123],v16 offset:512
ds_read_b64_tr_b16 a[124:125],v16 offset:8
ds_read_b64_tr_b16 a[126:127],v16 offset:520
s_waitcnt lgkmcnt(8)
s_waitcnt vmcnt(16)
s_add_u32 m0,s88,98304
s_add_u32 s61,s82,851968
buffer_load_dwordx4 v12,s[40:43],s61 offen offset:0  lds
s_add_u32 m0,s88,99328
ds_read_b64_tr_b16 a[128:129],v14 offset:16384
buffer_load_dwordx4 v13,s[40:43],s61 offen offset:0  lds
s_add_u32 m0,s88,100352
s_add_u32 s61,s82,884736
buffer_load_dwordx4 v12,s[40:43],s61 offen offset:0  lds
s_add_u32 m0,s88,101376
ds_read_b64_tr_b16 a[130:131],v14 offset:16896
buffer_load_dwordx4 v13,s[40:43],s61 offen offset:0  lds
ds_read_b64_tr_b16 a[132:133],v14 offset:16392
ds_read_b64_tr_b16 a[134:135],v14 offset:16904
ds_read_b64_tr_b16 a[136:137],v16 offset:16384
ds_read_b64_tr_b16 a[138:139],v16 offset:16896
ds_read_b64_tr_b16 a[140:141],v16 offset:16392
ds_read_b64_tr_b16 a[142:143],v16 offset:16904
s_waitcnt lgkmcnt(8)
s_waitcnt vmcnt(16)
s_add_u32 m0,s88,0
s_add_u32 s61,s82,917504
buffer_load_dwordx4 v12,s[40:43],s61 offen offset:0  lds
s_add_u32 m0,s88,1024
ds_read_b64_tr_b16 a[144:145],v14 offset:32768
buffer_load_dwordx4 v13,s[40:43],s61 offen offset:0  lds
s_add_u32 m0,s88,2048
s_add_u32 s61,s82,950272
buffer_load_dwordx4 v12,s[40:43],s61 offen offset:0  lds
s_add_u32 m0,s88,3072
ds_read_b64_tr_b16 a[146:147],v14 offset:33280
buffer_load_dwordx4 v13,s[40:43],s61 offen offset:0  lds
ds_read_b64_tr_b16 a[148:149],v14 offset:32776
ds_read_b64_tr_b16 a[150:151],v14 offset:33288
ds_read_b64_tr_b16 a[152:153],v16 offset:32768
ds_read_b64_tr_b16 a[154:155],v16 offset:33280
ds_read_b64_tr_b16 a[156:157],v16 offset:32776
ds_read_b64_tr_b16 a[158:159],v16 offset:33288
s_waitcnt lgkmcnt(8)
s_waitcnt vmcnt(16)
s_add_u32 m0,s88,16384
s_add_u32 s61,s82,983040
buffer_load_dwordx4 v12,s[40:43],s61 offen offset:0  lds
s_add_u32 m0,s88,17408
ds_read_b64_tr_b16 a[160:161],v28 offset:0
buffer_load_dwordx4 v13,s[40:43],s61 offen offset:0  lds
s_add_u32 m0,s88,18432
s_add_u32 s61,s82,1015808
buffer_load_dwordx4 v12,s[40:43],s61 offen offset:0  lds
s_add_u32 m0,s88,19456
ds_read_b64_tr_b16 a[162:163],v28 offset:512
buffer_load_dwordx4 v13,s[40:43],s61 offen offset:0  lds
ds_read_b64_tr_b16 a[164:165],v28 offset:8
ds_read_b64_tr_b16 a[166:167],v28 offset:520
ds_read_b64_tr_b16 a[168:169],v30 offset:0
ds_read_b64_tr_b16 a[170:171],v30 offset:512
ds_read_b64_tr_b16 a[172:173],v30 offset:8
ds_read_b64_tr_b16 a[174:175],v30 offset:520
s_waitcnt lgkmcnt(8)
s_waitcnt vmcnt(16)
s_add_u32 m0,s88,32768
s_add_u32 s61,s82,1048576
buffer_load_dwordx4 v12,s[40:43],s61 offen offset:0  lds
s_add_u32 m0,s88,33792
ds_read_b64_tr_b16 a[176:177],v28 offset:16384
buffer_load_dwordx4 v13,s[40:43],s61 offen offset:0  lds
s_add_u32 m0,s88,34816
s_add_u32 s61,s82,1081344
buffer_load_dwordx4 v12,s[40:43],s61 offen offset:0  lds
s_add_u32 m0,s88,35840
ds_read_b64_tr_b16 a[178:179],v28 offset:16896
buffer_load_dwordx4 v13,s[40:43],s61 offen offset:0  lds
ds_read_b64_tr_b16 a[180:181],v28 offset:16392
ds_read_b64_tr_b16 a[182:183],v28 offset:16904
ds_read_b64_tr_b16 a[184:185],v30 offset:16384
ds_read_b64_tr_b16 a[186:187],v30 offset:16896
ds_read_b64_tr_b16 a[188:189],v30 offset:16392
ds_read_b64_tr_b16 a[190:191],v30 offset:16904
s_waitcnt lgkmcnt(8)
s_waitcnt vmcnt(16)
s_add_u32 m0,s88,49152
s_add_u32 s61,s82,1114112
buffer_load_dwordx4 v12,s[40:43],s61 offen offset:0  lds
s_add_u32 m0,s88,50176
ds_read_b64_tr_b16 a[192:193],v28 offset:32768
buffer_load_dwordx4 v13,s[40:43],s61 offen offset:0  lds
s_add_u32 m0,s88,51200
s_add_u32 s61,s82,1146880
buffer_load_dwordx4 v12,s[40:43],s61 offen offset:0  lds
s_add_u32 m0,s88,52224
ds_read_b64_tr_b16 a[194:195],v28 offset:33280
buffer_load_dwordx4 v13,s[40:43],s61 offen offset:0  lds
ds_read_b64_tr_b16 a[196:197],v28 offset:32776
ds_read_b64_tr_b16 a[198:199],v28 offset:33288
ds_read_b64_tr_b16 a[200:201],v30 offset:32768
ds_read_b64_tr_b16 a[202:203],v30 offset:33280
ds_read_b64_tr_b16 a[204:205],v30 offset:32776
ds_read_b64_tr_b16 a[206:207],v30 offset:33288
s_waitcnt lgkmcnt(8)
s_waitcnt vmcnt(16)
s_add_u32 m0,s88,65536
s_add_u32 s61,s82,1179648
buffer_load_dwordx4 v12,s[40:43],s61 offen offset:0  lds
s_add_u32 m0,s88,66560
ds_read_b64_tr_b16 a[208:209],v32 offset:0
buffer_load_dwordx4 v13,s[40:43],s61 offen offset:0  lds
s_add_u32 m0,s88,67584
s_add_u32 s61,s82,1212416
buffer_load_dwordx4 v12,s[40:43],s61 offen offset:0  lds
s_add_u32 m0,s88,68608
ds_read_b64_tr_b16 a[210:211],v32 offset:512
buffer_load_dwordx4 v13,s[40:43],s61 offen offset:0  lds
ds_read_b64_tr_b16 a[212:213],v32 offset:8
ds_read_b64_tr_b16 a[214:215],v32 offset:520
ds_read_b64_tr_b16 a[216:217],v34 offset:0
ds_read_b64_tr_b16 a[218:219],v34 offset:512
ds_read_b64_tr_b16 a[220:221],v34 offset:8
ds_read_b64_tr_b16 a[222:223],v34 offset:520
s_waitcnt lgkmcnt(8)
s_waitcnt vmcnt(16)
s_add_u32 m0,s88,81920
s_add_u32 s61,s82,1245184
buffer_load_dwordx4 v12,s[40:43],s61 offen offset:0  lds
s_add_u32 m0,s88,82944
ds_read_b64_tr_b16 a[224:225],v14 offset:0
buffer_load_dwordx4 v13,s[40:43],s61 offen offset:0  lds
s_add_u32 m0,s88,83968
s_add_u32 s61,s82,1277952
buffer_load_dwordx4 v12,s[40:43],s61 offen offset:0  lds
s_add_u32 m0,s88,84992
ds_read_b64_tr_b16 a[226:227],v14 offset:512
buffer_load_dwordx4 v13,s[40:43],s61 offen offset:0  lds
ds_read_b64_tr_b16 a[228:229],v14 offset:8
ds_read_b64_tr_b16 a[230:231],v14 offset:520
ds_read_b64_tr_b16 a[232:233],v16 offset:0
ds_read_b64_tr_b16 a[234:235],v16 offset:512
ds_read_b64_tr_b16 a[236:237],v16 offset:8
ds_read_b64_tr_b16 a[238:239],v16 offset:520
s_waitcnt lgkmcnt(8)
s_waitcnt vmcnt(16)
s_add_u32 m0,s88,98304
s_add_u32 s61,s82,1310720
buffer_load_dwordx4 v12,s[40:43],s61 offen offset:0  lds)"
R"(
s_add_u32 m0,s88,99328
ds_read_b64_tr_b16 a[240:241],v14 offset:16384
buffer_load_dwordx4 v13,s[40:43],s61 offen offset:0  lds
s_add_u32 m0,s88,100352
s_add_u32 s61,s82,1343488
buffer_load_dwordx4 v12,s[40:43],s61 offen offset:0  lds
s_add_u32 m0,s88,101376
ds_read_b64_tr_b16 a[242:243],v14 offset:16896
buffer_load_dwordx4 v13,s[40:43],s61 offen offset:0  lds
ds_read_b64_tr_b16 a[244:245],v14 offset:16392
ds_read_b64_tr_b16 a[246:247],v14 offset:16904
ds_read_b64_tr_b16 a[248:249],v16 offset:16384
ds_read_b64_tr_b16 a[250:251],v16 offset:16896
ds_read_b64_tr_b16 a[252:253],v16 offset:16392
ds_read_b64_tr_b16 a[254:255],v16 offset:16904
s_waitcnt lgkmcnt(8)
s_waitcnt vmcnt(16)
s_add_u32 m0,s88,0
s_add_u32 s61,s82,1376256
buffer_load_dwordx4 v12,s[40:43],s61 offen offset:0  lds
s_add_u32 m0,s88,1024
ds_read_b64_tr_b16 v[100:101],v14 offset:32768
buffer_load_dwordx4 v13,s[40:43],s61 offen offset:0  lds
s_add_u32 m0,s88,2048
s_add_u32 s61,s82,1409024
buffer_load_dwordx4 v12,s[40:43],s61 offen offset:0  lds
s_add_u32 m0,s88,3072
ds_read_b64_tr_b16 v[102:103],v14 offset:33280
buffer_load_dwordx4 v13,s[40:43],s61 offen offset:0  lds
ds_read_b64_tr_b16 v[104:105],v14 offset:32776
ds_read_b64_tr_b16 v[106:107],v14 offset:33288
ds_read_b64_tr_b16 v[108:109],v16 offset:32768
ds_read_b64_tr_b16 v[110:111],v16 offset:33280
ds_read_b64_tr_b16 v[112:113],v16 offset:32776
ds_read_b64_tr_b16 v[114:115],v16 offset:33288
s_waitcnt lgkmcnt(8)
s_waitcnt vmcnt(16)
s_add_u32 m0,s88,16384
s_add_u32 s61,s82,1441792
buffer_load_dwordx4 v12,s[40:43],s61 offen offset:0  lds
s_add_u32 m0,s88,17408
ds_read_b64_tr_b16 v[116:117],v28 offset:0
buffer_load_dwordx4 v13,s[40:43],s61 offen offset:0  lds
s_add_u32 m0,s88,18432
s_add_u32 s61,s82,1474560
buffer_load_dwordx4 v12,s[40:43],s61 offen offset:0  lds
s_add_u32 m0,s88,19456
ds_read_b64_tr_b16 v[118:119],v28 offset:512
buffer_load_dwordx4 v13,s[40:43],s61 offen offset:0  lds
ds_read_b64_tr_b16 v[120:121],v28 offset:8
ds_read_b64_tr_b16 v[122:123],v28 offset:520
ds_read_b64_tr_b16 v[124:125],v30 offset:0
ds_read_b64_tr_b16 v[126:127],v30 offset:512
ds_read_b64_tr_b16 v[128:129],v30 offset:8
ds_read_b64_tr_b16 v[130:131],v30 offset:520
s_waitcnt lgkmcnt(8)
s_waitcnt vmcnt(16)
s_add_u32 m0,s88,32768
s_add_u32 s61,s82,1507328
buffer_load_dwordx4 v12,s[40:43],s61 offen offset:0  lds
s_add_u32 m0,s88,33792
ds_read_b64_tr_b16 v[132:133],v28 offset:16384
buffer_load_dwordx4 v13,s[40:43],s61 offen offset:0  lds
s_add_u32 m0,s88,34816
s_add_u32 s61,s82,1540096
buffer_load_dwordx4 v12,s[40:43],s61 offen offset:0  lds
s_add_u32 m0,s88,35840
ds_read_b64_tr_b16 v[134:135],v28 offset:16896
buffer_load_dwordx4 v13,s[40:43],s61 offen offset:0  lds
ds_read_b64_tr_b16 v[136:137],v28 offset:16392
ds_read_b64_tr_b16 v[138:139],v28 offset:16904
ds_read_b64_tr_b16 v[140:141],v30 offset:16384
ds_read_b64_tr_b16 v[142:143],v30 offset:16896
ds_read_b64_tr_b16 v[144:145],v30 offset:16392
ds_read_b64_tr_b16 v[146:147],v30 offset:16904
s_waitcnt lgkmcnt(8)
s_waitcnt vmcnt(16)
s_add_u32 m0,s88,49152
s_add_u32 s61,s82,1572864
buffer_load_dwordx4 v12,s[40:43],s61 offen offset:0  lds
s_add_u32 m0,s88,50176
ds_read_b64_tr_b16 v[148:149],v28 offset:32768
buffer_load_dwordx4 v13,s[40:43],s61 offen offset:0  lds
s_add_u32 m0,s88,51200
s_add_u32 s61,s82,1605632
buffer_load_dwordx4 v12,s[40:43],s61 offen offset:0  lds
s_add_u32 m0,s88,52224
ds_read_b64_tr_b16 v[150:151],v28 offset:33280
buffer_load_dwordx4 v13,s[40:43],s61 offen offset:0  lds
ds_read_b64_tr_b16 v[152:153],v28 offset:32776
ds_read_b64_tr_b16 v[154:155],v28 offset:33288
ds_read_b64_tr_b16 v[156:157],v30 offset:32768
ds_read_b64_tr_b16 v[158:159],v30 offset:33280
ds_read_b64_tr_b16 v[52:53],v30 offset:32776
ds_read_b64_tr_b16 v[54:55],v30 offset:33288
s_waitcnt lgkmcnt(8)
s_waitcnt vmcnt(16)
s_add_u32 m0,s88,65536
s_add_u32 s61,s82,1638400
buffer_load_dwordx4 v12,s[40:43],s61 offen offset:0  lds
s_add_u32 m0,s88,66560
ds_read_b64_tr_b16 v[160:161],v32 offset:0
buffer_load_dwordx4 v13,s[40:43],s61 offen offset:0  lds
s_add_u32 m0,s88,67584
s_add_u32 s61,s82,1671168
buffer_load_dwordx4 v12,s[40:43],s61 offen offset:0  lds
s_add_u32 m0,s88,68608
ds_read_b64_tr_b16 v[162:163],v32 offset:512
buffer_load_dwordx4 v13,s[40:43],s61 offen offset:0  lds
ds_read_b64_tr_b16 v[164:165],v32 offset:8
ds_read_b64_tr_b16 v[166:167],v32 offset:520
ds_read_b64_tr_b16 v[168:169],v34 offset:0
ds_read_b64_tr_b16 v[170:171],v34 offset:512
ds_read_b64_tr_b16 v[44:45],v34 offset:8
ds_read_b64_tr_b16 v[46:47],v34 offset:520
s_waitcnt lgkmcnt(8)
ds_write_b128 v10,v[52:55] offset:0
s_waitcnt vmcnt(16)
s_add_u32 m0,s88,81920
s_add_u32 s61,s82,1703936
buffer_load_dwordx4 v12,s[40:43],s61 offen offset:0  lds
s_add_u32 m0,s88,82944
ds_read_b64_tr_b16 v[172:173],v14 offset:0
buffer_load_dwordx4 v13,s[40:43],s61 offen offset:0  lds
s_add_u32 m0,s88,83968
s_add_u32 s61,s82,1736704
buffer_load_dwordx4 v12,s[40:43],s61 offen offset:0  lds
s_add_u32 m0,s88,84992
ds_read_b64_tr_b16 v[174:175],v14 offset:512
buffer_load_dwordx4 v13,s[40:43],s61 offen offset:0  lds
ds_read_b64_tr_b16 v[176:177],v14 offset:8
ds_read_b64_tr_b16 v[178:179],v14 offset:520
ds_read_b64_tr_b16 v[180:181],v16 offset:0
ds_read_b64_tr_b16 v[182:183],v16 offset:512
ds_read_b64_tr_b16 v[52:53],v16 offset:8
ds_read_b64_tr_b16 v[54:55],v16 offset:520
s_waitcnt lgkmcnt(8)
ds_write_b128 v10,v[44:47] offset:1024
s_waitcnt vmcnt(16)
s_add_u32 m0,s88,98304
s_add_u32 s61,s82,1769472
buffer_load_dwordx4 v12,s[40:43],s61 offen offset:0  lds
s_add_u32 m0,s88,99328
ds_read_b64_tr_b16 v[184:185],v14 offset:16384
buffer_load_dwordx4 v13,s[40:43],s61 offen offset:0  lds
s_add_u32 m0,s88,100352
s_add_u32 s61,s82,1802240
buffer_load_dwordx4 v12,s[40:43],s61 offen offset:0  lds
s_add_u32 m0,s88,101376
ds_read_b64_tr_b16 v[186:187],v14 offset:16896
buffer_load_dwordx4 v13,s[40:43],s61 offen offset:0  lds
ds_read_b64_tr_b16 v[188:189],v14 offset:16392
ds_read_b64_tr_b16 v[190:191],v14 offset:16904
ds_read_b64_tr_b16 v[192:193],v16 offset:16384
ds_read_b64_tr_b16 v[194:195],v16 offset:16896
ds_read_b64_tr_b16 v[44:45],v16 offset:16392
ds_read_b64_tr_b16 v[46:47],v16 offset:16904
s_waitcnt lgkmcnt(8)
ds_write_b128 v10,v[52:55] offset:2048
s_waitcnt vmcnt(16)
ds_read_b64_tr_b16 v[196:197],v14 offset:32768
ds_read_b64_tr_b16 v[198:199],v14 offset:33280
ds_read_b64_tr_b16 v[200:201],v14 offset:32776
ds_read_b64_tr_b16 v[202:203],v14 offset:33288
ds_read_b64_tr_b16 v[204:205],v16 offset:32768
ds_read_b64_tr_b16 v[206:207],v16 offset:33280
ds_read_b64_tr_b16 v[52:53],v16 offset:32776
ds_read_b64_tr_b16 v[54:55],v16 offset:33288
s_getpc_b64 s[46:47]
Lg4_cp_%=:
s_add_u32 s46,s46,Lg4_init_%=-Lg4_cp_%=
s_addc_u32 s47,s47,0
s_lshl_b32 s68,s54,13
v_lshl_add_u32 v76,v2,3,s68
v_min_u32 v77,Lg4_done_%=-Lg4_init_%=,v76
global_load_dword v80,v77,s[46:47]
s_waitcnt lgkmcnt(8)
ds_write_b128 v10,v[44:47] offset:3072
s_waitcnt vmcnt(13)
ds_read_b64_tr_b16 v[208:209],v28 offset:0
ds_read_b64_tr_b16 v[210:211],v28 offset:512
ds_read_b64_tr_b16 v[212:213],v28 offset:8
ds_read_b64_tr_b16 v[214:215],v28 offset:520
ds_read_b64_tr_b16 v[216:217],v30 offset:0
ds_read_b64_tr_b16 v[218:219],v30 offset:512
ds_read_b64_tr_b16 v[44:45],v30 offset:8
ds_read_b64_tr_b16 v[46:47],v30 offset:520
s_waitcnt lgkmcnt(8)
ds_write_b128 v10,v[52:55] offset:4096
s_waitcnt vmcnt(9)
ds_read_b64_tr_b16 v[220:221],v28 offset:16384
ds_read_b64_tr_b16 v[222:223],v28 offset:16896
ds_read_b64_tr_b16 v[224:225],v28 offset:16392
ds_read_b64_tr_b16 v[226:227],v28 offset:16904
ds_read_b64_tr_b16 v[228:229],v30 offset:16384
ds_read_b64_tr_b16 v[230:231],v30 offset:16896
ds_read_b64_tr_b16 v[52:53],v30 offset:16392
ds_read_b64_tr_b16 v[54:55],v30 offset:16904
s_waitcnt lgkmcnt(8)
ds_write_b128 v10,v[44:47] offset:5120)"
R"(
s_waitcnt vmcnt(5)
ds_read_b64_tr_b16 v[232:233],v28 offset:32768
ds_read_b64_tr_b16 v[234:235],v28 offset:33280
ds_read_b64_tr_b16 v[236:237],v28 offset:32776
ds_read_b64_tr_b16 v[238:239],v28 offset:33288
ds_read_b64_tr_b16 v[240:241],v30 offset:32768
ds_read_b64_tr_b16 v[242:243],v30 offset:33280
ds_read_b64_tr_b16 v[44:45],v30 offset:32776
ds_read_b64_tr_b16 v[46:47],v30 offset:33288
s_waitcnt lgkmcnt(8)
ds_write_b128 v10,v[52:55] offset:6144
s_waitcnt vmcnt(1)
ds_read_b64_tr_b16 v[244:245],v32 offset:0
ds_read_b64_tr_b16 v[246:247],v32 offset:512
ds_read_b64_tr_b16 v[248:249],v32 offset:8
ds_read_b64_tr_b16 v[250:251],v32 offset:520
ds_read_b64_tr_b16 v[252:253],v34 offset:0
ds_read_b64_tr_b16 v[254:255],v34 offset:512
ds_read_b64_tr_b16 v[52:53],v34 offset:8
ds_read_b64_tr_b16 v[54:55],v34 offset:520
s_waitcnt lgkmcnt(8)
ds_write_b128 v10,v[44:47] offset:7168
s_waitcnt lgkmcnt(0)
ds_write_b128 v10,v[52:55] offset:8192
s_waitcnt lgkmcnt(0)
s_barrier
Lg4_init_%=:
v_mov_b32 v6,s60
s_mov_b32 s59,536870912
s_add_u32 s57,s56,131072
s_mov_b32 s79,0
s_mov_b32 s75,0x3a800000
s_mov_b32 s76,0xc0135761
s_mov_b32 s77,0x3d372713
s_mov_b32 s44,s59
s_mov_b32 s45,s59
s_mov_b32 s46,s59
s_mov_b32 s47,s59
s_mov_b32 s48,s83
s_add_u32 s49,s48,s55
s_cmp_eq_u32 s49,s56
s_cselect_b32 s49,s57,s49
s_cmp_eq_u32 s49,s58
s_cselect_b32 s49,s59,s49
s_cmp_ge_u32 s49,6520832
s_cselect_b32 s55,196608,s55
s_add_u32 s50,s49,s55
s_cmp_eq_u32 s50,s56
s_cselect_b32 s50,s57,s50
s_cmp_eq_u32 s50,s58
s_cselect_b32 s50,s59,s50
s_cmp_ge_u32 s50,6520832
s_cselect_b32 s55,196608,s55
s_add_u32 s51,s50,s55
s_cmp_eq_u32 s51,s56
s_cselect_b32 s51,s57,s51
s_cmp_eq_u32 s51,s58
s_cselect_b32 s51,s59,s51
s_cmp_ge_u32 s51,6520832
s_cselect_b32 s55,196608,s55
s_add_u32 s52,s51,s55
s_cmp_eq_u32 s52,s56
s_cselect_b32 s52,s57,s52
s_cmp_eq_u32 s52,s58
s_cselect_b32 s52,s59,s52
s_cmp_ge_u32 s52,6520832
s_cselect_b32 s55,196608,s55
s_lshl_b32 s63,s54,12
s_mul_i32 s64,s54,3072
s_add_u32 s64,s64,16384
s_lshl_b32 s78,s54,8
s_add_u32 s78,s78,65536
s_getpc_b64 s[90:91]
Lg4_pc_%=:
s_mov_b32 s68,Lg4_d1w0_%=-Lg4_pc_%=
s_cmp_eq_u32 s54,1
s_cselect_b32 s68,Lg4_d1w1_%=-Lg4_pc_%=,s68
s_cmp_eq_u32 s54,2
s_cselect_b32 s68,Lg4_d1w2_%=-Lg4_pc_%=,s68
s_cmp_eq_u32 s54,3
s_cselect_b32 s68,Lg4_d1w3_%=-Lg4_pc_%=,s68
s_add_u32 s92,s90,s68
s_addc_u32 s93,s91,0
s_mov_b32 s68,Lg4_d0w0_%=-Lg4_pc_%=
s_cmp_eq_u32 s54,1
s_cselect_b32 s68,Lg4_d0w1_%=-Lg4_pc_%=,s68
s_cmp_eq_u32 s54,2
s_cselect_b32 s68,Lg4_d0w2_%=-Lg4_pc_%=,s68
s_cmp_eq_u32 s54,3
s_cselect_b32 s68,Lg4_d0w3_%=-Lg4_pc_%=,s68
s_add_u32 s90,s90,s68
s_addc_u32 s91,s91,0
s_mov_b32 s66,s59
s_mov_b32 s65,0
s_mov_b32 s62,s63
s_branch Lg4_slowb0_%=
Lg4_d0w0_%=:
v_mfma_f32_16x16x32_bf16 v[12:15],a[0:3],v[44:47],0
s_mov_b32 s73,s47
ds_read_b128 v[56:59],v2 offset:3072
v_lshlrev_b32 v60,16,v68
v_mfma_f32_16x16x32_bf16 v[16:19],a[4:7],v[44:47],0
s_max_u32 s73,s73,s65
v_and_b32 v61,0xffff0000,v68
v_lshlrev_b32 v62,16,v69
v_mfma_f32_16x16x32_bf16 v[20:23],a[8:11],v[44:47],0
s_mov_b32 s65,0
v_and_b32 v63,0xffff0000,v69
v_lshlrev_b32 v64,16,v70
v_mfma_f32_16x16x32_bf16 v[24:27],a[12:15],v[44:47],0
s_waitcnt lgkmcnt(2)
v_and_b32 v65,0xffff0000,v70
v_lshlrev_b32 v66,16,v71
v_mfma_f32_16x16x32_bf16 v[12:15],a[16:19],v[48:51],v[12:15]
s_mul_i32 s61,s49,7
ds_read_b128 v[44:47],v2 offset:4096
v_and_b32 v67,0xffff0000,v71
v_mfma_f32_16x16x32_bf16 v[16:19],a[20:23],v[48:51],v[16:19]
s_lshr_b32 s61,s61,1
v_cvt_pk_bf16_f32 v28,v28,v29
v_cvt_pk_bf16_f32 v29,v30,v31
v_mfma_f32_16x16x32_bf16 v[20:23],a[24:27],v[48:51],v[20:23]
v_cvt_pk_bf16_f32 v30,v32,v33
v_cvt_pk_bf16_f32 v31,v34,v35
v_mfma_f32_16x16x32_bf16 v[24:27],a[28:31],v[48:51],v[24:27]
s_waitcnt lgkmcnt(2)
v_add_f32 v36,v36,v60
v_add_f32 v37,v37,v61
v_mfma_f32_16x16x32_bf16 v[12:15],a[32:35],v[52:55],v[12:15]
buffer_store_dwordx4 v[28:31],v3,s[36:39],s73 offen offset:0 sc1
ds_read_b128 v[48:51],v2 offset:5120
v_add_f32 v38,v38,v62
v_add_f32 v39,v39,v63
v_mfma_f32_16x16x32_bf16 v[16:19],a[36:39],v[52:55],v[16:19]
v_add_f32 v40,v40,v64
v_add_f32 v41,v41,v65
v_mfma_f32_16x16x32_bf16 v[20:23],a[40:43],v[52:55],v[20:23]
v_add_f32 v42,v42,v66
v_add_f32 v43,v43,v67
v_mfma_f32_16x16x32_bf16 v[24:27],a[44:47],v[52:55],v[24:27]
s_waitcnt lgkmcnt(2)
v_cvt_pk_bf16_f32 v36,v36,v37
v_cvt_pk_bf16_f32 v37,v38,v39
v_mfma_f32_16x16x32_bf16 v[12:15],a[48:51],v[56:59],v[12:15]
ds_read_b128 v[52:55],v2 offset:6144
v_cvt_pk_bf16_f32 v38,v40,v41
v_mfma_f32_16x16x32_bf16 v[16:19],a[52:55],v[56:59],v[16:19]
v_cvt_pk_bf16_f32 v39,v42,v43
v_mfma_f32_16x16x32_bf16 v[20:23],a[56:59],v[56:59],v[20:23]
s_waitcnt vmcnt(1) lgkmcnt(2)
v_mfma_f32_16x16x32_bf16 v[24:27],a[60:63],v[56:59],v[24:27]
s_barrier
v_mfma_f32_16x16x32_bf16 v[12:15],a[64:67],v[44:47],v[12:15]
ds_read_b32 v11,v8 offset:4096
ds_read_b128 v[56:59],v2 offset:7168
v_mfma_f32_16x16x32_bf16 v[16:19],a[68:71],v[44:47],v[16:19]
v_mfma_f32_16x16x32_bf16 v[20:23],a[72:75],v[44:47],v[20:23]
v_mfma_f32_16x16x32_bf16 v[24:27],a[76:79],v[44:47],v[24:27]
s_waitcnt lgkmcnt(3)
v_mfma_f32_16x16x32_bf16 v[12:15],a[80:83],v[48:51],v[12:15]
s_waitcnt lgkmcnt(1)
buffer_store_dwordx4 v[36:39],v4,s[36:39],s73 offen offset:0 sc1
v_cmp_eq_u32 vcc,s60,v11
v_mfma_f32_16x16x32_bf16 v[16:19],a[84:87],v[48:51],v[16:19]
s_cmp_eq_u32 vcc_lo,-1
ds_read_b128 v[44:47],v2 offset:8192
s_cselect_b32 s66,s49,s59
v_mfma_f32_16x16x32_bf16 v[20:23],a[88:91],v[48:51],v[20:23]
s_cselect_b32 s61,s61,s59
s_add_u32 m0,s63,28672
s_cmp_ge_u32 s66,s59
v_mfma_f32_16x16x32_bf16 v[24:27],a[92:95],v[48:51],v[24:27]
s_cselect_b32 s68,s49,s59
s_mul_i32 s68,s68,5
v_mfma_f32_16x16x32_bf16 v[12:15],a[96:99],v[52:55],v[12:15]
buffer_load_dwordx4 v0,s[36:39],s61 offen offset:0 sc1 lds
v_mfma_f32_16x16x32_bf16 v[16:19],a[100:103],v[52:55],v[16:19]
s_lshr_b32 s68,s68,6
ds_read_b128 v[48:51],v2 offset:9216
v_mfma_f32_16x16x32_bf16 v[20:23],a[104:107],v[52:55],v[20:23]
v_mfma_f32_16x16x32_bf16 v[24:27],a[108:111],v[52:55],v[24:27]
s_waitcnt lgkmcnt(2)
v_mfma_f32_16x16x32_bf16 v[12:15],a[112:115],v[56:59],v[12:15]
buffer_load_dwordx4 v0,s[36:39],s61 offen offset:1024 sc1 lds
v_mfma_f32_16x16x32_bf16 v[16:19],a[116:119],v[56:59],v[16:19]
ds_read_b128 v[52:55],v2 offset:10240
v_mfma_f32_16x16x32_bf16 v[20:23],a[120:123],v[56:59],v[20:23]
v_mfma_f32_16x16x32_bf16 v[24:27],a[124:127],v[56:59],v[24:27]
s_waitcnt lgkmcnt(2)
v_mfma_f32_16x16x32_bf16 v[12:15],a[128:131],v[44:47],v[12:15]
buffer_load_dwordx4 v0,s[36:39],s61 offen offset:2048 sc1 lds
v_mfma_f32_16x16x32_bf16 v[16:19],a[132:135],v[44:47],v[16:19]
ds_read_b128 v[56:59],v2 offset:11264
v_mfma_f32_16x16x32_bf16 v[20:23],a[136:139],v[44:47],v[20:23]
v_mfma_f32_16x16x32_bf16 v[24:27],a[140:143],v[44:47],v[24:27]
s_waitcnt lgkmcnt(2)
v_mfma_f32_16x16x32_bf16 v[12:15],a[144:147],v[48:51],v[12:15]
buffer_load_dwordx4 v0,s[36:39],s61 offen offset:3072 sc1 lds
v_mfma_f32_16x16x32_bf16 v[16:19],a[148:151],v[48:51],v[16:19]
s_add_u32 m0,s78,6144)"
R"(
ds_read_b128 v[44:47],v2 offset:12288
s_add_u32 s53,s52,s55
v_mfma_f32_16x16x32_bf16 v[20:23],a[152:155],v[48:51],v[20:23]
s_cmp_eq_u32 s53,s56
s_cselect_b32 s53,s57,s53
s_cmp_eq_u32 s53,s58
v_mfma_f32_16x16x32_bf16 v[24:27],a[156:159],v[48:51],v[24:27]
s_waitcnt lgkmcnt(2)
s_cselect_b32 s53,s59,s53
s_cmp_ge_u32 s53,6520832
v_mfma_f32_16x16x32_bf16 v[12:15],a[160:163],v[52:55],v[12:15]
buffer_load_dwordx4 v[68:71],v9,s[36:39],s48 offen offset:0 sc1
v_mfma_f32_16x16x32_bf16 v[16:19],a[164:167],v[52:55],v[16:19]
s_cselect_b32 s55,196608,s55
ds_read_b128 v[48:51],v2 offset:13312
v_mfma_f32_16x16x32_bf16 v[20:23],a[168:171],v[52:55],v[20:23]
v_mfma_f32_16x16x32_bf16 v[24:27],a[172:175],v[52:55],v[24:27]
s_waitcnt lgkmcnt(2)
v_mfma_f32_16x16x32_bf16 v[12:15],a[176:179],v[56:59],v[12:15]
ds_read_b128 v[52:55],v2 offset:14336
v_mfma_f32_16x16x32_bf16 v[16:19],a[180:183],v[56:59],v[16:19]
buffer_load_dword v7,s[36:39],s68 offen offset:0 sc1 lds
v_mfma_f32_16x16x32_bf16 v[20:23],a[184:187],v[56:59],v[20:23]
s_add_u32 m0,s64,28672
s_mul_i32 s68,s50,5
s_lshr_b32 s68,s68,6
v_mfma_f32_16x16x32_bf16 v[24:27],a[188:191],v[56:59],v[24:27]
s_waitcnt lgkmcnt(2)
v_mfma_f32_16x16x32_bf16 v[12:15],a[192:195],v[44:47],v[12:15]
ds_read_b128 v[56:59],v2 offset:15360
v_mfma_f32_16x16x32_bf16 v[16:19],a[196:199],v[44:47],v[16:19]
v_mfma_f32_16x16x32_bf16 v[20:23],a[200:203],v[44:47],v[20:23]
s_waitcnt vmcnt(9) lgkmcnt(2)
v_mfma_f32_16x16x32_bf16 v[24:27],a[204:207],v[44:47],v[24:27]
s_barrier
v_mfma_f32_16x16x32_bf16 v[12:15],a[208:211],v[48:51],v[12:15]
ds_read_b128 v[44:47],v2 offset:16384
v_mfma_f32_16x16x32_bf16 v[16:19],a[212:215],v[48:51],v[16:19]
buffer_load_dwordx4 v1,s[36:39],s61 offen offset:0 sc1 lds
v_mfma_f32_16x16x32_bf16 v[20:23],a[216:219],v[48:51],v[20:23]
v_mfma_f32_16x16x32_bf16 v[24:27],a[220:223],v[48:51],v[24:27]
s_waitcnt lgkmcnt(2)
v_mfma_f32_16x16x32_bf16 v[12:15],a[224:227],v[52:55],v[12:15]
ds_read_b128 v[48:51],v2 offset:17408
v_mfma_f32_16x16x32_bf16 v[16:19],a[228:231],v[52:55],v[16:19]
v_mfma_f32_16x16x32_bf16 v[20:23],a[232:235],v[52:55],v[20:23]
v_mfma_f32_16x16x32_bf16 v[24:27],a[236:239],v[52:55],v[24:27]
s_waitcnt lgkmcnt(2)
buffer_load_dwordx4 v1,s[36:39],s61 offen offset:1024 sc1 lds
v_mfma_f32_16x16x32_bf16 v[12:15],a[240:243],v[56:59],v[12:15]
ds_read_b128 v[52:55],v2 offset:18432
v_mfma_f32_16x16x32_bf16 v[16:19],a[244:247],v[56:59],v[16:19]
v_mfma_f32_16x16x32_bf16 v[20:23],a[248:251],v[56:59],v[20:23]
v_mfma_f32_16x16x32_bf16 v[24:27],a[252:255],v[56:59],v[24:27]
s_waitcnt lgkmcnt(2)
v_mfma_f32_16x16x32_bf16 v[12:15],v[100:103],v[44:47],v[12:15]
ds_read_b128 v[56:59],v2 offset:19456
v_mfma_f32_16x16x32_bf16 v[16:19],v[104:107],v[44:47],v[16:19]
buffer_load_dwordx4 v1,s[36:39],s61 offen offset:2048 sc1 lds
v_mfma_f32_16x16x32_bf16 v[20:23],v[108:111],v[44:47],v[20:23]
s_add_u32 m0,s78,5120
ds_read_b128 v[88:91],v10 offset:0
s_cmp_ge_u32 s66,s59
v_mfma_f32_16x16x32_bf16 v[24:27],v[112:115],v[44:47],v[24:27]
s_waitcnt lgkmcnt(3)
v_mfma_f32_16x16x32_bf16 v[12:15],v[116:119],v[48:51],v[12:15]
ds_read_b128 v[44:47],v2 offset:20480
v_mfma_f32_16x16x32_bf16 v[16:19],v[120:123],v[48:51],v[16:19]
ds_read_b128 v[76:79],v10 offset:1024
v_mfma_f32_16x16x32_bf16 v[20:23],v[124:127],v[48:51],v[20:23]
v_mfma_f32_16x16x32_bf16 v[24:27],v[128:131],v[48:51],v[24:27]
s_waitcnt lgkmcnt(4)
v_mfma_f32_16x16x32_bf16 v[12:15],v[132:135],v[52:55],v[12:15]
buffer_load_dword v7,s[36:39],s68 offen offset:0 sc1 lds
v_mfma_f32_16x16x32_bf16 v[16:19],v[136:139],v[52:55],v[16:19]
ds_read_b128 v[48:51],v2 offset:21504
ds_read_b128 v[80:83],v10 offset:2048
v_mfma_f32_16x16x32_bf16 v[20:23],v[140:143],v[52:55],v[20:23]
v_mfma_f32_16x16x32_bf16 v[24:27],v[144:147],v[52:55],v[24:27]
s_waitcnt lgkmcnt(4)
v_mfma_f32_16x16x32_bf16 v[12:15],v[148:151],v[56:59],v[12:15]
ds_read_b128 v[52:55],v2 offset:22528
v_mfma_f32_16x16x32_bf16 v[16:19],v[152:155],v[56:59],v[16:19]
ds_read_b128 v[84:87],v10 offset:3072
v_mfma_f32_16x16x32_bf16 v[20:23],v[156:159],v[56:59],v[20:23]
v_mfma_f32_16x16x32_bf16 v[24:27],v[88:91],v[56:59],v[24:27]
s_waitcnt lgkmcnt(4)
v_mfma_f32_16x16x32_bf16 v[12:15],v[160:163],v[44:47],v[12:15]
ds_read_b128 v[56:59],v2 offset:23552
v_mfma_f32_16x16x32_bf16 v[16:19],v[164:167],v[44:47],v[16:19]
ds_read_b128 v[88:91],v10 offset:4096
v_mfma_f32_16x16x32_bf16 v[20:23],v[168:171],v[44:47],v[20:23]
v_mfma_f32_16x16x32_bf16 v[24:27],v[76:79],v[44:47],v[24:27]
s_waitcnt lgkmcnt(4)
v_mfma_f32_16x16x32_bf16 v[12:15],v[172:175],v[48:51],v[12:15]
ds_read_b128 v[44:47],v2 offset:24576
v_mfma_f32_16x16x32_bf16 v[16:19],v[176:179],v[48:51],v[16:19]
ds_read_b128 v[76:79],v10 offset:5120
v_mfma_f32_16x16x32_bf16 v[20:23],v[180:183],v[48:51],v[20:23]
s_waitcnt vmcnt(10) lgkmcnt(4)
v_mfma_f32_16x16x32_bf16 v[24:27],v[80:83],v[48:51],v[24:27]
s_barrier
v_mfma_f32_16x16x32_bf16 v[12:15],v[184:187],v[52:55],v[12:15]
ds_read_b128 v[48:51],v2 offset:25600
v_mfma_f32_16x16x32_bf16 v[16:19],v[188:191],v[52:55],v[16:19]
ds_read_b128 v[80:83],v10 offset:6144
v_mfma_f32_16x16x32_bf16 v[20:23],v[192:195],v[52:55],v[20:23]
v_mfma_f32_16x16x32_bf16 v[24:27],v[84:87],v[52:55],v[24:27]
s_waitcnt lgkmcnt(4)
v_mfma_f32_16x16x32_bf16 v[12:15],v[196:199],v[56:59],v[12:15]
ds_read_b128 v[52:55],v2 offset:26624
v_mfma_f32_16x16x32_bf16 v[16:19],v[200:203],v[56:59],v[16:19]
ds_read_b128 v[84:87],v10 offset:7168
v_mfma_f32_16x16x32_bf16 v[20:23],v[204:207],v[56:59],v[20:23]
v_mfma_f32_16x16x32_bf16 v[24:27],v[88:91],v[56:59],v[24:27]
s_waitcnt lgkmcnt(4)
v_mfma_f32_16x16x32_bf16 v[12:15],v[208:211],v[44:47],v[12:15]
ds_read_b128 v[56:59],v2 offset:27648
v_mfma_f32_16x16x32_bf16 v[16:19],v[212:215],v[44:47],v[16:19]
ds_read_b128 v[88:91],v10 offset:8192
v_mfma_f32_16x16x32_bf16 v[20:23],v[216:219],v[44:47],v[20:23]
s_waitcnt vmcnt(4) lgkmcnt(4)
v_mfma_f32_16x16x32_bf16 v[24:27],v[76:79],v[44:47],v[24:27]
s_barrier
v_mfma_f32_16x16x32_bf16 v[12:15],v[220:223],v[48:51],v[12:15]
ds_read_b128 v[44:47],v2 offset:28672
v_mfma_f32_16x16x32_bf16 v[16:19],v[224:227],v[48:51],v[16:19]
v_mfma_f32_16x16x32_bf16 v[20:23],v[228:231],v[48:51],v[20:23]
v_mfma_f32_16x16x32_bf16 v[24:27],v[80:83],v[48:51],v[24:27]
s_waitcnt lgkmcnt(3)
v_mfma_f32_16x16x32_bf16 v[12:15],v[232:235],v[52:55],v[12:15]
ds_read_b128 v[48:51],v2 offset:29696
v_mfma_f32_16x16x32_bf16 v[16:19],v[236:239],v[52:55],v[16:19]
v_mfma_f32_16x16x32_bf16 v[20:23],v[240:243],v[52:55],v[20:23]
v_mfma_f32_16x16x32_bf16 v[24:27],v[84:87],v[52:55],v[24:27]
s_waitcnt lgkmcnt(2)
v_mfma_f32_16x16x32_bf16 v[12:15],v[244:247],v[56:59],v[12:15]
ds_read_b128 v[52:55],v2 offset:30720
v_mfma_f32_16x16x32_bf16 v[16:19],v[248:251],v[56:59],v[16:19]
v_mfma_f32_16x16x32_bf16 v[20:23],v[252:255],v[56:59],v[20:23]
s_waitcnt lgkmcnt(2)
v_mfma_f32_16x16x32_bf16 v[24:27],v[88:91],v[56:59],v[24:27]
s_cbranch_scc1 Lg4_slow1_%=
Lg4_d1w0_%=:
v_mfma_f32_16x16x32_bf16 v[28:31],a[0:3],v[44:47],0
s_mov_b32 s73,s48
ds_read_b128 v[56:59],v2 offset:31744
v_lshlrev_b32 v60,16,v68
v_mfma_f32_16x16x32_bf16 v[32:35],a[4:7],v[44:47],0
s_max_u32 s73,s73,s65
v_and_b32 v61,0xffff0000,v68
v_lshlrev_b32 v62,16,v69
v_mfma_f32_16x16x32_bf16 v[36:39],a[8:11],v[44:47],0
s_mov_b32 s65,0
v_and_b32 v63,0xffff0000,v69
v_lshlrev_b32 v64,16,v70
v_mfma_f32_16x16x32_bf16 v[40:43],a[12:15],v[44:47],0
s_waitcnt lgkmcnt(2)
v_and_b32 v65,0xffff0000,v70
v_lshlrev_b32 v66,16,v71
v_mfma_f32_16x16x32_bf16 v[28:31],a[16:19],v[48:51],v[28:31]
s_mul_i32 s61,s50,7
ds_read_b128 v[44:47],v2 offset:32768
v_and_b32 v67,0xffff0000,v71
v_mfma_f32_16x16x32_bf16 v[32:35],a[20:23],v[48:51],v[32:35]
s_lshr_b32 s61,s61,1
v_cvt_pk_bf16_f32 v12,v12,v13
v_cvt_pk_bf16_f32 v13,v14,v15
v_mfma_f32_16x16x32_bf16 v[36:39],a[24:27],v[48:51],v[36:39]
s_mov_b64 s[44:45],s[46:47]
v_cvt_pk_bf16_f32 v14,v16,v17
v_cvt_pk_bf16_f32 v15,v18,v19
v_mfma_f32_16x16x32_bf16 v[40:43],a[28:31],v[48:51],v[40:43]
s_waitcnt lgkmcnt(2)
v_add_f32 v20,v20,v60
v_add_f32 v21,v21,v61
v_mfma_f32_16x16x32_bf16 v[28:31],a[32:35],v[52:55],v[28:31]
buffer_store_dwordx4 v[12:15],v3,s[36:39],s73 offen offset:0 sc1
ds_read_b128 v[48:51],v2 offset:33792
v_add_f32 v22,v22,v62
v_add_f32 v23,v23,v63
v_mfma_f32_16x16x32_bf16 v[32:35],a[36:39],v[52:55],v[32:35]
s_mov_b64 s[46:47],s[48:49]
v_add_f32 v24,v24,v64
v_add_f32 v25,v25,v65
v_mfma_f32_16x16x32_bf16 v[36:39],a[40:43],v[52:55],v[36:39]
v_add_f32 v26,v26,v66
v_add_f32 v27,v27,v67
v_mfma_f32_16x16x32_bf16 v[40:43],a[44:47],v[52:55],v[40:43]
s_waitcnt lgkmcnt(2)
v_cvt_pk_bf16_f32 v20,v20,v21
v_cvt_pk_bf16_f32 v21,v22,v23
v_mfma_f32_16x16x32_bf16 v[28:31],a[48:51],v[56:59],v[28:31]
ds_read_b128 v[52:55],v2 offset:34816
v_cvt_pk_bf16_f32 v22,v24,v25
v_mfma_f32_16x16x32_bf16 v[32:35],a[52:55],v[56:59],v[32:35]
v_cvt_pk_bf16_f32 v23,v26,v27
v_mfma_f32_16x16x32_bf16 v[36:39],a[56:59],v[56:59],v[36:39]
s_waitcnt vmcnt(1) lgkmcnt(2)
v_mfma_f32_16x16x32_bf16 v[40:43],a[60:63],v[56:59],v[40:43])"
R"(
s_barrier
v_mfma_f32_16x16x32_bf16 v[28:31],a[64:67],v[44:47],v[28:31]
ds_read_b32 v11,v8 offset:5120
ds_read_b128 v[56:59],v2 offset:35840
v_mfma_f32_16x16x32_bf16 v[32:35],a[68:71],v[44:47],v[32:35]
v_mfma_f32_16x16x32_bf16 v[36:39],a[72:75],v[44:47],v[36:39]
v_mfma_f32_16x16x32_bf16 v[40:43],a[76:79],v[44:47],v[40:43]
s_waitcnt lgkmcnt(3)
v_mfma_f32_16x16x32_bf16 v[28:31],a[80:83],v[48:51],v[28:31]
s_waitcnt lgkmcnt(1)
buffer_store_dwordx4 v[20:23],v4,s[36:39],s73 offen offset:0 sc1
v_cmp_eq_u32 vcc,s60,v11
v_mfma_f32_16x16x32_bf16 v[32:35],a[84:87],v[48:51],v[32:35]
s_cmp_eq_u32 vcc_lo,-1
ds_read_b128 v[44:47],v2 offset:36864
s_cselect_b32 s67,s50,s59
v_mfma_f32_16x16x32_bf16 v[36:39],a[88:91],v[48:51],v[36:39]
s_cselect_b32 s61,s61,s59
s_add_u32 m0,s63,0
s_cmp_ge_u32 s67,s59
v_mfma_f32_16x16x32_bf16 v[40:43],a[92:95],v[48:51],v[40:43]
s_cselect_b32 s68,s50,s59
s_mul_i32 s68,s68,5
v_mfma_f32_16x16x32_bf16 v[28:31],a[96:99],v[52:55],v[28:31]
buffer_load_dwordx4 v0,s[36:39],s61 offen offset:0 sc1 lds
v_mfma_f32_16x16x32_bf16 v[32:35],a[100:103],v[52:55],v[32:35]
s_lshr_b32 s68,s68,6
ds_read_b128 v[48:51],v2 offset:37888
v_mfma_f32_16x16x32_bf16 v[36:39],a[104:107],v[52:55],v[36:39]
v_mfma_f32_16x16x32_bf16 v[40:43],a[108:111],v[52:55],v[40:43]
s_waitcnt lgkmcnt(2)
v_mfma_f32_16x16x32_bf16 v[28:31],a[112:115],v[56:59],v[28:31]
buffer_load_dwordx4 v0,s[36:39],s61 offen offset:1024 sc1 lds
v_mfma_f32_16x16x32_bf16 v[32:35],a[116:119],v[56:59],v[32:35]
ds_read_b128 v[52:55],v2 offset:38912
v_mfma_f32_16x16x32_bf16 v[36:39],a[120:123],v[56:59],v[36:39]
v_mfma_f32_16x16x32_bf16 v[40:43],a[124:127],v[56:59],v[40:43]
s_waitcnt lgkmcnt(2)
v_mfma_f32_16x16x32_bf16 v[28:31],a[128:131],v[44:47],v[28:31]
buffer_load_dwordx4 v0,s[36:39],s61 offen offset:2048 sc1 lds
v_mfma_f32_16x16x32_bf16 v[32:35],a[132:135],v[44:47],v[32:35]
ds_read_b128 v[56:59],v2 offset:39936
v_mfma_f32_16x16x32_bf16 v[36:39],a[136:139],v[44:47],v[36:39]
v_mfma_f32_16x16x32_bf16 v[40:43],a[140:143],v[44:47],v[40:43]
s_waitcnt lgkmcnt(2)
v_mfma_f32_16x16x32_bf16 v[28:31],a[144:147],v[48:51],v[28:31]
buffer_load_dwordx4 v0,s[36:39],s61 offen offset:3072 sc1 lds
v_mfma_f32_16x16x32_bf16 v[32:35],a[148:151],v[48:51],v[32:35]
s_add_u32 m0,s78,6144
ds_read_b128 v[44:47],v2 offset:40960
s_add_u32 s74,s53,s55
v_mfma_f32_16x16x32_bf16 v[36:39],a[152:155],v[48:51],v[36:39]
s_cmp_eq_u32 s74,s56
s_cselect_b32 s74,s57,s74
s_cmp_eq_u32 s74,s58
v_mfma_f32_16x16x32_bf16 v[40:43],a[156:159],v[48:51],v[40:43]
s_waitcnt lgkmcnt(2)
s_cselect_b32 s74,s59,s74
s_cmp_ge_u32 s74,6520832
v_mfma_f32_16x16x32_bf16 v[28:31],a[160:163],v[52:55],v[28:31]
buffer_load_dwordx4 v[68:71],v9,s[36:39],s49 offen offset:0 sc1
v_mfma_f32_16x16x32_bf16 v[32:35],a[164:167],v[52:55],v[32:35]
s_cselect_b32 s55,196608,s55
ds_read_b128 v[48:51],v2 offset:41984
s_mov_b64 s[48:49],s[50:51]
v_mfma_f32_16x16x32_bf16 v[36:39],a[168:171],v[52:55],v[36:39]
v_mfma_f32_16x16x32_bf16 v[40:43],a[172:175],v[52:55],v[40:43]
s_waitcnt lgkmcnt(2)
v_mfma_f32_16x16x32_bf16 v[28:31],a[176:179],v[56:59],v[28:31]
ds_read_b128 v[52:55],v2 offset:43008
v_mfma_f32_16x16x32_bf16 v[32:35],a[180:183],v[56:59],v[32:35]
buffer_load_dword v7,s[36:39],s68 offen offset:0 sc1 lds
v_mfma_f32_16x16x32_bf16 v[36:39],a[184:187],v[56:59],v[36:39]
s_add_u32 m0,s64,0
s_mul_i32 s68,s51,5
s_lshr_b32 s68,s68,6
v_mfma_f32_16x16x32_bf16 v[40:43],a[188:191],v[56:59],v[40:43]
s_waitcnt lgkmcnt(2)
s_mov_b64 s[50:51],s[52:53]
s_mov_b32 s52,s74
v_mfma_f32_16x16x32_bf16 v[28:31],a[192:195],v[44:47],v[28:31]
ds_read_b128 v[56:59],v2 offset:44032
v_mfma_f32_16x16x32_bf16 v[32:35],a[196:199],v[44:47],v[32:35]
v_mfma_f32_16x16x32_bf16 v[36:39],a[200:203],v[44:47],v[36:39]
s_waitcnt vmcnt(9) lgkmcnt(2)
v_mfma_f32_16x16x32_bf16 v[40:43],a[204:207],v[44:47],v[40:43]
s_barrier
v_mfma_f32_16x16x32_bf16 v[28:31],a[208:211],v[48:51],v[28:31]
ds_read_b128 v[44:47],v2 offset:45056
v_mfma_f32_16x16x32_bf16 v[32:35],a[212:215],v[48:51],v[32:35]
buffer_load_dwordx4 v1,s[36:39],s61 offen offset:0 sc1 lds
v_mfma_f32_16x16x32_bf16 v[36:39],a[216:219],v[48:51],v[36:39]
v_mfma_f32_16x16x32_bf16 v[40:43],a[220:223],v[48:51],v[40:43]
s_waitcnt lgkmcnt(2)
v_mfma_f32_16x16x32_bf16 v[28:31],a[224:227],v[52:55],v[28:31]
ds_read_b128 v[48:51],v2 offset:46080
v_mfma_f32_16x16x32_bf16 v[32:35],a[228:231],v[52:55],v[32:35]
v_mfma_f32_16x16x32_bf16 v[36:39],a[232:235],v[52:55],v[36:39]
v_mfma_f32_16x16x32_bf16 v[40:43],a[236:239],v[52:55],v[40:43]
s_waitcnt lgkmcnt(2)
buffer_load_dwordx4 v1,s[36:39],s61 offen offset:1024 sc1 lds
v_mfma_f32_16x16x32_bf16 v[28:31],a[240:243],v[56:59],v[28:31]
ds_read_b128 v[52:55],v2 offset:47104
v_mfma_f32_16x16x32_bf16 v[32:35],a[244:247],v[56:59],v[32:35]
v_mfma_f32_16x16x32_bf16 v[36:39],a[248:251],v[56:59],v[36:39]
v_mfma_f32_16x16x32_bf16 v[40:43],a[252:255],v[56:59],v[40:43]
s_waitcnt lgkmcnt(2)
v_mfma_f32_16x16x32_bf16 v[28:31],v[100:103],v[44:47],v[28:31]
ds_read_b128 v[56:59],v2 offset:48128
v_mfma_f32_16x16x32_bf16 v[32:35],v[104:107],v[44:47],v[32:35]
buffer_load_dwordx4 v1,s[36:39],s61 offen offset:2048 sc1 lds
v_mfma_f32_16x16x32_bf16 v[36:39],v[108:111],v[44:47],v[36:39]
s_add_u32 m0,s78,4096
ds_read_b128 v[88:91],v10 offset:0
s_cmp_lt_u32 s67,s59
v_mfma_f32_16x16x32_bf16 v[40:43],v[112:115],v[44:47],v[40:43]
s_waitcnt lgkmcnt(3)
v_mfma_f32_16x16x32_bf16 v[28:31],v[116:119],v[48:51],v[28:31]
ds_read_b128 v[44:47],v2 offset:49152
v_mfma_f32_16x16x32_bf16 v[32:35],v[120:123],v[48:51],v[32:35]
ds_read_b128 v[76:79],v10 offset:1024
v_mfma_f32_16x16x32_bf16 v[36:39],v[124:127],v[48:51],v[36:39]
v_mfma_f32_16x16x32_bf16 v[40:43],v[128:131],v[48:51],v[40:43]
s_waitcnt lgkmcnt(4)
v_mfma_f32_16x16x32_bf16 v[28:31],v[132:135],v[52:55],v[28:31]
buffer_load_dword v7,s[36:39],s68 offen offset:0 sc1 lds
v_mfma_f32_16x16x32_bf16 v[32:35],v[136:139],v[52:55],v[32:35]
ds_read_b128 v[48:51],v2 offset:50176
ds_read_b128 v[80:83],v10 offset:2048
v_mfma_f32_16x16x32_bf16 v[36:39],v[140:143],v[52:55],v[36:39]
v_mfma_f32_16x16x32_bf16 v[40:43],v[144:147],v[52:55],v[40:43]
s_waitcnt lgkmcnt(4)
v_mfma_f32_16x16x32_bf16 v[28:31],v[148:151],v[56:59],v[28:31]
ds_read_b128 v[52:55],v2 offset:51200
v_mfma_f32_16x16x32_bf16 v[32:35],v[152:155],v[56:59],v[32:35]
ds_read_b128 v[84:87],v10 offset:3072
v_mfma_f32_16x16x32_bf16 v[36:39],v[156:159],v[56:59],v[36:39]
v_mfma_f32_16x16x32_bf16 v[40:43],v[88:91],v[56:59],v[40:43]
s_waitcnt lgkmcnt(4)
v_mfma_f32_16x16x32_bf16 v[28:31],v[160:163],v[44:47],v[28:31]
ds_read_b128 v[56:59],v2 offset:52224
v_mfma_f32_16x16x32_bf16 v[32:35],v[164:167],v[44:47],v[32:35]
ds_read_b128 v[88:91],v10 offset:4096
v_mfma_f32_16x16x32_bf16 v[36:39],v[168:171],v[44:47],v[36:39]
v_mfma_f32_16x16x32_bf16 v[40:43],v[76:79],v[44:47],v[40:43]
s_waitcnt lgkmcnt(4)
v_mfma_f32_16x16x32_bf16 v[28:31],v[172:175],v[48:51],v[28:31]
ds_read_b128 v[44:47],v2 offset:53248
v_mfma_f32_16x16x32_bf16 v[32:35],v[176:179],v[48:51],v[32:35]
ds_read_b128 v[76:79],v10 offset:5120
v_mfma_f32_16x16x32_bf16 v[36:39],v[180:183],v[48:51],v[36:39]
s_waitcnt vmcnt(10) lgkmcnt(4)
v_mfma_f32_16x16x32_bf16 v[40:43],v[80:83],v[48:51],v[40:43]
s_barrier
v_mfma_f32_16x16x32_bf16 v[28:31],v[184:187],v[52:55],v[28:31]
ds_read_b128 v[48:51],v2 offset:54272
v_mfma_f32_16x16x32_bf16 v[32:35],v[188:191],v[52:55],v[32:35]
ds_read_b128 v[80:83],v10 offset:6144
v_mfma_f32_16x16x32_bf16 v[36:39],v[192:195],v[52:55],v[36:39]
v_mfma_f32_16x16x32_bf16 v[40:43],v[84:87],v[52:55],v[40:43]
s_waitcnt lgkmcnt(4)
v_mfma_f32_16x16x32_bf16 v[28:31],v[196:199],v[56:59],v[28:31]
ds_read_b128 v[52:55],v2 offset:55296
v_mfma_f32_16x16x32_bf16 v[32:35],v[200:203],v[56:59],v[32:35]
ds_read_b128 v[84:87],v10 offset:7168
v_mfma_f32_16x16x32_bf16 v[36:39],v[204:207],v[56:59],v[36:39]
v_mfma_f32_16x16x32_bf16 v[40:43],v[88:91],v[56:59],v[40:43]
s_waitcnt lgkmcnt(4)
v_mfma_f32_16x16x32_bf16 v[28:31],v[208:211],v[44:47],v[28:31]
ds_read_b128 v[56:59],v2 offset:56320
v_mfma_f32_16x16x32_bf16 v[32:35],v[212:215],v[44:47],v[32:35]
ds_read_b128 v[88:91],v10 offset:8192
v_mfma_f32_16x16x32_bf16 v[36:39],v[216:219],v[44:47],v[36:39]
s_waitcnt vmcnt(4) lgkmcnt(4)
v_mfma_f32_16x16x32_bf16 v[40:43],v[76:79],v[44:47],v[40:43]
s_barrier
v_mfma_f32_16x16x32_bf16 v[28:31],v[220:223],v[48:51],v[28:31]
ds_read_b128 v[44:47],v2 offset:0
v_mfma_f32_16x16x32_bf16 v[32:35],v[224:227],v[48:51],v[32:35]
v_mfma_f32_16x16x32_bf16 v[36:39],v[228:231],v[48:51],v[36:39]
v_mfma_f32_16x16x32_bf16 v[40:43],v[80:83],v[48:51],v[40:43]
s_waitcnt lgkmcnt(3)
v_mfma_f32_16x16x32_bf16 v[28:31],v[232:235],v[52:55],v[28:31]
ds_read_b128 v[48:51],v2 offset:1024
v_mfma_f32_16x16x32_bf16 v[32:35],v[236:239],v[52:55],v[32:35]
v_mfma_f32_16x16x32_bf16 v[36:39],v[240:243],v[52:55],v[36:39]
v_mfma_f32_16x16x32_bf16 v[40:43],v[84:87],v[52:55],v[40:43]
s_waitcnt lgkmcnt(2)
v_mfma_f32_16x16x32_bf16 v[28:31],v[244:247],v[56:59],v[28:31]
ds_read_b128 v[52:55],v2 offset:2048
v_mfma_f32_16x16x32_bf16 v[32:35],v[248:251],v[56:59],v[32:35]
v_mfma_f32_16x16x32_bf16 v[36:39],v[252:255],v[56:59],v[36:39]
s_waitcnt lgkmcnt(2)
v_mfma_f32_16x16x32_bf16 v[40:43],v[88:91],v[56:59],v[40:43]
s_cbranch_scc1 Lg4_d0w0_%=
s_branch Lg4_slow0_%=
Lg4_d0w1_%=:
v_mfma_f32_16x16x32_bf16 v[12:15],a[0:3],v[44:47],0
s_mov_b32 s73,s47
ds_read_b128 v[56:59],v2 offset:3072)"
R"(
v_lshlrev_b32 v60,16,v68
v_mfma_f32_16x16x32_bf16 v[16:19],a[4:7],v[44:47],0
s_max_u32 s73,s73,s65
v_and_b32 v61,0xffff0000,v68
v_lshlrev_b32 v62,16,v69
v_mfma_f32_16x16x32_bf16 v[20:23],a[8:11],v[44:47],0
s_mov_b32 s65,0
v_and_b32 v63,0xffff0000,v69
v_lshlrev_b32 v64,16,v70
v_mfma_f32_16x16x32_bf16 v[24:27],a[12:15],v[44:47],0
s_waitcnt lgkmcnt(2)
v_and_b32 v65,0xffff0000,v70
v_lshlrev_b32 v66,16,v71
v_mfma_f32_16x16x32_bf16 v[12:15],a[16:19],v[48:51],v[12:15]
s_mul_i32 s61,s49,7
ds_read_b128 v[44:47],v2 offset:4096
v_and_b32 v67,0xffff0000,v71
v_mfma_f32_16x16x32_bf16 v[16:19],a[20:23],v[48:51],v[16:19]
s_lshr_b32 s61,s61,1
v_cvt_pk_bf16_f32 v28,v28,v29
v_cvt_pk_bf16_f32 v29,v30,v31
v_mfma_f32_16x16x32_bf16 v[20:23],a[24:27],v[48:51],v[20:23]
v_cvt_pk_bf16_f32 v30,v32,v33
v_cvt_pk_bf16_f32 v31,v34,v35
v_mfma_f32_16x16x32_bf16 v[24:27],a[28:31],v[48:51],v[24:27]
s_waitcnt lgkmcnt(2)
v_add_f32 v36,v36,v60
v_add_f32 v37,v37,v61
v_mfma_f32_16x16x32_bf16 v[12:15],a[32:35],v[52:55],v[12:15]
ds_read_b128 v[48:51],v2 offset:5120
v_add_f32 v38,v38,v62
v_mfma_f32_16x16x32_bf16 v[16:19],a[36:39],v[52:55],v[16:19]
buffer_store_dwordx4 v[28:31],v3,s[36:39],s73 offen offset:0 sc1
v_add_f32 v39,v39,v63
v_add_f32 v40,v40,v64
v_add_f32 v41,v41,v65
v_mfma_f32_16x16x32_bf16 v[20:23],a[40:43],v[52:55],v[20:23]
v_add_f32 v42,v42,v66
v_add_f32 v43,v43,v67
v_mfma_f32_16x16x32_bf16 v[24:27],a[44:47],v[52:55],v[24:27]
s_waitcnt lgkmcnt(2)
v_cvt_pk_bf16_f32 v36,v36,v37
v_cvt_pk_bf16_f32 v37,v38,v39
v_mfma_f32_16x16x32_bf16 v[12:15],a[48:51],v[56:59],v[12:15]
ds_read_b128 v[52:55],v2 offset:6144
v_cvt_pk_bf16_f32 v38,v40,v41
v_mfma_f32_16x16x32_bf16 v[16:19],a[52:55],v[56:59],v[16:19]
v_cvt_pk_bf16_f32 v39,v42,v43
v_mfma_f32_16x16x32_bf16 v[20:23],a[56:59],v[56:59],v[20:23]
s_waitcnt lgkmcnt(2)
s_nop 0
v_mfma_f32_16x16x32_bf16 v[24:27],a[60:63],v[56:59],v[24:27]
s_barrier
v_mfma_f32_16x16x32_bf16 v[12:15],a[64:67],v[44:47],v[12:15]
ds_read_b128 v[56:59],v2 offset:7168
ds_read_b32 v11,v8 offset:4096
v_mfma_f32_16x16x32_bf16 v[16:19],a[68:71],v[44:47],v[16:19]
v_mfma_f32_16x16x32_bf16 v[20:23],a[72:75],v[44:47],v[20:23]
v_mfma_f32_16x16x32_bf16 v[24:27],a[76:79],v[44:47],v[24:27]
s_waitcnt lgkmcnt(3)
v_mfma_f32_16x16x32_bf16 v[12:15],a[80:83],v[48:51],v[12:15]
s_waitcnt lgkmcnt(0)
ds_read_b128 v[44:47],v2 offset:8192
v_mfma_f32_16x16x32_bf16 v[16:19],a[84:87],v[48:51],v[16:19]
buffer_store_dwordx4 v[36:39],v4,s[36:39],s73 offen offset:0 sc1
v_cmp_eq_u32 vcc,s60,v11
v_mfma_f32_16x16x32_bf16 v[20:23],a[88:91],v[48:51],v[20:23]
s_cmp_eq_u32 vcc_lo,-1
s_cselect_b32 s66,s49,s59
s_cselect_b32 s61,s61,s59
v_mfma_f32_16x16x32_bf16 v[24:27],a[92:95],v[48:51],v[24:27]
s_add_u32 m0,s63,28672
s_add_u32 s53,s52,s55
v_mfma_f32_16x16x32_bf16 v[12:15],a[96:99],v[52:55],v[12:15]
s_cmp_eq_u32 s53,s56
ds_read_b128 v[48:51],v2 offset:9216
s_cselect_b32 s53,s57,s53
v_mfma_f32_16x16x32_bf16 v[16:19],a[100:103],v[52:55],v[16:19]
buffer_load_dwordx4 v0,s[36:39],s61 offen offset:0 sc1 lds
v_mfma_f32_16x16x32_bf16 v[20:23],a[104:107],v[52:55],v[20:23]
s_cmp_eq_u32 s53,s58
s_cselect_b32 s53,s59,s53
s_cmp_ge_u32 s53,6520832
v_mfma_f32_16x16x32_bf16 v[24:27],a[108:111],v[52:55],v[24:27]
s_cselect_b32 s55,196608,s55
v_mfma_f32_16x16x32_bf16 v[12:15],a[112:115],v[56:59],v[12:15]
ds_read_b128 v[52:55],v2 offset:10240
v_mfma_f32_16x16x32_bf16 v[16:19],a[116:119],v[56:59],v[16:19]
buffer_load_dwordx4 v0,s[36:39],s61 offen offset:1024 sc1 lds
v_mfma_f32_16x16x32_bf16 v[20:23],a[120:123],v[56:59],v[20:23]
v_mfma_f32_16x16x32_bf16 v[24:27],a[124:127],v[56:59],v[24:27]
s_waitcnt lgkmcnt(2)
v_mfma_f32_16x16x32_bf16 v[12:15],a[128:131],v[44:47],v[12:15]
ds_read_b128 v[56:59],v2 offset:11264
v_mfma_f32_16x16x32_bf16 v[16:19],a[132:135],v[44:47],v[16:19]
buffer_load_dwordx4 v0,s[36:39],s61 offen offset:2048 sc1 lds
v_mfma_f32_16x16x32_bf16 v[20:23],a[136:139],v[44:47],v[20:23]
v_mfma_f32_16x16x32_bf16 v[24:27],a[140:143],v[44:47],v[24:27]
s_waitcnt lgkmcnt(2)
v_mfma_f32_16x16x32_bf16 v[12:15],a[144:147],v[48:51],v[12:15]
ds_read_b128 v[44:47],v2 offset:12288
v_mfma_f32_16x16x32_bf16 v[16:19],a[148:151],v[48:51],v[16:19]
buffer_load_dwordx4 v0,s[36:39],s61 offen offset:3072 sc1 lds
v_mfma_f32_16x16x32_bf16 v[20:23],a[152:155],v[48:51],v[20:23]
s_add_u32 m0,s64,28672
s_cmp_ge_u32 s66,s59
v_mfma_f32_16x16x32_bf16 v[24:27],a[156:159],v[48:51],v[24:27]
s_waitcnt lgkmcnt(2)
v_mfma_f32_16x16x32_bf16 v[12:15],a[160:163],v[52:55],v[12:15]
ds_read_b128 v[48:51],v2 offset:13312
v_mfma_f32_16x16x32_bf16 v[16:19],a[164:167],v[52:55],v[16:19]
buffer_load_dwordx4 v[68:71],v9,s[36:39],s48 offen offset:0 sc1
v_mfma_f32_16x16x32_bf16 v[20:23],a[168:171],v[52:55],v[20:23]
v_mfma_f32_16x16x32_bf16 v[24:27],a[172:175],v[52:55],v[24:27]
s_waitcnt lgkmcnt(2)
v_mfma_f32_16x16x32_bf16 v[12:15],a[176:179],v[56:59],v[12:15]
ds_read_b128 v[52:55],v2 offset:14336
v_mfma_f32_16x16x32_bf16 v[16:19],a[180:183],v[56:59],v[16:19]
v_mfma_f32_16x16x32_bf16 v[20:23],a[184:187],v[56:59],v[20:23]
v_mfma_f32_16x16x32_bf16 v[24:27],a[188:191],v[56:59],v[24:27]
s_waitcnt lgkmcnt(2)
v_mfma_f32_16x16x32_bf16 v[12:15],a[192:195],v[44:47],v[12:15]
ds_read_b128 v[56:59],v2 offset:15360
v_mfma_f32_16x16x32_bf16 v[16:19],a[196:199],v[44:47],v[16:19]
v_mfma_f32_16x16x32_bf16 v[20:23],a[200:203],v[44:47],v[20:23]
s_waitcnt vmcnt(7) lgkmcnt(2)
v_mfma_f32_16x16x32_bf16 v[24:27],a[204:207],v[44:47],v[24:27]
s_barrier
v_mfma_f32_16x16x32_bf16 v[12:15],a[208:211],v[48:51],v[12:15]
ds_read_b128 v[44:47],v2 offset:16384
v_mfma_f32_16x16x32_bf16 v[16:19],a[212:215],v[48:51],v[16:19]
v_mfma_f32_16x16x32_bf16 v[20:23],a[216:219],v[48:51],v[20:23]
buffer_load_dwordx4 v1,s[36:39],s61 offen offset:0 sc1 lds
v_mfma_f32_16x16x32_bf16 v[24:27],a[220:223],v[48:51],v[24:27]
s_waitcnt lgkmcnt(2)
v_mfma_f32_16x16x32_bf16 v[12:15],a[224:227],v[52:55],v[12:15]
ds_read_b128 v[48:51],v2 offset:17408
v_mfma_f32_16x16x32_bf16 v[16:19],a[228:231],v[52:55],v[16:19]
v_mfma_f32_16x16x32_bf16 v[20:23],a[232:235],v[52:55],v[20:23]
v_mfma_f32_16x16x32_bf16 v[24:27],a[236:239],v[52:55],v[24:27]
s_waitcnt lgkmcnt(2)
v_mfma_f32_16x16x32_bf16 v[12:15],a[240:243],v[56:59],v[12:15]
buffer_load_dwordx4 v1,s[36:39],s61 offen offset:1024 sc1 lds
v_mfma_f32_16x16x32_bf16 v[16:19],a[244:247],v[56:59],v[16:19]
ds_read_b128 v[52:55],v2 offset:18432
v_mfma_f32_16x16x32_bf16 v[20:23],a[248:251],v[56:59],v[20:23]
v_mfma_f32_16x16x32_bf16 v[24:27],a[252:255],v[56:59],v[24:27]
s_waitcnt lgkmcnt(2)
v_mfma_f32_16x16x32_bf16 v[12:15],v[100:103],v[44:47],v[12:15]
ds_read_b128 v[56:59],v2 offset:19456
v_mfma_f32_16x16x32_bf16 v[16:19],v[104:107],v[44:47],v[16:19]
ds_read_b128 v[88:91],v10 offset:0
v_mfma_f32_16x16x32_bf16 v[20:23],v[108:111],v[44:47],v[20:23]
buffer_load_dwordx4 v1,s[36:39],s61 offen offset:2048 sc1 lds
v_mfma_f32_16x16x32_bf16 v[24:27],v[112:115],v[44:47],v[24:27]
s_waitcnt lgkmcnt(3)
v_mfma_f32_16x16x32_bf16 v[12:15],v[116:119],v[48:51],v[12:15]
ds_read_b128 v[44:47],v2 offset:20480
v_mfma_f32_16x16x32_bf16 v[16:19],v[120:123],v[48:51],v[16:19]
ds_read_b128 v[76:79],v10 offset:1024
v_mfma_f32_16x16x32_bf16 v[20:23],v[124:127],v[48:51],v[20:23]
v_mfma_f32_16x16x32_bf16 v[24:27],v[128:131],v[48:51],v[24:27]
s_waitcnt lgkmcnt(4)
v_mfma_f32_16x16x32_bf16 v[12:15],v[132:135],v[52:55],v[12:15]
ds_read_b128 v[48:51],v2 offset:21504
v_mfma_f32_16x16x32_bf16 v[16:19],v[136:139],v[52:55],v[16:19]
ds_read_b128 v[80:83],v10 offset:2048
v_mfma_f32_16x16x32_bf16 v[20:23],v[140:143],v[52:55],v[20:23]
v_mfma_f32_16x16x32_bf16 v[24:27],v[144:147],v[52:55],v[24:27]
s_waitcnt lgkmcnt(4)
v_mfma_f32_16x16x32_bf16 v[12:15],v[148:151],v[56:59],v[12:15]
ds_read_b128 v[52:55],v2 offset:22528
v_mfma_f32_16x16x32_bf16 v[16:19],v[152:155],v[56:59],v[16:19]
ds_read_b128 v[84:87],v10 offset:3072
v_mfma_f32_16x16x32_bf16 v[20:23],v[156:159],v[56:59],v[20:23]
v_mfma_f32_16x16x32_bf16 v[24:27],v[88:91],v[56:59],v[24:27]
s_waitcnt lgkmcnt(4)
v_mfma_f32_16x16x32_bf16 v[12:15],v[160:163],v[44:47],v[12:15]
ds_read_b128 v[56:59],v2 offset:23552
v_mfma_f32_16x16x32_bf16 v[16:19],v[164:167],v[44:47],v[16:19]
ds_read_b128 v[88:91],v10 offset:4096
v_mfma_f32_16x16x32_bf16 v[20:23],v[168:171],v[44:47],v[20:23]
v_mfma_f32_16x16x32_bf16 v[24:27],v[76:79],v[44:47],v[24:27]
s_waitcnt lgkmcnt(4)
v_mfma_f32_16x16x32_bf16 v[12:15],v[172:175],v[48:51],v[12:15]
ds_read_b128 v[44:47],v2 offset:24576
v_mfma_f32_16x16x32_bf16 v[16:19],v[176:179],v[48:51],v[16:19]
ds_read_b128 v[76:79],v10 offset:5120
v_mfma_f32_16x16x32_bf16 v[20:23],v[180:183],v[48:51],v[20:23]
s_waitcnt vmcnt(8) lgkmcnt(4)
v_mfma_f32_16x16x32_bf16 v[24:27],v[80:83],v[48:51],v[24:27]
s_barrier
v_mfma_f32_16x16x32_bf16 v[12:15],v[184:187],v[52:55],v[12:15]
ds_read_b128 v[48:51],v2 offset:25600
v_mfma_f32_16x16x32_bf16 v[16:19],v[188:191],v[52:55],v[16:19]
ds_read_b128 v[80:83],v10 offset:6144
v_mfma_f32_16x16x32_bf16 v[20:23],v[192:195],v[52:55],v[20:23]
v_mfma_f32_16x16x32_bf16 v[24:27],v[84:87],v[52:55],v[24:27]
s_waitcnt lgkmcnt(4)
v_mfma_f32_16x16x32_bf16 v[12:15],v[196:199],v[56:59],v[12:15])"
R"(
ds_read_b128 v[52:55],v2 offset:26624
v_mfma_f32_16x16x32_bf16 v[16:19],v[200:203],v[56:59],v[16:19]
ds_read_b128 v[84:87],v10 offset:7168
v_mfma_f32_16x16x32_bf16 v[20:23],v[204:207],v[56:59],v[20:23]
v_mfma_f32_16x16x32_bf16 v[24:27],v[88:91],v[56:59],v[24:27]
s_waitcnt lgkmcnt(4)
v_mfma_f32_16x16x32_bf16 v[12:15],v[208:211],v[44:47],v[12:15]
ds_read_b128 v[56:59],v2 offset:27648
v_mfma_f32_16x16x32_bf16 v[16:19],v[212:215],v[44:47],v[16:19]
ds_read_b128 v[88:91],v10 offset:8192
v_mfma_f32_16x16x32_bf16 v[20:23],v[216:219],v[44:47],v[20:23]
s_waitcnt vmcnt(3) lgkmcnt(4)
v_mfma_f32_16x16x32_bf16 v[24:27],v[76:79],v[44:47],v[24:27]
s_barrier
v_mfma_f32_16x16x32_bf16 v[12:15],v[220:223],v[48:51],v[12:15]
ds_read_b128 v[44:47],v2 offset:28672
v_mfma_f32_16x16x32_bf16 v[16:19],v[224:227],v[48:51],v[16:19]
v_mfma_f32_16x16x32_bf16 v[20:23],v[228:231],v[48:51],v[20:23]
v_mfma_f32_16x16x32_bf16 v[24:27],v[80:83],v[48:51],v[24:27]
s_waitcnt lgkmcnt(3)
v_mfma_f32_16x16x32_bf16 v[12:15],v[232:235],v[52:55],v[12:15]
ds_read_b128 v[48:51],v2 offset:29696
v_mfma_f32_16x16x32_bf16 v[16:19],v[236:239],v[52:55],v[16:19]
v_mfma_f32_16x16x32_bf16 v[20:23],v[240:243],v[52:55],v[20:23]
v_mfma_f32_16x16x32_bf16 v[24:27],v[84:87],v[52:55],v[24:27]
s_waitcnt lgkmcnt(2)
v_mfma_f32_16x16x32_bf16 v[12:15],v[244:247],v[56:59],v[12:15]
ds_read_b128 v[52:55],v2 offset:30720
v_mfma_f32_16x16x32_bf16 v[16:19],v[248:251],v[56:59],v[16:19]
v_mfma_f32_16x16x32_bf16 v[20:23],v[252:255],v[56:59],v[20:23]
s_waitcnt lgkmcnt(2)
v_mfma_f32_16x16x32_bf16 v[24:27],v[88:91],v[56:59],v[24:27]
s_cbranch_scc1 Lg4_slow1_%=
Lg4_d1w1_%=:
v_mfma_f32_16x16x32_bf16 v[28:31],a[0:3],v[44:47],0
s_mov_b32 s73,s48
ds_read_b128 v[56:59],v2 offset:31744
v_lshlrev_b32 v60,16,v68
v_mfma_f32_16x16x32_bf16 v[32:35],a[4:7],v[44:47],0
s_max_u32 s73,s73,s65
v_and_b32 v61,0xffff0000,v68
v_lshlrev_b32 v62,16,v69
v_mfma_f32_16x16x32_bf16 v[36:39],a[8:11],v[44:47],0
s_mov_b32 s65,0
v_and_b32 v63,0xffff0000,v69
v_lshlrev_b32 v64,16,v70
v_mfma_f32_16x16x32_bf16 v[40:43],a[12:15],v[44:47],0
s_waitcnt lgkmcnt(2)
v_and_b32 v65,0xffff0000,v70
v_lshlrev_b32 v66,16,v71
v_mfma_f32_16x16x32_bf16 v[28:31],a[16:19],v[48:51],v[28:31]
s_mul_i32 s61,s50,7
ds_read_b128 v[44:47],v2 offset:32768
v_and_b32 v67,0xffff0000,v71
v_mfma_f32_16x16x32_bf16 v[32:35],a[20:23],v[48:51],v[32:35]
s_lshr_b32 s61,s61,1
v_cvt_pk_bf16_f32 v12,v12,v13
v_cvt_pk_bf16_f32 v13,v14,v15
v_mfma_f32_16x16x32_bf16 v[36:39],a[24:27],v[48:51],v[36:39]
s_mov_b64 s[44:45],s[46:47]
v_cvt_pk_bf16_f32 v14,v16,v17
v_cvt_pk_bf16_f32 v15,v18,v19
v_mfma_f32_16x16x32_bf16 v[40:43],a[28:31],v[48:51],v[40:43]
s_waitcnt lgkmcnt(2)
v_add_f32 v20,v20,v60
v_add_f32 v21,v21,v61
v_mfma_f32_16x16x32_bf16 v[28:31],a[32:35],v[52:55],v[28:31]
s_mov_b64 s[46:47],s[48:49]
ds_read_b128 v[48:51],v2 offset:33792
v_add_f32 v22,v22,v62
v_mfma_f32_16x16x32_bf16 v[32:35],a[36:39],v[52:55],v[32:35]
buffer_store_dwordx4 v[12:15],v3,s[36:39],s73 offen offset:0 sc1
v_add_f32 v23,v23,v63
v_add_f32 v24,v24,v64
v_add_f32 v25,v25,v65
v_mfma_f32_16x16x32_bf16 v[36:39],a[40:43],v[52:55],v[36:39]
v_add_f32 v26,v26,v66
v_add_f32 v27,v27,v67
v_mfma_f32_16x16x32_bf16 v[40:43],a[44:47],v[52:55],v[40:43]
s_waitcnt lgkmcnt(2)
v_cvt_pk_bf16_f32 v20,v20,v21
v_cvt_pk_bf16_f32 v21,v22,v23
v_mfma_f32_16x16x32_bf16 v[28:31],a[48:51],v[56:59],v[28:31]
ds_read_b128 v[52:55],v2 offset:34816
v_cvt_pk_bf16_f32 v22,v24,v25
v_mfma_f32_16x16x32_bf16 v[32:35],a[52:55],v[56:59],v[32:35]
v_cvt_pk_bf16_f32 v23,v26,v27
v_mfma_f32_16x16x32_bf16 v[36:39],a[56:59],v[56:59],v[36:39]
s_waitcnt lgkmcnt(2)
s_nop 0
v_mfma_f32_16x16x32_bf16 v[40:43],a[60:63],v[56:59],v[40:43]
s_barrier
v_mfma_f32_16x16x32_bf16 v[28:31],a[64:67],v[44:47],v[28:31]
ds_read_b128 v[56:59],v2 offset:35840
ds_read_b32 v11,v8 offset:5120
v_mfma_f32_16x16x32_bf16 v[32:35],a[68:71],v[44:47],v[32:35]
v_mfma_f32_16x16x32_bf16 v[36:39],a[72:75],v[44:47],v[36:39]
v_mfma_f32_16x16x32_bf16 v[40:43],a[76:79],v[44:47],v[40:43]
s_waitcnt lgkmcnt(3)
v_mfma_f32_16x16x32_bf16 v[28:31],a[80:83],v[48:51],v[28:31]
s_waitcnt lgkmcnt(0)
ds_read_b128 v[44:47],v2 offset:36864
v_mfma_f32_16x16x32_bf16 v[32:35],a[84:87],v[48:51],v[32:35]
buffer_store_dwordx4 v[20:23],v4,s[36:39],s73 offen offset:0 sc1
v_cmp_eq_u32 vcc,s60,v11
v_mfma_f32_16x16x32_bf16 v[36:39],a[88:91],v[48:51],v[36:39]
s_cmp_eq_u32 vcc_lo,-1
s_cselect_b32 s67,s50,s59
s_cselect_b32 s61,s61,s59
v_mfma_f32_16x16x32_bf16 v[40:43],a[92:95],v[48:51],v[40:43]
s_add_u32 m0,s63,0
s_add_u32 s74,s53,s55
v_mfma_f32_16x16x32_bf16 v[28:31],a[96:99],v[52:55],v[28:31]
s_cmp_eq_u32 s74,s56
ds_read_b128 v[48:51],v2 offset:37888
s_cselect_b32 s74,s57,s74
v_mfma_f32_16x16x32_bf16 v[32:35],a[100:103],v[52:55],v[32:35]
buffer_load_dwordx4 v0,s[36:39],s61 offen offset:0 sc1 lds
v_mfma_f32_16x16x32_bf16 v[36:39],a[104:107],v[52:55],v[36:39]
s_cmp_eq_u32 s74,s58
s_cselect_b32 s74,s59,s74
s_cmp_ge_u32 s74,6520832
v_mfma_f32_16x16x32_bf16 v[40:43],a[108:111],v[52:55],v[40:43]
s_cselect_b32 s55,196608,s55
v_mfma_f32_16x16x32_bf16 v[28:31],a[112:115],v[56:59],v[28:31]
ds_read_b128 v[52:55],v2 offset:38912
v_mfma_f32_16x16x32_bf16 v[32:35],a[116:119],v[56:59],v[32:35]
buffer_load_dwordx4 v0,s[36:39],s61 offen offset:1024 sc1 lds
v_mfma_f32_16x16x32_bf16 v[36:39],a[120:123],v[56:59],v[36:39]
v_mfma_f32_16x16x32_bf16 v[40:43],a[124:127],v[56:59],v[40:43]
s_waitcnt lgkmcnt(2)
v_mfma_f32_16x16x32_bf16 v[28:31],a[128:131],v[44:47],v[28:31]
ds_read_b128 v[56:59],v2 offset:39936
v_mfma_f32_16x16x32_bf16 v[32:35],a[132:135],v[44:47],v[32:35]
buffer_load_dwordx4 v0,s[36:39],s61 offen offset:2048 sc1 lds
v_mfma_f32_16x16x32_bf16 v[36:39],a[136:139],v[44:47],v[36:39]
v_mfma_f32_16x16x32_bf16 v[40:43],a[140:143],v[44:47],v[40:43]
s_waitcnt lgkmcnt(2)
v_mfma_f32_16x16x32_bf16 v[28:31],a[144:147],v[48:51],v[28:31]
ds_read_b128 v[44:47],v2 offset:40960
v_mfma_f32_16x16x32_bf16 v[32:35],a[148:151],v[48:51],v[32:35]
buffer_load_dwordx4 v0,s[36:39],s61 offen offset:3072 sc1 lds
v_mfma_f32_16x16x32_bf16 v[36:39],a[152:155],v[48:51],v[36:39]
s_add_u32 m0,s64,0
s_cmp_lt_u32 s67,s59
v_mfma_f32_16x16x32_bf16 v[40:43],a[156:159],v[48:51],v[40:43]
s_waitcnt lgkmcnt(2)
v_mfma_f32_16x16x32_bf16 v[28:31],a[160:163],v[52:55],v[28:31]
ds_read_b128 v[48:51],v2 offset:41984
v_mfma_f32_16x16x32_bf16 v[32:35],a[164:167],v[52:55],v[32:35]
buffer_load_dwordx4 v[68:71],v9,s[36:39],s49 offen offset:0 sc1
v_mfma_f32_16x16x32_bf16 v[36:39],a[168:171],v[52:55],v[36:39]
s_mov_b64 s[48:49],s[50:51]
s_mov_b64 s[50:51],s[52:53]
s_mov_b32 s52,s74
v_mfma_f32_16x16x32_bf16 v[40:43],a[172:175],v[52:55],v[40:43]
s_waitcnt lgkmcnt(2)
v_mfma_f32_16x16x32_bf16 v[28:31],a[176:179],v[56:59],v[28:31]
ds_read_b128 v[52:55],v2 offset:43008
v_mfma_f32_16x16x32_bf16 v[32:35],a[180:183],v[56:59],v[32:35]
v_mfma_f32_16x16x32_bf16 v[36:39],a[184:187],v[56:59],v[36:39]
v_mfma_f32_16x16x32_bf16 v[40:43],a[188:191],v[56:59],v[40:43]
s_waitcnt lgkmcnt(2)
v_mfma_f32_16x16x32_bf16 v[28:31],a[192:195],v[44:47],v[28:31]
ds_read_b128 v[56:59],v2 offset:44032
v_mfma_f32_16x16x32_bf16 v[32:35],a[196:199],v[44:47],v[32:35]
v_mfma_f32_16x16x32_bf16 v[36:39],a[200:203],v[44:47],v[36:39]
s_waitcnt vmcnt(7) lgkmcnt(2)
v_mfma_f32_16x16x32_bf16 v[40:43],a[204:207],v[44:47],v[40:43]
s_barrier
v_mfma_f32_16x16x32_bf16 v[28:31],a[208:211],v[48:51],v[28:31]
ds_read_b128 v[44:47],v2 offset:45056
v_mfma_f32_16x16x32_bf16 v[32:35],a[212:215],v[48:51],v[32:35]
v_mfma_f32_16x16x32_bf16 v[36:39],a[216:219],v[48:51],v[36:39]
buffer_load_dwordx4 v1,s[36:39],s61 offen offset:0 sc1 lds
v_mfma_f32_16x16x32_bf16 v[40:43],a[220:223],v[48:51],v[40:43]
s_waitcnt lgkmcnt(2)
v_mfma_f32_16x16x32_bf16 v[28:31],a[224:227],v[52:55],v[28:31]
ds_read_b128 v[48:51],v2 offset:46080
v_mfma_f32_16x16x32_bf16 v[32:35],a[228:231],v[52:55],v[32:35]
v_mfma_f32_16x16x32_bf16 v[36:39],a[232:235],v[52:55],v[36:39]
v_mfma_f32_16x16x32_bf16 v[40:43],a[236:239],v[52:55],v[40:43]
s_waitcnt lgkmcnt(2)
v_mfma_f32_16x16x32_bf16 v[28:31],a[240:243],v[56:59],v[28:31]
buffer_load_dwordx4 v1,s[36:39],s61 offen offset:1024 sc1 lds
v_mfma_f32_16x16x32_bf16 v[32:35],a[244:247],v[56:59],v[32:35]
ds_read_b128 v[52:55],v2 offset:47104
v_mfma_f32_16x16x32_bf16 v[36:39],a[248:251],v[56:59],v[36:39]
v_mfma_f32_16x16x32_bf16 v[40:43],a[252:255],v[56:59],v[40:43]
s_waitcnt lgkmcnt(2)
v_mfma_f32_16x16x32_bf16 v[28:31],v[100:103],v[44:47],v[28:31]
ds_read_b128 v[56:59],v2 offset:48128
v_mfma_f32_16x16x32_bf16 v[32:35],v[104:107],v[44:47],v[32:35]
ds_read_b128 v[88:91],v10 offset:0
v_mfma_f32_16x16x32_bf16 v[36:39],v[108:111],v[44:47],v[36:39]
buffer_load_dwordx4 v1,s[36:39],s61 offen offset:2048 sc1 lds
v_mfma_f32_16x16x32_bf16 v[40:43],v[112:115],v[44:47],v[40:43]
s_waitcnt lgkmcnt(3)
v_mfma_f32_16x16x32_bf16 v[28:31],v[116:119],v[48:51],v[28:31]
ds_read_b128 v[44:47],v2 offset:49152)"
R"(
v_mfma_f32_16x16x32_bf16 v[32:35],v[120:123],v[48:51],v[32:35]
ds_read_b128 v[76:79],v10 offset:1024
v_mfma_f32_16x16x32_bf16 v[36:39],v[124:127],v[48:51],v[36:39]
v_mfma_f32_16x16x32_bf16 v[40:43],v[128:131],v[48:51],v[40:43]
s_waitcnt lgkmcnt(4)
v_mfma_f32_16x16x32_bf16 v[28:31],v[132:135],v[52:55],v[28:31]
ds_read_b128 v[48:51],v2 offset:50176
v_mfma_f32_16x16x32_bf16 v[32:35],v[136:139],v[52:55],v[32:35]
ds_read_b128 v[80:83],v10 offset:2048
v_mfma_f32_16x16x32_bf16 v[36:39],v[140:143],v[52:55],v[36:39]
v_mfma_f32_16x16x32_bf16 v[40:43],v[144:147],v[52:55],v[40:43]
s_waitcnt lgkmcnt(4)
v_mfma_f32_16x16x32_bf16 v[28:31],v[148:151],v[56:59],v[28:31]
ds_read_b128 v[52:55],v2 offset:51200
v_mfma_f32_16x16x32_bf16 v[32:35],v[152:155],v[56:59],v[32:35]
ds_read_b128 v[84:87],v10 offset:3072
v_mfma_f32_16x16x32_bf16 v[36:39],v[156:159],v[56:59],v[36:39]
v_mfma_f32_16x16x32_bf16 v[40:43],v[88:91],v[56:59],v[40:43]
s_waitcnt lgkmcnt(4)
v_mfma_f32_16x16x32_bf16 v[28:31],v[160:163],v[44:47],v[28:31]
ds_read_b128 v[56:59],v2 offset:52224
v_mfma_f32_16x16x32_bf16 v[32:35],v[164:167],v[44:47],v[32:35]
ds_read_b128 v[88:91],v10 offset:4096
v_mfma_f32_16x16x32_bf16 v[36:39],v[168:171],v[44:47],v[36:39]
v_mfma_f32_16x16x32_bf16 v[40:43],v[76:79],v[44:47],v[40:43]
s_waitcnt lgkmcnt(4)
v_mfma_f32_16x16x32_bf16 v[28:31],v[172:175],v[48:51],v[28:31]
ds_read_b128 v[44:47],v2 offset:53248
v_mfma_f32_16x16x32_bf16 v[32:35],v[176:179],v[48:51],v[32:35]
ds_read_b128 v[76:79],v10 offset:5120
v_mfma_f32_16x16x32_bf16 v[36:39],v[180:183],v[48:51],v[36:39]
s_waitcnt vmcnt(8) lgkmcnt(4)
v_mfma_f32_16x16x32_bf16 v[40:43],v[80:83],v[48:51],v[40:43]
s_barrier
v_mfma_f32_16x16x32_bf16 v[28:31],v[184:187],v[52:55],v[28:31]
ds_read_b128 v[48:51],v2 offset:54272
v_mfma_f32_16x16x32_bf16 v[32:35],v[188:191],v[52:55],v[32:35]
ds_read_b128 v[80:83],v10 offset:6144
v_mfma_f32_16x16x32_bf16 v[36:39],v[192:195],v[52:55],v[36:39]
v_mfma_f32_16x16x32_bf16 v[40:43],v[84:87],v[52:55],v[40:43]
s_waitcnt lgkmcnt(4)
v_mfma_f32_16x16x32_bf16 v[28:31],v[196:199],v[56:59],v[28:31]
ds_read_b128 v[52:55],v2 offset:55296
v_mfma_f32_16x16x32_bf16 v[32:35],v[200:203],v[56:59],v[32:35]
ds_read_b128 v[84:87],v10 offset:7168
v_mfma_f32_16x16x32_bf16 v[36:39],v[204:207],v[56:59],v[36:39]
v_mfma_f32_16x16x32_bf16 v[40:43],v[88:91],v[56:59],v[40:43]
s_waitcnt lgkmcnt(4)
v_mfma_f32_16x16x32_bf16 v[28:31],v[208:211],v[44:47],v[28:31]
ds_read_b128 v[56:59],v2 offset:56320
v_mfma_f32_16x16x32_bf16 v[32:35],v[212:215],v[44:47],v[32:35]
ds_read_b128 v[88:91],v10 offset:8192
v_mfma_f32_16x16x32_bf16 v[36:39],v[216:219],v[44:47],v[36:39]
s_waitcnt vmcnt(3) lgkmcnt(4)
v_mfma_f32_16x16x32_bf16 v[40:43],v[76:79],v[44:47],v[40:43]
s_barrier
v_mfma_f32_16x16x32_bf16 v[28:31],v[220:223],v[48:51],v[28:31]
ds_read_b128 v[44:47],v2 offset:0
v_mfma_f32_16x16x32_bf16 v[32:35],v[224:227],v[48:51],v[32:35]
v_mfma_f32_16x16x32_bf16 v[36:39],v[228:231],v[48:51],v[36:39]
v_mfma_f32_16x16x32_bf16 v[40:43],v[80:83],v[48:51],v[40:43]
s_waitcnt lgkmcnt(3)
v_mfma_f32_16x16x32_bf16 v[28:31],v[232:235],v[52:55],v[28:31]
ds_read_b128 v[48:51],v2 offset:1024
v_mfma_f32_16x16x32_bf16 v[32:35],v[236:239],v[52:55],v[32:35]
v_mfma_f32_16x16x32_bf16 v[36:39],v[240:243],v[52:55],v[36:39]
v_mfma_f32_16x16x32_bf16 v[40:43],v[84:87],v[52:55],v[40:43]
s_waitcnt lgkmcnt(2)
v_mfma_f32_16x16x32_bf16 v[28:31],v[244:247],v[56:59],v[28:31]
ds_read_b128 v[52:55],v2 offset:2048
v_mfma_f32_16x16x32_bf16 v[32:35],v[248:251],v[56:59],v[32:35]
v_mfma_f32_16x16x32_bf16 v[36:39],v[252:255],v[56:59],v[36:39]
s_waitcnt lgkmcnt(2)
v_mfma_f32_16x16x32_bf16 v[40:43],v[88:91],v[56:59],v[40:43]
s_cbranch_scc1 Lg4_d0w1_%=
s_branch Lg4_slow0_%=
Lg4_d0w2_%=:
v_mfma_f32_16x16x32_bf16 v[12:15],a[0:3],v[44:47],0
s_mov_b32 s73,s47
ds_read_b128 v[56:59],v2 offset:3072
v_lshlrev_b32 v60,16,v68
v_mfma_f32_16x16x32_bf16 v[16:19],a[4:7],v[44:47],0
s_max_u32 s73,s73,s65
v_and_b32 v61,0xffff0000,v68
v_lshlrev_b32 v62,16,v69
v_mfma_f32_16x16x32_bf16 v[20:23],a[8:11],v[44:47],0
s_mov_b32 s65,0
v_and_b32 v63,0xffff0000,v69
v_lshlrev_b32 v64,16,v70
v_mfma_f32_16x16x32_bf16 v[24:27],a[12:15],v[44:47],0
s_waitcnt lgkmcnt(2)
v_and_b32 v65,0xffff0000,v70
v_lshlrev_b32 v66,16,v71
v_mfma_f32_16x16x32_bf16 v[12:15],a[16:19],v[48:51],v[12:15]
s_mul_i32 s61,s49,7
ds_read_b128 v[44:47],v2 offset:4096
v_and_b32 v67,0xffff0000,v71
v_mfma_f32_16x16x32_bf16 v[16:19],a[20:23],v[48:51],v[16:19]
s_lshr_b32 s61,s61,1
v_cvt_pk_bf16_f32 v28,v28,v29
v_cvt_pk_bf16_f32 v29,v30,v31
v_mfma_f32_16x16x32_bf16 v[20:23],a[24:27],v[48:51],v[20:23]
v_cvt_pk_bf16_f32 v30,v32,v33
v_cvt_pk_bf16_f32 v31,v34,v35
v_mfma_f32_16x16x32_bf16 v[24:27],a[28:31],v[48:51],v[24:27]
s_waitcnt lgkmcnt(2)
v_add_f32 v36,v36,v60
v_add_f32 v37,v37,v61
v_mfma_f32_16x16x32_bf16 v[12:15],a[32:35],v[52:55],v[12:15]
ds_read_b128 v[48:51],v2 offset:5120
v_add_f32 v38,v38,v62
v_mfma_f32_16x16x32_bf16 v[16:19],a[36:39],v[52:55],v[16:19]
v_add_f32 v39,v39,v63
v_add_f32 v40,v40,v64
v_mfma_f32_16x16x32_bf16 v[20:23],a[40:43],v[52:55],v[20:23]
buffer_store_dwordx4 v[28:31],v3,s[36:39],s73 offen offset:0 sc1
v_add_f32 v41,v41,v65
v_add_f32 v42,v42,v66
v_add_f32 v43,v43,v67
v_mfma_f32_16x16x32_bf16 v[24:27],a[44:47],v[52:55],v[24:27]
s_waitcnt lgkmcnt(2)
v_cvt_pk_bf16_f32 v36,v36,v37
v_cvt_pk_bf16_f32 v37,v38,v39
v_mfma_f32_16x16x32_bf16 v[12:15],a[48:51],v[56:59],v[12:15]
ds_read_b128 v[52:55],v2 offset:6144
v_cvt_pk_bf16_f32 v38,v40,v41
v_mfma_f32_16x16x32_bf16 v[16:19],a[52:55],v[56:59],v[16:19]
v_cvt_pk_bf16_f32 v39,v42,v43
v_mfma_f32_16x16x32_bf16 v[20:23],a[56:59],v[56:59],v[20:23]
s_waitcnt lgkmcnt(2)
s_nop 0
v_mfma_f32_16x16x32_bf16 v[24:27],a[60:63],v[56:59],v[24:27]
s_barrier
v_mfma_f32_16x16x32_bf16 v[12:15],a[64:67],v[44:47],v[12:15]
ds_read_b128 v[56:59],v2 offset:7168
ds_read_b32 v11,v8 offset:4096
v_mfma_f32_16x16x32_bf16 v[16:19],a[68:71],v[44:47],v[16:19]
v_mfma_f32_16x16x32_bf16 v[20:23],a[72:75],v[44:47],v[20:23]
v_mfma_f32_16x16x32_bf16 v[24:27],a[76:79],v[44:47],v[24:27]
s_waitcnt lgkmcnt(3)
v_mfma_f32_16x16x32_bf16 v[12:15],a[80:83],v[48:51],v[12:15]
s_waitcnt lgkmcnt(0)
ds_read_b128 v[44:47],v2 offset:8192
v_mfma_f32_16x16x32_bf16 v[16:19],a[84:87],v[48:51],v[16:19]
v_mfma_f32_16x16x32_bf16 v[20:23],a[88:91],v[48:51],v[20:23]
buffer_store_dwordx4 v[36:39],v4,s[36:39],s73 offen offset:0 sc1
v_cmp_eq_u32 vcc,s60,v11
v_mfma_f32_16x16x32_bf16 v[24:27],a[92:95],v[48:51],v[24:27]
s_cmp_eq_u32 vcc_lo,-1
s_cselect_b32 s66,s49,s59
v_mfma_f32_16x16x32_bf16 v[12:15],a[96:99],v[52:55],v[12:15]
s_cselect_b32 s61,s61,s59
ds_read_b128 v[48:51],v2 offset:9216
s_add_u32 m0,s63,28672
v_mfma_f32_16x16x32_bf16 v[16:19],a[100:103],v[52:55],v[16:19]
s_add_u32 s53,s52,s55
s_cmp_eq_u32 s53,s56
s_cselect_b32 s53,s57,s53
v_mfma_f32_16x16x32_bf16 v[20:23],a[104:107],v[52:55],v[20:23]
buffer_load_dwordx4 v0,s[36:39],s61 offen offset:0 sc1 lds
v_mfma_f32_16x16x32_bf16 v[24:27],a[108:111],v[52:55],v[24:27]
s_cmp_eq_u32 s53,s58
s_cselect_b32 s53,s59,s53
v_mfma_f32_16x16x32_bf16 v[12:15],a[112:115],v[56:59],v[12:15]
s_cmp_ge_u32 s53,6520832
ds_read_b128 v[52:55],v2 offset:10240
s_cselect_b32 s55,196608,s55
v_mfma_f32_16x16x32_bf16 v[16:19],a[116:119],v[56:59],v[16:19]
v_mfma_f32_16x16x32_bf16 v[20:23],a[120:123],v[56:59],v[20:23]
buffer_load_dwordx4 v0,s[36:39],s61 offen offset:1024 sc1 lds
v_mfma_f32_16x16x32_bf16 v[24:27],a[124:127],v[56:59],v[24:27]
s_waitcnt lgkmcnt(2)
v_mfma_f32_16x16x32_bf16 v[12:15],a[128:131],v[44:47],v[12:15]
ds_read_b128 v[56:59],v2 offset:11264
v_mfma_f32_16x16x32_bf16 v[16:19],a[132:135],v[44:47],v[16:19]
v_mfma_f32_16x16x32_bf16 v[20:23],a[136:139],v[44:47],v[20:23]
buffer_load_dwordx4 v0,s[36:39],s61 offen offset:2048 sc1 lds
v_mfma_f32_16x16x32_bf16 v[24:27],a[140:143],v[44:47],v[24:27]
s_waitcnt lgkmcnt(2)
v_mfma_f32_16x16x32_bf16 v[12:15],a[144:147],v[48:51],v[12:15]
ds_read_b128 v[44:47],v2 offset:12288
v_mfma_f32_16x16x32_bf16 v[16:19],a[148:151],v[48:51],v[16:19]
v_mfma_f32_16x16x32_bf16 v[20:23],a[152:155],v[48:51],v[20:23]
buffer_load_dwordx4 v0,s[36:39],s61 offen offset:3072 sc1 lds
v_mfma_f32_16x16x32_bf16 v[24:27],a[156:159],v[48:51],v[24:27]
s_waitcnt lgkmcnt(2)
s_add_u32 m0,s64,28672
s_cmp_ge_u32 s66,s59
v_mfma_f32_16x16x32_bf16 v[12:15],a[160:163],v[52:55],v[12:15]
ds_read_b128 v[48:51],v2 offset:13312
v_mfma_f32_16x16x32_bf16 v[16:19],a[164:167],v[52:55],v[16:19]
v_mfma_f32_16x16x32_bf16 v[20:23],a[168:171],v[52:55],v[20:23]
buffer_load_dwordx4 v[68:71],v9,s[36:39],s48 offen offset:0 sc1
v_mfma_f32_16x16x32_bf16 v[24:27],a[172:175],v[52:55],v[24:27]
s_waitcnt lgkmcnt(2)
v_mfma_f32_16x16x32_bf16 v[12:15],a[176:179],v[56:59],v[12:15]
ds_read_b128 v[52:55],v2 offset:14336
v_mfma_f32_16x16x32_bf16 v[16:19],a[180:183],v[56:59],v[16:19]
v_mfma_f32_16x16x32_bf16 v[20:23],a[184:187],v[56:59],v[20:23]
v_mfma_f32_16x16x32_bf16 v[24:27],a[188:191],v[56:59],v[24:27])"
R"(
s_waitcnt lgkmcnt(2)
v_mfma_f32_16x16x32_bf16 v[12:15],a[192:195],v[44:47],v[12:15]
ds_read_b128 v[56:59],v2 offset:15360
v_mfma_f32_16x16x32_bf16 v[16:19],a[196:199],v[44:47],v[16:19]
v_mfma_f32_16x16x32_bf16 v[20:23],a[200:203],v[44:47],v[20:23]
s_waitcnt vmcnt(7) lgkmcnt(2)
v_mfma_f32_16x16x32_bf16 v[24:27],a[204:207],v[44:47],v[24:27]
s_barrier
v_mfma_f32_16x16x32_bf16 v[12:15],a[208:211],v[48:51],v[12:15]
ds_read_b128 v[44:47],v2 offset:16384
v_mfma_f32_16x16x32_bf16 v[16:19],a[212:215],v[48:51],v[16:19]
v_mfma_f32_16x16x32_bf16 v[20:23],a[216:219],v[48:51],v[20:23]
v_mfma_f32_16x16x32_bf16 v[24:27],a[220:223],v[48:51],v[24:27]
s_waitcnt lgkmcnt(2)
buffer_load_dwordx4 v1,s[36:39],s61 offen offset:0 sc1 lds
v_mfma_f32_16x16x32_bf16 v[12:15],a[224:227],v[52:55],v[12:15]
ds_read_b128 v[48:51],v2 offset:17408
v_mfma_f32_16x16x32_bf16 v[16:19],a[228:231],v[52:55],v[16:19]
v_mfma_f32_16x16x32_bf16 v[20:23],a[232:235],v[52:55],v[20:23]
v_mfma_f32_16x16x32_bf16 v[24:27],a[236:239],v[52:55],v[24:27]
s_waitcnt lgkmcnt(2)
v_mfma_f32_16x16x32_bf16 v[12:15],a[240:243],v[56:59],v[12:15]
ds_read_b128 v[52:55],v2 offset:18432
v_mfma_f32_16x16x32_bf16 v[16:19],a[244:247],v[56:59],v[16:19]
buffer_load_dwordx4 v1,s[36:39],s61 offen offset:1024 sc1 lds
v_mfma_f32_16x16x32_bf16 v[20:23],a[248:251],v[56:59],v[20:23]
v_mfma_f32_16x16x32_bf16 v[24:27],a[252:255],v[56:59],v[24:27]
s_waitcnt lgkmcnt(2)
v_mfma_f32_16x16x32_bf16 v[12:15],v[100:103],v[44:47],v[12:15]
ds_read_b128 v[56:59],v2 offset:19456
v_mfma_f32_16x16x32_bf16 v[16:19],v[104:107],v[44:47],v[16:19]
ds_read_b128 v[88:91],v10 offset:0
v_mfma_f32_16x16x32_bf16 v[20:23],v[108:111],v[44:47],v[20:23]
v_mfma_f32_16x16x32_bf16 v[24:27],v[112:115],v[44:47],v[24:27]
s_waitcnt lgkmcnt(3)
buffer_load_dwordx4 v1,s[36:39],s61 offen offset:2048 sc1 lds
v_mfma_f32_16x16x32_bf16 v[12:15],v[116:119],v[48:51],v[12:15]
ds_read_b128 v[44:47],v2 offset:20480
v_mfma_f32_16x16x32_bf16 v[16:19],v[120:123],v[48:51],v[16:19]
ds_read_b128 v[76:79],v10 offset:1024
v_mfma_f32_16x16x32_bf16 v[20:23],v[124:127],v[48:51],v[20:23]
v_mfma_f32_16x16x32_bf16 v[24:27],v[128:131],v[48:51],v[24:27]
s_waitcnt lgkmcnt(4)
v_mfma_f32_16x16x32_bf16 v[12:15],v[132:135],v[52:55],v[12:15]
ds_read_b128 v[48:51],v2 offset:21504
v_mfma_f32_16x16x32_bf16 v[16:19],v[136:139],v[52:55],v[16:19]
ds_read_b128 v[80:83],v10 offset:2048
v_mfma_f32_16x16x32_bf16 v[20:23],v[140:143],v[52:55],v[20:23]
v_mfma_f32_16x16x32_bf16 v[24:27],v[144:147],v[52:55],v[24:27]
s_waitcnt lgkmcnt(4)
v_mfma_f32_16x16x32_bf16 v[12:15],v[148:151],v[56:59],v[12:15]
ds_read_b128 v[52:55],v2 offset:22528
v_mfma_f32_16x16x32_bf16 v[16:19],v[152:155],v[56:59],v[16:19]
ds_read_b128 v[84:87],v10 offset:3072
v_mfma_f32_16x16x32_bf16 v[20:23],v[156:159],v[56:59],v[20:23]
v_mfma_f32_16x16x32_bf16 v[24:27],v[88:91],v[56:59],v[24:27]
s_waitcnt lgkmcnt(4)
v_mfma_f32_16x16x32_bf16 v[12:15],v[160:163],v[44:47],v[12:15]
ds_read_b128 v[56:59],v2 offset:23552
v_mfma_f32_16x16x32_bf16 v[16:19],v[164:167],v[44:47],v[16:19]
ds_read_b128 v[88:91],v10 offset:4096
v_mfma_f32_16x16x32_bf16 v[20:23],v[168:171],v[44:47],v[20:23]
v_mfma_f32_16x16x32_bf16 v[24:27],v[76:79],v[44:47],v[24:27]
s_waitcnt lgkmcnt(4)
v_mfma_f32_16x16x32_bf16 v[12:15],v[172:175],v[48:51],v[12:15]
ds_read_b128 v[44:47],v2 offset:24576
v_mfma_f32_16x16x32_bf16 v[16:19],v[176:179],v[48:51],v[16:19]
ds_read_b128 v[76:79],v10 offset:5120
v_mfma_f32_16x16x32_bf16 v[20:23],v[180:183],v[48:51],v[20:23]
s_waitcnt vmcnt(8) lgkmcnt(4)
v_mfma_f32_16x16x32_bf16 v[24:27],v[80:83],v[48:51],v[24:27]
s_barrier
v_mfma_f32_16x16x32_bf16 v[12:15],v[184:187],v[52:55],v[12:15]
ds_read_b128 v[48:51],v2 offset:25600
v_mfma_f32_16x16x32_bf16 v[16:19],v[188:191],v[52:55],v[16:19]
ds_read_b128 v[80:83],v10 offset:6144
v_mfma_f32_16x16x32_bf16 v[20:23],v[192:195],v[52:55],v[20:23]
v_mfma_f32_16x16x32_bf16 v[24:27],v[84:87],v[52:55],v[24:27]
s_waitcnt lgkmcnt(4)
v_mfma_f32_16x16x32_bf16 v[12:15],v[196:199],v[56:59],v[12:15]
ds_read_b128 v[52:55],v2 offset:26624
v_mfma_f32_16x16x32_bf16 v[16:19],v[200:203],v[56:59],v[16:19]
ds_read_b128 v[84:87],v10 offset:7168
v_mfma_f32_16x16x32_bf16 v[20:23],v[204:207],v[56:59],v[20:23]
v_mfma_f32_16x16x32_bf16 v[24:27],v[88:91],v[56:59],v[24:27]
s_waitcnt lgkmcnt(4)
v_mfma_f32_16x16x32_bf16 v[12:15],v[208:211],v[44:47],v[12:15]
ds_read_b128 v[56:59],v2 offset:27648
v_mfma_f32_16x16x32_bf16 v[16:19],v[212:215],v[44:47],v[16:19]
ds_read_b128 v[88:91],v10 offset:8192
v_mfma_f32_16x16x32_bf16 v[20:23],v[216:219],v[44:47],v[20:23]
s_waitcnt vmcnt(3) lgkmcnt(4)
v_mfma_f32_16x16x32_bf16 v[24:27],v[76:79],v[44:47],v[24:27]
s_barrier
v_mfma_f32_16x16x32_bf16 v[12:15],v[220:223],v[48:51],v[12:15]
ds_read_b128 v[44:47],v2 offset:28672
v_mfma_f32_16x16x32_bf16 v[16:19],v[224:227],v[48:51],v[16:19]
v_mfma_f32_16x16x32_bf16 v[20:23],v[228:231],v[48:51],v[20:23]
v_mfma_f32_16x16x32_bf16 v[24:27],v[80:83],v[48:51],v[24:27]
s_waitcnt lgkmcnt(3)
v_mfma_f32_16x16x32_bf16 v[12:15],v[232:235],v[52:55],v[12:15]
ds_read_b128 v[48:51],v2 offset:29696
v_mfma_f32_16x16x32_bf16 v[16:19],v[236:239],v[52:55],v[16:19]
v_mfma_f32_16x16x32_bf16 v[20:23],v[240:243],v[52:55],v[20:23]
v_mfma_f32_16x16x32_bf16 v[24:27],v[84:87],v[52:55],v[24:27]
s_waitcnt lgkmcnt(2)
v_mfma_f32_16x16x32_bf16 v[12:15],v[244:247],v[56:59],v[12:15]
ds_read_b128 v[52:55],v2 offset:30720
v_mfma_f32_16x16x32_bf16 v[16:19],v[248:251],v[56:59],v[16:19]
v_mfma_f32_16x16x32_bf16 v[20:23],v[252:255],v[56:59],v[20:23]
s_waitcnt lgkmcnt(2)
v_mfma_f32_16x16x32_bf16 v[24:27],v[88:91],v[56:59],v[24:27]
s_cbranch_scc1 Lg4_slow1_%=
Lg4_d1w2_%=:
v_mfma_f32_16x16x32_bf16 v[28:31],a[0:3],v[44:47],0
s_mov_b32 s73,s48
ds_read_b128 v[56:59],v2 offset:31744
v_lshlrev_b32 v60,16,v68
v_mfma_f32_16x16x32_bf16 v[32:35],a[4:7],v[44:47],0
s_max_u32 s73,s73,s65
v_and_b32 v61,0xffff0000,v68
v_lshlrev_b32 v62,16,v69
v_mfma_f32_16x16x32_bf16 v[36:39],a[8:11],v[44:47],0
s_mov_b32 s65,0
v_and_b32 v63,0xffff0000,v69
v_lshlrev_b32 v64,16,v70
v_mfma_f32_16x16x32_bf16 v[40:43],a[12:15],v[44:47],0
s_waitcnt lgkmcnt(2)
v_and_b32 v65,0xffff0000,v70
v_lshlrev_b32 v66,16,v71
v_mfma_f32_16x16x32_bf16 v[28:31],a[16:19],v[48:51],v[28:31]
s_mul_i32 s61,s50,7
ds_read_b128 v[44:47],v2 offset:32768
v_and_b32 v67,0xffff0000,v71
v_mfma_f32_16x16x32_bf16 v[32:35],a[20:23],v[48:51],v[32:35]
s_lshr_b32 s61,s61,1
v_cvt_pk_bf16_f32 v12,v12,v13
v_cvt_pk_bf16_f32 v13,v14,v15
v_mfma_f32_16x16x32_bf16 v[36:39],a[24:27],v[48:51],v[36:39]
s_mov_b64 s[44:45],s[46:47]
v_cvt_pk_bf16_f32 v14,v16,v17
v_cvt_pk_bf16_f32 v15,v18,v19
v_mfma_f32_16x16x32_bf16 v[40:43],a[28:31],v[48:51],v[40:43]
s_waitcnt lgkmcnt(2)
v_add_f32 v20,v20,v60
v_add_f32 v21,v21,v61
v_mfma_f32_16x16x32_bf16 v[28:31],a[32:35],v[52:55],v[28:31]
s_mov_b64 s[46:47],s[48:49]
ds_read_b128 v[48:51],v2 offset:33792
v_add_f32 v22,v22,v62
v_mfma_f32_16x16x32_bf16 v[32:35],a[36:39],v[52:55],v[32:35]
v_add_f32 v23,v23,v63
v_add_f32 v24,v24,v64
v_mfma_f32_16x16x32_bf16 v[36:39],a[40:43],v[52:55],v[36:39]
buffer_store_dwordx4 v[12:15],v3,s[36:39],s73 offen offset:0 sc1
v_add_f32 v25,v25,v65
v_add_f32 v26,v26,v66
v_add_f32 v27,v27,v67
v_mfma_f32_16x16x32_bf16 v[40:43],a[44:47],v[52:55],v[40:43]
s_waitcnt lgkmcnt(2)
v_cvt_pk_bf16_f32 v20,v20,v21
v_cvt_pk_bf16_f32 v21,v22,v23
v_mfma_f32_16x16x32_bf16 v[28:31],a[48:51],v[56:59],v[28:31]
ds_read_b128 v[52:55],v2 offset:34816
v_cvt_pk_bf16_f32 v22,v24,v25
v_mfma_f32_16x16x32_bf16 v[32:35],a[52:55],v[56:59],v[32:35]
v_cvt_pk_bf16_f32 v23,v26,v27
v_mfma_f32_16x16x32_bf16 v[36:39],a[56:59],v[56:59],v[36:39]
s_waitcnt lgkmcnt(2)
s_nop 0
v_mfma_f32_16x16x32_bf16 v[40:43],a[60:63],v[56:59],v[40:43]
s_barrier
v_mfma_f32_16x16x32_bf16 v[28:31],a[64:67],v[44:47],v[28:31]
ds_read_b128 v[56:59],v2 offset:35840
ds_read_b32 v11,v8 offset:5120
v_mfma_f32_16x16x32_bf16 v[32:35],a[68:71],v[44:47],v[32:35]
v_mfma_f32_16x16x32_bf16 v[36:39],a[72:75],v[44:47],v[36:39]
v_mfma_f32_16x16x32_bf16 v[40:43],a[76:79],v[44:47],v[40:43]
s_waitcnt lgkmcnt(3)
v_mfma_f32_16x16x32_bf16 v[28:31],a[80:83],v[48:51],v[28:31]
s_waitcnt lgkmcnt(0)
ds_read_b128 v[44:47],v2 offset:36864
v_mfma_f32_16x16x32_bf16 v[32:35],a[84:87],v[48:51],v[32:35]
v_mfma_f32_16x16x32_bf16 v[36:39],a[88:91],v[48:51],v[36:39]
buffer_store_dwordx4 v[20:23],v4,s[36:39],s73 offen offset:0 sc1
v_cmp_eq_u32 vcc,s60,v11
v_mfma_f32_16x16x32_bf16 v[40:43],a[92:95],v[48:51],v[40:43]
s_cmp_eq_u32 vcc_lo,-1
s_cselect_b32 s67,s50,s59
v_mfma_f32_16x16x32_bf16 v[28:31],a[96:99],v[52:55],v[28:31]
s_cselect_b32 s61,s61,s59
ds_read_b128 v[48:51],v2 offset:37888
s_add_u32 m0,s63,0
v_mfma_f32_16x16x32_bf16 v[32:35],a[100:103],v[52:55],v[32:35]
s_add_u32 s74,s53,s55
s_cmp_eq_u32 s74,s56
s_cselect_b32 s74,s57,s74
v_mfma_f32_16x16x32_bf16 v[36:39],a[104:107],v[52:55],v[36:39]
buffer_load_dwordx4 v0,s[36:39],s61 offen offset:0 sc1 lds
v_mfma_f32_16x16x32_bf16 v[40:43],a[108:111],v[52:55],v[40:43])"
R"(
s_cmp_eq_u32 s74,s58
s_cselect_b32 s74,s59,s74
v_mfma_f32_16x16x32_bf16 v[28:31],a[112:115],v[56:59],v[28:31]
s_cmp_ge_u32 s74,6520832
ds_read_b128 v[52:55],v2 offset:38912
s_cselect_b32 s55,196608,s55
v_mfma_f32_16x16x32_bf16 v[32:35],a[116:119],v[56:59],v[32:35]
v_mfma_f32_16x16x32_bf16 v[36:39],a[120:123],v[56:59],v[36:39]
buffer_load_dwordx4 v0,s[36:39],s61 offen offset:1024 sc1 lds
v_mfma_f32_16x16x32_bf16 v[40:43],a[124:127],v[56:59],v[40:43]
s_waitcnt lgkmcnt(2)
v_mfma_f32_16x16x32_bf16 v[28:31],a[128:131],v[44:47],v[28:31]
ds_read_b128 v[56:59],v2 offset:39936
v_mfma_f32_16x16x32_bf16 v[32:35],a[132:135],v[44:47],v[32:35]
v_mfma_f32_16x16x32_bf16 v[36:39],a[136:139],v[44:47],v[36:39]
buffer_load_dwordx4 v0,s[36:39],s61 offen offset:2048 sc1 lds
v_mfma_f32_16x16x32_bf16 v[40:43],a[140:143],v[44:47],v[40:43]
s_waitcnt lgkmcnt(2)
v_mfma_f32_16x16x32_bf16 v[28:31],a[144:147],v[48:51],v[28:31]
ds_read_b128 v[44:47],v2 offset:40960
v_mfma_f32_16x16x32_bf16 v[32:35],a[148:151],v[48:51],v[32:35]
v_mfma_f32_16x16x32_bf16 v[36:39],a[152:155],v[48:51],v[36:39]
buffer_load_dwordx4 v0,s[36:39],s61 offen offset:3072 sc1 lds
v_mfma_f32_16x16x32_bf16 v[40:43],a[156:159],v[48:51],v[40:43]
s_waitcnt lgkmcnt(2)
s_add_u32 m0,s64,0
s_cmp_lt_u32 s67,s59
v_mfma_f32_16x16x32_bf16 v[28:31],a[160:163],v[52:55],v[28:31]
ds_read_b128 v[48:51],v2 offset:41984
v_mfma_f32_16x16x32_bf16 v[32:35],a[164:167],v[52:55],v[32:35]
v_mfma_f32_16x16x32_bf16 v[36:39],a[168:171],v[52:55],v[36:39]
buffer_load_dwordx4 v[68:71],v9,s[36:39],s49 offen offset:0 sc1
v_mfma_f32_16x16x32_bf16 v[40:43],a[172:175],v[52:55],v[40:43]
s_waitcnt lgkmcnt(2)
s_mov_b64 s[48:49],s[50:51]
s_mov_b64 s[50:51],s[52:53]
v_mfma_f32_16x16x32_bf16 v[28:31],a[176:179],v[56:59],v[28:31]
s_mov_b32 s52,s74
ds_read_b128 v[52:55],v2 offset:43008
v_mfma_f32_16x16x32_bf16 v[32:35],a[180:183],v[56:59],v[32:35]
v_mfma_f32_16x16x32_bf16 v[36:39],a[184:187],v[56:59],v[36:39]
v_mfma_f32_16x16x32_bf16 v[40:43],a[188:191],v[56:59],v[40:43]
s_waitcnt lgkmcnt(2)
v_mfma_f32_16x16x32_bf16 v[28:31],a[192:195],v[44:47],v[28:31]
ds_read_b128 v[56:59],v2 offset:44032
v_mfma_f32_16x16x32_bf16 v[32:35],a[196:199],v[44:47],v[32:35]
v_mfma_f32_16x16x32_bf16 v[36:39],a[200:203],v[44:47],v[36:39]
s_waitcnt vmcnt(7) lgkmcnt(2)
v_mfma_f32_16x16x32_bf16 v[40:43],a[204:207],v[44:47],v[40:43]
s_barrier
v_mfma_f32_16x16x32_bf16 v[28:31],a[208:211],v[48:51],v[28:31]
ds_read_b128 v[44:47],v2 offset:45056
v_mfma_f32_16x16x32_bf16 v[32:35],a[212:215],v[48:51],v[32:35]
v_mfma_f32_16x16x32_bf16 v[36:39],a[216:219],v[48:51],v[36:39]
v_mfma_f32_16x16x32_bf16 v[40:43],a[220:223],v[48:51],v[40:43]
s_waitcnt lgkmcnt(2)
buffer_load_dwordx4 v1,s[36:39],s61 offen offset:0 sc1 lds
v_mfma_f32_16x16x32_bf16 v[28:31],a[224:227],v[52:55],v[28:31]
ds_read_b128 v[48:51],v2 offset:46080
v_mfma_f32_16x16x32_bf16 v[32:35],a[228:231],v[52:55],v[32:35]
v_mfma_f32_16x16x32_bf16 v[36:39],a[232:235],v[52:55],v[36:39]
v_mfma_f32_16x16x32_bf16 v[40:43],a[236:239],v[52:55],v[40:43]
s_waitcnt lgkmcnt(2)
v_mfma_f32_16x16x32_bf16 v[28:31],a[240:243],v[56:59],v[28:31]
ds_read_b128 v[52:55],v2 offset:47104
v_mfma_f32_16x16x32_bf16 v[32:35],a[244:247],v[56:59],v[32:35]
buffer_load_dwordx4 v1,s[36:39],s61 offen offset:1024 sc1 lds
v_mfma_f32_16x16x32_bf16 v[36:39],a[248:251],v[56:59],v[36:39]
v_mfma_f32_16x16x32_bf16 v[40:43],a[252:255],v[56:59],v[40:43]
s_waitcnt lgkmcnt(2)
v_mfma_f32_16x16x32_bf16 v[28:31],v[100:103],v[44:47],v[28:31]
ds_read_b128 v[56:59],v2 offset:48128
v_mfma_f32_16x16x32_bf16 v[32:35],v[104:107],v[44:47],v[32:35]
ds_read_b128 v[88:91],v10 offset:0
v_mfma_f32_16x16x32_bf16 v[36:39],v[108:111],v[44:47],v[36:39]
v_mfma_f32_16x16x32_bf16 v[40:43],v[112:115],v[44:47],v[40:43]
s_waitcnt lgkmcnt(3)
buffer_load_dwordx4 v1,s[36:39],s61 offen offset:2048 sc1 lds
v_mfma_f32_16x16x32_bf16 v[28:31],v[116:119],v[48:51],v[28:31]
ds_read_b128 v[44:47],v2 offset:49152
v_mfma_f32_16x16x32_bf16 v[32:35],v[120:123],v[48:51],v[32:35]
ds_read_b128 v[76:79],v10 offset:1024
v_mfma_f32_16x16x32_bf16 v[36:39],v[124:127],v[48:51],v[36:39]
v_mfma_f32_16x16x32_bf16 v[40:43],v[128:131],v[48:51],v[40:43]
s_waitcnt lgkmcnt(4)
v_mfma_f32_16x16x32_bf16 v[28:31],v[132:135],v[52:55],v[28:31]
ds_read_b128 v[48:51],v2 offset:50176
v_mfma_f32_16x16x32_bf16 v[32:35],v[136:139],v[52:55],v[32:35]
ds_read_b128 v[80:83],v10 offset:2048
v_mfma_f32_16x16x32_bf16 v[36:39],v[140:143],v[52:55],v[36:39]
v_mfma_f32_16x16x32_bf16 v[40:43],v[144:147],v[52:55],v[40:43]
s_waitcnt lgkmcnt(4)
v_mfma_f32_16x16x32_bf16 v[28:31],v[148:151],v[56:59],v[28:31]
ds_read_b128 v[52:55],v2 offset:51200
v_mfma_f32_16x16x32_bf16 v[32:35],v[152:155],v[56:59],v[32:35]
ds_read_b128 v[84:87],v10 offset:3072
v_mfma_f32_16x16x32_bf16 v[36:39],v[156:159],v[56:59],v[36:39]
v_mfma_f32_16x16x32_bf16 v[40:43],v[88:91],v[56:59],v[40:43]
s_waitcnt lgkmcnt(4)
v_mfma_f32_16x16x32_bf16 v[28:31],v[160:163],v[44:47],v[28:31]
ds_read_b128 v[56:59],v2 offset:52224
v_mfma_f32_16x16x32_bf16 v[32:35],v[164:167],v[44:47],v[32:35]
ds_read_b128 v[88:91],v10 offset:4096
v_mfma_f32_16x16x32_bf16 v[36:39],v[168:171],v[44:47],v[36:39]
v_mfma_f32_16x16x32_bf16 v[40:43],v[76:79],v[44:47],v[40:43]
s_waitcnt lgkmcnt(4)
v_mfma_f32_16x16x32_bf16 v[28:31],v[172:175],v[48:51],v[28:31]
ds_read_b128 v[44:47],v2 offset:53248
v_mfma_f32_16x16x32_bf16 v[32:35],v[176:179],v[48:51],v[32:35]
ds_read_b128 v[76:79],v10 offset:5120
v_mfma_f32_16x16x32_bf16 v[36:39],v[180:183],v[48:51],v[36:39]
s_waitcnt vmcnt(8) lgkmcnt(4)
v_mfma_f32_16x16x32_bf16 v[40:43],v[80:83],v[48:51],v[40:43]
s_barrier
v_mfma_f32_16x16x32_bf16 v[28:31],v[184:187],v[52:55],v[28:31]
ds_read_b128 v[48:51],v2 offset:54272
v_mfma_f32_16x16x32_bf16 v[32:35],v[188:191],v[52:55],v[32:35]
ds_read_b128 v[80:83],v10 offset:6144
v_mfma_f32_16x16x32_bf16 v[36:39],v[192:195],v[52:55],v[36:39]
v_mfma_f32_16x16x32_bf16 v[40:43],v[84:87],v[52:55],v[40:43]
s_waitcnt lgkmcnt(4)
v_mfma_f32_16x16x32_bf16 v[28:31],v[196:199],v[56:59],v[28:31]
ds_read_b128 v[52:55],v2 offset:55296
v_mfma_f32_16x16x32_bf16 v[32:35],v[200:203],v[56:59],v[32:35]
ds_read_b128 v[84:87],v10 offset:7168
v_mfma_f32_16x16x32_bf16 v[36:39],v[204:207],v[56:59],v[36:39]
v_mfma_f32_16x16x32_bf16 v[40:43],v[88:91],v[56:59],v[40:43]
s_waitcnt lgkmcnt(4)
v_mfma_f32_16x16x32_bf16 v[28:31],v[208:211],v[44:47],v[28:31]
ds_read_b128 v[56:59],v2 offset:56320
v_mfma_f32_16x16x32_bf16 v[32:35],v[212:215],v[44:47],v[32:35]
ds_read_b128 v[88:91],v10 offset:8192
v_mfma_f32_16x16x32_bf16 v[36:39],v[216:219],v[44:47],v[36:39]
s_waitcnt vmcnt(3) lgkmcnt(4)
v_mfma_f32_16x16x32_bf16 v[40:43],v[76:79],v[44:47],v[40:43]
s_barrier
v_mfma_f32_16x16x32_bf16 v[28:31],v[220:223],v[48:51],v[28:31]
ds_read_b128 v[44:47],v2 offset:0
v_mfma_f32_16x16x32_bf16 v[32:35],v[224:227],v[48:51],v[32:35]
v_mfma_f32_16x16x32_bf16 v[36:39],v[228:231],v[48:51],v[36:39]
v_mfma_f32_16x16x32_bf16 v[40:43],v[80:83],v[48:51],v[40:43]
s_waitcnt lgkmcnt(3)
v_mfma_f32_16x16x32_bf16 v[28:31],v[232:235],v[52:55],v[28:31]
ds_read_b128 v[48:51],v2 offset:1024
v_mfma_f32_16x16x32_bf16 v[32:35],v[236:239],v[52:55],v[32:35]
v_mfma_f32_16x16x32_bf16 v[36:39],v[240:243],v[52:55],v[36:39]
v_mfma_f32_16x16x32_bf16 v[40:43],v[84:87],v[52:55],v[40:43]
s_waitcnt lgkmcnt(2)
v_mfma_f32_16x16x32_bf16 v[28:31],v[244:247],v[56:59],v[28:31]
ds_read_b128 v[52:55],v2 offset:2048
v_mfma_f32_16x16x32_bf16 v[32:35],v[248:251],v[56:59],v[32:35]
v_mfma_f32_16x16x32_bf16 v[36:39],v[252:255],v[56:59],v[36:39]
s_waitcnt lgkmcnt(2)
v_mfma_f32_16x16x32_bf16 v[40:43],v[88:91],v[56:59],v[40:43]
s_cbranch_scc1 Lg4_d0w2_%=
s_branch Lg4_slow0_%=
Lg4_d0w3_%=:
v_mfma_f32_16x16x32_bf16 v[12:15],a[0:3],v[44:47],0
s_mov_b32 s73,s47
ds_read_b128 v[56:59],v2 offset:3072
v_lshlrev_b32 v60,16,v68
v_mfma_f32_16x16x32_bf16 v[16:19],a[4:7],v[44:47],0
s_max_u32 s73,s73,s65
v_and_b32 v61,0xffff0000,v68
v_lshlrev_b32 v62,16,v69
v_mfma_f32_16x16x32_bf16 v[20:23],a[8:11],v[44:47],0
s_mov_b32 s65,0
v_and_b32 v63,0xffff0000,v69
v_lshlrev_b32 v64,16,v70
v_mfma_f32_16x16x32_bf16 v[24:27],a[12:15],v[44:47],0
s_waitcnt lgkmcnt(2)
v_and_b32 v65,0xffff0000,v70
v_lshlrev_b32 v66,16,v71
v_mfma_f32_16x16x32_bf16 v[12:15],a[16:19],v[48:51],v[12:15]
s_mul_i32 s61,s49,7
ds_read_b128 v[44:47],v2 offset:4096
v_and_b32 v67,0xffff0000,v71
v_mfma_f32_16x16x32_bf16 v[16:19],a[20:23],v[48:51],v[16:19]
s_lshr_b32 s61,s61,1
v_cvt_pk_bf16_f32 v28,v28,v29
v_cvt_pk_bf16_f32 v29,v30,v31
v_mfma_f32_16x16x32_bf16 v[20:23],a[24:27],v[48:51],v[20:23]
v_cvt_pk_bf16_f32 v30,v32,v33
v_cvt_pk_bf16_f32 v31,v34,v35
v_mfma_f32_16x16x32_bf16 v[24:27],a[28:31],v[48:51],v[24:27]
s_waitcnt lgkmcnt(2)
v_add_f32 v36,v36,v60
v_add_f32 v37,v37,v61
v_mfma_f32_16x16x32_bf16 v[12:15],a[32:35],v[52:55],v[12:15]
ds_read_b128 v[48:51],v2 offset:5120
v_add_f32 v38,v38,v62
v_mfma_f32_16x16x32_bf16 v[16:19],a[36:39],v[52:55],v[16:19]
v_add_f32 v39,v39,v63
v_add_f32 v40,v40,v64
v_mfma_f32_16x16x32_bf16 v[20:23],a[40:43],v[52:55],v[20:23]
v_add_f32 v41,v41,v65
v_add_f32 v42,v42,v66
v_mfma_f32_16x16x32_bf16 v[24:27],a[44:47],v[52:55],v[24:27]
s_waitcnt lgkmcnt(2)
buffer_store_dwordx4 v[28:31],v3,s[36:39],s73 offen offset:0 sc1)"
R"(
v_add_f32 v43,v43,v67
v_cvt_pk_bf16_f32 v36,v36,v37
v_cvt_pk_bf16_f32 v37,v38,v39
v_mfma_f32_16x16x32_bf16 v[12:15],a[48:51],v[56:59],v[12:15]
ds_read_b128 v[52:55],v2 offset:6144
v_cvt_pk_bf16_f32 v38,v40,v41
v_mfma_f32_16x16x32_bf16 v[16:19],a[52:55],v[56:59],v[16:19]
v_cvt_pk_bf16_f32 v39,v42,v43
v_mfma_f32_16x16x32_bf16 v[20:23],a[56:59],v[56:59],v[20:23]
s_waitcnt lgkmcnt(2)
s_nop 0
v_mfma_f32_16x16x32_bf16 v[24:27],a[60:63],v[56:59],v[24:27]
s_barrier
v_mfma_f32_16x16x32_bf16 v[12:15],a[64:67],v[44:47],v[12:15]
ds_read_b128 v[56:59],v2 offset:7168
ds_read_b32 v11,v8 offset:4096
v_mfma_f32_16x16x32_bf16 v[16:19],a[68:71],v[44:47],v[16:19]
v_mfma_f32_16x16x32_bf16 v[20:23],a[72:75],v[44:47],v[20:23]
v_mfma_f32_16x16x32_bf16 v[24:27],a[76:79],v[44:47],v[24:27]
s_waitcnt lgkmcnt(3)
v_mfma_f32_16x16x32_bf16 v[12:15],a[80:83],v[48:51],v[12:15]
s_waitcnt lgkmcnt(0)
ds_read_b128 v[44:47],v2 offset:8192
v_mfma_f32_16x16x32_bf16 v[16:19],a[84:87],v[48:51],v[16:19]
v_mfma_f32_16x16x32_bf16 v[20:23],a[88:91],v[48:51],v[20:23]
v_mfma_f32_16x16x32_bf16 v[24:27],a[92:95],v[48:51],v[24:27]
buffer_store_dwordx4 v[36:39],v4,s[36:39],s73 offen offset:0 sc1
v_cmp_eq_u32 vcc,s60,v11
v_mfma_f32_16x16x32_bf16 v[12:15],a[96:99],v[52:55],v[12:15]
s_cmp_eq_u32 vcc_lo,-1
ds_read_b128 v[48:51],v2 offset:9216
s_cselect_b32 s66,s49,s59
v_mfma_f32_16x16x32_bf16 v[16:19],a[100:103],v[52:55],v[16:19]
s_cselect_b32 s61,s61,s59
s_add_u32 m0,s63,28672
s_add_u32 s53,s52,s55
v_mfma_f32_16x16x32_bf16 v[20:23],a[104:107],v[52:55],v[20:23]
s_cmp_eq_u32 s53,s56
s_cselect_b32 s53,s57,s53
s_cmp_eq_u32 s53,s58
v_mfma_f32_16x16x32_bf16 v[24:27],a[108:111],v[52:55],v[24:27]
buffer_load_dwordx4 v0,s[36:39],s61 offen offset:0 sc1 lds
v_mfma_f32_16x16x32_bf16 v[12:15],a[112:115],v[56:59],v[12:15]
s_cselect_b32 s53,s59,s53
ds_read_b128 v[52:55],v2 offset:10240
s_cmp_ge_u32 s53,6520832
v_mfma_f32_16x16x32_bf16 v[16:19],a[116:119],v[56:59],v[16:19]
s_cselect_b32 s55,196608,s55
v_mfma_f32_16x16x32_bf16 v[20:23],a[120:123],v[56:59],v[20:23]
v_mfma_f32_16x16x32_bf16 v[24:27],a[124:127],v[56:59],v[24:27]
s_waitcnt lgkmcnt(2)
buffer_load_dwordx4 v0,s[36:39],s61 offen offset:1024 sc1 lds
v_mfma_f32_16x16x32_bf16 v[12:15],a[128:131],v[44:47],v[12:15]
ds_read_b128 v[56:59],v2 offset:11264
v_mfma_f32_16x16x32_bf16 v[16:19],a[132:135],v[44:47],v[16:19]
v_mfma_f32_16x16x32_bf16 v[20:23],a[136:139],v[44:47],v[20:23]
v_mfma_f32_16x16x32_bf16 v[24:27],a[140:143],v[44:47],v[24:27]
s_waitcnt lgkmcnt(2)
buffer_load_dwordx4 v0,s[36:39],s61 offen offset:2048 sc1 lds
v_mfma_f32_16x16x32_bf16 v[12:15],a[144:147],v[48:51],v[12:15]
ds_read_b128 v[44:47],v2 offset:12288
v_mfma_f32_16x16x32_bf16 v[16:19],a[148:151],v[48:51],v[16:19]
v_mfma_f32_16x16x32_bf16 v[20:23],a[152:155],v[48:51],v[20:23]
v_mfma_f32_16x16x32_bf16 v[24:27],a[156:159],v[48:51],v[24:27]
s_waitcnt lgkmcnt(2)
buffer_load_dwordx4 v0,s[36:39],s61 offen offset:3072 sc1 lds
v_mfma_f32_16x16x32_bf16 v[12:15],a[160:163],v[52:55],v[12:15]
s_add_u32 m0,s64,28672
ds_read_b128 v[48:51],v2 offset:13312
s_lshr_b32 s70,s47,4
v_mfma_f32_16x16x32_bf16 v[16:19],a[164:167],v[52:55],v[16:19]
s_cmp_ge_u32 s66,s59
v_mfma_f32_16x16x32_bf16 v[20:23],a[168:171],v[52:55],v[20:23]
v_mfma_f32_16x16x32_bf16 v[24:27],a[172:175],v[52:55],v[24:27]
s_waitcnt lgkmcnt(2)
buffer_load_dwordx4 v[68:71],v9,s[36:39],s48 offen offset:0 sc1
v_mfma_f32_16x16x32_bf16 v[12:15],a[176:179],v[56:59],v[12:15]
ds_read_b128 v[52:55],v2 offset:14336
v_mfma_f32_16x16x32_bf16 v[16:19],a[180:183],v[56:59],v[16:19]
v_mfma_f32_16x16x32_bf16 v[20:23],a[184:187],v[56:59],v[20:23]
v_mfma_f32_16x16x32_bf16 v[24:27],a[188:191],v[56:59],v[24:27]
s_waitcnt lgkmcnt(2)
v_mfma_f32_16x16x32_bf16 v[12:15],a[192:195],v[44:47],v[12:15]
ds_read_b128 v[56:59],v2 offset:15360
v_mfma_f32_16x16x32_bf16 v[16:19],a[196:199],v[44:47],v[16:19]
v_mfma_f32_16x16x32_bf16 v[20:23],a[200:203],v[44:47],v[20:23]
s_waitcnt vmcnt(8) lgkmcnt(2)
v_mfma_f32_16x16x32_bf16 v[24:27],a[204:207],v[44:47],v[24:27]
s_barrier
v_mfma_f32_16x16x32_bf16 v[12:15],a[208:211],v[48:51],v[12:15]
ds_read_b128 v[44:47],v2 offset:16384
v_mfma_f32_16x16x32_bf16 v[16:19],a[212:215],v[48:51],v[16:19]
v_mfma_f32_16x16x32_bf16 v[20:23],a[216:219],v[48:51],v[20:23]
v_mfma_f32_16x16x32_bf16 v[24:27],a[220:223],v[48:51],v[24:27]
s_waitcnt lgkmcnt(2)
v_mfma_f32_16x16x32_bf16 v[12:15],a[224:227],v[52:55],v[12:15]
buffer_load_dwordx4 v1,s[36:39],s61 offen offset:0 sc1 lds
v_mfma_f32_16x16x32_bf16 v[16:19],a[228:231],v[52:55],v[16:19]
ds_read_b128 v[48:51],v2 offset:17408
v_mfma_f32_16x16x32_bf16 v[20:23],a[232:235],v[52:55],v[20:23]
v_mfma_f32_16x16x32_bf16 v[24:27],a[236:239],v[52:55],v[24:27]
s_waitcnt lgkmcnt(2)
v_mfma_f32_16x16x32_bf16 v[12:15],a[240:243],v[56:59],v[12:15]
ds_read_b128 v[52:55],v2 offset:18432
v_mfma_f32_16x16x32_bf16 v[16:19],a[244:247],v[56:59],v[16:19]
v_mfma_f32_16x16x32_bf16 v[20:23],a[248:251],v[56:59],v[20:23]
buffer_load_dwordx4 v1,s[36:39],s61 offen offset:1024 sc1 lds
v_mfma_f32_16x16x32_bf16 v[24:27],a[252:255],v[56:59],v[24:27]
s_waitcnt lgkmcnt(2)
v_mfma_f32_16x16x32_bf16 v[12:15],v[100:103],v[44:47],v[12:15]
ds_read_b128 v[56:59],v2 offset:19456
v_mfma_f32_16x16x32_bf16 v[16:19],v[104:107],v[44:47],v[16:19]
ds_read_b128 v[88:91],v10 offset:0
v_mfma_f32_16x16x32_bf16 v[20:23],v[108:111],v[44:47],v[20:23]
v_mfma_f32_16x16x32_bf16 v[24:27],v[112:115],v[44:47],v[24:27]
s_waitcnt lgkmcnt(3)
v_mfma_f32_16x16x32_bf16 v[12:15],v[116:119],v[48:51],v[12:15]
buffer_load_dwordx4 v1,s[36:39],s61 offen offset:2048 sc1 lds
v_mfma_f32_16x16x32_bf16 v[16:19],v[120:123],v[48:51],v[16:19]
ds_read_b128 v[44:47],v2 offset:20480
ds_read_b128 v[76:79],v10 offset:1024
v_mfma_f32_16x16x32_bf16 v[20:23],v[124:127],v[48:51],v[20:23]
v_mfma_f32_16x16x32_bf16 v[24:27],v[128:131],v[48:51],v[24:27]
s_waitcnt lgkmcnt(4)
v_mfma_f32_16x16x32_bf16 v[12:15],v[132:135],v[52:55],v[12:15]
ds_read_b128 v[48:51],v2 offset:21504
v_mfma_f32_16x16x32_bf16 v[16:19],v[136:139],v[52:55],v[16:19]
ds_read_b128 v[80:83],v10 offset:2048
v_mfma_f32_16x16x32_bf16 v[20:23],v[140:143],v[52:55],v[20:23]
v_mfma_f32_16x16x32_bf16 v[24:27],v[144:147],v[52:55],v[24:27]
s_waitcnt lgkmcnt(4)
v_mfma_f32_16x16x32_bf16 v[12:15],v[148:151],v[56:59],v[12:15]
ds_read_b128 v[52:55],v2 offset:22528
v_mfma_f32_16x16x32_bf16 v[16:19],v[152:155],v[56:59],v[16:19]
ds_read_b128 v[84:87],v10 offset:3072
v_mfma_f32_16x16x32_bf16 v[20:23],v[156:159],v[56:59],v[20:23]
v_mfma_f32_16x16x32_bf16 v[24:27],v[88:91],v[56:59],v[24:27]
s_waitcnt lgkmcnt(4)
v_mfma_f32_16x16x32_bf16 v[12:15],v[160:163],v[44:47],v[12:15]
ds_read_b128 v[56:59],v2 offset:23552
v_mfma_f32_16x16x32_bf16 v[16:19],v[164:167],v[44:47],v[16:19]
ds_read_b128 v[88:91],v10 offset:4096
v_mfma_f32_16x16x32_bf16 v[20:23],v[168:171],v[44:47],v[20:23]
v_mfma_f32_16x16x32_bf16 v[24:27],v[76:79],v[44:47],v[24:27]
s_waitcnt lgkmcnt(4)
v_mfma_f32_16x16x32_bf16 v[12:15],v[172:175],v[48:51],v[12:15]
ds_read_b128 v[44:47],v2 offset:24576
v_mfma_f32_16x16x32_bf16 v[16:19],v[176:179],v[48:51],v[16:19]
ds_read_b128 v[76:79],v10 offset:5120
v_mfma_f32_16x16x32_bf16 v[20:23],v[180:183],v[48:51],v[20:23]
s_waitcnt vmcnt(8) lgkmcnt(4)
v_mfma_f32_16x16x32_bf16 v[24:27],v[80:83],v[48:51],v[24:27]
s_barrier
v_mfma_f32_16x16x32_bf16 v[12:15],v[184:187],v[52:55],v[12:15]
buffer_store_dword v6,v5,s[36:39],s70 offen offset:0 sc1
ds_read_b128 v[48:51],v2 offset:25600
v_mfma_f32_16x16x32_bf16 v[16:19],v[188:191],v[52:55],v[16:19]
ds_read_b128 v[80:83],v10 offset:6144
v_mfma_f32_16x16x32_bf16 v[20:23],v[192:195],v[52:55],v[20:23]
v_mfma_f32_16x16x32_bf16 v[24:27],v[84:87],v[52:55],v[24:27]
s_waitcnt lgkmcnt(4)
v_mfma_f32_16x16x32_bf16 v[12:15],v[196:199],v[56:59],v[12:15]
ds_read_b128 v[52:55],v2 offset:26624
v_mfma_f32_16x16x32_bf16 v[16:19],v[200:203],v[56:59],v[16:19]
ds_read_b128 v[84:87],v10 offset:7168
v_mfma_f32_16x16x32_bf16 v[20:23],v[204:207],v[56:59],v[20:23]
v_mfma_f32_16x16x32_bf16 v[24:27],v[88:91],v[56:59],v[24:27]
s_waitcnt lgkmcnt(4)
v_mfma_f32_16x16x32_bf16 v[12:15],v[208:211],v[44:47],v[12:15]
ds_read_b128 v[56:59],v2 offset:27648
v_mfma_f32_16x16x32_bf16 v[16:19],v[212:215],v[44:47],v[16:19]
ds_read_b128 v[88:91],v10 offset:8192
v_mfma_f32_16x16x32_bf16 v[20:23],v[216:219],v[44:47],v[20:23]
s_waitcnt vmcnt(4) lgkmcnt(4)
v_mfma_f32_16x16x32_bf16 v[24:27],v[76:79],v[44:47],v[24:27]
s_barrier
v_mfma_f32_16x16x32_bf16 v[12:15],v[220:223],v[48:51],v[12:15]
ds_read_b128 v[44:47],v2 offset:28672
v_mfma_f32_16x16x32_bf16 v[16:19],v[224:227],v[48:51],v[16:19]
v_mfma_f32_16x16x32_bf16 v[20:23],v[228:231],v[48:51],v[20:23]
v_mfma_f32_16x16x32_bf16 v[24:27],v[80:83],v[48:51],v[24:27]
s_waitcnt lgkmcnt(3)
v_mfma_f32_16x16x32_bf16 v[12:15],v[232:235],v[52:55],v[12:15]
ds_read_b128 v[48:51],v2 offset:29696
v_mfma_f32_16x16x32_bf16 v[16:19],v[236:239],v[52:55],v[16:19]
v_mfma_f32_16x16x32_bf16 v[20:23],v[240:243],v[52:55],v[20:23]
v_mfma_f32_16x16x32_bf16 v[24:27],v[84:87],v[52:55],v[24:27]
s_waitcnt lgkmcnt(2)
v_mfma_f32_16x16x32_bf16 v[12:15],v[244:247],v[56:59],v[12:15]
ds_read_b128 v[52:55],v2 offset:30720
v_mfma_f32_16x16x32_bf16 v[16:19],v[248:251],v[56:59],v[16:19]
v_mfma_f32_16x16x32_bf16 v[20:23],v[252:255],v[56:59],v[20:23]
s_waitcnt lgkmcnt(2)
v_mfma_f32_16x16x32_bf16 v[24:27],v[88:91],v[56:59],v[24:27]
s_cbranch_scc1 Lg4_slow1_%=
Lg4_d1w3_%=:
v_mfma_f32_16x16x32_bf16 v[28:31],a[0:3],v[44:47],0
s_mov_b32 s73,s48
ds_read_b128 v[56:59],v2 offset:31744
v_lshlrev_b32 v60,16,v68)"
R"(
v_mfma_f32_16x16x32_bf16 v[32:35],a[4:7],v[44:47],0
s_max_u32 s73,s73,s65
v_and_b32 v61,0xffff0000,v68
v_lshlrev_b32 v62,16,v69
v_mfma_f32_16x16x32_bf16 v[36:39],a[8:11],v[44:47],0
s_mov_b32 s65,0
v_and_b32 v63,0xffff0000,v69
v_lshlrev_b32 v64,16,v70
v_mfma_f32_16x16x32_bf16 v[40:43],a[12:15],v[44:47],0
s_waitcnt lgkmcnt(2)
v_and_b32 v65,0xffff0000,v70
v_lshlrev_b32 v66,16,v71
v_mfma_f32_16x16x32_bf16 v[28:31],a[16:19],v[48:51],v[28:31]
s_mul_i32 s61,s50,7
ds_read_b128 v[44:47],v2 offset:32768
v_and_b32 v67,0xffff0000,v71
v_mfma_f32_16x16x32_bf16 v[32:35],a[20:23],v[48:51],v[32:35]
s_lshr_b32 s61,s61,1
v_cvt_pk_bf16_f32 v12,v12,v13
v_cvt_pk_bf16_f32 v13,v14,v15
v_mfma_f32_16x16x32_bf16 v[36:39],a[24:27],v[48:51],v[36:39]
s_mov_b64 s[44:45],s[46:47]
v_cvt_pk_bf16_f32 v14,v16,v17
v_cvt_pk_bf16_f32 v15,v18,v19
v_mfma_f32_16x16x32_bf16 v[40:43],a[28:31],v[48:51],v[40:43]
s_waitcnt lgkmcnt(2)
v_add_f32 v20,v20,v60
v_add_f32 v21,v21,v61
v_mfma_f32_16x16x32_bf16 v[28:31],a[32:35],v[52:55],v[28:31]
s_mov_b64 s[46:47],s[48:49]
ds_read_b128 v[48:51],v2 offset:33792
v_add_f32 v22,v22,v62
v_mfma_f32_16x16x32_bf16 v[32:35],a[36:39],v[52:55],v[32:35]
v_add_f32 v23,v23,v63
v_add_f32 v24,v24,v64
v_mfma_f32_16x16x32_bf16 v[36:39],a[40:43],v[52:55],v[36:39]
v_add_f32 v25,v25,v65
v_add_f32 v26,v26,v66
v_mfma_f32_16x16x32_bf16 v[40:43],a[44:47],v[52:55],v[40:43]
s_waitcnt lgkmcnt(2)
buffer_store_dwordx4 v[12:15],v3,s[36:39],s73 offen offset:0 sc1
v_add_f32 v27,v27,v67
v_cvt_pk_bf16_f32 v20,v20,v21
v_cvt_pk_bf16_f32 v21,v22,v23
v_mfma_f32_16x16x32_bf16 v[28:31],a[48:51],v[56:59],v[28:31]
ds_read_b128 v[52:55],v2 offset:34816
v_cvt_pk_bf16_f32 v22,v24,v25
v_mfma_f32_16x16x32_bf16 v[32:35],a[52:55],v[56:59],v[32:35]
v_cvt_pk_bf16_f32 v23,v26,v27
v_mfma_f32_16x16x32_bf16 v[36:39],a[56:59],v[56:59],v[36:39]
s_waitcnt lgkmcnt(2)
s_nop 0
v_mfma_f32_16x16x32_bf16 v[40:43],a[60:63],v[56:59],v[40:43]
s_barrier
v_mfma_f32_16x16x32_bf16 v[28:31],a[64:67],v[44:47],v[28:31]
ds_read_b128 v[56:59],v2 offset:35840
ds_read_b32 v11,v8 offset:5120
v_mfma_f32_16x16x32_bf16 v[32:35],a[68:71],v[44:47],v[32:35]
v_mfma_f32_16x16x32_bf16 v[36:39],a[72:75],v[44:47],v[36:39]
v_mfma_f32_16x16x32_bf16 v[40:43],a[76:79],v[44:47],v[40:43]
s_waitcnt lgkmcnt(3)
v_mfma_f32_16x16x32_bf16 v[28:31],a[80:83],v[48:51],v[28:31]
s_waitcnt lgkmcnt(0)
ds_read_b128 v[44:47],v2 offset:36864
v_mfma_f32_16x16x32_bf16 v[32:35],a[84:87],v[48:51],v[32:35]
v_mfma_f32_16x16x32_bf16 v[36:39],a[88:91],v[48:51],v[36:39]
v_mfma_f32_16x16x32_bf16 v[40:43],a[92:95],v[48:51],v[40:43]
buffer_store_dwordx4 v[20:23],v4,s[36:39],s73 offen offset:0 sc1
v_cmp_eq_u32 vcc,s60,v11
v_mfma_f32_16x16x32_bf16 v[28:31],a[96:99],v[52:55],v[28:31]
s_cmp_eq_u32 vcc_lo,-1
ds_read_b128 v[48:51],v2 offset:37888
s_cselect_b32 s67,s50,s59
v_mfma_f32_16x16x32_bf16 v[32:35],a[100:103],v[52:55],v[32:35]
s_cselect_b32 s61,s61,s59
s_add_u32 m0,s63,0
s_add_u32 s74,s53,s55
v_mfma_f32_16x16x32_bf16 v[36:39],a[104:107],v[52:55],v[36:39]
s_cmp_eq_u32 s74,s56
s_cselect_b32 s74,s57,s74
s_cmp_eq_u32 s74,s58
v_mfma_f32_16x16x32_bf16 v[40:43],a[108:111],v[52:55],v[40:43]
buffer_load_dwordx4 v0,s[36:39],s61 offen offset:0 sc1 lds
v_mfma_f32_16x16x32_bf16 v[28:31],a[112:115],v[56:59],v[28:31]
s_cselect_b32 s74,s59,s74
ds_read_b128 v[52:55],v2 offset:38912
s_cmp_ge_u32 s74,6520832
v_mfma_f32_16x16x32_bf16 v[32:35],a[116:119],v[56:59],v[32:35]
s_cselect_b32 s55,196608,s55
v_mfma_f32_16x16x32_bf16 v[36:39],a[120:123],v[56:59],v[36:39]
v_mfma_f32_16x16x32_bf16 v[40:43],a[124:127],v[56:59],v[40:43]
s_waitcnt lgkmcnt(2)
buffer_load_dwordx4 v0,s[36:39],s61 offen offset:1024 sc1 lds
v_mfma_f32_16x16x32_bf16 v[28:31],a[128:131],v[44:47],v[28:31]
ds_read_b128 v[56:59],v2 offset:39936
v_mfma_f32_16x16x32_bf16 v[32:35],a[132:135],v[44:47],v[32:35]
v_mfma_f32_16x16x32_bf16 v[36:39],a[136:139],v[44:47],v[36:39]
v_mfma_f32_16x16x32_bf16 v[40:43],a[140:143],v[44:47],v[40:43]
s_waitcnt lgkmcnt(2)
buffer_load_dwordx4 v0,s[36:39],s61 offen offset:2048 sc1 lds
v_mfma_f32_16x16x32_bf16 v[28:31],a[144:147],v[48:51],v[28:31]
ds_read_b128 v[44:47],v2 offset:40960
v_mfma_f32_16x16x32_bf16 v[32:35],a[148:151],v[48:51],v[32:35]
v_mfma_f32_16x16x32_bf16 v[36:39],a[152:155],v[48:51],v[36:39]
v_mfma_f32_16x16x32_bf16 v[40:43],a[156:159],v[48:51],v[40:43]
s_waitcnt lgkmcnt(2)
buffer_load_dwordx4 v0,s[36:39],s61 offen offset:3072 sc1 lds
v_mfma_f32_16x16x32_bf16 v[28:31],a[160:163],v[52:55],v[28:31]
s_add_u32 m0,s64,0
ds_read_b128 v[48:51],v2 offset:41984
s_lshr_b32 s70,s48,4
v_mfma_f32_16x16x32_bf16 v[32:35],a[164:167],v[52:55],v[32:35]
s_cmp_lt_u32 s67,s59
v_mfma_f32_16x16x32_bf16 v[36:39],a[168:171],v[52:55],v[36:39]
v_mfma_f32_16x16x32_bf16 v[40:43],a[172:175],v[52:55],v[40:43]
s_waitcnt lgkmcnt(2)
buffer_load_dwordx4 v[68:71],v9,s[36:39],s49 offen offset:0 sc1
v_mfma_f32_16x16x32_bf16 v[28:31],a[176:179],v[56:59],v[28:31]
s_mov_b64 s[48:49],s[50:51]
ds_read_b128 v[52:55],v2 offset:43008
s_mov_b64 s[50:51],s[52:53]
v_mfma_f32_16x16x32_bf16 v[32:35],a[180:183],v[56:59],v[32:35]
s_mov_b32 s52,s74
v_mfma_f32_16x16x32_bf16 v[36:39],a[184:187],v[56:59],v[36:39]
v_mfma_f32_16x16x32_bf16 v[40:43],a[188:191],v[56:59],v[40:43]
s_waitcnt lgkmcnt(2)
v_mfma_f32_16x16x32_bf16 v[28:31],a[192:195],v[44:47],v[28:31]
ds_read_b128 v[56:59],v2 offset:44032
v_mfma_f32_16x16x32_bf16 v[32:35],a[196:199],v[44:47],v[32:35]
v_mfma_f32_16x16x32_bf16 v[36:39],a[200:203],v[44:47],v[36:39]
s_waitcnt vmcnt(8) lgkmcnt(2)
v_mfma_f32_16x16x32_bf16 v[40:43],a[204:207],v[44:47],v[40:43]
s_barrier
v_mfma_f32_16x16x32_bf16 v[28:31],a[208:211],v[48:51],v[28:31]
ds_read_b128 v[44:47],v2 offset:45056
v_mfma_f32_16x16x32_bf16 v[32:35],a[212:215],v[48:51],v[32:35]
v_mfma_f32_16x16x32_bf16 v[36:39],a[216:219],v[48:51],v[36:39]
v_mfma_f32_16x16x32_bf16 v[40:43],a[220:223],v[48:51],v[40:43]
s_waitcnt lgkmcnt(2)
v_mfma_f32_16x16x32_bf16 v[28:31],a[224:227],v[52:55],v[28:31]
buffer_load_dwordx4 v1,s[36:39],s61 offen offset:0 sc1 lds
v_mfma_f32_16x16x32_bf16 v[32:35],a[228:231],v[52:55],v[32:35]
ds_read_b128 v[48:51],v2 offset:46080
v_mfma_f32_16x16x32_bf16 v[36:39],a[232:235],v[52:55],v[36:39]
v_mfma_f32_16x16x32_bf16 v[40:43],a[236:239],v[52:55],v[40:43]
s_waitcnt lgkmcnt(2)
v_mfma_f32_16x16x32_bf16 v[28:31],a[240:243],v[56:59],v[28:31]
ds_read_b128 v[52:55],v2 offset:47104
v_mfma_f32_16x16x32_bf16 v[32:35],a[244:247],v[56:59],v[32:35]
v_mfma_f32_16x16x32_bf16 v[36:39],a[248:251],v[56:59],v[36:39]
buffer_load_dwordx4 v1,s[36:39],s61 offen offset:1024 sc1 lds
v_mfma_f32_16x16x32_bf16 v[40:43],a[252:255],v[56:59],v[40:43]
s_waitcnt lgkmcnt(2)
v_mfma_f32_16x16x32_bf16 v[28:31],v[100:103],v[44:47],v[28:31]
ds_read_b128 v[56:59],v2 offset:48128
v_mfma_f32_16x16x32_bf16 v[32:35],v[104:107],v[44:47],v[32:35]
ds_read_b128 v[88:91],v10 offset:0
v_mfma_f32_16x16x32_bf16 v[36:39],v[108:111],v[44:47],v[36:39]
v_mfma_f32_16x16x32_bf16 v[40:43],v[112:115],v[44:47],v[40:43]
s_waitcnt lgkmcnt(3)
v_mfma_f32_16x16x32_bf16 v[28:31],v[116:119],v[48:51],v[28:31]
buffer_load_dwordx4 v1,s[36:39],s61 offen offset:2048 sc1 lds
v_mfma_f32_16x16x32_bf16 v[32:35],v[120:123],v[48:51],v[32:35]
ds_read_b128 v[44:47],v2 offset:49152
ds_read_b128 v[76:79],v10 offset:1024
v_mfma_f32_16x16x32_bf16 v[36:39],v[124:127],v[48:51],v[36:39]
v_mfma_f32_16x16x32_bf16 v[40:43],v[128:131],v[48:51],v[40:43]
s_waitcnt lgkmcnt(4)
v_mfma_f32_16x16x32_bf16 v[28:31],v[132:135],v[52:55],v[28:31]
ds_read_b128 v[48:51],v2 offset:50176
v_mfma_f32_16x16x32_bf16 v[32:35],v[136:139],v[52:55],v[32:35]
ds_read_b128 v[80:83],v10 offset:2048
v_mfma_f32_16x16x32_bf16 v[36:39],v[140:143],v[52:55],v[36:39]
v_mfma_f32_16x16x32_bf16 v[40:43],v[144:147],v[52:55],v[40:43]
s_waitcnt lgkmcnt(4)
v_mfma_f32_16x16x32_bf16 v[28:31],v[148:151],v[56:59],v[28:31]
ds_read_b128 v[52:55],v2 offset:51200
v_mfma_f32_16x16x32_bf16 v[32:35],v[152:155],v[56:59],v[32:35]
ds_read_b128 v[84:87],v10 offset:3072
v_mfma_f32_16x16x32_bf16 v[36:39],v[156:159],v[56:59],v[36:39]
v_mfma_f32_16x16x32_bf16 v[40:43],v[88:91],v[56:59],v[40:43]
s_waitcnt lgkmcnt(4)
v_mfma_f32_16x16x32_bf16 v[28:31],v[160:163],v[44:47],v[28:31]
ds_read_b128 v[56:59],v2 offset:52224
v_mfma_f32_16x16x32_bf16 v[32:35],v[164:167],v[44:47],v[32:35]
ds_read_b128 v[88:91],v10 offset:4096
v_mfma_f32_16x16x32_bf16 v[36:39],v[168:171],v[44:47],v[36:39]
v_mfma_f32_16x16x32_bf16 v[40:43],v[76:79],v[44:47],v[40:43]
s_waitcnt lgkmcnt(4)
v_mfma_f32_16x16x32_bf16 v[28:31],v[172:175],v[48:51],v[28:31]
ds_read_b128 v[44:47],v2 offset:53248
v_mfma_f32_16x16x32_bf16 v[32:35],v[176:179],v[48:51],v[32:35]
ds_read_b128 v[76:79],v10 offset:5120
v_mfma_f32_16x16x32_bf16 v[36:39],v[180:183],v[48:51],v[36:39]
s_waitcnt vmcnt(8) lgkmcnt(4)
v_mfma_f32_16x16x32_bf16 v[40:43],v[80:83],v[48:51],v[40:43]
s_barrier
v_mfma_f32_16x16x32_bf16 v[28:31],v[184:187],v[52:55],v[28:31]
buffer_store_dword v6,v5,s[36:39],s70 offen offset:0 sc1
ds_read_b128 v[48:51],v2 offset:54272)"
R"(
v_mfma_f32_16x16x32_bf16 v[32:35],v[188:191],v[52:55],v[32:35]
ds_read_b128 v[80:83],v10 offset:6144
v_mfma_f32_16x16x32_bf16 v[36:39],v[192:195],v[52:55],v[36:39]
v_mfma_f32_16x16x32_bf16 v[40:43],v[84:87],v[52:55],v[40:43]
s_waitcnt lgkmcnt(4)
v_mfma_f32_16x16x32_bf16 v[28:31],v[196:199],v[56:59],v[28:31]
ds_read_b128 v[52:55],v2 offset:55296
v_mfma_f32_16x16x32_bf16 v[32:35],v[200:203],v[56:59],v[32:35]
ds_read_b128 v[84:87],v10 offset:7168
v_mfma_f32_16x16x32_bf16 v[36:39],v[204:207],v[56:59],v[36:39]
v_mfma_f32_16x16x32_bf16 v[40:43],v[88:91],v[56:59],v[40:43]
s_waitcnt lgkmcnt(4)
v_mfma_f32_16x16x32_bf16 v[28:31],v[208:211],v[44:47],v[28:31]
ds_read_b128 v[56:59],v2 offset:56320
v_mfma_f32_16x16x32_bf16 v[32:35],v[212:215],v[44:47],v[32:35]
ds_read_b128 v[88:91],v10 offset:8192
v_mfma_f32_16x16x32_bf16 v[36:39],v[216:219],v[44:47],v[36:39]
s_waitcnt vmcnt(4) lgkmcnt(4)
v_mfma_f32_16x16x32_bf16 v[40:43],v[76:79],v[44:47],v[40:43]
s_barrier
v_mfma_f32_16x16x32_bf16 v[28:31],v[220:223],v[48:51],v[28:31]
ds_read_b128 v[44:47],v2 offset:0
v_mfma_f32_16x16x32_bf16 v[32:35],v[224:227],v[48:51],v[32:35]
v_mfma_f32_16x16x32_bf16 v[36:39],v[228:231],v[48:51],v[36:39]
v_mfma_f32_16x16x32_bf16 v[40:43],v[80:83],v[48:51],v[40:43]
s_waitcnt lgkmcnt(3)
v_mfma_f32_16x16x32_bf16 v[28:31],v[232:235],v[52:55],v[28:31]
ds_read_b128 v[48:51],v2 offset:1024
v_mfma_f32_16x16x32_bf16 v[32:35],v[236:239],v[52:55],v[32:35]
v_mfma_f32_16x16x32_bf16 v[36:39],v[240:243],v[52:55],v[36:39]
v_mfma_f32_16x16x32_bf16 v[40:43],v[84:87],v[52:55],v[40:43]
s_waitcnt lgkmcnt(2)
v_mfma_f32_16x16x32_bf16 v[28:31],v[244:247],v[56:59],v[28:31]
ds_read_b128 v[52:55],v2 offset:2048
v_mfma_f32_16x16x32_bf16 v[32:35],v[248:251],v[56:59],v[32:35]
v_mfma_f32_16x16x32_bf16 v[36:39],v[252:255],v[56:59],v[36:39]
s_waitcnt lgkmcnt(2)
v_mfma_f32_16x16x32_bf16 v[40:43],v[88:91],v[56:59],v[40:43]
s_cbranch_scc1 Lg4_d0w3_%=
s_branch Lg4_slow0_%=
Lg4_slow0_%=:
ds_read_b32 v11,v8 offset:6144
s_waitcnt lgkmcnt(0)
v_cmp_eq_u32 vcc,s60,v11
s_nop 0
s_cmp_eq_u32 vcc_lo,-1
s_cbranch_scc1 Lg4_gs0_%=
Lg4_slowb0_%=:
s_cmp_eq_u32 s54,1
s_cselect_b32 s68,s49,s48
s_mul_i32 s68,s68,5
s_lshr_b32 s68,s68,6
s_add_u32 m0,s78,0
s_nop 0
buffer_load_dword v7,s[36:39],s68 offen offset:0 sc1 lds
s_mov_b32 s73,s47
v_lshlrev_b32 v60,16,v68
v_and_b32 v61,0xffff0000,v68
v_lshlrev_b32 v62,16,v69
v_and_b32 v63,0xffff0000,v69
v_lshlrev_b32 v64,16,v70
v_and_b32 v65,0xffff0000,v70
v_lshlrev_b32 v66,16,v71
v_and_b32 v67,0xffff0000,v71
v_cvt_pk_bf16_f32 v28,v28,v29
v_cvt_pk_bf16_f32 v29,v30,v31
v_cvt_pk_bf16_f32 v30,v32,v33
v_cvt_pk_bf16_f32 v31,v34,v35
s_add_u32 m0,s78,1024
s_nop 0
buffer_load_dword v7,s[36:39],s68 offen offset:0 sc1 lds
buffer_store_dwordx4 v[28:31],v3,s[36:39],s73 offen offset:0 sc1
v_add_f32 v36,v36,v60
v_add_f32 v37,v37,v61
v_add_f32 v38,v38,v62
v_add_f32 v39,v39,v63
v_add_f32 v40,v40,v64
v_add_f32 v41,v41,v65
v_add_f32 v42,v42,v66
v_add_f32 v43,v43,v67
v_cvt_pk_bf16_f32 v36,v36,v37
v_cvt_pk_bf16_f32 v37,v38,v39
v_cvt_pk_bf16_f32 v38,v40,v41
v_cvt_pk_bf16_f32 v39,v42,v43
buffer_store_dwordx4 v[36:39],v4,s[36:39],s73 offen offset:0 sc1
s_cmp_ge_u32 s48,s59
s_cbranch_scc1 Lg4_done0_%=
s_add_u32 m0,s78,2048
s_nop 0
buffer_load_dword v7,s[36:39],s68 offen offset:0 sc1 lds
s_waitcnt vmcnt(4)
s_barrier
ds_read_b32 v11,v8 offset:0
s_mov_b32 s69,2304
s_waitcnt lgkmcnt(0)
v_cmp_eq_u32 vcc,s60,v11
s_nop 0
s_cmp_eq_u32 vcc_lo,-1
s_cbranch_scc1 Lg4_go0_%=
s_add_u32 m0,s78,3072
s_nop 0
buffer_load_dword v7,s[36:39],s68 offen offset:0 sc1 lds
s_waitcnt vmcnt(4)
s_barrier
ds_read_b32 v11,v8 offset:1024
s_mov_b32 s69,3328
s_waitcnt lgkmcnt(0)
v_cmp_eq_u32 vcc,s60,v11
s_nop 0
s_cmp_eq_u32 vcc_lo,-1
s_cbranch_scc1 Lg4_go0_%=
s_add_u32 m0,s78,0
s_nop 0
buffer_load_dword v7,s[36:39],s68 offen offset:0 sc1 lds
s_waitcnt vmcnt(2)
s_barrier
ds_read_b32 v11,v8 offset:2048
s_lshr_b32 s70,s47,4
buffer_store_dword v6,v5,s[36:39],s70 offen offset:0 sc1
s_mov_b32 s47,s59
s_mov_b32 s69,256
s_waitcnt lgkmcnt(0)
v_cmp_eq_u32 vcc,s60,v11
s_nop 0
s_cmp_eq_u32 vcc_lo,-1
s_cbranch_scc1 Lg4_go0_%=
s_add_u32 m0,s78,1024
s_nop 0
buffer_load_dword v7,s[36:39],s68 offen offset:0 sc1 lds
s_waitcnt vmcnt(3)
s_barrier
ds_read_b32 v11,v8 offset:3072
s_mov_b32 s69,1280
s_waitcnt lgkmcnt(0)
v_cmp_eq_u32 vcc,s60,v11
s_nop 0
s_cmp_eq_u32 vcc_lo,-1
s_cbranch_scc1 Lg4_go0_%=
Lg4_poll0_%=:
s_add_u32 m0,s78,2048
s_nop 0
buffer_load_dword v7,s[36:39],s68 offen offset:0 sc1 lds
s_waitcnt vmcnt(2)
s_barrier
ds_read_b32 v11,v8 offset:0
s_mov_b32 s69,2304
s_waitcnt lgkmcnt(0)
v_cmp_eq_u32 vcc,s60,v11
s_nop 0
s_cmp_eq_u32 vcc_lo,-1
s_cbranch_scc1 Lg4_go0_%=
s_sleep 4
s_add_u32 m0,s78,3072
s_nop 0
buffer_load_dword v7,s[36:39],s68 offen offset:0 sc1 lds
s_waitcnt vmcnt(2)
s_barrier
ds_read_b32 v11,v8 offset:1024
s_mov_b32 s69,3328
s_waitcnt lgkmcnt(0)
v_cmp_eq_u32 vcc,s60,v11
s_nop 0
s_cmp_eq_u32 vcc_lo,-1
s_cbranch_scc1 Lg4_go0_%=
s_sleep 4
s_add_u32 m0,s78,0
s_nop 0
buffer_load_dword v7,s[36:39],s68 offen offset:0 sc1 lds
s_waitcnt vmcnt(2)
s_barrier
ds_read_b32 v11,v8 offset:2048
s_mov_b32 s69,256
s_waitcnt lgkmcnt(0)
v_cmp_eq_u32 vcc,s60,v11
s_nop 0
s_cmp_eq_u32 vcc_lo,-1
s_cbranch_scc1 Lg4_go0_%=
s_sleep 4
s_add_u32 m0,s78,1024
s_nop 0
buffer_load_dword v7,s[36:39],s68 offen offset:0 sc1 lds
s_waitcnt vmcnt(2)
s_barrier
ds_read_b32 v11,v8 offset:3072
s_mov_b32 s69,1280
s_waitcnt lgkmcnt(0)
v_cmp_eq_u32 vcc,s60,v11
s_nop 0
s_cmp_eq_u32 vcc_lo,-1
s_cbranch_scc1 Lg4_go0_%=
s_sleep 4
s_add_u32 s79,s79,1
s_cmp_lt_u32 s79,20000
s_cbranch_scc1 Lg4_poll0_%=
Lg4_go0_%=:
s_mul_i32 s61,s48,7
s_lshr_b32 s61,s61,1
s_add_u32 m0,s63,0
s_nop 0
buffer_load_dwordx4 v0,s[36:39],s61 offen offset:0 sc1 lds)"
R"(
buffer_load_dwordx4 v0,s[36:39],s61 offen offset:1024 sc1 lds
buffer_load_dwordx4 v0,s[36:39],s61 offen offset:2048 sc1 lds
buffer_load_dwordx4 v0,s[36:39],s61 offen offset:3072 sc1 lds
s_add_u32 m0,s64,0
s_nop 0
buffer_load_dwordx4 v1,s[36:39],s61 offen offset:0 sc1 lds
buffer_load_dwordx4 v1,s[36:39],s61 offen offset:1024 sc1 lds
buffer_load_dwordx4 v1,s[36:39],s61 offen offset:2048 sc1 lds
s_mul_i32 s68,s49,5
s_lshr_b32 s68,s68,6
s_add_u32 m0,s78,4096
s_nop 0
buffer_load_dword v7,s[36:39],s68 offen offset:0 sc1 lds
s_waitcnt vmcnt(4)
s_barrier
ds_read_b128 v[44:47],v2 offset:0
ds_read_b128 v[48:51],v2 offset:1024
ds_read_b128 v[52:55],v2 offset:2048
s_lshr_b32 s70,s47,4
buffer_store_dword v6,v5,s[36:39],s70 offen offset:0 sc1
s_mov_b32 s47,s59
s_waitcnt lgkmcnt(2)
s_setpc_b64 s[90:91]
Lg4_gs0_%=:
s_mul_i32 s61,s48,7
s_lshr_b32 s61,s61,1
s_cmp_eq_u32 s54,1
s_cselect_b32 s68,s49,s48
s_mul_i32 s68,s68,5
s_lshr_b32 s68,s68,6
s_mov_b32 s73,s47
s_add_u32 m0,s63,0
s_nop 0
buffer_load_dwordx4 v0,s[36:39],s61 offen offset:0 sc1 lds
v_lshlrev_b32 v60,16,v68
v_and_b32 v61,0xffff0000,v68
v_lshlrev_b32 v62,16,v69
v_and_b32 v63,0xffff0000,v69
buffer_load_dwordx4 v0,s[36:39],s61 offen offset:1024 sc1 lds
v_lshlrev_b32 v64,16,v70
v_and_b32 v65,0xffff0000,v70
v_lshlrev_b32 v66,16,v71
v_and_b32 v67,0xffff0000,v71
buffer_load_dwordx4 v0,s[36:39],s61 offen offset:2048 sc1 lds
v_cvt_pk_bf16_f32 v28,v28,v29
v_cvt_pk_bf16_f32 v29,v30,v31
v_cvt_pk_bf16_f32 v30,v32,v33
v_cvt_pk_bf16_f32 v31,v34,v35
buffer_load_dwordx4 v0,s[36:39],s61 offen offset:3072 sc1 lds
buffer_store_dwordx4 v[28:31],v3,s[36:39],s73 offen offset:0 sc1
v_add_f32 v36,v36,v60
v_add_f32 v37,v37,v61
v_add_f32 v38,v38,v62
s_add_u32 m0,s64,0
s_nop 0
buffer_load_dwordx4 v1,s[36:39],s61 offen offset:0 sc1 lds
v_add_f32 v39,v39,v63
v_add_f32 v40,v40,v64
v_add_f32 v41,v41,v65
v_add_f32 v42,v42,v66
buffer_load_dwordx4 v1,s[36:39],s61 offen offset:1024 sc1 lds
v_add_f32 v43,v43,v67
v_cvt_pk_bf16_f32 v36,v36,v37
v_cvt_pk_bf16_f32 v37,v38,v39
v_cvt_pk_bf16_f32 v38,v40,v41
buffer_load_dwordx4 v1,s[36:39],s61 offen offset:2048 sc1 lds
v_cvt_pk_bf16_f32 v39,v42,v43
buffer_store_dwordx4 v[36:39],v4,s[36:39],s73 offen offset:0 sc1
s_mul_i32 s68,s49,5
s_lshr_b32 s68,s68,6
s_add_u32 m0,s78,4096
s_nop 0
buffer_load_dword v7,s[36:39],s68 offen offset:0 sc1 lds
s_waitcnt vmcnt(6)
s_barrier
ds_read_b128 v[44:47],v2 offset:0
ds_read_b128 v[48:51],v2 offset:1024
ds_read_b128 v[52:55],v2 offset:2048
s_mov_b32 s65,s59
s_lshr_b32 s70,s59,4
buffer_store_dword v6,v5,s[36:39],s70 offen offset:0 sc1
s_waitcnt lgkmcnt(2)
s_setpc_b64 s[90:91]
Lg4_done0_%=:
s_waitcnt vmcnt(0)
s_barrier
s_lshr_b32 s70,s47,4
buffer_store_dword v6,v5,s[36:39],s70 offen offset:0 sc1
s_branch Lg4_done_%=
Lg4_slow1_%=:
ds_read_b32 v11,v8 offset:6144
s_waitcnt lgkmcnt(0)
v_cmp_eq_u32 vcc,s60,v11
s_nop 0
s_cmp_eq_u32 vcc_lo,-1
s_cbranch_scc1 Lg4_gs1_%=
Lg4_slowb1_%=:
s_cmp_eq_u32 s54,1
s_cselect_b32 s68,s50,s49
s_mul_i32 s68,s68,5
s_lshr_b32 s68,s68,6
s_add_u32 m0,s78,0
s_nop 0
buffer_load_dword v7,s[36:39],s68 offen offset:0 sc1 lds
s_mov_b32 s73,s48
v_lshlrev_b32 v60,16,v68
v_and_b32 v61,0xffff0000,v68
v_lshlrev_b32 v62,16,v69
v_and_b32 v63,0xffff0000,v69
v_lshlrev_b32 v64,16,v70
v_and_b32 v65,0xffff0000,v70
v_lshlrev_b32 v66,16,v71
v_and_b32 v67,0xffff0000,v71
v_cvt_pk_bf16_f32 v12,v12,v13
v_cvt_pk_bf16_f32 v13,v14,v15
v_cvt_pk_bf16_f32 v14,v16,v17
v_cvt_pk_bf16_f32 v15,v18,v19
s_add_u32 m0,s78,1024
s_nop 0
buffer_load_dword v7,s[36:39],s68 offen offset:0 sc1 lds
buffer_store_dwordx4 v[12:15],v3,s[36:39],s73 offen offset:0 sc1
v_add_f32 v20,v20,v60
v_add_f32 v21,v21,v61
v_add_f32 v22,v22,v62
v_add_f32 v23,v23,v63
v_add_f32 v24,v24,v64
v_add_f32 v25,v25,v65
v_add_f32 v26,v26,v66
v_add_f32 v27,v27,v67
v_cvt_pk_bf16_f32 v20,v20,v21
v_cvt_pk_bf16_f32 v21,v22,v23
v_cvt_pk_bf16_f32 v22,v24,v25
v_cvt_pk_bf16_f32 v23,v26,v27
buffer_store_dwordx4 v[20:23],v4,s[36:39],s73 offen offset:0 sc1
s_cmp_ge_u32 s49,s59
s_cbranch_scc1 Lg4_done1_%=
s_add_u32 m0,s78,2048
s_nop 0
buffer_load_dword v7,s[36:39],s68 offen offset:0 sc1 lds
s_waitcnt vmcnt(4)
s_barrier
ds_read_b32 v11,v8 offset:0
s_mov_b32 s69,2304
s_waitcnt lgkmcnt(0)
v_cmp_eq_u32 vcc,s60,v11
s_nop 0
s_cmp_eq_u32 vcc_lo,-1
s_cbranch_scc1 Lg4_go1_%=
s_add_u32 m0,s78,3072
s_nop 0
buffer_load_dword v7,s[36:39],s68 offen offset:0 sc1 lds
s_waitcnt vmcnt(4)
s_barrier
ds_read_b32 v11,v8 offset:1024
s_mov_b32 s69,3328
s_waitcnt lgkmcnt(0)
v_cmp_eq_u32 vcc,s60,v11
s_nop 0
s_cmp_eq_u32 vcc_lo,-1
s_cbranch_scc1 Lg4_go1_%=
s_add_u32 m0,s78,0
s_nop 0
buffer_load_dword v7,s[36:39],s68 offen offset:0 sc1 lds
s_waitcnt vmcnt(2)
s_barrier
ds_read_b32 v11,v8 offset:2048
s_lshr_b32 s70,s48,4
buffer_store_dword v6,v5,s[36:39],s70 offen offset:0 sc1
s_mov_b32 s48,s59
s_mov_b32 s69,256
s_waitcnt lgkmcnt(0)
v_cmp_eq_u32 vcc,s60,v11
s_nop 0
s_cmp_eq_u32 vcc_lo,-1
s_cbranch_scc1 Lg4_go1_%=
s_add_u32 m0,s78,1024
s_nop 0
buffer_load_dword v7,s[36:39],s68 offen offset:0 sc1 lds
s_waitcnt vmcnt(3)
s_barrier
ds_read_b32 v11,v8 offset:3072
s_mov_b32 s69,1280
s_waitcnt lgkmcnt(0)
v_cmp_eq_u32 vcc,s60,v11
s_nop 0
s_cmp_eq_u32 vcc_lo,-1
s_cbranch_scc1 Lg4_go1_%=
Lg4_poll1_%=:
s_add_u32 m0,s78,2048
s_nop 0
buffer_load_dword v7,s[36:39],s68 offen offset:0 sc1 lds
s_waitcnt vmcnt(2)
s_barrier
ds_read_b32 v11,v8 offset:0
s_mov_b32 s69,2304
s_waitcnt lgkmcnt(0)
v_cmp_eq_u32 vcc,s60,v11
s_nop 0
s_cmp_eq_u32 vcc_lo,-1
s_cbranch_scc1 Lg4_go1_%=)"
R"(
s_sleep 4
s_add_u32 m0,s78,3072
s_nop 0
buffer_load_dword v7,s[36:39],s68 offen offset:0 sc1 lds
s_waitcnt vmcnt(2)
s_barrier
ds_read_b32 v11,v8 offset:1024
s_mov_b32 s69,3328
s_waitcnt lgkmcnt(0)
v_cmp_eq_u32 vcc,s60,v11
s_nop 0
s_cmp_eq_u32 vcc_lo,-1
s_cbranch_scc1 Lg4_go1_%=
s_sleep 4
s_add_u32 m0,s78,0
s_nop 0
buffer_load_dword v7,s[36:39],s68 offen offset:0 sc1 lds
s_waitcnt vmcnt(2)
s_barrier
ds_read_b32 v11,v8 offset:2048
s_mov_b32 s69,256
s_waitcnt lgkmcnt(0)
v_cmp_eq_u32 vcc,s60,v11
s_nop 0
s_cmp_eq_u32 vcc_lo,-1
s_cbranch_scc1 Lg4_go1_%=
s_sleep 4
s_add_u32 m0,s78,1024
s_nop 0
buffer_load_dword v7,s[36:39],s68 offen offset:0 sc1 lds
s_waitcnt vmcnt(2)
s_barrier
ds_read_b32 v11,v8 offset:3072
s_mov_b32 s69,1280
s_waitcnt lgkmcnt(0)
v_cmp_eq_u32 vcc,s60,v11
s_nop 0
s_cmp_eq_u32 vcc_lo,-1
s_cbranch_scc1 Lg4_go1_%=
s_sleep 4
s_add_u32 s79,s79,1
s_cmp_lt_u32 s79,20000
s_cbranch_scc1 Lg4_poll1_%=
Lg4_go1_%=:
s_mul_i32 s61,s49,7
s_lshr_b32 s61,s61,1
s_add_u32 m0,s63,28672
s_nop 0
buffer_load_dwordx4 v0,s[36:39],s61 offen offset:0 sc1 lds
buffer_load_dwordx4 v0,s[36:39],s61 offen offset:1024 sc1 lds
buffer_load_dwordx4 v0,s[36:39],s61 offen offset:2048 sc1 lds
buffer_load_dwordx4 v0,s[36:39],s61 offen offset:3072 sc1 lds
s_add_u32 m0,s64,28672
s_nop 0
buffer_load_dwordx4 v1,s[36:39],s61 offen offset:0 sc1 lds
buffer_load_dwordx4 v1,s[36:39],s61 offen offset:1024 sc1 lds
buffer_load_dwordx4 v1,s[36:39],s61 offen offset:2048 sc1 lds
s_mul_i32 s68,s50,5
s_lshr_b32 s68,s68,6
s_add_u32 m0,s78,5120
s_nop 0
buffer_load_dword v7,s[36:39],s68 offen offset:0 sc1 lds
s_waitcnt vmcnt(4)
s_barrier
ds_read_b128 v[44:47],v2 offset:28672
ds_read_b128 v[48:51],v2 offset:29696
ds_read_b128 v[52:55],v2 offset:30720
s_lshr_b32 s70,s48,4
buffer_store_dword v6,v5,s[36:39],s70 offen offset:0 sc1
s_mov_b32 s48,s59
s_waitcnt lgkmcnt(2)
s_setpc_b64 s[92:93]
Lg4_gs1_%=:
s_mul_i32 s61,s49,7
s_lshr_b32 s61,s61,1
s_cmp_eq_u32 s54,1
s_cselect_b32 s68,s50,s49
s_mul_i32 s68,s68,5
s_lshr_b32 s68,s68,6
s_mov_b32 s73,s48
s_add_u32 m0,s63,28672
s_nop 0
buffer_load_dwordx4 v0,s[36:39],s61 offen offset:0 sc1 lds
v_lshlrev_b32 v60,16,v68
v_and_b32 v61,0xffff0000,v68
v_lshlrev_b32 v62,16,v69
v_and_b32 v63,0xffff0000,v69
buffer_load_dwordx4 v0,s[36:39],s61 offen offset:1024 sc1 lds
v_lshlrev_b32 v64,16,v70
v_and_b32 v65,0xffff0000,v70
v_lshlrev_b32 v66,16,v71
v_and_b32 v67,0xffff0000,v71
buffer_load_dwordx4 v0,s[36:39],s61 offen offset:2048 sc1 lds
v_cvt_pk_bf16_f32 v12,v12,v13
v_cvt_pk_bf16_f32 v13,v14,v15
v_cvt_pk_bf16_f32 v14,v16,v17
v_cvt_pk_bf16_f32 v15,v18,v19
buffer_load_dwordx4 v0,s[36:39],s61 offen offset:3072 sc1 lds
buffer_store_dwordx4 v[12:15],v3,s[36:39],s73 offen offset:0 sc1
v_add_f32 v20,v20,v60
v_add_f32 v21,v21,v61
v_add_f32 v22,v22,v62
s_add_u32 m0,s64,28672
s_nop 0
buffer_load_dwordx4 v1,s[36:39],s61 offen offset:0 sc1 lds
v_add_f32 v23,v23,v63
v_add_f32 v24,v24,v64
v_add_f32 v25,v25,v65
v_add_f32 v26,v26,v66
buffer_load_dwordx4 v1,s[36:39],s61 offen offset:1024 sc1 lds
v_add_f32 v27,v27,v67
v_cvt_pk_bf16_f32 v20,v20,v21
v_cvt_pk_bf16_f32 v21,v22,v23
v_cvt_pk_bf16_f32 v22,v24,v25
buffer_load_dwordx4 v1,s[36:39],s61 offen offset:2048 sc1 lds
v_cvt_pk_bf16_f32 v23,v26,v27
buffer_store_dwordx4 v[20:23],v4,s[36:39],s73 offen offset:0 sc1
s_mul_i32 s68,s50,5
s_lshr_b32 s68,s68,6
s_add_u32 m0,s78,5120
s_nop 0
buffer_load_dword v7,s[36:39],s68 offen offset:0 sc1 lds
s_waitcnt vmcnt(6)
s_barrier
ds_read_b128 v[44:47],v2 offset:28672
ds_read_b128 v[48:51],v2 offset:29696
ds_read_b128 v[52:55],v2 offset:30720
s_mov_b32 s65,s59
s_lshr_b32 s70,s59,4
buffer_store_dword v6,v5,s[36:39],s70 offen offset:0 sc1
s_waitcnt lgkmcnt(2)
s_setpc_b64 s[92:93]
Lg4_done1_%=:
s_waitcnt vmcnt(0)
s_barrier
s_lshr_b32 s70,s48,4
buffer_store_dword v6,v5,s[36:39],s70 offen offset:0 sc1
s_branch Lg4_done_%=
Lg4_done_%=:
s_waitcnt vmcnt(0) lgkmcnt(0)
s_barrier
s_endpgm
Lcode_g4_e:)"
  :: "s"(ws), "s"(wave), "s"(sStep), "s"(sX1), "s"(sX2), "s"(sU0), "s"(sWw), "s"(sBuf), "s"(sEp), "v"(vSrc), "v"(vSrc2), "v"(vA), "v"(vOut), "v"(vOut2), "v"(vFlag), "v"(vPollOff), "v"(vSnapRd), "v"(vAuxS), "v"(vWd0), "v"(vWd1), "v"(vWr0), "v"(vWr1), "v"(vWr2), "s"(w4) : "v0", "v1", "v2", "v3", "v4", "v5", "v6", "v7", "v8", "v9", "v10", "v11", "v12", "v13", "v14", "v15", "v16", "v17", "v18", "v19", "v20", "v21", "v22", "v23", "v24", "v25", "v26", "v27", "v28", "v29", "v30", "v31", "v32", "v33", "v34", "v35", "v36", "v37", "v38", "v39", "v40", "v41", "v42", "v43", "v44", "v45", "v46", "v47", "v48", "v49", "v50", "v51", "v52", "v53", "v54", "v55", "v56", "v57", "v58", "v59", "v60", "v61", "v62", "v63", "v64", "v65", "v66", "v67", "v68", "v69", "v70", "v71", "v72", "v73", "v74", "v75", "v76", "v77", "v78", "v79", "v80", "v81", "v82", "v83", "v84", "v85", "v86", "v87", "v88", "v89", "v90", "v91", "v92", "v93", "v94", "v95", "v96", "v97", "v98", "v99", "v100", "v101", "v102", "v103", "v104", "v105", "v106", "v107", "v108", "v109", "v110", "v111", "v112", "v113", "v114", "v115", "v116", "v117", "v118", "v119", "v120", "v121", "v122", "v123", "v124", "v125", "v126", "v127", "v128", "v129", "v130", "v131", "v132", "v133", "v134", "v135", "v136", "v137", "v138", "v139", "v140", "v141", "v142", "v143", "v144", "v145", "v146", "v147", "v148", "v149", "v150", "v151", "v152", "v153", "v154", "v155", "v156", "v157", "v158", "v159", "v160", "v161", "v162", "v163", "v164", "v165", "v166", "v167", "v168", "v169", "v170", "v171", "v172", "v173", "v174", "v175", "v176", "v177", "v178", "v179", "v180", "v181", "v182", "v183", "v184", "v185", "v186", "v187", "v188", "v189", "v190", "v191", "v192", "v193", "v194", "v195", "v196", "v197", "v198", "v199", "v200", "v201", "v202", "v203", "v204", "v205", "v206", "v207", "v208", "v209", "v210", "v211", "v212", "v213", "v214", "v215", "v216", "v217", "v218", "v219", "v220", "v221", "v222", "v223", "v224", "v225", "v226", "v227", "v228", "v229", "v230", "v231", "v255", "a0", "a1", "a2", "a3", "a4", "a5", "a6", "a7", "a8", "a9", "a10", "a11", "a12", "a13", "a14", "a15", "a16", "a17", "a18", "a19", "a20", "a21", "a22", "a23", "a24", "a25", "a26", "a27", "a28", "a29", "a30", "a31", "a32", "a33", "a34", "a35", "a36", "a37", "a38", "a39", "a40", "a41", "a42", "a43", "a44", "a45", "a46", "a47", "a48", "a49", "a50", "a51", "a52", "a53", "a54", "a55", "a56", "a57", "a58", "a59", "a60", "a61", "a62", "a63", "a64", "a65", "a66", "a67", "a68", "a69", "a70", "a71", "a72", "a73", "a74", "a75", "a76", "a77", "a78", "a79", "a80", "a81", "a82", "a83", "a84", "a85", "a86", "a87", "a88", "a89", "a90", "a91", "a92", "a93", "a94", "a95", "a96", "a97", "a98", "a99", "a100", "a101", "a102", "a103", "a104", "a105", "a106", "a107", "a108", "a109", "a110", "a111", "a112", "a113", "a114", "a115", "a116", "a117", "a118", "a119", "a120", "a121", "a122", "a123", "a124", "a125", "a126", "a127", "a128", "a129", "a130", "a131", "a132", "a133", "a134", "a135", "a136", "a137", "a138", "a139", "a140", "a141", "a142", "a143", "a144", "a145", "a146", "a147", "a148", "a149", "a150", "a151", "a152", "a153", "a154", "a155", "a156", "a157", "a158", "a159", "a160", "a161", "a162", "a163", "a164", "a165", "a166", "a167", "a168", "a169", "a170", "a171", "a172", "a173", "a174", "a175", "a176", "a177", "a178", "a179", "a180", "a181", "a182", "a183", "a184", "a185", "a186", "a187", "a188", "a189", "a190", "a191", "a192", "a193", "a194", "a195", "a196", "a197", "a198", "a199", "a200", "a201", "a202", "a203", "a204", "a205", "a206", "a207", "a208", "a209", "a210", "a211", "a212", "a213", "a214", "a215", "a216", "a217", "a218", "a219", "a220", "a221", "a222", "a223", "a224", "a225", "a226", "a227", "a228", "a229", "a230", "a231", "a232", "a233", "a234", "a235", "a236", "a237", "a238", "a239", "a240", "a241", "a242", "a243", "a244", "a245", "a246", "a247", "a248", "a249", "a250", "a251", "a252", "a253", "a254", "a255", "s36", "s37", "s38", "s39", "s40", "s41", "s42", "s43", "s44", "s45", "s46", "s47", "s48", "s49", "s50", "s51", "s52", "s53", "s54", "s55", "s56", "s57", "s58", "s59", "s60", "s61", "s62", "s63", "s64", "s65", "s66", "s67", "s68", "s69", "s70", "s71", "s72", "s73", "s74", "s75", "s76", "s77", "s78", "s79", "s80", "s81", "s82", "s83", "s84", "s85", "s86", "s87", "s88", "s89", "s90", "s91", "s92", "s93", "s94", "s95", "s96", "s97", "s98", "s99", "vcc", "scc", "memory");
    __builtin_unreachable();
  } else {
    KARG(ws, 10) KARG(out_, 11)
    u16* out = (u16*)out_;
    LANE_ID(lane2);
    constexpr int NRF = N3 ? -16 * NX : N1 ? 2 : NRW;
    const int wk = kind == 6 ? slice : NRF + (kind - 1) * 32 + rep * 8 + slice;
    if (wk < 32 + NRF) rows_last(ws, out, wk, 32 + NRF, TE, lane2, wave, ep);
    rows_last(ws, out, TE + wk, 64 + NRF, 256 - FR * FINE, lane2, wave, ep);
    if (wk * 4 + wave < FINE * 16) {
#pragma unroll 1
      for (int i = FR; i > 0; --i) chunk_last(ws, out, (256 - i * FINE) * 16 + wk * 4 + wave, lane2, ep);
    }
    if (b == 0 && lane2 == 0 && wave == 0) __hip_atomic_store((gu32*)(ws + OFF_EP), ep, __ATOMIC_RELAXED, __HIP_MEMORY_SCOPE_AGENT);
    asm volatile("s_endpgm" ::: "memory");
    __builtin_unreachable();
  }
}

extern "C" void kernel_launch(void* const* d_in, const int* in_sizes, int n_in, void* d_out, int out_size,
                              void* d_ws, size_t ws_size, hipStream_t stream) {
  const u16* q = (const u16*)d_in[0];
  const u16* k = (const u16*)d_in[1];
  const u16* v = (const u16*)d_in[2];
  const u16* shift_k = (const u16*)d_in[3];
  const u16* ssm_k = (const u16*)d_in[4];
  const u16* ssm_D = (const u16*)d_in[5];
  const u16* w1 = (const u16*)d_in[7];
  const u16* w2 = (const u16*)d_in[8];
  const u16* w3 = (const u16*)d_in[9];
  const u16* w4 = (const u16*)d_in[10];
  flow_kernel<<<256, 256, 0, stream>>>(q, k, v, shift_k, ssm_k, ssm_D, w1, w2, w3, w4, (char*)d_ws, (u16*)d_out);
}
```

```cpp
#include <hip/hip_runtime.h>
#include <hip/hip_bf16.h>
#include <stdint.h>
#include <type_traits>

typedef short s16x8 __attribute__((ext_vector_type(8)));
typedef short s16x4 __attribute__((ext_vector_type(4)));
typedef float f32x4 __attribute__((ext_vector_type(4)));
typedef unsigned short u16;
typedef unsigned short u16x8 __attribute__((ext_vector_type(8)));
typedef unsigned short u16x4 __attribute__((ext_vector_type(4)));
typedef float f32x2 __attribute__((ext_vector_type(2)));
typedef __bf16 bf16x2 __attribute__((ext_vector_type(2)));
typedef unsigned u32x4v __attribute__((ext_vector_type(4)));
typedef __attribute__((address_space(1))) unsigned gu32;

constexpr int S_ = 2048, D_ = 1024, F_ = 3584;

constexpr int N1 = 195, N3 = 0, NX = 1, PL1 = 264, KE = 1, RT1 = 0, LB = 66, FL_BYTES = 2141312, WS_USED = 122727552;
constexpr int OFF_KVR = 4194304, OFF_YF = 8388608, OFF_XP = 12582912, OFF_HS = 22085632, OFF_X1 = 31457280, OFF_G = 40894464, OFF_P0 = 71303168, OFF_DBG = 80740352, OFF_P1 = 89128960, OFF_QS = 98566144, OFF_P2 = 99614720, OFF_P3 = 109051904, OFF_S1 = 118489088, OFF_ST = 119013376, OFF_FL = 120586240, OFF_FXP = 120587264, OFF_FH = 120859648, OFF_FX1 = 121132032, OFF_FP = 121405440, OFF_FG = 121950720, OFF_FKV = 122629120, OFF_FY = 122661888, OFF_EP = 122727424;


__device__ __forceinline__ float bf2f(u16 v) { return __uint_as_float(((unsigned)v) << 16); }
__device__ __forceinline__ u16 f2bf(float f) { return __builtin_bit_cast(u16, (__bf16)f); }
__device__ __forceinline__ float rbf(float f) { return bf2f(f2bf(f)); }
__device__ __forceinline__ void st16(void* p, u16x8 v) {
  asm volatile("global_store_dwordx4 %0, %1, off sc1\n\ts_nop 1" :: "v"(p), "v"(v) : "memory");
}
__device__ __forceinline__ void bst16(u32x4v v, __amdgpu_buffer_rsrc_t rs, unsigned vo, unsigned so) {
  asm volatile("buffer_store_dwordx4 %0, %1, %2, %3 offen sc1\n\ts_nop 1" :: "v"(v), "v"(vo), "s"(rs), "s"(so) : "memory");
}
#define WAIT_VM(n) asm volatile("s_waitcnt vmcnt(%0)" ::"n"(n) : "memory")
#define WAIT_LGKM0() asm volatile("s_waitcnt lgkmcnt(0)" ::: "memory")

__device__ __forceinline__ void touch_lds(const char* p, unsigned a) {
  asm volatile("s_mov_b32 m0, %1\n\ts_nop 0\n\tglobal_load_lds_dword %0, off" :: "v"(p), "s"(a) : "memory");
}
#define YSLEEP 4
#define KSLEEP 2
__device__ __forceinline__ f32x2 up2(unsigned p) { return (f32x2){__uint_as_float(p << 16), __uint_as_float(p & 0xffff0000u)}; }
__device__ __forceinline__ unsigned pk2(f32x2 v) { return __builtin_bit_cast(unsigned, __builtin_convertvector(v, bf16x2)); }
__device__ __forceinline__ f32x2 rbf2(f32x2 v) { return up2(pk2(v)); }
#define BARRIER() asm volatile("s_waitcnt lgkmcnt(0)\n\ts_barrier" ::: "memory")
template <int N, class F> __device__ __forceinline__ void sfor(F&& f) {
  if constexpr (N > 0) { sfor<N - 1>(f); f(std::integral_constant<int, N - 1>{}); }
}
template <int NL>
__device__ __forceinline__ void poll_flags(const char* p, int n, int lane, unsigned ep) {
#pragma unroll 1
  for (int it = 0; it < 20000; ++it) {
    bool ok = true;
#pragma unroll
    for (int i = 0; i < NL; ++i) {
      const int f = lane + 64 * i;
      ok &= __hip_atomic_load((gu32*)(p + (f < n ? f : 0) * 128), __ATOMIC_RELAXED, __HIP_MEMORY_SCOPE_AGENT) == ep;
    }
    if (__all(ok)) break;
    __builtin_amdgcn_s_sleep(YSLEEP);
  }
  asm volatile("" ::: "memory");
}

constexpr int CV_HP = (2048 + 32) * 2;
constexpr int CV_UR = (2048 + 256) * 2;
constexpr int CV_CH = 2 * CV_HP + CV_UR;
#define T0 0
#define T0R 4
#define T1 0
#define LATEQ 4
constexpr int CV_STG = 8 * CV_CH;

struct KvRegs { u32x4v k0, k1, k2, k3, k4, k5, k6, v0, v1, v2, v3; };
struct SkRegs { u32x4v s0, s1, s2, s3; };
#define LD16(d, p, pol) asm volatile("global_load_dwordx4 %0, %1, off " pol : "=v"(d) : "v"(p) : "memory")
#define LDS16(d, vo, sp, o, pol) asm volatile("global_load_dwordx4 %0, %1, %2 offset:" #o " " pol : "=v"(d) : "v"(vo), "s"(sp) : "memory")
__device__ __forceinline__ void kv_load(KvRegs& R, const u16* __restrict__ kin, const u16* __restrict__ vin, int b, int tid) {
  const unsigned tr = (b >> 2) * 32 + 4 * (tid >> 5), oa = tr * 2048 + (b & 3) * 512 + 16 * (tid & 31) + 4096;
  LDS16(R.k3, oa, kin, -4096, "nt"); LDS16(R.k4, oa, kin, -2048, "nt"); LDS16(R.k5, oa, kin, 0, "nt"); LDS16(R.k6, oa, kin, 2048, "nt");
  LDS16(R.v0, oa, vin, -4096, "nt"); LDS16(R.v1, oa, vin, -2048, "nt"); LDS16(R.v2, oa, vin, 0, "nt"); LDS16(R.v3, oa, vin, 2048, "nt");
  const unsigned ob = tr ? oa - 6144 : oa;
  LDS16(R.k0, ob, kin, -4096, "nt"); LDS16(R.k1, ob, kin, -2048, "nt"); LDS16(R.k2, ob, kin, 0, "nt");
}
__device__ __forceinline__ void sk_load(SkRegs& R, const u16* __restrict__ shift_k, int b, int tid) {
  const unsigned o = (b & 3) * 2048 + 64 * (tid & 31);
  LDS16(R.s0, o, shift_k, 0, ""); LDS16(R.s1, o, shift_k, 16, ""); LDS16(R.s2, o, shift_k, 32, ""); LDS16(R.s3, o, shift_k, 48, "");
}
template <int N>
__device__ __forceinline__ void kv_wait(KvRegs& R, SkRegs& Q) {
  asm volatile("s_waitcnt vmcnt(%15)" : "+v"(R.k0), "+v"(R.k1), "+v"(R.k2), "+v"(R.k3), "+v"(R.k4), "+v"(R.k5), "+v"(R.k6), "+v"(R.v0), "+v"(R.v1), "+v"(R.v2),
               "+v"(R.v3), "+v"(Q.s0), "+v"(Q.s1), "+v"(Q.s2), "+v"(Q.s3) : "n"(N) : "memory");
}
__device__ __forceinline__ void kv_store(KvRegs& R, const SkRegs& Q, char* __restrict__ ws, char* T, int b, int tid) {
  const int t0 = (b >> 2) * 32, c0 = (b & 3) * 256, ch8 = tid & 31, rg = tid >> 5;
  if (t0 + 4 * rg < 3) R.k0 = R.k1 = R.k2 = (u32x4v){0u, 0u, 0u, 0u};
  typedef unsigned u32x2 __attribute__((ext_vector_type(2)));
  const u32x4v kr[7] = {R.k0, R.k1, R.k2, R.k3, R.k4, R.k5, R.k6}, vr[4] = {R.v0, R.v1, R.v2, R.v3}, sk[4] = {Q.s0, Q.s1, Q.s2, Q.s3};
#pragma unroll
  for (int j2 = 0; j2 < 4; ++j2) {
    const f32x2 sa = up2(sk[j2][0]), sb = up2(sk[j2][1]), sc = up2(sk[j2][2]), sd = up2(sk[j2][3]);
    const f32x2 s0 = {sa[0], sc[0]}, s1 = {sa[1], sc[1]}, s2 = {sb[0], sd[0]}, s3 = {sb[1], sd[1]};
    f32x2 kf[7], o[4];
#pragma unroll
    for (int r = 0; r < 7; ++r) kf[r] = up2(kr[r][j2]);
#pragma unroll
    for (int r = 0; r < 4; ++r)
      o[r] = __builtin_elementwise_fma(kf[r + 3], s0, __builtin_elementwise_fma(kf[r + 2], s1, __builtin_elementwise_fma(kf[r + 1], s2, kf[r] * s3))) * up2(vr[r][j2]);
#pragma unroll
    for (int e = 0; e < 2; ++e)
      *(u32x2*)(T + ch8 * 520 + (2 * j2 + e) * 64 + (28 - 4 * rg) * 2) = (u32x2){pk2((f32x2){o[3][e], o[2][e]}), pk2((f32x2){o[1][e], o[0][e]})};
  }
  BARRIER();
  u16* kvr = (u16*)(ws + OFF_KVR);
#pragma unroll
  for (int i = 0; i < 4; ++i) {
    const int ch = (tid >> 2) + 64 * i, cb = tid & 3;
    const char* p = T + ch * 64 + (ch >> 3) * 8 + cb * 16;
    const u16x4 lo = *(const u16x4*)p, hi = *(const u16x4*)(p + 8);
    st16(kvr + (size_t)(c0 + ch) * S_ + (2016 - t0) + cb * 8, __builtin_shufflevector(lo, hi, 0, 1, 2, 3, 4, 5, 6, 7));
  }
}

typedef unsigned u32x2v __attribute__((ext_vector_type(2)));
__device__ __forceinline__ void h_load(u32x4v (&hv)[8], unsigned& hx, unsigned& dD, const u16* __restrict__ ssm_k, const u16* __restrict__ ssm_D, int c0, int tid) {
  const __amdgpu_buffer_rsrc_t rs = __builtin_amdgcn_make_buffer_rsrc((void*)ssm_k, 0, c0 < 0 ? 0 : D_ * S_ * 2, 0x00020000);
  const unsigned cb = c0 < 0 ? 0 : c0, lane = tid & 63, wave = tid >> 6;
#pragma unroll
  for (int i = 0; i < 8; ++i) asm volatile("buffer_load_dwordx4 %0, %1, %2, %3 offen nt" : "=v"(hv[i]) : "v"(tid * 16), "s"(rs), "s"((cb + i) * 4096) : "memory");
  asm volatile("buffer_load_dword %0, %1, %2, %3 offen" : "=v"(hx) : "v"((lane & 7) * 4096 + ((wave + 1) & 3) * 1024), "s"(rs), "s"(cb * 4096) : "memory");
  asm volatile("global_load_dword %0, %1, %2" : "=v"(dD) : "v"(cb * 2 + wave * 4), "s"(ssm_D) : "memory");
}
template <int N>
__device__ __forceinline__ void h_wait(u32x4v (&hv)[8], unsigned& hx, unsigned& dD) {
  asm volatile("s_waitcnt vmcnt(%10)" : "+v"(hv[0]), "+v"(hv[1]), "+v"(hv[2]), "+v"(hv[3]), "+v"(hv[4]), "+v"(hv[5]), "+v"(hv[6]), "+v"(hv[7]), "+v"(hx), "+v"(dD) : "n"(N) : "memory");
}
__device__ __forceinline__ void h_spread(const u32x4v (&hv)[8], unsigned hx, char* lds, int tid) {
  if (tid < 32 && (tid & 3) != 3) *(u32x4v*)(lds + (tid >> 2) * CV_CH + ((tid >> 1) & 1) * CV_HP + (tid & 1) * 16) = (u32x4v){0u, 0u, 0u, 0u};
  *(u32x4v*)(lds + (tid >> 5) * CV_CH + 2 * CV_HP + 4096 + (tid & 31) * 16) = (u32x4v){0u, 0u, 0u, 0u};
#pragma unroll
  for (int i = 0; i < 8; ++i) {
    char* base = lds + i * CV_CH + 32 + tid * 16;
    unsigned hn = __builtin_amdgcn_update_dpp(0, (int)hv[i][0], 0x130, 0xf, 0xf, true);
    const unsigned last = __builtin_amdgcn_readlane(hx, i);
    if ((tid & 63) == 63) hn = last;
#define AB(a, b) __builtin_amdgcn_alignbyte(a, b, 2)
    if (tid == 0) *(u32x4v*)(lds + i * CV_CH + CV_HP + 16) = (u32x4v){0u, 0u, 0u, hv[i][0] << 16};
    *(u32x4v*)(base) = hv[i];
    *(u32x4v*)(base + CV_HP) = (u32x4v){AB(hv[i][1], hv[i][0]), AB(hv[i][2], hv[i][1]), AB(hv[i][3], hv[i][2]), AB(hn, hv[i][3])};
#undef AB
  }
}

__device__ __forceinline__ void pads(char* __restrict__ ws, int w, int lane, unsigned ep) {
  if (w < 132) {
    const int k = w < 128 ? w >> 5 : w - 128, u = k == 0 ? -1 : k == 1 ? 128 : k == 2 ? 131 : 260;
    st16(ws + (w < 128 ? OFF_HS + u * 32768 + (w & 31) * 1024 : OFF_FH + u * 1024) + lane * 16, __builtin_bit_cast(u16x8, (u32x4v){w >= 128 && !(lane & 7) ? ep : 0u, 0u, 0u, 0u}));
  }
}
__device__ __forceinline__ void prewrite(char* __restrict__ ws, unsigned off, unsigned str, int rep, int tid) {
#pragma unroll
  for (int i = 0; i < 2; ++i) st16(ws + off + (rep + 4 * (tid >> 3) + 132 * i) * str + (tid & 7) * 16, __builtin_bit_cast(u16x8, (u32x4v){0u, 0u, 0u, 0u}));
}

__device__ __forceinline__ void conv_part(char* __restrict__ ws, unsigned dD, char* lds, int g, int tid, int lane, int wave, unsigned ep, const char* wp, unsigned wstep) {
  const __amdgpu_buffer_rsrc_t rs = __builtin_amdgcn_make_buffer_rsrc(ws, 0, WS_USED, 0x00020000);
  const u16* kvr = (const u16*)(ws + OFF_KVR) + (size_t)(8 * g + 2 * wave) * S_;
  auto wait1 = [&](unsigned off) {
#pragma unroll 1
    for (int it = 0; it < 20000; ++it) {
      if (__all(__hip_atomic_load((gu32*)(ws + off), __ATOMIC_RELAXED, __HIP_MEMORY_SCOPE_AGENT) == ep)) break;
      __builtin_amdgcn_s_sleep(KSLEEP);
    }
    asm volatile("" ::: "memory");
  };
  const unsigned lb = (unsigned)(size_t)lds;
  auto u_fetch = [&](int part) {
#pragma unroll
    for (int j = 0; j < 2; ++j)
      asm volatile("s_mov_b32 m0, %1\n\ts_nop 0\n\tglobal_load_lds_dwordx4 %0, off sc1" :: "v"(kvr + j * S_ + part * 512 + lane * 8),
                   "s"(lb + (2 * wave + j) * CV_CH + 2 * CV_HP + part * 1024) : "memory");
  };
  const unsigned xo = (tid >> 4) * 32768 + g * 256 + (tid & 15) * 16;
  u32x4v qv[6];
  u32x2v qs[6];
  auto q_fetch = [&](int s0, int ns) {
#pragma unroll
    for (int i = 0; i < 6; ++i)
      if (i < ns) {
        asm volatile("buffer_load_dwordx4 %0, %1, %2, %3 offen sc1" : "=v"(qv[i]) : "v"(xo), "s"(rs), "s"(OFF_XP + (s0 + i) * 524288) : "memory");
        asm volatile("buffer_load_dwordx2 %0, %1, %2, %3 offen sc1" : "=v"(qs[i]) : "v"(tid * 8), "s"(rs), "s"(OFF_QS + (s0 + i) * 2048) : "memory");
      }
  };
  const int cg = g >> 5;
  const int l15 = lane & 15, lg = lane >> 4, cp = (l15 + 1) & 1;
  const unsigned ao = cp * CV_HP + 2 * (8 * lg + (l15 + 1 - cp)), bo = 2 * CV_HP + 2 * (2032 - 16 * l15 + 8 * lg) - 2 * 256 * 7;
  const unsigned own = lb + 2 * wave * CV_CH;
  char* stg = lds + CV_STG;
  s16x8 ad[2];
#pragma unroll
  for (int j = 0; j < 2; ++j) {
    const int e = 15 - l15 - 8 * lg;
    const unsigned d = (j ? dD >> 16 : dD & 0xffffu) << ((e & 1) * 16);
    ad[j] = __builtin_bit_cast(s16x8, (u32x4v){e >> 1 == 0 ? d : 0u, e >> 1 == 1 ? d : 0u, e >> 1 == 2 ? d : 0u, e >> 1 == 3 ? d : 0u});
  }
  f32x4 acc[2][6];
  unsigned f0, f1;
  bool mid;
  auto finish = [&](auto s0_c, auto ns_c) {
    constexpr int S0 = decltype(s0_c)::value, NS = decltype(ns_c)::value, A0 = S0 == 2 ? 0 : 4, Q0 = S0 == 6 ? 4 : 0;
#pragma unroll
    for (int s = 0; s < NS; ++s)
#pragma unroll
      for (int r = 0; r < 4; ++r) *(unsigned*)(stg + (Q0 + s) * 4352 + l15 * 272 + (4 * lg + r) * 16 + wave * 4) = pk2((f32x2){acc[0][A0 + s][r], acc[1][A0 + s][r]});
    if (S0 == 0) {
      asm volatile("s_waitcnt vmcnt(0)" : "+v"(qv[0]), "+v"(qv[1]), "+v"(qs[0]), "+v"(qs[1]), "+v"(f0) :: "memory");
      mid = __all(f0 == ep);
      if (mid) { u_fetch(2); u_fetch(1); }
    } else if (S0 == 2) asm volatile("" : "+v"(qv[0]), "+v"(qv[1]), "+v"(qv[2]), "+v"(qv[3]), "+v"(qv[4]), "+v"(qv[5]), "+v"(qs[0]), "+v"(qs[1]), "+v"(qs[2]), "+v"(qs[3]), "+v"(qs[4]), "+v"(qs[5]) :: "memory");
    BARRIER();
    u32x4v y4[NS];
#pragma unroll
    for (int s = 0; s < NS; ++s) {
      y4[s] = *(const u32x4v*)(stg + (Q0 + s) * 4352 + (tid >> 4) * 272 + (tid & 15) * 16);
      const f32x2 rd2 = {__uint_as_float(qs[Q0 + s][0]), __uint_as_float(qs[Q0 + s][0])}, nm2 = {__uint_as_float(qs[Q0 + s][1]), __uint_as_float(qs[Q0 + s][1])};
      u32x4v o;
#pragma unroll
      for (int e = 0; e < 4; ++e) {
        const f32x2 x = up2(qv[Q0 + s][e]);
        o[e] = pk2(__builtin_elementwise_fma(__builtin_elementwise_fma(x, rd2, nm2), up2(y4[s][e]), x));
      }
      bst16(o, rs, xo, OFF_XP + (S0 + s) * 524288);
    }
#pragma unroll
    for (int s = 0; s < NS; ++s) bst16(y4[s], rs, (tid >> 3) * 16384 + g * 128 + (tid & 7) * 16, OFF_YF + (S0 + s) * 524288);
  };
  auto touches = [&](int n) {
#pragma unroll
    for (int i = 0; i < 3; ++i)
      if (i < n) { touch_lds(wp, lb + 163584); wp += wstep; }
  };
  auto count = [&](int s0, int ns, auto n_c) {
    WAIT_VM(decltype(n_c)::value);
    BARRIER();
    if (tid < 4 * ns) __hip_atomic_fetch_add((gu32*)(ws + OFF_FXP + (s0 + (tid >> 2)) * 16384 + (tid & 3) * 1024 + (g >> 4) * 128), 1u, __ATOMIC_RELAXED, __HIP_MEMORY_SCOPE_AGENT);
  };
  auto pair = [&](unsigned bb) {
    u32x2v ax[2][2][2][2];
    s16x8 bx[2][2][2];
    auto rdA = [&](int q) {
#pragma unroll
      for (int j = 0; j < 2; ++j)
#pragma unroll
        for (int m = 0; m < 2; ++m) {
          asm volatile("ds_read2_b32 %0, %1 offset0:%2 offset1:%3" : "=v"(ax[q & 1][j][m][0]) : "v"(own + j * CV_CH + ao), "n"(128 * m + 16 * q), "n"(128 * m + 16 * q + 1));
          asm volatile("ds_read2_b32 %0, %1 offset0:%2 offset1:%3" : "=v"(ax[q & 1][j][m][1]) : "v"(own + j * CV_CH + ao), "n"(128 * m + 16 * q + 2), "n"(128 * m + 16 * q + 3));
          asm volatile("ds_read_b128 %0, %1 offset:%2" : "=v"(bx[q & 1][j][m]) : "v"(bb), "n"(j * CV_CH + 512 * (1 - m) + 64 * q));
        }
    };
    rdA(0);
    sfor<8>([&](auto q_c) {
      constexpr int q = decltype(q_c)::value, p = q & 1;
      if (q + 1 < 8) rdA(q + 1);
      asm volatile("s_waitcnt lgkmcnt(%0)" :: "n"(q + 1 < 8 ? 12 : 0) : "memory");
      __builtin_amdgcn_sched_barrier(0);
#pragma unroll
      for (int j = 0; j < 2; ++j) {
        const s16x8 a0f = __builtin_bit_cast(s16x8, __builtin_shufflevector(ax[p][j][0][0], ax[p][j][0][1], 0, 1, 2, 3)), a1f = __builtin_bit_cast(s16x8, __builtin_shufflevector(ax[p][j][1][0], ax[p][j][1][1], 0, 1, 2, 3));
        if (q == 0) {
          acc[j][4] = __builtin_amdgcn_mfma_f32_16x16x32_bf16(ad[j], bx[p][j][0], acc[j][4], 0, 0, 0);
          acc[j][5] = __builtin_amdgcn_mfma_f32_16x16x32_bf16(ad[j], bx[p][j][1], acc[j][5], 0, 0, 0);
        }
        acc[j][4] = __builtin_amdgcn_mfma_f32_16x16x32_bf16(a0f, bx[p][j][0], acc[j][4], 0, 0, 0);
        acc[j][5] = __builtin_amdgcn_mfma_f32_16x16x32_bf16(a0f, bx[p][j][1], acc[j][5], 0, 0, 0);
        acc[j][5] = __builtin_amdgcn_mfma_f32_16x16x32_bf16(a1f, bx[p][j][0], acc[j][5], 0, 0, 0);
      }
      __builtin_amdgcn_sched_barrier(0);
    });
  };
  wait1(OFF_FKV + ((lane & 15) * 4 + cg) * 128);
  u_fetch(3);
  wait1(OFF_FY + (lane & 31) * 128);
  q_fetch(0, 2);
  const unsigned fm = OFF_FKV + ((16 + (lane & 31)) * 4 + cg) * 128;
  asm volatile("global_load_dword %0, %1, %2 sc1" : "=v"(f0) : "v"(fm), "s"(ws) : "memory");
#pragma unroll
  for (int i = 0; i < 4; ++i) acc[i >> 1][4 + (i & 1)] = (f32x4){0.f, 0.f, 0.f, 0.f};
  pair(own + bo + 512 * 6);
  finish(std::integral_constant<int, 0>{}, std::integral_constant<int, 2>{});
  u32x2v a0[8][2], a1[8][2];
  s16x8 b0[6], b1[6];
  unsigned pa[4], pb = own + bo;
#pragma unroll
  for (int i = 0; i < 4; ++i) pa[i] = own + ao + 1024 * i;
  auto rd1 = [&](int rr, int i, u32x2v (&ah)[8][2], s16x8 (&bfr)[6]) {
    const int m = i < 18 ? i / 3 : 6 + (i - 18) / 2, e = i < 18 ? i % 3 : i & 1;
    if (e < 2) asm volatile("ds_read2_b32 %0, %1 offset0:%2 offset1:%3" : "=v"(ah[m][e & 1]) : "v"(pa[m >> 1]), "n"(128 * (m & 1) + 2 * e + 16 * rr), "n"(128 * (m & 1) + 2 * e + 16 * rr + 1));
    else asm volatile("ds_read_b128 %0, %1 offset:%2" : "=v"(bfr[m < 6 ? m : 0]) : "v"(pb), "n"(512 * (7 - m) + 64 * rr));
  };
  auto round = [&](f32x4 (&ac)[6], u32x2v (&ah)[8][2], s16x8 (&bfr)[6], u32x2v (&ahn)[8][2], s16x8 (&bfn)[6], int rr, unsigned step) {
    int k = 0;
#pragma unroll
    for (int s = 2; s <= 7; ++s)
#pragma unroll
      for (int m = 0; m <= s; ++m) {
        if (s - m > 5) continue;
        const s16x8 af = __builtin_bit_cast(s16x8, __builtin_shufflevector(ah[m][0], ah[m][1], 0, 1, 2, 3));
        ac[s - 2] = __builtin_amdgcn_mfma_f32_16x16x32_bf16(af, bfr[s - m > 5 ? 0 : s - m], ac[s - 2], 0, 0, 0);
        __builtin_amdgcn_sched_barrier(0);
        if (m == s) asm volatile("s_waitcnt lgkmcnt(12)" ::: "memory");
#pragma unroll
        for (int i = 0; i < 22; ++i)
          if (i * 4 / 3 == k) {
            rd1(rr, i, ahn, bfn);
            if (rr && (i == 4 || i == 10 || i == 16 || i == 21)) asm volatile("v_add_u32 %0, %1, %0" : "+v"(pa[i / 6]) : "s"(step));
            if (rr && i == 17) asm volatile("v_add_u32 %0, %1, %0" : "+v"(pb) : "s"(step));
          }
        ++k;
        __builtin_amdgcn_sched_barrier(0);
      }
  };
  if (!mid) { wait1(fm); u_fetch(2); u_fetch(1); WAIT_VM(0); }
  WAIT_VM(4);
#pragma unroll
  for (int i = 0; i < 22; ++i) rd1(0, i, a0, b0);
  WAIT_LGKM0();
  auto channel = [&](auto j_c, auto r0_c, auto r1_c) {
    constexpr int j = decltype(j_c)::value, r0 = decltype(r0_c)::value, r1 = decltype(r1_c)::value;
    __builtin_amdgcn_sched_barrier(0);
    if (r0 == 0) {
#pragma unroll
      for (int s = 2; s <= 7; ++s) acc[j][s - 2] = s < 6 ? __builtin_amdgcn_mfma_f32_16x16x32_bf16(ad[j], b0[s], (f32x4){0.f, 0.f, 0.f, 0.f}, 0, 0, 0) : (f32x4){0.f, 0.f, 0.f, 0.f};
    }
#pragma unroll 1
    for (int r = r0; r < r1; r += 2) {
      if (j == 0 && r == 2) {
        asm volatile("s_waitcnt vmcnt(0)\n\ts_barrier" ::: "memory");
        if (tid < 8) __hip_atomic_fetch_add((gu32*)(ws + OFF_FXP + (tid >> 2) * 16384 + (tid & 3) * 1024 + (g >> 4) * 128), 1u, __ATOMIC_RELAXED, __HIP_MEMORY_SCOPE_AGENT);
      }
      if (T0 && j == 0 && r == T0R) touches(T0);
      round(acc[j], a0, b0, a1, b1, 1, r < 6 ? 128 : j ? 0 : CV_CH - 384);
      round(acc[j], a1, b1, a0, b0, 0, 0);
    }
    WAIT_LGKM0();
    __builtin_amdgcn_sched_barrier(0);
  };
  typedef std::integral_constant<int, 0> I0;
  typedef std::integral_constant<int, 1> I1;
  channel(I0{}, I0{}, std::integral_constant<int, 8>{});
  channel(I1{}, I0{}, std::integral_constant<int, LATEQ - 2>{});
  const unsigned fa = OFF_FY + (32 + lane) * 128, fb = lane < 32 ? OFF_FY + (96 + lane) * 128 : OFF_FKV + ((48 + (lane & 15)) * 4 + cg) * 128;
  asm volatile("global_load_dword %0, %1, %2 sc1" : "=v"(f0) : "v"(fa), "s"(ws) : "memory");
  asm volatile("global_load_dword %0, %1, %2 sc1" : "=v"(f1) : "v"(fb), "s"(ws) : "memory");
  channel(I1{}, std::integral_constant<int, LATEQ - 2>{}, std::integral_constant<int, LATEQ>{});
  asm volatile("s_waitcnt vmcnt(0)" : "+v"(f0), "+v"(f1) :: "memory");
  if (!__all(f0 == ep && f1 == ep)) { wait1(fa); wait1(fb); }
  u_fetch(0);
  q_fetch(2, 6);
  touches(T1);
  channel(I1{}, std::integral_constant<int, LATEQ>{}, std::integral_constant<int, 8>{});
  WAIT_VM(T1);
  finish(std::integral_constant<int, 2>{}, std::integral_constant<int, 4>{});
  pair(own + bo);
  count(2, 4, I0{});
  finish(std::integral_constant<int, 6>{}, std::integral_constant<int, 2>{});
  touches(3 - T0 - T1);
  count(6, 2, std::integral_constant<int, 3 - T0 - T1>{});
}
__device__ __forceinline__ float sum2_seg(float s, float q) {
  s += __builtin_bit_cast(float, __builtin_amdgcn_update_dpp(0, __builtin_bit_cast(int, s), 0x128, 0xf, 0xf, true));
  q += __builtin_bit_cast(float, __builtin_amdgcn_update_dpp(0, __builtin_bit_cast(int, q), 0x128, 0xf, 0xf, true));
  asm volatile("s_nop 1\n\tv_permlane16_swap_b32 %0, %1" : "+v"(s), "+v"(q));
  s += q;
  q = s;
  asm volatile("s_nop 1\n\tv_permlane32_swap_b32 %0, %1" : "+v"(s), "+v"(q));
  return s + q;
}
#define LASTSLEEP 8
__device__ __forceinline__ void wait_flags(const char* p, int lane, int n, unsigned ep) {
  gu32* f = (gu32*)(p + (lane < n ? lane : 0) * 128);
#pragma unroll 1
  for (int it = 0; it < 10000; ++it) {
    if (__all(__hip_atomic_load(f, __ATOMIC_RELAXED, __HIP_MEMORY_SCOPE_AGENT) == ep)) break;
    __builtin_amdgcn_s_sleep(LASTSLEEP);
  }
  asm volatile("" ::: "memory");
}
struct QRegs { u32x4v a[4], b[4]; };
__device__ __forceinline__ void q_load(QRegs& R, const u16* __restrict__ q, int f, int lane, int wave) {
  const unsigned o = (f * 16 + (lane & 7)) * 2048 + wave * 512 + (lane >> 3) * 16, o2 = o + 16384;
  LDS16(R.a[0], o, q, 0, "nt"); LDS16(R.b[0], o2, q, 0, "nt"); LDS16(R.a[1], o, q, 128, "nt"); LDS16(R.b[1], o2, q, 128, "nt");
  LDS16(R.a[2], o, q, 256, "nt"); LDS16(R.b[2], o2, q, 256, "nt"); LDS16(R.a[3], o, q, 384, "nt"); LDS16(R.b[3], o2, q, 384, "nt");
}
template <int N>
__device__ __forceinline__ void q_wait(QRegs& R, unsigned& e) {
  asm volatile("s_waitcnt vmcnt(%9)" : "+v"(R.a[0]), "+v"(R.a[1]), "+v"(R.a[2]), "+v"(R.a[3]), "+v"(R.b[0]), "+v"(R.b[1]), "+v"(R.b[2]), "+v"(R.b[3]), "+v"(e) : "n"(N) : "memory");
}
__device__ __forceinline__ void row_stats(const u32x4v (&xq)[4], char* lds, int k, int lane, int wave, float& mean, float& var) {
  f32x2* red = (f32x2*)(lds + 16640);
  const int r8 = lane & 7, seg = lane >> 3;
  f32x2 s = {0.f, 0.f}, sq = {0.f, 0.f};
#pragma unroll
  for (int i = 0; i < 16; ++i) { const f32x2 x = up2(xq[i >> 2][i & 3]); s += x; sq = __builtin_elementwise_fma(x, x, sq); }
  const float a = sum2_seg(s[0] + s[1], sq[0] + sq[1]);
  if ((seg & 5) == 0) ((float*)red)[((k * 4 + wave) * 8 + r8) * 2 + (seg >> 1)] = a;
  BARRIER();
  const f32x2 t = (red[(k * 4) * 8 + r8] + red[(k * 4 + 1) * 8 + r8]) + (red[(k * 4 + 2) * 8 + r8] + red[(k * 4 + 3) * 8 + r8]);
  mean = t[0] * (1.0f / 1024.0f);
  var = fmaxf(t[1] * (1.0f / 1024.0f) - mean * mean, 0.f);
}
__device__ __forceinline__ void q_store(char* __restrict__ ws, int t, int lane, int wave, const u32x4v (&xq)[4], float mean, float var) {
  const __amdgpu_buffer_rsrc_t rs = __builtin_amdgcn_make_buffer_rsrc(ws, 0, WS_USED, 0x00020000);
  const int r8 = lane & 7, seg = lane >> 3;
  const unsigned fo = (t >> 1) * 32768 + (wave * 8 + (seg >> 2)) * 1024 + ((seg & 3) * 16 + (t & 1) * 8 + r8) * 16;
#pragma unroll
  for (int i = 0; i < 4; ++i) bst16(xq[i], rs, fo + i * 2048, OFF_XP);
  const float rd = __builtin_amdgcn_rsqf(var + 1e-5f);
  if (wave == 0 && seg == 0) __builtin_amdgcn_raw_buffer_store_b64((u32x2v){__float_as_uint(rd), __float_as_uint(-mean * rd)}, rs, t * 64 + r8 * 8, OFF_QS, 16);
}
#define FINE (N3 ? 16 - 4 * NX : N1 ? 16 : 20)
template <int NC>
__device__ __forceinline__ void sums_last(char* __restrict__ ws, u16* __restrict__ out, int c, int step, int end, int nx, int lane, unsigned ep, unsigned& f) {
  const __amdgpu_buffer_rsrc_t rs = __builtin_amdgcn_make_buffer_rsrc(ws, 0, WS_USED, 0x00020000);
  auto flags = [&](int x) { return __hip_atomic_load((gu32*)(ws + OFF_FP + (2 * LB + (x >> 5)) * 2048 + ((lane & 3) * 4 + ((x >> 2) & 3)) * 128), __ATOMIC_RELAXED, __HIP_MEMORY_SCOPE_AGENT); };
  if (c >= end) f = ep + 1;
#pragma unroll 1
  for (; c < end; c += step) {
#pragma unroll 1
    for (int it = 0; it < 20000 && !__all(f == ep); ++it) {
      if (it) __builtin_amdgcn_s_sleep(NC == 4 ? LASTSLEEP : 4);
      f = flags(c);
    }
    asm volatile("" ::: "memory");
    const unsigned fo = PL1 * 16384 + c * 1024 + lane * 16;
    u32x4v p0[NC], p1[NC], p2[NC], p3[NC], o[NC];
#pragma unroll
    for (int i = 0; i < NC; ++i) {
      p0[i] = __builtin_bit_cast(u32x4v, __builtin_amdgcn_raw_buffer_load_b128(rs, fo + i * 1024, OFF_P0, 16));
      p1[i] = __builtin_bit_cast(u32x4v, __builtin_amdgcn_raw_buffer_load_b128(rs, fo + i * 1024, OFF_P1, 16));
      p2[i] = __builtin_bit_cast(u32x4v, __builtin_amdgcn_raw_buffer_load_b128(rs, fo + i * 1024, OFF_P2, 16));
      p3[i] = __builtin_bit_cast(u32x4v, __builtin_amdgcn_raw_buffer_load_b128(rs, fo + i * 1024, OFF_P3, 16));
    }
    f = flags(min(c + step < end ? c + step : nx, 4095));
#pragma unroll
    for (int i = 0; i < NC; ++i)
#pragma unroll
      for (int e = 0; e < 4; ++e) o[i][e] = pk2((up2(p0[i][e]) + up2(p1[i][e])) + (up2(p2[i][e]) + up2(p3[i][e])));
    asm volatile("" : "+v"(f));
#pragma unroll
    for (int i = 0; i < NC; ++i) st16(out + (size_t)((c >> 4) * 8 + (lane & 7)) * D_ + ((c & 15) + i) * 64 + (lane >> 3) * 8, __builtin_bit_cast(u16x8, o[i]));
  }
}

__device__ __forceinline__ int swzB(int r) { return ((((r >> 1) & 1) | (((r >> 3) & 1) << 1)) << 1); }
#define FLW 1
#define MAP3 1
#define MAP12 1
#define FLIP 0
#define KVL 0
#define RE 0
#define ROT 0
#define TE 126
#define FR2 0
#define FR 4
#define NRW 18
#define PFROWS 110
__global__ __launch_bounds__(256, 1) void flow_kernel(const u16* __restrict__ q, const u16* __restrict__ kin, const u16* __restrict__ vin,
                                                      const u16* __restrict__ shift_k, const u16* __restrict__ ssm_k, const u16* __restrict__ ssm_D,
                                                      const u16* __restrict__ w1, const u16* __restrict__ w2,
                                                      const u16* __restrict__ w3, const u16* __restrict__ w4, char* __restrict__ ws,
                                                      u16* __restrict__ out) {
  __shared__ __attribute__((aligned(16))) char lds[163840];
  const int tid = threadIdx.x, lane = tid & 63, wave = __builtin_amdgcn_readfirstlane(tid >> 6);
  const int b = blockIdx.x, xcd = b & 7, idx = b >> 3;
  const unsigned sLds = (unsigned)(size_t)lds, sScr = sLds + 163584;
  asm volatile("s_getpc_b64 s[98:99]\n\ts_mov_b32 m0, %1\n\ts_nop 0\n\tglobal_load_lds_dword %0, s[98:99]" :: "v"(tid * 128), "s"(sScr) : "s98", "s99", "memory");
  asm volatile("" :: "s"(q), "s"(kin), "s"(vin), "s"(shift_k), "s"(ssm_k), "s"(ssm_D), "s"(w1), "s"(w2), "s"(w3), "s"(w4), "s"(ws));
  int kind, rep, slice, kh = 0;
  const int P = xcd >> 1, par = (xcd & 1) ^ FLIP, li = idx - (par ? 20 + RE : 12 - RE);
  const int qq = (par ? 0 : 12 + RE) + idx - 8;
  auto code_touch = [&]() {
    unsigned long long cp;
    unsigned n;
    asm volatile("s_getpc_b64 %0\nLpd%=:\n"
                 "s_mov_b32 s98, Lcode_r_b-Lpd%=\ns_mov_b32 %1, Lcode_r_e-Lcode_r_b\n"
                 "s_cmp_eq_u32 %2, 1\ns_cselect_b32 s98, Lcode_g12_b-Lpd%=, s98\ns_cselect_b32 %1, Lcode_g2_b-Lcode_g12_b, %1\n"
                 "s_cmp_eq_u32 %2, 2\ns_cselect_b32 s98, Lcode_g2_b-Lpd%=, s98\ns_cselect_b32 %1, Lcode_g12_e-Lcode_g2_b, %1\n"
                 "s_cmp_eq_u32 %2, 3\ns_cselect_b32 s98, Lcode_g3_b-Lpd%=, s98\ns_cselect_b32 %1, Lcode_g3_e-Lcode_g3_b, %1\n"
                 "s_cmp_eq_u32 %2, 4\ns_cselect_b32 s98, Lcode_g4_b-Lpd%=, s98\ns_cselect_b32 %1, Lcode_g4_e-Lcode_g4_b, %1\n"
                 "s_cmp_eq_u32 %2, 5\ns_cselect_b32 s98, Lcode_g3o_b-Lpd%=, s98\ns_cselect_b32 %1, Lcode_g3o_e-Lcode_g3o_b, %1\n"
                 "s_ashr_i32 s99, s98, 31\ns_mov_b64 vcc, %0\ns_add_u32 s98, s98, vcc_lo\ns_addc_u32 s99, s99, vcc_hi\ns_mov_b64 %0, s[98:99]"
                 : "=&s"(cp), "=&s"(n) : "s"(kind) : "s98", "s99", "scc", "vcc");
    asm volatile("s_mov_b32 m0, %3\n\ts_nop 0\n\tglobal_load_lds_dword %0, %2\n\tglobal_load_lds_dword %1, %2"
                 :: "v"(min(tid * 128u, n - 4)), "v"(min(32768 + tid * 128u, n - 4)), "s"(cp), "s"(sScr) : "memory");
  };
  unsigned ep = 0;
#define EP_LOAD() asm volatile("global_load_dword %0, %1, %2 sc1" : "=v"(ep) : "v"(OFF_EP), "s"(ws) : "memory")
  auto epoch = [&]() { const unsigned e = __builtin_amdgcn_readfirstlane(ep) + 1u; return e ? e : 1u; };
  auto q_unit = [&](QRegs& QR, int f) {
    float ma, va, mb, vb_;
    row_stats(QR.a, lds + CV_STG, 0, lane, wave, ma, va);
    row_stats(QR.b, lds + CV_STG, 1, lane, wave, mb, vb_);
    q_store(ws, 2 * f, lane, wave, QR.a, ma, va);
    q_store(ws, 2 * f + 1, lane, wave, QR.b, mb, vb_);
  };
  auto publish = [&](unsigned off) {
    BARRIER();
    if (tid == 0) __hip_atomic_store((gu32*)(ws + off), ep, __ATOMIC_RELAXED, __HIP_MEMORY_SCOPE_AGENT);
  };
  if (idx < 8 && par == MAP12) {
    kind = 1; rep = P; slice = idx;
    code_touch();
    prewrite(ws, OFF_FH + slice * 128, 1024, rep, tid);
  } else {
    const int cv = (li + (par ? 20 + RE : 0)) * 4 + P, n = idx < 8 ? 64 + P * 8 + idx : qq * 4 + P;
    QRegs QR;
    if (idx >= 8) {
      KvRegs KR;
      SkRegs SK;
      if (li >= 0) {
        const int tile = 64 + cv;
        u32x4v hv[8];
        unsigned hx, dD;
        h_load(hv, hx, dD, ssm_k, ssm_D, 8 * cv, tid);
        EP_LOAD();
        sk_load(SK, shift_k, tile, tid);
        kv_load(KR, kin, vin, tile, tid);
        {
          int l2 = li, p2 = par;
          asm volatile("" : "+s"(l2), "+s"(p2));
          const int nl = 8 - (P & 1), l = ((15 * P + 1) >> 1) + l2, tl = (l * 205) >> 10, kl = l - 5 * tl;
          kind = l2 < 20 ? 4 : 6; rep = l2 < 20 ? l2 >> 2 : 0; slice = l2 < 20 ? l2 & 3 : ((9 * P) >> 1) + l2 - 20; kh = P;
          if (p2 == 1) {
            kind = l2 < nl ? 3 : 6; rep = l2 < nl ? 3 + ((kl & 1) | (kl >> 2)) : 0;
            slice = l2 < nl ? 3 * tl + (kl >> 1) : ((9 * P) >> 1) + RE + l2 - nl;
          }
        }
        touch_lds((const char*)((par ^ MAP12) ? w2 : w1) + (size_t)(par ? (li < 8 ? li * 256 + tid : 0) : (li < 16 ? li * 128 + (tid & 127) : 0)) * 1024 + 384, sScr);
        code_touch();
        h_wait<18>(hv, hx, dD);
        asm volatile("" : "+v"(ep));
        ep = epoch();
        h_spread(hv, hx, lds, tid);
        kv_wait<2>(KR, SK);
        kv_store(KR, SK, ws, lds + CV_STG, tile, tid);
        WAIT_VM(0);
        publish(OFF_FKV + tile * 128);
        const char* wp = (const char*)w4 + (kind != 4 ? 0 : (size_t)(kh * 896 + li * 15) * 2048 + min(tid, 239) * 128);
        conv_part(ws, dD, lds, cv, tid, lane, wave, ep, wp, kind != 4 ? 0 : 298 * 2048);
      } else {
        kv_load(KR, kin, vin, n, tid);
        sk_load(SK, shift_k, n, tid);
        EP_LOAD();
        {
          int q2 = qq;
          asm volatile("" : "+s"(q2));
          const int e = 15 * P + q2, t = (e * 205) >> 11, k = e - 10 * t, k3 = min((k * 11) >> 5, 2);
          kind = q2 < 15 ? 3 : 5; rep = q2 < 15 ? k - 3 * k3 : P; slice = q2 < 15 ? 3 * t + k3 : 18;
        }
        kv_wait<0>(KR, SK);
        asm volatile("" : "+v"(ep));
        ep = epoch();
        kv_store(KR, SK, ws, lds + CV_STG, n, tid);
        pads(ws, n * 4 + wave, lane, ep);
        if (n < 8)
          st16(ws + OFF_FXP + (lane >> 3) * 16384 + wave * 1024 + (2 * (n & 3) + (n >> 2)) * 128 + (lane & 7) * 16, __builtin_bit_cast(u16x8, (u32x4v){ep - 16u, 0u, 0u, 0u}));
        kv_load(KR, kin, vin, 192 + n, tid);
        WAIT_VM(11);
        publish(OFF_FKV + n * 128);
        q_load(QR, q, 32 + n, lane, wave);
        kv_wait<8>(KR, SK);
        kv_store(KR, SK, ws, lds, 192 + n, tid);
        q_wait<0>(QR, ep);
        publish(OFF_FKV + (192 + n) * 128);
      }
    } else {
      QRegs Q0;
      q_load(Q0, q, n - 64, lane, wave);
      EP_LOAD();
      kind = 2; rep = P; slice = idx;
      q_wait<0>(Q0, ep);
      ep = epoch();
      q_load(QR, q, 32 + n, lane, wave);
      q_unit(Q0, n - 64);
      q_wait<0>(QR, ep);
      publish(OFF_FY + (n - 64) * 128);
    }
    if (li < 0) {
      q_unit(QR, 32 + n);
      {
        const char* wp = (const char*)w2 + (size_t)(n < 64 ? 0 : (n - 64) * 512 + tid) * 128;
        if (!(ROT & 2)) {
          touch_lds(wp, sScr);
          touch_lds(wp + (n < 64 ? 0 : 32768), sScr);
        }
      }
      WAIT_VM((ROT & 2) ? 0 : 2);
      publish(OFF_FY + (32 + n) * 128);
      if (n >= 64) {
        prewrite(ws, OFF_FX1 + slice * 128, 1024, rep, tid);
        prewrite(ws, OFF_FP + slice * 128, 2048, rep, tid);
        prewrite(ws, OFF_FP + (8 + slice) * 128, 2048, rep, tid);
        prewrite(ws, OFF_FG + slice * 128, 2560, rep, tid);
        prewrite(ws, OFF_FG + (8 + slice) * 128, 2560, rep, tid);
        if (slice < 3) prewrite(ws, OFF_FG + (16 + slice) * 128, 2560, rep, tid);
        st16(ws + OFF_FXP + slice * 128 + (132 + rep + 4 * (tid >> 3)) * 1024 + (tid & 7) * 16, __builtin_bit_cast(u16x8, (u32x4v){0u, 0u, 0u, 0u}));
      }
      code_touch();
      if (n < 64) {
#pragma unroll
        for (int i = 0; i < 4; ++i) {
          const unsigned x = rep * 1024 + i * 256 + tid, row = kind == 3 ? (x * 21846) >> 16 : x >> 1;
          if (rep < (kind == 3 ? 3 : 2)) touch_lds((const char*)w3 + (size_t)row * 7168 + slice * 384 + (x - row * (kind == 3 ? 3 : 2)) * 128, sScr);
        }
      }
    }
    BARRIER();
  }
  typedef char* const __attribute__((address_space(4)))* KArgs;
#define KARG(x, i) char* x; { KArgs ka_ = (KArgs)__builtin_amdgcn_kernarg_segment_ptr(); asm volatile("" : "+s"(ka_)); x = ka_[i]; }
  const unsigned sDbg0 = OFF_DBG + (b * 4 + wave) * 8192;
#define LANE_ID(x) int x; asm volatile("v_mbcnt_lo_u32_b32 %0, -1, 0\n\tv_mbcnt_hi_u32_b32 %0, -1, %0" : "=v"(x))
#define OOB 0x80000000u
#define COMMON(NF, FIN, FOUT, SN0, WS, LDW, NREP)                                                                                    \
  LANE_ID(ln);                                                                                                                       \
  const int l15 = ln & 15, lg = ln >> 4, lq = l15 >> 2, lp = ln & 3, rr = 8 * lg + lq;                                               \
  const unsigned vA = ln * 16, vFlag = (ln < FLW && wave == 3) ? (FOUT) + ln * 4 : OOB;                                                        \
  const unsigned vPollOff = wave < 2 && ln < (NF) ? (FIN) + ((NF) == 16 && ln >= 8 ? (ln & 2 ? ln : ln - 16) : ln) * 128 : OOB, vSnapOff = wave == 0 ? vPollOff : OOB;                   \
  const unsigned vSnapRd = (SN0) + (ln >> 5) * (WS) + ((ln & 31) < (NF) ? (ln & 31) : 0) * 4;                                        \
  const unsigned vWd0 = (ln >> 3) * (LDW) + ((ln & 7) ^ swzB(ln >> 3)) * 16, vWd1 = ((ln >> 3) + 8) * (LDW) + ((ln & 7) ^ swzB(8 + (ln >> 3))) * 16; \
  const unsigned vWr0 = (kind == 4 ? wave * 4096 : (wave >> 1) * 8192) + rr * 128 + (((kind == 4 ? kh >> 1 : wave & 1) * 4 + lp) ^ swzB(rr)) * 16, vWr1 = vWr0 + 8;                  \
  const unsigned vWr2 = kind == 4 ? vWr0 ^ 64 : 16384 + rr * 128 + ((2 * wave + (lp >> 1)) ^ swzB(rr)) * 16 + (lp & 1) * 8;                                  \
                                            \
  const int res = kind == 3 ? rep + 3 * KE : rep;                     \
  const int nm = kind == 4 ? N1 : kind == 3 ? N3 : 0, nr = kind == 4 ? 5 + NX : 6;                  \
  const unsigned sStep = (rep >= 5 ? nr : (NREP)) << 15, sU0 = (rep < 3 ? rep : rep >= 5 ? nm + rep + 4 : res) << 15;                 \
  const unsigned sX1 = ((NREP) == 4 ? 128 + rep : 128 + (res + 2) % 5) << 15, sDbg = sDbg0 + (rep >= 5 ? 1024 : 0);                   \
  const unsigned sX2 = ((NREP) == 4 ? 260 + rep : nm ? 260 + (nm + rep + 164) % nr : 260 + (res + 4) % 5) << 15;                      \
  const unsigned sEp = __builtin_amdgcn_readfirstlane(ep), sBuf = kind != 3 ? sEp : (rep < 3 ? 3 : rep < 5 ? 5 : 6) << 15
  constexpr unsigned AUX = 131072;
    if (kind == 6)
    {
      KARG(ws, 10)
      LANE_ID(lr);
      wait_flags(ws + OFF_FXP + 7 * 16384 + wave * 1024, lr, 8, ep);
      const int r8 = lr & 7, seg = lr >> 3;
      const unsigned sEpR = __builtin_amdgcn_readfirstlane(ep), sK = slice, sStep = NRW, sK1 = RT1 + slice, sRq = OFF_FXP + LB * 2048 + wave * 128, sDbg = sDbg0 + (kind == 6 ? 0 : 1024);
      const unsigned vRF = (wave * 8 + (seg >> 2)) * 1024 + ((seg & 3) * 16 + r8) * 16;
      const unsigned vRwr = sLds + (seg == 0 ? r8 * 64 + wave * 4 : 512 + lr * 4), vRrd = sLds + r8 * 64;
      const unsigned vRP = sLds + ((lr >> 4) == ((lr & 15) >> 2) ? r8 * 64 + 16 + wave * 8 + (seg & 1) * 4 : 768 + lr * 4);
      const unsigned vRS = (wave == 0 && seg == 0) ? r8 * 16 : 0x80000000u, vFlag = lr == 0 ? 0u : 0x80000000u, vL4 = ((lr & 3) * 4 + wave) * 128, vRL = wave * 4096 + lr * 16;
      asm volatile(R"(
Lcode_r_b:
s_mov_b64 s[36:37],%0
s_mov_b32 s38,122727552
s_mov_b32 s39,0x00020000
s_mov_b32 s54,%1
s_mov_b32 s50,%2
s_mov_b32 s92,%3
s_mov_b32 s48,%4
v_mov_b32 v0,%5
v_mov_b32 v1,%6
v_mov_b32 v2,%7
v_mov_b32 v3,%8
v_mov_b32 v4,%9
v_mov_b32 v5,%10
v_mov_b32 v6,%11
v_mov_b32 v11,%12
v_lshrrev_b32 v10,1,v4
v_mov_b32 v7,s48
v_mbcnt_lo_u32_b32 v9,-1,0
v_mbcnt_hi_u32_b32 v9,-1,v9
v_lshlrev_b32 v9,4,v9
s_mov_b32 s74,0
s_mov_b32 s96,0
s_mov_b32 s60,536870912
s_mov_b32 s46,0xaaaaaaaa
s_mov_b32 s47,0xaaaaaaaa
s_mov_b32 s56,0xcccccccc
s_mov_b32 s57,0xcccccccc
s_mov_b32 s76,0x3a800000
s_mov_b32 s77,0x3727c5ac
s_mov_b32 s78,0xc0135761
s_mov_b32 s79,0x3d372713
v_mov_b32 v192,0x3f803f80
v_mov_b32 v193,0x3f803f80
v_mov_b32 v194,0x3f803f80
v_mov_b32 v195,0x3f803f80
s_lshr_b32 s63,s54,1
s_and_b32 s64,s54,1
s_lshl_b32 s64,s64,7
s_lshl_b32 s87,s54,14
s_lshl_b32 s58,s63,15
s_add_u32 s58,s58,s64
s_add_u32 s58,s58,16908288
s_lshl_b32 s59,s63,9
s_add_u32 s59,s59,s64
s_add_u32 s59,s59,118556672
s_lshl_b32 s49,s54,9
s_add_u32 s49,s49,s92
s_lshl_b32 s91,s63,11
s_add_u32 s91,s91,121405440
s_lshl_b32 s69,s50,10
s_add_u32 s69,s69,s91
v_mov_b32 v20,0
v_mov_b32 v21,0
v_mov_b32 v22,0
v_mov_b32 v23,0
v_mov_b32 v24,0
v_mov_b32 v25,0
v_mov_b32 v26,0
v_mov_b32 v27,0
v_mov_b32 v16,0x3f80
v_mov_b32 v17,0x3f800000
v_cndmask_b32 v16,v16,v17,s[46:47]
s_mov_b32 s98,0x00300003
s_mov_b32 s99,0x30000300
v_cndmask_b32 v20,v20,v16,s[98:99]
s_lshl_b64 s[98:99],s[98:99],2
v_cndmask_b32 v21,v21,v16,s[98:99]
v_mov_b32 v26,v20
v_mov_b32 v27,v21
v_readfirstlane_b32 s52,v11
s_add_u32 s51,s87,8388608
s_mov_b32 s66,4096
s_nop 3
Lr_yp_%=:
s_add_u32 m0,s66,s52
s_cmp_ge_u32 s51,12582912
s_cbranch_scc1 Lr_ys1_%=
buffer_load_dwordx4 v11,s[36:39],s51 offen offset:0 sc1 lds
buffer_load_dwordx4 v11,s[36:39],s51 offen offset:1024 sc1 lds
buffer_load_dwordx4 v11,s[36:39],s51 offen offset:2048 sc1 lds
buffer_load_dwordx4 v11,s[36:39],s51 offen offset:3072 sc1 lds
s_lshl_b32 s63,s50,14
s_add_u32 s51,s51,s63
Lr_ys1_%=:
s_add_u32 s66,s66,16384
s_cmp_lt_u32 s66,151552
s_cbranch_scc1 Lr_yp_%=
s_mov_b32 s66,4096
v_add_u32 v18,4096,v11
Lr_bp9_%=:
buffer_load_dword v8,v6,s[36:39],s91 offen offset:0 sc1
s_waitcnt vmcnt(0)
v_cmp_eq_u32 vcc,s48,v8
s_nop 0
s_cmp_eq_u64 vcc,exec
s_cselect_b32 s63,1,s96
s_cmp_lg_u32 s63,0
s_cbranch_scc1 Lr_bq9_%=
s_add_u32 s74,s74,1
s_cmp_ge_u32 s74,20000
s_cselect_b32 s96,1,0
s_sleep 4
s_branch Lr_bp9_%=
Lr_bq9_%=:
s_add_u32 s63,s87,71303168
buffer_load_dwordx4 a[0:3],v11,s[36:39],s63 offen offset:0 sc1
buffer_load_dwordx4 a[4:7],v11,s[36:39],s63 offen offset:1024 sc1
buffer_load_dwordx4 a[8:11],v11,s[36:39],s63 offen offset:2048 sc1
buffer_load_dwordx4 a[12:15],v11,s[36:39],s63 offen offset:3072 sc1
s_add_u32 s63,s87,89128960
buffer_load_dwordx4 a[16:19],v11,s[36:39],s63 offen offset:0 sc1
buffer_load_dwordx4 a[20:23],v11,s[36:39],s63 offen offset:1024 sc1
buffer_load_dwordx4 a[24:27],v11,s[36:39],s63 offen offset:2048 sc1
buffer_load_dwordx4 a[28:31],v11,s[36:39],s63 offen offset:3072 sc1
s_add_u32 s63,s87,99614720
buffer_load_dwordx4 a[32:35],v11,s[36:39],s63 offen offset:0 sc1
buffer_load_dwordx4 a[36:39],v11,s[36:39],s63 offen offset:1024 sc1
buffer_load_dwordx4 a[40:43],v11,s[36:39],s63 offen offset:2048 sc1
buffer_load_dwordx4 a[44:47],v11,s[36:39],s63 offen offset:3072 sc1
s_add_u32 s63,s87,109051904
buffer_load_dwordx4 a[48:51],v11,s[36:39],s63 offen offset:0 sc1
buffer_load_dwordx4 a[52:55],v11,s[36:39],s63 offen offset:1024 sc1
buffer_load_dwordx4 a[56:59],v11,s[36:39],s63 offen offset:2048 sc1
buffer_load_dwordx4 a[60:63],v11,s[36:39],s63 offen offset:3072 sc1
buffer_load_dword v8,v6,s[36:39],s69 offen offset:0 sc1
s_waitcnt vmcnt(0)
Lr_it0_%=:
s_mov_b32 s88,s58
s_mov_b32 s89,s59
s_mov_b32 s90,s49
s_add_u32 s54,s54,s50
s_lshr_b32 s63,s54,1
s_and_b32 s64,s54,1
s_lshl_b32 s64,s64,7
s_lshl_b32 s87,s54,14
s_lshl_b32 s58,s63,15
s_add_u32 s58,s58,s64
s_add_u32 s58,s58,16908288
s_lshl_b32 s59,s63,9
s_add_u32 s59,s59,s64
s_add_u32 s59,s59,118556672
s_lshl_b32 s49,s54,9
s_add_u32 s49,s49,s92
s_lshl_b32 s91,s63,11
s_add_u32 s91,s91,121405440
s_lshl_b32 s69,s50,10
s_add_u32 s69,s69,s91
v_cmp_eq_u32 vcc,s48,v8
s_nop 0
s_cmp_eq_u64 vcc,exec
s_cselect_b32 s63,s54,256
s_cmp_lt_u32 s63,256
s_cbranch_scc1 Lr_ta0_%=
ds_read_b128 v[32:35],v18 offset:0
ds_read_b128 v[36:39],v18 offset:1024
ds_read_b128 v[40:43],v18 offset:2048
ds_read_b128 v[44:47],v18 offset:3072
v_mfma_f32_16x16x32_bf16 v[128:131],v[20:23],a[0:3],0
v_mfma_f32_16x16x32_bf16 v[132:135],v[24:27],a[0:3],0
v_mfma_f32_16x16x32_bf16 v[128:131],v[20:23],a[16:19],v[128:131]
v_mfma_f32_16x16x32_bf16 v[132:135],v[24:27],a[16:19],v[132:135]
v_mfma_f32_16x16x32_bf16 v[128:131],v[20:23],a[32:35],v[128:131]
v_mfma_f32_16x16x32_bf16 v[132:135],v[24:27],a[32:35],v[132:135]
v_mfma_f32_16x16x32_bf16 v[128:131],v[20:23],a[48:51],v[128:131]
v_mfma_f32_16x16x32_bf16 v[132:135],v[24:27],a[48:51],v[132:135]
v_mfma_f32_16x16x32_bf16 v[136:139],v[20:23],a[4:7],0
v_mfma_f32_16x16x32_bf16 v[140:143],v[24:27],a[4:7],0
v_mfma_f32_16x16x32_bf16 v[136:139],v[20:23],a[20:23],v[136:139]
v_mfma_f32_16x16x32_bf16 v[140:143],v[24:27],a[20:23],v[140:143]
v_mfma_f32_16x16x32_bf16 v[136:139],v[20:23],a[36:39],v[136:139]
s_nop 1
v_cvt_pk_bf16_f32 v208,v128,v129
v_cvt_pk_bf16_f32 v209,v130,v131
v_mfma_f32_16x16x32_bf16 v[140:143],v[24:27],a[36:39],v[140:143]
v_cvt_pk_bf16_f32 v210,v132,v133
v_cvt_pk_bf16_f32 v211,v134,v135
v_mfma_f32_16x16x32_bf16 v[136:139],v[20:23],a[52:55],v[136:139]
v_mfma_f32_16x16x32_bf16 v[140:143],v[24:27],a[52:55],v[140:143]
v_mfma_f32_16x16x32_bf16 v[144:147],v[20:23],a[8:11],0
v_mfma_f32_16x16x32_bf16 v[148:151],v[24:27],a[8:11],0
v_mfma_f32_16x16x32_bf16 v[144:147],v[20:23],a[24:27],v[144:147]
v_mfma_f32_16x16x32_bf16 v[148:151],v[24:27],a[24:27],v[148:151]
v_mfma_f32_16x16x32_bf16 v[144:147],v[20:23],a[40:43],v[144:147]
s_nop 1
v_cvt_pk_bf16_f32 v212,v136,v137
s_waitcnt vmcnt(0)
buffer_store_dword v7,v5,s[36:39],s60 offen offset:0 sc1
s_mov_b32 s60,536870912
v_cvt_pk_bf16_f32 v213,v138,v139
v_mfma_f32_16x16x32_bf16 v[148:151],v[24:27],a[40:43],v[148:151]
v_cvt_pk_bf16_f32 v214,v140,v141
v_cvt_pk_bf16_f32 v215,v142,v143
v_mfma_f32_16x16x32_bf16 v[144:147],v[20:23],a[56:59],v[144:147]
v_mfma_f32_16x16x32_bf16 v[148:151],v[24:27],a[56:59],v[148:151]
v_mfma_f32_16x16x32_bf16 v[28:31],v[192:195],v[208:211],0
v_mfma_f32_16x16x32_bf16 v[204:207],v[208:211],v[208:211],0
v_mfma_f32_16x16x32_bf16 v[152:155],v[20:23],a[12:15],0
v_mfma_f32_16x16x32_bf16 v[156:159],v[24:27],a[12:15],0
v_mfma_f32_16x16x32_bf16 v[152:155],v[20:23],a[28:31],v[152:155])"
R"(
v_mfma_f32_16x16x32_bf16 v[156:159],v[24:27],a[28:31],v[156:159]
v_mfma_f32_16x16x32_bf16 v[152:155],v[20:23],a[44:47],v[152:155]
v_cvt_pk_bf16_f32 v216,v144,v145
v_cvt_pk_bf16_f32 v217,v146,v147
v_mfma_f32_16x16x32_bf16 v[156:159],v[24:27],a[44:47],v[156:159]
v_cvt_pk_bf16_f32 v218,v148,v149
v_cvt_pk_bf16_f32 v219,v150,v151
v_mfma_f32_16x16x32_bf16 v[152:155],v[20:23],a[60:63],v[152:155]
v_mfma_f32_16x16x32_bf16 v[156:159],v[24:27],a[60:63],v[156:159]
v_mfma_f32_16x16x32_bf16 v[28:31],v[192:195],v[212:215],v[28:31]
v_mfma_f32_16x16x32_bf16 v[204:207],v[212:215],v[212:215],v[204:207]
v_mfma_f32_16x16x32_bf16 v[28:31],v[192:195],v[216:219],v[28:31]
v_mfma_f32_16x16x32_bf16 v[204:207],v[216:219],v[216:219],v[204:207]
s_waitcnt lgkmcnt(0)
v_mfma_f32_16x16x32_bf16 v[160:163],v[20:23],v[32:35],0
s_nop 0
v_cvt_pk_bf16_f32 v220,v152,v153
v_cvt_pk_bf16_f32 v221,v154,v155
v_mfma_f32_16x16x32_bf16 v[164:167],v[24:27],v[32:35],0
v_cvt_pk_bf16_f32 v222,v156,v157
v_cvt_pk_bf16_f32 v223,v158,v159
v_mfma_f32_16x16x32_bf16 v[168:171],v[20:23],v[36:39],0
s_nop 0
v_mfma_f32_16x16x32_bf16 v[28:31],v[192:195],v[220:223],v[28:31]
v_mfma_f32_16x16x32_bf16 v[204:207],v[220:223],v[220:223],v[204:207]
v_mfma_f32_16x16x32_bf16 v[172:175],v[24:27],v[36:39],0
v_mfma_f32_16x16x32_bf16 v[176:179],v[20:23],v[40:43],0
v_mfma_f32_16x16x32_bf16 v[180:183],v[24:27],v[40:43],0
s_nop 3
v_add_f32_dpp v224,v28,v28 row_ror:8 row_mask:0xf bank_mask:0xf
v_cndmask_b32 v225,v204,v205,s[46:47]
v_cndmask_b32 v226,v206,v207,s[46:47]
v_cndmask_b32 v225,v225,v226,s[56:57]
v_mfma_f32_16x16x32_bf16 v[184:187],v[20:23],v[44:47],0
ds_write_b32 v2,v224 offset:0
ds_write_b32 v0,v225 offset:0
v_mfma_f32_16x16x32_bf16 v[188:191],v[24:27],v[44:47],0
s_waitcnt lgkmcnt(0)
s_barrier
ds_read_b128 v[196:199],v3 offset:0
ds_read_b128 v[200:203],v3 offset:16
ds_read_b128 v[204:207],v3 offset:32
s_waitcnt lgkmcnt(0)
v_add_f32 v196,v196,v197
v_add_f32 v198,v198,v199
v_add_f32 v200,v200,v204
v_add_f32 v201,v201,v205
v_add_f32 v202,v202,v206
v_add_f32 v203,v203,v207
v_add_f32 v196,v196,v198
v_add_f32 v200,v200,v201
v_add_f32 v202,v202,v203
v_mul_f32 v196,s76,v196
v_add_f32 v197,v200,v202
v_mul_f32 v197,s76,v197
v_fma_f32 v197,-v196,v196,v197
v_max_f32 v197,0,v197
v_add_f32 v197,s77,v197
v_rsq_f32 v228,v197
s_nop 0
v_mul_f32_e64 v230,-v196,v228
v_mov_b32 v229,v228
v_mov_b32 v231,v230
v_pk_fma_f32 v[196:197],v[128:129],v[228:229],v[230:231]
v_pk_fma_f32 v[198:199],v[130:131],v[228:229],v[230:231]
v_pk_fma_f32 v[200:201],v[132:133],v[228:229],v[230:231]
v_pk_fma_f32 v[202:203],v[134:135],v[228:229],v[230:231]
v_pk_fma_f32 v[128:129],v[196:197],v[160:161],v[128:129]
v_pk_fma_f32 v[130:131],v[198:199],v[162:163],v[130:131]
v_pk_fma_f32 v[132:133],v[200:201],v[164:165],v[132:133]
v_pk_fma_f32 v[134:135],v[202:203],v[166:167],v[134:135]
v_pk_fma_f32 v[196:197],v[136:137],v[228:229],v[230:231]
v_pk_fma_f32 v[198:199],v[138:139],v[228:229],v[230:231]
v_pk_fma_f32 v[200:201],v[140:141],v[228:229],v[230:231]
v_pk_fma_f32 v[202:203],v[142:143],v[228:229],v[230:231]
v_pk_fma_f32 v[136:137],v[196:197],v[168:169],v[136:137]
v_pk_fma_f32 v[138:139],v[198:199],v[170:171],v[138:139]
v_pk_fma_f32 v[140:141],v[200:201],v[172:173],v[140:141]
v_pk_fma_f32 v[142:143],v[202:203],v[174:175],v[142:143]
v_pk_fma_f32 v[196:197],v[144:145],v[228:229],v[230:231]
v_pk_fma_f32 v[198:199],v[146:147],v[228:229],v[230:231]
v_pk_fma_f32 v[200:201],v[148:149],v[228:229],v[230:231]
v_pk_fma_f32 v[202:203],v[150:151],v[228:229],v[230:231]
v_pk_fma_f32 v[144:145],v[196:197],v[176:177],v[144:145]
v_pk_fma_f32 v[146:147],v[198:199],v[178:179],v[146:147]
v_pk_fma_f32 v[148:149],v[200:201],v[180:181],v[148:149]
v_pk_fma_f32 v[150:151],v[202:203],v[182:183],v[150:151]
v_pk_fma_f32 v[196:197],v[152:153],v[228:229],v[230:231]
v_pk_fma_f32 v[198:199],v[154:155],v[228:229],v[230:231]
v_pk_fma_f32 v[200:201],v[156:157],v[228:229],v[230:231]
v_pk_fma_f32 v[202:203],v[158:159],v[228:229],v[230:231]
v_pk_fma_f32 v[152:153],v[196:197],v[184:185],v[152:153]
v_pk_fma_f32 v[154:155],v[198:199],v[186:187],v[154:155]
v_pk_fma_f32 v[156:157],v[200:201],v[188:189],v[156:157]
v_pk_fma_f32 v[158:159],v[202:203],v[190:191],v[158:159]
v_cvt_pk_bf16_f32 v208,v128,v129
v_cvt_pk_bf16_f32 v209,v130,v131
v_cvt_pk_bf16_f32 v210,v132,v133
v_cvt_pk_bf16_f32 v211,v134,v135
v_cvt_pk_bf16_f32 v212,v136,v137
v_cvt_pk_bf16_f32 v213,v138,v139
v_cvt_pk_bf16_f32 v214,v140,v141
v_cvt_pk_bf16_f32 v215,v142,v143
v_mfma_f32_16x16x32_bf16 v[28:31],v[192:195],v[208:211],0
v_cvt_pk_bf16_f32 v216,v144,v145
v_cvt_pk_bf16_f32 v217,v146,v147
v_mfma_f32_16x16x32_bf16 v[204:207],v[208:211],v[208:211],0
v_cvt_pk_bf16_f32 v218,v148,v149
v_cvt_pk_bf16_f32 v219,v150,v151
v_mfma_f32_16x16x32_bf16 v[28:31],v[192:195],v[212:215],v[28:31]
v_cvt_pk_bf16_f32 v220,v152,v153
v_cvt_pk_bf16_f32 v221,v154,v155
v_mfma_f32_16x16x32_bf16 v[204:207],v[212:215],v[212:215],v[204:207]
v_cvt_pk_bf16_f32 v222,v156,v157
v_cvt_pk_bf16_f32 v223,v158,v159
v_mfma_f32_16x16x32_bf16 v[28:31],v[192:195],v[216:219],v[28:31]
v_mfma_f32_16x16x32_bf16 v[204:207],v[216:219],v[216:219],v[204:207]
v_mfma_f32_16x16x32_bf16 v[28:31],v[192:195],v[220:223],v[28:31]
v_mfma_f32_16x16x32_bf16 v[204:207],v[220:223],v[220:223],v[204:207]
buffer_load_dword v8,v6,s[36:39],s91 offen offset:0 sc1
buffer_store_dwordx4 v[208:211],v1,s[36:39],s88 offen offset:0 sc1
buffer_store_dwordx4 v[212:215],v1,s[36:39],s88 offen offset:2048 sc1
s_add_u32 s88,s88,4096
s_nop 2
v_add_f32_dpp v224,v28,v28 row_ror:8 row_mask:0xf bank_mask:0xf
v_cndmask_b32 v225,v204,v205,s[46:47]
v_cndmask_b32 v226,v206,v207,s[46:47]
v_cndmask_b32 v225,v225,v226,s[56:57]
s_and_b32 s63,s89,128
ds_write_b32 v2,v224 offset:1024
ds_write_b32 v0,v225 offset:1024
buffer_store_dwordx4 v[216:219],v1,s[36:39],s88 offen offset:0 sc1
buffer_store_dwordx4 v[220:223],v1,s[36:39],s88 offen offset:2048 sc1
s_waitcnt lgkmcnt(0)
s_barrier
ds_read_b128 v[196:199],v3 offset:1024
ds_read_b128 v[200:203],v3 offset:1040
ds_read_b128 v[204:207],v3 offset:1056
s_waitcnt lgkmcnt(0)
v_add_f32 v196,v196,v197
v_add_f32 v198,v198,v199
v_add_f32 v200,v200,v204
v_add_f32 v201,v201,v205
v_add_f32 v202,v202,v206
v_add_f32 v203,v203,v207
v_add_f32 v196,v196,v198
v_add_f32 v200,v200,v201
v_add_f32 v202,v202,v203
v_mul_f32 v196,s76,v196
v_add_f32 v197,v200,v202
v_mul_f32 v197,s76,v197
v_fma_f32 v197,-v196,v196,v197
v_max_f32 v197,0,v197
v_add_f32 v198,s77,v197
v_fma_f32 v199,s77,v198,v197
v_rsq_f32 v205,v198
v_rsq_f32 v198,v199
v_mov_b32 v200,v196
v_mul_f32_e64 v204,-v196,v205
v_mul_f32 v203,v199,v198
v_mul_f32 v201,s78,v198
v_mul_f32 v199,v198,v198
v_mul_f32 v202,s79,v199
v_mul_f32 v202,v202,v201
s_lshr_b32 s63,s63,1
s_sub_u32 s63,s89,s63
buffer_store_dwordx4 v[200:203],v4,s[36:39],s89 offen offset:0 sc1
buffer_store_dwordx2 v[204:205],v10,s[36:39],s63 offen offset:256 sc1
s_add_u32 m0,s66,s52
s_cmp_ge_u32 s51,12582912
s_cbranch_scc1 Lr_ys2_%=
buffer_load_dwordx4 v11,s[36:39],s51 offen offset:0 sc1 lds
buffer_load_dwordx4 v11,s[36:39],s51 offen offset:1024 sc1 lds
buffer_load_dwordx4 v11,s[36:39],s51 offen offset:2048 sc1 lds
buffer_load_dwordx4 v11,s[36:39],s51 offen offset:3072 sc1 lds
s_lshl_b32 s63,s50,14
s_add_u32 s51,s51,s63
Lr_ys2_%=:
s_add_u32 s66,s66,16384
s_cmp_eq_u32 s66,151552
s_cselect_b32 s66,4096,s66
v_add_u32 v18,s66,v11
s_mov_b32 s60,s90
s_waitcnt vmcnt(6)
v_cmp_eq_u32 vcc,s48,v8
s_nop 0
s_cmp_eq_u64 vcc,exec
s_cselect_b32 s63,s54,256
s_cmp_lt_u32 s63,256
s_cbranch_scc1 Lr_tc0_%=
buffer_load_dword v8,v6,s[36:39],s91 offen offset:0 sc1
s_waitcnt vmcnt(0)
buffer_store_dword v7,v5,s[36:39],s60 offen offset:0 sc1
s_mov_b32 s60,536870912
v_cmp_eq_u32 vcc,s48,v8
s_nop 0
s_cmp_eq_u64 vcc,exec
s_cselect_b32 s63,s54,256
s_cmp_lt_u32 s63,256
s_cbranch_scc1 Lr_bq0_%=)"
R"(
s_cmp_ge_u32 s54,256
s_cbranch_scc1 Lr_done_%=
Lr_bp0_%=:
buffer_load_dword v8,v6,s[36:39],s91 offen offset:0 sc1
s_waitcnt vmcnt(0)
v_cmp_eq_u32 vcc,s48,v8
s_nop 0
s_cmp_eq_u64 vcc,exec
s_cselect_b32 s63,1,s96
s_cmp_lg_u32 s63,0
s_cbranch_scc1 Lr_bq0_%=
s_add_u32 s74,s74,1
s_cmp_ge_u32 s74,20000
s_cselect_b32 s96,1,0
s_sleep 4
s_branch Lr_bp0_%=
Lr_bq0_%=:
s_add_u32 s63,s87,71303168
buffer_load_dwordx4 a[64:67],v11,s[36:39],s63 offen offset:0 sc1
buffer_load_dwordx4 a[68:71],v11,s[36:39],s63 offen offset:1024 sc1
buffer_load_dwordx4 a[72:75],v11,s[36:39],s63 offen offset:2048 sc1
buffer_load_dwordx4 a[76:79],v11,s[36:39],s63 offen offset:3072 sc1
s_add_u32 s63,s87,89128960
buffer_load_dwordx4 a[80:83],v11,s[36:39],s63 offen offset:0 sc1
buffer_load_dwordx4 a[84:87],v11,s[36:39],s63 offen offset:1024 sc1
buffer_load_dwordx4 a[88:91],v11,s[36:39],s63 offen offset:2048 sc1
buffer_load_dwordx4 a[92:95],v11,s[36:39],s63 offen offset:3072 sc1
s_add_u32 s63,s87,99614720
buffer_load_dwordx4 a[96:99],v11,s[36:39],s63 offen offset:0 sc1
buffer_load_dwordx4 a[100:103],v11,s[36:39],s63 offen offset:1024 sc1
buffer_load_dwordx4 a[104:107],v11,s[36:39],s63 offen offset:2048 sc1
buffer_load_dwordx4 a[108:111],v11,s[36:39],s63 offen offset:3072 sc1
s_add_u32 s63,s87,109051904
buffer_load_dwordx4 a[112:115],v11,s[36:39],s63 offen offset:0 sc1
buffer_load_dwordx4 a[116:119],v11,s[36:39],s63 offen offset:1024 sc1
buffer_load_dwordx4 a[120:123],v11,s[36:39],s63 offen offset:2048 sc1
buffer_load_dwordx4 a[124:127],v11,s[36:39],s63 offen offset:3072 sc1
buffer_load_dword v8,v6,s[36:39],s69 offen offset:0 sc1
s_waitcnt vmcnt(0)
s_branch Lr_it1_%=
Lr_ta0_%=:
ds_read_b128 v[32:35],v18 offset:0
ds_read_b128 v[36:39],v18 offset:1024
ds_read_b128 v[40:43],v18 offset:2048
ds_read_b128 v[44:47],v18 offset:3072
v_mfma_f32_16x16x32_bf16 v[128:131],v[20:23],a[0:3],0
s_add_u32 s63,s87,71303168
v_mfma_f32_16x16x32_bf16 v[132:135],v[24:27],a[0:3],0
buffer_load_dwordx4 a[64:67],v11,s[36:39],s63 offen offset:0 sc1
v_mfma_f32_16x16x32_bf16 v[128:131],v[20:23],a[16:19],v[128:131]
v_mfma_f32_16x16x32_bf16 v[132:135],v[24:27],a[16:19],v[132:135]
buffer_load_dwordx4 a[68:71],v11,s[36:39],s63 offen offset:1024 sc1
v_mfma_f32_16x16x32_bf16 v[128:131],v[20:23],a[32:35],v[128:131]
v_mfma_f32_16x16x32_bf16 v[132:135],v[24:27],a[32:35],v[132:135]
v_mfma_f32_16x16x32_bf16 v[128:131],v[20:23],a[48:51],v[128:131]
v_mfma_f32_16x16x32_bf16 v[132:135],v[24:27],a[48:51],v[132:135]
buffer_load_dwordx4 a[72:75],v11,s[36:39],s63 offen offset:2048 sc1
v_mfma_f32_16x16x32_bf16 v[136:139],v[20:23],a[4:7],0
v_mfma_f32_16x16x32_bf16 v[140:143],v[24:27],a[4:7],0
v_mfma_f32_16x16x32_bf16 v[136:139],v[20:23],a[20:23],v[136:139]
v_mfma_f32_16x16x32_bf16 v[140:143],v[24:27],a[20:23],v[140:143]
buffer_load_dwordx4 a[76:79],v11,s[36:39],s63 offen offset:3072 sc1
v_mfma_f32_16x16x32_bf16 v[136:139],v[20:23],a[36:39],v[136:139]
v_cvt_pk_bf16_f32 v208,v128,v129
v_cvt_pk_bf16_f32 v209,v130,v131
v_mfma_f32_16x16x32_bf16 v[140:143],v[24:27],a[36:39],v[140:143]
v_cvt_pk_bf16_f32 v210,v132,v133
v_cvt_pk_bf16_f32 v211,v134,v135
v_mfma_f32_16x16x32_bf16 v[136:139],v[20:23],a[52:55],v[136:139]
s_add_u32 s63,s87,89128960
v_mfma_f32_16x16x32_bf16 v[140:143],v[24:27],a[52:55],v[140:143]
buffer_load_dwordx4 a[80:83],v11,s[36:39],s63 offen offset:0 sc1
v_mfma_f32_16x16x32_bf16 v[144:147],v[20:23],a[8:11],0
v_mfma_f32_16x16x32_bf16 v[148:151],v[24:27],a[8:11],0
v_mfma_f32_16x16x32_bf16 v[144:147],v[20:23],a[24:27],v[144:147]
v_mfma_f32_16x16x32_bf16 v[148:151],v[24:27],a[24:27],v[148:151]
buffer_load_dwordx4 a[84:87],v11,s[36:39],s63 offen offset:1024 sc1
v_mfma_f32_16x16x32_bf16 v[144:147],v[20:23],a[40:43],v[144:147]
v_cvt_pk_bf16_f32 v212,v136,v137
v_cvt_pk_bf16_f32 v213,v138,v139
v_mfma_f32_16x16x32_bf16 v[148:151],v[24:27],a[40:43],v[148:151]
v_cvt_pk_bf16_f32 v214,v140,v141
v_cvt_pk_bf16_f32 v215,v142,v143
v_mfma_f32_16x16x32_bf16 v[144:147],v[20:23],a[56:59],v[144:147]
v_mfma_f32_16x16x32_bf16 v[148:151],v[24:27],a[56:59],v[148:151]
s_waitcnt vmcnt(6)
buffer_store_dword v7,v5,s[36:39],s60 offen offset:0 sc1
s_mov_b32 s60,536870912
buffer_load_dwordx4 a[88:91],v11,s[36:39],s63 offen offset:2048 sc1
v_mfma_f32_16x16x32_bf16 v[28:31],v[192:195],v[208:211],0
v_mfma_f32_16x16x32_bf16 v[204:207],v[208:211],v[208:211],0
v_mfma_f32_16x16x32_bf16 v[152:155],v[20:23],a[12:15],0
v_mfma_f32_16x16x32_bf16 v[156:159],v[24:27],a[12:15],0
buffer_load_dwordx4 a[92:95],v11,s[36:39],s63 offen offset:3072 sc1
v_mfma_f32_16x16x32_bf16 v[152:155],v[20:23],a[28:31],v[152:155]
s_add_u32 s63,s87,99614720
v_mfma_f32_16x16x32_bf16 v[156:159],v[24:27],a[28:31],v[156:159]
v_mfma_f32_16x16x32_bf16 v[152:155],v[20:23],a[44:47],v[152:155]
v_cvt_pk_bf16_f32 v216,v144,v145
v_cvt_pk_bf16_f32 v217,v146,v147
v_mfma_f32_16x16x32_bf16 v[156:159],v[24:27],a[44:47],v[156:159]
v_cvt_pk_bf16_f32 v218,v148,v149
v_cvt_pk_bf16_f32 v219,v150,v151
v_mfma_f32_16x16x32_bf16 v[152:155],v[20:23],a[60:63],v[152:155]
buffer_load_dwordx4 a[96:99],v11,s[36:39],s63 offen offset:0 sc1
v_mfma_f32_16x16x32_bf16 v[156:159],v[24:27],a[60:63],v[156:159]
v_mfma_f32_16x16x32_bf16 v[28:31],v[192:195],v[212:215],v[28:31]
v_mfma_f32_16x16x32_bf16 v[204:207],v[212:215],v[212:215],v[204:207]
buffer_load_dwordx4 a[100:103],v11,s[36:39],s63 offen offset:1024 sc1
v_mfma_f32_16x16x32_bf16 v[28:31],v[192:195],v[216:219],v[28:31]
v_mfma_f32_16x16x32_bf16 v[204:207],v[216:219],v[216:219],v[204:207]
s_waitcnt lgkmcnt(0)
v_mfma_f32_16x16x32_bf16 v[160:163],v[20:23],v[32:35],0
v_cvt_pk_bf16_f32 v220,v152,v153
v_cvt_pk_bf16_f32 v221,v154,v155
v_mfma_f32_16x16x32_bf16 v[164:167],v[24:27],v[32:35],0
v_cvt_pk_bf16_f32 v222,v156,v157
v_cvt_pk_bf16_f32 v223,v158,v159
v_mfma_f32_16x16x32_bf16 v[168:171],v[20:23],v[36:39],0
buffer_load_dwordx4 a[104:107],v11,s[36:39],s63 offen offset:2048 sc1
v_mfma_f32_16x16x32_bf16 v[28:31],v[192:195],v[220:223],v[28:31]
buffer_load_dwordx4 a[108:111],v11,s[36:39],s63 offen offset:3072 sc1
v_mfma_f32_16x16x32_bf16 v[204:207],v[220:223],v[220:223],v[204:207]
s_add_u32 s63,s87,109051904
v_mfma_f32_16x16x32_bf16 v[172:175],v[24:27],v[36:39],0
v_mfma_f32_16x16x32_bf16 v[176:179],v[20:23],v[40:43],0
buffer_load_dwordx4 a[112:115],v11,s[36:39],s63 offen offset:0 sc1
v_mfma_f32_16x16x32_bf16 v[180:183],v[24:27],v[40:43],0
s_nop 0
v_add_f32_dpp v224,v28,v28 row_ror:8 row_mask:0xf bank_mask:0xf
s_nop 0
v_cndmask_b32 v225,v204,v205,s[46:47]
v_cndmask_b32 v226,v206,v207,s[46:47]
v_cndmask_b32 v225,v225,v226,s[56:57]
v_mfma_f32_16x16x32_bf16 v[184:187],v[20:23],v[44:47],0
ds_write_b32 v2,v224 offset:0
ds_write_b32 v0,v225 offset:0
v_mfma_f32_16x16x32_bf16 v[188:191],v[24:27],v[44:47],0
s_waitcnt lgkmcnt(0)
buffer_load_dwordx4 a[116:119],v11,s[36:39],s63 offen offset:1024 sc1
s_barrier
buffer_load_dwordx4 a[120:123],v11,s[36:39],s63 offen offset:2048 sc1
ds_read_b128 v[196:199],v3 offset:0
buffer_load_dwordx4 a[124:127],v11,s[36:39],s63 offen offset:3072 sc1
ds_read_b128 v[200:203],v3 offset:16
ds_read_b128 v[204:207],v3 offset:32
s_waitcnt lgkmcnt(0)
buffer_load_dword v8,v6,s[36:39],s69 offen offset:0 sc1
v_add_f32 v196,v196,v197
v_add_f32 v198,v198,v199
v_add_f32 v200,v200,v204
v_add_f32 v201,v201,v205
v_add_f32 v202,v202,v206
v_add_f32 v203,v203,v207
v_add_f32 v196,v196,v198
v_add_f32 v200,v200,v201
v_add_f32 v202,v202,v203
v_mul_f32 v196,s76,v196
v_add_f32 v197,v200,v202
v_mul_f32 v197,s76,v197
v_fma_f32 v197,-v196,v196,v197
v_max_f32 v197,0,v197
v_add_f32 v197,s77,v197
v_rsq_f32 v228,v197
s_nop 0
v_mul_f32_e64 v230,-v196,v228
v_mov_b32 v229,v228
v_mov_b32 v231,v230
v_pk_fma_f32 v[196:197],v[128:129],v[228:229],v[230:231]
v_pk_fma_f32 v[198:199],v[130:131],v[228:229],v[230:231]
v_pk_fma_f32 v[200:201],v[132:133],v[228:229],v[230:231]
v_pk_fma_f32 v[202:203],v[134:135],v[228:229],v[230:231]
v_pk_fma_f32 v[128:129],v[196:197],v[160:161],v[128:129]
v_pk_fma_f32 v[130:131],v[198:199],v[162:163],v[130:131]
v_pk_fma_f32 v[132:133],v[200:201],v[164:165],v[132:133]
v_pk_fma_f32 v[134:135],v[202:203],v[166:167],v[134:135]
v_pk_fma_f32 v[196:197],v[136:137],v[228:229],v[230:231]
v_pk_fma_f32 v[198:199],v[138:139],v[228:229],v[230:231]
v_pk_fma_f32 v[200:201],v[140:141],v[228:229],v[230:231]
v_pk_fma_f32 v[202:203],v[142:143],v[228:229],v[230:231]
v_pk_fma_f32 v[136:137],v[196:197],v[168:169],v[136:137]
v_pk_fma_f32 v[138:139],v[198:199],v[170:171],v[138:139]
v_pk_fma_f32 v[140:141],v[200:201],v[172:173],v[140:141]
v_pk_fma_f32 v[142:143],v[202:203],v[174:175],v[142:143]
v_pk_fma_f32 v[196:197],v[144:145],v[228:229],v[230:231]
v_pk_fma_f32 v[198:199],v[146:147],v[228:229],v[230:231]
v_pk_fma_f32 v[200:201],v[148:149],v[228:229],v[230:231]
v_pk_fma_f32 v[202:203],v[150:151],v[228:229],v[230:231]
v_pk_fma_f32 v[144:145],v[196:197],v[176:177],v[144:145]
v_pk_fma_f32 v[146:147],v[198:199],v[178:179],v[146:147]
v_pk_fma_f32 v[148:149],v[200:201],v[180:181],v[148:149]
v_pk_fma_f32 v[150:151],v[202:203],v[182:183],v[150:151]
v_pk_fma_f32 v[196:197],v[152:153],v[228:229],v[230:231]
v_pk_fma_f32 v[198:199],v[154:155],v[228:229],v[230:231]
v_pk_fma_f32 v[200:201],v[156:157],v[228:229],v[230:231]
v_pk_fma_f32 v[202:203],v[158:159],v[228:229],v[230:231]
v_pk_fma_f32 v[152:153],v[196:197],v[184:185],v[152:153]
v_pk_fma_f32 v[154:155],v[198:199],v[186:187],v[154:155]
v_pk_fma_f32 v[156:157],v[200:201],v[188:189],v[156:157]
v_pk_fma_f32 v[158:159],v[202:203],v[190:191],v[158:159])"
R"(
v_cvt_pk_bf16_f32 v208,v128,v129
v_cvt_pk_bf16_f32 v209,v130,v131
v_cvt_pk_bf16_f32 v210,v132,v133
v_cvt_pk_bf16_f32 v211,v134,v135
v_cvt_pk_bf16_f32 v212,v136,v137
v_cvt_pk_bf16_f32 v213,v138,v139
v_cvt_pk_bf16_f32 v214,v140,v141
v_cvt_pk_bf16_f32 v215,v142,v143
v_mfma_f32_16x16x32_bf16 v[28:31],v[192:195],v[208:211],0
v_cvt_pk_bf16_f32 v216,v144,v145
v_cvt_pk_bf16_f32 v217,v146,v147
v_mfma_f32_16x16x32_bf16 v[204:207],v[208:211],v[208:211],0
v_cvt_pk_bf16_f32 v218,v148,v149
v_cvt_pk_bf16_f32 v219,v150,v151
v_mfma_f32_16x16x32_bf16 v[28:31],v[192:195],v[212:215],v[28:31]
v_cvt_pk_bf16_f32 v220,v152,v153
v_cvt_pk_bf16_f32 v221,v154,v155
v_mfma_f32_16x16x32_bf16 v[204:207],v[212:215],v[212:215],v[204:207]
v_cvt_pk_bf16_f32 v222,v156,v157
v_cvt_pk_bf16_f32 v223,v158,v159
v_mfma_f32_16x16x32_bf16 v[28:31],v[192:195],v[216:219],v[28:31]
v_mfma_f32_16x16x32_bf16 v[204:207],v[216:219],v[216:219],v[204:207]
v_mfma_f32_16x16x32_bf16 v[28:31],v[192:195],v[220:223],v[28:31]
v_mfma_f32_16x16x32_bf16 v[204:207],v[220:223],v[220:223],v[204:207]
buffer_store_dwordx4 v[208:211],v1,s[36:39],s88 offen offset:0 sc1
buffer_store_dwordx4 v[212:215],v1,s[36:39],s88 offen offset:2048 sc1
s_add_u32 s88,s88,4096
s_nop 3
v_add_f32_dpp v224,v28,v28 row_ror:8 row_mask:0xf bank_mask:0xf
v_cndmask_b32 v225,v204,v205,s[46:47]
v_cndmask_b32 v226,v206,v207,s[46:47]
v_cndmask_b32 v225,v225,v226,s[56:57]
s_and_b32 s63,s89,128
ds_write_b32 v2,v224 offset:1024
ds_write_b32 v0,v225 offset:1024
buffer_store_dwordx4 v[216:219],v1,s[36:39],s88 offen offset:0 sc1
buffer_store_dwordx4 v[220:223],v1,s[36:39],s88 offen offset:2048 sc1
s_waitcnt lgkmcnt(0)
s_barrier
ds_read_b128 v[196:199],v3 offset:1024
ds_read_b128 v[200:203],v3 offset:1040
ds_read_b128 v[204:207],v3 offset:1056
s_waitcnt lgkmcnt(0)
v_add_f32 v196,v196,v197
v_add_f32 v198,v198,v199
v_add_f32 v200,v200,v204
v_add_f32 v201,v201,v205
v_add_f32 v202,v202,v206
v_add_f32 v203,v203,v207
v_add_f32 v196,v196,v198
v_add_f32 v200,v200,v201
v_add_f32 v202,v202,v203
v_mul_f32 v196,s76,v196
v_add_f32 v197,v200,v202
v_mul_f32 v197,s76,v197
v_fma_f32 v197,-v196,v196,v197
v_max_f32 v197,0,v197
v_add_f32 v198,s77,v197
v_fma_f32 v199,s77,v198,v197
v_rsq_f32 v205,v198
v_rsq_f32 v198,v199
v_mov_b32 v200,v196
v_mul_f32_e64 v204,-v196,v205
v_mul_f32 v203,v199,v198
v_mul_f32 v201,s78,v198
v_mul_f32 v199,v198,v198
v_mul_f32 v202,s79,v199
v_mul_f32 v202,v202,v201
s_lshr_b32 s63,s63,1
s_sub_u32 s63,s89,s63
buffer_store_dwordx4 v[200:203],v4,s[36:39],s89 offen offset:0 sc1
buffer_store_dwordx2 v[204:205],v10,s[36:39],s63 offen offset:256 sc1
s_add_u32 m0,s66,s52
s_cmp_ge_u32 s51,12582912
s_cbranch_scc1 Lr_ys3_%=
buffer_load_dwordx4 v11,s[36:39],s51 offen offset:0 sc1 lds
buffer_load_dwordx4 v11,s[36:39],s51 offen offset:1024 sc1 lds
buffer_load_dwordx4 v11,s[36:39],s51 offen offset:2048 sc1 lds
buffer_load_dwordx4 v11,s[36:39],s51 offen offset:3072 sc1 lds
s_lshl_b32 s63,s50,14
s_add_u32 s51,s51,s63
Lr_ys3_%=:
s_add_u32 s66,s66,16384
s_cmp_eq_u32 s66,151552
s_cselect_b32 s66,4096,s66
v_add_u32 v18,s66,v11
s_mov_b32 s60,s90
s_waitcnt vmcnt(6)
s_branch Lr_it1_%=
Lr_tc0_%=:
s_add_u32 s63,s87,71303168
buffer_load_dwordx4 a[64:67],v11,s[36:39],s63 offen offset:0 sc1
buffer_load_dwordx4 a[68:71],v11,s[36:39],s63 offen offset:1024 sc1
buffer_load_dwordx4 a[72:75],v11,s[36:39],s63 offen offset:2048 sc1
buffer_load_dwordx4 a[76:79],v11,s[36:39],s63 offen offset:3072 sc1
s_add_u32 s63,s87,89128960
buffer_load_dwordx4 a[80:83],v11,s[36:39],s63 offen offset:0 sc1
buffer_load_dwordx4 a[84:87],v11,s[36:39],s63 offen offset:1024 sc1
buffer_load_dwordx4 a[88:91],v11,s[36:39],s63 offen offset:2048 sc1
buffer_load_dwordx4 a[92:95],v11,s[36:39],s63 offen offset:3072 sc1
s_add_u32 s63,s87,99614720
buffer_load_dwordx4 a[96:99],v11,s[36:39],s63 offen offset:0 sc1
buffer_load_dwordx4 a[100:103],v11,s[36:39],s63 offen offset:1024 sc1
buffer_load_dwordx4 a[104:107],v11,s[36:39],s63 offen offset:2048 sc1
buffer_load_dwordx4 a[108:111],v11,s[36:39],s63 offen offset:3072 sc1
s_add_u32 s63,s87,109051904
buffer_load_dwordx4 a[112:115],v11,s[36:39],s63 offen offset:0 sc1
buffer_load_dwordx4 a[116:119],v11,s[36:39],s63 offen offset:1024 sc1
buffer_load_dwordx4 a[120:123],v11,s[36:39],s63 offen offset:2048 sc1
buffer_load_dwordx4 a[124:127],v11,s[36:39],s63 offen offset:3072 sc1
buffer_load_dword v8,v6,s[36:39],s69 offen offset:0 sc1
s_waitcnt vmcnt(17)
buffer_store_dword v7,v5,s[36:39],s60 offen offset:0 sc1
s_mov_b32 s60,536870912
s_waitcnt vmcnt(1)
s_branch Lr_it1_%=
Lr_it1_%=:
s_mov_b32 s88,s58
s_mov_b32 s89,s59
s_mov_b32 s90,s49
s_add_u32 s54,s54,s50
s_lshr_b32 s63,s54,1
s_and_b32 s64,s54,1
s_lshl_b32 s64,s64,7
s_lshl_b32 s87,s54,14
s_lshl_b32 s58,s63,15
s_add_u32 s58,s58,s64
s_add_u32 s58,s58,16908288
s_lshl_b32 s59,s63,9
s_add_u32 s59,s59,s64
s_add_u32 s59,s59,118556672
s_lshl_b32 s49,s54,9
s_add_u32 s49,s49,s92
s_lshl_b32 s91,s63,11
s_add_u32 s91,s91,121405440
s_lshl_b32 s69,s50,10
s_add_u32 s69,s69,s91
v_cmp_eq_u32 vcc,s48,v8
s_nop 0
s_cmp_eq_u64 vcc,exec
s_cselect_b32 s63,s54,256
s_cmp_lt_u32 s63,256
s_cbranch_scc1 Lr_ta1_%=
ds_read_b128 v[32:35],v18 offset:0
ds_read_b128 v[36:39],v18 offset:1024
ds_read_b128 v[40:43],v18 offset:2048
ds_read_b128 v[44:47],v18 offset:3072
v_mfma_f32_16x16x32_bf16 v[128:131],v[20:23],a[64:67],0
v_mfma_f32_16x16x32_bf16 v[132:135],v[24:27],a[64:67],0
v_mfma_f32_16x16x32_bf16 v[128:131],v[20:23],a[80:83],v[128:131]
v_mfma_f32_16x16x32_bf16 v[132:135],v[24:27],a[80:83],v[132:135]
v_mfma_f32_16x16x32_bf16 v[128:131],v[20:23],a[96:99],v[128:131]
v_mfma_f32_16x16x32_bf16 v[132:135],v[24:27],a[96:99],v[132:135]
v_mfma_f32_16x16x32_bf16 v[128:131],v[20:23],a[112:115],v[128:131]
v_mfma_f32_16x16x32_bf16 v[132:135],v[24:27],a[112:115],v[132:135]
v_mfma_f32_16x16x32_bf16 v[136:139],v[20:23],a[68:71],0
v_mfma_f32_16x16x32_bf16 v[140:143],v[24:27],a[68:71],0
v_mfma_f32_16x16x32_bf16 v[136:139],v[20:23],a[84:87],v[136:139]
v_mfma_f32_16x16x32_bf16 v[140:143],v[24:27],a[84:87],v[140:143]
v_mfma_f32_16x16x32_bf16 v[136:139],v[20:23],a[100:103],v[136:139]
s_nop 1
v_cvt_pk_bf16_f32 v208,v128,v129
v_cvt_pk_bf16_f32 v209,v130,v131
v_mfma_f32_16x16x32_bf16 v[140:143],v[24:27],a[100:103],v[140:143]
v_cvt_pk_bf16_f32 v210,v132,v133
v_cvt_pk_bf16_f32 v211,v134,v135
v_mfma_f32_16x16x32_bf16 v[136:139],v[20:23],a[116:119],v[136:139]
v_mfma_f32_16x16x32_bf16 v[140:143],v[24:27],a[116:119],v[140:143]
v_mfma_f32_16x16x32_bf16 v[144:147],v[20:23],a[72:75],0
v_mfma_f32_16x16x32_bf16 v[148:151],v[24:27],a[72:75],0
v_mfma_f32_16x16x32_bf16 v[144:147],v[20:23],a[88:91],v[144:147]
v_mfma_f32_16x16x32_bf16 v[148:151],v[24:27],a[88:91],v[148:151]
v_mfma_f32_16x16x32_bf16 v[144:147],v[20:23],a[104:107],v[144:147]
s_nop 1
v_cvt_pk_bf16_f32 v212,v136,v137
s_waitcnt vmcnt(0)
buffer_store_dword v7,v5,s[36:39],s60 offen offset:0 sc1
s_mov_b32 s60,536870912
v_cvt_pk_bf16_f32 v213,v138,v139
v_mfma_f32_16x16x32_bf16 v[148:151],v[24:27],a[104:107],v[148:151]
v_cvt_pk_bf16_f32 v214,v140,v141
v_cvt_pk_bf16_f32 v215,v142,v143
v_mfma_f32_16x16x32_bf16 v[144:147],v[20:23],a[120:123],v[144:147]
v_mfma_f32_16x16x32_bf16 v[148:151],v[24:27],a[120:123],v[148:151]
v_mfma_f32_16x16x32_bf16 v[28:31],v[192:195],v[208:211],0
v_mfma_f32_16x16x32_bf16 v[204:207],v[208:211],v[208:211],0
v_mfma_f32_16x16x32_bf16 v[152:155],v[20:23],a[76:79],0
v_mfma_f32_16x16x32_bf16 v[156:159],v[24:27],a[76:79],0
v_mfma_f32_16x16x32_bf16 v[152:155],v[20:23],a[92:95],v[152:155]
v_mfma_f32_16x16x32_bf16 v[156:159],v[24:27],a[92:95],v[156:159]
v_mfma_f32_16x16x32_bf16 v[152:155],v[20:23],a[108:111],v[152:155]
v_cvt_pk_bf16_f32 v216,v144,v145
v_cvt_pk_bf16_f32 v217,v146,v147
v_mfma_f32_16x16x32_bf16 v[156:159],v[24:27],a[108:111],v[156:159]
v_cvt_pk_bf16_f32 v218,v148,v149
v_cvt_pk_bf16_f32 v219,v150,v151
v_mfma_f32_16x16x32_bf16 v[152:155],v[20:23],a[124:127],v[152:155]
v_mfma_f32_16x16x32_bf16 v[156:159],v[24:27],a[124:127],v[156:159]
v_mfma_f32_16x16x32_bf16 v[28:31],v[192:195],v[212:215],v[28:31]
v_mfma_f32_16x16x32_bf16 v[204:207],v[212:215],v[212:215],v[204:207])"
R"(
v_mfma_f32_16x16x32_bf16 v[28:31],v[192:195],v[216:219],v[28:31]
v_mfma_f32_16x16x32_bf16 v[204:207],v[216:219],v[216:219],v[204:207]
s_waitcnt lgkmcnt(0)
v_mfma_f32_16x16x32_bf16 v[160:163],v[20:23],v[32:35],0
s_nop 0
v_cvt_pk_bf16_f32 v220,v152,v153
v_cvt_pk_bf16_f32 v221,v154,v155
v_mfma_f32_16x16x32_bf16 v[164:167],v[24:27],v[32:35],0
v_cvt_pk_bf16_f32 v222,v156,v157
v_cvt_pk_bf16_f32 v223,v158,v159
v_mfma_f32_16x16x32_bf16 v[168:171],v[20:23],v[36:39],0
s_nop 0
v_mfma_f32_16x16x32_bf16 v[28:31],v[192:195],v[220:223],v[28:31]
v_mfma_f32_16x16x32_bf16 v[204:207],v[220:223],v[220:223],v[204:207]
v_mfma_f32_16x16x32_bf16 v[172:175],v[24:27],v[36:39],0
v_mfma_f32_16x16x32_bf16 v[176:179],v[20:23],v[40:43],0
v_mfma_f32_16x16x32_bf16 v[180:183],v[24:27],v[40:43],0
s_nop 3
v_add_f32_dpp v224,v28,v28 row_ror:8 row_mask:0xf bank_mask:0xf
v_cndmask_b32 v225,v204,v205,s[46:47]
v_cndmask_b32 v226,v206,v207,s[46:47]
v_cndmask_b32 v225,v225,v226,s[56:57]
v_mfma_f32_16x16x32_bf16 v[184:187],v[20:23],v[44:47],0
ds_write_b32 v2,v224 offset:0
ds_write_b32 v0,v225 offset:0
v_mfma_f32_16x16x32_bf16 v[188:191],v[24:27],v[44:47],0
s_waitcnt lgkmcnt(0)
s_barrier
ds_read_b128 v[196:199],v3 offset:0
ds_read_b128 v[200:203],v3 offset:16
ds_read_b128 v[204:207],v3 offset:32
s_waitcnt lgkmcnt(0)
v_add_f32 v196,v196,v197
v_add_f32 v198,v198,v199
v_add_f32 v200,v200,v204
v_add_f32 v201,v201,v205
v_add_f32 v202,v202,v206
v_add_f32 v203,v203,v207
v_add_f32 v196,v196,v198
v_add_f32 v200,v200,v201
v_add_f32 v202,v202,v203
v_mul_f32 v196,s76,v196
v_add_f32 v197,v200,v202
v_mul_f32 v197,s76,v197
v_fma_f32 v197,-v196,v196,v197
v_max_f32 v197,0,v197
v_add_f32 v197,s77,v197
v_rsq_f32 v228,v197
s_nop 0
v_mul_f32_e64 v230,-v196,v228
v_mov_b32 v229,v228
v_mov_b32 v231,v230
v_pk_fma_f32 v[196:197],v[128:129],v[228:229],v[230:231]
v_pk_fma_f32 v[198:199],v[130:131],v[228:229],v[230:231]
v_pk_fma_f32 v[200:201],v[132:133],v[228:229],v[230:231]
v_pk_fma_f32 v[202:203],v[134:135],v[228:229],v[230:231]
v_pk_fma_f32 v[128:129],v[196:197],v[160:161],v[128:129]
v_pk_fma_f32 v[130:131],v[198:199],v[162:163],v[130:131]
v_pk_fma_f32 v[132:133],v[200:201],v[164:165],v[132:133]
v_pk_fma_f32 v[134:135],v[202:203],v[166:167],v[134:135]
v_pk_fma_f32 v[196:197],v[136:137],v[228:229],v[230:231]
v_pk_fma_f32 v[198:199],v[138:139],v[228:229],v[230:231]
v_pk_fma_f32 v[200:201],v[140:141],v[228:229],v[230:231]
v_pk_fma_f32 v[202:203],v[142:143],v[228:229],v[230:231]
v_pk_fma_f32 v[136:137],v[196:197],v[168:169],v[136:137]
v_pk_fma_f32 v[138:139],v[198:199],v[170:171],v[138:139]
v_pk_fma_f32 v[140:141],v[200:201],v[172:173],v[140:141]
v_pk_fma_f32 v[142:143],v[202:203],v[174:175],v[142:143]
v_pk_fma_f32 v[196:197],v[144:145],v[228:229],v[230:231]
v_pk_fma_f32 v[198:199],v[146:147],v[228:229],v[230:231]
v_pk_fma_f32 v[200:201],v[148:149],v[228:229],v[230:231]
v_pk_fma_f32 v[202:203],v[150:151],v[228:229],v[230:231]
v_pk_fma_f32 v[144:145],v[196:197],v[176:177],v[144:145]
v_pk_fma_f32 v[146:147],v[198:199],v[178:179],v[146:147]
v_pk_fma_f32 v[148:149],v[200:201],v[180:181],v[148:149]
v_pk_fma_f32 v[150:151],v[202:203],v[182:183],v[150:151]
v_pk_fma_f32 v[196:197],v[152:153],v[228:229],v[230:231]
v_pk_fma_f32 v[198:199],v[154:155],v[228:229],v[230:231]
v_pk_fma_f32 v[200:201],v[156:157],v[228:229],v[230:231]
v_pk_fma_f32 v[202:203],v[158:159],v[228:229],v[230:231]
v_pk_fma_f32 v[152:153],v[196:197],v[184:185],v[152:153]
v_pk_fma_f32 v[154:155],v[198:199],v[186:187],v[154:155]
v_pk_fma_f32 v[156:157],v[200:201],v[188:189],v[156:157]
v_pk_fma_f32 v[158:159],v[202:203],v[190:191],v[158:159]
v_cvt_pk_bf16_f32 v208,v128,v129
v_cvt_pk_bf16_f32 v209,v130,v131
v_cvt_pk_bf16_f32 v210,v132,v133
v_cvt_pk_bf16_f32 v211,v134,v135
v_cvt_pk_bf16_f32 v212,v136,v137
v_cvt_pk_bf16_f32 v213,v138,v139
v_cvt_pk_bf16_f32 v214,v140,v141
v_cvt_pk_bf16_f32 v215,v142,v143
v_mfma_f32_16x16x32_bf16 v[28:31],v[192:195],v[208:211],0
v_cvt_pk_bf16_f32 v216,v144,v145
v_cvt_pk_bf16_f32 v217,v146,v147
v_mfma_f32_16x16x32_bf16 v[204:207],v[208:211],v[208:211],0
v_cvt_pk_bf16_f32 v218,v148,v149
v_cvt_pk_bf16_f32 v219,v150,v151
v_mfma_f32_16x16x32_bf16 v[28:31],v[192:195],v[212:215],v[28:31]
v_cvt_pk_bf16_f32 v220,v152,v153
v_cvt_pk_bf16_f32 v221,v154,v155
v_mfma_f32_16x16x32_bf16 v[204:207],v[212:215],v[212:215],v[204:207]
v_cvt_pk_bf16_f32 v222,v156,v157
v_cvt_pk_bf16_f32 v223,v158,v159
v_mfma_f32_16x16x32_bf16 v[28:31],v[192:195],v[216:219],v[28:31]
v_mfma_f32_16x16x32_bf16 v[204:207],v[216:219],v[216:219],v[204:207]
v_mfma_f32_16x16x32_bf16 v[28:31],v[192:195],v[220:223],v[28:31]
v_mfma_f32_16x16x32_bf16 v[204:207],v[220:223],v[220:223],v[204:207]
buffer_load_dword v8,v6,s[36:39],s91 offen offset:0 sc1
buffer_store_dwordx4 v[208:211],v1,s[36:39],s88 offen offset:0 sc1
buffer_store_dwordx4 v[212:215],v1,s[36:39],s88 offen offset:2048 sc1
s_add_u32 s88,s88,4096
s_nop 2
v_add_f32_dpp v224,v28,v28 row_ror:8 row_mask:0xf bank_mask:0xf
v_cndmask_b32 v225,v204,v205,s[46:47]
v_cndmask_b32 v226,v206,v207,s[46:47]
v_cndmask_b32 v225,v225,v226,s[56:57]
s_and_b32 s63,s89,128
ds_write_b32 v2,v224 offset:1024
ds_write_b32 v0,v225 offset:1024
buffer_store_dwordx4 v[216:219],v1,s[36:39],s88 offen offset:0 sc1
buffer_store_dwordx4 v[220:223],v1,s[36:39],s88 offen offset:2048 sc1
s_waitcnt lgkmcnt(0)
s_barrier
ds_read_b128 v[196:199],v3 offset:1024
ds_read_b128 v[200:203],v3 offset:1040
ds_read_b128 v[204:207],v3 offset:1056
s_waitcnt lgkmcnt(0)
v_add_f32 v196,v196,v197
v_add_f32 v198,v198,v199
v_add_f32 v200,v200,v204
v_add_f32 v201,v201,v205
v_add_f32 v202,v202,v206
v_add_f32 v203,v203,v207
v_add_f32 v196,v196,v198
v_add_f32 v200,v200,v201
v_add_f32 v202,v202,v203
v_mul_f32 v196,s76,v196
v_add_f32 v197,v200,v202
v_mul_f32 v197,s76,v197
v_fma_f32 v197,-v196,v196,v197
v_max_f32 v197,0,v197
v_add_f32 v198,s77,v197
v_fma_f32 v199,s77,v198,v197
v_rsq_f32 v205,v198
v_rsq_f32 v198,v199
v_mov_b32 v200,v196
v_mul_f32_e64 v204,-v196,v205
v_mul_f32 v203,v199,v198
v_mul_f32 v201,s78,v198
v_mul_f32 v199,v198,v198
v_mul_f32 v202,s79,v199
v_mul_f32 v202,v202,v201
s_lshr_b32 s63,s63,1
s_sub_u32 s63,s89,s63
buffer_store_dwordx4 v[200:203],v4,s[36:39],s89 offen offset:0 sc1
buffer_store_dwordx2 v[204:205],v10,s[36:39],s63 offen offset:256 sc1
s_add_u32 m0,s66,s52
s_cmp_ge_u32 s51,12582912
s_cbranch_scc1 Lr_ys4_%=
buffer_load_dwordx4 v11,s[36:39],s51 offen offset:0 sc1 lds
buffer_load_dwordx4 v11,s[36:39],s51 offen offset:1024 sc1 lds
buffer_load_dwordx4 v11,s[36:39],s51 offen offset:2048 sc1 lds
buffer_load_dwordx4 v11,s[36:39],s51 offen offset:3072 sc1 lds
s_lshl_b32 s63,s50,14
s_add_u32 s51,s51,s63
Lr_ys4_%=:
s_add_u32 s66,s66,16384
s_cmp_eq_u32 s66,151552
s_cselect_b32 s66,4096,s66
v_add_u32 v18,s66,v11
s_mov_b32 s60,s90
s_waitcnt vmcnt(6)
v_cmp_eq_u32 vcc,s48,v8
s_nop 0
s_cmp_eq_u64 vcc,exec
s_cselect_b32 s63,s54,256
s_cmp_lt_u32 s63,256
s_cbranch_scc1 Lr_tc1_%=
buffer_load_dword v8,v6,s[36:39],s91 offen offset:0 sc1
s_waitcnt vmcnt(0)
buffer_store_dword v7,v5,s[36:39],s60 offen offset:0 sc1
s_mov_b32 s60,536870912
v_cmp_eq_u32 vcc,s48,v8
s_nop 0
s_cmp_eq_u64 vcc,exec
s_cselect_b32 s63,s54,256
s_cmp_lt_u32 s63,256
s_cbranch_scc1 Lr_bq1_%=
s_cmp_ge_u32 s54,256
s_cbranch_scc1 Lr_done_%=
Lr_bp1_%=:
buffer_load_dword v8,v6,s[36:39],s91 offen offset:0 sc1
s_waitcnt vmcnt(0)
v_cmp_eq_u32 vcc,s48,v8
s_nop 0
s_cmp_eq_u64 vcc,exec
s_cselect_b32 s63,1,s96
s_cmp_lg_u32 s63,0
s_cbranch_scc1 Lr_bq1_%=)"
R"(
s_add_u32 s74,s74,1
s_cmp_ge_u32 s74,20000
s_cselect_b32 s96,1,0
s_sleep 4
s_branch Lr_bp1_%=
Lr_bq1_%=:
s_add_u32 s63,s87,71303168
buffer_load_dwordx4 a[0:3],v11,s[36:39],s63 offen offset:0 sc1
buffer_load_dwordx4 a[4:7],v11,s[36:39],s63 offen offset:1024 sc1
buffer_load_dwordx4 a[8:11],v11,s[36:39],s63 offen offset:2048 sc1
buffer_load_dwordx4 a[12:15],v11,s[36:39],s63 offen offset:3072 sc1
s_add_u32 s63,s87,89128960
buffer_load_dwordx4 a[16:19],v11,s[36:39],s63 offen offset:0 sc1
buffer_load_dwordx4 a[20:23],v11,s[36:39],s63 offen offset:1024 sc1
buffer_load_dwordx4 a[24:27],v11,s[36:39],s63 offen offset:2048 sc1
buffer_load_dwordx4 a[28:31],v11,s[36:39],s63 offen offset:3072 sc1
s_add_u32 s63,s87,99614720
buffer_load_dwordx4 a[32:35],v11,s[36:39],s63 offen offset:0 sc1
buffer_load_dwordx4 a[36:39],v11,s[36:39],s63 offen offset:1024 sc1
buffer_load_dwordx4 a[40:43],v11,s[36:39],s63 offen offset:2048 sc1
buffer_load_dwordx4 a[44:47],v11,s[36:39],s63 offen offset:3072 sc1
s_add_u32 s63,s87,109051904
buffer_load_dwordx4 a[48:51],v11,s[36:39],s63 offen offset:0 sc1
buffer_load_dwordx4 a[52:55],v11,s[36:39],s63 offen offset:1024 sc1
buffer_load_dwordx4 a[56:59],v11,s[36:39],s63 offen offset:2048 sc1
buffer_load_dwordx4 a[60:63],v11,s[36:39],s63 offen offset:3072 sc1
buffer_load_dword v8,v6,s[36:39],s69 offen offset:0 sc1
s_waitcnt vmcnt(0)
s_branch Lr_it0_%=
Lr_ta1_%=:
ds_read_b128 v[32:35],v18 offset:0
ds_read_b128 v[36:39],v18 offset:1024
ds_read_b128 v[40:43],v18 offset:2048
ds_read_b128 v[44:47],v18 offset:3072
v_mfma_f32_16x16x32_bf16 v[128:131],v[20:23],a[64:67],0
s_add_u32 s63,s87,71303168
v_mfma_f32_16x16x32_bf16 v[132:135],v[24:27],a[64:67],0
buffer_load_dwordx4 a[0:3],v11,s[36:39],s63 offen offset:0 sc1
v_mfma_f32_16x16x32_bf16 v[128:131],v[20:23],a[80:83],v[128:131]
v_mfma_f32_16x16x32_bf16 v[132:135],v[24:27],a[80:83],v[132:135]
buffer_load_dwordx4 a[4:7],v11,s[36:39],s63 offen offset:1024 sc1
v_mfma_f32_16x16x32_bf16 v[128:131],v[20:23],a[96:99],v[128:131]
v_mfma_f32_16x16x32_bf16 v[132:135],v[24:27],a[96:99],v[132:135]
v_mfma_f32_16x16x32_bf16 v[128:131],v[20:23],a[112:115],v[128:131]
v_mfma_f32_16x16x32_bf16 v[132:135],v[24:27],a[112:115],v[132:135]
buffer_load_dwordx4 a[8:11],v11,s[36:39],s63 offen offset:2048 sc1
v_mfma_f32_16x16x32_bf16 v[136:139],v[20:23],a[68:71],0
v_mfma_f32_16x16x32_bf16 v[140:143],v[24:27],a[68:71],0
v_mfma_f32_16x16x32_bf16 v[136:139],v[20:23],a[84:87],v[136:139]
v_mfma_f32_16x16x32_bf16 v[140:143],v[24:27],a[84:87],v[140:143]
buffer_load_dwordx4 a[12:15],v11,s[36:39],s63 offen offset:3072 sc1
v_mfma_f32_16x16x32_bf16 v[136:139],v[20:23],a[100:103],v[136:139]
v_cvt_pk_bf16_f32 v208,v128,v129
v_cvt_pk_bf16_f32 v209,v130,v131
v_mfma_f32_16x16x32_bf16 v[140:143],v[24:27],a[100:103],v[140:143]
v_cvt_pk_bf16_f32 v210,v132,v133
v_cvt_pk_bf16_f32 v211,v134,v135
v_mfma_f32_16x16x32_bf16 v[136:139],v[20:23],a[116:119],v[136:139]
s_add_u32 s63,s87,89128960
v_mfma_f32_16x16x32_bf16 v[140:143],v[24:27],a[116:119],v[140:143]
buffer_load_dwordx4 a[16:19],v11,s[36:39],s63 offen offset:0 sc1
v_mfma_f32_16x16x32_bf16 v[144:147],v[20:23],a[72:75],0
v_mfma_f32_16x16x32_bf16 v[148:151],v[24:27],a[72:75],0
v_mfma_f32_16x16x32_bf16 v[144:147],v[20:23],a[88:91],v[144:147]
v_mfma_f32_16x16x32_bf16 v[148:151],v[24:27],a[88:91],v[148:151]
buffer_load_dwordx4 a[20:23],v11,s[36:39],s63 offen offset:1024 sc1
v_mfma_f32_16x16x32_bf16 v[144:147],v[20:23],a[104:107],v[144:147]
v_cvt_pk_bf16_f32 v212,v136,v137
v_cvt_pk_bf16_f32 v213,v138,v139
v_mfma_f32_16x16x32_bf16 v[148:151],v[24:27],a[104:107],v[148:151]
v_cvt_pk_bf16_f32 v214,v140,v141
v_cvt_pk_bf16_f32 v215,v142,v143
v_mfma_f32_16x16x32_bf16 v[144:147],v[20:23],a[120:123],v[144:147]
v_mfma_f32_16x16x32_bf16 v[148:151],v[24:27],a[120:123],v[148:151]
s_waitcnt vmcnt(6)
buffer_store_dword v7,v5,s[36:39],s60 offen offset:0 sc1
s_mov_b32 s60,536870912
buffer_load_dwordx4 a[24:27],v11,s[36:39],s63 offen offset:2048 sc1
v_mfma_f32_16x16x32_bf16 v[28:31],v[192:195],v[208:211],0
v_mfma_f32_16x16x32_bf16 v[204:207],v[208:211],v[208:211],0
v_mfma_f32_16x16x32_bf16 v[152:155],v[20:23],a[76:79],0
v_mfma_f32_16x16x32_bf16 v[156:159],v[24:27],a[76:79],0
buffer_load_dwordx4 a[28:31],v11,s[36:39],s63 offen offset:3072 sc1
v_mfma_f32_16x16x32_bf16 v[152:155],v[20:23],a[92:95],v[152:155]
s_add_u32 s63,s87,99614720
v_mfma_f32_16x16x32_bf16 v[156:159],v[24:27],a[92:95],v[156:159]
v_mfma_f32_16x16x32_bf16 v[152:155],v[20:23],a[108:111],v[152:155]
v_cvt_pk_bf16_f32 v216,v144,v145
v_cvt_pk_bf16_f32 v217,v146,v147
v_mfma_f32_16x16x32_bf16 v[156:159],v[24:27],a[108:111],v[156:159]
v_cvt_pk_bf16_f32 v218,v148,v149
v_cvt_pk_bf16_f32 v219,v150,v151
v_mfma_f32_16x16x32_bf16 v[152:155],v[20:23],a[124:127],v[152:155]
buffer_load_dwordx4 a[32:35],v11,s[36:39],s63 offen offset:0 sc1
v_mfma_f32_16x16x32_bf16 v[156:159],v[24:27],a[124:127],v[156:159]
v_mfma_f32_16x16x32_bf16 v[28:31],v[192:195],v[212:215],v[28:31]
v_mfma_f32_16x16x32_bf16 v[204:207],v[212:215],v[212:215],v[204:207]
buffer_load_dwordx4 a[36:39],v11,s[36:39],s63 offen offset:1024 sc1
v_mfma_f32_16x16x32_bf16 v[28:31],v[192:195],v[216:219],v[28:31]
v_mfma_f32_16x16x32_bf16 v[204:207],v[216:219],v[216:219],v[204:207]
s_waitcnt lgkmcnt(0)
v_mfma_f32_16x16x32_bf16 v[160:163],v[20:23],v[32:35],0
v_cvt_pk_bf16_f32 v220,v152,v153
v_cvt_pk_bf16_f32 v221,v154,v155
v_mfma_f32_16x16x32_bf16 v[164:167],v[24:27],v[32:35],0
v_cvt_pk_bf16_f32 v222,v156,v157
v_cvt_pk_bf16_f32 v223,v158,v159
v_mfma_f32_16x16x32_bf16 v[168:171],v[20:23],v[36:39],0
buffer_load_dwordx4 a[40:43],v11,s[36:39],s63 offen offset:2048 sc1
v_mfma_f32_16x16x32_bf16 v[28:31],v[192:195],v[220:223],v[28:31]
buffer_load_dwordx4 a[44:47],v11,s[36:39],s63 offen offset:3072 sc1
v_mfma_f32_16x16x32_bf16 v[204:207],v[220:223],v[220:223],v[204:207]
s_add_u32 s63,s87,109051904
v_mfma_f32_16x16x32_bf16 v[172:175],v[24:27],v[36:39],0
v_mfma_f32_16x16x32_bf16 v[176:179],v[20:23],v[40:43],0
buffer_load_dwordx4 a[48:51],v11,s[36:39],s63 offen offset:0 sc1
v_mfma_f32_16x16x32_bf16 v[180:183],v[24:27],v[40:43],0
s_nop 0
v_add_f32_dpp v224,v28,v28 row_ror:8 row_mask:0xf bank_mask:0xf
s_nop 0
v_cndmask_b32 v225,v204,v205,s[46:47]
v_cndmask_b32 v226,v206,v207,s[46:47]
v_cndmask_b32 v225,v225,v226,s[56:57]
v_mfma_f32_16x16x32_bf16 v[184:187],v[20:23],v[44:47],0
ds_write_b32 v2,v224 offset:0
ds_write_b32 v0,v225 offset:0
v_mfma_f32_16x16x32_bf16 v[188:191],v[24:27],v[44:47],0
s_waitcnt lgkmcnt(0)
buffer_load_dwordx4 a[52:55],v11,s[36:39],s63 offen offset:1024 sc1
s_barrier
buffer_load_dwordx4 a[56:59],v11,s[36:39],s63 offen offset:2048 sc1
ds_read_b128 v[196:199],v3 offset:0
buffer_load_dwordx4 a[60:63],v11,s[36:39],s63 offen offset:3072 sc1
ds_read_b128 v[200:203],v3 offset:16
ds_read_b128 v[204:207],v3 offset:32
s_waitcnt lgkmcnt(0)
buffer_load_dword v8,v6,s[36:39],s69 offen offset:0 sc1
v_add_f32 v196,v196,v197
v_add_f32 v198,v198,v199
v_add_f32 v200,v200,v204
v_add_f32 v201,v201,v205
v_add_f32 v202,v202,v206
v_add_f32 v203,v203,v207
v_add_f32 v196,v196,v198
v_add_f32 v200,v200,v201
v_add_f32 v202,v202,v203
v_mul_f32 v196,s76,v196
v_add_f32 v197,v200,v202
v_mul_f32 v197,s76,v197
v_fma_f32 v197,-v196,v196,v197
v_max_f32 v197,0,v197
v_add_f32 v197,s77,v197
v_rsq_f32 v228,v197
s_nop 0
v_mul_f32_e64 v230,-v196,v228
v_mov_b32 v229,v228
v_mov_b32 v231,v230
v_pk_fma_f32 v[196:197],v[128:129],v[228:229],v[230:231]
v_pk_fma_f32 v[198:199],v[130:131],v[228:229],v[230:231]
v_pk_fma_f32 v[200:201],v[132:133],v[228:229],v[230:231]
v_pk_fma_f32 v[202:203],v[134:135],v[228:229],v[230:231]
v_pk_fma_f32 v[128:129],v[196:197],v[160:161],v[128:129]
v_pk_fma_f32 v[130:131],v[198:199],v[162:163],v[130:131]
v_pk_fma_f32 v[132:133],v[200:201],v[164:165],v[132:133]
v_pk_fma_f32 v[134:135],v[202:203],v[166:167],v[134:135]
v_pk_fma_f32 v[196:197],v[136:137],v[228:229],v[230:231]
v_pk_fma_f32 v[198:199],v[138:139],v[228:229],v[230:231]
v_pk_fma_f32 v[200:201],v[140:141],v[228:229],v[230:231]
v_pk_fma_f32 v[202:203],v[142:143],v[228:229],v[230:231]
v_pk_fma_f32 v[136:137],v[196:197],v[168:169],v[136:137]
v_pk_fma_f32 v[138:139],v[198:199],v[170:171],v[138:139]
v_pk_fma_f32 v[140:141],v[200:201],v[172:173],v[140:141]
v_pk_fma_f32 v[142:143],v[202:203],v[174:175],v[142:143]
v_pk_fma_f32 v[196:197],v[144:145],v[228:229],v[230:231]
v_pk_fma_f32 v[198:199],v[146:147],v[228:229],v[230:231]
v_pk_fma_f32 v[200:201],v[148:149],v[228:229],v[230:231]
v_pk_fma_f32 v[202:203],v[150:151],v[228:229],v[230:231]
v_pk_fma_f32 v[144:145],v[196:197],v[176:177],v[144:145]
v_pk_fma_f32 v[146:147],v[198:199],v[178:179],v[146:147]
v_pk_fma_f32 v[148:149],v[200:201],v[180:181],v[148:149]
v_pk_fma_f32 v[150:151],v[202:203],v[182:183],v[150:151]
v_pk_fma_f32 v[196:197],v[152:153],v[228:229],v[230:231]
v_pk_fma_f32 v[198:199],v[154:155],v[228:229],v[230:231]
v_pk_fma_f32 v[200:201],v[156:157],v[228:229],v[230:231]
v_pk_fma_f32 v[202:203],v[158:159],v[228:229],v[230:231]
v_pk_fma_f32 v[152:153],v[196:197],v[184:185],v[152:153]
v_pk_fma_f32 v[154:155],v[198:199],v[186:187],v[154:155]
v_pk_fma_f32 v[156:157],v[200:201],v[188:189],v[156:157]
v_pk_fma_f32 v[158:159],v[202:203],v[190:191],v[158:159]
v_cvt_pk_bf16_f32 v208,v128,v129
v_cvt_pk_bf16_f32 v209,v130,v131
v_cvt_pk_bf16_f32 v210,v132,v133
v_cvt_pk_bf16_f32 v211,v134,v135
v_cvt_pk_bf16_f32 v212,v136,v137
v_cvt_pk_bf16_f32 v213,v138,v139
v_cvt_pk_bf16_f32 v214,v140,v141
v_cvt_pk_bf16_f32 v215,v142,v143
v_mfma_f32_16x16x32_bf16 v[28:31],v[192:195],v[208:211],0
v_cvt_pk_bf16_f32 v216,v144,v145
v_cvt_pk_bf16_f32 v217,v146,v147)"
R"(
v_mfma_f32_16x16x32_bf16 v[204:207],v[208:211],v[208:211],0
v_cvt_pk_bf16_f32 v218,v148,v149
v_cvt_pk_bf16_f32 v219,v150,v151
v_mfma_f32_16x16x32_bf16 v[28:31],v[192:195],v[212:215],v[28:31]
v_cvt_pk_bf16_f32 v220,v152,v153
v_cvt_pk_bf16_f32 v221,v154,v155
v_mfma_f32_16x16x32_bf16 v[204:207],v[212:215],v[212:215],v[204:207]
v_cvt_pk_bf16_f32 v222,v156,v157
v_cvt_pk_bf16_f32 v223,v158,v159
v_mfma_f32_16x16x32_bf16 v[28:31],v[192:195],v[216:219],v[28:31]
v_mfma_f32_16x16x32_bf16 v[204:207],v[216:219],v[216:219],v[204:207]
v_mfma_f32_16x16x32_bf16 v[28:31],v[192:195],v[220:223],v[28:31]
v_mfma_f32_16x16x32_bf16 v[204:207],v[220:223],v[220:223],v[204:207]
buffer_store_dwordx4 v[208:211],v1,s[36:39],s88 offen offset:0 sc1
buffer_store_dwordx4 v[212:215],v1,s[36:39],s88 offen offset:2048 sc1
s_add_u32 s88,s88,4096
s_nop 3
v_add_f32_dpp v224,v28,v28 row_ror:8 row_mask:0xf bank_mask:0xf
v_cndmask_b32 v225,v204,v205,s[46:47]
v_cndmask_b32 v226,v206,v207,s[46:47]
v_cndmask_b32 v225,v225,v226,s[56:57]
s_and_b32 s63,s89,128
ds_write_b32 v2,v224 offset:1024
ds_write_b32 v0,v225 offset:1024
buffer_store_dwordx4 v[216:219],v1,s[36:39],s88 offen offset:0 sc1
buffer_store_dwordx4 v[220:223],v1,s[36:39],s88 offen offset:2048 sc1
s_waitcnt lgkmcnt(0)
s_barrier
ds_read_b128 v[196:199],v3 offset:1024
ds_read_b128 v[200:203],v3 offset:1040
ds_read_b128 v[204:207],v3 offset:1056
s_waitcnt lgkmcnt(0)
v_add_f32 v196,v196,v197
v_add_f32 v198,v198,v199
v_add_f32 v200,v200,v204
v_add_f32 v201,v201,v205
v_add_f32 v202,v202,v206
v_add_f32 v203,v203,v207
v_add_f32 v196,v196,v198
v_add_f32 v200,v200,v201
v_add_f32 v202,v202,v203
v_mul_f32 v196,s76,v196
v_add_f32 v197,v200,v202
v_mul_f32 v197,s76,v197
v_fma_f32 v197,-v196,v196,v197
v_max_f32 v197,0,v197
v_add_f32 v198,s77,v197
v_fma_f32 v199,s77,v198,v197
v_rsq_f32 v205,v198
v_rsq_f32 v198,v199
v_mov_b32 v200,v196
v_mul_f32_e64 v204,-v196,v205
v_mul_f32 v203,v199,v198
v_mul_f32 v201,s78,v198
v_mul_f32 v199,v198,v198
v_mul_f32 v202,s79,v199
v_mul_f32 v202,v202,v201
s_lshr_b32 s63,s63,1
s_sub_u32 s63,s89,s63
buffer_store_dwordx4 v[200:203],v4,s[36:39],s89 offen offset:0 sc1
buffer_store_dwordx2 v[204:205],v10,s[36:39],s63 offen offset:256 sc1
s_add_u32 m0,s66,s52
s_cmp_ge_u32 s51,12582912
s_cbranch_scc1 Lr_ys5_%=
buffer_load_dwordx4 v11,s[36:39],s51 offen offset:0 sc1 lds
buffer_load_dwordx4 v11,s[36:39],s51 offen offset:1024 sc1 lds
buffer_load_dwordx4 v11,s[36:39],s51 offen offset:2048 sc1 lds
buffer_load_dwordx4 v11,s[36:39],s51 offen offset:3072 sc1 lds
s_lshl_b32 s63,s50,14
s_add_u32 s51,s51,s63
Lr_ys5_%=:
s_add_u32 s66,s66,16384
s_cmp_eq_u32 s66,151552
s_cselect_b32 s66,4096,s66
v_add_u32 v18,s66,v11
s_mov_b32 s60,s90
s_waitcnt vmcnt(6)
s_branch Lr_it0_%=
Lr_tc1_%=:
s_add_u32 s63,s87,71303168
buffer_load_dwordx4 a[0:3],v11,s[36:39],s63 offen offset:0 sc1
buffer_load_dwordx4 a[4:7],v11,s[36:39],s63 offen offset:1024 sc1
buffer_load_dwordx4 a[8:11],v11,s[36:39],s63 offen offset:2048 sc1
buffer_load_dwordx4 a[12:15],v11,s[36:39],s63 offen offset:3072 sc1
s_add_u32 s63,s87,89128960
buffer_load_dwordx4 a[16:19],v11,s[36:39],s63 offen offset:0 sc1
buffer_load_dwordx4 a[20:23],v11,s[36:39],s63 offen offset:1024 sc1
buffer_load_dwordx4 a[24:27],v11,s[36:39],s63 offen offset:2048 sc1
buffer_load_dwordx4 a[28:31],v11,s[36:39],s63 offen offset:3072 sc1
s_add_u32 s63,s87,99614720
buffer_load_dwordx4 a[32:35],v11,s[36:39],s63 offen offset:0 sc1
buffer_load_dwordx4 a[36:39],v11,s[36:39],s63 offen offset:1024 sc1
buffer_load_dwordx4 a[40:43],v11,s[36:39],s63 offen offset:2048 sc1
buffer_load_dwordx4 a[44:47],v11,s[36:39],s63 offen offset:3072 sc1
s_add_u32 s63,s87,109051904
buffer_load_dwordx4 a[48:51],v11,s[36:39],s63 offen offset:0 sc1
buffer_load_dwordx4 a[52:55],v11,s[36:39],s63 offen offset:1024 sc1
buffer_load_dwordx4 a[56:59],v11,s[36:39],s63 offen offset:2048 sc1
buffer_load_dwordx4 a[60:63],v11,s[36:39],s63 offen offset:3072 sc1
buffer_load_dword v8,v6,s[36:39],s69 offen offset:0 sc1
s_waitcnt vmcnt(17)
buffer_store_dword v7,v5,s[36:39],s60 offen offset:0 sc1
s_mov_b32 s60,536870912
s_waitcnt vmcnt(1)
s_branch Lr_it0_%=
Lr_done_%=:
s_waitcnt vmcnt(0) lgkmcnt(0)
s_barrier
Lcode_r_e:)"
  :: "s"(ws), "s"(sK), "s"(sStep), "s"(sRq), "s"(sEpR), "v"(vRP), "v"(vRF), "v"(vRwr), "v"(vRrd), "v"(vRS), "v"(vFlag), "v"(vL4), "v"(vRL) : "v0", "v1", "v2", "v3", "v4", "v5", "v6", "v7", "v8", "v9", "v10", "v11", "v12", "v13", "v14", "v15", "v16", "v17", "v18", "v19", "v20", "v21", "v22", "v23", "v24", "v25", "v26", "v27", "v28", "v29", "v30", "v31", "v32", "v33", "v34", "v35", "v36", "v37", "v38", "v39", "v40", "v41", "v42", "v43", "v44", "v45", "v46", "v47", "v48", "v49", "v50", "v51", "v52", "v53", "v54", "v55", "v56", "v57", "v58", "v59", "v60", "v61", "v62", "v63", "v64", "v65", "v66", "v67", "v68", "v69", "v70", "v71", "v72", "v73", "v74", "v75", "v76", "v77", "v78", "v79", "v80", "v81", "v82", "v83", "v84", "v85", "v86", "v87", "v88", "v89", "v90", "v91", "v92", "v93", "v94", "v95", "v96", "v97", "v98", "v99", "v100", "v101", "v102", "v103", "v104", "v105", "v106", "v107", "v108", "v109", "v110", "v111", "v112", "v113", "v114", "v115", "v116", "v117", "v118", "v119", "v120", "v121", "v122", "v123", "v124", "v125", "v126", "v127", "v128", "v129", "v130", "v131", "v132", "v133", "v134", "v135", "v136", "v137", "v138", "v139", "v140", "v141", "v142", "v143", "v144", "v145", "v146", "v147", "v148", "v149", "v150", "v151", "v152", "v153", "v154", "v155", "v156", "v157", "v158", "v159", "v160", "v161", "v162", "v163", "v164", "v165", "v166", "v167", "v168", "v169", "v170", "v171", "v172", "v173", "v174", "v175", "v176", "v177", "v178", "v179", "v180", "v181", "v182", "v183", "v184", "v185", "v186", "v187", "v188", "v189", "v190", "v191", "v192", "v193", "v194", "v195", "v196", "v197", "v198", "v199", "v200", "v201", "v202", "v203", "v204", "v205", "v206", "v207", "v208", "v209", "v210", "v211", "v212", "v213", "v214", "v215", "v216", "v217", "v218", "v219", "v220", "v221", "v222", "v223", "v224", "v225", "v226", "v227", "v228", "v229", "v230", "v231", "v255", "a0", "a1", "a2", "a3", "a4", "a5", "a6", "a7", "a8", "a9", "a10", "a11", "a12", "a13", "a14", "a15", "a16", "a17", "a18", "a19", "a20", "a21", "a22", "a23", "a24", "a25", "a26", "a27", "a28", "a29", "a30", "a31", "a32", "a33", "a34", "a35", "a36", "a37", "a38", "a39", "a40", "a41", "a42", "a43", "a44", "a45", "a46", "a47", "a48", "a49", "a50", "a51", "a52", "a53", "a54", "a55", "a56", "a57", "a58", "a59", "a60", "a61", "a62", "a63", "a64", "a65", "a66", "a67", "a68", "a69", "a70", "a71", "a72", "a73", "a74", "a75", "a76", "a77", "a78", "a79", "a80", "a81", "a82", "a83", "a84", "a85", "a86", "a87", "a88", "a89", "a90", "a91", "a92", "a93", "a94", "a95", "a96", "a97", "a98", "a99", "a100", "a101", "a102", "a103", "a104", "a105", "a106", "a107", "a108", "a109", "a110", "a111", "a112", "a113", "a114", "a115", "a116", "a117", "a118", "a119", "a120", "a121", "a122", "a123", "a124", "a125", "a126", "a127", "a128", "a129", "a130", "a131", "a132", "a133", "a134", "a135", "a136", "a137", "a138", "a139", "a140", "a141", "a142", "a143", "a144", "a145", "a146", "a147", "a148", "a149", "a150", "a151", "a152", "a153", "a154", "a155", "a156", "a157", "a158", "a159", "a160", "a161", "a162", "a163", "a164", "a165", "a166", "a167", "a168", "a169", "a170", "a171", "a172", "a173", "a174", "a175", "a176", "a177", "a178", "a179", "a180", "a181", "a182", "a183", "a184", "a185", "a186", "a187", "a188", "a189", "a190", "a191", "a192", "a193", "a194", "a195", "a196", "a197", "a198", "a199", "a200", "a201", "a202", "a203", "a204", "a205", "a206", "a207", "a208", "a209", "a210", "a211", "a212", "a213", "a214", "a215", "a216", "a217", "a218", "a219", "a220", "a221", "a222", "a223", "a224", "a225", "a226", "a227", "a228", "a229", "a230", "a231", "a232", "a233", "a234", "a235", "a236", "a237", "a238", "a239", "a240", "a241", "a242", "a243", "a244", "a245", "a246", "a247", "a248", "a249", "a250", "a251", "a252", "a253", "a254", "a255", "s36", "s37", "s38", "s39", "s40", "s41", "s42", "s43", "s44", "s45", "s46", "s47", "s48", "s49", "s50", "s51", "s52", "s53", "s54", "s55", "s56", "s57", "s58", "s59", "s60", "s61", "s62", "s63", "s64", "s65", "s66", "s67", "s68", "s69", "s70", "s71", "s72", "s73", "s74", "s75", "s76", "s77", "s78", "s79", "s80", "s81", "s82", "s83", "s84", "s85", "s86", "s87", "s88", "s89", "s90", "s91", "s92", "s93", "s94", "s95", "s96", "s97", "s98", "s99", "vcc", "scc", "memory");
      if (N3) { kind = 3; rep = 5; }
      else if (N1 && slice >= 2) { kind = 4; rep = 5; kh = (slice - 2) >> 2; slice = (slice - 2) & 3; }
    }
   if (kind < 3) {
    KARG(ws, 10) KARG(w12, 5 + kind)
    COMMON(kind == 1 ? 8 : 16, kind == 1 ? OFF_FXP : OFF_FH, (kind == 1 ? OFF_FH : OFF_FX1) + slice * 128, AUX + 1536, 2048, 2048, 4);
    const int rot = (ROT >> (kind - 1) & 1) * rep, g0 = ((wave >> 1) + rot) & 3, g1 = g0 ^ 2;
    const int r0 = 16 + l15 - (kind == 2 ? ((g0 & 1) ? -1 : 1) * (1 + (g0 >> 1)) : 0), r1 = 16 + l15 - (kind == 2 ? ((g1 & 1) ? -1 : 1) * (1 + (g1 >> 1)) : 0);
    const unsigned sb = (kind == 1 ? OFF_XP : OFF_HS) - 32768 + (wave & 1) * 4096 + lg * 256;
    const unsigned vSrc = sb + g0 * 8192 + (r0 >> 4) * 32768 + (r0 & 15) * 16, vSrc2 = sb + g1 * 8192 + (r1 >> 4) * 32768 + (r1 & 15) * 16;
    const unsigned vOut = (kind == 1 ? OFF_HS : OFF_X1) + (slice * 4 + wave) * 1024 + vA;
    const unsigned vX = AUX + wave * 2048 + (kind == 1 ? l15 * 16 : vA), vX2 = AUX + wave * 2048 + l15 * 8;
    const unsigned vAuxS = kind == 1 ? OFF_S1 + ln * 4 : OFF_XP + (slice * 4 + wave) * 1024 + vA;
    const unsigned vAuxS2 = ln < 32 ? OFF_S1 - 1024 + 256 + ln * 4 : OOB;
    constexpr unsigned RED = AUX + 24576 + 1536;
    const unsigned vOut2 = wave != 2 ? OOB : kind == 1 ? (slice == 0 && ln < 16 ? OFF_S1 + 256 + ln * 8 : OOB) : ln < 32 ? OFF_ST + slice * 128 + ln * 4 : OOB;
    const unsigned vStW = RED + wave * 4 + (kind == 1 ? l15 * 32 + (lg == lq ? 16 : 0) : (l15 * 2 + (lg & 1)) * 16), vStR = RED + (kind == 1 ? l15 * 32 : (ln & 31) * 16);
    const unsigned sWw = (rot * 256 + 16 * wave) * 2048 + slice * 256;
    unsigned epOut;
    asm volatile(R"(
Lcode_g12_b:
s_cmp_lg_u32 %8,0
s_cbranch_scc1 Lg2_begin_%=
s_mov_b64 s[36:37],%1
s_mov_b32 s38,122727552
s_mov_b32 s39,0x00020000
s_mov_b32 s54,%2
s_mov_b32 s55,%3
s_mov_b32 s56,%4
s_mov_b32 s58,%5
s_mov_b32 s83,%6
s_mov_b32 s82,%7
s_mov_b32 s84,%8
v_mov_b32 v0,%9
v_mov_b32 v1,%10
v_mov_b32 v2,%11
v_mov_b32 v3,%12
v_mov_b32 v4,%13
v_mov_b32 v6,%14
v_mov_b32 v7,%15
v_mov_b32 v8,%16
v_mov_b32 v9,%17
v_mov_b32 v10,%18
v_mov_b32 v11,%19
v_mov_b32 v12,%20
v_mov_b32 v13,%21
v_mov_b32 v14,%22
v_mov_b32 v28,%23
v_mov_b32 v29,%24
v_mov_b32 v30,%25
v_mov_b32 v31,%26
v_mov_b32 v32,%27
s_mov_b64 s[40:41],%28
s_mov_b32 s42,2097152
s_mov_b32 s43,0x00020000
s_lshl_b32 s88,s54,11
s_mov_b32 s69,122727424
buffer_load_dword v5,off,s[36:39],s69 sc1
s_add_u32 m0,s88,0
s_add_u32 s61,s82,0
s_and_b32 s61,s61,2097151
buffer_load_dwordx4 v28,s[40:43],s61 offen offset:0  lds
s_add_u32 m0,s88,1024
s_nop 0
buffer_load_dwordx4 v29,s[40:43],s61 offen offset:0  lds
s_add_u32 m0,s88,8064
s_nop 0
buffer_load_dwordx4 v28,s[40:43],s61 offen offset:128  lds
s_add_u32 m0,s88,9088
s_nop 0
buffer_load_dwordx4 v29,s[40:43],s61 offen offset:128  lds
s_add_u32 m0,s88,16384
s_add_u32 s61,s82,131072
s_and_b32 s61,s61,2097151
buffer_load_dwordx4 v28,s[40:43],s61 offen offset:0  lds
s_add_u32 m0,s88,17408
s_nop 0
buffer_load_dwordx4 v29,s[40:43],s61 offen offset:0  lds
s_add_u32 m0,s88,24448
s_nop 0
buffer_load_dwordx4 v28,s[40:43],s61 offen offset:128  lds
s_add_u32 m0,s88,25472
s_nop 0
buffer_load_dwordx4 v29,s[40:43],s61 offen offset:128  lds
s_add_u32 m0,s88,32768
s_add_u32 s61,s82,262144
s_and_b32 s61,s61,2097151
buffer_load_dwordx4 v28,s[40:43],s61 offen offset:0  lds
s_add_u32 m0,s88,33792
s_nop 0
buffer_load_dwordx4 v29,s[40:43],s61 offen offset:0  lds
s_add_u32 m0,s88,40832
s_nop 0
buffer_load_dwordx4 v28,s[40:43],s61 offen offset:128  lds
s_add_u32 m0,s88,41856
s_nop 0
buffer_load_dwordx4 v29,s[40:43],s61 offen offset:128  lds
s_add_u32 m0,s88,49152
s_add_u32 s61,s82,393216
s_and_b32 s61,s61,2097151
buffer_load_dwordx4 v28,s[40:43],s61 offen offset:0  lds
s_add_u32 m0,s88,50176
s_nop 0
buffer_load_dwordx4 v29,s[40:43],s61 offen offset:0  lds
s_add_u32 m0,s88,57216
s_nop 0
buffer_load_dwordx4 v28,s[40:43],s61 offen offset:128  lds
s_add_u32 m0,s88,58240
s_nop 0
buffer_load_dwordx4 v29,s[40:43],s61 offen offset:128  lds
s_add_u32 m0,s88,65536
s_add_u32 s61,s82,524288
s_and_b32 s61,s61,2097151
buffer_load_dwordx4 v28,s[40:43],s61 offen offset:0  lds
s_add_u32 m0,s88,66560
s_nop 0
buffer_load_dwordx4 v29,s[40:43],s61 offen offset:0  lds
s_add_u32 m0,s88,73600
s_nop 0
buffer_load_dwordx4 v28,s[40:43],s61 offen offset:128  lds
s_add_u32 m0,s88,74624
s_nop 0
buffer_load_dwordx4 v29,s[40:43],s61 offen offset:128  lds
s_add_u32 m0,s88,81920
s_add_u32 s61,s82,655360
s_and_b32 s61,s61,2097151
buffer_load_dwordx4 v28,s[40:43],s61 offen offset:0  lds
s_add_u32 m0,s88,82944
s_nop 0
buffer_load_dwordx4 v29,s[40:43],s61 offen offset:0  lds
s_add_u32 m0,s88,89984
s_nop 0
buffer_load_dwordx4 v28,s[40:43],s61 offen offset:128  lds
s_add_u32 m0,s88,91008
s_nop 0
buffer_load_dwordx4 v29,s[40:43],s61 offen offset:128  lds
s_add_u32 m0,s88,98304
s_add_u32 s61,s82,786432
s_and_b32 s61,s61,2097151
buffer_load_dwordx4 v28,s[40:43],s61 offen offset:0  lds
s_add_u32 m0,s88,99328
s_nop 0
buffer_load_dwordx4 v29,s[40:43],s61 offen offset:0  lds
s_add_u32 m0,s88,106368
s_nop 0
buffer_load_dwordx4 v28,s[40:43],s61 offen offset:128  lds
s_add_u32 m0,s88,107392
s_nop 0
buffer_load_dwordx4 v29,s[40:43],s61 offen offset:128  lds
v_add_u32 v36,49152,v30
v_add_u32 v37,49152,v31
v_add_u32 v38,98304,v30
v_add_u32 v39,98304,v31
v_mov_b32 v76,0x3f803f80
v_mov_b32 v77,0x3f803f80
v_mov_b32 v78,0x3f803f80
v_mov_b32 v79,0x3f803f80
s_waitcnt vmcnt(24)
s_barrier
s_add_u32 m0,s88,114688
s_add_u32 s61,s82,917504
s_and_b32 s61,s61,2097151
buffer_load_dwordx4 v28,s[40:43],s61 offen offset:0  lds
s_add_u32 m0,s88,115712
ds_read_b64_tr_b16 a[0:1],v30 offset:0
buffer_load_dwordx4 v29,s[40:43],s61 offen offset:0  lds
s_add_u32 m0,s88,122752
ds_read_b64_tr_b16 a[2:3],v30 offset:512
buffer_load_dwordx4 v28,s[40:43],s61 offen offset:128  lds
s_add_u32 m0,s88,123776
ds_read_b64_tr_b16 a[4:5],v31 offset:0
buffer_load_dwordx4 v29,s[40:43],s61 offen offset:128  lds
ds_read_b64_tr_b16 a[6:7],v31 offset:512
ds_read_b64_tr_b16 a[8:9],v30 offset:4096
ds_read_b64_tr_b16 a[10:11],v30 offset:4608
ds_read_b64_tr_b16 a[12:13],v31 offset:4096
ds_read_b64_tr_b16 a[14:15],v31 offset:4608
s_waitcnt vmcnt(24)
s_barrier
s_add_u32 m0,s88,131072
s_add_u32 s61,s82,1048576
s_and_b32 s61,s61,2097151
buffer_load_dwordx4 v28,s[40:43],s61 offen offset:0  lds
s_add_u32 m0,s88,132096
ds_read_b64_tr_b16 a[16:17],v30 offset:16384
buffer_load_dwordx4 v29,s[40:43],s61 offen offset:0  lds
s_add_u32 m0,s88,139136
ds_read_b64_tr_b16 a[18:19],v30 offset:16896
buffer_load_dwordx4 v28,s[40:43],s61 offen offset:128  lds
s_add_u32 m0,s88,140160
ds_read_b64_tr_b16 a[20:21],v31 offset:16384
buffer_load_dwordx4 v29,s[40:43],s61 offen offset:128  lds
ds_read_b64_tr_b16 a[22:23],v31 offset:16896
ds_read_b64_tr_b16 a[24:25],v30 offset:20480
ds_read_b64_tr_b16 a[26:27],v30 offset:20992
ds_read_b64_tr_b16 a[28:29],v31 offset:20480
ds_read_b64_tr_b16 a[30:31],v31 offset:20992
s_waitcnt lgkmcnt(8)
v_mfma_f32_16x16x32_bf16 v[20:23],a[0:3],v[76:79],0
v_mfma_f32_16x16x32_bf16 v[24:27],a[4:7],v[76:79],0
v_mfma_f32_16x16x32_bf16 v[20:23],a[8:11],v[76:79],v[20:23]
v_mfma_f32_16x16x32_bf16 v[24:27],a[12:15],v[76:79],v[24:27]
s_waitcnt vmcnt(24)
s_barrier
s_add_u32 m0,s88,0
s_add_u32 s61,s82,1179648
s_and_b32 s61,s61,2097151
buffer_load_dwordx4 v28,s[40:43],s61 offen offset:0  lds
s_add_u32 m0,s88,1024
ds_read_b64_tr_b16 a[32:33],v30 offset:32768
buffer_load_dwordx4 v29,s[40:43],s61 offen offset:0  lds
s_add_u32 m0,s88,8064
ds_read_b64_tr_b16 a[34:35],v30 offset:33280
buffer_load_dwordx4 v28,s[40:43],s61 offen offset:128  lds
s_add_u32 m0,s88,9088
ds_read_b64_tr_b16 a[36:37],v31 offset:32768
buffer_load_dwordx4 v29,s[40:43],s61 offen offset:128  lds
ds_read_b64_tr_b16 a[38:39],v31 offset:33280
ds_read_b64_tr_b16 a[40:41],v30 offset:36864
ds_read_b64_tr_b16 a[42:43],v30 offset:37376)"
R"(
ds_read_b64_tr_b16 a[44:45],v31 offset:36864
ds_read_b64_tr_b16 a[46:47],v31 offset:37376
s_waitcnt lgkmcnt(8)
v_mfma_f32_16x16x32_bf16 v[20:23],a[16:19],v[76:79],v[20:23]
v_mfma_f32_16x16x32_bf16 v[24:27],a[20:23],v[76:79],v[24:27]
v_mfma_f32_16x16x32_bf16 v[20:23],a[24:27],v[76:79],v[20:23]
v_mfma_f32_16x16x32_bf16 v[24:27],a[28:31],v[76:79],v[24:27]
s_waitcnt vmcnt(24)
s_barrier
s_add_u32 m0,s88,16384
s_add_u32 s61,s82,1310720
s_and_b32 s61,s61,2097151
buffer_load_dwordx4 v28,s[40:43],s61 offen offset:0  lds
s_add_u32 m0,s88,17408
ds_read_b64_tr_b16 a[48:49],v36 offset:0
buffer_load_dwordx4 v29,s[40:43],s61 offen offset:0  lds
s_add_u32 m0,s88,24448
ds_read_b64_tr_b16 a[50:51],v36 offset:512
buffer_load_dwordx4 v28,s[40:43],s61 offen offset:128  lds
s_add_u32 m0,s88,25472
ds_read_b64_tr_b16 a[52:53],v37 offset:0
buffer_load_dwordx4 v29,s[40:43],s61 offen offset:128  lds
ds_read_b64_tr_b16 a[54:55],v37 offset:512
ds_read_b64_tr_b16 a[56:57],v36 offset:4096
ds_read_b64_tr_b16 a[58:59],v36 offset:4608
ds_read_b64_tr_b16 a[60:61],v37 offset:4096
ds_read_b64_tr_b16 a[62:63],v37 offset:4608
s_waitcnt lgkmcnt(8)
v_mfma_f32_16x16x32_bf16 v[20:23],a[32:35],v[76:79],v[20:23]
v_mfma_f32_16x16x32_bf16 v[24:27],a[36:39],v[76:79],v[24:27]
v_mfma_f32_16x16x32_bf16 v[20:23],a[40:43],v[76:79],v[20:23]
v_mfma_f32_16x16x32_bf16 v[24:27],a[44:47],v[76:79],v[24:27]
s_waitcnt vmcnt(24)
s_barrier
s_add_u32 m0,s88,32768
s_add_u32 s61,s82,1441792
s_and_b32 s61,s61,2097151
buffer_load_dwordx4 v28,s[40:43],s61 offen offset:0  lds
s_add_u32 m0,s88,33792
ds_read_b64_tr_b16 a[64:65],v36 offset:16384
buffer_load_dwordx4 v29,s[40:43],s61 offen offset:0  lds
s_add_u32 m0,s88,40832
ds_read_b64_tr_b16 a[66:67],v36 offset:16896
buffer_load_dwordx4 v28,s[40:43],s61 offen offset:128  lds
s_add_u32 m0,s88,41856
ds_read_b64_tr_b16 a[68:69],v37 offset:16384
buffer_load_dwordx4 v29,s[40:43],s61 offen offset:128  lds
ds_read_b64_tr_b16 a[70:71],v37 offset:16896
ds_read_b64_tr_b16 a[72:73],v36 offset:20480
ds_read_b64_tr_b16 a[74:75],v36 offset:20992
ds_read_b64_tr_b16 a[76:77],v37 offset:20480
ds_read_b64_tr_b16 a[78:79],v37 offset:20992
s_waitcnt lgkmcnt(8)
v_mfma_f32_16x16x32_bf16 v[20:23],a[48:51],v[76:79],v[20:23]
v_mfma_f32_16x16x32_bf16 v[24:27],a[52:55],v[76:79],v[24:27]
v_mfma_f32_16x16x32_bf16 v[20:23],a[56:59],v[76:79],v[20:23]
v_mfma_f32_16x16x32_bf16 v[24:27],a[60:63],v[76:79],v[24:27]
s_waitcnt vmcnt(24)
s_barrier
s_add_u32 m0,s88,49152
s_add_u32 s61,s82,1572864
s_and_b32 s61,s61,2097151
buffer_load_dwordx4 v28,s[40:43],s61 offen offset:0  lds
s_add_u32 m0,s88,50176
ds_read_b64_tr_b16 a[80:81],v36 offset:32768
buffer_load_dwordx4 v29,s[40:43],s61 offen offset:0  lds
s_add_u32 m0,s88,57216
ds_read_b64_tr_b16 a[82:83],v36 offset:33280
buffer_load_dwordx4 v28,s[40:43],s61 offen offset:128  lds
s_add_u32 m0,s88,58240
ds_read_b64_tr_b16 a[84:85],v37 offset:32768
buffer_load_dwordx4 v29,s[40:43],s61 offen offset:128  lds
ds_read_b64_tr_b16 a[86:87],v37 offset:33280
ds_read_b64_tr_b16 a[88:89],v36 offset:36864
ds_read_b64_tr_b16 a[90:91],v36 offset:37376
ds_read_b64_tr_b16 a[92:93],v37 offset:36864
ds_read_b64_tr_b16 a[94:95],v37 offset:37376
s_waitcnt lgkmcnt(8)
v_mfma_f32_16x16x32_bf16 v[20:23],a[64:67],v[76:79],v[20:23]
v_mfma_f32_16x16x32_bf16 v[24:27],a[68:71],v[76:79],v[24:27]
v_mfma_f32_16x16x32_bf16 v[20:23],a[72:75],v[76:79],v[20:23]
v_mfma_f32_16x16x32_bf16 v[24:27],a[76:79],v[76:79],v[24:27]
s_waitcnt vmcnt(24)
s_barrier
s_add_u32 m0,s88,65536
s_add_u32 s61,s82,1703936
s_and_b32 s61,s61,2097151
buffer_load_dwordx4 v28,s[40:43],s61 offen offset:0  lds
s_add_u32 m0,s88,66560
ds_read_b64_tr_b16 a[96:97],v38 offset:0
buffer_load_dwordx4 v29,s[40:43],s61 offen offset:0  lds
s_add_u32 m0,s88,73600
ds_read_b64_tr_b16 a[98:99],v38 offset:512
buffer_load_dwordx4 v28,s[40:43],s61 offen offset:128  lds
s_add_u32 m0,s88,74624
ds_read_b64_tr_b16 a[100:101],v39 offset:0
buffer_load_dwordx4 v29,s[40:43],s61 offen offset:128  lds
ds_read_b64_tr_b16 a[102:103],v39 offset:512
ds_read_b64_tr_b16 a[104:105],v38 offset:4096
ds_read_b64_tr_b16 a[106:107],v38 offset:4608
ds_read_b64_tr_b16 a[108:109],v39 offset:4096
ds_read_b64_tr_b16 a[110:111],v39 offset:4608
s_waitcnt lgkmcnt(8)
v_mfma_f32_16x16x32_bf16 v[20:23],a[80:83],v[76:79],v[20:23]
v_mfma_f32_16x16x32_bf16 v[24:27],a[84:87],v[76:79],v[24:27]
v_mfma_f32_16x16x32_bf16 v[20:23],a[88:91],v[76:79],v[20:23]
v_mfma_f32_16x16x32_bf16 v[24:27],a[92:95],v[76:79],v[24:27]
s_waitcnt vmcnt(24)
s_barrier
s_add_u32 m0,s88,81920
s_add_u32 s61,s82,1835008
s_and_b32 s61,s61,2097151
buffer_load_dwordx4 v28,s[40:43],s61 offen offset:0  lds
s_add_u32 m0,s88,82944
ds_read_b64_tr_b16 a[112:113],v38 offset:16384
buffer_load_dwordx4 v29,s[40:43],s61 offen offset:0  lds
s_add_u32 m0,s88,89984
ds_read_b64_tr_b16 a[114:115],v38 offset:16896
buffer_load_dwordx4 v28,s[40:43],s61 offen offset:128  lds
s_add_u32 m0,s88,91008
ds_read_b64_tr_b16 a[116:117],v39 offset:16384
buffer_load_dwordx4 v29,s[40:43],s61 offen offset:128  lds
ds_read_b64_tr_b16 a[118:119],v39 offset:16896
ds_read_b64_tr_b16 a[120:121],v38 offset:20480
ds_read_b64_tr_b16 a[122:123],v38 offset:20992
ds_read_b64_tr_b16 a[124:125],v39 offset:20480
ds_read_b64_tr_b16 a[126:127],v39 offset:20992
s_waitcnt lgkmcnt(8)
v_mfma_f32_16x16x32_bf16 v[20:23],a[96:99],v[76:79],v[20:23]
v_mfma_f32_16x16x32_bf16 v[24:27],a[100:103],v[76:79],v[24:27]
v_mfma_f32_16x16x32_bf16 v[20:23],a[104:107],v[76:79],v[20:23]
v_mfma_f32_16x16x32_bf16 v[24:27],a[108:111],v[76:79],v[24:27]
s_waitcnt vmcnt(24)
s_barrier
s_add_u32 m0,s88,98304
s_add_u32 s61,s82,1966080
s_and_b32 s61,s61,2097151
buffer_load_dwordx4 v28,s[40:43],s61 offen offset:0  lds
s_add_u32 m0,s88,99328
ds_read_b64_tr_b16 a[128:129],v38 offset:32768
buffer_load_dwordx4 v29,s[40:43],s61 offen offset:0  lds
s_add_u32 m0,s88,106368
ds_read_b64_tr_b16 a[130:131],v38 offset:33280
buffer_load_dwordx4 v28,s[40:43],s61 offen offset:128  lds
s_add_u32 m0,s88,107392
ds_read_b64_tr_b16 a[132:133],v39 offset:32768
buffer_load_dwordx4 v29,s[40:43],s61 offen offset:128  lds
ds_read_b64_tr_b16 a[134:135],v39 offset:33280
ds_read_b64_tr_b16 a[136:137],v38 offset:36864
ds_read_b64_tr_b16 a[138:139],v38 offset:37376
ds_read_b64_tr_b16 a[140:141],v39 offset:36864
ds_read_b64_tr_b16 a[142:143],v39 offset:37376
s_waitcnt lgkmcnt(8)
v_mfma_f32_16x16x32_bf16 v[20:23],a[112:115],v[76:79],v[20:23]
v_mfma_f32_16x16x32_bf16 v[24:27],a[116:119],v[76:79],v[24:27]
v_mfma_f32_16x16x32_bf16 v[20:23],a[120:123],v[76:79],v[20:23]
v_mfma_f32_16x16x32_bf16 v[24:27],a[124:127],v[76:79],v[24:27]
s_waitcnt vmcnt(24)
s_barrier
ds_read_b64_tr_b16 a[144:145],v30 offset:0
ds_read_b64_tr_b16 a[146:147],v30 offset:512
ds_read_b64_tr_b16 a[148:149],v31 offset:0
ds_read_b64_tr_b16 a[150:151],v31 offset:512
ds_read_b64_tr_b16 a[152:153],v30 offset:4096
ds_read_b64_tr_b16 a[154:155],v30 offset:4608
ds_read_b64_tr_b16 a[156:157],v31 offset:4096
ds_read_b64_tr_b16 a[158:159],v31 offset:4608
s_waitcnt lgkmcnt(8)
v_mfma_f32_16x16x32_bf16 v[20:23],a[128:131],v[76:79],v[20:23]
v_mfma_f32_16x16x32_bf16 v[24:27],a[132:135],v[76:79],v[24:27]
v_mfma_f32_16x16x32_bf16 v[20:23],a[136:139],v[76:79],v[20:23]
v_mfma_f32_16x16x32_bf16 v[24:27],a[140:143],v[76:79],v[24:27]
s_waitcnt vmcnt(20)
s_barrier
ds_read_b64_tr_b16 a[160:161],v30 offset:16384
ds_read_b64_tr_b16 a[162:163],v30 offset:16896
ds_read_b64_tr_b16 a[164:165],v31 offset:16384
ds_read_b64_tr_b16 a[166:167],v31 offset:16896
ds_read_b64_tr_b16 a[168:169],v30 offset:20480
ds_read_b64_tr_b16 a[170:171],v30 offset:20992
ds_read_b64_tr_b16 a[172:173],v31 offset:20480
ds_read_b64_tr_b16 a[174:175],v31 offset:20992
s_waitcnt lgkmcnt(8)
v_mfma_f32_16x16x32_bf16 v[20:23],a[144:147],v[76:79],v[20:23]
v_mfma_f32_16x16x32_bf16 v[24:27],a[148:151],v[76:79],v[24:27]
v_mfma_f32_16x16x32_bf16 v[20:23],a[152:155],v[76:79],v[20:23]
v_mfma_f32_16x16x32_bf16 v[24:27],a[156:159],v[76:79],v[24:27]
s_waitcnt vmcnt(16)
s_barrier
ds_read_b64_tr_b16 a[176:177],v30 offset:32768
ds_read_b64_tr_b16 a[178:179],v30 offset:33280
ds_read_b64_tr_b16 a[180:181],v31 offset:32768
ds_read_b64_tr_b16 a[182:183],v31 offset:33280
ds_read_b64_tr_b16 a[184:185],v30 offset:36864
ds_read_b64_tr_b16 a[186:187],v30 offset:37376
ds_read_b64_tr_b16 a[188:189],v31 offset:36864
ds_read_b64_tr_b16 a[190:191],v31 offset:37376
s_getpc_b64 s[46:47]
Lg1_cp_%=:
s_add_u32 s46,s46,Lg1_init_%=-Lg1_cp_%=)"
R"(
s_addc_u32 s47,s47,0
s_lshl_b32 s68,s54,13
v_lshl_add_u32 v60,v2,3,s68
v_min_u32 v61,Lg1_done_%=-Lg1_init_%=,v60
global_load_dword v64,v61,s[46:47]
v_add_u32 v60,32768,v60
v_min_u32 v62,Lg1_done_%=-Lg1_init_%=,v60
global_load_dword v65,v62,s[46:47]
s_waitcnt lgkmcnt(8)
v_mfma_f32_16x16x32_bf16 v[20:23],a[160:163],v[76:79],v[20:23]
v_mfma_f32_16x16x32_bf16 v[24:27],a[164:167],v[76:79],v[24:27]
v_mfma_f32_16x16x32_bf16 v[20:23],a[168:171],v[76:79],v[20:23]
v_mfma_f32_16x16x32_bf16 v[24:27],a[172:175],v[76:79],v[24:27]
s_waitcnt vmcnt(14)
s_barrier
ds_read_b64_tr_b16 a[192:193],v36 offset:0
ds_read_b64_tr_b16 a[194:195],v36 offset:512
ds_read_b64_tr_b16 a[196:197],v37 offset:0
ds_read_b64_tr_b16 a[198:199],v37 offset:512
ds_read_b64_tr_b16 a[200:201],v36 offset:4096
ds_read_b64_tr_b16 a[202:203],v36 offset:4608
ds_read_b64_tr_b16 a[204:205],v37 offset:4096
ds_read_b64_tr_b16 a[206:207],v37 offset:4608
s_waitcnt lgkmcnt(8)
v_mfma_f32_16x16x32_bf16 v[20:23],a[176:179],v[76:79],v[20:23]
v_mfma_f32_16x16x32_bf16 v[24:27],a[180:183],v[76:79],v[24:27]
v_mfma_f32_16x16x32_bf16 v[20:23],a[184:187],v[76:79],v[20:23]
v_mfma_f32_16x16x32_bf16 v[24:27],a[188:191],v[76:79],v[24:27]
s_waitcnt vmcnt(10)
s_barrier
ds_read_b64_tr_b16 a[208:209],v36 offset:16384
ds_read_b64_tr_b16 a[210:211],v36 offset:16896
ds_read_b64_tr_b16 a[212:213],v37 offset:16384
ds_read_b64_tr_b16 a[214:215],v37 offset:16896
ds_read_b64_tr_b16 a[216:217],v36 offset:20480
ds_read_b64_tr_b16 a[218:219],v36 offset:20992
ds_read_b64_tr_b16 a[220:221],v37 offset:20480
ds_read_b64_tr_b16 a[222:223],v37 offset:20992
s_waitcnt lgkmcnt(8)
v_mfma_f32_16x16x32_bf16 v[20:23],a[192:195],v[76:79],v[20:23]
v_mfma_f32_16x16x32_bf16 v[24:27],a[196:199],v[76:79],v[24:27]
v_mfma_f32_16x16x32_bf16 v[20:23],a[200:203],v[76:79],v[20:23]
v_mfma_f32_16x16x32_bf16 v[24:27],a[204:207],v[76:79],v[24:27]
s_waitcnt vmcnt(6)
s_barrier
ds_read_b64_tr_b16 a[224:225],v36 offset:32768
ds_read_b64_tr_b16 a[226:227],v36 offset:33280
ds_read_b64_tr_b16 a[228:229],v37 offset:32768
ds_read_b64_tr_b16 a[230:231],v37 offset:33280
ds_read_b64_tr_b16 a[232:233],v36 offset:36864
ds_read_b64_tr_b16 a[234:235],v36 offset:37376
ds_read_b64_tr_b16 a[236:237],v37 offset:36864
ds_read_b64_tr_b16 a[238:239],v37 offset:37376
s_waitcnt lgkmcnt(8)
v_mfma_f32_16x16x32_bf16 v[20:23],a[208:211],v[76:79],v[20:23]
v_mfma_f32_16x16x32_bf16 v[24:27],a[212:215],v[76:79],v[24:27]
v_mfma_f32_16x16x32_bf16 v[20:23],a[216:219],v[76:79],v[20:23]
v_mfma_f32_16x16x32_bf16 v[24:27],a[220:223],v[76:79],v[24:27]
s_waitcnt vmcnt(2)
s_barrier
ds_read_b64_tr_b16 a[240:241],v38 offset:0
ds_read_b64_tr_b16 a[242:243],v38 offset:512
ds_read_b64_tr_b16 a[244:245],v39 offset:0
ds_read_b64_tr_b16 a[246:247],v39 offset:512
ds_read_b64_tr_b16 a[248:249],v38 offset:4096
ds_read_b64_tr_b16 a[250:251],v38 offset:4608
ds_read_b64_tr_b16 a[252:253],v39 offset:4096
ds_read_b64_tr_b16 a[254:255],v39 offset:4608
s_waitcnt lgkmcnt(8)
v_mfma_f32_16x16x32_bf16 v[20:23],a[224:227],v[76:79],v[20:23]
v_mfma_f32_16x16x32_bf16 v[24:27],a[228:231],v[76:79],v[24:27]
v_mfma_f32_16x16x32_bf16 v[20:23],a[232:235],v[76:79],v[20:23]
v_mfma_f32_16x16x32_bf16 v[24:27],a[236:239],v[76:79],v[24:27]
s_waitcnt lgkmcnt(0)
v_mfma_f32_16x16x32_bf16 v[20:23],a[240:243],v[76:79],v[20:23]
v_mfma_f32_16x16x32_bf16 v[24:27],a[244:247],v[76:79],v[24:27]
v_mfma_f32_16x16x32_bf16 v[20:23],a[248:251],v[76:79],v[20:23]
v_mfma_f32_16x16x32_bf16 v[24:27],a[252:255],v[76:79],v[24:27]
s_waitcnt lgkmcnt(0)
s_barrier
v_readfirstlane_b32 s60,v5
s_nop 3
s_add_u32 s60,s60,1
s_cmp_eq_u32 s60,0
s_cselect_b32 s60,1,s60
Lg1_init_%=:
v_mov_b32 v5,s60
s_mov_b32 s59,536870912
s_add_u32 s57,s56,131072
s_mov_b32 s79,0
s_mov_b32 s75,0x3a800000
s_mov_b32 s76,0xc0135761
s_mov_b32 s77,0x3d372713
s_mov_b32 s44,s59
s_mov_b32 s45,s59
s_mov_b32 s46,s59
s_mov_b32 s47,s59
s_mov_b32 s48,s83
s_add_u32 s49,s48,s55
s_cmp_eq_u32 s49,s56
s_cselect_b32 s49,s57,s49
s_cmp_eq_u32 s49,s58
s_cselect_b32 s49,s59,s49
s_add_u32 s50,s49,s55
s_cmp_eq_u32 s50,s56
s_cselect_b32 s50,s57,s50
s_cmp_eq_u32 s50,s58
s_cselect_b32 s50,s59,s50
s_add_u32 s51,s50,s55
s_cmp_eq_u32 s51,s56
s_cselect_b32 s51,s57,s51
s_cmp_eq_u32 s51,s58
s_cselect_b32 s51,s59,s51
s_add_u32 s52,s51,s55
s_cmp_eq_u32 s52,s56
s_cselect_b32 s52,s57,s52
s_cmp_eq_u32 s52,s58
s_cselect_b32 s52,s59,s52
s_lshl_b32 s63,s54,12
s_add_u32 s63,s63,65536
s_add_u32 s64,s63,32768
s_lshl_b32 s88,s54,10
s_add_u32 s88,s88,131072
s_lshl_b32 s78,s54,11
s_add_u32 s78,s78,132608
v_mov_b32 v15,0x3727c5ac
v_mov_b32 v100,0x3f803f80
v_mov_b32 v101,0x3f803f80
v_mov_b32 v102,0x3f803f80
v_mov_b32 v103,0x3f803f80
s_mov_b32 s89,1048576
s_mov_b32 s82,0xaaaaaaaa
s_mov_b32 s83,0xaaaaaaaa
s_mov_b32 s84,0xcccccccc
s_mov_b32 s85,0xcccccccc
s_mov_b32 s98,0x00f0000f
s_mov_b32 s99,0xf0000f00
s_getpc_b64 s[90:91]
Lg1_pc_%=:
s_mov_b32 s68,Lg1_d1w0_%=-Lg1_pc_%=
s_cmp_eq_u32 s54,1
s_cselect_b32 s68,Lg1_d1w1_%=-Lg1_pc_%=,s68
s_cmp_eq_u32 s54,2
s_cselect_b32 s68,Lg1_d1w2_%=-Lg1_pc_%=,s68
s_cmp_eq_u32 s54,3
s_cselect_b32 s68,Lg1_d1w3_%=-Lg1_pc_%=,s68
s_add_u32 s96,s90,s68
s_addc_u32 s97,s91,0
s_mov_b32 s68,Lg1_d0w0_%=-Lg1_pc_%=
s_cmp_eq_u32 s54,1
s_cselect_b32 s68,Lg1_d0w1_%=-Lg1_pc_%=,s68
s_cmp_eq_u32 s54,2
s_cselect_b32 s68,Lg1_d0w2_%=-Lg1_pc_%=,s68
s_cmp_eq_u32 s54,3
s_cselect_b32 s68,Lg1_d0w3_%=-Lg1_pc_%=,s68
s_add_u32 s94,s90,s68
s_addc_u32 s95,s91,0
s_mov_b32 s68,Lg1_a1w0_%=-Lg1_pc_%=
s_cmp_eq_u32 s54,1
s_cselect_b32 s68,Lg1_a1w1_%=-Lg1_pc_%=,s68
s_cmp_eq_u32 s54,2
s_cselect_b32 s68,Lg1_a1w2_%=-Lg1_pc_%=,s68
s_cmp_eq_u32 s54,3
s_cselect_b32 s68,Lg1_a1w3_%=-Lg1_pc_%=,s68
s_add_u32 s92,s90,s68
s_addc_u32 s93,s91,0
s_mov_b32 s68,Lg1_a0w0_%=-Lg1_pc_%=
s_cmp_eq_u32 s54,1
s_cselect_b32 s68,Lg1_a0w1_%=-Lg1_pc_%=,s68
s_cmp_eq_u32 s54,2
s_cselect_b32 s68,Lg1_a0w2_%=-Lg1_pc_%=,s68
s_cmp_eq_u32 s54,3
s_cselect_b32 s68,Lg1_a0w3_%=-Lg1_pc_%=,s68
s_add_u32 s90,s90,s68
s_addc_u32 s91,s91,0
s_mov_b32 s66,s59
s_mov_b32 s65,0
s_mov_b32 s62,s63
s_branch Lg1_slow0_%=
Lg1_a0w0_%=:
v_mfma_f32_16x16x32_bf16 v[28:31],a[0:3],v[44:47],0
s_add_u32 m0,s62,16384
ds_read_b128 v[72:75],v2 offset:7168
s_sub_u32 s69,s66,1048576
v_mfma_f32_16x16x32_bf16 v[32:35],a[4:7],v[44:47],0
buffer_load_dwordx4 v1,s[36:39],s66 offen offset:0 sc1 lds
v_mfma_f32_16x16x32_bf16 v[104:107],v[100:103],v[44:47],0
s_cmp_lt_u32 s69,2097152
s_cselect_b32 s89,3145728,s89
s_sub_u32 s69,s66,3145728
v_mfma_f32_16x16x32_bf16 v[108:111],v[44:47],v[44:47],0
s_cmp_lt_u32 s69,1048576
v_fma_f32 v36,-v96,v20,v36
v_fma_f32 v37,-v96,v21,v37
v_mfma_f32_16x16x32_bf16 v[28:31],a[8:11],v[48:51],v[28:31]
s_cselect_b32 s89,4194304,s89
ds_read_b128 v[44:47],v2 offset:8192
v_fma_f32 v38,-v96,v22,v38
v_mfma_f32_16x16x32_bf16 v[32:35],a[12:15],v[48:51],v[32:35]
buffer_load_dwordx4 v1,s[36:39],s66 offen offset:1024 sc1 lds)"
R"(
v_mfma_f32_16x16x32_bf16 v[104:107],v[100:103],v[48:51],v[104:107]
s_lshr_b32 s73,s47,0
v_fma_f32 v39,-v96,v23,v39
v_mul_f32 v76,v36,v36
v_mfma_f32_16x16x32_bf16 v[108:111],v[48:51],v[48:51],v[108:111]
s_lshr_b32 s72,s66,6
v_mul_f32 v77,v37,v37
v_mul_f32 v78,v38,v38
v_mfma_f32_16x16x32_bf16 v[28:31],a[16:19],v[52:55],v[28:31]
s_lshr_b32 s71,s62,2
ds_read_b128 v[48:51],v2 offset:9216
v_mul_f32 v79,v39,v39
v_mfma_f32_16x16x32_bf16 v[32:35],a[20:23],v[52:55],v[32:35]
buffer_load_dwordx4 v1,s[36:39],s66 offen offset:2048 sc1 lds
v_mfma_f32_16x16x32_bf16 v[104:107],v[100:103],v[52:55],v[104:107]
s_cmp_lt_u32 s66,4194304
v_fma_f32 v76,v98,v76,v97
v_mul_f32 v76,v36,v76
v_mfma_f32_16x16x32_bf16 v[108:111],v[52:55],v[52:55],v[108:111]
s_cselect_b32 s72,s59,s72
v_exp_f32 v76,v76
v_mfma_f32_16x16x32_bf16 v[28:31],a[24:27],v[56:59],v[28:31]
ds_read_b128 v[52:55],v2 offset:10240
v_fma_f32 v76,v76,v99,v99
v_mfma_f32_16x16x32_bf16 v[32:35],a[28:31],v[56:59],v[32:35]
buffer_load_dwordx4 v1,s[36:39],s66 offen offset:3072 sc1 lds
v_mfma_f32_16x16x32_bf16 v[104:107],v[100:103],v[56:59],v[104:107]
s_add_u32 m0,s71,s88
v_rcp_f32 v76,v76
v_mfma_f32_16x16x32_bf16 v[108:111],v[56:59],v[56:59],v[108:111]
s_waitcnt lgkmcnt(3)
buffer_load_dword v10,s[36:39],s72 offen offset:0 sc1 lds
v_mfma_f32_16x16x32_bf16 v[28:31],a[32:35],v[60:63],v[28:31]
s_xor_b32 s87,s64,65536
ds_read_b128 v[56:59],v2 offset:11264
v_fma_f32 v77,v98,v77,v97
v_mfma_f32_16x16x32_bf16 v[32:35],a[36:39],v[60:63],v[32:35]
s_cmp_ge_u32 s66,s59
v_mul_f32 v77,v37,v77
v_fma_f32 v78,v98,v78,v97
v_mfma_f32_16x16x32_bf16 v[28:31],a[40:43],v[64:67],v[28:31]
s_cselect_b32 s86,s49,s50
ds_read_b128 v[60:63],v2 offset:12288
v_mul_f32 v78,v38,v78
v_mfma_f32_16x16x32_bf16 v[32:35],a[44:47],v[64:67],v[32:35]
s_cselect_b32 s87,s87,s63
v_exp_f32 v77,v77
v_mfma_f32_16x16x32_bf16 v[28:31],a[48:51],v[68:71],v[28:31]
s_mov_b32 s62,s87
ds_read_b128 v[64:67],v2 offset:13312
v_fma_f32 v77,v77,v99,v99
v_mfma_f32_16x16x32_bf16 v[32:35],a[52:55],v[68:71],v[32:35]
s_cmp_lt_u32 s86,s89
v_exp_f32 v78,v78
v_mfma_f32_16x16x32_bf16 v[28:31],a[56:59],v[72:75],v[28:31]
ds_read_b128 v[68:71],v2 offset:14336
v_fma_f32 v78,v78,v99,v99
v_mfma_f32_16x16x32_bf16 v[32:35],a[60:63],v[72:75],v[32:35]
s_waitcnt lgkmcnt(3)
v_rcp_f32 v77,v77
v_mfma_f32_16x16x32_bf16 v[28:31],a[64:67],v[44:47],v[28:31]
s_waitcnt vmcnt(10)
ds_read_b128 v[72:75],v2 offset:15360
v_fma_f32 v79,v98,v79,v97
v_mfma_f32_16x16x32_bf16 v[32:35],a[68:71],v[44:47],v[32:35]
s_barrier
v_mfma_f32_16x16x32_bf16 v[28:31],a[72:75],v[48:51],v[28:31]
ds_read_b128 v[44:47],v2 offset:16384
ds_read_b32 v16,v7 offset:0
v_mfma_f32_16x16x32_bf16 v[32:35],a[76:79],v[48:51],v[32:35]
v_rcp_f32 v78,v78
v_mfma_f32_16x16x32_bf16 v[28:31],a[80:83],v[52:55],v[28:31]
ds_read_b128 v[48:51],v2 offset:17408
v_mul_f32 v79,v39,v79
v_mfma_f32_16x16x32_bf16 v[32:35],a[84:87],v[52:55],v[32:35]
v_exp_f32 v79,v79
v_mfma_f32_16x16x32_bf16 v[28:31],a[88:91],v[56:59],v[28:31]
s_waitcnt lgkmcnt(1)
ds_read_b128 v[52:55],v2 offset:18432
v_fma_f32 v79,v79,v99,v99
v_mfma_f32_16x16x32_bf16 v[32:35],a[92:95],v[56:59],v[32:35]
v_cmp_eq_u32 vcc,s60,v16
v_rcp_f32 v79,v79
v_mul_f32 v36,v36,v76
v_mfma_f32_16x16x32_bf16 v[28:31],a[96:99],v[60:63],v[28:31]
s_cselect_b32 vcc_lo,-1,vcc_lo
ds_read_b128 v[56:59],v2 offset:19456
v_mul_f32 v37,v37,v77
v_mfma_f32_16x16x32_bf16 v[32:35],a[100:103],v[60:63],v[32:35]
s_cmp_eq_u32 vcc_lo,-1
v_mul_f32 v38,v38,v78
v_mul_f32 v39,v39,v79
v_mfma_f32_16x16x32_bf16 v[28:31],a[104:107],v[64:67],v[28:31]
s_cselect_b32 s67,s86,s59
ds_read_b128 v[60:63],v2 offset:20480
v_cvt_pk_bf16_f32 v84,v36,v37
v_mfma_f32_16x16x32_bf16 v[32:35],a[108:111],v[64:67],v[32:35]
s_lshr_b32 s69,s50,5
v_cvt_pk_bf16_f32 v85,v38,v39
v_fma_f32 v40,-v96,v24,v40
v_mfma_f32_16x16x32_bf16 v[28:31],a[112:115],v[68:71],v[28:31]
s_cmp_lt_u32 s50,4194304
ds_read_b128 v[64:67],v2 offset:21504
v_fma_f32 v41,-v96,v25,v41
v_mfma_f32_16x16x32_bf16 v[32:35],a[116:119],v[68:71],v[32:35]
s_cselect_b32 s68,0xffffcfff,-1
v_fma_f32 v42,-v96,v26,v42
v_fma_f32 v43,-v96,v27,v43
v_mfma_f32_16x16x32_bf16 v[28:31],a[120:123],v[72:75],v[28:31]
s_and_b32 s68,s68,s69
ds_read_b128 v[68:71],v2 offset:22528
v_mul_f32 v80,v40,v40
v_mfma_f32_16x16x32_bf16 v[32:35],a[124:127],v[72:75],v[32:35]
s_waitcnt lgkmcnt(3)
v_mul_f32 v81,v41,v41
v_mul_f32 v82,v42,v42
v_mfma_f32_16x16x32_bf16 v[28:31],a[128:131],v[44:47],v[28:31]
s_cmp_lt_u32 s50,s89
ds_read_b128 v[72:75],v2 offset:23552
v_mul_f32 v83,v43,v43
v_mfma_f32_16x16x32_bf16 v[32:35],a[132:135],v[44:47],v[32:35]
s_cselect_b32 s68,s59,s68
v_fma_f32 v80,v98,v80,v97
v_mul_f32 v80,v40,v80
v_mfma_f32_16x16x32_bf16 v[104:107],v[100:103],v[44:47],v[104:107]
s_lshr_b32 s69,s51,5
v_exp_f32 v80,v80
v_mfma_f32_16x16x32_bf16 v[108:111],v[44:47],v[44:47],v[108:111]
s_cmp_lt_u32 s51,4194304
v_fma_f32 v80,v80,v99,v99
v_fma_f32 v81,v98,v81,v97
v_mfma_f32_16x16x32_bf16 v[28:31],a[136:139],v[48:51],v[28:31]
s_cselect_b32 s70,0xffffcfff,-1
ds_read_b128 v[44:47],v2 offset:24576
v_mul_f32 v81,v41,v81
v_mfma_f32_16x16x32_bf16 v[32:35],a[140:143],v[48:51],v[32:35]
s_and_b32 s70,s70,s69
v_exp_f32 v81,v81
v_mfma_f32_16x16x32_bf16 v[104:107],v[100:103],v[48:51],v[104:107]
s_cmp_lt_u32 s51,s89
v_rcp_f32 v80,v80
v_mfma_f32_16x16x32_bf16 v[108:111],v[48:51],v[48:51],v[108:111]
s_cselect_b32 s70,s59,s70
v_fma_f32 v81,v81,v99,v99
v_fma_f32 v82,v98,v82,v97
v_mfma_f32_16x16x32_bf16 v[28:31],a[144:147],v[52:55],v[28:31]
s_add_u32 s53,s52,s55
ds_read_b128 v[48:51],v2 offset:25600
v_mul_f32 v82,v42,v82
v_mfma_f32_16x16x32_bf16 v[32:35],a[148:151],v[52:55],v[32:35]
s_cmp_eq_u32 s53,s56
v_exp_f32 v82,v82
v_mfma_f32_16x16x32_bf16 v[104:107],v[100:103],v[52:55],v[104:107]
s_cselect_b32 s53,s57,s53
v_rcp_f32 v81,v81
v_mfma_f32_16x16x32_bf16 v[108:111],v[52:55],v[52:55],v[108:111]
s_cmp_eq_u32 s53,s58
v_fma_f32 v82,v82,v99,v99
v_fma_f32 v83,v98,v83,v97
v_mfma_f32_16x16x32_bf16 v[28:31],a[152:155],v[56:59],v[28:31]
s_cselect_b32 s53,s59,s53
ds_read_b128 v[52:55],v2 offset:26624
v_mul_f32 v83,v43,v83
v_mfma_f32_16x16x32_bf16 v[32:35],a[156:159],v[56:59],v[32:35]
s_xor_b32 s63,s63,65536
v_exp_f32 v83,v83
v_mfma_f32_16x16x32_bf16 v[104:107],v[100:103],v[56:59],v[104:107]
s_cmp_ge_u32 s67,s59
v_rcp_f32 v82,v82
v_mfma_f32_16x16x32_bf16 v[108:111],v[56:59],v[56:59],v[108:111]
s_waitcnt lgkmcnt(3)
v_fma_f32 v83,v83,v99,v99
v_mul_f32 v40,v40,v80
v_mfma_f32_16x16x32_bf16 v[28:31],a[160:163],v[60:63],v[28:31]
s_cselect_b32 s68,s68,s70
ds_read_b128 v[56:59],v2 offset:27648
v_mul_f32 v41,v41,v81
v_mfma_f32_16x16x32_bf16 v[32:35],a[164:167],v[60:63],v[32:35]
s_add_u32 m0,s78,256
v_rcp_f32 v83,v83
v_mfma_f32_16x16x32_bf16 v[28:31],a[168:171],v[64:67],v[28:31]
buffer_load_dword v6,s[36:39],s68 offen offset:0 sc1 lds
v_cndmask_b32 v112,v108,v109,s[82:83]
v_cndmask_b32 v113,v110,v111,s[82:83]
v_cndmask_b32 v112,v112,v113,s[84:85]
v_cndmask_b32 v112,v104,v112,s[98:99]
v_mfma_f32_16x16x32_bf16 v[32:35],a[172:175],v[64:67],v[32:35]
s_mov_b32 m0,s62
ds_read_b128 v[60:63],v2 offset:28672
ds_write_b32 v13,v112 offset:0
v_mfma_f32_16x16x32_bf16 v[28:31],a[176:179],v[68:71],v[28:31]
buffer_load_dwordx4 v0,s[36:39],s67 offen offset:0 sc1 lds
v_mfma_f32_16x16x32_bf16 v[32:35],a[180:183],v[68:71],v[32:35]
s_cmp_ge_u32 s66,s59
ds_read_b128 v[64:67],v2 offset:29696
v_mul_f32 v42,v42,v82
v_mfma_f32_16x16x32_bf16 v[28:31],a[184:187],v[72:75],v[28:31]
ds_read_b128 v[68:71],v2 offset:30720
v_mul_f32 v43,v43,v83
v_mfma_f32_16x16x32_bf16 v[32:35],a[188:191],v[72:75],v[32:35])"
R"(
s_waitcnt lgkmcnt(4)
v_cvt_pk_bf16_f32 v86,v40,v41
v_cvt_pk_bf16_f32 v87,v42,v43
v_mfma_f32_16x16x32_bf16 v[28:31],a[192:195],v[44:47],v[28:31]
s_waitcnt vmcnt(7) lgkmcnt(2)
ds_read_b128 v[72:75],v2 offset:31744
v_mfma_f32_16x16x32_bf16 v[32:35],a[196:199],v[44:47],v[32:35]
s_barrier
v_mfma_f32_16x16x32_bf16 v[28:31],a[200:203],v[48:51],v[28:31]
buffer_load_dwordx4 v0,s[36:39],s67 offen offset:1024 sc1 lds
v_mfma_f32_16x16x32_bf16 v[32:35],a[204:207],v[48:51],v[32:35]
ds_read_b128 v[44:47],v2 offset:32768
ds_read_b128 v[116:119],v14 offset:0
v_mfma_f32_16x16x32_bf16 v[28:31],a[208:211],v[52:55],v[28:31]
ds_read_b128 v[120:123],v14 offset:16
ds_read_b128 v[48:51],v2 offset:33792
v_mfma_f32_16x16x32_bf16 v[32:35],a[212:215],v[52:55],v[32:35]
v_mfma_f32_16x16x32_bf16 v[28:31],a[216:219],v[56:59],v[28:31]
buffer_load_dwordx4 v0,s[36:39],s67 offen offset:2048 sc1 lds
v_mfma_f32_16x16x32_bf16 v[32:35],a[220:223],v[56:59],v[32:35]
s_waitcnt lgkmcnt(4)
ds_read_b128 v[52:55],v2 offset:34816
v_mfma_f32_16x16x32_bf16 v[28:31],a[224:227],v[60:63],v[28:31]
s_waitcnt lgkmcnt(2)
ds_read_b128 v[56:59],v2 offset:35840
v_add_f32 v116,v116,v117
v_mfma_f32_16x16x32_bf16 v[32:35],a[228:231],v[60:63],v[32:35]
v_add_f32 v118,v118,v119
v_add_f32 v116,v116,v118
v_mfma_f32_16x16x32_bf16 v[28:31],a[232:235],v[64:67],v[28:31]
buffer_load_dwordx4 v0,s[36:39],s67 offen offset:3072 sc1 lds
v_mfma_f32_16x16x32_bf16 v[32:35],a[236:239],v[64:67],v[32:35]
ds_read_b128 v[60:63],v2 offset:36864
v_mul_f32 v92,s75,v116
v_mfma_f32_16x16x32_bf16 v[28:31],a[240:243],v[68:71],v[28:31]
ds_read_b128 v[64:67],v2 offset:37888
v_add_f32 v120,v120,v121
v_mfma_f32_16x16x32_bf16 v[32:35],a[244:247],v[68:71],v[32:35]
v_add_f32 v122,v122,v123
v_add_f32 v120,v120,v122
v_mul_f32 v120,s75,v120
v_fma_f32 v120,-v92,v92,v120
v_max_f32 v120,0,v120
v_add_f32 v115,v15,v120
v_fma_f32 v120,v15,v115,v120
v_rsq_f32 v116,v120
v_mfma_f32_16x16x32_bf16 v[28:31],a[248:251],v[72:75],v[28:31]
s_waitcnt lgkmcnt(2)
ds_read_b128 v[68:71],v2 offset:38912
buffer_store_dwordx4 v[84:87],v3,s[36:39],s73 offen offset:0 sc1
v_mul_f32 v93,s76,v116
v_mul_f32 v94,v116,v116
v_mul_f32 v95,v120,v116
v_mul_f32 v94,s77,v94
v_mul_f32 v94,v94,v93
v_mfma_f32_16x16x32_bf16 v[32:35],a[252:255],v[72:75],v[32:35]
s_cbranch_scc1 Lg1_slow1_%=
Lg1_a1w0_%=:
v_mfma_f32_16x16x32_bf16 v[36:39],a[0:3],v[44:47],0
s_add_u32 m0,s62,16384
ds_read_b128 v[72:75],v2 offset:39936
s_sub_u32 s69,s67,1048576
v_mfma_f32_16x16x32_bf16 v[40:43],a[4:7],v[44:47],0
buffer_load_dwordx4 v1,s[36:39],s67 offen offset:0 sc1 lds
v_mfma_f32_16x16x32_bf16 v[104:107],v[100:103],v[44:47],0
s_cmp_lt_u32 s69,2097152
s_cselect_b32 s89,3145728,s89
s_sub_u32 s69,s67,3145728
v_mfma_f32_16x16x32_bf16 v[108:111],v[44:47],v[44:47],0
s_cmp_lt_u32 s69,1048576
v_fma_f32 v28,-v92,v20,v28
v_fma_f32 v29,-v92,v21,v29
v_mfma_f32_16x16x32_bf16 v[36:39],a[8:11],v[48:51],v[36:39]
s_cselect_b32 s89,4194304,s89
ds_read_b128 v[44:47],v2 offset:40960
v_fma_f32 v30,-v92,v22,v30
v_mfma_f32_16x16x32_bf16 v[40:43],a[12:15],v[48:51],v[40:43]
buffer_load_dwordx4 v1,s[36:39],s67 offen offset:1024 sc1 lds
v_mfma_f32_16x16x32_bf16 v[104:107],v[100:103],v[48:51],v[104:107]
s_lshr_b32 s73,s48,0
v_fma_f32 v31,-v92,v23,v31
v_mul_f32 v76,v28,v28
v_mfma_f32_16x16x32_bf16 v[108:111],v[48:51],v[48:51],v[108:111]
s_lshr_b32 s72,s67,6
v_mul_f32 v77,v29,v29
v_mul_f32 v78,v30,v30
v_mfma_f32_16x16x32_bf16 v[36:39],a[16:19],v[52:55],v[36:39]
s_lshr_b32 s71,s62,2
ds_read_b128 v[48:51],v2 offset:41984
v_mul_f32 v79,v31,v31
v_mfma_f32_16x16x32_bf16 v[40:43],a[20:23],v[52:55],v[40:43]
buffer_load_dwordx4 v1,s[36:39],s67 offen offset:2048 sc1 lds
v_mfma_f32_16x16x32_bf16 v[104:107],v[100:103],v[52:55],v[104:107]
s_cmp_lt_u32 s67,4194304
v_fma_f32 v76,v94,v76,v93
v_mul_f32 v76,v28,v76
v_mfma_f32_16x16x32_bf16 v[108:111],v[52:55],v[52:55],v[108:111]
s_cselect_b32 s72,s59,s72
v_exp_f32 v76,v76
v_mfma_f32_16x16x32_bf16 v[36:39],a[24:27],v[56:59],v[36:39]
s_mov_b64 s[44:45],s[46:47]
ds_read_b128 v[52:55],v2 offset:43008
v_fma_f32 v76,v76,v95,v95
v_mfma_f32_16x16x32_bf16 v[40:43],a[28:31],v[56:59],v[40:43]
buffer_load_dwordx4 v1,s[36:39],s67 offen offset:3072 sc1 lds
v_mfma_f32_16x16x32_bf16 v[104:107],v[100:103],v[56:59],v[104:107]
s_add_u32 m0,s71,s88
v_rcp_f32 v76,v76
v_mfma_f32_16x16x32_bf16 v[108:111],v[56:59],v[56:59],v[108:111]
s_waitcnt lgkmcnt(3)
buffer_load_dword v10,s[36:39],s72 offen offset:0 sc1 lds
v_mfma_f32_16x16x32_bf16 v[36:39],a[32:35],v[60:63],v[36:39]
s_xor_b32 s87,s63,65536
ds_read_b128 v[56:59],v2 offset:44032
v_fma_f32 v77,v94,v77,v93
v_mfma_f32_16x16x32_bf16 v[40:43],a[36:39],v[60:63],v[40:43]
s_cmp_ge_u32 s67,s59
v_mul_f32 v77,v29,v77
v_fma_f32 v78,v94,v78,v93
v_mfma_f32_16x16x32_bf16 v[36:39],a[40:43],v[64:67],v[36:39]
s_cselect_b32 s86,s50,s51
ds_read_b128 v[60:63],v2 offset:45056
v_mul_f32 v78,v30,v78
v_mfma_f32_16x16x32_bf16 v[40:43],a[44:47],v[64:67],v[40:43]
s_cselect_b32 s87,s87,s64
v_exp_f32 v77,v77
v_mfma_f32_16x16x32_bf16 v[36:39],a[48:51],v[68:71],v[36:39]
s_mov_b32 s62,s87
ds_read_b128 v[64:67],v2 offset:46080
v_fma_f32 v77,v77,v95,v95
v_mfma_f32_16x16x32_bf16 v[40:43],a[52:55],v[68:71],v[40:43]
s_cmp_lt_u32 s86,s89
v_exp_f32 v78,v78
v_mfma_f32_16x16x32_bf16 v[36:39],a[56:59],v[72:75],v[36:39]
s_mov_b64 s[46:47],s[48:49]
ds_read_b128 v[68:71],v2 offset:47104
v_fma_f32 v78,v78,v95,v95
v_mfma_f32_16x16x32_bf16 v[40:43],a[60:63],v[72:75],v[40:43]
s_waitcnt lgkmcnt(3)
v_rcp_f32 v77,v77
v_mfma_f32_16x16x32_bf16 v[36:39],a[64:67],v[44:47],v[36:39]
s_waitcnt vmcnt(10)
ds_read_b128 v[72:75],v2 offset:48128
v_fma_f32 v79,v94,v79,v93
v_mfma_f32_16x16x32_bf16 v[40:43],a[68:71],v[44:47],v[40:43]
s_barrier
v_mfma_f32_16x16x32_bf16 v[36:39],a[72:75],v[48:51],v[36:39]
s_mov_b64 s[48:49],s[50:51]
ds_read_b128 v[44:47],v2 offset:49152
ds_read_b32 v16,v7 offset:256
v_mfma_f32_16x16x32_bf16 v[40:43],a[76:79],v[48:51],v[40:43]
v_rcp_f32 v78,v78
v_mfma_f32_16x16x32_bf16 v[36:39],a[80:83],v[52:55],v[36:39]
ds_read_b128 v[48:51],v2 offset:50176
v_mul_f32 v79,v31,v79
v_mfma_f32_16x16x32_bf16 v[40:43],a[84:87],v[52:55],v[40:43]
v_exp_f32 v79,v79
v_mfma_f32_16x16x32_bf16 v[36:39],a[88:91],v[56:59],v[36:39]
s_waitcnt lgkmcnt(1)
ds_read_b128 v[52:55],v2 offset:51200
v_fma_f32 v79,v79,v95,v95
v_mfma_f32_16x16x32_bf16 v[40:43],a[92:95],v[56:59],v[40:43]
v_cmp_eq_u32 vcc,s60,v16
v_rcp_f32 v79,v79
v_mul_f32 v28,v28,v76
v_mfma_f32_16x16x32_bf16 v[36:39],a[96:99],v[60:63],v[36:39]
s_cselect_b32 vcc_lo,-1,vcc_lo
ds_read_b128 v[56:59],v2 offset:52224
v_mul_f32 v29,v29,v77
v_mfma_f32_16x16x32_bf16 v[40:43],a[100:103],v[60:63],v[40:43]
s_cmp_eq_u32 vcc_lo,-1
v_mul_f32 v30,v30,v78
v_mul_f32 v31,v31,v79
v_mfma_f32_16x16x32_bf16 v[36:39],a[104:107],v[64:67],v[36:39]
s_cselect_b32 s66,s86,s59
ds_read_b128 v[60:63],v2 offset:53248
v_cvt_pk_bf16_f32 v84,v28,v29
v_mfma_f32_16x16x32_bf16 v[40:43],a[108:111],v[64:67],v[40:43]
s_lshr_b32 s69,s51,5
v_cvt_pk_bf16_f32 v85,v30,v31
v_fma_f32 v32,-v92,v24,v32
v_mfma_f32_16x16x32_bf16 v[36:39],a[112:115],v[68:71],v[36:39]
s_cmp_lt_u32 s51,4194304
ds_read_b128 v[64:67],v2 offset:54272
v_fma_f32 v33,-v92,v25,v33
v_mfma_f32_16x16x32_bf16 v[40:43],a[116:119],v[68:71],v[40:43]
s_cselect_b32 s68,0xffffcfff,-1
v_fma_f32 v34,-v92,v26,v34
v_fma_f32 v35,-v92,v27,v35
v_mfma_f32_16x16x32_bf16 v[36:39],a[120:123],v[72:75],v[36:39]
s_and_b32 s68,s68,s69
ds_read_b128 v[68:71],v2 offset:55296
v_mul_f32 v80,v32,v32
v_mfma_f32_16x16x32_bf16 v[40:43],a[124:127],v[72:75],v[40:43]
s_waitcnt lgkmcnt(3)
v_mul_f32 v81,v33,v33
v_mul_f32 v82,v34,v34
v_mfma_f32_16x16x32_bf16 v[36:39],a[128:131],v[44:47],v[36:39]
s_cmp_lt_u32 s51,s89
ds_read_b128 v[72:75],v2 offset:56320)"
R"(
v_mul_f32 v83,v35,v35
v_mfma_f32_16x16x32_bf16 v[40:43],a[132:135],v[44:47],v[40:43]
s_cselect_b32 s68,s59,s68
v_fma_f32 v80,v94,v80,v93
v_mul_f32 v80,v32,v80
v_mfma_f32_16x16x32_bf16 v[104:107],v[100:103],v[44:47],v[104:107]
s_lshr_b32 s69,s52,5
v_exp_f32 v80,v80
v_mfma_f32_16x16x32_bf16 v[108:111],v[44:47],v[44:47],v[108:111]
s_cmp_lt_u32 s52,4194304
v_fma_f32 v80,v80,v95,v95
v_fma_f32 v81,v94,v81,v93
v_mfma_f32_16x16x32_bf16 v[36:39],a[136:139],v[48:51],v[36:39]
s_cselect_b32 s70,0xffffcfff,-1
ds_read_b128 v[44:47],v2 offset:57344
v_mul_f32 v81,v33,v81
v_mfma_f32_16x16x32_bf16 v[40:43],a[140:143],v[48:51],v[40:43]
s_and_b32 s70,s70,s69
v_exp_f32 v81,v81
v_mfma_f32_16x16x32_bf16 v[104:107],v[100:103],v[48:51],v[104:107]
s_cmp_lt_u32 s52,s89
v_rcp_f32 v80,v80
v_mfma_f32_16x16x32_bf16 v[108:111],v[48:51],v[48:51],v[108:111]
s_cselect_b32 s70,s59,s70
v_fma_f32 v81,v81,v95,v95
v_fma_f32 v82,v94,v82,v93
v_mfma_f32_16x16x32_bf16 v[36:39],a[144:147],v[52:55],v[36:39]
s_add_u32 s74,s53,s55
ds_read_b128 v[48:51],v2 offset:58368
v_mul_f32 v82,v34,v82
v_mfma_f32_16x16x32_bf16 v[40:43],a[148:151],v[52:55],v[40:43]
s_cmp_eq_u32 s74,s56
v_exp_f32 v82,v82
v_mfma_f32_16x16x32_bf16 v[104:107],v[100:103],v[52:55],v[104:107]
s_cselect_b32 s74,s57,s74
v_rcp_f32 v81,v81
v_mfma_f32_16x16x32_bf16 v[108:111],v[52:55],v[52:55],v[108:111]
s_cmp_eq_u32 s74,s58
v_fma_f32 v82,v82,v95,v95
v_fma_f32 v83,v94,v83,v93
v_mfma_f32_16x16x32_bf16 v[36:39],a[152:155],v[56:59],v[36:39]
s_cselect_b32 s74,s59,s74
ds_read_b128 v[52:55],v2 offset:59392
v_mul_f32 v83,v35,v83
v_mfma_f32_16x16x32_bf16 v[40:43],a[156:159],v[56:59],v[40:43]
s_xor_b32 s64,s64,65536
v_exp_f32 v83,v83
v_mfma_f32_16x16x32_bf16 v[104:107],v[100:103],v[56:59],v[104:107]
s_cmp_ge_u32 s66,s59
v_rcp_f32 v82,v82
v_mfma_f32_16x16x32_bf16 v[108:111],v[56:59],v[56:59],v[108:111]
s_waitcnt lgkmcnt(3)
v_fma_f32 v83,v83,v95,v95
v_mul_f32 v32,v32,v80
v_mfma_f32_16x16x32_bf16 v[36:39],a[160:163],v[60:63],v[36:39]
s_cselect_b32 s68,s68,s70
ds_read_b128 v[56:59],v2 offset:60416
v_mul_f32 v33,v33,v81
v_mfma_f32_16x16x32_bf16 v[40:43],a[164:167],v[60:63],v[40:43]
s_add_u32 m0,s78,0
v_rcp_f32 v83,v83
v_mfma_f32_16x16x32_bf16 v[36:39],a[168:171],v[64:67],v[36:39]
buffer_load_dword v6,s[36:39],s68 offen offset:0 sc1 lds
v_cndmask_b32 v112,v108,v109,s[82:83]
v_cndmask_b32 v113,v110,v111,s[82:83]
v_cndmask_b32 v112,v112,v113,s[84:85]
v_cndmask_b32 v112,v104,v112,s[98:99]
v_mfma_f32_16x16x32_bf16 v[40:43],a[172:175],v[64:67],v[40:43]
s_mov_b32 m0,s62
ds_read_b128 v[60:63],v2 offset:61440
ds_write_b32 v13,v112 offset:2048
v_mfma_f32_16x16x32_bf16 v[36:39],a[176:179],v[68:71],v[36:39]
buffer_load_dwordx4 v0,s[36:39],s66 offen offset:0 sc1 lds
v_mfma_f32_16x16x32_bf16 v[40:43],a[180:183],v[68:71],v[40:43]
s_mov_b64 s[50:51],s[52:53]
ds_read_b128 v[64:67],v2 offset:62464
v_mul_f32 v34,v34,v82
v_mfma_f32_16x16x32_bf16 v[36:39],a[184:187],v[72:75],v[36:39]
s_mov_b32 s52,s74
ds_read_b128 v[68:71],v2 offset:63488
v_mul_f32 v35,v35,v83
v_mfma_f32_16x16x32_bf16 v[40:43],a[188:191],v[72:75],v[40:43]
s_waitcnt lgkmcnt(4)
v_cvt_pk_bf16_f32 v86,v32,v33
v_cvt_pk_bf16_f32 v87,v34,v35
v_mfma_f32_16x16x32_bf16 v[36:39],a[192:195],v[44:47],v[36:39]
s_waitcnt vmcnt(7) lgkmcnt(2)
ds_read_b128 v[72:75],v2 offset:64512
v_xor_b32 v2,65536,v2
v_mfma_f32_16x16x32_bf16 v[40:43],a[196:199],v[44:47],v[40:43]
s_barrier
v_mfma_f32_16x16x32_bf16 v[36:39],a[200:203],v[48:51],v[36:39]
buffer_load_dwordx4 v0,s[36:39],s66 offen offset:1024 sc1 lds
v_mfma_f32_16x16x32_bf16 v[40:43],a[204:207],v[48:51],v[40:43]
s_cmp_lt_u32 s67,4194304
ds_read_b128 v[44:47],v2 offset:0
ds_read_b128 v[116:119],v14 offset:2048
v_mfma_f32_16x16x32_bf16 v[36:39],a[208:211],v[52:55],v[36:39]
ds_read_b128 v[120:123],v14 offset:2064
ds_read_b128 v[48:51],v2 offset:1024
v_mfma_f32_16x16x32_bf16 v[40:43],a[212:215],v[52:55],v[40:43]
v_xor_b32 v8,16384,v8
v_mfma_f32_16x16x32_bf16 v[36:39],a[216:219],v[56:59],v[36:39]
buffer_load_dwordx4 v0,s[36:39],s66 offen offset:2048 sc1 lds
v_mfma_f32_16x16x32_bf16 v[40:43],a[220:223],v[56:59],v[40:43]
s_waitcnt lgkmcnt(4)
ds_read_b128 v[52:55],v2 offset:2048
v_mfma_f32_16x16x32_bf16 v[36:39],a[224:227],v[60:63],v[36:39]
s_waitcnt lgkmcnt(2)
ds_read_b128 v[56:59],v2 offset:3072
v_add_f32 v116,v116,v117
v_mfma_f32_16x16x32_bf16 v[40:43],a[228:231],v[60:63],v[40:43]
v_add_f32 v118,v118,v119
v_add_f32 v116,v116,v118
v_mfma_f32_16x16x32_bf16 v[36:39],a[232:235],v[64:67],v[36:39]
buffer_load_dwordx4 v0,s[36:39],s66 offen offset:3072 sc1 lds
v_mfma_f32_16x16x32_bf16 v[40:43],a[236:239],v[64:67],v[40:43]
ds_read_b128 v[60:63],v2 offset:4096
v_mul_f32 v96,s75,v116
v_mfma_f32_16x16x32_bf16 v[36:39],a[240:243],v[68:71],v[36:39]
ds_read_b128 v[64:67],v2 offset:5120
v_add_f32 v120,v120,v121
v_mfma_f32_16x16x32_bf16 v[40:43],a[244:247],v[68:71],v[40:43]
v_add_f32 v122,v122,v123
v_add_f32 v120,v120,v122
v_mul_f32 v120,s75,v120
v_fma_f32 v120,-v96,v96,v120
v_max_f32 v120,0,v120
v_add_f32 v115,v15,v120
v_fma_f32 v120,v15,v115,v120
v_rsq_f32 v116,v120
v_mfma_f32_16x16x32_bf16 v[36:39],a[248:251],v[72:75],v[36:39]
s_waitcnt lgkmcnt(2)
ds_read_b128 v[68:71],v2 offset:6144
buffer_store_dwordx4 v[84:87],v3,s[36:39],s73 offen offset:0 sc1
v_mul_f32 v97,s76,v116
v_mul_f32 v98,v116,v116
v_mul_f32 v99,v120,v116
v_mul_f32 v98,s77,v98
v_mul_f32 v98,v98,v97
v_mfma_f32_16x16x32_bf16 v[40:43],a[252:255],v[72:75],v[40:43]
s_cbranch_scc1 Lg1_a0w0_%=
s_cmp_ge_u32 s48,4194304
s_cselect_b64 s[90:91],s[94:95],s[90:91]
s_cselect_b64 s[92:93],s[96:97],s[92:93]
s_cmp_lt_u32 s67,s59
s_cbranch_scc1 Lg1_d0w0_%=
s_branch Lg1_slow0_%=
Lg1_a0w1_%=:
v_mfma_f32_16x16x32_bf16 v[28:31],a[32:35],v[44:47],0
s_add_u32 m0,s62,16384
ds_read_b128 v[72:75],v2 offset:3072
s_sub_u32 s69,s66,1048576
v_mfma_f32_16x16x32_bf16 v[32:35],a[36:39],v[44:47],0
s_cmp_lt_u32 s69,2097152
s_cselect_b32 s89,3145728,s89
s_sub_u32 s69,s66,3145728
v_mfma_f32_16x16x32_bf16 v[104:107],v[100:103],v[44:47],0
buffer_load_dwordx4 v1,s[36:39],s66 offen offset:0 sc1 lds
v_mfma_f32_16x16x32_bf16 v[108:111],v[44:47],v[44:47],0
s_cmp_lt_u32 s69,1048576
v_fma_f32 v36,-v96,v20,v36
v_fma_f32 v37,-v96,v21,v37
v_mfma_f32_16x16x32_bf16 v[28:31],a[40:43],v[48:51],v[28:31]
s_cselect_b32 s89,4194304,s89
ds_read_b128 v[44:47],v2 offset:12288
v_fma_f32 v38,-v96,v22,v38
v_mfma_f32_16x16x32_bf16 v[32:35],a[44:47],v[48:51],v[32:35]
s_lshr_b32 s73,s47,0
v_fma_f32 v39,-v96,v23,v39
v_mul_f32 v76,v36,v36
v_mfma_f32_16x16x32_bf16 v[104:107],v[100:103],v[48:51],v[104:107]
buffer_load_dwordx4 v1,s[36:39],s66 offen offset:1024 sc1 lds
v_mfma_f32_16x16x32_bf16 v[108:111],v[48:51],v[48:51],v[108:111]
s_lshr_b32 s72,s66,6
v_mul_f32 v77,v37,v37
v_mul_f32 v78,v38,v38
v_mfma_f32_16x16x32_bf16 v[28:31],a[48:51],v[52:55],v[28:31]
s_lshr_b32 s71,s62,2
ds_read_b128 v[48:51],v2 offset:13312
v_mul_f32 v79,v39,v39
v_mfma_f32_16x16x32_bf16 v[32:35],a[52:55],v[52:55],v[32:35]
s_cmp_lt_u32 s66,4194304
v_fma_f32 v76,v98,v76,v97
v_mul_f32 v76,v36,v76
v_mfma_f32_16x16x32_bf16 v[104:107],v[100:103],v[52:55],v[104:107]
buffer_load_dwordx4 v1,s[36:39],s66 offen offset:2048 sc1 lds
v_mfma_f32_16x16x32_bf16 v[108:111],v[52:55],v[52:55],v[108:111]
s_cselect_b32 s72,s59,s72
v_exp_f32 v76,v76
v_mfma_f32_16x16x32_bf16 v[28:31],a[56:59],v[56:59],v[28:31]
ds_read_b128 v[52:55],v2 offset:14336
v_fma_f32 v76,v76,v99,v99
v_mfma_f32_16x16x32_bf16 v[32:35],a[60:63],v[56:59],v[32:35]
v_rcp_f32 v76,v76
v_mfma_f32_16x16x32_bf16 v[104:107],v[100:103],v[56:59],v[104:107]
buffer_load_dwordx4 v1,s[36:39],s66 offen offset:3072 sc1 lds
v_mfma_f32_16x16x32_bf16 v[108:111],v[56:59],v[56:59],v[108:111]
s_waitcnt lgkmcnt(3)
v_fma_f32 v77,v98,v77,v97)"
R"(
v_mul_f32 v77,v37,v77
v_mfma_f32_16x16x32_bf16 v[28:31],a[0:3],v[60:63],v[28:31]
s_add_u32 m0,s71,s88
ds_read_b128 v[56:59],v2 offset:15360
v_fma_f32 v78,v98,v78,v97
v_mfma_f32_16x16x32_bf16 v[32:35],a[4:7],v[60:63],v[32:35]
buffer_load_dword v10,s[36:39],s72 offen offset:0 sc1 lds
v_mfma_f32_16x16x32_bf16 v[28:31],a[8:11],v[64:67],v[28:31]
s_xor_b32 s87,s64,65536
ds_read_b128 v[60:63],v2 offset:8192
v_mul_f32 v78,v38,v78
v_mfma_f32_16x16x32_bf16 v[32:35],a[12:15],v[64:67],v[32:35]
s_cmp_ge_u32 s66,s59
v_exp_f32 v77,v77
v_mfma_f32_16x16x32_bf16 v[28:31],a[16:19],v[68:71],v[28:31]
s_cselect_b32 s86,s49,s50
ds_read_b128 v[64:67],v2 offset:9216
v_fma_f32 v77,v77,v99,v99
v_mfma_f32_16x16x32_bf16 v[32:35],a[20:23],v[68:71],v[32:35]
s_cselect_b32 s87,s87,s63
v_exp_f32 v78,v78
v_mfma_f32_16x16x32_bf16 v[28:31],a[24:27],v[72:75],v[28:31]
s_mov_b32 s62,s87
ds_read_b128 v[68:71],v2 offset:10240
v_fma_f32 v78,v78,v99,v99
v_mfma_f32_16x16x32_bf16 v[32:35],a[28:31],v[72:75],v[32:35]
s_waitcnt lgkmcnt(3)
v_rcp_f32 v77,v77
v_mfma_f32_16x16x32_bf16 v[28:31],a[96:99],v[44:47],v[28:31]
s_waitcnt vmcnt(10)
ds_read_b128 v[72:75],v2 offset:11264
v_fma_f32 v79,v98,v79,v97
v_mfma_f32_16x16x32_bf16 v[32:35],a[100:103],v[44:47],v[32:35]
s_barrier
v_mfma_f32_16x16x32_bf16 v[28:31],a[104:107],v[48:51],v[28:31]
s_cmp_lt_u32 s86,s89
ds_read_b128 v[44:47],v2 offset:20480
ds_read_b32 v16,v7 offset:0
v_mfma_f32_16x16x32_bf16 v[32:35],a[108:111],v[48:51],v[32:35]
s_mov_b32 m0,s62
v_rcp_f32 v78,v78
v_mfma_f32_16x16x32_bf16 v[28:31],a[112:115],v[52:55],v[28:31]
ds_read_b128 v[48:51],v2 offset:21504
v_mul_f32 v79,v39,v79
v_mfma_f32_16x16x32_bf16 v[32:35],a[116:119],v[52:55],v[32:35]
v_exp_f32 v79,v79
v_mfma_f32_16x16x32_bf16 v[28:31],a[120:123],v[56:59],v[28:31]
s_waitcnt lgkmcnt(1)
ds_read_b128 v[52:55],v2 offset:22528
v_fma_f32 v79,v79,v99,v99
v_mfma_f32_16x16x32_bf16 v[32:35],a[124:127],v[56:59],v[32:35]
v_cmp_eq_u32 vcc,s60,v16
v_rcp_f32 v79,v79
v_mul_f32 v36,v36,v76
v_mfma_f32_16x16x32_bf16 v[28:31],a[64:67],v[60:63],v[28:31]
s_cselect_b32 vcc_lo,-1,vcc_lo
ds_read_b128 v[56:59],v2 offset:23552
v_mul_f32 v37,v37,v77
v_mfma_f32_16x16x32_bf16 v[32:35],a[68:71],v[60:63],v[32:35]
s_cmp_eq_u32 vcc_lo,-1
v_mul_f32 v38,v38,v78
v_mul_f32 v39,v39,v79
v_mfma_f32_16x16x32_bf16 v[28:31],a[72:75],v[64:67],v[28:31]
s_cselect_b32 s67,s86,s59
ds_read_b128 v[60:63],v2 offset:16384
v_cvt_pk_bf16_f32 v84,v36,v37
v_mfma_f32_16x16x32_bf16 v[32:35],a[76:79],v[64:67],v[32:35]
s_add_u32 s53,s52,s55
v_cvt_pk_bf16_f32 v85,v38,v39
v_fma_f32 v40,-v96,v24,v40
v_mfma_f32_16x16x32_bf16 v[28:31],a[80:83],v[68:71],v[28:31]
s_cmp_eq_u32 s53,s56
ds_read_b128 v[64:67],v2 offset:17408
v_fma_f32 v41,-v96,v25,v41
v_mfma_f32_16x16x32_bf16 v[32:35],a[84:87],v[68:71],v[32:35]
s_cselect_b32 s53,s57,s53
v_fma_f32 v42,-v96,v26,v42
v_fma_f32 v43,-v96,v27,v43
v_mfma_f32_16x16x32_bf16 v[28:31],a[88:91],v[72:75],v[28:31]
s_cmp_eq_u32 s53,s58
ds_read_b128 v[68:71],v2 offset:18432
v_mul_f32 v80,v40,v40
v_mfma_f32_16x16x32_bf16 v[32:35],a[92:95],v[72:75],v[32:35]
s_waitcnt lgkmcnt(3)
v_mul_f32 v81,v41,v41
v_mul_f32 v82,v42,v42
v_mfma_f32_16x16x32_bf16 v[28:31],a[160:163],v[44:47],v[28:31]
s_cselect_b32 s53,s59,s53
ds_read_b128 v[72:75],v2 offset:19456
v_mul_f32 v83,v43,v43
v_mfma_f32_16x16x32_bf16 v[32:35],a[164:167],v[44:47],v[32:35]
s_xor_b32 s63,s63,65536
v_fma_f32 v80,v98,v80,v97
v_mul_f32 v80,v40,v80
v_mfma_f32_16x16x32_bf16 v[104:107],v[100:103],v[44:47],v[104:107]
s_cmp_ge_u32 s66,s59
v_exp_f32 v80,v80
v_mfma_f32_16x16x32_bf16 v[108:111],v[44:47],v[44:47],v[108:111]
v_fma_f32 v80,v80,v99,v99
v_fma_f32 v81,v98,v81,v97
v_mfma_f32_16x16x32_bf16 v[28:31],a[168:171],v[48:51],v[28:31]
ds_read_b128 v[44:47],v2 offset:28672
v_mul_f32 v81,v41,v81
v_mfma_f32_16x16x32_bf16 v[32:35],a[172:175],v[48:51],v[32:35]
v_exp_f32 v81,v81
v_mfma_f32_16x16x32_bf16 v[104:107],v[100:103],v[48:51],v[104:107]
v_rcp_f32 v80,v80
v_mfma_f32_16x16x32_bf16 v[108:111],v[48:51],v[48:51],v[108:111]
v_fma_f32 v81,v81,v99,v99
v_fma_f32 v82,v98,v82,v97
v_mfma_f32_16x16x32_bf16 v[28:31],a[176:179],v[52:55],v[28:31]
ds_read_b128 v[48:51],v2 offset:29696
v_mul_f32 v82,v42,v82
v_mfma_f32_16x16x32_bf16 v[32:35],a[180:183],v[52:55],v[32:35]
v_exp_f32 v82,v82
v_mfma_f32_16x16x32_bf16 v[104:107],v[100:103],v[52:55],v[104:107]
v_rcp_f32 v81,v81
v_mfma_f32_16x16x32_bf16 v[108:111],v[52:55],v[52:55],v[108:111]
v_fma_f32 v82,v82,v99,v99
v_fma_f32 v83,v98,v83,v97
v_mfma_f32_16x16x32_bf16 v[28:31],a[184:187],v[56:59],v[28:31]
ds_read_b128 v[52:55],v2 offset:30720
v_mul_f32 v83,v43,v83
v_mfma_f32_16x16x32_bf16 v[32:35],a[188:191],v[56:59],v[32:35]
v_exp_f32 v83,v83
v_mfma_f32_16x16x32_bf16 v[104:107],v[100:103],v[56:59],v[104:107]
v_rcp_f32 v82,v82
v_mfma_f32_16x16x32_bf16 v[108:111],v[56:59],v[56:59],v[108:111]
s_waitcnt lgkmcnt(3)
v_fma_f32 v83,v83,v99,v99
v_mul_f32 v40,v40,v80
v_mfma_f32_16x16x32_bf16 v[28:31],a[128:131],v[60:63],v[28:31]
ds_read_b128 v[56:59],v2 offset:31744
v_mul_f32 v41,v41,v81
v_mfma_f32_16x16x32_bf16 v[32:35],a[132:135],v[60:63],v[32:35]
buffer_load_dwordx4 v0,s[36:39],s67 offen offset:0 sc1 lds
v_mfma_f32_16x16x32_bf16 v[28:31],a[136:139],v[64:67],v[28:31]
v_cndmask_b32 v112,v108,v109,s[82:83]
v_cndmask_b32 v113,v110,v111,s[82:83]
v_cndmask_b32 v112,v112,v113,s[84:85]
v_cndmask_b32 v112,v104,v112,s[98:99]
v_mfma_f32_16x16x32_bf16 v[32:35],a[140:143],v[64:67],v[32:35]
ds_read_b128 v[60:63],v2 offset:24576
ds_write_b32 v13,v112 offset:0
v_mfma_f32_16x16x32_bf16 v[28:31],a[144:147],v[68:71],v[28:31]
ds_read_b128 v[64:67],v2 offset:25600
v_mul_f32 v42,v42,v82
v_mfma_f32_16x16x32_bf16 v[32:35],a[148:151],v[68:71],v[32:35]
buffer_load_dwordx4 v0,s[36:39],s67 offen offset:1024 sc1 lds
v_mfma_f32_16x16x32_bf16 v[28:31],a[152:155],v[72:75],v[28:31]
ds_read_b128 v[68:71],v2 offset:26624
v_cvt_pk_bf16_f32 v86,v40,v41
v_mfma_f32_16x16x32_bf16 v[32:35],a[156:159],v[72:75],v[32:35]
s_waitcnt lgkmcnt(4)
v_rcp_f32 v83,v83
v_mfma_f32_16x16x32_bf16 v[28:31],a[224:227],v[44:47],v[28:31]
s_waitcnt vmcnt(7) lgkmcnt(2)
ds_read_b128 v[72:75],v2 offset:27648
v_mul_f32 v43,v43,v83
v_mfma_f32_16x16x32_bf16 v[32:35],a[228:231],v[44:47],v[32:35]
s_barrier
v_mfma_f32_16x16x32_bf16 v[28:31],a[232:235],v[48:51],v[28:31]
ds_read_b128 v[116:119],v14 offset:0
ds_read_b128 v[120:123],v14 offset:16
v_mfma_f32_16x16x32_bf16 v[32:35],a[236:239],v[48:51],v[32:35]
ds_read_b128 v[44:47],v2 offset:36864
buffer_load_dwordx4 v0,s[36:39],s67 offen offset:2048 sc1 lds
v_mfma_f32_16x16x32_bf16 v[28:31],a[240:243],v[52:55],v[28:31]
ds_read_b128 v[48:51],v2 offset:37888
v_cvt_pk_bf16_f32 v87,v42,v43
v_mfma_f32_16x16x32_bf16 v[32:35],a[244:247],v[52:55],v[32:35]
v_mfma_f32_16x16x32_bf16 v[28:31],a[248:251],v[56:59],v[28:31]
s_waitcnt lgkmcnt(2)
ds_read_b128 v[52:55],v2 offset:38912
v_add_f32 v116,v116,v117
v_mfma_f32_16x16x32_bf16 v[32:35],a[252:255],v[56:59],v[32:35]
buffer_load_dwordx4 v0,s[36:39],s67 offen offset:3072 sc1 lds
v_mfma_f32_16x16x32_bf16 v[28:31],a[192:195],v[60:63],v[28:31]
ds_read_b128 v[56:59],v2 offset:39936
v_add_f32 v118,v118,v119
v_mfma_f32_16x16x32_bf16 v[32:35],a[196:199],v[60:63],v[32:35]
v_add_f32 v116,v116,v118
v_mul_f32 v92,s75,v116
v_mfma_f32_16x16x32_bf16 v[28:31],a[200:203],v[64:67],v[28:31]
ds_read_b128 v[60:63],v2 offset:32768
v_add_f32 v120,v120,v121
v_mfma_f32_16x16x32_bf16 v[32:35],a[204:207],v[64:67],v[32:35]
buffer_store_dwordx4 v[84:87],v3,s[36:39],s73 offen offset:0 sc1
v_add_f32 v122,v122,v123
v_add_f32 v120,v120,v122
v_mul_f32 v120,s75,v120
v_mfma_f32_16x16x32_bf16 v[28:31],a[208:211],v[68:71],v[28:31]
ds_read_b128 v[64:67],v2 offset:33792
v_fma_f32 v120,-v92,v92,v120
v_mfma_f32_16x16x32_bf16 v[32:35],a[212:215],v[68:71],v[32:35]
v_max_f32 v120,0,v120
v_add_f32 v115,v15,v120
v_fma_f32 v120,v15,v115,v120
v_rsq_f32 v116,v120
v_mfma_f32_16x16x32_bf16 v[28:31],a[216:219],v[72:75],v[28:31])"
R"(
s_waitcnt lgkmcnt(2)
ds_read_b128 v[68:71],v2 offset:34816
v_mul_f32 v93,s76,v116
v_mul_f32 v94,v116,v116
v_mul_f32 v95,v120,v116
v_mul_f32 v94,s77,v94
v_mul_f32 v94,v94,v93
v_mfma_f32_16x16x32_bf16 v[32:35],a[220:223],v[72:75],v[32:35]
s_cbranch_scc1 Lg1_slow1_%=
Lg1_a1w1_%=:
v_mfma_f32_16x16x32_bf16 v[36:39],a[32:35],v[44:47],0
s_add_u32 m0,s62,16384
ds_read_b128 v[72:75],v2 offset:35840
s_sub_u32 s69,s67,1048576
v_mfma_f32_16x16x32_bf16 v[40:43],a[36:39],v[44:47],0
s_cmp_lt_u32 s69,2097152
s_cselect_b32 s89,3145728,s89
s_sub_u32 s69,s67,3145728
v_mfma_f32_16x16x32_bf16 v[104:107],v[100:103],v[44:47],0
buffer_load_dwordx4 v1,s[36:39],s67 offen offset:0 sc1 lds
v_mfma_f32_16x16x32_bf16 v[108:111],v[44:47],v[44:47],0
s_cmp_lt_u32 s69,1048576
v_fma_f32 v28,-v92,v20,v28
v_fma_f32 v29,-v92,v21,v29
v_mfma_f32_16x16x32_bf16 v[36:39],a[40:43],v[48:51],v[36:39]
s_cselect_b32 s89,4194304,s89
ds_read_b128 v[44:47],v2 offset:45056
v_fma_f32 v30,-v92,v22,v30
v_mfma_f32_16x16x32_bf16 v[40:43],a[44:47],v[48:51],v[40:43]
s_lshr_b32 s73,s48,0
v_fma_f32 v31,-v92,v23,v31
v_mul_f32 v76,v28,v28
v_mfma_f32_16x16x32_bf16 v[104:107],v[100:103],v[48:51],v[104:107]
buffer_load_dwordx4 v1,s[36:39],s67 offen offset:1024 sc1 lds
v_mfma_f32_16x16x32_bf16 v[108:111],v[48:51],v[48:51],v[108:111]
s_lshr_b32 s72,s67,6
v_mul_f32 v77,v29,v29
v_mul_f32 v78,v30,v30
v_mfma_f32_16x16x32_bf16 v[36:39],a[48:51],v[52:55],v[36:39]
s_lshr_b32 s71,s62,2
ds_read_b128 v[48:51],v2 offset:46080
v_mul_f32 v79,v31,v31
v_mfma_f32_16x16x32_bf16 v[40:43],a[52:55],v[52:55],v[40:43]
s_cmp_lt_u32 s67,4194304
v_fma_f32 v76,v94,v76,v93
v_mul_f32 v76,v28,v76
v_mfma_f32_16x16x32_bf16 v[104:107],v[100:103],v[52:55],v[104:107]
buffer_load_dwordx4 v1,s[36:39],s67 offen offset:2048 sc1 lds
v_mfma_f32_16x16x32_bf16 v[108:111],v[52:55],v[52:55],v[108:111]
s_cselect_b32 s72,s59,s72
v_exp_f32 v76,v76
v_mfma_f32_16x16x32_bf16 v[36:39],a[56:59],v[56:59],v[36:39]
s_mov_b64 s[44:45],s[46:47]
ds_read_b128 v[52:55],v2 offset:47104
v_fma_f32 v76,v76,v95,v95
v_mfma_f32_16x16x32_bf16 v[40:43],a[60:63],v[56:59],v[40:43]
s_mov_b64 s[46:47],s[48:49]
v_rcp_f32 v76,v76
v_mfma_f32_16x16x32_bf16 v[104:107],v[100:103],v[56:59],v[104:107]
buffer_load_dwordx4 v1,s[36:39],s67 offen offset:3072 sc1 lds
v_mfma_f32_16x16x32_bf16 v[108:111],v[56:59],v[56:59],v[108:111]
s_waitcnt lgkmcnt(3)
v_fma_f32 v77,v94,v77,v93
v_mul_f32 v77,v29,v77
v_mfma_f32_16x16x32_bf16 v[36:39],a[0:3],v[60:63],v[36:39]
s_add_u32 m0,s71,s88
ds_read_b128 v[56:59],v2 offset:48128
v_fma_f32 v78,v94,v78,v93
v_mfma_f32_16x16x32_bf16 v[40:43],a[4:7],v[60:63],v[40:43]
buffer_load_dword v10,s[36:39],s72 offen offset:0 sc1 lds
v_mfma_f32_16x16x32_bf16 v[36:39],a[8:11],v[64:67],v[36:39]
s_xor_b32 s87,s63,65536
ds_read_b128 v[60:63],v2 offset:40960
v_mul_f32 v78,v30,v78
v_mfma_f32_16x16x32_bf16 v[40:43],a[12:15],v[64:67],v[40:43]
s_cmp_ge_u32 s67,s59
v_exp_f32 v77,v77
v_mfma_f32_16x16x32_bf16 v[36:39],a[16:19],v[68:71],v[36:39]
s_cselect_b32 s86,s50,s51
ds_read_b128 v[64:67],v2 offset:41984
v_fma_f32 v77,v77,v95,v95
v_mfma_f32_16x16x32_bf16 v[40:43],a[20:23],v[68:71],v[40:43]
s_cselect_b32 s87,s87,s64
v_exp_f32 v78,v78
v_mfma_f32_16x16x32_bf16 v[36:39],a[24:27],v[72:75],v[36:39]
s_mov_b32 s62,s87
ds_read_b128 v[68:71],v2 offset:43008
v_fma_f32 v78,v78,v95,v95
v_mfma_f32_16x16x32_bf16 v[40:43],a[28:31],v[72:75],v[40:43]
s_waitcnt lgkmcnt(3)
v_rcp_f32 v77,v77
v_mfma_f32_16x16x32_bf16 v[36:39],a[96:99],v[44:47],v[36:39]
s_waitcnt vmcnt(10)
ds_read_b128 v[72:75],v2 offset:44032
v_fma_f32 v79,v94,v79,v93
v_mfma_f32_16x16x32_bf16 v[40:43],a[100:103],v[44:47],v[40:43]
s_barrier
v_mfma_f32_16x16x32_bf16 v[36:39],a[104:107],v[48:51],v[36:39]
s_cmp_lt_u32 s86,s89
ds_read_b128 v[44:47],v2 offset:53248
ds_read_b32 v16,v7 offset:256
v_mfma_f32_16x16x32_bf16 v[40:43],a[108:111],v[48:51],v[40:43]
s_mov_b32 m0,s62
v_rcp_f32 v78,v78
v_mfma_f32_16x16x32_bf16 v[36:39],a[112:115],v[52:55],v[36:39]
s_mov_b64 s[48:49],s[50:51]
ds_read_b128 v[48:51],v2 offset:54272
v_mul_f32 v79,v31,v79
v_mfma_f32_16x16x32_bf16 v[40:43],a[116:119],v[52:55],v[40:43]
s_mov_b64 s[50:51],s[52:53]
v_exp_f32 v79,v79
v_mfma_f32_16x16x32_bf16 v[36:39],a[120:123],v[56:59],v[36:39]
s_waitcnt lgkmcnt(1)
ds_read_b128 v[52:55],v2 offset:55296
v_fma_f32 v79,v79,v95,v95
v_mfma_f32_16x16x32_bf16 v[40:43],a[124:127],v[56:59],v[40:43]
v_cmp_eq_u32 vcc,s60,v16
v_rcp_f32 v79,v79
v_mul_f32 v28,v28,v76
v_mfma_f32_16x16x32_bf16 v[36:39],a[64:67],v[60:63],v[36:39]
s_cselect_b32 vcc_lo,-1,vcc_lo
ds_read_b128 v[56:59],v2 offset:56320
v_mul_f32 v29,v29,v77
v_mfma_f32_16x16x32_bf16 v[40:43],a[68:71],v[60:63],v[40:43]
s_cmp_eq_u32 vcc_lo,-1
v_mul_f32 v30,v30,v78
v_mul_f32 v31,v31,v79
v_mfma_f32_16x16x32_bf16 v[36:39],a[72:75],v[64:67],v[36:39]
s_cselect_b32 s66,s86,s59
ds_read_b128 v[60:63],v2 offset:49152
v_cvt_pk_bf16_f32 v84,v28,v29
v_mfma_f32_16x16x32_bf16 v[40:43],a[76:79],v[64:67],v[40:43]
s_add_u32 s74,s53,s55
v_cvt_pk_bf16_f32 v85,v30,v31
v_fma_f32 v32,-v92,v24,v32
v_mfma_f32_16x16x32_bf16 v[36:39],a[80:83],v[68:71],v[36:39]
s_cmp_eq_u32 s74,s56
ds_read_b128 v[64:67],v2 offset:50176
v_fma_f32 v33,-v92,v25,v33
v_mfma_f32_16x16x32_bf16 v[40:43],a[84:87],v[68:71],v[40:43]
s_cselect_b32 s74,s57,s74
v_fma_f32 v34,-v92,v26,v34
v_fma_f32 v35,-v92,v27,v35
v_mfma_f32_16x16x32_bf16 v[36:39],a[88:91],v[72:75],v[36:39]
s_cmp_eq_u32 s74,s58
ds_read_b128 v[68:71],v2 offset:51200
v_mul_f32 v80,v32,v32
v_mfma_f32_16x16x32_bf16 v[40:43],a[92:95],v[72:75],v[40:43]
s_waitcnt lgkmcnt(3)
v_mul_f32 v81,v33,v33
v_mul_f32 v82,v34,v34
v_mfma_f32_16x16x32_bf16 v[36:39],a[160:163],v[44:47],v[36:39]
s_cselect_b32 s74,s59,s74
ds_read_b128 v[72:75],v2 offset:52224
v_mul_f32 v83,v35,v35
v_mfma_f32_16x16x32_bf16 v[40:43],a[164:167],v[44:47],v[40:43]
s_xor_b32 s64,s64,65536
v_fma_f32 v80,v94,v80,v93
v_mul_f32 v80,v32,v80
v_mfma_f32_16x16x32_bf16 v[104:107],v[100:103],v[44:47],v[104:107]
s_mov_b32 s52,s74
v_exp_f32 v80,v80
v_mfma_f32_16x16x32_bf16 v[108:111],v[44:47],v[44:47],v[108:111]
s_cmp_lt_u32 s67,4194304
v_fma_f32 v80,v80,v95,v95
v_fma_f32 v81,v94,v81,v93
v_mfma_f32_16x16x32_bf16 v[36:39],a[168:171],v[48:51],v[36:39]
ds_read_b128 v[44:47],v2 offset:61440
v_mul_f32 v81,v33,v81
v_mfma_f32_16x16x32_bf16 v[40:43],a[172:175],v[48:51],v[40:43]
v_exp_f32 v81,v81
v_mfma_f32_16x16x32_bf16 v[104:107],v[100:103],v[48:51],v[104:107]
v_rcp_f32 v80,v80
v_mfma_f32_16x16x32_bf16 v[108:111],v[48:51],v[48:51],v[108:111]
v_fma_f32 v81,v81,v95,v95
v_fma_f32 v82,v94,v82,v93
v_mfma_f32_16x16x32_bf16 v[36:39],a[176:179],v[52:55],v[36:39]
ds_read_b128 v[48:51],v2 offset:62464
v_mul_f32 v82,v34,v82
v_mfma_f32_16x16x32_bf16 v[40:43],a[180:183],v[52:55],v[40:43]
v_exp_f32 v82,v82
v_mfma_f32_16x16x32_bf16 v[104:107],v[100:103],v[52:55],v[104:107]
v_rcp_f32 v81,v81
v_mfma_f32_16x16x32_bf16 v[108:111],v[52:55],v[52:55],v[108:111]
v_fma_f32 v82,v82,v95,v95
v_fma_f32 v83,v94,v83,v93
v_mfma_f32_16x16x32_bf16 v[36:39],a[184:187],v[56:59],v[36:39]
ds_read_b128 v[52:55],v2 offset:63488
v_mul_f32 v83,v35,v83
v_mfma_f32_16x16x32_bf16 v[40:43],a[188:191],v[56:59],v[40:43]
v_exp_f32 v83,v83
v_mfma_f32_16x16x32_bf16 v[104:107],v[100:103],v[56:59],v[104:107]
v_rcp_f32 v82,v82
v_mfma_f32_16x16x32_bf16 v[108:111],v[56:59],v[56:59],v[108:111]
s_waitcnt lgkmcnt(3)
v_fma_f32 v83,v83,v95,v95
v_mul_f32 v32,v32,v80
v_mfma_f32_16x16x32_bf16 v[36:39],a[128:131],v[60:63],v[36:39]
ds_read_b128 v[56:59],v2 offset:64512
v_mul_f32 v33,v33,v81)"
R"(
v_mfma_f32_16x16x32_bf16 v[40:43],a[132:135],v[60:63],v[40:43]
buffer_load_dwordx4 v0,s[36:39],s66 offen offset:0 sc1 lds
v_mfma_f32_16x16x32_bf16 v[36:39],a[136:139],v[64:67],v[36:39]
v_cndmask_b32 v112,v108,v109,s[82:83]
v_cndmask_b32 v113,v110,v111,s[82:83]
v_cndmask_b32 v112,v112,v113,s[84:85]
v_cndmask_b32 v112,v104,v112,s[98:99]
v_mfma_f32_16x16x32_bf16 v[40:43],a[140:143],v[64:67],v[40:43]
ds_read_b128 v[60:63],v2 offset:57344
ds_write_b32 v13,v112 offset:2048
v_mfma_f32_16x16x32_bf16 v[36:39],a[144:147],v[68:71],v[36:39]
ds_read_b128 v[64:67],v2 offset:58368
v_mul_f32 v34,v34,v82
v_mfma_f32_16x16x32_bf16 v[40:43],a[148:151],v[68:71],v[40:43]
buffer_load_dwordx4 v0,s[36:39],s66 offen offset:1024 sc1 lds
v_mfma_f32_16x16x32_bf16 v[36:39],a[152:155],v[72:75],v[36:39]
ds_read_b128 v[68:71],v2 offset:59392
v_cvt_pk_bf16_f32 v86,v32,v33
v_mfma_f32_16x16x32_bf16 v[40:43],a[156:159],v[72:75],v[40:43]
s_waitcnt lgkmcnt(4)
v_rcp_f32 v83,v83
v_mfma_f32_16x16x32_bf16 v[36:39],a[224:227],v[44:47],v[36:39]
s_waitcnt vmcnt(7) lgkmcnt(2)
ds_read_b128 v[72:75],v2 offset:60416
v_xor_b32 v2,65536,v2
v_mfma_f32_16x16x32_bf16 v[40:43],a[228:231],v[44:47],v[40:43]
s_barrier
v_mfma_f32_16x16x32_bf16 v[36:39],a[232:235],v[48:51],v[36:39]
ds_read_b128 v[116:119],v14 offset:2048
ds_read_b128 v[120:123],v14 offset:2064
v_mfma_f32_16x16x32_bf16 v[40:43],a[236:239],v[48:51],v[40:43]
ds_read_b128 v[44:47],v2 offset:4096
buffer_load_dwordx4 v0,s[36:39],s66 offen offset:2048 sc1 lds
v_mfma_f32_16x16x32_bf16 v[36:39],a[240:243],v[52:55],v[36:39]
ds_read_b128 v[48:51],v2 offset:5120
v_mul_f32 v35,v35,v83
v_mfma_f32_16x16x32_bf16 v[40:43],a[244:247],v[52:55],v[40:43]
v_cvt_pk_bf16_f32 v87,v34,v35
v_xor_b32 v8,16384,v8
v_mfma_f32_16x16x32_bf16 v[36:39],a[248:251],v[56:59],v[36:39]
s_waitcnt lgkmcnt(2)
ds_read_b128 v[52:55],v2 offset:6144
v_add_f32 v116,v116,v117
v_mfma_f32_16x16x32_bf16 v[40:43],a[252:255],v[56:59],v[40:43]
buffer_load_dwordx4 v0,s[36:39],s66 offen offset:3072 sc1 lds
v_mfma_f32_16x16x32_bf16 v[36:39],a[192:195],v[60:63],v[36:39]
ds_read_b128 v[56:59],v2 offset:7168
v_add_f32 v118,v118,v119
v_mfma_f32_16x16x32_bf16 v[40:43],a[196:199],v[60:63],v[40:43]
v_add_f32 v116,v116,v118
v_mul_f32 v96,s75,v116
v_mfma_f32_16x16x32_bf16 v[36:39],a[200:203],v[64:67],v[36:39]
ds_read_b128 v[60:63],v2 offset:0
v_add_f32 v120,v120,v121
v_mfma_f32_16x16x32_bf16 v[40:43],a[204:207],v[64:67],v[40:43]
buffer_store_dwordx4 v[84:87],v3,s[36:39],s73 offen offset:0 sc1
v_add_f32 v122,v122,v123
v_add_f32 v120,v120,v122
v_mul_f32 v120,s75,v120
v_mfma_f32_16x16x32_bf16 v[36:39],a[208:211],v[68:71],v[36:39]
ds_read_b128 v[64:67],v2 offset:1024
v_fma_f32 v120,-v96,v96,v120
v_mfma_f32_16x16x32_bf16 v[40:43],a[212:215],v[68:71],v[40:43]
v_max_f32 v120,0,v120
v_add_f32 v115,v15,v120
v_fma_f32 v120,v15,v115,v120
v_rsq_f32 v116,v120
v_mfma_f32_16x16x32_bf16 v[36:39],a[216:219],v[72:75],v[36:39]
s_waitcnt lgkmcnt(2)
ds_read_b128 v[68:71],v2 offset:2048
v_mul_f32 v97,s76,v116
v_mul_f32 v98,v116,v116
v_mul_f32 v99,v120,v116
v_mul_f32 v98,s77,v98
v_mul_f32 v98,v98,v97
v_mfma_f32_16x16x32_bf16 v[40:43],a[220:223],v[72:75],v[40:43]
s_cbranch_scc1 Lg1_a0w1_%=
s_cmp_ge_u32 s48,4194304
s_cselect_b64 s[90:91],s[94:95],s[90:91]
s_cselect_b64 s[92:93],s[96:97],s[92:93]
s_cmp_lt_u32 s67,s59
s_cbranch_scc1 Lg1_d0w1_%=
s_branch Lg1_slow0_%=
Lg1_a0w2_%=:
v_mfma_f32_16x16x32_bf16 v[28:31],a[64:67],v[44:47],0
s_sub_u32 s69,s66,1048576
ds_read_b128 v[72:75],v2 offset:15360
s_cmp_lt_u32 s69,2097152
v_mfma_f32_16x16x32_bf16 v[32:35],a[68:71],v[44:47],0
s_cselect_b32 s89,3145728,s89
s_sub_u32 s69,s66,3145728
s_cmp_lt_u32 s69,1048576
v_mfma_f32_16x16x32_bf16 v[104:107],v[100:103],v[44:47],0
s_cselect_b32 s89,4194304,s89
s_add_u32 m0,s62,16384
s_lshr_b32 s73,s47,0
v_mfma_f32_16x16x32_bf16 v[108:111],v[44:47],v[44:47],0
buffer_load_dwordx4 v1,s[36:39],s66 offen offset:0 sc1 lds
v_mfma_f32_16x16x32_bf16 v[28:31],a[72:75],v[48:51],v[28:31]
s_lshr_b32 s65,s48,6
ds_read_b128 v[44:47],v2 offset:0
v_fma_f32 v36,-v96,v20,v36
v_mfma_f32_16x16x32_bf16 v[32:35],a[76:79],v[48:51],v[32:35]
s_lshr_b32 s72,s66,6
v_fma_f32 v37,-v96,v21,v37
v_fma_f32 v38,-v96,v22,v38
v_mfma_f32_16x16x32_bf16 v[104:107],v[100:103],v[48:51],v[104:107]
s_lshr_b32 s71,s62,2
v_fma_f32 v39,-v96,v23,v39
v_mul_f32 v76,v36,v36
v_mfma_f32_16x16x32_bf16 v[108:111],v[48:51],v[48:51],v[108:111]
buffer_load_dwordx4 v1,s[36:39],s66 offen offset:1024 sc1 lds
v_mfma_f32_16x16x32_bf16 v[28:31],a[80:83],v[52:55],v[28:31]
s_cmp_lt_u32 s66,4194304
ds_read_b128 v[48:51],v2 offset:1024
v_mul_f32 v77,v37,v37
v_mfma_f32_16x16x32_bf16 v[32:35],a[84:87],v[52:55],v[32:35]
s_cselect_b32 s72,s59,s72
v_mul_f32 v78,v38,v38
v_mul_f32 v79,v39,v39
v_mfma_f32_16x16x32_bf16 v[104:107],v[100:103],v[52:55],v[104:107]
v_fma_f32 v76,v98,v76,v97
v_mul_f32 v76,v36,v76
v_mfma_f32_16x16x32_bf16 v[108:111],v[52:55],v[52:55],v[108:111]
buffer_load_dwordx4 v1,s[36:39],s66 offen offset:2048 sc1 lds
v_mfma_f32_16x16x32_bf16 v[28:31],a[88:91],v[56:59],v[28:31]
ds_read_b128 v[52:55],v2 offset:2048
v_fma_f32 v77,v98,v77,v97
v_mfma_f32_16x16x32_bf16 v[32:35],a[92:95],v[56:59],v[32:35]
v_exp_f32 v76,v76
v_mfma_f32_16x16x32_bf16 v[104:107],v[100:103],v[56:59],v[104:107]
v_mul_f32 v77,v37,v77
v_fma_f32 v76,v76,v99,v99
v_mfma_f32_16x16x32_bf16 v[108:111],v[56:59],v[56:59],v[108:111]
s_waitcnt lgkmcnt(3)
buffer_load_dwordx4 v1,s[36:39],s66 offen offset:3072 sc1 lds
v_mfma_f32_16x16x32_bf16 v[28:31],a[96:99],v[60:63],v[28:31]
s_add_u32 m0,s71,s88
ds_read_b128 v[56:59],v2 offset:3072
v_fma_f32 v78,v98,v78,v97
v_mfma_f32_16x16x32_bf16 v[32:35],a[100:103],v[60:63],v[32:35]
buffer_load_dword v10,s[36:39],s72 offen offset:0 sc1 lds
v_mfma_f32_16x16x32_bf16 v[28:31],a[104:107],v[64:67],v[28:31]
s_xor_b32 s87,s64,65536
ds_read_b128 v[60:63],v2 offset:4096
v_fma_f32 v79,v98,v79,v97
v_mfma_f32_16x16x32_bf16 v[32:35],a[108:111],v[64:67],v[32:35]
s_cmp_ge_u32 s66,s59
v_exp_f32 v77,v77
v_mfma_f32_16x16x32_bf16 v[28:31],a[112:115],v[68:71],v[28:31]
s_cselect_b32 s86,s49,s50
ds_read_b128 v[64:67],v2 offset:5120
v_mul_f32 v78,v38,v78
v_mfma_f32_16x16x32_bf16 v[32:35],a[116:119],v[68:71],v[32:35]
s_cselect_b32 s87,s87,s63
v_exp_f32 v78,v78
v_mfma_f32_16x16x32_bf16 v[28:31],a[120:123],v[72:75],v[28:31]
s_mov_b32 s62,s87
ds_read_b128 v[68:71],v2 offset:6144
v_mul_f32 v79,v39,v79
v_mfma_f32_16x16x32_bf16 v[32:35],a[124:127],v[72:75],v[32:35]
s_waitcnt lgkmcnt(3)
v_exp_f32 v79,v79
v_mfma_f32_16x16x32_bf16 v[28:31],a[0:3],v[44:47],v[28:31]
s_waitcnt vmcnt(10)
ds_read_b128 v[72:75],v2 offset:7168
v_fma_f32 v77,v77,v99,v99
v_mfma_f32_16x16x32_bf16 v[32:35],a[4:7],v[44:47],v[32:35]
s_barrier
v_mfma_f32_16x16x32_bf16 v[28:31],a[8:11],v[48:51],v[28:31]
s_cmp_lt_u32 s86,s89
ds_read_b128 v[44:47],v2 offset:24576
ds_read_b32 v16,v7 offset:0
v_mfma_f32_16x16x32_bf16 v[32:35],a[12:15],v[48:51],v[32:35]
s_mov_b32 m0,s62
v_rcp_f32 v76,v76
v_mfma_f32_16x16x32_bf16 v[28:31],a[16:19],v[52:55],v[28:31]
ds_read_b128 v[48:51],v2 offset:25600
v_fma_f32 v78,v78,v99,v99
v_mfma_f32_16x16x32_bf16 v[32:35],a[20:23],v[52:55],v[32:35]
v_rcp_f32 v77,v77
v_mfma_f32_16x16x32_bf16 v[28:31],a[24:27],v[56:59],v[28:31]
s_waitcnt lgkmcnt(1)
ds_read_b128 v[52:55],v2 offset:26624
v_fma_f32 v79,v79,v99,v99
v_mfma_f32_16x16x32_bf16 v[32:35],a[28:31],v[56:59],v[32:35]
v_cmp_eq_u32 vcc,s60,v16
v_rcp_f32 v78,v78
v_mul_f32 v36,v36,v76
v_mfma_f32_16x16x32_bf16 v[28:31],a[32:35],v[60:63],v[28:31]
s_cselect_b32 vcc_lo,-1,vcc_lo
ds_read_b128 v[56:59],v2 offset:27648
v_mul_f32 v37,v37,v77
v_mfma_f32_16x16x32_bf16 v[32:35],a[36:39],v[60:63],v[32:35]
s_cmp_eq_u32 vcc_lo,-1
v_rcp_f32 v79,v79
v_mfma_f32_16x16x32_bf16 v[28:31],a[40:43],v[64:67],v[28:31]
s_cselect_b32 s67,s86,s59
ds_read_b128 v[60:63],v2 offset:28672
v_mul_f32 v38,v38,v78)"
R"(
v_mfma_f32_16x16x32_bf16 v[32:35],a[44:47],v[64:67],v[32:35]
s_add_u32 s53,s52,s55
v_mul_f32 v39,v39,v79
v_cvt_pk_bf16_f32 v84,v36,v37
v_mfma_f32_16x16x32_bf16 v[28:31],a[48:51],v[68:71],v[28:31]
s_cmp_eq_u32 s53,s56
ds_read_b128 v[64:67],v2 offset:29696
v_cvt_pk_bf16_f32 v85,v38,v39
v_mfma_f32_16x16x32_bf16 v[32:35],a[52:55],v[68:71],v[32:35]
s_cselect_b32 s53,s57,s53
v_fma_f32 v40,-v96,v24,v40
v_fma_f32 v41,-v96,v25,v41
v_mfma_f32_16x16x32_bf16 v[28:31],a[56:59],v[72:75],v[28:31]
s_cmp_eq_u32 s53,s58
ds_read_b128 v[68:71],v2 offset:30720
v_fma_f32 v42,-v96,v26,v42
v_mfma_f32_16x16x32_bf16 v[32:35],a[60:63],v[72:75],v[32:35]
s_waitcnt lgkmcnt(3)
v_fma_f32 v43,-v96,v27,v43
v_mul_f32 v80,v40,v40
v_mfma_f32_16x16x32_bf16 v[28:31],a[192:195],v[44:47],v[28:31]
s_cselect_b32 s53,s59,s53
ds_read_b128 v[72:75],v2 offset:31744
v_mul_f32 v81,v41,v41
v_mfma_f32_16x16x32_bf16 v[32:35],a[196:199],v[44:47],v[32:35]
s_xor_b32 s63,s63,65536
v_mul_f32 v82,v42,v42
v_mul_f32 v83,v43,v43
v_mfma_f32_16x16x32_bf16 v[104:107],v[100:103],v[44:47],v[104:107]
s_cmp_ge_u32 s66,s59
v_fma_f32 v80,v98,v80,v97
v_mul_f32 v80,v40,v80
v_mfma_f32_16x16x32_bf16 v[108:111],v[44:47],v[44:47],v[108:111]
v_exp_f32 v80,v80
v_mfma_f32_16x16x32_bf16 v[28:31],a[200:203],v[48:51],v[28:31]
ds_read_b128 v[44:47],v2 offset:16384
v_fma_f32 v80,v80,v99,v99
v_mfma_f32_16x16x32_bf16 v[32:35],a[204:207],v[48:51],v[32:35]
v_rcp_f32 v80,v80
v_mfma_f32_16x16x32_bf16 v[104:107],v[100:103],v[48:51],v[104:107]
v_fma_f32 v81,v98,v81,v97
v_mul_f32 v81,v41,v81
v_mfma_f32_16x16x32_bf16 v[108:111],v[48:51],v[48:51],v[108:111]
v_exp_f32 v81,v81
v_mfma_f32_16x16x32_bf16 v[28:31],a[208:211],v[52:55],v[28:31]
ds_read_b128 v[48:51],v2 offset:17408
v_fma_f32 v81,v81,v99,v99
v_mfma_f32_16x16x32_bf16 v[32:35],a[212:215],v[52:55],v[32:35]
v_rcp_f32 v81,v81
v_mfma_f32_16x16x32_bf16 v[104:107],v[100:103],v[52:55],v[104:107]
v_fma_f32 v82,v98,v82,v97
v_mul_f32 v82,v42,v82
v_mfma_f32_16x16x32_bf16 v[108:111],v[52:55],v[52:55],v[108:111]
v_exp_f32 v82,v82
v_mfma_f32_16x16x32_bf16 v[28:31],a[216:219],v[56:59],v[28:31]
ds_read_b128 v[52:55],v2 offset:18432
v_fma_f32 v82,v82,v99,v99
v_mfma_f32_16x16x32_bf16 v[32:35],a[220:223],v[56:59],v[32:35]
v_rcp_f32 v82,v82
v_mfma_f32_16x16x32_bf16 v[104:107],v[100:103],v[56:59],v[104:107]
v_fma_f32 v83,v98,v83,v97
v_mul_f32 v83,v43,v83
v_mfma_f32_16x16x32_bf16 v[108:111],v[56:59],v[56:59],v[108:111]
s_waitcnt lgkmcnt(3)
v_exp_f32 v83,v83
v_mfma_f32_16x16x32_bf16 v[28:31],a[224:227],v[60:63],v[28:31]
ds_read_b128 v[56:59],v2 offset:19456
v_fma_f32 v83,v83,v99,v99
v_mfma_f32_16x16x32_bf16 v[32:35],a[228:231],v[60:63],v[32:35]
v_rcp_f32 v83,v83
v_mfma_f32_16x16x32_bf16 v[28:31],a[232:235],v[64:67],v[28:31]
buffer_load_dwordx4 v0,s[36:39],s67 offen offset:0 sc1 lds
v_cndmask_b32 v112,v108,v109,s[82:83]
v_cndmask_b32 v113,v110,v111,s[82:83]
v_cndmask_b32 v112,v112,v113,s[84:85]
v_cndmask_b32 v112,v104,v112,s[98:99]
v_mfma_f32_16x16x32_bf16 v[32:35],a[236:239],v[64:67],v[32:35]
ds_read_b128 v[60:63],v2 offset:20480
ds_write_b32 v13,v112 offset:0
v_mfma_f32_16x16x32_bf16 v[28:31],a[240:243],v[68:71],v[28:31]
ds_read_b128 v[64:67],v2 offset:21504
v_mul_f32 v40,v40,v80
v_mfma_f32_16x16x32_bf16 v[32:35],a[244:247],v[68:71],v[32:35]
v_mul_f32 v41,v41,v81
v_mul_f32 v42,v42,v82
v_mfma_f32_16x16x32_bf16 v[28:31],a[248:251],v[72:75],v[28:31]
buffer_load_dwordx4 v0,s[36:39],s67 offen offset:1024 sc1 lds
v_mfma_f32_16x16x32_bf16 v[32:35],a[252:255],v[72:75],v[32:35]
s_waitcnt lgkmcnt(3)
ds_read_b128 v[68:71],v2 offset:22528
v_mul_f32 v43,v43,v83
v_mfma_f32_16x16x32_bf16 v[28:31],a[128:131],v[44:47],v[28:31]
s_waitcnt vmcnt(7) lgkmcnt(2)
ds_read_b128 v[72:75],v2 offset:23552
v_cvt_pk_bf16_f32 v86,v40,v41
v_mfma_f32_16x16x32_bf16 v[32:35],a[132:135],v[44:47],v[32:35]
s_barrier
v_mfma_f32_16x16x32_bf16 v[28:31],a[136:139],v[48:51],v[28:31]
ds_read_b128 v[116:119],v14 offset:0
ds_read_b128 v[120:123],v14 offset:16
v_mfma_f32_16x16x32_bf16 v[32:35],a[140:143],v[48:51],v[32:35]
ds_read_b128 v[44:47],v2 offset:40960
v_cvt_pk_bf16_f32 v87,v42,v43
v_mfma_f32_16x16x32_bf16 v[28:31],a[144:147],v[52:55],v[28:31]
buffer_load_dwordx4 v0,s[36:39],s67 offen offset:2048 sc1 lds
v_mfma_f32_16x16x32_bf16 v[32:35],a[148:151],v[52:55],v[32:35]
ds_read_b128 v[48:51],v2 offset:41984
v_mfma_f32_16x16x32_bf16 v[28:31],a[152:155],v[56:59],v[28:31]
s_waitcnt lgkmcnt(2)
ds_read_b128 v[52:55],v2 offset:43008
v_add_f32 v116,v116,v117
v_mfma_f32_16x16x32_bf16 v[32:35],a[156:159],v[56:59],v[32:35]
v_add_f32 v118,v118,v119
v_add_f32 v116,v116,v118
v_mfma_f32_16x16x32_bf16 v[28:31],a[160:163],v[60:63],v[28:31]
buffer_load_dwordx4 v0,s[36:39],s67 offen offset:3072 sc1 lds
v_mfma_f32_16x16x32_bf16 v[32:35],a[164:167],v[60:63],v[32:35]
ds_read_b128 v[56:59],v2 offset:44032
v_mul_f32 v92,s75,v116
v_mfma_f32_16x16x32_bf16 v[28:31],a[168:171],v[64:67],v[28:31]
ds_read_b128 v[60:63],v2 offset:45056
v_add_f32 v120,v120,v121
v_mfma_f32_16x16x32_bf16 v[32:35],a[172:175],v[64:67],v[32:35]
v_add_f32 v122,v122,v123
v_add_f32 v120,v120,v122
v_mfma_f32_16x16x32_bf16 v[28:31],a[176:179],v[68:71],v[28:31]
buffer_store_dwordx4 v[84:87],v3,s[36:39],s73 offen offset:0 sc1
v_mul_f32 v120,s75,v120
v_fma_f32 v120,-v92,v92,v120
v_max_f32 v120,0,v120
v_add_f32 v115,v15,v120
v_fma_f32 v120,v15,v115,v120
v_rsq_f32 v115,v115
v_mfma_f32_16x16x32_bf16 v[32:35],a[180:183],v[68:71],v[32:35]
ds_read_b128 v[64:67],v2 offset:46080
v_rsq_f32 v116,v120
v_mul_f32_e64 v114,-v92,v115
v_mfma_f32_16x16x32_bf16 v[28:31],a[184:187],v[72:75],v[28:31]
s_waitcnt lgkmcnt(2)
ds_read_b128 v[68:71],v2 offset:47104
buffer_store_dwordx2 v[114:115],v12,s[36:39],s65 offen offset:0 sc1
v_mul_f32 v93,s76,v116
v_mul_f32 v94,v116,v116
v_mul_f32 v95,v120,v116
v_mul_f32 v94,s77,v94
v_mul_f32 v94,v94,v93
v_mfma_f32_16x16x32_bf16 v[32:35],a[188:191],v[72:75],v[32:35]
s_cbranch_scc1 Lg1_slow1_%=
Lg1_a1w2_%=:
v_mfma_f32_16x16x32_bf16 v[36:39],a[64:67],v[44:47],0
s_sub_u32 s69,s67,1048576
ds_read_b128 v[72:75],v2 offset:48128
s_cmp_lt_u32 s69,2097152
v_mfma_f32_16x16x32_bf16 v[40:43],a[68:71],v[44:47],0
s_cselect_b32 s89,3145728,s89
s_sub_u32 s69,s67,3145728
s_cmp_lt_u32 s69,1048576
v_mfma_f32_16x16x32_bf16 v[104:107],v[100:103],v[44:47],0
s_cselect_b32 s89,4194304,s89
s_add_u32 m0,s62,16384
s_lshr_b32 s73,s48,0
v_mfma_f32_16x16x32_bf16 v[108:111],v[44:47],v[44:47],0
buffer_load_dwordx4 v1,s[36:39],s67 offen offset:0 sc1 lds
v_mfma_f32_16x16x32_bf16 v[36:39],a[72:75],v[48:51],v[36:39]
s_lshr_b32 s65,s49,6
ds_read_b128 v[44:47],v2 offset:32768
v_fma_f32 v28,-v92,v20,v28
v_mfma_f32_16x16x32_bf16 v[40:43],a[76:79],v[48:51],v[40:43]
s_lshr_b32 s72,s67,6
v_fma_f32 v29,-v92,v21,v29
v_fma_f32 v30,-v92,v22,v30
v_mfma_f32_16x16x32_bf16 v[104:107],v[100:103],v[48:51],v[104:107]
s_lshr_b32 s71,s62,2
v_fma_f32 v31,-v92,v23,v31
v_mul_f32 v76,v28,v28
v_mfma_f32_16x16x32_bf16 v[108:111],v[48:51],v[48:51],v[108:111]
buffer_load_dwordx4 v1,s[36:39],s67 offen offset:1024 sc1 lds
v_mfma_f32_16x16x32_bf16 v[36:39],a[80:83],v[52:55],v[36:39]
s_cmp_lt_u32 s67,4194304
ds_read_b128 v[48:51],v2 offset:33792
v_mul_f32 v77,v29,v29
v_mfma_f32_16x16x32_bf16 v[40:43],a[84:87],v[52:55],v[40:43]
s_cselect_b32 s72,s59,s72
v_mul_f32 v78,v30,v30
v_mul_f32 v79,v31,v31
v_mfma_f32_16x16x32_bf16 v[104:107],v[100:103],v[52:55],v[104:107]
s_mov_b64 s[44:45],s[46:47]
v_fma_f32 v76,v94,v76,v93
v_mul_f32 v76,v28,v76
v_mfma_f32_16x16x32_bf16 v[108:111],v[52:55],v[52:55],v[108:111]
buffer_load_dwordx4 v1,s[36:39],s67 offen offset:2048 sc1 lds
v_mfma_f32_16x16x32_bf16 v[36:39],a[88:91],v[56:59],v[36:39]
s_mov_b64 s[46:47],s[48:49]
ds_read_b128 v[52:55],v2 offset:34816
v_fma_f32 v77,v94,v77,v93
v_mfma_f32_16x16x32_bf16 v[40:43],a[92:95],v[56:59],v[40:43]
s_mov_b64 s[48:49],s[50:51]
v_exp_f32 v76,v76
v_mfma_f32_16x16x32_bf16 v[104:107],v[100:103],v[56:59],v[104:107]
v_mul_f32 v77,v29,v77)"
R"(
v_fma_f32 v76,v76,v95,v95
v_mfma_f32_16x16x32_bf16 v[108:111],v[56:59],v[56:59],v[108:111]
s_waitcnt lgkmcnt(3)
buffer_load_dwordx4 v1,s[36:39],s67 offen offset:3072 sc1 lds
v_mfma_f32_16x16x32_bf16 v[36:39],a[96:99],v[60:63],v[36:39]
s_add_u32 m0,s71,s88
ds_read_b128 v[56:59],v2 offset:35840
v_fma_f32 v78,v94,v78,v93
v_mfma_f32_16x16x32_bf16 v[40:43],a[100:103],v[60:63],v[40:43]
buffer_load_dword v10,s[36:39],s72 offen offset:0 sc1 lds
v_mfma_f32_16x16x32_bf16 v[36:39],a[104:107],v[64:67],v[36:39]
s_xor_b32 s87,s63,65536
ds_read_b128 v[60:63],v2 offset:36864
v_fma_f32 v79,v94,v79,v93
v_mfma_f32_16x16x32_bf16 v[40:43],a[108:111],v[64:67],v[40:43]
s_cmp_ge_u32 s67,s59
v_exp_f32 v77,v77
v_mfma_f32_16x16x32_bf16 v[36:39],a[112:115],v[68:71],v[36:39]
s_cselect_b32 s86,s50,s51
ds_read_b128 v[64:67],v2 offset:37888
v_mul_f32 v78,v30,v78
v_mfma_f32_16x16x32_bf16 v[40:43],a[116:119],v[68:71],v[40:43]
s_cselect_b32 s87,s87,s64
v_exp_f32 v78,v78
v_mfma_f32_16x16x32_bf16 v[36:39],a[120:123],v[72:75],v[36:39]
s_mov_b32 s62,s87
ds_read_b128 v[68:71],v2 offset:38912
v_mul_f32 v79,v31,v79
v_mfma_f32_16x16x32_bf16 v[40:43],a[124:127],v[72:75],v[40:43]
s_waitcnt lgkmcnt(3)
v_exp_f32 v79,v79
v_mfma_f32_16x16x32_bf16 v[36:39],a[0:3],v[44:47],v[36:39]
s_waitcnt vmcnt(10)
ds_read_b128 v[72:75],v2 offset:39936
v_fma_f32 v77,v77,v95,v95
v_mfma_f32_16x16x32_bf16 v[40:43],a[4:7],v[44:47],v[40:43]
s_barrier
v_mfma_f32_16x16x32_bf16 v[36:39],a[8:11],v[48:51],v[36:39]
s_cmp_lt_u32 s86,s89
ds_read_b128 v[44:47],v2 offset:57344
ds_read_b32 v16,v7 offset:256
v_mfma_f32_16x16x32_bf16 v[40:43],a[12:15],v[48:51],v[40:43]
s_mov_b32 m0,s62
v_rcp_f32 v76,v76
v_mfma_f32_16x16x32_bf16 v[36:39],a[16:19],v[52:55],v[36:39]
s_mov_b64 s[50:51],s[52:53]
ds_read_b128 v[48:51],v2 offset:58368
v_fma_f32 v78,v78,v95,v95
v_mfma_f32_16x16x32_bf16 v[40:43],a[20:23],v[52:55],v[40:43]
v_rcp_f32 v77,v77
v_mfma_f32_16x16x32_bf16 v[36:39],a[24:27],v[56:59],v[36:39]
s_waitcnt lgkmcnt(1)
ds_read_b128 v[52:55],v2 offset:59392
v_fma_f32 v79,v79,v95,v95
v_mfma_f32_16x16x32_bf16 v[40:43],a[28:31],v[56:59],v[40:43]
v_cmp_eq_u32 vcc,s60,v16
v_rcp_f32 v78,v78
v_mul_f32 v28,v28,v76
v_mfma_f32_16x16x32_bf16 v[36:39],a[32:35],v[60:63],v[36:39]
s_cselect_b32 vcc_lo,-1,vcc_lo
ds_read_b128 v[56:59],v2 offset:60416
v_mul_f32 v29,v29,v77
v_mfma_f32_16x16x32_bf16 v[40:43],a[36:39],v[60:63],v[40:43]
s_cmp_eq_u32 vcc_lo,-1
v_rcp_f32 v79,v79
v_mfma_f32_16x16x32_bf16 v[36:39],a[40:43],v[64:67],v[36:39]
s_cselect_b32 s66,s86,s59
ds_read_b128 v[60:63],v2 offset:61440
v_mul_f32 v30,v30,v78
v_mfma_f32_16x16x32_bf16 v[40:43],a[44:47],v[64:67],v[40:43]
s_add_u32 s74,s53,s55
v_mul_f32 v31,v31,v79
v_cvt_pk_bf16_f32 v84,v28,v29
v_mfma_f32_16x16x32_bf16 v[36:39],a[48:51],v[68:71],v[36:39]
s_cmp_eq_u32 s74,s56
ds_read_b128 v[64:67],v2 offset:62464
v_cvt_pk_bf16_f32 v85,v30,v31
v_mfma_f32_16x16x32_bf16 v[40:43],a[52:55],v[68:71],v[40:43]
s_cselect_b32 s74,s57,s74
v_fma_f32 v32,-v92,v24,v32
v_fma_f32 v33,-v92,v25,v33
v_mfma_f32_16x16x32_bf16 v[36:39],a[56:59],v[72:75],v[36:39]
s_cmp_eq_u32 s74,s58
ds_read_b128 v[68:71],v2 offset:63488
v_fma_f32 v34,-v92,v26,v34
v_mfma_f32_16x16x32_bf16 v[40:43],a[60:63],v[72:75],v[40:43]
s_waitcnt lgkmcnt(3)
v_fma_f32 v35,-v92,v27,v35
v_mul_f32 v80,v32,v32
v_mfma_f32_16x16x32_bf16 v[36:39],a[192:195],v[44:47],v[36:39]
s_cselect_b32 s74,s59,s74
ds_read_b128 v[72:75],v2 offset:64512
v_mul_f32 v81,v33,v33
v_mfma_f32_16x16x32_bf16 v[40:43],a[196:199],v[44:47],v[40:43]
s_xor_b32 s64,s64,65536
v_mul_f32 v82,v34,v34
v_mul_f32 v83,v35,v35
v_mfma_f32_16x16x32_bf16 v[104:107],v[100:103],v[44:47],v[104:107]
s_mov_b32 s52,s74
v_fma_f32 v80,v94,v80,v93
v_mul_f32 v80,v32,v80
v_mfma_f32_16x16x32_bf16 v[108:111],v[44:47],v[44:47],v[108:111]
s_cmp_lt_u32 s67,4194304
v_exp_f32 v80,v80
v_mfma_f32_16x16x32_bf16 v[36:39],a[200:203],v[48:51],v[36:39]
ds_read_b128 v[44:47],v2 offset:49152
v_fma_f32 v80,v80,v95,v95
v_mfma_f32_16x16x32_bf16 v[40:43],a[204:207],v[48:51],v[40:43]
v_rcp_f32 v80,v80
v_mfma_f32_16x16x32_bf16 v[104:107],v[100:103],v[48:51],v[104:107]
v_fma_f32 v81,v94,v81,v93
v_mul_f32 v81,v33,v81
v_mfma_f32_16x16x32_bf16 v[108:111],v[48:51],v[48:51],v[108:111]
v_exp_f32 v81,v81
v_mfma_f32_16x16x32_bf16 v[36:39],a[208:211],v[52:55],v[36:39]
ds_read_b128 v[48:51],v2 offset:50176
v_fma_f32 v81,v81,v95,v95
v_mfma_f32_16x16x32_bf16 v[40:43],a[212:215],v[52:55],v[40:43]
v_rcp_f32 v81,v81
v_mfma_f32_16x16x32_bf16 v[104:107],v[100:103],v[52:55],v[104:107]
v_fma_f32 v82,v94,v82,v93
v_mul_f32 v82,v34,v82
v_mfma_f32_16x16x32_bf16 v[108:111],v[52:55],v[52:55],v[108:111]
v_exp_f32 v82,v82
v_mfma_f32_16x16x32_bf16 v[36:39],a[216:219],v[56:59],v[36:39]
ds_read_b128 v[52:55],v2 offset:51200
v_fma_f32 v82,v82,v95,v95
v_mfma_f32_16x16x32_bf16 v[40:43],a[220:223],v[56:59],v[40:43]
v_rcp_f32 v82,v82
v_mfma_f32_16x16x32_bf16 v[104:107],v[100:103],v[56:59],v[104:107]
v_fma_f32 v83,v94,v83,v93
v_mul_f32 v83,v35,v83
v_mfma_f32_16x16x32_bf16 v[108:111],v[56:59],v[56:59],v[108:111]
s_waitcnt lgkmcnt(3)
v_exp_f32 v83,v83
v_mfma_f32_16x16x32_bf16 v[36:39],a[224:227],v[60:63],v[36:39]
ds_read_b128 v[56:59],v2 offset:52224
v_fma_f32 v83,v83,v95,v95
v_mfma_f32_16x16x32_bf16 v[40:43],a[228:231],v[60:63],v[40:43]
v_rcp_f32 v83,v83
v_mfma_f32_16x16x32_bf16 v[36:39],a[232:235],v[64:67],v[36:39]
buffer_load_dwordx4 v0,s[36:39],s66 offen offset:0 sc1 lds
v_cndmask_b32 v112,v108,v109,s[82:83]
v_cndmask_b32 v113,v110,v111,s[82:83]
v_cndmask_b32 v112,v112,v113,s[84:85]
v_cndmask_b32 v112,v104,v112,s[98:99]
v_mfma_f32_16x16x32_bf16 v[40:43],a[236:239],v[64:67],v[40:43]
ds_read_b128 v[60:63],v2 offset:53248
ds_write_b32 v13,v112 offset:2048
v_mfma_f32_16x16x32_bf16 v[36:39],a[240:243],v[68:71],v[36:39]
ds_read_b128 v[64:67],v2 offset:54272
v_mul_f32 v32,v32,v80
v_mfma_f32_16x16x32_bf16 v[40:43],a[244:247],v[68:71],v[40:43]
v_mul_f32 v33,v33,v81
v_mul_f32 v34,v34,v82
v_mfma_f32_16x16x32_bf16 v[36:39],a[248:251],v[72:75],v[36:39]
buffer_load_dwordx4 v0,s[36:39],s66 offen offset:1024 sc1 lds
v_mfma_f32_16x16x32_bf16 v[40:43],a[252:255],v[72:75],v[40:43]
s_waitcnt lgkmcnt(3)
ds_read_b128 v[68:71],v2 offset:55296
v_mul_f32 v35,v35,v83
v_mfma_f32_16x16x32_bf16 v[36:39],a[128:131],v[44:47],v[36:39]
s_waitcnt vmcnt(7) lgkmcnt(2)
ds_read_b128 v[72:75],v2 offset:56320
v_xor_b32 v2,65536,v2
v_mfma_f32_16x16x32_bf16 v[40:43],a[132:135],v[44:47],v[40:43]
s_barrier
v_mfma_f32_16x16x32_bf16 v[36:39],a[136:139],v[48:51],v[36:39]
ds_read_b128 v[116:119],v14 offset:2048
ds_read_b128 v[120:123],v14 offset:2064
v_mfma_f32_16x16x32_bf16 v[40:43],a[140:143],v[48:51],v[40:43]
ds_read_b128 v[44:47],v2 offset:8192
v_cvt_pk_bf16_f32 v86,v32,v33
v_mfma_f32_16x16x32_bf16 v[36:39],a[144:147],v[52:55],v[36:39]
buffer_load_dwordx4 v0,s[36:39],s66 offen offset:2048 sc1 lds
v_mfma_f32_16x16x32_bf16 v[40:43],a[148:151],v[52:55],v[40:43]
ds_read_b128 v[48:51],v2 offset:9216
v_cvt_pk_bf16_f32 v87,v34,v35
v_mfma_f32_16x16x32_bf16 v[36:39],a[152:155],v[56:59],v[36:39]
s_waitcnt lgkmcnt(2)
ds_read_b128 v[52:55],v2 offset:10240
v_xor_b32 v8,16384,v8
v_mfma_f32_16x16x32_bf16 v[40:43],a[156:159],v[56:59],v[40:43]
v_add_f32 v116,v116,v117
v_add_f32 v118,v118,v119
v_mfma_f32_16x16x32_bf16 v[36:39],a[160:163],v[60:63],v[36:39]
buffer_load_dwordx4 v0,s[36:39],s66 offen offset:3072 sc1 lds
v_mfma_f32_16x16x32_bf16 v[40:43],a[164:167],v[60:63],v[40:43]
ds_read_b128 v[56:59],v2 offset:11264
v_add_f32 v116,v116,v118
v_mfma_f32_16x16x32_bf16 v[36:39],a[168:171],v[64:67],v[36:39]
ds_read_b128 v[60:63],v2 offset:12288
v_mul_f32 v96,s75,v116
v_mfma_f32_16x16x32_bf16 v[40:43],a[172:175],v[64:67],v[40:43]
v_add_f32 v120,v120,v121
v_add_f32 v122,v122,v123
v_mfma_f32_16x16x32_bf16 v[36:39],a[176:179],v[68:71],v[36:39]
buffer_store_dwordx4 v[84:87],v3,s[36:39],s73 offen offset:0 sc1
v_add_f32 v120,v120,v122
v_mul_f32 v120,s75,v120)"
R"(
v_fma_f32 v120,-v96,v96,v120
v_max_f32 v120,0,v120
v_add_f32 v115,v15,v120
v_fma_f32 v120,v15,v115,v120
v_rsq_f32 v115,v115
v_mfma_f32_16x16x32_bf16 v[40:43],a[180:183],v[68:71],v[40:43]
ds_read_b128 v[64:67],v2 offset:13312
v_rsq_f32 v116,v120
v_mul_f32_e64 v114,-v96,v115
v_mfma_f32_16x16x32_bf16 v[36:39],a[184:187],v[72:75],v[36:39]
s_waitcnt lgkmcnt(2)
ds_read_b128 v[68:71],v2 offset:14336
buffer_store_dwordx2 v[114:115],v12,s[36:39],s65 offen offset:0 sc1
v_mul_f32 v97,s76,v116
v_mul_f32 v98,v116,v116
v_mul_f32 v99,v120,v116
v_mul_f32 v98,s77,v98
v_mul_f32 v98,v98,v97
v_mfma_f32_16x16x32_bf16 v[40:43],a[188:191],v[72:75],v[40:43]
s_cbranch_scc1 Lg1_a0w2_%=
s_cmp_ge_u32 s48,4194304
s_cselect_b64 s[90:91],s[94:95],s[90:91]
s_cselect_b64 s[92:93],s[96:97],s[92:93]
s_cmp_lt_u32 s67,s59
s_cbranch_scc1 Lg1_d0w2_%=
s_branch Lg1_slow0_%=
Lg1_a0w3_%=:
v_mfma_f32_16x16x32_bf16 v[28:31],a[96:99],v[44:47],0
s_sub_u32 s69,s66,1048576
ds_read_b128 v[72:75],v2 offset:11264
s_cmp_lt_u32 s69,2097152
v_mfma_f32_16x16x32_bf16 v[32:35],a[100:103],v[44:47],0
s_cselect_b32 s89,3145728,s89
s_sub_u32 s69,s66,3145728
s_cmp_lt_u32 s69,1048576
v_mfma_f32_16x16x32_bf16 v[104:107],v[100:103],v[44:47],0
s_cselect_b32 s89,4194304,s89
s_add_u32 m0,s62,16384
s_lshr_b32 s73,s47,0
v_mfma_f32_16x16x32_bf16 v[108:111],v[44:47],v[44:47],0
s_lshr_b32 s72,s66,6
v_fma_f32 v36,-v96,v20,v36
v_fma_f32 v37,-v96,v21,v37
v_mfma_f32_16x16x32_bf16 v[28:31],a[104:107],v[48:51],v[28:31]
buffer_load_dwordx4 v1,s[36:39],s66 offen offset:0 sc1 lds
v_mfma_f32_16x16x32_bf16 v[32:35],a[108:111],v[48:51],v[32:35]
s_lshr_b32 s71,s62,2
ds_read_b128 v[44:47],v2 offset:4096
v_fma_f32 v38,-v96,v22,v38
v_mfma_f32_16x16x32_bf16 v[104:107],v[100:103],v[48:51],v[104:107]
s_cmp_lt_u32 s66,4194304
v_fma_f32 v39,-v96,v23,v39
v_mul_f32 v76,v36,v36
v_mfma_f32_16x16x32_bf16 v[108:111],v[48:51],v[48:51],v[108:111]
s_cselect_b32 s72,s59,s72
v_mul_f32 v77,v37,v37
v_mul_f32 v78,v38,v38
v_mfma_f32_16x16x32_bf16 v[28:31],a[112:115],v[52:55],v[28:31]
buffer_load_dwordx4 v1,s[36:39],s66 offen offset:1024 sc1 lds
v_mfma_f32_16x16x32_bf16 v[32:35],a[116:119],v[52:55],v[32:35]
ds_read_b128 v[48:51],v2 offset:5120
v_mul_f32 v79,v39,v39
v_mfma_f32_16x16x32_bf16 v[104:107],v[100:103],v[52:55],v[104:107]
v_fma_f32 v76,v98,v76,v97
v_mul_f32 v76,v36,v76
v_mfma_f32_16x16x32_bf16 v[108:111],v[52:55],v[52:55],v[108:111]
v_exp_f32 v76,v76
v_mfma_f32_16x16x32_bf16 v[28:31],a[120:123],v[56:59],v[28:31]
buffer_load_dwordx4 v1,s[36:39],s66 offen offset:2048 sc1 lds
v_mfma_f32_16x16x32_bf16 v[32:35],a[124:127],v[56:59],v[32:35]
ds_read_b128 v[52:55],v2 offset:6144
v_fma_f32 v76,v76,v99,v99
v_mfma_f32_16x16x32_bf16 v[104:107],v[100:103],v[56:59],v[104:107]
v_rcp_f32 v76,v76
v_mfma_f32_16x16x32_bf16 v[108:111],v[56:59],v[56:59],v[108:111]
s_waitcnt lgkmcnt(3)
v_fma_f32 v77,v98,v77,v97
v_mul_f32 v77,v37,v77
v_mfma_f32_16x16x32_bf16 v[28:31],a[64:67],v[60:63],v[28:31]
buffer_load_dwordx4 v1,s[36:39],s66 offen offset:3072 sc1 lds
v_mfma_f32_16x16x32_bf16 v[32:35],a[68:71],v[60:63],v[32:35]
s_add_u32 m0,s71,s88
ds_read_b128 v[56:59],v2 offset:7168
v_fma_f32 v78,v98,v78,v97
v_mfma_f32_16x16x32_bf16 v[28:31],a[72:75],v[64:67],v[28:31]
buffer_load_dword v10,s[36:39],s72 offen offset:0 sc1 lds
v_mfma_f32_16x16x32_bf16 v[32:35],a[76:79],v[64:67],v[32:35]
s_xor_b32 s87,s64,65536
ds_read_b128 v[60:63],v2 offset:0
v_mul_f32 v78,v38,v78
v_mfma_f32_16x16x32_bf16 v[28:31],a[80:83],v[68:71],v[28:31]
s_cmp_ge_u32 s66,s59
ds_read_b128 v[64:67],v2 offset:1024
v_fma_f32 v79,v98,v79,v97
v_mfma_f32_16x16x32_bf16 v[32:35],a[84:87],v[68:71],v[32:35]
s_cselect_b32 s86,s49,s50
v_exp_f32 v77,v77
v_mfma_f32_16x16x32_bf16 v[28:31],a[88:91],v[72:75],v[28:31]
s_cselect_b32 s87,s87,s63
ds_read_b128 v[68:71],v2 offset:2048
v_mul_f32 v79,v39,v79
v_mfma_f32_16x16x32_bf16 v[32:35],a[92:95],v[72:75],v[32:35]
s_waitcnt lgkmcnt(3)
v_exp_f32 v78,v78
v_mfma_f32_16x16x32_bf16 v[28:31],a[32:35],v[44:47],v[28:31]
s_waitcnt vmcnt(11)
ds_read_b128 v[72:75],v2 offset:3072
v_fma_f32 v77,v77,v99,v99
v_mfma_f32_16x16x32_bf16 v[32:35],a[36:39],v[44:47],v[32:35]
s_barrier
v_mfma_f32_16x16x32_bf16 v[28:31],a[40:43],v[48:51],v[28:31]
s_mov_b32 s62,s87
ds_read_b128 v[44:47],v2 offset:28672
ds_read_b32 v16,v7 offset:0
v_mfma_f32_16x16x32_bf16 v[32:35],a[44:47],v[48:51],v[32:35]
s_cmp_lt_u32 s86,s89
v_exp_f32 v79,v79
v_mfma_f32_16x16x32_bf16 v[28:31],a[48:51],v[52:55],v[28:31]
s_mov_b32 m0,s62
ds_read_b128 v[48:51],v2 offset:29696
v_fma_f32 v78,v78,v99,v99
v_mfma_f32_16x16x32_bf16 v[32:35],a[52:55],v[52:55],v[32:35]
v_rcp_f32 v77,v77
v_mfma_f32_16x16x32_bf16 v[28:31],a[56:59],v[56:59],v[28:31]
s_waitcnt lgkmcnt(1)
ds_read_b128 v[52:55],v2 offset:30720
v_fma_f32 v79,v79,v99,v99
v_mfma_f32_16x16x32_bf16 v[32:35],a[60:63],v[56:59],v[32:35]
v_cmp_eq_u32 vcc,s60,v16
v_rcp_f32 v78,v78
v_mul_f32 v36,v36,v76
v_mfma_f32_16x16x32_bf16 v[28:31],a[0:3],v[60:63],v[28:31]
s_cselect_b32 vcc_lo,-1,vcc_lo
ds_read_b128 v[56:59],v2 offset:31744
v_mul_f32 v37,v37,v77
v_mfma_f32_16x16x32_bf16 v[32:35],a[4:7],v[60:63],v[32:35]
s_cmp_eq_u32 vcc_lo,-1
v_rcp_f32 v79,v79
v_mfma_f32_16x16x32_bf16 v[28:31],a[8:11],v[64:67],v[28:31]
s_cselect_b32 s67,s86,s59
ds_read_b128 v[60:63],v2 offset:24576
v_mul_f32 v38,v38,v78
v_mfma_f32_16x16x32_bf16 v[32:35],a[12:15],v[64:67],v[32:35]
s_add_u32 s53,s52,s55
v_mul_f32 v39,v39,v79
v_cvt_pk_bf16_f32 v84,v36,v37
v_mfma_f32_16x16x32_bf16 v[28:31],a[16:19],v[68:71],v[28:31]
s_cmp_eq_u32 s53,s56
ds_read_b128 v[64:67],v2 offset:25600
v_cvt_pk_bf16_f32 v85,v38,v39
v_mfma_f32_16x16x32_bf16 v[32:35],a[20:23],v[68:71],v[32:35]
s_cselect_b32 s53,s57,s53
v_fma_f32 v40,-v96,v24,v40
v_fma_f32 v41,-v96,v25,v41
v_mfma_f32_16x16x32_bf16 v[28:31],a[24:27],v[72:75],v[28:31]
s_cmp_eq_u32 s53,s58
ds_read_b128 v[68:71],v2 offset:26624
v_fma_f32 v42,-v96,v26,v42
v_mfma_f32_16x16x32_bf16 v[32:35],a[28:31],v[72:75],v[32:35]
s_waitcnt lgkmcnt(3)
v_fma_f32 v43,-v96,v27,v43
v_mul_f32 v80,v40,v40
v_mfma_f32_16x16x32_bf16 v[28:31],a[224:227],v[44:47],v[28:31]
s_cselect_b32 s53,s59,s53
ds_read_b128 v[72:75],v2 offset:27648
v_mul_f32 v81,v41,v41
v_mfma_f32_16x16x32_bf16 v[32:35],a[228:231],v[44:47],v[32:35]
s_xor_b32 s63,s63,65536
v_mul_f32 v82,v42,v42
v_mul_f32 v83,v43,v43
v_mfma_f32_16x16x32_bf16 v[104:107],v[100:103],v[44:47],v[104:107]
s_lshr_b32 s70,s46,5
v_fma_f32 v80,v98,v80,v97
v_mul_f32 v80,v40,v80
v_mfma_f32_16x16x32_bf16 v[108:111],v[44:47],v[44:47],v[108:111]
s_cmp_ge_u32 s66,s59
v_exp_f32 v80,v80
v_mfma_f32_16x16x32_bf16 v[28:31],a[232:235],v[48:51],v[28:31]
ds_read_b128 v[44:47],v2 offset:20480
v_fma_f32 v80,v80,v99,v99
v_mfma_f32_16x16x32_bf16 v[32:35],a[236:239],v[48:51],v[32:35]
v_rcp_f32 v80,v80
v_mfma_f32_16x16x32_bf16 v[104:107],v[100:103],v[48:51],v[104:107]
v_fma_f32 v81,v98,v81,v97
v_mul_f32 v81,v41,v81
v_mfma_f32_16x16x32_bf16 v[108:111],v[48:51],v[48:51],v[108:111]
v_exp_f32 v81,v81
v_mfma_f32_16x16x32_bf16 v[28:31],a[240:243],v[52:55],v[28:31]
ds_read_b128 v[48:51],v2 offset:21504
v_fma_f32 v81,v81,v99,v99
v_mfma_f32_16x16x32_bf16 v[32:35],a[244:247],v[52:55],v[32:35]
v_rcp_f32 v81,v81
v_mfma_f32_16x16x32_bf16 v[104:107],v[100:103],v[52:55],v[104:107]
v_fma_f32 v82,v98,v82,v97
v_mul_f32 v82,v42,v82
v_mfma_f32_16x16x32_bf16 v[108:111],v[52:55],v[52:55],v[108:111]
v_exp_f32 v82,v82
v_mfma_f32_16x16x32_bf16 v[28:31],a[248:251],v[56:59],v[28:31]
ds_read_b128 v[52:55],v2 offset:22528
v_fma_f32 v82,v82,v99,v99)"
R"(
v_mfma_f32_16x16x32_bf16 v[32:35],a[252:255],v[56:59],v[32:35]
v_rcp_f32 v82,v82
v_mfma_f32_16x16x32_bf16 v[104:107],v[100:103],v[56:59],v[104:107]
v_fma_f32 v83,v98,v83,v97
v_mul_f32 v83,v43,v83
v_mfma_f32_16x16x32_bf16 v[108:111],v[56:59],v[56:59],v[108:111]
s_waitcnt lgkmcnt(3)
v_exp_f32 v83,v83
v_mfma_f32_16x16x32_bf16 v[28:31],a[192:195],v[60:63],v[28:31]
ds_read_b128 v[56:59],v2 offset:23552
v_fma_f32 v83,v83,v99,v99
v_mfma_f32_16x16x32_bf16 v[32:35],a[196:199],v[60:63],v[32:35]
v_rcp_f32 v83,v83
v_mfma_f32_16x16x32_bf16 v[28:31],a[200:203],v[64:67],v[28:31]
v_cndmask_b32 v112,v108,v109,s[82:83]
v_cndmask_b32 v113,v110,v111,s[82:83]
v_cndmask_b32 v112,v112,v113,s[84:85]
v_cndmask_b32 v112,v104,v112,s[98:99]
v_mfma_f32_16x16x32_bf16 v[32:35],a[204:207],v[64:67],v[32:35]
ds_read_b128 v[60:63],v2 offset:16384
ds_write_b32 v13,v112 offset:0
buffer_load_dwordx4 v0,s[36:39],s67 offen offset:0 sc1 lds
v_mfma_f32_16x16x32_bf16 v[28:31],a[208:211],v[68:71],v[28:31]
ds_read_b128 v[64:67],v2 offset:17408
v_mul_f32 v40,v40,v80
v_mfma_f32_16x16x32_bf16 v[32:35],a[212:215],v[68:71],v[32:35]
v_mul_f32 v41,v41,v81
v_mul_f32 v42,v42,v82
v_mfma_f32_16x16x32_bf16 v[28:31],a[216:219],v[72:75],v[28:31]
ds_read_b128 v[68:71],v2 offset:18432
v_mul_f32 v43,v43,v83
v_mfma_f32_16x16x32_bf16 v[32:35],a[220:223],v[72:75],v[32:35]
s_waitcnt lgkmcnt(4)
buffer_load_dwordx4 v0,s[36:39],s67 offen offset:1024 sc1 lds
v_mfma_f32_16x16x32_bf16 v[28:31],a[160:163],v[44:47],v[28:31]
s_waitcnt vmcnt(7) lgkmcnt(2)
ds_read_b128 v[72:75],v2 offset:19456
v_cvt_pk_bf16_f32 v86,v40,v41
v_mfma_f32_16x16x32_bf16 v[32:35],a[164:167],v[44:47],v[32:35]
s_barrier
v_mfma_f32_16x16x32_bf16 v[28:31],a[168:171],v[48:51],v[28:31]
buffer_store_dword v5,v4,s[36:39],s70 offen offset:0 sc1
ds_read_b128 v[116:119],v14 offset:0
ds_read_b128 v[120:123],v14 offset:16
ds_read_b128 v[44:47],v2 offset:45056
v_mfma_f32_16x16x32_bf16 v[32:35],a[172:175],v[48:51],v[32:35]
v_cvt_pk_bf16_f32 v87,v42,v43
v_mfma_f32_16x16x32_bf16 v[28:31],a[176:179],v[52:55],v[28:31]
ds_read_b128 v[48:51],v2 offset:46080
v_mfma_f32_16x16x32_bf16 v[32:35],a[180:183],v[52:55],v[32:35]
buffer_load_dwordx4 v0,s[36:39],s67 offen offset:2048 sc1 lds
v_mfma_f32_16x16x32_bf16 v[28:31],a[184:187],v[56:59],v[28:31]
s_waitcnt lgkmcnt(2)
ds_read_b128 v[52:55],v2 offset:47104
v_add_f32 v116,v116,v117
v_mfma_f32_16x16x32_bf16 v[32:35],a[188:191],v[56:59],v[32:35]
v_add_f32 v118,v118,v119
v_add_f32 v116,v116,v118
v_mfma_f32_16x16x32_bf16 v[28:31],a[128:131],v[60:63],v[28:31]
ds_read_b128 v[56:59],v2 offset:48128
v_mul_f32 v92,s75,v116
v_mfma_f32_16x16x32_bf16 v[32:35],a[132:135],v[60:63],v[32:35]
buffer_load_dwordx4 v0,s[36:39],s67 offen offset:3072 sc1 lds
v_mfma_f32_16x16x32_bf16 v[28:31],a[136:139],v[64:67],v[28:31]
ds_read_b128 v[60:63],v2 offset:40960
v_add_f32 v120,v120,v121
v_mfma_f32_16x16x32_bf16 v[32:35],a[140:143],v[64:67],v[32:35]
v_add_f32 v122,v122,v123
v_add_f32 v120,v120,v122
v_mfma_f32_16x16x32_bf16 v[28:31],a[144:147],v[68:71],v[28:31]
ds_read_b128 v[64:67],v2 offset:41984
v_mul_f32 v120,s75,v120
v_mfma_f32_16x16x32_bf16 v[32:35],a[148:151],v[68:71],v[32:35]
buffer_store_dwordx4 v[84:87],v3,s[36:39],s73 offen offset:0 sc1
v_fma_f32 v120,-v92,v92,v120
v_max_f32 v120,0,v120
v_add_f32 v115,v15,v120
v_fma_f32 v120,v15,v115,v120
v_rsq_f32 v116,v120
v_mfma_f32_16x16x32_bf16 v[28:31],a[152:155],v[72:75],v[28:31]
s_waitcnt lgkmcnt(2)
ds_read_b128 v[68:71],v2 offset:43008
v_mul_f32 v93,s76,v116
v_mul_f32 v94,v116,v116
v_mul_f32 v95,v120,v116
v_mul_f32 v94,s77,v94
v_mul_f32 v94,v94,v93
v_mfma_f32_16x16x32_bf16 v[32:35],a[156:159],v[72:75],v[32:35]
s_cbranch_scc1 Lg1_slow1_%=
Lg1_a1w3_%=:
v_mfma_f32_16x16x32_bf16 v[36:39],a[96:99],v[44:47],0
s_sub_u32 s69,s67,1048576
ds_read_b128 v[72:75],v2 offset:44032
s_cmp_lt_u32 s69,2097152
v_mfma_f32_16x16x32_bf16 v[40:43],a[100:103],v[44:47],0
s_cselect_b32 s89,3145728,s89
s_sub_u32 s69,s67,3145728
s_cmp_lt_u32 s69,1048576
v_mfma_f32_16x16x32_bf16 v[104:107],v[100:103],v[44:47],0
s_cselect_b32 s89,4194304,s89
s_add_u32 m0,s62,16384
s_lshr_b32 s73,s48,0
v_mfma_f32_16x16x32_bf16 v[108:111],v[44:47],v[44:47],0
s_lshr_b32 s72,s67,6
v_fma_f32 v28,-v92,v20,v28
v_fma_f32 v29,-v92,v21,v29
v_mfma_f32_16x16x32_bf16 v[36:39],a[104:107],v[48:51],v[36:39]
buffer_load_dwordx4 v1,s[36:39],s67 offen offset:0 sc1 lds
v_mfma_f32_16x16x32_bf16 v[40:43],a[108:111],v[48:51],v[40:43]
s_lshr_b32 s71,s62,2
ds_read_b128 v[44:47],v2 offset:36864
v_fma_f32 v30,-v92,v22,v30
v_mfma_f32_16x16x32_bf16 v[104:107],v[100:103],v[48:51],v[104:107]
s_cmp_lt_u32 s67,4194304
v_fma_f32 v31,-v92,v23,v31
v_mul_f32 v76,v28,v28
v_mfma_f32_16x16x32_bf16 v[108:111],v[48:51],v[48:51],v[108:111]
s_cselect_b32 s72,s59,s72
v_mul_f32 v77,v29,v29
v_mul_f32 v78,v30,v30
v_mfma_f32_16x16x32_bf16 v[36:39],a[112:115],v[52:55],v[36:39]
buffer_load_dwordx4 v1,s[36:39],s67 offen offset:1024 sc1 lds
v_mfma_f32_16x16x32_bf16 v[40:43],a[116:119],v[52:55],v[40:43]
s_mov_b64 s[44:45],s[46:47]
ds_read_b128 v[48:51],v2 offset:37888
v_mul_f32 v79,v31,v31
v_mfma_f32_16x16x32_bf16 v[104:107],v[100:103],v[52:55],v[104:107]
v_fma_f32 v76,v94,v76,v93
v_mul_f32 v76,v28,v76
v_mfma_f32_16x16x32_bf16 v[108:111],v[52:55],v[52:55],v[108:111]
v_exp_f32 v76,v76
v_mfma_f32_16x16x32_bf16 v[36:39],a[120:123],v[56:59],v[36:39]
buffer_load_dwordx4 v1,s[36:39],s67 offen offset:2048 sc1 lds
v_mfma_f32_16x16x32_bf16 v[40:43],a[124:127],v[56:59],v[40:43]
ds_read_b128 v[52:55],v2 offset:38912
v_fma_f32 v76,v76,v95,v95
v_mfma_f32_16x16x32_bf16 v[104:107],v[100:103],v[56:59],v[104:107]
v_rcp_f32 v76,v76
v_mfma_f32_16x16x32_bf16 v[108:111],v[56:59],v[56:59],v[108:111]
s_waitcnt lgkmcnt(3)
v_fma_f32 v77,v94,v77,v93
v_mul_f32 v77,v29,v77
v_mfma_f32_16x16x32_bf16 v[36:39],a[64:67],v[60:63],v[36:39]
buffer_load_dwordx4 v1,s[36:39],s67 offen offset:3072 sc1 lds
v_mfma_f32_16x16x32_bf16 v[40:43],a[68:71],v[60:63],v[40:43]
s_add_u32 m0,s71,s88
ds_read_b128 v[56:59],v2 offset:39936
v_fma_f32 v78,v94,v78,v93
v_mfma_f32_16x16x32_bf16 v[36:39],a[72:75],v[64:67],v[36:39]
buffer_load_dword v10,s[36:39],s72 offen offset:0 sc1 lds
v_mfma_f32_16x16x32_bf16 v[40:43],a[76:79],v[64:67],v[40:43]
s_xor_b32 s87,s63,65536
ds_read_b128 v[60:63],v2 offset:32768
v_mul_f32 v78,v30,v78
v_mfma_f32_16x16x32_bf16 v[36:39],a[80:83],v[68:71],v[36:39]
s_cmp_ge_u32 s67,s59
ds_read_b128 v[64:67],v2 offset:33792
v_fma_f32 v79,v94,v79,v93
v_mfma_f32_16x16x32_bf16 v[40:43],a[84:87],v[68:71],v[40:43]
s_cselect_b32 s86,s50,s51
v_exp_f32 v77,v77
v_mfma_f32_16x16x32_bf16 v[36:39],a[88:91],v[72:75],v[36:39]
s_cselect_b32 s87,s87,s64
ds_read_b128 v[68:71],v2 offset:34816
v_mul_f32 v79,v31,v79
v_mfma_f32_16x16x32_bf16 v[40:43],a[92:95],v[72:75],v[40:43]
s_waitcnt lgkmcnt(3)
v_exp_f32 v78,v78
v_mfma_f32_16x16x32_bf16 v[36:39],a[32:35],v[44:47],v[36:39]
s_waitcnt vmcnt(11)
ds_read_b128 v[72:75],v2 offset:35840
v_fma_f32 v77,v77,v95,v95
v_mfma_f32_16x16x32_bf16 v[40:43],a[36:39],v[44:47],v[40:43]
s_barrier
v_mfma_f32_16x16x32_bf16 v[36:39],a[40:43],v[48:51],v[36:39]
s_mov_b32 s62,s87
ds_read_b128 v[44:47],v2 offset:61440
ds_read_b32 v16,v7 offset:256
v_mfma_f32_16x16x32_bf16 v[40:43],a[44:47],v[48:51],v[40:43]
s_cmp_lt_u32 s86,s89
v_exp_f32 v79,v79
v_mfma_f32_16x16x32_bf16 v[36:39],a[48:51],v[52:55],v[36:39]
s_mov_b32 m0,s62
ds_read_b128 v[48:51],v2 offset:62464
v_fma_f32 v78,v78,v95,v95
v_mfma_f32_16x16x32_bf16 v[40:43],a[52:55],v[52:55],v[40:43]
v_rcp_f32 v77,v77
v_mfma_f32_16x16x32_bf16 v[36:39],a[56:59],v[56:59],v[36:39]
s_waitcnt lgkmcnt(1)
ds_read_b128 v[52:55],v2 offset:63488
v_fma_f32 v79,v79,v95,v95
v_mfma_f32_16x16x32_bf16 v[40:43],a[60:63],v[56:59],v[40:43]
v_cmp_eq_u32 vcc,s60,v16
v_rcp_f32 v78,v78
v_mul_f32 v28,v28,v76
v_mfma_f32_16x16x32_bf16 v[36:39],a[0:3],v[60:63],v[36:39]
s_cselect_b32 vcc_lo,-1,vcc_lo
ds_read_b128 v[56:59],v2 offset:64512
v_mul_f32 v29,v29,v77
v_mfma_f32_16x16x32_bf16 v[40:43],a[4:7],v[60:63],v[40:43])"
R"(
s_cmp_eq_u32 vcc_lo,-1
v_rcp_f32 v79,v79
v_mfma_f32_16x16x32_bf16 v[36:39],a[8:11],v[64:67],v[36:39]
s_cselect_b32 s66,s86,s59
ds_read_b128 v[60:63],v2 offset:57344
v_mul_f32 v30,v30,v78
v_mfma_f32_16x16x32_bf16 v[40:43],a[12:15],v[64:67],v[40:43]
s_add_u32 s74,s53,s55
v_mul_f32 v31,v31,v79
v_cvt_pk_bf16_f32 v84,v28,v29
v_mfma_f32_16x16x32_bf16 v[36:39],a[16:19],v[68:71],v[36:39]
s_cmp_eq_u32 s74,s56
ds_read_b128 v[64:67],v2 offset:58368
v_cvt_pk_bf16_f32 v85,v30,v31
v_mfma_f32_16x16x32_bf16 v[40:43],a[20:23],v[68:71],v[40:43]
s_cselect_b32 s74,s57,s74
v_fma_f32 v32,-v92,v24,v32
v_fma_f32 v33,-v92,v25,v33
v_mfma_f32_16x16x32_bf16 v[36:39],a[24:27],v[72:75],v[36:39]
s_cmp_eq_u32 s74,s58
ds_read_b128 v[68:71],v2 offset:59392
v_fma_f32 v34,-v92,v26,v34
v_mfma_f32_16x16x32_bf16 v[40:43],a[28:31],v[72:75],v[40:43]
s_waitcnt lgkmcnt(3)
v_fma_f32 v35,-v92,v27,v35
v_mul_f32 v80,v32,v32
v_mfma_f32_16x16x32_bf16 v[36:39],a[224:227],v[44:47],v[36:39]
s_cselect_b32 s74,s59,s74
ds_read_b128 v[72:75],v2 offset:60416
v_mul_f32 v81,v33,v33
v_mfma_f32_16x16x32_bf16 v[40:43],a[228:231],v[44:47],v[40:43]
s_xor_b32 s64,s64,65536
v_mul_f32 v82,v34,v34
v_mul_f32 v83,v35,v35
v_mfma_f32_16x16x32_bf16 v[104:107],v[100:103],v[44:47],v[104:107]
s_lshr_b32 s70,s47,5
v_fma_f32 v80,v94,v80,v93
v_mul_f32 v80,v32,v80
v_mfma_f32_16x16x32_bf16 v[108:111],v[44:47],v[44:47],v[108:111]
s_mov_b64 s[46:47],s[48:49]
v_exp_f32 v80,v80
v_mfma_f32_16x16x32_bf16 v[36:39],a[232:235],v[48:51],v[36:39]
s_mov_b64 s[48:49],s[50:51]
ds_read_b128 v[44:47],v2 offset:53248
v_fma_f32 v80,v80,v95,v95
v_mfma_f32_16x16x32_bf16 v[40:43],a[236:239],v[48:51],v[40:43]
s_mov_b64 s[50:51],s[52:53]
v_rcp_f32 v80,v80
v_mfma_f32_16x16x32_bf16 v[104:107],v[100:103],v[48:51],v[104:107]
s_mov_b32 s52,s74
v_fma_f32 v81,v94,v81,v93
v_mul_f32 v81,v33,v81
v_mfma_f32_16x16x32_bf16 v[108:111],v[48:51],v[48:51],v[108:111]
s_cmp_lt_u32 s67,4194304
v_exp_f32 v81,v81
v_mfma_f32_16x16x32_bf16 v[36:39],a[240:243],v[52:55],v[36:39]
ds_read_b128 v[48:51],v2 offset:54272
v_fma_f32 v81,v81,v95,v95
v_mfma_f32_16x16x32_bf16 v[40:43],a[244:247],v[52:55],v[40:43]
v_rcp_f32 v81,v81
v_mfma_f32_16x16x32_bf16 v[104:107],v[100:103],v[52:55],v[104:107]
v_fma_f32 v82,v94,v82,v93
v_mul_f32 v82,v34,v82
v_mfma_f32_16x16x32_bf16 v[108:111],v[52:55],v[52:55],v[108:111]
v_exp_f32 v82,v82
v_mfma_f32_16x16x32_bf16 v[36:39],a[248:251],v[56:59],v[36:39]
ds_read_b128 v[52:55],v2 offset:55296
v_fma_f32 v82,v82,v95,v95
v_mfma_f32_16x16x32_bf16 v[40:43],a[252:255],v[56:59],v[40:43]
v_rcp_f32 v82,v82
v_mfma_f32_16x16x32_bf16 v[104:107],v[100:103],v[56:59],v[104:107]
v_fma_f32 v83,v94,v83,v93
v_mul_f32 v83,v35,v83
v_mfma_f32_16x16x32_bf16 v[108:111],v[56:59],v[56:59],v[108:111]
s_waitcnt lgkmcnt(3)
v_exp_f32 v83,v83
v_mfma_f32_16x16x32_bf16 v[36:39],a[192:195],v[60:63],v[36:39]
ds_read_b128 v[56:59],v2 offset:56320
v_fma_f32 v83,v83,v95,v95
v_mfma_f32_16x16x32_bf16 v[40:43],a[196:199],v[60:63],v[40:43]
v_rcp_f32 v83,v83
v_mfma_f32_16x16x32_bf16 v[36:39],a[200:203],v[64:67],v[36:39]
v_cndmask_b32 v112,v108,v109,s[82:83]
v_cndmask_b32 v113,v110,v111,s[82:83]
v_cndmask_b32 v112,v112,v113,s[84:85]
v_cndmask_b32 v112,v104,v112,s[98:99]
v_mfma_f32_16x16x32_bf16 v[40:43],a[204:207],v[64:67],v[40:43]
ds_read_b128 v[60:63],v2 offset:49152
ds_write_b32 v13,v112 offset:2048
buffer_load_dwordx4 v0,s[36:39],s66 offen offset:0 sc1 lds
v_mfma_f32_16x16x32_bf16 v[36:39],a[208:211],v[68:71],v[36:39]
ds_read_b128 v[64:67],v2 offset:50176
v_mul_f32 v32,v32,v80
v_mfma_f32_16x16x32_bf16 v[40:43],a[212:215],v[68:71],v[40:43]
v_mul_f32 v33,v33,v81
v_mul_f32 v34,v34,v82
v_mfma_f32_16x16x32_bf16 v[36:39],a[216:219],v[72:75],v[36:39]
ds_read_b128 v[68:71],v2 offset:51200
v_mul_f32 v35,v35,v83
v_mfma_f32_16x16x32_bf16 v[40:43],a[220:223],v[72:75],v[40:43]
s_waitcnt lgkmcnt(4)
buffer_load_dwordx4 v0,s[36:39],s66 offen offset:1024 sc1 lds
v_mfma_f32_16x16x32_bf16 v[36:39],a[160:163],v[44:47],v[36:39]
s_waitcnt vmcnt(7) lgkmcnt(2)
ds_read_b128 v[72:75],v2 offset:52224
v_xor_b32 v2,65536,v2
v_mfma_f32_16x16x32_bf16 v[40:43],a[164:167],v[44:47],v[40:43]
s_barrier
v_mfma_f32_16x16x32_bf16 v[36:39],a[168:171],v[48:51],v[36:39]
buffer_store_dword v5,v4,s[36:39],s70 offen offset:0 sc1
ds_read_b128 v[116:119],v14 offset:2048
ds_read_b128 v[120:123],v14 offset:2064
ds_read_b128 v[44:47],v2 offset:12288
v_mfma_f32_16x16x32_bf16 v[40:43],a[172:175],v[48:51],v[40:43]
v_cvt_pk_bf16_f32 v86,v32,v33
v_cvt_pk_bf16_f32 v87,v34,v35
v_mfma_f32_16x16x32_bf16 v[36:39],a[176:179],v[52:55],v[36:39]
ds_read_b128 v[48:51],v2 offset:13312
v_xor_b32 v8,16384,v8
v_mfma_f32_16x16x32_bf16 v[40:43],a[180:183],v[52:55],v[40:43]
buffer_load_dwordx4 v0,s[36:39],s66 offen offset:2048 sc1 lds
v_mfma_f32_16x16x32_bf16 v[36:39],a[184:187],v[56:59],v[36:39]
s_waitcnt lgkmcnt(2)
ds_read_b128 v[52:55],v2 offset:14336
v_add_f32 v116,v116,v117
v_mfma_f32_16x16x32_bf16 v[40:43],a[188:191],v[56:59],v[40:43]
v_add_f32 v118,v118,v119
v_add_f32 v116,v116,v118
v_mfma_f32_16x16x32_bf16 v[36:39],a[128:131],v[60:63],v[36:39]
ds_read_b128 v[56:59],v2 offset:15360
v_mul_f32 v96,s75,v116
v_mfma_f32_16x16x32_bf16 v[40:43],a[132:135],v[60:63],v[40:43]
buffer_load_dwordx4 v0,s[36:39],s66 offen offset:3072 sc1 lds
v_mfma_f32_16x16x32_bf16 v[36:39],a[136:139],v[64:67],v[36:39]
ds_read_b128 v[60:63],v2 offset:8192
v_add_f32 v120,v120,v121
v_mfma_f32_16x16x32_bf16 v[40:43],a[140:143],v[64:67],v[40:43]
v_add_f32 v122,v122,v123
v_add_f32 v120,v120,v122
v_mfma_f32_16x16x32_bf16 v[36:39],a[144:147],v[68:71],v[36:39]
ds_read_b128 v[64:67],v2 offset:9216
v_mul_f32 v120,s75,v120
v_mfma_f32_16x16x32_bf16 v[40:43],a[148:151],v[68:71],v[40:43]
buffer_store_dwordx4 v[84:87],v3,s[36:39],s73 offen offset:0 sc1
v_fma_f32 v120,-v96,v96,v120
v_max_f32 v120,0,v120
v_add_f32 v115,v15,v120
v_fma_f32 v120,v15,v115,v120
v_rsq_f32 v116,v120
v_mfma_f32_16x16x32_bf16 v[36:39],a[152:155],v[72:75],v[36:39]
s_waitcnt lgkmcnt(2)
ds_read_b128 v[68:71],v2 offset:10240
v_mul_f32 v97,s76,v116
v_mul_f32 v98,v116,v116
v_mul_f32 v99,v120,v116
v_mul_f32 v98,s77,v98
v_mul_f32 v98,v98,v97
v_mfma_f32_16x16x32_bf16 v[40:43],a[156:159],v[72:75],v[40:43]
s_cbranch_scc1 Lg1_a0w3_%=
s_cmp_ge_u32 s48,4194304
s_cselect_b64 s[90:91],s[94:95],s[90:91]
s_cselect_b64 s[92:93],s[96:97],s[92:93]
s_cmp_lt_u32 s67,s59
s_cbranch_scc1 Lg1_d0w3_%=
s_branch Lg1_slow0_%=
Lg1_d0w0_%=:
v_mfma_f32_16x16x32_bf16 v[28:31],a[0:3],v[44:47],0
s_add_u32 m0,s62,16384
ds_read_b128 v[72:75],v2 offset:7168
s_lshr_b32 s73,s47,0
v_mfma_f32_16x16x32_bf16 v[32:35],a[4:7],v[44:47],0
buffer_load_dwordx4 v1,s[36:39],s66 offen offset:0 sc1 lds
v_mfma_f32_16x16x32_bf16 v[28:31],a[8:11],v[48:51],v[28:31]
s_lshr_b32 s72,s66,6
ds_read_b128 v[44:47],v2 offset:8192
s_lshr_b32 s71,s62,2
v_mfma_f32_16x16x32_bf16 v[32:35],a[12:15],v[48:51],v[32:35]
s_cmp_lt_u32 s66,4194304
v_fma_f32 v36,-v96,v20,v36
v_fma_f32 v37,-v96,v21,v37
v_mfma_f32_16x16x32_bf16 v[28:31],a[16:19],v[52:55],v[28:31]
s_cselect_b32 s72,s59,s72
ds_read_b128 v[48:51],v2 offset:9216
v_fma_f32 v38,-v96,v22,v38
v_mfma_f32_16x16x32_bf16 v[32:35],a[20:23],v[52:55],v[32:35]
buffer_load_dwordx4 v1,s[36:39],s66 offen offset:1024 sc1 lds
v_mfma_f32_16x16x32_bf16 v[28:31],a[24:27],v[56:59],v[28:31]
ds_read_b128 v[52:55],v2 offset:10240
v_fma_f32 v39,-v96,v23,v39
v_mfma_f32_16x16x32_bf16 v[32:35],a[28:31],v[56:59],v[32:35]
s_waitcnt lgkmcnt(3)
v_mul_f32 v76,v36,v36
v_mul_f32 v77,v37,v37
v_mfma_f32_16x16x32_bf16 v[28:31],a[32:35],v[60:63],v[28:31]
ds_read_b128 v[56:59],v2 offset:11264
v_mul_f32 v78,v38,v38
v_mfma_f32_16x16x32_bf16 v[32:35],a[36:39],v[60:63],v[32:35]
buffer_load_dwordx4 v1,s[36:39],s66 offen offset:2048 sc1 lds
v_mfma_f32_16x16x32_bf16 v[28:31],a[40:43],v[64:67],v[28:31]
ds_read_b128 v[60:63],v2 offset:12288)"
R"(
v_mul_f32 v79,v39,v39
v_mfma_f32_16x16x32_bf16 v[32:35],a[44:47],v[64:67],v[32:35]
v_fma_f32 v76,v98,v76,v97
v_mul_f32 v76,v36,v76
v_mfma_f32_16x16x32_bf16 v[28:31],a[48:51],v[68:71],v[28:31]
ds_read_b128 v[64:67],v2 offset:13312
v_fma_f32 v77,v98,v77,v97
v_mfma_f32_16x16x32_bf16 v[32:35],a[52:55],v[68:71],v[32:35]
buffer_load_dwordx4 v1,s[36:39],s66 offen offset:3072 sc1 lds
v_mfma_f32_16x16x32_bf16 v[28:31],a[56:59],v[72:75],v[28:31]
s_add_u32 m0,s71,s88
ds_read_b128 v[68:71],v2 offset:14336
v_mul_f32 v77,v37,v77
v_mfma_f32_16x16x32_bf16 v[32:35],a[60:63],v[72:75],v[32:35]
s_waitcnt lgkmcnt(3)
buffer_load_dword v10,s[36:39],s72 offen offset:0 sc1 lds
v_mfma_f32_16x16x32_bf16 v[28:31],a[64:67],v[44:47],v[28:31]
s_waitcnt vmcnt(11)
ds_read_b128 v[72:75],v2 offset:15360
v_fma_f32 v78,v98,v78,v97
v_mfma_f32_16x16x32_bf16 v[32:35],a[68:71],v[44:47],v[32:35]
s_barrier
v_mfma_f32_16x16x32_bf16 v[28:31],a[72:75],v[48:51],v[28:31]
s_xor_b32 s87,s64,65536
ds_read_b128 v[44:47],v2 offset:16384
v_fma_f32 v79,v98,v79,v97
v_mfma_f32_16x16x32_bf16 v[32:35],a[76:79],v[48:51],v[32:35]
s_cmp_ge_u32 s66,s59
v_exp_f32 v76,v76
v_mfma_f32_16x16x32_bf16 v[28:31],a[80:83],v[52:55],v[28:31]
s_cselect_b32 s86,s49,s50
ds_read_b128 v[48:51],v2 offset:17408
v_mul_f32 v78,v38,v78
v_mfma_f32_16x16x32_bf16 v[32:35],a[84:87],v[52:55],v[32:35]
s_cselect_b32 s87,s87,s63
v_exp_f32 v77,v77
v_mfma_f32_16x16x32_bf16 v[28:31],a[88:91],v[56:59],v[28:31]
s_lshr_b32 s69,s50,5
ds_read_b128 v[52:55],v2 offset:18432
v_mul_f32 v79,v39,v79
v_mfma_f32_16x16x32_bf16 v[32:35],a[92:95],v[56:59],v[32:35]
s_waitcnt lgkmcnt(3)
v_exp_f32 v78,v78
v_mfma_f32_16x16x32_bf16 v[28:31],a[96:99],v[60:63],v[28:31]
s_cmp_lt_u32 s50,4194304
ds_read_b128 v[56:59],v2 offset:19456
v_fma_f32 v76,v76,v99,v99
v_mfma_f32_16x16x32_bf16 v[32:35],a[100:103],v[60:63],v[32:35]
s_cselect_b32 s68,0xffffcfff,-1
v_exp_f32 v79,v79
v_mfma_f32_16x16x32_bf16 v[28:31],a[104:107],v[64:67],v[28:31]
s_and_b32 s68,s68,s69
ds_read_b128 v[60:63],v2 offset:20480
v_fma_f32 v77,v77,v99,v99
v_mfma_f32_16x16x32_bf16 v[32:35],a[108:111],v[64:67],v[32:35]
s_lshr_b32 s69,s51,5
v_rcp_f32 v76,v76
v_mfma_f32_16x16x32_bf16 v[28:31],a[112:115],v[68:71],v[28:31]
s_cmp_lt_u32 s51,4194304
ds_read_b128 v[64:67],v2 offset:21504
v_fma_f32 v78,v78,v99,v99
v_mfma_f32_16x16x32_bf16 v[32:35],a[116:119],v[68:71],v[32:35]
s_cselect_b32 s70,0xffffcfff,-1
v_rcp_f32 v77,v77
v_mfma_f32_16x16x32_bf16 v[28:31],a[120:123],v[72:75],v[28:31]
s_and_b32 s70,s70,s69
ds_read_b128 v[68:71],v2 offset:22528
v_fma_f32 v79,v79,v99,v99
v_mfma_f32_16x16x32_bf16 v[32:35],a[124:127],v[72:75],v[32:35]
s_waitcnt lgkmcnt(3)
v_rcp_f32 v78,v78
v_mfma_f32_16x16x32_bf16 v[28:31],a[128:131],v[44:47],v[28:31]
s_waitcnt vmcnt(10)
ds_read_b128 v[72:75],v2 offset:23552
v_mul_f32 v36,v36,v76
v_mfma_f32_16x16x32_bf16 v[32:35],a[132:135],v[44:47],v[32:35]
s_barrier
v_mfma_f32_16x16x32_bf16 v[28:31],a[136:139],v[48:51],v[28:31]
s_mov_b32 s62,s87
ds_read_b128 v[44:47],v2 offset:24576
ds_read_b32 v16,v7 offset:0
v_mfma_f32_16x16x32_bf16 v[32:35],a[140:143],v[48:51],v[32:35]
v_rcp_f32 v79,v79
v_mfma_f32_16x16x32_bf16 v[28:31],a[144:147],v[52:55],v[28:31]
ds_read_b128 v[48:51],v2 offset:25600
v_mul_f32 v37,v37,v77
v_mfma_f32_16x16x32_bf16 v[32:35],a[148:151],v[52:55],v[32:35]
v_mul_f32 v38,v38,v78
v_mul_f32 v39,v39,v79
v_mfma_f32_16x16x32_bf16 v[28:31],a[152:155],v[56:59],v[28:31]
s_waitcnt lgkmcnt(1)
ds_read_b128 v[52:55],v2 offset:26624
v_cvt_pk_bf16_f32 v84,v36,v37
v_mfma_f32_16x16x32_bf16 v[32:35],a[156:159],v[56:59],v[32:35]
v_cmp_eq_u32 vcc,s60,v16
v_cvt_pk_bf16_f32 v85,v38,v39
v_fma_f32 v40,-v96,v24,v40
v_fma_f32 v41,-v96,v25,v41
v_mfma_f32_16x16x32_bf16 v[28:31],a[160:163],v[60:63],v[28:31]
s_cmp_eq_u32 vcc_lo,-1
ds_read_b128 v[56:59],v2 offset:27648
v_fma_f32 v42,-v96,v26,v42
v_mfma_f32_16x16x32_bf16 v[32:35],a[164:167],v[60:63],v[32:35]
s_cselect_b32 s67,s86,s59
s_cmp_ge_u32 s67,s59
s_cselect_b32 s68,s68,s70
s_add_u32 m0,s78,256
v_mfma_f32_16x16x32_bf16 v[28:31],a[168:171],v[64:67],v[28:31]
buffer_load_dword v6,s[36:39],s68 offen offset:0 sc1 lds
v_mfma_f32_16x16x32_bf16 v[32:35],a[172:175],v[64:67],v[32:35]
s_mov_b32 m0,s62
ds_read_b128 v[60:63],v2 offset:28672
v_fma_f32 v43,-v96,v27,v43
v_mfma_f32_16x16x32_bf16 v[28:31],a[176:179],v[68:71],v[28:31]
buffer_load_dwordx4 v0,s[36:39],s67 offen offset:0 sc1 lds
v_mfma_f32_16x16x32_bf16 v[32:35],a[180:183],v[68:71],v[32:35]
s_xor_b32 s63,s63,65536
ds_read_b128 v[64:67],v2 offset:29696
v_mul_f32 v80,v40,v40
v_mfma_f32_16x16x32_bf16 v[28:31],a[184:187],v[72:75],v[28:31]
s_add_u32 s53,s52,s55
ds_read_b128 v[68:71],v2 offset:30720
v_mul_f32 v81,v41,v41
v_mfma_f32_16x16x32_bf16 v[32:35],a[188:191],v[72:75],v[32:35]
s_waitcnt lgkmcnt(3)
v_mul_f32 v82,v42,v42
v_mul_f32 v83,v43,v43
v_mfma_f32_16x16x32_bf16 v[28:31],a[192:195],v[44:47],v[28:31]
s_waitcnt vmcnt(7)
ds_read_b128 v[72:75],v2 offset:31744
v_fma_f32 v80,v98,v80,v97
v_mfma_f32_16x16x32_bf16 v[32:35],a[196:199],v[44:47],v[32:35]
s_barrier
v_mfma_f32_16x16x32_bf16 v[28:31],a[200:203],v[48:51],v[28:31]
buffer_load_dwordx4 v0,s[36:39],s67 offen offset:1024 sc1 lds
v_mfma_f32_16x16x32_bf16 v[32:35],a[204:207],v[48:51],v[32:35]
s_cmp_eq_u32 s53,s56
ds_read_b128 v[44:47],v2 offset:32768
ds_read_b128 v[92:95],v8 offset:0
v_mfma_f32_16x16x32_bf16 v[28:31],a[208:211],v[52:55],v[28:31]
s_cselect_b32 s53,s57,s53
ds_read_b128 v[48:51],v2 offset:33792
v_mul_f32 v80,v40,v80
v_mfma_f32_16x16x32_bf16 v[32:35],a[212:215],v[52:55],v[32:35]
s_cmp_eq_u32 s53,s58
v_fma_f32 v81,v98,v81,v97
v_fma_f32 v82,v98,v82,v97
v_fma_f32 v83,v98,v83,v97
v_mul_f32 v81,v41,v81
v_mul_f32 v82,v42,v82
v_mul_f32 v83,v43,v83
v_exp_f32 v80,v80
v_exp_f32 v81,v81
v_exp_f32 v82,v82
v_exp_f32 v83,v83
v_mfma_f32_16x16x32_bf16 v[28:31],a[216:219],v[56:59],v[28:31]
buffer_load_dwordx4 v0,s[36:39],s67 offen offset:2048 sc1 lds
v_fma_f32 v80,v80,v99,v99
v_fma_f32 v81,v81,v99,v99
v_fma_f32 v82,v82,v99,v99
v_fma_f32 v83,v83,v99,v99
v_rcp_f32 v80,v80
v_rcp_f32 v81,v81
v_rcp_f32 v82,v82
v_rcp_f32 v83,v83
v_mfma_f32_16x16x32_bf16 v[32:35],a[220:223],v[56:59],v[32:35]
s_waitcnt lgkmcnt(3)
ds_read_b128 v[52:55],v2 offset:34816
v_mul_f32 v40,v40,v80
v_mul_f32 v41,v41,v81
v_mul_f32 v42,v42,v82
v_mul_f32 v43,v43,v83
v_cvt_pk_bf16_f32 v86,v40,v41
v_cvt_pk_bf16_f32 v87,v42,v43
v_mfma_f32_16x16x32_bf16 v[28:31],a[224:227],v[60:63],v[28:31]
buffer_store_dwordx4 v[84:87],v3,s[36:39],s73 offen offset:0 sc1
ds_read_b128 v[56:59],v2 offset:35840
v_mfma_f32_16x16x32_bf16 v[32:35],a[228:231],v[60:63],v[32:35]
s_waitcnt lgkmcnt(3)
s_cselect_b32 s53,s59,s53
s_cmp_ge_u32 s66,s59
v_mfma_f32_16x16x32_bf16 v[28:31],a[232:235],v[64:67],v[28:31]
buffer_load_dwordx4 v0,s[36:39],s67 offen offset:3072 sc1 lds
v_mfma_f32_16x16x32_bf16 v[32:35],a[236:239],v[64:67],v[32:35]
ds_read_b128 v[60:63],v2 offset:36864
v_mfma_f32_16x16x32_bf16 v[28:31],a[240:243],v[68:71],v[28:31]
ds_read_b128 v[64:67],v2 offset:37888
v_mfma_f32_16x16x32_bf16 v[32:35],a[244:247],v[68:71],v[32:35]
v_mfma_f32_16x16x32_bf16 v[28:31],a[248:251],v[72:75],v[28:31]
s_waitcnt lgkmcnt(2)
ds_read_b128 v[68:71],v2 offset:38912
v_mfma_f32_16x16x32_bf16 v[32:35],a[252:255],v[72:75],v[32:35]
s_cbranch_scc1 Lg1_slow1_%=
Lg1_d1w0_%=:
v_mfma_f32_16x16x32_bf16 v[36:39],a[0:3],v[44:47],0
s_add_u32 m0,s62,16384
ds_read_b128 v[72:75],v2 offset:39936
s_lshr_b32 s73,s48,0
v_mfma_f32_16x16x32_bf16 v[40:43],a[4:7],v[44:47],0
buffer_load_dwordx4 v1,s[36:39],s67 offen offset:0 sc1 lds)"
R"(
v_mfma_f32_16x16x32_bf16 v[36:39],a[8:11],v[48:51],v[36:39]
s_lshr_b32 s72,s67,6
ds_read_b128 v[44:47],v2 offset:40960
s_lshr_b32 s71,s62,2
v_mfma_f32_16x16x32_bf16 v[40:43],a[12:15],v[48:51],v[40:43]
s_cmp_lt_u32 s67,4194304
v_fma_f32 v28,-v92,v20,v28
v_fma_f32 v29,-v92,v21,v29
v_mfma_f32_16x16x32_bf16 v[36:39],a[16:19],v[52:55],v[36:39]
s_cselect_b32 s72,s59,s72
ds_read_b128 v[48:51],v2 offset:41984
v_fma_f32 v30,-v92,v22,v30
v_mfma_f32_16x16x32_bf16 v[40:43],a[20:23],v[52:55],v[40:43]
buffer_load_dwordx4 v1,s[36:39],s67 offen offset:1024 sc1 lds
v_mfma_f32_16x16x32_bf16 v[36:39],a[24:27],v[56:59],v[36:39]
s_mov_b64 s[44:45],s[46:47]
ds_read_b128 v[52:55],v2 offset:43008
v_fma_f32 v31,-v92,v23,v31
v_mfma_f32_16x16x32_bf16 v[40:43],a[28:31],v[56:59],v[40:43]
s_waitcnt lgkmcnt(3)
v_mul_f32 v76,v28,v28
v_mul_f32 v77,v29,v29
v_mfma_f32_16x16x32_bf16 v[36:39],a[32:35],v[60:63],v[36:39]
s_mov_b64 s[46:47],s[48:49]
ds_read_b128 v[56:59],v2 offset:44032
v_mul_f32 v78,v30,v30
v_mfma_f32_16x16x32_bf16 v[40:43],a[36:39],v[60:63],v[40:43]
buffer_load_dwordx4 v1,s[36:39],s67 offen offset:2048 sc1 lds
v_mfma_f32_16x16x32_bf16 v[36:39],a[40:43],v[64:67],v[36:39]
s_mov_b64 s[48:49],s[50:51]
ds_read_b128 v[60:63],v2 offset:45056
v_mul_f32 v79,v31,v31
v_mfma_f32_16x16x32_bf16 v[40:43],a[44:47],v[64:67],v[40:43]
v_fma_f32 v76,v94,v76,v93
v_mul_f32 v76,v28,v76
v_mfma_f32_16x16x32_bf16 v[36:39],a[48:51],v[68:71],v[36:39]
ds_read_b128 v[64:67],v2 offset:46080
v_fma_f32 v77,v94,v77,v93
v_mfma_f32_16x16x32_bf16 v[40:43],a[52:55],v[68:71],v[40:43]
buffer_load_dwordx4 v1,s[36:39],s67 offen offset:3072 sc1 lds
v_mfma_f32_16x16x32_bf16 v[36:39],a[56:59],v[72:75],v[36:39]
s_add_u32 m0,s71,s88
ds_read_b128 v[68:71],v2 offset:47104
v_mul_f32 v77,v29,v77
v_mfma_f32_16x16x32_bf16 v[40:43],a[60:63],v[72:75],v[40:43]
s_waitcnt lgkmcnt(3)
buffer_load_dword v10,s[36:39],s72 offen offset:0 sc1 lds
v_mfma_f32_16x16x32_bf16 v[36:39],a[64:67],v[44:47],v[36:39]
s_waitcnt vmcnt(11)
ds_read_b128 v[72:75],v2 offset:48128
v_fma_f32 v78,v94,v78,v93
v_mfma_f32_16x16x32_bf16 v[40:43],a[68:71],v[44:47],v[40:43]
s_barrier
v_mfma_f32_16x16x32_bf16 v[36:39],a[72:75],v[48:51],v[36:39]
s_xor_b32 s87,s63,65536
ds_read_b128 v[44:47],v2 offset:49152
v_fma_f32 v79,v94,v79,v93
v_mfma_f32_16x16x32_bf16 v[40:43],a[76:79],v[48:51],v[40:43]
s_cmp_ge_u32 s67,s59
v_exp_f32 v76,v76
v_mfma_f32_16x16x32_bf16 v[36:39],a[80:83],v[52:55],v[36:39]
s_cselect_b32 s86,s50,s51
ds_read_b128 v[48:51],v2 offset:50176
v_mul_f32 v78,v30,v78
v_mfma_f32_16x16x32_bf16 v[40:43],a[84:87],v[52:55],v[40:43]
s_cselect_b32 s87,s87,s64
v_exp_f32 v77,v77
v_mfma_f32_16x16x32_bf16 v[36:39],a[88:91],v[56:59],v[36:39]
s_lshr_b32 s69,s51,5
ds_read_b128 v[52:55],v2 offset:51200
v_mul_f32 v79,v31,v79
v_mfma_f32_16x16x32_bf16 v[40:43],a[92:95],v[56:59],v[40:43]
s_waitcnt lgkmcnt(3)
v_exp_f32 v78,v78
v_mfma_f32_16x16x32_bf16 v[36:39],a[96:99],v[60:63],v[36:39]
s_cmp_lt_u32 s51,4194304
ds_read_b128 v[56:59],v2 offset:52224
v_fma_f32 v76,v76,v95,v95
v_mfma_f32_16x16x32_bf16 v[40:43],a[100:103],v[60:63],v[40:43]
s_cselect_b32 s68,0xffffcfff,-1
v_exp_f32 v79,v79
v_mfma_f32_16x16x32_bf16 v[36:39],a[104:107],v[64:67],v[36:39]
s_and_b32 s68,s68,s69
ds_read_b128 v[60:63],v2 offset:53248
v_fma_f32 v77,v77,v95,v95
v_mfma_f32_16x16x32_bf16 v[40:43],a[108:111],v[64:67],v[40:43]
s_lshr_b32 s69,s52,5
v_rcp_f32 v76,v76
v_mfma_f32_16x16x32_bf16 v[36:39],a[112:115],v[68:71],v[36:39]
s_cmp_lt_u32 s52,4194304
ds_read_b128 v[64:67],v2 offset:54272
v_fma_f32 v78,v78,v95,v95
v_mfma_f32_16x16x32_bf16 v[40:43],a[116:119],v[68:71],v[40:43]
s_cselect_b32 s70,0xffffcfff,-1
v_rcp_f32 v77,v77
v_mfma_f32_16x16x32_bf16 v[36:39],a[120:123],v[72:75],v[36:39]
s_and_b32 s70,s70,s69
ds_read_b128 v[68:71],v2 offset:55296
v_fma_f32 v79,v79,v95,v95
v_mfma_f32_16x16x32_bf16 v[40:43],a[124:127],v[72:75],v[40:43]
s_waitcnt lgkmcnt(3)
v_rcp_f32 v78,v78
v_mfma_f32_16x16x32_bf16 v[36:39],a[128:131],v[44:47],v[36:39]
s_waitcnt vmcnt(10)
ds_read_b128 v[72:75],v2 offset:56320
v_mul_f32 v28,v28,v76
v_mfma_f32_16x16x32_bf16 v[40:43],a[132:135],v[44:47],v[40:43]
s_barrier
v_mfma_f32_16x16x32_bf16 v[36:39],a[136:139],v[48:51],v[36:39]
s_mov_b32 s62,s87
ds_read_b128 v[44:47],v2 offset:57344
ds_read_b32 v16,v7 offset:256
v_mfma_f32_16x16x32_bf16 v[40:43],a[140:143],v[48:51],v[40:43]
s_mov_b64 s[50:51],s[52:53]
v_rcp_f32 v79,v79
v_mfma_f32_16x16x32_bf16 v[36:39],a[144:147],v[52:55],v[36:39]
ds_read_b128 v[48:51],v2 offset:58368
v_mul_f32 v29,v29,v77
v_mfma_f32_16x16x32_bf16 v[40:43],a[148:151],v[52:55],v[40:43]
v_mul_f32 v30,v30,v78
v_mul_f32 v31,v31,v79
v_mfma_f32_16x16x32_bf16 v[36:39],a[152:155],v[56:59],v[36:39]
s_waitcnt lgkmcnt(1)
ds_read_b128 v[52:55],v2 offset:59392
v_cvt_pk_bf16_f32 v84,v28,v29
v_mfma_f32_16x16x32_bf16 v[40:43],a[156:159],v[56:59],v[40:43]
v_cmp_eq_u32 vcc,s60,v16
v_cvt_pk_bf16_f32 v85,v30,v31
v_fma_f32 v32,-v92,v24,v32
v_fma_f32 v33,-v92,v25,v33
v_mfma_f32_16x16x32_bf16 v[36:39],a[160:163],v[60:63],v[36:39]
s_cmp_eq_u32 vcc_lo,-1
ds_read_b128 v[56:59],v2 offset:60416
v_fma_f32 v34,-v92,v26,v34
v_mfma_f32_16x16x32_bf16 v[40:43],a[164:167],v[60:63],v[40:43]
s_cselect_b32 s66,s86,s59
s_cmp_ge_u32 s66,s59
s_cselect_b32 s68,s68,s70
s_add_u32 m0,s78,0
v_mfma_f32_16x16x32_bf16 v[36:39],a[168:171],v[64:67],v[36:39]
buffer_load_dword v6,s[36:39],s68 offen offset:0 sc1 lds
v_mfma_f32_16x16x32_bf16 v[40:43],a[172:175],v[64:67],v[40:43]
s_mov_b32 m0,s62
ds_read_b128 v[60:63],v2 offset:61440
v_fma_f32 v35,-v92,v27,v35
v_mfma_f32_16x16x32_bf16 v[36:39],a[176:179],v[68:71],v[36:39]
buffer_load_dwordx4 v0,s[36:39],s66 offen offset:0 sc1 lds
v_mfma_f32_16x16x32_bf16 v[40:43],a[180:183],v[68:71],v[40:43]
s_xor_b32 s64,s64,65536
ds_read_b128 v[64:67],v2 offset:62464
v_mul_f32 v80,v32,v32
v_mfma_f32_16x16x32_bf16 v[36:39],a[184:187],v[72:75],v[36:39]
s_add_u32 s74,s53,s55
ds_read_b128 v[68:71],v2 offset:63488
v_mul_f32 v81,v33,v33
v_mfma_f32_16x16x32_bf16 v[40:43],a[188:191],v[72:75],v[40:43]
s_waitcnt lgkmcnt(3)
v_mul_f32 v82,v34,v34
v_mul_f32 v83,v35,v35
v_mfma_f32_16x16x32_bf16 v[36:39],a[192:195],v[44:47],v[36:39]
s_waitcnt vmcnt(7)
ds_read_b128 v[72:75],v2 offset:64512
v_xor_b32 v2,65536,v2
v_mfma_f32_16x16x32_bf16 v[40:43],a[196:199],v[44:47],v[40:43]
s_barrier
v_mfma_f32_16x16x32_bf16 v[36:39],a[200:203],v[48:51],v[36:39]
buffer_load_dwordx4 v0,s[36:39],s66 offen offset:1024 sc1 lds
v_mfma_f32_16x16x32_bf16 v[40:43],a[204:207],v[48:51],v[40:43]
s_cmp_eq_u32 s74,s56
ds_read_b128 v[44:47],v2 offset:0
ds_read_b128 v[96:99],v8 offset:8192
v_mfma_f32_16x16x32_bf16 v[36:39],a[208:211],v[52:55],v[36:39]
s_cselect_b32 s74,s57,s74
ds_read_b128 v[48:51],v2 offset:1024
v_fma_f32 v80,v94,v80,v93
v_mfma_f32_16x16x32_bf16 v[40:43],a[212:215],v[52:55],v[40:43]
s_cmp_eq_u32 s74,s58
v_fma_f32 v81,v94,v81,v93
v_fma_f32 v82,v94,v82,v93
v_fma_f32 v83,v94,v83,v93
v_mul_f32 v80,v32,v80
v_mul_f32 v81,v33,v81
v_mul_f32 v82,v34,v82
v_mul_f32 v83,v35,v83
v_exp_f32 v80,v80
v_exp_f32 v81,v81
v_exp_f32 v82,v82
v_exp_f32 v83,v83
v_mfma_f32_16x16x32_bf16 v[36:39],a[216:219],v[56:59],v[36:39]
buffer_load_dwordx4 v0,s[36:39],s66 offen offset:2048 sc1 lds
v_fma_f32 v80,v80,v95,v95
v_fma_f32 v81,v81,v95,v95
v_fma_f32 v82,v82,v95,v95
v_fma_f32 v83,v83,v95,v95
v_rcp_f32 v80,v80
v_rcp_f32 v81,v81
v_rcp_f32 v82,v82
v_rcp_f32 v83,v83
v_mfma_f32_16x16x32_bf16 v[40:43],a[220:223],v[56:59],v[40:43]
s_waitcnt lgkmcnt(3))"
R"(
ds_read_b128 v[52:55],v2 offset:2048
v_mul_f32 v32,v32,v80
v_mul_f32 v33,v33,v81
v_mul_f32 v34,v34,v82
v_mul_f32 v35,v35,v83
v_cvt_pk_bf16_f32 v86,v32,v33
v_cvt_pk_bf16_f32 v87,v34,v35
v_mfma_f32_16x16x32_bf16 v[36:39],a[224:227],v[60:63],v[36:39]
buffer_store_dwordx4 v[84:87],v3,s[36:39],s73 offen offset:0 sc1
ds_read_b128 v[56:59],v2 offset:3072
v_xor_b32 v8,16384,v8
v_mfma_f32_16x16x32_bf16 v[40:43],a[228:231],v[60:63],v[40:43]
s_waitcnt lgkmcnt(3)
s_cselect_b32 s74,s59,s74
s_mov_b32 s52,s74
v_mfma_f32_16x16x32_bf16 v[36:39],a[232:235],v[64:67],v[36:39]
buffer_load_dwordx4 v0,s[36:39],s66 offen offset:3072 sc1 lds
v_mfma_f32_16x16x32_bf16 v[40:43],a[236:239],v[64:67],v[40:43]
s_cmp_lt_u32 s67,s59
ds_read_b128 v[60:63],v2 offset:4096
v_mfma_f32_16x16x32_bf16 v[36:39],a[240:243],v[68:71],v[36:39]
ds_read_b128 v[64:67],v2 offset:5120
v_mfma_f32_16x16x32_bf16 v[40:43],a[244:247],v[68:71],v[40:43]
v_mfma_f32_16x16x32_bf16 v[36:39],a[248:251],v[72:75],v[36:39]
s_waitcnt lgkmcnt(2)
ds_read_b128 v[68:71],v2 offset:6144
v_mfma_f32_16x16x32_bf16 v[40:43],a[252:255],v[72:75],v[40:43]
s_cbranch_scc1 Lg1_d0w0_%=
s_branch Lg1_slow0_%=
Lg1_d0w1_%=:
v_mfma_f32_16x16x32_bf16 v[28:31],a[32:35],v[44:47],0
s_add_u32 m0,s62,16384
ds_read_b128 v[72:75],v2 offset:3072
s_lshr_b32 s73,s47,0
v_mfma_f32_16x16x32_bf16 v[32:35],a[36:39],v[44:47],0
s_lshr_b32 s72,s66,6
s_lshr_b32 s71,s62,2
s_cmp_lt_u32 s66,4194304
v_mfma_f32_16x16x32_bf16 v[28:31],a[40:43],v[48:51],v[28:31]
buffer_load_dwordx4 v1,s[36:39],s66 offen offset:0 sc1 lds
v_mfma_f32_16x16x32_bf16 v[32:35],a[44:47],v[48:51],v[32:35]
s_cselect_b32 s72,s59,s72
ds_read_b128 v[44:47],v2 offset:12288
v_fma_f32 v36,-v96,v20,v36
v_mfma_f32_16x16x32_bf16 v[28:31],a[48:51],v[52:55],v[28:31]
ds_read_b128 v[48:51],v2 offset:13312
v_fma_f32 v37,-v96,v21,v37
v_mfma_f32_16x16x32_bf16 v[32:35],a[52:55],v[52:55],v[32:35]
v_fma_f32 v38,-v96,v22,v38
v_fma_f32 v39,-v96,v23,v39
v_mfma_f32_16x16x32_bf16 v[28:31],a[56:59],v[56:59],v[28:31]
buffer_load_dwordx4 v1,s[36:39],s66 offen offset:1024 sc1 lds
v_mfma_f32_16x16x32_bf16 v[32:35],a[60:63],v[56:59],v[32:35]
s_waitcnt lgkmcnt(2)
ds_read_b128 v[52:55],v2 offset:14336
v_mul_f32 v76,v36,v36
v_mfma_f32_16x16x32_bf16 v[28:31],a[0:3],v[60:63],v[28:31]
ds_read_b128 v[56:59],v2 offset:15360
v_mul_f32 v77,v37,v37
v_mfma_f32_16x16x32_bf16 v[32:35],a[4:7],v[60:63],v[32:35]
v_mul_f32 v78,v38,v38
v_mul_f32 v79,v39,v39
v_mfma_f32_16x16x32_bf16 v[28:31],a[8:11],v[64:67],v[28:31]
buffer_load_dwordx4 v1,s[36:39],s66 offen offset:2048 sc1 lds
v_mfma_f32_16x16x32_bf16 v[32:35],a[12:15],v[64:67],v[32:35]
ds_read_b128 v[60:63],v2 offset:8192
v_fma_f32 v76,v98,v76,v97
v_mfma_f32_16x16x32_bf16 v[28:31],a[16:19],v[68:71],v[28:31]
ds_read_b128 v[64:67],v2 offset:9216
v_mul_f32 v76,v36,v76
v_mfma_f32_16x16x32_bf16 v[32:35],a[20:23],v[68:71],v[32:35]
v_exp_f32 v76,v76
v_mfma_f32_16x16x32_bf16 v[28:31],a[24:27],v[72:75],v[28:31]
buffer_load_dwordx4 v1,s[36:39],s66 offen offset:3072 sc1 lds
v_mfma_f32_16x16x32_bf16 v[32:35],a[28:31],v[72:75],v[32:35]
s_waitcnt lgkmcnt(2)
ds_read_b128 v[68:71],v2 offset:10240
s_add_u32 m0,s71,s88
v_mfma_f32_16x16x32_bf16 v[28:31],a[96:99],v[44:47],v[28:31]
s_xor_b32 s87,s64,65536
ds_read_b128 v[72:75],v2 offset:11264
buffer_load_dword v10,s[36:39],s72 offen offset:0 sc1 lds
s_waitcnt vmcnt(10)
v_mfma_f32_16x16x32_bf16 v[32:35],a[100:103],v[44:47],v[32:35]
s_barrier
v_mfma_f32_16x16x32_bf16 v[28:31],a[104:107],v[48:51],v[28:31]
s_cmp_ge_u32 s66,s59
ds_read_b128 v[44:47],v2 offset:20480
v_fma_f32 v76,v76,v99,v99
v_mfma_f32_16x16x32_bf16 v[32:35],a[108:111],v[48:51],v[32:35]
s_cselect_b32 s86,s49,s50
v_rcp_f32 v76,v76
v_mfma_f32_16x16x32_bf16 v[28:31],a[112:115],v[52:55],v[28:31]
s_cselect_b32 s87,s87,s63
ds_read_b128 v[48:51],v2 offset:21504
v_fma_f32 v77,v98,v77,v97
v_mfma_f32_16x16x32_bf16 v[32:35],a[116:119],v[52:55],v[32:35]
s_mov_b32 s62,s87
v_mul_f32 v77,v37,v77
v_fma_f32 v78,v98,v78,v97
v_mfma_f32_16x16x32_bf16 v[28:31],a[120:123],v[56:59],v[28:31]
s_mov_b32 m0,s62
ds_read_b128 v[52:55],v2 offset:22528
v_mul_f32 v78,v38,v78
v_mfma_f32_16x16x32_bf16 v[32:35],a[124:127],v[56:59],v[32:35]
s_waitcnt lgkmcnt(3)
v_exp_f32 v77,v77
v_mfma_f32_16x16x32_bf16 v[28:31],a[64:67],v[60:63],v[28:31]
ds_read_b128 v[56:59],v2 offset:23552
v_fma_f32 v77,v77,v99,v99
v_mfma_f32_16x16x32_bf16 v[32:35],a[68:71],v[60:63],v[32:35]
v_exp_f32 v78,v78
v_mfma_f32_16x16x32_bf16 v[28:31],a[72:75],v[64:67],v[28:31]
ds_read_b128 v[60:63],v2 offset:16384
v_fma_f32 v78,v78,v99,v99
v_mfma_f32_16x16x32_bf16 v[32:35],a[76:79],v[64:67],v[32:35]
v_rcp_f32 v77,v77
v_mfma_f32_16x16x32_bf16 v[28:31],a[80:83],v[68:71],v[28:31]
ds_read_b128 v[64:67],v2 offset:17408
v_fma_f32 v79,v98,v79,v97
v_mfma_f32_16x16x32_bf16 v[32:35],a[84:87],v[68:71],v[32:35]
v_rcp_f32 v78,v78
v_mfma_f32_16x16x32_bf16 v[28:31],a[88:91],v[72:75],v[28:31]
ds_read_b128 v[68:71],v2 offset:18432
v_mul_f32 v79,v39,v79
v_mfma_f32_16x16x32_bf16 v[32:35],a[92:95],v[72:75],v[32:35]
s_waitcnt lgkmcnt(3)
v_exp_f32 v79,v79
v_mfma_f32_16x16x32_bf16 v[28:31],a[160:163],v[44:47],v[28:31]
s_nop 0
ds_read_b128 v[72:75],v2 offset:19456
v_fma_f32 v79,v79,v99,v99
v_mfma_f32_16x16x32_bf16 v[32:35],a[164:167],v[44:47],v[32:35]
s_barrier
v_mfma_f32_16x16x32_bf16 v[28:31],a[168:171],v[48:51],v[28:31]
ds_read_b128 v[44:47],v2 offset:28672
ds_read_b32 v16,v7 offset:0
v_mfma_f32_16x16x32_bf16 v[32:35],a[172:175],v[48:51],v[32:35]
v_rcp_f32 v79,v79
v_mfma_f32_16x16x32_bf16 v[28:31],a[176:179],v[52:55],v[28:31]
ds_read_b128 v[48:51],v2 offset:29696
v_mul_f32 v36,v36,v76
v_mfma_f32_16x16x32_bf16 v[32:35],a[180:183],v[52:55],v[32:35]
v_mul_f32 v37,v37,v77
v_mul_f32 v38,v38,v78
v_mfma_f32_16x16x32_bf16 v[28:31],a[184:187],v[56:59],v[28:31]
s_waitcnt lgkmcnt(1)
ds_read_b128 v[52:55],v2 offset:30720
v_mul_f32 v39,v39,v79
v_mfma_f32_16x16x32_bf16 v[32:35],a[188:191],v[56:59],v[32:35]
v_cmp_eq_u32 vcc,s60,v16
v_cvt_pk_bf16_f32 v84,v36,v37
v_cvt_pk_bf16_f32 v85,v38,v39
v_fma_f32 v40,-v96,v24,v40
v_mfma_f32_16x16x32_bf16 v[28:31],a[128:131],v[60:63],v[28:31]
s_cmp_eq_u32 vcc_lo,-1
ds_read_b128 v[56:59],v2 offset:31744
s_cselect_b32 s67,s86,s59
v_mfma_f32_16x16x32_bf16 v[32:35],a[132:135],v[60:63],v[32:35]
buffer_load_dwordx4 v0,s[36:39],s67 offen offset:0 sc1 lds
v_mfma_f32_16x16x32_bf16 v[28:31],a[136:139],v[64:67],v[28:31]
s_xor_b32 s63,s63,65536
ds_read_b128 v[60:63],v2 offset:24576
v_fma_f32 v41,-v96,v25,v41
v_mfma_f32_16x16x32_bf16 v[32:35],a[140:143],v[64:67],v[32:35]
s_add_u32 s53,s52,s55
v_fma_f32 v42,-v96,v26,v42
v_fma_f32 v43,-v96,v27,v43
v_mfma_f32_16x16x32_bf16 v[28:31],a[144:147],v[68:71],v[28:31]
s_cmp_eq_u32 s53,s56
ds_read_b128 v[64:67],v2 offset:25600
v_mul_f32 v80,v40,v40
v_mfma_f32_16x16x32_bf16 v[32:35],a[148:151],v[68:71],v[32:35]
buffer_load_dwordx4 v0,s[36:39],s67 offen offset:1024 sc1 lds
v_mfma_f32_16x16x32_bf16 v[28:31],a[152:155],v[72:75],v[28:31]
s_cselect_b32 s53,s57,s53
ds_read_b128 v[68:71],v2 offset:26624
v_mul_f32 v81,v41,v41
v_mfma_f32_16x16x32_bf16 v[32:35],a[156:159],v[72:75],v[32:35]
s_waitcnt lgkmcnt(3)
v_mul_f32 v82,v42,v42
v_mul_f32 v83,v43,v43
v_mfma_f32_16x16x32_bf16 v[28:31],a[224:227],v[44:47],v[28:31]
s_waitcnt vmcnt(7)
ds_read_b128 v[72:75],v2 offset:27648
v_fma_f32 v80,v98,v80,v97
v_mfma_f32_16x16x32_bf16 v[32:35],a[228:231],v[44:47],v[32:35]
s_barrier
v_mfma_f32_16x16x32_bf16 v[28:31],a[232:235],v[48:51],v[28:31]
s_cmp_eq_u32 s53,s58
ds_read_b128 v[44:47],v2 offset:36864
ds_read_b128 v[92:95],v8 offset:0
v_mfma_f32_16x16x32_bf16 v[32:35],a[236:239],v[48:51],v[32:35]
buffer_load_dwordx4 v0,s[36:39],s67 offen offset:2048 sc1 lds
v_mfma_f32_16x16x32_bf16 v[28:31],a[240:243],v[52:55],v[28:31]
s_cselect_b32 s53,s59,s53
ds_read_b128 v[48:51],v2 offset:37888
v_mul_f32 v80,v40,v80
v_mfma_f32_16x16x32_bf16 v[32:35],a[244:247],v[52:55],v[32:35]
s_cmp_ge_u32 s66,s59)"
R"(
v_exp_f32 v80,v80
v_mfma_f32_16x16x32_bf16 v[28:31],a[248:251],v[56:59],v[28:31]
v_fma_f32 v81,v98,v81,v97
v_fma_f32 v82,v98,v82,v97
v_fma_f32 v83,v98,v83,v97
v_mul_f32 v81,v41,v81
v_mul_f32 v82,v42,v82
v_mul_f32 v83,v43,v83
v_exp_f32 v81,v81
v_exp_f32 v82,v82
v_exp_f32 v83,v83
v_mfma_f32_16x16x32_bf16 v[32:35],a[252:255],v[56:59],v[32:35]
s_waitcnt lgkmcnt(3)
ds_read_b128 v[52:55],v2 offset:38912
buffer_load_dwordx4 v0,s[36:39],s67 offen offset:3072 sc1 lds
v_fma_f32 v80,v80,v99,v99
v_fma_f32 v81,v81,v99,v99
v_fma_f32 v82,v82,v99,v99
v_fma_f32 v83,v83,v99,v99
v_rcp_f32 v80,v80
v_rcp_f32 v81,v81
v_rcp_f32 v82,v82
v_rcp_f32 v83,v83
v_mfma_f32_16x16x32_bf16 v[28:31],a[192:195],v[60:63],v[28:31]
s_waitcnt lgkmcnt(2)
v_mul_f32 v40,v40,v80
v_mul_f32 v41,v41,v81
v_mul_f32 v42,v42,v82
v_mul_f32 v43,v43,v83
v_cvt_pk_bf16_f32 v86,v40,v41
v_cvt_pk_bf16_f32 v87,v42,v43
v_mfma_f32_16x16x32_bf16 v[32:35],a[196:199],v[60:63],v[32:35]
ds_read_b128 v[56:59],v2 offset:39936
buffer_store_dwordx4 v[84:87],v3,s[36:39],s73 offen offset:0 sc1
v_mfma_f32_16x16x32_bf16 v[28:31],a[200:203],v[64:67],v[28:31]
ds_read_b128 v[60:63],v2 offset:32768
v_mfma_f32_16x16x32_bf16 v[32:35],a[204:207],v[64:67],v[32:35]
v_mfma_f32_16x16x32_bf16 v[28:31],a[208:211],v[68:71],v[28:31]
ds_read_b128 v[64:67],v2 offset:33792
v_mfma_f32_16x16x32_bf16 v[32:35],a[212:215],v[68:71],v[32:35]
v_mfma_f32_16x16x32_bf16 v[28:31],a[216:219],v[72:75],v[28:31]
s_waitcnt lgkmcnt(2)
ds_read_b128 v[68:71],v2 offset:34816
v_mfma_f32_16x16x32_bf16 v[32:35],a[220:223],v[72:75],v[32:35]
s_cbranch_scc1 Lg1_slow1_%=
Lg1_d1w1_%=:
v_mfma_f32_16x16x32_bf16 v[36:39],a[32:35],v[44:47],0
s_add_u32 m0,s62,16384
ds_read_b128 v[72:75],v2 offset:35840
s_lshr_b32 s73,s48,0
v_mfma_f32_16x16x32_bf16 v[40:43],a[36:39],v[44:47],0
s_lshr_b32 s72,s67,6
s_lshr_b32 s71,s62,2
s_cmp_lt_u32 s67,4194304
v_mfma_f32_16x16x32_bf16 v[36:39],a[40:43],v[48:51],v[36:39]
buffer_load_dwordx4 v1,s[36:39],s67 offen offset:0 sc1 lds
v_mfma_f32_16x16x32_bf16 v[40:43],a[44:47],v[48:51],v[40:43]
s_cselect_b32 s72,s59,s72
ds_read_b128 v[44:47],v2 offset:45056
v_fma_f32 v28,-v92,v20,v28
v_mfma_f32_16x16x32_bf16 v[36:39],a[48:51],v[52:55],v[36:39]
s_mov_b64 s[44:45],s[46:47]
ds_read_b128 v[48:51],v2 offset:46080
v_fma_f32 v29,-v92,v21,v29
v_mfma_f32_16x16x32_bf16 v[40:43],a[52:55],v[52:55],v[40:43]
s_mov_b64 s[46:47],s[48:49]
v_fma_f32 v30,-v92,v22,v30
v_fma_f32 v31,-v92,v23,v31
v_mfma_f32_16x16x32_bf16 v[36:39],a[56:59],v[56:59],v[36:39]
buffer_load_dwordx4 v1,s[36:39],s67 offen offset:1024 sc1 lds
v_mfma_f32_16x16x32_bf16 v[40:43],a[60:63],v[56:59],v[40:43]
s_waitcnt lgkmcnt(2)
ds_read_b128 v[52:55],v2 offset:47104
v_mul_f32 v76,v28,v28
v_mfma_f32_16x16x32_bf16 v[36:39],a[0:3],v[60:63],v[36:39]
s_mov_b64 s[48:49],s[50:51]
ds_read_b128 v[56:59],v2 offset:48128
v_mul_f32 v77,v29,v29
v_mfma_f32_16x16x32_bf16 v[40:43],a[4:7],v[60:63],v[40:43]
v_mul_f32 v78,v30,v30
v_mul_f32 v79,v31,v31
v_mfma_f32_16x16x32_bf16 v[36:39],a[8:11],v[64:67],v[36:39]
buffer_load_dwordx4 v1,s[36:39],s67 offen offset:2048 sc1 lds
v_mfma_f32_16x16x32_bf16 v[40:43],a[12:15],v[64:67],v[40:43]
ds_read_b128 v[60:63],v2 offset:40960
v_fma_f32 v76,v94,v76,v93
v_mfma_f32_16x16x32_bf16 v[36:39],a[16:19],v[68:71],v[36:39]
ds_read_b128 v[64:67],v2 offset:41984
v_mul_f32 v76,v28,v76
v_mfma_f32_16x16x32_bf16 v[40:43],a[20:23],v[68:71],v[40:43]
v_exp_f32 v76,v76
v_mfma_f32_16x16x32_bf16 v[36:39],a[24:27],v[72:75],v[36:39]
buffer_load_dwordx4 v1,s[36:39],s67 offen offset:3072 sc1 lds
v_mfma_f32_16x16x32_bf16 v[40:43],a[28:31],v[72:75],v[40:43]
s_waitcnt lgkmcnt(2)
ds_read_b128 v[68:71],v2 offset:43008
s_add_u32 m0,s71,s88
v_mfma_f32_16x16x32_bf16 v[36:39],a[96:99],v[44:47],v[36:39]
s_xor_b32 s87,s63,65536
ds_read_b128 v[72:75],v2 offset:44032
buffer_load_dword v10,s[36:39],s72 offen offset:0 sc1 lds
s_waitcnt vmcnt(10)
v_mfma_f32_16x16x32_bf16 v[40:43],a[100:103],v[44:47],v[40:43]
s_barrier
v_mfma_f32_16x16x32_bf16 v[36:39],a[104:107],v[48:51],v[36:39]
s_cmp_ge_u32 s67,s59
ds_read_b128 v[44:47],v2 offset:53248
v_fma_f32 v76,v76,v95,v95
v_mfma_f32_16x16x32_bf16 v[40:43],a[108:111],v[48:51],v[40:43]
s_cselect_b32 s86,s50,s51
v_rcp_f32 v76,v76
v_mfma_f32_16x16x32_bf16 v[36:39],a[112:115],v[52:55],v[36:39]
s_cselect_b32 s87,s87,s64
ds_read_b128 v[48:51],v2 offset:54272
v_fma_f32 v77,v94,v77,v93
v_mfma_f32_16x16x32_bf16 v[40:43],a[116:119],v[52:55],v[40:43]
s_mov_b32 s62,s87
v_mul_f32 v77,v29,v77
v_fma_f32 v78,v94,v78,v93
v_mfma_f32_16x16x32_bf16 v[36:39],a[120:123],v[56:59],v[36:39]
s_mov_b32 m0,s62
ds_read_b128 v[52:55],v2 offset:55296
v_mul_f32 v78,v30,v78
v_mfma_f32_16x16x32_bf16 v[40:43],a[124:127],v[56:59],v[40:43]
s_waitcnt lgkmcnt(3)
v_exp_f32 v77,v77
v_mfma_f32_16x16x32_bf16 v[36:39],a[64:67],v[60:63],v[36:39]
s_mov_b64 s[50:51],s[52:53]
ds_read_b128 v[56:59],v2 offset:56320
v_fma_f32 v77,v77,v95,v95
v_mfma_f32_16x16x32_bf16 v[40:43],a[68:71],v[60:63],v[40:43]
v_exp_f32 v78,v78
v_mfma_f32_16x16x32_bf16 v[36:39],a[72:75],v[64:67],v[36:39]
ds_read_b128 v[60:63],v2 offset:49152
v_fma_f32 v78,v78,v95,v95
v_mfma_f32_16x16x32_bf16 v[40:43],a[76:79],v[64:67],v[40:43]
v_rcp_f32 v77,v77
v_mfma_f32_16x16x32_bf16 v[36:39],a[80:83],v[68:71],v[36:39]
ds_read_b128 v[64:67],v2 offset:50176
v_fma_f32 v79,v94,v79,v93
v_mfma_f32_16x16x32_bf16 v[40:43],a[84:87],v[68:71],v[40:43]
v_rcp_f32 v78,v78
v_mfma_f32_16x16x32_bf16 v[36:39],a[88:91],v[72:75],v[36:39]
ds_read_b128 v[68:71],v2 offset:51200
v_mul_f32 v79,v31,v79
v_mfma_f32_16x16x32_bf16 v[40:43],a[92:95],v[72:75],v[40:43]
s_waitcnt lgkmcnt(3)
v_exp_f32 v79,v79
v_mfma_f32_16x16x32_bf16 v[36:39],a[160:163],v[44:47],v[36:39]
s_nop 0
ds_read_b128 v[72:75],v2 offset:52224
v_fma_f32 v79,v79,v95,v95
v_mfma_f32_16x16x32_bf16 v[40:43],a[164:167],v[44:47],v[40:43]
s_barrier
v_mfma_f32_16x16x32_bf16 v[36:39],a[168:171],v[48:51],v[36:39]
ds_read_b128 v[44:47],v2 offset:61440
ds_read_b32 v16,v7 offset:256
v_mfma_f32_16x16x32_bf16 v[40:43],a[172:175],v[48:51],v[40:43]
v_rcp_f32 v79,v79
v_mfma_f32_16x16x32_bf16 v[36:39],a[176:179],v[52:55],v[36:39]
ds_read_b128 v[48:51],v2 offset:62464
v_mul_f32 v28,v28,v76
v_mfma_f32_16x16x32_bf16 v[40:43],a[180:183],v[52:55],v[40:43]
v_mul_f32 v29,v29,v77
v_mul_f32 v30,v30,v78
v_mfma_f32_16x16x32_bf16 v[36:39],a[184:187],v[56:59],v[36:39]
s_waitcnt lgkmcnt(1)
ds_read_b128 v[52:55],v2 offset:63488
v_mul_f32 v31,v31,v79
v_mfma_f32_16x16x32_bf16 v[40:43],a[188:191],v[56:59],v[40:43]
v_cmp_eq_u32 vcc,s60,v16
v_cvt_pk_bf16_f32 v84,v28,v29
v_cvt_pk_bf16_f32 v85,v30,v31
v_fma_f32 v32,-v92,v24,v32
v_mfma_f32_16x16x32_bf16 v[36:39],a[128:131],v[60:63],v[36:39]
s_cmp_eq_u32 vcc_lo,-1
ds_read_b128 v[56:59],v2 offset:64512
s_cselect_b32 s66,s86,s59
v_mfma_f32_16x16x32_bf16 v[40:43],a[132:135],v[60:63],v[40:43]
buffer_load_dwordx4 v0,s[36:39],s66 offen offset:0 sc1 lds
v_mfma_f32_16x16x32_bf16 v[36:39],a[136:139],v[64:67],v[36:39]
s_xor_b32 s64,s64,65536
ds_read_b128 v[60:63],v2 offset:57344
v_fma_f32 v33,-v92,v25,v33
v_mfma_f32_16x16x32_bf16 v[40:43],a[140:143],v[64:67],v[40:43]
s_add_u32 s74,s53,s55
v_fma_f32 v34,-v92,v26,v34
v_fma_f32 v35,-v92,v27,v35
v_mfma_f32_16x16x32_bf16 v[36:39],a[144:147],v[68:71],v[36:39]
s_cmp_eq_u32 s74,s56
ds_read_b128 v[64:67],v2 offset:58368
v_mul_f32 v80,v32,v32
v_mfma_f32_16x16x32_bf16 v[40:43],a[148:151],v[68:71],v[40:43]
buffer_load_dwordx4 v0,s[36:39],s66 offen offset:1024 sc1 lds
v_mfma_f32_16x16x32_bf16 v[36:39],a[152:155],v[72:75],v[36:39]
s_cselect_b32 s74,s57,s74
ds_read_b128 v[68:71],v2 offset:59392
v_mul_f32 v81,v33,v33
v_mfma_f32_16x16x32_bf16 v[40:43],a[156:159],v[72:75],v[40:43]
s_waitcnt lgkmcnt(3))"
R"(
v_mul_f32 v82,v34,v34
v_mul_f32 v83,v35,v35
v_mfma_f32_16x16x32_bf16 v[36:39],a[224:227],v[44:47],v[36:39]
s_waitcnt vmcnt(7)
ds_read_b128 v[72:75],v2 offset:60416
v_xor_b32 v2,65536,v2
v_mfma_f32_16x16x32_bf16 v[40:43],a[228:231],v[44:47],v[40:43]
s_barrier
v_mfma_f32_16x16x32_bf16 v[36:39],a[232:235],v[48:51],v[36:39]
s_cmp_eq_u32 s74,s58
ds_read_b128 v[44:47],v2 offset:4096
ds_read_b128 v[96:99],v8 offset:8192
v_mfma_f32_16x16x32_bf16 v[40:43],a[236:239],v[48:51],v[40:43]
buffer_load_dwordx4 v0,s[36:39],s66 offen offset:2048 sc1 lds
v_mfma_f32_16x16x32_bf16 v[36:39],a[240:243],v[52:55],v[36:39]
s_cselect_b32 s74,s59,s74
ds_read_b128 v[48:51],v2 offset:5120
v_fma_f32 v80,v94,v80,v93
v_mfma_f32_16x16x32_bf16 v[40:43],a[244:247],v[52:55],v[40:43]
s_mov_b32 s52,s74
v_mul_f32 v80,v32,v80
v_fma_f32 v81,v94,v81,v93
v_mfma_f32_16x16x32_bf16 v[36:39],a[248:251],v[56:59],v[36:39]
s_cmp_lt_u32 s67,s59
v_fma_f32 v82,v94,v82,v93
v_fma_f32 v83,v94,v83,v93
v_mul_f32 v81,v33,v81
v_mul_f32 v82,v34,v82
v_mul_f32 v83,v35,v83
v_exp_f32 v80,v80
v_exp_f32 v81,v81
v_exp_f32 v82,v82
v_exp_f32 v83,v83
v_mfma_f32_16x16x32_bf16 v[40:43],a[252:255],v[56:59],v[40:43]
s_waitcnt lgkmcnt(3)
ds_read_b128 v[52:55],v2 offset:6144
buffer_load_dwordx4 v0,s[36:39],s66 offen offset:3072 sc1 lds
v_fma_f32 v80,v80,v95,v95
v_fma_f32 v81,v81,v95,v95
v_fma_f32 v82,v82,v95,v95
v_fma_f32 v83,v83,v95,v95
v_rcp_f32 v80,v80
v_rcp_f32 v81,v81
v_rcp_f32 v82,v82
v_rcp_f32 v83,v83
v_mfma_f32_16x16x32_bf16 v[36:39],a[192:195],v[60:63],v[36:39]
s_waitcnt lgkmcnt(2)
v_mul_f32 v32,v32,v80
v_mul_f32 v33,v33,v81
v_mul_f32 v34,v34,v82
v_mul_f32 v35,v35,v83
v_cvt_pk_bf16_f32 v86,v32,v33
v_cvt_pk_bf16_f32 v87,v34,v35
v_mfma_f32_16x16x32_bf16 v[40:43],a[196:199],v[60:63],v[40:43]
ds_read_b128 v[56:59],v2 offset:7168
buffer_store_dwordx4 v[84:87],v3,s[36:39],s73 offen offset:0 sc1
v_xor_b32 v8,16384,v8
v_mfma_f32_16x16x32_bf16 v[36:39],a[200:203],v[64:67],v[36:39]
ds_read_b128 v[60:63],v2 offset:0
v_mfma_f32_16x16x32_bf16 v[40:43],a[204:207],v[64:67],v[40:43]
v_mfma_f32_16x16x32_bf16 v[36:39],a[208:211],v[68:71],v[36:39]
ds_read_b128 v[64:67],v2 offset:1024
v_mfma_f32_16x16x32_bf16 v[40:43],a[212:215],v[68:71],v[40:43]
v_mfma_f32_16x16x32_bf16 v[36:39],a[216:219],v[72:75],v[36:39]
s_waitcnt lgkmcnt(2)
ds_read_b128 v[68:71],v2 offset:2048
v_mfma_f32_16x16x32_bf16 v[40:43],a[220:223],v[72:75],v[40:43]
s_cbranch_scc1 Lg1_d0w1_%=
s_branch Lg1_slow0_%=
Lg1_d0w2_%=:
v_mfma_f32_16x16x32_bf16 v[28:31],a[64:67],v[44:47],0
s_add_u32 m0,s62,16384
ds_read_b128 v[72:75],v2 offset:15360
s_lshr_b32 s73,s47,0
v_mfma_f32_16x16x32_bf16 v[32:35],a[68:71],v[44:47],0
s_lshr_b32 s72,s66,6
s_lshr_b32 s71,s62,2
s_cmp_lt_u32 s66,4194304
v_mfma_f32_16x16x32_bf16 v[28:31],a[72:75],v[48:51],v[28:31]
s_cselect_b32 s72,s59,s72
ds_read_b128 v[44:47],v2 offset:0
v_mfma_f32_16x16x32_bf16 v[32:35],a[76:79],v[48:51],v[32:35]
buffer_load_dwordx4 v1,s[36:39],s66 offen offset:0 sc1 lds
v_mfma_f32_16x16x32_bf16 v[28:31],a[80:83],v[52:55],v[28:31]
ds_read_b128 v[48:51],v2 offset:1024
v_fma_f32 v36,-v96,v20,v36
v_mfma_f32_16x16x32_bf16 v[32:35],a[84:87],v[52:55],v[32:35]
v_fma_f32 v37,-v96,v21,v37
v_fma_f32 v38,-v96,v22,v38
v_mfma_f32_16x16x32_bf16 v[28:31],a[88:91],v[56:59],v[28:31]
ds_read_b128 v[52:55],v2 offset:2048
v_fma_f32 v39,-v96,v23,v39
v_mfma_f32_16x16x32_bf16 v[32:35],a[92:95],v[56:59],v[32:35]
s_waitcnt lgkmcnt(3)
buffer_load_dwordx4 v1,s[36:39],s66 offen offset:1024 sc1 lds
v_mfma_f32_16x16x32_bf16 v[28:31],a[96:99],v[60:63],v[28:31]
ds_read_b128 v[56:59],v2 offset:3072
v_mul_f32 v76,v36,v36
v_mfma_f32_16x16x32_bf16 v[32:35],a[100:103],v[60:63],v[32:35]
v_mul_f32 v77,v37,v37
v_mul_f32 v78,v38,v38
v_mfma_f32_16x16x32_bf16 v[28:31],a[104:107],v[64:67],v[28:31]
ds_read_b128 v[60:63],v2 offset:4096
v_mul_f32 v79,v39,v39
v_mfma_f32_16x16x32_bf16 v[32:35],a[108:111],v[64:67],v[32:35]
buffer_load_dwordx4 v1,s[36:39],s66 offen offset:2048 sc1 lds
v_mfma_f32_16x16x32_bf16 v[28:31],a[112:115],v[68:71],v[28:31]
ds_read_b128 v[64:67],v2 offset:5120
v_fma_f32 v76,v98,v76,v97
v_mfma_f32_16x16x32_bf16 v[32:35],a[116:119],v[68:71],v[32:35]
v_mul_f32 v76,v36,v76
v_fma_f32 v77,v98,v77,v97
v_mfma_f32_16x16x32_bf16 v[28:31],a[120:123],v[72:75],v[28:31]
ds_read_b128 v[68:71],v2 offset:6144
v_mul_f32 v77,v37,v77
v_mfma_f32_16x16x32_bf16 v[32:35],a[124:127],v[72:75],v[32:35]
s_waitcnt lgkmcnt(3)
buffer_load_dwordx4 v1,s[36:39],s66 offen offset:3072 sc1 lds
s_add_u32 m0,s71,s88
v_mfma_f32_16x16x32_bf16 v[28:31],a[0:3],v[44:47],v[28:31]
s_xor_b32 s87,s64,65536
ds_read_b128 v[72:75],v2 offset:7168
buffer_load_dword v10,s[36:39],s72 offen offset:0 sc1 lds
s_waitcnt vmcnt(10)
v_mfma_f32_16x16x32_bf16 v[32:35],a[4:7],v[44:47],v[32:35]
s_barrier
v_mfma_f32_16x16x32_bf16 v[28:31],a[8:11],v[48:51],v[28:31]
s_cmp_ge_u32 s66,s59
ds_read_b128 v[44:47],v2 offset:24576
v_fma_f32 v78,v98,v78,v97
v_mfma_f32_16x16x32_bf16 v[32:35],a[12:15],v[48:51],v[32:35]
s_cselect_b32 s86,s49,s50
v_exp_f32 v76,v76
v_mfma_f32_16x16x32_bf16 v[28:31],a[16:19],v[52:55],v[28:31]
s_cselect_b32 s87,s87,s63
ds_read_b128 v[48:51],v2 offset:25600
v_mul_f32 v78,v38,v78
v_mfma_f32_16x16x32_bf16 v[32:35],a[20:23],v[52:55],v[32:35]
s_mov_b32 s62,s87
v_exp_f32 v77,v77
v_mfma_f32_16x16x32_bf16 v[28:31],a[24:27],v[56:59],v[28:31]
s_mov_b32 m0,s62
ds_read_b128 v[52:55],v2 offset:26624
v_fma_f32 v76,v76,v99,v99
v_mfma_f32_16x16x32_bf16 v[32:35],a[28:31],v[56:59],v[32:35]
s_waitcnt lgkmcnt(3)
v_exp_f32 v78,v78
v_mfma_f32_16x16x32_bf16 v[28:31],a[32:35],v[60:63],v[28:31]
ds_read_b128 v[56:59],v2 offset:27648
v_fma_f32 v77,v77,v99,v99
v_mfma_f32_16x16x32_bf16 v[32:35],a[36:39],v[60:63],v[32:35]
v_rcp_f32 v76,v76
v_mfma_f32_16x16x32_bf16 v[28:31],a[40:43],v[64:67],v[28:31]
ds_read_b128 v[60:63],v2 offset:28672
v_fma_f32 v78,v78,v99,v99
v_mfma_f32_16x16x32_bf16 v[32:35],a[44:47],v[64:67],v[32:35]
v_rcp_f32 v77,v77
v_mfma_f32_16x16x32_bf16 v[28:31],a[48:51],v[68:71],v[28:31]
ds_read_b128 v[64:67],v2 offset:29696
v_fma_f32 v79,v98,v79,v97
v_mfma_f32_16x16x32_bf16 v[32:35],a[52:55],v[68:71],v[32:35]
v_rcp_f32 v78,v78
v_mfma_f32_16x16x32_bf16 v[28:31],a[56:59],v[72:75],v[28:31]
ds_read_b128 v[68:71],v2 offset:30720
v_mul_f32 v79,v39,v79
v_mfma_f32_16x16x32_bf16 v[32:35],a[60:63],v[72:75],v[32:35]
s_waitcnt lgkmcnt(3)
v_exp_f32 v79,v79
v_mfma_f32_16x16x32_bf16 v[28:31],a[192:195],v[44:47],v[28:31]
s_nop 0
ds_read_b128 v[72:75],v2 offset:31744
v_fma_f32 v79,v79,v99,v99
v_mfma_f32_16x16x32_bf16 v[32:35],a[196:199],v[44:47],v[32:35]
s_barrier
v_mfma_f32_16x16x32_bf16 v[28:31],a[200:203],v[48:51],v[28:31]
ds_read_b128 v[44:47],v2 offset:16384
ds_read_b32 v16,v7 offset:0
v_mfma_f32_16x16x32_bf16 v[32:35],a[204:207],v[48:51],v[32:35]
v_rcp_f32 v79,v79
v_mfma_f32_16x16x32_bf16 v[28:31],a[208:211],v[52:55],v[28:31]
ds_read_b128 v[48:51],v2 offset:17408
v_mul_f32 v36,v36,v76
v_mfma_f32_16x16x32_bf16 v[32:35],a[212:215],v[52:55],v[32:35]
v_mul_f32 v37,v37,v77
v_mul_f32 v38,v38,v78
v_mfma_f32_16x16x32_bf16 v[28:31],a[216:219],v[56:59],v[28:31]
s_waitcnt lgkmcnt(1)
ds_read_b128 v[52:55],v2 offset:18432
v_mul_f32 v39,v39,v79
v_mfma_f32_16x16x32_bf16 v[32:35],a[220:223],v[56:59],v[32:35]
v_cmp_eq_u32 vcc,s60,v16
v_cvt_pk_bf16_f32 v84,v36,v37
v_cvt_pk_bf16_f32 v85,v38,v39
v_fma_f32 v40,-v96,v24,v40
v_mfma_f32_16x16x32_bf16 v[28:31],a[224:227],v[60:63],v[28:31]
s_cmp_eq_u32 vcc_lo,-1
ds_read_b128 v[56:59],v2 offset:19456
v_fma_f32 v41,-v96,v25,v41
v_mfma_f32_16x16x32_bf16 v[32:35],a[228:231],v[60:63],v[32:35]
s_cselect_b32 s67,s86,s59)"
R"(
v_fma_f32 v42,-v96,v26,v42
v_fma_f32 v43,-v96,v27,v43
v_mfma_f32_16x16x32_bf16 v[28:31],a[232:235],v[64:67],v[28:31]
buffer_load_dwordx4 v0,s[36:39],s67 offen offset:0 sc1 lds
v_mfma_f32_16x16x32_bf16 v[32:35],a[236:239],v[64:67],v[32:35]
s_xor_b32 s63,s63,65536
ds_read_b128 v[60:63],v2 offset:20480
v_mul_f32 v80,v40,v40
v_mfma_f32_16x16x32_bf16 v[28:31],a[240:243],v[68:71],v[28:31]
s_add_u32 s53,s52,s55
ds_read_b128 v[64:67],v2 offset:21504
v_mul_f32 v81,v41,v41
v_mfma_f32_16x16x32_bf16 v[32:35],a[244:247],v[68:71],v[32:35]
s_cmp_eq_u32 s53,s56
v_mul_f32 v82,v42,v42
v_mul_f32 v83,v43,v43
v_mfma_f32_16x16x32_bf16 v[28:31],a[248:251],v[72:75],v[28:31]
buffer_load_dwordx4 v0,s[36:39],s67 offen offset:1024 sc1 lds
v_mfma_f32_16x16x32_bf16 v[32:35],a[252:255],v[72:75],v[32:35]
s_waitcnt lgkmcnt(2)
ds_read_b128 v[68:71],v2 offset:22528
v_fma_f32 v80,v98,v80,v97
v_mfma_f32_16x16x32_bf16 v[28:31],a[128:131],v[44:47],v[28:31]
s_waitcnt vmcnt(7)
ds_read_b128 v[72:75],v2 offset:23552
v_mul_f32 v80,v40,v80
v_mfma_f32_16x16x32_bf16 v[32:35],a[132:135],v[44:47],v[32:35]
s_barrier
v_mfma_f32_16x16x32_bf16 v[28:31],a[136:139],v[48:51],v[28:31]
s_cselect_b32 s53,s57,s53
ds_read_b128 v[44:47],v2 offset:40960
ds_read_b128 v[92:95],v8 offset:0
v_mfma_f32_16x16x32_bf16 v[32:35],a[140:143],v[48:51],v[32:35]
s_cmp_eq_u32 s53,s58
v_exp_f32 v80,v80
v_mfma_f32_16x16x32_bf16 v[28:31],a[144:147],v[52:55],v[28:31]
buffer_load_dwordx4 v0,s[36:39],s67 offen offset:2048 sc1 lds
v_mfma_f32_16x16x32_bf16 v[32:35],a[148:151],v[52:55],v[32:35]
s_cselect_b32 s53,s59,s53
ds_read_b128 v[48:51],v2 offset:41984
v_fma_f32 v80,v80,v99,v99
v_mfma_f32_16x16x32_bf16 v[28:31],a[152:155],v[56:59],v[28:31]
s_cmp_ge_u32 s66,s59
ds_read_b128 v[52:55],v2 offset:43008
v_fma_f32 v81,v98,v81,v97
v_mfma_f32_16x16x32_bf16 v[32:35],a[156:159],v[56:59],v[32:35]
s_waitcnt lgkmcnt(4)
v_fma_f32 v82,v98,v82,v97
v_fma_f32 v83,v98,v83,v97
v_mul_f32 v81,v41,v81
v_mul_f32 v82,v42,v82
v_mul_f32 v83,v43,v83
v_exp_f32 v81,v81
v_exp_f32 v82,v82
v_exp_f32 v83,v83
v_mfma_f32_16x16x32_bf16 v[28:31],a[160:163],v[60:63],v[28:31]
s_waitcnt lgkmcnt(2)
buffer_load_dwordx4 v0,s[36:39],s67 offen offset:3072 sc1 lds
v_fma_f32 v81,v81,v99,v99
v_fma_f32 v82,v82,v99,v99
v_fma_f32 v83,v83,v99,v99
v_rcp_f32 v80,v80
v_rcp_f32 v81,v81
v_rcp_f32 v82,v82
v_rcp_f32 v83,v83
v_mfma_f32_16x16x32_bf16 v[32:35],a[164:167],v[60:63],v[32:35]
ds_read_b128 v[56:59],v2 offset:44032
v_mul_f32 v40,v40,v80
v_mul_f32 v41,v41,v81
v_mul_f32 v42,v42,v82
v_mul_f32 v43,v43,v83
v_cvt_pk_bf16_f32 v86,v40,v41
v_cvt_pk_bf16_f32 v87,v42,v43
v_mfma_f32_16x16x32_bf16 v[28:31],a[168:171],v[64:67],v[28:31]
buffer_store_dwordx4 v[84:87],v3,s[36:39],s73 offen offset:0 sc1
ds_read_b128 v[60:63],v2 offset:45056
v_mfma_f32_16x16x32_bf16 v[32:35],a[172:175],v[64:67],v[32:35]
v_mfma_f32_16x16x32_bf16 v[28:31],a[176:179],v[68:71],v[28:31]
ds_read_b128 v[64:67],v2 offset:46080
v_mfma_f32_16x16x32_bf16 v[32:35],a[180:183],v[68:71],v[32:35]
v_mfma_f32_16x16x32_bf16 v[28:31],a[184:187],v[72:75],v[28:31]
s_waitcnt lgkmcnt(2)
ds_read_b128 v[68:71],v2 offset:47104
v_mfma_f32_16x16x32_bf16 v[32:35],a[188:191],v[72:75],v[32:35]
s_cbranch_scc1 Lg1_slow1_%=
Lg1_d1w2_%=:
v_mfma_f32_16x16x32_bf16 v[36:39],a[64:67],v[44:47],0
s_add_u32 m0,s62,16384
ds_read_b128 v[72:75],v2 offset:48128
s_lshr_b32 s73,s48,0
v_mfma_f32_16x16x32_bf16 v[40:43],a[68:71],v[44:47],0
s_lshr_b32 s72,s67,6
s_lshr_b32 s71,s62,2
s_cmp_lt_u32 s67,4194304
v_mfma_f32_16x16x32_bf16 v[36:39],a[72:75],v[48:51],v[36:39]
s_cselect_b32 s72,s59,s72
ds_read_b128 v[44:47],v2 offset:32768
s_mov_b64 s[44:45],s[46:47]
v_mfma_f32_16x16x32_bf16 v[40:43],a[76:79],v[48:51],v[40:43]
buffer_load_dwordx4 v1,s[36:39],s67 offen offset:0 sc1 lds
v_mfma_f32_16x16x32_bf16 v[36:39],a[80:83],v[52:55],v[36:39]
s_mov_b64 s[46:47],s[48:49]
ds_read_b128 v[48:51],v2 offset:33792
v_fma_f32 v28,-v92,v20,v28
v_mfma_f32_16x16x32_bf16 v[40:43],a[84:87],v[52:55],v[40:43]
s_mov_b64 s[48:49],s[50:51]
v_fma_f32 v29,-v92,v21,v29
v_fma_f32 v30,-v92,v22,v30
v_mfma_f32_16x16x32_bf16 v[36:39],a[88:91],v[56:59],v[36:39]
ds_read_b128 v[52:55],v2 offset:34816
v_fma_f32 v31,-v92,v23,v31
v_mfma_f32_16x16x32_bf16 v[40:43],a[92:95],v[56:59],v[40:43]
s_waitcnt lgkmcnt(3)
buffer_load_dwordx4 v1,s[36:39],s67 offen offset:1024 sc1 lds
v_mfma_f32_16x16x32_bf16 v[36:39],a[96:99],v[60:63],v[36:39]
ds_read_b128 v[56:59],v2 offset:35840
v_mul_f32 v76,v28,v28
v_mfma_f32_16x16x32_bf16 v[40:43],a[100:103],v[60:63],v[40:43]
v_mul_f32 v77,v29,v29
v_mul_f32 v78,v30,v30
v_mfma_f32_16x16x32_bf16 v[36:39],a[104:107],v[64:67],v[36:39]
ds_read_b128 v[60:63],v2 offset:36864
v_mul_f32 v79,v31,v31
v_mfma_f32_16x16x32_bf16 v[40:43],a[108:111],v[64:67],v[40:43]
buffer_load_dwordx4 v1,s[36:39],s67 offen offset:2048 sc1 lds
v_mfma_f32_16x16x32_bf16 v[36:39],a[112:115],v[68:71],v[36:39]
ds_read_b128 v[64:67],v2 offset:37888
v_fma_f32 v76,v94,v76,v93
v_mfma_f32_16x16x32_bf16 v[40:43],a[116:119],v[68:71],v[40:43]
v_mul_f32 v76,v28,v76
v_fma_f32 v77,v94,v77,v93
v_mfma_f32_16x16x32_bf16 v[36:39],a[120:123],v[72:75],v[36:39]
ds_read_b128 v[68:71],v2 offset:38912
v_mul_f32 v77,v29,v77
v_mfma_f32_16x16x32_bf16 v[40:43],a[124:127],v[72:75],v[40:43]
s_waitcnt lgkmcnt(3)
buffer_load_dwordx4 v1,s[36:39],s67 offen offset:3072 sc1 lds
s_add_u32 m0,s71,s88
v_mfma_f32_16x16x32_bf16 v[36:39],a[0:3],v[44:47],v[36:39]
s_xor_b32 s87,s63,65536
ds_read_b128 v[72:75],v2 offset:39936
buffer_load_dword v10,s[36:39],s72 offen offset:0 sc1 lds
s_waitcnt vmcnt(10)
v_mfma_f32_16x16x32_bf16 v[40:43],a[4:7],v[44:47],v[40:43]
s_barrier
v_mfma_f32_16x16x32_bf16 v[36:39],a[8:11],v[48:51],v[36:39]
s_cmp_ge_u32 s67,s59
ds_read_b128 v[44:47],v2 offset:57344
v_fma_f32 v78,v94,v78,v93
v_mfma_f32_16x16x32_bf16 v[40:43],a[12:15],v[48:51],v[40:43]
s_cselect_b32 s86,s50,s51
v_exp_f32 v76,v76
v_mfma_f32_16x16x32_bf16 v[36:39],a[16:19],v[52:55],v[36:39]
s_cselect_b32 s87,s87,s64
ds_read_b128 v[48:51],v2 offset:58368
v_mul_f32 v78,v30,v78
v_mfma_f32_16x16x32_bf16 v[40:43],a[20:23],v[52:55],v[40:43]
s_mov_b32 s62,s87
v_exp_f32 v77,v77
v_mfma_f32_16x16x32_bf16 v[36:39],a[24:27],v[56:59],v[36:39]
s_mov_b32 m0,s62
ds_read_b128 v[52:55],v2 offset:59392
v_fma_f32 v76,v76,v95,v95
v_mfma_f32_16x16x32_bf16 v[40:43],a[28:31],v[56:59],v[40:43]
s_waitcnt lgkmcnt(3)
v_exp_f32 v78,v78
v_mfma_f32_16x16x32_bf16 v[36:39],a[32:35],v[60:63],v[36:39]
s_mov_b64 s[50:51],s[52:53]
ds_read_b128 v[56:59],v2 offset:60416
v_fma_f32 v77,v77,v95,v95
v_mfma_f32_16x16x32_bf16 v[40:43],a[36:39],v[60:63],v[40:43]
v_rcp_f32 v76,v76
v_mfma_f32_16x16x32_bf16 v[36:39],a[40:43],v[64:67],v[36:39]
ds_read_b128 v[60:63],v2 offset:61440
v_fma_f32 v78,v78,v95,v95
v_mfma_f32_16x16x32_bf16 v[40:43],a[44:47],v[64:67],v[40:43]
v_rcp_f32 v77,v77
v_mfma_f32_16x16x32_bf16 v[36:39],a[48:51],v[68:71],v[36:39]
ds_read_b128 v[64:67],v2 offset:62464
v_fma_f32 v79,v94,v79,v93
v_mfma_f32_16x16x32_bf16 v[40:43],a[52:55],v[68:71],v[40:43]
v_rcp_f32 v78,v78
v_mfma_f32_16x16x32_bf16 v[36:39],a[56:59],v[72:75],v[36:39]
ds_read_b128 v[68:71],v2 offset:63488
v_mul_f32 v79,v31,v79
v_mfma_f32_16x16x32_bf16 v[40:43],a[60:63],v[72:75],v[40:43]
s_waitcnt lgkmcnt(3)
v_exp_f32 v79,v79
v_mfma_f32_16x16x32_bf16 v[36:39],a[192:195],v[44:47],v[36:39]
s_nop 0
ds_read_b128 v[72:75],v2 offset:64512
v_fma_f32 v79,v79,v95,v95
v_mfma_f32_16x16x32_bf16 v[40:43],a[196:199],v[44:47],v[40:43]
s_barrier
v_mfma_f32_16x16x32_bf16 v[36:39],a[200:203],v[48:51],v[36:39]
ds_read_b128 v[44:47],v2 offset:49152
ds_read_b32 v16,v7 offset:256
v_mfma_f32_16x16x32_bf16 v[40:43],a[204:207],v[48:51],v[40:43]
v_rcp_f32 v79,v79
v_mfma_f32_16x16x32_bf16 v[36:39],a[208:211],v[52:55],v[36:39])"
R"(
ds_read_b128 v[48:51],v2 offset:50176
v_mul_f32 v28,v28,v76
v_mfma_f32_16x16x32_bf16 v[40:43],a[212:215],v[52:55],v[40:43]
v_mul_f32 v29,v29,v77
v_mul_f32 v30,v30,v78
v_mfma_f32_16x16x32_bf16 v[36:39],a[216:219],v[56:59],v[36:39]
s_waitcnt lgkmcnt(1)
ds_read_b128 v[52:55],v2 offset:51200
v_mul_f32 v31,v31,v79
v_mfma_f32_16x16x32_bf16 v[40:43],a[220:223],v[56:59],v[40:43]
v_cmp_eq_u32 vcc,s60,v16
v_cvt_pk_bf16_f32 v84,v28,v29
v_cvt_pk_bf16_f32 v85,v30,v31
v_fma_f32 v32,-v92,v24,v32
v_mfma_f32_16x16x32_bf16 v[36:39],a[224:227],v[60:63],v[36:39]
s_cmp_eq_u32 vcc_lo,-1
ds_read_b128 v[56:59],v2 offset:52224
v_fma_f32 v33,-v92,v25,v33
v_mfma_f32_16x16x32_bf16 v[40:43],a[228:231],v[60:63],v[40:43]
s_cselect_b32 s66,s86,s59
v_fma_f32 v34,-v92,v26,v34
v_fma_f32 v35,-v92,v27,v35
v_mfma_f32_16x16x32_bf16 v[36:39],a[232:235],v[64:67],v[36:39]
buffer_load_dwordx4 v0,s[36:39],s66 offen offset:0 sc1 lds
v_mfma_f32_16x16x32_bf16 v[40:43],a[236:239],v[64:67],v[40:43]
s_xor_b32 s64,s64,65536
ds_read_b128 v[60:63],v2 offset:53248
v_mul_f32 v80,v32,v32
v_mfma_f32_16x16x32_bf16 v[36:39],a[240:243],v[68:71],v[36:39]
s_add_u32 s74,s53,s55
ds_read_b128 v[64:67],v2 offset:54272
v_mul_f32 v81,v33,v33
v_mfma_f32_16x16x32_bf16 v[40:43],a[244:247],v[68:71],v[40:43]
s_cmp_eq_u32 s74,s56
v_mul_f32 v82,v34,v34
v_mul_f32 v83,v35,v35
v_mfma_f32_16x16x32_bf16 v[36:39],a[248:251],v[72:75],v[36:39]
buffer_load_dwordx4 v0,s[36:39],s66 offen offset:1024 sc1 lds
v_mfma_f32_16x16x32_bf16 v[40:43],a[252:255],v[72:75],v[40:43]
s_waitcnt lgkmcnt(2)
ds_read_b128 v[68:71],v2 offset:55296
v_fma_f32 v80,v94,v80,v93
v_mfma_f32_16x16x32_bf16 v[36:39],a[128:131],v[44:47],v[36:39]
s_waitcnt vmcnt(7)
ds_read_b128 v[72:75],v2 offset:56320
v_xor_b32 v2,65536,v2
v_mfma_f32_16x16x32_bf16 v[40:43],a[132:135],v[44:47],v[40:43]
s_barrier
v_mfma_f32_16x16x32_bf16 v[36:39],a[136:139],v[48:51],v[36:39]
s_cselect_b32 s74,s57,s74
ds_read_b128 v[44:47],v2 offset:8192
ds_read_b128 v[96:99],v8 offset:8192
v_mfma_f32_16x16x32_bf16 v[40:43],a[140:143],v[48:51],v[40:43]
s_cmp_eq_u32 s74,s58
v_mul_f32 v80,v32,v80
v_fma_f32 v81,v94,v81,v93
v_mfma_f32_16x16x32_bf16 v[36:39],a[144:147],v[52:55],v[36:39]
buffer_load_dwordx4 v0,s[36:39],s66 offen offset:2048 sc1 lds
v_mfma_f32_16x16x32_bf16 v[40:43],a[148:151],v[52:55],v[40:43]
s_cselect_b32 s74,s59,s74
ds_read_b128 v[48:51],v2 offset:9216
v_mul_f32 v81,v33,v81
v_mfma_f32_16x16x32_bf16 v[36:39],a[152:155],v[56:59],v[36:39]
s_mov_b32 s52,s74
ds_read_b128 v[52:55],v2 offset:10240
v_fma_f32 v82,v94,v82,v93
v_mfma_f32_16x16x32_bf16 v[40:43],a[156:159],v[56:59],v[40:43]
s_waitcnt lgkmcnt(4)
v_fma_f32 v83,v94,v83,v93
v_mul_f32 v82,v34,v82
v_mul_f32 v83,v35,v83
v_exp_f32 v80,v80
v_exp_f32 v81,v81
v_exp_f32 v82,v82
v_exp_f32 v83,v83
v_mfma_f32_16x16x32_bf16 v[36:39],a[160:163],v[60:63],v[36:39]
s_waitcnt lgkmcnt(2)
buffer_load_dwordx4 v0,s[36:39],s66 offen offset:3072 sc1 lds
v_fma_f32 v80,v80,v95,v95
v_fma_f32 v81,v81,v95,v95
v_fma_f32 v82,v82,v95,v95
v_fma_f32 v83,v83,v95,v95
v_rcp_f32 v80,v80
v_rcp_f32 v81,v81
v_rcp_f32 v82,v82
v_rcp_f32 v83,v83
v_mfma_f32_16x16x32_bf16 v[40:43],a[164:167],v[60:63],v[40:43]
s_cmp_lt_u32 s67,s59
ds_read_b128 v[56:59],v2 offset:11264
v_mul_f32 v32,v32,v80
v_mul_f32 v33,v33,v81
v_mul_f32 v34,v34,v82
v_mul_f32 v35,v35,v83
v_cvt_pk_bf16_f32 v86,v32,v33
v_cvt_pk_bf16_f32 v87,v34,v35
v_mfma_f32_16x16x32_bf16 v[36:39],a[168:171],v[64:67],v[36:39]
buffer_store_dwordx4 v[84:87],v3,s[36:39],s73 offen offset:0 sc1
ds_read_b128 v[60:63],v2 offset:12288
v_xor_b32 v8,16384,v8
v_mfma_f32_16x16x32_bf16 v[40:43],a[172:175],v[64:67],v[40:43]
v_mfma_f32_16x16x32_bf16 v[36:39],a[176:179],v[68:71],v[36:39]
ds_read_b128 v[64:67],v2 offset:13312
v_mfma_f32_16x16x32_bf16 v[40:43],a[180:183],v[68:71],v[40:43]
v_mfma_f32_16x16x32_bf16 v[36:39],a[184:187],v[72:75],v[36:39]
s_waitcnt lgkmcnt(2)
ds_read_b128 v[68:71],v2 offset:14336
v_mfma_f32_16x16x32_bf16 v[40:43],a[188:191],v[72:75],v[40:43]
s_cbranch_scc1 Lg1_d0w2_%=
s_branch Lg1_slow0_%=
Lg1_d0w3_%=:
v_mfma_f32_16x16x32_bf16 v[28:31],a[96:99],v[44:47],0
s_add_u32 m0,s62,16384
ds_read_b128 v[72:75],v2 offset:11264
s_lshr_b32 s73,s47,0
v_mfma_f32_16x16x32_bf16 v[32:35],a[100:103],v[44:47],0
s_lshr_b32 s72,s66,6
s_lshr_b32 s71,s62,2
s_cmp_lt_u32 s66,4194304
v_mfma_f32_16x16x32_bf16 v[28:31],a[104:107],v[48:51],v[28:31]
s_cselect_b32 s72,s59,s72
ds_read_b128 v[44:47],v2 offset:4096
v_mfma_f32_16x16x32_bf16 v[32:35],a[108:111],v[48:51],v[32:35]
v_fma_f32 v36,-v96,v20,v36
v_fma_f32 v37,-v96,v21,v37
v_mfma_f32_16x16x32_bf16 v[28:31],a[112:115],v[52:55],v[28:31]
buffer_load_dwordx4 v1,s[36:39],s66 offen offset:0 sc1 lds
v_mfma_f32_16x16x32_bf16 v[32:35],a[116:119],v[52:55],v[32:35]
ds_read_b128 v[48:51],v2 offset:5120
v_fma_f32 v38,-v96,v22,v38
v_mfma_f32_16x16x32_bf16 v[28:31],a[120:123],v[56:59],v[28:31]
ds_read_b128 v[52:55],v2 offset:6144
v_fma_f32 v39,-v96,v23,v39
v_mfma_f32_16x16x32_bf16 v[32:35],a[124:127],v[56:59],v[32:35]
s_waitcnt lgkmcnt(3)
v_mul_f32 v76,v36,v36
v_mul_f32 v77,v37,v37
v_mfma_f32_16x16x32_bf16 v[28:31],a[64:67],v[60:63],v[28:31]
buffer_load_dwordx4 v1,s[36:39],s66 offen offset:1024 sc1 lds
v_mfma_f32_16x16x32_bf16 v[32:35],a[68:71],v[60:63],v[32:35]
ds_read_b128 v[56:59],v2 offset:7168
v_mul_f32 v78,v38,v38
v_mfma_f32_16x16x32_bf16 v[28:31],a[72:75],v[64:67],v[28:31]
ds_read_b128 v[60:63],v2 offset:0
v_mul_f32 v79,v39,v39
v_mfma_f32_16x16x32_bf16 v[32:35],a[76:79],v[64:67],v[32:35]
v_fma_f32 v76,v98,v76,v97
v_mul_f32 v76,v36,v76
v_mfma_f32_16x16x32_bf16 v[28:31],a[80:83],v[68:71],v[28:31]
buffer_load_dwordx4 v1,s[36:39],s66 offen offset:2048 sc1 lds
v_mfma_f32_16x16x32_bf16 v[32:35],a[84:87],v[68:71],v[32:35]
ds_read_b128 v[64:67],v2 offset:1024
v_fma_f32 v77,v98,v77,v97
v_mfma_f32_16x16x32_bf16 v[28:31],a[88:91],v[72:75],v[28:31]
ds_read_b128 v[68:71],v2 offset:2048
v_mul_f32 v77,v37,v77
v_mfma_f32_16x16x32_bf16 v[32:35],a[92:95],v[72:75],v[32:35]
s_waitcnt lgkmcnt(3)
v_exp_f32 v76,v76
v_mfma_f32_16x16x32_bf16 v[28:31],a[32:35],v[44:47],v[28:31]
s_waitcnt vmcnt(9)
ds_read_b128 v[72:75],v2 offset:3072
v_fma_f32 v76,v76,v99,v99
v_mfma_f32_16x16x32_bf16 v[32:35],a[36:39],v[44:47],v[32:35]
s_barrier
v_mfma_f32_16x16x32_bf16 v[28:31],a[40:43],v[48:51],v[28:31]
buffer_load_dwordx4 v1,s[36:39],s66 offen offset:3072 sc1 lds
v_mfma_f32_16x16x32_bf16 v[32:35],a[44:47],v[48:51],v[32:35]
s_add_u32 m0,s71,s88
ds_read_b128 v[44:47],v2 offset:28672
v_fma_f32 v78,v98,v78,v97
v_mfma_f32_16x16x32_bf16 v[28:31],a[48:51],v[52:55],v[28:31]
buffer_load_dword v10,s[36:39],s72 offen offset:0 sc1 lds
v_mfma_f32_16x16x32_bf16 v[32:35],a[52:55],v[52:55],v[32:35]
s_xor_b32 s87,s64,65536
ds_read_b128 v[48:51],v2 offset:29696
v_fma_f32 v79,v98,v79,v97
v_mfma_f32_16x16x32_bf16 v[28:31],a[56:59],v[56:59],v[28:31]
s_cmp_ge_u32 s66,s59
ds_read_b128 v[52:55],v2 offset:30720
v_mul_f32 v78,v38,v78
v_mfma_f32_16x16x32_bf16 v[32:35],a[60:63],v[56:59],v[32:35]
s_waitcnt lgkmcnt(3)
v_exp_f32 v77,v77
v_mfma_f32_16x16x32_bf16 v[28:31],a[0:3],v[60:63],v[28:31]
s_cselect_b32 s86,s49,s50
ds_read_b128 v[56:59],v2 offset:31744
v_mul_f32 v79,v39,v79
v_mfma_f32_16x16x32_bf16 v[32:35],a[4:7],v[60:63],v[32:35]
s_cselect_b32 s87,s87,s63
v_exp_f32 v78,v78
v_mfma_f32_16x16x32_bf16 v[28:31],a[8:11],v[64:67],v[28:31]
s_mov_b32 s62,s87
ds_read_b128 v[60:63],v2 offset:24576
v_fma_f32 v77,v77,v99,v99
v_mfma_f32_16x16x32_bf16 v[32:35],a[12:15],v[64:67],v[32:35]
s_mov_b32 m0,s62
v_exp_f32 v79,v79
v_mfma_f32_16x16x32_bf16 v[28:31],a[16:19],v[68:71],v[28:31]
ds_read_b128 v[64:67],v2 offset:25600
v_fma_f32 v78,v78,v99,v99)"
R"(
v_mfma_f32_16x16x32_bf16 v[32:35],a[20:23],v[68:71],v[32:35]
v_rcp_f32 v76,v76
v_mfma_f32_16x16x32_bf16 v[28:31],a[24:27],v[72:75],v[28:31]
ds_read_b128 v[68:71],v2 offset:26624
v_fma_f32 v79,v79,v99,v99
v_mfma_f32_16x16x32_bf16 v[32:35],a[28:31],v[72:75],v[32:35]
s_waitcnt lgkmcnt(3)
v_rcp_f32 v77,v77
v_mfma_f32_16x16x32_bf16 v[28:31],a[224:227],v[44:47],v[28:31]
s_nop 0
ds_read_b128 v[72:75],v2 offset:27648
v_mul_f32 v36,v36,v76
v_mfma_f32_16x16x32_bf16 v[32:35],a[228:231],v[44:47],v[32:35]
s_barrier
v_mfma_f32_16x16x32_bf16 v[28:31],a[232:235],v[48:51],v[28:31]
ds_read_b128 v[44:47],v2 offset:20480
ds_read_b32 v16,v7 offset:0
v_mfma_f32_16x16x32_bf16 v[32:35],a[236:239],v[48:51],v[32:35]
v_rcp_f32 v78,v78
v_mfma_f32_16x16x32_bf16 v[28:31],a[240:243],v[52:55],v[28:31]
ds_read_b128 v[48:51],v2 offset:21504
v_mul_f32 v37,v37,v77
v_mfma_f32_16x16x32_bf16 v[32:35],a[244:247],v[52:55],v[32:35]
v_rcp_f32 v79,v79
v_mfma_f32_16x16x32_bf16 v[28:31],a[248:251],v[56:59],v[28:31]
s_waitcnt lgkmcnt(1)
ds_read_b128 v[52:55],v2 offset:22528
v_mul_f32 v38,v38,v78
v_mfma_f32_16x16x32_bf16 v[32:35],a[252:255],v[56:59],v[32:35]
v_cmp_eq_u32 vcc,s60,v16
v_mul_f32 v39,v39,v79
v_cvt_pk_bf16_f32 v84,v36,v37
v_cvt_pk_bf16_f32 v85,v38,v39
v_mfma_f32_16x16x32_bf16 v[28:31],a[192:195],v[60:63],v[28:31]
s_cmp_eq_u32 vcc_lo,-1
ds_read_b128 v[56:59],v2 offset:23552
v_fma_f32 v40,-v96,v24,v40
v_mfma_f32_16x16x32_bf16 v[32:35],a[196:199],v[60:63],v[32:35]
s_cselect_b32 s67,s86,s59
v_fma_f32 v41,-v96,v25,v41
v_fma_f32 v42,-v96,v26,v42
v_mfma_f32_16x16x32_bf16 v[28:31],a[200:203],v[64:67],v[28:31]
s_lshr_b32 s70,s46,5
ds_read_b128 v[60:63],v2 offset:16384
v_fma_f32 v43,-v96,v27,v43
v_mfma_f32_16x16x32_bf16 v[32:35],a[204:207],v[64:67],v[32:35]
buffer_load_dwordx4 v0,s[36:39],s67 offen offset:0 sc1 lds
v_mfma_f32_16x16x32_bf16 v[28:31],a[208:211],v[68:71],v[28:31]
s_xor_b32 s63,s63,65536
ds_read_b128 v[64:67],v2 offset:17408
v_mul_f32 v80,v40,v40
v_mfma_f32_16x16x32_bf16 v[32:35],a[212:215],v[68:71],v[32:35]
s_add_u32 s53,s52,s55
v_mul_f32 v81,v41,v41
v_mul_f32 v82,v42,v42
v_mfma_f32_16x16x32_bf16 v[28:31],a[216:219],v[72:75],v[28:31]
s_cmp_eq_u32 s53,s56
ds_read_b128 v[68:71],v2 offset:18432
v_mul_f32 v83,v43,v43
v_mfma_f32_16x16x32_bf16 v[32:35],a[220:223],v[72:75],v[32:35]
s_waitcnt lgkmcnt(3)
buffer_load_dwordx4 v0,s[36:39],s67 offen offset:1024 sc1 lds
v_mfma_f32_16x16x32_bf16 v[28:31],a[160:163],v[44:47],v[28:31]
s_waitcnt vmcnt(7)
ds_read_b128 v[72:75],v2 offset:19456
v_fma_f32 v80,v98,v80,v97
v_mfma_f32_16x16x32_bf16 v[32:35],a[164:167],v[44:47],v[32:35]
s_barrier
v_mfma_f32_16x16x32_bf16 v[28:31],a[168:171],v[48:51],v[28:31]
buffer_store_dword v5,v4,s[36:39],s70 offen offset:0 sc1
ds_read_b128 v[44:47],v2 offset:45056
ds_read_b128 v[92:95],v8 offset:0
v_mul_f32 v80,v40,v80
v_mfma_f32_16x16x32_bf16 v[32:35],a[172:175],v[48:51],v[32:35]
s_cselect_b32 s53,s57,s53
v_exp_f32 v80,v80
v_mfma_f32_16x16x32_bf16 v[28:31],a[176:179],v[52:55],v[28:31]
s_cmp_eq_u32 s53,s58
ds_read_b128 v[48:51],v2 offset:46080
v_fma_f32 v80,v80,v99,v99
v_mfma_f32_16x16x32_bf16 v[32:35],a[180:183],v[52:55],v[32:35]
buffer_load_dwordx4 v0,s[36:39],s67 offen offset:2048 sc1 lds
v_mfma_f32_16x16x32_bf16 v[28:31],a[184:187],v[56:59],v[28:31]
s_cselect_b32 s53,s59,s53
ds_read_b128 v[52:55],v2 offset:47104
v_fma_f32 v81,v98,v81,v97
v_mfma_f32_16x16x32_bf16 v[32:35],a[188:191],v[56:59],v[32:35]
s_waitcnt lgkmcnt(4)
v_rcp_f32 v80,v80
v_mfma_f32_16x16x32_bf16 v[28:31],a[128:131],v[60:63],v[28:31]
s_waitcnt lgkmcnt(2)
v_fma_f32 v82,v98,v82,v97
v_fma_f32 v83,v98,v83,v97
v_mul_f32 v81,v41,v81
v_mul_f32 v82,v42,v82
v_mul_f32 v83,v43,v83
v_exp_f32 v81,v81
v_exp_f32 v82,v82
v_exp_f32 v83,v83
v_mfma_f32_16x16x32_bf16 v[32:35],a[132:135],v[60:63],v[32:35]
s_cmp_ge_u32 s66,s59
ds_read_b128 v[56:59],v2 offset:48128
buffer_load_dwordx4 v0,s[36:39],s67 offen offset:3072 sc1 lds
v_fma_f32 v81,v81,v99,v99
v_fma_f32 v82,v82,v99,v99
v_fma_f32 v83,v83,v99,v99
v_rcp_f32 v81,v81
v_rcp_f32 v82,v82
v_rcp_f32 v83,v83
v_mfma_f32_16x16x32_bf16 v[28:31],a[136:139],v[64:67],v[28:31]
v_mul_f32 v40,v40,v80
v_mul_f32 v41,v41,v81
v_mul_f32 v42,v42,v82
v_mul_f32 v43,v43,v83
v_cvt_pk_bf16_f32 v86,v40,v41
v_cvt_pk_bf16_f32 v87,v42,v43
v_mfma_f32_16x16x32_bf16 v[32:35],a[140:143],v[64:67],v[32:35]
ds_read_b128 v[60:63],v2 offset:40960
buffer_store_dwordx4 v[84:87],v3,s[36:39],s73 offen offset:0 sc1
v_mfma_f32_16x16x32_bf16 v[28:31],a[144:147],v[68:71],v[28:31]
ds_read_b128 v[64:67],v2 offset:41984
v_mfma_f32_16x16x32_bf16 v[32:35],a[148:151],v[68:71],v[32:35]
v_mfma_f32_16x16x32_bf16 v[28:31],a[152:155],v[72:75],v[28:31]
s_waitcnt lgkmcnt(2)
ds_read_b128 v[68:71],v2 offset:43008
v_mfma_f32_16x16x32_bf16 v[32:35],a[156:159],v[72:75],v[32:35]
s_cbranch_scc1 Lg1_slow1_%=
Lg1_d1w3_%=:
v_mfma_f32_16x16x32_bf16 v[36:39],a[96:99],v[44:47],0
s_add_u32 m0,s62,16384
ds_read_b128 v[72:75],v2 offset:44032
s_lshr_b32 s73,s48,0
v_mfma_f32_16x16x32_bf16 v[40:43],a[100:103],v[44:47],0
s_lshr_b32 s72,s67,6
s_lshr_b32 s71,s62,2
s_cmp_lt_u32 s67,4194304
v_mfma_f32_16x16x32_bf16 v[36:39],a[104:107],v[48:51],v[36:39]
s_cselect_b32 s72,s59,s72
ds_read_b128 v[44:47],v2 offset:36864
s_mov_b64 s[44:45],s[46:47]
v_mfma_f32_16x16x32_bf16 v[40:43],a[108:111],v[48:51],v[40:43]
v_fma_f32 v28,-v92,v20,v28
v_fma_f32 v29,-v92,v21,v29
v_mfma_f32_16x16x32_bf16 v[36:39],a[112:115],v[52:55],v[36:39]
buffer_load_dwordx4 v1,s[36:39],s67 offen offset:0 sc1 lds
v_mfma_f32_16x16x32_bf16 v[40:43],a[116:119],v[52:55],v[40:43]
ds_read_b128 v[48:51],v2 offset:37888
v_fma_f32 v30,-v92,v22,v30
v_mfma_f32_16x16x32_bf16 v[36:39],a[120:123],v[56:59],v[36:39]
ds_read_b128 v[52:55],v2 offset:38912
v_fma_f32 v31,-v92,v23,v31
v_mfma_f32_16x16x32_bf16 v[40:43],a[124:127],v[56:59],v[40:43]
s_waitcnt lgkmcnt(3)
v_mul_f32 v76,v28,v28
v_mul_f32 v77,v29,v29
v_mfma_f32_16x16x32_bf16 v[36:39],a[64:67],v[60:63],v[36:39]
buffer_load_dwordx4 v1,s[36:39],s67 offen offset:1024 sc1 lds
v_mfma_f32_16x16x32_bf16 v[40:43],a[68:71],v[60:63],v[40:43]
ds_read_b128 v[56:59],v2 offset:39936
v_mul_f32 v78,v30,v30
v_mfma_f32_16x16x32_bf16 v[36:39],a[72:75],v[64:67],v[36:39]
ds_read_b128 v[60:63],v2 offset:32768
v_mul_f32 v79,v31,v31
v_mfma_f32_16x16x32_bf16 v[40:43],a[76:79],v[64:67],v[40:43]
v_fma_f32 v76,v94,v76,v93
v_mul_f32 v76,v28,v76
v_mfma_f32_16x16x32_bf16 v[36:39],a[80:83],v[68:71],v[36:39]
buffer_load_dwordx4 v1,s[36:39],s67 offen offset:2048 sc1 lds
v_mfma_f32_16x16x32_bf16 v[40:43],a[84:87],v[68:71],v[40:43]
ds_read_b128 v[64:67],v2 offset:33792
v_fma_f32 v77,v94,v77,v93
v_mfma_f32_16x16x32_bf16 v[36:39],a[88:91],v[72:75],v[36:39]
ds_read_b128 v[68:71],v2 offset:34816
v_mul_f32 v77,v29,v77
v_mfma_f32_16x16x32_bf16 v[40:43],a[92:95],v[72:75],v[40:43]
s_waitcnt lgkmcnt(3)
v_exp_f32 v76,v76
v_mfma_f32_16x16x32_bf16 v[36:39],a[32:35],v[44:47],v[36:39]
s_waitcnt vmcnt(9)
ds_read_b128 v[72:75],v2 offset:35840
v_fma_f32 v76,v76,v95,v95
v_mfma_f32_16x16x32_bf16 v[40:43],a[36:39],v[44:47],v[40:43]
s_barrier
v_mfma_f32_16x16x32_bf16 v[36:39],a[40:43],v[48:51],v[36:39]
buffer_load_dwordx4 v1,s[36:39],s67 offen offset:3072 sc1 lds
v_mfma_f32_16x16x32_bf16 v[40:43],a[44:47],v[48:51],v[40:43]
s_add_u32 m0,s71,s88
ds_read_b128 v[44:47],v2 offset:61440
v_fma_f32 v78,v94,v78,v93
v_mfma_f32_16x16x32_bf16 v[36:39],a[48:51],v[52:55],v[36:39]
buffer_load_dword v10,s[36:39],s72 offen offset:0 sc1 lds
v_mfma_f32_16x16x32_bf16 v[40:43],a[52:55],v[52:55],v[40:43]
s_xor_b32 s87,s63,65536
ds_read_b128 v[48:51],v2 offset:62464
v_fma_f32 v79,v94,v79,v93
v_mfma_f32_16x16x32_bf16 v[36:39],a[56:59],v[56:59],v[36:39]
s_cmp_ge_u32 s67,s59
ds_read_b128 v[52:55],v2 offset:63488
v_mul_f32 v78,v30,v78
v_mfma_f32_16x16x32_bf16 v[40:43],a[60:63],v[56:59],v[40:43])"
R"(
s_waitcnt lgkmcnt(3)
v_exp_f32 v77,v77
v_mfma_f32_16x16x32_bf16 v[36:39],a[0:3],v[60:63],v[36:39]
s_cselect_b32 s86,s50,s51
ds_read_b128 v[56:59],v2 offset:64512
v_mul_f32 v79,v31,v79
v_mfma_f32_16x16x32_bf16 v[40:43],a[4:7],v[60:63],v[40:43]
s_cselect_b32 s87,s87,s64
v_exp_f32 v78,v78
v_mfma_f32_16x16x32_bf16 v[36:39],a[8:11],v[64:67],v[36:39]
s_mov_b32 s62,s87
ds_read_b128 v[60:63],v2 offset:57344
v_fma_f32 v77,v77,v95,v95
v_mfma_f32_16x16x32_bf16 v[40:43],a[12:15],v[64:67],v[40:43]
s_mov_b32 m0,s62
v_exp_f32 v79,v79
v_mfma_f32_16x16x32_bf16 v[36:39],a[16:19],v[68:71],v[36:39]
ds_read_b128 v[64:67],v2 offset:58368
v_fma_f32 v78,v78,v95,v95
v_mfma_f32_16x16x32_bf16 v[40:43],a[20:23],v[68:71],v[40:43]
v_rcp_f32 v76,v76
v_mfma_f32_16x16x32_bf16 v[36:39],a[24:27],v[72:75],v[36:39]
ds_read_b128 v[68:71],v2 offset:59392
v_fma_f32 v79,v79,v95,v95
v_mfma_f32_16x16x32_bf16 v[40:43],a[28:31],v[72:75],v[40:43]
s_waitcnt lgkmcnt(3)
v_rcp_f32 v77,v77
v_mfma_f32_16x16x32_bf16 v[36:39],a[224:227],v[44:47],v[36:39]
s_nop 0
ds_read_b128 v[72:75],v2 offset:60416
v_mul_f32 v28,v28,v76
v_mfma_f32_16x16x32_bf16 v[40:43],a[228:231],v[44:47],v[40:43]
s_barrier
v_mfma_f32_16x16x32_bf16 v[36:39],a[232:235],v[48:51],v[36:39]
ds_read_b128 v[44:47],v2 offset:53248
ds_read_b32 v16,v7 offset:256
v_mfma_f32_16x16x32_bf16 v[40:43],a[236:239],v[48:51],v[40:43]
v_rcp_f32 v78,v78
v_mfma_f32_16x16x32_bf16 v[36:39],a[240:243],v[52:55],v[36:39]
ds_read_b128 v[48:51],v2 offset:54272
v_mul_f32 v29,v29,v77
v_mfma_f32_16x16x32_bf16 v[40:43],a[244:247],v[52:55],v[40:43]
v_rcp_f32 v79,v79
v_mfma_f32_16x16x32_bf16 v[36:39],a[248:251],v[56:59],v[36:39]
s_waitcnt lgkmcnt(1)
ds_read_b128 v[52:55],v2 offset:55296
v_mul_f32 v30,v30,v78
v_mfma_f32_16x16x32_bf16 v[40:43],a[252:255],v[56:59],v[40:43]
v_cmp_eq_u32 vcc,s60,v16
v_mul_f32 v31,v31,v79
v_cvt_pk_bf16_f32 v84,v28,v29
v_cvt_pk_bf16_f32 v85,v30,v31
v_mfma_f32_16x16x32_bf16 v[36:39],a[192:195],v[60:63],v[36:39]
s_cmp_eq_u32 vcc_lo,-1
ds_read_b128 v[56:59],v2 offset:56320
v_fma_f32 v32,-v92,v24,v32
v_mfma_f32_16x16x32_bf16 v[40:43],a[196:199],v[60:63],v[40:43]
s_cselect_b32 s66,s86,s59
v_fma_f32 v33,-v92,v25,v33
v_fma_f32 v34,-v92,v26,v34
v_mfma_f32_16x16x32_bf16 v[36:39],a[200:203],v[64:67],v[36:39]
s_lshr_b32 s70,s47,5
ds_read_b128 v[60:63],v2 offset:49152
v_fma_f32 v35,-v92,v27,v35
v_mfma_f32_16x16x32_bf16 v[40:43],a[204:207],v[64:67],v[40:43]
buffer_load_dwordx4 v0,s[36:39],s66 offen offset:0 sc1 lds
v_mfma_f32_16x16x32_bf16 v[36:39],a[208:211],v[68:71],v[36:39]
s_xor_b32 s64,s64,65536
ds_read_b128 v[64:67],v2 offset:50176
v_mul_f32 v80,v32,v32
v_mfma_f32_16x16x32_bf16 v[40:43],a[212:215],v[68:71],v[40:43]
s_add_u32 s74,s53,s55
v_mul_f32 v81,v33,v33
v_mul_f32 v82,v34,v34
v_mfma_f32_16x16x32_bf16 v[36:39],a[216:219],v[72:75],v[36:39]
s_cmp_eq_u32 s74,s56
ds_read_b128 v[68:71],v2 offset:51200
v_mul_f32 v83,v35,v35
v_mfma_f32_16x16x32_bf16 v[40:43],a[220:223],v[72:75],v[40:43]
s_waitcnt lgkmcnt(3)
buffer_load_dwordx4 v0,s[36:39],s66 offen offset:1024 sc1 lds
v_mfma_f32_16x16x32_bf16 v[36:39],a[160:163],v[44:47],v[36:39]
s_waitcnt vmcnt(7)
ds_read_b128 v[72:75],v2 offset:52224
v_xor_b32 v2,65536,v2
v_mfma_f32_16x16x32_bf16 v[40:43],a[164:167],v[44:47],v[40:43]
s_barrier
v_mfma_f32_16x16x32_bf16 v[36:39],a[168:171],v[48:51],v[36:39]
buffer_store_dword v5,v4,s[36:39],s70 offen offset:0 sc1
ds_read_b128 v[44:47],v2 offset:12288
ds_read_b128 v[96:99],v8 offset:8192
v_fma_f32 v80,v94,v80,v93
v_mfma_f32_16x16x32_bf16 v[40:43],a[172:175],v[48:51],v[40:43]
s_cselect_b32 s74,s57,s74
v_mul_f32 v80,v32,v80
v_fma_f32 v81,v94,v81,v93
v_mfma_f32_16x16x32_bf16 v[36:39],a[176:179],v[52:55],v[36:39]
s_cmp_eq_u32 s74,s58
ds_read_b128 v[48:51],v2 offset:13312
v_mul_f32 v81,v33,v81
v_mfma_f32_16x16x32_bf16 v[40:43],a[180:183],v[52:55],v[40:43]
buffer_load_dwordx4 v0,s[36:39],s66 offen offset:2048 sc1 lds
v_mfma_f32_16x16x32_bf16 v[36:39],a[184:187],v[56:59],v[36:39]
s_cselect_b32 s74,s59,s74
ds_read_b128 v[52:55],v2 offset:14336
v_fma_f32 v82,v94,v82,v93
v_mfma_f32_16x16x32_bf16 v[40:43],a[188:191],v[56:59],v[40:43]
s_waitcnt lgkmcnt(4)
v_exp_f32 v80,v80
v_mfma_f32_16x16x32_bf16 v[36:39],a[128:131],v[60:63],v[36:39]
s_waitcnt lgkmcnt(2)
v_fma_f32 v83,v94,v83,v93
v_mul_f32 v82,v34,v82
v_mul_f32 v83,v35,v83
v_exp_f32 v81,v81
v_exp_f32 v82,v82
v_exp_f32 v83,v83
v_mfma_f32_16x16x32_bf16 v[40:43],a[132:135],v[60:63],v[40:43]
s_mov_b64 s[46:47],s[48:49]
ds_read_b128 v[56:59],v2 offset:15360
buffer_load_dwordx4 v0,s[36:39],s66 offen offset:3072 sc1 lds
v_fma_f32 v80,v80,v95,v95
v_fma_f32 v81,v81,v95,v95
v_fma_f32 v82,v82,v95,v95
v_fma_f32 v83,v83,v95,v95
v_rcp_f32 v80,v80
v_rcp_f32 v81,v81
v_rcp_f32 v82,v82
v_rcp_f32 v83,v83
v_mfma_f32_16x16x32_bf16 v[36:39],a[136:139],v[64:67],v[36:39]
s_mov_b64 s[48:49],s[50:51]
v_mul_f32 v32,v32,v80
v_mul_f32 v33,v33,v81
v_mul_f32 v34,v34,v82
v_mul_f32 v35,v35,v83
v_cvt_pk_bf16_f32 v86,v32,v33
v_cvt_pk_bf16_f32 v87,v34,v35
v_mfma_f32_16x16x32_bf16 v[40:43],a[140:143],v[64:67],v[40:43]
s_mov_b64 s[50:51],s[52:53]
ds_read_b128 v[60:63],v2 offset:8192
buffer_store_dwordx4 v[84:87],v3,s[36:39],s73 offen offset:0 sc1
v_xor_b32 v8,16384,v8
s_mov_b32 s52,s74
s_cmp_lt_u32 s67,s59
v_mfma_f32_16x16x32_bf16 v[36:39],a[144:147],v[68:71],v[36:39]
ds_read_b128 v[64:67],v2 offset:9216
v_mfma_f32_16x16x32_bf16 v[40:43],a[148:151],v[68:71],v[40:43]
v_mfma_f32_16x16x32_bf16 v[36:39],a[152:155],v[72:75],v[36:39]
s_waitcnt lgkmcnt(2)
ds_read_b128 v[68:71],v2 offset:10240
v_mfma_f32_16x16x32_bf16 v[40:43],a[156:159],v[72:75],v[40:43]
s_cbranch_scc1 Lg1_d0w3_%=
s_branch Lg1_slow0_%=
Lg1_slow0_%=:
s_cmp_lt_u32 s66,s59
s_cbranch_scc1 Lg1_gs0_%=
Lg1_slowb0_%=:
s_cmp_eq_u32 s54,1
s_cselect_b32 s68,s49,s48
s_lshr_b32 s69,s68,5
s_cmp_lt_u32 s68,4194304
s_cselect_b32 s68,0xffffcfff,-1
s_and_b32 s68,s68,s69
s_add_u32 m0,s78,8192
s_nop 0
buffer_load_dword v6,s[36:39],s68 offen offset:0 sc1 lds
s_lshr_b32 s73,s47,0
v_fma_f32 v36,-v96,v20,v36
v_fma_f32 v37,-v96,v21,v37
v_fma_f32 v38,-v96,v22,v38
v_fma_f32 v39,-v96,v23,v39
v_mul_f32 v76,v36,v36
v_mul_f32 v77,v37,v37
v_mul_f32 v78,v38,v38
v_mul_f32 v79,v39,v39
v_fma_f32 v76,v98,v76,v97
v_fma_f32 v77,v98,v77,v97
v_fma_f32 v78,v98,v78,v97
v_fma_f32 v79,v98,v79,v97
v_mul_f32 v76,v36,v76
v_mul_f32 v77,v37,v77
v_mul_f32 v78,v38,v78
v_mul_f32 v79,v39,v79
v_exp_f32 v76,v76
v_exp_f32 v77,v77
v_exp_f32 v78,v78
v_exp_f32 v79,v79
v_fma_f32 v76,v76,v99,v99
v_fma_f32 v77,v77,v99,v99
v_fma_f32 v78,v78,v99,v99
v_fma_f32 v79,v79,v99,v99
v_rcp_f32 v76,v76
v_rcp_f32 v77,v77
v_rcp_f32 v78,v78
v_rcp_f32 v79,v79
v_mul_f32 v36,v36,v76
v_mul_f32 v37,v37,v77
v_mul_f32 v38,v38,v78
v_mul_f32 v39,v39,v79
v_cvt_pk_bf16_f32 v84,v36,v37)"
R"(
v_cvt_pk_bf16_f32 v85,v38,v39
s_add_u32 m0,s78,8448
s_nop 0
buffer_load_dword v6,s[36:39],s68 offen offset:0 sc1 lds
v_fma_f32 v40,-v96,v24,v40
v_fma_f32 v41,-v96,v25,v41
v_fma_f32 v42,-v96,v26,v42
v_fma_f32 v43,-v96,v27,v43
v_mul_f32 v80,v40,v40
v_mul_f32 v81,v41,v41
v_mul_f32 v82,v42,v42
v_mul_f32 v83,v43,v43
v_fma_f32 v80,v98,v80,v97
v_fma_f32 v81,v98,v81,v97
v_fma_f32 v82,v98,v82,v97
v_fma_f32 v83,v98,v83,v97
v_mul_f32 v80,v40,v80
v_mul_f32 v81,v41,v81
v_mul_f32 v82,v42,v82
v_mul_f32 v83,v43,v83
v_exp_f32 v80,v80
v_exp_f32 v81,v81
v_exp_f32 v82,v82
v_exp_f32 v83,v83
v_fma_f32 v80,v80,v99,v99
v_fma_f32 v81,v81,v99,v99
v_fma_f32 v82,v82,v99,v99
v_fma_f32 v83,v83,v99,v99
v_rcp_f32 v80,v80
v_rcp_f32 v81,v81
v_rcp_f32 v82,v82
v_rcp_f32 v83,v83
v_mul_f32 v40,v40,v80
v_mul_f32 v41,v41,v81
v_mul_f32 v42,v42,v82
v_mul_f32 v43,v43,v83
v_cvt_pk_bf16_f32 v86,v40,v41
v_cvt_pk_bf16_f32 v87,v42,v43
buffer_store_dwordx4 v[84:87],v3,s[36:39],s73 offen offset:0 sc1
s_cmp_ge_u32 s48,s59
s_cbranch_scc1 Lg1_done0_%=
s_add_u32 m0,s78,16384
s_nop 0
buffer_load_dword v6,s[36:39],s68 offen offset:0 sc1 lds
s_waitcnt vmcnt(3)
s_barrier
ds_read_b32 v16,v7 offset:8192
s_lshr_b32 s70,s46,5
buffer_store_dword v5,v4,s[36:39],s70 offen offset:0 sc1
s_mov_b32 s46,s59
s_mov_b32 s69,18432
s_waitcnt lgkmcnt(0)
v_cmp_eq_u32 vcc,s60,v16
s_nop 0
s_cmp_eq_u32 vcc_lo,-1
s_cbranch_scc1 Lg1_go0_%=
s_add_u32 m0,s78,16640
s_nop 0
buffer_load_dword v6,s[36:39],s68 offen offset:0 sc1 lds
s_waitcnt vmcnt(4)
s_barrier
ds_read_b32 v16,v7 offset:8448
s_mov_b32 s69,18688
s_waitcnt lgkmcnt(0)
v_cmp_eq_u32 vcc,s60,v16
s_nop 0
s_cmp_eq_u32 vcc_lo,-1
s_cbranch_scc1 Lg1_go0_%=
s_add_u32 m0,s78,8192
s_nop 0
buffer_load_dword v6,s[36:39],s68 offen offset:0 sc1 lds
s_waitcnt vmcnt(3)
s_barrier
ds_read_b32 v16,v7 offset:16384
s_lshr_b32 s70,s47,5
buffer_store_dword v5,v4,s[36:39],s70 offen offset:0 sc1
s_mov_b32 s47,s59
s_mov_b32 s69,10240
s_waitcnt lgkmcnt(0)
v_cmp_eq_u32 vcc,s60,v16
s_nop 0
s_cmp_eq_u32 vcc_lo,-1
s_cbranch_scc1 Lg1_go0_%=
s_add_u32 m0,s78,8448
s_nop 0
buffer_load_dword v6,s[36:39],s68 offen offset:0 sc1 lds
s_waitcnt vmcnt(3)
s_barrier
ds_read_b32 v16,v7 offset:16640
s_mov_b32 s69,10496
s_waitcnt lgkmcnt(0)
v_cmp_eq_u32 vcc,s60,v16
s_nop 0
s_cmp_eq_u32 vcc_lo,-1
s_cbranch_scc1 Lg1_go0_%=
Lg1_poll0_%=:
s_add_u32 m0,s78,16384
s_nop 0
buffer_load_dword v6,s[36:39],s68 offen offset:0 sc1 lds
s_waitcnt vmcnt(2)
s_barrier
ds_read_b32 v16,v7 offset:8192
s_mov_b32 s69,18432
s_waitcnt lgkmcnt(0)
v_cmp_eq_u32 vcc,s60,v16
s_nop 0
s_cmp_eq_u32 vcc_lo,-1
s_cbranch_scc1 Lg1_go0_%=
s_sleep 4
s_add_u32 m0,s78,16640
s_nop 0
buffer_load_dword v6,s[36:39],s68 offen offset:0 sc1 lds
s_waitcnt vmcnt(2)
s_barrier
ds_read_b32 v16,v7 offset:8448
s_mov_b32 s69,18688
s_waitcnt lgkmcnt(0)
v_cmp_eq_u32 vcc,s60,v16
s_nop 0
s_cmp_eq_u32 vcc_lo,-1
s_cbranch_scc1 Lg1_go0_%=
s_sleep 4
s_add_u32 m0,s78,8192
s_nop 0
buffer_load_dword v6,s[36:39],s68 offen offset:0 sc1 lds
s_waitcnt vmcnt(2)
s_barrier
ds_read_b32 v16,v7 offset:16384
s_mov_b32 s69,10240
s_waitcnt lgkmcnt(0)
v_cmp_eq_u32 vcc,s60,v16
s_nop 0
s_cmp_eq_u32 vcc_lo,-1
s_cbranch_scc1 Lg1_go0_%=
s_sleep 4
s_add_u32 m0,s78,8448
s_nop 0
buffer_load_dword v6,s[36:39],s68 offen offset:0 sc1 lds
s_waitcnt vmcnt(2)
s_barrier
ds_read_b32 v16,v7 offset:16640
s_mov_b32 s69,10496
s_waitcnt lgkmcnt(0)
v_cmp_eq_u32 vcc,s60,v16
s_nop 0
s_cmp_eq_u32 vcc_lo,-1
s_cbranch_scc1 Lg1_go0_%=
s_sleep 4
s_add_u32 s79,s79,1
s_cmp_lt_u32 s79,20000
s_cbranch_scc1 Lg1_poll0_%=
Lg1_go0_%=:
s_xor_b32 s62,s63,65536
s_mov_b32 m0,s62
s_nop 0
buffer_load_dwordx4 v0,s[36:39],s48 offen offset:0 sc1 lds
buffer_load_dwordx4 v0,s[36:39],s48 offen offset:1024 sc1 lds
buffer_load_dwordx4 v0,s[36:39],s48 offen offset:2048 sc1 lds
buffer_load_dwordx4 v0,s[36:39],s48 offen offset:3072 sc1 lds
s_add_u32 m0,s62,16384
s_nop 0
buffer_load_dwordx4 v1,s[36:39],s48 offen offset:0 sc1 lds
buffer_load_dwordx4 v1,s[36:39],s48 offen offset:1024 sc1 lds
buffer_load_dwordx4 v1,s[36:39],s48 offen offset:2048 sc1 lds
buffer_load_dwordx4 v1,s[36:39],s48 offen offset:3072 sc1 lds
s_lshr_b32 s72,s48,6
s_lshr_b32 s71,s62,2
s_cmp_lt_u32 s48,4194304
s_cselect_b32 s72,s59,s72
s_add_u32 m0,s71,s88
s_nop 0
buffer_load_dword v10,s[36:39],s72 offen offset:0 sc1 lds
s_cmp_eq_u32 vcc_hi,-1
s_cselect_b32 s66,s49,s59
s_xor_b32 s62,s64,65536
s_cmp_ge_u32 s66,s59
s_cselect_b32 s68,s49,s50
s_lshr_b32 s69,s68,5
s_cmp_lt_u32 s68,4194304
s_cselect_b32 s68,0xffffcfff,-1
s_and_b32 s68,s68,s69
s_add_u32 m0,s78,0
s_nop 0
buffer_load_dword v6,s[36:39],s68 offen offset:0 sc1 lds
s_mov_b32 m0,s62
s_nop 0
buffer_load_dwordx4 v0,s[36:39],s66 offen offset:0 sc1 lds
buffer_load_dwordx4 v0,s[36:39],s66 offen offset:1024 sc1 lds
buffer_load_dwordx4 v0,s[36:39],s66 offen offset:2048 sc1 lds
buffer_load_dwordx4 v0,s[36:39],s66 offen offset:3072 sc1 lds
s_waitcnt vmcnt(10)
s_barrier
s_lshl_b32 s70,s54,12
v_add_u32 v76,s70,v2
v_xor_b32 v77,4096,v76
ds_read_b128 v[44:47],v76 offset:0
ds_read_b128 v[48:51],v76 offset:1024
ds_read_b128 v[52:55],v76 offset:2048
ds_read_b128 v[56:59],v76 offset:3072
ds_read_b128 v[60:63],v77 offset:0)"
R"(
ds_read_b128 v[64:67],v77 offset:1024
ds_read_b128 v[68:71],v77 offset:2048
s_lshr_b32 s70,s46,5
buffer_store_dword v5,v4,s[36:39],s70 offen offset:0 sc1
s_mov_b32 s46,s59
s_lshr_b32 s70,s47,5
buffer_store_dword v5,v4,s[36:39],s70 offen offset:0 sc1
s_mov_b32 s47,s59
s_lshr_b32 s70,s59,5
buffer_store_dword v5,v4,s[36:39],s70 offen offset:0 sc1
s_waitcnt lgkmcnt(3)
s_setpc_b64 s[90:91]
Lg1_gs0_%=:
s_cmp_eq_u32 s54,1
s_cselect_b32 s68,s49,s48
s_lshr_b32 s69,s68,5
s_cmp_lt_u32 s68,4194304
s_cselect_b32 s68,0xffffcfff,-1
s_and_b32 s68,s68,s69
s_lshr_b32 s73,s47,0
s_add_u32 m0,s78,8192
s_nop 0
buffer_load_dword v6,s[36:39],s68 offen offset:0 sc1 lds
v_fma_f32 v36,-v96,v20,v36
v_fma_f32 v37,-v96,v21,v37
v_fma_f32 v38,-v96,v22,v38
v_fma_f32 v39,-v96,v23,v39
v_mul_f32 v76,v36,v36
v_mul_f32 v77,v37,v37
v_mul_f32 v78,v38,v38
v_mul_f32 v79,v39,v39
v_fma_f32 v76,v98,v76,v97
v_fma_f32 v77,v98,v77,v97
v_fma_f32 v78,v98,v78,v97
s_add_u32 m0,s62,16384
s_nop 0
buffer_load_dwordx4 v1,s[36:39],s66 offen offset:0 sc1 lds
v_fma_f32 v79,v98,v79,v97
v_mul_f32 v76,v36,v76
v_mul_f32 v77,v37,v77
v_mul_f32 v78,v38,v78
v_mul_f32 v79,v39,v79
v_exp_f32 v76,v76
v_exp_f32 v77,v77
v_exp_f32 v78,v78
v_exp_f32 v79,v79
v_fma_f32 v76,v76,v99,v99
v_fma_f32 v77,v77,v99,v99
buffer_load_dwordx4 v1,s[36:39],s66 offen offset:1024 sc1 lds
v_fma_f32 v78,v78,v99,v99
v_fma_f32 v79,v79,v99,v99
v_rcp_f32 v76,v76
v_rcp_f32 v77,v77
v_rcp_f32 v78,v78
v_rcp_f32 v79,v79
v_mul_f32 v36,v36,v76
v_mul_f32 v37,v37,v77
v_mul_f32 v38,v38,v78
v_mul_f32 v39,v39,v79
v_cvt_pk_bf16_f32 v84,v36,v37
buffer_load_dwordx4 v1,s[36:39],s66 offen offset:2048 sc1 lds
v_cvt_pk_bf16_f32 v85,v38,v39
v_fma_f32 v40,-v96,v24,v40
v_fma_f32 v41,-v96,v25,v41
v_fma_f32 v42,-v96,v26,v42
v_fma_f32 v43,-v96,v27,v43
v_mul_f32 v80,v40,v40
v_mul_f32 v81,v41,v41
v_mul_f32 v82,v42,v42
v_mul_f32 v83,v43,v43
v_fma_f32 v80,v98,v80,v97
v_fma_f32 v81,v98,v81,v97
buffer_load_dwordx4 v1,s[36:39],s66 offen offset:3072 sc1 lds
v_fma_f32 v82,v98,v82,v97
v_fma_f32 v83,v98,v83,v97
v_mul_f32 v80,v40,v80
v_mul_f32 v81,v41,v81
v_mul_f32 v82,v42,v82
v_mul_f32 v83,v43,v83
v_exp_f32 v80,v80
v_exp_f32 v81,v81
v_exp_f32 v82,v82
v_exp_f32 v83,v83
v_fma_f32 v80,v80,v99,v99
s_lshr_b32 s72,s66,6
s_lshr_b32 s71,s62,2
s_cmp_lt_u32 s66,4194304
s_cselect_b32 s72,s59,s72
s_add_u32 m0,s71,s88
s_nop 0
buffer_load_dword v10,s[36:39],s72 offen offset:0 sc1 lds
v_fma_f32 v81,v81,v99,v99
v_fma_f32 v82,v82,v99,v99
v_fma_f32 v83,v83,v99,v99
v_rcp_f32 v80,v80
v_rcp_f32 v81,v81
v_rcp_f32 v82,v82
v_rcp_f32 v83,v83
v_mul_f32 v40,v40,v80
v_mul_f32 v41,v41,v81
v_mul_f32 v42,v42,v82
v_mul_f32 v43,v43,v83
v_cvt_pk_bf16_f32 v86,v40,v41
v_cvt_pk_bf16_f32 v87,v42,v43
buffer_store_dwordx4 v[84:87],v3,s[36:39],s73 offen offset:0 sc1
s_waitcnt vmcnt(6)
s_barrier
ds_read_b32 v16,v7 offset:8192
s_lshl_b32 s70,s54,12
v_add_u32 v76,s70,v2
v_xor_b32 v77,4096,v76
ds_read_b128 v[44:47],v76 offset:0
ds_read_b128 v[48:51],v76 offset:1024
ds_read_b128 v[52:55],v76 offset:2048
ds_read_b128 v[56:59],v76 offset:3072
ds_read_b128 v[60:63],v77 offset:0
ds_read_b128 v[64:67],v77 offset:1024
ds_read_b128 v[68:71],v77 offset:2048
s_lshr_b32 s70,s46,5
buffer_store_dword v5,v4,s[36:39],s70 offen offset:0 sc1
s_waitcnt lgkmcnt(7)
v_cmp_eq_u32 vcc,s60,v16
s_nop 0
s_cmp_eq_u32 vcc_hi,-1
s_cselect_b32 s66,s49,s59
s_xor_b32 s62,s64,65536
s_cmp_ge_u32 s66,s59
s_cselect_b32 s68,s49,s50
s_lshr_b32 s69,s68,5
s_cmp_lt_u32 s68,4194304
s_cselect_b32 s68,0xffffcfff,-1
s_and_b32 s68,s68,s69
s_add_u32 m0,s78,0
s_nop 0
buffer_load_dword v6,s[36:39],s68 offen offset:0 sc1 lds
s_mov_b32 m0,s62
s_nop 0
buffer_load_dwordx4 v0,s[36:39],s66 offen offset:0 sc1 lds
buffer_load_dwordx4 v0,s[36:39],s66 offen offset:1024 sc1 lds
buffer_load_dwordx4 v0,s[36:39],s66 offen offset:2048 sc1 lds
buffer_load_dwordx4 v0,s[36:39],s66 offen offset:3072 sc1 lds
s_mov_b32 s46,s47
s_mov_b32 s47,s59
s_lshr_b32 s70,s59,5
buffer_store_dword v5,v4,s[36:39],s70 offen offset:0 sc1
s_waitcnt lgkmcnt(3)
s_setpc_b64 s[90:91]
Lg1_done0_%=:
s_waitcnt vmcnt(0)
s_barrier
s_lshr_b32 s70,s46,5
buffer_store_dword v5,v4,s[36:39],s70 offen offset:0 sc1
s_lshr_b32 s70,s47,5
buffer_store_dword v5,v4,s[36:39],s70 offen offset:0 sc1
s_branch Lg1_done_%=
Lg1_slow1_%=:
s_cmp_lt_u32 s67,s59
s_cbranch_scc1 Lg1_gs1_%=
Lg1_slowb1_%=:
s_cmp_eq_u32 s54,1
s_cselect_b32 s68,s50,s49
s_lshr_b32 s69,s68,5
s_cmp_lt_u32 s68,4194304
s_cselect_b32 s68,0xffffcfff,-1
s_and_b32 s68,s68,s69
s_add_u32 m0,s78,8192
s_nop 0
buffer_load_dword v6,s[36:39],s68 offen offset:0 sc1 lds
s_lshr_b32 s73,s48,0
v_fma_f32 v28,-v92,v20,v28
v_fma_f32 v29,-v92,v21,v29
v_fma_f32 v30,-v92,v22,v30
v_fma_f32 v31,-v92,v23,v31
v_mul_f32 v76,v28,v28
v_mul_f32 v77,v29,v29
v_mul_f32 v78,v30,v30
v_mul_f32 v79,v31,v31
v_fma_f32 v76,v94,v76,v93
v_fma_f32 v77,v94,v77,v93
v_fma_f32 v78,v94,v78,v93
v_fma_f32 v79,v94,v79,v93
v_mul_f32 v76,v28,v76
v_mul_f32 v77,v29,v77
v_mul_f32 v78,v30,v78
v_mul_f32 v79,v31,v79
v_exp_f32 v76,v76
v_exp_f32 v77,v77
v_exp_f32 v78,v78
v_exp_f32 v79,v79
v_fma_f32 v76,v76,v95,v95
v_fma_f32 v77,v77,v95,v95
v_fma_f32 v78,v78,v95,v95
v_fma_f32 v79,v79,v95,v95
v_rcp_f32 v76,v76
v_rcp_f32 v77,v77
v_rcp_f32 v78,v78
v_rcp_f32 v79,v79
v_mul_f32 v28,v28,v76
v_mul_f32 v29,v29,v77
v_mul_f32 v30,v30,v78)"
R"(
v_mul_f32 v31,v31,v79
v_cvt_pk_bf16_f32 v84,v28,v29
v_cvt_pk_bf16_f32 v85,v30,v31
s_add_u32 m0,s78,8448
s_nop 0
buffer_load_dword v6,s[36:39],s68 offen offset:0 sc1 lds
v_fma_f32 v32,-v92,v24,v32
v_fma_f32 v33,-v92,v25,v33
v_fma_f32 v34,-v92,v26,v34
v_fma_f32 v35,-v92,v27,v35
v_mul_f32 v80,v32,v32
v_mul_f32 v81,v33,v33
v_mul_f32 v82,v34,v34
v_mul_f32 v83,v35,v35
v_fma_f32 v80,v94,v80,v93
v_fma_f32 v81,v94,v81,v93
v_fma_f32 v82,v94,v82,v93
v_fma_f32 v83,v94,v83,v93
v_mul_f32 v80,v32,v80
v_mul_f32 v81,v33,v81
v_mul_f32 v82,v34,v82
v_mul_f32 v83,v35,v83
v_exp_f32 v80,v80
v_exp_f32 v81,v81
v_exp_f32 v82,v82
v_exp_f32 v83,v83
v_fma_f32 v80,v80,v95,v95
v_fma_f32 v81,v81,v95,v95
v_fma_f32 v82,v82,v95,v95
v_fma_f32 v83,v83,v95,v95
v_rcp_f32 v80,v80
v_rcp_f32 v81,v81
v_rcp_f32 v82,v82
v_rcp_f32 v83,v83
v_mul_f32 v32,v32,v80
v_mul_f32 v33,v33,v81
v_mul_f32 v34,v34,v82
v_mul_f32 v35,v35,v83
v_cvt_pk_bf16_f32 v86,v32,v33
v_cvt_pk_bf16_f32 v87,v34,v35
buffer_store_dwordx4 v[84:87],v3,s[36:39],s73 offen offset:0 sc1
s_cmp_ge_u32 s49,s59
s_cbranch_scc1 Lg1_done1_%=
s_add_u32 m0,s78,16384
s_nop 0
buffer_load_dword v6,s[36:39],s68 offen offset:0 sc1 lds
s_waitcnt vmcnt(3)
s_barrier
ds_read_b32 v16,v7 offset:8192
s_lshr_b32 s70,s47,5
buffer_store_dword v5,v4,s[36:39],s70 offen offset:0 sc1
s_mov_b32 s47,s59
s_mov_b32 s69,18432
s_waitcnt lgkmcnt(0)
v_cmp_eq_u32 vcc,s60,v16
s_nop 0
s_cmp_eq_u32 vcc_lo,-1
s_cbranch_scc1 Lg1_go1_%=
s_add_u32 m0,s78,16640
s_nop 0
buffer_load_dword v6,s[36:39],s68 offen offset:0 sc1 lds
s_waitcnt vmcnt(4)
s_barrier
ds_read_b32 v16,v7 offset:8448
s_mov_b32 s69,18688
s_waitcnt lgkmcnt(0)
v_cmp_eq_u32 vcc,s60,v16
s_nop 0
s_cmp_eq_u32 vcc_lo,-1
s_cbranch_scc1 Lg1_go1_%=
s_add_u32 m0,s78,8192
s_nop 0
buffer_load_dword v6,s[36:39],s68 offen offset:0 sc1 lds
s_waitcnt vmcnt(3)
s_barrier
ds_read_b32 v16,v7 offset:16384
s_lshr_b32 s70,s48,5
buffer_store_dword v5,v4,s[36:39],s70 offen offset:0 sc1
s_mov_b32 s48,s59
s_mov_b32 s69,10240
s_waitcnt lgkmcnt(0)
v_cmp_eq_u32 vcc,s60,v16
s_nop 0
s_cmp_eq_u32 vcc_lo,-1
s_cbranch_scc1 Lg1_go1_%=
s_add_u32 m0,s78,8448
s_nop 0
buffer_load_dword v6,s[36:39],s68 offen offset:0 sc1 lds
s_waitcnt vmcnt(3)
s_barrier
ds_read_b32 v16,v7 offset:16640
s_mov_b32 s69,10496
s_waitcnt lgkmcnt(0)
v_cmp_eq_u32 vcc,s60,v16
s_nop 0
s_cmp_eq_u32 vcc_lo,-1
s_cbranch_scc1 Lg1_go1_%=
Lg1_poll1_%=:
s_add_u32 m0,s78,16384
s_nop 0
buffer_load_dword v6,s[36:39],s68 offen offset:0 sc1 lds
s_waitcnt vmcnt(2)
s_barrier
ds_read_b32 v16,v7 offset:8192
s_mov_b32 s69,18432
s_waitcnt lgkmcnt(0)
v_cmp_eq_u32 vcc,s60,v16
s_nop 0
s_cmp_eq_u32 vcc_lo,-1
s_cbranch_scc1 Lg1_go1_%=
s_sleep 4
s_add_u32 m0,s78,16640
s_nop 0
buffer_load_dword v6,s[36:39],s68 offen offset:0 sc1 lds
s_waitcnt vmcnt(2)
s_barrier
ds_read_b32 v16,v7 offset:8448
s_mov_b32 s69,18688
s_waitcnt lgkmcnt(0)
v_cmp_eq_u32 vcc,s60,v16
s_nop 0
s_cmp_eq_u32 vcc_lo,-1
s_cbranch_scc1 Lg1_go1_%=
s_sleep 4
s_add_u32 m0,s78,8192
s_nop 0
buffer_load_dword v6,s[36:39],s68 offen offset:0 sc1 lds
s_waitcnt vmcnt(2)
s_barrier
ds_read_b32 v16,v7 offset:16384
s_mov_b32 s69,10240
s_waitcnt lgkmcnt(0)
v_cmp_eq_u32 vcc,s60,v16
s_nop 0
s_cmp_eq_u32 vcc_lo,-1
s_cbranch_scc1 Lg1_go1_%=
s_sleep 4
s_add_u32 m0,s78,8448
s_nop 0
buffer_load_dword v6,s[36:39],s68 offen offset:0 sc1 lds
s_waitcnt vmcnt(2)
s_barrier
ds_read_b32 v16,v7 offset:16640
s_mov_b32 s69,10496
s_waitcnt lgkmcnt(0)
v_cmp_eq_u32 vcc,s60,v16
s_nop 0
s_cmp_eq_u32 vcc_lo,-1
s_cbranch_scc1 Lg1_go1_%=
s_sleep 4
s_add_u32 s79,s79,1
s_cmp_lt_u32 s79,20000
s_cbranch_scc1 Lg1_poll1_%=
Lg1_go1_%=:
s_xor_b32 s62,s64,65536
s_mov_b32 m0,s62
s_nop 0
buffer_load_dwordx4 v0,s[36:39],s49 offen offset:0 sc1 lds
buffer_load_dwordx4 v0,s[36:39],s49 offen offset:1024 sc1 lds
buffer_load_dwordx4 v0,s[36:39],s49 offen offset:2048 sc1 lds
buffer_load_dwordx4 v0,s[36:39],s49 offen offset:3072 sc1 lds
s_add_u32 m0,s62,16384
s_nop 0
buffer_load_dwordx4 v1,s[36:39],s49 offen offset:0 sc1 lds
buffer_load_dwordx4 v1,s[36:39],s49 offen offset:1024 sc1 lds
buffer_load_dwordx4 v1,s[36:39],s49 offen offset:2048 sc1 lds
buffer_load_dwordx4 v1,s[36:39],s49 offen offset:3072 sc1 lds
s_lshr_b32 s72,s49,6
s_lshr_b32 s71,s62,2
s_cmp_lt_u32 s49,4194304
s_cselect_b32 s72,s59,s72
s_add_u32 m0,s71,s88
s_nop 0
buffer_load_dword v10,s[36:39],s72 offen offset:0 sc1 lds
s_cmp_eq_u32 vcc_hi,-1
s_cselect_b32 s67,s50,s59
s_xor_b32 s62,s63,65536
s_cmp_ge_u32 s67,s59
s_cselect_b32 s68,s50,s51
s_lshr_b32 s69,s68,5
s_cmp_lt_u32 s68,4194304
s_cselect_b32 s68,0xffffcfff,-1
s_and_b32 s68,s68,s69
s_add_u32 m0,s78,256
s_nop 0
buffer_load_dword v6,s[36:39],s68 offen offset:0 sc1 lds
s_mov_b32 m0,s62
s_nop 0
buffer_load_dwordx4 v0,s[36:39],s67 offen offset:0 sc1 lds
buffer_load_dwordx4 v0,s[36:39],s67 offen offset:1024 sc1 lds
buffer_load_dwordx4 v0,s[36:39],s67 offen offset:2048 sc1 lds
buffer_load_dwordx4 v0,s[36:39],s67 offen offset:3072 sc1 lds
s_waitcnt vmcnt(10)
s_barrier
s_lshl_b32 s70,s54,12
v_add_u32 v76,s70,v2
v_xor_b32 v77,4096,v76
ds_read_b128 v[44:47],v76 offset:32768
ds_read_b128 v[48:51],v76 offset:33792
ds_read_b128 v[52:55],v76 offset:34816)"
R"(
ds_read_b128 v[56:59],v76 offset:35840
ds_read_b128 v[60:63],v77 offset:32768
ds_read_b128 v[64:67],v77 offset:33792
ds_read_b128 v[68:71],v77 offset:34816
s_lshr_b32 s70,s47,5
buffer_store_dword v5,v4,s[36:39],s70 offen offset:0 sc1
s_mov_b32 s47,s59
s_lshr_b32 s70,s48,5
buffer_store_dword v5,v4,s[36:39],s70 offen offset:0 sc1
s_mov_b32 s48,s59
s_lshr_b32 s70,s59,5
buffer_store_dword v5,v4,s[36:39],s70 offen offset:0 sc1
s_waitcnt lgkmcnt(3)
s_setpc_b64 s[92:93]
Lg1_gs1_%=:
s_cmp_eq_u32 s54,1
s_cselect_b32 s68,s50,s49
s_lshr_b32 s69,s68,5
s_cmp_lt_u32 s68,4194304
s_cselect_b32 s68,0xffffcfff,-1
s_and_b32 s68,s68,s69
s_lshr_b32 s73,s48,0
s_add_u32 m0,s78,8192
s_nop 0
buffer_load_dword v6,s[36:39],s68 offen offset:0 sc1 lds
v_fma_f32 v28,-v92,v20,v28
v_fma_f32 v29,-v92,v21,v29
v_fma_f32 v30,-v92,v22,v30
v_fma_f32 v31,-v92,v23,v31
v_mul_f32 v76,v28,v28
v_mul_f32 v77,v29,v29
v_mul_f32 v78,v30,v30
v_mul_f32 v79,v31,v31
v_fma_f32 v76,v94,v76,v93
v_fma_f32 v77,v94,v77,v93
v_fma_f32 v78,v94,v78,v93
s_add_u32 m0,s62,16384
s_nop 0
buffer_load_dwordx4 v1,s[36:39],s67 offen offset:0 sc1 lds
v_fma_f32 v79,v94,v79,v93
v_mul_f32 v76,v28,v76
v_mul_f32 v77,v29,v77
v_mul_f32 v78,v30,v78
v_mul_f32 v79,v31,v79
v_exp_f32 v76,v76
v_exp_f32 v77,v77
v_exp_f32 v78,v78
v_exp_f32 v79,v79
v_fma_f32 v76,v76,v95,v95
v_fma_f32 v77,v77,v95,v95
buffer_load_dwordx4 v1,s[36:39],s67 offen offset:1024 sc1 lds
v_fma_f32 v78,v78,v95,v95
v_fma_f32 v79,v79,v95,v95
v_rcp_f32 v76,v76
v_rcp_f32 v77,v77
v_rcp_f32 v78,v78
v_rcp_f32 v79,v79
v_mul_f32 v28,v28,v76
v_mul_f32 v29,v29,v77
v_mul_f32 v30,v30,v78
v_mul_f32 v31,v31,v79
v_cvt_pk_bf16_f32 v84,v28,v29
buffer_load_dwordx4 v1,s[36:39],s67 offen offset:2048 sc1 lds
v_cvt_pk_bf16_f32 v85,v30,v31
v_fma_f32 v32,-v92,v24,v32
v_fma_f32 v33,-v92,v25,v33
v_fma_f32 v34,-v92,v26,v34
v_fma_f32 v35,-v92,v27,v35
v_mul_f32 v80,v32,v32
v_mul_f32 v81,v33,v33
v_mul_f32 v82,v34,v34
v_mul_f32 v83,v35,v35
v_fma_f32 v80,v94,v80,v93
v_fma_f32 v81,v94,v81,v93
buffer_load_dwordx4 v1,s[36:39],s67 offen offset:3072 sc1 lds
v_fma_f32 v82,v94,v82,v93
v_fma_f32 v83,v94,v83,v93
v_mul_f32 v80,v32,v80
v_mul_f32 v81,v33,v81
v_mul_f32 v82,v34,v82
v_mul_f32 v83,v35,v83
v_exp_f32 v80,v80
v_exp_f32 v81,v81
v_exp_f32 v82,v82
v_exp_f32 v83,v83
v_fma_f32 v80,v80,v95,v95
s_lshr_b32 s72,s67,6
s_lshr_b32 s71,s62,2
s_cmp_lt_u32 s67,4194304
s_cselect_b32 s72,s59,s72
s_add_u32 m0,s71,s88
s_nop 0
buffer_load_dword v10,s[36:39],s72 offen offset:0 sc1 lds
v_fma_f32 v81,v81,v95,v95
v_fma_f32 v82,v82,v95,v95
v_fma_f32 v83,v83,v95,v95
v_rcp_f32 v80,v80
v_rcp_f32 v81,v81
v_rcp_f32 v82,v82
v_rcp_f32 v83,v83
v_mul_f32 v32,v32,v80
v_mul_f32 v33,v33,v81
v_mul_f32 v34,v34,v82
v_mul_f32 v35,v35,v83
v_cvt_pk_bf16_f32 v86,v32,v33
v_cvt_pk_bf16_f32 v87,v34,v35
buffer_store_dwordx4 v[84:87],v3,s[36:39],s73 offen offset:0 sc1
s_waitcnt vmcnt(6)
s_barrier
ds_read_b32 v16,v7 offset:8192
s_lshl_b32 s70,s54,12
v_add_u32 v76,s70,v2
v_xor_b32 v77,4096,v76
ds_read_b128 v[44:47],v76 offset:32768
ds_read_b128 v[48:51],v76 offset:33792
ds_read_b128 v[52:55],v76 offset:34816
ds_read_b128 v[56:59],v76 offset:35840
ds_read_b128 v[60:63],v77 offset:32768
ds_read_b128 v[64:67],v77 offset:33792
ds_read_b128 v[68:71],v77 offset:34816
s_lshr_b32 s70,s47,5
buffer_store_dword v5,v4,s[36:39],s70 offen offset:0 sc1
s_waitcnt lgkmcnt(7)
v_cmp_eq_u32 vcc,s60,v16
s_nop 0
s_cmp_eq_u32 vcc_hi,-1
s_cselect_b32 s67,s50,s59
s_xor_b32 s62,s63,65536
s_cmp_ge_u32 s67,s59
s_cselect_b32 s68,s50,s51
s_lshr_b32 s69,s68,5
s_cmp_lt_u32 s68,4194304
s_cselect_b32 s68,0xffffcfff,-1
s_and_b32 s68,s68,s69
s_add_u32 m0,s78,256
s_nop 0
buffer_load_dword v6,s[36:39],s68 offen offset:0 sc1 lds
s_mov_b32 m0,s62
s_nop 0
buffer_load_dwordx4 v0,s[36:39],s67 offen offset:0 sc1 lds
buffer_load_dwordx4 v0,s[36:39],s67 offen offset:1024 sc1 lds
buffer_load_dwordx4 v0,s[36:39],s67 offen offset:2048 sc1 lds
buffer_load_dwordx4 v0,s[36:39],s67 offen offset:3072 sc1 lds
s_mov_b32 s47,s48
s_mov_b32 s48,s59
s_lshr_b32 s70,s59,5
buffer_store_dword v5,v4,s[36:39],s70 offen offset:0 sc1
s_waitcnt lgkmcnt(3)
s_setpc_b64 s[92:93]
Lg1_done1_%=:
s_waitcnt vmcnt(0)
s_barrier
s_lshr_b32 s70,s47,5
buffer_store_dword v5,v4,s[36:39],s70 offen offset:0 sc1
s_lshr_b32 s70,s48,5
buffer_store_dword v5,v4,s[36:39],s70 offen offset:0 sc1
s_branch Lg1_done_%=
Lg1_done_%=:
s_waitcnt vmcnt(0) lgkmcnt(0)
s_barrier
s_branch Lg2_end_%=
Lg2_begin_%=:
Lcode_g2_b:
s_mov_b64 s[36:37],%1
s_mov_b32 s38,122727552
s_mov_b32 s39,0x00020000
s_mov_b32 s54,%2
s_mov_b32 s55,%3
s_mov_b32 s56,%4
s_mov_b32 s58,%5
s_mov_b32 s83,%6
s_mov_b32 s82,%7
s_mov_b32 s84,%8
v_mov_b32 v0,%9
v_mov_b32 v1,%10
v_mov_b32 v2,%11
v_mov_b32 v3,%12
v_mov_b32 v4,%13
v_mov_b32 v6,%14
v_mov_b32 v7,%15
v_mov_b32 v8,%16
v_mov_b32 v9,%17
v_mov_b32 v10,%18
v_mov_b32 v11,%19
v_mov_b32 v12,%20
v_mov_b32 v13,%21
v_mov_b32 v14,%22
v_mov_b32 v16,%23
v_mov_b32 v17,%24
v_mov_b32 v18,%25
v_mov_b32 v19,%26
v_mov_b32 v20,%27
s_mov_b64 s[40:41],%28
s_mov_b32 s42,2097152
s_mov_b32 s43,0x00020000
s_lshl_b32 s88,s54,11
s_add_u32 m0,s88,0
s_add_u32 s61,s82,0
s_and_b32 s61,s61,2097151
buffer_load_dwordx4 v16,s[40:43],s61 offen offset:0  lds)"
R"(
s_add_u32 m0,s88,1024
s_nop 0
buffer_load_dwordx4 v17,s[40:43],s61 offen offset:0  lds
s_add_u32 m0,s88,8064
s_nop 0
buffer_load_dwordx4 v16,s[40:43],s61 offen offset:128  lds
s_add_u32 m0,s88,9088
s_nop 0
buffer_load_dwordx4 v17,s[40:43],s61 offen offset:128  lds
s_add_u32 m0,s88,16384
s_add_u32 s61,s82,131072
s_and_b32 s61,s61,2097151
buffer_load_dwordx4 v16,s[40:43],s61 offen offset:0  lds
s_add_u32 m0,s88,17408
s_nop 0
buffer_load_dwordx4 v17,s[40:43],s61 offen offset:0  lds
s_add_u32 m0,s88,24448
s_nop 0
buffer_load_dwordx4 v16,s[40:43],s61 offen offset:128  lds
s_add_u32 m0,s88,25472
s_nop 0
buffer_load_dwordx4 v17,s[40:43],s61 offen offset:128  lds
s_add_u32 m0,s88,32768
s_add_u32 s61,s82,262144
s_and_b32 s61,s61,2097151
buffer_load_dwordx4 v16,s[40:43],s61 offen offset:0  lds
s_add_u32 m0,s88,33792
s_nop 0
buffer_load_dwordx4 v17,s[40:43],s61 offen offset:0  lds
s_add_u32 m0,s88,40832
s_nop 0
buffer_load_dwordx4 v16,s[40:43],s61 offen offset:128  lds
s_add_u32 m0,s88,41856
s_nop 0
buffer_load_dwordx4 v17,s[40:43],s61 offen offset:128  lds
s_add_u32 m0,s88,49152
s_add_u32 s61,s82,393216
s_and_b32 s61,s61,2097151
buffer_load_dwordx4 v16,s[40:43],s61 offen offset:0  lds
s_add_u32 m0,s88,50176
s_nop 0
buffer_load_dwordx4 v17,s[40:43],s61 offen offset:0  lds
s_add_u32 m0,s88,57216
s_nop 0
buffer_load_dwordx4 v16,s[40:43],s61 offen offset:128  lds
s_add_u32 m0,s88,58240
s_nop 0
buffer_load_dwordx4 v17,s[40:43],s61 offen offset:128  lds
s_add_u32 m0,s88,65536
s_add_u32 s61,s82,524288
s_and_b32 s61,s61,2097151
buffer_load_dwordx4 v16,s[40:43],s61 offen offset:0  lds
s_add_u32 m0,s88,66560
s_nop 0
buffer_load_dwordx4 v17,s[40:43],s61 offen offset:0  lds
s_add_u32 m0,s88,73600
s_nop 0
buffer_load_dwordx4 v16,s[40:43],s61 offen offset:128  lds
s_add_u32 m0,s88,74624
s_nop 0
buffer_load_dwordx4 v17,s[40:43],s61 offen offset:128  lds
s_add_u32 m0,s88,81920
s_add_u32 s61,s82,655360
s_and_b32 s61,s61,2097151
buffer_load_dwordx4 v16,s[40:43],s61 offen offset:0  lds
s_add_u32 m0,s88,82944
s_nop 0
buffer_load_dwordx4 v17,s[40:43],s61 offen offset:0  lds
s_add_u32 m0,s88,89984
s_nop 0
buffer_load_dwordx4 v16,s[40:43],s61 offen offset:128  lds
s_add_u32 m0,s88,91008
s_nop 0
buffer_load_dwordx4 v17,s[40:43],s61 offen offset:128  lds
s_add_u32 m0,s88,98304
s_add_u32 s61,s82,786432
s_and_b32 s61,s61,2097151
buffer_load_dwordx4 v16,s[40:43],s61 offen offset:0  lds
s_add_u32 m0,s88,99328
s_nop 0
buffer_load_dwordx4 v17,s[40:43],s61 offen offset:0  lds
s_add_u32 m0,s88,106368
s_nop 0
buffer_load_dwordx4 v16,s[40:43],s61 offen offset:128  lds
s_add_u32 m0,s88,107392
s_nop 0
buffer_load_dwordx4 v17,s[40:43],s61 offen offset:128  lds
v_add_u32 v24,49152,v18
v_add_u32 v25,49152,v19
v_add_u32 v26,98304,v18
v_add_u32 v27,98304,v19
s_waitcnt vmcnt(24)
s_barrier
s_add_u32 m0,s88,114688
s_add_u32 s61,s82,917504
s_and_b32 s61,s61,2097151
buffer_load_dwordx4 v16,s[40:43],s61 offen offset:0  lds
s_add_u32 m0,s88,115712
ds_read_b64_tr_b16 a[0:1],v18 offset:0
buffer_load_dwordx4 v17,s[40:43],s61 offen offset:0  lds
s_add_u32 m0,s88,122752
ds_read_b64_tr_b16 a[2:3],v18 offset:512
buffer_load_dwordx4 v16,s[40:43],s61 offen offset:128  lds
s_add_u32 m0,s88,123776
ds_read_b64_tr_b16 a[4:5],v19 offset:0
buffer_load_dwordx4 v17,s[40:43],s61 offen offset:128  lds
ds_read_b64_tr_b16 a[6:7],v19 offset:512
ds_read_b64_tr_b16 a[8:9],v18 offset:4096
ds_read_b64_tr_b16 a[10:11],v18 offset:4608
ds_read_b64_tr_b16 a[12:13],v19 offset:4096
ds_read_b64_tr_b16 a[14:15],v19 offset:4608
s_waitcnt vmcnt(24)
s_barrier
s_add_u32 m0,s88,131072
s_add_u32 s61,s82,1048576
s_and_b32 s61,s61,2097151
buffer_load_dwordx4 v16,s[40:43],s61 offen offset:0  lds
s_add_u32 m0,s88,132096
ds_read_b64_tr_b16 a[16:17],v18 offset:16384
buffer_load_dwordx4 v17,s[40:43],s61 offen offset:0  lds
s_add_u32 m0,s88,139136
ds_read_b64_tr_b16 a[18:19],v18 offset:16896
buffer_load_dwordx4 v16,s[40:43],s61 offen offset:128  lds
s_add_u32 m0,s88,140160
ds_read_b64_tr_b16 a[20:21],v19 offset:16384
buffer_load_dwordx4 v17,s[40:43],s61 offen offset:128  lds
ds_read_b64_tr_b16 a[22:23],v19 offset:16896
ds_read_b64_tr_b16 a[24:25],v18 offset:20480
ds_read_b64_tr_b16 a[26:27],v18 offset:20992
ds_read_b64_tr_b16 a[28:29],v19 offset:20480
ds_read_b64_tr_b16 a[30:31],v19 offset:20992
s_waitcnt lgkmcnt(8)
s_waitcnt vmcnt(24)
s_barrier
s_add_u32 m0,s88,0
s_add_u32 s61,s82,1179648
s_and_b32 s61,s61,2097151
buffer_load_dwordx4 v16,s[40:43],s61 offen offset:0  lds
s_add_u32 m0,s88,1024
ds_read_b64_tr_b16 a[32:33],v18 offset:32768
buffer_load_dwordx4 v17,s[40:43],s61 offen offset:0  lds
s_add_u32 m0,s88,8064
ds_read_b64_tr_b16 a[34:35],v18 offset:33280
buffer_load_dwordx4 v16,s[40:43],s61 offen offset:128  lds
s_add_u32 m0,s88,9088
ds_read_b64_tr_b16 a[36:37],v19 offset:32768
buffer_load_dwordx4 v17,s[40:43],s61 offen offset:128  lds
ds_read_b64_tr_b16 a[38:39],v19 offset:33280
ds_read_b64_tr_b16 a[40:41],v18 offset:36864
ds_read_b64_tr_b16 a[42:43],v18 offset:37376
ds_read_b64_tr_b16 a[44:45],v19 offset:36864
ds_read_b64_tr_b16 a[46:47],v19 offset:37376
s_waitcnt lgkmcnt(8)
s_waitcnt vmcnt(24)
s_barrier
s_add_u32 m0,s88,16384
s_add_u32 s61,s82,1310720
s_and_b32 s61,s61,2097151
buffer_load_dwordx4 v16,s[40:43],s61 offen offset:0  lds
s_add_u32 m0,s88,17408
ds_read_b64_tr_b16 a[48:49],v24 offset:0
buffer_load_dwordx4 v17,s[40:43],s61 offen offset:0  lds
s_add_u32 m0,s88,24448
ds_read_b64_tr_b16 a[50:51],v24 offset:512
buffer_load_dwordx4 v16,s[40:43],s61 offen offset:128  lds
s_add_u32 m0,s88,25472
ds_read_b64_tr_b16 a[52:53],v25 offset:0
buffer_load_dwordx4 v17,s[40:43],s61 offen offset:128  lds
ds_read_b64_tr_b16 a[54:55],v25 offset:512
ds_read_b64_tr_b16 a[56:57],v24 offset:4096
ds_read_b64_tr_b16 a[58:59],v24 offset:4608
ds_read_b64_tr_b16 a[60:61],v25 offset:4096
ds_read_b64_tr_b16 a[62:63],v25 offset:4608
s_waitcnt lgkmcnt(8)
s_waitcnt vmcnt(24)
s_barrier
s_add_u32 m0,s88,32768
s_add_u32 s61,s82,1441792
s_and_b32 s61,s61,2097151
buffer_load_dwordx4 v16,s[40:43],s61 offen offset:0  lds
s_add_u32 m0,s88,33792
ds_read_b64_tr_b16 a[64:65],v24 offset:16384
buffer_load_dwordx4 v17,s[40:43],s61 offen offset:0  lds
s_add_u32 m0,s88,40832
ds_read_b64_tr_b16 a[66:67],v24 offset:16896
buffer_load_dwordx4 v16,s[40:43],s61 offen offset:128  lds
s_add_u32 m0,s88,41856
ds_read_b64_tr_b16 a[68:69],v25 offset:16384
buffer_load_dwordx4 v17,s[40:43],s61 offen offset:128  lds
ds_read_b64_tr_b16 a[70:71],v25 offset:16896
ds_read_b64_tr_b16 a[72:73],v24 offset:20480
ds_read_b64_tr_b16 a[74:75],v24 offset:20992
ds_read_b64_tr_b16 a[76:77],v25 offset:20480
ds_read_b64_tr_b16 a[78:79],v25 offset:20992
s_waitcnt lgkmcnt(8)
s_waitcnt vmcnt(24)
s_barrier
s_add_u32 m0,s88,49152
s_add_u32 s61,s82,1572864
s_and_b32 s61,s61,2097151)"
R"(
buffer_load_dwordx4 v16,s[40:43],s61 offen offset:0  lds
s_add_u32 m0,s88,50176
ds_read_b64_tr_b16 a[80:81],v24 offset:32768
buffer_load_dwordx4 v17,s[40:43],s61 offen offset:0  lds
s_add_u32 m0,s88,57216
ds_read_b64_tr_b16 a[82:83],v24 offset:33280
buffer_load_dwordx4 v16,s[40:43],s61 offen offset:128  lds
s_add_u32 m0,s88,58240
ds_read_b64_tr_b16 a[84:85],v25 offset:32768
buffer_load_dwordx4 v17,s[40:43],s61 offen offset:128  lds
ds_read_b64_tr_b16 a[86:87],v25 offset:33280
ds_read_b64_tr_b16 a[88:89],v24 offset:36864
ds_read_b64_tr_b16 a[90:91],v24 offset:37376
ds_read_b64_tr_b16 a[92:93],v25 offset:36864
ds_read_b64_tr_b16 a[94:95],v25 offset:37376
s_waitcnt lgkmcnt(8)
s_waitcnt vmcnt(24)
s_barrier
s_add_u32 m0,s88,65536
s_add_u32 s61,s82,1703936
s_and_b32 s61,s61,2097151
buffer_load_dwordx4 v16,s[40:43],s61 offen offset:0  lds
s_add_u32 m0,s88,66560
ds_read_b64_tr_b16 a[96:97],v26 offset:0
buffer_load_dwordx4 v17,s[40:43],s61 offen offset:0  lds
s_add_u32 m0,s88,73600
ds_read_b64_tr_b16 a[98:99],v26 offset:512
buffer_load_dwordx4 v16,s[40:43],s61 offen offset:128  lds
s_add_u32 m0,s88,74624
ds_read_b64_tr_b16 a[100:101],v27 offset:0
buffer_load_dwordx4 v17,s[40:43],s61 offen offset:128  lds
ds_read_b64_tr_b16 a[102:103],v27 offset:512
ds_read_b64_tr_b16 a[104:105],v26 offset:4096
ds_read_b64_tr_b16 a[106:107],v26 offset:4608
ds_read_b64_tr_b16 a[108:109],v27 offset:4096
ds_read_b64_tr_b16 a[110:111],v27 offset:4608
s_waitcnt lgkmcnt(8)
s_waitcnt vmcnt(24)
s_barrier
s_add_u32 m0,s88,81920
s_add_u32 s61,s82,1835008
s_and_b32 s61,s61,2097151
buffer_load_dwordx4 v16,s[40:43],s61 offen offset:0  lds
s_add_u32 m0,s88,82944
ds_read_b64_tr_b16 a[112:113],v26 offset:16384
buffer_load_dwordx4 v17,s[40:43],s61 offen offset:0  lds
s_add_u32 m0,s88,89984
ds_read_b64_tr_b16 a[114:115],v26 offset:16896
buffer_load_dwordx4 v16,s[40:43],s61 offen offset:128  lds
s_add_u32 m0,s88,91008
ds_read_b64_tr_b16 a[116:117],v27 offset:16384
buffer_load_dwordx4 v17,s[40:43],s61 offen offset:128  lds
ds_read_b64_tr_b16 a[118:119],v27 offset:16896
ds_read_b64_tr_b16 a[120:121],v26 offset:20480
ds_read_b64_tr_b16 a[122:123],v26 offset:20992
ds_read_b64_tr_b16 a[124:125],v27 offset:20480
ds_read_b64_tr_b16 a[126:127],v27 offset:20992
s_waitcnt lgkmcnt(8)
s_waitcnt vmcnt(24)
s_barrier
s_add_u32 m0,s88,98304
s_add_u32 s61,s82,1966080
s_and_b32 s61,s61,2097151
buffer_load_dwordx4 v16,s[40:43],s61 offen offset:0  lds
s_add_u32 m0,s88,99328
ds_read_b64_tr_b16 a[128:129],v26 offset:32768
buffer_load_dwordx4 v17,s[40:43],s61 offen offset:0  lds
s_add_u32 m0,s88,106368
ds_read_b64_tr_b16 a[130:131],v26 offset:33280
buffer_load_dwordx4 v16,s[40:43],s61 offen offset:128  lds
s_add_u32 m0,s88,107392
ds_read_b64_tr_b16 a[132:133],v27 offset:32768
buffer_load_dwordx4 v17,s[40:43],s61 offen offset:128  lds
ds_read_b64_tr_b16 a[134:135],v27 offset:33280
ds_read_b64_tr_b16 a[136:137],v26 offset:36864
ds_read_b64_tr_b16 a[138:139],v26 offset:37376
ds_read_b64_tr_b16 a[140:141],v27 offset:36864
ds_read_b64_tr_b16 a[142:143],v27 offset:37376
s_waitcnt lgkmcnt(8)
s_waitcnt vmcnt(24)
s_barrier
ds_read_b64_tr_b16 a[144:145],v18 offset:0
ds_read_b64_tr_b16 a[146:147],v18 offset:512
ds_read_b64_tr_b16 a[148:149],v19 offset:0
ds_read_b64_tr_b16 a[150:151],v19 offset:512
ds_read_b64_tr_b16 a[152:153],v18 offset:4096
ds_read_b64_tr_b16 a[154:155],v18 offset:4608
ds_read_b64_tr_b16 a[156:157],v19 offset:4096
ds_read_b64_tr_b16 a[158:159],v19 offset:4608
s_waitcnt lgkmcnt(8)
s_waitcnt vmcnt(20)
s_barrier
ds_read_b64_tr_b16 a[160:161],v18 offset:16384
ds_read_b64_tr_b16 a[162:163],v18 offset:16896
ds_read_b64_tr_b16 a[164:165],v19 offset:16384
ds_read_b64_tr_b16 a[166:167],v19 offset:16896
ds_read_b64_tr_b16 a[168:169],v18 offset:20480
ds_read_b64_tr_b16 a[170:171],v18 offset:20992
ds_read_b64_tr_b16 a[172:173],v19 offset:20480
ds_read_b64_tr_b16 a[174:175],v19 offset:20992
s_waitcnt lgkmcnt(8)
s_waitcnt vmcnt(16)
s_barrier
ds_read_b64_tr_b16 a[176:177],v18 offset:32768
ds_read_b64_tr_b16 a[178:179],v18 offset:33280
ds_read_b64_tr_b16 a[180:181],v19 offset:32768
ds_read_b64_tr_b16 a[182:183],v19 offset:33280
ds_read_b64_tr_b16 a[184:185],v18 offset:36864
ds_read_b64_tr_b16 a[186:187],v18 offset:37376
ds_read_b64_tr_b16 a[188:189],v19 offset:36864
ds_read_b64_tr_b16 a[190:191],v19 offset:37376
s_getpc_b64 s[46:47]
Lg2_cp_%=:
s_add_u32 s46,s46,Lg2_init_%=-Lg2_cp_%=
s_addc_u32 s47,s47,0
s_lshl_b32 s68,s54,13
v_lshl_add_u32 v48,v2,3,s68
v_min_u32 v49,Lg2_done_%=-Lg2_init_%=,v48
global_load_dword v52,v49,s[46:47]
s_waitcnt lgkmcnt(8)
s_waitcnt vmcnt(13)
s_barrier
ds_read_b64_tr_b16 a[192:193],v24 offset:0
ds_read_b64_tr_b16 a[194:195],v24 offset:512
ds_read_b64_tr_b16 a[196:197],v25 offset:0
ds_read_b64_tr_b16 a[198:199],v25 offset:512
ds_read_b64_tr_b16 a[200:201],v24 offset:4096
ds_read_b64_tr_b16 a[202:203],v24 offset:4608
ds_read_b64_tr_b16 a[204:205],v25 offset:4096
ds_read_b64_tr_b16 a[206:207],v25 offset:4608
s_waitcnt lgkmcnt(8)
s_waitcnt vmcnt(9)
s_barrier
ds_read_b64_tr_b16 a[208:209],v24 offset:16384
ds_read_b64_tr_b16 a[210:211],v24 offset:16896
ds_read_b64_tr_b16 a[212:213],v25 offset:16384
ds_read_b64_tr_b16 a[214:215],v25 offset:16896
ds_read_b64_tr_b16 a[216:217],v24 offset:20480
ds_read_b64_tr_b16 a[218:219],v24 offset:20992
ds_read_b64_tr_b16 a[220:221],v25 offset:20480
ds_read_b64_tr_b16 a[222:223],v25 offset:20992
s_waitcnt lgkmcnt(8)
s_waitcnt vmcnt(5)
s_barrier
ds_read_b64_tr_b16 a[224:225],v24 offset:32768
ds_read_b64_tr_b16 a[226:227],v24 offset:33280
ds_read_b64_tr_b16 a[228:229],v25 offset:32768
ds_read_b64_tr_b16 a[230:231],v25 offset:33280
ds_read_b64_tr_b16 a[232:233],v24 offset:36864
ds_read_b64_tr_b16 a[234:235],v24 offset:37376
ds_read_b64_tr_b16 a[236:237],v25 offset:36864
ds_read_b64_tr_b16 a[238:239],v25 offset:37376
s_waitcnt lgkmcnt(8)
s_waitcnt vmcnt(1)
s_barrier
ds_read_b64_tr_b16 a[240:241],v26 offset:0
ds_read_b64_tr_b16 a[242:243],v26 offset:512
ds_read_b64_tr_b16 a[244:245],v27 offset:0
ds_read_b64_tr_b16 a[246:247],v27 offset:512
ds_read_b64_tr_b16 a[248:249],v26 offset:4096
ds_read_b64_tr_b16 a[250:251],v26 offset:4608
ds_read_b64_tr_b16 a[252:253],v27 offset:4096
ds_read_b64_tr_b16 a[254:255],v27 offset:4608
s_waitcnt lgkmcnt(8)
s_waitcnt lgkmcnt(0)
s_waitcnt lgkmcnt(0)
s_barrier
s_mov_b32 s60,s84
Lg2_init_%=:
v_mov_b32 v5,s60
s_mov_b32 s59,536870912
s_add_u32 s57,s56,131072
s_mov_b32 s79,0
s_mov_b32 s75,0x3a800000
s_mov_b32 s76,0xc0135761
s_mov_b32 s77,0x3d372713
s_mov_b32 s44,s59
s_mov_b32 s45,s59
s_mov_b32 s46,s59
s_mov_b32 s47,s59
s_mov_b32 s48,s83
s_add_u32 s49,s48,s55
s_cmp_eq_u32 s49,s56
s_cselect_b32 s49,s57,s49
s_cmp_eq_u32 s49,s58
s_cselect_b32 s49,s59,s49
s_add_u32 s50,s49,s55
s_cmp_eq_u32 s50,s56
s_cselect_b32 s50,s57,s50
s_cmp_eq_u32 s50,s58
s_cselect_b32 s50,s59,s50
s_add_u32 s51,s50,s55
s_cmp_eq_u32 s51,s56
s_cselect_b32 s51,s57,s51
s_cmp_eq_u32 s51,s58
s_cselect_b32 s51,s59,s51
s_add_u32 s52,s51,s55
s_cmp_eq_u32 s52,s56
s_cselect_b32 s52,s57,s52
s_cmp_eq_u32 s52,s58)"
R"(
s_cselect_b32 s52,s59,s52
s_lshl_b32 s63,s54,12
s_add_u32 s63,s63,65536
s_add_u32 s64,s63,32768
s_lshl_b32 s88,s54,10
s_add_u32 s88,s88,131072
s_lshl_b32 s78,s54,11
s_add_u32 s78,s78,132608
s_getpc_b64 s[90:91]
Lg2_pc_%=:
s_mov_b32 s68,Lg2_d1w0_%=-Lg2_pc_%=
s_cmp_eq_u32 s54,1
s_cselect_b32 s68,Lg2_d1w1_%=-Lg2_pc_%=,s68
s_cmp_eq_u32 s54,2
s_cselect_b32 s68,Lg2_d1w2_%=-Lg2_pc_%=,s68
s_cmp_eq_u32 s54,3
s_cselect_b32 s68,Lg2_d1w3_%=-Lg2_pc_%=,s68
s_add_u32 s92,s90,s68
s_addc_u32 s93,s91,0
s_mov_b32 s68,Lg2_d0w0_%=-Lg2_pc_%=
s_cmp_eq_u32 s54,1
s_cselect_b32 s68,Lg2_d0w1_%=-Lg2_pc_%=,s68
s_cmp_eq_u32 s54,2
s_cselect_b32 s68,Lg2_d0w2_%=-Lg2_pc_%=,s68
s_cmp_eq_u32 s54,3
s_cselect_b32 s68,Lg2_d0w3_%=-Lg2_pc_%=,s68
s_add_u32 s90,s90,s68
s_addc_u32 s91,s91,0
s_mov_b32 s66,s59
s_mov_b32 s65,0
s_mov_b32 s62,s63
s_branch Lg2_slow0_%=
Lg2_d0w0_%=:
v_mfma_f32_16x16x32_bf16 v[16:19],a[0:3],v[32:35],0
s_add_u32 m0,s62,16384
ds_read_b128 v[60:63],v2 offset:7168
v_lshlrev_b32 v64,16,v84
v_mfma_f32_16x16x32_bf16 v[20:23],a[4:7],v[32:35],0
buffer_load_dwordx4 v1,s[36:39],s66 offen offset:0 sc1 lds
v_mfma_f32_16x16x32_bf16 v[16:19],a[8:11],v[36:39],v[16:19]
s_lshr_b32 s73,s47,0
ds_read_b128 v[32:35],v2 offset:8192
v_and_b32 v65,0xffff0000,v84
v_mfma_f32_16x16x32_bf16 v[20:23],a[12:15],v[36:39],v[20:23]
s_xor_b32 s87,s64,65536
v_lshlrev_b32 v66,16,v85
v_and_b32 v67,0xffff0000,v85
v_mfma_f32_16x16x32_bf16 v[16:19],a[16:19],v[40:43],v[16:19]
s_cmp_ge_u32 s66,s59
ds_read_b128 v[36:39],v2 offset:9216
v_lshlrev_b32 v68,16,v86
v_mfma_f32_16x16x32_bf16 v[20:23],a[20:23],v[40:43],v[20:23]
buffer_load_dwordx4 v1,s[36:39],s66 offen offset:1024 sc1 lds
v_mfma_f32_16x16x32_bf16 v[16:19],a[24:27],v[44:47],v[16:19]
s_cselect_b32 s86,s49,s50
ds_read_b128 v[40:43],v2 offset:10240
v_and_b32 v69,0xffff0000,v86
v_mfma_f32_16x16x32_bf16 v[20:23],a[28:31],v[44:47],v[20:23]
s_waitcnt lgkmcnt(3)
v_lshlrev_b32 v70,16,v87
v_and_b32 v71,0xffff0000,v87
v_mfma_f32_16x16x32_bf16 v[16:19],a[32:35],v[48:51],v[16:19]
s_cselect_b32 s87,s87,s63
ds_read_b128 v[44:47],v2 offset:11264
v_fma_f32 v64,v64,v91,v90
v_mfma_f32_16x16x32_bf16 v[20:23],a[36:39],v[48:51],v[20:23]
buffer_load_dwordx4 v1,s[36:39],s66 offen offset:2048 sc1 lds
v_mfma_f32_16x16x32_bf16 v[16:19],a[40:43],v[52:55],v[16:19]
s_lshr_b32 s72,s66,6
ds_read_b128 v[48:51],v2 offset:12288
v_fma_f32 v65,v65,v91,v90
v_mfma_f32_16x16x32_bf16 v[20:23],a[44:47],v[52:55],v[20:23]
s_lshr_b32 s71,s62,2
v_fma_f32 v66,v66,v91,v90
v_fma_f32 v67,v67,v91,v90
v_mfma_f32_16x16x32_bf16 v[16:19],a[48:51],v[56:59],v[16:19]
s_mov_b32 s62,s87
ds_read_b128 v[52:55],v2 offset:13312
v_fma_f32 v68,v68,v91,v90
v_mfma_f32_16x16x32_bf16 v[20:23],a[52:55],v[56:59],v[20:23]
buffer_load_dwordx4 v1,s[36:39],s66 offen offset:3072 sc1 lds
v_mfma_f32_16x16x32_bf16 v[16:19],a[56:59],v[60:63],v[16:19]
s_add_u32 m0,s71,s88
ds_read_b128 v[56:59],v2 offset:14336
v_fma_f32 v69,v69,v91,v90
v_mfma_f32_16x16x32_bf16 v[20:23],a[60:63],v[60:63],v[20:23]
s_waitcnt lgkmcnt(3)
v_fma_f32 v70,v70,v91,v90
v_fma_f32 v71,v71,v91,v90
v_mfma_f32_16x16x32_bf16 v[16:19],a[64:67],v[32:35],v[16:19]
s_waitcnt vmcnt(10)
ds_read_b128 v[60:63],v2 offset:15360
v_add_f32 v24,v24,v64
v_mfma_f32_16x16x32_bf16 v[20:23],a[68:71],v[32:35],v[20:23]
s_barrier
v_mfma_f32_16x16x32_bf16 v[16:19],a[72:75],v[36:39],v[16:19]
s_lshr_b32 s68,s50,5
ds_read_b128 v[32:35],v2 offset:16384
v_add_f32 v25,v25,v65
v_mfma_f32_16x16x32_bf16 v[20:23],a[76:79],v[36:39],v[20:23]
buffer_load_dwordx4 v10,s[36:39],s66 offen offset:0 sc1 lds
v_mfma_f32_16x16x32_bf16 v[16:19],a[80:83],v[40:43],v[16:19]
buffer_load_dword v11,s[36:39],s72 offen offset:1024 sc1 lds
v_mfma_f32_16x16x32_bf16 v[20:23],a[84:87],v[40:43],v[20:23]
s_lshr_b32 s70,s51,5
ds_read_b128 v[36:39],v2 offset:17408
v_add_f32 v26,v26,v66
v_mfma_f32_16x16x32_bf16 v[16:19],a[88:91],v[44:47],v[16:19]
ds_read_b128 v[40:43],v2 offset:18432
v_add_f32 v27,v27,v67
v_mfma_f32_16x16x32_bf16 v[20:23],a[92:95],v[44:47],v[20:23]
s_waitcnt lgkmcnt(3)
v_add_f32 v28,v28,v68
v_add_f32 v29,v29,v69
v_add_f32 v30,v30,v70
v_add_f32 v31,v31,v71
v_cvt_pk_bf16_f32 v72,v24,v25
v_cvt_pk_bf16_f32 v73,v26,v27
v_cvt_pk_bf16_f32 v74,v28,v29
v_cvt_pk_bf16_f32 v75,v30,v31
v_mfma_f32_16x16x32_bf16 v[16:19],a[96:99],v[48:51],v[16:19]
buffer_store_dwordx4 v[72:75],v3,s[36:39],s73 offen offset:0 sc1
ds_read_b128 v[44:47],v2 offset:19456
v_add_f32 v64,v24,v25
v_mul_f32 v65,v24,v24
v_mfma_f32_16x16x32_bf16 v[20:23],a[100:103],v[48:51],v[20:23]
v_fma_f32 v65,v25,v25,v65
v_add_f32 v64,v64,v26
v_mfma_f32_16x16x32_bf16 v[16:19],a[104:107],v[52:55],v[16:19]
ds_read_b128 v[48:51],v2 offset:20480
v_fma_f32 v65,v26,v26,v65
v_mfma_f32_16x16x32_bf16 v[20:23],a[108:111],v[52:55],v[20:23]
v_add_f32 v64,v64,v27
v_fma_f32 v65,v27,v27,v65
v_mfma_f32_16x16x32_bf16 v[16:19],a[112:115],v[56:59],v[16:19]
ds_read_b128 v[52:55],v2 offset:21504
v_add_f32 v64,v64,v28
v_mfma_f32_16x16x32_bf16 v[20:23],a[116:119],v[56:59],v[20:23]
v_fma_f32 v65,v28,v28,v65
v_add_f32 v64,v64,v29
v_fma_f32 v65,v29,v29,v65
v_add_f32 v64,v64,v30
v_fma_f32 v65,v30,v30,v65
v_add_f32 v64,v64,v31
v_fma_f32 v65,v31,v31,v65
v_mfma_f32_16x16x32_bf16 v[16:19],a[120:123],v[60:63],v[16:19]
s_nop 0
v_permlane16_swap_b32 v64,v65
v_add_f32 v64,v64,v65
v_mov_b32 v65,v64
v_mfma_f32_16x16x32_bf16 v[20:23],a[124:127],v[60:63],v[20:23]
s_waitcnt lgkmcnt(2)
ds_read_b128 v[56:59],v2 offset:22528
v_permlane32_swap_b32 v64,v65
v_add_f32 v64,v64,v65
v_mfma_f32_16x16x32_bf16 v[16:19],a[128:131],v[32:35],v[16:19]
ds_write_b32 v13,v64 offset:0
v_mfma_f32_16x16x32_bf16 v[20:23],a[132:135],v[32:35],v[20:23]
ds_read_b128 v[60:63],v2 offset:23552
v_mfma_f32_16x16x32_bf16 v[16:19],a[136:139],v[36:39],v[16:19]
ds_read_b128 v[32:35],v2 offset:24576
v_mfma_f32_16x16x32_bf16 v[20:23],a[140:143],v[36:39],v[20:23]
v_mfma_f32_16x16x32_bf16 v[16:19],a[144:147],v[40:43],v[16:19]
s_waitcnt vmcnt(11) lgkmcnt(2)
ds_read_b128 v[36:39],v2 offset:25600
v_mfma_f32_16x16x32_bf16 v[20:23],a[148:151],v[40:43],v[20:23]
s_barrier
v_mfma_f32_16x16x32_bf16 v[16:19],a[152:155],v[44:47],v[16:19]
ds_read_b128 v[40:43],v2 offset:26624
ds_read_b32 v15,v7 offset:0
v_mfma_f32_16x16x32_bf16 v[20:23],a[156:159],v[44:47],v[20:23]
s_waitcnt lgkmcnt(4)
v_mfma_f32_16x16x32_bf16 v[16:19],a[160:163],v[48:51],v[16:19]
ds_read_b128 v[44:47],v2 offset:27648
v_mfma_f32_16x16x32_bf16 v[20:23],a[164:167],v[48:51],v[20:23]
v_mfma_f32_16x16x32_bf16 v[16:19],a[168:171],v[52:55],v[16:19]
s_waitcnt lgkmcnt(1)
ds_read_b128 v[48:51],v2 offset:28672
v_mfma_f32_16x16x32_bf16 v[20:23],a[172:175],v[52:55],v[20:23]
v_cmp_eq_u32 vcc,s60,v15
v_mfma_f32_16x16x32_bf16 v[16:19],a[176:179],v[56:59],v[16:19]
s_cmp_eq_u32 vcc_lo,-1
ds_read_b128 v[52:55],v2 offset:29696
s_cselect_b32 s67,s86,s59
v_mfma_f32_16x16x32_bf16 v[20:23],a[180:183],v[56:59],v[20:23]
s_cmp_ge_u32 s67,s59
s_cselect_b32 s68,s68,s70
s_add_u32 m0,s78,256
v_mfma_f32_16x16x32_bf16 v[16:19],a[184:187],v[60:63],v[16:19]
buffer_load_dword v6,s[36:39],s68 offen offset:0 sc1 lds
v_mfma_f32_16x16x32_bf16 v[20:23],a[188:191],v[60:63],v[20:23]
s_waitcnt lgkmcnt(2)
ds_read_b128 v[56:59],v2 offset:30720
s_xor_b32 s63,s63,65536
v_mfma_f32_16x16x32_bf16 v[16:19],a[192:195],v[32:35],v[16:19]
s_waitcnt vmcnt(8)
ds_read_b128 v[60:63],v2 offset:31744
s_mov_b32 m0,s62
v_mfma_f32_16x16x32_bf16 v[20:23],a[196:199],v[32:35],v[20:23]
s_barrier)"
R"(
v_mfma_f32_16x16x32_bf16 v[16:19],a[200:203],v[36:39],v[16:19]
buffer_load_dwordx4 v0,s[36:39],s67 offen offset:0 sc1 lds
v_mfma_f32_16x16x32_bf16 v[20:23],a[204:207],v[36:39],v[20:23]
s_add_u32 s53,s52,s55
ds_read_b128 v[32:35],v2 offset:32768
ds_read_b128 v[80:83],v8 offset:0
v_mfma_f32_16x16x32_bf16 v[16:19],a[208:211],v[40:43],v[16:19]
s_cmp_eq_u32 s53,s56
ds_read_b128 v[36:39],v2 offset:33792
ds_read_b64 v[88:89],v9 offset:1024
v_mfma_f32_16x16x32_bf16 v[20:23],a[212:215],v[40:43],v[20:23]
s_cselect_b32 s53,s57,s53
s_cmp_eq_u32 s53,s58
s_cselect_b32 s53,s59,s53
v_mfma_f32_16x16x32_bf16 v[16:19],a[216:219],v[44:47],v[16:19]
buffer_load_dwordx4 v0,s[36:39],s67 offen offset:1024 sc1 lds
v_mfma_f32_16x16x32_bf16 v[20:23],a[220:223],v[44:47],v[20:23]
s_waitcnt lgkmcnt(4)
ds_read_b128 v[40:43],v2 offset:34816
s_cmp_ge_u32 s66,s59
v_mfma_f32_16x16x32_bf16 v[16:19],a[224:227],v[48:51],v[16:19]
ds_read_b128 v[44:47],v2 offset:35840
v_mfma_f32_16x16x32_bf16 v[20:23],a[228:231],v[48:51],v[20:23]
v_mfma_f32_16x16x32_bf16 v[16:19],a[232:235],v[52:55],v[16:19]
s_waitcnt lgkmcnt(2)
buffer_load_dwordx4 v0,s[36:39],s67 offen offset:2048 sc1 lds
v_mfma_f32_16x16x32_bf16 v[20:23],a[236:239],v[52:55],v[20:23]
ds_read_b128 v[48:51],v2 offset:36864
v_mfma_f32_16x16x32_bf16 v[16:19],a[240:243],v[56:59],v[16:19]
ds_read_b128 v[52:55],v2 offset:37888
v_mfma_f32_16x16x32_bf16 v[20:23],a[244:247],v[56:59],v[20:23]
v_mfma_f32_16x16x32_bf16 v[16:19],a[248:251],v[60:63],v[16:19]
s_waitcnt lgkmcnt(2)
ds_read_b128 v[56:59],v2 offset:38912
buffer_load_dwordx4 v0,s[36:39],s67 offen offset:3072 sc1 lds
v_mfma_f32_16x16x32_bf16 v[20:23],a[252:255],v[60:63],v[20:23]
s_cbranch_scc1 Lg2_slow1_%=
Lg2_d1w0_%=:
v_mfma_f32_16x16x32_bf16 v[24:27],a[0:3],v[32:35],0
s_add_u32 m0,s62,16384
ds_read_b128 v[60:63],v2 offset:39936
v_lshlrev_b32 v64,16,v80
v_mfma_f32_16x16x32_bf16 v[28:31],a[4:7],v[32:35],0
buffer_load_dwordx4 v1,s[36:39],s67 offen offset:0 sc1 lds
v_mfma_f32_16x16x32_bf16 v[24:27],a[8:11],v[36:39],v[24:27]
s_lshr_b32 s73,s48,0
ds_read_b128 v[32:35],v2 offset:40960
v_and_b32 v65,0xffff0000,v80
v_mfma_f32_16x16x32_bf16 v[28:31],a[12:15],v[36:39],v[28:31]
s_xor_b32 s87,s63,65536
v_lshlrev_b32 v66,16,v81
v_and_b32 v67,0xffff0000,v81
v_mfma_f32_16x16x32_bf16 v[24:27],a[16:19],v[40:43],v[24:27]
s_cmp_ge_u32 s67,s59
ds_read_b128 v[36:39],v2 offset:41984
v_lshlrev_b32 v68,16,v82
v_mfma_f32_16x16x32_bf16 v[28:31],a[20:23],v[40:43],v[28:31]
buffer_load_dwordx4 v1,s[36:39],s67 offen offset:1024 sc1 lds
v_mfma_f32_16x16x32_bf16 v[24:27],a[24:27],v[44:47],v[24:27]
s_cselect_b32 s86,s50,s51
ds_read_b128 v[40:43],v2 offset:43008
v_and_b32 v69,0xffff0000,v82
v_mfma_f32_16x16x32_bf16 v[28:31],a[28:31],v[44:47],v[28:31]
s_waitcnt lgkmcnt(3)
v_lshlrev_b32 v70,16,v83
v_and_b32 v71,0xffff0000,v83
v_mfma_f32_16x16x32_bf16 v[24:27],a[32:35],v[48:51],v[24:27]
s_cselect_b32 s87,s87,s64
ds_read_b128 v[44:47],v2 offset:44032
v_fma_f32 v64,v64,v89,v88
v_mfma_f32_16x16x32_bf16 v[28:31],a[36:39],v[48:51],v[28:31]
buffer_load_dwordx4 v1,s[36:39],s67 offen offset:2048 sc1 lds
v_mfma_f32_16x16x32_bf16 v[24:27],a[40:43],v[52:55],v[24:27]
s_lshr_b32 s72,s67,6
ds_read_b128 v[48:51],v2 offset:45056
v_fma_f32 v65,v65,v89,v88
v_mfma_f32_16x16x32_bf16 v[28:31],a[44:47],v[52:55],v[28:31]
s_lshr_b32 s71,s62,2
v_fma_f32 v66,v66,v89,v88
v_fma_f32 v67,v67,v89,v88
v_mfma_f32_16x16x32_bf16 v[24:27],a[48:51],v[56:59],v[24:27]
s_mov_b32 s62,s87
ds_read_b128 v[52:55],v2 offset:46080
v_fma_f32 v68,v68,v89,v88
v_mfma_f32_16x16x32_bf16 v[28:31],a[52:55],v[56:59],v[28:31]
buffer_load_dwordx4 v1,s[36:39],s67 offen offset:3072 sc1 lds
v_mfma_f32_16x16x32_bf16 v[24:27],a[56:59],v[60:63],v[24:27]
s_add_u32 m0,s71,s88
ds_read_b128 v[56:59],v2 offset:47104
v_fma_f32 v69,v69,v89,v88
v_mfma_f32_16x16x32_bf16 v[28:31],a[60:63],v[60:63],v[28:31]
s_waitcnt lgkmcnt(3)
v_fma_f32 v70,v70,v89,v88
v_fma_f32 v71,v71,v89,v88
v_mfma_f32_16x16x32_bf16 v[24:27],a[64:67],v[32:35],v[24:27]
s_waitcnt vmcnt(10)
ds_read_b128 v[60:63],v2 offset:48128
v_add_f32 v16,v16,v64
v_mfma_f32_16x16x32_bf16 v[28:31],a[68:71],v[32:35],v[28:31]
s_barrier
v_mfma_f32_16x16x32_bf16 v[24:27],a[72:75],v[36:39],v[24:27]
s_lshr_b32 s68,s51,5
ds_read_b128 v[32:35],v2 offset:49152
v_add_f32 v17,v17,v65
v_mfma_f32_16x16x32_bf16 v[28:31],a[76:79],v[36:39],v[28:31]
buffer_load_dwordx4 v10,s[36:39],s67 offen offset:0 sc1 lds
v_mfma_f32_16x16x32_bf16 v[24:27],a[80:83],v[40:43],v[24:27]
buffer_load_dword v11,s[36:39],s72 offen offset:1024 sc1 lds
v_mfma_f32_16x16x32_bf16 v[28:31],a[84:87],v[40:43],v[28:31]
s_lshr_b32 s70,s52,5
ds_read_b128 v[36:39],v2 offset:50176
v_add_f32 v18,v18,v66
v_mfma_f32_16x16x32_bf16 v[24:27],a[88:91],v[44:47],v[24:27]
s_mov_b64 s[44:45],s[46:47]
ds_read_b128 v[40:43],v2 offset:51200
v_add_f32 v19,v19,v67
v_mfma_f32_16x16x32_bf16 v[28:31],a[92:95],v[44:47],v[28:31]
s_waitcnt lgkmcnt(3)
v_add_f32 v20,v20,v68
v_add_f32 v21,v21,v69
v_add_f32 v22,v22,v70
v_add_f32 v23,v23,v71
v_cvt_pk_bf16_f32 v72,v16,v17
v_cvt_pk_bf16_f32 v73,v18,v19
v_cvt_pk_bf16_f32 v74,v20,v21
v_cvt_pk_bf16_f32 v75,v22,v23
v_mfma_f32_16x16x32_bf16 v[24:27],a[96:99],v[48:51],v[24:27]
buffer_store_dwordx4 v[72:75],v3,s[36:39],s73 offen offset:0 sc1
ds_read_b128 v[44:47],v2 offset:52224
v_add_f32 v64,v16,v17
v_mul_f32 v65,v16,v16
v_mfma_f32_16x16x32_bf16 v[28:31],a[100:103],v[48:51],v[28:31]
s_mov_b64 s[46:47],s[48:49]
v_fma_f32 v65,v17,v17,v65
v_add_f32 v64,v64,v18
v_mfma_f32_16x16x32_bf16 v[24:27],a[104:107],v[52:55],v[24:27]
s_mov_b64 s[48:49],s[50:51]
ds_read_b128 v[48:51],v2 offset:53248
v_fma_f32 v65,v18,v18,v65
v_mfma_f32_16x16x32_bf16 v[28:31],a[108:111],v[52:55],v[28:31]
s_mov_b64 s[50:51],s[52:53]
v_add_f32 v64,v64,v19
v_fma_f32 v65,v19,v19,v65
v_mfma_f32_16x16x32_bf16 v[24:27],a[112:115],v[56:59],v[24:27]
ds_read_b128 v[52:55],v2 offset:54272
v_add_f32 v64,v64,v20
v_mfma_f32_16x16x32_bf16 v[28:31],a[116:119],v[56:59],v[28:31]
v_fma_f32 v65,v20,v20,v65
v_add_f32 v64,v64,v21
v_fma_f32 v65,v21,v21,v65
v_add_f32 v64,v64,v22
v_fma_f32 v65,v22,v22,v65
v_add_f32 v64,v64,v23
v_fma_f32 v65,v23,v23,v65
v_mfma_f32_16x16x32_bf16 v[24:27],a[120:123],v[60:63],v[24:27]
s_nop 0
v_permlane16_swap_b32 v64,v65
v_add_f32 v64,v64,v65
v_mov_b32 v65,v64
v_mfma_f32_16x16x32_bf16 v[28:31],a[124:127],v[60:63],v[28:31]
s_waitcnt lgkmcnt(2)
ds_read_b128 v[56:59],v2 offset:55296
v_permlane32_swap_b32 v64,v65
v_add_f32 v64,v64,v65
v_mfma_f32_16x16x32_bf16 v[24:27],a[128:131],v[32:35],v[24:27]
ds_write_b32 v13,v64 offset:2048
v_mfma_f32_16x16x32_bf16 v[28:31],a[132:135],v[32:35],v[28:31]
ds_read_b128 v[60:63],v2 offset:56320
v_mfma_f32_16x16x32_bf16 v[24:27],a[136:139],v[36:39],v[24:27]
ds_read_b128 v[32:35],v2 offset:57344
v_mfma_f32_16x16x32_bf16 v[28:31],a[140:143],v[36:39],v[28:31]
v_mfma_f32_16x16x32_bf16 v[24:27],a[144:147],v[40:43],v[24:27]
s_waitcnt vmcnt(11) lgkmcnt(2)
ds_read_b128 v[36:39],v2 offset:58368
v_mfma_f32_16x16x32_bf16 v[28:31],a[148:151],v[40:43],v[28:31]
s_barrier
v_mfma_f32_16x16x32_bf16 v[24:27],a[152:155],v[44:47],v[24:27]
ds_read_b128 v[40:43],v2 offset:59392
ds_read_b32 v15,v7 offset:256
v_mfma_f32_16x16x32_bf16 v[28:31],a[156:159],v[44:47],v[28:31]
s_waitcnt lgkmcnt(4)
v_mfma_f32_16x16x32_bf16 v[24:27],a[160:163],v[48:51],v[24:27]
ds_read_b128 v[44:47],v2 offset:60416
v_mfma_f32_16x16x32_bf16 v[28:31],a[164:167],v[48:51],v[28:31]
v_mfma_f32_16x16x32_bf16 v[24:27],a[168:171],v[52:55],v[24:27]
s_waitcnt lgkmcnt(1)
ds_read_b128 v[48:51],v2 offset:61440
v_mfma_f32_16x16x32_bf16 v[28:31],a[172:175],v[52:55],v[28:31]
v_cmp_eq_u32 vcc,s60,v15
v_mfma_f32_16x16x32_bf16 v[24:27],a[176:179],v[56:59],v[24:27]
s_cmp_eq_u32 vcc_lo,-1
ds_read_b128 v[52:55],v2 offset:62464
s_cselect_b32 s66,s86,s59
v_mfma_f32_16x16x32_bf16 v[28:31],a[180:183],v[56:59],v[28:31]
s_cmp_ge_u32 s66,s59
s_cselect_b32 s68,s68,s70
s_add_u32 m0,s78,0
v_mfma_f32_16x16x32_bf16 v[24:27],a[184:187],v[60:63],v[24:27]
buffer_load_dword v6,s[36:39],s68 offen offset:0 sc1 lds
v_mfma_f32_16x16x32_bf16 v[28:31],a[188:191],v[60:63],v[28:31])"
R"(
s_waitcnt lgkmcnt(2)
ds_read_b128 v[56:59],v2 offset:63488
s_xor_b32 s64,s64,65536
v_mfma_f32_16x16x32_bf16 v[24:27],a[192:195],v[32:35],v[24:27]
s_waitcnt vmcnt(8)
ds_read_b128 v[60:63],v2 offset:64512
v_xor_b32 v2,65536,v2
v_mfma_f32_16x16x32_bf16 v[28:31],a[196:199],v[32:35],v[28:31]
s_barrier
s_mov_b32 m0,s62
v_mfma_f32_16x16x32_bf16 v[24:27],a[200:203],v[36:39],v[24:27]
buffer_load_dwordx4 v0,s[36:39],s66 offen offset:0 sc1 lds
v_mfma_f32_16x16x32_bf16 v[28:31],a[204:207],v[36:39],v[28:31]
s_add_u32 s74,s53,s55
ds_read_b128 v[32:35],v2 offset:0
ds_read_b128 v[84:87],v8 offset:8192
v_mfma_f32_16x16x32_bf16 v[24:27],a[208:211],v[40:43],v[24:27]
s_cmp_eq_u32 s74,s56
ds_read_b128 v[36:39],v2 offset:1024
ds_read_b64 v[90:91],v9 offset:9216
v_mfma_f32_16x16x32_bf16 v[28:31],a[212:215],v[40:43],v[28:31]
s_cselect_b32 s74,s57,s74
v_xor_b32 v8,16384,v8
v_xor_b32 v9,16384,v9
v_mfma_f32_16x16x32_bf16 v[24:27],a[216:219],v[44:47],v[24:27]
buffer_load_dwordx4 v0,s[36:39],s66 offen offset:1024 sc1 lds
v_mfma_f32_16x16x32_bf16 v[28:31],a[220:223],v[44:47],v[28:31]
s_waitcnt lgkmcnt(4)
ds_read_b128 v[40:43],v2 offset:2048
s_cmp_eq_u32 s74,s58
v_mfma_f32_16x16x32_bf16 v[24:27],a[224:227],v[48:51],v[24:27]
s_cselect_b32 s74,s59,s74
ds_read_b128 v[44:47],v2 offset:3072
s_mov_b32 s52,s74
v_mfma_f32_16x16x32_bf16 v[28:31],a[228:231],v[48:51],v[28:31]
s_cmp_lt_u32 s67,s59
v_mfma_f32_16x16x32_bf16 v[24:27],a[232:235],v[52:55],v[24:27]
s_waitcnt lgkmcnt(2)
buffer_load_dwordx4 v0,s[36:39],s66 offen offset:2048 sc1 lds
v_mfma_f32_16x16x32_bf16 v[28:31],a[236:239],v[52:55],v[28:31]
ds_read_b128 v[48:51],v2 offset:4096
v_mfma_f32_16x16x32_bf16 v[24:27],a[240:243],v[56:59],v[24:27]
ds_read_b128 v[52:55],v2 offset:5120
v_mfma_f32_16x16x32_bf16 v[28:31],a[244:247],v[56:59],v[28:31]
v_mfma_f32_16x16x32_bf16 v[24:27],a[248:251],v[60:63],v[24:27]
s_waitcnt lgkmcnt(2)
ds_read_b128 v[56:59],v2 offset:6144
buffer_load_dwordx4 v0,s[36:39],s66 offen offset:3072 sc1 lds
v_mfma_f32_16x16x32_bf16 v[28:31],a[252:255],v[60:63],v[28:31]
s_cbranch_scc1 Lg2_d0w0_%=
s_branch Lg2_slow0_%=
Lg2_d0w1_%=:
v_mfma_f32_16x16x32_bf16 v[16:19],a[0:3],v[32:35],0
s_add_u32 m0,s62,16384
ds_read_b128 v[60:63],v2 offset:7168
v_lshlrev_b32 v64,16,v84
v_mfma_f32_16x16x32_bf16 v[20:23],a[4:7],v[32:35],0
s_lshr_b32 s73,s47,0
v_and_b32 v65,0xffff0000,v84
v_lshlrev_b32 v66,16,v85
v_mfma_f32_16x16x32_bf16 v[16:19],a[8:11],v[36:39],v[16:19]
buffer_load_dwordx4 v1,s[36:39],s66 offen offset:0 sc1 lds
v_mfma_f32_16x16x32_bf16 v[20:23],a[12:15],v[36:39],v[20:23]
s_xor_b32 s87,s64,65536
ds_read_b128 v[32:35],v2 offset:8192
v_and_b32 v67,0xffff0000,v85
v_mfma_f32_16x16x32_bf16 v[16:19],a[16:19],v[40:43],v[16:19]
s_cmp_ge_u32 s66,s59
ds_read_b128 v[36:39],v2 offset:9216
v_lshlrev_b32 v68,16,v86
v_mfma_f32_16x16x32_bf16 v[20:23],a[20:23],v[40:43],v[20:23]
s_cselect_b32 s86,s49,s50
v_and_b32 v69,0xffff0000,v86
v_lshlrev_b32 v70,16,v87
v_mfma_f32_16x16x32_bf16 v[16:19],a[24:27],v[44:47],v[16:19]
buffer_load_dwordx4 v1,s[36:39],s66 offen offset:1024 sc1 lds
v_mfma_f32_16x16x32_bf16 v[20:23],a[28:31],v[44:47],v[20:23]
s_waitcnt lgkmcnt(2)
ds_read_b128 v[40:43],v2 offset:10240
v_and_b32 v71,0xffff0000,v87
v_mfma_f32_16x16x32_bf16 v[16:19],a[32:35],v[48:51],v[16:19]
s_cselect_b32 s87,s87,s63
ds_read_b128 v[44:47],v2 offset:11264
v_fma_f32 v64,v64,v91,v90
v_mfma_f32_16x16x32_bf16 v[20:23],a[36:39],v[48:51],v[20:23]
s_lshr_b32 s72,s66,6
v_fma_f32 v65,v65,v91,v90
v_fma_f32 v66,v66,v91,v90
v_mfma_f32_16x16x32_bf16 v[16:19],a[40:43],v[52:55],v[16:19]
buffer_load_dwordx4 v1,s[36:39],s66 offen offset:2048 sc1 lds
v_mfma_f32_16x16x32_bf16 v[20:23],a[44:47],v[52:55],v[20:23]
s_lshr_b32 s71,s62,2
ds_read_b128 v[48:51],v2 offset:12288
v_fma_f32 v67,v67,v91,v90
v_mfma_f32_16x16x32_bf16 v[16:19],a[48:51],v[56:59],v[16:19]
s_mov_b32 s62,s87
ds_read_b128 v[52:55],v2 offset:13312
v_fma_f32 v68,v68,v91,v90
v_mfma_f32_16x16x32_bf16 v[20:23],a[52:55],v[56:59],v[20:23]
v_fma_f32 v69,v69,v91,v90
v_fma_f32 v70,v70,v91,v90
v_mfma_f32_16x16x32_bf16 v[16:19],a[56:59],v[60:63],v[16:19]
buffer_load_dwordx4 v1,s[36:39],s66 offen offset:3072 sc1 lds
v_mfma_f32_16x16x32_bf16 v[20:23],a[60:63],v[60:63],v[20:23]
s_waitcnt lgkmcnt(2)
ds_read_b128 v[56:59],v2 offset:14336
v_fma_f32 v71,v71,v91,v90
v_mfma_f32_16x16x32_bf16 v[16:19],a[64:67],v[32:35],v[16:19]
s_waitcnt vmcnt(9)
ds_read_b128 v[60:63],v2 offset:15360
v_add_f32 v24,v24,v64
v_mfma_f32_16x16x32_bf16 v[20:23],a[68:71],v[32:35],v[20:23]
s_barrier
v_mfma_f32_16x16x32_bf16 v[16:19],a[72:75],v[36:39],v[16:19]
s_add_u32 m0,s71,s88
ds_read_b128 v[32:35],v2 offset:16384
v_add_f32 v25,v25,v65
v_mfma_f32_16x16x32_bf16 v[20:23],a[76:79],v[36:39],v[20:23]
v_add_f32 v26,v26,v66
v_add_f32 v27,v27,v67
v_mfma_f32_16x16x32_bf16 v[16:19],a[80:83],v[40:43],v[16:19]
buffer_load_dwordx4 v10,s[36:39],s66 offen offset:0 sc1 lds
v_mfma_f32_16x16x32_bf16 v[20:23],a[84:87],v[40:43],v[20:23]
ds_read_b128 v[36:39],v2 offset:17408
v_add_f32 v28,v28,v68
v_mfma_f32_16x16x32_bf16 v[16:19],a[88:91],v[44:47],v[16:19]
buffer_load_dword v11,s[36:39],s72 offen offset:1024 sc1 lds
v_mfma_f32_16x16x32_bf16 v[20:23],a[92:95],v[44:47],v[20:23]
s_waitcnt lgkmcnt(2)
ds_read_b128 v[40:43],v2 offset:18432
v_add_f32 v29,v29,v69
v_mfma_f32_16x16x32_bf16 v[16:19],a[96:99],v[48:51],v[16:19]
s_mov_b32 m0,s62
v_add_f32 v30,v30,v70
v_add_f32 v31,v31,v71
v_cvt_pk_bf16_f32 v72,v24,v25
v_cvt_pk_bf16_f32 v73,v26,v27
v_cvt_pk_bf16_f32 v74,v28,v29
v_cvt_pk_bf16_f32 v75,v30,v31
v_mfma_f32_16x16x32_bf16 v[20:23],a[100:103],v[48:51],v[20:23]
ds_read_b128 v[44:47],v2 offset:19456
buffer_store_dwordx4 v[72:75],v3,s[36:39],s73 offen offset:0 sc1
v_add_f32 v64,v24,v25
v_mul_f32 v65,v24,v24
v_fma_f32 v65,v25,v25,v65
v_mfma_f32_16x16x32_bf16 v[16:19],a[104:107],v[52:55],v[16:19]
ds_read_b128 v[48:51],v2 offset:20480
v_add_f32 v64,v64,v26
v_mfma_f32_16x16x32_bf16 v[20:23],a[108:111],v[52:55],v[20:23]
v_fma_f32 v65,v26,v26,v65
v_add_f32 v64,v64,v27
v_mfma_f32_16x16x32_bf16 v[16:19],a[112:115],v[56:59],v[16:19]
ds_read_b128 v[52:55],v2 offset:21504
v_fma_f32 v65,v27,v27,v65
v_mfma_f32_16x16x32_bf16 v[20:23],a[116:119],v[56:59],v[20:23]
v_add_f32 v64,v64,v28
v_fma_f32 v65,v28,v28,v65
v_add_f32 v64,v64,v29
v_fma_f32 v65,v29,v29,v65
v_add_f32 v64,v64,v30
v_fma_f32 v65,v30,v30,v65
v_add_f32 v64,v64,v31
v_fma_f32 v65,v31,v31,v65
v_mfma_f32_16x16x32_bf16 v[16:19],a[120:123],v[60:63],v[16:19]
s_nop 0
v_permlane16_swap_b32 v64,v65
v_add_f32 v64,v64,v65
v_mov_b32 v65,v64
v_mfma_f32_16x16x32_bf16 v[20:23],a[124:127],v[60:63],v[20:23]
s_waitcnt lgkmcnt(2)
ds_read_b128 v[56:59],v2 offset:22528
v_permlane32_swap_b32 v64,v65
v_add_f32 v64,v64,v65
v_mfma_f32_16x16x32_bf16 v[16:19],a[128:131],v[32:35],v[16:19]
ds_write_b32 v13,v64 offset:0
v_mfma_f32_16x16x32_bf16 v[20:23],a[132:135],v[32:35],v[20:23]
ds_read_b128 v[60:63],v2 offset:23552
v_mfma_f32_16x16x32_bf16 v[16:19],a[136:139],v[36:39],v[16:19]
ds_read_b128 v[32:35],v2 offset:24576
v_mfma_f32_16x16x32_bf16 v[20:23],a[140:143],v[36:39],v[20:23]
v_mfma_f32_16x16x32_bf16 v[16:19],a[144:147],v[40:43],v[16:19]
s_waitcnt vmcnt(11) lgkmcnt(2)
ds_read_b128 v[36:39],v2 offset:25600
v_mfma_f32_16x16x32_bf16 v[20:23],a[148:151],v[40:43],v[20:23]
s_barrier
v_mfma_f32_16x16x32_bf16 v[16:19],a[152:155],v[44:47],v[16:19]
ds_read_b128 v[40:43],v2 offset:26624
ds_read_b32 v15,v7 offset:0
v_mfma_f32_16x16x32_bf16 v[20:23],a[156:159],v[44:47],v[20:23]
s_waitcnt lgkmcnt(4)
v_mfma_f32_16x16x32_bf16 v[16:19],a[160:163],v[48:51],v[16:19]
ds_read_b128 v[44:47],v2 offset:27648
v_mfma_f32_16x16x32_bf16 v[20:23],a[164:167],v[48:51],v[20:23]
v_mfma_f32_16x16x32_bf16 v[16:19],a[168:171],v[52:55],v[16:19]
s_waitcnt lgkmcnt(1)
ds_read_b128 v[48:51],v2 offset:28672
v_mfma_f32_16x16x32_bf16 v[20:23],a[172:175],v[52:55],v[20:23]
v_cmp_eq_u32 vcc,s60,v15
v_mfma_f32_16x16x32_bf16 v[16:19],a[176:179],v[56:59],v[16:19]
s_cmp_eq_u32 vcc_lo,-1)"
R"(
ds_read_b128 v[52:55],v2 offset:29696
s_cselect_b32 s67,s86,s59
v_mfma_f32_16x16x32_bf16 v[20:23],a[180:183],v[56:59],v[20:23]
buffer_load_dwordx4 v0,s[36:39],s67 offen offset:0 sc1 lds
v_mfma_f32_16x16x32_bf16 v[16:19],a[184:187],v[60:63],v[16:19]
s_xor_b32 s63,s63,65536
ds_read_b128 v[56:59],v2 offset:30720
s_add_u32 s53,s52,s55
v_mfma_f32_16x16x32_bf16 v[20:23],a[188:191],v[60:63],v[20:23]
s_waitcnt lgkmcnt(3)
s_cmp_eq_u32 s53,s56
s_cselect_b32 s53,s57,s53
v_mfma_f32_16x16x32_bf16 v[16:19],a[192:195],v[32:35],v[16:19]
s_waitcnt vmcnt(8)
ds_read_b128 v[60:63],v2 offset:31744
s_cmp_eq_u32 s53,s58
v_mfma_f32_16x16x32_bf16 v[20:23],a[196:199],v[32:35],v[20:23]
s_barrier
v_mfma_f32_16x16x32_bf16 v[16:19],a[200:203],v[36:39],v[16:19]
s_cselect_b32 s53,s59,s53
ds_read_b128 v[32:35],v2 offset:32768
ds_read_b128 v[80:83],v8 offset:0
v_mfma_f32_16x16x32_bf16 v[20:23],a[204:207],v[36:39],v[20:23]
buffer_load_dwordx4 v0,s[36:39],s67 offen offset:1024 sc1 lds
v_mfma_f32_16x16x32_bf16 v[16:19],a[208:211],v[40:43],v[16:19]
s_cmp_ge_u32 s66,s59
ds_read_b128 v[36:39],v2 offset:33792
ds_read_b64 v[88:89],v9 offset:1024
v_mfma_f32_16x16x32_bf16 v[20:23],a[212:215],v[40:43],v[20:23]
v_mfma_f32_16x16x32_bf16 v[16:19],a[216:219],v[44:47],v[16:19]
ds_read_b128 v[40:43],v2 offset:34816
v_mfma_f32_16x16x32_bf16 v[20:23],a[220:223],v[44:47],v[20:23]
s_waitcnt lgkmcnt(5)
buffer_load_dwordx4 v0,s[36:39],s67 offen offset:2048 sc1 lds
v_mfma_f32_16x16x32_bf16 v[16:19],a[224:227],v[48:51],v[16:19]
ds_read_b128 v[44:47],v2 offset:35840
v_mfma_f32_16x16x32_bf16 v[20:23],a[228:231],v[48:51],v[20:23]
v_mfma_f32_16x16x32_bf16 v[16:19],a[232:235],v[52:55],v[16:19]
s_waitcnt lgkmcnt(2)
ds_read_b128 v[48:51],v2 offset:36864
v_mfma_f32_16x16x32_bf16 v[20:23],a[236:239],v[52:55],v[20:23]
buffer_load_dwordx4 v0,s[36:39],s67 offen offset:3072 sc1 lds
v_mfma_f32_16x16x32_bf16 v[16:19],a[240:243],v[56:59],v[16:19]
ds_read_b128 v[52:55],v2 offset:37888
v_mfma_f32_16x16x32_bf16 v[20:23],a[244:247],v[56:59],v[20:23]
v_mfma_f32_16x16x32_bf16 v[16:19],a[248:251],v[60:63],v[16:19]
s_waitcnt lgkmcnt(2)
ds_read_b128 v[56:59],v2 offset:38912
v_mfma_f32_16x16x32_bf16 v[20:23],a[252:255],v[60:63],v[20:23]
s_cbranch_scc1 Lg2_slow1_%=
Lg2_d1w1_%=:
v_mfma_f32_16x16x32_bf16 v[24:27],a[0:3],v[32:35],0
s_add_u32 m0,s62,16384
ds_read_b128 v[60:63],v2 offset:39936
v_lshlrev_b32 v64,16,v80
v_mfma_f32_16x16x32_bf16 v[28:31],a[4:7],v[32:35],0
s_lshr_b32 s73,s48,0
v_and_b32 v65,0xffff0000,v80
v_lshlrev_b32 v66,16,v81
v_mfma_f32_16x16x32_bf16 v[24:27],a[8:11],v[36:39],v[24:27]
buffer_load_dwordx4 v1,s[36:39],s67 offen offset:0 sc1 lds
v_mfma_f32_16x16x32_bf16 v[28:31],a[12:15],v[36:39],v[28:31]
s_xor_b32 s87,s63,65536
ds_read_b128 v[32:35],v2 offset:40960
v_and_b32 v67,0xffff0000,v81
v_mfma_f32_16x16x32_bf16 v[24:27],a[16:19],v[40:43],v[24:27]
s_cmp_ge_u32 s67,s59
ds_read_b128 v[36:39],v2 offset:41984
v_lshlrev_b32 v68,16,v82
v_mfma_f32_16x16x32_bf16 v[28:31],a[20:23],v[40:43],v[28:31]
s_cselect_b32 s86,s50,s51
v_and_b32 v69,0xffff0000,v82
v_lshlrev_b32 v70,16,v83
v_mfma_f32_16x16x32_bf16 v[24:27],a[24:27],v[44:47],v[24:27]
buffer_load_dwordx4 v1,s[36:39],s67 offen offset:1024 sc1 lds
v_mfma_f32_16x16x32_bf16 v[28:31],a[28:31],v[44:47],v[28:31]
s_waitcnt lgkmcnt(2)
ds_read_b128 v[40:43],v2 offset:43008
v_and_b32 v71,0xffff0000,v83
v_mfma_f32_16x16x32_bf16 v[24:27],a[32:35],v[48:51],v[24:27]
s_cselect_b32 s87,s87,s64
ds_read_b128 v[44:47],v2 offset:44032
v_fma_f32 v64,v64,v89,v88
v_mfma_f32_16x16x32_bf16 v[28:31],a[36:39],v[48:51],v[28:31]
s_lshr_b32 s72,s67,6
v_fma_f32 v65,v65,v89,v88
v_fma_f32 v66,v66,v89,v88
v_mfma_f32_16x16x32_bf16 v[24:27],a[40:43],v[52:55],v[24:27]
buffer_load_dwordx4 v1,s[36:39],s67 offen offset:2048 sc1 lds
v_mfma_f32_16x16x32_bf16 v[28:31],a[44:47],v[52:55],v[28:31]
s_lshr_b32 s71,s62,2
ds_read_b128 v[48:51],v2 offset:45056
v_fma_f32 v67,v67,v89,v88
v_mfma_f32_16x16x32_bf16 v[24:27],a[48:51],v[56:59],v[24:27]
s_mov_b32 s62,s87
ds_read_b128 v[52:55],v2 offset:46080
v_fma_f32 v68,v68,v89,v88
v_mfma_f32_16x16x32_bf16 v[28:31],a[52:55],v[56:59],v[28:31]
s_mov_b64 s[44:45],s[46:47]
v_fma_f32 v69,v69,v89,v88
v_fma_f32 v70,v70,v89,v88
v_mfma_f32_16x16x32_bf16 v[24:27],a[56:59],v[60:63],v[24:27]
buffer_load_dwordx4 v1,s[36:39],s67 offen offset:3072 sc1 lds
v_mfma_f32_16x16x32_bf16 v[28:31],a[60:63],v[60:63],v[28:31]
s_waitcnt lgkmcnt(2)
ds_read_b128 v[56:59],v2 offset:47104
v_fma_f32 v71,v71,v89,v88
v_mfma_f32_16x16x32_bf16 v[24:27],a[64:67],v[32:35],v[24:27]
s_waitcnt vmcnt(9)
ds_read_b128 v[60:63],v2 offset:48128
v_add_f32 v16,v16,v64
v_mfma_f32_16x16x32_bf16 v[28:31],a[68:71],v[32:35],v[28:31]
s_barrier
v_mfma_f32_16x16x32_bf16 v[24:27],a[72:75],v[36:39],v[24:27]
s_add_u32 m0,s71,s88
ds_read_b128 v[32:35],v2 offset:49152
v_add_f32 v17,v17,v65
v_mfma_f32_16x16x32_bf16 v[28:31],a[76:79],v[36:39],v[28:31]
s_mov_b64 s[46:47],s[48:49]
v_add_f32 v18,v18,v66
v_add_f32 v19,v19,v67
v_mfma_f32_16x16x32_bf16 v[24:27],a[80:83],v[40:43],v[24:27]
buffer_load_dwordx4 v10,s[36:39],s67 offen offset:0 sc1 lds
v_mfma_f32_16x16x32_bf16 v[28:31],a[84:87],v[40:43],v[28:31]
s_mov_b64 s[48:49],s[50:51]
ds_read_b128 v[36:39],v2 offset:50176
v_add_f32 v20,v20,v68
v_mfma_f32_16x16x32_bf16 v[24:27],a[88:91],v[44:47],v[24:27]
buffer_load_dword v11,s[36:39],s72 offen offset:1024 sc1 lds
v_mfma_f32_16x16x32_bf16 v[28:31],a[92:95],v[44:47],v[28:31]
s_waitcnt lgkmcnt(2)
ds_read_b128 v[40:43],v2 offset:51200
v_add_f32 v21,v21,v69
v_mfma_f32_16x16x32_bf16 v[24:27],a[96:99],v[48:51],v[24:27]
s_mov_b32 m0,s62
v_add_f32 v22,v22,v70
v_add_f32 v23,v23,v71
v_cvt_pk_bf16_f32 v72,v16,v17
v_cvt_pk_bf16_f32 v73,v18,v19
v_cvt_pk_bf16_f32 v74,v20,v21
v_cvt_pk_bf16_f32 v75,v22,v23
v_mfma_f32_16x16x32_bf16 v[28:31],a[100:103],v[48:51],v[28:31]
s_mov_b64 s[50:51],s[52:53]
ds_read_b128 v[44:47],v2 offset:52224
buffer_store_dwordx4 v[72:75],v3,s[36:39],s73 offen offset:0 sc1
v_add_f32 v64,v16,v17
v_mul_f32 v65,v16,v16
v_fma_f32 v65,v17,v17,v65
v_mfma_f32_16x16x32_bf16 v[24:27],a[104:107],v[52:55],v[24:27]
ds_read_b128 v[48:51],v2 offset:53248
v_add_f32 v64,v64,v18
v_mfma_f32_16x16x32_bf16 v[28:31],a[108:111],v[52:55],v[28:31]
v_fma_f32 v65,v18,v18,v65
v_add_f32 v64,v64,v19
v_mfma_f32_16x16x32_bf16 v[24:27],a[112:115],v[56:59],v[24:27]
ds_read_b128 v[52:55],v2 offset:54272
v_fma_f32 v65,v19,v19,v65
v_mfma_f32_16x16x32_bf16 v[28:31],a[116:119],v[56:59],v[28:31]
v_add_f32 v64,v64,v20
v_fma_f32 v65,v20,v20,v65
v_add_f32 v64,v64,v21
v_fma_f32 v65,v21,v21,v65
v_add_f32 v64,v64,v22
v_fma_f32 v65,v22,v22,v65
v_add_f32 v64,v64,v23
v_fma_f32 v65,v23,v23,v65
v_mfma_f32_16x16x32_bf16 v[24:27],a[120:123],v[60:63],v[24:27]
s_nop 0
v_permlane16_swap_b32 v64,v65
v_add_f32 v64,v64,v65
v_mov_b32 v65,v64
v_mfma_f32_16x16x32_bf16 v[28:31],a[124:127],v[60:63],v[28:31]
s_waitcnt lgkmcnt(2)
ds_read_b128 v[56:59],v2 offset:55296
v_permlane32_swap_b32 v64,v65
v_add_f32 v64,v64,v65
v_mfma_f32_16x16x32_bf16 v[24:27],a[128:131],v[32:35],v[24:27]
ds_write_b32 v13,v64 offset:2048
v_mfma_f32_16x16x32_bf16 v[28:31],a[132:135],v[32:35],v[28:31]
ds_read_b128 v[60:63],v2 offset:56320
v_mfma_f32_16x16x32_bf16 v[24:27],a[136:139],v[36:39],v[24:27]
ds_read_b128 v[32:35],v2 offset:57344
v_mfma_f32_16x16x32_bf16 v[28:31],a[140:143],v[36:39],v[28:31]
v_mfma_f32_16x16x32_bf16 v[24:27],a[144:147],v[40:43],v[24:27]
s_waitcnt vmcnt(11) lgkmcnt(2)
ds_read_b128 v[36:39],v2 offset:58368
v_mfma_f32_16x16x32_bf16 v[28:31],a[148:151],v[40:43],v[28:31]
s_barrier
v_mfma_f32_16x16x32_bf16 v[24:27],a[152:155],v[44:47],v[24:27]
ds_read_b128 v[40:43],v2 offset:59392
ds_read_b32 v15,v7 offset:256
v_mfma_f32_16x16x32_bf16 v[28:31],a[156:159],v[44:47],v[28:31]
s_waitcnt lgkmcnt(4)
v_mfma_f32_16x16x32_bf16 v[24:27],a[160:163],v[48:51],v[24:27]
ds_read_b128 v[44:47],v2 offset:60416
v_mfma_f32_16x16x32_bf16 v[28:31],a[164:167],v[48:51],v[28:31]
v_mfma_f32_16x16x32_bf16 v[24:27],a[168:171],v[52:55],v[24:27]
s_waitcnt lgkmcnt(1)
ds_read_b128 v[48:51],v2 offset:61440
v_mfma_f32_16x16x32_bf16 v[28:31],a[172:175],v[52:55],v[28:31])"
R"(
v_cmp_eq_u32 vcc,s60,v15
v_mfma_f32_16x16x32_bf16 v[24:27],a[176:179],v[56:59],v[24:27]
s_cmp_eq_u32 vcc_lo,-1
ds_read_b128 v[52:55],v2 offset:62464
s_cselect_b32 s66,s86,s59
v_mfma_f32_16x16x32_bf16 v[28:31],a[180:183],v[56:59],v[28:31]
buffer_load_dwordx4 v0,s[36:39],s66 offen offset:0 sc1 lds
v_mfma_f32_16x16x32_bf16 v[24:27],a[184:187],v[60:63],v[24:27]
s_xor_b32 s64,s64,65536
ds_read_b128 v[56:59],v2 offset:63488
s_add_u32 s74,s53,s55
v_mfma_f32_16x16x32_bf16 v[28:31],a[188:191],v[60:63],v[28:31]
s_waitcnt lgkmcnt(3)
s_cmp_eq_u32 s74,s56
s_cselect_b32 s74,s57,s74
v_mfma_f32_16x16x32_bf16 v[24:27],a[192:195],v[32:35],v[24:27]
s_waitcnt vmcnt(8)
ds_read_b128 v[60:63],v2 offset:64512
v_xor_b32 v2,65536,v2
v_mfma_f32_16x16x32_bf16 v[28:31],a[196:199],v[32:35],v[28:31]
s_barrier
v_mfma_f32_16x16x32_bf16 v[24:27],a[200:203],v[36:39],v[24:27]
s_cmp_eq_u32 s74,s58
ds_read_b128 v[32:35],v2 offset:0
ds_read_b128 v[84:87],v8 offset:8192
v_mfma_f32_16x16x32_bf16 v[28:31],a[204:207],v[36:39],v[28:31]
buffer_load_dwordx4 v0,s[36:39],s66 offen offset:1024 sc1 lds
v_mfma_f32_16x16x32_bf16 v[24:27],a[208:211],v[40:43],v[24:27]
s_cselect_b32 s74,s59,s74
ds_read_b128 v[36:39],v2 offset:1024
ds_read_b64 v[90:91],v9 offset:9216
v_mfma_f32_16x16x32_bf16 v[28:31],a[212:215],v[40:43],v[28:31]
s_mov_b32 s52,s74
v_xor_b32 v8,16384,v8
v_xor_b32 v9,16384,v9
v_mfma_f32_16x16x32_bf16 v[24:27],a[216:219],v[44:47],v[24:27]
s_cmp_lt_u32 s67,s59
ds_read_b128 v[40:43],v2 offset:2048
v_mfma_f32_16x16x32_bf16 v[28:31],a[220:223],v[44:47],v[28:31]
s_waitcnt lgkmcnt(5)
buffer_load_dwordx4 v0,s[36:39],s66 offen offset:2048 sc1 lds
v_mfma_f32_16x16x32_bf16 v[24:27],a[224:227],v[48:51],v[24:27]
ds_read_b128 v[44:47],v2 offset:3072
v_mfma_f32_16x16x32_bf16 v[28:31],a[228:231],v[48:51],v[28:31]
v_mfma_f32_16x16x32_bf16 v[24:27],a[232:235],v[52:55],v[24:27]
s_waitcnt lgkmcnt(2)
ds_read_b128 v[48:51],v2 offset:4096
v_mfma_f32_16x16x32_bf16 v[28:31],a[236:239],v[52:55],v[28:31]
buffer_load_dwordx4 v0,s[36:39],s66 offen offset:3072 sc1 lds
v_mfma_f32_16x16x32_bf16 v[24:27],a[240:243],v[56:59],v[24:27]
ds_read_b128 v[52:55],v2 offset:5120
v_mfma_f32_16x16x32_bf16 v[28:31],a[244:247],v[56:59],v[28:31]
v_mfma_f32_16x16x32_bf16 v[24:27],a[248:251],v[60:63],v[24:27]
s_waitcnt lgkmcnt(2)
ds_read_b128 v[56:59],v2 offset:6144
v_mfma_f32_16x16x32_bf16 v[28:31],a[252:255],v[60:63],v[28:31]
s_cbranch_scc1 Lg2_d0w1_%=
s_branch Lg2_slow0_%=
Lg2_d0w2_%=:
v_mfma_f32_16x16x32_bf16 v[16:19],a[0:3],v[32:35],0
s_add_u32 m0,s62,16384
ds_read_b128 v[60:63],v2 offset:7168
v_lshlrev_b32 v64,16,v84
v_mfma_f32_16x16x32_bf16 v[20:23],a[4:7],v[32:35],0
s_lshr_b32 s73,s47,0
v_and_b32 v65,0xffff0000,v84
v_lshlrev_b32 v66,16,v85
v_mfma_f32_16x16x32_bf16 v[16:19],a[8:11],v[36:39],v[16:19]
s_lshr_b32 s85,s47,5
ds_read_b128 v[32:35],v2 offset:8192
v_and_b32 v67,0xffff0000,v85
v_mfma_f32_16x16x32_bf16 v[20:23],a[12:15],v[36:39],v[20:23]
buffer_load_dwordx4 v1,s[36:39],s66 offen offset:0 sc1 lds
v_mfma_f32_16x16x32_bf16 v[16:19],a[16:19],v[40:43],v[16:19]
s_xor_b32 s87,s64,65536
ds_read_b128 v[36:39],v2 offset:9216
v_lshlrev_b32 v68,16,v86
v_mfma_f32_16x16x32_bf16 v[20:23],a[20:23],v[40:43],v[20:23]
s_cmp_ge_u32 s66,s59
v_and_b32 v69,0xffff0000,v86
v_lshlrev_b32 v70,16,v87
v_mfma_f32_16x16x32_bf16 v[16:19],a[24:27],v[44:47],v[16:19]
s_cselect_b32 s86,s49,s50
ds_read_b128 v[40:43],v2 offset:10240
v_and_b32 v71,0xffff0000,v87
v_mfma_f32_16x16x32_bf16 v[20:23],a[28:31],v[44:47],v[20:23]
s_waitcnt lgkmcnt(3)
buffer_load_dwordx4 v1,s[36:39],s66 offen offset:1024 sc1 lds
v_mfma_f32_16x16x32_bf16 v[16:19],a[32:35],v[48:51],v[16:19]
s_cselect_b32 s87,s87,s63
ds_read_b128 v[44:47],v2 offset:11264
v_fma_f32 v64,v64,v91,v90
v_mfma_f32_16x16x32_bf16 v[20:23],a[36:39],v[48:51],v[20:23]
s_lshr_b32 s72,s66,6
v_fma_f32 v65,v65,v91,v90
v_fma_f32 v66,v66,v91,v90
v_mfma_f32_16x16x32_bf16 v[16:19],a[40:43],v[52:55],v[16:19]
s_lshr_b32 s71,s62,2
ds_read_b128 v[48:51],v2 offset:12288
v_fma_f32 v67,v67,v91,v90
v_mfma_f32_16x16x32_bf16 v[20:23],a[44:47],v[52:55],v[20:23]
buffer_load_dwordx4 v1,s[36:39],s66 offen offset:2048 sc1 lds
v_mfma_f32_16x16x32_bf16 v[16:19],a[48:51],v[56:59],v[16:19]
s_mov_b32 s62,s87
ds_read_b128 v[52:55],v2 offset:13312
v_fma_f32 v68,v68,v91,v90
v_mfma_f32_16x16x32_bf16 v[20:23],a[52:55],v[56:59],v[20:23]
v_fma_f32 v69,v69,v91,v90
v_fma_f32 v70,v70,v91,v90
v_mfma_f32_16x16x32_bf16 v[16:19],a[56:59],v[60:63],v[16:19]
ds_read_b128 v[56:59],v2 offset:14336
v_fma_f32 v71,v71,v91,v90
v_mfma_f32_16x16x32_bf16 v[20:23],a[60:63],v[60:63],v[20:23]
s_waitcnt lgkmcnt(3)
buffer_load_dwordx4 v1,s[36:39],s66 offen offset:3072 sc1 lds
v_mfma_f32_16x16x32_bf16 v[16:19],a[64:67],v[32:35],v[16:19]
s_waitcnt vmcnt(10)
ds_read_b128 v[60:63],v2 offset:15360
v_add_f32 v24,v24,v64
v_mfma_f32_16x16x32_bf16 v[20:23],a[68:71],v[32:35],v[20:23]
s_barrier
v_mfma_f32_16x16x32_bf16 v[16:19],a[72:75],v[36:39],v[16:19]
s_add_u32 m0,s71,s88
ds_read_b128 v[32:35],v2 offset:16384
v_add_f32 v25,v25,v65
v_mfma_f32_16x16x32_bf16 v[20:23],a[76:79],v[36:39],v[20:23]
v_add_f32 v26,v26,v66
v_add_f32 v27,v27,v67
v_mfma_f32_16x16x32_bf16 v[16:19],a[80:83],v[40:43],v[16:19]
ds_read_b128 v[36:39],v2 offset:17408
v_add_f32 v28,v28,v68
v_mfma_f32_16x16x32_bf16 v[20:23],a[84:87],v[40:43],v[20:23]
buffer_load_dwordx4 v10,s[36:39],s66 offen offset:0 sc1 lds
v_mfma_f32_16x16x32_bf16 v[16:19],a[88:91],v[44:47],v[16:19]
buffer_load_dword v11,s[36:39],s72 offen offset:1024 sc1 lds
v_mfma_f32_16x16x32_bf16 v[20:23],a[92:95],v[44:47],v[20:23]
s_waitcnt lgkmcnt(2)
ds_read_b128 v[40:43],v2 offset:18432
v_add_f32 v29,v29,v69
v_mfma_f32_16x16x32_bf16 v[16:19],a[96:99],v[48:51],v[16:19]
s_mov_b32 m0,s62
ds_read_b128 v[44:47],v2 offset:19456
v_add_f32 v30,v30,v70
v_mfma_f32_16x16x32_bf16 v[20:23],a[100:103],v[48:51],v[20:23]
v_add_f32 v31,v31,v71
v_cvt_pk_bf16_f32 v72,v24,v25
v_cvt_pk_bf16_f32 v73,v26,v27
v_cvt_pk_bf16_f32 v74,v28,v29
v_cvt_pk_bf16_f32 v75,v30,v31
v_mfma_f32_16x16x32_bf16 v[16:19],a[104:107],v[52:55],v[16:19]
buffer_store_dwordx4 v[72:75],v3,s[36:39],s73 offen offset:0 sc1
ds_read_b128 v[48:51],v2 offset:20480
v_add_f32 v64,v24,v25
v_mul_f32 v65,v24,v24
v_mfma_f32_16x16x32_bf16 v[20:23],a[108:111],v[52:55],v[20:23]
v_fma_f32 v65,v25,v25,v65
v_add_f32 v64,v64,v26
v_mfma_f32_16x16x32_bf16 v[16:19],a[112:115],v[56:59],v[16:19]
ds_read_b128 v[52:55],v2 offset:21504
v_fma_f32 v65,v26,v26,v65
v_mfma_f32_16x16x32_bf16 v[20:23],a[116:119],v[56:59],v[20:23]
v_add_f32 v64,v64,v27
v_fma_f32 v65,v27,v27,v65
v_add_f32 v64,v64,v28
v_fma_f32 v65,v28,v28,v65
v_add_f32 v64,v64,v29
v_fma_f32 v65,v29,v29,v65
v_add_f32 v64,v64,v30
v_fma_f32 v65,v30,v30,v65
v_add_f32 v64,v64,v31
v_fma_f32 v65,v31,v31,v65
v_mfma_f32_16x16x32_bf16 v[16:19],a[120:123],v[60:63],v[16:19]
s_nop 0
v_permlane16_swap_b32 v64,v65
v_add_f32 v64,v64,v65
v_mov_b32 v65,v64
v_mfma_f32_16x16x32_bf16 v[20:23],a[124:127],v[60:63],v[20:23]
s_waitcnt lgkmcnt(2)
ds_read_b128 v[56:59],v2 offset:22528
v_permlane32_swap_b32 v64,v65
v_add_f32 v64,v64,v65
v_mfma_f32_16x16x32_bf16 v[16:19],a[128:131],v[32:35],v[16:19]
ds_write_b32 v13,v64 offset:0
v_mfma_f32_16x16x32_bf16 v[20:23],a[132:135],v[32:35],v[20:23]
ds_read_b128 v[60:63],v2 offset:23552
v_mfma_f32_16x16x32_bf16 v[16:19],a[136:139],v[36:39],v[16:19]
ds_read_b128 v[32:35],v2 offset:24576
v_mfma_f32_16x16x32_bf16 v[20:23],a[140:143],v[36:39],v[20:23]
v_mfma_f32_16x16x32_bf16 v[16:19],a[144:147],v[40:43],v[16:19]
s_waitcnt vmcnt(11) lgkmcnt(2)
ds_read_b128 v[36:39],v2 offset:25600
v_mfma_f32_16x16x32_bf16 v[20:23],a[148:151],v[40:43],v[20:23]
s_barrier
v_mfma_f32_16x16x32_bf16 v[16:19],a[152:155],v[44:47],v[16:19]
ds_read_b128 v[40:43],v2 offset:26624
ds_read_b32 v15,v7 offset:0
v_mfma_f32_16x16x32_bf16 v[20:23],a[156:159],v[44:47],v[20:23]
s_waitcnt lgkmcnt(4)
ds_read_b128 v[76:79],v14 offset:0
v_mfma_f32_16x16x32_bf16 v[16:19],a[160:163],v[48:51],v[16:19])"
R"(
ds_read_b128 v[44:47],v2 offset:27648
v_mfma_f32_16x16x32_bf16 v[20:23],a[164:167],v[48:51],v[20:23]
v_mfma_f32_16x16x32_bf16 v[16:19],a[168:171],v[52:55],v[16:19]
s_waitcnt lgkmcnt(2)
ds_read_b128 v[48:51],v2 offset:28672
v_mfma_f32_16x16x32_bf16 v[20:23],a[172:175],v[52:55],v[20:23]
s_waitcnt lgkmcnt(2)
v_cmp_eq_u32 vcc,s60,v15
v_add_f32 v76,v76,v77
v_add_f32 v78,v78,v79
v_add_f32 v76,v76,v78
v_mfma_f32_16x16x32_bf16 v[16:19],a[176:179],v[56:59],v[16:19]
buffer_store_dword v76,v12,s[36:39],s85 offen offset:0 sc1
ds_read_b128 v[52:55],v2 offset:29696
v_mfma_f32_16x16x32_bf16 v[20:23],a[180:183],v[56:59],v[20:23]
s_cmp_eq_u32 vcc_lo,-1
s_cselect_b32 s67,s86,s59
s_xor_b32 s63,s63,65536
v_mfma_f32_16x16x32_bf16 v[16:19],a[184:187],v[60:63],v[16:19]
buffer_load_dwordx4 v0,s[36:39],s67 offen offset:0 sc1 lds
v_mfma_f32_16x16x32_bf16 v[20:23],a[188:191],v[60:63],v[20:23]
s_waitcnt lgkmcnt(2)
ds_read_b128 v[56:59],v2 offset:30720
s_add_u32 s53,s52,s55
v_mfma_f32_16x16x32_bf16 v[16:19],a[192:195],v[32:35],v[16:19]
s_waitcnt vmcnt(9)
ds_read_b128 v[60:63],v2 offset:31744
s_cmp_eq_u32 s53,s56
v_mfma_f32_16x16x32_bf16 v[20:23],a[196:199],v[32:35],v[20:23]
s_barrier
v_mfma_f32_16x16x32_bf16 v[16:19],a[200:203],v[36:39],v[16:19]
s_cselect_b32 s53,s57,s53
ds_read_b128 v[32:35],v2 offset:32768
ds_read_b128 v[80:83],v8 offset:0
v_mfma_f32_16x16x32_bf16 v[20:23],a[204:207],v[36:39],v[20:23]
s_cmp_eq_u32 s53,s58
ds_read_b64 v[88:89],v9 offset:1024
s_cselect_b32 s53,s59,s53
v_mfma_f32_16x16x32_bf16 v[16:19],a[208:211],v[40:43],v[16:19]
buffer_load_dwordx4 v0,s[36:39],s67 offen offset:1024 sc1 lds
v_mfma_f32_16x16x32_bf16 v[20:23],a[212:215],v[40:43],v[20:23]
s_cmp_ge_u32 s66,s59
ds_read_b128 v[36:39],v2 offset:33792
v_mfma_f32_16x16x32_bf16 v[16:19],a[216:219],v[44:47],v[16:19]
ds_read_b128 v[40:43],v2 offset:34816
v_mfma_f32_16x16x32_bf16 v[20:23],a[220:223],v[44:47],v[20:23]
s_waitcnt lgkmcnt(5)
v_mfma_f32_16x16x32_bf16 v[16:19],a[224:227],v[48:51],v[16:19]
buffer_load_dwordx4 v0,s[36:39],s67 offen offset:2048 sc1 lds
v_mfma_f32_16x16x32_bf16 v[20:23],a[228:231],v[48:51],v[20:23]
s_waitcnt lgkmcnt(2)
ds_read_b128 v[44:47],v2 offset:35840
v_mfma_f32_16x16x32_bf16 v[16:19],a[232:235],v[52:55],v[16:19]
ds_read_b128 v[48:51],v2 offset:36864
v_mfma_f32_16x16x32_bf16 v[20:23],a[236:239],v[52:55],v[20:23]
v_mfma_f32_16x16x32_bf16 v[16:19],a[240:243],v[56:59],v[16:19]
buffer_load_dwordx4 v0,s[36:39],s67 offen offset:3072 sc1 lds
v_mfma_f32_16x16x32_bf16 v[20:23],a[244:247],v[56:59],v[20:23]
ds_read_b128 v[52:55],v2 offset:37888
v_mfma_f32_16x16x32_bf16 v[16:19],a[248:251],v[60:63],v[16:19]
s_waitcnt lgkmcnt(2)
ds_read_b128 v[56:59],v2 offset:38912
v_mfma_f32_16x16x32_bf16 v[20:23],a[252:255],v[60:63],v[20:23]
s_cbranch_scc1 Lg2_slow1_%=
Lg2_d1w2_%=:
v_mfma_f32_16x16x32_bf16 v[24:27],a[0:3],v[32:35],0
s_add_u32 m0,s62,16384
ds_read_b128 v[60:63],v2 offset:39936
v_lshlrev_b32 v64,16,v80
v_mfma_f32_16x16x32_bf16 v[28:31],a[4:7],v[32:35],0
s_lshr_b32 s73,s48,0
v_and_b32 v65,0xffff0000,v80
v_lshlrev_b32 v66,16,v81
v_mfma_f32_16x16x32_bf16 v[24:27],a[8:11],v[36:39],v[24:27]
s_lshr_b32 s85,s48,5
ds_read_b128 v[32:35],v2 offset:40960
v_and_b32 v67,0xffff0000,v81
v_mfma_f32_16x16x32_bf16 v[28:31],a[12:15],v[36:39],v[28:31]
buffer_load_dwordx4 v1,s[36:39],s67 offen offset:0 sc1 lds
v_mfma_f32_16x16x32_bf16 v[24:27],a[16:19],v[40:43],v[24:27]
s_xor_b32 s87,s63,65536
ds_read_b128 v[36:39],v2 offset:41984
v_lshlrev_b32 v68,16,v82
v_mfma_f32_16x16x32_bf16 v[28:31],a[20:23],v[40:43],v[28:31]
s_cmp_ge_u32 s67,s59
v_and_b32 v69,0xffff0000,v82
v_lshlrev_b32 v70,16,v83
v_mfma_f32_16x16x32_bf16 v[24:27],a[24:27],v[44:47],v[24:27]
s_cselect_b32 s86,s50,s51
ds_read_b128 v[40:43],v2 offset:43008
v_and_b32 v71,0xffff0000,v83
v_mfma_f32_16x16x32_bf16 v[28:31],a[28:31],v[44:47],v[28:31]
s_waitcnt lgkmcnt(3)
buffer_load_dwordx4 v1,s[36:39],s67 offen offset:1024 sc1 lds
v_mfma_f32_16x16x32_bf16 v[24:27],a[32:35],v[48:51],v[24:27]
s_cselect_b32 s87,s87,s64
ds_read_b128 v[44:47],v2 offset:44032
v_fma_f32 v64,v64,v89,v88
v_mfma_f32_16x16x32_bf16 v[28:31],a[36:39],v[48:51],v[28:31]
s_lshr_b32 s72,s67,6
v_fma_f32 v65,v65,v89,v88
v_fma_f32 v66,v66,v89,v88
v_mfma_f32_16x16x32_bf16 v[24:27],a[40:43],v[52:55],v[24:27]
s_lshr_b32 s71,s62,2
ds_read_b128 v[48:51],v2 offset:45056
v_fma_f32 v67,v67,v89,v88
v_mfma_f32_16x16x32_bf16 v[28:31],a[44:47],v[52:55],v[28:31]
buffer_load_dwordx4 v1,s[36:39],s67 offen offset:2048 sc1 lds
v_mfma_f32_16x16x32_bf16 v[24:27],a[48:51],v[56:59],v[24:27]
s_mov_b32 s62,s87
ds_read_b128 v[52:55],v2 offset:46080
v_fma_f32 v68,v68,v89,v88
v_mfma_f32_16x16x32_bf16 v[28:31],a[52:55],v[56:59],v[28:31]
s_mov_b64 s[44:45],s[46:47]
v_fma_f32 v69,v69,v89,v88
v_fma_f32 v70,v70,v89,v88
v_mfma_f32_16x16x32_bf16 v[24:27],a[56:59],v[60:63],v[24:27]
s_mov_b64 s[46:47],s[48:49]
ds_read_b128 v[56:59],v2 offset:47104
v_fma_f32 v71,v71,v89,v88
v_mfma_f32_16x16x32_bf16 v[28:31],a[60:63],v[60:63],v[28:31]
s_waitcnt lgkmcnt(3)
buffer_load_dwordx4 v1,s[36:39],s67 offen offset:3072 sc1 lds
v_mfma_f32_16x16x32_bf16 v[24:27],a[64:67],v[32:35],v[24:27]
s_waitcnt vmcnt(10)
ds_read_b128 v[60:63],v2 offset:48128
v_add_f32 v16,v16,v64
v_mfma_f32_16x16x32_bf16 v[28:31],a[68:71],v[32:35],v[28:31]
s_barrier
v_mfma_f32_16x16x32_bf16 v[24:27],a[72:75],v[36:39],v[24:27]
s_add_u32 m0,s71,s88
ds_read_b128 v[32:35],v2 offset:49152
v_add_f32 v17,v17,v65
v_mfma_f32_16x16x32_bf16 v[28:31],a[76:79],v[36:39],v[28:31]
s_mov_b64 s[48:49],s[50:51]
v_add_f32 v18,v18,v66
v_add_f32 v19,v19,v67
v_mfma_f32_16x16x32_bf16 v[24:27],a[80:83],v[40:43],v[24:27]
s_mov_b64 s[50:51],s[52:53]
ds_read_b128 v[36:39],v2 offset:50176
v_add_f32 v20,v20,v68
v_mfma_f32_16x16x32_bf16 v[28:31],a[84:87],v[40:43],v[28:31]
buffer_load_dwordx4 v10,s[36:39],s67 offen offset:0 sc1 lds
v_mfma_f32_16x16x32_bf16 v[24:27],a[88:91],v[44:47],v[24:27]
buffer_load_dword v11,s[36:39],s72 offen offset:1024 sc1 lds
v_mfma_f32_16x16x32_bf16 v[28:31],a[92:95],v[44:47],v[28:31]
s_waitcnt lgkmcnt(2)
ds_read_b128 v[40:43],v2 offset:51200
v_add_f32 v21,v21,v69
v_mfma_f32_16x16x32_bf16 v[24:27],a[96:99],v[48:51],v[24:27]
s_mov_b32 m0,s62
ds_read_b128 v[44:47],v2 offset:52224
v_add_f32 v22,v22,v70
v_mfma_f32_16x16x32_bf16 v[28:31],a[100:103],v[48:51],v[28:31]
v_add_f32 v23,v23,v71
v_cvt_pk_bf16_f32 v72,v16,v17
v_cvt_pk_bf16_f32 v73,v18,v19
v_cvt_pk_bf16_f32 v74,v20,v21
v_cvt_pk_bf16_f32 v75,v22,v23
v_mfma_f32_16x16x32_bf16 v[24:27],a[104:107],v[52:55],v[24:27]
buffer_store_dwordx4 v[72:75],v3,s[36:39],s73 offen offset:0 sc1
ds_read_b128 v[48:51],v2 offset:53248
v_add_f32 v64,v16,v17
v_mul_f32 v65,v16,v16
v_mfma_f32_16x16x32_bf16 v[28:31],a[108:111],v[52:55],v[28:31]
v_fma_f32 v65,v17,v17,v65
v_add_f32 v64,v64,v18
v_mfma_f32_16x16x32_bf16 v[24:27],a[112:115],v[56:59],v[24:27]
ds_read_b128 v[52:55],v2 offset:54272
v_fma_f32 v65,v18,v18,v65
v_mfma_f32_16x16x32_bf16 v[28:31],a[116:119],v[56:59],v[28:31]
v_add_f32 v64,v64,v19
v_fma_f32 v65,v19,v19,v65
v_add_f32 v64,v64,v20
v_fma_f32 v65,v20,v20,v65
v_add_f32 v64,v64,v21
v_fma_f32 v65,v21,v21,v65
v_add_f32 v64,v64,v22
v_fma_f32 v65,v22,v22,v65
v_add_f32 v64,v64,v23
v_fma_f32 v65,v23,v23,v65
v_mfma_f32_16x16x32_bf16 v[24:27],a[120:123],v[60:63],v[24:27]
s_nop 0
v_permlane16_swap_b32 v64,v65
v_add_f32 v64,v64,v65
v_mov_b32 v65,v64
v_mfma_f32_16x16x32_bf16 v[28:31],a[124:127],v[60:63],v[28:31]
s_waitcnt lgkmcnt(2)
ds_read_b128 v[56:59],v2 offset:55296
v_permlane32_swap_b32 v64,v65
v_add_f32 v64,v64,v65
v_mfma_f32_16x16x32_bf16 v[24:27],a[128:131],v[32:35],v[24:27]
ds_write_b32 v13,v64 offset:2048
v_mfma_f32_16x16x32_bf16 v[28:31],a[132:135],v[32:35],v[28:31]
ds_read_b128 v[60:63],v2 offset:56320
v_mfma_f32_16x16x32_bf16 v[24:27],a[136:139],v[36:39],v[24:27]
ds_read_b128 v[32:35],v2 offset:57344
v_mfma_f32_16x16x32_bf16 v[28:31],a[140:143],v[36:39],v[28:31]
v_mfma_f32_16x16x32_bf16 v[24:27],a[144:147],v[40:43],v[24:27]
s_waitcnt vmcnt(11) lgkmcnt(2))"
R"(
ds_read_b128 v[36:39],v2 offset:58368
v_mfma_f32_16x16x32_bf16 v[28:31],a[148:151],v[40:43],v[28:31]
s_barrier
v_mfma_f32_16x16x32_bf16 v[24:27],a[152:155],v[44:47],v[24:27]
ds_read_b128 v[40:43],v2 offset:59392
ds_read_b32 v15,v7 offset:256
v_mfma_f32_16x16x32_bf16 v[28:31],a[156:159],v[44:47],v[28:31]
s_waitcnt lgkmcnt(4)
ds_read_b128 v[76:79],v14 offset:2048
v_mfma_f32_16x16x32_bf16 v[24:27],a[160:163],v[48:51],v[24:27]
ds_read_b128 v[44:47],v2 offset:60416
v_mfma_f32_16x16x32_bf16 v[28:31],a[164:167],v[48:51],v[28:31]
v_mfma_f32_16x16x32_bf16 v[24:27],a[168:171],v[52:55],v[24:27]
s_waitcnt lgkmcnt(2)
ds_read_b128 v[48:51],v2 offset:61440
v_mfma_f32_16x16x32_bf16 v[28:31],a[172:175],v[52:55],v[28:31]
s_waitcnt lgkmcnt(2)
v_cmp_eq_u32 vcc,s60,v15
v_add_f32 v76,v76,v77
v_add_f32 v78,v78,v79
v_add_f32 v76,v76,v78
v_mfma_f32_16x16x32_bf16 v[24:27],a[176:179],v[56:59],v[24:27]
buffer_store_dword v76,v12,s[36:39],s85 offen offset:0 sc1
ds_read_b128 v[52:55],v2 offset:62464
v_mfma_f32_16x16x32_bf16 v[28:31],a[180:183],v[56:59],v[28:31]
s_cmp_eq_u32 vcc_lo,-1
s_cselect_b32 s66,s86,s59
s_xor_b32 s64,s64,65536
v_mfma_f32_16x16x32_bf16 v[24:27],a[184:187],v[60:63],v[24:27]
buffer_load_dwordx4 v0,s[36:39],s66 offen offset:0 sc1 lds
v_mfma_f32_16x16x32_bf16 v[28:31],a[188:191],v[60:63],v[28:31]
s_waitcnt lgkmcnt(2)
ds_read_b128 v[56:59],v2 offset:63488
s_add_u32 s74,s53,s55
v_mfma_f32_16x16x32_bf16 v[24:27],a[192:195],v[32:35],v[24:27]
s_waitcnt vmcnt(9)
ds_read_b128 v[60:63],v2 offset:64512
v_xor_b32 v2,65536,v2
v_mfma_f32_16x16x32_bf16 v[28:31],a[196:199],v[32:35],v[28:31]
s_barrier
v_mfma_f32_16x16x32_bf16 v[24:27],a[200:203],v[36:39],v[24:27]
s_cmp_eq_u32 s74,s56
ds_read_b128 v[32:35],v2 offset:0
ds_read_b128 v[84:87],v8 offset:8192
v_mfma_f32_16x16x32_bf16 v[28:31],a[204:207],v[36:39],v[28:31]
s_cselect_b32 s74,s57,s74
ds_read_b64 v[90:91],v9 offset:9216
v_xor_b32 v8,16384,v8
v_mfma_f32_16x16x32_bf16 v[24:27],a[208:211],v[40:43],v[24:27]
buffer_load_dwordx4 v0,s[36:39],s66 offen offset:1024 sc1 lds
v_mfma_f32_16x16x32_bf16 v[28:31],a[212:215],v[40:43],v[28:31]
s_cmp_eq_u32 s74,s58
ds_read_b128 v[36:39],v2 offset:1024
v_xor_b32 v9,16384,v9
v_mfma_f32_16x16x32_bf16 v[24:27],a[216:219],v[44:47],v[24:27]
s_cselect_b32 s74,s59,s74
ds_read_b128 v[40:43],v2 offset:2048
s_mov_b32 s52,s74
v_mfma_f32_16x16x32_bf16 v[28:31],a[220:223],v[44:47],v[28:31]
s_waitcnt lgkmcnt(5)
s_cmp_lt_u32 s67,s59
v_mfma_f32_16x16x32_bf16 v[24:27],a[224:227],v[48:51],v[24:27]
buffer_load_dwordx4 v0,s[36:39],s66 offen offset:2048 sc1 lds
v_mfma_f32_16x16x32_bf16 v[28:31],a[228:231],v[48:51],v[28:31]
s_waitcnt lgkmcnt(2)
ds_read_b128 v[44:47],v2 offset:3072
v_mfma_f32_16x16x32_bf16 v[24:27],a[232:235],v[52:55],v[24:27]
ds_read_b128 v[48:51],v2 offset:4096
v_mfma_f32_16x16x32_bf16 v[28:31],a[236:239],v[52:55],v[28:31]
v_mfma_f32_16x16x32_bf16 v[24:27],a[240:243],v[56:59],v[24:27]
buffer_load_dwordx4 v0,s[36:39],s66 offen offset:3072 sc1 lds
v_mfma_f32_16x16x32_bf16 v[28:31],a[244:247],v[56:59],v[28:31]
ds_read_b128 v[52:55],v2 offset:5120
v_mfma_f32_16x16x32_bf16 v[24:27],a[248:251],v[60:63],v[24:27]
s_waitcnt lgkmcnt(2)
ds_read_b128 v[56:59],v2 offset:6144
v_mfma_f32_16x16x32_bf16 v[28:31],a[252:255],v[60:63],v[28:31]
s_cbranch_scc1 Lg2_d0w2_%=
s_branch Lg2_slow0_%=
Lg2_d0w3_%=:
v_mfma_f32_16x16x32_bf16 v[16:19],a[0:3],v[32:35],0
s_add_u32 m0,s62,16384
ds_read_b128 v[60:63],v2 offset:7168
v_lshlrev_b32 v64,16,v84
v_mfma_f32_16x16x32_bf16 v[20:23],a[4:7],v[32:35],0
s_lshr_b32 s73,s47,0
v_and_b32 v65,0xffff0000,v84
v_lshlrev_b32 v66,16,v85
v_mfma_f32_16x16x32_bf16 v[16:19],a[8:11],v[36:39],v[16:19]
s_xor_b32 s87,s64,65536
ds_read_b128 v[32:35],v2 offset:8192
v_and_b32 v67,0xffff0000,v85
v_mfma_f32_16x16x32_bf16 v[20:23],a[12:15],v[36:39],v[20:23]
s_cmp_ge_u32 s66,s59
v_lshlrev_b32 v68,16,v86
v_and_b32 v69,0xffff0000,v86
v_mfma_f32_16x16x32_bf16 v[16:19],a[16:19],v[40:43],v[16:19]
buffer_load_dwordx4 v1,s[36:39],s66 offen offset:0 sc1 lds
v_mfma_f32_16x16x32_bf16 v[20:23],a[20:23],v[40:43],v[20:23]
s_cselect_b32 s86,s49,s50
ds_read_b128 v[36:39],v2 offset:9216
v_lshlrev_b32 v70,16,v87
v_mfma_f32_16x16x32_bf16 v[16:19],a[24:27],v[44:47],v[16:19]
s_cselect_b32 s87,s87,s63
ds_read_b128 v[40:43],v2 offset:10240
v_and_b32 v71,0xffff0000,v87
v_mfma_f32_16x16x32_bf16 v[20:23],a[28:31],v[44:47],v[20:23]
s_waitcnt lgkmcnt(3)
v_fma_f32 v64,v64,v91,v90
v_fma_f32 v65,v65,v91,v90
v_mfma_f32_16x16x32_bf16 v[16:19],a[32:35],v[48:51],v[16:19]
buffer_load_dwordx4 v1,s[36:39],s66 offen offset:1024 sc1 lds
v_mfma_f32_16x16x32_bf16 v[20:23],a[36:39],v[48:51],v[20:23]
s_lshr_b32 s72,s66,6
ds_read_b128 v[44:47],v2 offset:11264
v_fma_f32 v66,v66,v91,v90
v_mfma_f32_16x16x32_bf16 v[16:19],a[40:43],v[52:55],v[16:19]
s_lshr_b32 s71,s62,2
ds_read_b128 v[48:51],v2 offset:12288
v_fma_f32 v67,v67,v91,v90
v_mfma_f32_16x16x32_bf16 v[20:23],a[44:47],v[52:55],v[20:23]
s_mov_b32 s62,s87
v_fma_f32 v68,v68,v91,v90
v_fma_f32 v69,v69,v91,v90
v_mfma_f32_16x16x32_bf16 v[16:19],a[48:51],v[56:59],v[16:19]
buffer_load_dwordx4 v1,s[36:39],s66 offen offset:2048 sc1 lds
v_mfma_f32_16x16x32_bf16 v[20:23],a[52:55],v[56:59],v[20:23]
ds_read_b128 v[52:55],v2 offset:13312
v_fma_f32 v70,v70,v91,v90
v_mfma_f32_16x16x32_bf16 v[16:19],a[56:59],v[60:63],v[16:19]
ds_read_b128 v[56:59],v2 offset:14336
v_fma_f32 v71,v71,v91,v90
v_mfma_f32_16x16x32_bf16 v[20:23],a[60:63],v[60:63],v[20:23]
s_waitcnt lgkmcnt(3)
v_add_f32 v24,v24,v64
v_add_f32 v25,v25,v65
v_mfma_f32_16x16x32_bf16 v[16:19],a[64:67],v[32:35],v[16:19]
s_waitcnt vmcnt(9)
ds_read_b128 v[60:63],v2 offset:15360
v_add_f32 v26,v26,v66
v_mfma_f32_16x16x32_bf16 v[20:23],a[68:71],v[32:35],v[20:23]
s_barrier
v_mfma_f32_16x16x32_bf16 v[16:19],a[72:75],v[36:39],v[16:19]
buffer_load_dwordx4 v1,s[36:39],s66 offen offset:3072 sc1 lds
v_mfma_f32_16x16x32_bf16 v[20:23],a[76:79],v[36:39],v[20:23]
s_add_u32 m0,s71,s88
ds_read_b128 v[32:35],v2 offset:16384
v_add_f32 v27,v27,v67
v_mfma_f32_16x16x32_bf16 v[16:19],a[80:83],v[40:43],v[16:19]
s_lshr_b32 s70,s46,5
ds_read_b128 v[36:39],v2 offset:17408
v_add_f32 v28,v28,v68
v_mfma_f32_16x16x32_bf16 v[20:23],a[84:87],v[40:43],v[20:23]
v_add_f32 v29,v29,v69
v_add_f32 v30,v30,v70
v_mfma_f32_16x16x32_bf16 v[16:19],a[88:91],v[44:47],v[16:19]
buffer_load_dwordx4 v10,s[36:39],s66 offen offset:0 sc1 lds
v_mfma_f32_16x16x32_bf16 v[20:23],a[92:95],v[44:47],v[20:23]
s_waitcnt lgkmcnt(2)
ds_read_b128 v[40:43],v2 offset:18432
v_add_f32 v31,v31,v71
v_mfma_f32_16x16x32_bf16 v[16:19],a[96:99],v[48:51],v[16:19]
buffer_load_dword v11,s[36:39],s72 offen offset:1024 sc1 lds
v_mfma_f32_16x16x32_bf16 v[20:23],a[100:103],v[48:51],v[20:23]
s_mov_b32 m0,s62
ds_read_b128 v[44:47],v2 offset:19456
v_cvt_pk_bf16_f32 v72,v24,v25
v_mfma_f32_16x16x32_bf16 v[16:19],a[104:107],v[52:55],v[16:19]
v_cvt_pk_bf16_f32 v73,v26,v27
v_cvt_pk_bf16_f32 v74,v28,v29
v_cvt_pk_bf16_f32 v75,v30,v31
v_mfma_f32_16x16x32_bf16 v[20:23],a[108:111],v[52:55],v[20:23]
ds_read_b128 v[48:51],v2 offset:20480
buffer_store_dwordx4 v[72:75],v3,s[36:39],s73 offen offset:0 sc1
v_add_f32 v64,v24,v25
v_mul_f32 v65,v24,v24
v_fma_f32 v65,v25,v25,v65
v_mfma_f32_16x16x32_bf16 v[16:19],a[112:115],v[56:59],v[16:19]
ds_read_b128 v[52:55],v2 offset:21504
v_add_f32 v64,v64,v26
v_mfma_f32_16x16x32_bf16 v[20:23],a[116:119],v[56:59],v[20:23]
v_fma_f32 v65,v26,v26,v65
v_add_f32 v64,v64,v27
v_fma_f32 v65,v27,v27,v65
v_add_f32 v64,v64,v28
v_fma_f32 v65,v28,v28,v65
v_add_f32 v64,v64,v29
v_fma_f32 v65,v29,v29,v65
v_add_f32 v64,v64,v30
v_fma_f32 v65,v30,v30,v65
v_add_f32 v64,v64,v31
v_fma_f32 v65,v31,v31,v65
v_mfma_f32_16x16x32_bf16 v[16:19],a[120:123],v[60:63],v[16:19]
s_nop 0
v_permlane16_swap_b32 v64,v65
v_add_f32 v64,v64,v65
v_mov_b32 v65,v64
v_mfma_f32_16x16x32_bf16 v[20:23],a[124:127],v[60:63],v[20:23]
s_waitcnt lgkmcnt(2)
ds_read_b128 v[56:59],v2 offset:22528)"
R"(
v_permlane32_swap_b32 v64,v65
v_add_f32 v64,v64,v65
v_mfma_f32_16x16x32_bf16 v[16:19],a[128:131],v[32:35],v[16:19]
ds_write_b32 v13,v64 offset:0
v_mfma_f32_16x16x32_bf16 v[20:23],a[132:135],v[32:35],v[20:23]
ds_read_b128 v[60:63],v2 offset:23552
v_mfma_f32_16x16x32_bf16 v[16:19],a[136:139],v[36:39],v[16:19]
ds_read_b128 v[32:35],v2 offset:24576
v_mfma_f32_16x16x32_bf16 v[20:23],a[140:143],v[36:39],v[20:23]
v_mfma_f32_16x16x32_bf16 v[16:19],a[144:147],v[40:43],v[16:19]
s_waitcnt vmcnt(12) lgkmcnt(2)
ds_read_b128 v[36:39],v2 offset:25600
v_mfma_f32_16x16x32_bf16 v[20:23],a[148:151],v[40:43],v[20:23]
s_barrier
v_mfma_f32_16x16x32_bf16 v[16:19],a[152:155],v[44:47],v[16:19]
buffer_store_dword v5,v4,s[36:39],s70 offen offset:0 sc1
ds_read_b128 v[40:43],v2 offset:26624
ds_read_b32 v15,v7 offset:0
v_mfma_f32_16x16x32_bf16 v[20:23],a[156:159],v[44:47],v[20:23]
s_waitcnt lgkmcnt(4)
v_mfma_f32_16x16x32_bf16 v[16:19],a[160:163],v[48:51],v[16:19]
ds_read_b128 v[44:47],v2 offset:27648
v_mfma_f32_16x16x32_bf16 v[20:23],a[164:167],v[48:51],v[20:23]
v_mfma_f32_16x16x32_bf16 v[16:19],a[168:171],v[52:55],v[16:19]
s_waitcnt lgkmcnt(1)
ds_read_b128 v[48:51],v2 offset:28672
v_mfma_f32_16x16x32_bf16 v[20:23],a[172:175],v[52:55],v[20:23]
v_cmp_eq_u32 vcc,s60,v15
v_mfma_f32_16x16x32_bf16 v[16:19],a[176:179],v[56:59],v[16:19]
s_cmp_eq_u32 vcc_lo,-1
ds_read_b128 v[52:55],v2 offset:29696
s_cselect_b32 s67,s86,s59
v_mfma_f32_16x16x32_bf16 v[20:23],a[180:183],v[56:59],v[20:23]
s_xor_b32 s63,s63,65536
s_add_u32 s53,s52,s55
s_cmp_eq_u32 s53,s56
v_mfma_f32_16x16x32_bf16 v[16:19],a[184:187],v[60:63],v[16:19]
s_cselect_b32 s53,s57,s53
ds_read_b128 v[56:59],v2 offset:30720
s_cmp_eq_u32 s53,s58
v_mfma_f32_16x16x32_bf16 v[20:23],a[188:191],v[60:63],v[20:23]
s_waitcnt lgkmcnt(3)
buffer_load_dwordx4 v0,s[36:39],s67 offen offset:0 sc1 lds
v_mfma_f32_16x16x32_bf16 v[16:19],a[192:195],v[32:35],v[16:19]
s_waitcnt vmcnt(9)
ds_read_b128 v[60:63],v2 offset:31744
s_cselect_b32 s53,s59,s53
v_mfma_f32_16x16x32_bf16 v[20:23],a[196:199],v[32:35],v[20:23]
s_barrier
v_mfma_f32_16x16x32_bf16 v[16:19],a[200:203],v[36:39],v[16:19]
s_cmp_ge_u32 s66,s59
ds_read_b128 v[32:35],v2 offset:32768
ds_read_b128 v[80:83],v8 offset:0
v_mfma_f32_16x16x32_bf16 v[20:23],a[204:207],v[36:39],v[20:23]
ds_read_b64 v[88:89],v9 offset:1024
v_mfma_f32_16x16x32_bf16 v[16:19],a[208:211],v[40:43],v[16:19]
ds_read_b128 v[36:39],v2 offset:33792
v_mfma_f32_16x16x32_bf16 v[20:23],a[212:215],v[40:43],v[20:23]
buffer_load_dwordx4 v0,s[36:39],s67 offen offset:1024 sc1 lds
v_mfma_f32_16x16x32_bf16 v[16:19],a[216:219],v[44:47],v[16:19]
ds_read_b128 v[40:43],v2 offset:34816
v_mfma_f32_16x16x32_bf16 v[20:23],a[220:223],v[44:47],v[20:23]
s_waitcnt lgkmcnt(5)
v_mfma_f32_16x16x32_bf16 v[16:19],a[224:227],v[48:51],v[16:19]
ds_read_b128 v[44:47],v2 offset:35840
v_mfma_f32_16x16x32_bf16 v[20:23],a[228:231],v[48:51],v[20:23]
s_waitcnt lgkmcnt(3)
buffer_load_dwordx4 v0,s[36:39],s67 offen offset:2048 sc1 lds
v_mfma_f32_16x16x32_bf16 v[16:19],a[232:235],v[52:55],v[16:19]
ds_read_b128 v[48:51],v2 offset:36864
v_mfma_f32_16x16x32_bf16 v[20:23],a[236:239],v[52:55],v[20:23]
v_mfma_f32_16x16x32_bf16 v[16:19],a[240:243],v[56:59],v[16:19]
ds_read_b128 v[52:55],v2 offset:37888
v_mfma_f32_16x16x32_bf16 v[20:23],a[244:247],v[56:59],v[20:23]
buffer_load_dwordx4 v0,s[36:39],s67 offen offset:3072 sc1 lds
v_mfma_f32_16x16x32_bf16 v[16:19],a[248:251],v[60:63],v[16:19]
s_waitcnt lgkmcnt(2)
ds_read_b128 v[56:59],v2 offset:38912
v_mfma_f32_16x16x32_bf16 v[20:23],a[252:255],v[60:63],v[20:23]
s_cbranch_scc1 Lg2_slow1_%=
Lg2_d1w3_%=:
v_mfma_f32_16x16x32_bf16 v[24:27],a[0:3],v[32:35],0
s_add_u32 m0,s62,16384
ds_read_b128 v[60:63],v2 offset:39936
v_lshlrev_b32 v64,16,v80
v_mfma_f32_16x16x32_bf16 v[28:31],a[4:7],v[32:35],0
s_lshr_b32 s73,s48,0
v_and_b32 v65,0xffff0000,v80
v_lshlrev_b32 v66,16,v81
v_mfma_f32_16x16x32_bf16 v[24:27],a[8:11],v[36:39],v[24:27]
s_xor_b32 s87,s63,65536
ds_read_b128 v[32:35],v2 offset:40960
v_and_b32 v67,0xffff0000,v81
v_mfma_f32_16x16x32_bf16 v[28:31],a[12:15],v[36:39],v[28:31]
s_cmp_ge_u32 s67,s59
v_lshlrev_b32 v68,16,v82
v_and_b32 v69,0xffff0000,v82
v_mfma_f32_16x16x32_bf16 v[24:27],a[16:19],v[40:43],v[24:27]
buffer_load_dwordx4 v1,s[36:39],s67 offen offset:0 sc1 lds
v_mfma_f32_16x16x32_bf16 v[28:31],a[20:23],v[40:43],v[28:31]
s_cselect_b32 s86,s50,s51
ds_read_b128 v[36:39],v2 offset:41984
v_lshlrev_b32 v70,16,v83
v_mfma_f32_16x16x32_bf16 v[24:27],a[24:27],v[44:47],v[24:27]
s_cselect_b32 s87,s87,s64
ds_read_b128 v[40:43],v2 offset:43008
v_and_b32 v71,0xffff0000,v83
v_mfma_f32_16x16x32_bf16 v[28:31],a[28:31],v[44:47],v[28:31]
s_waitcnt lgkmcnt(3)
v_fma_f32 v64,v64,v89,v88
v_fma_f32 v65,v65,v89,v88
v_mfma_f32_16x16x32_bf16 v[24:27],a[32:35],v[48:51],v[24:27]
buffer_load_dwordx4 v1,s[36:39],s67 offen offset:1024 sc1 lds
v_mfma_f32_16x16x32_bf16 v[28:31],a[36:39],v[48:51],v[28:31]
s_lshr_b32 s72,s67,6
ds_read_b128 v[44:47],v2 offset:44032
v_fma_f32 v66,v66,v89,v88
v_mfma_f32_16x16x32_bf16 v[24:27],a[40:43],v[52:55],v[24:27]
s_lshr_b32 s71,s62,2
ds_read_b128 v[48:51],v2 offset:45056
v_fma_f32 v67,v67,v89,v88
v_mfma_f32_16x16x32_bf16 v[28:31],a[44:47],v[52:55],v[28:31]
s_mov_b32 s62,s87
v_fma_f32 v68,v68,v89,v88
v_fma_f32 v69,v69,v89,v88
v_mfma_f32_16x16x32_bf16 v[24:27],a[48:51],v[56:59],v[24:27]
buffer_load_dwordx4 v1,s[36:39],s67 offen offset:2048 sc1 lds
v_mfma_f32_16x16x32_bf16 v[28:31],a[52:55],v[56:59],v[28:31]
s_mov_b64 s[44:45],s[46:47]
ds_read_b128 v[52:55],v2 offset:46080
v_fma_f32 v70,v70,v89,v88
v_mfma_f32_16x16x32_bf16 v[24:27],a[56:59],v[60:63],v[24:27]
ds_read_b128 v[56:59],v2 offset:47104
v_fma_f32 v71,v71,v89,v88
v_mfma_f32_16x16x32_bf16 v[28:31],a[60:63],v[60:63],v[28:31]
s_waitcnt lgkmcnt(3)
v_add_f32 v16,v16,v64
v_add_f32 v17,v17,v65
v_mfma_f32_16x16x32_bf16 v[24:27],a[64:67],v[32:35],v[24:27]
s_waitcnt vmcnt(9)
ds_read_b128 v[60:63],v2 offset:48128
v_add_f32 v18,v18,v66
v_mfma_f32_16x16x32_bf16 v[28:31],a[68:71],v[32:35],v[28:31]
s_barrier
v_mfma_f32_16x16x32_bf16 v[24:27],a[72:75],v[36:39],v[24:27]
buffer_load_dwordx4 v1,s[36:39],s67 offen offset:3072 sc1 lds
v_mfma_f32_16x16x32_bf16 v[28:31],a[76:79],v[36:39],v[28:31]
s_add_u32 m0,s71,s88
ds_read_b128 v[32:35],v2 offset:49152
v_add_f32 v19,v19,v67
v_mfma_f32_16x16x32_bf16 v[24:27],a[80:83],v[40:43],v[24:27]
s_lshr_b32 s70,s47,5
ds_read_b128 v[36:39],v2 offset:50176
v_add_f32 v20,v20,v68
v_mfma_f32_16x16x32_bf16 v[28:31],a[84:87],v[40:43],v[28:31]
s_mov_b64 s[46:47],s[48:49]
v_add_f32 v21,v21,v69
v_add_f32 v22,v22,v70
v_mfma_f32_16x16x32_bf16 v[24:27],a[88:91],v[44:47],v[24:27]
buffer_load_dwordx4 v10,s[36:39],s67 offen offset:0 sc1 lds
v_mfma_f32_16x16x32_bf16 v[28:31],a[92:95],v[44:47],v[28:31]
s_waitcnt lgkmcnt(2)
ds_read_b128 v[40:43],v2 offset:51200
v_add_f32 v23,v23,v71
v_mfma_f32_16x16x32_bf16 v[24:27],a[96:99],v[48:51],v[24:27]
buffer_load_dword v11,s[36:39],s72 offen offset:1024 sc1 lds
v_mfma_f32_16x16x32_bf16 v[28:31],a[100:103],v[48:51],v[28:31]
s_mov_b32 m0,s62
ds_read_b128 v[44:47],v2 offset:52224
v_cvt_pk_bf16_f32 v72,v16,v17
v_mfma_f32_16x16x32_bf16 v[24:27],a[104:107],v[52:55],v[24:27]
s_mov_b64 s[48:49],s[50:51]
v_cvt_pk_bf16_f32 v73,v18,v19
v_cvt_pk_bf16_f32 v74,v20,v21
v_cvt_pk_bf16_f32 v75,v22,v23
v_mfma_f32_16x16x32_bf16 v[28:31],a[108:111],v[52:55],v[28:31]
s_mov_b64 s[50:51],s[52:53]
ds_read_b128 v[48:51],v2 offset:53248
buffer_store_dwordx4 v[72:75],v3,s[36:39],s73 offen offset:0 sc1
v_add_f32 v64,v16,v17
v_mul_f32 v65,v16,v16
v_fma_f32 v65,v17,v17,v65
v_mfma_f32_16x16x32_bf16 v[24:27],a[112:115],v[56:59],v[24:27]
ds_read_b128 v[52:55],v2 offset:54272
v_add_f32 v64,v64,v18
v_mfma_f32_16x16x32_bf16 v[28:31],a[116:119],v[56:59],v[28:31]
v_fma_f32 v65,v18,v18,v65
v_add_f32 v64,v64,v19
v_fma_f32 v65,v19,v19,v65
v_add_f32 v64,v64,v20
v_fma_f32 v65,v20,v20,v65
v_add_f32 v64,v64,v21
v_fma_f32 v65,v21,v21,v65
v_add_f32 v64,v64,v22
v_fma_f32 v65,v22,v22,v65
v_add_f32 v64,v64,v23
v_fma_f32 v65,v23,v23,v65
v_mfma_f32_16x16x32_bf16 v[24:27],a[120:123],v[60:63],v[24:27]
s_nop 0
v_permlane16_swap_b32 v64,v65)"
R"(
v_add_f32 v64,v64,v65
v_mov_b32 v65,v64
v_mfma_f32_16x16x32_bf16 v[28:31],a[124:127],v[60:63],v[28:31]
s_waitcnt lgkmcnt(2)
ds_read_b128 v[56:59],v2 offset:55296
v_permlane32_swap_b32 v64,v65
v_add_f32 v64,v64,v65
v_mfma_f32_16x16x32_bf16 v[24:27],a[128:131],v[32:35],v[24:27]
ds_write_b32 v13,v64 offset:2048
v_mfma_f32_16x16x32_bf16 v[28:31],a[132:135],v[32:35],v[28:31]
ds_read_b128 v[60:63],v2 offset:56320
v_mfma_f32_16x16x32_bf16 v[24:27],a[136:139],v[36:39],v[24:27]
ds_read_b128 v[32:35],v2 offset:57344
v_mfma_f32_16x16x32_bf16 v[28:31],a[140:143],v[36:39],v[28:31]
v_mfma_f32_16x16x32_bf16 v[24:27],a[144:147],v[40:43],v[24:27]
s_waitcnt vmcnt(12) lgkmcnt(2)
ds_read_b128 v[36:39],v2 offset:58368
v_mfma_f32_16x16x32_bf16 v[28:31],a[148:151],v[40:43],v[28:31]
s_barrier
v_mfma_f32_16x16x32_bf16 v[24:27],a[152:155],v[44:47],v[24:27]
buffer_store_dword v5,v4,s[36:39],s70 offen offset:0 sc1
ds_read_b128 v[40:43],v2 offset:59392
ds_read_b32 v15,v7 offset:256
v_mfma_f32_16x16x32_bf16 v[28:31],a[156:159],v[44:47],v[28:31]
s_waitcnt lgkmcnt(4)
v_mfma_f32_16x16x32_bf16 v[24:27],a[160:163],v[48:51],v[24:27]
ds_read_b128 v[44:47],v2 offset:60416
v_mfma_f32_16x16x32_bf16 v[28:31],a[164:167],v[48:51],v[28:31]
v_mfma_f32_16x16x32_bf16 v[24:27],a[168:171],v[52:55],v[24:27]
s_waitcnt lgkmcnt(1)
ds_read_b128 v[48:51],v2 offset:61440
v_mfma_f32_16x16x32_bf16 v[28:31],a[172:175],v[52:55],v[28:31]
v_cmp_eq_u32 vcc,s60,v15
v_mfma_f32_16x16x32_bf16 v[24:27],a[176:179],v[56:59],v[24:27]
s_cmp_eq_u32 vcc_lo,-1
ds_read_b128 v[52:55],v2 offset:62464
s_cselect_b32 s66,s86,s59
v_mfma_f32_16x16x32_bf16 v[28:31],a[180:183],v[56:59],v[28:31]
s_xor_b32 s64,s64,65536
s_add_u32 s74,s53,s55
s_cmp_eq_u32 s74,s56
v_mfma_f32_16x16x32_bf16 v[24:27],a[184:187],v[60:63],v[24:27]
s_cselect_b32 s74,s57,s74
ds_read_b128 v[56:59],v2 offset:63488
s_cmp_eq_u32 s74,s58
v_mfma_f32_16x16x32_bf16 v[28:31],a[188:191],v[60:63],v[28:31]
s_waitcnt lgkmcnt(3)
buffer_load_dwordx4 v0,s[36:39],s66 offen offset:0 sc1 lds
v_mfma_f32_16x16x32_bf16 v[24:27],a[192:195],v[32:35],v[24:27]
s_waitcnt vmcnt(9)
ds_read_b128 v[60:63],v2 offset:64512
v_xor_b32 v2,65536,v2
v_mfma_f32_16x16x32_bf16 v[28:31],a[196:199],v[32:35],v[28:31]
s_barrier
v_mfma_f32_16x16x32_bf16 v[24:27],a[200:203],v[36:39],v[24:27]
s_cselect_b32 s74,s59,s74
ds_read_b128 v[32:35],v2 offset:0
ds_read_b128 v[84:87],v8 offset:8192
v_mfma_f32_16x16x32_bf16 v[28:31],a[204:207],v[36:39],v[28:31]
s_mov_b32 s52,s74
ds_read_b64 v[90:91],v9 offset:9216
v_xor_b32 v8,16384,v8
v_mfma_f32_16x16x32_bf16 v[24:27],a[208:211],v[40:43],v[24:27]
s_cmp_lt_u32 s67,s59
ds_read_b128 v[36:39],v2 offset:1024
v_xor_b32 v9,16384,v9
v_mfma_f32_16x16x32_bf16 v[28:31],a[212:215],v[40:43],v[28:31]
buffer_load_dwordx4 v0,s[36:39],s66 offen offset:1024 sc1 lds
v_mfma_f32_16x16x32_bf16 v[24:27],a[216:219],v[44:47],v[24:27]
ds_read_b128 v[40:43],v2 offset:2048
v_mfma_f32_16x16x32_bf16 v[28:31],a[220:223],v[44:47],v[28:31]
s_waitcnt lgkmcnt(5)
v_mfma_f32_16x16x32_bf16 v[24:27],a[224:227],v[48:51],v[24:27]
ds_read_b128 v[44:47],v2 offset:3072
v_mfma_f32_16x16x32_bf16 v[28:31],a[228:231],v[48:51],v[28:31]
s_waitcnt lgkmcnt(3)
buffer_load_dwordx4 v0,s[36:39],s66 offen offset:2048 sc1 lds
v_mfma_f32_16x16x32_bf16 v[24:27],a[232:235],v[52:55],v[24:27]
ds_read_b128 v[48:51],v2 offset:4096
v_mfma_f32_16x16x32_bf16 v[28:31],a[236:239],v[52:55],v[28:31]
v_mfma_f32_16x16x32_bf16 v[24:27],a[240:243],v[56:59],v[24:27]
ds_read_b128 v[52:55],v2 offset:5120
v_mfma_f32_16x16x32_bf16 v[28:31],a[244:247],v[56:59],v[28:31]
buffer_load_dwordx4 v0,s[36:39],s66 offen offset:3072 sc1 lds
v_mfma_f32_16x16x32_bf16 v[24:27],a[248:251],v[60:63],v[24:27]
s_waitcnt lgkmcnt(2)
ds_read_b128 v[56:59],v2 offset:6144
v_mfma_f32_16x16x32_bf16 v[28:31],a[252:255],v[60:63],v[28:31]
s_cbranch_scc1 Lg2_d0w3_%=
s_branch Lg2_slow0_%=
Lg2_slow0_%=:
s_cmp_lt_u32 s66,s59
s_cbranch_scc1 Lg2_gs0_%=
Lg2_slowb0_%=:
s_cmp_eq_u32 s54,1
s_cselect_b32 s68,s49,s48
s_lshr_b32 s68,s68,5
s_add_u32 m0,s78,8192
s_nop 0
buffer_load_dword v6,s[36:39],s68 offen offset:0 sc1 lds
s_lshr_b32 s73,s47,0
s_lshr_b32 s85,s47,5
v_lshlrev_b32 v64,16,v84
v_and_b32 v65,0xffff0000,v84
v_lshlrev_b32 v66,16,v85
v_and_b32 v67,0xffff0000,v85
v_lshlrev_b32 v68,16,v86
v_and_b32 v69,0xffff0000,v86
v_lshlrev_b32 v70,16,v87
v_and_b32 v71,0xffff0000,v87
v_fma_f32 v64,v64,v91,v90
v_fma_f32 v65,v65,v91,v90
v_fma_f32 v66,v66,v91,v90
v_fma_f32 v67,v67,v91,v90
v_fma_f32 v68,v68,v91,v90
v_fma_f32 v69,v69,v91,v90
v_fma_f32 v70,v70,v91,v90
v_fma_f32 v71,v71,v91,v90
v_add_f32 v24,v24,v64
v_add_f32 v25,v25,v65
v_add_f32 v26,v26,v66
v_add_f32 v27,v27,v67
v_add_f32 v28,v28,v68
v_add_f32 v29,v29,v69
v_add_f32 v30,v30,v70
v_add_f32 v31,v31,v71
v_cvt_pk_bf16_f32 v72,v24,v25
v_cvt_pk_bf16_f32 v73,v26,v27
v_cvt_pk_bf16_f32 v74,v28,v29
v_cvt_pk_bf16_f32 v75,v30,v31
s_add_u32 m0,s78,8448
s_nop 0
buffer_load_dword v6,s[36:39],s68 offen offset:0 sc1 lds
buffer_store_dwordx4 v[72:75],v3,s[36:39],s73 offen offset:0 sc1
v_add_f32 v64,v24,v25
v_mul_f32 v65,v24,v24
v_fma_f32 v65,v25,v25,v65
v_add_f32 v64,v64,v26
v_fma_f32 v65,v26,v26,v65
v_add_f32 v64,v64,v27
v_fma_f32 v65,v27,v27,v65
v_add_f32 v64,v64,v28
v_fma_f32 v65,v28,v28,v65
v_add_f32 v64,v64,v29
v_fma_f32 v65,v29,v29,v65
v_add_f32 v64,v64,v30
v_fma_f32 v65,v30,v30,v65
v_add_f32 v64,v64,v31
v_fma_f32 v65,v31,v31,v65
s_nop 1
v_permlane16_swap_b32 v64,v65
v_add_f32 v64,v64,v65
v_mov_b32 v65,v64
s_nop 1
v_permlane32_swap_b32 v64,v65
v_add_f32 v64,v64,v65
ds_write_b32 v13,v64 offset:0
s_waitcnt lgkmcnt(0)
s_barrier
ds_read_b128 v[76:79],v14 offset:0
s_waitcnt lgkmcnt(0)
v_add_f32 v76,v76,v77
v_add_f32 v78,v78,v79
v_add_f32 v76,v76,v78
buffer_store_dword v76,v12,s[36:39],s85 offen offset:0 sc1
s_cmp_ge_u32 s48,s59
s_cbranch_scc1 Lg2_done0_%=
s_add_u32 m0,s78,16384
s_nop 0
buffer_load_dword v6,s[36:39],s68 offen offset:0 sc1 lds
s_waitcnt vmcnt(4)
s_barrier
ds_read_b32 v15,v7 offset:8192
s_lshr_b32 s70,s46,5
buffer_store_dword v5,v4,s[36:39],s70 offen offset:0 sc1
s_mov_b32 s46,s59
s_mov_b32 s69,18432
s_waitcnt lgkmcnt(0)
v_cmp_eq_u32 vcc,s60,v15
s_nop 0
s_cmp_eq_u32 vcc_lo,-1
s_cbranch_scc1 Lg2_go0_%=
s_add_u32 m0,s78,16640
s_nop 0
buffer_load_dword v6,s[36:39],s68 offen offset:0 sc1 lds
s_waitcnt vmcnt(5)
s_barrier
ds_read_b32 v15,v7 offset:8448
s_mov_b32 s69,18688
s_waitcnt lgkmcnt(0)
v_cmp_eq_u32 vcc,s60,v15
s_nop 0
s_cmp_eq_u32 vcc_lo,-1
s_cbranch_scc1 Lg2_go0_%=
s_add_u32 m0,s78,8192
s_nop 0
buffer_load_dword v6,s[36:39],s68 offen offset:0 sc1 lds
s_waitcnt vmcnt(3)
s_barrier
ds_read_b32 v15,v7 offset:16384)"
R"(
s_lshr_b32 s70,s47,5
buffer_store_dword v5,v4,s[36:39],s70 offen offset:0 sc1
s_mov_b32 s47,s59
s_mov_b32 s69,10240
s_waitcnt lgkmcnt(0)
v_cmp_eq_u32 vcc,s60,v15
s_nop 0
s_cmp_eq_u32 vcc_lo,-1
s_cbranch_scc1 Lg2_go0_%=
s_add_u32 m0,s78,8448
s_nop 0
buffer_load_dword v6,s[36:39],s68 offen offset:0 sc1 lds
s_waitcnt vmcnt(3)
s_barrier
ds_read_b32 v15,v7 offset:16640
s_mov_b32 s69,10496
s_waitcnt lgkmcnt(0)
v_cmp_eq_u32 vcc,s60,v15
s_nop 0
s_cmp_eq_u32 vcc_lo,-1
s_cbranch_scc1 Lg2_go0_%=
Lg2_poll0_%=:
s_add_u32 m0,s78,16384
s_nop 0
buffer_load_dword v6,s[36:39],s68 offen offset:0 sc1 lds
s_waitcnt vmcnt(2)
s_barrier
ds_read_b32 v15,v7 offset:8192
s_mov_b32 s69,18432
s_waitcnt lgkmcnt(0)
v_cmp_eq_u32 vcc,s60,v15
s_nop 0
s_cmp_eq_u32 vcc_lo,-1
s_cbranch_scc1 Lg2_go0_%=
s_sleep 4
s_add_u32 m0,s78,16640
s_nop 0
buffer_load_dword v6,s[36:39],s68 offen offset:0 sc1 lds
s_waitcnt vmcnt(2)
s_barrier
ds_read_b32 v15,v7 offset:8448
s_mov_b32 s69,18688
s_waitcnt lgkmcnt(0)
v_cmp_eq_u32 vcc,s60,v15
s_nop 0
s_cmp_eq_u32 vcc_lo,-1
s_cbranch_scc1 Lg2_go0_%=
s_sleep 4
s_add_u32 m0,s78,8192
s_nop 0
buffer_load_dword v6,s[36:39],s68 offen offset:0 sc1 lds
s_waitcnt vmcnt(2)
s_barrier
ds_read_b32 v15,v7 offset:16384
s_mov_b32 s69,10240
s_waitcnt lgkmcnt(0)
v_cmp_eq_u32 vcc,s60,v15
s_nop 0
s_cmp_eq_u32 vcc_lo,-1
s_cbranch_scc1 Lg2_go0_%=
s_sleep 4
s_add_u32 m0,s78,8448
s_nop 0
buffer_load_dword v6,s[36:39],s68 offen offset:0 sc1 lds
s_waitcnt vmcnt(2)
s_barrier
ds_read_b32 v15,v7 offset:16640
s_mov_b32 s69,10496
s_waitcnt lgkmcnt(0)
v_cmp_eq_u32 vcc,s60,v15
s_nop 0
s_cmp_eq_u32 vcc_lo,-1
s_cbranch_scc1 Lg2_go0_%=
s_sleep 4
s_add_u32 s79,s79,1
s_cmp_lt_u32 s79,20000
s_cbranch_scc1 Lg2_poll0_%=
Lg2_go0_%=:
s_xor_b32 s62,s63,65536
s_mov_b32 m0,s62
s_nop 0
buffer_load_dwordx4 v0,s[36:39],s48 offen offset:0 sc1 lds
buffer_load_dwordx4 v0,s[36:39],s48 offen offset:1024 sc1 lds
buffer_load_dwordx4 v0,s[36:39],s48 offen offset:2048 sc1 lds
buffer_load_dwordx4 v0,s[36:39],s48 offen offset:3072 sc1 lds
s_add_u32 m0,s62,16384
s_nop 0
buffer_load_dwordx4 v1,s[36:39],s48 offen offset:0 sc1 lds
buffer_load_dwordx4 v1,s[36:39],s48 offen offset:1024 sc1 lds
buffer_load_dwordx4 v1,s[36:39],s48 offen offset:2048 sc1 lds
buffer_load_dwordx4 v1,s[36:39],s48 offen offset:3072 sc1 lds
s_lshr_b32 s72,s48,6
s_lshr_b32 s71,s62,2
s_add_u32 m0,s71,s88
s_nop 0
buffer_load_dwordx4 v10,s[36:39],s48 offen offset:0 sc1 lds
buffer_load_dword v11,s[36:39],s72 offen offset:1024 sc1 lds
s_cmp_eq_u32 vcc_hi,-1
s_cselect_b32 s66,s49,s59
s_xor_b32 s62,s64,65536
s_cmp_ge_u32 s66,s59
s_cselect_b32 s68,s49,s50
s_lshr_b32 s68,s68,5
s_add_u32 m0,s78,0
s_nop 0
buffer_load_dword v6,s[36:39],s68 offen offset:0 sc1 lds
s_mov_b32 m0,s62
s_nop 0
buffer_load_dwordx4 v0,s[36:39],s66 offen offset:0 sc1 lds
buffer_load_dwordx4 v0,s[36:39],s66 offen offset:1024 sc1 lds
buffer_load_dwordx4 v0,s[36:39],s66 offen offset:2048 sc1 lds
buffer_load_dwordx4 v0,s[36:39],s66 offen offset:3072 sc1 lds
s_waitcnt vmcnt(11)
s_barrier
ds_read_b128 v[32:35],v2 offset:0
ds_read_b128 v[36:39],v2 offset:1024
ds_read_b128 v[40:43],v2 offset:2048
ds_read_b128 v[44:47],v2 offset:3072
ds_read_b128 v[48:51],v2 offset:4096
ds_read_b128 v[52:55],v2 offset:5120
ds_read_b128 v[56:59],v2 offset:6144
s_lshr_b32 s70,s46,5
buffer_store_dword v5,v4,s[36:39],s70 offen offset:0 sc1
s_mov_b32 s46,s59
s_lshr_b32 s70,s47,5
buffer_store_dword v5,v4,s[36:39],s70 offen offset:0 sc1
s_mov_b32 s47,s59
s_lshr_b32 s70,s59,5
buffer_store_dword v5,v4,s[36:39],s70 offen offset:0 sc1
s_waitcnt lgkmcnt(3)
s_setpc_b64 s[90:91]
Lg2_gs0_%=:
s_cmp_eq_u32 s54,1
s_cselect_b32 s68,s49,s48
s_lshr_b32 s68,s68,5
s_lshr_b32 s73,s47,0
s_lshr_b32 s85,s47,5
s_add_u32 m0,s78,8192
s_nop 0
buffer_load_dword v6,s[36:39],s68 offen offset:0 sc1 lds
v_lshlrev_b32 v64,16,v84
v_and_b32 v65,0xffff0000,v84
v_lshlrev_b32 v66,16,v85
v_and_b32 v67,0xffff0000,v85
v_lshlrev_b32 v68,16,v86
v_and_b32 v69,0xffff0000,v86
v_lshlrev_b32 v70,16,v87
v_and_b32 v71,0xffff0000,v87
s_add_u32 m0,s62,16384
s_nop 0
buffer_load_dwordx4 v1,s[36:39],s66 offen offset:0 sc1 lds
v_fma_f32 v64,v64,v91,v90
v_fma_f32 v65,v65,v91,v90
v_fma_f32 v66,v66,v91,v90
v_fma_f32 v67,v67,v91,v90
v_fma_f32 v68,v68,v91,v90
v_fma_f32 v69,v69,v91,v90
v_fma_f32 v70,v70,v91,v90
v_fma_f32 v71,v71,v91,v90
buffer_load_dwordx4 v1,s[36:39],s66 offen offset:1024 sc1 lds
v_add_f32 v24,v24,v64
v_add_f32 v25,v25,v65
v_add_f32 v26,v26,v66
v_add_f32 v27,v27,v67
v_add_f32 v28,v28,v68
v_add_f32 v29,v29,v69
v_add_f32 v30,v30,v70
v_add_f32 v31,v31,v71
buffer_load_dwordx4 v1,s[36:39],s66 offen offset:2048 sc1 lds
v_cvt_pk_bf16_f32 v72,v24,v25
v_cvt_pk_bf16_f32 v73,v26,v27
v_cvt_pk_bf16_f32 v74,v28,v29
v_cvt_pk_bf16_f32 v75,v30,v31
buffer_store_dwordx4 v[72:75],v3,s[36:39],s73 offen offset:0 sc1
v_add_f32 v64,v24,v25
v_mul_f32 v65,v24,v24
v_fma_f32 v65,v25,v25,v65
buffer_load_dwordx4 v1,s[36:39],s66 offen offset:3072 sc1 lds
v_add_f32 v64,v64,v26
v_fma_f32 v65,v26,v26,v65
v_add_f32 v64,v64,v27
v_fma_f32 v65,v27,v27,v65
v_add_f32 v64,v64,v28
v_fma_f32 v65,v28,v28,v65
v_add_f32 v64,v64,v29
v_fma_f32 v65,v29,v29,v65
s_lshr_b32 s72,s66,6
s_lshr_b32 s71,s62,2
s_add_u32 m0,s71,s88
s_nop 0
buffer_load_dwordx4 v10,s[36:39],s66 offen offset:0 sc1 lds
v_add_f32 v64,v64,v30
v_fma_f32 v65,v30,v30,v65
v_add_f32 v64,v64,v31
v_fma_f32 v65,v31,v31,v65
s_nop 1
v_permlane16_swap_b32 v64,v65
v_add_f32 v64,v64,v65
v_mov_b32 v65,v64
s_nop 1)"
R"(
v_permlane32_swap_b32 v64,v65
buffer_load_dword v11,s[36:39],s72 offen offset:1024 sc1 lds
v_add_f32 v64,v64,v65
ds_write_b32 v13,v64 offset:0
s_waitcnt lgkmcnt(0)
s_barrier
ds_read_b128 v[76:79],v14 offset:0
s_waitcnt lgkmcnt(0)
v_add_f32 v76,v76,v77
v_add_f32 v78,v78,v79
v_add_f32 v76,v76,v78
buffer_store_dword v76,v12,s[36:39],s85 offen offset:0 sc1
s_waitcnt vmcnt(8)
s_barrier
ds_read_b32 v15,v7 offset:8192
ds_read_b128 v[32:35],v2 offset:0
ds_read_b128 v[36:39],v2 offset:1024
ds_read_b128 v[40:43],v2 offset:2048
ds_read_b128 v[44:47],v2 offset:3072
ds_read_b128 v[48:51],v2 offset:4096
ds_read_b128 v[52:55],v2 offset:5120
ds_read_b128 v[56:59],v2 offset:6144
s_lshr_b32 s70,s46,5
buffer_store_dword v5,v4,s[36:39],s70 offen offset:0 sc1
s_waitcnt lgkmcnt(7)
v_cmp_eq_u32 vcc,s60,v15
s_nop 0
s_cmp_eq_u32 vcc_hi,-1
s_cselect_b32 s66,s49,s59
s_xor_b32 s62,s64,65536
s_cmp_ge_u32 s66,s59
s_cselect_b32 s68,s49,s50
s_lshr_b32 s68,s68,5
s_add_u32 m0,s78,0
s_nop 0
buffer_load_dword v6,s[36:39],s68 offen offset:0 sc1 lds
s_mov_b32 m0,s62
s_nop 0
buffer_load_dwordx4 v0,s[36:39],s66 offen offset:0 sc1 lds
buffer_load_dwordx4 v0,s[36:39],s66 offen offset:1024 sc1 lds
buffer_load_dwordx4 v0,s[36:39],s66 offen offset:2048 sc1 lds
buffer_load_dwordx4 v0,s[36:39],s66 offen offset:3072 sc1 lds
s_mov_b32 s46,s47
s_mov_b32 s47,s59
s_lshr_b32 s70,s59,5
buffer_store_dword v5,v4,s[36:39],s70 offen offset:0 sc1
s_lshr_b32 s70,s59,5
buffer_store_dword v5,v4,s[36:39],s70 offen offset:0 sc1
s_waitcnt lgkmcnt(3)
s_setpc_b64 s[90:91]
Lg2_done0_%=:
s_waitcnt vmcnt(0)
s_barrier
s_lshr_b32 s70,s46,5
buffer_store_dword v5,v4,s[36:39],s70 offen offset:0 sc1
s_lshr_b32 s70,s47,5
buffer_store_dword v5,v4,s[36:39],s70 offen offset:0 sc1
s_branch Lg2_done_%=
Lg2_slow1_%=:
s_cmp_lt_u32 s67,s59
s_cbranch_scc1 Lg2_gs1_%=
Lg2_slowb1_%=:
s_cmp_eq_u32 s54,1
s_cselect_b32 s68,s50,s49
s_lshr_b32 s68,s68,5
s_add_u32 m0,s78,8192
s_nop 0
buffer_load_dword v6,s[36:39],s68 offen offset:0 sc1 lds
s_lshr_b32 s73,s48,0
s_lshr_b32 s85,s48,5
v_lshlrev_b32 v64,16,v80
v_and_b32 v65,0xffff0000,v80
v_lshlrev_b32 v66,16,v81
v_and_b32 v67,0xffff0000,v81
v_lshlrev_b32 v68,16,v82
v_and_b32 v69,0xffff0000,v82
v_lshlrev_b32 v70,16,v83
v_and_b32 v71,0xffff0000,v83
v_fma_f32 v64,v64,v89,v88
v_fma_f32 v65,v65,v89,v88
v_fma_f32 v66,v66,v89,v88
v_fma_f32 v67,v67,v89,v88
v_fma_f32 v68,v68,v89,v88
v_fma_f32 v69,v69,v89,v88
v_fma_f32 v70,v70,v89,v88
v_fma_f32 v71,v71,v89,v88
v_add_f32 v16,v16,v64
v_add_f32 v17,v17,v65
v_add_f32 v18,v18,v66
v_add_f32 v19,v19,v67
v_add_f32 v20,v20,v68
v_add_f32 v21,v21,v69
v_add_f32 v22,v22,v70
v_add_f32 v23,v23,v71
v_cvt_pk_bf16_f32 v72,v16,v17
v_cvt_pk_bf16_f32 v73,v18,v19
v_cvt_pk_bf16_f32 v74,v20,v21
v_cvt_pk_bf16_f32 v75,v22,v23
s_add_u32 m0,s78,8448
s_nop 0
buffer_load_dword v6,s[36:39],s68 offen offset:0 sc1 lds
buffer_store_dwordx4 v[72:75],v3,s[36:39],s73 offen offset:0 sc1
v_add_f32 v64,v16,v17
v_mul_f32 v65,v16,v16
v_fma_f32 v65,v17,v17,v65
v_add_f32 v64,v64,v18
v_fma_f32 v65,v18,v18,v65
v_add_f32 v64,v64,v19
v_fma_f32 v65,v19,v19,v65
v_add_f32 v64,v64,v20
v_fma_f32 v65,v20,v20,v65
v_add_f32 v64,v64,v21
v_fma_f32 v65,v21,v21,v65
v_add_f32 v64,v64,v22
v_fma_f32 v65,v22,v22,v65
v_add_f32 v64,v64,v23
v_fma_f32 v65,v23,v23,v65
s_nop 1
v_permlane16_swap_b32 v64,v65
v_add_f32 v64,v64,v65
v_mov_b32 v65,v64
s_nop 1
v_permlane32_swap_b32 v64,v65
v_add_f32 v64,v64,v65
ds_write_b32 v13,v64 offset:2048
s_waitcnt lgkmcnt(0)
s_barrier
ds_read_b128 v[76:79],v14 offset:2048
s_waitcnt lgkmcnt(0)
v_add_f32 v76,v76,v77
v_add_f32 v78,v78,v79
v_add_f32 v76,v76,v78
buffer_store_dword v76,v12,s[36:39],s85 offen offset:0 sc1
s_cmp_ge_u32 s49,s59
s_cbranch_scc1 Lg2_done1_%=
s_add_u32 m0,s78,16384
s_nop 0
buffer_load_dword v6,s[36:39],s68 offen offset:0 sc1 lds
s_waitcnt vmcnt(4)
s_barrier
ds_read_b32 v15,v7 offset:8192
s_lshr_b32 s70,s47,5
buffer_store_dword v5,v4,s[36:39],s70 offen offset:0 sc1
s_mov_b32 s47,s59
s_mov_b32 s69,18432
s_waitcnt lgkmcnt(0)
v_cmp_eq_u32 vcc,s60,v15
s_nop 0
s_cmp_eq_u32 vcc_lo,-1
s_cbranch_scc1 Lg2_go1_%=
s_add_u32 m0,s78,16640
s_nop 0
buffer_load_dword v6,s[36:39],s68 offen offset:0 sc1 lds
s_waitcnt vmcnt(5)
s_barrier
ds_read_b32 v15,v7 offset:8448
s_mov_b32 s69,18688
s_waitcnt lgkmcnt(0)
v_cmp_eq_u32 vcc,s60,v15
s_nop 0
s_cmp_eq_u32 vcc_lo,-1
s_cbranch_scc1 Lg2_go1_%=
s_add_u32 m0,s78,8192
s_nop 0
buffer_load_dword v6,s[36:39],s68 offen offset:0 sc1 lds
s_waitcnt vmcnt(3)
s_barrier
ds_read_b32 v15,v7 offset:16384
s_lshr_b32 s70,s48,5
buffer_store_dword v5,v4,s[36:39],s70 offen offset:0 sc1
s_mov_b32 s48,s59
s_mov_b32 s69,10240
s_waitcnt lgkmcnt(0)
v_cmp_eq_u32 vcc,s60,v15
s_nop 0
s_cmp_eq_u32 vcc_lo,-1
s_cbranch_scc1 Lg2_go1_%=
s_add_u32 m0,s78,8448
s_nop 0
buffer_load_dword v6,s[36:39],s68 offen offset:0 sc1 lds
s_waitcnt vmcnt(3)
s_barrier
ds_read_b32 v15,v7 offset:16640
s_mov_b32 s69,10496
s_waitcnt lgkmcnt(0)
v_cmp_eq_u32 vcc,s60,v15
s_nop 0
s_cmp_eq_u32 vcc_lo,-1
s_cbranch_scc1 Lg2_go1_%=
Lg2_poll1_%=:
s_add_u32 m0,s78,16384
s_nop 0
buffer_load_dword v6,s[36:39],s68 offen offset:0 sc1 lds
s_waitcnt vmcnt(2)
s_barrier
ds_read_b32 v15,v7 offset:8192
s_mov_b32 s69,18432
s_waitcnt lgkmcnt(0)
v_cmp_eq_u32 vcc,s60,v15
s_nop 0)"
R"(
s_cmp_eq_u32 vcc_lo,-1
s_cbranch_scc1 Lg2_go1_%=
s_sleep 4
s_add_u32 m0,s78,16640
s_nop 0
buffer_load_dword v6,s[36:39],s68 offen offset:0 sc1 lds
s_waitcnt vmcnt(2)
s_barrier
ds_read_b32 v15,v7 offset:8448
s_mov_b32 s69,18688
s_waitcnt lgkmcnt(0)
v_cmp_eq_u32 vcc,s60,v15
s_nop 0
s_cmp_eq_u32 vcc_lo,-1
s_cbranch_scc1 Lg2_go1_%=
s_sleep 4
s_add_u32 m0,s78,8192
s_nop 0
buffer_load_dword v6,s[36:39],s68 offen offset:0 sc1 lds
s_waitcnt vmcnt(2)
s_barrier
ds_read_b32 v15,v7 offset:16384
s_mov_b32 s69,10240
s_waitcnt lgkmcnt(0)
v_cmp_eq_u32 vcc,s60,v15
s_nop 0
s_cmp_eq_u32 vcc_lo,-1
s_cbranch_scc1 Lg2_go1_%=
s_sleep 4
s_add_u32 m0,s78,8448
s_nop 0
buffer_load_dword v6,s[36:39],s68 offen offset:0 sc1 lds
s_waitcnt vmcnt(2)
s_barrier
ds_read_b32 v15,v7 offset:16640
s_mov_b32 s69,10496
s_waitcnt lgkmcnt(0)
v_cmp_eq_u32 vcc,s60,v15
s_nop 0
s_cmp_eq_u32 vcc_lo,-1
s_cbranch_scc1 Lg2_go1_%=
s_sleep 4
s_add_u32 s79,s79,1
s_cmp_lt_u32 s79,20000
s_cbranch_scc1 Lg2_poll1_%=
Lg2_go1_%=:
s_xor_b32 s62,s64,65536
s_mov_b32 m0,s62
s_nop 0
buffer_load_dwordx4 v0,s[36:39],s49 offen offset:0 sc1 lds
buffer_load_dwordx4 v0,s[36:39],s49 offen offset:1024 sc1 lds
buffer_load_dwordx4 v0,s[36:39],s49 offen offset:2048 sc1 lds
buffer_load_dwordx4 v0,s[36:39],s49 offen offset:3072 sc1 lds
s_add_u32 m0,s62,16384
s_nop 0
buffer_load_dwordx4 v1,s[36:39],s49 offen offset:0 sc1 lds
buffer_load_dwordx4 v1,s[36:39],s49 offen offset:1024 sc1 lds
buffer_load_dwordx4 v1,s[36:39],s49 offen offset:2048 sc1 lds
buffer_load_dwordx4 v1,s[36:39],s49 offen offset:3072 sc1 lds
s_lshr_b32 s72,s49,6
s_lshr_b32 s71,s62,2
s_add_u32 m0,s71,s88
s_nop 0
buffer_load_dwordx4 v10,s[36:39],s49 offen offset:0 sc1 lds
buffer_load_dword v11,s[36:39],s72 offen offset:1024 sc1 lds
s_cmp_eq_u32 vcc_hi,-1
s_cselect_b32 s67,s50,s59
s_xor_b32 s62,s63,65536
s_cmp_ge_u32 s67,s59
s_cselect_b32 s68,s50,s51
s_lshr_b32 s68,s68,5
s_add_u32 m0,s78,256
s_nop 0
buffer_load_dword v6,s[36:39],s68 offen offset:0 sc1 lds
s_mov_b32 m0,s62
s_nop 0
buffer_load_dwordx4 v0,s[36:39],s67 offen offset:0 sc1 lds
buffer_load_dwordx4 v0,s[36:39],s67 offen offset:1024 sc1 lds
buffer_load_dwordx4 v0,s[36:39],s67 offen offset:2048 sc1 lds
buffer_load_dwordx4 v0,s[36:39],s67 offen offset:3072 sc1 lds
s_waitcnt vmcnt(11)
s_barrier
ds_read_b128 v[32:35],v2 offset:32768
ds_read_b128 v[36:39],v2 offset:33792
ds_read_b128 v[40:43],v2 offset:34816
ds_read_b128 v[44:47],v2 offset:35840
ds_read_b128 v[48:51],v2 offset:36864
ds_read_b128 v[52:55],v2 offset:37888
ds_read_b128 v[56:59],v2 offset:38912
s_lshr_b32 s70,s47,5
buffer_store_dword v5,v4,s[36:39],s70 offen offset:0 sc1
s_mov_b32 s47,s59
s_lshr_b32 s70,s48,5
buffer_store_dword v5,v4,s[36:39],s70 offen offset:0 sc1
s_mov_b32 s48,s59
s_lshr_b32 s70,s59,5
buffer_store_dword v5,v4,s[36:39],s70 offen offset:0 sc1
s_waitcnt lgkmcnt(3)
s_setpc_b64 s[92:93]
Lg2_gs1_%=:
s_cmp_eq_u32 s54,1
s_cselect_b32 s68,s50,s49
s_lshr_b32 s68,s68,5
s_lshr_b32 s73,s48,0
s_lshr_b32 s85,s48,5
s_add_u32 m0,s78,8192
s_nop 0
buffer_load_dword v6,s[36:39],s68 offen offset:0 sc1 lds
v_lshlrev_b32 v64,16,v80
v_and_b32 v65,0xffff0000,v80
v_lshlrev_b32 v66,16,v81
v_and_b32 v67,0xffff0000,v81
v_lshlrev_b32 v68,16,v82
v_and_b32 v69,0xffff0000,v82
v_lshlrev_b32 v70,16,v83
v_and_b32 v71,0xffff0000,v83
s_add_u32 m0,s62,16384
s_nop 0
buffer_load_dwordx4 v1,s[36:39],s67 offen offset:0 sc1 lds
v_fma_f32 v64,v64,v89,v88
v_fma_f32 v65,v65,v89,v88
v_fma_f32 v66,v66,v89,v88
v_fma_f32 v67,v67,v89,v88
v_fma_f32 v68,v68,v89,v88
v_fma_f32 v69,v69,v89,v88
v_fma_f32 v70,v70,v89,v88
v_fma_f32 v71,v71,v89,v88
buffer_load_dwordx4 v1,s[36:39],s67 offen offset:1024 sc1 lds
v_add_f32 v16,v16,v64
v_add_f32 v17,v17,v65
v_add_f32 v18,v18,v66
v_add_f32 v19,v19,v67
v_add_f32 v20,v20,v68
v_add_f32 v21,v21,v69
v_add_f32 v22,v22,v70
v_add_f32 v23,v23,v71
buffer_load_dwordx4 v1,s[36:39],s67 offen offset:2048 sc1 lds
v_cvt_pk_bf16_f32 v72,v16,v17
v_cvt_pk_bf16_f32 v73,v18,v19
v_cvt_pk_bf16_f32 v74,v20,v21
v_cvt_pk_bf16_f32 v75,v22,v23
buffer_store_dwordx4 v[72:75],v3,s[36:39],s73 offen offset:0 sc1
v_add_f32 v64,v16,v17
v_mul_f32 v65,v16,v16
v_fma_f32 v65,v17,v17,v65
buffer_load_dwordx4 v1,s[36:39],s67 offen offset:3072 sc1 lds
v_add_f32 v64,v64,v18
v_fma_f32 v65,v18,v18,v65
v_add_f32 v64,v64,v19
v_fma_f32 v65,v19,v19,v65
v_add_f32 v64,v64,v20
v_fma_f32 v65,v20,v20,v65
v_add_f32 v64,v64,v21
v_fma_f32 v65,v21,v21,v65
s_lshr_b32 s72,s67,6
s_lshr_b32 s71,s62,2
s_add_u32 m0,s71,s88
s_nop 0
buffer_load_dwordx4 v10,s[36:39],s67 offen offset:0 sc1 lds
v_add_f32 v64,v64,v22
v_fma_f32 v65,v22,v22,v65
v_add_f32 v64,v64,v23
v_fma_f32 v65,v23,v23,v65
s_nop 1
v_permlane16_swap_b32 v64,v65
v_add_f32 v64,v64,v65
v_mov_b32 v65,v64
s_nop 1
v_permlane32_swap_b32 v64,v65
buffer_load_dword v11,s[36:39],s72 offen offset:1024 sc1 lds
v_add_f32 v64,v64,v65
ds_write_b32 v13,v64 offset:2048
s_waitcnt lgkmcnt(0)
s_barrier
ds_read_b128 v[76:79],v14 offset:2048
s_waitcnt lgkmcnt(0)
v_add_f32 v76,v76,v77
v_add_f32 v78,v78,v79
v_add_f32 v76,v76,v78
buffer_store_dword v76,v12,s[36:39],s85 offen offset:0 sc1
s_waitcnt vmcnt(8)
s_barrier
ds_read_b32 v15,v7 offset:8192
ds_read_b128 v[32:35],v2 offset:32768
ds_read_b128 v[36:39],v2 offset:33792
ds_read_b128 v[40:43],v2 offset:34816
ds_read_b128 v[44:47],v2 offset:35840
ds_read_b128 v[48:51],v2 offset:36864
ds_read_b128 v[52:55],v2 offset:37888
ds_read_b128 v[56:59],v2 offset:38912
s_lshr_b32 s70,s47,5
buffer_store_dword v5,v4,s[36:39],s70 offen offset:0 sc1
s_waitcnt lgkmcnt(7)
v_cmp_eq_u32 vcc,s60,v15
s_nop 0
s_cmp_eq_u32 vcc_hi,-1
s_cselect_b32 s67,s50,s59
s_xor_b32 s62,s63,65536
s_cmp_ge_u32 s67,s59
s_cselect_b32 s68,s50,s51)"
R"(
s_lshr_b32 s68,s68,5
s_add_u32 m0,s78,256
s_nop 0
buffer_load_dword v6,s[36:39],s68 offen offset:0 sc1 lds
s_mov_b32 m0,s62
s_nop 0
buffer_load_dwordx4 v0,s[36:39],s67 offen offset:0 sc1 lds
buffer_load_dwordx4 v0,s[36:39],s67 offen offset:1024 sc1 lds
buffer_load_dwordx4 v0,s[36:39],s67 offen offset:2048 sc1 lds
buffer_load_dwordx4 v0,s[36:39],s67 offen offset:3072 sc1 lds
s_mov_b32 s47,s48
s_mov_b32 s48,s59
s_lshr_b32 s70,s59,5
buffer_store_dword v5,v4,s[36:39],s70 offen offset:0 sc1
s_lshr_b32 s70,s59,5
buffer_store_dword v5,v4,s[36:39],s70 offen offset:0 sc1
s_waitcnt lgkmcnt(3)
s_setpc_b64 s[92:93]
Lg2_done1_%=:
s_waitcnt vmcnt(0)
s_barrier
s_lshr_b32 s70,s47,5
buffer_store_dword v5,v4,s[36:39],s70 offen offset:0 sc1
s_lshr_b32 s70,s48,5
buffer_store_dword v5,v4,s[36:39],s70 offen offset:0 sc1
s_branch Lg2_done_%=
Lg2_done_%=:
s_waitcnt vmcnt(0) lgkmcnt(0)
s_barrier
Lg2_end_%=:
s_mov_b32 %0,s60
Lcode_g12_e:)"
  : "=&s"(epOut) : "s"(ws), "s"(wave), "s"(sStep), "s"(sX1), "s"(sX2), "s"(sU0), "s"(sWw), "s"(sBuf), "v"(vSrc), "v"(vSrc2), "v"(vA), "v"(vOut), "v"(vFlag), "v"(vPollOff), "v"(vSnapRd), "v"(vX), "v"(vX2), "v"(vAuxS), "v"(vAuxS2), "v"(vOut2), "v"(vStW), "v"(vStR), "v"(vWd0), "v"(vWd1), "v"(vWr0), "v"(vWr1), "v"(vWr2), "s"(w12) : "v0", "v1", "v2", "v3", "v4", "v5", "v6", "v7", "v8", "v9", "v10", "v11", "v12", "v13", "v14", "v15", "v16", "v17", "v18", "v19", "v20", "v21", "v22", "v23", "v24", "v25", "v26", "v27", "v28", "v29", "v30", "v31", "v32", "v33", "v34", "v35", "v36", "v37", "v38", "v39", "v40", "v41", "v42", "v43", "v44", "v45", "v46", "v47", "v48", "v49", "v50", "v51", "v52", "v53", "v54", "v55", "v56", "v57", "v58", "v59", "v60", "v61", "v62", "v63", "v64", "v65", "v66", "v67", "v68", "v69", "v70", "v71", "v72", "v73", "v74", "v75", "v76", "v77", "v78", "v79", "v80", "v81", "v82", "v83", "v84", "v85", "v86", "v87", "v88", "v89", "v90", "v91", "v92", "v93", "v94", "v95", "v96", "v97", "v98", "v99", "v100", "v101", "v102", "v103", "v104", "v105", "v106", "v107", "v108", "v109", "v110", "v111", "v112", "v113", "v114", "v115", "v116", "v117", "v118", "v119", "v120", "v121", "v122", "v123", "v124", "v125", "v126", "v127", "v128", "v129", "v130", "v131", "v132", "v133", "v134", "v135", "v136", "v137", "v138", "v139", "v140", "v141", "v142", "v143", "v144", "v145", "v146", "v147", "v148", "v149", "v150", "v151", "v152", "v153", "v154", "v155", "v156", "v157", "v158", "v159", "v160", "v161", "v162", "v163", "v164", "v165", "v166", "v167", "v168", "v169", "v170", "v171", "v172", "v173", "v174", "v175", "v176", "v177", "v178", "v179", "v180", "v181", "v182", "v183", "v184", "v185", "v186", "v187", "v188", "v189", "v190", "v191", "v192", "v193", "v194", "v195", "v196", "v197", "v198", "v199", "v200", "v201", "v202", "v203", "v204", "v205", "v206", "v207", "v208", "v209", "v210", "v211", "v212", "v213", "v214", "v215", "v216", "v217", "v218", "v219", "v220", "v221", "v222", "v223", "v224", "v225", "v226", "v227", "v228", "v229", "v230", "v231", "v255", "a0", "a1", "a2", "a3", "a4", "a5", "a6", "a7", "a8", "a9", "a10", "a11", "a12", "a13", "a14", "a15", "a16", "a17", "a18", "a19", "a20", "a21", "a22", "a23", "a24", "a25", "a26", "a27", "a28", "a29", "a30", "a31", "a32", "a33", "a34", "a35", "a36", "a37", "a38", "a39", "a40", "a41", "a42", "a43", "a44", "a45", "a46", "a47", "a48", "a49", "a50", "a51", "a52", "a53", "a54", "a55", "a56", "a57", "a58", "a59", "a60", "a61", "a62", "a63", "a64", "a65", "a66", "a67", "a68", "a69", "a70", "a71", "a72", "a73", "a74", "a75", "a76", "a77", "a78", "a79", "a80", "a81", "a82", "a83", "a84", "a85", "a86", "a87", "a88", "a89", "a90", "a91", "a92", "a93", "a94", "a95", "a96", "a97", "a98", "a99", "a100", "a101", "a102", "a103", "a104", "a105", "a106", "a107", "a108", "a109", "a110", "a111", "a112", "a113", "a114", "a115", "a116", "a117", "a118", "a119", "a120", "a121", "a122", "a123", "a124", "a125", "a126", "a127", "a128", "a129", "a130", "a131", "a132", "a133", "a134", "a135", "a136", "a137", "a138", "a139", "a140", "a141", "a142", "a143", "a144", "a145", "a146", "a147", "a148", "a149", "a150", "a151", "a152", "a153", "a154", "a155", "a156", "a157", "a158", "a159", "a160", "a161", "a162", "a163", "a164", "a165", "a166", "a167", "a168", "a169", "a170", "a171", "a172", "a173", "a174", "a175", "a176", "a177", "a178", "a179", "a180", "a181", "a182", "a183", "a184", "a185", "a186", "a187", "a188", "a189", "a190", "a191", "a192", "a193", "a194", "a195", "a196", "a197", "a198", "a199", "a200", "a201", "a202", "a203", "a204", "a205", "a206", "a207", "a208", "a209", "a210", "a211", "a212", "a213", "a214", "a215", "a216", "a217", "a218", "a219", "a220", "a221", "a222", "a223", "a224", "a225", "a226", "a227", "a228", "a229", "a230", "a231", "a232", "a233", "a234", "a235", "a236", "a237", "a238", "a239", "a240", "a241", "a242", "a243", "a244", "a245", "a246", "a247", "a248", "a249", "a250", "a251", "a252", "a253", "a254", "a255", "s36", "s37", "s38", "s39", "s40", "s41", "s42", "s43", "s44", "s45", "s46", "s47", "s48", "s49", "s50", "s51", "s52", "s53", "s54", "s55", "s56", "s57", "s58", "s59", "s60", "s61", "s62", "s63", "s64", "s65", "s66", "s67", "s68", "s69", "s70", "s71", "s72", "s73", "s74", "s75", "s76", "s77", "s78", "s79", "s80", "s81", "s82", "s83", "s84", "s85", "s86", "s87", "s88", "s89", "s90", "s91", "s92", "s93", "s94", "s95", "s96", "s97", "s98", "s99", "vcc", "scc", "memory");
    ep = epOut;
    if (N3 && kind == 1 && rep < 2 * NX) { kind = 4; kh = (rep & 1) * 2 + (slice >> 2); slice &= 3; rep = 5 + (rep >> 1); }
   }
  if (kind == 3 || kind == 5) {
    KARG(ws, 10) KARG(w3, 8)
    COMMON(8, OFF_FX1, OFF_FG + slice * 128, AUX + 1536, 2048, 7168, kind == 3 ? 5 : 4);
    const unsigned vSrc = OFF_X1 + wave * 4096 + vA, vOut = OFF_G + (slice * 6 + wave) * 1024 + vA;
    const unsigned vOut2 = OFF_G + (slice * 6 + 4 + (wave >> 1)) * 1024 + (((wave & 1) * 2 + (lg >> 1)) * 16 + l15) * 16 + (lg & 1) * 8;
    const unsigned vAuxS = OFF_ST + (ln & 7) * 128 + (wave * 8 + (ln >> 3)) * 4, vX = AUX + wave * 2048 + ln * 4;
    const unsigned vStW = (ln & 7) == 0 ? AUX + 1280 + (wave * 8 + (ln >> 3)) * 4 : AUX + 24576 + wave * 2048 + 1536 + ln * 4, vStR = AUX + 1280 + l15 * 8;
    const unsigned sWw = 16 * wave * 7168 + slice * 384;
    if (kind == 3) { asm volatile(R"(
Lcode_g3_b:
s_mov_b64 s[36:37],%0
s_mov_b32 s38,122727552
s_mov_b32 s39,0x00020000
s_mov_b32 s54,%1
s_mov_b32 s55,%2
s_mov_b32 s56,%3
s_mov_b32 s58,%4
s_mov_b32 s83,%5
s_mov_b32 s82,%6
s_mov_b32 s84,%7
s_mov_b32 s60,%8
v_mov_b32 v0,%9
v_mov_b32 v2,%10
v_mov_b32 v3,%11
v_mov_b32 v4,%12
v_mov_b32 v6,%13
v_mov_b32 v7,%14
v_mov_b32 v8,%15
v_mov_b32 v9,%16
v_mov_b32 v10,%17
v_mov_b32 v11,%18
v_mov_b32 v28,%19
v_mov_b32 v29,%20
v_mov_b32 v30,%21
v_mov_b32 v31,%22
v_mov_b32 v32,%23
s_mov_b64 s[40:41],%24
s_mov_b32 s42,7340032
s_mov_b32 s43,0x00020000
s_lshl_b32 s88,s54,11
s_add_u32 m0,s88,0
s_add_u32 s61,s82,0
buffer_load_dwordx4 v28,s[40:43],s61 offen offset:0  lds
s_add_u32 m0,s88,1024
s_nop 0
buffer_load_dwordx4 v29,s[40:43],s61 offen offset:0  lds
s_add_u32 m0,s88,8064
s_nop 0
buffer_load_dwordx4 v28,s[40:43],s61 offen offset:128  lds
s_add_u32 m0,s88,9088
s_nop 0
buffer_load_dwordx4 v29,s[40:43],s61 offen offset:128  lds
s_add_u32 m0,s88,16128
s_nop 0
buffer_load_dwordx4 v28,s[40:43],s61 offen offset:256  lds
s_add_u32 m0,s88,17152
s_nop 0
buffer_load_dwordx4 v29,s[40:43],s61 offen offset:256  lds
s_add_u32 m0,s88,24576
s_add_u32 s61,s82,458752
buffer_load_dwordx4 v28,s[40:43],s61 offen offset:0  lds
s_add_u32 m0,s88,25600
s_nop 0
buffer_load_dwordx4 v29,s[40:43],s61 offen offset:0  lds
s_add_u32 m0,s88,32640
s_nop 0
buffer_load_dwordx4 v28,s[40:43],s61 offen offset:128  lds
s_add_u32 m0,s88,33664
s_nop 0
buffer_load_dwordx4 v29,s[40:43],s61 offen offset:128  lds
s_add_u32 m0,s88,40704
s_nop 0
buffer_load_dwordx4 v28,s[40:43],s61 offen offset:256  lds
s_add_u32 m0,s88,41728
s_nop 0
buffer_load_dwordx4 v29,s[40:43],s61 offen offset:256  lds
s_add_u32 m0,s88,49152
s_add_u32 s61,s82,917504
buffer_load_dwordx4 v28,s[40:43],s61 offen offset:0  lds
s_add_u32 m0,s88,50176
s_nop 0
buffer_load_dwordx4 v29,s[40:43],s61 offen offset:0  lds
s_add_u32 m0,s88,57216
s_nop 0
buffer_load_dwordx4 v28,s[40:43],s61 offen offset:128  lds
s_add_u32 m0,s88,58240
s_nop 0
buffer_load_dwordx4 v29,s[40:43],s61 offen offset:128  lds
s_add_u32 m0,s88,65280
s_nop 0
buffer_load_dwordx4 v28,s[40:43],s61 offen offset:256  lds
s_add_u32 m0,s88,66304
s_nop 0
buffer_load_dwordx4 v29,s[40:43],s61 offen offset:256  lds
s_add_u32 m0,s88,73728
s_add_u32 s61,s82,1376256
buffer_load_dwordx4 v28,s[40:43],s61 offen offset:0  lds
s_add_u32 m0,s88,74752
s_nop 0
buffer_load_dwordx4 v29,s[40:43],s61 offen offset:0  lds
s_add_u32 m0,s88,81792
s_nop 0
buffer_load_dwordx4 v28,s[40:43],s61 offen offset:128  lds
s_add_u32 m0,s88,82816
s_nop 0
buffer_load_dwordx4 v29,s[40:43],s61 offen offset:128  lds
s_add_u32 m0,s88,89856
s_nop 0
buffer_load_dwordx4 v28,s[40:43],s61 offen offset:256  lds
s_add_u32 m0,s88,90880
s_nop 0
buffer_load_dwordx4 v29,s[40:43],s61 offen offset:256  lds
v_add_u32 v40,49152,v30
v_add_u32 v41,49152,v31
v_add_u32 v42,49152,v32
v_add_u32 v43,98304,v30
v_add_u32 v44,98304,v31
v_add_u32 v45,98304,v32
v_mov_b32 v84,0x3f803f80
v_mov_b32 v85,0x3f803f80
v_mov_b32 v86,0x3f803f80
v_mov_b32 v87,0x3f803f80
s_waitcnt vmcnt(18)
s_barrier
s_add_u32 m0,s88,98304
s_add_u32 s61,s82,1835008
buffer_load_dwordx4 v28,s[40:43],s61 offen offset:0  lds
s_add_u32 m0,s88,99328
ds_read_b64_tr_b16 a[0:1],v30 offset:0
buffer_load_dwordx4 v29,s[40:43],s61 offen offset:0  lds
s_add_u32 m0,s88,106368
ds_read_b64_tr_b16 a[2:3],v30 offset:512
buffer_load_dwordx4 v28,s[40:43],s61 offen offset:128  lds
s_add_u32 m0,s88,107392
ds_read_b64_tr_b16 a[4:5],v31 offset:0
buffer_load_dwordx4 v29,s[40:43],s61 offen offset:128  lds
s_add_u32 m0,s88,114432
ds_read_b64_tr_b16 a[6:7],v31 offset:512
buffer_load_dwordx4 v28,s[40:43],s61 offen offset:256  lds
s_add_u32 m0,s88,115456
ds_read_b64_tr_b16 a[8:9],v32 offset:0
buffer_load_dwordx4 v29,s[40:43],s61 offen offset:256  lds
ds_read_b64_tr_b16 a[10:11],v32 offset:512
ds_read_b64_tr_b16 a[12:13],v30 offset:4096
ds_read_b64_tr_b16 a[14:15],v30 offset:4608
ds_read_b64_tr_b16 a[16:17],v31 offset:4096
ds_read_b64_tr_b16 a[18:19],v31 offset:4608
ds_read_b64_tr_b16 a[20:21],v32 offset:4096
ds_read_b64_tr_b16 a[22:23],v32 offset:4608
s_waitcnt vmcnt(18)
s_barrier
s_add_u32 m0,s88,122880
s_add_u32 s61,s82,2293760
buffer_load_dwordx4 v28,s[40:43],s61 offen offset:0  lds
s_add_u32 m0,s88,123904
ds_read_b64_tr_b16 a[24:25],v30 offset:24576
buffer_load_dwordx4 v29,s[40:43],s61 offen offset:0  lds
s_add_u32 m0,s88,130944
ds_read_b64_tr_b16 a[26:27],v30 offset:25088
buffer_load_dwordx4 v28,s[40:43],s61 offen offset:128  lds
s_add_u32 m0,s88,131968
ds_read_b64_tr_b16 a[28:29],v31 offset:24576
buffer_load_dwordx4 v29,s[40:43],s61 offen offset:128  lds
s_add_u32 m0,s88,139008
ds_read_b64_tr_b16 a[30:31],v31 offset:25088
buffer_load_dwordx4 v28,s[40:43],s61 offen offset:256  lds
s_add_u32 m0,s88,140032
ds_read_b64_tr_b16 a[32:33],v32 offset:24576
buffer_load_dwordx4 v29,s[40:43],s61 offen offset:256  lds
ds_read_b64_tr_b16 a[34:35],v32 offset:25088
ds_read_b64_tr_b16 a[36:37],v30 offset:28672
ds_read_b64_tr_b16 a[38:39],v30 offset:29184
ds_read_b64_tr_b16 a[40:41],v31 offset:28672
ds_read_b64_tr_b16 a[42:43],v31 offset:29184
ds_read_b64_tr_b16 a[44:45],v32 offset:28672
ds_read_b64_tr_b16 a[46:47],v32 offset:29184
s_waitcnt lgkmcnt(12)
v_mfma_f32_16x16x32_bf16 v[16:19],a[0:3],v[84:87],0
v_mfma_f32_16x16x32_bf16 v[20:23],a[4:7],v[84:87],0
v_mfma_f32_16x16x32_bf16 v[24:27],a[8:11],v[84:87],0
v_mfma_f32_16x16x32_bf16 v[16:19],a[12:15],v[84:87],v[16:19]
v_mfma_f32_16x16x32_bf16 v[20:23],a[16:19],v[84:87],v[20:23]
v_mfma_f32_16x16x32_bf16 v[24:27],a[20:23],v[84:87],v[24:27]
s_waitcnt vmcnt(18)
s_barrier
s_add_u32 m0,s88,0
s_add_u32 s61,s82,2752512
buffer_load_dwordx4 v28,s[40:43],s61 offen offset:0  lds
s_add_u32 m0,s88,1024
ds_read_b64_tr_b16 a[48:49],v40 offset:0
buffer_load_dwordx4 v29,s[40:43],s61 offen offset:0  lds
s_add_u32 m0,s88,8064
ds_read_b64_tr_b16 a[50:51],v40 offset:512
buffer_load_dwordx4 v28,s[40:43],s61 offen offset:128  lds
s_add_u32 m0,s88,9088
ds_read_b64_tr_b16 a[52:53],v41 offset:0
buffer_load_dwordx4 v29,s[40:43],s61 offen offset:128  lds
s_add_u32 m0,s88,16128
ds_read_b64_tr_b16 a[54:55],v41 offset:512
buffer_load_dwordx4 v28,s[40:43],s61 offen offset:256  lds
s_add_u32 m0,s88,17152
ds_read_b64_tr_b16 a[56:57],v42 offset:0
buffer_load_dwordx4 v29,s[40:43],s61 offen offset:256  lds
ds_read_b64_tr_b16 a[58:59],v42 offset:512
ds_read_b64_tr_b16 a[60:61],v40 offset:4096
ds_read_b64_tr_b16 a[62:63],v40 offset:4608
ds_read_b64_tr_b16 a[64:65],v41 offset:4096
ds_read_b64_tr_b16 a[66:67],v41 offset:4608
ds_read_b64_tr_b16 a[68:69],v42 offset:4096)"
R"(
ds_read_b64_tr_b16 a[70:71],v42 offset:4608
s_waitcnt lgkmcnt(12)
v_mfma_f32_16x16x32_bf16 v[16:19],a[24:27],v[84:87],v[16:19]
v_mfma_f32_16x16x32_bf16 v[20:23],a[28:31],v[84:87],v[20:23]
v_mfma_f32_16x16x32_bf16 v[24:27],a[32:35],v[84:87],v[24:27]
v_mfma_f32_16x16x32_bf16 v[16:19],a[36:39],v[84:87],v[16:19]
v_mfma_f32_16x16x32_bf16 v[20:23],a[40:43],v[84:87],v[20:23]
v_mfma_f32_16x16x32_bf16 v[24:27],a[44:47],v[84:87],v[24:27]
s_waitcnt vmcnt(18)
s_barrier
s_add_u32 m0,s88,24576
s_add_u32 s61,s82,3211264
buffer_load_dwordx4 v28,s[40:43],s61 offen offset:0  lds
s_add_u32 m0,s88,25600
ds_read_b64_tr_b16 a[72:73],v40 offset:24576
buffer_load_dwordx4 v29,s[40:43],s61 offen offset:0  lds
s_add_u32 m0,s88,32640
ds_read_b64_tr_b16 a[74:75],v40 offset:25088
buffer_load_dwordx4 v28,s[40:43],s61 offen offset:128  lds
s_add_u32 m0,s88,33664
ds_read_b64_tr_b16 a[76:77],v41 offset:24576
buffer_load_dwordx4 v29,s[40:43],s61 offen offset:128  lds
s_add_u32 m0,s88,40704
ds_read_b64_tr_b16 a[78:79],v41 offset:25088
buffer_load_dwordx4 v28,s[40:43],s61 offen offset:256  lds
s_add_u32 m0,s88,41728
ds_read_b64_tr_b16 a[80:81],v42 offset:24576
buffer_load_dwordx4 v29,s[40:43],s61 offen offset:256  lds
ds_read_b64_tr_b16 a[82:83],v42 offset:25088
ds_read_b64_tr_b16 a[84:85],v40 offset:28672
ds_read_b64_tr_b16 a[86:87],v40 offset:29184
ds_read_b64_tr_b16 a[88:89],v41 offset:28672
ds_read_b64_tr_b16 a[90:91],v41 offset:29184
ds_read_b64_tr_b16 a[92:93],v42 offset:28672
ds_read_b64_tr_b16 a[94:95],v42 offset:29184
s_waitcnt lgkmcnt(12)
v_mfma_f32_16x16x32_bf16 v[16:19],a[48:51],v[84:87],v[16:19]
v_mfma_f32_16x16x32_bf16 v[20:23],a[52:55],v[84:87],v[20:23]
v_mfma_f32_16x16x32_bf16 v[24:27],a[56:59],v[84:87],v[24:27]
v_mfma_f32_16x16x32_bf16 v[16:19],a[60:63],v[84:87],v[16:19]
v_mfma_f32_16x16x32_bf16 v[20:23],a[64:67],v[84:87],v[20:23]
v_mfma_f32_16x16x32_bf16 v[24:27],a[68:71],v[84:87],v[24:27]
s_waitcnt vmcnt(18)
s_barrier
s_add_u32 m0,s88,49152
s_add_u32 s61,s82,3670016
buffer_load_dwordx4 v28,s[40:43],s61 offen offset:0  lds
s_add_u32 m0,s88,50176
ds_read_b64_tr_b16 a[96:97],v43 offset:0
buffer_load_dwordx4 v29,s[40:43],s61 offen offset:0  lds
s_add_u32 m0,s88,57216
ds_read_b64_tr_b16 a[98:99],v43 offset:512
buffer_load_dwordx4 v28,s[40:43],s61 offen offset:128  lds
s_add_u32 m0,s88,58240
ds_read_b64_tr_b16 a[100:101],v44 offset:0
buffer_load_dwordx4 v29,s[40:43],s61 offen offset:128  lds
s_add_u32 m0,s88,65280
ds_read_b64_tr_b16 a[102:103],v44 offset:512
buffer_load_dwordx4 v28,s[40:43],s61 offen offset:256  lds
s_add_u32 m0,s88,66304
ds_read_b64_tr_b16 a[104:105],v45 offset:0
buffer_load_dwordx4 v29,s[40:43],s61 offen offset:256  lds
ds_read_b64_tr_b16 a[106:107],v45 offset:512
ds_read_b64_tr_b16 a[108:109],v43 offset:4096
ds_read_b64_tr_b16 a[110:111],v43 offset:4608
ds_read_b64_tr_b16 a[112:113],v44 offset:4096
ds_read_b64_tr_b16 a[114:115],v44 offset:4608
ds_read_b64_tr_b16 a[116:117],v45 offset:4096
ds_read_b64_tr_b16 a[118:119],v45 offset:4608
s_waitcnt lgkmcnt(12)
v_mfma_f32_16x16x32_bf16 v[16:19],a[72:75],v[84:87],v[16:19]
v_mfma_f32_16x16x32_bf16 v[20:23],a[76:79],v[84:87],v[20:23]
v_mfma_f32_16x16x32_bf16 v[24:27],a[80:83],v[84:87],v[24:27]
v_mfma_f32_16x16x32_bf16 v[16:19],a[84:87],v[84:87],v[16:19]
v_mfma_f32_16x16x32_bf16 v[20:23],a[88:91],v[84:87],v[20:23]
v_mfma_f32_16x16x32_bf16 v[24:27],a[92:95],v[84:87],v[24:27]
s_waitcnt vmcnt(18)
s_barrier
s_add_u32 m0,s88,73728
s_add_u32 s61,s82,4128768
buffer_load_dwordx4 v28,s[40:43],s61 offen offset:0  lds
s_add_u32 m0,s88,74752
ds_read_b64_tr_b16 a[120:121],v43 offset:24576
buffer_load_dwordx4 v29,s[40:43],s61 offen offset:0  lds
s_add_u32 m0,s88,81792
ds_read_b64_tr_b16 a[122:123],v43 offset:25088
buffer_load_dwordx4 v28,s[40:43],s61 offen offset:128  lds
s_add_u32 m0,s88,82816
ds_read_b64_tr_b16 a[124:125],v44 offset:24576
buffer_load_dwordx4 v29,s[40:43],s61 offen offset:128  lds
s_add_u32 m0,s88,89856
ds_read_b64_tr_b16 a[126:127],v44 offset:25088
buffer_load_dwordx4 v28,s[40:43],s61 offen offset:256  lds
s_add_u32 m0,s88,90880
ds_read_b64_tr_b16 a[128:129],v45 offset:24576
buffer_load_dwordx4 v29,s[40:43],s61 offen offset:256  lds
ds_read_b64_tr_b16 a[130:131],v45 offset:25088
ds_read_b64_tr_b16 a[132:133],v43 offset:28672
ds_read_b64_tr_b16 a[134:135],v43 offset:29184
ds_read_b64_tr_b16 a[136:137],v44 offset:28672
ds_read_b64_tr_b16 a[138:139],v44 offset:29184
ds_read_b64_tr_b16 a[140:141],v45 offset:28672
ds_read_b64_tr_b16 a[142:143],v45 offset:29184
s_waitcnt lgkmcnt(12)
v_mfma_f32_16x16x32_bf16 v[16:19],a[96:99],v[84:87],v[16:19]
v_mfma_f32_16x16x32_bf16 v[20:23],a[100:103],v[84:87],v[20:23]
v_mfma_f32_16x16x32_bf16 v[24:27],a[104:107],v[84:87],v[24:27]
v_mfma_f32_16x16x32_bf16 v[16:19],a[108:111],v[84:87],v[16:19]
v_mfma_f32_16x16x32_bf16 v[20:23],a[112:115],v[84:87],v[20:23]
v_mfma_f32_16x16x32_bf16 v[24:27],a[116:119],v[84:87],v[24:27]
s_waitcnt vmcnt(18)
s_barrier
s_add_u32 m0,s88,98304
s_add_u32 s61,s82,4587520
buffer_load_dwordx4 v28,s[40:43],s61 offen offset:0  lds
s_add_u32 m0,s88,99328
ds_read_b64_tr_b16 a[144:145],v30 offset:0
buffer_load_dwordx4 v29,s[40:43],s61 offen offset:0  lds
s_add_u32 m0,s88,106368
ds_read_b64_tr_b16 a[146:147],v30 offset:512
buffer_load_dwordx4 v28,s[40:43],s61 offen offset:128  lds
s_add_u32 m0,s88,107392
ds_read_b64_tr_b16 a[148:149],v31 offset:0
buffer_load_dwordx4 v29,s[40:43],s61 offen offset:128  lds
s_add_u32 m0,s88,114432
ds_read_b64_tr_b16 a[150:151],v31 offset:512
buffer_load_dwordx4 v28,s[40:43],s61 offen offset:256  lds
s_add_u32 m0,s88,115456
ds_read_b64_tr_b16 a[152:153],v32 offset:0
buffer_load_dwordx4 v29,s[40:43],s61 offen offset:256  lds
ds_read_b64_tr_b16 a[154:155],v32 offset:512
ds_read_b64_tr_b16 a[156:157],v30 offset:4096
ds_read_b64_tr_b16 a[158:159],v30 offset:4608
ds_read_b64_tr_b16 a[160:161],v31 offset:4096
ds_read_b64_tr_b16 a[162:163],v31 offset:4608
ds_read_b64_tr_b16 a[164:165],v32 offset:4096
ds_read_b64_tr_b16 a[166:167],v32 offset:4608
s_waitcnt lgkmcnt(12)
v_mfma_f32_16x16x32_bf16 v[16:19],a[120:123],v[84:87],v[16:19]
v_mfma_f32_16x16x32_bf16 v[20:23],a[124:127],v[84:87],v[20:23]
v_mfma_f32_16x16x32_bf16 v[24:27],a[128:131],v[84:87],v[24:27]
v_mfma_f32_16x16x32_bf16 v[16:19],a[132:135],v[84:87],v[16:19]
v_mfma_f32_16x16x32_bf16 v[20:23],a[136:139],v[84:87],v[20:23]
v_mfma_f32_16x16x32_bf16 v[24:27],a[140:143],v[84:87],v[24:27]
s_waitcnt vmcnt(18)
s_barrier
s_add_u32 m0,s88,122880
s_add_u32 s61,s82,5046272
buffer_load_dwordx4 v28,s[40:43],s61 offen offset:0  lds
s_add_u32 m0,s88,123904
ds_read_b64_tr_b16 a[168:169],v30 offset:24576
buffer_load_dwordx4 v29,s[40:43],s61 offen offset:0  lds
s_add_u32 m0,s88,130944
ds_read_b64_tr_b16 a[170:171],v30 offset:25088
buffer_load_dwordx4 v28,s[40:43],s61 offen offset:128  lds
s_add_u32 m0,s88,131968
ds_read_b64_tr_b16 a[172:173],v31 offset:24576
buffer_load_dwordx4 v29,s[40:43],s61 offen offset:128  lds
s_add_u32 m0,s88,139008
ds_read_b64_tr_b16 a[174:175],v31 offset:25088
buffer_load_dwordx4 v28,s[40:43],s61 offen offset:256  lds
s_add_u32 m0,s88,140032
ds_read_b64_tr_b16 a[176:177],v32 offset:24576
buffer_load_dwordx4 v29,s[40:43],s61 offen offset:256  lds
ds_read_b64_tr_b16 a[178:179],v32 offset:25088
ds_read_b64_tr_b16 a[180:181],v30 offset:28672
ds_read_b64_tr_b16 a[182:183],v30 offset:29184
ds_read_b64_tr_b16 a[184:185],v31 offset:28672
ds_read_b64_tr_b16 a[186:187],v31 offset:29184
ds_read_b64_tr_b16 a[188:189],v32 offset:28672
ds_read_b64_tr_b16 a[190:191],v32 offset:29184
s_waitcnt lgkmcnt(12)
v_mfma_f32_16x16x32_bf16 v[16:19],a[144:147],v[84:87],v[16:19]
v_mfma_f32_16x16x32_bf16 v[20:23],a[148:151],v[84:87],v[20:23]
v_mfma_f32_16x16x32_bf16 v[24:27],a[152:155],v[84:87],v[24:27]
v_mfma_f32_16x16x32_bf16 v[16:19],a[156:159],v[84:87],v[16:19]
v_mfma_f32_16x16x32_bf16 v[20:23],a[160:163],v[84:87],v[20:23]
v_mfma_f32_16x16x32_bf16 v[24:27],a[164:167],v[84:87],v[24:27]
s_waitcnt vmcnt(18)
s_barrier
s_add_u32 m0,s88,0
s_add_u32 s61,s82,5505024
buffer_load_dwordx4 v28,s[40:43],s61 offen offset:0  lds
s_add_u32 m0,s88,1024
ds_read_b64_tr_b16 a[192:193],v40 offset:0
buffer_load_dwordx4 v29,s[40:43],s61 offen offset:0  lds
s_add_u32 m0,s88,8064
ds_read_b64_tr_b16 a[194:195],v40 offset:512
buffer_load_dwordx4 v28,s[40:43],s61 offen offset:128  lds
s_add_u32 m0,s88,9088
ds_read_b64_tr_b16 a[196:197],v41 offset:0
buffer_load_dwordx4 v29,s[40:43],s61 offen offset:128  lds
s_add_u32 m0,s88,16128
ds_read_b64_tr_b16 a[198:199],v41 offset:512
buffer_load_dwordx4 v28,s[40:43],s61 offen offset:256  lds
s_add_u32 m0,s88,17152
ds_read_b64_tr_b16 a[200:201],v42 offset:0
buffer_load_dwordx4 v29,s[40:43],s61 offen offset:256  lds
ds_read_b64_tr_b16 a[202:203],v42 offset:512
ds_read_b64_tr_b16 a[204:205],v40 offset:4096)"
R"(
ds_read_b64_tr_b16 a[206:207],v40 offset:4608
ds_read_b64_tr_b16 a[208:209],v41 offset:4096
ds_read_b64_tr_b16 a[210:211],v41 offset:4608
ds_read_b64_tr_b16 a[212:213],v42 offset:4096
ds_read_b64_tr_b16 a[214:215],v42 offset:4608
s_waitcnt lgkmcnt(12)
v_mfma_f32_16x16x32_bf16 v[16:19],a[168:171],v[84:87],v[16:19]
v_mfma_f32_16x16x32_bf16 v[20:23],a[172:175],v[84:87],v[20:23]
v_mfma_f32_16x16x32_bf16 v[24:27],a[176:179],v[84:87],v[24:27]
v_mfma_f32_16x16x32_bf16 v[16:19],a[180:183],v[84:87],v[16:19]
v_mfma_f32_16x16x32_bf16 v[20:23],a[184:187],v[84:87],v[20:23]
v_mfma_f32_16x16x32_bf16 v[24:27],a[188:191],v[84:87],v[24:27]
s_waitcnt vmcnt(18)
s_barrier
s_add_u32 m0,s88,24576
s_add_u32 s61,s82,5963776
buffer_load_dwordx4 v28,s[40:43],s61 offen offset:0  lds
s_add_u32 m0,s88,25600
ds_read_b64_tr_b16 a[216:217],v40 offset:24576
buffer_load_dwordx4 v29,s[40:43],s61 offen offset:0  lds
s_add_u32 m0,s88,32640
ds_read_b64_tr_b16 a[218:219],v40 offset:25088
buffer_load_dwordx4 v28,s[40:43],s61 offen offset:128  lds
s_add_u32 m0,s88,33664
ds_read_b64_tr_b16 a[220:221],v41 offset:24576
buffer_load_dwordx4 v29,s[40:43],s61 offen offset:128  lds
s_add_u32 m0,s88,40704
ds_read_b64_tr_b16 a[222:223],v41 offset:25088
buffer_load_dwordx4 v28,s[40:43],s61 offen offset:256  lds
s_add_u32 m0,s88,41728
ds_read_b64_tr_b16 a[224:225],v42 offset:24576
buffer_load_dwordx4 v29,s[40:43],s61 offen offset:256  lds
ds_read_b64_tr_b16 a[226:227],v42 offset:25088
ds_read_b64_tr_b16 a[228:229],v40 offset:28672
ds_read_b64_tr_b16 a[230:231],v40 offset:29184
ds_read_b64_tr_b16 a[232:233],v41 offset:28672
ds_read_b64_tr_b16 a[234:235],v41 offset:29184
ds_read_b64_tr_b16 a[236:237],v42 offset:28672
ds_read_b64_tr_b16 a[238:239],v42 offset:29184
s_waitcnt lgkmcnt(12)
v_mfma_f32_16x16x32_bf16 v[16:19],a[192:195],v[84:87],v[16:19]
v_mfma_f32_16x16x32_bf16 v[20:23],a[196:199],v[84:87],v[20:23]
v_mfma_f32_16x16x32_bf16 v[24:27],a[200:203],v[84:87],v[24:27]
v_mfma_f32_16x16x32_bf16 v[16:19],a[204:207],v[84:87],v[16:19]
v_mfma_f32_16x16x32_bf16 v[20:23],a[208:211],v[84:87],v[20:23]
v_mfma_f32_16x16x32_bf16 v[24:27],a[212:215],v[84:87],v[24:27]
s_waitcnt vmcnt(18)
s_barrier
s_add_u32 m0,s88,49152
s_add_u32 s61,s82,6422528
buffer_load_dwordx4 v28,s[40:43],s61 offen offset:0  lds
s_add_u32 m0,s88,50176
ds_read_b64_tr_b16 a[240:241],v43 offset:0
buffer_load_dwordx4 v29,s[40:43],s61 offen offset:0  lds
s_add_u32 m0,s88,57216
ds_read_b64_tr_b16 a[242:243],v43 offset:512
buffer_load_dwordx4 v28,s[40:43],s61 offen offset:128  lds
s_add_u32 m0,s88,58240
ds_read_b64_tr_b16 a[244:245],v44 offset:0
buffer_load_dwordx4 v29,s[40:43],s61 offen offset:128  lds
s_add_u32 m0,s88,65280
ds_read_b64_tr_b16 a[246:247],v44 offset:512
buffer_load_dwordx4 v28,s[40:43],s61 offen offset:256  lds
s_add_u32 m0,s88,66304
ds_read_b64_tr_b16 a[248:249],v45 offset:0
buffer_load_dwordx4 v29,s[40:43],s61 offen offset:256  lds
ds_read_b64_tr_b16 a[250:251],v45 offset:512
ds_read_b64_tr_b16 a[252:253],v43 offset:4096
ds_read_b64_tr_b16 a[254:255],v43 offset:4608
ds_read_b64_tr_b16 v[128:129],v44 offset:4096
ds_read_b64_tr_b16 v[130:131],v44 offset:4608
ds_read_b64_tr_b16 v[132:133],v45 offset:4096
ds_read_b64_tr_b16 v[134:135],v45 offset:4608
s_waitcnt lgkmcnt(12)
v_mfma_f32_16x16x32_bf16 v[16:19],a[216:219],v[84:87],v[16:19]
v_mfma_f32_16x16x32_bf16 v[20:23],a[220:223],v[84:87],v[20:23]
v_mfma_f32_16x16x32_bf16 v[24:27],a[224:227],v[84:87],v[24:27]
v_mfma_f32_16x16x32_bf16 v[16:19],a[228:231],v[84:87],v[16:19]
v_mfma_f32_16x16x32_bf16 v[20:23],a[232:235],v[84:87],v[20:23]
v_mfma_f32_16x16x32_bf16 v[24:27],a[236:239],v[84:87],v[24:27]
s_waitcnt vmcnt(18)
s_barrier
s_add_u32 m0,s88,73728
s_add_u32 s61,s82,6881280
buffer_load_dwordx4 v28,s[40:43],s61 offen offset:0  lds
s_add_u32 m0,s88,74752
ds_read_b64_tr_b16 v[136:137],v43 offset:24576
buffer_load_dwordx4 v29,s[40:43],s61 offen offset:0  lds
s_add_u32 m0,s88,81792
ds_read_b64_tr_b16 v[138:139],v43 offset:25088
buffer_load_dwordx4 v28,s[40:43],s61 offen offset:128  lds
s_add_u32 m0,s88,82816
ds_read_b64_tr_b16 v[140:141],v44 offset:24576
buffer_load_dwordx4 v29,s[40:43],s61 offen offset:128  lds
s_add_u32 m0,s88,89856
ds_read_b64_tr_b16 v[142:143],v44 offset:25088
buffer_load_dwordx4 v28,s[40:43],s61 offen offset:256  lds
s_add_u32 m0,s88,90880
ds_read_b64_tr_b16 v[144:145],v45 offset:24576
buffer_load_dwordx4 v29,s[40:43],s61 offen offset:256  lds
ds_read_b64_tr_b16 v[146:147],v45 offset:25088
ds_read_b64_tr_b16 v[148:149],v43 offset:28672
ds_read_b64_tr_b16 v[150:151],v43 offset:29184
ds_read_b64_tr_b16 v[152:153],v44 offset:28672
ds_read_b64_tr_b16 v[154:155],v44 offset:29184
ds_read_b64_tr_b16 v[156:157],v45 offset:28672
ds_read_b64_tr_b16 v[158:159],v45 offset:29184
s_getpc_b64 s[46:47]
Lg3_cp_%=:
s_add_u32 s46,s46,Lg3_init_%=-Lg3_cp_%=
s_addc_u32 s47,s47,0
s_lshl_b32 s68,s54,13
v_lshl_add_u32 v68,v2,3,s68
v_min_u32 v69,Lg3_done_%=-Lg3_init_%=,v68
global_load_dword v72,v69,s[46:47]
s_waitcnt lgkmcnt(12)
v_mfma_f32_16x16x32_bf16 v[16:19],a[240:243],v[84:87],v[16:19]
v_mfma_f32_16x16x32_bf16 v[20:23],a[244:247],v[84:87],v[20:23]
v_mfma_f32_16x16x32_bf16 v[24:27],a[248:251],v[84:87],v[24:27]
v_mfma_f32_16x16x32_bf16 v[16:19],a[252:255],v[84:87],v[16:19]
v_mfma_f32_16x16x32_bf16 v[20:23],v[128:131],v[84:87],v[20:23]
v_mfma_f32_16x16x32_bf16 v[24:27],v[132:135],v[84:87],v[24:27]
s_waitcnt vmcnt(19)
s_barrier
ds_read_b64_tr_b16 v[160:161],v30 offset:0
ds_read_b64_tr_b16 v[162:163],v30 offset:512
ds_read_b64_tr_b16 v[164:165],v31 offset:0
ds_read_b64_tr_b16 v[166:167],v31 offset:512
ds_read_b64_tr_b16 v[168:169],v32 offset:0
ds_read_b64_tr_b16 v[170:171],v32 offset:512
ds_read_b64_tr_b16 v[172:173],v30 offset:4096
ds_read_b64_tr_b16 v[174:175],v30 offset:4608
ds_read_b64_tr_b16 v[176:177],v31 offset:4096
ds_read_b64_tr_b16 v[178:179],v31 offset:4608
ds_read_b64_tr_b16 v[180:181],v32 offset:4096
ds_read_b64_tr_b16 v[182:183],v32 offset:4608
s_waitcnt lgkmcnt(12)
v_mfma_f32_16x16x32_bf16 v[16:19],v[136:139],v[84:87],v[16:19]
v_mfma_f32_16x16x32_bf16 v[20:23],v[140:143],v[84:87],v[20:23]
v_mfma_f32_16x16x32_bf16 v[24:27],v[144:147],v[84:87],v[24:27]
v_mfma_f32_16x16x32_bf16 v[16:19],v[148:151],v[84:87],v[16:19]
v_mfma_f32_16x16x32_bf16 v[20:23],v[152:155],v[84:87],v[20:23]
v_mfma_f32_16x16x32_bf16 v[24:27],v[156:159],v[84:87],v[24:27]
s_waitcnt vmcnt(13)
s_barrier
ds_read_b64_tr_b16 v[184:185],v30 offset:24576
ds_read_b64_tr_b16 v[186:187],v30 offset:25088
ds_read_b64_tr_b16 v[188:189],v31 offset:24576
ds_read_b64_tr_b16 v[190:191],v31 offset:25088
ds_read_b64_tr_b16 v[192:193],v32 offset:24576
ds_read_b64_tr_b16 v[194:195],v32 offset:25088
ds_read_b64_tr_b16 v[196:197],v30 offset:28672
ds_read_b64_tr_b16 v[198:199],v30 offset:29184
ds_read_b64_tr_b16 v[200:201],v31 offset:28672
ds_read_b64_tr_b16 v[202:203],v31 offset:29184
ds_read_b64_tr_b16 v[204:205],v32 offset:28672
ds_read_b64_tr_b16 v[206:207],v32 offset:29184
s_waitcnt lgkmcnt(12)
v_mfma_f32_16x16x32_bf16 v[16:19],v[160:163],v[84:87],v[16:19]
v_mfma_f32_16x16x32_bf16 v[20:23],v[164:167],v[84:87],v[20:23]
v_mfma_f32_16x16x32_bf16 v[24:27],v[168:171],v[84:87],v[24:27]
v_mfma_f32_16x16x32_bf16 v[16:19],v[172:175],v[84:87],v[16:19]
v_mfma_f32_16x16x32_bf16 v[20:23],v[176:179],v[84:87],v[20:23]
v_mfma_f32_16x16x32_bf16 v[24:27],v[180:183],v[84:87],v[24:27]
s_waitcnt vmcnt(7)
s_barrier
ds_read_b64_tr_b16 v[208:209],v40 offset:0
ds_read_b64_tr_b16 v[210:211],v40 offset:512
ds_read_b64_tr_b16 v[212:213],v41 offset:0
ds_read_b64_tr_b16 v[214:215],v41 offset:512
ds_read_b64_tr_b16 v[216:217],v42 offset:0
ds_read_b64_tr_b16 v[218:219],v42 offset:512
ds_read_b64_tr_b16 v[220:221],v40 offset:4096
ds_read_b64_tr_b16 v[222:223],v40 offset:4608
ds_read_b64_tr_b16 v[224:225],v41 offset:4096
ds_read_b64_tr_b16 v[226:227],v41 offset:4608
ds_read_b64_tr_b16 v[228:229],v42 offset:4096
ds_read_b64_tr_b16 v[230:231],v42 offset:4608
s_waitcnt lgkmcnt(12)
v_mfma_f32_16x16x32_bf16 v[16:19],v[184:187],v[84:87],v[16:19]
v_mfma_f32_16x16x32_bf16 v[20:23],v[188:191],v[84:87],v[20:23]
v_mfma_f32_16x16x32_bf16 v[24:27],v[192:195],v[84:87],v[24:27]
v_mfma_f32_16x16x32_bf16 v[16:19],v[196:199],v[84:87],v[16:19]
v_mfma_f32_16x16x32_bf16 v[20:23],v[200:203],v[84:87],v[20:23]
v_mfma_f32_16x16x32_bf16 v[24:27],v[204:207],v[84:87],v[24:27]
s_waitcnt vmcnt(1)
s_barrier
ds_read_b64_tr_b16 v[232:233],v40 offset:24576
ds_read_b64_tr_b16 v[234:235],v40 offset:25088
ds_read_b64_tr_b16 v[236:237],v41 offset:24576
ds_read_b64_tr_b16 v[238:239],v41 offset:25088
ds_read_b64_tr_b16 v[240:241],v42 offset:24576
ds_read_b64_tr_b16 v[242:243],v42 offset:25088
ds_read_b64_tr_b16 v[244:245],v40 offset:28672
ds_read_b64_tr_b16 v[246:247],v40 offset:29184
ds_read_b64_tr_b16 v[248:249],v41 offset:28672
ds_read_b64_tr_b16 v[250:251],v41 offset:29184
ds_read_b64_tr_b16 v[252:253],v42 offset:28672
ds_read_b64_tr_b16 v[254:255],v42 offset:29184
s_waitcnt lgkmcnt(12))"
R"(
v_mfma_f32_16x16x32_bf16 v[16:19],v[208:211],v[84:87],v[16:19]
v_mfma_f32_16x16x32_bf16 v[20:23],v[212:215],v[84:87],v[20:23]
v_mfma_f32_16x16x32_bf16 v[24:27],v[216:219],v[84:87],v[24:27]
v_mfma_f32_16x16x32_bf16 v[16:19],v[220:223],v[84:87],v[16:19]
v_mfma_f32_16x16x32_bf16 v[20:23],v[224:227],v[84:87],v[20:23]
v_mfma_f32_16x16x32_bf16 v[24:27],v[228:231],v[84:87],v[24:27]
s_waitcnt lgkmcnt(0)
v_mfma_f32_16x16x32_bf16 v[16:19],v[232:235],v[84:87],v[16:19]
v_mfma_f32_16x16x32_bf16 v[20:23],v[236:239],v[84:87],v[20:23]
v_mfma_f32_16x16x32_bf16 v[24:27],v[240:243],v[84:87],v[24:27]
v_mfma_f32_16x16x32_bf16 v[16:19],v[244:247],v[84:87],v[16:19]
v_mfma_f32_16x16x32_bf16 v[20:23],v[248:251],v[84:87],v[20:23]
v_mfma_f32_16x16x32_bf16 v[24:27],v[252:255],v[84:87],v[24:27]
s_waitcnt lgkmcnt(0)
s_barrier
Lg3_init_%=:
v_mov_b32 v5,s60
s_mov_b32 s59,536870912
s_add_u32 s57,s56,131072
s_mov_b32 s79,0
s_mov_b32 s75,0x3a800000
s_mov_b32 s76,0xc0135761
s_mov_b32 s77,0x3d372713
s_mov_b32 s44,s59
s_mov_b32 s45,s59
s_mov_b32 s46,s59
s_mov_b32 s47,s59
s_mov_b32 s48,s83
s_add_u32 s49,s48,s84
s_add_u32 s50,s49,s55
s_cmp_eq_u32 s50,s56
s_cselect_b32 s50,s57,s50
s_cmp_eq_u32 s50,s58
s_cselect_b32 s50,s59,s50
s_add_u32 s51,s50,s55
s_cmp_eq_u32 s51,s56
s_cselect_b32 s51,s57,s51
s_cmp_eq_u32 s51,s58
s_cselect_b32 s51,s59,s51
s_add_u32 s52,s51,s55
s_cmp_eq_u32 s52,s56
s_cselect_b32 s52,s57,s52
s_cmp_eq_u32 s52,s58
s_cselect_b32 s52,s59,s52
s_lshl_b32 s63,s54,12
s_add_u32 s63,s63,65536
s_add_u32 s64,s63,32768
s_lshl_b32 s88,s54,10
s_add_u32 s88,s88,131072
s_lshl_b32 s78,s54,11
s_add_u32 s78,s78,132608
v_add_u32 v1,16384,v0
v_mov_b32 v12,0x3727c5ac
s_getpc_b64 s[90:91]
Lg3_pc_%=:
s_mov_b32 s68,Lg3_d1w0_%=-Lg3_pc_%=
s_cmp_eq_u32 s54,1
s_cselect_b32 s68,Lg3_d1w1_%=-Lg3_pc_%=,s68
s_cmp_eq_u32 s54,2
s_cselect_b32 s68,Lg3_d1w2_%=-Lg3_pc_%=,s68
s_cmp_eq_u32 s54,3
s_cselect_b32 s68,Lg3_d1w3_%=-Lg3_pc_%=,s68
s_add_u32 s92,s90,s68
s_addc_u32 s93,s91,0
s_mov_b32 s68,Lg3_d0w0_%=-Lg3_pc_%=
s_cmp_eq_u32 s54,1
s_cselect_b32 s68,Lg3_d0w1_%=-Lg3_pc_%=,s68
s_cmp_eq_u32 s54,2
s_cselect_b32 s68,Lg3_d0w2_%=-Lg3_pc_%=,s68
s_cmp_eq_u32 s54,3
s_cselect_b32 s68,Lg3_d0w3_%=-Lg3_pc_%=,s68
s_add_u32 s90,s90,s68
s_addc_u32 s91,s91,0
s_mov_b32 s66,s59
s_mov_b32 s65,0
s_mov_b32 s62,s63
s_branch Lg3_slow0_%=
Lg3_d0w0_%=:
v_mfma_f32_16x16x32_bf16 v[28:31],a[0:3],v[52:55],0
s_mov_b32 m0,s62
ds_read_b128 v[72:75],v2 offset:5120
v_mul_f32 v117,s76,v112
v_mfma_f32_16x16x32_bf16 v[32:35],a[4:7],v[52:55],0
buffer_load_dwordx4 v0,s[36:39],s61 offen offset:0 sc1 lds
v_mfma_f32_16x16x32_bf16 v[36:39],a[8:11],v[52:55],0
s_mul_i32 s73,s47,7
v_mul_f32 v118,v112,v112
v_mul_f32 v119,v113,v112
v_mfma_f32_16x16x32_bf16 v[28:31],a[12:15],v[56:59],v[28:31]
s_lshr_b32 s73,s73,1
ds_read_b128 v[76:79],v2 offset:6144
v_mul_f32 v118,s77,v118
v_mfma_f32_16x16x32_bf16 v[32:35],a[16:19],v[56:59],v[32:35]
s_add_u32 s53,s52,s55
v_mul_f32 v118,v118,v117
v_fma_f32 v40,-v116,v16,v40
v_mfma_f32_16x16x32_bf16 v[36:39],a[20:23],v[56:59],v[36:39]
s_waitcnt lgkmcnt(3)
buffer_load_dwordx4 v0,s[36:39],s61 offen offset:1024 sc1 lds
v_mfma_f32_16x16x32_bf16 v[28:31],a[24:27],v[60:63],v[28:31]
s_cmp_eq_u32 s53,s56
ds_read_b128 v[80:83],v2 offset:7168
v_fma_f32 v41,-v116,v17,v41
v_mfma_f32_16x16x32_bf16 v[32:35],a[28:31],v[60:63],v[32:35]
s_cselect_b32 s53,s57,s53
v_fma_f32 v42,-v116,v18,v42
v_fma_f32 v43,-v116,v19,v43
v_mfma_f32_16x16x32_bf16 v[36:39],a[32:35],v[60:63],v[36:39]
s_cmp_eq_u32 s53,s58
v_mul_f32 v84,v40,v40
v_mul_f32 v85,v41,v41
v_mfma_f32_16x16x32_bf16 v[28:31],a[36:39],v[64:67],v[28:31]
buffer_load_dwordx4 v0,s[36:39],s61 offen offset:2048 sc1 lds
v_mfma_f32_16x16x32_bf16 v[32:35],a[40:43],v[64:67],v[32:35]
s_cselect_b32 s53,s59,s53
ds_read_b128 v[52:55],v2 offset:8192
v_mul_f32 v86,v42,v42
v_mfma_f32_16x16x32_bf16 v[36:39],a[44:47],v[64:67],v[36:39]
s_waitcnt lgkmcnt(3)
v_mul_f32 v87,v43,v43
v_fma_f32 v84,v118,v84,v117
v_mfma_f32_16x16x32_bf16 v[28:31],a[48:51],v[68:71],v[28:31]
s_cmp_ge_u32 s66,s59
ds_read_b128 v[56:59],v2 offset:9216
v_mul_f32 v84,v40,v84
v_mfma_f32_16x16x32_bf16 v[32:35],a[52:55],v[68:71],v[32:35]
buffer_load_dwordx4 v0,s[36:39],s61 offen offset:3072 sc1 lds
v_mfma_f32_16x16x32_bf16 v[36:39],a[56:59],v[68:71],v[36:39]
s_cselect_b32 s86,s49,s50
v_exp_f32 v84,v84
v_mfma_f32_16x16x32_bf16 v[28:31],a[60:63],v[72:75],v[28:31]
s_lshr_b32 s68,s86,5
ds_read_b128 v[60:63],v2 offset:10240
v_fma_f32 v84,v84,v119,v119
v_mfma_f32_16x16x32_bf16 v[32:35],a[64:67],v[72:75],v[32:35]
s_add_u32 m0,s78,0
v_rcp_f32 v84,v84
v_mfma_f32_16x16x32_bf16 v[36:39],a[68:71],v[72:75],v[36:39]
s_waitcnt lgkmcnt(3)
v_fma_f32 v85,v118,v85,v117
v_mul_f32 v85,v41,v85
v_mfma_f32_16x16x32_bf16 v[28:31],a[72:75],v[76:79],v[28:31]
ds_read_b128 v[64:67],v2 offset:11264
v_fma_f32 v86,v118,v86,v117
v_mfma_f32_16x16x32_bf16 v[32:35],a[76:79],v[76:79],v[32:35]
v_exp_f32 v85,v85
v_mfma_f32_16x16x32_bf16 v[36:39],a[80:83],v[76:79],v[36:39]
v_mul_f32 v86,v42,v86
v_fma_f32 v85,v85,v119,v119
v_mfma_f32_16x16x32_bf16 v[28:31],a[84:87],v[80:83],v[28:31]
ds_read_b128 v[68:71],v2 offset:12288
v_fma_f32 v87,v118,v87,v117
v_mfma_f32_16x16x32_bf16 v[32:35],a[88:91],v[80:83],v[32:35]
v_exp_f32 v86,v86
v_mfma_f32_16x16x32_bf16 v[36:39],a[92:95],v[80:83],v[36:39]
s_waitcnt lgkmcnt(3)
v_rcp_f32 v85,v85
v_mfma_f32_16x16x32_bf16 v[28:31],a[96:99],v[52:55],v[28:31]
ds_read_b128 v[72:75],v2 offset:13312
v_mul_f32 v87,v43,v87
v_mfma_f32_16x16x32_bf16 v[32:35],a[100:103],v[52:55],v[32:35]
v_exp_f32 v87,v87
v_mfma_f32_16x16x32_bf16 v[36:39],a[104:107],v[52:55],v[36:39]
v_fma_f32 v86,v86,v119,v119
v_fma_f32 v87,v87,v119,v119
v_mfma_f32_16x16x32_bf16 v[28:31],a[108:111],v[56:59],v[28:31]
ds_read_b128 v[76:79],v2 offset:14336
v_mul_f32 v40,v40,v84
v_mfma_f32_16x16x32_bf16 v[32:35],a[112:115],v[56:59],v[32:35]
v_rcp_f32 v86,v86
v_mfma_f32_16x16x32_bf16 v[36:39],a[116:119],v[56:59],v[36:39]
s_waitcnt lgkmcnt(3)
v_rcp_f32 v87,v87
v_mfma_f32_16x16x32_bf16 v[28:31],a[120:123],v[60:63],v[28:31]
ds_read_b128 v[80:83],v2 offset:15360
v_mul_f32 v41,v41,v85
v_mfma_f32_16x16x32_bf16 v[32:35],a[124:127],v[60:63],v[32:35]
s_waitcnt vmcnt(6)
v_mul_f32 v42,v42,v86
v_mul_f32 v43,v43,v87
v_mfma_f32_16x16x32_bf16 v[36:39],a[128:131],v[60:63],v[36:39]
s_barrier
v_mfma_f32_16x16x32_bf16 v[28:31],a[132:135],v[64:67],v[28:31]
ds_read_b128 v[52:55],v2 offset:16384
v_add_f32_dpp v104,v104,v104 quad_perm:[1,0,3,2] row_mask:0xf bank_mask:0xf
v_mfma_f32_16x16x32_bf16 v[32:35],a[136:139],v[64:67],v[32:35]
s_nop 0
v_add_f32_dpp v104,v104,v104 quad_perm:[2,3,0,1] row_mask:0xf bank_mask:0xf
v_cvt_pk_bf16_f32 v96,v40,v41
v_mfma_f32_16x16x32_bf16 v[36:39],a[140:143],v[64:67],v[36:39]
s_waitcnt lgkmcnt(3)
v_add_f32_dpp v104,v104,v104 row_half_mirror row_mask:0xf bank_mask:0xf
v_cvt_pk_bf16_f32 v97,v42,v43
v_mfma_f32_16x16x32_bf16 v[28:31],a[144:147],v[68:71],v[28:31]
ds_read_b128 v[56:59],v2 offset:17408
v_fma_f32 v44,-v116,v20,v44
v_mfma_f32_16x16x32_bf16 v[32:35],a[148:151],v[68:71],v[32:35]
v_fma_f32 v45,-v116,v21,v45
v_fma_f32 v46,-v116,v22,v46
v_mfma_f32_16x16x32_bf16 v[36:39],a[152:155],v[68:71],v[36:39])"
R"(
v_fma_f32 v47,-v116,v23,v47
v_mul_f32 v88,v44,v44
v_mfma_f32_16x16x32_bf16 v[28:31],a[156:159],v[72:75],v[28:31]
ds_read_b128 v[60:63],v2 offset:18432
v_mul_f32 v89,v45,v45
v_mfma_f32_16x16x32_bf16 v[32:35],a[160:163],v[72:75],v[32:35]
buffer_load_dword v6,s[36:39],s68 offen offset:0 sc1 lds
v_mfma_f32_16x16x32_bf16 v[36:39],a[164:167],v[72:75],v[36:39]
s_waitcnt lgkmcnt(3)
v_mul_f32 v90,v46,v46
v_mul_f32 v91,v47,v47
v_mfma_f32_16x16x32_bf16 v[28:31],a[168:171],v[76:79],v[28:31]
s_add_u32 m0,s62,16384
ds_read_b128 v[64:67],v2 offset:19456
v_fma_f32 v88,v118,v88,v117
v_mfma_f32_16x16x32_bf16 v[32:35],a[172:175],v[76:79],v[32:35]
buffer_load_dwordx4 v1,s[36:39],s61 offen offset:0 sc1 lds
v_mfma_f32_16x16x32_bf16 v[36:39],a[176:179],v[76:79],v[36:39]
s_lshr_b32 s72,s61,5
v_mul_f32 v88,v44,v88
v_fma_f32 v89,v118,v89,v117
v_mfma_f32_16x16x32_bf16 v[28:31],a[180:183],v[80:83],v[28:31]
s_mov_b32 s62,s63
ds_read_b128 v[68:71],v2 offset:20480
v_mul_f32 v89,v45,v89
v_mfma_f32_16x16x32_bf16 v[32:35],a[184:187],v[80:83],v[32:35]
ds_write_b32 v10,v104 offset:0
v_mfma_f32_16x16x32_bf16 v[36:39],a[188:191],v[80:83],v[36:39]
s_waitcnt lgkmcnt(4)
buffer_load_dwordx4 v1,s[36:39],s61 offen offset:1024 sc1 lds
v_mfma_f32_16x16x32_bf16 v[28:31],a[192:195],v[52:55],v[28:31]
ds_read_b128 v[72:75],v2 offset:21504
v_fma_f32 v90,v118,v90,v117
v_mfma_f32_16x16x32_bf16 v[32:35],a[196:199],v[52:55],v[32:35]
v_exp_f32 v88,v88
v_mfma_f32_16x16x32_bf16 v[36:39],a[200:203],v[52:55],v[36:39]
v_exp_f32 v89,v89
v_mfma_f32_16x16x32_bf16 v[28:31],a[204:207],v[56:59],v[28:31]
buffer_load_dwordx4 v1,s[36:39],s61 offen offset:2048 sc1 lds
v_mfma_f32_16x16x32_bf16 v[32:35],a[208:211],v[56:59],v[32:35]
ds_read_b128 v[76:79],v2 offset:22528
v_mul_f32 v90,v46,v90
v_mfma_f32_16x16x32_bf16 v[36:39],a[212:215],v[56:59],v[36:39]
s_waitcnt lgkmcnt(4)
v_exp_f32 v90,v90
v_mfma_f32_16x16x32_bf16 v[28:31],a[216:219],v[60:63],v[28:31]
ds_read_b128 v[80:83],v2 offset:23552
v_fma_f32 v88,v88,v119,v119
v_mfma_f32_16x16x32_bf16 v[32:35],a[220:223],v[60:63],v[32:35]
buffer_load_dwordx4 v1,s[36:39],s61 offen offset:3072 sc1 lds
v_mfma_f32_16x16x32_bf16 v[36:39],a[224:227],v[60:63],v[36:39]
buffer_load_dword v108,v8,s[36:39],s72 offen offset:0 sc1
v_mfma_f32_16x16x32_bf16 v[28:31],a[228:231],v[64:67],v[28:31]
ds_read_b128 v[52:55],v2 offset:24576
v_fma_f32 v89,v89,v119,v119
v_mfma_f32_16x16x32_bf16 v[32:35],a[232:235],v[64:67],v[32:35]
v_rcp_f32 v88,v88
v_mfma_f32_16x16x32_bf16 v[36:39],a[236:239],v[64:67],v[36:39]
s_waitcnt lgkmcnt(3)
v_rcp_f32 v89,v89
v_mfma_f32_16x16x32_bf16 v[28:31],a[240:243],v[68:71],v[28:31]
ds_read_b128 v[56:59],v2 offset:25600
v_fma_f32 v90,v90,v119,v119
v_mfma_f32_16x16x32_bf16 v[32:35],a[244:247],v[68:71],v[32:35]
v_rcp_f32 v90,v90
v_mfma_f32_16x16x32_bf16 v[36:39],a[248:251],v[68:71],v[36:39]
v_fma_f32 v91,v118,v91,v117
v_mul_f32 v91,v47,v91
v_mfma_f32_16x16x32_bf16 v[28:31],a[252:255],v[72:75],v[28:31]
ds_read_b128 v[60:63],v2 offset:26624
v_mul_f32 v44,v44,v88
v_mfma_f32_16x16x32_bf16 v[32:35],v[128:131],v[72:75],v[32:35]
v_exp_f32 v91,v91
v_mfma_f32_16x16x32_bf16 v[36:39],v[132:135],v[72:75],v[36:39]
s_waitcnt lgkmcnt(3)
v_fma_f32 v91,v91,v119,v119
v_mul_f32 v45,v45,v89
v_mfma_f32_16x16x32_bf16 v[28:31],v[136:139],v[76:79],v[28:31]
ds_read_b128 v[64:67],v2 offset:27648
v_mul_f32 v46,v46,v90
v_mfma_f32_16x16x32_bf16 v[32:35],v[140:143],v[76:79],v[32:35]
v_rcp_f32 v91,v91
v_mfma_f32_16x16x32_bf16 v[36:39],v[144:147],v[76:79],v[36:39]
v_mul_f32 v47,v47,v91
v_cvt_pk_bf16_f32 v98,v44,v45
v_mfma_f32_16x16x32_bf16 v[28:31],v[148:151],v[80:83],v[28:31]
ds_read_b128 v[68:71],v2 offset:28672
v_cvt_pk_bf16_f32 v99,v46,v47
v_mfma_f32_16x16x32_bf16 v[32:35],v[152:155],v[80:83],v[32:35]
v_fma_f32 v48,-v116,v24,v48
v_fma_f32 v49,-v116,v25,v49
v_mfma_f32_16x16x32_bf16 v[36:39],v[156:159],v[80:83],v[36:39]
s_waitcnt lgkmcnt(3)
v_fma_f32 v50,-v116,v26,v50
v_fma_f32 v51,-v116,v27,v51
v_mfma_f32_16x16x32_bf16 v[28:31],v[160:163],v[52:55],v[28:31]
ds_read_b128 v[72:75],v2 offset:29696
v_mul_f32 v92,v48,v48
v_mfma_f32_16x16x32_bf16 v[32:35],v[164:167],v[52:55],v[32:35]
v_mul_f32 v93,v49,v49
v_mul_f32 v94,v50,v50
v_mfma_f32_16x16x32_bf16 v[36:39],v[168:171],v[52:55],v[36:39]
v_mul_f32 v95,v51,v51
v_fma_f32 v92,v118,v92,v117
v_mfma_f32_16x16x32_bf16 v[28:31],v[172:175],v[56:59],v[28:31]
ds_read_b128 v[76:79],v2 offset:30720
v_mul_f32 v92,v48,v92
v_mfma_f32_16x16x32_bf16 v[32:35],v[176:179],v[56:59],v[32:35]
v_exp_f32 v92,v92
v_mfma_f32_16x16x32_bf16 v[36:39],v[180:183],v[56:59],v[36:39]
s_waitcnt lgkmcnt(3)
buffer_store_dwordx4 v[96:99],v3,s[36:39],s73 offen offset:0 sc1
v_fma_f32 v92,v92,v119,v119
v_rcp_f32 v92,v92
v_mfma_f32_16x16x32_bf16 v[28:31],v[184:187],v[60:63],v[28:31]
ds_read_b128 v[80:83],v2 offset:31744
v_fma_f32 v93,v118,v93,v117
v_mfma_f32_16x16x32_bf16 v[32:35],v[188:191],v[60:63],v[32:35]
s_waitcnt vmcnt(6)
v_mul_f32 v93,v49,v93
v_fma_f32 v94,v118,v94,v117
v_mfma_f32_16x16x32_bf16 v[36:39],v[192:195],v[60:63],v[36:39]
s_barrier
v_mfma_f32_16x16x32_bf16 v[28:31],v[196:199],v[64:67],v[28:31]
ds_read_b128 v[52:55],v2 offset:32768
ds_read_b32 v13,v7 offset:0
v_mfma_f32_16x16x32_bf16 v[32:35],v[200:203],v[64:67],v[32:35]
ds_read_b64 v[112:113],v11 offset:0
v_mul_f32 v94,v50,v94
v_mfma_f32_16x16x32_bf16 v[36:39],v[204:207],v[64:67],v[36:39]
s_waitcnt lgkmcnt(5)
v_exp_f32 v93,v93
v_mfma_f32_16x16x32_bf16 v[28:31],v[208:211],v[68:71],v[28:31]
ds_read_b128 v[56:59],v2 offset:33792
v_fma_f32 v93,v93,v119,v119
v_mfma_f32_16x16x32_bf16 v[32:35],v[212:215],v[68:71],v[32:35]
s_waitcnt lgkmcnt(2)
v_exp_f32 v94,v94
v_mfma_f32_16x16x32_bf16 v[36:39],v[216:219],v[68:71],v[36:39]
s_waitcnt lgkmcnt(1)
v_cmp_eq_u32 vcc,s60,v13
v_rcp_f32 v93,v93
v_fma_f32 v94,v94,v119,v119
v_mfma_f32_16x16x32_bf16 v[28:31],v[220:223],v[72:75],v[28:31]
s_cmp_eq_u32 vcc_lo,-1
ds_read_b128 v[60:63],v2 offset:34816
v_mul_f32 v116,s75,v112
v_mfma_f32_16x16x32_bf16 v[32:35],v[224:227],v[72:75],v[32:35]
s_cselect_b32 s67,s86,s59
v_rcp_f32 v94,v94
v_mfma_f32_16x16x32_bf16 v[36:39],v[228:231],v[72:75],v[36:39]
v_fma_f32 v95,v118,v95,v117
v_mul_f32 v95,v51,v95
v_mfma_f32_16x16x32_bf16 v[28:31],v[232:235],v[76:79],v[28:31]
s_lshr_b32 s61,s67,0
ds_read_b128 v[64:67],v2 offset:35840
v_mul_f32 v48,v48,v92
v_mfma_f32_16x16x32_bf16 v[32:35],v[236:239],v[76:79],v[32:35]
s_xor_b32 s63,s63,65536
v_exp_f32 v95,v95
v_mfma_f32_16x16x32_bf16 v[36:39],v[240:243],v[76:79],v[36:39]
s_cmp_ge_u32 s66,s59
v_fma_f32 v95,v95,v119,v119
v_rcp_f32 v95,v95
v_mfma_f32_16x16x32_bf16 v[28:31],v[244:247],v[80:83],v[28:31]
v_mul_f32 v49,v49,v93
v_mul_f32 v50,v50,v94
v_mul_f32 v51,v51,v95
v_cvt_pk_bf16_f32 v100,v48,v49
v_cvt_pk_bf16_f32 v101,v50,v51
v_mfma_f32_16x16x32_bf16 v[32:35],v[248:251],v[80:83],v[32:35]
s_waitcnt lgkmcnt(2)
ds_read_b128 v[68:71],v2 offset:36864
buffer_store_dwordx2 v[100:101],v9,s[36:39],s73 offen offset:0 sc1
v_fma_f32 v113,v113,s75,v12
v_fma_f32 v113,-v116,v116,v113
v_max_f32 v113,v113,v12
v_rsq_f32 v112,v113
v_mfma_f32_16x16x32_bf16 v[36:39],v[252:255],v[80:83],v[36:39]
s_cbranch_scc1 Lg3_slow1_%=
Lg3_d1w0_%=:
v_mfma_f32_16x16x32_bf16 v[40:43],a[0:3],v[52:55],0
s_mov_b32 m0,s62
ds_read_b128 v[72:75],v2 offset:37888
v_mul_f32 v117,s76,v112
v_mfma_f32_16x16x32_bf16 v[44:47],a[4:7],v[52:55],0
buffer_load_dwordx4 v0,s[36:39],s61 offen offset:0 sc1 lds
v_mfma_f32_16x16x32_bf16 v[48:51],a[8:11],v[52:55],0
s_mul_i32 s73,s48,7
v_mul_f32 v118,v112,v112
v_mul_f32 v119,v113,v112
v_mfma_f32_16x16x32_bf16 v[40:43],a[12:15],v[56:59],v[40:43]
s_lshr_b32 s73,s73,1
ds_read_b128 v[76:79],v2 offset:38912
v_mul_f32 v118,s77,v118
v_mfma_f32_16x16x32_bf16 v[44:47],a[16:19],v[56:59],v[44:47]
s_add_u32 s74,s53,s55
v_mul_f32 v118,v118,v117
v_fma_f32 v28,-v116,v16,v28
v_mfma_f32_16x16x32_bf16 v[48:51],a[20:23],v[56:59],v[48:51])"
R"(
s_waitcnt lgkmcnt(3)
buffer_load_dwordx4 v0,s[36:39],s61 offen offset:1024 sc1 lds
v_mfma_f32_16x16x32_bf16 v[40:43],a[24:27],v[60:63],v[40:43]
s_cmp_eq_u32 s74,s56
ds_read_b128 v[80:83],v2 offset:39936
v_fma_f32 v29,-v116,v17,v29
v_mfma_f32_16x16x32_bf16 v[44:47],a[28:31],v[60:63],v[44:47]
s_cselect_b32 s74,s57,s74
v_fma_f32 v30,-v116,v18,v30
v_fma_f32 v31,-v116,v19,v31
v_mfma_f32_16x16x32_bf16 v[48:51],a[32:35],v[60:63],v[48:51]
s_cmp_eq_u32 s74,s58
v_mul_f32 v84,v28,v28
v_mul_f32 v85,v29,v29
v_mfma_f32_16x16x32_bf16 v[40:43],a[36:39],v[64:67],v[40:43]
buffer_load_dwordx4 v0,s[36:39],s61 offen offset:2048 sc1 lds
v_mfma_f32_16x16x32_bf16 v[44:47],a[40:43],v[64:67],v[44:47]
s_cselect_b32 s74,s59,s74
ds_read_b128 v[52:55],v2 offset:40960
v_mul_f32 v86,v30,v30
v_mfma_f32_16x16x32_bf16 v[48:51],a[44:47],v[64:67],v[48:51]
s_waitcnt lgkmcnt(3)
v_mul_f32 v87,v31,v31
v_fma_f32 v84,v118,v84,v117
v_mfma_f32_16x16x32_bf16 v[40:43],a[48:51],v[68:71],v[40:43]
s_cmp_ge_u32 s67,s59
ds_read_b128 v[56:59],v2 offset:41984
v_mul_f32 v84,v28,v84
v_mfma_f32_16x16x32_bf16 v[44:47],a[52:55],v[68:71],v[44:47]
buffer_load_dwordx4 v0,s[36:39],s61 offen offset:3072 sc1 lds
v_mfma_f32_16x16x32_bf16 v[48:51],a[56:59],v[68:71],v[48:51]
s_cselect_b32 s86,s50,s51
v_exp_f32 v84,v84
v_mfma_f32_16x16x32_bf16 v[40:43],a[60:63],v[72:75],v[40:43]
s_lshr_b32 s68,s86,5
ds_read_b128 v[60:63],v2 offset:43008
v_fma_f32 v84,v84,v119,v119
v_mfma_f32_16x16x32_bf16 v[44:47],a[64:67],v[72:75],v[44:47]
s_add_u32 m0,s78,256
v_rcp_f32 v84,v84
v_mfma_f32_16x16x32_bf16 v[48:51],a[68:71],v[72:75],v[48:51]
s_waitcnt lgkmcnt(3)
v_fma_f32 v85,v118,v85,v117
v_mul_f32 v85,v29,v85
v_mfma_f32_16x16x32_bf16 v[40:43],a[72:75],v[76:79],v[40:43]
s_mov_b64 s[44:45],s[46:47]
ds_read_b128 v[64:67],v2 offset:44032
v_fma_f32 v86,v118,v86,v117
v_mfma_f32_16x16x32_bf16 v[44:47],a[76:79],v[76:79],v[44:47]
s_mov_b64 s[46:47],s[48:49]
v_exp_f32 v85,v85
v_mfma_f32_16x16x32_bf16 v[48:51],a[80:83],v[76:79],v[48:51]
s_mov_b64 s[48:49],s[50:51]
v_mul_f32 v86,v30,v86
v_fma_f32 v85,v85,v119,v119
v_mfma_f32_16x16x32_bf16 v[40:43],a[84:87],v[80:83],v[40:43]
s_mov_b64 s[50:51],s[52:53]
ds_read_b128 v[68:71],v2 offset:45056
v_fma_f32 v87,v118,v87,v117
v_mfma_f32_16x16x32_bf16 v[44:47],a[88:91],v[80:83],v[44:47]
s_mov_b32 s52,s74
v_exp_f32 v86,v86
v_mfma_f32_16x16x32_bf16 v[48:51],a[92:95],v[80:83],v[48:51]
s_waitcnt lgkmcnt(3)
v_rcp_f32 v85,v85
v_mfma_f32_16x16x32_bf16 v[40:43],a[96:99],v[52:55],v[40:43]
ds_read_b128 v[72:75],v2 offset:46080
v_mul_f32 v87,v31,v87
v_mfma_f32_16x16x32_bf16 v[44:47],a[100:103],v[52:55],v[44:47]
v_exp_f32 v87,v87
v_mfma_f32_16x16x32_bf16 v[48:51],a[104:107],v[52:55],v[48:51]
v_fma_f32 v86,v86,v119,v119
v_fma_f32 v87,v87,v119,v119
v_mfma_f32_16x16x32_bf16 v[40:43],a[108:111],v[56:59],v[40:43]
ds_read_b128 v[76:79],v2 offset:47104
v_mul_f32 v28,v28,v84
v_mfma_f32_16x16x32_bf16 v[44:47],a[112:115],v[56:59],v[44:47]
v_rcp_f32 v86,v86
v_mfma_f32_16x16x32_bf16 v[48:51],a[116:119],v[56:59],v[48:51]
s_waitcnt lgkmcnt(3)
v_rcp_f32 v87,v87
v_mfma_f32_16x16x32_bf16 v[40:43],a[120:123],v[60:63],v[40:43]
ds_read_b128 v[80:83],v2 offset:48128
v_mul_f32 v29,v29,v85
v_mfma_f32_16x16x32_bf16 v[44:47],a[124:127],v[60:63],v[44:47]
s_waitcnt vmcnt(6)
v_mul_f32 v30,v30,v86
v_mul_f32 v31,v31,v87
v_mfma_f32_16x16x32_bf16 v[48:51],a[128:131],v[60:63],v[48:51]
s_barrier
v_mfma_f32_16x16x32_bf16 v[40:43],a[132:135],v[64:67],v[40:43]
ds_read_b128 v[52:55],v2 offset:49152
v_add_f32_dpp v108,v108,v108 quad_perm:[1,0,3,2] row_mask:0xf bank_mask:0xf
v_mfma_f32_16x16x32_bf16 v[44:47],a[136:139],v[64:67],v[44:47]
s_nop 0
v_add_f32_dpp v108,v108,v108 quad_perm:[2,3,0,1] row_mask:0xf bank_mask:0xf
v_cvt_pk_bf16_f32 v96,v28,v29
v_mfma_f32_16x16x32_bf16 v[48:51],a[140:143],v[64:67],v[48:51]
s_waitcnt lgkmcnt(3)
v_add_f32_dpp v108,v108,v108 row_half_mirror row_mask:0xf bank_mask:0xf
v_cvt_pk_bf16_f32 v97,v30,v31
v_mfma_f32_16x16x32_bf16 v[40:43],a[144:147],v[68:71],v[40:43]
ds_read_b128 v[56:59],v2 offset:50176
v_fma_f32 v32,-v116,v20,v32
v_mfma_f32_16x16x32_bf16 v[44:47],a[148:151],v[68:71],v[44:47]
v_fma_f32 v33,-v116,v21,v33
v_fma_f32 v34,-v116,v22,v34
v_mfma_f32_16x16x32_bf16 v[48:51],a[152:155],v[68:71],v[48:51]
v_fma_f32 v35,-v116,v23,v35
v_mul_f32 v88,v32,v32
v_mfma_f32_16x16x32_bf16 v[40:43],a[156:159],v[72:75],v[40:43]
ds_read_b128 v[60:63],v2 offset:51200
v_mul_f32 v89,v33,v33
v_mfma_f32_16x16x32_bf16 v[44:47],a[160:163],v[72:75],v[44:47]
buffer_load_dword v6,s[36:39],s68 offen offset:0 sc1 lds
v_mfma_f32_16x16x32_bf16 v[48:51],a[164:167],v[72:75],v[48:51]
s_waitcnt lgkmcnt(3)
v_mul_f32 v90,v34,v34
v_mul_f32 v91,v35,v35
v_mfma_f32_16x16x32_bf16 v[40:43],a[168:171],v[76:79],v[40:43]
s_add_u32 m0,s62,16384
ds_read_b128 v[64:67],v2 offset:52224
v_fma_f32 v88,v118,v88,v117
v_mfma_f32_16x16x32_bf16 v[44:47],a[172:175],v[76:79],v[44:47]
buffer_load_dwordx4 v1,s[36:39],s61 offen offset:0 sc1 lds
v_mfma_f32_16x16x32_bf16 v[48:51],a[176:179],v[76:79],v[48:51]
s_lshr_b32 s72,s61,5
v_mul_f32 v88,v32,v88
v_fma_f32 v89,v118,v89,v117
v_mfma_f32_16x16x32_bf16 v[40:43],a[180:183],v[80:83],v[40:43]
s_mov_b32 s62,s64
ds_read_b128 v[68:71],v2 offset:53248
v_mul_f32 v89,v33,v89
v_mfma_f32_16x16x32_bf16 v[44:47],a[184:187],v[80:83],v[44:47]
ds_write_b32 v10,v108 offset:128
v_mfma_f32_16x16x32_bf16 v[48:51],a[188:191],v[80:83],v[48:51]
s_waitcnt lgkmcnt(4)
buffer_load_dwordx4 v1,s[36:39],s61 offen offset:1024 sc1 lds
v_mfma_f32_16x16x32_bf16 v[40:43],a[192:195],v[52:55],v[40:43]
ds_read_b128 v[72:75],v2 offset:54272
v_fma_f32 v90,v118,v90,v117
v_mfma_f32_16x16x32_bf16 v[44:47],a[196:199],v[52:55],v[44:47]
v_exp_f32 v88,v88
v_mfma_f32_16x16x32_bf16 v[48:51],a[200:203],v[52:55],v[48:51]
v_exp_f32 v89,v89
v_mfma_f32_16x16x32_bf16 v[40:43],a[204:207],v[56:59],v[40:43]
buffer_load_dwordx4 v1,s[36:39],s61 offen offset:2048 sc1 lds
v_mfma_f32_16x16x32_bf16 v[44:47],a[208:211],v[56:59],v[44:47]
ds_read_b128 v[76:79],v2 offset:55296
v_mul_f32 v90,v34,v90
v_mfma_f32_16x16x32_bf16 v[48:51],a[212:215],v[56:59],v[48:51]
s_waitcnt lgkmcnt(4)
v_exp_f32 v90,v90
v_mfma_f32_16x16x32_bf16 v[40:43],a[216:219],v[60:63],v[40:43]
ds_read_b128 v[80:83],v2 offset:56320
v_fma_f32 v88,v88,v119,v119
v_mfma_f32_16x16x32_bf16 v[44:47],a[220:223],v[60:63],v[44:47]
buffer_load_dwordx4 v1,s[36:39],s61 offen offset:3072 sc1 lds
v_mfma_f32_16x16x32_bf16 v[48:51],a[224:227],v[60:63],v[48:51]
buffer_load_dword v104,v8,s[36:39],s72 offen offset:0 sc1
v_mfma_f32_16x16x32_bf16 v[40:43],a[228:231],v[64:67],v[40:43]
ds_read_b128 v[52:55],v2 offset:57344
v_fma_f32 v89,v89,v119,v119
v_mfma_f32_16x16x32_bf16 v[44:47],a[232:235],v[64:67],v[44:47]
v_rcp_f32 v88,v88
v_mfma_f32_16x16x32_bf16 v[48:51],a[236:239],v[64:67],v[48:51]
s_waitcnt lgkmcnt(3)
v_rcp_f32 v89,v89
v_mfma_f32_16x16x32_bf16 v[40:43],a[240:243],v[68:71],v[40:43]
ds_read_b128 v[56:59],v2 offset:58368
v_fma_f32 v90,v90,v119,v119
v_mfma_f32_16x16x32_bf16 v[44:47],a[244:247],v[68:71],v[44:47]
v_rcp_f32 v90,v90
v_mfma_f32_16x16x32_bf16 v[48:51],a[248:251],v[68:71],v[48:51]
v_fma_f32 v91,v118,v91,v117
v_mul_f32 v91,v35,v91
v_mfma_f32_16x16x32_bf16 v[40:43],a[252:255],v[72:75],v[40:43]
ds_read_b128 v[60:63],v2 offset:59392
v_mul_f32 v32,v32,v88
v_mfma_f32_16x16x32_bf16 v[44:47],v[128:131],v[72:75],v[44:47]
v_exp_f32 v91,v91
v_mfma_f32_16x16x32_bf16 v[48:51],v[132:135],v[72:75],v[48:51]
s_waitcnt lgkmcnt(3)
v_fma_f32 v91,v91,v119,v119
v_mul_f32 v33,v33,v89
v_mfma_f32_16x16x32_bf16 v[40:43],v[136:139],v[76:79],v[40:43]
ds_read_b128 v[64:67],v2 offset:60416
v_mul_f32 v34,v34,v90
v_mfma_f32_16x16x32_bf16 v[44:47],v[140:143],v[76:79],v[44:47]
v_rcp_f32 v91,v91
v_mfma_f32_16x16x32_bf16 v[48:51],v[144:147],v[76:79],v[48:51]
v_mul_f32 v35,v35,v91
v_cvt_pk_bf16_f32 v98,v32,v33
v_mfma_f32_16x16x32_bf16 v[40:43],v[148:151],v[80:83],v[40:43]
ds_read_b128 v[68:71],v2 offset:61440
v_cvt_pk_bf16_f32 v99,v34,v35
v_mfma_f32_16x16x32_bf16 v[44:47],v[152:155],v[80:83],v[44:47]
v_fma_f32 v36,-v116,v24,v36
v_fma_f32 v37,-v116,v25,v37
v_mfma_f32_16x16x32_bf16 v[48:51],v[156:159],v[80:83],v[48:51])"
R"(
s_waitcnt lgkmcnt(3)
v_fma_f32 v38,-v116,v26,v38
v_fma_f32 v39,-v116,v27,v39
v_mfma_f32_16x16x32_bf16 v[40:43],v[160:163],v[52:55],v[40:43]
ds_read_b128 v[72:75],v2 offset:62464
v_mul_f32 v92,v36,v36
v_mfma_f32_16x16x32_bf16 v[44:47],v[164:167],v[52:55],v[44:47]
v_mul_f32 v93,v37,v37
v_mul_f32 v94,v38,v38
v_mfma_f32_16x16x32_bf16 v[48:51],v[168:171],v[52:55],v[48:51]
v_mul_f32 v95,v39,v39
v_fma_f32 v92,v118,v92,v117
v_mfma_f32_16x16x32_bf16 v[40:43],v[172:175],v[56:59],v[40:43]
ds_read_b128 v[76:79],v2 offset:63488
v_mul_f32 v92,v36,v92
v_mfma_f32_16x16x32_bf16 v[44:47],v[176:179],v[56:59],v[44:47]
v_exp_f32 v92,v92
v_mfma_f32_16x16x32_bf16 v[48:51],v[180:183],v[56:59],v[48:51]
s_waitcnt lgkmcnt(3)
buffer_store_dwordx4 v[96:99],v3,s[36:39],s73 offen offset:0 sc1
v_fma_f32 v92,v92,v119,v119
v_rcp_f32 v92,v92
v_mfma_f32_16x16x32_bf16 v[40:43],v[184:187],v[60:63],v[40:43]
ds_read_b128 v[80:83],v2 offset:64512
v_xor_b32 v2,65536,v2
v_mfma_f32_16x16x32_bf16 v[44:47],v[188:191],v[60:63],v[44:47]
s_waitcnt vmcnt(6)
v_fma_f32 v93,v118,v93,v117
v_mul_f32 v93,v37,v93
v_mfma_f32_16x16x32_bf16 v[48:51],v[192:195],v[60:63],v[48:51]
s_barrier
v_mfma_f32_16x16x32_bf16 v[40:43],v[196:199],v[64:67],v[40:43]
ds_read_b128 v[52:55],v2 offset:0
ds_read_b32 v13,v7 offset:256
v_mfma_f32_16x16x32_bf16 v[44:47],v[200:203],v[64:67],v[44:47]
ds_read_b64 v[112:113],v11 offset:128
v_fma_f32 v94,v118,v94,v117
v_mfma_f32_16x16x32_bf16 v[48:51],v[204:207],v[64:67],v[48:51]
s_waitcnt lgkmcnt(5)
v_exp_f32 v93,v93
v_mfma_f32_16x16x32_bf16 v[40:43],v[208:211],v[68:71],v[40:43]
ds_read_b128 v[56:59],v2 offset:1024
v_mul_f32 v94,v38,v94
v_mfma_f32_16x16x32_bf16 v[44:47],v[212:215],v[68:71],v[44:47]
s_waitcnt lgkmcnt(2)
v_exp_f32 v94,v94
v_mfma_f32_16x16x32_bf16 v[48:51],v[216:219],v[68:71],v[48:51]
s_waitcnt lgkmcnt(1)
v_cmp_eq_u32 vcc,s60,v13
v_fma_f32 v93,v93,v119,v119
v_rcp_f32 v93,v93
v_mfma_f32_16x16x32_bf16 v[40:43],v[220:223],v[72:75],v[40:43]
s_cmp_eq_u32 vcc_lo,-1
ds_read_b128 v[60:63],v2 offset:2048
v_fma_f32 v94,v94,v119,v119
v_mfma_f32_16x16x32_bf16 v[44:47],v[224:227],v[72:75],v[44:47]
s_cselect_b32 s66,s86,s59
v_rcp_f32 v94,v94
v_mfma_f32_16x16x32_bf16 v[48:51],v[228:231],v[72:75],v[48:51]
v_mul_f32 v116,s75,v112
v_fma_f32 v95,v118,v95,v117
v_mfma_f32_16x16x32_bf16 v[40:43],v[232:235],v[76:79],v[40:43]
s_lshr_b32 s61,s66,0
ds_read_b128 v[64:67],v2 offset:3072
v_mul_f32 v95,v39,v95
v_mfma_f32_16x16x32_bf16 v[44:47],v[236:239],v[76:79],v[44:47]
s_xor_b32 s64,s64,65536
v_exp_f32 v95,v95
v_mfma_f32_16x16x32_bf16 v[48:51],v[240:243],v[76:79],v[48:51]
s_cmp_lt_u32 s67,s59
v_fma_f32 v95,v95,v119,v119
v_rcp_f32 v95,v95
v_mfma_f32_16x16x32_bf16 v[40:43],v[244:247],v[80:83],v[40:43]
v_mul_f32 v36,v36,v92
v_mul_f32 v37,v37,v93
v_mul_f32 v38,v38,v94
v_mul_f32 v39,v39,v95
v_cvt_pk_bf16_f32 v100,v36,v37
v_cvt_pk_bf16_f32 v101,v38,v39
v_mfma_f32_16x16x32_bf16 v[44:47],v[248:251],v[80:83],v[44:47]
s_waitcnt lgkmcnt(2)
ds_read_b128 v[68:71],v2 offset:4096
buffer_store_dwordx2 v[100:101],v9,s[36:39],s73 offen offset:0 sc1
v_fma_f32 v113,v113,s75,v12
v_fma_f32 v113,-v116,v116,v113
v_max_f32 v113,v113,v12
v_rsq_f32 v112,v113
v_mfma_f32_16x16x32_bf16 v[48:51],v[252:255],v[80:83],v[48:51]
s_cbranch_scc1 Lg3_d0w0_%=
s_branch Lg3_slow0_%=
Lg3_d0w1_%=:
v_mfma_f32_16x16x32_bf16 v[28:31],a[0:3],v[52:55],0
s_mov_b32 m0,s62
ds_read_b128 v[72:75],v2 offset:5120
v_mul_f32 v117,s76,v112
v_mfma_f32_16x16x32_bf16 v[32:35],a[4:7],v[52:55],0
s_mul_i32 s73,s47,7
v_mul_f32 v118,v112,v112
v_mul_f32 v119,v113,v112
v_mfma_f32_16x16x32_bf16 v[36:39],a[8:11],v[52:55],0
buffer_load_dwordx4 v0,s[36:39],s61 offen offset:0 sc1 lds
v_mfma_f32_16x16x32_bf16 v[28:31],a[12:15],v[56:59],v[28:31]
s_lshr_b32 s73,s73,1
ds_read_b128 v[76:79],v2 offset:6144
v_mul_f32 v118,s77,v118
v_mfma_f32_16x16x32_bf16 v[32:35],a[16:19],v[56:59],v[32:35]
s_add_u32 s53,s52,s55
v_mul_f32 v118,v118,v117
v_fma_f32 v40,-v116,v16,v40
v_mfma_f32_16x16x32_bf16 v[36:39],a[20:23],v[56:59],v[36:39]
s_waitcnt lgkmcnt(3)
v_fma_f32 v41,-v116,v17,v41
v_fma_f32 v42,-v116,v18,v42
v_mfma_f32_16x16x32_bf16 v[28:31],a[24:27],v[60:63],v[28:31]
buffer_load_dwordx4 v0,s[36:39],s61 offen offset:1024 sc1 lds
v_mfma_f32_16x16x32_bf16 v[32:35],a[28:31],v[60:63],v[32:35]
s_cmp_eq_u32 s53,s56
ds_read_b128 v[80:83],v2 offset:7168
v_fma_f32 v43,-v116,v19,v43
v_mfma_f32_16x16x32_bf16 v[36:39],a[32:35],v[60:63],v[36:39]
s_cselect_b32 s53,s57,s53
v_mul_f32 v84,v40,v40
v_mul_f32 v85,v41,v41
v_mfma_f32_16x16x32_bf16 v[28:31],a[36:39],v[64:67],v[28:31]
s_cmp_eq_u32 s53,s58
ds_read_b128 v[52:55],v2 offset:8192
v_mul_f32 v86,v42,v42
v_mfma_f32_16x16x32_bf16 v[32:35],a[40:43],v[64:67],v[32:35]
buffer_load_dwordx4 v0,s[36:39],s61 offen offset:2048 sc1 lds
v_mfma_f32_16x16x32_bf16 v[36:39],a[44:47],v[64:67],v[36:39]
s_waitcnt lgkmcnt(3)
v_mul_f32 v87,v43,v43
v_fma_f32 v84,v118,v84,v117
v_mfma_f32_16x16x32_bf16 v[28:31],a[48:51],v[68:71],v[28:31]
s_cselect_b32 s53,s59,s53
ds_read_b128 v[56:59],v2 offset:9216
v_mul_f32 v84,v40,v84
v_mfma_f32_16x16x32_bf16 v[32:35],a[52:55],v[68:71],v[32:35]
s_cmp_ge_u32 s66,s59
v_exp_f32 v84,v84
v_mfma_f32_16x16x32_bf16 v[36:39],a[56:59],v[68:71],v[36:39]
buffer_load_dwordx4 v0,s[36:39],s61 offen offset:3072 sc1 lds
v_mfma_f32_16x16x32_bf16 v[28:31],a[60:63],v[72:75],v[28:31]
s_cselect_b32 s86,s49,s50
ds_read_b128 v[60:63],v2 offset:10240
v_fma_f32 v84,v84,v119,v119
v_mfma_f32_16x16x32_bf16 v[32:35],a[64:67],v[72:75],v[32:35]
s_add_u32 m0,s62,16384
v_rcp_f32 v84,v84
v_mfma_f32_16x16x32_bf16 v[36:39],a[68:71],v[72:75],v[36:39]
s_waitcnt lgkmcnt(3)
v_fma_f32 v85,v118,v85,v117
v_mul_f32 v85,v41,v85
v_mfma_f32_16x16x32_bf16 v[28:31],a[72:75],v[76:79],v[28:31]
s_lshr_b32 s72,s61,5
ds_read_b128 v[64:67],v2 offset:11264
v_fma_f32 v86,v118,v86,v117
v_mfma_f32_16x16x32_bf16 v[32:35],a[76:79],v[76:79],v[32:35]
s_mov_b32 s62,s63
v_exp_f32 v85,v85
v_mfma_f32_16x16x32_bf16 v[36:39],a[80:83],v[76:79],v[36:39]
v_mul_f32 v86,v42,v86
v_fma_f32 v85,v85,v119,v119
v_mfma_f32_16x16x32_bf16 v[28:31],a[84:87],v[80:83],v[28:31]
ds_read_b128 v[68:71],v2 offset:12288
v_fma_f32 v87,v118,v87,v117
v_mfma_f32_16x16x32_bf16 v[32:35],a[88:91],v[80:83],v[32:35]
v_exp_f32 v86,v86
v_mfma_f32_16x16x32_bf16 v[36:39],a[92:95],v[80:83],v[36:39]
s_waitcnt lgkmcnt(3)
v_rcp_f32 v85,v85
v_mfma_f32_16x16x32_bf16 v[28:31],a[96:99],v[52:55],v[28:31]
ds_read_b128 v[72:75],v2 offset:13312
v_mul_f32 v87,v43,v87
v_mfma_f32_16x16x32_bf16 v[32:35],a[100:103],v[52:55],v[32:35]
v_exp_f32 v87,v87
v_mfma_f32_16x16x32_bf16 v[36:39],a[104:107],v[52:55],v[36:39]
v_fma_f32 v86,v86,v119,v119
v_fma_f32 v87,v87,v119,v119
v_mfma_f32_16x16x32_bf16 v[28:31],a[108:111],v[56:59],v[28:31]
ds_read_b128 v[76:79],v2 offset:14336
v_mul_f32 v40,v40,v84
v_mfma_f32_16x16x32_bf16 v[32:35],a[112:115],v[56:59],v[32:35]
v_rcp_f32 v86,v86
v_mfma_f32_16x16x32_bf16 v[36:39],a[116:119],v[56:59],v[36:39]
s_waitcnt lgkmcnt(3)
v_rcp_f32 v87,v87
v_mfma_f32_16x16x32_bf16 v[28:31],a[120:123],v[60:63],v[28:31]
ds_read_b128 v[80:83],v2 offset:15360
v_mul_f32 v41,v41,v85
v_mfma_f32_16x16x32_bf16 v[32:35],a[124:127],v[60:63],v[32:35]
s_waitcnt vmcnt(6)
v_mul_f32 v42,v42,v86
v_mul_f32 v43,v43,v87
v_mfma_f32_16x16x32_bf16 v[36:39],a[128:131],v[60:63],v[36:39]
s_barrier
v_mfma_f32_16x16x32_bf16 v[28:31],a[132:135],v[64:67],v[28:31]
ds_read_b128 v[52:55],v2 offset:16384
v_add_f32_dpp v104,v104,v104 quad_perm:[1,0,3,2] row_mask:0xf bank_mask:0xf
v_mfma_f32_16x16x32_bf16 v[32:35],a[136:139],v[64:67],v[32:35])"
R"(
s_nop 0
v_add_f32_dpp v104,v104,v104 quad_perm:[2,3,0,1] row_mask:0xf bank_mask:0xf
v_cvt_pk_bf16_f32 v96,v40,v41
v_mfma_f32_16x16x32_bf16 v[36:39],a[140:143],v[64:67],v[36:39]
s_waitcnt lgkmcnt(3)
v_add_f32_dpp v104,v104,v104 row_half_mirror row_mask:0xf bank_mask:0xf
v_cvt_pk_bf16_f32 v97,v42,v43
v_mfma_f32_16x16x32_bf16 v[28:31],a[144:147],v[68:71],v[28:31]
ds_read_b128 v[56:59],v2 offset:17408
v_fma_f32 v44,-v116,v20,v44
v_mfma_f32_16x16x32_bf16 v[32:35],a[148:151],v[68:71],v[32:35]
v_fma_f32 v45,-v116,v21,v45
v_fma_f32 v46,-v116,v22,v46
v_mfma_f32_16x16x32_bf16 v[36:39],a[152:155],v[68:71],v[36:39]
v_fma_f32 v47,-v116,v23,v47
v_mul_f32 v88,v44,v44
v_mfma_f32_16x16x32_bf16 v[28:31],a[156:159],v[72:75],v[28:31]
ds_read_b128 v[60:63],v2 offset:18432
v_mul_f32 v89,v45,v45
v_mfma_f32_16x16x32_bf16 v[32:35],a[160:163],v[72:75],v[32:35]
v_mul_f32 v90,v46,v46
v_mul_f32 v91,v47,v47
v_mfma_f32_16x16x32_bf16 v[36:39],a[164:167],v[72:75],v[36:39]
s_waitcnt lgkmcnt(3)
v_fma_f32 v88,v118,v88,v117
v_mul_f32 v88,v44,v88
v_mfma_f32_16x16x32_bf16 v[28:31],a[168:171],v[76:79],v[28:31]
ds_read_b128 v[64:67],v2 offset:19456
v_fma_f32 v89,v118,v89,v117
v_mfma_f32_16x16x32_bf16 v[32:35],a[172:175],v[76:79],v[32:35]
v_exp_f32 v88,v88
v_mfma_f32_16x16x32_bf16 v[36:39],a[176:179],v[76:79],v[36:39]
buffer_load_dwordx4 v1,s[36:39],s61 offen offset:0 sc1 lds
v_mfma_f32_16x16x32_bf16 v[28:31],a[180:183],v[80:83],v[28:31]
ds_read_b128 v[68:71],v2 offset:20480
v_mul_f32 v89,v45,v89
v_mfma_f32_16x16x32_bf16 v[32:35],a[184:187],v[80:83],v[32:35]
ds_write_b32 v10,v104 offset:0
v_mfma_f32_16x16x32_bf16 v[36:39],a[188:191],v[80:83],v[36:39]
s_waitcnt lgkmcnt(4)
v_exp_f32 v89,v89
v_mfma_f32_16x16x32_bf16 v[28:31],a[192:195],v[52:55],v[28:31]
buffer_load_dwordx4 v1,s[36:39],s61 offen offset:1024 sc1 lds
v_mfma_f32_16x16x32_bf16 v[32:35],a[196:199],v[52:55],v[32:35]
ds_read_b128 v[72:75],v2 offset:21504
v_fma_f32 v88,v88,v119,v119
v_mfma_f32_16x16x32_bf16 v[36:39],a[200:203],v[52:55],v[36:39]
v_rcp_f32 v88,v88
v_mfma_f32_16x16x32_bf16 v[28:31],a[204:207],v[56:59],v[28:31]
ds_read_b128 v[76:79],v2 offset:22528
v_fma_f32 v89,v89,v119,v119
v_mfma_f32_16x16x32_bf16 v[32:35],a[208:211],v[56:59],v[32:35]
buffer_load_dwordx4 v1,s[36:39],s61 offen offset:2048 sc1 lds
v_mfma_f32_16x16x32_bf16 v[36:39],a[212:215],v[56:59],v[36:39]
s_waitcnt lgkmcnt(4)
v_rcp_f32 v89,v89
v_mfma_f32_16x16x32_bf16 v[28:31],a[216:219],v[60:63],v[28:31]
ds_read_b128 v[80:83],v2 offset:23552
v_fma_f32 v90,v118,v90,v117
v_mfma_f32_16x16x32_bf16 v[32:35],a[220:223],v[60:63],v[32:35]
v_mul_f32 v90,v46,v90
v_fma_f32 v91,v118,v91,v117
v_mfma_f32_16x16x32_bf16 v[36:39],a[224:227],v[60:63],v[36:39]
buffer_load_dwordx4 v1,s[36:39],s61 offen offset:3072 sc1 lds
v_mfma_f32_16x16x32_bf16 v[28:31],a[228:231],v[64:67],v[28:31]
buffer_load_dword v108,v8,s[36:39],s72 offen offset:0 sc1
v_mfma_f32_16x16x32_bf16 v[32:35],a[232:235],v[64:67],v[32:35]
ds_read_b128 v[52:55],v2 offset:24576
v_mul_f32 v91,v47,v91
v_mfma_f32_16x16x32_bf16 v[36:39],a[236:239],v[64:67],v[36:39]
s_waitcnt lgkmcnt(3)
v_exp_f32 v90,v90
v_mfma_f32_16x16x32_bf16 v[28:31],a[240:243],v[68:71],v[28:31]
ds_read_b128 v[56:59],v2 offset:25600
v_fma_f32 v90,v90,v119,v119
v_mfma_f32_16x16x32_bf16 v[32:35],a[244:247],v[68:71],v[32:35]
v_exp_f32 v91,v91
v_mfma_f32_16x16x32_bf16 v[36:39],a[248:251],v[68:71],v[36:39]
v_rcp_f32 v90,v90
v_mfma_f32_16x16x32_bf16 v[28:31],a[252:255],v[72:75],v[28:31]
ds_read_b128 v[60:63],v2 offset:26624
v_fma_f32 v91,v91,v119,v119
v_mfma_f32_16x16x32_bf16 v[32:35],v[128:131],v[72:75],v[32:35]
v_rcp_f32 v91,v91
v_mfma_f32_16x16x32_bf16 v[36:39],v[132:135],v[72:75],v[36:39]
s_waitcnt lgkmcnt(3)
v_mul_f32 v44,v44,v88
v_mul_f32 v45,v45,v89
v_mfma_f32_16x16x32_bf16 v[28:31],v[136:139],v[76:79],v[28:31]
ds_read_b128 v[64:67],v2 offset:27648
v_mul_f32 v46,v46,v90
v_mfma_f32_16x16x32_bf16 v[32:35],v[140:143],v[76:79],v[32:35]
v_mul_f32 v47,v47,v91
v_cvt_pk_bf16_f32 v98,v44,v45
v_mfma_f32_16x16x32_bf16 v[36:39],v[144:147],v[76:79],v[36:39]
v_cvt_pk_bf16_f32 v99,v46,v47
v_fma_f32 v48,-v116,v24,v48
v_mfma_f32_16x16x32_bf16 v[28:31],v[148:151],v[80:83],v[28:31]
ds_read_b128 v[68:71],v2 offset:28672
v_fma_f32 v49,-v116,v25,v49
v_mfma_f32_16x16x32_bf16 v[32:35],v[152:155],v[80:83],v[32:35]
v_fma_f32 v50,-v116,v26,v50
v_fma_f32 v51,-v116,v27,v51
v_mfma_f32_16x16x32_bf16 v[36:39],v[156:159],v[80:83],v[36:39]
s_waitcnt lgkmcnt(3)
v_mul_f32 v92,v48,v48
v_mul_f32 v93,v49,v49
v_mfma_f32_16x16x32_bf16 v[28:31],v[160:163],v[52:55],v[28:31]
ds_read_b128 v[72:75],v2 offset:29696
v_mul_f32 v94,v50,v50
v_mfma_f32_16x16x32_bf16 v[32:35],v[164:167],v[52:55],v[32:35]
v_mul_f32 v95,v51,v51
v_fma_f32 v92,v118,v92,v117
v_mfma_f32_16x16x32_bf16 v[36:39],v[168:171],v[52:55],v[36:39]
v_mul_f32 v92,v48,v92
v_fma_f32 v93,v118,v93,v117
v_mfma_f32_16x16x32_bf16 v[28:31],v[172:175],v[56:59],v[28:31]
buffer_store_dwordx4 v[96:99],v3,s[36:39],s73 offen offset:0 sc1
ds_read_b128 v[76:79],v2 offset:30720
v_exp_f32 v92,v92
v_mfma_f32_16x16x32_bf16 v[32:35],v[176:179],v[56:59],v[32:35]
v_mul_f32 v93,v49,v93
v_fma_f32 v92,v92,v119,v119
v_mfma_f32_16x16x32_bf16 v[36:39],v[180:183],v[56:59],v[36:39]
s_waitcnt lgkmcnt(3)
v_exp_f32 v93,v93
v_mfma_f32_16x16x32_bf16 v[28:31],v[184:187],v[60:63],v[28:31]
ds_read_b128 v[80:83],v2 offset:31744
v_fma_f32 v93,v93,v119,v119
v_mfma_f32_16x16x32_bf16 v[32:35],v[188:191],v[60:63],v[32:35]
s_waitcnt vmcnt(6)
v_rcp_f32 v92,v92
v_mfma_f32_16x16x32_bf16 v[36:39],v[192:195],v[60:63],v[36:39]
s_barrier
v_mfma_f32_16x16x32_bf16 v[28:31],v[196:199],v[64:67],v[28:31]
ds_read_b128 v[52:55],v2 offset:32768
ds_read_b32 v13,v7 offset:0
v_mfma_f32_16x16x32_bf16 v[32:35],v[200:203],v[64:67],v[32:35]
ds_read_b64 v[112:113],v11 offset:0
v_fma_f32 v94,v118,v94,v117
v_mfma_f32_16x16x32_bf16 v[36:39],v[204:207],v[64:67],v[36:39]
s_waitcnt lgkmcnt(5)
v_rcp_f32 v93,v93
v_mfma_f32_16x16x32_bf16 v[28:31],v[208:211],v[68:71],v[28:31]
ds_read_b128 v[56:59],v2 offset:33792
v_mul_f32 v94,v50,v94
v_mfma_f32_16x16x32_bf16 v[32:35],v[212:215],v[68:71],v[32:35]
s_waitcnt lgkmcnt(2)
v_exp_f32 v94,v94
v_mfma_f32_16x16x32_bf16 v[36:39],v[216:219],v[68:71],v[36:39]
s_waitcnt lgkmcnt(1)
v_cmp_eq_u32 vcc,s60,v13
v_fma_f32 v94,v94,v119,v119
v_rcp_f32 v94,v94
v_mfma_f32_16x16x32_bf16 v[28:31],v[220:223],v[72:75],v[28:31]
s_cmp_eq_u32 vcc_lo,-1
ds_read_b128 v[60:63],v2 offset:34816
v_mul_f32 v116,s75,v112
v_mfma_f32_16x16x32_bf16 v[32:35],v[224:227],v[72:75],v[32:35]
s_cselect_b32 s67,s86,s59
v_fma_f32 v95,v118,v95,v117
v_mul_f32 v95,v51,v95
v_mfma_f32_16x16x32_bf16 v[36:39],v[228:231],v[72:75],v[36:39]
v_exp_f32 v95,v95
v_mfma_f32_16x16x32_bf16 v[28:31],v[232:235],v[76:79],v[28:31]
s_lshr_b32 s61,s67,0
v_fma_f32 v95,v95,v119,v119
v_rcp_f32 v95,v95
v_mfma_f32_16x16x32_bf16 v[32:35],v[236:239],v[76:79],v[32:35]
s_xor_b32 s63,s63,65536
v_mul_f32 v48,v48,v92
v_mul_f32 v49,v49,v93
v_mul_f32 v50,v50,v94
v_mul_f32 v51,v51,v95
v_cvt_pk_bf16_f32 v100,v48,v49
v_cvt_pk_bf16_f32 v101,v50,v51
v_mfma_f32_16x16x32_bf16 v[36:39],v[240:243],v[76:79],v[36:39]
s_cmp_ge_u32 s66,s59
ds_read_b128 v[64:67],v2 offset:35840
buffer_store_dwordx2 v[100:101],v9,s[36:39],s73 offen offset:0 sc1
v_fma_f32 v113,v113,s75,v12
v_fma_f32 v113,-v116,v116,v113
v_max_f32 v113,v113,v12
v_mfma_f32_16x16x32_bf16 v[28:31],v[244:247],v[80:83],v[28:31]
ds_read_b128 v[68:71],v2 offset:36864
v_mfma_f32_16x16x32_bf16 v[32:35],v[248:251],v[80:83],v[32:35]
s_waitcnt lgkmcnt(3)
v_rsq_f32 v112,v113
v_mfma_f32_16x16x32_bf16 v[36:39],v[252:255],v[80:83],v[36:39]
s_cbranch_scc1 Lg3_slow1_%=
Lg3_d1w1_%=:
v_mfma_f32_16x16x32_bf16 v[40:43],a[0:3],v[52:55],0
s_mov_b32 m0,s62
ds_read_b128 v[72:75],v2 offset:37888
v_mul_f32 v117,s76,v112
v_mfma_f32_16x16x32_bf16 v[44:47],a[4:7],v[52:55],0
s_mul_i32 s73,s48,7
v_mul_f32 v118,v112,v112
v_mul_f32 v119,v113,v112
v_mfma_f32_16x16x32_bf16 v[48:51],a[8:11],v[52:55],0)"
R"(
buffer_load_dwordx4 v0,s[36:39],s61 offen offset:0 sc1 lds
v_mfma_f32_16x16x32_bf16 v[40:43],a[12:15],v[56:59],v[40:43]
s_lshr_b32 s73,s73,1
ds_read_b128 v[76:79],v2 offset:38912
v_mul_f32 v118,s77,v118
v_mfma_f32_16x16x32_bf16 v[44:47],a[16:19],v[56:59],v[44:47]
s_add_u32 s74,s53,s55
v_mul_f32 v118,v118,v117
v_fma_f32 v28,-v116,v16,v28
v_mfma_f32_16x16x32_bf16 v[48:51],a[20:23],v[56:59],v[48:51]
s_waitcnt lgkmcnt(3)
v_fma_f32 v29,-v116,v17,v29
v_fma_f32 v30,-v116,v18,v30
v_mfma_f32_16x16x32_bf16 v[40:43],a[24:27],v[60:63],v[40:43]
buffer_load_dwordx4 v0,s[36:39],s61 offen offset:1024 sc1 lds
v_mfma_f32_16x16x32_bf16 v[44:47],a[28:31],v[60:63],v[44:47]
s_cmp_eq_u32 s74,s56
ds_read_b128 v[80:83],v2 offset:39936
v_fma_f32 v31,-v116,v19,v31
v_mfma_f32_16x16x32_bf16 v[48:51],a[32:35],v[60:63],v[48:51]
s_cselect_b32 s74,s57,s74
v_mul_f32 v84,v28,v28
v_mul_f32 v85,v29,v29
v_mfma_f32_16x16x32_bf16 v[40:43],a[36:39],v[64:67],v[40:43]
s_cmp_eq_u32 s74,s58
ds_read_b128 v[52:55],v2 offset:40960
v_mul_f32 v86,v30,v30
v_mfma_f32_16x16x32_bf16 v[44:47],a[40:43],v[64:67],v[44:47]
buffer_load_dwordx4 v0,s[36:39],s61 offen offset:2048 sc1 lds
v_mfma_f32_16x16x32_bf16 v[48:51],a[44:47],v[64:67],v[48:51]
s_waitcnt lgkmcnt(3)
v_mul_f32 v87,v31,v31
v_fma_f32 v84,v118,v84,v117
v_mfma_f32_16x16x32_bf16 v[40:43],a[48:51],v[68:71],v[40:43]
s_cselect_b32 s74,s59,s74
ds_read_b128 v[56:59],v2 offset:41984
v_mul_f32 v84,v28,v84
v_mfma_f32_16x16x32_bf16 v[44:47],a[52:55],v[68:71],v[44:47]
s_cmp_ge_u32 s67,s59
v_exp_f32 v84,v84
v_mfma_f32_16x16x32_bf16 v[48:51],a[56:59],v[68:71],v[48:51]
buffer_load_dwordx4 v0,s[36:39],s61 offen offset:3072 sc1 lds
v_mfma_f32_16x16x32_bf16 v[40:43],a[60:63],v[72:75],v[40:43]
s_cselect_b32 s86,s50,s51
ds_read_b128 v[60:63],v2 offset:43008
v_fma_f32 v84,v84,v119,v119
v_mfma_f32_16x16x32_bf16 v[44:47],a[64:67],v[72:75],v[44:47]
s_add_u32 m0,s62,16384
v_rcp_f32 v84,v84
v_mfma_f32_16x16x32_bf16 v[48:51],a[68:71],v[72:75],v[48:51]
s_waitcnt lgkmcnt(3)
v_fma_f32 v85,v118,v85,v117
v_mul_f32 v85,v29,v85
v_mfma_f32_16x16x32_bf16 v[40:43],a[72:75],v[76:79],v[40:43]
s_lshr_b32 s72,s61,5
ds_read_b128 v[64:67],v2 offset:44032
v_fma_f32 v86,v118,v86,v117
v_mfma_f32_16x16x32_bf16 v[44:47],a[76:79],v[76:79],v[44:47]
s_mov_b32 s62,s64
v_exp_f32 v85,v85
v_mfma_f32_16x16x32_bf16 v[48:51],a[80:83],v[76:79],v[48:51]
s_mov_b64 s[44:45],s[46:47]
v_mul_f32 v86,v30,v86
v_fma_f32 v85,v85,v119,v119
v_mfma_f32_16x16x32_bf16 v[40:43],a[84:87],v[80:83],v[40:43]
s_mov_b64 s[46:47],s[48:49]
ds_read_b128 v[68:71],v2 offset:45056
v_fma_f32 v87,v118,v87,v117
v_mfma_f32_16x16x32_bf16 v[44:47],a[88:91],v[80:83],v[44:47]
s_mov_b64 s[48:49],s[50:51]
v_exp_f32 v86,v86
v_mfma_f32_16x16x32_bf16 v[48:51],a[92:95],v[80:83],v[48:51]
s_waitcnt lgkmcnt(3)
v_rcp_f32 v85,v85
v_mfma_f32_16x16x32_bf16 v[40:43],a[96:99],v[52:55],v[40:43]
s_mov_b64 s[50:51],s[52:53]
ds_read_b128 v[72:75],v2 offset:46080
v_mul_f32 v87,v31,v87
v_mfma_f32_16x16x32_bf16 v[44:47],a[100:103],v[52:55],v[44:47]
s_mov_b32 s52,s74
v_exp_f32 v87,v87
v_mfma_f32_16x16x32_bf16 v[48:51],a[104:107],v[52:55],v[48:51]
v_fma_f32 v86,v86,v119,v119
v_fma_f32 v87,v87,v119,v119
v_mfma_f32_16x16x32_bf16 v[40:43],a[108:111],v[56:59],v[40:43]
ds_read_b128 v[76:79],v2 offset:47104
v_mul_f32 v28,v28,v84
v_mfma_f32_16x16x32_bf16 v[44:47],a[112:115],v[56:59],v[44:47]
v_rcp_f32 v86,v86
v_mfma_f32_16x16x32_bf16 v[48:51],a[116:119],v[56:59],v[48:51]
s_waitcnt lgkmcnt(3)
v_rcp_f32 v87,v87
v_mfma_f32_16x16x32_bf16 v[40:43],a[120:123],v[60:63],v[40:43]
ds_read_b128 v[80:83],v2 offset:48128
v_mul_f32 v29,v29,v85
v_mfma_f32_16x16x32_bf16 v[44:47],a[124:127],v[60:63],v[44:47]
s_waitcnt vmcnt(6)
v_mul_f32 v30,v30,v86
v_mul_f32 v31,v31,v87
v_mfma_f32_16x16x32_bf16 v[48:51],a[128:131],v[60:63],v[48:51]
s_barrier
v_mfma_f32_16x16x32_bf16 v[40:43],a[132:135],v[64:67],v[40:43]
ds_read_b128 v[52:55],v2 offset:49152
v_add_f32_dpp v108,v108,v108 quad_perm:[1,0,3,2] row_mask:0xf bank_mask:0xf
v_mfma_f32_16x16x32_bf16 v[44:47],a[136:139],v[64:67],v[44:47]
s_nop 0
v_add_f32_dpp v108,v108,v108 quad_perm:[2,3,0,1] row_mask:0xf bank_mask:0xf
v_cvt_pk_bf16_f32 v96,v28,v29
v_mfma_f32_16x16x32_bf16 v[48:51],a[140:143],v[64:67],v[48:51]
s_waitcnt lgkmcnt(3)
v_add_f32_dpp v108,v108,v108 row_half_mirror row_mask:0xf bank_mask:0xf
v_cvt_pk_bf16_f32 v97,v30,v31
v_mfma_f32_16x16x32_bf16 v[40:43],a[144:147],v[68:71],v[40:43]
ds_read_b128 v[56:59],v2 offset:50176
v_fma_f32 v32,-v116,v20,v32
v_mfma_f32_16x16x32_bf16 v[44:47],a[148:151],v[68:71],v[44:47]
v_fma_f32 v33,-v116,v21,v33
v_fma_f32 v34,-v116,v22,v34
v_mfma_f32_16x16x32_bf16 v[48:51],a[152:155],v[68:71],v[48:51]
v_fma_f32 v35,-v116,v23,v35
v_mul_f32 v88,v32,v32
v_mfma_f32_16x16x32_bf16 v[40:43],a[156:159],v[72:75],v[40:43]
ds_read_b128 v[60:63],v2 offset:51200
v_mul_f32 v89,v33,v33
v_mfma_f32_16x16x32_bf16 v[44:47],a[160:163],v[72:75],v[44:47]
v_mul_f32 v90,v34,v34
v_mul_f32 v91,v35,v35
v_mfma_f32_16x16x32_bf16 v[48:51],a[164:167],v[72:75],v[48:51]
s_waitcnt lgkmcnt(3)
v_fma_f32 v88,v118,v88,v117
v_mul_f32 v88,v32,v88
v_mfma_f32_16x16x32_bf16 v[40:43],a[168:171],v[76:79],v[40:43]
ds_read_b128 v[64:67],v2 offset:52224
v_fma_f32 v89,v118,v89,v117
v_mfma_f32_16x16x32_bf16 v[44:47],a[172:175],v[76:79],v[44:47]
v_exp_f32 v88,v88
v_mfma_f32_16x16x32_bf16 v[48:51],a[176:179],v[76:79],v[48:51]
buffer_load_dwordx4 v1,s[36:39],s61 offen offset:0 sc1 lds
v_mfma_f32_16x16x32_bf16 v[40:43],a[180:183],v[80:83],v[40:43]
ds_read_b128 v[68:71],v2 offset:53248
v_mul_f32 v89,v33,v89
v_mfma_f32_16x16x32_bf16 v[44:47],a[184:187],v[80:83],v[44:47]
ds_write_b32 v10,v108 offset:128
v_mfma_f32_16x16x32_bf16 v[48:51],a[188:191],v[80:83],v[48:51]
s_waitcnt lgkmcnt(4)
v_exp_f32 v89,v89
v_mfma_f32_16x16x32_bf16 v[40:43],a[192:195],v[52:55],v[40:43]
buffer_load_dwordx4 v1,s[36:39],s61 offen offset:1024 sc1 lds
v_mfma_f32_16x16x32_bf16 v[44:47],a[196:199],v[52:55],v[44:47]
ds_read_b128 v[72:75],v2 offset:54272
v_fma_f32 v88,v88,v119,v119
v_mfma_f32_16x16x32_bf16 v[48:51],a[200:203],v[52:55],v[48:51]
v_rcp_f32 v88,v88
v_mfma_f32_16x16x32_bf16 v[40:43],a[204:207],v[56:59],v[40:43]
ds_read_b128 v[76:79],v2 offset:55296
v_fma_f32 v89,v89,v119,v119
v_mfma_f32_16x16x32_bf16 v[44:47],a[208:211],v[56:59],v[44:47]
buffer_load_dwordx4 v1,s[36:39],s61 offen offset:2048 sc1 lds
v_mfma_f32_16x16x32_bf16 v[48:51],a[212:215],v[56:59],v[48:51]
s_waitcnt lgkmcnt(4)
v_rcp_f32 v89,v89
v_mfma_f32_16x16x32_bf16 v[40:43],a[216:219],v[60:63],v[40:43]
ds_read_b128 v[80:83],v2 offset:56320
v_fma_f32 v90,v118,v90,v117
v_mfma_f32_16x16x32_bf16 v[44:47],a[220:223],v[60:63],v[44:47]
v_mul_f32 v90,v34,v90
v_fma_f32 v91,v118,v91,v117
v_mfma_f32_16x16x32_bf16 v[48:51],a[224:227],v[60:63],v[48:51]
buffer_load_dwordx4 v1,s[36:39],s61 offen offset:3072 sc1 lds
v_mfma_f32_16x16x32_bf16 v[40:43],a[228:231],v[64:67],v[40:43]
buffer_load_dword v104,v8,s[36:39],s72 offen offset:0 sc1
v_mfma_f32_16x16x32_bf16 v[44:47],a[232:235],v[64:67],v[44:47]
ds_read_b128 v[52:55],v2 offset:57344
v_mul_f32 v91,v35,v91
v_mfma_f32_16x16x32_bf16 v[48:51],a[236:239],v[64:67],v[48:51]
s_waitcnt lgkmcnt(3)
v_exp_f32 v90,v90
v_mfma_f32_16x16x32_bf16 v[40:43],a[240:243],v[68:71],v[40:43]
ds_read_b128 v[56:59],v2 offset:58368
v_fma_f32 v90,v90,v119,v119
v_mfma_f32_16x16x32_bf16 v[44:47],a[244:247],v[68:71],v[44:47]
v_exp_f32 v91,v91
v_mfma_f32_16x16x32_bf16 v[48:51],a[248:251],v[68:71],v[48:51]
v_rcp_f32 v90,v90
v_mfma_f32_16x16x32_bf16 v[40:43],a[252:255],v[72:75],v[40:43]
ds_read_b128 v[60:63],v2 offset:59392
v_fma_f32 v91,v91,v119,v119
v_mfma_f32_16x16x32_bf16 v[44:47],v[128:131],v[72:75],v[44:47]
v_rcp_f32 v91,v91
v_mfma_f32_16x16x32_bf16 v[48:51],v[132:135],v[72:75],v[48:51]
s_waitcnt lgkmcnt(3)
v_mul_f32 v32,v32,v88
v_mul_f32 v33,v33,v89
v_mfma_f32_16x16x32_bf16 v[40:43],v[136:139],v[76:79],v[40:43]
ds_read_b128 v[64:67],v2 offset:60416
v_mul_f32 v34,v34,v90
v_mfma_f32_16x16x32_bf16 v[44:47],v[140:143],v[76:79],v[44:47]
v_mul_f32 v35,v35,v91
v_cvt_pk_bf16_f32 v98,v32,v33
v_mfma_f32_16x16x32_bf16 v[48:51],v[144:147],v[76:79],v[48:51])"
R"(
v_cvt_pk_bf16_f32 v99,v34,v35
v_fma_f32 v36,-v116,v24,v36
v_mfma_f32_16x16x32_bf16 v[40:43],v[148:151],v[80:83],v[40:43]
ds_read_b128 v[68:71],v2 offset:61440
v_fma_f32 v37,-v116,v25,v37
v_mfma_f32_16x16x32_bf16 v[44:47],v[152:155],v[80:83],v[44:47]
v_fma_f32 v38,-v116,v26,v38
v_fma_f32 v39,-v116,v27,v39
v_mfma_f32_16x16x32_bf16 v[48:51],v[156:159],v[80:83],v[48:51]
s_waitcnt lgkmcnt(3)
v_mul_f32 v92,v36,v36
v_mul_f32 v93,v37,v37
v_mfma_f32_16x16x32_bf16 v[40:43],v[160:163],v[52:55],v[40:43]
ds_read_b128 v[72:75],v2 offset:62464
v_mul_f32 v94,v38,v38
v_mfma_f32_16x16x32_bf16 v[44:47],v[164:167],v[52:55],v[44:47]
v_mul_f32 v95,v39,v39
v_fma_f32 v92,v118,v92,v117
v_mfma_f32_16x16x32_bf16 v[48:51],v[168:171],v[52:55],v[48:51]
v_mul_f32 v92,v36,v92
v_fma_f32 v93,v118,v93,v117
v_mfma_f32_16x16x32_bf16 v[40:43],v[172:175],v[56:59],v[40:43]
buffer_store_dwordx4 v[96:99],v3,s[36:39],s73 offen offset:0 sc1
ds_read_b128 v[76:79],v2 offset:63488
v_exp_f32 v92,v92
v_mfma_f32_16x16x32_bf16 v[44:47],v[176:179],v[56:59],v[44:47]
v_mul_f32 v93,v37,v93
v_fma_f32 v92,v92,v119,v119
v_mfma_f32_16x16x32_bf16 v[48:51],v[180:183],v[56:59],v[48:51]
s_waitcnt lgkmcnt(3)
v_exp_f32 v93,v93
v_mfma_f32_16x16x32_bf16 v[40:43],v[184:187],v[60:63],v[40:43]
ds_read_b128 v[80:83],v2 offset:64512
v_fma_f32 v93,v93,v119,v119
v_mfma_f32_16x16x32_bf16 v[44:47],v[188:191],v[60:63],v[44:47]
s_waitcnt vmcnt(6)
v_xor_b32 v2,65536,v2
v_fma_f32 v94,v118,v94,v117
v_mfma_f32_16x16x32_bf16 v[48:51],v[192:195],v[60:63],v[48:51]
s_barrier
v_mfma_f32_16x16x32_bf16 v[40:43],v[196:199],v[64:67],v[40:43]
ds_read_b128 v[52:55],v2 offset:0
ds_read_b32 v13,v7 offset:256
v_mfma_f32_16x16x32_bf16 v[44:47],v[200:203],v[64:67],v[44:47]
ds_read_b64 v[112:113],v11 offset:128
v_fma_f32 v95,v118,v95,v117
v_mfma_f32_16x16x32_bf16 v[48:51],v[204:207],v[64:67],v[48:51]
s_waitcnt lgkmcnt(5)
v_rcp_f32 v92,v92
v_mfma_f32_16x16x32_bf16 v[40:43],v[208:211],v[68:71],v[40:43]
ds_read_b128 v[56:59],v2 offset:1024
v_mul_f32 v94,v38,v94
v_mfma_f32_16x16x32_bf16 v[44:47],v[212:215],v[68:71],v[44:47]
s_waitcnt lgkmcnt(2)
v_exp_f32 v94,v94
v_mfma_f32_16x16x32_bf16 v[48:51],v[216:219],v[68:71],v[48:51]
s_waitcnt lgkmcnt(1)
v_cmp_eq_u32 vcc,s60,v13
v_rcp_f32 v93,v93
v_mul_f32 v95,v39,v95
v_mfma_f32_16x16x32_bf16 v[40:43],v[220:223],v[72:75],v[40:43]
s_cmp_eq_u32 vcc_lo,-1
ds_read_b128 v[60:63],v2 offset:2048
v_fma_f32 v94,v94,v119,v119
v_mfma_f32_16x16x32_bf16 v[44:47],v[224:227],v[72:75],v[44:47]
s_cselect_b32 s66,s86,s59
v_exp_f32 v95,v95
v_mfma_f32_16x16x32_bf16 v[48:51],v[228:231],v[72:75],v[48:51]
v_rcp_f32 v94,v94
v_mfma_f32_16x16x32_bf16 v[40:43],v[232:235],v[76:79],v[40:43]
s_lshr_b32 s61,s66,0
v_fma_f32 v95,v95,v119,v119
v_rcp_f32 v95,v95
v_mfma_f32_16x16x32_bf16 v[44:47],v[236:239],v[76:79],v[44:47]
s_xor_b32 s64,s64,65536
v_mul_f32 v36,v36,v92
v_mul_f32 v37,v37,v93
v_mul_f32 v38,v38,v94
v_mul_f32 v39,v39,v95
v_cvt_pk_bf16_f32 v100,v36,v37
v_cvt_pk_bf16_f32 v101,v38,v39
v_mfma_f32_16x16x32_bf16 v[48:51],v[240:243],v[76:79],v[48:51]
s_cmp_lt_u32 s67,s59
ds_read_b128 v[64:67],v2 offset:3072
buffer_store_dwordx2 v[100:101],v9,s[36:39],s73 offen offset:0 sc1
v_mul_f32 v116,s75,v112
v_fma_f32 v113,v113,s75,v12
v_fma_f32 v113,-v116,v116,v113
v_mfma_f32_16x16x32_bf16 v[40:43],v[244:247],v[80:83],v[40:43]
ds_read_b128 v[68:71],v2 offset:4096
v_max_f32 v113,v113,v12
v_mfma_f32_16x16x32_bf16 v[44:47],v[248:251],v[80:83],v[44:47]
s_waitcnt lgkmcnt(3)
v_rsq_f32 v112,v113
v_mfma_f32_16x16x32_bf16 v[48:51],v[252:255],v[80:83],v[48:51]
s_cbranch_scc1 Lg3_d0w1_%=
s_branch Lg3_slow0_%=
Lg3_d0w2_%=:
v_mfma_f32_16x16x32_bf16 v[28:31],a[0:3],v[52:55],0
s_mov_b32 m0,s62
ds_read_b128 v[72:75],v2 offset:5120
v_mul_f32 v117,s76,v112
v_mfma_f32_16x16x32_bf16 v[32:35],a[4:7],v[52:55],0
s_mul_i32 s73,s47,7
v_mul_f32 v118,v112,v112
v_mul_f32 v119,v113,v112
v_mfma_f32_16x16x32_bf16 v[36:39],a[8:11],v[52:55],0
s_lshr_b32 s73,s73,1
v_mul_f32 v118,s77,v118
v_mul_f32 v118,v118,v117
v_mfma_f32_16x16x32_bf16 v[28:31],a[12:15],v[56:59],v[28:31]
buffer_load_dwordx4 v0,s[36:39],s61 offen offset:0 sc1 lds
v_mfma_f32_16x16x32_bf16 v[32:35],a[16:19],v[56:59],v[32:35]
s_add_u32 s53,s52,s55
ds_read_b128 v[76:79],v2 offset:6144
v_fma_f32 v40,-v116,v16,v40
v_mfma_f32_16x16x32_bf16 v[36:39],a[20:23],v[56:59],v[36:39]
s_waitcnt lgkmcnt(3)
v_fma_f32 v41,-v116,v17,v41
v_fma_f32 v42,-v116,v18,v42
v_mfma_f32_16x16x32_bf16 v[28:31],a[24:27],v[60:63],v[28:31]
s_cmp_eq_u32 s53,s56
ds_read_b128 v[80:83],v2 offset:7168
v_fma_f32 v43,-v116,v19,v43
v_mfma_f32_16x16x32_bf16 v[32:35],a[28:31],v[60:63],v[32:35]
buffer_load_dwordx4 v0,s[36:39],s61 offen offset:1024 sc1 lds
v_mfma_f32_16x16x32_bf16 v[36:39],a[32:35],v[60:63],v[36:39]
s_cselect_b32 s53,s57,s53
v_mul_f32 v84,v40,v40
v_mul_f32 v85,v41,v41
v_mfma_f32_16x16x32_bf16 v[28:31],a[36:39],v[64:67],v[28:31]
s_cmp_eq_u32 s53,s58
ds_read_b128 v[52:55],v2 offset:8192
v_mul_f32 v86,v42,v42
v_mfma_f32_16x16x32_bf16 v[32:35],a[40:43],v[64:67],v[32:35]
s_cselect_b32 s53,s59,s53
v_mul_f32 v87,v43,v43
v_fma_f32 v84,v118,v84,v117
v_mfma_f32_16x16x32_bf16 v[36:39],a[44:47],v[64:67],v[36:39]
s_waitcnt lgkmcnt(3)
buffer_load_dwordx4 v0,s[36:39],s61 offen offset:2048 sc1 lds
v_mfma_f32_16x16x32_bf16 v[28:31],a[48:51],v[68:71],v[28:31]
s_cmp_ge_u32 s66,s59
ds_read_b128 v[56:59],v2 offset:9216
v_mul_f32 v84,v40,v84
v_mfma_f32_16x16x32_bf16 v[32:35],a[52:55],v[68:71],v[32:35]
s_cselect_b32 s86,s49,s50
v_exp_f32 v84,v84
v_mfma_f32_16x16x32_bf16 v[36:39],a[56:59],v[68:71],v[36:39]
v_fma_f32 v84,v84,v119,v119
v_fma_f32 v85,v118,v85,v117
v_mfma_f32_16x16x32_bf16 v[28:31],a[60:63],v[72:75],v[28:31]
buffer_load_dwordx4 v0,s[36:39],s61 offen offset:3072 sc1 lds
v_mfma_f32_16x16x32_bf16 v[32:35],a[64:67],v[72:75],v[32:35]
s_add_u32 m0,s62,16384
ds_read_b128 v[60:63],v2 offset:10240
v_mul_f32 v85,v41,v85
v_mfma_f32_16x16x32_bf16 v[36:39],a[68:71],v[72:75],v[36:39]
s_waitcnt lgkmcnt(3)
v_exp_f32 v85,v85
v_mfma_f32_16x16x32_bf16 v[28:31],a[72:75],v[76:79],v[28:31]
s_lshr_b32 s72,s61,5
ds_read_b128 v[64:67],v2 offset:11264
v_fma_f32 v85,v85,v119,v119
v_mfma_f32_16x16x32_bf16 v[32:35],a[76:79],v[76:79],v[32:35]
s_mov_b32 s62,s63
v_rcp_f32 v84,v84
v_mfma_f32_16x16x32_bf16 v[36:39],a[80:83],v[76:79],v[36:39]
v_rcp_f32 v85,v85
v_mfma_f32_16x16x32_bf16 v[28:31],a[84:87],v[80:83],v[28:31]
ds_read_b128 v[68:71],v2 offset:12288
v_fma_f32 v86,v118,v86,v117
v_mfma_f32_16x16x32_bf16 v[32:35],a[88:91],v[80:83],v[32:35]
v_mul_f32 v86,v42,v86
v_fma_f32 v87,v118,v87,v117
v_mfma_f32_16x16x32_bf16 v[36:39],a[92:95],v[80:83],v[36:39]
s_waitcnt lgkmcnt(3)
v_exp_f32 v86,v86
v_mfma_f32_16x16x32_bf16 v[28:31],a[96:99],v[52:55],v[28:31]
ds_read_b128 v[72:75],v2 offset:13312
v_mul_f32 v87,v43,v87
v_mfma_f32_16x16x32_bf16 v[32:35],a[100:103],v[52:55],v[32:35]
v_exp_f32 v87,v87
v_mfma_f32_16x16x32_bf16 v[36:39],a[104:107],v[52:55],v[36:39]
v_fma_f32 v86,v86,v119,v119
v_fma_f32 v87,v87,v119,v119
v_mfma_f32_16x16x32_bf16 v[28:31],a[108:111],v[56:59],v[28:31]
ds_read_b128 v[76:79],v2 offset:14336
v_mul_f32 v40,v40,v84
v_mfma_f32_16x16x32_bf16 v[32:35],a[112:115],v[56:59],v[32:35]
v_rcp_f32 v86,v86
v_mfma_f32_16x16x32_bf16 v[36:39],a[116:119],v[56:59],v[36:39]
s_waitcnt lgkmcnt(3)
v_rcp_f32 v87,v87
v_mfma_f32_16x16x32_bf16 v[28:31],a[120:123],v[60:63],v[28:31]
ds_read_b128 v[80:83],v2 offset:15360
v_mul_f32 v41,v41,v85
v_mfma_f32_16x16x32_bf16 v[32:35],a[124:127],v[60:63],v[32:35]
s_waitcnt vmcnt(6)
v_mul_f32 v42,v42,v86)"
R"(
v_mul_f32 v43,v43,v87
v_mfma_f32_16x16x32_bf16 v[36:39],a[128:131],v[60:63],v[36:39]
s_barrier
v_mfma_f32_16x16x32_bf16 v[28:31],a[132:135],v[64:67],v[28:31]
ds_read_b128 v[52:55],v2 offset:16384
v_add_f32_dpp v104,v104,v104 quad_perm:[1,0,3,2] row_mask:0xf bank_mask:0xf
v_mfma_f32_16x16x32_bf16 v[32:35],a[136:139],v[64:67],v[32:35]
s_nop 0
v_add_f32_dpp v104,v104,v104 quad_perm:[2,3,0,1] row_mask:0xf bank_mask:0xf
v_cvt_pk_bf16_f32 v96,v40,v41
v_mfma_f32_16x16x32_bf16 v[36:39],a[140:143],v[64:67],v[36:39]
s_waitcnt lgkmcnt(3)
v_add_f32_dpp v104,v104,v104 row_half_mirror row_mask:0xf bank_mask:0xf
v_cvt_pk_bf16_f32 v97,v42,v43
v_mfma_f32_16x16x32_bf16 v[28:31],a[144:147],v[68:71],v[28:31]
ds_read_b128 v[56:59],v2 offset:17408
v_fma_f32 v44,-v116,v20,v44
v_mfma_f32_16x16x32_bf16 v[32:35],a[148:151],v[68:71],v[32:35]
v_fma_f32 v45,-v116,v21,v45
v_fma_f32 v46,-v116,v22,v46
v_mfma_f32_16x16x32_bf16 v[36:39],a[152:155],v[68:71],v[36:39]
v_fma_f32 v47,-v116,v23,v47
v_mul_f32 v88,v44,v44
v_mfma_f32_16x16x32_bf16 v[28:31],a[156:159],v[72:75],v[28:31]
ds_read_b128 v[60:63],v2 offset:18432
v_mul_f32 v89,v45,v45
v_mfma_f32_16x16x32_bf16 v[32:35],a[160:163],v[72:75],v[32:35]
v_mul_f32 v90,v46,v46
v_mul_f32 v91,v47,v47
v_mfma_f32_16x16x32_bf16 v[36:39],a[164:167],v[72:75],v[36:39]
s_waitcnt lgkmcnt(3)
v_fma_f32 v88,v118,v88,v117
v_mul_f32 v88,v44,v88
v_mfma_f32_16x16x32_bf16 v[28:31],a[168:171],v[76:79],v[28:31]
ds_read_b128 v[64:67],v2 offset:19456
v_fma_f32 v89,v118,v89,v117
v_mfma_f32_16x16x32_bf16 v[32:35],a[172:175],v[76:79],v[32:35]
v_exp_f32 v88,v88
v_mfma_f32_16x16x32_bf16 v[36:39],a[176:179],v[76:79],v[36:39]
v_mul_f32 v89,v45,v89
v_fma_f32 v88,v88,v119,v119
v_mfma_f32_16x16x32_bf16 v[28:31],a[180:183],v[80:83],v[28:31]
buffer_load_dwordx4 v1,s[36:39],s61 offen offset:0 sc1 lds
v_mfma_f32_16x16x32_bf16 v[32:35],a[184:187],v[80:83],v[32:35]
ds_write_b32 v10,v104 offset:0
v_mfma_f32_16x16x32_bf16 v[36:39],a[188:191],v[80:83],v[36:39]
s_waitcnt lgkmcnt(3)
ds_read_b128 v[68:71],v2 offset:20480
v_exp_f32 v89,v89
v_mfma_f32_16x16x32_bf16 v[28:31],a[192:195],v[52:55],v[28:31]
ds_read_b128 v[72:75],v2 offset:21504
v_fma_f32 v89,v89,v119,v119
v_mfma_f32_16x16x32_bf16 v[32:35],a[196:199],v[52:55],v[32:35]
buffer_load_dwordx4 v1,s[36:39],s61 offen offset:1024 sc1 lds
v_mfma_f32_16x16x32_bf16 v[36:39],a[200:203],v[52:55],v[36:39]
v_rcp_f32 v88,v88
v_mfma_f32_16x16x32_bf16 v[28:31],a[204:207],v[56:59],v[28:31]
ds_read_b128 v[76:79],v2 offset:22528
v_fma_f32 v90,v118,v90,v117
v_mfma_f32_16x16x32_bf16 v[32:35],a[208:211],v[56:59],v[32:35]
v_rcp_f32 v89,v89
v_mfma_f32_16x16x32_bf16 v[36:39],a[212:215],v[56:59],v[36:39]
s_waitcnt lgkmcnt(4)
buffer_load_dwordx4 v1,s[36:39],s61 offen offset:2048 sc1 lds
v_mfma_f32_16x16x32_bf16 v[28:31],a[216:219],v[60:63],v[28:31]
ds_read_b128 v[80:83],v2 offset:23552
v_mul_f32 v90,v46,v90
v_mfma_f32_16x16x32_bf16 v[32:35],a[220:223],v[60:63],v[32:35]
v_exp_f32 v90,v90
v_mfma_f32_16x16x32_bf16 v[36:39],a[224:227],v[60:63],v[36:39]
v_fma_f32 v90,v90,v119,v119
v_fma_f32 v91,v118,v91,v117
v_mfma_f32_16x16x32_bf16 v[28:31],a[228:231],v[64:67],v[28:31]
buffer_load_dwordx4 v1,s[36:39],s61 offen offset:3072 sc1 lds
v_mfma_f32_16x16x32_bf16 v[32:35],a[232:235],v[64:67],v[32:35]
buffer_load_dword v108,v8,s[36:39],s72 offen offset:0 sc1
v_mfma_f32_16x16x32_bf16 v[36:39],a[236:239],v[64:67],v[36:39]
s_waitcnt lgkmcnt(2)
ds_read_b128 v[52:55],v2 offset:24576
v_mul_f32 v91,v47,v91
v_mfma_f32_16x16x32_bf16 v[28:31],a[240:243],v[68:71],v[28:31]
ds_read_b128 v[56:59],v2 offset:25600
v_mul_f32 v44,v44,v88
v_mfma_f32_16x16x32_bf16 v[32:35],a[244:247],v[68:71],v[32:35]
v_exp_f32 v91,v91
v_mfma_f32_16x16x32_bf16 v[36:39],a[248:251],v[68:71],v[36:39]
v_rcp_f32 v90,v90
v_mfma_f32_16x16x32_bf16 v[28:31],a[252:255],v[72:75],v[28:31]
ds_read_b128 v[60:63],v2 offset:26624
v_fma_f32 v91,v91,v119,v119
v_mfma_f32_16x16x32_bf16 v[32:35],v[128:131],v[72:75],v[32:35]
v_rcp_f32 v91,v91
v_mfma_f32_16x16x32_bf16 v[36:39],v[132:135],v[72:75],v[36:39]
s_waitcnt lgkmcnt(3)
v_mul_f32 v45,v45,v89
v_mul_f32 v46,v46,v90
v_mfma_f32_16x16x32_bf16 v[28:31],v[136:139],v[76:79],v[28:31]
ds_read_b128 v[64:67],v2 offset:27648
v_mul_f32 v47,v47,v91
v_mfma_f32_16x16x32_bf16 v[32:35],v[140:143],v[76:79],v[32:35]
v_cvt_pk_bf16_f32 v98,v44,v45
v_cvt_pk_bf16_f32 v99,v46,v47
v_mfma_f32_16x16x32_bf16 v[36:39],v[144:147],v[76:79],v[36:39]
v_fma_f32 v48,-v116,v24,v48
v_fma_f32 v49,-v116,v25,v49
v_mfma_f32_16x16x32_bf16 v[28:31],v[148:151],v[80:83],v[28:31]
ds_read_b128 v[68:71],v2 offset:28672
v_fma_f32 v50,-v116,v26,v50
v_mfma_f32_16x16x32_bf16 v[32:35],v[152:155],v[80:83],v[32:35]
v_fma_f32 v51,-v116,v27,v51
v_mul_f32 v92,v48,v48
v_mfma_f32_16x16x32_bf16 v[36:39],v[156:159],v[80:83],v[36:39]
s_waitcnt lgkmcnt(3)
v_mul_f32 v93,v49,v49
v_mul_f32 v94,v50,v50
v_mfma_f32_16x16x32_bf16 v[28:31],v[160:163],v[52:55],v[28:31]
ds_read_b128 v[72:75],v2 offset:29696
v_mul_f32 v95,v51,v51
v_mfma_f32_16x16x32_bf16 v[32:35],v[164:167],v[52:55],v[32:35]
v_fma_f32 v92,v118,v92,v117
v_mul_f32 v92,v48,v92
v_mfma_f32_16x16x32_bf16 v[36:39],v[168:171],v[52:55],v[36:39]
buffer_store_dwordx4 v[96:99],v3,s[36:39],s73 offen offset:0 sc1
v_exp_f32 v92,v92
v_fma_f32 v93,v118,v93,v117
v_mfma_f32_16x16x32_bf16 v[28:31],v[172:175],v[56:59],v[28:31]
ds_read_b128 v[76:79],v2 offset:30720
v_mul_f32 v93,v49,v93
v_mfma_f32_16x16x32_bf16 v[32:35],v[176:179],v[56:59],v[32:35]
v_exp_f32 v93,v93
v_mfma_f32_16x16x32_bf16 v[36:39],v[180:183],v[56:59],v[36:39]
s_waitcnt lgkmcnt(3)
v_fma_f32 v92,v92,v119,v119
v_fma_f32 v93,v93,v119,v119
v_mfma_f32_16x16x32_bf16 v[28:31],v[184:187],v[60:63],v[28:31]
ds_read_b128 v[80:83],v2 offset:31744
v_fma_f32 v94,v118,v94,v117
v_mfma_f32_16x16x32_bf16 v[32:35],v[188:191],v[60:63],v[32:35]
s_waitcnt vmcnt(6)
v_rcp_f32 v92,v92
v_mfma_f32_16x16x32_bf16 v[36:39],v[192:195],v[60:63],v[36:39]
s_barrier
v_mfma_f32_16x16x32_bf16 v[28:31],v[196:199],v[64:67],v[28:31]
ds_read_b128 v[52:55],v2 offset:32768
ds_read_b32 v13,v7 offset:0
v_mfma_f32_16x16x32_bf16 v[32:35],v[200:203],v[64:67],v[32:35]
ds_read_b64 v[112:113],v11 offset:0
v_mul_f32 v94,v50,v94
v_mfma_f32_16x16x32_bf16 v[36:39],v[204:207],v[64:67],v[36:39]
s_waitcnt lgkmcnt(5)
v_exp_f32 v94,v94
v_mfma_f32_16x16x32_bf16 v[28:31],v[208:211],v[68:71],v[28:31]
ds_read_b128 v[56:59],v2 offset:33792
v_fma_f32 v94,v94,v119,v119
v_mfma_f32_16x16x32_bf16 v[32:35],v[212:215],v[68:71],v[32:35]
s_waitcnt lgkmcnt(2)
v_rcp_f32 v93,v93
v_mfma_f32_16x16x32_bf16 v[36:39],v[216:219],v[68:71],v[36:39]
s_waitcnt lgkmcnt(1)
v_cmp_eq_u32 vcc,s60,v13
v_rcp_f32 v94,v94
v_fma_f32 v95,v118,v95,v117
v_mfma_f32_16x16x32_bf16 v[28:31],v[220:223],v[72:75],v[28:31]
s_cmp_eq_u32 vcc_lo,-1
ds_read_b128 v[60:63],v2 offset:34816
v_mul_f32 v95,v51,v95
v_mfma_f32_16x16x32_bf16 v[32:35],v[224:227],v[72:75],v[32:35]
s_cselect_b32 s67,s86,s59
v_exp_f32 v95,v95
v_mfma_f32_16x16x32_bf16 v[36:39],v[228:231],v[72:75],v[36:39]
v_fma_f32 v95,v95,v119,v119
v_rcp_f32 v95,v95
v_mfma_f32_16x16x32_bf16 v[28:31],v[232:235],v[76:79],v[28:31]
s_lshr_b32 s61,s67,0
v_mul_f32 v48,v48,v92
v_mul_f32 v49,v49,v93
v_mul_f32 v50,v50,v94
v_mul_f32 v51,v51,v95
v_cvt_pk_bf16_f32 v100,v48,v49
v_cvt_pk_bf16_f32 v101,v50,v51
v_mfma_f32_16x16x32_bf16 v[32:35],v[236:239],v[76:79],v[32:35]
buffer_store_dwordx2 v[100:101],v9,s[36:39],s73 offen offset:0 sc1
ds_read_b128 v[64:67],v2 offset:35840
v_mul_f32 v116,s75,v112
v_fma_f32 v113,v113,s75,v12
v_mfma_f32_16x16x32_bf16 v[36:39],v[240:243],v[76:79],v[36:39]
s_xor_b32 s63,s63,65536
v_fma_f32 v113,-v116,v116,v113
v_max_f32 v113,v113,v12
v_mfma_f32_16x16x32_bf16 v[28:31],v[244:247],v[80:83],v[28:31]
s_cmp_ge_u32 s66,s59
ds_read_b128 v[68:71],v2 offset:36864
v_mfma_f32_16x16x32_bf16 v[32:35],v[248:251],v[80:83],v[32:35]
s_waitcnt lgkmcnt(3)
v_rsq_f32 v112,v113
v_mfma_f32_16x16x32_bf16 v[36:39],v[252:255],v[80:83],v[36:39]
s_cbranch_scc1 Lg3_slow1_%=
Lg3_d1w2_%=:
v_mfma_f32_16x16x32_bf16 v[40:43],a[0:3],v[52:55],0
s_mov_b32 m0,s62)"
R"(
ds_read_b128 v[72:75],v2 offset:37888
v_mul_f32 v117,s76,v112
v_mfma_f32_16x16x32_bf16 v[44:47],a[4:7],v[52:55],0
s_mul_i32 s73,s48,7
v_mul_f32 v118,v112,v112
v_mul_f32 v119,v113,v112
v_mfma_f32_16x16x32_bf16 v[48:51],a[8:11],v[52:55],0
s_lshr_b32 s73,s73,1
v_mul_f32 v118,s77,v118
v_mul_f32 v118,v118,v117
v_mfma_f32_16x16x32_bf16 v[40:43],a[12:15],v[56:59],v[40:43]
buffer_load_dwordx4 v0,s[36:39],s61 offen offset:0 sc1 lds
v_mfma_f32_16x16x32_bf16 v[44:47],a[16:19],v[56:59],v[44:47]
s_add_u32 s74,s53,s55
ds_read_b128 v[76:79],v2 offset:38912
v_fma_f32 v28,-v116,v16,v28
v_mfma_f32_16x16x32_bf16 v[48:51],a[20:23],v[56:59],v[48:51]
s_waitcnt lgkmcnt(3)
v_fma_f32 v29,-v116,v17,v29
v_fma_f32 v30,-v116,v18,v30
v_mfma_f32_16x16x32_bf16 v[40:43],a[24:27],v[60:63],v[40:43]
s_cmp_eq_u32 s74,s56
ds_read_b128 v[80:83],v2 offset:39936
v_fma_f32 v31,-v116,v19,v31
v_mfma_f32_16x16x32_bf16 v[44:47],a[28:31],v[60:63],v[44:47]
buffer_load_dwordx4 v0,s[36:39],s61 offen offset:1024 sc1 lds
v_mfma_f32_16x16x32_bf16 v[48:51],a[32:35],v[60:63],v[48:51]
s_cselect_b32 s74,s57,s74
v_mul_f32 v84,v28,v28
v_mul_f32 v85,v29,v29
v_mfma_f32_16x16x32_bf16 v[40:43],a[36:39],v[64:67],v[40:43]
s_cmp_eq_u32 s74,s58
ds_read_b128 v[52:55],v2 offset:40960
v_mul_f32 v86,v30,v30
v_mfma_f32_16x16x32_bf16 v[44:47],a[40:43],v[64:67],v[44:47]
s_cselect_b32 s74,s59,s74
v_mul_f32 v87,v31,v31
v_fma_f32 v84,v118,v84,v117
v_mfma_f32_16x16x32_bf16 v[48:51],a[44:47],v[64:67],v[48:51]
s_waitcnt lgkmcnt(3)
buffer_load_dwordx4 v0,s[36:39],s61 offen offset:2048 sc1 lds
v_mfma_f32_16x16x32_bf16 v[40:43],a[48:51],v[68:71],v[40:43]
s_cmp_ge_u32 s67,s59
ds_read_b128 v[56:59],v2 offset:41984
v_mul_f32 v84,v28,v84
v_mfma_f32_16x16x32_bf16 v[44:47],a[52:55],v[68:71],v[44:47]
s_cselect_b32 s86,s50,s51
v_exp_f32 v84,v84
v_mfma_f32_16x16x32_bf16 v[48:51],a[56:59],v[68:71],v[48:51]
s_mov_b64 s[44:45],s[46:47]
v_fma_f32 v84,v84,v119,v119
v_fma_f32 v85,v118,v85,v117
v_mfma_f32_16x16x32_bf16 v[40:43],a[60:63],v[72:75],v[40:43]
buffer_load_dwordx4 v0,s[36:39],s61 offen offset:3072 sc1 lds
v_mfma_f32_16x16x32_bf16 v[44:47],a[64:67],v[72:75],v[44:47]
s_add_u32 m0,s62,16384
ds_read_b128 v[60:63],v2 offset:43008
v_mul_f32 v85,v29,v85
v_mfma_f32_16x16x32_bf16 v[48:51],a[68:71],v[72:75],v[48:51]
s_waitcnt lgkmcnt(3)
v_exp_f32 v85,v85
v_mfma_f32_16x16x32_bf16 v[40:43],a[72:75],v[76:79],v[40:43]
s_lshr_b32 s72,s61,5
ds_read_b128 v[64:67],v2 offset:44032
v_fma_f32 v85,v85,v119,v119
v_mfma_f32_16x16x32_bf16 v[44:47],a[76:79],v[76:79],v[44:47]
s_mov_b32 s62,s64
v_rcp_f32 v84,v84
v_mfma_f32_16x16x32_bf16 v[48:51],a[80:83],v[76:79],v[48:51]
s_mov_b64 s[46:47],s[48:49]
v_rcp_f32 v85,v85
v_mfma_f32_16x16x32_bf16 v[40:43],a[84:87],v[80:83],v[40:43]
s_mov_b64 s[48:49],s[50:51]
ds_read_b128 v[68:71],v2 offset:45056
v_fma_f32 v86,v118,v86,v117
v_mfma_f32_16x16x32_bf16 v[44:47],a[88:91],v[80:83],v[44:47]
s_mov_b64 s[50:51],s[52:53]
v_mul_f32 v86,v30,v86
v_fma_f32 v87,v118,v87,v117
v_mfma_f32_16x16x32_bf16 v[48:51],a[92:95],v[80:83],v[48:51]
s_waitcnt lgkmcnt(3)
v_exp_f32 v86,v86
v_mfma_f32_16x16x32_bf16 v[40:43],a[96:99],v[52:55],v[40:43]
s_mov_b32 s52,s74
ds_read_b128 v[72:75],v2 offset:46080
v_mul_f32 v87,v31,v87
v_mfma_f32_16x16x32_bf16 v[44:47],a[100:103],v[52:55],v[44:47]
v_exp_f32 v87,v87
v_mfma_f32_16x16x32_bf16 v[48:51],a[104:107],v[52:55],v[48:51]
v_fma_f32 v86,v86,v119,v119
v_fma_f32 v87,v87,v119,v119
v_mfma_f32_16x16x32_bf16 v[40:43],a[108:111],v[56:59],v[40:43]
ds_read_b128 v[76:79],v2 offset:47104
v_mul_f32 v28,v28,v84
v_mfma_f32_16x16x32_bf16 v[44:47],a[112:115],v[56:59],v[44:47]
v_rcp_f32 v86,v86
v_mfma_f32_16x16x32_bf16 v[48:51],a[116:119],v[56:59],v[48:51]
s_waitcnt lgkmcnt(3)
v_rcp_f32 v87,v87
v_mfma_f32_16x16x32_bf16 v[40:43],a[120:123],v[60:63],v[40:43]
ds_read_b128 v[80:83],v2 offset:48128
v_mul_f32 v29,v29,v85
v_mfma_f32_16x16x32_bf16 v[44:47],a[124:127],v[60:63],v[44:47]
s_waitcnt vmcnt(6)
v_mul_f32 v30,v30,v86
v_mul_f32 v31,v31,v87
v_mfma_f32_16x16x32_bf16 v[48:51],a[128:131],v[60:63],v[48:51]
s_barrier
v_mfma_f32_16x16x32_bf16 v[40:43],a[132:135],v[64:67],v[40:43]
ds_read_b128 v[52:55],v2 offset:49152
v_add_f32_dpp v108,v108,v108 quad_perm:[1,0,3,2] row_mask:0xf bank_mask:0xf
v_mfma_f32_16x16x32_bf16 v[44:47],a[136:139],v[64:67],v[44:47]
s_nop 0
v_add_f32_dpp v108,v108,v108 quad_perm:[2,3,0,1] row_mask:0xf bank_mask:0xf
v_cvt_pk_bf16_f32 v96,v28,v29
v_mfma_f32_16x16x32_bf16 v[48:51],a[140:143],v[64:67],v[48:51]
s_waitcnt lgkmcnt(3)
v_add_f32_dpp v108,v108,v108 row_half_mirror row_mask:0xf bank_mask:0xf
v_cvt_pk_bf16_f32 v97,v30,v31
v_mfma_f32_16x16x32_bf16 v[40:43],a[144:147],v[68:71],v[40:43]
ds_read_b128 v[56:59],v2 offset:50176
v_fma_f32 v32,-v116,v20,v32
v_mfma_f32_16x16x32_bf16 v[44:47],a[148:151],v[68:71],v[44:47]
v_fma_f32 v33,-v116,v21,v33
v_fma_f32 v34,-v116,v22,v34
v_mfma_f32_16x16x32_bf16 v[48:51],a[152:155],v[68:71],v[48:51]
v_fma_f32 v35,-v116,v23,v35
v_mul_f32 v88,v32,v32
v_mfma_f32_16x16x32_bf16 v[40:43],a[156:159],v[72:75],v[40:43]
ds_read_b128 v[60:63],v2 offset:51200
v_mul_f32 v89,v33,v33
v_mfma_f32_16x16x32_bf16 v[44:47],a[160:163],v[72:75],v[44:47]
v_mul_f32 v90,v34,v34
v_mul_f32 v91,v35,v35
v_mfma_f32_16x16x32_bf16 v[48:51],a[164:167],v[72:75],v[48:51]
s_waitcnt lgkmcnt(3)
v_fma_f32 v88,v118,v88,v117
v_mul_f32 v88,v32,v88
v_mfma_f32_16x16x32_bf16 v[40:43],a[168:171],v[76:79],v[40:43]
ds_read_b128 v[64:67],v2 offset:52224
v_fma_f32 v89,v118,v89,v117
v_mfma_f32_16x16x32_bf16 v[44:47],a[172:175],v[76:79],v[44:47]
v_exp_f32 v88,v88
v_mfma_f32_16x16x32_bf16 v[48:51],a[176:179],v[76:79],v[48:51]
v_mul_f32 v89,v33,v89
v_fma_f32 v88,v88,v119,v119
v_mfma_f32_16x16x32_bf16 v[40:43],a[180:183],v[80:83],v[40:43]
buffer_load_dwordx4 v1,s[36:39],s61 offen offset:0 sc1 lds
v_mfma_f32_16x16x32_bf16 v[44:47],a[184:187],v[80:83],v[44:47]
ds_write_b32 v10,v108 offset:128
v_mfma_f32_16x16x32_bf16 v[48:51],a[188:191],v[80:83],v[48:51]
s_waitcnt lgkmcnt(3)
ds_read_b128 v[68:71],v2 offset:53248
v_fma_f32 v90,v118,v90,v117
v_mfma_f32_16x16x32_bf16 v[40:43],a[192:195],v[52:55],v[40:43]
ds_read_b128 v[72:75],v2 offset:54272
v_fma_f32 v91,v118,v91,v117
v_mfma_f32_16x16x32_bf16 v[44:47],a[196:199],v[52:55],v[44:47]
buffer_load_dwordx4 v1,s[36:39],s61 offen offset:1024 sc1 lds
v_mfma_f32_16x16x32_bf16 v[48:51],a[200:203],v[52:55],v[48:51]
v_exp_f32 v89,v89
v_mfma_f32_16x16x32_bf16 v[40:43],a[204:207],v[56:59],v[40:43]
ds_read_b128 v[76:79],v2 offset:55296
v_mul_f32 v90,v34,v90
v_mfma_f32_16x16x32_bf16 v[44:47],a[208:211],v[56:59],v[44:47]
v_exp_f32 v90,v90
v_mfma_f32_16x16x32_bf16 v[48:51],a[212:215],v[56:59],v[48:51]
s_waitcnt lgkmcnt(4)
buffer_load_dwordx4 v1,s[36:39],s61 offen offset:2048 sc1 lds
v_mfma_f32_16x16x32_bf16 v[40:43],a[216:219],v[60:63],v[40:43]
ds_read_b128 v[80:83],v2 offset:56320
v_mul_f32 v91,v35,v91
v_mfma_f32_16x16x32_bf16 v[44:47],a[220:223],v[60:63],v[44:47]
v_exp_f32 v91,v91
v_mfma_f32_16x16x32_bf16 v[48:51],a[224:227],v[60:63],v[48:51]
v_rcp_f32 v88,v88
v_mfma_f32_16x16x32_bf16 v[40:43],a[228:231],v[64:67],v[40:43]
buffer_load_dwordx4 v1,s[36:39],s61 offen offset:3072 sc1 lds
v_mfma_f32_16x16x32_bf16 v[44:47],a[232:235],v[64:67],v[44:47]
buffer_load_dword v104,v8,s[36:39],s72 offen offset:0 sc1
v_mfma_f32_16x16x32_bf16 v[48:51],a[236:239],v[64:67],v[48:51]
s_waitcnt lgkmcnt(2)
ds_read_b128 v[52:55],v2 offset:57344
v_fma_f32 v89,v89,v119,v119
v_mfma_f32_16x16x32_bf16 v[40:43],a[240:243],v[68:71],v[40:43]
ds_read_b128 v[56:59],v2 offset:58368
v_fma_f32 v90,v90,v119,v119
v_mfma_f32_16x16x32_bf16 v[44:47],a[244:247],v[68:71],v[44:47]
v_rcp_f32 v89,v89
v_mfma_f32_16x16x32_bf16 v[48:51],a[248:251],v[68:71],v[48:51]
v_rcp_f32 v90,v90
v_mfma_f32_16x16x32_bf16 v[40:43],a[252:255],v[72:75],v[40:43]
ds_read_b128 v[60:63],v2 offset:59392
v_fma_f32 v91,v91,v119,v119
v_mfma_f32_16x16x32_bf16 v[44:47],v[128:131],v[72:75],v[44:47]
v_rcp_f32 v91,v91
v_mfma_f32_16x16x32_bf16 v[48:51],v[132:135],v[72:75],v[48:51]
s_waitcnt lgkmcnt(3)
v_mul_f32 v32,v32,v88
v_mul_f32 v33,v33,v89)"
R"(
v_mfma_f32_16x16x32_bf16 v[40:43],v[136:139],v[76:79],v[40:43]
ds_read_b128 v[64:67],v2 offset:60416
v_mul_f32 v34,v34,v90
v_mfma_f32_16x16x32_bf16 v[44:47],v[140:143],v[76:79],v[44:47]
v_mul_f32 v35,v35,v91
v_cvt_pk_bf16_f32 v98,v32,v33
v_mfma_f32_16x16x32_bf16 v[48:51],v[144:147],v[76:79],v[48:51]
v_cvt_pk_bf16_f32 v99,v34,v35
v_fma_f32 v36,-v116,v24,v36
v_mfma_f32_16x16x32_bf16 v[40:43],v[148:151],v[80:83],v[40:43]
ds_read_b128 v[68:71],v2 offset:61440
v_fma_f32 v37,-v116,v25,v37
v_mfma_f32_16x16x32_bf16 v[44:47],v[152:155],v[80:83],v[44:47]
v_fma_f32 v38,-v116,v26,v38
v_fma_f32 v39,-v116,v27,v39
v_mfma_f32_16x16x32_bf16 v[48:51],v[156:159],v[80:83],v[48:51]
s_waitcnt lgkmcnt(3)
v_mul_f32 v92,v36,v36
v_mul_f32 v93,v37,v37
v_mfma_f32_16x16x32_bf16 v[40:43],v[160:163],v[52:55],v[40:43]
ds_read_b128 v[72:75],v2 offset:62464
v_mul_f32 v94,v38,v38
v_mfma_f32_16x16x32_bf16 v[44:47],v[164:167],v[52:55],v[44:47]
v_mul_f32 v95,v39,v39
v_fma_f32 v92,v118,v92,v117
v_mfma_f32_16x16x32_bf16 v[48:51],v[168:171],v[52:55],v[48:51]
buffer_store_dwordx4 v[96:99],v3,s[36:39],s73 offen offset:0 sc1
v_mul_f32 v92,v36,v92
v_exp_f32 v92,v92
v_mfma_f32_16x16x32_bf16 v[40:43],v[172:175],v[56:59],v[40:43]
ds_read_b128 v[76:79],v2 offset:63488
v_fma_f32 v92,v92,v119,v119
v_mfma_f32_16x16x32_bf16 v[44:47],v[176:179],v[56:59],v[44:47]
v_rcp_f32 v92,v92
v_mfma_f32_16x16x32_bf16 v[48:51],v[180:183],v[56:59],v[48:51]
s_waitcnt lgkmcnt(3)
v_fma_f32 v93,v118,v93,v117
v_mul_f32 v93,v37,v93
v_mfma_f32_16x16x32_bf16 v[40:43],v[184:187],v[60:63],v[40:43]
ds_read_b128 v[80:83],v2 offset:64512
v_xor_b32 v2,65536,v2
v_mfma_f32_16x16x32_bf16 v[44:47],v[188:191],v[60:63],v[44:47]
s_waitcnt vmcnt(6)
v_exp_f32 v93,v93
v_mfma_f32_16x16x32_bf16 v[48:51],v[192:195],v[60:63],v[48:51]
s_barrier
v_mfma_f32_16x16x32_bf16 v[40:43],v[196:199],v[64:67],v[40:43]
ds_read_b128 v[52:55],v2 offset:0
ds_read_b32 v13,v7 offset:256
v_mfma_f32_16x16x32_bf16 v[44:47],v[200:203],v[64:67],v[44:47]
ds_read_b64 v[112:113],v11 offset:128
v_fma_f32 v93,v93,v119,v119
v_mfma_f32_16x16x32_bf16 v[48:51],v[204:207],v[64:67],v[48:51]
s_waitcnt lgkmcnt(5)
v_rcp_f32 v93,v93
v_mfma_f32_16x16x32_bf16 v[40:43],v[208:211],v[68:71],v[40:43]
ds_read_b128 v[56:59],v2 offset:1024
v_fma_f32 v94,v118,v94,v117
v_mfma_f32_16x16x32_bf16 v[44:47],v[212:215],v[68:71],v[44:47]
s_waitcnt lgkmcnt(2)
v_mul_f32 v94,v38,v94
v_fma_f32 v95,v118,v95,v117
v_mfma_f32_16x16x32_bf16 v[48:51],v[216:219],v[68:71],v[48:51]
s_waitcnt lgkmcnt(1)
v_cmp_eq_u32 vcc,s60,v13
v_exp_f32 v94,v94
v_mul_f32 v95,v39,v95
v_mfma_f32_16x16x32_bf16 v[40:43],v[220:223],v[72:75],v[40:43]
s_cmp_eq_u32 vcc_lo,-1
ds_read_b128 v[60:63],v2 offset:2048
v_fma_f32 v94,v94,v119,v119
v_mfma_f32_16x16x32_bf16 v[44:47],v[224:227],v[72:75],v[44:47]
s_cselect_b32 s66,s86,s59
v_exp_f32 v95,v95
v_mfma_f32_16x16x32_bf16 v[48:51],v[228:231],v[72:75],v[48:51]
v_fma_f32 v95,v95,v119,v119
v_rcp_f32 v94,v94
v_rcp_f32 v95,v95
v_mfma_f32_16x16x32_bf16 v[40:43],v[232:235],v[76:79],v[40:43]
s_lshr_b32 s61,s66,0
v_mul_f32 v36,v36,v92
v_mul_f32 v37,v37,v93
v_mul_f32 v38,v38,v94
v_mul_f32 v39,v39,v95
v_cvt_pk_bf16_f32 v100,v36,v37
v_cvt_pk_bf16_f32 v101,v38,v39
v_mfma_f32_16x16x32_bf16 v[44:47],v[236:239],v[76:79],v[44:47]
buffer_store_dwordx2 v[100:101],v9,s[36:39],s73 offen offset:0 sc1
ds_read_b128 v[64:67],v2 offset:3072
v_mul_f32 v116,s75,v112
v_fma_f32 v113,v113,s75,v12
v_mfma_f32_16x16x32_bf16 v[48:51],v[240:243],v[76:79],v[48:51]
s_xor_b32 s64,s64,65536
v_fma_f32 v113,-v116,v116,v113
v_max_f32 v113,v113,v12
v_mfma_f32_16x16x32_bf16 v[40:43],v[244:247],v[80:83],v[40:43]
s_cmp_lt_u32 s67,s59
ds_read_b128 v[68:71],v2 offset:4096
v_mfma_f32_16x16x32_bf16 v[44:47],v[248:251],v[80:83],v[44:47]
s_waitcnt lgkmcnt(3)
v_rsq_f32 v112,v113
v_mfma_f32_16x16x32_bf16 v[48:51],v[252:255],v[80:83],v[48:51]
s_cbranch_scc1 Lg3_d0w2_%=
s_branch Lg3_slow0_%=
Lg3_d0w3_%=:
v_mfma_f32_16x16x32_bf16 v[28:31],a[0:3],v[52:55],0
s_mov_b32 m0,s62
ds_read_b128 v[72:75],v2 offset:5120
v_mul_f32 v117,s76,v112
v_mfma_f32_16x16x32_bf16 v[32:35],a[4:7],v[52:55],0
s_mul_i32 s73,s47,7
v_mul_f32 v118,v112,v112
v_mul_f32 v119,v113,v112
v_mfma_f32_16x16x32_bf16 v[36:39],a[8:11],v[52:55],0
s_lshr_b32 s73,s73,1
v_mul_f32 v118,s77,v118
v_mul_f32 v118,v118,v117
v_mfma_f32_16x16x32_bf16 v[28:31],a[12:15],v[56:59],v[28:31]
s_add_u32 s53,s52,s55
ds_read_b128 v[76:79],v2 offset:6144
v_fma_f32 v40,-v116,v16,v40
v_mfma_f32_16x16x32_bf16 v[32:35],a[16:19],v[56:59],v[32:35]
buffer_load_dwordx4 v0,s[36:39],s61 offen offset:0 sc1 lds
v_mfma_f32_16x16x32_bf16 v[36:39],a[20:23],v[56:59],v[36:39]
s_waitcnt lgkmcnt(3)
v_fma_f32 v41,-v116,v17,v41
v_fma_f32 v42,-v116,v18,v42
v_mfma_f32_16x16x32_bf16 v[28:31],a[24:27],v[60:63],v[28:31]
s_cmp_eq_u32 s53,s56
ds_read_b128 v[80:83],v2 offset:7168
v_fma_f32 v43,-v116,v19,v43
v_mfma_f32_16x16x32_bf16 v[32:35],a[28:31],v[60:63],v[32:35]
s_cselect_b32 s53,s57,s53
v_mul_f32 v84,v40,v40
v_mul_f32 v85,v41,v41
v_mfma_f32_16x16x32_bf16 v[36:39],a[32:35],v[60:63],v[36:39]
buffer_load_dwordx4 v0,s[36:39],s61 offen offset:1024 sc1 lds
v_mfma_f32_16x16x32_bf16 v[28:31],a[36:39],v[64:67],v[28:31]
s_cmp_eq_u32 s53,s58
ds_read_b128 v[52:55],v2 offset:8192
v_mul_f32 v86,v42,v42
v_mfma_f32_16x16x32_bf16 v[32:35],a[40:43],v[64:67],v[32:35]
s_cselect_b32 s53,s59,s53
v_mul_f32 v87,v43,v43
v_fma_f32 v84,v118,v84,v117
v_mfma_f32_16x16x32_bf16 v[36:39],a[44:47],v[64:67],v[36:39]
s_waitcnt lgkmcnt(3)
v_mul_f32 v84,v40,v84
v_fma_f32 v85,v118,v85,v117
v_mfma_f32_16x16x32_bf16 v[28:31],a[48:51],v[68:71],v[28:31]
buffer_load_dwordx4 v0,s[36:39],s61 offen offset:2048 sc1 lds
v_mfma_f32_16x16x32_bf16 v[32:35],a[52:55],v[68:71],v[32:35]
s_cmp_ge_u32 s66,s59
ds_read_b128 v[56:59],v2 offset:9216
v_mul_f32 v85,v41,v85
v_mfma_f32_16x16x32_bf16 v[36:39],a[56:59],v[68:71],v[36:39]
s_cselect_b32 s86,s49,s50
v_exp_f32 v84,v84
v_mfma_f32_16x16x32_bf16 v[28:31],a[60:63],v[72:75],v[28:31]
s_mul_i32 s70,s46,5
ds_read_b128 v[60:63],v2 offset:10240
v_fma_f32 v84,v84,v119,v119
v_mfma_f32_16x16x32_bf16 v[32:35],a[64:67],v[72:75],v[32:35]
buffer_load_dwordx4 v0,s[36:39],s61 offen offset:3072 sc1 lds
v_mfma_f32_16x16x32_bf16 v[36:39],a[68:71],v[72:75],v[36:39]
s_waitcnt lgkmcnt(3)
v_exp_f32 v85,v85
v_mfma_f32_16x16x32_bf16 v[28:31],a[72:75],v[76:79],v[28:31]
s_add_u32 m0,s62,16384
ds_read_b128 v[64:67],v2 offset:11264
v_fma_f32 v85,v85,v119,v119
v_mfma_f32_16x16x32_bf16 v[32:35],a[76:79],v[76:79],v[32:35]
s_lshr_b32 s72,s61,5
v_rcp_f32 v84,v84
v_mfma_f32_16x16x32_bf16 v[36:39],a[80:83],v[76:79],v[36:39]
s_lshr_b32 s70,s70,6
v_rcp_f32 v85,v85
v_mfma_f32_16x16x32_bf16 v[28:31],a[84:87],v[80:83],v[28:31]
s_mov_b32 s62,s63
ds_read_b128 v[68:71],v2 offset:12288
v_fma_f32 v86,v118,v86,v117
v_mfma_f32_16x16x32_bf16 v[32:35],a[88:91],v[80:83],v[32:35]
v_mul_f32 v86,v42,v86
v_fma_f32 v87,v118,v87,v117
v_mfma_f32_16x16x32_bf16 v[36:39],a[92:95],v[80:83],v[36:39]
s_waitcnt lgkmcnt(3)
v_exp_f32 v86,v86
v_mfma_f32_16x16x32_bf16 v[28:31],a[96:99],v[52:55],v[28:31]
ds_read_b128 v[72:75],v2 offset:13312
v_mul_f32 v87,v43,v87
v_mfma_f32_16x16x32_bf16 v[32:35],a[100:103],v[52:55],v[32:35]
v_exp_f32 v87,v87
v_mfma_f32_16x16x32_bf16 v[36:39],a[104:107],v[52:55],v[36:39]
v_fma_f32 v86,v86,v119,v119
v_fma_f32 v87,v87,v119,v119
v_mfma_f32_16x16x32_bf16 v[28:31],a[108:111],v[56:59],v[28:31]
ds_read_b128 v[76:79],v2 offset:14336
v_mul_f32 v40,v40,v84
v_mfma_f32_16x16x32_bf16 v[32:35],a[112:115],v[56:59],v[32:35]
v_rcp_f32 v86,v86)"
R"(
v_mfma_f32_16x16x32_bf16 v[36:39],a[116:119],v[56:59],v[36:39]
s_waitcnt lgkmcnt(3)
v_rcp_f32 v87,v87
v_mfma_f32_16x16x32_bf16 v[28:31],a[120:123],v[60:63],v[28:31]
ds_read_b128 v[80:83],v2 offset:15360
v_mul_f32 v41,v41,v85
v_mfma_f32_16x16x32_bf16 v[32:35],a[124:127],v[60:63],v[32:35]
s_waitcnt vmcnt(7)
v_mul_f32 v42,v42,v86
v_mul_f32 v43,v43,v87
v_mfma_f32_16x16x32_bf16 v[36:39],a[128:131],v[60:63],v[36:39]
s_barrier
v_mfma_f32_16x16x32_bf16 v[28:31],a[132:135],v[64:67],v[28:31]
ds_read_b128 v[52:55],v2 offset:16384
v_add_f32_dpp v104,v104,v104 quad_perm:[1,0,3,2] row_mask:0xf bank_mask:0xf
v_mfma_f32_16x16x32_bf16 v[32:35],a[136:139],v[64:67],v[32:35]
s_nop 0
v_add_f32_dpp v104,v104,v104 quad_perm:[2,3,0,1] row_mask:0xf bank_mask:0xf
v_cvt_pk_bf16_f32 v96,v40,v41
v_mfma_f32_16x16x32_bf16 v[36:39],a[140:143],v[64:67],v[36:39]
s_waitcnt lgkmcnt(3)
v_add_f32_dpp v104,v104,v104 row_half_mirror row_mask:0xf bank_mask:0xf
v_cvt_pk_bf16_f32 v97,v42,v43
v_mfma_f32_16x16x32_bf16 v[28:31],a[144:147],v[68:71],v[28:31]
ds_read_b128 v[56:59],v2 offset:17408
v_fma_f32 v44,-v116,v20,v44
v_mfma_f32_16x16x32_bf16 v[32:35],a[148:151],v[68:71],v[32:35]
v_fma_f32 v45,-v116,v21,v45
v_fma_f32 v46,-v116,v22,v46
v_mfma_f32_16x16x32_bf16 v[36:39],a[152:155],v[68:71],v[36:39]
v_fma_f32 v47,-v116,v23,v47
v_mul_f32 v88,v44,v44
v_mfma_f32_16x16x32_bf16 v[28:31],a[156:159],v[72:75],v[28:31]
ds_read_b128 v[60:63],v2 offset:18432
v_mul_f32 v89,v45,v45
v_mfma_f32_16x16x32_bf16 v[32:35],a[160:163],v[72:75],v[32:35]
v_mul_f32 v90,v46,v46
v_mul_f32 v91,v47,v47
v_mfma_f32_16x16x32_bf16 v[36:39],a[164:167],v[72:75],v[36:39]
s_waitcnt lgkmcnt(3)
v_fma_f32 v88,v118,v88,v117
v_mul_f32 v88,v44,v88
v_mfma_f32_16x16x32_bf16 v[28:31],a[168:171],v[76:79],v[28:31]
ds_read_b128 v[64:67],v2 offset:19456
v_fma_f32 v89,v118,v89,v117
v_mfma_f32_16x16x32_bf16 v[32:35],a[172:175],v[76:79],v[32:35]
v_exp_f32 v88,v88
v_mfma_f32_16x16x32_bf16 v[36:39],a[176:179],v[76:79],v[36:39]
v_mul_f32 v89,v45,v89
v_fma_f32 v88,v88,v119,v119
v_mfma_f32_16x16x32_bf16 v[28:31],a[180:183],v[80:83],v[28:31]
ds_read_b128 v[68:71],v2 offset:20480
v_fma_f32 v90,v118,v90,v117
v_mfma_f32_16x16x32_bf16 v[32:35],a[184:187],v[80:83],v[32:35]
buffer_load_dwordx4 v1,s[36:39],s61 offen offset:0 sc1 lds
v_mfma_f32_16x16x32_bf16 v[36:39],a[188:191],v[80:83],v[36:39]
s_waitcnt lgkmcnt(3)
ds_write_b32 v10,v104 offset:0
v_mfma_f32_16x16x32_bf16 v[28:31],a[192:195],v[52:55],v[28:31]
ds_read_b128 v[72:75],v2 offset:21504
v_fma_f32 v91,v118,v91,v117
v_mfma_f32_16x16x32_bf16 v[32:35],a[196:199],v[52:55],v[32:35]
v_exp_f32 v89,v89
v_mfma_f32_16x16x32_bf16 v[36:39],a[200:203],v[52:55],v[36:39]
buffer_load_dwordx4 v1,s[36:39],s61 offen offset:1024 sc1 lds
v_mfma_f32_16x16x32_bf16 v[28:31],a[204:207],v[56:59],v[28:31]
ds_read_b128 v[76:79],v2 offset:22528
v_mul_f32 v90,v46,v90
v_mfma_f32_16x16x32_bf16 v[32:35],a[208:211],v[56:59],v[32:35]
v_exp_f32 v90,v90
v_mfma_f32_16x16x32_bf16 v[36:39],a[212:215],v[56:59],v[36:39]
s_waitcnt lgkmcnt(4)
v_rcp_f32 v88,v88
v_mfma_f32_16x16x32_bf16 v[28:31],a[216:219],v[60:63],v[28:31]
buffer_load_dwordx4 v1,s[36:39],s61 offen offset:2048 sc1 lds
v_mfma_f32_16x16x32_bf16 v[32:35],a[220:223],v[60:63],v[32:35]
ds_read_b128 v[80:83],v2 offset:23552
v_mul_f32 v91,v47,v91
v_mfma_f32_16x16x32_bf16 v[36:39],a[224:227],v[60:63],v[36:39]
v_exp_f32 v91,v91
v_mfma_f32_16x16x32_bf16 v[28:31],a[228:231],v[64:67],v[28:31]
ds_read_b128 v[52:55],v2 offset:24576
v_fma_f32 v89,v89,v119,v119
v_mfma_f32_16x16x32_bf16 v[32:35],a[232:235],v[64:67],v[32:35]
buffer_load_dwordx4 v1,s[36:39],s61 offen offset:3072 sc1 lds
v_mfma_f32_16x16x32_bf16 v[36:39],a[236:239],v[64:67],v[36:39]
s_waitcnt lgkmcnt(3)
buffer_load_dword v108,v8,s[36:39],s72 offen offset:0 sc1
v_mfma_f32_16x16x32_bf16 v[28:31],a[240:243],v[68:71],v[28:31]
ds_read_b128 v[56:59],v2 offset:25600
v_fma_f32 v90,v90,v119,v119
v_mfma_f32_16x16x32_bf16 v[32:35],a[244:247],v[68:71],v[32:35]
v_rcp_f32 v89,v89
v_mfma_f32_16x16x32_bf16 v[36:39],a[248:251],v[68:71],v[36:39]
v_rcp_f32 v90,v90
v_mfma_f32_16x16x32_bf16 v[28:31],a[252:255],v[72:75],v[28:31]
ds_read_b128 v[60:63],v2 offset:26624
v_fma_f32 v91,v91,v119,v119
v_mfma_f32_16x16x32_bf16 v[32:35],v[128:131],v[72:75],v[32:35]
v_rcp_f32 v91,v91
v_mfma_f32_16x16x32_bf16 v[36:39],v[132:135],v[72:75],v[36:39]
s_waitcnt lgkmcnt(3)
v_mul_f32 v44,v44,v88
v_mul_f32 v45,v45,v89
v_mfma_f32_16x16x32_bf16 v[28:31],v[136:139],v[76:79],v[28:31]
ds_read_b128 v[64:67],v2 offset:27648
v_mul_f32 v46,v46,v90
v_mfma_f32_16x16x32_bf16 v[32:35],v[140:143],v[76:79],v[32:35]
v_mul_f32 v47,v47,v91
v_cvt_pk_bf16_f32 v98,v44,v45
v_mfma_f32_16x16x32_bf16 v[36:39],v[144:147],v[76:79],v[36:39]
v_cvt_pk_bf16_f32 v99,v46,v47
v_fma_f32 v48,-v116,v24,v48
v_mfma_f32_16x16x32_bf16 v[28:31],v[148:151],v[80:83],v[28:31]
ds_read_b128 v[68:71],v2 offset:28672
v_fma_f32 v49,-v116,v25,v49
v_mfma_f32_16x16x32_bf16 v[32:35],v[152:155],v[80:83],v[32:35]
v_fma_f32 v50,-v116,v26,v50
v_fma_f32 v51,-v116,v27,v51
v_mfma_f32_16x16x32_bf16 v[36:39],v[156:159],v[80:83],v[36:39]
s_waitcnt lgkmcnt(3)
v_mul_f32 v92,v48,v48
v_mul_f32 v93,v49,v49
v_mfma_f32_16x16x32_bf16 v[28:31],v[160:163],v[52:55],v[28:31]
ds_read_b128 v[72:75],v2 offset:29696
v_mul_f32 v94,v50,v50
v_mfma_f32_16x16x32_bf16 v[32:35],v[164:167],v[52:55],v[32:35]
v_mul_f32 v95,v51,v51
v_fma_f32 v92,v118,v92,v117
v_mfma_f32_16x16x32_bf16 v[36:39],v[168:171],v[52:55],v[36:39]
v_mul_f32 v92,v48,v92
v_fma_f32 v93,v118,v93,v117
v_mfma_f32_16x16x32_bf16 v[28:31],v[172:175],v[56:59],v[28:31]
ds_read_b128 v[76:79],v2 offset:30720
v_mul_f32 v93,v49,v93
v_mfma_f32_16x16x32_bf16 v[32:35],v[176:179],v[56:59],v[32:35]
buffer_store_dwordx4 v[96:99],v3,s[36:39],s73 offen offset:0 sc1
v_exp_f32 v92,v92
v_fma_f32 v94,v118,v94,v117
v_mfma_f32_16x16x32_bf16 v[36:39],v[180:183],v[56:59],v[36:39]
s_waitcnt lgkmcnt(3)
v_exp_f32 v93,v93
v_mfma_f32_16x16x32_bf16 v[28:31],v[184:187],v[60:63],v[28:31]
ds_read_b128 v[80:83],v2 offset:31744
v_mul_f32 v94,v50,v94
v_mfma_f32_16x16x32_bf16 v[32:35],v[188:191],v[60:63],v[32:35]
s_waitcnt vmcnt(6)
v_exp_f32 v94,v94
v_mfma_f32_16x16x32_bf16 v[36:39],v[192:195],v[60:63],v[36:39]
s_barrier
v_mfma_f32_16x16x32_bf16 v[28:31],v[196:199],v[64:67],v[28:31]
buffer_store_dword v5,v4,s[36:39],s70 offen offset:0 sc1
ds_read_b128 v[52:55],v2 offset:32768
ds_read_b32 v13,v7 offset:0
ds_read_b64 v[112:113],v11 offset:0
v_mfma_f32_16x16x32_bf16 v[32:35],v[200:203],v[64:67],v[32:35]
v_fma_f32 v92,v92,v119,v119
v_fma_f32 v93,v93,v119,v119
v_mfma_f32_16x16x32_bf16 v[36:39],v[204:207],v[64:67],v[36:39]
s_waitcnt lgkmcnt(5)
v_rcp_f32 v92,v92
v_mfma_f32_16x16x32_bf16 v[28:31],v[208:211],v[68:71],v[28:31]
ds_read_b128 v[56:59],v2 offset:33792
v_fma_f32 v94,v94,v119,v119
v_mfma_f32_16x16x32_bf16 v[32:35],v[212:215],v[68:71],v[32:35]
s_waitcnt lgkmcnt(2)
v_rcp_f32 v93,v93
v_mfma_f32_16x16x32_bf16 v[36:39],v[216:219],v[68:71],v[36:39]
s_waitcnt lgkmcnt(1)
v_cmp_eq_u32 vcc,s60,v13
v_rcp_f32 v94,v94
v_fma_f32 v95,v118,v95,v117
v_mfma_f32_16x16x32_bf16 v[28:31],v[220:223],v[72:75],v[28:31]
s_cmp_eq_u32 vcc_lo,-1
ds_read_b128 v[60:63],v2 offset:34816
v_mul_f32 v95,v51,v95
v_mfma_f32_16x16x32_bf16 v[32:35],v[224:227],v[72:75],v[32:35]
s_cselect_b32 s67,s86,s59
v_exp_f32 v95,v95
v_mfma_f32_16x16x32_bf16 v[36:39],v[228:231],v[72:75],v[36:39]
v_fma_f32 v95,v95,v119,v119
v_mul_f32 v116,s75,v112
v_mfma_f32_16x16x32_bf16 v[28:31],v[232:235],v[76:79],v[28:31]
s_lshr_b32 s61,s67,0
ds_read_b128 v[64:67],v2 offset:35840
v_mul_f32 v48,v48,v92
v_mfma_f32_16x16x32_bf16 v[32:35],v[236:239],v[76:79],v[32:35]
s_xor_b32 s63,s63,65536
v_rcp_f32 v95,v95
v_mfma_f32_16x16x32_bf16 v[36:39],v[240:243],v[76:79],v[36:39]
s_cmp_ge_u32 s66,s59
v_mul_f32 v49,v49,v93
v_mul_f32 v50,v50,v94
v_mul_f32 v51,v51,v95
v_cvt_pk_bf16_f32 v100,v48,v49
v_cvt_pk_bf16_f32 v101,v50,v51
v_mfma_f32_16x16x32_bf16 v[28:31],v[244:247],v[80:83],v[28:31]
buffer_store_dwordx2 v[100:101],v9,s[36:39],s73 offen offset:0 sc1
ds_read_b128 v[68:71],v2 offset:36864
v_fma_f32 v113,v113,s75,v12)"
R"(
v_fma_f32 v113,-v116,v116,v113
v_mfma_f32_16x16x32_bf16 v[32:35],v[248:251],v[80:83],v[32:35]
s_waitcnt lgkmcnt(3)
v_max_f32 v113,v113,v12
v_rsq_f32 v112,v113
v_mfma_f32_16x16x32_bf16 v[36:39],v[252:255],v[80:83],v[36:39]
s_cbranch_scc1 Lg3_slow1_%=
Lg3_d1w3_%=:
v_mfma_f32_16x16x32_bf16 v[40:43],a[0:3],v[52:55],0
s_mov_b32 m0,s62
ds_read_b128 v[72:75],v2 offset:37888
v_mul_f32 v117,s76,v112
v_mfma_f32_16x16x32_bf16 v[44:47],a[4:7],v[52:55],0
s_mul_i32 s73,s48,7
v_mul_f32 v118,v112,v112
v_mul_f32 v119,v113,v112
v_mfma_f32_16x16x32_bf16 v[48:51],a[8:11],v[52:55],0
s_lshr_b32 s73,s73,1
v_mul_f32 v118,s77,v118
v_mul_f32 v118,v118,v117
v_mfma_f32_16x16x32_bf16 v[40:43],a[12:15],v[56:59],v[40:43]
s_add_u32 s74,s53,s55
ds_read_b128 v[76:79],v2 offset:38912
v_fma_f32 v28,-v116,v16,v28
v_mfma_f32_16x16x32_bf16 v[44:47],a[16:19],v[56:59],v[44:47]
buffer_load_dwordx4 v0,s[36:39],s61 offen offset:0 sc1 lds
v_mfma_f32_16x16x32_bf16 v[48:51],a[20:23],v[56:59],v[48:51]
s_waitcnt lgkmcnt(3)
v_fma_f32 v29,-v116,v17,v29
v_fma_f32 v30,-v116,v18,v30
v_mfma_f32_16x16x32_bf16 v[40:43],a[24:27],v[60:63],v[40:43]
s_cmp_eq_u32 s74,s56
ds_read_b128 v[80:83],v2 offset:39936
v_fma_f32 v31,-v116,v19,v31
v_mfma_f32_16x16x32_bf16 v[44:47],a[28:31],v[60:63],v[44:47]
s_cselect_b32 s74,s57,s74
v_mul_f32 v84,v28,v28
v_mul_f32 v85,v29,v29
v_mfma_f32_16x16x32_bf16 v[48:51],a[32:35],v[60:63],v[48:51]
buffer_load_dwordx4 v0,s[36:39],s61 offen offset:1024 sc1 lds
v_mfma_f32_16x16x32_bf16 v[40:43],a[36:39],v[64:67],v[40:43]
s_cmp_eq_u32 s74,s58
ds_read_b128 v[52:55],v2 offset:40960
v_mul_f32 v86,v30,v30
v_mfma_f32_16x16x32_bf16 v[44:47],a[40:43],v[64:67],v[44:47]
s_cselect_b32 s74,s59,s74
v_mul_f32 v87,v31,v31
v_fma_f32 v84,v118,v84,v117
v_mfma_f32_16x16x32_bf16 v[48:51],a[44:47],v[64:67],v[48:51]
s_waitcnt lgkmcnt(3)
v_mul_f32 v84,v28,v84
v_fma_f32 v85,v118,v85,v117
v_mfma_f32_16x16x32_bf16 v[40:43],a[48:51],v[68:71],v[40:43]
buffer_load_dwordx4 v0,s[36:39],s61 offen offset:2048 sc1 lds
v_mfma_f32_16x16x32_bf16 v[44:47],a[52:55],v[68:71],v[44:47]
s_cmp_ge_u32 s67,s59
ds_read_b128 v[56:59],v2 offset:41984
v_mul_f32 v85,v29,v85
v_mfma_f32_16x16x32_bf16 v[48:51],a[56:59],v[68:71],v[48:51]
s_cselect_b32 s86,s50,s51
v_exp_f32 v84,v84
v_mfma_f32_16x16x32_bf16 v[40:43],a[60:63],v[72:75],v[40:43]
s_mul_i32 s70,s47,5
ds_read_b128 v[60:63],v2 offset:43008
v_fma_f32 v84,v84,v119,v119
v_mfma_f32_16x16x32_bf16 v[44:47],a[64:67],v[72:75],v[44:47]
buffer_load_dwordx4 v0,s[36:39],s61 offen offset:3072 sc1 lds
v_mfma_f32_16x16x32_bf16 v[48:51],a[68:71],v[72:75],v[48:51]
s_waitcnt lgkmcnt(3)
v_exp_f32 v85,v85
v_mfma_f32_16x16x32_bf16 v[40:43],a[72:75],v[76:79],v[40:43]
s_add_u32 m0,s62,16384
ds_read_b128 v[64:67],v2 offset:44032
v_fma_f32 v85,v85,v119,v119
v_mfma_f32_16x16x32_bf16 v[44:47],a[76:79],v[76:79],v[44:47]
s_lshr_b32 s72,s61,5
v_rcp_f32 v84,v84
v_mfma_f32_16x16x32_bf16 v[48:51],a[80:83],v[76:79],v[48:51]
s_lshr_b32 s70,s70,6
v_rcp_f32 v85,v85
v_mfma_f32_16x16x32_bf16 v[40:43],a[84:87],v[80:83],v[40:43]
s_mov_b32 s62,s64
ds_read_b128 v[68:71],v2 offset:45056
v_fma_f32 v86,v118,v86,v117
v_mfma_f32_16x16x32_bf16 v[44:47],a[88:91],v[80:83],v[44:47]
s_mov_b64 s[44:45],s[46:47]
v_mul_f32 v86,v30,v86
v_fma_f32 v87,v118,v87,v117
v_mfma_f32_16x16x32_bf16 v[48:51],a[92:95],v[80:83],v[48:51]
s_waitcnt lgkmcnt(3)
v_exp_f32 v86,v86
v_mfma_f32_16x16x32_bf16 v[40:43],a[96:99],v[52:55],v[40:43]
s_mov_b64 s[46:47],s[48:49]
ds_read_b128 v[72:75],v2 offset:46080
v_mul_f32 v87,v31,v87
v_mfma_f32_16x16x32_bf16 v[44:47],a[100:103],v[52:55],v[44:47]
s_mov_b64 s[48:49],s[50:51]
v_exp_f32 v87,v87
v_mfma_f32_16x16x32_bf16 v[48:51],a[104:107],v[52:55],v[48:51]
s_mov_b64 s[50:51],s[52:53]
v_fma_f32 v86,v86,v119,v119
v_fma_f32 v87,v87,v119,v119
v_mfma_f32_16x16x32_bf16 v[40:43],a[108:111],v[56:59],v[40:43]
s_mov_b32 s52,s74
ds_read_b128 v[76:79],v2 offset:47104
v_mul_f32 v28,v28,v84
v_mfma_f32_16x16x32_bf16 v[44:47],a[112:115],v[56:59],v[44:47]
v_rcp_f32 v86,v86
v_mfma_f32_16x16x32_bf16 v[48:51],a[116:119],v[56:59],v[48:51]
s_waitcnt lgkmcnt(3)
v_rcp_f32 v87,v87
v_mfma_f32_16x16x32_bf16 v[40:43],a[120:123],v[60:63],v[40:43]
ds_read_b128 v[80:83],v2 offset:48128
v_mul_f32 v29,v29,v85
v_mfma_f32_16x16x32_bf16 v[44:47],a[124:127],v[60:63],v[44:47]
s_waitcnt vmcnt(7)
v_mul_f32 v30,v30,v86
v_mul_f32 v31,v31,v87
v_mfma_f32_16x16x32_bf16 v[48:51],a[128:131],v[60:63],v[48:51]
s_barrier
v_mfma_f32_16x16x32_bf16 v[40:43],a[132:135],v[64:67],v[40:43]
ds_read_b128 v[52:55],v2 offset:49152
v_add_f32_dpp v108,v108,v108 quad_perm:[1,0,3,2] row_mask:0xf bank_mask:0xf
v_mfma_f32_16x16x32_bf16 v[44:47],a[136:139],v[64:67],v[44:47]
s_nop 0
v_add_f32_dpp v108,v108,v108 quad_perm:[2,3,0,1] row_mask:0xf bank_mask:0xf
v_cvt_pk_bf16_f32 v96,v28,v29
v_mfma_f32_16x16x32_bf16 v[48:51],a[140:143],v[64:67],v[48:51]
s_waitcnt lgkmcnt(3)
v_add_f32_dpp v108,v108,v108 row_half_mirror row_mask:0xf bank_mask:0xf
v_cvt_pk_bf16_f32 v97,v30,v31
v_mfma_f32_16x16x32_bf16 v[40:43],a[144:147],v[68:71],v[40:43]
ds_read_b128 v[56:59],v2 offset:50176
v_fma_f32 v32,-v116,v20,v32
v_mfma_f32_16x16x32_bf16 v[44:47],a[148:151],v[68:71],v[44:47]
v_fma_f32 v33,-v116,v21,v33
v_fma_f32 v34,-v116,v22,v34
v_mfma_f32_16x16x32_bf16 v[48:51],a[152:155],v[68:71],v[48:51]
v_fma_f32 v35,-v116,v23,v35
v_mul_f32 v88,v32,v32
v_mfma_f32_16x16x32_bf16 v[40:43],a[156:159],v[72:75],v[40:43]
ds_read_b128 v[60:63],v2 offset:51200
v_mul_f32 v89,v33,v33
v_mfma_f32_16x16x32_bf16 v[44:47],a[160:163],v[72:75],v[44:47]
v_mul_f32 v90,v34,v34
v_mul_f32 v91,v35,v35
v_mfma_f32_16x16x32_bf16 v[48:51],a[164:167],v[72:75],v[48:51]
s_waitcnt lgkmcnt(3)
v_fma_f32 v88,v118,v88,v117
v_mul_f32 v88,v32,v88
v_mfma_f32_16x16x32_bf16 v[40:43],a[168:171],v[76:79],v[40:43]
ds_read_b128 v[64:67],v2 offset:52224
v_fma_f32 v89,v118,v89,v117
v_mfma_f32_16x16x32_bf16 v[44:47],a[172:175],v[76:79],v[44:47]
v_exp_f32 v88,v88
v_mfma_f32_16x16x32_bf16 v[48:51],a[176:179],v[76:79],v[48:51]
v_mul_f32 v89,v33,v89
v_fma_f32 v88,v88,v119,v119
v_mfma_f32_16x16x32_bf16 v[40:43],a[180:183],v[80:83],v[40:43]
ds_read_b128 v[68:71],v2 offset:53248
v_fma_f32 v90,v118,v90,v117
v_mfma_f32_16x16x32_bf16 v[44:47],a[184:187],v[80:83],v[44:47]
buffer_load_dwordx4 v1,s[36:39],s61 offen offset:0 sc1 lds
v_mfma_f32_16x16x32_bf16 v[48:51],a[188:191],v[80:83],v[48:51]
s_waitcnt lgkmcnt(3)
ds_write_b32 v10,v108 offset:128
v_mfma_f32_16x16x32_bf16 v[40:43],a[192:195],v[52:55],v[40:43]
ds_read_b128 v[72:75],v2 offset:54272
v_fma_f32 v91,v118,v91,v117
v_mfma_f32_16x16x32_bf16 v[44:47],a[196:199],v[52:55],v[44:47]
v_exp_f32 v89,v89
v_mfma_f32_16x16x32_bf16 v[48:51],a[200:203],v[52:55],v[48:51]
buffer_load_dwordx4 v1,s[36:39],s61 offen offset:1024 sc1 lds
v_mfma_f32_16x16x32_bf16 v[40:43],a[204:207],v[56:59],v[40:43]
ds_read_b128 v[76:79],v2 offset:55296
v_mul_f32 v90,v34,v90
v_mfma_f32_16x16x32_bf16 v[44:47],a[208:211],v[56:59],v[44:47]
v_exp_f32 v90,v90
v_mfma_f32_16x16x32_bf16 v[48:51],a[212:215],v[56:59],v[48:51]
s_waitcnt lgkmcnt(4)
v_rcp_f32 v88,v88
v_mfma_f32_16x16x32_bf16 v[40:43],a[216:219],v[60:63],v[40:43]
buffer_load_dwordx4 v1,s[36:39],s61 offen offset:2048 sc1 lds
v_mfma_f32_16x16x32_bf16 v[44:47],a[220:223],v[60:63],v[44:47]
ds_read_b128 v[80:83],v2 offset:56320
v_mul_f32 v91,v35,v91
v_mfma_f32_16x16x32_bf16 v[48:51],a[224:227],v[60:63],v[48:51]
v_exp_f32 v91,v91
v_mfma_f32_16x16x32_bf16 v[40:43],a[228:231],v[64:67],v[40:43]
ds_read_b128 v[52:55],v2 offset:57344
v_fma_f32 v89,v89,v119,v119
v_mfma_f32_16x16x32_bf16 v[44:47],a[232:235],v[64:67],v[44:47]
buffer_load_dwordx4 v1,s[36:39],s61 offen offset:3072 sc1 lds
v_mfma_f32_16x16x32_bf16 v[48:51],a[236:239],v[64:67],v[48:51]
s_waitcnt lgkmcnt(3)
buffer_load_dword v104,v8,s[36:39],s72 offen offset:0 sc1
v_mfma_f32_16x16x32_bf16 v[40:43],a[240:243],v[68:71],v[40:43]
ds_read_b128 v[56:59],v2 offset:58368
v_fma_f32 v90,v90,v119,v119
v_mfma_f32_16x16x32_bf16 v[44:47],a[244:247],v[68:71],v[44:47])"
R"(
v_rcp_f32 v89,v89
v_mfma_f32_16x16x32_bf16 v[48:51],a[248:251],v[68:71],v[48:51]
v_rcp_f32 v90,v90
v_mfma_f32_16x16x32_bf16 v[40:43],a[252:255],v[72:75],v[40:43]
ds_read_b128 v[60:63],v2 offset:59392
v_fma_f32 v91,v91,v119,v119
v_mfma_f32_16x16x32_bf16 v[44:47],v[128:131],v[72:75],v[44:47]
v_rcp_f32 v91,v91
v_mfma_f32_16x16x32_bf16 v[48:51],v[132:135],v[72:75],v[48:51]
s_waitcnt lgkmcnt(3)
v_mul_f32 v32,v32,v88
v_mul_f32 v33,v33,v89
v_mfma_f32_16x16x32_bf16 v[40:43],v[136:139],v[76:79],v[40:43]
ds_read_b128 v[64:67],v2 offset:60416
v_mul_f32 v34,v34,v90
v_mfma_f32_16x16x32_bf16 v[44:47],v[140:143],v[76:79],v[44:47]
v_mul_f32 v35,v35,v91
v_cvt_pk_bf16_f32 v98,v32,v33
v_mfma_f32_16x16x32_bf16 v[48:51],v[144:147],v[76:79],v[48:51]
v_cvt_pk_bf16_f32 v99,v34,v35
v_fma_f32 v36,-v116,v24,v36
v_mfma_f32_16x16x32_bf16 v[40:43],v[148:151],v[80:83],v[40:43]
ds_read_b128 v[68:71],v2 offset:61440
v_fma_f32 v37,-v116,v25,v37
v_mfma_f32_16x16x32_bf16 v[44:47],v[152:155],v[80:83],v[44:47]
v_fma_f32 v38,-v116,v26,v38
v_fma_f32 v39,-v116,v27,v39
v_mfma_f32_16x16x32_bf16 v[48:51],v[156:159],v[80:83],v[48:51]
s_waitcnt lgkmcnt(3)
v_mul_f32 v92,v36,v36
v_mul_f32 v93,v37,v37
v_mfma_f32_16x16x32_bf16 v[40:43],v[160:163],v[52:55],v[40:43]
ds_read_b128 v[72:75],v2 offset:62464
v_mul_f32 v94,v38,v38
v_mfma_f32_16x16x32_bf16 v[44:47],v[164:167],v[52:55],v[44:47]
v_mul_f32 v95,v39,v39
v_fma_f32 v92,v118,v92,v117
v_mfma_f32_16x16x32_bf16 v[48:51],v[168:171],v[52:55],v[48:51]
v_mul_f32 v92,v36,v92
v_fma_f32 v93,v118,v93,v117
v_mfma_f32_16x16x32_bf16 v[40:43],v[172:175],v[56:59],v[40:43]
ds_read_b128 v[76:79],v2 offset:63488
v_mul_f32 v93,v37,v93
v_mfma_f32_16x16x32_bf16 v[44:47],v[176:179],v[56:59],v[44:47]
buffer_store_dwordx4 v[96:99],v3,s[36:39],s73 offen offset:0 sc1
v_exp_f32 v92,v92
v_fma_f32 v94,v118,v94,v117
v_mfma_f32_16x16x32_bf16 v[48:51],v[180:183],v[56:59],v[48:51]
s_waitcnt lgkmcnt(3)
v_exp_f32 v93,v93
v_mfma_f32_16x16x32_bf16 v[40:43],v[184:187],v[60:63],v[40:43]
ds_read_b128 v[80:83],v2 offset:64512
v_mul_f32 v94,v38,v94
v_mfma_f32_16x16x32_bf16 v[44:47],v[188:191],v[60:63],v[44:47]
s_waitcnt vmcnt(6)
v_xor_b32 v2,65536,v2
v_fma_f32 v92,v92,v119,v119
v_mfma_f32_16x16x32_bf16 v[48:51],v[192:195],v[60:63],v[48:51]
s_barrier
v_mfma_f32_16x16x32_bf16 v[40:43],v[196:199],v[64:67],v[40:43]
buffer_store_dword v5,v4,s[36:39],s70 offen offset:0 sc1
ds_read_b128 v[52:55],v2 offset:0
ds_read_b32 v13,v7 offset:256
ds_read_b64 v[112:113],v11 offset:128
v_mfma_f32_16x16x32_bf16 v[44:47],v[200:203],v[64:67],v[44:47]
v_exp_f32 v94,v94
v_mfma_f32_16x16x32_bf16 v[48:51],v[204:207],v[64:67],v[48:51]
s_waitcnt lgkmcnt(5)
v_rcp_f32 v92,v92
v_mfma_f32_16x16x32_bf16 v[40:43],v[208:211],v[68:71],v[40:43]
ds_read_b128 v[56:59],v2 offset:1024
v_fma_f32 v93,v93,v119,v119
v_mfma_f32_16x16x32_bf16 v[44:47],v[212:215],v[68:71],v[44:47]
s_waitcnt lgkmcnt(2)
v_rcp_f32 v93,v93
v_mfma_f32_16x16x32_bf16 v[48:51],v[216:219],v[68:71],v[48:51]
s_waitcnt lgkmcnt(1)
v_cmp_eq_u32 vcc,s60,v13
v_fma_f32 v94,v94,v119,v119
v_rcp_f32 v94,v94
v_mfma_f32_16x16x32_bf16 v[40:43],v[220:223],v[72:75],v[40:43]
s_cmp_eq_u32 vcc_lo,-1
ds_read_b128 v[60:63],v2 offset:2048
v_mul_f32 v116,s75,v112
v_mfma_f32_16x16x32_bf16 v[44:47],v[224:227],v[72:75],v[44:47]
s_cselect_b32 s66,s86,s59
v_fma_f32 v95,v118,v95,v117
v_mul_f32 v95,v39,v95
v_mfma_f32_16x16x32_bf16 v[48:51],v[228:231],v[72:75],v[48:51]
v_exp_f32 v95,v95
v_mfma_f32_16x16x32_bf16 v[40:43],v[232:235],v[76:79],v[40:43]
s_lshr_b32 s61,s66,0
ds_read_b128 v[64:67],v2 offset:3072
v_fma_f32 v95,v95,v119,v119
v_mfma_f32_16x16x32_bf16 v[44:47],v[236:239],v[76:79],v[44:47]
s_xor_b32 s64,s64,65536
v_rcp_f32 v95,v95
v_mfma_f32_16x16x32_bf16 v[48:51],v[240:243],v[76:79],v[48:51]
s_cmp_lt_u32 s67,s59
v_mul_f32 v36,v36,v92
v_mul_f32 v37,v37,v93
v_mul_f32 v38,v38,v94
v_mul_f32 v39,v39,v95
v_cvt_pk_bf16_f32 v100,v36,v37
v_cvt_pk_bf16_f32 v101,v38,v39
v_mfma_f32_16x16x32_bf16 v[40:43],v[244:247],v[80:83],v[40:43]
buffer_store_dwordx2 v[100:101],v9,s[36:39],s73 offen offset:0 sc1
ds_read_b128 v[68:71],v2 offset:4096
v_fma_f32 v113,v113,s75,v12
v_fma_f32 v113,-v116,v116,v113
v_mfma_f32_16x16x32_bf16 v[44:47],v[248:251],v[80:83],v[44:47]
s_waitcnt lgkmcnt(3)
v_max_f32 v113,v113,v12
v_rsq_f32 v112,v113
v_mfma_f32_16x16x32_bf16 v[48:51],v[252:255],v[80:83],v[48:51]
s_cbranch_scc1 Lg3_d0w3_%=
s_branch Lg3_slow0_%=
Lg3_slow0_%=:
s_cmp_lt_u32 s66,s59
s_cbranch_scc1 Lg3_gs0_%=
Lg3_slowb0_%=:
s_cmp_eq_u32 s54,1
s_cselect_b32 s68,s49,s48
s_lshr_b32 s68,s68,5
s_add_u32 m0,s78,8192
s_nop 0
buffer_load_dword v6,s[36:39],s68 offen offset:0 sc1 lds
s_mul_i32 s73,s47,7
s_lshr_b32 s73,s73,1
v_mul_f32 v117,s76,v112
v_mul_f32 v118,v112,v112
v_mul_f32 v119,v113,v112
v_mul_f32 v118,s77,v118
v_mul_f32 v118,v118,v117
v_fma_f32 v40,-v116,v16,v40
v_fma_f32 v41,-v116,v17,v41
v_fma_f32 v42,-v116,v18,v42
v_fma_f32 v43,-v116,v19,v43
v_mul_f32 v84,v40,v40
v_mul_f32 v85,v41,v41
v_mul_f32 v86,v42,v42
v_mul_f32 v87,v43,v43
v_fma_f32 v84,v118,v84,v117
v_fma_f32 v85,v118,v85,v117
v_fma_f32 v86,v118,v86,v117
v_fma_f32 v87,v118,v87,v117
v_mul_f32 v84,v40,v84
v_mul_f32 v85,v41,v85
v_mul_f32 v86,v42,v86
v_mul_f32 v87,v43,v87
v_exp_f32 v84,v84
v_exp_f32 v85,v85
v_exp_f32 v86,v86
v_exp_f32 v87,v87
v_fma_f32 v84,v84,v119,v119
v_fma_f32 v85,v85,v119,v119
v_fma_f32 v86,v86,v119,v119
v_fma_f32 v87,v87,v119,v119
v_rcp_f32 v84,v84
v_rcp_f32 v85,v85
v_rcp_f32 v86,v86
v_rcp_f32 v87,v87
v_mul_f32 v40,v40,v84
v_mul_f32 v41,v41,v85
v_mul_f32 v42,v42,v86
v_mul_f32 v43,v43,v87
v_cvt_pk_bf16_f32 v96,v40,v41
v_cvt_pk_bf16_f32 v97,v42,v43
v_fma_f32 v44,-v116,v20,v44
v_fma_f32 v45,-v116,v21,v45
v_fma_f32 v46,-v116,v22,v46
v_fma_f32 v47,-v116,v23,v47
v_mul_f32 v88,v44,v44
v_mul_f32 v89,v45,v45
v_mul_f32 v90,v46,v46
v_mul_f32 v91,v47,v47
v_fma_f32 v88,v118,v88,v117
v_fma_f32 v89,v118,v89,v117
v_fma_f32 v90,v118,v90,v117
v_fma_f32 v91,v118,v91,v117
v_mul_f32 v88,v44,v88
v_mul_f32 v89,v45,v89
v_mul_f32 v90,v46,v90
s_add_u32 m0,s78,8448
s_nop 0
buffer_load_dword v6,s[36:39],s68 offen offset:0 sc1 lds
v_mul_f32 v91,v47,v91
v_exp_f32 v88,v88
v_exp_f32 v89,v89
v_exp_f32 v90,v90
v_exp_f32 v91,v91
v_fma_f32 v88,v88,v119,v119
v_fma_f32 v89,v89,v119,v119
v_fma_f32 v90,v90,v119,v119
v_fma_f32 v91,v91,v119,v119
v_rcp_f32 v88,v88
v_rcp_f32 v89,v89
v_rcp_f32 v90,v90
v_rcp_f32 v91,v91
v_mul_f32 v44,v44,v88)"
R"(
v_mul_f32 v45,v45,v89
v_mul_f32 v46,v46,v90
v_mul_f32 v47,v47,v91
v_cvt_pk_bf16_f32 v98,v44,v45
v_cvt_pk_bf16_f32 v99,v46,v47
buffer_store_dwordx4 v[96:99],v3,s[36:39],s73 offen offset:0 sc1
v_fma_f32 v48,-v116,v24,v48
v_fma_f32 v49,-v116,v25,v49
v_fma_f32 v50,-v116,v26,v50
v_fma_f32 v51,-v116,v27,v51
v_mul_f32 v92,v48,v48
v_mul_f32 v93,v49,v49
v_mul_f32 v94,v50,v50
v_mul_f32 v95,v51,v51
v_fma_f32 v92,v118,v92,v117
v_fma_f32 v93,v118,v93,v117
v_fma_f32 v94,v118,v94,v117
v_fma_f32 v95,v118,v95,v117
v_mul_f32 v92,v48,v92
v_mul_f32 v93,v49,v93
v_mul_f32 v94,v50,v94
v_mul_f32 v95,v51,v95
v_exp_f32 v92,v92
v_exp_f32 v93,v93
v_exp_f32 v94,v94
v_exp_f32 v95,v95
v_fma_f32 v92,v92,v119,v119
v_fma_f32 v93,v93,v119,v119
v_fma_f32 v94,v94,v119,v119
v_fma_f32 v95,v95,v119,v119
v_rcp_f32 v92,v92
v_rcp_f32 v93,v93
v_rcp_f32 v94,v94
v_rcp_f32 v95,v95
v_mul_f32 v48,v48,v92
v_mul_f32 v49,v49,v93
v_mul_f32 v50,v50,v94
v_mul_f32 v51,v51,v95
v_cvt_pk_bf16_f32 v100,v48,v49
v_cvt_pk_bf16_f32 v101,v50,v51
buffer_store_dwordx2 v[100:101],v9,s[36:39],s73 offen offset:0 sc1
s_cmp_ge_u32 s48,s59
s_cbranch_scc1 Lg3_done0_%=
s_add_u32 m0,s78,16384
s_nop 0
buffer_load_dword v6,s[36:39],s68 offen offset:0 sc1 lds
s_waitcnt vmcnt(4)
s_barrier
ds_read_b32 v13,v7 offset:8192
s_mul_i32 s70,s46,5
s_lshr_b32 s70,s70,6
buffer_store_dword v5,v4,s[36:39],s70 offen offset:0 sc1
s_mov_b32 s46,s59
s_mov_b32 s69,18432
s_waitcnt lgkmcnt(0)
v_cmp_eq_u32 vcc,s60,v13
s_nop 0
s_cmp_eq_u32 vcc_lo,-1
s_cbranch_scc1 Lg3_go0_%=
s_add_u32 m0,s78,16640
s_nop 0
buffer_load_dword v6,s[36:39],s68 offen offset:0 sc1 lds
s_waitcnt vmcnt(5)
s_barrier
ds_read_b32 v13,v7 offset:8448
s_mov_b32 s69,18688
s_waitcnt lgkmcnt(0)
v_cmp_eq_u32 vcc,s60,v13
s_nop 0
s_cmp_eq_u32 vcc_lo,-1
s_cbranch_scc1 Lg3_go0_%=
s_add_u32 m0,s78,8192
s_nop 0
buffer_load_dword v6,s[36:39],s68 offen offset:0 sc1 lds
s_waitcnt vmcnt(3)
s_barrier
ds_read_b32 v13,v7 offset:16384
s_mul_i32 s70,s47,5
s_lshr_b32 s70,s70,6
buffer_store_dword v5,v4,s[36:39],s70 offen offset:0 sc1
s_mov_b32 s47,s59
s_mov_b32 s69,10240
s_waitcnt lgkmcnt(0)
v_cmp_eq_u32 vcc,s60,v13
s_nop 0
s_cmp_eq_u32 vcc_lo,-1
s_cbranch_scc1 Lg3_go0_%=
s_add_u32 m0,s78,8448
s_nop 0
buffer_load_dword v6,s[36:39],s68 offen offset:0 sc1 lds
s_waitcnt vmcnt(3)
s_barrier
ds_read_b32 v13,v7 offset:16640
s_mov_b32 s69,10496
s_waitcnt lgkmcnt(0)
v_cmp_eq_u32 vcc,s60,v13
s_nop 0
s_cmp_eq_u32 vcc_lo,-1
s_cbranch_scc1 Lg3_go0_%=
Lg3_poll0_%=:
s_add_u32 m0,s78,16384
s_nop 0
buffer_load_dword v6,s[36:39],s68 offen offset:0 sc1 lds
s_waitcnt vmcnt(2)
s_barrier
ds_read_b32 v13,v7 offset:8192
s_mov_b32 s69,18432
s_waitcnt lgkmcnt(0)
v_cmp_eq_u32 vcc,s60,v13
s_nop 0
s_cmp_eq_u32 vcc_lo,-1
s_cbranch_scc1 Lg3_go0_%=
s_sleep 4
s_add_u32 m0,s78,16640
s_nop 0
buffer_load_dword v6,s[36:39],s68 offen offset:0 sc1 lds
s_waitcnt vmcnt(2)
s_barrier
ds_read_b32 v13,v7 offset:8448
s_mov_b32 s69,18688
s_waitcnt lgkmcnt(0)
v_cmp_eq_u32 vcc,s60,v13
s_nop 0
s_cmp_eq_u32 vcc_lo,-1
s_cbranch_scc1 Lg3_go0_%=
s_sleep 4
s_add_u32 m0,s78,8192
s_nop 0
buffer_load_dword v6,s[36:39],s68 offen offset:0 sc1 lds
s_waitcnt vmcnt(2)
s_barrier
ds_read_b32 v13,v7 offset:16384
s_mov_b32 s69,10240
s_waitcnt lgkmcnt(0)
v_cmp_eq_u32 vcc,s60,v13
s_nop 0
s_cmp_eq_u32 vcc_lo,-1
s_cbranch_scc1 Lg3_go0_%=
s_sleep 4
s_add_u32 m0,s78,8448
s_nop 0
buffer_load_dword v6,s[36:39],s68 offen offset:0 sc1 lds
s_waitcnt vmcnt(2)
s_barrier
ds_read_b32 v13,v7 offset:16640
s_mov_b32 s69,10496
s_waitcnt lgkmcnt(0)
v_cmp_eq_u32 vcc,s60,v13
s_nop 0
s_cmp_eq_u32 vcc_lo,-1
s_cbranch_scc1 Lg3_go0_%=
s_sleep 4
s_add_u32 s79,s79,1
s_cmp_lt_u32 s79,20000
s_cbranch_scc1 Lg3_poll0_%=
Lg3_go0_%=:
s_xor_b32 s62,s63,65536
s_lshr_b32 s61,s48,0
s_mov_b32 m0,s62
s_nop 0
buffer_load_dwordx4 v0,s[36:39],s61 offen offset:0 sc1 lds
buffer_load_dwordx4 v0,s[36:39],s61 offen offset:1024 sc1 lds
buffer_load_dwordx4 v0,s[36:39],s61 offen offset:2048 sc1 lds
buffer_load_dwordx4 v0,s[36:39],s61 offen offset:3072 sc1 lds
s_add_u32 m0,s62,16384
s_nop 0
buffer_load_dwordx4 v1,s[36:39],s61 offen offset:0 sc1 lds
buffer_load_dwordx4 v1,s[36:39],s61 offen offset:1024 sc1 lds
buffer_load_dwordx4 v1,s[36:39],s61 offen offset:2048 sc1 lds
buffer_load_dwordx4 v1,s[36:39],s61 offen offset:3072 sc1 lds
s_lshr_b32 s72,s61,5
buffer_load_dword v104,v8,s[36:39],s72 offen offset:0 sc1
v_add_u32 v84,s69,v7
s_waitcnt vmcnt(5)
s_barrier
ds_read_b32 v13,v84 offset:0
ds_read_b128 v[52:55],v2 offset:0
ds_read_b128 v[56:59],v2 offset:1024
ds_read_b128 v[60:63],v2 offset:2048
ds_read_b128 v[64:67],v2 offset:3072
ds_read_b128 v[68:71],v2 offset:4096
s_mul_i32 s70,s46,5
s_lshr_b32 s70,s70,6
buffer_store_dword v5,v4,s[36:39],s70 offen offset:0 sc1
s_mov_b32 s46,s59
s_mul_i32 s70,s47,5
s_lshr_b32 s70,s70,6
buffer_store_dword v5,v4,s[36:39],s70 offen offset:0 sc1
s_mov_b32 s47,s59
s_mul_i32 s70,s59,5
s_lshr_b32 s70,s70,6
buffer_store_dword v5,v4,s[36:39],s70 offen offset:0 sc1
s_waitcnt lgkmcnt(5)
v_cmp_eq_u32 vcc,s60,v13
s_nop 0
s_cmp_eq_u32 vcc_lo,-1
s_cselect_b32 s66,s49,s59
s_lshr_b32 s61,s66,0
s_xor_b32 s62,s64,65536
s_waitcnt lgkmcnt(3))"
R"(
s_setpc_b64 s[90:91]
Lg3_gs0_%=:
s_xor_b32 s62,s63,65536
s_lshr_b32 s61,s48,0
s_cmp_eq_u32 s54,1
s_cselect_b32 s68,s49,s48
s_lshr_b32 s68,s68,5
s_mul_i32 s73,s47,7
s_lshr_b32 s73,s73,1
s_mov_b32 m0,s62
s_nop 0
buffer_load_dwordx4 v0,s[36:39],s61 offen offset:0 sc1 lds
v_mul_f32 v117,s76,v112
v_mul_f32 v118,v112,v112
v_mul_f32 v119,v113,v112
v_mul_f32 v118,s77,v118
v_mul_f32 v118,v118,v117
v_fma_f32 v40,-v116,v16,v40
v_fma_f32 v41,-v116,v17,v41
v_fma_f32 v42,-v116,v18,v42
v_fma_f32 v43,-v116,v19,v43
v_mul_f32 v84,v40,v40
buffer_load_dwordx4 v0,s[36:39],s61 offen offset:1024 sc1 lds
v_mul_f32 v85,v41,v41
v_mul_f32 v86,v42,v42
v_mul_f32 v87,v43,v43
v_fma_f32 v84,v118,v84,v117
v_fma_f32 v85,v118,v85,v117
v_fma_f32 v86,v118,v86,v117
v_fma_f32 v87,v118,v87,v117
v_mul_f32 v84,v40,v84
v_mul_f32 v85,v41,v85
v_mul_f32 v86,v42,v86
buffer_load_dwordx4 v0,s[36:39],s61 offen offset:2048 sc1 lds
v_mul_f32 v87,v43,v87
v_exp_f32 v84,v84
v_exp_f32 v85,v85
v_exp_f32 v86,v86
v_exp_f32 v87,v87
v_fma_f32 v84,v84,v119,v119
v_fma_f32 v85,v85,v119,v119
v_fma_f32 v86,v86,v119,v119
v_fma_f32 v87,v87,v119,v119
v_rcp_f32 v84,v84
buffer_load_dwordx4 v0,s[36:39],s61 offen offset:3072 sc1 lds
v_rcp_f32 v85,v85
v_rcp_f32 v86,v86
v_rcp_f32 v87,v87
v_mul_f32 v40,v40,v84
v_mul_f32 v41,v41,v85
v_mul_f32 v42,v42,v86
v_mul_f32 v43,v43,v87
v_cvt_pk_bf16_f32 v96,v40,v41
v_cvt_pk_bf16_f32 v97,v42,v43
v_fma_f32 v44,-v116,v20,v44
s_add_u32 m0,s78,8192
s_nop 0
buffer_load_dword v6,s[36:39],s68 offen offset:0 sc1 lds
v_fma_f32 v45,-v116,v21,v45
v_fma_f32 v46,-v116,v22,v46
v_fma_f32 v47,-v116,v23,v47
v_mul_f32 v88,v44,v44
v_mul_f32 v89,v45,v45
v_mul_f32 v90,v46,v46
v_mul_f32 v91,v47,v47
v_fma_f32 v88,v118,v88,v117
v_fma_f32 v89,v118,v89,v117
v_fma_f32 v90,v118,v90,v117
s_add_u32 m0,s62,16384
s_nop 0
buffer_load_dwordx4 v1,s[36:39],s61 offen offset:0 sc1 lds
v_fma_f32 v91,v118,v91,v117
v_mul_f32 v88,v44,v88
v_mul_f32 v89,v45,v89
v_mul_f32 v90,v46,v90
v_mul_f32 v91,v47,v91
v_exp_f32 v88,v88
v_exp_f32 v89,v89
v_exp_f32 v90,v90
v_exp_f32 v91,v91
v_fma_f32 v88,v88,v119,v119
buffer_load_dwordx4 v1,s[36:39],s61 offen offset:1024 sc1 lds
v_fma_f32 v89,v89,v119,v119
v_fma_f32 v90,v90,v119,v119
v_fma_f32 v91,v91,v119,v119
v_rcp_f32 v88,v88
v_rcp_f32 v89,v89
v_rcp_f32 v90,v90
v_rcp_f32 v91,v91
v_mul_f32 v44,v44,v88
v_mul_f32 v45,v45,v89
v_mul_f32 v46,v46,v90
buffer_load_dwordx4 v1,s[36:39],s61 offen offset:2048 sc1 lds
v_mul_f32 v47,v47,v91
v_cvt_pk_bf16_f32 v98,v44,v45
v_cvt_pk_bf16_f32 v99,v46,v47
buffer_store_dwordx4 v[96:99],v3,s[36:39],s73 offen offset:0 sc1
v_fma_f32 v48,-v116,v24,v48
v_fma_f32 v49,-v116,v25,v49
v_fma_f32 v50,-v116,v26,v50
v_fma_f32 v51,-v116,v27,v51
v_mul_f32 v92,v48,v48
v_mul_f32 v93,v49,v49
buffer_load_dwordx4 v1,s[36:39],s61 offen offset:3072 sc1 lds
v_mul_f32 v94,v50,v50
v_mul_f32 v95,v51,v51
v_fma_f32 v92,v118,v92,v117
v_fma_f32 v93,v118,v93,v117
v_fma_f32 v94,v118,v94,v117
v_fma_f32 v95,v118,v95,v117
v_mul_f32 v92,v48,v92
v_mul_f32 v93,v49,v93
v_mul_f32 v94,v50,v94
v_mul_f32 v95,v51,v95
s_lshr_b32 s72,s61,5
buffer_load_dword v104,v8,s[36:39],s72 offen offset:0 sc1
v_exp_f32 v92,v92
v_exp_f32 v93,v93
v_exp_f32 v94,v94
v_exp_f32 v95,v95
v_fma_f32 v92,v92,v119,v119
v_fma_f32 v93,v93,v119,v119
v_fma_f32 v94,v94,v119,v119
v_fma_f32 v95,v95,v119,v119
v_rcp_f32 v92,v92
v_rcp_f32 v93,v93
v_rcp_f32 v94,v94
v_rcp_f32 v95,v95
v_mul_f32 v48,v48,v92
v_mul_f32 v49,v49,v93
v_mul_f32 v50,v50,v94
v_mul_f32 v51,v51,v95
v_cvt_pk_bf16_f32 v100,v48,v49
v_cvt_pk_bf16_f32 v101,v50,v51
buffer_store_dwordx2 v[100:101],v9,s[36:39],s73 offen offset:0 sc1
s_waitcnt vmcnt(7)
s_barrier
ds_read_b32 v13,v7 offset:8192
ds_read_b128 v[52:55],v2 offset:0
ds_read_b128 v[56:59],v2 offset:1024
ds_read_b128 v[60:63],v2 offset:2048
ds_read_b128 v[64:67],v2 offset:3072
ds_read_b128 v[68:71],v2 offset:4096
s_mul_i32 s70,s46,5
s_lshr_b32 s70,s70,6
buffer_store_dword v5,v4,s[36:39],s70 offen offset:0 sc1
s_waitcnt lgkmcnt(5)
v_cmp_eq_u32 vcc,s60,v13
s_nop 0
s_cmp_eq_u32 vcc_hi,-1
s_cselect_b32 s66,s49,s59
s_lshr_b32 s61,s66,0
s_xor_b32 s62,s64,65536
s_mov_b32 s46,s47
s_mov_b32 s47,s59
s_mul_i32 s70,s59,5
s_lshr_b32 s70,s70,6
buffer_store_dword v5,v4,s[36:39],s70 offen offset:0 sc1
s_waitcnt lgkmcnt(3)
s_setpc_b64 s[90:91]
Lg3_done0_%=:
s_waitcnt vmcnt(0)
s_barrier
s_mul_i32 s70,s46,5
s_lshr_b32 s70,s70,6
buffer_store_dword v5,v4,s[36:39],s70 offen offset:0 sc1
s_mul_i32 s70,s47,5
s_lshr_b32 s70,s70,6
buffer_store_dword v5,v4,s[36:39],s70 offen offset:0 sc1
s_branch Lg3_done_%=
Lg3_slow1_%=:
s_cmp_lt_u32 s67,s59
s_cbranch_scc1 Lg3_gs1_%=
Lg3_slowb1_%=:
s_cmp_eq_u32 s54,1
s_cselect_b32 s68,s50,s49
s_lshr_b32 s68,s68,5
s_add_u32 m0,s78,8192
s_nop 0
buffer_load_dword v6,s[36:39],s68 offen offset:0 sc1 lds
s_mul_i32 s73,s48,7
s_lshr_b32 s73,s73,1
v_mul_f32 v117,s76,v112
v_mul_f32 v118,v112,v112
v_mul_f32 v119,v113,v112
v_mul_f32 v118,s77,v118
v_mul_f32 v118,v118,v117
v_fma_f32 v28,-v116,v16,v28
v_fma_f32 v29,-v116,v17,v29
v_fma_f32 v30,-v116,v18,v30
v_fma_f32 v31,-v116,v19,v31
v_mul_f32 v84,v28,v28
v_mul_f32 v85,v29,v29
v_mul_f32 v86,v30,v30
v_mul_f32 v87,v31,v31
v_fma_f32 v84,v118,v84,v117
v_fma_f32 v85,v118,v85,v117
v_fma_f32 v86,v118,v86,v117
v_fma_f32 v87,v118,v87,v117
v_mul_f32 v84,v28,v84)"
R"(
v_mul_f32 v85,v29,v85
v_mul_f32 v86,v30,v86
v_mul_f32 v87,v31,v87
v_exp_f32 v84,v84
v_exp_f32 v85,v85
v_exp_f32 v86,v86
v_exp_f32 v87,v87
v_fma_f32 v84,v84,v119,v119
v_fma_f32 v85,v85,v119,v119
v_fma_f32 v86,v86,v119,v119
v_fma_f32 v87,v87,v119,v119
v_rcp_f32 v84,v84
v_rcp_f32 v85,v85
v_rcp_f32 v86,v86
v_rcp_f32 v87,v87
v_mul_f32 v28,v28,v84
v_mul_f32 v29,v29,v85
v_mul_f32 v30,v30,v86
v_mul_f32 v31,v31,v87
v_cvt_pk_bf16_f32 v96,v28,v29
v_cvt_pk_bf16_f32 v97,v30,v31
v_fma_f32 v32,-v116,v20,v32
v_fma_f32 v33,-v116,v21,v33
v_fma_f32 v34,-v116,v22,v34
v_fma_f32 v35,-v116,v23,v35
v_mul_f32 v88,v32,v32
v_mul_f32 v89,v33,v33
v_mul_f32 v90,v34,v34
v_mul_f32 v91,v35,v35
v_fma_f32 v88,v118,v88,v117
v_fma_f32 v89,v118,v89,v117
v_fma_f32 v90,v118,v90,v117
v_fma_f32 v91,v118,v91,v117
v_mul_f32 v88,v32,v88
v_mul_f32 v89,v33,v89
v_mul_f32 v90,v34,v90
s_add_u32 m0,s78,8448
s_nop 0
buffer_load_dword v6,s[36:39],s68 offen offset:0 sc1 lds
v_mul_f32 v91,v35,v91
v_exp_f32 v88,v88
v_exp_f32 v89,v89
v_exp_f32 v90,v90
v_exp_f32 v91,v91
v_fma_f32 v88,v88,v119,v119
v_fma_f32 v89,v89,v119,v119
v_fma_f32 v90,v90,v119,v119
v_fma_f32 v91,v91,v119,v119
v_rcp_f32 v88,v88
v_rcp_f32 v89,v89
v_rcp_f32 v90,v90
v_rcp_f32 v91,v91
v_mul_f32 v32,v32,v88
v_mul_f32 v33,v33,v89
v_mul_f32 v34,v34,v90
v_mul_f32 v35,v35,v91
v_cvt_pk_bf16_f32 v98,v32,v33
v_cvt_pk_bf16_f32 v99,v34,v35
buffer_store_dwordx4 v[96:99],v3,s[36:39],s73 offen offset:0 sc1
v_fma_f32 v36,-v116,v24,v36
v_fma_f32 v37,-v116,v25,v37
v_fma_f32 v38,-v116,v26,v38
v_fma_f32 v39,-v116,v27,v39
v_mul_f32 v92,v36,v36
v_mul_f32 v93,v37,v37
v_mul_f32 v94,v38,v38
v_mul_f32 v95,v39,v39
v_fma_f32 v92,v118,v92,v117
v_fma_f32 v93,v118,v93,v117
v_fma_f32 v94,v118,v94,v117
v_fma_f32 v95,v118,v95,v117
v_mul_f32 v92,v36,v92
v_mul_f32 v93,v37,v93
v_mul_f32 v94,v38,v94
v_mul_f32 v95,v39,v95
v_exp_f32 v92,v92
v_exp_f32 v93,v93
v_exp_f32 v94,v94
v_exp_f32 v95,v95
v_fma_f32 v92,v92,v119,v119
v_fma_f32 v93,v93,v119,v119
v_fma_f32 v94,v94,v119,v119
v_fma_f32 v95,v95,v119,v119
v_rcp_f32 v92,v92
v_rcp_f32 v93,v93
v_rcp_f32 v94,v94
v_rcp_f32 v95,v95
v_mul_f32 v36,v36,v92
v_mul_f32 v37,v37,v93
v_mul_f32 v38,v38,v94
v_mul_f32 v39,v39,v95
v_cvt_pk_bf16_f32 v100,v36,v37
v_cvt_pk_bf16_f32 v101,v38,v39
buffer_store_dwordx2 v[100:101],v9,s[36:39],s73 offen offset:0 sc1
s_cmp_ge_u32 s49,s59
s_cbranch_scc1 Lg3_done1_%=
s_add_u32 m0,s78,16384
s_nop 0
buffer_load_dword v6,s[36:39],s68 offen offset:0 sc1 lds
s_waitcnt vmcnt(4)
s_barrier
ds_read_b32 v13,v7 offset:8192
s_mul_i32 s70,s47,5
s_lshr_b32 s70,s70,6
buffer_store_dword v5,v4,s[36:39],s70 offen offset:0 sc1
s_mov_b32 s47,s59
s_mov_b32 s69,18432
s_waitcnt lgkmcnt(0)
v_cmp_eq_u32 vcc,s60,v13
s_nop 0
s_cmp_eq_u32 vcc_lo,-1
s_cbranch_scc1 Lg3_go1_%=
s_add_u32 m0,s78,16640
s_nop 0
buffer_load_dword v6,s[36:39],s68 offen offset:0 sc1 lds
s_waitcnt vmcnt(5)
s_barrier
ds_read_b32 v13,v7 offset:8448
s_mov_b32 s69,18688
s_waitcnt lgkmcnt(0)
v_cmp_eq_u32 vcc,s60,v13
s_nop 0
s_cmp_eq_u32 vcc_lo,-1
s_cbranch_scc1 Lg3_go1_%=
s_add_u32 m0,s78,8192
s_nop 0
buffer_load_dword v6,s[36:39],s68 offen offset:0 sc1 lds
s_waitcnt vmcnt(3)
s_barrier
ds_read_b32 v13,v7 offset:16384
s_mul_i32 s70,s48,5
s_lshr_b32 s70,s70,6
buffer_store_dword v5,v4,s[36:39],s70 offen offset:0 sc1
s_mov_b32 s48,s59
s_mov_b32 s69,10240
s_waitcnt lgkmcnt(0)
v_cmp_eq_u32 vcc,s60,v13
s_nop 0
s_cmp_eq_u32 vcc_lo,-1
s_cbranch_scc1 Lg3_go1_%=
s_add_u32 m0,s78,8448
s_nop 0
buffer_load_dword v6,s[36:39],s68 offen offset:0 sc1 lds
s_waitcnt vmcnt(3)
s_barrier
ds_read_b32 v13,v7 offset:16640
s_mov_b32 s69,10496
s_waitcnt lgkmcnt(0)
v_cmp_eq_u32 vcc,s60,v13
s_nop 0
s_cmp_eq_u32 vcc_lo,-1
s_cbranch_scc1 Lg3_go1_%=
Lg3_poll1_%=:
s_add_u32 m0,s78,16384
s_nop 0
buffer_load_dword v6,s[36:39],s68 offen offset:0 sc1 lds
s_waitcnt vmcnt(2)
s_barrier
ds_read_b32 v13,v7 offset:8192
s_mov_b32 s69,18432
s_waitcnt lgkmcnt(0)
v_cmp_eq_u32 vcc,s60,v13
s_nop 0
s_cmp_eq_u32 vcc_lo,-1
s_cbranch_scc1 Lg3_go1_%=
s_sleep 4
s_add_u32 m0,s78,16640
s_nop 0
buffer_load_dword v6,s[36:39],s68 offen offset:0 sc1 lds
s_waitcnt vmcnt(2)
s_barrier
ds_read_b32 v13,v7 offset:8448
s_mov_b32 s69,18688
s_waitcnt lgkmcnt(0)
v_cmp_eq_u32 vcc,s60,v13
s_nop 0
s_cmp_eq_u32 vcc_lo,-1
s_cbranch_scc1 Lg3_go1_%=
s_sleep 4
s_add_u32 m0,s78,8192
s_nop 0
buffer_load_dword v6,s[36:39],s68 offen offset:0 sc1 lds
s_waitcnt vmcnt(2)
s_barrier
ds_read_b32 v13,v7 offset:16384
s_mov_b32 s69,10240
s_waitcnt lgkmcnt(0)
v_cmp_eq_u32 vcc,s60,v13
s_nop 0
s_cmp_eq_u32 vcc_lo,-1
s_cbranch_scc1 Lg3_go1_%=
s_sleep 4
s_add_u32 m0,s78,8448
s_nop 0
buffer_load_dword v6,s[36:39],s68 offen offset:0 sc1 lds
s_waitcnt vmcnt(2)
s_barrier
ds_read_b32 v13,v7 offset:16640
s_mov_b32 s69,10496
s_waitcnt lgkmcnt(0))"
R"(
v_cmp_eq_u32 vcc,s60,v13
s_nop 0
s_cmp_eq_u32 vcc_lo,-1
s_cbranch_scc1 Lg3_go1_%=
s_sleep 4
s_add_u32 s79,s79,1
s_cmp_lt_u32 s79,20000
s_cbranch_scc1 Lg3_poll1_%=
Lg3_go1_%=:
s_xor_b32 s62,s64,65536
s_lshr_b32 s61,s49,0
s_mov_b32 m0,s62
s_nop 0
buffer_load_dwordx4 v0,s[36:39],s61 offen offset:0 sc1 lds
buffer_load_dwordx4 v0,s[36:39],s61 offen offset:1024 sc1 lds
buffer_load_dwordx4 v0,s[36:39],s61 offen offset:2048 sc1 lds
buffer_load_dwordx4 v0,s[36:39],s61 offen offset:3072 sc1 lds
s_add_u32 m0,s62,16384
s_nop 0
buffer_load_dwordx4 v1,s[36:39],s61 offen offset:0 sc1 lds
buffer_load_dwordx4 v1,s[36:39],s61 offen offset:1024 sc1 lds
buffer_load_dwordx4 v1,s[36:39],s61 offen offset:2048 sc1 lds
buffer_load_dwordx4 v1,s[36:39],s61 offen offset:3072 sc1 lds
s_lshr_b32 s72,s61,5
buffer_load_dword v108,v8,s[36:39],s72 offen offset:0 sc1
v_add_u32 v84,s69,v7
s_waitcnt vmcnt(5)
s_barrier
ds_read_b32 v13,v84 offset:0
ds_read_b128 v[52:55],v2 offset:32768
ds_read_b128 v[56:59],v2 offset:33792
ds_read_b128 v[60:63],v2 offset:34816
ds_read_b128 v[64:67],v2 offset:35840
ds_read_b128 v[68:71],v2 offset:36864
s_mul_i32 s70,s47,5
s_lshr_b32 s70,s70,6
buffer_store_dword v5,v4,s[36:39],s70 offen offset:0 sc1
s_mov_b32 s47,s59
s_mul_i32 s70,s48,5
s_lshr_b32 s70,s70,6
buffer_store_dword v5,v4,s[36:39],s70 offen offset:0 sc1
s_mov_b32 s48,s59
s_mul_i32 s70,s59,5
s_lshr_b32 s70,s70,6
buffer_store_dword v5,v4,s[36:39],s70 offen offset:0 sc1
s_waitcnt lgkmcnt(5)
v_cmp_eq_u32 vcc,s60,v13
s_nop 0
s_cmp_eq_u32 vcc_lo,-1
s_cselect_b32 s67,s50,s59
s_lshr_b32 s61,s67,0
s_xor_b32 s62,s63,65536
s_waitcnt lgkmcnt(3)
s_setpc_b64 s[92:93]
Lg3_gs1_%=:
s_xor_b32 s62,s64,65536
s_lshr_b32 s61,s49,0
s_cmp_eq_u32 s54,1
s_cselect_b32 s68,s50,s49
s_lshr_b32 s68,s68,5
s_mul_i32 s73,s48,7
s_lshr_b32 s73,s73,1
s_mov_b32 m0,s62
s_nop 0
buffer_load_dwordx4 v0,s[36:39],s61 offen offset:0 sc1 lds
v_mul_f32 v117,s76,v112
v_mul_f32 v118,v112,v112
v_mul_f32 v119,v113,v112
v_mul_f32 v118,s77,v118
v_mul_f32 v118,v118,v117
v_fma_f32 v28,-v116,v16,v28
v_fma_f32 v29,-v116,v17,v29
v_fma_f32 v30,-v116,v18,v30
v_fma_f32 v31,-v116,v19,v31
v_mul_f32 v84,v28,v28
buffer_load_dwordx4 v0,s[36:39],s61 offen offset:1024 sc1 lds
v_mul_f32 v85,v29,v29
v_mul_f32 v86,v30,v30
v_mul_f32 v87,v31,v31
v_fma_f32 v84,v118,v84,v117
v_fma_f32 v85,v118,v85,v117
v_fma_f32 v86,v118,v86,v117
v_fma_f32 v87,v118,v87,v117
v_mul_f32 v84,v28,v84
v_mul_f32 v85,v29,v85
v_mul_f32 v86,v30,v86
buffer_load_dwordx4 v0,s[36:39],s61 offen offset:2048 sc1 lds
v_mul_f32 v87,v31,v87
v_exp_f32 v84,v84
v_exp_f32 v85,v85
v_exp_f32 v86,v86
v_exp_f32 v87,v87
v_fma_f32 v84,v84,v119,v119
v_fma_f32 v85,v85,v119,v119
v_fma_f32 v86,v86,v119,v119
v_fma_f32 v87,v87,v119,v119
v_rcp_f32 v84,v84
buffer_load_dwordx4 v0,s[36:39],s61 offen offset:3072 sc1 lds
v_rcp_f32 v85,v85
v_rcp_f32 v86,v86
v_rcp_f32 v87,v87
v_mul_f32 v28,v28,v84
v_mul_f32 v29,v29,v85
v_mul_f32 v30,v30,v86
v_mul_f32 v31,v31,v87
v_cvt_pk_bf16_f32 v96,v28,v29
v_cvt_pk_bf16_f32 v97,v30,v31
v_fma_f32 v32,-v116,v20,v32
s_add_u32 m0,s78,8192
s_nop 0
buffer_load_dword v6,s[36:39],s68 offen offset:0 sc1 lds
v_fma_f32 v33,-v116,v21,v33
v_fma_f32 v34,-v116,v22,v34
v_fma_f32 v35,-v116,v23,v35
v_mul_f32 v88,v32,v32
v_mul_f32 v89,v33,v33
v_mul_f32 v90,v34,v34
v_mul_f32 v91,v35,v35
v_fma_f32 v88,v118,v88,v117
v_fma_f32 v89,v118,v89,v117
v_fma_f32 v90,v118,v90,v117
s_add_u32 m0,s62,16384
s_nop 0
buffer_load_dwordx4 v1,s[36:39],s61 offen offset:0 sc1 lds
v_fma_f32 v91,v118,v91,v117
v_mul_f32 v88,v32,v88
v_mul_f32 v89,v33,v89
v_mul_f32 v90,v34,v90
v_mul_f32 v91,v35,v91
v_exp_f32 v88,v88
v_exp_f32 v89,v89
v_exp_f32 v90,v90
v_exp_f32 v91,v91
v_fma_f32 v88,v88,v119,v119
buffer_load_dwordx4 v1,s[36:39],s61 offen offset:1024 sc1 lds
v_fma_f32 v89,v89,v119,v119
v_fma_f32 v90,v90,v119,v119
v_fma_f32 v91,v91,v119,v119
v_rcp_f32 v88,v88
v_rcp_f32 v89,v89
v_rcp_f32 v90,v90
v_rcp_f32 v91,v91
v_mul_f32 v32,v32,v88
v_mul_f32 v33,v33,v89
v_mul_f32 v34,v34,v90
buffer_load_dwordx4 v1,s[36:39],s61 offen offset:2048 sc1 lds
v_mul_f32 v35,v35,v91
v_cvt_pk_bf16_f32 v98,v32,v33
v_cvt_pk_bf16_f32 v99,v34,v35
buffer_store_dwordx4 v[96:99],v3,s[36:39],s73 offen offset:0 sc1
v_fma_f32 v36,-v116,v24,v36
v_fma_f32 v37,-v116,v25,v37
v_fma_f32 v38,-v116,v26,v38
v_fma_f32 v39,-v116,v27,v39
v_mul_f32 v92,v36,v36
v_mul_f32 v93,v37,v37
buffer_load_dwordx4 v1,s[36:39],s61 offen offset:3072 sc1 lds
v_mul_f32 v94,v38,v38
v_mul_f32 v95,v39,v39
v_fma_f32 v92,v118,v92,v117
v_fma_f32 v93,v118,v93,v117
v_fma_f32 v94,v118,v94,v117
v_fma_f32 v95,v118,v95,v117
v_mul_f32 v92,v36,v92
v_mul_f32 v93,v37,v93
v_mul_f32 v94,v38,v94
v_mul_f32 v95,v39,v95
s_lshr_b32 s72,s61,5
buffer_load_dword v108,v8,s[36:39],s72 offen offset:0 sc1
v_exp_f32 v92,v92
v_exp_f32 v93,v93
v_exp_f32 v94,v94
v_exp_f32 v95,v95
v_fma_f32 v92,v92,v119,v119
v_fma_f32 v93,v93,v119,v119
v_fma_f32 v94,v94,v119,v119
v_fma_f32 v95,v95,v119,v119
v_rcp_f32 v92,v92
v_rcp_f32 v93,v93
v_rcp_f32 v94,v94
v_rcp_f32 v95,v95
v_mul_f32 v36,v36,v92
v_mul_f32 v37,v37,v93
v_mul_f32 v38,v38,v94
v_mul_f32 v39,v39,v95
v_cvt_pk_bf16_f32 v100,v36,v37
v_cvt_pk_bf16_f32 v101,v38,v39
buffer_store_dwordx2 v[100:101],v9,s[36:39],s73 offen offset:0 sc1
s_waitcnt vmcnt(7)
s_barrier
ds_read_b32 v13,v7 offset:8192
ds_read_b128 v[52:55],v2 offset:32768
ds_read_b128 v[56:59],v2 offset:33792
ds_read_b128 v[60:63],v2 offset:34816
ds_read_b128 v[64:67],v2 offset:35840
ds_read_b128 v[68:71],v2 offset:36864
s_mul_i32 s70,s47,5
s_lshr_b32 s70,s70,6
buffer_store_dword v5,v4,s[36:39],s70 offen offset:0 sc1
s_waitcnt lgkmcnt(5))"
R"(
v_cmp_eq_u32 vcc,s60,v13
s_nop 0
s_cmp_eq_u32 vcc_hi,-1
s_cselect_b32 s67,s50,s59
s_lshr_b32 s61,s67,0
s_xor_b32 s62,s63,65536
s_mov_b32 s47,s48
s_mov_b32 s48,s59
s_mul_i32 s70,s59,5
s_lshr_b32 s70,s70,6
buffer_store_dword v5,v4,s[36:39],s70 offen offset:0 sc1
s_waitcnt lgkmcnt(3)
s_setpc_b64 s[92:93]
Lg3_done1_%=:
s_waitcnt vmcnt(0)
s_barrier
s_mul_i32 s70,s47,5
s_lshr_b32 s70,s70,6
buffer_store_dword v5,v4,s[36:39],s70 offen offset:0 sc1
s_mul_i32 s70,s48,5
s_lshr_b32 s70,s70,6
buffer_store_dword v5,v4,s[36:39],s70 offen offset:0 sc1
s_branch Lg3_done_%=
Lg3_done_%=:
s_waitcnt vmcnt(0) lgkmcnt(0)
s_barrier
s_endpgm
Lcode_g3_e:)"
  :: "s"(ws), "s"(wave), "s"(sStep), "s"(sX1), "s"(sX2), "s"(sU0), "s"(sWw), "s"(sBuf), "s"(sEp), "v"(vSrc), "v"(vA), "v"(vOut), "v"(vFlag), "v"(vPollOff), "v"(vSnapRd), "v"(vAuxS), "v"(vOut2), "v"(vStW), "v"(vStR), "v"(vWd0), "v"(vWd1), "v"(vWr0), "v"(vWr1), "v"(vWr2), "s"(w3) : "v0", "v1", "v2", "v3", "v4", "v5", "v6", "v7", "v8", "v9", "v10", "v11", "v12", "v13", "v14", "v15", "v16", "v17", "v18", "v19", "v20", "v21", "v22", "v23", "v24", "v25", "v26", "v27", "v28", "v29", "v30", "v31", "v32", "v33", "v34", "v35", "v36", "v37", "v38", "v39", "v40", "v41", "v42", "v43", "v44", "v45", "v46", "v47", "v48", "v49", "v50", "v51", "v52", "v53", "v54", "v55", "v56", "v57", "v58", "v59", "v60", "v61", "v62", "v63", "v64", "v65", "v66", "v67", "v68", "v69", "v70", "v71", "v72", "v73", "v74", "v75", "v76", "v77", "v78", "v79", "v80", "v81", "v82", "v83", "v84", "v85", "v86", "v87", "v88", "v89", "v90", "v91", "v92", "v93", "v94", "v95", "v96", "v97", "v98", "v99", "v100", "v101", "v102", "v103", "v104", "v105", "v106", "v107", "v108", "v109", "v110", "v111", "v112", "v113", "v114", "v115", "v116", "v117", "v118", "v119", "v120", "v121", "v122", "v123", "v124", "v125", "v126", "v127", "v128", "v129", "v130", "v131", "v132", "v133", "v134", "v135", "v136", "v137", "v138", "v139", "v140", "v141", "v142", "v143", "v144", "v145", "v146", "v147", "v148", "v149", "v150", "v151", "v152", "v153", "v154", "v155", "v156", "v157", "v158", "v159", "v160", "v161", "v162", "v163", "v164", "v165", "v166", "v167", "v168", "v169", "v170", "v171", "v172", "v173", "v174", "v175", "v176", "v177", "v178", "v179", "v180", "v181", "v182", "v183", "v184", "v185", "v186", "v187", "v188", "v189", "v190", "v191", "v192", "v193", "v194", "v195", "v196", "v197", "v198", "v199", "v200", "v201", "v202", "v203", "v204", "v205", "v206", "v207", "v208", "v209", "v210", "v211", "v212", "v213", "v214", "v215", "v216", "v217", "v218", "v219", "v220", "v221", "v222", "v223", "v224", "v225", "v226", "v227", "v228", "v229", "v230", "v231", "v255", "a0", "a1", "a2", "a3", "a4", "a5", "a6", "a7", "a8", "a9", "a10", "a11", "a12", "a13", "a14", "a15", "a16", "a17", "a18", "a19", "a20", "a21", "a22", "a23", "a24", "a25", "a26", "a27", "a28", "a29", "a30", "a31", "a32", "a33", "a34", "a35", "a36", "a37", "a38", "a39", "a40", "a41", "a42", "a43", "a44", "a45", "a46", "a47", "a48", "a49", "a50", "a51", "a52", "a53", "a54", "a55", "a56", "a57", "a58", "a59", "a60", "a61", "a62", "a63", "a64", "a65", "a66", "a67", "a68", "a69", "a70", "a71", "a72", "a73", "a74", "a75", "a76", "a77", "a78", "a79", "a80", "a81", "a82", "a83", "a84", "a85", "a86", "a87", "a88", "a89", "a90", "a91", "a92", "a93", "a94", "a95", "a96", "a97", "a98", "a99", "a100", "a101", "a102", "a103", "a104", "a105", "a106", "a107", "a108", "a109", "a110", "a111", "a112", "a113", "a114", "a115", "a116", "a117", "a118", "a119", "a120", "a121", "a122", "a123", "a124", "a125", "a126", "a127", "a128", "a129", "a130", "a131", "a132", "a133", "a134", "a135", "a136", "a137", "a138", "a139", "a140", "a141", "a142", "a143", "a144", "a145", "a146", "a147", "a148", "a149", "a150", "a151", "a152", "a153", "a154", "a155", "a156", "a157", "a158", "a159", "a160", "a161", "a162", "a163", "a164", "a165", "a166", "a167", "a168", "a169", "a170", "a171", "a172", "a173", "a174", "a175", "a176", "a177", "a178", "a179", "a180", "a181", "a182", "a183", "a184", "a185", "a186", "a187", "a188", "a189", "a190", "a191", "a192", "a193", "a194", "a195", "a196", "a197", "a198", "a199", "a200", "a201", "a202", "a203", "a204", "a205", "a206", "a207", "a208", "a209", "a210", "a211", "a212", "a213", "a214", "a215", "a216", "a217", "a218", "a219", "a220", "a221", "a222", "a223", "a224", "a225", "a226", "a227", "a228", "a229", "a230", "a231", "a232", "a233", "a234", "a235", "a236", "a237", "a238", "a239", "a240", "a241", "a242", "a243", "a244", "a245", "a246", "a247", "a248", "a249", "a250", "a251", "a252", "a253", "a254", "a255", "s36", "s37", "s38", "s39", "s40", "s41", "s42", "s43", "s44", "s45", "s46", "s47", "s48", "s49", "s50", "s51", "s52", "s53", "s54", "s55", "s56", "s57", "s58", "s59", "s60", "s61", "s62", "s63", "s64", "s65", "s66", "s67", "s68", "s69", "s70", "s71", "s72", "s73", "s74", "s75", "s76", "s77", "s78", "s79", "s80", "s81", "s82", "s83", "s84", "s85", "s86", "s87", "s88", "s89", "s90", "s91", "s92", "s93", "s94", "s95", "s96", "s97", "s98", "s99", "vcc", "scc", "memory"); } else { asm volatile(R"(
Lcode_g3o_b:
s_mov_b64 s[36:37],%0
s_mov_b32 s38,122727552
s_mov_b32 s39,0x00020000
s_mov_b32 s54,%1
s_mov_b32 s55,%2
s_mov_b32 s56,%3
s_mov_b32 s58,%4
s_mov_b32 s83,%5
s_mov_b32 s82,%6
s_mov_b32 s84,%7
s_mov_b32 s60,%8
v_mov_b32 v0,%9
v_mov_b32 v2,%10
v_mov_b32 v3,%11
v_mov_b32 v4,%12
v_mov_b32 v6,%13
v_mov_b32 v7,%14
v_mov_b32 v8,%15
v_mov_b32 v9,%16
v_mov_b32 v10,%17
v_mov_b32 v11,%18
v_mov_b32 v24,%19
v_mov_b32 v25,%20
v_mov_b32 v26,%21
v_mov_b32 v27,%22
v_mov_b32 v28,%23
s_mov_b64 s[40:41],%24
s_mov_b32 s42,7340032
s_mov_b32 s43,0x00020000
s_lshl_b32 s88,s54,11
s_add_u32 m0,s88,0
s_add_u32 s61,s82,0
buffer_load_dwordx4 v24,s[40:43],s61 offen offset:0  lds
s_add_u32 m0,s88,1024
s_nop 0
buffer_load_dwordx4 v25,s[40:43],s61 offen offset:0  lds
s_add_u32 m0,s88,8064
s_nop 0
buffer_load_dwordx4 v24,s[40:43],s61 offen offset:128  lds
s_add_u32 m0,s88,9088
s_nop 0
buffer_load_dwordx4 v25,s[40:43],s61 offen offset:128  lds
s_add_u32 m0,s88,16384
s_add_u32 s61,s82,458752
buffer_load_dwordx4 v24,s[40:43],s61 offen offset:0  lds
s_add_u32 m0,s88,17408
s_nop 0
buffer_load_dwordx4 v25,s[40:43],s61 offen offset:0  lds
s_add_u32 m0,s88,24448
s_nop 0
buffer_load_dwordx4 v24,s[40:43],s61 offen offset:128  lds
s_add_u32 m0,s88,25472
s_nop 0
buffer_load_dwordx4 v25,s[40:43],s61 offen offset:128  lds
s_add_u32 m0,s88,32768
s_add_u32 s61,s82,917504
buffer_load_dwordx4 v24,s[40:43],s61 offen offset:0  lds
s_add_u32 m0,s88,33792
s_nop 0
buffer_load_dwordx4 v25,s[40:43],s61 offen offset:0  lds
s_add_u32 m0,s88,40832
s_nop 0
buffer_load_dwordx4 v24,s[40:43],s61 offen offset:128  lds
s_add_u32 m0,s88,41856
s_nop 0
buffer_load_dwordx4 v25,s[40:43],s61 offen offset:128  lds
s_add_u32 m0,s88,49152
s_add_u32 s61,s82,1376256
buffer_load_dwordx4 v24,s[40:43],s61 offen offset:0  lds
s_add_u32 m0,s88,50176
s_nop 0
buffer_load_dwordx4 v25,s[40:43],s61 offen offset:0  lds
s_add_u32 m0,s88,57216
s_nop 0
buffer_load_dwordx4 v24,s[40:43],s61 offen offset:128  lds
s_add_u32 m0,s88,58240
s_nop 0
buffer_load_dwordx4 v25,s[40:43],s61 offen offset:128  lds
s_add_u32 m0,s88,65536
s_add_u32 s61,s82,1835008
buffer_load_dwordx4 v24,s[40:43],s61 offen offset:0  lds
s_add_u32 m0,s88,66560
s_nop 0
buffer_load_dwordx4 v25,s[40:43],s61 offen offset:0  lds
s_add_u32 m0,s88,73600
s_nop 0
buffer_load_dwordx4 v24,s[40:43],s61 offen offset:128  lds
s_add_u32 m0,s88,74624
s_nop 0
buffer_load_dwordx4 v25,s[40:43],s61 offen offset:128  lds
s_add_u32 m0,s88,81920
s_add_u32 s61,s82,2293760
buffer_load_dwordx4 v24,s[40:43],s61 offen offset:0  lds
s_add_u32 m0,s88,82944
s_nop 0
buffer_load_dwordx4 v25,s[40:43],s61 offen offset:0  lds
s_add_u32 m0,s88,89984
s_nop 0
buffer_load_dwordx4 v24,s[40:43],s61 offen offset:128  lds
s_add_u32 m0,s88,91008
s_nop 0
buffer_load_dwordx4 v25,s[40:43],s61 offen offset:128  lds
s_add_u32 m0,s88,98304
s_add_u32 s61,s82,2752512
buffer_load_dwordx4 v24,s[40:43],s61 offen offset:0  lds
s_add_u32 m0,s88,99328
s_nop 0
buffer_load_dwordx4 v25,s[40:43],s61 offen offset:0  lds
s_add_u32 m0,s88,106368
s_nop 0
buffer_load_dwordx4 v24,s[40:43],s61 offen offset:128  lds
s_add_u32 m0,s88,107392
s_nop 0
buffer_load_dwordx4 v25,s[40:43],s61 offen offset:128  lds
v_add_u32 v32,49152,v26
v_add_u32 v33,49152,v27
v_add_u32 v34,98304,v26
v_add_u32 v35,98304,v27
v_mov_b32 v72,0x3f803f80
v_mov_b32 v73,0x3f803f80
v_mov_b32 v74,0x3f803f80
v_mov_b32 v75,0x3f803f80
s_waitcnt vmcnt(24)
s_barrier
s_add_u32 m0,s88,114688
s_add_u32 s61,s82,3211264
buffer_load_dwordx4 v24,s[40:43],s61 offen offset:0  lds
s_add_u32 m0,s88,115712
ds_read_b64_tr_b16 a[0:1],v26 offset:0
buffer_load_dwordx4 v25,s[40:43],s61 offen offset:0  lds
s_add_u32 m0,s88,122752
ds_read_b64_tr_b16 a[2:3],v26 offset:512
buffer_load_dwordx4 v24,s[40:43],s61 offen offset:128  lds
s_add_u32 m0,s88,123776
ds_read_b64_tr_b16 a[4:5],v27 offset:0
buffer_load_dwordx4 v25,s[40:43],s61 offen offset:128  lds
ds_read_b64_tr_b16 a[6:7],v27 offset:512
ds_read_b64_tr_b16 a[8:9],v26 offset:4096
ds_read_b64_tr_b16 a[10:11],v26 offset:4608
ds_read_b64_tr_b16 a[12:13],v27 offset:4096
ds_read_b64_tr_b16 a[14:15],v27 offset:4608
s_waitcnt vmcnt(24)
s_barrier
s_add_u32 m0,s88,131072
s_add_u32 s61,s82,3670016
buffer_load_dwordx4 v24,s[40:43],s61 offen offset:0  lds
s_add_u32 m0,s88,132096
ds_read_b64_tr_b16 a[16:17],v26 offset:16384
buffer_load_dwordx4 v25,s[40:43],s61 offen offset:0  lds
s_add_u32 m0,s88,139136
ds_read_b64_tr_b16 a[18:19],v26 offset:16896
buffer_load_dwordx4 v24,s[40:43],s61 offen offset:128  lds
s_add_u32 m0,s88,140160
ds_read_b64_tr_b16 a[20:21],v27 offset:16384
buffer_load_dwordx4 v25,s[40:43],s61 offen offset:128  lds
ds_read_b64_tr_b16 a[22:23],v27 offset:16896
ds_read_b64_tr_b16 a[24:25],v26 offset:20480
ds_read_b64_tr_b16 a[26:27],v26 offset:20992
ds_read_b64_tr_b16 a[28:29],v27 offset:20480
ds_read_b64_tr_b16 a[30:31],v27 offset:20992
s_waitcnt lgkmcnt(8)
v_mfma_f32_16x16x32_bf16 v[16:19],a[0:3],v[72:75],0
v_mfma_f32_16x16x32_bf16 v[20:23],a[4:7],v[72:75],0
v_mfma_f32_16x16x32_bf16 v[16:19],a[8:11],v[72:75],v[16:19]
v_mfma_f32_16x16x32_bf16 v[20:23],a[12:15],v[72:75],v[20:23]
s_waitcnt vmcnt(24)
s_barrier
s_add_u32 m0,s88,0
s_add_u32 s61,s82,4128768
buffer_load_dwordx4 v24,s[40:43],s61 offen offset:0  lds
s_add_u32 m0,s88,1024
ds_read_b64_tr_b16 a[32:33],v26 offset:32768
buffer_load_dwordx4 v25,s[40:43],s61 offen offset:0  lds
s_add_u32 m0,s88,8064
ds_read_b64_tr_b16 a[34:35],v26 offset:33280
buffer_load_dwordx4 v24,s[40:43],s61 offen offset:128  lds
s_add_u32 m0,s88,9088
ds_read_b64_tr_b16 a[36:37],v27 offset:32768
buffer_load_dwordx4 v25,s[40:43],s61 offen offset:128  lds
ds_read_b64_tr_b16 a[38:39],v27 offset:33280
ds_read_b64_tr_b16 a[40:41],v26 offset:36864
ds_read_b64_tr_b16 a[42:43],v26 offset:37376
ds_read_b64_tr_b16 a[44:45],v27 offset:36864
ds_read_b64_tr_b16 a[46:47],v27 offset:37376
s_waitcnt lgkmcnt(8)
v_mfma_f32_16x16x32_bf16 v[16:19],a[16:19],v[72:75],v[16:19]
v_mfma_f32_16x16x32_bf16 v[20:23],a[20:23],v[72:75],v[20:23]
v_mfma_f32_16x16x32_bf16 v[16:19],a[24:27],v[72:75],v[16:19]
v_mfma_f32_16x16x32_bf16 v[20:23],a[28:31],v[72:75],v[20:23]
s_waitcnt vmcnt(24)
s_barrier
s_add_u32 m0,s88,16384
s_add_u32 s61,s82,4587520
buffer_load_dwordx4 v24,s[40:43],s61 offen offset:0  lds
s_add_u32 m0,s88,17408
ds_read_b64_tr_b16 a[48:49],v32 offset:0
buffer_load_dwordx4 v25,s[40:43],s61 offen offset:0  lds
s_add_u32 m0,s88,24448
ds_read_b64_tr_b16 a[50:51],v32 offset:512)"
R"(
buffer_load_dwordx4 v24,s[40:43],s61 offen offset:128  lds
s_add_u32 m0,s88,25472
ds_read_b64_tr_b16 a[52:53],v33 offset:0
buffer_load_dwordx4 v25,s[40:43],s61 offen offset:128  lds
ds_read_b64_tr_b16 a[54:55],v33 offset:512
ds_read_b64_tr_b16 a[56:57],v32 offset:4096
ds_read_b64_tr_b16 a[58:59],v32 offset:4608
ds_read_b64_tr_b16 a[60:61],v33 offset:4096
ds_read_b64_tr_b16 a[62:63],v33 offset:4608
s_waitcnt lgkmcnt(8)
v_mfma_f32_16x16x32_bf16 v[16:19],a[32:35],v[72:75],v[16:19]
v_mfma_f32_16x16x32_bf16 v[20:23],a[36:39],v[72:75],v[20:23]
v_mfma_f32_16x16x32_bf16 v[16:19],a[40:43],v[72:75],v[16:19]
v_mfma_f32_16x16x32_bf16 v[20:23],a[44:47],v[72:75],v[20:23]
s_waitcnt vmcnt(24)
s_barrier
s_add_u32 m0,s88,32768
s_add_u32 s61,s82,5046272
buffer_load_dwordx4 v24,s[40:43],s61 offen offset:0  lds
s_add_u32 m0,s88,33792
ds_read_b64_tr_b16 a[64:65],v32 offset:16384
buffer_load_dwordx4 v25,s[40:43],s61 offen offset:0  lds
s_add_u32 m0,s88,40832
ds_read_b64_tr_b16 a[66:67],v32 offset:16896
buffer_load_dwordx4 v24,s[40:43],s61 offen offset:128  lds
s_add_u32 m0,s88,41856
ds_read_b64_tr_b16 a[68:69],v33 offset:16384
buffer_load_dwordx4 v25,s[40:43],s61 offen offset:128  lds
ds_read_b64_tr_b16 a[70:71],v33 offset:16896
ds_read_b64_tr_b16 a[72:73],v32 offset:20480
ds_read_b64_tr_b16 a[74:75],v32 offset:20992
ds_read_b64_tr_b16 a[76:77],v33 offset:20480
ds_read_b64_tr_b16 a[78:79],v33 offset:20992
s_waitcnt lgkmcnt(8)
v_mfma_f32_16x16x32_bf16 v[16:19],a[48:51],v[72:75],v[16:19]
v_mfma_f32_16x16x32_bf16 v[20:23],a[52:55],v[72:75],v[20:23]
v_mfma_f32_16x16x32_bf16 v[16:19],a[56:59],v[72:75],v[16:19]
v_mfma_f32_16x16x32_bf16 v[20:23],a[60:63],v[72:75],v[20:23]
s_waitcnt vmcnt(24)
s_barrier
s_add_u32 m0,s88,49152
s_add_u32 s61,s82,5505024
buffer_load_dwordx4 v24,s[40:43],s61 offen offset:0  lds
s_add_u32 m0,s88,50176
ds_read_b64_tr_b16 a[80:81],v32 offset:32768
buffer_load_dwordx4 v25,s[40:43],s61 offen offset:0  lds
s_add_u32 m0,s88,57216
ds_read_b64_tr_b16 a[82:83],v32 offset:33280
buffer_load_dwordx4 v24,s[40:43],s61 offen offset:128  lds
s_add_u32 m0,s88,58240
ds_read_b64_tr_b16 a[84:85],v33 offset:32768
buffer_load_dwordx4 v25,s[40:43],s61 offen offset:128  lds
ds_read_b64_tr_b16 a[86:87],v33 offset:33280
ds_read_b64_tr_b16 a[88:89],v32 offset:36864
ds_read_b64_tr_b16 a[90:91],v32 offset:37376
ds_read_b64_tr_b16 a[92:93],v33 offset:36864
ds_read_b64_tr_b16 a[94:95],v33 offset:37376
s_waitcnt lgkmcnt(8)
v_mfma_f32_16x16x32_bf16 v[16:19],a[64:67],v[72:75],v[16:19]
v_mfma_f32_16x16x32_bf16 v[20:23],a[68:71],v[72:75],v[20:23]
v_mfma_f32_16x16x32_bf16 v[16:19],a[72:75],v[72:75],v[16:19]
v_mfma_f32_16x16x32_bf16 v[20:23],a[76:79],v[72:75],v[20:23]
s_waitcnt vmcnt(24)
s_barrier
s_add_u32 m0,s88,65536
s_add_u32 s61,s82,5963776
buffer_load_dwordx4 v24,s[40:43],s61 offen offset:0  lds
s_add_u32 m0,s88,66560
ds_read_b64_tr_b16 a[96:97],v34 offset:0
buffer_load_dwordx4 v25,s[40:43],s61 offen offset:0  lds
s_add_u32 m0,s88,73600
ds_read_b64_tr_b16 a[98:99],v34 offset:512
buffer_load_dwordx4 v24,s[40:43],s61 offen offset:128  lds
s_add_u32 m0,s88,74624
ds_read_b64_tr_b16 a[100:101],v35 offset:0
buffer_load_dwordx4 v25,s[40:43],s61 offen offset:128  lds
ds_read_b64_tr_b16 a[102:103],v35 offset:512
ds_read_b64_tr_b16 a[104:105],v34 offset:4096
ds_read_b64_tr_b16 a[106:107],v34 offset:4608
ds_read_b64_tr_b16 a[108:109],v35 offset:4096
ds_read_b64_tr_b16 a[110:111],v35 offset:4608
s_waitcnt lgkmcnt(8)
v_mfma_f32_16x16x32_bf16 v[16:19],a[80:83],v[72:75],v[16:19]
v_mfma_f32_16x16x32_bf16 v[20:23],a[84:87],v[72:75],v[20:23]
v_mfma_f32_16x16x32_bf16 v[16:19],a[88:91],v[72:75],v[16:19]
v_mfma_f32_16x16x32_bf16 v[20:23],a[92:95],v[72:75],v[20:23]
s_waitcnt vmcnt(24)
s_barrier
s_add_u32 m0,s88,81920
s_add_u32 s61,s82,6422528
buffer_load_dwordx4 v24,s[40:43],s61 offen offset:0  lds
s_add_u32 m0,s88,82944
ds_read_b64_tr_b16 a[112:113],v34 offset:16384
buffer_load_dwordx4 v25,s[40:43],s61 offen offset:0  lds
s_add_u32 m0,s88,89984
ds_read_b64_tr_b16 a[114:115],v34 offset:16896
buffer_load_dwordx4 v24,s[40:43],s61 offen offset:128  lds
s_add_u32 m0,s88,91008
ds_read_b64_tr_b16 a[116:117],v35 offset:16384
buffer_load_dwordx4 v25,s[40:43],s61 offen offset:128  lds
ds_read_b64_tr_b16 a[118:119],v35 offset:16896
ds_read_b64_tr_b16 a[120:121],v34 offset:20480
ds_read_b64_tr_b16 a[122:123],v34 offset:20992
ds_read_b64_tr_b16 a[124:125],v35 offset:20480
ds_read_b64_tr_b16 a[126:127],v35 offset:20992
s_waitcnt lgkmcnt(8)
v_mfma_f32_16x16x32_bf16 v[16:19],a[96:99],v[72:75],v[16:19]
v_mfma_f32_16x16x32_bf16 v[20:23],a[100:103],v[72:75],v[20:23]
v_mfma_f32_16x16x32_bf16 v[16:19],a[104:107],v[72:75],v[16:19]
v_mfma_f32_16x16x32_bf16 v[20:23],a[108:111],v[72:75],v[20:23]
s_waitcnt vmcnt(24)
s_barrier
s_add_u32 m0,s88,98304
s_add_u32 s61,s82,6881280
buffer_load_dwordx4 v24,s[40:43],s61 offen offset:0  lds
s_add_u32 m0,s88,99328
ds_read_b64_tr_b16 a[128:129],v34 offset:32768
buffer_load_dwordx4 v25,s[40:43],s61 offen offset:0  lds
s_add_u32 m0,s88,106368
ds_read_b64_tr_b16 a[130:131],v34 offset:33280
buffer_load_dwordx4 v24,s[40:43],s61 offen offset:128  lds
s_add_u32 m0,s88,107392
ds_read_b64_tr_b16 a[132:133],v35 offset:32768
buffer_load_dwordx4 v25,s[40:43],s61 offen offset:128  lds
ds_read_b64_tr_b16 a[134:135],v35 offset:33280
ds_read_b64_tr_b16 a[136:137],v34 offset:36864
ds_read_b64_tr_b16 a[138:139],v34 offset:37376
ds_read_b64_tr_b16 a[140:141],v35 offset:36864
ds_read_b64_tr_b16 a[142:143],v35 offset:37376
s_waitcnt lgkmcnt(8)
v_mfma_f32_16x16x32_bf16 v[16:19],a[112:115],v[72:75],v[16:19]
v_mfma_f32_16x16x32_bf16 v[20:23],a[116:119],v[72:75],v[20:23]
v_mfma_f32_16x16x32_bf16 v[16:19],a[120:123],v[72:75],v[16:19]
v_mfma_f32_16x16x32_bf16 v[20:23],a[124:127],v[72:75],v[20:23]
s_waitcnt vmcnt(24)
s_barrier
ds_read_b64_tr_b16 a[144:145],v26 offset:0
ds_read_b64_tr_b16 a[146:147],v26 offset:512
ds_read_b64_tr_b16 a[148:149],v27 offset:0
ds_read_b64_tr_b16 a[150:151],v27 offset:512
ds_read_b64_tr_b16 a[152:153],v26 offset:4096
ds_read_b64_tr_b16 a[154:155],v26 offset:4608
ds_read_b64_tr_b16 a[156:157],v27 offset:4096
ds_read_b64_tr_b16 a[158:159],v27 offset:4608
s_waitcnt lgkmcnt(8)
v_mfma_f32_16x16x32_bf16 v[16:19],a[128:131],v[72:75],v[16:19]
v_mfma_f32_16x16x32_bf16 v[20:23],a[132:135],v[72:75],v[20:23]
v_mfma_f32_16x16x32_bf16 v[16:19],a[136:139],v[72:75],v[16:19]
v_mfma_f32_16x16x32_bf16 v[20:23],a[140:143],v[72:75],v[20:23]
s_waitcnt vmcnt(20)
s_barrier
ds_read_b64_tr_b16 a[160:161],v26 offset:16384
ds_read_b64_tr_b16 a[162:163],v26 offset:16896
ds_read_b64_tr_b16 a[164:165],v27 offset:16384
ds_read_b64_tr_b16 a[166:167],v27 offset:16896
ds_read_b64_tr_b16 a[168:169],v26 offset:20480
ds_read_b64_tr_b16 a[170:171],v26 offset:20992
ds_read_b64_tr_b16 a[172:173],v27 offset:20480
ds_read_b64_tr_b16 a[174:175],v27 offset:20992
s_waitcnt lgkmcnt(8)
v_mfma_f32_16x16x32_bf16 v[16:19],a[144:147],v[72:75],v[16:19]
v_mfma_f32_16x16x32_bf16 v[20:23],a[148:151],v[72:75],v[20:23]
v_mfma_f32_16x16x32_bf16 v[16:19],a[152:155],v[72:75],v[16:19]
v_mfma_f32_16x16x32_bf16 v[20:23],a[156:159],v[72:75],v[20:23]
s_waitcnt vmcnt(16)
s_barrier
ds_read_b64_tr_b16 a[176:177],v26 offset:32768
ds_read_b64_tr_b16 a[178:179],v26 offset:33280
ds_read_b64_tr_b16 a[180:181],v27 offset:32768
ds_read_b64_tr_b16 a[182:183],v27 offset:33280
ds_read_b64_tr_b16 a[184:185],v26 offset:36864
ds_read_b64_tr_b16 a[186:187],v26 offset:37376
ds_read_b64_tr_b16 a[188:189],v27 offset:36864
ds_read_b64_tr_b16 a[190:191],v27 offset:37376
s_getpc_b64 s[46:47]
Lg3o_cp_%=:
s_add_u32 s46,s46,Lg3o_init_%=-Lg3o_cp_%=
s_addc_u32 s47,s47,0
s_lshl_b32 s68,s54,13
v_lshl_add_u32 v56,v2,3,s68
v_min_u32 v57,Lg3o_done_%=-Lg3o_init_%=,v56
global_load_dword v60,v57,s[46:47]
s_waitcnt lgkmcnt(8)
v_mfma_f32_16x16x32_bf16 v[16:19],a[160:163],v[72:75],v[16:19]
v_mfma_f32_16x16x32_bf16 v[20:23],a[164:167],v[72:75],v[20:23]
v_mfma_f32_16x16x32_bf16 v[16:19],a[168:171],v[72:75],v[16:19]
v_mfma_f32_16x16x32_bf16 v[20:23],a[172:175],v[72:75],v[20:23]
s_waitcnt vmcnt(13)
s_barrier
ds_read_b64_tr_b16 a[192:193],v32 offset:0
ds_read_b64_tr_b16 a[194:195],v32 offset:512
ds_read_b64_tr_b16 a[196:197],v33 offset:0
ds_read_b64_tr_b16 a[198:199],v33 offset:512
ds_read_b64_tr_b16 a[200:201],v32 offset:4096
ds_read_b64_tr_b16 a[202:203],v32 offset:4608
ds_read_b64_tr_b16 a[204:205],v33 offset:4096
ds_read_b64_tr_b16 a[206:207],v33 offset:4608
s_waitcnt lgkmcnt(8)
v_mfma_f32_16x16x32_bf16 v[16:19],a[176:179],v[72:75],v[16:19]
v_mfma_f32_16x16x32_bf16 v[20:23],a[180:183],v[72:75],v[20:23])"
R"(
v_mfma_f32_16x16x32_bf16 v[16:19],a[184:187],v[72:75],v[16:19]
v_mfma_f32_16x16x32_bf16 v[20:23],a[188:191],v[72:75],v[20:23]
s_waitcnt vmcnt(9)
s_barrier
ds_read_b64_tr_b16 a[208:209],v32 offset:16384
ds_read_b64_tr_b16 a[210:211],v32 offset:16896
ds_read_b64_tr_b16 a[212:213],v33 offset:16384
ds_read_b64_tr_b16 a[214:215],v33 offset:16896
ds_read_b64_tr_b16 a[216:217],v32 offset:20480
ds_read_b64_tr_b16 a[218:219],v32 offset:20992
ds_read_b64_tr_b16 a[220:221],v33 offset:20480
ds_read_b64_tr_b16 a[222:223],v33 offset:20992
s_waitcnt lgkmcnt(8)
v_mfma_f32_16x16x32_bf16 v[16:19],a[192:195],v[72:75],v[16:19]
v_mfma_f32_16x16x32_bf16 v[20:23],a[196:199],v[72:75],v[20:23]
v_mfma_f32_16x16x32_bf16 v[16:19],a[200:203],v[72:75],v[16:19]
v_mfma_f32_16x16x32_bf16 v[20:23],a[204:207],v[72:75],v[20:23]
s_waitcnt vmcnt(5)
s_barrier
ds_read_b64_tr_b16 a[224:225],v32 offset:32768
ds_read_b64_tr_b16 a[226:227],v32 offset:33280
ds_read_b64_tr_b16 a[228:229],v33 offset:32768
ds_read_b64_tr_b16 a[230:231],v33 offset:33280
ds_read_b64_tr_b16 a[232:233],v32 offset:36864
ds_read_b64_tr_b16 a[234:235],v32 offset:37376
ds_read_b64_tr_b16 a[236:237],v33 offset:36864
ds_read_b64_tr_b16 a[238:239],v33 offset:37376
s_waitcnt lgkmcnt(8)
v_mfma_f32_16x16x32_bf16 v[16:19],a[208:211],v[72:75],v[16:19]
v_mfma_f32_16x16x32_bf16 v[20:23],a[212:215],v[72:75],v[20:23]
v_mfma_f32_16x16x32_bf16 v[16:19],a[216:219],v[72:75],v[16:19]
v_mfma_f32_16x16x32_bf16 v[20:23],a[220:223],v[72:75],v[20:23]
s_waitcnt vmcnt(1)
s_barrier
ds_read_b64_tr_b16 a[240:241],v34 offset:0
ds_read_b64_tr_b16 a[242:243],v34 offset:512
ds_read_b64_tr_b16 a[244:245],v35 offset:0
ds_read_b64_tr_b16 a[246:247],v35 offset:512
ds_read_b64_tr_b16 a[248:249],v34 offset:4096
ds_read_b64_tr_b16 a[250:251],v34 offset:4608
ds_read_b64_tr_b16 a[252:253],v35 offset:4096
ds_read_b64_tr_b16 a[254:255],v35 offset:4608
s_waitcnt lgkmcnt(8)
v_mfma_f32_16x16x32_bf16 v[16:19],a[224:227],v[72:75],v[16:19]
v_mfma_f32_16x16x32_bf16 v[20:23],a[228:231],v[72:75],v[20:23]
v_mfma_f32_16x16x32_bf16 v[16:19],a[232:235],v[72:75],v[16:19]
v_mfma_f32_16x16x32_bf16 v[20:23],a[236:239],v[72:75],v[20:23]
s_waitcnt lgkmcnt(0)
v_mfma_f32_16x16x32_bf16 v[16:19],a[240:243],v[72:75],v[16:19]
v_mfma_f32_16x16x32_bf16 v[20:23],a[244:247],v[72:75],v[20:23]
v_mfma_f32_16x16x32_bf16 v[16:19],a[248:251],v[72:75],v[16:19]
v_mfma_f32_16x16x32_bf16 v[20:23],a[252:255],v[72:75],v[20:23]
s_waitcnt lgkmcnt(0)
s_barrier
Lg3o_init_%=:
v_mov_b32 v5,s60
s_mov_b32 s59,536870912
s_add_u32 s57,s56,131072
s_mov_b32 s79,0
s_mov_b32 s75,0x3a800000
s_mov_b32 s76,0xc0135761
s_mov_b32 s77,0x3d372713
s_mov_b32 s44,s59
s_mov_b32 s45,s59
s_mov_b32 s46,s59
s_mov_b32 s47,s59
s_mov_b32 s48,s83
s_add_u32 s49,s48,s55
s_cmp_eq_u32 s49,s56
s_cselect_b32 s49,s57,s49
s_cmp_eq_u32 s49,s58
s_cselect_b32 s49,s59,s49
s_add_u32 s50,s49,s55
s_cmp_eq_u32 s50,s56
s_cselect_b32 s50,s57,s50
s_cmp_eq_u32 s50,s58
s_cselect_b32 s50,s59,s50
s_add_u32 s51,s50,s55
s_cmp_eq_u32 s51,s56
s_cselect_b32 s51,s57,s51
s_cmp_eq_u32 s51,s58
s_cselect_b32 s51,s59,s51
s_add_u32 s52,s51,s55
s_cmp_eq_u32 s52,s56
s_cselect_b32 s52,s57,s52
s_cmp_eq_u32 s52,s58
s_cselect_b32 s52,s59,s52
s_lshl_b32 s63,s54,12
s_add_u32 s63,s63,65536
s_add_u32 s64,s63,32768
s_lshl_b32 s88,s54,10
s_add_u32 s88,s88,131072
s_lshl_b32 s78,s54,11
s_add_u32 s78,s78,132608
v_add_u32 v1,16384,v0
v_mov_b32 v12,0x3727c5ac
s_getpc_b64 s[90:91]
Lg3o_pc_%=:
s_mov_b32 s68,Lg3o_d1w0_%=-Lg3o_pc_%=
s_cmp_eq_u32 s54,1
s_cselect_b32 s68,Lg3o_d1w1_%=-Lg3o_pc_%=,s68
s_cmp_eq_u32 s54,2
s_cselect_b32 s68,Lg3o_d1w2_%=-Lg3o_pc_%=,s68
s_cmp_eq_u32 s54,3
s_cselect_b32 s68,Lg3o_d1w3_%=-Lg3o_pc_%=,s68
s_add_u32 s92,s90,s68
s_addc_u32 s93,s91,0
s_mov_b32 s68,Lg3o_d0w0_%=-Lg3o_pc_%=
s_cmp_eq_u32 s54,1
s_cselect_b32 s68,Lg3o_d0w1_%=-Lg3o_pc_%=,s68
s_cmp_eq_u32 s54,2
s_cselect_b32 s68,Lg3o_d0w2_%=-Lg3o_pc_%=,s68
s_cmp_eq_u32 s54,3
s_cselect_b32 s68,Lg3o_d0w3_%=-Lg3o_pc_%=,s68
s_add_u32 s90,s90,s68
s_addc_u32 s91,s91,0
s_mov_b32 s66,s59
s_mov_b32 s65,0
s_mov_b32 s62,s63
s_branch Lg3o_slow0_%=
Lg3o_d0w0_%=:
v_mfma_f32_16x16x32_bf16 v[24:27],a[0:3],v[40:43],0
s_add_u32 m0,s62,16384
ds_read_b128 v[68:71],v2 offset:7168
v_mul_f32 v99,v93,v92
v_mfma_f32_16x16x32_bf16 v[28:31],a[4:7],v[40:43],0
buffer_load_dwordx4 v1,s[36:39],s66 offen offset:0 sc1 lds
v_mfma_f32_16x16x32_bf16 v[24:27],a[8:11],v[44:47],v[24:27]
s_mul_i32 s73,s47,7
ds_read_b128 v[40:43],v2 offset:8192
v_mul_f32 v98,s77,v98
v_mfma_f32_16x16x32_bf16 v[28:31],a[12:15],v[44:47],v[28:31]
s_lshr_b32 s73,s73,1
v_mul_f32 v98,v98,v97
v_fma_f32 v32,-v96,v16,v32
v_mfma_f32_16x16x32_bf16 v[24:27],a[16:19],v[48:51],v[24:27]
s_lshr_b32 s72,s66,5
ds_read_b128 v[44:47],v2 offset:9216
v_fma_f32 v33,-v96,v17,v33
v_mfma_f32_16x16x32_bf16 v[28:31],a[20:23],v[48:51],v[28:31]
buffer_load_dwordx4 v1,s[36:39],s66 offen offset:1024 sc1 lds
v_mfma_f32_16x16x32_bf16 v[24:27],a[24:27],v[52:55],v[24:27]
s_lshr_b32 s71,s62,2
ds_read_b128 v[48:51],v2 offset:10240
v_fma_f32 v34,-v96,v18,v34
v_mfma_f32_16x16x32_bf16 v[28:31],a[28:31],v[52:55],v[28:31]
s_waitcnt lgkmcnt(3)
v_fma_f32 v35,-v96,v19,v35
v_mul_f32 v72,v32,v32
v_mfma_f32_16x16x32_bf16 v[24:27],a[32:35],v[56:59],v[24:27]
ds_read_b128 v[52:55],v2 offset:11264
v_mul_f32 v73,v33,v33
v_mfma_f32_16x16x32_bf16 v[28:31],a[36:39],v[56:59],v[28:31]
buffer_load_dwordx4 v1,s[36:39],s66 offen offset:2048 sc1 lds
v_mfma_f32_16x16x32_bf16 v[24:27],a[40:43],v[60:63],v[24:27]
ds_read_b128 v[56:59],v2 offset:12288
v_mul_f32 v74,v34,v34
v_mfma_f32_16x16x32_bf16 v[28:31],a[44:47],v[60:63],v[28:31]
v_mul_f32 v75,v35,v35
v_fma_f32 v72,v98,v72,v97
v_mfma_f32_16x16x32_bf16 v[24:27],a[48:51],v[64:67],v[24:27]
ds_read_b128 v[60:63],v2 offset:13312
v_mul_f32 v72,v32,v72
v_mfma_f32_16x16x32_bf16 v[28:31],a[52:55],v[64:67],v[28:31]
buffer_load_dwordx4 v1,s[36:39],s66 offen offset:3072 sc1 lds
v_mfma_f32_16x16x32_bf16 v[24:27],a[56:59],v[68:71],v[24:27]
s_add_u32 m0,s71,s88
ds_read_b128 v[64:67],v2 offset:14336
v_fma_f32 v73,v98,v73,v97
v_mfma_f32_16x16x32_bf16 v[28:31],a[60:63],v[68:71],v[28:31]
s_waitcnt lgkmcnt(3)
buffer_load_dword v9,s[36:39],s72 offen offset:0 sc1 lds
v_mfma_f32_16x16x32_bf16 v[24:27],a[64:67],v[40:43],v[24:27]
s_waitcnt vmcnt(11)
ds_read_b128 v[68:71],v2 offset:15360
v_mul_f32 v73,v33,v73
v_mfma_f32_16x16x32_bf16 v[28:31],a[68:71],v[40:43],v[28:31]
s_barrier
v_mfma_f32_16x16x32_bf16 v[24:27],a[72:75],v[44:47],v[24:27]
s_xor_b32 s87,s64,65536
ds_read_b128 v[40:43],v2 offset:16384
ds_read_b32 v88,v8 offset:0
v_mfma_f32_16x16x32_bf16 v[28:31],a[76:79],v[44:47],v[28:31]
s_cmp_ge_u32 s66,s59
v_exp_f32 v72,v72
v_mfma_f32_16x16x32_bf16 v[24:27],a[80:83],v[48:51],v[24:27]
s_cselect_b32 s86,s49,s50
ds_read_b128 v[44:47],v2 offset:17408
v_fma_f32 v72,v72,v99,v99
v_mfma_f32_16x16x32_bf16 v[28:31],a[84:87],v[48:51],v[28:31]
s_cselect_b32 s87,s87,s63
v_exp_f32 v73,v73
v_mfma_f32_16x16x32_bf16 v[24:27],a[88:91],v[52:55],v[24:27]
s_lshr_b32 s68,s50,5
ds_read_b128 v[48:51],v2 offset:18432
v_fma_f32 v73,v73,v99,v99
v_mfma_f32_16x16x32_bf16 v[28:31],a[92:95],v[52:55],v[28:31]
s_waitcnt lgkmcnt(4)
v_rcp_f32 v72,v72
v_mfma_f32_16x16x32_bf16 v[24:27],a[96:99],v[56:59],v[24:27])"
R"(
s_waitcnt vmcnt(10) lgkmcnt(2)
ds_read_b128 v[52:55],v2 offset:19456
v_fma_f32 v74,v98,v74,v97
v_mfma_f32_16x16x32_bf16 v[28:31],a[100:103],v[56:59],v[28:31]
s_barrier
v_mfma_f32_16x16x32_bf16 v[24:27],a[104:107],v[60:63],v[24:27]
s_lshr_b32 s70,s51,5
ds_read_b128 v[56:59],v2 offset:20480
ds_read_b32 v13,v7 offset:0
v_mfma_f32_16x16x32_bf16 v[28:31],a[108:111],v[60:63],v[28:31]
s_mov_b32 s62,s87
v_rcp_f32 v73,v73
v_mfma_f32_16x16x32_bf16 v[24:27],a[112:115],v[64:67],v[24:27]
ds_read_b128 v[60:63],v2 offset:21504
v_mul_f32 v74,v34,v74
v_mfma_f32_16x16x32_bf16 v[28:31],a[116:119],v[64:67],v[28:31]
v_exp_f32 v74,v74
v_mfma_f32_16x16x32_bf16 v[24:27],a[120:123],v[68:71],v[24:27]
s_waitcnt lgkmcnt(1)
ds_read_b128 v[64:67],v2 offset:22528
v_fma_f32 v74,v74,v99,v99
v_mfma_f32_16x16x32_bf16 v[28:31],a[124:127],v[68:71],v[28:31]
v_cmp_eq_u32 vcc,s60,v13
v_rcp_f32 v74,v74
v_add_f32_dpp v88,v88,v88 quad_perm:[1,0,3,2] row_mask:0xf bank_mask:0xf
v_mfma_f32_16x16x32_bf16 v[24:27],a[128:131],v[40:43],v[24:27]
s_cmp_eq_u32 vcc_lo,-1
ds_read_b128 v[68:71],v2 offset:23552
v_add_f32_dpp v88,v88,v88 quad_perm:[2,3,0,1] row_mask:0xf bank_mask:0xf
v_mfma_f32_16x16x32_bf16 v[28:31],a[132:135],v[40:43],v[28:31]
s_cselect_b32 s67,s86,s59
s_cmp_ge_u32 s67,s59
s_cselect_b32 s68,s68,s70
s_add_u32 m0,s78,256
v_mfma_f32_16x16x32_bf16 v[24:27],a[136:139],v[44:47],v[24:27]
buffer_load_dword v6,s[36:39],s68 offen offset:0 sc1 lds
v_mfma_f32_16x16x32_bf16 v[28:31],a[140:143],v[44:47],v[28:31]
s_mov_b32 m0,s62
ds_read_b128 v[40:43],v2 offset:24576
v_add_f32_dpp v88,v88,v88 row_half_mirror row_mask:0xf bank_mask:0xf
v_mfma_f32_16x16x32_bf16 v[24:27],a[144:147],v[48:51],v[24:27]
buffer_load_dwordx4 v0,s[36:39],s67 offen offset:0 sc1 lds
v_mfma_f32_16x16x32_bf16 v[28:31],a[148:151],v[48:51],v[28:31]
s_xor_b32 s63,s63,65536
ds_read_b128 v[44:47],v2 offset:25600
v_fma_f32 v75,v98,v75,v97
v_mfma_f32_16x16x32_bf16 v[24:27],a[152:155],v[52:55],v[24:27]
s_add_u32 s53,s52,s55
ds_write_b32 v10,v88 offset:0
v_mfma_f32_16x16x32_bf16 v[28:31],a[156:159],v[52:55],v[28:31]
s_waitcnt lgkmcnt(3)
ds_read_b128 v[48:51],v2 offset:26624
v_mul_f32 v75,v35,v75
v_mfma_f32_16x16x32_bf16 v[24:27],a[160:163],v[56:59],v[24:27]
buffer_load_dwordx4 v0,s[36:39],s67 offen offset:1024 sc1 lds
v_mfma_f32_16x16x32_bf16 v[28:31],a[164:167],v[56:59],v[28:31]
s_cmp_eq_u32 s53,s56
ds_read_b128 v[52:55],v2 offset:27648
v_mul_f32 v32,v32,v72
v_mfma_f32_16x16x32_bf16 v[24:27],a[168:171],v[60:63],v[24:27]
s_cselect_b32 s53,s57,s53
ds_read_b128 v[56:59],v2 offset:28672
v_mul_f32 v33,v33,v73
v_mfma_f32_16x16x32_bf16 v[28:31],a[172:175],v[60:63],v[28:31]
s_cmp_eq_u32 s53,s58
v_exp_f32 v75,v75
v_mfma_f32_16x16x32_bf16 v[24:27],a[176:179],v[64:67],v[24:27]
buffer_load_dwordx4 v0,s[36:39],s67 offen offset:2048 sc1 lds
v_mfma_f32_16x16x32_bf16 v[28:31],a[180:183],v[64:67],v[28:31]
s_cselect_b32 s53,s59,s53
ds_read_b128 v[60:63],v2 offset:29696
v_fma_f32 v75,v75,v99,v99
v_mfma_f32_16x16x32_bf16 v[24:27],a[184:187],v[68:71],v[24:27]
s_cmp_ge_u32 s66,s59
ds_read_b128 v[64:67],v2 offset:30720
v_mul_f32 v34,v34,v74
v_mfma_f32_16x16x32_bf16 v[28:31],a[188:191],v[68:71],v[28:31]
s_waitcnt lgkmcnt(3)
v_rcp_f32 v75,v75
v_mfma_f32_16x16x32_bf16 v[24:27],a[192:195],v[40:43],v[24:27]
s_waitcnt vmcnt(9)
ds_read_b128 v[68:71],v2 offset:31744
v_mul_f32 v35,v35,v75
v_mfma_f32_16x16x32_bf16 v[28:31],a[196:199],v[40:43],v[28:31]
s_barrier
v_fma_f32 v36,-v96,v20,v36
v_fma_f32 v37,-v96,v21,v37
v_fma_f32 v38,-v96,v22,v38
v_fma_f32 v39,-v96,v23,v39
v_mul_f32 v76,v36,v36
v_mul_f32 v77,v37,v37
v_mul_f32 v78,v38,v38
v_mul_f32 v79,v39,v39
v_fma_f32 v76,v98,v76,v97
v_fma_f32 v77,v98,v77,v97
v_fma_f32 v78,v98,v78,v97
v_fma_f32 v79,v98,v79,v97
v_mul_f32 v76,v36,v76
v_mul_f32 v77,v37,v77
v_mul_f32 v78,v38,v78
v_mul_f32 v79,v39,v79
v_exp_f32 v76,v76
v_exp_f32 v77,v77
v_exp_f32 v78,v78
v_exp_f32 v79,v79
v_mfma_f32_16x16x32_bf16 v[24:27],a[200:203],v[44:47],v[24:27]
buffer_load_dwordx4 v0,s[36:39],s67 offen offset:3072 sc1 lds
v_fma_f32 v76,v76,v99,v99
v_fma_f32 v77,v77,v99,v99
v_fma_f32 v78,v78,v99,v99
v_fma_f32 v79,v79,v99,v99
v_rcp_f32 v76,v76
v_rcp_f32 v77,v77
v_rcp_f32 v78,v78
v_rcp_f32 v79,v79
v_mfma_f32_16x16x32_bf16 v[28:31],a[204:207],v[44:47],v[28:31]
ds_read_b128 v[40:43],v2 offset:32768
v_cvt_pk_bf16_f32 v80,v32,v33
v_cvt_pk_bf16_f32 v81,v34,v35
v_mul_f32 v36,v36,v76
v_mul_f32 v37,v37,v77
v_mul_f32 v38,v38,v78
v_mul_f32 v39,v39,v79
v_cvt_pk_bf16_f32 v82,v36,v37
v_cvt_pk_bf16_f32 v83,v38,v39
v_mfma_f32_16x16x32_bf16 v[24:27],a[208:211],v[48:51],v[24:27]
buffer_store_dwordx4 v[80:83],v3,s[36:39],s73 offen offset:0 sc1
ds_read_b128 v[44:47],v2 offset:33792
ds_read_b64 v[92:93],v11 offset:0
v_mfma_f32_16x16x32_bf16 v[28:31],a[212:215],v[48:51],v[28:31]
v_mfma_f32_16x16x32_bf16 v[24:27],a[216:219],v[52:55],v[24:27]
ds_read_b128 v[48:51],v2 offset:34816
v_mfma_f32_16x16x32_bf16 v[28:31],a[220:223],v[52:55],v[28:31]
s_waitcnt lgkmcnt(4)
v_mfma_f32_16x16x32_bf16 v[24:27],a[224:227],v[56:59],v[24:27]
s_waitcnt lgkmcnt(1)
ds_read_b128 v[52:55],v2 offset:35840
v_mul_f32 v96,s75,v92
v_mfma_f32_16x16x32_bf16 v[28:31],a[228:231],v[56:59],v[28:31]
v_fma_f32 v93,v93,s75,v12
v_fma_f32 v93,-v96,v96,v93
v_mfma_f32_16x16x32_bf16 v[24:27],a[232:235],v[60:63],v[24:27]
ds_read_b128 v[56:59],v2 offset:36864
v_max_f32 v93,v93,v12
v_mfma_f32_16x16x32_bf16 v[28:31],a[236:239],v[60:63],v[28:31]
v_rsq_f32 v92,v93
v_mfma_f32_16x16x32_bf16 v[24:27],a[240:243],v[64:67],v[24:27]
ds_read_b128 v[60:63],v2 offset:37888
v_mul_f32 v97,s76,v92
v_mfma_f32_16x16x32_bf16 v[28:31],a[244:247],v[64:67],v[28:31]
v_mul_f32 v98,v92,v92
v_mfma_f32_16x16x32_bf16 v[24:27],a[248:251],v[68:71],v[24:27]
s_waitcnt lgkmcnt(2)
ds_read_b128 v[64:67],v2 offset:38912
v_mfma_f32_16x16x32_bf16 v[28:31],a[252:255],v[68:71],v[28:31]
s_cbranch_scc1 Lg3o_slow1_%=
Lg3o_d1w0_%=:
v_mfma_f32_16x16x32_bf16 v[32:35],a[0:3],v[40:43],0
s_add_u32 m0,s62,16384
ds_read_b128 v[68:71],v2 offset:39936
v_mul_f32 v99,v93,v92
v_mfma_f32_16x16x32_bf16 v[36:39],a[4:7],v[40:43],0
buffer_load_dwordx4 v1,s[36:39],s67 offen offset:0 sc1 lds
v_mfma_f32_16x16x32_bf16 v[32:35],a[8:11],v[44:47],v[32:35]
s_mul_i32 s73,s48,7
ds_read_b128 v[40:43],v2 offset:40960
v_mul_f32 v98,s77,v98
v_mfma_f32_16x16x32_bf16 v[36:39],a[12:15],v[44:47],v[36:39]
s_lshr_b32 s73,s73,1
v_mul_f32 v98,v98,v97
v_fma_f32 v24,-v96,v16,v24
v_mfma_f32_16x16x32_bf16 v[32:35],a[16:19],v[48:51],v[32:35]
s_lshr_b32 s72,s67,5
ds_read_b128 v[44:47],v2 offset:41984
v_fma_f32 v25,-v96,v17,v25
v_mfma_f32_16x16x32_bf16 v[36:39],a[20:23],v[48:51],v[36:39]
buffer_load_dwordx4 v1,s[36:39],s67 offen offset:1024 sc1 lds
v_mfma_f32_16x16x32_bf16 v[32:35],a[24:27],v[52:55],v[32:35]
s_lshr_b32 s71,s62,2
ds_read_b128 v[48:51],v2 offset:43008
v_fma_f32 v26,-v96,v18,v26
v_mfma_f32_16x16x32_bf16 v[36:39],a[28:31],v[52:55],v[36:39]
s_waitcnt lgkmcnt(3)
v_fma_f32 v27,-v96,v19,v27
v_mul_f32 v72,v24,v24
v_mfma_f32_16x16x32_bf16 v[32:35],a[32:35],v[56:59],v[32:35]
s_mov_b64 s[44:45],s[46:47]
ds_read_b128 v[52:55],v2 offset:44032
v_mul_f32 v73,v25,v25
v_mfma_f32_16x16x32_bf16 v[36:39],a[36:39],v[56:59],v[36:39]
buffer_load_dwordx4 v1,s[36:39],s67 offen offset:2048 sc1 lds
v_mfma_f32_16x16x32_bf16 v[32:35],a[40:43],v[60:63],v[32:35]
s_mov_b64 s[46:47],s[48:49]
ds_read_b128 v[56:59],v2 offset:45056
v_mul_f32 v74,v26,v26
v_mfma_f32_16x16x32_bf16 v[36:39],a[44:47],v[60:63],v[36:39]
s_mov_b64 s[48:49],s[50:51]
v_mul_f32 v75,v27,v27
v_fma_f32 v72,v98,v72,v97
v_mfma_f32_16x16x32_bf16 v[32:35],a[48:51],v[64:67],v[32:35])"
R"(
ds_read_b128 v[60:63],v2 offset:46080
v_mul_f32 v72,v24,v72
v_mfma_f32_16x16x32_bf16 v[36:39],a[52:55],v[64:67],v[36:39]
buffer_load_dwordx4 v1,s[36:39],s67 offen offset:3072 sc1 lds
v_mfma_f32_16x16x32_bf16 v[32:35],a[56:59],v[68:71],v[32:35]
s_add_u32 m0,s71,s88
ds_read_b128 v[64:67],v2 offset:47104
v_fma_f32 v73,v98,v73,v97
v_mfma_f32_16x16x32_bf16 v[36:39],a[60:63],v[68:71],v[36:39]
s_waitcnt lgkmcnt(3)
buffer_load_dword v9,s[36:39],s72 offen offset:0 sc1 lds
v_mfma_f32_16x16x32_bf16 v[32:35],a[64:67],v[40:43],v[32:35]
s_waitcnt vmcnt(11)
ds_read_b128 v[68:71],v2 offset:48128
v_mul_f32 v73,v25,v73
v_mfma_f32_16x16x32_bf16 v[36:39],a[68:71],v[40:43],v[36:39]
s_barrier
v_mfma_f32_16x16x32_bf16 v[32:35],a[72:75],v[44:47],v[32:35]
s_xor_b32 s87,s63,65536
ds_read_b128 v[40:43],v2 offset:49152
ds_read_b32 v88,v8 offset:8192
v_mfma_f32_16x16x32_bf16 v[36:39],a[76:79],v[44:47],v[36:39]
s_cmp_ge_u32 s67,s59
v_exp_f32 v72,v72
v_mfma_f32_16x16x32_bf16 v[32:35],a[80:83],v[48:51],v[32:35]
s_cselect_b32 s86,s50,s51
ds_read_b128 v[44:47],v2 offset:50176
v_fma_f32 v72,v72,v99,v99
v_mfma_f32_16x16x32_bf16 v[36:39],a[84:87],v[48:51],v[36:39]
s_cselect_b32 s87,s87,s64
v_exp_f32 v73,v73
v_mfma_f32_16x16x32_bf16 v[32:35],a[88:91],v[52:55],v[32:35]
s_lshr_b32 s68,s51,5
ds_read_b128 v[48:51],v2 offset:51200
v_fma_f32 v73,v73,v99,v99
v_mfma_f32_16x16x32_bf16 v[36:39],a[92:95],v[52:55],v[36:39]
s_waitcnt lgkmcnt(4)
v_rcp_f32 v72,v72
v_mfma_f32_16x16x32_bf16 v[32:35],a[96:99],v[56:59],v[32:35]
s_waitcnt vmcnt(10) lgkmcnt(2)
ds_read_b128 v[52:55],v2 offset:52224
v_fma_f32 v74,v98,v74,v97
v_mfma_f32_16x16x32_bf16 v[36:39],a[100:103],v[56:59],v[36:39]
s_barrier
v_mfma_f32_16x16x32_bf16 v[32:35],a[104:107],v[60:63],v[32:35]
s_lshr_b32 s70,s52,5
ds_read_b128 v[56:59],v2 offset:53248
ds_read_b32 v13,v7 offset:256
v_mfma_f32_16x16x32_bf16 v[36:39],a[108:111],v[60:63],v[36:39]
s_mov_b32 s62,s87
v_rcp_f32 v73,v73
v_mfma_f32_16x16x32_bf16 v[32:35],a[112:115],v[64:67],v[32:35]
s_mov_b64 s[50:51],s[52:53]
ds_read_b128 v[60:63],v2 offset:54272
v_mul_f32 v74,v26,v74
v_mfma_f32_16x16x32_bf16 v[36:39],a[116:119],v[64:67],v[36:39]
v_exp_f32 v74,v74
v_mfma_f32_16x16x32_bf16 v[32:35],a[120:123],v[68:71],v[32:35]
s_waitcnt lgkmcnt(1)
ds_read_b128 v[64:67],v2 offset:55296
v_fma_f32 v74,v74,v99,v99
v_mfma_f32_16x16x32_bf16 v[36:39],a[124:127],v[68:71],v[36:39]
v_cmp_eq_u32 vcc,s60,v13
v_rcp_f32 v74,v74
v_add_f32_dpp v88,v88,v88 quad_perm:[1,0,3,2] row_mask:0xf bank_mask:0xf
v_mfma_f32_16x16x32_bf16 v[32:35],a[128:131],v[40:43],v[32:35]
s_cmp_eq_u32 vcc_lo,-1
ds_read_b128 v[68:71],v2 offset:56320
v_add_f32_dpp v88,v88,v88 quad_perm:[2,3,0,1] row_mask:0xf bank_mask:0xf
v_mfma_f32_16x16x32_bf16 v[36:39],a[132:135],v[40:43],v[36:39]
s_cselect_b32 s66,s86,s59
s_cmp_ge_u32 s66,s59
s_cselect_b32 s68,s68,s70
s_add_u32 m0,s78,0
v_mfma_f32_16x16x32_bf16 v[32:35],a[136:139],v[44:47],v[32:35]
buffer_load_dword v6,s[36:39],s68 offen offset:0 sc1 lds
v_mfma_f32_16x16x32_bf16 v[36:39],a[140:143],v[44:47],v[36:39]
s_mov_b32 m0,s62
ds_read_b128 v[40:43],v2 offset:57344
v_add_f32_dpp v88,v88,v88 row_half_mirror row_mask:0xf bank_mask:0xf
v_mfma_f32_16x16x32_bf16 v[32:35],a[144:147],v[48:51],v[32:35]
buffer_load_dwordx4 v0,s[36:39],s66 offen offset:0 sc1 lds
v_mfma_f32_16x16x32_bf16 v[36:39],a[148:151],v[48:51],v[36:39]
s_xor_b32 s64,s64,65536
ds_read_b128 v[44:47],v2 offset:58368
v_fma_f32 v75,v98,v75,v97
v_mfma_f32_16x16x32_bf16 v[32:35],a[152:155],v[52:55],v[32:35]
s_add_u32 s74,s53,s55
ds_write_b32 v10,v88 offset:128
v_mfma_f32_16x16x32_bf16 v[36:39],a[156:159],v[52:55],v[36:39]
s_waitcnt lgkmcnt(3)
ds_read_b128 v[48:51],v2 offset:59392
v_mul_f32 v75,v27,v75
v_mfma_f32_16x16x32_bf16 v[32:35],a[160:163],v[56:59],v[32:35]
buffer_load_dwordx4 v0,s[36:39],s66 offen offset:1024 sc1 lds
v_mfma_f32_16x16x32_bf16 v[36:39],a[164:167],v[56:59],v[36:39]
s_cmp_eq_u32 s74,s56
ds_read_b128 v[52:55],v2 offset:60416
v_mul_f32 v24,v24,v72
v_mfma_f32_16x16x32_bf16 v[32:35],a[168:171],v[60:63],v[32:35]
s_cselect_b32 s74,s57,s74
ds_read_b128 v[56:59],v2 offset:61440
v_mul_f32 v25,v25,v73
v_mfma_f32_16x16x32_bf16 v[36:39],a[172:175],v[60:63],v[36:39]
s_cmp_eq_u32 s74,s58
v_exp_f32 v75,v75
v_mfma_f32_16x16x32_bf16 v[32:35],a[176:179],v[64:67],v[32:35]
buffer_load_dwordx4 v0,s[36:39],s66 offen offset:2048 sc1 lds
v_mfma_f32_16x16x32_bf16 v[36:39],a[180:183],v[64:67],v[36:39]
s_cselect_b32 s74,s59,s74
ds_read_b128 v[60:63],v2 offset:62464
v_fma_f32 v75,v75,v99,v99
v_mfma_f32_16x16x32_bf16 v[32:35],a[184:187],v[68:71],v[32:35]
s_mov_b32 s52,s74
ds_read_b128 v[64:67],v2 offset:63488
v_mul_f32 v26,v26,v74
v_mfma_f32_16x16x32_bf16 v[36:39],a[188:191],v[68:71],v[36:39]
s_waitcnt lgkmcnt(3)
v_rcp_f32 v75,v75
v_mfma_f32_16x16x32_bf16 v[32:35],a[192:195],v[40:43],v[32:35]
s_waitcnt vmcnt(9)
ds_read_b128 v[68:71],v2 offset:64512
v_xor_b32 v2,65536,v2
v_mfma_f32_16x16x32_bf16 v[36:39],a[196:199],v[40:43],v[36:39]
s_barrier
v_fma_f32 v28,-v96,v20,v28
v_fma_f32 v29,-v96,v21,v29
v_fma_f32 v30,-v96,v22,v30
v_fma_f32 v31,-v96,v23,v31
v_mul_f32 v76,v28,v28
v_mul_f32 v77,v29,v29
v_mul_f32 v78,v30,v30
v_mul_f32 v79,v31,v31
v_fma_f32 v76,v98,v76,v97
v_fma_f32 v77,v98,v77,v97
v_fma_f32 v78,v98,v78,v97
v_fma_f32 v79,v98,v79,v97
v_mul_f32 v76,v28,v76
v_mul_f32 v77,v29,v77
v_mul_f32 v78,v30,v78
v_mul_f32 v79,v31,v79
v_exp_f32 v76,v76
v_exp_f32 v77,v77
v_exp_f32 v78,v78
v_exp_f32 v79,v79
v_mfma_f32_16x16x32_bf16 v[32:35],a[200:203],v[44:47],v[32:35]
buffer_load_dwordx4 v0,s[36:39],s66 offen offset:3072 sc1 lds
v_fma_f32 v76,v76,v99,v99
v_fma_f32 v77,v77,v99,v99
v_fma_f32 v78,v78,v99,v99
v_fma_f32 v79,v79,v99,v99
v_rcp_f32 v76,v76
v_rcp_f32 v77,v77
v_rcp_f32 v78,v78
v_rcp_f32 v79,v79
v_mfma_f32_16x16x32_bf16 v[36:39],a[204:207],v[44:47],v[36:39]
s_cmp_lt_u32 s67,s59
ds_read_b128 v[40:43],v2 offset:0
v_mul_f32 v27,v27,v75
v_cvt_pk_bf16_f32 v80,v24,v25
v_cvt_pk_bf16_f32 v81,v26,v27
v_mul_f32 v28,v28,v76
v_mul_f32 v29,v29,v77
v_mul_f32 v30,v30,v78
v_mul_f32 v31,v31,v79
v_cvt_pk_bf16_f32 v82,v28,v29
v_cvt_pk_bf16_f32 v83,v30,v31
v_mfma_f32_16x16x32_bf16 v[32:35],a[208:211],v[48:51],v[32:35]
buffer_store_dwordx4 v[80:83],v3,s[36:39],s73 offen offset:0 sc1
ds_read_b128 v[44:47],v2 offset:1024
ds_read_b64 v[92:93],v11 offset:128
v_xor_b32 v8,16384,v8
v_mfma_f32_16x16x32_bf16 v[36:39],a[212:215],v[48:51],v[36:39]
v_mfma_f32_16x16x32_bf16 v[32:35],a[216:219],v[52:55],v[32:35]
ds_read_b128 v[48:51],v2 offset:2048
v_mfma_f32_16x16x32_bf16 v[36:39],a[220:223],v[52:55],v[36:39]
s_waitcnt lgkmcnt(4)
v_mfma_f32_16x16x32_bf16 v[32:35],a[224:227],v[56:59],v[32:35]
s_waitcnt lgkmcnt(1)
ds_read_b128 v[52:55],v2 offset:3072
v_mul_f32 v96,s75,v92
v_mfma_f32_16x16x32_bf16 v[36:39],a[228:231],v[56:59],v[36:39]
v_fma_f32 v93,v93,s75,v12
v_fma_f32 v93,-v96,v96,v93
v_mfma_f32_16x16x32_bf16 v[32:35],a[232:235],v[60:63],v[32:35]
ds_read_b128 v[56:59],v2 offset:4096
v_max_f32 v93,v93,v12
v_mfma_f32_16x16x32_bf16 v[36:39],a[236:239],v[60:63],v[36:39]
v_rsq_f32 v92,v93
v_mfma_f32_16x16x32_bf16 v[32:35],a[240:243],v[64:67],v[32:35]
ds_read_b128 v[60:63],v2 offset:5120
v_mul_f32 v97,s76,v92
v_mfma_f32_16x16x32_bf16 v[36:39],a[244:247],v[64:67],v[36:39]
v_mul_f32 v98,v92,v92
v_mfma_f32_16x16x32_bf16 v[32:35],a[248:251],v[68:71],v[32:35]
s_waitcnt lgkmcnt(2)
ds_read_b128 v[64:67],v2 offset:6144
v_mfma_f32_16x16x32_bf16 v[36:39],a[252:255],v[68:71],v[36:39]
s_cbranch_scc1 Lg3o_d0w0_%=
s_branch Lg3o_slow0_%=)"
R"(
Lg3o_d0w1_%=:
v_mfma_f32_16x16x32_bf16 v[24:27],a[0:3],v[40:43],0
s_add_u32 m0,s62,16384
ds_read_b128 v[68:71],v2 offset:7168
v_mul_f32 v99,v93,v92
v_mfma_f32_16x16x32_bf16 v[28:31],a[4:7],v[40:43],0
s_mul_i32 s73,s47,7
v_mul_f32 v98,s77,v98
v_mul_f32 v98,v98,v97
v_mfma_f32_16x16x32_bf16 v[24:27],a[8:11],v[44:47],v[24:27]
buffer_load_dwordx4 v1,s[36:39],s66 offen offset:0 sc1 lds
v_mfma_f32_16x16x32_bf16 v[28:31],a[12:15],v[44:47],v[28:31]
s_lshr_b32 s73,s73,1
ds_read_b128 v[40:43],v2 offset:8192
v_fma_f32 v32,-v96,v16,v32
v_mfma_f32_16x16x32_bf16 v[24:27],a[16:19],v[48:51],v[24:27]
s_lshr_b32 s72,s66,5
ds_read_b128 v[44:47],v2 offset:9216
v_fma_f32 v33,-v96,v17,v33
v_mfma_f32_16x16x32_bf16 v[28:31],a[20:23],v[48:51],v[28:31]
s_lshr_b32 s71,s62,2
v_fma_f32 v34,-v96,v18,v34
v_fma_f32 v35,-v96,v19,v35
v_mfma_f32_16x16x32_bf16 v[24:27],a[24:27],v[52:55],v[24:27]
buffer_load_dwordx4 v1,s[36:39],s66 offen offset:1024 sc1 lds
v_mfma_f32_16x16x32_bf16 v[28:31],a[28:31],v[52:55],v[28:31]
s_waitcnt lgkmcnt(2)
ds_read_b128 v[48:51],v2 offset:10240
v_mul_f32 v72,v32,v32
v_mfma_f32_16x16x32_bf16 v[24:27],a[32:35],v[56:59],v[24:27]
ds_read_b128 v[52:55],v2 offset:11264
v_mul_f32 v73,v33,v33
v_mfma_f32_16x16x32_bf16 v[28:31],a[36:39],v[56:59],v[28:31]
v_mul_f32 v74,v34,v34
v_mul_f32 v75,v35,v35
v_mfma_f32_16x16x32_bf16 v[24:27],a[40:43],v[60:63],v[24:27]
buffer_load_dwordx4 v1,s[36:39],s66 offen offset:2048 sc1 lds
v_mfma_f32_16x16x32_bf16 v[28:31],a[44:47],v[60:63],v[28:31]
ds_read_b128 v[56:59],v2 offset:12288
v_fma_f32 v72,v98,v72,v97
v_mfma_f32_16x16x32_bf16 v[24:27],a[48:51],v[64:67],v[24:27]
ds_read_b128 v[60:63],v2 offset:13312
v_mul_f32 v72,v32,v72
v_mfma_f32_16x16x32_bf16 v[28:31],a[52:55],v[64:67],v[28:31]
v_exp_f32 v72,v72
v_mfma_f32_16x16x32_bf16 v[24:27],a[56:59],v[68:71],v[24:27]
buffer_load_dwordx4 v1,s[36:39],s66 offen offset:3072 sc1 lds
v_mfma_f32_16x16x32_bf16 v[28:31],a[60:63],v[68:71],v[28:31]
s_waitcnt lgkmcnt(2)
ds_read_b128 v[64:67],v2 offset:14336
s_add_u32 m0,s71,s88
v_mfma_f32_16x16x32_bf16 v[24:27],a[64:67],v[40:43],v[24:27]
s_xor_b32 s87,s64,65536
ds_read_b128 v[68:71],v2 offset:15360
buffer_load_dword v9,s[36:39],s72 offen offset:0 sc1 lds
s_waitcnt vmcnt(10)
v_mfma_f32_16x16x32_bf16 v[28:31],a[68:71],v[40:43],v[28:31]
s_barrier
v_mfma_f32_16x16x32_bf16 v[24:27],a[72:75],v[44:47],v[24:27]
s_cmp_ge_u32 s66,s59
ds_read_b128 v[40:43],v2 offset:16384
ds_read_b32 v88,v8 offset:0
v_mfma_f32_16x16x32_bf16 v[28:31],a[76:79],v[44:47],v[28:31]
s_cselect_b32 s86,s49,s50
v_fma_f32 v72,v72,v99,v99
v_fma_f32 v73,v98,v73,v97
v_mfma_f32_16x16x32_bf16 v[24:27],a[80:83],v[48:51],v[24:27]
s_cselect_b32 s87,s87,s63
ds_read_b128 v[44:47],v2 offset:17408
v_mul_f32 v73,v33,v73
v_mfma_f32_16x16x32_bf16 v[28:31],a[84:87],v[48:51],v[28:31]
s_mov_b32 s62,s87
v_exp_f32 v73,v73
v_mfma_f32_16x16x32_bf16 v[24:27],a[88:91],v[52:55],v[24:27]
s_mov_b32 m0,s62
ds_read_b128 v[48:51],v2 offset:18432
v_fma_f32 v73,v73,v99,v99
v_mfma_f32_16x16x32_bf16 v[28:31],a[92:95],v[52:55],v[28:31]
s_waitcnt lgkmcnt(4)
v_rcp_f32 v72,v72
v_mfma_f32_16x16x32_bf16 v[24:27],a[96:99],v[56:59],v[24:27]
s_nop 0
s_waitcnt lgkmcnt(2)
ds_read_b128 v[52:55],v2 offset:19456
v_fma_f32 v74,v98,v74,v97
v_mfma_f32_16x16x32_bf16 v[28:31],a[100:103],v[56:59],v[28:31]
s_barrier
v_mfma_f32_16x16x32_bf16 v[24:27],a[104:107],v[60:63],v[24:27]
ds_read_b128 v[56:59],v2 offset:20480
ds_read_b32 v13,v7 offset:0
v_mfma_f32_16x16x32_bf16 v[28:31],a[108:111],v[60:63],v[28:31]
v_rcp_f32 v73,v73
v_mfma_f32_16x16x32_bf16 v[24:27],a[112:115],v[64:67],v[24:27]
ds_read_b128 v[60:63],v2 offset:21504
v_mul_f32 v74,v34,v74
v_mfma_f32_16x16x32_bf16 v[28:31],a[116:119],v[64:67],v[28:31]
v_exp_f32 v74,v74
v_mfma_f32_16x16x32_bf16 v[24:27],a[120:123],v[68:71],v[24:27]
s_waitcnt lgkmcnt(1)
ds_read_b128 v[64:67],v2 offset:22528
v_fma_f32 v74,v74,v99,v99
v_mfma_f32_16x16x32_bf16 v[28:31],a[124:127],v[68:71],v[28:31]
v_cmp_eq_u32 vcc,s60,v13
v_rcp_f32 v74,v74
v_add_f32_dpp v88,v88,v88 quad_perm:[1,0,3,2] row_mask:0xf bank_mask:0xf
v_mfma_f32_16x16x32_bf16 v[24:27],a[128:131],v[40:43],v[24:27]
s_cmp_eq_u32 vcc_lo,-1
ds_read_b128 v[68:71],v2 offset:23552
s_cselect_b32 s67,s86,s59
v_mfma_f32_16x16x32_bf16 v[28:31],a[132:135],v[40:43],v[28:31]
buffer_load_dwordx4 v0,s[36:39],s67 offen offset:0 sc1 lds
v_mfma_f32_16x16x32_bf16 v[24:27],a[136:139],v[44:47],v[24:27]
s_xor_b32 s63,s63,65536
ds_read_b128 v[40:43],v2 offset:24576
v_add_f32_dpp v88,v88,v88 quad_perm:[2,3,0,1] row_mask:0xf bank_mask:0xf
v_mfma_f32_16x16x32_bf16 v[28:31],a[140:143],v[44:47],v[28:31]
s_add_u32 s53,s52,s55
v_add_f32_dpp v88,v88,v88 row_half_mirror row_mask:0xf bank_mask:0xf
v_fma_f32 v75,v98,v75,v97
v_mfma_f32_16x16x32_bf16 v[24:27],a[144:147],v[48:51],v[24:27]
s_cmp_eq_u32 s53,s56
ds_write_b32 v10,v88 offset:0
v_mfma_f32_16x16x32_bf16 v[28:31],a[148:151],v[48:51],v[28:31]
s_cselect_b32 s53,s57,s53
ds_read_b128 v[44:47],v2 offset:25600
buffer_load_dwordx4 v0,s[36:39],s67 offen offset:1024 sc1 lds
v_mfma_f32_16x16x32_bf16 v[24:27],a[152:155],v[52:55],v[24:27]
s_cmp_eq_u32 s53,s58
ds_read_b128 v[48:51],v2 offset:26624
v_mul_f32 v75,v35,v75
v_mfma_f32_16x16x32_bf16 v[28:31],a[156:159],v[52:55],v[28:31]
s_waitcnt lgkmcnt(4)
v_exp_f32 v75,v75
v_mfma_f32_16x16x32_bf16 v[24:27],a[160:163],v[56:59],v[24:27]
s_cselect_b32 s53,s59,s53
ds_read_b128 v[52:55],v2 offset:27648
v_fma_f32 v75,v75,v99,v99
v_mfma_f32_16x16x32_bf16 v[28:31],a[164:167],v[56:59],v[28:31]
buffer_load_dwordx4 v0,s[36:39],s67 offen offset:2048 sc1 lds
v_mfma_f32_16x16x32_bf16 v[24:27],a[168:171],v[60:63],v[24:27]
s_cmp_ge_u32 s66,s59
ds_read_b128 v[56:59],v2 offset:28672
v_mul_f32 v32,v32,v72
v_mfma_f32_16x16x32_bf16 v[28:31],a[172:175],v[60:63],v[28:31]
v_rcp_f32 v75,v75
v_mfma_f32_16x16x32_bf16 v[24:27],a[176:179],v[64:67],v[24:27]
ds_read_b128 v[60:63],v2 offset:29696
v_mul_f32 v33,v33,v73
v_mfma_f32_16x16x32_bf16 v[28:31],a[180:183],v[64:67],v[28:31]
buffer_load_dwordx4 v0,s[36:39],s67 offen offset:3072 sc1 lds
v_mfma_f32_16x16x32_bf16 v[24:27],a[184:187],v[68:71],v[24:27]
ds_read_b128 v[64:67],v2 offset:30720
v_mul_f32 v34,v34,v74
v_mfma_f32_16x16x32_bf16 v[28:31],a[188:191],v[68:71],v[28:31]
s_waitcnt lgkmcnt(3)
v_mul_f32 v35,v35,v75
v_cvt_pk_bf16_f32 v80,v32,v33
v_mfma_f32_16x16x32_bf16 v[24:27],a[192:195],v[40:43],v[24:27]
s_waitcnt vmcnt(9)
ds_read_b128 v[68:71],v2 offset:31744
v_cvt_pk_bf16_f32 v81,v34,v35
v_mfma_f32_16x16x32_bf16 v[28:31],a[196:199],v[40:43],v[28:31]
s_barrier
v_mfma_f32_16x16x32_bf16 v[24:27],a[200:203],v[44:47],v[24:27]
v_fma_f32 v36,-v96,v20,v36
v_fma_f32 v37,-v96,v21,v37
v_fma_f32 v38,-v96,v22,v38
v_fma_f32 v39,-v96,v23,v39
v_mul_f32 v76,v36,v36
v_mul_f32 v77,v37,v37
v_mul_f32 v78,v38,v38
v_mul_f32 v79,v39,v39
v_fma_f32 v76,v98,v76,v97
v_fma_f32 v77,v98,v77,v97
v_fma_f32 v78,v98,v78,v97
v_fma_f32 v79,v98,v79,v97
v_mul_f32 v76,v36,v76
v_mul_f32 v77,v37,v77
v_mul_f32 v78,v38,v78
v_mul_f32 v79,v39,v79
v_exp_f32 v76,v76
v_exp_f32 v77,v77
v_exp_f32 v78,v78
v_exp_f32 v79,v79
v_mfma_f32_16x16x32_bf16 v[28:31],a[204:207],v[44:47],v[28:31]
ds_read_b128 v[40:43],v2 offset:32768
v_fma_f32 v76,v76,v99,v99
v_fma_f32 v77,v77,v99,v99
v_fma_f32 v78,v78,v99,v99
v_fma_f32 v79,v79,v99,v99
v_rcp_f32 v76,v76
v_rcp_f32 v77,v77
v_rcp_f32 v78,v78
v_rcp_f32 v79,v79
v_mfma_f32_16x16x32_bf16 v[24:27],a[208:211],v[48:51],v[24:27]
v_mul_f32 v36,v36,v76
v_mul_f32 v37,v37,v77
v_mul_f32 v38,v38,v78
v_mul_f32 v39,v39,v79
v_cvt_pk_bf16_f32 v82,v36,v37)"
R"(
v_cvt_pk_bf16_f32 v83,v38,v39
v_mfma_f32_16x16x32_bf16 v[28:31],a[212:215],v[48:51],v[28:31]
ds_read_b128 v[44:47],v2 offset:33792
buffer_store_dwordx4 v[80:83],v3,s[36:39],s73 offen offset:0 sc1
ds_read_b64 v[92:93],v11 offset:0
v_mfma_f32_16x16x32_bf16 v[24:27],a[216:219],v[52:55],v[24:27]
ds_read_b128 v[48:51],v2 offset:34816
v_mfma_f32_16x16x32_bf16 v[28:31],a[220:223],v[52:55],v[28:31]
s_waitcnt lgkmcnt(4)
v_mfma_f32_16x16x32_bf16 v[24:27],a[224:227],v[56:59],v[24:27]
ds_read_b128 v[52:55],v2 offset:35840
v_mfma_f32_16x16x32_bf16 v[28:31],a[228:231],v[56:59],v[28:31]
s_waitcnt lgkmcnt(2)
v_mul_f32 v96,s75,v92
v_fma_f32 v93,v93,s75,v12
v_mfma_f32_16x16x32_bf16 v[24:27],a[232:235],v[60:63],v[24:27]
ds_read_b128 v[56:59],v2 offset:36864
v_fma_f32 v93,-v96,v96,v93
v_mfma_f32_16x16x32_bf16 v[28:31],a[236:239],v[60:63],v[28:31]
v_max_f32 v93,v93,v12
v_mfma_f32_16x16x32_bf16 v[24:27],a[240:243],v[64:67],v[24:27]
ds_read_b128 v[60:63],v2 offset:37888
v_mfma_f32_16x16x32_bf16 v[28:31],a[244:247],v[64:67],v[28:31]
v_rsq_f32 v92,v93
v_mfma_f32_16x16x32_bf16 v[24:27],a[248:251],v[68:71],v[24:27]
s_waitcnt lgkmcnt(2)
ds_read_b128 v[64:67],v2 offset:38912
v_mul_f32 v97,s76,v92
v_mul_f32 v98,v92,v92
v_mfma_f32_16x16x32_bf16 v[28:31],a[252:255],v[68:71],v[28:31]
s_cbranch_scc1 Lg3o_slow1_%=
Lg3o_d1w1_%=:
v_mfma_f32_16x16x32_bf16 v[32:35],a[0:3],v[40:43],0
s_add_u32 m0,s62,16384
ds_read_b128 v[68:71],v2 offset:39936
v_mul_f32 v99,v93,v92
v_mfma_f32_16x16x32_bf16 v[36:39],a[4:7],v[40:43],0
s_mul_i32 s73,s48,7
v_mul_f32 v98,s77,v98
v_mul_f32 v98,v98,v97
v_mfma_f32_16x16x32_bf16 v[32:35],a[8:11],v[44:47],v[32:35]
buffer_load_dwordx4 v1,s[36:39],s67 offen offset:0 sc1 lds
v_mfma_f32_16x16x32_bf16 v[36:39],a[12:15],v[44:47],v[36:39]
s_lshr_b32 s73,s73,1
ds_read_b128 v[40:43],v2 offset:40960
v_fma_f32 v24,-v96,v16,v24
v_mfma_f32_16x16x32_bf16 v[32:35],a[16:19],v[48:51],v[32:35]
s_lshr_b32 s72,s67,5
ds_read_b128 v[44:47],v2 offset:41984
v_fma_f32 v25,-v96,v17,v25
v_mfma_f32_16x16x32_bf16 v[36:39],a[20:23],v[48:51],v[36:39]
s_lshr_b32 s71,s62,2
v_fma_f32 v26,-v96,v18,v26
v_fma_f32 v27,-v96,v19,v27
v_mfma_f32_16x16x32_bf16 v[32:35],a[24:27],v[52:55],v[32:35]
buffer_load_dwordx4 v1,s[36:39],s67 offen offset:1024 sc1 lds
v_mfma_f32_16x16x32_bf16 v[36:39],a[28:31],v[52:55],v[36:39]
s_waitcnt lgkmcnt(2)
ds_read_b128 v[48:51],v2 offset:43008
v_mul_f32 v72,v24,v24
v_mfma_f32_16x16x32_bf16 v[32:35],a[32:35],v[56:59],v[32:35]
s_mov_b64 s[44:45],s[46:47]
ds_read_b128 v[52:55],v2 offset:44032
v_mul_f32 v73,v25,v25
v_mfma_f32_16x16x32_bf16 v[36:39],a[36:39],v[56:59],v[36:39]
s_mov_b64 s[46:47],s[48:49]
v_mul_f32 v74,v26,v26
v_mul_f32 v75,v27,v27
v_mfma_f32_16x16x32_bf16 v[32:35],a[40:43],v[60:63],v[32:35]
buffer_load_dwordx4 v1,s[36:39],s67 offen offset:2048 sc1 lds
v_mfma_f32_16x16x32_bf16 v[36:39],a[44:47],v[60:63],v[36:39]
s_mov_b64 s[48:49],s[50:51]
ds_read_b128 v[56:59],v2 offset:45056
v_fma_f32 v72,v98,v72,v97
v_mfma_f32_16x16x32_bf16 v[32:35],a[48:51],v[64:67],v[32:35]
ds_read_b128 v[60:63],v2 offset:46080
v_mul_f32 v72,v24,v72
v_mfma_f32_16x16x32_bf16 v[36:39],a[52:55],v[64:67],v[36:39]
v_exp_f32 v72,v72
v_mfma_f32_16x16x32_bf16 v[32:35],a[56:59],v[68:71],v[32:35]
buffer_load_dwordx4 v1,s[36:39],s67 offen offset:3072 sc1 lds
v_mfma_f32_16x16x32_bf16 v[36:39],a[60:63],v[68:71],v[36:39]
s_waitcnt lgkmcnt(2)
ds_read_b128 v[64:67],v2 offset:47104
s_add_u32 m0,s71,s88
v_mfma_f32_16x16x32_bf16 v[32:35],a[64:67],v[40:43],v[32:35]
s_xor_b32 s87,s63,65536
ds_read_b128 v[68:71],v2 offset:48128
buffer_load_dword v9,s[36:39],s72 offen offset:0 sc1 lds
s_waitcnt vmcnt(10)
v_mfma_f32_16x16x32_bf16 v[36:39],a[68:71],v[40:43],v[36:39]
s_barrier
v_mfma_f32_16x16x32_bf16 v[32:35],a[72:75],v[44:47],v[32:35]
s_cmp_ge_u32 s67,s59
ds_read_b128 v[40:43],v2 offset:49152
ds_read_b32 v88,v8 offset:8192
v_mfma_f32_16x16x32_bf16 v[36:39],a[76:79],v[44:47],v[36:39]
s_cselect_b32 s86,s50,s51
v_fma_f32 v72,v72,v99,v99
v_fma_f32 v73,v98,v73,v97
v_mfma_f32_16x16x32_bf16 v[32:35],a[80:83],v[48:51],v[32:35]
s_cselect_b32 s87,s87,s64
ds_read_b128 v[44:47],v2 offset:50176
v_mul_f32 v73,v25,v73
v_mfma_f32_16x16x32_bf16 v[36:39],a[84:87],v[48:51],v[36:39]
s_mov_b32 s62,s87
v_exp_f32 v73,v73
v_mfma_f32_16x16x32_bf16 v[32:35],a[88:91],v[52:55],v[32:35]
s_mov_b32 m0,s62
ds_read_b128 v[48:51],v2 offset:51200
v_fma_f32 v73,v73,v99,v99
v_mfma_f32_16x16x32_bf16 v[36:39],a[92:95],v[52:55],v[36:39]
s_waitcnt lgkmcnt(4)
v_rcp_f32 v72,v72
v_mfma_f32_16x16x32_bf16 v[32:35],a[96:99],v[56:59],v[32:35]
s_nop 0
s_waitcnt lgkmcnt(2)
ds_read_b128 v[52:55],v2 offset:52224
v_fma_f32 v74,v98,v74,v97
v_mfma_f32_16x16x32_bf16 v[36:39],a[100:103],v[56:59],v[36:39]
s_barrier
v_mfma_f32_16x16x32_bf16 v[32:35],a[104:107],v[60:63],v[32:35]
s_mov_b64 s[50:51],s[52:53]
ds_read_b128 v[56:59],v2 offset:53248
ds_read_b32 v13,v7 offset:256
v_mfma_f32_16x16x32_bf16 v[36:39],a[108:111],v[60:63],v[36:39]
v_rcp_f32 v73,v73
v_mfma_f32_16x16x32_bf16 v[32:35],a[112:115],v[64:67],v[32:35]
ds_read_b128 v[60:63],v2 offset:54272
v_mul_f32 v74,v26,v74
v_mfma_f32_16x16x32_bf16 v[36:39],a[116:119],v[64:67],v[36:39]
v_exp_f32 v74,v74
v_mfma_f32_16x16x32_bf16 v[32:35],a[120:123],v[68:71],v[32:35]
s_waitcnt lgkmcnt(1)
ds_read_b128 v[64:67],v2 offset:55296
v_fma_f32 v74,v74,v99,v99
v_mfma_f32_16x16x32_bf16 v[36:39],a[124:127],v[68:71],v[36:39]
v_cmp_eq_u32 vcc,s60,v13
v_rcp_f32 v74,v74
v_add_f32_dpp v88,v88,v88 quad_perm:[1,0,3,2] row_mask:0xf bank_mask:0xf
v_mfma_f32_16x16x32_bf16 v[32:35],a[128:131],v[40:43],v[32:35]
s_cmp_eq_u32 vcc_lo,-1
ds_read_b128 v[68:71],v2 offset:56320
s_cselect_b32 s66,s86,s59
v_mfma_f32_16x16x32_bf16 v[36:39],a[132:135],v[40:43],v[36:39]
buffer_load_dwordx4 v0,s[36:39],s66 offen offset:0 sc1 lds
v_mfma_f32_16x16x32_bf16 v[32:35],a[136:139],v[44:47],v[32:35]
s_xor_b32 s64,s64,65536
ds_read_b128 v[40:43],v2 offset:57344
v_add_f32_dpp v88,v88,v88 quad_perm:[2,3,0,1] row_mask:0xf bank_mask:0xf
v_mfma_f32_16x16x32_bf16 v[36:39],a[140:143],v[44:47],v[36:39]
s_add_u32 s74,s53,s55
v_add_f32_dpp v88,v88,v88 row_half_mirror row_mask:0xf bank_mask:0xf
v_fma_f32 v75,v98,v75,v97
v_mfma_f32_16x16x32_bf16 v[32:35],a[144:147],v[48:51],v[32:35]
s_cmp_eq_u32 s74,s56
ds_write_b32 v10,v88 offset:128
v_mfma_f32_16x16x32_bf16 v[36:39],a[148:151],v[48:51],v[36:39]
s_cselect_b32 s74,s57,s74
ds_read_b128 v[44:47],v2 offset:58368
buffer_load_dwordx4 v0,s[36:39],s66 offen offset:1024 sc1 lds
v_mfma_f32_16x16x32_bf16 v[32:35],a[152:155],v[52:55],v[32:35]
s_cmp_eq_u32 s74,s58
ds_read_b128 v[48:51],v2 offset:59392
v_mul_f32 v75,v27,v75
v_mfma_f32_16x16x32_bf16 v[36:39],a[156:159],v[52:55],v[36:39]
s_waitcnt lgkmcnt(4)
v_exp_f32 v75,v75
v_mfma_f32_16x16x32_bf16 v[32:35],a[160:163],v[56:59],v[32:35]
s_cselect_b32 s74,s59,s74
ds_read_b128 v[52:55],v2 offset:60416
v_fma_f32 v75,v75,v99,v99
v_mfma_f32_16x16x32_bf16 v[36:39],a[164:167],v[56:59],v[36:39]
buffer_load_dwordx4 v0,s[36:39],s66 offen offset:2048 sc1 lds
v_mfma_f32_16x16x32_bf16 v[32:35],a[168:171],v[60:63],v[32:35]
s_mov_b32 s52,s74
ds_read_b128 v[56:59],v2 offset:61440
v_mul_f32 v24,v24,v72
v_mfma_f32_16x16x32_bf16 v[36:39],a[172:175],v[60:63],v[36:39]
s_cmp_lt_u32 s67,s59
v_rcp_f32 v75,v75
v_mfma_f32_16x16x32_bf16 v[32:35],a[176:179],v[64:67],v[32:35]
ds_read_b128 v[60:63],v2 offset:62464
v_mul_f32 v25,v25,v73
v_mfma_f32_16x16x32_bf16 v[36:39],a[180:183],v[64:67],v[36:39]
buffer_load_dwordx4 v0,s[36:39],s66 offen offset:3072 sc1 lds
v_mfma_f32_16x16x32_bf16 v[32:35],a[184:187],v[68:71],v[32:35]
ds_read_b128 v[64:67],v2 offset:63488
v_mul_f32 v26,v26,v74
v_mfma_f32_16x16x32_bf16 v[36:39],a[188:191],v[68:71],v[36:39]
s_waitcnt lgkmcnt(3)
v_mul_f32 v27,v27,v75
v_cvt_pk_bf16_f32 v80,v24,v25
v_mfma_f32_16x16x32_bf16 v[32:35],a[192:195],v[40:43],v[32:35]
s_waitcnt vmcnt(9)
ds_read_b128 v[68:71],v2 offset:64512
v_xor_b32 v2,65536,v2
v_mfma_f32_16x16x32_bf16 v[36:39],a[196:199],v[40:43],v[36:39]
s_barrier
v_mfma_f32_16x16x32_bf16 v[32:35],a[200:203],v[44:47],v[32:35])"
R"(
v_fma_f32 v28,-v96,v20,v28
v_fma_f32 v29,-v96,v21,v29
v_fma_f32 v30,-v96,v22,v30
v_fma_f32 v31,-v96,v23,v31
v_mul_f32 v76,v28,v28
v_mul_f32 v77,v29,v29
v_mul_f32 v78,v30,v30
v_mul_f32 v79,v31,v31
v_fma_f32 v76,v98,v76,v97
v_fma_f32 v77,v98,v77,v97
v_fma_f32 v78,v98,v78,v97
v_fma_f32 v79,v98,v79,v97
v_mul_f32 v76,v28,v76
v_mul_f32 v77,v29,v77
v_mul_f32 v78,v30,v78
v_mul_f32 v79,v31,v79
v_exp_f32 v76,v76
v_exp_f32 v77,v77
v_exp_f32 v78,v78
v_exp_f32 v79,v79
v_mfma_f32_16x16x32_bf16 v[36:39],a[204:207],v[44:47],v[36:39]
ds_read_b128 v[40:43],v2 offset:0
v_fma_f32 v76,v76,v99,v99
v_fma_f32 v77,v77,v99,v99
v_fma_f32 v78,v78,v99,v99
v_fma_f32 v79,v79,v99,v99
v_rcp_f32 v76,v76
v_rcp_f32 v77,v77
v_rcp_f32 v78,v78
v_rcp_f32 v79,v79
v_mfma_f32_16x16x32_bf16 v[32:35],a[208:211],v[48:51],v[32:35]
v_cvt_pk_bf16_f32 v81,v26,v27
v_mul_f32 v28,v28,v76
v_mul_f32 v29,v29,v77
v_mul_f32 v30,v30,v78
v_mul_f32 v31,v31,v79
v_cvt_pk_bf16_f32 v82,v28,v29
v_cvt_pk_bf16_f32 v83,v30,v31
v_mfma_f32_16x16x32_bf16 v[36:39],a[212:215],v[48:51],v[36:39]
ds_read_b128 v[44:47],v2 offset:1024
buffer_store_dwordx4 v[80:83],v3,s[36:39],s73 offen offset:0 sc1
ds_read_b64 v[92:93],v11 offset:128
v_xor_b32 v8,16384,v8
v_mfma_f32_16x16x32_bf16 v[32:35],a[216:219],v[52:55],v[32:35]
ds_read_b128 v[48:51],v2 offset:2048
v_mfma_f32_16x16x32_bf16 v[36:39],a[220:223],v[52:55],v[36:39]
s_waitcnt lgkmcnt(4)
v_mfma_f32_16x16x32_bf16 v[32:35],a[224:227],v[56:59],v[32:35]
ds_read_b128 v[52:55],v2 offset:3072
v_mfma_f32_16x16x32_bf16 v[36:39],a[228:231],v[56:59],v[36:39]
s_waitcnt lgkmcnt(2)
v_mul_f32 v96,s75,v92
v_fma_f32 v93,v93,s75,v12
v_mfma_f32_16x16x32_bf16 v[32:35],a[232:235],v[60:63],v[32:35]
ds_read_b128 v[56:59],v2 offset:4096
v_fma_f32 v93,-v96,v96,v93
v_mfma_f32_16x16x32_bf16 v[36:39],a[236:239],v[60:63],v[36:39]
v_max_f32 v93,v93,v12
v_mfma_f32_16x16x32_bf16 v[32:35],a[240:243],v[64:67],v[32:35]
ds_read_b128 v[60:63],v2 offset:5120
v_mfma_f32_16x16x32_bf16 v[36:39],a[244:247],v[64:67],v[36:39]
v_rsq_f32 v92,v93
v_mfma_f32_16x16x32_bf16 v[32:35],a[248:251],v[68:71],v[32:35]
s_waitcnt lgkmcnt(2)
ds_read_b128 v[64:67],v2 offset:6144
v_mul_f32 v97,s76,v92
v_mul_f32 v98,v92,v92
v_mfma_f32_16x16x32_bf16 v[36:39],a[252:255],v[68:71],v[36:39]
s_cbranch_scc1 Lg3o_d0w1_%=
s_branch Lg3o_slow0_%=
Lg3o_d0w2_%=:
v_mfma_f32_16x16x32_bf16 v[24:27],a[0:3],v[40:43],0
s_add_u32 m0,s62,16384
ds_read_b128 v[68:71],v2 offset:7168
v_mul_f32 v99,v93,v92
v_mfma_f32_16x16x32_bf16 v[28:31],a[4:7],v[40:43],0
s_mul_i32 s73,s47,7
v_mul_f32 v98,s77,v98
v_mul_f32 v98,v98,v97
v_mfma_f32_16x16x32_bf16 v[24:27],a[8:11],v[44:47],v[24:27]
s_lshr_b32 s73,s73,1
ds_read_b128 v[40:43],v2 offset:8192
s_lshr_b32 s72,s66,5
v_mfma_f32_16x16x32_bf16 v[28:31],a[12:15],v[44:47],v[28:31]
buffer_load_dwordx4 v1,s[36:39],s66 offen offset:0 sc1 lds
v_mfma_f32_16x16x32_bf16 v[24:27],a[16:19],v[48:51],v[24:27]
s_lshr_b32 s71,s62,2
ds_read_b128 v[44:47],v2 offset:9216
v_fma_f32 v32,-v96,v16,v32
v_mfma_f32_16x16x32_bf16 v[28:31],a[20:23],v[48:51],v[28:31]
v_fma_f32 v33,-v96,v17,v33
v_fma_f32 v34,-v96,v18,v34
v_mfma_f32_16x16x32_bf16 v[24:27],a[24:27],v[52:55],v[24:27]
ds_read_b128 v[48:51],v2 offset:10240
v_fma_f32 v35,-v96,v19,v35
v_mfma_f32_16x16x32_bf16 v[28:31],a[28:31],v[52:55],v[28:31]
s_waitcnt lgkmcnt(3)
buffer_load_dwordx4 v1,s[36:39],s66 offen offset:1024 sc1 lds
v_mfma_f32_16x16x32_bf16 v[24:27],a[32:35],v[56:59],v[24:27]
ds_read_b128 v[52:55],v2 offset:11264
v_mul_f32 v72,v32,v32
v_mfma_f32_16x16x32_bf16 v[28:31],a[36:39],v[56:59],v[28:31]
v_mul_f32 v73,v33,v33
v_mul_f32 v74,v34,v34
v_mfma_f32_16x16x32_bf16 v[24:27],a[40:43],v[60:63],v[24:27]
ds_read_b128 v[56:59],v2 offset:12288
v_mul_f32 v75,v35,v35
v_mfma_f32_16x16x32_bf16 v[28:31],a[44:47],v[60:63],v[28:31]
buffer_load_dwordx4 v1,s[36:39],s66 offen offset:2048 sc1 lds
v_mfma_f32_16x16x32_bf16 v[24:27],a[48:51],v[64:67],v[24:27]
ds_read_b128 v[60:63],v2 offset:13312
v_fma_f32 v72,v98,v72,v97
v_mfma_f32_16x16x32_bf16 v[28:31],a[52:55],v[64:67],v[28:31]
v_mul_f32 v72,v32,v72
v_fma_f32 v73,v98,v73,v97
v_mfma_f32_16x16x32_bf16 v[24:27],a[56:59],v[68:71],v[24:27]
ds_read_b128 v[64:67],v2 offset:14336
v_mul_f32 v73,v33,v73
v_mfma_f32_16x16x32_bf16 v[28:31],a[60:63],v[68:71],v[28:31]
s_waitcnt lgkmcnt(3)
buffer_load_dwordx4 v1,s[36:39],s66 offen offset:3072 sc1 lds
s_add_u32 m0,s71,s88
v_mfma_f32_16x16x32_bf16 v[24:27],a[64:67],v[40:43],v[24:27]
s_xor_b32 s87,s64,65536
ds_read_b128 v[68:71],v2 offset:15360
buffer_load_dword v9,s[36:39],s72 offen offset:0 sc1 lds
s_waitcnt vmcnt(10)
v_mfma_f32_16x16x32_bf16 v[28:31],a[68:71],v[40:43],v[28:31]
s_barrier
v_mfma_f32_16x16x32_bf16 v[24:27],a[72:75],v[44:47],v[24:27]
s_cmp_ge_u32 s66,s59
ds_read_b128 v[40:43],v2 offset:16384
ds_read_b32 v88,v8 offset:0
v_mfma_f32_16x16x32_bf16 v[28:31],a[76:79],v[44:47],v[28:31]
s_cselect_b32 s86,s49,s50
v_exp_f32 v72,v72
v_mfma_f32_16x16x32_bf16 v[24:27],a[80:83],v[48:51],v[24:27]
s_cselect_b32 s87,s87,s63
ds_read_b128 v[44:47],v2 offset:17408
v_fma_f32 v72,v72,v99,v99
v_mfma_f32_16x16x32_bf16 v[28:31],a[84:87],v[48:51],v[28:31]
s_mov_b32 s62,s87
v_exp_f32 v73,v73
v_mfma_f32_16x16x32_bf16 v[24:27],a[88:91],v[52:55],v[24:27]
s_mov_b32 m0,s62
ds_read_b128 v[48:51],v2 offset:18432
v_fma_f32 v73,v73,v99,v99
v_mfma_f32_16x16x32_bf16 v[28:31],a[92:95],v[52:55],v[28:31]
s_waitcnt lgkmcnt(4)
v_rcp_f32 v72,v72
v_mfma_f32_16x16x32_bf16 v[24:27],a[96:99],v[56:59],v[24:27]
s_nop 0
s_waitcnt lgkmcnt(2)
ds_read_b128 v[52:55],v2 offset:19456
v_fma_f32 v74,v98,v74,v97
v_mfma_f32_16x16x32_bf16 v[28:31],a[100:103],v[56:59],v[28:31]
s_barrier
v_mfma_f32_16x16x32_bf16 v[24:27],a[104:107],v[60:63],v[24:27]
ds_read_b128 v[56:59],v2 offset:20480
ds_read_b32 v13,v7 offset:0
v_mfma_f32_16x16x32_bf16 v[28:31],a[108:111],v[60:63],v[28:31]
v_rcp_f32 v73,v73
v_mfma_f32_16x16x32_bf16 v[24:27],a[112:115],v[64:67],v[24:27]
ds_read_b128 v[60:63],v2 offset:21504
v_mul_f32 v74,v34,v74
v_mfma_f32_16x16x32_bf16 v[28:31],a[116:119],v[64:67],v[28:31]
v_exp_f32 v74,v74
v_mfma_f32_16x16x32_bf16 v[24:27],a[120:123],v[68:71],v[24:27]
s_waitcnt lgkmcnt(1)
ds_read_b128 v[64:67],v2 offset:22528
v_fma_f32 v74,v74,v99,v99
v_mfma_f32_16x16x32_bf16 v[28:31],a[124:127],v[68:71],v[28:31]
v_cmp_eq_u32 vcc,s60,v13
v_rcp_f32 v74,v74
v_add_f32_dpp v88,v88,v88 quad_perm:[1,0,3,2] row_mask:0xf bank_mask:0xf
v_mfma_f32_16x16x32_bf16 v[24:27],a[128:131],v[40:43],v[24:27]
s_cmp_eq_u32 vcc_lo,-1
ds_read_b128 v[68:71],v2 offset:23552
v_add_f32_dpp v88,v88,v88 quad_perm:[2,3,0,1] row_mask:0xf bank_mask:0xf
v_mfma_f32_16x16x32_bf16 v[28:31],a[132:135],v[40:43],v[28:31]
s_cselect_b32 s67,s86,s59
v_add_f32_dpp v88,v88,v88 row_half_mirror row_mask:0xf bank_mask:0xf
v_fma_f32 v75,v98,v75,v97
v_mfma_f32_16x16x32_bf16 v[24:27],a[136:139],v[44:47],v[24:27]
buffer_load_dwordx4 v0,s[36:39],s67 offen offset:0 sc1 lds
v_mfma_f32_16x16x32_bf16 v[28:31],a[140:143],v[44:47],v[28:31]
s_xor_b32 s63,s63,65536
ds_read_b128 v[40:43],v2 offset:24576
v_mul_f32 v75,v35,v75
v_mfma_f32_16x16x32_bf16 v[24:27],a[144:147],v[48:51],v[24:27]
s_add_u32 s53,s52,s55
ds_write_b32 v10,v88 offset:0
v_mfma_f32_16x16x32_bf16 v[28:31],a[148:151],v[48:51],v[28:31]
s_cmp_eq_u32 s53,s56
ds_read_b128 v[44:47],v2 offset:25600
v_mul_f32 v32,v32,v72
v_mfma_f32_16x16x32_bf16 v[24:27],a[152:155],v[52:55],v[24:27]
buffer_load_dwordx4 v0,s[36:39],s67 offen offset:1024 sc1 lds
v_mfma_f32_16x16x32_bf16 v[28:31],a[156:159],v[52:55],v[28:31]
s_waitcnt lgkmcnt(3))"
R"(
ds_read_b128 v[48:51],v2 offset:26624
v_mul_f32 v33,v33,v73
v_mfma_f32_16x16x32_bf16 v[24:27],a[160:163],v[56:59],v[24:27]
s_cselect_b32 s53,s57,s53
ds_read_b128 v[52:55],v2 offset:27648
v_mul_f32 v34,v34,v74
v_mfma_f32_16x16x32_bf16 v[28:31],a[164:167],v[56:59],v[28:31]
s_cmp_eq_u32 s53,s58
v_exp_f32 v75,v75
v_mfma_f32_16x16x32_bf16 v[24:27],a[168:171],v[60:63],v[24:27]
buffer_load_dwordx4 v0,s[36:39],s67 offen offset:2048 sc1 lds
v_mfma_f32_16x16x32_bf16 v[28:31],a[172:175],v[60:63],v[28:31]
s_cselect_b32 s53,s59,s53
ds_read_b128 v[56:59],v2 offset:28672
v_fma_f32 v75,v75,v99,v99
v_mfma_f32_16x16x32_bf16 v[24:27],a[176:179],v[64:67],v[24:27]
s_cmp_ge_u32 s66,s59
ds_read_b128 v[60:63],v2 offset:29696
v_cvt_pk_bf16_f32 v80,v32,v33
v_mfma_f32_16x16x32_bf16 v[28:31],a[180:183],v[64:67],v[28:31]
v_rcp_f32 v75,v75
v_mfma_f32_16x16x32_bf16 v[24:27],a[184:187],v[68:71],v[24:27]
buffer_load_dwordx4 v0,s[36:39],s67 offen offset:3072 sc1 lds
v_mfma_f32_16x16x32_bf16 v[28:31],a[188:191],v[68:71],v[28:31]
s_waitcnt lgkmcnt(2)
ds_read_b128 v[64:67],v2 offset:30720
v_mul_f32 v35,v35,v75
v_mfma_f32_16x16x32_bf16 v[24:27],a[192:195],v[40:43],v[24:27]
s_waitcnt vmcnt(9)
ds_read_b128 v[68:71],v2 offset:31744
v_cvt_pk_bf16_f32 v81,v34,v35
v_mfma_f32_16x16x32_bf16 v[28:31],a[196:199],v[40:43],v[28:31]
s_barrier
v_mfma_f32_16x16x32_bf16 v[24:27],a[200:203],v[44:47],v[24:27]
ds_read_b128 v[40:43],v2 offset:32768
ds_read_b64 v[92:93],v11 offset:0
v_mfma_f32_16x16x32_bf16 v[28:31],a[204:207],v[44:47],v[28:31]
v_fma_f32 v36,-v96,v20,v36
v_fma_f32 v37,-v96,v21,v37
v_fma_f32 v38,-v96,v22,v38
v_fma_f32 v39,-v96,v23,v39
v_mul_f32 v76,v36,v36
v_mul_f32 v77,v37,v37
v_mul_f32 v78,v38,v38
v_mul_f32 v79,v39,v39
v_fma_f32 v76,v98,v76,v97
v_fma_f32 v77,v98,v77,v97
v_fma_f32 v78,v98,v78,v97
v_fma_f32 v79,v98,v79,v97
v_mul_f32 v76,v36,v76
v_mul_f32 v77,v37,v77
v_mul_f32 v78,v38,v78
v_mul_f32 v79,v39,v79
v_exp_f32 v76,v76
v_exp_f32 v77,v77
v_exp_f32 v78,v78
v_exp_f32 v79,v79
v_mfma_f32_16x16x32_bf16 v[24:27],a[208:211],v[48:51],v[24:27]
v_fma_f32 v76,v76,v99,v99
v_fma_f32 v77,v77,v99,v99
v_fma_f32 v78,v78,v99,v99
v_fma_f32 v79,v79,v99,v99
v_rcp_f32 v76,v76
v_rcp_f32 v77,v77
v_rcp_f32 v78,v78
v_rcp_f32 v79,v79
v_mfma_f32_16x16x32_bf16 v[28:31],a[212:215],v[48:51],v[28:31]
ds_read_b128 v[44:47],v2 offset:33792
v_mul_f32 v36,v36,v76
v_mul_f32 v37,v37,v77
v_mul_f32 v38,v38,v78
v_mul_f32 v39,v39,v79
v_cvt_pk_bf16_f32 v82,v36,v37
v_cvt_pk_bf16_f32 v83,v38,v39
v_mfma_f32_16x16x32_bf16 v[24:27],a[216:219],v[52:55],v[24:27]
s_waitcnt lgkmcnt(1)
buffer_store_dwordx4 v[80:83],v3,s[36:39],s73 offen offset:0 sc1
ds_read_b128 v[48:51],v2 offset:34816
v_mul_f32 v96,s75,v92
v_fma_f32 v93,v93,s75,v12
v_mfma_f32_16x16x32_bf16 v[28:31],a[220:223],v[52:55],v[28:31]
v_fma_f32 v93,-v96,v96,v93
v_max_f32 v93,v93,v12
v_mfma_f32_16x16x32_bf16 v[24:27],a[224:227],v[56:59],v[24:27]
ds_read_b128 v[52:55],v2 offset:35840
v_mfma_f32_16x16x32_bf16 v[28:31],a[228:231],v[56:59],v[28:31]
v_rsq_f32 v92,v93
v_mfma_f32_16x16x32_bf16 v[24:27],a[232:235],v[60:63],v[24:27]
ds_read_b128 v[56:59],v2 offset:36864
v_mul_f32 v97,s76,v92
v_mfma_f32_16x16x32_bf16 v[28:31],a[236:239],v[60:63],v[28:31]
v_mul_f32 v98,v92,v92
v_mfma_f32_16x16x32_bf16 v[24:27],a[240:243],v[64:67],v[24:27]
ds_read_b128 v[60:63],v2 offset:37888
v_mfma_f32_16x16x32_bf16 v[28:31],a[244:247],v[64:67],v[28:31]
v_mfma_f32_16x16x32_bf16 v[24:27],a[248:251],v[68:71],v[24:27]
s_waitcnt lgkmcnt(2)
ds_read_b128 v[64:67],v2 offset:38912
v_mfma_f32_16x16x32_bf16 v[28:31],a[252:255],v[68:71],v[28:31]
s_cbranch_scc1 Lg3o_slow1_%=
Lg3o_d1w2_%=:
v_mfma_f32_16x16x32_bf16 v[32:35],a[0:3],v[40:43],0
s_add_u32 m0,s62,16384
ds_read_b128 v[68:71],v2 offset:39936
v_mul_f32 v99,v93,v92
v_mfma_f32_16x16x32_bf16 v[36:39],a[4:7],v[40:43],0
s_mul_i32 s73,s48,7
v_mul_f32 v98,s77,v98
v_mul_f32 v98,v98,v97
v_mfma_f32_16x16x32_bf16 v[32:35],a[8:11],v[44:47],v[32:35]
s_lshr_b32 s73,s73,1
ds_read_b128 v[40:43],v2 offset:40960
s_lshr_b32 s72,s67,5
v_mfma_f32_16x16x32_bf16 v[36:39],a[12:15],v[44:47],v[36:39]
buffer_load_dwordx4 v1,s[36:39],s67 offen offset:0 sc1 lds
v_mfma_f32_16x16x32_bf16 v[32:35],a[16:19],v[48:51],v[32:35]
s_lshr_b32 s71,s62,2
ds_read_b128 v[44:47],v2 offset:41984
v_fma_f32 v24,-v96,v16,v24
v_mfma_f32_16x16x32_bf16 v[36:39],a[20:23],v[48:51],v[36:39]
s_mov_b64 s[44:45],s[46:47]
v_fma_f32 v25,-v96,v17,v25
v_fma_f32 v26,-v96,v18,v26
v_mfma_f32_16x16x32_bf16 v[32:35],a[24:27],v[52:55],v[32:35]
s_mov_b64 s[46:47],s[48:49]
ds_read_b128 v[48:51],v2 offset:43008
v_fma_f32 v27,-v96,v19,v27
v_mfma_f32_16x16x32_bf16 v[36:39],a[28:31],v[52:55],v[36:39]
s_waitcnt lgkmcnt(3)
buffer_load_dwordx4 v1,s[36:39],s67 offen offset:1024 sc1 lds
v_mfma_f32_16x16x32_bf16 v[32:35],a[32:35],v[56:59],v[32:35]
s_mov_b64 s[48:49],s[50:51]
ds_read_b128 v[52:55],v2 offset:44032
v_mul_f32 v72,v24,v24
v_mfma_f32_16x16x32_bf16 v[36:39],a[36:39],v[56:59],v[36:39]
v_mul_f32 v73,v25,v25
v_mul_f32 v74,v26,v26
v_mfma_f32_16x16x32_bf16 v[32:35],a[40:43],v[60:63],v[32:35]
ds_read_b128 v[56:59],v2 offset:45056
v_mul_f32 v75,v27,v27
v_mfma_f32_16x16x32_bf16 v[36:39],a[44:47],v[60:63],v[36:39]
buffer_load_dwordx4 v1,s[36:39],s67 offen offset:2048 sc1 lds
v_mfma_f32_16x16x32_bf16 v[32:35],a[48:51],v[64:67],v[32:35]
ds_read_b128 v[60:63],v2 offset:46080
v_fma_f32 v72,v98,v72,v97
v_mfma_f32_16x16x32_bf16 v[36:39],a[52:55],v[64:67],v[36:39]
v_mul_f32 v72,v24,v72
v_fma_f32 v73,v98,v73,v97
v_mfma_f32_16x16x32_bf16 v[32:35],a[56:59],v[68:71],v[32:35]
ds_read_b128 v[64:67],v2 offset:47104
v_mul_f32 v73,v25,v73
v_mfma_f32_16x16x32_bf16 v[36:39],a[60:63],v[68:71],v[36:39]
s_waitcnt lgkmcnt(3)
buffer_load_dwordx4 v1,s[36:39],s67 offen offset:3072 sc1 lds
s_add_u32 m0,s71,s88
v_mfma_f32_16x16x32_bf16 v[32:35],a[64:67],v[40:43],v[32:35]
s_xor_b32 s87,s63,65536
ds_read_b128 v[68:71],v2 offset:48128
buffer_load_dword v9,s[36:39],s72 offen offset:0 sc1 lds
s_waitcnt vmcnt(10)
v_mfma_f32_16x16x32_bf16 v[36:39],a[68:71],v[40:43],v[36:39]
s_barrier
v_mfma_f32_16x16x32_bf16 v[32:35],a[72:75],v[44:47],v[32:35]
s_cmp_ge_u32 s67,s59
ds_read_b128 v[40:43],v2 offset:49152
ds_read_b32 v88,v8 offset:8192
v_mfma_f32_16x16x32_bf16 v[36:39],a[76:79],v[44:47],v[36:39]
s_cselect_b32 s86,s50,s51
v_exp_f32 v72,v72
v_mfma_f32_16x16x32_bf16 v[32:35],a[80:83],v[48:51],v[32:35]
s_cselect_b32 s87,s87,s64
ds_read_b128 v[44:47],v2 offset:50176
v_fma_f32 v72,v72,v99,v99
v_mfma_f32_16x16x32_bf16 v[36:39],a[84:87],v[48:51],v[36:39]
s_mov_b32 s62,s87
v_exp_f32 v73,v73
v_mfma_f32_16x16x32_bf16 v[32:35],a[88:91],v[52:55],v[32:35]
s_mov_b32 m0,s62
ds_read_b128 v[48:51],v2 offset:51200
v_fma_f32 v73,v73,v99,v99
v_mfma_f32_16x16x32_bf16 v[36:39],a[92:95],v[52:55],v[36:39]
s_waitcnt lgkmcnt(4)
v_rcp_f32 v72,v72
v_mfma_f32_16x16x32_bf16 v[32:35],a[96:99],v[56:59],v[32:35]
s_nop 0
s_waitcnt lgkmcnt(2)
ds_read_b128 v[52:55],v2 offset:52224
v_fma_f32 v74,v98,v74,v97
v_mfma_f32_16x16x32_bf16 v[36:39],a[100:103],v[56:59],v[36:39]
s_barrier
v_mfma_f32_16x16x32_bf16 v[32:35],a[104:107],v[60:63],v[32:35]
s_mov_b64 s[50:51],s[52:53]
ds_read_b128 v[56:59],v2 offset:53248
ds_read_b32 v13,v7 offset:256
v_mfma_f32_16x16x32_bf16 v[36:39],a[108:111],v[60:63],v[36:39]
v_rcp_f32 v73,v73
v_mfma_f32_16x16x32_bf16 v[32:35],a[112:115],v[64:67],v[32:35]
ds_read_b128 v[60:63],v2 offset:54272
v_mul_f32 v74,v26,v74
v_mfma_f32_16x16x32_bf16 v[36:39],a[116:119],v[64:67],v[36:39])"
R"(
v_exp_f32 v74,v74
v_mfma_f32_16x16x32_bf16 v[32:35],a[120:123],v[68:71],v[32:35]
s_waitcnt lgkmcnt(1)
ds_read_b128 v[64:67],v2 offset:55296
v_fma_f32 v74,v74,v99,v99
v_mfma_f32_16x16x32_bf16 v[36:39],a[124:127],v[68:71],v[36:39]
v_cmp_eq_u32 vcc,s60,v13
v_rcp_f32 v74,v74
v_add_f32_dpp v88,v88,v88 quad_perm:[1,0,3,2] row_mask:0xf bank_mask:0xf
v_mfma_f32_16x16x32_bf16 v[32:35],a[128:131],v[40:43],v[32:35]
s_cmp_eq_u32 vcc_lo,-1
ds_read_b128 v[68:71],v2 offset:56320
v_add_f32_dpp v88,v88,v88 quad_perm:[2,3,0,1] row_mask:0xf bank_mask:0xf
v_mfma_f32_16x16x32_bf16 v[36:39],a[132:135],v[40:43],v[36:39]
s_cselect_b32 s66,s86,s59
v_add_f32_dpp v88,v88,v88 row_half_mirror row_mask:0xf bank_mask:0xf
v_fma_f32 v75,v98,v75,v97
v_mfma_f32_16x16x32_bf16 v[32:35],a[136:139],v[44:47],v[32:35]
buffer_load_dwordx4 v0,s[36:39],s66 offen offset:0 sc1 lds
v_mfma_f32_16x16x32_bf16 v[36:39],a[140:143],v[44:47],v[36:39]
s_xor_b32 s64,s64,65536
ds_read_b128 v[40:43],v2 offset:57344
v_mul_f32 v75,v27,v75
v_mfma_f32_16x16x32_bf16 v[32:35],a[144:147],v[48:51],v[32:35]
s_add_u32 s74,s53,s55
ds_write_b32 v10,v88 offset:128
v_mfma_f32_16x16x32_bf16 v[36:39],a[148:151],v[48:51],v[36:39]
s_cmp_eq_u32 s74,s56
ds_read_b128 v[44:47],v2 offset:58368
v_mul_f32 v24,v24,v72
v_mfma_f32_16x16x32_bf16 v[32:35],a[152:155],v[52:55],v[32:35]
buffer_load_dwordx4 v0,s[36:39],s66 offen offset:1024 sc1 lds
v_mfma_f32_16x16x32_bf16 v[36:39],a[156:159],v[52:55],v[36:39]
s_waitcnt lgkmcnt(3)
ds_read_b128 v[48:51],v2 offset:59392
v_mul_f32 v25,v25,v73
v_mfma_f32_16x16x32_bf16 v[32:35],a[160:163],v[56:59],v[32:35]
s_cselect_b32 s74,s57,s74
ds_read_b128 v[52:55],v2 offset:60416
v_mul_f32 v26,v26,v74
v_mfma_f32_16x16x32_bf16 v[36:39],a[164:167],v[56:59],v[36:39]
s_cmp_eq_u32 s74,s58
v_exp_f32 v75,v75
v_mfma_f32_16x16x32_bf16 v[32:35],a[168:171],v[60:63],v[32:35]
buffer_load_dwordx4 v0,s[36:39],s66 offen offset:2048 sc1 lds
v_mfma_f32_16x16x32_bf16 v[36:39],a[172:175],v[60:63],v[36:39]
s_cselect_b32 s74,s59,s74
ds_read_b128 v[56:59],v2 offset:61440
v_fma_f32 v75,v75,v99,v99
v_mfma_f32_16x16x32_bf16 v[32:35],a[176:179],v[64:67],v[32:35]
s_mov_b32 s52,s74
ds_read_b128 v[60:63],v2 offset:62464
v_cvt_pk_bf16_f32 v80,v24,v25
v_mfma_f32_16x16x32_bf16 v[36:39],a[180:183],v[64:67],v[36:39]
s_cmp_lt_u32 s67,s59
v_rcp_f32 v75,v75
v_mfma_f32_16x16x32_bf16 v[32:35],a[184:187],v[68:71],v[32:35]
buffer_load_dwordx4 v0,s[36:39],s66 offen offset:3072 sc1 lds
v_mfma_f32_16x16x32_bf16 v[36:39],a[188:191],v[68:71],v[36:39]
s_waitcnt lgkmcnt(2)
ds_read_b128 v[64:67],v2 offset:63488
v_mul_f32 v27,v27,v75
v_mfma_f32_16x16x32_bf16 v[32:35],a[192:195],v[40:43],v[32:35]
s_waitcnt vmcnt(9)
ds_read_b128 v[68:71],v2 offset:64512
v_xor_b32 v2,65536,v2
v_mfma_f32_16x16x32_bf16 v[36:39],a[196:199],v[40:43],v[36:39]
s_barrier
v_mfma_f32_16x16x32_bf16 v[32:35],a[200:203],v[44:47],v[32:35]
ds_read_b128 v[40:43],v2 offset:0
ds_read_b64 v[92:93],v11 offset:128
v_mfma_f32_16x16x32_bf16 v[36:39],a[204:207],v[44:47],v[36:39]
v_fma_f32 v28,-v96,v20,v28
v_fma_f32 v29,-v96,v21,v29
v_fma_f32 v30,-v96,v22,v30
v_fma_f32 v31,-v96,v23,v31
v_mul_f32 v76,v28,v28
v_mul_f32 v77,v29,v29
v_mul_f32 v78,v30,v30
v_mul_f32 v79,v31,v31
v_fma_f32 v76,v98,v76,v97
v_fma_f32 v77,v98,v77,v97
v_fma_f32 v78,v98,v78,v97
v_fma_f32 v79,v98,v79,v97
v_mul_f32 v76,v28,v76
v_mul_f32 v77,v29,v77
v_mul_f32 v78,v30,v78
v_mul_f32 v79,v31,v79
v_exp_f32 v76,v76
v_exp_f32 v77,v77
v_exp_f32 v78,v78
v_exp_f32 v79,v79
v_mfma_f32_16x16x32_bf16 v[32:35],a[208:211],v[48:51],v[32:35]
v_fma_f32 v76,v76,v99,v99
v_fma_f32 v77,v77,v99,v99
v_fma_f32 v78,v78,v99,v99
v_fma_f32 v79,v79,v99,v99
v_rcp_f32 v76,v76
v_rcp_f32 v77,v77
v_rcp_f32 v78,v78
v_rcp_f32 v79,v79
v_mfma_f32_16x16x32_bf16 v[36:39],a[212:215],v[48:51],v[36:39]
ds_read_b128 v[44:47],v2 offset:1024
v_cvt_pk_bf16_f32 v81,v26,v27
v_mul_f32 v28,v28,v76
v_mul_f32 v29,v29,v77
v_mul_f32 v30,v30,v78
v_mul_f32 v31,v31,v79
v_cvt_pk_bf16_f32 v82,v28,v29
v_cvt_pk_bf16_f32 v83,v30,v31
v_mfma_f32_16x16x32_bf16 v[32:35],a[216:219],v[52:55],v[32:35]
s_waitcnt lgkmcnt(1)
buffer_store_dwordx4 v[80:83],v3,s[36:39],s73 offen offset:0 sc1
ds_read_b128 v[48:51],v2 offset:2048
v_xor_b32 v8,16384,v8
v_mul_f32 v96,s75,v92
v_mfma_f32_16x16x32_bf16 v[36:39],a[220:223],v[52:55],v[36:39]
v_fma_f32 v93,v93,s75,v12
v_fma_f32 v93,-v96,v96,v93
v_mfma_f32_16x16x32_bf16 v[32:35],a[224:227],v[56:59],v[32:35]
ds_read_b128 v[52:55],v2 offset:3072
v_max_f32 v93,v93,v12
v_mfma_f32_16x16x32_bf16 v[36:39],a[228:231],v[56:59],v[36:39]
v_rsq_f32 v92,v93
v_mfma_f32_16x16x32_bf16 v[32:35],a[232:235],v[60:63],v[32:35]
ds_read_b128 v[56:59],v2 offset:4096
v_mul_f32 v97,s76,v92
v_mfma_f32_16x16x32_bf16 v[36:39],a[236:239],v[60:63],v[36:39]
v_mul_f32 v98,v92,v92
v_mfma_f32_16x16x32_bf16 v[32:35],a[240:243],v[64:67],v[32:35]
ds_read_b128 v[60:63],v2 offset:5120
v_mfma_f32_16x16x32_bf16 v[36:39],a[244:247],v[64:67],v[36:39]
v_mfma_f32_16x16x32_bf16 v[32:35],a[248:251],v[68:71],v[32:35]
s_waitcnt lgkmcnt(2)
ds_read_b128 v[64:67],v2 offset:6144
v_mfma_f32_16x16x32_bf16 v[36:39],a[252:255],v[68:71],v[36:39]
s_cbranch_scc1 Lg3o_d0w2_%=
s_branch Lg3o_slow0_%=
Lg3o_d0w3_%=:
v_mfma_f32_16x16x32_bf16 v[24:27],a[0:3],v[40:43],0
s_add_u32 m0,s62,16384
ds_read_b128 v[68:71],v2 offset:7168
v_mul_f32 v99,v93,v92
v_mfma_f32_16x16x32_bf16 v[28:31],a[4:7],v[40:43],0
s_mul_i32 s73,s47,7
v_mul_f32 v98,s77,v98
v_mul_f32 v98,v98,v97
v_mfma_f32_16x16x32_bf16 v[24:27],a[8:11],v[44:47],v[24:27]
s_lshr_b32 s73,s73,1
ds_read_b128 v[40:43],v2 offset:8192
s_lshr_b32 s72,s66,5
v_mfma_f32_16x16x32_bf16 v[28:31],a[12:15],v[44:47],v[28:31]
s_lshr_b32 s71,s62,2
v_fma_f32 v32,-v96,v16,v32
v_fma_f32 v33,-v96,v17,v33
v_mfma_f32_16x16x32_bf16 v[24:27],a[16:19],v[48:51],v[24:27]
buffer_load_dwordx4 v1,s[36:39],s66 offen offset:0 sc1 lds
v_mfma_f32_16x16x32_bf16 v[28:31],a[20:23],v[48:51],v[28:31]
s_mul_i32 s70,s46,5
ds_read_b128 v[44:47],v2 offset:9216
v_fma_f32 v34,-v96,v18,v34
v_mfma_f32_16x16x32_bf16 v[24:27],a[24:27],v[52:55],v[24:27]
ds_read_b128 v[48:51],v2 offset:10240
v_fma_f32 v35,-v96,v19,v35
v_mfma_f32_16x16x32_bf16 v[28:31],a[28:31],v[52:55],v[28:31]
s_waitcnt lgkmcnt(3)
v_mul_f32 v72,v32,v32
v_mul_f32 v73,v33,v33
v_mfma_f32_16x16x32_bf16 v[24:27],a[32:35],v[56:59],v[24:27]
buffer_load_dwordx4 v1,s[36:39],s66 offen offset:1024 sc1 lds
v_mfma_f32_16x16x32_bf16 v[28:31],a[36:39],v[56:59],v[28:31]
ds_read_b128 v[52:55],v2 offset:11264
v_mul_f32 v74,v34,v34
v_mfma_f32_16x16x32_bf16 v[24:27],a[40:43],v[60:63],v[24:27]
ds_read_b128 v[56:59],v2 offset:12288
v_mul_f32 v75,v35,v35
v_mfma_f32_16x16x32_bf16 v[28:31],a[44:47],v[60:63],v[28:31]
v_fma_f32 v72,v98,v72,v97
v_mul_f32 v72,v32,v72
v_mfma_f32_16x16x32_bf16 v[24:27],a[48:51],v[64:67],v[24:27]
buffer_load_dwordx4 v1,s[36:39],s66 offen offset:2048 sc1 lds
v_mfma_f32_16x16x32_bf16 v[28:31],a[52:55],v[64:67],v[28:31]
ds_read_b128 v[60:63],v2 offset:13312
v_fma_f32 v73,v98,v73,v97
v_mfma_f32_16x16x32_bf16 v[24:27],a[56:59],v[68:71],v[24:27]
ds_read_b128 v[64:67],v2 offset:14336
v_mul_f32 v73,v33,v73
v_mfma_f32_16x16x32_bf16 v[28:31],a[60:63],v[68:71],v[28:31]
s_waitcnt lgkmcnt(3)
v_exp_f32 v72,v72
v_mfma_f32_16x16x32_bf16 v[24:27],a[64:67],v[40:43],v[24:27]
s_waitcnt vmcnt(9)
ds_read_b128 v[68:71],v2 offset:15360
v_fma_f32 v72,v72,v99,v99
v_mfma_f32_16x16x32_bf16 v[28:31],a[68:71],v[40:43],v[28:31]
s_barrier
v_mfma_f32_16x16x32_bf16 v[24:27],a[72:75],v[44:47],v[24:27]
buffer_load_dwordx4 v1,s[36:39],s66 offen offset:3072 sc1 lds
v_mfma_f32_16x16x32_bf16 v[28:31],a[76:79],v[44:47],v[28:31]
s_add_u32 m0,s71,s88)"
R"(
ds_read_b128 v[40:43],v2 offset:16384
ds_read_b32 v88,v8 offset:0
v_mfma_f32_16x16x32_bf16 v[24:27],a[80:83],v[48:51],v[24:27]
buffer_load_dword v9,s[36:39],s72 offen offset:0 sc1 lds
v_mfma_f32_16x16x32_bf16 v[28:31],a[84:87],v[48:51],v[28:31]
s_xor_b32 s87,s64,65536
ds_read_b128 v[44:47],v2 offset:17408
v_fma_f32 v74,v98,v74,v97
v_mfma_f32_16x16x32_bf16 v[24:27],a[88:91],v[52:55],v[24:27]
s_cmp_ge_u32 s66,s59
ds_read_b128 v[48:51],v2 offset:18432
v_fma_f32 v75,v98,v75,v97
v_mfma_f32_16x16x32_bf16 v[28:31],a[92:95],v[52:55],v[28:31]
s_waitcnt lgkmcnt(4)
v_exp_f32 v73,v73
v_mfma_f32_16x16x32_bf16 v[24:27],a[96:99],v[56:59],v[24:27]
s_nop 0
ds_read_b128 v[52:55],v2 offset:19456
v_mul_f32 v74,v34,v74
v_mfma_f32_16x16x32_bf16 v[28:31],a[100:103],v[56:59],v[28:31]
s_barrier
v_mfma_f32_16x16x32_bf16 v[24:27],a[104:107],v[60:63],v[24:27]
s_waitcnt lgkmcnt(3)
ds_read_b128 v[56:59],v2 offset:20480
ds_read_b32 v13,v7 offset:0
v_mfma_f32_16x16x32_bf16 v[28:31],a[108:111],v[60:63],v[28:31]
s_cselect_b32 s86,s49,s50
v_exp_f32 v74,v74
v_mfma_f32_16x16x32_bf16 v[24:27],a[112:115],v[64:67],v[24:27]
s_cselect_b32 s87,s87,s63
ds_read_b128 v[60:63],v2 offset:21504
v_mul_f32 v75,v35,v75
v_mfma_f32_16x16x32_bf16 v[28:31],a[116:119],v[64:67],v[28:31]
s_mov_b32 s62,s87
v_exp_f32 v75,v75
v_mfma_f32_16x16x32_bf16 v[24:27],a[120:123],v[68:71],v[24:27]
s_waitcnt lgkmcnt(1)
ds_read_b128 v[64:67],v2 offset:22528
v_fma_f32 v73,v73,v99,v99
v_mfma_f32_16x16x32_bf16 v[28:31],a[124:127],v[68:71],v[28:31]
v_cmp_eq_u32 vcc,s60,v13
v_rcp_f32 v72,v72
v_fma_f32 v74,v74,v99,v99
v_mfma_f32_16x16x32_bf16 v[24:27],a[128:131],v[40:43],v[24:27]
s_cmp_eq_u32 vcc_lo,-1
ds_read_b128 v[68:71],v2 offset:23552
v_add_f32_dpp v88,v88,v88 quad_perm:[1,0,3,2] row_mask:0xf bank_mask:0xf
v_mfma_f32_16x16x32_bf16 v[28:31],a[132:135],v[40:43],v[28:31]
s_cselect_b32 s67,s86,s59
v_rcp_f32 v73,v73
v_mfma_f32_16x16x32_bf16 v[24:27],a[136:139],v[44:47],v[24:27]
s_mov_b32 m0,s62
ds_read_b128 v[40:43],v2 offset:24576
v_fma_f32 v75,v75,v99,v99
v_mfma_f32_16x16x32_bf16 v[28:31],a[140:143],v[44:47],v[28:31]
buffer_load_dwordx4 v0,s[36:39],s67 offen offset:0 sc1 lds
v_mfma_f32_16x16x32_bf16 v[24:27],a[144:147],v[48:51],v[24:27]
s_xor_b32 s63,s63,65536
ds_read_b128 v[44:47],v2 offset:25600
v_add_f32_dpp v88,v88,v88 quad_perm:[2,3,0,1] row_mask:0xf bank_mask:0xf
v_mfma_f32_16x16x32_bf16 v[28:31],a[148:151],v[48:51],v[28:31]
s_add_u32 s53,s52,s55
v_rcp_f32 v74,v74
v_mfma_f32_16x16x32_bf16 v[24:27],a[152:155],v[52:55],v[24:27]
s_cmp_eq_u32 s53,s56
ds_read_b128 v[48:51],v2 offset:26624
v_add_f32_dpp v88,v88,v88 row_half_mirror row_mask:0xf bank_mask:0xf
v_mfma_f32_16x16x32_bf16 v[28:31],a[156:159],v[52:55],v[28:31]
s_waitcnt lgkmcnt(3)
buffer_load_dwordx4 v0,s[36:39],s67 offen offset:1024 sc1 lds
v_mfma_f32_16x16x32_bf16 v[24:27],a[160:163],v[56:59],v[24:27]
s_cselect_b32 s53,s57,s53
ds_write_b32 v10,v88 offset:0
v_mfma_f32_16x16x32_bf16 v[28:31],a[164:167],v[56:59],v[28:31]
s_cmp_eq_u32 s53,s58
ds_read_b128 v[52:55],v2 offset:27648
v_mul_f32 v32,v32,v72
v_mfma_f32_16x16x32_bf16 v[24:27],a[168:171],v[60:63],v[24:27]
s_cselect_b32 s53,s59,s53
ds_read_b128 v[56:59],v2 offset:28672
v_mul_f32 v33,v33,v73
v_mfma_f32_16x16x32_bf16 v[28:31],a[172:175],v[60:63],v[28:31]
buffer_load_dwordx4 v0,s[36:39],s67 offen offset:2048 sc1 lds
v_mfma_f32_16x16x32_bf16 v[24:27],a[176:179],v[64:67],v[24:27]
s_lshr_b32 s70,s70,6
ds_read_b128 v[60:63],v2 offset:29696
v_mul_f32 v34,v34,v74
v_mfma_f32_16x16x32_bf16 v[28:31],a[180:183],v[64:67],v[28:31]
s_cmp_ge_u32 s66,s59
v_rcp_f32 v75,v75
v_mfma_f32_16x16x32_bf16 v[24:27],a[184:187],v[68:71],v[24:27]
ds_read_b128 v[64:67],v2 offset:30720
v_mul_f32 v35,v35,v75
v_mfma_f32_16x16x32_bf16 v[28:31],a[188:191],v[68:71],v[28:31]
s_waitcnt lgkmcnt(3)
buffer_load_dwordx4 v0,s[36:39],s67 offen offset:3072 sc1 lds
v_mfma_f32_16x16x32_bf16 v[24:27],a[192:195],v[40:43],v[24:27]
s_waitcnt vmcnt(9)
ds_read_b128 v[68:71],v2 offset:31744
v_cvt_pk_bf16_f32 v80,v32,v33
v_mfma_f32_16x16x32_bf16 v[28:31],a[196:199],v[40:43],v[28:31]
s_barrier
v_mfma_f32_16x16x32_bf16 v[24:27],a[200:203],v[44:47],v[24:27]
buffer_store_dword v5,v4,s[36:39],s70 offen offset:0 sc1
ds_read_b128 v[40:43],v2 offset:32768
ds_read_b64 v[92:93],v11 offset:0
v_cvt_pk_bf16_f32 v81,v34,v35
v_mfma_f32_16x16x32_bf16 v[28:31],a[204:207],v[44:47],v[28:31]
v_fma_f32 v36,-v96,v20,v36
v_fma_f32 v37,-v96,v21,v37
v_mfma_f32_16x16x32_bf16 v[24:27],a[208:211],v[48:51],v[24:27]
v_fma_f32 v38,-v96,v22,v38
v_fma_f32 v39,-v96,v23,v39
v_mul_f32 v76,v36,v36
v_mul_f32 v77,v37,v37
v_mul_f32 v78,v38,v38
v_mul_f32 v79,v39,v39
v_fma_f32 v76,v98,v76,v97
v_fma_f32 v77,v98,v77,v97
v_fma_f32 v78,v98,v78,v97
v_fma_f32 v79,v98,v79,v97
v_mul_f32 v76,v36,v76
v_mul_f32 v77,v37,v77
v_mul_f32 v78,v38,v78
v_mul_f32 v79,v39,v79
v_exp_f32 v76,v76
v_exp_f32 v77,v77
v_exp_f32 v78,v78
v_exp_f32 v79,v79
v_mfma_f32_16x16x32_bf16 v[28:31],a[212:215],v[48:51],v[28:31]
ds_read_b128 v[44:47],v2 offset:33792
v_fma_f32 v76,v76,v99,v99
v_fma_f32 v77,v77,v99,v99
v_fma_f32 v78,v78,v99,v99
v_fma_f32 v79,v79,v99,v99
v_rcp_f32 v76,v76
v_rcp_f32 v77,v77
v_rcp_f32 v78,v78
v_rcp_f32 v79,v79
v_mfma_f32_16x16x32_bf16 v[24:27],a[216:219],v[52:55],v[24:27]
s_waitcnt lgkmcnt(1)
v_mul_f32 v36,v36,v76
v_mul_f32 v37,v37,v77
v_mul_f32 v38,v38,v78
v_mul_f32 v39,v39,v79
v_cvt_pk_bf16_f32 v82,v36,v37
v_cvt_pk_bf16_f32 v83,v38,v39
v_mfma_f32_16x16x32_bf16 v[28:31],a[220:223],v[52:55],v[28:31]
ds_read_b128 v[48:51],v2 offset:34816
buffer_store_dwordx4 v[80:83],v3,s[36:39],s73 offen offset:0 sc1
v_mul_f32 v96,s75,v92
v_fma_f32 v93,v93,s75,v12
v_fma_f32 v93,-v96,v96,v93
v_mfma_f32_16x16x32_bf16 v[24:27],a[224:227],v[56:59],v[24:27]
ds_read_b128 v[52:55],v2 offset:35840
v_max_f32 v93,v93,v12
v_mfma_f32_16x16x32_bf16 v[28:31],a[228:231],v[56:59],v[28:31]
v_rsq_f32 v92,v93
v_mfma_f32_16x16x32_bf16 v[24:27],a[232:235],v[60:63],v[24:27]
ds_read_b128 v[56:59],v2 offset:36864
v_mul_f32 v97,s76,v92
v_mfma_f32_16x16x32_bf16 v[28:31],a[236:239],v[60:63],v[28:31]
v_mul_f32 v98,v92,v92
v_mfma_f32_16x16x32_bf16 v[24:27],a[240:243],v[64:67],v[24:27]
ds_read_b128 v[60:63],v2 offset:37888
v_mfma_f32_16x16x32_bf16 v[28:31],a[244:247],v[64:67],v[28:31]
v_mfma_f32_16x16x32_bf16 v[24:27],a[248:251],v[68:71],v[24:27]
s_waitcnt lgkmcnt(2)
ds_read_b128 v[64:67],v2 offset:38912
v_mfma_f32_16x16x32_bf16 v[28:31],a[252:255],v[68:71],v[28:31]
s_cbranch_scc1 Lg3o_slow1_%=
Lg3o_d1w3_%=:
v_mfma_f32_16x16x32_bf16 v[32:35],a[0:3],v[40:43],0
s_add_u32 m0,s62,16384
ds_read_b128 v[68:71],v2 offset:39936
v_mul_f32 v99,v93,v92
v_mfma_f32_16x16x32_bf16 v[36:39],a[4:7],v[40:43],0
s_mul_i32 s73,s48,7
v_mul_f32 v98,s77,v98
v_mul_f32 v98,v98,v97
v_mfma_f32_16x16x32_bf16 v[32:35],a[8:11],v[44:47],v[32:35]
s_lshr_b32 s73,s73,1
ds_read_b128 v[40:43],v2 offset:40960
s_lshr_b32 s72,s67,5
v_mfma_f32_16x16x32_bf16 v[36:39],a[12:15],v[44:47],v[36:39]
s_lshr_b32 s71,s62,2
v_fma_f32 v24,-v96,v16,v24
v_fma_f32 v25,-v96,v17,v25
v_mfma_f32_16x16x32_bf16 v[32:35],a[16:19],v[48:51],v[32:35]
buffer_load_dwordx4 v1,s[36:39],s67 offen offset:0 sc1 lds
v_mfma_f32_16x16x32_bf16 v[36:39],a[20:23],v[48:51],v[36:39]
s_mul_i32 s70,s47,5
ds_read_b128 v[44:47],v2 offset:41984
v_fma_f32 v26,-v96,v18,v26
v_mfma_f32_16x16x32_bf16 v[32:35],a[24:27],v[52:55],v[32:35]
s_mov_b64 s[44:45],s[46:47]
ds_read_b128 v[48:51],v2 offset:43008
v_fma_f32 v27,-v96,v19,v27
v_mfma_f32_16x16x32_bf16 v[36:39],a[28:31],v[52:55],v[36:39]
s_waitcnt lgkmcnt(3))"
R"(
v_mul_f32 v72,v24,v24
v_mul_f32 v73,v25,v25
v_mfma_f32_16x16x32_bf16 v[32:35],a[32:35],v[56:59],v[32:35]
buffer_load_dwordx4 v1,s[36:39],s67 offen offset:1024 sc1 lds
v_mfma_f32_16x16x32_bf16 v[36:39],a[36:39],v[56:59],v[36:39]
s_mov_b64 s[46:47],s[48:49]
ds_read_b128 v[52:55],v2 offset:44032
v_mul_f32 v74,v26,v26
v_mfma_f32_16x16x32_bf16 v[32:35],a[40:43],v[60:63],v[32:35]
s_mov_b64 s[48:49],s[50:51]
ds_read_b128 v[56:59],v2 offset:45056
v_mul_f32 v75,v27,v27
v_mfma_f32_16x16x32_bf16 v[36:39],a[44:47],v[60:63],v[36:39]
v_fma_f32 v72,v98,v72,v97
v_mul_f32 v72,v24,v72
v_mfma_f32_16x16x32_bf16 v[32:35],a[48:51],v[64:67],v[32:35]
buffer_load_dwordx4 v1,s[36:39],s67 offen offset:2048 sc1 lds
v_mfma_f32_16x16x32_bf16 v[36:39],a[52:55],v[64:67],v[36:39]
ds_read_b128 v[60:63],v2 offset:46080
v_fma_f32 v73,v98,v73,v97
v_mfma_f32_16x16x32_bf16 v[32:35],a[56:59],v[68:71],v[32:35]
ds_read_b128 v[64:67],v2 offset:47104
v_mul_f32 v73,v25,v73
v_mfma_f32_16x16x32_bf16 v[36:39],a[60:63],v[68:71],v[36:39]
s_waitcnt lgkmcnt(3)
v_exp_f32 v72,v72
v_mfma_f32_16x16x32_bf16 v[32:35],a[64:67],v[40:43],v[32:35]
s_waitcnt vmcnt(9)
ds_read_b128 v[68:71],v2 offset:48128
v_fma_f32 v72,v72,v99,v99
v_mfma_f32_16x16x32_bf16 v[36:39],a[68:71],v[40:43],v[36:39]
s_barrier
v_mfma_f32_16x16x32_bf16 v[32:35],a[72:75],v[44:47],v[32:35]
buffer_load_dwordx4 v1,s[36:39],s67 offen offset:3072 sc1 lds
v_mfma_f32_16x16x32_bf16 v[36:39],a[76:79],v[44:47],v[36:39]
s_add_u32 m0,s71,s88
ds_read_b128 v[40:43],v2 offset:49152
ds_read_b32 v88,v8 offset:8192
v_mfma_f32_16x16x32_bf16 v[32:35],a[80:83],v[48:51],v[32:35]
buffer_load_dword v9,s[36:39],s72 offen offset:0 sc1 lds
v_mfma_f32_16x16x32_bf16 v[36:39],a[84:87],v[48:51],v[36:39]
s_xor_b32 s87,s63,65536
ds_read_b128 v[44:47],v2 offset:50176
v_fma_f32 v74,v98,v74,v97
v_mfma_f32_16x16x32_bf16 v[32:35],a[88:91],v[52:55],v[32:35]
s_cmp_ge_u32 s67,s59
ds_read_b128 v[48:51],v2 offset:51200
v_fma_f32 v75,v98,v75,v97
v_mfma_f32_16x16x32_bf16 v[36:39],a[92:95],v[52:55],v[36:39]
s_waitcnt lgkmcnt(4)
v_exp_f32 v73,v73
v_mfma_f32_16x16x32_bf16 v[32:35],a[96:99],v[56:59],v[32:35]
s_nop 0
ds_read_b128 v[52:55],v2 offset:52224
v_mul_f32 v74,v26,v74
v_mfma_f32_16x16x32_bf16 v[36:39],a[100:103],v[56:59],v[36:39]
s_barrier
v_mfma_f32_16x16x32_bf16 v[32:35],a[104:107],v[60:63],v[32:35]
s_waitcnt lgkmcnt(3)
ds_read_b128 v[56:59],v2 offset:53248
ds_read_b32 v13,v7 offset:256
v_mfma_f32_16x16x32_bf16 v[36:39],a[108:111],v[60:63],v[36:39]
s_cselect_b32 s86,s50,s51
v_exp_f32 v74,v74
v_mfma_f32_16x16x32_bf16 v[32:35],a[112:115],v[64:67],v[32:35]
s_cselect_b32 s87,s87,s64
ds_read_b128 v[60:63],v2 offset:54272
v_mul_f32 v75,v27,v75
v_mfma_f32_16x16x32_bf16 v[36:39],a[116:119],v[64:67],v[36:39]
s_mov_b32 s62,s87
v_exp_f32 v75,v75
v_mfma_f32_16x16x32_bf16 v[32:35],a[120:123],v[68:71],v[32:35]
s_waitcnt lgkmcnt(1)
ds_read_b128 v[64:67],v2 offset:55296
v_fma_f32 v73,v73,v99,v99
v_mfma_f32_16x16x32_bf16 v[36:39],a[124:127],v[68:71],v[36:39]
v_cmp_eq_u32 vcc,s60,v13
v_rcp_f32 v72,v72
v_fma_f32 v74,v74,v99,v99
v_mfma_f32_16x16x32_bf16 v[32:35],a[128:131],v[40:43],v[32:35]
s_cmp_eq_u32 vcc_lo,-1
ds_read_b128 v[68:71],v2 offset:56320
v_add_f32_dpp v88,v88,v88 quad_perm:[1,0,3,2] row_mask:0xf bank_mask:0xf
v_mfma_f32_16x16x32_bf16 v[36:39],a[132:135],v[40:43],v[36:39]
s_cselect_b32 s66,s86,s59
v_rcp_f32 v73,v73
v_mfma_f32_16x16x32_bf16 v[32:35],a[136:139],v[44:47],v[32:35]
s_mov_b32 m0,s62
ds_read_b128 v[40:43],v2 offset:57344
v_fma_f32 v75,v75,v99,v99
v_mfma_f32_16x16x32_bf16 v[36:39],a[140:143],v[44:47],v[36:39]
buffer_load_dwordx4 v0,s[36:39],s66 offen offset:0 sc1 lds
v_mfma_f32_16x16x32_bf16 v[32:35],a[144:147],v[48:51],v[32:35]
s_xor_b32 s64,s64,65536
ds_read_b128 v[44:47],v2 offset:58368
v_add_f32_dpp v88,v88,v88 quad_perm:[2,3,0,1] row_mask:0xf bank_mask:0xf
v_mfma_f32_16x16x32_bf16 v[36:39],a[148:151],v[48:51],v[36:39]
s_add_u32 s74,s53,s55
v_rcp_f32 v74,v74
v_mfma_f32_16x16x32_bf16 v[32:35],a[152:155],v[52:55],v[32:35]
s_cmp_eq_u32 s74,s56
ds_read_b128 v[48:51],v2 offset:59392
v_add_f32_dpp v88,v88,v88 row_half_mirror row_mask:0xf bank_mask:0xf
v_mfma_f32_16x16x32_bf16 v[36:39],a[156:159],v[52:55],v[36:39]
s_waitcnt lgkmcnt(3)
buffer_load_dwordx4 v0,s[36:39],s66 offen offset:1024 sc1 lds
v_mfma_f32_16x16x32_bf16 v[32:35],a[160:163],v[56:59],v[32:35]
s_cselect_b32 s74,s57,s74
ds_write_b32 v10,v88 offset:128
v_mfma_f32_16x16x32_bf16 v[36:39],a[164:167],v[56:59],v[36:39]
s_cmp_eq_u32 s74,s58
ds_read_b128 v[52:55],v2 offset:60416
v_mul_f32 v24,v24,v72
v_mfma_f32_16x16x32_bf16 v[32:35],a[168:171],v[60:63],v[32:35]
s_cselect_b32 s74,s59,s74
ds_read_b128 v[56:59],v2 offset:61440
v_mul_f32 v25,v25,v73
v_mfma_f32_16x16x32_bf16 v[36:39],a[172:175],v[60:63],v[36:39]
buffer_load_dwordx4 v0,s[36:39],s66 offen offset:2048 sc1 lds
v_mfma_f32_16x16x32_bf16 v[32:35],a[176:179],v[64:67],v[32:35]
s_lshr_b32 s70,s70,6
ds_read_b128 v[60:63],v2 offset:62464
v_mul_f32 v26,v26,v74
v_mfma_f32_16x16x32_bf16 v[36:39],a[180:183],v[64:67],v[36:39]
s_mov_b64 s[50:51],s[52:53]
v_rcp_f32 v75,v75
v_mfma_f32_16x16x32_bf16 v[32:35],a[184:187],v[68:71],v[32:35]
s_mov_b32 s52,s74
ds_read_b128 v[64:67],v2 offset:63488
v_mul_f32 v27,v27,v75
v_mfma_f32_16x16x32_bf16 v[36:39],a[188:191],v[68:71],v[36:39]
s_waitcnt lgkmcnt(3)
buffer_load_dwordx4 v0,s[36:39],s66 offen offset:3072 sc1 lds
v_mfma_f32_16x16x32_bf16 v[32:35],a[192:195],v[40:43],v[32:35]
s_waitcnt vmcnt(9)
ds_read_b128 v[68:71],v2 offset:64512
v_xor_b32 v2,65536,v2
v_mfma_f32_16x16x32_bf16 v[36:39],a[196:199],v[40:43],v[36:39]
s_barrier
v_mfma_f32_16x16x32_bf16 v[32:35],a[200:203],v[44:47],v[32:35]
buffer_store_dword v5,v4,s[36:39],s70 offen offset:0 sc1
ds_read_b128 v[40:43],v2 offset:0
ds_read_b64 v[92:93],v11 offset:128
v_cvt_pk_bf16_f32 v80,v24,v25
v_mfma_f32_16x16x32_bf16 v[36:39],a[204:207],v[44:47],v[36:39]
s_cmp_lt_u32 s67,s59
v_cvt_pk_bf16_f32 v81,v26,v27
v_fma_f32 v28,-v96,v20,v28
v_mfma_f32_16x16x32_bf16 v[32:35],a[208:211],v[48:51],v[32:35]
v_fma_f32 v29,-v96,v21,v29
v_fma_f32 v30,-v96,v22,v30
v_fma_f32 v31,-v96,v23,v31
v_mul_f32 v76,v28,v28
v_mul_f32 v77,v29,v29
v_mul_f32 v78,v30,v30
v_mul_f32 v79,v31,v31
v_fma_f32 v76,v98,v76,v97
v_fma_f32 v77,v98,v77,v97
v_fma_f32 v78,v98,v78,v97
v_fma_f32 v79,v98,v79,v97
v_mul_f32 v76,v28,v76
v_mul_f32 v77,v29,v77
v_mul_f32 v78,v30,v78
v_mul_f32 v79,v31,v79
v_exp_f32 v76,v76
v_exp_f32 v77,v77
v_exp_f32 v78,v78
v_exp_f32 v79,v79
v_mfma_f32_16x16x32_bf16 v[36:39],a[212:215],v[48:51],v[36:39]
ds_read_b128 v[44:47],v2 offset:1024
v_fma_f32 v76,v76,v99,v99
v_fma_f32 v77,v77,v99,v99
v_fma_f32 v78,v78,v99,v99
v_fma_f32 v79,v79,v99,v99
v_rcp_f32 v76,v76
v_rcp_f32 v77,v77
v_rcp_f32 v78,v78
v_rcp_f32 v79,v79
v_mfma_f32_16x16x32_bf16 v[32:35],a[216:219],v[52:55],v[32:35]
s_waitcnt lgkmcnt(1)
v_mul_f32 v28,v28,v76
v_mul_f32 v29,v29,v77
v_mul_f32 v30,v30,v78
v_mul_f32 v31,v31,v79
v_cvt_pk_bf16_f32 v82,v28,v29
v_cvt_pk_bf16_f32 v83,v30,v31
v_mfma_f32_16x16x32_bf16 v[36:39],a[220:223],v[52:55],v[36:39]
ds_read_b128 v[48:51],v2 offset:2048
buffer_store_dwordx4 v[80:83],v3,s[36:39],s73 offen offset:0 sc1
v_mul_f32 v96,s75,v92
v_xor_b32 v8,16384,v8
v_fma_f32 v93,v93,s75,v12
v_mfma_f32_16x16x32_bf16 v[32:35],a[224:227],v[56:59],v[32:35]
ds_read_b128 v[52:55],v2 offset:3072
v_fma_f32 v93,-v96,v96,v93
v_mfma_f32_16x16x32_bf16 v[36:39],a[228:231],v[56:59],v[36:39]
v_max_f32 v93,v93,v12
v_mfma_f32_16x16x32_bf16 v[32:35],a[232:235],v[60:63],v[32:35]
ds_read_b128 v[56:59],v2 offset:4096
v_mfma_f32_16x16x32_bf16 v[36:39],a[236:239],v[60:63],v[36:39])"
R"(
v_rsq_f32 v92,v93
v_mfma_f32_16x16x32_bf16 v[32:35],a[240:243],v[64:67],v[32:35]
ds_read_b128 v[60:63],v2 offset:5120
v_mul_f32 v97,s76,v92
v_mfma_f32_16x16x32_bf16 v[36:39],a[244:247],v[64:67],v[36:39]
v_mul_f32 v98,v92,v92
v_mfma_f32_16x16x32_bf16 v[32:35],a[248:251],v[68:71],v[32:35]
s_waitcnt lgkmcnt(2)
ds_read_b128 v[64:67],v2 offset:6144
v_mfma_f32_16x16x32_bf16 v[36:39],a[252:255],v[68:71],v[36:39]
s_cbranch_scc1 Lg3o_d0w3_%=
s_branch Lg3o_slow0_%=
Lg3o_slow0_%=:
s_cmp_lt_u32 s66,s59
s_cbranch_scc1 Lg3o_gs0_%=
Lg3o_slowb0_%=:
s_cmp_eq_u32 s54,1
s_cselect_b32 s68,s49,s48
s_lshr_b32 s68,s68,5
s_add_u32 m0,s78,8192
s_nop 0
buffer_load_dword v6,s[36:39],s68 offen offset:0 sc1 lds
s_mul_i32 s73,s47,7
s_lshr_b32 s73,s73,1
v_mul_f32 v99,v93,v92
v_mul_f32 v98,s77,v98
v_mul_f32 v98,v98,v97
v_fma_f32 v32,-v96,v16,v32
v_fma_f32 v33,-v96,v17,v33
v_fma_f32 v34,-v96,v18,v34
v_fma_f32 v35,-v96,v19,v35
v_mul_f32 v72,v32,v32
v_mul_f32 v73,v33,v33
v_mul_f32 v74,v34,v34
v_mul_f32 v75,v35,v35
v_fma_f32 v72,v98,v72,v97
v_fma_f32 v73,v98,v73,v97
v_fma_f32 v74,v98,v74,v97
v_fma_f32 v75,v98,v75,v97
v_mul_f32 v72,v32,v72
v_mul_f32 v73,v33,v73
v_mul_f32 v74,v34,v74
v_mul_f32 v75,v35,v75
v_exp_f32 v72,v72
v_exp_f32 v73,v73
v_exp_f32 v74,v74
v_exp_f32 v75,v75
v_fma_f32 v72,v72,v99,v99
v_fma_f32 v73,v73,v99,v99
v_fma_f32 v74,v74,v99,v99
v_fma_f32 v75,v75,v99,v99
v_rcp_f32 v72,v72
v_rcp_f32 v73,v73
v_rcp_f32 v74,v74
v_rcp_f32 v75,v75
v_mul_f32 v32,v32,v72
v_mul_f32 v33,v33,v73
v_mul_f32 v34,v34,v74
v_mul_f32 v35,v35,v75
v_cvt_pk_bf16_f32 v80,v32,v33
s_add_u32 m0,s78,8448
s_nop 0
buffer_load_dword v6,s[36:39],s68 offen offset:0 sc1 lds
v_cvt_pk_bf16_f32 v81,v34,v35
v_fma_f32 v36,-v96,v20,v36
v_fma_f32 v37,-v96,v21,v37
v_fma_f32 v38,-v96,v22,v38
v_fma_f32 v39,-v96,v23,v39
v_mul_f32 v76,v36,v36
v_mul_f32 v77,v37,v37
v_mul_f32 v78,v38,v38
v_mul_f32 v79,v39,v39
v_fma_f32 v76,v98,v76,v97
v_fma_f32 v77,v98,v77,v97
v_fma_f32 v78,v98,v78,v97
v_fma_f32 v79,v98,v79,v97
v_mul_f32 v76,v36,v76
v_mul_f32 v77,v37,v77
v_mul_f32 v78,v38,v78
v_mul_f32 v79,v39,v79
v_exp_f32 v76,v76
v_exp_f32 v77,v77
v_exp_f32 v78,v78
v_exp_f32 v79,v79
v_fma_f32 v76,v76,v99,v99
v_fma_f32 v77,v77,v99,v99
v_fma_f32 v78,v78,v99,v99
v_fma_f32 v79,v79,v99,v99
v_rcp_f32 v76,v76
v_rcp_f32 v77,v77
v_rcp_f32 v78,v78
v_rcp_f32 v79,v79
v_mul_f32 v36,v36,v76
v_mul_f32 v37,v37,v77
v_mul_f32 v38,v38,v78
v_mul_f32 v39,v39,v79
v_cvt_pk_bf16_f32 v82,v36,v37
v_cvt_pk_bf16_f32 v83,v38,v39
buffer_store_dwordx4 v[80:83],v3,s[36:39],s73 offen offset:0 sc1
s_cmp_ge_u32 s48,s59
s_cbranch_scc1 Lg3o_done0_%=
s_add_u32 m0,s78,16384
s_nop 0
buffer_load_dword v6,s[36:39],s68 offen offset:0 sc1 lds
s_waitcnt vmcnt(3)
s_barrier
ds_read_b32 v13,v7 offset:8192
s_mul_i32 s70,s46,5
s_lshr_b32 s70,s70,6
buffer_store_dword v5,v4,s[36:39],s70 offen offset:0 sc1
s_mov_b32 s46,s59
s_mov_b32 s69,18432
s_waitcnt lgkmcnt(0)
v_cmp_eq_u32 vcc,s60,v13
s_nop 0
s_cmp_eq_u32 vcc_lo,-1
s_cbranch_scc1 Lg3o_go0_%=
s_add_u32 m0,s78,16640
s_nop 0
buffer_load_dword v6,s[36:39],s68 offen offset:0 sc1 lds
s_waitcnt vmcnt(4)
s_barrier
ds_read_b32 v13,v7 offset:8448
s_mov_b32 s69,18688
s_waitcnt lgkmcnt(0)
v_cmp_eq_u32 vcc,s60,v13
s_nop 0
s_cmp_eq_u32 vcc_lo,-1
s_cbranch_scc1 Lg3o_go0_%=
s_add_u32 m0,s78,8192
s_nop 0
buffer_load_dword v6,s[36:39],s68 offen offset:0 sc1 lds
s_waitcnt vmcnt(3)
s_barrier
ds_read_b32 v13,v7 offset:16384
s_mul_i32 s70,s47,5
s_lshr_b32 s70,s70,6
buffer_store_dword v5,v4,s[36:39],s70 offen offset:0 sc1
s_mov_b32 s47,s59
s_mov_b32 s69,10240
s_waitcnt lgkmcnt(0)
v_cmp_eq_u32 vcc,s60,v13
s_nop 0
s_cmp_eq_u32 vcc_lo,-1
s_cbranch_scc1 Lg3o_go0_%=
s_add_u32 m0,s78,8448
s_nop 0
buffer_load_dword v6,s[36:39],s68 offen offset:0 sc1 lds
s_waitcnt vmcnt(3)
s_barrier
ds_read_b32 v13,v7 offset:16640
s_mov_b32 s69,10496
s_waitcnt lgkmcnt(0)
v_cmp_eq_u32 vcc,s60,v13
s_nop 0
s_cmp_eq_u32 vcc_lo,-1
s_cbranch_scc1 Lg3o_go0_%=
Lg3o_poll0_%=:
s_add_u32 m0,s78,16384
s_nop 0
buffer_load_dword v6,s[36:39],s68 offen offset:0 sc1 lds
s_waitcnt vmcnt(2)
s_barrier
ds_read_b32 v13,v7 offset:8192
s_mov_b32 s69,18432
s_waitcnt lgkmcnt(0)
v_cmp_eq_u32 vcc,s60,v13
s_nop 0
s_cmp_eq_u32 vcc_lo,-1
s_cbranch_scc1 Lg3o_go0_%=
s_sleep 4
s_add_u32 m0,s78,16640
s_nop 0
buffer_load_dword v6,s[36:39],s68 offen offset:0 sc1 lds
s_waitcnt vmcnt(2)
s_barrier
ds_read_b32 v13,v7 offset:8448
s_mov_b32 s69,18688
s_waitcnt lgkmcnt(0)
v_cmp_eq_u32 vcc,s60,v13
s_nop 0
s_cmp_eq_u32 vcc_lo,-1
s_cbranch_scc1 Lg3o_go0_%=
s_sleep 4
s_add_u32 m0,s78,8192
s_nop 0
buffer_load_dword v6,s[36:39],s68 offen offset:0 sc1 lds
s_waitcnt vmcnt(2)
s_barrier
ds_read_b32 v13,v7 offset:16384
s_mov_b32 s69,10240
s_waitcnt lgkmcnt(0)
v_cmp_eq_u32 vcc,s60,v13
s_nop 0
s_cmp_eq_u32 vcc_lo,-1
s_cbranch_scc1 Lg3o_go0_%=
s_sleep 4
s_add_u32 m0,s78,8448
s_nop 0
buffer_load_dword v6,s[36:39],s68 offen offset:0 sc1 lds)"
R"(
s_waitcnt vmcnt(2)
s_barrier
ds_read_b32 v13,v7 offset:16640
s_mov_b32 s69,10496
s_waitcnt lgkmcnt(0)
v_cmp_eq_u32 vcc,s60,v13
s_nop 0
s_cmp_eq_u32 vcc_lo,-1
s_cbranch_scc1 Lg3o_go0_%=
s_sleep 4
s_add_u32 s79,s79,1
s_cmp_lt_u32 s79,20000
s_cbranch_scc1 Lg3o_poll0_%=
Lg3o_go0_%=:
s_xor_b32 s62,s63,65536
s_mov_b32 m0,s62
s_nop 0
buffer_load_dwordx4 v0,s[36:39],s48 offen offset:0 sc1 lds
buffer_load_dwordx4 v0,s[36:39],s48 offen offset:1024 sc1 lds
buffer_load_dwordx4 v0,s[36:39],s48 offen offset:2048 sc1 lds
buffer_load_dwordx4 v0,s[36:39],s48 offen offset:3072 sc1 lds
s_add_u32 m0,s62,16384
s_nop 0
buffer_load_dwordx4 v1,s[36:39],s48 offen offset:0 sc1 lds
buffer_load_dwordx4 v1,s[36:39],s48 offen offset:1024 sc1 lds
buffer_load_dwordx4 v1,s[36:39],s48 offen offset:2048 sc1 lds
buffer_load_dwordx4 v1,s[36:39],s48 offen offset:3072 sc1 lds
s_lshr_b32 s72,s48,5
s_lshr_b32 s71,s62,2
s_add_u32 m0,s71,s88
s_nop 0
buffer_load_dword v9,s[36:39],s72 offen offset:0 sc1 lds
s_cmp_eq_u32 vcc_hi,-1
s_cselect_b32 s66,s49,s59
s_xor_b32 s62,s64,65536
s_cmp_ge_u32 s66,s59
s_cselect_b32 s68,s49,s50
s_lshr_b32 s68,s68,5
s_add_u32 m0,s78,0
s_nop 0
buffer_load_dword v6,s[36:39],s68 offen offset:0 sc1 lds
s_mov_b32 m0,s62
s_nop 0
buffer_load_dwordx4 v0,s[36:39],s66 offen offset:0 sc1 lds
buffer_load_dwordx4 v0,s[36:39],s66 offen offset:1024 sc1 lds
buffer_load_dwordx4 v0,s[36:39],s66 offen offset:2048 sc1 lds
buffer_load_dwordx4 v0,s[36:39],s66 offen offset:3072 sc1 lds
s_waitcnt vmcnt(10)
s_barrier
ds_read_b128 v[40:43],v2 offset:0
ds_read_b128 v[44:47],v2 offset:1024
ds_read_b128 v[48:51],v2 offset:2048
ds_read_b128 v[52:55],v2 offset:3072
ds_read_b128 v[56:59],v2 offset:4096
ds_read_b128 v[60:63],v2 offset:5120
ds_read_b128 v[64:67],v2 offset:6144
s_mul_i32 s70,s46,5
s_lshr_b32 s70,s70,6
buffer_store_dword v5,v4,s[36:39],s70 offen offset:0 sc1
s_mov_b32 s46,s59
s_mul_i32 s70,s47,5
s_lshr_b32 s70,s70,6
buffer_store_dword v5,v4,s[36:39],s70 offen offset:0 sc1
s_mov_b32 s47,s59
s_mul_i32 s70,s59,5
s_lshr_b32 s70,s70,6
buffer_store_dword v5,v4,s[36:39],s70 offen offset:0 sc1
s_waitcnt lgkmcnt(3)
s_setpc_b64 s[90:91]
Lg3o_gs0_%=:
s_cmp_eq_u32 s54,1
s_cselect_b32 s68,s49,s48
s_lshr_b32 s68,s68,5
s_mul_i32 s73,s47,7
s_lshr_b32 s73,s73,1
s_add_u32 m0,s78,8192
s_nop 0
buffer_load_dword v6,s[36:39],s68 offen offset:0 sc1 lds
v_mul_f32 v99,v93,v92
v_mul_f32 v98,s77,v98
v_mul_f32 v98,v98,v97
v_fma_f32 v32,-v96,v16,v32
v_fma_f32 v33,-v96,v17,v33
v_fma_f32 v34,-v96,v18,v34
v_fma_f32 v35,-v96,v19,v35
v_mul_f32 v72,v32,v32
v_mul_f32 v73,v33,v33
v_mul_f32 v74,v34,v34
v_mul_f32 v75,v35,v35
v_fma_f32 v72,v98,v72,v97
s_add_u32 m0,s62,16384
s_nop 0
buffer_load_dwordx4 v1,s[36:39],s66 offen offset:0 sc1 lds
v_fma_f32 v73,v98,v73,v97
v_fma_f32 v74,v98,v74,v97
v_fma_f32 v75,v98,v75,v97
v_mul_f32 v72,v32,v72
v_mul_f32 v73,v33,v73
v_mul_f32 v74,v34,v74
v_mul_f32 v75,v35,v75
v_exp_f32 v72,v72
v_exp_f32 v73,v73
v_exp_f32 v74,v74
v_exp_f32 v75,v75
v_fma_f32 v72,v72,v99,v99
buffer_load_dwordx4 v1,s[36:39],s66 offen offset:1024 sc1 lds
v_fma_f32 v73,v73,v99,v99
v_fma_f32 v74,v74,v99,v99
v_fma_f32 v75,v75,v99,v99
v_rcp_f32 v72,v72
v_rcp_f32 v73,v73
v_rcp_f32 v74,v74
v_rcp_f32 v75,v75
v_mul_f32 v32,v32,v72
v_mul_f32 v33,v33,v73
v_mul_f32 v34,v34,v74
v_mul_f32 v35,v35,v75
v_cvt_pk_bf16_f32 v80,v32,v33
buffer_load_dwordx4 v1,s[36:39],s66 offen offset:2048 sc1 lds
v_cvt_pk_bf16_f32 v81,v34,v35
v_fma_f32 v36,-v96,v20,v36
v_fma_f32 v37,-v96,v21,v37
v_fma_f32 v38,-v96,v22,v38
v_fma_f32 v39,-v96,v23,v39
v_mul_f32 v76,v36,v36
v_mul_f32 v77,v37,v37
v_mul_f32 v78,v38,v38
v_mul_f32 v79,v39,v39
v_fma_f32 v76,v98,v76,v97
v_fma_f32 v77,v98,v77,v97
v_fma_f32 v78,v98,v78,v97
buffer_load_dwordx4 v1,s[36:39],s66 offen offset:3072 sc1 lds
v_fma_f32 v79,v98,v79,v97
v_mul_f32 v76,v36,v76
v_mul_f32 v77,v37,v77
v_mul_f32 v78,v38,v78
v_mul_f32 v79,v39,v79
v_exp_f32 v76,v76
v_exp_f32 v77,v77
v_exp_f32 v78,v78
v_exp_f32 v79,v79
v_fma_f32 v76,v76,v99,v99
v_fma_f32 v77,v77,v99,v99
v_fma_f32 v78,v78,v99,v99
s_lshr_b32 s72,s66,5
s_lshr_b32 s71,s62,2
s_add_u32 m0,s71,s88
s_nop 0
buffer_load_dword v9,s[36:39],s72 offen offset:0 sc1 lds
v_fma_f32 v79,v79,v99,v99
v_rcp_f32 v76,v76
v_rcp_f32 v77,v77
v_rcp_f32 v78,v78
v_rcp_f32 v79,v79
v_mul_f32 v36,v36,v76
v_mul_f32 v37,v37,v77
v_mul_f32 v38,v38,v78
v_mul_f32 v39,v39,v79
v_cvt_pk_bf16_f32 v82,v36,v37
v_cvt_pk_bf16_f32 v83,v38,v39
buffer_store_dwordx4 v[80:83],v3,s[36:39],s73 offen offset:0 sc1
s_waitcnt vmcnt(6)
s_barrier
ds_read_b32 v13,v7 offset:8192
ds_read_b128 v[40:43],v2 offset:0
ds_read_b128 v[44:47],v2 offset:1024
ds_read_b128 v[48:51],v2 offset:2048
ds_read_b128 v[52:55],v2 offset:3072
ds_read_b128 v[56:59],v2 offset:4096
ds_read_b128 v[60:63],v2 offset:5120
ds_read_b128 v[64:67],v2 offset:6144
s_mul_i32 s70,s46,5
s_lshr_b32 s70,s70,6
buffer_store_dword v5,v4,s[36:39],s70 offen offset:0 sc1
s_waitcnt lgkmcnt(7)
v_cmp_eq_u32 vcc,s60,v13
s_nop 0
s_cmp_eq_u32 vcc_hi,-1
s_cselect_b32 s66,s49,s59
s_xor_b32 s62,s64,65536
s_cmp_ge_u32 s66,s59
s_cselect_b32 s68,s49,s50
s_lshr_b32 s68,s68,5
s_add_u32 m0,s78,0
s_nop 0
buffer_load_dword v6,s[36:39],s68 offen offset:0 sc1 lds
s_mov_b32 m0,s62
s_nop 0
buffer_load_dwordx4 v0,s[36:39],s66 offen offset:0 sc1 lds
buffer_load_dwordx4 v0,s[36:39],s66 offen offset:1024 sc1 lds
buffer_load_dwordx4 v0,s[36:39],s66 offen offset:2048 sc1 lds
buffer_load_dwordx4 v0,s[36:39],s66 offen offset:3072 sc1 lds
s_mov_b32 s46,s47
s_mov_b32 s47,s59
s_mul_i32 s70,s59,5
s_lshr_b32 s70,s70,6
buffer_store_dword v5,v4,s[36:39],s70 offen offset:0 sc1
s_waitcnt lgkmcnt(3)
s_setpc_b64 s[90:91]
Lg3o_done0_%=:)"
R"(
s_waitcnt vmcnt(0)
s_barrier
s_mul_i32 s70,s46,5
s_lshr_b32 s70,s70,6
buffer_store_dword v5,v4,s[36:39],s70 offen offset:0 sc1
s_mul_i32 s70,s47,5
s_lshr_b32 s70,s70,6
buffer_store_dword v5,v4,s[36:39],s70 offen offset:0 sc1
s_branch Lg3o_done_%=
Lg3o_slow1_%=:
s_cmp_lt_u32 s67,s59
s_cbranch_scc1 Lg3o_gs1_%=
Lg3o_slowb1_%=:
s_cmp_eq_u32 s54,1
s_cselect_b32 s68,s50,s49
s_lshr_b32 s68,s68,5
s_add_u32 m0,s78,8192
s_nop 0
buffer_load_dword v6,s[36:39],s68 offen offset:0 sc1 lds
s_mul_i32 s73,s48,7
s_lshr_b32 s73,s73,1
v_mul_f32 v99,v93,v92
v_mul_f32 v98,s77,v98
v_mul_f32 v98,v98,v97
v_fma_f32 v24,-v96,v16,v24
v_fma_f32 v25,-v96,v17,v25
v_fma_f32 v26,-v96,v18,v26
v_fma_f32 v27,-v96,v19,v27
v_mul_f32 v72,v24,v24
v_mul_f32 v73,v25,v25
v_mul_f32 v74,v26,v26
v_mul_f32 v75,v27,v27
v_fma_f32 v72,v98,v72,v97
v_fma_f32 v73,v98,v73,v97
v_fma_f32 v74,v98,v74,v97
v_fma_f32 v75,v98,v75,v97
v_mul_f32 v72,v24,v72
v_mul_f32 v73,v25,v73
v_mul_f32 v74,v26,v74
v_mul_f32 v75,v27,v75
v_exp_f32 v72,v72
v_exp_f32 v73,v73
v_exp_f32 v74,v74
v_exp_f32 v75,v75
v_fma_f32 v72,v72,v99,v99
v_fma_f32 v73,v73,v99,v99
v_fma_f32 v74,v74,v99,v99
v_fma_f32 v75,v75,v99,v99
v_rcp_f32 v72,v72
v_rcp_f32 v73,v73
v_rcp_f32 v74,v74
v_rcp_f32 v75,v75
v_mul_f32 v24,v24,v72
v_mul_f32 v25,v25,v73
v_mul_f32 v26,v26,v74
v_mul_f32 v27,v27,v75
v_cvt_pk_bf16_f32 v80,v24,v25
s_add_u32 m0,s78,8448
s_nop 0
buffer_load_dword v6,s[36:39],s68 offen offset:0 sc1 lds
v_cvt_pk_bf16_f32 v81,v26,v27
v_fma_f32 v28,-v96,v20,v28
v_fma_f32 v29,-v96,v21,v29
v_fma_f32 v30,-v96,v22,v30
v_fma_f32 v31,-v96,v23,v31
v_mul_f32 v76,v28,v28
v_mul_f32 v77,v29,v29
v_mul_f32 v78,v30,v30
v_mul_f32 v79,v31,v31
v_fma_f32 v76,v98,v76,v97
v_fma_f32 v77,v98,v77,v97
v_fma_f32 v78,v98,v78,v97
v_fma_f32 v79,v98,v79,v97
v_mul_f32 v76,v28,v76
v_mul_f32 v77,v29,v77
v_mul_f32 v78,v30,v78
v_mul_f32 v79,v31,v79
v_exp_f32 v76,v76
v_exp_f32 v77,v77
v_exp_f32 v78,v78
v_exp_f32 v79,v79
v_fma_f32 v76,v76,v99,v99
v_fma_f32 v77,v77,v99,v99
v_fma_f32 v78,v78,v99,v99
v_fma_f32 v79,v79,v99,v99
v_rcp_f32 v76,v76
v_rcp_f32 v77,v77
v_rcp_f32 v78,v78
v_rcp_f32 v79,v79
v_mul_f32 v28,v28,v76
v_mul_f32 v29,v29,v77
v_mul_f32 v30,v30,v78
v_mul_f32 v31,v31,v79
v_cvt_pk_bf16_f32 v82,v28,v29
v_cvt_pk_bf16_f32 v83,v30,v31
buffer_store_dwordx4 v[80:83],v3,s[36:39],s73 offen offset:0 sc1
s_cmp_ge_u32 s49,s59
s_cbranch_scc1 Lg3o_done1_%=
s_add_u32 m0,s78,16384
s_nop 0
buffer_load_dword v6,s[36:39],s68 offen offset:0 sc1 lds
s_waitcnt vmcnt(3)
s_barrier
ds_read_b32 v13,v7 offset:8192
s_mul_i32 s70,s47,5
s_lshr_b32 s70,s70,6
buffer_store_dword v5,v4,s[36:39],s70 offen offset:0 sc1
s_mov_b32 s47,s59
s_mov_b32 s69,18432
s_waitcnt lgkmcnt(0)
v_cmp_eq_u32 vcc,s60,v13
s_nop 0
s_cmp_eq_u32 vcc_lo,-1
s_cbranch_scc1 Lg3o_go1_%=
s_add_u32 m0,s78,16640
s_nop 0
buffer_load_dword v6,s[36:39],s68 offen offset:0 sc1 lds
s_waitcnt vmcnt(4)
s_barrier
ds_read_b32 v13,v7 offset:8448
s_mov_b32 s69,18688
s_waitcnt lgkmcnt(0)
v_cmp_eq_u32 vcc,s60,v13
s_nop 0
s_cmp_eq_u32 vcc_lo,-1
s_cbranch_scc1 Lg3o_go1_%=
s_add_u32 m0,s78,8192
s_nop 0
buffer_load_dword v6,s[36:39],s68 offen offset:0 sc1 lds
s_waitcnt vmcnt(3)
s_barrier
ds_read_b32 v13,v7 offset:16384
s_mul_i32 s70,s48,5
s_lshr_b32 s70,s70,6
buffer_store_dword v5,v4,s[36:39],s70 offen offset:0 sc1
s_mov_b32 s48,s59
s_mov_b32 s69,10240
s_waitcnt lgkmcnt(0)
v_cmp_eq_u32 vcc,s60,v13
s_nop 0
s_cmp_eq_u32 vcc_lo,-1
s_cbranch_scc1 Lg3o_go1_%=
s_add_u32 m0,s78,8448
s_nop 0
buffer_load_dword v6,s[36:39],s68 offen offset:0 sc1 lds
s_waitcnt vmcnt(3)
s_barrier
ds_read_b32 v13,v7 offset:16640
s_mov_b32 s69,10496
s_waitcnt lgkmcnt(0)
v_cmp_eq_u32 vcc,s60,v13
s_nop 0
s_cmp_eq_u32 vcc_lo,-1
s_cbranch_scc1 Lg3o_go1_%=
Lg3o_poll1_%=:
s_add_u32 m0,s78,16384
s_nop 0
buffer_load_dword v6,s[36:39],s68 offen offset:0 sc1 lds
s_waitcnt vmcnt(2)
s_barrier
ds_read_b32 v13,v7 offset:8192
s_mov_b32 s69,18432
s_waitcnt lgkmcnt(0)
v_cmp_eq_u32 vcc,s60,v13
s_nop 0
s_cmp_eq_u32 vcc_lo,-1
s_cbranch_scc1 Lg3o_go1_%=
s_sleep 4
s_add_u32 m0,s78,16640
s_nop 0
buffer_load_dword v6,s[36:39],s68 offen offset:0 sc1 lds
s_waitcnt vmcnt(2)
s_barrier
ds_read_b32 v13,v7 offset:8448
s_mov_b32 s69,18688
s_waitcnt lgkmcnt(0)
v_cmp_eq_u32 vcc,s60,v13
s_nop 0
s_cmp_eq_u32 vcc_lo,-1
s_cbranch_scc1 Lg3o_go1_%=
s_sleep 4
s_add_u32 m0,s78,8192
s_nop 0
buffer_load_dword v6,s[36:39],s68 offen offset:0 sc1 lds
s_waitcnt vmcnt(2)
s_barrier
ds_read_b32 v13,v7 offset:16384
s_mov_b32 s69,10240
s_waitcnt lgkmcnt(0)
v_cmp_eq_u32 vcc,s60,v13
s_nop 0
s_cmp_eq_u32 vcc_lo,-1
s_cbranch_scc1 Lg3o_go1_%=
s_sleep 4
s_add_u32 m0,s78,8448
s_nop 0
buffer_load_dword v6,s[36:39],s68 offen offset:0 sc1 lds
s_waitcnt vmcnt(2)
s_barrier
ds_read_b32 v13,v7 offset:16640)"
R"(
s_mov_b32 s69,10496
s_waitcnt lgkmcnt(0)
v_cmp_eq_u32 vcc,s60,v13
s_nop 0
s_cmp_eq_u32 vcc_lo,-1
s_cbranch_scc1 Lg3o_go1_%=
s_sleep 4
s_add_u32 s79,s79,1
s_cmp_lt_u32 s79,20000
s_cbranch_scc1 Lg3o_poll1_%=
Lg3o_go1_%=:
s_xor_b32 s62,s64,65536
s_mov_b32 m0,s62
s_nop 0
buffer_load_dwordx4 v0,s[36:39],s49 offen offset:0 sc1 lds
buffer_load_dwordx4 v0,s[36:39],s49 offen offset:1024 sc1 lds
buffer_load_dwordx4 v0,s[36:39],s49 offen offset:2048 sc1 lds
buffer_load_dwordx4 v0,s[36:39],s49 offen offset:3072 sc1 lds
s_add_u32 m0,s62,16384
s_nop 0
buffer_load_dwordx4 v1,s[36:39],s49 offen offset:0 sc1 lds
buffer_load_dwordx4 v1,s[36:39],s49 offen offset:1024 sc1 lds
buffer_load_dwordx4 v1,s[36:39],s49 offen offset:2048 sc1 lds
buffer_load_dwordx4 v1,s[36:39],s49 offen offset:3072 sc1 lds
s_lshr_b32 s72,s49,5
s_lshr_b32 s71,s62,2
s_add_u32 m0,s71,s88
s_nop 0
buffer_load_dword v9,s[36:39],s72 offen offset:0 sc1 lds
s_cmp_eq_u32 vcc_hi,-1
s_cselect_b32 s67,s50,s59
s_xor_b32 s62,s63,65536
s_cmp_ge_u32 s67,s59
s_cselect_b32 s68,s50,s51
s_lshr_b32 s68,s68,5
s_add_u32 m0,s78,256
s_nop 0
buffer_load_dword v6,s[36:39],s68 offen offset:0 sc1 lds
s_mov_b32 m0,s62
s_nop 0
buffer_load_dwordx4 v0,s[36:39],s67 offen offset:0 sc1 lds
buffer_load_dwordx4 v0,s[36:39],s67 offen offset:1024 sc1 lds
buffer_load_dwordx4 v0,s[36:39],s67 offen offset:2048 sc1 lds
buffer_load_dwordx4 v0,s[36:39],s67 offen offset:3072 sc1 lds
s_waitcnt vmcnt(10)
s_barrier
ds_read_b128 v[40:43],v2 offset:32768
ds_read_b128 v[44:47],v2 offset:33792
ds_read_b128 v[48:51],v2 offset:34816
ds_read_b128 v[52:55],v2 offset:35840
ds_read_b128 v[56:59],v2 offset:36864
ds_read_b128 v[60:63],v2 offset:37888
ds_read_b128 v[64:67],v2 offset:38912
s_mul_i32 s70,s47,5
s_lshr_b32 s70,s70,6
buffer_store_dword v5,v4,s[36:39],s70 offen offset:0 sc1
s_mov_b32 s47,s59
s_mul_i32 s70,s48,5
s_lshr_b32 s70,s70,6
buffer_store_dword v5,v4,s[36:39],s70 offen offset:0 sc1
s_mov_b32 s48,s59
s_mul_i32 s70,s59,5
s_lshr_b32 s70,s70,6
buffer_store_dword v5,v4,s[36:39],s70 offen offset:0 sc1
s_waitcnt lgkmcnt(3)
s_setpc_b64 s[92:93]
Lg3o_gs1_%=:
s_cmp_eq_u32 s54,1
s_cselect_b32 s68,s50,s49
s_lshr_b32 s68,s68,5
s_mul_i32 s73,s48,7
s_lshr_b32 s73,s73,1
s_add_u32 m0,s78,8192
s_nop 0
buffer_load_dword v6,s[36:39],s68 offen offset:0 sc1 lds
v_mul_f32 v99,v93,v92
v_mul_f32 v98,s77,v98
v_mul_f32 v98,v98,v97
v_fma_f32 v24,-v96,v16,v24
v_fma_f32 v25,-v96,v17,v25
v_fma_f32 v26,-v96,v18,v26
v_fma_f32 v27,-v96,v19,v27
v_mul_f32 v72,v24,v24
v_mul_f32 v73,v25,v25
v_mul_f32 v74,v26,v26
v_mul_f32 v75,v27,v27
v_fma_f32 v72,v98,v72,v97
s_add_u32 m0,s62,16384
s_nop 0
buffer_load_dwordx4 v1,s[36:39],s67 offen offset:0 sc1 lds
v_fma_f32 v73,v98,v73,v97
v_fma_f32 v74,v98,v74,v97
v_fma_f32 v75,v98,v75,v97
v_mul_f32 v72,v24,v72
v_mul_f32 v73,v25,v73
v_mul_f32 v74,v26,v74
v_mul_f32 v75,v27,v75
v_exp_f32 v72,v72
v_exp_f32 v73,v73
v_exp_f32 v74,v74
v_exp_f32 v75,v75
v_fma_f32 v72,v72,v99,v99
buffer_load_dwordx4 v1,s[36:39],s67 offen offset:1024 sc1 lds
v_fma_f32 v73,v73,v99,v99
v_fma_f32 v74,v74,v99,v99
v_fma_f32 v75,v75,v99,v99
v_rcp_f32 v72,v72
v_rcp_f32 v73,v73
v_rcp_f32 v74,v74
v_rcp_f32 v75,v75
v_mul_f32 v24,v24,v72
v_mul_f32 v25,v25,v73
v_mul_f32 v26,v26,v74
v_mul_f32 v27,v27,v75
v_cvt_pk_bf16_f32 v80,v24,v25
buffer_load_dwordx4 v1,s[36:39],s67 offen offset:2048 sc1 lds
v_cvt_pk_bf16_f32 v81,v26,v27
v_fma_f32 v28,-v96,v20,v28
v_fma_f32 v29,-v96,v21,v29
v_fma_f32 v30,-v96,v22,v30
v_fma_f32 v31,-v96,v23,v31
v_mul_f32 v76,v28,v28
v_mul_f32 v77,v29,v29
v_mul_f32 v78,v30,v30
v_mul_f32 v79,v31,v31
v_fma_f32 v76,v98,v76,v97
v_fma_f32 v77,v98,v77,v97
v_fma_f32 v78,v98,v78,v97
buffer_load_dwordx4 v1,s[36:39],s67 offen offset:3072 sc1 lds
v_fma_f32 v79,v98,v79,v97
v_mul_f32 v76,v28,v76
v_mul_f32 v77,v29,v77
v_mul_f32 v78,v30,v78
v_mul_f32 v79,v31,v79
v_exp_f32 v76,v76
v_exp_f32 v77,v77
v_exp_f32 v78,v78
v_exp_f32 v79,v79
v_fma_f32 v76,v76,v99,v99
v_fma_f32 v77,v77,v99,v99
v_fma_f32 v78,v78,v99,v99
s_lshr_b32 s72,s67,5
s_lshr_b32 s71,s62,2
s_add_u32 m0,s71,s88
s_nop 0
buffer_load_dword v9,s[36:39],s72 offen offset:0 sc1 lds
v_fma_f32 v79,v79,v99,v99
v_rcp_f32 v76,v76
v_rcp_f32 v77,v77
v_rcp_f32 v78,v78
v_rcp_f32 v79,v79
v_mul_f32 v28,v28,v76
v_mul_f32 v29,v29,v77
v_mul_f32 v30,v30,v78
v_mul_f32 v31,v31,v79
v_cvt_pk_bf16_f32 v82,v28,v29
v_cvt_pk_bf16_f32 v83,v30,v31
buffer_store_dwordx4 v[80:83],v3,s[36:39],s73 offen offset:0 sc1
s_waitcnt vmcnt(6)
s_barrier
ds_read_b32 v13,v7 offset:8192
ds_read_b128 v[40:43],v2 offset:32768
ds_read_b128 v[44:47],v2 offset:33792
ds_read_b128 v[48:51],v2 offset:34816
ds_read_b128 v[52:55],v2 offset:35840
ds_read_b128 v[56:59],v2 offset:36864
ds_read_b128 v[60:63],v2 offset:37888
ds_read_b128 v[64:67],v2 offset:38912
s_mul_i32 s70,s47,5
s_lshr_b32 s70,s70,6
buffer_store_dword v5,v4,s[36:39],s70 offen offset:0 sc1
s_waitcnt lgkmcnt(7)
v_cmp_eq_u32 vcc,s60,v13
s_nop 0
s_cmp_eq_u32 vcc_hi,-1
s_cselect_b32 s67,s50,s59
s_xor_b32 s62,s63,65536
s_cmp_ge_u32 s67,s59
s_cselect_b32 s68,s50,s51
s_lshr_b32 s68,s68,5
s_add_u32 m0,s78,256
s_nop 0
buffer_load_dword v6,s[36:39],s68 offen offset:0 sc1 lds
s_mov_b32 m0,s62
s_nop 0
buffer_load_dwordx4 v0,s[36:39],s67 offen offset:0 sc1 lds
buffer_load_dwordx4 v0,s[36:39],s67 offen offset:1024 sc1 lds
buffer_load_dwordx4 v0,s[36:39],s67 offen offset:2048 sc1 lds
buffer_load_dwordx4 v0,s[36:39],s67 offen offset:3072 sc1 lds
s_mov_b32 s47,s48
s_mov_b32 s48,s59
s_mul_i32 s70,s59,5
s_lshr_b32 s70,s70,6
buffer_store_dword v5,v4,s[36:39],s70 offen offset:0 sc1
s_waitcnt lgkmcnt(3)
s_setpc_b64 s[92:93]
Lg3o_done1_%=:
s_waitcnt vmcnt(0)
s_barrier
s_mul_i32 s70,s47,5)"
R"(
s_lshr_b32 s70,s70,6
buffer_store_dword v5,v4,s[36:39],s70 offen offset:0 sc1
s_mul_i32 s70,s48,5
s_lshr_b32 s70,s70,6
buffer_store_dword v5,v4,s[36:39],s70 offen offset:0 sc1
s_branch Lg3o_done_%=
Lg3o_done_%=:
s_waitcnt vmcnt(0) lgkmcnt(0)
s_barrier
s_endpgm
Lcode_g3o_e:)"
  :: "s"(ws), "s"(wave), "s"(sStep), "s"(sX1), "s"(sX2), "s"(sU0), "s"(sWw), "s"(sBuf), "s"(sEp), "v"(vSrc), "v"(vA), "v"(vOut), "v"(vFlag), "v"(vPollOff), "v"(vSnapRd), "v"(vX), "v"(vAuxS), "v"(vStW), "v"(vStR), "v"(vWd0), "v"(vWd1), "v"(vWr0), "v"(vWr1), "v"(vWr2), "s"(w3) : "v0", "v1", "v2", "v3", "v4", "v5", "v6", "v7", "v8", "v9", "v10", "v11", "v12", "v13", "v14", "v15", "v16", "v17", "v18", "v19", "v20", "v21", "v22", "v23", "v24", "v25", "v26", "v27", "v28", "v29", "v30", "v31", "v32", "v33", "v34", "v35", "v36", "v37", "v38", "v39", "v40", "v41", "v42", "v43", "v44", "v45", "v46", "v47", "v48", "v49", "v50", "v51", "v52", "v53", "v54", "v55", "v56", "v57", "v58", "v59", "v60", "v61", "v62", "v63", "v64", "v65", "v66", "v67", "v68", "v69", "v70", "v71", "v72", "v73", "v74", "v75", "v76", "v77", "v78", "v79", "v80", "v81", "v82", "v83", "v84", "v85", "v86", "v87", "v88", "v89", "v90", "v91", "v92", "v93", "v94", "v95", "v96", "v97", "v98", "v99", "v100", "v101", "v102", "v103", "v104", "v105", "v106", "v107", "v108", "v109", "v110", "v111", "v112", "v113", "v114", "v115", "v116", "v117", "v118", "v119", "v120", "v121", "v122", "v123", "v124", "v125", "v126", "v127", "v128", "v129", "v130", "v131", "v132", "v133", "v134", "v135", "v136", "v137", "v138", "v139", "v140", "v141", "v142", "v143", "v144", "v145", "v146", "v147", "v148", "v149", "v150", "v151", "v152", "v153", "v154", "v155", "v156", "v157", "v158", "v159", "v160", "v161", "v162", "v163", "v164", "v165", "v166", "v167", "v168", "v169", "v170", "v171", "v172", "v173", "v174", "v175", "v176", "v177", "v178", "v179", "v180", "v181", "v182", "v183", "v184", "v185", "v186", "v187", "v188", "v189", "v190", "v191", "v192", "v193", "v194", "v195", "v196", "v197", "v198", "v199", "v200", "v201", "v202", "v203", "v204", "v205", "v206", "v207", "v208", "v209", "v210", "v211", "v212", "v213", "v214", "v215", "v216", "v217", "v218", "v219", "v220", "v221", "v222", "v223", "v224", "v225", "v226", "v227", "v228", "v229", "v230", "v231", "v255", "a0", "a1", "a2", "a3", "a4", "a5", "a6", "a7", "a8", "a9", "a10", "a11", "a12", "a13", "a14", "a15", "a16", "a17", "a18", "a19", "a20", "a21", "a22", "a23", "a24", "a25", "a26", "a27", "a28", "a29", "a30", "a31", "a32", "a33", "a34", "a35", "a36", "a37", "a38", "a39", "a40", "a41", "a42", "a43", "a44", "a45", "a46", "a47", "a48", "a49", "a50", "a51", "a52", "a53", "a54", "a55", "a56", "a57", "a58", "a59", "a60", "a61", "a62", "a63", "a64", "a65", "a66", "a67", "a68", "a69", "a70", "a71", "a72", "a73", "a74", "a75", "a76", "a77", "a78", "a79", "a80", "a81", "a82", "a83", "a84", "a85", "a86", "a87", "a88", "a89", "a90", "a91", "a92", "a93", "a94", "a95", "a96", "a97", "a98", "a99", "a100", "a101", "a102", "a103", "a104", "a105", "a106", "a107", "a108", "a109", "a110", "a111", "a112", "a113", "a114", "a115", "a116", "a117", "a118", "a119", "a120", "a121", "a122", "a123", "a124", "a125", "a126", "a127", "a128", "a129", "a130", "a131", "a132", "a133", "a134", "a135", "a136", "a137", "a138", "a139", "a140", "a141", "a142", "a143", "a144", "a145", "a146", "a147", "a148", "a149", "a150", "a151", "a152", "a153", "a154", "a155", "a156", "a157", "a158", "a159", "a160", "a161", "a162", "a163", "a164", "a165", "a166", "a167", "a168", "a169", "a170", "a171", "a172", "a173", "a174", "a175", "a176", "a177", "a178", "a179", "a180", "a181", "a182", "a183", "a184", "a185", "a186", "a187", "a188", "a189", "a190", "a191", "a192", "a193", "a194", "a195", "a196", "a197", "a198", "a199", "a200", "a201", "a202", "a203", "a204", "a205", "a206", "a207", "a208", "a209", "a210", "a211", "a212", "a213", "a214", "a215", "a216", "a217", "a218", "a219", "a220", "a221", "a222", "a223", "a224", "a225", "a226", "a227", "a228", "a229", "a230", "a231", "a232", "a233", "a234", "a235", "a236", "a237", "a238", "a239", "a240", "a241", "a242", "a243", "a244", "a245", "a246", "a247", "a248", "a249", "a250", "a251", "a252", "a253", "a254", "a255", "s36", "s37", "s38", "s39", "s40", "s41", "s42", "s43", "s44", "s45", "s46", "s47", "s48", "s49", "s50", "s51", "s52", "s53", "s54", "s55", "s56", "s57", "s58", "s59", "s60", "s61", "s62", "s63", "s64", "s65", "s66", "s67", "s68", "s69", "s70", "s71", "s72", "s73", "s74", "s75", "s76", "s77", "s78", "s79", "s80", "s81", "s82", "s83", "s84", "s85", "s86", "s87", "s88", "s89", "s90", "s91", "s92", "s93", "s94", "s95", "s96", "s97", "s98", "s99", "vcc", "scc", "memory"); }
    __builtin_unreachable();
  } else if (kind == 4) {
    KARG(ws, 10) KARG(w4, 9)
    COMMON(kh == 1 ? 6 : 5, OFF_FG + ((14 * kh) / 3) * 128, OFF_FP + (kh * 4 + slice) * 128, 65536, 256, 2048, 5);
    const unsigned vSrc = OFF_G + kh * 28672 + wave * 4096 + vA, vSrc2 = OFF_G + kh * 28672 + 16384 + wave * 3072 + vA;
    const unsigned vOut = (kh == 0 ? OFF_P0 : kh == 1 ? OFF_P1 : kh == 2 ? OFF_P2 : OFF_P3) + (l15 >> 3) * 16384 + (slice * 8 + wave * 2 + (kh >> 1)) * 512 + lg * 128 + (l15 & 7) * 16;
    const unsigned vOut2 = vOut ^ 512;
    const unsigned vAuxS = ((kh & 1) ? OFF_XP : OFF_X1) + (slice * 8 + wave * 2 + 1 - (kh >> 1)) * 1024 + vA;
    const unsigned sWw = __builtin_amdgcn_readfirstlane(kh * 896 * 2048 + slice * 512 + wave * 128);
    asm volatile(R"(
Lcode_g4_b:
s_mov_b64 s[36:37],%0
s_mov_b32 s38,122727552
s_mov_b32 s39,0x00020000
s_mov_b32 s54,%1
s_mov_b32 s55,%2
s_mov_b32 s56,%3
s_mov_b32 s58,%4
s_mov_b32 s83,%5
s_mov_b32 s82,%6
s_mov_b32 s84,%7
s_mov_b32 s60,%8
v_mov_b32 v0,%9
v_mov_b32 v1,%10
v_mov_b32 v2,%11
v_mov_b32 v3,%12
v_mov_b32 v4,%13
v_mov_b32 v5,%14
v_mov_b32 v7,%15
v_mov_b32 v8,%16
v_mov_b32 v9,%17
v_mov_b32 v12,%18
v_mov_b32 v13,%19
v_mov_b32 v14,%20
v_mov_b32 v15,%21
v_mov_b32 v16,%22
s_mov_b64 s[40:41],%23
s_mov_b32 s42,7340032
s_mov_b32 s43,0x00020000
s_lshl_b32 s88,s54,12
s_mul_i32 s68,s54,9216
s_add_u32 s68,s68,122880
v_add_u32 v10,s68,v2
s_add_u32 m0,s88,0
s_add_u32 s61,s82,0
buffer_load_dwordx4 v12,s[40:43],s61 offen offset:0  lds
s_add_u32 m0,s88,1024
s_nop 0
buffer_load_dwordx4 v13,s[40:43],s61 offen offset:0  lds
s_add_u32 m0,s88,2048
s_add_u32 s61,s82,32768
buffer_load_dwordx4 v12,s[40:43],s61 offen offset:0  lds
s_add_u32 m0,s88,3072
s_nop 0
buffer_load_dwordx4 v13,s[40:43],s61 offen offset:0  lds
s_add_u32 m0,s88,16384
s_add_u32 s61,s82,65536
buffer_load_dwordx4 v12,s[40:43],s61 offen offset:0  lds
s_add_u32 m0,s88,17408
s_nop 0
buffer_load_dwordx4 v13,s[40:43],s61 offen offset:0  lds
s_add_u32 m0,s88,18432
s_add_u32 s61,s82,98304
buffer_load_dwordx4 v12,s[40:43],s61 offen offset:0  lds
s_add_u32 m0,s88,19456
s_nop 0
buffer_load_dwordx4 v13,s[40:43],s61 offen offset:0  lds
s_add_u32 m0,s88,32768
s_add_u32 s61,s82,131072
buffer_load_dwordx4 v12,s[40:43],s61 offen offset:0  lds
s_add_u32 m0,s88,33792
s_nop 0
buffer_load_dwordx4 v13,s[40:43],s61 offen offset:0  lds
s_add_u32 m0,s88,34816
s_add_u32 s61,s82,163840
buffer_load_dwordx4 v12,s[40:43],s61 offen offset:0  lds
s_add_u32 m0,s88,35840
s_nop 0
buffer_load_dwordx4 v13,s[40:43],s61 offen offset:0  lds
s_add_u32 m0,s88,49152
s_add_u32 s61,s82,196608
buffer_load_dwordx4 v12,s[40:43],s61 offen offset:0  lds
s_add_u32 m0,s88,50176
s_nop 0
buffer_load_dwordx4 v13,s[40:43],s61 offen offset:0  lds
s_add_u32 m0,s88,51200
s_add_u32 s61,s82,229376
buffer_load_dwordx4 v12,s[40:43],s61 offen offset:0  lds
s_add_u32 m0,s88,52224
s_nop 0
buffer_load_dwordx4 v13,s[40:43],s61 offen offset:0  lds
s_add_u32 m0,s88,65536
s_add_u32 s61,s82,262144
buffer_load_dwordx4 v12,s[40:43],s61 offen offset:0  lds
s_add_u32 m0,s88,66560
s_nop 0
buffer_load_dwordx4 v13,s[40:43],s61 offen offset:0  lds
s_add_u32 m0,s88,67584
s_add_u32 s61,s82,294912
buffer_load_dwordx4 v12,s[40:43],s61 offen offset:0  lds
s_add_u32 m0,s88,68608
s_nop 0
buffer_load_dwordx4 v13,s[40:43],s61 offen offset:0  lds
v_add_u32 v28,49152,v14
v_add_u32 v30,49152,v16
v_add_u32 v32,98304,v14
v_add_u32 v34,98304,v16
s_waitcnt vmcnt(16)
s_add_u32 m0,s88,81920
s_add_u32 s61,s82,327680
buffer_load_dwordx4 v12,s[40:43],s61 offen offset:0  lds
s_add_u32 m0,s88,82944
ds_read_b64_tr_b16 a[0:1],v14 offset:0
buffer_load_dwordx4 v13,s[40:43],s61 offen offset:0  lds
s_add_u32 m0,s88,83968
s_add_u32 s61,s82,360448
buffer_load_dwordx4 v12,s[40:43],s61 offen offset:0  lds
s_add_u32 m0,s88,84992
ds_read_b64_tr_b16 a[2:3],v14 offset:512
buffer_load_dwordx4 v13,s[40:43],s61 offen offset:0  lds
ds_read_b64_tr_b16 a[4:5],v14 offset:8
ds_read_b64_tr_b16 a[6:7],v14 offset:520
ds_read_b64_tr_b16 a[8:9],v16 offset:0
ds_read_b64_tr_b16 a[10:11],v16 offset:512
ds_read_b64_tr_b16 a[12:13],v16 offset:8
ds_read_b64_tr_b16 a[14:15],v16 offset:520
s_waitcnt vmcnt(16)
s_add_u32 m0,s88,98304
s_add_u32 s61,s82,393216
buffer_load_dwordx4 v12,s[40:43],s61 offen offset:0  lds
s_add_u32 m0,s88,99328
ds_read_b64_tr_b16 a[16:17],v14 offset:16384
buffer_load_dwordx4 v13,s[40:43],s61 offen offset:0  lds
s_add_u32 m0,s88,100352
s_add_u32 s61,s82,425984
buffer_load_dwordx4 v12,s[40:43],s61 offen offset:0  lds
s_add_u32 m0,s88,101376
ds_read_b64_tr_b16 a[18:19],v14 offset:16896
buffer_load_dwordx4 v13,s[40:43],s61 offen offset:0  lds
ds_read_b64_tr_b16 a[20:21],v14 offset:16392
ds_read_b64_tr_b16 a[22:23],v14 offset:16904
ds_read_b64_tr_b16 a[24:25],v16 offset:16384
ds_read_b64_tr_b16 a[26:27],v16 offset:16896
ds_read_b64_tr_b16 a[28:29],v16 offset:16392
ds_read_b64_tr_b16 a[30:31],v16 offset:16904
s_waitcnt lgkmcnt(8)
s_waitcnt vmcnt(16)
s_add_u32 m0,s88,0
s_add_u32 s61,s82,458752
buffer_load_dwordx4 v12,s[40:43],s61 offen offset:0  lds
s_add_u32 m0,s88,1024
ds_read_b64_tr_b16 a[32:33],v14 offset:32768
buffer_load_dwordx4 v13,s[40:43],s61 offen offset:0  lds
s_add_u32 m0,s88,2048
s_add_u32 s61,s82,491520
buffer_load_dwordx4 v12,s[40:43],s61 offen offset:0  lds
s_add_u32 m0,s88,3072
ds_read_b64_tr_b16 a[34:35],v14 offset:33280
buffer_load_dwordx4 v13,s[40:43],s61 offen offset:0  lds
ds_read_b64_tr_b16 a[36:37],v14 offset:32776
ds_read_b64_tr_b16 a[38:39],v14 offset:33288
ds_read_b64_tr_b16 a[40:41],v16 offset:32768
ds_read_b64_tr_b16 a[42:43],v16 offset:33280
ds_read_b64_tr_b16 a[44:45],v16 offset:32776
ds_read_b64_tr_b16 a[46:47],v16 offset:33288
s_waitcnt lgkmcnt(8)
s_waitcnt vmcnt(16)
s_add_u32 m0,s88,16384
s_add_u32 s61,s82,524288
buffer_load_dwordx4 v12,s[40:43],s61 offen offset:0  lds
s_add_u32 m0,s88,17408
ds_read_b64_tr_b16 a[48:49],v28 offset:0
buffer_load_dwordx4 v13,s[40:43],s61 offen offset:0  lds
s_add_u32 m0,s88,18432
s_add_u32 s61,s82,557056
buffer_load_dwordx4 v12,s[40:43],s61 offen offset:0  lds
s_add_u32 m0,s88,19456
ds_read_b64_tr_b16 a[50:51],v28 offset:512
buffer_load_dwordx4 v13,s[40:43],s61 offen offset:0  lds
ds_read_b64_tr_b16 a[52:53],v28 offset:8
ds_read_b64_tr_b16 a[54:55],v28 offset:520
ds_read_b64_tr_b16 a[56:57],v30 offset:0
ds_read_b64_tr_b16 a[58:59],v30 offset:512
ds_read_b64_tr_b16 a[60:61],v30 offset:8
ds_read_b64_tr_b16 a[62:63],v30 offset:520
s_waitcnt lgkmcnt(8)
s_waitcnt vmcnt(16)
s_add_u32 m0,s88,32768
s_add_u32 s61,s82,589824
buffer_load_dwordx4 v12,s[40:43],s61 offen offset:0  lds
s_add_u32 m0,s88,33792
ds_read_b64_tr_b16 a[64:65],v28 offset:16384
buffer_load_dwordx4 v13,s[40:43],s61 offen offset:0  lds
s_add_u32 m0,s88,34816
s_add_u32 s61,s82,622592
buffer_load_dwordx4 v12,s[40:43],s61 offen offset:0  lds
s_add_u32 m0,s88,35840
ds_read_b64_tr_b16 a[66:67],v28 offset:16896
buffer_load_dwordx4 v13,s[40:43],s61 offen offset:0  lds
ds_read_b64_tr_b16 a[68:69],v28 offset:16392
ds_read_b64_tr_b16 a[70:71],v28 offset:16904
ds_read_b64_tr_b16 a[72:73],v30 offset:16384
ds_read_b64_tr_b16 a[74:75],v30 offset:16896
ds_read_b64_tr_b16 a[76:77],v30 offset:16392
ds_read_b64_tr_b16 a[78:79],v30 offset:16904
s_waitcnt lgkmcnt(8)
s_waitcnt vmcnt(16)
s_add_u32 m0,s88,49152
s_add_u32 s61,s82,655360
buffer_load_dwordx4 v12,s[40:43],s61 offen offset:0  lds)"
R"(
s_add_u32 m0,s88,50176
ds_read_b64_tr_b16 a[80:81],v28 offset:32768
buffer_load_dwordx4 v13,s[40:43],s61 offen offset:0  lds
s_add_u32 m0,s88,51200
s_add_u32 s61,s82,688128
buffer_load_dwordx4 v12,s[40:43],s61 offen offset:0  lds
s_add_u32 m0,s88,52224
ds_read_b64_tr_b16 a[82:83],v28 offset:33280
buffer_load_dwordx4 v13,s[40:43],s61 offen offset:0  lds
ds_read_b64_tr_b16 a[84:85],v28 offset:32776
ds_read_b64_tr_b16 a[86:87],v28 offset:33288
ds_read_b64_tr_b16 a[88:89],v30 offset:32768
ds_read_b64_tr_b16 a[90:91],v30 offset:33280
ds_read_b64_tr_b16 a[92:93],v30 offset:32776
ds_read_b64_tr_b16 a[94:95],v30 offset:33288
s_waitcnt lgkmcnt(8)
s_waitcnt vmcnt(16)
s_add_u32 m0,s88,65536
s_add_u32 s61,s82,720896
buffer_load_dwordx4 v12,s[40:43],s61 offen offset:0  lds
s_add_u32 m0,s88,66560
ds_read_b64_tr_b16 a[96:97],v32 offset:0
buffer_load_dwordx4 v13,s[40:43],s61 offen offset:0  lds
s_add_u32 m0,s88,67584
s_add_u32 s61,s82,753664
buffer_load_dwordx4 v12,s[40:43],s61 offen offset:0  lds
s_add_u32 m0,s88,68608
ds_read_b64_tr_b16 a[98:99],v32 offset:512
buffer_load_dwordx4 v13,s[40:43],s61 offen offset:0  lds
ds_read_b64_tr_b16 a[100:101],v32 offset:8
ds_read_b64_tr_b16 a[102:103],v32 offset:520
ds_read_b64_tr_b16 a[104:105],v34 offset:0
ds_read_b64_tr_b16 a[106:107],v34 offset:512
ds_read_b64_tr_b16 a[108:109],v34 offset:8
ds_read_b64_tr_b16 a[110:111],v34 offset:520
s_waitcnt lgkmcnt(8)
s_waitcnt vmcnt(16)
s_add_u32 m0,s88,81920
s_add_u32 s61,s82,786432
buffer_load_dwordx4 v12,s[40:43],s61 offen offset:0  lds
s_add_u32 m0,s88,82944
ds_read_b64_tr_b16 a[112:113],v14 offset:0
buffer_load_dwordx4 v13,s[40:43],s61 offen offset:0  lds
s_add_u32 m0,s88,83968
s_add_u32 s61,s82,819200
buffer_load_dwordx4 v12,s[40:43],s61 offen offset:0  lds
s_add_u32 m0,s88,84992
ds_read_b64_tr_b16 a[114:115],v14 offset:512
buffer_load_dwordx4 v13,s[40:43],s61 offen offset:0  lds
ds_read_b64_tr_b16 a[116:117],v14 offset:8
ds_read_b64_tr_b16 a[118:119],v14 offset:520
ds_read_b64_tr_b16 a[120:121],v16 offset:0
ds_read_b64_tr_b16 a[122:123],v16 offset:512
ds_read_b64_tr_b16 a[124:125],v16 offset:8
ds_read_b64_tr_b16 a[126:127],v16 offset:520
s_waitcnt lgkmcnt(8)
s_waitcnt vmcnt(16)
s_add_u32 m0,s88,98304
s_add_u32 s61,s82,851968
buffer_load_dwordx4 v12,s[40:43],s61 offen offset:0  lds
s_add_u32 m0,s88,99328
ds_read_b64_tr_b16 a[128:129],v14 offset:16384
buffer_load_dwordx4 v13,s[40:43],s61 offen offset:0  lds
s_add_u32 m0,s88,100352
s_add_u32 s61,s82,884736
buffer_load_dwordx4 v12,s[40:43],s61 offen offset:0  lds
s_add_u32 m0,s88,101376
ds_read_b64_tr_b16 a[130:131],v14 offset:16896
buffer_load_dwordx4 v13,s[40:43],s61 offen offset:0  lds
ds_read_b64_tr_b16 a[132:133],v14 offset:16392
ds_read_b64_tr_b16 a[134:135],v14 offset:16904
ds_read_b64_tr_b16 a[136:137],v16 offset:16384
ds_read_b64_tr_b16 a[138:139],v16 offset:16896
ds_read_b64_tr_b16 a[140:141],v16 offset:16392
ds_read_b64_tr_b16 a[142:143],v16 offset:16904
s_waitcnt lgkmcnt(8)
s_waitcnt vmcnt(16)
s_add_u32 m0,s88,0
s_add_u32 s61,s82,917504
buffer_load_dwordx4 v12,s[40:43],s61 offen offset:0  lds
s_add_u32 m0,s88,1024
ds_read_b64_tr_b16 a[144:145],v14 offset:32768
buffer_load_dwordx4 v13,s[40:43],s61 offen offset:0  lds
s_add_u32 m0,s88,2048
s_add_u32 s61,s82,950272
buffer_load_dwordx4 v12,s[40:43],s61 offen offset:0  lds
s_add_u32 m0,s88,3072
ds_read_b64_tr_b16 a[146:147],v14 offset:33280
buffer_load_dwordx4 v13,s[40:43],s61 offen offset:0  lds
ds_read_b64_tr_b16 a[148:149],v14 offset:32776
ds_read_b64_tr_b16 a[150:151],v14 offset:33288
ds_read_b64_tr_b16 a[152:153],v16 offset:32768
ds_read_b64_tr_b16 a[154:155],v16 offset:33280
ds_read_b64_tr_b16 a[156:157],v16 offset:32776
ds_read_b64_tr_b16 a[158:159],v16 offset:33288
s_waitcnt lgkmcnt(8)
s_waitcnt vmcnt(16)
s_add_u32 m0,s88,16384
s_add_u32 s61,s82,983040
buffer_load_dwordx4 v12,s[40:43],s61 offen offset:0  lds
s_add_u32 m0,s88,17408
ds_read_b64_tr_b16 a[160:161],v28 offset:0
buffer_load_dwordx4 v13,s[40:43],s61 offen offset:0  lds
s_add_u32 m0,s88,18432
s_add_u32 s61,s82,1015808
buffer_load_dwordx4 v12,s[40:43],s61 offen offset:0  lds
s_add_u32 m0,s88,19456
ds_read_b64_tr_b16 a[162:163],v28 offset:512
buffer_load_dwordx4 v13,s[40:43],s61 offen offset:0  lds
ds_read_b64_tr_b16 a[164:165],v28 offset:8
ds_read_b64_tr_b16 a[166:167],v28 offset:520
ds_read_b64_tr_b16 a[168:169],v30 offset:0
ds_read_b64_tr_b16 a[170:171],v30 offset:512
ds_read_b64_tr_b16 a[172:173],v30 offset:8
ds_read_b64_tr_b16 a[174:175],v30 offset:520
s_waitcnt lgkmcnt(8)
s_waitcnt vmcnt(16)
s_add_u32 m0,s88,32768
s_add_u32 s61,s82,1048576
buffer_load_dwordx4 v12,s[40:43],s61 offen offset:0  lds
s_add_u32 m0,s88,33792
ds_read_b64_tr_b16 a[176:177],v28 offset:16384
buffer_load_dwordx4 v13,s[40:43],s61 offen offset:0  lds
s_add_u32 m0,s88,34816
s_add_u32 s61,s82,1081344
buffer_load_dwordx4 v12,s[40:43],s61 offen offset:0  lds
s_add_u32 m0,s88,35840
ds_read_b64_tr_b16 a[178:179],v28 offset:16896
buffer_load_dwordx4 v13,s[40:43],s61 offen offset:0  lds
ds_read_b64_tr_b16 a[180:181],v28 offset:16392
ds_read_b64_tr_b16 a[182:183],v28 offset:16904
ds_read_b64_tr_b16 a[184:185],v30 offset:16384
ds_read_b64_tr_b16 a[186:187],v30 offset:16896
ds_read_b64_tr_b16 a[188:189],v30 offset:16392
ds_read_b64_tr_b16 a[190:191],v30 offset:16904
s_waitcnt lgkmcnt(8)
s_waitcnt vmcnt(16)
s_add_u32 m0,s88,49152
s_add_u32 s61,s82,1114112
buffer_load_dwordx4 v12,s[40:43],s61 offen offset:0  lds
s_add_u32 m0,s88,50176
ds_read_b64_tr_b16 a[192:193],v28 offset:32768
buffer_load_dwordx4 v13,s[40:43],s61 offen offset:0  lds
s_add_u32 m0,s88,51200
s_add_u32 s61,s82,1146880
buffer_load_dwordx4 v12,s[40:43],s61 offen offset:0  lds
s_add_u32 m0,s88,52224
ds_read_b64_tr_b16 a[194:195],v28 offset:33280
buffer_load_dwordx4 v13,s[40:43],s61 offen offset:0  lds
ds_read_b64_tr_b16 a[196:197],v28 offset:32776
ds_read_b64_tr_b16 a[198:199],v28 offset:33288
ds_read_b64_tr_b16 a[200:201],v30 offset:32768
ds_read_b64_tr_b16 a[202:203],v30 offset:33280
ds_read_b64_tr_b16 a[204:205],v30 offset:32776
ds_read_b64_tr_b16 a[206:207],v30 offset:33288
s_waitcnt lgkmcnt(8)
s_waitcnt vmcnt(16)
s_add_u32 m0,s88,65536
s_add_u32 s61,s82,1179648
buffer_load_dwordx4 v12,s[40:43],s61 offen offset:0  lds
s_add_u32 m0,s88,66560
ds_read_b64_tr_b16 a[208:209],v32 offset:0
buffer_load_dwordx4 v13,s[40:43],s61 offen offset:0  lds
s_add_u32 m0,s88,67584
s_add_u32 s61,s82,1212416
buffer_load_dwordx4 v12,s[40:43],s61 offen offset:0  lds
s_add_u32 m0,s88,68608
ds_read_b64_tr_b16 a[210:211],v32 offset:512
buffer_load_dwordx4 v13,s[40:43],s61 offen offset:0  lds
ds_read_b64_tr_b16 a[212:213],v32 offset:8
ds_read_b64_tr_b16 a[214:215],v32 offset:520
ds_read_b64_tr_b16 a[216:217],v34 offset:0
ds_read_b64_tr_b16 a[218:219],v34 offset:512
ds_read_b64_tr_b16 a[220:221],v34 offset:8
ds_read_b64_tr_b16 a[222:223],v34 offset:520
s_waitcnt lgkmcnt(8)
s_waitcnt vmcnt(16)
s_add_u32 m0,s88,81920
s_add_u32 s61,s82,1245184
buffer_load_dwordx4 v12,s[40:43],s61 offen offset:0  lds
s_add_u32 m0,s88,82944
ds_read_b64_tr_b16 a[224:225],v14 offset:0
buffer_load_dwordx4 v13,s[40:43],s61 offen offset:0  lds
s_add_u32 m0,s88,83968
s_add_u32 s61,s82,1277952
buffer_load_dwordx4 v12,s[40:43],s61 offen offset:0  lds
s_add_u32 m0,s88,84992
ds_read_b64_tr_b16 a[226:227],v14 offset:512
buffer_load_dwordx4 v13,s[40:43],s61 offen offset:0  lds
ds_read_b64_tr_b16 a[228:229],v14 offset:8
ds_read_b64_tr_b16 a[230:231],v14 offset:520
ds_read_b64_tr_b16 a[232:233],v16 offset:0
ds_read_b64_tr_b16 a[234:235],v16 offset:512
ds_read_b64_tr_b16 a[236:237],v16 offset:8
ds_read_b64_tr_b16 a[238:239],v16 offset:520
s_waitcnt lgkmcnt(8)
s_waitcnt vmcnt(16)
s_add_u32 m0,s88,98304
s_add_u32 s61,s82,1310720
buffer_load_dwordx4 v12,s[40:43],s61 offen offset:0  lds)"
R"(
s_add_u32 m0,s88,99328
ds_read_b64_tr_b16 a[240:241],v14 offset:16384
buffer_load_dwordx4 v13,s[40:43],s61 offen offset:0  lds
s_add_u32 m0,s88,100352
s_add_u32 s61,s82,1343488
buffer_load_dwordx4 v12,s[40:43],s61 offen offset:0  lds
s_add_u32 m0,s88,101376
ds_read_b64_tr_b16 a[242:243],v14 offset:16896
buffer_load_dwordx4 v13,s[40:43],s61 offen offset:0  lds
ds_read_b64_tr_b16 a[244:245],v14 offset:16392
ds_read_b64_tr_b16 a[246:247],v14 offset:16904
ds_read_b64_tr_b16 a[248:249],v16 offset:16384
ds_read_b64_tr_b16 a[250:251],v16 offset:16896
ds_read_b64_tr_b16 a[252:253],v16 offset:16392
ds_read_b64_tr_b16 a[254:255],v16 offset:16904
s_waitcnt lgkmcnt(8)
s_waitcnt vmcnt(16)
s_add_u32 m0,s88,0
s_add_u32 s61,s82,1376256
buffer_load_dwordx4 v12,s[40:43],s61 offen offset:0  lds
s_add_u32 m0,s88,1024
ds_read_b64_tr_b16 v[100:101],v14 offset:32768
buffer_load_dwordx4 v13,s[40:43],s61 offen offset:0  lds
s_add_u32 m0,s88,2048
s_add_u32 s61,s82,1409024
buffer_load_dwordx4 v12,s[40:43],s61 offen offset:0  lds
s_add_u32 m0,s88,3072
ds_read_b64_tr_b16 v[102:103],v14 offset:33280
buffer_load_dwordx4 v13,s[40:43],s61 offen offset:0  lds
ds_read_b64_tr_b16 v[104:105],v14 offset:32776
ds_read_b64_tr_b16 v[106:107],v14 offset:33288
ds_read_b64_tr_b16 v[108:109],v16 offset:32768
ds_read_b64_tr_b16 v[110:111],v16 offset:33280
ds_read_b64_tr_b16 v[112:113],v16 offset:32776
ds_read_b64_tr_b16 v[114:115],v16 offset:33288
s_waitcnt lgkmcnt(8)
s_waitcnt vmcnt(16)
s_add_u32 m0,s88,16384
s_add_u32 s61,s82,1441792
buffer_load_dwordx4 v12,s[40:43],s61 offen offset:0  lds
s_add_u32 m0,s88,17408
ds_read_b64_tr_b16 v[116:117],v28 offset:0
buffer_load_dwordx4 v13,s[40:43],s61 offen offset:0  lds
s_add_u32 m0,s88,18432
s_add_u32 s61,s82,1474560
buffer_load_dwordx4 v12,s[40:43],s61 offen offset:0  lds
s_add_u32 m0,s88,19456
ds_read_b64_tr_b16 v[118:119],v28 offset:512
buffer_load_dwordx4 v13,s[40:43],s61 offen offset:0  lds
ds_read_b64_tr_b16 v[120:121],v28 offset:8
ds_read_b64_tr_b16 v[122:123],v28 offset:520
ds_read_b64_tr_b16 v[124:125],v30 offset:0
ds_read_b64_tr_b16 v[126:127],v30 offset:512
ds_read_b64_tr_b16 v[128:129],v30 offset:8
ds_read_b64_tr_b16 v[130:131],v30 offset:520
s_waitcnt lgkmcnt(8)
s_waitcnt vmcnt(16)
s_add_u32 m0,s88,32768
s_add_u32 s61,s82,1507328
buffer_load_dwordx4 v12,s[40:43],s61 offen offset:0  lds
s_add_u32 m0,s88,33792
ds_read_b64_tr_b16 v[132:133],v28 offset:16384
buffer_load_dwordx4 v13,s[40:43],s61 offen offset:0  lds
s_add_u32 m0,s88,34816
s_add_u32 s61,s82,1540096
buffer_load_dwordx4 v12,s[40:43],s61 offen offset:0  lds
s_add_u32 m0,s88,35840
ds_read_b64_tr_b16 v[134:135],v28 offset:16896
buffer_load_dwordx4 v13,s[40:43],s61 offen offset:0  lds
ds_read_b64_tr_b16 v[136:137],v28 offset:16392
ds_read_b64_tr_b16 v[138:139],v28 offset:16904
ds_read_b64_tr_b16 v[140:141],v30 offset:16384
ds_read_b64_tr_b16 v[142:143],v30 offset:16896
ds_read_b64_tr_b16 v[144:145],v30 offset:16392
ds_read_b64_tr_b16 v[146:147],v30 offset:16904
s_waitcnt lgkmcnt(8)
s_waitcnt vmcnt(16)
s_add_u32 m0,s88,49152
s_add_u32 s61,s82,1572864
buffer_load_dwordx4 v12,s[40:43],s61 offen offset:0  lds
s_add_u32 m0,s88,50176
ds_read_b64_tr_b16 v[148:149],v28 offset:32768
buffer_load_dwordx4 v13,s[40:43],s61 offen offset:0  lds
s_add_u32 m0,s88,51200
s_add_u32 s61,s82,1605632
buffer_load_dwordx4 v12,s[40:43],s61 offen offset:0  lds
s_add_u32 m0,s88,52224
ds_read_b64_tr_b16 v[150:151],v28 offset:33280
buffer_load_dwordx4 v13,s[40:43],s61 offen offset:0  lds
ds_read_b64_tr_b16 v[152:153],v28 offset:32776
ds_read_b64_tr_b16 v[154:155],v28 offset:33288
ds_read_b64_tr_b16 v[156:157],v30 offset:32768
ds_read_b64_tr_b16 v[158:159],v30 offset:33280
ds_read_b64_tr_b16 v[52:53],v30 offset:32776
ds_read_b64_tr_b16 v[54:55],v30 offset:33288
s_waitcnt lgkmcnt(8)
s_waitcnt vmcnt(16)
s_add_u32 m0,s88,65536
s_add_u32 s61,s82,1638400
buffer_load_dwordx4 v12,s[40:43],s61 offen offset:0  lds
s_add_u32 m0,s88,66560
ds_read_b64_tr_b16 v[160:161],v32 offset:0
buffer_load_dwordx4 v13,s[40:43],s61 offen offset:0  lds
s_add_u32 m0,s88,67584
s_add_u32 s61,s82,1671168
buffer_load_dwordx4 v12,s[40:43],s61 offen offset:0  lds
s_add_u32 m0,s88,68608
ds_read_b64_tr_b16 v[162:163],v32 offset:512
buffer_load_dwordx4 v13,s[40:43],s61 offen offset:0  lds
ds_read_b64_tr_b16 v[164:165],v32 offset:8
ds_read_b64_tr_b16 v[166:167],v32 offset:520
ds_read_b64_tr_b16 v[168:169],v34 offset:0
ds_read_b64_tr_b16 v[170:171],v34 offset:512
ds_read_b64_tr_b16 v[44:45],v34 offset:8
ds_read_b64_tr_b16 v[46:47],v34 offset:520
s_waitcnt lgkmcnt(8)
ds_write_b128 v10,v[52:55] offset:0
s_waitcnt vmcnt(16)
s_add_u32 m0,s88,81920
s_add_u32 s61,s82,1703936
buffer_load_dwordx4 v12,s[40:43],s61 offen offset:0  lds
s_add_u32 m0,s88,82944
ds_read_b64_tr_b16 v[172:173],v14 offset:0
buffer_load_dwordx4 v13,s[40:43],s61 offen offset:0  lds
s_add_u32 m0,s88,83968
s_add_u32 s61,s82,1736704
buffer_load_dwordx4 v12,s[40:43],s61 offen offset:0  lds
s_add_u32 m0,s88,84992
ds_read_b64_tr_b16 v[174:175],v14 offset:512
buffer_load_dwordx4 v13,s[40:43],s61 offen offset:0  lds
ds_read_b64_tr_b16 v[176:177],v14 offset:8
ds_read_b64_tr_b16 v[178:179],v14 offset:520
ds_read_b64_tr_b16 v[180:181],v16 offset:0
ds_read_b64_tr_b16 v[182:183],v16 offset:512
ds_read_b64_tr_b16 v[52:53],v16 offset:8
ds_read_b64_tr_b16 v[54:55],v16 offset:520
s_waitcnt lgkmcnt(8)
ds_write_b128 v10,v[44:47] offset:1024
s_waitcnt vmcnt(16)
s_add_u32 m0,s88,98304
s_add_u32 s61,s82,1769472
buffer_load_dwordx4 v12,s[40:43],s61 offen offset:0  lds
s_add_u32 m0,s88,99328
ds_read_b64_tr_b16 v[184:185],v14 offset:16384
buffer_load_dwordx4 v13,s[40:43],s61 offen offset:0  lds
s_add_u32 m0,s88,100352
s_add_u32 s61,s82,1802240
buffer_load_dwordx4 v12,s[40:43],s61 offen offset:0  lds
s_add_u32 m0,s88,101376
ds_read_b64_tr_b16 v[186:187],v14 offset:16896
buffer_load_dwordx4 v13,s[40:43],s61 offen offset:0  lds
ds_read_b64_tr_b16 v[188:189],v14 offset:16392
ds_read_b64_tr_b16 v[190:191],v14 offset:16904
ds_read_b64_tr_b16 v[192:193],v16 offset:16384
ds_read_b64_tr_b16 v[194:195],v16 offset:16896
ds_read_b64_tr_b16 v[44:45],v16 offset:16392
ds_read_b64_tr_b16 v[46:47],v16 offset:16904
s_waitcnt lgkmcnt(8)
ds_write_b128 v10,v[52:55] offset:2048
s_waitcnt vmcnt(16)
ds_read_b64_tr_b16 v[196:197],v14 offset:32768
ds_read_b64_tr_b16 v[198:199],v14 offset:33280
ds_read_b64_tr_b16 v[200:201],v14 offset:32776
ds_read_b64_tr_b16 v[202:203],v14 offset:33288
ds_read_b64_tr_b16 v[204:205],v16 offset:32768
ds_read_b64_tr_b16 v[206:207],v16 offset:33280
ds_read_b64_tr_b16 v[52:53],v16 offset:32776
ds_read_b64_tr_b16 v[54:55],v16 offset:33288
s_getpc_b64 s[46:47]
Lg4_cp_%=:
s_add_u32 s46,s46,Lg4_init_%=-Lg4_cp_%=
s_addc_u32 s47,s47,0
s_lshl_b32 s68,s54,13
v_lshl_add_u32 v76,v2,3,s68
v_min_u32 v77,Lg4_done_%=-Lg4_init_%=,v76
global_load_dword v80,v77,s[46:47]
s_waitcnt lgkmcnt(8)
ds_write_b128 v10,v[44:47] offset:3072
s_waitcnt vmcnt(13)
ds_read_b64_tr_b16 v[208:209],v28 offset:0
ds_read_b64_tr_b16 v[210:211],v28 offset:512
ds_read_b64_tr_b16 v[212:213],v28 offset:8
ds_read_b64_tr_b16 v[214:215],v28 offset:520
ds_read_b64_tr_b16 v[216:217],v30 offset:0
ds_read_b64_tr_b16 v[218:219],v30 offset:512
ds_read_b64_tr_b16 v[44:45],v30 offset:8
ds_read_b64_tr_b16 v[46:47],v30 offset:520
s_waitcnt lgkmcnt(8)
ds_write_b128 v10,v[52:55] offset:4096
s_waitcnt vmcnt(9)
ds_read_b64_tr_b16 v[220:221],v28 offset:16384
ds_read_b64_tr_b16 v[222:223],v28 offset:16896
ds_read_b64_tr_b16 v[224:225],v28 offset:16392
ds_read_b64_tr_b16 v[226:227],v28 offset:16904
ds_read_b64_tr_b16 v[228:229],v30 offset:16384
ds_read_b64_tr_b16 v[230:231],v30 offset:16896
ds_read_b64_tr_b16 v[52:53],v30 offset:16392
ds_read_b64_tr_b16 v[54:55],v30 offset:16904
s_waitcnt lgkmcnt(8)
ds_write_b128 v10,v[44:47] offset:5120)"
R"(
s_waitcnt vmcnt(5)
ds_read_b64_tr_b16 v[232:233],v28 offset:32768
ds_read_b64_tr_b16 v[234:235],v28 offset:33280
ds_read_b64_tr_b16 v[236:237],v28 offset:32776
ds_read_b64_tr_b16 v[238:239],v28 offset:33288
ds_read_b64_tr_b16 v[240:241],v30 offset:32768
ds_read_b64_tr_b16 v[242:243],v30 offset:33280
ds_read_b64_tr_b16 v[44:45],v30 offset:32776
ds_read_b64_tr_b16 v[46:47],v30 offset:33288
s_waitcnt lgkmcnt(8)
ds_write_b128 v10,v[52:55] offset:6144
s_waitcnt vmcnt(1)
ds_read_b64_tr_b16 v[244:245],v32 offset:0
ds_read_b64_tr_b16 v[246:247],v32 offset:512
ds_read_b64_tr_b16 v[248:249],v32 offset:8
ds_read_b64_tr_b16 v[250:251],v32 offset:520
ds_read_b64_tr_b16 v[252:253],v34 offset:0
ds_read_b64_tr_b16 v[254:255],v34 offset:512
ds_read_b64_tr_b16 v[52:53],v34 offset:8
ds_read_b64_tr_b16 v[54:55],v34 offset:520
s_waitcnt lgkmcnt(8)
ds_write_b128 v10,v[44:47] offset:7168
s_waitcnt lgkmcnt(0)
ds_write_b128 v10,v[52:55] offset:8192
s_waitcnt lgkmcnt(0)
s_barrier
Lg4_init_%=:
v_mov_b32 v6,s60
s_mov_b32 s59,536870912
s_add_u32 s57,s56,131072
s_mov_b32 s79,0
s_mov_b32 s75,0x3a800000
s_mov_b32 s76,0xc0135761
s_mov_b32 s77,0x3d372713
s_mov_b32 s44,s59
s_mov_b32 s45,s59
s_mov_b32 s46,s59
s_mov_b32 s47,s59
s_mov_b32 s48,s83
s_add_u32 s49,s48,s55
s_cmp_eq_u32 s49,s56
s_cselect_b32 s49,s57,s49
s_cmp_eq_u32 s49,s58
s_cselect_b32 s49,s59,s49
s_cmp_ge_u32 s49,6520832
s_cselect_b32 s55,196608,s55
s_add_u32 s50,s49,s55
s_cmp_eq_u32 s50,s56
s_cselect_b32 s50,s57,s50
s_cmp_eq_u32 s50,s58
s_cselect_b32 s50,s59,s50
s_cmp_ge_u32 s50,6520832
s_cselect_b32 s55,196608,s55
s_add_u32 s51,s50,s55
s_cmp_eq_u32 s51,s56
s_cselect_b32 s51,s57,s51
s_cmp_eq_u32 s51,s58
s_cselect_b32 s51,s59,s51
s_cmp_ge_u32 s51,6520832
s_cselect_b32 s55,196608,s55
s_add_u32 s52,s51,s55
s_cmp_eq_u32 s52,s56
s_cselect_b32 s52,s57,s52
s_cmp_eq_u32 s52,s58
s_cselect_b32 s52,s59,s52
s_cmp_ge_u32 s52,6520832
s_cselect_b32 s55,196608,s55
s_lshl_b32 s63,s54,12
s_mul_i32 s64,s54,3072
s_add_u32 s64,s64,16384
s_lshl_b32 s78,s54,8
s_add_u32 s78,s78,65536
s_getpc_b64 s[90:91]
Lg4_pc_%=:
s_mov_b32 s68,Lg4_d1w0_%=-Lg4_pc_%=
s_cmp_eq_u32 s54,1
s_cselect_b32 s68,Lg4_d1w1_%=-Lg4_pc_%=,s68
s_cmp_eq_u32 s54,2
s_cselect_b32 s68,Lg4_d1w2_%=-Lg4_pc_%=,s68
s_cmp_eq_u32 s54,3
s_cselect_b32 s68,Lg4_d1w3_%=-Lg4_pc_%=,s68
s_add_u32 s92,s90,s68
s_addc_u32 s93,s91,0
s_mov_b32 s68,Lg4_d0w0_%=-Lg4_pc_%=
s_cmp_eq_u32 s54,1
s_cselect_b32 s68,Lg4_d0w1_%=-Lg4_pc_%=,s68
s_cmp_eq_u32 s54,2
s_cselect_b32 s68,Lg4_d0w2_%=-Lg4_pc_%=,s68
s_cmp_eq_u32 s54,3
s_cselect_b32 s68,Lg4_d0w3_%=-Lg4_pc_%=,s68
s_add_u32 s90,s90,s68
s_addc_u32 s91,s91,0
s_mov_b32 s66,s59
s_mov_b32 s65,0
s_mov_b32 s62,s63
s_branch Lg4_slowb0_%=
Lg4_d0w0_%=:
v_mfma_f32_16x16x32_bf16 v[12:15],a[0:3],v[44:47],0
s_mov_b32 s73,s47
ds_read_b128 v[56:59],v2 offset:3072
v_lshlrev_b32 v60,16,v68
v_mfma_f32_16x16x32_bf16 v[16:19],a[4:7],v[44:47],0
s_max_u32 s73,s73,s65
v_and_b32 v61,0xffff0000,v68
v_lshlrev_b32 v62,16,v69
v_mfma_f32_16x16x32_bf16 v[20:23],a[8:11],v[44:47],0
s_mov_b32 s65,0
v_and_b32 v63,0xffff0000,v69
v_lshlrev_b32 v64,16,v70
v_mfma_f32_16x16x32_bf16 v[24:27],a[12:15],v[44:47],0
s_waitcnt lgkmcnt(2)
v_and_b32 v65,0xffff0000,v70
v_lshlrev_b32 v66,16,v71
v_mfma_f32_16x16x32_bf16 v[12:15],a[16:19],v[48:51],v[12:15]
s_mul_i32 s61,s49,7
ds_read_b128 v[44:47],v2 offset:4096
v_and_b32 v67,0xffff0000,v71
v_mfma_f32_16x16x32_bf16 v[16:19],a[20:23],v[48:51],v[16:19]
s_lshr_b32 s61,s61,1
v_cvt_pk_bf16_f32 v28,v28,v29
v_cvt_pk_bf16_f32 v29,v30,v31
v_mfma_f32_16x16x32_bf16 v[20:23],a[24:27],v[48:51],v[20:23]
v_cvt_pk_bf16_f32 v30,v32,v33
v_cvt_pk_bf16_f32 v31,v34,v35
v_mfma_f32_16x16x32_bf16 v[24:27],a[28:31],v[48:51],v[24:27]
s_waitcnt lgkmcnt(2)
v_add_f32 v36,v36,v60
v_add_f32 v37,v37,v61
v_mfma_f32_16x16x32_bf16 v[12:15],a[32:35],v[52:55],v[12:15]
buffer_store_dwordx4 v[28:31],v3,s[36:39],s73 offen offset:0 sc1
ds_read_b128 v[48:51],v2 offset:5120
v_add_f32 v38,v38,v62
v_add_f32 v39,v39,v63
v_mfma_f32_16x16x32_bf16 v[16:19],a[36:39],v[52:55],v[16:19]
v_add_f32 v40,v40,v64
v_add_f32 v41,v41,v65
v_mfma_f32_16x16x32_bf16 v[20:23],a[40:43],v[52:55],v[20:23]
v_add_f32 v42,v42,v66
v_add_f32 v43,v43,v67
v_mfma_f32_16x16x32_bf16 v[24:27],a[44:47],v[52:55],v[24:27]
s_waitcnt lgkmcnt(2)
v_cvt_pk_bf16_f32 v36,v36,v37
v_cvt_pk_bf16_f32 v37,v38,v39
v_mfma_f32_16x16x32_bf16 v[12:15],a[48:51],v[56:59],v[12:15]
ds_read_b128 v[52:55],v2 offset:6144
v_cvt_pk_bf16_f32 v38,v40,v41
v_mfma_f32_16x16x32_bf16 v[16:19],a[52:55],v[56:59],v[16:19]
v_cvt_pk_bf16_f32 v39,v42,v43
v_mfma_f32_16x16x32_bf16 v[20:23],a[56:59],v[56:59],v[20:23]
s_waitcnt vmcnt(1) lgkmcnt(2)
v_mfma_f32_16x16x32_bf16 v[24:27],a[60:63],v[56:59],v[24:27]
s_barrier
v_mfma_f32_16x16x32_bf16 v[12:15],a[64:67],v[44:47],v[12:15]
ds_read_b32 v11,v8 offset:4096
ds_read_b128 v[56:59],v2 offset:7168
v_mfma_f32_16x16x32_bf16 v[16:19],a[68:71],v[44:47],v[16:19]
v_mfma_f32_16x16x32_bf16 v[20:23],a[72:75],v[44:47],v[20:23]
v_mfma_f32_16x16x32_bf16 v[24:27],a[76:79],v[44:47],v[24:27]
s_waitcnt lgkmcnt(3)
v_mfma_f32_16x16x32_bf16 v[12:15],a[80:83],v[48:51],v[12:15]
s_waitcnt lgkmcnt(1)
buffer_store_dwordx4 v[36:39],v4,s[36:39],s73 offen offset:0 sc1
v_cmp_eq_u32 vcc,s60,v11
v_mfma_f32_16x16x32_bf16 v[16:19],a[84:87],v[48:51],v[16:19]
s_cmp_eq_u32 vcc_lo,-1
ds_read_b128 v[44:47],v2 offset:8192
s_cselect_b32 s66,s49,s59
v_mfma_f32_16x16x32_bf16 v[20:23],a[88:91],v[48:51],v[20:23]
s_cselect_b32 s61,s61,s59
s_add_u32 m0,s63,28672
s_cmp_ge_u32 s66,s59
v_mfma_f32_16x16x32_bf16 v[24:27],a[92:95],v[48:51],v[24:27]
s_cselect_b32 s68,s49,s59
s_mul_i32 s68,s68,5
v_mfma_f32_16x16x32_bf16 v[12:15],a[96:99],v[52:55],v[12:15]
buffer_load_dwordx4 v0,s[36:39],s61 offen offset:0 sc1 lds
v_mfma_f32_16x16x32_bf16 v[16:19],a[100:103],v[52:55],v[16:19]
s_lshr_b32 s68,s68,6
ds_read_b128 v[48:51],v2 offset:9216
v_mfma_f32_16x16x32_bf16 v[20:23],a[104:107],v[52:55],v[20:23]
v_mfma_f32_16x16x32_bf16 v[24:27],a[108:111],v[52:55],v[24:27]
s_waitcnt lgkmcnt(2)
v_mfma_f32_16x16x32_bf16 v[12:15],a[112:115],v[56:59],v[12:15]
buffer_load_dwordx4 v0,s[36:39],s61 offen offset:1024 sc1 lds
v_mfma_f32_16x16x32_bf16 v[16:19],a[116:119],v[56:59],v[16:19]
ds_read_b128 v[52:55],v2 offset:10240
v_mfma_f32_16x16x32_bf16 v[20:23],a[120:123],v[56:59],v[20:23]
v_mfma_f32_16x16x32_bf16 v[24:27],a[124:127],v[56:59],v[24:27]
s_waitcnt lgkmcnt(2)
v_mfma_f32_16x16x32_bf16 v[12:15],a[128:131],v[44:47],v[12:15]
buffer_load_dwordx4 v0,s[36:39],s61 offen offset:2048 sc1 lds
v_mfma_f32_16x16x32_bf16 v[16:19],a[132:135],v[44:47],v[16:19]
ds_read_b128 v[56:59],v2 offset:11264
v_mfma_f32_16x16x32_bf16 v[20:23],a[136:139],v[44:47],v[20:23]
v_mfma_f32_16x16x32_bf16 v[24:27],a[140:143],v[44:47],v[24:27]
s_waitcnt lgkmcnt(2)
v_mfma_f32_16x16x32_bf16 v[12:15],a[144:147],v[48:51],v[12:15]
buffer_load_dwordx4 v0,s[36:39],s61 offen offset:3072 sc1 lds
v_mfma_f32_16x16x32_bf16 v[16:19],a[148:151],v[48:51],v[16:19]
s_add_u32 m0,s78,6144)"
R"(
ds_read_b128 v[44:47],v2 offset:12288
s_add_u32 s53,s52,s55
v_mfma_f32_16x16x32_bf16 v[20:23],a[152:155],v[48:51],v[20:23]
s_cmp_eq_u32 s53,s56
s_cselect_b32 s53,s57,s53
s_cmp_eq_u32 s53,s58
v_mfma_f32_16x16x32_bf16 v[24:27],a[156:159],v[48:51],v[24:27]
s_waitcnt lgkmcnt(2)
s_cselect_b32 s53,s59,s53
s_cmp_ge_u32 s53,6520832
v_mfma_f32_16x16x32_bf16 v[12:15],a[160:163],v[52:55],v[12:15]
buffer_load_dwordx4 v[68:71],v9,s[36:39],s48 offen offset:0 sc1
v_mfma_f32_16x16x32_bf16 v[16:19],a[164:167],v[52:55],v[16:19]
s_cselect_b32 s55,196608,s55
ds_read_b128 v[48:51],v2 offset:13312
v_mfma_f32_16x16x32_bf16 v[20:23],a[168:171],v[52:55],v[20:23]
v_mfma_f32_16x16x32_bf16 v[24:27],a[172:175],v[52:55],v[24:27]
s_waitcnt lgkmcnt(2)
v_mfma_f32_16x16x32_bf16 v[12:15],a[176:179],v[56:59],v[12:15]
ds_read_b128 v[52:55],v2 offset:14336
v_mfma_f32_16x16x32_bf16 v[16:19],a[180:183],v[56:59],v[16:19]
buffer_load_dword v7,s[36:39],s68 offen offset:0 sc1 lds
v_mfma_f32_16x16x32_bf16 v[20:23],a[184:187],v[56:59],v[20:23]
s_add_u32 m0,s64,28672
s_mul_i32 s68,s50,5
s_lshr_b32 s68,s68,6
v_mfma_f32_16x16x32_bf16 v[24:27],a[188:191],v[56:59],v[24:27]
s_waitcnt lgkmcnt(2)
v_mfma_f32_16x16x32_bf16 v[12:15],a[192:195],v[44:47],v[12:15]
ds_read_b128 v[56:59],v2 offset:15360
v_mfma_f32_16x16x32_bf16 v[16:19],a[196:199],v[44:47],v[16:19]
v_mfma_f32_16x16x32_bf16 v[20:23],a[200:203],v[44:47],v[20:23]
s_waitcnt vmcnt(9) lgkmcnt(2)
v_mfma_f32_16x16x32_bf16 v[24:27],a[204:207],v[44:47],v[24:27]
s_barrier
v_mfma_f32_16x16x32_bf16 v[12:15],a[208:211],v[48:51],v[12:15]
ds_read_b128 v[44:47],v2 offset:16384
v_mfma_f32_16x16x32_bf16 v[16:19],a[212:215],v[48:51],v[16:19]
buffer_load_dwordx4 v1,s[36:39],s61 offen offset:0 sc1 lds
v_mfma_f32_16x16x32_bf16 v[20:23],a[216:219],v[48:51],v[20:23]
v_mfma_f32_16x16x32_bf16 v[24:27],a[220:223],v[48:51],v[24:27]
s_waitcnt lgkmcnt(2)
v_mfma_f32_16x16x32_bf16 v[12:15],a[224:227],v[52:55],v[12:15]
ds_read_b128 v[48:51],v2 offset:17408
v_mfma_f32_16x16x32_bf16 v[16:19],a[228:231],v[52:55],v[16:19]
v_mfma_f32_16x16x32_bf16 v[20:23],a[232:235],v[52:55],v[20:23]
v_mfma_f32_16x16x32_bf16 v[24:27],a[236:239],v[52:55],v[24:27]
s_waitcnt lgkmcnt(2)
buffer_load_dwordx4 v1,s[36:39],s61 offen offset:1024 sc1 lds
v_mfma_f32_16x16x32_bf16 v[12:15],a[240:243],v[56:59],v[12:15]
ds_read_b128 v[52:55],v2 offset:18432
v_mfma_f32_16x16x32_bf16 v[16:19],a[244:247],v[56:59],v[16:19]
v_mfma_f32_16x16x32_bf16 v[20:23],a[248:251],v[56:59],v[20:23]
v_mfma_f32_16x16x32_bf16 v[24:27],a[252:255],v[56:59],v[24:27]
s_waitcnt lgkmcnt(2)
v_mfma_f32_16x16x32_bf16 v[12:15],v[100:103],v[44:47],v[12:15]
ds_read_b128 v[56:59],v2 offset:19456
v_mfma_f32_16x16x32_bf16 v[16:19],v[104:107],v[44:47],v[16:19]
buffer_load_dwordx4 v1,s[36:39],s61 offen offset:2048 sc1 lds
v_mfma_f32_16x16x32_bf16 v[20:23],v[108:111],v[44:47],v[20:23]
s_add_u32 m0,s78,5120
ds_read_b128 v[88:91],v10 offset:0
s_cmp_ge_u32 s66,s59
v_mfma_f32_16x16x32_bf16 v[24:27],v[112:115],v[44:47],v[24:27]
s_waitcnt lgkmcnt(3)
v_mfma_f32_16x16x32_bf16 v[12:15],v[116:119],v[48:51],v[12:15]
ds_read_b128 v[44:47],v2 offset:20480
v_mfma_f32_16x16x32_bf16 v[16:19],v[120:123],v[48:51],v[16:19]
ds_read_b128 v[76:79],v10 offset:1024
v_mfma_f32_16x16x32_bf16 v[20:23],v[124:127],v[48:51],v[20:23]
v_mfma_f32_16x16x32_bf16 v[24:27],v[128:131],v[48:51],v[24:27]
s_waitcnt lgkmcnt(4)
v_mfma_f32_16x16x32_bf16 v[12:15],v[132:135],v[52:55],v[12:15]
buffer_load_dword v7,s[36:39],s68 offen offset:0 sc1 lds
v_mfma_f32_16x16x32_bf16 v[16:19],v[136:139],v[52:55],v[16:19]
ds_read_b128 v[48:51],v2 offset:21504
ds_read_b128 v[80:83],v10 offset:2048
v_mfma_f32_16x16x32_bf16 v[20:23],v[140:143],v[52:55],v[20:23]
v_mfma_f32_16x16x32_bf16 v[24:27],v[144:147],v[52:55],v[24:27]
s_waitcnt lgkmcnt(4)
v_mfma_f32_16x16x32_bf16 v[12:15],v[148:151],v[56:59],v[12:15]
ds_read_b128 v[52:55],v2 offset:22528
v_mfma_f32_16x16x32_bf16 v[16:19],v[152:155],v[56:59],v[16:19]
ds_read_b128 v[84:87],v10 offset:3072
v_mfma_f32_16x16x32_bf16 v[20:23],v[156:159],v[56:59],v[20:23]
v_mfma_f32_16x16x32_bf16 v[24:27],v[88:91],v[56:59],v[24:27]
s_waitcnt lgkmcnt(4)
v_mfma_f32_16x16x32_bf16 v[12:15],v[160:163],v[44:47],v[12:15]
ds_read_b128 v[56:59],v2 offset:23552
v_mfma_f32_16x16x32_bf16 v[16:19],v[164:167],v[44:47],v[16:19]
ds_read_b128 v[88:91],v10 offset:4096
v_mfma_f32_16x16x32_bf16 v[20:23],v[168:171],v[44:47],v[20:23]
v_mfma_f32_16x16x32_bf16 v[24:27],v[76:79],v[44:47],v[24:27]
s_waitcnt lgkmcnt(4)
v_mfma_f32_16x16x32_bf16 v[12:15],v[172:175],v[48:51],v[12:15]
ds_read_b128 v[44:47],v2 offset:24576
v_mfma_f32_16x16x32_bf16 v[16:19],v[176:179],v[48:51],v[16:19]
ds_read_b128 v[76:79],v10 offset:5120
v_mfma_f32_16x16x32_bf16 v[20:23],v[180:183],v[48:51],v[20:23]
s_waitcnt vmcnt(10) lgkmcnt(4)
v_mfma_f32_16x16x32_bf16 v[24:27],v[80:83],v[48:51],v[24:27]
s_barrier
v_mfma_f32_16x16x32_bf16 v[12:15],v[184:187],v[52:55],v[12:15]
ds_read_b128 v[48:51],v2 offset:25600
v_mfma_f32_16x16x32_bf16 v[16:19],v[188:191],v[52:55],v[16:19]
ds_read_b128 v[80:83],v10 offset:6144
v_mfma_f32_16x16x32_bf16 v[20:23],v[192:195],v[52:55],v[20:23]
v_mfma_f32_16x16x32_bf16 v[24:27],v[84:87],v[52:55],v[24:27]
s_waitcnt lgkmcnt(4)
v_mfma_f32_16x16x32_bf16 v[12:15],v[196:199],v[56:59],v[12:15]
ds_read_b128 v[52:55],v2 offset:26624
v_mfma_f32_16x16x32_bf16 v[16:19],v[200:203],v[56:59],v[16:19]
ds_read_b128 v[84:87],v10 offset:7168
v_mfma_f32_16x16x32_bf16 v[20:23],v[204:207],v[56:59],v[20:23]
v_mfma_f32_16x16x32_bf16 v[24:27],v[88:91],v[56:59],v[24:27]
s_waitcnt lgkmcnt(4)
v_mfma_f32_16x16x32_bf16 v[12:15],v[208:211],v[44:47],v[12:15]
ds_read_b128 v[56:59],v2 offset:27648
v_mfma_f32_16x16x32_bf16 v[16:19],v[212:215],v[44:47],v[16:19]
ds_read_b128 v[88:91],v10 offset:8192
v_mfma_f32_16x16x32_bf16 v[20:23],v[216:219],v[44:47],v[20:23]
s_waitcnt vmcnt(4) lgkmcnt(4)
v_mfma_f32_16x16x32_bf16 v[24:27],v[76:79],v[44:47],v[24:27]
s_barrier
v_mfma_f32_16x16x32_bf16 v[12:15],v[220:223],v[48:51],v[12:15]
ds_read_b128 v[44:47],v2 offset:28672
v_mfma_f32_16x16x32_bf16 v[16:19],v[224:227],v[48:51],v[16:19]
v_mfma_f32_16x16x32_bf16 v[20:23],v[228:231],v[48:51],v[20:23]
v_mfma_f32_16x16x32_bf16 v[24:27],v[80:83],v[48:51],v[24:27]
s_waitcnt lgkmcnt(3)
v_mfma_f32_16x16x32_bf16 v[12:15],v[232:235],v[52:55],v[12:15]
ds_read_b128 v[48:51],v2 offset:29696
v_mfma_f32_16x16x32_bf16 v[16:19],v[236:239],v[52:55],v[16:19]
v_mfma_f32_16x16x32_bf16 v[20:23],v[240:243],v[52:55],v[20:23]
v_mfma_f32_16x16x32_bf16 v[24:27],v[84:87],v[52:55],v[24:27]
s_waitcnt lgkmcnt(2)
v_mfma_f32_16x16x32_bf16 v[12:15],v[244:247],v[56:59],v[12:15]
ds_read_b128 v[52:55],v2 offset:30720
v_mfma_f32_16x16x32_bf16 v[16:19],v[248:251],v[56:59],v[16:19]
v_mfma_f32_16x16x32_bf16 v[20:23],v[252:255],v[56:59],v[20:23]
s_waitcnt lgkmcnt(2)
v_mfma_f32_16x16x32_bf16 v[24:27],v[88:91],v[56:59],v[24:27]
s_cbranch_scc1 Lg4_slow1_%=
Lg4_d1w0_%=:
v_mfma_f32_16x16x32_bf16 v[28:31],a[0:3],v[44:47],0
s_mov_b32 s73,s48
ds_read_b128 v[56:59],v2 offset:31744
v_lshlrev_b32 v60,16,v68
v_mfma_f32_16x16x32_bf16 v[32:35],a[4:7],v[44:47],0
s_max_u32 s73,s73,s65
v_and_b32 v61,0xffff0000,v68
v_lshlrev_b32 v62,16,v69
v_mfma_f32_16x16x32_bf16 v[36:39],a[8:11],v[44:47],0
s_mov_b32 s65,0
v_and_b32 v63,0xffff0000,v69
v_lshlrev_b32 v64,16,v70
v_mfma_f32_16x16x32_bf16 v[40:43],a[12:15],v[44:47],0
s_waitcnt lgkmcnt(2)
v_and_b32 v65,0xffff0000,v70
v_lshlrev_b32 v66,16,v71
v_mfma_f32_16x16x32_bf16 v[28:31],a[16:19],v[48:51],v[28:31]
s_mul_i32 s61,s50,7
ds_read_b128 v[44:47],v2 offset:32768
v_and_b32 v67,0xffff0000,v71
v_mfma_f32_16x16x32_bf16 v[32:35],a[20:23],v[48:51],v[32:35]
s_lshr_b32 s61,s61,1
v_cvt_pk_bf16_f32 v12,v12,v13
v_cvt_pk_bf16_f32 v13,v14,v15
v_mfma_f32_16x16x32_bf16 v[36:39],a[24:27],v[48:51],v[36:39]
s_mov_b64 s[44:45],s[46:47]
v_cvt_pk_bf16_f32 v14,v16,v17
v_cvt_pk_bf16_f32 v15,v18,v19
v_mfma_f32_16x16x32_bf16 v[40:43],a[28:31],v[48:51],v[40:43]
s_waitcnt lgkmcnt(2)
v_add_f32 v20,v20,v60
v_add_f32 v21,v21,v61
v_mfma_f32_16x16x32_bf16 v[28:31],a[32:35],v[52:55],v[28:31]
buffer_store_dwordx4 v[12:15],v3,s[36:39],s73 offen offset:0 sc1
ds_read_b128 v[48:51],v2 offset:33792
v_add_f32 v22,v22,v62
v_add_f32 v23,v23,v63
v_mfma_f32_16x16x32_bf16 v[32:35],a[36:39],v[52:55],v[32:35]
s_mov_b64 s[46:47],s[48:49]
v_add_f32 v24,v24,v64
v_add_f32 v25,v25,v65
v_mfma_f32_16x16x32_bf16 v[36:39],a[40:43],v[52:55],v[36:39]
v_add_f32 v26,v26,v66
v_add_f32 v27,v27,v67
v_mfma_f32_16x16x32_bf16 v[40:43],a[44:47],v[52:55],v[40:43]
s_waitcnt lgkmcnt(2)
v_cvt_pk_bf16_f32 v20,v20,v21
v_cvt_pk_bf16_f32 v21,v22,v23
v_mfma_f32_16x16x32_bf16 v[28:31],a[48:51],v[56:59],v[28:31]
ds_read_b128 v[52:55],v2 offset:34816
v_cvt_pk_bf16_f32 v22,v24,v25
v_mfma_f32_16x16x32_bf16 v[32:35],a[52:55],v[56:59],v[32:35]
v_cvt_pk_bf16_f32 v23,v26,v27
v_mfma_f32_16x16x32_bf16 v[36:39],a[56:59],v[56:59],v[36:39]
s_waitcnt vmcnt(1) lgkmcnt(2)
v_mfma_f32_16x16x32_bf16 v[40:43],a[60:63],v[56:59],v[40:43])"
R"(
s_barrier
v_mfma_f32_16x16x32_bf16 v[28:31],a[64:67],v[44:47],v[28:31]
ds_read_b32 v11,v8 offset:5120
ds_read_b128 v[56:59],v2 offset:35840
v_mfma_f32_16x16x32_bf16 v[32:35],a[68:71],v[44:47],v[32:35]
v_mfma_f32_16x16x32_bf16 v[36:39],a[72:75],v[44:47],v[36:39]
v_mfma_f32_16x16x32_bf16 v[40:43],a[76:79],v[44:47],v[40:43]
s_waitcnt lgkmcnt(3)
v_mfma_f32_16x16x32_bf16 v[28:31],a[80:83],v[48:51],v[28:31]
s_waitcnt lgkmcnt(1)
buffer_store_dwordx4 v[20:23],v4,s[36:39],s73 offen offset:0 sc1
v_cmp_eq_u32 vcc,s60,v11
v_mfma_f32_16x16x32_bf16 v[32:35],a[84:87],v[48:51],v[32:35]
s_cmp_eq_u32 vcc_lo,-1
ds_read_b128 v[44:47],v2 offset:36864
s_cselect_b32 s67,s50,s59
v_mfma_f32_16x16x32_bf16 v[36:39],a[88:91],v[48:51],v[36:39]
s_cselect_b32 s61,s61,s59
s_add_u32 m0,s63,0
s_cmp_ge_u32 s67,s59
v_mfma_f32_16x16x32_bf16 v[40:43],a[92:95],v[48:51],v[40:43]
s_cselect_b32 s68,s50,s59
s_mul_i32 s68,s68,5
v_mfma_f32_16x16x32_bf16 v[28:31],a[96:99],v[52:55],v[28:31]
buffer_load_dwordx4 v0,s[36:39],s61 offen offset:0 sc1 lds
v_mfma_f32_16x16x32_bf16 v[32:35],a[100:103],v[52:55],v[32:35]
s_lshr_b32 s68,s68,6
ds_read_b128 v[48:51],v2 offset:37888
v_mfma_f32_16x16x32_bf16 v[36:39],a[104:107],v[52:55],v[36:39]
v_mfma_f32_16x16x32_bf16 v[40:43],a[108:111],v[52:55],v[40:43]
s_waitcnt lgkmcnt(2)
v_mfma_f32_16x16x32_bf16 v[28:31],a[112:115],v[56:59],v[28:31]
buffer_load_dwordx4 v0,s[36:39],s61 offen offset:1024 sc1 lds
v_mfma_f32_16x16x32_bf16 v[32:35],a[116:119],v[56:59],v[32:35]
ds_read_b128 v[52:55],v2 offset:38912
v_mfma_f32_16x16x32_bf16 v[36:39],a[120:123],v[56:59],v[36:39]
v_mfma_f32_16x16x32_bf16 v[40:43],a[124:127],v[56:59],v[40:43]
s_waitcnt lgkmcnt(2)
v_mfma_f32_16x16x32_bf16 v[28:31],a[128:131],v[44:47],v[28:31]
buffer_load_dwordx4 v0,s[36:39],s61 offen offset:2048 sc1 lds
v_mfma_f32_16x16x32_bf16 v[32:35],a[132:135],v[44:47],v[32:35]
ds_read_b128 v[56:59],v2 offset:39936
v_mfma_f32_16x16x32_bf16 v[36:39],a[136:139],v[44:47],v[36:39]
v_mfma_f32_16x16x32_bf16 v[40:43],a[140:143],v[44:47],v[40:43]
s_waitcnt lgkmcnt(2)
v_mfma_f32_16x16x32_bf16 v[28:31],a[144:147],v[48:51],v[28:31]
buffer_load_dwordx4 v0,s[36:39],s61 offen offset:3072 sc1 lds
v_mfma_f32_16x16x32_bf16 v[32:35],a[148:151],v[48:51],v[32:35]
s_add_u32 m0,s78,6144
ds_read_b128 v[44:47],v2 offset:40960
s_add_u32 s74,s53,s55
v_mfma_f32_16x16x32_bf16 v[36:39],a[152:155],v[48:51],v[36:39]
s_cmp_eq_u32 s74,s56
s_cselect_b32 s74,s57,s74
s_cmp_eq_u32 s74,s58
v_mfma_f32_16x16x32_bf16 v[40:43],a[156:159],v[48:51],v[40:43]
s_waitcnt lgkmcnt(2)
s_cselect_b32 s74,s59,s74
s_cmp_ge_u32 s74,6520832
v_mfma_f32_16x16x32_bf16 v[28:31],a[160:163],v[52:55],v[28:31]
buffer_load_dwordx4 v[68:71],v9,s[36:39],s49 offen offset:0 sc1
v_mfma_f32_16x16x32_bf16 v[32:35],a[164:167],v[52:55],v[32:35]
s_cselect_b32 s55,196608,s55
ds_read_b128 v[48:51],v2 offset:41984
s_mov_b64 s[48:49],s[50:51]
v_mfma_f32_16x16x32_bf16 v[36:39],a[168:171],v[52:55],v[36:39]
v_mfma_f32_16x16x32_bf16 v[40:43],a[172:175],v[52:55],v[40:43]
s_waitcnt lgkmcnt(2)
v_mfma_f32_16x16x32_bf16 v[28:31],a[176:179],v[56:59],v[28:31]
ds_read_b128 v[52:55],v2 offset:43008
v_mfma_f32_16x16x32_bf16 v[32:35],a[180:183],v[56:59],v[32:35]
buffer_load_dword v7,s[36:39],s68 offen offset:0 sc1 lds
v_mfma_f32_16x16x32_bf16 v[36:39],a[184:187],v[56:59],v[36:39]
s_add_u32 m0,s64,0
s_mul_i32 s68,s51,5
s_lshr_b32 s68,s68,6
v_mfma_f32_16x16x32_bf16 v[40:43],a[188:191],v[56:59],v[40:43]
s_waitcnt lgkmcnt(2)
s_mov_b64 s[50:51],s[52:53]
s_mov_b32 s52,s74
v_mfma_f32_16x16x32_bf16 v[28:31],a[192:195],v[44:47],v[28:31]
ds_read_b128 v[56:59],v2 offset:44032
v_mfma_f32_16x16x32_bf16 v[32:35],a[196:199],v[44:47],v[32:35]
v_mfma_f32_16x16x32_bf16 v[36:39],a[200:203],v[44:47],v[36:39]
s_waitcnt vmcnt(9) lgkmcnt(2)
v_mfma_f32_16x16x32_bf16 v[40:43],a[204:207],v[44:47],v[40:43]
s_barrier
v_mfma_f32_16x16x32_bf16 v[28:31],a[208:211],v[48:51],v[28:31]
ds_read_b128 v[44:47],v2 offset:45056
v_mfma_f32_16x16x32_bf16 v[32:35],a[212:215],v[48:51],v[32:35]
buffer_load_dwordx4 v1,s[36:39],s61 offen offset:0 sc1 lds
v_mfma_f32_16x16x32_bf16 v[36:39],a[216:219],v[48:51],v[36:39]
v_mfma_f32_16x16x32_bf16 v[40:43],a[220:223],v[48:51],v[40:43]
s_waitcnt lgkmcnt(2)
v_mfma_f32_16x16x32_bf16 v[28:31],a[224:227],v[52:55],v[28:31]
ds_read_b128 v[48:51],v2 offset:46080
v_mfma_f32_16x16x32_bf16 v[32:35],a[228:231],v[52:55],v[32:35]
v_mfma_f32_16x16x32_bf16 v[36:39],a[232:235],v[52:55],v[36:39]
v_mfma_f32_16x16x32_bf16 v[40:43],a[236:239],v[52:55],v[40:43]
s_waitcnt lgkmcnt(2)
buffer_load_dwordx4 v1,s[36:39],s61 offen offset:1024 sc1 lds
v_mfma_f32_16x16x32_bf16 v[28:31],a[240:243],v[56:59],v[28:31]
ds_read_b128 v[52:55],v2 offset:47104
v_mfma_f32_16x16x32_bf16 v[32:35],a[244:247],v[56:59],v[32:35]
v_mfma_f32_16x16x32_bf16 v[36:39],a[248:251],v[56:59],v[36:39]
v_mfma_f32_16x16x32_bf16 v[40:43],a[252:255],v[56:59],v[40:43]
s_waitcnt lgkmcnt(2)
v_mfma_f32_16x16x32_bf16 v[28:31],v[100:103],v[44:47],v[28:31]
ds_read_b128 v[56:59],v2 offset:48128
v_mfma_f32_16x16x32_bf16 v[32:35],v[104:107],v[44:47],v[32:35]
buffer_load_dwordx4 v1,s[36:39],s61 offen offset:2048 sc1 lds
v_mfma_f32_16x16x32_bf16 v[36:39],v[108:111],v[44:47],v[36:39]
s_add_u32 m0,s78,4096
ds_read_b128 v[88:91],v10 offset:0
s_cmp_lt_u32 s67,s59
v_mfma_f32_16x16x32_bf16 v[40:43],v[112:115],v[44:47],v[40:43]
s_waitcnt lgkmcnt(3)
v_mfma_f32_16x16x32_bf16 v[28:31],v[116:119],v[48:51],v[28:31]
ds_read_b128 v[44:47],v2 offset:49152
v_mfma_f32_16x16x32_bf16 v[32:35],v[120:123],v[48:51],v[32:35]
ds_read_b128 v[76:79],v10 offset:1024
v_mfma_f32_16x16x32_bf16 v[36:39],v[124:127],v[48:51],v[36:39]
v_mfma_f32_16x16x32_bf16 v[40:43],v[128:131],v[48:51],v[40:43]
s_waitcnt lgkmcnt(4)
v_mfma_f32_16x16x32_bf16 v[28:31],v[132:135],v[52:55],v[28:31]
buffer_load_dword v7,s[36:39],s68 offen offset:0 sc1 lds
v_mfma_f32_16x16x32_bf16 v[32:35],v[136:139],v[52:55],v[32:35]
ds_read_b128 v[48:51],v2 offset:50176
ds_read_b128 v[80:83],v10 offset:2048
v_mfma_f32_16x16x32_bf16 v[36:39],v[140:143],v[52:55],v[36:39]
v_mfma_f32_16x16x32_bf16 v[40:43],v[144:147],v[52:55],v[40:43]
s_waitcnt lgkmcnt(4)
v_mfma_f32_16x16x32_bf16 v[28:31],v[148:151],v[56:59],v[28:31]
ds_read_b128 v[52:55],v2 offset:51200
v_mfma_f32_16x16x32_bf16 v[32:35],v[152:155],v[56:59],v[32:35]
ds_read_b128 v[84:87],v10 offset:3072
v_mfma_f32_16x16x32_bf16 v[36:39],v[156:159],v[56:59],v[36:39]
v_mfma_f32_16x16x32_bf16 v[40:43],v[88:91],v[56:59],v[40:43]
s_waitcnt lgkmcnt(4)
v_mfma_f32_16x16x32_bf16 v[28:31],v[160:163],v[44:47],v[28:31]
ds_read_b128 v[56:59],v2 offset:52224
v_mfma_f32_16x16x32_bf16 v[32:35],v[164:167],v[44:47],v[32:35]
ds_read_b128 v[88:91],v10 offset:4096
v_mfma_f32_16x16x32_bf16 v[36:39],v[168:171],v[44:47],v[36:39]
v_mfma_f32_16x16x32_bf16 v[40:43],v[76:79],v[44:47],v[40:43]
s_waitcnt lgkmcnt(4)
v_mfma_f32_16x16x32_bf16 v[28:31],v[172:175],v[48:51],v[28:31]
ds_read_b128 v[44:47],v2 offset:53248
v_mfma_f32_16x16x32_bf16 v[32:35],v[176:179],v[48:51],v[32:35]
ds_read_b128 v[76:79],v10 offset:5120
v_mfma_f32_16x16x32_bf16 v[36:39],v[180:183],v[48:51],v[36:39]
s_waitcnt vmcnt(10) lgkmcnt(4)
v_mfma_f32_16x16x32_bf16 v[40:43],v[80:83],v[48:51],v[40:43]
s_barrier
v_mfma_f32_16x16x32_bf16 v[28:31],v[184:187],v[52:55],v[28:31]
ds_read_b128 v[48:51],v2 offset:54272
v_mfma_f32_16x16x32_bf16 v[32:35],v[188:191],v[52:55],v[32:35]
ds_read_b128 v[80:83],v10 offset:6144
v_mfma_f32_16x16x32_bf16 v[36:39],v[192:195],v[52:55],v[36:39]
v_mfma_f32_16x16x32_bf16 v[40:43],v[84:87],v[52:55],v[40:43]
s_waitcnt lgkmcnt(4)
v_mfma_f32_16x16x32_bf16 v[28:31],v[196:199],v[56:59],v[28:31]
ds_read_b128 v[52:55],v2 offset:55296
v_mfma_f32_16x16x32_bf16 v[32:35],v[200:203],v[56:59],v[32:35]
ds_read_b128 v[84:87],v10 offset:7168
v_mfma_f32_16x16x32_bf16 v[36:39],v[204:207],v[56:59],v[36:39]
v_mfma_f32_16x16x32_bf16 v[40:43],v[88:91],v[56:59],v[40:43]
s_waitcnt lgkmcnt(4)
v_mfma_f32_16x16x32_bf16 v[28:31],v[208:211],v[44:47],v[28:31]
ds_read_b128 v[56:59],v2 offset:56320
v_mfma_f32_16x16x32_bf16 v[32:35],v[212:215],v[44:47],v[32:35]
ds_read_b128 v[88:91],v10 offset:8192
v_mfma_f32_16x16x32_bf16 v[36:39],v[216:219],v[44:47],v[36:39]
s_waitcnt vmcnt(4) lgkmcnt(4)
v_mfma_f32_16x16x32_bf16 v[40:43],v[76:79],v[44:47],v[40:43]
s_barrier
v_mfma_f32_16x16x32_bf16 v[28:31],v[220:223],v[48:51],v[28:31]
ds_read_b128 v[44:47],v2 offset:0
v_mfma_f32_16x16x32_bf16 v[32:35],v[224:227],v[48:51],v[32:35]
v_mfma_f32_16x16x32_bf16 v[36:39],v[228:231],v[48:51],v[36:39]
v_mfma_f32_16x16x32_bf16 v[40:43],v[80:83],v[48:51],v[40:43]
s_waitcnt lgkmcnt(3)
v_mfma_f32_16x16x32_bf16 v[28:31],v[232:235],v[52:55],v[28:31]
ds_read_b128 v[48:51],v2 offset:1024
v_mfma_f32_16x16x32_bf16 v[32:35],v[236:239],v[52:55],v[32:35]
v_mfma_f32_16x16x32_bf16 v[36:39],v[240:243],v[52:55],v[36:39]
v_mfma_f32_16x16x32_bf16 v[40:43],v[84:87],v[52:55],v[40:43]
s_waitcnt lgkmcnt(2)
v_mfma_f32_16x16x32_bf16 v[28:31],v[244:247],v[56:59],v[28:31]
ds_read_b128 v[52:55],v2 offset:2048
v_mfma_f32_16x16x32_bf16 v[32:35],v[248:251],v[56:59],v[32:35]
v_mfma_f32_16x16x32_bf16 v[36:39],v[252:255],v[56:59],v[36:39]
s_waitcnt lgkmcnt(2)
v_mfma_f32_16x16x32_bf16 v[40:43],v[88:91],v[56:59],v[40:43]
s_cbranch_scc1 Lg4_d0w0_%=
s_branch Lg4_slow0_%=
Lg4_d0w1_%=:
v_mfma_f32_16x16x32_bf16 v[12:15],a[0:3],v[44:47],0
s_mov_b32 s73,s47
ds_read_b128 v[56:59],v2 offset:3072)"
R"(
v_lshlrev_b32 v60,16,v68
v_mfma_f32_16x16x32_bf16 v[16:19],a[4:7],v[44:47],0
s_max_u32 s73,s73,s65
v_and_b32 v61,0xffff0000,v68
v_lshlrev_b32 v62,16,v69
v_mfma_f32_16x16x32_bf16 v[20:23],a[8:11],v[44:47],0
s_mov_b32 s65,0
v_and_b32 v63,0xffff0000,v69
v_lshlrev_b32 v64,16,v70
v_mfma_f32_16x16x32_bf16 v[24:27],a[12:15],v[44:47],0
s_waitcnt lgkmcnt(2)
v_and_b32 v65,0xffff0000,v70
v_lshlrev_b32 v66,16,v71
v_mfma_f32_16x16x32_bf16 v[12:15],a[16:19],v[48:51],v[12:15]
s_mul_i32 s61,s49,7
ds_read_b128 v[44:47],v2 offset:4096
v_and_b32 v67,0xffff0000,v71
v_mfma_f32_16x16x32_bf16 v[16:19],a[20:23],v[48:51],v[16:19]
s_lshr_b32 s61,s61,1
v_cvt_pk_bf16_f32 v28,v28,v29
v_cvt_pk_bf16_f32 v29,v30,v31
v_mfma_f32_16x16x32_bf16 v[20:23],a[24:27],v[48:51],v[20:23]
v_cvt_pk_bf16_f32 v30,v32,v33
v_cvt_pk_bf16_f32 v31,v34,v35
v_mfma_f32_16x16x32_bf16 v[24:27],a[28:31],v[48:51],v[24:27]
s_waitcnt lgkmcnt(2)
v_add_f32 v36,v36,v60
v_add_f32 v37,v37,v61
v_mfma_f32_16x16x32_bf16 v[12:15],a[32:35],v[52:55],v[12:15]
ds_read_b128 v[48:51],v2 offset:5120
v_add_f32 v38,v38,v62
v_mfma_f32_16x16x32_bf16 v[16:19],a[36:39],v[52:55],v[16:19]
buffer_store_dwordx4 v[28:31],v3,s[36:39],s73 offen offset:0 sc1
v_add_f32 v39,v39,v63
v_add_f32 v40,v40,v64
v_add_f32 v41,v41,v65
v_mfma_f32_16x16x32_bf16 v[20:23],a[40:43],v[52:55],v[20:23]
v_add_f32 v42,v42,v66
v_add_f32 v43,v43,v67
v_mfma_f32_16x16x32_bf16 v[24:27],a[44:47],v[52:55],v[24:27]
s_waitcnt lgkmcnt(2)
v_cvt_pk_bf16_f32 v36,v36,v37
v_cvt_pk_bf16_f32 v37,v38,v39
v_mfma_f32_16x16x32_bf16 v[12:15],a[48:51],v[56:59],v[12:15]
ds_read_b128 v[52:55],v2 offset:6144
v_cvt_pk_bf16_f32 v38,v40,v41
v_mfma_f32_16x16x32_bf16 v[16:19],a[52:55],v[56:59],v[16:19]
v_cvt_pk_bf16_f32 v39,v42,v43
v_mfma_f32_16x16x32_bf16 v[20:23],a[56:59],v[56:59],v[20:23]
s_waitcnt lgkmcnt(2)
s_nop 0
v_mfma_f32_16x16x32_bf16 v[24:27],a[60:63],v[56:59],v[24:27]
s_barrier
v_mfma_f32_16x16x32_bf16 v[12:15],a[64:67],v[44:47],v[12:15]
ds_read_b128 v[56:59],v2 offset:7168
ds_read_b32 v11,v8 offset:4096
v_mfma_f32_16x16x32_bf16 v[16:19],a[68:71],v[44:47],v[16:19]
v_mfma_f32_16x16x32_bf16 v[20:23],a[72:75],v[44:47],v[20:23]
v_mfma_f32_16x16x32_bf16 v[24:27],a[76:79],v[44:47],v[24:27]
s_waitcnt lgkmcnt(3)
v_mfma_f32_16x16x32_bf16 v[12:15],a[80:83],v[48:51],v[12:15]
s_waitcnt lgkmcnt(0)
ds_read_b128 v[44:47],v2 offset:8192
v_mfma_f32_16x16x32_bf16 v[16:19],a[84:87],v[48:51],v[16:19]
buffer_store_dwordx4 v[36:39],v4,s[36:39],s73 offen offset:0 sc1
v_cmp_eq_u32 vcc,s60,v11
v_mfma_f32_16x16x32_bf16 v[20:23],a[88:91],v[48:51],v[20:23]
s_cmp_eq_u32 vcc_lo,-1
s_cselect_b32 s66,s49,s59
s_cselect_b32 s61,s61,s59
v_mfma_f32_16x16x32_bf16 v[24:27],a[92:95],v[48:51],v[24:27]
s_add_u32 m0,s63,28672
s_add_u32 s53,s52,s55
v_mfma_f32_16x16x32_bf16 v[12:15],a[96:99],v[52:55],v[12:15]
s_cmp_eq_u32 s53,s56
ds_read_b128 v[48:51],v2 offset:9216
s_cselect_b32 s53,s57,s53
v_mfma_f32_16x16x32_bf16 v[16:19],a[100:103],v[52:55],v[16:19]
buffer_load_dwordx4 v0,s[36:39],s61 offen offset:0 sc1 lds
v_mfma_f32_16x16x32_bf16 v[20:23],a[104:107],v[52:55],v[20:23]
s_cmp_eq_u32 s53,s58
s_cselect_b32 s53,s59,s53
s_cmp_ge_u32 s53,6520832
v_mfma_f32_16x16x32_bf16 v[24:27],a[108:111],v[52:55],v[24:27]
s_cselect_b32 s55,196608,s55
v_mfma_f32_16x16x32_bf16 v[12:15],a[112:115],v[56:59],v[12:15]
ds_read_b128 v[52:55],v2 offset:10240
v_mfma_f32_16x16x32_bf16 v[16:19],a[116:119],v[56:59],v[16:19]
buffer_load_dwordx4 v0,s[36:39],s61 offen offset:1024 sc1 lds
v_mfma_f32_16x16x32_bf16 v[20:23],a[120:123],v[56:59],v[20:23]
v_mfma_f32_16x16x32_bf16 v[24:27],a[124:127],v[56:59],v[24:27]
s_waitcnt lgkmcnt(2)
v_mfma_f32_16x16x32_bf16 v[12:15],a[128:131],v[44:47],v[12:15]
ds_read_b128 v[56:59],v2 offset:11264
v_mfma_f32_16x16x32_bf16 v[16:19],a[132:135],v[44:47],v[16:19]
buffer_load_dwordx4 v0,s[36:39],s61 offen offset:2048 sc1 lds
v_mfma_f32_16x16x32_bf16 v[20:23],a[136:139],v[44:47],v[20:23]
v_mfma_f32_16x16x32_bf16 v[24:27],a[140:143],v[44:47],v[24:27]
s_waitcnt lgkmcnt(2)
v_mfma_f32_16x16x32_bf16 v[12:15],a[144:147],v[48:51],v[12:15]
ds_read_b128 v[44:47],v2 offset:12288
v_mfma_f32_16x16x32_bf16 v[16:19],a[148:151],v[48:51],v[16:19]
buffer_load_dwordx4 v0,s[36:39],s61 offen offset:3072 sc1 lds
v_mfma_f32_16x16x32_bf16 v[20:23],a[152:155],v[48:51],v[20:23]
s_add_u32 m0,s64,28672
s_cmp_ge_u32 s66,s59
v_mfma_f32_16x16x32_bf16 v[24:27],a[156:159],v[48:51],v[24:27]
s_waitcnt lgkmcnt(2)
v_mfma_f32_16x16x32_bf16 v[12:15],a[160:163],v[52:55],v[12:15]
ds_read_b128 v[48:51],v2 offset:13312
v_mfma_f32_16x16x32_bf16 v[16:19],a[164:167],v[52:55],v[16:19]
buffer_load_dwordx4 v[68:71],v9,s[36:39],s48 offen offset:0 sc1
v_mfma_f32_16x16x32_bf16 v[20:23],a[168:171],v[52:55],v[20:23]
v_mfma_f32_16x16x32_bf16 v[24:27],a[172:175],v[52:55],v[24:27]
s_waitcnt lgkmcnt(2)
v_mfma_f32_16x16x32_bf16 v[12:15],a[176:179],v[56:59],v[12:15]
ds_read_b128 v[52:55],v2 offset:14336
v_mfma_f32_16x16x32_bf16 v[16:19],a[180:183],v[56:59],v[16:19]
v_mfma_f32_16x16x32_bf16 v[20:23],a[184:187],v[56:59],v[20:23]
v_mfma_f32_16x16x32_bf16 v[24:27],a[188:191],v[56:59],v[24:27]
s_waitcnt lgkmcnt(2)
v_mfma_f32_16x16x32_bf16 v[12:15],a[192:195],v[44:47],v[12:15]
ds_read_b128 v[56:59],v2 offset:15360
v_mfma_f32_16x16x32_bf16 v[16:19],a[196:199],v[44:47],v[16:19]
v_mfma_f32_16x16x32_bf16 v[20:23],a[200:203],v[44:47],v[20:23]
s_waitcnt vmcnt(7) lgkmcnt(2)
v_mfma_f32_16x16x32_bf16 v[24:27],a[204:207],v[44:47],v[24:27]
s_barrier
v_mfma_f32_16x16x32_bf16 v[12:15],a[208:211],v[48:51],v[12:15]
ds_read_b128 v[44:47],v2 offset:16384
v_mfma_f32_16x16x32_bf16 v[16:19],a[212:215],v[48:51],v[16:19]
v_mfma_f32_16x16x32_bf16 v[20:23],a[216:219],v[48:51],v[20:23]
buffer_load_dwordx4 v1,s[36:39],s61 offen offset:0 sc1 lds
v_mfma_f32_16x16x32_bf16 v[24:27],a[220:223],v[48:51],v[24:27]
s_waitcnt lgkmcnt(2)
v_mfma_f32_16x16x32_bf16 v[12:15],a[224:227],v[52:55],v[12:15]
ds_read_b128 v[48:51],v2 offset:17408
v_mfma_f32_16x16x32_bf16 v[16:19],a[228:231],v[52:55],v[16:19]
v_mfma_f32_16x16x32_bf16 v[20:23],a[232:235],v[52:55],v[20:23]
v_mfma_f32_16x16x32_bf16 v[24:27],a[236:239],v[52:55],v[24:27]
s_waitcnt lgkmcnt(2)
v_mfma_f32_16x16x32_bf16 v[12:15],a[240:243],v[56:59],v[12:15]
buffer_load_dwordx4 v1,s[36:39],s61 offen offset:1024 sc1 lds
v_mfma_f32_16x16x32_bf16 v[16:19],a[244:247],v[56:59],v[16:19]
ds_read_b128 v[52:55],v2 offset:18432
v_mfma_f32_16x16x32_bf16 v[20:23],a[248:251],v[56:59],v[20:23]
v_mfma_f32_16x16x32_bf16 v[24:27],a[252:255],v[56:59],v[24:27]
s_waitcnt lgkmcnt(2)
v_mfma_f32_16x16x32_bf16 v[12:15],v[100:103],v[44:47],v[12:15]
ds_read_b128 v[56:59],v2 offset:19456
v_mfma_f32_16x16x32_bf16 v[16:19],v[104:107],v[44:47],v[16:19]
ds_read_b128 v[88:91],v10 offset:0
v_mfma_f32_16x16x32_bf16 v[20:23],v[108:111],v[44:47],v[20:23]
buffer_load_dwordx4 v1,s[36:39],s61 offen offset:2048 sc1 lds
v_mfma_f32_16x16x32_bf16 v[24:27],v[112:115],v[44:47],v[24:27]
s_waitcnt lgkmcnt(3)
v_mfma_f32_16x16x32_bf16 v[12:15],v[116:119],v[48:51],v[12:15]
ds_read_b128 v[44:47],v2 offset:20480
v_mfma_f32_16x16x32_bf16 v[16:19],v[120:123],v[48:51],v[16:19]
ds_read_b128 v[76:79],v10 offset:1024
v_mfma_f32_16x16x32_bf16 v[20:23],v[124:127],v[48:51],v[20:23]
v_mfma_f32_16x16x32_bf16 v[24:27],v[128:131],v[48:51],v[24:27]
s_waitcnt lgkmcnt(4)
v_mfma_f32_16x16x32_bf16 v[12:15],v[132:135],v[52:55],v[12:15]
ds_read_b128 v[48:51],v2 offset:21504
v_mfma_f32_16x16x32_bf16 v[16:19],v[136:139],v[52:55],v[16:19]
ds_read_b128 v[80:83],v10 offset:2048
v_mfma_f32_16x16x32_bf16 v[20:23],v[140:143],v[52:55],v[20:23]
v_mfma_f32_16x16x32_bf16 v[24:27],v[144:147],v[52:55],v[24:27]
s_waitcnt lgkmcnt(4)
v_mfma_f32_16x16x32_bf16 v[12:15],v[148:151],v[56:59],v[12:15]
ds_read_b128 v[52:55],v2 offset:22528
v_mfma_f32_16x16x32_bf16 v[16:19],v[152:155],v[56:59],v[16:19]
ds_read_b128 v[84:87],v10 offset:3072
v_mfma_f32_16x16x32_bf16 v[20:23],v[156:159],v[56:59],v[20:23]
v_mfma_f32_16x16x32_bf16 v[24:27],v[88:91],v[56:59],v[24:27]
s_waitcnt lgkmcnt(4)
v_mfma_f32_16x16x32_bf16 v[12:15],v[160:163],v[44:47],v[12:15]
ds_read_b128 v[56:59],v2 offset:23552
v_mfma_f32_16x16x32_bf16 v[16:19],v[164:167],v[44:47],v[16:19]
ds_read_b128 v[88:91],v10 offset:4096
v_mfma_f32_16x16x32_bf16 v[20:23],v[168:171],v[44:47],v[20:23]
v_mfma_f32_16x16x32_bf16 v[24:27],v[76:79],v[44:47],v[24:27]
s_waitcnt lgkmcnt(4)
v_mfma_f32_16x16x32_bf16 v[12:15],v[172:175],v[48:51],v[12:15]
ds_read_b128 v[44:47],v2 offset:24576
v_mfma_f32_16x16x32_bf16 v[16:19],v[176:179],v[48:51],v[16:19]
ds_read_b128 v[76:79],v10 offset:5120
v_mfma_f32_16x16x32_bf16 v[20:23],v[180:183],v[48:51],v[20:23]
s_waitcnt vmcnt(8) lgkmcnt(4)
v_mfma_f32_16x16x32_bf16 v[24:27],v[80:83],v[48:51],v[24:27]
s_barrier
v_mfma_f32_16x16x32_bf16 v[12:15],v[184:187],v[52:55],v[12:15]
ds_read_b128 v[48:51],v2 offset:25600
v_mfma_f32_16x16x32_bf16 v[16:19],v[188:191],v[52:55],v[16:19]
ds_read_b128 v[80:83],v10 offset:6144
v_mfma_f32_16x16x32_bf16 v[20:23],v[192:195],v[52:55],v[20:23]
v_mfma_f32_16x16x32_bf16 v[24:27],v[84:87],v[52:55],v[24:27]
s_waitcnt lgkmcnt(4)
v_mfma_f32_16x16x32_bf16 v[12:15],v[196:199],v[56:59],v[12:15])"
R"(
ds_read_b128 v[52:55],v2 offset:26624
v_mfma_f32_16x16x32_bf16 v[16:19],v[200:203],v[56:59],v[16:19]
ds_read_b128 v[84:87],v10 offset:7168
v_mfma_f32_16x16x32_bf16 v[20:23],v[204:207],v[56:59],v[20:23]
v_mfma_f32_16x16x32_bf16 v[24:27],v[88:91],v[56:59],v[24:27]
s_waitcnt lgkmcnt(4)
v_mfma_f32_16x16x32_bf16 v[12:15],v[208:211],v[44:47],v[12:15]
ds_read_b128 v[56:59],v2 offset:27648
v_mfma_f32_16x16x32_bf16 v[16:19],v[212:215],v[44:47],v[16:19]
ds_read_b128 v[88:91],v10 offset:8192
v_mfma_f32_16x16x32_bf16 v[20:23],v[216:219],v[44:47],v[20:23]
s_waitcnt vmcnt(3) lgkmcnt(4)
v_mfma_f32_16x16x32_bf16 v[24:27],v[76:79],v[44:47],v[24:27]
s_barrier
v_mfma_f32_16x16x32_bf16 v[12:15],v[220:223],v[48:51],v[12:15]
ds_read_b128 v[44:47],v2 offset:28672
v_mfma_f32_16x16x32_bf16 v[16:19],v[224:227],v[48:51],v[16:19]
v_mfma_f32_16x16x32_bf16 v[20:23],v[228:231],v[48:51],v[20:23]
v_mfma_f32_16x16x32_bf16 v[24:27],v[80:83],v[48:51],v[24:27]
s_waitcnt lgkmcnt(3)
v_mfma_f32_16x16x32_bf16 v[12:15],v[232:235],v[52:55],v[12:15]
ds_read_b128 v[48:51],v2 offset:29696
v_mfma_f32_16x16x32_bf16 v[16:19],v[236:239],v[52:55],v[16:19]
v_mfma_f32_16x16x32_bf16 v[20:23],v[240:243],v[52:55],v[20:23]
v_mfma_f32_16x16x32_bf16 v[24:27],v[84:87],v[52:55],v[24:27]
s_waitcnt lgkmcnt(2)
v_mfma_f32_16x16x32_bf16 v[12:15],v[244:247],v[56:59],v[12:15]
ds_read_b128 v[52:55],v2 offset:30720
v_mfma_f32_16x16x32_bf16 v[16:19],v[248:251],v[56:59],v[16:19]
v_mfma_f32_16x16x32_bf16 v[20:23],v[252:255],v[56:59],v[20:23]
s_waitcnt lgkmcnt(2)
v_mfma_f32_16x16x32_bf16 v[24:27],v[88:91],v[56:59],v[24:27]
s_cbranch_scc1 Lg4_slow1_%=
Lg4_d1w1_%=:
v_mfma_f32_16x16x32_bf16 v[28:31],a[0:3],v[44:47],0
s_mov_b32 s73,s48
ds_read_b128 v[56:59],v2 offset:31744
v_lshlrev_b32 v60,16,v68
v_mfma_f32_16x16x32_bf16 v[32:35],a[4:7],v[44:47],0
s_max_u32 s73,s73,s65
v_and_b32 v61,0xffff0000,v68
v_lshlrev_b32 v62,16,v69
v_mfma_f32_16x16x32_bf16 v[36:39],a[8:11],v[44:47],0
s_mov_b32 s65,0
v_and_b32 v63,0xffff0000,v69
v_lshlrev_b32 v64,16,v70
v_mfma_f32_16x16x32_bf16 v[40:43],a[12:15],v[44:47],0
s_waitcnt lgkmcnt(2)
v_and_b32 v65,0xffff0000,v70
v_lshlrev_b32 v66,16,v71
v_mfma_f32_16x16x32_bf16 v[28:31],a[16:19],v[48:51],v[28:31]
s_mul_i32 s61,s50,7
ds_read_b128 v[44:47],v2 offset:32768
v_and_b32 v67,0xffff0000,v71
v_mfma_f32_16x16x32_bf16 v[32:35],a[20:23],v[48:51],v[32:35]
s_lshr_b32 s61,s61,1
v_cvt_pk_bf16_f32 v12,v12,v13
v_cvt_pk_bf16_f32 v13,v14,v15
v_mfma_f32_16x16x32_bf16 v[36:39],a[24:27],v[48:51],v[36:39]
s_mov_b64 s[44:45],s[46:47]
v_cvt_pk_bf16_f32 v14,v16,v17
v_cvt_pk_bf16_f32 v15,v18,v19
v_mfma_f32_16x16x32_bf16 v[40:43],a[28:31],v[48:51],v[40:43]
s_waitcnt lgkmcnt(2)
v_add_f32 v20,v20,v60
v_add_f32 v21,v21,v61
v_mfma_f32_16x16x32_bf16 v[28:31],a[32:35],v[52:55],v[28:31]
s_mov_b64 s[46:47],s[48:49]
ds_read_b128 v[48:51],v2 offset:33792
v_add_f32 v22,v22,v62
v_mfma_f32_16x16x32_bf16 v[32:35],a[36:39],v[52:55],v[32:35]
buffer_store_dwordx4 v[12:15],v3,s[36:39],s73 offen offset:0 sc1
v_add_f32 v23,v23,v63
v_add_f32 v24,v24,v64
v_add_f32 v25,v25,v65
v_mfma_f32_16x16x32_bf16 v[36:39],a[40:43],v[52:55],v[36:39]
v_add_f32 v26,v26,v66
v_add_f32 v27,v27,v67
v_mfma_f32_16x16x32_bf16 v[40:43],a[44:47],v[52:55],v[40:43]
s_waitcnt lgkmcnt(2)
v_cvt_pk_bf16_f32 v20,v20,v21
v_cvt_pk_bf16_f32 v21,v22,v23
v_mfma_f32_16x16x32_bf16 v[28:31],a[48:51],v[56:59],v[28:31]
ds_read_b128 v[52:55],v2 offset:34816
v_cvt_pk_bf16_f32 v22,v24,v25
v_mfma_f32_16x16x32_bf16 v[32:35],a[52:55],v[56:59],v[32:35]
v_cvt_pk_bf16_f32 v23,v26,v27
v_mfma_f32_16x16x32_bf16 v[36:39],a[56:59],v[56:59],v[36:39]
s_waitcnt lgkmcnt(2)
s_nop 0
v_mfma_f32_16x16x32_bf16 v[40:43],a[60:63],v[56:59],v[40:43]
s_barrier
v_mfma_f32_16x16x32_bf16 v[28:31],a[64:67],v[44:47],v[28:31]
ds_read_b128 v[56:59],v2 offset:35840
ds_read_b32 v11,v8 offset:5120
v_mfma_f32_16x16x32_bf16 v[32:35],a[68:71],v[44:47],v[32:35]
v_mfma_f32_16x16x32_bf16 v[36:39],a[72:75],v[44:47],v[36:39]
v_mfma_f32_16x16x32_bf16 v[40:43],a[76:79],v[44:47],v[40:43]
s_waitcnt lgkmcnt(3)
v_mfma_f32_16x16x32_bf16 v[28:31],a[80:83],v[48:51],v[28:31]
s_waitcnt lgkmcnt(0)
ds_read_b128 v[44:47],v2 offset:36864
v_mfma_f32_16x16x32_bf16 v[32:35],a[84:87],v[48:51],v[32:35]
buffer_store_dwordx4 v[20:23],v4,s[36:39],s73 offen offset:0 sc1
v_cmp_eq_u32 vcc,s60,v11
v_mfma_f32_16x16x32_bf16 v[36:39],a[88:91],v[48:51],v[36:39]
s_cmp_eq_u32 vcc_lo,-1
s_cselect_b32 s67,s50,s59
s_cselect_b32 s61,s61,s59
v_mfma_f32_16x16x32_bf16 v[40:43],a[92:95],v[48:51],v[40:43]
s_add_u32 m0,s63,0
s_add_u32 s74,s53,s55
v_mfma_f32_16x16x32_bf16 v[28:31],a[96:99],v[52:55],v[28:31]
s_cmp_eq_u32 s74,s56
ds_read_b128 v[48:51],v2 offset:37888
s_cselect_b32 s74,s57,s74
v_mfma_f32_16x16x32_bf16 v[32:35],a[100:103],v[52:55],v[32:35]
buffer_load_dwordx4 v0,s[36:39],s61 offen offset:0 sc1 lds
v_mfma_f32_16x16x32_bf16 v[36:39],a[104:107],v[52:55],v[36:39]
s_cmp_eq_u32 s74,s58
s_cselect_b32 s74,s59,s74
s_cmp_ge_u32 s74,6520832
v_mfma_f32_16x16x32_bf16 v[40:43],a[108:111],v[52:55],v[40:43]
s_cselect_b32 s55,196608,s55
v_mfma_f32_16x16x32_bf16 v[28:31],a[112:115],v[56:59],v[28:31]
ds_read_b128 v[52:55],v2 offset:38912
v_mfma_f32_16x16x32_bf16 v[32:35],a[116:119],v[56:59],v[32:35]
buffer_load_dwordx4 v0,s[36:39],s61 offen offset:1024 sc1 lds
v_mfma_f32_16x16x32_bf16 v[36:39],a[120:123],v[56:59],v[36:39]
v_mfma_f32_16x16x32_bf16 v[40:43],a[124:127],v[56:59],v[40:43]
s_waitcnt lgkmcnt(2)
v_mfma_f32_16x16x32_bf16 v[28:31],a[128:131],v[44:47],v[28:31]
ds_read_b128 v[56:59],v2 offset:39936
v_mfma_f32_16x16x32_bf16 v[32:35],a[132:135],v[44:47],v[32:35]
buffer_load_dwordx4 v0,s[36:39],s61 offen offset:2048 sc1 lds
v_mfma_f32_16x16x32_bf16 v[36:39],a[136:139],v[44:47],v[36:39]
v_mfma_f32_16x16x32_bf16 v[40:43],a[140:143],v[44:47],v[40:43]
s_waitcnt lgkmcnt(2)
v_mfma_f32_16x16x32_bf16 v[28:31],a[144:147],v[48:51],v[28:31]
ds_read_b128 v[44:47],v2 offset:40960
v_mfma_f32_16x16x32_bf16 v[32:35],a[148:151],v[48:51],v[32:35]
buffer_load_dwordx4 v0,s[36:39],s61 offen offset:3072 sc1 lds
v_mfma_f32_16x16x32_bf16 v[36:39],a[152:155],v[48:51],v[36:39]
s_add_u32 m0,s64,0
s_cmp_lt_u32 s67,s59
v_mfma_f32_16x16x32_bf16 v[40:43],a[156:159],v[48:51],v[40:43]
s_waitcnt lgkmcnt(2)
v_mfma_f32_16x16x32_bf16 v[28:31],a[160:163],v[52:55],v[28:31]
ds_read_b128 v[48:51],v2 offset:41984
v_mfma_f32_16x16x32_bf16 v[32:35],a[164:167],v[52:55],v[32:35]
buffer_load_dwordx4 v[68:71],v9,s[36:39],s49 offen offset:0 sc1
v_mfma_f32_16x16x32_bf16 v[36:39],a[168:171],v[52:55],v[36:39]
s_mov_b64 s[48:49],s[50:51]
s_mov_b64 s[50:51],s[52:53]
s_mov_b32 s52,s74
v_mfma_f32_16x16x32_bf16 v[40:43],a[172:175],v[52:55],v[40:43]
s_waitcnt lgkmcnt(2)
v_mfma_f32_16x16x32_bf16 v[28:31],a[176:179],v[56:59],v[28:31]
ds_read_b128 v[52:55],v2 offset:43008
v_mfma_f32_16x16x32_bf16 v[32:35],a[180:183],v[56:59],v[32:35]
v_mfma_f32_16x16x32_bf16 v[36:39],a[184:187],v[56:59],v[36:39]
v_mfma_f32_16x16x32_bf16 v[40:43],a[188:191],v[56:59],v[40:43]
s_waitcnt lgkmcnt(2)
v_mfma_f32_16x16x32_bf16 v[28:31],a[192:195],v[44:47],v[28:31]
ds_read_b128 v[56:59],v2 offset:44032
v_mfma_f32_16x16x32_bf16 v[32:35],a[196:199],v[44:47],v[32:35]
v_mfma_f32_16x16x32_bf16 v[36:39],a[200:203],v[44:47],v[36:39]
s_waitcnt vmcnt(7) lgkmcnt(2)
v_mfma_f32_16x16x32_bf16 v[40:43],a[204:207],v[44:47],v[40:43]
s_barrier
v_mfma_f32_16x16x32_bf16 v[28:31],a[208:211],v[48:51],v[28:31]
ds_read_b128 v[44:47],v2 offset:45056
v_mfma_f32_16x16x32_bf16 v[32:35],a[212:215],v[48:51],v[32:35]
v_mfma_f32_16x16x32_bf16 v[36:39],a[216:219],v[48:51],v[36:39]
buffer_load_dwordx4 v1,s[36:39],s61 offen offset:0 sc1 lds
v_mfma_f32_16x16x32_bf16 v[40:43],a[220:223],v[48:51],v[40:43]
s_waitcnt lgkmcnt(2)
v_mfma_f32_16x16x32_bf16 v[28:31],a[224:227],v[52:55],v[28:31]
ds_read_b128 v[48:51],v2 offset:46080
v_mfma_f32_16x16x32_bf16 v[32:35],a[228:231],v[52:55],v[32:35]
v_mfma_f32_16x16x32_bf16 v[36:39],a[232:235],v[52:55],v[36:39]
v_mfma_f32_16x16x32_bf16 v[40:43],a[236:239],v[52:55],v[40:43]
s_waitcnt lgkmcnt(2)
v_mfma_f32_16x16x32_bf16 v[28:31],a[240:243],v[56:59],v[28:31]
buffer_load_dwordx4 v1,s[36:39],s61 offen offset:1024 sc1 lds
v_mfma_f32_16x16x32_bf16 v[32:35],a[244:247],v[56:59],v[32:35]
ds_read_b128 v[52:55],v2 offset:47104
v_mfma_f32_16x16x32_bf16 v[36:39],a[248:251],v[56:59],v[36:39]
v_mfma_f32_16x16x32_bf16 v[40:43],a[252:255],v[56:59],v[40:43]
s_waitcnt lgkmcnt(2)
v_mfma_f32_16x16x32_bf16 v[28:31],v[100:103],v[44:47],v[28:31]
ds_read_b128 v[56:59],v2 offset:48128
v_mfma_f32_16x16x32_bf16 v[32:35],v[104:107],v[44:47],v[32:35]
ds_read_b128 v[88:91],v10 offset:0
v_mfma_f32_16x16x32_bf16 v[36:39],v[108:111],v[44:47],v[36:39]
buffer_load_dwordx4 v1,s[36:39],s61 offen offset:2048 sc1 lds
v_mfma_f32_16x16x32_bf16 v[40:43],v[112:115],v[44:47],v[40:43]
s_waitcnt lgkmcnt(3)
v_mfma_f32_16x16x32_bf16 v[28:31],v[116:119],v[48:51],v[28:31]
ds_read_b128 v[44:47],v2 offset:49152)"
R"(
v_mfma_f32_16x16x32_bf16 v[32:35],v[120:123],v[48:51],v[32:35]
ds_read_b128 v[76:79],v10 offset:1024
v_mfma_f32_16x16x32_bf16 v[36:39],v[124:127],v[48:51],v[36:39]
v_mfma_f32_16x16x32_bf16 v[40:43],v[128:131],v[48:51],v[40:43]
s_waitcnt lgkmcnt(4)
v_mfma_f32_16x16x32_bf16 v[28:31],v[132:135],v[52:55],v[28:31]
ds_read_b128 v[48:51],v2 offset:50176
v_mfma_f32_16x16x32_bf16 v[32:35],v[136:139],v[52:55],v[32:35]
ds_read_b128 v[80:83],v10 offset:2048
v_mfma_f32_16x16x32_bf16 v[36:39],v[140:143],v[52:55],v[36:39]
v_mfma_f32_16x16x32_bf16 v[40:43],v[144:147],v[52:55],v[40:43]
s_waitcnt lgkmcnt(4)
v_mfma_f32_16x16x32_bf16 v[28:31],v[148:151],v[56:59],v[28:31]
ds_read_b128 v[52:55],v2 offset:51200
v_mfma_f32_16x16x32_bf16 v[32:35],v[152:155],v[56:59],v[32:35]
ds_read_b128 v[84:87],v10 offset:3072
v_mfma_f32_16x16x32_bf16 v[36:39],v[156:159],v[56:59],v[36:39]
v_mfma_f32_16x16x32_bf16 v[40:43],v[88:91],v[56:59],v[40:43]
s_waitcnt lgkmcnt(4)
v_mfma_f32_16x16x32_bf16 v[28:31],v[160:163],v[44:47],v[28:31]
ds_read_b128 v[56:59],v2 offset:52224
v_mfma_f32_16x16x32_bf16 v[32:35],v[164:167],v[44:47],v[32:35]
ds_read_b128 v[88:91],v10 offset:4096
v_mfma_f32_16x16x32_bf16 v[36:39],v[168:171],v[44:47],v[36:39]
v_mfma_f32_16x16x32_bf16 v[40:43],v[76:79],v[44:47],v[40:43]
s_waitcnt lgkmcnt(4)
v_mfma_f32_16x16x32_bf16 v[28:31],v[172:175],v[48:51],v[28:31]
ds_read_b128 v[44:47],v2 offset:53248
v_mfma_f32_16x16x32_bf16 v[32:35],v[176:179],v[48:51],v[32:35]
ds_read_b128 v[76:79],v10 offset:5120
v_mfma_f32_16x16x32_bf16 v[36:39],v[180:183],v[48:51],v[36:39]
s_waitcnt vmcnt(8) lgkmcnt(4)
v_mfma_f32_16x16x32_bf16 v[40:43],v[80:83],v[48:51],v[40:43]
s_barrier
v_mfma_f32_16x16x32_bf16 v[28:31],v[184:187],v[52:55],v[28:31]
ds_read_b128 v[48:51],v2 offset:54272
v_mfma_f32_16x16x32_bf16 v[32:35],v[188:191],v[52:55],v[32:35]
ds_read_b128 v[80:83],v10 offset:6144
v_mfma_f32_16x16x32_bf16 v[36:39],v[192:195],v[52:55],v[36:39]
v_mfma_f32_16x16x32_bf16 v[40:43],v[84:87],v[52:55],v[40:43]
s_waitcnt lgkmcnt(4)
v_mfma_f32_16x16x32_bf16 v[28:31],v[196:199],v[56:59],v[28:31]
ds_read_b128 v[52:55],v2 offset:55296
v_mfma_f32_16x16x32_bf16 v[32:35],v[200:203],v[56:59],v[32:35]
ds_read_b128 v[84:87],v10 offset:7168
v_mfma_f32_16x16x32_bf16 v[36:39],v[204:207],v[56:59],v[36:39]
v_mfma_f32_16x16x32_bf16 v[40:43],v[88:91],v[56:59],v[40:43]
s_waitcnt lgkmcnt(4)
v_mfma_f32_16x16x32_bf16 v[28:31],v[208:211],v[44:47],v[28:31]
ds_read_b128 v[56:59],v2 offset:56320
v_mfma_f32_16x16x32_bf16 v[32:35],v[212:215],v[44:47],v[32:35]
ds_read_b128 v[88:91],v10 offset:8192
v_mfma_f32_16x16x32_bf16 v[36:39],v[216:219],v[44:47],v[36:39]
s_waitcnt vmcnt(3) lgkmcnt(4)
v_mfma_f32_16x16x32_bf16 v[40:43],v[76:79],v[44:47],v[40:43]
s_barrier
v_mfma_f32_16x16x32_bf16 v[28:31],v[220:223],v[48:51],v[28:31]
ds_read_b128 v[44:47],v2 offset:0
v_mfma_f32_16x16x32_bf16 v[32:35],v[224:227],v[48:51],v[32:35]
v_mfma_f32_16x16x32_bf16 v[36:39],v[228:231],v[48:51],v[36:39]
v_mfma_f32_16x16x32_bf16 v[40:43],v[80:83],v[48:51],v[40:43]
s_waitcnt lgkmcnt(3)
v_mfma_f32_16x16x32_bf16 v[28:31],v[232:235],v[52:55],v[28:31]
ds_read_b128 v[48:51],v2 offset:1024
v_mfma_f32_16x16x32_bf16 v[32:35],v[236:239],v[52:55],v[32:35]
v_mfma_f32_16x16x32_bf16 v[36:39],v[240:243],v[52:55],v[36:39]
v_mfma_f32_16x16x32_bf16 v[40:43],v[84:87],v[52:55],v[40:43]
s_waitcnt lgkmcnt(2)
v_mfma_f32_16x16x32_bf16 v[28:31],v[244:247],v[56:59],v[28:31]
ds_read_b128 v[52:55],v2 offset:2048
v_mfma_f32_16x16x32_bf16 v[32:35],v[248:251],v[56:59],v[32:35]
v_mfma_f32_16x16x32_bf16 v[36:39],v[252:255],v[56:59],v[36:39]
s_waitcnt lgkmcnt(2)
v_mfma_f32_16x16x32_bf16 v[40:43],v[88:91],v[56:59],v[40:43]
s_cbranch_scc1 Lg4_d0w1_%=
s_branch Lg4_slow0_%=
Lg4_d0w2_%=:
v_mfma_f32_16x16x32_bf16 v[12:15],a[0:3],v[44:47],0
s_mov_b32 s73,s47
ds_read_b128 v[56:59],v2 offset:3072
v_lshlrev_b32 v60,16,v68
v_mfma_f32_16x16x32_bf16 v[16:19],a[4:7],v[44:47],0
s_max_u32 s73,s73,s65
v_and_b32 v61,0xffff0000,v68
v_lshlrev_b32 v62,16,v69
v_mfma_f32_16x16x32_bf16 v[20:23],a[8:11],v[44:47],0
s_mov_b32 s65,0
v_and_b32 v63,0xffff0000,v69
v_lshlrev_b32 v64,16,v70
v_mfma_f32_16x16x32_bf16 v[24:27],a[12:15],v[44:47],0
s_waitcnt lgkmcnt(2)
v_and_b32 v65,0xffff0000,v70
v_lshlrev_b32 v66,16,v71
v_mfma_f32_16x16x32_bf16 v[12:15],a[16:19],v[48:51],v[12:15]
s_mul_i32 s61,s49,7
ds_read_b128 v[44:47],v2 offset:4096
v_and_b32 v67,0xffff0000,v71
v_mfma_f32_16x16x32_bf16 v[16:19],a[20:23],v[48:51],v[16:19]
s_lshr_b32 s61,s61,1
v_cvt_pk_bf16_f32 v28,v28,v29
v_cvt_pk_bf16_f32 v29,v30,v31
v_mfma_f32_16x16x32_bf16 v[20:23],a[24:27],v[48:51],v[20:23]
v_cvt_pk_bf16_f32 v30,v32,v33
v_cvt_pk_bf16_f32 v31,v34,v35
v_mfma_f32_16x16x32_bf16 v[24:27],a[28:31],v[48:51],v[24:27]
s_waitcnt lgkmcnt(2)
v_add_f32 v36,v36,v60
v_add_f32 v37,v37,v61
v_mfma_f32_16x16x32_bf16 v[12:15],a[32:35],v[52:55],v[12:15]
ds_read_b128 v[48:51],v2 offset:5120
v_add_f32 v38,v38,v62
v_mfma_f32_16x16x32_bf16 v[16:19],a[36:39],v[52:55],v[16:19]
v_add_f32 v39,v39,v63
v_add_f32 v40,v40,v64
v_mfma_f32_16x16x32_bf16 v[20:23],a[40:43],v[52:55],v[20:23]
buffer_store_dwordx4 v[28:31],v3,s[36:39],s73 offen offset:0 sc1
v_add_f32 v41,v41,v65
v_add_f32 v42,v42,v66
v_add_f32 v43,v43,v67
v_mfma_f32_16x16x32_bf16 v[24:27],a[44:47],v[52:55],v[24:27]
s_waitcnt lgkmcnt(2)
v_cvt_pk_bf16_f32 v36,v36,v37
v_cvt_pk_bf16_f32 v37,v38,v39
v_mfma_f32_16x16x32_bf16 v[12:15],a[48:51],v[56:59],v[12:15]
ds_read_b128 v[52:55],v2 offset:6144
v_cvt_pk_bf16_f32 v38,v40,v41
v_mfma_f32_16x16x32_bf16 v[16:19],a[52:55],v[56:59],v[16:19]
v_cvt_pk_bf16_f32 v39,v42,v43
v_mfma_f32_16x16x32_bf16 v[20:23],a[56:59],v[56:59],v[20:23]
s_waitcnt lgkmcnt(2)
s_nop 0
v_mfma_f32_16x16x32_bf16 v[24:27],a[60:63],v[56:59],v[24:27]
s_barrier
v_mfma_f32_16x16x32_bf16 v[12:15],a[64:67],v[44:47],v[12:15]
ds_read_b128 v[56:59],v2 offset:7168
ds_read_b32 v11,v8 offset:4096
v_mfma_f32_16x16x32_bf16 v[16:19],a[68:71],v[44:47],v[16:19]
v_mfma_f32_16x16x32_bf16 v[20:23],a[72:75],v[44:47],v[20:23]
v_mfma_f32_16x16x32_bf16 v[24:27],a[76:79],v[44:47],v[24:27]
s_waitcnt lgkmcnt(3)
v_mfma_f32_16x16x32_bf16 v[12:15],a[80:83],v[48:51],v[12:15]
s_waitcnt lgkmcnt(0)
ds_read_b128 v[44:47],v2 offset:8192
v_mfma_f32_16x16x32_bf16 v[16:19],a[84:87],v[48:51],v[16:19]
v_mfma_f32_16x16x32_bf16 v[20:23],a[88:91],v[48:51],v[20:23]
buffer_store_dwordx4 v[36:39],v4,s[36:39],s73 offen offset:0 sc1
v_cmp_eq_u32 vcc,s60,v11
v_mfma_f32_16x16x32_bf16 v[24:27],a[92:95],v[48:51],v[24:27]
s_cmp_eq_u32 vcc_lo,-1
s_cselect_b32 s66,s49,s59
v_mfma_f32_16x16x32_bf16 v[12:15],a[96:99],v[52:55],v[12:15]
s_cselect_b32 s61,s61,s59
ds_read_b128 v[48:51],v2 offset:9216
s_add_u32 m0,s63,28672
v_mfma_f32_16x16x32_bf16 v[16:19],a[100:103],v[52:55],v[16:19]
s_add_u32 s53,s52,s55
s_cmp_eq_u32 s53,s56
s_cselect_b32 s53,s57,s53
v_mfma_f32_16x16x32_bf16 v[20:23],a[104:107],v[52:55],v[20:23]
buffer_load_dwordx4 v0,s[36:39],s61 offen offset:0 sc1 lds
v_mfma_f32_16x16x32_bf16 v[24:27],a[108:111],v[52:55],v[24:27]
s_cmp_eq_u32 s53,s58
s_cselect_b32 s53,s59,s53
v_mfma_f32_16x16x32_bf16 v[12:15],a[112:115],v[56:59],v[12:15]
s_cmp_ge_u32 s53,6520832
ds_read_b128 v[52:55],v2 offset:10240
s_cselect_b32 s55,196608,s55
v_mfma_f32_16x16x32_bf16 v[16:19],a[116:119],v[56:59],v[16:19]
v_mfma_f32_16x16x32_bf16 v[20:23],a[120:123],v[56:59],v[20:23]
buffer_load_dwordx4 v0,s[36:39],s61 offen offset:1024 sc1 lds
v_mfma_f32_16x16x32_bf16 v[24:27],a[124:127],v[56:59],v[24:27]
s_waitcnt lgkmcnt(2)
v_mfma_f32_16x16x32_bf16 v[12:15],a[128:131],v[44:47],v[12:15]
ds_read_b128 v[56:59],v2 offset:11264
v_mfma_f32_16x16x32_bf16 v[16:19],a[132:135],v[44:47],v[16:19]
v_mfma_f32_16x16x32_bf16 v[20:23],a[136:139],v[44:47],v[20:23]
buffer_load_dwordx4 v0,s[36:39],s61 offen offset:2048 sc1 lds
v_mfma_f32_16x16x32_bf16 v[24:27],a[140:143],v[44:47],v[24:27]
s_waitcnt lgkmcnt(2)
v_mfma_f32_16x16x32_bf16 v[12:15],a[144:147],v[48:51],v[12:15]
ds_read_b128 v[44:47],v2 offset:12288
v_mfma_f32_16x16x32_bf16 v[16:19],a[148:151],v[48:51],v[16:19]
v_mfma_f32_16x16x32_bf16 v[20:23],a[152:155],v[48:51],v[20:23]
buffer_load_dwordx4 v0,s[36:39],s61 offen offset:3072 sc1 lds
v_mfma_f32_16x16x32_bf16 v[24:27],a[156:159],v[48:51],v[24:27]
s_waitcnt lgkmcnt(2)
s_add_u32 m0,s64,28672
s_cmp_ge_u32 s66,s59
v_mfma_f32_16x16x32_bf16 v[12:15],a[160:163],v[52:55],v[12:15]
ds_read_b128 v[48:51],v2 offset:13312
v_mfma_f32_16x16x32_bf16 v[16:19],a[164:167],v[52:55],v[16:19]
v_mfma_f32_16x16x32_bf16 v[20:23],a[168:171],v[52:55],v[20:23]
buffer_load_dwordx4 v[68:71],v9,s[36:39],s48 offen offset:0 sc1
v_mfma_f32_16x16x32_bf16 v[24:27],a[172:175],v[52:55],v[24:27]
s_waitcnt lgkmcnt(2)
v_mfma_f32_16x16x32_bf16 v[12:15],a[176:179],v[56:59],v[12:15]
ds_read_b128 v[52:55],v2 offset:14336
v_mfma_f32_16x16x32_bf16 v[16:19],a[180:183],v[56:59],v[16:19]
v_mfma_f32_16x16x32_bf16 v[20:23],a[184:187],v[56:59],v[20:23]
v_mfma_f32_16x16x32_bf16 v[24:27],a[188:191],v[56:59],v[24:27])"
R"(
s_waitcnt lgkmcnt(2)
v_mfma_f32_16x16x32_bf16 v[12:15],a[192:195],v[44:47],v[12:15]
ds_read_b128 v[56:59],v2 offset:15360
v_mfma_f32_16x16x32_bf16 v[16:19],a[196:199],v[44:47],v[16:19]
v_mfma_f32_16x16x32_bf16 v[20:23],a[200:203],v[44:47],v[20:23]
s_waitcnt vmcnt(7) lgkmcnt(2)
v_mfma_f32_16x16x32_bf16 v[24:27],a[204:207],v[44:47],v[24:27]
s_barrier
v_mfma_f32_16x16x32_bf16 v[12:15],a[208:211],v[48:51],v[12:15]
ds_read_b128 v[44:47],v2 offset:16384
v_mfma_f32_16x16x32_bf16 v[16:19],a[212:215],v[48:51],v[16:19]
v_mfma_f32_16x16x32_bf16 v[20:23],a[216:219],v[48:51],v[20:23]
v_mfma_f32_16x16x32_bf16 v[24:27],a[220:223],v[48:51],v[24:27]
s_waitcnt lgkmcnt(2)
buffer_load_dwordx4 v1,s[36:39],s61 offen offset:0 sc1 lds
v_mfma_f32_16x16x32_bf16 v[12:15],a[224:227],v[52:55],v[12:15]
ds_read_b128 v[48:51],v2 offset:17408
v_mfma_f32_16x16x32_bf16 v[16:19],a[228:231],v[52:55],v[16:19]
v_mfma_f32_16x16x32_bf16 v[20:23],a[232:235],v[52:55],v[20:23]
v_mfma_f32_16x16x32_bf16 v[24:27],a[236:239],v[52:55],v[24:27]
s_waitcnt lgkmcnt(2)
v_mfma_f32_16x16x32_bf16 v[12:15],a[240:243],v[56:59],v[12:15]
ds_read_b128 v[52:55],v2 offset:18432
v_mfma_f32_16x16x32_bf16 v[16:19],a[244:247],v[56:59],v[16:19]
buffer_load_dwordx4 v1,s[36:39],s61 offen offset:1024 sc1 lds
v_mfma_f32_16x16x32_bf16 v[20:23],a[248:251],v[56:59],v[20:23]
v_mfma_f32_16x16x32_bf16 v[24:27],a[252:255],v[56:59],v[24:27]
s_waitcnt lgkmcnt(2)
v_mfma_f32_16x16x32_bf16 v[12:15],v[100:103],v[44:47],v[12:15]
ds_read_b128 v[56:59],v2 offset:19456
v_mfma_f32_16x16x32_bf16 v[16:19],v[104:107],v[44:47],v[16:19]
ds_read_b128 v[88:91],v10 offset:0
v_mfma_f32_16x16x32_bf16 v[20:23],v[108:111],v[44:47],v[20:23]
v_mfma_f32_16x16x32_bf16 v[24:27],v[112:115],v[44:47],v[24:27]
s_waitcnt lgkmcnt(3)
buffer_load_dwordx4 v1,s[36:39],s61 offen offset:2048 sc1 lds
v_mfma_f32_16x16x32_bf16 v[12:15],v[116:119],v[48:51],v[12:15]
ds_read_b128 v[44:47],v2 offset:20480
v_mfma_f32_16x16x32_bf16 v[16:19],v[120:123],v[48:51],v[16:19]
ds_read_b128 v[76:79],v10 offset:1024
v_mfma_f32_16x16x32_bf16 v[20:23],v[124:127],v[48:51],v[20:23]
v_mfma_f32_16x16x32_bf16 v[24:27],v[128:131],v[48:51],v[24:27]
s_waitcnt lgkmcnt(4)
v_mfma_f32_16x16x32_bf16 v[12:15],v[132:135],v[52:55],v[12:15]
ds_read_b128 v[48:51],v2 offset:21504
v_mfma_f32_16x16x32_bf16 v[16:19],v[136:139],v[52:55],v[16:19]
ds_read_b128 v[80:83],v10 offset:2048
v_mfma_f32_16x16x32_bf16 v[20:23],v[140:143],v[52:55],v[20:23]
v_mfma_f32_16x16x32_bf16 v[24:27],v[144:147],v[52:55],v[24:27]
s_waitcnt lgkmcnt(4)
v_mfma_f32_16x16x32_bf16 v[12:15],v[148:151],v[56:59],v[12:15]
ds_read_b128 v[52:55],v2 offset:22528
v_mfma_f32_16x16x32_bf16 v[16:19],v[152:155],v[56:59],v[16:19]
ds_read_b128 v[84:87],v10 offset:3072
v_mfma_f32_16x16x32_bf16 v[20:23],v[156:159],v[56:59],v[20:23]
v_mfma_f32_16x16x32_bf16 v[24:27],v[88:91],v[56:59],v[24:27]
s_waitcnt lgkmcnt(4)
v_mfma_f32_16x16x32_bf16 v[12:15],v[160:163],v[44:47],v[12:15]
ds_read_b128 v[56:59],v2 offset:23552
v_mfma_f32_16x16x32_bf16 v[16:19],v[164:167],v[44:47],v[16:19]
ds_read_b128 v[88:91],v10 offset:4096
v_mfma_f32_16x16x32_bf16 v[20:23],v[168:171],v[44:47],v[20:23]
v_mfma_f32_16x16x32_bf16 v[24:27],v[76:79],v[44:47],v[24:27]
s_waitcnt lgkmcnt(4)
v_mfma_f32_16x16x32_bf16 v[12:15],v[172:175],v[48:51],v[12:15]
ds_read_b128 v[44:47],v2 offset:24576
v_mfma_f32_16x16x32_bf16 v[16:19],v[176:179],v[48:51],v[16:19]
ds_read_b128 v[76:79],v10 offset:5120
v_mfma_f32_16x16x32_bf16 v[20:23],v[180:183],v[48:51],v[20:23]
s_waitcnt vmcnt(8) lgkmcnt(4)
v_mfma_f32_16x16x32_bf16 v[24:27],v[80:83],v[48:51],v[24:27]
s_barrier
v_mfma_f32_16x16x32_bf16 v[12:15],v[184:187],v[52:55],v[12:15]
ds_read_b128 v[48:51],v2 offset:25600
v_mfma_f32_16x16x32_bf16 v[16:19],v[188:191],v[52:55],v[16:19]
ds_read_b128 v[80:83],v10 offset:6144
v_mfma_f32_16x16x32_bf16 v[20:23],v[192:195],v[52:55],v[20:23]
v_mfma_f32_16x16x32_bf16 v[24:27],v[84:87],v[52:55],v[24:27]
s_waitcnt lgkmcnt(4)
v_mfma_f32_16x16x32_bf16 v[12:15],v[196:199],v[56:59],v[12:15]
ds_read_b128 v[52:55],v2 offset:26624
v_mfma_f32_16x16x32_bf16 v[16:19],v[200:203],v[56:59],v[16:19]
ds_read_b128 v[84:87],v10 offset:7168
v_mfma_f32_16x16x32_bf16 v[20:23],v[204:207],v[56:59],v[20:23]
v_mfma_f32_16x16x32_bf16 v[24:27],v[88:91],v[56:59],v[24:27]
s_waitcnt lgkmcnt(4)
v_mfma_f32_16x16x32_bf16 v[12:15],v[208:211],v[44:47],v[12:15]
ds_read_b128 v[56:59],v2 offset:27648
v_mfma_f32_16x16x32_bf16 v[16:19],v[212:215],v[44:47],v[16:19]
ds_read_b128 v[88:91],v10 offset:8192
v_mfma_f32_16x16x32_bf16 v[20:23],v[216:219],v[44:47],v[20:23]
s_waitcnt vmcnt(3) lgkmcnt(4)
v_mfma_f32_16x16x32_bf16 v[24:27],v[76:79],v[44:47],v[24:27]
s_barrier
v_mfma_f32_16x16x32_bf16 v[12:15],v[220:223],v[48:51],v[12:15]
ds_read_b128 v[44:47],v2 offset:28672
v_mfma_f32_16x16x32_bf16 v[16:19],v[224:227],v[48:51],v[16:19]
v_mfma_f32_16x16x32_bf16 v[20:23],v[228:231],v[48:51],v[20:23]
v_mfma_f32_16x16x32_bf16 v[24:27],v[80:83],v[48:51],v[24:27]
s_waitcnt lgkmcnt(3)
v_mfma_f32_16x16x32_bf16 v[12:15],v[232:235],v[52:55],v[12:15]
ds_read_b128 v[48:51],v2 offset:29696
v_mfma_f32_16x16x32_bf16 v[16:19],v[236:239],v[52:55],v[16:19]
v_mfma_f32_16x16x32_bf16 v[20:23],v[240:243],v[52:55],v[20:23]
v_mfma_f32_16x16x32_bf16 v[24:27],v[84:87],v[52:55],v[24:27]
s_waitcnt lgkmcnt(2)
v_mfma_f32_16x16x32_bf16 v[12:15],v[244:247],v[56:59],v[12:15]
ds_read_b128 v[52:55],v2 offset:30720
v_mfma_f32_16x16x32_bf16 v[16:19],v[248:251],v[56:59],v[16:19]
v_mfma_f32_16x16x32_bf16 v[20:23],v[252:255],v[56:59],v[20:23]
s_waitcnt lgkmcnt(2)
v_mfma_f32_16x16x32_bf16 v[24:27],v[88:91],v[56:59],v[24:27]
s_cbranch_scc1 Lg4_slow1_%=
Lg4_d1w2_%=:
v_mfma_f32_16x16x32_bf16 v[28:31],a[0:3],v[44:47],0
s_mov_b32 s73,s48
ds_read_b128 v[56:59],v2 offset:31744
v_lshlrev_b32 v60,16,v68
v_mfma_f32_16x16x32_bf16 v[32:35],a[4:7],v[44:47],0
s_max_u32 s73,s73,s65
v_and_b32 v61,0xffff0000,v68
v_lshlrev_b32 v62,16,v69
v_mfma_f32_16x16x32_bf16 v[36:39],a[8:11],v[44:47],0
s_mov_b32 s65,0
v_and_b32 v63,0xffff0000,v69
v_lshlrev_b32 v64,16,v70
v_mfma_f32_16x16x32_bf16 v[40:43],a[12:15],v[44:47],0
s_waitcnt lgkmcnt(2)
v_and_b32 v65,0xffff0000,v70
v_lshlrev_b32 v66,16,v71
v_mfma_f32_16x16x32_bf16 v[28:31],a[16:19],v[48:51],v[28:31]
s_mul_i32 s61,s50,7
ds_read_b128 v[44:47],v2 offset:32768
v_and_b32 v67,0xffff0000,v71
v_mfma_f32_16x16x32_bf16 v[32:35],a[20:23],v[48:51],v[32:35]
s_lshr_b32 s61,s61,1
v_cvt_pk_bf16_f32 v12,v12,v13
v_cvt_pk_bf16_f32 v13,v14,v15
v_mfma_f32_16x16x32_bf16 v[36:39],a[24:27],v[48:51],v[36:39]
s_mov_b64 s[44:45],s[46:47]
v_cvt_pk_bf16_f32 v14,v16,v17
v_cvt_pk_bf16_f32 v15,v18,v19
v_mfma_f32_16x16x32_bf16 v[40:43],a[28:31],v[48:51],v[40:43]
s_waitcnt lgkmcnt(2)
v_add_f32 v20,v20,v60
v_add_f32 v21,v21,v61
v_mfma_f32_16x16x32_bf16 v[28:31],a[32:35],v[52:55],v[28:31]
s_mov_b64 s[46:47],s[48:49]
ds_read_b128 v[48:51],v2 offset:33792
v_add_f32 v22,v22,v62
v_mfma_f32_16x16x32_bf16 v[32:35],a[36:39],v[52:55],v[32:35]
v_add_f32 v23,v23,v63
v_add_f32 v24,v24,v64
v_mfma_f32_16x16x32_bf16 v[36:39],a[40:43],v[52:55],v[36:39]
buffer_store_dwordx4 v[12:15],v3,s[36:39],s73 offen offset:0 sc1
v_add_f32 v25,v25,v65
v_add_f32 v26,v26,v66
v_add_f32 v27,v27,v67
v_mfma_f32_16x16x32_bf16 v[40:43],a[44:47],v[52:55],v[40:43]
s_waitcnt lgkmcnt(2)
v_cvt_pk_bf16_f32 v20,v20,v21
v_cvt_pk_bf16_f32 v21,v22,v23
v_mfma_f32_16x16x32_bf16 v[28:31],a[48:51],v[56:59],v[28:31]
ds_read_b128 v[52:55],v2 offset:34816
v_cvt_pk_bf16_f32 v22,v24,v25
v_mfma_f32_16x16x32_bf16 v[32:35],a[52:55],v[56:59],v[32:35]
v_cvt_pk_bf16_f32 v23,v26,v27
v_mfma_f32_16x16x32_bf16 v[36:39],a[56:59],v[56:59],v[36:39]
s_waitcnt lgkmcnt(2)
s_nop 0
v_mfma_f32_16x16x32_bf16 v[40:43],a[60:63],v[56:59],v[40:43]
s_barrier
v_mfma_f32_16x16x32_bf16 v[28:31],a[64:67],v[44:47],v[28:31]
ds_read_b128 v[56:59],v2 offset:35840
ds_read_b32 v11,v8 offset:5120
v_mfma_f32_16x16x32_bf16 v[32:35],a[68:71],v[44:47],v[32:35]
v_mfma_f32_16x16x32_bf16 v[36:39],a[72:75],v[44:47],v[36:39]
v_mfma_f32_16x16x32_bf16 v[40:43],a[76:79],v[44:47],v[40:43]
s_waitcnt lgkmcnt(3)
v_mfma_f32_16x16x32_bf16 v[28:31],a[80:83],v[48:51],v[28:31]
s_waitcnt lgkmcnt(0)
ds_read_b128 v[44:47],v2 offset:36864
v_mfma_f32_16x16x32_bf16 v[32:35],a[84:87],v[48:51],v[32:35]
v_mfma_f32_16x16x32_bf16 v[36:39],a[88:91],v[48:51],v[36:39]
buffer_store_dwordx4 v[20:23],v4,s[36:39],s73 offen offset:0 sc1
v_cmp_eq_u32 vcc,s60,v11
v_mfma_f32_16x16x32_bf16 v[40:43],a[92:95],v[48:51],v[40:43]
s_cmp_eq_u32 vcc_lo,-1
s_cselect_b32 s67,s50,s59
v_mfma_f32_16x16x32_bf16 v[28:31],a[96:99],v[52:55],v[28:31]
s_cselect_b32 s61,s61,s59
ds_read_b128 v[48:51],v2 offset:37888
s_add_u32 m0,s63,0
v_mfma_f32_16x16x32_bf16 v[32:35],a[100:103],v[52:55],v[32:35]
s_add_u32 s74,s53,s55
s_cmp_eq_u32 s74,s56
s_cselect_b32 s74,s57,s74
v_mfma_f32_16x16x32_bf16 v[36:39],a[104:107],v[52:55],v[36:39]
buffer_load_dwordx4 v0,s[36:39],s61 offen offset:0 sc1 lds
v_mfma_f32_16x16x32_bf16 v[40:43],a[108:111],v[52:55],v[40:43])"
R"(
s_cmp_eq_u32 s74,s58
s_cselect_b32 s74,s59,s74
v_mfma_f32_16x16x32_bf16 v[28:31],a[112:115],v[56:59],v[28:31]
s_cmp_ge_u32 s74,6520832
ds_read_b128 v[52:55],v2 offset:38912
s_cselect_b32 s55,196608,s55
v_mfma_f32_16x16x32_bf16 v[32:35],a[116:119],v[56:59],v[32:35]
v_mfma_f32_16x16x32_bf16 v[36:39],a[120:123],v[56:59],v[36:39]
buffer_load_dwordx4 v0,s[36:39],s61 offen offset:1024 sc1 lds
v_mfma_f32_16x16x32_bf16 v[40:43],a[124:127],v[56:59],v[40:43]
s_waitcnt lgkmcnt(2)
v_mfma_f32_16x16x32_bf16 v[28:31],a[128:131],v[44:47],v[28:31]
ds_read_b128 v[56:59],v2 offset:39936
v_mfma_f32_16x16x32_bf16 v[32:35],a[132:135],v[44:47],v[32:35]
v_mfma_f32_16x16x32_bf16 v[36:39],a[136:139],v[44:47],v[36:39]
buffer_load_dwordx4 v0,s[36:39],s61 offen offset:2048 sc1 lds
v_mfma_f32_16x16x32_bf16 v[40:43],a[140:143],v[44:47],v[40:43]
s_waitcnt lgkmcnt(2)
v_mfma_f32_16x16x32_bf16 v[28:31],a[144:147],v[48:51],v[28:31]
ds_read_b128 v[44:47],v2 offset:40960
v_mfma_f32_16x16x32_bf16 v[32:35],a[148:151],v[48:51],v[32:35]
v_mfma_f32_16x16x32_bf16 v[36:39],a[152:155],v[48:51],v[36:39]
buffer_load_dwordx4 v0,s[36:39],s61 offen offset:3072 sc1 lds
v_mfma_f32_16x16x32_bf16 v[40:43],a[156:159],v[48:51],v[40:43]
s_waitcnt lgkmcnt(2)
s_add_u32 m0,s64,0
s_cmp_lt_u32 s67,s59
v_mfma_f32_16x16x32_bf16 v[28:31],a[160:163],v[52:55],v[28:31]
ds_read_b128 v[48:51],v2 offset:41984
v_mfma_f32_16x16x32_bf16 v[32:35],a[164:167],v[52:55],v[32:35]
v_mfma_f32_16x16x32_bf16 v[36:39],a[168:171],v[52:55],v[36:39]
buffer_load_dwordx4 v[68:71],v9,s[36:39],s49 offen offset:0 sc1
v_mfma_f32_16x16x32_bf16 v[40:43],a[172:175],v[52:55],v[40:43]
s_waitcnt lgkmcnt(2)
s_mov_b64 s[48:49],s[50:51]
s_mov_b64 s[50:51],s[52:53]
v_mfma_f32_16x16x32_bf16 v[28:31],a[176:179],v[56:59],v[28:31]
s_mov_b32 s52,s74
ds_read_b128 v[52:55],v2 offset:43008
v_mfma_f32_16x16x32_bf16 v[32:35],a[180:183],v[56:59],v[32:35]
v_mfma_f32_16x16x32_bf16 v[36:39],a[184:187],v[56:59],v[36:39]
v_mfma_f32_16x16x32_bf16 v[40:43],a[188:191],v[56:59],v[40:43]
s_waitcnt lgkmcnt(2)
v_mfma_f32_16x16x32_bf16 v[28:31],a[192:195],v[44:47],v[28:31]
ds_read_b128 v[56:59],v2 offset:44032
v_mfma_f32_16x16x32_bf16 v[32:35],a[196:199],v[44:47],v[32:35]
v_mfma_f32_16x16x32_bf16 v[36:39],a[200:203],v[44:47],v[36:39]
s_waitcnt vmcnt(7) lgkmcnt(2)
v_mfma_f32_16x16x32_bf16 v[40:43],a[204:207],v[44:47],v[40:43]
s_barrier
v_mfma_f32_16x16x32_bf16 v[28:31],a[208:211],v[48:51],v[28:31]
ds_read_b128 v[44:47],v2 offset:45056
v_mfma_f32_16x16x32_bf16 v[32:35],a[212:215],v[48:51],v[32:35]
v_mfma_f32_16x16x32_bf16 v[36:39],a[216:219],v[48:51],v[36:39]
v_mfma_f32_16x16x32_bf16 v[40:43],a[220:223],v[48:51],v[40:43]
s_waitcnt lgkmcnt(2)
buffer_load_dwordx4 v1,s[36:39],s61 offen offset:0 sc1 lds
v_mfma_f32_16x16x32_bf16 v[28:31],a[224:227],v[52:55],v[28:31]
ds_read_b128 v[48:51],v2 offset:46080
v_mfma_f32_16x16x32_bf16 v[32:35],a[228:231],v[52:55],v[32:35]
v_mfma_f32_16x16x32_bf16 v[36:39],a[232:235],v[52:55],v[36:39]
v_mfma_f32_16x16x32_bf16 v[40:43],a[236:239],v[52:55],v[40:43]
s_waitcnt lgkmcnt(2)
v_mfma_f32_16x16x32_bf16 v[28:31],a[240:243],v[56:59],v[28:31]
ds_read_b128 v[52:55],v2 offset:47104
v_mfma_f32_16x16x32_bf16 v[32:35],a[244:247],v[56:59],v[32:35]
buffer_load_dwordx4 v1,s[36:39],s61 offen offset:1024 sc1 lds
v_mfma_f32_16x16x32_bf16 v[36:39],a[248:251],v[56:59],v[36:39]
v_mfma_f32_16x16x32_bf16 v[40:43],a[252:255],v[56:59],v[40:43]
s_waitcnt lgkmcnt(2)
v_mfma_f32_16x16x32_bf16 v[28:31],v[100:103],v[44:47],v[28:31]
ds_read_b128 v[56:59],v2 offset:48128
v_mfma_f32_16x16x32_bf16 v[32:35],v[104:107],v[44:47],v[32:35]
ds_read_b128 v[88:91],v10 offset:0
v_mfma_f32_16x16x32_bf16 v[36:39],v[108:111],v[44:47],v[36:39]
v_mfma_f32_16x16x32_bf16 v[40:43],v[112:115],v[44:47],v[40:43]
s_waitcnt lgkmcnt(3)
buffer_load_dwordx4 v1,s[36:39],s61 offen offset:2048 sc1 lds
v_mfma_f32_16x16x32_bf16 v[28:31],v[116:119],v[48:51],v[28:31]
ds_read_b128 v[44:47],v2 offset:49152
v_mfma_f32_16x16x32_bf16 v[32:35],v[120:123],v[48:51],v[32:35]
ds_read_b128 v[76:79],v10 offset:1024
v_mfma_f32_16x16x32_bf16 v[36:39],v[124:127],v[48:51],v[36:39]
v_mfma_f32_16x16x32_bf16 v[40:43],v[128:131],v[48:51],v[40:43]
s_waitcnt lgkmcnt(4)
v_mfma_f32_16x16x32_bf16 v[28:31],v[132:135],v[52:55],v[28:31]
ds_read_b128 v[48:51],v2 offset:50176
v_mfma_f32_16x16x32_bf16 v[32:35],v[136:139],v[52:55],v[32:35]
ds_read_b128 v[80:83],v10 offset:2048
v_mfma_f32_16x16x32_bf16 v[36:39],v[140:143],v[52:55],v[36:39]
v_mfma_f32_16x16x32_bf16 v[40:43],v[144:147],v[52:55],v[40:43]
s_waitcnt lgkmcnt(4)
v_mfma_f32_16x16x32_bf16 v[28:31],v[148:151],v[56:59],v[28:31]
ds_read_b128 v[52:55],v2 offset:51200
v_mfma_f32_16x16x32_bf16 v[32:35],v[152:155],v[56:59],v[32:35]
ds_read_b128 v[84:87],v10 offset:3072
v_mfma_f32_16x16x32_bf16 v[36:39],v[156:159],v[56:59],v[36:39]
v_mfma_f32_16x16x32_bf16 v[40:43],v[88:91],v[56:59],v[40:43]
s_waitcnt lgkmcnt(4)
v_mfma_f32_16x16x32_bf16 v[28:31],v[160:163],v[44:47],v[28:31]
ds_read_b128 v[56:59],v2 offset:52224
v_mfma_f32_16x16x32_bf16 v[32:35],v[164:167],v[44:47],v[32:35]
ds_read_b128 v[88:91],v10 offset:4096
v_mfma_f32_16x16x32_bf16 v[36:39],v[168:171],v[44:47],v[36:39]
v_mfma_f32_16x16x32_bf16 v[40:43],v[76:79],v[44:47],v[40:43]
s_waitcnt lgkmcnt(4)
v_mfma_f32_16x16x32_bf16 v[28:31],v[172:175],v[48:51],v[28:31]
ds_read_b128 v[44:47],v2 offset:53248
v_mfma_f32_16x16x32_bf16 v[32:35],v[176:179],v[48:51],v[32:35]
ds_read_b128 v[76:79],v10 offset:5120
v_mfma_f32_16x16x32_bf16 v[36:39],v[180:183],v[48:51],v[36:39]
s_waitcnt vmcnt(8) lgkmcnt(4)
v_mfma_f32_16x16x32_bf16 v[40:43],v[80:83],v[48:51],v[40:43]
s_barrier
v_mfma_f32_16x16x32_bf16 v[28:31],v[184:187],v[52:55],v[28:31]
ds_read_b128 v[48:51],v2 offset:54272
v_mfma_f32_16x16x32_bf16 v[32:35],v[188:191],v[52:55],v[32:35]
ds_read_b128 v[80:83],v10 offset:6144
v_mfma_f32_16x16x32_bf16 v[36:39],v[192:195],v[52:55],v[36:39]
v_mfma_f32_16x16x32_bf16 v[40:43],v[84:87],v[52:55],v[40:43]
s_waitcnt lgkmcnt(4)
v_mfma_f32_16x16x32_bf16 v[28:31],v[196:199],v[56:59],v[28:31]
ds_read_b128 v[52:55],v2 offset:55296
v_mfma_f32_16x16x32_bf16 v[32:35],v[200:203],v[56:59],v[32:35]
ds_read_b128 v[84:87],v10 offset:7168
v_mfma_f32_16x16x32_bf16 v[36:39],v[204:207],v[56:59],v[36:39]
v_mfma_f32_16x16x32_bf16 v[40:43],v[88:91],v[56:59],v[40:43]
s_waitcnt lgkmcnt(4)
v_mfma_f32_16x16x32_bf16 v[28:31],v[208:211],v[44:47],v[28:31]
ds_read_b128 v[56:59],v2 offset:56320
v_mfma_f32_16x16x32_bf16 v[32:35],v[212:215],v[44:47],v[32:35]
ds_read_b128 v[88:91],v10 offset:8192
v_mfma_f32_16x16x32_bf16 v[36:39],v[216:219],v[44:47],v[36:39]
s_waitcnt vmcnt(3) lgkmcnt(4)
v_mfma_f32_16x16x32_bf16 v[40:43],v[76:79],v[44:47],v[40:43]
s_barrier
v_mfma_f32_16x16x32_bf16 v[28:31],v[220:223],v[48:51],v[28:31]
ds_read_b128 v[44:47],v2 offset:0
v_mfma_f32_16x16x32_bf16 v[32:35],v[224:227],v[48:51],v[32:35]
v_mfma_f32_16x16x32_bf16 v[36:39],v[228:231],v[48:51],v[36:39]
v_mfma_f32_16x16x32_bf16 v[40:43],v[80:83],v[48:51],v[40:43]
s_waitcnt lgkmcnt(3)
v_mfma_f32_16x16x32_bf16 v[28:31],v[232:235],v[52:55],v[28:31]
ds_read_b128 v[48:51],v2 offset:1024
v_mfma_f32_16x16x32_bf16 v[32:35],v[236:239],v[52:55],v[32:35]
v_mfma_f32_16x16x32_bf16 v[36:39],v[240:243],v[52:55],v[36:39]
v_mfma_f32_16x16x32_bf16 v[40:43],v[84:87],v[52:55],v[40:43]
s_waitcnt lgkmcnt(2)
v_mfma_f32_16x16x32_bf16 v[28:31],v[244:247],v[56:59],v[28:31]
ds_read_b128 v[52:55],v2 offset:2048
v_mfma_f32_16x16x32_bf16 v[32:35],v[248:251],v[56:59],v[32:35]
v_mfma_f32_16x16x32_bf16 v[36:39],v[252:255],v[56:59],v[36:39]
s_waitcnt lgkmcnt(2)
v_mfma_f32_16x16x32_bf16 v[40:43],v[88:91],v[56:59],v[40:43]
s_cbranch_scc1 Lg4_d0w2_%=
s_branch Lg4_slow0_%=
Lg4_d0w3_%=:
v_mfma_f32_16x16x32_bf16 v[12:15],a[0:3],v[44:47],0
s_mov_b32 s73,s47
ds_read_b128 v[56:59],v2 offset:3072
v_lshlrev_b32 v60,16,v68
v_mfma_f32_16x16x32_bf16 v[16:19],a[4:7],v[44:47],0
s_max_u32 s73,s73,s65
v_and_b32 v61,0xffff0000,v68
v_lshlrev_b32 v62,16,v69
v_mfma_f32_16x16x32_bf16 v[20:23],a[8:11],v[44:47],0
s_mov_b32 s65,0
v_and_b32 v63,0xffff0000,v69
v_lshlrev_b32 v64,16,v70
v_mfma_f32_16x16x32_bf16 v[24:27],a[12:15],v[44:47],0
s_waitcnt lgkmcnt(2)
v_and_b32 v65,0xffff0000,v70
v_lshlrev_b32 v66,16,v71
v_mfma_f32_16x16x32_bf16 v[12:15],a[16:19],v[48:51],v[12:15]
s_mul_i32 s61,s49,7
ds_read_b128 v[44:47],v2 offset:4096
v_and_b32 v67,0xffff0000,v71
v_mfma_f32_16x16x32_bf16 v[16:19],a[20:23],v[48:51],v[16:19]
s_lshr_b32 s61,s61,1
v_cvt_pk_bf16_f32 v28,v28,v29
v_cvt_pk_bf16_f32 v29,v30,v31
v_mfma_f32_16x16x32_bf16 v[20:23],a[24:27],v[48:51],v[20:23]
v_cvt_pk_bf16_f32 v30,v32,v33
v_cvt_pk_bf16_f32 v31,v34,v35
v_mfma_f32_16x16x32_bf16 v[24:27],a[28:31],v[48:51],v[24:27]
s_waitcnt lgkmcnt(2)
v_add_f32 v36,v36,v60
v_add_f32 v37,v37,v61
v_mfma_f32_16x16x32_bf16 v[12:15],a[32:35],v[52:55],v[12:15]
ds_read_b128 v[48:51],v2 offset:5120
v_add_f32 v38,v38,v62
v_mfma_f32_16x16x32_bf16 v[16:19],a[36:39],v[52:55],v[16:19]
v_add_f32 v39,v39,v63
v_add_f32 v40,v40,v64
v_mfma_f32_16x16x32_bf16 v[20:23],a[40:43],v[52:55],v[20:23]
v_add_f32 v41,v41,v65
v_add_f32 v42,v42,v66
v_mfma_f32_16x16x32_bf16 v[24:27],a[44:47],v[52:55],v[24:27]
s_waitcnt lgkmcnt(2)
buffer_store_dwordx4 v[28:31],v3,s[36:39],s73 offen offset:0 sc1)"
R"(
v_add_f32 v43,v43,v67
v_cvt_pk_bf16_f32 v36,v36,v37
v_cvt_pk_bf16_f32 v37,v38,v39
v_mfma_f32_16x16x32_bf16 v[12:15],a[48:51],v[56:59],v[12:15]
ds_read_b128 v[52:55],v2 offset:6144
v_cvt_pk_bf16_f32 v38,v40,v41
v_mfma_f32_16x16x32_bf16 v[16:19],a[52:55],v[56:59],v[16:19]
v_cvt_pk_bf16_f32 v39,v42,v43
v_mfma_f32_16x16x32_bf16 v[20:23],a[56:59],v[56:59],v[20:23]
s_waitcnt lgkmcnt(2)
s_nop 0
v_mfma_f32_16x16x32_bf16 v[24:27],a[60:63],v[56:59],v[24:27]
s_barrier
v_mfma_f32_16x16x32_bf16 v[12:15],a[64:67],v[44:47],v[12:15]
ds_read_b128 v[56:59],v2 offset:7168
ds_read_b32 v11,v8 offset:4096
v_mfma_f32_16x16x32_bf16 v[16:19],a[68:71],v[44:47],v[16:19]
v_mfma_f32_16x16x32_bf16 v[20:23],a[72:75],v[44:47],v[20:23]
v_mfma_f32_16x16x32_bf16 v[24:27],a[76:79],v[44:47],v[24:27]
s_waitcnt lgkmcnt(3)
v_mfma_f32_16x16x32_bf16 v[12:15],a[80:83],v[48:51],v[12:15]
s_waitcnt lgkmcnt(0)
ds_read_b128 v[44:47],v2 offset:8192
v_mfma_f32_16x16x32_bf16 v[16:19],a[84:87],v[48:51],v[16:19]
v_mfma_f32_16x16x32_bf16 v[20:23],a[88:91],v[48:51],v[20:23]
v_mfma_f32_16x16x32_bf16 v[24:27],a[92:95],v[48:51],v[24:27]
buffer_store_dwordx4 v[36:39],v4,s[36:39],s73 offen offset:0 sc1
v_cmp_eq_u32 vcc,s60,v11
v_mfma_f32_16x16x32_bf16 v[12:15],a[96:99],v[52:55],v[12:15]
s_cmp_eq_u32 vcc_lo,-1
ds_read_b128 v[48:51],v2 offset:9216
s_cselect_b32 s66,s49,s59
v_mfma_f32_16x16x32_bf16 v[16:19],a[100:103],v[52:55],v[16:19]
s_cselect_b32 s61,s61,s59
s_add_u32 m0,s63,28672
s_add_u32 s53,s52,s55
v_mfma_f32_16x16x32_bf16 v[20:23],a[104:107],v[52:55],v[20:23]
s_cmp_eq_u32 s53,s56
s_cselect_b32 s53,s57,s53
s_cmp_eq_u32 s53,s58
v_mfma_f32_16x16x32_bf16 v[24:27],a[108:111],v[52:55],v[24:27]
buffer_load_dwordx4 v0,s[36:39],s61 offen offset:0 sc1 lds
v_mfma_f32_16x16x32_bf16 v[12:15],a[112:115],v[56:59],v[12:15]
s_cselect_b32 s53,s59,s53
ds_read_b128 v[52:55],v2 offset:10240
s_cmp_ge_u32 s53,6520832
v_mfma_f32_16x16x32_bf16 v[16:19],a[116:119],v[56:59],v[16:19]
s_cselect_b32 s55,196608,s55
v_mfma_f32_16x16x32_bf16 v[20:23],a[120:123],v[56:59],v[20:23]
v_mfma_f32_16x16x32_bf16 v[24:27],a[124:127],v[56:59],v[24:27]
s_waitcnt lgkmcnt(2)
buffer_load_dwordx4 v0,s[36:39],s61 offen offset:1024 sc1 lds
v_mfma_f32_16x16x32_bf16 v[12:15],a[128:131],v[44:47],v[12:15]
ds_read_b128 v[56:59],v2 offset:11264
v_mfma_f32_16x16x32_bf16 v[16:19],a[132:135],v[44:47],v[16:19]
v_mfma_f32_16x16x32_bf16 v[20:23],a[136:139],v[44:47],v[20:23]
v_mfma_f32_16x16x32_bf16 v[24:27],a[140:143],v[44:47],v[24:27]
s_waitcnt lgkmcnt(2)
buffer_load_dwordx4 v0,s[36:39],s61 offen offset:2048 sc1 lds
v_mfma_f32_16x16x32_bf16 v[12:15],a[144:147],v[48:51],v[12:15]
ds_read_b128 v[44:47],v2 offset:12288
v_mfma_f32_16x16x32_bf16 v[16:19],a[148:151],v[48:51],v[16:19]
v_mfma_f32_16x16x32_bf16 v[20:23],a[152:155],v[48:51],v[20:23]
v_mfma_f32_16x16x32_bf16 v[24:27],a[156:159],v[48:51],v[24:27]
s_waitcnt lgkmcnt(2)
buffer_load_dwordx4 v0,s[36:39],s61 offen offset:3072 sc1 lds
v_mfma_f32_16x16x32_bf16 v[12:15],a[160:163],v[52:55],v[12:15]
s_add_u32 m0,s64,28672
ds_read_b128 v[48:51],v2 offset:13312
s_lshr_b32 s70,s47,4
v_mfma_f32_16x16x32_bf16 v[16:19],a[164:167],v[52:55],v[16:19]
s_cmp_ge_u32 s66,s59
v_mfma_f32_16x16x32_bf16 v[20:23],a[168:171],v[52:55],v[20:23]
v_mfma_f32_16x16x32_bf16 v[24:27],a[172:175],v[52:55],v[24:27]
s_waitcnt lgkmcnt(2)
buffer_load_dwordx4 v[68:71],v9,s[36:39],s48 offen offset:0 sc1
v_mfma_f32_16x16x32_bf16 v[12:15],a[176:179],v[56:59],v[12:15]
ds_read_b128 v[52:55],v2 offset:14336
v_mfma_f32_16x16x32_bf16 v[16:19],a[180:183],v[56:59],v[16:19]
v_mfma_f32_16x16x32_bf16 v[20:23],a[184:187],v[56:59],v[20:23]
v_mfma_f32_16x16x32_bf16 v[24:27],a[188:191],v[56:59],v[24:27]
s_waitcnt lgkmcnt(2)
v_mfma_f32_16x16x32_bf16 v[12:15],a[192:195],v[44:47],v[12:15]
ds_read_b128 v[56:59],v2 offset:15360
v_mfma_f32_16x16x32_bf16 v[16:19],a[196:199],v[44:47],v[16:19]
v_mfma_f32_16x16x32_bf16 v[20:23],a[200:203],v[44:47],v[20:23]
s_waitcnt vmcnt(8) lgkmcnt(2)
v_mfma_f32_16x16x32_bf16 v[24:27],a[204:207],v[44:47],v[24:27]
s_barrier
v_mfma_f32_16x16x32_bf16 v[12:15],a[208:211],v[48:51],v[12:15]
ds_read_b128 v[44:47],v2 offset:16384
v_mfma_f32_16x16x32_bf16 v[16:19],a[212:215],v[48:51],v[16:19]
v_mfma_f32_16x16x32_bf16 v[20:23],a[216:219],v[48:51],v[20:23]
v_mfma_f32_16x16x32_bf16 v[24:27],a[220:223],v[48:51],v[24:27]
s_waitcnt lgkmcnt(2)
v_mfma_f32_16x16x32_bf16 v[12:15],a[224:227],v[52:55],v[12:15]
buffer_load_dwordx4 v1,s[36:39],s61 offen offset:0 sc1 lds
v_mfma_f32_16x16x32_bf16 v[16:19],a[228:231],v[52:55],v[16:19]
ds_read_b128 v[48:51],v2 offset:17408
v_mfma_f32_16x16x32_bf16 v[20:23],a[232:235],v[52:55],v[20:23]
v_mfma_f32_16x16x32_bf16 v[24:27],a[236:239],v[52:55],v[24:27]
s_waitcnt lgkmcnt(2)
v_mfma_f32_16x16x32_bf16 v[12:15],a[240:243],v[56:59],v[12:15]
ds_read_b128 v[52:55],v2 offset:18432
v_mfma_f32_16x16x32_bf16 v[16:19],a[244:247],v[56:59],v[16:19]
v_mfma_f32_16x16x32_bf16 v[20:23],a[248:251],v[56:59],v[20:23]
buffer_load_dwordx4 v1,s[36:39],s61 offen offset:1024 sc1 lds
v_mfma_f32_16x16x32_bf16 v[24:27],a[252:255],v[56:59],v[24:27]
s_waitcnt lgkmcnt(2)
v_mfma_f32_16x16x32_bf16 v[12:15],v[100:103],v[44:47],v[12:15]
ds_read_b128 v[56:59],v2 offset:19456
v_mfma_f32_16x16x32_bf16 v[16:19],v[104:107],v[44:47],v[16:19]
ds_read_b128 v[88:91],v10 offset:0
v_mfma_f32_16x16x32_bf16 v[20:23],v[108:111],v[44:47],v[20:23]
v_mfma_f32_16x16x32_bf16 v[24:27],v[112:115],v[44:47],v[24:27]
s_waitcnt lgkmcnt(3)
v_mfma_f32_16x16x32_bf16 v[12:15],v[116:119],v[48:51],v[12:15]
buffer_load_dwordx4 v1,s[36:39],s61 offen offset:2048 sc1 lds
v_mfma_f32_16x16x32_bf16 v[16:19],v[120:123],v[48:51],v[16:19]
ds_read_b128 v[44:47],v2 offset:20480
ds_read_b128 v[76:79],v10 offset:1024
v_mfma_f32_16x16x32_bf16 v[20:23],v[124:127],v[48:51],v[20:23]
v_mfma_f32_16x16x32_bf16 v[24:27],v[128:131],v[48:51],v[24:27]
s_waitcnt lgkmcnt(4)
v_mfma_f32_16x16x32_bf16 v[12:15],v[132:135],v[52:55],v[12:15]
ds_read_b128 v[48:51],v2 offset:21504
v_mfma_f32_16x16x32_bf16 v[16:19],v[136:139],v[52:55],v[16:19]
ds_read_b128 v[80:83],v10 offset:2048
v_mfma_f32_16x16x32_bf16 v[20:23],v[140:143],v[52:55],v[20:23]
v_mfma_f32_16x16x32_bf16 v[24:27],v[144:147],v[52:55],v[24:27]
s_waitcnt lgkmcnt(4)
v_mfma_f32_16x16x32_bf16 v[12:15],v[148:151],v[56:59],v[12:15]
ds_read_b128 v[52:55],v2 offset:22528
v_mfma_f32_16x16x32_bf16 v[16:19],v[152:155],v[56:59],v[16:19]
ds_read_b128 v[84:87],v10 offset:3072
v_mfma_f32_16x16x32_bf16 v[20:23],v[156:159],v[56:59],v[20:23]
v_mfma_f32_16x16x32_bf16 v[24:27],v[88:91],v[56:59],v[24:27]
s_waitcnt lgkmcnt(4)
v_mfma_f32_16x16x32_bf16 v[12:15],v[160:163],v[44:47],v[12:15]
ds_read_b128 v[56:59],v2 offset:23552
v_mfma_f32_16x16x32_bf16 v[16:19],v[164:167],v[44:47],v[16:19]
ds_read_b128 v[88:91],v10 offset:4096
v_mfma_f32_16x16x32_bf16 v[20:23],v[168:171],v[44:47],v[20:23]
v_mfma_f32_16x16x32_bf16 v[24:27],v[76:79],v[44:47],v[24:27]
s_waitcnt lgkmcnt(4)
v_mfma_f32_16x16x32_bf16 v[12:15],v[172:175],v[48:51],v[12:15]
ds_read_b128 v[44:47],v2 offset:24576
v_mfma_f32_16x16x32_bf16 v[16:19],v[176:179],v[48:51],v[16:19]
ds_read_b128 v[76:79],v10 offset:5120
v_mfma_f32_16x16x32_bf16 v[20:23],v[180:183],v[48:51],v[20:23]
s_waitcnt vmcnt(8) lgkmcnt(4)
v_mfma_f32_16x16x32_bf16 v[24:27],v[80:83],v[48:51],v[24:27]
s_barrier
v_mfma_f32_16x16x32_bf16 v[12:15],v[184:187],v[52:55],v[12:15]
buffer_store_dword v6,v5,s[36:39],s70 offen offset:0 sc1
ds_read_b128 v[48:51],v2 offset:25600
v_mfma_f32_16x16x32_bf16 v[16:19],v[188:191],v[52:55],v[16:19]
ds_read_b128 v[80:83],v10 offset:6144
v_mfma_f32_16x16x32_bf16 v[20:23],v[192:195],v[52:55],v[20:23]
v_mfma_f32_16x16x32_bf16 v[24:27],v[84:87],v[52:55],v[24:27]
s_waitcnt lgkmcnt(4)
v_mfma_f32_16x16x32_bf16 v[12:15],v[196:199],v[56:59],v[12:15]
ds_read_b128 v[52:55],v2 offset:26624
v_mfma_f32_16x16x32_bf16 v[16:19],v[200:203],v[56:59],v[16:19]
ds_read_b128 v[84:87],v10 offset:7168
v_mfma_f32_16x16x32_bf16 v[20:23],v[204:207],v[56:59],v[20:23]
v_mfma_f32_16x16x32_bf16 v[24:27],v[88:91],v[56:59],v[24:27]
s_waitcnt lgkmcnt(4)
v_mfma_f32_16x16x32_bf16 v[12:15],v[208:211],v[44:47],v[12:15]
ds_read_b128 v[56:59],v2 offset:27648
v_mfma_f32_16x16x32_bf16 v[16:19],v[212:215],v[44:47],v[16:19]
ds_read_b128 v[88:91],v10 offset:8192
v_mfma_f32_16x16x32_bf16 v[20:23],v[216:219],v[44:47],v[20:23]
s_waitcnt vmcnt(4) lgkmcnt(4)
v_mfma_f32_16x16x32_bf16 v[24:27],v[76:79],v[44:47],v[24:27]
s_barrier
v_mfma_f32_16x16x32_bf16 v[12:15],v[220:223],v[48:51],v[12:15]
ds_read_b128 v[44:47],v2 offset:28672
v_mfma_f32_16x16x32_bf16 v[16:19],v[224:227],v[48:51],v[16:19]
v_mfma_f32_16x16x32_bf16 v[20:23],v[228:231],v[48:51],v[20:23]
v_mfma_f32_16x16x32_bf16 v[24:27],v[80:83],v[48:51],v[24:27]
s_waitcnt lgkmcnt(3)
v_mfma_f32_16x16x32_bf16 v[12:15],v[232:235],v[52:55],v[12:15]
ds_read_b128 v[48:51],v2 offset:29696
v_mfma_f32_16x16x32_bf16 v[16:19],v[236:239],v[52:55],v[16:19]
v_mfma_f32_16x16x32_bf16 v[20:23],v[240:243],v[52:55],v[20:23]
v_mfma_f32_16x16x32_bf16 v[24:27],v[84:87],v[52:55],v[24:27]
s_waitcnt lgkmcnt(2)
v_mfma_f32_16x16x32_bf16 v[12:15],v[244:247],v[56:59],v[12:15]
ds_read_b128 v[52:55],v2 offset:30720
v_mfma_f32_16x16x32_bf16 v[16:19],v[248:251],v[56:59],v[16:19]
v_mfma_f32_16x16x32_bf16 v[20:23],v[252:255],v[56:59],v[20:23]
s_waitcnt lgkmcnt(2)
v_mfma_f32_16x16x32_bf16 v[24:27],v[88:91],v[56:59],v[24:27]
s_cbranch_scc1 Lg4_slow1_%=
Lg4_d1w3_%=:
v_mfma_f32_16x16x32_bf16 v[28:31],a[0:3],v[44:47],0
s_mov_b32 s73,s48
ds_read_b128 v[56:59],v2 offset:31744
v_lshlrev_b32 v60,16,v68)"
R"(
v_mfma_f32_16x16x32_bf16 v[32:35],a[4:7],v[44:47],0
s_max_u32 s73,s73,s65
v_and_b32 v61,0xffff0000,v68
v_lshlrev_b32 v62,16,v69
v_mfma_f32_16x16x32_bf16 v[36:39],a[8:11],v[44:47],0
s_mov_b32 s65,0
v_and_b32 v63,0xffff0000,v69
v_lshlrev_b32 v64,16,v70
v_mfma_f32_16x16x32_bf16 v[40:43],a[12:15],v[44:47],0
s_waitcnt lgkmcnt(2)
v_and_b32 v65,0xffff0000,v70
v_lshlrev_b32 v66,16,v71
v_mfma_f32_16x16x32_bf16 v[28:31],a[16:19],v[48:51],v[28:31]
s_mul_i32 s61,s50,7
ds_read_b128 v[44:47],v2 offset:32768
v_and_b32 v67,0xffff0000,v71
v_mfma_f32_16x16x32_bf16 v[32:35],a[20:23],v[48:51],v[32:35]
s_lshr_b32 s61,s61,1
v_cvt_pk_bf16_f32 v12,v12,v13
v_cvt_pk_bf16_f32 v13,v14,v15
v_mfma_f32_16x16x32_bf16 v[36:39],a[24:27],v[48:51],v[36:39]
s_mov_b64 s[44:45],s[46:47]
v_cvt_pk_bf16_f32 v14,v16,v17
v_cvt_pk_bf16_f32 v15,v18,v19
v_mfma_f32_16x16x32_bf16 v[40:43],a[28:31],v[48:51],v[40:43]
s_waitcnt lgkmcnt(2)
v_add_f32 v20,v20,v60
v_add_f32 v21,v21,v61
v_mfma_f32_16x16x32_bf16 v[28:31],a[32:35],v[52:55],v[28:31]
s_mov_b64 s[46:47],s[48:49]
ds_read_b128 v[48:51],v2 offset:33792
v_add_f32 v22,v22,v62
v_mfma_f32_16x16x32_bf16 v[32:35],a[36:39],v[52:55],v[32:35]
v_add_f32 v23,v23,v63
v_add_f32 v24,v24,v64
v_mfma_f32_16x16x32_bf16 v[36:39],a[40:43],v[52:55],v[36:39]
v_add_f32 v25,v25,v65
v_add_f32 v26,v26,v66
v_mfma_f32_16x16x32_bf16 v[40:43],a[44:47],v[52:55],v[40:43]
s_waitcnt lgkmcnt(2)
buffer_store_dwordx4 v[12:15],v3,s[36:39],s73 offen offset:0 sc1
v_add_f32 v27,v27,v67
v_cvt_pk_bf16_f32 v20,v20,v21
v_cvt_pk_bf16_f32 v21,v22,v23
v_mfma_f32_16x16x32_bf16 v[28:31],a[48:51],v[56:59],v[28:31]
ds_read_b128 v[52:55],v2 offset:34816
v_cvt_pk_bf16_f32 v22,v24,v25
v_mfma_f32_16x16x32_bf16 v[32:35],a[52:55],v[56:59],v[32:35]
v_cvt_pk_bf16_f32 v23,v26,v27
v_mfma_f32_16x16x32_bf16 v[36:39],a[56:59],v[56:59],v[36:39]
s_waitcnt lgkmcnt(2)
s_nop 0
v_mfma_f32_16x16x32_bf16 v[40:43],a[60:63],v[56:59],v[40:43]
s_barrier
v_mfma_f32_16x16x32_bf16 v[28:31],a[64:67],v[44:47],v[28:31]
ds_read_b128 v[56:59],v2 offset:35840
ds_read_b32 v11,v8 offset:5120
v_mfma_f32_16x16x32_bf16 v[32:35],a[68:71],v[44:47],v[32:35]
v_mfma_f32_16x16x32_bf16 v[36:39],a[72:75],v[44:47],v[36:39]
v_mfma_f32_16x16x32_bf16 v[40:43],a[76:79],v[44:47],v[40:43]
s_waitcnt lgkmcnt(3)
v_mfma_f32_16x16x32_bf16 v[28:31],a[80:83],v[48:51],v[28:31]
s_waitcnt lgkmcnt(0)
ds_read_b128 v[44:47],v2 offset:36864
v_mfma_f32_16x16x32_bf16 v[32:35],a[84:87],v[48:51],v[32:35]
v_mfma_f32_16x16x32_bf16 v[36:39],a[88:91],v[48:51],v[36:39]
v_mfma_f32_16x16x32_bf16 v[40:43],a[92:95],v[48:51],v[40:43]
buffer_store_dwordx4 v[20:23],v4,s[36:39],s73 offen offset:0 sc1
v_cmp_eq_u32 vcc,s60,v11
v_mfma_f32_16x16x32_bf16 v[28:31],a[96:99],v[52:55],v[28:31]
s_cmp_eq_u32 vcc_lo,-1
ds_read_b128 v[48:51],v2 offset:37888
s_cselect_b32 s67,s50,s59
v_mfma_f32_16x16x32_bf16 v[32:35],a[100:103],v[52:55],v[32:35]
s_cselect_b32 s61,s61,s59
s_add_u32 m0,s63,0
s_add_u32 s74,s53,s55
v_mfma_f32_16x16x32_bf16 v[36:39],a[104:107],v[52:55],v[36:39]
s_cmp_eq_u32 s74,s56
s_cselect_b32 s74,s57,s74
s_cmp_eq_u32 s74,s58
v_mfma_f32_16x16x32_bf16 v[40:43],a[108:111],v[52:55],v[40:43]
buffer_load_dwordx4 v0,s[36:39],s61 offen offset:0 sc1 lds
v_mfma_f32_16x16x32_bf16 v[28:31],a[112:115],v[56:59],v[28:31]
s_cselect_b32 s74,s59,s74
ds_read_b128 v[52:55],v2 offset:38912
s_cmp_ge_u32 s74,6520832
v_mfma_f32_16x16x32_bf16 v[32:35],a[116:119],v[56:59],v[32:35]
s_cselect_b32 s55,196608,s55
v_mfma_f32_16x16x32_bf16 v[36:39],a[120:123],v[56:59],v[36:39]
v_mfma_f32_16x16x32_bf16 v[40:43],a[124:127],v[56:59],v[40:43]
s_waitcnt lgkmcnt(2)
buffer_load_dwordx4 v0,s[36:39],s61 offen offset:1024 sc1 lds
v_mfma_f32_16x16x32_bf16 v[28:31],a[128:131],v[44:47],v[28:31]
ds_read_b128 v[56:59],v2 offset:39936
v_mfma_f32_16x16x32_bf16 v[32:35],a[132:135],v[44:47],v[32:35]
v_mfma_f32_16x16x32_bf16 v[36:39],a[136:139],v[44:47],v[36:39]
v_mfma_f32_16x16x32_bf16 v[40:43],a[140:143],v[44:47],v[40:43]
s_waitcnt lgkmcnt(2)
buffer_load_dwordx4 v0,s[36:39],s61 offen offset:2048 sc1 lds
v_mfma_f32_16x16x32_bf16 v[28:31],a[144:147],v[48:51],v[28:31]
ds_read_b128 v[44:47],v2 offset:40960
v_mfma_f32_16x16x32_bf16 v[32:35],a[148:151],v[48:51],v[32:35]
v_mfma_f32_16x16x32_bf16 v[36:39],a[152:155],v[48:51],v[36:39]
v_mfma_f32_16x16x32_bf16 v[40:43],a[156:159],v[48:51],v[40:43]
s_waitcnt lgkmcnt(2)
buffer_load_dwordx4 v0,s[36:39],s61 offen offset:3072 sc1 lds
v_mfma_f32_16x16x32_bf16 v[28:31],a[160:163],v[52:55],v[28:31]
s_add_u32 m0,s64,0
ds_read_b128 v[48:51],v2 offset:41984
s_lshr_b32 s70,s48,4
v_mfma_f32_16x16x32_bf16 v[32:35],a[164:167],v[52:55],v[32:35]
s_cmp_lt_u32 s67,s59
v_mfma_f32_16x16x32_bf16 v[36:39],a[168:171],v[52:55],v[36:39]
v_mfma_f32_16x16x32_bf16 v[40:43],a[172:175],v[52:55],v[40:43]
s_waitcnt lgkmcnt(2)
buffer_load_dwordx4 v[68:71],v9,s[36:39],s49 offen offset:0 sc1
v_mfma_f32_16x16x32_bf16 v[28:31],a[176:179],v[56:59],v[28:31]
s_mov_b64 s[48:49],s[50:51]
ds_read_b128 v[52:55],v2 offset:43008
s_mov_b64 s[50:51],s[52:53]
v_mfma_f32_16x16x32_bf16 v[32:35],a[180:183],v[56:59],v[32:35]
s_mov_b32 s52,s74
v_mfma_f32_16x16x32_bf16 v[36:39],a[184:187],v[56:59],v[36:39]
v_mfma_f32_16x16x32_bf16 v[40:43],a[188:191],v[56:59],v[40:43]
s_waitcnt lgkmcnt(2)
v_mfma_f32_16x16x32_bf16 v[28:31],a[192:195],v[44:47],v[28:31]
ds_read_b128 v[56:59],v2 offset:44032
v_mfma_f32_16x16x32_bf16 v[32:35],a[196:199],v[44:47],v[32:35]
v_mfma_f32_16x16x32_bf16 v[36:39],a[200:203],v[44:47],v[36:39]
s_waitcnt vmcnt(8) lgkmcnt(2)
v_mfma_f32_16x16x32_bf16 v[40:43],a[204:207],v[44:47],v[40:43]
s_barrier
v_mfma_f32_16x16x32_bf16 v[28:31],a[208:211],v[48:51],v[28:31]
ds_read_b128 v[44:47],v2 offset:45056
v_mfma_f32_16x16x32_bf16 v[32:35],a[212:215],v[48:51],v[32:35]
v_mfma_f32_16x16x32_bf16 v[36:39],a[216:219],v[48:51],v[36:39]
v_mfma_f32_16x16x32_bf16 v[40:43],a[220:223],v[48:51],v[40:43]
s_waitcnt lgkmcnt(2)
v_mfma_f32_16x16x32_bf16 v[28:31],a[224:227],v[52:55],v[28:31]
buffer_load_dwordx4 v1,s[36:39],s61 offen offset:0 sc1 lds
v_mfma_f32_16x16x32_bf16 v[32:35],a[228:231],v[52:55],v[32:35]
ds_read_b128 v[48:51],v2 offset:46080
v_mfma_f32_16x16x32_bf16 v[36:39],a[232:235],v[52:55],v[36:39]
v_mfma_f32_16x16x32_bf16 v[40:43],a[236:239],v[52:55],v[40:43]
s_waitcnt lgkmcnt(2)
v_mfma_f32_16x16x32_bf16 v[28:31],a[240:243],v[56:59],v[28:31]
ds_read_b128 v[52:55],v2 offset:47104
v_mfma_f32_16x16x32_bf16 v[32:35],a[244:247],v[56:59],v[32:35]
v_mfma_f32_16x16x32_bf16 v[36:39],a[248:251],v[56:59],v[36:39]
buffer_load_dwordx4 v1,s[36:39],s61 offen offset:1024 sc1 lds
v_mfma_f32_16x16x32_bf16 v[40:43],a[252:255],v[56:59],v[40:43]
s_waitcnt lgkmcnt(2)
v_mfma_f32_16x16x32_bf16 v[28:31],v[100:103],v[44:47],v[28:31]
ds_read_b128 v[56:59],v2 offset:48128
v_mfma_f32_16x16x32_bf16 v[32:35],v[104:107],v[44:47],v[32:35]
ds_read_b128 v[88:91],v10 offset:0
v_mfma_f32_16x16x32_bf16 v[36:39],v[108:111],v[44:47],v[36:39]
v_mfma_f32_16x16x32_bf16 v[40:43],v[112:115],v[44:47],v[40:43]
s_waitcnt lgkmcnt(3)
v_mfma_f32_16x16x32_bf16 v[28:31],v[116:119],v[48:51],v[28:31]
buffer_load_dwordx4 v1,s[36:39],s61 offen offset:2048 sc1 lds
v_mfma_f32_16x16x32_bf16 v[32:35],v[120:123],v[48:51],v[32:35]
ds_read_b128 v[44:47],v2 offset:49152
ds_read_b128 v[76:79],v10 offset:1024
v_mfma_f32_16x16x32_bf16 v[36:39],v[124:127],v[48:51],v[36:39]
v_mfma_f32_16x16x32_bf16 v[40:43],v[128:131],v[48:51],v[40:43]
s_waitcnt lgkmcnt(4)
v_mfma_f32_16x16x32_bf16 v[28:31],v[132:135],v[52:55],v[28:31]
ds_read_b128 v[48:51],v2 offset:50176
v_mfma_f32_16x16x32_bf16 v[32:35],v[136:139],v[52:55],v[32:35]
ds_read_b128 v[80:83],v10 offset:2048
v_mfma_f32_16x16x32_bf16 v[36:39],v[140:143],v[52:55],v[36:39]
v_mfma_f32_16x16x32_bf16 v[40:43],v[144:147],v[52:55],v[40:43]
s_waitcnt lgkmcnt(4)
v_mfma_f32_16x16x32_bf16 v[28:31],v[148:151],v[56:59],v[28:31]
ds_read_b128 v[52:55],v2 offset:51200
v_mfma_f32_16x16x32_bf16 v[32:35],v[152:155],v[56:59],v[32:35]
ds_read_b128 v[84:87],v10 offset:3072
v_mfma_f32_16x16x32_bf16 v[36:39],v[156:159],v[56:59],v[36:39]
v_mfma_f32_16x16x32_bf16 v[40:43],v[88:91],v[56:59],v[40:43]
s_waitcnt lgkmcnt(4)
v_mfma_f32_16x16x32_bf16 v[28:31],v[160:163],v[44:47],v[28:31]
ds_read_b128 v[56:59],v2 offset:52224
v_mfma_f32_16x16x32_bf16 v[32:35],v[164:167],v[44:47],v[32:35]
ds_read_b128 v[88:91],v10 offset:4096
v_mfma_f32_16x16x32_bf16 v[36:39],v[168:171],v[44:47],v[36:39]
v_mfma_f32_16x16x32_bf16 v[40:43],v[76:79],v[44:47],v[40:43]
s_waitcnt lgkmcnt(4)
v_mfma_f32_16x16x32_bf16 v[28:31],v[172:175],v[48:51],v[28:31]
ds_read_b128 v[44:47],v2 offset:53248
v_mfma_f32_16x16x32_bf16 v[32:35],v[176:179],v[48:51],v[32:35]
ds_read_b128 v[76:79],v10 offset:5120
v_mfma_f32_16x16x32_bf16 v[36:39],v[180:183],v[48:51],v[36:39]
s_waitcnt vmcnt(8) lgkmcnt(4)
v_mfma_f32_16x16x32_bf16 v[40:43],v[80:83],v[48:51],v[40:43]
s_barrier
v_mfma_f32_16x16x32_bf16 v[28:31],v[184:187],v[52:55],v[28:31]
buffer_store_dword v6,v5,s[36:39],s70 offen offset:0 sc1
ds_read_b128 v[48:51],v2 offset:54272)"
R"(
v_mfma_f32_16x16x32_bf16 v[32:35],v[188:191],v[52:55],v[32:35]
ds_read_b128 v[80:83],v10 offset:6144
v_mfma_f32_16x16x32_bf16 v[36:39],v[192:195],v[52:55],v[36:39]
v_mfma_f32_16x16x32_bf16 v[40:43],v[84:87],v[52:55],v[40:43]
s_waitcnt lgkmcnt(4)
v_mfma_f32_16x16x32_bf16 v[28:31],v[196:199],v[56:59],v[28:31]
ds_read_b128 v[52:55],v2 offset:55296
v_mfma_f32_16x16x32_bf16 v[32:35],v[200:203],v[56:59],v[32:35]
ds_read_b128 v[84:87],v10 offset:7168
v_mfma_f32_16x16x32_bf16 v[36:39],v[204:207],v[56:59],v[36:39]
v_mfma_f32_16x16x32_bf16 v[40:43],v[88:91],v[56:59],v[40:43]
s_waitcnt lgkmcnt(4)
v_mfma_f32_16x16x32_bf16 v[28:31],v[208:211],v[44:47],v[28:31]
ds_read_b128 v[56:59],v2 offset:56320
v_mfma_f32_16x16x32_bf16 v[32:35],v[212:215],v[44:47],v[32:35]
ds_read_b128 v[88:91],v10 offset:8192
v_mfma_f32_16x16x32_bf16 v[36:39],v[216:219],v[44:47],v[36:39]
s_waitcnt vmcnt(4) lgkmcnt(4)
v_mfma_f32_16x16x32_bf16 v[40:43],v[76:79],v[44:47],v[40:43]
s_barrier
v_mfma_f32_16x16x32_bf16 v[28:31],v[220:223],v[48:51],v[28:31]
ds_read_b128 v[44:47],v2 offset:0
v_mfma_f32_16x16x32_bf16 v[32:35],v[224:227],v[48:51],v[32:35]
v_mfma_f32_16x16x32_bf16 v[36:39],v[228:231],v[48:51],v[36:39]
v_mfma_f32_16x16x32_bf16 v[40:43],v[80:83],v[48:51],v[40:43]
s_waitcnt lgkmcnt(3)
v_mfma_f32_16x16x32_bf16 v[28:31],v[232:235],v[52:55],v[28:31]
ds_read_b128 v[48:51],v2 offset:1024
v_mfma_f32_16x16x32_bf16 v[32:35],v[236:239],v[52:55],v[32:35]
v_mfma_f32_16x16x32_bf16 v[36:39],v[240:243],v[52:55],v[36:39]
v_mfma_f32_16x16x32_bf16 v[40:43],v[84:87],v[52:55],v[40:43]
s_waitcnt lgkmcnt(2)
v_mfma_f32_16x16x32_bf16 v[28:31],v[244:247],v[56:59],v[28:31]
ds_read_b128 v[52:55],v2 offset:2048
v_mfma_f32_16x16x32_bf16 v[32:35],v[248:251],v[56:59],v[32:35]
v_mfma_f32_16x16x32_bf16 v[36:39],v[252:255],v[56:59],v[36:39]
s_waitcnt lgkmcnt(2)
v_mfma_f32_16x16x32_bf16 v[40:43],v[88:91],v[56:59],v[40:43]
s_cbranch_scc1 Lg4_d0w3_%=
s_branch Lg4_slow0_%=
Lg4_slow0_%=:
ds_read_b32 v11,v8 offset:6144
s_waitcnt lgkmcnt(0)
v_cmp_eq_u32 vcc,s60,v11
s_nop 0
s_cmp_eq_u32 vcc_lo,-1
s_cbranch_scc1 Lg4_gs0_%=
Lg4_slowb0_%=:
s_cmp_eq_u32 s54,1
s_cselect_b32 s68,s49,s48
s_mul_i32 s68,s68,5
s_lshr_b32 s68,s68,6
s_add_u32 m0,s78,0
s_nop 0
buffer_load_dword v7,s[36:39],s68 offen offset:0 sc1 lds
s_mov_b32 s73,s47
v_lshlrev_b32 v60,16,v68
v_and_b32 v61,0xffff0000,v68
v_lshlrev_b32 v62,16,v69
v_and_b32 v63,0xffff0000,v69
v_lshlrev_b32 v64,16,v70
v_and_b32 v65,0xffff0000,v70
v_lshlrev_b32 v66,16,v71
v_and_b32 v67,0xffff0000,v71
v_cvt_pk_bf16_f32 v28,v28,v29
v_cvt_pk_bf16_f32 v29,v30,v31
v_cvt_pk_bf16_f32 v30,v32,v33
v_cvt_pk_bf16_f32 v31,v34,v35
s_add_u32 m0,s78,1024
s_nop 0
buffer_load_dword v7,s[36:39],s68 offen offset:0 sc1 lds
buffer_store_dwordx4 v[28:31],v3,s[36:39],s73 offen offset:0 sc1
v_add_f32 v36,v36,v60
v_add_f32 v37,v37,v61
v_add_f32 v38,v38,v62
v_add_f32 v39,v39,v63
v_add_f32 v40,v40,v64
v_add_f32 v41,v41,v65
v_add_f32 v42,v42,v66
v_add_f32 v43,v43,v67
v_cvt_pk_bf16_f32 v36,v36,v37
v_cvt_pk_bf16_f32 v37,v38,v39
v_cvt_pk_bf16_f32 v38,v40,v41
v_cvt_pk_bf16_f32 v39,v42,v43
buffer_store_dwordx4 v[36:39],v4,s[36:39],s73 offen offset:0 sc1
s_cmp_ge_u32 s48,s59
s_cbranch_scc1 Lg4_done0_%=
s_add_u32 m0,s78,2048
s_nop 0
buffer_load_dword v7,s[36:39],s68 offen offset:0 sc1 lds
s_waitcnt vmcnt(4)
s_barrier
ds_read_b32 v11,v8 offset:0
s_mov_b32 s69,2304
s_waitcnt lgkmcnt(0)
v_cmp_eq_u32 vcc,s60,v11
s_nop 0
s_cmp_eq_u32 vcc_lo,-1
s_cbranch_scc1 Lg4_go0_%=
s_add_u32 m0,s78,3072
s_nop 0
buffer_load_dword v7,s[36:39],s68 offen offset:0 sc1 lds
s_waitcnt vmcnt(4)
s_barrier
ds_read_b32 v11,v8 offset:1024
s_mov_b32 s69,3328
s_waitcnt lgkmcnt(0)
v_cmp_eq_u32 vcc,s60,v11
s_nop 0
s_cmp_eq_u32 vcc_lo,-1
s_cbranch_scc1 Lg4_go0_%=
s_add_u32 m0,s78,0
s_nop 0
buffer_load_dword v7,s[36:39],s68 offen offset:0 sc1 lds
s_waitcnt vmcnt(2)
s_barrier
ds_read_b32 v11,v8 offset:2048
s_lshr_b32 s70,s47,4
buffer_store_dword v6,v5,s[36:39],s70 offen offset:0 sc1
s_mov_b32 s47,s59
s_mov_b32 s69,256
s_waitcnt lgkmcnt(0)
v_cmp_eq_u32 vcc,s60,v11
s_nop 0
s_cmp_eq_u32 vcc_lo,-1
s_cbranch_scc1 Lg4_go0_%=
s_add_u32 m0,s78,1024
s_nop 0
buffer_load_dword v7,s[36:39],s68 offen offset:0 sc1 lds
s_waitcnt vmcnt(3)
s_barrier
ds_read_b32 v11,v8 offset:3072
s_mov_b32 s69,1280
s_waitcnt lgkmcnt(0)
v_cmp_eq_u32 vcc,s60,v11
s_nop 0
s_cmp_eq_u32 vcc_lo,-1
s_cbranch_scc1 Lg4_go0_%=
Lg4_poll0_%=:
s_add_u32 m0,s78,2048
s_nop 0
buffer_load_dword v7,s[36:39],s68 offen offset:0 sc1 lds
s_waitcnt vmcnt(2)
s_barrier
ds_read_b32 v11,v8 offset:0
s_mov_b32 s69,2304
s_waitcnt lgkmcnt(0)
v_cmp_eq_u32 vcc,s60,v11
s_nop 0
s_cmp_eq_u32 vcc_lo,-1
s_cbranch_scc1 Lg4_go0_%=
s_sleep 4
s_add_u32 m0,s78,3072
s_nop 0
buffer_load_dword v7,s[36:39],s68 offen offset:0 sc1 lds
s_waitcnt vmcnt(2)
s_barrier
ds_read_b32 v11,v8 offset:1024
s_mov_b32 s69,3328
s_waitcnt lgkmcnt(0)
v_cmp_eq_u32 vcc,s60,v11
s_nop 0
s_cmp_eq_u32 vcc_lo,-1
s_cbranch_scc1 Lg4_go0_%=
s_sleep 4
s_add_u32 m0,s78,0
s_nop 0
buffer_load_dword v7,s[36:39],s68 offen offset:0 sc1 lds
s_waitcnt vmcnt(2)
s_barrier
ds_read_b32 v11,v8 offset:2048
s_mov_b32 s69,256
s_waitcnt lgkmcnt(0)
v_cmp_eq_u32 vcc,s60,v11
s_nop 0
s_cmp_eq_u32 vcc_lo,-1
s_cbranch_scc1 Lg4_go0_%=
s_sleep 4
s_add_u32 m0,s78,1024
s_nop 0
buffer_load_dword v7,s[36:39],s68 offen offset:0 sc1 lds
s_waitcnt vmcnt(2)
s_barrier
ds_read_b32 v11,v8 offset:3072
s_mov_b32 s69,1280
s_waitcnt lgkmcnt(0)
v_cmp_eq_u32 vcc,s60,v11
s_nop 0
s_cmp_eq_u32 vcc_lo,-1
s_cbranch_scc1 Lg4_go0_%=
s_sleep 4
s_add_u32 s79,s79,1
s_cmp_lt_u32 s79,20000
s_cbranch_scc1 Lg4_poll0_%=
Lg4_go0_%=:
s_mul_i32 s61,s48,7
s_lshr_b32 s61,s61,1
s_add_u32 m0,s63,0
s_nop 0
buffer_load_dwordx4 v0,s[36:39],s61 offen offset:0 sc1 lds)"
R"(
buffer_load_dwordx4 v0,s[36:39],s61 offen offset:1024 sc1 lds
buffer_load_dwordx4 v0,s[36:39],s61 offen offset:2048 sc1 lds
buffer_load_dwordx4 v0,s[36:39],s61 offen offset:3072 sc1 lds
s_add_u32 m0,s64,0
s_nop 0
buffer_load_dwordx4 v1,s[36:39],s61 offen offset:0 sc1 lds
buffer_load_dwordx4 v1,s[36:39],s61 offen offset:1024 sc1 lds
buffer_load_dwordx4 v1,s[36:39],s61 offen offset:2048 sc1 lds
s_mul_i32 s68,s49,5
s_lshr_b32 s68,s68,6
s_add_u32 m0,s78,4096
s_nop 0
buffer_load_dword v7,s[36:39],s68 offen offset:0 sc1 lds
s_waitcnt vmcnt(4)
s_barrier
ds_read_b128 v[44:47],v2 offset:0
ds_read_b128 v[48:51],v2 offset:1024
ds_read_b128 v[52:55],v2 offset:2048
s_lshr_b32 s70,s47,4
buffer_store_dword v6,v5,s[36:39],s70 offen offset:0 sc1
s_mov_b32 s47,s59
s_waitcnt lgkmcnt(2)
s_setpc_b64 s[90:91]
Lg4_gs0_%=:
s_mul_i32 s61,s48,7
s_lshr_b32 s61,s61,1
s_cmp_eq_u32 s54,1
s_cselect_b32 s68,s49,s48
s_mul_i32 s68,s68,5
s_lshr_b32 s68,s68,6
s_mov_b32 s73,s47
s_add_u32 m0,s63,0
s_nop 0
buffer_load_dwordx4 v0,s[36:39],s61 offen offset:0 sc1 lds
v_lshlrev_b32 v60,16,v68
v_and_b32 v61,0xffff0000,v68
v_lshlrev_b32 v62,16,v69
v_and_b32 v63,0xffff0000,v69
buffer_load_dwordx4 v0,s[36:39],s61 offen offset:1024 sc1 lds
v_lshlrev_b32 v64,16,v70
v_and_b32 v65,0xffff0000,v70
v_lshlrev_b32 v66,16,v71
v_and_b32 v67,0xffff0000,v71
buffer_load_dwordx4 v0,s[36:39],s61 offen offset:2048 sc1 lds
v_cvt_pk_bf16_f32 v28,v28,v29
v_cvt_pk_bf16_f32 v29,v30,v31
v_cvt_pk_bf16_f32 v30,v32,v33
v_cvt_pk_bf16_f32 v31,v34,v35
buffer_load_dwordx4 v0,s[36:39],s61 offen offset:3072 sc1 lds
buffer_store_dwordx4 v[28:31],v3,s[36:39],s73 offen offset:0 sc1
v_add_f32 v36,v36,v60
v_add_f32 v37,v37,v61
v_add_f32 v38,v38,v62
s_add_u32 m0,s64,0
s_nop 0
buffer_load_dwordx4 v1,s[36:39],s61 offen offset:0 sc1 lds
v_add_f32 v39,v39,v63
v_add_f32 v40,v40,v64
v_add_f32 v41,v41,v65
v_add_f32 v42,v42,v66
buffer_load_dwordx4 v1,s[36:39],s61 offen offset:1024 sc1 lds
v_add_f32 v43,v43,v67
v_cvt_pk_bf16_f32 v36,v36,v37
v_cvt_pk_bf16_f32 v37,v38,v39
v_cvt_pk_bf16_f32 v38,v40,v41
buffer_load_dwordx4 v1,s[36:39],s61 offen offset:2048 sc1 lds
v_cvt_pk_bf16_f32 v39,v42,v43
buffer_store_dwordx4 v[36:39],v4,s[36:39],s73 offen offset:0 sc1
s_mul_i32 s68,s49,5
s_lshr_b32 s68,s68,6
s_add_u32 m0,s78,4096
s_nop 0
buffer_load_dword v7,s[36:39],s68 offen offset:0 sc1 lds
s_waitcnt vmcnt(6)
s_barrier
ds_read_b128 v[44:47],v2 offset:0
ds_read_b128 v[48:51],v2 offset:1024
ds_read_b128 v[52:55],v2 offset:2048
s_mov_b32 s65,s59
s_lshr_b32 s70,s59,4
buffer_store_dword v6,v5,s[36:39],s70 offen offset:0 sc1
s_waitcnt lgkmcnt(2)
s_setpc_b64 s[90:91]
Lg4_done0_%=:
s_waitcnt vmcnt(0)
s_barrier
s_lshr_b32 s70,s47,4
buffer_store_dword v6,v5,s[36:39],s70 offen offset:0 sc1
s_branch Lg4_done_%=
Lg4_slow1_%=:
ds_read_b32 v11,v8 offset:6144
s_waitcnt lgkmcnt(0)
v_cmp_eq_u32 vcc,s60,v11
s_nop 0
s_cmp_eq_u32 vcc_lo,-1
s_cbranch_scc1 Lg4_gs1_%=
Lg4_slowb1_%=:
s_cmp_eq_u32 s54,1
s_cselect_b32 s68,s50,s49
s_mul_i32 s68,s68,5
s_lshr_b32 s68,s68,6
s_add_u32 m0,s78,0
s_nop 0
buffer_load_dword v7,s[36:39],s68 offen offset:0 sc1 lds
s_mov_b32 s73,s48
v_lshlrev_b32 v60,16,v68
v_and_b32 v61,0xffff0000,v68
v_lshlrev_b32 v62,16,v69
v_and_b32 v63,0xffff0000,v69
v_lshlrev_b32 v64,16,v70
v_and_b32 v65,0xffff0000,v70
v_lshlrev_b32 v66,16,v71
v_and_b32 v67,0xffff0000,v71
v_cvt_pk_bf16_f32 v12,v12,v13
v_cvt_pk_bf16_f32 v13,v14,v15
v_cvt_pk_bf16_f32 v14,v16,v17
v_cvt_pk_bf16_f32 v15,v18,v19
s_add_u32 m0,s78,1024
s_nop 0
buffer_load_dword v7,s[36:39],s68 offen offset:0 sc1 lds
buffer_store_dwordx4 v[12:15],v3,s[36:39],s73 offen offset:0 sc1
v_add_f32 v20,v20,v60
v_add_f32 v21,v21,v61
v_add_f32 v22,v22,v62
v_add_f32 v23,v23,v63
v_add_f32 v24,v24,v64
v_add_f32 v25,v25,v65
v_add_f32 v26,v26,v66
v_add_f32 v27,v27,v67
v_cvt_pk_bf16_f32 v20,v20,v21
v_cvt_pk_bf16_f32 v21,v22,v23
v_cvt_pk_bf16_f32 v22,v24,v25
v_cvt_pk_bf16_f32 v23,v26,v27
buffer_store_dwordx4 v[20:23],v4,s[36:39],s73 offen offset:0 sc1
s_cmp_ge_u32 s49,s59
s_cbranch_scc1 Lg4_done1_%=
s_add_u32 m0,s78,2048
s_nop 0
buffer_load_dword v7,s[36:39],s68 offen offset:0 sc1 lds
s_waitcnt vmcnt(4)
s_barrier
ds_read_b32 v11,v8 offset:0
s_mov_b32 s69,2304
s_waitcnt lgkmcnt(0)
v_cmp_eq_u32 vcc,s60,v11
s_nop 0
s_cmp_eq_u32 vcc_lo,-1
s_cbranch_scc1 Lg4_go1_%=
s_add_u32 m0,s78,3072
s_nop 0
buffer_load_dword v7,s[36:39],s68 offen offset:0 sc1 lds
s_waitcnt vmcnt(4)
s_barrier
ds_read_b32 v11,v8 offset:1024
s_mov_b32 s69,3328
s_waitcnt lgkmcnt(0)
v_cmp_eq_u32 vcc,s60,v11
s_nop 0
s_cmp_eq_u32 vcc_lo,-1
s_cbranch_scc1 Lg4_go1_%=
s_add_u32 m0,s78,0
s_nop 0
buffer_load_dword v7,s[36:39],s68 offen offset:0 sc1 lds
s_waitcnt vmcnt(2)
s_barrier
ds_read_b32 v11,v8 offset:2048
s_lshr_b32 s70,s48,4
buffer_store_dword v6,v5,s[36:39],s70 offen offset:0 sc1
s_mov_b32 s48,s59
s_mov_b32 s69,256
s_waitcnt lgkmcnt(0)
v_cmp_eq_u32 vcc,s60,v11
s_nop 0
s_cmp_eq_u32 vcc_lo,-1
s_cbranch_scc1 Lg4_go1_%=
s_add_u32 m0,s78,1024
s_nop 0
buffer_load_dword v7,s[36:39],s68 offen offset:0 sc1 lds
s_waitcnt vmcnt(3)
s_barrier
ds_read_b32 v11,v8 offset:3072
s_mov_b32 s69,1280
s_waitcnt lgkmcnt(0)
v_cmp_eq_u32 vcc,s60,v11
s_nop 0
s_cmp_eq_u32 vcc_lo,-1
s_cbranch_scc1 Lg4_go1_%=
Lg4_poll1_%=:
s_add_u32 m0,s78,2048
s_nop 0
buffer_load_dword v7,s[36:39],s68 offen offset:0 sc1 lds
s_waitcnt vmcnt(2)
s_barrier
ds_read_b32 v11,v8 offset:0
s_mov_b32 s69,2304
s_waitcnt lgkmcnt(0)
v_cmp_eq_u32 vcc,s60,v11
s_nop 0
s_cmp_eq_u32 vcc_lo,-1
s_cbranch_scc1 Lg4_go1_%=)"
R"(
s_sleep 4
s_add_u32 m0,s78,3072
s_nop 0
buffer_load_dword v7,s[36:39],s68 offen offset:0 sc1 lds
s_waitcnt vmcnt(2)
s_barrier
ds_read_b32 v11,v8 offset:1024
s_mov_b32 s69,3328
s_waitcnt lgkmcnt(0)
v_cmp_eq_u32 vcc,s60,v11
s_nop 0
s_cmp_eq_u32 vcc_lo,-1
s_cbranch_scc1 Lg4_go1_%=
s_sleep 4
s_add_u32 m0,s78,0
s_nop 0
buffer_load_dword v7,s[36:39],s68 offen offset:0 sc1 lds
s_waitcnt vmcnt(2)
s_barrier
ds_read_b32 v11,v8 offset:2048
s_mov_b32 s69,256
s_waitcnt lgkmcnt(0)
v_cmp_eq_u32 vcc,s60,v11
s_nop 0
s_cmp_eq_u32 vcc_lo,-1
s_cbranch_scc1 Lg4_go1_%=
s_sleep 4
s_add_u32 m0,s78,1024
s_nop 0
buffer_load_dword v7,s[36:39],s68 offen offset:0 sc1 lds
s_waitcnt vmcnt(2)
s_barrier
ds_read_b32 v11,v8 offset:3072
s_mov_b32 s69,1280
s_waitcnt lgkmcnt(0)
v_cmp_eq_u32 vcc,s60,v11
s_nop 0
s_cmp_eq_u32 vcc_lo,-1
s_cbranch_scc1 Lg4_go1_%=
s_sleep 4
s_add_u32 s79,s79,1
s_cmp_lt_u32 s79,20000
s_cbranch_scc1 Lg4_poll1_%=
Lg4_go1_%=:
s_mul_i32 s61,s49,7
s_lshr_b32 s61,s61,1
s_add_u32 m0,s63,28672
s_nop 0
buffer_load_dwordx4 v0,s[36:39],s61 offen offset:0 sc1 lds
buffer_load_dwordx4 v0,s[36:39],s61 offen offset:1024 sc1 lds
buffer_load_dwordx4 v0,s[36:39],s61 offen offset:2048 sc1 lds
buffer_load_dwordx4 v0,s[36:39],s61 offen offset:3072 sc1 lds
s_add_u32 m0,s64,28672
s_nop 0
buffer_load_dwordx4 v1,s[36:39],s61 offen offset:0 sc1 lds
buffer_load_dwordx4 v1,s[36:39],s61 offen offset:1024 sc1 lds
buffer_load_dwordx4 v1,s[36:39],s61 offen offset:2048 sc1 lds
s_mul_i32 s68,s50,5
s_lshr_b32 s68,s68,6
s_add_u32 m0,s78,5120
s_nop 0
buffer_load_dword v7,s[36:39],s68 offen offset:0 sc1 lds
s_waitcnt vmcnt(4)
s_barrier
ds_read_b128 v[44:47],v2 offset:28672
ds_read_b128 v[48:51],v2 offset:29696
ds_read_b128 v[52:55],v2 offset:30720
s_lshr_b32 s70,s48,4
buffer_store_dword v6,v5,s[36:39],s70 offen offset:0 sc1
s_mov_b32 s48,s59
s_waitcnt lgkmcnt(2)
s_setpc_b64 s[92:93]
Lg4_gs1_%=:
s_mul_i32 s61,s49,7
s_lshr_b32 s61,s61,1
s_cmp_eq_u32 s54,1
s_cselect_b32 s68,s50,s49
s_mul_i32 s68,s68,5
s_lshr_b32 s68,s68,6
s_mov_b32 s73,s48
s_add_u32 m0,s63,28672
s_nop 0
buffer_load_dwordx4 v0,s[36:39],s61 offen offset:0 sc1 lds
v_lshlrev_b32 v60,16,v68
v_and_b32 v61,0xffff0000,v68
v_lshlrev_b32 v62,16,v69
v_and_b32 v63,0xffff0000,v69
buffer_load_dwordx4 v0,s[36:39],s61 offen offset:1024 sc1 lds
v_lshlrev_b32 v64,16,v70
v_and_b32 v65,0xffff0000,v70
v_lshlrev_b32 v66,16,v71
v_and_b32 v67,0xffff0000,v71
buffer_load_dwordx4 v0,s[36:39],s61 offen offset:2048 sc1 lds
v_cvt_pk_bf16_f32 v12,v12,v13
v_cvt_pk_bf16_f32 v13,v14,v15
v_cvt_pk_bf16_f32 v14,v16,v17
v_cvt_pk_bf16_f32 v15,v18,v19
buffer_load_dwordx4 v0,s[36:39],s61 offen offset:3072 sc1 lds
buffer_store_dwordx4 v[12:15],v3,s[36:39],s73 offen offset:0 sc1
v_add_f32 v20,v20,v60
v_add_f32 v21,v21,v61
v_add_f32 v22,v22,v62
s_add_u32 m0,s64,28672
s_nop 0
buffer_load_dwordx4 v1,s[36:39],s61 offen offset:0 sc1 lds
v_add_f32 v23,v23,v63
v_add_f32 v24,v24,v64
v_add_f32 v25,v25,v65
v_add_f32 v26,v26,v66
buffer_load_dwordx4 v1,s[36:39],s61 offen offset:1024 sc1 lds
v_add_f32 v27,v27,v67
v_cvt_pk_bf16_f32 v20,v20,v21
v_cvt_pk_bf16_f32 v21,v22,v23
v_cvt_pk_bf16_f32 v22,v24,v25
buffer_load_dwordx4 v1,s[36:39],s61 offen offset:2048 sc1 lds
v_cvt_pk_bf16_f32 v23,v26,v27
buffer_store_dwordx4 v[20:23],v4,s[36:39],s73 offen offset:0 sc1
s_mul_i32 s68,s50,5
s_lshr_b32 s68,s68,6
s_add_u32 m0,s78,5120
s_nop 0
buffer_load_dword v7,s[36:39],s68 offen offset:0 sc1 lds
s_waitcnt vmcnt(6)
s_barrier
ds_read_b128 v[44:47],v2 offset:28672
ds_read_b128 v[48:51],v2 offset:29696
ds_read_b128 v[52:55],v2 offset:30720
s_mov_b32 s65,s59
s_lshr_b32 s70,s59,4
buffer_store_dword v6,v5,s[36:39],s70 offen offset:0 sc1
s_waitcnt lgkmcnt(2)
s_setpc_b64 s[92:93]
Lg4_done1_%=:
s_waitcnt vmcnt(0)
s_barrier
s_lshr_b32 s70,s48,4
buffer_store_dword v6,v5,s[36:39],s70 offen offset:0 sc1
s_branch Lg4_done_%=
Lg4_done_%=:
s_waitcnt vmcnt(0) lgkmcnt(0)
s_barrier
s_endpgm
Lcode_g4_e:)"
  :: "s"(ws), "s"(wave), "s"(sStep), "s"(sX1), "s"(sX2), "s"(sU0), "s"(sWw), "s"(sBuf), "s"(sEp), "v"(vSrc), "v"(vSrc2), "v"(vA), "v"(vOut), "v"(vOut2), "v"(vFlag), "v"(vPollOff), "v"(vSnapRd), "v"(vAuxS), "v"(vWd0), "v"(vWd1), "v"(vWr0), "v"(vWr1), "v"(vWr2), "s"(w4) : "v0", "v1", "v2", "v3", "v4", "v5", "v6", "v7", "v8", "v9", "v10", "v11", "v12", "v13", "v14", "v15", "v16", "v17", "v18", "v19", "v20", "v21", "v22", "v23", "v24", "v25", "v26", "v27", "v28", "v29", "v30", "v31", "v32", "v33", "v34", "v35", "v36", "v37", "v38", "v39", "v40", "v41", "v42", "v43", "v44", "v45", "v46", "v47", "v48", "v49", "v50", "v51", "v52", "v53", "v54", "v55", "v56", "v57", "v58", "v59", "v60", "v61", "v62", "v63", "v64", "v65", "v66", "v67", "v68", "v69", "v70", "v71", "v72", "v73", "v74", "v75", "v76", "v77", "v78", "v79", "v80", "v81", "v82", "v83", "v84", "v85", "v86", "v87", "v88", "v89", "v90", "v91", "v92", "v93", "v94", "v95", "v96", "v97", "v98", "v99", "v100", "v101", "v102", "v103", "v104", "v105", "v106", "v107", "v108", "v109", "v110", "v111", "v112", "v113", "v114", "v115", "v116", "v117", "v118", "v119", "v120", "v121", "v122", "v123", "v124", "v125", "v126", "v127", "v128", "v129", "v130", "v131", "v132", "v133", "v134", "v135", "v136", "v137", "v138", "v139", "v140", "v141", "v142", "v143", "v144", "v145", "v146", "v147", "v148", "v149", "v150", "v151", "v152", "v153", "v154", "v155", "v156", "v157", "v158", "v159", "v160", "v161", "v162", "v163", "v164", "v165", "v166", "v167", "v168", "v169", "v170", "v171", "v172", "v173", "v174", "v175", "v176", "v177", "v178", "v179", "v180", "v181", "v182", "v183", "v184", "v185", "v186", "v187", "v188", "v189", "v190", "v191", "v192", "v193", "v194", "v195", "v196", "v197", "v198", "v199", "v200", "v201", "v202", "v203", "v204", "v205", "v206", "v207", "v208", "v209", "v210", "v211", "v212", "v213", "v214", "v215", "v216", "v217", "v218", "v219", "v220", "v221", "v222", "v223", "v224", "v225", "v226", "v227", "v228", "v229", "v230", "v231", "v255", "a0", "a1", "a2", "a3", "a4", "a5", "a6", "a7", "a8", "a9", "a10", "a11", "a12", "a13", "a14", "a15", "a16", "a17", "a18", "a19", "a20", "a21", "a22", "a23", "a24", "a25", "a26", "a27", "a28", "a29", "a30", "a31", "a32", "a33", "a34", "a35", "a36", "a37", "a38", "a39", "a40", "a41", "a42", "a43", "a44", "a45", "a46", "a47", "a48", "a49", "a50", "a51", "a52", "a53", "a54", "a55", "a56", "a57", "a58", "a59", "a60", "a61", "a62", "a63", "a64", "a65", "a66", "a67", "a68", "a69", "a70", "a71", "a72", "a73", "a74", "a75", "a76", "a77", "a78", "a79", "a80", "a81", "a82", "a83", "a84", "a85", "a86", "a87", "a88", "a89", "a90", "a91", "a92", "a93", "a94", "a95", "a96", "a97", "a98", "a99", "a100", "a101", "a102", "a103", "a104", "a105", "a106", "a107", "a108", "a109", "a110", "a111", "a112", "a113", "a114", "a115", "a116", "a117", "a118", "a119", "a120", "a121", "a122", "a123", "a124", "a125", "a126", "a127", "a128", "a129", "a130", "a131", "a132", "a133", "a134", "a135", "a136", "a137", "a138", "a139", "a140", "a141", "a142", "a143", "a144", "a145", "a146", "a147", "a148", "a149", "a150", "a151", "a152", "a153", "a154", "a155", "a156", "a157", "a158", "a159", "a160", "a161", "a162", "a163", "a164", "a165", "a166", "a167", "a168", "a169", "a170", "a171", "a172", "a173", "a174", "a175", "a176", "a177", "a178", "a179", "a180", "a181", "a182", "a183", "a184", "a185", "a186", "a187", "a188", "a189", "a190", "a191", "a192", "a193", "a194", "a195", "a196", "a197", "a198", "a199", "a200", "a201", "a202", "a203", "a204", "a205", "a206", "a207", "a208", "a209", "a210", "a211", "a212", "a213", "a214", "a215", "a216", "a217", "a218", "a219", "a220", "a221", "a222", "a223", "a224", "a225", "a226", "a227", "a228", "a229", "a230", "a231", "a232", "a233", "a234", "a235", "a236", "a237", "a238", "a239", "a240", "a241", "a242", "a243", "a244", "a245", "a246", "a247", "a248", "a249", "a250", "a251", "a252", "a253", "a254", "a255", "s36", "s37", "s38", "s39", "s40", "s41", "s42", "s43", "s44", "s45", "s46", "s47", "s48", "s49", "s50", "s51", "s52", "s53", "s54", "s55", "s56", "s57", "s58", "s59", "s60", "s61", "s62", "s63", "s64", "s65", "s66", "s67", "s68", "s69", "s70", "s71", "s72", "s73", "s74", "s75", "s76", "s77", "s78", "s79", "s80", "s81", "s82", "s83", "s84", "s85", "s86", "s87", "s88", "s89", "s90", "s91", "s92", "s93", "s94", "s95", "s96", "s97", "s98", "s99", "vcc", "scc", "memory");
    __builtin_unreachable();
  } else {
    KARG(ws, 10) KARG(out_, 11)
    u16* out = (u16*)out_;
    LANE_ID(lane2);
    constexpr int NRF = N3 ? -16 * NX : N1 ? 2 : NRW;
    const int wk = kind == 6 ? slice : NRF + (kind - 1) * 32 + rep * 8 + slice;
    constexpr int T1_ = 256 - FR * FINE, T2_ = T1_ - 2 * FR2 * FINE;
    const int wv = wk * 4 + wave, fine = wv < FINE * 16 ? 0 : 4096, c4 = (TE + wk) * 16 + wave * 4, c2 = T2_ * 16 + 2 * wv + fine, c1 = T1_ * 16 + wv + fine;
    unsigned f = ep + 1;
    sums_last<4>(ws, out, wv * 4 + (wk < 32 + NRF ? 0 : 4096), (32 + NRF) * 16, TE * 16, c4, lane2, ep, f);
    sums_last<4>(ws, out, c4, (64 + NRF) * 16, T2_ * 16, FR2 ? c2 : c1, lane2, ep, f);
    if (FR2) sums_last<2>(ws, out, c2, FINE * 32, T1_ * 16, c1, lane2, ep, f);
    sums_last<1>(ws, out, c1, FINE * 16, 4096, 4095, lane2, ep, f);
    if (b == 0 && lane2 == 0 && wave == 0) __hip_atomic_store((gu32*)(ws + OFF_EP), ep, __ATOMIC_RELAXED, __HIP_MEMORY_SCOPE_AGENT);
    asm volatile("s_endpgm" ::: "memory");
    __builtin_unreachable();
  }
}

extern "C" void kernel_launch(void* const* d_in, const int* in_sizes, int n_in, void* d_out, int out_size,
                              void* d_ws, size_t ws_size, hipStream_t stream) {
  const u16* q = (const u16*)d_in[0];
  const u16* k = (const u16*)d_in[1];
  const u16* v = (const u16*)d_in[2];
  const u16* shift_k = (const u16*)d_in[3];
  const u16* ssm_k = (const u16*)d_in[4];
  const u16* ssm_D = (const u16*)d_in[5];
  const u16* w1 = (const u16*)d_in[7];
  const u16* w2 = (const u16*)d_in[8];
  const u16* w3 = (const u16*)d_in[9];
  const u16* w4 = (const u16*)d_in[10];
  flow_kernel<<<256, 256, 0, stream>>>(q, k, v, shift_k, ssm_k, ssm_D, w1, w2, w3, w4, (char*)d_ws, (u16*)d_out);
}
```

```cpp
#include <hip/hip_runtime.h>
#include <hip/hip_bf16.h>
#include <stdint.h>
#include <type_traits>

typedef short s16x8 __attribute__((ext_vector_type(8)));
typedef short s16x4 __attribute__((ext_vector_type(4)));
typedef float f32x4 __attribute__((ext_vector_type(4)));
typedef unsigned short u16;
typedef unsigned short u16x8 __attribute__((ext_vector_type(8)));
typedef unsigned short u16x4 __attribute__((ext_vector_type(4)));
typedef float f32x2 __attribute__((ext_vector_type(2)));
typedef __bf16 bf16x2 __attribute__((ext_vector_type(2)));
typedef unsigned u32x4v __attribute__((ext_vector_type(4)));
typedef __attribute__((address_space(1))) unsigned gu32;

constexpr int S_ = 2048, D_ = 1024, F_ = 3584;

constexpr int N1 = 195, N3 = 0, NX = 1, PL1 = 264, KE = 1, RT1 = 0, LB = 66, FL_BYTES = 2141312, WS_USED = 122727552;
constexpr int OFF_KVR = 4194304, OFF_YF = 8388608, OFF_XP = 12582912, OFF_HS = 22085632, OFF_X1 = 31457280, OFF_G = 40894464, OFF_P0 = 71303168, OFF_DBG = 80740352, OFF_P1 = 89128960, OFF_QS = 98566144, OFF_P2 = 99614720, OFF_P3 = 109051904, OFF_S1 = 118489088, OFF_ST = 119013376, OFF_FL = 120586240, OFF_FXP = 120587264, OFF_FH = 120859648, OFF_FX1 = 121132032, OFF_FP = 121405440, OFF_FG = 121950720, OFF_FKV = 122629120, OFF_FY = 122661888, OFF_EP = 122727424;


__device__ __forceinline__ float bf2f(u16 v) { return __uint_as_float(((unsigned)v) << 16); }
__device__ __forceinline__ u16 f2bf(float f) { return __builtin_bit_cast(u16, (__bf16)f); }
__device__ __forceinline__ float rbf(float f) { return bf2f(f2bf(f)); }
__device__ __forceinline__ void st16(void* p, u16x8 v) {
  asm volatile("global_store_dwordx4 %0, %1, off sc1\n\ts_nop 1" :: "v"(p), "v"(v) : "memory");
}
__device__ __forceinline__ void bst16(u32x4v v, __amdgpu_buffer_rsrc_t rs, unsigned vo, unsigned so) {
  asm volatile("buffer_store_dwordx4 %0, %1, %2, %3 offen sc1\n\ts_nop 1" :: "v"(v), "v"(vo), "s"(rs), "s"(so) : "memory");
}
#define WAIT_VM(n) asm volatile("s_waitcnt vmcnt(%0)" ::"n"(n) : "memory")
#define WAIT_LGKM0() asm volatile("s_waitcnt lgkmcnt(0)" ::: "memory")

__device__ __forceinline__ void touch_lds(const char* p, unsigned a) {
  asm volatile("s_mov_b32 m0, %1\n\ts_nop 0\n\tglobal_load_lds_dword %0, off" :: "v"(p), "s"(a) : "memory");
}
#define YSLEEP 4
#define KSLEEP 2
__device__ __forceinline__ f32x2 up2(unsigned p) { return (f32x2){__uint_as_float(p << 16), __uint_as_float(p & 0xffff0000u)}; }
__device__ __forceinline__ unsigned pk2(f32x2 v) { return __builtin_bit_cast(unsigned, __builtin_convertvector(v, bf16x2)); }
__device__ __forceinline__ f32x2 rbf2(f32x2 v) { return up2(pk2(v)); }
#define BARRIER() asm volatile("s_waitcnt lgkmcnt(0)\n\ts_barrier" ::: "memory")
template <int N, class F> __device__ __forceinline__ void sfor(F&& f) {
  if constexpr (N > 0) { sfor<N - 1>(f); f(std::integral_constant<int, N - 1>{}); }
}
template <int NL>
__device__ __forceinline__ void poll_flags(const char* p, int n, int lane, unsigned ep) {
#pragma unroll 1
  for (int it = 0; it < 20000; ++it) {
    bool ok = true;
#pragma unroll
    for (int i = 0; i < NL; ++i) {
      const int f = lane + 64 * i;
      ok &= __hip_atomic_load((gu32*)(p + (f < n ? f : 0) * 128), __ATOMIC_RELAXED, __HIP_MEMORY_SCOPE_AGENT) == ep;
    }
    if (__all(ok)) break;
    __builtin_amdgcn_s_sleep(YSLEEP);
  }
  asm volatile("" ::: "memory");
}

constexpr int CV_HP = (2048 + 32) * 2;
constexpr int CV_UR = (2048 + 256) * 2;
constexpr int CV_CH = 2 * CV_HP + CV_UR;
#define T0 0
#define T0R 4
#define T1 0
#define LATEQ 4
constexpr int CV_STG = 8 * CV_CH;

struct KvRegs { u32x4v k0, k1, k2, k3, k4, k5, k6, v0, v1, v2, v3; };
struct SkRegs { u32x4v s0, s1, s2, s3; };
#define LD16(d, p, pol) asm volatile("global_load_dwordx4 %0, %1, off " pol : "=v"(d) : "v"(p) : "memory")
#define LDS16(d, vo, sp, o, pol) asm volatile("global_load_dwordx4 %0, %1, %2 offset:" #o " " pol : "=v"(d) : "v"(vo), "s"(sp) : "memory")
__device__ __forceinline__ void kv_load(KvRegs& R, const u16* __restrict__ kin, const u16* __restrict__ vin, int b, int tid) {
  const unsigned tr = (b >> 2) * 32 + 4 * (tid >> 5), oa = tr * 2048 + (b & 3) * 512 + 16 * (tid & 31) + 4096;
  LDS16(R.k3, oa, kin, -4096, "nt"); LDS16(R.k4, oa, kin, -2048, "nt"); LDS16(R.k5, oa, kin, 0, "nt"); LDS16(R.k6, oa, kin, 2048, "nt");
  LDS16(R.v0, oa, vin, -4096, "nt"); LDS16(R.v1, oa, vin, -2048, "nt"); LDS16(R.v2, oa, vin, 0, "nt"); LDS16(R.v3, oa, vin, 2048, "nt");
  const unsigned ob = tr ? oa - 6144 : oa;
  LDS16(R.k0, ob, kin, -4096, "nt"); LDS16(R.k1, ob, kin, -2048, "nt"); LDS16(R.k2, ob, kin, 0, "nt");
}
__device__ __forceinline__ void sk_load(SkRegs& R, const u16* __restrict__ shift_k, int b, int tid) {
  const unsigned o = (b & 3) * 2048 + 64 * (tid & 31);
  LDS16(R.s0, o, shift_k, 0, ""); LDS16(R.s1, o, shift_k, 16, ""); LDS16(R.s2, o, shift_k, 32, ""); LDS16(R.s3, o, shift_k, 48, "");
}
template <int N>
__device__ __forceinline__ void kv_wait(KvRegs& R, SkRegs& Q) {
  asm volatile("s_waitcnt vmcnt(%15)" : "+v"(R.k0), "+v"(R.k1), "+v"(R.k2), "+v"(R.k3), "+v"(R.k4), "+v"(R.k5), "+v"(R.k6), "+v"(R.v0), "+v"(R.v1), "+v"(R.v2),
               "+v"(R.v3), "+v"(Q.s0), "+v"(Q.s1), "+v"(Q.s2), "+v"(Q.s3) : "n"(N) : "memory");
}
__device__ __forceinline__ void kv_store(KvRegs& R, const SkRegs& Q, char* __restrict__ ws, char* T, int b, int tid) {
  const int t0 = (b >> 2) * 32, c0 = (b & 3) * 256, ch8 = tid & 31, rg = tid >> 5;
  if (t0 + 4 * rg < 3) R.k0 = R.k1 = R.k2 = (u32x4v){0u, 0u, 0u, 0u};
  typedef unsigned u32x2 __attribute__((ext_vector_type(2)));
  const u32x4v kr[7] = {R.k0, R.k1, R.k2, R.k3, R.k4, R.k5, R.k6}, vr[4] = {R.v0, R.v1, R.v2, R.v3}, sk[4] = {Q.s0, Q.s1, Q.s2, Q.s3};
#pragma unroll
  for (int j2 = 0; j2 < 4; ++j2) {
    const f32x2 sa = up2(sk[j2][0]), sb = up2(sk[j2][1]), sc = up2(sk[j2][2]), sd = up2(sk[j2][3]);
    const f32x2 s0 = {sa[0], sc[0]}, s1 = {sa[1], sc[1]}, s2 = {sb[0], sd[0]}, s3 = {sb[1], sd[1]};
    f32x2 kf[7], o[4];
#pragma unroll
    for (int r = 0; r < 7; ++r) kf[r] = up2(kr[r][j2]);
#pragma unroll
    for (int r = 0; r < 4; ++r)
      o[r] = __builtin_elementwise_fma(kf[r + 3], s0, __builtin_elementwise_fma(kf[r + 2], s1, __builtin_elementwise_fma(kf[r + 1], s2, kf[r] * s3))) * up2(vr[r][j2]);
#pragma unroll
    for (int e = 0; e < 2; ++e)
      *(u32x2*)(T + ch8 * 520 + (2 * j2 + e) * 64 + (28 - 4 * rg) * 2) = (u32x2){pk2((f32x2){o[3][e], o[2][e]}), pk2((f32x2){o[1][e], o[0][e]})};
  }
  BARRIER();
  u16* kvr = (u16*)(ws + OFF_KVR);
#pragma unroll
  for (int i = 0; i < 4; ++i) {
    const int ch = (tid >> 2) + 64 * i, cb = tid & 3;
    const char* p = T + ch * 64 + (ch >> 3) * 8 + cb * 16;
    const u16x4 lo = *(const u16x4*)p, hi = *(const u16x4*)(p + 8);
    st16(kvr + (size_t)(c0 + ch) * S_ + (2016 - t0) + cb * 8, __builtin_shufflevector(lo, hi, 0, 1, 2, 3, 4, 5, 6, 7));
  }
}

typedef unsigned u32x2v __attribute__((ext_vector_type(2)));
__device__ __forceinline__ void h_load(u32x4v (&hv)[8], unsigned& hx, unsigned& dD, const u16* __restrict__ ssm_k, const u16* __restrict__ ssm_D, int c0, int tid) {
  const __amdgpu_buffer_rsrc_t rs = __builtin_amdgcn_make_buffer_rsrc((void*)ssm_k, 0, c0 < 0 ? 0 : D_ * S_ * 2, 0x00020000);
  const unsigned cb = c0 < 0 ? 0 : c0, lane = tid & 63, wave = tid >> 6;
#pragma unroll
  for (int i = 0; i < 8; ++i) asm volatile("buffer_load_dwordx4 %0, %1, %2, %3 offen nt" : "=v"(hv[i]) : "v"(tid * 16), "s"(rs), "s"((cb + i) * 4096) : "memory");
  asm volatile("buffer_load_dword %0, %1, %2, %3 offen" : "=v"(hx) : "v"((lane & 7) * 4096 + ((wave + 1) & 3) * 1024), "s"(rs), "s"(cb * 4096) : "memory");
  asm volatile("global_load_dword %0, %1, %2" : "=v"(dD) : "v"(cb * 2 + wave * 4), "s"(ssm_D) : "memory");
}
template <int N>
__device__ __forceinline__ void h_wait(u32x4v (&hv)[8], unsigned& hx, unsigned& dD) {
  asm volatile("s_waitcnt vmcnt(%10)" : "+v"(hv[0]), "+v"(hv[1]), "+v"(hv[2]), "+v"(hv[3]), "+v"(hv[4]), "+v"(hv[5]), "+v"(hv[6]), "+v"(hv[7]), "+v"(hx), "+v"(dD) : "n"(N) : "memory");
}
__device__ __forceinline__ void h_spread(const u32x4v (&hv)[8], unsigned hx, char* lds, int tid) {
  if (tid < 32 && (tid & 3) != 3) *(u32x4v*)(lds + (tid >> 2) * CV_CH + ((tid >> 1) & 1) * CV_HP + (tid & 1) * 16) = (u32x4v){0u, 0u, 0u, 0u};
  *(u32x4v*)(lds + (tid >> 5) * CV_CH + 2 * CV_HP + 4096 + (tid & 31) * 16) = (u32x4v){0u, 0u, 0u, 0u};
#pragma unroll
  for (int i = 0; i < 8; ++i) {
    char* base = lds + i * CV_CH + 32 + tid * 16;
    unsigned hn = __builtin_amdgcn_update_dpp(0, (int)hv[i][0], 0x130, 0xf, 0xf, true);
    const unsigned last = __builtin_amdgcn_readlane(hx, i);
    if ((tid & 63) == 63) hn = last;
#define AB(a, b) __builtin_amdgcn_alignbyte(a, b, 2)
    if (tid == 0) *(u32x4v*)(lds + i * CV_CH + CV_HP + 16) = (u32x4v){0u, 0u, 0u, hv[i][0] << 16};
    *(u32x4v*)(base) = hv[i];
    *(u32x4v*)(base + CV_HP) = (u32x4v){AB(hv[i][1], hv[i][0]), AB(hv[i][2], hv[i][1]), AB(hv[i][3], hv[i][2]), AB(hn, hv[i][3])};
#undef AB
  }
}

__device__ __forceinline__ void pads(char* __restrict__ ws, int w, int lane, unsigned ep) {
  if (w < 132) {
    const int k = w < 128 ? w >> 5 : w - 128, u = k == 0 ? -1 : k == 1 ? 128 : k == 2 ? 131 : 260;
    st16(ws + (w < 128 ? OFF_HS + u * 32768 + (w & 31) * 1024 : OFF_FH + u * 1024) + lane * 16, __builtin_bit_cast(u16x8, (u32x4v){w >= 128 && !(lane & 7) ? ep : 0u, 0u, 0u, 0u}));
  }
}
__device__ __forceinline__ void prewrite(char* __restrict__ ws, unsigned off, unsigned str, int rep, int tid) {
#pragma unroll
  for (int i = 0; i < 2; ++i) st16(ws + off + (rep + 4 * (tid >> 3) + 132 * i) * str + (tid & 7) * 16, __builtin_bit_cast(u16x8, (u32x4v){0u, 0u, 0u, 0u}));
}

__device__ __forceinline__ void conv_part(char* __restrict__ ws, unsigned dD, char* lds, int g, int tid, int lane, int wave, unsigned ep, const char* wp, unsigned wstep) {
  const __amdgpu_buffer_rsrc_t rs = __builtin_amdgcn_make_buffer_rsrc(ws, 0, WS_USED, 0x00020000);
  const u16* kvr = (const u16*)(ws + OFF_KVR) + (size_t)(8 * g + 2 * wave) * S_;
  auto wait1 = [&](unsigned off) {
#pragma unroll 1
    for (int it = 0; it < 20000; ++it) {
      if (__all(__hip_atomic_load((gu32*)(ws + off), __ATOMIC_RELAXED, __HIP_MEMORY_SCOPE_AGENT) == ep)) break;
      __builtin_amdgcn_s_sleep(KSLEEP);
    }
    asm volatile("" ::: "memory");
  };
  const unsigned lb = (unsigned)(size_t)lds;
  auto u_fetch = [&](int part) {
#pragma unroll
    for (int j = 0; j < 2; ++j)
      asm volatile("s_mov_b32 m0, %1\n\ts_nop 0\n\tglobal_load_lds_dwordx4 %0, off sc1" :: "v"(kvr + j * S_ + part * 512 + lane * 8),
                   "s"(lb + (2 * wave + j) * CV_CH + 2 * CV_HP + part * 1024) : "memory");
  };
  const unsigned xo = (tid >> 4) * 32768 + g * 256 + (tid & 15) * 16;
  u32x4v qv[6];
  u32x2v qs[6];
  auto q_fetch = [&](int s0, int ns) {
#pragma unroll
    for (int i = 0; i < 6; ++i)
      if (i < ns) {
        asm volatile("buffer_load_dwordx4 %0, %1, %2, %3 offen sc1" : "=v"(qv[i]) : "v"(xo), "s"(rs), "s"(OFF_XP + (s0 + i) * 524288) : "memory");
        asm volatile("buffer_load_dwordx2 %0, %1, %2, %3 offen sc1" : "=v"(qs[i]) : "v"(tid * 8), "s"(rs), "s"(OFF_QS + (s0 + i) * 2048) : "memory");
      }
  };
  const int cg = g >> 5;
  const int l15 = lane & 15, lg = lane >> 4, cp = (l15 + 1) & 1;
  const unsigned ao = cp * CV_HP + 2 * (8 * lg + (l15 + 1 - cp)), bo = 2 * CV_HP + 2 * (2032 - 16 * l15 + 8 * lg) - 2 * 256 * 7;
  const unsigned own = lb + 2 * wave * CV_CH;
  char* stg = lds + CV_STG;
  s16x8 ad[2];
#pragma unroll
  for (int j = 0; j < 2; ++j) {
    const int e = 15 - l15 - 8 * lg;
    const unsigned d = (j ? dD >> 16 : dD & 0xffffu) << ((e & 1) * 16);
    ad[j] = __builtin_bit_cast(s16x8, (u32x4v){e >> 1 == 0 ? d : 0u, e >> 1 == 1 ? d : 0u, e >> 1 == 2 ? d : 0u, e >> 1 == 3 ? d : 0u});
  }
  f32x4 acc[2][6];
  unsigned f0, f1;
  bool mid;
  auto finish = [&](auto s0_c, auto ns_c) {
    constexpr int S0 = decltype(s0_c)::value, NS = decltype(ns_c)::value, A0 = S0 == 2 ? 0 : 4, Q0 = S0 == 6 ? 4 : 0;
#pragma unroll
    for (int s = 0; s < NS; ++s)
#pragma unroll
      for (int r = 0; r < 4; ++r) *(unsigned*)(stg + (Q0 + s) * 4352 + l15 * 272 + (4 * lg + r) * 16 + wave * 4) = pk2((f32x2){acc[0][A0 + s][r], acc[1][A0 + s][r]});
    if (S0 == 0) {
      asm volatile("s_waitcnt vmcnt(0)" : "+v"(qv[0]), "+v"(qv[1]), "+v"(qs[0]), "+v"(qs[1]), "+v"(f0) :: "memory");
      mid = __all(f0 == ep);
      if (mid) { u_fetch(2); u_fetch(1); }
    } else if (S0 == 2) asm volatile("" : "+v"(qv[0]), "+v"(qv[1]), "+v"(qv[2]), "+v"(qv[3]), "+v"(qv[4]), "+v"(qv[5]), "+v"(qs[0]), "+v"(qs[1]), "+v"(qs[2]), "+v"(qs[3]), "+v"(qs[4]), "+v"(qs[5]) :: "memory");
    BARRIER();
    u32x4v y4[NS];
#pragma unroll
    for (int s = 0; s < NS; ++s) {
      y4[s] = *(const u32x4v*)(stg + (Q0 + s) * 4352 + (tid >> 4) * 272 + (tid & 15) * 16);
      const f32x2 rd2 = {__uint_as_float(qs[Q0 + s][0]), __uint_as_float(qs[Q0 + s][0])}, nm2 = {__uint_as_float(qs[Q0 + s][1]), __uint_as_float(qs[Q0 + s][1])};
      u32x4v o;
#pragma unroll
      for (int e = 0; e < 4; ++e) {
        const f32x2 x = up2(qv[Q0 + s][e]);
        o[e] = pk2(__builtin_elementwise_fma(__builtin_elementwise_fma(x, rd2, nm2), up2(y4[s][e]), x));
      }
      bst16(o, rs, xo, OFF_XP + (S0 + s) * 524288);
    }
#pragma unroll
    for (int s = 0; s < NS; ++s) bst16(y4[s], rs, (tid >> 3) * 16384 + g * 128 + (tid & 7) * 16, OFF_YF + (S0 + s) * 524288);
  };
  auto touches = [&](int n) {
#pragma unroll
    for (int i = 0; i < 3; ++i)
      if (i < n) { touch_lds(wp, lb + 163584); wp += wstep; }
  };
  auto count = [&](int s0, int ns, auto n_c) {
    WAIT_VM(decltype(n_c)::value);
    BARRIER();
    if (tid < 4 * ns) __hip_atomic_fetch_add((gu32*)(ws + OFF_FXP + (s0 + (tid >> 2)) * 16384 + (tid & 3) * 1024 + (g >> 4) * 128), 1u, __ATOMIC_RELAXED, __HIP_MEMORY_SCOPE_AGENT);
  };
  auto pair = [&](unsigned bb) {
    u32x2v ax[2][2][2][2];
    s16x8 bx[2][2][2];
    auto rdA = [&](int q) {
#pragma unroll
      for (int j = 0; j < 2; ++j)
#pragma unroll
        for (int m = 0; m < 2; ++m) {
          asm volatile("ds_read2_b32 %0, %1 offset0:%2 offset1:%3" : "=v"(ax[q & 1][j][m][0]) : "v"(own + j * CV_CH + ao), "n"(128 * m + 16 * q), "n"(128 * m + 16 * q + 1));
          asm volatile("ds_read2_b32 %0, %1 offset0:%2 offset1:%3" : "=v"(ax[q & 1][j][m][1]) : "v"(own + j * CV_CH + ao), "n"(128 * m + 16 * q + 2), "n"(128 * m + 16 * q + 3));
          asm volatile("ds_read_b128 %0, %1 offset:%2" : "=v"(bx[q & 1][j][m]) : "v"(bb), "n"(j * CV_CH + 512 * (1 - m) + 64 * q));
        }
    };
    rdA(0);
    sfor<8>([&](auto q_c) {
      constexpr int q = decltype(q_c)::value, p = q & 1;
      if (q + 1 < 8) rdA(q + 1);
      asm volatile("s_waitcnt lgkmcnt(%0)" :: "n"(q + 1 < 8 ? 12 : 0) : "memory");
      __builtin_amdgcn_sched_barrier(0);
#pragma unroll
      for (int j = 0; j < 2; ++j) {
        const s16x8 a0f = __builtin_bit_cast(s16x8, __builtin_shufflevector(ax[p][j][0][0], ax[p][j][0][1], 0, 1, 2, 3)), a1f = __builtin_bit_cast(s16x8, __builtin_shufflevector(ax[p][j][1][0], ax[p][j][1][1], 0, 1, 2, 3));
        if (q == 0) {
          acc[j][4] = __builtin_amdgcn_mfma_f32_16x16x32_bf16(ad[j], bx[p][j][0], acc[j][4], 0, 0, 0);
          acc[j][5] = __builtin_amdgcn_mfma_f32_16x16x32_bf16(ad[j], bx[p][j][1], acc[j][5], 0, 0, 0);
        }
        acc[j][4] = __builtin_amdgcn_mfma_f32_16x16x32_bf16(a0f, bx[p][j][0], acc[j][4], 0, 0, 0);
        acc[j][5] = __builtin_amdgcn_mfma_f32_16x16x32_bf16(a0f, bx[p][j][1], acc[j][5], 0, 0, 0);
        acc[j][5] = __builtin_amdgcn_mfma_f32_16x16x32_bf16(a1f, bx[p][j][0], acc[j][5], 0, 0, 0);
      }
      __builtin_amdgcn_sched_barrier(0);
    });
  };
  wait1(OFF_FKV + ((lane & 15) * 4 + cg) * 128);
  u_fetch(3);
  wait1(OFF_FY + (lane & 31) * 128);
  q_fetch(0, 2);
  const unsigned fm = OFF_FKV + ((16 + (lane & 31)) * 4 + cg) * 128;
  asm volatile("global_load_dword %0, %1, %2 sc1" : "=v"(f0) : "v"(fm), "s"(ws) : "memory");
#pragma unroll
  for (int i = 0; i < 4; ++i) acc[i >> 1][4 + (i & 1)] = (f32x4){0.f, 0.f, 0.f, 0.f};
  pair(own + bo + 512 * 6);
  finish(std::integral_constant<int, 0>{}, std::integral_constant<int, 2>{});
  u32x2v a0[8][2], a1[8][2];
  s16x8 b0[6], b1[6];
  unsigned pa[4], pb = own + bo;
#pragma unroll
  for (int i = 0; i < 4; ++i) pa[i] = own + ao + 1024 * i;
  auto rd1 = [&](int rr, int i, u32x2v (&ah)[8][2], s16x8 (&bfr)[6]) {
    const int m = i < 18 ? i / 3 : 6 + (i - 18) / 2, e = i < 18 ? i % 3 : i & 1;
    if (e < 2) asm volatile("ds_read2_b32 %0, %1 offset0:%2 offset1:%3" : "=v"(ah[m][e & 1]) : "v"(pa[m >> 1]), "n"(128 * (m & 1) + 2 * e + 16 * rr), "n"(128 * (m & 1) + 2 * e + 16 * rr + 1));
    else asm volatile("ds_read_b128 %0, %1 offset:%2" : "=v"(bfr[m < 6 ? m : 0]) : "v"(pb), "n"(512 * (7 - m) + 64 * rr));
  };
  auto round = [&](f32x4 (&ac)[6], u32x2v (&ah)[8][2], s16x8 (&bfr)[6], u32x2v (&ahn)[8][2], s16x8 (&bfn)[6], int rr, unsigned step) {
    int k = 0;
#pragma unroll
    for (int s = 2; s <= 7; ++s)
#pragma unroll
      for (int m = 0; m <= s; ++m) {
        if (s - m > 5) continue;
        const s16x8 af = __builtin_bit_cast(s16x8, __builtin_shufflevector(ah[m][0], ah[m][1], 0, 1, 2, 3));
        ac[s - 2] = __builtin_amdgcn_mfma_f32_16x16x32_bf16(af, bfr[s - m > 5 ? 0 : s - m], ac[s - 2], 0, 0, 0);
        __builtin_amdgcn_sched_barrier(0);
        if (m == s) asm volatile("s_waitcnt lgkmcnt(12)" ::: "memory");
#pragma unroll
        for (int i = 0; i < 22; ++i)
          if (i * 4 / 3 == k) {
            rd1(rr, i, ahn, bfn);
            if (rr && (i == 4 || i == 10 || i == 16 || i == 21)) asm volatile("v_add_u32 %0, %1, %0" : "+v"(pa[i / 6]) : "s"(step));
            if (rr && i == 17) asm volatile("v_add_u32 %0, %1, %0" : "+v"(pb) : "s"(step));
          }
        ++k;
        __builtin_amdgcn_sched_barrier(0);
      }
  };
  if (!mid) { wait1(fm); u_fetch(2); u_fetch(1); WAIT_VM(0); }
  WAIT_VM(4);
#pragma unroll
  for (int i = 0; i < 22; ++i) rd1(0, i, a0, b0);
  WAIT_LGKM0();
  auto channel = [&](auto j_c, auto r0_c, auto r1_c) {
    constexpr int j = decltype(j_c)::value, r0 = decltype(r0_c)::value, r1 = decltype(r1_c)::value;
    __builtin_amdgcn_sched_barrier(0);
    if (r0 == 0) {
#pragma unroll
      for (int s = 2; s <= 7; ++s) acc[j][s - 2] = s < 6 ? __builtin_amdgcn_mfma_f32_16x16x32_bf16(ad[j], b0[s], (f32x4){0.f, 0.f, 0.f, 0.f}, 0, 0, 0) : (f32x4){0.f, 0.f, 0.f, 0.f};
    }
#pragma unroll 1
    for (int r = r0; r < r1; r += 2) {
      if (j == 0 && r == 2) {
        asm volatile("s_waitcnt vmcnt(0)\n\ts_barrier" ::: "memory");
        if (tid < 8) __hip_atomic_fetch_add((gu32*)(ws + OFF_FXP + (tid >> 2) * 16384 + (tid & 3) * 1024 + (g >> 4) * 128), 1u, __ATOMIC_RELAXED, __HIP_MEMORY_SCOPE_AGENT);
      }
      if (T0 && j == 0 && r == T0R) touches(T0);
      round(acc[j], a0, b0, a1, b1, 1, r < 6 ? 128 : j ? 0 : CV_CH - 384);
      round(acc[j], a1, b1, a0, b0, 0, 0);
    }
    WAIT_LGKM0();
    __builtin_amdgcn_sched_barrier(0);
  };
  typedef std::integral_constant<int, 0> I0;
  typedef std::integral_constant<int, 1> I1;
  channel(I0{}, I0{}, std::integral_constant<int, 8>{});
  channel(I1{}, I0{}, std::integral_constant<int, LATEQ - 2>{});
  const unsigned fa = OFF_FY + (32 + lane) * 128, fb = lane < 32 ? OFF_FY + (96 + lane) * 128 : OFF_FKV + ((48 + (lane & 15)) * 4 + cg) * 128;
  asm volatile("global_load_dword %0, %1, %2 sc1" : "=v"(f0) : "v"(fa), "s"(ws) : "memory");
  asm volatile("global_load_dword %0, %1, %2 sc1" : "=v"(f1) : "v"(fb), "s"(ws) : "memory");
  channel(I1{}, std::integral_constant<int, LATEQ - 2>{}, std::integral_constant<int, LATEQ>{});
  asm volatile("s_waitcnt vmcnt(0)" : "+v"(f0), "+v"(f1) :: "memory");
  if (!__all(f0 == ep && f1 == ep)) { wait1(fa); wait1(fb); }
  u_fetch(0);
  q_fetch(2, 6);
  touches(T1);
  channel(I1{}, std::integral_constant<int, LATEQ>{}, std::integral_constant<int, 8>{});
  WAIT_VM(T1);
  finish(std::integral_constant<int, 2>{}, std::integral_constant<int, 4>{});
  pair(own + bo);
  count(2, 4, I0{});
  finish(std::integral_constant<int, 6>{}, std::integral_constant<int, 2>{});
  touches(3 - T0 - T1);
  count(6, 2, std::integral_constant<int, 3 - T0 - T1>{});
}
__device__ __forceinline__ float sum2_seg(float s, float q) {
  s += __builtin_bit_cast(float, __builtin_amdgcn_update_dpp(0, __builtin_bit_cast(int, s), 0x128, 0xf, 0xf, true));
  q += __builtin_bit_cast(float, __builtin_amdgcn_update_dpp(0, __builtin_bit_cast(int, q), 0x128, 0xf, 0xf, true));
  asm volatile("s_nop 1\n\tv_permlane16_swap_b32 %0, %1" : "+v"(s), "+v"(q));
  s += q;
  q = s;
  asm volatile("s_nop 1\n\tv_permlane32_swap_b32 %0, %1" : "+v"(s), "+v"(q));
  return s + q;
}
#define LASTSLEEP 8
__device__ __forceinline__ void wait_flags(const char* p, int lane, int n, unsigned ep) {
  gu32* f = (gu32*)(p + (lane < n ? lane : 0) * 128);
#pragma unroll 1
  for (int it = 0; it < 10000; ++it) {
    if (__all(__hip_atomic_load(f, __ATOMIC_RELAXED, __HIP_MEMORY_SCOPE_AGENT) == ep)) break;
    __builtin_amdgcn_s_sleep(LASTSLEEP);
  }
  asm volatile("" ::: "memory");
}
struct QRegs { u32x4v a[4], b[4]; };
__device__ __forceinline__ void q_load(QRegs& R, const u16* __restrict__ q, int f, int lane, int wave) {
  const unsigned o = (f * 16 + (lane & 7)) * 2048 + wave * 512 + (lane >> 3) * 16, o2 = o + 16384;
  LDS16(R.a[0], o, q, 0, "nt"); LDS16(R.b[0], o2, q, 0, "nt"); LDS16(R.a[1], o, q, 128, "nt"); LDS16(R.b[1], o2, q, 128, "nt");
  LDS16(R.a[2], o, q, 256, "nt"); LDS16(R.b[2], o2, q, 256, "nt"); LDS16(R.a[3], o, q, 384, "nt"); LDS16(R.b[3], o2, q, 384, "nt");
}
template <int N>
__device__ __forceinline__ void q_wait(QRegs& R, unsigned& e) {
  asm volatile("s_waitcnt vmcnt(%9)" : "+v"(R.a[0]), "+v"(R.a[1]), "+v"(R.a[2]), "+v"(R.a[3]), "+v"(R.b[0]), "+v"(R.b[1]), "+v"(R.b[2]), "+v"(R.b[3]), "+v"(e) : "n"(N) : "memory");
}
__device__ __forceinline__ void row_stats(const u32x4v (&xq)[4], char* lds, int k, int lane, int wave, float& mean, float& var) {
  f32x2* red = (f32x2*)(lds + 16640);
  const int r8 = lane & 7, seg = lane >> 3;
  f32x2 s = {0.f, 0.f}, sq = {0.f, 0.f};
#pragma unroll
  for (int i = 0; i < 16; ++i) { const f32x2 x = up2(xq[i >> 2][i & 3]); s += x; sq = __builtin_elementwise_fma(x, x, sq); }
  const float a = sum2_seg(s[0] + s[1], sq[0] + sq[1]);
  if ((seg & 5) == 0) ((float*)red)[((k * 4 + wave) * 8 + r8) * 2 + (seg >> 1)] = a;
  BARRIER();
  const f32x2 t = (red[(k * 4) * 8 + r8] + red[(k * 4 + 1) * 8 + r8]) + (red[(k * 4 + 2) * 8 + r8] + red[(k * 4 + 3) * 8 + r8]);
  mean = t[0] * (1.0f / 1024.0f);
  var = fmaxf(t[1] * (1.0f / 1024.0f) - mean * mean, 0.f);
}
__device__ __forceinline__ void q_store(char* __restrict__ ws, int t, int lane, int wave, const u32x4v (&xq)[4], float mean, float var) {
  const __amdgpu_buffer_rsrc_t rs = __builtin_amdgcn_make_buffer_rsrc(ws, 0, WS_USED, 0x00020000);
  const int r8 = lane & 7, seg = lane >> 3;
  const unsigned fo = (t >> 1) * 32768 + (wave * 8 + (seg >> 2)) * 1024 + ((seg & 3) * 16 + (t & 1) * 8 + r8) * 16;
#pragma unroll
  for (int i = 0; i < 4; ++i) bst16(xq[i], rs, fo + i * 2048, OFF_XP);
  const float rd = __builtin_amdgcn_rsqf(var + 1e-5f);
  if (wave == 0 && seg == 0) __builtin_amdgcn_raw_buffer_store_b64((u32x2v){__float_as_uint(rd), __float_as_uint(-mean * rd)}, rs, t * 64 + r8 * 8, OFF_QS, 16);
}
#define FINE (N3 ? 16 - 4 * NX : N1 ? 16 : 20)
template <int NC>
__device__ __forceinline__ void sums_last(char* __restrict__ ws, u16* __restrict__ out, int c, int step, int end, int nx, int lane, unsigned ep, unsigned& f) {
  const __amdgpu_buffer_rsrc_t rs = __builtin_amdgcn_make_buffer_rsrc(ws, 0, WS_USED, 0x00020000);
  auto flags = [&](int x) { return __hip_atomic_load((gu32*)(ws + OFF_FP + (2 * LB + (x >> 5)) * 2048 + ((lane & 3) * 4 + ((x >> 2) & 3)) * 128), __ATOMIC_RELAXED, __HIP_MEMORY_SCOPE_AGENT); };
  if (c >= end) f = ep + 1;
#pragma unroll 1
  for (; c < end; c += step) {
#pragma unroll 1
    for (int it = 0; it < 20000 && !__all(f == ep); ++it) {
      if (it) __builtin_amdgcn_s_sleep(NC == 4 ? LASTSLEEP : 4);
      f = flags(c);
    }
    asm volatile("" ::: "memory");
    const unsigned fo = PL1 * 16384 + c * 1024 + lane * 16;
    u32x4v p0[NC], p1[NC], p2[NC], p3[NC], o[NC];
#pragma unroll
    for (int i = 0; i < NC; ++i) {
      p0[i] = __builtin_bit_cast(u32x4v, __builtin_amdgcn_raw_buffer_load_b128(rs, fo + i * 1024, OFF_P0, 16));
      p1[i] = __builtin_bit_cast(u32x4v, __builtin_amdgcn_raw_buffer_load_b128(rs, fo + i * 1024, OFF_P1, 16));
      p2[i] = __builtin_bit_cast(u32x4v, __builtin_amdgcn_raw_buffer_load_b128(rs, fo + i * 1024, OFF_P2, 16));
      p3[i] = __builtin_bit_cast(u32x4v, __builtin_amdgcn_raw_buffer_load_b128(rs, fo + i * 1024, OFF_P3, 16));
    }
    f = flags(min(c + step < end ? c + step : nx, 4095));
#pragma unroll
    for (int i = 0; i < NC; ++i)
#pragma unroll
      for (int e = 0; e < 4; ++e) o[i][e] = pk2((up2(p0[i][e]) + up2(p1[i][e])) + (up2(p2[i][e]) + up2(p3[i][e])));
    asm volatile("" : "+v"(f));
#pragma unroll
    for (int i = 0; i < NC; ++i) st16(out + (size_t)((c >> 4) * 8 + (lane & 7)) * D_ + ((c & 15) + i) * 64 + (lane >> 3) * 8, __builtin_bit_cast(u16x8, o[i]));
  }
}

__device__ __forceinline__ int swzB(int r) { return ((((r >> 1) & 1) | (((r >> 3) & 1) << 1)) << 1); }
#define FLW 1
#define MAP3 1
#define MAP12 1
#define FLIP 0
#define KVL 0
#define RE 0
#define ROT 0
#define TE 126
#define FR2 0
#define FR 4
#define NRW 18
#define PFROWS 110
__global__ __launch_bounds__(256, 1) void flow_kernel(const u16* __restrict__ q, const u16* __restrict__ kin, const u16* __restrict__ vin,
                                                      const u16* __restrict__ shift_k, const u16* __restrict__ ssm_k, const u16* __restrict__ ssm_D,
                                                      const u16* __restrict__ w1, const u16* __restrict__ w2,
                                                      const u16* __restrict__ w3, const u16* __restrict__ w4, char* __restrict__ ws,
                                                      u16* __restrict__ out) {
  __shared__ __attribute__((aligned(16))) char lds[163840];
  const int tid = threadIdx.x, lane = tid & 63, wave = __builtin_amdgcn_readfirstlane(tid >> 6);
  const int b = blockIdx.x, xcd = b & 7, idx = b >> 3;
  const unsigned sLds = (unsigned)(size_t)lds, sScr = sLds + 163584;
  asm volatile("s_getpc_b64 s[98:99]\n\ts_mov_b32 m0, %1\n\ts_nop 0\n\tglobal_load_lds_dword %0, s[98:99]" :: "v"(tid * 128), "s"(sScr) : "s98", "s99", "memory");
  asm volatile("" :: "s"(q), "s"(kin), "s"(vin), "s"(shift_k), "s"(ssm_k), "s"(ssm_D), "s"(w1), "s"(w2), "s"(w3), "s"(w4), "s"(ws));
  int kind, rep, slice, kh = 0;
  const int P = xcd >> 1, par = (xcd & 1) ^ FLIP, li = idx - (par ? 20 + RE : 12 - RE);
  const int qq = (par ? 0 : 12 + RE) + idx - 8;
  auto code_touch = [&]() {
    unsigned long long cp;
    unsigned n;
    asm volatile("s_getpc_b64 %0\nLpd%=:\n"
                 "s_mov_b32 s98, Lcode_r_b-Lpd%=\ns_mov_b32 %1, Lcode_r_e-Lcode_r_b\n"
                 "s_cmp_eq_u32 %2, 1\ns_cselect_b32 s98, Lcode_g12_b-Lpd%=, s98\ns_cselect_b32 %1, Lcode_g2_b-Lcode_g12_b, %1\n"
                 "s_cmp_eq_u32 %2, 2\ns_cselect_b32 s98, Lcode_g2_b-Lpd%=, s98\ns_cselect_b32 %1, Lcode_g12_e-Lcode_g2_b, %1\n"
                 "s_cmp_eq_u32 %2, 3\ns_cselect_b32 s98, Lcode_g3_b-Lpd%=, s98\ns_cselect_b32 %1, Lcode_g3_e-Lcode_g3_b, %1\n"
                 "s_cmp_eq_u32 %2, 4\ns_cselect_b32 s98, Lcode_g4_b-Lpd%=, s98\ns_cselect_b32 %1, Lcode_g4_e-Lcode_g4_b, %1\n"
                 "s_cmp_eq_u32 %2, 5\ns_cselect_b32 s98, Lcode_g3o_b-Lpd%=, s98\ns_cselect_b32 %1, Lcode_g3o_e-Lcode_g3o_b, %1\n"
                 "s_ashr_i32 s99, s98, 31\ns_mov_b64 vcc, %0\ns_add_u32 s98, s98, vcc_lo\ns_addc_u32 s99, s99, vcc_hi\ns_mov_b64 %0, s[98:99]"
                 : "=&s"(cp), "=&s"(n) : "s"(kind) : "s98", "s99", "scc", "vcc");
    asm volatile("s_mov_b32 m0, %3\n\ts_nop 0\n\tglobal_load_lds_dword %0, %2\n\tglobal_load_lds_dword %1, %2"
                 :: "v"(min(tid * 128u, n - 4)), "v"(min(32768 + tid * 128u, n - 4)), "s"(cp), "s"(sScr) : "memory");
  };
  unsigned ep = 0;
#define EP_LOAD() asm volatile("global_load_dword %0, %1, %2 sc1" : "=v"(ep) : "v"(OFF_EP), "s"(ws) : "memory")
  auto epoch = [&]() { const unsigned e = __builtin_amdgcn_readfirstlane(ep) + 1u; return e ? e : 1u; };
  auto q_unit = [&](QRegs& QR, int f) {
    float ma, va, mb, vb_;
    row_stats(QR.a, lds + CV_STG, 0, lane, wave, ma, va);
    row_stats(QR.b, lds + CV_STG, 1, lane, wave, mb, vb_);
    q_store(ws, 2 * f, lane, wave, QR.a, ma, va);
    q_store(ws, 2 * f + 1, lane, wave, QR.b, mb, vb_);
  };
  auto publish = [&](unsigned off) {
    BARRIER();
    if (tid == 0) __hip_atomic_store((gu32*)(ws + off), ep, __ATOMIC_RELAXED, __HIP_MEMORY_SCOPE_AGENT);
  };
  if (idx < 8 && par == MAP12) {
    kind = 1; rep = P; slice = idx;
    code_touch();
    prewrite(ws, OFF_FH + slice * 128, 1024, rep, tid);
  } else {
    const int cv = (li + (par ? 20 + RE : 0)) * 4 + P, n = idx < 8 ? 64 + P * 8 + idx : qq * 4 + P;
    QRegs QR;
    if (idx >= 8) {
      KvRegs KR;
      SkRegs SK;
      if (li >= 0) {
        const int tile = 64 + cv;
        u32x4v hv[8];
        unsigned hx, dD;
        h_load(hv, hx, dD, ssm_k, ssm_D, 8 * cv, tid);
        EP_LOAD();
        sk_load(SK, shift_k, tile, tid);
        kv_load(KR, kin, vin, tile, tid);
        {
          int l2 = li, p2 = par;
          asm volatile("" : "+s"(l2), "+s"(p2));
          const int nl = 8 - (P & 1), l = ((15 * P + 1) >> 1) + l2, tl = (l * 205) >> 10, kl = l - 5 * tl;
          kind = l2 < 20 ? 4 : 6; rep = l2 < 20 ? l2 >> 2 : 0; slice = l2 < 20 ? l2 & 3 : ((9 * P) >> 1) + l2 - 20; kh = P;
          if (p2 == 1) {
            kind = l2 < nl ? 3 : 6; rep = l2 < nl ? 3 + ((kl & 1) | (kl >> 2)) : 0;
            slice = l2 < nl ? 3 * tl + (kl >> 1) : ((9 * P) >> 1) + RE + l2 - nl;
          }
        }
        touch_lds((const char*)((par ^ MAP12) ? w2 : w1) + (size_t)(par ? (li < 8 ? li * 256 + tid : 0) : (li < 16 ? li * 128 + (tid & 127) : 0)) * 1024 + 384, sScr);
        code_touch();
        h_wait<18>(hv, hx, dD);
        asm volatile("" : "+v"(ep));
        ep = epoch();
        h_spread(hv, hx, lds, tid);
        kv_wait<2>(KR, SK);
        kv_store(KR, SK, ws, lds + CV_STG, tile, tid);
        WAIT_VM(0);
        publish(OFF_FKV + tile * 128);
        const char* wp = (const char*)w4 + (kind != 4 ? 0 : (size_t)(kh * 896 + li * 15) * 2048 + min(tid, 239) * 128);
        conv_part(ws, dD, lds, cv, tid, lane, wave, ep, wp, kind != 4 ? 0 : 298 * 2048);
      } else {
        kv_load(KR, kin, vin, n, tid);
        sk_load(SK, shift_k, n, tid);
        EP_LOAD();
        {
          int q2 = qq;
          asm volatile("" : "+s"(q2));
          const int e = 15 * P + q2, t = (e * 205) >> 11, k = e - 10 * t, k3 = min((k * 11) >> 5, 2);
          kind = q2 < 15 ? 3 : 5; rep = q2 < 15 ? k - 3 * k3 : P; slice = q2 < 15 ? 3 * t + k3 : 18;
        }
        kv_wait<0>(KR, SK);
        asm volatile("" : "+v"(ep));
        ep = epoch();
        kv_store(KR, SK, ws, lds + CV_STG, n, tid);
        pads(ws, n * 4 + wave, lane, ep);
        if (n < 8)
          st16(ws + OFF_FXP + (lane >> 3) * 16384 + wave * 1024 + (2 * (n & 3) + (n >> 2)) * 128 + (lane & 7) * 16, __builtin_bit_cast(u16x8, (u32x4v){ep - 16u, 0u, 0u, 0u}));
        kv_load(KR, kin, vin, 192 + n, tid);
        WAIT_VM(11);
        publish(OFF_FKV + n * 128);
        q_load(QR, q, 32 + n, lane, wave);
        kv_wait<8>(KR, SK);
        kv_store(KR, SK, ws, lds, 192 + n, tid);
        q_wait<0>(QR, ep);
        publish(OFF_FKV + (192 + n) * 128);
      }
    } else {
      QRegs Q0;
      q_load(Q0, q, n - 64, lane, wave);
      EP_LOAD();
      kind = 2; rep = P; slice = idx;
      q_wait<0>(Q0, ep);
      ep = epoch();
      q_load(QR, q, 32 + n, lane, wave);
      q_unit(Q0, n - 64);
      q_wait<0>(QR, ep);
      publish(OFF_FY + (n - 64) * 128);
    }
    if (li < 0) {
      q_unit(QR, 32 + n);
      {
        const char* wp = (const char*)w2 + (size_t)(n < 64 ? 0 : (n - 64) * 512 + tid) * 128;
        if (!(ROT & 2)) {
          touch_lds(wp, sScr);
          touch_lds(wp + (n < 64 ? 0 : 32768), sScr);
        }
      }
      WAIT_VM((ROT & 2) ? 0 : 2);
      publish(OFF_FY + (32 + n) * 128);
      if (n >= 64) {
        prewrite(ws, OFF_FX1 + slice * 128, 1024, rep, tid);
        prewrite(ws, OFF_FP + slice * 128, 2048, rep, tid);
        prewrite(ws, OFF_FP + (8 + slice) * 128, 2048, rep, tid);
        prewrite(ws, OFF_FG + slice * 128, 2560, rep, tid);
        prewrite(ws, OFF_FG + (8 + slice) * 128, 2560, rep, tid);
        if (slice < 3) prewrite(ws, OFF_FG + (16 + slice) * 128, 2560, rep, tid);
        st16(ws + OFF_FXP + slice * 128 + (132 + rep + 4 * (tid >> 3)) * 1024 + (tid & 7) * 16, __builtin_bit_cast(u16x8, (u32x4v){0u, 0u, 0u, 0u}));
      }
      code_touch();
      if (n < 64) {
#pragma unroll
        for (int i = 0; i < 4; ++i) {
          const unsigned x = rep * 1024 + i * 256 + tid, row = kind == 3 ? (x * 21846) >> 16 : x >> 1;
          if (rep < (kind == 3 ? 3 : 2)) touch_lds((const char*)w3 + (size_t)row * 7168 + slice * 384 + (x - row * (kind == 3 ? 3 : 2)) * 128, sScr);
        }
      }
    }
    BARRIER();
  }
  typedef char* const __attribute__((address_space(4)))* KArgs;
#define KARG(x, i) char* x; { KArgs ka_ = (KArgs)__builtin_amdgcn_kernarg_segment_ptr(); asm volatile("" : "+s"(ka_)); x = ka_[i]; }
  const unsigned sDbg0 = OFF_DBG + (b * 4 + wave) * 8192;
#define LANE_ID(x) int x; asm volatile("v_mbcnt_lo_u32_b32 %0, -1, 0\n\tv_mbcnt_hi_u32_b32 %0, -1, %0" : "=v"(x))
#define OOB 0x80000000u
#define COMMON(NF, FIN, FOUT, SN0, WS, LDW, NREP)                                                                                    \
  LANE_ID(ln);                                                                                                                       \
  const int l15 = ln & 15, lg = ln >> 4, lq = l15 >> 2, lp = ln & 3, rr = 8 * lg + lq;                                               \
  const unsigned vA = ln * 16, vFlag = (ln < FLW && wave == 3) ? (FOUT) + ln * 4 : OOB;                                                        \
  const unsigned vPollOff = wave < 2 && ln < (NF) ? (FIN) + ((NF) == 16 && ln >= 8 ? (ln & 2 ? ln : ln - 16) : ln) * 128 : OOB, vSnapOff = wave == 0 ? vPollOff : OOB;                   \
  const unsigned vSnapRd = (SN0) + (ln >> 5) * (WS) + ((ln & 31) < (NF) ? (ln & 31) : 0) * 4;                                        \
  const unsigned vWd0 = (ln >> 3) * (LDW) + ((ln & 7) ^ swzB(ln >> 3)) * 16, vWd1 = ((ln >> 3) + 8) * (LDW) + ((ln & 7) ^ swzB(8 + (ln >> 3))) * 16; \
  const unsigned vWr0 = (kind == 4 ? wave * 4096 : (wave >> 1) * 8192) + rr * 128 + (((kind == 4 ? kh >> 1 : wave & 1) * 4 + lp) ^ swzB(rr)) * 16, vWr1 = vWr0 + 8;                  \
  const unsigned vWr2 = kind == 4 ? vWr0 ^ 64 : 16384 + rr * 128 + ((2 * wave + (lp >> 1)) ^ swzB(rr)) * 16 + (lp & 1) * 8;                                  \
                                            \
  const int res = kind == 3 ? rep + 3 * KE : rep;                     \
  const int nm = kind == 4 ? N1 : kind == 3 ? N3 : 0, nr = kind == 4 ? 5 + NX : 6;                  \
  const unsigned sStep = (rep >= 5 ? nr : (NREP)) << 15, sU0 = (rep < 3 ? rep : rep >= 5 ? nm + rep + 4 : res) << 15;                 \
  const unsigned sX1 = ((NREP) == 4 ? 128 + rep : 128 + (res + 2) % 5) << 15, sDbg = sDbg0 + (rep >= 5 ? 1024 : 0);                   \
  const unsigned sX2 = ((NREP) == 4 ? 260 + rep : nm ? 260 + (nm + rep + 164) % nr : 260 + (res + 4) % 5) << 15;                      \
  const unsigned sEp = __builtin_amdgcn_readfirstlane(ep), sBuf = kind != 3 ? sEp : (rep < 3 ? 3 : rep < 5 ? 5 : 6) << 15
  constexpr unsigned AUX = 131072;
    if (kind == 6)
    {
      KARG(ws, 10)
      LANE_ID(lr);
      wait_flags(ws + OFF_FXP + 7 * 16384 + wave * 1024, lr, 8, ep);
      const int r8 = lr & 7, seg = lr >> 3;
      const unsigned sEpR = __builtin_amdgcn_readfirstlane(ep), sK = slice, sStep = NRW, sK1 = RT1 + slice, sRq = OFF_FXP + LB * 2048 + wave * 128, sDbg = sDbg0 + (kind == 6 ? 0 : 1024);
      const unsigned vRF = (wave * 8 + (seg >> 2)) * 1024 + ((seg & 3) * 16 + r8) * 16;
      const unsigned vRwr = sLds + (seg == 0 ? r8 * 64 + wave * 4 : 512 + lr * 4), vRrd = sLds + r8 * 64;
      const unsigned vRP = sLds + ((lr >> 4) == ((lr & 15) >> 2) ? r8 * 64 + 16 + wave * 8 + (seg & 1) * 4 : 768 + lr * 4);
      const unsigned vRS = (wave == 0 && seg == 0) ? r8 * 16 : 0x80000000u, vFlag = lr == 0 ? 0u : 0x80000000u, vL4 = ((lr & 3) * 4 + wave) * 128, vRL = wave * 4096 + lr * 16;
      asm volatile(R"(
Lcode_r_b:
s_mov_b64 s[36:37],%0
s_mov_b32 s38,122727552
s_mov_b32 s39,0x00020000
s_mov_b32 s54,%1
s_mov_b32 s50,%2
s_mov_b32 s92,%3
s_mov_b32 s48,%4
v_mov_b32 v0,%5
v_mov_b32 v1,%6
v_mov_b32 v2,%7
v_mov_b32 v3,%8
v_mov_b32 v4,%9
v_mov_b32 v5,%10
v_mov_b32 v6,%11
v_mov_b32 v11,%12
v_lshrrev_b32 v10,1,v4
v_mov_b32 v7,s48
v_mbcnt_lo_u32_b32 v9,-1,0
v_mbcnt_hi_u32_b32 v9,-1,v9
v_lshlrev_b32 v9,4,v9
s_mov_b32 s74,0
s_mov_b32 s96,0
s_mov_b32 s60,536870912
s_mov_b32 s46,0xaaaaaaaa
s_mov_b32 s47,0xaaaaaaaa
s_mov_b32 s56,0xcccccccc
s_mov_b32 s57,0xcccccccc
s_mov_b32 s76,0x3a800000
s_mov_b32 s77,0x3727c5ac
s_mov_b32 s78,0xc0135761
s_mov_b32 s79,0x3d372713
v_mov_b32 v192,0x3f803f80
v_mov_b32 v193,0x3f803f80
v_mov_b32 v194,0x3f803f80
v_mov_b32 v195,0x3f803f80
s_lshr_b32 s63,s54,1
s_and_b32 s64,s54,1
s_lshl_b32 s64,s64,7
s_lshl_b32 s87,s54,14
s_lshl_b32 s58,s63,15
s_add_u32 s58,s58,s64
s_add_u32 s58,s58,16908288
s_lshl_b32 s59,s63,9
s_add_u32 s59,s59,s64
s_add_u32 s59,s59,118556672
s_lshl_b32 s49,s54,9
s_add_u32 s49,s49,s92
s_lshl_b32 s91,s63,11
s_add_u32 s91,s91,121405440
s_lshl_b32 s69,s50,10
s_add_u32 s69,s69,s91
v_mov_b32 v20,0
v_mov_b32 v21,0
v_mov_b32 v22,0
v_mov_b32 v23,0
v_mov_b32 v24,0
v_mov_b32 v25,0
v_mov_b32 v26,0
v_mov_b32 v27,0
v_mov_b32 v16,0x3f80
v_mov_b32 v17,0x3f800000
v_cndmask_b32 v16,v16,v17,s[46:47]
s_mov_b32 s98,0x00300003
s_mov_b32 s99,0x30000300
v_cndmask_b32 v20,v20,v16,s[98:99]
s_lshl_b64 s[98:99],s[98:99],2
v_cndmask_b32 v21,v21,v16,s[98:99]
v_mov_b32 v26,v20
v_mov_b32 v27,v21
v_readfirstlane_b32 s52,v11
s_add_u32 s51,s87,8388608
s_mov_b32 s66,4096
s_nop 3
Lr_yp_%=:
s_add_u32 m0,s66,s52
s_cmp_ge_u32 s51,12582912
s_cbranch_scc1 Lr_ys1_%=
buffer_load_dwordx4 v11,s[36:39],s51 offen offset:0 sc1 lds
buffer_load_dwordx4 v11,s[36:39],s51 offen offset:1024 sc1 lds
buffer_load_dwordx4 v11,s[36:39],s51 offen offset:2048 sc1 lds
buffer_load_dwordx4 v11,s[36:39],s51 offen offset:3072 sc1 lds
s_lshl_b32 s63,s50,14
s_add_u32 s51,s51,s63
Lr_ys1_%=:
s_add_u32 s66,s66,16384
s_cmp_lt_u32 s66,151552
s_cbranch_scc1 Lr_yp_%=
s_mov_b32 s66,4096
v_add_u32 v18,4096,v11
Lr_bp9_%=:
buffer_load_dword v8,v6,s[36:39],s91 offen offset:0 sc1
s_waitcnt vmcnt(0)
v_cmp_eq_u32 vcc,s48,v8
s_nop 0
s_cmp_eq_u64 vcc,exec
s_cselect_b32 s63,1,s96
s_cmp_lg_u32 s63,0
s_cbranch_scc1 Lr_bq9_%=
s_add_u32 s74,s74,1
s_cmp_ge_u32 s74,20000
s_cselect_b32 s96,1,0
s_sleep 4
s_branch Lr_bp9_%=
Lr_bq9_%=:
s_add_u32 s63,s87,71303168
buffer_load_dwordx4 a[0:3],v11,s[36:39],s63 offen offset:0 sc1
buffer_load_dwordx4 a[4:7],v11,s[36:39],s63 offen offset:1024 sc1
buffer_load_dwordx4 a[8:11],v11,s[36:39],s63 offen offset:2048 sc1
buffer_load_dwordx4 a[12:15],v11,s[36:39],s63 offen offset:3072 sc1
s_add_u32 s63,s87,89128960
buffer_load_dwordx4 a[16:19],v11,s[36:39],s63 offen offset:0 sc1
buffer_load_dwordx4 a[20:23],v11,s[36:39],s63 offen offset:1024 sc1
buffer_load_dwordx4 a[24:27],v11,s[36:39],s63 offen offset:2048 sc1
buffer_load_dwordx4 a[28:31],v11,s[36:39],s63 offen offset:3072 sc1
s_add_u32 s63,s87,99614720
buffer_load_dwordx4 a[32:35],v11,s[36:39],s63 offen offset:0 sc1
buffer_load_dwordx4 a[36:39],v11,s[36:39],s63 offen offset:1024 sc1
buffer_load_dwordx4 a[40:43],v11,s[36:39],s63 offen offset:2048 sc1
buffer_load_dwordx4 a[44:47],v11,s[36:39],s63 offen offset:3072 sc1
s_add_u32 s63,s87,109051904
buffer_load_dwordx4 a[48:51],v11,s[36:39],s63 offen offset:0 sc1
buffer_load_dwordx4 a[52:55],v11,s[36:39],s63 offen offset:1024 sc1
buffer_load_dwordx4 a[56:59],v11,s[36:39],s63 offen offset:2048 sc1
buffer_load_dwordx4 a[60:63],v11,s[36:39],s63 offen offset:3072 sc1
buffer_load_dword v8,v6,s[36:39],s69 offen offset:0 sc1
s_waitcnt vmcnt(0)
Lr_it0_%=:
s_mov_b32 s88,s58
s_mov_b32 s89,s59
s_mov_b32 s90,s49
s_add_u32 s54,s54,s50
s_lshr_b32 s63,s54,1
s_and_b32 s64,s54,1
s_lshl_b32 s64,s64,7
s_lshl_b32 s87,s54,14
s_lshl_b32 s58,s63,15
s_add_u32 s58,s58,s64
s_add_u32 s58,s58,16908288
s_lshl_b32 s59,s63,9
s_add_u32 s59,s59,s64
s_add_u32 s59,s59,118556672
s_lshl_b32 s49,s54,9
s_add_u32 s49,s49,s92
s_lshl_b32 s91,s63,11
s_add_u32 s91,s91,121405440
s_lshl_b32 s69,s50,10
s_add_u32 s69,s69,s91
v_cmp_eq_u32 vcc,s48,v8
s_nop 0
s_cmp_eq_u64 vcc,exec
s_cselect_b32 s63,s54,256
s_cmp_lt_u32 s63,256
s_cbranch_scc1 Lr_ta0_%=
ds_read_b128 v[32:35],v18 offset:0
ds_read_b128 v[36:39],v18 offset:1024
ds_read_b128 v[40:43],v18 offset:2048
ds_read_b128 v[44:47],v18 offset:3072
v_mfma_f32_16x16x32_bf16 v[128:131],v[20:23],a[0:3],0
v_mfma_f32_16x16x32_bf16 v[128:131],v[20:23],a[16:19],v[128:131]
v_mfma_f32_16x16x32_bf16 v[128:131],v[20:23],a[32:35],v[128:131]
v_mfma_f32_16x16x32_bf16 v[128:131],v[20:23],a[48:51],v[128:131]
v_mfma_f32_16x16x32_bf16 v[132:135],v[24:27],a[0:3],0
v_mfma_f32_16x16x32_bf16 v[132:135],v[24:27],a[16:19],v[132:135]
v_mfma_f32_16x16x32_bf16 v[132:135],v[24:27],a[32:35],v[132:135]
v_mfma_f32_16x16x32_bf16 v[132:135],v[24:27],a[48:51],v[132:135]
v_mfma_f32_16x16x32_bf16 v[136:139],v[20:23],a[4:7],0
v_mfma_f32_16x16x32_bf16 v[136:139],v[20:23],a[20:23],v[136:139]
v_mfma_f32_16x16x32_bf16 v[136:139],v[20:23],a[36:39],v[136:139]
s_nop 0
v_cvt_pk_bf16_f32 v208,v128,v129
v_cvt_pk_bf16_f32 v209,v130,v131
v_mfma_f32_16x16x32_bf16 v[136:139],v[20:23],a[52:55],v[136:139]
v_mfma_f32_16x16x32_bf16 v[140:143],v[24:27],a[4:7],0
v_mfma_f32_16x16x32_bf16 v[140:143],v[24:27],a[20:23],v[140:143]
v_mfma_f32_16x16x32_bf16 v[140:143],v[24:27],a[36:39],v[140:143]
v_cvt_pk_bf16_f32 v210,v132,v133
v_cvt_pk_bf16_f32 v211,v134,v135
v_mfma_f32_16x16x32_bf16 v[140:143],v[24:27],a[52:55],v[140:143]
v_mfma_f32_16x16x32_bf16 v[144:147],v[20:23],a[8:11],0
v_mfma_f32_16x16x32_bf16 v[144:147],v[20:23],a[24:27],v[144:147]
v_mfma_f32_16x16x32_bf16 v[144:147],v[20:23],a[40:43],v[144:147]
v_cvt_pk_bf16_f32 v212,v136,v137
v_cvt_pk_bf16_f32 v213,v138,v139
v_mfma_f32_16x16x32_bf16 v[144:147],v[20:23],a[56:59],v[144:147]
s_waitcnt vmcnt(0)
buffer_store_dword v7,v5,s[36:39],s60 offen offset:0 sc1
s_mov_b32 s60,536870912
v_mfma_f32_16x16x32_bf16 v[148:151],v[24:27],a[8:11],0
v_mfma_f32_16x16x32_bf16 v[148:151],v[24:27],a[24:27],v[148:151]
v_mfma_f32_16x16x32_bf16 v[148:151],v[24:27],a[40:43],v[148:151]
v_cvt_pk_bf16_f32 v214,v140,v141
v_cvt_pk_bf16_f32 v215,v142,v143
v_mfma_f32_16x16x32_bf16 v[148:151],v[24:27],a[56:59],v[148:151]
v_mfma_f32_16x16x32_bf16 v[28:31],v[192:195],v[208:211],0
v_mfma_f32_16x16x32_bf16 v[204:207],v[208:211],v[208:211],0
v_mfma_f32_16x16x32_bf16 v[152:155],v[20:23],a[12:15],0
v_mfma_f32_16x16x32_bf16 v[152:155],v[20:23],a[28:31],v[152:155]
v_mfma_f32_16x16x32_bf16 v[152:155],v[20:23],a[44:47],v[152:155]
v_cvt_pk_bf16_f32 v216,v144,v145)"
R"(
v_cvt_pk_bf16_f32 v217,v146,v147
v_mfma_f32_16x16x32_bf16 v[152:155],v[20:23],a[60:63],v[152:155]
v_mfma_f32_16x16x32_bf16 v[156:159],v[24:27],a[12:15],0
v_mfma_f32_16x16x32_bf16 v[156:159],v[24:27],a[28:31],v[156:159]
v_mfma_f32_16x16x32_bf16 v[156:159],v[24:27],a[44:47],v[156:159]
v_cvt_pk_bf16_f32 v218,v148,v149
v_cvt_pk_bf16_f32 v219,v150,v151
v_mfma_f32_16x16x32_bf16 v[156:159],v[24:27],a[60:63],v[156:159]
v_mfma_f32_16x16x32_bf16 v[28:31],v[192:195],v[212:215],v[28:31]
v_mfma_f32_16x16x32_bf16 v[204:207],v[212:215],v[212:215],v[204:207]
v_mfma_f32_16x16x32_bf16 v[28:31],v[192:195],v[216:219],v[28:31]
v_mfma_f32_16x16x32_bf16 v[204:207],v[216:219],v[216:219],v[204:207]
s_waitcnt lgkmcnt(0)
v_mfma_f32_16x16x32_bf16 v[160:163],v[20:23],v[32:35],0
v_cvt_pk_bf16_f32 v220,v152,v153
v_cvt_pk_bf16_f32 v221,v154,v155
v_mfma_f32_16x16x32_bf16 v[164:167],v[24:27],v[32:35],0
v_cvt_pk_bf16_f32 v222,v156,v157
v_cvt_pk_bf16_f32 v223,v158,v159
v_mfma_f32_16x16x32_bf16 v[168:171],v[20:23],v[36:39],0
s_nop 0
v_mfma_f32_16x16x32_bf16 v[28:31],v[192:195],v[220:223],v[28:31]
v_mfma_f32_16x16x32_bf16 v[204:207],v[220:223],v[220:223],v[204:207]
v_mfma_f32_16x16x32_bf16 v[172:175],v[24:27],v[36:39],0
v_mfma_f32_16x16x32_bf16 v[176:179],v[20:23],v[40:43],0
v_mfma_f32_16x16x32_bf16 v[180:183],v[24:27],v[40:43],0
s_nop 3
v_add_f32_dpp v224,v28,v28 row_ror:8 row_mask:0xf bank_mask:0xf
v_cndmask_b32 v225,v204,v205,s[46:47]
v_cndmask_b32 v226,v206,v207,s[46:47]
v_cndmask_b32 v225,v225,v226,s[56:57]
v_mfma_f32_16x16x32_bf16 v[184:187],v[20:23],v[44:47],0
ds_write_b32 v2,v224 offset:0
ds_write_b32 v0,v225 offset:0
v_mfma_f32_16x16x32_bf16 v[188:191],v[24:27],v[44:47],0
s_waitcnt lgkmcnt(0)
s_barrier
ds_read_b128 v[196:199],v3 offset:0
ds_read_b128 v[200:203],v3 offset:16
ds_read_b128 v[204:207],v3 offset:32
s_waitcnt lgkmcnt(0)
v_add_f32 v196,v196,v197
v_add_f32 v198,v198,v199
v_add_f32 v200,v200,v204
v_add_f32 v201,v201,v205
v_add_f32 v202,v202,v206
v_add_f32 v203,v203,v207
v_add_f32 v196,v196,v198
v_add_f32 v200,v200,v201
v_add_f32 v202,v202,v203
v_mul_f32 v196,s76,v196
v_add_f32 v197,v200,v202
v_mul_f32 v197,s76,v197
v_fma_f32 v197,-v196,v196,v197
v_max_f32 v197,0,v197
v_add_f32 v197,s77,v197
v_rsq_f32 v228,v197
s_nop 0
v_mul_f32_e64 v230,-v196,v228
v_mov_b32 v229,v228
v_mov_b32 v231,v230
v_pk_fma_f32 v[196:197],v[128:129],v[228:229],v[230:231]
v_pk_fma_f32 v[198:199],v[130:131],v[228:229],v[230:231]
v_pk_fma_f32 v[200:201],v[132:133],v[228:229],v[230:231]
v_pk_fma_f32 v[202:203],v[134:135],v[228:229],v[230:231]
v_pk_fma_f32 v[128:129],v[196:197],v[160:161],v[128:129]
v_pk_fma_f32 v[130:131],v[198:199],v[162:163],v[130:131]
v_pk_fma_f32 v[132:133],v[200:201],v[164:165],v[132:133]
v_pk_fma_f32 v[134:135],v[202:203],v[166:167],v[134:135]
v_pk_fma_f32 v[196:197],v[136:137],v[228:229],v[230:231]
v_pk_fma_f32 v[198:199],v[138:139],v[228:229],v[230:231]
v_pk_fma_f32 v[200:201],v[140:141],v[228:229],v[230:231]
v_pk_fma_f32 v[202:203],v[142:143],v[228:229],v[230:231]
v_pk_fma_f32 v[136:137],v[196:197],v[168:169],v[136:137]
v_pk_fma_f32 v[138:139],v[198:199],v[170:171],v[138:139]
v_pk_fma_f32 v[140:141],v[200:201],v[172:173],v[140:141]
v_pk_fma_f32 v[142:143],v[202:203],v[174:175],v[142:143]
v_pk_fma_f32 v[196:197],v[144:145],v[228:229],v[230:231]
v_pk_fma_f32 v[198:199],v[146:147],v[228:229],v[230:231]
v_pk_fma_f32 v[200:201],v[148:149],v[228:229],v[230:231]
v_pk_fma_f32 v[202:203],v[150:151],v[228:229],v[230:231]
v_pk_fma_f32 v[144:145],v[196:197],v[176:177],v[144:145]
v_pk_fma_f32 v[146:147],v[198:199],v[178:179],v[146:147]
v_pk_fma_f32 v[148:149],v[200:201],v[180:181],v[148:149]
v_pk_fma_f32 v[150:151],v[202:203],v[182:183],v[150:151]
v_pk_fma_f32 v[196:197],v[152:153],v[228:229],v[230:231]
v_pk_fma_f32 v[198:199],v[154:155],v[228:229],v[230:231]
v_pk_fma_f32 v[200:201],v[156:157],v[228:229],v[230:231]
v_pk_fma_f32 v[202:203],v[158:159],v[228:229],v[230:231]
v_pk_fma_f32 v[152:153],v[196:197],v[184:185],v[152:153]
v_pk_fma_f32 v[154:155],v[198:199],v[186:187],v[154:155]
v_pk_fma_f32 v[156:157],v[200:201],v[188:189],v[156:157]
v_pk_fma_f32 v[158:159],v[202:203],v[190:191],v[158:159]
v_cvt_pk_bf16_f32 v208,v128,v129
v_cvt_pk_bf16_f32 v209,v130,v131
v_cvt_pk_bf16_f32 v210,v132,v133
v_cvt_pk_bf16_f32 v211,v134,v135
v_cvt_pk_bf16_f32 v212,v136,v137
v_cvt_pk_bf16_f32 v213,v138,v139
v_cvt_pk_bf16_f32 v214,v140,v141
v_cvt_pk_bf16_f32 v215,v142,v143
v_mfma_f32_16x16x32_bf16 v[28:31],v[192:195],v[208:211],0
v_cvt_pk_bf16_f32 v216,v144,v145
v_cvt_pk_bf16_f32 v217,v146,v147
v_mfma_f32_16x16x32_bf16 v[204:207],v[208:211],v[208:211],0
v_cvt_pk_bf16_f32 v218,v148,v149
v_cvt_pk_bf16_f32 v219,v150,v151
v_mfma_f32_16x16x32_bf16 v[28:31],v[192:195],v[212:215],v[28:31]
v_cvt_pk_bf16_f32 v220,v152,v153
v_cvt_pk_bf16_f32 v221,v154,v155
v_mfma_f32_16x16x32_bf16 v[204:207],v[212:215],v[212:215],v[204:207]
v_cvt_pk_bf16_f32 v222,v156,v157
v_cvt_pk_bf16_f32 v223,v158,v159
v_mfma_f32_16x16x32_bf16 v[28:31],v[192:195],v[216:219],v[28:31]
v_mfma_f32_16x16x32_bf16 v[204:207],v[216:219],v[216:219],v[204:207]
v_mfma_f32_16x16x32_bf16 v[28:31],v[192:195],v[220:223],v[28:31]
v_mfma_f32_16x16x32_bf16 v[204:207],v[220:223],v[220:223],v[204:207]
buffer_load_dword v8,v6,s[36:39],s91 offen offset:0 sc1
buffer_store_dwordx4 v[208:211],v1,s[36:39],s88 offen offset:0 sc1
buffer_store_dwordx4 v[212:215],v1,s[36:39],s88 offen offset:2048 sc1
s_add_u32 s88,s88,4096
s_nop 2
v_add_f32_dpp v224,v28,v28 row_ror:8 row_mask:0xf bank_mask:0xf
v_cndmask_b32 v225,v204,v205,s[46:47]
v_cndmask_b32 v226,v206,v207,s[46:47]
v_cndmask_b32 v225,v225,v226,s[56:57]
s_and_b32 s63,s89,128
ds_write_b32 v2,v224 offset:1024
ds_write_b32 v0,v225 offset:1024
buffer_store_dwordx4 v[216:219],v1,s[36:39],s88 offen offset:0 sc1
buffer_store_dwordx4 v[220:223],v1,s[36:39],s88 offen offset:2048 sc1
s_waitcnt lgkmcnt(0)
s_barrier
ds_read_b128 v[196:199],v3 offset:1024
ds_read_b128 v[200:203],v3 offset:1040
ds_read_b128 v[204:207],v3 offset:1056
s_waitcnt lgkmcnt(0)
v_add_f32 v196,v196,v197
v_add_f32 v198,v198,v199
v_add_f32 v200,v200,v204
v_add_f32 v201,v201,v205
v_add_f32 v202,v202,v206
v_add_f32 v203,v203,v207
v_add_f32 v196,v196,v198
v_add_f32 v200,v200,v201
v_add_f32 v202,v202,v203
v_mul_f32 v196,s76,v196
v_add_f32 v197,v200,v202
v_mul_f32 v197,s76,v197
v_fma_f32 v197,-v196,v196,v197
v_max_f32 v197,0,v197
v_add_f32 v198,s77,v197
v_fma_f32 v199,s77,v198,v197
v_rsq_f32 v205,v198
v_rsq_f32 v198,v199
v_mov_b32 v200,v196
v_mul_f32_e64 v204,-v196,v205
v_mul_f32 v203,v199,v198
v_mul_f32 v201,s78,v198
v_mul_f32 v199,v198,v198
v_mul_f32 v202,s79,v199
v_mul_f32 v202,v202,v201
s_lshr_b32 s63,s63,1
s_sub_u32 s63,s89,s63
buffer_store_dwordx4 v[200:203],v4,s[36:39],s89 offen offset:0 sc1
buffer_store_dwordx2 v[204:205],v10,s[36:39],s63 offen offset:256 sc1
s_add_u32 m0,s66,s52
s_cmp_ge_u32 s51,12582912
s_cbranch_scc1 Lr_ys2_%=
buffer_load_dwordx4 v11,s[36:39],s51 offen offset:0 sc1 lds
buffer_load_dwordx4 v11,s[36:39],s51 offen offset:1024 sc1 lds
buffer_load_dwordx4 v11,s[36:39],s51 offen offset:2048 sc1 lds
buffer_load_dwordx4 v11,s[36:39],s51 offen offset:3072 sc1 lds
s_lshl_b32 s63,s50,14
s_add_u32 s51,s51,s63
Lr_ys2_%=:
s_add_u32 s66,s66,16384
s_cmp_eq_u32 s66,151552
s_cselect_b32 s66,4096,s66
v_add_u32 v18,s66,v11
s_mov_b32 s60,s90
s_waitcnt vmcnt(6)
v_cmp_eq_u32 vcc,s48,v8
s_nop 0
s_cmp_eq_u64 vcc,exec
s_cselect_b32 s63,s54,256
s_cmp_lt_u32 s63,256
s_cbranch_scc1 Lr_tc0_%=
buffer_load_dword v8,v6,s[36:39],s91 offen offset:0 sc1
s_waitcnt vmcnt(0)
buffer_store_dword v7,v5,s[36:39],s60 offen offset:0 sc1
s_mov_b32 s60,536870912
v_cmp_eq_u32 vcc,s48,v8
s_nop 0
s_cmp_eq_u64 vcc,exec
s_cselect_b32 s63,s54,256
s_cmp_lt_u32 s63,256
s_cbranch_scc1 Lr_bq0_%=
s_cmp_ge_u32 s54,256
s_cbranch_scc1 Lr_done_%=)"
R"(
Lr_bp0_%=:
buffer_load_dword v8,v6,s[36:39],s91 offen offset:0 sc1
s_waitcnt vmcnt(0)
v_cmp_eq_u32 vcc,s48,v8
s_nop 0
s_cmp_eq_u64 vcc,exec
s_cselect_b32 s63,1,s96
s_cmp_lg_u32 s63,0
s_cbranch_scc1 Lr_bq0_%=
s_add_u32 s74,s74,1
s_cmp_ge_u32 s74,20000
s_cselect_b32 s96,1,0
s_sleep 4
s_branch Lr_bp0_%=
Lr_bq0_%=:
s_add_u32 s63,s87,71303168
buffer_load_dwordx4 a[64:67],v11,s[36:39],s63 offen offset:0 sc1
buffer_load_dwordx4 a[68:71],v11,s[36:39],s63 offen offset:1024 sc1
buffer_load_dwordx4 a[72:75],v11,s[36:39],s63 offen offset:2048 sc1
buffer_load_dwordx4 a[76:79],v11,s[36:39],s63 offen offset:3072 sc1
s_add_u32 s63,s87,89128960
buffer_load_dwordx4 a[80:83],v11,s[36:39],s63 offen offset:0 sc1
buffer_load_dwordx4 a[84:87],v11,s[36:39],s63 offen offset:1024 sc1
buffer_load_dwordx4 a[88:91],v11,s[36:39],s63 offen offset:2048 sc1
buffer_load_dwordx4 a[92:95],v11,s[36:39],s63 offen offset:3072 sc1
s_add_u32 s63,s87,99614720
buffer_load_dwordx4 a[96:99],v11,s[36:39],s63 offen offset:0 sc1
buffer_load_dwordx4 a[100:103],v11,s[36:39],s63 offen offset:1024 sc1
buffer_load_dwordx4 a[104:107],v11,s[36:39],s63 offen offset:2048 sc1
buffer_load_dwordx4 a[108:111],v11,s[36:39],s63 offen offset:3072 sc1
s_add_u32 s63,s87,109051904
buffer_load_dwordx4 a[112:115],v11,s[36:39],s63 offen offset:0 sc1
buffer_load_dwordx4 a[116:119],v11,s[36:39],s63 offen offset:1024 sc1
buffer_load_dwordx4 a[120:123],v11,s[36:39],s63 offen offset:2048 sc1
buffer_load_dwordx4 a[124:127],v11,s[36:39],s63 offen offset:3072 sc1
buffer_load_dword v8,v6,s[36:39],s69 offen offset:0 sc1
s_waitcnt vmcnt(0)
s_branch Lr_it1_%=
Lr_ta0_%=:
ds_read_b128 v[32:35],v18 offset:0
ds_read_b128 v[36:39],v18 offset:1024
ds_read_b128 v[40:43],v18 offset:2048
ds_read_b128 v[44:47],v18 offset:3072
v_mfma_f32_16x16x32_bf16 v[128:131],v[20:23],a[0:3],0
s_add_u32 s63,s87,71303168
v_mfma_f32_16x16x32_bf16 v[128:131],v[20:23],a[16:19],v[128:131]
buffer_load_dwordx4 a[64:67],v11,s[36:39],s63 offen offset:0 sc1
v_mfma_f32_16x16x32_bf16 v[128:131],v[20:23],a[32:35],v[128:131]
v_mfma_f32_16x16x32_bf16 v[128:131],v[20:23],a[48:51],v[128:131]
buffer_load_dwordx4 a[68:71],v11,s[36:39],s63 offen offset:1024 sc1
v_mfma_f32_16x16x32_bf16 v[132:135],v[24:27],a[0:3],0
v_mfma_f32_16x16x32_bf16 v[132:135],v[24:27],a[16:19],v[132:135]
v_mfma_f32_16x16x32_bf16 v[132:135],v[24:27],a[32:35],v[132:135]
v_mfma_f32_16x16x32_bf16 v[132:135],v[24:27],a[48:51],v[132:135]
buffer_load_dwordx4 a[72:75],v11,s[36:39],s63 offen offset:2048 sc1
v_mfma_f32_16x16x32_bf16 v[136:139],v[20:23],a[4:7],0
v_mfma_f32_16x16x32_bf16 v[136:139],v[20:23],a[20:23],v[136:139]
v_mfma_f32_16x16x32_bf16 v[136:139],v[20:23],a[36:39],v[136:139]
v_cvt_pk_bf16_f32 v208,v128,v129
v_cvt_pk_bf16_f32 v209,v130,v131
v_mfma_f32_16x16x32_bf16 v[136:139],v[20:23],a[52:55],v[136:139]
buffer_load_dwordx4 a[76:79],v11,s[36:39],s63 offen offset:3072 sc1
v_mfma_f32_16x16x32_bf16 v[140:143],v[24:27],a[4:7],0
s_add_u32 s63,s87,89128960
v_mfma_f32_16x16x32_bf16 v[140:143],v[24:27],a[20:23],v[140:143]
v_mfma_f32_16x16x32_bf16 v[140:143],v[24:27],a[36:39],v[140:143]
v_cvt_pk_bf16_f32 v210,v132,v133
v_cvt_pk_bf16_f32 v211,v134,v135
v_mfma_f32_16x16x32_bf16 v[140:143],v[24:27],a[52:55],v[140:143]
buffer_load_dwordx4 a[80:83],v11,s[36:39],s63 offen offset:0 sc1
v_mfma_f32_16x16x32_bf16 v[144:147],v[20:23],a[8:11],0
v_mfma_f32_16x16x32_bf16 v[144:147],v[20:23],a[24:27],v[144:147]
v_mfma_f32_16x16x32_bf16 v[144:147],v[20:23],a[40:43],v[144:147]
v_cvt_pk_bf16_f32 v212,v136,v137
v_cvt_pk_bf16_f32 v213,v138,v139
v_mfma_f32_16x16x32_bf16 v[144:147],v[20:23],a[56:59],v[144:147]
buffer_load_dwordx4 a[84:87],v11,s[36:39],s63 offen offset:1024 sc1
v_mfma_f32_16x16x32_bf16 v[148:151],v[24:27],a[8:11],0
v_mfma_f32_16x16x32_bf16 v[148:151],v[24:27],a[24:27],v[148:151]
v_mfma_f32_16x16x32_bf16 v[148:151],v[24:27],a[40:43],v[148:151]
v_cvt_pk_bf16_f32 v214,v140,v141
v_cvt_pk_bf16_f32 v215,v142,v143
v_mfma_f32_16x16x32_bf16 v[148:151],v[24:27],a[56:59],v[148:151]
s_waitcnt vmcnt(6)
buffer_store_dword v7,v5,s[36:39],s60 offen offset:0 sc1
s_mov_b32 s60,536870912
buffer_load_dwordx4 a[88:91],v11,s[36:39],s63 offen offset:2048 sc1
v_mfma_f32_16x16x32_bf16 v[28:31],v[192:195],v[208:211],0
v_mfma_f32_16x16x32_bf16 v[204:207],v[208:211],v[208:211],0
v_mfma_f32_16x16x32_bf16 v[152:155],v[20:23],a[12:15],0
v_mfma_f32_16x16x32_bf16 v[152:155],v[20:23],a[28:31],v[152:155]
buffer_load_dwordx4 a[92:95],v11,s[36:39],s63 offen offset:3072 sc1
v_mfma_f32_16x16x32_bf16 v[152:155],v[20:23],a[44:47],v[152:155]
v_cvt_pk_bf16_f32 v216,v144,v145
v_cvt_pk_bf16_f32 v217,v146,v147
v_mfma_f32_16x16x32_bf16 v[152:155],v[20:23],a[60:63],v[152:155]
s_add_u32 s63,s87,99614720
v_mfma_f32_16x16x32_bf16 v[156:159],v[24:27],a[12:15],0
v_mfma_f32_16x16x32_bf16 v[156:159],v[24:27],a[28:31],v[156:159]
buffer_load_dwordx4 a[96:99],v11,s[36:39],s63 offen offset:0 sc1
v_mfma_f32_16x16x32_bf16 v[156:159],v[24:27],a[44:47],v[156:159]
v_cvt_pk_bf16_f32 v218,v148,v149
v_cvt_pk_bf16_f32 v219,v150,v151
v_mfma_f32_16x16x32_bf16 v[156:159],v[24:27],a[60:63],v[156:159]
v_mfma_f32_16x16x32_bf16 v[28:31],v[192:195],v[212:215],v[28:31]
v_mfma_f32_16x16x32_bf16 v[204:207],v[212:215],v[212:215],v[204:207]
buffer_load_dwordx4 a[100:103],v11,s[36:39],s63 offen offset:1024 sc1
v_mfma_f32_16x16x32_bf16 v[28:31],v[192:195],v[216:219],v[28:31]
v_mfma_f32_16x16x32_bf16 v[204:207],v[216:219],v[216:219],v[204:207]
s_waitcnt lgkmcnt(0)
v_mfma_f32_16x16x32_bf16 v[160:163],v[20:23],v[32:35],0
v_cvt_pk_bf16_f32 v220,v152,v153
v_cvt_pk_bf16_f32 v221,v154,v155
v_mfma_f32_16x16x32_bf16 v[164:167],v[24:27],v[32:35],0
v_cvt_pk_bf16_f32 v222,v156,v157
v_cvt_pk_bf16_f32 v223,v158,v159
v_mfma_f32_16x16x32_bf16 v[168:171],v[20:23],v[36:39],0
buffer_load_dwordx4 a[104:107],v11,s[36:39],s63 offen offset:2048 sc1
v_mfma_f32_16x16x32_bf16 v[28:31],v[192:195],v[220:223],v[28:31]
buffer_load_dwordx4 a[108:111],v11,s[36:39],s63 offen offset:3072 sc1
v_mfma_f32_16x16x32_bf16 v[204:207],v[220:223],v[220:223],v[204:207]
s_add_u32 s63,s87,109051904
v_mfma_f32_16x16x32_bf16 v[172:175],v[24:27],v[36:39],0
v_mfma_f32_16x16x32_bf16 v[176:179],v[20:23],v[40:43],0
buffer_load_dwordx4 a[112:115],v11,s[36:39],s63 offen offset:0 sc1
v_mfma_f32_16x16x32_bf16 v[180:183],v[24:27],v[40:43],0
s_nop 0
v_add_f32_dpp v224,v28,v28 row_ror:8 row_mask:0xf bank_mask:0xf
s_nop 0
v_cndmask_b32 v225,v204,v205,s[46:47]
v_cndmask_b32 v226,v206,v207,s[46:47]
v_cndmask_b32 v225,v225,v226,s[56:57]
v_mfma_f32_16x16x32_bf16 v[184:187],v[20:23],v[44:47],0
ds_write_b32 v2,v224 offset:0
ds_write_b32 v0,v225 offset:0
v_mfma_f32_16x16x32_bf16 v[188:191],v[24:27],v[44:47],0
s_waitcnt lgkmcnt(0)
buffer_load_dwordx4 a[116:119],v11,s[36:39],s63 offen offset:1024 sc1
s_barrier
buffer_load_dwordx4 a[120:123],v11,s[36:39],s63 offen offset:2048 sc1
ds_read_b128 v[196:199],v3 offset:0
buffer_load_dwordx4 a[124:127],v11,s[36:39],s63 offen offset:3072 sc1
ds_read_b128 v[200:203],v3 offset:16
ds_read_b128 v[204:207],v3 offset:32
s_waitcnt lgkmcnt(0)
buffer_load_dword v8,v6,s[36:39],s69 offen offset:0 sc1
v_add_f32 v196,v196,v197
v_add_f32 v198,v198,v199
v_add_f32 v200,v200,v204
v_add_f32 v201,v201,v205
v_add_f32 v202,v202,v206
v_add_f32 v203,v203,v207
v_add_f32 v196,v196,v198
v_add_f32 v200,v200,v201
v_add_f32 v202,v202,v203
v_mul_f32 v196,s76,v196
v_add_f32 v197,v200,v202
v_mul_f32 v197,s76,v197
v_fma_f32 v197,-v196,v196,v197
v_max_f32 v197,0,v197
v_add_f32 v197,s77,v197
v_rsq_f32 v228,v197
s_nop 0
v_mul_f32_e64 v230,-v196,v228
v_mov_b32 v229,v228
v_mov_b32 v231,v230
v_pk_fma_f32 v[196:197],v[128:129],v[228:229],v[230:231]
v_pk_fma_f32 v[198:199],v[130:131],v[228:229],v[230:231]
v_pk_fma_f32 v[200:201],v[132:133],v[228:229],v[230:231]
v_pk_fma_f32 v[202:203],v[134:135],v[228:229],v[230:231]
v_pk_fma_f32 v[128:129],v[196:197],v[160:161],v[128:129]
v_pk_fma_f32 v[130:131],v[198:199],v[162:163],v[130:131]
v_pk_fma_f32 v[132:133],v[200:201],v[164:165],v[132:133]
v_pk_fma_f32 v[134:135],v[202:203],v[166:167],v[134:135]
v_pk_fma_f32 v[196:197],v[136:137],v[228:229],v[230:231]
v_pk_fma_f32 v[198:199],v[138:139],v[228:229],v[230:231]
v_pk_fma_f32 v[200:201],v[140:141],v[228:229],v[230:231]
v_pk_fma_f32 v[202:203],v[142:143],v[228:229],v[230:231]
v_pk_fma_f32 v[136:137],v[196:197],v[168:169],v[136:137]
v_pk_fma_f32 v[138:139],v[198:199],v[170:171],v[138:139]
v_pk_fma_f32 v[140:141],v[200:201],v[172:173],v[140:141]
v_pk_fma_f32 v[142:143],v[202:203],v[174:175],v[142:143]
v_pk_fma_f32 v[196:197],v[144:145],v[228:229],v[230:231]
v_pk_fma_f32 v[198:199],v[146:147],v[228:229],v[230:231]
v_pk_fma_f32 v[200:201],v[148:149],v[228:229],v[230:231]
v_pk_fma_f32 v[202:203],v[150:151],v[228:229],v[230:231]
v_pk_fma_f32 v[144:145],v[196:197],v[176:177],v[144:145]
v_pk_fma_f32 v[146:147],v[198:199],v[178:179],v[146:147]
v_pk_fma_f32 v[148:149],v[200:201],v[180:181],v[148:149]
v_pk_fma_f32 v[150:151],v[202:203],v[182:183],v[150:151]
v_pk_fma_f32 v[196:197],v[152:153],v[228:229],v[230:231]
v_pk_fma_f32 v[198:199],v[154:155],v[228:229],v[230:231]
v_pk_fma_f32 v[200:201],v[156:157],v[228:229],v[230:231]
v_pk_fma_f32 v[202:203],v[158:159],v[228:229],v[230:231]
v_pk_fma_f32 v[152:153],v[196:197],v[184:185],v[152:153]
v_pk_fma_f32 v[154:155],v[198:199],v[186:187],v[154:155]
v_pk_fma_f32 v[156:157],v[200:201],v[188:189],v[156:157]
v_pk_fma_f32 v[158:159],v[202:203],v[190:191],v[158:159]
v_cvt_pk_bf16_f32 v208,v128,v129
v_cvt_pk_bf16_f32 v209,v130,v131)"
R"(
v_cvt_pk_bf16_f32 v210,v132,v133
v_cvt_pk_bf16_f32 v211,v134,v135
v_cvt_pk_bf16_f32 v212,v136,v137
v_cvt_pk_bf16_f32 v213,v138,v139
v_cvt_pk_bf16_f32 v214,v140,v141
v_cvt_pk_bf16_f32 v215,v142,v143
v_mfma_f32_16x16x32_bf16 v[28:31],v[192:195],v[208:211],0
v_cvt_pk_bf16_f32 v216,v144,v145
v_cvt_pk_bf16_f32 v217,v146,v147
v_mfma_f32_16x16x32_bf16 v[204:207],v[208:211],v[208:211],0
v_cvt_pk_bf16_f32 v218,v148,v149
v_cvt_pk_bf16_f32 v219,v150,v151
v_mfma_f32_16x16x32_bf16 v[28:31],v[192:195],v[212:215],v[28:31]
v_cvt_pk_bf16_f32 v220,v152,v153
v_cvt_pk_bf16_f32 v221,v154,v155
v_mfma_f32_16x16x32_bf16 v[204:207],v[212:215],v[212:215],v[204:207]
v_cvt_pk_bf16_f32 v222,v156,v157
v_cvt_pk_bf16_f32 v223,v158,v159
v_mfma_f32_16x16x32_bf16 v[28:31],v[192:195],v[216:219],v[28:31]
v_mfma_f32_16x16x32_bf16 v[204:207],v[216:219],v[216:219],v[204:207]
v_mfma_f32_16x16x32_bf16 v[28:31],v[192:195],v[220:223],v[28:31]
v_mfma_f32_16x16x32_bf16 v[204:207],v[220:223],v[220:223],v[204:207]
buffer_store_dwordx4 v[208:211],v1,s[36:39],s88 offen offset:0 sc1
buffer_store_dwordx4 v[212:215],v1,s[36:39],s88 offen offset:2048 sc1
s_add_u32 s88,s88,4096
s_nop 3
v_add_f32_dpp v224,v28,v28 row_ror:8 row_mask:0xf bank_mask:0xf
v_cndmask_b32 v225,v204,v205,s[46:47]
v_cndmask_b32 v226,v206,v207,s[46:47]
v_cndmask_b32 v225,v225,v226,s[56:57]
s_and_b32 s63,s89,128
ds_write_b32 v2,v224 offset:1024
ds_write_b32 v0,v225 offset:1024
buffer_store_dwordx4 v[216:219],v1,s[36:39],s88 offen offset:0 sc1
buffer_store_dwordx4 v[220:223],v1,s[36:39],s88 offen offset:2048 sc1
s_waitcnt lgkmcnt(0)
s_barrier
ds_read_b128 v[196:199],v3 offset:1024
ds_read_b128 v[200:203],v3 offset:1040
ds_read_b128 v[204:207],v3 offset:1056
s_waitcnt lgkmcnt(0)
v_add_f32 v196,v196,v197
v_add_f32 v198,v198,v199
v_add_f32 v200,v200,v204
v_add_f32 v201,v201,v205
v_add_f32 v202,v202,v206
v_add_f32 v203,v203,v207
v_add_f32 v196,v196,v198
v_add_f32 v200,v200,v201
v_add_f32 v202,v202,v203
v_mul_f32 v196,s76,v196
v_add_f32 v197,v200,v202
v_mul_f32 v197,s76,v197
v_fma_f32 v197,-v196,v196,v197
v_max_f32 v197,0,v197
v_add_f32 v198,s77,v197
v_fma_f32 v199,s77,v198,v197
v_rsq_f32 v205,v198
v_rsq_f32 v198,v199
v_mov_b32 v200,v196
v_mul_f32_e64 v204,-v196,v205
v_mul_f32 v203,v199,v198
v_mul_f32 v201,s78,v198
v_mul_f32 v199,v198,v198
v_mul_f32 v202,s79,v199
v_mul_f32 v202,v202,v201
s_lshr_b32 s63,s63,1
s_sub_u32 s63,s89,s63
buffer_store_dwordx4 v[200:203],v4,s[36:39],s89 offen offset:0 sc1
buffer_store_dwordx2 v[204:205],v10,s[36:39],s63 offen offset:256 sc1
s_add_u32 m0,s66,s52
s_cmp_ge_u32 s51,12582912
s_cbranch_scc1 Lr_ys3_%=
buffer_load_dwordx4 v11,s[36:39],s51 offen offset:0 sc1 lds
buffer_load_dwordx4 v11,s[36:39],s51 offen offset:1024 sc1 lds
buffer_load_dwordx4 v11,s[36:39],s51 offen offset:2048 sc1 lds
buffer_load_dwordx4 v11,s[36:39],s51 offen offset:3072 sc1 lds
s_lshl_b32 s63,s50,14
s_add_u32 s51,s51,s63
Lr_ys3_%=:
s_add_u32 s66,s66,16384
s_cmp_eq_u32 s66,151552
s_cselect_b32 s66,4096,s66
v_add_u32 v18,s66,v11
s_mov_b32 s60,s90
s_waitcnt vmcnt(6)
s_branch Lr_it1_%=
Lr_tc0_%=:
s_add_u32 s63,s87,71303168
buffer_load_dwordx4 a[64:67],v11,s[36:39],s63 offen offset:0 sc1
buffer_load_dwordx4 a[68:71],v11,s[36:39],s63 offen offset:1024 sc1
buffer_load_dwordx4 a[72:75],v11,s[36:39],s63 offen offset:2048 sc1
buffer_load_dwordx4 a[76:79],v11,s[36:39],s63 offen offset:3072 sc1
s_add_u32 s63,s87,89128960
buffer_load_dwordx4 a[80:83],v11,s[36:39],s63 offen offset:0 sc1
buffer_load_dwordx4 a[84:87],v11,s[36:39],s63 offen offset:1024 sc1
buffer_load_dwordx4 a[88:91],v11,s[36:39],s63 offen offset:2048 sc1
buffer_load_dwordx4 a[92:95],v11,s[36:39],s63 offen offset:3072 sc1
s_add_u32 s63,s87,99614720
buffer_load_dwordx4 a[96:99],v11,s[36:39],s63 offen offset:0 sc1
buffer_load_dwordx4 a[100:103],v11,s[36:39],s63 offen offset:1024 sc1
buffer_load_dwordx4 a[104:107],v11,s[36:39],s63 offen offset:2048 sc1
buffer_load_dwordx4 a[108:111],v11,s[36:39],s63 offen offset:3072 sc1
s_add_u32 s63,s87,109051904
buffer_load_dwordx4 a[112:115],v11,s[36:39],s63 offen offset:0 sc1
buffer_load_dwordx4 a[116:119],v11,s[36:39],s63 offen offset:1024 sc1
buffer_load_dwordx4 a[120:123],v11,s[36:39],s63 offen offset:2048 sc1
buffer_load_dwordx4 a[124:127],v11,s[36:39],s63 offen offset:3072 sc1
buffer_load_dword v8,v6,s[36:39],s69 offen offset:0 sc1
s_waitcnt vmcnt(17)
buffer_store_dword v7,v5,s[36:39],s60 offen offset:0 sc1
s_mov_b32 s60,536870912
s_waitcnt vmcnt(1)
s_branch Lr_it1_%=
Lr_it1_%=:
s_mov_b32 s88,s58
s_mov_b32 s89,s59
s_mov_b32 s90,s49
s_add_u32 s54,s54,s50
s_lshr_b32 s63,s54,1
s_and_b32 s64,s54,1
s_lshl_b32 s64,s64,7
s_lshl_b32 s87,s54,14
s_lshl_b32 s58,s63,15
s_add_u32 s58,s58,s64
s_add_u32 s58,s58,16908288
s_lshl_b32 s59,s63,9
s_add_u32 s59,s59,s64
s_add_u32 s59,s59,118556672
s_lshl_b32 s49,s54,9
s_add_u32 s49,s49,s92
s_lshl_b32 s91,s63,11
s_add_u32 s91,s91,121405440
s_lshl_b32 s69,s50,10
s_add_u32 s69,s69,s91
v_cmp_eq_u32 vcc,s48,v8
s_nop 0
s_cmp_eq_u64 vcc,exec
s_cselect_b32 s63,s54,256
s_cmp_lt_u32 s63,256
s_cbranch_scc1 Lr_ta1_%=
ds_read_b128 v[32:35],v18 offset:0
ds_read_b128 v[36:39],v18 offset:1024
ds_read_b128 v[40:43],v18 offset:2048
ds_read_b128 v[44:47],v18 offset:3072
v_mfma_f32_16x16x32_bf16 v[128:131],v[20:23],a[64:67],0
v_mfma_f32_16x16x32_bf16 v[128:131],v[20:23],a[80:83],v[128:131]
v_mfma_f32_16x16x32_bf16 v[128:131],v[20:23],a[96:99],v[128:131]
v_mfma_f32_16x16x32_bf16 v[128:131],v[20:23],a[112:115],v[128:131]
v_mfma_f32_16x16x32_bf16 v[132:135],v[24:27],a[64:67],0
v_mfma_f32_16x16x32_bf16 v[132:135],v[24:27],a[80:83],v[132:135]
v_mfma_f32_16x16x32_bf16 v[132:135],v[24:27],a[96:99],v[132:135]
v_mfma_f32_16x16x32_bf16 v[132:135],v[24:27],a[112:115],v[132:135]
v_mfma_f32_16x16x32_bf16 v[136:139],v[20:23],a[68:71],0
v_mfma_f32_16x16x32_bf16 v[136:139],v[20:23],a[84:87],v[136:139]
v_mfma_f32_16x16x32_bf16 v[136:139],v[20:23],a[100:103],v[136:139]
s_nop 0
v_cvt_pk_bf16_f32 v208,v128,v129
v_cvt_pk_bf16_f32 v209,v130,v131
v_mfma_f32_16x16x32_bf16 v[136:139],v[20:23],a[116:119],v[136:139]
v_mfma_f32_16x16x32_bf16 v[140:143],v[24:27],a[68:71],0
v_mfma_f32_16x16x32_bf16 v[140:143],v[24:27],a[84:87],v[140:143]
v_mfma_f32_16x16x32_bf16 v[140:143],v[24:27],a[100:103],v[140:143]
v_cvt_pk_bf16_f32 v210,v132,v133
v_cvt_pk_bf16_f32 v211,v134,v135
v_mfma_f32_16x16x32_bf16 v[140:143],v[24:27],a[116:119],v[140:143]
v_mfma_f32_16x16x32_bf16 v[144:147],v[20:23],a[72:75],0
v_mfma_f32_16x16x32_bf16 v[144:147],v[20:23],a[88:91],v[144:147]
v_mfma_f32_16x16x32_bf16 v[144:147],v[20:23],a[104:107],v[144:147]
v_cvt_pk_bf16_f32 v212,v136,v137
v_cvt_pk_bf16_f32 v213,v138,v139
v_mfma_f32_16x16x32_bf16 v[144:147],v[20:23],a[120:123],v[144:147]
s_waitcnt vmcnt(0)
buffer_store_dword v7,v5,s[36:39],s60 offen offset:0 sc1
s_mov_b32 s60,536870912
v_mfma_f32_16x16x32_bf16 v[148:151],v[24:27],a[72:75],0
v_mfma_f32_16x16x32_bf16 v[148:151],v[24:27],a[88:91],v[148:151]
v_mfma_f32_16x16x32_bf16 v[148:151],v[24:27],a[104:107],v[148:151]
v_cvt_pk_bf16_f32 v214,v140,v141
v_cvt_pk_bf16_f32 v215,v142,v143
v_mfma_f32_16x16x32_bf16 v[148:151],v[24:27],a[120:123],v[148:151]
v_mfma_f32_16x16x32_bf16 v[28:31],v[192:195],v[208:211],0
v_mfma_f32_16x16x32_bf16 v[204:207],v[208:211],v[208:211],0
v_mfma_f32_16x16x32_bf16 v[152:155],v[20:23],a[76:79],0
v_mfma_f32_16x16x32_bf16 v[152:155],v[20:23],a[92:95],v[152:155]
v_mfma_f32_16x16x32_bf16 v[152:155],v[20:23],a[108:111],v[152:155]
v_cvt_pk_bf16_f32 v216,v144,v145
v_cvt_pk_bf16_f32 v217,v146,v147
v_mfma_f32_16x16x32_bf16 v[152:155],v[20:23],a[124:127],v[152:155]
v_mfma_f32_16x16x32_bf16 v[156:159],v[24:27],a[76:79],0
v_mfma_f32_16x16x32_bf16 v[156:159],v[24:27],a[92:95],v[156:159]
v_mfma_f32_16x16x32_bf16 v[156:159],v[24:27],a[108:111],v[156:159]
v_cvt_pk_bf16_f32 v218,v148,v149
v_cvt_pk_bf16_f32 v219,v150,v151
v_mfma_f32_16x16x32_bf16 v[156:159],v[24:27],a[124:127],v[156:159]
v_mfma_f32_16x16x32_bf16 v[28:31],v[192:195],v[212:215],v[28:31]
v_mfma_f32_16x16x32_bf16 v[204:207],v[212:215],v[212:215],v[204:207]
v_mfma_f32_16x16x32_bf16 v[28:31],v[192:195],v[216:219],v[28:31]
v_mfma_f32_16x16x32_bf16 v[204:207],v[216:219],v[216:219],v[204:207]
s_waitcnt lgkmcnt(0))"
R"(
v_mfma_f32_16x16x32_bf16 v[160:163],v[20:23],v[32:35],0
v_cvt_pk_bf16_f32 v220,v152,v153
v_cvt_pk_bf16_f32 v221,v154,v155
v_mfma_f32_16x16x32_bf16 v[164:167],v[24:27],v[32:35],0
v_cvt_pk_bf16_f32 v222,v156,v157
v_cvt_pk_bf16_f32 v223,v158,v159
v_mfma_f32_16x16x32_bf16 v[168:171],v[20:23],v[36:39],0
s_nop 0
v_mfma_f32_16x16x32_bf16 v[28:31],v[192:195],v[220:223],v[28:31]
v_mfma_f32_16x16x32_bf16 v[204:207],v[220:223],v[220:223],v[204:207]
v_mfma_f32_16x16x32_bf16 v[172:175],v[24:27],v[36:39],0
v_mfma_f32_16x16x32_bf16 v[176:179],v[20:23],v[40:43],0
v_mfma_f32_16x16x32_bf16 v[180:183],v[24:27],v[40:43],0
s_nop 3
v_add_f32_dpp v224,v28,v28 row_ror:8 row_mask:0xf bank_mask:0xf
v_cndmask_b32 v225,v204,v205,s[46:47]
v_cndmask_b32 v226,v206,v207,s[46:47]
v_cndmask_b32 v225,v225,v226,s[56:57]
v_mfma_f32_16x16x32_bf16 v[184:187],v[20:23],v[44:47],0
ds_write_b32 v2,v224 offset:0
ds_write_b32 v0,v225 offset:0
v_mfma_f32_16x16x32_bf16 v[188:191],v[24:27],v[44:47],0
s_waitcnt lgkmcnt(0)
s_barrier
ds_read_b128 v[196:199],v3 offset:0
ds_read_b128 v[200:203],v3 offset:16
ds_read_b128 v[204:207],v3 offset:32
s_waitcnt lgkmcnt(0)
v_add_f32 v196,v196,v197
v_add_f32 v198,v198,v199
v_add_f32 v200,v200,v204
v_add_f32 v201,v201,v205
v_add_f32 v202,v202,v206
v_add_f32 v203,v203,v207
v_add_f32 v196,v196,v198
v_add_f32 v200,v200,v201
v_add_f32 v202,v202,v203
v_mul_f32 v196,s76,v196
v_add_f32 v197,v200,v202
v_mul_f32 v197,s76,v197
v_fma_f32 v197,-v196,v196,v197
v_max_f32 v197,0,v197
v_add_f32 v197,s77,v197
v_rsq_f32 v228,v197
s_nop 0
v_mul_f32_e64 v230,-v196,v228
v_mov_b32 v229,v228
v_mov_b32 v231,v230
v_pk_fma_f32 v[196:197],v[128:129],v[228:229],v[230:231]
v_pk_fma_f32 v[198:199],v[130:131],v[228:229],v[230:231]
v_pk_fma_f32 v[200:201],v[132:133],v[228:229],v[230:231]
v_pk_fma_f32 v[202:203],v[134:135],v[228:229],v[230:231]
v_pk_fma_f32 v[128:129],v[196:197],v[160:161],v[128:129]
v_pk_fma_f32 v[130:131],v[198:199],v[162:163],v[130:131]
v_pk_fma_f32 v[132:133],v[200:201],v[164:165],v[132:133]
v_pk_fma_f32 v[134:135],v[202:203],v[166:167],v[134:135]
v_pk_fma_f32 v[196:197],v[136:137],v[228:229],v[230:231]
v_pk_fma_f32 v[198:199],v[138:139],v[228:229],v[230:231]
v_pk_fma_f32 v[200:201],v[140:141],v[228:229],v[230:231]
v_pk_fma_f32 v[202:203],v[142:143],v[228:229],v[230:231]
v_pk_fma_f32 v[136:137],v[196:197],v[168:169],v[136:137]
v_pk_fma_f32 v[138:139],v[198:199],v[170:171],v[138:139]
v_pk_fma_f32 v[140:141],v[200:201],v[172:173],v[140:141]
v_pk_fma_f32 v[142:143],v[202:203],v[174:175],v[142:143]
v_pk_fma_f32 v[196:197],v[144:145],v[228:229],v[230:231]
v_pk_fma_f32 v[198:199],v[146:147],v[228:229],v[230:231]
v_pk_fma_f32 v[200:201],v[148:149],v[228:229],v[230:231]
v_pk_fma_f32 v[202:203],v[150:151],v[228:229],v[230:231]
v_pk_fma_f32 v[144:145],v[196:197],v[176:177],v[144:145]
v_pk_fma_f32 v[146:147],v[198:199],v[178:179],v[146:147]
v_pk_fma_f32 v[148:149],v[200:201],v[180:181],v[148:149]
v_pk_fma_f32 v[150:151],v[202:203],v[182:183],v[150:151]
v_pk_fma_f32 v[196:197],v[152:153],v[228:229],v[230:231]
v_pk_fma_f32 v[198:199],v[154:155],v[228:229],v[230:231]
v_pk_fma_f32 v[200:201],v[156:157],v[228:229],v[230:231]
v_pk_fma_f32 v[202:203],v[158:159],v[228:229],v[230:231]
v_pk_fma_f32 v[152:153],v[196:197],v[184:185],v[152:153]
v_pk_fma_f32 v[154:155],v[198:199],v[186:187],v[154:155]
v_pk_fma_f32 v[156:157],v[200:201],v[188:189],v[156:157]
v_pk_fma_f32 v[158:159],v[202:203],v[190:191],v[158:159]
v_cvt_pk_bf16_f32 v208,v128,v129
v_cvt_pk_bf16_f32 v209,v130,v131
v_cvt_pk_bf16_f32 v210,v132,v133
v_cvt_pk_bf16_f32 v211,v134,v135
v_cvt_pk_bf16_f32 v212,v136,v137
v_cvt_pk_bf16_f32 v213,v138,v139
v_cvt_pk_bf16_f32 v214,v140,v141
v_cvt_pk_bf16_f32 v215,v142,v143
v_mfma_f32_16x16x32_bf16 v[28:31],v[192:195],v[208:211],0
v_cvt_pk_bf16_f32 v216,v144,v145
v_cvt_pk_bf16_f32 v217,v146,v147
v_mfma_f32_16x16x32_bf16 v[204:207],v[208:211],v[208:211],0
v_cvt_pk_bf16_f32 v218,v148,v149
v_cvt_pk_bf16_f32 v219,v150,v151
v_mfma_f32_16x16x32_bf16 v[28:31],v[192:195],v[212:215],v[28:31]
v_cvt_pk_bf16_f32 v220,v152,v153
v_cvt_pk_bf16_f32 v221,v154,v155
v_mfma_f32_16x16x32_bf16 v[204:207],v[212:215],v[212:215],v[204:207]
v_cvt_pk_bf16_f32 v222,v156,v157
v_cvt_pk_bf16_f32 v223,v158,v159
v_mfma_f32_16x16x32_bf16 v[28:31],v[192:195],v[216:219],v[28:31]
v_mfma_f32_16x16x32_bf16 v[204:207],v[216:219],v[216:219],v[204:207]
v_mfma_f32_16x16x32_bf16 v[28:31],v[192:195],v[220:223],v[28:31]
v_mfma_f32_16x16x32_bf16 v[204:207],v[220:223],v[220:223],v[204:207]
buffer_load_dword v8,v6,s[36:39],s91 offen offset:0 sc1
buffer_store_dwordx4 v[208:211],v1,s[36:39],s88 offen offset:0 sc1
buffer_store_dwordx4 v[212:215],v1,s[36:39],s88 offen offset:2048 sc1
s_add_u32 s88,s88,4096
s_nop 2
v_add_f32_dpp v224,v28,v28 row_ror:8 row_mask:0xf bank_mask:0xf
v_cndmask_b32 v225,v204,v205,s[46:47]
v_cndmask_b32 v226,v206,v207,s[46:47]
v_cndmask_b32 v225,v225,v226,s[56:57]
s_and_b32 s63,s89,128
ds_write_b32 v2,v224 offset:1024
ds_write_b32 v0,v225 offset:1024
buffer_store_dwordx4 v[216:219],v1,s[36:39],s88 offen offset:0 sc1
buffer_store_dwordx4 v[220:223],v1,s[36:39],s88 offen offset:2048 sc1
s_waitcnt lgkmcnt(0)
s_barrier
ds_read_b128 v[196:199],v3 offset:1024
ds_read_b128 v[200:203],v3 offset:1040
ds_read_b128 v[204:207],v3 offset:1056
s_waitcnt lgkmcnt(0)
v_add_f32 v196,v196,v197
v_add_f32 v198,v198,v199
v_add_f32 v200,v200,v204
v_add_f32 v201,v201,v205
v_add_f32 v202,v202,v206
v_add_f32 v203,v203,v207
v_add_f32 v196,v196,v198
v_add_f32 v200,v200,v201
v_add_f32 v202,v202,v203
v_mul_f32 v196,s76,v196
v_add_f32 v197,v200,v202
v_mul_f32 v197,s76,v197
v_fma_f32 v197,-v196,v196,v197
v_max_f32 v197,0,v197
v_add_f32 v198,s77,v197
v_fma_f32 v199,s77,v198,v197
v_rsq_f32 v205,v198
v_rsq_f32 v198,v199
v_mov_b32 v200,v196
v_mul_f32_e64 v204,-v196,v205
v_mul_f32 v203,v199,v198
v_mul_f32 v201,s78,v198
v_mul_f32 v199,v198,v198
v_mul_f32 v202,s79,v199
v_mul_f32 v202,v202,v201
s_lshr_b32 s63,s63,1
s_sub_u32 s63,s89,s63
buffer_store_dwordx4 v[200:203],v4,s[36:39],s89 offen offset:0 sc1
buffer_store_dwordx2 v[204:205],v10,s[36:39],s63 offen offset:256 sc1
s_add_u32 m0,s66,s52
s_cmp_ge_u32 s51,12582912
s_cbranch_scc1 Lr_ys4_%=
buffer_load_dwordx4 v11,s[36:39],s51 offen offset:0 sc1 lds
buffer_load_dwordx4 v11,s[36:39],s51 offen offset:1024 sc1 lds
buffer_load_dwordx4 v11,s[36:39],s51 offen offset:2048 sc1 lds
buffer_load_dwordx4 v11,s[36:39],s51 offen offset:3072 sc1 lds
s_lshl_b32 s63,s50,14
s_add_u32 s51,s51,s63
Lr_ys4_%=:
s_add_u32 s66,s66,16384
s_cmp_eq_u32 s66,151552
s_cselect_b32 s66,4096,s66
v_add_u32 v18,s66,v11
s_mov_b32 s60,s90
s_waitcnt vmcnt(6)
v_cmp_eq_u32 vcc,s48,v8
s_nop 0
s_cmp_eq_u64 vcc,exec
s_cselect_b32 s63,s54,256
s_cmp_lt_u32 s63,256
s_cbranch_scc1 Lr_tc1_%=
buffer_load_dword v8,v6,s[36:39],s91 offen offset:0 sc1
s_waitcnt vmcnt(0)
buffer_store_dword v7,v5,s[36:39],s60 offen offset:0 sc1
s_mov_b32 s60,536870912
v_cmp_eq_u32 vcc,s48,v8
s_nop 0
s_cmp_eq_u64 vcc,exec
s_cselect_b32 s63,s54,256
s_cmp_lt_u32 s63,256
s_cbranch_scc1 Lr_bq1_%=
s_cmp_ge_u32 s54,256
s_cbranch_scc1 Lr_done_%=
Lr_bp1_%=:
buffer_load_dword v8,v6,s[36:39],s91 offen offset:0 sc1
s_waitcnt vmcnt(0)
v_cmp_eq_u32 vcc,s48,v8
s_nop 0
s_cmp_eq_u64 vcc,exec
s_cselect_b32 s63,1,s96
s_cmp_lg_u32 s63,0
s_cbranch_scc1 Lr_bq1_%=
s_add_u32 s74,s74,1
s_cmp_ge_u32 s74,20000
s_cselect_b32 s96,1,0
s_sleep 4)"
R"(
s_branch Lr_bp1_%=
Lr_bq1_%=:
s_add_u32 s63,s87,71303168
buffer_load_dwordx4 a[0:3],v11,s[36:39],s63 offen offset:0 sc1
buffer_load_dwordx4 a[4:7],v11,s[36:39],s63 offen offset:1024 sc1
buffer_load_dwordx4 a[8:11],v11,s[36:39],s63 offen offset:2048 sc1
buffer_load_dwordx4 a[12:15],v11,s[36:39],s63 offen offset:3072 sc1
s_add_u32 s63,s87,89128960
buffer_load_dwordx4 a[16:19],v11,s[36:39],s63 offen offset:0 sc1
buffer_load_dwordx4 a[20:23],v11,s[36:39],s63 offen offset:1024 sc1
buffer_load_dwordx4 a[24:27],v11,s[36:39],s63 offen offset:2048 sc1
buffer_load_dwordx4 a[28:31],v11,s[36:39],s63 offen offset:3072 sc1
s_add_u32 s63,s87,99614720
buffer_load_dwordx4 a[32:35],v11,s[36:39],s63 offen offset:0 sc1
buffer_load_dwordx4 a[36:39],v11,s[36:39],s63 offen offset:1024 sc1
buffer_load_dwordx4 a[40:43],v11,s[36:39],s63 offen offset:2048 sc1
buffer_load_dwordx4 a[44:47],v11,s[36:39],s63 offen offset:3072 sc1
s_add_u32 s63,s87,109051904
buffer_load_dwordx4 a[48:51],v11,s[36:39],s63 offen offset:0 sc1
buffer_load_dwordx4 a[52:55],v11,s[36:39],s63 offen offset:1024 sc1
buffer_load_dwordx4 a[56:59],v11,s[36:39],s63 offen offset:2048 sc1
buffer_load_dwordx4 a[60:63],v11,s[36:39],s63 offen offset:3072 sc1
buffer_load_dword v8,v6,s[36:39],s69 offen offset:0 sc1
s_waitcnt vmcnt(0)
s_branch Lr_it0_%=
Lr_ta1_%=:
ds_read_b128 v[32:35],v18 offset:0
ds_read_b128 v[36:39],v18 offset:1024
ds_read_b128 v[40:43],v18 offset:2048
ds_read_b128 v[44:47],v18 offset:3072
v_mfma_f32_16x16x32_bf16 v[128:131],v[20:23],a[64:67],0
s_add_u32 s63,s87,71303168
v_mfma_f32_16x16x32_bf16 v[128:131],v[20:23],a[80:83],v[128:131]
buffer_load_dwordx4 a[0:3],v11,s[36:39],s63 offen offset:0 sc1
v_mfma_f32_16x16x32_bf16 v[128:131],v[20:23],a[96:99],v[128:131]
v_mfma_f32_16x16x32_bf16 v[128:131],v[20:23],a[112:115],v[128:131]
buffer_load_dwordx4 a[4:7],v11,s[36:39],s63 offen offset:1024 sc1
v_mfma_f32_16x16x32_bf16 v[132:135],v[24:27],a[64:67],0
v_mfma_f32_16x16x32_bf16 v[132:135],v[24:27],a[80:83],v[132:135]
v_mfma_f32_16x16x32_bf16 v[132:135],v[24:27],a[96:99],v[132:135]
v_mfma_f32_16x16x32_bf16 v[132:135],v[24:27],a[112:115],v[132:135]
buffer_load_dwordx4 a[8:11],v11,s[36:39],s63 offen offset:2048 sc1
v_mfma_f32_16x16x32_bf16 v[136:139],v[20:23],a[68:71],0
v_mfma_f32_16x16x32_bf16 v[136:139],v[20:23],a[84:87],v[136:139]
v_mfma_f32_16x16x32_bf16 v[136:139],v[20:23],a[100:103],v[136:139]
v_cvt_pk_bf16_f32 v208,v128,v129
v_cvt_pk_bf16_f32 v209,v130,v131
v_mfma_f32_16x16x32_bf16 v[136:139],v[20:23],a[116:119],v[136:139]
buffer_load_dwordx4 a[12:15],v11,s[36:39],s63 offen offset:3072 sc1
v_mfma_f32_16x16x32_bf16 v[140:143],v[24:27],a[68:71],0
s_add_u32 s63,s87,89128960
v_mfma_f32_16x16x32_bf16 v[140:143],v[24:27],a[84:87],v[140:143]
v_mfma_f32_16x16x32_bf16 v[140:143],v[24:27],a[100:103],v[140:143]
v_cvt_pk_bf16_f32 v210,v132,v133
v_cvt_pk_bf16_f32 v211,v134,v135
v_mfma_f32_16x16x32_bf16 v[140:143],v[24:27],a[116:119],v[140:143]
buffer_load_dwordx4 a[16:19],v11,s[36:39],s63 offen offset:0 sc1
v_mfma_f32_16x16x32_bf16 v[144:147],v[20:23],a[72:75],0
v_mfma_f32_16x16x32_bf16 v[144:147],v[20:23],a[88:91],v[144:147]
v_mfma_f32_16x16x32_bf16 v[144:147],v[20:23],a[104:107],v[144:147]
v_cvt_pk_bf16_f32 v212,v136,v137
v_cvt_pk_bf16_f32 v213,v138,v139
v_mfma_f32_16x16x32_bf16 v[144:147],v[20:23],a[120:123],v[144:147]
buffer_load_dwordx4 a[20:23],v11,s[36:39],s63 offen offset:1024 sc1
v_mfma_f32_16x16x32_bf16 v[148:151],v[24:27],a[72:75],0
v_mfma_f32_16x16x32_bf16 v[148:151],v[24:27],a[88:91],v[148:151]
v_mfma_f32_16x16x32_bf16 v[148:151],v[24:27],a[104:107],v[148:151]
v_cvt_pk_bf16_f32 v214,v140,v141
v_cvt_pk_bf16_f32 v215,v142,v143
v_mfma_f32_16x16x32_bf16 v[148:151],v[24:27],a[120:123],v[148:151]
s_waitcnt vmcnt(6)
buffer_store_dword v7,v5,s[36:39],s60 offen offset:0 sc1
s_mov_b32 s60,536870912
buffer_load_dwordx4 a[24:27],v11,s[36:39],s63 offen offset:2048 sc1
v_mfma_f32_16x16x32_bf16 v[28:31],v[192:195],v[208:211],0
v_mfma_f32_16x16x32_bf16 v[204:207],v[208:211],v[208:211],0
v_mfma_f32_16x16x32_bf16 v[152:155],v[20:23],a[76:79],0
v_mfma_f32_16x16x32_bf16 v[152:155],v[20:23],a[92:95],v[152:155]
buffer_load_dwordx4 a[28:31],v11,s[36:39],s63 offen offset:3072 sc1
v_mfma_f32_16x16x32_bf16 v[152:155],v[20:23],a[108:111],v[152:155]
v_cvt_pk_bf16_f32 v216,v144,v145
v_cvt_pk_bf16_f32 v217,v146,v147
v_mfma_f32_16x16x32_bf16 v[152:155],v[20:23],a[124:127],v[152:155]
s_add_u32 s63,s87,99614720
v_mfma_f32_16x16x32_bf16 v[156:159],v[24:27],a[76:79],0
v_mfma_f32_16x16x32_bf16 v[156:159],v[24:27],a[92:95],v[156:159]
buffer_load_dwordx4 a[32:35],v11,s[36:39],s63 offen offset:0 sc1
v_mfma_f32_16x16x32_bf16 v[156:159],v[24:27],a[108:111],v[156:159]
v_cvt_pk_bf16_f32 v218,v148,v149
v_cvt_pk_bf16_f32 v219,v150,v151
v_mfma_f32_16x16x32_bf16 v[156:159],v[24:27],a[124:127],v[156:159]
v_mfma_f32_16x16x32_bf16 v[28:31],v[192:195],v[212:215],v[28:31]
v_mfma_f32_16x16x32_bf16 v[204:207],v[212:215],v[212:215],v[204:207]
buffer_load_dwordx4 a[36:39],v11,s[36:39],s63 offen offset:1024 sc1
v_mfma_f32_16x16x32_bf16 v[28:31],v[192:195],v[216:219],v[28:31]
v_mfma_f32_16x16x32_bf16 v[204:207],v[216:219],v[216:219],v[204:207]
s_waitcnt lgkmcnt(0)
v_mfma_f32_16x16x32_bf16 v[160:163],v[20:23],v[32:35],0
v_cvt_pk_bf16_f32 v220,v152,v153
v_cvt_pk_bf16_f32 v221,v154,v155
v_mfma_f32_16x16x32_bf16 v[164:167],v[24:27],v[32:35],0
v_cvt_pk_bf16_f32 v222,v156,v157
v_cvt_pk_bf16_f32 v223,v158,v159
v_mfma_f32_16x16x32_bf16 v[168:171],v[20:23],v[36:39],0
buffer_load_dwordx4 a[40:43],v11,s[36:39],s63 offen offset:2048 sc1
v_mfma_f32_16x16x32_bf16 v[28:31],v[192:195],v[220:223],v[28:31]
buffer_load_dwordx4 a[44:47],v11,s[36:39],s63 offen offset:3072 sc1
v_mfma_f32_16x16x32_bf16 v[204:207],v[220:223],v[220:223],v[204:207]
s_add_u32 s63,s87,109051904
v_mfma_f32_16x16x32_bf16 v[172:175],v[24:27],v[36:39],0
v_mfma_f32_16x16x32_bf16 v[176:179],v[20:23],v[40:43],0
buffer_load_dwordx4 a[48:51],v11,s[36:39],s63 offen offset:0 sc1
v_mfma_f32_16x16x32_bf16 v[180:183],v[24:27],v[40:43],0
s_nop 0
v_add_f32_dpp v224,v28,v28 row_ror:8 row_mask:0xf bank_mask:0xf
s_nop 0
v_cndmask_b32 v225,v204,v205,s[46:47]
v_cndmask_b32 v226,v206,v207,s[46:47]
v_cndmask_b32 v225,v225,v226,s[56:57]
v_mfma_f32_16x16x32_bf16 v[184:187],v[20:23],v[44:47],0
ds_write_b32 v2,v224 offset:0
ds_write_b32 v0,v225 offset:0
v_mfma_f32_16x16x32_bf16 v[188:191],v[24:27],v[44:47],0
s_waitcnt lgkmcnt(0)
buffer_load_dwordx4 a[52:55],v11,s[36:39],s63 offen offset:1024 sc1
s_barrier
buffer_load_dwordx4 a[56:59],v11,s[36:39],s63 offen offset:2048 sc1
ds_read_b128 v[196:199],v3 offset:0
buffer_load_dwordx4 a[60:63],v11,s[36:39],s63 offen offset:3072 sc1
ds_read_b128 v[200:203],v3 offset:16
ds_read_b128 v[204:207],v3 offset:32
s_waitcnt lgkmcnt(0)
buffer_load_dword v8,v6,s[36:39],s69 offen offset:0 sc1
v_add_f32 v196,v196,v197
v_add_f32 v198,v198,v199
v_add_f32 v200,v200,v204
v_add_f32 v201,v201,v205
v_add_f32 v202,v202,v206
v_add_f32 v203,v203,v207
v_add_f32 v196,v196,v198
v_add_f32 v200,v200,v201
v_add_f32 v202,v202,v203
v_mul_f32 v196,s76,v196
v_add_f32 v197,v200,v202
v_mul_f32 v197,s76,v197
v_fma_f32 v197,-v196,v196,v197
v_max_f32 v197,0,v197
v_add_f32 v197,s77,v197
v_rsq_f32 v228,v197
s_nop 0
v_mul_f32_e64 v230,-v196,v228
v_mov_b32 v229,v228
v_mov_b32 v231,v230
v_pk_fma_f32 v[196:197],v[128:129],v[228:229],v[230:231]
v_pk_fma_f32 v[198:199],v[130:131],v[228:229],v[230:231]
v_pk_fma_f32 v[200:201],v[132:133],v[228:229],v[230:231]
v_pk_fma_f32 v[202:203],v[134:135],v[228:229],v[230:231]
v_pk_fma_f32 v[128:129],v[196:197],v[160:161],v[128:129]
v_pk_fma_f32 v[130:131],v[198:199],v[162:163],v[130:131]
v_pk_fma_f32 v[132:133],v[200:201],v[164:165],v[132:133]
v_pk_fma_f32 v[134:135],v[202:203],v[166:167],v[134:135]
v_pk_fma_f32 v[196:197],v[136:137],v[228:229],v[230:231]
v_pk_fma_f32 v[198:199],v[138:139],v[228:229],v[230:231]
v_pk_fma_f32 v[200:201],v[140:141],v[228:229],v[230:231]
v_pk_fma_f32 v[202:203],v[142:143],v[228:229],v[230:231]
v_pk_fma_f32 v[136:137],v[196:197],v[168:169],v[136:137]
v_pk_fma_f32 v[138:139],v[198:199],v[170:171],v[138:139]
v_pk_fma_f32 v[140:141],v[200:201],v[172:173],v[140:141]
v_pk_fma_f32 v[142:143],v[202:203],v[174:175],v[142:143]
v_pk_fma_f32 v[196:197],v[144:145],v[228:229],v[230:231]
v_pk_fma_f32 v[198:199],v[146:147],v[228:229],v[230:231]
v_pk_fma_f32 v[200:201],v[148:149],v[228:229],v[230:231]
v_pk_fma_f32 v[202:203],v[150:151],v[228:229],v[230:231]
v_pk_fma_f32 v[144:145],v[196:197],v[176:177],v[144:145]
v_pk_fma_f32 v[146:147],v[198:199],v[178:179],v[146:147]
v_pk_fma_f32 v[148:149],v[200:201],v[180:181],v[148:149]
v_pk_fma_f32 v[150:151],v[202:203],v[182:183],v[150:151]
v_pk_fma_f32 v[196:197],v[152:153],v[228:229],v[230:231]
v_pk_fma_f32 v[198:199],v[154:155],v[228:229],v[230:231]
v_pk_fma_f32 v[200:201],v[156:157],v[228:229],v[230:231]
v_pk_fma_f32 v[202:203],v[158:159],v[228:229],v[230:231]
v_pk_fma_f32 v[152:153],v[196:197],v[184:185],v[152:153]
v_pk_fma_f32 v[154:155],v[198:199],v[186:187],v[154:155]
v_pk_fma_f32 v[156:157],v[200:201],v[188:189],v[156:157]
v_pk_fma_f32 v[158:159],v[202:203],v[190:191],v[158:159]
v_cvt_pk_bf16_f32 v208,v128,v129
v_cvt_pk_bf16_f32 v209,v130,v131
v_cvt_pk_bf16_f32 v210,v132,v133
v_cvt_pk_bf16_f32 v211,v134,v135
v_cvt_pk_bf16_f32 v212,v136,v137
v_cvt_pk_bf16_f32 v213,v138,v139
v_cvt_pk_bf16_f32 v214,v140,v141
v_cvt_pk_bf16_f32 v215,v142,v143
v_mfma_f32_16x16x32_bf16 v[28:31],v[192:195],v[208:211],0
v_cvt_pk_bf16_f32 v216,v144,v145
v_cvt_pk_bf16_f32 v217,v146,v147
v_mfma_f32_16x16x32_bf16 v[204:207],v[208:211],v[208:211],0
v_cvt_pk_bf16_f32 v218,v148,v149
v_cvt_pk_bf16_f32 v219,v150,v151
v_mfma_f32_16x16x32_bf16 v[28:31],v[192:195],v[212:215],v[28:31])"
R"(
v_cvt_pk_bf16_f32 v220,v152,v153
v_cvt_pk_bf16_f32 v221,v154,v155
v_mfma_f32_16x16x32_bf16 v[204:207],v[212:215],v[212:215],v[204:207]
v_cvt_pk_bf16_f32 v222,v156,v157
v_cvt_pk_bf16_f32 v223,v158,v159
v_mfma_f32_16x16x32_bf16 v[28:31],v[192:195],v[216:219],v[28:31]
v_mfma_f32_16x16x32_bf16 v[204:207],v[216:219],v[216:219],v[204:207]
v_mfma_f32_16x16x32_bf16 v[28:31],v[192:195],v[220:223],v[28:31]
v_mfma_f32_16x16x32_bf16 v[204:207],v[220:223],v[220:223],v[204:207]
buffer_store_dwordx4 v[208:211],v1,s[36:39],s88 offen offset:0 sc1
buffer_store_dwordx4 v[212:215],v1,s[36:39],s88 offen offset:2048 sc1
s_add_u32 s88,s88,4096
s_nop 3
v_add_f32_dpp v224,v28,v28 row_ror:8 row_mask:0xf bank_mask:0xf
v_cndmask_b32 v225,v204,v205,s[46:47]
v_cndmask_b32 v226,v206,v207,s[46:47]
v_cndmask_b32 v225,v225,v226,s[56:57]
s_and_b32 s63,s89,128
ds_write_b32 v2,v224 offset:1024
ds_write_b32 v0,v225 offset:1024
buffer_store_dwordx4 v[216:219],v1,s[36:39],s88 offen offset:0 sc1
buffer_store_dwordx4 v[220:223],v1,s[36:39],s88 offen offset:2048 sc1
s_waitcnt lgkmcnt(0)
s_barrier
ds_read_b128 v[196:199],v3 offset:1024
ds_read_b128 v[200:203],v3 offset:1040
ds_read_b128 v[204:207],v3 offset:1056
s_waitcnt lgkmcnt(0)
v_add_f32 v196,v196,v197
v_add_f32 v198,v198,v199
v_add_f32 v200,v200,v204
v_add_f32 v201,v201,v205
v_add_f32 v202,v202,v206
v_add_f32 v203,v203,v207
v_add_f32 v196,v196,v198
v_add_f32 v200,v200,v201
v_add_f32 v202,v202,v203
v_mul_f32 v196,s76,v196
v_add_f32 v197,v200,v202
v_mul_f32 v197,s76,v197
v_fma_f32 v197,-v196,v196,v197
v_max_f32 v197,0,v197
v_add_f32 v198,s77,v197
v_fma_f32 v199,s77,v198,v197
v_rsq_f32 v205,v198
v_rsq_f32 v198,v199
v_mov_b32 v200,v196
v_mul_f32_e64 v204,-v196,v205
v_mul_f32 v203,v199,v198
v_mul_f32 v201,s78,v198
v_mul_f32 v199,v198,v198
v_mul_f32 v202,s79,v199
v_mul_f32 v202,v202,v201
s_lshr_b32 s63,s63,1
s_sub_u32 s63,s89,s63
buffer_store_dwordx4 v[200:203],v4,s[36:39],s89 offen offset:0 sc1
buffer_store_dwordx2 v[204:205],v10,s[36:39],s63 offen offset:256 sc1
s_add_u32 m0,s66,s52
s_cmp_ge_u32 s51,12582912
s_cbranch_scc1 Lr_ys5_%=
buffer_load_dwordx4 v11,s[36:39],s51 offen offset:0 sc1 lds
buffer_load_dwordx4 v11,s[36:39],s51 offen offset:1024 sc1 lds
buffer_load_dwordx4 v11,s[36:39],s51 offen offset:2048 sc1 lds
buffer_load_dwordx4 v11,s[36:39],s51 offen offset:3072 sc1 lds
s_lshl_b32 s63,s50,14
s_add_u32 s51,s51,s63
Lr_ys5_%=:
s_add_u32 s66,s66,16384
s_cmp_eq_u32 s66,151552
s_cselect_b32 s66,4096,s66
v_add_u32 v18,s66,v11
s_mov_b32 s60,s90
s_waitcnt vmcnt(6)
s_branch Lr_it0_%=
Lr_tc1_%=:
s_add_u32 s63,s87,71303168
buffer_load_dwordx4 a[0:3],v11,s[36:39],s63 offen offset:0 sc1
buffer_load_dwordx4 a[4:7],v11,s[36:39],s63 offen offset:1024 sc1
buffer_load_dwordx4 a[8:11],v11,s[36:39],s63 offen offset:2048 sc1
buffer_load_dwordx4 a[12:15],v11,s[36:39],s63 offen offset:3072 sc1
s_add_u32 s63,s87,89128960
buffer_load_dwordx4 a[16:19],v11,s[36:39],s63 offen offset:0 sc1
buffer_load_dwordx4 a[20:23],v11,s[36:39],s63 offen offset:1024 sc1
buffer_load_dwordx4 a[24:27],v11,s[36:39],s63 offen offset:2048 sc1
buffer_load_dwordx4 a[28:31],v11,s[36:39],s63 offen offset:3072 sc1
s_add_u32 s63,s87,99614720
buffer_load_dwordx4 a[32:35],v11,s[36:39],s63 offen offset:0 sc1
buffer_load_dwordx4 a[36:39],v11,s[36:39],s63 offen offset:1024 sc1
buffer_load_dwordx4 a[40:43],v11,s[36:39],s63 offen offset:2048 sc1
buffer_load_dwordx4 a[44:47],v11,s[36:39],s63 offen offset:3072 sc1
s_add_u32 s63,s87,109051904
buffer_load_dwordx4 a[48:51],v11,s[36:39],s63 offen offset:0 sc1
buffer_load_dwordx4 a[52:55],v11,s[36:39],s63 offen offset:1024 sc1
buffer_load_dwordx4 a[56:59],v11,s[36:39],s63 offen offset:2048 sc1
buffer_load_dwordx4 a[60:63],v11,s[36:39],s63 offen offset:3072 sc1
buffer_load_dword v8,v6,s[36:39],s69 offen offset:0 sc1
s_waitcnt vmcnt(17)
buffer_store_dword v7,v5,s[36:39],s60 offen offset:0 sc1
s_mov_b32 s60,536870912
s_waitcnt vmcnt(1)
s_branch Lr_it0_%=
Lr_done_%=:
s_waitcnt vmcnt(0) lgkmcnt(0)
s_barrier
Lcode_r_e:)"
  :: "s"(ws), "s"(sK), "s"(sStep), "s"(sRq), "s"(sEpR), "v"(vRP), "v"(vRF), "v"(vRwr), "v"(vRrd), "v"(vRS), "v"(vFlag), "v"(vL4), "v"(vRL) : "v0", "v1", "v2", "v3", "v4", "v5", "v6", "v7", "v8", "v9", "v10", "v11", "v12", "v13", "v14", "v15", "v16", "v17", "v18", "v19", "v20", "v21", "v22", "v23", "v24", "v25", "v26", "v27", "v28", "v29", "v30", "v31", "v32", "v33", "v34", "v35", "v36", "v37", "v38", "v39", "v40", "v41", "v42", "v43", "v44", "v45", "v46", "v47", "v48", "v49", "v50", "v51", "v52", "v53", "v54", "v55", "v56", "v57", "v58", "v59", "v60", "v61", "v62", "v63", "v64", "v65", "v66", "v67", "v68", "v69", "v70", "v71", "v72", "v73", "v74", "v75", "v76", "v77", "v78", "v79", "v80", "v81", "v82", "v83", "v84", "v85", "v86", "v87", "v88", "v89", "v90", "v91", "v92", "v93", "v94", "v95", "v96", "v97", "v98", "v99", "v100", "v101", "v102", "v103", "v104", "v105", "v106", "v107", "v108", "v109", "v110", "v111", "v112", "v113", "v114", "v115", "v116", "v117", "v118", "v119", "v120", "v121", "v122", "v123", "v124", "v125", "v126", "v127", "v128", "v129", "v130", "v131", "v132", "v133", "v134", "v135", "v136", "v137", "v138", "v139", "v140", "v141", "v142", "v143", "v144", "v145", "v146", "v147", "v148", "v149", "v150", "v151", "v152", "v153", "v154", "v155", "v156", "v157", "v158", "v159", "v160", "v161", "v162", "v163", "v164", "v165", "v166", "v167", "v168", "v169", "v170", "v171", "v172", "v173", "v174", "v175", "v176", "v177", "v178", "v179", "v180", "v181", "v182", "v183", "v184", "v185", "v186", "v187", "v188", "v189", "v190", "v191", "v192", "v193", "v194", "v195", "v196", "v197", "v198", "v199", "v200", "v201", "v202", "v203", "v204", "v205", "v206", "v207", "v208", "v209", "v210", "v211", "v212", "v213", "v214", "v215", "v216", "v217", "v218", "v219", "v220", "v221", "v222", "v223", "v224", "v225", "v226", "v227", "v228", "v229", "v230", "v231", "v255", "a0", "a1", "a2", "a3", "a4", "a5", "a6", "a7", "a8", "a9", "a10", "a11", "a12", "a13", "a14", "a15", "a16", "a17", "a18", "a19", "a20", "a21", "a22", "a23", "a24", "a25", "a26", "a27", "a28", "a29", "a30", "a31", "a32", "a33", "a34", "a35", "a36", "a37", "a38", "a39", "a40", "a41", "a42", "a43", "a44", "a45", "a46", "a47", "a48", "a49", "a50", "a51", "a52", "a53", "a54", "a55", "a56", "a57", "a58", "a59", "a60", "a61", "a62", "a63", "a64", "a65", "a66", "a67", "a68", "a69", "a70", "a71", "a72", "a73", "a74", "a75", "a76", "a77", "a78", "a79", "a80", "a81", "a82", "a83", "a84", "a85", "a86", "a87", "a88", "a89", "a90", "a91", "a92", "a93", "a94", "a95", "a96", "a97", "a98", "a99", "a100", "a101", "a102", "a103", "a104", "a105", "a106", "a107", "a108", "a109", "a110", "a111", "a112", "a113", "a114", "a115", "a116", "a117", "a118", "a119", "a120", "a121", "a122", "a123", "a124", "a125", "a126", "a127", "a128", "a129", "a130", "a131", "a132", "a133", "a134", "a135", "a136", "a137", "a138", "a139", "a140", "a141", "a142", "a143", "a144", "a145", "a146", "a147", "a148", "a149", "a150", "a151", "a152", "a153", "a154", "a155", "a156", "a157", "a158", "a159", "a160", "a161", "a162", "a163", "a164", "a165", "a166", "a167", "a168", "a169", "a170", "a171", "a172", "a173", "a174", "a175", "a176", "a177", "a178", "a179", "a180", "a181", "a182", "a183", "a184", "a185", "a186", "a187", "a188", "a189", "a190", "a191", "a192", "a193", "a194", "a195", "a196", "a197", "a198", "a199", "a200", "a201", "a202", "a203", "a204", "a205", "a206", "a207", "a208", "a209", "a210", "a211", "a212", "a213", "a214", "a215", "a216", "a217", "a218", "a219", "a220", "a221", "a222", "a223", "a224", "a225", "a226", "a227", "a228", "a229", "a230", "a231", "a232", "a233", "a234", "a235", "a236", "a237", "a238", "a239", "a240", "a241", "a242", "a243", "a244", "a245", "a246", "a247", "a248", "a249", "a250", "a251", "a252", "a253", "a254", "a255", "s36", "s37", "s38", "s39", "s40", "s41", "s42", "s43", "s44", "s45", "s46", "s47", "s48", "s49", "s50", "s51", "s52", "s53", "s54", "s55", "s56", "s57", "s58", "s59", "s60", "s61", "s62", "s63", "s64", "s65", "s66", "s67", "s68", "s69", "s70", "s71", "s72", "s73", "s74", "s75", "s76", "s77", "s78", "s79", "s80", "s81", "s82", "s83", "s84", "s85", "s86", "s87", "s88", "s89", "s90", "s91", "s92", "s93", "s94", "s95", "s96", "s97", "s98", "s99", "vcc", "scc", "memory");
      if (N3) { kind = 3; rep = 5; }
      else if (N1 && slice >= 2) { kind = 4; rep = 5; kh = (slice - 2) >> 2; slice = (slice - 2) & 3; }
    }
   if (kind < 3) {
    KARG(ws, 10) KARG(w12, 5 + kind)
    COMMON(kind == 1 ? 8 : 16, kind == 1 ? OFF_FXP : OFF_FH, (kind == 1 ? OFF_FH : OFF_FX1) + slice * 128, AUX + 1536, 2048, 2048, 4);
    const int rot = (ROT >> (kind - 1) & 1) * rep, g0 = ((wave >> 1) + rot) & 3, g1 = g0 ^ 2;
    const int r0 = 16 + l15 - (kind == 2 ? ((g0 & 1) ? -1 : 1) * (1 + (g0 >> 1)) : 0), r1 = 16 + l15 - (kind == 2 ? ((g1 & 1) ? -1 : 1) * (1 + (g1 >> 1)) : 0);
    const unsigned sb = (kind == 1 ? OFF_XP : OFF_HS) - 32768 + (wave & 1) * 4096 + lg * 256;
    const unsigned vSrc = sb + g0 * 8192 + (r0 >> 4) * 32768 + (r0 & 15) * 16, vSrc2 = sb + g1 * 8192 + (r1 >> 4) * 32768 + (r1 & 15) * 16;
    const unsigned vOut = (kind == 1 ? OFF_HS : OFF_X1) + (slice * 4 + wave) * 1024 + vA;
    const unsigned vX = AUX + wave * 2048 + (kind == 1 ? l15 * 16 : vA), vX2 = AUX + wave * 2048 + l15 * 8;
    const unsigned vAuxS = kind == 1 ? OFF_S1 + ln * 4 : OFF_XP + (slice * 4 + wave) * 1024 + vA;
    const unsigned vAuxS2 = ln < 32 ? OFF_S1 - 1024 + 256 + ln * 4 : OOB;
    constexpr unsigned RED = AUX + 24576 + 1536;
    const unsigned vOut2 = wave != 2 ? OOB : kind == 1 ? (slice == 0 && ln < 16 ? OFF_S1 + 256 + ln * 8 : OOB) : ln < 32 ? OFF_ST + slice * 128 + ln * 4 : OOB;
    const unsigned vStW = RED + wave * 4 + (kind == 1 ? l15 * 32 + (lg == lq ? 16 : 0) : (l15 * 2 + (lg & 1)) * 16), vStR = RED + (kind == 1 ? l15 * 32 : (ln & 31) * 16);
    const unsigned sWw = (rot * 256 + 16 * wave) * 2048 + slice * 256;
    unsigned epOut;
    asm volatile(R"(
Lcode_g12_b:
s_cmp_lg_u32 %8,0
s_cbranch_scc1 Lg2_begin_%=
s_mov_b64 s[36:37],%1
s_mov_b32 s38,122727552
s_mov_b32 s39,0x00020000
s_mov_b32 s54,%2
s_mov_b32 s55,%3
s_mov_b32 s56,%4
s_mov_b32 s58,%5
s_mov_b32 s83,%6
s_mov_b32 s82,%7
s_mov_b32 s84,%8
v_mov_b32 v0,%9
v_mov_b32 v1,%10
v_mov_b32 v2,%11
v_mov_b32 v3,%12
v_mov_b32 v4,%13
v_mov_b32 v6,%14
v_mov_b32 v7,%15
v_mov_b32 v8,%16
v_mov_b32 v9,%17
v_mov_b32 v10,%18
v_mov_b32 v11,%19
v_mov_b32 v12,%20
v_mov_b32 v13,%21
v_mov_b32 v14,%22
v_mov_b32 v28,%23
v_mov_b32 v29,%24
v_mov_b32 v30,%25
v_mov_b32 v31,%26
v_mov_b32 v32,%27
s_mov_b64 s[40:41],%28
s_mov_b32 s42,2097152
s_mov_b32 s43,0x00020000
s_lshl_b32 s88,s54,11
s_mov_b32 s69,122727424
buffer_load_dword v5,off,s[36:39],s69 sc1
s_add_u32 m0,s88,0
s_add_u32 s61,s82,0
s_and_b32 s61,s61,2097151
buffer_load_dwordx4 v28,s[40:43],s61 offen offset:0  lds
s_add_u32 m0,s88,1024
s_nop 0
buffer_load_dwordx4 v29,s[40:43],s61 offen offset:0  lds
s_add_u32 m0,s88,8064
s_nop 0
buffer_load_dwordx4 v28,s[40:43],s61 offen offset:128  lds
s_add_u32 m0,s88,9088
s_nop 0
buffer_load_dwordx4 v29,s[40:43],s61 offen offset:128  lds
s_add_u32 m0,s88,16384
s_add_u32 s61,s82,131072
s_and_b32 s61,s61,2097151
buffer_load_dwordx4 v28,s[40:43],s61 offen offset:0  lds
s_add_u32 m0,s88,17408
s_nop 0
buffer_load_dwordx4 v29,s[40:43],s61 offen offset:0  lds
s_add_u32 m0,s88,24448
s_nop 0
buffer_load_dwordx4 v28,s[40:43],s61 offen offset:128  lds
s_add_u32 m0,s88,25472
s_nop 0
buffer_load_dwordx4 v29,s[40:43],s61 offen offset:128  lds
s_add_u32 m0,s88,32768
s_add_u32 s61,s82,262144
s_and_b32 s61,s61,2097151
buffer_load_dwordx4 v28,s[40:43],s61 offen offset:0  lds
s_add_u32 m0,s88,33792
s_nop 0
buffer_load_dwordx4 v29,s[40:43],s61 offen offset:0  lds
s_add_u32 m0,s88,40832
s_nop 0
buffer_load_dwordx4 v28,s[40:43],s61 offen offset:128  lds
s_add_u32 m0,s88,41856
s_nop 0
buffer_load_dwordx4 v29,s[40:43],s61 offen offset:128  lds
s_add_u32 m0,s88,49152
s_add_u32 s61,s82,393216
s_and_b32 s61,s61,2097151
buffer_load_dwordx4 v28,s[40:43],s61 offen offset:0  lds
s_add_u32 m0,s88,50176
s_nop 0
buffer_load_dwordx4 v29,s[40:43],s61 offen offset:0  lds
s_add_u32 m0,s88,57216
s_nop 0
buffer_load_dwordx4 v28,s[40:43],s61 offen offset:128  lds
s_add_u32 m0,s88,58240
s_nop 0
buffer_load_dwordx4 v29,s[40:43],s61 offen offset:128  lds
s_add_u32 m0,s88,65536
s_add_u32 s61,s82,524288
s_and_b32 s61,s61,2097151
buffer_load_dwordx4 v28,s[40:43],s61 offen offset:0  lds
s_add_u32 m0,s88,66560
s_nop 0
buffer_load_dwordx4 v29,s[40:43],s61 offen offset:0  lds
s_add_u32 m0,s88,73600
s_nop 0
buffer_load_dwordx4 v28,s[40:43],s61 offen offset:128  lds
s_add_u32 m0,s88,74624
s_nop 0
buffer_load_dwordx4 v29,s[40:43],s61 offen offset:128  lds
s_add_u32 m0,s88,81920
s_add_u32 s61,s82,655360
s_and_b32 s61,s61,2097151
buffer_load_dwordx4 v28,s[40:43],s61 offen offset:0  lds
s_add_u32 m0,s88,82944
s_nop 0
buffer_load_dwordx4 v29,s[40:43],s61 offen offset:0  lds
s_add_u32 m0,s88,89984
s_nop 0
buffer_load_dwordx4 v28,s[40:43],s61 offen offset:128  lds
s_add_u32 m0,s88,91008
s_nop 0
buffer_load_dwordx4 v29,s[40:43],s61 offen offset:128  lds
s_add_u32 m0,s88,98304
s_add_u32 s61,s82,786432
s_and_b32 s61,s61,2097151
buffer_load_dwordx4 v28,s[40:43],s61 offen offset:0  lds
s_add_u32 m0,s88,99328
s_nop 0
buffer_load_dwordx4 v29,s[40:43],s61 offen offset:0  lds
s_add_u32 m0,s88,106368
s_nop 0
buffer_load_dwordx4 v28,s[40:43],s61 offen offset:128  lds
s_add_u32 m0,s88,107392
s_nop 0
buffer_load_dwordx4 v29,s[40:43],s61 offen offset:128  lds
v_add_u32 v36,49152,v30
v_add_u32 v37,49152,v31
v_add_u32 v38,98304,v30
v_add_u32 v39,98304,v31
v_mov_b32 v44,0x3f803f80
v_mov_b32 v45,0x3f803f80
v_mov_b32 v46,0x3f803f80
v_mov_b32 v47,0x3f803f80
s_waitcnt vmcnt(24)
s_barrier
s_add_u32 m0,s88,114688
s_add_u32 s61,s82,917504
s_and_b32 s61,s61,2097151
buffer_load_dwordx4 v28,s[40:43],s61 offen offset:0  lds
s_add_u32 m0,s88,115712
ds_read_b64_tr_b16 a[0:1],v30 offset:0
buffer_load_dwordx4 v29,s[40:43],s61 offen offset:0  lds
s_add_u32 m0,s88,122752
ds_read_b64_tr_b16 a[2:3],v30 offset:512
buffer_load_dwordx4 v28,s[40:43],s61 offen offset:128  lds
s_add_u32 m0,s88,123776
ds_read_b64_tr_b16 a[4:5],v31 offset:0
buffer_load_dwordx4 v29,s[40:43],s61 offen offset:128  lds
ds_read_b64_tr_b16 a[6:7],v31 offset:512
ds_read_b64_tr_b16 a[8:9],v30 offset:4096
ds_read_b64_tr_b16 a[10:11],v30 offset:4608
ds_read_b64_tr_b16 a[12:13],v31 offset:4096
ds_read_b64_tr_b16 a[14:15],v31 offset:4608
s_waitcnt vmcnt(24)
s_barrier
s_add_u32 m0,s88,131072
s_add_u32 s61,s82,1048576
s_and_b32 s61,s61,2097151
buffer_load_dwordx4 v28,s[40:43],s61 offen offset:0  lds
s_add_u32 m0,s88,132096
ds_read_b64_tr_b16 a[16:17],v30 offset:16384
buffer_load_dwordx4 v29,s[40:43],s61 offen offset:0  lds
s_add_u32 m0,s88,139136
ds_read_b64_tr_b16 a[18:19],v30 offset:16896
buffer_load_dwordx4 v28,s[40:43],s61 offen offset:128  lds
s_add_u32 m0,s88,140160
ds_read_b64_tr_b16 a[20:21],v31 offset:16384
buffer_load_dwordx4 v29,s[40:43],s61 offen offset:128  lds
ds_read_b64_tr_b16 a[22:23],v31 offset:16896
ds_read_b64_tr_b16 a[24:25],v30 offset:20480
ds_read_b64_tr_b16 a[26:27],v30 offset:20992
ds_read_b64_tr_b16 a[28:29],v31 offset:20480
ds_read_b64_tr_b16 a[30:31],v31 offset:20992
s_waitcnt lgkmcnt(8)
v_mfma_f32_16x16x32_bf16 v[20:23],a[0:3],v[44:47],0
v_mfma_f32_16x16x32_bf16 v[24:27],a[4:7],v[44:47],0
v_mfma_f32_16x16x32_bf16 v[20:23],a[8:11],v[44:47],v[20:23]
v_mfma_f32_16x16x32_bf16 v[24:27],a[12:15],v[44:47],v[24:27]
s_waitcnt vmcnt(24)
s_barrier
s_add_u32 m0,s88,0
s_add_u32 s61,s82,1179648
s_and_b32 s61,s61,2097151
buffer_load_dwordx4 v28,s[40:43],s61 offen offset:0  lds
s_add_u32 m0,s88,1024
ds_read_b64_tr_b16 a[32:33],v30 offset:32768
buffer_load_dwordx4 v29,s[40:43],s61 offen offset:0  lds
s_add_u32 m0,s88,8064
ds_read_b64_tr_b16 a[34:35],v30 offset:33280
buffer_load_dwordx4 v28,s[40:43],s61 offen offset:128  lds
s_add_u32 m0,s88,9088
ds_read_b64_tr_b16 a[36:37],v31 offset:32768
buffer_load_dwordx4 v29,s[40:43],s61 offen offset:128  lds
ds_read_b64_tr_b16 a[38:39],v31 offset:33280
ds_read_b64_tr_b16 a[40:41],v30 offset:36864
ds_read_b64_tr_b16 a[42:43],v30 offset:37376)"
R"(
ds_read_b64_tr_b16 a[44:45],v31 offset:36864
ds_read_b64_tr_b16 a[46:47],v31 offset:37376
s_waitcnt lgkmcnt(8)
v_mfma_f32_16x16x32_bf16 v[20:23],a[16:19],v[44:47],v[20:23]
v_mfma_f32_16x16x32_bf16 v[24:27],a[20:23],v[44:47],v[24:27]
v_mfma_f32_16x16x32_bf16 v[20:23],a[24:27],v[44:47],v[20:23]
v_mfma_f32_16x16x32_bf16 v[24:27],a[28:31],v[44:47],v[24:27]
s_waitcnt vmcnt(24)
s_barrier
s_add_u32 m0,s88,16384
s_add_u32 s61,s82,1310720
s_and_b32 s61,s61,2097151
buffer_load_dwordx4 v28,s[40:43],s61 offen offset:0  lds
s_add_u32 m0,s88,17408
ds_read_b64_tr_b16 a[48:49],v36 offset:0
buffer_load_dwordx4 v29,s[40:43],s61 offen offset:0  lds
s_add_u32 m0,s88,24448
ds_read_b64_tr_b16 a[50:51],v36 offset:512
buffer_load_dwordx4 v28,s[40:43],s61 offen offset:128  lds
s_add_u32 m0,s88,25472
ds_read_b64_tr_b16 a[52:53],v37 offset:0
buffer_load_dwordx4 v29,s[40:43],s61 offen offset:128  lds
ds_read_b64_tr_b16 a[54:55],v37 offset:512
ds_read_b64_tr_b16 a[56:57],v36 offset:4096
ds_read_b64_tr_b16 a[58:59],v36 offset:4608
ds_read_b64_tr_b16 a[60:61],v37 offset:4096
ds_read_b64_tr_b16 a[62:63],v37 offset:4608
s_waitcnt lgkmcnt(8)
v_mfma_f32_16x16x32_bf16 v[20:23],a[32:35],v[44:47],v[20:23]
v_mfma_f32_16x16x32_bf16 v[24:27],a[36:39],v[44:47],v[24:27]
v_mfma_f32_16x16x32_bf16 v[20:23],a[40:43],v[44:47],v[20:23]
v_mfma_f32_16x16x32_bf16 v[24:27],a[44:47],v[44:47],v[24:27]
s_waitcnt vmcnt(24)
s_barrier
s_add_u32 m0,s88,32768
s_add_u32 s61,s82,1441792
s_and_b32 s61,s61,2097151
buffer_load_dwordx4 v28,s[40:43],s61 offen offset:0  lds
s_add_u32 m0,s88,33792
ds_read_b64_tr_b16 a[64:65],v36 offset:16384
buffer_load_dwordx4 v29,s[40:43],s61 offen offset:0  lds
s_add_u32 m0,s88,40832
ds_read_b64_tr_b16 a[66:67],v36 offset:16896
buffer_load_dwordx4 v28,s[40:43],s61 offen offset:128  lds
s_add_u32 m0,s88,41856
ds_read_b64_tr_b16 a[68:69],v37 offset:16384
buffer_load_dwordx4 v29,s[40:43],s61 offen offset:128  lds
ds_read_b64_tr_b16 a[70:71],v37 offset:16896
ds_read_b64_tr_b16 a[72:73],v36 offset:20480
ds_read_b64_tr_b16 a[74:75],v36 offset:20992
ds_read_b64_tr_b16 a[76:77],v37 offset:20480
ds_read_b64_tr_b16 a[78:79],v37 offset:20992
s_waitcnt lgkmcnt(8)
v_mfma_f32_16x16x32_bf16 v[20:23],a[48:51],v[44:47],v[20:23]
v_mfma_f32_16x16x32_bf16 v[24:27],a[52:55],v[44:47],v[24:27]
v_mfma_f32_16x16x32_bf16 v[20:23],a[56:59],v[44:47],v[20:23]
v_mfma_f32_16x16x32_bf16 v[24:27],a[60:63],v[44:47],v[24:27]
s_waitcnt vmcnt(24)
s_barrier
s_add_u32 m0,s88,49152
s_add_u32 s61,s82,1572864
s_and_b32 s61,s61,2097151
buffer_load_dwordx4 v28,s[40:43],s61 offen offset:0  lds
s_add_u32 m0,s88,50176
ds_read_b64_tr_b16 a[80:81],v36 offset:32768
buffer_load_dwordx4 v29,s[40:43],s61 offen offset:0  lds
s_add_u32 m0,s88,57216
ds_read_b64_tr_b16 a[82:83],v36 offset:33280
buffer_load_dwordx4 v28,s[40:43],s61 offen offset:128  lds
s_add_u32 m0,s88,58240
ds_read_b64_tr_b16 a[84:85],v37 offset:32768
buffer_load_dwordx4 v29,s[40:43],s61 offen offset:128  lds
ds_read_b64_tr_b16 a[86:87],v37 offset:33280
ds_read_b64_tr_b16 a[88:89],v36 offset:36864
ds_read_b64_tr_b16 a[90:91],v36 offset:37376
ds_read_b64_tr_b16 a[92:93],v37 offset:36864
ds_read_b64_tr_b16 a[94:95],v37 offset:37376
s_waitcnt lgkmcnt(8)
v_mfma_f32_16x16x32_bf16 v[20:23],a[64:67],v[44:47],v[20:23]
v_mfma_f32_16x16x32_bf16 v[24:27],a[68:71],v[44:47],v[24:27]
v_mfma_f32_16x16x32_bf16 v[20:23],a[72:75],v[44:47],v[20:23]
v_mfma_f32_16x16x32_bf16 v[24:27],a[76:79],v[44:47],v[24:27]
s_waitcnt vmcnt(24)
s_barrier
s_add_u32 m0,s88,65536
s_add_u32 s61,s82,1703936
s_and_b32 s61,s61,2097151
buffer_load_dwordx4 v28,s[40:43],s61 offen offset:0  lds
s_add_u32 m0,s88,66560
ds_read_b64_tr_b16 a[96:97],v38 offset:0
buffer_load_dwordx4 v29,s[40:43],s61 offen offset:0  lds
s_add_u32 m0,s88,73600
ds_read_b64_tr_b16 a[98:99],v38 offset:512
buffer_load_dwordx4 v28,s[40:43],s61 offen offset:128  lds
s_add_u32 m0,s88,74624
ds_read_b64_tr_b16 a[100:101],v39 offset:0
buffer_load_dwordx4 v29,s[40:43],s61 offen offset:128  lds
ds_read_b64_tr_b16 a[102:103],v39 offset:512
ds_read_b64_tr_b16 a[104:105],v38 offset:4096
ds_read_b64_tr_b16 a[106:107],v38 offset:4608
ds_read_b64_tr_b16 a[108:109],v39 offset:4096
ds_read_b64_tr_b16 a[110:111],v39 offset:4608
s_waitcnt lgkmcnt(8)
v_mfma_f32_16x16x32_bf16 v[20:23],a[80:83],v[44:47],v[20:23]
v_mfma_f32_16x16x32_bf16 v[24:27],a[84:87],v[44:47],v[24:27]
v_mfma_f32_16x16x32_bf16 v[20:23],a[88:91],v[44:47],v[20:23]
v_mfma_f32_16x16x32_bf16 v[24:27],a[92:95],v[44:47],v[24:27]
s_waitcnt vmcnt(24)
s_barrier
s_add_u32 m0,s88,81920
s_add_u32 s61,s82,1835008
s_and_b32 s61,s61,2097151
buffer_load_dwordx4 v28,s[40:43],s61 offen offset:0  lds
s_add_u32 m0,s88,82944
ds_read_b64_tr_b16 a[112:113],v38 offset:16384
buffer_load_dwordx4 v29,s[40:43],s61 offen offset:0  lds
s_add_u32 m0,s88,89984
ds_read_b64_tr_b16 a[114:115],v38 offset:16896
buffer_load_dwordx4 v28,s[40:43],s61 offen offset:128  lds
s_add_u32 m0,s88,91008
ds_read_b64_tr_b16 a[116:117],v39 offset:16384
buffer_load_dwordx4 v29,s[40:43],s61 offen offset:128  lds
ds_read_b64_tr_b16 a[118:119],v39 offset:16896
ds_read_b64_tr_b16 a[120:121],v38 offset:20480
ds_read_b64_tr_b16 a[122:123],v38 offset:20992
ds_read_b64_tr_b16 a[124:125],v39 offset:20480
ds_read_b64_tr_b16 a[126:127],v39 offset:20992
s_waitcnt lgkmcnt(8)
v_mfma_f32_16x16x32_bf16 v[20:23],a[96:99],v[44:47],v[20:23]
v_mfma_f32_16x16x32_bf16 v[24:27],a[100:103],v[44:47],v[24:27]
v_mfma_f32_16x16x32_bf16 v[20:23],a[104:107],v[44:47],v[20:23]
v_mfma_f32_16x16x32_bf16 v[24:27],a[108:111],v[44:47],v[24:27]
s_waitcnt vmcnt(24)
s_barrier
s_add_u32 m0,s88,98304
s_add_u32 s61,s82,1966080
s_and_b32 s61,s61,2097151
buffer_load_dwordx4 v28,s[40:43],s61 offen offset:0  lds
s_add_u32 m0,s88,99328
ds_read_b64_tr_b16 a[128:129],v38 offset:32768
buffer_load_dwordx4 v29,s[40:43],s61 offen offset:0  lds
s_add_u32 m0,s88,106368
ds_read_b64_tr_b16 a[130:131],v38 offset:33280
buffer_load_dwordx4 v28,s[40:43],s61 offen offset:128  lds
s_add_u32 m0,s88,107392
ds_read_b64_tr_b16 a[132:133],v39 offset:32768
buffer_load_dwordx4 v29,s[40:43],s61 offen offset:128  lds
ds_read_b64_tr_b16 a[134:135],v39 offset:33280
ds_read_b64_tr_b16 a[136:137],v38 offset:36864
ds_read_b64_tr_b16 a[138:139],v38 offset:37376
ds_read_b64_tr_b16 a[140:141],v39 offset:36864
ds_read_b64_tr_b16 a[142:143],v39 offset:37376
s_waitcnt lgkmcnt(8)
v_mfma_f32_16x16x32_bf16 v[20:23],a[112:115],v[44:47],v[20:23]
v_mfma_f32_16x16x32_bf16 v[24:27],a[116:119],v[44:47],v[24:27]
v_mfma_f32_16x16x32_bf16 v[20:23],a[120:123],v[44:47],v[20:23]
v_mfma_f32_16x16x32_bf16 v[24:27],a[124:127],v[44:47],v[24:27]
s_waitcnt vmcnt(24)
s_barrier
ds_read_b64_tr_b16 a[144:145],v30 offset:0
ds_read_b64_tr_b16 a[146:147],v30 offset:512
ds_read_b64_tr_b16 a[148:149],v31 offset:0
ds_read_b64_tr_b16 a[150:151],v31 offset:512
ds_read_b64_tr_b16 a[152:153],v30 offset:4096
ds_read_b64_tr_b16 a[154:155],v30 offset:4608
ds_read_b64_tr_b16 a[156:157],v31 offset:4096
ds_read_b64_tr_b16 a[158:159],v31 offset:4608
s_waitcnt lgkmcnt(8)
v_mfma_f32_16x16x32_bf16 v[20:23],a[128:131],v[44:47],v[20:23]
v_mfma_f32_16x16x32_bf16 v[24:27],a[132:135],v[44:47],v[24:27]
v_mfma_f32_16x16x32_bf16 v[20:23],a[136:139],v[44:47],v[20:23]
v_mfma_f32_16x16x32_bf16 v[24:27],a[140:143],v[44:47],v[24:27]
s_waitcnt vmcnt(20)
s_barrier
ds_read_b64_tr_b16 a[160:161],v30 offset:16384
ds_read_b64_tr_b16 a[162:163],v30 offset:16896
ds_read_b64_tr_b16 a[164:165],v31 offset:16384
ds_read_b64_tr_b16 a[166:167],v31 offset:16896
ds_read_b64_tr_b16 a[168:169],v30 offset:20480
ds_read_b64_tr_b16 a[170:171],v30 offset:20992
ds_read_b64_tr_b16 a[172:173],v31 offset:20480
ds_read_b64_tr_b16 a[174:175],v31 offset:20992
s_waitcnt lgkmcnt(8)
v_mfma_f32_16x16x32_bf16 v[20:23],a[144:147],v[44:47],v[20:23]
v_mfma_f32_16x16x32_bf16 v[24:27],a[148:151],v[44:47],v[24:27]
v_mfma_f32_16x16x32_bf16 v[20:23],a[152:155],v[44:47],v[20:23]
v_mfma_f32_16x16x32_bf16 v[24:27],a[156:159],v[44:47],v[24:27]
s_waitcnt vmcnt(16)
s_barrier
ds_read_b64_tr_b16 a[176:177],v30 offset:32768
ds_read_b64_tr_b16 a[178:179],v30 offset:33280
ds_read_b64_tr_b16 a[180:181],v31 offset:32768
ds_read_b64_tr_b16 a[182:183],v31 offset:33280
ds_read_b64_tr_b16 a[184:185],v30 offset:36864
ds_read_b64_tr_b16 a[186:187],v30 offset:37376
ds_read_b64_tr_b16 a[188:189],v31 offset:36864
ds_read_b64_tr_b16 a[190:191],v31 offset:37376
s_getpc_b64 s[46:47]
Lg1_cp_%=:
s_add_u32 s46,s46,Lg1_init_%=-Lg1_cp_%=)"
R"(
s_addc_u32 s47,s47,0
s_lshl_b32 s68,s54,13
v_lshl_add_u32 v144,v2,3,s68
v_min_u32 v145,Lg1_done_%=-Lg1_init_%=,v144
global_load_dword v148,v145,s[46:47]
v_add_u32 v144,32768,v144
v_min_u32 v146,Lg1_done_%=-Lg1_init_%=,v144
global_load_dword v149,v146,s[46:47]
s_waitcnt lgkmcnt(8)
v_mfma_f32_16x16x32_bf16 v[20:23],a[160:163],v[44:47],v[20:23]
v_mfma_f32_16x16x32_bf16 v[24:27],a[164:167],v[44:47],v[24:27]
v_mfma_f32_16x16x32_bf16 v[20:23],a[168:171],v[44:47],v[20:23]
v_mfma_f32_16x16x32_bf16 v[24:27],a[172:175],v[44:47],v[24:27]
s_waitcnt vmcnt(14)
s_barrier
ds_read_b64_tr_b16 a[192:193],v36 offset:0
ds_read_b64_tr_b16 a[194:195],v36 offset:512
ds_read_b64_tr_b16 a[196:197],v37 offset:0
ds_read_b64_tr_b16 a[198:199],v37 offset:512
ds_read_b64_tr_b16 a[200:201],v36 offset:4096
ds_read_b64_tr_b16 a[202:203],v36 offset:4608
ds_read_b64_tr_b16 a[204:205],v37 offset:4096
ds_read_b64_tr_b16 a[206:207],v37 offset:4608
s_waitcnt lgkmcnt(8)
v_mfma_f32_16x16x32_bf16 v[20:23],a[176:179],v[44:47],v[20:23]
v_mfma_f32_16x16x32_bf16 v[24:27],a[180:183],v[44:47],v[24:27]
v_mfma_f32_16x16x32_bf16 v[20:23],a[184:187],v[44:47],v[20:23]
v_mfma_f32_16x16x32_bf16 v[24:27],a[188:191],v[44:47],v[24:27]
s_waitcnt vmcnt(10)
s_barrier
ds_read_b64_tr_b16 a[208:209],v36 offset:16384
ds_read_b64_tr_b16 a[210:211],v36 offset:16896
ds_read_b64_tr_b16 a[212:213],v37 offset:16384
ds_read_b64_tr_b16 a[214:215],v37 offset:16896
ds_read_b64_tr_b16 a[216:217],v36 offset:20480
ds_read_b64_tr_b16 a[218:219],v36 offset:20992
ds_read_b64_tr_b16 a[220:221],v37 offset:20480
ds_read_b64_tr_b16 a[222:223],v37 offset:20992
s_waitcnt lgkmcnt(8)
v_mfma_f32_16x16x32_bf16 v[20:23],a[192:195],v[44:47],v[20:23]
v_mfma_f32_16x16x32_bf16 v[24:27],a[196:199],v[44:47],v[24:27]
v_mfma_f32_16x16x32_bf16 v[20:23],a[200:203],v[44:47],v[20:23]
v_mfma_f32_16x16x32_bf16 v[24:27],a[204:207],v[44:47],v[24:27]
s_waitcnt vmcnt(6)
s_barrier
ds_read_b64_tr_b16 a[224:225],v36 offset:32768
ds_read_b64_tr_b16 a[226:227],v36 offset:33280
ds_read_b64_tr_b16 a[228:229],v37 offset:32768
ds_read_b64_tr_b16 a[230:231],v37 offset:33280
ds_read_b64_tr_b16 a[232:233],v36 offset:36864
ds_read_b64_tr_b16 a[234:235],v36 offset:37376
ds_read_b64_tr_b16 a[236:237],v37 offset:36864
ds_read_b64_tr_b16 a[238:239],v37 offset:37376
s_waitcnt lgkmcnt(8)
v_mfma_f32_16x16x32_bf16 v[20:23],a[208:211],v[44:47],v[20:23]
v_mfma_f32_16x16x32_bf16 v[24:27],a[212:215],v[44:47],v[24:27]
v_mfma_f32_16x16x32_bf16 v[20:23],a[216:219],v[44:47],v[20:23]
v_mfma_f32_16x16x32_bf16 v[24:27],a[220:223],v[44:47],v[24:27]
s_waitcnt vmcnt(2)
s_barrier
ds_read_b64_tr_b16 a[240:241],v38 offset:0
ds_read_b64_tr_b16 a[242:243],v38 offset:512
ds_read_b64_tr_b16 a[244:245],v39 offset:0
ds_read_b64_tr_b16 a[246:247],v39 offset:512
ds_read_b64_tr_b16 a[248:249],v38 offset:4096
ds_read_b64_tr_b16 a[250:251],v38 offset:4608
ds_read_b64_tr_b16 a[252:253],v39 offset:4096
ds_read_b64_tr_b16 a[254:255],v39 offset:4608
s_waitcnt lgkmcnt(8)
v_mfma_f32_16x16x32_bf16 v[20:23],a[224:227],v[44:47],v[20:23]
v_mfma_f32_16x16x32_bf16 v[24:27],a[228:231],v[44:47],v[24:27]
v_mfma_f32_16x16x32_bf16 v[20:23],a[232:235],v[44:47],v[20:23]
v_mfma_f32_16x16x32_bf16 v[24:27],a[236:239],v[44:47],v[24:27]
s_waitcnt lgkmcnt(0)
v_mfma_f32_16x16x32_bf16 v[20:23],a[240:243],v[44:47],v[20:23]
v_mfma_f32_16x16x32_bf16 v[24:27],a[244:247],v[44:47],v[24:27]
v_mfma_f32_16x16x32_bf16 v[20:23],a[248:251],v[44:47],v[20:23]
v_mfma_f32_16x16x32_bf16 v[24:27],a[252:255],v[44:47],v[24:27]
s_waitcnt lgkmcnt(0)
s_barrier
v_readfirstlane_b32 s60,v5
s_nop 3
s_add_u32 s60,s60,1
s_cmp_eq_u32 s60,0
s_cselect_b32 s60,1,s60
Lg1_init_%=:
v_mov_b32 v5,s60
s_mov_b32 s59,536870912
s_add_u32 s57,s56,131072
s_mov_b32 s79,0
s_mov_b32 s75,0x3a800000
s_mov_b32 s76,0xc0135761
s_mov_b32 s77,0x3d372713
s_mov_b32 s44,s59
s_mov_b32 s45,s59
s_mov_b32 s46,s59
s_mov_b32 s47,s59
s_mov_b32 s48,s83
s_add_u32 s49,s48,s55
s_cmp_eq_u32 s49,s56
s_cselect_b32 s49,s57,s49
s_cmp_eq_u32 s49,s58
s_cselect_b32 s49,s59,s49
s_add_u32 s50,s49,s55
s_cmp_eq_u32 s50,s56
s_cselect_b32 s50,s57,s50
s_cmp_eq_u32 s50,s58
s_cselect_b32 s50,s59,s50
s_add_u32 s51,s50,s55
s_cmp_eq_u32 s51,s56
s_cselect_b32 s51,s57,s51
s_cmp_eq_u32 s51,s58
s_cselect_b32 s51,s59,s51
s_add_u32 s52,s51,s55
s_cmp_eq_u32 s52,s56
s_cselect_b32 s52,s57,s52
s_cmp_eq_u32 s52,s58
s_cselect_b32 s52,s59,s52
s_lshl_b32 s63,s54,12
s_add_u32 s63,s63,65536
s_add_u32 s64,s63,32768
s_lshl_b32 s88,s54,10
s_add_u32 s88,s88,131072
s_lshl_b32 s78,s54,11
s_add_u32 s78,s78,132608
v_mov_b32 v15,0x3727c5ac
v_mov_b32 v68,0x3f803f80
v_mov_b32 v69,0x3f803f80
v_mov_b32 v70,0x3f803f80
v_mov_b32 v71,0x3f803f80
s_mov_b32 s89,1048576
s_mov_b32 s82,0xaaaaaaaa
s_mov_b32 s83,0xaaaaaaaa
s_mov_b32 s84,0xcccccccc
s_mov_b32 s85,0xcccccccc
s_mov_b32 s98,0x00f0000f
s_mov_b32 s99,0xf0000f00
s_getpc_b64 s[90:91]
Lg1_pc_%=:
s_mov_b32 s68,Lg1_d1w0_%=-Lg1_pc_%=
s_cmp_eq_u32 s54,1
s_cselect_b32 s68,Lg1_d1w1_%=-Lg1_pc_%=,s68
s_cmp_eq_u32 s54,2
s_cselect_b32 s68,Lg1_d1w2_%=-Lg1_pc_%=,s68
s_cmp_eq_u32 s54,3
s_cselect_b32 s68,Lg1_d1w3_%=-Lg1_pc_%=,s68
s_add_u32 s96,s90,s68
s_addc_u32 s97,s91,0
s_mov_b32 s68,Lg1_d0w0_%=-Lg1_pc_%=
s_cmp_eq_u32 s54,1
s_cselect_b32 s68,Lg1_d0w1_%=-Lg1_pc_%=,s68
s_cmp_eq_u32 s54,2
s_cselect_b32 s68,Lg1_d0w2_%=-Lg1_pc_%=,s68
s_cmp_eq_u32 s54,3
s_cselect_b32 s68,Lg1_d0w3_%=-Lg1_pc_%=,s68
s_add_u32 s94,s90,s68
s_addc_u32 s95,s91,0
s_mov_b32 s68,Lg1_a1w0_%=-Lg1_pc_%=
s_cmp_eq_u32 s54,1
s_cselect_b32 s68,Lg1_a1w1_%=-Lg1_pc_%=,s68
s_cmp_eq_u32 s54,2
s_cselect_b32 s68,Lg1_a1w2_%=-Lg1_pc_%=,s68
s_cmp_eq_u32 s54,3
s_cselect_b32 s68,Lg1_a1w3_%=-Lg1_pc_%=,s68
s_add_u32 s92,s90,s68
s_addc_u32 s93,s91,0
s_mov_b32 s68,Lg1_a0w0_%=-Lg1_pc_%=
s_cmp_eq_u32 s54,1
s_cselect_b32 s68,Lg1_a0w1_%=-Lg1_pc_%=,s68
s_cmp_eq_u32 s54,2
s_cselect_b32 s68,Lg1_a0w2_%=-Lg1_pc_%=,s68
s_cmp_eq_u32 s54,3
s_cselect_b32 s68,Lg1_a0w3_%=-Lg1_pc_%=,s68
s_add_u32 s90,s90,s68
s_addc_u32 s91,s91,0
s_mov_b32 s66,s59
s_mov_b32 s65,0
s_mov_b32 s62,s63
s_branch Lg1_slow0_%=
Lg1_a0w0_%=:
v_mfma_f32_16x16x32_bf16 v[32:35],a[4:7],v[128:131],0
s_add_u32 m0,s62,16384
ds_read_b128 v[156:159],v2 offset:7168
s_sub_u32 s69,s66,1048576
v_mfma_f32_16x16x32_bf16 v[32:35],a[12:15],v[132:135],v[32:35]
buffer_load_dwordx4 v1,s[36:39],s66 offen offset:0 sc1 lds
v_mfma_f32_16x16x32_bf16 v[28:31],a[8:11],v[132:135],0
s_cmp_lt_u32 s69,2097152
ds_read_b128 v[160:163],v2 offset:8192
s_cselect_b32 s89,3145728,s89
v_mfma_f32_16x16x32_bf16 v[28:31],a[0:3],v[128:131],v[28:31]
s_sub_u32 s69,s66,3145728
v_fma_f32 v36,-v64,v20,v36
v_fma_f32 v37,-v64,v21,v37
v_mfma_f32_16x16x32_bf16 v[28:31],a[16:19],v[136:139],v[28:31]
s_cmp_lt_u32 s69,1048576
ds_read_b128 v[164:167],v2 offset:9216
v_fma_f32 v38,-v64,v22,v38
v_mfma_f32_16x16x32_bf16 v[28:31],a[24:27],v[140:143],v[28:31]
buffer_load_dwordx4 v1,s[36:39],s66 offen offset:1024 sc1 lds)"
R"(
v_mfma_f32_16x16x32_bf16 v[32:35],a[28:31],v[140:143],v[32:35]
s_cselect_b32 s89,4194304,s89
ds_read_b128 v[168:171],v2 offset:10240
v_fma_f32 v39,-v64,v23,v39
v_mfma_f32_16x16x32_bf16 v[32:35],a[20:23],v[136:139],v[32:35]
s_lshr_b32 s73,s47,0
v_mul_f32 v44,v36,v36
v_mul_f32 v45,v37,v37
v_mfma_f32_16x16x32_bf16 v[72:75],v[68:71],v[128:131],0
s_lshr_b32 s72,s66,6
v_mul_f32 v46,v38,v38
v_mul_f32 v47,v39,v39
v_mfma_f32_16x16x32_bf16 v[72:75],v[68:71],v[132:135],v[72:75]
buffer_load_dwordx4 v1,s[36:39],s66 offen offset:2048 sc1 lds
v_mfma_f32_16x16x32_bf16 v[72:75],v[68:71],v[136:139],v[72:75]
s_lshr_b32 s71,s62,2
v_fma_f32 v44,v66,v44,v65
v_mul_f32 v44,v36,v44
v_mfma_f32_16x16x32_bf16 v[72:75],v[68:71],v[140:143],v[72:75]
s_cmp_lt_u32 s66,4194304
v_exp_f32 v44,v44
v_mfma_f32_16x16x32_bf16 v[76:79],v[140:143],v[140:143],0
s_cselect_b32 s72,s59,s72
v_fma_f32 v44,v44,v67,v67
v_fma_f32 v45,v66,v45,v65
v_mfma_f32_16x16x32_bf16 v[76:79],v[136:139],v[136:139],v[76:79]
buffer_load_dwordx4 v1,s[36:39],s66 offen offset:3072 sc1 lds
v_mfma_f32_16x16x32_bf16 v[76:79],v[132:135],v[132:135],v[76:79]
s_add_u32 m0,s71,s88
v_rcp_f32 v44,v44
v_mfma_f32_16x16x32_bf16 v[76:79],v[128:131],v[128:131],v[76:79]
s_waitcnt lgkmcnt(3)
buffer_load_dword v10,s[36:39],s72 offen offset:0 sc1 lds
v_mfma_f32_16x16x32_bf16 v[32:35],a[36:39],v[144:147],v[32:35]
s_xor_b32 s87,s64,65536
ds_read_b128 v[172:175],v2 offset:11264
v_mul_f32 v45,v37,v45
v_mfma_f32_16x16x32_bf16 v[32:35],a[44:47],v[148:151],v[32:35]
s_cmp_ge_u32 s66,s59
v_exp_f32 v45,v45
v_mfma_f32_16x16x32_bf16 v[28:31],a[40:43],v[148:151],v[28:31]
s_cselect_b32 s86,s49,s50
ds_read_b128 v[176:179],v2 offset:12288
v_fma_f32 v45,v45,v67,v67
v_mfma_f32_16x16x32_bf16 v[28:31],a[32:35],v[144:147],v[28:31]
s_cselect_b32 s87,s87,s63
v_rcp_f32 v45,v45
v_mfma_f32_16x16x32_bf16 v[28:31],a[48:51],v[152:155],v[28:31]
s_mov_b32 s62,s87
ds_read_b128 v[180:183],v2 offset:13312
v_fma_f32 v46,v66,v46,v65
v_mfma_f32_16x16x32_bf16 v[28:31],a[56:59],v[156:159],v[28:31]
s_cmp_lt_u32 s86,s89
v_mul_f32 v46,v38,v46
v_fma_f32 v47,v66,v47,v65
v_mfma_f32_16x16x32_bf16 v[32:35],a[60:63],v[156:159],v[32:35]
ds_read_b128 v[184:187],v2 offset:14336
v_mul_f32 v47,v39,v47
v_mfma_f32_16x16x32_bf16 v[32:35],a[52:55],v[152:155],v[32:35]
s_waitcnt lgkmcnt(3)
v_exp_f32 v46,v46
v_mfma_f32_16x16x32_bf16 v[32:35],a[68:71],v[160:163],v[32:35]
s_waitcnt vmcnt(10)
ds_read_b128 v[188:191],v2 offset:15360
v_fma_f32 v46,v46,v67,v67
v_mfma_f32_16x16x32_bf16 v[32:35],a[76:79],v[164:167],v[32:35]
s_barrier
v_mfma_f32_16x16x32_bf16 v[28:31],a[72:75],v[164:167],v[28:31]
ds_read_b128 v[128:131],v2 offset:16384
ds_read_b32 v16,v7 offset:0
v_mfma_f32_16x16x32_bf16 v[28:31],a[64:67],v[160:163],v[28:31]
v_exp_f32 v47,v47
v_mfma_f32_16x16x32_bf16 v[28:31],a[80:83],v[168:171],v[28:31]
ds_read_b128 v[132:135],v2 offset:17408
v_fma_f32 v47,v47,v67,v67
v_mfma_f32_16x16x32_bf16 v[28:31],a[88:91],v[172:175],v[28:31]
v_rcp_f32 v46,v46
v_mfma_f32_16x16x32_bf16 v[32:35],a[92:95],v[172:175],v[32:35]
s_waitcnt lgkmcnt(1)
ds_read_b128 v[136:139],v2 offset:18432
v_mul_f32 v36,v36,v44
v_mfma_f32_16x16x32_bf16 v[32:35],a[84:87],v[168:171],v[32:35]
v_rcp_f32 v47,v47
v_mfma_f32_16x16x32_bf16 v[32:35],a[100:103],v[176:179],v[32:35]
ds_read_b128 v[140:143],v2 offset:19456
v_mul_f32 v37,v37,v45
v_mfma_f32_16x16x32_bf16 v[32:35],a[108:111],v[180:183],v[32:35]
v_cmp_eq_u32 vcc,s60,v16
v_mul_f32 v38,v38,v46
v_mul_f32 v39,v39,v47
v_cvt_pk_bf16_f32 v52,v36,v37
v_mfma_f32_16x16x32_bf16 v[28:31],a[104:107],v[180:183],v[28:31]
s_cselect_b32 vcc_lo,-1,vcc_lo
ds_read_b128 v[144:147],v2 offset:20480
v_cvt_pk_bf16_f32 v53,v38,v39
v_mfma_f32_16x16x32_bf16 v[28:31],a[96:99],v[176:179],v[28:31]
s_cmp_eq_u32 vcc_lo,-1
v_fma_f32 v40,-v64,v24,v40
v_fma_f32 v41,-v64,v25,v41
v_mfma_f32_16x16x32_bf16 v[28:31],a[112:115],v[184:187],v[28:31]
s_cselect_b32 s67,s86,s59
ds_read_b128 v[148:151],v2 offset:21504
v_fma_f32 v42,-v64,v26,v42
v_mfma_f32_16x16x32_bf16 v[28:31],a[120:123],v[188:191],v[28:31]
s_lshr_b32 s69,s50,5
v_fma_f32 v43,-v64,v27,v43
v_mul_f32 v48,v40,v40
v_mfma_f32_16x16x32_bf16 v[32:35],a[124:127],v[188:191],v[32:35]
s_cmp_lt_u32 s50,4194304
ds_read_b128 v[152:155],v2 offset:22528
v_mul_f32 v49,v41,v41
v_mfma_f32_16x16x32_bf16 v[32:35],a[116:119],v[184:187],v[32:35]
s_waitcnt lgkmcnt(3)
v_mul_f32 v50,v42,v42
v_mul_f32 v51,v43,v43
v_mfma_f32_16x16x32_bf16 v[32:35],a[132:135],v[128:131],v[32:35]
s_cselect_b32 s68,0xffffcfff,-1
ds_read_b128 v[156:159],v2 offset:23552
v_fma_f32 v48,v66,v48,v65
v_mfma_f32_16x16x32_bf16 v[32:35],a[140:143],v[132:135],v[32:35]
s_and_b32 s68,s68,s69
v_mul_f32 v48,v40,v48
v_fma_f32 v49,v66,v49,v65
v_mfma_f32_16x16x32_bf16 v[28:31],a[136:139],v[132:135],v[28:31]
s_cmp_lt_u32 s50,s89
ds_read_b128 v[160:163],v2 offset:24576
v_mul_f32 v49,v41,v49
v_mfma_f32_16x16x32_bf16 v[28:31],a[128:131],v[128:131],v[28:31]
s_cselect_b32 s68,s59,s68
v_exp_f32 v48,v48
v_mfma_f32_16x16x32_bf16 v[28:31],a[144:147],v[136:139],v[28:31]
s_lshr_b32 s69,s51,5
ds_read_b128 v[164:167],v2 offset:25600
v_fma_f32 v48,v48,v67,v67
v_mfma_f32_16x16x32_bf16 v[28:31],a[152:155],v[140:143],v[28:31]
s_cmp_lt_u32 s51,4194304
v_exp_f32 v49,v49
v_mfma_f32_16x16x32_bf16 v[32:35],a[156:159],v[140:143],v[32:35]
s_cselect_b32 s70,0xffffcfff,-1
ds_read_b128 v[168:171],v2 offset:26624
v_fma_f32 v49,v49,v67,v67
v_mfma_f32_16x16x32_bf16 v[32:35],a[148:151],v[136:139],v[32:35]
s_and_b32 s70,s70,s69
v_rcp_f32 v48,v48
v_mfma_f32_16x16x32_bf16 v[72:75],v[68:71],v[128:131],v[72:75]
s_cmp_lt_u32 s51,s89
v_rcp_f32 v49,v49
v_mfma_f32_16x16x32_bf16 v[72:75],v[68:71],v[132:135],v[72:75]
s_cselect_b32 s70,s59,s70
v_fma_f32 v50,v66,v50,v65
v_mul_f32 v50,v42,v50
v_mfma_f32_16x16x32_bf16 v[72:75],v[68:71],v[136:139],v[72:75]
s_add_u32 s53,s52,s55
v_exp_f32 v50,v50
v_mfma_f32_16x16x32_bf16 v[72:75],v[68:71],v[140:143],v[72:75]
s_cmp_eq_u32 s53,s56
v_fma_f32 v50,v50,v67,v67
v_fma_f32 v51,v66,v51,v65
v_mfma_f32_16x16x32_bf16 v[76:79],v[140:143],v[140:143],v[76:79]
s_cselect_b32 s53,s57,s53
v_rcp_f32 v50,v50
v_mfma_f32_16x16x32_bf16 v[76:79],v[136:139],v[136:139],v[76:79]
s_cmp_eq_u32 s53,s58
v_mul_f32 v51,v43,v51
v_mul_f32 v40,v40,v48
v_mfma_f32_16x16x32_bf16 v[76:79],v[132:135],v[132:135],v[76:79]
s_cselect_b32 s53,s59,s53
v_exp_f32 v51,v51
v_mfma_f32_16x16x32_bf16 v[76:79],v[128:131],v[128:131],v[76:79]
s_waitcnt lgkmcnt(3)
v_fma_f32 v51,v51,v67,v67
v_mul_f32 v41,v41,v49
v_mfma_f32_16x16x32_bf16 v[32:35],a[164:167],v[144:147],v[32:35]
s_xor_b32 s63,s63,65536
ds_read_b128 v[172:175],v2 offset:27648
v_mul_f32 v42,v42,v50
v_mfma_f32_16x16x32_bf16 v[32:35],a[172:175],v[148:151],v[32:35]
s_cmp_ge_u32 s67,s59
s_cselect_b32 s68,s68,s70
s_add_u32 m0,s78,256
v_mfma_f32_16x16x32_bf16 v[28:31],a[168:171],v[148:151],v[28:31]
buffer_load_dword v6,s[36:39],s68 offen offset:0 sc1 lds
v_cndmask_b32 v80,v76,v77,s[82:83]
v_cndmask_b32 v81,v78,v79,s[82:83]
v_cndmask_b32 v80,v80,v81,s[84:85]
v_cndmask_b32 v80,v72,v80,s[98:99]
v_mfma_f32_16x16x32_bf16 v[28:31],a[160:163],v[144:147],v[28:31]
s_mov_b32 m0,s62
ds_read_b128 v[176:179],v2 offset:28672
ds_write_b32 v13,v80 offset:0
v_mfma_f32_16x16x32_bf16 v[28:31],a[176:179],v[152:155],v[28:31]
buffer_load_dwordx4 v0,s[36:39],s67 offen offset:0 sc1 lds
v_mfma_f32_16x16x32_bf16 v[28:31],a[184:187],v[156:159],v[28:31]
s_cmp_ge_u32 s66,s59
ds_read_b128 v[180:183],v2 offset:29696
v_cvt_pk_bf16_f32 v54,v40,v41
v_mfma_f32_16x16x32_bf16 v[32:35],a[188:191],v[156:159],v[32:35]
ds_read_b128 v[184:187],v2 offset:30720
v_mfma_f32_16x16x32_bf16 v[32:35],a[180:183],v[152:155],v[32:35]
s_waitcnt lgkmcnt(4))"
R"(
v_rcp_f32 v51,v51
v_mfma_f32_16x16x32_bf16 v[32:35],a[196:199],v[160:163],v[32:35]
s_waitcnt vmcnt(7) lgkmcnt(2)
ds_read_b128 v[188:191],v2 offset:31744
v_mul_f32 v43,v43,v51
v_mfma_f32_16x16x32_bf16 v[32:35],a[204:207],v[164:167],v[32:35]
s_barrier
v_mfma_f32_16x16x32_bf16 v[28:31],a[200:203],v[164:167],v[28:31]
buffer_load_dwordx4 v0,s[36:39],s67 offen offset:1024 sc1 lds
v_mfma_f32_16x16x32_bf16 v[28:31],a[192:195],v[160:163],v[28:31]
ds_read_b128 v[128:131],v2 offset:32768
ds_read_b128 v[84:87],v14 offset:0
v_mfma_f32_16x16x32_bf16 v[28:31],a[208:211],v[168:171],v[28:31]
ds_read_b128 v[88:91],v14 offset:16
ds_read_b128 v[132:135],v2 offset:33792
v_mfma_f32_16x16x32_bf16 v[28:31],a[216:219],v[172:175],v[28:31]
v_cvt_pk_bf16_f32 v55,v42,v43
v_mfma_f32_16x16x32_bf16 v[32:35],a[220:223],v[172:175],v[32:35]
buffer_load_dwordx4 v0,s[36:39],s67 offen offset:2048 sc1 lds
v_mfma_f32_16x16x32_bf16 v[32:35],a[212:215],v[168:171],v[32:35]
s_waitcnt lgkmcnt(4)
ds_read_b128 v[136:139],v2 offset:34816
v_mfma_f32_16x16x32_bf16 v[32:35],a[228:231],v[176:179],v[32:35]
s_waitcnt lgkmcnt(2)
ds_read_b128 v[140:143],v2 offset:35840
v_add_f32 v84,v84,v85
v_mfma_f32_16x16x32_bf16 v[32:35],a[236:239],v[180:183],v[32:35]
v_add_f32 v86,v86,v87
v_add_f32 v84,v84,v86
v_mfma_f32_16x16x32_bf16 v[28:31],a[232:235],v[180:183],v[28:31]
buffer_load_dwordx4 v0,s[36:39],s67 offen offset:3072 sc1 lds
v_mfma_f32_16x16x32_bf16 v[28:31],a[224:227],v[176:179],v[28:31]
ds_read_b128 v[144:147],v2 offset:36864
v_mul_f32 v60,s75,v84
v_mfma_f32_16x16x32_bf16 v[28:31],a[240:243],v[184:187],v[28:31]
ds_read_b128 v[148:151],v2 offset:37888
v_add_f32 v88,v88,v89
v_mfma_f32_16x16x32_bf16 v[28:31],a[248:251],v[188:191],v[28:31]
v_add_f32 v90,v90,v91
v_add_f32 v88,v88,v90
v_mul_f32 v88,s75,v88
v_fma_f32 v88,-v60,v60,v88
v_max_f32 v88,0,v88
v_add_f32 v83,v15,v88
v_fma_f32 v88,v15,v83,v88
v_rsq_f32 v84,v88
v_mfma_f32_16x16x32_bf16 v[32:35],a[252:255],v[188:191],v[32:35]
s_waitcnt lgkmcnt(2)
ds_read_b128 v[152:155],v2 offset:38912
buffer_store_dwordx4 v[52:55],v3,s[36:39],s73 offen offset:0 sc1
v_mul_f32 v61,s76,v84
v_mul_f32 v62,v84,v84
v_mul_f32 v63,v88,v84
v_mul_f32 v62,s77,v62
v_mul_f32 v62,v62,v61
v_mfma_f32_16x16x32_bf16 v[32:35],a[244:247],v[184:187],v[32:35]
s_cbranch_scc1 Lg1_slow1_%=
Lg1_a1w0_%=:
v_mfma_f32_16x16x32_bf16 v[40:43],a[4:7],v[128:131],0
s_add_u32 m0,s62,16384
ds_read_b128 v[156:159],v2 offset:39936
s_sub_u32 s69,s67,1048576
v_mfma_f32_16x16x32_bf16 v[40:43],a[12:15],v[132:135],v[40:43]
buffer_load_dwordx4 v1,s[36:39],s67 offen offset:0 sc1 lds
v_mfma_f32_16x16x32_bf16 v[36:39],a[8:11],v[132:135],0
s_cmp_lt_u32 s69,2097152
ds_read_b128 v[160:163],v2 offset:40960
s_cselect_b32 s89,3145728,s89
v_mfma_f32_16x16x32_bf16 v[36:39],a[0:3],v[128:131],v[36:39]
s_sub_u32 s69,s67,3145728
v_fma_f32 v28,-v60,v20,v28
v_fma_f32 v29,-v60,v21,v29
v_mfma_f32_16x16x32_bf16 v[36:39],a[16:19],v[136:139],v[36:39]
s_cmp_lt_u32 s69,1048576
ds_read_b128 v[164:167],v2 offset:41984
v_fma_f32 v30,-v60,v22,v30
v_mfma_f32_16x16x32_bf16 v[36:39],a[24:27],v[140:143],v[36:39]
buffer_load_dwordx4 v1,s[36:39],s67 offen offset:1024 sc1 lds
v_mfma_f32_16x16x32_bf16 v[40:43],a[28:31],v[140:143],v[40:43]
s_cselect_b32 s89,4194304,s89
ds_read_b128 v[168:171],v2 offset:43008
v_fma_f32 v31,-v60,v23,v31
v_mfma_f32_16x16x32_bf16 v[40:43],a[20:23],v[136:139],v[40:43]
s_lshr_b32 s73,s48,0
v_mul_f32 v44,v28,v28
v_mul_f32 v45,v29,v29
v_mfma_f32_16x16x32_bf16 v[72:75],v[68:71],v[128:131],0
s_lshr_b32 s72,s67,6
v_mul_f32 v46,v30,v30
v_mul_f32 v47,v31,v31
v_mfma_f32_16x16x32_bf16 v[72:75],v[68:71],v[132:135],v[72:75]
buffer_load_dwordx4 v1,s[36:39],s67 offen offset:2048 sc1 lds
v_mfma_f32_16x16x32_bf16 v[72:75],v[68:71],v[136:139],v[72:75]
s_lshr_b32 s71,s62,2
v_fma_f32 v44,v62,v44,v61
v_mul_f32 v44,v28,v44
v_mfma_f32_16x16x32_bf16 v[72:75],v[68:71],v[140:143],v[72:75]
s_cmp_lt_u32 s67,4194304
v_exp_f32 v44,v44
v_mfma_f32_16x16x32_bf16 v[76:79],v[140:143],v[140:143],0
s_cselect_b32 s72,s59,s72
v_fma_f32 v44,v44,v63,v63
v_fma_f32 v45,v62,v45,v61
v_mfma_f32_16x16x32_bf16 v[76:79],v[136:139],v[136:139],v[76:79]
buffer_load_dwordx4 v1,s[36:39],s67 offen offset:3072 sc1 lds
v_mfma_f32_16x16x32_bf16 v[76:79],v[132:135],v[132:135],v[76:79]
s_add_u32 m0,s71,s88
v_rcp_f32 v44,v44
v_mfma_f32_16x16x32_bf16 v[76:79],v[128:131],v[128:131],v[76:79]
s_waitcnt lgkmcnt(3)
buffer_load_dword v10,s[36:39],s72 offen offset:0 sc1 lds
v_mfma_f32_16x16x32_bf16 v[40:43],a[36:39],v[144:147],v[40:43]
s_xor_b32 s87,s63,65536
ds_read_b128 v[172:175],v2 offset:44032
v_mul_f32 v45,v29,v45
v_mfma_f32_16x16x32_bf16 v[40:43],a[44:47],v[148:151],v[40:43]
s_cmp_ge_u32 s67,s59
v_exp_f32 v45,v45
v_mfma_f32_16x16x32_bf16 v[36:39],a[40:43],v[148:151],v[36:39]
s_cselect_b32 s86,s50,s51
ds_read_b128 v[176:179],v2 offset:45056
v_fma_f32 v45,v45,v63,v63
v_mfma_f32_16x16x32_bf16 v[36:39],a[32:35],v[144:147],v[36:39]
s_cselect_b32 s87,s87,s64
v_rcp_f32 v45,v45
v_mfma_f32_16x16x32_bf16 v[36:39],a[48:51],v[152:155],v[36:39]
s_mov_b32 s62,s87
ds_read_b128 v[180:183],v2 offset:46080
v_fma_f32 v46,v62,v46,v61
v_mfma_f32_16x16x32_bf16 v[36:39],a[56:59],v[156:159],v[36:39]
s_cmp_lt_u32 s86,s89
v_mul_f32 v46,v30,v46
v_fma_f32 v47,v62,v47,v61
v_mfma_f32_16x16x32_bf16 v[40:43],a[60:63],v[156:159],v[40:43]
s_mov_b64 s[44:45],s[46:47]
ds_read_b128 v[184:187],v2 offset:47104
v_mul_f32 v47,v31,v47
v_mfma_f32_16x16x32_bf16 v[40:43],a[52:55],v[152:155],v[40:43]
s_waitcnt lgkmcnt(3)
v_exp_f32 v46,v46
v_mfma_f32_16x16x32_bf16 v[40:43],a[68:71],v[160:163],v[40:43]
s_waitcnt vmcnt(10)
ds_read_b128 v[188:191],v2 offset:48128
v_fma_f32 v46,v46,v63,v63
v_mfma_f32_16x16x32_bf16 v[40:43],a[76:79],v[164:167],v[40:43]
s_barrier
v_mfma_f32_16x16x32_bf16 v[36:39],a[72:75],v[164:167],v[36:39]
s_mov_b64 s[46:47],s[48:49]
ds_read_b128 v[128:131],v2 offset:49152
ds_read_b32 v16,v7 offset:256
v_mfma_f32_16x16x32_bf16 v[36:39],a[64:67],v[160:163],v[36:39]
s_mov_b64 s[48:49],s[50:51]
v_exp_f32 v47,v47
v_mfma_f32_16x16x32_bf16 v[36:39],a[80:83],v[168:171],v[36:39]
ds_read_b128 v[132:135],v2 offset:50176
v_fma_f32 v47,v47,v63,v63
v_mfma_f32_16x16x32_bf16 v[36:39],a[88:91],v[172:175],v[36:39]
v_rcp_f32 v46,v46
v_mfma_f32_16x16x32_bf16 v[40:43],a[92:95],v[172:175],v[40:43]
s_waitcnt lgkmcnt(1)
ds_read_b128 v[136:139],v2 offset:51200
v_mul_f32 v28,v28,v44
v_mfma_f32_16x16x32_bf16 v[40:43],a[84:87],v[168:171],v[40:43]
v_rcp_f32 v47,v47
v_mfma_f32_16x16x32_bf16 v[40:43],a[100:103],v[176:179],v[40:43]
ds_read_b128 v[140:143],v2 offset:52224
v_mul_f32 v29,v29,v45
v_mfma_f32_16x16x32_bf16 v[40:43],a[108:111],v[180:183],v[40:43]
v_cmp_eq_u32 vcc,s60,v16
v_mul_f32 v30,v30,v46
v_mul_f32 v31,v31,v47
v_cvt_pk_bf16_f32 v52,v28,v29
v_mfma_f32_16x16x32_bf16 v[36:39],a[104:107],v[180:183],v[36:39]
s_cselect_b32 vcc_lo,-1,vcc_lo
ds_read_b128 v[144:147],v2 offset:53248
v_cvt_pk_bf16_f32 v53,v30,v31
v_mfma_f32_16x16x32_bf16 v[36:39],a[96:99],v[176:179],v[36:39]
s_cmp_eq_u32 vcc_lo,-1
v_fma_f32 v32,-v60,v24,v32
v_fma_f32 v33,-v60,v25,v33
v_mfma_f32_16x16x32_bf16 v[36:39],a[112:115],v[184:187],v[36:39]
s_cselect_b32 s66,s86,s59
ds_read_b128 v[148:151],v2 offset:54272
v_fma_f32 v34,-v60,v26,v34
v_mfma_f32_16x16x32_bf16 v[36:39],a[120:123],v[188:191],v[36:39]
s_lshr_b32 s69,s51,5
v_fma_f32 v35,-v60,v27,v35
v_mul_f32 v48,v32,v32
v_mfma_f32_16x16x32_bf16 v[40:43],a[124:127],v[188:191],v[40:43]
s_cmp_lt_u32 s51,4194304
ds_read_b128 v[152:155],v2 offset:55296
v_mul_f32 v49,v33,v33
v_mfma_f32_16x16x32_bf16 v[40:43],a[116:119],v[184:187],v[40:43]
s_waitcnt lgkmcnt(3)
v_mul_f32 v50,v34,v34
v_mul_f32 v51,v35,v35
v_mfma_f32_16x16x32_bf16 v[40:43],a[132:135],v[128:131],v[40:43]
s_cselect_b32 s68,0xffffcfff,-1
ds_read_b128 v[156:159],v2 offset:56320
v_fma_f32 v48,v62,v48,v61)"
R"(
v_mfma_f32_16x16x32_bf16 v[40:43],a[140:143],v[132:135],v[40:43]
s_and_b32 s68,s68,s69
v_mul_f32 v48,v32,v48
v_fma_f32 v49,v62,v49,v61
v_mfma_f32_16x16x32_bf16 v[36:39],a[136:139],v[132:135],v[36:39]
s_cmp_lt_u32 s51,s89
ds_read_b128 v[160:163],v2 offset:57344
v_mul_f32 v49,v33,v49
v_mfma_f32_16x16x32_bf16 v[36:39],a[128:131],v[128:131],v[36:39]
s_cselect_b32 s68,s59,s68
v_exp_f32 v48,v48
v_mfma_f32_16x16x32_bf16 v[36:39],a[144:147],v[136:139],v[36:39]
s_lshr_b32 s69,s52,5
ds_read_b128 v[164:167],v2 offset:58368
v_fma_f32 v48,v48,v63,v63
v_mfma_f32_16x16x32_bf16 v[36:39],a[152:155],v[140:143],v[36:39]
s_cmp_lt_u32 s52,4194304
v_exp_f32 v49,v49
v_mfma_f32_16x16x32_bf16 v[40:43],a[156:159],v[140:143],v[40:43]
s_cselect_b32 s70,0xffffcfff,-1
ds_read_b128 v[168:171],v2 offset:59392
v_fma_f32 v49,v49,v63,v63
v_mfma_f32_16x16x32_bf16 v[40:43],a[148:151],v[136:139],v[40:43]
s_and_b32 s70,s70,s69
v_rcp_f32 v48,v48
v_mfma_f32_16x16x32_bf16 v[72:75],v[68:71],v[128:131],v[72:75]
s_cmp_lt_u32 s52,s89
v_rcp_f32 v49,v49
v_mfma_f32_16x16x32_bf16 v[72:75],v[68:71],v[132:135],v[72:75]
s_cselect_b32 s70,s59,s70
v_fma_f32 v50,v62,v50,v61
v_mul_f32 v50,v34,v50
v_mfma_f32_16x16x32_bf16 v[72:75],v[68:71],v[136:139],v[72:75]
s_add_u32 s74,s53,s55
v_exp_f32 v50,v50
v_mfma_f32_16x16x32_bf16 v[72:75],v[68:71],v[140:143],v[72:75]
s_cmp_eq_u32 s74,s56
v_fma_f32 v50,v50,v63,v63
v_fma_f32 v51,v62,v51,v61
v_mfma_f32_16x16x32_bf16 v[76:79],v[140:143],v[140:143],v[76:79]
s_cselect_b32 s74,s57,s74
v_rcp_f32 v50,v50
v_mfma_f32_16x16x32_bf16 v[76:79],v[136:139],v[136:139],v[76:79]
s_cmp_eq_u32 s74,s58
v_mul_f32 v51,v35,v51
v_mul_f32 v32,v32,v48
v_mfma_f32_16x16x32_bf16 v[76:79],v[132:135],v[132:135],v[76:79]
s_cselect_b32 s74,s59,s74
v_exp_f32 v51,v51
v_mfma_f32_16x16x32_bf16 v[76:79],v[128:131],v[128:131],v[76:79]
s_waitcnt lgkmcnt(3)
v_fma_f32 v51,v51,v63,v63
v_mul_f32 v33,v33,v49
v_mfma_f32_16x16x32_bf16 v[40:43],a[164:167],v[144:147],v[40:43]
s_xor_b32 s64,s64,65536
ds_read_b128 v[172:175],v2 offset:60416
v_mul_f32 v34,v34,v50
v_mfma_f32_16x16x32_bf16 v[40:43],a[172:175],v[148:151],v[40:43]
s_cmp_ge_u32 s66,s59
s_cselect_b32 s68,s68,s70
s_add_u32 m0,s78,0
v_mfma_f32_16x16x32_bf16 v[36:39],a[168:171],v[148:151],v[36:39]
buffer_load_dword v6,s[36:39],s68 offen offset:0 sc1 lds
v_cndmask_b32 v80,v76,v77,s[82:83]
v_cndmask_b32 v81,v78,v79,s[82:83]
v_cndmask_b32 v80,v80,v81,s[84:85]
v_cndmask_b32 v80,v72,v80,s[98:99]
v_mfma_f32_16x16x32_bf16 v[36:39],a[160:163],v[144:147],v[36:39]
s_mov_b32 m0,s62
ds_read_b128 v[176:179],v2 offset:61440
ds_write_b32 v13,v80 offset:2048
v_mfma_f32_16x16x32_bf16 v[36:39],a[176:179],v[152:155],v[36:39]
buffer_load_dwordx4 v0,s[36:39],s66 offen offset:0 sc1 lds
v_mfma_f32_16x16x32_bf16 v[36:39],a[184:187],v[156:159],v[36:39]
s_mov_b64 s[50:51],s[52:53]
ds_read_b128 v[180:183],v2 offset:62464
v_cvt_pk_bf16_f32 v54,v32,v33
v_mfma_f32_16x16x32_bf16 v[40:43],a[188:191],v[156:159],v[40:43]
s_mov_b32 s52,s74
ds_read_b128 v[184:187],v2 offset:63488
s_cmp_lt_u32 s67,4194304
v_mfma_f32_16x16x32_bf16 v[40:43],a[180:183],v[152:155],v[40:43]
s_waitcnt lgkmcnt(4)
v_rcp_f32 v51,v51
v_mfma_f32_16x16x32_bf16 v[40:43],a[196:199],v[160:163],v[40:43]
s_waitcnt vmcnt(7) lgkmcnt(2)
ds_read_b128 v[188:191],v2 offset:64512
v_xor_b32 v2,65536,v2
v_mfma_f32_16x16x32_bf16 v[40:43],a[204:207],v[164:167],v[40:43]
s_barrier
v_mfma_f32_16x16x32_bf16 v[36:39],a[200:203],v[164:167],v[36:39]
buffer_load_dwordx4 v0,s[36:39],s66 offen offset:1024 sc1 lds
v_mfma_f32_16x16x32_bf16 v[36:39],a[192:195],v[160:163],v[36:39]
ds_read_b128 v[128:131],v2 offset:0
ds_read_b128 v[84:87],v14 offset:2048
v_mfma_f32_16x16x32_bf16 v[36:39],a[208:211],v[168:171],v[36:39]
ds_read_b128 v[88:91],v14 offset:2064
ds_read_b128 v[132:135],v2 offset:1024
v_mfma_f32_16x16x32_bf16 v[36:39],a[216:219],v[172:175],v[36:39]
v_mul_f32 v35,v35,v51
v_cvt_pk_bf16_f32 v55,v34,v35
v_mfma_f32_16x16x32_bf16 v[40:43],a[220:223],v[172:175],v[40:43]
buffer_load_dwordx4 v0,s[36:39],s66 offen offset:2048 sc1 lds
v_mfma_f32_16x16x32_bf16 v[40:43],a[212:215],v[168:171],v[40:43]
s_waitcnt lgkmcnt(4)
ds_read_b128 v[136:139],v2 offset:2048
v_xor_b32 v8,16384,v8
v_mfma_f32_16x16x32_bf16 v[40:43],a[228:231],v[176:179],v[40:43]
s_waitcnt lgkmcnt(2)
ds_read_b128 v[140:143],v2 offset:3072
v_add_f32 v84,v84,v85
v_mfma_f32_16x16x32_bf16 v[40:43],a[236:239],v[180:183],v[40:43]
v_add_f32 v86,v86,v87
v_add_f32 v84,v84,v86
v_mfma_f32_16x16x32_bf16 v[36:39],a[232:235],v[180:183],v[36:39]
buffer_load_dwordx4 v0,s[36:39],s66 offen offset:3072 sc1 lds
v_mfma_f32_16x16x32_bf16 v[36:39],a[224:227],v[176:179],v[36:39]
ds_read_b128 v[144:147],v2 offset:4096
v_mul_f32 v64,s75,v84
v_mfma_f32_16x16x32_bf16 v[36:39],a[240:243],v[184:187],v[36:39]
ds_read_b128 v[148:151],v2 offset:5120
v_add_f32 v88,v88,v89
v_mfma_f32_16x16x32_bf16 v[36:39],a[248:251],v[188:191],v[36:39]
v_add_f32 v90,v90,v91
v_add_f32 v88,v88,v90
v_mul_f32 v88,s75,v88
v_fma_f32 v88,-v64,v64,v88
v_max_f32 v88,0,v88
v_add_f32 v83,v15,v88
v_fma_f32 v88,v15,v83,v88
v_rsq_f32 v84,v88
v_mfma_f32_16x16x32_bf16 v[40:43],a[252:255],v[188:191],v[40:43]
s_waitcnt lgkmcnt(2)
ds_read_b128 v[152:155],v2 offset:6144
buffer_store_dwordx4 v[52:55],v3,s[36:39],s73 offen offset:0 sc1
v_mul_f32 v65,s76,v84
v_mul_f32 v66,v84,v84
v_mul_f32 v67,v88,v84
v_mul_f32 v66,s77,v66
v_mul_f32 v66,v66,v65
v_mfma_f32_16x16x32_bf16 v[40:43],a[244:247],v[184:187],v[40:43]
s_cbranch_scc1 Lg1_a0w0_%=
s_cmp_ge_u32 s48,4194304
s_cselect_b64 s[90:91],s[94:95],s[90:91]
s_cselect_b64 s[92:93],s[96:97],s[92:93]
s_cmp_lt_u32 s67,s59
s_cbranch_scc1 Lg1_d0w0_%=
s_branch Lg1_slow0_%=
Lg1_a0w1_%=:
v_mfma_f32_16x16x32_bf16 v[32:35],a[36:39],v[128:131],0
s_add_u32 m0,s62,16384
ds_read_b128 v[156:159],v2 offset:3072
s_sub_u32 s69,s66,1048576
v_mfma_f32_16x16x32_bf16 v[32:35],a[44:47],v[132:135],v[32:35]
s_cmp_lt_u32 s69,2097152
s_cselect_b32 s89,3145728,s89
s_sub_u32 s69,s66,3145728
v_mfma_f32_16x16x32_bf16 v[28:31],a[40:43],v[132:135],0
buffer_load_dwordx4 v1,s[36:39],s66 offen offset:0 sc1 lds
v_mfma_f32_16x16x32_bf16 v[28:31],a[32:35],v[128:131],v[28:31]
s_cmp_lt_u32 s69,1048576
ds_read_b128 v[160:163],v2 offset:12288
v_fma_f32 v36,-v64,v20,v36
v_mfma_f32_16x16x32_bf16 v[28:31],a[48:51],v[136:139],v[28:31]
s_cselect_b32 s89,4194304,s89
ds_read_b128 v[164:167],v2 offset:13312
v_fma_f32 v37,-v64,v21,v37
v_mfma_f32_16x16x32_bf16 v[28:31],a[56:59],v[140:143],v[28:31]
s_lshr_b32 s73,s47,0
v_fma_f32 v38,-v64,v22,v38
v_fma_f32 v39,-v64,v23,v39
v_mfma_f32_16x16x32_bf16 v[32:35],a[60:63],v[140:143],v[32:35]
buffer_load_dwordx4 v1,s[36:39],s66 offen offset:1024 sc1 lds
v_mfma_f32_16x16x32_bf16 v[32:35],a[52:55],v[136:139],v[32:35]
s_lshr_b32 s72,s66,6
ds_read_b128 v[168:171],v2 offset:14336
v_mul_f32 v44,v36,v36
v_mfma_f32_16x16x32_bf16 v[72:75],v[68:71],v[128:131],0
s_lshr_b32 s71,s62,2
v_mul_f32 v45,v37,v37
v_mul_f32 v46,v38,v38
v_mfma_f32_16x16x32_bf16 v[72:75],v[68:71],v[132:135],v[72:75]
s_cmp_lt_u32 s66,4194304
v_mul_f32 v47,v39,v39
v_fma_f32 v44,v66,v44,v65
v_mfma_f32_16x16x32_bf16 v[72:75],v[68:71],v[136:139],v[72:75]
buffer_load_dwordx4 v1,s[36:39],s66 offen offset:2048 sc1 lds
v_mfma_f32_16x16x32_bf16 v[72:75],v[68:71],v[140:143],v[72:75]
s_cselect_b32 s72,s59,s72
v_mul_f32 v44,v36,v44
v_fma_f32 v45,v66,v45,v65
v_mfma_f32_16x16x32_bf16 v[76:79],v[140:143],v[140:143],0
v_exp_f32 v44,v44
v_mfma_f32_16x16x32_bf16 v[76:79],v[136:139],v[136:139],v[76:79]
v_mul_f32 v45,v37,v45
v_fma_f32 v44,v44,v67,v67
v_mfma_f32_16x16x32_bf16 v[76:79],v[132:135],v[132:135],v[76:79]
buffer_load_dwordx4 v1,s[36:39],s66 offen offset:3072 sc1 lds
v_mfma_f32_16x16x32_bf16 v[76:79],v[128:131],v[128:131],v[76:79]
s_waitcnt lgkmcnt(3))"
R"(
v_exp_f32 v45,v45
v_mfma_f32_16x16x32_bf16 v[32:35],a[4:7],v[144:147],v[32:35]
s_add_u32 m0,s71,s88
ds_read_b128 v[172:175],v2 offset:15360
v_fma_f32 v45,v45,v67,v67
v_mfma_f32_16x16x32_bf16 v[32:35],a[12:15],v[148:151],v[32:35]
buffer_load_dword v10,s[36:39],s72 offen offset:0 sc1 lds
v_mfma_f32_16x16x32_bf16 v[28:31],a[8:11],v[148:151],v[28:31]
s_xor_b32 s87,s64,65536
ds_read_b128 v[176:179],v2 offset:8192
v_fma_f32 v46,v66,v46,v65
v_mfma_f32_16x16x32_bf16 v[28:31],a[0:3],v[144:147],v[28:31]
s_cmp_ge_u32 s66,s59
v_rcp_f32 v44,v44
v_mfma_f32_16x16x32_bf16 v[28:31],a[16:19],v[152:155],v[28:31]
s_cselect_b32 s86,s49,s50
ds_read_b128 v[180:183],v2 offset:9216
v_mul_f32 v46,v38,v46
v_mfma_f32_16x16x32_bf16 v[28:31],a[24:27],v[156:159],v[28:31]
s_cselect_b32 s87,s87,s63
v_exp_f32 v46,v46
v_mfma_f32_16x16x32_bf16 v[32:35],a[28:31],v[156:159],v[32:35]
s_mov_b32 s62,s87
ds_read_b128 v[184:187],v2 offset:10240
v_fma_f32 v46,v46,v67,v67
v_mfma_f32_16x16x32_bf16 v[32:35],a[20:23],v[152:155],v[32:35]
s_waitcnt lgkmcnt(3)
v_rcp_f32 v45,v45
v_mfma_f32_16x16x32_bf16 v[32:35],a[100:103],v[160:163],v[32:35]
s_waitcnt vmcnt(10)
ds_read_b128 v[188:191],v2 offset:11264
v_fma_f32 v47,v66,v47,v65
v_mfma_f32_16x16x32_bf16 v[32:35],a[108:111],v[164:167],v[32:35]
s_barrier
v_mfma_f32_16x16x32_bf16 v[28:31],a[104:107],v[164:167],v[28:31]
s_cmp_lt_u32 s86,s89
ds_read_b128 v[128:131],v2 offset:20480
ds_read_b32 v16,v7 offset:0
v_mfma_f32_16x16x32_bf16 v[28:31],a[96:99],v[160:163],v[28:31]
s_mov_b32 m0,s62
v_rcp_f32 v46,v46
v_mfma_f32_16x16x32_bf16 v[28:31],a[112:115],v[168:171],v[28:31]
ds_read_b128 v[132:135],v2 offset:21504
v_mul_f32 v47,v39,v47
v_mfma_f32_16x16x32_bf16 v[28:31],a[120:123],v[172:175],v[28:31]
v_exp_f32 v47,v47
v_mfma_f32_16x16x32_bf16 v[32:35],a[124:127],v[172:175],v[32:35]
s_waitcnt lgkmcnt(1)
ds_read_b128 v[136:139],v2 offset:22528
v_fma_f32 v47,v47,v67,v67
v_mfma_f32_16x16x32_bf16 v[32:35],a[116:119],v[168:171],v[32:35]
v_rcp_f32 v47,v47
v_mfma_f32_16x16x32_bf16 v[32:35],a[68:71],v[176:179],v[32:35]
ds_read_b128 v[140:143],v2 offset:23552
v_mul_f32 v36,v36,v44
v_mfma_f32_16x16x32_bf16 v[32:35],a[76:79],v[180:183],v[32:35]
v_cmp_eq_u32 vcc,s60,v16
v_mul_f32 v37,v37,v45
v_mul_f32 v38,v38,v46
v_mul_f32 v39,v39,v47
v_mfma_f32_16x16x32_bf16 v[28:31],a[72:75],v[180:183],v[28:31]
s_cselect_b32 vcc_lo,-1,vcc_lo
ds_read_b128 v[144:147],v2 offset:16384
v_cvt_pk_bf16_f32 v52,v36,v37
v_mfma_f32_16x16x32_bf16 v[28:31],a[64:67],v[176:179],v[28:31]
s_cmp_eq_u32 vcc_lo,-1
v_cvt_pk_bf16_f32 v53,v38,v39
v_fma_f32 v40,-v64,v24,v40
v_mfma_f32_16x16x32_bf16 v[28:31],a[80:83],v[184:187],v[28:31]
s_cselect_b32 s67,s86,s59
ds_read_b128 v[148:151],v2 offset:17408
v_fma_f32 v41,-v64,v25,v41
v_mfma_f32_16x16x32_bf16 v[28:31],a[88:91],v[188:191],v[28:31]
s_add_u32 s53,s52,s55
v_fma_f32 v42,-v64,v26,v42
v_fma_f32 v43,-v64,v27,v43
v_mfma_f32_16x16x32_bf16 v[32:35],a[92:95],v[188:191],v[32:35]
s_cmp_eq_u32 s53,s56
ds_read_b128 v[152:155],v2 offset:18432
v_mul_f32 v48,v40,v40
v_mfma_f32_16x16x32_bf16 v[32:35],a[84:87],v[184:187],v[32:35]
s_waitcnt lgkmcnt(3)
v_mul_f32 v49,v41,v41
v_mul_f32 v50,v42,v42
v_mfma_f32_16x16x32_bf16 v[32:35],a[164:167],v[128:131],v[32:35]
s_cselect_b32 s53,s57,s53
ds_read_b128 v[156:159],v2 offset:19456
v_mul_f32 v51,v43,v43
v_mfma_f32_16x16x32_bf16 v[32:35],a[172:175],v[132:135],v[32:35]
s_cmp_eq_u32 s53,s58
v_fma_f32 v48,v66,v48,v65
v_mul_f32 v48,v40,v48
v_mfma_f32_16x16x32_bf16 v[28:31],a[168:171],v[132:135],v[28:31]
s_cselect_b32 s53,s59,s53
ds_read_b128 v[160:163],v2 offset:28672
v_fma_f32 v49,v66,v49,v65
v_mfma_f32_16x16x32_bf16 v[28:31],a[160:163],v[128:131],v[28:31]
s_xor_b32 s63,s63,65536
v_exp_f32 v48,v48
v_mfma_f32_16x16x32_bf16 v[28:31],a[176:179],v[136:139],v[28:31]
s_cmp_ge_u32 s66,s59
ds_read_b128 v[164:167],v2 offset:29696
v_mul_f32 v49,v41,v49
v_mfma_f32_16x16x32_bf16 v[28:31],a[184:187],v[140:143],v[28:31]
v_exp_f32 v49,v49
v_mfma_f32_16x16x32_bf16 v[32:35],a[188:191],v[140:143],v[32:35]
ds_read_b128 v[168:171],v2 offset:30720
v_fma_f32 v48,v48,v67,v67
v_mfma_f32_16x16x32_bf16 v[32:35],a[180:183],v[136:139],v[32:35]
v_rcp_f32 v48,v48
v_mfma_f32_16x16x32_bf16 v[72:75],v[68:71],v[128:131],v[72:75]
v_fma_f32 v49,v49,v67,v67
v_fma_f32 v50,v66,v50,v65
v_mfma_f32_16x16x32_bf16 v[72:75],v[68:71],v[132:135],v[72:75]
v_rcp_f32 v49,v49
v_mfma_f32_16x16x32_bf16 v[72:75],v[68:71],v[136:139],v[72:75]
v_mul_f32 v50,v42,v50
v_fma_f32 v51,v66,v51,v65
v_mfma_f32_16x16x32_bf16 v[72:75],v[68:71],v[140:143],v[72:75]
v_exp_f32 v50,v50
v_mfma_f32_16x16x32_bf16 v[76:79],v[140:143],v[140:143],v[76:79]
v_mul_f32 v51,v43,v51
v_fma_f32 v50,v50,v67,v67
v_mfma_f32_16x16x32_bf16 v[76:79],v[136:139],v[136:139],v[76:79]
v_exp_f32 v51,v51
v_mfma_f32_16x16x32_bf16 v[76:79],v[132:135],v[132:135],v[76:79]
v_rcp_f32 v50,v50
v_mfma_f32_16x16x32_bf16 v[76:79],v[128:131],v[128:131],v[76:79]
s_waitcnt lgkmcnt(3)
v_fma_f32 v51,v51,v67,v67
v_mul_f32 v40,v40,v48
v_mfma_f32_16x16x32_bf16 v[32:35],a[132:135],v[144:147],v[32:35]
ds_read_b128 v[172:175],v2 offset:31744
v_mul_f32 v41,v41,v49
v_mfma_f32_16x16x32_bf16 v[32:35],a[140:143],v[148:151],v[32:35]
buffer_load_dwordx4 v0,s[36:39],s67 offen offset:0 sc1 lds
v_mfma_f32_16x16x32_bf16 v[28:31],a[136:139],v[148:151],v[28:31]
v_cndmask_b32 v80,v76,v77,s[82:83]
v_cndmask_b32 v81,v78,v79,s[82:83]
v_cndmask_b32 v80,v80,v81,s[84:85]
v_cndmask_b32 v80,v72,v80,s[98:99]
v_mfma_f32_16x16x32_bf16 v[28:31],a[128:131],v[144:147],v[28:31]
ds_read_b128 v[176:179],v2 offset:24576
ds_write_b32 v13,v80 offset:0
v_mfma_f32_16x16x32_bf16 v[28:31],a[144:147],v[152:155],v[28:31]
ds_read_b128 v[180:183],v2 offset:25600
v_mul_f32 v42,v42,v50
v_mfma_f32_16x16x32_bf16 v[28:31],a[152:155],v[156:159],v[28:31]
buffer_load_dwordx4 v0,s[36:39],s67 offen offset:1024 sc1 lds
v_mfma_f32_16x16x32_bf16 v[32:35],a[156:159],v[156:159],v[32:35]
ds_read_b128 v[184:187],v2 offset:26624
v_cvt_pk_bf16_f32 v54,v40,v41
v_mfma_f32_16x16x32_bf16 v[32:35],a[148:151],v[152:155],v[32:35]
s_waitcnt lgkmcnt(4)
v_rcp_f32 v51,v51
v_mfma_f32_16x16x32_bf16 v[32:35],a[228:231],v[160:163],v[32:35]
s_waitcnt vmcnt(7) lgkmcnt(2)
ds_read_b128 v[188:191],v2 offset:27648
v_mul_f32 v43,v43,v51
v_mfma_f32_16x16x32_bf16 v[32:35],a[236:239],v[164:167],v[32:35]
s_barrier
v_mfma_f32_16x16x32_bf16 v[28:31],a[232:235],v[164:167],v[28:31]
ds_read_b128 v[84:87],v14 offset:0
ds_read_b128 v[88:91],v14 offset:16
v_mfma_f32_16x16x32_bf16 v[28:31],a[224:227],v[160:163],v[28:31]
ds_read_b128 v[128:131],v2 offset:36864
buffer_load_dwordx4 v0,s[36:39],s67 offen offset:2048 sc1 lds
v_mfma_f32_16x16x32_bf16 v[28:31],a[240:243],v[168:171],v[28:31]
ds_read_b128 v[132:135],v2 offset:37888
v_cvt_pk_bf16_f32 v55,v42,v43
v_mfma_f32_16x16x32_bf16 v[28:31],a[248:251],v[172:175],v[28:31]
v_mfma_f32_16x16x32_bf16 v[32:35],a[252:255],v[172:175],v[32:35]
s_waitcnt lgkmcnt(2)
ds_read_b128 v[136:139],v2 offset:38912
v_add_f32 v84,v84,v85
v_mfma_f32_16x16x32_bf16 v[32:35],a[244:247],v[168:171],v[32:35]
buffer_load_dwordx4 v0,s[36:39],s67 offen offset:3072 sc1 lds
v_mfma_f32_16x16x32_bf16 v[32:35],a[196:199],v[176:179],v[32:35]
ds_read_b128 v[140:143],v2 offset:39936
v_add_f32 v86,v86,v87
v_mfma_f32_16x16x32_bf16 v[32:35],a[204:207],v[180:183],v[32:35]
v_add_f32 v84,v84,v86
v_mul_f32 v60,s75,v84
v_mfma_f32_16x16x32_bf16 v[28:31],a[200:203],v[180:183],v[28:31]
ds_read_b128 v[144:147],v2 offset:32768
v_add_f32 v88,v88,v89
v_mfma_f32_16x16x32_bf16 v[28:31],a[192:195],v[176:179],v[28:31]
buffer_store_dwordx4 v[52:55],v3,s[36:39],s73 offen offset:0 sc1
v_add_f32 v90,v90,v91
v_add_f32 v88,v88,v90
v_mul_f32 v88,s75,v88
v_mfma_f32_16x16x32_bf16 v[28:31],a[208:211],v[184:187],v[28:31]
ds_read_b128 v[148:151],v2 offset:33792
v_fma_f32 v88,-v60,v60,v88
v_mfma_f32_16x16x32_bf16 v[28:31],a[216:219],v[188:191],v[28:31]
v_max_f32 v88,0,v88
v_add_f32 v83,v15,v88
v_fma_f32 v88,v15,v83,v88
v_rsq_f32 v84,v88
v_mfma_f32_16x16x32_bf16 v[32:35],a[220:223],v[188:191],v[32:35])"
R"(
s_waitcnt lgkmcnt(2)
ds_read_b128 v[152:155],v2 offset:34816
v_mul_f32 v61,s76,v84
v_mul_f32 v62,v84,v84
v_mul_f32 v63,v88,v84
v_mul_f32 v62,s77,v62
v_mul_f32 v62,v62,v61
v_mfma_f32_16x16x32_bf16 v[32:35],a[212:215],v[184:187],v[32:35]
s_cbranch_scc1 Lg1_slow1_%=
Lg1_a1w1_%=:
v_mfma_f32_16x16x32_bf16 v[40:43],a[36:39],v[128:131],0
s_add_u32 m0,s62,16384
ds_read_b128 v[156:159],v2 offset:35840
s_sub_u32 s69,s67,1048576
v_mfma_f32_16x16x32_bf16 v[40:43],a[44:47],v[132:135],v[40:43]
s_cmp_lt_u32 s69,2097152
s_cselect_b32 s89,3145728,s89
s_sub_u32 s69,s67,3145728
v_mfma_f32_16x16x32_bf16 v[36:39],a[40:43],v[132:135],0
buffer_load_dwordx4 v1,s[36:39],s67 offen offset:0 sc1 lds
v_mfma_f32_16x16x32_bf16 v[36:39],a[32:35],v[128:131],v[36:39]
s_cmp_lt_u32 s69,1048576
ds_read_b128 v[160:163],v2 offset:45056
v_fma_f32 v28,-v60,v20,v28
v_mfma_f32_16x16x32_bf16 v[36:39],a[48:51],v[136:139],v[36:39]
s_cselect_b32 s89,4194304,s89
ds_read_b128 v[164:167],v2 offset:46080
v_fma_f32 v29,-v60,v21,v29
v_mfma_f32_16x16x32_bf16 v[36:39],a[56:59],v[140:143],v[36:39]
s_lshr_b32 s73,s48,0
v_fma_f32 v30,-v60,v22,v30
v_fma_f32 v31,-v60,v23,v31
v_mfma_f32_16x16x32_bf16 v[40:43],a[60:63],v[140:143],v[40:43]
buffer_load_dwordx4 v1,s[36:39],s67 offen offset:1024 sc1 lds
v_mfma_f32_16x16x32_bf16 v[40:43],a[52:55],v[136:139],v[40:43]
s_lshr_b32 s72,s67,6
ds_read_b128 v[168:171],v2 offset:47104
v_mul_f32 v44,v28,v28
v_mfma_f32_16x16x32_bf16 v[72:75],v[68:71],v[128:131],0
s_lshr_b32 s71,s62,2
v_mul_f32 v45,v29,v29
v_mul_f32 v46,v30,v30
v_mfma_f32_16x16x32_bf16 v[72:75],v[68:71],v[132:135],v[72:75]
s_cmp_lt_u32 s67,4194304
v_mul_f32 v47,v31,v31
v_fma_f32 v44,v62,v44,v61
v_mfma_f32_16x16x32_bf16 v[72:75],v[68:71],v[136:139],v[72:75]
buffer_load_dwordx4 v1,s[36:39],s67 offen offset:2048 sc1 lds
v_mfma_f32_16x16x32_bf16 v[72:75],v[68:71],v[140:143],v[72:75]
s_cselect_b32 s72,s59,s72
v_mul_f32 v44,v28,v44
v_fma_f32 v45,v62,v45,v61
v_mfma_f32_16x16x32_bf16 v[76:79],v[140:143],v[140:143],0
s_mov_b64 s[44:45],s[46:47]
v_exp_f32 v44,v44
v_mfma_f32_16x16x32_bf16 v[76:79],v[136:139],v[136:139],v[76:79]
s_mov_b64 s[46:47],s[48:49]
v_mul_f32 v45,v29,v45
v_fma_f32 v44,v44,v63,v63
v_mfma_f32_16x16x32_bf16 v[76:79],v[132:135],v[132:135],v[76:79]
buffer_load_dwordx4 v1,s[36:39],s67 offen offset:3072 sc1 lds
v_mfma_f32_16x16x32_bf16 v[76:79],v[128:131],v[128:131],v[76:79]
s_waitcnt lgkmcnt(3)
v_exp_f32 v45,v45
v_mfma_f32_16x16x32_bf16 v[40:43],a[4:7],v[144:147],v[40:43]
s_add_u32 m0,s71,s88
ds_read_b128 v[172:175],v2 offset:48128
v_fma_f32 v45,v45,v63,v63
v_mfma_f32_16x16x32_bf16 v[40:43],a[12:15],v[148:151],v[40:43]
buffer_load_dword v10,s[36:39],s72 offen offset:0 sc1 lds
v_mfma_f32_16x16x32_bf16 v[36:39],a[8:11],v[148:151],v[36:39]
s_xor_b32 s87,s63,65536
ds_read_b128 v[176:179],v2 offset:40960
v_fma_f32 v46,v62,v46,v61
v_mfma_f32_16x16x32_bf16 v[36:39],a[0:3],v[144:147],v[36:39]
s_cmp_ge_u32 s67,s59
v_rcp_f32 v44,v44
v_mfma_f32_16x16x32_bf16 v[36:39],a[16:19],v[152:155],v[36:39]
s_cselect_b32 s86,s50,s51
ds_read_b128 v[180:183],v2 offset:41984
v_mul_f32 v46,v30,v46
v_mfma_f32_16x16x32_bf16 v[36:39],a[24:27],v[156:159],v[36:39]
s_cselect_b32 s87,s87,s64
v_exp_f32 v46,v46
v_mfma_f32_16x16x32_bf16 v[40:43],a[28:31],v[156:159],v[40:43]
s_mov_b32 s62,s87
ds_read_b128 v[184:187],v2 offset:43008
v_fma_f32 v46,v46,v63,v63
v_mfma_f32_16x16x32_bf16 v[40:43],a[20:23],v[152:155],v[40:43]
s_waitcnt lgkmcnt(3)
v_rcp_f32 v45,v45
v_mfma_f32_16x16x32_bf16 v[40:43],a[100:103],v[160:163],v[40:43]
s_waitcnt vmcnt(10)
ds_read_b128 v[188:191],v2 offset:44032
v_fma_f32 v47,v62,v47,v61
v_mfma_f32_16x16x32_bf16 v[40:43],a[108:111],v[164:167],v[40:43]
s_barrier
v_mfma_f32_16x16x32_bf16 v[36:39],a[104:107],v[164:167],v[36:39]
s_cmp_lt_u32 s86,s89
ds_read_b128 v[128:131],v2 offset:53248
ds_read_b32 v16,v7 offset:256
v_mfma_f32_16x16x32_bf16 v[36:39],a[96:99],v[160:163],v[36:39]
s_mov_b32 m0,s62
v_rcp_f32 v46,v46
v_mfma_f32_16x16x32_bf16 v[36:39],a[112:115],v[168:171],v[36:39]
s_mov_b64 s[48:49],s[50:51]
ds_read_b128 v[132:135],v2 offset:54272
v_mul_f32 v47,v31,v47
v_mfma_f32_16x16x32_bf16 v[36:39],a[120:123],v[172:175],v[36:39]
s_mov_b64 s[50:51],s[52:53]
v_exp_f32 v47,v47
v_mfma_f32_16x16x32_bf16 v[40:43],a[124:127],v[172:175],v[40:43]
s_waitcnt lgkmcnt(1)
ds_read_b128 v[136:139],v2 offset:55296
v_fma_f32 v47,v47,v63,v63
v_mfma_f32_16x16x32_bf16 v[40:43],a[116:119],v[168:171],v[40:43]
v_rcp_f32 v47,v47
v_mfma_f32_16x16x32_bf16 v[40:43],a[68:71],v[176:179],v[40:43]
ds_read_b128 v[140:143],v2 offset:56320
v_mul_f32 v28,v28,v44
v_mfma_f32_16x16x32_bf16 v[40:43],a[76:79],v[180:183],v[40:43]
v_cmp_eq_u32 vcc,s60,v16
v_mul_f32 v29,v29,v45
v_mul_f32 v30,v30,v46
v_mul_f32 v31,v31,v47
v_mfma_f32_16x16x32_bf16 v[36:39],a[72:75],v[180:183],v[36:39]
s_cselect_b32 vcc_lo,-1,vcc_lo
ds_read_b128 v[144:147],v2 offset:49152
v_cvt_pk_bf16_f32 v52,v28,v29
v_mfma_f32_16x16x32_bf16 v[36:39],a[64:67],v[176:179],v[36:39]
s_cmp_eq_u32 vcc_lo,-1
v_cvt_pk_bf16_f32 v53,v30,v31
v_fma_f32 v32,-v60,v24,v32
v_mfma_f32_16x16x32_bf16 v[36:39],a[80:83],v[184:187],v[36:39]
s_cselect_b32 s66,s86,s59
ds_read_b128 v[148:151],v2 offset:50176
v_fma_f32 v33,-v60,v25,v33
v_mfma_f32_16x16x32_bf16 v[36:39],a[88:91],v[188:191],v[36:39]
s_add_u32 s74,s53,s55
v_fma_f32 v34,-v60,v26,v34
v_fma_f32 v35,-v60,v27,v35
v_mfma_f32_16x16x32_bf16 v[40:43],a[92:95],v[188:191],v[40:43]
s_cmp_eq_u32 s74,s56
ds_read_b128 v[152:155],v2 offset:51200
v_mul_f32 v48,v32,v32
v_mfma_f32_16x16x32_bf16 v[40:43],a[84:87],v[184:187],v[40:43]
s_waitcnt lgkmcnt(3)
v_mul_f32 v49,v33,v33
v_mul_f32 v50,v34,v34
v_mfma_f32_16x16x32_bf16 v[40:43],a[164:167],v[128:131],v[40:43]
s_cselect_b32 s74,s57,s74
ds_read_b128 v[156:159],v2 offset:52224
v_mul_f32 v51,v35,v35
v_mfma_f32_16x16x32_bf16 v[40:43],a[172:175],v[132:135],v[40:43]
s_cmp_eq_u32 s74,s58
v_fma_f32 v48,v62,v48,v61
v_mul_f32 v48,v32,v48
v_mfma_f32_16x16x32_bf16 v[36:39],a[168:171],v[132:135],v[36:39]
s_cselect_b32 s74,s59,s74
ds_read_b128 v[160:163],v2 offset:61440
v_fma_f32 v49,v62,v49,v61
v_mfma_f32_16x16x32_bf16 v[36:39],a[160:163],v[128:131],v[36:39]
s_xor_b32 s64,s64,65536
v_exp_f32 v48,v48
v_mfma_f32_16x16x32_bf16 v[36:39],a[176:179],v[136:139],v[36:39]
s_mov_b32 s52,s74
ds_read_b128 v[164:167],v2 offset:62464
v_mul_f32 v49,v33,v49
v_mfma_f32_16x16x32_bf16 v[36:39],a[184:187],v[140:143],v[36:39]
s_cmp_lt_u32 s67,4194304
v_exp_f32 v49,v49
v_mfma_f32_16x16x32_bf16 v[40:43],a[188:191],v[140:143],v[40:43]
ds_read_b128 v[168:171],v2 offset:63488
v_fma_f32 v48,v48,v63,v63
v_mfma_f32_16x16x32_bf16 v[40:43],a[180:183],v[136:139],v[40:43]
v_rcp_f32 v48,v48
v_mfma_f32_16x16x32_bf16 v[72:75],v[68:71],v[128:131],v[72:75]
v_fma_f32 v49,v49,v63,v63
v_fma_f32 v50,v62,v50,v61
v_mfma_f32_16x16x32_bf16 v[72:75],v[68:71],v[132:135],v[72:75]
v_rcp_f32 v49,v49
v_mfma_f32_16x16x32_bf16 v[72:75],v[68:71],v[136:139],v[72:75]
v_mul_f32 v50,v34,v50
v_fma_f32 v51,v62,v51,v61
v_mfma_f32_16x16x32_bf16 v[72:75],v[68:71],v[140:143],v[72:75]
v_exp_f32 v50,v50
v_mfma_f32_16x16x32_bf16 v[76:79],v[140:143],v[140:143],v[76:79]
v_mul_f32 v51,v35,v51
v_fma_f32 v50,v50,v63,v63
v_mfma_f32_16x16x32_bf16 v[76:79],v[136:139],v[136:139],v[76:79]
v_exp_f32 v51,v51
v_mfma_f32_16x16x32_bf16 v[76:79],v[132:135],v[132:135],v[76:79]
v_rcp_f32 v50,v50
v_mfma_f32_16x16x32_bf16 v[76:79],v[128:131],v[128:131],v[76:79]
s_waitcnt lgkmcnt(3)
v_fma_f32 v51,v51,v63,v63
v_mul_f32 v32,v32,v48
v_mfma_f32_16x16x32_bf16 v[40:43],a[132:135],v[144:147],v[40:43]
ds_read_b128 v[172:175],v2 offset:64512
v_mul_f32 v33,v33,v49)"
R"(
v_mfma_f32_16x16x32_bf16 v[40:43],a[140:143],v[148:151],v[40:43]
buffer_load_dwordx4 v0,s[36:39],s66 offen offset:0 sc1 lds
v_mfma_f32_16x16x32_bf16 v[36:39],a[136:139],v[148:151],v[36:39]
v_cndmask_b32 v80,v76,v77,s[82:83]
v_cndmask_b32 v81,v78,v79,s[82:83]
v_cndmask_b32 v80,v80,v81,s[84:85]
v_cndmask_b32 v80,v72,v80,s[98:99]
v_mfma_f32_16x16x32_bf16 v[36:39],a[128:131],v[144:147],v[36:39]
ds_read_b128 v[176:179],v2 offset:57344
ds_write_b32 v13,v80 offset:2048
v_mfma_f32_16x16x32_bf16 v[36:39],a[144:147],v[152:155],v[36:39]
ds_read_b128 v[180:183],v2 offset:58368
v_mul_f32 v34,v34,v50
v_mfma_f32_16x16x32_bf16 v[36:39],a[152:155],v[156:159],v[36:39]
buffer_load_dwordx4 v0,s[36:39],s66 offen offset:1024 sc1 lds
v_mfma_f32_16x16x32_bf16 v[40:43],a[156:159],v[156:159],v[40:43]
ds_read_b128 v[184:187],v2 offset:59392
v_cvt_pk_bf16_f32 v54,v32,v33
v_mfma_f32_16x16x32_bf16 v[40:43],a[148:151],v[152:155],v[40:43]
s_waitcnt lgkmcnt(4)
v_rcp_f32 v51,v51
v_mfma_f32_16x16x32_bf16 v[40:43],a[228:231],v[160:163],v[40:43]
s_waitcnt vmcnt(7) lgkmcnt(2)
ds_read_b128 v[188:191],v2 offset:60416
v_xor_b32 v2,65536,v2
v_mfma_f32_16x16x32_bf16 v[40:43],a[236:239],v[164:167],v[40:43]
s_barrier
v_mfma_f32_16x16x32_bf16 v[36:39],a[232:235],v[164:167],v[36:39]
ds_read_b128 v[84:87],v14 offset:2048
ds_read_b128 v[88:91],v14 offset:2064
v_mfma_f32_16x16x32_bf16 v[36:39],a[224:227],v[160:163],v[36:39]
ds_read_b128 v[128:131],v2 offset:4096
buffer_load_dwordx4 v0,s[36:39],s66 offen offset:2048 sc1 lds
v_mfma_f32_16x16x32_bf16 v[36:39],a[240:243],v[168:171],v[36:39]
ds_read_b128 v[132:135],v2 offset:5120
v_mul_f32 v35,v35,v51
v_mfma_f32_16x16x32_bf16 v[36:39],a[248:251],v[172:175],v[36:39]
v_cvt_pk_bf16_f32 v55,v34,v35
v_xor_b32 v8,16384,v8
v_mfma_f32_16x16x32_bf16 v[40:43],a[252:255],v[172:175],v[40:43]
s_waitcnt lgkmcnt(2)
ds_read_b128 v[136:139],v2 offset:6144
v_add_f32 v84,v84,v85
v_mfma_f32_16x16x32_bf16 v[40:43],a[244:247],v[168:171],v[40:43]
buffer_load_dwordx4 v0,s[36:39],s66 offen offset:3072 sc1 lds
v_mfma_f32_16x16x32_bf16 v[40:43],a[196:199],v[176:179],v[40:43]
ds_read_b128 v[140:143],v2 offset:7168
v_add_f32 v86,v86,v87
v_mfma_f32_16x16x32_bf16 v[40:43],a[204:207],v[180:183],v[40:43]
v_add_f32 v84,v84,v86
v_mul_f32 v64,s75,v84
v_mfma_f32_16x16x32_bf16 v[36:39],a[200:203],v[180:183],v[36:39]
ds_read_b128 v[144:147],v2 offset:0
v_add_f32 v88,v88,v89
v_mfma_f32_16x16x32_bf16 v[36:39],a[192:195],v[176:179],v[36:39]
buffer_store_dwordx4 v[52:55],v3,s[36:39],s73 offen offset:0 sc1
v_add_f32 v90,v90,v91
v_add_f32 v88,v88,v90
v_mul_f32 v88,s75,v88
v_mfma_f32_16x16x32_bf16 v[36:39],a[208:211],v[184:187],v[36:39]
ds_read_b128 v[148:151],v2 offset:1024
v_fma_f32 v88,-v64,v64,v88
v_mfma_f32_16x16x32_bf16 v[36:39],a[216:219],v[188:191],v[36:39]
v_max_f32 v88,0,v88
v_add_f32 v83,v15,v88
v_fma_f32 v88,v15,v83,v88
v_rsq_f32 v84,v88
v_mfma_f32_16x16x32_bf16 v[40:43],a[220:223],v[188:191],v[40:43]
s_waitcnt lgkmcnt(2)
ds_read_b128 v[152:155],v2 offset:2048
v_mul_f32 v65,s76,v84
v_mul_f32 v66,v84,v84
v_mul_f32 v67,v88,v84
v_mul_f32 v66,s77,v66
v_mul_f32 v66,v66,v65
v_mfma_f32_16x16x32_bf16 v[40:43],a[212:215],v[184:187],v[40:43]
s_cbranch_scc1 Lg1_a0w1_%=
s_cmp_ge_u32 s48,4194304
s_cselect_b64 s[90:91],s[94:95],s[90:91]
s_cselect_b64 s[92:93],s[96:97],s[92:93]
s_cmp_lt_u32 s67,s59
s_cbranch_scc1 Lg1_d0w1_%=
s_branch Lg1_slow0_%=
Lg1_a0w2_%=:
v_mfma_f32_16x16x32_bf16 v[32:35],a[68:71],v[128:131],0
s_sub_u32 s69,s66,1048576
ds_read_b128 v[156:159],v2 offset:15360
s_cmp_lt_u32 s69,2097152
v_mfma_f32_16x16x32_bf16 v[32:35],a[76:79],v[132:135],v[32:35]
s_cselect_b32 s89,3145728,s89
s_sub_u32 s69,s66,3145728
s_cmp_lt_u32 s69,1048576
v_mfma_f32_16x16x32_bf16 v[28:31],a[72:75],v[132:135],0
s_cselect_b32 s89,4194304,s89
ds_read_b128 v[160:163],v2 offset:0
s_add_u32 m0,s62,16384
v_mfma_f32_16x16x32_bf16 v[28:31],a[64:67],v[128:131],v[28:31]
buffer_load_dwordx4 v1,s[36:39],s66 offen offset:0 sc1 lds
v_mfma_f32_16x16x32_bf16 v[28:31],a[80:83],v[136:139],v[28:31]
s_lshr_b32 s73,s47,0
ds_read_b128 v[164:167],v2 offset:1024
v_fma_f32 v36,-v64,v20,v36
v_mfma_f32_16x16x32_bf16 v[28:31],a[88:91],v[140:143],v[28:31]
s_lshr_b32 s65,s48,6
v_fma_f32 v37,-v64,v21,v37
v_fma_f32 v38,-v64,v22,v38
v_mfma_f32_16x16x32_bf16 v[32:35],a[92:95],v[140:143],v[32:35]
s_lshr_b32 s72,s66,6
ds_read_b128 v[168:171],v2 offset:2048
v_fma_f32 v39,-v64,v23,v39
v_mfma_f32_16x16x32_bf16 v[32:35],a[84:87],v[136:139],v[32:35]
buffer_load_dwordx4 v1,s[36:39],s66 offen offset:1024 sc1 lds
v_mfma_f32_16x16x32_bf16 v[72:75],v[68:71],v[128:131],0
s_lshr_b32 s71,s62,2
v_mul_f32 v44,v36,v36
v_mul_f32 v45,v37,v37
v_mfma_f32_16x16x32_bf16 v[72:75],v[68:71],v[132:135],v[72:75]
s_cmp_lt_u32 s66,4194304
v_mul_f32 v46,v38,v38
v_mul_f32 v47,v39,v39
v_mfma_f32_16x16x32_bf16 v[72:75],v[68:71],v[136:139],v[72:75]
s_cselect_b32 s72,s59,s72
v_fma_f32 v44,v66,v44,v65
v_mul_f32 v44,v36,v44
v_mfma_f32_16x16x32_bf16 v[72:75],v[68:71],v[140:143],v[72:75]
buffer_load_dwordx4 v1,s[36:39],s66 offen offset:2048 sc1 lds
v_mfma_f32_16x16x32_bf16 v[76:79],v[140:143],v[140:143],0
v_exp_f32 v44,v44
v_mfma_f32_16x16x32_bf16 v[76:79],v[136:139],v[136:139],v[76:79]
v_fma_f32 v44,v44,v67,v67
v_fma_f32 v45,v66,v45,v65
v_mfma_f32_16x16x32_bf16 v[76:79],v[132:135],v[132:135],v[76:79]
v_rcp_f32 v44,v44
v_mfma_f32_16x16x32_bf16 v[76:79],v[128:131],v[128:131],v[76:79]
s_waitcnt lgkmcnt(3)
buffer_load_dwordx4 v1,s[36:39],s66 offen offset:3072 sc1 lds
v_mfma_f32_16x16x32_bf16 v[32:35],a[100:103],v[144:147],v[32:35]
s_add_u32 m0,s71,s88
ds_read_b128 v[172:175],v2 offset:3072
v_mul_f32 v45,v37,v45
v_mfma_f32_16x16x32_bf16 v[32:35],a[108:111],v[148:151],v[32:35]
buffer_load_dword v10,s[36:39],s72 offen offset:0 sc1 lds
v_mfma_f32_16x16x32_bf16 v[28:31],a[104:107],v[148:151],v[28:31]
s_xor_b32 s87,s64,65536
ds_read_b128 v[176:179],v2 offset:4096
v_fma_f32 v46,v66,v46,v65
v_mfma_f32_16x16x32_bf16 v[28:31],a[96:99],v[144:147],v[28:31]
s_cmp_ge_u32 s66,s59
v_exp_f32 v45,v45
v_mfma_f32_16x16x32_bf16 v[28:31],a[112:115],v[152:155],v[28:31]
s_cselect_b32 s86,s49,s50
ds_read_b128 v[180:183],v2 offset:5120
v_mul_f32 v46,v38,v46
v_mfma_f32_16x16x32_bf16 v[28:31],a[120:123],v[156:159],v[28:31]
s_cselect_b32 s87,s87,s63
v_exp_f32 v46,v46
v_mfma_f32_16x16x32_bf16 v[32:35],a[124:127],v[156:159],v[32:35]
s_mov_b32 s62,s87
ds_read_b128 v[184:187],v2 offset:6144
v_fma_f32 v45,v45,v67,v67
v_mfma_f32_16x16x32_bf16 v[32:35],a[116:119],v[152:155],v[32:35]
s_waitcnt lgkmcnt(3)
v_rcp_f32 v45,v45
v_mfma_f32_16x16x32_bf16 v[32:35],a[4:7],v[160:163],v[32:35]
s_waitcnt vmcnt(10)
ds_read_b128 v[188:191],v2 offset:7168
v_fma_f32 v46,v46,v67,v67
v_mfma_f32_16x16x32_bf16 v[32:35],a[12:15],v[164:167],v[32:35]
s_barrier
v_mfma_f32_16x16x32_bf16 v[28:31],a[8:11],v[164:167],v[28:31]
s_cmp_lt_u32 s86,s89
ds_read_b128 v[128:131],v2 offset:24576
ds_read_b32 v16,v7 offset:0
v_mfma_f32_16x16x32_bf16 v[28:31],a[0:3],v[160:163],v[28:31]
s_mov_b32 m0,s62
v_rcp_f32 v46,v46
v_mfma_f32_16x16x32_bf16 v[28:31],a[16:19],v[168:171],v[28:31]
ds_read_b128 v[132:135],v2 offset:25600
v_fma_f32 v47,v66,v47,v65
v_mfma_f32_16x16x32_bf16 v[28:31],a[24:27],v[172:175],v[28:31]
v_mul_f32 v47,v39,v47
v_mul_f32 v36,v36,v44
v_mfma_f32_16x16x32_bf16 v[32:35],a[28:31],v[172:175],v[32:35]
s_waitcnt lgkmcnt(1)
ds_read_b128 v[136:139],v2 offset:26624
v_mul_f32 v37,v37,v45
v_mfma_f32_16x16x32_bf16 v[32:35],a[20:23],v[168:171],v[32:35]
v_exp_f32 v47,v47
v_mfma_f32_16x16x32_bf16 v[32:35],a[36:39],v[176:179],v[32:35]
ds_read_b128 v[140:143],v2 offset:27648
v_fma_f32 v47,v47,v67,v67
v_mfma_f32_16x16x32_bf16 v[32:35],a[44:47],v[180:183],v[32:35]
v_cmp_eq_u32 vcc,s60,v16
v_rcp_f32 v47,v47
v_mul_f32 v38,v38,v46
v_mfma_f32_16x16x32_bf16 v[28:31],a[40:43],v[180:183],v[28:31]
s_cselect_b32 vcc_lo,-1,vcc_lo
ds_read_b128 v[144:147],v2 offset:28672
v_mul_f32 v39,v39,v47
v_mfma_f32_16x16x32_bf16 v[28:31],a[32:35],v[176:179],v[28:31])"
R"(
s_cmp_eq_u32 vcc_lo,-1
v_cvt_pk_bf16_f32 v52,v36,v37
v_cvt_pk_bf16_f32 v53,v38,v39
v_mfma_f32_16x16x32_bf16 v[28:31],a[48:51],v[184:187],v[28:31]
s_cselect_b32 s67,s86,s59
ds_read_b128 v[148:151],v2 offset:29696
v_fma_f32 v40,-v64,v24,v40
v_mfma_f32_16x16x32_bf16 v[28:31],a[56:59],v[188:191],v[28:31]
s_add_u32 s53,s52,s55
v_fma_f32 v41,-v64,v25,v41
v_fma_f32 v42,-v64,v26,v42
v_mfma_f32_16x16x32_bf16 v[32:35],a[60:63],v[188:191],v[32:35]
s_cmp_eq_u32 s53,s56
ds_read_b128 v[152:155],v2 offset:30720
v_fma_f32 v43,-v64,v27,v43
v_mfma_f32_16x16x32_bf16 v[32:35],a[52:55],v[184:187],v[32:35]
s_waitcnt lgkmcnt(3)
v_mul_f32 v48,v40,v40
v_mul_f32 v49,v41,v41
v_mfma_f32_16x16x32_bf16 v[32:35],a[196:199],v[128:131],v[32:35]
s_cselect_b32 s53,s57,s53
ds_read_b128 v[156:159],v2 offset:31744
v_mul_f32 v50,v42,v42
v_mfma_f32_16x16x32_bf16 v[32:35],a[204:207],v[132:135],v[32:35]
s_cmp_eq_u32 s53,s58
v_mul_f32 v51,v43,v43
v_fma_f32 v48,v66,v48,v65
v_mfma_f32_16x16x32_bf16 v[28:31],a[200:203],v[132:135],v[28:31]
s_cselect_b32 s53,s59,s53
ds_read_b128 v[160:163],v2 offset:16384
v_mul_f32 v48,v40,v48
v_mfma_f32_16x16x32_bf16 v[28:31],a[192:195],v[128:131],v[28:31]
s_xor_b32 s63,s63,65536
v_exp_f32 v48,v48
v_mfma_f32_16x16x32_bf16 v[28:31],a[208:211],v[136:139],v[28:31]
s_cmp_ge_u32 s66,s59
ds_read_b128 v[164:167],v2 offset:17408
v_fma_f32 v48,v48,v67,v67
v_mfma_f32_16x16x32_bf16 v[28:31],a[216:219],v[140:143],v[28:31]
v_rcp_f32 v48,v48
v_mfma_f32_16x16x32_bf16 v[32:35],a[220:223],v[140:143],v[32:35]
ds_read_b128 v[168:171],v2 offset:18432
v_fma_f32 v49,v66,v49,v65
v_mfma_f32_16x16x32_bf16 v[32:35],a[212:215],v[136:139],v[32:35]
v_mul_f32 v49,v41,v49
v_fma_f32 v50,v66,v50,v65
v_mfma_f32_16x16x32_bf16 v[72:75],v[68:71],v[128:131],v[72:75]
v_exp_f32 v49,v49
v_mfma_f32_16x16x32_bf16 v[72:75],v[68:71],v[132:135],v[72:75]
v_mul_f32 v50,v42,v50
v_fma_f32 v49,v49,v67,v67
v_mfma_f32_16x16x32_bf16 v[72:75],v[68:71],v[136:139],v[72:75]
v_exp_f32 v50,v50
v_mfma_f32_16x16x32_bf16 v[72:75],v[68:71],v[140:143],v[72:75]
v_rcp_f32 v49,v49
v_mfma_f32_16x16x32_bf16 v[76:79],v[140:143],v[140:143],v[76:79]
v_fma_f32 v50,v50,v67,v67
v_fma_f32 v51,v66,v51,v65
v_mfma_f32_16x16x32_bf16 v[76:79],v[136:139],v[136:139],v[76:79]
v_rcp_f32 v50,v50
v_mfma_f32_16x16x32_bf16 v[76:79],v[132:135],v[132:135],v[76:79]
v_mul_f32 v51,v43,v51
v_mul_f32 v40,v40,v48
v_mfma_f32_16x16x32_bf16 v[76:79],v[128:131],v[128:131],v[76:79]
s_waitcnt lgkmcnt(3)
v_exp_f32 v51,v51
v_mfma_f32_16x16x32_bf16 v[32:35],a[228:231],v[144:147],v[32:35]
ds_read_b128 v[172:175],v2 offset:19456
v_fma_f32 v51,v51,v67,v67
v_mfma_f32_16x16x32_bf16 v[32:35],a[236:239],v[148:151],v[32:35]
v_rcp_f32 v51,v51
v_mfma_f32_16x16x32_bf16 v[28:31],a[232:235],v[148:151],v[28:31]
buffer_load_dwordx4 v0,s[36:39],s67 offen offset:0 sc1 lds
v_cndmask_b32 v80,v76,v77,s[82:83]
v_cndmask_b32 v81,v78,v79,s[82:83]
v_cndmask_b32 v80,v80,v81,s[84:85]
v_cndmask_b32 v80,v72,v80,s[98:99]
v_mfma_f32_16x16x32_bf16 v[28:31],a[224:227],v[144:147],v[28:31]
ds_read_b128 v[176:179],v2 offset:20480
ds_write_b32 v13,v80 offset:0
v_mfma_f32_16x16x32_bf16 v[28:31],a[240:243],v[152:155],v[28:31]
ds_read_b128 v[180:183],v2 offset:21504
v_mul_f32 v41,v41,v49
v_mfma_f32_16x16x32_bf16 v[28:31],a[248:251],v[156:159],v[28:31]
v_mul_f32 v42,v42,v50
v_mul_f32 v43,v43,v51
v_mfma_f32_16x16x32_bf16 v[32:35],a[252:255],v[156:159],v[32:35]
buffer_load_dwordx4 v0,s[36:39],s67 offen offset:1024 sc1 lds
v_mfma_f32_16x16x32_bf16 v[32:35],a[244:247],v[152:155],v[32:35]
s_waitcnt lgkmcnt(3)
ds_read_b128 v[184:187],v2 offset:22528
v_cvt_pk_bf16_f32 v54,v40,v41
v_mfma_f32_16x16x32_bf16 v[32:35],a[132:135],v[160:163],v[32:35]
s_waitcnt vmcnt(7) lgkmcnt(2)
ds_read_b128 v[188:191],v2 offset:23552
v_cvt_pk_bf16_f32 v55,v42,v43
v_mfma_f32_16x16x32_bf16 v[32:35],a[140:143],v[164:167],v[32:35]
s_barrier
v_mfma_f32_16x16x32_bf16 v[28:31],a[136:139],v[164:167],v[28:31]
ds_read_b128 v[84:87],v14 offset:0
ds_read_b128 v[88:91],v14 offset:16
v_mfma_f32_16x16x32_bf16 v[28:31],a[128:131],v[160:163],v[28:31]
ds_read_b128 v[128:131],v2 offset:40960
v_mfma_f32_16x16x32_bf16 v[28:31],a[144:147],v[168:171],v[28:31]
buffer_load_dwordx4 v0,s[36:39],s67 offen offset:2048 sc1 lds
v_mfma_f32_16x16x32_bf16 v[28:31],a[152:155],v[172:175],v[28:31]
ds_read_b128 v[132:135],v2 offset:41984
v_mfma_f32_16x16x32_bf16 v[32:35],a[156:159],v[172:175],v[32:35]
s_waitcnt lgkmcnt(2)
ds_read_b128 v[136:139],v2 offset:43008
v_add_f32 v84,v84,v85
v_mfma_f32_16x16x32_bf16 v[32:35],a[148:151],v[168:171],v[32:35]
v_add_f32 v86,v86,v87
v_add_f32 v84,v84,v86
v_mfma_f32_16x16x32_bf16 v[32:35],a[164:167],v[176:179],v[32:35]
buffer_load_dwordx4 v0,s[36:39],s67 offen offset:3072 sc1 lds
v_mfma_f32_16x16x32_bf16 v[32:35],a[172:175],v[180:183],v[32:35]
ds_read_b128 v[140:143],v2 offset:44032
v_mul_f32 v60,s75,v84
v_mfma_f32_16x16x32_bf16 v[28:31],a[168:171],v[180:183],v[28:31]
ds_read_b128 v[144:147],v2 offset:45056
v_add_f32 v88,v88,v89
v_mfma_f32_16x16x32_bf16 v[28:31],a[160:163],v[176:179],v[28:31]
v_add_f32 v90,v90,v91
v_add_f32 v88,v88,v90
v_mfma_f32_16x16x32_bf16 v[28:31],a[176:179],v[184:187],v[28:31]
buffer_store_dwordx4 v[52:55],v3,s[36:39],s73 offen offset:0 sc1
v_mul_f32 v88,s75,v88
v_fma_f32 v88,-v60,v60,v88
v_max_f32 v88,0,v88
v_add_f32 v83,v15,v88
v_fma_f32 v88,v15,v83,v88
v_rsq_f32 v83,v83
v_mfma_f32_16x16x32_bf16 v[28:31],a[184:187],v[188:191],v[28:31]
ds_read_b128 v[148:151],v2 offset:46080
v_rsq_f32 v84,v88
v_mul_f32_e64 v82,-v60,v83
v_mfma_f32_16x16x32_bf16 v[32:35],a[188:191],v[188:191],v[32:35]
s_waitcnt lgkmcnt(2)
ds_read_b128 v[152:155],v2 offset:47104
buffer_store_dwordx2 v[82:83],v12,s[36:39],s65 offen offset:0 sc1
v_mul_f32 v61,s76,v84
v_mul_f32 v62,v84,v84
v_mul_f32 v63,v88,v84
v_mul_f32 v62,s77,v62
v_mul_f32 v62,v62,v61
v_mfma_f32_16x16x32_bf16 v[32:35],a[180:183],v[184:187],v[32:35]
s_cbranch_scc1 Lg1_slow1_%=
Lg1_a1w2_%=:
v_mfma_f32_16x16x32_bf16 v[40:43],a[68:71],v[128:131],0
s_sub_u32 s69,s67,1048576
ds_read_b128 v[156:159],v2 offset:48128
s_cmp_lt_u32 s69,2097152
v_mfma_f32_16x16x32_bf16 v[40:43],a[76:79],v[132:135],v[40:43]
s_cselect_b32 s89,3145728,s89
s_sub_u32 s69,s67,3145728
s_cmp_lt_u32 s69,1048576
v_mfma_f32_16x16x32_bf16 v[36:39],a[72:75],v[132:135],0
s_cselect_b32 s89,4194304,s89
ds_read_b128 v[160:163],v2 offset:32768
s_add_u32 m0,s62,16384
v_mfma_f32_16x16x32_bf16 v[36:39],a[64:67],v[128:131],v[36:39]
buffer_load_dwordx4 v1,s[36:39],s67 offen offset:0 sc1 lds
v_mfma_f32_16x16x32_bf16 v[36:39],a[80:83],v[136:139],v[36:39]
s_lshr_b32 s73,s48,0
ds_read_b128 v[164:167],v2 offset:33792
v_fma_f32 v28,-v60,v20,v28
v_mfma_f32_16x16x32_bf16 v[36:39],a[88:91],v[140:143],v[36:39]
s_lshr_b32 s65,s49,6
v_fma_f32 v29,-v60,v21,v29
v_fma_f32 v30,-v60,v22,v30
v_mfma_f32_16x16x32_bf16 v[40:43],a[92:95],v[140:143],v[40:43]
s_lshr_b32 s72,s67,6
ds_read_b128 v[168:171],v2 offset:34816
v_fma_f32 v31,-v60,v23,v31
v_mfma_f32_16x16x32_bf16 v[40:43],a[84:87],v[136:139],v[40:43]
buffer_load_dwordx4 v1,s[36:39],s67 offen offset:1024 sc1 lds
v_mfma_f32_16x16x32_bf16 v[72:75],v[68:71],v[128:131],0
s_lshr_b32 s71,s62,2
v_mul_f32 v44,v28,v28
v_mul_f32 v45,v29,v29
v_mfma_f32_16x16x32_bf16 v[72:75],v[68:71],v[132:135],v[72:75]
s_cmp_lt_u32 s67,4194304
v_mul_f32 v46,v30,v30
v_mul_f32 v47,v31,v31
v_mfma_f32_16x16x32_bf16 v[72:75],v[68:71],v[136:139],v[72:75]
s_cselect_b32 s72,s59,s72
v_fma_f32 v44,v62,v44,v61
v_mul_f32 v44,v28,v44
v_mfma_f32_16x16x32_bf16 v[72:75],v[68:71],v[140:143],v[72:75]
buffer_load_dwordx4 v1,s[36:39],s67 offen offset:2048 sc1 lds
v_mfma_f32_16x16x32_bf16 v[76:79],v[140:143],v[140:143],0
s_mov_b64 s[44:45],s[46:47]
v_exp_f32 v44,v44
v_mfma_f32_16x16x32_bf16 v[76:79],v[136:139],v[136:139],v[76:79]
s_mov_b64 s[46:47],s[48:49]
v_fma_f32 v44,v44,v63,v63
v_fma_f32 v45,v62,v45,v61
v_mfma_f32_16x16x32_bf16 v[76:79],v[132:135],v[132:135],v[76:79]
s_mov_b64 s[48:49],s[50:51])"
R"(
v_rcp_f32 v44,v44
v_mfma_f32_16x16x32_bf16 v[76:79],v[128:131],v[128:131],v[76:79]
s_waitcnt lgkmcnt(3)
buffer_load_dwordx4 v1,s[36:39],s67 offen offset:3072 sc1 lds
v_mfma_f32_16x16x32_bf16 v[40:43],a[100:103],v[144:147],v[40:43]
s_add_u32 m0,s71,s88
ds_read_b128 v[172:175],v2 offset:35840
v_mul_f32 v45,v29,v45
v_mfma_f32_16x16x32_bf16 v[40:43],a[108:111],v[148:151],v[40:43]
buffer_load_dword v10,s[36:39],s72 offen offset:0 sc1 lds
v_mfma_f32_16x16x32_bf16 v[36:39],a[104:107],v[148:151],v[36:39]
s_xor_b32 s87,s63,65536
ds_read_b128 v[176:179],v2 offset:36864
v_fma_f32 v46,v62,v46,v61
v_mfma_f32_16x16x32_bf16 v[36:39],a[96:99],v[144:147],v[36:39]
s_cmp_ge_u32 s67,s59
v_exp_f32 v45,v45
v_mfma_f32_16x16x32_bf16 v[36:39],a[112:115],v[152:155],v[36:39]
s_cselect_b32 s86,s50,s51
ds_read_b128 v[180:183],v2 offset:37888
v_mul_f32 v46,v30,v46
v_mfma_f32_16x16x32_bf16 v[36:39],a[120:123],v[156:159],v[36:39]
s_cselect_b32 s87,s87,s64
v_exp_f32 v46,v46
v_mfma_f32_16x16x32_bf16 v[40:43],a[124:127],v[156:159],v[40:43]
s_mov_b32 s62,s87
ds_read_b128 v[184:187],v2 offset:38912
v_fma_f32 v45,v45,v63,v63
v_mfma_f32_16x16x32_bf16 v[40:43],a[116:119],v[152:155],v[40:43]
s_waitcnt lgkmcnt(3)
v_rcp_f32 v45,v45
v_mfma_f32_16x16x32_bf16 v[40:43],a[4:7],v[160:163],v[40:43]
s_waitcnt vmcnt(10)
ds_read_b128 v[188:191],v2 offset:39936
v_fma_f32 v46,v46,v63,v63
v_mfma_f32_16x16x32_bf16 v[40:43],a[12:15],v[164:167],v[40:43]
s_barrier
v_mfma_f32_16x16x32_bf16 v[36:39],a[8:11],v[164:167],v[36:39]
s_cmp_lt_u32 s86,s89
ds_read_b128 v[128:131],v2 offset:57344
ds_read_b32 v16,v7 offset:256
v_mfma_f32_16x16x32_bf16 v[36:39],a[0:3],v[160:163],v[36:39]
s_mov_b32 m0,s62
v_rcp_f32 v46,v46
v_mfma_f32_16x16x32_bf16 v[36:39],a[16:19],v[168:171],v[36:39]
s_mov_b64 s[50:51],s[52:53]
ds_read_b128 v[132:135],v2 offset:58368
v_fma_f32 v47,v62,v47,v61
v_mfma_f32_16x16x32_bf16 v[36:39],a[24:27],v[172:175],v[36:39]
v_mul_f32 v47,v31,v47
v_mul_f32 v28,v28,v44
v_mfma_f32_16x16x32_bf16 v[40:43],a[28:31],v[172:175],v[40:43]
s_waitcnt lgkmcnt(1)
ds_read_b128 v[136:139],v2 offset:59392
v_mul_f32 v29,v29,v45
v_mfma_f32_16x16x32_bf16 v[40:43],a[20:23],v[168:171],v[40:43]
v_exp_f32 v47,v47
v_mfma_f32_16x16x32_bf16 v[40:43],a[36:39],v[176:179],v[40:43]
ds_read_b128 v[140:143],v2 offset:60416
v_fma_f32 v47,v47,v63,v63
v_mfma_f32_16x16x32_bf16 v[40:43],a[44:47],v[180:183],v[40:43]
v_cmp_eq_u32 vcc,s60,v16
v_rcp_f32 v47,v47
v_mul_f32 v30,v30,v46
v_mfma_f32_16x16x32_bf16 v[36:39],a[40:43],v[180:183],v[36:39]
s_cselect_b32 vcc_lo,-1,vcc_lo
ds_read_b128 v[144:147],v2 offset:61440
v_mul_f32 v31,v31,v47
v_mfma_f32_16x16x32_bf16 v[36:39],a[32:35],v[176:179],v[36:39]
s_cmp_eq_u32 vcc_lo,-1
v_cvt_pk_bf16_f32 v52,v28,v29
v_cvt_pk_bf16_f32 v53,v30,v31
v_mfma_f32_16x16x32_bf16 v[36:39],a[48:51],v[184:187],v[36:39]
s_cselect_b32 s66,s86,s59
ds_read_b128 v[148:151],v2 offset:62464
v_fma_f32 v32,-v60,v24,v32
v_mfma_f32_16x16x32_bf16 v[36:39],a[56:59],v[188:191],v[36:39]
s_add_u32 s74,s53,s55
v_fma_f32 v33,-v60,v25,v33
v_fma_f32 v34,-v60,v26,v34
v_mfma_f32_16x16x32_bf16 v[40:43],a[60:63],v[188:191],v[40:43]
s_cmp_eq_u32 s74,s56
ds_read_b128 v[152:155],v2 offset:63488
v_fma_f32 v35,-v60,v27,v35
v_mfma_f32_16x16x32_bf16 v[40:43],a[52:55],v[184:187],v[40:43]
s_waitcnt lgkmcnt(3)
v_mul_f32 v48,v32,v32
v_mul_f32 v49,v33,v33
v_mfma_f32_16x16x32_bf16 v[40:43],a[196:199],v[128:131],v[40:43]
s_cselect_b32 s74,s57,s74
ds_read_b128 v[156:159],v2 offset:64512
v_mul_f32 v50,v34,v34
v_mfma_f32_16x16x32_bf16 v[40:43],a[204:207],v[132:135],v[40:43]
s_cmp_eq_u32 s74,s58
v_mul_f32 v51,v35,v35
v_fma_f32 v48,v62,v48,v61
v_mfma_f32_16x16x32_bf16 v[36:39],a[200:203],v[132:135],v[36:39]
s_cselect_b32 s74,s59,s74
ds_read_b128 v[160:163],v2 offset:49152
v_mul_f32 v48,v32,v48
v_mfma_f32_16x16x32_bf16 v[36:39],a[192:195],v[128:131],v[36:39]
s_xor_b32 s64,s64,65536
v_exp_f32 v48,v48
v_mfma_f32_16x16x32_bf16 v[36:39],a[208:211],v[136:139],v[36:39]
s_mov_b32 s52,s74
ds_read_b128 v[164:167],v2 offset:50176
v_fma_f32 v48,v48,v63,v63
v_mfma_f32_16x16x32_bf16 v[36:39],a[216:219],v[140:143],v[36:39]
s_cmp_lt_u32 s67,4194304
v_rcp_f32 v48,v48
v_mfma_f32_16x16x32_bf16 v[40:43],a[220:223],v[140:143],v[40:43]
ds_read_b128 v[168:171],v2 offset:51200
v_fma_f32 v49,v62,v49,v61
v_mfma_f32_16x16x32_bf16 v[40:43],a[212:215],v[136:139],v[40:43]
v_mul_f32 v49,v33,v49
v_fma_f32 v50,v62,v50,v61
v_mfma_f32_16x16x32_bf16 v[72:75],v[68:71],v[128:131],v[72:75]
v_exp_f32 v49,v49
v_mfma_f32_16x16x32_bf16 v[72:75],v[68:71],v[132:135],v[72:75]
v_mul_f32 v50,v34,v50
v_fma_f32 v49,v49,v63,v63
v_mfma_f32_16x16x32_bf16 v[72:75],v[68:71],v[136:139],v[72:75]
v_exp_f32 v50,v50
v_mfma_f32_16x16x32_bf16 v[72:75],v[68:71],v[140:143],v[72:75]
v_rcp_f32 v49,v49
v_mfma_f32_16x16x32_bf16 v[76:79],v[140:143],v[140:143],v[76:79]
v_fma_f32 v50,v50,v63,v63
v_fma_f32 v51,v62,v51,v61
v_mfma_f32_16x16x32_bf16 v[76:79],v[136:139],v[136:139],v[76:79]
v_rcp_f32 v50,v50
v_mfma_f32_16x16x32_bf16 v[76:79],v[132:135],v[132:135],v[76:79]
v_mul_f32 v51,v35,v51
v_mul_f32 v32,v32,v48
v_mfma_f32_16x16x32_bf16 v[76:79],v[128:131],v[128:131],v[76:79]
s_waitcnt lgkmcnt(3)
v_exp_f32 v51,v51
v_mfma_f32_16x16x32_bf16 v[40:43],a[228:231],v[144:147],v[40:43]
ds_read_b128 v[172:175],v2 offset:52224
v_fma_f32 v51,v51,v63,v63
v_mfma_f32_16x16x32_bf16 v[40:43],a[236:239],v[148:151],v[40:43]
v_rcp_f32 v51,v51
v_mfma_f32_16x16x32_bf16 v[36:39],a[232:235],v[148:151],v[36:39]
buffer_load_dwordx4 v0,s[36:39],s66 offen offset:0 sc1 lds
v_cndmask_b32 v80,v76,v77,s[82:83]
v_cndmask_b32 v81,v78,v79,s[82:83]
v_cndmask_b32 v80,v80,v81,s[84:85]
v_cndmask_b32 v80,v72,v80,s[98:99]
v_mfma_f32_16x16x32_bf16 v[36:39],a[224:227],v[144:147],v[36:39]
ds_read_b128 v[176:179],v2 offset:53248
ds_write_b32 v13,v80 offset:2048
v_mfma_f32_16x16x32_bf16 v[36:39],a[240:243],v[152:155],v[36:39]
ds_read_b128 v[180:183],v2 offset:54272
v_mul_f32 v33,v33,v49
v_mfma_f32_16x16x32_bf16 v[36:39],a[248:251],v[156:159],v[36:39]
v_mul_f32 v34,v34,v50
v_mul_f32 v35,v35,v51
v_mfma_f32_16x16x32_bf16 v[40:43],a[252:255],v[156:159],v[40:43]
buffer_load_dwordx4 v0,s[36:39],s66 offen offset:1024 sc1 lds
v_mfma_f32_16x16x32_bf16 v[40:43],a[244:247],v[152:155],v[40:43]
s_waitcnt lgkmcnt(3)
ds_read_b128 v[184:187],v2 offset:55296
v_cvt_pk_bf16_f32 v54,v32,v33
v_mfma_f32_16x16x32_bf16 v[40:43],a[132:135],v[160:163],v[40:43]
s_waitcnt vmcnt(7) lgkmcnt(2)
ds_read_b128 v[188:191],v2 offset:56320
v_xor_b32 v2,65536,v2
v_mfma_f32_16x16x32_bf16 v[40:43],a[140:143],v[164:167],v[40:43]
s_barrier
v_mfma_f32_16x16x32_bf16 v[36:39],a[136:139],v[164:167],v[36:39]
ds_read_b128 v[84:87],v14 offset:2048
ds_read_b128 v[88:91],v14 offset:2064
v_mfma_f32_16x16x32_bf16 v[36:39],a[128:131],v[160:163],v[36:39]
ds_read_b128 v[128:131],v2 offset:8192
v_cvt_pk_bf16_f32 v55,v34,v35
v_mfma_f32_16x16x32_bf16 v[36:39],a[144:147],v[168:171],v[36:39]
buffer_load_dwordx4 v0,s[36:39],s66 offen offset:2048 sc1 lds
v_mfma_f32_16x16x32_bf16 v[36:39],a[152:155],v[172:175],v[36:39]
ds_read_b128 v[132:135],v2 offset:9216
v_xor_b32 v8,16384,v8
v_mfma_f32_16x16x32_bf16 v[40:43],a[156:159],v[172:175],v[40:43]
s_waitcnt lgkmcnt(2)
ds_read_b128 v[136:139],v2 offset:10240
v_add_f32 v84,v84,v85
v_mfma_f32_16x16x32_bf16 v[40:43],a[148:151],v[168:171],v[40:43]
v_add_f32 v86,v86,v87
v_add_f32 v84,v84,v86
v_mfma_f32_16x16x32_bf16 v[40:43],a[164:167],v[176:179],v[40:43]
buffer_load_dwordx4 v0,s[36:39],s66 offen offset:3072 sc1 lds
v_mfma_f32_16x16x32_bf16 v[40:43],a[172:175],v[180:183],v[40:43]
ds_read_b128 v[140:143],v2 offset:11264
v_mul_f32 v64,s75,v84
v_mfma_f32_16x16x32_bf16 v[36:39],a[168:171],v[180:183],v[36:39]
ds_read_b128 v[144:147],v2 offset:12288
v_add_f32 v88,v88,v89
v_mfma_f32_16x16x32_bf16 v[36:39],a[160:163],v[176:179],v[36:39]
v_add_f32 v90,v90,v91
v_add_f32 v88,v88,v90
v_mfma_f32_16x16x32_bf16 v[36:39],a[176:179],v[184:187],v[36:39]
buffer_store_dwordx4 v[52:55],v3,s[36:39],s73 offen offset:0 sc1
v_mul_f32 v88,s75,v88)"
R"(
v_fma_f32 v88,-v64,v64,v88
v_max_f32 v88,0,v88
v_add_f32 v83,v15,v88
v_fma_f32 v88,v15,v83,v88
v_rsq_f32 v83,v83
v_mfma_f32_16x16x32_bf16 v[36:39],a[184:187],v[188:191],v[36:39]
ds_read_b128 v[148:151],v2 offset:13312
v_rsq_f32 v84,v88
v_mul_f32_e64 v82,-v64,v83
v_mfma_f32_16x16x32_bf16 v[40:43],a[188:191],v[188:191],v[40:43]
s_waitcnt lgkmcnt(2)
ds_read_b128 v[152:155],v2 offset:14336
buffer_store_dwordx2 v[82:83],v12,s[36:39],s65 offen offset:0 sc1
v_mul_f32 v65,s76,v84
v_mul_f32 v66,v84,v84
v_mul_f32 v67,v88,v84
v_mul_f32 v66,s77,v66
v_mul_f32 v66,v66,v65
v_mfma_f32_16x16x32_bf16 v[40:43],a[180:183],v[184:187],v[40:43]
s_cbranch_scc1 Lg1_a0w2_%=
s_cmp_ge_u32 s48,4194304
s_cselect_b64 s[90:91],s[94:95],s[90:91]
s_cselect_b64 s[92:93],s[96:97],s[92:93]
s_cmp_lt_u32 s67,s59
s_cbranch_scc1 Lg1_d0w2_%=
s_branch Lg1_slow0_%=
Lg1_a0w3_%=:
v_mfma_f32_16x16x32_bf16 v[32:35],a[100:103],v[128:131],0
s_sub_u32 s69,s66,1048576
ds_read_b128 v[156:159],v2 offset:11264
s_cmp_lt_u32 s69,2097152
v_mfma_f32_16x16x32_bf16 v[32:35],a[108:111],v[132:135],v[32:35]
s_cselect_b32 s89,3145728,s89
s_sub_u32 s69,s66,3145728
s_cmp_lt_u32 s69,1048576
v_mfma_f32_16x16x32_bf16 v[28:31],a[104:107],v[132:135],0
s_cselect_b32 s89,4194304,s89
ds_read_b128 v[160:163],v2 offset:4096
s_add_u32 m0,s62,16384
v_mfma_f32_16x16x32_bf16 v[28:31],a[96:99],v[128:131],v[28:31]
s_lshr_b32 s73,s47,0
v_fma_f32 v36,-v64,v20,v36
v_fma_f32 v37,-v64,v21,v37
v_mfma_f32_16x16x32_bf16 v[28:31],a[112:115],v[136:139],v[28:31]
buffer_load_dwordx4 v1,s[36:39],s66 offen offset:0 sc1 lds
v_mfma_f32_16x16x32_bf16 v[28:31],a[120:123],v[140:143],v[28:31]
s_lshr_b32 s72,s66,6
ds_read_b128 v[164:167],v2 offset:5120
v_fma_f32 v38,-v64,v22,v38
v_mfma_f32_16x16x32_bf16 v[32:35],a[124:127],v[140:143],v[32:35]
s_lshr_b32 s71,s62,2
ds_read_b128 v[168:171],v2 offset:6144
v_fma_f32 v39,-v64,v23,v39
v_mfma_f32_16x16x32_bf16 v[32:35],a[116:119],v[136:139],v[32:35]
s_cmp_lt_u32 s66,4194304
v_mul_f32 v44,v36,v36
v_mul_f32 v45,v37,v37
v_mfma_f32_16x16x32_bf16 v[72:75],v[68:71],v[128:131],0
buffer_load_dwordx4 v1,s[36:39],s66 offen offset:1024 sc1 lds
v_mfma_f32_16x16x32_bf16 v[72:75],v[68:71],v[132:135],v[72:75]
s_cselect_b32 s72,s59,s72
v_mul_f32 v46,v38,v38
v_mul_f32 v47,v39,v39
v_mfma_f32_16x16x32_bf16 v[72:75],v[68:71],v[136:139],v[72:75]
v_fma_f32 v44,v66,v44,v65
v_mul_f32 v44,v36,v44
v_mfma_f32_16x16x32_bf16 v[72:75],v[68:71],v[140:143],v[72:75]
v_exp_f32 v44,v44
v_mfma_f32_16x16x32_bf16 v[76:79],v[140:143],v[140:143],0
buffer_load_dwordx4 v1,s[36:39],s66 offen offset:2048 sc1 lds
v_mfma_f32_16x16x32_bf16 v[76:79],v[136:139],v[136:139],v[76:79]
v_fma_f32 v44,v44,v67,v67
v_fma_f32 v45,v66,v45,v65
v_mfma_f32_16x16x32_bf16 v[76:79],v[132:135],v[132:135],v[76:79]
v_rcp_f32 v44,v44
v_mfma_f32_16x16x32_bf16 v[76:79],v[128:131],v[128:131],v[76:79]
s_waitcnt lgkmcnt(3)
v_mul_f32 v45,v37,v45
v_fma_f32 v46,v66,v46,v65
v_mfma_f32_16x16x32_bf16 v[32:35],a[68:71],v[144:147],v[32:35]
buffer_load_dwordx4 v1,s[36:39],s66 offen offset:3072 sc1 lds
v_mfma_f32_16x16x32_bf16 v[32:35],a[76:79],v[148:151],v[32:35]
s_add_u32 m0,s71,s88
ds_read_b128 v[172:175],v2 offset:7168
v_mul_f32 v46,v38,v46
v_mfma_f32_16x16x32_bf16 v[28:31],a[72:75],v[148:151],v[28:31]
buffer_load_dword v10,s[36:39],s72 offen offset:0 sc1 lds
v_mfma_f32_16x16x32_bf16 v[28:31],a[64:67],v[144:147],v[28:31]
s_xor_b32 s87,s64,65536
ds_read_b128 v[176:179],v2 offset:0
v_fma_f32 v47,v66,v47,v65
v_mfma_f32_16x16x32_bf16 v[28:31],a[80:83],v[152:155],v[28:31]
s_cmp_ge_u32 s66,s59
ds_read_b128 v[180:183],v2 offset:1024
v_mul_f32 v47,v39,v47
v_mfma_f32_16x16x32_bf16 v[28:31],a[88:91],v[156:159],v[28:31]
s_cselect_b32 s86,s49,s50
v_exp_f32 v45,v45
v_mfma_f32_16x16x32_bf16 v[32:35],a[92:95],v[156:159],v[32:35]
s_cselect_b32 s87,s87,s63
ds_read_b128 v[184:187],v2 offset:2048
v_fma_f32 v45,v45,v67,v67
v_mfma_f32_16x16x32_bf16 v[32:35],a[84:87],v[152:155],v[32:35]
s_waitcnt lgkmcnt(3)
v_exp_f32 v46,v46
v_mfma_f32_16x16x32_bf16 v[32:35],a[36:39],v[160:163],v[32:35]
s_waitcnt vmcnt(11)
ds_read_b128 v[188:191],v2 offset:3072
v_fma_f32 v46,v46,v67,v67
v_mfma_f32_16x16x32_bf16 v[32:35],a[44:47],v[164:167],v[32:35]
s_barrier
v_mfma_f32_16x16x32_bf16 v[28:31],a[40:43],v[164:167],v[28:31]
s_mov_b32 s62,s87
ds_read_b128 v[128:131],v2 offset:28672
ds_read_b32 v16,v7 offset:0
v_mfma_f32_16x16x32_bf16 v[28:31],a[32:35],v[160:163],v[28:31]
s_cmp_lt_u32 s86,s89
v_exp_f32 v47,v47
v_mfma_f32_16x16x32_bf16 v[28:31],a[48:51],v[168:171],v[28:31]
s_mov_b32 m0,s62
ds_read_b128 v[132:135],v2 offset:29696
v_fma_f32 v47,v47,v67,v67
v_mfma_f32_16x16x32_bf16 v[28:31],a[56:59],v[172:175],v[28:31]
v_rcp_f32 v45,v45
v_mfma_f32_16x16x32_bf16 v[32:35],a[60:63],v[172:175],v[32:35]
s_waitcnt lgkmcnt(1)
ds_read_b128 v[136:139],v2 offset:30720
v_mul_f32 v36,v36,v44
v_mfma_f32_16x16x32_bf16 v[32:35],a[52:55],v[168:171],v[32:35]
v_rcp_f32 v46,v46
v_mfma_f32_16x16x32_bf16 v[32:35],a[4:7],v[176:179],v[32:35]
ds_read_b128 v[140:143],v2 offset:31744
v_mul_f32 v37,v37,v45
v_mfma_f32_16x16x32_bf16 v[32:35],a[12:15],v[180:183],v[32:35]
v_cmp_eq_u32 vcc,s60,v16
v_rcp_f32 v47,v47
v_mul_f32 v38,v38,v46
v_mfma_f32_16x16x32_bf16 v[28:31],a[8:11],v[180:183],v[28:31]
s_cselect_b32 vcc_lo,-1,vcc_lo
ds_read_b128 v[144:147],v2 offset:24576
v_mul_f32 v39,v39,v47
v_mfma_f32_16x16x32_bf16 v[28:31],a[0:3],v[176:179],v[28:31]
s_cmp_eq_u32 vcc_lo,-1
v_cvt_pk_bf16_f32 v52,v36,v37
v_cvt_pk_bf16_f32 v53,v38,v39
v_mfma_f32_16x16x32_bf16 v[28:31],a[16:19],v[184:187],v[28:31]
s_cselect_b32 s67,s86,s59
ds_read_b128 v[148:151],v2 offset:25600
v_fma_f32 v40,-v64,v24,v40
v_mfma_f32_16x16x32_bf16 v[28:31],a[24:27],v[188:191],v[28:31]
s_add_u32 s53,s52,s55
v_fma_f32 v41,-v64,v25,v41
v_fma_f32 v42,-v64,v26,v42
v_mfma_f32_16x16x32_bf16 v[32:35],a[28:31],v[188:191],v[32:35]
s_cmp_eq_u32 s53,s56
ds_read_b128 v[152:155],v2 offset:26624
v_fma_f32 v43,-v64,v27,v43
v_mfma_f32_16x16x32_bf16 v[32:35],a[20:23],v[184:187],v[32:35]
s_waitcnt lgkmcnt(3)
v_mul_f32 v48,v40,v40
v_mul_f32 v49,v41,v41
v_mfma_f32_16x16x32_bf16 v[32:35],a[228:231],v[128:131],v[32:35]
s_cselect_b32 s53,s57,s53
ds_read_b128 v[156:159],v2 offset:27648
v_mul_f32 v50,v42,v42
v_mfma_f32_16x16x32_bf16 v[32:35],a[236:239],v[132:135],v[32:35]
s_cmp_eq_u32 s53,s58
v_mul_f32 v51,v43,v43
v_fma_f32 v48,v66,v48,v65
v_mfma_f32_16x16x32_bf16 v[28:31],a[232:235],v[132:135],v[28:31]
s_cselect_b32 s53,s59,s53
ds_read_b128 v[160:163],v2 offset:20480
v_mul_f32 v48,v40,v48
v_mfma_f32_16x16x32_bf16 v[28:31],a[224:227],v[128:131],v[28:31]
s_xor_b32 s63,s63,65536
v_exp_f32 v48,v48
v_mfma_f32_16x16x32_bf16 v[28:31],a[240:243],v[136:139],v[28:31]
s_lshr_b32 s70,s46,5
ds_read_b128 v[164:167],v2 offset:21504
v_fma_f32 v48,v48,v67,v67
v_mfma_f32_16x16x32_bf16 v[28:31],a[248:251],v[140:143],v[28:31]
s_cmp_ge_u32 s66,s59
v_rcp_f32 v48,v48
v_mfma_f32_16x16x32_bf16 v[32:35],a[252:255],v[140:143],v[32:35]
ds_read_b128 v[168:171],v2 offset:22528
v_fma_f32 v49,v66,v49,v65
v_mfma_f32_16x16x32_bf16 v[32:35],a[244:247],v[136:139],v[32:35]
v_mul_f32 v49,v41,v49
v_fma_f32 v50,v66,v50,v65
v_mfma_f32_16x16x32_bf16 v[72:75],v[68:71],v[128:131],v[72:75]
v_exp_f32 v49,v49
v_mfma_f32_16x16x32_bf16 v[72:75],v[68:71],v[132:135],v[72:75]
v_mul_f32 v50,v42,v50
v_fma_f32 v49,v49,v67,v67
v_mfma_f32_16x16x32_bf16 v[72:75],v[68:71],v[136:139],v[72:75]
v_exp_f32 v50,v50
v_mfma_f32_16x16x32_bf16 v[72:75],v[68:71],v[140:143],v[72:75]
v_rcp_f32 v49,v49
v_mfma_f32_16x16x32_bf16 v[76:79],v[140:143],v[140:143],v[76:79]
v_fma_f32 v50,v50,v67,v67)"
R"(
v_fma_f32 v51,v66,v51,v65
v_mfma_f32_16x16x32_bf16 v[76:79],v[136:139],v[136:139],v[76:79]
v_rcp_f32 v50,v50
v_mfma_f32_16x16x32_bf16 v[76:79],v[132:135],v[132:135],v[76:79]
v_mul_f32 v51,v43,v51
v_mul_f32 v40,v40,v48
v_mfma_f32_16x16x32_bf16 v[76:79],v[128:131],v[128:131],v[76:79]
s_waitcnt lgkmcnt(3)
v_exp_f32 v51,v51
v_mfma_f32_16x16x32_bf16 v[32:35],a[196:199],v[144:147],v[32:35]
ds_read_b128 v[172:175],v2 offset:23552
v_fma_f32 v51,v51,v67,v67
v_mfma_f32_16x16x32_bf16 v[32:35],a[204:207],v[148:151],v[32:35]
v_rcp_f32 v51,v51
v_mfma_f32_16x16x32_bf16 v[28:31],a[200:203],v[148:151],v[28:31]
v_cndmask_b32 v80,v76,v77,s[82:83]
v_cndmask_b32 v81,v78,v79,s[82:83]
v_cndmask_b32 v80,v80,v81,s[84:85]
v_cndmask_b32 v80,v72,v80,s[98:99]
v_mfma_f32_16x16x32_bf16 v[28:31],a[192:195],v[144:147],v[28:31]
ds_read_b128 v[176:179],v2 offset:16384
ds_write_b32 v13,v80 offset:0
buffer_load_dwordx4 v0,s[36:39],s67 offen offset:0 sc1 lds
v_mfma_f32_16x16x32_bf16 v[28:31],a[208:211],v[152:155],v[28:31]
ds_read_b128 v[180:183],v2 offset:17408
v_mul_f32 v41,v41,v49
v_mfma_f32_16x16x32_bf16 v[28:31],a[216:219],v[156:159],v[28:31]
v_mul_f32 v42,v42,v50
v_mul_f32 v43,v43,v51
v_mfma_f32_16x16x32_bf16 v[32:35],a[220:223],v[156:159],v[32:35]
ds_read_b128 v[184:187],v2 offset:18432
v_cvt_pk_bf16_f32 v54,v40,v41
v_mfma_f32_16x16x32_bf16 v[32:35],a[212:215],v[152:155],v[32:35]
s_waitcnt lgkmcnt(4)
buffer_load_dwordx4 v0,s[36:39],s67 offen offset:1024 sc1 lds
v_mfma_f32_16x16x32_bf16 v[32:35],a[164:167],v[160:163],v[32:35]
s_waitcnt vmcnt(7) lgkmcnt(2)
ds_read_b128 v[188:191],v2 offset:19456
v_cvt_pk_bf16_f32 v55,v42,v43
v_mfma_f32_16x16x32_bf16 v[32:35],a[172:175],v[164:167],v[32:35]
s_barrier
v_mfma_f32_16x16x32_bf16 v[28:31],a[168:171],v[164:167],v[28:31]
ds_read_b128 v[84:87],v14 offset:0
ds_read_b128 v[88:91],v14 offset:16
v_mfma_f32_16x16x32_bf16 v[28:31],a[160:163],v[160:163],v[28:31]
ds_read_b128 v[128:131],v2 offset:45056
v_mfma_f32_16x16x32_bf16 v[28:31],a[176:179],v[168:171],v[28:31]
ds_read_b128 v[132:135],v2 offset:46080
v_mfma_f32_16x16x32_bf16 v[28:31],a[184:187],v[172:175],v[28:31]
buffer_store_dword v5,v4,s[36:39],s70 offen offset:0 sc1
buffer_load_dwordx4 v0,s[36:39],s67 offen offset:2048 sc1 lds
v_mfma_f32_16x16x32_bf16 v[32:35],a[188:191],v[172:175],v[32:35]
s_waitcnt lgkmcnt(2)
ds_read_b128 v[136:139],v2 offset:47104
v_add_f32 v84,v84,v85
v_mfma_f32_16x16x32_bf16 v[32:35],a[180:183],v[168:171],v[32:35]
v_add_f32 v86,v86,v87
v_add_f32 v84,v84,v86
v_mfma_f32_16x16x32_bf16 v[32:35],a[132:135],v[176:179],v[32:35]
ds_read_b128 v[140:143],v2 offset:48128
v_mul_f32 v60,s75,v84
v_mfma_f32_16x16x32_bf16 v[32:35],a[140:143],v[180:183],v[32:35]
buffer_load_dwordx4 v0,s[36:39],s67 offen offset:3072 sc1 lds
v_mfma_f32_16x16x32_bf16 v[28:31],a[136:139],v[180:183],v[28:31]
ds_read_b128 v[144:147],v2 offset:40960
v_add_f32 v88,v88,v89
v_mfma_f32_16x16x32_bf16 v[28:31],a[128:131],v[176:179],v[28:31]
v_add_f32 v90,v90,v91
v_add_f32 v88,v88,v90
v_mfma_f32_16x16x32_bf16 v[28:31],a[144:147],v[184:187],v[28:31]
ds_read_b128 v[148:151],v2 offset:41984
v_mul_f32 v88,s75,v88
v_mfma_f32_16x16x32_bf16 v[28:31],a[152:155],v[188:191],v[28:31]
buffer_store_dwordx4 v[52:55],v3,s[36:39],s73 offen offset:0 sc1
v_fma_f32 v88,-v60,v60,v88
v_max_f32 v88,0,v88
v_add_f32 v83,v15,v88
v_fma_f32 v88,v15,v83,v88
v_rsq_f32 v84,v88
v_mfma_f32_16x16x32_bf16 v[32:35],a[156:159],v[188:191],v[32:35]
s_waitcnt lgkmcnt(2)
ds_read_b128 v[152:155],v2 offset:43008
v_mul_f32 v61,s76,v84
v_mul_f32 v62,v84,v84
v_mul_f32 v63,v88,v84
v_mul_f32 v62,s77,v62
v_mul_f32 v62,v62,v61
v_mfma_f32_16x16x32_bf16 v[32:35],a[148:151],v[184:187],v[32:35]
s_cbranch_scc1 Lg1_slow1_%=
Lg1_a1w3_%=:
v_mfma_f32_16x16x32_bf16 v[40:43],a[100:103],v[128:131],0
s_sub_u32 s69,s67,1048576
ds_read_b128 v[156:159],v2 offset:44032
s_cmp_lt_u32 s69,2097152
v_mfma_f32_16x16x32_bf16 v[40:43],a[108:111],v[132:135],v[40:43]
s_cselect_b32 s89,3145728,s89
s_sub_u32 s69,s67,3145728
s_cmp_lt_u32 s69,1048576
v_mfma_f32_16x16x32_bf16 v[36:39],a[104:107],v[132:135],0
s_cselect_b32 s89,4194304,s89
ds_read_b128 v[160:163],v2 offset:36864
s_add_u32 m0,s62,16384
v_mfma_f32_16x16x32_bf16 v[36:39],a[96:99],v[128:131],v[36:39]
s_lshr_b32 s73,s48,0
v_fma_f32 v28,-v60,v20,v28
v_fma_f32 v29,-v60,v21,v29
v_mfma_f32_16x16x32_bf16 v[36:39],a[112:115],v[136:139],v[36:39]
buffer_load_dwordx4 v1,s[36:39],s67 offen offset:0 sc1 lds
v_mfma_f32_16x16x32_bf16 v[36:39],a[120:123],v[140:143],v[36:39]
s_lshr_b32 s72,s67,6
ds_read_b128 v[164:167],v2 offset:37888
v_fma_f32 v30,-v60,v22,v30
v_mfma_f32_16x16x32_bf16 v[40:43],a[124:127],v[140:143],v[40:43]
s_lshr_b32 s71,s62,2
ds_read_b128 v[168:171],v2 offset:38912
v_fma_f32 v31,-v60,v23,v31
v_mfma_f32_16x16x32_bf16 v[40:43],a[116:119],v[136:139],v[40:43]
s_cmp_lt_u32 s67,4194304
v_mul_f32 v44,v28,v28
v_mul_f32 v45,v29,v29
v_mfma_f32_16x16x32_bf16 v[72:75],v[68:71],v[128:131],0
buffer_load_dwordx4 v1,s[36:39],s67 offen offset:1024 sc1 lds
v_mfma_f32_16x16x32_bf16 v[72:75],v[68:71],v[132:135],v[72:75]
s_cselect_b32 s72,s59,s72
v_mul_f32 v46,v30,v30
v_mul_f32 v47,v31,v31
v_mfma_f32_16x16x32_bf16 v[72:75],v[68:71],v[136:139],v[72:75]
s_mov_b64 s[44:45],s[46:47]
v_fma_f32 v44,v62,v44,v61
v_mul_f32 v44,v28,v44
v_mfma_f32_16x16x32_bf16 v[72:75],v[68:71],v[140:143],v[72:75]
v_exp_f32 v44,v44
v_mfma_f32_16x16x32_bf16 v[76:79],v[140:143],v[140:143],0
buffer_load_dwordx4 v1,s[36:39],s67 offen offset:2048 sc1 lds
v_mfma_f32_16x16x32_bf16 v[76:79],v[136:139],v[136:139],v[76:79]
v_fma_f32 v44,v44,v63,v63
v_fma_f32 v45,v62,v45,v61
v_mfma_f32_16x16x32_bf16 v[76:79],v[132:135],v[132:135],v[76:79]
v_rcp_f32 v44,v44
v_mfma_f32_16x16x32_bf16 v[76:79],v[128:131],v[128:131],v[76:79]
s_waitcnt lgkmcnt(3)
v_mul_f32 v45,v29,v45
v_fma_f32 v46,v62,v46,v61
v_mfma_f32_16x16x32_bf16 v[40:43],a[68:71],v[144:147],v[40:43]
buffer_load_dwordx4 v1,s[36:39],s67 offen offset:3072 sc1 lds
v_mfma_f32_16x16x32_bf16 v[40:43],a[76:79],v[148:151],v[40:43]
s_add_u32 m0,s71,s88
ds_read_b128 v[172:175],v2 offset:39936
v_mul_f32 v46,v30,v46
v_mfma_f32_16x16x32_bf16 v[36:39],a[72:75],v[148:151],v[36:39]
buffer_load_dword v10,s[36:39],s72 offen offset:0 sc1 lds
v_mfma_f32_16x16x32_bf16 v[36:39],a[64:67],v[144:147],v[36:39]
s_xor_b32 s87,s63,65536
ds_read_b128 v[176:179],v2 offset:32768
v_fma_f32 v47,v62,v47,v61
v_mfma_f32_16x16x32_bf16 v[36:39],a[80:83],v[152:155],v[36:39]
s_cmp_ge_u32 s67,s59
ds_read_b128 v[180:183],v2 offset:33792
v_mul_f32 v47,v31,v47
v_mfma_f32_16x16x32_bf16 v[36:39],a[88:91],v[156:159],v[36:39]
s_cselect_b32 s86,s50,s51
v_exp_f32 v45,v45
v_mfma_f32_16x16x32_bf16 v[40:43],a[92:95],v[156:159],v[40:43]
s_cselect_b32 s87,s87,s64
ds_read_b128 v[184:187],v2 offset:34816
v_fma_f32 v45,v45,v63,v63
v_mfma_f32_16x16x32_bf16 v[40:43],a[84:87],v[152:155],v[40:43]
s_waitcnt lgkmcnt(3)
v_exp_f32 v46,v46
v_mfma_f32_16x16x32_bf16 v[40:43],a[36:39],v[160:163],v[40:43]
s_waitcnt vmcnt(11)
ds_read_b128 v[188:191],v2 offset:35840
v_fma_f32 v46,v46,v63,v63
v_mfma_f32_16x16x32_bf16 v[40:43],a[44:47],v[164:167],v[40:43]
s_barrier
v_mfma_f32_16x16x32_bf16 v[36:39],a[40:43],v[164:167],v[36:39]
s_mov_b32 s62,s87
ds_read_b128 v[128:131],v2 offset:61440
ds_read_b32 v16,v7 offset:256
v_mfma_f32_16x16x32_bf16 v[36:39],a[32:35],v[160:163],v[36:39]
s_cmp_lt_u32 s86,s89
v_exp_f32 v47,v47
v_mfma_f32_16x16x32_bf16 v[36:39],a[48:51],v[168:171],v[36:39]
s_mov_b32 m0,s62
ds_read_b128 v[132:135],v2 offset:62464
v_fma_f32 v47,v47,v63,v63
v_mfma_f32_16x16x32_bf16 v[36:39],a[56:59],v[172:175],v[36:39]
v_rcp_f32 v45,v45
v_mfma_f32_16x16x32_bf16 v[40:43],a[60:63],v[172:175],v[40:43]
s_waitcnt lgkmcnt(1)
ds_read_b128 v[136:139],v2 offset:63488
v_mul_f32 v28,v28,v44
v_mfma_f32_16x16x32_bf16 v[40:43],a[52:55],v[168:171],v[40:43]
v_rcp_f32 v46,v46
v_mfma_f32_16x16x32_bf16 v[40:43],a[4:7],v[176:179],v[40:43]
ds_read_b128 v[140:143],v2 offset:64512
v_mul_f32 v29,v29,v45
v_mfma_f32_16x16x32_bf16 v[40:43],a[12:15],v[180:183],v[40:43]
v_cmp_eq_u32 vcc,s60,v16
v_rcp_f32 v47,v47)"
R"(
v_mul_f32 v30,v30,v46
v_mfma_f32_16x16x32_bf16 v[36:39],a[8:11],v[180:183],v[36:39]
s_cselect_b32 vcc_lo,-1,vcc_lo
ds_read_b128 v[144:147],v2 offset:57344
v_mul_f32 v31,v31,v47
v_mfma_f32_16x16x32_bf16 v[36:39],a[0:3],v[176:179],v[36:39]
s_cmp_eq_u32 vcc_lo,-1
v_cvt_pk_bf16_f32 v52,v28,v29
v_cvt_pk_bf16_f32 v53,v30,v31
v_mfma_f32_16x16x32_bf16 v[36:39],a[16:19],v[184:187],v[36:39]
s_cselect_b32 s66,s86,s59
ds_read_b128 v[148:151],v2 offset:58368
v_fma_f32 v32,-v60,v24,v32
v_mfma_f32_16x16x32_bf16 v[36:39],a[24:27],v[188:191],v[36:39]
s_add_u32 s74,s53,s55
v_fma_f32 v33,-v60,v25,v33
v_fma_f32 v34,-v60,v26,v34
v_mfma_f32_16x16x32_bf16 v[40:43],a[28:31],v[188:191],v[40:43]
s_cmp_eq_u32 s74,s56
ds_read_b128 v[152:155],v2 offset:59392
v_fma_f32 v35,-v60,v27,v35
v_mfma_f32_16x16x32_bf16 v[40:43],a[20:23],v[184:187],v[40:43]
s_waitcnt lgkmcnt(3)
v_mul_f32 v48,v32,v32
v_mul_f32 v49,v33,v33
v_mfma_f32_16x16x32_bf16 v[40:43],a[228:231],v[128:131],v[40:43]
s_cselect_b32 s74,s57,s74
ds_read_b128 v[156:159],v2 offset:60416
v_mul_f32 v50,v34,v34
v_mfma_f32_16x16x32_bf16 v[40:43],a[236:239],v[132:135],v[40:43]
s_cmp_eq_u32 s74,s58
v_mul_f32 v51,v35,v35
v_fma_f32 v48,v62,v48,v61
v_mfma_f32_16x16x32_bf16 v[36:39],a[232:235],v[132:135],v[36:39]
s_cselect_b32 s74,s59,s74
ds_read_b128 v[160:163],v2 offset:53248
v_mul_f32 v48,v32,v48
v_mfma_f32_16x16x32_bf16 v[36:39],a[224:227],v[128:131],v[36:39]
s_xor_b32 s64,s64,65536
v_exp_f32 v48,v48
v_mfma_f32_16x16x32_bf16 v[36:39],a[240:243],v[136:139],v[36:39]
s_lshr_b32 s70,s47,5
ds_read_b128 v[164:167],v2 offset:54272
v_fma_f32 v48,v48,v63,v63
v_mfma_f32_16x16x32_bf16 v[36:39],a[248:251],v[140:143],v[36:39]
s_mov_b64 s[46:47],s[48:49]
v_rcp_f32 v48,v48
v_mfma_f32_16x16x32_bf16 v[40:43],a[252:255],v[140:143],v[40:43]
s_mov_b64 s[48:49],s[50:51]
ds_read_b128 v[168:171],v2 offset:55296
v_fma_f32 v49,v62,v49,v61
v_mfma_f32_16x16x32_bf16 v[40:43],a[244:247],v[136:139],v[40:43]
s_mov_b64 s[50:51],s[52:53]
v_mul_f32 v49,v33,v49
v_fma_f32 v50,v62,v50,v61
v_mfma_f32_16x16x32_bf16 v[72:75],v[68:71],v[128:131],v[72:75]
s_mov_b32 s52,s74
v_exp_f32 v49,v49
v_mfma_f32_16x16x32_bf16 v[72:75],v[68:71],v[132:135],v[72:75]
s_cmp_lt_u32 s67,4194304
v_mul_f32 v50,v34,v50
v_fma_f32 v49,v49,v63,v63
v_mfma_f32_16x16x32_bf16 v[72:75],v[68:71],v[136:139],v[72:75]
v_exp_f32 v50,v50
v_mfma_f32_16x16x32_bf16 v[72:75],v[68:71],v[140:143],v[72:75]
v_rcp_f32 v49,v49
v_mfma_f32_16x16x32_bf16 v[76:79],v[140:143],v[140:143],v[76:79]
v_fma_f32 v50,v50,v63,v63
v_fma_f32 v51,v62,v51,v61
v_mfma_f32_16x16x32_bf16 v[76:79],v[136:139],v[136:139],v[76:79]
v_rcp_f32 v50,v50
v_mfma_f32_16x16x32_bf16 v[76:79],v[132:135],v[132:135],v[76:79]
v_mul_f32 v51,v35,v51
v_mul_f32 v32,v32,v48
v_mfma_f32_16x16x32_bf16 v[76:79],v[128:131],v[128:131],v[76:79]
s_waitcnt lgkmcnt(3)
v_exp_f32 v51,v51
v_mfma_f32_16x16x32_bf16 v[40:43],a[196:199],v[144:147],v[40:43]
ds_read_b128 v[172:175],v2 offset:56320
v_fma_f32 v51,v51,v63,v63
v_mfma_f32_16x16x32_bf16 v[40:43],a[204:207],v[148:151],v[40:43]
v_rcp_f32 v51,v51
v_mfma_f32_16x16x32_bf16 v[36:39],a[200:203],v[148:151],v[36:39]
v_cndmask_b32 v80,v76,v77,s[82:83]
v_cndmask_b32 v81,v78,v79,s[82:83]
v_cndmask_b32 v80,v80,v81,s[84:85]
v_cndmask_b32 v80,v72,v80,s[98:99]
v_mfma_f32_16x16x32_bf16 v[36:39],a[192:195],v[144:147],v[36:39]
ds_read_b128 v[176:179],v2 offset:49152
ds_write_b32 v13,v80 offset:2048
buffer_load_dwordx4 v0,s[36:39],s66 offen offset:0 sc1 lds
v_mfma_f32_16x16x32_bf16 v[36:39],a[208:211],v[152:155],v[36:39]
ds_read_b128 v[180:183],v2 offset:50176
v_mul_f32 v33,v33,v49
v_mfma_f32_16x16x32_bf16 v[36:39],a[216:219],v[156:159],v[36:39]
v_mul_f32 v34,v34,v50
v_mul_f32 v35,v35,v51
v_mfma_f32_16x16x32_bf16 v[40:43],a[220:223],v[156:159],v[40:43]
ds_read_b128 v[184:187],v2 offset:51200
v_cvt_pk_bf16_f32 v54,v32,v33
v_mfma_f32_16x16x32_bf16 v[40:43],a[212:215],v[152:155],v[40:43]
s_waitcnt lgkmcnt(4)
buffer_load_dwordx4 v0,s[36:39],s66 offen offset:1024 sc1 lds
v_mfma_f32_16x16x32_bf16 v[40:43],a[164:167],v[160:163],v[40:43]
s_waitcnt vmcnt(7) lgkmcnt(2)
ds_read_b128 v[188:191],v2 offset:52224
v_xor_b32 v2,65536,v2
v_mfma_f32_16x16x32_bf16 v[40:43],a[172:175],v[164:167],v[40:43]
s_barrier
v_mfma_f32_16x16x32_bf16 v[36:39],a[168:171],v[164:167],v[36:39]
ds_read_b128 v[84:87],v14 offset:2048
ds_read_b128 v[88:91],v14 offset:2064
v_mfma_f32_16x16x32_bf16 v[36:39],a[160:163],v[160:163],v[36:39]
ds_read_b128 v[128:131],v2 offset:12288
v_cvt_pk_bf16_f32 v55,v34,v35
v_mfma_f32_16x16x32_bf16 v[36:39],a[176:179],v[168:171],v[36:39]
ds_read_b128 v[132:135],v2 offset:13312
v_xor_b32 v8,16384,v8
v_mfma_f32_16x16x32_bf16 v[36:39],a[184:187],v[172:175],v[36:39]
buffer_store_dword v5,v4,s[36:39],s70 offen offset:0 sc1
buffer_load_dwordx4 v0,s[36:39],s66 offen offset:2048 sc1 lds
v_mfma_f32_16x16x32_bf16 v[40:43],a[188:191],v[172:175],v[40:43]
s_waitcnt lgkmcnt(2)
ds_read_b128 v[136:139],v2 offset:14336
v_add_f32 v84,v84,v85
v_mfma_f32_16x16x32_bf16 v[40:43],a[180:183],v[168:171],v[40:43]
v_add_f32 v86,v86,v87
v_add_f32 v84,v84,v86
v_mfma_f32_16x16x32_bf16 v[40:43],a[132:135],v[176:179],v[40:43]
ds_read_b128 v[140:143],v2 offset:15360
v_mul_f32 v64,s75,v84
v_mfma_f32_16x16x32_bf16 v[40:43],a[140:143],v[180:183],v[40:43]
buffer_load_dwordx4 v0,s[36:39],s66 offen offset:3072 sc1 lds
v_mfma_f32_16x16x32_bf16 v[36:39],a[136:139],v[180:183],v[36:39]
ds_read_b128 v[144:147],v2 offset:8192
v_add_f32 v88,v88,v89
v_mfma_f32_16x16x32_bf16 v[36:39],a[128:131],v[176:179],v[36:39]
v_add_f32 v90,v90,v91
v_add_f32 v88,v88,v90
v_mfma_f32_16x16x32_bf16 v[36:39],a[144:147],v[184:187],v[36:39]
ds_read_b128 v[148:151],v2 offset:9216
v_mul_f32 v88,s75,v88
v_mfma_f32_16x16x32_bf16 v[36:39],a[152:155],v[188:191],v[36:39]
buffer_store_dwordx4 v[52:55],v3,s[36:39],s73 offen offset:0 sc1
v_fma_f32 v88,-v64,v64,v88
v_max_f32 v88,0,v88
v_add_f32 v83,v15,v88
v_fma_f32 v88,v15,v83,v88
v_rsq_f32 v84,v88
v_mfma_f32_16x16x32_bf16 v[40:43],a[156:159],v[188:191],v[40:43]
s_waitcnt lgkmcnt(2)
ds_read_b128 v[152:155],v2 offset:10240
v_mul_f32 v65,s76,v84
v_mul_f32 v66,v84,v84
v_mul_f32 v67,v88,v84
v_mul_f32 v66,s77,v66
v_mul_f32 v66,v66,v65
v_mfma_f32_16x16x32_bf16 v[40:43],a[148:151],v[184:187],v[40:43]
s_cbranch_scc1 Lg1_a0w3_%=
s_cmp_ge_u32 s48,4194304
s_cselect_b64 s[90:91],s[94:95],s[90:91]
s_cselect_b64 s[92:93],s[96:97],s[92:93]
s_cmp_lt_u32 s67,s59
s_cbranch_scc1 Lg1_d0w3_%=
s_branch Lg1_slow0_%=
Lg1_d0w0_%=:
v_mfma_f32_16x16x32_bf16 v[32:35],a[4:7],v[128:131],0
s_add_u32 m0,s62,16384
ds_read_b128 v[156:159],v2 offset:7168
s_lshr_b32 s73,s47,0
v_mfma_f32_16x16x32_bf16 v[32:35],a[12:15],v[132:135],v[32:35]
buffer_load_dwordx4 v1,s[36:39],s66 offen offset:0 sc1 lds
v_mfma_f32_16x16x32_bf16 v[28:31],a[8:11],v[132:135],0
s_lshr_b32 s72,s66,6
ds_read_b128 v[160:163],v2 offset:8192
s_lshr_b32 s71,s62,2
v_mfma_f32_16x16x32_bf16 v[28:31],a[0:3],v[128:131],v[28:31]
s_cmp_lt_u32 s66,4194304
v_fma_f32 v36,-v64,v20,v36
v_fma_f32 v37,-v64,v21,v37
v_mfma_f32_16x16x32_bf16 v[28:31],a[16:19],v[136:139],v[28:31]
s_cselect_b32 s72,s59,s72
ds_read_b128 v[164:167],v2 offset:9216
v_fma_f32 v38,-v64,v22,v38
v_mfma_f32_16x16x32_bf16 v[28:31],a[24:27],v[140:143],v[28:31]
buffer_load_dwordx4 v1,s[36:39],s66 offen offset:1024 sc1 lds
v_fma_f32 v39,-v64,v23,v39
v_mul_f32 v44,v36,v36
v_mul_f32 v45,v37,v37
v_mfma_f32_16x16x32_bf16 v[32:35],a[28:31],v[140:143],v[32:35]
ds_read_b128 v[168:171],v2 offset:10240
v_mul_f32 v46,v38,v38
v_mfma_f32_16x16x32_bf16 v[32:35],a[20:23],v[136:139],v[32:35]
s_waitcnt lgkmcnt(3)
v_mul_f32 v47,v39,v39
v_fma_f32 v44,v66,v44,v65
v_mfma_f32_16x16x32_bf16 v[32:35],a[36:39],v[144:147],v[32:35]
ds_read_b128 v[172:175],v2 offset:11264
v_mul_f32 v44,v36,v44
v_mfma_f32_16x16x32_bf16 v[32:35],a[44:47],v[148:151],v[32:35])"
R"(
buffer_load_dwordx4 v1,s[36:39],s66 offen offset:2048 sc1 lds
v_exp_f32 v44,v44
v_mfma_f32_16x16x32_bf16 v[28:31],a[40:43],v[148:151],v[28:31]
ds_read_b128 v[176:179],v2 offset:12288
v_fma_f32 v44,v44,v67,v67
v_mfma_f32_16x16x32_bf16 v[28:31],a[32:35],v[144:147],v[28:31]
v_rcp_f32 v44,v44
v_mfma_f32_16x16x32_bf16 v[28:31],a[48:51],v[152:155],v[28:31]
ds_read_b128 v[180:183],v2 offset:13312
v_fma_f32 v45,v66,v45,v65
v_mfma_f32_16x16x32_bf16 v[28:31],a[56:59],v[156:159],v[28:31]
buffer_load_dwordx4 v1,s[36:39],s66 offen offset:3072 sc1 lds
v_mul_f32 v45,v37,v45
v_exp_f32 v45,v45
v_mfma_f32_16x16x32_bf16 v[32:35],a[60:63],v[156:159],v[32:35]
s_add_u32 m0,s71,s88
ds_read_b128 v[184:187],v2 offset:14336
v_fma_f32 v45,v45,v67,v67
v_mfma_f32_16x16x32_bf16 v[32:35],a[52:55],v[152:155],v[32:35]
s_waitcnt lgkmcnt(3)
buffer_load_dword v10,s[36:39],s72 offen offset:0 sc1 lds
v_mfma_f32_16x16x32_bf16 v[32:35],a[68:71],v[160:163],v[32:35]
s_waitcnt vmcnt(11)
ds_read_b128 v[188:191],v2 offset:15360
v_fma_f32 v46,v66,v46,v65
v_mfma_f32_16x16x32_bf16 v[32:35],a[76:79],v[164:167],v[32:35]
s_barrier
v_mfma_f32_16x16x32_bf16 v[28:31],a[72:75],v[164:167],v[28:31]
s_xor_b32 s87,s64,65536
ds_read_b128 v[128:131],v2 offset:16384
v_mul_f32 v46,v38,v46
v_mfma_f32_16x16x32_bf16 v[28:31],a[64:67],v[160:163],v[28:31]
s_cmp_ge_u32 s66,s59
v_exp_f32 v46,v46
v_mfma_f32_16x16x32_bf16 v[28:31],a[80:83],v[168:171],v[28:31]
s_cselect_b32 s86,s49,s50
ds_read_b128 v[132:135],v2 offset:17408
v_fma_f32 v46,v46,v67,v67
v_mfma_f32_16x16x32_bf16 v[28:31],a[88:91],v[172:175],v[28:31]
s_cselect_b32 s87,s87,s63
v_rcp_f32 v45,v45
v_rcp_f32 v46,v46
v_mfma_f32_16x16x32_bf16 v[32:35],a[92:95],v[172:175],v[32:35]
s_lshr_b32 s69,s50,5
ds_read_b128 v[136:139],v2 offset:18432
v_fma_f32 v47,v66,v47,v65
v_mfma_f32_16x16x32_bf16 v[32:35],a[84:87],v[168:171],v[32:35]
s_waitcnt lgkmcnt(3)
v_mul_f32 v47,v39,v47
v_mul_f32 v36,v36,v44
v_mfma_f32_16x16x32_bf16 v[32:35],a[100:103],v[176:179],v[32:35]
s_cmp_lt_u32 s50,4194304
ds_read_b128 v[140:143],v2 offset:19456
v_mul_f32 v37,v37,v45
v_mfma_f32_16x16x32_bf16 v[32:35],a[108:111],v[180:183],v[32:35]
s_cselect_b32 s68,0xffffcfff,-1
v_exp_f32 v47,v47
v_mul_f32 v38,v38,v46
v_cvt_pk_bf16_f32 v52,v36,v37
v_fma_f32 v40,-v64,v24,v40
v_mfma_f32_16x16x32_bf16 v[28:31],a[104:107],v[180:183],v[28:31]
s_and_b32 s68,s68,s69
ds_read_b128 v[144:147],v2 offset:20480
v_fma_f32 v47,v47,v67,v67
v_mfma_f32_16x16x32_bf16 v[28:31],a[96:99],v[176:179],v[28:31]
s_lshr_b32 s69,s51,5
v_rcp_f32 v47,v47
v_mfma_f32_16x16x32_bf16 v[28:31],a[112:115],v[184:187],v[28:31]
s_cmp_lt_u32 s51,4194304
ds_read_b128 v[148:151],v2 offset:21504
v_mul_f32 v39,v39,v47
v_mfma_f32_16x16x32_bf16 v[28:31],a[120:123],v[188:191],v[28:31]
s_cselect_b32 s70,0xffffcfff,-1
v_cvt_pk_bf16_f32 v53,v38,v39
v_fma_f32 v41,-v64,v25,v41
v_fma_f32 v42,-v64,v26,v42
v_fma_f32 v43,-v64,v27,v43
v_mfma_f32_16x16x32_bf16 v[32:35],a[124:127],v[188:191],v[32:35]
s_and_b32 s70,s70,s69
ds_read_b128 v[152:155],v2 offset:22528
v_mul_f32 v48,v40,v40
v_mfma_f32_16x16x32_bf16 v[32:35],a[116:119],v[184:187],v[32:35]
s_waitcnt lgkmcnt(3)
v_mul_f32 v49,v41,v41
v_mul_f32 v50,v42,v42
v_mfma_f32_16x16x32_bf16 v[32:35],a[132:135],v[128:131],v[32:35]
s_waitcnt vmcnt(10)
ds_read_b128 v[156:159],v2 offset:23552
v_mul_f32 v51,v43,v43
v_mfma_f32_16x16x32_bf16 v[32:35],a[140:143],v[132:135],v[32:35]
s_barrier
v_mfma_f32_16x16x32_bf16 v[28:31],a[136:139],v[132:135],v[28:31]
s_mov_b32 s62,s87
ds_read_b128 v[160:163],v2 offset:24576
ds_read_b32 v16,v7 offset:0
v_mfma_f32_16x16x32_bf16 v[28:31],a[128:131],v[128:131],v[28:31]
v_fma_f32 v48,v66,v48,v65
v_mul_f32 v48,v40,v48
v_mfma_f32_16x16x32_bf16 v[28:31],a[144:147],v[136:139],v[28:31]
ds_read_b128 v[164:167],v2 offset:25600
v_fma_f32 v49,v66,v49,v65
v_mfma_f32_16x16x32_bf16 v[28:31],a[152:155],v[140:143],v[28:31]
v_exp_f32 v48,v48
v_mul_f32 v49,v41,v49
v_exp_f32 v49,v49
v_mfma_f32_16x16x32_bf16 v[32:35],a[156:159],v[140:143],v[32:35]
s_waitcnt lgkmcnt(1)
ds_read_b128 v[168:171],v2 offset:26624
v_fma_f32 v48,v48,v67,v67
v_mfma_f32_16x16x32_bf16 v[32:35],a[148:151],v[136:139],v[32:35]
v_rcp_f32 v48,v48
v_mfma_f32_16x16x32_bf16 v[32:35],a[164:167],v[144:147],v[32:35]
v_cmp_eq_u32 vcc,s60,v16
v_fma_f32 v49,v49,v67,v67
v_rcp_f32 v49,v49
v_mfma_f32_16x16x32_bf16 v[32:35],a[172:175],v[148:151],v[32:35]
s_cmp_eq_u32 vcc_lo,-1
s_cselect_b32 s67,s86,s59
s_cmp_ge_u32 s67,s59
s_cselect_b32 s68,s68,s70
ds_read_b128 v[172:175],v2 offset:27648
s_add_u32 m0,s78,256
v_mfma_f32_16x16x32_bf16 v[28:31],a[168:171],v[148:151],v[28:31]
buffer_load_dword v6,s[36:39],s68 offen offset:0 sc1 lds
v_mfma_f32_16x16x32_bf16 v[28:31],a[160:163],v[144:147],v[28:31]
s_mov_b32 m0,s62
ds_read_b128 v[176:179],v2 offset:28672
v_fma_f32 v50,v66,v50,v65
v_mfma_f32_16x16x32_bf16 v[28:31],a[176:179],v[152:155],v[28:31]
buffer_load_dwordx4 v0,s[36:39],s67 offen offset:0 sc1 lds
v_mfma_f32_16x16x32_bf16 v[28:31],a[184:187],v[156:159],v[28:31]
s_xor_b32 s63,s63,65536
ds_read_b128 v[180:183],v2 offset:29696
v_mul_f32 v50,v42,v50
v_exp_f32 v50,v50
v_fma_f32 v51,v66,v51,v65
v_mfma_f32_16x16x32_bf16 v[32:35],a[188:191],v[156:159],v[32:35]
s_add_u32 s53,s52,s55
ds_read_b128 v[184:187],v2 offset:30720
v_mul_f32 v51,v43,v51
v_mfma_f32_16x16x32_bf16 v[32:35],a[180:183],v[152:155],v[32:35]
s_waitcnt lgkmcnt(3)
v_exp_f32 v51,v51
v_mfma_f32_16x16x32_bf16 v[32:35],a[196:199],v[160:163],v[32:35]
s_waitcnt vmcnt(7)
ds_read_b128 v[188:191],v2 offset:31744
v_fma_f32 v50,v50,v67,v67
v_mfma_f32_16x16x32_bf16 v[32:35],a[204:207],v[164:167],v[32:35]
s_barrier
v_mfma_f32_16x16x32_bf16 v[28:31],a[200:203],v[164:167],v[28:31]
buffer_load_dwordx4 v0,s[36:39],s67 offen offset:1024 sc1 lds
v_mfma_f32_16x16x32_bf16 v[28:31],a[192:195],v[160:163],v[28:31]
s_cmp_eq_u32 s53,s56
ds_read_b128 v[128:131],v2 offset:32768
ds_read_b128 v[60:63],v8 offset:0
v_mfma_f32_16x16x32_bf16 v[28:31],a[208:211],v[168:171],v[28:31]
s_cselect_b32 s53,s57,s53
ds_read_b128 v[132:135],v2 offset:33792
v_fma_f32 v51,v51,v67,v67
v_mfma_f32_16x16x32_bf16 v[28:31],a[216:219],v[172:175],v[28:31]
s_cmp_eq_u32 s53,s58
v_rcp_f32 v50,v50
v_rcp_f32 v51,v51
v_mfma_f32_16x16x32_bf16 v[32:35],a[220:223],v[172:175],v[32:35]
buffer_load_dwordx4 v0,s[36:39],s67 offen offset:2048 sc1 lds
v_mfma_f32_16x16x32_bf16 v[32:35],a[212:215],v[168:171],v[32:35]
s_waitcnt lgkmcnt(3)
ds_read_b128 v[136:139],v2 offset:34816
v_mul_f32 v40,v40,v48
v_mul_f32 v41,v41,v49
v_mul_f32 v42,v42,v50
v_mul_f32 v43,v43,v51
v_cvt_pk_bf16_f32 v54,v40,v41
v_cvt_pk_bf16_f32 v55,v42,v43
v_mfma_f32_16x16x32_bf16 v[32:35],a[228:231],v[176:179],v[32:35]
buffer_store_dwordx4 v[52:55],v3,s[36:39],s73 offen offset:0 sc1
ds_read_b128 v[140:143],v2 offset:35840
v_mfma_f32_16x16x32_bf16 v[32:35],a[236:239],v[180:183],v[32:35]
s_waitcnt lgkmcnt(3)
s_cselect_b32 s53,s59,s53
s_cmp_ge_u32 s66,s59
v_mfma_f32_16x16x32_bf16 v[28:31],a[232:235],v[180:183],v[28:31]
buffer_load_dwordx4 v0,s[36:39],s67 offen offset:3072 sc1 lds
v_mfma_f32_16x16x32_bf16 v[28:31],a[224:227],v[176:179],v[28:31]
ds_read_b128 v[144:147],v2 offset:36864
v_mfma_f32_16x16x32_bf16 v[28:31],a[240:243],v[184:187],v[28:31]
ds_read_b128 v[148:151],v2 offset:37888
v_mfma_f32_16x16x32_bf16 v[28:31],a[248:251],v[188:191],v[28:31]
v_mfma_f32_16x16x32_bf16 v[32:35],a[252:255],v[188:191],v[32:35]
s_waitcnt lgkmcnt(2)
ds_read_b128 v[152:155],v2 offset:38912
v_mfma_f32_16x16x32_bf16 v[32:35],a[244:247],v[184:187],v[32:35]
s_cbranch_scc1 Lg1_slow1_%=
Lg1_d1w0_%=:
v_mfma_f32_16x16x32_bf16 v[40:43],a[4:7],v[128:131],0
s_add_u32 m0,s62,16384
ds_read_b128 v[156:159],v2 offset:39936
s_lshr_b32 s73,s48,0
v_mfma_f32_16x16x32_bf16 v[40:43],a[12:15],v[132:135],v[40:43]
buffer_load_dwordx4 v1,s[36:39],s67 offen offset:0 sc1 lds)"
R"(
v_mfma_f32_16x16x32_bf16 v[36:39],a[8:11],v[132:135],0
s_lshr_b32 s72,s67,6
ds_read_b128 v[160:163],v2 offset:40960
s_lshr_b32 s71,s62,2
v_mfma_f32_16x16x32_bf16 v[36:39],a[0:3],v[128:131],v[36:39]
s_cmp_lt_u32 s67,4194304
v_fma_f32 v28,-v60,v20,v28
v_fma_f32 v29,-v60,v21,v29
v_mfma_f32_16x16x32_bf16 v[36:39],a[16:19],v[136:139],v[36:39]
s_cselect_b32 s72,s59,s72
ds_read_b128 v[164:167],v2 offset:41984
v_fma_f32 v30,-v60,v22,v30
v_mfma_f32_16x16x32_bf16 v[36:39],a[24:27],v[140:143],v[36:39]
buffer_load_dwordx4 v1,s[36:39],s67 offen offset:1024 sc1 lds
v_fma_f32 v31,-v60,v23,v31
v_mul_f32 v44,v28,v28
v_mul_f32 v45,v29,v29
v_mfma_f32_16x16x32_bf16 v[40:43],a[28:31],v[140:143],v[40:43]
s_mov_b64 s[44:45],s[46:47]
ds_read_b128 v[168:171],v2 offset:43008
v_mul_f32 v46,v30,v30
v_mfma_f32_16x16x32_bf16 v[40:43],a[20:23],v[136:139],v[40:43]
s_waitcnt lgkmcnt(3)
v_mul_f32 v47,v31,v31
v_fma_f32 v44,v62,v44,v61
v_mfma_f32_16x16x32_bf16 v[40:43],a[36:39],v[144:147],v[40:43]
s_mov_b64 s[46:47],s[48:49]
ds_read_b128 v[172:175],v2 offset:44032
v_mul_f32 v44,v28,v44
v_mfma_f32_16x16x32_bf16 v[40:43],a[44:47],v[148:151],v[40:43]
buffer_load_dwordx4 v1,s[36:39],s67 offen offset:2048 sc1 lds
v_exp_f32 v44,v44
v_fma_f32 v45,v62,v45,v61
v_mfma_f32_16x16x32_bf16 v[36:39],a[40:43],v[148:151],v[36:39]
s_mov_b64 s[48:49],s[50:51]
ds_read_b128 v[176:179],v2 offset:45056
v_mul_f32 v45,v29,v45
v_mfma_f32_16x16x32_bf16 v[36:39],a[32:35],v[144:147],v[36:39]
v_exp_f32 v45,v45
v_mfma_f32_16x16x32_bf16 v[36:39],a[48:51],v[152:155],v[36:39]
ds_read_b128 v[180:183],v2 offset:46080
v_fma_f32 v44,v44,v63,v63
v_mfma_f32_16x16x32_bf16 v[36:39],a[56:59],v[156:159],v[36:39]
buffer_load_dwordx4 v1,s[36:39],s67 offen offset:3072 sc1 lds
v_rcp_f32 v44,v44
v_mfma_f32_16x16x32_bf16 v[40:43],a[60:63],v[156:159],v[40:43]
s_add_u32 m0,s71,s88
ds_read_b128 v[184:187],v2 offset:47104
v_fma_f32 v45,v45,v63,v63
v_mfma_f32_16x16x32_bf16 v[40:43],a[52:55],v[152:155],v[40:43]
s_waitcnt lgkmcnt(3)
buffer_load_dword v10,s[36:39],s72 offen offset:0 sc1 lds
v_mfma_f32_16x16x32_bf16 v[40:43],a[68:71],v[160:163],v[40:43]
s_waitcnt vmcnt(11)
ds_read_b128 v[188:191],v2 offset:48128
v_fma_f32 v46,v62,v46,v61
v_mfma_f32_16x16x32_bf16 v[40:43],a[76:79],v[164:167],v[40:43]
s_barrier
v_mfma_f32_16x16x32_bf16 v[36:39],a[72:75],v[164:167],v[36:39]
s_xor_b32 s87,s63,65536
ds_read_b128 v[128:131],v2 offset:49152
v_mul_f32 v46,v30,v46
v_mfma_f32_16x16x32_bf16 v[36:39],a[64:67],v[160:163],v[36:39]
s_cmp_ge_u32 s67,s59
v_exp_f32 v46,v46
v_mfma_f32_16x16x32_bf16 v[36:39],a[80:83],v[168:171],v[36:39]
s_cselect_b32 s86,s50,s51
ds_read_b128 v[132:135],v2 offset:50176
v_fma_f32 v46,v46,v63,v63
v_mfma_f32_16x16x32_bf16 v[36:39],a[88:91],v[172:175],v[36:39]
s_cselect_b32 s87,s87,s64
v_rcp_f32 v45,v45
v_rcp_f32 v46,v46
v_fma_f32 v47,v62,v47,v61
v_mfma_f32_16x16x32_bf16 v[40:43],a[92:95],v[172:175],v[40:43]
s_lshr_b32 s69,s51,5
ds_read_b128 v[136:139],v2 offset:51200
v_mul_f32 v47,v31,v47
v_mfma_f32_16x16x32_bf16 v[40:43],a[84:87],v[168:171],v[40:43]
s_waitcnt lgkmcnt(3)
v_exp_f32 v47,v47
v_mfma_f32_16x16x32_bf16 v[40:43],a[100:103],v[176:179],v[40:43]
s_cmp_lt_u32 s51,4194304
ds_read_b128 v[140:143],v2 offset:52224
v_fma_f32 v47,v47,v63,v63
v_mfma_f32_16x16x32_bf16 v[40:43],a[108:111],v[180:183],v[40:43]
s_cselect_b32 s68,0xffffcfff,-1
v_rcp_f32 v47,v47
v_mul_f32 v28,v28,v44
v_mul_f32 v29,v29,v45
v_mfma_f32_16x16x32_bf16 v[36:39],a[104:107],v[180:183],v[36:39]
s_and_b32 s68,s68,s69
ds_read_b128 v[144:147],v2 offset:53248
v_mul_f32 v30,v30,v46
v_mfma_f32_16x16x32_bf16 v[36:39],a[96:99],v[176:179],v[36:39]
s_lshr_b32 s69,s52,5
v_mul_f32 v31,v31,v47
v_cvt_pk_bf16_f32 v52,v28,v29
v_mfma_f32_16x16x32_bf16 v[36:39],a[112:115],v[184:187],v[36:39]
s_cmp_lt_u32 s52,4194304
ds_read_b128 v[148:151],v2 offset:54272
v_cvt_pk_bf16_f32 v53,v30,v31
v_mfma_f32_16x16x32_bf16 v[36:39],a[120:123],v[188:191],v[36:39]
s_cselect_b32 s70,0xffffcfff,-1
v_fma_f32 v32,-v60,v24,v32
v_fma_f32 v33,-v60,v25,v33
v_fma_f32 v34,-v60,v26,v34
v_fma_f32 v35,-v60,v27,v35
v_mul_f32 v48,v32,v32
v_mfma_f32_16x16x32_bf16 v[40:43],a[124:127],v[188:191],v[40:43]
s_and_b32 s70,s70,s69
ds_read_b128 v[152:155],v2 offset:55296
v_mul_f32 v49,v33,v33
v_mfma_f32_16x16x32_bf16 v[40:43],a[116:119],v[184:187],v[40:43]
s_waitcnt lgkmcnt(3)
v_mul_f32 v50,v34,v34
v_mul_f32 v51,v35,v35
v_mfma_f32_16x16x32_bf16 v[40:43],a[132:135],v[128:131],v[40:43]
s_waitcnt vmcnt(10)
ds_read_b128 v[156:159],v2 offset:56320
v_fma_f32 v48,v62,v48,v61
v_mfma_f32_16x16x32_bf16 v[40:43],a[140:143],v[132:135],v[40:43]
s_barrier
v_mfma_f32_16x16x32_bf16 v[36:39],a[136:139],v[132:135],v[36:39]
s_mov_b32 s62,s87
ds_read_b128 v[160:163],v2 offset:57344
ds_read_b32 v16,v7 offset:256
v_mfma_f32_16x16x32_bf16 v[36:39],a[128:131],v[128:131],v[36:39]
s_mov_b64 s[50:51],s[52:53]
v_mul_f32 v48,v32,v48
v_fma_f32 v49,v62,v49,v61
v_mfma_f32_16x16x32_bf16 v[36:39],a[144:147],v[136:139],v[36:39]
ds_read_b128 v[164:167],v2 offset:58368
v_mul_f32 v49,v33,v49
v_mfma_f32_16x16x32_bf16 v[36:39],a[152:155],v[140:143],v[36:39]
v_exp_f32 v48,v48
v_exp_f32 v49,v49
v_fma_f32 v50,v62,v50,v61
v_mfma_f32_16x16x32_bf16 v[40:43],a[156:159],v[140:143],v[40:43]
s_waitcnt lgkmcnt(1)
ds_read_b128 v[168:171],v2 offset:59392
v_mul_f32 v50,v34,v50
v_mfma_f32_16x16x32_bf16 v[40:43],a[148:151],v[136:139],v[40:43]
v_exp_f32 v50,v50
v_mfma_f32_16x16x32_bf16 v[40:43],a[164:167],v[144:147],v[40:43]
v_cmp_eq_u32 vcc,s60,v16
v_fma_f32 v48,v48,v63,v63
v_rcp_f32 v48,v48
v_mfma_f32_16x16x32_bf16 v[40:43],a[172:175],v[148:151],v[40:43]
s_cmp_eq_u32 vcc_lo,-1
s_cselect_b32 s66,s86,s59
s_cmp_ge_u32 s66,s59
s_cselect_b32 s68,s68,s70
ds_read_b128 v[172:175],v2 offset:60416
s_add_u32 m0,s78,0
v_mfma_f32_16x16x32_bf16 v[36:39],a[168:171],v[148:151],v[36:39]
buffer_load_dword v6,s[36:39],s68 offen offset:0 sc1 lds
v_mfma_f32_16x16x32_bf16 v[36:39],a[160:163],v[144:147],v[36:39]
s_mov_b32 m0,s62
ds_read_b128 v[176:179],v2 offset:61440
v_fma_f32 v49,v49,v63,v63
v_mfma_f32_16x16x32_bf16 v[36:39],a[176:179],v[152:155],v[36:39]
buffer_load_dwordx4 v0,s[36:39],s66 offen offset:0 sc1 lds
v_mfma_f32_16x16x32_bf16 v[36:39],a[184:187],v[156:159],v[36:39]
s_xor_b32 s64,s64,65536
ds_read_b128 v[180:183],v2 offset:62464
v_rcp_f32 v49,v49
v_fma_f32 v50,v50,v63,v63
v_fma_f32 v51,v62,v51,v61
v_mfma_f32_16x16x32_bf16 v[40:43],a[188:191],v[156:159],v[40:43]
s_add_u32 s74,s53,s55
ds_read_b128 v[184:187],v2 offset:63488
v_mul_f32 v51,v35,v51
v_mfma_f32_16x16x32_bf16 v[40:43],a[180:183],v[152:155],v[40:43]
s_waitcnt lgkmcnt(3)
v_exp_f32 v51,v51
v_mfma_f32_16x16x32_bf16 v[40:43],a[196:199],v[160:163],v[40:43]
s_waitcnt vmcnt(7)
ds_read_b128 v[188:191],v2 offset:64512
v_xor_b32 v2,65536,v2
v_mfma_f32_16x16x32_bf16 v[40:43],a[204:207],v[164:167],v[40:43]
s_barrier
v_mfma_f32_16x16x32_bf16 v[36:39],a[200:203],v[164:167],v[36:39]
buffer_load_dwordx4 v0,s[36:39],s66 offen offset:1024 sc1 lds
v_mfma_f32_16x16x32_bf16 v[36:39],a[192:195],v[160:163],v[36:39]
s_cmp_eq_u32 s74,s56
ds_read_b128 v[128:131],v2 offset:0
ds_read_b128 v[64:67],v8 offset:8192
v_mfma_f32_16x16x32_bf16 v[36:39],a[208:211],v[168:171],v[36:39]
s_cselect_b32 s74,s57,s74
ds_read_b128 v[132:135],v2 offset:1024
v_fma_f32 v51,v51,v63,v63
v_mfma_f32_16x16x32_bf16 v[36:39],a[216:219],v[172:175],v[36:39]
s_cmp_eq_u32 s74,s58
v_rcp_f32 v50,v50
v_rcp_f32 v51,v51
v_mfma_f32_16x16x32_bf16 v[40:43],a[220:223],v[172:175],v[40:43]
buffer_load_dwordx4 v0,s[36:39],s66 offen offset:2048 sc1 lds
v_mfma_f32_16x16x32_bf16 v[40:43],a[212:215],v[168:171],v[40:43]
s_waitcnt lgkmcnt(3))"
R"(
ds_read_b128 v[136:139],v2 offset:2048
v_mul_f32 v32,v32,v48
v_mul_f32 v33,v33,v49
v_mul_f32 v34,v34,v50
v_mul_f32 v35,v35,v51
v_cvt_pk_bf16_f32 v54,v32,v33
v_cvt_pk_bf16_f32 v55,v34,v35
v_mfma_f32_16x16x32_bf16 v[40:43],a[228:231],v[176:179],v[40:43]
buffer_store_dwordx4 v[52:55],v3,s[36:39],s73 offen offset:0 sc1
ds_read_b128 v[140:143],v2 offset:3072
v_xor_b32 v8,16384,v8
v_mfma_f32_16x16x32_bf16 v[40:43],a[236:239],v[180:183],v[40:43]
s_waitcnt lgkmcnt(3)
s_cselect_b32 s74,s59,s74
s_mov_b32 s52,s74
v_mfma_f32_16x16x32_bf16 v[36:39],a[232:235],v[180:183],v[36:39]
buffer_load_dwordx4 v0,s[36:39],s66 offen offset:3072 sc1 lds
v_mfma_f32_16x16x32_bf16 v[36:39],a[224:227],v[176:179],v[36:39]
s_cmp_lt_u32 s67,s59
ds_read_b128 v[144:147],v2 offset:4096
v_mfma_f32_16x16x32_bf16 v[36:39],a[240:243],v[184:187],v[36:39]
ds_read_b128 v[148:151],v2 offset:5120
v_mfma_f32_16x16x32_bf16 v[36:39],a[248:251],v[188:191],v[36:39]
v_mfma_f32_16x16x32_bf16 v[40:43],a[252:255],v[188:191],v[40:43]
s_waitcnt lgkmcnt(2)
ds_read_b128 v[152:155],v2 offset:6144
v_mfma_f32_16x16x32_bf16 v[40:43],a[244:247],v[184:187],v[40:43]
s_cbranch_scc1 Lg1_d0w0_%=
s_branch Lg1_slow0_%=
Lg1_d0w1_%=:
v_mfma_f32_16x16x32_bf16 v[32:35],a[36:39],v[128:131],0
s_add_u32 m0,s62,16384
ds_read_b128 v[156:159],v2 offset:3072
s_lshr_b32 s73,s47,0
v_mfma_f32_16x16x32_bf16 v[32:35],a[44:47],v[132:135],v[32:35]
s_lshr_b32 s72,s66,6
s_lshr_b32 s71,s62,2
s_cmp_lt_u32 s66,4194304
v_mfma_f32_16x16x32_bf16 v[28:31],a[40:43],v[132:135],0
buffer_load_dwordx4 v1,s[36:39],s66 offen offset:0 sc1 lds
v_mfma_f32_16x16x32_bf16 v[28:31],a[32:35],v[128:131],v[28:31]
s_cselect_b32 s72,s59,s72
ds_read_b128 v[160:163],v2 offset:12288
v_fma_f32 v36,-v64,v20,v36
v_mfma_f32_16x16x32_bf16 v[28:31],a[48:51],v[136:139],v[28:31]
ds_read_b128 v[164:167],v2 offset:13312
v_fma_f32 v37,-v64,v21,v37
v_mfma_f32_16x16x32_bf16 v[28:31],a[56:59],v[140:143],v[28:31]
v_fma_f32 v38,-v64,v22,v38
v_fma_f32 v39,-v64,v23,v39
v_mul_f32 v44,v36,v36
v_mul_f32 v45,v37,v37
v_mul_f32 v46,v38,v38
v_mfma_f32_16x16x32_bf16 v[32:35],a[60:63],v[140:143],v[32:35]
buffer_load_dwordx4 v1,s[36:39],s66 offen offset:1024 sc1 lds
v_mfma_f32_16x16x32_bf16 v[32:35],a[52:55],v[136:139],v[32:35]
s_waitcnt lgkmcnt(2)
ds_read_b128 v[168:171],v2 offset:14336
v_mul_f32 v47,v39,v39
v_mfma_f32_16x16x32_bf16 v[32:35],a[4:7],v[144:147],v[32:35]
ds_read_b128 v[172:175],v2 offset:15360
v_fma_f32 v44,v66,v44,v65
v_mfma_f32_16x16x32_bf16 v[32:35],a[12:15],v[148:151],v[32:35]
v_mul_f32 v44,v36,v44
v_exp_f32 v44,v44
v_fma_f32 v45,v66,v45,v65
v_mul_f32 v45,v37,v45
v_mfma_f32_16x16x32_bf16 v[28:31],a[8:11],v[148:151],v[28:31]
buffer_load_dwordx4 v1,s[36:39],s66 offen offset:2048 sc1 lds
v_mfma_f32_16x16x32_bf16 v[28:31],a[0:3],v[144:147],v[28:31]
ds_read_b128 v[176:179],v2 offset:8192
v_fma_f32 v44,v44,v67,v67
v_mfma_f32_16x16x32_bf16 v[28:31],a[16:19],v[152:155],v[28:31]
ds_read_b128 v[180:183],v2 offset:9216
v_fma_f32 v46,v66,v46,v65
v_mfma_f32_16x16x32_bf16 v[28:31],a[24:27],v[156:159],v[28:31]
v_exp_f32 v45,v45
v_rcp_f32 v44,v44
v_mul_f32 v46,v38,v46
v_mfma_f32_16x16x32_bf16 v[32:35],a[28:31],v[156:159],v[32:35]
buffer_load_dwordx4 v1,s[36:39],s66 offen offset:3072 sc1 lds
v_mfma_f32_16x16x32_bf16 v[32:35],a[20:23],v[152:155],v[32:35]
s_waitcnt lgkmcnt(2)
ds_read_b128 v[184:187],v2 offset:10240
s_add_u32 m0,s71,s88
v_mfma_f32_16x16x32_bf16 v[32:35],a[100:103],v[160:163],v[32:35]
s_xor_b32 s87,s64,65536
ds_read_b128 v[188:191],v2 offset:11264
buffer_load_dword v10,s[36:39],s72 offen offset:0 sc1 lds
s_waitcnt vmcnt(10)
v_mfma_f32_16x16x32_bf16 v[32:35],a[108:111],v[164:167],v[32:35]
s_barrier
v_mfma_f32_16x16x32_bf16 v[28:31],a[104:107],v[164:167],v[28:31]
s_cmp_ge_u32 s66,s59
ds_read_b128 v[128:131],v2 offset:20480
v_fma_f32 v45,v45,v67,v67
v_mfma_f32_16x16x32_bf16 v[28:31],a[96:99],v[160:163],v[28:31]
s_cselect_b32 s86,s49,s50
v_exp_f32 v46,v46
v_mfma_f32_16x16x32_bf16 v[28:31],a[112:115],v[168:171],v[28:31]
s_cselect_b32 s87,s87,s63
ds_read_b128 v[132:135],v2 offset:21504
v_fma_f32 v46,v46,v67,v67
v_mfma_f32_16x16x32_bf16 v[28:31],a[120:123],v[172:175],v[28:31]
s_mov_b32 s62,s87
v_rcp_f32 v45,v45
v_rcp_f32 v46,v46
v_mfma_f32_16x16x32_bf16 v[32:35],a[124:127],v[172:175],v[32:35]
s_mov_b32 m0,s62
ds_read_b128 v[136:139],v2 offset:22528
v_fma_f32 v47,v66,v47,v65
v_mfma_f32_16x16x32_bf16 v[32:35],a[116:119],v[168:171],v[32:35]
s_waitcnt lgkmcnt(3)
v_mul_f32 v47,v39,v47
v_mul_f32 v36,v36,v44
v_mfma_f32_16x16x32_bf16 v[32:35],a[68:71],v[176:179],v[32:35]
ds_read_b128 v[140:143],v2 offset:23552
v_mul_f32 v37,v37,v45
v_mfma_f32_16x16x32_bf16 v[32:35],a[76:79],v[180:183],v[32:35]
v_exp_f32 v47,v47
v_mul_f32 v38,v38,v46
v_cvt_pk_bf16_f32 v52,v36,v37
v_fma_f32 v40,-v64,v24,v40
v_mfma_f32_16x16x32_bf16 v[28:31],a[72:75],v[180:183],v[28:31]
ds_read_b128 v[144:147],v2 offset:16384
v_fma_f32 v47,v47,v67,v67
v_mfma_f32_16x16x32_bf16 v[28:31],a[64:67],v[176:179],v[28:31]
v_rcp_f32 v47,v47
v_mfma_f32_16x16x32_bf16 v[28:31],a[80:83],v[184:187],v[28:31]
ds_read_b128 v[148:151],v2 offset:17408
v_mul_f32 v39,v39,v47
v_mfma_f32_16x16x32_bf16 v[28:31],a[88:91],v[188:191],v[28:31]
v_cvt_pk_bf16_f32 v53,v38,v39
v_fma_f32 v41,-v64,v25,v41
v_fma_f32 v42,-v64,v26,v42
v_fma_f32 v43,-v64,v27,v43
v_mul_f32 v48,v40,v40
v_mfma_f32_16x16x32_bf16 v[32:35],a[92:95],v[188:191],v[32:35]
ds_read_b128 v[152:155],v2 offset:18432
v_mul_f32 v49,v41,v41
v_mfma_f32_16x16x32_bf16 v[32:35],a[84:87],v[184:187],v[32:35]
s_waitcnt lgkmcnt(3)
v_mul_f32 v50,v42,v42
v_mul_f32 v51,v43,v43
v_mfma_f32_16x16x32_bf16 v[32:35],a[164:167],v[128:131],v[32:35]
s_nop 0
ds_read_b128 v[156:159],v2 offset:19456
v_fma_f32 v48,v66,v48,v65
v_mfma_f32_16x16x32_bf16 v[32:35],a[172:175],v[132:135],v[32:35]
s_barrier
v_mfma_f32_16x16x32_bf16 v[28:31],a[168:171],v[132:135],v[28:31]
ds_read_b128 v[160:163],v2 offset:28672
ds_read_b32 v16,v7 offset:0
v_mfma_f32_16x16x32_bf16 v[28:31],a[160:163],v[128:131],v[28:31]
v_mul_f32 v48,v40,v48
v_fma_f32 v49,v66,v49,v65
v_mfma_f32_16x16x32_bf16 v[28:31],a[176:179],v[136:139],v[28:31]
ds_read_b128 v[164:167],v2 offset:29696
v_mul_f32 v49,v41,v49
v_mfma_f32_16x16x32_bf16 v[28:31],a[184:187],v[140:143],v[28:31]
v_exp_f32 v48,v48
v_exp_f32 v49,v49
v_fma_f32 v50,v66,v50,v65
v_mfma_f32_16x16x32_bf16 v[32:35],a[188:191],v[140:143],v[32:35]
s_waitcnt lgkmcnt(1)
ds_read_b128 v[168:171],v2 offset:30720
v_mul_f32 v50,v42,v50
v_mfma_f32_16x16x32_bf16 v[32:35],a[180:183],v[136:139],v[32:35]
v_cmp_eq_u32 vcc,s60,v16
v_exp_f32 v50,v50
v_fma_f32 v48,v48,v67,v67
v_mfma_f32_16x16x32_bf16 v[32:35],a[132:135],v[144:147],v[32:35]
s_cmp_eq_u32 vcc_lo,-1
ds_read_b128 v[172:175],v2 offset:31744
s_cselect_b32 s67,s86,s59
v_mfma_f32_16x16x32_bf16 v[32:35],a[140:143],v[148:151],v[32:35]
buffer_load_dwordx4 v0,s[36:39],s67 offen offset:0 sc1 lds
v_rcp_f32 v48,v48
v_mfma_f32_16x16x32_bf16 v[28:31],a[136:139],v[148:151],v[28:31]
s_xor_b32 s63,s63,65536
ds_read_b128 v[176:179],v2 offset:24576
v_fma_f32 v49,v49,v67,v67
v_mfma_f32_16x16x32_bf16 v[28:31],a[128:131],v[144:147],v[28:31]
s_add_u32 s53,s52,s55
v_rcp_f32 v49,v49
v_mfma_f32_16x16x32_bf16 v[28:31],a[144:147],v[152:155],v[28:31]
s_cmp_eq_u32 s53,s56
ds_read_b128 v[180:183],v2 offset:25600
v_fma_f32 v50,v50,v67,v67
v_mfma_f32_16x16x32_bf16 v[28:31],a[152:155],v[156:159],v[28:31]
buffer_load_dwordx4 v0,s[36:39],s67 offen offset:1024 sc1 lds
v_rcp_f32 v50,v50
v_fma_f32 v51,v66,v51,v65
v_mfma_f32_16x16x32_bf16 v[32:35],a[156:159],v[156:159],v[32:35]
s_cselect_b32 s53,s57,s53
ds_read_b128 v[184:187],v2 offset:26624
v_mul_f32 v51,v43,v51
v_mfma_f32_16x16x32_bf16 v[32:35],a[148:151],v[152:155],v[32:35]
s_waitcnt lgkmcnt(3)
v_exp_f32 v51,v51)"
R"(
v_mfma_f32_16x16x32_bf16 v[32:35],a[228:231],v[160:163],v[32:35]
s_waitcnt vmcnt(7)
ds_read_b128 v[188:191],v2 offset:27648
v_fma_f32 v51,v51,v67,v67
v_mfma_f32_16x16x32_bf16 v[32:35],a[236:239],v[164:167],v[32:35]
s_barrier
v_mfma_f32_16x16x32_bf16 v[28:31],a[232:235],v[164:167],v[28:31]
s_cmp_eq_u32 s53,s58
ds_read_b128 v[128:131],v2 offset:36864
ds_read_b128 v[60:63],v8 offset:0
v_mfma_f32_16x16x32_bf16 v[28:31],a[224:227],v[160:163],v[28:31]
buffer_load_dwordx4 v0,s[36:39],s67 offen offset:2048 sc1 lds
v_mfma_f32_16x16x32_bf16 v[28:31],a[240:243],v[168:171],v[28:31]
s_cselect_b32 s53,s59,s53
ds_read_b128 v[132:135],v2 offset:37888
v_mul_f32 v40,v40,v48
v_mfma_f32_16x16x32_bf16 v[28:31],a[248:251],v[172:175],v[28:31]
s_cmp_ge_u32 s66,s59
v_rcp_f32 v51,v51
v_mul_f32 v41,v41,v49
v_mul_f32 v42,v42,v50
v_cvt_pk_bf16_f32 v54,v40,v41
v_mfma_f32_16x16x32_bf16 v[32:35],a[252:255],v[172:175],v[32:35]
ds_read_b128 v[136:139],v2 offset:38912
v_mul_f32 v43,v43,v51
v_mfma_f32_16x16x32_bf16 v[32:35],a[244:247],v[168:171],v[32:35]
s_waitcnt lgkmcnt(4)
buffer_load_dwordx4 v0,s[36:39],s67 offen offset:3072 sc1 lds
v_mfma_f32_16x16x32_bf16 v[32:35],a[196:199],v[176:179],v[32:35]
s_waitcnt lgkmcnt(2)
ds_read_b128 v[140:143],v2 offset:39936
v_cvt_pk_bf16_f32 v55,v42,v43
v_mfma_f32_16x16x32_bf16 v[32:35],a[204:207],v[180:183],v[32:35]
buffer_store_dwordx4 v[52:55],v3,s[36:39],s73 offen offset:0 sc1
v_mfma_f32_16x16x32_bf16 v[28:31],a[200:203],v[180:183],v[28:31]
ds_read_b128 v[144:147],v2 offset:32768
v_mfma_f32_16x16x32_bf16 v[28:31],a[192:195],v[176:179],v[28:31]
v_mfma_f32_16x16x32_bf16 v[28:31],a[208:211],v[184:187],v[28:31]
ds_read_b128 v[148:151],v2 offset:33792
v_mfma_f32_16x16x32_bf16 v[28:31],a[216:219],v[188:191],v[28:31]
v_mfma_f32_16x16x32_bf16 v[32:35],a[220:223],v[188:191],v[32:35]
s_waitcnt lgkmcnt(2)
ds_read_b128 v[152:155],v2 offset:34816
v_mfma_f32_16x16x32_bf16 v[32:35],a[212:215],v[184:187],v[32:35]
s_cbranch_scc1 Lg1_slow1_%=
Lg1_d1w1_%=:
v_mfma_f32_16x16x32_bf16 v[40:43],a[36:39],v[128:131],0
s_add_u32 m0,s62,16384
ds_read_b128 v[156:159],v2 offset:35840
s_lshr_b32 s73,s48,0
v_mfma_f32_16x16x32_bf16 v[40:43],a[44:47],v[132:135],v[40:43]
s_lshr_b32 s72,s67,6
s_lshr_b32 s71,s62,2
s_cmp_lt_u32 s67,4194304
v_mfma_f32_16x16x32_bf16 v[36:39],a[40:43],v[132:135],0
buffer_load_dwordx4 v1,s[36:39],s67 offen offset:0 sc1 lds
v_mfma_f32_16x16x32_bf16 v[36:39],a[32:35],v[128:131],v[36:39]
s_cselect_b32 s72,s59,s72
ds_read_b128 v[160:163],v2 offset:45056
v_fma_f32 v28,-v60,v20,v28
v_mfma_f32_16x16x32_bf16 v[36:39],a[48:51],v[136:139],v[36:39]
s_mov_b64 s[44:45],s[46:47]
ds_read_b128 v[164:167],v2 offset:46080
v_fma_f32 v29,-v60,v21,v29
v_mfma_f32_16x16x32_bf16 v[36:39],a[56:59],v[140:143],v[36:39]
s_mov_b64 s[46:47],s[48:49]
v_fma_f32 v30,-v60,v22,v30
v_fma_f32 v31,-v60,v23,v31
v_mul_f32 v44,v28,v28
v_mul_f32 v45,v29,v29
v_mul_f32 v46,v30,v30
v_mfma_f32_16x16x32_bf16 v[40:43],a[60:63],v[140:143],v[40:43]
buffer_load_dwordx4 v1,s[36:39],s67 offen offset:1024 sc1 lds
v_mfma_f32_16x16x32_bf16 v[40:43],a[52:55],v[136:139],v[40:43]
s_waitcnt lgkmcnt(2)
ds_read_b128 v[168:171],v2 offset:47104
v_mul_f32 v47,v31,v31
v_mfma_f32_16x16x32_bf16 v[40:43],a[4:7],v[144:147],v[40:43]
s_mov_b64 s[48:49],s[50:51]
ds_read_b128 v[172:175],v2 offset:48128
v_fma_f32 v44,v62,v44,v61
v_mfma_f32_16x16x32_bf16 v[40:43],a[12:15],v[148:151],v[40:43]
v_mul_f32 v44,v28,v44
v_exp_f32 v44,v44
v_fma_f32 v45,v62,v45,v61
v_mul_f32 v45,v29,v45
v_mfma_f32_16x16x32_bf16 v[36:39],a[8:11],v[148:151],v[36:39]
buffer_load_dwordx4 v1,s[36:39],s67 offen offset:2048 sc1 lds
v_mfma_f32_16x16x32_bf16 v[36:39],a[0:3],v[144:147],v[36:39]
ds_read_b128 v[176:179],v2 offset:40960
v_fma_f32 v44,v44,v63,v63
v_mfma_f32_16x16x32_bf16 v[36:39],a[16:19],v[152:155],v[36:39]
ds_read_b128 v[180:183],v2 offset:41984
v_fma_f32 v46,v62,v46,v61
v_mfma_f32_16x16x32_bf16 v[36:39],a[24:27],v[156:159],v[36:39]
v_exp_f32 v45,v45
v_rcp_f32 v44,v44
v_mul_f32 v46,v30,v46
v_mfma_f32_16x16x32_bf16 v[40:43],a[28:31],v[156:159],v[40:43]
buffer_load_dwordx4 v1,s[36:39],s67 offen offset:3072 sc1 lds
v_mfma_f32_16x16x32_bf16 v[40:43],a[20:23],v[152:155],v[40:43]
s_waitcnt lgkmcnt(2)
ds_read_b128 v[184:187],v2 offset:43008
s_add_u32 m0,s71,s88
v_mfma_f32_16x16x32_bf16 v[40:43],a[100:103],v[160:163],v[40:43]
s_xor_b32 s87,s63,65536
ds_read_b128 v[188:191],v2 offset:44032
buffer_load_dword v10,s[36:39],s72 offen offset:0 sc1 lds
s_waitcnt vmcnt(10)
v_mfma_f32_16x16x32_bf16 v[40:43],a[108:111],v[164:167],v[40:43]
s_barrier
v_mfma_f32_16x16x32_bf16 v[36:39],a[104:107],v[164:167],v[36:39]
s_cmp_ge_u32 s67,s59
ds_read_b128 v[128:131],v2 offset:53248
v_fma_f32 v45,v45,v63,v63
v_mfma_f32_16x16x32_bf16 v[36:39],a[96:99],v[160:163],v[36:39]
s_cselect_b32 s86,s50,s51
v_exp_f32 v46,v46
v_mfma_f32_16x16x32_bf16 v[36:39],a[112:115],v[168:171],v[36:39]
s_cselect_b32 s87,s87,s64
ds_read_b128 v[132:135],v2 offset:54272
v_fma_f32 v46,v46,v63,v63
v_mfma_f32_16x16x32_bf16 v[36:39],a[120:123],v[172:175],v[36:39]
s_mov_b32 s62,s87
v_rcp_f32 v45,v45
v_rcp_f32 v46,v46
v_mfma_f32_16x16x32_bf16 v[40:43],a[124:127],v[172:175],v[40:43]
s_mov_b32 m0,s62
ds_read_b128 v[136:139],v2 offset:55296
v_fma_f32 v47,v62,v47,v61
v_mfma_f32_16x16x32_bf16 v[40:43],a[116:119],v[168:171],v[40:43]
s_waitcnt lgkmcnt(3)
v_mul_f32 v47,v31,v47
v_mul_f32 v28,v28,v44
v_mfma_f32_16x16x32_bf16 v[40:43],a[68:71],v[176:179],v[40:43]
s_mov_b64 s[50:51],s[52:53]
ds_read_b128 v[140:143],v2 offset:56320
v_mul_f32 v29,v29,v45
v_mfma_f32_16x16x32_bf16 v[40:43],a[76:79],v[180:183],v[40:43]
v_exp_f32 v47,v47
v_mul_f32 v30,v30,v46
v_cvt_pk_bf16_f32 v52,v28,v29
v_fma_f32 v32,-v60,v24,v32
v_mfma_f32_16x16x32_bf16 v[36:39],a[72:75],v[180:183],v[36:39]
ds_read_b128 v[144:147],v2 offset:49152
v_fma_f32 v47,v47,v63,v63
v_mfma_f32_16x16x32_bf16 v[36:39],a[64:67],v[176:179],v[36:39]
v_rcp_f32 v47,v47
v_mfma_f32_16x16x32_bf16 v[36:39],a[80:83],v[184:187],v[36:39]
ds_read_b128 v[148:151],v2 offset:50176
v_mul_f32 v31,v31,v47
v_mfma_f32_16x16x32_bf16 v[36:39],a[88:91],v[188:191],v[36:39]
v_cvt_pk_bf16_f32 v53,v30,v31
v_fma_f32 v33,-v60,v25,v33
v_fma_f32 v34,-v60,v26,v34
v_fma_f32 v35,-v60,v27,v35
v_mul_f32 v48,v32,v32
v_mfma_f32_16x16x32_bf16 v[40:43],a[92:95],v[188:191],v[40:43]
ds_read_b128 v[152:155],v2 offset:51200
v_mul_f32 v49,v33,v33
v_mfma_f32_16x16x32_bf16 v[40:43],a[84:87],v[184:187],v[40:43]
s_waitcnt lgkmcnt(3)
v_mul_f32 v50,v34,v34
v_mul_f32 v51,v35,v35
v_mfma_f32_16x16x32_bf16 v[40:43],a[164:167],v[128:131],v[40:43]
s_nop 0
ds_read_b128 v[156:159],v2 offset:52224
v_fma_f32 v48,v62,v48,v61
v_mfma_f32_16x16x32_bf16 v[40:43],a[172:175],v[132:135],v[40:43]
s_barrier
v_mfma_f32_16x16x32_bf16 v[36:39],a[168:171],v[132:135],v[36:39]
ds_read_b128 v[160:163],v2 offset:61440
ds_read_b32 v16,v7 offset:256
v_mfma_f32_16x16x32_bf16 v[36:39],a[160:163],v[128:131],v[36:39]
v_mul_f32 v48,v32,v48
v_fma_f32 v49,v62,v49,v61
v_mfma_f32_16x16x32_bf16 v[36:39],a[176:179],v[136:139],v[36:39]
ds_read_b128 v[164:167],v2 offset:62464
v_mul_f32 v49,v33,v49
v_mfma_f32_16x16x32_bf16 v[36:39],a[184:187],v[140:143],v[36:39]
v_exp_f32 v48,v48
v_exp_f32 v49,v49
v_fma_f32 v50,v62,v50,v61
v_mfma_f32_16x16x32_bf16 v[40:43],a[188:191],v[140:143],v[40:43]
s_waitcnt lgkmcnt(1)
ds_read_b128 v[168:171],v2 offset:63488
v_mul_f32 v50,v34,v50
v_mfma_f32_16x16x32_bf16 v[40:43],a[180:183],v[136:139],v[40:43]
v_cmp_eq_u32 vcc,s60,v16
v_exp_f32 v50,v50
v_fma_f32 v48,v48,v63,v63
v_mfma_f32_16x16x32_bf16 v[40:43],a[132:135],v[144:147],v[40:43]
s_cmp_eq_u32 vcc_lo,-1
ds_read_b128 v[172:175],v2 offset:64512
s_cselect_b32 s66,s86,s59
v_mfma_f32_16x16x32_bf16 v[40:43],a[140:143],v[148:151],v[40:43]
buffer_load_dwordx4 v0,s[36:39],s66 offen offset:0 sc1 lds
v_rcp_f32 v48,v48
v_mfma_f32_16x16x32_bf16 v[36:39],a[136:139],v[148:151],v[36:39]
s_xor_b32 s64,s64,65536)"
R"(
ds_read_b128 v[176:179],v2 offset:57344
v_fma_f32 v49,v49,v63,v63
v_mfma_f32_16x16x32_bf16 v[36:39],a[128:131],v[144:147],v[36:39]
s_add_u32 s74,s53,s55
v_rcp_f32 v49,v49
v_mfma_f32_16x16x32_bf16 v[36:39],a[144:147],v[152:155],v[36:39]
s_cmp_eq_u32 s74,s56
ds_read_b128 v[180:183],v2 offset:58368
v_fma_f32 v50,v50,v63,v63
v_mfma_f32_16x16x32_bf16 v[36:39],a[152:155],v[156:159],v[36:39]
buffer_load_dwordx4 v0,s[36:39],s66 offen offset:1024 sc1 lds
v_rcp_f32 v50,v50
v_fma_f32 v51,v62,v51,v61
v_mfma_f32_16x16x32_bf16 v[40:43],a[156:159],v[156:159],v[40:43]
s_cselect_b32 s74,s57,s74
ds_read_b128 v[184:187],v2 offset:59392
v_mul_f32 v51,v35,v51
v_mfma_f32_16x16x32_bf16 v[40:43],a[148:151],v[152:155],v[40:43]
s_waitcnt lgkmcnt(3)
v_exp_f32 v51,v51
v_mfma_f32_16x16x32_bf16 v[40:43],a[228:231],v[160:163],v[40:43]
s_waitcnt vmcnt(7)
ds_read_b128 v[188:191],v2 offset:60416
v_xor_b32 v2,65536,v2
v_mfma_f32_16x16x32_bf16 v[40:43],a[236:239],v[164:167],v[40:43]
s_barrier
v_mfma_f32_16x16x32_bf16 v[36:39],a[232:235],v[164:167],v[36:39]
s_cmp_eq_u32 s74,s58
ds_read_b128 v[128:131],v2 offset:4096
ds_read_b128 v[64:67],v8 offset:8192
v_mfma_f32_16x16x32_bf16 v[36:39],a[224:227],v[160:163],v[36:39]
buffer_load_dwordx4 v0,s[36:39],s66 offen offset:2048 sc1 lds
v_mfma_f32_16x16x32_bf16 v[36:39],a[240:243],v[168:171],v[36:39]
s_cselect_b32 s74,s59,s74
ds_read_b128 v[132:135],v2 offset:5120
v_fma_f32 v51,v51,v63,v63
v_mfma_f32_16x16x32_bf16 v[36:39],a[248:251],v[172:175],v[36:39]
s_mov_b32 s52,s74
v_rcp_f32 v51,v51
v_mul_f32 v32,v32,v48
v_mul_f32 v33,v33,v49
v_mul_f32 v34,v34,v50
v_mfma_f32_16x16x32_bf16 v[40:43],a[252:255],v[172:175],v[40:43]
s_cmp_lt_u32 s67,s59
ds_read_b128 v[136:139],v2 offset:6144
v_mul_f32 v35,v35,v51
v_mfma_f32_16x16x32_bf16 v[40:43],a[244:247],v[168:171],v[40:43]
s_waitcnt lgkmcnt(4)
buffer_load_dwordx4 v0,s[36:39],s66 offen offset:3072 sc1 lds
v_mfma_f32_16x16x32_bf16 v[40:43],a[196:199],v[176:179],v[40:43]
s_waitcnt lgkmcnt(2)
v_cvt_pk_bf16_f32 v54,v32,v33
v_cvt_pk_bf16_f32 v55,v34,v35
v_mfma_f32_16x16x32_bf16 v[40:43],a[204:207],v[180:183],v[40:43]
ds_read_b128 v[140:143],v2 offset:7168
buffer_store_dwordx4 v[52:55],v3,s[36:39],s73 offen offset:0 sc1
v_xor_b32 v8,16384,v8
v_mfma_f32_16x16x32_bf16 v[36:39],a[200:203],v[180:183],v[36:39]
ds_read_b128 v[144:147],v2 offset:0
v_mfma_f32_16x16x32_bf16 v[36:39],a[192:195],v[176:179],v[36:39]
v_mfma_f32_16x16x32_bf16 v[36:39],a[208:211],v[184:187],v[36:39]
ds_read_b128 v[148:151],v2 offset:1024
v_mfma_f32_16x16x32_bf16 v[36:39],a[216:219],v[188:191],v[36:39]
v_mfma_f32_16x16x32_bf16 v[40:43],a[220:223],v[188:191],v[40:43]
s_waitcnt lgkmcnt(2)
ds_read_b128 v[152:155],v2 offset:2048
v_mfma_f32_16x16x32_bf16 v[40:43],a[212:215],v[184:187],v[40:43]
s_cbranch_scc1 Lg1_d0w1_%=
s_branch Lg1_slow0_%=
Lg1_d0w2_%=:
v_mfma_f32_16x16x32_bf16 v[32:35],a[68:71],v[128:131],0
s_add_u32 m0,s62,16384
ds_read_b128 v[156:159],v2 offset:15360
s_lshr_b32 s73,s47,0
v_mfma_f32_16x16x32_bf16 v[32:35],a[76:79],v[132:135],v[32:35]
s_lshr_b32 s72,s66,6
s_lshr_b32 s71,s62,2
s_cmp_lt_u32 s66,4194304
v_mfma_f32_16x16x32_bf16 v[28:31],a[72:75],v[132:135],0
s_cselect_b32 s72,s59,s72
ds_read_b128 v[160:163],v2 offset:0
v_mfma_f32_16x16x32_bf16 v[28:31],a[64:67],v[128:131],v[28:31]
buffer_load_dwordx4 v1,s[36:39],s66 offen offset:0 sc1 lds
v_mfma_f32_16x16x32_bf16 v[28:31],a[80:83],v[136:139],v[28:31]
ds_read_b128 v[164:167],v2 offset:1024
v_fma_f32 v36,-v64,v20,v36
v_mfma_f32_16x16x32_bf16 v[28:31],a[88:91],v[140:143],v[28:31]
v_fma_f32 v37,-v64,v21,v37
v_fma_f32 v38,-v64,v22,v38
v_fma_f32 v39,-v64,v23,v39
v_mul_f32 v44,v36,v36
v_mul_f32 v45,v37,v37
v_mfma_f32_16x16x32_bf16 v[32:35],a[92:95],v[140:143],v[32:35]
ds_read_b128 v[168:171],v2 offset:2048
v_mul_f32 v46,v38,v38
v_mfma_f32_16x16x32_bf16 v[32:35],a[84:87],v[136:139],v[32:35]
s_waitcnt lgkmcnt(3)
buffer_load_dwordx4 v1,s[36:39],s66 offen offset:1024 sc1 lds
v_mfma_f32_16x16x32_bf16 v[32:35],a[100:103],v[144:147],v[32:35]
ds_read_b128 v[172:175],v2 offset:3072
v_mul_f32 v47,v39,v39
v_mfma_f32_16x16x32_bf16 v[32:35],a[108:111],v[148:151],v[32:35]
v_fma_f32 v44,v66,v44,v65
v_mul_f32 v44,v36,v44
v_exp_f32 v44,v44
v_mfma_f32_16x16x32_bf16 v[28:31],a[104:107],v[148:151],v[28:31]
ds_read_b128 v[176:179],v2 offset:4096
v_fma_f32 v44,v44,v67,v67
v_mfma_f32_16x16x32_bf16 v[28:31],a[96:99],v[144:147],v[28:31]
buffer_load_dwordx4 v1,s[36:39],s66 offen offset:2048 sc1 lds
v_mfma_f32_16x16x32_bf16 v[28:31],a[112:115],v[152:155],v[28:31]
ds_read_b128 v[180:183],v2 offset:5120
v_fma_f32 v45,v66,v45,v65
v_mfma_f32_16x16x32_bf16 v[28:31],a[120:123],v[156:159],v[28:31]
v_rcp_f32 v44,v44
v_mul_f32 v45,v37,v45
v_fma_f32 v46,v66,v46,v65
v_mfma_f32_16x16x32_bf16 v[32:35],a[124:127],v[156:159],v[32:35]
ds_read_b128 v[184:187],v2 offset:6144
v_mul_f32 v46,v38,v46
v_mfma_f32_16x16x32_bf16 v[32:35],a[116:119],v[152:155],v[32:35]
s_waitcnt lgkmcnt(3)
buffer_load_dwordx4 v1,s[36:39],s66 offen offset:3072 sc1 lds
s_add_u32 m0,s71,s88
v_mfma_f32_16x16x32_bf16 v[32:35],a[4:7],v[160:163],v[32:35]
s_xor_b32 s87,s64,65536
ds_read_b128 v[188:191],v2 offset:7168
buffer_load_dword v10,s[36:39],s72 offen offset:0 sc1 lds
s_waitcnt vmcnt(10)
v_mfma_f32_16x16x32_bf16 v[32:35],a[12:15],v[164:167],v[32:35]
s_barrier
v_mfma_f32_16x16x32_bf16 v[28:31],a[8:11],v[164:167],v[28:31]
s_cmp_ge_u32 s66,s59
ds_read_b128 v[128:131],v2 offset:24576
v_fma_f32 v47,v66,v47,v65
v_mfma_f32_16x16x32_bf16 v[28:31],a[0:3],v[160:163],v[28:31]
s_cselect_b32 s86,s49,s50
v_exp_f32 v45,v45
v_mfma_f32_16x16x32_bf16 v[28:31],a[16:19],v[168:171],v[28:31]
s_cselect_b32 s87,s87,s63
ds_read_b128 v[132:135],v2 offset:25600
v_mul_f32 v47,v39,v47
v_mfma_f32_16x16x32_bf16 v[28:31],a[24:27],v[172:175],v[28:31]
s_mov_b32 s62,s87
v_exp_f32 v46,v46
v_exp_f32 v47,v47
v_mfma_f32_16x16x32_bf16 v[32:35],a[28:31],v[172:175],v[32:35]
s_mov_b32 m0,s62
ds_read_b128 v[136:139],v2 offset:26624
v_fma_f32 v45,v45,v67,v67
v_mfma_f32_16x16x32_bf16 v[32:35],a[20:23],v[168:171],v[32:35]
s_waitcnt lgkmcnt(3)
v_rcp_f32 v45,v45
v_mfma_f32_16x16x32_bf16 v[32:35],a[36:39],v[176:179],v[32:35]
ds_read_b128 v[140:143],v2 offset:27648
v_fma_f32 v46,v46,v67,v67
v_mfma_f32_16x16x32_bf16 v[32:35],a[44:47],v[180:183],v[32:35]
v_rcp_f32 v46,v46
v_fma_f32 v47,v47,v67,v67
v_mul_f32 v36,v36,v44
v_mfma_f32_16x16x32_bf16 v[28:31],a[40:43],v[180:183],v[28:31]
ds_read_b128 v[144:147],v2 offset:28672
v_mul_f32 v37,v37,v45
v_mfma_f32_16x16x32_bf16 v[28:31],a[32:35],v[176:179],v[28:31]
v_rcp_f32 v47,v47
v_mfma_f32_16x16x32_bf16 v[28:31],a[48:51],v[184:187],v[28:31]
ds_read_b128 v[148:151],v2 offset:29696
v_mul_f32 v38,v38,v46
v_mfma_f32_16x16x32_bf16 v[28:31],a[56:59],v[188:191],v[28:31]
v_mul_f32 v39,v39,v47
v_cvt_pk_bf16_f32 v52,v36,v37
v_cvt_pk_bf16_f32 v53,v38,v39
v_fma_f32 v40,-v64,v24,v40
v_mfma_f32_16x16x32_bf16 v[32:35],a[60:63],v[188:191],v[32:35]
ds_read_b128 v[152:155],v2 offset:30720
v_fma_f32 v41,-v64,v25,v41
v_mfma_f32_16x16x32_bf16 v[32:35],a[52:55],v[184:187],v[32:35]
s_waitcnt lgkmcnt(3)
v_fma_f32 v42,-v64,v26,v42
v_fma_f32 v43,-v64,v27,v43
v_mfma_f32_16x16x32_bf16 v[32:35],a[196:199],v[128:131],v[32:35]
s_nop 0
ds_read_b128 v[156:159],v2 offset:31744
v_mul_f32 v48,v40,v40
v_mfma_f32_16x16x32_bf16 v[32:35],a[204:207],v[132:135],v[32:35]
s_barrier
v_mfma_f32_16x16x32_bf16 v[28:31],a[200:203],v[132:135],v[28:31]
ds_read_b128 v[160:163],v2 offset:16384
ds_read_b32 v16,v7 offset:0
v_mfma_f32_16x16x32_bf16 v[28:31],a[192:195],v[128:131],v[28:31]
v_mul_f32 v49,v41,v41
v_mul_f32 v50,v42,v42
v_mfma_f32_16x16x32_bf16 v[28:31],a[208:211],v[136:139],v[28:31]
ds_read_b128 v[164:167],v2 offset:17408
v_mul_f32 v51,v43,v43
v_mfma_f32_16x16x32_bf16 v[28:31],a[216:219],v[140:143],v[28:31]
v_fma_f32 v48,v66,v48,v65
v_mul_f32 v48,v40,v48
v_exp_f32 v48,v48
v_mfma_f32_16x16x32_bf16 v[32:35],a[220:223],v[140:143],v[32:35])"
R"(
s_waitcnt lgkmcnt(1)
ds_read_b128 v[168:171],v2 offset:18432
v_fma_f32 v48,v48,v67,v67
v_mfma_f32_16x16x32_bf16 v[32:35],a[212:215],v[136:139],v[32:35]
v_rcp_f32 v48,v48
v_mfma_f32_16x16x32_bf16 v[32:35],a[228:231],v[144:147],v[32:35]
v_cmp_eq_u32 vcc,s60,v16
v_fma_f32 v49,v66,v49,v65
v_mul_f32 v49,v41,v49
v_fma_f32 v50,v66,v50,v65
v_mfma_f32_16x16x32_bf16 v[32:35],a[236:239],v[148:151],v[32:35]
s_cmp_eq_u32 vcc_lo,-1
ds_read_b128 v[172:175],v2 offset:19456
v_exp_f32 v49,v49
s_cselect_b32 s67,s86,s59
v_mfma_f32_16x16x32_bf16 v[28:31],a[232:235],v[148:151],v[28:31]
buffer_load_dwordx4 v0,s[36:39],s67 offen offset:0 sc1 lds
v_mfma_f32_16x16x32_bf16 v[28:31],a[224:227],v[144:147],v[28:31]
s_xor_b32 s63,s63,65536
ds_read_b128 v[176:179],v2 offset:20480
v_mul_f32 v50,v42,v50
v_mfma_f32_16x16x32_bf16 v[28:31],a[240:243],v[152:155],v[28:31]
s_add_u32 s53,s52,s55
ds_read_b128 v[180:183],v2 offset:21504
v_fma_f32 v49,v49,v67,v67
v_mfma_f32_16x16x32_bf16 v[28:31],a[248:251],v[156:159],v[28:31]
s_cmp_eq_u32 s53,s56
v_exp_f32 v50,v50
v_rcp_f32 v49,v49
v_mfma_f32_16x16x32_bf16 v[32:35],a[252:255],v[156:159],v[32:35]
buffer_load_dwordx4 v0,s[36:39],s67 offen offset:1024 sc1 lds
v_mfma_f32_16x16x32_bf16 v[32:35],a[244:247],v[152:155],v[32:35]
s_waitcnt lgkmcnt(2)
ds_read_b128 v[184:187],v2 offset:22528
v_fma_f32 v50,v50,v67,v67
v_mfma_f32_16x16x32_bf16 v[32:35],a[132:135],v[160:163],v[32:35]
s_waitcnt vmcnt(7)
ds_read_b128 v[188:191],v2 offset:23552
v_fma_f32 v51,v66,v51,v65
v_mfma_f32_16x16x32_bf16 v[32:35],a[140:143],v[164:167],v[32:35]
s_barrier
v_mfma_f32_16x16x32_bf16 v[28:31],a[136:139],v[164:167],v[28:31]
s_cselect_b32 s53,s57,s53
ds_read_b128 v[128:131],v2 offset:40960
ds_read_b128 v[60:63],v8 offset:0
v_mfma_f32_16x16x32_bf16 v[28:31],a[128:131],v[160:163],v[28:31]
s_cmp_eq_u32 s53,s58
v_rcp_f32 v50,v50
v_mfma_f32_16x16x32_bf16 v[28:31],a[144:147],v[168:171],v[28:31]
buffer_load_dwordx4 v0,s[36:39],s67 offen offset:2048 sc1 lds
v_mfma_f32_16x16x32_bf16 v[28:31],a[152:155],v[172:175],v[28:31]
s_cselect_b32 s53,s59,s53
ds_read_b128 v[132:135],v2 offset:41984
v_mul_f32 v51,v43,v51
v_exp_f32 v51,v51
v_mfma_f32_16x16x32_bf16 v[32:35],a[156:159],v[172:175],v[32:35]
s_cmp_ge_u32 s66,s59
ds_read_b128 v[136:139],v2 offset:43008
v_fma_f32 v51,v51,v67,v67
v_mfma_f32_16x16x32_bf16 v[32:35],a[148:151],v[168:171],v[32:35]
s_waitcnt lgkmcnt(4)
v_rcp_f32 v51,v51
v_mfma_f32_16x16x32_bf16 v[32:35],a[164:167],v[176:179],v[32:35]
s_waitcnt lgkmcnt(2)
buffer_load_dwordx4 v0,s[36:39],s67 offen offset:3072 sc1 lds
v_mfma_f32_16x16x32_bf16 v[32:35],a[172:175],v[180:183],v[32:35]
ds_read_b128 v[140:143],v2 offset:44032
v_mul_f32 v40,v40,v48
v_mul_f32 v41,v41,v49
v_mul_f32 v42,v42,v50
v_mul_f32 v43,v43,v51
v_cvt_pk_bf16_f32 v54,v40,v41
v_cvt_pk_bf16_f32 v55,v42,v43
v_mfma_f32_16x16x32_bf16 v[28:31],a[168:171],v[180:183],v[28:31]
buffer_store_dwordx4 v[52:55],v3,s[36:39],s73 offen offset:0 sc1
ds_read_b128 v[144:147],v2 offset:45056
v_mfma_f32_16x16x32_bf16 v[28:31],a[160:163],v[176:179],v[28:31]
v_mfma_f32_16x16x32_bf16 v[28:31],a[176:179],v[184:187],v[28:31]
ds_read_b128 v[148:151],v2 offset:46080
v_mfma_f32_16x16x32_bf16 v[28:31],a[184:187],v[188:191],v[28:31]
v_mfma_f32_16x16x32_bf16 v[32:35],a[188:191],v[188:191],v[32:35]
s_waitcnt lgkmcnt(2)
ds_read_b128 v[152:155],v2 offset:47104
v_mfma_f32_16x16x32_bf16 v[32:35],a[180:183],v[184:187],v[32:35]
s_cbranch_scc1 Lg1_slow1_%=
Lg1_d1w2_%=:
v_mfma_f32_16x16x32_bf16 v[40:43],a[68:71],v[128:131],0
s_add_u32 m0,s62,16384
ds_read_b128 v[156:159],v2 offset:48128
s_lshr_b32 s73,s48,0
v_mfma_f32_16x16x32_bf16 v[40:43],a[76:79],v[132:135],v[40:43]
s_lshr_b32 s72,s67,6
s_lshr_b32 s71,s62,2
s_cmp_lt_u32 s67,4194304
v_mfma_f32_16x16x32_bf16 v[36:39],a[72:75],v[132:135],0
s_cselect_b32 s72,s59,s72
ds_read_b128 v[160:163],v2 offset:32768
s_mov_b64 s[44:45],s[46:47]
v_mfma_f32_16x16x32_bf16 v[36:39],a[64:67],v[128:131],v[36:39]
buffer_load_dwordx4 v1,s[36:39],s67 offen offset:0 sc1 lds
v_mfma_f32_16x16x32_bf16 v[36:39],a[80:83],v[136:139],v[36:39]
s_mov_b64 s[46:47],s[48:49]
ds_read_b128 v[164:167],v2 offset:33792
v_fma_f32 v28,-v60,v20,v28
v_mfma_f32_16x16x32_bf16 v[36:39],a[88:91],v[140:143],v[36:39]
s_mov_b64 s[48:49],s[50:51]
v_fma_f32 v29,-v60,v21,v29
v_fma_f32 v30,-v60,v22,v30
v_fma_f32 v31,-v60,v23,v31
v_mul_f32 v44,v28,v28
v_mul_f32 v45,v29,v29
v_mfma_f32_16x16x32_bf16 v[40:43],a[92:95],v[140:143],v[40:43]
ds_read_b128 v[168:171],v2 offset:34816
v_mul_f32 v46,v30,v30
v_mfma_f32_16x16x32_bf16 v[40:43],a[84:87],v[136:139],v[40:43]
s_waitcnt lgkmcnt(3)
buffer_load_dwordx4 v1,s[36:39],s67 offen offset:1024 sc1 lds
v_mfma_f32_16x16x32_bf16 v[40:43],a[100:103],v[144:147],v[40:43]
ds_read_b128 v[172:175],v2 offset:35840
v_mul_f32 v47,v31,v31
v_mfma_f32_16x16x32_bf16 v[40:43],a[108:111],v[148:151],v[40:43]
v_fma_f32 v44,v62,v44,v61
v_mul_f32 v44,v28,v44
v_exp_f32 v44,v44
v_mfma_f32_16x16x32_bf16 v[36:39],a[104:107],v[148:151],v[36:39]
ds_read_b128 v[176:179],v2 offset:36864
v_fma_f32 v44,v44,v63,v63
v_mfma_f32_16x16x32_bf16 v[36:39],a[96:99],v[144:147],v[36:39]
buffer_load_dwordx4 v1,s[36:39],s67 offen offset:2048 sc1 lds
v_mfma_f32_16x16x32_bf16 v[36:39],a[112:115],v[152:155],v[36:39]
ds_read_b128 v[180:183],v2 offset:37888
v_fma_f32 v45,v62,v45,v61
v_mfma_f32_16x16x32_bf16 v[36:39],a[120:123],v[156:159],v[36:39]
v_rcp_f32 v44,v44
v_mul_f32 v45,v29,v45
v_fma_f32 v46,v62,v46,v61
v_mfma_f32_16x16x32_bf16 v[40:43],a[124:127],v[156:159],v[40:43]
ds_read_b128 v[184:187],v2 offset:38912
v_mul_f32 v46,v30,v46
v_mfma_f32_16x16x32_bf16 v[40:43],a[116:119],v[152:155],v[40:43]
s_waitcnt lgkmcnt(3)
buffer_load_dwordx4 v1,s[36:39],s67 offen offset:3072 sc1 lds
s_add_u32 m0,s71,s88
v_mfma_f32_16x16x32_bf16 v[40:43],a[4:7],v[160:163],v[40:43]
s_xor_b32 s87,s63,65536
ds_read_b128 v[188:191],v2 offset:39936
buffer_load_dword v10,s[36:39],s72 offen offset:0 sc1 lds
s_waitcnt vmcnt(10)
v_mfma_f32_16x16x32_bf16 v[40:43],a[12:15],v[164:167],v[40:43]
s_barrier
v_mfma_f32_16x16x32_bf16 v[36:39],a[8:11],v[164:167],v[36:39]
s_cmp_ge_u32 s67,s59
ds_read_b128 v[128:131],v2 offset:57344
v_fma_f32 v47,v62,v47,v61
v_mfma_f32_16x16x32_bf16 v[36:39],a[0:3],v[160:163],v[36:39]
s_cselect_b32 s86,s50,s51
v_exp_f32 v45,v45
v_mfma_f32_16x16x32_bf16 v[36:39],a[16:19],v[168:171],v[36:39]
s_cselect_b32 s87,s87,s64
ds_read_b128 v[132:135],v2 offset:58368
v_mul_f32 v47,v31,v47
v_mfma_f32_16x16x32_bf16 v[36:39],a[24:27],v[172:175],v[36:39]
s_mov_b32 s62,s87
v_exp_f32 v46,v46
v_exp_f32 v47,v47
v_mfma_f32_16x16x32_bf16 v[40:43],a[28:31],v[172:175],v[40:43]
s_mov_b32 m0,s62
ds_read_b128 v[136:139],v2 offset:59392
v_fma_f32 v45,v45,v63,v63
v_mfma_f32_16x16x32_bf16 v[40:43],a[20:23],v[168:171],v[40:43]
s_waitcnt lgkmcnt(3)
v_rcp_f32 v45,v45
v_mfma_f32_16x16x32_bf16 v[40:43],a[36:39],v[176:179],v[40:43]
s_mov_b64 s[50:51],s[52:53]
ds_read_b128 v[140:143],v2 offset:60416
v_fma_f32 v46,v46,v63,v63
v_mfma_f32_16x16x32_bf16 v[40:43],a[44:47],v[180:183],v[40:43]
v_rcp_f32 v46,v46
v_fma_f32 v47,v47,v63,v63
v_mul_f32 v28,v28,v44
v_mfma_f32_16x16x32_bf16 v[36:39],a[40:43],v[180:183],v[36:39]
ds_read_b128 v[144:147],v2 offset:61440
v_mul_f32 v29,v29,v45
v_mfma_f32_16x16x32_bf16 v[36:39],a[32:35],v[176:179],v[36:39]
v_rcp_f32 v47,v47
v_mfma_f32_16x16x32_bf16 v[36:39],a[48:51],v[184:187],v[36:39]
ds_read_b128 v[148:151],v2 offset:62464
v_mul_f32 v30,v30,v46
v_mfma_f32_16x16x32_bf16 v[36:39],a[56:59],v[188:191],v[36:39]
v_mul_f32 v31,v31,v47
v_cvt_pk_bf16_f32 v52,v28,v29
v_cvt_pk_bf16_f32 v53,v30,v31
v_fma_f32 v32,-v60,v24,v32
v_fma_f32 v33,-v60,v25,v33
v_mfma_f32_16x16x32_bf16 v[40:43],a[60:63],v[188:191],v[40:43]
ds_read_b128 v[152:155],v2 offset:63488
v_fma_f32 v34,-v60,v26,v34
v_mfma_f32_16x16x32_bf16 v[40:43],a[52:55],v[184:187],v[40:43]
s_waitcnt lgkmcnt(3)
v_fma_f32 v35,-v60,v27,v35)"
R"(
v_mul_f32 v48,v32,v32
v_mfma_f32_16x16x32_bf16 v[40:43],a[196:199],v[128:131],v[40:43]
s_nop 0
ds_read_b128 v[156:159],v2 offset:64512
v_mul_f32 v49,v33,v33
v_mfma_f32_16x16x32_bf16 v[40:43],a[204:207],v[132:135],v[40:43]
s_barrier
v_mfma_f32_16x16x32_bf16 v[36:39],a[200:203],v[132:135],v[36:39]
ds_read_b128 v[160:163],v2 offset:49152
ds_read_b32 v16,v7 offset:256
v_mfma_f32_16x16x32_bf16 v[36:39],a[192:195],v[128:131],v[36:39]
v_mul_f32 v50,v34,v34
v_mul_f32 v51,v35,v35
v_mfma_f32_16x16x32_bf16 v[36:39],a[208:211],v[136:139],v[36:39]
ds_read_b128 v[164:167],v2 offset:50176
v_fma_f32 v48,v62,v48,v61
v_mfma_f32_16x16x32_bf16 v[36:39],a[216:219],v[140:143],v[36:39]
v_mul_f32 v48,v32,v48
v_exp_f32 v48,v48
v_fma_f32 v49,v62,v49,v61
v_mfma_f32_16x16x32_bf16 v[40:43],a[220:223],v[140:143],v[40:43]
s_waitcnt lgkmcnt(1)
ds_read_b128 v[168:171],v2 offset:51200
v_mul_f32 v49,v33,v49
v_mfma_f32_16x16x32_bf16 v[40:43],a[212:215],v[136:139],v[40:43]
v_exp_f32 v49,v49
v_mfma_f32_16x16x32_bf16 v[40:43],a[228:231],v[144:147],v[40:43]
v_cmp_eq_u32 vcc,s60,v16
v_fma_f32 v48,v48,v63,v63
v_rcp_f32 v48,v48
v_mfma_f32_16x16x32_bf16 v[40:43],a[236:239],v[148:151],v[40:43]
s_cmp_eq_u32 vcc_lo,-1
ds_read_b128 v[172:175],v2 offset:52224
v_fma_f32 v49,v49,v63,v63
v_fma_f32 v50,v62,v50,v61
s_cselect_b32 s66,s86,s59
v_mfma_f32_16x16x32_bf16 v[36:39],a[232:235],v[148:151],v[36:39]
buffer_load_dwordx4 v0,s[36:39],s66 offen offset:0 sc1 lds
v_mfma_f32_16x16x32_bf16 v[36:39],a[224:227],v[144:147],v[36:39]
s_xor_b32 s64,s64,65536
ds_read_b128 v[176:179],v2 offset:53248
v_mul_f32 v50,v34,v50
v_mfma_f32_16x16x32_bf16 v[36:39],a[240:243],v[152:155],v[36:39]
s_add_u32 s74,s53,s55
ds_read_b128 v[180:183],v2 offset:54272
v_fma_f32 v51,v62,v51,v61
v_mfma_f32_16x16x32_bf16 v[36:39],a[248:251],v[156:159],v[36:39]
s_cmp_eq_u32 s74,s56
v_exp_f32 v50,v50
v_rcp_f32 v49,v49
v_mfma_f32_16x16x32_bf16 v[40:43],a[252:255],v[156:159],v[40:43]
buffer_load_dwordx4 v0,s[36:39],s66 offen offset:1024 sc1 lds
v_mfma_f32_16x16x32_bf16 v[40:43],a[244:247],v[152:155],v[40:43]
s_waitcnt lgkmcnt(2)
ds_read_b128 v[184:187],v2 offset:55296
v_mul_f32 v51,v35,v51
v_mfma_f32_16x16x32_bf16 v[40:43],a[132:135],v[160:163],v[40:43]
s_waitcnt vmcnt(7)
ds_read_b128 v[188:191],v2 offset:56320
v_xor_b32 v2,65536,v2
v_mfma_f32_16x16x32_bf16 v[40:43],a[140:143],v[164:167],v[40:43]
s_barrier
v_mfma_f32_16x16x32_bf16 v[36:39],a[136:139],v[164:167],v[36:39]
s_cselect_b32 s74,s57,s74
ds_read_b128 v[128:131],v2 offset:8192
ds_read_b128 v[64:67],v8 offset:8192
v_mfma_f32_16x16x32_bf16 v[36:39],a[128:131],v[160:163],v[36:39]
s_cmp_eq_u32 s74,s58
v_exp_f32 v51,v51
v_mfma_f32_16x16x32_bf16 v[36:39],a[144:147],v[168:171],v[36:39]
buffer_load_dwordx4 v0,s[36:39],s66 offen offset:2048 sc1 lds
v_mfma_f32_16x16x32_bf16 v[36:39],a[152:155],v[172:175],v[36:39]
s_cselect_b32 s74,s59,s74
ds_read_b128 v[132:135],v2 offset:9216
v_fma_f32 v50,v50,v63,v63
v_rcp_f32 v50,v50
v_mfma_f32_16x16x32_bf16 v[40:43],a[156:159],v[172:175],v[40:43]
s_mov_b32 s52,s74
ds_read_b128 v[136:139],v2 offset:10240
v_fma_f32 v51,v51,v63,v63
v_mfma_f32_16x16x32_bf16 v[40:43],a[148:151],v[168:171],v[40:43]
s_waitcnt lgkmcnt(4)
v_rcp_f32 v51,v51
v_mfma_f32_16x16x32_bf16 v[40:43],a[164:167],v[176:179],v[40:43]
s_waitcnt lgkmcnt(2)
buffer_load_dwordx4 v0,s[36:39],s66 offen offset:3072 sc1 lds
v_mfma_f32_16x16x32_bf16 v[40:43],a[172:175],v[180:183],v[40:43]
s_cmp_lt_u32 s67,s59
ds_read_b128 v[140:143],v2 offset:11264
v_mul_f32 v32,v32,v48
v_mul_f32 v33,v33,v49
v_mul_f32 v34,v34,v50
v_mul_f32 v35,v35,v51
v_cvt_pk_bf16_f32 v54,v32,v33
v_cvt_pk_bf16_f32 v55,v34,v35
v_mfma_f32_16x16x32_bf16 v[36:39],a[168:171],v[180:183],v[36:39]
buffer_store_dwordx4 v[52:55],v3,s[36:39],s73 offen offset:0 sc1
ds_read_b128 v[144:147],v2 offset:12288
v_xor_b32 v8,16384,v8
v_mfma_f32_16x16x32_bf16 v[36:39],a[160:163],v[176:179],v[36:39]
v_mfma_f32_16x16x32_bf16 v[36:39],a[176:179],v[184:187],v[36:39]
ds_read_b128 v[148:151],v2 offset:13312
v_mfma_f32_16x16x32_bf16 v[36:39],a[184:187],v[188:191],v[36:39]
v_mfma_f32_16x16x32_bf16 v[40:43],a[188:191],v[188:191],v[40:43]
s_waitcnt lgkmcnt(2)
ds_read_b128 v[152:155],v2 offset:14336
v_mfma_f32_16x16x32_bf16 v[40:43],a[180:183],v[184:187],v[40:43]
s_cbranch_scc1 Lg1_d0w2_%=
s_branch Lg1_slow0_%=
Lg1_d0w3_%=:
v_mfma_f32_16x16x32_bf16 v[32:35],a[100:103],v[128:131],0
s_add_u32 m0,s62,16384
ds_read_b128 v[156:159],v2 offset:11264
s_lshr_b32 s73,s47,0
v_mfma_f32_16x16x32_bf16 v[32:35],a[108:111],v[132:135],v[32:35]
s_lshr_b32 s72,s66,6
s_lshr_b32 s71,s62,2
s_cmp_lt_u32 s66,4194304
v_mfma_f32_16x16x32_bf16 v[28:31],a[104:107],v[132:135],0
s_cselect_b32 s72,s59,s72
ds_read_b128 v[160:163],v2 offset:4096
v_mfma_f32_16x16x32_bf16 v[28:31],a[96:99],v[128:131],v[28:31]
v_fma_f32 v36,-v64,v20,v36
v_fma_f32 v37,-v64,v21,v37
v_mfma_f32_16x16x32_bf16 v[28:31],a[112:115],v[136:139],v[28:31]
buffer_load_dwordx4 v1,s[36:39],s66 offen offset:0 sc1 lds
v_mfma_f32_16x16x32_bf16 v[28:31],a[120:123],v[140:143],v[28:31]
ds_read_b128 v[164:167],v2 offset:5120
v_fma_f32 v38,-v64,v22,v38
v_fma_f32 v39,-v64,v23,v39
v_mfma_f32_16x16x32_bf16 v[32:35],a[124:127],v[140:143],v[32:35]
ds_read_b128 v[168:171],v2 offset:6144
v_mul_f32 v44,v36,v36
v_mfma_f32_16x16x32_bf16 v[32:35],a[116:119],v[136:139],v[32:35]
s_waitcnt lgkmcnt(3)
v_mul_f32 v45,v37,v37
v_mul_f32 v46,v38,v38
v_mfma_f32_16x16x32_bf16 v[32:35],a[68:71],v[144:147],v[32:35]
buffer_load_dwordx4 v1,s[36:39],s66 offen offset:1024 sc1 lds
v_mfma_f32_16x16x32_bf16 v[32:35],a[76:79],v[148:151],v[32:35]
ds_read_b128 v[172:175],v2 offset:7168
v_mul_f32 v47,v39,v39
v_fma_f32 v44,v66,v44,v65
v_mfma_f32_16x16x32_bf16 v[28:31],a[72:75],v[148:151],v[28:31]
ds_read_b128 v[176:179],v2 offset:0
v_mul_f32 v44,v36,v44
v_mfma_f32_16x16x32_bf16 v[28:31],a[64:67],v[144:147],v[28:31]
v_exp_f32 v44,v44
v_mfma_f32_16x16x32_bf16 v[28:31],a[80:83],v[152:155],v[28:31]
buffer_load_dwordx4 v1,s[36:39],s66 offen offset:2048 sc1 lds
v_mfma_f32_16x16x32_bf16 v[28:31],a[88:91],v[156:159],v[28:31]
ds_read_b128 v[180:183],v2 offset:1024
v_fma_f32 v44,v44,v67,v67
v_fma_f32 v45,v66,v45,v65
v_mfma_f32_16x16x32_bf16 v[32:35],a[92:95],v[156:159],v[32:35]
ds_read_b128 v[184:187],v2 offset:2048
v_mul_f32 v45,v37,v45
v_mfma_f32_16x16x32_bf16 v[32:35],a[84:87],v[152:155],v[32:35]
s_waitcnt lgkmcnt(3)
v_exp_f32 v45,v45
v_mfma_f32_16x16x32_bf16 v[32:35],a[36:39],v[160:163],v[32:35]
s_waitcnt vmcnt(9)
ds_read_b128 v[188:191],v2 offset:3072
v_fma_f32 v45,v45,v67,v67
v_mfma_f32_16x16x32_bf16 v[32:35],a[44:47],v[164:167],v[32:35]
s_barrier
v_mfma_f32_16x16x32_bf16 v[28:31],a[40:43],v[164:167],v[28:31]
buffer_load_dwordx4 v1,s[36:39],s66 offen offset:3072 sc1 lds
v_mfma_f32_16x16x32_bf16 v[28:31],a[32:35],v[160:163],v[28:31]
s_add_u32 m0,s71,s88
ds_read_b128 v[128:131],v2 offset:28672
v_fma_f32 v46,v66,v46,v65
v_mfma_f32_16x16x32_bf16 v[28:31],a[48:51],v[168:171],v[28:31]
buffer_load_dword v10,s[36:39],s72 offen offset:0 sc1 lds
v_mfma_f32_16x16x32_bf16 v[28:31],a[56:59],v[172:175],v[28:31]
s_xor_b32 s87,s64,65536
ds_read_b128 v[132:135],v2 offset:29696
v_rcp_f32 v44,v44
v_mul_f32 v46,v38,v46
v_mfma_f32_16x16x32_bf16 v[32:35],a[60:63],v[172:175],v[32:35]
s_cmp_ge_u32 s66,s59
ds_read_b128 v[136:139],v2 offset:30720
v_fma_f32 v47,v66,v47,v65
v_mfma_f32_16x16x32_bf16 v[32:35],a[52:55],v[168:171],v[32:35]
s_waitcnt lgkmcnt(3)
v_exp_f32 v46,v46
v_mfma_f32_16x16x32_bf16 v[32:35],a[4:7],v[176:179],v[32:35]
s_cselect_b32 s86,s49,s50
ds_read_b128 v[140:143],v2 offset:31744
v_mul_f32 v47,v39,v47
v_mfma_f32_16x16x32_bf16 v[32:35],a[12:15],v[180:183],v[32:35]
s_cselect_b32 s87,s87,s63
v_exp_f32 v47,v47
v_rcp_f32 v45,v45
v_mfma_f32_16x16x32_bf16 v[28:31],a[8:11],v[180:183],v[28:31]
s_mov_b32 s62,s87
ds_read_b128 v[144:147],v2 offset:24576
v_fma_f32 v46,v46,v67,v67
v_mfma_f32_16x16x32_bf16 v[28:31],a[0:3],v[176:179],v[28:31]
s_mov_b32 m0,s62)"
R"(
v_rcp_f32 v46,v46
v_mfma_f32_16x16x32_bf16 v[28:31],a[16:19],v[184:187],v[28:31]
ds_read_b128 v[148:151],v2 offset:25600
v_fma_f32 v47,v47,v67,v67
v_mfma_f32_16x16x32_bf16 v[28:31],a[24:27],v[188:191],v[28:31]
v_rcp_f32 v47,v47
v_mul_f32 v36,v36,v44
v_mul_f32 v37,v37,v45
v_mul_f32 v38,v38,v46
v_mfma_f32_16x16x32_bf16 v[32:35],a[28:31],v[188:191],v[32:35]
ds_read_b128 v[152:155],v2 offset:26624
v_mul_f32 v39,v39,v47
v_mfma_f32_16x16x32_bf16 v[32:35],a[20:23],v[184:187],v[32:35]
s_waitcnt lgkmcnt(3)
v_cvt_pk_bf16_f32 v52,v36,v37
v_cvt_pk_bf16_f32 v53,v38,v39
v_mfma_f32_16x16x32_bf16 v[32:35],a[228:231],v[128:131],v[32:35]
s_nop 0
ds_read_b128 v[156:159],v2 offset:27648
v_fma_f32 v40,-v64,v24,v40
v_mfma_f32_16x16x32_bf16 v[32:35],a[236:239],v[132:135],v[32:35]
s_barrier
v_mfma_f32_16x16x32_bf16 v[28:31],a[232:235],v[132:135],v[28:31]
ds_read_b128 v[160:163],v2 offset:20480
ds_read_b32 v16,v7 offset:0
v_mfma_f32_16x16x32_bf16 v[28:31],a[224:227],v[128:131],v[28:31]
v_fma_f32 v41,-v64,v25,v41
v_fma_f32 v42,-v64,v26,v42
v_mfma_f32_16x16x32_bf16 v[28:31],a[240:243],v[136:139],v[28:31]
ds_read_b128 v[164:167],v2 offset:21504
v_fma_f32 v43,-v64,v27,v43
v_mfma_f32_16x16x32_bf16 v[28:31],a[248:251],v[140:143],v[28:31]
v_mul_f32 v48,v40,v40
v_mul_f32 v49,v41,v41
v_mul_f32 v50,v42,v42
v_mul_f32 v51,v43,v43
v_mfma_f32_16x16x32_bf16 v[32:35],a[252:255],v[140:143],v[32:35]
s_waitcnt lgkmcnt(1)
ds_read_b128 v[168:171],v2 offset:22528
v_fma_f32 v48,v66,v48,v65
v_mfma_f32_16x16x32_bf16 v[32:35],a[244:247],v[136:139],v[32:35]
v_mul_f32 v48,v40,v48
v_fma_f32 v49,v66,v49,v65
v_mfma_f32_16x16x32_bf16 v[32:35],a[196:199],v[144:147],v[32:35]
ds_read_b128 v[172:175],v2 offset:23552
v_mul_f32 v49,v41,v49
v_mfma_f32_16x16x32_bf16 v[32:35],a[204:207],v[148:151],v[32:35]
v_cmp_eq_u32 vcc,s60,v16
v_exp_f32 v48,v48
v_exp_f32 v49,v49
v_mfma_f32_16x16x32_bf16 v[28:31],a[200:203],v[148:151],v[28:31]
s_cmp_eq_u32 vcc_lo,-1
ds_read_b128 v[176:179],v2 offset:16384
s_cselect_b32 s67,s86,s59
v_mfma_f32_16x16x32_bf16 v[28:31],a[192:195],v[144:147],v[28:31]
buffer_load_dwordx4 v0,s[36:39],s67 offen offset:0 sc1 lds
v_mfma_f32_16x16x32_bf16 v[28:31],a[208:211],v[152:155],v[28:31]
s_lshr_b32 s70,s46,5
ds_read_b128 v[180:183],v2 offset:17408
v_fma_f32 v48,v48,v67,v67
v_mfma_f32_16x16x32_bf16 v[28:31],a[216:219],v[156:159],v[28:31]
s_xor_b32 s63,s63,65536
v_rcp_f32 v48,v48
v_fma_f32 v49,v49,v67,v67
v_rcp_f32 v49,v49
v_mfma_f32_16x16x32_bf16 v[32:35],a[220:223],v[156:159],v[32:35]
s_add_u32 s53,s52,s55
ds_read_b128 v[184:187],v2 offset:18432
v_fma_f32 v50,v66,v50,v65
v_mfma_f32_16x16x32_bf16 v[32:35],a[212:215],v[152:155],v[32:35]
s_waitcnt lgkmcnt(3)
buffer_load_dwordx4 v0,s[36:39],s67 offen offset:1024 sc1 lds
v_mfma_f32_16x16x32_bf16 v[32:35],a[164:167],v[160:163],v[32:35]
s_waitcnt vmcnt(7)
ds_read_b128 v[188:191],v2 offset:19456
v_mul_f32 v50,v42,v50
v_mfma_f32_16x16x32_bf16 v[32:35],a[172:175],v[164:167],v[32:35]
s_barrier
v_mfma_f32_16x16x32_bf16 v[28:31],a[168:171],v[164:167],v[28:31]
s_cmp_eq_u32 s53,s56
ds_read_b128 v[128:131],v2 offset:45056
ds_read_b128 v[60:63],v8 offset:0
v_mfma_f32_16x16x32_bf16 v[28:31],a[160:163],v[160:163],v[28:31]
s_cselect_b32 s53,s57,s53
v_exp_f32 v50,v50
v_mfma_f32_16x16x32_bf16 v[28:31],a[176:179],v[168:171],v[28:31]
s_cmp_eq_u32 s53,s58
ds_read_b128 v[132:135],v2 offset:46080
v_fma_f32 v50,v50,v67,v67
v_mfma_f32_16x16x32_bf16 v[28:31],a[184:187],v[172:175],v[28:31]
buffer_store_dword v5,v4,s[36:39],s70 offen offset:0 sc1
buffer_load_dwordx4 v0,s[36:39],s67 offen offset:2048 sc1 lds
v_rcp_f32 v50,v50
v_mfma_f32_16x16x32_bf16 v[32:35],a[188:191],v[172:175],v[32:35]
s_cselect_b32 s53,s59,s53
ds_read_b128 v[136:139],v2 offset:47104
v_fma_f32 v51,v66,v51,v65
v_mfma_f32_16x16x32_bf16 v[32:35],a[180:183],v[168:171],v[32:35]
s_waitcnt lgkmcnt(4)
v_mul_f32 v51,v43,v51
v_mul_f32 v40,v40,v48
v_mfma_f32_16x16x32_bf16 v[32:35],a[132:135],v[176:179],v[32:35]
s_waitcnt lgkmcnt(2)
v_exp_f32 v51,v51
v_mfma_f32_16x16x32_bf16 v[32:35],a[140:143],v[180:183],v[32:35]
s_cmp_ge_u32 s66,s59
ds_read_b128 v[140:143],v2 offset:48128
buffer_load_dwordx4 v0,s[36:39],s67 offen offset:3072 sc1 lds
v_fma_f32 v51,v51,v67,v67
v_rcp_f32 v51,v51
v_mfma_f32_16x16x32_bf16 v[28:31],a[136:139],v[180:183],v[28:31]
v_mul_f32 v41,v41,v49
v_mul_f32 v42,v42,v50
v_mul_f32 v43,v43,v51
v_cvt_pk_bf16_f32 v54,v40,v41
v_cvt_pk_bf16_f32 v55,v42,v43
v_mfma_f32_16x16x32_bf16 v[28:31],a[128:131],v[176:179],v[28:31]
ds_read_b128 v[144:147],v2 offset:40960
buffer_store_dwordx4 v[52:55],v3,s[36:39],s73 offen offset:0 sc1
v_mfma_f32_16x16x32_bf16 v[28:31],a[144:147],v[184:187],v[28:31]
ds_read_b128 v[148:151],v2 offset:41984
v_mfma_f32_16x16x32_bf16 v[28:31],a[152:155],v[188:191],v[28:31]
v_mfma_f32_16x16x32_bf16 v[32:35],a[156:159],v[188:191],v[32:35]
s_waitcnt lgkmcnt(2)
ds_read_b128 v[152:155],v2 offset:43008
v_mfma_f32_16x16x32_bf16 v[32:35],a[148:151],v[184:187],v[32:35]
s_cbranch_scc1 Lg1_slow1_%=
Lg1_d1w3_%=:
v_mfma_f32_16x16x32_bf16 v[40:43],a[100:103],v[128:131],0
s_add_u32 m0,s62,16384
ds_read_b128 v[156:159],v2 offset:44032
s_lshr_b32 s73,s48,0
v_mfma_f32_16x16x32_bf16 v[40:43],a[108:111],v[132:135],v[40:43]
s_lshr_b32 s72,s67,6
s_lshr_b32 s71,s62,2
s_cmp_lt_u32 s67,4194304
v_mfma_f32_16x16x32_bf16 v[36:39],a[104:107],v[132:135],0
s_cselect_b32 s72,s59,s72
ds_read_b128 v[160:163],v2 offset:36864
s_mov_b64 s[44:45],s[46:47]
v_mfma_f32_16x16x32_bf16 v[36:39],a[96:99],v[128:131],v[36:39]
v_fma_f32 v28,-v60,v20,v28
v_fma_f32 v29,-v60,v21,v29
v_mfma_f32_16x16x32_bf16 v[36:39],a[112:115],v[136:139],v[36:39]
buffer_load_dwordx4 v1,s[36:39],s67 offen offset:0 sc1 lds
v_mfma_f32_16x16x32_bf16 v[36:39],a[120:123],v[140:143],v[36:39]
ds_read_b128 v[164:167],v2 offset:37888
v_fma_f32 v30,-v60,v22,v30
v_fma_f32 v31,-v60,v23,v31
v_mfma_f32_16x16x32_bf16 v[40:43],a[124:127],v[140:143],v[40:43]
ds_read_b128 v[168:171],v2 offset:38912
v_mul_f32 v44,v28,v28
v_mfma_f32_16x16x32_bf16 v[40:43],a[116:119],v[136:139],v[40:43]
s_waitcnt lgkmcnt(3)
v_mul_f32 v45,v29,v29
v_mul_f32 v46,v30,v30
v_mfma_f32_16x16x32_bf16 v[40:43],a[68:71],v[144:147],v[40:43]
buffer_load_dwordx4 v1,s[36:39],s67 offen offset:1024 sc1 lds
v_mfma_f32_16x16x32_bf16 v[40:43],a[76:79],v[148:151],v[40:43]
ds_read_b128 v[172:175],v2 offset:39936
v_mul_f32 v47,v31,v31
v_fma_f32 v44,v62,v44,v61
v_mfma_f32_16x16x32_bf16 v[36:39],a[72:75],v[148:151],v[36:39]
ds_read_b128 v[176:179],v2 offset:32768
v_mul_f32 v44,v28,v44
v_mfma_f32_16x16x32_bf16 v[36:39],a[64:67],v[144:147],v[36:39]
v_exp_f32 v44,v44
v_mfma_f32_16x16x32_bf16 v[36:39],a[80:83],v[152:155],v[36:39]
buffer_load_dwordx4 v1,s[36:39],s67 offen offset:2048 sc1 lds
v_mfma_f32_16x16x32_bf16 v[36:39],a[88:91],v[156:159],v[36:39]
ds_read_b128 v[180:183],v2 offset:33792
v_fma_f32 v44,v44,v63,v63
v_fma_f32 v45,v62,v45,v61
v_mfma_f32_16x16x32_bf16 v[40:43],a[92:95],v[156:159],v[40:43]
ds_read_b128 v[184:187],v2 offset:34816
v_mul_f32 v45,v29,v45
v_mfma_f32_16x16x32_bf16 v[40:43],a[84:87],v[152:155],v[40:43]
s_waitcnt lgkmcnt(3)
v_exp_f32 v45,v45
v_mfma_f32_16x16x32_bf16 v[40:43],a[36:39],v[160:163],v[40:43]
s_waitcnt vmcnt(9)
ds_read_b128 v[188:191],v2 offset:35840
v_fma_f32 v45,v45,v63,v63
v_mfma_f32_16x16x32_bf16 v[40:43],a[44:47],v[164:167],v[40:43]
s_barrier
v_mfma_f32_16x16x32_bf16 v[36:39],a[40:43],v[164:167],v[36:39]
buffer_load_dwordx4 v1,s[36:39],s67 offen offset:3072 sc1 lds
v_mfma_f32_16x16x32_bf16 v[36:39],a[32:35],v[160:163],v[36:39]
s_add_u32 m0,s71,s88
ds_read_b128 v[128:131],v2 offset:61440
v_fma_f32 v46,v62,v46,v61
v_mfma_f32_16x16x32_bf16 v[36:39],a[48:51],v[168:171],v[36:39]
buffer_load_dword v10,s[36:39],s72 offen offset:0 sc1 lds
v_mfma_f32_16x16x32_bf16 v[36:39],a[56:59],v[172:175],v[36:39]
s_xor_b32 s87,s63,65536
ds_read_b128 v[132:135],v2 offset:62464
v_rcp_f32 v44,v44
v_mul_f32 v46,v30,v46
v_mfma_f32_16x16x32_bf16 v[40:43],a[60:63],v[172:175],v[40:43]
s_cmp_ge_u32 s67,s59)"
R"(
ds_read_b128 v[136:139],v2 offset:63488
v_fma_f32 v47,v62,v47,v61
v_mfma_f32_16x16x32_bf16 v[40:43],a[52:55],v[168:171],v[40:43]
s_waitcnt lgkmcnt(3)
v_exp_f32 v46,v46
v_mfma_f32_16x16x32_bf16 v[40:43],a[4:7],v[176:179],v[40:43]
s_cselect_b32 s86,s50,s51
ds_read_b128 v[140:143],v2 offset:64512
v_mul_f32 v47,v31,v47
v_mfma_f32_16x16x32_bf16 v[40:43],a[12:15],v[180:183],v[40:43]
s_cselect_b32 s87,s87,s64
v_exp_f32 v47,v47
v_rcp_f32 v45,v45
v_mfma_f32_16x16x32_bf16 v[36:39],a[8:11],v[180:183],v[36:39]
s_mov_b32 s62,s87
ds_read_b128 v[144:147],v2 offset:57344
v_fma_f32 v46,v46,v63,v63
v_mfma_f32_16x16x32_bf16 v[36:39],a[0:3],v[176:179],v[36:39]
s_mov_b32 m0,s62
v_rcp_f32 v46,v46
v_mfma_f32_16x16x32_bf16 v[36:39],a[16:19],v[184:187],v[36:39]
ds_read_b128 v[148:151],v2 offset:58368
v_fma_f32 v47,v47,v63,v63
v_mfma_f32_16x16x32_bf16 v[36:39],a[24:27],v[188:191],v[36:39]
v_rcp_f32 v47,v47
v_mul_f32 v28,v28,v44
v_mul_f32 v29,v29,v45
v_mul_f32 v30,v30,v46
v_mfma_f32_16x16x32_bf16 v[40:43],a[28:31],v[188:191],v[40:43]
ds_read_b128 v[152:155],v2 offset:59392
v_mul_f32 v31,v31,v47
v_mfma_f32_16x16x32_bf16 v[40:43],a[20:23],v[184:187],v[40:43]
s_waitcnt lgkmcnt(3)
v_cvt_pk_bf16_f32 v52,v28,v29
v_cvt_pk_bf16_f32 v53,v30,v31
v_mfma_f32_16x16x32_bf16 v[40:43],a[228:231],v[128:131],v[40:43]
s_nop 0
ds_read_b128 v[156:159],v2 offset:60416
v_fma_f32 v32,-v60,v24,v32
v_mfma_f32_16x16x32_bf16 v[40:43],a[236:239],v[132:135],v[40:43]
s_barrier
v_mfma_f32_16x16x32_bf16 v[36:39],a[232:235],v[132:135],v[36:39]
ds_read_b128 v[160:163],v2 offset:53248
ds_read_b32 v16,v7 offset:256
v_mfma_f32_16x16x32_bf16 v[36:39],a[224:227],v[128:131],v[36:39]
v_fma_f32 v33,-v60,v25,v33
v_fma_f32 v34,-v60,v26,v34
v_mfma_f32_16x16x32_bf16 v[36:39],a[240:243],v[136:139],v[36:39]
ds_read_b128 v[164:167],v2 offset:54272
v_fma_f32 v35,-v60,v27,v35
v_mfma_f32_16x16x32_bf16 v[36:39],a[248:251],v[140:143],v[36:39]
v_mul_f32 v48,v32,v32
v_mul_f32 v49,v33,v33
v_mul_f32 v50,v34,v34
v_mul_f32 v51,v35,v35
v_mfma_f32_16x16x32_bf16 v[40:43],a[252:255],v[140:143],v[40:43]
s_waitcnt lgkmcnt(1)
ds_read_b128 v[168:171],v2 offset:55296
v_fma_f32 v48,v62,v48,v61
v_mfma_f32_16x16x32_bf16 v[40:43],a[244:247],v[136:139],v[40:43]
v_mul_f32 v48,v32,v48
v_fma_f32 v49,v62,v49,v61
v_mfma_f32_16x16x32_bf16 v[40:43],a[196:199],v[144:147],v[40:43]
ds_read_b128 v[172:175],v2 offset:56320
v_mul_f32 v49,v33,v49
v_mfma_f32_16x16x32_bf16 v[40:43],a[204:207],v[148:151],v[40:43]
v_cmp_eq_u32 vcc,s60,v16
v_exp_f32 v48,v48
v_exp_f32 v49,v49
v_mfma_f32_16x16x32_bf16 v[36:39],a[200:203],v[148:151],v[36:39]
s_cmp_eq_u32 vcc_lo,-1
ds_read_b128 v[176:179],v2 offset:49152
s_cselect_b32 s66,s86,s59
v_mfma_f32_16x16x32_bf16 v[36:39],a[192:195],v[144:147],v[36:39]
buffer_load_dwordx4 v0,s[36:39],s66 offen offset:0 sc1 lds
v_mfma_f32_16x16x32_bf16 v[36:39],a[208:211],v[152:155],v[36:39]
s_lshr_b32 s70,s47,5
ds_read_b128 v[180:183],v2 offset:50176
v_fma_f32 v48,v48,v63,v63
v_mfma_f32_16x16x32_bf16 v[36:39],a[216:219],v[156:159],v[36:39]
s_xor_b32 s64,s64,65536
v_rcp_f32 v48,v48
v_fma_f32 v49,v49,v63,v63
v_rcp_f32 v49,v49
v_mfma_f32_16x16x32_bf16 v[40:43],a[220:223],v[156:159],v[40:43]
s_add_u32 s74,s53,s55
ds_read_b128 v[184:187],v2 offset:51200
v_fma_f32 v50,v62,v50,v61
v_mfma_f32_16x16x32_bf16 v[40:43],a[212:215],v[152:155],v[40:43]
s_waitcnt lgkmcnt(3)
buffer_load_dwordx4 v0,s[36:39],s66 offen offset:1024 sc1 lds
v_mfma_f32_16x16x32_bf16 v[40:43],a[164:167],v[160:163],v[40:43]
s_waitcnt vmcnt(7)
ds_read_b128 v[188:191],v2 offset:52224
v_xor_b32 v2,65536,v2
v_mfma_f32_16x16x32_bf16 v[40:43],a[172:175],v[164:167],v[40:43]
s_barrier
v_mfma_f32_16x16x32_bf16 v[36:39],a[168:171],v[164:167],v[36:39]
s_cmp_eq_u32 s74,s56
ds_read_b128 v[128:131],v2 offset:12288
ds_read_b128 v[64:67],v8 offset:8192
v_mfma_f32_16x16x32_bf16 v[36:39],a[160:163],v[160:163],v[36:39]
s_cselect_b32 s74,s57,s74
v_mul_f32 v50,v34,v50
v_fma_f32 v51,v62,v51,v61
v_mfma_f32_16x16x32_bf16 v[36:39],a[176:179],v[168:171],v[36:39]
s_cmp_eq_u32 s74,s58
ds_read_b128 v[132:135],v2 offset:13312
v_mul_f32 v51,v35,v51
v_mfma_f32_16x16x32_bf16 v[36:39],a[184:187],v[172:175],v[36:39]
buffer_store_dword v5,v4,s[36:39],s70 offen offset:0 sc1
buffer_load_dwordx4 v0,s[36:39],s66 offen offset:2048 sc1 lds
v_exp_f32 v50,v50
v_mfma_f32_16x16x32_bf16 v[40:43],a[188:191],v[172:175],v[40:43]
s_cselect_b32 s74,s59,s74
ds_read_b128 v[136:139],v2 offset:14336
v_fma_f32 v50,v50,v63,v63
v_mfma_f32_16x16x32_bf16 v[40:43],a[180:183],v[168:171],v[40:43]
s_waitcnt lgkmcnt(4)
v_exp_f32 v51,v51
v_mfma_f32_16x16x32_bf16 v[40:43],a[132:135],v[176:179],v[40:43]
s_waitcnt lgkmcnt(2)
ds_read_b128 v[140:143],v2 offset:15360
v_fma_f32 v51,v51,v63,v63
v_mfma_f32_16x16x32_bf16 v[40:43],a[140:143],v[180:183],v[40:43]
buffer_load_dwordx4 v0,s[36:39],s66 offen offset:3072 sc1 lds
v_rcp_f32 v50,v50
v_rcp_f32 v51,v51
v_mfma_f32_16x16x32_bf16 v[36:39],a[136:139],v[180:183],v[36:39]
s_mov_b64 s[46:47],s[48:49]
v_mul_f32 v32,v32,v48
v_mul_f32 v33,v33,v49
v_mul_f32 v34,v34,v50
v_mul_f32 v35,v35,v51
v_cvt_pk_bf16_f32 v54,v32,v33
v_cvt_pk_bf16_f32 v55,v34,v35
v_mfma_f32_16x16x32_bf16 v[36:39],a[128:131],v[176:179],v[36:39]
s_mov_b64 s[48:49],s[50:51]
ds_read_b128 v[144:147],v2 offset:8192
buffer_store_dwordx4 v[52:55],v3,s[36:39],s73 offen offset:0 sc1
v_xor_b32 v8,16384,v8
s_mov_b64 s[50:51],s[52:53]
s_cmp_lt_u32 s67,s59
v_mfma_f32_16x16x32_bf16 v[36:39],a[144:147],v[184:187],v[36:39]
s_mov_b32 s52,s74
ds_read_b128 v[148:151],v2 offset:9216
v_mfma_f32_16x16x32_bf16 v[36:39],a[152:155],v[188:191],v[36:39]
v_mfma_f32_16x16x32_bf16 v[40:43],a[156:159],v[188:191],v[40:43]
s_waitcnt lgkmcnt(2)
ds_read_b128 v[152:155],v2 offset:10240
v_mfma_f32_16x16x32_bf16 v[40:43],a[148:151],v[184:187],v[40:43]
s_cbranch_scc1 Lg1_d0w3_%=
s_branch Lg1_slow0_%=
Lg1_slow0_%=:
s_cmp_lt_u32 s66,s59
s_cbranch_scc1 Lg1_gs0_%=
Lg1_slowb0_%=:
s_cmp_eq_u32 s54,1
s_cselect_b32 s68,s49,s48
s_lshr_b32 s69,s68,5
s_cmp_lt_u32 s68,4194304
s_cselect_b32 s68,0xffffcfff,-1
s_and_b32 s68,s68,s69
s_add_u32 m0,s78,8192
s_nop 0
buffer_load_dword v6,s[36:39],s68 offen offset:0 sc1 lds
s_lshr_b32 s73,s47,0
v_fma_f32 v36,-v64,v20,v36
v_fma_f32 v37,-v64,v21,v37
v_fma_f32 v38,-v64,v22,v38
v_fma_f32 v39,-v64,v23,v39
v_mul_f32 v44,v36,v36
v_mul_f32 v45,v37,v37
v_mul_f32 v46,v38,v38
v_mul_f32 v47,v39,v39
v_fma_f32 v44,v66,v44,v65
v_fma_f32 v45,v66,v45,v65
v_fma_f32 v46,v66,v46,v65
v_fma_f32 v47,v66,v47,v65
v_mul_f32 v44,v36,v44
v_mul_f32 v45,v37,v45
v_mul_f32 v46,v38,v46
v_mul_f32 v47,v39,v47
v_exp_f32 v44,v44
v_exp_f32 v45,v45
v_exp_f32 v46,v46
v_exp_f32 v47,v47
v_fma_f32 v44,v44,v67,v67
v_fma_f32 v45,v45,v67,v67
v_fma_f32 v46,v46,v67,v67
v_fma_f32 v47,v47,v67,v67
v_rcp_f32 v44,v44
v_rcp_f32 v45,v45
v_rcp_f32 v46,v46
v_rcp_f32 v47,v47
v_mul_f32 v36,v36,v44
v_mul_f32 v37,v37,v45
v_mul_f32 v38,v38,v46
v_mul_f32 v39,v39,v47
v_cvt_pk_bf16_f32 v52,v36,v37)"
R"(
v_cvt_pk_bf16_f32 v53,v38,v39
s_add_u32 m0,s78,8448
s_nop 0
buffer_load_dword v6,s[36:39],s68 offen offset:0 sc1 lds
v_fma_f32 v40,-v64,v24,v40
v_fma_f32 v41,-v64,v25,v41
v_fma_f32 v42,-v64,v26,v42
v_fma_f32 v43,-v64,v27,v43
v_mul_f32 v48,v40,v40
v_mul_f32 v49,v41,v41
v_mul_f32 v50,v42,v42
v_mul_f32 v51,v43,v43
v_fma_f32 v48,v66,v48,v65
v_fma_f32 v49,v66,v49,v65
v_fma_f32 v50,v66,v50,v65
v_fma_f32 v51,v66,v51,v65
v_mul_f32 v48,v40,v48
v_mul_f32 v49,v41,v49
v_mul_f32 v50,v42,v50
v_mul_f32 v51,v43,v51
v_exp_f32 v48,v48
v_exp_f32 v49,v49
v_exp_f32 v50,v50
v_exp_f32 v51,v51
v_fma_f32 v48,v48,v67,v67
v_fma_f32 v49,v49,v67,v67
v_fma_f32 v50,v50,v67,v67
v_fma_f32 v51,v51,v67,v67
v_rcp_f32 v48,v48
v_rcp_f32 v49,v49
v_rcp_f32 v50,v50
v_rcp_f32 v51,v51
v_mul_f32 v40,v40,v48
v_mul_f32 v41,v41,v49
v_mul_f32 v42,v42,v50
v_mul_f32 v43,v43,v51
v_cvt_pk_bf16_f32 v54,v40,v41
v_cvt_pk_bf16_f32 v55,v42,v43
buffer_store_dwordx4 v[52:55],v3,s[36:39],s73 offen offset:0 sc1
s_cmp_ge_u32 s48,s59
s_cbranch_scc1 Lg1_done0_%=
s_add_u32 m0,s78,16384
s_nop 0
buffer_load_dword v6,s[36:39],s68 offen offset:0 sc1 lds
s_waitcnt vmcnt(3)
s_barrier
ds_read_b32 v16,v7 offset:8192
s_lshr_b32 s70,s46,5
buffer_store_dword v5,v4,s[36:39],s70 offen offset:0 sc1
s_mov_b32 s46,s59
s_mov_b32 s69,18432
s_waitcnt lgkmcnt(0)
v_cmp_eq_u32 vcc,s60,v16
s_nop 0
s_cmp_eq_u32 vcc_lo,-1
s_cbranch_scc1 Lg1_go0_%=
s_add_u32 m0,s78,16640
s_nop 0
buffer_load_dword v6,s[36:39],s68 offen offset:0 sc1 lds
s_waitcnt vmcnt(4)
s_barrier
ds_read_b32 v16,v7 offset:8448
s_mov_b32 s69,18688
s_waitcnt lgkmcnt(0)
v_cmp_eq_u32 vcc,s60,v16
s_nop 0
s_cmp_eq_u32 vcc_lo,-1
s_cbranch_scc1 Lg1_go0_%=
s_add_u32 m0,s78,8192
s_nop 0
buffer_load_dword v6,s[36:39],s68 offen offset:0 sc1 lds
s_waitcnt vmcnt(3)
s_barrier
ds_read_b32 v16,v7 offset:16384
s_lshr_b32 s70,s47,5
buffer_store_dword v5,v4,s[36:39],s70 offen offset:0 sc1
s_mov_b32 s47,s59
s_mov_b32 s69,10240
s_waitcnt lgkmcnt(0)
v_cmp_eq_u32 vcc,s60,v16
s_nop 0
s_cmp_eq_u32 vcc_lo,-1
s_cbranch_scc1 Lg1_go0_%=
s_add_u32 m0,s78,8448
s_nop 0
buffer_load_dword v6,s[36:39],s68 offen offset:0 sc1 lds
s_waitcnt vmcnt(3)
s_barrier
ds_read_b32 v16,v7 offset:16640
s_mov_b32 s69,10496
s_waitcnt lgkmcnt(0)
v_cmp_eq_u32 vcc,s60,v16
s_nop 0
s_cmp_eq_u32 vcc_lo,-1
s_cbranch_scc1 Lg1_go0_%=
Lg1_poll0_%=:
s_add_u32 m0,s78,16384
s_nop 0
buffer_load_dword v6,s[36:39],s68 offen offset:0 sc1 lds
s_waitcnt vmcnt(2)
s_barrier
ds_read_b32 v16,v7 offset:8192
s_mov_b32 s69,18432
s_waitcnt lgkmcnt(0)
v_cmp_eq_u32 vcc,s60,v16
s_nop 0
s_cmp_eq_u32 vcc_lo,-1
s_cbranch_scc1 Lg1_go0_%=
s_sleep 4
s_add_u32 m0,s78,16640
s_nop 0
buffer_load_dword v6,s[36:39],s68 offen offset:0 sc1 lds
s_waitcnt vmcnt(2)
s_barrier
ds_read_b32 v16,v7 offset:8448
s_mov_b32 s69,18688
s_waitcnt lgkmcnt(0)
v_cmp_eq_u32 vcc,s60,v16
s_nop 0
s_cmp_eq_u32 vcc_lo,-1
s_cbranch_scc1 Lg1_go0_%=
s_sleep 4
s_add_u32 m0,s78,8192
s_nop 0
buffer_load_dword v6,s[36:39],s68 offen offset:0 sc1 lds
s_waitcnt vmcnt(2)
s_barrier
ds_read_b32 v16,v7 offset:16384
s_mov_b32 s69,10240
s_waitcnt lgkmcnt(0)
v_cmp_eq_u32 vcc,s60,v16
s_nop 0
s_cmp_eq_u32 vcc_lo,-1
s_cbranch_scc1 Lg1_go0_%=
s_sleep 4
s_add_u32 m0,s78,8448
s_nop 0
buffer_load_dword v6,s[36:39],s68 offen offset:0 sc1 lds
s_waitcnt vmcnt(2)
s_barrier
ds_read_b32 v16,v7 offset:16640
s_mov_b32 s69,10496
s_waitcnt lgkmcnt(0)
v_cmp_eq_u32 vcc,s60,v16
s_nop 0
s_cmp_eq_u32 vcc_lo,-1
s_cbranch_scc1 Lg1_go0_%=
s_sleep 4
s_add_u32 s79,s79,1
s_cmp_lt_u32 s79,20000
s_cbranch_scc1 Lg1_poll0_%=
Lg1_go0_%=:
s_xor_b32 s62,s63,65536
s_mov_b32 m0,s62
s_nop 0
buffer_load_dwordx4 v0,s[36:39],s48 offen offset:0 sc1 lds
buffer_load_dwordx4 v0,s[36:39],s48 offen offset:1024 sc1 lds
buffer_load_dwordx4 v0,s[36:39],s48 offen offset:2048 sc1 lds
buffer_load_dwordx4 v0,s[36:39],s48 offen offset:3072 sc1 lds
s_add_u32 m0,s62,16384
s_nop 0
buffer_load_dwordx4 v1,s[36:39],s48 offen offset:0 sc1 lds
buffer_load_dwordx4 v1,s[36:39],s48 offen offset:1024 sc1 lds
buffer_load_dwordx4 v1,s[36:39],s48 offen offset:2048 sc1 lds
buffer_load_dwordx4 v1,s[36:39],s48 offen offset:3072 sc1 lds
s_lshr_b32 s72,s48,6
s_lshr_b32 s71,s62,2
s_cmp_lt_u32 s48,4194304
s_cselect_b32 s72,s59,s72
s_add_u32 m0,s71,s88
s_nop 0
buffer_load_dword v10,s[36:39],s72 offen offset:0 sc1 lds
s_cmp_eq_u32 vcc_hi,-1
s_cselect_b32 s66,s49,s59
s_xor_b32 s62,s64,65536
s_cmp_ge_u32 s66,s59
s_cselect_b32 s68,s49,s50
s_lshr_b32 s69,s68,5
s_cmp_lt_u32 s68,4194304
s_cselect_b32 s68,0xffffcfff,-1
s_and_b32 s68,s68,s69
s_add_u32 m0,s78,0
s_nop 0
buffer_load_dword v6,s[36:39],s68 offen offset:0 sc1 lds
s_mov_b32 m0,s62
s_nop 0
buffer_load_dwordx4 v0,s[36:39],s66 offen offset:0 sc1 lds
buffer_load_dwordx4 v0,s[36:39],s66 offen offset:1024 sc1 lds
buffer_load_dwordx4 v0,s[36:39],s66 offen offset:2048 sc1 lds
buffer_load_dwordx4 v0,s[36:39],s66 offen offset:3072 sc1 lds
s_waitcnt vmcnt(10)
s_barrier
s_lshl_b32 s70,s54,12
v_add_u32 v44,s70,v2
v_xor_b32 v45,4096,v44
ds_read_b128 v[128:131],v44 offset:0
ds_read_b128 v[132:135],v44 offset:1024
ds_read_b128 v[136:139],v44 offset:2048
ds_read_b128 v[140:143],v44 offset:3072
ds_read_b128 v[144:147],v45 offset:0)"
R"(
ds_read_b128 v[148:151],v45 offset:1024
ds_read_b128 v[152:155],v45 offset:2048
s_lshr_b32 s70,s46,5
buffer_store_dword v5,v4,s[36:39],s70 offen offset:0 sc1
s_mov_b32 s46,s59
s_lshr_b32 s70,s47,5
buffer_store_dword v5,v4,s[36:39],s70 offen offset:0 sc1
s_mov_b32 s47,s59
s_lshr_b32 s70,s59,5
buffer_store_dword v5,v4,s[36:39],s70 offen offset:0 sc1
s_waitcnt lgkmcnt(3)
s_setpc_b64 s[90:91]
Lg1_gs0_%=:
s_cmp_eq_u32 s54,1
s_cselect_b32 s68,s49,s48
s_lshr_b32 s69,s68,5
s_cmp_lt_u32 s68,4194304
s_cselect_b32 s68,0xffffcfff,-1
s_and_b32 s68,s68,s69
s_add_u32 m0,s78,8192
s_nop 0
buffer_load_dword v6,s[36:39],s68 offen offset:0 sc1 lds
s_add_u32 m0,s62,16384
s_nop 0
buffer_load_dwordx4 v1,s[36:39],s66 offen offset:0 sc1 lds
buffer_load_dwordx4 v1,s[36:39],s66 offen offset:1024 sc1 lds
buffer_load_dwordx4 v1,s[36:39],s66 offen offset:2048 sc1 lds
buffer_load_dwordx4 v1,s[36:39],s66 offen offset:3072 sc1 lds
s_lshr_b32 s72,s66,6
s_lshr_b32 s71,s62,2
s_cmp_lt_u32 s66,4194304
s_cselect_b32 s72,s59,s72
s_add_u32 m0,s71,s88
s_nop 0
buffer_load_dword v10,s[36:39],s72 offen offset:0 sc1 lds
s_waitcnt vmcnt(5)
s_barrier
ds_read_b32 v16,v7 offset:8192
s_lshl_b32 s70,s54,12
v_add_u32 v44,s70,v2
v_xor_b32 v45,4096,v44
ds_read_b128 v[128:131],v44 offset:0
ds_read_b128 v[132:135],v44 offset:1024
ds_read_b128 v[136:139],v44 offset:2048
ds_read_b128 v[140:143],v44 offset:3072
ds_read_b128 v[144:147],v45 offset:0
ds_read_b128 v[148:151],v45 offset:1024
ds_read_b128 v[152:155],v45 offset:2048
s_lshr_b32 s70,s46,5
buffer_store_dword v5,v4,s[36:39],s70 offen offset:0 sc1
s_mov_b32 s46,s59
s_waitcnt lgkmcnt(7)
v_cmp_eq_u32 vcc,s60,v16
s_nop 0
s_cmp_eq_u32 vcc_hi,-1
s_cselect_b32 s66,s49,s59
s_xor_b32 s62,s64,65536
s_cmp_ge_u32 s66,s59
s_cselect_b32 s68,s49,s50
s_lshr_b32 s69,s68,5
s_cmp_lt_u32 s68,4194304
s_cselect_b32 s68,0xffffcfff,-1
s_and_b32 s68,s68,s69
s_add_u32 m0,s78,0
s_nop 0
buffer_load_dword v6,s[36:39],s68 offen offset:0 sc1 lds
s_mov_b32 m0,s62
s_nop 0
buffer_load_dwordx4 v0,s[36:39],s66 offen offset:0 sc1 lds
buffer_load_dwordx4 v0,s[36:39],s66 offen offset:1024 sc1 lds
buffer_load_dwordx4 v0,s[36:39],s66 offen offset:2048 sc1 lds
buffer_load_dwordx4 v0,s[36:39],s66 offen offset:3072 sc1 lds
s_lshr_b32 s70,s59,5
buffer_store_dword v5,v4,s[36:39],s70 offen offset:0 sc1
s_lshr_b32 s70,s59,5
buffer_store_dword v5,v4,s[36:39],s70 offen offset:0 sc1
s_waitcnt lgkmcnt(3)
s_setpc_b64 s[90:91]
Lg1_done0_%=:
s_waitcnt vmcnt(0)
s_barrier
s_lshr_b32 s70,s46,5
buffer_store_dword v5,v4,s[36:39],s70 offen offset:0 sc1
s_lshr_b32 s70,s47,5
buffer_store_dword v5,v4,s[36:39],s70 offen offset:0 sc1
s_branch Lg1_done_%=
Lg1_slow1_%=:
s_cmp_lt_u32 s67,s59
s_cbranch_scc1 Lg1_gs1_%=
Lg1_slowb1_%=:
s_cmp_eq_u32 s54,1
s_cselect_b32 s68,s50,s49
s_lshr_b32 s69,s68,5
s_cmp_lt_u32 s68,4194304
s_cselect_b32 s68,0xffffcfff,-1
s_and_b32 s68,s68,s69
s_add_u32 m0,s78,8192
s_nop 0
buffer_load_dword v6,s[36:39],s68 offen offset:0 sc1 lds
s_lshr_b32 s73,s48,0
v_fma_f32 v28,-v60,v20,v28
v_fma_f32 v29,-v60,v21,v29
v_fma_f32 v30,-v60,v22,v30
v_fma_f32 v31,-v60,v23,v31
v_mul_f32 v44,v28,v28
v_mul_f32 v45,v29,v29
v_mul_f32 v46,v30,v30
v_mul_f32 v47,v31,v31
v_fma_f32 v44,v62,v44,v61
v_fma_f32 v45,v62,v45,v61
v_fma_f32 v46,v62,v46,v61
v_fma_f32 v47,v62,v47,v61
v_mul_f32 v44,v28,v44
v_mul_f32 v45,v29,v45
v_mul_f32 v46,v30,v46
v_mul_f32 v47,v31,v47
v_exp_f32 v44,v44
v_exp_f32 v45,v45
v_exp_f32 v46,v46
v_exp_f32 v47,v47
v_fma_f32 v44,v44,v63,v63
v_fma_f32 v45,v45,v63,v63
v_fma_f32 v46,v46,v63,v63
v_fma_f32 v47,v47,v63,v63
v_rcp_f32 v44,v44
v_rcp_f32 v45,v45
v_rcp_f32 v46,v46
v_rcp_f32 v47,v47
v_mul_f32 v28,v28,v44
v_mul_f32 v29,v29,v45
v_mul_f32 v30,v30,v46
v_mul_f32 v31,v31,v47
v_cvt_pk_bf16_f32 v52,v28,v29
v_cvt_pk_bf16_f32 v53,v30,v31
s_add_u32 m0,s78,8448
s_nop 0
buffer_load_dword v6,s[36:39],s68 offen offset:0 sc1 lds
v_fma_f32 v32,-v60,v24,v32
v_fma_f32 v33,-v60,v25,v33
v_fma_f32 v34,-v60,v26,v34
v_fma_f32 v35,-v60,v27,v35
v_mul_f32 v48,v32,v32
v_mul_f32 v49,v33,v33
v_mul_f32 v50,v34,v34
v_mul_f32 v51,v35,v35
v_fma_f32 v48,v62,v48,v61
v_fma_f32 v49,v62,v49,v61
v_fma_f32 v50,v62,v50,v61
v_fma_f32 v51,v62,v51,v61
v_mul_f32 v48,v32,v48
v_mul_f32 v49,v33,v49
v_mul_f32 v50,v34,v50
v_mul_f32 v51,v35,v51
v_exp_f32 v48,v48
v_exp_f32 v49,v49
v_exp_f32 v50,v50
v_exp_f32 v51,v51
v_fma_f32 v48,v48,v63,v63
v_fma_f32 v49,v49,v63,v63
v_fma_f32 v50,v50,v63,v63
v_fma_f32 v51,v51,v63,v63
v_rcp_f32 v48,v48
v_rcp_f32 v49,v49
v_rcp_f32 v50,v50
v_rcp_f32 v51,v51
v_mul_f32 v32,v32,v48
v_mul_f32 v33,v33,v49
v_mul_f32 v34,v34,v50
v_mul_f32 v35,v35,v51
v_cvt_pk_bf16_f32 v54,v32,v33
v_cvt_pk_bf16_f32 v55,v34,v35
buffer_store_dwordx4 v[52:55],v3,s[36:39],s73 offen offset:0 sc1
s_cmp_ge_u32 s49,s59
s_cbranch_scc1 Lg1_done1_%=
s_add_u32 m0,s78,16384
s_nop 0
buffer_load_dword v6,s[36:39],s68 offen offset:0 sc1 lds
s_waitcnt vmcnt(3)
s_barrier
ds_read_b32 v16,v7 offset:8192
s_lshr_b32 s70,s47,5
buffer_store_dword v5,v4,s[36:39],s70 offen offset:0 sc1
s_mov_b32 s47,s59
s_mov_b32 s69,18432
s_waitcnt lgkmcnt(0)
v_cmp_eq_u32 vcc,s60,v16
s_nop 0
s_cmp_eq_u32 vcc_lo,-1
s_cbranch_scc1 Lg1_go1_%=
s_add_u32 m0,s78,16640
s_nop 0
buffer_load_dword v6,s[36:39],s68 offen offset:0 sc1 lds
s_waitcnt vmcnt(4)
s_barrier
ds_read_b32 v16,v7 offset:8448
s_mov_b32 s69,18688
s_waitcnt lgkmcnt(0)
v_cmp_eq_u32 vcc,s60,v16
s_nop 0
s_cmp_eq_u32 vcc_lo,-1)"
R"(
s_cbranch_scc1 Lg1_go1_%=
s_add_u32 m0,s78,8192
s_nop 0
buffer_load_dword v6,s[36:39],s68 offen offset:0 sc1 lds
s_waitcnt vmcnt(3)
s_barrier
ds_read_b32 v16,v7 offset:16384
s_lshr_b32 s70,s48,5
buffer_store_dword v5,v4,s[36:39],s70 offen offset:0 sc1
s_mov_b32 s48,s59
s_mov_b32 s69,10240
s_waitcnt lgkmcnt(0)
v_cmp_eq_u32 vcc,s60,v16
s_nop 0
s_cmp_eq_u32 vcc_lo,-1
s_cbranch_scc1 Lg1_go1_%=
s_add_u32 m0,s78,8448
s_nop 0
buffer_load_dword v6,s[36:39],s68 offen offset:0 sc1 lds
s_waitcnt vmcnt(3)
s_barrier
ds_read_b32 v16,v7 offset:16640
s_mov_b32 s69,10496
s_waitcnt lgkmcnt(0)
v_cmp_eq_u32 vcc,s60,v16
s_nop 0
s_cmp_eq_u32 vcc_lo,-1
s_cbranch_scc1 Lg1_go1_%=
Lg1_poll1_%=:
s_add_u32 m0,s78,16384
s_nop 0
buffer_load_dword v6,s[36:39],s68 offen offset:0 sc1 lds
s_waitcnt vmcnt(2)
s_barrier
ds_read_b32 v16,v7 offset:8192
s_mov_b32 s69,18432
s_waitcnt lgkmcnt(0)
v_cmp_eq_u32 vcc,s60,v16
s_nop 0
s_cmp_eq_u32 vcc_lo,-1
s_cbranch_scc1 Lg1_go1_%=
s_sleep 4
s_add_u32 m0,s78,16640
s_nop 0
buffer_load_dword v6,s[36:39],s68 offen offset:0 sc1 lds
s_waitcnt vmcnt(2)
s_barrier
ds_read_b32 v16,v7 offset:8448
s_mov_b32 s69,18688
s_waitcnt lgkmcnt(0)
v_cmp_eq_u32 vcc,s60,v16
s_nop 0
s_cmp_eq_u32 vcc_lo,-1
s_cbranch_scc1 Lg1_go1_%=
s_sleep 4
s_add_u32 m0,s78,8192
s_nop 0
buffer_load_dword v6,s[36:39],s68 offen offset:0 sc1 lds
s_waitcnt vmcnt(2)
s_barrier
ds_read_b32 v16,v7 offset:16384
s_mov_b32 s69,10240
s_waitcnt lgkmcnt(0)
v_cmp_eq_u32 vcc,s60,v16
s_nop 0
s_cmp_eq_u32 vcc_lo,-1
s_cbranch_scc1 Lg1_go1_%=
s_sleep 4
s_add_u32 m0,s78,8448
s_nop 0
buffer_load_dword v6,s[36:39],s68 offen offset:0 sc1 lds
s_waitcnt vmcnt(2)
s_barrier
ds_read_b32 v16,v7 offset:16640
s_mov_b32 s69,10496
s_waitcnt lgkmcnt(0)
v_cmp_eq_u32 vcc,s60,v16
s_nop 0
s_cmp_eq_u32 vcc_lo,-1
s_cbranch_scc1 Lg1_go1_%=
s_sleep 4
s_add_u32 s79,s79,1
s_cmp_lt_u32 s79,20000
s_cbranch_scc1 Lg1_poll1_%=
Lg1_go1_%=:
s_xor_b32 s62,s64,65536
s_mov_b32 m0,s62
s_nop 0
buffer_load_dwordx4 v0,s[36:39],s49 offen offset:0 sc1 lds
buffer_load_dwordx4 v0,s[36:39],s49 offen offset:1024 sc1 lds
buffer_load_dwordx4 v0,s[36:39],s49 offen offset:2048 sc1 lds
buffer_load_dwordx4 v0,s[36:39],s49 offen offset:3072 sc1 lds
s_add_u32 m0,s62,16384
s_nop 0
buffer_load_dwordx4 v1,s[36:39],s49 offen offset:0 sc1 lds
buffer_load_dwordx4 v1,s[36:39],s49 offen offset:1024 sc1 lds
buffer_load_dwordx4 v1,s[36:39],s49 offen offset:2048 sc1 lds
buffer_load_dwordx4 v1,s[36:39],s49 offen offset:3072 sc1 lds
s_lshr_b32 s72,s49,6
s_lshr_b32 s71,s62,2
s_cmp_lt_u32 s49,4194304
s_cselect_b32 s72,s59,s72
s_add_u32 m0,s71,s88
s_nop 0
buffer_load_dword v10,s[36:39],s72 offen offset:0 sc1 lds
s_cmp_eq_u32 vcc_hi,-1
s_cselect_b32 s67,s50,s59
s_xor_b32 s62,s63,65536
s_cmp_ge_u32 s67,s59
s_cselect_b32 s68,s50,s51
s_lshr_b32 s69,s68,5
s_cmp_lt_u32 s68,4194304
s_cselect_b32 s68,0xffffcfff,-1
s_and_b32 s68,s68,s69
s_add_u32 m0,s78,256
s_nop 0
buffer_load_dword v6,s[36:39],s68 offen offset:0 sc1 lds
s_mov_b32 m0,s62
s_nop 0
buffer_load_dwordx4 v0,s[36:39],s67 offen offset:0 sc1 lds
buffer_load_dwordx4 v0,s[36:39],s67 offen offset:1024 sc1 lds
buffer_load_dwordx4 v0,s[36:39],s67 offen offset:2048 sc1 lds
buffer_load_dwordx4 v0,s[36:39],s67 offen offset:3072 sc1 lds
s_waitcnt vmcnt(10)
s_barrier
s_lshl_b32 s70,s54,12
v_add_u32 v44,s70,v2
v_xor_b32 v45,4096,v44
ds_read_b128 v[128:131],v44 offset:32768
ds_read_b128 v[132:135],v44 offset:33792
ds_read_b128 v[136:139],v44 offset:34816
ds_read_b128 v[140:143],v44 offset:35840
ds_read_b128 v[144:147],v45 offset:32768
ds_read_b128 v[148:151],v45 offset:33792
ds_read_b128 v[152:155],v45 offset:34816
s_lshr_b32 s70,s47,5
buffer_store_dword v5,v4,s[36:39],s70 offen offset:0 sc1
s_mov_b32 s47,s59
s_lshr_b32 s70,s48,5
buffer_store_dword v5,v4,s[36:39],s70 offen offset:0 sc1
s_mov_b32 s48,s59
s_lshr_b32 s70,s59,5
buffer_store_dword v5,v4,s[36:39],s70 offen offset:0 sc1
s_waitcnt lgkmcnt(3)
s_setpc_b64 s[92:93]
Lg1_gs1_%=:
s_cmp_eq_u32 s54,1
s_cselect_b32 s68,s50,s49
s_lshr_b32 s69,s68,5
s_cmp_lt_u32 s68,4194304
s_cselect_b32 s68,0xffffcfff,-1
s_and_b32 s68,s68,s69
s_add_u32 m0,s78,8192
s_nop 0
buffer_load_dword v6,s[36:39],s68 offen offset:0 sc1 lds
s_add_u32 m0,s62,16384
s_nop 0
buffer_load_dwordx4 v1,s[36:39],s67 offen offset:0 sc1 lds
buffer_load_dwordx4 v1,s[36:39],s67 offen offset:1024 sc1 lds
buffer_load_dwordx4 v1,s[36:39],s67 offen offset:2048 sc1 lds
buffer_load_dwordx4 v1,s[36:39],s67 offen offset:3072 sc1 lds
s_lshr_b32 s72,s67,6
s_lshr_b32 s71,s62,2
s_cmp_lt_u32 s67,4194304
s_cselect_b32 s72,s59,s72
s_add_u32 m0,s71,s88
s_nop 0
buffer_load_dword v10,s[36:39],s72 offen offset:0 sc1 lds
s_waitcnt vmcnt(5)
s_barrier
ds_read_b32 v16,v7 offset:8192
s_lshl_b32 s70,s54,12
v_add_u32 v44,s70,v2
v_xor_b32 v45,4096,v44
ds_read_b128 v[128:131],v44 offset:32768
ds_read_b128 v[132:135],v44 offset:33792
ds_read_b128 v[136:139],v44 offset:34816
ds_read_b128 v[140:143],v44 offset:35840
ds_read_b128 v[144:147],v45 offset:32768
ds_read_b128 v[148:151],v45 offset:33792
ds_read_b128 v[152:155],v45 offset:34816
s_lshr_b32 s70,s47,5
buffer_store_dword v5,v4,s[36:39],s70 offen offset:0 sc1
s_mov_b32 s47,s59
s_waitcnt lgkmcnt(7)
v_cmp_eq_u32 vcc,s60,v16
s_nop 0
s_cmp_eq_u32 vcc_hi,-1
s_cselect_b32 s67,s50,s59
s_xor_b32 s62,s63,65536
s_cmp_ge_u32 s67,s59
s_cselect_b32 s68,s50,s51
s_lshr_b32 s69,s68,5
s_cmp_lt_u32 s68,4194304
s_cselect_b32 s68,0xffffcfff,-1
s_and_b32 s68,s68,s69
s_add_u32 m0,s78,256
s_nop 0
buffer_load_dword v6,s[36:39],s68 offen offset:0 sc1 lds
s_mov_b32 m0,s62)"
R"(
s_nop 0
buffer_load_dwordx4 v0,s[36:39],s67 offen offset:0 sc1 lds
buffer_load_dwordx4 v0,s[36:39],s67 offen offset:1024 sc1 lds
buffer_load_dwordx4 v0,s[36:39],s67 offen offset:2048 sc1 lds
buffer_load_dwordx4 v0,s[36:39],s67 offen offset:3072 sc1 lds
s_lshr_b32 s70,s59,5
buffer_store_dword v5,v4,s[36:39],s70 offen offset:0 sc1
s_lshr_b32 s70,s59,5
buffer_store_dword v5,v4,s[36:39],s70 offen offset:0 sc1
s_waitcnt lgkmcnt(3)
s_setpc_b64 s[92:93]
Lg1_done1_%=:
s_waitcnt vmcnt(0)
s_barrier
s_lshr_b32 s70,s47,5
buffer_store_dword v5,v4,s[36:39],s70 offen offset:0 sc1
s_lshr_b32 s70,s48,5
buffer_store_dword v5,v4,s[36:39],s70 offen offset:0 sc1
s_branch Lg1_done_%=
Lg1_done_%=:
s_waitcnt vmcnt(0) lgkmcnt(0)
s_barrier
s_branch Lg2_end_%=
Lg2_begin_%=:
Lcode_g2_b:
s_mov_b64 s[36:37],%1
s_mov_b32 s38,122727552
s_mov_b32 s39,0x00020000
s_mov_b32 s54,%2
s_mov_b32 s55,%3
s_mov_b32 s56,%4
s_mov_b32 s58,%5
s_mov_b32 s83,%6
s_mov_b32 s82,%7
s_mov_b32 s84,%8
v_mov_b32 v0,%9
v_mov_b32 v1,%10
v_mov_b32 v2,%11
v_mov_b32 v3,%12
v_mov_b32 v4,%13
v_mov_b32 v6,%14
v_mov_b32 v7,%15
v_mov_b32 v8,%16
v_mov_b32 v9,%17
v_mov_b32 v10,%18
v_mov_b32 v11,%19
v_mov_b32 v12,%20
v_mov_b32 v13,%21
v_mov_b32 v14,%22
v_mov_b32 v16,%23
v_mov_b32 v17,%24
v_mov_b32 v18,%25
v_mov_b32 v19,%26
v_mov_b32 v20,%27
s_mov_b64 s[40:41],%28
s_mov_b32 s42,2097152
s_mov_b32 s43,0x00020000
s_lshl_b32 s88,s54,11
s_add_u32 m0,s88,0
s_add_u32 s61,s82,0
s_and_b32 s61,s61,2097151
buffer_load_dwordx4 v16,s[40:43],s61 offen offset:0  lds
s_add_u32 m0,s88,1024
s_nop 0
buffer_load_dwordx4 v17,s[40:43],s61 offen offset:0  lds
s_add_u32 m0,s88,8064
s_nop 0
buffer_load_dwordx4 v16,s[40:43],s61 offen offset:128  lds
s_add_u32 m0,s88,9088
s_nop 0
buffer_load_dwordx4 v17,s[40:43],s61 offen offset:128  lds
s_add_u32 m0,s88,16384
s_add_u32 s61,s82,131072
s_and_b32 s61,s61,2097151
buffer_load_dwordx4 v16,s[40:43],s61 offen offset:0  lds
s_add_u32 m0,s88,17408
s_nop 0
buffer_load_dwordx4 v17,s[40:43],s61 offen offset:0  lds
s_add_u32 m0,s88,24448
s_nop 0
buffer_load_dwordx4 v16,s[40:43],s61 offen offset:128  lds
s_add_u32 m0,s88,25472
s_nop 0
buffer_load_dwordx4 v17,s[40:43],s61 offen offset:128  lds
s_add_u32 m0,s88,32768
s_add_u32 s61,s82,262144
s_and_b32 s61,s61,2097151
buffer_load_dwordx4 v16,s[40:43],s61 offen offset:0  lds
s_add_u32 m0,s88,33792
s_nop 0
buffer_load_dwordx4 v17,s[40:43],s61 offen offset:0  lds
s_add_u32 m0,s88,40832
s_nop 0
buffer_load_dwordx4 v16,s[40:43],s61 offen offset:128  lds
s_add_u32 m0,s88,41856
s_nop 0
buffer_load_dwordx4 v17,s[40:43],s61 offen offset:128  lds
s_add_u32 m0,s88,49152
s_add_u32 s61,s82,393216
s_and_b32 s61,s61,2097151
buffer_load_dwordx4 v16,s[40:43],s61 offen offset:0  lds
s_add_u32 m0,s88,50176
s_nop 0
buffer_load_dwordx4 v17,s[40:43],s61 offen offset:0  lds
s_add_u32 m0,s88,57216
s_nop 0
buffer_load_dwordx4 v16,s[40:43],s61 offen offset:128  lds
s_add_u32 m0,s88,58240
s_nop 0
buffer_load_dwordx4 v17,s[40:43],s61 offen offset:128  lds
s_add_u32 m0,s88,65536
s_add_u32 s61,s82,524288
s_and_b32 s61,s61,2097151
buffer_load_dwordx4 v16,s[40:43],s61 offen offset:0  lds
s_add_u32 m0,s88,66560
s_nop 0
buffer_load_dwordx4 v17,s[40:43],s61 offen offset:0  lds
s_add_u32 m0,s88,73600
s_nop 0
buffer_load_dwordx4 v16,s[40:43],s61 offen offset:128  lds
s_add_u32 m0,s88,74624
s_nop 0
buffer_load_dwordx4 v17,s[40:43],s61 offen offset:128  lds
s_add_u32 m0,s88,81920
s_add_u32 s61,s82,655360
s_and_b32 s61,s61,2097151
buffer_load_dwordx4 v16,s[40:43],s61 offen offset:0  lds
s_add_u32 m0,s88,82944
s_nop 0
buffer_load_dwordx4 v17,s[40:43],s61 offen offset:0  lds
s_add_u32 m0,s88,89984
s_nop 0
buffer_load_dwordx4 v16,s[40:43],s61 offen offset:128  lds
s_add_u32 m0,s88,91008
s_nop 0
buffer_load_dwordx4 v17,s[40:43],s61 offen offset:128  lds
s_add_u32 m0,s88,98304
s_add_u32 s61,s82,786432
s_and_b32 s61,s61,2097151
buffer_load_dwordx4 v16,s[40:43],s61 offen offset:0  lds
s_add_u32 m0,s88,99328
s_nop 0
buffer_load_dwordx4 v17,s[40:43],s61 offen offset:0  lds
s_add_u32 m0,s88,106368
s_nop 0
buffer_load_dwordx4 v16,s[40:43],s61 offen offset:128  lds
s_add_u32 m0,s88,107392
s_nop 0
buffer_load_dwordx4 v17,s[40:43],s61 offen offset:128  lds
v_add_u32 v24,49152,v18
v_add_u32 v25,49152,v19
v_add_u32 v26,98304,v18
v_add_u32 v27,98304,v19
s_waitcnt vmcnt(24)
s_barrier
s_add_u32 m0,s88,114688
s_add_u32 s61,s82,917504
s_and_b32 s61,s61,2097151
buffer_load_dwordx4 v16,s[40:43],s61 offen offset:0  lds
s_add_u32 m0,s88,115712
ds_read_b64_tr_b16 a[0:1],v18 offset:0
buffer_load_dwordx4 v17,s[40:43],s61 offen offset:0  lds
s_add_u32 m0,s88,122752
ds_read_b64_tr_b16 a[2:3],v18 offset:512
buffer_load_dwordx4 v16,s[40:43],s61 offen offset:128  lds
s_add_u32 m0,s88,123776
ds_read_b64_tr_b16 a[4:5],v19 offset:0
buffer_load_dwordx4 v17,s[40:43],s61 offen offset:128  lds
ds_read_b64_tr_b16 a[6:7],v19 offset:512
ds_read_b64_tr_b16 a[8:9],v18 offset:4096
ds_read_b64_tr_b16 a[10:11],v18 offset:4608
ds_read_b64_tr_b16 a[12:13],v19 offset:4096
ds_read_b64_tr_b16 a[14:15],v19 offset:4608
s_waitcnt vmcnt(24)
s_barrier
s_add_u32 m0,s88,131072
s_add_u32 s61,s82,1048576
s_and_b32 s61,s61,2097151
buffer_load_dwordx4 v16,s[40:43],s61 offen offset:0  lds
s_add_u32 m0,s88,132096
ds_read_b64_tr_b16 a[16:17],v18 offset:16384
buffer_load_dwordx4 v17,s[40:43],s61 offen offset:0  lds
s_add_u32 m0,s88,139136
ds_read_b64_tr_b16 a[18:19],v18 offset:16896
buffer_load_dwordx4 v16,s[40:43],s61 offen offset:128  lds
s_add_u32 m0,s88,140160
ds_read_b64_tr_b16 a[20:21],v19 offset:16384
buffer_load_dwordx4 v17,s[40:43],s61 offen offset:128  lds
ds_read_b64_tr_b16 a[22:23],v19 offset:16896
ds_read_b64_tr_b16 a[24:25],v18 offset:20480
ds_read_b64_tr_b16 a[26:27],v18 offset:20992
ds_read_b64_tr_b16 a[28:29],v19 offset:20480
ds_read_b64_tr_b16 a[30:31],v19 offset:20992
s_waitcnt lgkmcnt(8)
s_waitcnt vmcnt(24)
s_barrier
s_add_u32 m0,s88,0
s_add_u32 s61,s82,1179648
s_and_b32 s61,s61,2097151
buffer_load_dwordx4 v16,s[40:43],s61 offen offset:0  lds)"
R"(
s_add_u32 m0,s88,1024
ds_read_b64_tr_b16 a[32:33],v18 offset:32768
buffer_load_dwordx4 v17,s[40:43],s61 offen offset:0  lds
s_add_u32 m0,s88,8064
ds_read_b64_tr_b16 a[34:35],v18 offset:33280
buffer_load_dwordx4 v16,s[40:43],s61 offen offset:128  lds
s_add_u32 m0,s88,9088
ds_read_b64_tr_b16 a[36:37],v19 offset:32768
buffer_load_dwordx4 v17,s[40:43],s61 offen offset:128  lds
ds_read_b64_tr_b16 a[38:39],v19 offset:33280
ds_read_b64_tr_b16 a[40:41],v18 offset:36864
ds_read_b64_tr_b16 a[42:43],v18 offset:37376
ds_read_b64_tr_b16 a[44:45],v19 offset:36864
ds_read_b64_tr_b16 a[46:47],v19 offset:37376
s_waitcnt lgkmcnt(8)
s_waitcnt vmcnt(24)
s_barrier
s_add_u32 m0,s88,16384
s_add_u32 s61,s82,1310720
s_and_b32 s61,s61,2097151
buffer_load_dwordx4 v16,s[40:43],s61 offen offset:0  lds
s_add_u32 m0,s88,17408
ds_read_b64_tr_b16 a[48:49],v24 offset:0
buffer_load_dwordx4 v17,s[40:43],s61 offen offset:0  lds
s_add_u32 m0,s88,24448
ds_read_b64_tr_b16 a[50:51],v24 offset:512
buffer_load_dwordx4 v16,s[40:43],s61 offen offset:128  lds
s_add_u32 m0,s88,25472
ds_read_b64_tr_b16 a[52:53],v25 offset:0
buffer_load_dwordx4 v17,s[40:43],s61 offen offset:128  lds
ds_read_b64_tr_b16 a[54:55],v25 offset:512
ds_read_b64_tr_b16 a[56:57],v24 offset:4096
ds_read_b64_tr_b16 a[58:59],v24 offset:4608
ds_read_b64_tr_b16 a[60:61],v25 offset:4096
ds_read_b64_tr_b16 a[62:63],v25 offset:4608
s_waitcnt lgkmcnt(8)
s_waitcnt vmcnt(24)
s_barrier
s_add_u32 m0,s88,32768
s_add_u32 s61,s82,1441792
s_and_b32 s61,s61,2097151
buffer_load_dwordx4 v16,s[40:43],s61 offen offset:0  lds
s_add_u32 m0,s88,33792
ds_read_b64_tr_b16 a[64:65],v24 offset:16384
buffer_load_dwordx4 v17,s[40:43],s61 offen offset:0  lds
s_add_u32 m0,s88,40832
ds_read_b64_tr_b16 a[66:67],v24 offset:16896
buffer_load_dwordx4 v16,s[40:43],s61 offen offset:128  lds
s_add_u32 m0,s88,41856
ds_read_b64_tr_b16 a[68:69],v25 offset:16384
buffer_load_dwordx4 v17,s[40:43],s61 offen offset:128  lds
ds_read_b64_tr_b16 a[70:71],v25 offset:16896
ds_read_b64_tr_b16 a[72:73],v24 offset:20480
ds_read_b64_tr_b16 a[74:75],v24 offset:20992
ds_read_b64_tr_b16 a[76:77],v25 offset:20480
ds_read_b64_tr_b16 a[78:79],v25 offset:20992
s_waitcnt lgkmcnt(8)
s_waitcnt vmcnt(24)
s_barrier
s_add_u32 m0,s88,49152
s_add_u32 s61,s82,1572864
s_and_b32 s61,s61,2097151
buffer_load_dwordx4 v16,s[40:43],s61 offen offset:0  lds
s_add_u32 m0,s88,50176
ds_read_b64_tr_b16 a[80:81],v24 offset:32768
buffer_load_dwordx4 v17,s[40:43],s61 offen offset:0  lds
s_add_u32 m0,s88,57216
ds_read_b64_tr_b16 a[82:83],v24 offset:33280
buffer_load_dwordx4 v16,s[40:43],s61 offen offset:128  lds
s_add_u32 m0,s88,58240
ds_read_b64_tr_b16 a[84:85],v25 offset:32768
buffer_load_dwordx4 v17,s[40:43],s61 offen offset:128  lds
ds_read_b64_tr_b16 a[86:87],v25 offset:33280
ds_read_b64_tr_b16 a[88:89],v24 offset:36864
ds_read_b64_tr_b16 a[90:91],v24 offset:37376
ds_read_b64_tr_b16 a[92:93],v25 offset:36864
ds_read_b64_tr_b16 a[94:95],v25 offset:37376
s_waitcnt lgkmcnt(8)
s_waitcnt vmcnt(24)
s_barrier
s_add_u32 m0,s88,65536
s_add_u32 s61,s82,1703936
s_and_b32 s61,s61,2097151
buffer_load_dwordx4 v16,s[40:43],s61 offen offset:0  lds
s_add_u32 m0,s88,66560
ds_read_b64_tr_b16 a[96:97],v26 offset:0
buffer_load_dwordx4 v17,s[40:43],s61 offen offset:0  lds
s_add_u32 m0,s88,73600
ds_read_b64_tr_b16 a[98:99],v26 offset:512
buffer_load_dwordx4 v16,s[40:43],s61 offen offset:128  lds
s_add_u32 m0,s88,74624
ds_read_b64_tr_b16 a[100:101],v27 offset:0
buffer_load_dwordx4 v17,s[40:43],s61 offen offset:128  lds
ds_read_b64_tr_b16 a[102:103],v27 offset:512
ds_read_b64_tr_b16 a[104:105],v26 offset:4096
ds_read_b64_tr_b16 a[106:107],v26 offset:4608
ds_read_b64_tr_b16 a[108:109],v27 offset:4096
ds_read_b64_tr_b16 a[110:111],v27 offset:4608
s_waitcnt lgkmcnt(8)
s_waitcnt vmcnt(24)
s_barrier
s_add_u32 m0,s88,81920
s_add_u32 s61,s82,1835008
s_and_b32 s61,s61,2097151
buffer_load_dwordx4 v16,s[40:43],s61 offen offset:0  lds
s_add_u32 m0,s88,82944
ds_read_b64_tr_b16 a[112:113],v26 offset:16384
buffer_load_dwordx4 v17,s[40:43],s61 offen offset:0  lds
s_add_u32 m0,s88,89984
ds_read_b64_tr_b16 a[114:115],v26 offset:16896
buffer_load_dwordx4 v16,s[40:43],s61 offen offset:128  lds
s_add_u32 m0,s88,91008
ds_read_b64_tr_b16 a[116:117],v27 offset:16384
buffer_load_dwordx4 v17,s[40:43],s61 offen offset:128  lds
ds_read_b64_tr_b16 a[118:119],v27 offset:16896
ds_read_b64_tr_b16 a[120:121],v26 offset:20480
ds_read_b64_tr_b16 a[122:123],v26 offset:20992
ds_read_b64_tr_b16 a[124:125],v27 offset:20480
ds_read_b64_tr_b16 a[126:127],v27 offset:20992
s_waitcnt lgkmcnt(8)
s_waitcnt vmcnt(24)
s_barrier
s_add_u32 m0,s88,98304
s_add_u32 s61,s82,1966080
s_and_b32 s61,s61,2097151
buffer_load_dwordx4 v16,s[40:43],s61 offen offset:0  lds
s_add_u32 m0,s88,99328
ds_read_b64_tr_b16 a[128:129],v26 offset:32768
buffer_load_dwordx4 v17,s[40:43],s61 offen offset:0  lds
s_add_u32 m0,s88,106368
ds_read_b64_tr_b16 a[130:131],v26 offset:33280
buffer_load_dwordx4 v16,s[40:43],s61 offen offset:128  lds
s_add_u32 m0,s88,107392
ds_read_b64_tr_b16 a[132:133],v27 offset:32768
buffer_load_dwordx4 v17,s[40:43],s61 offen offset:128  lds
ds_read_b64_tr_b16 a[134:135],v27 offset:33280
ds_read_b64_tr_b16 a[136:137],v26 offset:36864
ds_read_b64_tr_b16 a[138:139],v26 offset:37376
ds_read_b64_tr_b16 a[140:141],v27 offset:36864
ds_read_b64_tr_b16 a[142:143],v27 offset:37376
s_waitcnt lgkmcnt(8)
s_waitcnt vmcnt(24)
s_barrier
ds_read_b64_tr_b16 a[144:145],v18 offset:0
ds_read_b64_tr_b16 a[146:147],v18 offset:512
ds_read_b64_tr_b16 a[148:149],v19 offset:0
ds_read_b64_tr_b16 a[150:151],v19 offset:512
ds_read_b64_tr_b16 a[152:153],v18 offset:4096
ds_read_b64_tr_b16 a[154:155],v18 offset:4608
ds_read_b64_tr_b16 a[156:157],v19 offset:4096
ds_read_b64_tr_b16 a[158:159],v19 offset:4608
s_waitcnt lgkmcnt(8)
s_waitcnt vmcnt(20)
s_barrier
ds_read_b64_tr_b16 a[160:161],v18 offset:16384
ds_read_b64_tr_b16 a[162:163],v18 offset:16896
ds_read_b64_tr_b16 a[164:165],v19 offset:16384
ds_read_b64_tr_b16 a[166:167],v19 offset:16896
ds_read_b64_tr_b16 a[168:169],v18 offset:20480
ds_read_b64_tr_b16 a[170:171],v18 offset:20992
ds_read_b64_tr_b16 a[172:173],v19 offset:20480
ds_read_b64_tr_b16 a[174:175],v19 offset:20992
s_waitcnt lgkmcnt(8)
s_waitcnt vmcnt(16)
s_barrier
ds_read_b64_tr_b16 a[176:177],v18 offset:32768
ds_read_b64_tr_b16 a[178:179],v18 offset:33280
ds_read_b64_tr_b16 a[180:181],v19 offset:32768
ds_read_b64_tr_b16 a[182:183],v19 offset:33280
ds_read_b64_tr_b16 a[184:185],v18 offset:36864
ds_read_b64_tr_b16 a[186:187],v18 offset:37376
ds_read_b64_tr_b16 a[188:189],v19 offset:36864
ds_read_b64_tr_b16 a[190:191],v19 offset:37376
s_getpc_b64 s[46:47]
Lg2_cp_%=:
s_add_u32 s46,s46,Lg2_init_%=-Lg2_cp_%=
s_addc_u32 s47,s47,0
s_lshl_b32 s68,s54,13
v_lshl_add_u32 v144,v2,3,s68
v_min_u32 v145,Lg2_done_%=-Lg2_init_%=,v144
global_load_dword v148,v145,s[46:47]
s_waitcnt lgkmcnt(8)
s_waitcnt vmcnt(13)
s_barrier
ds_read_b64_tr_b16 a[192:193],v24 offset:0
ds_read_b64_tr_b16 a[194:195],v24 offset:512
ds_read_b64_tr_b16 a[196:197],v25 offset:0
ds_read_b64_tr_b16 a[198:199],v25 offset:512
ds_read_b64_tr_b16 a[200:201],v24 offset:4096
ds_read_b64_tr_b16 a[202:203],v24 offset:4608
ds_read_b64_tr_b16 a[204:205],v25 offset:4096
ds_read_b64_tr_b16 a[206:207],v25 offset:4608
s_waitcnt lgkmcnt(8)
s_waitcnt vmcnt(9)
s_barrier
ds_read_b64_tr_b16 a[208:209],v24 offset:16384
ds_read_b64_tr_b16 a[210:211],v24 offset:16896
ds_read_b64_tr_b16 a[212:213],v25 offset:16384
ds_read_b64_tr_b16 a[214:215],v25 offset:16896
ds_read_b64_tr_b16 a[216:217],v24 offset:20480)"
R"(
ds_read_b64_tr_b16 a[218:219],v24 offset:20992
ds_read_b64_tr_b16 a[220:221],v25 offset:20480
ds_read_b64_tr_b16 a[222:223],v25 offset:20992
s_waitcnt lgkmcnt(8)
s_waitcnt vmcnt(5)
s_barrier
ds_read_b64_tr_b16 a[224:225],v24 offset:32768
ds_read_b64_tr_b16 a[226:227],v24 offset:33280
ds_read_b64_tr_b16 a[228:229],v25 offset:32768
ds_read_b64_tr_b16 a[230:231],v25 offset:33280
ds_read_b64_tr_b16 a[232:233],v24 offset:36864
ds_read_b64_tr_b16 a[234:235],v24 offset:37376
ds_read_b64_tr_b16 a[236:237],v25 offset:36864
ds_read_b64_tr_b16 a[238:239],v25 offset:37376
s_waitcnt lgkmcnt(8)
s_waitcnt vmcnt(1)
s_barrier
ds_read_b64_tr_b16 a[240:241],v26 offset:0
ds_read_b64_tr_b16 a[242:243],v26 offset:512
ds_read_b64_tr_b16 a[244:245],v27 offset:0
ds_read_b64_tr_b16 a[246:247],v27 offset:512
ds_read_b64_tr_b16 a[248:249],v26 offset:4096
ds_read_b64_tr_b16 a[250:251],v26 offset:4608
ds_read_b64_tr_b16 a[252:253],v27 offset:4096
ds_read_b64_tr_b16 a[254:255],v27 offset:4608
s_waitcnt lgkmcnt(8)
s_waitcnt lgkmcnt(0)
s_waitcnt lgkmcnt(0)
s_barrier
s_mov_b32 s60,s84
Lg2_init_%=:
v_mov_b32 v5,s60
s_mov_b32 s59,536870912
s_add_u32 s57,s56,131072
s_mov_b32 s79,0
s_mov_b32 s75,0x3a800000
s_mov_b32 s76,0xc0135761
s_mov_b32 s77,0x3d372713
s_mov_b32 s44,s59
s_mov_b32 s45,s59
s_mov_b32 s46,s59
s_mov_b32 s47,s59
s_mov_b32 s48,s83
s_add_u32 s49,s48,s55
s_cmp_eq_u32 s49,s56
s_cselect_b32 s49,s57,s49
s_cmp_eq_u32 s49,s58
s_cselect_b32 s49,s59,s49
s_add_u32 s50,s49,s55
s_cmp_eq_u32 s50,s56
s_cselect_b32 s50,s57,s50
s_cmp_eq_u32 s50,s58
s_cselect_b32 s50,s59,s50
s_add_u32 s51,s50,s55
s_cmp_eq_u32 s51,s56
s_cselect_b32 s51,s57,s51
s_cmp_eq_u32 s51,s58
s_cselect_b32 s51,s59,s51
s_add_u32 s52,s51,s55
s_cmp_eq_u32 s52,s56
s_cselect_b32 s52,s57,s52
s_cmp_eq_u32 s52,s58
s_cselect_b32 s52,s59,s52
s_lshl_b32 s63,s54,12
s_add_u32 s63,s63,65536
s_add_u32 s64,s63,32768
s_lshl_b32 s88,s54,10
s_add_u32 s88,s88,131072
s_lshl_b32 s78,s54,11
s_add_u32 s78,s78,132608
s_getpc_b64 s[90:91]
Lg2_pc_%=:
s_mov_b32 s68,Lg2_d1w0_%=-Lg2_pc_%=
s_cmp_eq_u32 s54,1
s_cselect_b32 s68,Lg2_d1w1_%=-Lg2_pc_%=,s68
s_cmp_eq_u32 s54,2
s_cselect_b32 s68,Lg2_d1w2_%=-Lg2_pc_%=,s68
s_cmp_eq_u32 s54,3
s_cselect_b32 s68,Lg2_d1w3_%=-Lg2_pc_%=,s68
s_add_u32 s92,s90,s68
s_addc_u32 s93,s91,0
s_mov_b32 s68,Lg2_d0w0_%=-Lg2_pc_%=
s_cmp_eq_u32 s54,1
s_cselect_b32 s68,Lg2_d0w1_%=-Lg2_pc_%=,s68
s_cmp_eq_u32 s54,2
s_cselect_b32 s68,Lg2_d0w2_%=-Lg2_pc_%=,s68
s_cmp_eq_u32 s54,3
s_cselect_b32 s68,Lg2_d0w3_%=-Lg2_pc_%=,s68
s_add_u32 s90,s90,s68
s_addc_u32 s91,s91,0
s_mov_b32 s66,s59
s_mov_b32 s65,0
s_mov_b32 s62,s63
s_branch Lg2_slow0_%=
Lg2_d0w0_%=:
v_mfma_f32_16x16x32_bf16 v[20:23],a[4:7],v[128:131],0
s_add_u32 m0,s62,16384
ds_read_b128 v[156:159],v2 offset:7168
v_lshlrev_b32 v32,16,v52
v_mfma_f32_16x16x32_bf16 v[20:23],a[12:15],v[132:135],v[20:23]
buffer_load_dwordx4 v1,s[36:39],s66 offen offset:0 sc1 lds
v_mfma_f32_16x16x32_bf16 v[16:19],a[8:11],v[132:135],0
s_lshr_b32 s73,s47,0
ds_read_b128 v[160:163],v2 offset:8192
v_and_b32 v33,0xffff0000,v52
v_mfma_f32_16x16x32_bf16 v[16:19],a[0:3],v[128:131],v[16:19]
s_xor_b32 s87,s64,65536
v_lshlrev_b32 v34,16,v53
v_and_b32 v35,0xffff0000,v53
v_mfma_f32_16x16x32_bf16 v[16:19],a[16:19],v[136:139],v[16:19]
s_cmp_ge_u32 s66,s59
ds_read_b128 v[164:167],v2 offset:9216
v_lshlrev_b32 v36,16,v54
v_mfma_f32_16x16x32_bf16 v[16:19],a[24:27],v[140:143],v[16:19]
buffer_load_dwordx4 v1,s[36:39],s66 offen offset:1024 sc1 lds
v_and_b32 v37,0xffff0000,v54
v_lshlrev_b32 v38,16,v55
v_and_b32 v39,0xffff0000,v55
v_fma_f32 v32,v32,v59,v58
v_mfma_f32_16x16x32_bf16 v[20:23],a[28:31],v[140:143],v[20:23]
s_cselect_b32 s86,s49,s50
ds_read_b128 v[168:171],v2 offset:10240
v_fma_f32 v33,v33,v59,v58
v_mfma_f32_16x16x32_bf16 v[20:23],a[20:23],v[136:139],v[20:23]
s_waitcnt lgkmcnt(3)
v_fma_f32 v34,v34,v59,v58
v_fma_f32 v35,v35,v59,v58
v_mfma_f32_16x16x32_bf16 v[20:23],a[36:39],v[144:147],v[20:23]
s_cselect_b32 s87,s87,s63
ds_read_b128 v[172:175],v2 offset:11264
v_fma_f32 v36,v36,v59,v58
v_mfma_f32_16x16x32_bf16 v[20:23],a[44:47],v[148:151],v[20:23]
buffer_load_dwordx4 v1,s[36:39],s66 offen offset:2048 sc1 lds
v_fma_f32 v37,v37,v59,v58
v_fma_f32 v38,v38,v59,v58
v_fma_f32 v39,v39,v59,v58
v_mfma_f32_16x16x32_bf16 v[16:19],a[40:43],v[148:151],v[16:19]
s_lshr_b32 s72,s66,6
ds_read_b128 v[176:179],v2 offset:12288
v_add_f32 v24,v24,v32
v_mfma_f32_16x16x32_bf16 v[16:19],a[32:35],v[144:147],v[16:19]
s_lshr_b32 s71,s62,2
v_add_f32 v25,v25,v33
v_add_f32 v26,v26,v34
v_mfma_f32_16x16x32_bf16 v[16:19],a[48:51],v[152:155],v[16:19]
s_mov_b32 s62,s87
ds_read_b128 v[180:183],v2 offset:13312
v_add_f32 v27,v27,v35
v_mfma_f32_16x16x32_bf16 v[16:19],a[56:59],v[156:159],v[16:19]
buffer_load_dwordx4 v1,s[36:39],s66 offen offset:3072 sc1 lds
v_add_f32 v28,v28,v36
v_add_f32 v29,v29,v37
v_add_f32 v30,v30,v38
v_mfma_f32_16x16x32_bf16 v[20:23],a[60:63],v[156:159],v[20:23]
s_add_u32 m0,s71,s88
ds_read_b128 v[184:187],v2 offset:14336
v_add_f32 v31,v31,v39
v_mfma_f32_16x16x32_bf16 v[20:23],a[52:55],v[152:155],v[20:23]
s_waitcnt lgkmcnt(3)
v_cvt_pk_bf16_f32 v40,v24,v25
v_cvt_pk_bf16_f32 v41,v26,v27
v_mfma_f32_16x16x32_bf16 v[20:23],a[68:71],v[160:163],v[20:23]
s_waitcnt vmcnt(10)
ds_read_b128 v[188:191],v2 offset:15360
v_cvt_pk_bf16_f32 v42,v28,v29
v_mfma_f32_16x16x32_bf16 v[20:23],a[76:79],v[164:167],v[20:23]
s_barrier
v_mfma_f32_16x16x32_bf16 v[16:19],a[72:75],v[164:167],v[16:19]
s_lshr_b32 s68,s50,5
ds_read_b128 v[128:131],v2 offset:16384
v_cvt_pk_bf16_f32 v43,v30,v31
v_mfma_f32_16x16x32_bf16 v[16:19],a[64:67],v[160:163],v[16:19]
buffer_load_dwordx4 v10,s[36:39],s66 offen offset:0 sc1 lds
v_mfma_f32_16x16x32_bf16 v[16:19],a[80:83],v[168:171],v[16:19]
buffer_load_dword v11,s[36:39],s72 offen offset:1024 sc1 lds
v_mfma_f32_16x16x32_bf16 v[16:19],a[88:91],v[172:175],v[16:19]
s_lshr_b32 s70,s51,5
ds_read_b128 v[132:135],v2 offset:17408
v_add_f32 v32,v24,v25
v_mul_f32 v33,v24,v24
v_fma_f32 v33,v25,v25,v33
v_add_f32 v32,v32,v26
v_mfma_f32_16x16x32_bf16 v[20:23],a[92:95],v[172:175],v[20:23]
ds_read_b128 v[136:139],v2 offset:18432
v_fma_f32 v33,v26,v26,v33
v_mfma_f32_16x16x32_bf16 v[20:23],a[84:87],v[168:171],v[20:23]
s_waitcnt lgkmcnt(3)
v_add_f32 v32,v32,v27
v_fma_f32 v33,v27,v27,v33
v_mfma_f32_16x16x32_bf16 v[20:23],a[100:103],v[176:179],v[20:23]
buffer_store_dwordx4 v[40:43],v3,s[36:39],s73 offen offset:0 sc1
ds_read_b128 v[140:143],v2 offset:19456
v_add_f32 v32,v32,v28
v_fma_f32 v33,v28,v28,v33
v_mfma_f32_16x16x32_bf16 v[20:23],a[108:111],v[180:183],v[20:23]
v_add_f32 v32,v32,v29
v_fma_f32 v33,v29,v29,v33
v_mfma_f32_16x16x32_bf16 v[16:19],a[104:107],v[180:183],v[16:19]
ds_read_b128 v[144:147],v2 offset:20480
v_add_f32 v32,v32,v30)"
R"(
v_mfma_f32_16x16x32_bf16 v[16:19],a[96:99],v[176:179],v[16:19]
v_add_f32 v32,v32,v31
v_fma_f32 v33,v30,v30,v33
v_mfma_f32_16x16x32_bf16 v[16:19],a[112:115],v[184:187],v[16:19]
ds_read_b128 v[148:151],v2 offset:21504
v_fma_f32 v33,v31,v31,v33
v_mfma_f32_16x16x32_bf16 v[16:19],a[120:123],v[188:191],v[16:19]
s_nop 0
v_permlane16_swap_b32 v32,v33
v_mfma_f32_16x16x32_bf16 v[20:23],a[124:127],v[188:191],v[20:23]
v_add_f32 v32,v32,v33
v_mov_b32 v33,v32
v_mfma_f32_16x16x32_bf16 v[20:23],a[116:119],v[184:187],v[20:23]
s_waitcnt lgkmcnt(2)
ds_read_b128 v[152:155],v2 offset:22528
v_permlane32_swap_b32 v32,v33
v_add_f32 v32,v32,v33
v_mfma_f32_16x16x32_bf16 v[20:23],a[132:135],v[128:131],v[20:23]
ds_write_b32 v13,v32 offset:0
v_mfma_f32_16x16x32_bf16 v[20:23],a[140:143],v[132:135],v[20:23]
ds_read_b128 v[156:159],v2 offset:23552
v_mfma_f32_16x16x32_bf16 v[16:19],a[136:139],v[132:135],v[16:19]
ds_read_b128 v[160:163],v2 offset:24576
v_mfma_f32_16x16x32_bf16 v[16:19],a[128:131],v[128:131],v[16:19]
v_mfma_f32_16x16x32_bf16 v[16:19],a[144:147],v[136:139],v[16:19]
s_waitcnt vmcnt(11) lgkmcnt(2)
ds_read_b128 v[164:167],v2 offset:25600
v_mfma_f32_16x16x32_bf16 v[16:19],a[152:155],v[140:143],v[16:19]
s_barrier
v_mfma_f32_16x16x32_bf16 v[20:23],a[156:159],v[140:143],v[20:23]
ds_read_b128 v[168:171],v2 offset:26624
ds_read_b32 v15,v7 offset:0
v_mfma_f32_16x16x32_bf16 v[20:23],a[148:151],v[136:139],v[20:23]
s_waitcnt lgkmcnt(4)
v_mfma_f32_16x16x32_bf16 v[20:23],a[164:167],v[144:147],v[20:23]
ds_read_b128 v[172:175],v2 offset:27648
v_mfma_f32_16x16x32_bf16 v[20:23],a[172:175],v[148:151],v[20:23]
v_mfma_f32_16x16x32_bf16 v[16:19],a[168:171],v[148:151],v[16:19]
s_waitcnt lgkmcnt(1)
ds_read_b128 v[176:179],v2 offset:28672
v_mfma_f32_16x16x32_bf16 v[16:19],a[160:163],v[144:147],v[16:19]
v_mfma_f32_16x16x32_bf16 v[16:19],a[176:179],v[152:155],v[16:19]
v_cmp_eq_u32 vcc,s60,v15
v_mfma_f32_16x16x32_bf16 v[16:19],a[184:187],v[156:159],v[16:19]
s_cmp_eq_u32 vcc_lo,-1
s_cselect_b32 s67,s86,s59
s_cmp_ge_u32 s67,s59
s_cselect_b32 s68,s68,s70
ds_read_b128 v[180:183],v2 offset:29696
s_add_u32 m0,s78,256
v_mfma_f32_16x16x32_bf16 v[20:23],a[188:191],v[156:159],v[20:23]
buffer_load_dword v6,s[36:39],s68 offen offset:0 sc1 lds
v_mfma_f32_16x16x32_bf16 v[20:23],a[180:183],v[152:155],v[20:23]
s_waitcnt lgkmcnt(2)
ds_read_b128 v[184:187],v2 offset:30720
s_xor_b32 s63,s63,65536
v_mfma_f32_16x16x32_bf16 v[20:23],a[196:199],v[160:163],v[20:23]
s_waitcnt vmcnt(8)
ds_read_b128 v[188:191],v2 offset:31744
s_mov_b32 m0,s62
v_mfma_f32_16x16x32_bf16 v[20:23],a[204:207],v[164:167],v[20:23]
s_barrier
v_mfma_f32_16x16x32_bf16 v[16:19],a[200:203],v[164:167],v[16:19]
buffer_load_dwordx4 v0,s[36:39],s67 offen offset:0 sc1 lds
v_mfma_f32_16x16x32_bf16 v[16:19],a[192:195],v[160:163],v[16:19]
s_add_u32 s53,s52,s55
ds_read_b128 v[128:131],v2 offset:32768
ds_read_b128 v[48:51],v8 offset:0
v_mfma_f32_16x16x32_bf16 v[16:19],a[208:211],v[168:171],v[16:19]
s_cmp_eq_u32 s53,s56
ds_read_b128 v[132:135],v2 offset:33792
ds_read_b64 v[56:57],v9 offset:1024
v_mfma_f32_16x16x32_bf16 v[16:19],a[216:219],v[172:175],v[16:19]
s_cselect_b32 s53,s57,s53
s_cmp_eq_u32 s53,s58
s_cselect_b32 s53,s59,s53
v_mfma_f32_16x16x32_bf16 v[20:23],a[220:223],v[172:175],v[20:23]
buffer_load_dwordx4 v0,s[36:39],s67 offen offset:1024 sc1 lds
v_mfma_f32_16x16x32_bf16 v[20:23],a[212:215],v[168:171],v[20:23]
s_waitcnt lgkmcnt(4)
ds_read_b128 v[136:139],v2 offset:34816
s_cmp_ge_u32 s66,s59
v_mfma_f32_16x16x32_bf16 v[20:23],a[228:231],v[176:179],v[20:23]
ds_read_b128 v[140:143],v2 offset:35840
v_mfma_f32_16x16x32_bf16 v[20:23],a[236:239],v[180:183],v[20:23]
v_mfma_f32_16x16x32_bf16 v[16:19],a[232:235],v[180:183],v[16:19]
s_waitcnt lgkmcnt(2)
buffer_load_dwordx4 v0,s[36:39],s67 offen offset:2048 sc1 lds
v_mfma_f32_16x16x32_bf16 v[16:19],a[224:227],v[176:179],v[16:19]
ds_read_b128 v[144:147],v2 offset:36864
v_mfma_f32_16x16x32_bf16 v[16:19],a[240:243],v[184:187],v[16:19]
ds_read_b128 v[148:151],v2 offset:37888
v_mfma_f32_16x16x32_bf16 v[16:19],a[248:251],v[188:191],v[16:19]
v_mfma_f32_16x16x32_bf16 v[20:23],a[252:255],v[188:191],v[20:23]
s_waitcnt lgkmcnt(2)
ds_read_b128 v[152:155],v2 offset:38912
buffer_load_dwordx4 v0,s[36:39],s67 offen offset:3072 sc1 lds
v_mfma_f32_16x16x32_bf16 v[20:23],a[244:247],v[184:187],v[20:23]
s_cbranch_scc1 Lg2_slow1_%=
Lg2_d1w0_%=:
v_mfma_f32_16x16x32_bf16 v[28:31],a[4:7],v[128:131],0
s_add_u32 m0,s62,16384
ds_read_b128 v[156:159],v2 offset:39936
v_lshlrev_b32 v32,16,v48
v_mfma_f32_16x16x32_bf16 v[28:31],a[12:15],v[132:135],v[28:31]
buffer_load_dwordx4 v1,s[36:39],s67 offen offset:0 sc1 lds
v_mfma_f32_16x16x32_bf16 v[24:27],a[8:11],v[132:135],0
s_lshr_b32 s73,s48,0
ds_read_b128 v[160:163],v2 offset:40960
v_and_b32 v33,0xffff0000,v48
v_mfma_f32_16x16x32_bf16 v[24:27],a[0:3],v[128:131],v[24:27]
s_xor_b32 s87,s63,65536
v_lshlrev_b32 v34,16,v49
v_and_b32 v35,0xffff0000,v49
v_mfma_f32_16x16x32_bf16 v[24:27],a[16:19],v[136:139],v[24:27]
s_cmp_ge_u32 s67,s59
ds_read_b128 v[164:167],v2 offset:41984
v_lshlrev_b32 v36,16,v50
v_mfma_f32_16x16x32_bf16 v[24:27],a[24:27],v[140:143],v[24:27]
buffer_load_dwordx4 v1,s[36:39],s67 offen offset:1024 sc1 lds
v_mfma_f32_16x16x32_bf16 v[28:31],a[28:31],v[140:143],v[28:31]
s_cselect_b32 s86,s50,s51
ds_read_b128 v[168:171],v2 offset:43008
v_and_b32 v37,0xffff0000,v50
v_mfma_f32_16x16x32_bf16 v[28:31],a[20:23],v[136:139],v[28:31]
s_waitcnt lgkmcnt(3)
v_lshlrev_b32 v38,16,v51
v_and_b32 v39,0xffff0000,v51
v_mfma_f32_16x16x32_bf16 v[28:31],a[36:39],v[144:147],v[28:31]
s_cselect_b32 s87,s87,s64
ds_read_b128 v[172:175],v2 offset:44032
v_fma_f32 v32,v32,v57,v56
v_mfma_f32_16x16x32_bf16 v[28:31],a[44:47],v[148:151],v[28:31]
buffer_load_dwordx4 v1,s[36:39],s67 offen offset:2048 sc1 lds
v_mfma_f32_16x16x32_bf16 v[24:27],a[40:43],v[148:151],v[24:27]
s_lshr_b32 s72,s67,6
ds_read_b128 v[176:179],v2 offset:45056
v_fma_f32 v33,v33,v57,v56
v_mfma_f32_16x16x32_bf16 v[24:27],a[32:35],v[144:147],v[24:27]
s_lshr_b32 s71,s62,2
v_fma_f32 v34,v34,v57,v56
v_fma_f32 v35,v35,v57,v56
v_mfma_f32_16x16x32_bf16 v[24:27],a[48:51],v[152:155],v[24:27]
s_mov_b32 s62,s87
ds_read_b128 v[180:183],v2 offset:46080
v_fma_f32 v36,v36,v57,v56
v_mfma_f32_16x16x32_bf16 v[24:27],a[56:59],v[156:159],v[24:27]
buffer_load_dwordx4 v1,s[36:39],s67 offen offset:3072 sc1 lds
v_mfma_f32_16x16x32_bf16 v[28:31],a[60:63],v[156:159],v[28:31]
s_add_u32 m0,s71,s88
ds_read_b128 v[184:187],v2 offset:47104
v_fma_f32 v37,v37,v57,v56
v_mfma_f32_16x16x32_bf16 v[28:31],a[52:55],v[152:155],v[28:31]
s_waitcnt lgkmcnt(3)
v_fma_f32 v38,v38,v57,v56
v_fma_f32 v39,v39,v57,v56
v_mfma_f32_16x16x32_bf16 v[28:31],a[68:71],v[160:163],v[28:31]
s_waitcnt vmcnt(10)
ds_read_b128 v[188:191],v2 offset:48128
v_add_f32 v16,v16,v32
v_mfma_f32_16x16x32_bf16 v[28:31],a[76:79],v[164:167],v[28:31]
s_barrier
v_mfma_f32_16x16x32_bf16 v[24:27],a[72:75],v[164:167],v[24:27]
s_lshr_b32 s68,s51,5
ds_read_b128 v[128:131],v2 offset:49152
v_add_f32 v17,v17,v33
v_mfma_f32_16x16x32_bf16 v[24:27],a[64:67],v[160:163],v[24:27]
buffer_load_dwordx4 v10,s[36:39],s67 offen offset:0 sc1 lds
v_mfma_f32_16x16x32_bf16 v[24:27],a[80:83],v[168:171],v[24:27]
buffer_load_dword v11,s[36:39],s72 offen offset:1024 sc1 lds
v_mfma_f32_16x16x32_bf16 v[24:27],a[88:91],v[172:175],v[24:27]
s_lshr_b32 s70,s52,5
ds_read_b128 v[132:135],v2 offset:50176
v_add_f32 v18,v18,v34
v_mfma_f32_16x16x32_bf16 v[28:31],a[92:95],v[172:175],v[28:31]
s_mov_b64 s[44:45],s[46:47]
ds_read_b128 v[136:139],v2 offset:51200
v_add_f32 v19,v19,v35
v_mfma_f32_16x16x32_bf16 v[28:31],a[84:87],v[168:171],v[28:31]
s_waitcnt lgkmcnt(3)
v_add_f32 v20,v20,v36
v_add_f32 v21,v21,v37
v_add_f32 v22,v22,v38
v_add_f32 v23,v23,v39
v_cvt_pk_bf16_f32 v40,v16,v17
v_cvt_pk_bf16_f32 v41,v18,v19
v_cvt_pk_bf16_f32 v42,v20,v21
v_cvt_pk_bf16_f32 v43,v22,v23
v_mfma_f32_16x16x32_bf16 v[28:31],a[100:103],v[176:179],v[28:31]
buffer_store_dwordx4 v[40:43],v3,s[36:39],s73 offen offset:0 sc1
ds_read_b128 v[140:143],v2 offset:52224
v_add_f32 v32,v16,v17
v_mul_f32 v33,v16,v16
v_mfma_f32_16x16x32_bf16 v[28:31],a[108:111],v[180:183],v[28:31]
s_mov_b64 s[46:47],s[48:49]
v_fma_f32 v33,v17,v17,v33
v_add_f32 v32,v32,v18
v_mfma_f32_16x16x32_bf16 v[24:27],a[104:107],v[180:183],v[24:27]
s_mov_b64 s[48:49],s[50:51]
ds_read_b128 v[144:147],v2 offset:53248)"
R"(
v_fma_f32 v33,v18,v18,v33
v_mfma_f32_16x16x32_bf16 v[24:27],a[96:99],v[176:179],v[24:27]
s_mov_b64 s[50:51],s[52:53]
v_add_f32 v32,v32,v19
v_fma_f32 v33,v19,v19,v33
v_mfma_f32_16x16x32_bf16 v[24:27],a[112:115],v[184:187],v[24:27]
ds_read_b128 v[148:151],v2 offset:54272
v_add_f32 v32,v32,v20
v_mfma_f32_16x16x32_bf16 v[24:27],a[120:123],v[188:191],v[24:27]
v_fma_f32 v33,v20,v20,v33
v_add_f32 v32,v32,v21
v_fma_f32 v33,v21,v21,v33
v_add_f32 v32,v32,v22
v_fma_f32 v33,v22,v22,v33
v_add_f32 v32,v32,v23
v_fma_f32 v33,v23,v23,v33
v_mfma_f32_16x16x32_bf16 v[28:31],a[124:127],v[188:191],v[28:31]
s_nop 0
v_permlane16_swap_b32 v32,v33
v_add_f32 v32,v32,v33
v_mov_b32 v33,v32
v_mfma_f32_16x16x32_bf16 v[28:31],a[116:119],v[184:187],v[28:31]
s_waitcnt lgkmcnt(2)
ds_read_b128 v[152:155],v2 offset:55296
v_permlane32_swap_b32 v32,v33
v_add_f32 v32,v32,v33
v_mfma_f32_16x16x32_bf16 v[28:31],a[132:135],v[128:131],v[28:31]
ds_write_b32 v13,v32 offset:2048
v_mfma_f32_16x16x32_bf16 v[28:31],a[140:143],v[132:135],v[28:31]
ds_read_b128 v[156:159],v2 offset:56320
v_mfma_f32_16x16x32_bf16 v[24:27],a[136:139],v[132:135],v[24:27]
ds_read_b128 v[160:163],v2 offset:57344
v_mfma_f32_16x16x32_bf16 v[24:27],a[128:131],v[128:131],v[24:27]
v_mfma_f32_16x16x32_bf16 v[24:27],a[144:147],v[136:139],v[24:27]
s_waitcnt vmcnt(11) lgkmcnt(2)
ds_read_b128 v[164:167],v2 offset:58368
v_mfma_f32_16x16x32_bf16 v[24:27],a[152:155],v[140:143],v[24:27]
s_barrier
v_mfma_f32_16x16x32_bf16 v[28:31],a[156:159],v[140:143],v[28:31]
ds_read_b128 v[168:171],v2 offset:59392
ds_read_b32 v15,v7 offset:256
v_mfma_f32_16x16x32_bf16 v[28:31],a[148:151],v[136:139],v[28:31]
s_waitcnt lgkmcnt(4)
v_mfma_f32_16x16x32_bf16 v[28:31],a[164:167],v[144:147],v[28:31]
ds_read_b128 v[172:175],v2 offset:60416
v_mfma_f32_16x16x32_bf16 v[28:31],a[172:175],v[148:151],v[28:31]
v_mfma_f32_16x16x32_bf16 v[24:27],a[168:171],v[148:151],v[24:27]
s_waitcnt lgkmcnt(1)
ds_read_b128 v[176:179],v2 offset:61440
v_mfma_f32_16x16x32_bf16 v[24:27],a[160:163],v[144:147],v[24:27]
v_mfma_f32_16x16x32_bf16 v[24:27],a[176:179],v[152:155],v[24:27]
v_cmp_eq_u32 vcc,s60,v15
v_mfma_f32_16x16x32_bf16 v[24:27],a[184:187],v[156:159],v[24:27]
s_cmp_eq_u32 vcc_lo,-1
s_cselect_b32 s66,s86,s59
s_cmp_ge_u32 s66,s59
s_cselect_b32 s68,s68,s70
ds_read_b128 v[180:183],v2 offset:62464
s_add_u32 m0,s78,0
v_mfma_f32_16x16x32_bf16 v[28:31],a[188:191],v[156:159],v[28:31]
buffer_load_dword v6,s[36:39],s68 offen offset:0 sc1 lds
v_mfma_f32_16x16x32_bf16 v[28:31],a[180:183],v[152:155],v[28:31]
s_waitcnt lgkmcnt(2)
ds_read_b128 v[184:187],v2 offset:63488
s_xor_b32 s64,s64,65536
v_mfma_f32_16x16x32_bf16 v[28:31],a[196:199],v[160:163],v[28:31]
s_waitcnt vmcnt(8)
ds_read_b128 v[188:191],v2 offset:64512
v_xor_b32 v2,65536,v2
v_mfma_f32_16x16x32_bf16 v[28:31],a[204:207],v[164:167],v[28:31]
s_barrier
s_mov_b32 m0,s62
v_mfma_f32_16x16x32_bf16 v[24:27],a[200:203],v[164:167],v[24:27]
buffer_load_dwordx4 v0,s[36:39],s66 offen offset:0 sc1 lds
v_mfma_f32_16x16x32_bf16 v[24:27],a[192:195],v[160:163],v[24:27]
s_add_u32 s74,s53,s55
ds_read_b128 v[128:131],v2 offset:0
ds_read_b128 v[52:55],v8 offset:8192
v_mfma_f32_16x16x32_bf16 v[24:27],a[208:211],v[168:171],v[24:27]
s_cmp_eq_u32 s74,s56
ds_read_b128 v[132:135],v2 offset:1024
ds_read_b64 v[58:59],v9 offset:9216
v_mfma_f32_16x16x32_bf16 v[24:27],a[216:219],v[172:175],v[24:27]
s_cselect_b32 s74,s57,s74
v_xor_b32 v8,16384,v8
v_xor_b32 v9,16384,v9
v_mfma_f32_16x16x32_bf16 v[28:31],a[220:223],v[172:175],v[28:31]
buffer_load_dwordx4 v0,s[36:39],s66 offen offset:1024 sc1 lds
v_mfma_f32_16x16x32_bf16 v[28:31],a[212:215],v[168:171],v[28:31]
s_waitcnt lgkmcnt(4)
ds_read_b128 v[136:139],v2 offset:2048
s_cmp_eq_u32 s74,s58
v_mfma_f32_16x16x32_bf16 v[28:31],a[228:231],v[176:179],v[28:31]
s_cselect_b32 s74,s59,s74
ds_read_b128 v[140:143],v2 offset:3072
s_mov_b32 s52,s74
v_mfma_f32_16x16x32_bf16 v[28:31],a[236:239],v[180:183],v[28:31]
s_cmp_lt_u32 s67,s59
v_mfma_f32_16x16x32_bf16 v[24:27],a[232:235],v[180:183],v[24:27]
s_waitcnt lgkmcnt(2)
buffer_load_dwordx4 v0,s[36:39],s66 offen offset:2048 sc1 lds
v_mfma_f32_16x16x32_bf16 v[24:27],a[224:227],v[176:179],v[24:27]
ds_read_b128 v[144:147],v2 offset:4096
v_mfma_f32_16x16x32_bf16 v[24:27],a[240:243],v[184:187],v[24:27]
ds_read_b128 v[148:151],v2 offset:5120
v_mfma_f32_16x16x32_bf16 v[24:27],a[248:251],v[188:191],v[24:27]
v_mfma_f32_16x16x32_bf16 v[28:31],a[252:255],v[188:191],v[28:31]
s_waitcnt lgkmcnt(2)
ds_read_b128 v[152:155],v2 offset:6144
buffer_load_dwordx4 v0,s[36:39],s66 offen offset:3072 sc1 lds
v_mfma_f32_16x16x32_bf16 v[28:31],a[244:247],v[184:187],v[28:31]
s_cbranch_scc1 Lg2_d0w0_%=
s_branch Lg2_slow0_%=
Lg2_d0w1_%=:
v_mfma_f32_16x16x32_bf16 v[20:23],a[4:7],v[128:131],0
s_add_u32 m0,s62,16384
ds_read_b128 v[156:159],v2 offset:7168
v_lshlrev_b32 v32,16,v52
v_mfma_f32_16x16x32_bf16 v[20:23],a[12:15],v[132:135],v[20:23]
s_lshr_b32 s73,s47,0
v_and_b32 v33,0xffff0000,v52
v_lshlrev_b32 v34,16,v53
v_mfma_f32_16x16x32_bf16 v[16:19],a[8:11],v[132:135],0
buffer_load_dwordx4 v1,s[36:39],s66 offen offset:0 sc1 lds
v_mfma_f32_16x16x32_bf16 v[16:19],a[0:3],v[128:131],v[16:19]
s_xor_b32 s87,s64,65536
ds_read_b128 v[160:163],v2 offset:8192
v_and_b32 v35,0xffff0000,v53
v_mfma_f32_16x16x32_bf16 v[16:19],a[16:19],v[136:139],v[16:19]
s_cmp_ge_u32 s66,s59
ds_read_b128 v[164:167],v2 offset:9216
v_lshlrev_b32 v36,16,v54
v_mfma_f32_16x16x32_bf16 v[16:19],a[24:27],v[140:143],v[16:19]
s_cselect_b32 s86,s49,s50
v_and_b32 v37,0xffff0000,v54
v_lshlrev_b32 v38,16,v55
v_and_b32 v39,0xffff0000,v55
v_fma_f32 v32,v32,v59,v58
v_mfma_f32_16x16x32_bf16 v[20:23],a[28:31],v[140:143],v[20:23]
buffer_load_dwordx4 v1,s[36:39],s66 offen offset:1024 sc1 lds
v_mfma_f32_16x16x32_bf16 v[20:23],a[20:23],v[136:139],v[20:23]
s_waitcnt lgkmcnt(2)
ds_read_b128 v[168:171],v2 offset:10240
v_fma_f32 v33,v33,v59,v58
v_mfma_f32_16x16x32_bf16 v[20:23],a[36:39],v[144:147],v[20:23]
s_cselect_b32 s87,s87,s63
ds_read_b128 v[172:175],v2 offset:11264
v_fma_f32 v34,v34,v59,v58
v_mfma_f32_16x16x32_bf16 v[20:23],a[44:47],v[148:151],v[20:23]
s_lshr_b32 s72,s66,6
v_fma_f32 v35,v35,v59,v58
v_fma_f32 v36,v36,v59,v58
v_fma_f32 v37,v37,v59,v58
v_fma_f32 v38,v38,v59,v58
v_mfma_f32_16x16x32_bf16 v[16:19],a[40:43],v[148:151],v[16:19]
buffer_load_dwordx4 v1,s[36:39],s66 offen offset:2048 sc1 lds
v_mfma_f32_16x16x32_bf16 v[16:19],a[32:35],v[144:147],v[16:19]
s_lshr_b32 s71,s62,2
ds_read_b128 v[176:179],v2 offset:12288
v_fma_f32 v39,v39,v59,v58
v_mfma_f32_16x16x32_bf16 v[16:19],a[48:51],v[152:155],v[16:19]
s_mov_b32 s62,s87
ds_read_b128 v[180:183],v2 offset:13312
v_add_f32 v24,v24,v32
v_mfma_f32_16x16x32_bf16 v[16:19],a[56:59],v[156:159],v[16:19]
v_add_f32 v25,v25,v33
v_add_f32 v26,v26,v34
v_add_f32 v27,v27,v35
v_add_f32 v28,v28,v36
v_add_f32 v29,v29,v37
v_mfma_f32_16x16x32_bf16 v[20:23],a[60:63],v[156:159],v[20:23]
buffer_load_dwordx4 v1,s[36:39],s66 offen offset:3072 sc1 lds
v_mfma_f32_16x16x32_bf16 v[20:23],a[52:55],v[152:155],v[20:23]
s_waitcnt lgkmcnt(2)
ds_read_b128 v[184:187],v2 offset:14336
v_add_f32 v30,v30,v38
v_mfma_f32_16x16x32_bf16 v[20:23],a[68:71],v[160:163],v[20:23]
s_waitcnt vmcnt(9)
ds_read_b128 v[188:191],v2 offset:15360
v_add_f32 v31,v31,v39
v_mfma_f32_16x16x32_bf16 v[20:23],a[76:79],v[164:167],v[20:23]
s_barrier
v_mfma_f32_16x16x32_bf16 v[16:19],a[72:75],v[164:167],v[16:19]
s_add_u32 m0,s71,s88
ds_read_b128 v[128:131],v2 offset:16384
v_cvt_pk_bf16_f32 v40,v24,v25
v_mfma_f32_16x16x32_bf16 v[16:19],a[64:67],v[160:163],v[16:19]
v_cvt_pk_bf16_f32 v41,v26,v27
v_cvt_pk_bf16_f32 v42,v28,v29
v_mfma_f32_16x16x32_bf16 v[16:19],a[80:83],v[168:171],v[16:19]
buffer_load_dwordx4 v10,s[36:39],s66 offen offset:0 sc1 lds
v_mfma_f32_16x16x32_bf16 v[16:19],a[88:91],v[172:175],v[16:19]
ds_read_b128 v[132:135],v2 offset:17408
buffer_load_dword v11,s[36:39],s72 offen offset:1024 sc1 lds
v_cvt_pk_bf16_f32 v43,v30,v31
v_add_f32 v32,v24,v25
v_mul_f32 v33,v24,v24
v_fma_f32 v33,v25,v25,v33
v_mfma_f32_16x16x32_bf16 v[20:23],a[92:95],v[172:175],v[20:23]
s_mov_b32 m0,s62)"
R"(
ds_read_b128 v[136:139],v2 offset:18432
v_add_f32 v32,v32,v26
v_mfma_f32_16x16x32_bf16 v[20:23],a[84:87],v[168:171],v[20:23]
s_waitcnt lgkmcnt(3)
v_fma_f32 v33,v26,v26,v33
v_add_f32 v32,v32,v27
v_mfma_f32_16x16x32_bf16 v[20:23],a[100:103],v[176:179],v[20:23]
ds_read_b128 v[140:143],v2 offset:19456
v_fma_f32 v33,v27,v27,v33
v_mfma_f32_16x16x32_bf16 v[20:23],a[108:111],v[180:183],v[20:23]
buffer_store_dwordx4 v[40:43],v3,s[36:39],s73 offen offset:0 sc1
v_add_f32 v32,v32,v28
v_fma_f32 v33,v28,v28,v33
v_add_f32 v32,v32,v29
v_fma_f32 v33,v29,v29,v33
v_mfma_f32_16x16x32_bf16 v[16:19],a[104:107],v[180:183],v[16:19]
ds_read_b128 v[144:147],v2 offset:20480
v_add_f32 v32,v32,v30
v_mfma_f32_16x16x32_bf16 v[16:19],a[96:99],v[176:179],v[16:19]
v_add_f32 v32,v32,v31
v_fma_f32 v33,v30,v30,v33
v_mfma_f32_16x16x32_bf16 v[16:19],a[112:115],v[184:187],v[16:19]
ds_read_b128 v[148:151],v2 offset:21504
v_fma_f32 v33,v31,v31,v33
v_mfma_f32_16x16x32_bf16 v[16:19],a[120:123],v[188:191],v[16:19]
s_nop 0
v_permlane16_swap_b32 v32,v33
v_mfma_f32_16x16x32_bf16 v[20:23],a[124:127],v[188:191],v[20:23]
v_add_f32 v32,v32,v33
v_mov_b32 v33,v32
v_mfma_f32_16x16x32_bf16 v[20:23],a[116:119],v[184:187],v[20:23]
s_waitcnt lgkmcnt(2)
ds_read_b128 v[152:155],v2 offset:22528
v_permlane32_swap_b32 v32,v33
v_add_f32 v32,v32,v33
v_mfma_f32_16x16x32_bf16 v[20:23],a[132:135],v[128:131],v[20:23]
ds_write_b32 v13,v32 offset:0
v_mfma_f32_16x16x32_bf16 v[20:23],a[140:143],v[132:135],v[20:23]
ds_read_b128 v[156:159],v2 offset:23552
v_mfma_f32_16x16x32_bf16 v[16:19],a[136:139],v[132:135],v[16:19]
ds_read_b128 v[160:163],v2 offset:24576
v_mfma_f32_16x16x32_bf16 v[16:19],a[128:131],v[128:131],v[16:19]
v_mfma_f32_16x16x32_bf16 v[16:19],a[144:147],v[136:139],v[16:19]
s_waitcnt vmcnt(11) lgkmcnt(2)
ds_read_b128 v[164:167],v2 offset:25600
v_mfma_f32_16x16x32_bf16 v[16:19],a[152:155],v[140:143],v[16:19]
s_barrier
v_mfma_f32_16x16x32_bf16 v[20:23],a[156:159],v[140:143],v[20:23]
ds_read_b128 v[168:171],v2 offset:26624
ds_read_b32 v15,v7 offset:0
v_mfma_f32_16x16x32_bf16 v[20:23],a[148:151],v[136:139],v[20:23]
s_waitcnt lgkmcnt(4)
v_mfma_f32_16x16x32_bf16 v[20:23],a[164:167],v[144:147],v[20:23]
ds_read_b128 v[172:175],v2 offset:27648
v_mfma_f32_16x16x32_bf16 v[20:23],a[172:175],v[148:151],v[20:23]
v_mfma_f32_16x16x32_bf16 v[16:19],a[168:171],v[148:151],v[16:19]
s_waitcnt lgkmcnt(1)
ds_read_b128 v[176:179],v2 offset:28672
v_mfma_f32_16x16x32_bf16 v[16:19],a[160:163],v[144:147],v[16:19]
v_cmp_eq_u32 vcc,s60,v15
v_mfma_f32_16x16x32_bf16 v[16:19],a[176:179],v[152:155],v[16:19]
s_cmp_eq_u32 vcc_lo,-1
ds_read_b128 v[180:183],v2 offset:29696
s_cselect_b32 s67,s86,s59
v_mfma_f32_16x16x32_bf16 v[16:19],a[184:187],v[156:159],v[16:19]
buffer_load_dwordx4 v0,s[36:39],s67 offen offset:0 sc1 lds
v_mfma_f32_16x16x32_bf16 v[20:23],a[188:191],v[156:159],v[20:23]
s_xor_b32 s63,s63,65536
ds_read_b128 v[184:187],v2 offset:30720
s_add_u32 s53,s52,s55
v_mfma_f32_16x16x32_bf16 v[20:23],a[180:183],v[152:155],v[20:23]
s_waitcnt lgkmcnt(3)
s_cmp_eq_u32 s53,s56
s_cselect_b32 s53,s57,s53
v_mfma_f32_16x16x32_bf16 v[20:23],a[196:199],v[160:163],v[20:23]
s_waitcnt vmcnt(8)
ds_read_b128 v[188:191],v2 offset:31744
s_cmp_eq_u32 s53,s58
v_mfma_f32_16x16x32_bf16 v[20:23],a[204:207],v[164:167],v[20:23]
s_barrier
v_mfma_f32_16x16x32_bf16 v[16:19],a[200:203],v[164:167],v[16:19]
s_cselect_b32 s53,s59,s53
ds_read_b128 v[128:131],v2 offset:32768
ds_read_b128 v[48:51],v8 offset:0
v_mfma_f32_16x16x32_bf16 v[16:19],a[192:195],v[160:163],v[16:19]
buffer_load_dwordx4 v0,s[36:39],s67 offen offset:1024 sc1 lds
v_mfma_f32_16x16x32_bf16 v[16:19],a[208:211],v[168:171],v[16:19]
s_cmp_ge_u32 s66,s59
ds_read_b128 v[132:135],v2 offset:33792
ds_read_b64 v[56:57],v9 offset:1024
v_mfma_f32_16x16x32_bf16 v[16:19],a[216:219],v[172:175],v[16:19]
v_mfma_f32_16x16x32_bf16 v[20:23],a[220:223],v[172:175],v[20:23]
ds_read_b128 v[136:139],v2 offset:34816
v_mfma_f32_16x16x32_bf16 v[20:23],a[212:215],v[168:171],v[20:23]
s_waitcnt lgkmcnt(5)
buffer_load_dwordx4 v0,s[36:39],s67 offen offset:2048 sc1 lds
v_mfma_f32_16x16x32_bf16 v[20:23],a[228:231],v[176:179],v[20:23]
ds_read_b128 v[140:143],v2 offset:35840
v_mfma_f32_16x16x32_bf16 v[20:23],a[236:239],v[180:183],v[20:23]
v_mfma_f32_16x16x32_bf16 v[16:19],a[232:235],v[180:183],v[16:19]
s_waitcnt lgkmcnt(2)
ds_read_b128 v[144:147],v2 offset:36864
v_mfma_f32_16x16x32_bf16 v[16:19],a[224:227],v[176:179],v[16:19]
buffer_load_dwordx4 v0,s[36:39],s67 offen offset:3072 sc1 lds
v_mfma_f32_16x16x32_bf16 v[16:19],a[240:243],v[184:187],v[16:19]
ds_read_b128 v[148:151],v2 offset:37888
v_mfma_f32_16x16x32_bf16 v[16:19],a[248:251],v[188:191],v[16:19]
v_mfma_f32_16x16x32_bf16 v[20:23],a[252:255],v[188:191],v[20:23]
s_waitcnt lgkmcnt(2)
ds_read_b128 v[152:155],v2 offset:38912
v_mfma_f32_16x16x32_bf16 v[20:23],a[244:247],v[184:187],v[20:23]
s_cbranch_scc1 Lg2_slow1_%=
Lg2_d1w1_%=:
v_mfma_f32_16x16x32_bf16 v[28:31],a[4:7],v[128:131],0
s_add_u32 m0,s62,16384
ds_read_b128 v[156:159],v2 offset:39936
v_lshlrev_b32 v32,16,v48
v_mfma_f32_16x16x32_bf16 v[28:31],a[12:15],v[132:135],v[28:31]
s_lshr_b32 s73,s48,0
v_and_b32 v33,0xffff0000,v48
v_lshlrev_b32 v34,16,v49
v_mfma_f32_16x16x32_bf16 v[24:27],a[8:11],v[132:135],0
buffer_load_dwordx4 v1,s[36:39],s67 offen offset:0 sc1 lds
v_mfma_f32_16x16x32_bf16 v[24:27],a[0:3],v[128:131],v[24:27]
s_xor_b32 s87,s63,65536
ds_read_b128 v[160:163],v2 offset:40960
v_and_b32 v35,0xffff0000,v49
v_mfma_f32_16x16x32_bf16 v[24:27],a[16:19],v[136:139],v[24:27]
s_cmp_ge_u32 s67,s59
ds_read_b128 v[164:167],v2 offset:41984
v_lshlrev_b32 v36,16,v50
v_mfma_f32_16x16x32_bf16 v[24:27],a[24:27],v[140:143],v[24:27]
s_cselect_b32 s86,s50,s51
v_and_b32 v37,0xffff0000,v50
v_lshlrev_b32 v38,16,v51
v_and_b32 v39,0xffff0000,v51
v_fma_f32 v32,v32,v57,v56
v_mfma_f32_16x16x32_bf16 v[28:31],a[28:31],v[140:143],v[28:31]
buffer_load_dwordx4 v1,s[36:39],s67 offen offset:1024 sc1 lds
v_mfma_f32_16x16x32_bf16 v[28:31],a[20:23],v[136:139],v[28:31]
s_waitcnt lgkmcnt(2)
ds_read_b128 v[168:171],v2 offset:43008
v_fma_f32 v33,v33,v57,v56
v_mfma_f32_16x16x32_bf16 v[28:31],a[36:39],v[144:147],v[28:31]
s_cselect_b32 s87,s87,s64
ds_read_b128 v[172:175],v2 offset:44032
v_fma_f32 v34,v34,v57,v56
v_mfma_f32_16x16x32_bf16 v[28:31],a[44:47],v[148:151],v[28:31]
s_lshr_b32 s72,s67,6
v_fma_f32 v35,v35,v57,v56
v_fma_f32 v36,v36,v57,v56
v_fma_f32 v37,v37,v57,v56
v_fma_f32 v38,v38,v57,v56
v_mfma_f32_16x16x32_bf16 v[24:27],a[40:43],v[148:151],v[24:27]
buffer_load_dwordx4 v1,s[36:39],s67 offen offset:2048 sc1 lds
v_mfma_f32_16x16x32_bf16 v[24:27],a[32:35],v[144:147],v[24:27]
s_lshr_b32 s71,s62,2
ds_read_b128 v[176:179],v2 offset:45056
v_fma_f32 v39,v39,v57,v56
v_mfma_f32_16x16x32_bf16 v[24:27],a[48:51],v[152:155],v[24:27]
s_mov_b32 s62,s87
ds_read_b128 v[180:183],v2 offset:46080
v_add_f32 v16,v16,v32
v_mfma_f32_16x16x32_bf16 v[24:27],a[56:59],v[156:159],v[24:27]
s_mov_b64 s[44:45],s[46:47]
v_add_f32 v17,v17,v33
v_add_f32 v18,v18,v34
v_add_f32 v19,v19,v35
v_add_f32 v20,v20,v36
v_add_f32 v21,v21,v37
v_mfma_f32_16x16x32_bf16 v[28:31],a[60:63],v[156:159],v[28:31]
buffer_load_dwordx4 v1,s[36:39],s67 offen offset:3072 sc1 lds
v_mfma_f32_16x16x32_bf16 v[28:31],a[52:55],v[152:155],v[28:31]
s_waitcnt lgkmcnt(2)
ds_read_b128 v[184:187],v2 offset:47104
v_add_f32 v22,v22,v38
v_mfma_f32_16x16x32_bf16 v[28:31],a[68:71],v[160:163],v[28:31]
s_waitcnt vmcnt(9)
ds_read_b128 v[188:191],v2 offset:48128
v_add_f32 v23,v23,v39
v_mfma_f32_16x16x32_bf16 v[28:31],a[76:79],v[164:167],v[28:31]
s_barrier
v_mfma_f32_16x16x32_bf16 v[24:27],a[72:75],v[164:167],v[24:27]
s_add_u32 m0,s71,s88
ds_read_b128 v[128:131],v2 offset:49152
v_cvt_pk_bf16_f32 v40,v16,v17
v_mfma_f32_16x16x32_bf16 v[24:27],a[64:67],v[160:163],v[24:27]
s_mov_b64 s[46:47],s[48:49]
v_cvt_pk_bf16_f32 v41,v18,v19
v_cvt_pk_bf16_f32 v42,v20,v21
v_mfma_f32_16x16x32_bf16 v[24:27],a[80:83],v[168:171],v[24:27]
buffer_load_dwordx4 v10,s[36:39],s67 offen offset:0 sc1 lds
v_mfma_f32_16x16x32_bf16 v[24:27],a[88:91],v[172:175],v[24:27]
s_mov_b64 s[48:49],s[50:51]
ds_read_b128 v[132:135],v2 offset:50176
buffer_load_dword v11,s[36:39],s72 offen offset:1024 sc1 lds
v_cvt_pk_bf16_f32 v43,v22,v23
v_add_f32 v32,v16,v17
v_mul_f32 v33,v16,v16
v_fma_f32 v33,v17,v17,v33)"
R"(
v_mfma_f32_16x16x32_bf16 v[28:31],a[92:95],v[172:175],v[28:31]
s_mov_b32 m0,s62
ds_read_b128 v[136:139],v2 offset:51200
v_add_f32 v32,v32,v18
v_mfma_f32_16x16x32_bf16 v[28:31],a[84:87],v[168:171],v[28:31]
s_waitcnt lgkmcnt(3)
v_fma_f32 v33,v18,v18,v33
v_add_f32 v32,v32,v19
v_mfma_f32_16x16x32_bf16 v[28:31],a[100:103],v[176:179],v[28:31]
s_mov_b64 s[50:51],s[52:53]
ds_read_b128 v[140:143],v2 offset:52224
v_fma_f32 v33,v19,v19,v33
v_mfma_f32_16x16x32_bf16 v[28:31],a[108:111],v[180:183],v[28:31]
buffer_store_dwordx4 v[40:43],v3,s[36:39],s73 offen offset:0 sc1
v_add_f32 v32,v32,v20
v_fma_f32 v33,v20,v20,v33
v_add_f32 v32,v32,v21
v_fma_f32 v33,v21,v21,v33
v_mfma_f32_16x16x32_bf16 v[24:27],a[104:107],v[180:183],v[24:27]
ds_read_b128 v[144:147],v2 offset:53248
v_add_f32 v32,v32,v22
v_mfma_f32_16x16x32_bf16 v[24:27],a[96:99],v[176:179],v[24:27]
v_add_f32 v32,v32,v23
v_fma_f32 v33,v22,v22,v33
v_mfma_f32_16x16x32_bf16 v[24:27],a[112:115],v[184:187],v[24:27]
ds_read_b128 v[148:151],v2 offset:54272
v_fma_f32 v33,v23,v23,v33
v_mfma_f32_16x16x32_bf16 v[24:27],a[120:123],v[188:191],v[24:27]
s_nop 0
v_permlane16_swap_b32 v32,v33
v_mfma_f32_16x16x32_bf16 v[28:31],a[124:127],v[188:191],v[28:31]
v_add_f32 v32,v32,v33
v_mov_b32 v33,v32
v_mfma_f32_16x16x32_bf16 v[28:31],a[116:119],v[184:187],v[28:31]
s_waitcnt lgkmcnt(2)
ds_read_b128 v[152:155],v2 offset:55296
v_permlane32_swap_b32 v32,v33
v_add_f32 v32,v32,v33
v_mfma_f32_16x16x32_bf16 v[28:31],a[132:135],v[128:131],v[28:31]
ds_write_b32 v13,v32 offset:2048
v_mfma_f32_16x16x32_bf16 v[28:31],a[140:143],v[132:135],v[28:31]
ds_read_b128 v[156:159],v2 offset:56320
v_mfma_f32_16x16x32_bf16 v[24:27],a[136:139],v[132:135],v[24:27]
ds_read_b128 v[160:163],v2 offset:57344
v_mfma_f32_16x16x32_bf16 v[24:27],a[128:131],v[128:131],v[24:27]
v_mfma_f32_16x16x32_bf16 v[24:27],a[144:147],v[136:139],v[24:27]
s_waitcnt vmcnt(11) lgkmcnt(2)
ds_read_b128 v[164:167],v2 offset:58368
v_mfma_f32_16x16x32_bf16 v[24:27],a[152:155],v[140:143],v[24:27]
s_barrier
v_mfma_f32_16x16x32_bf16 v[28:31],a[156:159],v[140:143],v[28:31]
ds_read_b128 v[168:171],v2 offset:59392
ds_read_b32 v15,v7 offset:256
v_mfma_f32_16x16x32_bf16 v[28:31],a[148:151],v[136:139],v[28:31]
s_waitcnt lgkmcnt(4)
v_mfma_f32_16x16x32_bf16 v[28:31],a[164:167],v[144:147],v[28:31]
ds_read_b128 v[172:175],v2 offset:60416
v_mfma_f32_16x16x32_bf16 v[28:31],a[172:175],v[148:151],v[28:31]
v_mfma_f32_16x16x32_bf16 v[24:27],a[168:171],v[148:151],v[24:27]
s_waitcnt lgkmcnt(1)
ds_read_b128 v[176:179],v2 offset:61440
v_mfma_f32_16x16x32_bf16 v[24:27],a[160:163],v[144:147],v[24:27]
v_cmp_eq_u32 vcc,s60,v15
v_mfma_f32_16x16x32_bf16 v[24:27],a[176:179],v[152:155],v[24:27]
s_cmp_eq_u32 vcc_lo,-1
ds_read_b128 v[180:183],v2 offset:62464
s_cselect_b32 s66,s86,s59
v_mfma_f32_16x16x32_bf16 v[24:27],a[184:187],v[156:159],v[24:27]
buffer_load_dwordx4 v0,s[36:39],s66 offen offset:0 sc1 lds
v_mfma_f32_16x16x32_bf16 v[28:31],a[188:191],v[156:159],v[28:31]
s_xor_b32 s64,s64,65536
ds_read_b128 v[184:187],v2 offset:63488
s_add_u32 s74,s53,s55
v_mfma_f32_16x16x32_bf16 v[28:31],a[180:183],v[152:155],v[28:31]
s_waitcnt lgkmcnt(3)
s_cmp_eq_u32 s74,s56
s_cselect_b32 s74,s57,s74
v_mfma_f32_16x16x32_bf16 v[28:31],a[196:199],v[160:163],v[28:31]
s_waitcnt vmcnt(8)
ds_read_b128 v[188:191],v2 offset:64512
v_xor_b32 v2,65536,v2
v_mfma_f32_16x16x32_bf16 v[28:31],a[204:207],v[164:167],v[28:31]
s_barrier
v_mfma_f32_16x16x32_bf16 v[24:27],a[200:203],v[164:167],v[24:27]
s_cmp_eq_u32 s74,s58
ds_read_b128 v[128:131],v2 offset:0
ds_read_b128 v[52:55],v8 offset:8192
v_mfma_f32_16x16x32_bf16 v[24:27],a[192:195],v[160:163],v[24:27]
buffer_load_dwordx4 v0,s[36:39],s66 offen offset:1024 sc1 lds
v_mfma_f32_16x16x32_bf16 v[24:27],a[208:211],v[168:171],v[24:27]
s_cselect_b32 s74,s59,s74
ds_read_b128 v[132:135],v2 offset:1024
ds_read_b64 v[58:59],v9 offset:9216
v_mfma_f32_16x16x32_bf16 v[24:27],a[216:219],v[172:175],v[24:27]
s_mov_b32 s52,s74
v_xor_b32 v8,16384,v8
v_xor_b32 v9,16384,v9
v_mfma_f32_16x16x32_bf16 v[28:31],a[220:223],v[172:175],v[28:31]
s_cmp_lt_u32 s67,s59
ds_read_b128 v[136:139],v2 offset:2048
v_mfma_f32_16x16x32_bf16 v[28:31],a[212:215],v[168:171],v[28:31]
s_waitcnt lgkmcnt(5)
buffer_load_dwordx4 v0,s[36:39],s66 offen offset:2048 sc1 lds
v_mfma_f32_16x16x32_bf16 v[28:31],a[228:231],v[176:179],v[28:31]
ds_read_b128 v[140:143],v2 offset:3072
v_mfma_f32_16x16x32_bf16 v[28:31],a[236:239],v[180:183],v[28:31]
v_mfma_f32_16x16x32_bf16 v[24:27],a[232:235],v[180:183],v[24:27]
s_waitcnt lgkmcnt(2)
ds_read_b128 v[144:147],v2 offset:4096
v_mfma_f32_16x16x32_bf16 v[24:27],a[224:227],v[176:179],v[24:27]
buffer_load_dwordx4 v0,s[36:39],s66 offen offset:3072 sc1 lds
v_mfma_f32_16x16x32_bf16 v[24:27],a[240:243],v[184:187],v[24:27]
ds_read_b128 v[148:151],v2 offset:5120
v_mfma_f32_16x16x32_bf16 v[24:27],a[248:251],v[188:191],v[24:27]
v_mfma_f32_16x16x32_bf16 v[28:31],a[252:255],v[188:191],v[28:31]
s_waitcnt lgkmcnt(2)
ds_read_b128 v[152:155],v2 offset:6144
v_mfma_f32_16x16x32_bf16 v[28:31],a[244:247],v[184:187],v[28:31]
s_cbranch_scc1 Lg2_d0w1_%=
s_branch Lg2_slow0_%=
Lg2_d0w2_%=:
v_mfma_f32_16x16x32_bf16 v[20:23],a[4:7],v[128:131],0
s_add_u32 m0,s62,16384
ds_read_b128 v[156:159],v2 offset:7168
v_lshlrev_b32 v32,16,v52
v_mfma_f32_16x16x32_bf16 v[20:23],a[12:15],v[132:135],v[20:23]
s_lshr_b32 s73,s47,0
v_and_b32 v33,0xffff0000,v52
v_lshlrev_b32 v34,16,v53
v_mfma_f32_16x16x32_bf16 v[16:19],a[8:11],v[132:135],0
s_lshr_b32 s85,s47,5
ds_read_b128 v[160:163],v2 offset:8192
v_and_b32 v35,0xffff0000,v53
v_mfma_f32_16x16x32_bf16 v[16:19],a[0:3],v[128:131],v[16:19]
buffer_load_dwordx4 v1,s[36:39],s66 offen offset:0 sc1 lds
v_mfma_f32_16x16x32_bf16 v[16:19],a[16:19],v[136:139],v[16:19]
s_xor_b32 s87,s64,65536
ds_read_b128 v[164:167],v2 offset:9216
v_lshlrev_b32 v36,16,v54
v_mfma_f32_16x16x32_bf16 v[16:19],a[24:27],v[140:143],v[16:19]
s_cmp_ge_u32 s66,s59
v_and_b32 v37,0xffff0000,v54
v_lshlrev_b32 v38,16,v55
v_and_b32 v39,0xffff0000,v55
v_fma_f32 v32,v32,v59,v58
v_fma_f32 v33,v33,v59,v58
v_mfma_f32_16x16x32_bf16 v[20:23],a[28:31],v[140:143],v[20:23]
s_cselect_b32 s86,s49,s50
ds_read_b128 v[168:171],v2 offset:10240
v_fma_f32 v34,v34,v59,v58
v_mfma_f32_16x16x32_bf16 v[20:23],a[20:23],v[136:139],v[20:23]
s_waitcnt lgkmcnt(3)
buffer_load_dwordx4 v1,s[36:39],s66 offen offset:1024 sc1 lds
v_mfma_f32_16x16x32_bf16 v[20:23],a[36:39],v[144:147],v[20:23]
s_cselect_b32 s87,s87,s63
ds_read_b128 v[172:175],v2 offset:11264
v_fma_f32 v35,v35,v59,v58
v_mfma_f32_16x16x32_bf16 v[20:23],a[44:47],v[148:151],v[20:23]
s_lshr_b32 s72,s66,6
v_fma_f32 v36,v36,v59,v58
v_fma_f32 v37,v37,v59,v58
v_fma_f32 v38,v38,v59,v58
v_fma_f32 v39,v39,v59,v58
v_add_f32 v24,v24,v32
v_mfma_f32_16x16x32_bf16 v[16:19],a[40:43],v[148:151],v[16:19]
s_lshr_b32 s71,s62,2
ds_read_b128 v[176:179],v2 offset:12288
v_add_f32 v25,v25,v33
v_mfma_f32_16x16x32_bf16 v[16:19],a[32:35],v[144:147],v[16:19]
buffer_load_dwordx4 v1,s[36:39],s66 offen offset:2048 sc1 lds
v_mfma_f32_16x16x32_bf16 v[16:19],a[48:51],v[152:155],v[16:19]
s_mov_b32 s62,s87
ds_read_b128 v[180:183],v2 offset:13312
v_add_f32 v26,v26,v34
v_mfma_f32_16x16x32_bf16 v[16:19],a[56:59],v[156:159],v[16:19]
v_add_f32 v27,v27,v35
v_add_f32 v28,v28,v36
v_add_f32 v29,v29,v37
v_add_f32 v30,v30,v38
v_mfma_f32_16x16x32_bf16 v[20:23],a[60:63],v[156:159],v[20:23]
ds_read_b128 v[184:187],v2 offset:14336
v_add_f32 v31,v31,v39
v_mfma_f32_16x16x32_bf16 v[20:23],a[52:55],v[152:155],v[20:23]
s_waitcnt lgkmcnt(3)
buffer_load_dwordx4 v1,s[36:39],s66 offen offset:3072 sc1 lds
v_mfma_f32_16x16x32_bf16 v[20:23],a[68:71],v[160:163],v[20:23]
s_waitcnt vmcnt(10)
ds_read_b128 v[188:191],v2 offset:15360
v_cvt_pk_bf16_f32 v40,v24,v25
v_mfma_f32_16x16x32_bf16 v[20:23],a[76:79],v[164:167],v[20:23]
s_barrier
v_mfma_f32_16x16x32_bf16 v[16:19],a[72:75],v[164:167],v[16:19]
s_add_u32 m0,s71,s88
ds_read_b128 v[128:131],v2 offset:16384
v_cvt_pk_bf16_f32 v41,v26,v27
v_mfma_f32_16x16x32_bf16 v[16:19],a[64:67],v[160:163],v[16:19]
v_cvt_pk_bf16_f32 v42,v28,v29
v_cvt_pk_bf16_f32 v43,v30,v31
v_mfma_f32_16x16x32_bf16 v[16:19],a[80:83],v[168:171],v[16:19]
ds_read_b128 v[132:135],v2 offset:17408)"
R"(
v_add_f32 v32,v24,v25
v_mfma_f32_16x16x32_bf16 v[16:19],a[88:91],v[172:175],v[16:19]
buffer_load_dwordx4 v10,s[36:39],s66 offen offset:0 sc1 lds
buffer_load_dword v11,s[36:39],s72 offen offset:1024 sc1 lds
v_mul_f32 v33,v24,v24
v_mfma_f32_16x16x32_bf16 v[20:23],a[92:95],v[172:175],v[20:23]
s_mov_b32 m0,s62
ds_read_b128 v[136:139],v2 offset:18432
v_fma_f32 v33,v25,v25,v33
v_mfma_f32_16x16x32_bf16 v[20:23],a[84:87],v[168:171],v[20:23]
s_waitcnt lgkmcnt(3)
v_add_f32 v32,v32,v26
v_fma_f32 v33,v26,v26,v33
v_mfma_f32_16x16x32_bf16 v[20:23],a[100:103],v[176:179],v[20:23]
ds_read_b128 v[140:143],v2 offset:19456
v_add_f32 v32,v32,v27
v_mfma_f32_16x16x32_bf16 v[20:23],a[108:111],v[180:183],v[20:23]
v_fma_f32 v33,v27,v27,v33
v_add_f32 v32,v32,v28
v_fma_f32 v33,v28,v28,v33
v_add_f32 v32,v32,v29
v_mfma_f32_16x16x32_bf16 v[16:19],a[104:107],v[180:183],v[16:19]
buffer_store_dwordx4 v[40:43],v3,s[36:39],s73 offen offset:0 sc1
ds_read_b128 v[144:147],v2 offset:20480
v_fma_f32 v33,v29,v29,v33
v_add_f32 v32,v32,v30
v_mfma_f32_16x16x32_bf16 v[16:19],a[96:99],v[176:179],v[16:19]
v_add_f32 v32,v32,v31
v_fma_f32 v33,v30,v30,v33
v_mfma_f32_16x16x32_bf16 v[16:19],a[112:115],v[184:187],v[16:19]
ds_read_b128 v[148:151],v2 offset:21504
v_fma_f32 v33,v31,v31,v33
v_mfma_f32_16x16x32_bf16 v[16:19],a[120:123],v[188:191],v[16:19]
s_nop 0
v_permlane16_swap_b32 v32,v33
v_mfma_f32_16x16x32_bf16 v[20:23],a[124:127],v[188:191],v[20:23]
v_add_f32 v32,v32,v33
v_mov_b32 v33,v32
v_mfma_f32_16x16x32_bf16 v[20:23],a[116:119],v[184:187],v[20:23]
s_waitcnt lgkmcnt(2)
ds_read_b128 v[152:155],v2 offset:22528
v_permlane32_swap_b32 v32,v33
v_add_f32 v32,v32,v33
v_mfma_f32_16x16x32_bf16 v[20:23],a[132:135],v[128:131],v[20:23]
ds_write_b32 v13,v32 offset:0
v_mfma_f32_16x16x32_bf16 v[20:23],a[140:143],v[132:135],v[20:23]
ds_read_b128 v[156:159],v2 offset:23552
v_mfma_f32_16x16x32_bf16 v[16:19],a[136:139],v[132:135],v[16:19]
ds_read_b128 v[160:163],v2 offset:24576
v_mfma_f32_16x16x32_bf16 v[16:19],a[128:131],v[128:131],v[16:19]
v_mfma_f32_16x16x32_bf16 v[16:19],a[144:147],v[136:139],v[16:19]
s_waitcnt vmcnt(11) lgkmcnt(2)
ds_read_b128 v[164:167],v2 offset:25600
v_mfma_f32_16x16x32_bf16 v[16:19],a[152:155],v[140:143],v[16:19]
s_barrier
v_mfma_f32_16x16x32_bf16 v[20:23],a[156:159],v[140:143],v[20:23]
ds_read_b128 v[168:171],v2 offset:26624
ds_read_b32 v15,v7 offset:0
v_mfma_f32_16x16x32_bf16 v[20:23],a[148:151],v[136:139],v[20:23]
s_waitcnt lgkmcnt(4)
ds_read_b128 v[44:47],v14 offset:0
v_mfma_f32_16x16x32_bf16 v[20:23],a[164:167],v[144:147],v[20:23]
ds_read_b128 v[172:175],v2 offset:27648
v_mfma_f32_16x16x32_bf16 v[20:23],a[172:175],v[148:151],v[20:23]
v_mfma_f32_16x16x32_bf16 v[16:19],a[168:171],v[148:151],v[16:19]
s_waitcnt lgkmcnt(2)
ds_read_b128 v[176:179],v2 offset:28672
v_mfma_f32_16x16x32_bf16 v[16:19],a[160:163],v[144:147],v[16:19]
s_waitcnt lgkmcnt(2)
v_add_f32 v44,v44,v45
v_add_f32 v46,v46,v47
v_mfma_f32_16x16x32_bf16 v[16:19],a[176:179],v[152:155],v[16:19]
v_cmp_eq_u32 vcc,s60,v15
v_add_f32 v44,v44,v46
v_mfma_f32_16x16x32_bf16 v[16:19],a[184:187],v[156:159],v[16:19]
s_cmp_eq_u32 vcc_lo,-1
ds_read_b128 v[180:183],v2 offset:29696
s_cselect_b32 s67,s86,s59
v_mfma_f32_16x16x32_bf16 v[20:23],a[188:191],v[156:159],v[20:23]
buffer_store_dword v44,v12,s[36:39],s85 offen offset:0 sc1
buffer_load_dwordx4 v0,s[36:39],s67 offen offset:0 sc1 lds
ds_read_b128 v[184:187],v2 offset:30720
v_mfma_f32_16x16x32_bf16 v[20:23],a[180:183],v[152:155],v[20:23]
s_waitcnt lgkmcnt(3)
s_xor_b32 s63,s63,65536
s_add_u32 s53,s52,s55
v_mfma_f32_16x16x32_bf16 v[20:23],a[196:199],v[160:163],v[20:23]
s_waitcnt vmcnt(9)
ds_read_b128 v[188:191],v2 offset:31744
s_cmp_eq_u32 s53,s56
v_mfma_f32_16x16x32_bf16 v[20:23],a[204:207],v[164:167],v[20:23]
s_barrier
v_mfma_f32_16x16x32_bf16 v[16:19],a[200:203],v[164:167],v[16:19]
s_cselect_b32 s53,s57,s53
ds_read_b128 v[128:131],v2 offset:32768
ds_read_b128 v[48:51],v8 offset:0
v_mfma_f32_16x16x32_bf16 v[16:19],a[192:195],v[160:163],v[16:19]
s_cmp_eq_u32 s53,s58
ds_read_b64 v[56:57],v9 offset:1024
s_cselect_b32 s53,s59,s53
v_mfma_f32_16x16x32_bf16 v[16:19],a[208:211],v[168:171],v[16:19]
buffer_load_dwordx4 v0,s[36:39],s67 offen offset:1024 sc1 lds
v_mfma_f32_16x16x32_bf16 v[16:19],a[216:219],v[172:175],v[16:19]
s_cmp_ge_u32 s66,s59
ds_read_b128 v[132:135],v2 offset:33792
v_mfma_f32_16x16x32_bf16 v[20:23],a[220:223],v[172:175],v[20:23]
ds_read_b128 v[136:139],v2 offset:34816
v_mfma_f32_16x16x32_bf16 v[20:23],a[212:215],v[168:171],v[20:23]
s_waitcnt lgkmcnt(5)
v_mfma_f32_16x16x32_bf16 v[20:23],a[228:231],v[176:179],v[20:23]
buffer_load_dwordx4 v0,s[36:39],s67 offen offset:2048 sc1 lds
v_mfma_f32_16x16x32_bf16 v[20:23],a[236:239],v[180:183],v[20:23]
s_waitcnt lgkmcnt(2)
ds_read_b128 v[140:143],v2 offset:35840
v_mfma_f32_16x16x32_bf16 v[16:19],a[232:235],v[180:183],v[16:19]
ds_read_b128 v[144:147],v2 offset:36864
v_mfma_f32_16x16x32_bf16 v[16:19],a[224:227],v[176:179],v[16:19]
v_mfma_f32_16x16x32_bf16 v[16:19],a[240:243],v[184:187],v[16:19]
buffer_load_dwordx4 v0,s[36:39],s67 offen offset:3072 sc1 lds
v_mfma_f32_16x16x32_bf16 v[16:19],a[248:251],v[188:191],v[16:19]
ds_read_b128 v[148:151],v2 offset:37888
v_mfma_f32_16x16x32_bf16 v[20:23],a[252:255],v[188:191],v[20:23]
s_waitcnt lgkmcnt(2)
ds_read_b128 v[152:155],v2 offset:38912
v_mfma_f32_16x16x32_bf16 v[20:23],a[244:247],v[184:187],v[20:23]
s_cbranch_scc1 Lg2_slow1_%=
Lg2_d1w2_%=:
v_mfma_f32_16x16x32_bf16 v[28:31],a[4:7],v[128:131],0
s_add_u32 m0,s62,16384
ds_read_b128 v[156:159],v2 offset:39936
v_lshlrev_b32 v32,16,v48
v_mfma_f32_16x16x32_bf16 v[28:31],a[12:15],v[132:135],v[28:31]
s_lshr_b32 s73,s48,0
v_and_b32 v33,0xffff0000,v48
v_lshlrev_b32 v34,16,v49
v_mfma_f32_16x16x32_bf16 v[24:27],a[8:11],v[132:135],0
s_lshr_b32 s85,s48,5
ds_read_b128 v[160:163],v2 offset:40960
v_and_b32 v35,0xffff0000,v49
v_mfma_f32_16x16x32_bf16 v[24:27],a[0:3],v[128:131],v[24:27]
buffer_load_dwordx4 v1,s[36:39],s67 offen offset:0 sc1 lds
v_mfma_f32_16x16x32_bf16 v[24:27],a[16:19],v[136:139],v[24:27]
s_xor_b32 s87,s63,65536
ds_read_b128 v[164:167],v2 offset:41984
v_lshlrev_b32 v36,16,v50
v_mfma_f32_16x16x32_bf16 v[24:27],a[24:27],v[140:143],v[24:27]
s_cmp_ge_u32 s67,s59
v_and_b32 v37,0xffff0000,v50
v_lshlrev_b32 v38,16,v51
v_and_b32 v39,0xffff0000,v51
v_fma_f32 v32,v32,v57,v56
v_fma_f32 v33,v33,v57,v56
v_mfma_f32_16x16x32_bf16 v[28:31],a[28:31],v[140:143],v[28:31]
s_cselect_b32 s86,s50,s51
ds_read_b128 v[168:171],v2 offset:43008
v_fma_f32 v34,v34,v57,v56
v_mfma_f32_16x16x32_bf16 v[28:31],a[20:23],v[136:139],v[28:31]
s_waitcnt lgkmcnt(3)
buffer_load_dwordx4 v1,s[36:39],s67 offen offset:1024 sc1 lds
v_mfma_f32_16x16x32_bf16 v[28:31],a[36:39],v[144:147],v[28:31]
s_cselect_b32 s87,s87,s64
ds_read_b128 v[172:175],v2 offset:44032
v_fma_f32 v35,v35,v57,v56
v_mfma_f32_16x16x32_bf16 v[28:31],a[44:47],v[148:151],v[28:31]
s_lshr_b32 s72,s67,6
v_fma_f32 v36,v36,v57,v56
v_fma_f32 v37,v37,v57,v56
v_fma_f32 v38,v38,v57,v56
v_fma_f32 v39,v39,v57,v56
v_add_f32 v16,v16,v32
v_mfma_f32_16x16x32_bf16 v[24:27],a[40:43],v[148:151],v[24:27]
s_lshr_b32 s71,s62,2
ds_read_b128 v[176:179],v2 offset:45056
v_add_f32 v17,v17,v33
v_mfma_f32_16x16x32_bf16 v[24:27],a[32:35],v[144:147],v[24:27]
buffer_load_dwordx4 v1,s[36:39],s67 offen offset:2048 sc1 lds
v_mfma_f32_16x16x32_bf16 v[24:27],a[48:51],v[152:155],v[24:27]
s_mov_b32 s62,s87
ds_read_b128 v[180:183],v2 offset:46080
v_add_f32 v18,v18,v34
v_mfma_f32_16x16x32_bf16 v[24:27],a[56:59],v[156:159],v[24:27]
s_mov_b64 s[44:45],s[46:47]
v_add_f32 v19,v19,v35
v_add_f32 v20,v20,v36
v_add_f32 v21,v21,v37
v_add_f32 v22,v22,v38
v_mfma_f32_16x16x32_bf16 v[28:31],a[60:63],v[156:159],v[28:31]
s_mov_b64 s[46:47],s[48:49]
ds_read_b128 v[184:187],v2 offset:47104
v_add_f32 v23,v23,v39
v_mfma_f32_16x16x32_bf16 v[28:31],a[52:55],v[152:155],v[28:31]
s_waitcnt lgkmcnt(3)
buffer_load_dwordx4 v1,s[36:39],s67 offen offset:3072 sc1 lds
v_mfma_f32_16x16x32_bf16 v[28:31],a[68:71],v[160:163],v[28:31]
s_waitcnt vmcnt(10)
ds_read_b128 v[188:191],v2 offset:48128
v_cvt_pk_bf16_f32 v40,v16,v17
v_mfma_f32_16x16x32_bf16 v[28:31],a[76:79],v[164:167],v[28:31]
s_barrier
v_mfma_f32_16x16x32_bf16 v[24:27],a[72:75],v[164:167],v[24:27])"
R"(
s_add_u32 m0,s71,s88
ds_read_b128 v[128:131],v2 offset:49152
v_cvt_pk_bf16_f32 v41,v18,v19
v_mfma_f32_16x16x32_bf16 v[24:27],a[64:67],v[160:163],v[24:27]
s_mov_b64 s[48:49],s[50:51]
v_cvt_pk_bf16_f32 v42,v20,v21
v_cvt_pk_bf16_f32 v43,v22,v23
v_mfma_f32_16x16x32_bf16 v[24:27],a[80:83],v[168:171],v[24:27]
s_mov_b64 s[50:51],s[52:53]
ds_read_b128 v[132:135],v2 offset:50176
v_add_f32 v32,v16,v17
v_mfma_f32_16x16x32_bf16 v[24:27],a[88:91],v[172:175],v[24:27]
buffer_load_dwordx4 v10,s[36:39],s67 offen offset:0 sc1 lds
buffer_load_dword v11,s[36:39],s72 offen offset:1024 sc1 lds
v_mul_f32 v33,v16,v16
v_mfma_f32_16x16x32_bf16 v[28:31],a[92:95],v[172:175],v[28:31]
s_mov_b32 m0,s62
ds_read_b128 v[136:139],v2 offset:51200
v_fma_f32 v33,v17,v17,v33
v_mfma_f32_16x16x32_bf16 v[28:31],a[84:87],v[168:171],v[28:31]
s_waitcnt lgkmcnt(3)
v_add_f32 v32,v32,v18
v_fma_f32 v33,v18,v18,v33
v_mfma_f32_16x16x32_bf16 v[28:31],a[100:103],v[176:179],v[28:31]
ds_read_b128 v[140:143],v2 offset:52224
v_add_f32 v32,v32,v19
v_mfma_f32_16x16x32_bf16 v[28:31],a[108:111],v[180:183],v[28:31]
v_fma_f32 v33,v19,v19,v33
v_add_f32 v32,v32,v20
v_fma_f32 v33,v20,v20,v33
v_add_f32 v32,v32,v21
v_mfma_f32_16x16x32_bf16 v[24:27],a[104:107],v[180:183],v[24:27]
buffer_store_dwordx4 v[40:43],v3,s[36:39],s73 offen offset:0 sc1
ds_read_b128 v[144:147],v2 offset:53248
v_fma_f32 v33,v21,v21,v33
v_add_f32 v32,v32,v22
v_mfma_f32_16x16x32_bf16 v[24:27],a[96:99],v[176:179],v[24:27]
v_add_f32 v32,v32,v23
v_fma_f32 v33,v22,v22,v33
v_mfma_f32_16x16x32_bf16 v[24:27],a[112:115],v[184:187],v[24:27]
ds_read_b128 v[148:151],v2 offset:54272
v_fma_f32 v33,v23,v23,v33
v_mfma_f32_16x16x32_bf16 v[24:27],a[120:123],v[188:191],v[24:27]
s_nop 0
v_permlane16_swap_b32 v32,v33
v_mfma_f32_16x16x32_bf16 v[28:31],a[124:127],v[188:191],v[28:31]
v_add_f32 v32,v32,v33
v_mov_b32 v33,v32
v_mfma_f32_16x16x32_bf16 v[28:31],a[116:119],v[184:187],v[28:31]
s_waitcnt lgkmcnt(2)
ds_read_b128 v[152:155],v2 offset:55296
v_permlane32_swap_b32 v32,v33
v_add_f32 v32,v32,v33
v_mfma_f32_16x16x32_bf16 v[28:31],a[132:135],v[128:131],v[28:31]
ds_write_b32 v13,v32 offset:2048
v_mfma_f32_16x16x32_bf16 v[28:31],a[140:143],v[132:135],v[28:31]
ds_read_b128 v[156:159],v2 offset:56320
v_mfma_f32_16x16x32_bf16 v[24:27],a[136:139],v[132:135],v[24:27]
ds_read_b128 v[160:163],v2 offset:57344
v_mfma_f32_16x16x32_bf16 v[24:27],a[128:131],v[128:131],v[24:27]
v_mfma_f32_16x16x32_bf16 v[24:27],a[144:147],v[136:139],v[24:27]
s_waitcnt vmcnt(11) lgkmcnt(2)
ds_read_b128 v[164:167],v2 offset:58368
v_mfma_f32_16x16x32_bf16 v[24:27],a[152:155],v[140:143],v[24:27]
s_barrier
v_mfma_f32_16x16x32_bf16 v[28:31],a[156:159],v[140:143],v[28:31]
ds_read_b128 v[168:171],v2 offset:59392
ds_read_b32 v15,v7 offset:256
v_mfma_f32_16x16x32_bf16 v[28:31],a[148:151],v[136:139],v[28:31]
s_waitcnt lgkmcnt(4)
ds_read_b128 v[44:47],v14 offset:2048
v_mfma_f32_16x16x32_bf16 v[28:31],a[164:167],v[144:147],v[28:31]
ds_read_b128 v[172:175],v2 offset:60416
v_mfma_f32_16x16x32_bf16 v[28:31],a[172:175],v[148:151],v[28:31]
v_mfma_f32_16x16x32_bf16 v[24:27],a[168:171],v[148:151],v[24:27]
s_waitcnt lgkmcnt(2)
ds_read_b128 v[176:179],v2 offset:61440
v_mfma_f32_16x16x32_bf16 v[24:27],a[160:163],v[144:147],v[24:27]
s_waitcnt lgkmcnt(2)
v_add_f32 v44,v44,v45
v_add_f32 v46,v46,v47
v_mfma_f32_16x16x32_bf16 v[24:27],a[176:179],v[152:155],v[24:27]
v_cmp_eq_u32 vcc,s60,v15
v_add_f32 v44,v44,v46
v_mfma_f32_16x16x32_bf16 v[24:27],a[184:187],v[156:159],v[24:27]
s_cmp_eq_u32 vcc_lo,-1
ds_read_b128 v[180:183],v2 offset:62464
s_cselect_b32 s66,s86,s59
v_mfma_f32_16x16x32_bf16 v[28:31],a[188:191],v[156:159],v[28:31]
buffer_store_dword v44,v12,s[36:39],s85 offen offset:0 sc1
buffer_load_dwordx4 v0,s[36:39],s66 offen offset:0 sc1 lds
ds_read_b128 v[184:187],v2 offset:63488
v_mfma_f32_16x16x32_bf16 v[28:31],a[180:183],v[152:155],v[28:31]
s_waitcnt lgkmcnt(3)
s_xor_b32 s64,s64,65536
s_add_u32 s74,s53,s55
v_mfma_f32_16x16x32_bf16 v[28:31],a[196:199],v[160:163],v[28:31]
s_waitcnt vmcnt(9)
ds_read_b128 v[188:191],v2 offset:64512
v_xor_b32 v2,65536,v2
v_mfma_f32_16x16x32_bf16 v[28:31],a[204:207],v[164:167],v[28:31]
s_barrier
v_mfma_f32_16x16x32_bf16 v[24:27],a[200:203],v[164:167],v[24:27]
s_cmp_eq_u32 s74,s56
ds_read_b128 v[128:131],v2 offset:0
ds_read_b128 v[52:55],v8 offset:8192
v_mfma_f32_16x16x32_bf16 v[24:27],a[192:195],v[160:163],v[24:27]
s_cselect_b32 s74,s57,s74
ds_read_b64 v[58:59],v9 offset:9216
v_xor_b32 v8,16384,v8
v_mfma_f32_16x16x32_bf16 v[24:27],a[208:211],v[168:171],v[24:27]
buffer_load_dwordx4 v0,s[36:39],s66 offen offset:1024 sc1 lds
v_mfma_f32_16x16x32_bf16 v[24:27],a[216:219],v[172:175],v[24:27]
s_cmp_eq_u32 s74,s58
ds_read_b128 v[132:135],v2 offset:1024
v_xor_b32 v9,16384,v9
v_mfma_f32_16x16x32_bf16 v[28:31],a[220:223],v[172:175],v[28:31]
s_cselect_b32 s74,s59,s74
ds_read_b128 v[136:139],v2 offset:2048
s_mov_b32 s52,s74
v_mfma_f32_16x16x32_bf16 v[28:31],a[212:215],v[168:171],v[28:31]
s_waitcnt lgkmcnt(5)
s_cmp_lt_u32 s67,s59
v_mfma_f32_16x16x32_bf16 v[28:31],a[228:231],v[176:179],v[28:31]
buffer_load_dwordx4 v0,s[36:39],s66 offen offset:2048 sc1 lds
v_mfma_f32_16x16x32_bf16 v[28:31],a[236:239],v[180:183],v[28:31]
s_waitcnt lgkmcnt(2)
ds_read_b128 v[140:143],v2 offset:3072
v_mfma_f32_16x16x32_bf16 v[24:27],a[232:235],v[180:183],v[24:27]
ds_read_b128 v[144:147],v2 offset:4096
v_mfma_f32_16x16x32_bf16 v[24:27],a[224:227],v[176:179],v[24:27]
v_mfma_f32_16x16x32_bf16 v[24:27],a[240:243],v[184:187],v[24:27]
buffer_load_dwordx4 v0,s[36:39],s66 offen offset:3072 sc1 lds
v_mfma_f32_16x16x32_bf16 v[24:27],a[248:251],v[188:191],v[24:27]
ds_read_b128 v[148:151],v2 offset:5120
v_mfma_f32_16x16x32_bf16 v[28:31],a[252:255],v[188:191],v[28:31]
s_waitcnt lgkmcnt(2)
ds_read_b128 v[152:155],v2 offset:6144
v_mfma_f32_16x16x32_bf16 v[28:31],a[244:247],v[184:187],v[28:31]
s_cbranch_scc1 Lg2_d0w2_%=
s_branch Lg2_slow0_%=
Lg2_d0w3_%=:
v_mfma_f32_16x16x32_bf16 v[20:23],a[4:7],v[128:131],0
s_add_u32 m0,s62,16384
ds_read_b128 v[156:159],v2 offset:7168
v_lshlrev_b32 v32,16,v52
v_mfma_f32_16x16x32_bf16 v[20:23],a[12:15],v[132:135],v[20:23]
s_lshr_b32 s73,s47,0
v_and_b32 v33,0xffff0000,v52
v_lshlrev_b32 v34,16,v53
v_mfma_f32_16x16x32_bf16 v[16:19],a[8:11],v[132:135],0
s_xor_b32 s87,s64,65536
ds_read_b128 v[160:163],v2 offset:8192
v_and_b32 v35,0xffff0000,v53
v_mfma_f32_16x16x32_bf16 v[16:19],a[0:3],v[128:131],v[16:19]
s_cmp_ge_u32 s66,s59
v_lshlrev_b32 v36,16,v54
v_and_b32 v37,0xffff0000,v54
v_mfma_f32_16x16x32_bf16 v[16:19],a[16:19],v[136:139],v[16:19]
buffer_load_dwordx4 v1,s[36:39],s66 offen offset:0 sc1 lds
v_mfma_f32_16x16x32_bf16 v[16:19],a[24:27],v[140:143],v[16:19]
s_cselect_b32 s86,s49,s50
ds_read_b128 v[164:167],v2 offset:9216
v_lshlrev_b32 v38,16,v55
v_and_b32 v39,0xffff0000,v55
v_fma_f32 v32,v32,v59,v58
v_fma_f32 v33,v33,v59,v58
v_mfma_f32_16x16x32_bf16 v[20:23],a[28:31],v[140:143],v[20:23]
s_cselect_b32 s87,s87,s63
ds_read_b128 v[168:171],v2 offset:10240
v_fma_f32 v34,v34,v59,v58
v_mfma_f32_16x16x32_bf16 v[20:23],a[20:23],v[136:139],v[20:23]
s_waitcnt lgkmcnt(3)
v_fma_f32 v35,v35,v59,v58
v_fma_f32 v36,v36,v59,v58
v_mfma_f32_16x16x32_bf16 v[20:23],a[36:39],v[144:147],v[20:23]
buffer_load_dwordx4 v1,s[36:39],s66 offen offset:1024 sc1 lds
v_mfma_f32_16x16x32_bf16 v[20:23],a[44:47],v[148:151],v[20:23]
s_lshr_b32 s72,s66,6
ds_read_b128 v[172:175],v2 offset:11264
v_fma_f32 v37,v37,v59,v58
v_fma_f32 v38,v38,v59,v58
v_fma_f32 v39,v39,v59,v58
v_add_f32 v24,v24,v32
v_mfma_f32_16x16x32_bf16 v[16:19],a[40:43],v[148:151],v[16:19]
s_lshr_b32 s71,s62,2
ds_read_b128 v[176:179],v2 offset:12288
v_add_f32 v25,v25,v33
v_mfma_f32_16x16x32_bf16 v[16:19],a[32:35],v[144:147],v[16:19]
s_mov_b32 s62,s87
v_add_f32 v26,v26,v34
v_add_f32 v27,v27,v35
v_mfma_f32_16x16x32_bf16 v[16:19],a[48:51],v[152:155],v[16:19]
buffer_load_dwordx4 v1,s[36:39],s66 offen offset:2048 sc1 lds
v_mfma_f32_16x16x32_bf16 v[16:19],a[56:59],v[156:159],v[16:19]
ds_read_b128 v[180:183],v2 offset:13312
v_add_f32 v28,v28,v36
v_add_f32 v29,v29,v37
v_add_f32 v30,v30,v38
v_mfma_f32_16x16x32_bf16 v[20:23],a[60:63],v[156:159],v[20:23])"
R"(
ds_read_b128 v[184:187],v2 offset:14336
v_add_f32 v31,v31,v39
v_mfma_f32_16x16x32_bf16 v[20:23],a[52:55],v[152:155],v[20:23]
s_waitcnt lgkmcnt(3)
v_cvt_pk_bf16_f32 v40,v24,v25
v_cvt_pk_bf16_f32 v41,v26,v27
v_mfma_f32_16x16x32_bf16 v[20:23],a[68:71],v[160:163],v[20:23]
s_waitcnt vmcnt(9)
ds_read_b128 v[188:191],v2 offset:15360
v_cvt_pk_bf16_f32 v42,v28,v29
v_mfma_f32_16x16x32_bf16 v[20:23],a[76:79],v[164:167],v[20:23]
s_barrier
v_mfma_f32_16x16x32_bf16 v[16:19],a[72:75],v[164:167],v[16:19]
buffer_load_dwordx4 v1,s[36:39],s66 offen offset:3072 sc1 lds
v_mfma_f32_16x16x32_bf16 v[16:19],a[64:67],v[160:163],v[16:19]
s_add_u32 m0,s71,s88
ds_read_b128 v[128:131],v2 offset:16384
v_cvt_pk_bf16_f32 v43,v30,v31
v_mfma_f32_16x16x32_bf16 v[16:19],a[80:83],v[168:171],v[16:19]
s_lshr_b32 s70,s46,5
ds_read_b128 v[132:135],v2 offset:17408
v_add_f32 v32,v24,v25
v_mfma_f32_16x16x32_bf16 v[16:19],a[88:91],v[172:175],v[16:19]
v_mul_f32 v33,v24,v24
v_fma_f32 v33,v25,v25,v33
v_add_f32 v32,v32,v26
v_fma_f32 v33,v26,v26,v33
v_add_f32 v32,v32,v27
v_mfma_f32_16x16x32_bf16 v[20:23],a[92:95],v[172:175],v[20:23]
buffer_load_dwordx4 v10,s[36:39],s66 offen offset:0 sc1 lds
v_mfma_f32_16x16x32_bf16 v[20:23],a[84:87],v[168:171],v[20:23]
s_waitcnt lgkmcnt(2)
ds_read_b128 v[136:139],v2 offset:18432
v_fma_f32 v33,v27,v27,v33
v_mfma_f32_16x16x32_bf16 v[20:23],a[100:103],v[176:179],v[20:23]
buffer_load_dword v11,s[36:39],s72 offen offset:1024 sc1 lds
v_mfma_f32_16x16x32_bf16 v[20:23],a[108:111],v[180:183],v[20:23]
s_mov_b32 m0,s62
ds_read_b128 v[140:143],v2 offset:19456
v_add_f32 v32,v32,v28
v_fma_f32 v33,v28,v28,v33
v_add_f32 v32,v32,v29
v_mfma_f32_16x16x32_bf16 v[16:19],a[104:107],v[180:183],v[16:19]
ds_read_b128 v[144:147],v2 offset:20480
v_fma_f32 v33,v29,v29,v33
v_mfma_f32_16x16x32_bf16 v[16:19],a[96:99],v[176:179],v[16:19]
buffer_store_dwordx4 v[40:43],v3,s[36:39],s73 offen offset:0 sc1
v_add_f32 v32,v32,v30
v_add_f32 v32,v32,v31
v_fma_f32 v33,v30,v30,v33
v_mfma_f32_16x16x32_bf16 v[16:19],a[112:115],v[184:187],v[16:19]
ds_read_b128 v[148:151],v2 offset:21504
v_fma_f32 v33,v31,v31,v33
v_mfma_f32_16x16x32_bf16 v[16:19],a[120:123],v[188:191],v[16:19]
s_nop 0
v_permlane16_swap_b32 v32,v33
v_mfma_f32_16x16x32_bf16 v[20:23],a[124:127],v[188:191],v[20:23]
v_add_f32 v32,v32,v33
v_mov_b32 v33,v32
v_mfma_f32_16x16x32_bf16 v[20:23],a[116:119],v[184:187],v[20:23]
s_waitcnt lgkmcnt(2)
ds_read_b128 v[152:155],v2 offset:22528
v_permlane32_swap_b32 v32,v33
v_add_f32 v32,v32,v33
v_mfma_f32_16x16x32_bf16 v[20:23],a[132:135],v[128:131],v[20:23]
ds_write_b32 v13,v32 offset:0
v_mfma_f32_16x16x32_bf16 v[20:23],a[140:143],v[132:135],v[20:23]
ds_read_b128 v[156:159],v2 offset:23552
v_mfma_f32_16x16x32_bf16 v[16:19],a[136:139],v[132:135],v[16:19]
ds_read_b128 v[160:163],v2 offset:24576
v_mfma_f32_16x16x32_bf16 v[16:19],a[128:131],v[128:131],v[16:19]
v_mfma_f32_16x16x32_bf16 v[16:19],a[144:147],v[136:139],v[16:19]
s_waitcnt vmcnt(12) lgkmcnt(2)
ds_read_b128 v[164:167],v2 offset:25600
v_mfma_f32_16x16x32_bf16 v[16:19],a[152:155],v[140:143],v[16:19]
s_barrier
v_mfma_f32_16x16x32_bf16 v[20:23],a[156:159],v[140:143],v[20:23]
ds_read_b128 v[168:171],v2 offset:26624
ds_read_b32 v15,v7 offset:0
v_mfma_f32_16x16x32_bf16 v[20:23],a[148:151],v[136:139],v[20:23]
s_waitcnt lgkmcnt(4)
v_mfma_f32_16x16x32_bf16 v[20:23],a[164:167],v[144:147],v[20:23]
ds_read_b128 v[172:175],v2 offset:27648
v_mfma_f32_16x16x32_bf16 v[20:23],a[172:175],v[148:151],v[20:23]
buffer_store_dword v5,v4,s[36:39],s70 offen offset:0 sc1
v_mfma_f32_16x16x32_bf16 v[16:19],a[168:171],v[148:151],v[16:19]
s_waitcnt lgkmcnt(1)
ds_read_b128 v[176:179],v2 offset:28672
v_mfma_f32_16x16x32_bf16 v[16:19],a[160:163],v[144:147],v[16:19]
v_mfma_f32_16x16x32_bf16 v[16:19],a[176:179],v[152:155],v[16:19]
ds_read_b128 v[180:183],v2 offset:29696
v_mfma_f32_16x16x32_bf16 v[16:19],a[184:187],v[156:159],v[16:19]
v_cmp_eq_u32 vcc,s60,v15
v_mfma_f32_16x16x32_bf16 v[20:23],a[188:191],v[156:159],v[20:23]
s_cmp_eq_u32 vcc_lo,-1
ds_read_b128 v[184:187],v2 offset:30720
s_cselect_b32 s67,s86,s59
v_mfma_f32_16x16x32_bf16 v[20:23],a[180:183],v[152:155],v[20:23]
s_waitcnt lgkmcnt(3)
buffer_load_dwordx4 v0,s[36:39],s67 offen offset:0 sc1 lds
v_mfma_f32_16x16x32_bf16 v[20:23],a[196:199],v[160:163],v[20:23]
s_waitcnt vmcnt(9)
ds_read_b128 v[188:191],v2 offset:31744
s_xor_b32 s63,s63,65536
v_mfma_f32_16x16x32_bf16 v[20:23],a[204:207],v[164:167],v[20:23]
s_barrier
v_mfma_f32_16x16x32_bf16 v[16:19],a[200:203],v[164:167],v[16:19]
s_add_u32 s53,s52,s55
ds_read_b128 v[128:131],v2 offset:32768
ds_read_b128 v[48:51],v8 offset:0
v_mfma_f32_16x16x32_bf16 v[16:19],a[192:195],v[160:163],v[16:19]
s_cmp_eq_u32 s53,s56
ds_read_b64 v[56:57],v9 offset:1024
s_cselect_b32 s53,s57,s53
v_mfma_f32_16x16x32_bf16 v[16:19],a[208:211],v[168:171],v[16:19]
s_cmp_eq_u32 s53,s58
ds_read_b128 v[132:135],v2 offset:33792
s_cselect_b32 s53,s59,s53
v_mfma_f32_16x16x32_bf16 v[16:19],a[216:219],v[172:175],v[16:19]
buffer_load_dwordx4 v0,s[36:39],s67 offen offset:1024 sc1 lds
v_mfma_f32_16x16x32_bf16 v[20:23],a[220:223],v[172:175],v[20:23]
s_cmp_ge_u32 s66,s59
ds_read_b128 v[136:139],v2 offset:34816
v_mfma_f32_16x16x32_bf16 v[20:23],a[212:215],v[168:171],v[20:23]
s_waitcnt lgkmcnt(5)
v_mfma_f32_16x16x32_bf16 v[20:23],a[228:231],v[176:179],v[20:23]
ds_read_b128 v[140:143],v2 offset:35840
v_mfma_f32_16x16x32_bf16 v[20:23],a[236:239],v[180:183],v[20:23]
s_waitcnt lgkmcnt(3)
buffer_load_dwordx4 v0,s[36:39],s67 offen offset:2048 sc1 lds
v_mfma_f32_16x16x32_bf16 v[16:19],a[232:235],v[180:183],v[16:19]
ds_read_b128 v[144:147],v2 offset:36864
v_mfma_f32_16x16x32_bf16 v[16:19],a[224:227],v[176:179],v[16:19]
v_mfma_f32_16x16x32_bf16 v[16:19],a[240:243],v[184:187],v[16:19]
ds_read_b128 v[148:151],v2 offset:37888
v_mfma_f32_16x16x32_bf16 v[16:19],a[248:251],v[188:191],v[16:19]
buffer_load_dwordx4 v0,s[36:39],s67 offen offset:3072 sc1 lds
v_mfma_f32_16x16x32_bf16 v[20:23],a[252:255],v[188:191],v[20:23]
s_waitcnt lgkmcnt(2)
ds_read_b128 v[152:155],v2 offset:38912
v_mfma_f32_16x16x32_bf16 v[20:23],a[244:247],v[184:187],v[20:23]
s_cbranch_scc1 Lg2_slow1_%=
Lg2_d1w3_%=:
v_mfma_f32_16x16x32_bf16 v[28:31],a[4:7],v[128:131],0
s_add_u32 m0,s62,16384
ds_read_b128 v[156:159],v2 offset:39936
v_lshlrev_b32 v32,16,v48
v_mfma_f32_16x16x32_bf16 v[28:31],a[12:15],v[132:135],v[28:31]
s_lshr_b32 s73,s48,0
v_and_b32 v33,0xffff0000,v48
v_lshlrev_b32 v34,16,v49
v_mfma_f32_16x16x32_bf16 v[24:27],a[8:11],v[132:135],0
s_xor_b32 s87,s63,65536
ds_read_b128 v[160:163],v2 offset:40960
v_and_b32 v35,0xffff0000,v49
v_mfma_f32_16x16x32_bf16 v[24:27],a[0:3],v[128:131],v[24:27]
s_cmp_ge_u32 s67,s59
v_lshlrev_b32 v36,16,v50
v_and_b32 v37,0xffff0000,v50
v_mfma_f32_16x16x32_bf16 v[24:27],a[16:19],v[136:139],v[24:27]
buffer_load_dwordx4 v1,s[36:39],s67 offen offset:0 sc1 lds
v_mfma_f32_16x16x32_bf16 v[24:27],a[24:27],v[140:143],v[24:27]
s_cselect_b32 s86,s50,s51
ds_read_b128 v[164:167],v2 offset:41984
v_lshlrev_b32 v38,16,v51
v_and_b32 v39,0xffff0000,v51
v_fma_f32 v32,v32,v57,v56
v_fma_f32 v33,v33,v57,v56
v_mfma_f32_16x16x32_bf16 v[28:31],a[28:31],v[140:143],v[28:31]
s_cselect_b32 s87,s87,s64
ds_read_b128 v[168:171],v2 offset:43008
v_fma_f32 v34,v34,v57,v56
v_mfma_f32_16x16x32_bf16 v[28:31],a[20:23],v[136:139],v[28:31]
s_waitcnt lgkmcnt(3)
v_fma_f32 v35,v35,v57,v56
v_fma_f32 v36,v36,v57,v56
v_mfma_f32_16x16x32_bf16 v[28:31],a[36:39],v[144:147],v[28:31]
buffer_load_dwordx4 v1,s[36:39],s67 offen offset:1024 sc1 lds
v_mfma_f32_16x16x32_bf16 v[28:31],a[44:47],v[148:151],v[28:31]
s_lshr_b32 s72,s67,6
ds_read_b128 v[172:175],v2 offset:44032
v_fma_f32 v37,v37,v57,v56
v_fma_f32 v38,v38,v57,v56
v_fma_f32 v39,v39,v57,v56
v_add_f32 v16,v16,v32
v_mfma_f32_16x16x32_bf16 v[24:27],a[40:43],v[148:151],v[24:27]
s_lshr_b32 s71,s62,2
ds_read_b128 v[176:179],v2 offset:45056
v_add_f32 v17,v17,v33
v_mfma_f32_16x16x32_bf16 v[24:27],a[32:35],v[144:147],v[24:27]
s_mov_b32 s62,s87
v_add_f32 v18,v18,v34
v_add_f32 v19,v19,v35
v_mfma_f32_16x16x32_bf16 v[24:27],a[48:51],v[152:155],v[24:27]
buffer_load_dwordx4 v1,s[36:39],s67 offen offset:2048 sc1 lds
v_mfma_f32_16x16x32_bf16 v[24:27],a[56:59],v[156:159],v[24:27]
s_mov_b64 s[44:45],s[46:47]
ds_read_b128 v[180:183],v2 offset:46080
v_add_f32 v20,v20,v36
v_add_f32 v21,v21,v37)"
R"(
v_add_f32 v22,v22,v38
v_mfma_f32_16x16x32_bf16 v[28:31],a[60:63],v[156:159],v[28:31]
ds_read_b128 v[184:187],v2 offset:47104
v_add_f32 v23,v23,v39
v_mfma_f32_16x16x32_bf16 v[28:31],a[52:55],v[152:155],v[28:31]
s_waitcnt lgkmcnt(3)
v_cvt_pk_bf16_f32 v40,v16,v17
v_cvt_pk_bf16_f32 v41,v18,v19
v_mfma_f32_16x16x32_bf16 v[28:31],a[68:71],v[160:163],v[28:31]
s_waitcnt vmcnt(9)
ds_read_b128 v[188:191],v2 offset:48128
v_cvt_pk_bf16_f32 v42,v20,v21
v_mfma_f32_16x16x32_bf16 v[28:31],a[76:79],v[164:167],v[28:31]
s_barrier
v_mfma_f32_16x16x32_bf16 v[24:27],a[72:75],v[164:167],v[24:27]
buffer_load_dwordx4 v1,s[36:39],s67 offen offset:3072 sc1 lds
v_mfma_f32_16x16x32_bf16 v[24:27],a[64:67],v[160:163],v[24:27]
s_add_u32 m0,s71,s88
ds_read_b128 v[128:131],v2 offset:49152
v_cvt_pk_bf16_f32 v43,v22,v23
v_mfma_f32_16x16x32_bf16 v[24:27],a[80:83],v[168:171],v[24:27]
s_lshr_b32 s70,s47,5
ds_read_b128 v[132:135],v2 offset:50176
v_add_f32 v32,v16,v17
v_mfma_f32_16x16x32_bf16 v[24:27],a[88:91],v[172:175],v[24:27]
s_mov_b64 s[46:47],s[48:49]
v_mul_f32 v33,v16,v16
v_fma_f32 v33,v17,v17,v33
v_add_f32 v32,v32,v18
v_fma_f32 v33,v18,v18,v33
v_add_f32 v32,v32,v19
v_mfma_f32_16x16x32_bf16 v[28:31],a[92:95],v[172:175],v[28:31]
buffer_load_dwordx4 v10,s[36:39],s67 offen offset:0 sc1 lds
v_mfma_f32_16x16x32_bf16 v[28:31],a[84:87],v[168:171],v[28:31]
s_waitcnt lgkmcnt(2)
ds_read_b128 v[136:139],v2 offset:51200
v_fma_f32 v33,v19,v19,v33
v_mfma_f32_16x16x32_bf16 v[28:31],a[100:103],v[176:179],v[28:31]
buffer_load_dword v11,s[36:39],s72 offen offset:1024 sc1 lds
v_mfma_f32_16x16x32_bf16 v[28:31],a[108:111],v[180:183],v[28:31]
s_mov_b32 m0,s62
ds_read_b128 v[140:143],v2 offset:52224
v_add_f32 v32,v32,v20
v_fma_f32 v33,v20,v20,v33
v_add_f32 v32,v32,v21
v_mfma_f32_16x16x32_bf16 v[24:27],a[104:107],v[180:183],v[24:27]
s_mov_b64 s[48:49],s[50:51]
ds_read_b128 v[144:147],v2 offset:53248
v_fma_f32 v33,v21,v21,v33
v_mfma_f32_16x16x32_bf16 v[24:27],a[96:99],v[176:179],v[24:27]
buffer_store_dwordx4 v[40:43],v3,s[36:39],s73 offen offset:0 sc1
v_add_f32 v32,v32,v22
v_add_f32 v32,v32,v23
v_fma_f32 v33,v22,v22,v33
v_mfma_f32_16x16x32_bf16 v[24:27],a[112:115],v[184:187],v[24:27]
s_mov_b64 s[50:51],s[52:53]
ds_read_b128 v[148:151],v2 offset:54272
v_fma_f32 v33,v23,v23,v33
v_mfma_f32_16x16x32_bf16 v[24:27],a[120:123],v[188:191],v[24:27]
s_nop 0
v_permlane16_swap_b32 v32,v33
v_mfma_f32_16x16x32_bf16 v[28:31],a[124:127],v[188:191],v[28:31]
v_add_f32 v32,v32,v33
v_mov_b32 v33,v32
v_mfma_f32_16x16x32_bf16 v[28:31],a[116:119],v[184:187],v[28:31]
s_waitcnt lgkmcnt(2)
ds_read_b128 v[152:155],v2 offset:55296
v_permlane32_swap_b32 v32,v33
v_add_f32 v32,v32,v33
v_mfma_f32_16x16x32_bf16 v[28:31],a[132:135],v[128:131],v[28:31]
ds_write_b32 v13,v32 offset:2048
v_mfma_f32_16x16x32_bf16 v[28:31],a[140:143],v[132:135],v[28:31]
ds_read_b128 v[156:159],v2 offset:56320
v_mfma_f32_16x16x32_bf16 v[24:27],a[136:139],v[132:135],v[24:27]
ds_read_b128 v[160:163],v2 offset:57344
v_mfma_f32_16x16x32_bf16 v[24:27],a[128:131],v[128:131],v[24:27]
v_mfma_f32_16x16x32_bf16 v[24:27],a[144:147],v[136:139],v[24:27]
s_waitcnt vmcnt(12) lgkmcnt(2)
ds_read_b128 v[164:167],v2 offset:58368
v_mfma_f32_16x16x32_bf16 v[24:27],a[152:155],v[140:143],v[24:27]
s_barrier
v_mfma_f32_16x16x32_bf16 v[28:31],a[156:159],v[140:143],v[28:31]
ds_read_b128 v[168:171],v2 offset:59392
ds_read_b32 v15,v7 offset:256
v_mfma_f32_16x16x32_bf16 v[28:31],a[148:151],v[136:139],v[28:31]
s_waitcnt lgkmcnt(4)
v_mfma_f32_16x16x32_bf16 v[28:31],a[164:167],v[144:147],v[28:31]
ds_read_b128 v[172:175],v2 offset:60416
v_mfma_f32_16x16x32_bf16 v[28:31],a[172:175],v[148:151],v[28:31]
buffer_store_dword v5,v4,s[36:39],s70 offen offset:0 sc1
v_mfma_f32_16x16x32_bf16 v[24:27],a[168:171],v[148:151],v[24:27]
s_waitcnt lgkmcnt(1)
ds_read_b128 v[176:179],v2 offset:61440
v_mfma_f32_16x16x32_bf16 v[24:27],a[160:163],v[144:147],v[24:27]
v_mfma_f32_16x16x32_bf16 v[24:27],a[176:179],v[152:155],v[24:27]
ds_read_b128 v[180:183],v2 offset:62464
v_mfma_f32_16x16x32_bf16 v[24:27],a[184:187],v[156:159],v[24:27]
v_cmp_eq_u32 vcc,s60,v15
v_mfma_f32_16x16x32_bf16 v[28:31],a[188:191],v[156:159],v[28:31]
s_cmp_eq_u32 vcc_lo,-1
ds_read_b128 v[184:187],v2 offset:63488
s_cselect_b32 s66,s86,s59
v_mfma_f32_16x16x32_bf16 v[28:31],a[180:183],v[152:155],v[28:31]
s_waitcnt lgkmcnt(3)
buffer_load_dwordx4 v0,s[36:39],s66 offen offset:0 sc1 lds
v_mfma_f32_16x16x32_bf16 v[28:31],a[196:199],v[160:163],v[28:31]
s_waitcnt vmcnt(9)
ds_read_b128 v[188:191],v2 offset:64512
v_xor_b32 v2,65536,v2
v_mfma_f32_16x16x32_bf16 v[28:31],a[204:207],v[164:167],v[28:31]
s_barrier
v_mfma_f32_16x16x32_bf16 v[24:27],a[200:203],v[164:167],v[24:27]
s_xor_b32 s64,s64,65536
ds_read_b128 v[128:131],v2 offset:0
ds_read_b128 v[52:55],v8 offset:8192
v_mfma_f32_16x16x32_bf16 v[24:27],a[192:195],v[160:163],v[24:27]
s_add_u32 s74,s53,s55
ds_read_b64 v[58:59],v9 offset:9216
v_xor_b32 v8,16384,v8
v_mfma_f32_16x16x32_bf16 v[24:27],a[208:211],v[168:171],v[24:27]
s_cmp_eq_u32 s74,s56
ds_read_b128 v[132:135],v2 offset:1024
v_xor_b32 v9,16384,v9
v_mfma_f32_16x16x32_bf16 v[24:27],a[216:219],v[172:175],v[24:27]
buffer_load_dwordx4 v0,s[36:39],s66 offen offset:1024 sc1 lds
v_mfma_f32_16x16x32_bf16 v[28:31],a[220:223],v[172:175],v[28:31]
s_cselect_b32 s74,s57,s74
ds_read_b128 v[136:139],v2 offset:2048
s_cmp_eq_u32 s74,s58
v_mfma_f32_16x16x32_bf16 v[28:31],a[212:215],v[168:171],v[28:31]
s_waitcnt lgkmcnt(5)
s_cselect_b32 s74,s59,s74
s_mov_b32 s52,s74
v_mfma_f32_16x16x32_bf16 v[28:31],a[228:231],v[176:179],v[28:31]
s_cmp_lt_u32 s67,s59
ds_read_b128 v[140:143],v2 offset:3072
v_mfma_f32_16x16x32_bf16 v[28:31],a[236:239],v[180:183],v[28:31]
s_waitcnt lgkmcnt(3)
buffer_load_dwordx4 v0,s[36:39],s66 offen offset:2048 sc1 lds
v_mfma_f32_16x16x32_bf16 v[24:27],a[232:235],v[180:183],v[24:27]
ds_read_b128 v[144:147],v2 offset:4096
v_mfma_f32_16x16x32_bf16 v[24:27],a[224:227],v[176:179],v[24:27]
v_mfma_f32_16x16x32_bf16 v[24:27],a[240:243],v[184:187],v[24:27]
ds_read_b128 v[148:151],v2 offset:5120
v_mfma_f32_16x16x32_bf16 v[24:27],a[248:251],v[188:191],v[24:27]
buffer_load_dwordx4 v0,s[36:39],s66 offen offset:3072 sc1 lds
v_mfma_f32_16x16x32_bf16 v[28:31],a[252:255],v[188:191],v[28:31]
s_waitcnt lgkmcnt(2)
ds_read_b128 v[152:155],v2 offset:6144
v_mfma_f32_16x16x32_bf16 v[28:31],a[244:247],v[184:187],v[28:31]
s_cbranch_scc1 Lg2_d0w3_%=
s_branch Lg2_slow0_%=
Lg2_slow0_%=:
s_cmp_lt_u32 s66,s59
s_cbranch_scc1 Lg2_gs0_%=
Lg2_slowb0_%=:
s_cmp_eq_u32 s54,1
s_cselect_b32 s68,s49,s48
s_lshr_b32 s68,s68,5
s_add_u32 m0,s78,8192
s_nop 0
buffer_load_dword v6,s[36:39],s68 offen offset:0 sc1 lds
s_lshr_b32 s73,s47,0
s_lshr_b32 s85,s47,5
v_lshlrev_b32 v32,16,v52
v_and_b32 v33,0xffff0000,v52
v_lshlrev_b32 v34,16,v53
v_and_b32 v35,0xffff0000,v53
v_lshlrev_b32 v36,16,v54
v_and_b32 v37,0xffff0000,v54
v_lshlrev_b32 v38,16,v55
v_and_b32 v39,0xffff0000,v55
v_fma_f32 v32,v32,v59,v58
v_fma_f32 v33,v33,v59,v58
v_fma_f32 v34,v34,v59,v58
v_fma_f32 v35,v35,v59,v58
v_fma_f32 v36,v36,v59,v58
v_fma_f32 v37,v37,v59,v58
v_fma_f32 v38,v38,v59,v58
v_fma_f32 v39,v39,v59,v58
v_add_f32 v24,v24,v32
v_add_f32 v25,v25,v33
v_add_f32 v26,v26,v34
v_add_f32 v27,v27,v35
v_add_f32 v28,v28,v36
v_add_f32 v29,v29,v37
v_add_f32 v30,v30,v38
v_add_f32 v31,v31,v39
v_cvt_pk_bf16_f32 v40,v24,v25
v_cvt_pk_bf16_f32 v41,v26,v27
v_cvt_pk_bf16_f32 v42,v28,v29
v_cvt_pk_bf16_f32 v43,v30,v31
s_add_u32 m0,s78,8448
s_nop 0
buffer_load_dword v6,s[36:39],s68 offen offset:0 sc1 lds
buffer_store_dwordx4 v[40:43],v3,s[36:39],s73 offen offset:0 sc1
v_add_f32 v32,v24,v25
v_mul_f32 v33,v24,v24
v_fma_f32 v33,v25,v25,v33
v_add_f32 v32,v32,v26)"
R"(
v_fma_f32 v33,v26,v26,v33
v_add_f32 v32,v32,v27
v_fma_f32 v33,v27,v27,v33
v_add_f32 v32,v32,v28
v_fma_f32 v33,v28,v28,v33
v_add_f32 v32,v32,v29
v_fma_f32 v33,v29,v29,v33
v_add_f32 v32,v32,v30
v_fma_f32 v33,v30,v30,v33
v_add_f32 v32,v32,v31
v_fma_f32 v33,v31,v31,v33
s_nop 1
v_permlane16_swap_b32 v32,v33
v_add_f32 v32,v32,v33
v_mov_b32 v33,v32
s_nop 1
v_permlane32_swap_b32 v32,v33
v_add_f32 v32,v32,v33
ds_write_b32 v13,v32 offset:0
s_waitcnt lgkmcnt(0)
s_barrier
ds_read_b128 v[44:47],v14 offset:0
s_waitcnt lgkmcnt(0)
v_add_f32 v44,v44,v45
v_add_f32 v46,v46,v47
v_add_f32 v44,v44,v46
buffer_store_dword v44,v12,s[36:39],s85 offen offset:0 sc1
s_cmp_ge_u32 s48,s59
s_cbranch_scc1 Lg2_done0_%=
s_add_u32 m0,s78,16384
s_nop 0
buffer_load_dword v6,s[36:39],s68 offen offset:0 sc1 lds
s_waitcnt vmcnt(4)
s_barrier
ds_read_b32 v15,v7 offset:8192
s_lshr_b32 s70,s46,5
buffer_store_dword v5,v4,s[36:39],s70 offen offset:0 sc1
s_mov_b32 s46,s59
s_mov_b32 s69,18432
s_waitcnt lgkmcnt(0)
v_cmp_eq_u32 vcc,s60,v15
s_nop 0
s_cmp_eq_u32 vcc_lo,-1
s_cbranch_scc1 Lg2_go0_%=
s_add_u32 m0,s78,16640
s_nop 0
buffer_load_dword v6,s[36:39],s68 offen offset:0 sc1 lds
s_waitcnt vmcnt(5)
s_barrier
ds_read_b32 v15,v7 offset:8448
s_mov_b32 s69,18688
s_waitcnt lgkmcnt(0)
v_cmp_eq_u32 vcc,s60,v15
s_nop 0
s_cmp_eq_u32 vcc_lo,-1
s_cbranch_scc1 Lg2_go0_%=
s_add_u32 m0,s78,8192
s_nop 0
buffer_load_dword v6,s[36:39],s68 offen offset:0 sc1 lds
s_waitcnt vmcnt(3)
s_barrier
ds_read_b32 v15,v7 offset:16384
s_lshr_b32 s70,s47,5
buffer_store_dword v5,v4,s[36:39],s70 offen offset:0 sc1
s_mov_b32 s47,s59
s_mov_b32 s69,10240
s_waitcnt lgkmcnt(0)
v_cmp_eq_u32 vcc,s60,v15
s_nop 0
s_cmp_eq_u32 vcc_lo,-1
s_cbranch_scc1 Lg2_go0_%=
s_add_u32 m0,s78,8448
s_nop 0
buffer_load_dword v6,s[36:39],s68 offen offset:0 sc1 lds
s_waitcnt vmcnt(3)
s_barrier
ds_read_b32 v15,v7 offset:16640
s_mov_b32 s69,10496
s_waitcnt lgkmcnt(0)
v_cmp_eq_u32 vcc,s60,v15
s_nop 0
s_cmp_eq_u32 vcc_lo,-1
s_cbranch_scc1 Lg2_go0_%=
Lg2_poll0_%=:
s_add_u32 m0,s78,16384
s_nop 0
buffer_load_dword v6,s[36:39],s68 offen offset:0 sc1 lds
s_waitcnt vmcnt(2)
s_barrier
ds_read_b32 v15,v7 offset:8192
s_mov_b32 s69,18432
s_waitcnt lgkmcnt(0)
v_cmp_eq_u32 vcc,s60,v15
s_nop 0
s_cmp_eq_u32 vcc_lo,-1
s_cbranch_scc1 Lg2_go0_%=
s_sleep 4
s_add_u32 m0,s78,16640
s_nop 0
buffer_load_dword v6,s[36:39],s68 offen offset:0 sc1 lds
s_waitcnt vmcnt(2)
s_barrier
ds_read_b32 v15,v7 offset:8448
s_mov_b32 s69,18688
s_waitcnt lgkmcnt(0)
v_cmp_eq_u32 vcc,s60,v15
s_nop 0
s_cmp_eq_u32 vcc_lo,-1
s_cbranch_scc1 Lg2_go0_%=
s_sleep 4
s_add_u32 m0,s78,8192
s_nop 0
buffer_load_dword v6,s[36:39],s68 offen offset:0 sc1 lds
s_waitcnt vmcnt(2)
s_barrier
ds_read_b32 v15,v7 offset:16384
s_mov_b32 s69,10240
s_waitcnt lgkmcnt(0)
v_cmp_eq_u32 vcc,s60,v15
s_nop 0
s_cmp_eq_u32 vcc_lo,-1
s_cbranch_scc1 Lg2_go0_%=
s_sleep 4
s_add_u32 m0,s78,8448
s_nop 0
buffer_load_dword v6,s[36:39],s68 offen offset:0 sc1 lds
s_waitcnt vmcnt(2)
s_barrier
ds_read_b32 v15,v7 offset:16640
s_mov_b32 s69,10496
s_waitcnt lgkmcnt(0)
v_cmp_eq_u32 vcc,s60,v15
s_nop 0
s_cmp_eq_u32 vcc_lo,-1
s_cbranch_scc1 Lg2_go0_%=
s_sleep 4
s_add_u32 s79,s79,1
s_cmp_lt_u32 s79,20000
s_cbranch_scc1 Lg2_poll0_%=
Lg2_go0_%=:
s_xor_b32 s62,s63,65536
s_mov_b32 m0,s62
s_nop 0
buffer_load_dwordx4 v0,s[36:39],s48 offen offset:0 sc1 lds
buffer_load_dwordx4 v0,s[36:39],s48 offen offset:1024 sc1 lds
buffer_load_dwordx4 v0,s[36:39],s48 offen offset:2048 sc1 lds
buffer_load_dwordx4 v0,s[36:39],s48 offen offset:3072 sc1 lds
s_add_u32 m0,s62,16384
s_nop 0
buffer_load_dwordx4 v1,s[36:39],s48 offen offset:0 sc1 lds
buffer_load_dwordx4 v1,s[36:39],s48 offen offset:1024 sc1 lds
buffer_load_dwordx4 v1,s[36:39],s48 offen offset:2048 sc1 lds
buffer_load_dwordx4 v1,s[36:39],s48 offen offset:3072 sc1 lds
s_lshr_b32 s72,s48,6
s_lshr_b32 s71,s62,2
s_add_u32 m0,s71,s88
s_nop 0
buffer_load_dwordx4 v10,s[36:39],s48 offen offset:0 sc1 lds
buffer_load_dword v11,s[36:39],s72 offen offset:1024 sc1 lds
s_cmp_eq_u32 vcc_hi,-1
s_cselect_b32 s66,s49,s59
s_xor_b32 s62,s64,65536
s_cmp_ge_u32 s66,s59
s_cselect_b32 s68,s49,s50
s_lshr_b32 s68,s68,5
s_add_u32 m0,s78,0
s_nop 0
buffer_load_dword v6,s[36:39],s68 offen offset:0 sc1 lds
s_mov_b32 m0,s62
s_nop 0
buffer_load_dwordx4 v0,s[36:39],s66 offen offset:0 sc1 lds
buffer_load_dwordx4 v0,s[36:39],s66 offen offset:1024 sc1 lds
buffer_load_dwordx4 v0,s[36:39],s66 offen offset:2048 sc1 lds
buffer_load_dwordx4 v0,s[36:39],s66 offen offset:3072 sc1 lds
s_waitcnt vmcnt(11)
s_barrier
ds_read_b128 v[128:131],v2 offset:0
ds_read_b128 v[132:135],v2 offset:1024
ds_read_b128 v[136:139],v2 offset:2048
ds_read_b128 v[140:143],v2 offset:3072
ds_read_b128 v[144:147],v2 offset:4096
ds_read_b128 v[148:151],v2 offset:5120
ds_read_b128 v[152:155],v2 offset:6144
s_lshr_b32 s70,s46,5
buffer_store_dword v5,v4,s[36:39],s70 offen offset:0 sc1
s_mov_b32 s46,s59
s_lshr_b32 s70,s47,5
buffer_store_dword v5,v4,s[36:39],s70 offen offset:0 sc1
s_mov_b32 s47,s59
s_lshr_b32 s70,s59,5
buffer_store_dword v5,v4,s[36:39],s70 offen offset:0 sc1
s_waitcnt lgkmcnt(3)
s_setpc_b64 s[90:91]
Lg2_gs0_%=:
s_cmp_eq_u32 s54,1
s_cselect_b32 s68,s49,s48
s_lshr_b32 s68,s68,5
s_add_u32 m0,s78,8192
s_nop 0
buffer_load_dword v6,s[36:39],s68 offen offset:0 sc1 lds)"
R"(
s_add_u32 m0,s62,16384
s_nop 0
buffer_load_dwordx4 v1,s[36:39],s66 offen offset:0 sc1 lds
buffer_load_dwordx4 v1,s[36:39],s66 offen offset:1024 sc1 lds
buffer_load_dwordx4 v1,s[36:39],s66 offen offset:2048 sc1 lds
buffer_load_dwordx4 v1,s[36:39],s66 offen offset:3072 sc1 lds
s_lshr_b32 s72,s66,6
s_lshr_b32 s71,s62,2
s_add_u32 m0,s71,s88
s_nop 0
buffer_load_dwordx4 v10,s[36:39],s66 offen offset:0 sc1 lds
buffer_load_dword v11,s[36:39],s72 offen offset:1024 sc1 lds
s_waitcnt vmcnt(6)
s_barrier
ds_read_b32 v15,v7 offset:8192
ds_read_b128 v[128:131],v2 offset:0
ds_read_b128 v[132:135],v2 offset:1024
ds_read_b128 v[136:139],v2 offset:2048
ds_read_b128 v[140:143],v2 offset:3072
ds_read_b128 v[144:147],v2 offset:4096
ds_read_b128 v[148:151],v2 offset:5120
ds_read_b128 v[152:155],v2 offset:6144
s_lshr_b32 s70,s46,5
buffer_store_dword v5,v4,s[36:39],s70 offen offset:0 sc1
s_mov_b32 s46,s59
s_waitcnt lgkmcnt(7)
v_cmp_eq_u32 vcc,s60,v15
s_nop 0
s_cmp_eq_u32 vcc_hi,-1
s_cselect_b32 s66,s49,s59
s_xor_b32 s62,s64,65536
s_cmp_ge_u32 s66,s59
s_cselect_b32 s68,s49,s50
s_lshr_b32 s68,s68,5
s_add_u32 m0,s78,0
s_nop 0
buffer_load_dword v6,s[36:39],s68 offen offset:0 sc1 lds
s_mov_b32 m0,s62
s_nop 0
buffer_load_dwordx4 v0,s[36:39],s66 offen offset:0 sc1 lds
buffer_load_dwordx4 v0,s[36:39],s66 offen offset:1024 sc1 lds
buffer_load_dwordx4 v0,s[36:39],s66 offen offset:2048 sc1 lds
buffer_load_dwordx4 v0,s[36:39],s66 offen offset:3072 sc1 lds
s_lshr_b32 s70,s59,5
buffer_store_dword v5,v4,s[36:39],s70 offen offset:0 sc1
s_lshr_b32 s70,s59,5
buffer_store_dword v5,v4,s[36:39],s70 offen offset:0 sc1
s_lshr_b32 s70,s59,5
buffer_store_dword v5,v4,s[36:39],s70 offen offset:0 sc1
s_waitcnt lgkmcnt(3)
s_setpc_b64 s[90:91]
Lg2_done0_%=:
s_waitcnt vmcnt(0)
s_barrier
s_lshr_b32 s70,s46,5
buffer_store_dword v5,v4,s[36:39],s70 offen offset:0 sc1
s_lshr_b32 s70,s47,5
buffer_store_dword v5,v4,s[36:39],s70 offen offset:0 sc1
s_branch Lg2_done_%=
Lg2_slow1_%=:
s_cmp_lt_u32 s67,s59
s_cbranch_scc1 Lg2_gs1_%=
Lg2_slowb1_%=:
s_cmp_eq_u32 s54,1
s_cselect_b32 s68,s50,s49
s_lshr_b32 s68,s68,5
s_add_u32 m0,s78,8192
s_nop 0
buffer_load_dword v6,s[36:39],s68 offen offset:0 sc1 lds
s_lshr_b32 s73,s48,0
s_lshr_b32 s85,s48,5
v_lshlrev_b32 v32,16,v48
v_and_b32 v33,0xffff0000,v48
v_lshlrev_b32 v34,16,v49
v_and_b32 v35,0xffff0000,v49
v_lshlrev_b32 v36,16,v50
v_and_b32 v37,0xffff0000,v50
v_lshlrev_b32 v38,16,v51
v_and_b32 v39,0xffff0000,v51
v_fma_f32 v32,v32,v57,v56
v_fma_f32 v33,v33,v57,v56
v_fma_f32 v34,v34,v57,v56
v_fma_f32 v35,v35,v57,v56
v_fma_f32 v36,v36,v57,v56
v_fma_f32 v37,v37,v57,v56
v_fma_f32 v38,v38,v57,v56
v_fma_f32 v39,v39,v57,v56
v_add_f32 v16,v16,v32
v_add_f32 v17,v17,v33
v_add_f32 v18,v18,v34
v_add_f32 v19,v19,v35
v_add_f32 v20,v20,v36
v_add_f32 v21,v21,v37
v_add_f32 v22,v22,v38
v_add_f32 v23,v23,v39
v_cvt_pk_bf16_f32 v40,v16,v17
v_cvt_pk_bf16_f32 v41,v18,v19
v_cvt_pk_bf16_f32 v42,v20,v21
v_cvt_pk_bf16_f32 v43,v22,v23
s_add_u32 m0,s78,8448
s_nop 0
buffer_load_dword v6,s[36:39],s68 offen offset:0 sc1 lds
buffer_store_dwordx4 v[40:43],v3,s[36:39],s73 offen offset:0 sc1
v_add_f32 v32,v16,v17
v_mul_f32 v33,v16,v16
v_fma_f32 v33,v17,v17,v33
v_add_f32 v32,v32,v18
v_fma_f32 v33,v18,v18,v33
v_add_f32 v32,v32,v19
v_fma_f32 v33,v19,v19,v33
v_add_f32 v32,v32,v20
v_fma_f32 v33,v20,v20,v33
v_add_f32 v32,v32,v21
v_fma_f32 v33,v21,v21,v33
v_add_f32 v32,v32,v22
v_fma_f32 v33,v22,v22,v33
v_add_f32 v32,v32,v23
v_fma_f32 v33,v23,v23,v33
s_nop 1
v_permlane16_swap_b32 v32,v33
v_add_f32 v32,v32,v33
v_mov_b32 v33,v32
s_nop 1
v_permlane32_swap_b32 v32,v33
v_add_f32 v32,v32,v33
ds_write_b32 v13,v32 offset:2048
s_waitcnt lgkmcnt(0)
s_barrier
ds_read_b128 v[44:47],v14 offset:2048
s_waitcnt lgkmcnt(0)
v_add_f32 v44,v44,v45
v_add_f32 v46,v46,v47
v_add_f32 v44,v44,v46
buffer_store_dword v44,v12,s[36:39],s85 offen offset:0 sc1
s_cmp_ge_u32 s49,s59
s_cbranch_scc1 Lg2_done1_%=
s_add_u32 m0,s78,16384
s_nop 0
buffer_load_dword v6,s[36:39],s68 offen offset:0 sc1 lds
s_waitcnt vmcnt(4)
s_barrier
ds_read_b32 v15,v7 offset:8192
s_lshr_b32 s70,s47,5
buffer_store_dword v5,v4,s[36:39],s70 offen offset:0 sc1
s_mov_b32 s47,s59
s_mov_b32 s69,18432
s_waitcnt lgkmcnt(0)
v_cmp_eq_u32 vcc,s60,v15
s_nop 0
s_cmp_eq_u32 vcc_lo,-1
s_cbranch_scc1 Lg2_go1_%=
s_add_u32 m0,s78,16640
s_nop 0
buffer_load_dword v6,s[36:39],s68 offen offset:0 sc1 lds
s_waitcnt vmcnt(5)
s_barrier
ds_read_b32 v15,v7 offset:8448
s_mov_b32 s69,18688
s_waitcnt lgkmcnt(0)
v_cmp_eq_u32 vcc,s60,v15
s_nop 0
s_cmp_eq_u32 vcc_lo,-1
s_cbranch_scc1 Lg2_go1_%=
s_add_u32 m0,s78,8192
s_nop 0
buffer_load_dword v6,s[36:39],s68 offen offset:0 sc1 lds
s_waitcnt vmcnt(3)
s_barrier
ds_read_b32 v15,v7 offset:16384
s_lshr_b32 s70,s48,5
buffer_store_dword v5,v4,s[36:39],s70 offen offset:0 sc1
s_mov_b32 s48,s59
s_mov_b32 s69,10240
s_waitcnt lgkmcnt(0)
v_cmp_eq_u32 vcc,s60,v15
s_nop 0
s_cmp_eq_u32 vcc_lo,-1
s_cbranch_scc1 Lg2_go1_%=
s_add_u32 m0,s78,8448
s_nop 0
buffer_load_dword v6,s[36:39],s68 offen offset:0 sc1 lds
s_waitcnt vmcnt(3)
s_barrier
ds_read_b32 v15,v7 offset:16640
s_mov_b32 s69,10496
s_waitcnt lgkmcnt(0)
v_cmp_eq_u32 vcc,s60,v15
s_nop 0
s_cmp_eq_u32 vcc_lo,-1
s_cbranch_scc1 Lg2_go1_%=
Lg2_poll1_%=:
s_add_u32 m0,s78,16384
s_nop 0
buffer_load_dword v6,s[36:39],s68 offen offset:0 sc1 lds
s_waitcnt vmcnt(2)
s_barrier
ds_read_b32 v15,v7 offset:8192
s_mov_b32 s69,18432
s_waitcnt lgkmcnt(0)
v_cmp_eq_u32 vcc,s60,v15)"
R"(
s_nop 0
s_cmp_eq_u32 vcc_lo,-1
s_cbranch_scc1 Lg2_go1_%=
s_sleep 4
s_add_u32 m0,s78,16640
s_nop 0
buffer_load_dword v6,s[36:39],s68 offen offset:0 sc1 lds
s_waitcnt vmcnt(2)
s_barrier
ds_read_b32 v15,v7 offset:8448
s_mov_b32 s69,18688
s_waitcnt lgkmcnt(0)
v_cmp_eq_u32 vcc,s60,v15
s_nop 0
s_cmp_eq_u32 vcc_lo,-1
s_cbranch_scc1 Lg2_go1_%=
s_sleep 4
s_add_u32 m0,s78,8192
s_nop 0
buffer_load_dword v6,s[36:39],s68 offen offset:0 sc1 lds
s_waitcnt vmcnt(2)
s_barrier
ds_read_b32 v15,v7 offset:16384
s_mov_b32 s69,10240
s_waitcnt lgkmcnt(0)
v_cmp_eq_u32 vcc,s60,v15
s_nop 0
s_cmp_eq_u32 vcc_lo,-1
s_cbranch_scc1 Lg2_go1_%=
s_sleep 4
s_add_u32 m0,s78,8448
s_nop 0
buffer_load_dword v6,s[36:39],s68 offen offset:0 sc1 lds
s_waitcnt vmcnt(2)
s_barrier
ds_read_b32 v15,v7 offset:16640
s_mov_b32 s69,10496
s_waitcnt lgkmcnt(0)
v_cmp_eq_u32 vcc,s60,v15
s_nop 0
s_cmp_eq_u32 vcc_lo,-1
s_cbranch_scc1 Lg2_go1_%=
s_sleep 4
s_add_u32 s79,s79,1
s_cmp_lt_u32 s79,20000
s_cbranch_scc1 Lg2_poll1_%=
Lg2_go1_%=:
s_xor_b32 s62,s64,65536
s_mov_b32 m0,s62
s_nop 0
buffer_load_dwordx4 v0,s[36:39],s49 offen offset:0 sc1 lds
buffer_load_dwordx4 v0,s[36:39],s49 offen offset:1024 sc1 lds
buffer_load_dwordx4 v0,s[36:39],s49 offen offset:2048 sc1 lds
buffer_load_dwordx4 v0,s[36:39],s49 offen offset:3072 sc1 lds
s_add_u32 m0,s62,16384
s_nop 0
buffer_load_dwordx4 v1,s[36:39],s49 offen offset:0 sc1 lds
buffer_load_dwordx4 v1,s[36:39],s49 offen offset:1024 sc1 lds
buffer_load_dwordx4 v1,s[36:39],s49 offen offset:2048 sc1 lds
buffer_load_dwordx4 v1,s[36:39],s49 offen offset:3072 sc1 lds
s_lshr_b32 s72,s49,6
s_lshr_b32 s71,s62,2
s_add_u32 m0,s71,s88
s_nop 0
buffer_load_dwordx4 v10,s[36:39],s49 offen offset:0 sc1 lds
buffer_load_dword v11,s[36:39],s72 offen offset:1024 sc1 lds
s_cmp_eq_u32 vcc_hi,-1
s_cselect_b32 s67,s50,s59
s_xor_b32 s62,s63,65536
s_cmp_ge_u32 s67,s59
s_cselect_b32 s68,s50,s51
s_lshr_b32 s68,s68,5
s_add_u32 m0,s78,256
s_nop 0
buffer_load_dword v6,s[36:39],s68 offen offset:0 sc1 lds
s_mov_b32 m0,s62
s_nop 0
buffer_load_dwordx4 v0,s[36:39],s67 offen offset:0 sc1 lds
buffer_load_dwordx4 v0,s[36:39],s67 offen offset:1024 sc1 lds
buffer_load_dwordx4 v0,s[36:39],s67 offen offset:2048 sc1 lds
buffer_load_dwordx4 v0,s[36:39],s67 offen offset:3072 sc1 lds
s_waitcnt vmcnt(11)
s_barrier
ds_read_b128 v[128:131],v2 offset:32768
ds_read_b128 v[132:135],v2 offset:33792
ds_read_b128 v[136:139],v2 offset:34816
ds_read_b128 v[140:143],v2 offset:35840
ds_read_b128 v[144:147],v2 offset:36864
ds_read_b128 v[148:151],v2 offset:37888
ds_read_b128 v[152:155],v2 offset:38912
s_lshr_b32 s70,s47,5
buffer_store_dword v5,v4,s[36:39],s70 offen offset:0 sc1
s_mov_b32 s47,s59
s_lshr_b32 s70,s48,5
buffer_store_dword v5,v4,s[36:39],s70 offen offset:0 sc1
s_mov_b32 s48,s59
s_lshr_b32 s70,s59,5
buffer_store_dword v5,v4,s[36:39],s70 offen offset:0 sc1
s_waitcnt lgkmcnt(3)
s_setpc_b64 s[92:93]
Lg2_gs1_%=:
s_cmp_eq_u32 s54,1
s_cselect_b32 s68,s50,s49
s_lshr_b32 s68,s68,5
s_add_u32 m0,s78,8192
s_nop 0
buffer_load_dword v6,s[36:39],s68 offen offset:0 sc1 lds
s_add_u32 m0,s62,16384
s_nop 0
buffer_load_dwordx4 v1,s[36:39],s67 offen offset:0 sc1 lds
buffer_load_dwordx4 v1,s[36:39],s67 offen offset:1024 sc1 lds
buffer_load_dwordx4 v1,s[36:39],s67 offen offset:2048 sc1 lds
buffer_load_dwordx4 v1,s[36:39],s67 offen offset:3072 sc1 lds
s_lshr_b32 s72,s67,6
s_lshr_b32 s71,s62,2
s_add_u32 m0,s71,s88
s_nop 0
buffer_load_dwordx4 v10,s[36:39],s67 offen offset:0 sc1 lds
buffer_load_dword v11,s[36:39],s72 offen offset:1024 sc1 lds
s_waitcnt vmcnt(6)
s_barrier
ds_read_b32 v15,v7 offset:8192
ds_read_b128 v[128:131],v2 offset:32768
ds_read_b128 v[132:135],v2 offset:33792
ds_read_b128 v[136:139],v2 offset:34816
ds_read_b128 v[140:143],v2 offset:35840
ds_read_b128 v[144:147],v2 offset:36864
ds_read_b128 v[148:151],v2 offset:37888
ds_read_b128 v[152:155],v2 offset:38912
s_lshr_b32 s70,s47,5
buffer_store_dword v5,v4,s[36:39],s70 offen offset:0 sc1
s_mov_b32 s47,s59
s_waitcnt lgkmcnt(7)
v_cmp_eq_u32 vcc,s60,v15
s_nop 0
s_cmp_eq_u32 vcc_hi,-1
s_cselect_b32 s67,s50,s59
s_xor_b32 s62,s63,65536
s_cmp_ge_u32 s67,s59
s_cselect_b32 s68,s50,s51
s_lshr_b32 s68,s68,5
s_add_u32 m0,s78,256
s_nop 0
buffer_load_dword v6,s[36:39],s68 offen offset:0 sc1 lds
s_mov_b32 m0,s62
s_nop 0
buffer_load_dwordx4 v0,s[36:39],s67 offen offset:0 sc1 lds
buffer_load_dwordx4 v0,s[36:39],s67 offen offset:1024 sc1 lds
buffer_load_dwordx4 v0,s[36:39],s67 offen offset:2048 sc1 lds
buffer_load_dwordx4 v0,s[36:39],s67 offen offset:3072 sc1 lds
s_lshr_b32 s70,s59,5
buffer_store_dword v5,v4,s[36:39],s70 offen offset:0 sc1
s_lshr_b32 s70,s59,5
buffer_store_dword v5,v4,s[36:39],s70 offen offset:0 sc1
s_lshr_b32 s70,s59,5
buffer_store_dword v5,v4,s[36:39],s70 offen offset:0 sc1
s_waitcnt lgkmcnt(3)
s_setpc_b64 s[92:93]
Lg2_done1_%=:
s_waitcnt vmcnt(0)
s_barrier
s_lshr_b32 s70,s47,5
buffer_store_dword v5,v4,s[36:39],s70 offen offset:0 sc1
s_lshr_b32 s70,s48,5
buffer_store_dword v5,v4,s[36:39],s70 offen offset:0 sc1
s_branch Lg2_done_%=
Lg2_done_%=:
s_waitcnt vmcnt(0) lgkmcnt(0)
s_barrier
Lg2_end_%=:
s_mov_b32 %0,s60
Lcode_g12_e:)"
  : "=&s"(epOut) : "s"(ws), "s"(wave), "s"(sStep), "s"(sX1), "s"(sX2), "s"(sU0), "s"(sWw), "s"(sBuf), "v"(vSrc), "v"(vSrc2), "v"(vA), "v"(vOut), "v"(vFlag), "v"(vPollOff), "v"(vSnapRd), "v"(vX), "v"(vX2), "v"(vAuxS), "v"(vAuxS2), "v"(vOut2), "v"(vStW), "v"(vStR), "v"(vWd0), "v"(vWd1), "v"(vWr0), "v"(vWr1), "v"(vWr2), "s"(w12) : "v0", "v1", "v2", "v3", "v4", "v5", "v6", "v7", "v8", "v9", "v10", "v11", "v12", "v13", "v14", "v15", "v16", "v17", "v18", "v19", "v20", "v21", "v22", "v23", "v24", "v25", "v26", "v27", "v28", "v29", "v30", "v31", "v32", "v33", "v34", "v35", "v36", "v37", "v38", "v39", "v40", "v41", "v42", "v43", "v44", "v45", "v46", "v47", "v48", "v49", "v50", "v51", "v52", "v53", "v54", "v55", "v56", "v57", "v58", "v59", "v60", "v61", "v62", "v63", "v64", "v65", "v66", "v67", "v68", "v69", "v70", "v71", "v72", "v73", "v74", "v75", "v76", "v77", "v78", "v79", "v80", "v81", "v82", "v83", "v84", "v85", "v86", "v87", "v88", "v89", "v90", "v91", "v92", "v93", "v94", "v95", "v96", "v97", "v98", "v99", "v100", "v101", "v102", "v103", "v104", "v105", "v106", "v107", "v108", "v109", "v110", "v111", "v112", "v113", "v114", "v115", "v116", "v117", "v118", "v119", "v120", "v121", "v122", "v123", "v124", "v125", "v126", "v127", "v128", "v129", "v130", "v131", "v132", "v133", "v134", "v135", "v136", "v137", "v138", "v139", "v140", "v141", "v142", "v143", "v144", "v145", "v146", "v147", "v148", "v149", "v150", "v151", "v152", "v153", "v154", "v155", "v156", "v157", "v158", "v159", "v160", "v161", "v162", "v163", "v164", "v165", "v166", "v167", "v168", "v169", "v170", "v171", "v172", "v173", "v174", "v175", "v176", "v177", "v178", "v179", "v180", "v181", "v182", "v183", "v184", "v185", "v186", "v187", "v188", "v189", "v190", "v191", "v192", "v193", "v194", "v195", "v196", "v197", "v198", "v199", "v200", "v201", "v202", "v203", "v204", "v205", "v206", "v207", "v208", "v209", "v210", "v211", "v212", "v213", "v214", "v215", "v216", "v217", "v218", "v219", "v220", "v221", "v222", "v223", "v224", "v225", "v226", "v227", "v228", "v229", "v230", "v231", "v255", "a0", "a1", "a2", "a3", "a4", "a5", "a6", "a7", "a8", "a9", "a10", "a11", "a12", "a13", "a14", "a15", "a16", "a17", "a18", "a19", "a20", "a21", "a22", "a23", "a24", "a25", "a26", "a27", "a28", "a29", "a30", "a31", "a32", "a33", "a34", "a35", "a36", "a37", "a38", "a39", "a40", "a41", "a42", "a43", "a44", "a45", "a46", "a47", "a48", "a49", "a50", "a51", "a52", "a53", "a54", "a55", "a56", "a57", "a58", "a59", "a60", "a61", "a62", "a63", "a64", "a65", "a66", "a67", "a68", "a69", "a70", "a71", "a72", "a73", "a74", "a75", "a76", "a77", "a78", "a79", "a80", "a81", "a82", "a83", "a84", "a85", "a86", "a87", "a88", "a89", "a90", "a91", "a92", "a93", "a94", "a95", "a96", "a97", "a98", "a99", "a100", "a101", "a102", "a103", "a104", "a105", "a106", "a107", "a108", "a109", "a110", "a111", "a112", "a113", "a114", "a115", "a116", "a117", "a118", "a119", "a120", "a121", "a122", "a123", "a124", "a125", "a126", "a127", "a128", "a129", "a130", "a131", "a132", "a133", "a134", "a135", "a136", "a137", "a138", "a139", "a140", "a141", "a142", "a143", "a144", "a145", "a146", "a147", "a148", "a149", "a150", "a151", "a152", "a153", "a154", "a155", "a156", "a157", "a158", "a159", "a160", "a161", "a162", "a163", "a164", "a165", "a166", "a167", "a168", "a169", "a170", "a171", "a172", "a173", "a174", "a175", "a176", "a177", "a178", "a179", "a180", "a181", "a182", "a183", "a184", "a185", "a186", "a187", "a188", "a189", "a190", "a191", "a192", "a193", "a194", "a195", "a196", "a197", "a198", "a199", "a200", "a201", "a202", "a203", "a204", "a205", "a206", "a207", "a208", "a209", "a210", "a211", "a212", "a213", "a214", "a215", "a216", "a217", "a218", "a219", "a220", "a221", "a222", "a223", "a224", "a225", "a226", "a227", "a228", "a229", "a230", "a231", "a232", "a233", "a234", "a235", "a236", "a237", "a238", "a239", "a240", "a241", "a242", "a243", "a244", "a245", "a246", "a247", "a248", "a249", "a250", "a251", "a252", "a253", "a254", "a255", "s36", "s37", "s38", "s39", "s40", "s41", "s42", "s43", "s44", "s45", "s46", "s47", "s48", "s49", "s50", "s51", "s52", "s53", "s54", "s55", "s56", "s57", "s58", "s59", "s60", "s61", "s62", "s63", "s64", "s65", "s66", "s67", "s68", "s69", "s70", "s71", "s72", "s73", "s74", "s75", "s76", "s77", "s78", "s79", "s80", "s81", "s82", "s83", "s84", "s85", "s86", "s87", "s88", "s89", "s90", "s91", "s92", "s93", "s94", "s95", "s96", "s97", "s98", "s99", "vcc", "scc", "memory");
    ep = epOut;
    if (N3 && kind == 1 && rep < 2 * NX) { kind = 4; kh = (rep & 1) * 2 + (slice >> 2); slice &= 3; rep = 5 + (rep >> 1); }
   }
  if (kind == 3 || kind == 5) {
    KARG(ws, 10) KARG(w3, 8)
    COMMON(8, OFF_FX1, OFF_FG + slice * 128, AUX + 1536, 2048, 7168, kind == 3 ? 5 : 4);
    const unsigned vSrc = OFF_X1 + wave * 4096 + vA, vOut = OFF_G + (slice * 6 + wave) * 1024 + vA;
    const unsigned vOut2 = OFF_G + (slice * 6 + 4 + (wave >> 1)) * 1024 + (((wave & 1) * 2 + (lg >> 1)) * 16 + l15) * 16 + (lg & 1) * 8;
    const unsigned vAuxS = OFF_ST + (ln & 7) * 128 + (wave * 8 + (ln >> 3)) * 4, vX = AUX + wave * 2048 + ln * 4;
    const unsigned vStW = (ln & 7) == 0 ? AUX + 1280 + (wave * 8 + (ln >> 3)) * 4 : AUX + 24576 + wave * 2048 + 1536 + ln * 4, vStR = AUX + 1280 + l15 * 8;
    const unsigned sWw = 16 * wave * 7168 + slice * 384;
    if (kind == 3) { asm volatile(R"(
Lcode_g3_b:
s_mov_b64 s[36:37],%0
s_mov_b32 s38,122727552
s_mov_b32 s39,0x00020000
s_mov_b32 s54,%1
s_mov_b32 s55,%2
s_mov_b32 s56,%3
s_mov_b32 s58,%4
s_mov_b32 s83,%5
s_mov_b32 s82,%6
s_mov_b32 s84,%7
s_mov_b32 s60,%8
v_mov_b32 v0,%9
v_mov_b32 v2,%10
v_mov_b32 v3,%11
v_mov_b32 v4,%12
v_mov_b32 v6,%13
v_mov_b32 v7,%14
v_mov_b32 v8,%15
v_mov_b32 v9,%16
v_mov_b32 v10,%17
v_mov_b32 v11,%18
v_mov_b32 v28,%19
v_mov_b32 v29,%20
v_mov_b32 v30,%21
v_mov_b32 v31,%22
v_mov_b32 v32,%23
s_mov_b64 s[40:41],%24
s_mov_b32 s42,7340032
s_mov_b32 s43,0x00020000
s_lshl_b32 s88,s54,11
s_add_u32 m0,s88,0
s_add_u32 s61,s82,0
buffer_load_dwordx4 v28,s[40:43],s61 offen offset:0  lds
s_add_u32 m0,s88,1024
s_nop 0
buffer_load_dwordx4 v29,s[40:43],s61 offen offset:0  lds
s_add_u32 m0,s88,8064
s_nop 0
buffer_load_dwordx4 v28,s[40:43],s61 offen offset:128  lds
s_add_u32 m0,s88,9088
s_nop 0
buffer_load_dwordx4 v29,s[40:43],s61 offen offset:128  lds
s_add_u32 m0,s88,16128
s_nop 0
buffer_load_dwordx4 v28,s[40:43],s61 offen offset:256  lds
s_add_u32 m0,s88,17152
s_nop 0
buffer_load_dwordx4 v29,s[40:43],s61 offen offset:256  lds
s_add_u32 m0,s88,24576
s_add_u32 s61,s82,458752
buffer_load_dwordx4 v28,s[40:43],s61 offen offset:0  lds
s_add_u32 m0,s88,25600
s_nop 0
buffer_load_dwordx4 v29,s[40:43],s61 offen offset:0  lds
s_add_u32 m0,s88,32640
s_nop 0
buffer_load_dwordx4 v28,s[40:43],s61 offen offset:128  lds
s_add_u32 m0,s88,33664
s_nop 0
buffer_load_dwordx4 v29,s[40:43],s61 offen offset:128  lds
s_add_u32 m0,s88,40704
s_nop 0
buffer_load_dwordx4 v28,s[40:43],s61 offen offset:256  lds
s_add_u32 m0,s88,41728
s_nop 0
buffer_load_dwordx4 v29,s[40:43],s61 offen offset:256  lds
s_add_u32 m0,s88,49152
s_add_u32 s61,s82,917504
buffer_load_dwordx4 v28,s[40:43],s61 offen offset:0  lds
s_add_u32 m0,s88,50176
s_nop 0
buffer_load_dwordx4 v29,s[40:43],s61 offen offset:0  lds
s_add_u32 m0,s88,57216
s_nop 0
buffer_load_dwordx4 v28,s[40:43],s61 offen offset:128  lds
s_add_u32 m0,s88,58240
s_nop 0
buffer_load_dwordx4 v29,s[40:43],s61 offen offset:128  lds
s_add_u32 m0,s88,65280
s_nop 0
buffer_load_dwordx4 v28,s[40:43],s61 offen offset:256  lds
s_add_u32 m0,s88,66304
s_nop 0
buffer_load_dwordx4 v29,s[40:43],s61 offen offset:256  lds
s_add_u32 m0,s88,73728
s_add_u32 s61,s82,1376256
buffer_load_dwordx4 v28,s[40:43],s61 offen offset:0  lds
s_add_u32 m0,s88,74752
s_nop 0
buffer_load_dwordx4 v29,s[40:43],s61 offen offset:0  lds
s_add_u32 m0,s88,81792
s_nop 0
buffer_load_dwordx4 v28,s[40:43],s61 offen offset:128  lds
s_add_u32 m0,s88,82816
s_nop 0
buffer_load_dwordx4 v29,s[40:43],s61 offen offset:128  lds
s_add_u32 m0,s88,89856
s_nop 0
buffer_load_dwordx4 v28,s[40:43],s61 offen offset:256  lds
s_add_u32 m0,s88,90880
s_nop 0
buffer_load_dwordx4 v29,s[40:43],s61 offen offset:256  lds
v_add_u32 v40,49152,v30
v_add_u32 v41,49152,v31
v_add_u32 v42,49152,v32
v_add_u32 v43,98304,v30
v_add_u32 v44,98304,v31
v_add_u32 v45,98304,v32
v_mov_b32 v84,0x3f803f80
v_mov_b32 v85,0x3f803f80
v_mov_b32 v86,0x3f803f80
v_mov_b32 v87,0x3f803f80
s_waitcnt vmcnt(18)
s_barrier
s_add_u32 m0,s88,98304
s_add_u32 s61,s82,1835008
buffer_load_dwordx4 v28,s[40:43],s61 offen offset:0  lds
s_add_u32 m0,s88,99328
ds_read_b64_tr_b16 a[0:1],v30 offset:0
buffer_load_dwordx4 v29,s[40:43],s61 offen offset:0  lds
s_add_u32 m0,s88,106368
ds_read_b64_tr_b16 a[2:3],v30 offset:512
buffer_load_dwordx4 v28,s[40:43],s61 offen offset:128  lds
s_add_u32 m0,s88,107392
ds_read_b64_tr_b16 a[4:5],v31 offset:0
buffer_load_dwordx4 v29,s[40:43],s61 offen offset:128  lds
s_add_u32 m0,s88,114432
ds_read_b64_tr_b16 a[6:7],v31 offset:512
buffer_load_dwordx4 v28,s[40:43],s61 offen offset:256  lds
s_add_u32 m0,s88,115456
ds_read_b64_tr_b16 a[8:9],v32 offset:0
buffer_load_dwordx4 v29,s[40:43],s61 offen offset:256  lds
ds_read_b64_tr_b16 a[10:11],v32 offset:512
ds_read_b64_tr_b16 a[12:13],v30 offset:4096
ds_read_b64_tr_b16 a[14:15],v30 offset:4608
ds_read_b64_tr_b16 a[16:17],v31 offset:4096
ds_read_b64_tr_b16 a[18:19],v31 offset:4608
ds_read_b64_tr_b16 a[20:21],v32 offset:4096
ds_read_b64_tr_b16 a[22:23],v32 offset:4608
s_waitcnt vmcnt(18)
s_barrier
s_add_u32 m0,s88,122880
s_add_u32 s61,s82,2293760
buffer_load_dwordx4 v28,s[40:43],s61 offen offset:0  lds
s_add_u32 m0,s88,123904
ds_read_b64_tr_b16 a[24:25],v30 offset:24576
buffer_load_dwordx4 v29,s[40:43],s61 offen offset:0  lds
s_add_u32 m0,s88,130944
ds_read_b64_tr_b16 a[26:27],v30 offset:25088
buffer_load_dwordx4 v28,s[40:43],s61 offen offset:128  lds
s_add_u32 m0,s88,131968
ds_read_b64_tr_b16 a[28:29],v31 offset:24576
buffer_load_dwordx4 v29,s[40:43],s61 offen offset:128  lds
s_add_u32 m0,s88,139008
ds_read_b64_tr_b16 a[30:31],v31 offset:25088
buffer_load_dwordx4 v28,s[40:43],s61 offen offset:256  lds
s_add_u32 m0,s88,140032
ds_read_b64_tr_b16 a[32:33],v32 offset:24576
buffer_load_dwordx4 v29,s[40:43],s61 offen offset:256  lds
ds_read_b64_tr_b16 a[34:35],v32 offset:25088
ds_read_b64_tr_b16 a[36:37],v30 offset:28672
ds_read_b64_tr_b16 a[38:39],v30 offset:29184
ds_read_b64_tr_b16 a[40:41],v31 offset:28672
ds_read_b64_tr_b16 a[42:43],v31 offset:29184
ds_read_b64_tr_b16 a[44:45],v32 offset:28672
ds_read_b64_tr_b16 a[46:47],v32 offset:29184
s_waitcnt lgkmcnt(12)
v_mfma_f32_16x16x32_bf16 v[16:19],a[0:3],v[84:87],0
v_mfma_f32_16x16x32_bf16 v[20:23],a[4:7],v[84:87],0
v_mfma_f32_16x16x32_bf16 v[24:27],a[8:11],v[84:87],0
v_mfma_f32_16x16x32_bf16 v[16:19],a[12:15],v[84:87],v[16:19]
v_mfma_f32_16x16x32_bf16 v[20:23],a[16:19],v[84:87],v[20:23]
v_mfma_f32_16x16x32_bf16 v[24:27],a[20:23],v[84:87],v[24:27]
s_waitcnt vmcnt(18)
s_barrier
s_add_u32 m0,s88,0
s_add_u32 s61,s82,2752512
buffer_load_dwordx4 v28,s[40:43],s61 offen offset:0  lds
s_add_u32 m0,s88,1024
ds_read_b64_tr_b16 a[48:49],v40 offset:0
buffer_load_dwordx4 v29,s[40:43],s61 offen offset:0  lds
s_add_u32 m0,s88,8064
ds_read_b64_tr_b16 a[50:51],v40 offset:512
buffer_load_dwordx4 v28,s[40:43],s61 offen offset:128  lds
s_add_u32 m0,s88,9088
ds_read_b64_tr_b16 a[52:53],v41 offset:0
buffer_load_dwordx4 v29,s[40:43],s61 offen offset:128  lds
s_add_u32 m0,s88,16128
ds_read_b64_tr_b16 a[54:55],v41 offset:512
buffer_load_dwordx4 v28,s[40:43],s61 offen offset:256  lds
s_add_u32 m0,s88,17152
ds_read_b64_tr_b16 a[56:57],v42 offset:0
buffer_load_dwordx4 v29,s[40:43],s61 offen offset:256  lds
ds_read_b64_tr_b16 a[58:59],v42 offset:512
ds_read_b64_tr_b16 a[60:61],v40 offset:4096
ds_read_b64_tr_b16 a[62:63],v40 offset:4608
ds_read_b64_tr_b16 a[64:65],v41 offset:4096
ds_read_b64_tr_b16 a[66:67],v41 offset:4608
ds_read_b64_tr_b16 a[68:69],v42 offset:4096)"
R"(
ds_read_b64_tr_b16 a[70:71],v42 offset:4608
s_waitcnt lgkmcnt(12)
v_mfma_f32_16x16x32_bf16 v[16:19],a[24:27],v[84:87],v[16:19]
v_mfma_f32_16x16x32_bf16 v[20:23],a[28:31],v[84:87],v[20:23]
v_mfma_f32_16x16x32_bf16 v[24:27],a[32:35],v[84:87],v[24:27]
v_mfma_f32_16x16x32_bf16 v[16:19],a[36:39],v[84:87],v[16:19]
v_mfma_f32_16x16x32_bf16 v[20:23],a[40:43],v[84:87],v[20:23]
v_mfma_f32_16x16x32_bf16 v[24:27],a[44:47],v[84:87],v[24:27]
s_waitcnt vmcnt(18)
s_barrier
s_add_u32 m0,s88,24576
s_add_u32 s61,s82,3211264
buffer_load_dwordx4 v28,s[40:43],s61 offen offset:0  lds
s_add_u32 m0,s88,25600
ds_read_b64_tr_b16 a[72:73],v40 offset:24576
buffer_load_dwordx4 v29,s[40:43],s61 offen offset:0  lds
s_add_u32 m0,s88,32640
ds_read_b64_tr_b16 a[74:75],v40 offset:25088
buffer_load_dwordx4 v28,s[40:43],s61 offen offset:128  lds
s_add_u32 m0,s88,33664
ds_read_b64_tr_b16 a[76:77],v41 offset:24576
buffer_load_dwordx4 v29,s[40:43],s61 offen offset:128  lds
s_add_u32 m0,s88,40704
ds_read_b64_tr_b16 a[78:79],v41 offset:25088
buffer_load_dwordx4 v28,s[40:43],s61 offen offset:256  lds
s_add_u32 m0,s88,41728
ds_read_b64_tr_b16 a[80:81],v42 offset:24576
buffer_load_dwordx4 v29,s[40:43],s61 offen offset:256  lds
ds_read_b64_tr_b16 a[82:83],v42 offset:25088
ds_read_b64_tr_b16 a[84:85],v40 offset:28672
ds_read_b64_tr_b16 a[86:87],v40 offset:29184
ds_read_b64_tr_b16 a[88:89],v41 offset:28672
ds_read_b64_tr_b16 a[90:91],v41 offset:29184
ds_read_b64_tr_b16 a[92:93],v42 offset:28672
ds_read_b64_tr_b16 a[94:95],v42 offset:29184
s_waitcnt lgkmcnt(12)
v_mfma_f32_16x16x32_bf16 v[16:19],a[48:51],v[84:87],v[16:19]
v_mfma_f32_16x16x32_bf16 v[20:23],a[52:55],v[84:87],v[20:23]
v_mfma_f32_16x16x32_bf16 v[24:27],a[56:59],v[84:87],v[24:27]
v_mfma_f32_16x16x32_bf16 v[16:19],a[60:63],v[84:87],v[16:19]
v_mfma_f32_16x16x32_bf16 v[20:23],a[64:67],v[84:87],v[20:23]
v_mfma_f32_16x16x32_bf16 v[24:27],a[68:71],v[84:87],v[24:27]
s_waitcnt vmcnt(18)
s_barrier
s_add_u32 m0,s88,49152
s_add_u32 s61,s82,3670016
buffer_load_dwordx4 v28,s[40:43],s61 offen offset:0  lds
s_add_u32 m0,s88,50176
ds_read_b64_tr_b16 a[96:97],v43 offset:0
buffer_load_dwordx4 v29,s[40:43],s61 offen offset:0  lds
s_add_u32 m0,s88,57216
ds_read_b64_tr_b16 a[98:99],v43 offset:512
buffer_load_dwordx4 v28,s[40:43],s61 offen offset:128  lds
s_add_u32 m0,s88,58240
ds_read_b64_tr_b16 a[100:101],v44 offset:0
buffer_load_dwordx4 v29,s[40:43],s61 offen offset:128  lds
s_add_u32 m0,s88,65280
ds_read_b64_tr_b16 a[102:103],v44 offset:512
buffer_load_dwordx4 v28,s[40:43],s61 offen offset:256  lds
s_add_u32 m0,s88,66304
ds_read_b64_tr_b16 a[104:105],v45 offset:0
buffer_load_dwordx4 v29,s[40:43],s61 offen offset:256  lds
ds_read_b64_tr_b16 a[106:107],v45 offset:512
ds_read_b64_tr_b16 a[108:109],v43 offset:4096
ds_read_b64_tr_b16 a[110:111],v43 offset:4608
ds_read_b64_tr_b16 a[112:113],v44 offset:4096
ds_read_b64_tr_b16 a[114:115],v44 offset:4608
ds_read_b64_tr_b16 a[116:117],v45 offset:4096
ds_read_b64_tr_b16 a[118:119],v45 offset:4608
s_waitcnt lgkmcnt(12)
v_mfma_f32_16x16x32_bf16 v[16:19],a[72:75],v[84:87],v[16:19]
v_mfma_f32_16x16x32_bf16 v[20:23],a[76:79],v[84:87],v[20:23]
v_mfma_f32_16x16x32_bf16 v[24:27],a[80:83],v[84:87],v[24:27]
v_mfma_f32_16x16x32_bf16 v[16:19],a[84:87],v[84:87],v[16:19]
v_mfma_f32_16x16x32_bf16 v[20:23],a[88:91],v[84:87],v[20:23]
v_mfma_f32_16x16x32_bf16 v[24:27],a[92:95],v[84:87],v[24:27]
s_waitcnt vmcnt(18)
s_barrier
s_add_u32 m0,s88,73728
s_add_u32 s61,s82,4128768
buffer_load_dwordx4 v28,s[40:43],s61 offen offset:0  lds
s_add_u32 m0,s88,74752
ds_read_b64_tr_b16 a[120:121],v43 offset:24576
buffer_load_dwordx4 v29,s[40:43],s61 offen offset:0  lds
s_add_u32 m0,s88,81792
ds_read_b64_tr_b16 a[122:123],v43 offset:25088
buffer_load_dwordx4 v28,s[40:43],s61 offen offset:128  lds
s_add_u32 m0,s88,82816
ds_read_b64_tr_b16 a[124:125],v44 offset:24576
buffer_load_dwordx4 v29,s[40:43],s61 offen offset:128  lds
s_add_u32 m0,s88,89856
ds_read_b64_tr_b16 a[126:127],v44 offset:25088
buffer_load_dwordx4 v28,s[40:43],s61 offen offset:256  lds
s_add_u32 m0,s88,90880
ds_read_b64_tr_b16 a[128:129],v45 offset:24576
buffer_load_dwordx4 v29,s[40:43],s61 offen offset:256  lds
ds_read_b64_tr_b16 a[130:131],v45 offset:25088
ds_read_b64_tr_b16 a[132:133],v43 offset:28672
ds_read_b64_tr_b16 a[134:135],v43 offset:29184
ds_read_b64_tr_b16 a[136:137],v44 offset:28672
ds_read_b64_tr_b16 a[138:139],v44 offset:29184
ds_read_b64_tr_b16 a[140:141],v45 offset:28672
ds_read_b64_tr_b16 a[142:143],v45 offset:29184
s_waitcnt lgkmcnt(12)
v_mfma_f32_16x16x32_bf16 v[16:19],a[96:99],v[84:87],v[16:19]
v_mfma_f32_16x16x32_bf16 v[20:23],a[100:103],v[84:87],v[20:23]
v_mfma_f32_16x16x32_bf16 v[24:27],a[104:107],v[84:87],v[24:27]
v_mfma_f32_16x16x32_bf16 v[16:19],a[108:111],v[84:87],v[16:19]
v_mfma_f32_16x16x32_bf16 v[20:23],a[112:115],v[84:87],v[20:23]
v_mfma_f32_16x16x32_bf16 v[24:27],a[116:119],v[84:87],v[24:27]
s_waitcnt vmcnt(18)
s_barrier
s_add_u32 m0,s88,98304
s_add_u32 s61,s82,4587520
buffer_load_dwordx4 v28,s[40:43],s61 offen offset:0  lds
s_add_u32 m0,s88,99328
ds_read_b64_tr_b16 a[144:145],v30 offset:0
buffer_load_dwordx4 v29,s[40:43],s61 offen offset:0  lds
s_add_u32 m0,s88,106368
ds_read_b64_tr_b16 a[146:147],v30 offset:512
buffer_load_dwordx4 v28,s[40:43],s61 offen offset:128  lds
s_add_u32 m0,s88,107392
ds_read_b64_tr_b16 a[148:149],v31 offset:0
buffer_load_dwordx4 v29,s[40:43],s61 offen offset:128  lds
s_add_u32 m0,s88,114432
ds_read_b64_tr_b16 a[150:151],v31 offset:512
buffer_load_dwordx4 v28,s[40:43],s61 offen offset:256  lds
s_add_u32 m0,s88,115456
ds_read_b64_tr_b16 a[152:153],v32 offset:0
buffer_load_dwordx4 v29,s[40:43],s61 offen offset:256  lds
ds_read_b64_tr_b16 a[154:155],v32 offset:512
ds_read_b64_tr_b16 a[156:157],v30 offset:4096
ds_read_b64_tr_b16 a[158:159],v30 offset:4608
ds_read_b64_tr_b16 a[160:161],v31 offset:4096
ds_read_b64_tr_b16 a[162:163],v31 offset:4608
ds_read_b64_tr_b16 a[164:165],v32 offset:4096
ds_read_b64_tr_b16 a[166:167],v32 offset:4608
s_waitcnt lgkmcnt(12)
v_mfma_f32_16x16x32_bf16 v[16:19],a[120:123],v[84:87],v[16:19]
v_mfma_f32_16x16x32_bf16 v[20:23],a[124:127],v[84:87],v[20:23]
v_mfma_f32_16x16x32_bf16 v[24:27],a[128:131],v[84:87],v[24:27]
v_mfma_f32_16x16x32_bf16 v[16:19],a[132:135],v[84:87],v[16:19]
v_mfma_f32_16x16x32_bf16 v[20:23],a[136:139],v[84:87],v[20:23]
v_mfma_f32_16x16x32_bf16 v[24:27],a[140:143],v[84:87],v[24:27]
s_waitcnt vmcnt(18)
s_barrier
s_add_u32 m0,s88,122880
s_add_u32 s61,s82,5046272
buffer_load_dwordx4 v28,s[40:43],s61 offen offset:0  lds
s_add_u32 m0,s88,123904
ds_read_b64_tr_b16 a[168:169],v30 offset:24576
buffer_load_dwordx4 v29,s[40:43],s61 offen offset:0  lds
s_add_u32 m0,s88,130944
ds_read_b64_tr_b16 a[170:171],v30 offset:25088
buffer_load_dwordx4 v28,s[40:43],s61 offen offset:128  lds
s_add_u32 m0,s88,131968
ds_read_b64_tr_b16 a[172:173],v31 offset:24576
buffer_load_dwordx4 v29,s[40:43],s61 offen offset:128  lds
s_add_u32 m0,s88,139008
ds_read_b64_tr_b16 a[174:175],v31 offset:25088
buffer_load_dwordx4 v28,s[40:43],s61 offen offset:256  lds
s_add_u32 m0,s88,140032
ds_read_b64_tr_b16 a[176:177],v32 offset:24576
buffer_load_dwordx4 v29,s[40:43],s61 offen offset:256  lds
ds_read_b64_tr_b16 a[178:179],v32 offset:25088
ds_read_b64_tr_b16 a[180:181],v30 offset:28672
ds_read_b64_tr_b16 a[182:183],v30 offset:29184
ds_read_b64_tr_b16 a[184:185],v31 offset:28672
ds_read_b64_tr_b16 a[186:187],v31 offset:29184
ds_read_b64_tr_b16 a[188:189],v32 offset:28672
ds_read_b64_tr_b16 a[190:191],v32 offset:29184
s_waitcnt lgkmcnt(12)
v_mfma_f32_16x16x32_bf16 v[16:19],a[144:147],v[84:87],v[16:19]
v_mfma_f32_16x16x32_bf16 v[20:23],a[148:151],v[84:87],v[20:23]
v_mfma_f32_16x16x32_bf16 v[24:27],a[152:155],v[84:87],v[24:27]
v_mfma_f32_16x16x32_bf16 v[16:19],a[156:159],v[84:87],v[16:19]
v_mfma_f32_16x16x32_bf16 v[20:23],a[160:163],v[84:87],v[20:23]
v_mfma_f32_16x16x32_bf16 v[24:27],a[164:167],v[84:87],v[24:27]
s_waitcnt vmcnt(18)
s_barrier
s_add_u32 m0,s88,0
s_add_u32 s61,s82,5505024
buffer_load_dwordx4 v28,s[40:43],s61 offen offset:0  lds
s_add_u32 m0,s88,1024
ds_read_b64_tr_b16 a[192:193],v40 offset:0
buffer_load_dwordx4 v29,s[40:43],s61 offen offset:0  lds
s_add_u32 m0,s88,8064
ds_read_b64_tr_b16 a[194:195],v40 offset:512
buffer_load_dwordx4 v28,s[40:43],s61 offen offset:128  lds
s_add_u32 m0,s88,9088
ds_read_b64_tr_b16 a[196:197],v41 offset:0
buffer_load_dwordx4 v29,s[40:43],s61 offen offset:128  lds
s_add_u32 m0,s88,16128
ds_read_b64_tr_b16 a[198:199],v41 offset:512
buffer_load_dwordx4 v28,s[40:43],s61 offen offset:256  lds
s_add_u32 m0,s88,17152
ds_read_b64_tr_b16 a[200:201],v42 offset:0
buffer_load_dwordx4 v29,s[40:43],s61 offen offset:256  lds
ds_read_b64_tr_b16 a[202:203],v42 offset:512
ds_read_b64_tr_b16 a[204:205],v40 offset:4096)"
R"(
ds_read_b64_tr_b16 a[206:207],v40 offset:4608
ds_read_b64_tr_b16 a[208:209],v41 offset:4096
ds_read_b64_tr_b16 a[210:211],v41 offset:4608
ds_read_b64_tr_b16 a[212:213],v42 offset:4096
ds_read_b64_tr_b16 a[214:215],v42 offset:4608
s_waitcnt lgkmcnt(12)
v_mfma_f32_16x16x32_bf16 v[16:19],a[168:171],v[84:87],v[16:19]
v_mfma_f32_16x16x32_bf16 v[20:23],a[172:175],v[84:87],v[20:23]
v_mfma_f32_16x16x32_bf16 v[24:27],a[176:179],v[84:87],v[24:27]
v_mfma_f32_16x16x32_bf16 v[16:19],a[180:183],v[84:87],v[16:19]
v_mfma_f32_16x16x32_bf16 v[20:23],a[184:187],v[84:87],v[20:23]
v_mfma_f32_16x16x32_bf16 v[24:27],a[188:191],v[84:87],v[24:27]
s_waitcnt vmcnt(18)
s_barrier
s_add_u32 m0,s88,24576
s_add_u32 s61,s82,5963776
buffer_load_dwordx4 v28,s[40:43],s61 offen offset:0  lds
s_add_u32 m0,s88,25600
ds_read_b64_tr_b16 a[216:217],v40 offset:24576
buffer_load_dwordx4 v29,s[40:43],s61 offen offset:0  lds
s_add_u32 m0,s88,32640
ds_read_b64_tr_b16 a[218:219],v40 offset:25088
buffer_load_dwordx4 v28,s[40:43],s61 offen offset:128  lds
s_add_u32 m0,s88,33664
ds_read_b64_tr_b16 a[220:221],v41 offset:24576
buffer_load_dwordx4 v29,s[40:43],s61 offen offset:128  lds
s_add_u32 m0,s88,40704
ds_read_b64_tr_b16 a[222:223],v41 offset:25088
buffer_load_dwordx4 v28,s[40:43],s61 offen offset:256  lds
s_add_u32 m0,s88,41728
ds_read_b64_tr_b16 a[224:225],v42 offset:24576
buffer_load_dwordx4 v29,s[40:43],s61 offen offset:256  lds
ds_read_b64_tr_b16 a[226:227],v42 offset:25088
ds_read_b64_tr_b16 a[228:229],v40 offset:28672
ds_read_b64_tr_b16 a[230:231],v40 offset:29184
ds_read_b64_tr_b16 a[232:233],v41 offset:28672
ds_read_b64_tr_b16 a[234:235],v41 offset:29184
ds_read_b64_tr_b16 a[236:237],v42 offset:28672
ds_read_b64_tr_b16 a[238:239],v42 offset:29184
s_waitcnt lgkmcnt(12)
v_mfma_f32_16x16x32_bf16 v[16:19],a[192:195],v[84:87],v[16:19]
v_mfma_f32_16x16x32_bf16 v[20:23],a[196:199],v[84:87],v[20:23]
v_mfma_f32_16x16x32_bf16 v[24:27],a[200:203],v[84:87],v[24:27]
v_mfma_f32_16x16x32_bf16 v[16:19],a[204:207],v[84:87],v[16:19]
v_mfma_f32_16x16x32_bf16 v[20:23],a[208:211],v[84:87],v[20:23]
v_mfma_f32_16x16x32_bf16 v[24:27],a[212:215],v[84:87],v[24:27]
s_waitcnt vmcnt(18)
s_barrier
s_add_u32 m0,s88,49152
s_add_u32 s61,s82,6422528
buffer_load_dwordx4 v28,s[40:43],s61 offen offset:0  lds
s_add_u32 m0,s88,50176
ds_read_b64_tr_b16 a[240:241],v43 offset:0
buffer_load_dwordx4 v29,s[40:43],s61 offen offset:0  lds
s_add_u32 m0,s88,57216
ds_read_b64_tr_b16 a[242:243],v43 offset:512
buffer_load_dwordx4 v28,s[40:43],s61 offen offset:128  lds
s_add_u32 m0,s88,58240
ds_read_b64_tr_b16 a[244:245],v44 offset:0
buffer_load_dwordx4 v29,s[40:43],s61 offen offset:128  lds
s_add_u32 m0,s88,65280
ds_read_b64_tr_b16 a[246:247],v44 offset:512
buffer_load_dwordx4 v28,s[40:43],s61 offen offset:256  lds
s_add_u32 m0,s88,66304
ds_read_b64_tr_b16 a[248:249],v45 offset:0
buffer_load_dwordx4 v29,s[40:43],s61 offen offset:256  lds
ds_read_b64_tr_b16 a[250:251],v45 offset:512
ds_read_b64_tr_b16 a[252:253],v43 offset:4096
ds_read_b64_tr_b16 a[254:255],v43 offset:4608
ds_read_b64_tr_b16 v[128:129],v44 offset:4096
ds_read_b64_tr_b16 v[130:131],v44 offset:4608
ds_read_b64_tr_b16 v[132:133],v45 offset:4096
ds_read_b64_tr_b16 v[134:135],v45 offset:4608
s_waitcnt lgkmcnt(12)
v_mfma_f32_16x16x32_bf16 v[16:19],a[216:219],v[84:87],v[16:19]
v_mfma_f32_16x16x32_bf16 v[20:23],a[220:223],v[84:87],v[20:23]
v_mfma_f32_16x16x32_bf16 v[24:27],a[224:227],v[84:87],v[24:27]
v_mfma_f32_16x16x32_bf16 v[16:19],a[228:231],v[84:87],v[16:19]
v_mfma_f32_16x16x32_bf16 v[20:23],a[232:235],v[84:87],v[20:23]
v_mfma_f32_16x16x32_bf16 v[24:27],a[236:239],v[84:87],v[24:27]
s_waitcnt vmcnt(18)
s_barrier
s_add_u32 m0,s88,73728
s_add_u32 s61,s82,6881280
buffer_load_dwordx4 v28,s[40:43],s61 offen offset:0  lds
s_add_u32 m0,s88,74752
ds_read_b64_tr_b16 v[136:137],v43 offset:24576
buffer_load_dwordx4 v29,s[40:43],s61 offen offset:0  lds
s_add_u32 m0,s88,81792
ds_read_b64_tr_b16 v[138:139],v43 offset:25088
buffer_load_dwordx4 v28,s[40:43],s61 offen offset:128  lds
s_add_u32 m0,s88,82816
ds_read_b64_tr_b16 v[140:141],v44 offset:24576
buffer_load_dwordx4 v29,s[40:43],s61 offen offset:128  lds
s_add_u32 m0,s88,89856
ds_read_b64_tr_b16 v[142:143],v44 offset:25088
buffer_load_dwordx4 v28,s[40:43],s61 offen offset:256  lds
s_add_u32 m0,s88,90880
ds_read_b64_tr_b16 v[144:145],v45 offset:24576
buffer_load_dwordx4 v29,s[40:43],s61 offen offset:256  lds
ds_read_b64_tr_b16 v[146:147],v45 offset:25088
ds_read_b64_tr_b16 v[148:149],v43 offset:28672
ds_read_b64_tr_b16 v[150:151],v43 offset:29184
ds_read_b64_tr_b16 v[152:153],v44 offset:28672
ds_read_b64_tr_b16 v[154:155],v44 offset:29184
ds_read_b64_tr_b16 v[156:157],v45 offset:28672
ds_read_b64_tr_b16 v[158:159],v45 offset:29184
s_getpc_b64 s[46:47]
Lg3_cp_%=:
s_add_u32 s46,s46,Lg3_init_%=-Lg3_cp_%=
s_addc_u32 s47,s47,0
s_lshl_b32 s68,s54,13
v_lshl_add_u32 v68,v2,3,s68
v_min_u32 v69,Lg3_done_%=-Lg3_init_%=,v68
global_load_dword v72,v69,s[46:47]
s_waitcnt lgkmcnt(12)
v_mfma_f32_16x16x32_bf16 v[16:19],a[240:243],v[84:87],v[16:19]
v_mfma_f32_16x16x32_bf16 v[20:23],a[244:247],v[84:87],v[20:23]
v_mfma_f32_16x16x32_bf16 v[24:27],a[248:251],v[84:87],v[24:27]
v_mfma_f32_16x16x32_bf16 v[16:19],a[252:255],v[84:87],v[16:19]
v_mfma_f32_16x16x32_bf16 v[20:23],v[128:131],v[84:87],v[20:23]
v_mfma_f32_16x16x32_bf16 v[24:27],v[132:135],v[84:87],v[24:27]
s_waitcnt vmcnt(19)
s_barrier
ds_read_b64_tr_b16 v[160:161],v30 offset:0
ds_read_b64_tr_b16 v[162:163],v30 offset:512
ds_read_b64_tr_b16 v[164:165],v31 offset:0
ds_read_b64_tr_b16 v[166:167],v31 offset:512
ds_read_b64_tr_b16 v[168:169],v32 offset:0
ds_read_b64_tr_b16 v[170:171],v32 offset:512
ds_read_b64_tr_b16 v[172:173],v30 offset:4096
ds_read_b64_tr_b16 v[174:175],v30 offset:4608
ds_read_b64_tr_b16 v[176:177],v31 offset:4096
ds_read_b64_tr_b16 v[178:179],v31 offset:4608
ds_read_b64_tr_b16 v[180:181],v32 offset:4096
ds_read_b64_tr_b16 v[182:183],v32 offset:4608
s_waitcnt lgkmcnt(12)
v_mfma_f32_16x16x32_bf16 v[16:19],v[136:139],v[84:87],v[16:19]
v_mfma_f32_16x16x32_bf16 v[20:23],v[140:143],v[84:87],v[20:23]
v_mfma_f32_16x16x32_bf16 v[24:27],v[144:147],v[84:87],v[24:27]
v_mfma_f32_16x16x32_bf16 v[16:19],v[148:151],v[84:87],v[16:19]
v_mfma_f32_16x16x32_bf16 v[20:23],v[152:155],v[84:87],v[20:23]
v_mfma_f32_16x16x32_bf16 v[24:27],v[156:159],v[84:87],v[24:27]
s_waitcnt vmcnt(13)
s_barrier
ds_read_b64_tr_b16 v[184:185],v30 offset:24576
ds_read_b64_tr_b16 v[186:187],v30 offset:25088
ds_read_b64_tr_b16 v[188:189],v31 offset:24576
ds_read_b64_tr_b16 v[190:191],v31 offset:25088
ds_read_b64_tr_b16 v[192:193],v32 offset:24576
ds_read_b64_tr_b16 v[194:195],v32 offset:25088
ds_read_b64_tr_b16 v[196:197],v30 offset:28672
ds_read_b64_tr_b16 v[198:199],v30 offset:29184
ds_read_b64_tr_b16 v[200:201],v31 offset:28672
ds_read_b64_tr_b16 v[202:203],v31 offset:29184
ds_read_b64_tr_b16 v[204:205],v32 offset:28672
ds_read_b64_tr_b16 v[206:207],v32 offset:29184
s_waitcnt lgkmcnt(12)
v_mfma_f32_16x16x32_bf16 v[16:19],v[160:163],v[84:87],v[16:19]
v_mfma_f32_16x16x32_bf16 v[20:23],v[164:167],v[84:87],v[20:23]
v_mfma_f32_16x16x32_bf16 v[24:27],v[168:171],v[84:87],v[24:27]
v_mfma_f32_16x16x32_bf16 v[16:19],v[172:175],v[84:87],v[16:19]
v_mfma_f32_16x16x32_bf16 v[20:23],v[176:179],v[84:87],v[20:23]
v_mfma_f32_16x16x32_bf16 v[24:27],v[180:183],v[84:87],v[24:27]
s_waitcnt vmcnt(7)
s_barrier
ds_read_b64_tr_b16 v[208:209],v40 offset:0
ds_read_b64_tr_b16 v[210:211],v40 offset:512
ds_read_b64_tr_b16 v[212:213],v41 offset:0
ds_read_b64_tr_b16 v[214:215],v41 offset:512
ds_read_b64_tr_b16 v[216:217],v42 offset:0
ds_read_b64_tr_b16 v[218:219],v42 offset:512
ds_read_b64_tr_b16 v[220:221],v40 offset:4096
ds_read_b64_tr_b16 v[222:223],v40 offset:4608
ds_read_b64_tr_b16 v[224:225],v41 offset:4096
ds_read_b64_tr_b16 v[226:227],v41 offset:4608
ds_read_b64_tr_b16 v[228:229],v42 offset:4096
ds_read_b64_tr_b16 v[230:231],v42 offset:4608
s_waitcnt lgkmcnt(12)
v_mfma_f32_16x16x32_bf16 v[16:19],v[184:187],v[84:87],v[16:19]
v_mfma_f32_16x16x32_bf16 v[20:23],v[188:191],v[84:87],v[20:23]
v_mfma_f32_16x16x32_bf16 v[24:27],v[192:195],v[84:87],v[24:27]
v_mfma_f32_16x16x32_bf16 v[16:19],v[196:199],v[84:87],v[16:19]
v_mfma_f32_16x16x32_bf16 v[20:23],v[200:203],v[84:87],v[20:23]
v_mfma_f32_16x16x32_bf16 v[24:27],v[204:207],v[84:87],v[24:27]
s_waitcnt vmcnt(1)
s_barrier
ds_read_b64_tr_b16 v[232:233],v40 offset:24576
ds_read_b64_tr_b16 v[234:235],v40 offset:25088
ds_read_b64_tr_b16 v[236:237],v41 offset:24576
ds_read_b64_tr_b16 v[238:239],v41 offset:25088
ds_read_b64_tr_b16 v[240:241],v42 offset:24576
ds_read_b64_tr_b16 v[242:243],v42 offset:25088
ds_read_b64_tr_b16 v[244:245],v40 offset:28672
ds_read_b64_tr_b16 v[246:247],v40 offset:29184
ds_read_b64_tr_b16 v[248:249],v41 offset:28672
ds_read_b64_tr_b16 v[250:251],v41 offset:29184
ds_read_b64_tr_b16 v[252:253],v42 offset:28672
ds_read_b64_tr_b16 v[254:255],v42 offset:29184
s_waitcnt lgkmcnt(12))"
R"(
v_mfma_f32_16x16x32_bf16 v[16:19],v[208:211],v[84:87],v[16:19]
v_mfma_f32_16x16x32_bf16 v[20:23],v[212:215],v[84:87],v[20:23]
v_mfma_f32_16x16x32_bf16 v[24:27],v[216:219],v[84:87],v[24:27]
v_mfma_f32_16x16x32_bf16 v[16:19],v[220:223],v[84:87],v[16:19]
v_mfma_f32_16x16x32_bf16 v[20:23],v[224:227],v[84:87],v[20:23]
v_mfma_f32_16x16x32_bf16 v[24:27],v[228:231],v[84:87],v[24:27]
s_waitcnt lgkmcnt(0)
v_mfma_f32_16x16x32_bf16 v[16:19],v[232:235],v[84:87],v[16:19]
v_mfma_f32_16x16x32_bf16 v[20:23],v[236:239],v[84:87],v[20:23]
v_mfma_f32_16x16x32_bf16 v[24:27],v[240:243],v[84:87],v[24:27]
v_mfma_f32_16x16x32_bf16 v[16:19],v[244:247],v[84:87],v[16:19]
v_mfma_f32_16x16x32_bf16 v[20:23],v[248:251],v[84:87],v[20:23]
v_mfma_f32_16x16x32_bf16 v[24:27],v[252:255],v[84:87],v[24:27]
s_waitcnt lgkmcnt(0)
s_barrier
Lg3_init_%=:
v_mov_b32 v5,s60
s_mov_b32 s59,536870912
s_add_u32 s57,s56,131072
s_mov_b32 s79,0
s_mov_b32 s75,0x3a800000
s_mov_b32 s76,0xc0135761
s_mov_b32 s77,0x3d372713
s_mov_b32 s44,s59
s_mov_b32 s45,s59
s_mov_b32 s46,s59
s_mov_b32 s47,s59
s_mov_b32 s48,s83
s_add_u32 s49,s48,s84
s_add_u32 s50,s49,s55
s_cmp_eq_u32 s50,s56
s_cselect_b32 s50,s57,s50
s_cmp_eq_u32 s50,s58
s_cselect_b32 s50,s59,s50
s_add_u32 s51,s50,s55
s_cmp_eq_u32 s51,s56
s_cselect_b32 s51,s57,s51
s_cmp_eq_u32 s51,s58
s_cselect_b32 s51,s59,s51
s_add_u32 s52,s51,s55
s_cmp_eq_u32 s52,s56
s_cselect_b32 s52,s57,s52
s_cmp_eq_u32 s52,s58
s_cselect_b32 s52,s59,s52
s_lshl_b32 s63,s54,12
s_add_u32 s63,s63,65536
s_add_u32 s64,s63,32768
s_lshl_b32 s88,s54,10
s_add_u32 s88,s88,131072
s_lshl_b32 s78,s54,11
s_add_u32 s78,s78,132608
v_add_u32 v1,16384,v0
v_mov_b32 v12,0x3727c5ac
s_getpc_b64 s[90:91]
Lg3_pc_%=:
s_mov_b32 s68,Lg3_d1w0_%=-Lg3_pc_%=
s_cmp_eq_u32 s54,1
s_cselect_b32 s68,Lg3_d1w1_%=-Lg3_pc_%=,s68
s_cmp_eq_u32 s54,2
s_cselect_b32 s68,Lg3_d1w2_%=-Lg3_pc_%=,s68
s_cmp_eq_u32 s54,3
s_cselect_b32 s68,Lg3_d1w3_%=-Lg3_pc_%=,s68
s_add_u32 s92,s90,s68
s_addc_u32 s93,s91,0
s_mov_b32 s68,Lg3_d0w0_%=-Lg3_pc_%=
s_cmp_eq_u32 s54,1
s_cselect_b32 s68,Lg3_d0w1_%=-Lg3_pc_%=,s68
s_cmp_eq_u32 s54,2
s_cselect_b32 s68,Lg3_d0w2_%=-Lg3_pc_%=,s68
s_cmp_eq_u32 s54,3
s_cselect_b32 s68,Lg3_d0w3_%=-Lg3_pc_%=,s68
s_add_u32 s90,s90,s68
s_addc_u32 s91,s91,0
s_mov_b32 s66,s59
s_mov_b32 s65,0
s_mov_b32 s62,s63
s_branch Lg3_slow0_%=
Lg3_d0w0_%=:
v_mfma_f32_16x16x32_bf16 v[36:39],a[8:11],v[52:55],0
s_mov_b32 m0,s62
ds_read_b128 v[72:75],v2 offset:5120
v_mul_f32 v117,s76,v112
v_mfma_f32_16x16x32_bf16 v[36:39],a[20:23],v[56:59],v[36:39]
buffer_load_dwordx4 v0,s[36:39],s61 offen offset:0 sc1 lds
v_mfma_f32_16x16x32_bf16 v[32:35],a[16:19],v[56:59],0
s_mul_i32 s73,s47,7
v_mul_f32 v118,v112,v112
v_mul_f32 v119,v113,v112
v_mfma_f32_16x16x32_bf16 v[32:35],a[4:7],v[52:55],v[32:35]
s_lshr_b32 s73,s73,1
ds_read_b128 v[76:79],v2 offset:6144
v_mul_f32 v118,s77,v118
v_mfma_f32_16x16x32_bf16 v[28:31],a[0:3],v[52:55],0
s_add_u32 s53,s52,s55
v_mul_f32 v118,v118,v117
v_fma_f32 v40,-v116,v16,v40
v_mfma_f32_16x16x32_bf16 v[28:31],a[12:15],v[56:59],v[28:31]
s_waitcnt lgkmcnt(3)
buffer_load_dwordx4 v0,s[36:39],s61 offen offset:1024 sc1 lds
v_mfma_f32_16x16x32_bf16 v[28:31],a[24:27],v[60:63],v[28:31]
s_cmp_eq_u32 s53,s56
ds_read_b128 v[80:83],v2 offset:7168
v_fma_f32 v41,-v116,v17,v41
v_mfma_f32_16x16x32_bf16 v[28:31],a[36:39],v[64:67],v[28:31]
s_cselect_b32 s53,s57,s53
v_fma_f32 v42,-v116,v18,v42
v_fma_f32 v43,-v116,v19,v43
v_mfma_f32_16x16x32_bf16 v[32:35],a[40:43],v[64:67],v[32:35]
s_cmp_eq_u32 s53,s58
v_mul_f32 v84,v40,v40
v_mul_f32 v85,v41,v41
v_mfma_f32_16x16x32_bf16 v[32:35],a[28:31],v[60:63],v[32:35]
buffer_load_dwordx4 v0,s[36:39],s61 offen offset:2048 sc1 lds
v_mfma_f32_16x16x32_bf16 v[36:39],a[32:35],v[60:63],v[36:39]
s_cselect_b32 s53,s59,s53
ds_read_b128 v[52:55],v2 offset:8192
v_mul_f32 v86,v42,v42
v_mfma_f32_16x16x32_bf16 v[36:39],a[44:47],v[64:67],v[36:39]
s_waitcnt lgkmcnt(3)
v_mul_f32 v87,v43,v43
v_fma_f32 v84,v118,v84,v117
v_mfma_f32_16x16x32_bf16 v[36:39],a[56:59],v[68:71],v[36:39]
s_cmp_ge_u32 s66,s59
ds_read_b128 v[56:59],v2 offset:9216
v_mul_f32 v84,v40,v84
v_mfma_f32_16x16x32_bf16 v[36:39],a[68:71],v[72:75],v[36:39]
buffer_load_dwordx4 v0,s[36:39],s61 offen offset:3072 sc1 lds
v_mfma_f32_16x16x32_bf16 v[32:35],a[64:67],v[72:75],v[32:35]
s_cselect_b32 s86,s49,s50
v_exp_f32 v84,v84
v_mfma_f32_16x16x32_bf16 v[32:35],a[52:55],v[68:71],v[32:35]
s_lshr_b32 s68,s86,5
ds_read_b128 v[60:63],v2 offset:10240
v_fma_f32 v84,v84,v119,v119
v_mfma_f32_16x16x32_bf16 v[28:31],a[48:51],v[68:71],v[28:31]
s_add_u32 m0,s78,0
v_rcp_f32 v84,v84
v_mfma_f32_16x16x32_bf16 v[28:31],a[60:63],v[72:75],v[28:31]
s_waitcnt lgkmcnt(3)
v_fma_f32 v85,v118,v85,v117
v_mul_f32 v85,v41,v85
v_mfma_f32_16x16x32_bf16 v[28:31],a[72:75],v[76:79],v[28:31]
ds_read_b128 v[64:67],v2 offset:11264
v_fma_f32 v86,v118,v86,v117
v_mfma_f32_16x16x32_bf16 v[28:31],a[84:87],v[80:83],v[28:31]
v_exp_f32 v85,v85
v_mfma_f32_16x16x32_bf16 v[32:35],a[88:91],v[80:83],v[32:35]
v_mul_f32 v86,v42,v86
v_fma_f32 v85,v85,v119,v119
v_mfma_f32_16x16x32_bf16 v[32:35],a[76:79],v[76:79],v[32:35]
ds_read_b128 v[68:71],v2 offset:12288
v_fma_f32 v87,v118,v87,v117
v_mfma_f32_16x16x32_bf16 v[36:39],a[80:83],v[76:79],v[36:39]
v_exp_f32 v86,v86
v_mfma_f32_16x16x32_bf16 v[36:39],a[92:95],v[80:83],v[36:39]
s_waitcnt lgkmcnt(3)
v_rcp_f32 v85,v85
v_mfma_f32_16x16x32_bf16 v[36:39],a[104:107],v[52:55],v[36:39]
ds_read_b128 v[72:75],v2 offset:13312
v_mul_f32 v87,v43,v87
v_mfma_f32_16x16x32_bf16 v[36:39],a[116:119],v[56:59],v[36:39]
v_exp_f32 v87,v87
v_mfma_f32_16x16x32_bf16 v[32:35],a[112:115],v[56:59],v[32:35]
v_fma_f32 v86,v86,v119,v119
v_fma_f32 v87,v87,v119,v119
v_mfma_f32_16x16x32_bf16 v[32:35],a[100:103],v[52:55],v[32:35]
ds_read_b128 v[76:79],v2 offset:14336
v_mul_f32 v40,v40,v84
v_mfma_f32_16x16x32_bf16 v[28:31],a[96:99],v[52:55],v[28:31]
v_rcp_f32 v86,v86
v_mfma_f32_16x16x32_bf16 v[28:31],a[108:111],v[56:59],v[28:31]
s_waitcnt lgkmcnt(3)
v_rcp_f32 v87,v87
v_mfma_f32_16x16x32_bf16 v[28:31],a[120:123],v[60:63],v[28:31]
s_waitcnt vmcnt(6)
ds_read_b128 v[80:83],v2 offset:15360
v_mul_f32 v41,v41,v85
v_mfma_f32_16x16x32_bf16 v[28:31],a[132:135],v[64:67],v[28:31]
s_barrier
v_mfma_f32_16x16x32_bf16 v[32:35],a[136:139],v[64:67],v[32:35]
v_add_f32_dpp v104,v104,v104 quad_perm:[1,0,3,2] row_mask:0xf bank_mask:0xf
v_mul_f32 v42,v42,v86
v_mfma_f32_16x16x32_bf16 v[32:35],a[124:127],v[60:63],v[32:35]
ds_read_b128 v[52:55],v2 offset:16384
v_add_f32_dpp v104,v104,v104 quad_perm:[2,3,0,1] row_mask:0xf bank_mask:0xf
v_mfma_f32_16x16x32_bf16 v[36:39],a[128:131],v[60:63],v[36:39]
s_nop 0
v_add_f32_dpp v104,v104,v104 row_half_mirror row_mask:0xf bank_mask:0xf
v_mul_f32 v43,v43,v87
v_mfma_f32_16x16x32_bf16 v[36:39],a[140:143],v[64:67],v[36:39]
s_waitcnt lgkmcnt(3)
v_cvt_pk_bf16_f32 v96,v40,v41
v_cvt_pk_bf16_f32 v97,v42,v43
v_mfma_f32_16x16x32_bf16 v[36:39],a[152:155],v[68:71],v[36:39]
ds_read_b128 v[56:59],v2 offset:17408
v_fma_f32 v44,-v116,v20,v44
v_mfma_f32_16x16x32_bf16 v[36:39],a[164:167],v[72:75],v[36:39]
v_fma_f32 v45,-v116,v21,v45
v_fma_f32 v46,-v116,v22,v46
v_mfma_f32_16x16x32_bf16 v[32:35],a[160:163],v[72:75],v[32:35])"
R"(
v_fma_f32 v47,-v116,v23,v47
v_mul_f32 v88,v44,v44
v_mfma_f32_16x16x32_bf16 v[32:35],a[148:151],v[68:71],v[32:35]
ds_read_b128 v[60:63],v2 offset:18432
v_mul_f32 v89,v45,v45
v_mfma_f32_16x16x32_bf16 v[28:31],a[144:147],v[68:71],v[28:31]
buffer_load_dword v6,s[36:39],s68 offen offset:0 sc1 lds
v_mfma_f32_16x16x32_bf16 v[28:31],a[156:159],v[72:75],v[28:31]
s_waitcnt lgkmcnt(3)
v_mul_f32 v90,v46,v46
v_mul_f32 v91,v47,v47
v_mfma_f32_16x16x32_bf16 v[28:31],a[168:171],v[76:79],v[28:31]
s_add_u32 m0,s62,16384
ds_read_b128 v[64:67],v2 offset:19456
v_fma_f32 v88,v118,v88,v117
v_mfma_f32_16x16x32_bf16 v[28:31],a[180:183],v[80:83],v[28:31]
buffer_load_dwordx4 v1,s[36:39],s61 offen offset:0 sc1 lds
v_mfma_f32_16x16x32_bf16 v[32:35],a[184:187],v[80:83],v[32:35]
s_lshr_b32 s72,s61,5
v_mul_f32 v88,v44,v88
v_fma_f32 v89,v118,v89,v117
v_mfma_f32_16x16x32_bf16 v[32:35],a[172:175],v[76:79],v[32:35]
s_mov_b32 s62,s63
ds_read_b128 v[68:71],v2 offset:20480
v_mul_f32 v89,v45,v89
v_mfma_f32_16x16x32_bf16 v[36:39],a[176:179],v[76:79],v[36:39]
ds_write_b32 v10,v104 offset:0
v_mfma_f32_16x16x32_bf16 v[36:39],a[188:191],v[80:83],v[36:39]
s_waitcnt lgkmcnt(4)
buffer_load_dwordx4 v1,s[36:39],s61 offen offset:1024 sc1 lds
v_mfma_f32_16x16x32_bf16 v[36:39],a[200:203],v[52:55],v[36:39]
ds_read_b128 v[72:75],v2 offset:21504
v_fma_f32 v90,v118,v90,v117
v_mfma_f32_16x16x32_bf16 v[36:39],a[212:215],v[56:59],v[36:39]
v_exp_f32 v88,v88
v_mfma_f32_16x16x32_bf16 v[32:35],a[208:211],v[56:59],v[32:35]
v_exp_f32 v89,v89
v_mfma_f32_16x16x32_bf16 v[32:35],a[196:199],v[52:55],v[32:35]
buffer_load_dwordx4 v1,s[36:39],s61 offen offset:2048 sc1 lds
v_mfma_f32_16x16x32_bf16 v[28:31],a[192:195],v[52:55],v[28:31]
ds_read_b128 v[76:79],v2 offset:22528
v_mul_f32 v90,v46,v90
v_mfma_f32_16x16x32_bf16 v[28:31],a[204:207],v[56:59],v[28:31]
s_waitcnt lgkmcnt(4)
v_exp_f32 v90,v90
v_mfma_f32_16x16x32_bf16 v[28:31],a[216:219],v[60:63],v[28:31]
ds_read_b128 v[80:83],v2 offset:23552
v_fma_f32 v88,v88,v119,v119
v_mfma_f32_16x16x32_bf16 v[28:31],a[228:231],v[64:67],v[28:31]
buffer_load_dwordx4 v1,s[36:39],s61 offen offset:3072 sc1 lds
v_mfma_f32_16x16x32_bf16 v[32:35],a[232:235],v[64:67],v[32:35]
buffer_load_dword v108,v8,s[36:39],s72 offen offset:0 sc1
v_mfma_f32_16x16x32_bf16 v[32:35],a[220:223],v[60:63],v[32:35]
ds_read_b128 v[52:55],v2 offset:24576
v_fma_f32 v89,v89,v119,v119
v_mfma_f32_16x16x32_bf16 v[36:39],a[224:227],v[60:63],v[36:39]
v_rcp_f32 v88,v88
v_mfma_f32_16x16x32_bf16 v[36:39],a[236:239],v[64:67],v[36:39]
s_waitcnt lgkmcnt(3)
v_rcp_f32 v89,v89
v_mfma_f32_16x16x32_bf16 v[36:39],a[248:251],v[68:71],v[36:39]
ds_read_b128 v[56:59],v2 offset:25600
v_fma_f32 v90,v90,v119,v119
v_mfma_f32_16x16x32_bf16 v[36:39],v[132:135],v[72:75],v[36:39]
v_rcp_f32 v90,v90
v_mfma_f32_16x16x32_bf16 v[32:35],v[128:131],v[72:75],v[32:35]
v_fma_f32 v91,v118,v91,v117
v_mul_f32 v91,v47,v91
v_mfma_f32_16x16x32_bf16 v[32:35],a[244:247],v[68:71],v[32:35]
ds_read_b128 v[60:63],v2 offset:26624
v_mul_f32 v44,v44,v88
v_mfma_f32_16x16x32_bf16 v[28:31],a[240:243],v[68:71],v[28:31]
v_exp_f32 v91,v91
v_mfma_f32_16x16x32_bf16 v[28:31],a[252:255],v[72:75],v[28:31]
s_waitcnt lgkmcnt(3)
v_fma_f32 v91,v91,v119,v119
v_mul_f32 v45,v45,v89
v_mfma_f32_16x16x32_bf16 v[28:31],v[136:139],v[76:79],v[28:31]
ds_read_b128 v[64:67],v2 offset:27648
v_mul_f32 v46,v46,v90
v_mfma_f32_16x16x32_bf16 v[28:31],v[148:151],v[80:83],v[28:31]
v_rcp_f32 v91,v91
v_mfma_f32_16x16x32_bf16 v[32:35],v[152:155],v[80:83],v[32:35]
v_mul_f32 v47,v47,v91
v_cvt_pk_bf16_f32 v98,v44,v45
v_mfma_f32_16x16x32_bf16 v[32:35],v[140:143],v[76:79],v[32:35]
ds_read_b128 v[68:71],v2 offset:28672
v_cvt_pk_bf16_f32 v99,v46,v47
v_mfma_f32_16x16x32_bf16 v[36:39],v[144:147],v[76:79],v[36:39]
v_fma_f32 v48,-v116,v24,v48
v_fma_f32 v49,-v116,v25,v49
v_mfma_f32_16x16x32_bf16 v[36:39],v[156:159],v[80:83],v[36:39]
s_waitcnt lgkmcnt(3)
v_fma_f32 v50,-v116,v26,v50
v_fma_f32 v51,-v116,v27,v51
v_mfma_f32_16x16x32_bf16 v[36:39],v[168:171],v[52:55],v[36:39]
ds_read_b128 v[72:75],v2 offset:29696
v_mul_f32 v92,v48,v48
v_mfma_f32_16x16x32_bf16 v[36:39],v[180:183],v[56:59],v[36:39]
v_mul_f32 v93,v49,v49
v_mul_f32 v94,v50,v50
v_mfma_f32_16x16x32_bf16 v[32:35],v[176:179],v[56:59],v[32:35]
v_mul_f32 v95,v51,v51
v_fma_f32 v92,v118,v92,v117
v_mfma_f32_16x16x32_bf16 v[32:35],v[164:167],v[52:55],v[32:35]
ds_read_b128 v[76:79],v2 offset:30720
v_mul_f32 v92,v48,v92
v_mfma_f32_16x16x32_bf16 v[28:31],v[160:163],v[52:55],v[28:31]
buffer_store_dwordx4 v[96:99],v3,s[36:39],s73 offen offset:0 sc1
v_exp_f32 v92,v92
v_fma_f32 v93,v118,v93,v117
v_mfma_f32_16x16x32_bf16 v[28:31],v[172:175],v[56:59],v[28:31]
s_waitcnt lgkmcnt(3)
v_mul_f32 v93,v49,v93
v_fma_f32 v92,v92,v119,v119
v_mfma_f32_16x16x32_bf16 v[28:31],v[184:187],v[60:63],v[28:31]
s_waitcnt vmcnt(6)
ds_read_b128 v[80:83],v2 offset:31744
v_fma_f32 v94,v118,v94,v117
v_mfma_f32_16x16x32_bf16 v[28:31],v[196:199],v[64:67],v[28:31]
s_barrier
v_mfma_f32_16x16x32_bf16 v[32:35],v[200:203],v[64:67],v[32:35]
ds_read_b32 v13,v7 offset:0
ds_read_b64 v[112:113],v11 offset:0
v_mfma_f32_16x16x32_bf16 v[32:35],v[188:191],v[60:63],v[32:35]
ds_read_b128 v[52:55],v2 offset:32768
v_fma_f32 v95,v118,v95,v117
v_mfma_f32_16x16x32_bf16 v[36:39],v[192:195],v[60:63],v[36:39]
v_exp_f32 v93,v93
v_mfma_f32_16x16x32_bf16 v[36:39],v[204:207],v[64:67],v[36:39]
s_waitcnt lgkmcnt(5)
v_rcp_f32 v92,v92
v_mfma_f32_16x16x32_bf16 v[36:39],v[216:219],v[68:71],v[36:39]
s_waitcnt lgkmcnt(2)
ds_read_b128 v[56:59],v2 offset:33792
v_mul_f32 v94,v50,v94
v_mfma_f32_16x16x32_bf16 v[36:39],v[228:231],v[72:75],v[36:39]
s_waitcnt lgkmcnt(2)
v_cmp_eq_u32 vcc,s60,v13
v_exp_f32 v94,v94
v_mul_f32 v95,v51,v95
v_mfma_f32_16x16x32_bf16 v[32:35],v[224:227],v[72:75],v[32:35]
s_cmp_eq_u32 vcc_lo,-1
v_exp_f32 v95,v95
v_mfma_f32_16x16x32_bf16 v[32:35],v[212:215],v[68:71],v[32:35]
s_cselect_b32 s67,s86,s59
ds_read_b128 v[60:63],v2 offset:34816
v_fma_f32 v93,v93,v119,v119
v_mfma_f32_16x16x32_bf16 v[28:31],v[208:211],v[68:71],v[28:31]
s_lshr_b32 s61,s67,0
v_rcp_f32 v93,v93
v_mfma_f32_16x16x32_bf16 v[28:31],v[220:223],v[72:75],v[28:31]
v_fma_f32 v94,v94,v119,v119
v_fma_f32 v95,v95,v119,v119
v_mfma_f32_16x16x32_bf16 v[28:31],v[232:235],v[76:79],v[28:31]
s_xor_b32 s63,s63,65536
ds_read_b128 v[64:67],v2 offset:35840
v_mul_f32 v48,v48,v92
v_mfma_f32_16x16x32_bf16 v[28:31],v[244:247],v[80:83],v[28:31]
s_cmp_ge_u32 s66,s59
v_rcp_f32 v94,v94
v_mfma_f32_16x16x32_bf16 v[32:35],v[248:251],v[80:83],v[32:35]
v_rcp_f32 v95,v95
v_mfma_f32_16x16x32_bf16 v[32:35],v[236:239],v[76:79],v[32:35]
v_mul_f32 v49,v49,v93
v_mul_f32 v50,v50,v94
v_mul_f32 v51,v51,v95
v_cvt_pk_bf16_f32 v100,v48,v49
v_cvt_pk_bf16_f32 v101,v50,v51
v_mfma_f32_16x16x32_bf16 v[36:39],v[240:243],v[76:79],v[36:39]
s_waitcnt lgkmcnt(2)
ds_read_b128 v[68:71],v2 offset:36864
buffer_store_dwordx2 v[100:101],v9,s[36:39],s73 offen offset:0 sc1
v_mul_f32 v116,s75,v112
v_fma_f32 v113,v113,s75,v12
v_fma_f32 v113,-v116,v116,v113
v_max_f32 v113,v113,v12
v_rsq_f32 v112,v113
v_mfma_f32_16x16x32_bf16 v[36:39],v[252:255],v[80:83],v[36:39]
s_cbranch_scc1 Lg3_slow1_%=
Lg3_d1w0_%=:
v_mfma_f32_16x16x32_bf16 v[48:51],a[8:11],v[52:55],0
s_mov_b32 m0,s62
ds_read_b128 v[72:75],v2 offset:37888
v_mul_f32 v117,s76,v112
v_mfma_f32_16x16x32_bf16 v[48:51],a[20:23],v[56:59],v[48:51]
buffer_load_dwordx4 v0,s[36:39],s61 offen offset:0 sc1 lds
v_mfma_f32_16x16x32_bf16 v[44:47],a[16:19],v[56:59],0
s_mul_i32 s73,s48,7
v_mul_f32 v118,v112,v112
v_mul_f32 v119,v113,v112
v_mfma_f32_16x16x32_bf16 v[44:47],a[4:7],v[52:55],v[44:47]
s_lshr_b32 s73,s73,1
ds_read_b128 v[76:79],v2 offset:38912
v_mul_f32 v118,s77,v118
v_mul_f32 v118,v118,v117
v_mfma_f32_16x16x32_bf16 v[40:43],a[0:3],v[52:55],0
s_add_u32 s74,s53,s55
v_fma_f32 v28,-v116,v16,v28
v_fma_f32 v29,-v116,v17,v29)"
R"(
v_mfma_f32_16x16x32_bf16 v[40:43],a[12:15],v[56:59],v[40:43]
s_waitcnt lgkmcnt(3)
buffer_load_dwordx4 v0,s[36:39],s61 offen offset:1024 sc1 lds
v_mfma_f32_16x16x32_bf16 v[40:43],a[24:27],v[60:63],v[40:43]
s_cmp_eq_u32 s74,s56
ds_read_b128 v[80:83],v2 offset:39936
v_fma_f32 v30,-v116,v18,v30
v_mfma_f32_16x16x32_bf16 v[40:43],a[36:39],v[64:67],v[40:43]
s_cselect_b32 s74,s57,s74
v_fma_f32 v31,-v116,v19,v31
v_mul_f32 v84,v28,v28
v_mfma_f32_16x16x32_bf16 v[44:47],a[40:43],v[64:67],v[44:47]
s_cmp_eq_u32 s74,s58
v_mul_f32 v85,v29,v29
v_mul_f32 v86,v30,v30
v_mfma_f32_16x16x32_bf16 v[44:47],a[28:31],v[60:63],v[44:47]
buffer_load_dwordx4 v0,s[36:39],s61 offen offset:2048 sc1 lds
v_mfma_f32_16x16x32_bf16 v[48:51],a[32:35],v[60:63],v[48:51]
s_cselect_b32 s74,s59,s74
ds_read_b128 v[52:55],v2 offset:40960
v_mul_f32 v87,v31,v31
v_mfma_f32_16x16x32_bf16 v[48:51],a[44:47],v[64:67],v[48:51]
s_waitcnt lgkmcnt(3)
v_fma_f32 v84,v118,v84,v117
v_mul_f32 v84,v28,v84
v_mfma_f32_16x16x32_bf16 v[48:51],a[56:59],v[68:71],v[48:51]
s_cmp_ge_u32 s67,s59
ds_read_b128 v[56:59],v2 offset:41984
v_fma_f32 v85,v118,v85,v117
v_mfma_f32_16x16x32_bf16 v[48:51],a[68:71],v[72:75],v[48:51]
buffer_load_dwordx4 v0,s[36:39],s61 offen offset:3072 sc1 lds
v_mfma_f32_16x16x32_bf16 v[44:47],a[64:67],v[72:75],v[44:47]
s_cselect_b32 s86,s50,s51
v_exp_f32 v84,v84
v_mfma_f32_16x16x32_bf16 v[44:47],a[52:55],v[68:71],v[44:47]
s_lshr_b32 s68,s86,5
ds_read_b128 v[60:63],v2 offset:43008
v_mul_f32 v85,v29,v85
v_mfma_f32_16x16x32_bf16 v[40:43],a[48:51],v[68:71],v[40:43]
s_add_u32 m0,s78,256
v_exp_f32 v85,v85
v_mfma_f32_16x16x32_bf16 v[40:43],a[60:63],v[72:75],v[40:43]
s_waitcnt lgkmcnt(3)
v_fma_f32 v84,v84,v119,v119
v_fma_f32 v85,v85,v119,v119
v_mfma_f32_16x16x32_bf16 v[40:43],a[72:75],v[76:79],v[40:43]
s_mov_b64 s[44:45],s[46:47]
ds_read_b128 v[64:67],v2 offset:44032
v_fma_f32 v86,v118,v86,v117
v_mfma_f32_16x16x32_bf16 v[40:43],a[84:87],v[80:83],v[40:43]
s_mov_b64 s[46:47],s[48:49]
v_rcp_f32 v84,v84
v_mfma_f32_16x16x32_bf16 v[44:47],a[88:91],v[80:83],v[44:47]
s_mov_b64 s[48:49],s[50:51]
v_rcp_f32 v85,v85
v_mfma_f32_16x16x32_bf16 v[44:47],a[76:79],v[76:79],v[44:47]
s_mov_b64 s[50:51],s[52:53]
ds_read_b128 v[68:71],v2 offset:45056
v_mul_f32 v86,v30,v86
v_mfma_f32_16x16x32_bf16 v[48:51],a[80:83],v[76:79],v[48:51]
s_mov_b32 s52,s74
v_exp_f32 v86,v86
v_mfma_f32_16x16x32_bf16 v[48:51],a[92:95],v[80:83],v[48:51]
s_waitcnt lgkmcnt(3)
v_fma_f32 v86,v86,v119,v119
v_fma_f32 v87,v118,v87,v117
v_mfma_f32_16x16x32_bf16 v[48:51],a[104:107],v[52:55],v[48:51]
ds_read_b128 v[72:75],v2 offset:46080
v_mul_f32 v87,v31,v87
v_mfma_f32_16x16x32_bf16 v[48:51],a[116:119],v[56:59],v[48:51]
v_exp_f32 v87,v87
v_mfma_f32_16x16x32_bf16 v[44:47],a[112:115],v[56:59],v[44:47]
v_rcp_f32 v86,v86
v_mfma_f32_16x16x32_bf16 v[44:47],a[100:103],v[52:55],v[44:47]
ds_read_b128 v[76:79],v2 offset:47104
v_fma_f32 v87,v87,v119,v119
v_mfma_f32_16x16x32_bf16 v[40:43],a[96:99],v[52:55],v[40:43]
v_rcp_f32 v87,v87
v_mfma_f32_16x16x32_bf16 v[40:43],a[108:111],v[56:59],v[40:43]
s_waitcnt lgkmcnt(3)
v_mul_f32 v28,v28,v84
v_mul_f32 v29,v29,v85
v_mfma_f32_16x16x32_bf16 v[40:43],a[120:123],v[60:63],v[40:43]
s_waitcnt vmcnt(6)
ds_read_b128 v[80:83],v2 offset:48128
v_mul_f32 v30,v30,v86
v_mfma_f32_16x16x32_bf16 v[40:43],a[132:135],v[64:67],v[40:43]
s_barrier
v_mfma_f32_16x16x32_bf16 v[44:47],a[136:139],v[64:67],v[44:47]
v_add_f32_dpp v108,v108,v108 quad_perm:[1,0,3,2] row_mask:0xf bank_mask:0xf
v_mul_f32 v31,v31,v87
v_mfma_f32_16x16x32_bf16 v[44:47],a[124:127],v[60:63],v[44:47]
ds_read_b128 v[52:55],v2 offset:49152
v_add_f32_dpp v108,v108,v108 quad_perm:[2,3,0,1] row_mask:0xf bank_mask:0xf
v_mfma_f32_16x16x32_bf16 v[48:51],a[128:131],v[60:63],v[48:51]
s_nop 0
v_add_f32_dpp v108,v108,v108 row_half_mirror row_mask:0xf bank_mask:0xf
v_cvt_pk_bf16_f32 v96,v28,v29
v_mfma_f32_16x16x32_bf16 v[48:51],a[140:143],v[64:67],v[48:51]
s_waitcnt lgkmcnt(3)
v_cvt_pk_bf16_f32 v97,v30,v31
v_fma_f32 v32,-v116,v20,v32
v_mfma_f32_16x16x32_bf16 v[48:51],a[152:155],v[68:71],v[48:51]
ds_read_b128 v[56:59],v2 offset:50176
v_fma_f32 v33,-v116,v21,v33
v_mfma_f32_16x16x32_bf16 v[48:51],a[164:167],v[72:75],v[48:51]
v_fma_f32 v34,-v116,v22,v34
v_fma_f32 v35,-v116,v23,v35
v_mfma_f32_16x16x32_bf16 v[44:47],a[160:163],v[72:75],v[44:47]
v_mul_f32 v88,v32,v32
v_mul_f32 v89,v33,v33
v_mfma_f32_16x16x32_bf16 v[44:47],a[148:151],v[68:71],v[44:47]
ds_read_b128 v[60:63],v2 offset:51200
v_mul_f32 v90,v34,v34
v_mfma_f32_16x16x32_bf16 v[40:43],a[144:147],v[68:71],v[40:43]
buffer_load_dword v6,s[36:39],s68 offen offset:0 sc1 lds
v_mfma_f32_16x16x32_bf16 v[40:43],a[156:159],v[72:75],v[40:43]
s_waitcnt lgkmcnt(3)
v_mul_f32 v91,v35,v35
v_fma_f32 v88,v118,v88,v117
v_mfma_f32_16x16x32_bf16 v[40:43],a[168:171],v[76:79],v[40:43]
s_add_u32 m0,s62,16384
ds_read_b128 v[64:67],v2 offset:52224
v_mul_f32 v88,v32,v88
v_mfma_f32_16x16x32_bf16 v[40:43],a[180:183],v[80:83],v[40:43]
buffer_load_dwordx4 v1,s[36:39],s61 offen offset:0 sc1 lds
v_mfma_f32_16x16x32_bf16 v[44:47],a[184:187],v[80:83],v[44:47]
s_lshr_b32 s72,s61,5
v_exp_f32 v88,v88
v_mfma_f32_16x16x32_bf16 v[44:47],a[172:175],v[76:79],v[44:47]
s_mov_b32 s62,s64
ds_read_b128 v[68:71],v2 offset:53248
v_fma_f32 v88,v88,v119,v119
v_mfma_f32_16x16x32_bf16 v[48:51],a[176:179],v[76:79],v[48:51]
ds_write_b32 v10,v108 offset:128
v_mfma_f32_16x16x32_bf16 v[48:51],a[188:191],v[80:83],v[48:51]
s_waitcnt lgkmcnt(4)
buffer_load_dwordx4 v1,s[36:39],s61 offen offset:1024 sc1 lds
v_mfma_f32_16x16x32_bf16 v[48:51],a[200:203],v[52:55],v[48:51]
ds_read_b128 v[72:75],v2 offset:54272
v_fma_f32 v89,v118,v89,v117
v_mfma_f32_16x16x32_bf16 v[48:51],a[212:215],v[56:59],v[48:51]
v_rcp_f32 v88,v88
v_mul_f32 v89,v33,v89
v_mfma_f32_16x16x32_bf16 v[44:47],a[208:211],v[56:59],v[44:47]
v_exp_f32 v89,v89
v_mfma_f32_16x16x32_bf16 v[44:47],a[196:199],v[52:55],v[44:47]
buffer_load_dwordx4 v1,s[36:39],s61 offen offset:2048 sc1 lds
v_mfma_f32_16x16x32_bf16 v[40:43],a[192:195],v[52:55],v[40:43]
ds_read_b128 v[76:79],v2 offset:55296
v_fma_f32 v89,v89,v119,v119
v_mfma_f32_16x16x32_bf16 v[40:43],a[204:207],v[56:59],v[40:43]
s_waitcnt lgkmcnt(4)
v_rcp_f32 v89,v89
v_mfma_f32_16x16x32_bf16 v[40:43],a[216:219],v[60:63],v[40:43]
ds_read_b128 v[80:83],v2 offset:56320
v_fma_f32 v90,v118,v90,v117
v_mfma_f32_16x16x32_bf16 v[40:43],a[228:231],v[64:67],v[40:43]
buffer_load_dwordx4 v1,s[36:39],s61 offen offset:3072 sc1 lds
v_mfma_f32_16x16x32_bf16 v[44:47],a[232:235],v[64:67],v[44:47]
buffer_load_dword v104,v8,s[36:39],s72 offen offset:0 sc1
v_mfma_f32_16x16x32_bf16 v[44:47],a[220:223],v[60:63],v[44:47]
ds_read_b128 v[52:55],v2 offset:57344
v_mul_f32 v90,v34,v90
v_mfma_f32_16x16x32_bf16 v[48:51],a[224:227],v[60:63],v[48:51]
v_exp_f32 v90,v90
v_mfma_f32_16x16x32_bf16 v[48:51],a[236:239],v[64:67],v[48:51]
s_waitcnt lgkmcnt(3)
v_fma_f32 v90,v90,v119,v119
v_fma_f32 v91,v118,v91,v117
v_mfma_f32_16x16x32_bf16 v[48:51],a[248:251],v[68:71],v[48:51]
ds_read_b128 v[56:59],v2 offset:58368
v_mul_f32 v91,v35,v91
v_mfma_f32_16x16x32_bf16 v[48:51],v[132:135],v[72:75],v[48:51]
v_exp_f32 v91,v91
v_mfma_f32_16x16x32_bf16 v[44:47],v[128:131],v[72:75],v[44:47]
v_rcp_f32 v90,v90
v_mfma_f32_16x16x32_bf16 v[44:47],a[244:247],v[68:71],v[44:47]
ds_read_b128 v[60:63],v2 offset:59392
v_fma_f32 v91,v91,v119,v119
v_mfma_f32_16x16x32_bf16 v[40:43],a[240:243],v[68:71],v[40:43]
v_rcp_f32 v91,v91
v_mfma_f32_16x16x32_bf16 v[40:43],a[252:255],v[72:75],v[40:43]
s_waitcnt lgkmcnt(3)
v_mul_f32 v32,v32,v88
v_mul_f32 v33,v33,v89
v_mfma_f32_16x16x32_bf16 v[40:43],v[136:139],v[76:79],v[40:43]
ds_read_b128 v[64:67],v2 offset:60416
v_mul_f32 v34,v34,v90
v_mfma_f32_16x16x32_bf16 v[40:43],v[148:151],v[80:83],v[40:43]
v_mul_f32 v35,v35,v91
v_cvt_pk_bf16_f32 v98,v32,v33
v_mfma_f32_16x16x32_bf16 v[44:47],v[152:155],v[80:83],v[44:47]
v_cvt_pk_bf16_f32 v99,v34,v35
v_fma_f32 v36,-v116,v24,v36
v_mfma_f32_16x16x32_bf16 v[44:47],v[140:143],v[76:79],v[44:47]
ds_read_b128 v[68:71],v2 offset:61440
v_fma_f32 v37,-v116,v25,v37
v_mfma_f32_16x16x32_bf16 v[48:51],v[144:147],v[76:79],v[48:51]
v_fma_f32 v38,-v116,v26,v38)"
R"(
v_fma_f32 v39,-v116,v27,v39
v_mfma_f32_16x16x32_bf16 v[48:51],v[156:159],v[80:83],v[48:51]
s_waitcnt lgkmcnt(3)
v_mul_f32 v92,v36,v36
v_mul_f32 v93,v37,v37
v_mfma_f32_16x16x32_bf16 v[48:51],v[168:171],v[52:55],v[48:51]
ds_read_b128 v[72:75],v2 offset:62464
v_mul_f32 v94,v38,v38
v_mfma_f32_16x16x32_bf16 v[48:51],v[180:183],v[56:59],v[48:51]
v_mul_f32 v95,v39,v39
v_fma_f32 v92,v118,v92,v117
v_mfma_f32_16x16x32_bf16 v[44:47],v[176:179],v[56:59],v[44:47]
v_mul_f32 v92,v36,v92
v_fma_f32 v93,v118,v93,v117
v_mfma_f32_16x16x32_bf16 v[44:47],v[164:167],v[52:55],v[44:47]
ds_read_b128 v[76:79],v2 offset:63488
v_mul_f32 v93,v37,v93
v_mfma_f32_16x16x32_bf16 v[40:43],v[160:163],v[52:55],v[40:43]
buffer_store_dwordx4 v[96:99],v3,s[36:39],s73 offen offset:0 sc1
v_exp_f32 v92,v92
v_fma_f32 v94,v118,v94,v117
v_mfma_f32_16x16x32_bf16 v[40:43],v[172:175],v[56:59],v[40:43]
s_waitcnt lgkmcnt(3)
v_exp_f32 v93,v93
v_mfma_f32_16x16x32_bf16 v[40:43],v[184:187],v[60:63],v[40:43]
s_waitcnt vmcnt(6)
ds_read_b128 v[80:83],v2 offset:64512
v_mul_f32 v94,v38,v94
v_mfma_f32_16x16x32_bf16 v[40:43],v[196:199],v[64:67],v[40:43]
s_barrier
v_mfma_f32_16x16x32_bf16 v[44:47],v[200:203],v[64:67],v[44:47]
ds_read_b32 v13,v7 offset:256
v_xor_b32 v2,65536,v2
v_mfma_f32_16x16x32_bf16 v[44:47],v[188:191],v[60:63],v[44:47]
ds_read_b128 v[52:55],v2 offset:0
ds_read_b64 v[112:113],v11 offset:128
v_mfma_f32_16x16x32_bf16 v[48:51],v[192:195],v[60:63],v[48:51]
v_exp_f32 v94,v94
v_mfma_f32_16x16x32_bf16 v[48:51],v[204:207],v[64:67],v[48:51]
s_waitcnt lgkmcnt(5)
v_fma_f32 v92,v92,v119,v119
v_fma_f32 v93,v93,v119,v119
v_mfma_f32_16x16x32_bf16 v[48:51],v[216:219],v[68:71],v[48:51]
s_waitcnt lgkmcnt(2)
ds_read_b128 v[56:59],v2 offset:1024
v_fma_f32 v95,v118,v95,v117
v_mfma_f32_16x16x32_bf16 v[48:51],v[228:231],v[72:75],v[48:51]
s_waitcnt lgkmcnt(1)
v_cmp_eq_u32 vcc,s60,v13
v_rcp_f32 v92,v92
v_mul_f32 v95,v39,v95
v_mfma_f32_16x16x32_bf16 v[44:47],v[224:227],v[72:75],v[44:47]
s_cmp_eq_u32 vcc_lo,-1
v_exp_f32 v95,v95
v_mfma_f32_16x16x32_bf16 v[44:47],v[212:215],v[68:71],v[44:47]
s_cselect_b32 s66,s86,s59
ds_read_b128 v[60:63],v2 offset:2048
v_fma_f32 v94,v94,v119,v119
v_mfma_f32_16x16x32_bf16 v[40:43],v[208:211],v[68:71],v[40:43]
s_lshr_b32 s61,s66,0
v_rcp_f32 v93,v93
v_mfma_f32_16x16x32_bf16 v[40:43],v[220:223],v[72:75],v[40:43]
v_rcp_f32 v94,v94
v_mfma_f32_16x16x32_bf16 v[40:43],v[232:235],v[76:79],v[40:43]
s_xor_b32 s64,s64,65536
ds_read_b128 v[64:67],v2 offset:3072
v_fma_f32 v95,v95,v119,v119
v_mfma_f32_16x16x32_bf16 v[40:43],v[244:247],v[80:83],v[40:43]
s_cmp_lt_u32 s67,s59
v_rcp_f32 v95,v95
v_mfma_f32_16x16x32_bf16 v[44:47],v[248:251],v[80:83],v[44:47]
v_mul_f32 v116,s75,v112
v_mul_f32 v36,v36,v92
v_mfma_f32_16x16x32_bf16 v[44:47],v[236:239],v[76:79],v[44:47]
v_mul_f32 v37,v37,v93
v_mul_f32 v38,v38,v94
v_mul_f32 v39,v39,v95
v_cvt_pk_bf16_f32 v100,v36,v37
v_cvt_pk_bf16_f32 v101,v38,v39
v_mfma_f32_16x16x32_bf16 v[48:51],v[240:243],v[76:79],v[48:51]
s_waitcnt lgkmcnt(2)
ds_read_b128 v[68:71],v2 offset:4096
buffer_store_dwordx2 v[100:101],v9,s[36:39],s73 offen offset:0 sc1
v_fma_f32 v113,v113,s75,v12
v_fma_f32 v113,-v116,v116,v113
v_max_f32 v113,v113,v12
v_rsq_f32 v112,v113
v_mfma_f32_16x16x32_bf16 v[48:51],v[252:255],v[80:83],v[48:51]
s_cbranch_scc1 Lg3_d0w0_%=
s_branch Lg3_slow0_%=
Lg3_d0w1_%=:
v_mfma_f32_16x16x32_bf16 v[36:39],a[8:11],v[52:55],0
s_mov_b32 m0,s62
ds_read_b128 v[72:75],v2 offset:5120
v_mul_f32 v117,s76,v112
v_mfma_f32_16x16x32_bf16 v[36:39],a[20:23],v[56:59],v[36:39]
s_mul_i32 s73,s47,7
v_mul_f32 v118,v112,v112
v_mul_f32 v119,v113,v112
v_mfma_f32_16x16x32_bf16 v[32:35],a[16:19],v[56:59],0
buffer_load_dwordx4 v0,s[36:39],s61 offen offset:0 sc1 lds
v_mfma_f32_16x16x32_bf16 v[32:35],a[4:7],v[52:55],v[32:35]
s_lshr_b32 s73,s73,1
ds_read_b128 v[76:79],v2 offset:6144
v_mul_f32 v118,s77,v118
v_mul_f32 v118,v118,v117
v_mfma_f32_16x16x32_bf16 v[28:31],a[0:3],v[52:55],0
s_add_u32 s53,s52,s55
v_fma_f32 v40,-v116,v16,v40
v_fma_f32 v41,-v116,v17,v41
v_mfma_f32_16x16x32_bf16 v[28:31],a[12:15],v[56:59],v[28:31]
s_waitcnt lgkmcnt(3)
v_fma_f32 v42,-v116,v18,v42
v_fma_f32 v43,-v116,v19,v43
v_mfma_f32_16x16x32_bf16 v[28:31],a[24:27],v[60:63],v[28:31]
buffer_load_dwordx4 v0,s[36:39],s61 offen offset:1024 sc1 lds
v_mfma_f32_16x16x32_bf16 v[28:31],a[36:39],v[64:67],v[28:31]
s_cmp_eq_u32 s53,s56
ds_read_b128 v[80:83],v2 offset:7168
v_mul_f32 v84,v40,v40
v_mfma_f32_16x16x32_bf16 v[32:35],a[40:43],v[64:67],v[32:35]
s_cselect_b32 s53,s57,s53
v_mul_f32 v85,v41,v41
v_mul_f32 v86,v42,v42
v_mfma_f32_16x16x32_bf16 v[32:35],a[28:31],v[60:63],v[32:35]
s_cmp_eq_u32 s53,s58
ds_read_b128 v[52:55],v2 offset:8192
v_mul_f32 v87,v43,v43
v_mfma_f32_16x16x32_bf16 v[36:39],a[32:35],v[60:63],v[36:39]
buffer_load_dwordx4 v0,s[36:39],s61 offen offset:2048 sc1 lds
v_mfma_f32_16x16x32_bf16 v[36:39],a[44:47],v[64:67],v[36:39]
s_waitcnt lgkmcnt(3)
v_fma_f32 v84,v118,v84,v117
v_mul_f32 v84,v40,v84
v_mfma_f32_16x16x32_bf16 v[36:39],a[56:59],v[68:71],v[36:39]
s_cselect_b32 s53,s59,s53
ds_read_b128 v[56:59],v2 offset:9216
v_fma_f32 v85,v118,v85,v117
v_mfma_f32_16x16x32_bf16 v[36:39],a[68:71],v[72:75],v[36:39]
s_cmp_ge_u32 s66,s59
v_exp_f32 v84,v84
v_mfma_f32_16x16x32_bf16 v[32:35],a[64:67],v[72:75],v[32:35]
buffer_load_dwordx4 v0,s[36:39],s61 offen offset:3072 sc1 lds
v_mfma_f32_16x16x32_bf16 v[32:35],a[52:55],v[68:71],v[32:35]
s_cselect_b32 s86,s49,s50
ds_read_b128 v[60:63],v2 offset:10240
v_mul_f32 v85,v41,v85
v_mfma_f32_16x16x32_bf16 v[28:31],a[48:51],v[68:71],v[28:31]
s_add_u32 m0,s62,16384
v_exp_f32 v85,v85
v_mfma_f32_16x16x32_bf16 v[28:31],a[60:63],v[72:75],v[28:31]
s_waitcnt lgkmcnt(3)
v_fma_f32 v84,v84,v119,v119
v_fma_f32 v85,v85,v119,v119
v_mfma_f32_16x16x32_bf16 v[28:31],a[72:75],v[76:79],v[28:31]
s_lshr_b32 s72,s61,5
ds_read_b128 v[64:67],v2 offset:11264
v_fma_f32 v86,v118,v86,v117
v_mfma_f32_16x16x32_bf16 v[28:31],a[84:87],v[80:83],v[28:31]
s_mov_b32 s62,s63
v_rcp_f32 v84,v84
v_mfma_f32_16x16x32_bf16 v[32:35],a[88:91],v[80:83],v[32:35]
v_rcp_f32 v85,v85
v_mfma_f32_16x16x32_bf16 v[32:35],a[76:79],v[76:79],v[32:35]
ds_read_b128 v[68:71],v2 offset:12288
v_mul_f32 v86,v42,v86
v_mfma_f32_16x16x32_bf16 v[36:39],a[80:83],v[76:79],v[36:39]
v_exp_f32 v86,v86
v_mfma_f32_16x16x32_bf16 v[36:39],a[92:95],v[80:83],v[36:39]
s_waitcnt lgkmcnt(3)
v_fma_f32 v86,v86,v119,v119
v_fma_f32 v87,v118,v87,v117
v_mfma_f32_16x16x32_bf16 v[36:39],a[104:107],v[52:55],v[36:39]
ds_read_b128 v[72:75],v2 offset:13312
v_mul_f32 v87,v43,v87
v_mfma_f32_16x16x32_bf16 v[36:39],a[116:119],v[56:59],v[36:39]
v_exp_f32 v87,v87
v_mfma_f32_16x16x32_bf16 v[32:35],a[112:115],v[56:59],v[32:35]
v_rcp_f32 v86,v86
v_mfma_f32_16x16x32_bf16 v[32:35],a[100:103],v[52:55],v[32:35]
ds_read_b128 v[76:79],v2 offset:14336
v_fma_f32 v87,v87,v119,v119
v_mfma_f32_16x16x32_bf16 v[28:31],a[96:99],v[52:55],v[28:31]
v_rcp_f32 v87,v87
v_mfma_f32_16x16x32_bf16 v[28:31],a[108:111],v[56:59],v[28:31]
s_waitcnt lgkmcnt(3)
v_mul_f32 v40,v40,v84
v_mul_f32 v41,v41,v85
v_mfma_f32_16x16x32_bf16 v[28:31],a[120:123],v[60:63],v[28:31]
s_waitcnt vmcnt(6)
ds_read_b128 v[80:83],v2 offset:15360
v_mul_f32 v42,v42,v86
v_mfma_f32_16x16x32_bf16 v[28:31],a[132:135],v[64:67],v[28:31]
s_barrier
v_mfma_f32_16x16x32_bf16 v[32:35],a[136:139],v[64:67],v[32:35]
v_add_f32_dpp v104,v104,v104 quad_perm:[1,0,3,2] row_mask:0xf bank_mask:0xf
v_mul_f32 v43,v43,v87
v_mfma_f32_16x16x32_bf16 v[32:35],a[124:127],v[60:63],v[32:35]
ds_read_b128 v[52:55],v2 offset:16384
v_add_f32_dpp v104,v104,v104 quad_perm:[2,3,0,1] row_mask:0xf bank_mask:0xf)"
R"(
v_mfma_f32_16x16x32_bf16 v[36:39],a[128:131],v[60:63],v[36:39]
s_nop 0
v_add_f32_dpp v104,v104,v104 row_half_mirror row_mask:0xf bank_mask:0xf
v_cvt_pk_bf16_f32 v96,v40,v41
v_mfma_f32_16x16x32_bf16 v[36:39],a[140:143],v[64:67],v[36:39]
s_waitcnt lgkmcnt(3)
v_cvt_pk_bf16_f32 v97,v42,v43
v_fma_f32 v44,-v116,v20,v44
v_mfma_f32_16x16x32_bf16 v[36:39],a[152:155],v[68:71],v[36:39]
ds_read_b128 v[56:59],v2 offset:17408
v_fma_f32 v45,-v116,v21,v45
v_mfma_f32_16x16x32_bf16 v[36:39],a[164:167],v[72:75],v[36:39]
v_fma_f32 v46,-v116,v22,v46
v_fma_f32 v47,-v116,v23,v47
v_mfma_f32_16x16x32_bf16 v[32:35],a[160:163],v[72:75],v[32:35]
v_mul_f32 v88,v44,v44
v_mul_f32 v89,v45,v45
v_mfma_f32_16x16x32_bf16 v[32:35],a[148:151],v[68:71],v[32:35]
ds_read_b128 v[60:63],v2 offset:18432
v_mul_f32 v90,v46,v46
v_mfma_f32_16x16x32_bf16 v[28:31],a[144:147],v[68:71],v[28:31]
v_mul_f32 v91,v47,v47
v_fma_f32 v88,v118,v88,v117
v_mfma_f32_16x16x32_bf16 v[28:31],a[156:159],v[72:75],v[28:31]
s_waitcnt lgkmcnt(3)
v_mul_f32 v88,v44,v88
v_fma_f32 v89,v118,v89,v117
v_mfma_f32_16x16x32_bf16 v[28:31],a[168:171],v[76:79],v[28:31]
ds_read_b128 v[64:67],v2 offset:19456
v_mul_f32 v89,v45,v89
v_mfma_f32_16x16x32_bf16 v[28:31],a[180:183],v[80:83],v[28:31]
v_exp_f32 v88,v88
v_mfma_f32_16x16x32_bf16 v[32:35],a[184:187],v[80:83],v[32:35]
buffer_load_dwordx4 v1,s[36:39],s61 offen offset:0 sc1 lds
v_mfma_f32_16x16x32_bf16 v[32:35],a[172:175],v[76:79],v[32:35]
ds_read_b128 v[68:71],v2 offset:20480
v_fma_f32 v88,v88,v119,v119
v_mfma_f32_16x16x32_bf16 v[36:39],a[176:179],v[76:79],v[36:39]
ds_write_b32 v10,v104 offset:0
v_mfma_f32_16x16x32_bf16 v[36:39],a[188:191],v[80:83],v[36:39]
s_waitcnt lgkmcnt(4)
v_exp_f32 v89,v89
v_mfma_f32_16x16x32_bf16 v[36:39],a[200:203],v[52:55],v[36:39]
buffer_load_dwordx4 v1,s[36:39],s61 offen offset:1024 sc1 lds
v_mfma_f32_16x16x32_bf16 v[36:39],a[212:215],v[56:59],v[36:39]
ds_read_b128 v[72:75],v2 offset:21504
v_fma_f32 v89,v89,v119,v119
v_mfma_f32_16x16x32_bf16 v[32:35],a[208:211],v[56:59],v[32:35]
v_rcp_f32 v88,v88
v_mfma_f32_16x16x32_bf16 v[32:35],a[196:199],v[52:55],v[32:35]
ds_read_b128 v[76:79],v2 offset:22528
v_fma_f32 v90,v118,v90,v117
v_mfma_f32_16x16x32_bf16 v[28:31],a[192:195],v[52:55],v[28:31]
buffer_load_dwordx4 v1,s[36:39],s61 offen offset:2048 sc1 lds
v_mfma_f32_16x16x32_bf16 v[28:31],a[204:207],v[56:59],v[28:31]
s_waitcnt lgkmcnt(4)
v_rcp_f32 v89,v89
v_mfma_f32_16x16x32_bf16 v[28:31],a[216:219],v[60:63],v[28:31]
ds_read_b128 v[80:83],v2 offset:23552
v_mul_f32 v90,v46,v90
v_mfma_f32_16x16x32_bf16 v[28:31],a[228:231],v[64:67],v[28:31]
v_exp_f32 v90,v90
v_mfma_f32_16x16x32_bf16 v[32:35],a[232:235],v[64:67],v[32:35]
buffer_load_dwordx4 v1,s[36:39],s61 offen offset:3072 sc1 lds
v_mfma_f32_16x16x32_bf16 v[32:35],a[220:223],v[60:63],v[32:35]
buffer_load_dword v108,v8,s[36:39],s72 offen offset:0 sc1
v_mfma_f32_16x16x32_bf16 v[36:39],a[224:227],v[60:63],v[36:39]
ds_read_b128 v[52:55],v2 offset:24576
v_fma_f32 v90,v90,v119,v119
v_mfma_f32_16x16x32_bf16 v[36:39],a[236:239],v[64:67],v[36:39]
s_waitcnt lgkmcnt(3)
v_rcp_f32 v90,v90
v_mfma_f32_16x16x32_bf16 v[36:39],a[248:251],v[68:71],v[36:39]
ds_read_b128 v[56:59],v2 offset:25600
v_fma_f32 v91,v118,v91,v117
v_mfma_f32_16x16x32_bf16 v[36:39],v[132:135],v[72:75],v[36:39]
v_mul_f32 v91,v47,v91
v_mul_f32 v44,v44,v88
v_mfma_f32_16x16x32_bf16 v[32:35],v[128:131],v[72:75],v[32:35]
v_exp_f32 v91,v91
v_mfma_f32_16x16x32_bf16 v[32:35],a[244:247],v[68:71],v[32:35]
ds_read_b128 v[60:63],v2 offset:26624
v_fma_f32 v91,v91,v119,v119
v_mfma_f32_16x16x32_bf16 v[28:31],a[240:243],v[68:71],v[28:31]
v_rcp_f32 v91,v91
v_mfma_f32_16x16x32_bf16 v[28:31],a[252:255],v[72:75],v[28:31]
s_waitcnt lgkmcnt(3)
v_mul_f32 v45,v45,v89
v_mul_f32 v46,v46,v90
v_mfma_f32_16x16x32_bf16 v[28:31],v[136:139],v[76:79],v[28:31]
ds_read_b128 v[64:67],v2 offset:27648
v_mul_f32 v47,v47,v91
v_mfma_f32_16x16x32_bf16 v[28:31],v[148:151],v[80:83],v[28:31]
v_cvt_pk_bf16_f32 v98,v44,v45
v_cvt_pk_bf16_f32 v99,v46,v47
v_mfma_f32_16x16x32_bf16 v[32:35],v[152:155],v[80:83],v[32:35]
v_fma_f32 v48,-v116,v24,v48
v_fma_f32 v49,-v116,v25,v49
v_mfma_f32_16x16x32_bf16 v[32:35],v[140:143],v[76:79],v[32:35]
ds_read_b128 v[68:71],v2 offset:28672
v_fma_f32 v50,-v116,v26,v50
v_mfma_f32_16x16x32_bf16 v[36:39],v[144:147],v[76:79],v[36:39]
v_fma_f32 v51,-v116,v27,v51
v_mul_f32 v92,v48,v48
v_mfma_f32_16x16x32_bf16 v[36:39],v[156:159],v[80:83],v[36:39]
s_waitcnt lgkmcnt(3)
v_mul_f32 v93,v49,v49
v_mul_f32 v94,v50,v50
v_mfma_f32_16x16x32_bf16 v[36:39],v[168:171],v[52:55],v[36:39]
ds_read_b128 v[72:75],v2 offset:29696
v_mul_f32 v95,v51,v51
v_mfma_f32_16x16x32_bf16 v[36:39],v[180:183],v[56:59],v[36:39]
v_fma_f32 v92,v118,v92,v117
v_mul_f32 v92,v48,v92
v_mfma_f32_16x16x32_bf16 v[32:35],v[176:179],v[56:59],v[32:35]
buffer_store_dwordx4 v[96:99],v3,s[36:39],s73 offen offset:0 sc1
v_exp_f32 v92,v92
v_fma_f32 v93,v118,v93,v117
v_mfma_f32_16x16x32_bf16 v[32:35],v[164:167],v[52:55],v[32:35]
ds_read_b128 v[76:79],v2 offset:30720
v_mul_f32 v93,v49,v93
v_mfma_f32_16x16x32_bf16 v[28:31],v[160:163],v[52:55],v[28:31]
v_exp_f32 v93,v93
v_mfma_f32_16x16x32_bf16 v[28:31],v[172:175],v[56:59],v[28:31]
s_waitcnt lgkmcnt(3)
v_fma_f32 v92,v92,v119,v119
v_fma_f32 v93,v93,v119,v119
v_mfma_f32_16x16x32_bf16 v[28:31],v[184:187],v[60:63],v[28:31]
s_waitcnt vmcnt(6)
ds_read_b128 v[80:83],v2 offset:31744
v_fma_f32 v94,v118,v94,v117
v_mfma_f32_16x16x32_bf16 v[28:31],v[196:199],v[64:67],v[28:31]
s_barrier
v_mfma_f32_16x16x32_bf16 v[32:35],v[200:203],v[64:67],v[32:35]
ds_read_b32 v13,v7 offset:0
ds_read_b64 v[112:113],v11 offset:0
v_mfma_f32_16x16x32_bf16 v[32:35],v[188:191],v[60:63],v[32:35]
ds_read_b128 v[52:55],v2 offset:32768
v_fma_f32 v95,v118,v95,v117
v_mfma_f32_16x16x32_bf16 v[36:39],v[192:195],v[60:63],v[36:39]
v_rcp_f32 v92,v92
v_mfma_f32_16x16x32_bf16 v[36:39],v[204:207],v[64:67],v[36:39]
s_waitcnt lgkmcnt(5)
v_rcp_f32 v93,v93
v_mfma_f32_16x16x32_bf16 v[36:39],v[216:219],v[68:71],v[36:39]
s_waitcnt lgkmcnt(2)
ds_read_b128 v[56:59],v2 offset:33792
v_mul_f32 v94,v50,v94
v_mfma_f32_16x16x32_bf16 v[36:39],v[228:231],v[72:75],v[36:39]
s_waitcnt lgkmcnt(2)
v_cmp_eq_u32 vcc,s60,v13
v_exp_f32 v94,v94
v_mul_f32 v95,v51,v95
v_mfma_f32_16x16x32_bf16 v[32:35],v[224:227],v[72:75],v[32:35]
s_cmp_eq_u32 vcc_lo,-1
v_exp_f32 v95,v95
v_mfma_f32_16x16x32_bf16 v[32:35],v[212:215],v[68:71],v[32:35]
s_cselect_b32 s67,s86,s59
ds_read_b128 v[60:63],v2 offset:34816
v_fma_f32 v94,v94,v119,v119
v_mfma_f32_16x16x32_bf16 v[28:31],v[208:211],v[68:71],v[28:31]
s_lshr_b32 s61,s67,0
v_rcp_f32 v94,v94
v_mfma_f32_16x16x32_bf16 v[28:31],v[220:223],v[72:75],v[28:31]
v_fma_f32 v95,v95,v119,v119
v_mul_f32 v116,s75,v112
v_mfma_f32_16x16x32_bf16 v[28:31],v[232:235],v[76:79],v[28:31]
s_xor_b32 s63,s63,65536
v_rcp_f32 v95,v95
v_mfma_f32_16x16x32_bf16 v[28:31],v[244:247],v[80:83],v[28:31]
s_cmp_ge_u32 s66,s59
v_mul_f32 v48,v48,v92
v_mul_f32 v49,v49,v93
v_mul_f32 v50,v50,v94
v_mul_f32 v51,v51,v95
v_cvt_pk_bf16_f32 v100,v48,v49
v_cvt_pk_bf16_f32 v101,v50,v51
v_mfma_f32_16x16x32_bf16 v[32:35],v[248:251],v[80:83],v[32:35]
ds_read_b128 v[64:67],v2 offset:35840
buffer_store_dwordx2 v[100:101],v9,s[36:39],s73 offen offset:0 sc1
v_fma_f32 v113,v113,s75,v12
v_fma_f32 v113,-v116,v116,v113
v_max_f32 v113,v113,v12
v_mfma_f32_16x16x32_bf16 v[32:35],v[236:239],v[76:79],v[32:35]
ds_read_b128 v[68:71],v2 offset:36864
v_mfma_f32_16x16x32_bf16 v[36:39],v[240:243],v[76:79],v[36:39]
s_waitcnt lgkmcnt(3)
v_rsq_f32 v112,v113
v_mfma_f32_16x16x32_bf16 v[36:39],v[252:255],v[80:83],v[36:39]
s_cbranch_scc1 Lg3_slow1_%=
Lg3_d1w1_%=:
v_mfma_f32_16x16x32_bf16 v[48:51],a[8:11],v[52:55],0
s_mov_b32 m0,s62
ds_read_b128 v[72:75],v2 offset:37888
v_mul_f32 v117,s76,v112
v_mfma_f32_16x16x32_bf16 v[48:51],a[20:23],v[56:59],v[48:51]
s_mul_i32 s73,s48,7
v_mul_f32 v118,v112,v112
v_mul_f32 v119,v113,v112
v_mfma_f32_16x16x32_bf16 v[44:47],a[16:19],v[56:59],0)"
R"(
buffer_load_dwordx4 v0,s[36:39],s61 offen offset:0 sc1 lds
v_mfma_f32_16x16x32_bf16 v[44:47],a[4:7],v[52:55],v[44:47]
s_lshr_b32 s73,s73,1
ds_read_b128 v[76:79],v2 offset:38912
v_mul_f32 v118,s77,v118
v_mfma_f32_16x16x32_bf16 v[40:43],a[0:3],v[52:55],0
s_add_u32 s74,s53,s55
v_mul_f32 v118,v118,v117
v_fma_f32 v28,-v116,v16,v28
v_mfma_f32_16x16x32_bf16 v[40:43],a[12:15],v[56:59],v[40:43]
s_waitcnt lgkmcnt(3)
v_fma_f32 v29,-v116,v17,v29
v_fma_f32 v30,-v116,v18,v30
v_mfma_f32_16x16x32_bf16 v[40:43],a[24:27],v[60:63],v[40:43]
buffer_load_dwordx4 v0,s[36:39],s61 offen offset:1024 sc1 lds
v_mfma_f32_16x16x32_bf16 v[40:43],a[36:39],v[64:67],v[40:43]
s_cmp_eq_u32 s74,s56
ds_read_b128 v[80:83],v2 offset:39936
v_fma_f32 v31,-v116,v19,v31
v_mfma_f32_16x16x32_bf16 v[44:47],a[40:43],v[64:67],v[44:47]
s_cselect_b32 s74,s57,s74
v_mul_f32 v84,v28,v28
v_mul_f32 v85,v29,v29
v_mfma_f32_16x16x32_bf16 v[44:47],a[28:31],v[60:63],v[44:47]
s_cmp_eq_u32 s74,s58
ds_read_b128 v[52:55],v2 offset:40960
v_mul_f32 v86,v30,v30
v_mfma_f32_16x16x32_bf16 v[48:51],a[32:35],v[60:63],v[48:51]
buffer_load_dwordx4 v0,s[36:39],s61 offen offset:2048 sc1 lds
v_mfma_f32_16x16x32_bf16 v[48:51],a[44:47],v[64:67],v[48:51]
s_waitcnt lgkmcnt(3)
v_mul_f32 v87,v31,v31
v_fma_f32 v84,v118,v84,v117
v_mfma_f32_16x16x32_bf16 v[48:51],a[56:59],v[68:71],v[48:51]
s_cselect_b32 s74,s59,s74
ds_read_b128 v[56:59],v2 offset:41984
v_mul_f32 v84,v28,v84
v_mfma_f32_16x16x32_bf16 v[48:51],a[68:71],v[72:75],v[48:51]
s_cmp_ge_u32 s67,s59
v_exp_f32 v84,v84
v_mfma_f32_16x16x32_bf16 v[44:47],a[64:67],v[72:75],v[44:47]
buffer_load_dwordx4 v0,s[36:39],s61 offen offset:3072 sc1 lds
v_mfma_f32_16x16x32_bf16 v[44:47],a[52:55],v[68:71],v[44:47]
s_cselect_b32 s86,s50,s51
ds_read_b128 v[60:63],v2 offset:43008
v_fma_f32 v84,v84,v119,v119
v_mfma_f32_16x16x32_bf16 v[40:43],a[48:51],v[68:71],v[40:43]
s_add_u32 m0,s62,16384
v_rcp_f32 v84,v84
v_mfma_f32_16x16x32_bf16 v[40:43],a[60:63],v[72:75],v[40:43]
s_waitcnt lgkmcnt(3)
v_fma_f32 v85,v118,v85,v117
v_mul_f32 v85,v29,v85
v_mfma_f32_16x16x32_bf16 v[40:43],a[72:75],v[76:79],v[40:43]
s_lshr_b32 s72,s61,5
ds_read_b128 v[64:67],v2 offset:44032
v_fma_f32 v86,v118,v86,v117
v_mfma_f32_16x16x32_bf16 v[40:43],a[84:87],v[80:83],v[40:43]
s_mov_b32 s62,s64
v_exp_f32 v85,v85
v_mfma_f32_16x16x32_bf16 v[44:47],a[88:91],v[80:83],v[44:47]
s_mov_b64 s[44:45],s[46:47]
v_mul_f32 v86,v30,v86
v_fma_f32 v85,v85,v119,v119
v_mfma_f32_16x16x32_bf16 v[44:47],a[76:79],v[76:79],v[44:47]
s_mov_b64 s[46:47],s[48:49]
ds_read_b128 v[68:71],v2 offset:45056
v_fma_f32 v87,v118,v87,v117
v_mfma_f32_16x16x32_bf16 v[48:51],a[80:83],v[76:79],v[48:51]
s_mov_b64 s[48:49],s[50:51]
v_exp_f32 v86,v86
v_mfma_f32_16x16x32_bf16 v[48:51],a[92:95],v[80:83],v[48:51]
s_waitcnt lgkmcnt(3)
v_rcp_f32 v85,v85
v_mfma_f32_16x16x32_bf16 v[48:51],a[104:107],v[52:55],v[48:51]
s_mov_b64 s[50:51],s[52:53]
ds_read_b128 v[72:75],v2 offset:46080
v_mul_f32 v87,v31,v87
v_mfma_f32_16x16x32_bf16 v[48:51],a[116:119],v[56:59],v[48:51]
s_mov_b32 s52,s74
v_exp_f32 v87,v87
v_mfma_f32_16x16x32_bf16 v[44:47],a[112:115],v[56:59],v[44:47]
v_fma_f32 v86,v86,v119,v119
v_fma_f32 v87,v87,v119,v119
v_mfma_f32_16x16x32_bf16 v[44:47],a[100:103],v[52:55],v[44:47]
ds_read_b128 v[76:79],v2 offset:47104
v_mul_f32 v28,v28,v84
v_mfma_f32_16x16x32_bf16 v[40:43],a[96:99],v[52:55],v[40:43]
v_rcp_f32 v86,v86
v_mfma_f32_16x16x32_bf16 v[40:43],a[108:111],v[56:59],v[40:43]
s_waitcnt lgkmcnt(3)
v_rcp_f32 v87,v87
v_mfma_f32_16x16x32_bf16 v[40:43],a[120:123],v[60:63],v[40:43]
s_waitcnt vmcnt(6)
ds_read_b128 v[80:83],v2 offset:48128
v_mul_f32 v29,v29,v85
v_mfma_f32_16x16x32_bf16 v[40:43],a[132:135],v[64:67],v[40:43]
s_barrier
v_mfma_f32_16x16x32_bf16 v[44:47],a[136:139],v[64:67],v[44:47]
v_add_f32_dpp v108,v108,v108 quad_perm:[1,0,3,2] row_mask:0xf bank_mask:0xf
v_mul_f32 v30,v30,v86
v_mfma_f32_16x16x32_bf16 v[44:47],a[124:127],v[60:63],v[44:47]
ds_read_b128 v[52:55],v2 offset:49152
v_add_f32_dpp v108,v108,v108 quad_perm:[2,3,0,1] row_mask:0xf bank_mask:0xf
v_mfma_f32_16x16x32_bf16 v[48:51],a[128:131],v[60:63],v[48:51]
s_nop 0
v_add_f32_dpp v108,v108,v108 row_half_mirror row_mask:0xf bank_mask:0xf
v_mul_f32 v31,v31,v87
v_mfma_f32_16x16x32_bf16 v[48:51],a[140:143],v[64:67],v[48:51]
s_waitcnt lgkmcnt(3)
v_cvt_pk_bf16_f32 v96,v28,v29
v_cvt_pk_bf16_f32 v97,v30,v31
v_mfma_f32_16x16x32_bf16 v[48:51],a[152:155],v[68:71],v[48:51]
ds_read_b128 v[56:59],v2 offset:50176
v_fma_f32 v32,-v116,v20,v32
v_mfma_f32_16x16x32_bf16 v[48:51],a[164:167],v[72:75],v[48:51]
v_fma_f32 v33,-v116,v21,v33
v_fma_f32 v34,-v116,v22,v34
v_mfma_f32_16x16x32_bf16 v[44:47],a[160:163],v[72:75],v[44:47]
v_fma_f32 v35,-v116,v23,v35
v_mul_f32 v88,v32,v32
v_mfma_f32_16x16x32_bf16 v[44:47],a[148:151],v[68:71],v[44:47]
ds_read_b128 v[60:63],v2 offset:51200
v_mul_f32 v89,v33,v33
v_mfma_f32_16x16x32_bf16 v[40:43],a[144:147],v[68:71],v[40:43]
v_mul_f32 v90,v34,v34
v_mul_f32 v91,v35,v35
v_mfma_f32_16x16x32_bf16 v[40:43],a[156:159],v[72:75],v[40:43]
s_waitcnt lgkmcnt(3)
v_fma_f32 v88,v118,v88,v117
v_mul_f32 v88,v32,v88
v_mfma_f32_16x16x32_bf16 v[40:43],a[168:171],v[76:79],v[40:43]
ds_read_b128 v[64:67],v2 offset:52224
v_fma_f32 v89,v118,v89,v117
v_mfma_f32_16x16x32_bf16 v[40:43],a[180:183],v[80:83],v[40:43]
v_exp_f32 v88,v88
v_mfma_f32_16x16x32_bf16 v[44:47],a[184:187],v[80:83],v[44:47]
buffer_load_dwordx4 v1,s[36:39],s61 offen offset:0 sc1 lds
v_mfma_f32_16x16x32_bf16 v[44:47],a[172:175],v[76:79],v[44:47]
ds_read_b128 v[68:71],v2 offset:53248
v_mul_f32 v89,v33,v89
v_mfma_f32_16x16x32_bf16 v[48:51],a[176:179],v[76:79],v[48:51]
ds_write_b32 v10,v108 offset:128
v_mfma_f32_16x16x32_bf16 v[48:51],a[188:191],v[80:83],v[48:51]
s_waitcnt lgkmcnt(4)
v_exp_f32 v89,v89
v_mfma_f32_16x16x32_bf16 v[48:51],a[200:203],v[52:55],v[48:51]
buffer_load_dwordx4 v1,s[36:39],s61 offen offset:1024 sc1 lds
v_mfma_f32_16x16x32_bf16 v[48:51],a[212:215],v[56:59],v[48:51]
ds_read_b128 v[72:75],v2 offset:54272
v_fma_f32 v88,v88,v119,v119
v_mfma_f32_16x16x32_bf16 v[44:47],a[208:211],v[56:59],v[44:47]
v_rcp_f32 v88,v88
v_mfma_f32_16x16x32_bf16 v[44:47],a[196:199],v[52:55],v[44:47]
ds_read_b128 v[76:79],v2 offset:55296
v_fma_f32 v89,v89,v119,v119
v_mfma_f32_16x16x32_bf16 v[40:43],a[192:195],v[52:55],v[40:43]
buffer_load_dwordx4 v1,s[36:39],s61 offen offset:2048 sc1 lds
v_mfma_f32_16x16x32_bf16 v[40:43],a[204:207],v[56:59],v[40:43]
s_waitcnt lgkmcnt(4)
v_rcp_f32 v89,v89
v_mfma_f32_16x16x32_bf16 v[40:43],a[216:219],v[60:63],v[40:43]
ds_read_b128 v[80:83],v2 offset:56320
v_fma_f32 v90,v118,v90,v117
v_mfma_f32_16x16x32_bf16 v[40:43],a[228:231],v[64:67],v[40:43]
v_mul_f32 v90,v34,v90
v_fma_f32 v91,v118,v91,v117
v_mfma_f32_16x16x32_bf16 v[44:47],a[232:235],v[64:67],v[44:47]
buffer_load_dwordx4 v1,s[36:39],s61 offen offset:3072 sc1 lds
v_mfma_f32_16x16x32_bf16 v[44:47],a[220:223],v[60:63],v[44:47]
buffer_load_dword v104,v8,s[36:39],s72 offen offset:0 sc1
v_mfma_f32_16x16x32_bf16 v[48:51],a[224:227],v[60:63],v[48:51]
ds_read_b128 v[52:55],v2 offset:57344
v_mul_f32 v91,v35,v91
v_mfma_f32_16x16x32_bf16 v[48:51],a[236:239],v[64:67],v[48:51]
s_waitcnt lgkmcnt(3)
v_exp_f32 v90,v90
v_mfma_f32_16x16x32_bf16 v[48:51],a[248:251],v[68:71],v[48:51]
ds_read_b128 v[56:59],v2 offset:58368
v_fma_f32 v90,v90,v119,v119
v_mfma_f32_16x16x32_bf16 v[48:51],v[132:135],v[72:75],v[48:51]
v_exp_f32 v91,v91
v_mfma_f32_16x16x32_bf16 v[44:47],v[128:131],v[72:75],v[44:47]
v_rcp_f32 v90,v90
v_mfma_f32_16x16x32_bf16 v[44:47],a[244:247],v[68:71],v[44:47]
ds_read_b128 v[60:63],v2 offset:59392
v_fma_f32 v91,v91,v119,v119
v_mfma_f32_16x16x32_bf16 v[40:43],a[240:243],v[68:71],v[40:43]
v_rcp_f32 v91,v91
v_mfma_f32_16x16x32_bf16 v[40:43],a[252:255],v[72:75],v[40:43]
s_waitcnt lgkmcnt(3)
v_mul_f32 v32,v32,v88
v_mul_f32 v33,v33,v89
v_mfma_f32_16x16x32_bf16 v[40:43],v[136:139],v[76:79],v[40:43]
ds_read_b128 v[64:67],v2 offset:60416
v_mul_f32 v34,v34,v90
v_mfma_f32_16x16x32_bf16 v[40:43],v[148:151],v[80:83],v[40:43]
v_mul_f32 v35,v35,v91
v_cvt_pk_bf16_f32 v98,v32,v33
v_mfma_f32_16x16x32_bf16 v[44:47],v[152:155],v[80:83],v[44:47])"
R"(
v_cvt_pk_bf16_f32 v99,v34,v35
v_fma_f32 v36,-v116,v24,v36
v_mfma_f32_16x16x32_bf16 v[44:47],v[140:143],v[76:79],v[44:47]
ds_read_b128 v[68:71],v2 offset:61440
v_fma_f32 v37,-v116,v25,v37
v_mfma_f32_16x16x32_bf16 v[48:51],v[144:147],v[76:79],v[48:51]
v_fma_f32 v38,-v116,v26,v38
v_fma_f32 v39,-v116,v27,v39
v_mfma_f32_16x16x32_bf16 v[48:51],v[156:159],v[80:83],v[48:51]
s_waitcnt lgkmcnt(3)
v_mul_f32 v92,v36,v36
v_mul_f32 v93,v37,v37
v_mfma_f32_16x16x32_bf16 v[48:51],v[168:171],v[52:55],v[48:51]
ds_read_b128 v[72:75],v2 offset:62464
v_mul_f32 v94,v38,v38
v_mfma_f32_16x16x32_bf16 v[48:51],v[180:183],v[56:59],v[48:51]
v_mul_f32 v95,v39,v39
v_fma_f32 v92,v118,v92,v117
v_mfma_f32_16x16x32_bf16 v[44:47],v[176:179],v[56:59],v[44:47]
buffer_store_dwordx4 v[96:99],v3,s[36:39],s73 offen offset:0 sc1
v_mul_f32 v92,v36,v92
v_exp_f32 v92,v92
v_mfma_f32_16x16x32_bf16 v[44:47],v[164:167],v[52:55],v[44:47]
ds_read_b128 v[76:79],v2 offset:63488
v_fma_f32 v92,v92,v119,v119
v_mfma_f32_16x16x32_bf16 v[40:43],v[160:163],v[52:55],v[40:43]
v_rcp_f32 v92,v92
v_mfma_f32_16x16x32_bf16 v[40:43],v[172:175],v[56:59],v[40:43]
s_waitcnt lgkmcnt(3)
v_fma_f32 v93,v118,v93,v117
v_mul_f32 v93,v37,v93
v_mfma_f32_16x16x32_bf16 v[40:43],v[184:187],v[60:63],v[40:43]
s_waitcnt vmcnt(6)
ds_read_b128 v[80:83],v2 offset:64512
v_xor_b32 v2,65536,v2
v_mfma_f32_16x16x32_bf16 v[40:43],v[196:199],v[64:67],v[40:43]
s_barrier
v_mfma_f32_16x16x32_bf16 v[44:47],v[200:203],v[64:67],v[44:47]
ds_read_b32 v13,v7 offset:256
ds_read_b64 v[112:113],v11 offset:128
v_mfma_f32_16x16x32_bf16 v[44:47],v[188:191],v[60:63],v[44:47]
ds_read_b128 v[52:55],v2 offset:0
v_fma_f32 v94,v118,v94,v117
v_mfma_f32_16x16x32_bf16 v[48:51],v[192:195],v[60:63],v[48:51]
v_exp_f32 v93,v93
v_mfma_f32_16x16x32_bf16 v[48:51],v[204:207],v[64:67],v[48:51]
s_waitcnt lgkmcnt(5)
v_mul_f32 v94,v38,v94
v_fma_f32 v93,v93,v119,v119
v_mfma_f32_16x16x32_bf16 v[48:51],v[216:219],v[68:71],v[48:51]
s_waitcnt lgkmcnt(2)
ds_read_b128 v[56:59],v2 offset:1024
v_fma_f32 v95,v118,v95,v117
v_mfma_f32_16x16x32_bf16 v[48:51],v[228:231],v[72:75],v[48:51]
s_waitcnt lgkmcnt(2)
v_cmp_eq_u32 vcc,s60,v13
v_exp_f32 v94,v94
v_mul_f32 v95,v39,v95
v_mfma_f32_16x16x32_bf16 v[44:47],v[224:227],v[72:75],v[44:47]
s_cmp_eq_u32 vcc_lo,-1
v_exp_f32 v95,v95
v_mfma_f32_16x16x32_bf16 v[44:47],v[212:215],v[68:71],v[44:47]
s_cselect_b32 s66,s86,s59
ds_read_b128 v[60:63],v2 offset:2048
v_fma_f32 v94,v94,v119,v119
v_mfma_f32_16x16x32_bf16 v[40:43],v[208:211],v[68:71],v[40:43]
s_lshr_b32 s61,s66,0
v_rcp_f32 v93,v93
v_mfma_f32_16x16x32_bf16 v[40:43],v[220:223],v[72:75],v[40:43]
v_rcp_f32 v94,v94
v_mfma_f32_16x16x32_bf16 v[40:43],v[232:235],v[76:79],v[40:43]
s_xor_b32 s64,s64,65536
v_fma_f32 v95,v95,v119,v119
v_rcp_f32 v95,v95
v_mfma_f32_16x16x32_bf16 v[40:43],v[244:247],v[80:83],v[40:43]
s_cmp_lt_u32 s67,s59
v_mul_f32 v36,v36,v92
v_mul_f32 v37,v37,v93
v_mul_f32 v38,v38,v94
v_mul_f32 v39,v39,v95
v_cvt_pk_bf16_f32 v100,v36,v37
v_cvt_pk_bf16_f32 v101,v38,v39
v_mfma_f32_16x16x32_bf16 v[44:47],v[248:251],v[80:83],v[44:47]
ds_read_b128 v[64:67],v2 offset:3072
buffer_store_dwordx2 v[100:101],v9,s[36:39],s73 offen offset:0 sc1
v_mul_f32 v116,s75,v112
v_fma_f32 v113,v113,s75,v12
v_fma_f32 v113,-v116,v116,v113
v_mfma_f32_16x16x32_bf16 v[44:47],v[236:239],v[76:79],v[44:47]
ds_read_b128 v[68:71],v2 offset:4096
v_max_f32 v113,v113,v12
v_mfma_f32_16x16x32_bf16 v[48:51],v[240:243],v[76:79],v[48:51]
s_waitcnt lgkmcnt(3)
v_rsq_f32 v112,v113
v_mfma_f32_16x16x32_bf16 v[48:51],v[252:255],v[80:83],v[48:51]
s_cbranch_scc1 Lg3_d0w1_%=
s_branch Lg3_slow0_%=
Lg3_d0w2_%=:
v_mfma_f32_16x16x32_bf16 v[36:39],a[8:11],v[52:55],0
s_mov_b32 m0,s62
ds_read_b128 v[72:75],v2 offset:5120
v_mul_f32 v117,s76,v112
v_mfma_f32_16x16x32_bf16 v[36:39],a[20:23],v[56:59],v[36:39]
s_mul_i32 s73,s47,7
v_mul_f32 v118,v112,v112
v_mul_f32 v119,v113,v112
v_mfma_f32_16x16x32_bf16 v[32:35],a[16:19],v[56:59],0
s_lshr_b32 s73,s73,1
v_mul_f32 v118,s77,v118
v_mul_f32 v118,v118,v117
v_mfma_f32_16x16x32_bf16 v[32:35],a[4:7],v[52:55],v[32:35]
buffer_load_dwordx4 v0,s[36:39],s61 offen offset:0 sc1 lds
ds_read_b128 v[76:79],v2 offset:6144
v_mfma_f32_16x16x32_bf16 v[28:31],a[0:3],v[52:55],0
s_add_u32 s53,s52,s55
v_fma_f32 v40,-v116,v16,v40
v_fma_f32 v41,-v116,v17,v41
v_mfma_f32_16x16x32_bf16 v[28:31],a[12:15],v[56:59],v[28:31]
s_waitcnt lgkmcnt(3)
v_fma_f32 v42,-v116,v18,v42
v_fma_f32 v43,-v116,v19,v43
v_mfma_f32_16x16x32_bf16 v[28:31],a[24:27],v[60:63],v[28:31]
s_cmp_eq_u32 s53,s56
ds_read_b128 v[80:83],v2 offset:7168
v_mul_f32 v84,v40,v40
v_mfma_f32_16x16x32_bf16 v[28:31],a[36:39],v[64:67],v[28:31]
buffer_load_dwordx4 v0,s[36:39],s61 offen offset:1024 sc1 lds
v_mfma_f32_16x16x32_bf16 v[32:35],a[40:43],v[64:67],v[32:35]
s_cselect_b32 s53,s57,s53
v_mul_f32 v85,v41,v41
v_mul_f32 v86,v42,v42
v_mfma_f32_16x16x32_bf16 v[32:35],a[28:31],v[60:63],v[32:35]
s_cmp_eq_u32 s53,s58
ds_read_b128 v[52:55],v2 offset:8192
v_mul_f32 v87,v43,v43
v_mfma_f32_16x16x32_bf16 v[36:39],a[32:35],v[60:63],v[36:39]
s_cselect_b32 s53,s59,s53
v_fma_f32 v84,v118,v84,v117
v_mul_f32 v84,v40,v84
v_mfma_f32_16x16x32_bf16 v[36:39],a[44:47],v[64:67],v[36:39]
s_waitcnt lgkmcnt(3)
buffer_load_dwordx4 v0,s[36:39],s61 offen offset:2048 sc1 lds
v_mfma_f32_16x16x32_bf16 v[36:39],a[56:59],v[68:71],v[36:39]
s_cmp_ge_u32 s66,s59
ds_read_b128 v[56:59],v2 offset:9216
v_fma_f32 v85,v118,v85,v117
v_mfma_f32_16x16x32_bf16 v[36:39],a[68:71],v[72:75],v[36:39]
s_cselect_b32 s86,s49,s50
v_exp_f32 v84,v84
v_mfma_f32_16x16x32_bf16 v[32:35],a[64:67],v[72:75],v[32:35]
v_mul_f32 v85,v41,v85
v_fma_f32 v84,v84,v119,v119
v_mfma_f32_16x16x32_bf16 v[32:35],a[52:55],v[68:71],v[32:35]
buffer_load_dwordx4 v0,s[36:39],s61 offen offset:3072 sc1 lds
v_mfma_f32_16x16x32_bf16 v[28:31],a[48:51],v[68:71],v[28:31]
s_add_u32 m0,s62,16384
ds_read_b128 v[60:63],v2 offset:10240
v_fma_f32 v86,v118,v86,v117
v_mfma_f32_16x16x32_bf16 v[28:31],a[60:63],v[72:75],v[28:31]
s_waitcnt lgkmcnt(3)
v_exp_f32 v85,v85
v_mfma_f32_16x16x32_bf16 v[28:31],a[72:75],v[76:79],v[28:31]
s_lshr_b32 s72,s61,5
ds_read_b128 v[64:67],v2 offset:11264
v_mul_f32 v86,v42,v86
v_mfma_f32_16x16x32_bf16 v[28:31],a[84:87],v[80:83],v[28:31]
s_mov_b32 s62,s63
v_exp_f32 v86,v86
v_mfma_f32_16x16x32_bf16 v[32:35],a[88:91],v[80:83],v[32:35]
v_rcp_f32 v84,v84
v_mfma_f32_16x16x32_bf16 v[32:35],a[76:79],v[76:79],v[32:35]
ds_read_b128 v[68:71],v2 offset:12288
v_fma_f32 v85,v85,v119,v119
v_mfma_f32_16x16x32_bf16 v[36:39],a[80:83],v[76:79],v[36:39]
v_rcp_f32 v85,v85
v_mfma_f32_16x16x32_bf16 v[36:39],a[92:95],v[80:83],v[36:39]
s_waitcnt lgkmcnt(3)
v_fma_f32 v86,v86,v119,v119
v_fma_f32 v87,v118,v87,v117
v_mfma_f32_16x16x32_bf16 v[36:39],a[104:107],v[52:55],v[36:39]
ds_read_b128 v[72:75],v2 offset:13312
v_mul_f32 v87,v43,v87
v_mfma_f32_16x16x32_bf16 v[36:39],a[116:119],v[56:59],v[36:39]
v_exp_f32 v87,v87
v_mfma_f32_16x16x32_bf16 v[32:35],a[112:115],v[56:59],v[32:35]
v_rcp_f32 v86,v86
v_mfma_f32_16x16x32_bf16 v[32:35],a[100:103],v[52:55],v[32:35]
ds_read_b128 v[76:79],v2 offset:14336
v_fma_f32 v87,v87,v119,v119
v_mfma_f32_16x16x32_bf16 v[28:31],a[96:99],v[52:55],v[28:31]
v_rcp_f32 v87,v87
v_mfma_f32_16x16x32_bf16 v[28:31],a[108:111],v[56:59],v[28:31]
s_waitcnt lgkmcnt(3)
v_mul_f32 v40,v40,v84
v_mul_f32 v41,v41,v85
v_mfma_f32_16x16x32_bf16 v[28:31],a[120:123],v[60:63],v[28:31]
s_waitcnt vmcnt(6)
ds_read_b128 v[80:83],v2 offset:15360
v_mul_f32 v42,v42,v86
v_mfma_f32_16x16x32_bf16 v[28:31],a[132:135],v[64:67],v[28:31])"
R"(
s_barrier
v_mfma_f32_16x16x32_bf16 v[32:35],a[136:139],v[64:67],v[32:35]
v_add_f32_dpp v104,v104,v104 quad_perm:[1,0,3,2] row_mask:0xf bank_mask:0xf
v_mul_f32 v43,v43,v87
v_mfma_f32_16x16x32_bf16 v[32:35],a[124:127],v[60:63],v[32:35]
ds_read_b128 v[52:55],v2 offset:16384
v_add_f32_dpp v104,v104,v104 quad_perm:[2,3,0,1] row_mask:0xf bank_mask:0xf
v_mfma_f32_16x16x32_bf16 v[36:39],a[128:131],v[60:63],v[36:39]
s_nop 0
v_add_f32_dpp v104,v104,v104 row_half_mirror row_mask:0xf bank_mask:0xf
v_cvt_pk_bf16_f32 v96,v40,v41
v_mfma_f32_16x16x32_bf16 v[36:39],a[140:143],v[64:67],v[36:39]
s_waitcnt lgkmcnt(3)
v_cvt_pk_bf16_f32 v97,v42,v43
v_fma_f32 v44,-v116,v20,v44
v_mfma_f32_16x16x32_bf16 v[36:39],a[152:155],v[68:71],v[36:39]
ds_read_b128 v[56:59],v2 offset:17408
v_fma_f32 v45,-v116,v21,v45
v_mfma_f32_16x16x32_bf16 v[36:39],a[164:167],v[72:75],v[36:39]
v_fma_f32 v46,-v116,v22,v46
v_fma_f32 v47,-v116,v23,v47
v_mfma_f32_16x16x32_bf16 v[32:35],a[160:163],v[72:75],v[32:35]
v_mul_f32 v88,v44,v44
v_mul_f32 v89,v45,v45
v_mfma_f32_16x16x32_bf16 v[32:35],a[148:151],v[68:71],v[32:35]
ds_read_b128 v[60:63],v2 offset:18432
v_mul_f32 v90,v46,v46
v_mfma_f32_16x16x32_bf16 v[28:31],a[144:147],v[68:71],v[28:31]
v_mul_f32 v91,v47,v47
v_fma_f32 v88,v118,v88,v117
v_mfma_f32_16x16x32_bf16 v[28:31],a[156:159],v[72:75],v[28:31]
s_waitcnt lgkmcnt(3)
v_mul_f32 v88,v44,v88
v_fma_f32 v89,v118,v89,v117
v_mfma_f32_16x16x32_bf16 v[28:31],a[168:171],v[76:79],v[28:31]
ds_read_b128 v[64:67],v2 offset:19456
v_mul_f32 v89,v45,v89
v_mfma_f32_16x16x32_bf16 v[28:31],a[180:183],v[80:83],v[28:31]
v_exp_f32 v88,v88
v_mfma_f32_16x16x32_bf16 v[32:35],a[184:187],v[80:83],v[32:35]
v_exp_f32 v89,v89
v_mfma_f32_16x16x32_bf16 v[32:35],a[172:175],v[76:79],v[32:35]
buffer_load_dwordx4 v1,s[36:39],s61 offen offset:0 sc1 lds
v_mfma_f32_16x16x32_bf16 v[36:39],a[176:179],v[76:79],v[36:39]
ds_write_b32 v10,v104 offset:0
v_mfma_f32_16x16x32_bf16 v[36:39],a[188:191],v[80:83],v[36:39]
s_waitcnt lgkmcnt(3)
ds_read_b128 v[68:71],v2 offset:20480
v_fma_f32 v88,v88,v119,v119
v_mfma_f32_16x16x32_bf16 v[36:39],a[200:203],v[52:55],v[36:39]
ds_read_b128 v[72:75],v2 offset:21504
v_fma_f32 v89,v89,v119,v119
v_mfma_f32_16x16x32_bf16 v[36:39],a[212:215],v[56:59],v[36:39]
buffer_load_dwordx4 v1,s[36:39],s61 offen offset:1024 sc1 lds
v_mfma_f32_16x16x32_bf16 v[32:35],a[208:211],v[56:59],v[32:35]
v_rcp_f32 v88,v88
v_mfma_f32_16x16x32_bf16 v[32:35],a[196:199],v[52:55],v[32:35]
ds_read_b128 v[76:79],v2 offset:22528
v_fma_f32 v90,v118,v90,v117
v_mfma_f32_16x16x32_bf16 v[28:31],a[192:195],v[52:55],v[28:31]
v_rcp_f32 v89,v89
v_mfma_f32_16x16x32_bf16 v[28:31],a[204:207],v[56:59],v[28:31]
s_waitcnt lgkmcnt(4)
buffer_load_dwordx4 v1,s[36:39],s61 offen offset:2048 sc1 lds
v_mfma_f32_16x16x32_bf16 v[28:31],a[216:219],v[60:63],v[28:31]
ds_read_b128 v[80:83],v2 offset:23552
v_mul_f32 v90,v46,v90
v_mfma_f32_16x16x32_bf16 v[28:31],a[228:231],v[64:67],v[28:31]
v_exp_f32 v90,v90
v_mfma_f32_16x16x32_bf16 v[32:35],a[232:235],v[64:67],v[32:35]
v_fma_f32 v90,v90,v119,v119
v_fma_f32 v91,v118,v91,v117
v_mfma_f32_16x16x32_bf16 v[32:35],a[220:223],v[60:63],v[32:35]
buffer_load_dwordx4 v1,s[36:39],s61 offen offset:3072 sc1 lds
v_mfma_f32_16x16x32_bf16 v[36:39],a[224:227],v[60:63],v[36:39]
buffer_load_dword v108,v8,s[36:39],s72 offen offset:0 sc1
v_mfma_f32_16x16x32_bf16 v[36:39],a[236:239],v[64:67],v[36:39]
s_waitcnt lgkmcnt(2)
ds_read_b128 v[52:55],v2 offset:24576
v_mul_f32 v91,v47,v91
v_mfma_f32_16x16x32_bf16 v[36:39],a[248:251],v[68:71],v[36:39]
ds_read_b128 v[56:59],v2 offset:25600
v_mul_f32 v44,v44,v88
v_mfma_f32_16x16x32_bf16 v[36:39],v[132:135],v[72:75],v[36:39]
v_exp_f32 v91,v91
v_mfma_f32_16x16x32_bf16 v[32:35],v[128:131],v[72:75],v[32:35]
v_rcp_f32 v90,v90
v_mfma_f32_16x16x32_bf16 v[32:35],a[244:247],v[68:71],v[32:35]
ds_read_b128 v[60:63],v2 offset:26624
v_fma_f32 v91,v91,v119,v119
v_mfma_f32_16x16x32_bf16 v[28:31],a[240:243],v[68:71],v[28:31]
v_rcp_f32 v91,v91
v_mfma_f32_16x16x32_bf16 v[28:31],a[252:255],v[72:75],v[28:31]
s_waitcnt lgkmcnt(3)
v_mul_f32 v45,v45,v89
v_mul_f32 v46,v46,v90
v_mfma_f32_16x16x32_bf16 v[28:31],v[136:139],v[76:79],v[28:31]
ds_read_b128 v[64:67],v2 offset:27648
v_mul_f32 v47,v47,v91
v_mfma_f32_16x16x32_bf16 v[28:31],v[148:151],v[80:83],v[28:31]
v_cvt_pk_bf16_f32 v98,v44,v45
v_cvt_pk_bf16_f32 v99,v46,v47
v_mfma_f32_16x16x32_bf16 v[32:35],v[152:155],v[80:83],v[32:35]
v_fma_f32 v48,-v116,v24,v48
v_fma_f32 v49,-v116,v25,v49
v_mfma_f32_16x16x32_bf16 v[32:35],v[140:143],v[76:79],v[32:35]
ds_read_b128 v[68:71],v2 offset:28672
v_fma_f32 v50,-v116,v26,v50
v_mfma_f32_16x16x32_bf16 v[36:39],v[144:147],v[76:79],v[36:39]
v_fma_f32 v51,-v116,v27,v51
v_mul_f32 v92,v48,v48
v_mfma_f32_16x16x32_bf16 v[36:39],v[156:159],v[80:83],v[36:39]
s_waitcnt lgkmcnt(3)
v_mul_f32 v93,v49,v49
v_mul_f32 v94,v50,v50
v_mfma_f32_16x16x32_bf16 v[36:39],v[168:171],v[52:55],v[36:39]
ds_read_b128 v[72:75],v2 offset:29696
v_mul_f32 v95,v51,v51
v_mfma_f32_16x16x32_bf16 v[36:39],v[180:183],v[56:59],v[36:39]
buffer_store_dwordx4 v[96:99],v3,s[36:39],s73 offen offset:0 sc1
v_fma_f32 v92,v118,v92,v117
v_mul_f32 v92,v48,v92
v_fma_f32 v93,v118,v93,v117
v_mfma_f32_16x16x32_bf16 v[32:35],v[176:179],v[56:59],v[32:35]
v_exp_f32 v92,v92
v_mfma_f32_16x16x32_bf16 v[32:35],v[164:167],v[52:55],v[32:35]
ds_read_b128 v[76:79],v2 offset:30720
v_mul_f32 v93,v49,v93
v_mfma_f32_16x16x32_bf16 v[28:31],v[160:163],v[52:55],v[28:31]
v_exp_f32 v93,v93
v_mfma_f32_16x16x32_bf16 v[28:31],v[172:175],v[56:59],v[28:31]
s_waitcnt lgkmcnt(3)
v_fma_f32 v92,v92,v119,v119
v_fma_f32 v93,v93,v119,v119
v_mfma_f32_16x16x32_bf16 v[28:31],v[184:187],v[60:63],v[28:31]
s_waitcnt vmcnt(6)
ds_read_b128 v[80:83],v2 offset:31744
v_fma_f32 v94,v118,v94,v117
v_mfma_f32_16x16x32_bf16 v[28:31],v[196:199],v[64:67],v[28:31]
s_barrier
v_mfma_f32_16x16x32_bf16 v[32:35],v[200:203],v[64:67],v[32:35]
ds_read_b32 v13,v7 offset:0
ds_read_b64 v[112:113],v11 offset:0
v_mfma_f32_16x16x32_bf16 v[32:35],v[188:191],v[60:63],v[32:35]
ds_read_b128 v[52:55],v2 offset:32768
v_fma_f32 v95,v118,v95,v117
v_mfma_f32_16x16x32_bf16 v[36:39],v[192:195],v[60:63],v[36:39]
v_rcp_f32 v92,v92
v_mfma_f32_16x16x32_bf16 v[36:39],v[204:207],v[64:67],v[36:39]
s_waitcnt lgkmcnt(5)
v_rcp_f32 v93,v93
v_mfma_f32_16x16x32_bf16 v[36:39],v[216:219],v[68:71],v[36:39]
s_waitcnt lgkmcnt(2)
ds_read_b128 v[56:59],v2 offset:33792
v_mul_f32 v94,v50,v94
v_mfma_f32_16x16x32_bf16 v[36:39],v[228:231],v[72:75],v[36:39]
s_waitcnt lgkmcnt(2)
v_cmp_eq_u32 vcc,s60,v13
v_exp_f32 v94,v94
v_mul_f32 v95,v51,v95
v_mfma_f32_16x16x32_bf16 v[32:35],v[224:227],v[72:75],v[32:35]
s_cmp_eq_u32 vcc_lo,-1
v_exp_f32 v95,v95
v_mfma_f32_16x16x32_bf16 v[32:35],v[212:215],v[68:71],v[32:35]
s_cselect_b32 s67,s86,s59
ds_read_b128 v[60:63],v2 offset:34816
v_fma_f32 v94,v94,v119,v119
v_mfma_f32_16x16x32_bf16 v[28:31],v[208:211],v[68:71],v[28:31]
s_lshr_b32 s61,s67,0
v_rcp_f32 v94,v94
v_mfma_f32_16x16x32_bf16 v[28:31],v[220:223],v[72:75],v[28:31]
v_fma_f32 v95,v95,v119,v119
v_rcp_f32 v95,v95
v_mfma_f32_16x16x32_bf16 v[28:31],v[232:235],v[76:79],v[28:31]
s_xor_b32 s63,s63,65536
v_mul_f32 v48,v48,v92
v_mul_f32 v49,v49,v93
v_mul_f32 v50,v50,v94
v_mul_f32 v51,v51,v95
v_cvt_pk_bf16_f32 v100,v48,v49
v_cvt_pk_bf16_f32 v101,v50,v51
v_mfma_f32_16x16x32_bf16 v[28:31],v[244:247],v[80:83],v[28:31]
buffer_store_dwordx2 v[100:101],v9,s[36:39],s73 offen offset:0 sc1
ds_read_b128 v[64:67],v2 offset:35840
v_mul_f32 v116,s75,v112
v_fma_f32 v113,v113,s75,v12
v_mfma_f32_16x16x32_bf16 v[32:35],v[248:251],v[80:83],v[32:35]
s_cmp_ge_u32 s66,s59
v_fma_f32 v113,-v116,v116,v113
v_max_f32 v113,v113,v12
v_mfma_f32_16x16x32_bf16 v[32:35],v[236:239],v[76:79],v[32:35]
ds_read_b128 v[68:71],v2 offset:36864
v_mfma_f32_16x16x32_bf16 v[36:39],v[240:243],v[76:79],v[36:39]
s_waitcnt lgkmcnt(3)
v_rsq_f32 v112,v113
v_mfma_f32_16x16x32_bf16 v[36:39],v[252:255],v[80:83],v[36:39]
s_cbranch_scc1 Lg3_slow1_%=
Lg3_d1w2_%=:
v_mfma_f32_16x16x32_bf16 v[48:51],a[8:11],v[52:55],0
s_mov_b32 m0,s62)"
R"(
ds_read_b128 v[72:75],v2 offset:37888
v_mul_f32 v117,s76,v112
v_mfma_f32_16x16x32_bf16 v[48:51],a[20:23],v[56:59],v[48:51]
s_mul_i32 s73,s48,7
v_mul_f32 v118,v112,v112
v_mul_f32 v119,v113,v112
v_mfma_f32_16x16x32_bf16 v[44:47],a[16:19],v[56:59],0
s_lshr_b32 s73,s73,1
v_mul_f32 v118,s77,v118
v_mul_f32 v118,v118,v117
v_mfma_f32_16x16x32_bf16 v[44:47],a[4:7],v[52:55],v[44:47]
buffer_load_dwordx4 v0,s[36:39],s61 offen offset:0 sc1 lds
ds_read_b128 v[76:79],v2 offset:38912
v_mfma_f32_16x16x32_bf16 v[40:43],a[0:3],v[52:55],0
s_add_u32 s74,s53,s55
v_fma_f32 v28,-v116,v16,v28
v_fma_f32 v29,-v116,v17,v29
v_mfma_f32_16x16x32_bf16 v[40:43],a[12:15],v[56:59],v[40:43]
s_waitcnt lgkmcnt(3)
v_fma_f32 v30,-v116,v18,v30
v_fma_f32 v31,-v116,v19,v31
v_mfma_f32_16x16x32_bf16 v[40:43],a[24:27],v[60:63],v[40:43]
s_cmp_eq_u32 s74,s56
ds_read_b128 v[80:83],v2 offset:39936
v_mul_f32 v84,v28,v28
v_mfma_f32_16x16x32_bf16 v[40:43],a[36:39],v[64:67],v[40:43]
buffer_load_dwordx4 v0,s[36:39],s61 offen offset:1024 sc1 lds
v_mfma_f32_16x16x32_bf16 v[44:47],a[40:43],v[64:67],v[44:47]
s_cselect_b32 s74,s57,s74
v_mul_f32 v85,v29,v29
v_mul_f32 v86,v30,v30
v_mfma_f32_16x16x32_bf16 v[44:47],a[28:31],v[60:63],v[44:47]
s_cmp_eq_u32 s74,s58
ds_read_b128 v[52:55],v2 offset:40960
v_mul_f32 v87,v31,v31
v_mfma_f32_16x16x32_bf16 v[48:51],a[32:35],v[60:63],v[48:51]
s_cselect_b32 s74,s59,s74
v_fma_f32 v84,v118,v84,v117
v_mul_f32 v84,v28,v84
v_mfma_f32_16x16x32_bf16 v[48:51],a[44:47],v[64:67],v[48:51]
s_waitcnt lgkmcnt(3)
buffer_load_dwordx4 v0,s[36:39],s61 offen offset:2048 sc1 lds
v_mfma_f32_16x16x32_bf16 v[48:51],a[56:59],v[68:71],v[48:51]
s_cmp_ge_u32 s67,s59
ds_read_b128 v[56:59],v2 offset:41984
v_fma_f32 v85,v118,v85,v117
v_mfma_f32_16x16x32_bf16 v[48:51],a[68:71],v[72:75],v[48:51]
s_cselect_b32 s86,s50,s51
v_exp_f32 v84,v84
v_mfma_f32_16x16x32_bf16 v[44:47],a[64:67],v[72:75],v[44:47]
s_mov_b64 s[44:45],s[46:47]
v_mul_f32 v85,v29,v85
v_fma_f32 v84,v84,v119,v119
v_mfma_f32_16x16x32_bf16 v[44:47],a[52:55],v[68:71],v[44:47]
buffer_load_dwordx4 v0,s[36:39],s61 offen offset:3072 sc1 lds
v_mfma_f32_16x16x32_bf16 v[40:43],a[48:51],v[68:71],v[40:43]
s_add_u32 m0,s62,16384
ds_read_b128 v[60:63],v2 offset:43008
v_fma_f32 v86,v118,v86,v117
v_mfma_f32_16x16x32_bf16 v[40:43],a[60:63],v[72:75],v[40:43]
s_waitcnt lgkmcnt(3)
v_exp_f32 v85,v85
v_mfma_f32_16x16x32_bf16 v[40:43],a[72:75],v[76:79],v[40:43]
s_lshr_b32 s72,s61,5
ds_read_b128 v[64:67],v2 offset:44032
v_mul_f32 v86,v30,v86
v_mfma_f32_16x16x32_bf16 v[40:43],a[84:87],v[80:83],v[40:43]
s_mov_b32 s62,s64
v_exp_f32 v86,v86
v_mfma_f32_16x16x32_bf16 v[44:47],a[88:91],v[80:83],v[44:47]
s_mov_b64 s[46:47],s[48:49]
v_rcp_f32 v84,v84
v_mfma_f32_16x16x32_bf16 v[44:47],a[76:79],v[76:79],v[44:47]
s_mov_b64 s[48:49],s[50:51]
ds_read_b128 v[68:71],v2 offset:45056
v_fma_f32 v85,v85,v119,v119
v_mfma_f32_16x16x32_bf16 v[48:51],a[80:83],v[76:79],v[48:51]
s_mov_b64 s[50:51],s[52:53]
v_rcp_f32 v85,v85
v_mfma_f32_16x16x32_bf16 v[48:51],a[92:95],v[80:83],v[48:51]
s_waitcnt lgkmcnt(3)
v_fma_f32 v86,v86,v119,v119
v_fma_f32 v87,v118,v87,v117
v_mfma_f32_16x16x32_bf16 v[48:51],a[104:107],v[52:55],v[48:51]
s_mov_b32 s52,s74
ds_read_b128 v[72:75],v2 offset:46080
v_mul_f32 v87,v31,v87
v_mfma_f32_16x16x32_bf16 v[48:51],a[116:119],v[56:59],v[48:51]
v_exp_f32 v87,v87
v_mfma_f32_16x16x32_bf16 v[44:47],a[112:115],v[56:59],v[44:47]
v_rcp_f32 v86,v86
v_mfma_f32_16x16x32_bf16 v[44:47],a[100:103],v[52:55],v[44:47]
ds_read_b128 v[76:79],v2 offset:47104
v_fma_f32 v87,v87,v119,v119
v_mfma_f32_16x16x32_bf16 v[40:43],a[96:99],v[52:55],v[40:43]
v_rcp_f32 v87,v87
v_mfma_f32_16x16x32_bf16 v[40:43],a[108:111],v[56:59],v[40:43]
s_waitcnt lgkmcnt(3)
v_mul_f32 v28,v28,v84
v_mul_f32 v29,v29,v85
v_mfma_f32_16x16x32_bf16 v[40:43],a[120:123],v[60:63],v[40:43]
s_waitcnt vmcnt(6)
ds_read_b128 v[80:83],v2 offset:48128
v_mul_f32 v30,v30,v86
v_mfma_f32_16x16x32_bf16 v[40:43],a[132:135],v[64:67],v[40:43]
s_barrier
v_mfma_f32_16x16x32_bf16 v[44:47],a[136:139],v[64:67],v[44:47]
v_add_f32_dpp v108,v108,v108 quad_perm:[1,0,3,2] row_mask:0xf bank_mask:0xf
v_mul_f32 v31,v31,v87
v_mfma_f32_16x16x32_bf16 v[44:47],a[124:127],v[60:63],v[44:47]
ds_read_b128 v[52:55],v2 offset:49152
v_add_f32_dpp v108,v108,v108 quad_perm:[2,3,0,1] row_mask:0xf bank_mask:0xf
v_cvt_pk_bf16_f32 v96,v28,v29
v_mfma_f32_16x16x32_bf16 v[48:51],a[128:131],v[60:63],v[48:51]
v_add_f32_dpp v108,v108,v108 row_half_mirror row_mask:0xf bank_mask:0xf
v_cvt_pk_bf16_f32 v97,v30,v31
v_mfma_f32_16x16x32_bf16 v[48:51],a[140:143],v[64:67],v[48:51]
s_waitcnt lgkmcnt(3)
v_fma_f32 v32,-v116,v20,v32
v_fma_f32 v33,-v116,v21,v33
v_mfma_f32_16x16x32_bf16 v[48:51],a[152:155],v[68:71],v[48:51]
ds_read_b128 v[56:59],v2 offset:50176
v_fma_f32 v34,-v116,v22,v34
v_mfma_f32_16x16x32_bf16 v[48:51],a[164:167],v[72:75],v[48:51]
v_fma_f32 v35,-v116,v23,v35
v_mul_f32 v88,v32,v32
v_mfma_f32_16x16x32_bf16 v[44:47],a[160:163],v[72:75],v[44:47]
v_mul_f32 v89,v33,v33
v_mul_f32 v90,v34,v34
v_mfma_f32_16x16x32_bf16 v[44:47],a[148:151],v[68:71],v[44:47]
ds_read_b128 v[60:63],v2 offset:51200
v_mul_f32 v91,v35,v35
v_mfma_f32_16x16x32_bf16 v[40:43],a[144:147],v[68:71],v[40:43]
v_fma_f32 v88,v118,v88,v117
v_mul_f32 v88,v32,v88
v_mfma_f32_16x16x32_bf16 v[40:43],a[156:159],v[72:75],v[40:43]
s_waitcnt lgkmcnt(3)
v_exp_f32 v88,v88
v_mfma_f32_16x16x32_bf16 v[40:43],a[168:171],v[76:79],v[40:43]
ds_read_b128 v[64:67],v2 offset:52224
v_fma_f32 v88,v88,v119,v119
v_mfma_f32_16x16x32_bf16 v[40:43],a[180:183],v[80:83],v[40:43]
v_rcp_f32 v88,v88
v_mfma_f32_16x16x32_bf16 v[44:47],a[184:187],v[80:83],v[44:47]
v_fma_f32 v89,v118,v89,v117
v_mul_f32 v89,v33,v89
v_mfma_f32_16x16x32_bf16 v[44:47],a[172:175],v[76:79],v[44:47]
buffer_load_dwordx4 v1,s[36:39],s61 offen offset:0 sc1 lds
v_mfma_f32_16x16x32_bf16 v[48:51],a[176:179],v[76:79],v[48:51]
ds_write_b32 v10,v108 offset:128
v_mfma_f32_16x16x32_bf16 v[48:51],a[188:191],v[80:83],v[48:51]
s_waitcnt lgkmcnt(3)
ds_read_b128 v[68:71],v2 offset:53248
v_fma_f32 v90,v118,v90,v117
v_mfma_f32_16x16x32_bf16 v[48:51],a[200:203],v[52:55],v[48:51]
ds_read_b128 v[72:75],v2 offset:54272
v_mul_f32 v90,v34,v90
v_mfma_f32_16x16x32_bf16 v[48:51],a[212:215],v[56:59],v[48:51]
buffer_load_dwordx4 v1,s[36:39],s61 offen offset:1024 sc1 lds
v_mfma_f32_16x16x32_bf16 v[44:47],a[208:211],v[56:59],v[44:47]
v_exp_f32 v89,v89
v_mfma_f32_16x16x32_bf16 v[44:47],a[196:199],v[52:55],v[44:47]
ds_read_b128 v[76:79],v2 offset:55296
v_fma_f32 v89,v89,v119,v119
v_mfma_f32_16x16x32_bf16 v[40:43],a[192:195],v[52:55],v[40:43]
v_exp_f32 v90,v90
v_mfma_f32_16x16x32_bf16 v[40:43],a[204:207],v[56:59],v[40:43]
s_waitcnt lgkmcnt(4)
buffer_load_dwordx4 v1,s[36:39],s61 offen offset:2048 sc1 lds
v_mfma_f32_16x16x32_bf16 v[40:43],a[216:219],v[60:63],v[40:43]
ds_read_b128 v[80:83],v2 offset:56320
v_fma_f32 v90,v90,v119,v119
v_mfma_f32_16x16x32_bf16 v[40:43],a[228:231],v[64:67],v[40:43]
v_rcp_f32 v89,v89
v_mfma_f32_16x16x32_bf16 v[44:47],a[232:235],v[64:67],v[44:47]
v_rcp_f32 v90,v90
v_mfma_f32_16x16x32_bf16 v[44:47],a[220:223],v[60:63],v[44:47]
buffer_load_dwordx4 v1,s[36:39],s61 offen offset:3072 sc1 lds
v_mfma_f32_16x16x32_bf16 v[48:51],a[224:227],v[60:63],v[48:51]
buffer_load_dword v104,v8,s[36:39],s72 offen offset:0 sc1
v_mfma_f32_16x16x32_bf16 v[48:51],a[236:239],v[64:67],v[48:51]
s_waitcnt lgkmcnt(2)
ds_read_b128 v[52:55],v2 offset:57344
v_fma_f32 v91,v118,v91,v117
v_mfma_f32_16x16x32_bf16 v[48:51],a[248:251],v[68:71],v[48:51]
ds_read_b128 v[56:59],v2 offset:58368
v_mul_f32 v91,v35,v91
v_mfma_f32_16x16x32_bf16 v[48:51],v[132:135],v[72:75],v[48:51]
v_exp_f32 v91,v91
v_mul_f32 v32,v32,v88
v_mfma_f32_16x16x32_bf16 v[44:47],v[128:131],v[72:75],v[44:47]
v_fma_f32 v91,v91,v119,v119
v_mul_f32 v33,v33,v89
v_mfma_f32_16x16x32_bf16 v[44:47],a[244:247],v[68:71],v[44:47]
ds_read_b128 v[60:63],v2 offset:59392
v_mul_f32 v34,v34,v90
v_mfma_f32_16x16x32_bf16 v[40:43],a[240:243],v[68:71],v[40:43]
v_rcp_f32 v91,v91
v_mfma_f32_16x16x32_bf16 v[40:43],a[252:255],v[72:75],v[40:43]
s_waitcnt lgkmcnt(3))"
R"(
v_mul_f32 v35,v35,v91
v_cvt_pk_bf16_f32 v98,v32,v33
v_mfma_f32_16x16x32_bf16 v[40:43],v[136:139],v[76:79],v[40:43]
ds_read_b128 v[64:67],v2 offset:60416
v_cvt_pk_bf16_f32 v99,v34,v35
v_mfma_f32_16x16x32_bf16 v[40:43],v[148:151],v[80:83],v[40:43]
v_fma_f32 v36,-v116,v24,v36
v_fma_f32 v37,-v116,v25,v37
v_mfma_f32_16x16x32_bf16 v[44:47],v[152:155],v[80:83],v[44:47]
v_fma_f32 v38,-v116,v26,v38
v_fma_f32 v39,-v116,v27,v39
v_mfma_f32_16x16x32_bf16 v[44:47],v[140:143],v[76:79],v[44:47]
ds_read_b128 v[68:71],v2 offset:61440
v_mul_f32 v92,v36,v36
v_mfma_f32_16x16x32_bf16 v[48:51],v[144:147],v[76:79],v[48:51]
v_mul_f32 v93,v37,v37
v_mul_f32 v94,v38,v38
v_mfma_f32_16x16x32_bf16 v[48:51],v[156:159],v[80:83],v[48:51]
s_waitcnt lgkmcnt(3)
v_mul_f32 v95,v39,v39
v_fma_f32 v92,v118,v92,v117
v_mfma_f32_16x16x32_bf16 v[48:51],v[168:171],v[52:55],v[48:51]
ds_read_b128 v[72:75],v2 offset:62464
v_mul_f32 v92,v36,v92
v_mfma_f32_16x16x32_bf16 v[48:51],v[180:183],v[56:59],v[48:51]
buffer_store_dwordx4 v[96:99],v3,s[36:39],s73 offen offset:0 sc1
v_exp_f32 v92,v92
v_fma_f32 v93,v118,v93,v117
v_mfma_f32_16x16x32_bf16 v[44:47],v[176:179],v[56:59],v[44:47]
v_mul_f32 v93,v37,v93
v_fma_f32 v92,v92,v119,v119
v_mfma_f32_16x16x32_bf16 v[44:47],v[164:167],v[52:55],v[44:47]
ds_read_b128 v[76:79],v2 offset:63488
v_fma_f32 v94,v118,v94,v117
v_mfma_f32_16x16x32_bf16 v[40:43],v[160:163],v[52:55],v[40:43]
v_exp_f32 v93,v93
v_mfma_f32_16x16x32_bf16 v[40:43],v[172:175],v[56:59],v[40:43]
s_waitcnt lgkmcnt(3)
v_rcp_f32 v92,v92
v_mfma_f32_16x16x32_bf16 v[40:43],v[184:187],v[60:63],v[40:43]
s_waitcnt vmcnt(6)
ds_read_b128 v[80:83],v2 offset:64512
v_mul_f32 v94,v38,v94
v_mfma_f32_16x16x32_bf16 v[40:43],v[196:199],v[64:67],v[40:43]
s_barrier
v_mfma_f32_16x16x32_bf16 v[44:47],v[200:203],v[64:67],v[44:47]
ds_read_b32 v13,v7 offset:256
v_xor_b32 v2,65536,v2
v_mfma_f32_16x16x32_bf16 v[44:47],v[188:191],v[60:63],v[44:47]
ds_read_b128 v[52:55],v2 offset:0
ds_read_b64 v[112:113],v11 offset:128
v_mfma_f32_16x16x32_bf16 v[48:51],v[192:195],v[60:63],v[48:51]
v_exp_f32 v94,v94
v_mfma_f32_16x16x32_bf16 v[48:51],v[204:207],v[64:67],v[48:51]
s_waitcnt lgkmcnt(5)
v_fma_f32 v93,v93,v119,v119
v_fma_f32 v94,v94,v119,v119
v_mfma_f32_16x16x32_bf16 v[48:51],v[216:219],v[68:71],v[48:51]
s_waitcnt lgkmcnt(2)
ds_read_b128 v[56:59],v2 offset:1024
v_fma_f32 v95,v118,v95,v117
v_mfma_f32_16x16x32_bf16 v[48:51],v[228:231],v[72:75],v[48:51]
s_waitcnt lgkmcnt(1)
v_cmp_eq_u32 vcc,s60,v13
v_rcp_f32 v93,v93
v_mul_f32 v95,v39,v95
v_mfma_f32_16x16x32_bf16 v[44:47],v[224:227],v[72:75],v[44:47]
s_cmp_eq_u32 vcc_lo,-1
v_exp_f32 v95,v95
v_mfma_f32_16x16x32_bf16 v[44:47],v[212:215],v[68:71],v[44:47]
s_cselect_b32 s66,s86,s59
ds_read_b128 v[60:63],v2 offset:2048
v_fma_f32 v95,v95,v119,v119
v_mfma_f32_16x16x32_bf16 v[40:43],v[208:211],v[68:71],v[40:43]
s_lshr_b32 s61,s66,0
v_rcp_f32 v94,v94
v_mfma_f32_16x16x32_bf16 v[40:43],v[220:223],v[72:75],v[40:43]
v_rcp_f32 v95,v95
v_mfma_f32_16x16x32_bf16 v[40:43],v[232:235],v[76:79],v[40:43]
s_xor_b32 s64,s64,65536
v_mul_f32 v36,v36,v92
v_mul_f32 v37,v37,v93
v_mul_f32 v38,v38,v94
v_mul_f32 v39,v39,v95
v_cvt_pk_bf16_f32 v100,v36,v37
v_cvt_pk_bf16_f32 v101,v38,v39
v_mfma_f32_16x16x32_bf16 v[40:43],v[244:247],v[80:83],v[40:43]
buffer_store_dwordx2 v[100:101],v9,s[36:39],s73 offen offset:0 sc1
ds_read_b128 v[64:67],v2 offset:3072
v_mul_f32 v116,s75,v112
v_fma_f32 v113,v113,s75,v12
v_mfma_f32_16x16x32_bf16 v[44:47],v[248:251],v[80:83],v[44:47]
s_cmp_lt_u32 s67,s59
v_fma_f32 v113,-v116,v116,v113
v_max_f32 v113,v113,v12
v_mfma_f32_16x16x32_bf16 v[44:47],v[236:239],v[76:79],v[44:47]
ds_read_b128 v[68:71],v2 offset:4096
v_mfma_f32_16x16x32_bf16 v[48:51],v[240:243],v[76:79],v[48:51]
s_waitcnt lgkmcnt(3)
v_rsq_f32 v112,v113
v_mfma_f32_16x16x32_bf16 v[48:51],v[252:255],v[80:83],v[48:51]
s_cbranch_scc1 Lg3_d0w2_%=
s_branch Lg3_slow0_%=
Lg3_d0w3_%=:
v_mfma_f32_16x16x32_bf16 v[36:39],a[8:11],v[52:55],0
s_mov_b32 m0,s62
ds_read_b128 v[72:75],v2 offset:5120
v_mul_f32 v117,s76,v112
v_mfma_f32_16x16x32_bf16 v[36:39],a[20:23],v[56:59],v[36:39]
s_mul_i32 s73,s47,7
v_mul_f32 v118,v112,v112
v_mul_f32 v119,v113,v112
v_mfma_f32_16x16x32_bf16 v[32:35],a[16:19],v[56:59],0
s_lshr_b32 s73,s73,1
v_mul_f32 v118,s77,v118
v_mul_f32 v118,v118,v117
v_mfma_f32_16x16x32_bf16 v[32:35],a[4:7],v[52:55],v[32:35]
s_add_u32 s53,s52,s55
ds_read_b128 v[76:79],v2 offset:6144
v_fma_f32 v40,-v116,v16,v40
v_fma_f32 v41,-v116,v17,v41
v_mfma_f32_16x16x32_bf16 v[28:31],a[0:3],v[52:55],0
buffer_load_dwordx4 v0,s[36:39],s61 offen offset:0 sc1 lds
v_mfma_f32_16x16x32_bf16 v[28:31],a[12:15],v[56:59],v[28:31]
s_waitcnt lgkmcnt(3)
v_fma_f32 v42,-v116,v18,v42
v_fma_f32 v43,-v116,v19,v43
v_mfma_f32_16x16x32_bf16 v[28:31],a[24:27],v[60:63],v[28:31]
s_cmp_eq_u32 s53,s56
ds_read_b128 v[80:83],v2 offset:7168
v_mul_f32 v84,v40,v40
v_mfma_f32_16x16x32_bf16 v[28:31],a[36:39],v[64:67],v[28:31]
s_cselect_b32 s53,s57,s53
v_mul_f32 v85,v41,v41
v_mul_f32 v86,v42,v42
v_mfma_f32_16x16x32_bf16 v[32:35],a[40:43],v[64:67],v[32:35]
buffer_load_dwordx4 v0,s[36:39],s61 offen offset:1024 sc1 lds
v_mfma_f32_16x16x32_bf16 v[32:35],a[28:31],v[60:63],v[32:35]
s_cmp_eq_u32 s53,s58
ds_read_b128 v[52:55],v2 offset:8192
v_mul_f32 v87,v43,v43
v_mfma_f32_16x16x32_bf16 v[36:39],a[32:35],v[60:63],v[36:39]
s_cselect_b32 s53,s59,s53
v_fma_f32 v84,v118,v84,v117
v_mul_f32 v84,v40,v84
v_mfma_f32_16x16x32_bf16 v[36:39],a[44:47],v[64:67],v[36:39]
s_waitcnt lgkmcnt(3)
v_exp_f32 v84,v84
v_mfma_f32_16x16x32_bf16 v[36:39],a[56:59],v[68:71],v[36:39]
buffer_load_dwordx4 v0,s[36:39],s61 offen offset:2048 sc1 lds
v_mfma_f32_16x16x32_bf16 v[36:39],a[68:71],v[72:75],v[36:39]
s_cmp_ge_u32 s66,s59
ds_read_b128 v[56:59],v2 offset:9216
v_fma_f32 v84,v84,v119,v119
v_mfma_f32_16x16x32_bf16 v[32:35],a[64:67],v[72:75],v[32:35]
s_cselect_b32 s86,s49,s50
v_rcp_f32 v84,v84
v_mfma_f32_16x16x32_bf16 v[32:35],a[52:55],v[68:71],v[32:35]
s_mul_i32 s70,s46,5
ds_read_b128 v[60:63],v2 offset:10240
v_fma_f32 v85,v118,v85,v117
v_mfma_f32_16x16x32_bf16 v[28:31],a[48:51],v[68:71],v[28:31]
buffer_load_dwordx4 v0,s[36:39],s61 offen offset:3072 sc1 lds
v_mfma_f32_16x16x32_bf16 v[28:31],a[60:63],v[72:75],v[28:31]
s_waitcnt lgkmcnt(3)
v_mul_f32 v85,v41,v85
v_fma_f32 v86,v118,v86,v117
v_mfma_f32_16x16x32_bf16 v[28:31],a[72:75],v[76:79],v[28:31]
s_add_u32 m0,s62,16384
ds_read_b128 v[64:67],v2 offset:11264
v_mul_f32 v86,v42,v86
v_mfma_f32_16x16x32_bf16 v[28:31],a[84:87],v[80:83],v[28:31]
s_lshr_b32 s72,s61,5
v_exp_f32 v85,v85
v_mfma_f32_16x16x32_bf16 v[32:35],a[88:91],v[80:83],v[32:35]
s_lshr_b32 s70,s70,6
v_exp_f32 v86,v86
v_mfma_f32_16x16x32_bf16 v[32:35],a[76:79],v[76:79],v[32:35]
s_mov_b32 s62,s63
ds_read_b128 v[68:71],v2 offset:12288
v_fma_f32 v85,v85,v119,v119
v_mfma_f32_16x16x32_bf16 v[36:39],a[80:83],v[76:79],v[36:39]
v_rcp_f32 v85,v85
v_mfma_f32_16x16x32_bf16 v[36:39],a[92:95],v[80:83],v[36:39]
s_waitcnt lgkmcnt(3)
v_fma_f32 v86,v86,v119,v119
v_fma_f32 v87,v118,v87,v117
v_mfma_f32_16x16x32_bf16 v[36:39],a[104:107],v[52:55],v[36:39]
ds_read_b128 v[72:75],v2 offset:13312
v_mul_f32 v87,v43,v87
v_mfma_f32_16x16x32_bf16 v[36:39],a[116:119],v[56:59],v[36:39]
v_exp_f32 v87,v87
v_mul_f32 v40,v40,v84
v_mfma_f32_16x16x32_bf16 v[32:35],a[112:115],v[56:59],v[32:35]
v_rcp_f32 v86,v86
v_mfma_f32_16x16x32_bf16 v[32:35],a[100:103],v[52:55],v[32:35]
ds_read_b128 v[76:79],v2 offset:14336
v_fma_f32 v87,v87,v119,v119
v_mfma_f32_16x16x32_bf16 v[28:31],a[96:99],v[52:55],v[28:31]
v_rcp_f32 v87,v87)"
R"(
v_mfma_f32_16x16x32_bf16 v[28:31],a[108:111],v[56:59],v[28:31]
s_waitcnt lgkmcnt(3)
v_mul_f32 v41,v41,v85
v_mul_f32 v42,v42,v86
v_mfma_f32_16x16x32_bf16 v[28:31],a[120:123],v[60:63],v[28:31]
s_waitcnt vmcnt(7)
ds_read_b128 v[80:83],v2 offset:15360
v_mul_f32 v43,v43,v87
v_mfma_f32_16x16x32_bf16 v[28:31],a[132:135],v[64:67],v[28:31]
s_barrier
v_mfma_f32_16x16x32_bf16 v[32:35],a[136:139],v[64:67],v[32:35]
v_add_f32_dpp v104,v104,v104 quad_perm:[1,0,3,2] row_mask:0xf bank_mask:0xf
v_cvt_pk_bf16_f32 v96,v40,v41
v_mfma_f32_16x16x32_bf16 v[32:35],a[124:127],v[60:63],v[32:35]
ds_read_b128 v[52:55],v2 offset:16384
v_add_f32_dpp v104,v104,v104 quad_perm:[2,3,0,1] row_mask:0xf bank_mask:0xf
v_mfma_f32_16x16x32_bf16 v[36:39],a[128:131],v[60:63],v[36:39]
s_nop 0
v_add_f32_dpp v104,v104,v104 row_half_mirror row_mask:0xf bank_mask:0xf
v_cvt_pk_bf16_f32 v97,v42,v43
v_mfma_f32_16x16x32_bf16 v[36:39],a[140:143],v[64:67],v[36:39]
s_waitcnt lgkmcnt(3)
v_fma_f32 v44,-v116,v20,v44
v_fma_f32 v45,-v116,v21,v45
v_mfma_f32_16x16x32_bf16 v[36:39],a[152:155],v[68:71],v[36:39]
ds_read_b128 v[56:59],v2 offset:17408
v_fma_f32 v46,-v116,v22,v46
v_mfma_f32_16x16x32_bf16 v[36:39],a[164:167],v[72:75],v[36:39]
v_fma_f32 v47,-v116,v23,v47
v_mul_f32 v88,v44,v44
v_mfma_f32_16x16x32_bf16 v[32:35],a[160:163],v[72:75],v[32:35]
v_mul_f32 v89,v45,v45
v_mul_f32 v90,v46,v46
v_mfma_f32_16x16x32_bf16 v[32:35],a[148:151],v[68:71],v[32:35]
ds_read_b128 v[60:63],v2 offset:18432
v_mul_f32 v91,v47,v47
v_mfma_f32_16x16x32_bf16 v[28:31],a[144:147],v[68:71],v[28:31]
v_fma_f32 v88,v118,v88,v117
v_mul_f32 v88,v44,v88
v_mfma_f32_16x16x32_bf16 v[28:31],a[156:159],v[72:75],v[28:31]
s_waitcnt lgkmcnt(3)
v_exp_f32 v88,v88
v_mfma_f32_16x16x32_bf16 v[28:31],a[168:171],v[76:79],v[28:31]
ds_read_b128 v[64:67],v2 offset:19456
v_fma_f32 v88,v88,v119,v119
v_mfma_f32_16x16x32_bf16 v[28:31],a[180:183],v[80:83],v[28:31]
v_rcp_f32 v88,v88
v_mfma_f32_16x16x32_bf16 v[32:35],a[184:187],v[80:83],v[32:35]
v_fma_f32 v89,v118,v89,v117
v_mul_f32 v89,v45,v89
v_mfma_f32_16x16x32_bf16 v[32:35],a[172:175],v[76:79],v[32:35]
ds_read_b128 v[68:71],v2 offset:20480
v_fma_f32 v90,v118,v90,v117
v_mfma_f32_16x16x32_bf16 v[36:39],a[176:179],v[76:79],v[36:39]
buffer_load_dwordx4 v1,s[36:39],s61 offen offset:0 sc1 lds
v_mfma_f32_16x16x32_bf16 v[36:39],a[188:191],v[80:83],v[36:39]
s_waitcnt lgkmcnt(3)
ds_write_b32 v10,v104 offset:0
v_mfma_f32_16x16x32_bf16 v[36:39],a[200:203],v[52:55],v[36:39]
ds_read_b128 v[72:75],v2 offset:21504
v_mul_f32 v90,v46,v90
v_mfma_f32_16x16x32_bf16 v[36:39],a[212:215],v[56:59],v[36:39]
v_exp_f32 v89,v89
v_fma_f32 v91,v118,v91,v117
v_mfma_f32_16x16x32_bf16 v[32:35],a[208:211],v[56:59],v[32:35]
buffer_load_dwordx4 v1,s[36:39],s61 offen offset:1024 sc1 lds
v_mfma_f32_16x16x32_bf16 v[32:35],a[196:199],v[52:55],v[32:35]
ds_read_b128 v[76:79],v2 offset:22528
v_mul_f32 v91,v47,v91
v_mfma_f32_16x16x32_bf16 v[28:31],a[192:195],v[52:55],v[28:31]
v_exp_f32 v90,v90
v_mfma_f32_16x16x32_bf16 v[28:31],a[204:207],v[56:59],v[28:31]
s_waitcnt lgkmcnt(4)
v_exp_f32 v91,v91
v_mfma_f32_16x16x32_bf16 v[28:31],a[216:219],v[60:63],v[28:31]
buffer_load_dwordx4 v1,s[36:39],s61 offen offset:2048 sc1 lds
v_mfma_f32_16x16x32_bf16 v[28:31],a[228:231],v[64:67],v[28:31]
ds_read_b128 v[80:83],v2 offset:23552
v_fma_f32 v89,v89,v119,v119
v_mfma_f32_16x16x32_bf16 v[32:35],a[232:235],v[64:67],v[32:35]
v_rcp_f32 v89,v89
v_mfma_f32_16x16x32_bf16 v[32:35],a[220:223],v[60:63],v[32:35]
ds_read_b128 v[52:55],v2 offset:24576
v_fma_f32 v90,v90,v119,v119
v_mfma_f32_16x16x32_bf16 v[36:39],a[224:227],v[60:63],v[36:39]
buffer_load_dwordx4 v1,s[36:39],s61 offen offset:3072 sc1 lds
v_mfma_f32_16x16x32_bf16 v[36:39],a[236:239],v[64:67],v[36:39]
s_waitcnt lgkmcnt(3)
buffer_load_dword v108,v8,s[36:39],s72 offen offset:0 sc1
v_mfma_f32_16x16x32_bf16 v[36:39],a[248:251],v[68:71],v[36:39]
ds_read_b128 v[56:59],v2 offset:25600
v_fma_f32 v91,v91,v119,v119
v_mfma_f32_16x16x32_bf16 v[36:39],v[132:135],v[72:75],v[36:39]
v_rcp_f32 v90,v90
v_mfma_f32_16x16x32_bf16 v[32:35],v[128:131],v[72:75],v[32:35]
v_rcp_f32 v91,v91
v_mfma_f32_16x16x32_bf16 v[32:35],a[244:247],v[68:71],v[32:35]
ds_read_b128 v[60:63],v2 offset:26624
v_mul_f32 v44,v44,v88
v_mfma_f32_16x16x32_bf16 v[28:31],a[240:243],v[68:71],v[28:31]
v_mul_f32 v45,v45,v89
v_mul_f32 v46,v46,v90
v_mfma_f32_16x16x32_bf16 v[28:31],a[252:255],v[72:75],v[28:31]
s_waitcnt lgkmcnt(3)
v_mul_f32 v47,v47,v91
v_cvt_pk_bf16_f32 v98,v44,v45
v_mfma_f32_16x16x32_bf16 v[28:31],v[136:139],v[76:79],v[28:31]
ds_read_b128 v[64:67],v2 offset:27648
v_cvt_pk_bf16_f32 v99,v46,v47
v_mfma_f32_16x16x32_bf16 v[28:31],v[148:151],v[80:83],v[28:31]
v_fma_f32 v48,-v116,v24,v48
v_fma_f32 v49,-v116,v25,v49
v_mfma_f32_16x16x32_bf16 v[32:35],v[152:155],v[80:83],v[32:35]
v_fma_f32 v50,-v116,v26,v50
v_fma_f32 v51,-v116,v27,v51
v_mfma_f32_16x16x32_bf16 v[32:35],v[140:143],v[76:79],v[32:35]
ds_read_b128 v[68:71],v2 offset:28672
v_mul_f32 v92,v48,v48
v_mul_f32 v93,v49,v49
v_mfma_f32_16x16x32_bf16 v[36:39],v[144:147],v[76:79],v[36:39]
v_mul_f32 v94,v50,v50
v_mul_f32 v95,v51,v51
v_mfma_f32_16x16x32_bf16 v[36:39],v[156:159],v[80:83],v[36:39]
s_waitcnt lgkmcnt(3)
v_fma_f32 v92,v118,v92,v117
v_mul_f32 v92,v48,v92
v_mfma_f32_16x16x32_bf16 v[36:39],v[168:171],v[52:55],v[36:39]
ds_read_b128 v[72:75],v2 offset:29696
v_fma_f32 v93,v118,v93,v117
v_mfma_f32_16x16x32_bf16 v[36:39],v[180:183],v[56:59],v[36:39]
v_exp_f32 v92,v92
v_mfma_f32_16x16x32_bf16 v[32:35],v[176:179],v[56:59],v[32:35]
v_mul_f32 v93,v49,v93
v_fma_f32 v92,v92,v119,v119
v_mfma_f32_16x16x32_bf16 v[32:35],v[164:167],v[52:55],v[32:35]
buffer_store_dwordx4 v[96:99],v3,s[36:39],s73 offen offset:0 sc1
ds_read_b128 v[76:79],v2 offset:30720
v_exp_f32 v93,v93
v_mfma_f32_16x16x32_bf16 v[28:31],v[160:163],v[52:55],v[28:31]
v_rcp_f32 v92,v92
v_mfma_f32_16x16x32_bf16 v[28:31],v[172:175],v[56:59],v[28:31]
s_waitcnt lgkmcnt(3)
v_fma_f32 v93,v93,v119,v119
v_fma_f32 v94,v118,v94,v117
v_mfma_f32_16x16x32_bf16 v[28:31],v[184:187],v[60:63],v[28:31]
s_waitcnt vmcnt(6)
ds_read_b128 v[80:83],v2 offset:31744
v_mul_f32 v94,v50,v94
v_mfma_f32_16x16x32_bf16 v[28:31],v[196:199],v[64:67],v[28:31]
s_barrier
v_mfma_f32_16x16x32_bf16 v[32:35],v[200:203],v[64:67],v[32:35]
ds_read_b32 v13,v7 offset:0
ds_read_b64 v[112:113],v11 offset:0
v_mfma_f32_16x16x32_bf16 v[32:35],v[188:191],v[60:63],v[32:35]
buffer_store_dword v5,v4,s[36:39],s70 offen offset:0 sc1
ds_read_b128 v[52:55],v2 offset:32768
v_exp_f32 v94,v94
v_mfma_f32_16x16x32_bf16 v[36:39],v[192:195],v[60:63],v[36:39]
v_rcp_f32 v93,v93
v_mfma_f32_16x16x32_bf16 v[36:39],v[204:207],v[64:67],v[36:39]
s_waitcnt lgkmcnt(5)
v_fma_f32 v94,v94,v119,v119
v_fma_f32 v95,v118,v95,v117
v_mfma_f32_16x16x32_bf16 v[36:39],v[216:219],v[68:71],v[36:39]
s_waitcnt lgkmcnt(2)
ds_read_b128 v[56:59],v2 offset:33792
v_mul_f32 v95,v51,v95
v_mfma_f32_16x16x32_bf16 v[36:39],v[228:231],v[72:75],v[36:39]
s_waitcnt lgkmcnt(2)
v_cmp_eq_u32 vcc,s60,v13
v_exp_f32 v95,v95
v_mul_f32 v48,v48,v92
v_mfma_f32_16x16x32_bf16 v[32:35],v[224:227],v[72:75],v[32:35]
s_cmp_eq_u32 vcc_lo,-1
v_rcp_f32 v94,v94
v_mfma_f32_16x16x32_bf16 v[32:35],v[212:215],v[68:71],v[32:35]
s_cselect_b32 s67,s86,s59
ds_read_b128 v[60:63],v2 offset:34816
v_fma_f32 v95,v95,v119,v119
v_mfma_f32_16x16x32_bf16 v[28:31],v[208:211],v[68:71],v[28:31]
s_lshr_b32 s61,s67,0
v_rcp_f32 v95,v95
v_mfma_f32_16x16x32_bf16 v[28:31],v[220:223],v[72:75],v[28:31]
v_mul_f32 v116,s75,v112
v_mul_f32 v49,v49,v93
v_mfma_f32_16x16x32_bf16 v[28:31],v[232:235],v[76:79],v[28:31]
s_xor_b32 s63,s63,65536
ds_read_b128 v[64:67],v2 offset:35840
v_mul_f32 v50,v50,v94
v_mfma_f32_16x16x32_bf16 v[28:31],v[244:247],v[80:83],v[28:31]
s_cmp_ge_u32 s66,s59
v_mul_f32 v51,v51,v95
v_cvt_pk_bf16_f32 v100,v48,v49
v_mfma_f32_16x16x32_bf16 v[32:35],v[248:251],v[80:83],v[32:35]
v_cvt_pk_bf16_f32 v101,v50,v51
v_fma_f32 v113,v113,s75,v12
v_mfma_f32_16x16x32_bf16 v[32:35],v[236:239],v[76:79],v[32:35]
buffer_store_dwordx2 v[100:101],v9,s[36:39],s73 offen offset:0 sc1
ds_read_b128 v[68:71],v2 offset:36864
v_fma_f32 v113,-v116,v116,v113)"
R"(
v_max_f32 v113,v113,v12
v_mfma_f32_16x16x32_bf16 v[36:39],v[240:243],v[76:79],v[36:39]
s_waitcnt lgkmcnt(3)
v_rsq_f32 v112,v113
v_mfma_f32_16x16x32_bf16 v[36:39],v[252:255],v[80:83],v[36:39]
s_cbranch_scc1 Lg3_slow1_%=
Lg3_d1w3_%=:
v_mfma_f32_16x16x32_bf16 v[48:51],a[8:11],v[52:55],0
s_mov_b32 m0,s62
ds_read_b128 v[72:75],v2 offset:37888
v_mul_f32 v117,s76,v112
v_mfma_f32_16x16x32_bf16 v[48:51],a[20:23],v[56:59],v[48:51]
s_mul_i32 s73,s48,7
v_mul_f32 v118,v112,v112
v_mul_f32 v119,v113,v112
v_mfma_f32_16x16x32_bf16 v[44:47],a[16:19],v[56:59],0
s_lshr_b32 s73,s73,1
v_mul_f32 v118,s77,v118
v_mul_f32 v118,v118,v117
v_mfma_f32_16x16x32_bf16 v[44:47],a[4:7],v[52:55],v[44:47]
s_add_u32 s74,s53,s55
ds_read_b128 v[76:79],v2 offset:38912
v_fma_f32 v28,-v116,v16,v28
v_fma_f32 v29,-v116,v17,v29
v_mfma_f32_16x16x32_bf16 v[40:43],a[0:3],v[52:55],0
buffer_load_dwordx4 v0,s[36:39],s61 offen offset:0 sc1 lds
v_mfma_f32_16x16x32_bf16 v[40:43],a[12:15],v[56:59],v[40:43]
s_waitcnt lgkmcnt(3)
v_fma_f32 v30,-v116,v18,v30
v_fma_f32 v31,-v116,v19,v31
v_mfma_f32_16x16x32_bf16 v[40:43],a[24:27],v[60:63],v[40:43]
s_cmp_eq_u32 s74,s56
ds_read_b128 v[80:83],v2 offset:39936
v_mul_f32 v84,v28,v28
v_mfma_f32_16x16x32_bf16 v[40:43],a[36:39],v[64:67],v[40:43]
s_cselect_b32 s74,s57,s74
v_mul_f32 v85,v29,v29
v_mul_f32 v86,v30,v30
v_mfma_f32_16x16x32_bf16 v[44:47],a[40:43],v[64:67],v[44:47]
buffer_load_dwordx4 v0,s[36:39],s61 offen offset:1024 sc1 lds
v_mfma_f32_16x16x32_bf16 v[44:47],a[28:31],v[60:63],v[44:47]
s_cmp_eq_u32 s74,s58
ds_read_b128 v[52:55],v2 offset:40960
v_mul_f32 v87,v31,v31
v_mfma_f32_16x16x32_bf16 v[48:51],a[32:35],v[60:63],v[48:51]
s_cselect_b32 s74,s59,s74
v_fma_f32 v84,v118,v84,v117
v_mul_f32 v84,v28,v84
v_mfma_f32_16x16x32_bf16 v[48:51],a[44:47],v[64:67],v[48:51]
s_waitcnt lgkmcnt(3)
v_exp_f32 v84,v84
v_mfma_f32_16x16x32_bf16 v[48:51],a[56:59],v[68:71],v[48:51]
buffer_load_dwordx4 v0,s[36:39],s61 offen offset:2048 sc1 lds
v_mfma_f32_16x16x32_bf16 v[48:51],a[68:71],v[72:75],v[48:51]
s_cmp_ge_u32 s67,s59
ds_read_b128 v[56:59],v2 offset:41984
v_fma_f32 v84,v84,v119,v119
v_mfma_f32_16x16x32_bf16 v[44:47],a[64:67],v[72:75],v[44:47]
s_cselect_b32 s86,s50,s51
v_rcp_f32 v84,v84
v_mfma_f32_16x16x32_bf16 v[44:47],a[52:55],v[68:71],v[44:47]
s_mul_i32 s70,s47,5
ds_read_b128 v[60:63],v2 offset:43008
v_fma_f32 v85,v118,v85,v117
v_mfma_f32_16x16x32_bf16 v[40:43],a[48:51],v[68:71],v[40:43]
buffer_load_dwordx4 v0,s[36:39],s61 offen offset:3072 sc1 lds
v_mfma_f32_16x16x32_bf16 v[40:43],a[60:63],v[72:75],v[40:43]
s_waitcnt lgkmcnt(3)
v_mul_f32 v85,v29,v85
v_fma_f32 v86,v118,v86,v117
v_mfma_f32_16x16x32_bf16 v[40:43],a[72:75],v[76:79],v[40:43]
s_add_u32 m0,s62,16384
ds_read_b128 v[64:67],v2 offset:44032
v_mul_f32 v86,v30,v86
v_mfma_f32_16x16x32_bf16 v[40:43],a[84:87],v[80:83],v[40:43]
s_lshr_b32 s72,s61,5
v_exp_f32 v85,v85
v_fma_f32 v87,v118,v87,v117
v_mfma_f32_16x16x32_bf16 v[44:47],a[88:91],v[80:83],v[44:47]
s_lshr_b32 s70,s70,6
v_exp_f32 v86,v86
v_mfma_f32_16x16x32_bf16 v[44:47],a[76:79],v[76:79],v[44:47]
s_mov_b32 s62,s64
ds_read_b128 v[68:71],v2 offset:45056
v_mul_f32 v87,v31,v87
v_mfma_f32_16x16x32_bf16 v[48:51],a[80:83],v[76:79],v[48:51]
s_mov_b64 s[44:45],s[46:47]
v_exp_f32 v87,v87
v_mfma_f32_16x16x32_bf16 v[48:51],a[92:95],v[80:83],v[48:51]
s_waitcnt lgkmcnt(3)
v_fma_f32 v85,v85,v119,v119
v_fma_f32 v86,v86,v119,v119
v_mfma_f32_16x16x32_bf16 v[48:51],a[104:107],v[52:55],v[48:51]
s_mov_b64 s[46:47],s[48:49]
ds_read_b128 v[72:75],v2 offset:46080
v_fma_f32 v87,v87,v119,v119
v_mfma_f32_16x16x32_bf16 v[48:51],a[116:119],v[56:59],v[48:51]
s_mov_b64 s[48:49],s[50:51]
v_rcp_f32 v85,v85
v_mfma_f32_16x16x32_bf16 v[44:47],a[112:115],v[56:59],v[44:47]
s_mov_b64 s[50:51],s[52:53]
v_rcp_f32 v86,v86
v_mfma_f32_16x16x32_bf16 v[44:47],a[100:103],v[52:55],v[44:47]
s_mov_b32 s52,s74
ds_read_b128 v[76:79],v2 offset:47104
v_mul_f32 v28,v28,v84
v_mfma_f32_16x16x32_bf16 v[40:43],a[96:99],v[52:55],v[40:43]
v_rcp_f32 v87,v87
v_mfma_f32_16x16x32_bf16 v[40:43],a[108:111],v[56:59],v[40:43]
s_waitcnt lgkmcnt(3)
v_mul_f32 v29,v29,v85
v_mul_f32 v30,v30,v86
v_mfma_f32_16x16x32_bf16 v[40:43],a[120:123],v[60:63],v[40:43]
s_waitcnt vmcnt(7)
ds_read_b128 v[80:83],v2 offset:48128
v_mul_f32 v31,v31,v87
v_mfma_f32_16x16x32_bf16 v[40:43],a[132:135],v[64:67],v[40:43]
s_barrier
v_mfma_f32_16x16x32_bf16 v[44:47],a[136:139],v[64:67],v[44:47]
v_add_f32_dpp v108,v108,v108 quad_perm:[1,0,3,2] row_mask:0xf bank_mask:0xf
v_cvt_pk_bf16_f32 v96,v28,v29
v_mfma_f32_16x16x32_bf16 v[44:47],a[124:127],v[60:63],v[44:47]
ds_read_b128 v[52:55],v2 offset:49152
v_add_f32_dpp v108,v108,v108 quad_perm:[2,3,0,1] row_mask:0xf bank_mask:0xf
v_mfma_f32_16x16x32_bf16 v[48:51],a[128:131],v[60:63],v[48:51]
s_nop 0
v_add_f32_dpp v108,v108,v108 row_half_mirror row_mask:0xf bank_mask:0xf
v_cvt_pk_bf16_f32 v97,v30,v31
v_mfma_f32_16x16x32_bf16 v[48:51],a[140:143],v[64:67],v[48:51]
s_waitcnt lgkmcnt(3)
v_fma_f32 v32,-v116,v20,v32
v_fma_f32 v33,-v116,v21,v33
v_mfma_f32_16x16x32_bf16 v[48:51],a[152:155],v[68:71],v[48:51]
ds_read_b128 v[56:59],v2 offset:50176
v_fma_f32 v34,-v116,v22,v34
v_mfma_f32_16x16x32_bf16 v[48:51],a[164:167],v[72:75],v[48:51]
v_fma_f32 v35,-v116,v23,v35
v_mul_f32 v88,v32,v32
v_mfma_f32_16x16x32_bf16 v[44:47],a[160:163],v[72:75],v[44:47]
v_mul_f32 v89,v33,v33
v_mul_f32 v90,v34,v34
v_mfma_f32_16x16x32_bf16 v[44:47],a[148:151],v[68:71],v[44:47]
ds_read_b128 v[60:63],v2 offset:51200
v_mul_f32 v91,v35,v35
v_fma_f32 v88,v118,v88,v117
v_mfma_f32_16x16x32_bf16 v[40:43],a[144:147],v[68:71],v[40:43]
v_mul_f32 v88,v32,v88
v_fma_f32 v89,v118,v89,v117
v_mfma_f32_16x16x32_bf16 v[40:43],a[156:159],v[72:75],v[40:43]
s_waitcnt lgkmcnt(3)
v_exp_f32 v88,v88
v_mfma_f32_16x16x32_bf16 v[40:43],a[168:171],v[76:79],v[40:43]
ds_read_b128 v[64:67],v2 offset:52224
v_mul_f32 v89,v33,v89
v_mfma_f32_16x16x32_bf16 v[40:43],a[180:183],v[80:83],v[40:43]
v_exp_f32 v89,v89
v_mfma_f32_16x16x32_bf16 v[44:47],a[184:187],v[80:83],v[44:47]
v_fma_f32 v88,v88,v119,v119
v_fma_f32 v89,v89,v119,v119
v_mfma_f32_16x16x32_bf16 v[44:47],a[172:175],v[76:79],v[44:47]
ds_read_b128 v[68:71],v2 offset:53248
v_fma_f32 v90,v118,v90,v117
v_mfma_f32_16x16x32_bf16 v[48:51],a[176:179],v[76:79],v[48:51]
buffer_load_dwordx4 v1,s[36:39],s61 offen offset:0 sc1 lds
v_mfma_f32_16x16x32_bf16 v[48:51],a[188:191],v[80:83],v[48:51]
s_waitcnt lgkmcnt(3)
ds_write_b32 v10,v108 offset:128
v_mfma_f32_16x16x32_bf16 v[48:51],a[200:203],v[52:55],v[48:51]
ds_read_b128 v[72:75],v2 offset:54272
v_fma_f32 v91,v118,v91,v117
v_mfma_f32_16x16x32_bf16 v[48:51],a[212:215],v[56:59],v[48:51]
v_rcp_f32 v88,v88
v_mfma_f32_16x16x32_bf16 v[44:47],a[208:211],v[56:59],v[44:47]
buffer_load_dwordx4 v1,s[36:39],s61 offen offset:1024 sc1 lds
v_mfma_f32_16x16x32_bf16 v[44:47],a[196:199],v[52:55],v[44:47]
ds_read_b128 v[76:79],v2 offset:55296
v_mul_f32 v90,v34,v90
v_mfma_f32_16x16x32_bf16 v[40:43],a[192:195],v[52:55],v[40:43]
v_exp_f32 v90,v90
v_mfma_f32_16x16x32_bf16 v[40:43],a[204:207],v[56:59],v[40:43]
s_waitcnt lgkmcnt(4)
v_rcp_f32 v89,v89
v_mfma_f32_16x16x32_bf16 v[40:43],a[216:219],v[60:63],v[40:43]
buffer_load_dwordx4 v1,s[36:39],s61 offen offset:2048 sc1 lds
v_mfma_f32_16x16x32_bf16 v[40:43],a[228:231],v[64:67],v[40:43]
ds_read_b128 v[80:83],v2 offset:56320
v_mul_f32 v91,v35,v91
v_fma_f32 v90,v90,v119,v119
v_mfma_f32_16x16x32_bf16 v[44:47],a[232:235],v[64:67],v[44:47]
v_exp_f32 v91,v91
v_mfma_f32_16x16x32_bf16 v[44:47],a[220:223],v[60:63],v[44:47]
ds_read_b128 v[52:55],v2 offset:57344
v_fma_f32 v91,v91,v119,v119
v_mfma_f32_16x16x32_bf16 v[48:51],a[224:227],v[60:63],v[48:51]
buffer_load_dwordx4 v1,s[36:39],s61 offen offset:3072 sc1 lds
v_mfma_f32_16x16x32_bf16 v[48:51],a[236:239],v[64:67],v[48:51]
s_waitcnt lgkmcnt(3)
buffer_load_dword v104,v8,s[36:39],s72 offen offset:0 sc1
v_mfma_f32_16x16x32_bf16 v[48:51],a[248:251],v[68:71],v[48:51]
ds_read_b128 v[56:59],v2 offset:58368)"
R"(
v_mul_f32 v32,v32,v88
v_mfma_f32_16x16x32_bf16 v[48:51],v[132:135],v[72:75],v[48:51]
v_rcp_f32 v90,v90
v_mfma_f32_16x16x32_bf16 v[44:47],v[128:131],v[72:75],v[44:47]
v_rcp_f32 v91,v91
v_mfma_f32_16x16x32_bf16 v[44:47],a[244:247],v[68:71],v[44:47]
ds_read_b128 v[60:63],v2 offset:59392
v_mul_f32 v33,v33,v89
v_mfma_f32_16x16x32_bf16 v[40:43],a[240:243],v[68:71],v[40:43]
v_mul_f32 v34,v34,v90
v_mul_f32 v35,v35,v91
v_mfma_f32_16x16x32_bf16 v[40:43],a[252:255],v[72:75],v[40:43]
s_waitcnt lgkmcnt(3)
v_cvt_pk_bf16_f32 v98,v32,v33
v_cvt_pk_bf16_f32 v99,v34,v35
v_mfma_f32_16x16x32_bf16 v[40:43],v[136:139],v[76:79],v[40:43]
ds_read_b128 v[64:67],v2 offset:60416
v_fma_f32 v36,-v116,v24,v36
v_mfma_f32_16x16x32_bf16 v[40:43],v[148:151],v[80:83],v[40:43]
v_fma_f32 v37,-v116,v25,v37
v_fma_f32 v38,-v116,v26,v38
v_mfma_f32_16x16x32_bf16 v[44:47],v[152:155],v[80:83],v[44:47]
v_fma_f32 v39,-v116,v27,v39
v_mul_f32 v92,v36,v36
v_mfma_f32_16x16x32_bf16 v[44:47],v[140:143],v[76:79],v[44:47]
ds_read_b128 v[68:71],v2 offset:61440
v_mul_f32 v93,v37,v37
v_mfma_f32_16x16x32_bf16 v[48:51],v[144:147],v[76:79],v[48:51]
v_mul_f32 v94,v38,v38
v_mul_f32 v95,v39,v39
v_mfma_f32_16x16x32_bf16 v[48:51],v[156:159],v[80:83],v[48:51]
s_waitcnt lgkmcnt(3)
v_fma_f32 v92,v118,v92,v117
v_mul_f32 v92,v36,v92
v_mfma_f32_16x16x32_bf16 v[48:51],v[168:171],v[52:55],v[48:51]
ds_read_b128 v[72:75],v2 offset:62464
v_fma_f32 v93,v118,v93,v117
v_mfma_f32_16x16x32_bf16 v[48:51],v[180:183],v[56:59],v[48:51]
v_exp_f32 v92,v92
v_mul_f32 v93,v37,v93
v_mfma_f32_16x16x32_bf16 v[44:47],v[176:179],v[56:59],v[44:47]
v_exp_f32 v93,v93
v_mfma_f32_16x16x32_bf16 v[44:47],v[164:167],v[52:55],v[44:47]
buffer_store_dwordx4 v[96:99],v3,s[36:39],s73 offen offset:0 sc1
ds_read_b128 v[76:79],v2 offset:63488
v_fma_f32 v92,v92,v119,v119
v_fma_f32 v93,v93,v119,v119
v_mfma_f32_16x16x32_bf16 v[40:43],v[160:163],v[52:55],v[40:43]
v_rcp_f32 v92,v92
v_mfma_f32_16x16x32_bf16 v[40:43],v[172:175],v[56:59],v[40:43]
s_waitcnt lgkmcnt(3)
v_rcp_f32 v93,v93
v_mfma_f32_16x16x32_bf16 v[40:43],v[184:187],v[60:63],v[40:43]
s_waitcnt vmcnt(6)
ds_read_b128 v[80:83],v2 offset:64512
v_xor_b32 v2,65536,v2
v_mfma_f32_16x16x32_bf16 v[40:43],v[196:199],v[64:67],v[40:43]
s_barrier
v_mfma_f32_16x16x32_bf16 v[44:47],v[200:203],v[64:67],v[44:47]
ds_read_b32 v13,v7 offset:256
ds_read_b64 v[112:113],v11 offset:128
v_mfma_f32_16x16x32_bf16 v[44:47],v[188:191],v[60:63],v[44:47]
buffer_store_dword v5,v4,s[36:39],s70 offen offset:0 sc1
ds_read_b128 v[52:55],v2 offset:0
v_fma_f32 v94,v118,v94,v117
v_mul_f32 v94,v38,v94
v_mfma_f32_16x16x32_bf16 v[48:51],v[192:195],v[60:63],v[48:51]
v_exp_f32 v94,v94
v_mfma_f32_16x16x32_bf16 v[48:51],v[204:207],v[64:67],v[48:51]
s_waitcnt lgkmcnt(5)
v_fma_f32 v94,v94,v119,v119
v_fma_f32 v95,v118,v95,v117
v_mfma_f32_16x16x32_bf16 v[48:51],v[216:219],v[68:71],v[48:51]
s_waitcnt lgkmcnt(2)
ds_read_b128 v[56:59],v2 offset:1024
v_mul_f32 v95,v39,v95
v_mfma_f32_16x16x32_bf16 v[48:51],v[228:231],v[72:75],v[48:51]
s_waitcnt lgkmcnt(2)
v_cmp_eq_u32 vcc,s60,v13
v_exp_f32 v95,v95
v_mul_f32 v36,v36,v92
v_mfma_f32_16x16x32_bf16 v[44:47],v[224:227],v[72:75],v[44:47]
s_cmp_eq_u32 vcc_lo,-1
v_rcp_f32 v94,v94
v_mfma_f32_16x16x32_bf16 v[44:47],v[212:215],v[68:71],v[44:47]
s_cselect_b32 s66,s86,s59
ds_read_b128 v[60:63],v2 offset:2048
v_fma_f32 v95,v95,v119,v119
v_mfma_f32_16x16x32_bf16 v[40:43],v[208:211],v[68:71],v[40:43]
s_lshr_b32 s61,s66,0
v_rcp_f32 v95,v95
v_mfma_f32_16x16x32_bf16 v[40:43],v[220:223],v[72:75],v[40:43]
v_mul_f32 v116,s75,v112
v_mul_f32 v37,v37,v93
v_mfma_f32_16x16x32_bf16 v[40:43],v[232:235],v[76:79],v[40:43]
s_xor_b32 s64,s64,65536
ds_read_b128 v[64:67],v2 offset:3072
v_mul_f32 v38,v38,v94
v_mfma_f32_16x16x32_bf16 v[40:43],v[244:247],v[80:83],v[40:43]
s_cmp_lt_u32 s67,s59
v_mul_f32 v39,v39,v95
v_cvt_pk_bf16_f32 v100,v36,v37
v_mfma_f32_16x16x32_bf16 v[44:47],v[248:251],v[80:83],v[44:47]
v_cvt_pk_bf16_f32 v101,v38,v39
v_fma_f32 v113,v113,s75,v12
v_mfma_f32_16x16x32_bf16 v[44:47],v[236:239],v[76:79],v[44:47]
buffer_store_dwordx2 v[100:101],v9,s[36:39],s73 offen offset:0 sc1
ds_read_b128 v[68:71],v2 offset:4096
v_fma_f32 v113,-v116,v116,v113
v_max_f32 v113,v113,v12
v_mfma_f32_16x16x32_bf16 v[48:51],v[240:243],v[76:79],v[48:51]
s_waitcnt lgkmcnt(3)
v_rsq_f32 v112,v113
v_mfma_f32_16x16x32_bf16 v[48:51],v[252:255],v[80:83],v[48:51]
s_cbranch_scc1 Lg3_d0w3_%=
s_branch Lg3_slow0_%=
Lg3_slow0_%=:
s_cmp_lt_u32 s66,s59
s_cbranch_scc1 Lg3_gs0_%=
Lg3_slowb0_%=:
s_cmp_eq_u32 s54,1
s_cselect_b32 s68,s49,s48
s_lshr_b32 s68,s68,5
s_add_u32 m0,s78,8192
s_nop 0
buffer_load_dword v6,s[36:39],s68 offen offset:0 sc1 lds
s_mul_i32 s73,s47,7
s_lshr_b32 s73,s73,1
v_mul_f32 v117,s76,v112
v_mul_f32 v118,v112,v112
v_mul_f32 v119,v113,v112
v_mul_f32 v118,s77,v118
v_mul_f32 v118,v118,v117
v_fma_f32 v40,-v116,v16,v40
v_fma_f32 v41,-v116,v17,v41
v_fma_f32 v42,-v116,v18,v42
v_fma_f32 v43,-v116,v19,v43
v_mul_f32 v84,v40,v40
v_mul_f32 v85,v41,v41
v_mul_f32 v86,v42,v42
v_mul_f32 v87,v43,v43
v_fma_f32 v84,v118,v84,v117
v_fma_f32 v85,v118,v85,v117
v_fma_f32 v86,v118,v86,v117
v_fma_f32 v87,v118,v87,v117
v_mul_f32 v84,v40,v84
v_mul_f32 v85,v41,v85
v_mul_f32 v86,v42,v86
v_mul_f32 v87,v43,v87
v_exp_f32 v84,v84
v_exp_f32 v85,v85
v_exp_f32 v86,v86
v_exp_f32 v87,v87
v_fma_f32 v84,v84,v119,v119
v_fma_f32 v85,v85,v119,v119
v_fma_f32 v86,v86,v119,v119
v_fma_f32 v87,v87,v119,v119
v_rcp_f32 v84,v84
v_rcp_f32 v85,v85
v_rcp_f32 v86,v86
v_rcp_f32 v87,v87
v_mul_f32 v40,v40,v84
v_mul_f32 v41,v41,v85
v_mul_f32 v42,v42,v86
v_mul_f32 v43,v43,v87
v_cvt_pk_bf16_f32 v96,v40,v41
v_cvt_pk_bf16_f32 v97,v42,v43
v_fma_f32 v44,-v116,v20,v44
v_fma_f32 v45,-v116,v21,v45
v_fma_f32 v46,-v116,v22,v46
v_fma_f32 v47,-v116,v23,v47
v_mul_f32 v88,v44,v44
v_mul_f32 v89,v45,v45
v_mul_f32 v90,v46,v46
v_mul_f32 v91,v47,v47
v_fma_f32 v88,v118,v88,v117
v_fma_f32 v89,v118,v89,v117
v_fma_f32 v90,v118,v90,v117
v_fma_f32 v91,v118,v91,v117
v_mul_f32 v88,v44,v88
v_mul_f32 v89,v45,v89
v_mul_f32 v90,v46,v90
s_add_u32 m0,s78,8448
s_nop 0
buffer_load_dword v6,s[36:39],s68 offen offset:0 sc1 lds
v_mul_f32 v91,v47,v91
v_exp_f32 v88,v88
v_exp_f32 v89,v89
v_exp_f32 v90,v90
v_exp_f32 v91,v91
v_fma_f32 v88,v88,v119,v119
v_fma_f32 v89,v89,v119,v119
v_fma_f32 v90,v90,v119,v119
v_fma_f32 v91,v91,v119,v119
v_rcp_f32 v88,v88
v_rcp_f32 v89,v89
v_rcp_f32 v90,v90
v_rcp_f32 v91,v91
v_mul_f32 v44,v44,v88
v_mul_f32 v45,v45,v89)"
R"(
v_mul_f32 v46,v46,v90
v_mul_f32 v47,v47,v91
v_cvt_pk_bf16_f32 v98,v44,v45
v_cvt_pk_bf16_f32 v99,v46,v47
buffer_store_dwordx4 v[96:99],v3,s[36:39],s73 offen offset:0 sc1
v_fma_f32 v48,-v116,v24,v48
v_fma_f32 v49,-v116,v25,v49
v_fma_f32 v50,-v116,v26,v50
v_fma_f32 v51,-v116,v27,v51
v_mul_f32 v92,v48,v48
v_mul_f32 v93,v49,v49
v_mul_f32 v94,v50,v50
v_mul_f32 v95,v51,v51
v_fma_f32 v92,v118,v92,v117
v_fma_f32 v93,v118,v93,v117
v_fma_f32 v94,v118,v94,v117
v_fma_f32 v95,v118,v95,v117
v_mul_f32 v92,v48,v92
v_mul_f32 v93,v49,v93
v_mul_f32 v94,v50,v94
v_mul_f32 v95,v51,v95
v_exp_f32 v92,v92
v_exp_f32 v93,v93
v_exp_f32 v94,v94
v_exp_f32 v95,v95
v_fma_f32 v92,v92,v119,v119
v_fma_f32 v93,v93,v119,v119
v_fma_f32 v94,v94,v119,v119
v_fma_f32 v95,v95,v119,v119
v_rcp_f32 v92,v92
v_rcp_f32 v93,v93
v_rcp_f32 v94,v94
v_rcp_f32 v95,v95
v_mul_f32 v48,v48,v92
v_mul_f32 v49,v49,v93
v_mul_f32 v50,v50,v94
v_mul_f32 v51,v51,v95
v_cvt_pk_bf16_f32 v100,v48,v49
v_cvt_pk_bf16_f32 v101,v50,v51
buffer_store_dwordx2 v[100:101],v9,s[36:39],s73 offen offset:0 sc1
s_cmp_ge_u32 s48,s59
s_cbranch_scc1 Lg3_done0_%=
s_add_u32 m0,s78,16384
s_nop 0
buffer_load_dword v6,s[36:39],s68 offen offset:0 sc1 lds
s_waitcnt vmcnt(4)
s_barrier
ds_read_b32 v13,v7 offset:8192
s_mul_i32 s70,s46,5
s_lshr_b32 s70,s70,6
buffer_store_dword v5,v4,s[36:39],s70 offen offset:0 sc1
s_mov_b32 s46,s59
s_mov_b32 s69,18432
s_waitcnt lgkmcnt(0)
v_cmp_eq_u32 vcc,s60,v13
s_nop 0
s_cmp_eq_u32 vcc_lo,-1
s_cbranch_scc1 Lg3_go0_%=
s_add_u32 m0,s78,16640
s_nop 0
buffer_load_dword v6,s[36:39],s68 offen offset:0 sc1 lds
s_waitcnt vmcnt(5)
s_barrier
ds_read_b32 v13,v7 offset:8448
s_mov_b32 s69,18688
s_waitcnt lgkmcnt(0)
v_cmp_eq_u32 vcc,s60,v13
s_nop 0
s_cmp_eq_u32 vcc_lo,-1
s_cbranch_scc1 Lg3_go0_%=
s_add_u32 m0,s78,8192
s_nop 0
buffer_load_dword v6,s[36:39],s68 offen offset:0 sc1 lds
s_waitcnt vmcnt(3)
s_barrier
ds_read_b32 v13,v7 offset:16384
s_mul_i32 s70,s47,5
s_lshr_b32 s70,s70,6
buffer_store_dword v5,v4,s[36:39],s70 offen offset:0 sc1
s_mov_b32 s47,s59
s_mov_b32 s69,10240
s_waitcnt lgkmcnt(0)
v_cmp_eq_u32 vcc,s60,v13
s_nop 0
s_cmp_eq_u32 vcc_lo,-1
s_cbranch_scc1 Lg3_go0_%=
s_add_u32 m0,s78,8448
s_nop 0
buffer_load_dword v6,s[36:39],s68 offen offset:0 sc1 lds
s_waitcnt vmcnt(3)
s_barrier
ds_read_b32 v13,v7 offset:16640
s_mov_b32 s69,10496
s_waitcnt lgkmcnt(0)
v_cmp_eq_u32 vcc,s60,v13
s_nop 0
s_cmp_eq_u32 vcc_lo,-1
s_cbranch_scc1 Lg3_go0_%=
Lg3_poll0_%=:
s_add_u32 m0,s78,16384
s_nop 0
buffer_load_dword v6,s[36:39],s68 offen offset:0 sc1 lds
s_waitcnt vmcnt(2)
s_barrier
ds_read_b32 v13,v7 offset:8192
s_mov_b32 s69,18432
s_waitcnt lgkmcnt(0)
v_cmp_eq_u32 vcc,s60,v13
s_nop 0
s_cmp_eq_u32 vcc_lo,-1
s_cbranch_scc1 Lg3_go0_%=
s_sleep 4
s_add_u32 m0,s78,16640
s_nop 0
buffer_load_dword v6,s[36:39],s68 offen offset:0 sc1 lds
s_waitcnt vmcnt(2)
s_barrier
ds_read_b32 v13,v7 offset:8448
s_mov_b32 s69,18688
s_waitcnt lgkmcnt(0)
v_cmp_eq_u32 vcc,s60,v13
s_nop 0
s_cmp_eq_u32 vcc_lo,-1
s_cbranch_scc1 Lg3_go0_%=
s_sleep 4
s_add_u32 m0,s78,8192
s_nop 0
buffer_load_dword v6,s[36:39],s68 offen offset:0 sc1 lds
s_waitcnt vmcnt(2)
s_barrier
ds_read_b32 v13,v7 offset:16384
s_mov_b32 s69,10240
s_waitcnt lgkmcnt(0)
v_cmp_eq_u32 vcc,s60,v13
s_nop 0
s_cmp_eq_u32 vcc_lo,-1
s_cbranch_scc1 Lg3_go0_%=
s_sleep 4
s_add_u32 m0,s78,8448
s_nop 0
buffer_load_dword v6,s[36:39],s68 offen offset:0 sc1 lds
s_waitcnt vmcnt(2)
s_barrier
ds_read_b32 v13,v7 offset:16640
s_mov_b32 s69,10496
s_waitcnt lgkmcnt(0)
v_cmp_eq_u32 vcc,s60,v13
s_nop 0
s_cmp_eq_u32 vcc_lo,-1
s_cbranch_scc1 Lg3_go0_%=
s_sleep 4
s_add_u32 s79,s79,1
s_cmp_lt_u32 s79,20000
s_cbranch_scc1 Lg3_poll0_%=
Lg3_go0_%=:
s_xor_b32 s62,s63,65536
s_lshr_b32 s61,s48,0
s_mov_b32 m0,s62
s_nop 0
buffer_load_dwordx4 v0,s[36:39],s61 offen offset:0 sc1 lds
buffer_load_dwordx4 v0,s[36:39],s61 offen offset:1024 sc1 lds
buffer_load_dwordx4 v0,s[36:39],s61 offen offset:2048 sc1 lds
buffer_load_dwordx4 v0,s[36:39],s61 offen offset:3072 sc1 lds
s_add_u32 m0,s62,16384
s_nop 0
buffer_load_dwordx4 v1,s[36:39],s61 offen offset:0 sc1 lds
buffer_load_dwordx4 v1,s[36:39],s61 offen offset:1024 sc1 lds
buffer_load_dwordx4 v1,s[36:39],s61 offen offset:2048 sc1 lds
buffer_load_dwordx4 v1,s[36:39],s61 offen offset:3072 sc1 lds
s_lshr_b32 s72,s61,5
buffer_load_dword v104,v8,s[36:39],s72 offen offset:0 sc1
v_add_u32 v84,s69,v7
s_waitcnt vmcnt(5)
s_barrier
ds_read_b32 v13,v84 offset:0
ds_read_b128 v[52:55],v2 offset:0
ds_read_b128 v[56:59],v2 offset:1024
ds_read_b128 v[60:63],v2 offset:2048
ds_read_b128 v[64:67],v2 offset:3072
ds_read_b128 v[68:71],v2 offset:4096
s_mul_i32 s70,s46,5
s_lshr_b32 s70,s70,6
buffer_store_dword v5,v4,s[36:39],s70 offen offset:0 sc1
s_mov_b32 s46,s59
s_mul_i32 s70,s47,5
s_lshr_b32 s70,s70,6
buffer_store_dword v5,v4,s[36:39],s70 offen offset:0 sc1
s_mov_b32 s47,s59
s_mul_i32 s70,s59,5
s_lshr_b32 s70,s70,6
buffer_store_dword v5,v4,s[36:39],s70 offen offset:0 sc1
s_waitcnt lgkmcnt(5)
v_cmp_eq_u32 vcc,s60,v13
s_nop 0
s_cmp_eq_u32 vcc_lo,-1
s_cselect_b32 s66,s49,s59
s_lshr_b32 s61,s66,0
s_xor_b32 s62,s64,65536
s_waitcnt lgkmcnt(3)
s_setpc_b64 s[90:91])"
R"(
Lg3_gs0_%=:
s_xor_b32 s62,s63,65536
s_lshr_b32 s61,s48,0
s_cmp_eq_u32 s54,1
s_cselect_b32 s68,s49,s48
s_lshr_b32 s68,s68,5
s_mov_b32 m0,s62
s_nop 0
buffer_load_dwordx4 v0,s[36:39],s61 offen offset:0 sc1 lds
buffer_load_dwordx4 v0,s[36:39],s61 offen offset:1024 sc1 lds
buffer_load_dwordx4 v0,s[36:39],s61 offen offset:2048 sc1 lds
buffer_load_dwordx4 v0,s[36:39],s61 offen offset:3072 sc1 lds
s_add_u32 m0,s78,8192
s_nop 0
buffer_load_dword v6,s[36:39],s68 offen offset:0 sc1 lds
s_add_u32 m0,s62,16384
s_nop 0
buffer_load_dwordx4 v1,s[36:39],s61 offen offset:0 sc1 lds
buffer_load_dwordx4 v1,s[36:39],s61 offen offset:1024 sc1 lds
buffer_load_dwordx4 v1,s[36:39],s61 offen offset:2048 sc1 lds
buffer_load_dwordx4 v1,s[36:39],s61 offen offset:3072 sc1 lds
s_lshr_b32 s72,s61,5
buffer_load_dword v104,v8,s[36:39],s72 offen offset:0 sc1
s_waitcnt vmcnt(5)
s_barrier
ds_read_b32 v13,v7 offset:8192
ds_read_b128 v[52:55],v2 offset:0
ds_read_b128 v[56:59],v2 offset:1024
ds_read_b128 v[60:63],v2 offset:2048
ds_read_b128 v[64:67],v2 offset:3072
ds_read_b128 v[68:71],v2 offset:4096
s_mul_i32 s70,s46,5
s_lshr_b32 s70,s70,6
buffer_store_dword v5,v4,s[36:39],s70 offen offset:0 sc1
s_mov_b32 s46,s59
s_waitcnt lgkmcnt(5)
v_cmp_eq_u32 vcc,s60,v13
s_nop 0
s_cmp_eq_u32 vcc_hi,-1
s_cselect_b32 s66,s49,s59
s_lshr_b32 s61,s66,0
s_xor_b32 s62,s64,65536
s_mul_i32 s70,s59,5
s_lshr_b32 s70,s70,6
buffer_store_dword v5,v4,s[36:39],s70 offen offset:0 sc1
s_mul_i32 s70,s59,5
s_lshr_b32 s70,s70,6
buffer_store_dword v5,v4,s[36:39],s70 offen offset:0 sc1
s_waitcnt lgkmcnt(3)
s_setpc_b64 s[90:91]
Lg3_done0_%=:
s_waitcnt vmcnt(0)
s_barrier
s_mul_i32 s70,s46,5
s_lshr_b32 s70,s70,6
buffer_store_dword v5,v4,s[36:39],s70 offen offset:0 sc1
s_mul_i32 s70,s47,5
s_lshr_b32 s70,s70,6
buffer_store_dword v5,v4,s[36:39],s70 offen offset:0 sc1
s_branch Lg3_done_%=
Lg3_slow1_%=:
s_cmp_lt_u32 s67,s59
s_cbranch_scc1 Lg3_gs1_%=
Lg3_slowb1_%=:
s_cmp_eq_u32 s54,1
s_cselect_b32 s68,s50,s49
s_lshr_b32 s68,s68,5
s_add_u32 m0,s78,8192
s_nop 0
buffer_load_dword v6,s[36:39],s68 offen offset:0 sc1 lds
s_mul_i32 s73,s48,7
s_lshr_b32 s73,s73,1
v_mul_f32 v117,s76,v112
v_mul_f32 v118,v112,v112
v_mul_f32 v119,v113,v112
v_mul_f32 v118,s77,v118
v_mul_f32 v118,v118,v117
v_fma_f32 v28,-v116,v16,v28
v_fma_f32 v29,-v116,v17,v29
v_fma_f32 v30,-v116,v18,v30
v_fma_f32 v31,-v116,v19,v31
v_mul_f32 v84,v28,v28
v_mul_f32 v85,v29,v29
v_mul_f32 v86,v30,v30
v_mul_f32 v87,v31,v31
v_fma_f32 v84,v118,v84,v117
v_fma_f32 v85,v118,v85,v117
v_fma_f32 v86,v118,v86,v117
v_fma_f32 v87,v118,v87,v117
v_mul_f32 v84,v28,v84
v_mul_f32 v85,v29,v85
v_mul_f32 v86,v30,v86
v_mul_f32 v87,v31,v87
v_exp_f32 v84,v84
v_exp_f32 v85,v85
v_exp_f32 v86,v86
v_exp_f32 v87,v87
v_fma_f32 v84,v84,v119,v119
v_fma_f32 v85,v85,v119,v119
v_fma_f32 v86,v86,v119,v119
v_fma_f32 v87,v87,v119,v119
v_rcp_f32 v84,v84
v_rcp_f32 v85,v85
v_rcp_f32 v86,v86
v_rcp_f32 v87,v87
v_mul_f32 v28,v28,v84
v_mul_f32 v29,v29,v85
v_mul_f32 v30,v30,v86
v_mul_f32 v31,v31,v87
v_cvt_pk_bf16_f32 v96,v28,v29
v_cvt_pk_bf16_f32 v97,v30,v31
v_fma_f32 v32,-v116,v20,v32
v_fma_f32 v33,-v116,v21,v33
v_fma_f32 v34,-v116,v22,v34
v_fma_f32 v35,-v116,v23,v35
v_mul_f32 v88,v32,v32
v_mul_f32 v89,v33,v33
v_mul_f32 v90,v34,v34
v_mul_f32 v91,v35,v35
v_fma_f32 v88,v118,v88,v117
v_fma_f32 v89,v118,v89,v117
v_fma_f32 v90,v118,v90,v117
v_fma_f32 v91,v118,v91,v117
v_mul_f32 v88,v32,v88
v_mul_f32 v89,v33,v89
v_mul_f32 v90,v34,v90
s_add_u32 m0,s78,8448
s_nop 0
buffer_load_dword v6,s[36:39],s68 offen offset:0 sc1 lds
v_mul_f32 v91,v35,v91
v_exp_f32 v88,v88
v_exp_f32 v89,v89
v_exp_f32 v90,v90
v_exp_f32 v91,v91
v_fma_f32 v88,v88,v119,v119
v_fma_f32 v89,v89,v119,v119
v_fma_f32 v90,v90,v119,v119
v_fma_f32 v91,v91,v119,v119
v_rcp_f32 v88,v88
v_rcp_f32 v89,v89
v_rcp_f32 v90,v90
v_rcp_f32 v91,v91
v_mul_f32 v32,v32,v88
v_mul_f32 v33,v33,v89
v_mul_f32 v34,v34,v90
v_mul_f32 v35,v35,v91
v_cvt_pk_bf16_f32 v98,v32,v33
v_cvt_pk_bf16_f32 v99,v34,v35
buffer_store_dwordx4 v[96:99],v3,s[36:39],s73 offen offset:0 sc1
v_fma_f32 v36,-v116,v24,v36
v_fma_f32 v37,-v116,v25,v37
v_fma_f32 v38,-v116,v26,v38
v_fma_f32 v39,-v116,v27,v39
v_mul_f32 v92,v36,v36
v_mul_f32 v93,v37,v37
v_mul_f32 v94,v38,v38
v_mul_f32 v95,v39,v39
v_fma_f32 v92,v118,v92,v117
v_fma_f32 v93,v118,v93,v117
v_fma_f32 v94,v118,v94,v117
v_fma_f32 v95,v118,v95,v117
v_mul_f32 v92,v36,v92
v_mul_f32 v93,v37,v93
v_mul_f32 v94,v38,v94
v_mul_f32 v95,v39,v95
v_exp_f32 v92,v92
v_exp_f32 v93,v93
v_exp_f32 v94,v94
v_exp_f32 v95,v95
v_fma_f32 v92,v92,v119,v119
v_fma_f32 v93,v93,v119,v119
v_fma_f32 v94,v94,v119,v119
v_fma_f32 v95,v95,v119,v119
v_rcp_f32 v92,v92
v_rcp_f32 v93,v93
v_rcp_f32 v94,v94
v_rcp_f32 v95,v95
v_mul_f32 v36,v36,v92
v_mul_f32 v37,v37,v93
v_mul_f32 v38,v38,v94
v_mul_f32 v39,v39,v95
v_cvt_pk_bf16_f32 v100,v36,v37
v_cvt_pk_bf16_f32 v101,v38,v39
buffer_store_dwordx2 v[100:101],v9,s[36:39],s73 offen offset:0 sc1
s_cmp_ge_u32 s49,s59
s_cbranch_scc1 Lg3_done1_%=
s_add_u32 m0,s78,16384
s_nop 0
buffer_load_dword v6,s[36:39],s68 offen offset:0 sc1 lds
s_waitcnt vmcnt(4)
s_barrier
ds_read_b32 v13,v7 offset:8192
s_mul_i32 s70,s47,5
s_lshr_b32 s70,s70,6
buffer_store_dword v5,v4,s[36:39],s70 offen offset:0 sc1
s_mov_b32 s47,s59
s_mov_b32 s69,18432
s_waitcnt lgkmcnt(0)
v_cmp_eq_u32 vcc,s60,v13
s_nop 0)"
R"(
s_cmp_eq_u32 vcc_lo,-1
s_cbranch_scc1 Lg3_go1_%=
s_add_u32 m0,s78,16640
s_nop 0
buffer_load_dword v6,s[36:39],s68 offen offset:0 sc1 lds
s_waitcnt vmcnt(5)
s_barrier
ds_read_b32 v13,v7 offset:8448
s_mov_b32 s69,18688
s_waitcnt lgkmcnt(0)
v_cmp_eq_u32 vcc,s60,v13
s_nop 0
s_cmp_eq_u32 vcc_lo,-1
s_cbranch_scc1 Lg3_go1_%=
s_add_u32 m0,s78,8192
s_nop 0
buffer_load_dword v6,s[36:39],s68 offen offset:0 sc1 lds
s_waitcnt vmcnt(3)
s_barrier
ds_read_b32 v13,v7 offset:16384
s_mul_i32 s70,s48,5
s_lshr_b32 s70,s70,6
buffer_store_dword v5,v4,s[36:39],s70 offen offset:0 sc1
s_mov_b32 s48,s59
s_mov_b32 s69,10240
s_waitcnt lgkmcnt(0)
v_cmp_eq_u32 vcc,s60,v13
s_nop 0
s_cmp_eq_u32 vcc_lo,-1
s_cbranch_scc1 Lg3_go1_%=
s_add_u32 m0,s78,8448
s_nop 0
buffer_load_dword v6,s[36:39],s68 offen offset:0 sc1 lds
s_waitcnt vmcnt(3)
s_barrier
ds_read_b32 v13,v7 offset:16640
s_mov_b32 s69,10496
s_waitcnt lgkmcnt(0)
v_cmp_eq_u32 vcc,s60,v13
s_nop 0
s_cmp_eq_u32 vcc_lo,-1
s_cbranch_scc1 Lg3_go1_%=
Lg3_poll1_%=:
s_add_u32 m0,s78,16384
s_nop 0
buffer_load_dword v6,s[36:39],s68 offen offset:0 sc1 lds
s_waitcnt vmcnt(2)
s_barrier
ds_read_b32 v13,v7 offset:8192
s_mov_b32 s69,18432
s_waitcnt lgkmcnt(0)
v_cmp_eq_u32 vcc,s60,v13
s_nop 0
s_cmp_eq_u32 vcc_lo,-1
s_cbranch_scc1 Lg3_go1_%=
s_sleep 4
s_add_u32 m0,s78,16640
s_nop 0
buffer_load_dword v6,s[36:39],s68 offen offset:0 sc1 lds
s_waitcnt vmcnt(2)
s_barrier
ds_read_b32 v13,v7 offset:8448
s_mov_b32 s69,18688
s_waitcnt lgkmcnt(0)
v_cmp_eq_u32 vcc,s60,v13
s_nop 0
s_cmp_eq_u32 vcc_lo,-1
s_cbranch_scc1 Lg3_go1_%=
s_sleep 4
s_add_u32 m0,s78,8192
s_nop 0
buffer_load_dword v6,s[36:39],s68 offen offset:0 sc1 lds
s_waitcnt vmcnt(2)
s_barrier
ds_read_b32 v13,v7 offset:16384
s_mov_b32 s69,10240
s_waitcnt lgkmcnt(0)
v_cmp_eq_u32 vcc,s60,v13
s_nop 0
s_cmp_eq_u32 vcc_lo,-1
s_cbranch_scc1 Lg3_go1_%=
s_sleep 4
s_add_u32 m0,s78,8448
s_nop 0
buffer_load_dword v6,s[36:39],s68 offen offset:0 sc1 lds
s_waitcnt vmcnt(2)
s_barrier
ds_read_b32 v13,v7 offset:16640
s_mov_b32 s69,10496
s_waitcnt lgkmcnt(0)
v_cmp_eq_u32 vcc,s60,v13
s_nop 0
s_cmp_eq_u32 vcc_lo,-1
s_cbranch_scc1 Lg3_go1_%=
s_sleep 4
s_add_u32 s79,s79,1
s_cmp_lt_u32 s79,20000
s_cbranch_scc1 Lg3_poll1_%=
Lg3_go1_%=:
s_xor_b32 s62,s64,65536
s_lshr_b32 s61,s49,0
s_mov_b32 m0,s62
s_nop 0
buffer_load_dwordx4 v0,s[36:39],s61 offen offset:0 sc1 lds
buffer_load_dwordx4 v0,s[36:39],s61 offen offset:1024 sc1 lds
buffer_load_dwordx4 v0,s[36:39],s61 offen offset:2048 sc1 lds
buffer_load_dwordx4 v0,s[36:39],s61 offen offset:3072 sc1 lds
s_add_u32 m0,s62,16384
s_nop 0
buffer_load_dwordx4 v1,s[36:39],s61 offen offset:0 sc1 lds
buffer_load_dwordx4 v1,s[36:39],s61 offen offset:1024 sc1 lds
buffer_load_dwordx4 v1,s[36:39],s61 offen offset:2048 sc1 lds
buffer_load_dwordx4 v1,s[36:39],s61 offen offset:3072 sc1 lds
s_lshr_b32 s72,s61,5
buffer_load_dword v108,v8,s[36:39],s72 offen offset:0 sc1
v_add_u32 v84,s69,v7
s_waitcnt vmcnt(5)
s_barrier
ds_read_b32 v13,v84 offset:0
ds_read_b128 v[52:55],v2 offset:32768
ds_read_b128 v[56:59],v2 offset:33792
ds_read_b128 v[60:63],v2 offset:34816
ds_read_b128 v[64:67],v2 offset:35840
ds_read_b128 v[68:71],v2 offset:36864
s_mul_i32 s70,s47,5
s_lshr_b32 s70,s70,6
buffer_store_dword v5,v4,s[36:39],s70 offen offset:0 sc1
s_mov_b32 s47,s59
s_mul_i32 s70,s48,5
s_lshr_b32 s70,s70,6
buffer_store_dword v5,v4,s[36:39],s70 offen offset:0 sc1
s_mov_b32 s48,s59
s_mul_i32 s70,s59,5
s_lshr_b32 s70,s70,6
buffer_store_dword v5,v4,s[36:39],s70 offen offset:0 sc1
s_waitcnt lgkmcnt(5)
v_cmp_eq_u32 vcc,s60,v13
s_nop 0
s_cmp_eq_u32 vcc_lo,-1
s_cselect_b32 s67,s50,s59
s_lshr_b32 s61,s67,0
s_xor_b32 s62,s63,65536
s_waitcnt lgkmcnt(3)
s_setpc_b64 s[92:93]
Lg3_gs1_%=:
s_xor_b32 s62,s64,65536
s_lshr_b32 s61,s49,0
s_cmp_eq_u32 s54,1
s_cselect_b32 s68,s50,s49
s_lshr_b32 s68,s68,5
s_mov_b32 m0,s62
s_nop 0
buffer_load_dwordx4 v0,s[36:39],s61 offen offset:0 sc1 lds
buffer_load_dwordx4 v0,s[36:39],s61 offen offset:1024 sc1 lds
buffer_load_dwordx4 v0,s[36:39],s61 offen offset:2048 sc1 lds
buffer_load_dwordx4 v0,s[36:39],s61 offen offset:3072 sc1 lds
s_add_u32 m0,s78,8192
s_nop 0
buffer_load_dword v6,s[36:39],s68 offen offset:0 sc1 lds
s_add_u32 m0,s62,16384
s_nop 0
buffer_load_dwordx4 v1,s[36:39],s61 offen offset:0 sc1 lds
buffer_load_dwordx4 v1,s[36:39],s61 offen offset:1024 sc1 lds
buffer_load_dwordx4 v1,s[36:39],s61 offen offset:2048 sc1 lds
buffer_load_dwordx4 v1,s[36:39],s61 offen offset:3072 sc1 lds
s_lshr_b32 s72,s61,5
buffer_load_dword v108,v8,s[36:39],s72 offen offset:0 sc1
s_waitcnt vmcnt(5)
s_barrier
ds_read_b32 v13,v7 offset:8192
ds_read_b128 v[52:55],v2 offset:32768
ds_read_b128 v[56:59],v2 offset:33792
ds_read_b128 v[60:63],v2 offset:34816
ds_read_b128 v[64:67],v2 offset:35840
ds_read_b128 v[68:71],v2 offset:36864
s_mul_i32 s70,s47,5
s_lshr_b32 s70,s70,6
buffer_store_dword v5,v4,s[36:39],s70 offen offset:0 sc1
s_mov_b32 s47,s59
s_waitcnt lgkmcnt(5)
v_cmp_eq_u32 vcc,s60,v13
s_nop 0
s_cmp_eq_u32 vcc_hi,-1
s_cselect_b32 s67,s50,s59
s_lshr_b32 s61,s67,0
s_xor_b32 s62,s63,65536
s_mul_i32 s70,s59,5
s_lshr_b32 s70,s70,6
buffer_store_dword v5,v4,s[36:39],s70 offen offset:0 sc1
s_mul_i32 s70,s59,5
s_lshr_b32 s70,s70,6
buffer_store_dword v5,v4,s[36:39],s70 offen offset:0 sc1
s_waitcnt lgkmcnt(3)
s_setpc_b64 s[92:93]
Lg3_done1_%=:
s_waitcnt vmcnt(0)
s_barrier
s_mul_i32 s70,s47,5
s_lshr_b32 s70,s70,6
buffer_store_dword v5,v4,s[36:39],s70 offen offset:0 sc1)"
R"(
s_mul_i32 s70,s48,5
s_lshr_b32 s70,s70,6
buffer_store_dword v5,v4,s[36:39],s70 offen offset:0 sc1
s_branch Lg3_done_%=
Lg3_done_%=:
s_waitcnt vmcnt(0) lgkmcnt(0)
s_barrier
s_endpgm
Lcode_g3_e:)"
  :: "s"(ws), "s"(wave), "s"(sStep), "s"(sX1), "s"(sX2), "s"(sU0), "s"(sWw), "s"(sBuf), "s"(sEp), "v"(vSrc), "v"(vA), "v"(vOut), "v"(vFlag), "v"(vPollOff), "v"(vSnapRd), "v"(vAuxS), "v"(vOut2), "v"(vStW), "v"(vStR), "v"(vWd0), "v"(vWd1), "v"(vWr0), "v"(vWr1), "v"(vWr2), "s"(w3) : "v0", "v1", "v2", "v3", "v4", "v5", "v6", "v7", "v8", "v9", "v10", "v11", "v12", "v13", "v14", "v15", "v16", "v17", "v18", "v19", "v20", "v21", "v22", "v23", "v24", "v25", "v26", "v27", "v28", "v29", "v30", "v31", "v32", "v33", "v34", "v35", "v36", "v37", "v38", "v39", "v40", "v41", "v42", "v43", "v44", "v45", "v46", "v47", "v48", "v49", "v50", "v51", "v52", "v53", "v54", "v55", "v56", "v57", "v58", "v59", "v60", "v61", "v62", "v63", "v64", "v65", "v66", "v67", "v68", "v69", "v70", "v71", "v72", "v73", "v74", "v75", "v76", "v77", "v78", "v79", "v80", "v81", "v82", "v83", "v84", "v85", "v86", "v87", "v88", "v89", "v90", "v91", "v92", "v93", "v94", "v95", "v96", "v97", "v98", "v99", "v100", "v101", "v102", "v103", "v104", "v105", "v106", "v107", "v108", "v109", "v110", "v111", "v112", "v113", "v114", "v115", "v116", "v117", "v118", "v119", "v120", "v121", "v122", "v123", "v124", "v125", "v126", "v127", "v128", "v129", "v130", "v131", "v132", "v133", "v134", "v135", "v136", "v137", "v138", "v139", "v140", "v141", "v142", "v143", "v144", "v145", "v146", "v147", "v148", "v149", "v150", "v151", "v152", "v153", "v154", "v155", "v156", "v157", "v158", "v159", "v160", "v161", "v162", "v163", "v164", "v165", "v166", "v167", "v168", "v169", "v170", "v171", "v172", "v173", "v174", "v175", "v176", "v177", "v178", "v179", "v180", "v181", "v182", "v183", "v184", "v185", "v186", "v187", "v188", "v189", "v190", "v191", "v192", "v193", "v194", "v195", "v196", "v197", "v198", "v199", "v200", "v201", "v202", "v203", "v204", "v205", "v206", "v207", "v208", "v209", "v210", "v211", "v212", "v213", "v214", "v215", "v216", "v217", "v218", "v219", "v220", "v221", "v222", "v223", "v224", "v225", "v226", "v227", "v228", "v229", "v230", "v231", "v255", "a0", "a1", "a2", "a3", "a4", "a5", "a6", "a7", "a8", "a9", "a10", "a11", "a12", "a13", "a14", "a15", "a16", "a17", "a18", "a19", "a20", "a21", "a22", "a23", "a24", "a25", "a26", "a27", "a28", "a29", "a30", "a31", "a32", "a33", "a34", "a35", "a36", "a37", "a38", "a39", "a40", "a41", "a42", "a43", "a44", "a45", "a46", "a47", "a48", "a49", "a50", "a51", "a52", "a53", "a54", "a55", "a56", "a57", "a58", "a59", "a60", "a61", "a62", "a63", "a64", "a65", "a66", "a67", "a68", "a69", "a70", "a71", "a72", "a73", "a74", "a75", "a76", "a77", "a78", "a79", "a80", "a81", "a82", "a83", "a84", "a85", "a86", "a87", "a88", "a89", "a90", "a91", "a92", "a93", "a94", "a95", "a96", "a97", "a98", "a99", "a100", "a101", "a102", "a103", "a104", "a105", "a106", "a107", "a108", "a109", "a110", "a111", "a112", "a113", "a114", "a115", "a116", "a117", "a118", "a119", "a120", "a121", "a122", "a123", "a124", "a125", "a126", "a127", "a128", "a129", "a130", "a131", "a132", "a133", "a134", "a135", "a136", "a137", "a138", "a139", "a140", "a141", "a142", "a143", "a144", "a145", "a146", "a147", "a148", "a149", "a150", "a151", "a152", "a153", "a154", "a155", "a156", "a157", "a158", "a159", "a160", "a161", "a162", "a163", "a164", "a165", "a166", "a167", "a168", "a169", "a170", "a171", "a172", "a173", "a174", "a175", "a176", "a177", "a178", "a179", "a180", "a181", "a182", "a183", "a184", "a185", "a186", "a187", "a188", "a189", "a190", "a191", "a192", "a193", "a194", "a195", "a196", "a197", "a198", "a199", "a200", "a201", "a202", "a203", "a204", "a205", "a206", "a207", "a208", "a209", "a210", "a211", "a212", "a213", "a214", "a215", "a216", "a217", "a218", "a219", "a220", "a221", "a222", "a223", "a224", "a225", "a226", "a227", "a228", "a229", "a230", "a231", "a232", "a233", "a234", "a235", "a236", "a237", "a238", "a239", "a240", "a241", "a242", "a243", "a244", "a245", "a246", "a247", "a248", "a249", "a250", "a251", "a252", "a253", "a254", "a255", "s36", "s37", "s38", "s39", "s40", "s41", "s42", "s43", "s44", "s45", "s46", "s47", "s48", "s49", "s50", "s51", "s52", "s53", "s54", "s55", "s56", "s57", "s58", "s59", "s60", "s61", "s62", "s63", "s64", "s65", "s66", "s67", "s68", "s69", "s70", "s71", "s72", "s73", "s74", "s75", "s76", "s77", "s78", "s79", "s80", "s81", "s82", "s83", "s84", "s85", "s86", "s87", "s88", "s89", "s90", "s91", "s92", "s93", "s94", "s95", "s96", "s97", "s98", "s99", "vcc", "scc", "memory"); } else { asm volatile(R"(
Lcode_g3o_b:
s_mov_b64 s[36:37],%0
s_mov_b32 s38,122727552
s_mov_b32 s39,0x00020000
s_mov_b32 s54,%1
s_mov_b32 s55,%2
s_mov_b32 s56,%3
s_mov_b32 s58,%4
s_mov_b32 s83,%5
s_mov_b32 s82,%6
s_mov_b32 s84,%7
s_mov_b32 s60,%8
v_mov_b32 v0,%9
v_mov_b32 v2,%10
v_mov_b32 v3,%11
v_mov_b32 v4,%12
v_mov_b32 v6,%13
v_mov_b32 v7,%14
v_mov_b32 v8,%15
v_mov_b32 v9,%16
v_mov_b32 v10,%17
v_mov_b32 v11,%18
v_mov_b32 v24,%19
v_mov_b32 v25,%20
v_mov_b32 v26,%21
v_mov_b32 v27,%22
v_mov_b32 v28,%23
s_mov_b64 s[40:41],%24
s_mov_b32 s42,7340032
s_mov_b32 s43,0x00020000
s_lshl_b32 s88,s54,11
s_add_u32 m0,s88,0
s_add_u32 s61,s82,0
buffer_load_dwordx4 v24,s[40:43],s61 offen offset:0  lds
s_add_u32 m0,s88,1024
s_nop 0
buffer_load_dwordx4 v25,s[40:43],s61 offen offset:0  lds
s_add_u32 m0,s88,8064
s_nop 0
buffer_load_dwordx4 v24,s[40:43],s61 offen offset:128  lds
s_add_u32 m0,s88,9088
s_nop 0
buffer_load_dwordx4 v25,s[40:43],s61 offen offset:128  lds
s_add_u32 m0,s88,16384
s_add_u32 s61,s82,458752
buffer_load_dwordx4 v24,s[40:43],s61 offen offset:0  lds
s_add_u32 m0,s88,17408
s_nop 0
buffer_load_dwordx4 v25,s[40:43],s61 offen offset:0  lds
s_add_u32 m0,s88,24448
s_nop 0
buffer_load_dwordx4 v24,s[40:43],s61 offen offset:128  lds
s_add_u32 m0,s88,25472
s_nop 0
buffer_load_dwordx4 v25,s[40:43],s61 offen offset:128  lds
s_add_u32 m0,s88,32768
s_add_u32 s61,s82,917504
buffer_load_dwordx4 v24,s[40:43],s61 offen offset:0  lds
s_add_u32 m0,s88,33792
s_nop 0
buffer_load_dwordx4 v25,s[40:43],s61 offen offset:0  lds
s_add_u32 m0,s88,40832
s_nop 0
buffer_load_dwordx4 v24,s[40:43],s61 offen offset:128  lds
s_add_u32 m0,s88,41856
s_nop 0
buffer_load_dwordx4 v25,s[40:43],s61 offen offset:128  lds
s_add_u32 m0,s88,49152
s_add_u32 s61,s82,1376256
buffer_load_dwordx4 v24,s[40:43],s61 offen offset:0  lds
s_add_u32 m0,s88,50176
s_nop 0
buffer_load_dwordx4 v25,s[40:43],s61 offen offset:0  lds
s_add_u32 m0,s88,57216
s_nop 0
buffer_load_dwordx4 v24,s[40:43],s61 offen offset:128  lds
s_add_u32 m0,s88,58240
s_nop 0
buffer_load_dwordx4 v25,s[40:43],s61 offen offset:128  lds
s_add_u32 m0,s88,65536
s_add_u32 s61,s82,1835008
buffer_load_dwordx4 v24,s[40:43],s61 offen offset:0  lds
s_add_u32 m0,s88,66560
s_nop 0
buffer_load_dwordx4 v25,s[40:43],s61 offen offset:0  lds
s_add_u32 m0,s88,73600
s_nop 0
buffer_load_dwordx4 v24,s[40:43],s61 offen offset:128  lds
s_add_u32 m0,s88,74624
s_nop 0
buffer_load_dwordx4 v25,s[40:43],s61 offen offset:128  lds
s_add_u32 m0,s88,81920
s_add_u32 s61,s82,2293760
buffer_load_dwordx4 v24,s[40:43],s61 offen offset:0  lds
s_add_u32 m0,s88,82944
s_nop 0
buffer_load_dwordx4 v25,s[40:43],s61 offen offset:0  lds
s_add_u32 m0,s88,89984
s_nop 0
buffer_load_dwordx4 v24,s[40:43],s61 offen offset:128  lds
s_add_u32 m0,s88,91008
s_nop 0
buffer_load_dwordx4 v25,s[40:43],s61 offen offset:128  lds
s_add_u32 m0,s88,98304
s_add_u32 s61,s82,2752512
buffer_load_dwordx4 v24,s[40:43],s61 offen offset:0  lds
s_add_u32 m0,s88,99328
s_nop 0
buffer_load_dwordx4 v25,s[40:43],s61 offen offset:0  lds
s_add_u32 m0,s88,106368
s_nop 0
buffer_load_dwordx4 v24,s[40:43],s61 offen offset:128  lds
s_add_u32 m0,s88,107392
s_nop 0
buffer_load_dwordx4 v25,s[40:43],s61 offen offset:128  lds
v_add_u32 v32,49152,v26
v_add_u32 v33,49152,v27
v_add_u32 v34,98304,v26
v_add_u32 v35,98304,v27
v_mov_b32 v40,0x3f803f80
v_mov_b32 v41,0x3f803f80
v_mov_b32 v42,0x3f803f80
v_mov_b32 v43,0x3f803f80
s_waitcnt vmcnt(24)
s_barrier
s_add_u32 m0,s88,114688
s_add_u32 s61,s82,3211264
buffer_load_dwordx4 v24,s[40:43],s61 offen offset:0  lds
s_add_u32 m0,s88,115712
ds_read_b64_tr_b16 a[0:1],v26 offset:0
buffer_load_dwordx4 v25,s[40:43],s61 offen offset:0  lds
s_add_u32 m0,s88,122752
ds_read_b64_tr_b16 a[2:3],v26 offset:512
buffer_load_dwordx4 v24,s[40:43],s61 offen offset:128  lds
s_add_u32 m0,s88,123776
ds_read_b64_tr_b16 a[4:5],v27 offset:0
buffer_load_dwordx4 v25,s[40:43],s61 offen offset:128  lds
ds_read_b64_tr_b16 a[6:7],v27 offset:512
ds_read_b64_tr_b16 a[8:9],v26 offset:4096
ds_read_b64_tr_b16 a[10:11],v26 offset:4608
ds_read_b64_tr_b16 a[12:13],v27 offset:4096
ds_read_b64_tr_b16 a[14:15],v27 offset:4608
s_waitcnt vmcnt(24)
s_barrier
s_add_u32 m0,s88,131072
s_add_u32 s61,s82,3670016
buffer_load_dwordx4 v24,s[40:43],s61 offen offset:0  lds
s_add_u32 m0,s88,132096
ds_read_b64_tr_b16 a[16:17],v26 offset:16384
buffer_load_dwordx4 v25,s[40:43],s61 offen offset:0  lds
s_add_u32 m0,s88,139136
ds_read_b64_tr_b16 a[18:19],v26 offset:16896
buffer_load_dwordx4 v24,s[40:43],s61 offen offset:128  lds
s_add_u32 m0,s88,140160
ds_read_b64_tr_b16 a[20:21],v27 offset:16384
buffer_load_dwordx4 v25,s[40:43],s61 offen offset:128  lds
ds_read_b64_tr_b16 a[22:23],v27 offset:16896
ds_read_b64_tr_b16 a[24:25],v26 offset:20480
ds_read_b64_tr_b16 a[26:27],v26 offset:20992
ds_read_b64_tr_b16 a[28:29],v27 offset:20480
ds_read_b64_tr_b16 a[30:31],v27 offset:20992
s_waitcnt lgkmcnt(8)
v_mfma_f32_16x16x32_bf16 v[16:19],a[0:3],v[40:43],0
v_mfma_f32_16x16x32_bf16 v[20:23],a[4:7],v[40:43],0
v_mfma_f32_16x16x32_bf16 v[16:19],a[8:11],v[40:43],v[16:19]
v_mfma_f32_16x16x32_bf16 v[20:23],a[12:15],v[40:43],v[20:23]
s_waitcnt vmcnt(24)
s_barrier
s_add_u32 m0,s88,0
s_add_u32 s61,s82,4128768
buffer_load_dwordx4 v24,s[40:43],s61 offen offset:0  lds
s_add_u32 m0,s88,1024
ds_read_b64_tr_b16 a[32:33],v26 offset:32768
buffer_load_dwordx4 v25,s[40:43],s61 offen offset:0  lds
s_add_u32 m0,s88,8064
ds_read_b64_tr_b16 a[34:35],v26 offset:33280
buffer_load_dwordx4 v24,s[40:43],s61 offen offset:128  lds
s_add_u32 m0,s88,9088
ds_read_b64_tr_b16 a[36:37],v27 offset:32768
buffer_load_dwordx4 v25,s[40:43],s61 offen offset:128  lds
ds_read_b64_tr_b16 a[38:39],v27 offset:33280
ds_read_b64_tr_b16 a[40:41],v26 offset:36864
ds_read_b64_tr_b16 a[42:43],v26 offset:37376
ds_read_b64_tr_b16 a[44:45],v27 offset:36864
ds_read_b64_tr_b16 a[46:47],v27 offset:37376
s_waitcnt lgkmcnt(8)
v_mfma_f32_16x16x32_bf16 v[16:19],a[16:19],v[40:43],v[16:19]
v_mfma_f32_16x16x32_bf16 v[20:23],a[20:23],v[40:43],v[20:23]
v_mfma_f32_16x16x32_bf16 v[16:19],a[24:27],v[40:43],v[16:19]
v_mfma_f32_16x16x32_bf16 v[20:23],a[28:31],v[40:43],v[20:23]
s_waitcnt vmcnt(24)
s_barrier
s_add_u32 m0,s88,16384
s_add_u32 s61,s82,4587520
buffer_load_dwordx4 v24,s[40:43],s61 offen offset:0  lds
s_add_u32 m0,s88,17408
ds_read_b64_tr_b16 a[48:49],v32 offset:0
buffer_load_dwordx4 v25,s[40:43],s61 offen offset:0  lds
s_add_u32 m0,s88,24448
ds_read_b64_tr_b16 a[50:51],v32 offset:512)"
R"(
buffer_load_dwordx4 v24,s[40:43],s61 offen offset:128  lds
s_add_u32 m0,s88,25472
ds_read_b64_tr_b16 a[52:53],v33 offset:0
buffer_load_dwordx4 v25,s[40:43],s61 offen offset:128  lds
ds_read_b64_tr_b16 a[54:55],v33 offset:512
ds_read_b64_tr_b16 a[56:57],v32 offset:4096
ds_read_b64_tr_b16 a[58:59],v32 offset:4608
ds_read_b64_tr_b16 a[60:61],v33 offset:4096
ds_read_b64_tr_b16 a[62:63],v33 offset:4608
s_waitcnt lgkmcnt(8)
v_mfma_f32_16x16x32_bf16 v[16:19],a[32:35],v[40:43],v[16:19]
v_mfma_f32_16x16x32_bf16 v[20:23],a[36:39],v[40:43],v[20:23]
v_mfma_f32_16x16x32_bf16 v[16:19],a[40:43],v[40:43],v[16:19]
v_mfma_f32_16x16x32_bf16 v[20:23],a[44:47],v[40:43],v[20:23]
s_waitcnt vmcnt(24)
s_barrier
s_add_u32 m0,s88,32768
s_add_u32 s61,s82,5046272
buffer_load_dwordx4 v24,s[40:43],s61 offen offset:0  lds
s_add_u32 m0,s88,33792
ds_read_b64_tr_b16 a[64:65],v32 offset:16384
buffer_load_dwordx4 v25,s[40:43],s61 offen offset:0  lds
s_add_u32 m0,s88,40832
ds_read_b64_tr_b16 a[66:67],v32 offset:16896
buffer_load_dwordx4 v24,s[40:43],s61 offen offset:128  lds
s_add_u32 m0,s88,41856
ds_read_b64_tr_b16 a[68:69],v33 offset:16384
buffer_load_dwordx4 v25,s[40:43],s61 offen offset:128  lds
ds_read_b64_tr_b16 a[70:71],v33 offset:16896
ds_read_b64_tr_b16 a[72:73],v32 offset:20480
ds_read_b64_tr_b16 a[74:75],v32 offset:20992
ds_read_b64_tr_b16 a[76:77],v33 offset:20480
ds_read_b64_tr_b16 a[78:79],v33 offset:20992
s_waitcnt lgkmcnt(8)
v_mfma_f32_16x16x32_bf16 v[16:19],a[48:51],v[40:43],v[16:19]
v_mfma_f32_16x16x32_bf16 v[20:23],a[52:55],v[40:43],v[20:23]
v_mfma_f32_16x16x32_bf16 v[16:19],a[56:59],v[40:43],v[16:19]
v_mfma_f32_16x16x32_bf16 v[20:23],a[60:63],v[40:43],v[20:23]
s_waitcnt vmcnt(24)
s_barrier
s_add_u32 m0,s88,49152
s_add_u32 s61,s82,5505024
buffer_load_dwordx4 v24,s[40:43],s61 offen offset:0  lds
s_add_u32 m0,s88,50176
ds_read_b64_tr_b16 a[80:81],v32 offset:32768
buffer_load_dwordx4 v25,s[40:43],s61 offen offset:0  lds
s_add_u32 m0,s88,57216
ds_read_b64_tr_b16 a[82:83],v32 offset:33280
buffer_load_dwordx4 v24,s[40:43],s61 offen offset:128  lds
s_add_u32 m0,s88,58240
ds_read_b64_tr_b16 a[84:85],v33 offset:32768
buffer_load_dwordx4 v25,s[40:43],s61 offen offset:128  lds
ds_read_b64_tr_b16 a[86:87],v33 offset:33280
ds_read_b64_tr_b16 a[88:89],v32 offset:36864
ds_read_b64_tr_b16 a[90:91],v32 offset:37376
ds_read_b64_tr_b16 a[92:93],v33 offset:36864
ds_read_b64_tr_b16 a[94:95],v33 offset:37376
s_waitcnt lgkmcnt(8)
v_mfma_f32_16x16x32_bf16 v[16:19],a[64:67],v[40:43],v[16:19]
v_mfma_f32_16x16x32_bf16 v[20:23],a[68:71],v[40:43],v[20:23]
v_mfma_f32_16x16x32_bf16 v[16:19],a[72:75],v[40:43],v[16:19]
v_mfma_f32_16x16x32_bf16 v[20:23],a[76:79],v[40:43],v[20:23]
s_waitcnt vmcnt(24)
s_barrier
s_add_u32 m0,s88,65536
s_add_u32 s61,s82,5963776
buffer_load_dwordx4 v24,s[40:43],s61 offen offset:0  lds
s_add_u32 m0,s88,66560
ds_read_b64_tr_b16 a[96:97],v34 offset:0
buffer_load_dwordx4 v25,s[40:43],s61 offen offset:0  lds
s_add_u32 m0,s88,73600
ds_read_b64_tr_b16 a[98:99],v34 offset:512
buffer_load_dwordx4 v24,s[40:43],s61 offen offset:128  lds
s_add_u32 m0,s88,74624
ds_read_b64_tr_b16 a[100:101],v35 offset:0
buffer_load_dwordx4 v25,s[40:43],s61 offen offset:128  lds
ds_read_b64_tr_b16 a[102:103],v35 offset:512
ds_read_b64_tr_b16 a[104:105],v34 offset:4096
ds_read_b64_tr_b16 a[106:107],v34 offset:4608
ds_read_b64_tr_b16 a[108:109],v35 offset:4096
ds_read_b64_tr_b16 a[110:111],v35 offset:4608
s_waitcnt lgkmcnt(8)
v_mfma_f32_16x16x32_bf16 v[16:19],a[80:83],v[40:43],v[16:19]
v_mfma_f32_16x16x32_bf16 v[20:23],a[84:87],v[40:43],v[20:23]
v_mfma_f32_16x16x32_bf16 v[16:19],a[88:91],v[40:43],v[16:19]
v_mfma_f32_16x16x32_bf16 v[20:23],a[92:95],v[40:43],v[20:23]
s_waitcnt vmcnt(24)
s_barrier
s_add_u32 m0,s88,81920
s_add_u32 s61,s82,6422528
buffer_load_dwordx4 v24,s[40:43],s61 offen offset:0  lds
s_add_u32 m0,s88,82944
ds_read_b64_tr_b16 a[112:113],v34 offset:16384
buffer_load_dwordx4 v25,s[40:43],s61 offen offset:0  lds
s_add_u32 m0,s88,89984
ds_read_b64_tr_b16 a[114:115],v34 offset:16896
buffer_load_dwordx4 v24,s[40:43],s61 offen offset:128  lds
s_add_u32 m0,s88,91008
ds_read_b64_tr_b16 a[116:117],v35 offset:16384
buffer_load_dwordx4 v25,s[40:43],s61 offen offset:128  lds
ds_read_b64_tr_b16 a[118:119],v35 offset:16896
ds_read_b64_tr_b16 a[120:121],v34 offset:20480
ds_read_b64_tr_b16 a[122:123],v34 offset:20992
ds_read_b64_tr_b16 a[124:125],v35 offset:20480
ds_read_b64_tr_b16 a[126:127],v35 offset:20992
s_waitcnt lgkmcnt(8)
v_mfma_f32_16x16x32_bf16 v[16:19],a[96:99],v[40:43],v[16:19]
v_mfma_f32_16x16x32_bf16 v[20:23],a[100:103],v[40:43],v[20:23]
v_mfma_f32_16x16x32_bf16 v[16:19],a[104:107],v[40:43],v[16:19]
v_mfma_f32_16x16x32_bf16 v[20:23],a[108:111],v[40:43],v[20:23]
s_waitcnt vmcnt(24)
s_barrier
s_add_u32 m0,s88,98304
s_add_u32 s61,s82,6881280
buffer_load_dwordx4 v24,s[40:43],s61 offen offset:0  lds
s_add_u32 m0,s88,99328
ds_read_b64_tr_b16 a[128:129],v34 offset:32768
buffer_load_dwordx4 v25,s[40:43],s61 offen offset:0  lds
s_add_u32 m0,s88,106368
ds_read_b64_tr_b16 a[130:131],v34 offset:33280
buffer_load_dwordx4 v24,s[40:43],s61 offen offset:128  lds
s_add_u32 m0,s88,107392
ds_read_b64_tr_b16 a[132:133],v35 offset:32768
buffer_load_dwordx4 v25,s[40:43],s61 offen offset:128  lds
ds_read_b64_tr_b16 a[134:135],v35 offset:33280
ds_read_b64_tr_b16 a[136:137],v34 offset:36864
ds_read_b64_tr_b16 a[138:139],v34 offset:37376
ds_read_b64_tr_b16 a[140:141],v35 offset:36864
ds_read_b64_tr_b16 a[142:143],v35 offset:37376
s_waitcnt lgkmcnt(8)
v_mfma_f32_16x16x32_bf16 v[16:19],a[112:115],v[40:43],v[16:19]
v_mfma_f32_16x16x32_bf16 v[20:23],a[116:119],v[40:43],v[20:23]
v_mfma_f32_16x16x32_bf16 v[16:19],a[120:123],v[40:43],v[16:19]
v_mfma_f32_16x16x32_bf16 v[20:23],a[124:127],v[40:43],v[20:23]
s_waitcnt vmcnt(24)
s_barrier
ds_read_b64_tr_b16 a[144:145],v26 offset:0
ds_read_b64_tr_b16 a[146:147],v26 offset:512
ds_read_b64_tr_b16 a[148:149],v27 offset:0
ds_read_b64_tr_b16 a[150:151],v27 offset:512
ds_read_b64_tr_b16 a[152:153],v26 offset:4096
ds_read_b64_tr_b16 a[154:155],v26 offset:4608
ds_read_b64_tr_b16 a[156:157],v27 offset:4096
ds_read_b64_tr_b16 a[158:159],v27 offset:4608
s_waitcnt lgkmcnt(8)
v_mfma_f32_16x16x32_bf16 v[16:19],a[128:131],v[40:43],v[16:19]
v_mfma_f32_16x16x32_bf16 v[20:23],a[132:135],v[40:43],v[20:23]
v_mfma_f32_16x16x32_bf16 v[16:19],a[136:139],v[40:43],v[16:19]
v_mfma_f32_16x16x32_bf16 v[20:23],a[140:143],v[40:43],v[20:23]
s_waitcnt vmcnt(20)
s_barrier
ds_read_b64_tr_b16 a[160:161],v26 offset:16384
ds_read_b64_tr_b16 a[162:163],v26 offset:16896
ds_read_b64_tr_b16 a[164:165],v27 offset:16384
ds_read_b64_tr_b16 a[166:167],v27 offset:16896
ds_read_b64_tr_b16 a[168:169],v26 offset:20480
ds_read_b64_tr_b16 a[170:171],v26 offset:20992
ds_read_b64_tr_b16 a[172:173],v27 offset:20480
ds_read_b64_tr_b16 a[174:175],v27 offset:20992
s_waitcnt lgkmcnt(8)
v_mfma_f32_16x16x32_bf16 v[16:19],a[144:147],v[40:43],v[16:19]
v_mfma_f32_16x16x32_bf16 v[20:23],a[148:151],v[40:43],v[20:23]
v_mfma_f32_16x16x32_bf16 v[16:19],a[152:155],v[40:43],v[16:19]
v_mfma_f32_16x16x32_bf16 v[20:23],a[156:159],v[40:43],v[20:23]
s_waitcnt vmcnt(16)
s_barrier
ds_read_b64_tr_b16 a[176:177],v26 offset:32768
ds_read_b64_tr_b16 a[178:179],v26 offset:33280
ds_read_b64_tr_b16 a[180:181],v27 offset:32768
ds_read_b64_tr_b16 a[182:183],v27 offset:33280
ds_read_b64_tr_b16 a[184:185],v26 offset:36864
ds_read_b64_tr_b16 a[186:187],v26 offset:37376
ds_read_b64_tr_b16 a[188:189],v27 offset:36864
ds_read_b64_tr_b16 a[190:191],v27 offset:37376
s_getpc_b64 s[46:47]
Lg3o_cp_%=:
s_add_u32 s46,s46,Lg3o_init_%=-Lg3o_cp_%=
s_addc_u32 s47,s47,0
s_lshl_b32 s68,s54,13
v_lshl_add_u32 v144,v2,3,s68
v_min_u32 v145,Lg3o_done_%=-Lg3o_init_%=,v144
global_load_dword v148,v145,s[46:47]
s_waitcnt lgkmcnt(8)
v_mfma_f32_16x16x32_bf16 v[16:19],a[160:163],v[40:43],v[16:19]
v_mfma_f32_16x16x32_bf16 v[20:23],a[164:167],v[40:43],v[20:23]
v_mfma_f32_16x16x32_bf16 v[16:19],a[168:171],v[40:43],v[16:19]
v_mfma_f32_16x16x32_bf16 v[20:23],a[172:175],v[40:43],v[20:23]
s_waitcnt vmcnt(13)
s_barrier
ds_read_b64_tr_b16 a[192:193],v32 offset:0
ds_read_b64_tr_b16 a[194:195],v32 offset:512
ds_read_b64_tr_b16 a[196:197],v33 offset:0
ds_read_b64_tr_b16 a[198:199],v33 offset:512
ds_read_b64_tr_b16 a[200:201],v32 offset:4096
ds_read_b64_tr_b16 a[202:203],v32 offset:4608
ds_read_b64_tr_b16 a[204:205],v33 offset:4096
ds_read_b64_tr_b16 a[206:207],v33 offset:4608
s_waitcnt lgkmcnt(8)
v_mfma_f32_16x16x32_bf16 v[16:19],a[176:179],v[40:43],v[16:19]
v_mfma_f32_16x16x32_bf16 v[20:23],a[180:183],v[40:43],v[20:23])"
R"(
v_mfma_f32_16x16x32_bf16 v[16:19],a[184:187],v[40:43],v[16:19]
v_mfma_f32_16x16x32_bf16 v[20:23],a[188:191],v[40:43],v[20:23]
s_waitcnt vmcnt(9)
s_barrier
ds_read_b64_tr_b16 a[208:209],v32 offset:16384
ds_read_b64_tr_b16 a[210:211],v32 offset:16896
ds_read_b64_tr_b16 a[212:213],v33 offset:16384
ds_read_b64_tr_b16 a[214:215],v33 offset:16896
ds_read_b64_tr_b16 a[216:217],v32 offset:20480
ds_read_b64_tr_b16 a[218:219],v32 offset:20992
ds_read_b64_tr_b16 a[220:221],v33 offset:20480
ds_read_b64_tr_b16 a[222:223],v33 offset:20992
s_waitcnt lgkmcnt(8)
v_mfma_f32_16x16x32_bf16 v[16:19],a[192:195],v[40:43],v[16:19]
v_mfma_f32_16x16x32_bf16 v[20:23],a[196:199],v[40:43],v[20:23]
v_mfma_f32_16x16x32_bf16 v[16:19],a[200:203],v[40:43],v[16:19]
v_mfma_f32_16x16x32_bf16 v[20:23],a[204:207],v[40:43],v[20:23]
s_waitcnt vmcnt(5)
s_barrier
ds_read_b64_tr_b16 a[224:225],v32 offset:32768
ds_read_b64_tr_b16 a[226:227],v32 offset:33280
ds_read_b64_tr_b16 a[228:229],v33 offset:32768
ds_read_b64_tr_b16 a[230:231],v33 offset:33280
ds_read_b64_tr_b16 a[232:233],v32 offset:36864
ds_read_b64_tr_b16 a[234:235],v32 offset:37376
ds_read_b64_tr_b16 a[236:237],v33 offset:36864
ds_read_b64_tr_b16 a[238:239],v33 offset:37376
s_waitcnt lgkmcnt(8)
v_mfma_f32_16x16x32_bf16 v[16:19],a[208:211],v[40:43],v[16:19]
v_mfma_f32_16x16x32_bf16 v[20:23],a[212:215],v[40:43],v[20:23]
v_mfma_f32_16x16x32_bf16 v[16:19],a[216:219],v[40:43],v[16:19]
v_mfma_f32_16x16x32_bf16 v[20:23],a[220:223],v[40:43],v[20:23]
s_waitcnt vmcnt(1)
s_barrier
ds_read_b64_tr_b16 a[240:241],v34 offset:0
ds_read_b64_tr_b16 a[242:243],v34 offset:512
ds_read_b64_tr_b16 a[244:245],v35 offset:0
ds_read_b64_tr_b16 a[246:247],v35 offset:512
ds_read_b64_tr_b16 a[248:249],v34 offset:4096
ds_read_b64_tr_b16 a[250:251],v34 offset:4608
ds_read_b64_tr_b16 a[252:253],v35 offset:4096
ds_read_b64_tr_b16 a[254:255],v35 offset:4608
s_waitcnt lgkmcnt(8)
v_mfma_f32_16x16x32_bf16 v[16:19],a[224:227],v[40:43],v[16:19]
v_mfma_f32_16x16x32_bf16 v[20:23],a[228:231],v[40:43],v[20:23]
v_mfma_f32_16x16x32_bf16 v[16:19],a[232:235],v[40:43],v[16:19]
v_mfma_f32_16x16x32_bf16 v[20:23],a[236:239],v[40:43],v[20:23]
s_waitcnt lgkmcnt(0)
v_mfma_f32_16x16x32_bf16 v[16:19],a[240:243],v[40:43],v[16:19]
v_mfma_f32_16x16x32_bf16 v[20:23],a[244:247],v[40:43],v[20:23]
v_mfma_f32_16x16x32_bf16 v[16:19],a[248:251],v[40:43],v[16:19]
v_mfma_f32_16x16x32_bf16 v[20:23],a[252:255],v[40:43],v[20:23]
s_waitcnt lgkmcnt(0)
s_barrier
Lg3o_init_%=:
v_mov_b32 v5,s60
s_mov_b32 s59,536870912
s_add_u32 s57,s56,131072
s_mov_b32 s79,0
s_mov_b32 s75,0x3a800000
s_mov_b32 s76,0xc0135761
s_mov_b32 s77,0x3d372713
s_mov_b32 s44,s59
s_mov_b32 s45,s59
s_mov_b32 s46,s59
s_mov_b32 s47,s59
s_mov_b32 s48,s83
s_add_u32 s49,s48,s55
s_cmp_eq_u32 s49,s56
s_cselect_b32 s49,s57,s49
s_cmp_eq_u32 s49,s58
s_cselect_b32 s49,s59,s49
s_add_u32 s50,s49,s55
s_cmp_eq_u32 s50,s56
s_cselect_b32 s50,s57,s50
s_cmp_eq_u32 s50,s58
s_cselect_b32 s50,s59,s50
s_add_u32 s51,s50,s55
s_cmp_eq_u32 s51,s56
s_cselect_b32 s51,s57,s51
s_cmp_eq_u32 s51,s58
s_cselect_b32 s51,s59,s51
s_add_u32 s52,s51,s55
s_cmp_eq_u32 s52,s56
s_cselect_b32 s52,s57,s52
s_cmp_eq_u32 s52,s58
s_cselect_b32 s52,s59,s52
s_lshl_b32 s63,s54,12
s_add_u32 s63,s63,65536
s_add_u32 s64,s63,32768
s_lshl_b32 s88,s54,10
s_add_u32 s88,s88,131072
s_lshl_b32 s78,s54,11
s_add_u32 s78,s78,132608
v_add_u32 v1,16384,v0
v_mov_b32 v12,0x3727c5ac
s_getpc_b64 s[90:91]
Lg3o_pc_%=:
s_mov_b32 s68,Lg3o_d1w0_%=-Lg3o_pc_%=
s_cmp_eq_u32 s54,1
s_cselect_b32 s68,Lg3o_d1w1_%=-Lg3o_pc_%=,s68
s_cmp_eq_u32 s54,2
s_cselect_b32 s68,Lg3o_d1w2_%=-Lg3o_pc_%=,s68
s_cmp_eq_u32 s54,3
s_cselect_b32 s68,Lg3o_d1w3_%=-Lg3o_pc_%=,s68
s_add_u32 s92,s90,s68
s_addc_u32 s93,s91,0
s_mov_b32 s68,Lg3o_d0w0_%=-Lg3o_pc_%=
s_cmp_eq_u32 s54,1
s_cselect_b32 s68,Lg3o_d0w1_%=-Lg3o_pc_%=,s68
s_cmp_eq_u32 s54,2
s_cselect_b32 s68,Lg3o_d0w2_%=-Lg3o_pc_%=,s68
s_cmp_eq_u32 s54,3
s_cselect_b32 s68,Lg3o_d0w3_%=-Lg3o_pc_%=,s68
s_add_u32 s90,s90,s68
s_addc_u32 s91,s91,0
s_mov_b32 s66,s59
s_mov_b32 s65,0
s_mov_b32 s62,s63
s_branch Lg3o_slow0_%=
Lg3o_d0w0_%=:
v_mfma_f32_16x16x32_bf16 v[28:31],a[4:7],v[128:131],0
s_add_u32 m0,s62,16384
ds_read_b128 v[156:159],v2 offset:7168
v_mul_f32 v67,v61,v60
v_mfma_f32_16x16x32_bf16 v[28:31],a[12:15],v[132:135],v[28:31]
buffer_load_dwordx4 v1,s[36:39],s66 offen offset:0 sc1 lds
v_mfma_f32_16x16x32_bf16 v[24:27],a[8:11],v[132:135],0
s_mul_i32 s73,s47,7
ds_read_b128 v[160:163],v2 offset:8192
v_mul_f32 v66,s77,v66
v_mfma_f32_16x16x32_bf16 v[24:27],a[0:3],v[128:131],v[24:27]
s_lshr_b32 s73,s73,1
v_mul_f32 v66,v66,v65
v_fma_f32 v32,-v64,v16,v32
v_mfma_f32_16x16x32_bf16 v[24:27],a[16:19],v[136:139],v[24:27]
s_lshr_b32 s72,s66,5
ds_read_b128 v[164:167],v2 offset:9216
v_fma_f32 v33,-v64,v17,v33
v_mfma_f32_16x16x32_bf16 v[24:27],a[24:27],v[140:143],v[24:27]
buffer_load_dwordx4 v1,s[36:39],s66 offen offset:1024 sc1 lds
v_fma_f32 v34,-v64,v18,v34
v_fma_f32 v35,-v64,v19,v35
v_mul_f32 v40,v32,v32
v_mul_f32 v41,v33,v33
v_mul_f32 v42,v34,v34
v_mfma_f32_16x16x32_bf16 v[28:31],a[28:31],v[140:143],v[28:31]
s_lshr_b32 s71,s62,2
ds_read_b128 v[168:171],v2 offset:10240
v_mul_f32 v43,v35,v35
v_mfma_f32_16x16x32_bf16 v[28:31],a[20:23],v[136:139],v[28:31]
s_waitcnt lgkmcnt(3)
v_fma_f32 v40,v66,v40,v65
v_mul_f32 v40,v32,v40
v_mfma_f32_16x16x32_bf16 v[28:31],a[36:39],v[144:147],v[28:31]
ds_read_b128 v[172:175],v2 offset:11264
v_fma_f32 v41,v66,v41,v65
v_mfma_f32_16x16x32_bf16 v[28:31],a[44:47],v[148:151],v[28:31]
buffer_load_dwordx4 v1,s[36:39],s66 offen offset:2048 sc1 lds
v_exp_f32 v40,v40
v_mul_f32 v41,v33,v41
v_exp_f32 v41,v41
v_mfma_f32_16x16x32_bf16 v[24:27],a[40:43],v[148:151],v[24:27]
ds_read_b128 v[176:179],v2 offset:12288
v_fma_f32 v40,v40,v67,v67
v_mfma_f32_16x16x32_bf16 v[24:27],a[32:35],v[144:147],v[24:27]
v_rcp_f32 v40,v40
v_mfma_f32_16x16x32_bf16 v[24:27],a[48:51],v[152:155],v[24:27]
ds_read_b128 v[180:183],v2 offset:13312
v_fma_f32 v41,v41,v67,v67
v_mfma_f32_16x16x32_bf16 v[24:27],a[56:59],v[156:159],v[24:27]
buffer_load_dwordx4 v1,s[36:39],s66 offen offset:3072 sc1 lds
v_rcp_f32 v41,v41
v_fma_f32 v42,v66,v42,v65
v_mul_f32 v42,v34,v42
v_fma_f32 v43,v66,v43,v65
v_mfma_f32_16x16x32_bf16 v[28:31],a[60:63],v[156:159],v[28:31]
s_add_u32 m0,s71,s88
ds_read_b128 v[184:187],v2 offset:14336
v_mul_f32 v43,v35,v43
v_mfma_f32_16x16x32_bf16 v[28:31],a[52:55],v[152:155],v[28:31]
s_waitcnt lgkmcnt(3)
buffer_load_dword v9,s[36:39],s72 offen offset:0 sc1 lds
v_mfma_f32_16x16x32_bf16 v[28:31],a[68:71],v[160:163],v[28:31]
s_waitcnt vmcnt(11)
ds_read_b128 v[188:191],v2 offset:15360
v_mul_f32 v32,v32,v40
v_mfma_f32_16x16x32_bf16 v[28:31],a[76:79],v[164:167],v[28:31]
s_barrier
v_mfma_f32_16x16x32_bf16 v[24:27],a[72:75],v[164:167],v[24:27]
s_xor_b32 s87,s64,65536
ds_read_b128 v[128:131],v2 offset:16384
ds_read_b32 v56,v8 offset:0
v_mfma_f32_16x16x32_bf16 v[24:27],a[64:67],v[160:163],v[24:27]
s_cmp_ge_u32 s66,s59
v_exp_f32 v42,v42
v_mfma_f32_16x16x32_bf16 v[24:27],a[80:83],v[168:171],v[24:27]
s_cselect_b32 s86,s49,s50
ds_read_b128 v[132:135],v2 offset:17408
v_fma_f32 v42,v42,v67,v67)"
R"(
v_mfma_f32_16x16x32_bf16 v[24:27],a[88:91],v[172:175],v[24:27]
s_cselect_b32 s87,s87,s63
v_exp_f32 v43,v43
v_rcp_f32 v42,v42
v_mul_f32 v33,v33,v41
v_cvt_pk_bf16_f32 v48,v32,v33
v_fma_f32 v36,-v64,v20,v36
v_mfma_f32_16x16x32_bf16 v[28:31],a[92:95],v[172:175],v[28:31]
s_lshr_b32 s68,s50,5
ds_read_b128 v[136:139],v2 offset:18432
v_fma_f32 v43,v43,v67,v67
v_mfma_f32_16x16x32_bf16 v[28:31],a[84:87],v[168:171],v[28:31]
s_waitcnt lgkmcnt(4)
v_rcp_f32 v43,v43
v_mfma_f32_16x16x32_bf16 v[28:31],a[100:103],v[176:179],v[28:31]
s_waitcnt vmcnt(10) lgkmcnt(2)
ds_read_b128 v[140:143],v2 offset:19456
v_mul_f32 v34,v34,v42
v_mfma_f32_16x16x32_bf16 v[28:31],a[108:111],v[180:183],v[28:31]
s_barrier
v_mfma_f32_16x16x32_bf16 v[24:27],a[104:107],v[180:183],v[24:27]
s_lshr_b32 s70,s51,5
ds_read_b128 v[144:147],v2 offset:20480
ds_read_b32 v13,v7 offset:0
v_mfma_f32_16x16x32_bf16 v[24:27],a[96:99],v[176:179],v[24:27]
s_mov_b32 s62,s87
v_add_f32_dpp v56,v56,v56 quad_perm:[1,0,3,2] row_mask:0xf bank_mask:0xf
v_mul_f32 v35,v35,v43
v_mfma_f32_16x16x32_bf16 v[24:27],a[112:115],v[184:187],v[24:27]
ds_read_b128 v[148:151],v2 offset:21504
v_add_f32_dpp v56,v56,v56 quad_perm:[2,3,0,1] row_mask:0xf bank_mask:0xf
v_mfma_f32_16x16x32_bf16 v[24:27],a[120:123],v[188:191],v[24:27]
s_nop 0
v_add_f32_dpp v56,v56,v56 row_half_mirror row_mask:0xf bank_mask:0xf
v_cvt_pk_bf16_f32 v49,v34,v35
v_fma_f32 v37,-v64,v21,v37
v_fma_f32 v38,-v64,v22,v38
v_fma_f32 v39,-v64,v23,v39
v_mul_f32 v44,v36,v36
v_mul_f32 v45,v37,v37
v_mfma_f32_16x16x32_bf16 v[28:31],a[124:127],v[188:191],v[28:31]
s_waitcnt lgkmcnt(1)
ds_write_b32 v10,v56 offset:0
v_mfma_f32_16x16x32_bf16 v[28:31],a[116:119],v[184:187],v[28:31]
ds_read_b128 v[152:155],v2 offset:22528
v_mul_f32 v46,v38,v38
v_mfma_f32_16x16x32_bf16 v[28:31],a[132:135],v[128:131],v[28:31]
v_cmp_eq_u32 vcc,s60,v13
v_mul_f32 v47,v39,v39
v_fma_f32 v44,v66,v44,v65
v_mul_f32 v44,v36,v44
v_mfma_f32_16x16x32_bf16 v[28:31],a[140:143],v[132:135],v[28:31]
s_cmp_eq_u32 vcc_lo,-1
s_cselect_b32 s67,s86,s59
s_cmp_ge_u32 s67,s59
s_cselect_b32 s68,s68,s70
ds_read_b128 v[156:159],v2 offset:23552
v_exp_f32 v44,v44
s_add_u32 m0,s78,256
v_mfma_f32_16x16x32_bf16 v[24:27],a[136:139],v[132:135],v[24:27]
buffer_load_dword v6,s[36:39],s68 offen offset:0 sc1 lds
v_mfma_f32_16x16x32_bf16 v[24:27],a[128:131],v[128:131],v[24:27]
s_mov_b32 m0,s62
ds_read_b128 v[160:163],v2 offset:24576
v_fma_f32 v44,v44,v67,v67
v_mfma_f32_16x16x32_bf16 v[24:27],a[144:147],v[136:139],v[24:27]
buffer_load_dwordx4 v0,s[36:39],s67 offen offset:0 sc1 lds
v_mfma_f32_16x16x32_bf16 v[24:27],a[152:155],v[140:143],v[24:27]
s_xor_b32 s63,s63,65536
ds_read_b128 v[164:167],v2 offset:25600
v_rcp_f32 v44,v44
v_fma_f32 v45,v66,v45,v65
v_mul_f32 v45,v37,v45
v_exp_f32 v45,v45
v_mfma_f32_16x16x32_bf16 v[28:31],a[156:159],v[140:143],v[28:31]
s_add_u32 s53,s52,s55
ds_read_b128 v[168:171],v2 offset:26624
v_fma_f32 v45,v45,v67,v67
v_mfma_f32_16x16x32_bf16 v[28:31],a[148:151],v[136:139],v[28:31]
s_waitcnt lgkmcnt(3)
v_rcp_f32 v45,v45
v_mfma_f32_16x16x32_bf16 v[28:31],a[164:167],v[144:147],v[28:31]
buffer_load_dwordx4 v0,s[36:39],s67 offen offset:1024 sc1 lds
v_mfma_f32_16x16x32_bf16 v[28:31],a[172:175],v[148:151],v[28:31]
s_cmp_eq_u32 s53,s56
ds_read_b128 v[172:175],v2 offset:27648
v_fma_f32 v46,v66,v46,v65
v_mul_f32 v46,v38,v46
v_exp_f32 v46,v46
v_fma_f32 v47,v66,v47,v65
v_mul_f32 v47,v39,v47
v_mfma_f32_16x16x32_bf16 v[24:27],a[168:171],v[148:151],v[24:27]
s_cselect_b32 s53,s57,s53
ds_read_b128 v[176:179],v2 offset:28672
v_fma_f32 v46,v46,v67,v67
v_mfma_f32_16x16x32_bf16 v[24:27],a[160:163],v[144:147],v[24:27]
s_cmp_eq_u32 s53,s58
v_exp_f32 v47,v47
v_mfma_f32_16x16x32_bf16 v[24:27],a[176:179],v[152:155],v[24:27]
buffer_load_dwordx4 v0,s[36:39],s67 offen offset:2048 sc1 lds
v_mfma_f32_16x16x32_bf16 v[24:27],a[184:187],v[156:159],v[24:27]
s_cselect_b32 s53,s59,s53
ds_read_b128 v[180:183],v2 offset:29696
v_rcp_f32 v46,v46
v_fma_f32 v47,v47,v67,v67
v_rcp_f32 v47,v47
v_mul_f32 v36,v36,v44
v_mfma_f32_16x16x32_bf16 v[28:31],a[188:191],v[156:159],v[28:31]
s_cmp_ge_u32 s66,s59
ds_read_b128 v[184:187],v2 offset:30720
v_mul_f32 v37,v37,v45
v_mfma_f32_16x16x32_bf16 v[28:31],a[180:183],v[152:155],v[28:31]
s_waitcnt lgkmcnt(3)
v_mul_f32 v38,v38,v46
v_mul_f32 v39,v39,v47
v_mfma_f32_16x16x32_bf16 v[28:31],a[196:199],v[160:163],v[28:31]
s_waitcnt vmcnt(9)
ds_read_b128 v[188:191],v2 offset:31744
v_cvt_pk_bf16_f32 v50,v36,v37
v_mfma_f32_16x16x32_bf16 v[28:31],a[204:207],v[164:167],v[28:31]
s_barrier
v_mfma_f32_16x16x32_bf16 v[24:27],a[200:203],v[164:167],v[24:27]
buffer_load_dwordx4 v0,s[36:39],s67 offen offset:3072 sc1 lds
v_mfma_f32_16x16x32_bf16 v[24:27],a[192:195],v[160:163],v[24:27]
ds_read_b128 v[128:131],v2 offset:32768
v_cvt_pk_bf16_f32 v51,v38,v39
v_mfma_f32_16x16x32_bf16 v[24:27],a[208:211],v[168:171],v[24:27]
buffer_store_dwordx4 v[48:51],v3,s[36:39],s73 offen offset:0 sc1
ds_read_b128 v[132:135],v2 offset:33792
ds_read_b64 v[60:61],v11 offset:0
v_mfma_f32_16x16x32_bf16 v[24:27],a[216:219],v[172:175],v[24:27]
v_mfma_f32_16x16x32_bf16 v[28:31],a[220:223],v[172:175],v[28:31]
ds_read_b128 v[136:139],v2 offset:34816
v_mfma_f32_16x16x32_bf16 v[28:31],a[212:215],v[168:171],v[28:31]
s_waitcnt lgkmcnt(4)
v_mfma_f32_16x16x32_bf16 v[28:31],a[228:231],v[176:179],v[28:31]
s_waitcnt lgkmcnt(1)
ds_read_b128 v[140:143],v2 offset:35840
v_mul_f32 v64,s75,v60
v_mfma_f32_16x16x32_bf16 v[28:31],a[236:239],v[180:183],v[28:31]
v_fma_f32 v61,v61,s75,v12
v_fma_f32 v61,-v64,v64,v61
v_mfma_f32_16x16x32_bf16 v[24:27],a[232:235],v[180:183],v[24:27]
ds_read_b128 v[144:147],v2 offset:36864
v_max_f32 v61,v61,v12
v_mfma_f32_16x16x32_bf16 v[24:27],a[224:227],v[176:179],v[24:27]
v_rsq_f32 v60,v61
v_mfma_f32_16x16x32_bf16 v[24:27],a[240:243],v[184:187],v[24:27]
ds_read_b128 v[148:151],v2 offset:37888
v_mul_f32 v65,s76,v60
v_mfma_f32_16x16x32_bf16 v[24:27],a[248:251],v[188:191],v[24:27]
v_mul_f32 v66,v60,v60
v_mfma_f32_16x16x32_bf16 v[28:31],a[252:255],v[188:191],v[28:31]
s_waitcnt lgkmcnt(2)
ds_read_b128 v[152:155],v2 offset:38912
v_mfma_f32_16x16x32_bf16 v[28:31],a[244:247],v[184:187],v[28:31]
s_cbranch_scc1 Lg3o_slow1_%=
Lg3o_d1w0_%=:
v_mfma_f32_16x16x32_bf16 v[36:39],a[4:7],v[128:131],0
s_add_u32 m0,s62,16384
ds_read_b128 v[156:159],v2 offset:39936
v_mul_f32 v67,v61,v60
v_mfma_f32_16x16x32_bf16 v[36:39],a[12:15],v[132:135],v[36:39]
buffer_load_dwordx4 v1,s[36:39],s67 offen offset:0 sc1 lds
v_mfma_f32_16x16x32_bf16 v[32:35],a[8:11],v[132:135],0
s_mul_i32 s73,s48,7
ds_read_b128 v[160:163],v2 offset:40960
v_mul_f32 v66,s77,v66
v_mfma_f32_16x16x32_bf16 v[32:35],a[0:3],v[128:131],v[32:35]
s_lshr_b32 s73,s73,1
v_mul_f32 v66,v66,v65
v_fma_f32 v24,-v64,v16,v24
v_mfma_f32_16x16x32_bf16 v[32:35],a[16:19],v[136:139],v[32:35]
s_lshr_b32 s72,s67,5
ds_read_b128 v[164:167],v2 offset:41984
v_fma_f32 v25,-v64,v17,v25
v_mfma_f32_16x16x32_bf16 v[32:35],a[24:27],v[140:143],v[32:35]
buffer_load_dwordx4 v1,s[36:39],s67 offen offset:1024 sc1 lds
v_fma_f32 v26,-v64,v18,v26
v_fma_f32 v27,-v64,v19,v27
v_mul_f32 v40,v24,v24
v_mul_f32 v41,v25,v25
v_mul_f32 v42,v26,v26
v_mfma_f32_16x16x32_bf16 v[36:39],a[28:31],v[140:143],v[36:39]
s_lshr_b32 s71,s62,2
ds_read_b128 v[168:171],v2 offset:43008
v_mul_f32 v43,v27,v27
v_mfma_f32_16x16x32_bf16 v[36:39],a[20:23],v[136:139],v[36:39]
s_waitcnt lgkmcnt(3)
v_fma_f32 v40,v66,v40,v65
v_mul_f32 v40,v24,v40
v_mfma_f32_16x16x32_bf16 v[36:39],a[36:39],v[144:147],v[36:39]
s_mov_b64 s[44:45],s[46:47]
ds_read_b128 v[172:175],v2 offset:44032
v_fma_f32 v41,v66,v41,v65
v_mfma_f32_16x16x32_bf16 v[36:39],a[44:47],v[148:151],v[36:39]
buffer_load_dwordx4 v1,s[36:39],s67 offen offset:2048 sc1 lds
v_exp_f32 v40,v40
v_mul_f32 v41,v25,v41
v_exp_f32 v41,v41)"
R"(
v_mfma_f32_16x16x32_bf16 v[32:35],a[40:43],v[148:151],v[32:35]
s_mov_b64 s[46:47],s[48:49]
ds_read_b128 v[176:179],v2 offset:45056
v_fma_f32 v40,v40,v67,v67
v_mfma_f32_16x16x32_bf16 v[32:35],a[32:35],v[144:147],v[32:35]
s_mov_b64 s[48:49],s[50:51]
v_rcp_f32 v40,v40
v_mfma_f32_16x16x32_bf16 v[32:35],a[48:51],v[152:155],v[32:35]
ds_read_b128 v[180:183],v2 offset:46080
v_fma_f32 v41,v41,v67,v67
v_mfma_f32_16x16x32_bf16 v[32:35],a[56:59],v[156:159],v[32:35]
buffer_load_dwordx4 v1,s[36:39],s67 offen offset:3072 sc1 lds
v_rcp_f32 v41,v41
v_fma_f32 v42,v66,v42,v65
v_mul_f32 v42,v26,v42
v_mfma_f32_16x16x32_bf16 v[36:39],a[60:63],v[156:159],v[36:39]
s_add_u32 m0,s71,s88
ds_read_b128 v[184:187],v2 offset:47104
v_fma_f32 v43,v66,v43,v65
v_mfma_f32_16x16x32_bf16 v[36:39],a[52:55],v[152:155],v[36:39]
s_waitcnt lgkmcnt(3)
buffer_load_dword v9,s[36:39],s72 offen offset:0 sc1 lds
v_mfma_f32_16x16x32_bf16 v[36:39],a[68:71],v[160:163],v[36:39]
s_waitcnt vmcnt(11)
ds_read_b128 v[188:191],v2 offset:48128
v_mul_f32 v43,v27,v43
v_mfma_f32_16x16x32_bf16 v[36:39],a[76:79],v[164:167],v[36:39]
s_barrier
v_mfma_f32_16x16x32_bf16 v[32:35],a[72:75],v[164:167],v[32:35]
s_xor_b32 s87,s63,65536
ds_read_b128 v[128:131],v2 offset:49152
ds_read_b32 v56,v8 offset:8192
v_mfma_f32_16x16x32_bf16 v[32:35],a[64:67],v[160:163],v[32:35]
s_cmp_ge_u32 s67,s59
v_exp_f32 v42,v42
v_mfma_f32_16x16x32_bf16 v[32:35],a[80:83],v[168:171],v[32:35]
s_cselect_b32 s86,s50,s51
ds_read_b128 v[132:135],v2 offset:50176
v_fma_f32 v42,v42,v67,v67
v_mfma_f32_16x16x32_bf16 v[32:35],a[88:91],v[172:175],v[32:35]
s_cselect_b32 s87,s87,s64
v_exp_f32 v43,v43
v_rcp_f32 v42,v42
v_mul_f32 v24,v24,v40
v_mul_f32 v25,v25,v41
v_cvt_pk_bf16_f32 v48,v24,v25
v_mfma_f32_16x16x32_bf16 v[36:39],a[92:95],v[172:175],v[36:39]
s_lshr_b32 s68,s51,5
ds_read_b128 v[136:139],v2 offset:51200
v_fma_f32 v43,v43,v67,v67
v_mfma_f32_16x16x32_bf16 v[36:39],a[84:87],v[168:171],v[36:39]
s_waitcnt lgkmcnt(4)
v_rcp_f32 v43,v43
v_mfma_f32_16x16x32_bf16 v[36:39],a[100:103],v[176:179],v[36:39]
s_waitcnt vmcnt(10) lgkmcnt(2)
ds_read_b128 v[140:143],v2 offset:52224
v_mul_f32 v26,v26,v42
v_mfma_f32_16x16x32_bf16 v[36:39],a[108:111],v[180:183],v[36:39]
s_barrier
v_mfma_f32_16x16x32_bf16 v[32:35],a[104:107],v[180:183],v[32:35]
s_lshr_b32 s70,s52,5
ds_read_b128 v[144:147],v2 offset:53248
ds_read_b32 v13,v7 offset:256
v_mfma_f32_16x16x32_bf16 v[32:35],a[96:99],v[176:179],v[32:35]
s_mov_b32 s62,s87
v_add_f32_dpp v56,v56,v56 quad_perm:[1,0,3,2] row_mask:0xf bank_mask:0xf
v_mul_f32 v27,v27,v43
v_mfma_f32_16x16x32_bf16 v[32:35],a[112:115],v[184:187],v[32:35]
s_mov_b64 s[50:51],s[52:53]
ds_read_b128 v[148:151],v2 offset:54272
v_add_f32_dpp v56,v56,v56 quad_perm:[2,3,0,1] row_mask:0xf bank_mask:0xf
v_mfma_f32_16x16x32_bf16 v[32:35],a[120:123],v[188:191],v[32:35]
s_nop 0
v_add_f32_dpp v56,v56,v56 row_half_mirror row_mask:0xf bank_mask:0xf
v_cvt_pk_bf16_f32 v49,v26,v27
v_fma_f32 v28,-v64,v20,v28
v_fma_f32 v29,-v64,v21,v29
v_fma_f32 v30,-v64,v22,v30
v_fma_f32 v31,-v64,v23,v31
v_mfma_f32_16x16x32_bf16 v[36:39],a[124:127],v[188:191],v[36:39]
s_waitcnt lgkmcnt(1)
ds_write_b32 v10,v56 offset:128
v_mfma_f32_16x16x32_bf16 v[36:39],a[116:119],v[184:187],v[36:39]
ds_read_b128 v[152:155],v2 offset:55296
v_mul_f32 v44,v28,v28
v_mfma_f32_16x16x32_bf16 v[36:39],a[132:135],v[128:131],v[36:39]
v_cmp_eq_u32 vcc,s60,v13
v_mul_f32 v45,v29,v29
v_mul_f32 v46,v30,v30
v_mul_f32 v47,v31,v31
v_mfma_f32_16x16x32_bf16 v[36:39],a[140:143],v[132:135],v[36:39]
s_cmp_eq_u32 vcc_lo,-1
s_cselect_b32 s66,s86,s59
s_cmp_ge_u32 s66,s59
s_cselect_b32 s68,s68,s70
ds_read_b128 v[156:159],v2 offset:56320
v_fma_f32 v44,v66,v44,v65
v_mul_f32 v44,v28,v44
s_add_u32 m0,s78,0
v_mfma_f32_16x16x32_bf16 v[32:35],a[136:139],v[132:135],v[32:35]
buffer_load_dword v6,s[36:39],s68 offen offset:0 sc1 lds
v_mfma_f32_16x16x32_bf16 v[32:35],a[128:131],v[128:131],v[32:35]
s_mov_b32 m0,s62
ds_read_b128 v[160:163],v2 offset:57344
v_fma_f32 v45,v66,v45,v65
v_mfma_f32_16x16x32_bf16 v[32:35],a[144:147],v[136:139],v[32:35]
buffer_load_dwordx4 v0,s[36:39],s66 offen offset:0 sc1 lds
v_mfma_f32_16x16x32_bf16 v[32:35],a[152:155],v[140:143],v[32:35]
s_xor_b32 s64,s64,65536
ds_read_b128 v[164:167],v2 offset:58368
v_exp_f32 v44,v44
v_mul_f32 v45,v29,v45
v_exp_f32 v45,v45
v_mfma_f32_16x16x32_bf16 v[36:39],a[156:159],v[140:143],v[36:39]
s_add_u32 s74,s53,s55
ds_read_b128 v[168:171],v2 offset:59392
v_fma_f32 v44,v44,v67,v67
v_mfma_f32_16x16x32_bf16 v[36:39],a[148:151],v[136:139],v[36:39]
s_waitcnt lgkmcnt(3)
v_rcp_f32 v44,v44
v_mfma_f32_16x16x32_bf16 v[36:39],a[164:167],v[144:147],v[36:39]
buffer_load_dwordx4 v0,s[36:39],s66 offen offset:1024 sc1 lds
v_mfma_f32_16x16x32_bf16 v[36:39],a[172:175],v[148:151],v[36:39]
s_cmp_eq_u32 s74,s56
ds_read_b128 v[172:175],v2 offset:60416
v_fma_f32 v45,v45,v67,v67
v_rcp_f32 v45,v45
v_fma_f32 v46,v66,v46,v65
v_mul_f32 v46,v30,v46
v_fma_f32 v47,v66,v47,v65
v_mfma_f32_16x16x32_bf16 v[32:35],a[168:171],v[148:151],v[32:35]
s_cselect_b32 s74,s57,s74
ds_read_b128 v[176:179],v2 offset:61440
v_mul_f32 v47,v31,v47
v_mfma_f32_16x16x32_bf16 v[32:35],a[160:163],v[144:147],v[32:35]
s_cmp_eq_u32 s74,s58
v_exp_f32 v46,v46
v_mfma_f32_16x16x32_bf16 v[32:35],a[176:179],v[152:155],v[32:35]
buffer_load_dwordx4 v0,s[36:39],s66 offen offset:2048 sc1 lds
v_mfma_f32_16x16x32_bf16 v[32:35],a[184:187],v[156:159],v[32:35]
s_cselect_b32 s74,s59,s74
ds_read_b128 v[180:183],v2 offset:62464
v_exp_f32 v47,v47
v_fma_f32 v46,v46,v67,v67
v_rcp_f32 v46,v46
v_mfma_f32_16x16x32_bf16 v[36:39],a[188:191],v[156:159],v[36:39]
s_mov_b32 s52,s74
ds_read_b128 v[184:187],v2 offset:63488
v_fma_f32 v47,v47,v67,v67
v_mfma_f32_16x16x32_bf16 v[36:39],a[180:183],v[152:155],v[36:39]
s_waitcnt lgkmcnt(3)
v_rcp_f32 v47,v47
v_mfma_f32_16x16x32_bf16 v[36:39],a[196:199],v[160:163],v[36:39]
s_waitcnt vmcnt(9)
ds_read_b128 v[188:191],v2 offset:64512
v_xor_b32 v2,65536,v2
v_mfma_f32_16x16x32_bf16 v[36:39],a[204:207],v[164:167],v[36:39]
s_barrier
v_mfma_f32_16x16x32_bf16 v[32:35],a[200:203],v[164:167],v[32:35]
buffer_load_dwordx4 v0,s[36:39],s66 offen offset:3072 sc1 lds
v_mfma_f32_16x16x32_bf16 v[32:35],a[192:195],v[160:163],v[32:35]
s_cmp_lt_u32 s67,s59
ds_read_b128 v[128:131],v2 offset:0
v_mul_f32 v28,v28,v44
v_mul_f32 v29,v29,v45
v_mul_f32 v30,v30,v46
v_mul_f32 v31,v31,v47
v_cvt_pk_bf16_f32 v50,v28,v29
v_cvt_pk_bf16_f32 v51,v30,v31
v_mfma_f32_16x16x32_bf16 v[32:35],a[208:211],v[168:171],v[32:35]
buffer_store_dwordx4 v[48:51],v3,s[36:39],s73 offen offset:0 sc1
ds_read_b128 v[132:135],v2 offset:1024
ds_read_b64 v[60:61],v11 offset:128
v_xor_b32 v8,16384,v8
v_mfma_f32_16x16x32_bf16 v[32:35],a[216:219],v[172:175],v[32:35]
v_mfma_f32_16x16x32_bf16 v[36:39],a[220:223],v[172:175],v[36:39]
ds_read_b128 v[136:139],v2 offset:2048
v_mfma_f32_16x16x32_bf16 v[36:39],a[212:215],v[168:171],v[36:39]
s_waitcnt lgkmcnt(4)
v_mfma_f32_16x16x32_bf16 v[36:39],a[228:231],v[176:179],v[36:39]
s_waitcnt lgkmcnt(1)
ds_read_b128 v[140:143],v2 offset:3072
v_mul_f32 v64,s75,v60
v_mfma_f32_16x16x32_bf16 v[36:39],a[236:239],v[180:183],v[36:39]
v_fma_f32 v61,v61,s75,v12
v_fma_f32 v61,-v64,v64,v61
v_mfma_f32_16x16x32_bf16 v[32:35],a[232:235],v[180:183],v[32:35]
ds_read_b128 v[144:147],v2 offset:4096
v_max_f32 v61,v61,v12
v_mfma_f32_16x16x32_bf16 v[32:35],a[224:227],v[176:179],v[32:35]
v_rsq_f32 v60,v61
v_mfma_f32_16x16x32_bf16 v[32:35],a[240:243],v[184:187],v[32:35]
ds_read_b128 v[148:151],v2 offset:5120
v_mul_f32 v65,s76,v60
v_mfma_f32_16x16x32_bf16 v[32:35],a[248:251],v[188:191],v[32:35]
v_mul_f32 v66,v60,v60
v_mfma_f32_16x16x32_bf16 v[36:39],a[252:255],v[188:191],v[36:39]
s_waitcnt lgkmcnt(2)
ds_read_b128 v[152:155],v2 offset:6144
v_mfma_f32_16x16x32_bf16 v[36:39],a[244:247],v[184:187],v[36:39])"
R"(
s_cbranch_scc1 Lg3o_d0w0_%=
s_branch Lg3o_slow0_%=
Lg3o_d0w1_%=:
v_mfma_f32_16x16x32_bf16 v[28:31],a[4:7],v[128:131],0
s_add_u32 m0,s62,16384
ds_read_b128 v[156:159],v2 offset:7168
v_mul_f32 v67,v61,v60
v_mfma_f32_16x16x32_bf16 v[28:31],a[12:15],v[132:135],v[28:31]
s_mul_i32 s73,s47,7
v_mul_f32 v66,s77,v66
v_mul_f32 v66,v66,v65
v_mfma_f32_16x16x32_bf16 v[24:27],a[8:11],v[132:135],0
buffer_load_dwordx4 v1,s[36:39],s66 offen offset:0 sc1 lds
v_mfma_f32_16x16x32_bf16 v[24:27],a[0:3],v[128:131],v[24:27]
s_lshr_b32 s73,s73,1
ds_read_b128 v[160:163],v2 offset:8192
v_fma_f32 v32,-v64,v16,v32
v_mfma_f32_16x16x32_bf16 v[24:27],a[16:19],v[136:139],v[24:27]
s_lshr_b32 s72,s66,5
ds_read_b128 v[164:167],v2 offset:9216
v_fma_f32 v33,-v64,v17,v33
v_mfma_f32_16x16x32_bf16 v[24:27],a[24:27],v[140:143],v[24:27]
s_lshr_b32 s71,s62,2
v_fma_f32 v34,-v64,v18,v34
v_fma_f32 v35,-v64,v19,v35
v_mul_f32 v40,v32,v32
v_mul_f32 v41,v33,v33
v_mul_f32 v42,v34,v34
v_mul_f32 v43,v35,v35
v_fma_f32 v40,v66,v40,v65
v_mfma_f32_16x16x32_bf16 v[28:31],a[28:31],v[140:143],v[28:31]
buffer_load_dwordx4 v1,s[36:39],s66 offen offset:1024 sc1 lds
v_mfma_f32_16x16x32_bf16 v[28:31],a[20:23],v[136:139],v[28:31]
s_waitcnt lgkmcnt(2)
ds_read_b128 v[168:171],v2 offset:10240
v_mul_f32 v40,v32,v40
v_mfma_f32_16x16x32_bf16 v[28:31],a[36:39],v[144:147],v[28:31]
ds_read_b128 v[172:175],v2 offset:11264
v_fma_f32 v41,v66,v41,v65
v_mfma_f32_16x16x32_bf16 v[28:31],a[44:47],v[148:151],v[28:31]
v_exp_f32 v40,v40
v_mul_f32 v41,v33,v41
v_exp_f32 v41,v41
v_fma_f32 v42,v66,v42,v65
v_mfma_f32_16x16x32_bf16 v[24:27],a[40:43],v[148:151],v[24:27]
buffer_load_dwordx4 v1,s[36:39],s66 offen offset:2048 sc1 lds
v_mfma_f32_16x16x32_bf16 v[24:27],a[32:35],v[144:147],v[24:27]
ds_read_b128 v[176:179],v2 offset:12288
v_mul_f32 v42,v34,v42
v_mfma_f32_16x16x32_bf16 v[24:27],a[48:51],v[152:155],v[24:27]
ds_read_b128 v[180:183],v2 offset:13312
v_fma_f32 v40,v40,v67,v67
v_mfma_f32_16x16x32_bf16 v[24:27],a[56:59],v[156:159],v[24:27]
v_exp_f32 v42,v42
v_rcp_f32 v40,v40
v_fma_f32 v41,v41,v67,v67
v_fma_f32 v43,v66,v43,v65
v_mfma_f32_16x16x32_bf16 v[28:31],a[60:63],v[156:159],v[28:31]
buffer_load_dwordx4 v1,s[36:39],s66 offen offset:3072 sc1 lds
v_mfma_f32_16x16x32_bf16 v[28:31],a[52:55],v[152:155],v[28:31]
s_waitcnt lgkmcnt(2)
ds_read_b128 v[184:187],v2 offset:14336
s_add_u32 m0,s71,s88
v_mfma_f32_16x16x32_bf16 v[28:31],a[68:71],v[160:163],v[28:31]
s_xor_b32 s87,s64,65536
ds_read_b128 v[188:191],v2 offset:15360
buffer_load_dword v9,s[36:39],s72 offen offset:0 sc1 lds
s_waitcnt vmcnt(10)
v_mfma_f32_16x16x32_bf16 v[28:31],a[76:79],v[164:167],v[28:31]
s_barrier
v_mfma_f32_16x16x32_bf16 v[24:27],a[72:75],v[164:167],v[24:27]
s_cmp_ge_u32 s66,s59
ds_read_b128 v[128:131],v2 offset:16384
ds_read_b32 v56,v8 offset:0
v_mfma_f32_16x16x32_bf16 v[24:27],a[64:67],v[160:163],v[24:27]
s_cselect_b32 s86,s49,s50
v_rcp_f32 v41,v41
v_mfma_f32_16x16x32_bf16 v[24:27],a[80:83],v[168:171],v[24:27]
s_cselect_b32 s87,s87,s63
ds_read_b128 v[132:135],v2 offset:17408
v_mul_f32 v43,v35,v43
v_mfma_f32_16x16x32_bf16 v[24:27],a[88:91],v[172:175],v[24:27]
s_mov_b32 s62,s87
v_exp_f32 v43,v43
v_fma_f32 v42,v42,v67,v67
v_rcp_f32 v42,v42
v_mul_f32 v32,v32,v40
v_mfma_f32_16x16x32_bf16 v[28:31],a[92:95],v[172:175],v[28:31]
s_mov_b32 m0,s62
ds_read_b128 v[136:139],v2 offset:18432
v_fma_f32 v43,v43,v67,v67
v_mfma_f32_16x16x32_bf16 v[28:31],a[84:87],v[168:171],v[28:31]
s_waitcnt lgkmcnt(4)
v_rcp_f32 v43,v43
v_mfma_f32_16x16x32_bf16 v[28:31],a[100:103],v[176:179],v[28:31]
s_nop 0
s_waitcnt lgkmcnt(2)
ds_read_b128 v[140:143],v2 offset:19456
v_mul_f32 v33,v33,v41
v_mfma_f32_16x16x32_bf16 v[28:31],a[108:111],v[180:183],v[28:31]
s_barrier
v_mfma_f32_16x16x32_bf16 v[24:27],a[104:107],v[180:183],v[24:27]
ds_read_b128 v[144:147],v2 offset:20480
ds_read_b32 v13,v7 offset:0
v_mfma_f32_16x16x32_bf16 v[24:27],a[96:99],v[176:179],v[24:27]
v_add_f32_dpp v56,v56,v56 quad_perm:[1,0,3,2] row_mask:0xf bank_mask:0xf
v_mul_f32 v34,v34,v42
v_mfma_f32_16x16x32_bf16 v[24:27],a[112:115],v[184:187],v[24:27]
ds_read_b128 v[148:151],v2 offset:21504
v_add_f32_dpp v56,v56,v56 quad_perm:[2,3,0,1] row_mask:0xf bank_mask:0xf
v_mfma_f32_16x16x32_bf16 v[24:27],a[120:123],v[188:191],v[24:27]
s_nop 0
v_add_f32_dpp v56,v56,v56 row_half_mirror row_mask:0xf bank_mask:0xf
v_mul_f32 v35,v35,v43
v_cvt_pk_bf16_f32 v48,v32,v33
v_cvt_pk_bf16_f32 v49,v34,v35
v_fma_f32 v36,-v64,v20,v36
v_fma_f32 v37,-v64,v21,v37
v_mfma_f32_16x16x32_bf16 v[28:31],a[124:127],v[188:191],v[28:31]
s_waitcnt lgkmcnt(1)
ds_write_b32 v10,v56 offset:0
v_mfma_f32_16x16x32_bf16 v[28:31],a[116:119],v[184:187],v[28:31]
ds_read_b128 v[152:155],v2 offset:22528
v_cmp_eq_u32 vcc,s60,v13
v_fma_f32 v38,-v64,v22,v38
v_fma_f32 v39,-v64,v23,v39
v_mul_f32 v44,v36,v36
v_mfma_f32_16x16x32_bf16 v[28:31],a[132:135],v[128:131],v[28:31]
s_cmp_eq_u32 vcc_lo,-1
ds_read_b128 v[156:159],v2 offset:23552
s_cselect_b32 s67,s86,s59
v_mfma_f32_16x16x32_bf16 v[28:31],a[140:143],v[132:135],v[28:31]
buffer_load_dwordx4 v0,s[36:39],s67 offen offset:0 sc1 lds
v_mul_f32 v45,v37,v37
v_mul_f32 v46,v38,v38
v_mul_f32 v47,v39,v39
v_fma_f32 v44,v66,v44,v65
v_mul_f32 v44,v36,v44
v_mfma_f32_16x16x32_bf16 v[24:27],a[136:139],v[132:135],v[24:27]
s_xor_b32 s63,s63,65536
ds_read_b128 v[160:163],v2 offset:24576
v_fma_f32 v45,v66,v45,v65
v_mfma_f32_16x16x32_bf16 v[24:27],a[128:131],v[128:131],v[24:27]
s_add_u32 s53,s52,s55
v_exp_f32 v44,v44
v_mfma_f32_16x16x32_bf16 v[24:27],a[144:147],v[136:139],v[24:27]
s_cmp_eq_u32 s53,s56
ds_read_b128 v[164:167],v2 offset:25600
v_mul_f32 v45,v37,v45
v_mfma_f32_16x16x32_bf16 v[24:27],a[152:155],v[140:143],v[24:27]
buffer_load_dwordx4 v0,s[36:39],s67 offen offset:1024 sc1 lds
v_exp_f32 v45,v45
v_fma_f32 v44,v44,v67,v67
v_fma_f32 v46,v66,v46,v65
v_mfma_f32_16x16x32_bf16 v[28:31],a[156:159],v[140:143],v[28:31]
s_cselect_b32 s53,s57,s53
ds_read_b128 v[168:171],v2 offset:26624
v_mul_f32 v46,v38,v46
v_mfma_f32_16x16x32_bf16 v[28:31],a[148:151],v[136:139],v[28:31]
s_waitcnt lgkmcnt(3)
v_exp_f32 v46,v46
v_mfma_f32_16x16x32_bf16 v[28:31],a[164:167],v[144:147],v[28:31]
s_cmp_eq_u32 s53,s58
ds_read_b128 v[172:175],v2 offset:27648
v_fma_f32 v45,v45,v67,v67
v_mfma_f32_16x16x32_bf16 v[28:31],a[172:175],v[148:151],v[28:31]
buffer_load_dwordx4 v0,s[36:39],s67 offen offset:2048 sc1 lds
v_rcp_f32 v44,v44
v_rcp_f32 v45,v45
v_mfma_f32_16x16x32_bf16 v[24:27],a[168:171],v[148:151],v[24:27]
s_cselect_b32 s53,s59,s53
ds_read_b128 v[176:179],v2 offset:28672
v_fma_f32 v46,v46,v67,v67
v_mfma_f32_16x16x32_bf16 v[24:27],a[160:163],v[144:147],v[24:27]
s_cmp_ge_u32 s66,s59
v_rcp_f32 v46,v46
v_mfma_f32_16x16x32_bf16 v[24:27],a[176:179],v[152:155],v[24:27]
ds_read_b128 v[180:183],v2 offset:29696
v_fma_f32 v47,v66,v47,v65
v_mfma_f32_16x16x32_bf16 v[24:27],a[184:187],v[156:159],v[24:27]
buffer_load_dwordx4 v0,s[36:39],s67 offen offset:3072 sc1 lds
v_mul_f32 v47,v39,v47
v_exp_f32 v47,v47
v_mul_f32 v36,v36,v44
v_mfma_f32_16x16x32_bf16 v[28:31],a[188:191],v[156:159],v[28:31]
ds_read_b128 v[184:187],v2 offset:30720
v_fma_f32 v47,v47,v67,v67
v_mfma_f32_16x16x32_bf16 v[28:31],a[180:183],v[152:155],v[28:31]
s_waitcnt lgkmcnt(3)
v_rcp_f32 v47,v47
v_mfma_f32_16x16x32_bf16 v[28:31],a[196:199],v[160:163],v[28:31]
s_waitcnt vmcnt(9)
ds_read_b128 v[188:191],v2 offset:31744
v_mul_f32 v37,v37,v45
v_mfma_f32_16x16x32_bf16 v[28:31],a[204:207],v[164:167],v[28:31]
s_barrier
v_mfma_f32_16x16x32_bf16 v[24:27],a[200:203],v[164:167],v[24:27]
ds_read_b128 v[128:131],v2 offset:32768
ds_read_b64 v[60:61],v11 offset:0
v_mfma_f32_16x16x32_bf16 v[24:27],a[192:195],v[160:163],v[24:27])"
R"(
v_mul_f32 v38,v38,v46
v_mul_f32 v39,v39,v47
v_mfma_f32_16x16x32_bf16 v[24:27],a[208:211],v[168:171],v[24:27]
v_cvt_pk_bf16_f32 v50,v36,v37
v_cvt_pk_bf16_f32 v51,v38,v39
v_mfma_f32_16x16x32_bf16 v[24:27],a[216:219],v[172:175],v[24:27]
ds_read_b128 v[132:135],v2 offset:33792
buffer_store_dwordx4 v[48:51],v3,s[36:39],s73 offen offset:0 sc1
v_mfma_f32_16x16x32_bf16 v[28:31],a[220:223],v[172:175],v[28:31]
s_waitcnt lgkmcnt(1)
ds_read_b128 v[136:139],v2 offset:34816
v_mul_f32 v64,s75,v60
v_mfma_f32_16x16x32_bf16 v[28:31],a[212:215],v[168:171],v[28:31]
v_fma_f32 v61,v61,s75,v12
v_fma_f32 v61,-v64,v64,v61
v_mfma_f32_16x16x32_bf16 v[28:31],a[228:231],v[176:179],v[28:31]
ds_read_b128 v[140:143],v2 offset:35840
v_max_f32 v61,v61,v12
v_mfma_f32_16x16x32_bf16 v[28:31],a[236:239],v[180:183],v[28:31]
v_rsq_f32 v60,v61
v_mfma_f32_16x16x32_bf16 v[24:27],a[232:235],v[180:183],v[24:27]
ds_read_b128 v[144:147],v2 offset:36864
v_mul_f32 v65,s76,v60
v_mfma_f32_16x16x32_bf16 v[24:27],a[224:227],v[176:179],v[24:27]
v_mul_f32 v66,v60,v60
v_mfma_f32_16x16x32_bf16 v[24:27],a[240:243],v[184:187],v[24:27]
ds_read_b128 v[148:151],v2 offset:37888
v_mfma_f32_16x16x32_bf16 v[24:27],a[248:251],v[188:191],v[24:27]
v_mfma_f32_16x16x32_bf16 v[28:31],a[252:255],v[188:191],v[28:31]
s_waitcnt lgkmcnt(2)
ds_read_b128 v[152:155],v2 offset:38912
v_mfma_f32_16x16x32_bf16 v[28:31],a[244:247],v[184:187],v[28:31]
s_cbranch_scc1 Lg3o_slow1_%=
Lg3o_d1w1_%=:
v_mfma_f32_16x16x32_bf16 v[36:39],a[4:7],v[128:131],0
s_add_u32 m0,s62,16384
ds_read_b128 v[156:159],v2 offset:39936
v_mul_f32 v67,v61,v60
v_mfma_f32_16x16x32_bf16 v[36:39],a[12:15],v[132:135],v[36:39]
s_mul_i32 s73,s48,7
v_mul_f32 v66,s77,v66
v_mul_f32 v66,v66,v65
v_mfma_f32_16x16x32_bf16 v[32:35],a[8:11],v[132:135],0
buffer_load_dwordx4 v1,s[36:39],s67 offen offset:0 sc1 lds
v_mfma_f32_16x16x32_bf16 v[32:35],a[0:3],v[128:131],v[32:35]
s_lshr_b32 s73,s73,1
ds_read_b128 v[160:163],v2 offset:40960
v_fma_f32 v24,-v64,v16,v24
v_mfma_f32_16x16x32_bf16 v[32:35],a[16:19],v[136:139],v[32:35]
s_lshr_b32 s72,s67,5
ds_read_b128 v[164:167],v2 offset:41984
v_fma_f32 v25,-v64,v17,v25
v_mfma_f32_16x16x32_bf16 v[32:35],a[24:27],v[140:143],v[32:35]
s_lshr_b32 s71,s62,2
v_fma_f32 v26,-v64,v18,v26
v_fma_f32 v27,-v64,v19,v27
v_mul_f32 v40,v24,v24
v_mul_f32 v41,v25,v25
v_mul_f32 v42,v26,v26
v_mul_f32 v43,v27,v27
v_fma_f32 v40,v66,v40,v65
v_mfma_f32_16x16x32_bf16 v[36:39],a[28:31],v[140:143],v[36:39]
buffer_load_dwordx4 v1,s[36:39],s67 offen offset:1024 sc1 lds
v_mfma_f32_16x16x32_bf16 v[36:39],a[20:23],v[136:139],v[36:39]
s_waitcnt lgkmcnt(2)
ds_read_b128 v[168:171],v2 offset:43008
v_mul_f32 v40,v24,v40
v_mfma_f32_16x16x32_bf16 v[36:39],a[36:39],v[144:147],v[36:39]
s_mov_b64 s[44:45],s[46:47]
ds_read_b128 v[172:175],v2 offset:44032
v_fma_f32 v41,v66,v41,v65
v_mfma_f32_16x16x32_bf16 v[36:39],a[44:47],v[148:151],v[36:39]
s_mov_b64 s[46:47],s[48:49]
v_exp_f32 v40,v40
v_mul_f32 v41,v25,v41
v_exp_f32 v41,v41
v_fma_f32 v42,v66,v42,v65
v_mfma_f32_16x16x32_bf16 v[32:35],a[40:43],v[148:151],v[32:35]
buffer_load_dwordx4 v1,s[36:39],s67 offen offset:2048 sc1 lds
v_mfma_f32_16x16x32_bf16 v[32:35],a[32:35],v[144:147],v[32:35]
s_mov_b64 s[48:49],s[50:51]
ds_read_b128 v[176:179],v2 offset:45056
v_mul_f32 v42,v26,v42
v_mfma_f32_16x16x32_bf16 v[32:35],a[48:51],v[152:155],v[32:35]
ds_read_b128 v[180:183],v2 offset:46080
v_fma_f32 v40,v40,v67,v67
v_mfma_f32_16x16x32_bf16 v[32:35],a[56:59],v[156:159],v[32:35]
v_exp_f32 v42,v42
v_rcp_f32 v40,v40
v_fma_f32 v41,v41,v67,v67
v_fma_f32 v43,v66,v43,v65
v_mfma_f32_16x16x32_bf16 v[36:39],a[60:63],v[156:159],v[36:39]
buffer_load_dwordx4 v1,s[36:39],s67 offen offset:3072 sc1 lds
v_mfma_f32_16x16x32_bf16 v[36:39],a[52:55],v[152:155],v[36:39]
s_waitcnt lgkmcnt(2)
ds_read_b128 v[184:187],v2 offset:47104
s_add_u32 m0,s71,s88
v_mfma_f32_16x16x32_bf16 v[36:39],a[68:71],v[160:163],v[36:39]
s_xor_b32 s87,s63,65536
ds_read_b128 v[188:191],v2 offset:48128
buffer_load_dword v9,s[36:39],s72 offen offset:0 sc1 lds
s_waitcnt vmcnt(10)
v_mfma_f32_16x16x32_bf16 v[36:39],a[76:79],v[164:167],v[36:39]
s_barrier
v_mfma_f32_16x16x32_bf16 v[32:35],a[72:75],v[164:167],v[32:35]
s_cmp_ge_u32 s67,s59
ds_read_b128 v[128:131],v2 offset:49152
ds_read_b32 v56,v8 offset:8192
v_mfma_f32_16x16x32_bf16 v[32:35],a[64:67],v[160:163],v[32:35]
s_cselect_b32 s86,s50,s51
v_rcp_f32 v41,v41
v_mfma_f32_16x16x32_bf16 v[32:35],a[80:83],v[168:171],v[32:35]
s_cselect_b32 s87,s87,s64
ds_read_b128 v[132:135],v2 offset:50176
v_mul_f32 v43,v27,v43
v_mfma_f32_16x16x32_bf16 v[32:35],a[88:91],v[172:175],v[32:35]
s_mov_b32 s62,s87
v_exp_f32 v43,v43
v_fma_f32 v42,v42,v67,v67
v_rcp_f32 v42,v42
v_mul_f32 v24,v24,v40
v_mul_f32 v25,v25,v41
v_mfma_f32_16x16x32_bf16 v[36:39],a[92:95],v[172:175],v[36:39]
s_mov_b32 m0,s62
ds_read_b128 v[136:139],v2 offset:51200
v_fma_f32 v43,v43,v67,v67
v_mfma_f32_16x16x32_bf16 v[36:39],a[84:87],v[168:171],v[36:39]
s_waitcnt lgkmcnt(4)
v_rcp_f32 v43,v43
v_mfma_f32_16x16x32_bf16 v[36:39],a[100:103],v[176:179],v[36:39]
s_nop 0
s_waitcnt lgkmcnt(2)
ds_read_b128 v[140:143],v2 offset:52224
v_mul_f32 v26,v26,v42
v_mfma_f32_16x16x32_bf16 v[36:39],a[108:111],v[180:183],v[36:39]
s_barrier
v_mfma_f32_16x16x32_bf16 v[32:35],a[104:107],v[180:183],v[32:35]
s_mov_b64 s[50:51],s[52:53]
ds_read_b128 v[144:147],v2 offset:53248
ds_read_b32 v13,v7 offset:256
v_mfma_f32_16x16x32_bf16 v[32:35],a[96:99],v[176:179],v[32:35]
v_add_f32_dpp v56,v56,v56 quad_perm:[1,0,3,2] row_mask:0xf bank_mask:0xf
v_mul_f32 v27,v27,v43
v_mfma_f32_16x16x32_bf16 v[32:35],a[112:115],v[184:187],v[32:35]
ds_read_b128 v[148:151],v2 offset:54272
v_add_f32_dpp v56,v56,v56 quad_perm:[2,3,0,1] row_mask:0xf bank_mask:0xf
v_mfma_f32_16x16x32_bf16 v[32:35],a[120:123],v[188:191],v[32:35]
s_nop 0
v_add_f32_dpp v56,v56,v56 row_half_mirror row_mask:0xf bank_mask:0xf
v_cvt_pk_bf16_f32 v48,v24,v25
v_cvt_pk_bf16_f32 v49,v26,v27
v_fma_f32 v28,-v64,v20,v28
v_fma_f32 v29,-v64,v21,v29
v_fma_f32 v30,-v64,v22,v30
v_mfma_f32_16x16x32_bf16 v[36:39],a[124:127],v[188:191],v[36:39]
s_waitcnt lgkmcnt(1)
ds_write_b32 v10,v56 offset:128
v_mfma_f32_16x16x32_bf16 v[36:39],a[116:119],v[184:187],v[36:39]
ds_read_b128 v[152:155],v2 offset:55296
v_cmp_eq_u32 vcc,s60,v13
v_fma_f32 v31,-v64,v23,v31
v_mul_f32 v44,v28,v28
v_mul_f32 v45,v29,v29
v_mfma_f32_16x16x32_bf16 v[36:39],a[132:135],v[128:131],v[36:39]
s_cmp_eq_u32 vcc_lo,-1
ds_read_b128 v[156:159],v2 offset:56320
s_cselect_b32 s66,s86,s59
v_mfma_f32_16x16x32_bf16 v[36:39],a[140:143],v[132:135],v[36:39]
buffer_load_dwordx4 v0,s[36:39],s66 offen offset:0 sc1 lds
v_mul_f32 v46,v30,v30
v_mul_f32 v47,v31,v31
v_fma_f32 v44,v66,v44,v65
v_mul_f32 v44,v28,v44
v_mfma_f32_16x16x32_bf16 v[32:35],a[136:139],v[132:135],v[32:35]
s_xor_b32 s64,s64,65536
ds_read_b128 v[160:163],v2 offset:57344
v_fma_f32 v45,v66,v45,v65
v_mfma_f32_16x16x32_bf16 v[32:35],a[128:131],v[128:131],v[32:35]
s_add_u32 s74,s53,s55
v_exp_f32 v44,v44
v_mfma_f32_16x16x32_bf16 v[32:35],a[144:147],v[136:139],v[32:35]
s_cmp_eq_u32 s74,s56
ds_read_b128 v[164:167],v2 offset:58368
v_mul_f32 v45,v29,v45
v_mfma_f32_16x16x32_bf16 v[32:35],a[152:155],v[140:143],v[32:35]
buffer_load_dwordx4 v0,s[36:39],s66 offen offset:1024 sc1 lds
v_exp_f32 v45,v45
v_fma_f32 v44,v44,v67,v67
v_rcp_f32 v44,v44
v_mfma_f32_16x16x32_bf16 v[36:39],a[156:159],v[140:143],v[36:39]
s_cselect_b32 s74,s57,s74
ds_read_b128 v[168:171],v2 offset:59392
v_fma_f32 v45,v45,v67,v67
v_mfma_f32_16x16x32_bf16 v[36:39],a[148:151],v[136:139],v[36:39]
s_waitcnt lgkmcnt(3)
v_rcp_f32 v45,v45
v_mfma_f32_16x16x32_bf16 v[36:39],a[164:167],v[144:147],v[36:39]
s_cmp_eq_u32 s74,s58
ds_read_b128 v[172:175],v2 offset:60416
v_fma_f32 v46,v66,v46,v65)"
R"(
v_mfma_f32_16x16x32_bf16 v[36:39],a[172:175],v[148:151],v[36:39]
buffer_load_dwordx4 v0,s[36:39],s66 offen offset:2048 sc1 lds
v_mul_f32 v46,v30,v46
v_exp_f32 v46,v46
v_fma_f32 v47,v66,v47,v65
v_mfma_f32_16x16x32_bf16 v[32:35],a[168:171],v[148:151],v[32:35]
s_cselect_b32 s74,s59,s74
ds_read_b128 v[176:179],v2 offset:61440
v_mul_f32 v47,v31,v47
v_mfma_f32_16x16x32_bf16 v[32:35],a[160:163],v[144:147],v[32:35]
s_mov_b32 s52,s74
v_exp_f32 v47,v47
v_mfma_f32_16x16x32_bf16 v[32:35],a[176:179],v[152:155],v[32:35]
s_cmp_lt_u32 s67,s59
ds_read_b128 v[180:183],v2 offset:62464
v_fma_f32 v46,v46,v67,v67
v_mfma_f32_16x16x32_bf16 v[32:35],a[184:187],v[156:159],v[32:35]
buffer_load_dwordx4 v0,s[36:39],s66 offen offset:3072 sc1 lds
v_rcp_f32 v46,v46
v_fma_f32 v47,v47,v67,v67
v_mul_f32 v28,v28,v44
v_mfma_f32_16x16x32_bf16 v[36:39],a[188:191],v[156:159],v[36:39]
ds_read_b128 v[184:187],v2 offset:63488
v_mul_f32 v29,v29,v45
v_mfma_f32_16x16x32_bf16 v[36:39],a[180:183],v[152:155],v[36:39]
s_waitcnt lgkmcnt(3)
v_rcp_f32 v47,v47
v_mfma_f32_16x16x32_bf16 v[36:39],a[196:199],v[160:163],v[36:39]
s_waitcnt vmcnt(9)
ds_read_b128 v[188:191],v2 offset:64512
v_xor_b32 v2,65536,v2
v_mfma_f32_16x16x32_bf16 v[36:39],a[204:207],v[164:167],v[36:39]
s_barrier
v_mfma_f32_16x16x32_bf16 v[32:35],a[200:203],v[164:167],v[32:35]
ds_read_b128 v[128:131],v2 offset:0
ds_read_b64 v[60:61],v11 offset:128
v_mfma_f32_16x16x32_bf16 v[32:35],a[192:195],v[160:163],v[32:35]
v_mul_f32 v30,v30,v46
v_mul_f32 v31,v31,v47
v_mfma_f32_16x16x32_bf16 v[32:35],a[208:211],v[168:171],v[32:35]
v_cvt_pk_bf16_f32 v50,v28,v29
v_cvt_pk_bf16_f32 v51,v30,v31
v_mfma_f32_16x16x32_bf16 v[32:35],a[216:219],v[172:175],v[32:35]
ds_read_b128 v[132:135],v2 offset:1024
buffer_store_dwordx4 v[48:51],v3,s[36:39],s73 offen offset:0 sc1
v_xor_b32 v8,16384,v8
v_mfma_f32_16x16x32_bf16 v[36:39],a[220:223],v[172:175],v[36:39]
s_waitcnt lgkmcnt(1)
ds_read_b128 v[136:139],v2 offset:2048
v_mul_f32 v64,s75,v60
v_mfma_f32_16x16x32_bf16 v[36:39],a[212:215],v[168:171],v[36:39]
v_fma_f32 v61,v61,s75,v12
v_fma_f32 v61,-v64,v64,v61
v_mfma_f32_16x16x32_bf16 v[36:39],a[228:231],v[176:179],v[36:39]
ds_read_b128 v[140:143],v2 offset:3072
v_max_f32 v61,v61,v12
v_mfma_f32_16x16x32_bf16 v[36:39],a[236:239],v[180:183],v[36:39]
v_rsq_f32 v60,v61
v_mfma_f32_16x16x32_bf16 v[32:35],a[232:235],v[180:183],v[32:35]
ds_read_b128 v[144:147],v2 offset:4096
v_mul_f32 v65,s76,v60
v_mfma_f32_16x16x32_bf16 v[32:35],a[224:227],v[176:179],v[32:35]
v_mul_f32 v66,v60,v60
v_mfma_f32_16x16x32_bf16 v[32:35],a[240:243],v[184:187],v[32:35]
ds_read_b128 v[148:151],v2 offset:5120
v_mfma_f32_16x16x32_bf16 v[32:35],a[248:251],v[188:191],v[32:35]
v_mfma_f32_16x16x32_bf16 v[36:39],a[252:255],v[188:191],v[36:39]
s_waitcnt lgkmcnt(2)
ds_read_b128 v[152:155],v2 offset:6144
v_mfma_f32_16x16x32_bf16 v[36:39],a[244:247],v[184:187],v[36:39]
s_cbranch_scc1 Lg3o_d0w1_%=
s_branch Lg3o_slow0_%=
Lg3o_d0w2_%=:
v_mfma_f32_16x16x32_bf16 v[28:31],a[4:7],v[128:131],0
s_add_u32 m0,s62,16384
ds_read_b128 v[156:159],v2 offset:7168
v_mul_f32 v67,v61,v60
v_mfma_f32_16x16x32_bf16 v[28:31],a[12:15],v[132:135],v[28:31]
s_mul_i32 s73,s47,7
v_mul_f32 v66,s77,v66
v_mul_f32 v66,v66,v65
v_mfma_f32_16x16x32_bf16 v[24:27],a[8:11],v[132:135],0
s_lshr_b32 s73,s73,1
ds_read_b128 v[160:163],v2 offset:8192
s_lshr_b32 s72,s66,5
v_mfma_f32_16x16x32_bf16 v[24:27],a[0:3],v[128:131],v[24:27]
buffer_load_dwordx4 v1,s[36:39],s66 offen offset:0 sc1 lds
v_mfma_f32_16x16x32_bf16 v[24:27],a[16:19],v[136:139],v[24:27]
s_lshr_b32 s71,s62,2
ds_read_b128 v[164:167],v2 offset:9216
v_fma_f32 v32,-v64,v16,v32
v_mfma_f32_16x16x32_bf16 v[24:27],a[24:27],v[140:143],v[24:27]
v_fma_f32 v33,-v64,v17,v33
v_fma_f32 v34,-v64,v18,v34
v_fma_f32 v35,-v64,v19,v35
v_mul_f32 v40,v32,v32
v_mul_f32 v41,v33,v33
v_mul_f32 v42,v34,v34
v_mfma_f32_16x16x32_bf16 v[28:31],a[28:31],v[140:143],v[28:31]
ds_read_b128 v[168:171],v2 offset:10240
v_mul_f32 v43,v35,v35
v_mfma_f32_16x16x32_bf16 v[28:31],a[20:23],v[136:139],v[28:31]
s_waitcnt lgkmcnt(3)
buffer_load_dwordx4 v1,s[36:39],s66 offen offset:1024 sc1 lds
v_mfma_f32_16x16x32_bf16 v[28:31],a[36:39],v[144:147],v[28:31]
ds_read_b128 v[172:175],v2 offset:11264
v_fma_f32 v40,v66,v40,v65
v_mfma_f32_16x16x32_bf16 v[28:31],a[44:47],v[148:151],v[28:31]
v_mul_f32 v40,v32,v40
v_exp_f32 v40,v40
v_fma_f32 v41,v66,v41,v65
v_mul_f32 v41,v33,v41
v_fma_f32 v42,v66,v42,v65
v_mfma_f32_16x16x32_bf16 v[24:27],a[40:43],v[148:151],v[24:27]
ds_read_b128 v[176:179],v2 offset:12288
v_mul_f32 v42,v34,v42
v_mfma_f32_16x16x32_bf16 v[24:27],a[32:35],v[144:147],v[24:27]
buffer_load_dwordx4 v1,s[36:39],s66 offen offset:2048 sc1 lds
v_mfma_f32_16x16x32_bf16 v[24:27],a[48:51],v[152:155],v[24:27]
ds_read_b128 v[180:183],v2 offset:13312
v_fma_f32 v43,v66,v43,v65
v_mfma_f32_16x16x32_bf16 v[24:27],a[56:59],v[156:159],v[24:27]
v_exp_f32 v41,v41
v_exp_f32 v42,v42
v_mul_f32 v43,v35,v43
v_mfma_f32_16x16x32_bf16 v[28:31],a[60:63],v[156:159],v[28:31]
ds_read_b128 v[184:187],v2 offset:14336
v_fma_f32 v40,v40,v67,v67
v_mfma_f32_16x16x32_bf16 v[28:31],a[52:55],v[152:155],v[28:31]
s_waitcnt lgkmcnt(3)
buffer_load_dwordx4 v1,s[36:39],s66 offen offset:3072 sc1 lds
s_add_u32 m0,s71,s88
v_mfma_f32_16x16x32_bf16 v[28:31],a[68:71],v[160:163],v[28:31]
s_xor_b32 s87,s64,65536
ds_read_b128 v[188:191],v2 offset:15360
buffer_load_dword v9,s[36:39],s72 offen offset:0 sc1 lds
s_waitcnt vmcnt(10)
v_mfma_f32_16x16x32_bf16 v[28:31],a[76:79],v[164:167],v[28:31]
s_barrier
v_mfma_f32_16x16x32_bf16 v[24:27],a[72:75],v[164:167],v[24:27]
s_cmp_ge_u32 s66,s59
ds_read_b128 v[128:131],v2 offset:16384
ds_read_b32 v56,v8 offset:0
v_mfma_f32_16x16x32_bf16 v[24:27],a[64:67],v[160:163],v[24:27]
s_cselect_b32 s86,s49,s50
v_exp_f32 v43,v43
v_mfma_f32_16x16x32_bf16 v[24:27],a[80:83],v[168:171],v[24:27]
s_cselect_b32 s87,s87,s63
ds_read_b128 v[132:135],v2 offset:17408
v_fma_f32 v41,v41,v67,v67
v_mfma_f32_16x16x32_bf16 v[24:27],a[88:91],v[172:175],v[24:27]
s_mov_b32 s62,s87
v_rcp_f32 v40,v40
v_rcp_f32 v41,v41
v_fma_f32 v42,v42,v67,v67
v_fma_f32 v43,v43,v67,v67
v_mfma_f32_16x16x32_bf16 v[28:31],a[92:95],v[172:175],v[28:31]
s_mov_b32 m0,s62
ds_read_b128 v[136:139],v2 offset:18432
v_mul_f32 v32,v32,v40
v_mfma_f32_16x16x32_bf16 v[28:31],a[84:87],v[168:171],v[28:31]
s_waitcnt lgkmcnt(4)
v_rcp_f32 v42,v42
v_mfma_f32_16x16x32_bf16 v[28:31],a[100:103],v[176:179],v[28:31]
s_nop 0
s_waitcnt lgkmcnt(2)
ds_read_b128 v[140:143],v2 offset:19456
v_mul_f32 v33,v33,v41
v_mfma_f32_16x16x32_bf16 v[28:31],a[108:111],v[180:183],v[28:31]
s_barrier
v_mfma_f32_16x16x32_bf16 v[24:27],a[104:107],v[180:183],v[24:27]
ds_read_b128 v[144:147],v2 offset:20480
ds_read_b32 v13,v7 offset:0
v_mfma_f32_16x16x32_bf16 v[24:27],a[96:99],v[176:179],v[24:27]
v_rcp_f32 v43,v43
v_mfma_f32_16x16x32_bf16 v[24:27],a[112:115],v[184:187],v[24:27]
ds_read_b128 v[148:151],v2 offset:21504
v_add_f32_dpp v56,v56,v56 quad_perm:[1,0,3,2] row_mask:0xf bank_mask:0xf
v_mfma_f32_16x16x32_bf16 v[24:27],a[120:123],v[188:191],v[24:27]
s_nop 0
v_add_f32_dpp v56,v56,v56 quad_perm:[2,3,0,1] row_mask:0xf bank_mask:0xf
v_mul_f32 v34,v34,v42
v_mul_f32 v35,v35,v43
v_cvt_pk_bf16_f32 v48,v32,v33
v_cvt_pk_bf16_f32 v49,v34,v35
v_mfma_f32_16x16x32_bf16 v[28:31],a[124:127],v[188:191],v[28:31]
s_waitcnt lgkmcnt(1)
ds_read_b128 v[152:155],v2 offset:22528
v_add_f32_dpp v56,v56,v56 row_half_mirror row_mask:0xf bank_mask:0xf
v_mfma_f32_16x16x32_bf16 v[28:31],a[116:119],v[184:187],v[28:31]
ds_write_b32 v10,v56 offset:0
v_mfma_f32_16x16x32_bf16 v[28:31],a[132:135],v[128:131],v[28:31]
v_cmp_eq_u32 vcc,s60,v13
v_fma_f32 v36,-v64,v20,v36
v_fma_f32 v37,-v64,v21,v37
v_fma_f32 v38,-v64,v22,v38
v_mfma_f32_16x16x32_bf16 v[28:31],a[140:143],v[132:135],v[28:31]
s_cmp_eq_u32 vcc_lo,-1
ds_read_b128 v[156:159],v2 offset:23552
v_fma_f32 v39,-v64,v23,v39)"
R"(
v_mul_f32 v44,v36,v36
v_mul_f32 v45,v37,v37
v_mul_f32 v46,v38,v38
s_cselect_b32 s67,s86,s59
v_mfma_f32_16x16x32_bf16 v[24:27],a[136:139],v[132:135],v[24:27]
buffer_load_dwordx4 v0,s[36:39],s67 offen offset:0 sc1 lds
v_mfma_f32_16x16x32_bf16 v[24:27],a[128:131],v[128:131],v[24:27]
s_xor_b32 s63,s63,65536
ds_read_b128 v[160:163],v2 offset:24576
v_mul_f32 v47,v39,v39
v_mfma_f32_16x16x32_bf16 v[24:27],a[144:147],v[136:139],v[24:27]
s_add_u32 s53,s52,s55
ds_read_b128 v[164:167],v2 offset:25600
v_fma_f32 v44,v66,v44,v65
v_mfma_f32_16x16x32_bf16 v[24:27],a[152:155],v[140:143],v[24:27]
s_cmp_eq_u32 s53,s56
v_mul_f32 v44,v36,v44
v_exp_f32 v44,v44
v_fma_f32 v45,v66,v45,v65
v_mul_f32 v45,v37,v45
v_fma_f32 v46,v66,v46,v65
v_mfma_f32_16x16x32_bf16 v[28:31],a[156:159],v[140:143],v[28:31]
buffer_load_dwordx4 v0,s[36:39],s67 offen offset:1024 sc1 lds
v_mfma_f32_16x16x32_bf16 v[28:31],a[148:151],v[136:139],v[28:31]
s_waitcnt lgkmcnt(2)
ds_read_b128 v[168:171],v2 offset:26624
v_mul_f32 v46,v38,v46
v_mfma_f32_16x16x32_bf16 v[28:31],a[164:167],v[144:147],v[28:31]
s_cselect_b32 s53,s57,s53
ds_read_b128 v[172:175],v2 offset:27648
v_fma_f32 v47,v66,v47,v65
v_mfma_f32_16x16x32_bf16 v[28:31],a[172:175],v[148:151],v[28:31]
s_cmp_eq_u32 s53,s58
v_exp_f32 v45,v45
v_exp_f32 v46,v46
v_mul_f32 v47,v39,v47
v_mfma_f32_16x16x32_bf16 v[24:27],a[168:171],v[148:151],v[24:27]
buffer_load_dwordx4 v0,s[36:39],s67 offen offset:2048 sc1 lds
v_mfma_f32_16x16x32_bf16 v[24:27],a[160:163],v[144:147],v[24:27]
s_cselect_b32 s53,s59,s53
ds_read_b128 v[176:179],v2 offset:28672
v_fma_f32 v44,v44,v67,v67
v_mfma_f32_16x16x32_bf16 v[24:27],a[176:179],v[152:155],v[24:27]
s_cmp_ge_u32 s66,s59
ds_read_b128 v[180:183],v2 offset:29696
v_fma_f32 v45,v45,v67,v67
v_mfma_f32_16x16x32_bf16 v[24:27],a[184:187],v[156:159],v[24:27]
v_exp_f32 v47,v47
v_rcp_f32 v44,v44
v_rcp_f32 v45,v45
v_mfma_f32_16x16x32_bf16 v[28:31],a[188:191],v[156:159],v[28:31]
buffer_load_dwordx4 v0,s[36:39],s67 offen offset:3072 sc1 lds
v_mfma_f32_16x16x32_bf16 v[28:31],a[180:183],v[152:155],v[28:31]
s_waitcnt lgkmcnt(2)
ds_read_b128 v[184:187],v2 offset:30720
v_fma_f32 v46,v46,v67,v67
v_mfma_f32_16x16x32_bf16 v[28:31],a[196:199],v[160:163],v[28:31]
s_waitcnt vmcnt(9)
ds_read_b128 v[188:191],v2 offset:31744
v_fma_f32 v47,v47,v67,v67
v_mfma_f32_16x16x32_bf16 v[28:31],a[204:207],v[164:167],v[28:31]
s_barrier
v_mfma_f32_16x16x32_bf16 v[24:27],a[200:203],v[164:167],v[24:27]
ds_read_b128 v[128:131],v2 offset:32768
ds_read_b64 v[60:61],v11 offset:0
v_mfma_f32_16x16x32_bf16 v[24:27],a[192:195],v[160:163],v[24:27]
v_rcp_f32 v46,v46
v_mfma_f32_16x16x32_bf16 v[24:27],a[208:211],v[168:171],v[24:27]
v_rcp_f32 v47,v47
v_mfma_f32_16x16x32_bf16 v[24:27],a[216:219],v[172:175],v[24:27]
ds_read_b128 v[132:135],v2 offset:33792
v_mul_f32 v36,v36,v44
v_mul_f32 v37,v37,v45
v_mul_f32 v38,v38,v46
v_mul_f32 v39,v39,v47
v_cvt_pk_bf16_f32 v50,v36,v37
v_cvt_pk_bf16_f32 v51,v38,v39
v_mfma_f32_16x16x32_bf16 v[28:31],a[220:223],v[172:175],v[28:31]
s_waitcnt lgkmcnt(1)
buffer_store_dwordx4 v[48:51],v3,s[36:39],s73 offen offset:0 sc1
ds_read_b128 v[136:139],v2 offset:34816
v_mul_f32 v64,s75,v60
v_fma_f32 v61,v61,s75,v12
v_mfma_f32_16x16x32_bf16 v[28:31],a[212:215],v[168:171],v[28:31]
v_fma_f32 v61,-v64,v64,v61
v_max_f32 v61,v61,v12
v_mfma_f32_16x16x32_bf16 v[28:31],a[228:231],v[176:179],v[28:31]
ds_read_b128 v[140:143],v2 offset:35840
v_mfma_f32_16x16x32_bf16 v[28:31],a[236:239],v[180:183],v[28:31]
v_rsq_f32 v60,v61
v_mfma_f32_16x16x32_bf16 v[24:27],a[232:235],v[180:183],v[24:27]
ds_read_b128 v[144:147],v2 offset:36864
v_mul_f32 v65,s76,v60
v_mfma_f32_16x16x32_bf16 v[24:27],a[224:227],v[176:179],v[24:27]
v_mul_f32 v66,v60,v60
v_mfma_f32_16x16x32_bf16 v[24:27],a[240:243],v[184:187],v[24:27]
ds_read_b128 v[148:151],v2 offset:37888
v_mfma_f32_16x16x32_bf16 v[24:27],a[248:251],v[188:191],v[24:27]
v_mfma_f32_16x16x32_bf16 v[28:31],a[252:255],v[188:191],v[28:31]
s_waitcnt lgkmcnt(2)
ds_read_b128 v[152:155],v2 offset:38912
v_mfma_f32_16x16x32_bf16 v[28:31],a[244:247],v[184:187],v[28:31]
s_cbranch_scc1 Lg3o_slow1_%=
Lg3o_d1w2_%=:
v_mfma_f32_16x16x32_bf16 v[36:39],a[4:7],v[128:131],0
s_add_u32 m0,s62,16384
ds_read_b128 v[156:159],v2 offset:39936
v_mul_f32 v67,v61,v60
v_mfma_f32_16x16x32_bf16 v[36:39],a[12:15],v[132:135],v[36:39]
s_mul_i32 s73,s48,7
v_mul_f32 v66,s77,v66
v_mul_f32 v66,v66,v65
v_mfma_f32_16x16x32_bf16 v[32:35],a[8:11],v[132:135],0
s_lshr_b32 s73,s73,1
ds_read_b128 v[160:163],v2 offset:40960
s_lshr_b32 s72,s67,5
v_mfma_f32_16x16x32_bf16 v[32:35],a[0:3],v[128:131],v[32:35]
buffer_load_dwordx4 v1,s[36:39],s67 offen offset:0 sc1 lds
v_mfma_f32_16x16x32_bf16 v[32:35],a[16:19],v[136:139],v[32:35]
s_lshr_b32 s71,s62,2
ds_read_b128 v[164:167],v2 offset:41984
v_fma_f32 v24,-v64,v16,v24
v_mfma_f32_16x16x32_bf16 v[32:35],a[24:27],v[140:143],v[32:35]
s_mov_b64 s[44:45],s[46:47]
v_fma_f32 v25,-v64,v17,v25
v_fma_f32 v26,-v64,v18,v26
v_fma_f32 v27,-v64,v19,v27
v_mul_f32 v40,v24,v24
v_mul_f32 v41,v25,v25
v_mul_f32 v42,v26,v26
v_mfma_f32_16x16x32_bf16 v[36:39],a[28:31],v[140:143],v[36:39]
s_mov_b64 s[46:47],s[48:49]
ds_read_b128 v[168:171],v2 offset:43008
v_mul_f32 v43,v27,v27
v_mfma_f32_16x16x32_bf16 v[36:39],a[20:23],v[136:139],v[36:39]
s_waitcnt lgkmcnt(3)
buffer_load_dwordx4 v1,s[36:39],s67 offen offset:1024 sc1 lds
v_mfma_f32_16x16x32_bf16 v[36:39],a[36:39],v[144:147],v[36:39]
s_mov_b64 s[48:49],s[50:51]
ds_read_b128 v[172:175],v2 offset:44032
v_fma_f32 v40,v66,v40,v65
v_mfma_f32_16x16x32_bf16 v[36:39],a[44:47],v[148:151],v[36:39]
v_mul_f32 v40,v24,v40
v_exp_f32 v40,v40
v_fma_f32 v41,v66,v41,v65
v_mul_f32 v41,v25,v41
v_fma_f32 v42,v66,v42,v65
v_mfma_f32_16x16x32_bf16 v[32:35],a[40:43],v[148:151],v[32:35]
ds_read_b128 v[176:179],v2 offset:45056
v_mul_f32 v42,v26,v42
v_mfma_f32_16x16x32_bf16 v[32:35],a[32:35],v[144:147],v[32:35]
buffer_load_dwordx4 v1,s[36:39],s67 offen offset:2048 sc1 lds
v_mfma_f32_16x16x32_bf16 v[32:35],a[48:51],v[152:155],v[32:35]
ds_read_b128 v[180:183],v2 offset:46080
v_fma_f32 v43,v66,v43,v65
v_mfma_f32_16x16x32_bf16 v[32:35],a[56:59],v[156:159],v[32:35]
v_exp_f32 v41,v41
v_exp_f32 v42,v42
v_mul_f32 v43,v27,v43
v_fma_f32 v40,v40,v67,v67
v_mfma_f32_16x16x32_bf16 v[36:39],a[60:63],v[156:159],v[36:39]
ds_read_b128 v[184:187],v2 offset:47104
v_fma_f32 v41,v41,v67,v67
v_mfma_f32_16x16x32_bf16 v[36:39],a[52:55],v[152:155],v[36:39]
s_waitcnt lgkmcnt(3)
buffer_load_dwordx4 v1,s[36:39],s67 offen offset:3072 sc1 lds
s_add_u32 m0,s71,s88
v_mfma_f32_16x16x32_bf16 v[36:39],a[68:71],v[160:163],v[36:39]
s_xor_b32 s87,s63,65536
ds_read_b128 v[188:191],v2 offset:48128
buffer_load_dword v9,s[36:39],s72 offen offset:0 sc1 lds
s_waitcnt vmcnt(10)
v_mfma_f32_16x16x32_bf16 v[36:39],a[76:79],v[164:167],v[36:39]
s_barrier
v_mfma_f32_16x16x32_bf16 v[32:35],a[72:75],v[164:167],v[32:35]
s_cmp_ge_u32 s67,s59
ds_read_b128 v[128:131],v2 offset:49152
ds_read_b32 v56,v8 offset:8192
v_mfma_f32_16x16x32_bf16 v[32:35],a[64:67],v[160:163],v[32:35]
s_cselect_b32 s86,s50,s51
v_exp_f32 v43,v43
v_mfma_f32_16x16x32_bf16 v[32:35],a[80:83],v[168:171],v[32:35]
s_cselect_b32 s87,s87,s64
ds_read_b128 v[132:135],v2 offset:50176
v_fma_f32 v42,v42,v67,v67
v_mfma_f32_16x16x32_bf16 v[32:35],a[88:91],v[172:175],v[32:35]
s_mov_b32 s62,s87
v_rcp_f32 v40,v40
v_rcp_f32 v41,v41
v_fma_f32 v43,v43,v67,v67
v_mfma_f32_16x16x32_bf16 v[36:39],a[92:95],v[172:175],v[36:39]
s_mov_b32 m0,s62
ds_read_b128 v[136:139],v2 offset:51200
v_mul_f32 v24,v24,v40
v_mfma_f32_16x16x32_bf16 v[36:39],a[84:87],v[168:171],v[36:39]
s_waitcnt lgkmcnt(4)
v_rcp_f32 v42,v42
v_mfma_f32_16x16x32_bf16 v[36:39],a[100:103],v[176:179],v[36:39]
s_nop 0
s_waitcnt lgkmcnt(2))"
R"(
ds_read_b128 v[140:143],v2 offset:52224
v_mul_f32 v25,v25,v41
v_mfma_f32_16x16x32_bf16 v[36:39],a[108:111],v[180:183],v[36:39]
s_barrier
v_mfma_f32_16x16x32_bf16 v[32:35],a[104:107],v[180:183],v[32:35]
s_mov_b64 s[50:51],s[52:53]
ds_read_b128 v[144:147],v2 offset:53248
ds_read_b32 v13,v7 offset:256
v_mfma_f32_16x16x32_bf16 v[32:35],a[96:99],v[176:179],v[32:35]
v_rcp_f32 v43,v43
v_mfma_f32_16x16x32_bf16 v[32:35],a[112:115],v[184:187],v[32:35]
ds_read_b128 v[148:151],v2 offset:54272
v_add_f32_dpp v56,v56,v56 quad_perm:[1,0,3,2] row_mask:0xf bank_mask:0xf
v_mfma_f32_16x16x32_bf16 v[32:35],a[120:123],v[188:191],v[32:35]
s_nop 0
v_add_f32_dpp v56,v56,v56 quad_perm:[2,3,0,1] row_mask:0xf bank_mask:0xf
v_mul_f32 v26,v26,v42
v_mul_f32 v27,v27,v43
v_cvt_pk_bf16_f32 v48,v24,v25
v_cvt_pk_bf16_f32 v49,v26,v27
v_fma_f32 v28,-v64,v20,v28
v_mfma_f32_16x16x32_bf16 v[36:39],a[124:127],v[188:191],v[36:39]
s_waitcnt lgkmcnt(1)
ds_read_b128 v[152:155],v2 offset:55296
v_add_f32_dpp v56,v56,v56 row_half_mirror row_mask:0xf bank_mask:0xf
v_mfma_f32_16x16x32_bf16 v[36:39],a[116:119],v[184:187],v[36:39]
ds_write_b32 v10,v56 offset:128
v_mfma_f32_16x16x32_bf16 v[36:39],a[132:135],v[128:131],v[36:39]
v_cmp_eq_u32 vcc,s60,v13
v_fma_f32 v29,-v64,v21,v29
v_fma_f32 v30,-v64,v22,v30
v_fma_f32 v31,-v64,v23,v31
v_mfma_f32_16x16x32_bf16 v[36:39],a[140:143],v[132:135],v[36:39]
s_cmp_eq_u32 vcc_lo,-1
ds_read_b128 v[156:159],v2 offset:56320
v_mul_f32 v44,v28,v28
v_mul_f32 v45,v29,v29
v_mul_f32 v46,v30,v30
v_mul_f32 v47,v31,v31
s_cselect_b32 s66,s86,s59
v_mfma_f32_16x16x32_bf16 v[32:35],a[136:139],v[132:135],v[32:35]
buffer_load_dwordx4 v0,s[36:39],s66 offen offset:0 sc1 lds
v_mfma_f32_16x16x32_bf16 v[32:35],a[128:131],v[128:131],v[32:35]
s_xor_b32 s64,s64,65536
ds_read_b128 v[160:163],v2 offset:57344
v_fma_f32 v44,v66,v44,v65
v_mfma_f32_16x16x32_bf16 v[32:35],a[144:147],v[136:139],v[32:35]
s_add_u32 s74,s53,s55
ds_read_b128 v[164:167],v2 offset:58368
v_mul_f32 v44,v28,v44
v_mfma_f32_16x16x32_bf16 v[32:35],a[152:155],v[140:143],v[32:35]
s_cmp_eq_u32 s74,s56
v_exp_f32 v44,v44
v_fma_f32 v45,v66,v45,v65
v_mul_f32 v45,v29,v45
v_fma_f32 v46,v66,v46,v65
v_mfma_f32_16x16x32_bf16 v[36:39],a[156:159],v[140:143],v[36:39]
buffer_load_dwordx4 v0,s[36:39],s66 offen offset:1024 sc1 lds
v_mfma_f32_16x16x32_bf16 v[36:39],a[148:151],v[136:139],v[36:39]
s_waitcnt lgkmcnt(2)
ds_read_b128 v[168:171],v2 offset:59392
v_mul_f32 v46,v30,v46
v_mfma_f32_16x16x32_bf16 v[36:39],a[164:167],v[144:147],v[36:39]
s_cselect_b32 s74,s57,s74
ds_read_b128 v[172:175],v2 offset:60416
v_fma_f32 v47,v66,v47,v65
v_mfma_f32_16x16x32_bf16 v[36:39],a[172:175],v[148:151],v[36:39]
s_cmp_eq_u32 s74,s58
v_exp_f32 v45,v45
v_exp_f32 v46,v46
v_mul_f32 v47,v31,v47
v_fma_f32 v44,v44,v67,v67
v_mfma_f32_16x16x32_bf16 v[32:35],a[168:171],v[148:151],v[32:35]
buffer_load_dwordx4 v0,s[36:39],s66 offen offset:2048 sc1 lds
v_mfma_f32_16x16x32_bf16 v[32:35],a[160:163],v[144:147],v[32:35]
s_cselect_b32 s74,s59,s74
ds_read_b128 v[176:179],v2 offset:61440
v_fma_f32 v45,v45,v67,v67
v_mfma_f32_16x16x32_bf16 v[32:35],a[176:179],v[152:155],v[32:35]
s_mov_b32 s52,s74
ds_read_b128 v[180:183],v2 offset:62464
v_fma_f32 v46,v46,v67,v67
v_mfma_f32_16x16x32_bf16 v[32:35],a[184:187],v[156:159],v[32:35]
s_cmp_lt_u32 s67,s59
v_exp_f32 v47,v47
v_rcp_f32 v44,v44
v_rcp_f32 v45,v45
v_mfma_f32_16x16x32_bf16 v[36:39],a[188:191],v[156:159],v[36:39]
buffer_load_dwordx4 v0,s[36:39],s66 offen offset:3072 sc1 lds
v_mfma_f32_16x16x32_bf16 v[36:39],a[180:183],v[152:155],v[36:39]
s_waitcnt lgkmcnt(2)
ds_read_b128 v[184:187],v2 offset:63488
v_fma_f32 v47,v47,v67,v67
v_mfma_f32_16x16x32_bf16 v[36:39],a[196:199],v[160:163],v[36:39]
s_waitcnt vmcnt(9)
ds_read_b128 v[188:191],v2 offset:64512
v_xor_b32 v2,65536,v2
v_mfma_f32_16x16x32_bf16 v[36:39],a[204:207],v[164:167],v[36:39]
s_barrier
v_mfma_f32_16x16x32_bf16 v[32:35],a[200:203],v[164:167],v[32:35]
ds_read_b128 v[128:131],v2 offset:0
ds_read_b64 v[60:61],v11 offset:128
v_mfma_f32_16x16x32_bf16 v[32:35],a[192:195],v[160:163],v[32:35]
v_rcp_f32 v46,v46
v_mfma_f32_16x16x32_bf16 v[32:35],a[208:211],v[168:171],v[32:35]
v_rcp_f32 v47,v47
v_mfma_f32_16x16x32_bf16 v[32:35],a[216:219],v[172:175],v[32:35]
ds_read_b128 v[132:135],v2 offset:1024
v_mul_f32 v28,v28,v44
v_mul_f32 v29,v29,v45
v_mul_f32 v30,v30,v46
v_mul_f32 v31,v31,v47
v_cvt_pk_bf16_f32 v50,v28,v29
v_cvt_pk_bf16_f32 v51,v30,v31
v_mfma_f32_16x16x32_bf16 v[36:39],a[220:223],v[172:175],v[36:39]
s_waitcnt lgkmcnt(1)
buffer_store_dwordx4 v[48:51],v3,s[36:39],s73 offen offset:0 sc1
ds_read_b128 v[136:139],v2 offset:2048
v_xor_b32 v8,16384,v8
v_mul_f32 v64,s75,v60
v_mfma_f32_16x16x32_bf16 v[36:39],a[212:215],v[168:171],v[36:39]
v_fma_f32 v61,v61,s75,v12
v_fma_f32 v61,-v64,v64,v61
v_mfma_f32_16x16x32_bf16 v[36:39],a[228:231],v[176:179],v[36:39]
ds_read_b128 v[140:143],v2 offset:3072
v_max_f32 v61,v61,v12
v_mfma_f32_16x16x32_bf16 v[36:39],a[236:239],v[180:183],v[36:39]
v_rsq_f32 v60,v61
v_mfma_f32_16x16x32_bf16 v[32:35],a[232:235],v[180:183],v[32:35]
ds_read_b128 v[144:147],v2 offset:4096
v_mul_f32 v65,s76,v60
v_mfma_f32_16x16x32_bf16 v[32:35],a[224:227],v[176:179],v[32:35]
v_mul_f32 v66,v60,v60
v_mfma_f32_16x16x32_bf16 v[32:35],a[240:243],v[184:187],v[32:35]
ds_read_b128 v[148:151],v2 offset:5120
v_mfma_f32_16x16x32_bf16 v[32:35],a[248:251],v[188:191],v[32:35]
v_mfma_f32_16x16x32_bf16 v[36:39],a[252:255],v[188:191],v[36:39]
s_waitcnt lgkmcnt(2)
ds_read_b128 v[152:155],v2 offset:6144
v_mfma_f32_16x16x32_bf16 v[36:39],a[244:247],v[184:187],v[36:39]
s_cbranch_scc1 Lg3o_d0w2_%=
s_branch Lg3o_slow0_%=
Lg3o_d0w3_%=:
v_mfma_f32_16x16x32_bf16 v[28:31],a[4:7],v[128:131],0
s_add_u32 m0,s62,16384
ds_read_b128 v[156:159],v2 offset:7168
v_mul_f32 v67,v61,v60
v_mfma_f32_16x16x32_bf16 v[28:31],a[12:15],v[132:135],v[28:31]
s_mul_i32 s73,s47,7
v_mul_f32 v66,s77,v66
v_mul_f32 v66,v66,v65
v_mfma_f32_16x16x32_bf16 v[24:27],a[8:11],v[132:135],0
s_lshr_b32 s73,s73,1
ds_read_b128 v[160:163],v2 offset:8192
s_lshr_b32 s72,s66,5
v_mfma_f32_16x16x32_bf16 v[24:27],a[0:3],v[128:131],v[24:27]
s_lshr_b32 s71,s62,2
v_fma_f32 v32,-v64,v16,v32
v_fma_f32 v33,-v64,v17,v33
v_mfma_f32_16x16x32_bf16 v[24:27],a[16:19],v[136:139],v[24:27]
buffer_load_dwordx4 v1,s[36:39],s66 offen offset:0 sc1 lds
v_mfma_f32_16x16x32_bf16 v[24:27],a[24:27],v[140:143],v[24:27]
s_mul_i32 s70,s46,5
ds_read_b128 v[164:167],v2 offset:9216
v_fma_f32 v34,-v64,v18,v34
v_fma_f32 v35,-v64,v19,v35
v_mul_f32 v40,v32,v32
v_mul_f32 v41,v33,v33
v_mul_f32 v42,v34,v34
v_mfma_f32_16x16x32_bf16 v[28:31],a[28:31],v[140:143],v[28:31]
ds_read_b128 v[168:171],v2 offset:10240
v_mul_f32 v43,v35,v35
v_mfma_f32_16x16x32_bf16 v[28:31],a[20:23],v[136:139],v[28:31]
s_waitcnt lgkmcnt(3)
v_fma_f32 v40,v66,v40,v65
v_mul_f32 v40,v32,v40
v_mfma_f32_16x16x32_bf16 v[28:31],a[36:39],v[144:147],v[28:31]
buffer_load_dwordx4 v1,s[36:39],s66 offen offset:1024 sc1 lds
v_mfma_f32_16x16x32_bf16 v[28:31],a[44:47],v[148:151],v[28:31]
ds_read_b128 v[172:175],v2 offset:11264
v_exp_f32 v40,v40
v_fma_f32 v41,v66,v41,v65
v_mul_f32 v41,v33,v41
v_fma_f32 v42,v66,v42,v65
v_mfma_f32_16x16x32_bf16 v[24:27],a[40:43],v[148:151],v[24:27]
ds_read_b128 v[176:179],v2 offset:12288
v_mul_f32 v42,v34,v42
v_mfma_f32_16x16x32_bf16 v[24:27],a[32:35],v[144:147],v[24:27]
v_exp_f32 v41,v41
v_mfma_f32_16x16x32_bf16 v[24:27],a[48:51],v[152:155],v[24:27]
buffer_load_dwordx4 v1,s[36:39],s66 offen offset:2048 sc1 lds
v_mfma_f32_16x16x32_bf16 v[24:27],a[56:59],v[156:159],v[24:27]
ds_read_b128 v[180:183],v2 offset:13312
v_exp_f32 v42,v42
v_fma_f32 v40,v40,v67,v67
v_rcp_f32 v40,v40
v_mfma_f32_16x16x32_bf16 v[28:31],a[60:63],v[156:159],v[28:31]
ds_read_b128 v[184:187],v2 offset:14336
v_fma_f32 v41,v41,v67,v67
v_mfma_f32_16x16x32_bf16 v[28:31],a[52:55],v[152:155],v[28:31])"
R"(
s_waitcnt lgkmcnt(3)
v_rcp_f32 v41,v41
v_mfma_f32_16x16x32_bf16 v[28:31],a[68:71],v[160:163],v[28:31]
s_waitcnt vmcnt(9)
ds_read_b128 v[188:191],v2 offset:15360
v_fma_f32 v42,v42,v67,v67
v_mfma_f32_16x16x32_bf16 v[28:31],a[76:79],v[164:167],v[28:31]
s_barrier
v_mfma_f32_16x16x32_bf16 v[24:27],a[72:75],v[164:167],v[24:27]
buffer_load_dwordx4 v1,s[36:39],s66 offen offset:3072 sc1 lds
v_mfma_f32_16x16x32_bf16 v[24:27],a[64:67],v[160:163],v[24:27]
s_add_u32 m0,s71,s88
ds_read_b128 v[128:131],v2 offset:16384
ds_read_b32 v56,v8 offset:0
v_mfma_f32_16x16x32_bf16 v[24:27],a[80:83],v[168:171],v[24:27]
buffer_load_dword v9,s[36:39],s72 offen offset:0 sc1 lds
v_mfma_f32_16x16x32_bf16 v[24:27],a[88:91],v[172:175],v[24:27]
s_xor_b32 s87,s64,65536
ds_read_b128 v[132:135],v2 offset:17408
v_rcp_f32 v42,v42
v_fma_f32 v43,v66,v43,v65
v_mul_f32 v43,v35,v43
v_mul_f32 v32,v32,v40
v_mfma_f32_16x16x32_bf16 v[28:31],a[92:95],v[172:175],v[28:31]
s_cmp_ge_u32 s66,s59
ds_read_b128 v[136:139],v2 offset:18432
v_mul_f32 v33,v33,v41
v_mfma_f32_16x16x32_bf16 v[28:31],a[84:87],v[168:171],v[28:31]
s_waitcnt lgkmcnt(4)
v_exp_f32 v43,v43
v_mfma_f32_16x16x32_bf16 v[28:31],a[100:103],v[176:179],v[28:31]
s_nop 0
ds_read_b128 v[140:143],v2 offset:19456
v_fma_f32 v43,v43,v67,v67
v_mfma_f32_16x16x32_bf16 v[28:31],a[108:111],v[180:183],v[28:31]
s_barrier
v_mfma_f32_16x16x32_bf16 v[24:27],a[104:107],v[180:183],v[24:27]
s_waitcnt lgkmcnt(3)
ds_read_b128 v[144:147],v2 offset:20480
ds_read_b32 v13,v7 offset:0
v_mfma_f32_16x16x32_bf16 v[24:27],a[96:99],v[176:179],v[24:27]
s_cselect_b32 s86,s49,s50
v_rcp_f32 v43,v43
v_mfma_f32_16x16x32_bf16 v[24:27],a[112:115],v[184:187],v[24:27]
s_cselect_b32 s87,s87,s63
ds_read_b128 v[148:151],v2 offset:21504
v_add_f32_dpp v56,v56,v56 quad_perm:[1,0,3,2] row_mask:0xf bank_mask:0xf
v_mfma_f32_16x16x32_bf16 v[24:27],a[120:123],v[188:191],v[24:27]
s_mov_b32 s62,s87
v_add_f32_dpp v56,v56,v56 quad_perm:[2,3,0,1] row_mask:0xf bank_mask:0xf
v_mul_f32 v34,v34,v42
v_mul_f32 v35,v35,v43
v_cvt_pk_bf16_f32 v48,v32,v33
v_cvt_pk_bf16_f32 v49,v34,v35
v_fma_f32 v36,-v64,v20,v36
v_mfma_f32_16x16x32_bf16 v[28:31],a[124:127],v[188:191],v[28:31]
s_waitcnt lgkmcnt(1)
ds_read_b128 v[152:155],v2 offset:22528
v_add_f32_dpp v56,v56,v56 row_half_mirror row_mask:0xf bank_mask:0xf
v_mfma_f32_16x16x32_bf16 v[28:31],a[116:119],v[184:187],v[28:31]
ds_write_b32 v10,v56 offset:0
v_mfma_f32_16x16x32_bf16 v[28:31],a[132:135],v[128:131],v[28:31]
s_mov_b32 m0,s62
ds_read_b128 v[156:159],v2 offset:23552
v_fma_f32 v37,-v64,v21,v37
v_mfma_f32_16x16x32_bf16 v[28:31],a[140:143],v[132:135],v[28:31]
v_cmp_eq_u32 vcc,s60,v13
v_fma_f32 v38,-v64,v22,v38
v_fma_f32 v39,-v64,v23,v39
v_mul_f32 v44,v36,v36
v_mul_f32 v45,v37,v37
v_mul_f32 v46,v38,v38
v_mfma_f32_16x16x32_bf16 v[24:27],a[136:139],v[132:135],v[24:27]
s_cmp_eq_u32 vcc_lo,-1
ds_read_b128 v[160:163],v2 offset:24576
s_cselect_b32 s67,s86,s59
v_mfma_f32_16x16x32_bf16 v[24:27],a[128:131],v[128:131],v[24:27]
buffer_load_dwordx4 v0,s[36:39],s67 offen offset:0 sc1 lds
v_mfma_f32_16x16x32_bf16 v[24:27],a[144:147],v[136:139],v[24:27]
s_xor_b32 s63,s63,65536
ds_read_b128 v[164:167],v2 offset:25600
v_mul_f32 v47,v39,v39
v_mfma_f32_16x16x32_bf16 v[24:27],a[152:155],v[140:143],v[24:27]
s_add_u32 s53,s52,s55
v_fma_f32 v44,v66,v44,v65
v_mul_f32 v44,v36,v44
v_exp_f32 v44,v44
v_fma_f32 v45,v66,v45,v65
v_mul_f32 v45,v37,v45
v_mfma_f32_16x16x32_bf16 v[28:31],a[156:159],v[140:143],v[28:31]
s_cmp_eq_u32 s53,s56
ds_read_b128 v[168:171],v2 offset:26624
v_fma_f32 v44,v44,v67,v67
v_mfma_f32_16x16x32_bf16 v[28:31],a[148:151],v[136:139],v[28:31]
s_waitcnt lgkmcnt(3)
buffer_load_dwordx4 v0,s[36:39],s67 offen offset:1024 sc1 lds
v_mfma_f32_16x16x32_bf16 v[28:31],a[164:167],v[144:147],v[28:31]
s_cselect_b32 s53,s57,s53
ds_read_b128 v[172:175],v2 offset:27648
v_fma_f32 v46,v66,v46,v65
v_mfma_f32_16x16x32_bf16 v[28:31],a[172:175],v[148:151],v[28:31]
s_cmp_eq_u32 s53,s58
v_exp_f32 v45,v45
v_rcp_f32 v44,v44
v_mul_f32 v46,v38,v46
v_fma_f32 v47,v66,v47,v65
v_mfma_f32_16x16x32_bf16 v[24:27],a[168:171],v[148:151],v[24:27]
s_cselect_b32 s53,s59,s53
ds_read_b128 v[176:179],v2 offset:28672
v_mul_f32 v47,v39,v47
v_mfma_f32_16x16x32_bf16 v[24:27],a[160:163],v[144:147],v[24:27]
buffer_load_dwordx4 v0,s[36:39],s67 offen offset:2048 sc1 lds
v_mfma_f32_16x16x32_bf16 v[24:27],a[176:179],v[152:155],v[24:27]
s_lshr_b32 s70,s70,6
ds_read_b128 v[180:183],v2 offset:29696
v_fma_f32 v45,v45,v67,v67
v_mfma_f32_16x16x32_bf16 v[24:27],a[184:187],v[156:159],v[24:27]
s_cmp_ge_u32 s66,s59
v_exp_f32 v46,v46
v_exp_f32 v47,v47
v_rcp_f32 v45,v45
v_mfma_f32_16x16x32_bf16 v[28:31],a[188:191],v[156:159],v[28:31]
ds_read_b128 v[184:187],v2 offset:30720
v_fma_f32 v46,v46,v67,v67
v_mfma_f32_16x16x32_bf16 v[28:31],a[180:183],v[152:155],v[28:31]
s_waitcnt lgkmcnt(3)
buffer_load_dwordx4 v0,s[36:39],s67 offen offset:3072 sc1 lds
v_mfma_f32_16x16x32_bf16 v[28:31],a[196:199],v[160:163],v[28:31]
s_waitcnt vmcnt(9)
ds_read_b128 v[188:191],v2 offset:31744
v_fma_f32 v47,v47,v67,v67
v_mfma_f32_16x16x32_bf16 v[28:31],a[204:207],v[164:167],v[28:31]
s_barrier
v_mfma_f32_16x16x32_bf16 v[24:27],a[200:203],v[164:167],v[24:27]
ds_read_b128 v[128:131],v2 offset:32768
ds_read_b64 v[60:61],v11 offset:0
v_mfma_f32_16x16x32_bf16 v[24:27],a[192:195],v[160:163],v[24:27]
v_rcp_f32 v46,v46
v_mfma_f32_16x16x32_bf16 v[24:27],a[208:211],v[168:171],v[24:27]
ds_read_b128 v[132:135],v2 offset:33792
v_mul_f32 v36,v36,v44
v_mfma_f32_16x16x32_bf16 v[24:27],a[216:219],v[172:175],v[24:27]
v_rcp_f32 v47,v47
v_mul_f32 v37,v37,v45
v_mul_f32 v38,v38,v46
v_cvt_pk_bf16_f32 v50,v36,v37
v_mfma_f32_16x16x32_bf16 v[28:31],a[220:223],v[172:175],v[28:31]
s_waitcnt lgkmcnt(1)
v_mul_f32 v39,v39,v47
v_cvt_pk_bf16_f32 v51,v38,v39
v_mfma_f32_16x16x32_bf16 v[28:31],a[212:215],v[168:171],v[28:31]
ds_read_b128 v[136:139],v2 offset:34816
buffer_store_dword v5,v4,s[36:39],s70 offen offset:0 sc1
buffer_store_dwordx4 v[48:51],v3,s[36:39],s73 offen offset:0 sc1
v_mul_f32 v64,s75,v60
v_fma_f32 v61,v61,s75,v12
v_fma_f32 v61,-v64,v64,v61
v_mfma_f32_16x16x32_bf16 v[28:31],a[228:231],v[176:179],v[28:31]
ds_read_b128 v[140:143],v2 offset:35840
v_max_f32 v61,v61,v12
v_mfma_f32_16x16x32_bf16 v[28:31],a[236:239],v[180:183],v[28:31]
v_rsq_f32 v60,v61
v_mfma_f32_16x16x32_bf16 v[24:27],a[232:235],v[180:183],v[24:27]
ds_read_b128 v[144:147],v2 offset:36864
v_mul_f32 v65,s76,v60
v_mfma_f32_16x16x32_bf16 v[24:27],a[224:227],v[176:179],v[24:27]
v_mul_f32 v66,v60,v60
v_mfma_f32_16x16x32_bf16 v[24:27],a[240:243],v[184:187],v[24:27]
ds_read_b128 v[148:151],v2 offset:37888
v_mfma_f32_16x16x32_bf16 v[24:27],a[248:251],v[188:191],v[24:27]
v_mfma_f32_16x16x32_bf16 v[28:31],a[252:255],v[188:191],v[28:31]
s_waitcnt lgkmcnt(2)
ds_read_b128 v[152:155],v2 offset:38912
v_mfma_f32_16x16x32_bf16 v[28:31],a[244:247],v[184:187],v[28:31]
s_cbranch_scc1 Lg3o_slow1_%=
Lg3o_d1w3_%=:
v_mfma_f32_16x16x32_bf16 v[36:39],a[4:7],v[128:131],0
s_add_u32 m0,s62,16384
ds_read_b128 v[156:159],v2 offset:39936
v_mul_f32 v67,v61,v60
v_mfma_f32_16x16x32_bf16 v[36:39],a[12:15],v[132:135],v[36:39]
s_mul_i32 s73,s48,7
v_mul_f32 v66,s77,v66
v_mul_f32 v66,v66,v65
v_mfma_f32_16x16x32_bf16 v[32:35],a[8:11],v[132:135],0
s_lshr_b32 s73,s73,1
ds_read_b128 v[160:163],v2 offset:40960
s_lshr_b32 s72,s67,5
v_mfma_f32_16x16x32_bf16 v[32:35],a[0:3],v[128:131],v[32:35]
s_lshr_b32 s71,s62,2
v_fma_f32 v24,-v64,v16,v24
v_fma_f32 v25,-v64,v17,v25
v_mfma_f32_16x16x32_bf16 v[32:35],a[16:19],v[136:139],v[32:35]
buffer_load_dwordx4 v1,s[36:39],s67 offen offset:0 sc1 lds
v_mfma_f32_16x16x32_bf16 v[32:35],a[24:27],v[140:143],v[32:35]
s_mul_i32 s70,s47,5
ds_read_b128 v[164:167],v2 offset:41984
v_fma_f32 v26,-v64,v18,v26
v_fma_f32 v27,-v64,v19,v27
v_mfma_f32_16x16x32_bf16 v[36:39],a[28:31],v[140:143],v[36:39])"
R"(
s_mov_b64 s[44:45],s[46:47]
ds_read_b128 v[168:171],v2 offset:43008
v_mul_f32 v40,v24,v24
v_mfma_f32_16x16x32_bf16 v[36:39],a[20:23],v[136:139],v[36:39]
s_waitcnt lgkmcnt(3)
v_mul_f32 v41,v25,v25
v_mul_f32 v42,v26,v26
v_mfma_f32_16x16x32_bf16 v[36:39],a[36:39],v[144:147],v[36:39]
buffer_load_dwordx4 v1,s[36:39],s67 offen offset:1024 sc1 lds
v_mfma_f32_16x16x32_bf16 v[36:39],a[44:47],v[148:151],v[36:39]
s_mov_b64 s[46:47],s[48:49]
ds_read_b128 v[172:175],v2 offset:44032
v_mul_f32 v43,v27,v27
v_fma_f32 v40,v66,v40,v65
v_mul_f32 v40,v24,v40
v_mfma_f32_16x16x32_bf16 v[32:35],a[40:43],v[148:151],v[32:35]
s_mov_b64 s[48:49],s[50:51]
ds_read_b128 v[176:179],v2 offset:45056
v_fma_f32 v41,v66,v41,v65
v_mfma_f32_16x16x32_bf16 v[32:35],a[32:35],v[144:147],v[32:35]
v_exp_f32 v40,v40
v_mfma_f32_16x16x32_bf16 v[32:35],a[48:51],v[152:155],v[32:35]
buffer_load_dwordx4 v1,s[36:39],s67 offen offset:2048 sc1 lds
v_mfma_f32_16x16x32_bf16 v[32:35],a[56:59],v[156:159],v[32:35]
ds_read_b128 v[180:183],v2 offset:46080
v_mul_f32 v41,v25,v41
v_exp_f32 v41,v41
v_mfma_f32_16x16x32_bf16 v[36:39],a[60:63],v[156:159],v[36:39]
ds_read_b128 v[184:187],v2 offset:47104
v_fma_f32 v40,v40,v67,v67
v_mfma_f32_16x16x32_bf16 v[36:39],a[52:55],v[152:155],v[36:39]
s_waitcnt lgkmcnt(3)
v_rcp_f32 v40,v40
v_mfma_f32_16x16x32_bf16 v[36:39],a[68:71],v[160:163],v[36:39]
s_waitcnt vmcnt(9)
ds_read_b128 v[188:191],v2 offset:48128
v_fma_f32 v41,v41,v67,v67
v_mfma_f32_16x16x32_bf16 v[36:39],a[76:79],v[164:167],v[36:39]
s_barrier
v_mfma_f32_16x16x32_bf16 v[32:35],a[72:75],v[164:167],v[32:35]
buffer_load_dwordx4 v1,s[36:39],s67 offen offset:3072 sc1 lds
v_mfma_f32_16x16x32_bf16 v[32:35],a[64:67],v[160:163],v[32:35]
s_add_u32 m0,s71,s88
ds_read_b128 v[128:131],v2 offset:49152
ds_read_b32 v56,v8 offset:8192
v_mfma_f32_16x16x32_bf16 v[32:35],a[80:83],v[168:171],v[32:35]
buffer_load_dword v9,s[36:39],s72 offen offset:0 sc1 lds
v_mfma_f32_16x16x32_bf16 v[32:35],a[88:91],v[172:175],v[32:35]
s_xor_b32 s87,s63,65536
ds_read_b128 v[132:135],v2 offset:50176
v_rcp_f32 v41,v41
v_fma_f32 v42,v66,v42,v65
v_mul_f32 v42,v26,v42
v_mfma_f32_16x16x32_bf16 v[36:39],a[92:95],v[172:175],v[36:39]
s_cmp_ge_u32 s67,s59
ds_read_b128 v[136:139],v2 offset:51200
v_fma_f32 v43,v66,v43,v65
v_mfma_f32_16x16x32_bf16 v[36:39],a[84:87],v[168:171],v[36:39]
s_waitcnt lgkmcnt(4)
v_exp_f32 v42,v42
v_mfma_f32_16x16x32_bf16 v[36:39],a[100:103],v[176:179],v[36:39]
s_nop 0
ds_read_b128 v[140:143],v2 offset:52224
v_mul_f32 v43,v27,v43
v_mfma_f32_16x16x32_bf16 v[36:39],a[108:111],v[180:183],v[36:39]
s_barrier
v_mfma_f32_16x16x32_bf16 v[32:35],a[104:107],v[180:183],v[32:35]
s_waitcnt lgkmcnt(3)
ds_read_b128 v[144:147],v2 offset:53248
ds_read_b32 v13,v7 offset:256
v_mfma_f32_16x16x32_bf16 v[32:35],a[96:99],v[176:179],v[32:35]
s_cselect_b32 s86,s50,s51
v_exp_f32 v43,v43
v_mfma_f32_16x16x32_bf16 v[32:35],a[112:115],v[184:187],v[32:35]
s_cselect_b32 s87,s87,s64
ds_read_b128 v[148:151],v2 offset:54272
v_fma_f32 v42,v42,v67,v67
v_mfma_f32_16x16x32_bf16 v[32:35],a[120:123],v[188:191],v[32:35]
s_mov_b32 s62,s87
v_rcp_f32 v42,v42
v_fma_f32 v43,v43,v67,v67
v_rcp_f32 v43,v43
v_mfma_f32_16x16x32_bf16 v[36:39],a[124:127],v[188:191],v[36:39]
s_waitcnt lgkmcnt(1)
ds_read_b128 v[152:155],v2 offset:55296
v_add_f32_dpp v56,v56,v56 quad_perm:[1,0,3,2] row_mask:0xf bank_mask:0xf
v_mfma_f32_16x16x32_bf16 v[36:39],a[116:119],v[184:187],v[36:39]
s_nop 0
v_add_f32_dpp v56,v56,v56 quad_perm:[2,3,0,1] row_mask:0xf bank_mask:0xf
v_mul_f32 v24,v24,v40
v_mfma_f32_16x16x32_bf16 v[36:39],a[132:135],v[128:131],v[36:39]
s_mov_b32 m0,s62
ds_read_b128 v[156:159],v2 offset:56320
v_add_f32_dpp v56,v56,v56 row_half_mirror row_mask:0xf bank_mask:0xf
v_mfma_f32_16x16x32_bf16 v[36:39],a[140:143],v[132:135],v[36:39]
s_mov_b64 s[50:51],s[52:53]
ds_write_b32 v10,v56 offset:128
v_cmp_eq_u32 vcc,s60,v13
v_mul_f32 v25,v25,v41
v_mul_f32 v26,v26,v42
v_mul_f32 v27,v27,v43
v_mfma_f32_16x16x32_bf16 v[32:35],a[136:139],v[132:135],v[32:35]
s_cmp_eq_u32 vcc_lo,-1
ds_read_b128 v[160:163],v2 offset:57344
s_cselect_b32 s66,s86,s59
v_mfma_f32_16x16x32_bf16 v[32:35],a[128:131],v[128:131],v[32:35]
buffer_load_dwordx4 v0,s[36:39],s66 offen offset:0 sc1 lds
v_mfma_f32_16x16x32_bf16 v[32:35],a[144:147],v[136:139],v[32:35]
s_xor_b32 s64,s64,65536
ds_read_b128 v[164:167],v2 offset:58368
v_cvt_pk_bf16_f32 v48,v24,v25
v_mfma_f32_16x16x32_bf16 v[32:35],a[152:155],v[140:143],v[32:35]
s_add_u32 s74,s53,s55
v_cvt_pk_bf16_f32 v49,v26,v27
v_fma_f32 v28,-v64,v20,v28
v_fma_f32 v29,-v64,v21,v29
v_fma_f32 v30,-v64,v22,v30
v_fma_f32 v31,-v64,v23,v31
v_mul_f32 v44,v28,v28
v_mfma_f32_16x16x32_bf16 v[36:39],a[156:159],v[140:143],v[36:39]
s_cmp_eq_u32 s74,s56
ds_read_b128 v[168:171],v2 offset:59392
v_mul_f32 v45,v29,v29
v_mfma_f32_16x16x32_bf16 v[36:39],a[148:151],v[136:139],v[36:39]
s_waitcnt lgkmcnt(4)
buffer_load_dwordx4 v0,s[36:39],s66 offen offset:1024 sc1 lds
v_mfma_f32_16x16x32_bf16 v[36:39],a[164:167],v[144:147],v[36:39]
s_cselect_b32 s74,s57,s74
ds_read_b128 v[172:175],v2 offset:60416
v_mul_f32 v46,v30,v30
v_mfma_f32_16x16x32_bf16 v[36:39],a[172:175],v[148:151],v[36:39]
s_cmp_eq_u32 s74,s58
v_mul_f32 v47,v31,v31
v_fma_f32 v44,v66,v44,v65
v_mul_f32 v44,v28,v44
v_exp_f32 v44,v44
v_fma_f32 v45,v66,v45,v65
v_mfma_f32_16x16x32_bf16 v[32:35],a[168:171],v[148:151],v[32:35]
s_cselect_b32 s74,s59,s74
ds_read_b128 v[176:179],v2 offset:61440
v_mul_f32 v45,v29,v45
v_mfma_f32_16x16x32_bf16 v[32:35],a[160:163],v[144:147],v[32:35]
buffer_load_dwordx4 v0,s[36:39],s66 offen offset:2048 sc1 lds
v_mfma_f32_16x16x32_bf16 v[32:35],a[176:179],v[152:155],v[32:35]
s_lshr_b32 s70,s70,6
ds_read_b128 v[180:183],v2 offset:62464
v_fma_f32 v44,v44,v67,v67
v_mfma_f32_16x16x32_bf16 v[32:35],a[184:187],v[156:159],v[32:35]
s_mov_b32 s52,s74
v_exp_f32 v45,v45
v_rcp_f32 v44,v44
v_fma_f32 v46,v66,v46,v65
v_mul_f32 v46,v30,v46
v_fma_f32 v47,v66,v47,v65
v_mfma_f32_16x16x32_bf16 v[36:39],a[188:191],v[156:159],v[36:39]
s_cmp_lt_u32 s67,s59
ds_read_b128 v[184:187],v2 offset:63488
v_mul_f32 v47,v31,v47
v_mfma_f32_16x16x32_bf16 v[36:39],a[180:183],v[152:155],v[36:39]
s_waitcnt lgkmcnt(3)
buffer_load_dwordx4 v0,s[36:39],s66 offen offset:3072 sc1 lds
v_mfma_f32_16x16x32_bf16 v[36:39],a[196:199],v[160:163],v[36:39]
s_waitcnt vmcnt(9)
ds_read_b128 v[188:191],v2 offset:64512
v_xor_b32 v2,65536,v2
v_mfma_f32_16x16x32_bf16 v[36:39],a[204:207],v[164:167],v[36:39]
s_barrier
v_mfma_f32_16x16x32_bf16 v[32:35],a[200:203],v[164:167],v[32:35]
ds_read_b128 v[128:131],v2 offset:0
ds_read_b64 v[60:61],v11 offset:128
v_mfma_f32_16x16x32_bf16 v[32:35],a[192:195],v[160:163],v[32:35]
v_exp_f32 v46,v46
v_mfma_f32_16x16x32_bf16 v[32:35],a[208:211],v[168:171],v[32:35]
v_exp_f32 v47,v47
v_mfma_f32_16x16x32_bf16 v[32:35],a[216:219],v[172:175],v[32:35]
ds_read_b128 v[132:135],v2 offset:1024
v_fma_f32 v45,v45,v67,v67
v_fma_f32 v46,v46,v67,v67
v_fma_f32 v47,v47,v67,v67
v_rcp_f32 v45,v45
v_rcp_f32 v46,v46
v_rcp_f32 v47,v47
v_mfma_f32_16x16x32_bf16 v[36:39],a[220:223],v[172:175],v[36:39]
s_waitcnt lgkmcnt(1)
v_mul_f32 v28,v28,v44
v_mul_f32 v29,v29,v45
v_mul_f32 v30,v30,v46
v_mul_f32 v31,v31,v47
v_cvt_pk_bf16_f32 v50,v28,v29
v_cvt_pk_bf16_f32 v51,v30,v31
v_mfma_f32_16x16x32_bf16 v[36:39],a[212:215],v[168:171],v[36:39]
ds_read_b128 v[136:139],v2 offset:2048
buffer_store_dword v5,v4,s[36:39],s70 offen offset:0 sc1
buffer_store_dwordx4 v[48:51],v3,s[36:39],s73 offen offset:0 sc1
v_mul_f32 v64,s75,v60
v_xor_b32 v8,16384,v8
v_fma_f32 v61,v61,s75,v12
v_mfma_f32_16x16x32_bf16 v[36:39],a[228:231],v[176:179],v[36:39]
ds_read_b128 v[140:143],v2 offset:3072
v_fma_f32 v61,-v64,v64,v61)"
R"(
v_mfma_f32_16x16x32_bf16 v[36:39],a[236:239],v[180:183],v[36:39]
v_max_f32 v61,v61,v12
v_mfma_f32_16x16x32_bf16 v[32:35],a[232:235],v[180:183],v[32:35]
ds_read_b128 v[144:147],v2 offset:4096
v_mfma_f32_16x16x32_bf16 v[32:35],a[224:227],v[176:179],v[32:35]
v_rsq_f32 v60,v61
v_mfma_f32_16x16x32_bf16 v[32:35],a[240:243],v[184:187],v[32:35]
ds_read_b128 v[148:151],v2 offset:5120
v_mul_f32 v65,s76,v60
v_mfma_f32_16x16x32_bf16 v[32:35],a[248:251],v[188:191],v[32:35]
v_mul_f32 v66,v60,v60
v_mfma_f32_16x16x32_bf16 v[36:39],a[252:255],v[188:191],v[36:39]
s_waitcnt lgkmcnt(2)
ds_read_b128 v[152:155],v2 offset:6144
v_mfma_f32_16x16x32_bf16 v[36:39],a[244:247],v[184:187],v[36:39]
s_cbranch_scc1 Lg3o_d0w3_%=
s_branch Lg3o_slow0_%=
Lg3o_slow0_%=:
s_cmp_lt_u32 s66,s59
s_cbranch_scc1 Lg3o_gs0_%=
Lg3o_slowb0_%=:
s_cmp_eq_u32 s54,1
s_cselect_b32 s68,s49,s48
s_lshr_b32 s68,s68,5
s_add_u32 m0,s78,8192
s_nop 0
buffer_load_dword v6,s[36:39],s68 offen offset:0 sc1 lds
s_mul_i32 s73,s47,7
s_lshr_b32 s73,s73,1
v_mul_f32 v67,v61,v60
v_mul_f32 v66,s77,v66
v_mul_f32 v66,v66,v65
v_fma_f32 v32,-v64,v16,v32
v_fma_f32 v33,-v64,v17,v33
v_fma_f32 v34,-v64,v18,v34
v_fma_f32 v35,-v64,v19,v35
v_mul_f32 v40,v32,v32
v_mul_f32 v41,v33,v33
v_mul_f32 v42,v34,v34
v_mul_f32 v43,v35,v35
v_fma_f32 v40,v66,v40,v65
v_fma_f32 v41,v66,v41,v65
v_fma_f32 v42,v66,v42,v65
v_fma_f32 v43,v66,v43,v65
v_mul_f32 v40,v32,v40
v_mul_f32 v41,v33,v41
v_mul_f32 v42,v34,v42
v_mul_f32 v43,v35,v43
v_exp_f32 v40,v40
v_exp_f32 v41,v41
v_exp_f32 v42,v42
v_exp_f32 v43,v43
v_fma_f32 v40,v40,v67,v67
v_fma_f32 v41,v41,v67,v67
v_fma_f32 v42,v42,v67,v67
v_fma_f32 v43,v43,v67,v67
v_rcp_f32 v40,v40
v_rcp_f32 v41,v41
v_rcp_f32 v42,v42
v_rcp_f32 v43,v43
v_mul_f32 v32,v32,v40
v_mul_f32 v33,v33,v41
v_mul_f32 v34,v34,v42
v_mul_f32 v35,v35,v43
v_cvt_pk_bf16_f32 v48,v32,v33
s_add_u32 m0,s78,8448
s_nop 0
buffer_load_dword v6,s[36:39],s68 offen offset:0 sc1 lds
v_cvt_pk_bf16_f32 v49,v34,v35
v_fma_f32 v36,-v64,v20,v36
v_fma_f32 v37,-v64,v21,v37
v_fma_f32 v38,-v64,v22,v38
v_fma_f32 v39,-v64,v23,v39
v_mul_f32 v44,v36,v36
v_mul_f32 v45,v37,v37
v_mul_f32 v46,v38,v38
v_mul_f32 v47,v39,v39
v_fma_f32 v44,v66,v44,v65
v_fma_f32 v45,v66,v45,v65
v_fma_f32 v46,v66,v46,v65
v_fma_f32 v47,v66,v47,v65
v_mul_f32 v44,v36,v44
v_mul_f32 v45,v37,v45
v_mul_f32 v46,v38,v46
v_mul_f32 v47,v39,v47
v_exp_f32 v44,v44
v_exp_f32 v45,v45
v_exp_f32 v46,v46
v_exp_f32 v47,v47
v_fma_f32 v44,v44,v67,v67
v_fma_f32 v45,v45,v67,v67
v_fma_f32 v46,v46,v67,v67
v_fma_f32 v47,v47,v67,v67
v_rcp_f32 v44,v44
v_rcp_f32 v45,v45
v_rcp_f32 v46,v46
v_rcp_f32 v47,v47
v_mul_f32 v36,v36,v44
v_mul_f32 v37,v37,v45
v_mul_f32 v38,v38,v46
v_mul_f32 v39,v39,v47
v_cvt_pk_bf16_f32 v50,v36,v37
v_cvt_pk_bf16_f32 v51,v38,v39
buffer_store_dwordx4 v[48:51],v3,s[36:39],s73 offen offset:0 sc1
s_cmp_ge_u32 s48,s59
s_cbranch_scc1 Lg3o_done0_%=
s_add_u32 m0,s78,16384
s_nop 0
buffer_load_dword v6,s[36:39],s68 offen offset:0 sc1 lds
s_waitcnt vmcnt(3)
s_barrier
ds_read_b32 v13,v7 offset:8192
s_mul_i32 s70,s46,5
s_lshr_b32 s70,s70,6
buffer_store_dword v5,v4,s[36:39],s70 offen offset:0 sc1
s_mov_b32 s46,s59
s_mov_b32 s69,18432
s_waitcnt lgkmcnt(0)
v_cmp_eq_u32 vcc,s60,v13
s_nop 0
s_cmp_eq_u32 vcc_lo,-1
s_cbranch_scc1 Lg3o_go0_%=
s_add_u32 m0,s78,16640
s_nop 0
buffer_load_dword v6,s[36:39],s68 offen offset:0 sc1 lds
s_waitcnt vmcnt(4)
s_barrier
ds_read_b32 v13,v7 offset:8448
s_mov_b32 s69,18688
s_waitcnt lgkmcnt(0)
v_cmp_eq_u32 vcc,s60,v13
s_nop 0
s_cmp_eq_u32 vcc_lo,-1
s_cbranch_scc1 Lg3o_go0_%=
s_add_u32 m0,s78,8192
s_nop 0
buffer_load_dword v6,s[36:39],s68 offen offset:0 sc1 lds
s_waitcnt vmcnt(3)
s_barrier
ds_read_b32 v13,v7 offset:16384
s_mul_i32 s70,s47,5
s_lshr_b32 s70,s70,6
buffer_store_dword v5,v4,s[36:39],s70 offen offset:0 sc1
s_mov_b32 s47,s59
s_mov_b32 s69,10240
s_waitcnt lgkmcnt(0)
v_cmp_eq_u32 vcc,s60,v13
s_nop 0
s_cmp_eq_u32 vcc_lo,-1
s_cbranch_scc1 Lg3o_go0_%=
s_add_u32 m0,s78,8448
s_nop 0
buffer_load_dword v6,s[36:39],s68 offen offset:0 sc1 lds
s_waitcnt vmcnt(3)
s_barrier
ds_read_b32 v13,v7 offset:16640
s_mov_b32 s69,10496
s_waitcnt lgkmcnt(0)
v_cmp_eq_u32 vcc,s60,v13
s_nop 0
s_cmp_eq_u32 vcc_lo,-1
s_cbranch_scc1 Lg3o_go0_%=
Lg3o_poll0_%=:
s_add_u32 m0,s78,16384
s_nop 0
buffer_load_dword v6,s[36:39],s68 offen offset:0 sc1 lds
s_waitcnt vmcnt(2)
s_barrier
ds_read_b32 v13,v7 offset:8192
s_mov_b32 s69,18432
s_waitcnt lgkmcnt(0)
v_cmp_eq_u32 vcc,s60,v13
s_nop 0
s_cmp_eq_u32 vcc_lo,-1
s_cbranch_scc1 Lg3o_go0_%=
s_sleep 4
s_add_u32 m0,s78,16640
s_nop 0
buffer_load_dword v6,s[36:39],s68 offen offset:0 sc1 lds
s_waitcnt vmcnt(2)
s_barrier
ds_read_b32 v13,v7 offset:8448
s_mov_b32 s69,18688
s_waitcnt lgkmcnt(0)
v_cmp_eq_u32 vcc,s60,v13
s_nop 0
s_cmp_eq_u32 vcc_lo,-1
s_cbranch_scc1 Lg3o_go0_%=
s_sleep 4
s_add_u32 m0,s78,8192
s_nop 0
buffer_load_dword v6,s[36:39],s68 offen offset:0 sc1 lds
s_waitcnt vmcnt(2)
s_barrier
ds_read_b32 v13,v7 offset:16384
s_mov_b32 s69,10240
s_waitcnt lgkmcnt(0)
v_cmp_eq_u32 vcc,s60,v13
s_nop 0
s_cmp_eq_u32 vcc_lo,-1)"
R"(
s_cbranch_scc1 Lg3o_go0_%=
s_sleep 4
s_add_u32 m0,s78,8448
s_nop 0
buffer_load_dword v6,s[36:39],s68 offen offset:0 sc1 lds
s_waitcnt vmcnt(2)
s_barrier
ds_read_b32 v13,v7 offset:16640
s_mov_b32 s69,10496
s_waitcnt lgkmcnt(0)
v_cmp_eq_u32 vcc,s60,v13
s_nop 0
s_cmp_eq_u32 vcc_lo,-1
s_cbranch_scc1 Lg3o_go0_%=
s_sleep 4
s_add_u32 s79,s79,1
s_cmp_lt_u32 s79,20000
s_cbranch_scc1 Lg3o_poll0_%=
Lg3o_go0_%=:
s_xor_b32 s62,s63,65536
s_mov_b32 m0,s62
s_nop 0
buffer_load_dwordx4 v0,s[36:39],s48 offen offset:0 sc1 lds
buffer_load_dwordx4 v0,s[36:39],s48 offen offset:1024 sc1 lds
buffer_load_dwordx4 v0,s[36:39],s48 offen offset:2048 sc1 lds
buffer_load_dwordx4 v0,s[36:39],s48 offen offset:3072 sc1 lds
s_add_u32 m0,s62,16384
s_nop 0
buffer_load_dwordx4 v1,s[36:39],s48 offen offset:0 sc1 lds
buffer_load_dwordx4 v1,s[36:39],s48 offen offset:1024 sc1 lds
buffer_load_dwordx4 v1,s[36:39],s48 offen offset:2048 sc1 lds
buffer_load_dwordx4 v1,s[36:39],s48 offen offset:3072 sc1 lds
s_lshr_b32 s72,s48,5
s_lshr_b32 s71,s62,2
s_add_u32 m0,s71,s88
s_nop 0
buffer_load_dword v9,s[36:39],s72 offen offset:0 sc1 lds
s_cmp_eq_u32 vcc_hi,-1
s_cselect_b32 s66,s49,s59
s_xor_b32 s62,s64,65536
s_cmp_ge_u32 s66,s59
s_cselect_b32 s68,s49,s50
s_lshr_b32 s68,s68,5
s_add_u32 m0,s78,0
s_nop 0
buffer_load_dword v6,s[36:39],s68 offen offset:0 sc1 lds
s_mov_b32 m0,s62
s_nop 0
buffer_load_dwordx4 v0,s[36:39],s66 offen offset:0 sc1 lds
buffer_load_dwordx4 v0,s[36:39],s66 offen offset:1024 sc1 lds
buffer_load_dwordx4 v0,s[36:39],s66 offen offset:2048 sc1 lds
buffer_load_dwordx4 v0,s[36:39],s66 offen offset:3072 sc1 lds
s_waitcnt vmcnt(10)
s_barrier
ds_read_b128 v[128:131],v2 offset:0
ds_read_b128 v[132:135],v2 offset:1024
ds_read_b128 v[136:139],v2 offset:2048
ds_read_b128 v[140:143],v2 offset:3072
ds_read_b128 v[144:147],v2 offset:4096
ds_read_b128 v[148:151],v2 offset:5120
ds_read_b128 v[152:155],v2 offset:6144
s_mul_i32 s70,s46,5
s_lshr_b32 s70,s70,6
buffer_store_dword v5,v4,s[36:39],s70 offen offset:0 sc1
s_mov_b32 s46,s59
s_mul_i32 s70,s47,5
s_lshr_b32 s70,s70,6
buffer_store_dword v5,v4,s[36:39],s70 offen offset:0 sc1
s_mov_b32 s47,s59
s_mul_i32 s70,s59,5
s_lshr_b32 s70,s70,6
buffer_store_dword v5,v4,s[36:39],s70 offen offset:0 sc1
s_waitcnt lgkmcnt(3)
s_setpc_b64 s[90:91]
Lg3o_gs0_%=:
s_cmp_eq_u32 s54,1
s_cselect_b32 s68,s49,s48
s_lshr_b32 s68,s68,5
s_add_u32 m0,s78,8192
s_nop 0
buffer_load_dword v6,s[36:39],s68 offen offset:0 sc1 lds
s_add_u32 m0,s62,16384
s_nop 0
buffer_load_dwordx4 v1,s[36:39],s66 offen offset:0 sc1 lds
buffer_load_dwordx4 v1,s[36:39],s66 offen offset:1024 sc1 lds
buffer_load_dwordx4 v1,s[36:39],s66 offen offset:2048 sc1 lds
buffer_load_dwordx4 v1,s[36:39],s66 offen offset:3072 sc1 lds
s_lshr_b32 s72,s66,5
s_lshr_b32 s71,s62,2
s_add_u32 m0,s71,s88
s_nop 0
buffer_load_dword v9,s[36:39],s72 offen offset:0 sc1 lds
s_waitcnt vmcnt(5)
s_barrier
ds_read_b32 v13,v7 offset:8192
ds_read_b128 v[128:131],v2 offset:0
ds_read_b128 v[132:135],v2 offset:1024
ds_read_b128 v[136:139],v2 offset:2048
ds_read_b128 v[140:143],v2 offset:3072
ds_read_b128 v[144:147],v2 offset:4096
ds_read_b128 v[148:151],v2 offset:5120
ds_read_b128 v[152:155],v2 offset:6144
s_mul_i32 s70,s46,5
s_lshr_b32 s70,s70,6
buffer_store_dword v5,v4,s[36:39],s70 offen offset:0 sc1
s_mov_b32 s46,s59
s_waitcnt lgkmcnt(7)
v_cmp_eq_u32 vcc,s60,v13
s_nop 0
s_cmp_eq_u32 vcc_hi,-1
s_cselect_b32 s66,s49,s59
s_xor_b32 s62,s64,65536
s_cmp_ge_u32 s66,s59
s_cselect_b32 s68,s49,s50
s_lshr_b32 s68,s68,5
s_add_u32 m0,s78,0
s_nop 0
buffer_load_dword v6,s[36:39],s68 offen offset:0 sc1 lds
s_mov_b32 m0,s62
s_nop 0
buffer_load_dwordx4 v0,s[36:39],s66 offen offset:0 sc1 lds
buffer_load_dwordx4 v0,s[36:39],s66 offen offset:1024 sc1 lds
buffer_load_dwordx4 v0,s[36:39],s66 offen offset:2048 sc1 lds
buffer_load_dwordx4 v0,s[36:39],s66 offen offset:3072 sc1 lds
s_mul_i32 s70,s59,5
s_lshr_b32 s70,s70,6
buffer_store_dword v5,v4,s[36:39],s70 offen offset:0 sc1
s_mul_i32 s70,s59,5
s_lshr_b32 s70,s70,6
buffer_store_dword v5,v4,s[36:39],s70 offen offset:0 sc1
s_waitcnt lgkmcnt(3)
s_setpc_b64 s[90:91]
Lg3o_done0_%=:
s_waitcnt vmcnt(0)
s_barrier
s_mul_i32 s70,s46,5
s_lshr_b32 s70,s70,6
buffer_store_dword v5,v4,s[36:39],s70 offen offset:0 sc1
s_mul_i32 s70,s47,5
s_lshr_b32 s70,s70,6
buffer_store_dword v5,v4,s[36:39],s70 offen offset:0 sc1
s_branch Lg3o_done_%=
Lg3o_slow1_%=:
s_cmp_lt_u32 s67,s59
s_cbranch_scc1 Lg3o_gs1_%=
Lg3o_slowb1_%=:
s_cmp_eq_u32 s54,1
s_cselect_b32 s68,s50,s49
s_lshr_b32 s68,s68,5
s_add_u32 m0,s78,8192
s_nop 0
buffer_load_dword v6,s[36:39],s68 offen offset:0 sc1 lds
s_mul_i32 s73,s48,7
s_lshr_b32 s73,s73,1
v_mul_f32 v67,v61,v60
v_mul_f32 v66,s77,v66
v_mul_f32 v66,v66,v65
v_fma_f32 v24,-v64,v16,v24
v_fma_f32 v25,-v64,v17,v25
v_fma_f32 v26,-v64,v18,v26
v_fma_f32 v27,-v64,v19,v27
v_mul_f32 v40,v24,v24
v_mul_f32 v41,v25,v25
v_mul_f32 v42,v26,v26
v_mul_f32 v43,v27,v27
v_fma_f32 v40,v66,v40,v65
v_fma_f32 v41,v66,v41,v65
v_fma_f32 v42,v66,v42,v65
v_fma_f32 v43,v66,v43,v65
v_mul_f32 v40,v24,v40
v_mul_f32 v41,v25,v41
v_mul_f32 v42,v26,v42
v_mul_f32 v43,v27,v43
v_exp_f32 v40,v40
v_exp_f32 v41,v41
v_exp_f32 v42,v42
v_exp_f32 v43,v43
v_fma_f32 v40,v40,v67,v67
v_fma_f32 v41,v41,v67,v67
v_fma_f32 v42,v42,v67,v67
v_fma_f32 v43,v43,v67,v67
v_rcp_f32 v40,v40
v_rcp_f32 v41,v41
v_rcp_f32 v42,v42
v_rcp_f32 v43,v43
v_mul_f32 v24,v24,v40
v_mul_f32 v25,v25,v41
v_mul_f32 v26,v26,v42
v_mul_f32 v27,v27,v43
v_cvt_pk_bf16_f32 v48,v24,v25
s_add_u32 m0,s78,8448
s_nop 0
buffer_load_dword v6,s[36:39],s68 offen offset:0 sc1 lds
v_cvt_pk_bf16_f32 v49,v26,v27
v_fma_f32 v28,-v64,v20,v28
v_fma_f32 v29,-v64,v21,v29
v_fma_f32 v30,-v64,v22,v30
v_fma_f32 v31,-v64,v23,v31
v_mul_f32 v44,v28,v28
v_mul_f32 v45,v29,v29)"
R"(
v_mul_f32 v46,v30,v30
v_mul_f32 v47,v31,v31
v_fma_f32 v44,v66,v44,v65
v_fma_f32 v45,v66,v45,v65
v_fma_f32 v46,v66,v46,v65
v_fma_f32 v47,v66,v47,v65
v_mul_f32 v44,v28,v44
v_mul_f32 v45,v29,v45
v_mul_f32 v46,v30,v46
v_mul_f32 v47,v31,v47
v_exp_f32 v44,v44
v_exp_f32 v45,v45
v_exp_f32 v46,v46
v_exp_f32 v47,v47
v_fma_f32 v44,v44,v67,v67
v_fma_f32 v45,v45,v67,v67
v_fma_f32 v46,v46,v67,v67
v_fma_f32 v47,v47,v67,v67
v_rcp_f32 v44,v44
v_rcp_f32 v45,v45
v_rcp_f32 v46,v46
v_rcp_f32 v47,v47
v_mul_f32 v28,v28,v44
v_mul_f32 v29,v29,v45
v_mul_f32 v30,v30,v46
v_mul_f32 v31,v31,v47
v_cvt_pk_bf16_f32 v50,v28,v29
v_cvt_pk_bf16_f32 v51,v30,v31
buffer_store_dwordx4 v[48:51],v3,s[36:39],s73 offen offset:0 sc1
s_cmp_ge_u32 s49,s59
s_cbranch_scc1 Lg3o_done1_%=
s_add_u32 m0,s78,16384
s_nop 0
buffer_load_dword v6,s[36:39],s68 offen offset:0 sc1 lds
s_waitcnt vmcnt(3)
s_barrier
ds_read_b32 v13,v7 offset:8192
s_mul_i32 s70,s47,5
s_lshr_b32 s70,s70,6
buffer_store_dword v5,v4,s[36:39],s70 offen offset:0 sc1
s_mov_b32 s47,s59
s_mov_b32 s69,18432
s_waitcnt lgkmcnt(0)
v_cmp_eq_u32 vcc,s60,v13
s_nop 0
s_cmp_eq_u32 vcc_lo,-1
s_cbranch_scc1 Lg3o_go1_%=
s_add_u32 m0,s78,16640
s_nop 0
buffer_load_dword v6,s[36:39],s68 offen offset:0 sc1 lds
s_waitcnt vmcnt(4)
s_barrier
ds_read_b32 v13,v7 offset:8448
s_mov_b32 s69,18688
s_waitcnt lgkmcnt(0)
v_cmp_eq_u32 vcc,s60,v13
s_nop 0
s_cmp_eq_u32 vcc_lo,-1
s_cbranch_scc1 Lg3o_go1_%=
s_add_u32 m0,s78,8192
s_nop 0
buffer_load_dword v6,s[36:39],s68 offen offset:0 sc1 lds
s_waitcnt vmcnt(3)
s_barrier
ds_read_b32 v13,v7 offset:16384
s_mul_i32 s70,s48,5
s_lshr_b32 s70,s70,6
buffer_store_dword v5,v4,s[36:39],s70 offen offset:0 sc1
s_mov_b32 s48,s59
s_mov_b32 s69,10240
s_waitcnt lgkmcnt(0)
v_cmp_eq_u32 vcc,s60,v13
s_nop 0
s_cmp_eq_u32 vcc_lo,-1
s_cbranch_scc1 Lg3o_go1_%=
s_add_u32 m0,s78,8448
s_nop 0
buffer_load_dword v6,s[36:39],s68 offen offset:0 sc1 lds
s_waitcnt vmcnt(3)
s_barrier
ds_read_b32 v13,v7 offset:16640
s_mov_b32 s69,10496
s_waitcnt lgkmcnt(0)
v_cmp_eq_u32 vcc,s60,v13
s_nop 0
s_cmp_eq_u32 vcc_lo,-1
s_cbranch_scc1 Lg3o_go1_%=
Lg3o_poll1_%=:
s_add_u32 m0,s78,16384
s_nop 0
buffer_load_dword v6,s[36:39],s68 offen offset:0 sc1 lds
s_waitcnt vmcnt(2)
s_barrier
ds_read_b32 v13,v7 offset:8192
s_mov_b32 s69,18432
s_waitcnt lgkmcnt(0)
v_cmp_eq_u32 vcc,s60,v13
s_nop 0
s_cmp_eq_u32 vcc_lo,-1
s_cbranch_scc1 Lg3o_go1_%=
s_sleep 4
s_add_u32 m0,s78,16640
s_nop 0
buffer_load_dword v6,s[36:39],s68 offen offset:0 sc1 lds
s_waitcnt vmcnt(2)
s_barrier
ds_read_b32 v13,v7 offset:8448
s_mov_b32 s69,18688
s_waitcnt lgkmcnt(0)
v_cmp_eq_u32 vcc,s60,v13
s_nop 0
s_cmp_eq_u32 vcc_lo,-1
s_cbranch_scc1 Lg3o_go1_%=
s_sleep 4
s_add_u32 m0,s78,8192
s_nop 0
buffer_load_dword v6,s[36:39],s68 offen offset:0 sc1 lds
s_waitcnt vmcnt(2)
s_barrier
ds_read_b32 v13,v7 offset:16384
s_mov_b32 s69,10240
s_waitcnt lgkmcnt(0)
v_cmp_eq_u32 vcc,s60,v13
s_nop 0
s_cmp_eq_u32 vcc_lo,-1
s_cbranch_scc1 Lg3o_go1_%=
s_sleep 4
s_add_u32 m0,s78,8448
s_nop 0
buffer_load_dword v6,s[36:39],s68 offen offset:0 sc1 lds
s_waitcnt vmcnt(2)
s_barrier
ds_read_b32 v13,v7 offset:16640
s_mov_b32 s69,10496
s_waitcnt lgkmcnt(0)
v_cmp_eq_u32 vcc,s60,v13
s_nop 0
s_cmp_eq_u32 vcc_lo,-1
s_cbranch_scc1 Lg3o_go1_%=
s_sleep 4
s_add_u32 s79,s79,1
s_cmp_lt_u32 s79,20000
s_cbranch_scc1 Lg3o_poll1_%=
Lg3o_go1_%=:
s_xor_b32 s62,s64,65536
s_mov_b32 m0,s62
s_nop 0
buffer_load_dwordx4 v0,s[36:39],s49 offen offset:0 sc1 lds
buffer_load_dwordx4 v0,s[36:39],s49 offen offset:1024 sc1 lds
buffer_load_dwordx4 v0,s[36:39],s49 offen offset:2048 sc1 lds
buffer_load_dwordx4 v0,s[36:39],s49 offen offset:3072 sc1 lds
s_add_u32 m0,s62,16384
s_nop 0
buffer_load_dwordx4 v1,s[36:39],s49 offen offset:0 sc1 lds
buffer_load_dwordx4 v1,s[36:39],s49 offen offset:1024 sc1 lds
buffer_load_dwordx4 v1,s[36:39],s49 offen offset:2048 sc1 lds
buffer_load_dwordx4 v1,s[36:39],s49 offen offset:3072 sc1 lds
s_lshr_b32 s72,s49,5
s_lshr_b32 s71,s62,2
s_add_u32 m0,s71,s88
s_nop 0
buffer_load_dword v9,s[36:39],s72 offen offset:0 sc1 lds
s_cmp_eq_u32 vcc_hi,-1
s_cselect_b32 s67,s50,s59
s_xor_b32 s62,s63,65536
s_cmp_ge_u32 s67,s59
s_cselect_b32 s68,s50,s51
s_lshr_b32 s68,s68,5
s_add_u32 m0,s78,256
s_nop 0
buffer_load_dword v6,s[36:39],s68 offen offset:0 sc1 lds
s_mov_b32 m0,s62
s_nop 0
buffer_load_dwordx4 v0,s[36:39],s67 offen offset:0 sc1 lds
buffer_load_dwordx4 v0,s[36:39],s67 offen offset:1024 sc1 lds
buffer_load_dwordx4 v0,s[36:39],s67 offen offset:2048 sc1 lds
buffer_load_dwordx4 v0,s[36:39],s67 offen offset:3072 sc1 lds
s_waitcnt vmcnt(10)
s_barrier
ds_read_b128 v[128:131],v2 offset:32768
ds_read_b128 v[132:135],v2 offset:33792
ds_read_b128 v[136:139],v2 offset:34816
ds_read_b128 v[140:143],v2 offset:35840
ds_read_b128 v[144:147],v2 offset:36864
ds_read_b128 v[148:151],v2 offset:37888
ds_read_b128 v[152:155],v2 offset:38912
s_mul_i32 s70,s47,5
s_lshr_b32 s70,s70,6
buffer_store_dword v5,v4,s[36:39],s70 offen offset:0 sc1
s_mov_b32 s47,s59
s_mul_i32 s70,s48,5
s_lshr_b32 s70,s70,6
buffer_store_dword v5,v4,s[36:39],s70 offen offset:0 sc1
s_mov_b32 s48,s59
s_mul_i32 s70,s59,5
s_lshr_b32 s70,s70,6
buffer_store_dword v5,v4,s[36:39],s70 offen offset:0 sc1
s_waitcnt lgkmcnt(3)
s_setpc_b64 s[92:93]
Lg3o_gs1_%=:)"
R"(
s_cmp_eq_u32 s54,1
s_cselect_b32 s68,s50,s49
s_lshr_b32 s68,s68,5
s_add_u32 m0,s78,8192
s_nop 0
buffer_load_dword v6,s[36:39],s68 offen offset:0 sc1 lds
s_add_u32 m0,s62,16384
s_nop 0
buffer_load_dwordx4 v1,s[36:39],s67 offen offset:0 sc1 lds
buffer_load_dwordx4 v1,s[36:39],s67 offen offset:1024 sc1 lds
buffer_load_dwordx4 v1,s[36:39],s67 offen offset:2048 sc1 lds
buffer_load_dwordx4 v1,s[36:39],s67 offen offset:3072 sc1 lds
s_lshr_b32 s72,s67,5
s_lshr_b32 s71,s62,2
s_add_u32 m0,s71,s88
s_nop 0
buffer_load_dword v9,s[36:39],s72 offen offset:0 sc1 lds
s_waitcnt vmcnt(5)
s_barrier
ds_read_b32 v13,v7 offset:8192
ds_read_b128 v[128:131],v2 offset:32768
ds_read_b128 v[132:135],v2 offset:33792
ds_read_b128 v[136:139],v2 offset:34816
ds_read_b128 v[140:143],v2 offset:35840
ds_read_b128 v[144:147],v2 offset:36864
ds_read_b128 v[148:151],v2 offset:37888
ds_read_b128 v[152:155],v2 offset:38912
s_mul_i32 s70,s47,5
s_lshr_b32 s70,s70,6
buffer_store_dword v5,v4,s[36:39],s70 offen offset:0 sc1
s_mov_b32 s47,s59
s_waitcnt lgkmcnt(7)
v_cmp_eq_u32 vcc,s60,v13
s_nop 0
s_cmp_eq_u32 vcc_hi,-1
s_cselect_b32 s67,s50,s59
s_xor_b32 s62,s63,65536
s_cmp_ge_u32 s67,s59
s_cselect_b32 s68,s50,s51
s_lshr_b32 s68,s68,5
s_add_u32 m0,s78,256
s_nop 0
buffer_load_dword v6,s[36:39],s68 offen offset:0 sc1 lds
s_mov_b32 m0,s62
s_nop 0
buffer_load_dwordx4 v0,s[36:39],s67 offen offset:0 sc1 lds
buffer_load_dwordx4 v0,s[36:39],s67 offen offset:1024 sc1 lds
buffer_load_dwordx4 v0,s[36:39],s67 offen offset:2048 sc1 lds
buffer_load_dwordx4 v0,s[36:39],s67 offen offset:3072 sc1 lds
s_mul_i32 s70,s59,5
s_lshr_b32 s70,s70,6
buffer_store_dword v5,v4,s[36:39],s70 offen offset:0 sc1
s_mul_i32 s70,s59,5
s_lshr_b32 s70,s70,6
buffer_store_dword v5,v4,s[36:39],s70 offen offset:0 sc1
s_waitcnt lgkmcnt(3)
s_setpc_b64 s[92:93]
Lg3o_done1_%=:
s_waitcnt vmcnt(0)
s_barrier
s_mul_i32 s70,s47,5
s_lshr_b32 s70,s70,6
buffer_store_dword v5,v4,s[36:39],s70 offen offset:0 sc1
s_mul_i32 s70,s48,5
s_lshr_b32 s70,s70,6
buffer_store_dword v5,v4,s[36:39],s70 offen offset:0 sc1
s_branch Lg3o_done_%=
Lg3o_done_%=:
s_waitcnt vmcnt(0) lgkmcnt(0)
s_barrier
s_endpgm
Lcode_g3o_e:)"
  :: "s"(ws), "s"(wave), "s"(sStep), "s"(sX1), "s"(sX2), "s"(sU0), "s"(sWw), "s"(sBuf), "s"(sEp), "v"(vSrc), "v"(vA), "v"(vOut), "v"(vFlag), "v"(vPollOff), "v"(vSnapRd), "v"(vX), "v"(vAuxS), "v"(vStW), "v"(vStR), "v"(vWd0), "v"(vWd1), "v"(vWr0), "v"(vWr1), "v"(vWr2), "s"(w3) : "v0", "v1", "v2", "v3", "v4", "v5", "v6", "v7", "v8", "v9", "v10", "v11", "v12", "v13", "v14", "v15", "v16", "v17", "v18", "v19", "v20", "v21", "v22", "v23", "v24", "v25", "v26", "v27", "v28", "v29", "v30", "v31", "v32", "v33", "v34", "v35", "v36", "v37", "v38", "v39", "v40", "v41", "v42", "v43", "v44", "v45", "v46", "v47", "v48", "v49", "v50", "v51", "v52", "v53", "v54", "v55", "v56", "v57", "v58", "v59", "v60", "v61", "v62", "v63", "v64", "v65", "v66", "v67", "v68", "v69", "v70", "v71", "v72", "v73", "v74", "v75", "v76", "v77", "v78", "v79", "v80", "v81", "v82", "v83", "v84", "v85", "v86", "v87", "v88", "v89", "v90", "v91", "v92", "v93", "v94", "v95", "v96", "v97", "v98", "v99", "v100", "v101", "v102", "v103", "v104", "v105", "v106", "v107", "v108", "v109", "v110", "v111", "v112", "v113", "v114", "v115", "v116", "v117", "v118", "v119", "v120", "v121", "v122", "v123", "v124", "v125", "v126", "v127", "v128", "v129", "v130", "v131", "v132", "v133", "v134", "v135", "v136", "v137", "v138", "v139", "v140", "v141", "v142", "v143", "v144", "v145", "v146", "v147", "v148", "v149", "v150", "v151", "v152", "v153", "v154", "v155", "v156", "v157", "v158", "v159", "v160", "v161", "v162", "v163", "v164", "v165", "v166", "v167", "v168", "v169", "v170", "v171", "v172", "v173", "v174", "v175", "v176", "v177", "v178", "v179", "v180", "v181", "v182", "v183", "v184", "v185", "v186", "v187", "v188", "v189", "v190", "v191", "v192", "v193", "v194", "v195", "v196", "v197", "v198", "v199", "v200", "v201", "v202", "v203", "v204", "v205", "v206", "v207", "v208", "v209", "v210", "v211", "v212", "v213", "v214", "v215", "v216", "v217", "v218", "v219", "v220", "v221", "v222", "v223", "v224", "v225", "v226", "v227", "v228", "v229", "v230", "v231", "v255", "a0", "a1", "a2", "a3", "a4", "a5", "a6", "a7", "a8", "a9", "a10", "a11", "a12", "a13", "a14", "a15", "a16", "a17", "a18", "a19", "a20", "a21", "a22", "a23", "a24", "a25", "a26", "a27", "a28", "a29", "a30", "a31", "a32", "a33", "a34", "a35", "a36", "a37", "a38", "a39", "a40", "a41", "a42", "a43", "a44", "a45", "a46", "a47", "a48", "a49", "a50", "a51", "a52", "a53", "a54", "a55", "a56", "a57", "a58", "a59", "a60", "a61", "a62", "a63", "a64", "a65", "a66", "a67", "a68", "a69", "a70", "a71", "a72", "a73", "a74", "a75", "a76", "a77", "a78", "a79", "a80", "a81", "a82", "a83", "a84", "a85", "a86", "a87", "a88", "a89", "a90", "a91", "a92", "a93", "a94", "a95", "a96", "a97", "a98", "a99", "a100", "a101", "a102", "a103", "a104", "a105", "a106", "a107", "a108", "a109", "a110", "a111", "a112", "a113", "a114", "a115", "a116", "a117", "a118", "a119", "a120", "a121", "a122", "a123", "a124", "a125", "a126", "a127", "a128", "a129", "a130", "a131", "a132", "a133", "a134", "a135", "a136", "a137", "a138", "a139", "a140", "a141", "a142", "a143", "a144", "a145", "a146", "a147", "a148", "a149", "a150", "a151", "a152", "a153", "a154", "a155", "a156", "a157", "a158", "a159", "a160", "a161", "a162", "a163", "a164", "a165", "a166", "a167", "a168", "a169", "a170", "a171", "a172", "a173", "a174", "a175", "a176", "a177", "a178", "a179", "a180", "a181", "a182", "a183", "a184", "a185", "a186", "a187", "a188", "a189", "a190", "a191", "a192", "a193", "a194", "a195", "a196", "a197", "a198", "a199", "a200", "a201", "a202", "a203", "a204", "a205", "a206", "a207", "a208", "a209", "a210", "a211", "a212", "a213", "a214", "a215", "a216", "a217", "a218", "a219", "a220", "a221", "a222", "a223", "a224", "a225", "a226", "a227", "a228", "a229", "a230", "a231", "a232", "a233", "a234", "a235", "a236", "a237", "a238", "a239", "a240", "a241", "a242", "a243", "a244", "a245", "a246", "a247", "a248", "a249", "a250", "a251", "a252", "a253", "a254", "a255", "s36", "s37", "s38", "s39", "s40", "s41", "s42", "s43", "s44", "s45", "s46", "s47", "s48", "s49", "s50", "s51", "s52", "s53", "s54", "s55", "s56", "s57", "s58", "s59", "s60", "s61", "s62", "s63", "s64", "s65", "s66", "s67", "s68", "s69", "s70", "s71", "s72", "s73", "s74", "s75", "s76", "s77", "s78", "s79", "s80", "s81", "s82", "s83", "s84", "s85", "s86", "s87", "s88", "s89", "s90", "s91", "s92", "s93", "s94", "s95", "s96", "s97", "s98", "s99", "vcc", "scc", "memory"); }
    __builtin_unreachable();
  } else if (kind == 4) {
    KARG(ws, 10) KARG(w4, 9)
    COMMON(kh == 1 ? 6 : 5, OFF_FG + ((14 * kh) / 3) * 128, OFF_FP + (kh * 4 + slice) * 128, 65536, 256, 2048, 5);
    const unsigned vSrc = OFF_G + kh * 28672 + wave * 4096 + vA, vSrc2 = OFF_G + kh * 28672 + 16384 + wave * 3072 + vA;
    const unsigned vOut = (kh == 0 ? OFF_P0 : kh == 1 ? OFF_P1 : kh == 2 ? OFF_P2 : OFF_P3) + (l15 >> 3) * 16384 + (slice * 8 + wave * 2 + (kh >> 1)) * 512 + lg * 128 + (l15 & 7) * 16;
    const unsigned vOut2 = vOut ^ 512;
    const unsigned vAuxS = ((kh & 1) ? OFF_XP : OFF_X1) + (slice * 8 + wave * 2 + 1 - (kh >> 1)) * 1024 + vA;
    const unsigned sWw = __builtin_amdgcn_readfirstlane(kh * 896 * 2048 + slice * 512 + wave * 128);
    asm volatile(R"(
Lcode_g4_b:
s_mov_b64 s[36:37],%0
s_mov_b32 s38,122727552
s_mov_b32 s39,0x00020000
s_mov_b32 s54,%1
s_mov_b32 s55,%2
s_mov_b32 s56,%3
s_mov_b32 s58,%4
s_mov_b32 s83,%5
s_mov_b32 s82,%6
s_mov_b32 s84,%7
s_mov_b32 s60,%8
v_mov_b32 v0,%9
v_mov_b32 v1,%10
v_mov_b32 v2,%11
v_mov_b32 v3,%12
v_mov_b32 v4,%13
v_mov_b32 v5,%14
v_mov_b32 v7,%15
v_mov_b32 v8,%16
v_mov_b32 v9,%17
v_mov_b32 v12,%18
v_mov_b32 v13,%19
v_mov_b32 v14,%20
v_mov_b32 v15,%21
v_mov_b32 v16,%22
s_mov_b64 s[40:41],%23
s_mov_b32 s42,7340032
s_mov_b32 s43,0x00020000
s_lshl_b32 s88,s54,12
s_mul_i32 s68,s54,9216
s_add_u32 s68,s68,122880
v_add_u32 v10,s68,v2
s_add_u32 m0,s88,0
s_add_u32 s61,s82,0
buffer_load_dwordx4 v12,s[40:43],s61 offen offset:0  lds
s_add_u32 m0,s88,1024
s_nop 0
buffer_load_dwordx4 v13,s[40:43],s61 offen offset:0  lds
s_add_u32 m0,s88,2048
s_add_u32 s61,s82,32768
buffer_load_dwordx4 v12,s[40:43],s61 offen offset:0  lds
s_add_u32 m0,s88,3072
s_nop 0
buffer_load_dwordx4 v13,s[40:43],s61 offen offset:0  lds
s_add_u32 m0,s88,16384
s_add_u32 s61,s82,65536
buffer_load_dwordx4 v12,s[40:43],s61 offen offset:0  lds
s_add_u32 m0,s88,17408
s_nop 0
buffer_load_dwordx4 v13,s[40:43],s61 offen offset:0  lds
s_add_u32 m0,s88,18432
s_add_u32 s61,s82,98304
buffer_load_dwordx4 v12,s[40:43],s61 offen offset:0  lds
s_add_u32 m0,s88,19456
s_nop 0
buffer_load_dwordx4 v13,s[40:43],s61 offen offset:0  lds
s_add_u32 m0,s88,32768
s_add_u32 s61,s82,131072
buffer_load_dwordx4 v12,s[40:43],s61 offen offset:0  lds
s_add_u32 m0,s88,33792
s_nop 0
buffer_load_dwordx4 v13,s[40:43],s61 offen offset:0  lds
s_add_u32 m0,s88,34816
s_add_u32 s61,s82,163840
buffer_load_dwordx4 v12,s[40:43],s61 offen offset:0  lds
s_add_u32 m0,s88,35840
s_nop 0
buffer_load_dwordx4 v13,s[40:43],s61 offen offset:0  lds
s_add_u32 m0,s88,49152
s_add_u32 s61,s82,196608
buffer_load_dwordx4 v12,s[40:43],s61 offen offset:0  lds
s_add_u32 m0,s88,50176
s_nop 0
buffer_load_dwordx4 v13,s[40:43],s61 offen offset:0  lds
s_add_u32 m0,s88,51200
s_add_u32 s61,s82,229376
buffer_load_dwordx4 v12,s[40:43],s61 offen offset:0  lds
s_add_u32 m0,s88,52224
s_nop 0
buffer_load_dwordx4 v13,s[40:43],s61 offen offset:0  lds
s_add_u32 m0,s88,65536
s_add_u32 s61,s82,262144
buffer_load_dwordx4 v12,s[40:43],s61 offen offset:0  lds
s_add_u32 m0,s88,66560
s_nop 0
buffer_load_dwordx4 v13,s[40:43],s61 offen offset:0  lds
s_add_u32 m0,s88,67584
s_add_u32 s61,s82,294912
buffer_load_dwordx4 v12,s[40:43],s61 offen offset:0  lds
s_add_u32 m0,s88,68608
s_nop 0
buffer_load_dwordx4 v13,s[40:43],s61 offen offset:0  lds
v_add_u32 v28,49152,v14
v_add_u32 v30,49152,v16
v_add_u32 v32,98304,v14
v_add_u32 v34,98304,v16
s_waitcnt vmcnt(16)
s_add_u32 m0,s88,81920
s_add_u32 s61,s82,327680
buffer_load_dwordx4 v12,s[40:43],s61 offen offset:0  lds
s_add_u32 m0,s88,82944
ds_read_b64_tr_b16 a[0:1],v14 offset:0
buffer_load_dwordx4 v13,s[40:43],s61 offen offset:0  lds
s_add_u32 m0,s88,83968
s_add_u32 s61,s82,360448
buffer_load_dwordx4 v12,s[40:43],s61 offen offset:0  lds
s_add_u32 m0,s88,84992
ds_read_b64_tr_b16 a[2:3],v14 offset:512
buffer_load_dwordx4 v13,s[40:43],s61 offen offset:0  lds
ds_read_b64_tr_b16 a[4:5],v14 offset:8
ds_read_b64_tr_b16 a[6:7],v14 offset:520
ds_read_b64_tr_b16 a[8:9],v16 offset:0
ds_read_b64_tr_b16 a[10:11],v16 offset:512
ds_read_b64_tr_b16 a[12:13],v16 offset:8
ds_read_b64_tr_b16 a[14:15],v16 offset:520
s_waitcnt vmcnt(16)
s_add_u32 m0,s88,98304
s_add_u32 s61,s82,393216
buffer_load_dwordx4 v12,s[40:43],s61 offen offset:0  lds
s_add_u32 m0,s88,99328
ds_read_b64_tr_b16 a[16:17],v14 offset:16384
buffer_load_dwordx4 v13,s[40:43],s61 offen offset:0  lds
s_add_u32 m0,s88,100352
s_add_u32 s61,s82,425984
buffer_load_dwordx4 v12,s[40:43],s61 offen offset:0  lds
s_add_u32 m0,s88,101376
ds_read_b64_tr_b16 a[18:19],v14 offset:16896
buffer_load_dwordx4 v13,s[40:43],s61 offen offset:0  lds
ds_read_b64_tr_b16 a[20:21],v14 offset:16392
ds_read_b64_tr_b16 a[22:23],v14 offset:16904
ds_read_b64_tr_b16 a[24:25],v16 offset:16384
ds_read_b64_tr_b16 a[26:27],v16 offset:16896
ds_read_b64_tr_b16 a[28:29],v16 offset:16392
ds_read_b64_tr_b16 a[30:31],v16 offset:16904
s_waitcnt lgkmcnt(8)
s_waitcnt vmcnt(16)
s_add_u32 m0,s88,0
s_add_u32 s61,s82,458752
buffer_load_dwordx4 v12,s[40:43],s61 offen offset:0  lds
s_add_u32 m0,s88,1024
ds_read_b64_tr_b16 a[32:33],v14 offset:32768
buffer_load_dwordx4 v13,s[40:43],s61 offen offset:0  lds
s_add_u32 m0,s88,2048
s_add_u32 s61,s82,491520
buffer_load_dwordx4 v12,s[40:43],s61 offen offset:0  lds
s_add_u32 m0,s88,3072
ds_read_b64_tr_b16 a[34:35],v14 offset:33280
buffer_load_dwordx4 v13,s[40:43],s61 offen offset:0  lds
ds_read_b64_tr_b16 a[36:37],v14 offset:32776
ds_read_b64_tr_b16 a[38:39],v14 offset:33288
ds_read_b64_tr_b16 a[40:41],v16 offset:32768
ds_read_b64_tr_b16 a[42:43],v16 offset:33280
ds_read_b64_tr_b16 a[44:45],v16 offset:32776
ds_read_b64_tr_b16 a[46:47],v16 offset:33288
s_waitcnt lgkmcnt(8)
s_waitcnt vmcnt(16)
s_add_u32 m0,s88,16384
s_add_u32 s61,s82,524288
buffer_load_dwordx4 v12,s[40:43],s61 offen offset:0  lds
s_add_u32 m0,s88,17408
ds_read_b64_tr_b16 a[48:49],v28 offset:0
buffer_load_dwordx4 v13,s[40:43],s61 offen offset:0  lds
s_add_u32 m0,s88,18432
s_add_u32 s61,s82,557056
buffer_load_dwordx4 v12,s[40:43],s61 offen offset:0  lds
s_add_u32 m0,s88,19456
ds_read_b64_tr_b16 a[50:51],v28 offset:512
buffer_load_dwordx4 v13,s[40:43],s61 offen offset:0  lds
ds_read_b64_tr_b16 a[52:53],v28 offset:8
ds_read_b64_tr_b16 a[54:55],v28 offset:520
ds_read_b64_tr_b16 a[56:57],v30 offset:0
ds_read_b64_tr_b16 a[58:59],v30 offset:512
ds_read_b64_tr_b16 a[60:61],v30 offset:8
ds_read_b64_tr_b16 a[62:63],v30 offset:520
s_waitcnt lgkmcnt(8)
s_waitcnt vmcnt(16)
s_add_u32 m0,s88,32768
s_add_u32 s61,s82,589824
buffer_load_dwordx4 v12,s[40:43],s61 offen offset:0  lds
s_add_u32 m0,s88,33792
ds_read_b64_tr_b16 a[64:65],v28 offset:16384
buffer_load_dwordx4 v13,s[40:43],s61 offen offset:0  lds
s_add_u32 m0,s88,34816
s_add_u32 s61,s82,622592
buffer_load_dwordx4 v12,s[40:43],s61 offen offset:0  lds
s_add_u32 m0,s88,35840
ds_read_b64_tr_b16 a[66:67],v28 offset:16896
buffer_load_dwordx4 v13,s[40:43],s61 offen offset:0  lds
ds_read_b64_tr_b16 a[68:69],v28 offset:16392
ds_read_b64_tr_b16 a[70:71],v28 offset:16904
ds_read_b64_tr_b16 a[72:73],v30 offset:16384
ds_read_b64_tr_b16 a[74:75],v30 offset:16896
ds_read_b64_tr_b16 a[76:77],v30 offset:16392
ds_read_b64_tr_b16 a[78:79],v30 offset:16904
s_waitcnt lgkmcnt(8)
s_waitcnt vmcnt(16)
s_add_u32 m0,s88,49152
s_add_u32 s61,s82,655360
buffer_load_dwordx4 v12,s[40:43],s61 offen offset:0  lds)"
R"(
s_add_u32 m0,s88,50176
ds_read_b64_tr_b16 a[80:81],v28 offset:32768
buffer_load_dwordx4 v13,s[40:43],s61 offen offset:0  lds
s_add_u32 m0,s88,51200
s_add_u32 s61,s82,688128
buffer_load_dwordx4 v12,s[40:43],s61 offen offset:0  lds
s_add_u32 m0,s88,52224
ds_read_b64_tr_b16 a[82:83],v28 offset:33280
buffer_load_dwordx4 v13,s[40:43],s61 offen offset:0  lds
ds_read_b64_tr_b16 a[84:85],v28 offset:32776
ds_read_b64_tr_b16 a[86:87],v28 offset:33288
ds_read_b64_tr_b16 a[88:89],v30 offset:32768
ds_read_b64_tr_b16 a[90:91],v30 offset:33280
ds_read_b64_tr_b16 a[92:93],v30 offset:32776
ds_read_b64_tr_b16 a[94:95],v30 offset:33288
s_waitcnt lgkmcnt(8)
s_waitcnt vmcnt(16)
s_add_u32 m0,s88,65536
s_add_u32 s61,s82,720896
buffer_load_dwordx4 v12,s[40:43],s61 offen offset:0  lds
s_add_u32 m0,s88,66560
ds_read_b64_tr_b16 a[96:97],v32 offset:0
buffer_load_dwordx4 v13,s[40:43],s61 offen offset:0  lds
s_add_u32 m0,s88,67584
s_add_u32 s61,s82,753664
buffer_load_dwordx4 v12,s[40:43],s61 offen offset:0  lds
s_add_u32 m0,s88,68608
ds_read_b64_tr_b16 a[98:99],v32 offset:512
buffer_load_dwordx4 v13,s[40:43],s61 offen offset:0  lds
ds_read_b64_tr_b16 a[100:101],v32 offset:8
ds_read_b64_tr_b16 a[102:103],v32 offset:520
ds_read_b64_tr_b16 a[104:105],v34 offset:0
ds_read_b64_tr_b16 a[106:107],v34 offset:512
ds_read_b64_tr_b16 a[108:109],v34 offset:8
ds_read_b64_tr_b16 a[110:111],v34 offset:520
s_waitcnt lgkmcnt(8)
s_waitcnt vmcnt(16)
s_add_u32 m0,s88,81920
s_add_u32 s61,s82,786432
buffer_load_dwordx4 v12,s[40:43],s61 offen offset:0  lds
s_add_u32 m0,s88,82944
ds_read_b64_tr_b16 a[112:113],v14 offset:0
buffer_load_dwordx4 v13,s[40:43],s61 offen offset:0  lds
s_add_u32 m0,s88,83968
s_add_u32 s61,s82,819200
buffer_load_dwordx4 v12,s[40:43],s61 offen offset:0  lds
s_add_u32 m0,s88,84992
ds_read_b64_tr_b16 a[114:115],v14 offset:512
buffer_load_dwordx4 v13,s[40:43],s61 offen offset:0  lds
ds_read_b64_tr_b16 a[116:117],v14 offset:8
ds_read_b64_tr_b16 a[118:119],v14 offset:520
ds_read_b64_tr_b16 a[120:121],v16 offset:0
ds_read_b64_tr_b16 a[122:123],v16 offset:512
ds_read_b64_tr_b16 a[124:125],v16 offset:8
ds_read_b64_tr_b16 a[126:127],v16 offset:520
s_waitcnt lgkmcnt(8)
s_waitcnt vmcnt(16)
s_add_u32 m0,s88,98304
s_add_u32 s61,s82,851968
buffer_load_dwordx4 v12,s[40:43],s61 offen offset:0  lds
s_add_u32 m0,s88,99328
ds_read_b64_tr_b16 a[128:129],v14 offset:16384
buffer_load_dwordx4 v13,s[40:43],s61 offen offset:0  lds
s_add_u32 m0,s88,100352
s_add_u32 s61,s82,884736
buffer_load_dwordx4 v12,s[40:43],s61 offen offset:0  lds
s_add_u32 m0,s88,101376
ds_read_b64_tr_b16 a[130:131],v14 offset:16896
buffer_load_dwordx4 v13,s[40:43],s61 offen offset:0  lds
ds_read_b64_tr_b16 a[132:133],v14 offset:16392
ds_read_b64_tr_b16 a[134:135],v14 offset:16904
ds_read_b64_tr_b16 a[136:137],v16 offset:16384
ds_read_b64_tr_b16 a[138:139],v16 offset:16896
ds_read_b64_tr_b16 a[140:141],v16 offset:16392
ds_read_b64_tr_b16 a[142:143],v16 offset:16904
s_waitcnt lgkmcnt(8)
s_waitcnt vmcnt(16)
s_add_u32 m0,s88,0
s_add_u32 s61,s82,917504
buffer_load_dwordx4 v12,s[40:43],s61 offen offset:0  lds
s_add_u32 m0,s88,1024
ds_read_b64_tr_b16 a[144:145],v14 offset:32768
buffer_load_dwordx4 v13,s[40:43],s61 offen offset:0  lds
s_add_u32 m0,s88,2048
s_add_u32 s61,s82,950272
buffer_load_dwordx4 v12,s[40:43],s61 offen offset:0  lds
s_add_u32 m0,s88,3072
ds_read_b64_tr_b16 a[146:147],v14 offset:33280
buffer_load_dwordx4 v13,s[40:43],s61 offen offset:0  lds
ds_read_b64_tr_b16 a[148:149],v14 offset:32776
ds_read_b64_tr_b16 a[150:151],v14 offset:33288
ds_read_b64_tr_b16 a[152:153],v16 offset:32768
ds_read_b64_tr_b16 a[154:155],v16 offset:33280
ds_read_b64_tr_b16 a[156:157],v16 offset:32776
ds_read_b64_tr_b16 a[158:159],v16 offset:33288
s_waitcnt lgkmcnt(8)
s_waitcnt vmcnt(16)
s_add_u32 m0,s88,16384
s_add_u32 s61,s82,983040
buffer_load_dwordx4 v12,s[40:43],s61 offen offset:0  lds
s_add_u32 m0,s88,17408
ds_read_b64_tr_b16 a[160:161],v28 offset:0
buffer_load_dwordx4 v13,s[40:43],s61 offen offset:0  lds
s_add_u32 m0,s88,18432
s_add_u32 s61,s82,1015808
buffer_load_dwordx4 v12,s[40:43],s61 offen offset:0  lds
s_add_u32 m0,s88,19456
ds_read_b64_tr_b16 a[162:163],v28 offset:512
buffer_load_dwordx4 v13,s[40:43],s61 offen offset:0  lds
ds_read_b64_tr_b16 a[164:165],v28 offset:8
ds_read_b64_tr_b16 a[166:167],v28 offset:520
ds_read_b64_tr_b16 a[168:169],v30 offset:0
ds_read_b64_tr_b16 a[170:171],v30 offset:512
ds_read_b64_tr_b16 a[172:173],v30 offset:8
ds_read_b64_tr_b16 a[174:175],v30 offset:520
s_waitcnt lgkmcnt(8)
s_waitcnt vmcnt(16)
s_add_u32 m0,s88,32768
s_add_u32 s61,s82,1048576
buffer_load_dwordx4 v12,s[40:43],s61 offen offset:0  lds
s_add_u32 m0,s88,33792
ds_read_b64_tr_b16 a[176:177],v28 offset:16384
buffer_load_dwordx4 v13,s[40:43],s61 offen offset:0  lds
s_add_u32 m0,s88,34816
s_add_u32 s61,s82,1081344
buffer_load_dwordx4 v12,s[40:43],s61 offen offset:0  lds
s_add_u32 m0,s88,35840
ds_read_b64_tr_b16 a[178:179],v28 offset:16896
buffer_load_dwordx4 v13,s[40:43],s61 offen offset:0  lds
ds_read_b64_tr_b16 v[52:53],v28 offset:16392
ds_read_b64_tr_b16 v[54:55],v28 offset:16904
ds_read_b64_tr_b16 a[180:181],v30 offset:16384
ds_read_b64_tr_b16 a[182:183],v30 offset:16896
ds_read_b64_tr_b16 a[184:185],v30 offset:16392
ds_read_b64_tr_b16 a[186:187],v30 offset:16904
s_waitcnt lgkmcnt(8)
s_waitcnt vmcnt(16)
s_add_u32 m0,s88,49152
s_add_u32 s61,s82,1114112
buffer_load_dwordx4 v12,s[40:43],s61 offen offset:0  lds
s_add_u32 m0,s88,50176
ds_read_b64_tr_b16 a[188:189],v28 offset:32768
buffer_load_dwordx4 v13,s[40:43],s61 offen offset:0  lds
s_add_u32 m0,s88,51200
s_add_u32 s61,s82,1146880
buffer_load_dwordx4 v12,s[40:43],s61 offen offset:0  lds
s_add_u32 m0,s88,52224
ds_read_b64_tr_b16 a[190:191],v28 offset:33280
buffer_load_dwordx4 v13,s[40:43],s61 offen offset:0  lds
ds_read_b64_tr_b16 a[192:193],v28 offset:32776
ds_read_b64_tr_b16 a[194:195],v28 offset:33288
ds_read_b64_tr_b16 a[196:197],v30 offset:32768
ds_read_b64_tr_b16 a[198:199],v30 offset:33280
ds_read_b64_tr_b16 a[200:201],v30 offset:32776
ds_read_b64_tr_b16 a[202:203],v30 offset:33288
s_waitcnt lgkmcnt(8)
ds_write_b128 v10,v[52:55] offset:0
s_waitcnt vmcnt(16)
s_add_u32 m0,s88,65536
s_add_u32 s61,s82,1179648
buffer_load_dwordx4 v12,s[40:43],s61 offen offset:0  lds
s_add_u32 m0,s88,66560
ds_read_b64_tr_b16 a[204:205],v32 offset:0
buffer_load_dwordx4 v13,s[40:43],s61 offen offset:0  lds
s_add_u32 m0,s88,67584
s_add_u32 s61,s82,1212416
buffer_load_dwordx4 v12,s[40:43],s61 offen offset:0  lds
s_add_u32 m0,s88,68608
ds_read_b64_tr_b16 a[206:207],v32 offset:512
buffer_load_dwordx4 v13,s[40:43],s61 offen offset:0  lds
ds_read_b64_tr_b16 v[52:53],v32 offset:8
ds_read_b64_tr_b16 v[54:55],v32 offset:520
ds_read_b64_tr_b16 a[208:209],v34 offset:0
ds_read_b64_tr_b16 a[210:211],v34 offset:512
ds_read_b64_tr_b16 a[212:213],v34 offset:8
ds_read_b64_tr_b16 a[214:215],v34 offset:520
s_waitcnt lgkmcnt(8)
s_waitcnt vmcnt(16)
s_add_u32 m0,s88,81920
s_add_u32 s61,s82,1245184
buffer_load_dwordx4 v12,s[40:43],s61 offen offset:0  lds
s_add_u32 m0,s88,82944
ds_read_b64_tr_b16 a[216:217],v14 offset:0
buffer_load_dwordx4 v13,s[40:43],s61 offen offset:0  lds
s_add_u32 m0,s88,83968
s_add_u32 s61,s82,1277952
buffer_load_dwordx4 v12,s[40:43],s61 offen offset:0  lds
s_add_u32 m0,s88,84992
ds_read_b64_tr_b16 a[218:219],v14 offset:512
buffer_load_dwordx4 v13,s[40:43],s61 offen offset:0  lds
ds_read_b64_tr_b16 a[220:221],v14 offset:8
ds_read_b64_tr_b16 a[222:223],v14 offset:520
ds_read_b64_tr_b16 a[224:225],v16 offset:0
ds_read_b64_tr_b16 a[226:227],v16 offset:512
ds_read_b64_tr_b16 a[228:229],v16 offset:8
ds_read_b64_tr_b16 a[230:231],v16 offset:520
s_waitcnt lgkmcnt(8)
ds_write_b128 v10,v[52:55] offset:1024
s_waitcnt vmcnt(16)
s_add_u32 m0,s88,98304)"
R"(
s_add_u32 s61,s82,1310720
buffer_load_dwordx4 v12,s[40:43],s61 offen offset:0  lds
s_add_u32 m0,s88,99328
ds_read_b64_tr_b16 a[232:233],v14 offset:16384
buffer_load_dwordx4 v13,s[40:43],s61 offen offset:0  lds
s_add_u32 m0,s88,100352
s_add_u32 s61,s82,1343488
buffer_load_dwordx4 v12,s[40:43],s61 offen offset:0  lds
s_add_u32 m0,s88,101376
ds_read_b64_tr_b16 a[234:235],v14 offset:16896
buffer_load_dwordx4 v13,s[40:43],s61 offen offset:0  lds
ds_read_b64_tr_b16 v[52:53],v14 offset:16392
ds_read_b64_tr_b16 v[54:55],v14 offset:16904
ds_read_b64_tr_b16 a[236:237],v16 offset:16384
ds_read_b64_tr_b16 a[238:239],v16 offset:16896
ds_read_b64_tr_b16 a[240:241],v16 offset:16392
ds_read_b64_tr_b16 a[242:243],v16 offset:16904
s_waitcnt lgkmcnt(8)
s_waitcnt vmcnt(16)
s_add_u32 m0,s88,0
s_add_u32 s61,s82,1376256
buffer_load_dwordx4 v12,s[40:43],s61 offen offset:0  lds
s_add_u32 m0,s88,1024
ds_read_b64_tr_b16 a[244:245],v14 offset:32768
buffer_load_dwordx4 v13,s[40:43],s61 offen offset:0  lds
s_add_u32 m0,s88,2048
s_add_u32 s61,s82,1409024
buffer_load_dwordx4 v12,s[40:43],s61 offen offset:0  lds
s_add_u32 m0,s88,3072
ds_read_b64_tr_b16 a[246:247],v14 offset:33280
buffer_load_dwordx4 v13,s[40:43],s61 offen offset:0  lds
ds_read_b64_tr_b16 a[248:249],v14 offset:32776
ds_read_b64_tr_b16 a[250:251],v14 offset:33288
ds_read_b64_tr_b16 a[252:253],v16 offset:32768
ds_read_b64_tr_b16 a[254:255],v16 offset:33280
ds_read_b64_tr_b16 v[100:101],v16 offset:32776
ds_read_b64_tr_b16 v[102:103],v16 offset:33288
s_waitcnt lgkmcnt(8)
ds_write_b128 v10,v[52:55] offset:2048
s_waitcnt vmcnt(16)
s_add_u32 m0,s88,16384
s_add_u32 s61,s82,1441792
buffer_load_dwordx4 v12,s[40:43],s61 offen offset:0  lds
s_add_u32 m0,s88,17408
ds_read_b64_tr_b16 v[104:105],v28 offset:0
buffer_load_dwordx4 v13,s[40:43],s61 offen offset:0  lds
s_add_u32 m0,s88,18432
s_add_u32 s61,s82,1474560
buffer_load_dwordx4 v12,s[40:43],s61 offen offset:0  lds
s_add_u32 m0,s88,19456
ds_read_b64_tr_b16 v[106:107],v28 offset:512
buffer_load_dwordx4 v13,s[40:43],s61 offen offset:0  lds
ds_read_b64_tr_b16 v[52:53],v28 offset:8
ds_read_b64_tr_b16 v[54:55],v28 offset:520
ds_read_b64_tr_b16 v[108:109],v30 offset:0
ds_read_b64_tr_b16 v[110:111],v30 offset:512
ds_read_b64_tr_b16 v[112:113],v30 offset:8
ds_read_b64_tr_b16 v[114:115],v30 offset:520
s_waitcnt lgkmcnt(8)
s_waitcnt vmcnt(16)
s_add_u32 m0,s88,32768
s_add_u32 s61,s82,1507328
buffer_load_dwordx4 v12,s[40:43],s61 offen offset:0  lds
s_add_u32 m0,s88,33792
ds_read_b64_tr_b16 v[116:117],v28 offset:16384
buffer_load_dwordx4 v13,s[40:43],s61 offen offset:0  lds
s_add_u32 m0,s88,34816
s_add_u32 s61,s82,1540096
buffer_load_dwordx4 v12,s[40:43],s61 offen offset:0  lds
s_add_u32 m0,s88,35840
ds_read_b64_tr_b16 v[118:119],v28 offset:16896
buffer_load_dwordx4 v13,s[40:43],s61 offen offset:0  lds
ds_read_b64_tr_b16 v[120:121],v28 offset:16392
ds_read_b64_tr_b16 v[122:123],v28 offset:16904
ds_read_b64_tr_b16 v[124:125],v30 offset:16384
ds_read_b64_tr_b16 v[126:127],v30 offset:16896
ds_read_b64_tr_b16 v[128:129],v30 offset:16392
ds_read_b64_tr_b16 v[130:131],v30 offset:16904
s_waitcnt lgkmcnt(8)
ds_write_b128 v10,v[52:55] offset:3072
s_waitcnt vmcnt(16)
s_add_u32 m0,s88,49152
s_add_u32 s61,s82,1572864
buffer_load_dwordx4 v12,s[40:43],s61 offen offset:0  lds
s_add_u32 m0,s88,50176
ds_read_b64_tr_b16 v[132:133],v28 offset:32768
buffer_load_dwordx4 v13,s[40:43],s61 offen offset:0  lds
s_add_u32 m0,s88,51200
s_add_u32 s61,s82,1605632
buffer_load_dwordx4 v12,s[40:43],s61 offen offset:0  lds
s_add_u32 m0,s88,52224
ds_read_b64_tr_b16 v[134:135],v28 offset:33280
buffer_load_dwordx4 v13,s[40:43],s61 offen offset:0  lds
ds_read_b64_tr_b16 v[52:53],v28 offset:32776
ds_read_b64_tr_b16 v[54:55],v28 offset:33288
ds_read_b64_tr_b16 v[136:137],v30 offset:32768
ds_read_b64_tr_b16 v[138:139],v30 offset:33280
ds_read_b64_tr_b16 v[140:141],v30 offset:32776
ds_read_b64_tr_b16 v[142:143],v30 offset:33288
s_waitcnt lgkmcnt(8)
s_waitcnt vmcnt(16)
s_add_u32 m0,s88,65536
s_add_u32 s61,s82,1638400
buffer_load_dwordx4 v12,s[40:43],s61 offen offset:0  lds
s_add_u32 m0,s88,66560
ds_read_b64_tr_b16 v[144:145],v32 offset:0
buffer_load_dwordx4 v13,s[40:43],s61 offen offset:0  lds
s_add_u32 m0,s88,67584
s_add_u32 s61,s82,1671168
buffer_load_dwordx4 v12,s[40:43],s61 offen offset:0  lds
s_add_u32 m0,s88,68608
ds_read_b64_tr_b16 v[146:147],v32 offset:512
buffer_load_dwordx4 v13,s[40:43],s61 offen offset:0  lds
ds_read_b64_tr_b16 v[148:149],v32 offset:8
ds_read_b64_tr_b16 v[150:151],v32 offset:520
ds_read_b64_tr_b16 v[152:153],v34 offset:0
ds_read_b64_tr_b16 v[154:155],v34 offset:512
ds_read_b64_tr_b16 v[156:157],v34 offset:8
ds_read_b64_tr_b16 v[158:159],v34 offset:520
s_waitcnt lgkmcnt(8)
ds_write_b128 v10,v[52:55] offset:4096
s_waitcnt vmcnt(16)
s_add_u32 m0,s88,81920
s_add_u32 s61,s82,1703936
buffer_load_dwordx4 v12,s[40:43],s61 offen offset:0  lds
s_add_u32 m0,s88,82944
ds_read_b64_tr_b16 v[160:161],v14 offset:0
buffer_load_dwordx4 v13,s[40:43],s61 offen offset:0  lds
s_add_u32 m0,s88,83968
s_add_u32 s61,s82,1736704
buffer_load_dwordx4 v12,s[40:43],s61 offen offset:0  lds
s_add_u32 m0,s88,84992
ds_read_b64_tr_b16 v[162:163],v14 offset:512
buffer_load_dwordx4 v13,s[40:43],s61 offen offset:0  lds
ds_read_b64_tr_b16 v[52:53],v14 offset:8
ds_read_b64_tr_b16 v[54:55],v14 offset:520
ds_read_b64_tr_b16 v[164:165],v16 offset:0
ds_read_b64_tr_b16 v[166:167],v16 offset:512
ds_read_b64_tr_b16 v[168:169],v16 offset:8
ds_read_b64_tr_b16 v[170:171],v16 offset:520
s_waitcnt lgkmcnt(8)
s_waitcnt vmcnt(16)
s_add_u32 m0,s88,98304
s_add_u32 s61,s82,1769472
buffer_load_dwordx4 v12,s[40:43],s61 offen offset:0  lds
s_add_u32 m0,s88,99328
ds_read_b64_tr_b16 v[172:173],v14 offset:16384
buffer_load_dwordx4 v13,s[40:43],s61 offen offset:0  lds
s_add_u32 m0,s88,100352
s_add_u32 s61,s82,1802240
buffer_load_dwordx4 v12,s[40:43],s61 offen offset:0  lds
s_add_u32 m0,s88,101376
ds_read_b64_tr_b16 v[174:175],v14 offset:16896
buffer_load_dwordx4 v13,s[40:43],s61 offen offset:0  lds
ds_read_b64_tr_b16 v[176:177],v14 offset:16392
ds_read_b64_tr_b16 v[178:179],v14 offset:16904
ds_read_b64_tr_b16 v[180:181],v16 offset:16384
ds_read_b64_tr_b16 v[182:183],v16 offset:16896
ds_read_b64_tr_b16 v[184:185],v16 offset:16392
ds_read_b64_tr_b16 v[186:187],v16 offset:16904
s_waitcnt lgkmcnt(8)
ds_write_b128 v10,v[52:55] offset:5120
s_waitcnt vmcnt(16)
ds_read_b64_tr_b16 v[188:189],v14 offset:32768
ds_read_b64_tr_b16 v[190:191],v14 offset:33280
ds_read_b64_tr_b16 v[52:53],v14 offset:32776
ds_read_b64_tr_b16 v[54:55],v14 offset:33288
ds_read_b64_tr_b16 v[192:193],v16 offset:32768
ds_read_b64_tr_b16 v[194:195],v16 offset:33280
ds_read_b64_tr_b16 v[196:197],v16 offset:32776
ds_read_b64_tr_b16 v[198:199],v16 offset:33288
s_getpc_b64 s[46:47]
Lg4_cp_%=:
s_add_u32 s46,s46,Lg4_init_%=-Lg4_cp_%=
s_addc_u32 s47,s47,0
s_lshl_b32 s68,s54,13
v_lshl_add_u32 v84,v2,3,s68
v_min_u32 v85,Lg4_done_%=-Lg4_init_%=,v84
global_load_dword v88,v85,s[46:47]
s_waitcnt lgkmcnt(8)
s_waitcnt vmcnt(13)
ds_read_b64_tr_b16 v[200:201],v28 offset:0
ds_read_b64_tr_b16 v[202:203],v28 offset:512
ds_read_b64_tr_b16 v[204:205],v28 offset:8
ds_read_b64_tr_b16 v[206:207],v28 offset:520
ds_read_b64_tr_b16 v[208:209],v30 offset:0
ds_read_b64_tr_b16 v[210:211],v30 offset:512
ds_read_b64_tr_b16 v[212:213],v30 offset:8
ds_read_b64_tr_b16 v[214:215],v30 offset:520
s_waitcnt lgkmcnt(8)
ds_write_b128 v10,v[52:55] offset:6144
s_waitcnt vmcnt(9)
ds_read_b64_tr_b16 v[216:217],v28 offset:16384
ds_read_b64_tr_b16 v[218:219],v28 offset:16896
ds_read_b64_tr_b16 v[52:53],v28 offset:16392
ds_read_b64_tr_b16 v[54:55],v28 offset:16904
ds_read_b64_tr_b16 v[220:221],v30 offset:16384
ds_read_b64_tr_b16 v[222:223],v30 offset:16896
ds_read_b64_tr_b16 v[224:225],v30 offset:16392
ds_read_b64_tr_b16 v[226:227],v30 offset:16904)"
R"(
s_waitcnt lgkmcnt(8)
s_waitcnt vmcnt(5)
ds_read_b64_tr_b16 v[228:229],v28 offset:32768
ds_read_b64_tr_b16 v[230:231],v28 offset:33280
ds_read_b64_tr_b16 v[232:233],v28 offset:32776
ds_read_b64_tr_b16 v[234:235],v28 offset:33288
ds_read_b64_tr_b16 v[236:237],v30 offset:32768
ds_read_b64_tr_b16 v[238:239],v30 offset:33280
ds_read_b64_tr_b16 v[240:241],v30 offset:32776
ds_read_b64_tr_b16 v[242:243],v30 offset:33288
s_waitcnt lgkmcnt(8)
ds_write_b128 v10,v[52:55] offset:7168
s_waitcnt vmcnt(1)
ds_read_b64_tr_b16 v[244:245],v32 offset:0
ds_read_b64_tr_b16 v[246:247],v32 offset:512
ds_read_b64_tr_b16 v[52:53],v32 offset:8
ds_read_b64_tr_b16 v[54:55],v32 offset:520
ds_read_b64_tr_b16 v[248:249],v34 offset:0
ds_read_b64_tr_b16 v[250:251],v34 offset:512
ds_read_b64_tr_b16 v[252:253],v34 offset:8
ds_read_b64_tr_b16 v[254:255],v34 offset:520
s_waitcnt lgkmcnt(8)
s_waitcnt lgkmcnt(0)
ds_write_b128 v10,v[52:55] offset:8192
s_waitcnt lgkmcnt(0)
s_barrier
Lg4_init_%=:
v_mov_b32 v6,s60
s_mov_b32 s59,536870912
s_add_u32 s57,s56,131072
s_mov_b32 s79,0
s_mov_b32 s75,0x3a800000
s_mov_b32 s76,0xc0135761
s_mov_b32 s77,0x3d372713
s_mov_b32 s44,s59
s_mov_b32 s45,s59
s_mov_b32 s46,s59
s_mov_b32 s47,s59
s_mov_b32 s48,s83
s_add_u32 s49,s48,s55
s_cmp_eq_u32 s49,s56
s_cselect_b32 s49,s57,s49
s_cmp_eq_u32 s49,s58
s_cselect_b32 s49,s59,s49
s_cmp_ge_u32 s49,6520832
s_cselect_b32 s55,196608,s55
s_add_u32 s50,s49,s55
s_cmp_eq_u32 s50,s56
s_cselect_b32 s50,s57,s50
s_cmp_eq_u32 s50,s58
s_cselect_b32 s50,s59,s50
s_cmp_ge_u32 s50,6520832
s_cselect_b32 s55,196608,s55
s_add_u32 s51,s50,s55
s_cmp_eq_u32 s51,s56
s_cselect_b32 s51,s57,s51
s_cmp_eq_u32 s51,s58
s_cselect_b32 s51,s59,s51
s_cmp_ge_u32 s51,6520832
s_cselect_b32 s55,196608,s55
s_add_u32 s52,s51,s55
s_cmp_eq_u32 s52,s56
s_cselect_b32 s52,s57,s52
s_cmp_eq_u32 s52,s58
s_cselect_b32 s52,s59,s52
s_cmp_ge_u32 s52,6520832
s_cselect_b32 s55,196608,s55
s_lshl_b32 s63,s54,12
s_mul_i32 s64,s54,3072
s_add_u32 s64,s64,16384
s_lshl_b32 s78,s54,8
s_add_u32 s78,s78,65536
s_getpc_b64 s[90:91]
Lg4_pc_%=:
s_mov_b32 s68,Lg4_d1w0_%=-Lg4_pc_%=
s_cmp_eq_u32 s54,1
s_cselect_b32 s68,Lg4_d1w1_%=-Lg4_pc_%=,s68
s_cmp_eq_u32 s54,2
s_cselect_b32 s68,Lg4_d1w2_%=-Lg4_pc_%=,s68
s_cmp_eq_u32 s54,3
s_cselect_b32 s68,Lg4_d1w3_%=-Lg4_pc_%=,s68
s_add_u32 s92,s90,s68
s_addc_u32 s93,s91,0
s_mov_b32 s68,Lg4_d0w0_%=-Lg4_pc_%=
s_cmp_eq_u32 s54,1
s_cselect_b32 s68,Lg4_d0w1_%=-Lg4_pc_%=,s68
s_cmp_eq_u32 s54,2
s_cselect_b32 s68,Lg4_d0w2_%=-Lg4_pc_%=,s68
s_cmp_eq_u32 s54,3
s_cselect_b32 s68,Lg4_d0w3_%=-Lg4_pc_%=,s68
s_add_u32 s90,s90,s68
s_addc_u32 s91,s91,0
s_mov_b32 s66,s59
s_mov_b32 s65,0
s_mov_b32 s62,s63
s_branch Lg4_slowb0_%=
Lg4_d0w0_%=:
v_mfma_f32_16x16x32_bf16 v[24:27],a[12:15],v[44:47],0
s_mov_b32 s73,s47
ds_read_b128 v[56:59],v2 offset:3072
v_lshlrev_b32 v72,16,v80
v_mfma_f32_16x16x32_bf16 v[24:27],a[28:31],v[48:51],v[24:27]
s_max_u32 s73,s73,s65
v_and_b32 v73,0xffff0000,v80
v_lshlrev_b32 v74,16,v81
v_mfma_f32_16x16x32_bf16 v[20:23],a[24:27],v[48:51],0
s_mov_b32 s65,0
v_and_b32 v75,0xffff0000,v81
v_lshlrev_b32 v76,16,v82
v_mfma_f32_16x16x32_bf16 v[20:23],a[8:11],v[44:47],v[20:23]
s_mul_i32 s61,s49,7
v_and_b32 v77,0xffff0000,v82
v_lshlrev_b32 v78,16,v83
v_mfma_f32_16x16x32_bf16 v[16:19],a[4:7],v[44:47],0
s_lshr_b32 s61,s61,1
ds_read_b128 v[60:63],v2 offset:4096
v_and_b32 v79,0xffff0000,v83
v_mfma_f32_16x16x32_bf16 v[16:19],a[20:23],v[48:51],v[16:19]
v_cvt_pk_bf16_f32 v28,v28,v29
v_cvt_pk_bf16_f32 v29,v30,v31
v_mfma_f32_16x16x32_bf16 v[12:15],a[16:19],v[48:51],0
v_cvt_pk_bf16_f32 v30,v32,v33
v_cvt_pk_bf16_f32 v31,v34,v35
v_mfma_f32_16x16x32_bf16 v[12:15],a[0:3],v[44:47],v[12:15]
s_waitcnt lgkmcnt(1)
buffer_store_dwordx4 v[28:31],v3,s[36:39],s73 offen offset:0 sc1
v_add_f32 v36,v36,v72
v_add_f32 v37,v37,v73
v_add_f32 v38,v38,v74
v_mfma_f32_16x16x32_bf16 v[12:15],a[32:35],v[52:55],v[12:15]
ds_read_b128 v[64:67],v2 offset:5120
v_add_f32 v39,v39,v75
v_mfma_f32_16x16x32_bf16 v[12:15],a[48:51],v[56:59],v[12:15]
v_add_f32 v40,v40,v76
v_add_f32 v41,v41,v77
v_mfma_f32_16x16x32_bf16 v[16:19],a[52:55],v[56:59],v[16:19]
v_add_f32 v42,v42,v78
v_add_f32 v43,v43,v79
v_mfma_f32_16x16x32_bf16 v[16:19],a[36:39],v[52:55],v[16:19]
v_cvt_pk_bf16_f32 v36,v36,v37
v_cvt_pk_bf16_f32 v37,v38,v39
v_mfma_f32_16x16x32_bf16 v[20:23],a[40:43],v[52:55],v[20:23]
s_waitcnt vmcnt(1)
ds_read_b128 v[68:71],v2 offset:6144
v_cvt_pk_bf16_f32 v38,v40,v41
v_mfma_f32_16x16x32_bf16 v[20:23],a[56:59],v[56:59],v[20:23]
s_barrier
v_mfma_f32_16x16x32_bf16 v[24:27],a[60:63],v[56:59],v[24:27]
ds_read_b32 v11,v8 offset:4096
v_cvt_pk_bf16_f32 v39,v42,v43
v_mfma_f32_16x16x32_bf16 v[24:27],a[44:47],v[52:55],v[24:27]
s_waitcnt lgkmcnt(2)
v_mfma_f32_16x16x32_bf16 v[24:27],a[76:79],v[60:63],v[24:27]
ds_read_b128 v[44:47],v2 offset:7168
v_mfma_f32_16x16x32_bf16 v[24:27],a[92:95],v[64:67],v[24:27]
v_mfma_f32_16x16x32_bf16 v[20:23],a[88:91],v[64:67],v[20:23]
s_waitcnt lgkmcnt(1)
v_mfma_f32_16x16x32_bf16 v[20:23],a[72:75],v[60:63],v[20:23]
buffer_store_dwordx4 v[36:39],v4,s[36:39],s73 offen offset:0 sc1
v_cmp_eq_u32 vcc,s60,v11
v_mfma_f32_16x16x32_bf16 v[16:19],a[68:71],v[60:63],v[16:19]
s_cmp_eq_u32 vcc_lo,-1
ds_read_b128 v[48:51],v2 offset:8192
s_cselect_b32 s66,s49,s59
v_mfma_f32_16x16x32_bf16 v[16:19],a[84:87],v[64:67],v[16:19]
s_cselect_b32 s61,s61,s59
s_add_u32 m0,s63,28672
s_cmp_ge_u32 s66,s59
v_mfma_f32_16x16x32_bf16 v[12:15],a[80:83],v[64:67],v[12:15]
s_cselect_b32 s68,s49,s59
s_mul_i32 s68,s68,5
s_lshr_b32 s68,s68,6
v_mfma_f32_16x16x32_bf16 v[12:15],a[64:67],v[60:63],v[12:15]
s_waitcnt lgkmcnt(1)
buffer_load_dwordx4 v0,s[36:39],s61 offen offset:0 sc1 lds
v_mfma_f32_16x16x32_bf16 v[12:15],a[96:99],v[68:71],v[12:15]
ds_read_b128 v[52:55],v2 offset:9216
v_mfma_f32_16x16x32_bf16 v[12:15],a[112:115],v[44:47],v[12:15]
v_mfma_f32_16x16x32_bf16 v[16:19],a[116:119],v[44:47],v[16:19]
v_mfma_f32_16x16x32_bf16 v[16:19],a[100:103],v[68:71],v[16:19]
buffer_load_dwordx4 v0,s[36:39],s61 offen offset:1024 sc1 lds
v_mfma_f32_16x16x32_bf16 v[20:23],a[104:107],v[68:71],v[20:23]
ds_read_b128 v[56:59],v2 offset:10240
v_mfma_f32_16x16x32_bf16 v[20:23],a[120:123],v[44:47],v[20:23]
v_mfma_f32_16x16x32_bf16 v[24:27],a[124:127],v[44:47],v[24:27]
v_mfma_f32_16x16x32_bf16 v[24:27],a[108:111],v[68:71],v[24:27]
s_waitcnt lgkmcnt(1)
buffer_load_dwordx4 v0,s[36:39],s61 offen offset:2048 sc1 lds
v_mfma_f32_16x16x32_bf16 v[24:27],a[140:143],v[48:51],v[24:27]
ds_read_b128 v[60:63],v2 offset:11264
v_mfma_f32_16x16x32_bf16 v[24:27],a[156:159],v[52:55],v[24:27]
ds_read_b128 v[88:91],v10 offset:0
v_mfma_f32_16x16x32_bf16 v[20:23],a[152:155],v[52:55],v[20:23]
v_mfma_f32_16x16x32_bf16 v[20:23],a[136:139],v[48:51],v[20:23]
buffer_load_dwordx4 v0,s[36:39],s61 offen offset:3072 sc1 lds
v_mfma_f32_16x16x32_bf16 v[16:19],a[132:135],v[48:51],v[16:19]
s_add_u32 m0,s78,6144
ds_read_b128 v[64:67],v2 offset:12288
s_add_u32 s53,s52,s55
v_mfma_f32_16x16x32_bf16 v[16:19],a[148:151],v[52:55],v[16:19])"
R"(
s_cmp_eq_u32 s53,s56
s_cselect_b32 s53,s57,s53
s_cmp_eq_u32 s53,s58
v_mfma_f32_16x16x32_bf16 v[12:15],a[144:147],v[52:55],v[12:15]
s_cselect_b32 s53,s59,s53
s_cmp_ge_u32 s53,6520832
s_cselect_b32 s55,196608,s55
v_mfma_f32_16x16x32_bf16 v[12:15],a[128:131],v[48:51],v[12:15]
s_waitcnt lgkmcnt(1)
v_mfma_f32_16x16x32_bf16 v[12:15],a[160:163],v[56:59],v[12:15]
buffer_load_dwordx4 v[80:83],v9,s[36:39],s48 offen offset:0 sc1
v_mfma_f32_16x16x32_bf16 v[12:15],a[176:179],v[60:63],v[12:15]
ds_read_b128 v[68:71],v2 offset:13312
ds_read_b128 v[84:87],v10 offset:1024
v_mfma_f32_16x16x32_bf16 v[16:19],v[88:91],v[60:63],v[16:19]
v_mfma_f32_16x16x32_bf16 v[16:19],a[164:167],v[56:59],v[16:19]
v_mfma_f32_16x16x32_bf16 v[20:23],a[168:171],v[56:59],v[20:23]
ds_read_b128 v[44:47],v2 offset:14336
v_mfma_f32_16x16x32_bf16 v[20:23],a[180:183],v[60:63],v[20:23]
buffer_load_dword v7,s[36:39],s68 offen offset:0 sc1 lds
v_mfma_f32_16x16x32_bf16 v[24:27],a[184:187],v[60:63],v[24:27]
s_add_u32 m0,s64,28672
s_mul_i32 s68,s50,5
s_lshr_b32 s68,s68,6
v_mfma_f32_16x16x32_bf16 v[24:27],a[172:175],v[56:59],v[24:27]
s_waitcnt lgkmcnt(1)
v_mfma_f32_16x16x32_bf16 v[24:27],a[200:203],v[64:67],v[24:27]
ds_read_b128 v[48:51],v2 offset:15360
v_mfma_f32_16x16x32_bf16 v[24:27],a[212:215],v[68:71],v[24:27]
ds_read_b128 v[88:91],v10 offset:2048
v_mfma_f32_16x16x32_bf16 v[20:23],a[208:211],v[68:71],v[20:23]
s_waitcnt vmcnt(9)
v_mfma_f32_16x16x32_bf16 v[20:23],a[196:199],v[64:67],v[20:23]
s_barrier
v_mfma_f32_16x16x32_bf16 v[16:19],a[192:195],v[64:67],v[16:19]
ds_read_b128 v[52:55],v2 offset:16384
v_mfma_f32_16x16x32_bf16 v[16:19],v[84:87],v[68:71],v[16:19]
buffer_load_dwordx4 v1,s[36:39],s61 offen offset:0 sc1 lds
v_mfma_f32_16x16x32_bf16 v[12:15],a[204:207],v[68:71],v[12:15]
v_mfma_f32_16x16x32_bf16 v[12:15],a[188:191],v[64:67],v[12:15]
s_waitcnt lgkmcnt(1)
v_mfma_f32_16x16x32_bf16 v[12:15],a[216:219],v[44:47],v[12:15]
ds_read_b128 v[56:59],v2 offset:17408
v_mfma_f32_16x16x32_bf16 v[12:15],a[232:235],v[48:51],v[12:15]
ds_read_b128 v[84:87],v10 offset:3072
v_mfma_f32_16x16x32_bf16 v[16:19],v[88:91],v[48:51],v[16:19]
v_mfma_f32_16x16x32_bf16 v[16:19],a[220:223],v[44:47],v[16:19]
buffer_load_dwordx4 v1,s[36:39],s61 offen offset:1024 sc1 lds
v_mfma_f32_16x16x32_bf16 v[20:23],a[224:227],v[44:47],v[20:23]
ds_read_b128 v[60:63],v2 offset:18432
v_mfma_f32_16x16x32_bf16 v[20:23],a[236:239],v[48:51],v[20:23]
v_mfma_f32_16x16x32_bf16 v[24:27],a[240:243],v[48:51],v[24:27]
v_mfma_f32_16x16x32_bf16 v[24:27],a[228:231],v[44:47],v[24:27]
s_waitcnt lgkmcnt(1)
v_mfma_f32_16x16x32_bf16 v[24:27],v[100:103],v[52:55],v[24:27]
ds_read_b128 v[64:67],v2 offset:19456
v_mfma_f32_16x16x32_bf16 v[24:27],v[112:115],v[56:59],v[24:27]
buffer_load_dwordx4 v1,s[36:39],s61 offen offset:2048 sc1 lds
v_mfma_f32_16x16x32_bf16 v[20:23],v[108:111],v[56:59],v[20:23]
s_add_u32 m0,s78,5120
ds_read_b128 v[88:91],v10 offset:4096
s_cmp_ge_u32 s66,s59
v_mfma_f32_16x16x32_bf16 v[20:23],a[252:255],v[52:55],v[20:23]
v_mfma_f32_16x16x32_bf16 v[16:19],a[248:251],v[52:55],v[16:19]
ds_read_b128 v[68:71],v2 offset:20480
v_mfma_f32_16x16x32_bf16 v[16:19],v[84:87],v[56:59],v[16:19]
v_mfma_f32_16x16x32_bf16 v[12:15],v[104:107],v[56:59],v[12:15]
v_mfma_f32_16x16x32_bf16 v[12:15],a[244:247],v[52:55],v[12:15]
s_waitcnt lgkmcnt(1)
v_mfma_f32_16x16x32_bf16 v[12:15],v[116:119],v[60:63],v[12:15]
buffer_load_dword v7,s[36:39],s68 offen offset:0 sc1 lds
v_mfma_f32_16x16x32_bf16 v[12:15],v[132:135],v[64:67],v[12:15]
ds_read_b128 v[44:47],v2 offset:21504
ds_read_b128 v[84:87],v10 offset:5120
v_mfma_f32_16x16x32_bf16 v[16:19],v[88:91],v[64:67],v[16:19]
v_mfma_f32_16x16x32_bf16 v[16:19],v[120:123],v[60:63],v[16:19]
v_mfma_f32_16x16x32_bf16 v[20:23],v[124:127],v[60:63],v[20:23]
ds_read_b128 v[48:51],v2 offset:22528
v_mfma_f32_16x16x32_bf16 v[20:23],v[136:139],v[64:67],v[20:23]
v_mfma_f32_16x16x32_bf16 v[24:27],v[140:143],v[64:67],v[24:27]
v_mfma_f32_16x16x32_bf16 v[24:27],v[128:131],v[60:63],v[24:27]
s_waitcnt lgkmcnt(1)
v_mfma_f32_16x16x32_bf16 v[24:27],v[156:159],v[68:71],v[24:27]
ds_read_b128 v[52:55],v2 offset:23552
v_mfma_f32_16x16x32_bf16 v[24:27],v[168:171],v[44:47],v[24:27]
ds_read_b128 v[88:91],v10 offset:6144
v_mfma_f32_16x16x32_bf16 v[20:23],v[164:167],v[44:47],v[20:23]
v_mfma_f32_16x16x32_bf16 v[20:23],v[152:155],v[68:71],v[20:23]
v_mfma_f32_16x16x32_bf16 v[16:19],v[148:151],v[68:71],v[16:19]
s_waitcnt vmcnt(10)
ds_read_b128 v[56:59],v2 offset:24576
v_mfma_f32_16x16x32_bf16 v[16:19],v[84:87],v[44:47],v[16:19]
s_barrier
v_mfma_f32_16x16x32_bf16 v[12:15],v[160:163],v[44:47],v[12:15]
v_mfma_f32_16x16x32_bf16 v[12:15],v[144:147],v[68:71],v[12:15]
s_waitcnt lgkmcnt(1)
v_mfma_f32_16x16x32_bf16 v[12:15],v[172:175],v[48:51],v[12:15]
ds_read_b128 v[60:63],v2 offset:25600
v_mfma_f32_16x16x32_bf16 v[12:15],v[188:191],v[52:55],v[12:15]
ds_read_b128 v[84:87],v10 offset:7168
v_mfma_f32_16x16x32_bf16 v[16:19],v[88:91],v[52:55],v[16:19]
v_mfma_f32_16x16x32_bf16 v[16:19],v[176:179],v[48:51],v[16:19]
v_mfma_f32_16x16x32_bf16 v[20:23],v[180:183],v[48:51],v[20:23]
ds_read_b128 v[64:67],v2 offset:26624
v_mfma_f32_16x16x32_bf16 v[20:23],v[192:195],v[52:55],v[20:23]
v_mfma_f32_16x16x32_bf16 v[24:27],v[196:199],v[52:55],v[24:27]
v_mfma_f32_16x16x32_bf16 v[24:27],v[184:187],v[48:51],v[24:27]
s_waitcnt lgkmcnt(1)
v_mfma_f32_16x16x32_bf16 v[24:27],v[212:215],v[56:59],v[24:27]
ds_read_b128 v[68:71],v2 offset:27648
v_mfma_f32_16x16x32_bf16 v[24:27],v[224:227],v[60:63],v[24:27]
ds_read_b128 v[88:91],v10 offset:8192
v_mfma_f32_16x16x32_bf16 v[20:23],v[220:223],v[60:63],v[20:23]
s_waitcnt vmcnt(4)
v_mfma_f32_16x16x32_bf16 v[20:23],v[208:211],v[56:59],v[20:23]
s_barrier
v_mfma_f32_16x16x32_bf16 v[16:19],v[204:207],v[56:59],v[16:19]
ds_read_b128 v[44:47],v2 offset:28672
v_mfma_f32_16x16x32_bf16 v[16:19],v[84:87],v[60:63],v[16:19]
v_mfma_f32_16x16x32_bf16 v[12:15],v[216:219],v[60:63],v[12:15]
v_mfma_f32_16x16x32_bf16 v[12:15],v[200:203],v[56:59],v[12:15]
s_waitcnt lgkmcnt(1)
v_mfma_f32_16x16x32_bf16 v[12:15],v[228:231],v[64:67],v[12:15]
ds_read_b128 v[48:51],v2 offset:29696
v_mfma_f32_16x16x32_bf16 v[12:15],v[244:247],v[68:71],v[12:15]
v_mfma_f32_16x16x32_bf16 v[16:19],v[88:91],v[68:71],v[16:19]
v_mfma_f32_16x16x32_bf16 v[16:19],v[232:235],v[64:67],v[16:19]
v_mfma_f32_16x16x32_bf16 v[20:23],v[236:239],v[64:67],v[20:23]
ds_read_b128 v[52:55],v2 offset:30720
v_mfma_f32_16x16x32_bf16 v[20:23],v[248:251],v[68:71],v[20:23]
v_mfma_f32_16x16x32_bf16 v[24:27],v[252:255],v[68:71],v[24:27]
s_waitcnt lgkmcnt(1)
v_mfma_f32_16x16x32_bf16 v[24:27],v[240:243],v[64:67],v[24:27]
s_cbranch_scc1 Lg4_slow1_%=
Lg4_d1w0_%=:
v_mfma_f32_16x16x32_bf16 v[40:43],a[12:15],v[44:47],0
s_mov_b32 s73,s48
ds_read_b128 v[56:59],v2 offset:31744
v_lshlrev_b32 v72,16,v80
v_mfma_f32_16x16x32_bf16 v[40:43],a[28:31],v[48:51],v[40:43]
s_max_u32 s73,s73,s65
v_and_b32 v73,0xffff0000,v80
v_lshlrev_b32 v74,16,v81
v_mfma_f32_16x16x32_bf16 v[36:39],a[24:27],v[48:51],0
s_mov_b32 s65,0
v_and_b32 v75,0xffff0000,v81
v_lshlrev_b32 v76,16,v82
v_mfma_f32_16x16x32_bf16 v[36:39],a[8:11],v[44:47],v[36:39]
s_mul_i32 s61,s50,7
v_and_b32 v77,0xffff0000,v82
v_lshlrev_b32 v78,16,v83
v_mfma_f32_16x16x32_bf16 v[32:35],a[4:7],v[44:47],0
s_lshr_b32 s61,s61,1
ds_read_b128 v[60:63],v2 offset:32768
v_and_b32 v79,0xffff0000,v83
v_mfma_f32_16x16x32_bf16 v[32:35],a[20:23],v[48:51],v[32:35]
s_mov_b64 s[44:45],s[46:47]
v_cvt_pk_bf16_f32 v12,v12,v13
v_cvt_pk_bf16_f32 v13,v14,v15
v_mfma_f32_16x16x32_bf16 v[28:31],a[16:19],v[48:51],0
s_mov_b64 s[46:47],s[48:49]
v_cvt_pk_bf16_f32 v14,v16,v17
v_cvt_pk_bf16_f32 v15,v18,v19
v_mfma_f32_16x16x32_bf16 v[28:31],a[0:3],v[44:47],v[28:31]
s_waitcnt lgkmcnt(1)
buffer_store_dwordx4 v[12:15],v3,s[36:39],s73 offen offset:0 sc1
v_add_f32 v20,v20,v72
v_add_f32 v21,v21,v73
v_add_f32 v22,v22,v74
v_mfma_f32_16x16x32_bf16 v[28:31],a[32:35],v[52:55],v[28:31]
ds_read_b128 v[64:67],v2 offset:33792
v_add_f32 v23,v23,v75
v_mfma_f32_16x16x32_bf16 v[28:31],a[48:51],v[56:59],v[28:31]
v_add_f32 v24,v24,v76
v_add_f32 v25,v25,v77
v_mfma_f32_16x16x32_bf16 v[32:35],a[52:55],v[56:59],v[32:35]
v_add_f32 v26,v26,v78
v_add_f32 v27,v27,v79
v_mfma_f32_16x16x32_bf16 v[32:35],a[36:39],v[52:55],v[32:35]
v_cvt_pk_bf16_f32 v20,v20,v21
v_cvt_pk_bf16_f32 v21,v22,v23
v_mfma_f32_16x16x32_bf16 v[36:39],a[40:43],v[52:55],v[36:39]
s_waitcnt vmcnt(1)
ds_read_b128 v[68:71],v2 offset:34816
v_cvt_pk_bf16_f32 v22,v24,v25
v_mfma_f32_16x16x32_bf16 v[36:39],a[56:59],v[56:59],v[36:39]
s_barrier
v_mfma_f32_16x16x32_bf16 v[40:43],a[60:63],v[56:59],v[40:43]
ds_read_b32 v11,v8 offset:5120
v_cvt_pk_bf16_f32 v23,v26,v27
v_mfma_f32_16x16x32_bf16 v[40:43],a[44:47],v[52:55],v[40:43]
s_waitcnt lgkmcnt(2)
v_mfma_f32_16x16x32_bf16 v[40:43],a[76:79],v[60:63],v[40:43]
ds_read_b128 v[44:47],v2 offset:35840
v_mfma_f32_16x16x32_bf16 v[40:43],a[92:95],v[64:67],v[40:43]
v_mfma_f32_16x16x32_bf16 v[36:39],a[88:91],v[64:67],v[36:39]
s_waitcnt lgkmcnt(1)
v_mfma_f32_16x16x32_bf16 v[36:39],a[72:75],v[60:63],v[36:39]
buffer_store_dwordx4 v[20:23],v4,s[36:39],s73 offen offset:0 sc1
v_cmp_eq_u32 vcc,s60,v11)"
R"(
v_mfma_f32_16x16x32_bf16 v[32:35],a[68:71],v[60:63],v[32:35]
s_cmp_eq_u32 vcc_lo,-1
ds_read_b128 v[48:51],v2 offset:36864
s_cselect_b32 s67,s50,s59
v_mfma_f32_16x16x32_bf16 v[32:35],a[84:87],v[64:67],v[32:35]
s_cselect_b32 s61,s61,s59
s_add_u32 m0,s63,0
s_cmp_ge_u32 s67,s59
v_mfma_f32_16x16x32_bf16 v[28:31],a[80:83],v[64:67],v[28:31]
s_cselect_b32 s68,s50,s59
s_mul_i32 s68,s68,5
s_lshr_b32 s68,s68,6
v_mfma_f32_16x16x32_bf16 v[28:31],a[64:67],v[60:63],v[28:31]
s_waitcnt lgkmcnt(1)
buffer_load_dwordx4 v0,s[36:39],s61 offen offset:0 sc1 lds
v_mfma_f32_16x16x32_bf16 v[28:31],a[96:99],v[68:71],v[28:31]
ds_read_b128 v[52:55],v2 offset:37888
v_mfma_f32_16x16x32_bf16 v[28:31],a[112:115],v[44:47],v[28:31]
v_mfma_f32_16x16x32_bf16 v[32:35],a[116:119],v[44:47],v[32:35]
v_mfma_f32_16x16x32_bf16 v[32:35],a[100:103],v[68:71],v[32:35]
buffer_load_dwordx4 v0,s[36:39],s61 offen offset:1024 sc1 lds
v_mfma_f32_16x16x32_bf16 v[36:39],a[104:107],v[68:71],v[36:39]
ds_read_b128 v[56:59],v2 offset:38912
v_mfma_f32_16x16x32_bf16 v[36:39],a[120:123],v[44:47],v[36:39]
v_mfma_f32_16x16x32_bf16 v[40:43],a[124:127],v[44:47],v[40:43]
v_mfma_f32_16x16x32_bf16 v[40:43],a[108:111],v[68:71],v[40:43]
s_waitcnt lgkmcnt(1)
buffer_load_dwordx4 v0,s[36:39],s61 offen offset:2048 sc1 lds
v_mfma_f32_16x16x32_bf16 v[40:43],a[140:143],v[48:51],v[40:43]
ds_read_b128 v[60:63],v2 offset:39936
v_mfma_f32_16x16x32_bf16 v[40:43],a[156:159],v[52:55],v[40:43]
ds_read_b128 v[88:91],v10 offset:0
v_mfma_f32_16x16x32_bf16 v[36:39],a[152:155],v[52:55],v[36:39]
v_mfma_f32_16x16x32_bf16 v[36:39],a[136:139],v[48:51],v[36:39]
buffer_load_dwordx4 v0,s[36:39],s61 offen offset:3072 sc1 lds
v_mfma_f32_16x16x32_bf16 v[32:35],a[132:135],v[48:51],v[32:35]
s_add_u32 m0,s78,6144
ds_read_b128 v[64:67],v2 offset:40960
s_add_u32 s74,s53,s55
v_mfma_f32_16x16x32_bf16 v[32:35],a[148:151],v[52:55],v[32:35]
s_cmp_eq_u32 s74,s56
s_cselect_b32 s74,s57,s74
s_cmp_eq_u32 s74,s58
v_mfma_f32_16x16x32_bf16 v[28:31],a[144:147],v[52:55],v[28:31]
s_cselect_b32 s74,s59,s74
s_cmp_ge_u32 s74,6520832
s_cselect_b32 s55,196608,s55
v_mfma_f32_16x16x32_bf16 v[28:31],a[128:131],v[48:51],v[28:31]
s_waitcnt lgkmcnt(1)
v_mfma_f32_16x16x32_bf16 v[28:31],a[160:163],v[56:59],v[28:31]
buffer_load_dwordx4 v[80:83],v9,s[36:39],s49 offen offset:0 sc1
v_mfma_f32_16x16x32_bf16 v[28:31],a[176:179],v[60:63],v[28:31]
s_mov_b64 s[48:49],s[50:51]
ds_read_b128 v[68:71],v2 offset:41984
ds_read_b128 v[84:87],v10 offset:1024
v_mfma_f32_16x16x32_bf16 v[32:35],v[88:91],v[60:63],v[32:35]
v_mfma_f32_16x16x32_bf16 v[32:35],a[164:167],v[56:59],v[32:35]
v_mfma_f32_16x16x32_bf16 v[36:39],a[168:171],v[56:59],v[36:39]
ds_read_b128 v[44:47],v2 offset:43008
v_mfma_f32_16x16x32_bf16 v[36:39],a[180:183],v[60:63],v[36:39]
buffer_load_dword v7,s[36:39],s68 offen offset:0 sc1 lds
v_mfma_f32_16x16x32_bf16 v[40:43],a[184:187],v[60:63],v[40:43]
s_add_u32 m0,s64,0
s_mul_i32 s68,s51,5
s_lshr_b32 s68,s68,6
v_mfma_f32_16x16x32_bf16 v[40:43],a[172:175],v[56:59],v[40:43]
s_waitcnt lgkmcnt(1)
s_mov_b64 s[50:51],s[52:53]
s_mov_b32 s52,s74
v_mfma_f32_16x16x32_bf16 v[40:43],a[200:203],v[64:67],v[40:43]
ds_read_b128 v[48:51],v2 offset:44032
v_mfma_f32_16x16x32_bf16 v[40:43],a[212:215],v[68:71],v[40:43]
ds_read_b128 v[88:91],v10 offset:2048
v_mfma_f32_16x16x32_bf16 v[36:39],a[208:211],v[68:71],v[36:39]
s_waitcnt vmcnt(9)
v_mfma_f32_16x16x32_bf16 v[36:39],a[196:199],v[64:67],v[36:39]
s_barrier
v_mfma_f32_16x16x32_bf16 v[32:35],a[192:195],v[64:67],v[32:35]
ds_read_b128 v[52:55],v2 offset:45056
v_mfma_f32_16x16x32_bf16 v[32:35],v[84:87],v[68:71],v[32:35]
buffer_load_dwordx4 v1,s[36:39],s61 offen offset:0 sc1 lds
v_mfma_f32_16x16x32_bf16 v[28:31],a[204:207],v[68:71],v[28:31]
v_mfma_f32_16x16x32_bf16 v[28:31],a[188:191],v[64:67],v[28:31]
s_waitcnt lgkmcnt(1)
v_mfma_f32_16x16x32_bf16 v[28:31],a[216:219],v[44:47],v[28:31]
ds_read_b128 v[56:59],v2 offset:46080
v_mfma_f32_16x16x32_bf16 v[28:31],a[232:235],v[48:51],v[28:31]
ds_read_b128 v[84:87],v10 offset:3072
v_mfma_f32_16x16x32_bf16 v[32:35],v[88:91],v[48:51],v[32:35]
v_mfma_f32_16x16x32_bf16 v[32:35],a[220:223],v[44:47],v[32:35]
buffer_load_dwordx4 v1,s[36:39],s61 offen offset:1024 sc1 lds
v_mfma_f32_16x16x32_bf16 v[36:39],a[224:227],v[44:47],v[36:39]
ds_read_b128 v[60:63],v2 offset:47104
v_mfma_f32_16x16x32_bf16 v[36:39],a[236:239],v[48:51],v[36:39]
v_mfma_f32_16x16x32_bf16 v[40:43],a[240:243],v[48:51],v[40:43]
v_mfma_f32_16x16x32_bf16 v[40:43],a[228:231],v[44:47],v[40:43]
s_waitcnt lgkmcnt(1)
v_mfma_f32_16x16x32_bf16 v[40:43],v[100:103],v[52:55],v[40:43]
ds_read_b128 v[64:67],v2 offset:48128
v_mfma_f32_16x16x32_bf16 v[40:43],v[112:115],v[56:59],v[40:43]
buffer_load_dwordx4 v1,s[36:39],s61 offen offset:2048 sc1 lds
v_mfma_f32_16x16x32_bf16 v[36:39],v[108:111],v[56:59],v[36:39]
s_add_u32 m0,s78,4096
ds_read_b128 v[88:91],v10 offset:4096
s_cmp_lt_u32 s67,s59
v_mfma_f32_16x16x32_bf16 v[36:39],a[252:255],v[52:55],v[36:39]
v_mfma_f32_16x16x32_bf16 v[32:35],a[248:251],v[52:55],v[32:35]
ds_read_b128 v[68:71],v2 offset:49152
v_mfma_f32_16x16x32_bf16 v[32:35],v[84:87],v[56:59],v[32:35]
v_mfma_f32_16x16x32_bf16 v[28:31],v[104:107],v[56:59],v[28:31]
v_mfma_f32_16x16x32_bf16 v[28:31],a[244:247],v[52:55],v[28:31]
s_waitcnt lgkmcnt(1)
v_mfma_f32_16x16x32_bf16 v[28:31],v[116:119],v[60:63],v[28:31]
buffer_load_dword v7,s[36:39],s68 offen offset:0 sc1 lds
v_mfma_f32_16x16x32_bf16 v[28:31],v[132:135],v[64:67],v[28:31]
ds_read_b128 v[44:47],v2 offset:50176
ds_read_b128 v[84:87],v10 offset:5120
v_mfma_f32_16x16x32_bf16 v[32:35],v[88:91],v[64:67],v[32:35]
v_mfma_f32_16x16x32_bf16 v[32:35],v[120:123],v[60:63],v[32:35]
v_mfma_f32_16x16x32_bf16 v[36:39],v[124:127],v[60:63],v[36:39]
ds_read_b128 v[48:51],v2 offset:51200
v_mfma_f32_16x16x32_bf16 v[36:39],v[136:139],v[64:67],v[36:39]
v_mfma_f32_16x16x32_bf16 v[40:43],v[140:143],v[64:67],v[40:43]
v_mfma_f32_16x16x32_bf16 v[40:43],v[128:131],v[60:63],v[40:43]
s_waitcnt lgkmcnt(1)
v_mfma_f32_16x16x32_bf16 v[40:43],v[156:159],v[68:71],v[40:43]
ds_read_b128 v[52:55],v2 offset:52224
v_mfma_f32_16x16x32_bf16 v[40:43],v[168:171],v[44:47],v[40:43]
ds_read_b128 v[88:91],v10 offset:6144
v_mfma_f32_16x16x32_bf16 v[36:39],v[164:167],v[44:47],v[36:39]
v_mfma_f32_16x16x32_bf16 v[36:39],v[152:155],v[68:71],v[36:39]
v_mfma_f32_16x16x32_bf16 v[32:35],v[148:151],v[68:71],v[32:35]
s_waitcnt vmcnt(10)
ds_read_b128 v[56:59],v2 offset:53248
v_mfma_f32_16x16x32_bf16 v[32:35],v[84:87],v[44:47],v[32:35]
s_barrier
v_mfma_f32_16x16x32_bf16 v[28:31],v[160:163],v[44:47],v[28:31]
v_mfma_f32_16x16x32_bf16 v[28:31],v[144:147],v[68:71],v[28:31]
s_waitcnt lgkmcnt(1)
v_mfma_f32_16x16x32_bf16 v[28:31],v[172:175],v[48:51],v[28:31]
ds_read_b128 v[60:63],v2 offset:54272
v_mfma_f32_16x16x32_bf16 v[28:31],v[188:191],v[52:55],v[28:31]
ds_read_b128 v[84:87],v10 offset:7168
v_mfma_f32_16x16x32_bf16 v[32:35],v[88:91],v[52:55],v[32:35]
v_mfma_f32_16x16x32_bf16 v[32:35],v[176:179],v[48:51],v[32:35]
v_mfma_f32_16x16x32_bf16 v[36:39],v[180:183],v[48:51],v[36:39]
ds_read_b128 v[64:67],v2 offset:55296
v_mfma_f32_16x16x32_bf16 v[36:39],v[192:195],v[52:55],v[36:39]
v_mfma_f32_16x16x32_bf16 v[40:43],v[196:199],v[52:55],v[40:43]
v_mfma_f32_16x16x32_bf16 v[40:43],v[184:187],v[48:51],v[40:43]
s_waitcnt lgkmcnt(1)
v_mfma_f32_16x16x32_bf16 v[40:43],v[212:215],v[56:59],v[40:43]
ds_read_b128 v[68:71],v2 offset:56320
v_mfma_f32_16x16x32_bf16 v[40:43],v[224:227],v[60:63],v[40:43]
ds_read_b128 v[88:91],v10 offset:8192
v_mfma_f32_16x16x32_bf16 v[36:39],v[220:223],v[60:63],v[36:39]
s_waitcnt vmcnt(4)
v_mfma_f32_16x16x32_bf16 v[36:39],v[208:211],v[56:59],v[36:39]
s_barrier
v_mfma_f32_16x16x32_bf16 v[32:35],v[204:207],v[56:59],v[32:35]
ds_read_b128 v[44:47],v2 offset:0
v_mfma_f32_16x16x32_bf16 v[32:35],v[84:87],v[60:63],v[32:35]
v_mfma_f32_16x16x32_bf16 v[28:31],v[216:219],v[60:63],v[28:31]
v_mfma_f32_16x16x32_bf16 v[28:31],v[200:203],v[56:59],v[28:31]
s_waitcnt lgkmcnt(1)
v_mfma_f32_16x16x32_bf16 v[28:31],v[228:231],v[64:67],v[28:31]
ds_read_b128 v[48:51],v2 offset:1024
v_mfma_f32_16x16x32_bf16 v[28:31],v[244:247],v[68:71],v[28:31]
v_mfma_f32_16x16x32_bf16 v[32:35],v[88:91],v[68:71],v[32:35]
v_mfma_f32_16x16x32_bf16 v[32:35],v[232:235],v[64:67],v[32:35]
v_mfma_f32_16x16x32_bf16 v[36:39],v[236:239],v[64:67],v[36:39]
ds_read_b128 v[52:55],v2 offset:2048
v_mfma_f32_16x16x32_bf16 v[36:39],v[248:251],v[68:71],v[36:39]
v_mfma_f32_16x16x32_bf16 v[40:43],v[252:255],v[68:71],v[40:43]
s_waitcnt lgkmcnt(1)
v_mfma_f32_16x16x32_bf16 v[40:43],v[240:243],v[64:67],v[40:43]
s_cbranch_scc1 Lg4_d0w0_%=
s_branch Lg4_slow0_%=
Lg4_d0w1_%=:
v_mfma_f32_16x16x32_bf16 v[24:27],a[12:15],v[44:47],0
s_mov_b32 s73,s47
ds_read_b128 v[56:59],v2 offset:3072
v_lshlrev_b32 v72,16,v80
v_mfma_f32_16x16x32_bf16 v[24:27],a[28:31],v[48:51],v[24:27]
s_max_u32 s73,s73,s65
v_and_b32 v73,0xffff0000,v80
v_lshlrev_b32 v74,16,v81
v_mfma_f32_16x16x32_bf16 v[20:23],a[24:27],v[48:51],0
s_mov_b32 s65,0
v_and_b32 v75,0xffff0000,v81
v_lshlrev_b32 v76,16,v82
v_mfma_f32_16x16x32_bf16 v[20:23],a[8:11],v[44:47],v[20:23]
s_mul_i32 s61,s49,7
v_and_b32 v77,0xffff0000,v82
v_lshlrev_b32 v78,16,v83
v_mfma_f32_16x16x32_bf16 v[16:19],a[4:7],v[44:47],0
s_lshr_b32 s61,s61,1
ds_read_b128 v[60:63],v2 offset:4096
v_and_b32 v79,0xffff0000,v83
v_mfma_f32_16x16x32_bf16 v[16:19],a[20:23],v[48:51],v[16:19])"
R"(
v_cvt_pk_bf16_f32 v28,v28,v29
v_cvt_pk_bf16_f32 v29,v30,v31
v_mfma_f32_16x16x32_bf16 v[12:15],a[16:19],v[48:51],0
v_cvt_pk_bf16_f32 v30,v32,v33
v_cvt_pk_bf16_f32 v31,v34,v35
v_mfma_f32_16x16x32_bf16 v[12:15],a[0:3],v[44:47],v[12:15]
s_waitcnt lgkmcnt(1)
v_add_f32 v36,v36,v72
v_add_f32 v37,v37,v73
v_mfma_f32_16x16x32_bf16 v[12:15],a[32:35],v[52:55],v[12:15]
buffer_store_dwordx4 v[28:31],v3,s[36:39],s73 offen offset:0 sc1
ds_read_b128 v[64:67],v2 offset:5120
v_add_f32 v38,v38,v74
v_add_f32 v39,v39,v75
v_mfma_f32_16x16x32_bf16 v[12:15],a[48:51],v[56:59],v[12:15]
v_add_f32 v40,v40,v76
v_add_f32 v41,v41,v77
v_mfma_f32_16x16x32_bf16 v[16:19],a[52:55],v[56:59],v[16:19]
v_add_f32 v42,v42,v78
v_add_f32 v43,v43,v79
v_mfma_f32_16x16x32_bf16 v[16:19],a[36:39],v[52:55],v[16:19]
v_cvt_pk_bf16_f32 v36,v36,v37
v_cvt_pk_bf16_f32 v37,v38,v39
v_mfma_f32_16x16x32_bf16 v[20:23],a[40:43],v[52:55],v[20:23]
s_nop 0
ds_read_b128 v[68:71],v2 offset:6144
v_cvt_pk_bf16_f32 v38,v40,v41
v_mfma_f32_16x16x32_bf16 v[20:23],a[56:59],v[56:59],v[20:23]
s_barrier
v_mfma_f32_16x16x32_bf16 v[24:27],a[60:63],v[56:59],v[24:27]
ds_read_b32 v11,v8 offset:4096
v_cvt_pk_bf16_f32 v39,v42,v43
v_mfma_f32_16x16x32_bf16 v[24:27],a[44:47],v[52:55],v[24:27]
s_waitcnt lgkmcnt(2)
v_mfma_f32_16x16x32_bf16 v[24:27],a[76:79],v[60:63],v[24:27]
ds_read_b128 v[44:47],v2 offset:7168
v_mfma_f32_16x16x32_bf16 v[24:27],a[92:95],v[64:67],v[24:27]
v_mfma_f32_16x16x32_bf16 v[20:23],a[88:91],v[64:67],v[20:23]
s_waitcnt lgkmcnt(1)
v_mfma_f32_16x16x32_bf16 v[20:23],a[72:75],v[60:63],v[20:23]
v_mfma_f32_16x16x32_bf16 v[16:19],a[68:71],v[60:63],v[16:19]
buffer_store_dwordx4 v[36:39],v4,s[36:39],s73 offen offset:0 sc1
v_cmp_eq_u32 vcc,s60,v11
v_mfma_f32_16x16x32_bf16 v[16:19],a[84:87],v[64:67],v[16:19]
s_cmp_eq_u32 vcc_lo,-1
ds_read_b128 v[48:51],v2 offset:8192
s_cselect_b32 s66,s49,s59
v_mfma_f32_16x16x32_bf16 v[12:15],a[80:83],v[64:67],v[12:15]
s_cselect_b32 s61,s61,s59
s_add_u32 m0,s63,28672
s_add_u32 s53,s52,s55
v_mfma_f32_16x16x32_bf16 v[12:15],a[64:67],v[60:63],v[12:15]
s_waitcnt lgkmcnt(1)
s_cmp_eq_u32 s53,s56
s_cselect_b32 s53,s57,s53
v_mfma_f32_16x16x32_bf16 v[12:15],a[96:99],v[68:71],v[12:15]
buffer_load_dwordx4 v0,s[36:39],s61 offen offset:0 sc1 lds
v_mfma_f32_16x16x32_bf16 v[12:15],a[112:115],v[44:47],v[12:15]
s_cmp_eq_u32 s53,s58
ds_read_b128 v[52:55],v2 offset:9216
s_cselect_b32 s53,s59,s53
v_mfma_f32_16x16x32_bf16 v[16:19],a[116:119],v[44:47],v[16:19]
s_cmp_ge_u32 s53,6520832
s_cselect_b32 s55,196608,s55
v_mfma_f32_16x16x32_bf16 v[16:19],a[100:103],v[68:71],v[16:19]
v_mfma_f32_16x16x32_bf16 v[20:23],a[104:107],v[68:71],v[20:23]
buffer_load_dwordx4 v0,s[36:39],s61 offen offset:1024 sc1 lds
v_mfma_f32_16x16x32_bf16 v[20:23],a[120:123],v[44:47],v[20:23]
ds_read_b128 v[56:59],v2 offset:10240
v_mfma_f32_16x16x32_bf16 v[24:27],a[124:127],v[44:47],v[24:27]
v_mfma_f32_16x16x32_bf16 v[24:27],a[108:111],v[68:71],v[24:27]
s_waitcnt lgkmcnt(1)
v_mfma_f32_16x16x32_bf16 v[24:27],a[140:143],v[48:51],v[24:27]
buffer_load_dwordx4 v0,s[36:39],s61 offen offset:2048 sc1 lds
v_mfma_f32_16x16x32_bf16 v[24:27],a[156:159],v[52:55],v[24:27]
ds_read_b128 v[60:63],v2 offset:11264
ds_read_b128 v[88:91],v10 offset:0
v_mfma_f32_16x16x32_bf16 v[20:23],a[152:155],v[52:55],v[20:23]
v_mfma_f32_16x16x32_bf16 v[20:23],a[136:139],v[48:51],v[20:23]
v_mfma_f32_16x16x32_bf16 v[16:19],a[132:135],v[48:51],v[16:19]
buffer_load_dwordx4 v0,s[36:39],s61 offen offset:3072 sc1 lds
v_mfma_f32_16x16x32_bf16 v[16:19],a[148:151],v[52:55],v[16:19]
s_add_u32 m0,s64,28672
ds_read_b128 v[64:67],v2 offset:12288
s_cmp_ge_u32 s66,s59
v_mfma_f32_16x16x32_bf16 v[12:15],a[144:147],v[52:55],v[12:15]
v_mfma_f32_16x16x32_bf16 v[12:15],a[128:131],v[48:51],v[12:15]
s_waitcnt lgkmcnt(1)
v_mfma_f32_16x16x32_bf16 v[12:15],a[160:163],v[56:59],v[12:15]
ds_read_b128 v[68:71],v2 offset:13312
v_mfma_f32_16x16x32_bf16 v[12:15],a[176:179],v[60:63],v[12:15]
buffer_load_dwordx4 v[80:83],v9,s[36:39],s48 offen offset:0 sc1
v_mfma_f32_16x16x32_bf16 v[16:19],v[88:91],v[60:63],v[16:19]
ds_read_b128 v[84:87],v10 offset:1024
v_mfma_f32_16x16x32_bf16 v[16:19],a[164:167],v[56:59],v[16:19]
v_mfma_f32_16x16x32_bf16 v[20:23],a[168:171],v[56:59],v[20:23]
ds_read_b128 v[44:47],v2 offset:14336
v_mfma_f32_16x16x32_bf16 v[20:23],a[180:183],v[60:63],v[20:23]
v_mfma_f32_16x16x32_bf16 v[24:27],a[184:187],v[60:63],v[24:27]
v_mfma_f32_16x16x32_bf16 v[24:27],a[172:175],v[56:59],v[24:27]
s_waitcnt lgkmcnt(1)
v_mfma_f32_16x16x32_bf16 v[24:27],a[200:203],v[64:67],v[24:27]
ds_read_b128 v[48:51],v2 offset:15360
v_mfma_f32_16x16x32_bf16 v[24:27],a[212:215],v[68:71],v[24:27]
ds_read_b128 v[88:91],v10 offset:2048
v_mfma_f32_16x16x32_bf16 v[20:23],a[208:211],v[68:71],v[20:23]
s_waitcnt vmcnt(7)
v_mfma_f32_16x16x32_bf16 v[20:23],a[196:199],v[64:67],v[20:23]
s_barrier
v_mfma_f32_16x16x32_bf16 v[16:19],a[192:195],v[64:67],v[16:19]
ds_read_b128 v[52:55],v2 offset:16384
v_mfma_f32_16x16x32_bf16 v[16:19],v[84:87],v[68:71],v[16:19]
v_mfma_f32_16x16x32_bf16 v[12:15],a[204:207],v[68:71],v[12:15]
buffer_load_dwordx4 v1,s[36:39],s61 offen offset:0 sc1 lds
v_mfma_f32_16x16x32_bf16 v[12:15],a[188:191],v[64:67],v[12:15]
s_waitcnt lgkmcnt(1)
v_mfma_f32_16x16x32_bf16 v[12:15],a[216:219],v[44:47],v[12:15]
ds_read_b128 v[56:59],v2 offset:17408
v_mfma_f32_16x16x32_bf16 v[12:15],a[232:235],v[48:51],v[12:15]
ds_read_b128 v[84:87],v10 offset:3072
v_mfma_f32_16x16x32_bf16 v[16:19],v[88:91],v[48:51],v[16:19]
v_mfma_f32_16x16x32_bf16 v[16:19],a[220:223],v[44:47],v[16:19]
v_mfma_f32_16x16x32_bf16 v[20:23],a[224:227],v[44:47],v[20:23]
buffer_load_dwordx4 v1,s[36:39],s61 offen offset:1024 sc1 lds
v_mfma_f32_16x16x32_bf16 v[20:23],a[236:239],v[48:51],v[20:23]
ds_read_b128 v[60:63],v2 offset:18432
v_mfma_f32_16x16x32_bf16 v[24:27],a[240:243],v[48:51],v[24:27]
v_mfma_f32_16x16x32_bf16 v[24:27],a[228:231],v[44:47],v[24:27]
s_waitcnt lgkmcnt(1)
v_mfma_f32_16x16x32_bf16 v[24:27],v[100:103],v[52:55],v[24:27]
ds_read_b128 v[64:67],v2 offset:19456
v_mfma_f32_16x16x32_bf16 v[24:27],v[112:115],v[56:59],v[24:27]
ds_read_b128 v[88:91],v10 offset:4096
v_mfma_f32_16x16x32_bf16 v[20:23],v[108:111],v[56:59],v[20:23]
buffer_load_dwordx4 v1,s[36:39],s61 offen offset:2048 sc1 lds
v_mfma_f32_16x16x32_bf16 v[20:23],a[252:255],v[52:55],v[20:23]
v_mfma_f32_16x16x32_bf16 v[16:19],a[248:251],v[52:55],v[16:19]
ds_read_b128 v[68:71],v2 offset:20480
v_mfma_f32_16x16x32_bf16 v[16:19],v[84:87],v[56:59],v[16:19]
v_mfma_f32_16x16x32_bf16 v[12:15],v[104:107],v[56:59],v[12:15]
v_mfma_f32_16x16x32_bf16 v[12:15],a[244:247],v[52:55],v[12:15]
s_waitcnt lgkmcnt(1)
v_mfma_f32_16x16x32_bf16 v[12:15],v[116:119],v[60:63],v[12:15]
ds_read_b128 v[44:47],v2 offset:21504
v_mfma_f32_16x16x32_bf16 v[12:15],v[132:135],v[64:67],v[12:15]
ds_read_b128 v[84:87],v10 offset:5120
v_mfma_f32_16x16x32_bf16 v[16:19],v[88:91],v[64:67],v[16:19]
v_mfma_f32_16x16x32_bf16 v[16:19],v[120:123],v[60:63],v[16:19]
v_mfma_f32_16x16x32_bf16 v[20:23],v[124:127],v[60:63],v[20:23]
ds_read_b128 v[48:51],v2 offset:22528
v_mfma_f32_16x16x32_bf16 v[20:23],v[136:139],v[64:67],v[20:23]
v_mfma_f32_16x16x32_bf16 v[24:27],v[140:143],v[64:67],v[24:27]
v_mfma_f32_16x16x32_bf16 v[24:27],v[128:131],v[60:63],v[24:27]
s_waitcnt lgkmcnt(1)
v_mfma_f32_16x16x32_bf16 v[24:27],v[156:159],v[68:71],v[24:27]
ds_read_b128 v[52:55],v2 offset:23552
v_mfma_f32_16x16x32_bf16 v[24:27],v[168:171],v[44:47],v[24:27]
ds_read_b128 v[88:91],v10 offset:6144
v_mfma_f32_16x16x32_bf16 v[20:23],v[164:167],v[44:47],v[20:23]
v_mfma_f32_16x16x32_bf16 v[20:23],v[152:155],v[68:71],v[20:23]
v_mfma_f32_16x16x32_bf16 v[16:19],v[148:151],v[68:71],v[16:19]
s_waitcnt vmcnt(8)
ds_read_b128 v[56:59],v2 offset:24576
v_mfma_f32_16x16x32_bf16 v[16:19],v[84:87],v[44:47],v[16:19]
s_barrier
v_mfma_f32_16x16x32_bf16 v[12:15],v[160:163],v[44:47],v[12:15]
v_mfma_f32_16x16x32_bf16 v[12:15],v[144:147],v[68:71],v[12:15]
s_waitcnt lgkmcnt(1)
v_mfma_f32_16x16x32_bf16 v[12:15],v[172:175],v[48:51],v[12:15]
ds_read_b128 v[60:63],v2 offset:25600
v_mfma_f32_16x16x32_bf16 v[12:15],v[188:191],v[52:55],v[12:15]
ds_read_b128 v[84:87],v10 offset:7168
v_mfma_f32_16x16x32_bf16 v[16:19],v[88:91],v[52:55],v[16:19]
v_mfma_f32_16x16x32_bf16 v[16:19],v[176:179],v[48:51],v[16:19]
v_mfma_f32_16x16x32_bf16 v[20:23],v[180:183],v[48:51],v[20:23]
ds_read_b128 v[64:67],v2 offset:26624
v_mfma_f32_16x16x32_bf16 v[20:23],v[192:195],v[52:55],v[20:23]
v_mfma_f32_16x16x32_bf16 v[24:27],v[196:199],v[52:55],v[24:27]
v_mfma_f32_16x16x32_bf16 v[24:27],v[184:187],v[48:51],v[24:27]
s_waitcnt lgkmcnt(1)
v_mfma_f32_16x16x32_bf16 v[24:27],v[212:215],v[56:59],v[24:27]
ds_read_b128 v[68:71],v2 offset:27648
v_mfma_f32_16x16x32_bf16 v[24:27],v[224:227],v[60:63],v[24:27]
ds_read_b128 v[88:91],v10 offset:8192
v_mfma_f32_16x16x32_bf16 v[20:23],v[220:223],v[60:63],v[20:23]
s_waitcnt vmcnt(3)
v_mfma_f32_16x16x32_bf16 v[20:23],v[208:211],v[56:59],v[20:23]
s_barrier
v_mfma_f32_16x16x32_bf16 v[16:19],v[204:207],v[56:59],v[16:19]
ds_read_b128 v[44:47],v2 offset:28672
v_mfma_f32_16x16x32_bf16 v[16:19],v[84:87],v[60:63],v[16:19]
v_mfma_f32_16x16x32_bf16 v[12:15],v[216:219],v[60:63],v[12:15]
v_mfma_f32_16x16x32_bf16 v[12:15],v[200:203],v[56:59],v[12:15]
s_waitcnt lgkmcnt(1)
v_mfma_f32_16x16x32_bf16 v[12:15],v[228:231],v[64:67],v[12:15]
ds_read_b128 v[48:51],v2 offset:29696
v_mfma_f32_16x16x32_bf16 v[12:15],v[244:247],v[68:71],v[12:15]
v_mfma_f32_16x16x32_bf16 v[16:19],v[88:91],v[68:71],v[16:19]
v_mfma_f32_16x16x32_bf16 v[16:19],v[232:235],v[64:67],v[16:19]
v_mfma_f32_16x16x32_bf16 v[20:23],v[236:239],v[64:67],v[20:23])"
R"(
ds_read_b128 v[52:55],v2 offset:30720
v_mfma_f32_16x16x32_bf16 v[20:23],v[248:251],v[68:71],v[20:23]
v_mfma_f32_16x16x32_bf16 v[24:27],v[252:255],v[68:71],v[24:27]
s_waitcnt lgkmcnt(1)
v_mfma_f32_16x16x32_bf16 v[24:27],v[240:243],v[64:67],v[24:27]
s_cbranch_scc1 Lg4_slow1_%=
Lg4_d1w1_%=:
v_mfma_f32_16x16x32_bf16 v[40:43],a[12:15],v[44:47],0
s_mov_b32 s73,s48
ds_read_b128 v[56:59],v2 offset:31744
v_lshlrev_b32 v72,16,v80
v_mfma_f32_16x16x32_bf16 v[40:43],a[28:31],v[48:51],v[40:43]
s_max_u32 s73,s73,s65
v_and_b32 v73,0xffff0000,v80
v_lshlrev_b32 v74,16,v81
v_mfma_f32_16x16x32_bf16 v[36:39],a[24:27],v[48:51],0
s_mov_b32 s65,0
v_and_b32 v75,0xffff0000,v81
v_lshlrev_b32 v76,16,v82
v_mfma_f32_16x16x32_bf16 v[36:39],a[8:11],v[44:47],v[36:39]
s_mul_i32 s61,s50,7
v_and_b32 v77,0xffff0000,v82
v_lshlrev_b32 v78,16,v83
v_mfma_f32_16x16x32_bf16 v[32:35],a[4:7],v[44:47],0
s_lshr_b32 s61,s61,1
ds_read_b128 v[60:63],v2 offset:32768
v_and_b32 v79,0xffff0000,v83
v_mfma_f32_16x16x32_bf16 v[32:35],a[20:23],v[48:51],v[32:35]
s_mov_b64 s[44:45],s[46:47]
v_cvt_pk_bf16_f32 v12,v12,v13
v_cvt_pk_bf16_f32 v13,v14,v15
v_mfma_f32_16x16x32_bf16 v[28:31],a[16:19],v[48:51],0
s_mov_b64 s[46:47],s[48:49]
v_cvt_pk_bf16_f32 v14,v16,v17
v_cvt_pk_bf16_f32 v15,v18,v19
v_mfma_f32_16x16x32_bf16 v[28:31],a[0:3],v[44:47],v[28:31]
s_waitcnt lgkmcnt(1)
v_add_f32 v20,v20,v72
v_add_f32 v21,v21,v73
v_mfma_f32_16x16x32_bf16 v[28:31],a[32:35],v[52:55],v[28:31]
buffer_store_dwordx4 v[12:15],v3,s[36:39],s73 offen offset:0 sc1
ds_read_b128 v[64:67],v2 offset:33792
v_add_f32 v22,v22,v74
v_add_f32 v23,v23,v75
v_mfma_f32_16x16x32_bf16 v[28:31],a[48:51],v[56:59],v[28:31]
v_add_f32 v24,v24,v76
v_add_f32 v25,v25,v77
v_mfma_f32_16x16x32_bf16 v[32:35],a[52:55],v[56:59],v[32:35]
v_add_f32 v26,v26,v78
v_add_f32 v27,v27,v79
v_mfma_f32_16x16x32_bf16 v[32:35],a[36:39],v[52:55],v[32:35]
v_cvt_pk_bf16_f32 v20,v20,v21
v_cvt_pk_bf16_f32 v21,v22,v23
v_mfma_f32_16x16x32_bf16 v[36:39],a[40:43],v[52:55],v[36:39]
s_nop 0
ds_read_b128 v[68:71],v2 offset:34816
v_cvt_pk_bf16_f32 v22,v24,v25
v_mfma_f32_16x16x32_bf16 v[36:39],a[56:59],v[56:59],v[36:39]
s_barrier
v_mfma_f32_16x16x32_bf16 v[40:43],a[60:63],v[56:59],v[40:43]
ds_read_b32 v11,v8 offset:5120
v_cvt_pk_bf16_f32 v23,v26,v27
v_mfma_f32_16x16x32_bf16 v[40:43],a[44:47],v[52:55],v[40:43]
s_waitcnt lgkmcnt(2)
v_mfma_f32_16x16x32_bf16 v[40:43],a[76:79],v[60:63],v[40:43]
ds_read_b128 v[44:47],v2 offset:35840
v_mfma_f32_16x16x32_bf16 v[40:43],a[92:95],v[64:67],v[40:43]
v_mfma_f32_16x16x32_bf16 v[36:39],a[88:91],v[64:67],v[36:39]
s_waitcnt lgkmcnt(1)
v_mfma_f32_16x16x32_bf16 v[36:39],a[72:75],v[60:63],v[36:39]
v_mfma_f32_16x16x32_bf16 v[32:35],a[68:71],v[60:63],v[32:35]
buffer_store_dwordx4 v[20:23],v4,s[36:39],s73 offen offset:0 sc1
v_cmp_eq_u32 vcc,s60,v11
v_mfma_f32_16x16x32_bf16 v[32:35],a[84:87],v[64:67],v[32:35]
s_cmp_eq_u32 vcc_lo,-1
ds_read_b128 v[48:51],v2 offset:36864
s_cselect_b32 s67,s50,s59
v_mfma_f32_16x16x32_bf16 v[28:31],a[80:83],v[64:67],v[28:31]
s_cselect_b32 s61,s61,s59
s_add_u32 m0,s63,0
s_add_u32 s74,s53,s55
v_mfma_f32_16x16x32_bf16 v[28:31],a[64:67],v[60:63],v[28:31]
s_waitcnt lgkmcnt(1)
s_cmp_eq_u32 s74,s56
s_cselect_b32 s74,s57,s74
v_mfma_f32_16x16x32_bf16 v[28:31],a[96:99],v[68:71],v[28:31]
buffer_load_dwordx4 v0,s[36:39],s61 offen offset:0 sc1 lds
v_mfma_f32_16x16x32_bf16 v[28:31],a[112:115],v[44:47],v[28:31]
s_cmp_eq_u32 s74,s58
ds_read_b128 v[52:55],v2 offset:37888
s_cselect_b32 s74,s59,s74
v_mfma_f32_16x16x32_bf16 v[32:35],a[116:119],v[44:47],v[32:35]
s_cmp_ge_u32 s74,6520832
s_cselect_b32 s55,196608,s55
v_mfma_f32_16x16x32_bf16 v[32:35],a[100:103],v[68:71],v[32:35]
v_mfma_f32_16x16x32_bf16 v[36:39],a[104:107],v[68:71],v[36:39]
buffer_load_dwordx4 v0,s[36:39],s61 offen offset:1024 sc1 lds
v_mfma_f32_16x16x32_bf16 v[36:39],a[120:123],v[44:47],v[36:39]
ds_read_b128 v[56:59],v2 offset:38912
v_mfma_f32_16x16x32_bf16 v[40:43],a[124:127],v[44:47],v[40:43]
v_mfma_f32_16x16x32_bf16 v[40:43],a[108:111],v[68:71],v[40:43]
s_waitcnt lgkmcnt(1)
v_mfma_f32_16x16x32_bf16 v[40:43],a[140:143],v[48:51],v[40:43]
buffer_load_dwordx4 v0,s[36:39],s61 offen offset:2048 sc1 lds
v_mfma_f32_16x16x32_bf16 v[40:43],a[156:159],v[52:55],v[40:43]
ds_read_b128 v[60:63],v2 offset:39936
ds_read_b128 v[88:91],v10 offset:0
v_mfma_f32_16x16x32_bf16 v[36:39],a[152:155],v[52:55],v[36:39]
v_mfma_f32_16x16x32_bf16 v[36:39],a[136:139],v[48:51],v[36:39]
v_mfma_f32_16x16x32_bf16 v[32:35],a[132:135],v[48:51],v[32:35]
buffer_load_dwordx4 v0,s[36:39],s61 offen offset:3072 sc1 lds
v_mfma_f32_16x16x32_bf16 v[32:35],a[148:151],v[52:55],v[32:35]
s_add_u32 m0,s64,0
ds_read_b128 v[64:67],v2 offset:40960
s_cmp_lt_u32 s67,s59
v_mfma_f32_16x16x32_bf16 v[28:31],a[144:147],v[52:55],v[28:31]
v_mfma_f32_16x16x32_bf16 v[28:31],a[128:131],v[48:51],v[28:31]
s_waitcnt lgkmcnt(1)
v_mfma_f32_16x16x32_bf16 v[28:31],a[160:163],v[56:59],v[28:31]
ds_read_b128 v[68:71],v2 offset:41984
v_mfma_f32_16x16x32_bf16 v[28:31],a[176:179],v[60:63],v[28:31]
buffer_load_dwordx4 v[80:83],v9,s[36:39],s49 offen offset:0 sc1
v_mfma_f32_16x16x32_bf16 v[32:35],v[88:91],v[60:63],v[32:35]
s_mov_b64 s[48:49],s[50:51]
ds_read_b128 v[84:87],v10 offset:1024
s_mov_b64 s[50:51],s[52:53]
v_mfma_f32_16x16x32_bf16 v[32:35],a[164:167],v[56:59],v[32:35]
s_mov_b32 s52,s74
v_mfma_f32_16x16x32_bf16 v[36:39],a[168:171],v[56:59],v[36:39]
ds_read_b128 v[44:47],v2 offset:43008
v_mfma_f32_16x16x32_bf16 v[36:39],a[180:183],v[60:63],v[36:39]
v_mfma_f32_16x16x32_bf16 v[40:43],a[184:187],v[60:63],v[40:43]
v_mfma_f32_16x16x32_bf16 v[40:43],a[172:175],v[56:59],v[40:43]
s_waitcnt lgkmcnt(1)
v_mfma_f32_16x16x32_bf16 v[40:43],a[200:203],v[64:67],v[40:43]
ds_read_b128 v[48:51],v2 offset:44032
v_mfma_f32_16x16x32_bf16 v[40:43],a[212:215],v[68:71],v[40:43]
ds_read_b128 v[88:91],v10 offset:2048
v_mfma_f32_16x16x32_bf16 v[36:39],a[208:211],v[68:71],v[36:39]
s_waitcnt vmcnt(7)
v_mfma_f32_16x16x32_bf16 v[36:39],a[196:199],v[64:67],v[36:39]
s_barrier
v_mfma_f32_16x16x32_bf16 v[32:35],a[192:195],v[64:67],v[32:35]
ds_read_b128 v[52:55],v2 offset:45056
v_mfma_f32_16x16x32_bf16 v[32:35],v[84:87],v[68:71],v[32:35]
v_mfma_f32_16x16x32_bf16 v[28:31],a[204:207],v[68:71],v[28:31]
buffer_load_dwordx4 v1,s[36:39],s61 offen offset:0 sc1 lds
v_mfma_f32_16x16x32_bf16 v[28:31],a[188:191],v[64:67],v[28:31]
s_waitcnt lgkmcnt(1)
v_mfma_f32_16x16x32_bf16 v[28:31],a[216:219],v[44:47],v[28:31]
ds_read_b128 v[56:59],v2 offset:46080
v_mfma_f32_16x16x32_bf16 v[28:31],a[232:235],v[48:51],v[28:31]
ds_read_b128 v[84:87],v10 offset:3072
v_mfma_f32_16x16x32_bf16 v[32:35],v[88:91],v[48:51],v[32:35]
v_mfma_f32_16x16x32_bf16 v[32:35],a[220:223],v[44:47],v[32:35]
v_mfma_f32_16x16x32_bf16 v[36:39],a[224:227],v[44:47],v[36:39]
buffer_load_dwordx4 v1,s[36:39],s61 offen offset:1024 sc1 lds
v_mfma_f32_16x16x32_bf16 v[36:39],a[236:239],v[48:51],v[36:39]
ds_read_b128 v[60:63],v2 offset:47104
v_mfma_f32_16x16x32_bf16 v[40:43],a[240:243],v[48:51],v[40:43]
v_mfma_f32_16x16x32_bf16 v[40:43],a[228:231],v[44:47],v[40:43]
s_waitcnt lgkmcnt(1)
v_mfma_f32_16x16x32_bf16 v[40:43],v[100:103],v[52:55],v[40:43]
ds_read_b128 v[64:67],v2 offset:48128
v_mfma_f32_16x16x32_bf16 v[40:43],v[112:115],v[56:59],v[40:43]
ds_read_b128 v[88:91],v10 offset:4096
v_mfma_f32_16x16x32_bf16 v[36:39],v[108:111],v[56:59],v[36:39]
buffer_load_dwordx4 v1,s[36:39],s61 offen offset:2048 sc1 lds
v_mfma_f32_16x16x32_bf16 v[36:39],a[252:255],v[52:55],v[36:39]
v_mfma_f32_16x16x32_bf16 v[32:35],a[248:251],v[52:55],v[32:35]
ds_read_b128 v[68:71],v2 offset:49152
v_mfma_f32_16x16x32_bf16 v[32:35],v[84:87],v[56:59],v[32:35]
v_mfma_f32_16x16x32_bf16 v[28:31],v[104:107],v[56:59],v[28:31]
v_mfma_f32_16x16x32_bf16 v[28:31],a[244:247],v[52:55],v[28:31]
s_waitcnt lgkmcnt(1)
v_mfma_f32_16x16x32_bf16 v[28:31],v[116:119],v[60:63],v[28:31]
ds_read_b128 v[44:47],v2 offset:50176
v_mfma_f32_16x16x32_bf16 v[28:31],v[132:135],v[64:67],v[28:31]
ds_read_b128 v[84:87],v10 offset:5120
v_mfma_f32_16x16x32_bf16 v[32:35],v[88:91],v[64:67],v[32:35]
v_mfma_f32_16x16x32_bf16 v[32:35],v[120:123],v[60:63],v[32:35]
v_mfma_f32_16x16x32_bf16 v[36:39],v[124:127],v[60:63],v[36:39]
ds_read_b128 v[48:51],v2 offset:51200
v_mfma_f32_16x16x32_bf16 v[36:39],v[136:139],v[64:67],v[36:39]
v_mfma_f32_16x16x32_bf16 v[40:43],v[140:143],v[64:67],v[40:43]
v_mfma_f32_16x16x32_bf16 v[40:43],v[128:131],v[60:63],v[40:43]
s_waitcnt lgkmcnt(1)
v_mfma_f32_16x16x32_bf16 v[40:43],v[156:159],v[68:71],v[40:43]
ds_read_b128 v[52:55],v2 offset:52224
v_mfma_f32_16x16x32_bf16 v[40:43],v[168:171],v[44:47],v[40:43]
ds_read_b128 v[88:91],v10 offset:6144
v_mfma_f32_16x16x32_bf16 v[36:39],v[164:167],v[44:47],v[36:39]
v_mfma_f32_16x16x32_bf16 v[36:39],v[152:155],v[68:71],v[36:39]
v_mfma_f32_16x16x32_bf16 v[32:35],v[148:151],v[68:71],v[32:35]
s_waitcnt vmcnt(8)
ds_read_b128 v[56:59],v2 offset:53248
v_mfma_f32_16x16x32_bf16 v[32:35],v[84:87],v[44:47],v[32:35]
s_barrier
v_mfma_f32_16x16x32_bf16 v[28:31],v[160:163],v[44:47],v[28:31]
v_mfma_f32_16x16x32_bf16 v[28:31],v[144:147],v[68:71],v[28:31])"
R"(
s_waitcnt lgkmcnt(1)
v_mfma_f32_16x16x32_bf16 v[28:31],v[172:175],v[48:51],v[28:31]
ds_read_b128 v[60:63],v2 offset:54272
v_mfma_f32_16x16x32_bf16 v[28:31],v[188:191],v[52:55],v[28:31]
ds_read_b128 v[84:87],v10 offset:7168
v_mfma_f32_16x16x32_bf16 v[32:35],v[88:91],v[52:55],v[32:35]
v_mfma_f32_16x16x32_bf16 v[32:35],v[176:179],v[48:51],v[32:35]
v_mfma_f32_16x16x32_bf16 v[36:39],v[180:183],v[48:51],v[36:39]
ds_read_b128 v[64:67],v2 offset:55296
v_mfma_f32_16x16x32_bf16 v[36:39],v[192:195],v[52:55],v[36:39]
v_mfma_f32_16x16x32_bf16 v[40:43],v[196:199],v[52:55],v[40:43]
v_mfma_f32_16x16x32_bf16 v[40:43],v[184:187],v[48:51],v[40:43]
s_waitcnt lgkmcnt(1)
v_mfma_f32_16x16x32_bf16 v[40:43],v[212:215],v[56:59],v[40:43]
ds_read_b128 v[68:71],v2 offset:56320
v_mfma_f32_16x16x32_bf16 v[40:43],v[224:227],v[60:63],v[40:43]
ds_read_b128 v[88:91],v10 offset:8192
v_mfma_f32_16x16x32_bf16 v[36:39],v[220:223],v[60:63],v[36:39]
s_waitcnt vmcnt(3)
v_mfma_f32_16x16x32_bf16 v[36:39],v[208:211],v[56:59],v[36:39]
s_barrier
v_mfma_f32_16x16x32_bf16 v[32:35],v[204:207],v[56:59],v[32:35]
ds_read_b128 v[44:47],v2 offset:0
v_mfma_f32_16x16x32_bf16 v[32:35],v[84:87],v[60:63],v[32:35]
v_mfma_f32_16x16x32_bf16 v[28:31],v[216:219],v[60:63],v[28:31]
v_mfma_f32_16x16x32_bf16 v[28:31],v[200:203],v[56:59],v[28:31]
s_waitcnt lgkmcnt(1)
v_mfma_f32_16x16x32_bf16 v[28:31],v[228:231],v[64:67],v[28:31]
ds_read_b128 v[48:51],v2 offset:1024
v_mfma_f32_16x16x32_bf16 v[28:31],v[244:247],v[68:71],v[28:31]
v_mfma_f32_16x16x32_bf16 v[32:35],v[88:91],v[68:71],v[32:35]
v_mfma_f32_16x16x32_bf16 v[32:35],v[232:235],v[64:67],v[32:35]
v_mfma_f32_16x16x32_bf16 v[36:39],v[236:239],v[64:67],v[36:39]
ds_read_b128 v[52:55],v2 offset:2048
v_mfma_f32_16x16x32_bf16 v[36:39],v[248:251],v[68:71],v[36:39]
v_mfma_f32_16x16x32_bf16 v[40:43],v[252:255],v[68:71],v[40:43]
s_waitcnt lgkmcnt(1)
v_mfma_f32_16x16x32_bf16 v[40:43],v[240:243],v[64:67],v[40:43]
s_cbranch_scc1 Lg4_d0w1_%=
s_branch Lg4_slow0_%=
Lg4_d0w2_%=:
v_mfma_f32_16x16x32_bf16 v[24:27],a[12:15],v[44:47],0
s_mov_b32 s73,s47
ds_read_b128 v[56:59],v2 offset:3072
v_lshlrev_b32 v72,16,v80
v_mfma_f32_16x16x32_bf16 v[24:27],a[28:31],v[48:51],v[24:27]
s_max_u32 s73,s73,s65
v_and_b32 v73,0xffff0000,v80
v_lshlrev_b32 v74,16,v81
v_mfma_f32_16x16x32_bf16 v[20:23],a[24:27],v[48:51],0
s_mov_b32 s65,0
v_and_b32 v75,0xffff0000,v81
v_lshlrev_b32 v76,16,v82
v_mfma_f32_16x16x32_bf16 v[20:23],a[8:11],v[44:47],v[20:23]
s_mul_i32 s61,s49,7
v_and_b32 v77,0xffff0000,v82
v_lshlrev_b32 v78,16,v83
v_mfma_f32_16x16x32_bf16 v[16:19],a[4:7],v[44:47],0
s_lshr_b32 s61,s61,1
ds_read_b128 v[60:63],v2 offset:4096
v_and_b32 v79,0xffff0000,v83
v_mfma_f32_16x16x32_bf16 v[16:19],a[20:23],v[48:51],v[16:19]
v_cvt_pk_bf16_f32 v28,v28,v29
v_cvt_pk_bf16_f32 v29,v30,v31
v_mfma_f32_16x16x32_bf16 v[12:15],a[16:19],v[48:51],0
v_cvt_pk_bf16_f32 v30,v32,v33
v_cvt_pk_bf16_f32 v31,v34,v35
v_mfma_f32_16x16x32_bf16 v[12:15],a[0:3],v[44:47],v[12:15]
s_waitcnt lgkmcnt(1)
v_add_f32 v36,v36,v72
v_add_f32 v37,v37,v73
v_mfma_f32_16x16x32_bf16 v[12:15],a[32:35],v[52:55],v[12:15]
ds_read_b128 v[64:67],v2 offset:5120
v_add_f32 v38,v38,v74
v_mfma_f32_16x16x32_bf16 v[12:15],a[48:51],v[56:59],v[12:15]
buffer_store_dwordx4 v[28:31],v3,s[36:39],s73 offen offset:0 sc1
v_add_f32 v39,v39,v75
v_add_f32 v40,v40,v76
v_add_f32 v41,v41,v77
v_mfma_f32_16x16x32_bf16 v[16:19],a[52:55],v[56:59],v[16:19]
v_add_f32 v42,v42,v78
v_add_f32 v43,v43,v79
v_mfma_f32_16x16x32_bf16 v[16:19],a[36:39],v[52:55],v[16:19]
v_cvt_pk_bf16_f32 v36,v36,v37
v_cvt_pk_bf16_f32 v37,v38,v39
v_mfma_f32_16x16x32_bf16 v[20:23],a[40:43],v[52:55],v[20:23]
s_nop 0
ds_read_b128 v[68:71],v2 offset:6144
v_cvt_pk_bf16_f32 v38,v40,v41
v_mfma_f32_16x16x32_bf16 v[20:23],a[56:59],v[56:59],v[20:23]
s_barrier
v_mfma_f32_16x16x32_bf16 v[24:27],a[60:63],v[56:59],v[24:27]
ds_read_b32 v11,v8 offset:4096
v_cvt_pk_bf16_f32 v39,v42,v43
v_mfma_f32_16x16x32_bf16 v[24:27],a[44:47],v[52:55],v[24:27]
s_waitcnt lgkmcnt(2)
v_mfma_f32_16x16x32_bf16 v[24:27],a[76:79],v[60:63],v[24:27]
ds_read_b128 v[44:47],v2 offset:7168
v_mfma_f32_16x16x32_bf16 v[24:27],a[92:95],v[64:67],v[24:27]
v_mfma_f32_16x16x32_bf16 v[20:23],a[88:91],v[64:67],v[20:23]
s_waitcnt lgkmcnt(1)
v_mfma_f32_16x16x32_bf16 v[20:23],a[72:75],v[60:63],v[20:23]
v_mfma_f32_16x16x32_bf16 v[16:19],a[68:71],v[60:63],v[16:19]
ds_read_b128 v[48:51],v2 offset:8192
v_mfma_f32_16x16x32_bf16 v[16:19],a[84:87],v[64:67],v[16:19]
buffer_store_dwordx4 v[36:39],v4,s[36:39],s73 offen offset:0 sc1
v_cmp_eq_u32 vcc,s60,v11
v_mfma_f32_16x16x32_bf16 v[12:15],a[80:83],v[64:67],v[12:15]
s_cmp_eq_u32 vcc_lo,-1
s_cselect_b32 s66,s49,s59
s_cselect_b32 s61,s61,s59
v_mfma_f32_16x16x32_bf16 v[12:15],a[64:67],v[60:63],v[12:15]
s_waitcnt lgkmcnt(1)
s_add_u32 m0,s63,28672
s_add_u32 s53,s52,s55
v_mfma_f32_16x16x32_bf16 v[12:15],a[96:99],v[68:71],v[12:15]
s_cmp_eq_u32 s53,s56
ds_read_b128 v[52:55],v2 offset:9216
s_cselect_b32 s53,s57,s53
v_mfma_f32_16x16x32_bf16 v[12:15],a[112:115],v[44:47],v[12:15]
buffer_load_dwordx4 v0,s[36:39],s61 offen offset:0 sc1 lds
v_mfma_f32_16x16x32_bf16 v[16:19],a[116:119],v[44:47],v[16:19]
s_cmp_eq_u32 s53,s58
s_cselect_b32 s53,s59,s53
s_cmp_ge_u32 s53,6520832
v_mfma_f32_16x16x32_bf16 v[16:19],a[100:103],v[68:71],v[16:19]
s_cselect_b32 s55,196608,s55
v_mfma_f32_16x16x32_bf16 v[20:23],a[104:107],v[68:71],v[20:23]
ds_read_b128 v[56:59],v2 offset:10240
v_mfma_f32_16x16x32_bf16 v[20:23],a[120:123],v[44:47],v[20:23]
buffer_load_dwordx4 v0,s[36:39],s61 offen offset:1024 sc1 lds
v_mfma_f32_16x16x32_bf16 v[24:27],a[124:127],v[44:47],v[24:27]
v_mfma_f32_16x16x32_bf16 v[24:27],a[108:111],v[68:71],v[24:27]
s_waitcnt lgkmcnt(1)
v_mfma_f32_16x16x32_bf16 v[24:27],a[140:143],v[48:51],v[24:27]
ds_read_b128 v[60:63],v2 offset:11264
v_mfma_f32_16x16x32_bf16 v[24:27],a[156:159],v[52:55],v[24:27]
buffer_load_dwordx4 v0,s[36:39],s61 offen offset:2048 sc1 lds
v_mfma_f32_16x16x32_bf16 v[20:23],a[152:155],v[52:55],v[20:23]
ds_read_b128 v[88:91],v10 offset:0
v_mfma_f32_16x16x32_bf16 v[20:23],a[136:139],v[48:51],v[20:23]
v_mfma_f32_16x16x32_bf16 v[16:19],a[132:135],v[48:51],v[16:19]
ds_read_b128 v[64:67],v2 offset:12288
v_mfma_f32_16x16x32_bf16 v[16:19],a[148:151],v[52:55],v[16:19]
buffer_load_dwordx4 v0,s[36:39],s61 offen offset:3072 sc1 lds
v_mfma_f32_16x16x32_bf16 v[12:15],a[144:147],v[52:55],v[12:15]
s_add_u32 m0,s64,28672
s_cmp_ge_u32 s66,s59
v_mfma_f32_16x16x32_bf16 v[12:15],a[128:131],v[48:51],v[12:15]
s_waitcnt lgkmcnt(1)
v_mfma_f32_16x16x32_bf16 v[12:15],a[160:163],v[56:59],v[12:15]
ds_read_b128 v[68:71],v2 offset:13312
v_mfma_f32_16x16x32_bf16 v[12:15],a[176:179],v[60:63],v[12:15]
ds_read_b128 v[84:87],v10 offset:1024
v_mfma_f32_16x16x32_bf16 v[16:19],v[88:91],v[60:63],v[16:19]
buffer_load_dwordx4 v[80:83],v9,s[36:39],s48 offen offset:0 sc1
v_mfma_f32_16x16x32_bf16 v[16:19],a[164:167],v[56:59],v[16:19]
v_mfma_f32_16x16x32_bf16 v[20:23],a[168:171],v[56:59],v[20:23]
ds_read_b128 v[44:47],v2 offset:14336
v_mfma_f32_16x16x32_bf16 v[20:23],a[180:183],v[60:63],v[20:23]
v_mfma_f32_16x16x32_bf16 v[24:27],a[184:187],v[60:63],v[24:27]
v_mfma_f32_16x16x32_bf16 v[24:27],a[172:175],v[56:59],v[24:27]
s_waitcnt lgkmcnt(1)
v_mfma_f32_16x16x32_bf16 v[24:27],a[200:203],v[64:67],v[24:27]
ds_read_b128 v[48:51],v2 offset:15360
v_mfma_f32_16x16x32_bf16 v[24:27],a[212:215],v[68:71],v[24:27]
ds_read_b128 v[88:91],v10 offset:2048
v_mfma_f32_16x16x32_bf16 v[20:23],a[208:211],v[68:71],v[20:23]
s_waitcnt vmcnt(7)
v_mfma_f32_16x16x32_bf16 v[20:23],a[196:199],v[64:67],v[20:23]
s_barrier
v_mfma_f32_16x16x32_bf16 v[16:19],a[192:195],v[64:67],v[16:19]
ds_read_b128 v[52:55],v2 offset:16384
v_mfma_f32_16x16x32_bf16 v[16:19],v[84:87],v[68:71],v[16:19]
v_mfma_f32_16x16x32_bf16 v[12:15],a[204:207],v[68:71],v[12:15]
v_mfma_f32_16x16x32_bf16 v[12:15],a[188:191],v[64:67],v[12:15]
s_waitcnt lgkmcnt(1)
buffer_load_dwordx4 v1,s[36:39],s61 offen offset:0 sc1 lds
v_mfma_f32_16x16x32_bf16 v[12:15],a[216:219],v[44:47],v[12:15]
ds_read_b128 v[56:59],v2 offset:17408
v_mfma_f32_16x16x32_bf16 v[12:15],a[232:235],v[48:51],v[12:15]
ds_read_b128 v[84:87],v10 offset:3072
v_mfma_f32_16x16x32_bf16 v[16:19],v[88:91],v[48:51],v[16:19]
v_mfma_f32_16x16x32_bf16 v[16:19],a[220:223],v[44:47],v[16:19]
v_mfma_f32_16x16x32_bf16 v[20:23],a[224:227],v[44:47],v[20:23]
ds_read_b128 v[60:63],v2 offset:18432
v_mfma_f32_16x16x32_bf16 v[20:23],a[236:239],v[48:51],v[20:23]
buffer_load_dwordx4 v1,s[36:39],s61 offen offset:1024 sc1 lds
v_mfma_f32_16x16x32_bf16 v[24:27],a[240:243],v[48:51],v[24:27]
v_mfma_f32_16x16x32_bf16 v[24:27],a[228:231],v[44:47],v[24:27]
s_waitcnt lgkmcnt(1)
v_mfma_f32_16x16x32_bf16 v[24:27],v[100:103],v[52:55],v[24:27]
ds_read_b128 v[64:67],v2 offset:19456
v_mfma_f32_16x16x32_bf16 v[24:27],v[112:115],v[56:59],v[24:27]
ds_read_b128 v[88:91],v10 offset:4096
v_mfma_f32_16x16x32_bf16 v[20:23],v[108:111],v[56:59],v[20:23]
v_mfma_f32_16x16x32_bf16 v[20:23],a[252:255],v[52:55],v[20:23]
buffer_load_dwordx4 v1,s[36:39],s61 offen offset:2048 sc1 lds
v_mfma_f32_16x16x32_bf16 v[16:19],a[248:251],v[52:55],v[16:19]
ds_read_b128 v[68:71],v2 offset:20480)"
R"(
v_mfma_f32_16x16x32_bf16 v[16:19],v[84:87],v[56:59],v[16:19]
v_mfma_f32_16x16x32_bf16 v[12:15],v[104:107],v[56:59],v[12:15]
v_mfma_f32_16x16x32_bf16 v[12:15],a[244:247],v[52:55],v[12:15]
s_waitcnt lgkmcnt(1)
v_mfma_f32_16x16x32_bf16 v[12:15],v[116:119],v[60:63],v[12:15]
ds_read_b128 v[44:47],v2 offset:21504
v_mfma_f32_16x16x32_bf16 v[12:15],v[132:135],v[64:67],v[12:15]
ds_read_b128 v[84:87],v10 offset:5120
v_mfma_f32_16x16x32_bf16 v[16:19],v[88:91],v[64:67],v[16:19]
v_mfma_f32_16x16x32_bf16 v[16:19],v[120:123],v[60:63],v[16:19]
v_mfma_f32_16x16x32_bf16 v[20:23],v[124:127],v[60:63],v[20:23]
ds_read_b128 v[48:51],v2 offset:22528
v_mfma_f32_16x16x32_bf16 v[20:23],v[136:139],v[64:67],v[20:23]
v_mfma_f32_16x16x32_bf16 v[24:27],v[140:143],v[64:67],v[24:27]
v_mfma_f32_16x16x32_bf16 v[24:27],v[128:131],v[60:63],v[24:27]
s_waitcnt lgkmcnt(1)
v_mfma_f32_16x16x32_bf16 v[24:27],v[156:159],v[68:71],v[24:27]
ds_read_b128 v[52:55],v2 offset:23552
v_mfma_f32_16x16x32_bf16 v[24:27],v[168:171],v[44:47],v[24:27]
ds_read_b128 v[88:91],v10 offset:6144
v_mfma_f32_16x16x32_bf16 v[20:23],v[164:167],v[44:47],v[20:23]
v_mfma_f32_16x16x32_bf16 v[20:23],v[152:155],v[68:71],v[20:23]
v_mfma_f32_16x16x32_bf16 v[16:19],v[148:151],v[68:71],v[16:19]
s_waitcnt vmcnt(8)
ds_read_b128 v[56:59],v2 offset:24576
v_mfma_f32_16x16x32_bf16 v[16:19],v[84:87],v[44:47],v[16:19]
s_barrier
v_mfma_f32_16x16x32_bf16 v[12:15],v[160:163],v[44:47],v[12:15]
v_mfma_f32_16x16x32_bf16 v[12:15],v[144:147],v[68:71],v[12:15]
s_waitcnt lgkmcnt(1)
v_mfma_f32_16x16x32_bf16 v[12:15],v[172:175],v[48:51],v[12:15]
ds_read_b128 v[60:63],v2 offset:25600
v_mfma_f32_16x16x32_bf16 v[12:15],v[188:191],v[52:55],v[12:15]
ds_read_b128 v[84:87],v10 offset:7168
v_mfma_f32_16x16x32_bf16 v[16:19],v[88:91],v[52:55],v[16:19]
v_mfma_f32_16x16x32_bf16 v[16:19],v[176:179],v[48:51],v[16:19]
v_mfma_f32_16x16x32_bf16 v[20:23],v[180:183],v[48:51],v[20:23]
ds_read_b128 v[64:67],v2 offset:26624
v_mfma_f32_16x16x32_bf16 v[20:23],v[192:195],v[52:55],v[20:23]
v_mfma_f32_16x16x32_bf16 v[24:27],v[196:199],v[52:55],v[24:27]
v_mfma_f32_16x16x32_bf16 v[24:27],v[184:187],v[48:51],v[24:27]
s_waitcnt lgkmcnt(1)
v_mfma_f32_16x16x32_bf16 v[24:27],v[212:215],v[56:59],v[24:27]
ds_read_b128 v[68:71],v2 offset:27648
v_mfma_f32_16x16x32_bf16 v[24:27],v[224:227],v[60:63],v[24:27]
ds_read_b128 v[88:91],v10 offset:8192
v_mfma_f32_16x16x32_bf16 v[20:23],v[220:223],v[60:63],v[20:23]
s_waitcnt vmcnt(3)
v_mfma_f32_16x16x32_bf16 v[20:23],v[208:211],v[56:59],v[20:23]
s_barrier
v_mfma_f32_16x16x32_bf16 v[16:19],v[204:207],v[56:59],v[16:19]
ds_read_b128 v[44:47],v2 offset:28672
v_mfma_f32_16x16x32_bf16 v[16:19],v[84:87],v[60:63],v[16:19]
v_mfma_f32_16x16x32_bf16 v[12:15],v[216:219],v[60:63],v[12:15]
v_mfma_f32_16x16x32_bf16 v[12:15],v[200:203],v[56:59],v[12:15]
s_waitcnt lgkmcnt(1)
v_mfma_f32_16x16x32_bf16 v[12:15],v[228:231],v[64:67],v[12:15]
ds_read_b128 v[48:51],v2 offset:29696
v_mfma_f32_16x16x32_bf16 v[12:15],v[244:247],v[68:71],v[12:15]
v_mfma_f32_16x16x32_bf16 v[16:19],v[88:91],v[68:71],v[16:19]
v_mfma_f32_16x16x32_bf16 v[16:19],v[232:235],v[64:67],v[16:19]
v_mfma_f32_16x16x32_bf16 v[20:23],v[236:239],v[64:67],v[20:23]
ds_read_b128 v[52:55],v2 offset:30720
v_mfma_f32_16x16x32_bf16 v[20:23],v[248:251],v[68:71],v[20:23]
v_mfma_f32_16x16x32_bf16 v[24:27],v[252:255],v[68:71],v[24:27]
s_waitcnt lgkmcnt(1)
v_mfma_f32_16x16x32_bf16 v[24:27],v[240:243],v[64:67],v[24:27]
s_cbranch_scc1 Lg4_slow1_%=
Lg4_d1w2_%=:
v_mfma_f32_16x16x32_bf16 v[40:43],a[12:15],v[44:47],0
s_mov_b32 s73,s48
ds_read_b128 v[56:59],v2 offset:31744
v_lshlrev_b32 v72,16,v80
v_mfma_f32_16x16x32_bf16 v[40:43],a[28:31],v[48:51],v[40:43]
s_max_u32 s73,s73,s65
v_and_b32 v73,0xffff0000,v80
v_lshlrev_b32 v74,16,v81
v_mfma_f32_16x16x32_bf16 v[36:39],a[24:27],v[48:51],0
s_mov_b32 s65,0
v_and_b32 v75,0xffff0000,v81
v_lshlrev_b32 v76,16,v82
v_mfma_f32_16x16x32_bf16 v[36:39],a[8:11],v[44:47],v[36:39]
s_mul_i32 s61,s50,7
v_and_b32 v77,0xffff0000,v82
v_lshlrev_b32 v78,16,v83
v_mfma_f32_16x16x32_bf16 v[32:35],a[4:7],v[44:47],0
s_lshr_b32 s61,s61,1
ds_read_b128 v[60:63],v2 offset:32768
v_and_b32 v79,0xffff0000,v83
v_mfma_f32_16x16x32_bf16 v[32:35],a[20:23],v[48:51],v[32:35]
s_mov_b64 s[44:45],s[46:47]
v_cvt_pk_bf16_f32 v12,v12,v13
v_cvt_pk_bf16_f32 v13,v14,v15
v_mfma_f32_16x16x32_bf16 v[28:31],a[16:19],v[48:51],0
s_mov_b64 s[46:47],s[48:49]
v_cvt_pk_bf16_f32 v14,v16,v17
v_cvt_pk_bf16_f32 v15,v18,v19
v_mfma_f32_16x16x32_bf16 v[28:31],a[0:3],v[44:47],v[28:31]
s_waitcnt lgkmcnt(1)
v_add_f32 v20,v20,v72
v_add_f32 v21,v21,v73
v_mfma_f32_16x16x32_bf16 v[28:31],a[32:35],v[52:55],v[28:31]
ds_read_b128 v[64:67],v2 offset:33792
v_add_f32 v22,v22,v74
v_mfma_f32_16x16x32_bf16 v[28:31],a[48:51],v[56:59],v[28:31]
buffer_store_dwordx4 v[12:15],v3,s[36:39],s73 offen offset:0 sc1
v_add_f32 v23,v23,v75
v_add_f32 v24,v24,v76
v_add_f32 v25,v25,v77
v_mfma_f32_16x16x32_bf16 v[32:35],a[52:55],v[56:59],v[32:35]
v_add_f32 v26,v26,v78
v_add_f32 v27,v27,v79
v_mfma_f32_16x16x32_bf16 v[32:35],a[36:39],v[52:55],v[32:35]
v_cvt_pk_bf16_f32 v20,v20,v21
v_cvt_pk_bf16_f32 v21,v22,v23
v_mfma_f32_16x16x32_bf16 v[36:39],a[40:43],v[52:55],v[36:39]
s_nop 0
ds_read_b128 v[68:71],v2 offset:34816
v_cvt_pk_bf16_f32 v22,v24,v25
v_mfma_f32_16x16x32_bf16 v[36:39],a[56:59],v[56:59],v[36:39]
s_barrier
v_mfma_f32_16x16x32_bf16 v[40:43],a[60:63],v[56:59],v[40:43]
ds_read_b32 v11,v8 offset:5120
v_cvt_pk_bf16_f32 v23,v26,v27
v_mfma_f32_16x16x32_bf16 v[40:43],a[44:47],v[52:55],v[40:43]
s_waitcnt lgkmcnt(2)
v_mfma_f32_16x16x32_bf16 v[40:43],a[76:79],v[60:63],v[40:43]
ds_read_b128 v[44:47],v2 offset:35840
v_mfma_f32_16x16x32_bf16 v[40:43],a[92:95],v[64:67],v[40:43]
v_mfma_f32_16x16x32_bf16 v[36:39],a[88:91],v[64:67],v[36:39]
s_waitcnt lgkmcnt(1)
v_mfma_f32_16x16x32_bf16 v[36:39],a[72:75],v[60:63],v[36:39]
v_mfma_f32_16x16x32_bf16 v[32:35],a[68:71],v[60:63],v[32:35]
ds_read_b128 v[48:51],v2 offset:36864
v_mfma_f32_16x16x32_bf16 v[32:35],a[84:87],v[64:67],v[32:35]
buffer_store_dwordx4 v[20:23],v4,s[36:39],s73 offen offset:0 sc1
v_cmp_eq_u32 vcc,s60,v11
v_mfma_f32_16x16x32_bf16 v[28:31],a[80:83],v[64:67],v[28:31]
s_cmp_eq_u32 vcc_lo,-1
s_cselect_b32 s67,s50,s59
s_cselect_b32 s61,s61,s59
v_mfma_f32_16x16x32_bf16 v[28:31],a[64:67],v[60:63],v[28:31]
s_waitcnt lgkmcnt(1)
s_add_u32 m0,s63,0
s_add_u32 s74,s53,s55
v_mfma_f32_16x16x32_bf16 v[28:31],a[96:99],v[68:71],v[28:31]
s_cmp_eq_u32 s74,s56
ds_read_b128 v[52:55],v2 offset:37888
s_cselect_b32 s74,s57,s74
v_mfma_f32_16x16x32_bf16 v[28:31],a[112:115],v[44:47],v[28:31]
buffer_load_dwordx4 v0,s[36:39],s61 offen offset:0 sc1 lds
v_mfma_f32_16x16x32_bf16 v[32:35],a[116:119],v[44:47],v[32:35]
s_cmp_eq_u32 s74,s58
s_cselect_b32 s74,s59,s74
s_cmp_ge_u32 s74,6520832
v_mfma_f32_16x16x32_bf16 v[32:35],a[100:103],v[68:71],v[32:35]
s_cselect_b32 s55,196608,s55
v_mfma_f32_16x16x32_bf16 v[36:39],a[104:107],v[68:71],v[36:39]
ds_read_b128 v[56:59],v2 offset:38912
v_mfma_f32_16x16x32_bf16 v[36:39],a[120:123],v[44:47],v[36:39]
buffer_load_dwordx4 v0,s[36:39],s61 offen offset:1024 sc1 lds
v_mfma_f32_16x16x32_bf16 v[40:43],a[124:127],v[44:47],v[40:43]
v_mfma_f32_16x16x32_bf16 v[40:43],a[108:111],v[68:71],v[40:43]
s_waitcnt lgkmcnt(1)
v_mfma_f32_16x16x32_bf16 v[40:43],a[140:143],v[48:51],v[40:43]
ds_read_b128 v[60:63],v2 offset:39936
v_mfma_f32_16x16x32_bf16 v[40:43],a[156:159],v[52:55],v[40:43]
buffer_load_dwordx4 v0,s[36:39],s61 offen offset:2048 sc1 lds
v_mfma_f32_16x16x32_bf16 v[36:39],a[152:155],v[52:55],v[36:39]
ds_read_b128 v[88:91],v10 offset:0
v_mfma_f32_16x16x32_bf16 v[36:39],a[136:139],v[48:51],v[36:39]
v_mfma_f32_16x16x32_bf16 v[32:35],a[132:135],v[48:51],v[32:35]
ds_read_b128 v[64:67],v2 offset:40960
v_mfma_f32_16x16x32_bf16 v[32:35],a[148:151],v[52:55],v[32:35]
buffer_load_dwordx4 v0,s[36:39],s61 offen offset:3072 sc1 lds
v_mfma_f32_16x16x32_bf16 v[28:31],a[144:147],v[52:55],v[28:31]
s_add_u32 m0,s64,0
s_cmp_lt_u32 s67,s59
v_mfma_f32_16x16x32_bf16 v[28:31],a[128:131],v[48:51],v[28:31]
s_waitcnt lgkmcnt(1)
v_mfma_f32_16x16x32_bf16 v[28:31],a[160:163],v[56:59],v[28:31]
ds_read_b128 v[68:71],v2 offset:41984
v_mfma_f32_16x16x32_bf16 v[28:31],a[176:179],v[60:63],v[28:31]
ds_read_b128 v[84:87],v10 offset:1024
v_mfma_f32_16x16x32_bf16 v[32:35],v[88:91],v[60:63],v[32:35]
buffer_load_dwordx4 v[80:83],v9,s[36:39],s49 offen offset:0 sc1
v_mfma_f32_16x16x32_bf16 v[32:35],a[164:167],v[56:59],v[32:35]
s_mov_b64 s[48:49],s[50:51]
s_mov_b64 s[50:51],s[52:53]
s_mov_b32 s52,s74
v_mfma_f32_16x16x32_bf16 v[36:39],a[168:171],v[56:59],v[36:39]
ds_read_b128 v[44:47],v2 offset:43008
v_mfma_f32_16x16x32_bf16 v[36:39],a[180:183],v[60:63],v[36:39]
v_mfma_f32_16x16x32_bf16 v[40:43],a[184:187],v[60:63],v[40:43]
v_mfma_f32_16x16x32_bf16 v[40:43],a[172:175],v[56:59],v[40:43]
s_waitcnt lgkmcnt(1)
v_mfma_f32_16x16x32_bf16 v[40:43],a[200:203],v[64:67],v[40:43]
ds_read_b128 v[48:51],v2 offset:44032
v_mfma_f32_16x16x32_bf16 v[40:43],a[212:215],v[68:71],v[40:43]
ds_read_b128 v[88:91],v10 offset:2048)"
R"(
v_mfma_f32_16x16x32_bf16 v[36:39],a[208:211],v[68:71],v[36:39]
s_waitcnt vmcnt(7)
v_mfma_f32_16x16x32_bf16 v[36:39],a[196:199],v[64:67],v[36:39]
s_barrier
v_mfma_f32_16x16x32_bf16 v[32:35],a[192:195],v[64:67],v[32:35]
ds_read_b128 v[52:55],v2 offset:45056
v_mfma_f32_16x16x32_bf16 v[32:35],v[84:87],v[68:71],v[32:35]
v_mfma_f32_16x16x32_bf16 v[28:31],a[204:207],v[68:71],v[28:31]
v_mfma_f32_16x16x32_bf16 v[28:31],a[188:191],v[64:67],v[28:31]
s_waitcnt lgkmcnt(1)
buffer_load_dwordx4 v1,s[36:39],s61 offen offset:0 sc1 lds
v_mfma_f32_16x16x32_bf16 v[28:31],a[216:219],v[44:47],v[28:31]
ds_read_b128 v[56:59],v2 offset:46080
v_mfma_f32_16x16x32_bf16 v[28:31],a[232:235],v[48:51],v[28:31]
ds_read_b128 v[84:87],v10 offset:3072
v_mfma_f32_16x16x32_bf16 v[32:35],v[88:91],v[48:51],v[32:35]
v_mfma_f32_16x16x32_bf16 v[32:35],a[220:223],v[44:47],v[32:35]
v_mfma_f32_16x16x32_bf16 v[36:39],a[224:227],v[44:47],v[36:39]
ds_read_b128 v[60:63],v2 offset:47104
v_mfma_f32_16x16x32_bf16 v[36:39],a[236:239],v[48:51],v[36:39]
buffer_load_dwordx4 v1,s[36:39],s61 offen offset:1024 sc1 lds
v_mfma_f32_16x16x32_bf16 v[40:43],a[240:243],v[48:51],v[40:43]
v_mfma_f32_16x16x32_bf16 v[40:43],a[228:231],v[44:47],v[40:43]
s_waitcnt lgkmcnt(1)
v_mfma_f32_16x16x32_bf16 v[40:43],v[100:103],v[52:55],v[40:43]
ds_read_b128 v[64:67],v2 offset:48128
v_mfma_f32_16x16x32_bf16 v[40:43],v[112:115],v[56:59],v[40:43]
ds_read_b128 v[88:91],v10 offset:4096
v_mfma_f32_16x16x32_bf16 v[36:39],v[108:111],v[56:59],v[36:39]
v_mfma_f32_16x16x32_bf16 v[36:39],a[252:255],v[52:55],v[36:39]
buffer_load_dwordx4 v1,s[36:39],s61 offen offset:2048 sc1 lds
v_mfma_f32_16x16x32_bf16 v[32:35],a[248:251],v[52:55],v[32:35]
ds_read_b128 v[68:71],v2 offset:49152
v_mfma_f32_16x16x32_bf16 v[32:35],v[84:87],v[56:59],v[32:35]
v_mfma_f32_16x16x32_bf16 v[28:31],v[104:107],v[56:59],v[28:31]
v_mfma_f32_16x16x32_bf16 v[28:31],a[244:247],v[52:55],v[28:31]
s_waitcnt lgkmcnt(1)
v_mfma_f32_16x16x32_bf16 v[28:31],v[116:119],v[60:63],v[28:31]
ds_read_b128 v[44:47],v2 offset:50176
v_mfma_f32_16x16x32_bf16 v[28:31],v[132:135],v[64:67],v[28:31]
ds_read_b128 v[84:87],v10 offset:5120
v_mfma_f32_16x16x32_bf16 v[32:35],v[88:91],v[64:67],v[32:35]
v_mfma_f32_16x16x32_bf16 v[32:35],v[120:123],v[60:63],v[32:35]
v_mfma_f32_16x16x32_bf16 v[36:39],v[124:127],v[60:63],v[36:39]
ds_read_b128 v[48:51],v2 offset:51200
v_mfma_f32_16x16x32_bf16 v[36:39],v[136:139],v[64:67],v[36:39]
v_mfma_f32_16x16x32_bf16 v[40:43],v[140:143],v[64:67],v[40:43]
v_mfma_f32_16x16x32_bf16 v[40:43],v[128:131],v[60:63],v[40:43]
s_waitcnt lgkmcnt(1)
v_mfma_f32_16x16x32_bf16 v[40:43],v[156:159],v[68:71],v[40:43]
ds_read_b128 v[52:55],v2 offset:52224
v_mfma_f32_16x16x32_bf16 v[40:43],v[168:171],v[44:47],v[40:43]
ds_read_b128 v[88:91],v10 offset:6144
v_mfma_f32_16x16x32_bf16 v[36:39],v[164:167],v[44:47],v[36:39]
v_mfma_f32_16x16x32_bf16 v[36:39],v[152:155],v[68:71],v[36:39]
v_mfma_f32_16x16x32_bf16 v[32:35],v[148:151],v[68:71],v[32:35]
s_waitcnt vmcnt(8)
ds_read_b128 v[56:59],v2 offset:53248
v_mfma_f32_16x16x32_bf16 v[32:35],v[84:87],v[44:47],v[32:35]
s_barrier
v_mfma_f32_16x16x32_bf16 v[28:31],v[160:163],v[44:47],v[28:31]
v_mfma_f32_16x16x32_bf16 v[28:31],v[144:147],v[68:71],v[28:31]
s_waitcnt lgkmcnt(1)
v_mfma_f32_16x16x32_bf16 v[28:31],v[172:175],v[48:51],v[28:31]
ds_read_b128 v[60:63],v2 offset:54272
v_mfma_f32_16x16x32_bf16 v[28:31],v[188:191],v[52:55],v[28:31]
ds_read_b128 v[84:87],v10 offset:7168
v_mfma_f32_16x16x32_bf16 v[32:35],v[88:91],v[52:55],v[32:35]
v_mfma_f32_16x16x32_bf16 v[32:35],v[176:179],v[48:51],v[32:35]
v_mfma_f32_16x16x32_bf16 v[36:39],v[180:183],v[48:51],v[36:39]
ds_read_b128 v[64:67],v2 offset:55296
v_mfma_f32_16x16x32_bf16 v[36:39],v[192:195],v[52:55],v[36:39]
v_mfma_f32_16x16x32_bf16 v[40:43],v[196:199],v[52:55],v[40:43]
v_mfma_f32_16x16x32_bf16 v[40:43],v[184:187],v[48:51],v[40:43]
s_waitcnt lgkmcnt(1)
v_mfma_f32_16x16x32_bf16 v[40:43],v[212:215],v[56:59],v[40:43]
ds_read_b128 v[68:71],v2 offset:56320
v_mfma_f32_16x16x32_bf16 v[40:43],v[224:227],v[60:63],v[40:43]
ds_read_b128 v[88:91],v10 offset:8192
v_mfma_f32_16x16x32_bf16 v[36:39],v[220:223],v[60:63],v[36:39]
s_waitcnt vmcnt(3)
v_mfma_f32_16x16x32_bf16 v[36:39],v[208:211],v[56:59],v[36:39]
s_barrier
v_mfma_f32_16x16x32_bf16 v[32:35],v[204:207],v[56:59],v[32:35]
ds_read_b128 v[44:47],v2 offset:0
v_mfma_f32_16x16x32_bf16 v[32:35],v[84:87],v[60:63],v[32:35]
v_mfma_f32_16x16x32_bf16 v[28:31],v[216:219],v[60:63],v[28:31]
v_mfma_f32_16x16x32_bf16 v[28:31],v[200:203],v[56:59],v[28:31]
s_waitcnt lgkmcnt(1)
v_mfma_f32_16x16x32_bf16 v[28:31],v[228:231],v[64:67],v[28:31]
ds_read_b128 v[48:51],v2 offset:1024
v_mfma_f32_16x16x32_bf16 v[28:31],v[244:247],v[68:71],v[28:31]
v_mfma_f32_16x16x32_bf16 v[32:35],v[88:91],v[68:71],v[32:35]
v_mfma_f32_16x16x32_bf16 v[32:35],v[232:235],v[64:67],v[32:35]
v_mfma_f32_16x16x32_bf16 v[36:39],v[236:239],v[64:67],v[36:39]
ds_read_b128 v[52:55],v2 offset:2048
v_mfma_f32_16x16x32_bf16 v[36:39],v[248:251],v[68:71],v[36:39]
v_mfma_f32_16x16x32_bf16 v[40:43],v[252:255],v[68:71],v[40:43]
s_waitcnt lgkmcnt(1)
v_mfma_f32_16x16x32_bf16 v[40:43],v[240:243],v[64:67],v[40:43]
s_cbranch_scc1 Lg4_d0w2_%=
s_branch Lg4_slow0_%=
Lg4_d0w3_%=:
v_mfma_f32_16x16x32_bf16 v[24:27],a[12:15],v[44:47],0
s_mov_b32 s73,s47
ds_read_b128 v[56:59],v2 offset:3072
v_lshlrev_b32 v72,16,v80
v_mfma_f32_16x16x32_bf16 v[24:27],a[28:31],v[48:51],v[24:27]
s_max_u32 s73,s73,s65
v_and_b32 v73,0xffff0000,v80
v_lshlrev_b32 v74,16,v81
v_mfma_f32_16x16x32_bf16 v[20:23],a[24:27],v[48:51],0
s_mov_b32 s65,0
v_and_b32 v75,0xffff0000,v81
v_lshlrev_b32 v76,16,v82
v_mfma_f32_16x16x32_bf16 v[20:23],a[8:11],v[44:47],v[20:23]
s_mul_i32 s61,s49,7
v_and_b32 v77,0xffff0000,v82
v_lshlrev_b32 v78,16,v83
v_mfma_f32_16x16x32_bf16 v[16:19],a[4:7],v[44:47],0
s_lshr_b32 s61,s61,1
ds_read_b128 v[60:63],v2 offset:4096
v_and_b32 v79,0xffff0000,v83
v_mfma_f32_16x16x32_bf16 v[16:19],a[20:23],v[48:51],v[16:19]
v_cvt_pk_bf16_f32 v28,v28,v29
v_cvt_pk_bf16_f32 v29,v30,v31
v_mfma_f32_16x16x32_bf16 v[12:15],a[16:19],v[48:51],0
v_cvt_pk_bf16_f32 v30,v32,v33
v_cvt_pk_bf16_f32 v31,v34,v35
v_mfma_f32_16x16x32_bf16 v[12:15],a[0:3],v[44:47],v[12:15]
s_waitcnt lgkmcnt(1)
v_add_f32 v36,v36,v72
v_add_f32 v37,v37,v73
v_mfma_f32_16x16x32_bf16 v[12:15],a[32:35],v[52:55],v[12:15]
ds_read_b128 v[64:67],v2 offset:5120
v_add_f32 v38,v38,v74
v_mfma_f32_16x16x32_bf16 v[12:15],a[48:51],v[56:59],v[12:15]
v_add_f32 v39,v39,v75
v_add_f32 v40,v40,v76
v_mfma_f32_16x16x32_bf16 v[16:19],a[52:55],v[56:59],v[16:19]
buffer_store_dwordx4 v[28:31],v3,s[36:39],s73 offen offset:0 sc1
v_add_f32 v41,v41,v77
v_add_f32 v42,v42,v78
v_add_f32 v43,v43,v79
v_mfma_f32_16x16x32_bf16 v[16:19],a[36:39],v[52:55],v[16:19]
v_cvt_pk_bf16_f32 v36,v36,v37
v_cvt_pk_bf16_f32 v37,v38,v39
v_mfma_f32_16x16x32_bf16 v[20:23],a[40:43],v[52:55],v[20:23]
s_nop 0
ds_read_b128 v[68:71],v2 offset:6144
v_cvt_pk_bf16_f32 v38,v40,v41
v_mfma_f32_16x16x32_bf16 v[20:23],a[56:59],v[56:59],v[20:23]
s_barrier
v_mfma_f32_16x16x32_bf16 v[24:27],a[60:63],v[56:59],v[24:27]
ds_read_b32 v11,v8 offset:4096
v_cvt_pk_bf16_f32 v39,v42,v43
v_mfma_f32_16x16x32_bf16 v[24:27],a[44:47],v[52:55],v[24:27]
s_waitcnt lgkmcnt(2)
v_mfma_f32_16x16x32_bf16 v[24:27],a[76:79],v[60:63],v[24:27]
ds_read_b128 v[44:47],v2 offset:7168
v_mfma_f32_16x16x32_bf16 v[24:27],a[92:95],v[64:67],v[24:27]
v_mfma_f32_16x16x32_bf16 v[20:23],a[88:91],v[64:67],v[20:23]
s_waitcnt lgkmcnt(1)
v_mfma_f32_16x16x32_bf16 v[20:23],a[72:75],v[60:63],v[20:23]
v_mfma_f32_16x16x32_bf16 v[16:19],a[68:71],v[60:63],v[16:19]
ds_read_b128 v[48:51],v2 offset:8192
v_mfma_f32_16x16x32_bf16 v[16:19],a[84:87],v[64:67],v[16:19]
v_mfma_f32_16x16x32_bf16 v[12:15],a[80:83],v[64:67],v[12:15]
buffer_store_dwordx4 v[36:39],v4,s[36:39],s73 offen offset:0 sc1
v_cmp_eq_u32 vcc,s60,v11
v_mfma_f32_16x16x32_bf16 v[12:15],a[64:67],v[60:63],v[12:15]
s_waitcnt lgkmcnt(1)
s_cmp_eq_u32 vcc_lo,-1
s_cselect_b32 s66,s49,s59
v_mfma_f32_16x16x32_bf16 v[12:15],a[96:99],v[68:71],v[12:15]
s_cselect_b32 s61,s61,s59
ds_read_b128 v[52:55],v2 offset:9216
s_add_u32 m0,s63,28672
v_mfma_f32_16x16x32_bf16 v[12:15],a[112:115],v[44:47],v[12:15]
s_add_u32 s53,s52,s55
s_cmp_eq_u32 s53,s56
s_cselect_b32 s53,s57,s53
v_mfma_f32_16x16x32_bf16 v[16:19],a[116:119],v[44:47],v[16:19]
buffer_load_dwordx4 v0,s[36:39],s61 offen offset:0 sc1 lds
v_mfma_f32_16x16x32_bf16 v[16:19],a[100:103],v[68:71],v[16:19]
s_cmp_eq_u32 s53,s58
s_cselect_b32 s53,s59,s53
s_cmp_ge_u32 s53,6520832
v_mfma_f32_16x16x32_bf16 v[20:23],a[104:107],v[68:71],v[20:23]
s_cselect_b32 s55,196608,s55
ds_read_b128 v[56:59],v2 offset:10240
v_mfma_f32_16x16x32_bf16 v[20:23],a[120:123],v[44:47],v[20:23]
v_mfma_f32_16x16x32_bf16 v[24:27],a[124:127],v[44:47],v[24:27]
buffer_load_dwordx4 v0,s[36:39],s61 offen offset:1024 sc1 lds
v_mfma_f32_16x16x32_bf16 v[24:27],a[108:111],v[68:71],v[24:27]
s_waitcnt lgkmcnt(1)
v_mfma_f32_16x16x32_bf16 v[24:27],a[140:143],v[48:51],v[24:27]
ds_read_b128 v[60:63],v2 offset:11264
v_mfma_f32_16x16x32_bf16 v[24:27],a[156:159],v[52:55],v[24:27]
ds_read_b128 v[88:91],v10 offset:0)"
R"(
v_mfma_f32_16x16x32_bf16 v[20:23],a[152:155],v[52:55],v[20:23]
buffer_load_dwordx4 v0,s[36:39],s61 offen offset:2048 sc1 lds
v_mfma_f32_16x16x32_bf16 v[20:23],a[136:139],v[48:51],v[20:23]
v_mfma_f32_16x16x32_bf16 v[16:19],a[132:135],v[48:51],v[16:19]
ds_read_b128 v[64:67],v2 offset:12288
v_mfma_f32_16x16x32_bf16 v[16:19],a[148:151],v[52:55],v[16:19]
v_mfma_f32_16x16x32_bf16 v[12:15],a[144:147],v[52:55],v[12:15]
buffer_load_dwordx4 v0,s[36:39],s61 offen offset:3072 sc1 lds
v_mfma_f32_16x16x32_bf16 v[12:15],a[128:131],v[48:51],v[12:15]
s_waitcnt lgkmcnt(1)
s_add_u32 m0,s64,28672
s_lshr_b32 s70,s47,4
v_mfma_f32_16x16x32_bf16 v[12:15],a[160:163],v[56:59],v[12:15]
s_cmp_ge_u32 s66,s59
ds_read_b128 v[68:71],v2 offset:13312
v_mfma_f32_16x16x32_bf16 v[12:15],a[176:179],v[60:63],v[12:15]
ds_read_b128 v[84:87],v10 offset:1024
v_mfma_f32_16x16x32_bf16 v[16:19],v[88:91],v[60:63],v[16:19]
v_mfma_f32_16x16x32_bf16 v[16:19],a[164:167],v[56:59],v[16:19]
buffer_load_dwordx4 v[80:83],v9,s[36:39],s48 offen offset:0 sc1
v_mfma_f32_16x16x32_bf16 v[20:23],a[168:171],v[56:59],v[20:23]
ds_read_b128 v[44:47],v2 offset:14336
v_mfma_f32_16x16x32_bf16 v[20:23],a[180:183],v[60:63],v[20:23]
v_mfma_f32_16x16x32_bf16 v[24:27],a[184:187],v[60:63],v[24:27]
v_mfma_f32_16x16x32_bf16 v[24:27],a[172:175],v[56:59],v[24:27]
s_waitcnt lgkmcnt(1)
v_mfma_f32_16x16x32_bf16 v[24:27],a[200:203],v[64:67],v[24:27]
ds_read_b128 v[48:51],v2 offset:15360
v_mfma_f32_16x16x32_bf16 v[24:27],a[212:215],v[68:71],v[24:27]
ds_read_b128 v[88:91],v10 offset:2048
v_mfma_f32_16x16x32_bf16 v[20:23],a[208:211],v[68:71],v[20:23]
s_waitcnt vmcnt(8)
v_mfma_f32_16x16x32_bf16 v[20:23],a[196:199],v[64:67],v[20:23]
s_barrier
v_mfma_f32_16x16x32_bf16 v[16:19],a[192:195],v[64:67],v[16:19]
ds_read_b128 v[52:55],v2 offset:16384
v_mfma_f32_16x16x32_bf16 v[16:19],v[84:87],v[68:71],v[16:19]
v_mfma_f32_16x16x32_bf16 v[12:15],a[204:207],v[68:71],v[12:15]
v_mfma_f32_16x16x32_bf16 v[12:15],a[188:191],v[64:67],v[12:15]
s_waitcnt lgkmcnt(1)
v_mfma_f32_16x16x32_bf16 v[12:15],a[216:219],v[44:47],v[12:15]
buffer_load_dwordx4 v1,s[36:39],s61 offen offset:0 sc1 lds
v_mfma_f32_16x16x32_bf16 v[12:15],a[232:235],v[48:51],v[12:15]
ds_read_b128 v[56:59],v2 offset:17408
ds_read_b128 v[84:87],v10 offset:3072
v_mfma_f32_16x16x32_bf16 v[16:19],v[88:91],v[48:51],v[16:19]
v_mfma_f32_16x16x32_bf16 v[16:19],a[220:223],v[44:47],v[16:19]
v_mfma_f32_16x16x32_bf16 v[20:23],a[224:227],v[44:47],v[20:23]
ds_read_b128 v[60:63],v2 offset:18432
v_mfma_f32_16x16x32_bf16 v[20:23],a[236:239],v[48:51],v[20:23]
v_mfma_f32_16x16x32_bf16 v[24:27],a[240:243],v[48:51],v[24:27]
buffer_load_dwordx4 v1,s[36:39],s61 offen offset:1024 sc1 lds
v_mfma_f32_16x16x32_bf16 v[24:27],a[228:231],v[44:47],v[24:27]
s_waitcnt lgkmcnt(1)
v_mfma_f32_16x16x32_bf16 v[24:27],v[100:103],v[52:55],v[24:27]
ds_read_b128 v[64:67],v2 offset:19456
v_mfma_f32_16x16x32_bf16 v[24:27],v[112:115],v[56:59],v[24:27]
ds_read_b128 v[88:91],v10 offset:4096
v_mfma_f32_16x16x32_bf16 v[20:23],v[108:111],v[56:59],v[20:23]
v_mfma_f32_16x16x32_bf16 v[20:23],a[252:255],v[52:55],v[20:23]
v_mfma_f32_16x16x32_bf16 v[16:19],a[248:251],v[52:55],v[16:19]
buffer_load_dwordx4 v1,s[36:39],s61 offen offset:2048 sc1 lds
v_mfma_f32_16x16x32_bf16 v[16:19],v[84:87],v[56:59],v[16:19]
ds_read_b128 v[68:71],v2 offset:20480
v_mfma_f32_16x16x32_bf16 v[12:15],v[104:107],v[56:59],v[12:15]
v_mfma_f32_16x16x32_bf16 v[12:15],a[244:247],v[52:55],v[12:15]
s_waitcnt lgkmcnt(1)
v_mfma_f32_16x16x32_bf16 v[12:15],v[116:119],v[60:63],v[12:15]
ds_read_b128 v[44:47],v2 offset:21504
v_mfma_f32_16x16x32_bf16 v[12:15],v[132:135],v[64:67],v[12:15]
ds_read_b128 v[84:87],v10 offset:5120
v_mfma_f32_16x16x32_bf16 v[16:19],v[88:91],v[64:67],v[16:19]
v_mfma_f32_16x16x32_bf16 v[16:19],v[120:123],v[60:63],v[16:19]
v_mfma_f32_16x16x32_bf16 v[20:23],v[124:127],v[60:63],v[20:23]
ds_read_b128 v[48:51],v2 offset:22528
v_mfma_f32_16x16x32_bf16 v[20:23],v[136:139],v[64:67],v[20:23]
v_mfma_f32_16x16x32_bf16 v[24:27],v[140:143],v[64:67],v[24:27]
v_mfma_f32_16x16x32_bf16 v[24:27],v[128:131],v[60:63],v[24:27]
s_waitcnt lgkmcnt(1)
v_mfma_f32_16x16x32_bf16 v[24:27],v[156:159],v[68:71],v[24:27]
ds_read_b128 v[52:55],v2 offset:23552
v_mfma_f32_16x16x32_bf16 v[24:27],v[168:171],v[44:47],v[24:27]
ds_read_b128 v[88:91],v10 offset:6144
v_mfma_f32_16x16x32_bf16 v[20:23],v[164:167],v[44:47],v[20:23]
v_mfma_f32_16x16x32_bf16 v[20:23],v[152:155],v[68:71],v[20:23]
v_mfma_f32_16x16x32_bf16 v[16:19],v[148:151],v[68:71],v[16:19]
s_waitcnt vmcnt(8)
ds_read_b128 v[56:59],v2 offset:24576
v_mfma_f32_16x16x32_bf16 v[16:19],v[84:87],v[44:47],v[16:19]
s_barrier
v_mfma_f32_16x16x32_bf16 v[12:15],v[160:163],v[44:47],v[12:15]
v_mfma_f32_16x16x32_bf16 v[12:15],v[144:147],v[68:71],v[12:15]
s_waitcnt lgkmcnt(1)
v_mfma_f32_16x16x32_bf16 v[12:15],v[172:175],v[48:51],v[12:15]
ds_read_b128 v[60:63],v2 offset:25600
v_mfma_f32_16x16x32_bf16 v[12:15],v[188:191],v[52:55],v[12:15]
buffer_store_dword v6,v5,s[36:39],s70 offen offset:0 sc1
ds_read_b128 v[84:87],v10 offset:7168
v_mfma_f32_16x16x32_bf16 v[16:19],v[88:91],v[52:55],v[16:19]
v_mfma_f32_16x16x32_bf16 v[16:19],v[176:179],v[48:51],v[16:19]
v_mfma_f32_16x16x32_bf16 v[20:23],v[180:183],v[48:51],v[20:23]
ds_read_b128 v[64:67],v2 offset:26624
v_mfma_f32_16x16x32_bf16 v[20:23],v[192:195],v[52:55],v[20:23]
v_mfma_f32_16x16x32_bf16 v[24:27],v[196:199],v[52:55],v[24:27]
v_mfma_f32_16x16x32_bf16 v[24:27],v[184:187],v[48:51],v[24:27]
s_waitcnt lgkmcnt(1)
v_mfma_f32_16x16x32_bf16 v[24:27],v[212:215],v[56:59],v[24:27]
ds_read_b128 v[68:71],v2 offset:27648
v_mfma_f32_16x16x32_bf16 v[24:27],v[224:227],v[60:63],v[24:27]
ds_read_b128 v[88:91],v10 offset:8192
v_mfma_f32_16x16x32_bf16 v[20:23],v[220:223],v[60:63],v[20:23]
s_waitcnt vmcnt(4)
v_mfma_f32_16x16x32_bf16 v[20:23],v[208:211],v[56:59],v[20:23]
s_barrier
v_mfma_f32_16x16x32_bf16 v[16:19],v[204:207],v[56:59],v[16:19]
ds_read_b128 v[44:47],v2 offset:28672
v_mfma_f32_16x16x32_bf16 v[16:19],v[84:87],v[60:63],v[16:19]
v_mfma_f32_16x16x32_bf16 v[12:15],v[216:219],v[60:63],v[12:15]
v_mfma_f32_16x16x32_bf16 v[12:15],v[200:203],v[56:59],v[12:15]
s_waitcnt lgkmcnt(1)
v_mfma_f32_16x16x32_bf16 v[12:15],v[228:231],v[64:67],v[12:15]
ds_read_b128 v[48:51],v2 offset:29696
v_mfma_f32_16x16x32_bf16 v[12:15],v[244:247],v[68:71],v[12:15]
v_mfma_f32_16x16x32_bf16 v[16:19],v[88:91],v[68:71],v[16:19]
v_mfma_f32_16x16x32_bf16 v[16:19],v[232:235],v[64:67],v[16:19]
v_mfma_f32_16x16x32_bf16 v[20:23],v[236:239],v[64:67],v[20:23]
ds_read_b128 v[52:55],v2 offset:30720
v_mfma_f32_16x16x32_bf16 v[20:23],v[248:251],v[68:71],v[20:23]
v_mfma_f32_16x16x32_bf16 v[24:27],v[252:255],v[68:71],v[24:27]
s_waitcnt lgkmcnt(1)
v_mfma_f32_16x16x32_bf16 v[24:27],v[240:243],v[64:67],v[24:27]
s_cbranch_scc1 Lg4_slow1_%=
Lg4_d1w3_%=:
v_mfma_f32_16x16x32_bf16 v[40:43],a[12:15],v[44:47],0
s_mov_b32 s73,s48
ds_read_b128 v[56:59],v2 offset:31744
v_lshlrev_b32 v72,16,v80
v_mfma_f32_16x16x32_bf16 v[40:43],a[28:31],v[48:51],v[40:43]
s_max_u32 s73,s73,s65
v_and_b32 v73,0xffff0000,v80
v_lshlrev_b32 v74,16,v81
v_mfma_f32_16x16x32_bf16 v[36:39],a[24:27],v[48:51],0
s_mov_b32 s65,0
v_and_b32 v75,0xffff0000,v81
v_lshlrev_b32 v76,16,v82
v_mfma_f32_16x16x32_bf16 v[36:39],a[8:11],v[44:47],v[36:39]
s_mul_i32 s61,s50,7
v_and_b32 v77,0xffff0000,v82
v_lshlrev_b32 v78,16,v83
v_mfma_f32_16x16x32_bf16 v[32:35],a[4:7],v[44:47],0
s_lshr_b32 s61,s61,1
ds_read_b128 v[60:63],v2 offset:32768
v_and_b32 v79,0xffff0000,v83
v_mfma_f32_16x16x32_bf16 v[32:35],a[20:23],v[48:51],v[32:35]
s_mov_b64 s[44:45],s[46:47]
v_cvt_pk_bf16_f32 v12,v12,v13
v_cvt_pk_bf16_f32 v13,v14,v15
v_mfma_f32_16x16x32_bf16 v[28:31],a[16:19],v[48:51],0
s_mov_b64 s[46:47],s[48:49]
v_cvt_pk_bf16_f32 v14,v16,v17
v_cvt_pk_bf16_f32 v15,v18,v19
v_mfma_f32_16x16x32_bf16 v[28:31],a[0:3],v[44:47],v[28:31]
s_waitcnt lgkmcnt(1)
v_add_f32 v20,v20,v72
v_add_f32 v21,v21,v73
v_mfma_f32_16x16x32_bf16 v[28:31],a[32:35],v[52:55],v[28:31]
ds_read_b128 v[64:67],v2 offset:33792
v_add_f32 v22,v22,v74
v_mfma_f32_16x16x32_bf16 v[28:31],a[48:51],v[56:59],v[28:31]
v_add_f32 v23,v23,v75
v_add_f32 v24,v24,v76
v_mfma_f32_16x16x32_bf16 v[32:35],a[52:55],v[56:59],v[32:35]
buffer_store_dwordx4 v[12:15],v3,s[36:39],s73 offen offset:0 sc1
v_add_f32 v25,v25,v77
v_add_f32 v26,v26,v78
v_add_f32 v27,v27,v79
v_mfma_f32_16x16x32_bf16 v[32:35],a[36:39],v[52:55],v[32:35]
v_cvt_pk_bf16_f32 v20,v20,v21
v_cvt_pk_bf16_f32 v21,v22,v23
v_mfma_f32_16x16x32_bf16 v[36:39],a[40:43],v[52:55],v[36:39]
s_nop 0
ds_read_b128 v[68:71],v2 offset:34816
v_cvt_pk_bf16_f32 v22,v24,v25
v_mfma_f32_16x16x32_bf16 v[36:39],a[56:59],v[56:59],v[36:39]
s_barrier
v_mfma_f32_16x16x32_bf16 v[40:43],a[60:63],v[56:59],v[40:43]
ds_read_b32 v11,v8 offset:5120
v_cvt_pk_bf16_f32 v23,v26,v27
v_mfma_f32_16x16x32_bf16 v[40:43],a[44:47],v[52:55],v[40:43]
s_waitcnt lgkmcnt(2)
v_mfma_f32_16x16x32_bf16 v[40:43],a[76:79],v[60:63],v[40:43]
ds_read_b128 v[44:47],v2 offset:35840
v_mfma_f32_16x16x32_bf16 v[40:43],a[92:95],v[64:67],v[40:43]
v_mfma_f32_16x16x32_bf16 v[36:39],a[88:91],v[64:67],v[36:39]
s_waitcnt lgkmcnt(1)
v_mfma_f32_16x16x32_bf16 v[36:39],a[72:75],v[60:63],v[36:39]
v_mfma_f32_16x16x32_bf16 v[32:35],a[68:71],v[60:63],v[32:35]
ds_read_b128 v[48:51],v2 offset:36864
v_mfma_f32_16x16x32_bf16 v[32:35],a[84:87],v[64:67],v[32:35]
v_mfma_f32_16x16x32_bf16 v[28:31],a[80:83],v[64:67],v[28:31])"
R"(
buffer_store_dwordx4 v[20:23],v4,s[36:39],s73 offen offset:0 sc1
v_cmp_eq_u32 vcc,s60,v11
v_mfma_f32_16x16x32_bf16 v[28:31],a[64:67],v[60:63],v[28:31]
s_waitcnt lgkmcnt(1)
s_cmp_eq_u32 vcc_lo,-1
s_cselect_b32 s67,s50,s59
v_mfma_f32_16x16x32_bf16 v[28:31],a[96:99],v[68:71],v[28:31]
s_cselect_b32 s61,s61,s59
ds_read_b128 v[52:55],v2 offset:37888
s_add_u32 m0,s63,0
v_mfma_f32_16x16x32_bf16 v[28:31],a[112:115],v[44:47],v[28:31]
s_add_u32 s74,s53,s55
s_cmp_eq_u32 s74,s56
s_cselect_b32 s74,s57,s74
v_mfma_f32_16x16x32_bf16 v[32:35],a[116:119],v[44:47],v[32:35]
buffer_load_dwordx4 v0,s[36:39],s61 offen offset:0 sc1 lds
v_mfma_f32_16x16x32_bf16 v[32:35],a[100:103],v[68:71],v[32:35]
s_cmp_eq_u32 s74,s58
s_cselect_b32 s74,s59,s74
s_cmp_ge_u32 s74,6520832
v_mfma_f32_16x16x32_bf16 v[36:39],a[104:107],v[68:71],v[36:39]
s_cselect_b32 s55,196608,s55
ds_read_b128 v[56:59],v2 offset:38912
v_mfma_f32_16x16x32_bf16 v[36:39],a[120:123],v[44:47],v[36:39]
v_mfma_f32_16x16x32_bf16 v[40:43],a[124:127],v[44:47],v[40:43]
buffer_load_dwordx4 v0,s[36:39],s61 offen offset:1024 sc1 lds
v_mfma_f32_16x16x32_bf16 v[40:43],a[108:111],v[68:71],v[40:43]
s_waitcnt lgkmcnt(1)
v_mfma_f32_16x16x32_bf16 v[40:43],a[140:143],v[48:51],v[40:43]
ds_read_b128 v[60:63],v2 offset:39936
v_mfma_f32_16x16x32_bf16 v[40:43],a[156:159],v[52:55],v[40:43]
ds_read_b128 v[88:91],v10 offset:0
v_mfma_f32_16x16x32_bf16 v[36:39],a[152:155],v[52:55],v[36:39]
buffer_load_dwordx4 v0,s[36:39],s61 offen offset:2048 sc1 lds
v_mfma_f32_16x16x32_bf16 v[36:39],a[136:139],v[48:51],v[36:39]
v_mfma_f32_16x16x32_bf16 v[32:35],a[132:135],v[48:51],v[32:35]
ds_read_b128 v[64:67],v2 offset:40960
v_mfma_f32_16x16x32_bf16 v[32:35],a[148:151],v[52:55],v[32:35]
v_mfma_f32_16x16x32_bf16 v[28:31],a[144:147],v[52:55],v[28:31]
buffer_load_dwordx4 v0,s[36:39],s61 offen offset:3072 sc1 lds
v_mfma_f32_16x16x32_bf16 v[28:31],a[128:131],v[48:51],v[28:31]
s_waitcnt lgkmcnt(1)
s_add_u32 m0,s64,0
s_lshr_b32 s70,s48,4
v_mfma_f32_16x16x32_bf16 v[28:31],a[160:163],v[56:59],v[28:31]
s_cmp_lt_u32 s67,s59
ds_read_b128 v[68:71],v2 offset:41984
v_mfma_f32_16x16x32_bf16 v[28:31],a[176:179],v[60:63],v[28:31]
ds_read_b128 v[84:87],v10 offset:1024
v_mfma_f32_16x16x32_bf16 v[32:35],v[88:91],v[60:63],v[32:35]
v_mfma_f32_16x16x32_bf16 v[32:35],a[164:167],v[56:59],v[32:35]
buffer_load_dwordx4 v[80:83],v9,s[36:39],s49 offen offset:0 sc1
v_mfma_f32_16x16x32_bf16 v[36:39],a[168:171],v[56:59],v[36:39]
s_mov_b64 s[48:49],s[50:51]
ds_read_b128 v[44:47],v2 offset:43008
s_mov_b64 s[50:51],s[52:53]
v_mfma_f32_16x16x32_bf16 v[36:39],a[180:183],v[60:63],v[36:39]
s_mov_b32 s52,s74
v_mfma_f32_16x16x32_bf16 v[40:43],a[184:187],v[60:63],v[40:43]
v_mfma_f32_16x16x32_bf16 v[40:43],a[172:175],v[56:59],v[40:43]
s_waitcnt lgkmcnt(1)
v_mfma_f32_16x16x32_bf16 v[40:43],a[200:203],v[64:67],v[40:43]
ds_read_b128 v[48:51],v2 offset:44032
v_mfma_f32_16x16x32_bf16 v[40:43],a[212:215],v[68:71],v[40:43]
ds_read_b128 v[88:91],v10 offset:2048
v_mfma_f32_16x16x32_bf16 v[36:39],a[208:211],v[68:71],v[36:39]
s_waitcnt vmcnt(8)
v_mfma_f32_16x16x32_bf16 v[36:39],a[196:199],v[64:67],v[36:39]
s_barrier
v_mfma_f32_16x16x32_bf16 v[32:35],a[192:195],v[64:67],v[32:35]
ds_read_b128 v[52:55],v2 offset:45056
v_mfma_f32_16x16x32_bf16 v[32:35],v[84:87],v[68:71],v[32:35]
v_mfma_f32_16x16x32_bf16 v[28:31],a[204:207],v[68:71],v[28:31]
v_mfma_f32_16x16x32_bf16 v[28:31],a[188:191],v[64:67],v[28:31]
s_waitcnt lgkmcnt(1)
v_mfma_f32_16x16x32_bf16 v[28:31],a[216:219],v[44:47],v[28:31]
buffer_load_dwordx4 v1,s[36:39],s61 offen offset:0 sc1 lds
v_mfma_f32_16x16x32_bf16 v[28:31],a[232:235],v[48:51],v[28:31]
ds_read_b128 v[56:59],v2 offset:46080
ds_read_b128 v[84:87],v10 offset:3072
v_mfma_f32_16x16x32_bf16 v[32:35],v[88:91],v[48:51],v[32:35]
v_mfma_f32_16x16x32_bf16 v[32:35],a[220:223],v[44:47],v[32:35]
v_mfma_f32_16x16x32_bf16 v[36:39],a[224:227],v[44:47],v[36:39]
ds_read_b128 v[60:63],v2 offset:47104
v_mfma_f32_16x16x32_bf16 v[36:39],a[236:239],v[48:51],v[36:39]
v_mfma_f32_16x16x32_bf16 v[40:43],a[240:243],v[48:51],v[40:43]
buffer_load_dwordx4 v1,s[36:39],s61 offen offset:1024 sc1 lds
v_mfma_f32_16x16x32_bf16 v[40:43],a[228:231],v[44:47],v[40:43]
s_waitcnt lgkmcnt(1)
v_mfma_f32_16x16x32_bf16 v[40:43],v[100:103],v[52:55],v[40:43]
ds_read_b128 v[64:67],v2 offset:48128
v_mfma_f32_16x16x32_bf16 v[40:43],v[112:115],v[56:59],v[40:43]
ds_read_b128 v[88:91],v10 offset:4096
v_mfma_f32_16x16x32_bf16 v[36:39],v[108:111],v[56:59],v[36:39]
v_mfma_f32_16x16x32_bf16 v[36:39],a[252:255],v[52:55],v[36:39]
v_mfma_f32_16x16x32_bf16 v[32:35],a[248:251],v[52:55],v[32:35]
buffer_load_dwordx4 v1,s[36:39],s61 offen offset:2048 sc1 lds
v_mfma_f32_16x16x32_bf16 v[32:35],v[84:87],v[56:59],v[32:35]
ds_read_b128 v[68:71],v2 offset:49152
v_mfma_f32_16x16x32_bf16 v[28:31],v[104:107],v[56:59],v[28:31]
v_mfma_f32_16x16x32_bf16 v[28:31],a[244:247],v[52:55],v[28:31]
s_waitcnt lgkmcnt(1)
v_mfma_f32_16x16x32_bf16 v[28:31],v[116:119],v[60:63],v[28:31]
ds_read_b128 v[44:47],v2 offset:50176
v_mfma_f32_16x16x32_bf16 v[28:31],v[132:135],v[64:67],v[28:31]
ds_read_b128 v[84:87],v10 offset:5120
v_mfma_f32_16x16x32_bf16 v[32:35],v[88:91],v[64:67],v[32:35]
v_mfma_f32_16x16x32_bf16 v[32:35],v[120:123],v[60:63],v[32:35]
v_mfma_f32_16x16x32_bf16 v[36:39],v[124:127],v[60:63],v[36:39]
ds_read_b128 v[48:51],v2 offset:51200
v_mfma_f32_16x16x32_bf16 v[36:39],v[136:139],v[64:67],v[36:39]
v_mfma_f32_16x16x32_bf16 v[40:43],v[140:143],v[64:67],v[40:43]
v_mfma_f32_16x16x32_bf16 v[40:43],v[128:131],v[60:63],v[40:43]
s_waitcnt lgkmcnt(1)
v_mfma_f32_16x16x32_bf16 v[40:43],v[156:159],v[68:71],v[40:43]
ds_read_b128 v[52:55],v2 offset:52224
v_mfma_f32_16x16x32_bf16 v[40:43],v[168:171],v[44:47],v[40:43]
ds_read_b128 v[88:91],v10 offset:6144
v_mfma_f32_16x16x32_bf16 v[36:39],v[164:167],v[44:47],v[36:39]
v_mfma_f32_16x16x32_bf16 v[36:39],v[152:155],v[68:71],v[36:39]
v_mfma_f32_16x16x32_bf16 v[32:35],v[148:151],v[68:71],v[32:35]
s_waitcnt vmcnt(8)
ds_read_b128 v[56:59],v2 offset:53248
v_mfma_f32_16x16x32_bf16 v[32:35],v[84:87],v[44:47],v[32:35]
s_barrier
v_mfma_f32_16x16x32_bf16 v[28:31],v[160:163],v[44:47],v[28:31]
v_mfma_f32_16x16x32_bf16 v[28:31],v[144:147],v[68:71],v[28:31]
s_waitcnt lgkmcnt(1)
v_mfma_f32_16x16x32_bf16 v[28:31],v[172:175],v[48:51],v[28:31]
ds_read_b128 v[60:63],v2 offset:54272
v_mfma_f32_16x16x32_bf16 v[28:31],v[188:191],v[52:55],v[28:31]
buffer_store_dword v6,v5,s[36:39],s70 offen offset:0 sc1
ds_read_b128 v[84:87],v10 offset:7168
v_mfma_f32_16x16x32_bf16 v[32:35],v[88:91],v[52:55],v[32:35]
v_mfma_f32_16x16x32_bf16 v[32:35],v[176:179],v[48:51],v[32:35]
v_mfma_f32_16x16x32_bf16 v[36:39],v[180:183],v[48:51],v[36:39]
ds_read_b128 v[64:67],v2 offset:55296
v_mfma_f32_16x16x32_bf16 v[36:39],v[192:195],v[52:55],v[36:39]
v_mfma_f32_16x16x32_bf16 v[40:43],v[196:199],v[52:55],v[40:43]
v_mfma_f32_16x16x32_bf16 v[40:43],v[184:187],v[48:51],v[40:43]
s_waitcnt lgkmcnt(1)
v_mfma_f32_16x16x32_bf16 v[40:43],v[212:215],v[56:59],v[40:43]
ds_read_b128 v[68:71],v2 offset:56320
v_mfma_f32_16x16x32_bf16 v[40:43],v[224:227],v[60:63],v[40:43]
ds_read_b128 v[88:91],v10 offset:8192
v_mfma_f32_16x16x32_bf16 v[36:39],v[220:223],v[60:63],v[36:39]
s_waitcnt vmcnt(4)
v_mfma_f32_16x16x32_bf16 v[36:39],v[208:211],v[56:59],v[36:39]
s_barrier
v_mfma_f32_16x16x32_bf16 v[32:35],v[204:207],v[56:59],v[32:35]
ds_read_b128 v[44:47],v2 offset:0
v_mfma_f32_16x16x32_bf16 v[32:35],v[84:87],v[60:63],v[32:35]
v_mfma_f32_16x16x32_bf16 v[28:31],v[216:219],v[60:63],v[28:31]
v_mfma_f32_16x16x32_bf16 v[28:31],v[200:203],v[56:59],v[28:31]
s_waitcnt lgkmcnt(1)
v_mfma_f32_16x16x32_bf16 v[28:31],v[228:231],v[64:67],v[28:31]
ds_read_b128 v[48:51],v2 offset:1024
v_mfma_f32_16x16x32_bf16 v[28:31],v[244:247],v[68:71],v[28:31]
v_mfma_f32_16x16x32_bf16 v[32:35],v[88:91],v[68:71],v[32:35]
v_mfma_f32_16x16x32_bf16 v[32:35],v[232:235],v[64:67],v[32:35]
v_mfma_f32_16x16x32_bf16 v[36:39],v[236:239],v[64:67],v[36:39]
ds_read_b128 v[52:55],v2 offset:2048
v_mfma_f32_16x16x32_bf16 v[36:39],v[248:251],v[68:71],v[36:39]
v_mfma_f32_16x16x32_bf16 v[40:43],v[252:255],v[68:71],v[40:43]
s_waitcnt lgkmcnt(1)
v_mfma_f32_16x16x32_bf16 v[40:43],v[240:243],v[64:67],v[40:43]
s_cbranch_scc1 Lg4_d0w3_%=
s_branch Lg4_slow0_%=
Lg4_slow0_%=:
ds_read_b32 v11,v8 offset:6144
s_waitcnt lgkmcnt(0)
v_cmp_eq_u32 vcc,s60,v11
s_nop 0
s_cmp_eq_u32 vcc_lo,-1
s_cbranch_scc1 Lg4_gs0_%=
Lg4_slowb0_%=:
s_cmp_eq_u32 s54,1
s_cselect_b32 s68,s49,s48
s_mul_i32 s68,s68,5
s_lshr_b32 s68,s68,6
s_add_u32 m0,s78,0
s_nop 0
buffer_load_dword v7,s[36:39],s68 offen offset:0 sc1 lds
s_mov_b32 s73,s47
v_lshlrev_b32 v72,16,v80
v_and_b32 v73,0xffff0000,v80
v_lshlrev_b32 v74,16,v81
v_and_b32 v75,0xffff0000,v81
v_lshlrev_b32 v76,16,v82
v_and_b32 v77,0xffff0000,v82
v_lshlrev_b32 v78,16,v83
v_and_b32 v79,0xffff0000,v83
v_cvt_pk_bf16_f32 v28,v28,v29
v_cvt_pk_bf16_f32 v29,v30,v31
v_cvt_pk_bf16_f32 v30,v32,v33
v_cvt_pk_bf16_f32 v31,v34,v35
s_add_u32 m0,s78,1024
s_nop 0
buffer_load_dword v7,s[36:39],s68 offen offset:0 sc1 lds
buffer_store_dwordx4 v[28:31],v3,s[36:39],s73 offen offset:0 sc1)"
R"(
v_add_f32 v36,v36,v72
v_add_f32 v37,v37,v73
v_add_f32 v38,v38,v74
v_add_f32 v39,v39,v75
v_add_f32 v40,v40,v76
v_add_f32 v41,v41,v77
v_add_f32 v42,v42,v78
v_add_f32 v43,v43,v79
v_cvt_pk_bf16_f32 v36,v36,v37
v_cvt_pk_bf16_f32 v37,v38,v39
v_cvt_pk_bf16_f32 v38,v40,v41
v_cvt_pk_bf16_f32 v39,v42,v43
buffer_store_dwordx4 v[36:39],v4,s[36:39],s73 offen offset:0 sc1
s_cmp_ge_u32 s48,s59
s_cbranch_scc1 Lg4_done0_%=
s_add_u32 m0,s78,2048
s_nop 0
buffer_load_dword v7,s[36:39],s68 offen offset:0 sc1 lds
s_waitcnt vmcnt(4)
s_barrier
ds_read_b32 v11,v8 offset:0
s_mov_b32 s69,2304
s_waitcnt lgkmcnt(0)
v_cmp_eq_u32 vcc,s60,v11
s_nop 0
s_cmp_eq_u32 vcc_lo,-1
s_cbranch_scc1 Lg4_go0_%=
s_add_u32 m0,s78,3072
s_nop 0
buffer_load_dword v7,s[36:39],s68 offen offset:0 sc1 lds
s_waitcnt vmcnt(4)
s_barrier
ds_read_b32 v11,v8 offset:1024
s_mov_b32 s69,3328
s_waitcnt lgkmcnt(0)
v_cmp_eq_u32 vcc,s60,v11
s_nop 0
s_cmp_eq_u32 vcc_lo,-1
s_cbranch_scc1 Lg4_go0_%=
s_add_u32 m0,s78,0
s_nop 0
buffer_load_dword v7,s[36:39],s68 offen offset:0 sc1 lds
s_waitcnt vmcnt(2)
s_barrier
ds_read_b32 v11,v8 offset:2048
s_lshr_b32 s70,s47,4
buffer_store_dword v6,v5,s[36:39],s70 offen offset:0 sc1
s_mov_b32 s47,s59
s_mov_b32 s69,256
s_waitcnt lgkmcnt(0)
v_cmp_eq_u32 vcc,s60,v11
s_nop 0
s_cmp_eq_u32 vcc_lo,-1
s_cbranch_scc1 Lg4_go0_%=
s_add_u32 m0,s78,1024
s_nop 0
buffer_load_dword v7,s[36:39],s68 offen offset:0 sc1 lds
s_waitcnt vmcnt(3)
s_barrier
ds_read_b32 v11,v8 offset:3072
s_mov_b32 s69,1280
s_waitcnt lgkmcnt(0)
v_cmp_eq_u32 vcc,s60,v11
s_nop 0
s_cmp_eq_u32 vcc_lo,-1
s_cbranch_scc1 Lg4_go0_%=
Lg4_poll0_%=:
s_add_u32 m0,s78,2048
s_nop 0
buffer_load_dword v7,s[36:39],s68 offen offset:0 sc1 lds
s_waitcnt vmcnt(2)
s_barrier
ds_read_b32 v11,v8 offset:0
s_mov_b32 s69,2304
s_waitcnt lgkmcnt(0)
v_cmp_eq_u32 vcc,s60,v11
s_nop 0
s_cmp_eq_u32 vcc_lo,-1
s_cbranch_scc1 Lg4_go0_%=
s_sleep 4
s_add_u32 m0,s78,3072
s_nop 0
buffer_load_dword v7,s[36:39],s68 offen offset:0 sc1 lds
s_waitcnt vmcnt(2)
s_barrier
ds_read_b32 v11,v8 offset:1024
s_mov_b32 s69,3328
s_waitcnt lgkmcnt(0)
v_cmp_eq_u32 vcc,s60,v11
s_nop 0
s_cmp_eq_u32 vcc_lo,-1
s_cbranch_scc1 Lg4_go0_%=
s_sleep 4
s_add_u32 m0,s78,0
s_nop 0
buffer_load_dword v7,s[36:39],s68 offen offset:0 sc1 lds
s_waitcnt vmcnt(2)
s_barrier
ds_read_b32 v11,v8 offset:2048
s_mov_b32 s69,256
s_waitcnt lgkmcnt(0)
v_cmp_eq_u32 vcc,s60,v11
s_nop 0
s_cmp_eq_u32 vcc_lo,-1
s_cbranch_scc1 Lg4_go0_%=
s_sleep 4
s_add_u32 m0,s78,1024
s_nop 0
buffer_load_dword v7,s[36:39],s68 offen offset:0 sc1 lds
s_waitcnt vmcnt(2)
s_barrier
ds_read_b32 v11,v8 offset:3072
s_mov_b32 s69,1280
s_waitcnt lgkmcnt(0)
v_cmp_eq_u32 vcc,s60,v11
s_nop 0
s_cmp_eq_u32 vcc_lo,-1
s_cbranch_scc1 Lg4_go0_%=
s_sleep 4
s_add_u32 s79,s79,1
s_cmp_lt_u32 s79,20000
s_cbranch_scc1 Lg4_poll0_%=
Lg4_go0_%=:
s_mul_i32 s61,s48,7
s_lshr_b32 s61,s61,1
s_add_u32 m0,s63,0
s_nop 0
buffer_load_dwordx4 v0,s[36:39],s61 offen offset:0 sc1 lds
buffer_load_dwordx4 v0,s[36:39],s61 offen offset:1024 sc1 lds
buffer_load_dwordx4 v0,s[36:39],s61 offen offset:2048 sc1 lds
buffer_load_dwordx4 v0,s[36:39],s61 offen offset:3072 sc1 lds
s_add_u32 m0,s64,0
s_nop 0
buffer_load_dwordx4 v1,s[36:39],s61 offen offset:0 sc1 lds
buffer_load_dwordx4 v1,s[36:39],s61 offen offset:1024 sc1 lds
buffer_load_dwordx4 v1,s[36:39],s61 offen offset:2048 sc1 lds
s_mul_i32 s68,s49,5
s_lshr_b32 s68,s68,6
s_add_u32 m0,s78,4096
s_nop 0
buffer_load_dword v7,s[36:39],s68 offen offset:0 sc1 lds
s_waitcnt vmcnt(4)
s_barrier
ds_read_b128 v[44:47],v2 offset:0
ds_read_b128 v[48:51],v2 offset:1024
ds_read_b128 v[52:55],v2 offset:2048
s_lshr_b32 s70,s47,4
buffer_store_dword v6,v5,s[36:39],s70 offen offset:0 sc1
s_mov_b32 s47,s59
s_waitcnt lgkmcnt(1)
s_setpc_b64 s[90:91]
Lg4_gs0_%=:
s_mul_i32 s61,s48,7
s_lshr_b32 s61,s61,1
s_cmp_eq_u32 s54,1
s_cselect_b32 s68,s49,s48
s_mul_i32 s68,s68,5
s_lshr_b32 s68,s68,6
s_add_u32 m0,s63,0
s_nop 0
buffer_load_dwordx4 v0,s[36:39],s61 offen offset:0 sc1 lds
buffer_load_dwordx4 v0,s[36:39],s61 offen offset:1024 sc1 lds
buffer_load_dwordx4 v0,s[36:39],s61 offen offset:2048 sc1 lds
buffer_load_dwordx4 v0,s[36:39],s61 offen offset:3072 sc1 lds
s_add_u32 m0,s64,0
s_nop 0
buffer_load_dwordx4 v1,s[36:39],s61 offen offset:0 sc1 lds
buffer_load_dwordx4 v1,s[36:39],s61 offen offset:1024 sc1 lds
buffer_load_dwordx4 v1,s[36:39],s61 offen offset:2048 sc1 lds
s_mul_i32 s68,s49,5
s_lshr_b32 s68,s68,6
s_add_u32 m0,s78,4096
s_nop 0
buffer_load_dword v7,s[36:39],s68 offen offset:0 sc1 lds
s_waitcnt vmcnt(4)
s_barrier
ds_read_b128 v[44:47],v2 offset:0
ds_read_b128 v[48:51],v2 offset:1024
ds_read_b128 v[52:55],v2 offset:2048
s_lshr_b32 s70,s59,4
buffer_store_dword v6,v5,s[36:39],s70 offen offset:0 sc1
s_waitcnt lgkmcnt(1)
s_setpc_b64 s[90:91]
Lg4_done0_%=:
s_waitcnt vmcnt(0)
s_barrier
s_lshr_b32 s70,s47,4
buffer_store_dword v6,v5,s[36:39],s70 offen offset:0 sc1
s_branch Lg4_done_%=
Lg4_slow1_%=:
ds_read_b32 v11,v8 offset:6144
s_waitcnt lgkmcnt(0)
v_cmp_eq_u32 vcc,s60,v11
s_nop 0
s_cmp_eq_u32 vcc_lo,-1
s_cbranch_scc1 Lg4_gs1_%=
Lg4_slowb1_%=:
s_cmp_eq_u32 s54,1
s_cselect_b32 s68,s50,s49
s_mul_i32 s68,s68,5)"
R"(
s_lshr_b32 s68,s68,6
s_add_u32 m0,s78,0
s_nop 0
buffer_load_dword v7,s[36:39],s68 offen offset:0 sc1 lds
s_mov_b32 s73,s48
v_lshlrev_b32 v72,16,v80
v_and_b32 v73,0xffff0000,v80
v_lshlrev_b32 v74,16,v81
v_and_b32 v75,0xffff0000,v81
v_lshlrev_b32 v76,16,v82
v_and_b32 v77,0xffff0000,v82
v_lshlrev_b32 v78,16,v83
v_and_b32 v79,0xffff0000,v83
v_cvt_pk_bf16_f32 v12,v12,v13
v_cvt_pk_bf16_f32 v13,v14,v15
v_cvt_pk_bf16_f32 v14,v16,v17
v_cvt_pk_bf16_f32 v15,v18,v19
s_add_u32 m0,s78,1024
s_nop 0
buffer_load_dword v7,s[36:39],s68 offen offset:0 sc1 lds
buffer_store_dwordx4 v[12:15],v3,s[36:39],s73 offen offset:0 sc1
v_add_f32 v20,v20,v72
v_add_f32 v21,v21,v73
v_add_f32 v22,v22,v74
v_add_f32 v23,v23,v75
v_add_f32 v24,v24,v76
v_add_f32 v25,v25,v77
v_add_f32 v26,v26,v78
v_add_f32 v27,v27,v79
v_cvt_pk_bf16_f32 v20,v20,v21
v_cvt_pk_bf16_f32 v21,v22,v23
v_cvt_pk_bf16_f32 v22,v24,v25
v_cvt_pk_bf16_f32 v23,v26,v27
buffer_store_dwordx4 v[20:23],v4,s[36:39],s73 offen offset:0 sc1
s_cmp_ge_u32 s49,s59
s_cbranch_scc1 Lg4_done1_%=
s_add_u32 m0,s78,2048
s_nop 0
buffer_load_dword v7,s[36:39],s68 offen offset:0 sc1 lds
s_waitcnt vmcnt(4)
s_barrier
ds_read_b32 v11,v8 offset:0
s_mov_b32 s69,2304
s_waitcnt lgkmcnt(0)
v_cmp_eq_u32 vcc,s60,v11
s_nop 0
s_cmp_eq_u32 vcc_lo,-1
s_cbranch_scc1 Lg4_go1_%=
s_add_u32 m0,s78,3072
s_nop 0
buffer_load_dword v7,s[36:39],s68 offen offset:0 sc1 lds
s_waitcnt vmcnt(4)
s_barrier
ds_read_b32 v11,v8 offset:1024
s_mov_b32 s69,3328
s_waitcnt lgkmcnt(0)
v_cmp_eq_u32 vcc,s60,v11
s_nop 0
s_cmp_eq_u32 vcc_lo,-1
s_cbranch_scc1 Lg4_go1_%=
s_add_u32 m0,s78,0
s_nop 0
buffer_load_dword v7,s[36:39],s68 offen offset:0 sc1 lds
s_waitcnt vmcnt(2)
s_barrier
ds_read_b32 v11,v8 offset:2048
s_lshr_b32 s70,s48,4
buffer_store_dword v6,v5,s[36:39],s70 offen offset:0 sc1
s_mov_b32 s48,s59
s_mov_b32 s69,256
s_waitcnt lgkmcnt(0)
v_cmp_eq_u32 vcc,s60,v11
s_nop 0
s_cmp_eq_u32 vcc_lo,-1
s_cbranch_scc1 Lg4_go1_%=
s_add_u32 m0,s78,1024
s_nop 0
buffer_load_dword v7,s[36:39],s68 offen offset:0 sc1 lds
s_waitcnt vmcnt(3)
s_barrier
ds_read_b32 v11,v8 offset:3072
s_mov_b32 s69,1280
s_waitcnt lgkmcnt(0)
v_cmp_eq_u32 vcc,s60,v11
s_nop 0
s_cmp_eq_u32 vcc_lo,-1
s_cbranch_scc1 Lg4_go1_%=
Lg4_poll1_%=:
s_add_u32 m0,s78,2048
s_nop 0
buffer_load_dword v7,s[36:39],s68 offen offset:0 sc1 lds
s_waitcnt vmcnt(2)
s_barrier
ds_read_b32 v11,v8 offset:0
s_mov_b32 s69,2304
s_waitcnt lgkmcnt(0)
v_cmp_eq_u32 vcc,s60,v11
s_nop 0
s_cmp_eq_u32 vcc_lo,-1
s_cbranch_scc1 Lg4_go1_%=
s_sleep 4
s_add_u32 m0,s78,3072
s_nop 0
buffer_load_dword v7,s[36:39],s68 offen offset:0 sc1 lds
s_waitcnt vmcnt(2)
s_barrier
ds_read_b32 v11,v8 offset:1024
s_mov_b32 s69,3328
s_waitcnt lgkmcnt(0)
v_cmp_eq_u32 vcc,s60,v11
s_nop 0
s_cmp_eq_u32 vcc_lo,-1
s_cbranch_scc1 Lg4_go1_%=
s_sleep 4
s_add_u32 m0,s78,0
s_nop 0
buffer_load_dword v7,s[36:39],s68 offen offset:0 sc1 lds
s_waitcnt vmcnt(2)
s_barrier
ds_read_b32 v11,v8 offset:2048
s_mov_b32 s69,256
s_waitcnt lgkmcnt(0)
v_cmp_eq_u32 vcc,s60,v11
s_nop 0
s_cmp_eq_u32 vcc_lo,-1
s_cbranch_scc1 Lg4_go1_%=
s_sleep 4
s_add_u32 m0,s78,1024
s_nop 0
buffer_load_dword v7,s[36:39],s68 offen offset:0 sc1 lds
s_waitcnt vmcnt(2)
s_barrier
ds_read_b32 v11,v8 offset:3072
s_mov_b32 s69,1280
s_waitcnt lgkmcnt(0)
v_cmp_eq_u32 vcc,s60,v11
s_nop 0
s_cmp_eq_u32 vcc_lo,-1
s_cbranch_scc1 Lg4_go1_%=
s_sleep 4
s_add_u32 s79,s79,1
s_cmp_lt_u32 s79,20000
s_cbranch_scc1 Lg4_poll1_%=
Lg4_go1_%=:
s_mul_i32 s61,s49,7
s_lshr_b32 s61,s61,1
s_add_u32 m0,s63,28672
s_nop 0
buffer_load_dwordx4 v0,s[36:39],s61 offen offset:0 sc1 lds
buffer_load_dwordx4 v0,s[36:39],s61 offen offset:1024 sc1 lds
buffer_load_dwordx4 v0,s[36:39],s61 offen offset:2048 sc1 lds
buffer_load_dwordx4 v0,s[36:39],s61 offen offset:3072 sc1 lds
s_add_u32 m0,s64,28672
s_nop 0
buffer_load_dwordx4 v1,s[36:39],s61 offen offset:0 sc1 lds
buffer_load_dwordx4 v1,s[36:39],s61 offen offset:1024 sc1 lds
buffer_load_dwordx4 v1,s[36:39],s61 offen offset:2048 sc1 lds
s_mul_i32 s68,s50,5
s_lshr_b32 s68,s68,6
s_add_u32 m0,s78,5120
s_nop 0
buffer_load_dword v7,s[36:39],s68 offen offset:0 sc1 lds
s_waitcnt vmcnt(4)
s_barrier
ds_read_b128 v[44:47],v2 offset:28672
ds_read_b128 v[48:51],v2 offset:29696
ds_read_b128 v[52:55],v2 offset:30720
s_lshr_b32 s70,s48,4
buffer_store_dword v6,v5,s[36:39],s70 offen offset:0 sc1
s_mov_b32 s48,s59
s_waitcnt lgkmcnt(1)
s_setpc_b64 s[92:93]
Lg4_gs1_%=:
s_mul_i32 s61,s49,7
s_lshr_b32 s61,s61,1
s_cmp_eq_u32 s54,1
s_cselect_b32 s68,s50,s49
s_mul_i32 s68,s68,5
s_lshr_b32 s68,s68,6
s_add_u32 m0,s63,28672
s_nop 0
buffer_load_dwordx4 v0,s[36:39],s61 offen offset:0 sc1 lds
buffer_load_dwordx4 v0,s[36:39],s61 offen offset:1024 sc1 lds
buffer_load_dwordx4 v0,s[36:39],s61 offen offset:2048 sc1 lds
buffer_load_dwordx4 v0,s[36:39],s61 offen offset:3072 sc1 lds
s_add_u32 m0,s64,28672
s_nop 0
buffer_load_dwordx4 v1,s[36:39],s61 offen offset:0 sc1 lds
buffer_load_dwordx4 v1,s[36:39],s61 offen offset:1024 sc1 lds
buffer_load_dwordx4 v1,s[36:39],s61 offen offset:2048 sc1 lds
s_mul_i32 s68,s50,5
s_lshr_b32 s68,s68,6
s_add_u32 m0,s78,5120
s_nop 0
buffer_load_dword v7,s[36:39],s68 offen offset:0 sc1 lds
s_waitcnt vmcnt(4)
s_barrier
ds_read_b128 v[44:47],v2 offset:28672
ds_read_b128 v[48:51],v2 offset:29696
ds_read_b128 v[52:55],v2 offset:30720)"
R"(
s_lshr_b32 s70,s59,4
buffer_store_dword v6,v5,s[36:39],s70 offen offset:0 sc1
s_waitcnt lgkmcnt(1)
s_setpc_b64 s[92:93]
Lg4_done1_%=:
s_waitcnt vmcnt(0)
s_barrier
s_lshr_b32 s70,s48,4
buffer_store_dword v6,v5,s[36:39],s70 offen offset:0 sc1
s_branch Lg4_done_%=
Lg4_done_%=:
s_waitcnt vmcnt(0) lgkmcnt(0)
s_barrier
s_endpgm
Lcode_g4_e:)"
  :: "s"(ws), "s"(wave), "s"(sStep), "s"(sX1), "s"(sX2), "s"(sU0), "s"(sWw), "s"(sBuf), "s"(sEp), "v"(vSrc), "v"(vSrc2), "v"(vA), "v"(vOut), "v"(vOut2), "v"(vFlag), "v"(vPollOff), "v"(vSnapRd), "v"(vAuxS), "v"(vWd0), "v"(vWd1), "v"(vWr0), "v"(vWr1), "v"(vWr2), "s"(w4) : "v0", "v1", "v2", "v3", "v4", "v5", "v6", "v7", "v8", "v9", "v10", "v11", "v12", "v13", "v14", "v15", "v16", "v17", "v18", "v19", "v20", "v21", "v22", "v23", "v24", "v25", "v26", "v27", "v28", "v29", "v30", "v31", "v32", "v33", "v34", "v35", "v36", "v37", "v38", "v39", "v40", "v41", "v42", "v43", "v44", "v45", "v46", "v47", "v48", "v49", "v50", "v51", "v52", "v53", "v54", "v55", "v56", "v57", "v58", "v59", "v60", "v61", "v62", "v63", "v64", "v65", "v66", "v67", "v68", "v69", "v70", "v71", "v72", "v73", "v74", "v75", "v76", "v77", "v78", "v79", "v80", "v81", "v82", "v83", "v84", "v85", "v86", "v87", "v88", "v89", "v90", "v91", "v92", "v93", "v94", "v95", "v96", "v97", "v98", "v99", "v100", "v101", "v102", "v103", "v104", "v105", "v106", "v107", "v108", "v109", "v110", "v111", "v112", "v113", "v114", "v115", "v116", "v117", "v118", "v119", "v120", "v121", "v122", "v123", "v124", "v125", "v126", "v127", "v128", "v129", "v130", "v131", "v132", "v133", "v134", "v135", "v136", "v137", "v138", "v139", "v140", "v141", "v142", "v143", "v144", "v145", "v146", "v147", "v148", "v149", "v150", "v151", "v152", "v153", "v154", "v155", "v156", "v157", "v158", "v159", "v160", "v161", "v162", "v163", "v164", "v165", "v166", "v167", "v168", "v169", "v170", "v171", "v172", "v173", "v174", "v175", "v176", "v177", "v178", "v179", "v180", "v181", "v182", "v183", "v184", "v185", "v186", "v187", "v188", "v189", "v190", "v191", "v192", "v193", "v194", "v195", "v196", "v197", "v198", "v199", "v200", "v201", "v202", "v203", "v204", "v205", "v206", "v207", "v208", "v209", "v210", "v211", "v212", "v213", "v214", "v215", "v216", "v217", "v218", "v219", "v220", "v221", "v222", "v223", "v224", "v225", "v226", "v227", "v228", "v229", "v230", "v231", "v255", "a0", "a1", "a2", "a3", "a4", "a5", "a6", "a7", "a8", "a9", "a10", "a11", "a12", "a13", "a14", "a15", "a16", "a17", "a18", "a19", "a20", "a21", "a22", "a23", "a24", "a25", "a26", "a27", "a28", "a29", "a30", "a31", "a32", "a33", "a34", "a35", "a36", "a37", "a38", "a39", "a40", "a41", "a42", "a43", "a44", "a45", "a46", "a47", "a48", "a49", "a50", "a51", "a52", "a53", "a54", "a55", "a56", "a57", "a58", "a59", "a60", "a61", "a62", "a63", "a64", "a65", "a66", "a67", "a68", "a69", "a70", "a71", "a72", "a73", "a74", "a75", "a76", "a77", "a78", "a79", "a80", "a81", "a82", "a83", "a84", "a85", "a86", "a87", "a88", "a89", "a90", "a91", "a92", "a93", "a94", "a95", "a96", "a97", "a98", "a99", "a100", "a101", "a102", "a103", "a104", "a105", "a106", "a107", "a108", "a109", "a110", "a111", "a112", "a113", "a114", "a115", "a116", "a117", "a118", "a119", "a120", "a121", "a122", "a123", "a124", "a125", "a126", "a127", "a128", "a129", "a130", "a131", "a132", "a133", "a134", "a135", "a136", "a137", "a138", "a139", "a140", "a141", "a142", "a143", "a144", "a145", "a146", "a147", "a148", "a149", "a150", "a151", "a152", "a153", "a154", "a155", "a156", "a157", "a158", "a159", "a160", "a161", "a162", "a163", "a164", "a165", "a166", "a167", "a168", "a169", "a170", "a171", "a172", "a173", "a174", "a175", "a176", "a177", "a178", "a179", "a180", "a181", "a182", "a183", "a184", "a185", "a186", "a187", "a188", "a189", "a190", "a191", "a192", "a193", "a194", "a195", "a196", "a197", "a198", "a199", "a200", "a201", "a202", "a203", "a204", "a205", "a206", "a207", "a208", "a209", "a210", "a211", "a212", "a213", "a214", "a215", "a216", "a217", "a218", "a219", "a220", "a221", "a222", "a223", "a224", "a225", "a226", "a227", "a228", "a229", "a230", "a231", "a232", "a233", "a234", "a235", "a236", "a237", "a238", "a239", "a240", "a241", "a242", "a243", "a244", "a245", "a246", "a247", "a248", "a249", "a250", "a251", "a252", "a253", "a254", "a255", "s36", "s37", "s38", "s39", "s40", "s41", "s42", "s43", "s44", "s45", "s46", "s47", "s48", "s49", "s50", "s51", "s52", "s53", "s54", "s55", "s56", "s57", "s58", "s59", "s60", "s61", "s62", "s63", "s64", "s65", "s66", "s67", "s68", "s69", "s70", "s71", "s72", "s73", "s74", "s75", "s76", "s77", "s78", "s79", "s80", "s81", "s82", "s83", "s84", "s85", "s86", "s87", "s88", "s89", "s90", "s91", "s92", "s93", "s94", "s95", "s96", "s97", "s98", "s99", "vcc", "scc", "memory");
    __builtin_unreachable();
  } else {
    KARG(ws, 10) KARG(out_, 11)
    u16* out = (u16*)out_;
    LANE_ID(lane2);
    constexpr int NRF = N3 ? -16 * NX : N1 ? 2 : NRW;
    const int wk = kind == 6 ? slice : NRF + (kind - 1) * 32 + rep * 8 + slice;
    constexpr int T1_ = 256 - FR * FINE, T2_ = T1_ - 2 * FR2 * FINE;
    const int wv = wk * 4 + wave, fine = wv < FINE * 16 ? 0 : 4096, c4 = (TE + wk) * 16 + wave * 4, c2 = T2_ * 16 + 2 * wv + fine, c1 = T1_ * 16 + wv + fine;
    unsigned f = ep + 1;
    sums_last<4>(ws, out, wv * 4 + (wk < 32 + NRF ? 0 : 4096), (32 + NRF) * 16, TE * 16, c4, lane2, ep, f);
    sums_last<4>(ws, out, c4, (64 + NRF) * 16, T2_ * 16, FR2 ? c2 : c1, lane2, ep, f);
    if (FR2) sums_last<2>(ws, out, c2, FINE * 32, T1_ * 16, c1, lane2, ep, f);
    sums_last<1>(ws, out, c1, FINE * 16, 4096, 4095, lane2, ep, f);
    if (b == 0 && lane2 == 0 && wave == 0) __hip_atomic_store((gu32*)(ws + OFF_EP), ep, __ATOMIC_RELAXED, __HIP_MEMORY_SCOPE_AGENT);
    asm volatile("s_endpgm" ::: "memory");
    __builtin_unreachable();
  }
}

extern "C" void kernel_launch(void* const* d_in, const int* in_sizes, int n_in, void* d_out, int out_size,
                              void* d_ws, size_t ws_size, hipStream_t stream) {
  const u16* q = (const u16*)d_in[0];
  const u16* k = (const u16*)d_in[1];
  const u16* v = (const u16*)d_in[2];
  const u16* shift_k = (const u16*)d_in[3];
  const u16* ssm_k = (const u16*)d_in[4];
  const u16* ssm_D = (const u16*)d_in[5];
  const u16* w1 = (const u16*)d_in[7];
  const u16* w2 = (const u16*)d_in[8];
  const u16* w3 = (const u16*)d_in[9];
  const u16* w4 = (const u16*)d_in[10];
  flow_kernel<<<256, 256, 0, stream>>>(q, k, v, shift_k, ssm_k, ssm_D, w1, w2, w3, w4, (char*)d_ws, (u16*)d_out);
}
```
